```python
import torch

def _softmax(x, axis=-1):
    return torch.softmax(x, dim=axis)
def _gelu(x):
    return torch.nn.functional.gelu(x, approximate="tanh")
def _silu(x):
    return torch.nn.functional.silu(x)
def _sigmoid(x):
    return torch.sigmoid(x)
def _layernorm(x, axis=-1, eps=1e-5):
    m = x.mean(dim=axis, keepdim=True)
    v = x.var(dim=axis, keepdim=True, unbiased=False)
    return (x - m) / torch.sqrt(v + eps)
def _scatter_add(tgt, idx, src, axis=0):
    return tgt.clone().index_add_(axis, idx, src)
def _segment_sum(src, seg, n):
    out = torch.zeros((n,) + src.shape[1:], dtype=src.dtype)
    return out.index_add_(0, seg, src)
def _pad(x, pw):
    flat = tuple(v for pair in reversed(pw) for v in pair)
    return torch.nn.functional.pad(x, flat)

kernel_name = 'gated_mix__sliding_local_dilated__compressed_kv_cla__prompt'

def setup_inputs():

    _w = (lambda a: (lambda t: t.to(torch.bfloat16) if t.is_floating_point() else t)(torch.as_tensor(a)))
    def _A_setup():
        B, S, D = 1, 2048, 576
        return {
            "x": _w((lambda *s: torch.randn(*s, dtype=torch.bfloat16) * 0.05)(B, S, D)),
            "wqkv": _w((lambda *s: torch.randn(*s, dtype=torch.bfloat16) * 1.6)(D, 3 * D)),
            "wo": _w((lambda *s: torch.randn(*s, dtype=torch.bfloat16) * 0.05)(D, D)),
        }
    _A_in = _A_setup()

    def _B_setup():
        B, H, Sq, Sk, D, M = 1, 32, 2048, 2048, 128, 576
        Dc = 4 * D
        return {
            "x": _w((lambda *s: torch.randn(*s, dtype=torch.bfloat16) * 0.05)(B, Sq, M)),
            "c_kv": _w((lambda *s: torch.randn(*s, dtype=torch.bfloat16) * 1.6)(B, Sk, Dc)),
            "w_q": _w((lambda *s: torch.randn(*s, dtype=torch.bfloat16) * 6.4)(M, H, D) * (M ** -0.5)),
            "w_uk": _w((lambda *s: torch.randn(*s, dtype=torch.bfloat16) * 6.4)(Dc, H, D) * (Dc ** -0.5)),
            "w_uv": _w((lambda *s: torch.randn(*s, dtype=torch.bfloat16) * 1.6)(Dc, H, D) * (Dc ** -0.5)),
            "w_o": _w((lambda *s: torch.randn(*s, dtype=torch.bfloat16) * 1.6)(H, D, M) * ((H * D) ** -0.5)),
        }
    _B_in = _B_setup()

    return {
        "A_x": _A_in["x"],
        "A_wqkv": _A_in["wqkv"],
        "A_wo": _A_in["wo"],
        "B_x": _B_in["x"],
        "B_c_kv": _B_in["c_kv"],
        "B_w_q": _B_in["w_q"],
        "B_w_uk": _B_in["w_uk"],
        "B_w_uv": _B_in["w_uv"],
        "B_w_o": _B_in["w_o"],
        "gate_w": _w((lambda *s: torch.randn(*s, dtype=torch.bfloat16) * 0.05)(576) * 16.0),
    }

def reference(A_x, A_wqkv, A_wo, B_x, B_c_kv, B_w_q, B_w_uk, B_w_uv, B_w_o, gate_w):
    def _A_ref(x, wqkv, wo):
        B, S, D = x.shape
        H = 32; Dh = D // H
        W, dil = 8, 2
        qkv = (lambda x, *d: x.permute(*d))((x @ wqkv).reshape(B, S, 3, H, Dh), 2, 0, 3, 1, 4)
        q, k, v = qkv[0], qkv[1], qkv[2]
        scale = Dh ** -0.5
        pos = torch.arange(S)
        off = torch.arange(W) * dil
        idx = pos[:, None] - off[None, :]
        mask = idx >= 0
        idx_c = torch.where(mask, idx, 0).reshape(-1)
        kg = k[:, :, idx_c, :].reshape(B, H, S, W, Dh)
        vg = v[:, :, idx_c, :].reshape(B, H, S, W, Dh)
        logits = torch.einsum("bhsd,bhswd->bhsw", q, kg) * scale
        logits = torch.where(mask[None, None, :, :], logits, -1e9)
        attn = _softmax(logits, axis=-1)
        o = torch.einsum("bhsw,bhswd->bhsd", attn, vg)
        return (lambda x, *d: x.permute(*d))(o, 0, 2, 1, 3).reshape(B, S, D) @ wo

    def _B_ref(x, c_kv, w_q, w_uk, w_uv, w_o):
        B, Sq, M = x.shape[0], x.shape[1], x.shape[2]
        Sk = c_kv.shape[1]
        H, D = w_q.shape[1], w_q.shape[2]
        q = torch.einsum("bqm,mhd->bhqd", x, w_q)
        k = torch.einsum("bkc,chd->bhkd", c_kv, w_uk)
        v = torch.einsum("bkc,chd->bhkd", c_kv, w_uv)
        scores = torch.einsum("bhqd,bhkd->bhqk", q, k) * (D ** -0.5)
        off = Sk - Sq
        qi = torch.arange(Sq)[:, None]
        kj = torch.arange(Sk)[None, :]
        cmask = kj <= (qi + off)
        scores = torch.where(cmask[None, None], scores, -1e9)
        p = _softmax(scores, axis=-1)
        ctx = torch.einsum("bhqk,bhkd->bhqd", p, v)
        return torch.einsum("bhqd,hdm->bqm", ctx, w_o)

    x = A_x
    g = _sigmoid((x * gate_w).sum(-1, keepdims=True))
    a = _A_ref(x, A_wqkv, A_wo)
    b = _B_ref(x, B_c_kv, B_w_q, B_w_uk, B_w_uv, B_w_o)
    return g * a + (1 - g) * b
```

```cpp
#include <hip/hip_runtime.h>
#include <hip/hip_bf16.h>
#include <stdint.h>
#include <utility>

typedef short bf16x8 __attribute__((ext_vector_type(8)));
typedef short s16x4 __attribute__((ext_vector_type(4)));
typedef float f32x16 __attribute__((ext_vector_type(16)));
typedef float f32x4 __attribute__((ext_vector_type(4)));
typedef unsigned u32x4 __attribute__((ext_vector_type(4)));
typedef unsigned short u16;

constexpr int S = 2048, DM = 576, NH = 32, HD = 128, DC = 512, NQKV = 1728, DHA = 18;
constexpr int NBIG = NH * HD;
constexpr int KS_TOT = 146;

#define SBAR() __builtin_amdgcn_sched_barrier(0)
constexpr size_t WS_Q = 0, WS_K = WS_Q + (size_t)S * NBIG * 2, WS_V = WS_K + (size_t)S * NBIG * 2, WS_AF = WS_V + (size_t)S * NBIG * 2, WS_X = WS_AF + (size_t)128 * KS_TOT * 1024,
                 WS_P = WS_X + (size_t)64 * 16 * 9216, WS_F = WS_P + (size_t)128 * (4 * 32 * 256 + 1024) * 4;
constexpr size_t WS_L = (size_t)96 << 20;
static_assert(WS_F == 96206848, "workspace map is baked into the generated asm");
constexpr int F_EPOCH = 0, F_J = 1024, F_ST = 8192, F_KV = 40960, F_R = 65536;
constexpr unsigned TAG_STEP = 0x9E3779B1u;
__device__ __forceinline__ unsigned ld_flag(const unsigned* p) { return __hip_atomic_load(p, __ATOMIC_RELAXED, __HIP_MEMORY_SCOPE_AGENT); }
__device__ __forceinline__ void st_flag(unsigned* p, unsigned v) { __hip_atomic_store(p, v, __ATOMIC_RELAXED, __HIP_MEMORY_SCOPE_AGENT); }
__device__ __forceinline__ unsigned ld_flag_async(const unsigned* p) { unsigned v; asm volatile("global_load_dword %0, %1, off sc1" : "=v"(v) : "v"(p) : "memory"); return v; }
#define WAITV(v, N) asm volatile("s_waitcnt vmcnt(" #N ")" : "+v"(v) :: "memory")
__device__ __forceinline__ void wait_flags(const unsigned* p, unsigned v, unsigned tag) {
    if (__builtin_expect(__builtin_amdgcn_ballot_w64(v != tag) != 0ull, 0)) {
        unsigned spins = 0;
        do { __builtin_amdgcn_s_sleep(1); } while (__builtin_amdgcn_ballot_w64(ld_flag(p) != tag) != 0ull && ++spins < (1u << 22));
    }
    asm volatile("" ::: "memory");
}

__device__ __forceinline__ unsigned cvtpk(float lo, float hi) {
    unsigned r; asm volatile("v_cvt_pk_bf16_f32 %0, %1, %2" : "=v"(r) : "v"(lo), "v"(hi)); return r;
}
__device__ __forceinline__ float bf2f(u16 v) { return __uint_as_float(((unsigned)v) << 16); }
__device__ __forceinline__ int crow(int r, int hi) { return (r & 3) + 8 * (r >> 2) + 4 * hi; }

typedef __attribute__((address_space(3))) s16x4* lds_s16x4_p;
__device__ __forceinline__ s16x4 trrd(unsigned addr) { return __builtin_amdgcn_ds_read_tr16_b64_v4i16((lds_s16x4_p)addr); }

__device__ __forceinline__ unsigned lane_id() { unsigned l; asm volatile("v_mbcnt_lo_u32_b32 %0, -1, 0\n\tv_mbcnt_hi_u32_b32 %0, -1, %0" : "=v"(l)); return l; }
#define RTS(slot) do {} while (0)
#define CT_L 5u
__device__ __forceinline__ void code_touch(unsigned wi, unsigned lds_scratch) {
    unsigned long long pc; asm volatile("s_getpc_b64 %0" : "=s"(pc));
    asm volatile("s_mov_b32 m0, %0\n\ts_mov_b64 exec, %3\n\tglobal_load_lds_dword %1, %2\n\ts_mov_b64 exec, -1" :: "s"(lds_scratch), "v"(((threadIdx.x & 63u) * 128u + wi) * 128u), "s"(pc & ~127ull), "n"((1u << CT_L) - 1u) : "m0", "memory");
}
#define ACLOB_LO "a0","a1","a2","a3","a4","a5","a6","a7","a8","a9","a10","a11","a12","a13","a14","a15","a16","a17","a18","a19","a20","a21","a22","a23","a24","a25","a26","a27","a28","a29","a30","a31","a32","a33","a34","a35","a36","a37","a38","a39","a40","a41","a42","a43","a44","a45","a46","a47","a48","a49","a50","a51","a52","a53","a54","a55","a56","a57","a58","a59","a60","a61","a62","a63","a64","a65","a66","a67","a68","a69","a70","a71","a72","a73","a74","a75","a76","a77","a78","a79","a80","a81","a82","a83","a84","a85","a86","a87","a88","a89","a90","a91","a92","a93","a94","a95","a96","a97","a98","a99","a100","a101","a102","a103","a104","a105","a106","a107","a108","a109","a110","a111","a112","a113","a114","a115","a116","a117","a118","a119","a120","a121","a122","a123","a124","a125","a126","a127"
#define ACLOB_HI "a128","a129","a130","a131","a132","a133","a134","a135","a136","a137","a138","a139","a140","a141","a142","a143","a144","a145","a146","a147","a148","a149","a150","a151","a152","a153","a154","a155","a156","a157","a158","a159","a160","a161","a162","a163","a164","a165","a166","a167","a168","a169","a170","a171","a172","a173","a174","a175","a176","a177","a178","a179","a180","a181","a182","a183","a184","a185","a186","a187","a188","a189","a190","a191","a192","a193","a194","a195","a196","a197","a198","a199","a200","a201","a202","a203","a204","a205","a206","a207","a208","a209","a210","a211","a212","a213","a214","a215","a216","a217","a218","a219","a220","a221","a222","a223","a224","a225","a226","a227","a228","a229","a230","a231","a232","a233","a234","a235","a236","a237","a238","a239","a240","a241","a242","a243","a244","a245","a246","a247","a248","a249","a250","a251","a252","a253","a254","a255"
#define PROJ_PRE0 \
"s_add_u32 s62, %[rc], 0x0\n" \
"s_and_b32 s62, s62, 0x3ff\n" \
"s_lshl_b32 s63, s62, 12\n" \
"s_add_u32 m0, %[mA], 0x0\n" \
"s_add_u32 s85, s62, 0x0\n" \
"buffer_load_dwordx4 v22, s[64:67], s85 offen lds\n" \
"s_add_u32 m0, %[mA], 0x400\n" \
"s_add_u32 s84, s62, 0x2000\n" \
"buffer_load_dwordx4 v23, s[64:67], s84 offen lds\n" \
"s_add_u32 m0, %[mB], 0x0\n" \
"s_add_u32 s85, s63, 0x0\n" \
"buffer_load_dwordx4 v26, s[68:71], s85 offen lds\n" \
"s_add_u32 m0, %[mB], 0x4000\n" \
"s_add_u32 s84, s63, 0x0\n" \
"buffer_load_dwordx4 v26, s[72:75], s84 offen lds\n" \
"s_add_u32 m0, %[mA], 0x800\n" \
"s_add_u32 s85, s62, 0x4000\n" \
"buffer_load_dwordx4 v22, s[64:67], s85 offen lds\n" \
"s_add_u32 m0, %[mA], 0xc00\n" \
"s_add_u32 s84, s62, 0x6000\n" \
"buffer_load_dwordx4 v23, s[64:67], s84 offen lds\n" \
"s_add_u32 m0, %[mB], 0x400\n" \
"s_add_u32 s85, s63, 0x8000\n" \
"buffer_load_dwordx4 v26, s[68:71], s85 offen lds\n" \
"s_add_u32 m0, %[mB], 0x4400\n" \
"s_add_u32 s84, s63, 0x8000\n" \
"buffer_load_dwordx4 v26, s[72:75], s84 offen lds\n"
#define PROJ_PRE1 \
"s_add_u32 s62, %[rc], 0x0\n" \
"s_and_b32 s62, s62, 0x3ff\n" \
"s_lshl_b32 s63, s62, 12\n" \
"s_add_u32 m0, %[mA], 0x1000\n" \
"s_add_u32 s85, s62, 0x8000\n" \
"buffer_load_dwordx4 v22, s[64:67], s85 offen lds\n" \
"s_add_u32 m0, %[mA], 0x1400\n" \
"s_add_u32 s84, s62, 0xa000\n" \
"buffer_load_dwordx4 v23, s[64:67], s84 offen lds\n" \
"s_add_u32 m0, %[mB], 0x800\n" \
"s_add_u32 s85, s63, 0x10000\n" \
"buffer_load_dwordx4 v27, s[68:71], s85 offen lds\n" \
"s_add_u32 m0, %[mB], 0x4800\n" \
"s_add_u32 s84, s63, 0x10000\n" \
"buffer_load_dwordx4 v27, s[72:75], s84 offen lds\n" \
"s_add_u32 m0, %[mA], 0x1800\n" \
"s_add_u32 s85, s62, 0xc000\n" \
"buffer_load_dwordx4 v22, s[64:67], s85 offen lds\n" \
"s_add_u32 m0, %[mA], 0x1c00\n" \
"s_add_u32 s84, s62, 0xe000\n" \
"buffer_load_dwordx4 v23, s[64:67], s84 offen lds\n" \
"s_add_u32 m0, %[mB], 0xc00\n" \
"s_add_u32 s85, s63, 0x18000\n" \
"buffer_load_dwordx4 v27, s[68:71], s85 offen lds\n" \
"s_add_u32 m0, %[mB], 0x4c00\n" \
"s_add_u32 s84, s63, 0x18000\n" \
"buffer_load_dwordx4 v27, s[72:75], s84 offen lds\n"
#define PROJ_PRE2 \
"s_add_u32 s62, %[rc], 0x80\n" \
"s_and_b32 s62, s62, 0x3ff\n" \
"s_lshl_b32 s63, s62, 12\n" \
"s_add_u32 m0, %[mA], 0x8000\n" \
"s_add_u32 s85, s62, 0x0\n" \
"buffer_load_dwordx4 v22, s[64:67], s85 offen lds\n" \
"s_add_u32 m0, %[mA], 0x8400\n" \
"s_add_u32 s84, s62, 0x2000\n" \
"buffer_load_dwordx4 v23, s[64:67], s84 offen lds\n" \
"s_add_u32 m0, %[mB], 0x8000\n" \
"s_add_u32 s85, s63, 0x0\n" \
"buffer_load_dwordx4 v26, s[68:71], s85 offen lds\n" \
"s_add_u32 m0, %[mB], 0xc000\n" \
"s_add_u32 s84, s63, 0x0\n" \
"buffer_load_dwordx4 v26, s[72:75], s84 offen lds\n" \
"s_add_u32 m0, %[mA], 0x8800\n" \
"s_add_u32 s85, s62, 0x4000\n" \
"buffer_load_dwordx4 v22, s[64:67], s85 offen lds\n" \
"s_add_u32 m0, %[mA], 0x8c00\n" \
"s_add_u32 s84, s62, 0x6000\n" \
"buffer_load_dwordx4 v23, s[64:67], s84 offen lds\n" \
"s_add_u32 m0, %[mB], 0x8400\n" \
"s_add_u32 s85, s63, 0x8000\n" \
"buffer_load_dwordx4 v26, s[68:71], s85 offen lds\n" \
"s_add_u32 m0, %[mB], 0xc400\n" \
"s_add_u32 s84, s63, 0x8000\n" \
"buffer_load_dwordx4 v26, s[72:75], s84 offen lds\n"
#define PROJ_PRE3 \
"s_add_u32 s62, %[rc], 0x80\n" \
"s_and_b32 s62, s62, 0x3ff\n" \
"s_lshl_b32 s63, s62, 12\n" \
"s_add_u32 m0, %[mA], 0x9000\n" \
"s_add_u32 s85, s62, 0x8000\n" \
"buffer_load_dwordx4 v22, s[64:67], s85 offen lds\n" \
"s_add_u32 m0, %[mA], 0x9400\n" \
"s_add_u32 s84, s62, 0xa000\n" \
"buffer_load_dwordx4 v23, s[64:67], s84 offen lds\n" \
"s_add_u32 m0, %[mB], 0x8800\n" \
"s_add_u32 s85, s63, 0x10000\n" \
"buffer_load_dwordx4 v27, s[68:71], s85 offen lds\n" \
"s_add_u32 m0, %[mB], 0xc800\n" \
"s_add_u32 s84, s63, 0x10000\n" \
"buffer_load_dwordx4 v27, s[72:75], s84 offen lds\n" \
"s_add_u32 m0, %[mA], 0x9800\n" \
"s_add_u32 s85, s62, 0xc000\n" \
"buffer_load_dwordx4 v22, s[64:67], s85 offen lds\n" \
"s_add_u32 m0, %[mA], 0x9c00\n" \
"s_add_u32 s84, s62, 0xe000\n" \
"buffer_load_dwordx4 v23, s[64:67], s84 offen lds\n" \
"s_add_u32 m0, %[mB], 0x8c00\n" \
"s_add_u32 s85, s63, 0x18000\n" \
"buffer_load_dwordx4 v27, s[68:71], s85 offen lds\n" \
"s_add_u32 m0, %[mB], 0xcc00\n" \
"s_add_u32 s84, s63, 0x18000\n" \
"buffer_load_dwordx4 v27, s[72:75], s84 offen lds\n"
#define PROJ_TKE 0
#define PROJ_T0 0
#define PROJ_ROT sp+mt
#define PROJ_TC 0
#define PROJ_ASM \
".p2align 3\n" \
"s_branch L_go_%=\n" \
"L_pl_%=:\n" \
"s_mov_b32 s89, 0\n" \
"L_ql_%=:\n" \
"s_sleep 1\n" \
"v_bfe_u32 v44, v20, 2, 5\n" \
"buffer_load_dword v44, v44, s[80:83], %[oFp] offen offset:64 sc1\n" \
"s_waitcnt vmcnt(0)\n" \
"v_cmp_ne_u32_e32 vcc, %[tag], v44\n" \
"s_add_u32 s89, s89, 1\n" \
"s_nop 3\n" \
"s_cbranch_vccz L_rl_%=\n" \
"s_cmp_gt_u32 s89, 0x400000\n" \
"s_cbranch_scc0 L_ql_%=\n" \
"s_branch L_rl_%=\n" \
"L_pa_%=:\n" \
"s_mov_b32 s89, 0\n" \
"L_qa_%=:\n" \
"s_sleep 1\n" \
"v_bfe_u32 v44, v20, 2, 5\n" \
"buffer_load_dword v44, v44, s[80:83], %[oFp] offen offset:0 sc1\n" \
"s_waitcnt vmcnt(0)\n" \
"v_cmp_ne_u32_e32 vcc, %[tag], v44\n" \
"s_add_u32 s89, s89, 1\n" \
"s_nop 3\n" \
"s_cbranch_vccz L_ra_%=\n" \
"s_cmp_gt_u32 s89, 0x400000\n" \
"s_cbranch_scc0 L_qa_%=\n" \
"s_branch L_ra_%=\n" \
"L_go_%=:\n" \
"v_xor_b32_e32 v33, 0x20, v32\n" \
"s_mov_b32 s80, %[wsl]\n" \
"s_and_b32 s81, %[wsh], 0xffff\n" \
"s_mov_b32 s82, 0x10000000\n" \
"s_mov_b32 s83, 0x20000\n" \
"s_mov_b32 s84, 0x5bc0000\n" \
"buffer_load_dword v180, off, s[80:83], s84 sc1\n" \
"v_xor_b32_e32 v34, 0x40, v32\n" \
"v_xor_b32_e32 v35, 0x60, v32\n" \
"s_waitcnt vmcnt(17)\n" \
"s_barrier\n" \
"ds_read_b64_tr_b16 v[120:121], v32 offset:8192\n" \
"ds_read_b64_tr_b16 v[122:123], v32 offset:9216\n" \
"ds_read_b128 v[56:59], v29 offset:16384\n" \
"ds_read_b64_tr_b16 v[116:117], v32 offset:0\n" \
"ds_read_b64_tr_b16 v[118:119], v32 offset:1024\n" \
"ds_read_b128 v[52:55], v28 offset:16384\n" \
"ds_read_b128 v[60:63], v28 offset:18432\n" \
"ds_read_b128 v[64:67], v29 offset:18432\n" \
"ds_read_b128 v[72:75], v29 offset:20480\n" \
"ds_read_b128 v[68:71], v28 offset:20480\n" \
"ds_read_b128 v[76:79], v28 offset:22528\n" \
"ds_read_b128 v[80:83], v29 offset:22528\n" \
"ds_read_b64_tr_b16 v[128:129], v33 offset:8192\n" \
"ds_read_b64_tr_b16 v[130:131], v33 offset:9216\n" \
"ds_read_b64_tr_b16 v[124:125], v33 offset:0\n" \
"ds_read_b64_tr_b16 v[126:127], v33 offset:1024\n" \
"ds_read_b64_tr_b16 v[136:137], v34 offset:8192\n" \
"ds_read_b64_tr_b16 v[138:139], v34 offset:9216\n" \
"ds_read_b64_tr_b16 v[132:133], v34 offset:0\n" \
"ds_read_b64_tr_b16 v[134:135], v34 offset:1024\n" \
"ds_read_b64_tr_b16 v[144:145], v35 offset:8192\n" \
"ds_read_b64_tr_b16 v[146:147], v35 offset:9216\n" \
"ds_read_b64_tr_b16 v[140:141], v35 offset:0\n" \
"ds_read_b64_tr_b16 v[142:143], v35 offset:1024\n" \
"s_mov_b32 m0, %[mS]\n" \
"s_mov_b32 s76, %[wvl]\n" \
"s_and_b32 s77, %[wvh], 0xffff\n" \
"s_mov_b32 s78, 0x1e6000\n" \
"s_mov_b32 s79, 0x20000\n" \
"s_add_u32 s88, %[oLs], 0x1000\n" \
"v_xor_b32_e32 v39, 0x60, v190\n" \
"v_xor_b32_e32 v37, 0x20, v190\n" \
"v_xor_b32_e32 v38, 0x40, v190\n" \
"v_add_u32_e32 v48, %[dHi], v28\n" \
"v_add_u32_e32 v49, %[dHi], v29\n" \
"v_xor_b32_e32 v36, 0x0, v190\n" \
"s_waitcnt lgkmcnt(15)\n" \
"v_mfma_f32_16x16x32_bf16 a[0:3], v[120:123], v[56:59], 0\n" \
"ds_read_b64_tr_b16 v[152:153], v32 offset:24576\n" \
"ds_read_b64_tr_b16 v[154:155], v32 offset:25600\n" \
"v_mfma_f32_16x16x32_bf16 a[0:3], v[116:119], v[52:55], a[0:3]\n" \
"ds_read_b64_tr_b16 v[148:149], v32 offset:16384\n" \
"ds_read_b64_tr_b16 v[150:151], v32 offset:17408\n" \
"v_mfma_f32_16x16x32_bf16 a[32:35], v[116:119], v[60:63], 0\n" \
"ds_read_b64_tr_b16 v[160:161], v33 offset:24576\n" \
"ds_read_b64_tr_b16 v[162:163], v33 offset:25600\n" \
"v_mfma_f32_16x16x32_bf16 a[32:35], v[120:123], v[64:67], a[32:35]\n" \
"ds_read_b64_tr_b16 v[156:157], v33 offset:16384\n" \
"ds_read_b64_tr_b16 v[158:159], v33 offset:17408\n" \
"v_mfma_f32_16x16x32_bf16 a[64:67], v[120:123], v[72:75], 0\n" \
"ds_read_b64_tr_b16 v[168:169], v34 offset:24576\n" \
"s_waitcnt lgkmcnt(15)\n" \
"s_nop 0\n" \
"v_mfma_f32_16x16x32_bf16 a[64:67], v[116:119], v[68:71], a[64:67]\n" \
"ds_read_b64_tr_b16 v[170:171], v34 offset:25600\n" \
"ds_read_b64_tr_b16 v[164:165], v34 offset:16384\n" \
"v_mfma_f32_16x16x32_bf16 a[96:99], v[116:119], v[76:79], 0\n" \
"ds_read_b64_tr_b16 v[166:167], v34 offset:17408\n" \
"ds_read_b64_tr_b16 v[176:177], v35 offset:24576\n" \
"v_mfma_f32_16x16x32_bf16 a[96:99], v[120:123], v[80:83], a[96:99]\n" \
"ds_read_b64_tr_b16 v[178:179], v35 offset:25600\n" \
"ds_read_b64_tr_b16 v[172:173], v35 offset:16384\n" \
"v_mfma_f32_16x16x32_bf16 a[4:7], v[128:131], v[56:59], 0\n" \
"ds_read_b64_tr_b16 v[174:175], v35 offset:17408\n" \
"ds_read_b128 v[84:87], v28 offset:0\n" \
"v_mfma_f32_16x16x32_bf16 a[4:7], v[124:127], v[52:55], a[4:7]\n" \
"ds_read_b128 v[88:91], v29 offset:0\n" \
"ds_read_b128 v[92:95], v28 offset:2048\n" \
"v_mfma_f32_16x16x32_bf16 a[36:39], v[124:127], v[60:63], 0\n" \
"ds_read_b128 v[96:99], v29 offset:2048\n" \
"ds_read_b128 v[100:103], v28 offset:4096\n" \
"v_mfma_f32_16x16x32_bf16 a[36:39], v[128:131], v[64:67], a[36:39]\n" \
"ds_read_b128 v[104:107], v29 offset:4096\n" \
"ds_read_b128 v[108:111], v28 offset:6144\n" \
"v_mfma_f32_16x16x32_bf16 a[68:71], v[128:131], v[72:75], 0\n" \
"ds_read_b128 v[112:115], v29 offset:6144\n" \
"v_mfma_f32_16x16x32_bf16 a[68:71], v[124:127], v[68:71], a[68:71]\n" \
"v_mfma_f32_16x16x32_bf16 a[100:103], v[124:127], v[76:79], 0\n" \
"v_mfma_f32_16x16x32_bf16 a[100:103], v[128:131], v[80:83], a[100:103]\n" \
"v_mfma_f32_16x16x32_bf16 a[8:11], v[136:139], v[56:59], 0\n" \
"s_waitcnt lgkmcnt(15)\n" \
"v_mfma_f32_16x16x32_bf16 a[8:11], v[132:135], v[52:55], a[8:11]\n" \
"v_mfma_f32_16x16x32_bf16 a[40:43], v[132:135], v[60:63], 0\n" \
"v_mfma_f32_16x16x32_bf16 a[40:43], v[136:139], v[64:67], a[40:43]\n" \
"v_mfma_f32_16x16x32_bf16 a[72:75], v[136:139], v[72:75], 0\n" \
"v_mfma_f32_16x16x32_bf16 a[72:75], v[132:135], v[68:71], a[72:75]\n" \
"s_mov_b32 exec_lo, 0x29\n" \
"s_mov_b32 exec_hi, 0x600000\n" \
"buffer_load_dword v11, s[80:83], 0 offen sc1 lds\n" \
"s_mov_b64 exec, -1\n" \
"v_mfma_f32_16x16x32_bf16 a[104:107], v[132:135], v[76:79], 0\n" \
"s_mov_b32 m0, %[mS]\n" \
"v_mfma_f32_16x16x32_bf16 a[104:107], v[136:139], v[80:83], a[104:107]\n" \
"v_mfma_f32_16x16x32_bf16 a[12:15], v[144:147], v[56:59], 0\n" \
"v_mfma_f32_16x16x32_bf16 a[12:15], v[140:143], v[52:55], a[12:15]\n" \
"v_mfma_f32_16x16x32_bf16 a[44:47], v[140:143], v[60:63], 0\n" \
"s_mov_b32 exec_lo, 0x7ffffff\n" \
"s_mov_b32 exec_hi, 0x0\n" \
"global_load_lds_dword v[186:187], off\n" \
"s_mov_b64 exec, -1\n" \
"v_mfma_f32_16x16x32_bf16 a[44:47], v[144:147], v[64:67], a[44:47]\n" \
"v_mfma_f32_16x16x32_bf16 a[76:79], v[144:147], v[72:75], 0\n" \
"v_mfma_f32_16x16x32_bf16 a[76:79], v[140:143], v[68:71], a[76:79]\n" \
"v_mfma_f32_16x16x32_bf16 a[108:111], v[140:143], v[76:79], 0\n" \
"v_mfma_f32_16x16x32_bf16 a[108:111], v[144:147], v[80:83], a[108:111]\n" \
"v_mfma_f32_16x16x32_bf16 a[16:19], v[56:59], v[152:155], 0\n" \
"v_mfma_f32_16x16x32_bf16 a[16:19], v[52:55], v[148:151], a[16:19]\n" \
"v_mfma_f32_16x16x32_bf16 a[48:51], v[60:63], v[148:151], 0\n" \
"v_mfma_f32_16x16x32_bf16 a[48:51], v[64:67], v[152:155], a[48:51]\n" \
"v_mfma_f32_16x16x32_bf16 a[80:83], v[72:75], v[152:155], 0\n" \
"v_mfma_f32_16x16x32_bf16 a[80:83], v[68:71], v[148:151], a[80:83]\n" \
"v_mfma_f32_16x16x32_bf16 a[112:115], v[76:79], v[148:151], 0\n" \
"v_mfma_f32_16x16x32_bf16 a[112:115], v[80:83], v[152:155], a[112:115]\n" \
"v_mfma_f32_16x16x32_bf16 a[20:23], v[56:59], v[160:163], 0\n" \
"v_mfma_f32_16x16x32_bf16 a[20:23], v[52:55], v[156:159], a[20:23]\n" \
"v_mfma_f32_16x16x32_bf16 a[52:55], v[60:63], v[156:159], 0\n" \
"v_mfma_f32_16x16x32_bf16 a[52:55], v[64:67], v[160:163], a[52:55]\n" \
"v_mfma_f32_16x16x32_bf16 a[84:87], v[72:75], v[160:163], 0\n" \
"v_mfma_f32_16x16x32_bf16 a[84:87], v[68:71], v[156:159], a[84:87]\n" \
"v_mfma_f32_16x16x32_bf16 a[116:119], v[76:79], v[156:159], 0\n" \
"v_mfma_f32_16x16x32_bf16 a[116:119], v[80:83], v[160:163], a[116:119]\n" \
"s_waitcnt lgkmcnt(0)\n" \
"s_nop 0\n" \
"v_mfma_f32_16x16x32_bf16 a[24:27], v[56:59], v[168:171], 0\n" \
"v_mfma_f32_16x16x32_bf16 a[24:27], v[52:55], v[164:167], a[24:27]\n" \
"v_mfma_f32_16x16x32_bf16 a[56:59], v[60:63], v[164:167], 0\n" \
"v_mfma_f32_16x16x32_bf16 a[56:59], v[64:67], v[168:171], a[56:59]\n" \
"v_mfma_f32_16x16x32_bf16 a[88:91], v[72:75], v[168:171], 0\n" \
"v_mfma_f32_16x16x32_bf16 a[88:91], v[68:71], v[164:167], a[88:91]\n" \
"v_mfma_f32_16x16x32_bf16 a[120:123], v[76:79], v[164:167], 0\n" \
"v_mfma_f32_16x16x32_bf16 a[120:123], v[80:83], v[168:171], a[120:123]\n" \
"v_mfma_f32_16x16x32_bf16 a[28:31], v[56:59], v[176:179], 0\n" \
"s_add_u32 s62, %[rc], 0x100\n" \
"s_and_b32 s62, s62, 0x3ff\n" \
"v_mfma_f32_16x16x32_bf16 a[28:31], v[52:55], v[172:175], a[28:31]\n" \
"s_lshl_b32 s63, s62, 12\n" \
"v_mfma_f32_16x16x32_bf16 a[60:63], v[60:63], v[172:175], 0\n" \
"v_mfma_f32_16x16x32_bf16 a[60:63], v[64:67], v[176:179], a[60:63]\n" \
"v_mfma_f32_16x16x32_bf16 a[92:95], v[72:75], v[176:179], 0\n" \
"v_mfma_f32_16x16x32_bf16 a[92:95], v[68:71], v[172:175], a[92:95]\n" \
"v_mfma_f32_16x16x32_bf16 a[124:127], v[76:79], v[172:175], 0\n" \
"v_mfma_f32_16x16x32_bf16 a[124:127], v[80:83], v[176:179], a[124:127]\n" \
"s_waitcnt vmcnt(3)\n" \
"s_barrier\n" \
"v_mfma_f32_16x16x32_bf16 a[144:147], v[88:91], v[152:155], 0\n" \
"s_add_u32 m0, %[mA], 0x0\n" \
"s_add_u32 s85, s62, 0x0\n" \
"s_nop 0\n" \
"v_mfma_f32_16x16x32_bf16 a[144:147], v[84:87], v[148:151], a[144:147]\n" \
"buffer_load_dwordx4 v22, s[64:67], s85 offen lds\n" \
"v_mfma_f32_16x16x32_bf16 a[176:179], v[92:95], v[148:151], 0\n" \
"ds_read_b128 v[56:59], v29 offset:49152\n" \
"ds_read_b128 v[52:55], v28 offset:49152\n" \
"v_mfma_f32_16x16x32_bf16 a[176:179], v[96:99], v[152:155], a[176:179]\n" \
"s_add_u32 m0, %[mA], 0x400\n" \
"s_add_u32 s84, s62, 0x2000\n" \
"v_mfma_f32_16x16x32_bf16 a[208:211], v[104:107], v[152:155], 0\n" \
"buffer_load_dwordx4 v23, s[64:67], s84 offen lds\n" \
"v_mfma_f32_16x16x32_bf16 a[208:211], v[100:103], v[148:151], a[208:211]\n" \
"ds_read_b128 v[60:63], v28 offset:51200\n" \
"ds_read_b128 v[64:67], v29 offset:51200\n" \
"v_mfma_f32_16x16x32_bf16 a[240:243], v[108:111], v[148:151], 0\n" \
"ds_read_b64_tr_b16 v[148:149], v32 offset:49152\n" \
"ds_read_b64_tr_b16 v[150:151], v32 offset:50176\n" \
"v_mfma_f32_16x16x32_bf16 a[240:243], v[112:115], v[152:155], a[240:243]\n" \
"s_add_u32 m0, %[mB], 0x0\n" \
"s_add_u32 s85, s63, 0x0\n" \
"v_mfma_f32_16x16x32_bf16 a[148:151], v[88:91], v[160:163], 0\n" \
"buffer_load_dwordx4 v26, s[68:71], s85 offen lds\n" \
"v_mfma_f32_16x16x32_bf16 a[148:151], v[84:87], v[156:159], a[148:151]\n" \
"ds_read_b64_tr_b16 v[152:153], v32 offset:57344\n" \
"ds_read_b64_tr_b16 v[154:155], v32 offset:58368\n" \
"v_mfma_f32_16x16x32_bf16 a[180:183], v[92:95], v[156:159], 0\n" \
"ds_read_b128 v[72:75], v29 offset:53248\n" \
"ds_read_b128 v[68:71], v28 offset:53248\n" \
"v_mfma_f32_16x16x32_bf16 a[180:183], v[96:99], v[160:163], a[180:183]\n" \
"s_add_u32 m0, %[mB], 0x4000\n" \
"s_add_u32 s84, s63, 0x0\n" \
"s_nop 0\n" \
"v_mfma_f32_16x16x32_bf16 a[212:215], v[104:107], v[160:163], 0\n" \
"buffer_load_dwordx4 v26, s[72:75], s84 offen lds\n" \
"v_mfma_f32_16x16x32_bf16 a[212:215], v[100:103], v[156:159], a[212:215]\n" \
"ds_read_b128 v[76:79], v28 offset:55296\n" \
"ds_read_b128 v[80:83], v29 offset:55296\n" \
"v_mfma_f32_16x16x32_bf16 a[244:247], v[108:111], v[156:159], 0\n" \
"ds_read_b64_tr_b16 v[156:157], v33 offset:49152\n" \
"ds_read_b64_tr_b16 v[158:159], v33 offset:50176\n" \
"v_mfma_f32_16x16x32_bf16 a[244:247], v[112:115], v[160:163], a[244:247]\n" \
"s_add_u32 m0, %[mA], 0x800\n" \
"s_add_u32 s85, s62, 0x4000\n" \
"v_mfma_f32_16x16x32_bf16 a[152:155], v[88:91], v[168:171], 0\n" \
"buffer_load_dwordx4 v22, s[64:67], s85 offen lds\n" \
"v_mfma_f32_16x16x32_bf16 a[152:155], v[84:87], v[164:167], a[152:155]\n" \
"ds_read_b64_tr_b16 v[160:161], v33 offset:57344\n" \
"ds_read_b64_tr_b16 v[162:163], v33 offset:58368\n" \
"v_mfma_f32_16x16x32_bf16 a[184:187], v[92:95], v[164:167], 0\n" \
"s_waitcnt vmcnt(7)\n" \
"v_readfirstlane_b32 %[tag], v180\n" \
"v_mfma_f32_16x16x32_bf16 a[184:187], v[96:99], v[168:171], a[184:187]\n" \
"s_add_u32 m0, %[mA], 0xc00\n" \
"s_add_u32 s84, s62, 0x6000\n" \
"v_mfma_f32_16x16x32_bf16 a[216:219], v[104:107], v[168:171], 0\n" \
"buffer_load_dwordx4 v23, s[64:67], s84 offen lds\n" \
"v_mfma_f32_16x16x32_bf16 a[216:219], v[100:103], v[164:167], a[216:219]\n" \
"s_add_u32 %[tag], %[tag], 0x9E3779B1\n" \
"v_mov_b32_e64 v45, %[tag]\n" \
"v_mfma_f32_16x16x32_bf16 a[248:251], v[108:111], v[164:167], 0\n" \
"ds_read_b64_tr_b16 v[164:165], v34 offset:49152\n" \
"ds_read_b64_tr_b16 v[166:167], v34 offset:50176\n" \
"v_mfma_f32_16x16x32_bf16 a[248:251], v[112:115], v[168:171], a[248:251]\n" \
"s_add_u32 m0, %[mB], 0x400\n" \
"s_add_u32 s85, s63, 0x8000\n" \
"v_mfma_f32_16x16x32_bf16 a[156:159], v[88:91], v[176:179], 0\n" \
"buffer_load_dwordx4 v26, s[68:71], s85 offen lds\n" \
"v_mfma_f32_16x16x32_bf16 a[156:159], v[84:87], v[172:175], a[156:159]\n" \
"ds_read_b64_tr_b16 v[168:169], v34 offset:57344\n" \
"ds_read_b64_tr_b16 v[170:171], v34 offset:58368\n" \
"v_mfma_f32_16x16x32_bf16 a[188:191], v[92:95], v[172:175], 0\n" \
"v_mfma_f32_16x16x32_bf16 a[188:191], v[96:99], v[176:179], a[188:191]\n" \
"s_add_u32 m0, %[mB], 0x4400\n" \
"s_add_u32 s84, s63, 0x8000\n" \
"v_mfma_f32_16x16x32_bf16 a[220:223], v[104:107], v[176:179], 0\n" \
"buffer_load_dwordx4 v26, s[72:75], s84 offen lds\n" \
"v_mfma_f32_16x16x32_bf16 a[220:223], v[100:103], v[172:175], a[220:223]\n" \
"v_mfma_f32_16x16x32_bf16 a[252:255], v[108:111], v[172:175], 0\n" \
"ds_read_b64_tr_b16 v[172:173], v35 offset:49152\n" \
"ds_read_b64_tr_b16 v[174:175], v35 offset:50176\n" \
"v_mfma_f32_16x16x32_bf16 a[252:255], v[112:115], v[176:179], a[252:255]\n" \
"s_add_u32 m0, %[mA], 0x1000\n" \
"s_add_u32 s85, s62, 0x8000\n" \
"v_mfma_f32_16x16x32_bf16 a[128:131], v[120:123], v[88:91], 0\n" \
"buffer_load_dwordx4 v22, s[64:67], s85 offen lds\n" \
"v_mfma_f32_16x16x32_bf16 a[128:131], v[116:119], v[84:87], a[128:131]\n" \
"ds_read_b64_tr_b16 v[176:177], v35 offset:57344\n" \
"ds_read_b64_tr_b16 v[178:179], v35 offset:58368\n" \
"v_mfma_f32_16x16x32_bf16 a[160:163], v[116:119], v[92:95], 0\n" \
"v_mfma_f32_16x16x32_bf16 a[160:163], v[120:123], v[96:99], a[160:163]\n" \
"s_add_u32 m0, %[mA], 0x1400\n" \
"s_add_u32 s84, s62, 0xa000\n" \
"v_mfma_f32_16x16x32_bf16 a[192:195], v[120:123], v[104:107], 0\n" \
"buffer_load_dwordx4 v23, s[64:67], s84 offen lds\n" \
"v_mfma_f32_16x16x32_bf16 a[192:195], v[116:119], v[100:103], a[192:195]\n" \
"v_mfma_f32_16x16x32_bf16 a[224:227], v[116:119], v[108:111], 0\n" \
"ds_read_b64_tr_b16 v[116:117], v32 offset:32768\n" \
"ds_read_b64_tr_b16 v[118:119], v32 offset:33792\n" \
"v_mfma_f32_16x16x32_bf16 a[224:227], v[120:123], v[112:115], a[224:227]\n" \
"s_add_u32 m0, %[mB], 0x800\n" \
"s_add_u32 s85, s63, 0x10000\n" \
"v_mfma_f32_16x16x32_bf16 a[132:135], v[128:131], v[88:91], 0\n" \
"buffer_load_dwordx4 v27, s[68:71], s85 offen lds\n" \
"v_mfma_f32_16x16x32_bf16 a[132:135], v[124:127], v[84:87], a[132:135]\n" \
"ds_read_b64_tr_b16 v[120:121], v32 offset:40960\n" \
"ds_read_b64_tr_b16 v[122:123], v32 offset:41984\n" \
"v_mfma_f32_16x16x32_bf16 a[164:167], v[124:127], v[92:95], 0\n" \
"v_mfma_f32_16x16x32_bf16 a[164:167], v[128:131], v[96:99], a[164:167]\n" \
"s_add_u32 m0, %[mB], 0x4800\n" \
"s_add_u32 s84, s63, 0x10000\n" \
"v_mfma_f32_16x16x32_bf16 a[196:199], v[128:131], v[104:107], 0\n" \
"buffer_load_dwordx4 v27, s[72:75], s84 offen lds\n" \
"v_mfma_f32_16x16x32_bf16 a[196:199], v[124:127], v[100:103], a[196:199]\n" \
"v_mfma_f32_16x16x32_bf16 a[228:231], v[124:127], v[108:111], 0\n" \
"ds_read_b64_tr_b16 v[124:125], v33 offset:32768\n" \
"ds_read_b64_tr_b16 v[126:127], v33 offset:33792\n" \
"v_mfma_f32_16x16x32_bf16 a[228:231], v[128:131], v[112:115], a[228:231]\n" \
"ds_read_b64_tr_b16 v[128:129], v33 offset:40960\n" \
"ds_read_b64_tr_b16 v[130:131], v33 offset:41984\n" \
"v_mfma_f32_16x16x32_bf16 a[136:139], v[136:139], v[88:91], 0\n" \
"v_mfma_f32_16x16x32_bf16 a[136:139], v[132:135], v[84:87], a[136:139]\n" \
"v_mfma_f32_16x16x32_bf16 a[168:171], v[132:135], v[92:95], 0\n" \
"v_mfma_f32_16x16x32_bf16 a[168:171], v[136:139], v[96:99], a[168:171]\n" \
"v_mfma_f32_16x16x32_bf16 a[200:203], v[136:139], v[104:107], 0\n" \
"v_mfma_f32_16x16x32_bf16 a[200:203], v[132:135], v[100:103], a[200:203]\n" \
"v_mfma_f32_16x16x32_bf16 a[232:235], v[132:135], v[108:111], 0\n" \
"ds_read_b64_tr_b16 v[132:133], v34 offset:32768\n" \
"ds_read_b64_tr_b16 v[134:135], v34 offset:33792\n" \
"v_mfma_f32_16x16x32_bf16 a[232:235], v[136:139], v[112:115], a[232:235]\n" \
"ds_read_b64_tr_b16 v[136:137], v34 offset:40960\n" \
"ds_read_b64_tr_b16 v[138:139], v34 offset:41984\n" \
"v_mfma_f32_16x16x32_bf16 a[140:143], v[144:147], v[88:91], 0\n" \
"ds_read_b128 v[88:91], v29 offset:32768\n" \
"v_mfma_f32_16x16x32_bf16 a[140:143], v[140:143], v[84:87], a[140:143]\n" \
"ds_read_b128 v[84:87], v28 offset:32768\n" \
"v_mfma_f32_16x16x32_bf16 a[172:175], v[140:143], v[92:95], 0\n" \
"ds_read_b128 v[92:95], v28 offset:34816\n" \
"v_mfma_f32_16x16x32_bf16 a[172:175], v[144:147], v[96:99], a[172:175]\n" \
"ds_read_b128 v[96:99], v29 offset:34816\n" \
"v_mfma_f32_16x16x32_bf16 a[204:207], v[144:147], v[104:107], 0\n" \
"ds_read_b128 v[104:107], v29 offset:36864\n" \
"v_mfma_f32_16x16x32_bf16 a[204:207], v[140:143], v[100:103], a[204:207]\n" \
"ds_read_b128 v[100:103], v28 offset:36864\n" \
"v_mfma_f32_16x16x32_bf16 a[236:239], v[140:143], v[108:111], 0\n" \
"ds_read_b64_tr_b16 v[140:141], v35 offset:32768\n" \
"ds_read_b64_tr_b16 v[142:143], v35 offset:33792\n" \
"v_mfma_f32_16x16x32_bf16 a[236:239], v[144:147], v[112:115], a[236:239]\n" \
"s_add_u32 m0, %[mA], 0x1800\n" \
"s_waitcnt lgkmcnt(12)\n" \
"s_nop 0\n" \
"v_mfma_f32_16x16x32_bf16 a[16:19], v[56:59], v[152:155], a[16:19]\n" \
"s_add_u32 s85, s62, 0xc000\n" \
"ds_read_b64_tr_b16 v[144:145], v35 offset:40960\n" \
"v_mfma_f32_16x16x32_bf16 a[16:19], v[52:55], v[148:151], a[16:19]\n" \
"buffer_load_dwordx4 v22, s[64:67], s85 offen lds\n" \
"v_mfma_f32_16x16x32_bf16 a[48:51], v[60:63], v[148:151], a[48:51]\n" \
"s_add_u32 m0, %[mA], 0x1c00\n" \
"s_add_u32 s84, s62, 0xe000\n" \
"v_mfma_f32_16x16x32_bf16 a[48:51], v[64:67], v[152:155], a[48:51]\n" \
"ds_read_b64_tr_b16 v[146:147], v35 offset:41984\n" \
"ds_read_b128 v[108:111], v28 offset:38912\n" \
"v_mfma_f32_16x16x32_bf16 a[80:83], v[72:75], v[152:155], a[80:83]\n" \
"buffer_load_dwordx4 v23, s[64:67], s84 offen lds\n" \
"L_pf0:\n" \
"s_cbranch_execz L_pf4\n" \
"s_nop 0\n" \
"v_mfma_f32_16x16x32_bf16 a[80:83], v[68:71], v[148:151], a[80:83]\n" \
"s_add_u32 m0, %[mB], 0xc00\n" \
"s_add_u32 s85, s63, 0x18000\n" \
"v_mfma_f32_16x16x32_bf16 a[112:115], v[76:79], v[148:151], a[112:115]\n" \
"ds_read_b128 v[112:115], v29 offset:38912\n" \
"v_mfma_f32_16x16x32_bf16 a[112:115], v[80:83], v[152:155], a[112:115]\n" \
"buffer_load_dwordx4 v27, s[68:71], s85 offen lds\n" \
"v_mfma_f32_16x16x32_bf16 a[20:23], v[56:59], v[160:163], a[20:23]\n" \
"s_add_u32 m0, %[mB], 0x4c00\n" \
"s_add_u32 s84, s63, 0x18000\n" \
"v_mfma_f32_16x16x32_bf16 a[20:23], v[52:55], v[156:159], a[20:23]\n" \
"v_mfma_f32_16x16x32_bf16 a[52:55], v[60:63], v[156:159], a[52:55]\n" \
"buffer_load_dwordx4 v27, s[72:75], s84 offen lds\n" \
"v_mfma_f32_16x16x32_bf16 a[52:55], v[64:67], v[160:163], a[52:55]\n" \
"v_mfma_f32_16x16x32_bf16 a[84:87], v[72:75], v[160:163], a[84:87]\n" \
"v_mfma_f32_16x16x32_bf16 a[84:87], v[68:71], v[156:159], a[84:87]\n" \
"v_mfma_f32_16x16x32_bf16 a[116:119], v[76:79], v[156:159], a[116:119]\n" \
"v_mfma_f32_16x16x32_bf16 a[116:119], v[80:83], v[160:163], a[116:119]\n" \
"s_mov_b32 m0, %[mS]\n" \
"v_mfma_f32_16x16x32_bf16 a[24:27], v[56:59], v[168:171], a[24:27]\n" \
"v_mfma_f32_16x16x32_bf16 a[24:27], v[52:55], v[164:167], a[24:27]\n" \
"v_mfma_f32_16x16x32_bf16 a[56:59], v[60:63], v[164:167], a[56:59]\n" \
"v_mfma_f32_16x16x32_bf16 a[56:59], v[64:67], v[168:171], a[56:59]\n" \
"v_mfma_f32_16x16x32_bf16 a[88:91], v[72:75], v[168:171], a[88:91]\n" \
"v_mfma_f32_16x16x32_bf16 a[88:91], v[68:71], v[164:167], a[88:91]\n" \
"s_mov_b32 exec_lo, 0x1fffff\n" \
"s_mov_b32 exec_hi, 0x0\n" \
"global_load_lds_dword v[16:17], off\n" \
"s_mov_b64 exec, -1\n" \
"s_nop 0\n" \
"v_mfma_f32_16x16x32_bf16 a[120:123], v[76:79], v[164:167], a[120:123]\n" \
"v_mfma_f32_16x16x32_bf16 a[120:123], v[80:83], v[168:171], a[120:123]\n" \
"v_mfma_f32_16x16x32_bf16 a[28:31], v[56:59], v[176:179], a[28:31]\n" \
"v_mfma_f32_16x16x32_bf16 a[28:31], v[52:55], v[172:175], a[28:31]\n" \
"v_mfma_f32_16x16x32_bf16 a[60:63], v[60:63], v[172:175], a[60:63]\n" \
"v_mfma_f32_16x16x32_bf16 a[60:63], v[64:67], v[176:179], a[60:63]\n" \
"v_mfma_f32_16x16x32_bf16 a[92:95], v[72:75], v[176:179], a[92:95]\n" \
"v_mfma_f32_16x16x32_bf16 a[92:95], v[68:71], v[172:175], a[92:95]\n" \
"v_mfma_f32_16x16x32_bf16 a[124:127], v[76:79], v[172:175], a[124:127]\n" \
"v_mfma_f32_16x16x32_bf16 a[124:127], v[80:83], v[176:179], a[124:127]\n" \
"v_mfma_f32_16x16x32_bf16 a[0:3], v[120:123], v[56:59], a[0:3]\n" \
"v_mfma_f32_16x16x32_bf16 a[0:3], v[116:119], v[52:55], a[0:3]\n" \
"v_mfma_f32_16x16x32_bf16 a[32:35], v[116:119], v[60:63], a[32:35]\n" \
"v_mfma_f32_16x16x32_bf16 a[32:35], v[120:123], v[64:67], a[32:35]\n" \
"v_mfma_f32_16x16x32_bf16 a[64:67], v[120:123], v[72:75], a[64:67]\n" \
"v_mfma_f32_16x16x32_bf16 a[64:67], v[116:119], v[68:71], a[64:67]\n" \
"v_mfma_f32_16x16x32_bf16 a[96:99], v[116:119], v[76:79], a[96:99]\n" \
"v_mfma_f32_16x16x32_bf16 a[96:99], v[120:123], v[80:83], a[96:99]\n" \
"v_mfma_f32_16x16x32_bf16 a[4:7], v[128:131], v[56:59], a[4:7]\n" \
"v_mfma_f32_16x16x32_bf16 a[4:7], v[124:127], v[52:55], a[4:7]\n" \
"v_mfma_f32_16x16x32_bf16 a[36:39], v[124:127], v[60:63], a[36:39]\n" \
"v_mfma_f32_16x16x32_bf16 a[36:39], v[128:131], v[64:67], a[36:39]\n" \
"v_mfma_f32_16x16x32_bf16 a[68:71], v[128:131], v[72:75], a[68:71]\n" \
"v_mfma_f32_16x16x32_bf16 a[68:71], v[124:127], v[68:71], a[68:71]\n" \
"v_mfma_f32_16x16x32_bf16 a[100:103], v[124:127], v[76:79], a[100:103]\n" \
"v_mfma_f32_16x16x32_bf16 a[100:103], v[128:131], v[80:83], a[100:103]\n" \
"s_waitcnt lgkmcnt(0)\n" \
"s_nop 0\n" \
"v_mfma_f32_16x16x32_bf16 a[8:11], v[136:139], v[56:59], a[8:11]\n" \
"v_mfma_f32_16x16x32_bf16 a[8:11], v[132:135], v[52:55], a[8:11]\n" \
"v_mfma_f32_16x16x32_bf16 a[40:43], v[132:135], v[60:63], a[40:43]\n" \
"v_mfma_f32_16x16x32_bf16 a[40:43], v[136:139], v[64:67], a[40:43]\n" \
"v_mfma_f32_16x16x32_bf16 a[72:75], v[136:139], v[72:75], a[72:75]\n" \
"v_mfma_f32_16x16x32_bf16 a[72:75], v[132:135], v[68:71], a[72:75]\n" \
"v_mfma_f32_16x16x32_bf16 a[104:107], v[132:135], v[76:79], a[104:107]\n" \
"v_mfma_f32_16x16x32_bf16 a[104:107], v[136:139], v[80:83], a[104:107]\n" \
"v_mfma_f32_16x16x32_bf16 a[12:15], v[144:147], v[56:59], a[12:15]\n" \
"s_add_u32 s62, %[rc], 0x180\n" \
"s_and_b32 s62, s62, 0x3ff\n" \
"v_mfma_f32_16x16x32_bf16 a[12:15], v[140:143], v[52:55], a[12:15]\n" \
"s_lshl_b32 s63, s62, 12\n" \
"v_mfma_f32_16x16x32_bf16 a[44:47], v[140:143], v[60:63], a[44:47]\n" \
"v_mfma_f32_16x16x32_bf16 a[44:47], v[144:147], v[64:67], a[44:47]\n" \
"v_mfma_f32_16x16x32_bf16 a[76:79], v[144:147], v[72:75], a[76:79]\n" \
"v_mfma_f32_16x16x32_bf16 a[76:79], v[140:143], v[68:71], a[76:79]\n" \
"v_mfma_f32_16x16x32_bf16 a[108:111], v[140:143], v[76:79], a[108:111]\n" \
"v_mfma_f32_16x16x32_bf16 a[108:111], v[144:147], v[80:83], a[108:111]\n" \
"s_waitcnt vmcnt(1)\n" \
"s_barrier\n" \
"v_mfma_f32_16x16x32_bf16 a[128:131], v[120:123], v[88:91], a[128:131]\n" \
"s_add_u32 m0, %[mA], 0x8000\n" \
"s_add_u32 s85, s62, 0x0\n" \
"v_mfma_f32_16x16x32_bf16 a[128:131], v[116:119], v[84:87], a[128:131]\n" \
"buffer_load_dwordx4 v22, s[64:67], s85 offen lds\n" \
"v_mfma_f32_16x16x32_bf16 a[160:163], v[116:119], v[92:95], a[160:163]\n" \
"ds_read_b128 v[56:59], v29 offset:16384\n" \
"ds_read_b128 v[52:55], v28 offset:16384\n" \
"v_mfma_f32_16x16x32_bf16 a[160:163], v[120:123], v[96:99], a[160:163]\n" \
"s_add_u32 m0, %[mA], 0x8400\n" \
"s_add_u32 s84, s62, 0x2000\n" \
"v_mfma_f32_16x16x32_bf16 a[192:195], v[120:123], v[104:107], a[192:195]\n" \
"buffer_load_dwordx4 v23, s[64:67], s84 offen lds\n" \
"v_mfma_f32_16x16x32_bf16 a[192:195], v[116:119], v[100:103], a[192:195]\n" \
"ds_read_b128 v[60:63], v28 offset:18432\n" \
"ds_read_b128 v[64:67], v29 offset:18432\n" \
"v_mfma_f32_16x16x32_bf16 a[224:227], v[116:119], v[108:111], a[224:227]\n" \
"ds_read_b64_tr_b16 v[116:117], v32 offset:0\n" \
"ds_read_b64_tr_b16 v[118:119], v32 offset:1024\n" \
"v_mfma_f32_16x16x32_bf16 a[224:227], v[120:123], v[112:115], a[224:227]\n" \
"s_add_u32 m0, %[mB], 0x8000\n" \
"s_add_u32 s85, s63, 0x0\n" \
"v_mfma_f32_16x16x32_bf16 a[132:135], v[128:131], v[88:91], a[132:135]\n" \
"buffer_load_dwordx4 v26, s[68:71], s85 offen lds\n" \
"v_mfma_f32_16x16x32_bf16 a[132:135], v[124:127], v[84:87], a[132:135]\n" \
"ds_read_b64_tr_b16 v[120:121], v32 offset:8192\n" \
"ds_read_b64_tr_b16 v[122:123], v32 offset:9216\n" \
"v_mfma_f32_16x16x32_bf16 a[164:167], v[124:127], v[92:95], a[164:167]\n" \
"ds_read_b128 v[72:75], v29 offset:20480\n" \
"ds_read_b128 v[68:71], v28 offset:20480\n" \
"v_mfma_f32_16x16x32_bf16 a[164:167], v[128:131], v[96:99], a[164:167]\n" \
"s_add_u32 m0, %[mB], 0xc000\n" \
"s_add_u32 s84, s63, 0x0\n" \
"v_mfma_f32_16x16x32_bf16 a[196:199], v[128:131], v[104:107], a[196:199]\n" \
"buffer_load_dwordx4 v26, s[72:75], s84 offen lds\n" \
"v_mfma_f32_16x16x32_bf16 a[196:199], v[124:127], v[100:103], a[196:199]\n" \
"ds_read_b128 v[76:79], v28 offset:22528\n" \
"ds_read_b128 v[80:83], v29 offset:22528\n" \
"v_mfma_f32_16x16x32_bf16 a[228:231], v[124:127], v[108:111], a[228:231]\n" \
"ds_read_b64_tr_b16 v[124:125], v33 offset:0\n" \
"ds_read_b64_tr_b16 v[126:127], v33 offset:1024\n" \
"v_mfma_f32_16x16x32_bf16 a[228:231], v[128:131], v[112:115], a[228:231]\n" \
"s_add_u32 m0, %[mA], 0x8800\n" \
"s_add_u32 s85, s62, 0x4000\n" \
"v_mfma_f32_16x16x32_bf16 a[136:139], v[136:139], v[88:91], a[136:139]\n" \
"buffer_load_dwordx4 v22, s[64:67], s85 offen lds\n" \
"v_mfma_f32_16x16x32_bf16 a[136:139], v[132:135], v[84:87], a[136:139]\n" \
"ds_read_b64_tr_b16 v[128:129], v33 offset:8192\n" \
"ds_read_b64_tr_b16 v[130:131], v33 offset:9216\n" \
"v_mfma_f32_16x16x32_bf16 a[168:171], v[132:135], v[92:95], a[168:171]\n" \
"v_mfma_f32_16x16x32_bf16 a[168:171], v[136:139], v[96:99], a[168:171]\n" \
"s_add_u32 m0, %[mA], 0x8c00\n" \
"s_add_u32 s84, s62, 0x6000\n" \
"v_mfma_f32_16x16x32_bf16 a[200:203], v[136:139], v[104:107], a[200:203]\n" \
"buffer_load_dwordx4 v23, s[64:67], s84 offen lds\n" \
"v_mfma_f32_16x16x32_bf16 a[200:203], v[132:135], v[100:103], a[200:203]\n" \
"v_mfma_f32_16x16x32_bf16 a[232:235], v[132:135], v[108:111], a[232:235]\n" \
"ds_read_b64_tr_b16 v[132:133], v34 offset:0\n" \
"ds_read_b64_tr_b16 v[134:135], v34 offset:1024\n" \
"v_mfma_f32_16x16x32_bf16 a[232:235], v[136:139], v[112:115], a[232:235]\n" \
"s_add_u32 m0, %[mB], 0x8400\n" \
"s_add_u32 s85, s63, 0x8000\n" \
"v_mfma_f32_16x16x32_bf16 a[140:143], v[144:147], v[88:91], a[140:143]\n" \
"buffer_load_dwordx4 v26, s[68:71], s85 offen lds\n" \
"v_mfma_f32_16x16x32_bf16 a[140:143], v[140:143], v[84:87], a[140:143]\n" \
"ds_read_b64_tr_b16 v[136:137], v34 offset:8192\n" \
"ds_read_b64_tr_b16 v[138:139], v34 offset:9216\n" \
"v_mfma_f32_16x16x32_bf16 a[172:175], v[140:143], v[92:95], a[172:175]\n" \
"v_mfma_f32_16x16x32_bf16 a[172:175], v[144:147], v[96:99], a[172:175]\n" \
"s_add_u32 m0, %[mB], 0xc400\n" \
"s_add_u32 s84, s63, 0x8000\n" \
"v_mfma_f32_16x16x32_bf16 a[204:207], v[144:147], v[104:107], a[204:207]\n" \
"buffer_load_dwordx4 v26, s[72:75], s84 offen lds\n" \
"v_mfma_f32_16x16x32_bf16 a[204:207], v[140:143], v[100:103], a[204:207]\n" \
"v_mfma_f32_16x16x32_bf16 a[236:239], v[140:143], v[108:111], a[236:239]\n" \
"ds_read_b64_tr_b16 v[140:141], v35 offset:0\n" \
"ds_read_b64_tr_b16 v[142:143], v35 offset:1024\n" \
"v_mfma_f32_16x16x32_bf16 a[236:239], v[144:147], v[112:115], a[236:239]\n" \
"s_add_u32 m0, %[mA], 0x9000\n" \
"s_add_u32 s85, s62, 0x8000\n" \
"v_mfma_f32_16x16x32_bf16 a[144:147], v[88:91], v[152:155], a[144:147]\n" \
"buffer_load_dwordx4 v22, s[64:67], s85 offen lds\n" \
"v_mfma_f32_16x16x32_bf16 a[144:147], v[84:87], v[148:151], a[144:147]\n" \
"ds_read_b64_tr_b16 v[144:145], v35 offset:8192\n" \
"ds_read_b64_tr_b16 v[146:147], v35 offset:9216\n" \
"v_mfma_f32_16x16x32_bf16 a[176:179], v[92:95], v[148:151], a[176:179]\n" \
"v_mfma_f32_16x16x32_bf16 a[176:179], v[96:99], v[152:155], a[176:179]\n" \
"s_add_u32 m0, %[mA], 0x9400\n" \
"s_add_u32 s84, s62, 0xa000\n" \
"v_mfma_f32_16x16x32_bf16 a[208:211], v[104:107], v[152:155], a[208:211]\n" \
"buffer_load_dwordx4 v23, s[64:67], s84 offen lds\n" \
"v_mfma_f32_16x16x32_bf16 a[208:211], v[100:103], v[148:151], a[208:211]\n" \
"v_mfma_f32_16x16x32_bf16 a[240:243], v[108:111], v[148:151], a[240:243]\n" \
"ds_read_b64_tr_b16 v[148:149], v32 offset:16384\n" \
"ds_read_b64_tr_b16 v[150:151], v32 offset:17408\n" \
"v_mfma_f32_16x16x32_bf16 a[240:243], v[112:115], v[152:155], a[240:243]\n" \
"s_add_u32 m0, %[mB], 0x8800\n" \
"s_add_u32 s85, s63, 0x10000\n" \
"v_mfma_f32_16x16x32_bf16 a[148:151], v[88:91], v[160:163], a[148:151]\n" \
"buffer_load_dwordx4 v27, s[68:71], s85 offen lds\n" \
"v_mfma_f32_16x16x32_bf16 a[148:151], v[84:87], v[156:159], a[148:151]\n" \
"ds_read_b64_tr_b16 v[152:153], v32 offset:24576\n" \
"ds_read_b64_tr_b16 v[154:155], v32 offset:25600\n" \
"v_mfma_f32_16x16x32_bf16 a[180:183], v[92:95], v[156:159], a[180:183]\n" \
"v_mfma_f32_16x16x32_bf16 a[180:183], v[96:99], v[160:163], a[180:183]\n" \
"s_add_u32 m0, %[mB], 0xc800\n" \
"s_add_u32 s84, s63, 0x10000\n" \
"v_mfma_f32_16x16x32_bf16 a[212:215], v[104:107], v[160:163], a[212:215]\n" \
"buffer_load_dwordx4 v27, s[72:75], s84 offen lds\n" \
"v_mfma_f32_16x16x32_bf16 a[212:215], v[100:103], v[156:159], a[212:215]\n" \
"v_mfma_f32_16x16x32_bf16 a[244:247], v[108:111], v[156:159], a[244:247]\n" \
"ds_read_b64_tr_b16 v[156:157], v33 offset:16384\n" \
"ds_read_b64_tr_b16 v[158:159], v33 offset:17408\n" \
"v_mfma_f32_16x16x32_bf16 a[244:247], v[112:115], v[160:163], a[244:247]\n" \
"ds_read_b64_tr_b16 v[160:161], v33 offset:24576\n" \
"ds_read_b64_tr_b16 v[162:163], v33 offset:25600\n" \
"v_mfma_f32_16x16x32_bf16 a[152:155], v[88:91], v[168:171], a[152:155]\n" \
"v_mfma_f32_16x16x32_bf16 a[152:155], v[84:87], v[164:167], a[152:155]\n" \
"v_mfma_f32_16x16x32_bf16 a[184:187], v[92:95], v[164:167], a[184:187]\n" \
"v_mfma_f32_16x16x32_bf16 a[184:187], v[96:99], v[168:171], a[184:187]\n" \
"v_mfma_f32_16x16x32_bf16 a[216:219], v[104:107], v[168:171], a[216:219]\n" \
"v_mfma_f32_16x16x32_bf16 a[216:219], v[100:103], v[164:167], a[216:219]\n" \
"v_mfma_f32_16x16x32_bf16 a[248:251], v[108:111], v[164:167], a[248:251]\n" \
"ds_read_b64_tr_b16 v[164:165], v34 offset:16384\n" \
"ds_read_b64_tr_b16 v[166:167], v34 offset:17408\n" \
"v_mfma_f32_16x16x32_bf16 a[248:251], v[112:115], v[168:171], a[248:251]\n" \
"ds_read_b64_tr_b16 v[168:169], v34 offset:24576\n" \
"ds_read_b64_tr_b16 v[170:171], v34 offset:25600\n" \
"v_mfma_f32_16x16x32_bf16 a[156:159], v[88:91], v[176:179], a[156:159]\n" \
"ds_read_b128 v[88:91], v29 offset:0\n" \
"v_mfma_f32_16x16x32_bf16 a[156:159], v[84:87], v[172:175], a[156:159]\n" \
"ds_read_b128 v[84:87], v28 offset:0\n" \
"v_mfma_f32_16x16x32_bf16 a[188:191], v[92:95], v[172:175], a[188:191]\n" \
"ds_read_b128 v[92:95], v28 offset:2048\n" \
"v_mfma_f32_16x16x32_bf16 a[188:191], v[96:99], v[176:179], a[188:191]\n" \
"ds_read_b128 v[96:99], v29 offset:2048\n" \
"v_mfma_f32_16x16x32_bf16 a[220:223], v[104:107], v[176:179], a[220:223]\n" \
"ds_read_b128 v[104:107], v29 offset:4096\n" \
"v_mfma_f32_16x16x32_bf16 a[220:223], v[100:103], v[172:175], a[220:223]\n" \
"ds_read_b128 v[100:103], v28 offset:4096\n" \
"v_mfma_f32_16x16x32_bf16 a[252:255], v[108:111], v[172:175], a[252:255]\n" \
"ds_read_b64_tr_b16 v[172:173], v35 offset:16384\n" \
"ds_read_b64_tr_b16 v[174:175], v35 offset:17408\n" \
"v_mfma_f32_16x16x32_bf16 a[252:255], v[112:115], v[176:179], a[252:255]\n" \
"s_add_u32 m0, %[mA], 0x9800\n" \
"s_waitcnt lgkmcnt(12)\n" \
"s_nop 0\n" \
"v_mfma_f32_16x16x32_bf16 a[0:3], v[120:123], v[56:59], a[0:3]\n" \
"s_add_u32 s85, s62, 0xc000\n" \
"ds_read_b64_tr_b16 v[176:177], v35 offset:24576\n" \
"v_mfma_f32_16x16x32_bf16 a[0:3], v[116:119], v[52:55], a[0:3]\n" \
"buffer_load_dwordx4 v22, s[64:67], s85 offen lds\n" \
"v_mfma_f32_16x16x32_bf16 a[32:35], v[116:119], v[60:63], a[32:35]\n" \
"s_add_u32 m0, %[mA], 0x9c00\n" \
"s_add_u32 s84, s62, 0xe000\n" \
"v_mfma_f32_16x16x32_bf16 a[32:35], v[120:123], v[64:67], a[32:35]\n" \
"ds_read_b64_tr_b16 v[178:179], v35 offset:25600\n" \
"ds_read_b128 v[108:111], v28 offset:6144\n" \
"v_mfma_f32_16x16x32_bf16 a[64:67], v[120:123], v[72:75], a[64:67]\n" \
"buffer_load_dwordx4 v23, s[64:67], s84 offen lds\n" \
"v_mfma_f32_16x16x32_bf16 a[64:67], v[116:119], v[68:71], a[64:67]\n" \
"s_add_u32 m0, %[mB], 0x8c00\n" \
"s_add_u32 s85, s63, 0x18000\n" \
"v_mfma_f32_16x16x32_bf16 a[96:99], v[116:119], v[76:79], a[96:99]\n" \
"ds_read_b128 v[112:115], v29 offset:6144\n" \
"v_mfma_f32_16x16x32_bf16 a[96:99], v[120:123], v[80:83], a[96:99]\n" \
"buffer_load_dwordx4 v27, s[68:71], s85 offen lds\n" \
"v_mfma_f32_16x16x32_bf16 a[4:7], v[128:131], v[56:59], a[4:7]\n" \
"s_add_u32 m0, %[mB], 0xcc00\n" \
"s_add_u32 s84, s63, 0x18000\n" \
"v_mfma_f32_16x16x32_bf16 a[4:7], v[124:127], v[52:55], a[4:7]\n" \
"v_mfma_f32_16x16x32_bf16 a[36:39], v[124:127], v[60:63], a[36:39]\n" \
"buffer_load_dwordx4 v27, s[72:75], s84 offen lds\n" \
"v_mfma_f32_16x16x32_bf16 a[36:39], v[128:131], v[64:67], a[36:39]\n" \
"v_mfma_f32_16x16x32_bf16 a[68:71], v[128:131], v[72:75], a[68:71]\n" \
"v_mfma_f32_16x16x32_bf16 a[68:71], v[124:127], v[68:71], a[68:71]\n" \
"buffer_load_dwordx4 v[200:203], v192, s[76:79], %[oW] offen offset:0\n" \
"v_mfma_f32_16x16x32_bf16 a[100:103], v[124:127], v[76:79], a[100:103]\n" \
"v_mfma_f32_16x16x32_bf16 a[100:103], v[128:131], v[80:83], a[100:103]\n" \
"v_mfma_f32_16x16x32_bf16 a[8:11], v[136:139], v[56:59], a[8:11]\n" \
"v_mfma_f32_16x16x32_bf16 a[8:11], v[132:135], v[52:55], a[8:11]\n" \
"v_mfma_f32_16x16x32_bf16 a[40:43], v[132:135], v[60:63], a[40:43]\n" \
"buffer_load_dwordx4 v[204:207], v193, s[76:79], %[oW] offen offset:0\n" \
"v_mfma_f32_16x16x32_bf16 a[40:43], v[136:139], v[64:67], a[40:43]\n" \
"v_mfma_f32_16x16x32_bf16 a[72:75], v[136:139], v[72:75], a[72:75]\n" \
"v_mfma_f32_16x16x32_bf16 a[72:75], v[132:135], v[68:71], a[72:75]\n" \
"v_mfma_f32_16x16x32_bf16 a[104:107], v[132:135], v[76:79], a[104:107]\n" \
"v_mfma_f32_16x16x32_bf16 a[104:107], v[136:139], v[80:83], a[104:107]\n" \
"v_mfma_f32_16x16x32_bf16 a[12:15], v[144:147], v[56:59], a[12:15]\n" \
"buffer_load_dword v208, v194, s[76:79], %[oW] offen offset:32\n" \
"v_mfma_f32_16x16x32_bf16 a[12:15], v[140:143], v[52:55], a[12:15]\n" \
"v_mfma_f32_16x16x32_bf16 a[44:47], v[140:143], v[60:63], a[44:47]\n" \
"v_mfma_f32_16x16x32_bf16 a[44:47], v[144:147], v[64:67], a[44:47]\n" \
"v_mfma_f32_16x16x32_bf16 a[76:79], v[144:147], v[72:75], a[76:79]\n" \
"v_mfma_f32_16x16x32_bf16 a[76:79], v[140:143], v[68:71], a[76:79]\n" \
"v_mfma_f32_16x16x32_bf16 a[108:111], v[140:143], v[76:79], a[108:111]\n" \
"buffer_load_dword v209, v194, s[76:79], %[oW] offen offset:1184\n" \
"v_mfma_f32_16x16x32_bf16 a[108:111], v[144:147], v[80:83], a[108:111]\n" \
"v_mfma_f32_16x16x32_bf16 a[16:19], v[56:59], v[152:155], a[16:19]\n" \
"v_mfma_f32_16x16x32_bf16 a[16:19], v[52:55], v[148:151], a[16:19]\n" \
"v_mfma_f32_16x16x32_bf16 a[48:51], v[60:63], v[148:151], a[48:51]\n" \
"v_mfma_f32_16x16x32_bf16 a[48:51], v[64:67], v[152:155], a[48:51]\n" \
"v_mfma_f32_16x16x32_bf16 a[80:83], v[72:75], v[152:155], a[80:83]\n" \
"buffer_load_dword v211, v194, s[76:79], %[oW] offen offset:2336\n" \
"v_mfma_f32_16x16x32_bf16 a[80:83], v[68:71], v[148:151], a[80:83]\n" \
"s_mov_b32 s76, %[gwl]\n" \
"s_and_b32 s77, %[gwh], 0xffff\n" \
"v_mfma_f32_16x16x32_bf16 a[112:115], v[76:79], v[148:151], a[112:115]\n" \
"s_mov_b32 s78, 0x480\n" \
"v_mfma_f32_16x16x32_bf16 a[112:115], v[80:83], v[152:155], a[112:115]\n" \
"v_mfma_f32_16x16x32_bf16 a[20:23], v[56:59], v[160:163], a[20:23]\n" \
"v_mfma_f32_16x16x32_bf16 a[20:23], v[52:55], v[156:159], a[20:23]\n" \
"v_mfma_f32_16x16x32_bf16 a[52:55], v[60:63], v[156:159], a[52:55]\n" \
"buffer_load_ushort v210, v195, s[76:79], 0 offen\n" \
"v_mfma_f32_16x16x32_bf16 a[52:55], v[64:67], v[160:163], a[52:55]\n" \
"s_mov_b32 s76, %[xl]\n" \
"s_and_b32 s77, %[xh], 0xffff\n" \
"v_mfma_f32_16x16x32_bf16 a[84:87], v[72:75], v[160:163], a[84:87]\n" \
"s_mov_b32 s78, 0x240000\n" \
"v_mfma_f32_16x16x32_bf16 a[84:87], v[68:71], v[156:159], a[84:87]\n" \
"v_mfma_f32_16x16x32_bf16 a[116:119], v[76:79], v[156:159], a[116:119]\n" \
"v_mfma_f32_16x16x32_bf16 a[116:119], v[80:83], v[160:163], a[116:119]\n" \
"s_waitcnt lgkmcnt(0)\n" \
"s_nop 0\n" \
"v_mfma_f32_16x16x32_bf16 a[24:27], v[56:59], v[168:171], a[24:27]\n" \
"v_mfma_f32_16x16x32_bf16 a[24:27], v[52:55], v[164:167], a[24:27]\n" \
"v_mfma_f32_16x16x32_bf16 a[56:59], v[60:63], v[164:167], a[56:59]\n" \
"v_mfma_f32_16x16x32_bf16 a[56:59], v[64:67], v[168:171], a[56:59]\n" \
"v_mfma_f32_16x16x32_bf16 a[88:91], v[72:75], v[168:171], a[88:91]\n" \
"v_mfma_f32_16x16x32_bf16 a[88:91], v[68:71], v[164:167], a[88:91]\n" \
"v_mfma_f32_16x16x32_bf16 a[120:123], v[76:79], v[164:167], a[120:123]\n" \
"v_mfma_f32_16x16x32_bf16 a[120:123], v[80:83], v[168:171], a[120:123]\n" \
"v_mfma_f32_16x16x32_bf16 a[28:31], v[56:59], v[176:179], a[28:31]\n" \
"s_add_u32 s62, %[rc], 0x200\n" \
"s_and_b32 s62, s62, 0x3ff\n" \
"v_mfma_f32_16x16x32_bf16 a[28:31], v[52:55], v[172:175], a[28:31]\n" \
"s_lshl_b32 s63, s62, 12\n" \
"v_mfma_f32_16x16x32_bf16 a[60:63], v[60:63], v[172:175], a[60:63]\n" \
"v_mfma_f32_16x16x32_bf16 a[60:63], v[64:67], v[176:179], a[60:63]\n" \
"v_mfma_f32_16x16x32_bf16 a[92:95], v[72:75], v[176:179], a[92:95]\n" \
"v_mfma_f32_16x16x32_bf16 a[92:95], v[68:71], v[172:175], a[92:95]\n" \
"v_mfma_f32_16x16x32_bf16 a[124:127], v[76:79], v[172:175], a[124:127]\n" \
"v_mfma_f32_16x16x32_bf16 a[124:127], v[80:83], v[176:179], a[124:127]\n" \
"s_waitcnt vmcnt(6)\n" \
"s_barrier\n" \
"v_mfma_f32_16x16x32_bf16 a[144:147], v[88:91], v[152:155], a[144:147]\n" \
"s_add_u32 m0, %[mA], 0x0\n" \
"s_add_u32 s85, s62, 0x0\n" \
"s_nop 0\n" \
"v_mfma_f32_16x16x32_bf16 a[144:147], v[84:87], v[148:151], a[144:147]\n" \
"buffer_load_dwordx4 v22, s[64:67], s85 offen lds\n" \
"v_mfma_f32_16x16x32_bf16 a[176:179], v[92:95], v[148:151], a[176:179]\n" \
"ds_read_b128 v[56:59], v29 offset:49152\n" \
"ds_read_b128 v[52:55], v28 offset:49152\n" \
"v_mfma_f32_16x16x32_bf16 a[176:179], v[96:99], v[152:155], a[176:179]\n" \
"s_add_u32 m0, %[mA], 0x400\n" \
"s_add_u32 s84, s62, 0x2000\n" \
"v_mfma_f32_16x16x32_bf16 a[208:211], v[104:107], v[152:155], a[208:211]\n" \
"buffer_load_dwordx4 v23, s[64:67], s84 offen lds\n" \
"v_mfma_f32_16x16x32_bf16 a[208:211], v[100:103], v[148:151], a[208:211]\n" \
"ds_read_b128 v[60:63], v28 offset:51200\n" \
"ds_read_b128 v[64:67], v29 offset:51200\n" \
"v_mfma_f32_16x16x32_bf16 a[240:243], v[108:111], v[148:151], a[240:243]\n" \
"ds_read_b64_tr_b16 v[148:149], v32 offset:49152\n" \
"ds_read_b64_tr_b16 v[150:151], v32 offset:50176\n" \
"v_mfma_f32_16x16x32_bf16 a[240:243], v[112:115], v[152:155], a[240:243]\n" \
"s_add_u32 m0, %[mB], 0x0\n" \
"s_add_u32 s85, s63, 0x0\n" \
"v_mfma_f32_16x16x32_bf16 a[148:151], v[88:91], v[160:163], a[148:151]\n" \
"buffer_load_dwordx4 v26, s[68:71], s85 offen lds\n" \
"v_mfma_f32_16x16x32_bf16 a[148:151], v[84:87], v[156:159], a[148:151]\n" \
"ds_read_b64_tr_b16 v[152:153], v32 offset:57344\n" \
"ds_read_b64_tr_b16 v[154:155], v32 offset:58368\n" \
"v_mfma_f32_16x16x32_bf16 a[180:183], v[92:95], v[156:159], a[180:183]\n" \
"ds_read_b128 v[72:75], v29 offset:53248\n" \
"ds_read_b128 v[68:71], v28 offset:53248\n" \
"v_mfma_f32_16x16x32_bf16 a[180:183], v[96:99], v[160:163], a[180:183]\n" \
"s_add_u32 m0, %[mB], 0x4000\n" \
"s_add_u32 s84, s63, 0x0\n" \
"s_nop 0\n" \
"v_mfma_f32_16x16x32_bf16 a[212:215], v[104:107], v[160:163], a[212:215]\n" \
"buffer_load_dwordx4 v26, s[72:75], s84 offen lds\n" \
"v_mfma_f32_16x16x32_bf16 a[212:215], v[100:103], v[156:159], a[212:215]\n" \
"ds_read_b128 v[76:79], v28 offset:55296\n" \
"ds_read_b128 v[80:83], v29 offset:55296\n" \
"v_mfma_f32_16x16x32_bf16 a[244:247], v[108:111], v[156:159], a[244:247]\n" \
"ds_read_b64_tr_b16 v[156:157], v33 offset:49152\n" \
"ds_read_b64_tr_b16 v[158:159], v33 offset:50176\n" \
"v_mfma_f32_16x16x32_bf16 a[244:247], v[112:115], v[160:163], a[244:247]\n" \
"s_add_u32 m0, %[mA], 0x800\n" \
"s_add_u32 s85, s62, 0x4000\n" \
"v_mfma_f32_16x16x32_bf16 a[152:155], v[88:91], v[168:171], a[152:155]\n" \
"buffer_load_dwordx4 v22, s[64:67], s85 offen lds\n" \
"v_mfma_f32_16x16x32_bf16 a[152:155], v[84:87], v[164:167], a[152:155]\n" \
"ds_read_b64_tr_b16 v[160:161], v33 offset:57344\n" \
"ds_read_b64_tr_b16 v[162:163], v33 offset:58368\n" \
"v_mfma_f32_16x16x32_bf16 a[184:187], v[92:95], v[164:167], a[184:187]\n" \
"v_mfma_f32_16x16x32_bf16 a[184:187], v[96:99], v[168:171], a[184:187]\n" \
"s_add_u32 m0, %[mA], 0xc00\n" \
"s_add_u32 s84, s62, 0x6000\n" \
"v_mfma_f32_16x16x32_bf16 a[216:219], v[104:107], v[168:171], a[216:219]\n" \
"buffer_load_dwordx4 v23, s[64:67], s84 offen lds\n" \
"v_mfma_f32_16x16x32_bf16 a[216:219], v[100:103], v[164:167], a[216:219]\n" \
"v_mfma_f32_16x16x32_bf16 a[248:251], v[108:111], v[164:167], a[248:251]\n" \
"ds_read_b64_tr_b16 v[164:165], v34 offset:49152\n" \
"ds_read_b64_tr_b16 v[166:167], v34 offset:50176\n" \
"v_mfma_f32_16x16x32_bf16 a[248:251], v[112:115], v[168:171], a[248:251]\n" \
"s_add_u32 m0, %[mB], 0x400\n" \
"s_add_u32 s85, s63, 0x8000\n" \
"v_mfma_f32_16x16x32_bf16 a[156:159], v[88:91], v[176:179], a[156:159]\n" \
"buffer_load_dwordx4 v26, s[68:71], s85 offen lds\n" \
"v_mfma_f32_16x16x32_bf16 a[156:159], v[84:87], v[172:175], a[156:159]\n" \
"ds_read_b64_tr_b16 v[168:169], v34 offset:57344\n" \
"ds_read_b64_tr_b16 v[170:171], v34 offset:58368\n" \
"v_mfma_f32_16x16x32_bf16 a[188:191], v[92:95], v[172:175], a[188:191]\n" \
"v_mfma_f32_16x16x32_bf16 a[188:191], v[96:99], v[176:179], a[188:191]\n" \
"s_add_u32 m0, %[mB], 0x4400\n" \
"s_add_u32 s84, s63, 0x8000\n" \
"v_mfma_f32_16x16x32_bf16 a[220:223], v[104:107], v[176:179], a[220:223]\n" \
"buffer_load_dwordx4 v26, s[72:75], s84 offen lds\n" \
"v_mfma_f32_16x16x32_bf16 a[220:223], v[100:103], v[172:175], a[220:223]\n" \
"v_mfma_f32_16x16x32_bf16 a[252:255], v[108:111], v[172:175], a[252:255]\n" \
"ds_read_b64_tr_b16 v[172:173], v35 offset:49152\n" \
"ds_read_b64_tr_b16 v[174:175], v35 offset:50176\n" \
"v_mfma_f32_16x16x32_bf16 a[252:255], v[112:115], v[176:179], a[252:255]\n" \
"s_add_u32 m0, %[mA], 0x1000\n" \
"s_add_u32 s85, s62, 0x8000\n" \
"v_mfma_f32_16x16x32_bf16 a[128:131], v[120:123], v[88:91], a[128:131]\n" \
"buffer_load_dwordx4 v22, s[64:67], s85 offen lds\n" \
"v_mfma_f32_16x16x32_bf16 a[128:131], v[116:119], v[84:87], a[128:131]\n" \
"ds_read_b64_tr_b16 v[176:177], v35 offset:57344\n" \
"ds_read_b64_tr_b16 v[178:179], v35 offset:58368\n" \
"v_mfma_f32_16x16x32_bf16 a[160:163], v[116:119], v[92:95], a[160:163]\n" \
"v_mfma_f32_16x16x32_bf16 a[160:163], v[120:123], v[96:99], a[160:163]\n" \
"s_add_u32 m0, %[mA], 0x1400\n" \
"s_add_u32 s84, s62, 0xa000\n" \
"v_mfma_f32_16x16x32_bf16 a[192:195], v[120:123], v[104:107], a[192:195]\n" \
"buffer_load_dwordx4 v23, s[64:67], s84 offen lds\n" \
"v_mfma_f32_16x16x32_bf16 a[192:195], v[116:119], v[100:103], a[192:195]\n" \
"v_mfma_f32_16x16x32_bf16 a[224:227], v[116:119], v[108:111], a[224:227]\n" \
"ds_read_b64_tr_b16 v[116:117], v32 offset:32768\n" \
"ds_read_b64_tr_b16 v[118:119], v32 offset:33792\n" \
"v_mfma_f32_16x16x32_bf16 a[224:227], v[120:123], v[112:115], a[224:227]\n" \
"s_add_u32 m0, %[mB], 0x800\n" \
"s_add_u32 s85, s63, 0x10000\n" \
"v_mfma_f32_16x16x32_bf16 a[132:135], v[128:131], v[88:91], a[132:135]\n" \
"buffer_load_dwordx4 v27, s[68:71], s85 offen lds\n" \
"v_mfma_f32_16x16x32_bf16 a[132:135], v[124:127], v[84:87], a[132:135]\n" \
"ds_read_b64_tr_b16 v[120:121], v32 offset:40960\n" \
"ds_read_b64_tr_b16 v[122:123], v32 offset:41984\n" \
"v_mfma_f32_16x16x32_bf16 a[164:167], v[124:127], v[92:95], a[164:167]\n" \
"v_mfma_f32_16x16x32_bf16 a[164:167], v[128:131], v[96:99], a[164:167]\n" \
"s_add_u32 m0, %[mB], 0x4800\n" \
"s_add_u32 s84, s63, 0x10000\n" \
"v_mfma_f32_16x16x32_bf16 a[196:199], v[128:131], v[104:107], a[196:199]\n" \
"buffer_load_dwordx4 v27, s[72:75], s84 offen lds\n" \
"v_mfma_f32_16x16x32_bf16 a[196:199], v[124:127], v[100:103], a[196:199]\n" \
"v_mfma_f32_16x16x32_bf16 a[228:231], v[124:127], v[108:111], a[228:231]\n" \
"ds_read_b64_tr_b16 v[124:125], v33 offset:32768\n" \
"ds_read_b64_tr_b16 v[126:127], v33 offset:33792\n" \
"v_mfma_f32_16x16x32_bf16 a[228:231], v[128:131], v[112:115], a[228:231]\n" \
"ds_read_b64_tr_b16 v[128:129], v33 offset:40960\n" \
"ds_read_b64_tr_b16 v[130:131], v33 offset:41984\n" \
"v_mfma_f32_16x16x32_bf16 a[136:139], v[136:139], v[88:91], a[136:139]\n" \
"v_mfma_f32_16x16x32_bf16 a[136:139], v[132:135], v[84:87], a[136:139]\n" \
"v_mfma_f32_16x16x32_bf16 a[168:171], v[132:135], v[92:95], a[168:171]\n" \
"v_mfma_f32_16x16x32_bf16 a[168:171], v[136:139], v[96:99], a[168:171]\n" \
"v_mfma_f32_16x16x32_bf16 a[200:203], v[136:139], v[104:107], a[200:203]\n" \
"v_mfma_f32_16x16x32_bf16 a[200:203], v[132:135], v[100:103], a[200:203]\n" \
"v_mfma_f32_16x16x32_bf16 a[232:235], v[132:135], v[108:111], a[232:235]\n" \
"ds_read_b64_tr_b16 v[132:133], v34 offset:32768\n" \
"ds_read_b64_tr_b16 v[134:135], v34 offset:33792\n" \
"v_mfma_f32_16x16x32_bf16 a[232:235], v[136:139], v[112:115], a[232:235]\n" \
"ds_read_b64_tr_b16 v[136:137], v34 offset:40960\n" \
"ds_read_b64_tr_b16 v[138:139], v34 offset:41984\n" \
"v_mfma_f32_16x16x32_bf16 a[140:143], v[144:147], v[88:91], a[140:143]\n" \
"ds_read_b128 v[88:91], v29 offset:32768\n" \
"v_mfma_f32_16x16x32_bf16 a[140:143], v[140:143], v[84:87], a[140:143]\n" \
"ds_read_b128 v[84:87], v28 offset:32768\n" \
"v_mfma_f32_16x16x32_bf16 a[172:175], v[140:143], v[92:95], a[172:175]\n" \
"ds_read_b128 v[92:95], v28 offset:34816\n" \
"v_mfma_f32_16x16x32_bf16 a[172:175], v[144:147], v[96:99], a[172:175]\n" \
"ds_read_b128 v[96:99], v29 offset:34816\n" \
"v_mfma_f32_16x16x32_bf16 a[204:207], v[144:147], v[104:107], a[204:207]\n" \
"ds_read_b128 v[104:107], v29 offset:36864\n" \
"v_mfma_f32_16x16x32_bf16 a[204:207], v[140:143], v[100:103], a[204:207]\n" \
"ds_read_b128 v[100:103], v28 offset:36864\n" \
"v_mfma_f32_16x16x32_bf16 a[236:239], v[140:143], v[108:111], a[236:239]\n" \
"ds_read_b64_tr_b16 v[140:141], v35 offset:32768\n" \
"ds_read_b64_tr_b16 v[142:143], v35 offset:33792\n" \
"v_mfma_f32_16x16x32_bf16 a[236:239], v[144:147], v[112:115], a[236:239]\n" \
"s_add_u32 m0, %[mA], 0x1800\n" \
"s_waitcnt lgkmcnt(12)\n" \
"s_nop 0\n" \
"v_mfma_f32_16x16x32_bf16 a[16:19], v[56:59], v[152:155], a[16:19]\n" \
"s_add_u32 s85, s62, 0xc000\n" \
"ds_read_b64_tr_b16 v[144:145], v35 offset:40960\n" \
"v_mfma_f32_16x16x32_bf16 a[16:19], v[52:55], v[148:151], a[16:19]\n" \
"buffer_load_dwordx4 v22, s[64:67], s85 offen lds\n" \
"v_mfma_f32_16x16x32_bf16 a[48:51], v[60:63], v[148:151], a[48:51]\n" \
"s_add_u32 m0, %[mA], 0x1c00\n" \
"s_add_u32 s84, s62, 0xe000\n" \
"v_mfma_f32_16x16x32_bf16 a[48:51], v[64:67], v[152:155], a[48:51]\n" \
"ds_read_b64_tr_b16 v[146:147], v35 offset:41984\n" \
"ds_read_b128 v[108:111], v28 offset:38912\n" \
"v_mfma_f32_16x16x32_bf16 a[80:83], v[72:75], v[152:155], a[80:83]\n" \
"buffer_load_dwordx4 v23, s[64:67], s84 offen lds\n" \
"v_mfma_f32_16x16x32_bf16 a[80:83], v[68:71], v[148:151], a[80:83]\n" \
"s_add_u32 m0, %[mB], 0xc00\n" \
"s_add_u32 s85, s63, 0x18000\n" \
"v_mfma_f32_16x16x32_bf16 a[112:115], v[76:79], v[148:151], a[112:115]\n" \
"ds_read_b128 v[112:115], v29 offset:38912\n" \
"v_mfma_f32_16x16x32_bf16 a[112:115], v[80:83], v[152:155], a[112:115]\n" \
"buffer_load_dwordx4 v27, s[68:71], s85 offen lds\n" \
"v_mfma_f32_16x16x32_bf16 a[20:23], v[56:59], v[160:163], a[20:23]\n" \
"s_add_u32 m0, %[mB], 0x4c00\n" \
"s_add_u32 s84, s63, 0x18000\n" \
"v_mfma_f32_16x16x32_bf16 a[20:23], v[52:55], v[156:159], a[20:23]\n" \
"s_waitcnt vmcnt(15)\n" \
"v_or_b32_e32 v206, v210, v206\n" \
"v_mfma_f32_16x16x32_bf16 a[52:55], v[60:63], v[156:159], a[52:55]\n" \
"buffer_load_dwordx4 v27, s[72:75], s84 offen lds\n" \
"v_mfma_f32_16x16x32_bf16 a[52:55], v[64:67], v[160:163], a[52:55]\n" \
"v_mfma_f32_16x16x32_bf16 a[84:87], v[72:75], v[160:163], a[84:87]\n" \
"v_mfma_f32_16x16x32_bf16 a[84:87], v[68:71], v[156:159], a[84:87]\n" \
"v_mfma_f32_16x16x32_bf16 a[116:119], v[76:79], v[156:159], a[116:119]\n" \
"v_mfma_f32_16x16x32_bf16 a[116:119], v[80:83], v[160:163], a[116:119]\n" \
"s_mov_b32 m0, %[mS]\n" \
"v_mfma_f32_16x16x32_bf16 a[24:27], v[56:59], v[168:171], a[24:27]\n" \
"v_mfma_f32_16x16x32_bf16 a[24:27], v[52:55], v[164:167], a[24:27]\n" \
"v_mfma_f32_16x16x32_bf16 a[56:59], v[60:63], v[164:167], a[56:59]\n" \
"v_mfma_f32_16x16x32_bf16 a[56:59], v[64:67], v[168:171], a[56:59]\n" \
"ds_write_b128 v196, v[200:203]\n" \
"v_mfma_f32_16x16x32_bf16 a[88:91], v[72:75], v[168:171], a[88:91]\n" \
"v_or_b32_e32 v204, v210, v204\n" \
"v_mov_b32_e64 v210, 0\n" \
"v_mfma_f32_16x16x32_bf16 a[88:91], v[68:71], v[164:167], a[88:91]\n" \
"ds_write_b128 v197, v[204:207]\n" \
"s_mov_b32 exec_lo, 0x3ffff\n" \
"s_mov_b32 exec_hi, 0x0\n" \
"global_load_lds_dword v[188:189], off\n" \
"s_mov_b64 exec, -1\n" \
"v_mfma_f32_16x16x32_bf16 a[120:123], v[76:79], v[164:167], a[120:123]\n" \
"ds_write_b128 v198, v[208:211]\n" \
"v_mfma_f32_16x16x32_bf16 a[120:123], v[80:83], v[168:171], a[120:123]\n" \
"v_mfma_f32_16x16x32_bf16 a[28:31], v[56:59], v[176:179], a[28:31]\n" \
"v_mfma_f32_16x16x32_bf16 a[28:31], v[52:55], v[172:175], a[28:31]\n" \
"v_mfma_f32_16x16x32_bf16 a[60:63], v[60:63], v[172:175], a[60:63]\n" \
"v_mfma_f32_16x16x32_bf16 a[60:63], v[64:67], v[176:179], a[60:63]\n" \
"v_mfma_f32_16x16x32_bf16 a[92:95], v[72:75], v[176:179], a[92:95]\n" \
"L_pf1:\n" \
"s_cbranch_execz L_pf5\n" \
"s_nop 0\n" \
"v_mfma_f32_16x16x32_bf16 a[92:95], v[68:71], v[172:175], a[92:95]\n" \
"v_mfma_f32_16x16x32_bf16 a[124:127], v[76:79], v[172:175], a[124:127]\n" \
"v_mfma_f32_16x16x32_bf16 a[124:127], v[80:83], v[176:179], a[124:127]\n" \
"v_mfma_f32_16x16x32_bf16 a[0:3], v[120:123], v[56:59], a[0:3]\n" \
"v_mfma_f32_16x16x32_bf16 a[0:3], v[116:119], v[52:55], a[0:3]\n" \
"v_mfma_f32_16x16x32_bf16 a[32:35], v[116:119], v[60:63], a[32:35]\n" \
"v_mfma_f32_16x16x32_bf16 a[32:35], v[120:123], v[64:67], a[32:35]\n" \
"v_mfma_f32_16x16x32_bf16 a[64:67], v[120:123], v[72:75], a[64:67]\n" \
"v_mfma_f32_16x16x32_bf16 a[64:67], v[116:119], v[68:71], a[64:67]\n" \
"v_mfma_f32_16x16x32_bf16 a[96:99], v[116:119], v[76:79], a[96:99]\n" \
"v_mfma_f32_16x16x32_bf16 a[96:99], v[120:123], v[80:83], a[96:99]\n" \
"v_mfma_f32_16x16x32_bf16 a[4:7], v[128:131], v[56:59], a[4:7]\n" \
"v_mfma_f32_16x16x32_bf16 a[4:7], v[124:127], v[52:55], a[4:7]\n" \
"v_mfma_f32_16x16x32_bf16 a[36:39], v[124:127], v[60:63], a[36:39]\n" \
"v_mfma_f32_16x16x32_bf16 a[36:39], v[128:131], v[64:67], a[36:39]\n" \
"v_mfma_f32_16x16x32_bf16 a[68:71], v[128:131], v[72:75], a[68:71]\n" \
"v_mfma_f32_16x16x32_bf16 a[68:71], v[124:127], v[68:71], a[68:71]\n" \
"v_mfma_f32_16x16x32_bf16 a[100:103], v[124:127], v[76:79], a[100:103]\n" \
"v_mfma_f32_16x16x32_bf16 a[100:103], v[128:131], v[80:83], a[100:103]\n" \
"s_waitcnt lgkmcnt(0)\n" \
"s_nop 0\n" \
"v_mfma_f32_16x16x32_bf16 a[8:11], v[136:139], v[56:59], a[8:11]\n" \
"v_mfma_f32_16x16x32_bf16 a[8:11], v[132:135], v[52:55], a[8:11]\n" \
"v_mfma_f32_16x16x32_bf16 a[40:43], v[132:135], v[60:63], a[40:43]\n" \
"v_mfma_f32_16x16x32_bf16 a[40:43], v[136:139], v[64:67], a[40:43]\n" \
"v_mfma_f32_16x16x32_bf16 a[72:75], v[136:139], v[72:75], a[72:75]\n" \
"v_mfma_f32_16x16x32_bf16 a[72:75], v[132:135], v[68:71], a[72:75]\n" \
"v_mfma_f32_16x16x32_bf16 a[104:107], v[132:135], v[76:79], a[104:107]\n" \
"v_mfma_f32_16x16x32_bf16 a[104:107], v[136:139], v[80:83], a[104:107]\n" \
"v_mfma_f32_16x16x32_bf16 a[12:15], v[144:147], v[56:59], a[12:15]\n" \
"s_add_u32 s62, %[rc], 0x280\n" \
"s_and_b32 s62, s62, 0x3ff\n" \
"v_mfma_f32_16x16x32_bf16 a[12:15], v[140:143], v[52:55], a[12:15]\n" \
"s_lshl_b32 s63, s62, 12\n" \
"v_mfma_f32_16x16x32_bf16 a[44:47], v[140:143], v[60:63], a[44:47]\n" \
"v_mfma_f32_16x16x32_bf16 a[44:47], v[144:147], v[64:67], a[44:47]\n" \
"v_mfma_f32_16x16x32_bf16 a[76:79], v[144:147], v[72:75], a[76:79]\n" \
"v_mfma_f32_16x16x32_bf16 a[76:79], v[140:143], v[68:71], a[76:79]\n" \
"v_mfma_f32_16x16x32_bf16 a[108:111], v[140:143], v[76:79], a[108:111]\n" \
"v_mfma_f32_16x16x32_bf16 a[108:111], v[144:147], v[80:83], a[108:111]\n" \
"s_waitcnt vmcnt(1)\n" \
"s_barrier\n" \
"v_mfma_f32_16x16x32_bf16 a[128:131], v[120:123], v[88:91], a[128:131]\n" \
"s_add_u32 m0, %[mA], 0x8000\n" \
"s_add_u32 s85, s62, 0x0\n" \
"v_mfma_f32_16x16x32_bf16 a[128:131], v[116:119], v[84:87], a[128:131]\n" \
"buffer_load_dwordx4 v22, s[64:67], s85 offen lds\n" \
"v_mfma_f32_16x16x32_bf16 a[160:163], v[116:119], v[92:95], a[160:163]\n" \
"ds_read_b128 v[56:59], v29 offset:16384\n" \
"ds_read_b128 v[52:55], v28 offset:16384\n" \
"v_mfma_f32_16x16x32_bf16 a[160:163], v[120:123], v[96:99], a[160:163]\n" \
"s_add_u32 m0, %[mA], 0x8400\n" \
"s_add_u32 s84, s62, 0x2000\n" \
"v_mfma_f32_16x16x32_bf16 a[192:195], v[120:123], v[104:107], a[192:195]\n" \
"buffer_load_dwordx4 v23, s[64:67], s84 offen lds\n" \
"v_mfma_f32_16x16x32_bf16 a[192:195], v[116:119], v[100:103], a[192:195]\n" \
"ds_read_b128 v[60:63], v28 offset:18432\n" \
"ds_read_b128 v[64:67], v29 offset:18432\n" \
"v_mfma_f32_16x16x32_bf16 a[224:227], v[116:119], v[108:111], a[224:227]\n" \
"ds_read_b64_tr_b16 v[116:117], v32 offset:0\n" \
"ds_read_b64_tr_b16 v[118:119], v32 offset:1024\n" \
"v_mfma_f32_16x16x32_bf16 a[224:227], v[120:123], v[112:115], a[224:227]\n" \
"s_add_u32 m0, %[mB], 0x8000\n" \
"s_add_u32 s85, s63, 0x0\n" \
"v_mfma_f32_16x16x32_bf16 a[132:135], v[128:131], v[88:91], a[132:135]\n" \
"buffer_load_dwordx4 v26, s[68:71], s85 offen lds\n" \
"v_mfma_f32_16x16x32_bf16 a[132:135], v[124:127], v[84:87], a[132:135]\n" \
"ds_read_b64_tr_b16 v[120:121], v32 offset:8192\n" \
"ds_read_b64_tr_b16 v[122:123], v32 offset:9216\n" \
"v_mfma_f32_16x16x32_bf16 a[164:167], v[124:127], v[92:95], a[164:167]\n" \
"ds_read_b128 v[72:75], v29 offset:20480\n" \
"ds_read_b128 v[68:71], v28 offset:20480\n" \
"v_mfma_f32_16x16x32_bf16 a[164:167], v[128:131], v[96:99], a[164:167]\n" \
"s_add_u32 m0, %[mB], 0xc000\n" \
"s_add_u32 s84, s63, 0x0\n" \
"v_mfma_f32_16x16x32_bf16 a[196:199], v[128:131], v[104:107], a[196:199]\n" \
"buffer_load_dwordx4 v26, s[72:75], s84 offen lds\n" \
"v_mfma_f32_16x16x32_bf16 a[196:199], v[124:127], v[100:103], a[196:199]\n" \
"ds_read_b128 v[76:79], v28 offset:22528\n" \
"ds_read_b128 v[80:83], v29 offset:22528\n" \
"v_mfma_f32_16x16x32_bf16 a[228:231], v[124:127], v[108:111], a[228:231]\n" \
"ds_read_b64_tr_b16 v[124:125], v33 offset:0\n" \
"ds_read_b64_tr_b16 v[126:127], v33 offset:1024\n" \
"v_mfma_f32_16x16x32_bf16 a[228:231], v[128:131], v[112:115], a[228:231]\n" \
"s_add_u32 m0, %[mA], 0x8800\n" \
"s_add_u32 s85, s62, 0x4000\n" \
"v_mfma_f32_16x16x32_bf16 a[136:139], v[136:139], v[88:91], a[136:139]\n" \
"buffer_load_dwordx4 v22, s[64:67], s85 offen lds\n" \
"v_mfma_f32_16x16x32_bf16 a[136:139], v[132:135], v[84:87], a[136:139]\n" \
"ds_read_b64_tr_b16 v[128:129], v33 offset:8192\n" \
"ds_read_b64_tr_b16 v[130:131], v33 offset:9216\n" \
"v_mfma_f32_16x16x32_bf16 a[168:171], v[132:135], v[92:95], a[168:171]\n" \
"ds_read_b128 v[200:203], v199 offset:0\n" \
"ds_read_b128 v[204:207], v199 offset:4096\n" \
"v_mfma_f32_16x16x32_bf16 a[168:171], v[136:139], v[96:99], a[168:171]\n" \
"s_add_u32 m0, %[mA], 0x8c00\n" \
"s_add_u32 s84, s62, 0x6000\n" \
"v_mfma_f32_16x16x32_bf16 a[200:203], v[136:139], v[104:107], a[200:203]\n" \
"buffer_load_dwordx4 v23, s[64:67], s84 offen lds\n" \
"v_mfma_f32_16x16x32_bf16 a[200:203], v[132:135], v[100:103], a[200:203]\n" \
"ds_read_b128 v[208:211], v199 offset:8192\n" \
"v_mfma_f32_16x16x32_bf16 a[232:235], v[132:135], v[108:111], a[232:235]\n" \
"ds_read_b64_tr_b16 v[132:133], v34 offset:0\n" \
"ds_read_b64_tr_b16 v[134:135], v34 offset:1024\n" \
"v_mfma_f32_16x16x32_bf16 a[232:235], v[136:139], v[112:115], a[232:235]\n" \
"s_add_u32 m0, %[mB], 0x8400\n" \
"s_add_u32 s85, s63, 0x8000\n" \
"v_mfma_f32_16x16x32_bf16 a[140:143], v[144:147], v[88:91], a[140:143]\n" \
"buffer_load_dwordx4 v26, s[68:71], s85 offen lds\n" \
"v_mfma_f32_16x16x32_bf16 a[140:143], v[140:143], v[84:87], a[140:143]\n" \
"ds_read_b64_tr_b16 v[136:137], v34 offset:8192\n" \
"ds_read_b64_tr_b16 v[138:139], v34 offset:9216\n" \
"v_mfma_f32_16x16x32_bf16 a[172:175], v[140:143], v[92:95], a[172:175]\n" \
"v_mfma_f32_16x16x32_bf16 a[172:175], v[144:147], v[96:99], a[172:175]\n" \
"s_add_u32 m0, %[mB], 0xc400\n" \
"s_add_u32 s84, s63, 0x8000\n" \
"v_mfma_f32_16x16x32_bf16 a[204:207], v[144:147], v[104:107], a[204:207]\n" \
"buffer_load_dwordx4 v26, s[72:75], s84 offen lds\n" \
"v_mfma_f32_16x16x32_bf16 a[204:207], v[140:143], v[100:103], a[204:207]\n" \
"v_mfma_f32_16x16x32_bf16 a[236:239], v[140:143], v[108:111], a[236:239]\n" \
"ds_read_b64_tr_b16 v[140:141], v35 offset:0\n" \
"ds_read_b64_tr_b16 v[142:143], v35 offset:1024\n" \
"v_mfma_f32_16x16x32_bf16 a[236:239], v[144:147], v[112:115], a[236:239]\n" \
"s_add_u32 m0, %[mA], 0x9000\n" \
"s_add_u32 s85, s62, 0x8000\n" \
"v_mfma_f32_16x16x32_bf16 a[144:147], v[88:91], v[152:155], a[144:147]\n" \
"buffer_load_dwordx4 v22, s[64:67], s85 offen lds\n" \
"v_mfma_f32_16x16x32_bf16 a[144:147], v[84:87], v[148:151], a[144:147]\n" \
"ds_read_b64_tr_b16 v[144:145], v35 offset:8192\n" \
"ds_read_b64_tr_b16 v[146:147], v35 offset:9216\n" \
"v_mfma_f32_16x16x32_bf16 a[176:179], v[92:95], v[148:151], a[176:179]\n" \
"v_mfma_f32_16x16x32_bf16 a[176:179], v[96:99], v[152:155], a[176:179]\n" \
"s_add_u32 m0, %[mA], 0x9400\n" \
"s_add_u32 s84, s62, 0xa000\n" \
"v_mfma_f32_16x16x32_bf16 a[208:211], v[104:107], v[152:155], a[208:211]\n" \
"buffer_load_dwordx4 v23, s[64:67], s84 offen lds\n" \
"v_mfma_f32_16x16x32_bf16 a[208:211], v[100:103], v[148:151], a[208:211]\n" \
"v_mfma_f32_16x16x32_bf16 a[240:243], v[108:111], v[148:151], a[240:243]\n" \
"ds_read_b64_tr_b16 v[148:149], v32 offset:16384\n" \
"ds_read_b64_tr_b16 v[150:151], v32 offset:17408\n" \
"v_mfma_f32_16x16x32_bf16 a[240:243], v[112:115], v[152:155], a[240:243]\n" \
"s_add_u32 m0, %[mB], 0x8800\n" \
"s_add_u32 s85, s63, 0x10000\n" \
"v_mfma_f32_16x16x32_bf16 a[148:151], v[88:91], v[160:163], a[148:151]\n" \
"buffer_load_dwordx4 v27, s[68:71], s85 offen lds\n" \
"v_mfma_f32_16x16x32_bf16 a[148:151], v[84:87], v[156:159], a[148:151]\n" \
"ds_read_b64_tr_b16 v[152:153], v32 offset:24576\n" \
"ds_read_b64_tr_b16 v[154:155], v32 offset:25600\n" \
"v_mfma_f32_16x16x32_bf16 a[180:183], v[92:95], v[156:159], a[180:183]\n" \
"v_mfma_f32_16x16x32_bf16 a[180:183], v[96:99], v[160:163], a[180:183]\n" \
"s_add_u32 m0, %[mB], 0xc800\n" \
"s_add_u32 s84, s63, 0x10000\n" \
"v_mfma_f32_16x16x32_bf16 a[212:215], v[104:107], v[160:163], a[212:215]\n" \
"buffer_load_dwordx4 v27, s[72:75], s84 offen lds\n" \
"v_mfma_f32_16x16x32_bf16 a[212:215], v[100:103], v[156:159], a[212:215]\n" \
"v_mfma_f32_16x16x32_bf16 a[244:247], v[108:111], v[156:159], a[244:247]\n" \
"ds_read_b64_tr_b16 v[156:157], v33 offset:16384\n" \
"ds_read_b64_tr_b16 v[158:159], v33 offset:17408\n" \
"v_mfma_f32_16x16x32_bf16 a[244:247], v[112:115], v[160:163], a[244:247]\n" \
"ds_read_b64_tr_b16 v[160:161], v33 offset:24576\n" \
"ds_read_b64_tr_b16 v[162:163], v33 offset:25600\n" \
"v_mfma_f32_16x16x32_bf16 a[152:155], v[88:91], v[168:171], a[152:155]\n" \
"v_mfma_f32_16x16x32_bf16 a[152:155], v[84:87], v[164:167], a[152:155]\n" \
"v_mfma_f32_16x16x32_bf16 a[184:187], v[92:95], v[164:167], a[184:187]\n" \
"v_mfma_f32_16x16x32_bf16 a[184:187], v[96:99], v[168:171], a[184:187]\n" \
"v_mfma_f32_16x16x32_bf16 a[216:219], v[104:107], v[168:171], a[216:219]\n" \
"v_mfma_f32_16x16x32_bf16 a[216:219], v[100:103], v[164:167], a[216:219]\n" \
"v_mfma_f32_16x16x32_bf16 a[248:251], v[108:111], v[164:167], a[248:251]\n" \
"ds_read_b64_tr_b16 v[164:165], v34 offset:16384\n" \
"ds_read_b64_tr_b16 v[166:167], v34 offset:17408\n" \
"v_mfma_f32_16x16x32_bf16 a[248:251], v[112:115], v[168:171], a[248:251]\n" \
"ds_read_b64_tr_b16 v[168:169], v34 offset:24576\n" \
"ds_read_b64_tr_b16 v[170:171], v34 offset:25600\n" \
"v_mfma_f32_16x16x32_bf16 a[156:159], v[88:91], v[176:179], a[156:159]\n" \
"ds_read_b128 v[88:91], v29 offset:0\n" \
"v_mfma_f32_16x16x32_bf16 a[156:159], v[84:87], v[172:175], a[156:159]\n" \
"ds_read_b128 v[84:87], v28 offset:0\n" \
"v_mfma_f32_16x16x32_bf16 a[188:191], v[92:95], v[172:175], a[188:191]\n" \
"ds_read_b128 v[92:95], v28 offset:2048\n" \
"v_mfma_f32_16x16x32_bf16 a[188:191], v[96:99], v[176:179], a[188:191]\n" \
"ds_read_b128 v[96:99], v29 offset:2048\n" \
"v_mfma_f32_16x16x32_bf16 a[220:223], v[104:107], v[176:179], a[220:223]\n" \
"ds_read_b128 v[104:107], v29 offset:4096\n" \
"v_mfma_f32_16x16x32_bf16 a[220:223], v[100:103], v[172:175], a[220:223]\n" \
"ds_read_b128 v[100:103], v28 offset:4096\n" \
"v_mfma_f32_16x16x32_bf16 a[252:255], v[108:111], v[172:175], a[252:255]\n" \
"ds_read_b64_tr_b16 v[172:173], v35 offset:16384\n" \
"ds_read_b64_tr_b16 v[174:175], v35 offset:17408\n" \
"v_mfma_f32_16x16x32_bf16 a[252:255], v[112:115], v[176:179], a[252:255]\n" \
"s_add_u32 m0, %[mA], 0x9800\n" \
"s_waitcnt lgkmcnt(12)\n" \
"s_nop 0\n" \
"v_mfma_f32_16x16x32_bf16 a[0:3], v[120:123], v[56:59], a[0:3]\n" \
"s_add_u32 s85, s62, 0xc000\n" \
"ds_read_b64_tr_b16 v[176:177], v35 offset:24576\n" \
"v_mfma_f32_16x16x32_bf16 a[0:3], v[116:119], v[52:55], a[0:3]\n" \
"buffer_load_dwordx4 v22, s[64:67], s85 offen lds\n" \
"v_mfma_f32_16x16x32_bf16 a[32:35], v[116:119], v[60:63], a[32:35]\n" \
"s_add_u32 m0, %[mA], 0x9c00\n" \
"s_add_u32 s84, s62, 0xe000\n" \
"v_mfma_f32_16x16x32_bf16 a[32:35], v[120:123], v[64:67], a[32:35]\n" \
"ds_read_b64_tr_b16 v[178:179], v35 offset:25600\n" \
"ds_read_b128 v[108:111], v28 offset:6144\n" \
"v_mfma_f32_16x16x32_bf16 a[64:67], v[120:123], v[72:75], a[64:67]\n" \
"buffer_load_dwordx4 v23, s[64:67], s84 offen lds\n" \
"v_mfma_f32_16x16x32_bf16 a[64:67], v[116:119], v[68:71], a[64:67]\n" \
"s_add_u32 m0, %[mB], 0x8c00\n" \
"s_add_u32 s85, s63, 0x18000\n" \
"v_mfma_f32_16x16x32_bf16 a[96:99], v[116:119], v[76:79], a[96:99]\n" \
"ds_read_b128 v[112:115], v29 offset:6144\n" \
"v_mfma_f32_16x16x32_bf16 a[96:99], v[120:123], v[80:83], a[96:99]\n" \
"buffer_load_dwordx4 v27, s[68:71], s85 offen lds\n" \
"v_mfma_f32_16x16x32_bf16 a[4:7], v[128:131], v[56:59], a[4:7]\n" \
"s_add_u32 m0, %[mB], 0xcc00\n" \
"s_add_u32 s84, s63, 0x18000\n" \
"v_mfma_f32_16x16x32_bf16 a[4:7], v[124:127], v[52:55], a[4:7]\n" \
"v_mfma_f32_16x16x32_bf16 a[36:39], v[124:127], v[60:63], a[36:39]\n" \
"buffer_load_dwordx4 v27, s[72:75], s84 offen lds\n" \
"v_mfma_f32_16x16x32_bf16 a[36:39], v[128:131], v[64:67], a[36:39]\n" \
"v_mfma_f32_16x16x32_bf16 a[68:71], v[128:131], v[72:75], a[68:71]\n" \
"v_mfma_f32_16x16x32_bf16 a[68:71], v[124:127], v[68:71], a[68:71]\n" \
"buffer_store_dwordx4 v[200:203], v20, s[80:83], %[oLs] offen sc1\n" \
"v_mfma_f32_16x16x32_bf16 a[100:103], v[124:127], v[76:79], a[100:103]\n" \
"v_mfma_f32_16x16x32_bf16 a[100:103], v[128:131], v[80:83], a[100:103]\n" \
"v_mfma_f32_16x16x32_bf16 a[8:11], v[136:139], v[56:59], a[8:11]\n" \
"v_mfma_f32_16x16x32_bf16 a[8:11], v[132:135], v[52:55], a[8:11]\n" \
"v_mfma_f32_16x16x32_bf16 a[40:43], v[132:135], v[60:63], a[40:43]\n" \
"buffer_store_dwordx4 v[204:207], v20, s[80:83], s88 offen sc1\n" \
"v_mfma_f32_16x16x32_bf16 a[40:43], v[136:139], v[64:67], a[40:43]\n" \
"v_mfma_f32_16x16x32_bf16 a[72:75], v[136:139], v[72:75], a[72:75]\n" \
"v_mfma_f32_16x16x32_bf16 a[72:75], v[132:135], v[68:71], a[72:75]\n" \
"v_mfma_f32_16x16x32_bf16 a[104:107], v[132:135], v[76:79], a[104:107]\n" \
"v_mfma_f32_16x16x32_bf16 a[104:107], v[136:139], v[80:83], a[104:107]\n" \
"v_mfma_f32_16x16x32_bf16 a[12:15], v[144:147], v[56:59], a[12:15]\n" \
"buffer_store_dwordx4 v[208:211], v20, s[80:83], %[oL3] offen sc1\n" \
"v_mfma_f32_16x16x32_bf16 a[12:15], v[140:143], v[52:55], a[12:15]\n" \
"v_mfma_f32_16x16x32_bf16 a[44:47], v[140:143], v[60:63], a[44:47]\n" \
"v_mfma_f32_16x16x32_bf16 a[44:47], v[144:147], v[64:67], a[44:47]\n" \
"v_mfma_f32_16x16x32_bf16 a[76:79], v[144:147], v[72:75], a[76:79]\n" \
"v_mfma_f32_16x16x32_bf16 a[76:79], v[140:143], v[68:71], a[76:79]\n" \
"v_mfma_f32_16x16x32_bf16 a[108:111], v[140:143], v[76:79], a[108:111]\n" \
"s_mov_b32 m0, %[mS]\n" \
"v_mfma_f32_16x16x32_bf16 a[108:111], v[144:147], v[80:83], a[108:111]\n" \
"v_mfma_f32_16x16x32_bf16 a[16:19], v[56:59], v[152:155], a[16:19]\n" \
"v_mfma_f32_16x16x32_bf16 a[16:19], v[52:55], v[148:151], a[16:19]\n" \
"v_mfma_f32_16x16x32_bf16 a[48:51], v[60:63], v[148:151], a[48:51]\n" \
"v_mfma_f32_16x16x32_bf16 a[48:51], v[64:67], v[152:155], a[48:51]\n" \
"v_mfma_f32_16x16x32_bf16 a[80:83], v[72:75], v[152:155], a[80:83]\n" \
"s_mov_b32 exec_lo, 0xfffc0000\n" \
"s_mov_b32 exec_hi, 0xf\n" \
"global_load_lds_dword v[188:189], off\n" \
"s_mov_b64 exec, -1\n" \
"s_nop 0\n" \
"v_mfma_f32_16x16x32_bf16 a[80:83], v[68:71], v[148:151], a[80:83]\n" \
"v_mfma_f32_16x16x32_bf16 a[112:115], v[76:79], v[148:151], a[112:115]\n" \
"v_mfma_f32_16x16x32_bf16 a[112:115], v[80:83], v[152:155], a[112:115]\n" \
"v_mfma_f32_16x16x32_bf16 a[20:23], v[56:59], v[160:163], a[20:23]\n" \
"v_mfma_f32_16x16x32_bf16 a[20:23], v[52:55], v[156:159], a[20:23]\n" \
"v_mfma_f32_16x16x32_bf16 a[52:55], v[60:63], v[156:159], a[52:55]\n" \
"v_mfma_f32_16x16x32_bf16 a[52:55], v[64:67], v[160:163], a[52:55]\n" \
"v_mfma_f32_16x16x32_bf16 a[84:87], v[72:75], v[160:163], a[84:87]\n" \
"v_mfma_f32_16x16x32_bf16 a[84:87], v[68:71], v[156:159], a[84:87]\n" \
"v_mfma_f32_16x16x32_bf16 a[116:119], v[76:79], v[156:159], a[116:119]\n" \
"v_mfma_f32_16x16x32_bf16 a[116:119], v[80:83], v[160:163], a[116:119]\n" \
"s_waitcnt lgkmcnt(0)\n" \
"s_nop 0\n" \
"v_mfma_f32_16x16x32_bf16 a[24:27], v[56:59], v[168:171], a[24:27]\n" \
"v_mfma_f32_16x16x32_bf16 a[24:27], v[52:55], v[164:167], a[24:27]\n" \
"v_mfma_f32_16x16x32_bf16 a[56:59], v[60:63], v[164:167], a[56:59]\n" \
"v_mfma_f32_16x16x32_bf16 a[56:59], v[64:67], v[168:171], a[56:59]\n" \
"v_mfma_f32_16x16x32_bf16 a[88:91], v[72:75], v[168:171], a[88:91]\n" \
"v_mfma_f32_16x16x32_bf16 a[88:91], v[68:71], v[164:167], a[88:91]\n" \
"v_mfma_f32_16x16x32_bf16 a[120:123], v[76:79], v[164:167], a[120:123]\n" \
"v_mfma_f32_16x16x32_bf16 a[120:123], v[80:83], v[168:171], a[120:123]\n" \
"v_mfma_f32_16x16x32_bf16 a[28:31], v[56:59], v[176:179], a[28:31]\n" \
"s_add_u32 s62, %[rc], 0x300\n" \
"s_and_b32 s62, s62, 0x3ff\n" \
"v_mfma_f32_16x16x32_bf16 a[28:31], v[52:55], v[172:175], a[28:31]\n" \
"s_lshl_b32 s63, s62, 12\n" \
"v_mfma_f32_16x16x32_bf16 a[60:63], v[60:63], v[172:175], a[60:63]\n" \
"v_mfma_f32_16x16x32_bf16 a[60:63], v[64:67], v[176:179], a[60:63]\n" \
"v_mfma_f32_16x16x32_bf16 a[92:95], v[72:75], v[176:179], a[92:95]\n" \
"v_mfma_f32_16x16x32_bf16 a[92:95], v[68:71], v[172:175], a[92:95]\n" \
"v_mfma_f32_16x16x32_bf16 a[124:127], v[76:79], v[172:175], a[124:127]\n" \
"v_mfma_f32_16x16x32_bf16 a[124:127], v[80:83], v[176:179], a[124:127]\n" \
"s_waitcnt vmcnt(4)\n" \
"s_barrier\n" \
"v_mfma_f32_16x16x32_bf16 a[144:147], v[88:91], v[152:155], a[144:147]\n" \
"s_add_u32 m0, %[mA], 0x0\n" \
"s_add_u32 s85, s62, 0x0\n" \
"s_nop 0\n" \
"v_mfma_f32_16x16x32_bf16 a[144:147], v[84:87], v[148:151], a[144:147]\n" \
"buffer_load_dwordx4 v22, s[64:67], s85 offen lds\n" \
"v_mfma_f32_16x16x32_bf16 a[176:179], v[92:95], v[148:151], a[176:179]\n" \
"ds_read_b128 v[56:59], v29 offset:49152\n" \
"ds_read_b128 v[52:55], v28 offset:49152\n" \
"v_mfma_f32_16x16x32_bf16 a[176:179], v[96:99], v[152:155], a[176:179]\n" \
"s_add_u32 m0, %[mA], 0x400\n" \
"s_add_u32 s84, s62, 0x2000\n" \
"v_mfma_f32_16x16x32_bf16 a[208:211], v[104:107], v[152:155], a[208:211]\n" \
"buffer_load_dwordx4 v23, s[64:67], s84 offen lds\n" \
"v_mfma_f32_16x16x32_bf16 a[208:211], v[100:103], v[148:151], a[208:211]\n" \
"ds_read_b128 v[60:63], v28 offset:51200\n" \
"ds_read_b128 v[64:67], v29 offset:51200\n" \
"v_mfma_f32_16x16x32_bf16 a[240:243], v[108:111], v[148:151], a[240:243]\n" \
"ds_read_b64_tr_b16 v[148:149], v32 offset:49152\n" \
"ds_read_b64_tr_b16 v[150:151], v32 offset:50176\n" \
"v_mfma_f32_16x16x32_bf16 a[240:243], v[112:115], v[152:155], a[240:243]\n" \
"s_add_u32 m0, %[mB], 0x0\n" \
"s_add_u32 s85, s63, 0x0\n" \
"v_mfma_f32_16x16x32_bf16 a[148:151], v[88:91], v[160:163], a[148:151]\n" \
"buffer_load_dwordx4 v26, s[68:71], s85 offen lds\n" \
"v_mfma_f32_16x16x32_bf16 a[148:151], v[84:87], v[156:159], a[148:151]\n" \
"ds_read_b64_tr_b16 v[152:153], v32 offset:57344\n" \
"ds_read_b64_tr_b16 v[154:155], v32 offset:58368\n" \
"v_mfma_f32_16x16x32_bf16 a[180:183], v[92:95], v[156:159], a[180:183]\n" \
"ds_read_b128 v[72:75], v29 offset:53248\n" \
"ds_read_b128 v[68:71], v28 offset:53248\n" \
"v_mfma_f32_16x16x32_bf16 a[180:183], v[96:99], v[160:163], a[180:183]\n" \
"s_add_u32 m0, %[mB], 0x4000\n" \
"s_add_u32 s84, s63, 0x0\n" \
"s_nop 0\n" \
"v_mfma_f32_16x16x32_bf16 a[212:215], v[104:107], v[160:163], a[212:215]\n" \
"buffer_load_dwordx4 v26, s[72:75], s84 offen lds\n" \
"v_mfma_f32_16x16x32_bf16 a[212:215], v[100:103], v[156:159], a[212:215]\n" \
"ds_read_b128 v[76:79], v28 offset:55296\n" \
"ds_read_b128 v[80:83], v29 offset:55296\n" \
"v_mfma_f32_16x16x32_bf16 a[244:247], v[108:111], v[156:159], a[244:247]\n" \
"ds_read_b64_tr_b16 v[156:157], v33 offset:49152\n" \
"ds_read_b64_tr_b16 v[158:159], v33 offset:50176\n" \
"v_mfma_f32_16x16x32_bf16 a[244:247], v[112:115], v[160:163], a[244:247]\n" \
"s_add_u32 m0, %[mA], 0x800\n" \
"s_add_u32 s85, s62, 0x4000\n" \
"v_mfma_f32_16x16x32_bf16 a[152:155], v[88:91], v[168:171], a[152:155]\n" \
"buffer_load_dwordx4 v22, s[64:67], s85 offen lds\n" \
"v_mfma_f32_16x16x32_bf16 a[152:155], v[84:87], v[164:167], a[152:155]\n" \
"ds_read_b64_tr_b16 v[160:161], v33 offset:57344\n" \
"ds_read_b64_tr_b16 v[162:163], v33 offset:58368\n" \
"v_mfma_f32_16x16x32_bf16 a[184:187], v[92:95], v[164:167], a[184:187]\n" \
"v_mfma_f32_16x16x32_bf16 a[184:187], v[96:99], v[168:171], a[184:187]\n" \
"s_add_u32 m0, %[mA], 0xc00\n" \
"s_add_u32 s84, s62, 0x6000\n" \
"v_mfma_f32_16x16x32_bf16 a[216:219], v[104:107], v[168:171], a[216:219]\n" \
"buffer_load_dwordx4 v23, s[64:67], s84 offen lds\n" \
"v_mfma_f32_16x16x32_bf16 a[216:219], v[100:103], v[164:167], a[216:219]\n" \
"v_mfma_f32_16x16x32_bf16 a[248:251], v[108:111], v[164:167], a[248:251]\n" \
"ds_read_b64_tr_b16 v[164:165], v34 offset:49152\n" \
"ds_read_b64_tr_b16 v[166:167], v34 offset:50176\n" \
"v_mfma_f32_16x16x32_bf16 a[248:251], v[112:115], v[168:171], a[248:251]\n" \
"s_add_u32 m0, %[mB], 0x400\n" \
"s_add_u32 s85, s63, 0x8000\n" \
"v_mfma_f32_16x16x32_bf16 a[156:159], v[88:91], v[176:179], a[156:159]\n" \
"buffer_load_dwordx4 v26, s[68:71], s85 offen lds\n" \
"v_mfma_f32_16x16x32_bf16 a[156:159], v[84:87], v[172:175], a[156:159]\n" \
"ds_read_b64_tr_b16 v[168:169], v34 offset:57344\n" \
"ds_read_b64_tr_b16 v[170:171], v34 offset:58368\n" \
"v_mfma_f32_16x16x32_bf16 a[188:191], v[92:95], v[172:175], a[188:191]\n" \
"v_mfma_f32_16x16x32_bf16 a[188:191], v[96:99], v[176:179], a[188:191]\n" \
"s_add_u32 m0, %[mB], 0x4400\n" \
"s_add_u32 s84, s63, 0x8000\n" \
"v_mfma_f32_16x16x32_bf16 a[220:223], v[104:107], v[176:179], a[220:223]\n" \
"buffer_load_dwordx4 v26, s[72:75], s84 offen lds\n" \
"v_mfma_f32_16x16x32_bf16 a[220:223], v[100:103], v[172:175], a[220:223]\n" \
"v_mfma_f32_16x16x32_bf16 a[252:255], v[108:111], v[172:175], a[252:255]\n" \
"ds_read_b64_tr_b16 v[172:173], v35 offset:49152\n" \
"ds_read_b64_tr_b16 v[174:175], v35 offset:50176\n" \
"v_mfma_f32_16x16x32_bf16 a[252:255], v[112:115], v[176:179], a[252:255]\n" \
"s_add_u32 m0, %[mA], 0x1000\n" \
"s_add_u32 s85, s62, 0x8000\n" \
"v_mfma_f32_16x16x32_bf16 a[128:131], v[120:123], v[88:91], a[128:131]\n" \
"buffer_load_dwordx4 v22, s[64:67], s85 offen lds\n" \
"v_mfma_f32_16x16x32_bf16 a[128:131], v[116:119], v[84:87], a[128:131]\n" \
"ds_read_b64_tr_b16 v[176:177], v35 offset:57344\n" \
"ds_read_b64_tr_b16 v[178:179], v35 offset:58368\n" \
"v_mfma_f32_16x16x32_bf16 a[160:163], v[116:119], v[92:95], a[160:163]\n" \
"v_mfma_f32_16x16x32_bf16 a[160:163], v[120:123], v[96:99], a[160:163]\n" \
"s_add_u32 m0, %[mA], 0x1400\n" \
"s_add_u32 s84, s62, 0xa000\n" \
"v_mfma_f32_16x16x32_bf16 a[192:195], v[120:123], v[104:107], a[192:195]\n" \
"buffer_load_dwordx4 v23, s[64:67], s84 offen lds\n" \
"v_mfma_f32_16x16x32_bf16 a[192:195], v[116:119], v[100:103], a[192:195]\n" \
"v_mfma_f32_16x16x32_bf16 a[224:227], v[116:119], v[108:111], a[224:227]\n" \
"ds_read_b64_tr_b16 v[116:117], v32 offset:32768\n" \
"ds_read_b64_tr_b16 v[118:119], v32 offset:33792\n" \
"v_mfma_f32_16x16x32_bf16 a[224:227], v[120:123], v[112:115], a[224:227]\n" \
"s_add_u32 m0, %[mB], 0x800\n" \
"s_add_u32 s85, s63, 0x10000\n" \
"v_mfma_f32_16x16x32_bf16 a[132:135], v[128:131], v[88:91], a[132:135]\n" \
"buffer_load_dwordx4 v27, s[68:71], s85 offen lds\n" \
"v_mfma_f32_16x16x32_bf16 a[132:135], v[124:127], v[84:87], a[132:135]\n" \
"ds_read_b64_tr_b16 v[120:121], v32 offset:40960\n" \
"ds_read_b64_tr_b16 v[122:123], v32 offset:41984\n" \
"v_mfma_f32_16x16x32_bf16 a[164:167], v[124:127], v[92:95], a[164:167]\n" \
"v_mfma_f32_16x16x32_bf16 a[164:167], v[128:131], v[96:99], a[164:167]\n" \
"s_add_u32 m0, %[mB], 0x4800\n" \
"s_add_u32 s84, s63, 0x10000\n" \
"v_mfma_f32_16x16x32_bf16 a[196:199], v[128:131], v[104:107], a[196:199]\n" \
"buffer_load_dwordx4 v27, s[72:75], s84 offen lds\n" \
"v_mfma_f32_16x16x32_bf16 a[196:199], v[124:127], v[100:103], a[196:199]\n" \
"v_mfma_f32_16x16x32_bf16 a[228:231], v[124:127], v[108:111], a[228:231]\n" \
"ds_read_b64_tr_b16 v[124:125], v33 offset:32768\n" \
"ds_read_b64_tr_b16 v[126:127], v33 offset:33792\n" \
"v_mfma_f32_16x16x32_bf16 a[228:231], v[128:131], v[112:115], a[228:231]\n" \
"ds_read_b64_tr_b16 v[128:129], v33 offset:40960\n" \
"ds_read_b64_tr_b16 v[130:131], v33 offset:41984\n" \
"v_mfma_f32_16x16x32_bf16 a[136:139], v[136:139], v[88:91], a[136:139]\n" \
"v_mfma_f32_16x16x32_bf16 a[136:139], v[132:135], v[84:87], a[136:139]\n" \
"v_mfma_f32_16x16x32_bf16 a[168:171], v[132:135], v[92:95], a[168:171]\n" \
"v_mfma_f32_16x16x32_bf16 a[168:171], v[136:139], v[96:99], a[168:171]\n" \
"v_mfma_f32_16x16x32_bf16 a[200:203], v[136:139], v[104:107], a[200:203]\n" \
"v_mfma_f32_16x16x32_bf16 a[200:203], v[132:135], v[100:103], a[200:203]\n" \
"v_mfma_f32_16x16x32_bf16 a[232:235], v[132:135], v[108:111], a[232:235]\n" \
"ds_read_b64_tr_b16 v[132:133], v34 offset:32768\n" \
"ds_read_b64_tr_b16 v[134:135], v34 offset:33792\n" \
"v_mfma_f32_16x16x32_bf16 a[232:235], v[136:139], v[112:115], a[232:235]\n" \
"ds_read_b64_tr_b16 v[136:137], v34 offset:40960\n" \
"ds_read_b64_tr_b16 v[138:139], v34 offset:41984\n" \
"v_mfma_f32_16x16x32_bf16 a[140:143], v[144:147], v[88:91], a[140:143]\n" \
"ds_read_b128 v[88:91], v29 offset:32768\n" \
"v_mfma_f32_16x16x32_bf16 a[140:143], v[140:143], v[84:87], a[140:143]\n" \
"ds_read_b128 v[84:87], v28 offset:32768\n" \
"v_mfma_f32_16x16x32_bf16 a[172:175], v[140:143], v[92:95], a[172:175]\n" \
"ds_read_b128 v[92:95], v28 offset:34816\n" \
"v_mfma_f32_16x16x32_bf16 a[172:175], v[144:147], v[96:99], a[172:175]\n" \
"ds_read_b128 v[96:99], v29 offset:34816\n" \
"v_mfma_f32_16x16x32_bf16 a[204:207], v[144:147], v[104:107], a[204:207]\n" \
"ds_read_b128 v[104:107], v29 offset:36864\n" \
"v_mfma_f32_16x16x32_bf16 a[204:207], v[140:143], v[100:103], a[204:207]\n" \
"ds_read_b128 v[100:103], v28 offset:36864\n" \
"v_mfma_f32_16x16x32_bf16 a[236:239], v[140:143], v[108:111], a[236:239]\n" \
"ds_read_b64_tr_b16 v[140:141], v35 offset:32768\n" \
"ds_read_b64_tr_b16 v[142:143], v35 offset:33792\n" \
"v_mfma_f32_16x16x32_bf16 a[236:239], v[144:147], v[112:115], a[236:239]\n" \
"s_add_u32 m0, %[mA], 0x1800\n" \
"s_waitcnt lgkmcnt(12)\n" \
"s_nop 0\n" \
"v_mfma_f32_16x16x32_bf16 a[16:19], v[56:59], v[152:155], a[16:19]\n" \
"s_add_u32 s85, s62, 0xc000\n" \
"ds_read_b64_tr_b16 v[144:145], v35 offset:40960\n" \
"v_mfma_f32_16x16x32_bf16 a[16:19], v[52:55], v[148:151], a[16:19]\n" \
"buffer_load_dwordx4 v22, s[64:67], s85 offen lds\n" \
"v_mfma_f32_16x16x32_bf16 a[48:51], v[60:63], v[148:151], a[48:51]\n" \
"s_add_u32 m0, %[mA], 0x1c00\n" \
"s_add_u32 s84, s62, 0xe000\n" \
"v_mfma_f32_16x16x32_bf16 a[48:51], v[64:67], v[152:155], a[48:51]\n" \
"ds_read_b64_tr_b16 v[146:147], v35 offset:41984\n" \
"ds_read_b128 v[108:111], v28 offset:38912\n" \
"v_mfma_f32_16x16x32_bf16 a[80:83], v[72:75], v[152:155], a[80:83]\n" \
"buffer_load_dwordx4 v23, s[64:67], s84 offen lds\n" \
"v_mfma_f32_16x16x32_bf16 a[80:83], v[68:71], v[148:151], a[80:83]\n" \
"s_add_u32 m0, %[mB], 0xc00\n" \
"s_add_u32 s85, s63, 0x18000\n" \
"v_mfma_f32_16x16x32_bf16 a[112:115], v[76:79], v[148:151], a[112:115]\n" \
"ds_read_b128 v[112:115], v29 offset:38912\n" \
"v_mfma_f32_16x16x32_bf16 a[112:115], v[80:83], v[152:155], a[112:115]\n" \
"buffer_load_dwordx4 v27, s[68:71], s85 offen lds\n" \
"v_mfma_f32_16x16x32_bf16 a[20:23], v[56:59], v[160:163], a[20:23]\n" \
"s_add_u32 m0, %[mB], 0x4c00\n" \
"s_add_u32 s84, s63, 0x18000\n" \
"v_mfma_f32_16x16x32_bf16 a[20:23], v[52:55], v[156:159], a[20:23]\n" \
"v_mfma_f32_16x16x32_bf16 a[52:55], v[60:63], v[156:159], a[52:55]\n" \
"buffer_load_dwordx4 v27, s[72:75], s84 offen lds\n" \
"v_mfma_f32_16x16x32_bf16 a[52:55], v[64:67], v[160:163], a[52:55]\n" \
"v_mfma_f32_16x16x32_bf16 a[84:87], v[72:75], v[160:163], a[84:87]\n" \
"v_mfma_f32_16x16x32_bf16 a[84:87], v[68:71], v[156:159], a[84:87]\n" \
"v_mfma_f32_16x16x32_bf16 a[116:119], v[76:79], v[156:159], a[116:119]\n" \
"v_mfma_f32_16x16x32_bf16 a[116:119], v[80:83], v[160:163], a[116:119]\n" \
"s_mov_b32 m0, %[mS]\n" \
"v_mfma_f32_16x16x32_bf16 a[24:27], v[56:59], v[168:171], a[24:27]\n" \
"v_mfma_f32_16x16x32_bf16 a[24:27], v[52:55], v[164:167], a[24:27]\n" \
"v_mfma_f32_16x16x32_bf16 a[56:59], v[60:63], v[164:167], a[56:59]\n" \
"v_mfma_f32_16x16x32_bf16 a[56:59], v[64:67], v[168:171], a[56:59]\n" \
"v_mfma_f32_16x16x32_bf16 a[88:91], v[72:75], v[168:171], a[88:91]\n" \
"v_mfma_f32_16x16x32_bf16 a[88:91], v[68:71], v[164:167], a[88:91]\n" \
"s_mov_b32 exec_lo, 0x0\n" \
"s_mov_b32 exec_hi, 0x3ffff0\n" \
"global_load_lds_dword v[188:189], off\n" \
"s_mov_b64 exec, -1\n" \
"s_nop 0\n" \
"v_mfma_f32_16x16x32_bf16 a[120:123], v[76:79], v[164:167], a[120:123]\n" \
"v_mfma_f32_16x16x32_bf16 a[120:123], v[80:83], v[168:171], a[120:123]\n" \
"v_mfma_f32_16x16x32_bf16 a[28:31], v[56:59], v[176:179], a[28:31]\n" \
"v_mfma_f32_16x16x32_bf16 a[28:31], v[52:55], v[172:175], a[28:31]\n" \
"v_mfma_f32_16x16x32_bf16 a[60:63], v[60:63], v[172:175], a[60:63]\n" \
"v_mfma_f32_16x16x32_bf16 a[60:63], v[64:67], v[176:179], a[60:63]\n" \
"v_mfma_f32_16x16x32_bf16 a[92:95], v[72:75], v[176:179], a[92:95]\n" \
"v_mfma_f32_16x16x32_bf16 a[92:95], v[68:71], v[172:175], a[92:95]\n" \
"v_mfma_f32_16x16x32_bf16 a[124:127], v[76:79], v[172:175], a[124:127]\n" \
"v_mfma_f32_16x16x32_bf16 a[124:127], v[80:83], v[176:179], a[124:127]\n" \
"v_mfma_f32_16x16x32_bf16 a[0:3], v[120:123], v[56:59], a[0:3]\n" \
"v_mfma_f32_16x16x32_bf16 a[0:3], v[116:119], v[52:55], a[0:3]\n" \
"v_mfma_f32_16x16x32_bf16 a[32:35], v[116:119], v[60:63], a[32:35]\n" \
"v_mfma_f32_16x16x32_bf16 a[32:35], v[120:123], v[64:67], a[32:35]\n" \
"v_mfma_f32_16x16x32_bf16 a[64:67], v[120:123], v[72:75], a[64:67]\n" \
"v_mfma_f32_16x16x32_bf16 a[64:67], v[116:119], v[68:71], a[64:67]\n" \
"v_mfma_f32_16x16x32_bf16 a[96:99], v[116:119], v[76:79], a[96:99]\n" \
"v_mfma_f32_16x16x32_bf16 a[96:99], v[120:123], v[80:83], a[96:99]\n" \
"v_mfma_f32_16x16x32_bf16 a[4:7], v[128:131], v[56:59], a[4:7]\n" \
"v_mfma_f32_16x16x32_bf16 a[4:7], v[124:127], v[52:55], a[4:7]\n" \
"v_mfma_f32_16x16x32_bf16 a[36:39], v[124:127], v[60:63], a[36:39]\n" \
"v_mfma_f32_16x16x32_bf16 a[36:39], v[128:131], v[64:67], a[36:39]\n" \
"v_mfma_f32_16x16x32_bf16 a[68:71], v[128:131], v[72:75], a[68:71]\n" \
"v_mfma_f32_16x16x32_bf16 a[68:71], v[124:127], v[68:71], a[68:71]\n" \
"v_mfma_f32_16x16x32_bf16 a[100:103], v[124:127], v[76:79], a[100:103]\n" \
"v_mfma_f32_16x16x32_bf16 a[100:103], v[128:131], v[80:83], a[100:103]\n" \
"s_waitcnt lgkmcnt(0)\n" \
"s_nop 0\n" \
"v_mfma_f32_16x16x32_bf16 a[8:11], v[136:139], v[56:59], a[8:11]\n" \
"v_mfma_f32_16x16x32_bf16 a[8:11], v[132:135], v[52:55], a[8:11]\n" \
"v_mfma_f32_16x16x32_bf16 a[40:43], v[132:135], v[60:63], a[40:43]\n" \
"v_mfma_f32_16x16x32_bf16 a[40:43], v[136:139], v[64:67], a[40:43]\n" \
"v_mfma_f32_16x16x32_bf16 a[72:75], v[136:139], v[72:75], a[72:75]\n" \
"v_mfma_f32_16x16x32_bf16 a[72:75], v[132:135], v[68:71], a[72:75]\n" \
"v_mfma_f32_16x16x32_bf16 a[104:107], v[132:135], v[76:79], a[104:107]\n" \
"v_mfma_f32_16x16x32_bf16 a[104:107], v[136:139], v[80:83], a[104:107]\n" \
"v_mfma_f32_16x16x32_bf16 a[12:15], v[144:147], v[56:59], a[12:15]\n" \
"s_add_u32 s62, %[rc], 0x380\n" \
"s_and_b32 s62, s62, 0x3ff\n" \
"v_mfma_f32_16x16x32_bf16 a[12:15], v[140:143], v[52:55], a[12:15]\n" \
"s_lshl_b32 s63, s62, 12\n" \
"v_mfma_f32_16x16x32_bf16 a[44:47], v[140:143], v[60:63], a[44:47]\n" \
"v_mfma_f32_16x16x32_bf16 a[44:47], v[144:147], v[64:67], a[44:47]\n" \
"v_mfma_f32_16x16x32_bf16 a[76:79], v[144:147], v[72:75], a[76:79]\n" \
"v_mfma_f32_16x16x32_bf16 a[76:79], v[140:143], v[68:71], a[76:79]\n" \
"v_mfma_f32_16x16x32_bf16 a[108:111], v[140:143], v[76:79], a[108:111]\n" \
"v_mfma_f32_16x16x32_bf16 a[108:111], v[144:147], v[80:83], a[108:111]\n" \
"s_waitcnt vmcnt(1)\n" \
"s_barrier\n" \
"s_mov_b64 exec, 1\n" \
"buffer_store_dword v45, off, s[80:83], %[oFs] offset:64 sc1\n" \
"L_pf2:\n" \
"s_cbranch_execz L_pf6\n" \
"s_nop 0\n" \
"s_mov_b64 exec, -1\n" \
"v_mfma_f32_16x16x32_bf16 a[128:131], v[120:123], v[88:91], a[128:131]\n" \
"s_add_u32 m0, %[mA], 0x8000\n" \
"s_add_u32 s85, s62, 0x0\n" \
"v_mfma_f32_16x16x32_bf16 a[128:131], v[116:119], v[84:87], a[128:131]\n" \
"ds_read_b128 v[56:59], v29 offset:16384\n" \
"ds_read_b128 v[52:55], v28 offset:16384\n" \
"v_mfma_f32_16x16x32_bf16 a[160:163], v[116:119], v[92:95], a[160:163]\n" \
"buffer_load_dwordx4 v22, s[64:67], s85 offen lds\n" \
"v_mfma_f32_16x16x32_bf16 a[160:163], v[120:123], v[96:99], a[160:163]\n" \
"s_add_u32 m0, %[mA], 0x8400\n" \
"s_add_u32 s84, s62, 0x2000\n" \
"v_mfma_f32_16x16x32_bf16 a[192:195], v[120:123], v[104:107], a[192:195]\n" \
"ds_read_b128 v[60:63], v28 offset:18432\n" \
"ds_read_b128 v[64:67], v29 offset:18432\n" \
"v_mfma_f32_16x16x32_bf16 a[192:195], v[116:119], v[100:103], a[192:195]\n" \
"buffer_load_dwordx4 v23, s[64:67], s84 offen lds\n" \
"v_mfma_f32_16x16x32_bf16 a[224:227], v[116:119], v[108:111], a[224:227]\n" \
"ds_read_b64_tr_b16 v[116:117], v32 offset:0\n" \
"ds_read_b64_tr_b16 v[118:119], v32 offset:1024\n" \
"v_mfma_f32_16x16x32_bf16 a[224:227], v[120:123], v[112:115], a[224:227]\n" \
"s_add_u32 m0, %[mB], 0x8000\n" \
"s_add_u32 s85, s63, 0x0\n" \
"v_mfma_f32_16x16x32_bf16 a[132:135], v[128:131], v[88:91], a[132:135]\n" \
"buffer_load_dwordx4 v26, s[68:71], s85 offen lds\n" \
"v_mfma_f32_16x16x32_bf16 a[132:135], v[124:127], v[84:87], a[132:135]\n" \
"ds_read_b64_tr_b16 v[120:121], v32 offset:8192\n" \
"ds_read_b64_tr_b16 v[122:123], v32 offset:9216\n" \
"v_mfma_f32_16x16x32_bf16 a[164:167], v[124:127], v[92:95], a[164:167]\n" \
"ds_read_b128 v[72:75], v29 offset:20480\n" \
"ds_read_b128 v[68:71], v28 offset:20480\n" \
"v_mfma_f32_16x16x32_bf16 a[164:167], v[128:131], v[96:99], a[164:167]\n" \
"s_add_u32 m0, %[mB], 0xc000\n" \
"s_add_u32 s84, s63, 0x0\n" \
"v_mfma_f32_16x16x32_bf16 a[196:199], v[128:131], v[104:107], a[196:199]\n" \
"buffer_load_dwordx4 v26, s[72:75], s84 offen lds\n" \
"v_mfma_f32_16x16x32_bf16 a[196:199], v[124:127], v[100:103], a[196:199]\n" \
"ds_read_b128 v[76:79], v28 offset:22528\n" \
"ds_read_b128 v[80:83], v29 offset:22528\n" \
"v_mfma_f32_16x16x32_bf16 a[228:231], v[124:127], v[108:111], a[228:231]\n" \
"ds_read_b64_tr_b16 v[124:125], v33 offset:0\n" \
"ds_read_b64_tr_b16 v[126:127], v33 offset:1024\n" \
"v_mfma_f32_16x16x32_bf16 a[228:231], v[128:131], v[112:115], a[228:231]\n" \
"s_add_u32 m0, %[mA], 0x8800\n" \
"s_add_u32 s85, s62, 0x4000\n" \
"v_mfma_f32_16x16x32_bf16 a[136:139], v[136:139], v[88:91], a[136:139]\n" \
"buffer_load_dwordx4 v22, s[64:67], s85 offen lds\n" \
"v_mfma_f32_16x16x32_bf16 a[136:139], v[132:135], v[84:87], a[136:139]\n" \
"ds_read_b64_tr_b16 v[128:129], v33 offset:8192\n" \
"ds_read_b64_tr_b16 v[130:131], v33 offset:9216\n" \
"v_mfma_f32_16x16x32_bf16 a[168:171], v[132:135], v[92:95], a[168:171]\n" \
"v_mov_b32_e64 v45, %[tag]\n" \
"v_mfma_f32_16x16x32_bf16 a[168:171], v[136:139], v[96:99], a[168:171]\n" \
"s_add_u32 m0, %[mA], 0x8c00\n" \
"s_add_u32 s84, s62, 0x6000\n" \
"v_mfma_f32_16x16x32_bf16 a[200:203], v[136:139], v[104:107], a[200:203]\n" \
"buffer_load_dwordx4 v23, s[64:67], s84 offen lds\n" \
"v_mfma_f32_16x16x32_bf16 a[200:203], v[132:135], v[100:103], a[200:203]\n" \
"v_mfma_f32_16x16x32_bf16 a[232:235], v[132:135], v[108:111], a[232:235]\n" \
"ds_read_b64_tr_b16 v[132:133], v34 offset:0\n" \
"ds_read_b64_tr_b16 v[134:135], v34 offset:1024\n" \
"v_mfma_f32_16x16x32_bf16 a[232:235], v[136:139], v[112:115], a[232:235]\n" \
"s_add_u32 m0, %[mB], 0x8400\n" \
"s_add_u32 s85, s63, 0x8000\n" \
"v_mfma_f32_16x16x32_bf16 a[140:143], v[144:147], v[88:91], a[140:143]\n" \
"buffer_load_dwordx4 v26, s[68:71], s85 offen lds\n" \
"v_mfma_f32_16x16x32_bf16 a[140:143], v[140:143], v[84:87], a[140:143]\n" \
"ds_read_b64_tr_b16 v[136:137], v34 offset:8192\n" \
"ds_read_b64_tr_b16 v[138:139], v34 offset:9216\n" \
"v_mfma_f32_16x16x32_bf16 a[172:175], v[140:143], v[92:95], a[172:175]\n" \
"v_mfma_f32_16x16x32_bf16 a[172:175], v[144:147], v[96:99], a[172:175]\n" \
"s_add_u32 m0, %[mB], 0xc400\n" \
"s_add_u32 s84, s63, 0x8000\n" \
"v_mfma_f32_16x16x32_bf16 a[204:207], v[144:147], v[104:107], a[204:207]\n" \
"buffer_load_dwordx4 v26, s[72:75], s84 offen lds\n" \
"v_mfma_f32_16x16x32_bf16 a[204:207], v[140:143], v[100:103], a[204:207]\n" \
"v_mfma_f32_16x16x32_bf16 a[236:239], v[140:143], v[108:111], a[236:239]\n" \
"ds_read_b64_tr_b16 v[140:141], v35 offset:0\n" \
"ds_read_b64_tr_b16 v[142:143], v35 offset:1024\n" \
"v_mfma_f32_16x16x32_bf16 a[236:239], v[144:147], v[112:115], a[236:239]\n" \
"s_add_u32 m0, %[mA], 0x9000\n" \
"s_add_u32 s85, s62, 0x8000\n" \
"v_mfma_f32_16x16x32_bf16 a[144:147], v[88:91], v[152:155], a[144:147]\n" \
"buffer_load_dwordx4 v22, s[64:67], s85 offen lds\n" \
"v_mfma_f32_16x16x32_bf16 a[144:147], v[84:87], v[148:151], a[144:147]\n" \
"ds_read_b64_tr_b16 v[144:145], v35 offset:8192\n" \
"ds_read_b64_tr_b16 v[146:147], v35 offset:9216\n" \
"v_mfma_f32_16x16x32_bf16 a[176:179], v[92:95], v[148:151], a[176:179]\n" \
"v_mfma_f32_16x16x32_bf16 a[176:179], v[96:99], v[152:155], a[176:179]\n" \
"s_add_u32 m0, %[mA], 0x9400\n" \
"s_add_u32 s84, s62, 0xa000\n" \
"v_mfma_f32_16x16x32_bf16 a[208:211], v[104:107], v[152:155], a[208:211]\n" \
"buffer_load_dwordx4 v23, s[64:67], s84 offen lds\n" \
"v_mfma_f32_16x16x32_bf16 a[208:211], v[100:103], v[148:151], a[208:211]\n" \
"v_mfma_f32_16x16x32_bf16 a[240:243], v[108:111], v[148:151], a[240:243]\n" \
"ds_read_b64_tr_b16 v[148:149], v32 offset:16384\n" \
"ds_read_b64_tr_b16 v[150:151], v32 offset:17408\n" \
"v_mfma_f32_16x16x32_bf16 a[240:243], v[112:115], v[152:155], a[240:243]\n" \
"s_add_u32 m0, %[mB], 0x8800\n" \
"s_add_u32 s85, s63, 0x10000\n" \
"v_mfma_f32_16x16x32_bf16 a[148:151], v[88:91], v[160:163], a[148:151]\n" \
"buffer_load_dwordx4 v27, s[68:71], s85 offen lds\n" \
"v_mfma_f32_16x16x32_bf16 a[148:151], v[84:87], v[156:159], a[148:151]\n" \
"ds_read_b64_tr_b16 v[152:153], v32 offset:24576\n" \
"ds_read_b64_tr_b16 v[154:155], v32 offset:25600\n" \
"v_mfma_f32_16x16x32_bf16 a[180:183], v[92:95], v[156:159], a[180:183]\n" \
"v_mfma_f32_16x16x32_bf16 a[180:183], v[96:99], v[160:163], a[180:183]\n" \
"s_add_u32 m0, %[mB], 0xc800\n" \
"s_add_u32 s84, s63, 0x10000\n" \
"v_mfma_f32_16x16x32_bf16 a[212:215], v[104:107], v[160:163], a[212:215]\n" \
"buffer_load_dwordx4 v27, s[72:75], s84 offen lds\n" \
"v_mfma_f32_16x16x32_bf16 a[212:215], v[100:103], v[156:159], a[212:215]\n" \
"v_mfma_f32_16x16x32_bf16 a[244:247], v[108:111], v[156:159], a[244:247]\n" \
"ds_read_b64_tr_b16 v[156:157], v33 offset:16384\n" \
"ds_read_b64_tr_b16 v[158:159], v33 offset:17408\n" \
"v_mfma_f32_16x16x32_bf16 a[244:247], v[112:115], v[160:163], a[244:247]\n" \
"ds_read_b64_tr_b16 v[160:161], v33 offset:24576\n" \
"ds_read_b64_tr_b16 v[162:163], v33 offset:25600\n" \
"v_mfma_f32_16x16x32_bf16 a[152:155], v[88:91], v[168:171], a[152:155]\n" \
"v_mfma_f32_16x16x32_bf16 a[152:155], v[84:87], v[164:167], a[152:155]\n" \
"v_mfma_f32_16x16x32_bf16 a[184:187], v[92:95], v[164:167], a[184:187]\n" \
"v_mfma_f32_16x16x32_bf16 a[184:187], v[96:99], v[168:171], a[184:187]\n" \
"v_mfma_f32_16x16x32_bf16 a[216:219], v[104:107], v[168:171], a[216:219]\n" \
"v_mfma_f32_16x16x32_bf16 a[216:219], v[100:103], v[164:167], a[216:219]\n" \
"v_mfma_f32_16x16x32_bf16 a[248:251], v[108:111], v[164:167], a[248:251]\n" \
"ds_read_b64_tr_b16 v[164:165], v34 offset:16384\n" \
"ds_read_b64_tr_b16 v[166:167], v34 offset:17408\n" \
"v_mfma_f32_16x16x32_bf16 a[248:251], v[112:115], v[168:171], a[248:251]\n" \
"ds_read_b64_tr_b16 v[168:169], v34 offset:24576\n" \
"ds_read_b64_tr_b16 v[170:171], v34 offset:25600\n" \
"v_mfma_f32_16x16x32_bf16 a[156:159], v[88:91], v[176:179], a[156:159]\n" \
"ds_read_b128 v[88:91], v29 offset:0\n" \
"v_mfma_f32_16x16x32_bf16 a[156:159], v[84:87], v[172:175], a[156:159]\n" \
"ds_read_b128 v[84:87], v28 offset:0\n" \
"v_mfma_f32_16x16x32_bf16 a[188:191], v[92:95], v[172:175], a[188:191]\n" \
"ds_read_b128 v[92:95], v28 offset:2048\n" \
"v_mfma_f32_16x16x32_bf16 a[188:191], v[96:99], v[176:179], a[188:191]\n" \
"ds_read_b128 v[96:99], v29 offset:2048\n" \
"v_mfma_f32_16x16x32_bf16 a[220:223], v[104:107], v[176:179], a[220:223]\n" \
"ds_read_b128 v[104:107], v29 offset:4096\n" \
"v_mfma_f32_16x16x32_bf16 a[220:223], v[100:103], v[172:175], a[220:223]\n" \
"ds_read_b128 v[100:103], v28 offset:4096\n" \
"v_mfma_f32_16x16x32_bf16 a[252:255], v[108:111], v[172:175], a[252:255]\n" \
"ds_read_b64_tr_b16 v[172:173], v35 offset:16384\n" \
"ds_read_b64_tr_b16 v[174:175], v35 offset:17408\n" \
"v_mfma_f32_16x16x32_bf16 a[252:255], v[112:115], v[176:179], a[252:255]\n" \
"s_add_u32 m0, %[mA], 0x9800\n" \
"s_waitcnt lgkmcnt(12)\n" \
"s_nop 0\n" \
"v_mfma_f32_16x16x32_bf16 a[0:3], v[120:123], v[56:59], a[0:3]\n" \
"s_add_u32 s85, s62, 0xc000\n" \
"ds_read_b64_tr_b16 v[176:177], v35 offset:24576\n" \
"v_mfma_f32_16x16x32_bf16 a[0:3], v[116:119], v[52:55], a[0:3]\n" \
"buffer_load_dwordx4 v22, s[64:67], s85 offen lds\n" \
"v_mfma_f32_16x16x32_bf16 a[32:35], v[116:119], v[60:63], a[32:35]\n" \
"s_add_u32 m0, %[mA], 0x9c00\n" \
"s_add_u32 s84, s62, 0xe000\n" \
"v_mfma_f32_16x16x32_bf16 a[32:35], v[120:123], v[64:67], a[32:35]\n" \
"ds_read_b64_tr_b16 v[178:179], v35 offset:25600\n" \
"ds_read_b128 v[108:111], v28 offset:6144\n" \
"v_mfma_f32_16x16x32_bf16 a[64:67], v[120:123], v[72:75], a[64:67]\n" \
"buffer_load_dwordx4 v23, s[64:67], s84 offen lds\n" \
"v_mfma_f32_16x16x32_bf16 a[64:67], v[116:119], v[68:71], a[64:67]\n" \
"s_add_u32 m0, %[mB], 0x8c00\n" \
"s_add_u32 s85, s63, 0x18000\n" \
"v_mfma_f32_16x16x32_bf16 a[96:99], v[116:119], v[76:79], a[96:99]\n" \
"ds_read_b128 v[112:115], v29 offset:6144\n" \
"v_add_u32_e64 v23, %[dH], v29\n" \
"v_mfma_f32_16x16x32_bf16 a[96:99], v[120:123], v[80:83], a[96:99]\n" \
"buffer_load_dwordx4 v27, s[68:71], s85 offen lds\n" \
"v_mfma_f32_16x16x32_bf16 a[4:7], v[128:131], v[56:59], a[4:7]\n" \
"s_add_u32 m0, %[mB], 0xcc00\n" \
"s_add_u32 s84, s63, 0x18000\n" \
"v_mfma_f32_16x16x32_bf16 a[4:7], v[124:127], v[52:55], a[4:7]\n" \
"s_mov_b32 s68, %[wql]\n" \
"s_and_b32 s69, %[wqh], 0xffff\n" \
"v_mfma_f32_16x16x32_bf16 a[36:39], v[124:127], v[60:63], a[36:39]\n" \
"buffer_load_dwordx4 v27, s[72:75], s84 offen lds\n" \
"s_mov_b32 s73, %[oP]\n" \
"v_mfma_f32_16x16x32_bf16 a[36:39], v[128:131], v[64:67], a[36:39]\n" \
"s_mov_b32 s70, 0x480000\n" \
"v_add_u32_e64 v22, %[dH], v28\n" \
"v_mfma_f32_16x16x32_bf16 a[68:71], v[128:131], v[72:75], a[68:71]\n" \
"v_bfe_u32 v44, v20, 2, 5\n" \
"s_mov_b32 s64, 0xaaaaaaaa\n" \
"v_mfma_f32_16x16x32_bf16 a[68:71], v[124:127], v[68:71], a[68:71]\n" \
"buffer_load_dword v44, v44, s[80:83], %[oFp] offen offset:64 sc1\n" \
"v_mfma_f32_16x16x32_bf16 a[100:103], v[124:127], v[76:79], a[100:103]\n" \
"s_mov_b32 s65, 0xaaaaaaaa\n" \
"s_mov_b32 s66, 0xfff0ffff\n" \
"v_mfma_f32_16x16x32_bf16 a[100:103], v[128:131], v[80:83], a[100:103]\n" \
"s_mov_b32 m0, %[mS]\n" \
"s_nop 0\n" \
"s_mov_b32 s67, 0xf000ff00\n" \
"v_mfma_f32_16x16x32_bf16 a[8:11], v[136:139], v[56:59], a[8:11]\n" \
"s_mov_b32 s72, 0x3eae1a93\n" \
"v_mfma_f32_16x16x32_bf16 a[8:11], v[132:135], v[52:55], a[8:11]\n" \
"v_mfma_f32_16x16x32_bf16 a[40:43], v[132:135], v[60:63], a[40:43]\n" \
"v_mfma_f32_16x16x32_bf16 a[40:43], v[136:139], v[64:67], a[40:43]\n" \
"v_mfma_f32_16x16x32_bf16 a[72:75], v[136:139], v[72:75], a[72:75]\n" \
"v_mfma_f32_16x16x32_bf16 a[72:75], v[132:135], v[68:71], a[72:75]\n" \
"s_mov_b32 exec_lo, 0xffe00000\n" \
"s_mov_b32 exec_hi, 0x3ff\n" \
"global_load_lds_dword v[16:17], off\n" \
"s_mov_b64 exec, -1\n" \
"s_nop 0\n" \
"v_mfma_f32_16x16x32_bf16 a[104:107], v[132:135], v[76:79], a[104:107]\n" \
"v_mfma_f32_16x16x32_bf16 a[104:107], v[136:139], v[80:83], a[104:107]\n" \
"v_mfma_f32_16x16x32_bf16 a[12:15], v[144:147], v[56:59], a[12:15]\n" \
"v_mfma_f32_16x16x32_bf16 a[12:15], v[140:143], v[52:55], a[12:15]\n" \
"v_mfma_f32_16x16x32_bf16 a[44:47], v[140:143], v[60:63], a[44:47]\n" \
"v_mfma_f32_16x16x32_bf16 a[44:47], v[144:147], v[64:67], a[44:47]\n" \
"v_mfma_f32_16x16x32_bf16 a[76:79], v[144:147], v[72:75], a[76:79]\n" \
"v_mfma_f32_16x16x32_bf16 a[76:79], v[140:143], v[68:71], a[76:79]\n" \
"v_mfma_f32_16x16x32_bf16 a[108:111], v[140:143], v[76:79], a[108:111]\n" \
"v_mfma_f32_16x16x32_bf16 a[108:111], v[144:147], v[80:83], a[108:111]\n" \
"v_mfma_f32_16x16x32_bf16 a[16:19], v[56:59], v[152:155], a[16:19]\n" \
"v_mfma_f32_16x16x32_bf16 a[16:19], v[52:55], v[148:151], a[16:19]\n" \
"v_mfma_f32_16x16x32_bf16 a[48:51], v[60:63], v[148:151], a[48:51]\n" \
"v_mfma_f32_16x16x32_bf16 a[48:51], v[64:67], v[152:155], a[48:51]\n" \
"v_mfma_f32_16x16x32_bf16 a[80:83], v[72:75], v[152:155], a[80:83]\n" \
"v_mfma_f32_16x16x32_bf16 a[80:83], v[68:71], v[148:151], a[80:83]\n" \
"v_mfma_f32_16x16x32_bf16 a[112:115], v[76:79], v[148:151], a[112:115]\n" \
"v_mfma_f32_16x16x32_bf16 a[112:115], v[80:83], v[152:155], a[112:115]\n" \
"v_mfma_f32_16x16x32_bf16 a[20:23], v[56:59], v[160:163], a[20:23]\n" \
"v_mfma_f32_16x16x32_bf16 a[20:23], v[52:55], v[156:159], a[20:23]\n" \
"v_mfma_f32_16x16x32_bf16 a[52:55], v[60:63], v[156:159], a[52:55]\n" \
"v_mfma_f32_16x16x32_bf16 a[52:55], v[64:67], v[160:163], a[52:55]\n" \
"v_mfma_f32_16x16x32_bf16 a[84:87], v[72:75], v[160:163], a[84:87]\n" \
"v_mfma_f32_16x16x32_bf16 a[84:87], v[68:71], v[156:159], a[84:87]\n" \
"v_mfma_f32_16x16x32_bf16 a[116:119], v[76:79], v[156:159], a[116:119]\n" \
"v_mfma_f32_16x16x32_bf16 a[116:119], v[80:83], v[160:163], a[116:119]\n" \
"s_waitcnt lgkmcnt(0)\n" \
"v_mfma_f32_16x16x32_bf16 a[24:27], v[56:59], v[168:171], a[24:27]\n" \
"v_mfma_f32_16x16x32_bf16 a[24:27], v[52:55], v[164:167], a[24:27]\n" \
"v_mfma_f32_16x16x32_bf16 a[56:59], v[60:63], v[164:167], a[56:59]\n" \
"v_mfma_f32_16x16x32_bf16 a[56:59], v[64:67], v[168:171], a[56:59]\n" \
"v_mfma_f32_16x16x32_bf16 a[88:91], v[72:75], v[168:171], a[88:91]\n" \
"v_mfma_f32_16x16x32_bf16 a[88:91], v[68:71], v[164:167], a[88:91]\n" \
"v_mfma_f32_16x16x32_bf16 a[120:123], v[76:79], v[164:167], a[120:123]\n" \
"v_mfma_f32_16x16x32_bf16 a[120:123], v[80:83], v[168:171], a[120:123]\n" \
"v_mfma_f32_16x16x32_bf16 a[28:31], v[56:59], v[176:179], a[28:31]\n" \
"v_mfma_f32_16x16x32_bf16 a[28:31], v[52:55], v[172:175], a[28:31]\n" \
"s_waitcnt vmcnt(1)\n" \
"v_cmp_ne_u32_e32 vcc, %[tag], v44\n" \
"v_mfma_f32_16x16x32_bf16 a[60:63], v[60:63], v[172:175], a[60:63]\n" \
"v_mfma_f32_16x16x32_bf16 a[60:63], v[64:67], v[176:179], a[60:63]\n" \
"v_mfma_f32_16x16x32_bf16 a[92:95], v[72:75], v[176:179], a[92:95]\n" \
"v_mfma_f32_16x16x32_bf16 a[92:95], v[68:71], v[172:175], a[92:95]\n" \
"v_mfma_f32_16x16x32_bf16 a[124:127], v[76:79], v[172:175], a[124:127]\n" \
"s_cbranch_vccnz L_pl_%=\n" \
"L_rl_%=:\n" \
"v_mfma_f32_16x16x32_bf16 a[124:127], v[80:83], v[176:179], a[124:127]\n" \
"s_barrier\n" \
"v_mfma_f32_16x16x32_bf16 a[144:147], v[88:91], v[152:155], a[144:147]\n" \
"s_add_u32 m0, %[mL], 0x0\n" \
"s_add_u32 s85, %[oL], 0x0\n" \
"s_nop 0\n" \
"v_mfma_f32_16x16x32_bf16 a[144:147], v[84:87], v[148:151], a[144:147]\n" \
"buffer_load_dwordx4 v20, s[80:83], s85 offen sc1 lds\n" \
"v_mfma_f32_16x16x32_bf16 a[176:179], v[92:95], v[148:151], a[176:179]\n" \
"ds_read_b128 v[56:59], v29 offset:49152\n" \
"ds_read_b128 v[52:55], v28 offset:49152\n" \
"v_mfma_f32_16x16x32_bf16 a[176:179], v[96:99], v[152:155], a[176:179]\n" \
"s_add_u32 m0, %[mL], 0x1000\n" \
"s_add_u32 s84, %[oL], 0x1000\n" \
"v_mfma_f32_16x16x32_bf16 a[208:211], v[104:107], v[152:155], a[208:211]\n" \
"buffer_load_dwordx4 v20, s[80:83], s84 offen sc1 lds\n" \
"v_mfma_f32_16x16x32_bf16 a[208:211], v[100:103], v[148:151], a[208:211]\n" \
"ds_read_b128 v[60:63], v28 offset:51200\n" \
"ds_read_b128 v[64:67], v29 offset:51200\n" \
"v_mfma_f32_16x16x32_bf16 a[240:243], v[108:111], v[148:151], a[240:243]\n" \
"ds_read_b64_tr_b16 v[148:149], v32 offset:49152\n" \
"ds_read_b64_tr_b16 v[150:151], v32 offset:50176\n" \
"v_mfma_f32_16x16x32_bf16 a[240:243], v[112:115], v[152:155], a[240:243]\n" \
"s_add_u32 m0, %[mL], 0x3000\n" \
"s_mov_b32 s85, 0x0\n" \
"v_mfma_f32_16x16x32_bf16 a[148:151], v[88:91], v[160:163], a[148:151]\n" \
"buffer_load_dwordx4 v21, s[76:79], s85 offen lds\n" \
"v_mfma_f32_16x16x32_bf16 a[148:151], v[84:87], v[156:159], a[148:151]\n" \
"ds_read_b64_tr_b16 v[152:153], v32 offset:57344\n" \
"ds_read_b64_tr_b16 v[154:155], v32 offset:58368\n" \
"v_mfma_f32_16x16x32_bf16 a[180:183], v[92:95], v[156:159], a[180:183]\n" \
"ds_read_b128 v[72:75], v29 offset:53248\n" \
"ds_read_b128 v[68:71], v28 offset:53248\n" \
"v_mfma_f32_16x16x32_bf16 a[180:183], v[96:99], v[160:163], a[180:183]\n" \
"s_add_u32 m0, %[mA], 0x0\n" \
"s_mov_b32 s84, 0x0\n" \
"s_nop 0\n" \
"v_mfma_f32_16x16x32_bf16 a[212:215], v[104:107], v[160:163], a[212:215]\n" \
"buffer_load_dwordx4 v24, s[76:79], s84 offen lds\n" \
"v_mfma_f32_16x16x32_bf16 a[212:215], v[100:103], v[156:159], a[212:215]\n" \
"ds_read_b128 v[76:79], v28 offset:55296\n" \
"ds_read_b128 v[80:83], v29 offset:55296\n" \
"v_mfma_f32_16x16x32_bf16 a[244:247], v[108:111], v[156:159], a[244:247]\n" \
"ds_read_b64_tr_b16 v[156:157], v33 offset:49152\n" \
"ds_read_b64_tr_b16 v[158:159], v33 offset:50176\n" \
"v_mfma_f32_16x16x32_bf16 a[244:247], v[112:115], v[160:163], a[244:247]\n" \
"s_add_u32 m0, %[mA], 0x400\n" \
"s_mov_b32 s85, 0x2400\n" \
"v_mfma_f32_16x16x32_bf16 a[152:155], v[88:91], v[168:171], a[152:155]\n" \
"buffer_load_dwordx4 v25, s[76:79], s85 offen lds\n" \
"v_mfma_f32_16x16x32_bf16 a[152:155], v[84:87], v[164:167], a[152:155]\n" \
"ds_read_b64_tr_b16 v[160:161], v33 offset:57344\n" \
"ds_read_b64_tr_b16 v[162:163], v33 offset:58368\n" \
"v_mfma_f32_16x16x32_bf16 a[184:187], v[92:95], v[164:167], a[184:187]\n" \
"v_mfma_f32_16x16x32_bf16 a[184:187], v[96:99], v[168:171], a[184:187]\n" \
"s_add_u32 m0, %[mB], 0x0\n" \
"s_mov_b32 s84, 0x0\n" \
"v_mfma_f32_16x16x32_bf16 a[216:219], v[104:107], v[168:171], a[216:219]\n" \
"buffer_load_dwordx4 v26, s[68:71], s84 offen lds\n" \
"v_mfma_f32_16x16x32_bf16 a[216:219], v[100:103], v[164:167], a[216:219]\n" \
"v_mfma_f32_16x16x32_bf16 a[248:251], v[108:111], v[164:167], a[248:251]\n" \
"ds_read_b64_tr_b16 v[164:165], v34 offset:49152\n" \
"ds_read_b64_tr_b16 v[166:167], v34 offset:50176\n" \
"v_mfma_f32_16x16x32_bf16 a[248:251], v[112:115], v[168:171], a[248:251]\n" \
"s_add_u32 m0, %[mA], 0x800\n" \
"s_mov_b32 s85, 0x4800\n" \
"v_mfma_f32_16x16x32_bf16 a[156:159], v[88:91], v[176:179], a[156:159]\n" \
"buffer_load_dwordx4 v24, s[76:79], s85 offen lds\n" \
"v_mfma_f32_16x16x32_bf16 a[156:159], v[84:87], v[172:175], a[156:159]\n" \
"ds_read_b64_tr_b16 v[168:169], v34 offset:57344\n" \
"ds_read_b64_tr_b16 v[170:171], v34 offset:58368\n" \
"v_mfma_f32_16x16x32_bf16 a[188:191], v[92:95], v[172:175], a[188:191]\n" \
"v_mfma_f32_16x16x32_bf16 a[188:191], v[96:99], v[176:179], a[188:191]\n" \
"s_add_u32 m0, %[mA], 0xc00\n" \
"s_mov_b32 s84, 0x6c00\n" \
"v_mfma_f32_16x16x32_bf16 a[220:223], v[104:107], v[176:179], a[220:223]\n" \
"buffer_load_dwordx4 v25, s[76:79], s84 offen lds\n" \
"v_mfma_f32_16x16x32_bf16 a[220:223], v[100:103], v[172:175], a[220:223]\n" \
"v_mfma_f32_16x16x32_bf16 a[252:255], v[108:111], v[172:175], a[252:255]\n" \
"ds_read_b64_tr_b16 v[172:173], v35 offset:49152\n" \
"ds_read_b64_tr_b16 v[174:175], v35 offset:50176\n" \
"v_mfma_f32_16x16x32_bf16 a[252:255], v[112:115], v[176:179], a[252:255]\n" \
"s_add_u32 m0, %[mB], 0x400\n" \
"s_mov_b32 s85, 0x8000\n" \
"v_mfma_f32_16x16x32_bf16 a[128:131], v[120:123], v[88:91], a[128:131]\n" \
"buffer_load_dwordx4 v26, s[68:71], s85 offen lds\n" \
"v_mfma_f32_16x16x32_bf16 a[128:131], v[116:119], v[84:87], a[128:131]\n" \
"ds_read_b64_tr_b16 v[176:177], v35 offset:57344\n" \
"ds_read_b64_tr_b16 v[178:179], v35 offset:58368\n" \
"v_mfma_f32_16x16x32_bf16 a[160:163], v[116:119], v[92:95], a[160:163]\n" \
"v_mfma_f32_16x16x32_bf16 a[160:163], v[120:123], v[96:99], a[160:163]\n" \
"s_add_u32 m0, %[mA], 0x1000\n" \
"s_mov_b32 s84, 0x9000\n" \
"v_mfma_f32_16x16x32_bf16 a[192:195], v[120:123], v[104:107], a[192:195]\n" \
"buffer_load_dwordx4 v24, s[76:79], s84 offen lds\n" \
"v_mfma_f32_16x16x32_bf16 a[192:195], v[116:119], v[100:103], a[192:195]\n" \
"v_mfma_f32_16x16x32_bf16 a[224:227], v[116:119], v[108:111], a[224:227]\n" \
"ds_read_b64_tr_b16 v[116:117], v32 offset:32768\n" \
"ds_read_b64_tr_b16 v[118:119], v32 offset:33792\n" \
"v_mfma_f32_16x16x32_bf16 a[224:227], v[120:123], v[112:115], a[224:227]\n" \
"s_add_u32 m0, %[mA], 0x1400\n" \
"s_mov_b32 s85, 0xb400\n" \
"v_mfma_f32_16x16x32_bf16 a[132:135], v[128:131], v[88:91], a[132:135]\n" \
"buffer_load_dwordx4 v25, s[76:79], s85 offen lds\n" \
"v_mfma_f32_16x16x32_bf16 a[132:135], v[124:127], v[84:87], a[132:135]\n" \
"ds_read_b64_tr_b16 v[120:121], v32 offset:40960\n" \
"ds_read_b64_tr_b16 v[122:123], v32 offset:41984\n" \
"v_mfma_f32_16x16x32_bf16 a[164:167], v[124:127], v[92:95], a[164:167]\n" \
"v_mfma_f32_16x16x32_bf16 a[164:167], v[128:131], v[96:99], a[164:167]\n" \
"s_add_u32 m0, %[mB], 0x800\n" \
"s_mov_b32 s84, 0x10000\n" \
"v_mfma_f32_16x16x32_bf16 a[196:199], v[128:131], v[104:107], a[196:199]\n" \
"buffer_load_dwordx4 v27, s[68:71], s84 offen lds\n" \
"v_mfma_f32_16x16x32_bf16 a[196:199], v[124:127], v[100:103], a[196:199]\n" \
"v_mfma_f32_16x16x32_bf16 a[228:231], v[124:127], v[108:111], a[228:231]\n" \
"ds_read_b64_tr_b16 v[124:125], v33 offset:32768\n" \
"ds_read_b64_tr_b16 v[126:127], v33 offset:33792\n" \
"v_mfma_f32_16x16x32_bf16 a[228:231], v[128:131], v[112:115], a[228:231]\n" \
"ds_read_b64_tr_b16 v[128:129], v33 offset:40960\n" \
"ds_read_b64_tr_b16 v[130:131], v33 offset:41984\n" \
"v_mfma_f32_16x16x32_bf16 a[136:139], v[136:139], v[88:91], a[136:139]\n" \
"v_mfma_f32_16x16x32_bf16 a[136:139], v[132:135], v[84:87], a[136:139]\n" \
"v_mfma_f32_16x16x32_bf16 a[168:171], v[132:135], v[92:95], a[168:171]\n" \
"v_mfma_f32_16x16x32_bf16 a[168:171], v[136:139], v[96:99], a[168:171]\n" \
"v_mfma_f32_16x16x32_bf16 a[200:203], v[136:139], v[104:107], a[200:203]\n" \
"v_mfma_f32_16x16x32_bf16 a[200:203], v[132:135], v[100:103], a[200:203]\n" \
"v_mfma_f32_16x16x32_bf16 a[232:235], v[132:135], v[108:111], a[232:235]\n" \
"ds_read_b64_tr_b16 v[132:133], v34 offset:32768\n" \
"ds_read_b64_tr_b16 v[134:135], v34 offset:33792\n" \
"v_mfma_f32_16x16x32_bf16 a[232:235], v[136:139], v[112:115], a[232:235]\n" \
"ds_read_b64_tr_b16 v[136:137], v34 offset:40960\n" \
"ds_read_b64_tr_b16 v[138:139], v34 offset:41984\n" \
"v_mfma_f32_16x16x32_bf16 a[140:143], v[144:147], v[88:91], a[140:143]\n" \
"ds_read_b128 v[88:91], v29 offset:32768\n" \
"v_mfma_f32_16x16x32_bf16 a[140:143], v[140:143], v[84:87], a[140:143]\n" \
"ds_read_b128 v[84:87], v28 offset:32768\n" \
"v_mfma_f32_16x16x32_bf16 a[172:175], v[140:143], v[92:95], a[172:175]\n" \
"ds_read_b128 v[92:95], v28 offset:34816\n" \
"v_mfma_f32_16x16x32_bf16 a[172:175], v[144:147], v[96:99], a[172:175]\n" \
"ds_read_b128 v[96:99], v29 offset:34816\n" \
"v_mfma_f32_16x16x32_bf16 a[204:207], v[144:147], v[104:107], a[204:207]\n" \
"ds_read_b128 v[104:107], v29 offset:36864\n" \
"v_mfma_f32_16x16x32_bf16 a[204:207], v[140:143], v[100:103], a[204:207]\n" \
"ds_read_b128 v[100:103], v28 offset:36864\n" \
"v_mfma_f32_16x16x32_bf16 a[236:239], v[140:143], v[108:111], a[236:239]\n" \
"ds_read_b64_tr_b16 v[140:141], v35 offset:32768\n" \
"ds_read_b64_tr_b16 v[142:143], v35 offset:33792\n" \
"v_mfma_f32_16x16x32_bf16 a[236:239], v[144:147], v[112:115], a[236:239]\n" \
"s_add_u32 m0, %[mA], 0x1800\n" \
"s_waitcnt lgkmcnt(12)\n" \
"s_nop 0\n" \
"v_mfma_f32_16x16x32_bf16 a[16:19], v[56:59], v[152:155], a[16:19]\n" \
"s_mov_b32 s84, 0xd800\n" \
"ds_read_b64_tr_b16 v[144:145], v35 offset:40960\n" \
"v_mfma_f32_16x16x32_bf16 a[16:19], v[52:55], v[148:151], a[16:19]\n" \
"buffer_load_dwordx4 v24, s[76:79], s84 offen lds\n" \
"v_mfma_f32_16x16x32_bf16 a[48:51], v[60:63], v[148:151], a[48:51]\n" \
"s_add_u32 m0, %[mA], 0x1c00\n" \
"s_mov_b32 s85, 0xfc00\n" \
"v_mfma_f32_16x16x32_bf16 a[48:51], v[64:67], v[152:155], a[48:51]\n" \
"ds_read_b64_tr_b16 v[146:147], v35 offset:41984\n" \
"ds_read_b128 v[108:111], v28 offset:38912\n" \
"v_mfma_f32_16x16x32_bf16 a[80:83], v[72:75], v[152:155], a[80:83]\n" \
"buffer_load_dwordx4 v25, s[76:79], s85 offen lds\n" \
"v_mfma_f32_16x16x32_bf16 a[80:83], v[68:71], v[148:151], a[80:83]\n" \
"s_add_u32 m0, %[mB], 0xc00\n" \
"s_mov_b32 s84, 0x18000\n" \
"v_mfma_f32_16x16x32_bf16 a[112:115], v[76:79], v[148:151], a[112:115]\n" \
"ds_read_b128 v[112:115], v29 offset:38912\n" \
"v_add_u32_e64 v28, %[dLo], v28\n" \
"v_mfma_f32_16x16x32_bf16 a[112:115], v[80:83], v[152:155], a[112:115]\n" \
"buffer_load_dwordx4 v27, s[68:71], s84 offen lds\n" \
"v_mfma_f32_16x16x32_bf16 a[20:23], v[56:59], v[160:163], a[20:23]\n" \
"v_add_u32_e64 v29, %[dLo], v29\n" \
"v_accvgpr_read_b32 v184, a16\n" \
"v_mfma_f32_16x16x32_bf16 a[20:23], v[52:55], v[156:159], a[20:23]\n" \
"v_accvgpr_read_b32 v185, a17\n" \
"v_accvgpr_read_b32 v186, a18\n" \
"v_mfma_f32_16x16x32_bf16 a[52:55], v[60:63], v[156:159], a[52:55]\n" \
"v_accvgpr_read_b32 v187, a19\n" \
"v_accvgpr_read_b32 v180, a48\n" \
"v_mfma_f32_16x16x32_bf16 a[52:55], v[64:67], v[160:163], a[52:55]\n" \
"v_accvgpr_read_b32 v181, a49\n" \
"v_accvgpr_read_b32 v182, a50\n" \
"v_mfma_f32_16x16x32_bf16 a[84:87], v[72:75], v[160:163], a[84:87]\n" \
"v_accvgpr_read_b32 v183, a51\n" \
"v_accvgpr_read_b32 v188, a80\n" \
"v_mfma_f32_16x16x32_bf16 a[84:87], v[68:71], v[156:159], a[84:87]\n" \
"v_accvgpr_read_b32 v189, a81\n" \
"v_accvgpr_read_b32 v190, a82\n" \
"v_mfma_f32_16x16x32_bf16 a[116:119], v[76:79], v[156:159], a[116:119]\n" \
"v_accvgpr_read_b32 v191, a83\n" \
"v_cvt_pk_bf16_f32 v184, v184, v185\n" \
"v_mfma_f32_16x16x32_bf16 a[116:119], v[80:83], v[160:163], a[116:119]\n" \
"v_cvt_pk_bf16_f32 v185, v186, v187\n" \
"v_cvt_pk_bf16_f32 v186, v180, v181\n" \
"v_mfma_f32_16x16x32_bf16 a[24:27], v[56:59], v[168:171], a[24:27]\n" \
"v_cvt_pk_bf16_f32 v187, v182, v183\n" \
"v_accvgpr_read_b32 v180, a112\n" \
"v_mfma_f32_16x16x32_bf16 a[24:27], v[52:55], v[164:167], a[24:27]\n" \
"v_accvgpr_read_b32 v181, a113\n" \
"v_accvgpr_read_b32 v182, a114\n" \
"v_mfma_f32_16x16x32_bf16 a[56:59], v[60:63], v[164:167], a[56:59]\n" \
"v_accvgpr_read_b32 v183, a115\n" \
"v_accvgpr_read_b32 v192, a20\n" \
"v_mfma_f32_16x16x32_bf16 a[56:59], v[64:67], v[168:171], a[56:59]\n" \
"v_accvgpr_read_b32 v193, a21\n" \
"v_accvgpr_read_b32 v194, a22\n" \
"v_mfma_f32_16x16x32_bf16 a[88:91], v[72:75], v[168:171], a[88:91]\n" \
"v_accvgpr_read_b32 v195, a23\n" \
"v_cvt_pk_bf16_f32 v188, v188, v189\n" \
"v_mfma_f32_16x16x32_bf16 a[88:91], v[68:71], v[164:167], a[88:91]\n" \
"v_cvt_pk_bf16_f32 v189, v190, v191\n" \
"v_cvt_pk_bf16_f32 v190, v180, v181\n" \
"v_mfma_f32_16x16x32_bf16 a[120:123], v[76:79], v[164:167], a[120:123]\n" \
"v_cvt_pk_bf16_f32 v191, v182, v183\n" \
"v_accvgpr_read_b32 v180, a52\n" \
"v_mfma_f32_16x16x32_bf16 a[120:123], v[80:83], v[168:171], a[120:123]\n" \
"v_accvgpr_read_b32 v181, a53\n" \
"v_accvgpr_read_b32 v182, a54\n" \
"v_mfma_f32_16x16x32_bf16 a[28:31], v[56:59], v[176:179], a[28:31]\n" \
"v_accvgpr_read_b32 v183, a55\n" \
"v_accvgpr_read_b32 v196, a84\n" \
"v_mfma_f32_16x16x32_bf16 a[28:31], v[52:55], v[172:175], a[28:31]\n" \
"v_accvgpr_read_b32 v197, a85\n" \
"v_accvgpr_read_b32 v198, a86\n" \
"v_mfma_f32_16x16x32_bf16 a[60:63], v[60:63], v[172:175], a[60:63]\n" \
"v_accvgpr_read_b32 v199, a87\n" \
"v_cvt_pk_bf16_f32 v192, v192, v193\n" \
"v_mfma_f32_16x16x32_bf16 a[60:63], v[64:67], v[176:179], a[60:63]\n" \
"v_cvt_pk_bf16_f32 v193, v194, v195\n" \
"v_cvt_pk_bf16_f32 v194, v180, v181\n" \
"v_mfma_f32_16x16x32_bf16 a[92:95], v[72:75], v[176:179], a[92:95]\n" \
"v_cvt_pk_bf16_f32 v195, v182, v183\n" \
"v_accvgpr_read_b32 v180, a116\n" \
"v_mfma_f32_16x16x32_bf16 a[92:95], v[68:71], v[172:175], a[92:95]\n" \
"v_accvgpr_read_b32 v181, a117\n" \
"v_accvgpr_read_b32 v182, a118\n" \
"v_mfma_f32_16x16x32_bf16 a[124:127], v[76:79], v[172:175], a[124:127]\n" \
"v_accvgpr_read_b32 v183, a119\n" \
"v_accvgpr_read_b32 v200, a24\n" \
"v_mfma_f32_16x16x32_bf16 a[124:127], v[80:83], v[176:179], a[124:127]\n" \
"v_accvgpr_read_b32 v201, a25\n" \
"v_accvgpr_read_b32 v202, a26\n" \
"v_mfma_f32_16x16x32_bf16 a[0:3], v[120:123], v[56:59], a[0:3]\n" \
"v_accvgpr_read_b32 v203, a27\n" \
"v_cvt_pk_bf16_f32 v196, v196, v197\n" \
"v_mfma_f32_16x16x32_bf16 a[0:3], v[116:119], v[52:55], a[0:3]\n" \
"v_cvt_pk_bf16_f32 v197, v198, v199\n" \
"v_cvt_pk_bf16_f32 v198, v180, v181\n" \
"v_mfma_f32_16x16x32_bf16 a[32:35], v[116:119], v[60:63], a[32:35]\n" \
"v_cvt_pk_bf16_f32 v199, v182, v183\n" \
"v_accvgpr_read_b32 v180, a56\n" \
"v_mfma_f32_16x16x32_bf16 a[32:35], v[120:123], v[64:67], a[32:35]\n" \
"v_accvgpr_read_b32 v181, a57\n" \
"v_accvgpr_read_b32 v182, a58\n" \
"v_mfma_f32_16x16x32_bf16 a[64:67], v[120:123], v[72:75], a[64:67]\n" \
"v_accvgpr_read_b32 v183, a59\n" \
"v_accvgpr_read_b32 v204, a88\n" \
"v_mfma_f32_16x16x32_bf16 a[64:67], v[116:119], v[68:71], a[64:67]\n" \
"v_accvgpr_read_b32 v205, a89\n" \
"v_accvgpr_read_b32 v206, a90\n" \
"v_mfma_f32_16x16x32_bf16 a[96:99], v[116:119], v[76:79], a[96:99]\n" \
"v_accvgpr_read_b32 v207, a91\n" \
"v_cvt_pk_bf16_f32 v200, v200, v201\n" \
"v_mfma_f32_16x16x32_bf16 a[96:99], v[120:123], v[80:83], a[96:99]\n" \
"v_cvt_pk_bf16_f32 v201, v202, v203\n" \
"v_cvt_pk_bf16_f32 v202, v180, v181\n" \
"v_mfma_f32_16x16x32_bf16 a[4:7], v[128:131], v[56:59], a[4:7]\n" \
"v_cvt_pk_bf16_f32 v203, v182, v183\n" \
"v_accvgpr_read_b32 v180, a120\n" \
"v_mfma_f32_16x16x32_bf16 a[4:7], v[124:127], v[52:55], a[4:7]\n" \
"v_accvgpr_read_b32 v181, a121\n" \
"v_accvgpr_read_b32 v182, a122\n" \
"L_pf3:\n" \
"s_cbranch_execz L_pf7\n" \
"s_nop 0\n" \
"v_mfma_f32_16x16x32_bf16 a[36:39], v[124:127], v[60:63], a[36:39]\n" \
"v_accvgpr_read_b32 v183, a123\n" \
"v_accvgpr_read_b32 v208, a28\n" \
"v_mfma_f32_16x16x32_bf16 a[36:39], v[128:131], v[64:67], a[36:39]\n" \
"v_accvgpr_read_b32 v209, a29\n" \
"v_accvgpr_read_b32 v210, a30\n" \
"v_mfma_f32_16x16x32_bf16 a[68:71], v[128:131], v[72:75], a[68:71]\n" \
"v_accvgpr_read_b32 v211, a31\n" \
"v_cvt_pk_bf16_f32 v204, v204, v205\n" \
"v_mfma_f32_16x16x32_bf16 a[68:71], v[124:127], v[68:71], a[68:71]\n" \
"v_cvt_pk_bf16_f32 v205, v206, v207\n" \
"v_cvt_pk_bf16_f32 v206, v180, v181\n" \
"v_mfma_f32_16x16x32_bf16 a[100:103], v[124:127], v[76:79], a[100:103]\n" \
"v_cvt_pk_bf16_f32 v207, v182, v183\n" \
"v_accvgpr_read_b32 v180, a60\n" \
"v_mfma_f32_16x16x32_bf16 a[100:103], v[128:131], v[80:83], a[100:103]\n" \
"v_accvgpr_read_b32 v181, a61\n" \
"s_waitcnt lgkmcnt(0)\n" \
"s_nop 0\n" \
"v_mfma_f32_16x16x32_bf16 a[8:11], v[136:139], v[56:59], a[8:11]\n" \
"v_accvgpr_read_b32 v182, a62\n" \
"v_accvgpr_read_b32 v183, a63\n" \
"v_mfma_f32_16x16x32_bf16 a[8:11], v[132:135], v[52:55], a[8:11]\n" \
"v_accvgpr_read_b32 v212, a92\n" \
"v_accvgpr_read_b32 v213, a93\n" \
"v_mfma_f32_16x16x32_bf16 a[40:43], v[132:135], v[60:63], a[40:43]\n" \
"v_accvgpr_read_b32 v214, a94\n" \
"v_accvgpr_read_b32 v215, a95\n" \
"v_mfma_f32_16x16x32_bf16 a[40:43], v[136:139], v[64:67], a[40:43]\n" \
"v_cvt_pk_bf16_f32 v208, v208, v209\n" \
"v_cvt_pk_bf16_f32 v209, v210, v211\n" \
"v_mfma_f32_16x16x32_bf16 a[72:75], v[136:139], v[72:75], a[72:75]\n" \
"v_cvt_pk_bf16_f32 v210, v180, v181\n" \
"v_cvt_pk_bf16_f32 v211, v182, v183\n" \
"v_mfma_f32_16x16x32_bf16 a[72:75], v[132:135], v[68:71], a[72:75]\n" \
"v_accvgpr_read_b32 v180, a124\n" \
"v_accvgpr_read_b32 v181, a125\n" \
"v_mfma_f32_16x16x32_bf16 a[104:107], v[132:135], v[76:79], a[104:107]\n" \
"v_accvgpr_read_b32 v182, a126\n" \
"v_accvgpr_read_b32 v183, a127\n" \
"v_mfma_f32_16x16x32_bf16 a[104:107], v[136:139], v[80:83], a[104:107]\n" \
"v_accvgpr_read_b32 v216, a0\n" \
"v_accvgpr_read_b32 v217, a1\n" \
"v_mfma_f32_16x16x32_bf16 a[12:15], v[144:147], v[56:59], a[12:15]\n" \
"v_accvgpr_read_b32 v218, a2\n" \
"v_accvgpr_read_b32 v219, a3\n" \
"v_mfma_f32_16x16x32_bf16 a[12:15], v[140:143], v[52:55], a[12:15]\n" \
"v_cvt_pk_bf16_f32 v212, v212, v213\n" \
"v_cvt_pk_bf16_f32 v213, v214, v215\n" \
"v_mfma_f32_16x16x32_bf16 a[44:47], v[140:143], v[60:63], a[44:47]\n" \
"v_cvt_pk_bf16_f32 v214, v180, v181\n" \
"v_cvt_pk_bf16_f32 v215, v182, v183\n" \
"v_mfma_f32_16x16x32_bf16 a[44:47], v[144:147], v[64:67], a[44:47]\n" \
"v_accvgpr_read_b32 v180, a4\n" \
"v_accvgpr_read_b32 v181, a5\n" \
"v_mfma_f32_16x16x32_bf16 a[76:79], v[144:147], v[72:75], a[76:79]\n" \
"v_accvgpr_read_b32 v182, a6\n" \
"v_accvgpr_read_b32 v183, a7\n" \
"v_mfma_f32_16x16x32_bf16 a[76:79], v[140:143], v[68:71], a[76:79]\n" \
"v_cvt_pk_bf16_f32 v216, v216, v217\n" \
"v_cvt_pk_bf16_f32 v217, v218, v219\n" \
"v_mfma_f32_16x16x32_bf16 a[108:111], v[140:143], v[76:79], a[108:111]\n" \
"v_accvgpr_read_b32 v220, a32\n" \
"v_accvgpr_read_b32 v221, a33\n" \
"v_mfma_f32_16x16x32_bf16 a[108:111], v[144:147], v[80:83], a[108:111]\n" \
"v_cvt_pk_bf16_f32 v218, v180, v181\n" \
"v_cvt_pk_bf16_f32 v219, v182, v183\n" \
"s_waitcnt vmcnt(0)\n" \
"s_barrier\n" \
"v_mfma_f32_16x16x32_bf16 a[128:131], v[120:123], v[88:91], a[128:131]\n" \
"s_add_u32 m0, %[mL], 0x8000\n" \
"s_add_u32 s85, %[oL], 0x2000\n" \
"v_mfma_f32_16x16x32_bf16 a[128:131], v[116:119], v[84:87], a[128:131]\n" \
"buffer_load_dwordx4 v20, s[80:83], s85 offen sc1 lds\n" \
"v_mfma_f32_16x16x32_bf16 a[160:163], v[116:119], v[92:95], a[160:163]\n" \
"ds_read_b128 v[52:55], v28 offset:0\n" \
"ds_read_b128 v[56:59], v29 offset:0\n" \
"v_mfma_f32_16x16x32_bf16 a[160:163], v[120:123], v[96:99], a[160:163]\n" \
"s_add_u32 m0, %[mL], 0x9000\n" \
"s_add_u32 s84, %[oL], 0x3000\n" \
"v_mfma_f32_16x16x32_bf16 a[192:195], v[120:123], v[104:107], a[192:195]\n" \
"buffer_load_dwordx4 v20, s[80:83], s84 offen sc1 lds\n" \
"v_mfma_f32_16x16x32_bf16 a[192:195], v[116:119], v[100:103], a[192:195]\n" \
"ds_read_b128 v[60:63], v28 offset:2048\n" \
"ds_read_b128 v[64:67], v29 offset:2048\n" \
"v_mfma_f32_16x16x32_bf16 a[224:227], v[116:119], v[108:111], a[224:227]\n" \
"ds_read_b64_tr_b16 v[116:117], v36 offset:0\n" \
"ds_read_b64_tr_b16 v[118:119], v36 offset:512\n" \
"v_mfma_f32_16x16x32_bf16 a[224:227], v[120:123], v[112:115], a[224:227]\n" \
"s_add_u32 m0, %[mL], 0xb000\n" \
"s_mov_b32 s85, 0x80\n" \
"v_mfma_f32_16x16x32_bf16 a[132:135], v[128:131], v[88:91], a[132:135]\n" \
"buffer_load_dwordx4 v21, s[76:79], s85 offen lds\n" \
"v_mfma_f32_16x16x32_bf16 a[132:135], v[124:127], v[84:87], a[132:135]\n" \
"ds_read_b64_tr_b16 v[120:121], v36 offset:4096\n" \
"ds_read_b64_tr_b16 v[122:123], v36 offset:4608\n" \
"v_mfma_f32_16x16x32_bf16 a[164:167], v[124:127], v[92:95], a[164:167]\n" \
"ds_read_b128 v[68:71], v28 offset:4096\n" \
"ds_read_b128 v[72:75], v29 offset:4096\n" \
"v_mfma_f32_16x16x32_bf16 a[164:167], v[128:131], v[96:99], a[164:167]\n" \
"s_add_u32 m0, %[mA], 0x8000\n" \
"s_mov_b32 s84, 0x80\n" \
"v_mfma_f32_16x16x32_bf16 a[196:199], v[128:131], v[104:107], a[196:199]\n" \
"buffer_load_dwordx4 v24, s[76:79], s84 offen lds\n" \
"v_mfma_f32_16x16x32_bf16 a[196:199], v[124:127], v[100:103], a[196:199]\n" \
"ds_read_b128 v[76:79], v28 offset:6144\n" \
"ds_read_b128 v[80:83], v29 offset:6144\n" \
"v_mfma_f32_16x16x32_bf16 a[228:231], v[124:127], v[108:111], a[228:231]\n" \
"ds_read_b64_tr_b16 v[124:125], v37 offset:0\n" \
"ds_read_b64_tr_b16 v[126:127], v37 offset:512\n" \
"v_mfma_f32_16x16x32_bf16 a[228:231], v[128:131], v[112:115], a[228:231]\n" \
"s_add_u32 m0, %[mA], 0x8400\n" \
"s_mov_b32 s85, 0x2480\n" \
"v_mfma_f32_16x16x32_bf16 a[136:139], v[136:139], v[88:91], a[136:139]\n" \
"buffer_load_dwordx4 v25, s[76:79], s85 offen lds\n" \
"v_mfma_f32_16x16x32_bf16 a[136:139], v[132:135], v[84:87], a[136:139]\n" \
"ds_read_b64_tr_b16 v[128:129], v37 offset:4096\n" \
"ds_read_b64_tr_b16 v[130:131], v37 offset:4608\n" \
"v_mfma_f32_16x16x32_bf16 a[168:171], v[132:135], v[92:95], a[168:171]\n" \
"v_accvgpr_read_b32 v222, a34\n" \
"v_accvgpr_read_b32 v223, a35\n" \
"v_mfma_f32_16x16x32_bf16 a[168:171], v[136:139], v[96:99], a[168:171]\n" \
"s_add_u32 m0, %[mB], 0x8000\n" \
"s_mov_b32 s84, 0x80000\n" \
"v_mfma_f32_16x16x32_bf16 a[200:203], v[136:139], v[104:107], a[200:203]\n" \
"buffer_load_dwordx4 v26, s[68:71], s84 offen lds\n" \
"v_mfma_f32_16x16x32_bf16 a[200:203], v[132:135], v[100:103], a[200:203]\n" \
"v_cvt_pk_bf16_f32 v220, v220, v221\n" \
"v_accvgpr_read_b32 v180, a36\n" \
"v_mfma_f32_16x16x32_bf16 a[232:235], v[132:135], v[108:111], a[232:235]\n" \
"ds_read_b64_tr_b16 v[132:133], v38 offset:0\n" \
"ds_read_b64_tr_b16 v[134:135], v38 offset:512\n" \
"v_mfma_f32_16x16x32_bf16 a[232:235], v[136:139], v[112:115], a[232:235]\n" \
"s_add_u32 m0, %[mA], 0x8800\n" \
"s_mov_b32 s85, 0x4880\n" \
"v_mfma_f32_16x16x32_bf16 a[140:143], v[144:147], v[88:91], a[140:143]\n" \
"buffer_load_dwordx4 v24, s[76:79], s85 offen lds\n" \
"v_mfma_f32_16x16x32_bf16 a[140:143], v[140:143], v[84:87], a[140:143]\n" \
"ds_read_b64_tr_b16 v[136:137], v38 offset:4096\n" \
"ds_read_b64_tr_b16 v[138:139], v38 offset:4608\n" \
"v_mfma_f32_16x16x32_bf16 a[172:175], v[140:143], v[92:95], a[172:175]\n" \
"v_accvgpr_read_b32 v181, a37\n" \
"v_accvgpr_read_b32 v182, a38\n" \
"v_mfma_f32_16x16x32_bf16 a[172:175], v[144:147], v[96:99], a[172:175]\n" \
"s_add_u32 m0, %[mA], 0x8c00\n" \
"s_mov_b32 s84, 0x6c80\n" \
"v_mfma_f32_16x16x32_bf16 a[204:207], v[144:147], v[104:107], a[204:207]\n" \
"buffer_load_dwordx4 v25, s[76:79], s84 offen lds\n" \
"v_mfma_f32_16x16x32_bf16 a[204:207], v[140:143], v[100:103], a[204:207]\n" \
"v_accvgpr_read_b32 v183, a39\n" \
"v_cvt_pk_bf16_f32 v221, v222, v223\n" \
"v_mfma_f32_16x16x32_bf16 a[236:239], v[140:143], v[108:111], a[236:239]\n" \
"ds_read_b64_tr_b16 v[140:141], v39 offset:0\n" \
"ds_read_b64_tr_b16 v[142:143], v39 offset:512\n" \
"v_mfma_f32_16x16x32_bf16 a[236:239], v[144:147], v[112:115], a[236:239]\n" \
"s_add_u32 m0, %[mB], 0x8400\n" \
"s_mov_b32 s85, 0x88000\n" \
"v_mfma_f32_16x16x32_bf16 a[144:147], v[88:91], v[152:155], a[144:147]\n" \
"buffer_load_dwordx4 v26, s[68:71], s85 offen lds\n" \
"v_mfma_f32_16x16x32_bf16 a[144:147], v[84:87], v[148:151], a[144:147]\n" \
"ds_read_b64_tr_b16 v[144:145], v39 offset:4096\n" \
"ds_read_b64_tr_b16 v[146:147], v39 offset:4608\n" \
"v_mfma_f32_16x16x32_bf16 a[148:151], v[84:87], v[156:159], a[148:151]\n" \
"v_accvgpr_read_b32 v224, a64\n" \
"v_accvgpr_read_b32 v225, a65\n" \
"v_mfma_f32_16x16x32_bf16 a[148:151], v[88:91], v[160:163], a[148:151]\n" \
"s_add_u32 m0, %[mA], 0x9000\n" \
"s_mov_b32 s84, 0x9080\n" \
"v_mfma_f32_16x16x32_bf16 a[152:155], v[88:91], v[168:171], a[152:155]\n" \
"buffer_load_dwordx4 v24, s[76:79], s84 offen lds\n" \
"v_mfma_f32_16x16x32_bf16 a[152:155], v[84:87], v[164:167], a[152:155]\n" \
"v_cvt_pk_bf16_f32 v222, v180, v181\n" \
"v_cvt_pk_bf16_f32 v223, v182, v183\n" \
"v_mfma_f32_16x16x32_bf16 a[156:159], v[84:87], v[172:175], a[156:159]\n" \
"ds_read_b128 v[84:87], v22 offset:0\n" \
"v_accvgpr_read_b32 v226, a66\n" \
"v_mfma_f32_16x16x32_bf16 a[156:159], v[88:91], v[176:179], a[156:159]\n" \
"s_add_u32 m0, %[mA], 0x9400\n" \
"s_mov_b32 s85, 0xb480\n" \
"v_mfma_f32_16x16x32_bf16 a[176:179], v[96:99], v[152:155], a[176:179]\n" \
"buffer_load_dwordx4 v25, s[76:79], s85 offen lds\n" \
"v_mfma_f32_16x16x32_bf16 a[176:179], v[92:95], v[148:151], a[176:179]\n" \
"ds_read_b128 v[88:91], v23 offset:0\n" \
"v_accvgpr_read_b32 v227, a67\n" \
"v_mfma_f32_16x16x32_bf16 a[180:183], v[92:95], v[156:159], a[180:183]\n" \
"v_cvt_pk_bf16_f32 v224, v224, v225\n" \
"v_accvgpr_read_b32 v180, a68\n" \
"v_mfma_f32_16x16x32_bf16 a[180:183], v[96:99], v[160:163], a[180:183]\n" \
"s_add_u32 m0, %[mB], 0x8800\n" \
"s_mov_b32 s84, 0x90000\n" \
"v_mfma_f32_16x16x32_bf16 a[184:187], v[96:99], v[168:171], a[184:187]\n" \
"buffer_load_dwordx4 v27, s[68:71], s84 offen lds\n" \
"v_mfma_f32_16x16x32_bf16 a[184:187], v[92:95], v[164:167], a[184:187]\n" \
"v_accvgpr_read_b32 v181, a69\n" \
"v_accvgpr_read_b32 v182, a70\n" \
"v_mfma_f32_16x16x32_bf16 a[188:191], v[92:95], v[172:175], a[188:191]\n" \
"ds_read_b128 v[92:95], v48 offset:2048\n" \
"v_accvgpr_read_b32 v183, a71\n" \
"v_mfma_f32_16x16x32_bf16 a[188:191], v[96:99], v[176:179], a[188:191]\n" \
"ds_read_b128 v[96:99], v49 offset:2048\n" \
"v_cvt_pk_bf16_f32 v225, v226, v227\n" \
"v_mfma_f32_16x16x32_bf16 a[208:211], v[104:107], v[152:155], a[208:211]\n" \
"v_accvgpr_read_b32 v228, a96\n" \
"v_accvgpr_read_b32 v229, a97\n" \
"v_mfma_f32_16x16x32_bf16 a[208:211], v[100:103], v[148:151], a[208:211]\n" \
"v_cvt_pk_bf16_f32 v226, v180, v181\n" \
"v_cvt_pk_bf16_f32 v227, v182, v183\n" \
"v_mfma_f32_16x16x32_bf16 a[212:215], v[100:103], v[156:159], a[212:215]\n" \
"v_accvgpr_read_b32 v230, a98\n" \
"v_accvgpr_read_b32 v231, a99\n" \
"v_mfma_f32_16x16x32_bf16 a[212:215], v[104:107], v[160:163], a[212:215]\n" \
"v_cvt_pk_bf16_f32 v228, v228, v229\n" \
"v_accvgpr_read_b32 v180, a100\n" \
"v_mfma_f32_16x16x32_bf16 a[216:219], v[104:107], v[168:171], a[216:219]\n" \
"v_accvgpr_read_b32 v181, a101\n" \
"v_accvgpr_read_b32 v182, a102\n" \
"v_mfma_f32_16x16x32_bf16 a[216:219], v[100:103], v[164:167], a[216:219]\n" \
"v_accvgpr_read_b32 v183, a103\n" \
"v_cvt_pk_bf16_f32 v229, v230, v231\n" \
"v_mfma_f32_16x16x32_bf16 a[220:223], v[100:103], v[172:175], a[220:223]\n" \
"ds_read_b128 v[100:103], v48 offset:4096\n" \
"v_accvgpr_read_b32 v232, a8\n" \
"v_mfma_f32_16x16x32_bf16 a[220:223], v[104:107], v[176:179], a[220:223]\n" \
"ds_read_b128 v[104:107], v49 offset:4096\n" \
"v_accvgpr_read_b32 v233, a9\n" \
"v_mfma_f32_16x16x32_bf16 a[240:243], v[112:115], v[152:155], a[240:243]\n" \
"ds_read_b64_tr_b16 v[152:153], v32 offset:8192\n" \
"ds_read_b64_tr_b16 v[154:155], v32 offset:9216\n" \
"v_mfma_f32_16x16x32_bf16 a[240:243], v[108:111], v[148:151], a[240:243]\n" \
"ds_read_b64_tr_b16 v[148:149], v32 offset:0\n" \
"ds_read_b64_tr_b16 v[150:151], v32 offset:1024\n" \
"v_mfma_f32_16x16x32_bf16 a[244:247], v[108:111], v[156:159], a[244:247]\n" \
"ds_read_b64_tr_b16 v[156:157], v33 offset:0\n" \
"ds_read_b64_tr_b16 v[158:159], v33 offset:1024\n" \
"v_mfma_f32_16x16x32_bf16 a[244:247], v[112:115], v[160:163], a[244:247]\n" \
"ds_read_b64_tr_b16 v[160:161], v33 offset:8192\n" \
"ds_read_b64_tr_b16 v[162:163], v33 offset:9216\n" \
"v_mfma_f32_16x16x32_bf16 a[248:251], v[112:115], v[168:171], a[248:251]\n" \
"ds_read_b64_tr_b16 v[168:169], v34 offset:8192\n" \
"ds_read_b64_tr_b16 v[170:171], v34 offset:9216\n" \
"v_mfma_f32_16x16x32_bf16 a[248:251], v[108:111], v[164:167], a[248:251]\n" \
"ds_read_b64_tr_b16 v[164:165], v34 offset:0\n" \
"ds_read_b64_tr_b16 v[166:167], v34 offset:1024\n" \
"v_mfma_f32_16x16x32_bf16 a[252:255], v[108:111], v[172:175], a[252:255]\n" \
"ds_read_b64_tr_b16 v[172:173], v35 offset:0\n" \
"ds_read_b64_tr_b16 v[174:175], v35 offset:1024\n" \
"v_mfma_f32_16x16x32_bf16 a[252:255], v[112:115], v[176:179], a[252:255]\n" \
"s_add_u32 m0, %[mA], 0x9800\n" \
"s_waitcnt lgkmcnt(15)\n" \
"s_nop 0\n" \
"v_mfma_f32_16x16x32_bf16 a[16:19], v[144:147], v[88:91], 0\n" \
"s_mov_b32 s84, 0xd880\n" \
"ds_read_b64_tr_b16 v[176:177], v35 offset:8192\n" \
"v_mfma_f32_16x16x32_bf16 a[16:19], v[140:143], v[84:87], a[16:19]\n" \
"buffer_load_dwordx4 v24, s[76:79], s84 offen lds\n" \
"v_mfma_f32_16x16x32_bf16 a[48:51], v[124:127], v[84:87], 0\n" \
"s_add_u32 m0, %[mA], 0x9c00\n" \
"s_mov_b32 s85, 0xfc80\n" \
"v_mfma_f32_16x16x32_bf16 a[48:51], v[128:131], v[88:91], a[48:51]\n" \
"ds_read_b64_tr_b16 v[178:179], v35 offset:9216\n" \
"ds_read_b128 v[108:111], v48 offset:6144\n" \
"v_mfma_f32_16x16x32_bf16 a[80:83], v[88:91], v[136:139], 0\n" \
"buffer_load_dwordx4 v25, s[76:79], s85 offen lds\n" \
"v_mfma_f32_16x16x32_bf16 a[80:83], v[84:87], v[132:135], a[80:83]\n" \
"s_add_u32 m0, %[mB], 0x8c00\n" \
"s_mov_b32 s84, 0x98000\n" \
"v_mfma_f32_16x16x32_bf16 a[112:115], v[140:143], v[52:55], 0\n" \
"ds_read_b128 v[84:87], v48 offset:0\n" \
"ds_read_b128 v[88:91], v49 offset:0\n" \
"buffer_load_dwordx4 v27, s[68:71], s84 offen lds\n" \
"v_mfma_f32_16x16x32_bf16 a[112:115], v[144:147], v[56:59], a[112:115]\n" \
"ds_read_b128 v[112:115], v49 offset:6144\n" \
"v_cvt_pk_bf16_f32 v230, v180, v181\n" \
"v_mfma_f32_16x16x32_bf16 a[20:23], v[128:131], v[56:59], 0\n" \
"v_cvt_pk_bf16_f32 v231, v182, v183\n" \
"v_accvgpr_read_b32 v234, a10\n" \
"v_mfma_f32_16x16x32_bf16 a[20:23], v[124:127], v[52:55], a[20:23]\n" \
"s_add_u32 s88, %[oVA], 0x0\n" \
"s_nop 0\n" \
"v_accvgpr_read_b32 v235, a11\n" \
"v_mfma_f32_16x16x32_bf16 a[52:55], v[116:119], v[52:55], 0\n" \
"buffer_store_dwordx4 v[184:187], v20, s[80:83], s88 offen offset:0 sc1\n" \
"v_mfma_f32_16x16x32_bf16 a[52:55], v[120:123], v[56:59], a[52:55]\n" \
"v_cvt_pk_bf16_f32 v232, v232, v233\n" \
"v_accvgpr_read_b32 v180, a12\n" \
"v_mfma_f32_16x16x32_bf16 a[84:87], v[56:59], v[136:139], 0\n" \
"v_accvgpr_read_b32 v181, a13\n" \
"v_accvgpr_read_b32 v182, a14\n" \
"v_mfma_f32_16x16x32_bf16 a[84:87], v[52:55], v[132:135], a[84:87]\n" \
"v_accvgpr_read_b32 v183, a15\n" \
"v_cvt_pk_bf16_f32 v233, v234, v235\n" \
"v_mfma_f32_16x16x32_bf16 a[116:119], v[140:143], v[60:63], 0\n" \
"v_accvgpr_read_b32 v236, a40\n" \
"v_accvgpr_read_b32 v237, a41\n" \
"v_mfma_f32_16x16x32_bf16 a[116:119], v[144:147], v[64:67], a[116:119]\n" \
"v_cvt_pk_bf16_f32 v234, v180, v181\n" \
"v_cvt_pk_bf16_f32 v235, v182, v183\n" \
"v_mfma_f32_16x16x32_bf16 a[24:27], v[128:131], v[64:67], 0\n" \
"v_accvgpr_read_b32 v238, a42\n" \
"v_accvgpr_read_b32 v239, a43\n" \
"v_mfma_f32_16x16x32_bf16 a[24:27], v[124:127], v[60:63], a[24:27]\n" \
"v_cvt_pk_bf16_f32 v236, v236, v237\n" \
"v_accvgpr_read_b32 v180, a44\n" \
"v_mfma_f32_16x16x32_bf16 a[56:59], v[116:119], v[60:63], 0\n" \
"v_accvgpr_read_b32 v181, a45\n" \
"v_accvgpr_read_b32 v182, a46\n" \
"v_mfma_f32_16x16x32_bf16 a[56:59], v[120:123], v[64:67], a[56:59]\n" \
"s_add_u32 s89, %[oVA], 0x2000\n" \
"v_accvgpr_read_b32 v183, a47\n" \
"v_mfma_f32_16x16x32_bf16 a[88:91], v[64:67], v[136:139], 0\n" \
"buffer_store_dwordx4 v[188:191], v20, s[80:83], s89 offen offset:0 sc1\n" \
"v_mfma_f32_16x16x32_bf16 a[88:91], v[60:63], v[132:135], a[88:91]\n" \
"v_cvt_pk_bf16_f32 v237, v238, v239\n" \
"v_accvgpr_read_b32 v240, a72\n" \
"v_mfma_f32_16x16x32_bf16 a[120:123], v[140:143], v[68:71], 0\n" \
"v_accvgpr_read_b32 v241, a73\n" \
"v_cvt_pk_bf16_f32 v238, v180, v181\n" \
"v_mfma_f32_16x16x32_bf16 a[120:123], v[144:147], v[72:75], a[120:123]\n" \
"v_cvt_pk_bf16_f32 v239, v182, v183\n" \
"v_accvgpr_read_b32 v242, a74\n" \
"v_mfma_f32_16x16x32_bf16 a[28:31], v[128:131], v[72:75], 0\n" \
"v_accvgpr_read_b32 v243, a75\n" \
"v_cvt_pk_bf16_f32 v240, v240, v241\n" \
"v_mfma_f32_16x16x32_bf16 a[28:31], v[124:127], v[68:71], a[28:31]\n" \
"v_accvgpr_read_b32 v180, a76\n" \
"v_accvgpr_read_b32 v181, a77\n" \
"v_mfma_f32_16x16x32_bf16 a[60:63], v[116:119], v[68:71], 0\n" \
"v_accvgpr_read_b32 v182, a78\n" \
"v_accvgpr_read_b32 v183, a79\n" \
"v_mfma_f32_16x16x32_bf16 a[60:63], v[120:123], v[72:75], a[60:63]\n" \
"v_cvt_pk_bf16_f32 v241, v242, v243\n" \
"v_accvgpr_read_b32 v244, a104\n" \
"v_mfma_f32_16x16x32_bf16 a[92:95], v[72:75], v[136:139], 0\n" \
"v_accvgpr_read_b32 v245, a105\n" \
"v_cvt_pk_bf16_f32 v242, v180, v181\n" \
"v_mfma_f32_16x16x32_bf16 a[92:95], v[68:71], v[132:135], a[92:95]\n" \
"v_cvt_pk_bf16_f32 v243, v182, v183\n" \
"v_accvgpr_read_b32 v246, a106\n" \
"v_mfma_f32_16x16x32_bf16 a[124:127], v[140:143], v[76:79], 0\n" \
"v_accvgpr_read_b32 v247, a107\n" \
"v_cvt_pk_bf16_f32 v244, v244, v245\n" \
"v_mfma_f32_16x16x32_bf16 a[124:127], v[144:147], v[80:83], a[124:127]\n" \
"v_accvgpr_read_b32 v248, a128\n" \
"v_accvgpr_read_b32 v249, a129\n" \
"v_mfma_f32_16x16x32_bf16 a[0:3], v[128:131], v[80:83], 0\n" \
"v_accvgpr_read_b32 v250, a130\n" \
"v_accvgpr_read_b32 v251, a131\n" \
"v_mfma_f32_16x16x32_bf16 a[0:3], v[124:127], v[76:79], a[0:3]\n" \
"v_accvgpr_read_b32 v180, a108\n" \
"v_accvgpr_read_b32 v181, a109\n" \
"v_mfma_f32_16x16x32_bf16 a[4:7], v[116:119], v[76:79], 0\n" \
"v_accvgpr_read_b32 v182, a110\n" \
"v_accvgpr_read_b32 v183, a111\n" \
"v_mfma_f32_16x16x32_bf16 a[4:7], v[120:123], v[80:83], a[4:7]\n" \
"v_cvt_pk_bf16_f32 v245, v246, v247\n" \
"v_cvt_pk_bf16_f32 v246, v180, v181\n" \
"v_mfma_f32_16x16x32_bf16 a[32:35], v[80:83], v[136:139], 0\n" \
"v_cvt_pk_bf16_f32 v247, v182, v183\n" \
"v_cvt_pk_bf16_f32 v248, v248, v249\n" \
"v_mfma_f32_16x16x32_bf16 a[32:35], v[76:79], v[132:135], a[32:35]\n" \
"v_accvgpr_read_b32 v180, a132\n" \
"s_waitcnt lgkmcnt(0)\n" \
"s_nop 0\n" \
"v_mfma_f32_16x16x32_bf16 a[128:131], v[148:151], v[52:55], 0\n" \
"v_accvgpr_read_b32 v181, a133\n" \
"v_accvgpr_read_b32 v182, a134\n" \
"v_mfma_f32_16x16x32_bf16 a[128:131], v[152:155], v[56:59], a[128:131]\n" \
"v_accvgpr_read_b32 v183, a135\n" \
"v_cvt_pk_bf16_f32 v249, v250, v251\n" \
"v_accvgpr_read_b32 v252, a160\n" \
"v_accvgpr_read_b32 v253, a161\n" \
"v_cvt_pk_bf16_f32 v250, v180, v181\n" \
"v_cvt_pk_bf16_f32 v251, v182, v183\n" \
"v_accvgpr_read_b32 v254, a162\n" \
"v_accvgpr_read_b32 v255, a163\n" \
"v_cvt_pk_bf16_f32 v252, v252, v253\n" \
"v_accvgpr_read_b32 v180, a164\n" \
"v_accvgpr_read_b32 v181, a165\n" \
"v_accvgpr_read_b32 v182, a166\n" \
"v_accvgpr_read_b32 v183, a167\n" \
"v_cvt_pk_bf16_f32 v253, v254, v255\n" \
"v_cvt_pk_bf16_f32 v254, v180, v181\n" \
"v_cvt_pk_bf16_f32 v255, v182, v183\n" \
"v_accvgpr_write_b32 a36, v252\n" \
"v_accvgpr_write_b32 a37, v253\n" \
"v_mfma_f32_16x16x32_bf16 a[132:135], v[160:163], v[56:59], 0\n" \
"v_accvgpr_write_b32 a38, v254\n" \
"v_accvgpr_write_b32 a39, v255\n" \
"v_accvgpr_read_b32 v252, a192\n" \
"v_accvgpr_read_b32 v253, a193\n" \
"v_accvgpr_read_b32 v254, a194\n" \
"v_accvgpr_read_b32 v255, a195\n" \
"v_cvt_pk_bf16_f32 v252, v252, v253\n" \
"s_add_u32 s88, %[oVA], 0x0\n" \
"s_nop 0\n" \
"v_accvgpr_read_b32 v180, a196\n" \
"v_accvgpr_read_b32 v181, a197\n" \
"v_accvgpr_read_b32 v182, a198\n" \
"v_accvgpr_read_b32 v183, a199\n" \
"v_cvt_pk_bf16_f32 v253, v254, v255\n" \
"buffer_store_dwordx4 v[192:195], v20, s[80:83], s88 offen offset:1024 sc1\n" \
"v_cvt_pk_bf16_f32 v254, v180, v181\n" \
"v_cvt_pk_bf16_f32 v255, v182, v183\n" \
"v_accvgpr_write_b32 a64, v252\n" \
"v_accvgpr_write_b32 a65, v253\n" \
"v_mfma_f32_16x16x32_bf16 a[132:135], v[156:159], v[52:55], a[132:135]\n" \
"v_accvgpr_write_b32 a66, v254\n" \
"v_accvgpr_write_b32 a67, v255\n" \
"v_accvgpr_read_b32 v252, a224\n" \
"v_accvgpr_read_b32 v253, a225\n" \
"v_accvgpr_read_b32 v254, a226\n" \
"v_accvgpr_read_b32 v255, a227\n" \
"v_cvt_pk_bf16_f32 v252, v252, v253\n" \
"v_accvgpr_read_b32 v180, a228\n" \
"v_accvgpr_read_b32 v181, a229\n" \
"v_accvgpr_read_b32 v182, a230\n" \
"v_accvgpr_read_b32 v183, a231\n" \
"v_cvt_pk_bf16_f32 v253, v254, v255\n" \
"v_cvt_pk_bf16_f32 v254, v180, v181\n" \
"v_cvt_pk_bf16_f32 v255, v182, v183\n" \
"v_accvgpr_write_b32 a68, v252\n" \
"v_accvgpr_write_b32 a69, v253\n" \
"v_accvgpr_write_b32 a70, v254\n" \
"v_accvgpr_write_b32 a71, v255\n" \
"v_accvgpr_read_b32 v252, a136\n" \
"v_accvgpr_read_b32 v253, a137\n" \
"v_accvgpr_read_b32 v254, a138\n" \
"v_accvgpr_read_b32 v255, a139\n" \
"v_mfma_f32_16x16x32_bf16 a[136:139], v[164:167], v[52:55], 0\n" \
"v_accvgpr_read_b32 v180, a140\n" \
"v_accvgpr_read_b32 v181, a141\n" \
"v_mfma_f32_16x16x32_bf16 a[136:139], v[168:171], v[56:59], a[136:139]\n" \
"v_accvgpr_read_b32 v182, a142\n" \
"v_accvgpr_read_b32 v183, a143\n" \
"v_mfma_f32_16x16x32_bf16 a[140:143], v[176:179], v[56:59], 0\n" \
"v_cvt_pk_bf16_f32 v252, v252, v253\n" \
"s_add_u32 s89, %[oVA], 0x2000\n" \
"v_mfma_f32_16x16x32_bf16 a[140:143], v[172:175], v[52:55], a[140:143]\n" \
"v_cvt_pk_bf16_f32 v253, v254, v255\n" \
"v_cvt_pk_bf16_f32 v254, v180, v181\n" \
"v_mfma_f32_16x16x32_bf16 a[160:163], v[148:151], v[60:63], 0\n" \
"buffer_store_dwordx4 v[196:199], v20, s[80:83], s89 offen offset:1024 sc1\n" \
"v_mfma_f32_16x16x32_bf16 a[160:163], v[152:155], v[64:67], a[160:163]\n" \
"v_cvt_pk_bf16_f32 v255, v182, v183\n" \
"v_accvgpr_write_b32 a96, v252\n" \
"s_waitcnt vmcnt(4)\n" \
"s_barrier\n" \
"v_mfma_f32_16x16x32_bf16 a[164:167], v[160:163], v[64:67], 0\n" \
"v_accvgpr_write_b32 a97, v253\n" \
"v_accvgpr_write_b32 a98, v254\n" \
"v_mfma_f32_16x16x32_bf16 a[164:167], v[156:159], v[60:63], a[164:167]\n" \
"v_accvgpr_write_b32 a99, v255\n" \
"v_accvgpr_read_b32 v252, a168\n" \
"v_accvgpr_read_b32 v253, a169\n" \
"v_accvgpr_read_b32 v254, a170\n" \
"v_accvgpr_read_b32 v255, a171\n" \
"v_mfma_f32_16x16x32_bf16 a[168:171], v[164:167], v[60:63], 0\n" \
"s_add_u32 m0, %[mL], 0x0\n" \
"s_nop 0\n" \
"s_add_u32 s85, %[oL], 0x4000\n" \
"v_mfma_f32_16x16x32_bf16 a[168:171], v[168:171], v[64:67], a[168:171]\n" \
"buffer_load_dwordx4 v20, s[80:83], s85 offen sc1 lds\n" \
"v_accvgpr_read_b32 v180, a172\n" \
"v_accvgpr_read_b32 v181, a173\n" \
"v_accvgpr_read_b32 v182, a174\n" \
"v_accvgpr_read_b32 v183, a175\n" \
"v_mfma_f32_16x16x32_bf16 a[172:175], v[176:179], v[64:67], 0\n" \
"s_add_u32 m0, %[mL], 0x1000\n" \
"s_add_u32 s84, %[oL], 0x5000\n" \
"v_mfma_f32_16x16x32_bf16 a[172:175], v[172:175], v[60:63], a[172:175]\n" \
"buffer_load_dwordx4 v20, s[80:83], s84 offen sc1 lds\n" \
"v_mfma_f32_16x16x32_bf16 a[192:195], v[148:151], v[68:71], 0\n" \
"v_cvt_pk_bf16_f32 v252, v252, v253\n" \
"v_cvt_pk_bf16_f32 v253, v254, v255\n" \
"v_mfma_f32_16x16x32_bf16 a[192:195], v[152:155], v[72:75], a[192:195]\n" \
"v_cvt_pk_bf16_f32 v254, v180, v181\n" \
"v_cvt_pk_bf16_f32 v255, v182, v183\n" \
"v_mfma_f32_16x16x32_bf16 a[196:199], v[160:163], v[72:75], 0\n" \
"v_accvgpr_write_b32 a100, v252\n" \
"v_accvgpr_write_b32 a101, v253\n" \
"v_mfma_f32_16x16x32_bf16 a[196:199], v[156:159], v[68:71], a[196:199]\n" \
"v_accvgpr_write_b32 a102, v254\n" \
"v_accvgpr_write_b32 a103, v255\n" \
"v_accvgpr_read_b32 v252, a200\n" \
"v_accvgpr_read_b32 v253, a201\n" \
"v_accvgpr_read_b32 v254, a202\n" \
"v_accvgpr_read_b32 v255, a203\n" \
"v_mfma_f32_16x16x32_bf16 a[200:203], v[164:167], v[68:71], 0\n" \
"s_add_u32 m0, %[mL], 0x3000\n" \
"s_mov_b32 s85, 0x100\n" \
"v_mfma_f32_16x16x32_bf16 a[200:203], v[168:171], v[72:75], a[200:203]\n" \
"buffer_load_dwordx4 v21, s[76:79], s85 offen lds\n" \
"v_accvgpr_read_b32 v180, a204\n" \
"v_accvgpr_read_b32 v181, a205\n" \
"v_accvgpr_read_b32 v182, a206\n" \
"v_accvgpr_read_b32 v183, a207\n" \
"v_mfma_f32_16x16x32_bf16 a[204:207], v[176:179], v[72:75], 0\n" \
"s_add_u32 m0, %[mA], 0x0\n" \
"s_nop 0\n" \
"s_mov_b32 s84, 0x100\n" \
"v_mfma_f32_16x16x32_bf16 a[204:207], v[172:175], v[68:71], a[204:207]\n" \
"buffer_load_dwordx4 v24, s[76:79], s84 offen lds\n" \
"v_mfma_f32_16x16x32_bf16 a[224:227], v[148:151], v[76:79], 0\n" \
"v_cvt_pk_bf16_f32 v252, v252, v253\n" \
"v_cvt_pk_bf16_f32 v253, v254, v255\n" \
"v_mfma_f32_16x16x32_bf16 a[224:227], v[152:155], v[80:83], a[224:227]\n" \
"v_cvt_pk_bf16_f32 v254, v180, v181\n" \
"v_cvt_pk_bf16_f32 v255, v182, v183\n" \
"v_mfma_f32_16x16x32_bf16 a[228:231], v[160:163], v[80:83], 0\n" \
"v_accvgpr_write_b32 a8, v252\n" \
"v_accvgpr_write_b32 a9, v253\n" \
"v_mfma_f32_16x16x32_bf16 a[228:231], v[156:159], v[76:79], a[228:231]\n" \
"v_accvgpr_write_b32 a10, v254\n" \
"v_accvgpr_write_b32 a11, v255\n" \
"v_accvgpr_read_b32 v252, a232\n" \
"v_accvgpr_read_b32 v253, a233\n" \
"v_accvgpr_read_b32 v254, a234\n" \
"v_accvgpr_read_b32 v255, a235\n" \
"v_mfma_f32_16x16x32_bf16 a[232:235], v[164:167], v[76:79], 0\n" \
"s_add_u32 m0, %[mA], 0x400\n" \
"L_pf4:\n" \
"s_cbranch_execz L_pf8\n" \
"s_nop 0\n" \
"s_mov_b32 s85, 0x2500\n" \
"v_mfma_f32_16x16x32_bf16 a[232:235], v[168:171], v[80:83], a[232:235]\n" \
"buffer_load_dwordx4 v25, s[76:79], s85 offen lds\n" \
"v_accvgpr_read_b32 v180, a236\n" \
"v_accvgpr_read_b32 v181, a237\n" \
"v_accvgpr_read_b32 v182, a238\n" \
"v_accvgpr_read_b32 v183, a239\n" \
"v_mfma_f32_16x16x32_bf16 a[236:239], v[176:179], v[80:83], 0\n" \
"v_cvt_pk_bf16_f32 v252, v252, v253\n" \
"v_cvt_pk_bf16_f32 v253, v254, v255\n" \
"v_mfma_f32_16x16x32_bf16 a[236:239], v[172:175], v[76:79], a[236:239]\n" \
"v_cvt_pk_bf16_f32 v254, v180, v181\n" \
"v_cvt_pk_bf16_f32 v255, v182, v183\n" \
"v_accvgpr_write_b32 a12, v252\n" \
"v_accvgpr_write_b32 a13, v253\n" \
"v_accvgpr_write_b32 a14, v254\n" \
"v_accvgpr_write_b32 a15, v255\n" \
"v_accvgpr_read_b32 v252, a144\n" \
"v_accvgpr_read_b32 v253, a145\n" \
"v_accvgpr_read_b32 v254, a146\n" \
"v_accvgpr_read_b32 v255, a147\n" \
"v_mfma_f32_16x16x32_bf16 a[144:147], v[148:151], v[84:87], 0\n" \
"v_cvt_pk_bf16_f32 v252, v252, v253\n" \
"s_add_u32 m0, %[mB], 0x0\n" \
"s_nop 0\n" \
"v_mfma_f32_16x16x32_bf16 a[144:147], v[152:155], v[88:91], a[144:147]\n" \
"s_mov_b32 s84, 0x100000\n" \
"v_accvgpr_read_b32 v180, a176\n" \
"v_accvgpr_read_b32 v181, a177\n" \
"v_accvgpr_read_b32 v182, a178\n" \
"v_accvgpr_read_b32 v183, a179\n" \
"v_cvt_pk_bf16_f32 v253, v254, v255\n" \
"buffer_load_dwordx4 v26, s[68:71], s84 offen lds\n" \
"v_cvt_pk_bf16_f32 v254, v180, v181\n" \
"v_cvt_pk_bf16_f32 v255, v182, v183\n" \
"v_accvgpr_write_b32 a40, v252\n" \
"v_accvgpr_write_b32 a41, v253\n" \
"v_accvgpr_write_b32 a42, v254\n" \
"v_accvgpr_write_b32 a43, v255\n" \
"v_accvgpr_read_b32 v252, a148\n" \
"v_accvgpr_read_b32 v253, a149\n" \
"v_accvgpr_read_b32 v254, a150\n" \
"v_accvgpr_read_b32 v255, a151\n" \
"v_mfma_f32_16x16x32_bf16 a[148:151], v[160:163], v[88:91], 0\n" \
"v_cvt_pk_bf16_f32 v252, v252, v253\n" \
"v_accvgpr_read_b32 v180, a180\n" \
"v_mfma_f32_16x16x32_bf16 a[148:151], v[156:159], v[84:87], a[148:151]\n" \
"v_accvgpr_read_b32 v181, a181\n" \
"v_accvgpr_read_b32 v182, a182\n" \
"v_accvgpr_read_b32 v183, a183\n" \
"v_cvt_pk_bf16_f32 v253, v254, v255\n" \
"v_cvt_pk_bf16_f32 v254, v180, v181\n" \
"v_cvt_pk_bf16_f32 v255, v182, v183\n" \
"v_accvgpr_write_b32 a44, v252\n" \
"v_accvgpr_write_b32 a45, v253\n" \
"v_accvgpr_write_b32 a46, v254\n" \
"v_accvgpr_write_b32 a47, v255\n" \
"v_accvgpr_read_b32 v252, a152\n" \
"v_accvgpr_read_b32 v253, a153\n" \
"v_accvgpr_read_b32 v254, a154\n" \
"v_accvgpr_read_b32 v255, a155\n" \
"v_mfma_f32_16x16x32_bf16 a[152:155], v[164:167], v[84:87], 0\n" \
"v_cvt_pk_bf16_f32 v252, v252, v253\n" \
"v_accvgpr_read_b32 v180, a184\n" \
"v_mfma_f32_16x16x32_bf16 a[152:155], v[168:171], v[88:91], a[152:155]\n" \
"v_accvgpr_read_b32 v181, a185\n" \
"v_accvgpr_read_b32 v182, a186\n" \
"v_accvgpr_read_b32 v183, a187\n" \
"v_cvt_pk_bf16_f32 v253, v254, v255\n" \
"v_cvt_pk_bf16_f32 v254, v180, v181\n" \
"v_cvt_pk_bf16_f32 v255, v182, v183\n" \
"v_accvgpr_write_b32 a72, v252\n" \
"v_accvgpr_write_b32 a73, v253\n" \
"v_accvgpr_write_b32 a74, v254\n" \
"v_accvgpr_write_b32 a75, v255\n" \
"v_accvgpr_read_b32 v252, a156\n" \
"v_accvgpr_read_b32 v253, a157\n" \
"v_accvgpr_read_b32 v254, a158\n" \
"v_accvgpr_read_b32 v255, a159\n" \
"v_mfma_f32_16x16x32_bf16 a[156:159], v[176:179], v[88:91], 0\n" \
"s_add_u32 m0, %[mA], 0x800\n" \
"s_mov_b32 s85, 0x4900\n" \
"buffer_load_dwordx4 v24, s[76:79], s85 offen lds\n" \
"v_mfma_f32_16x16x32_bf16 a[156:159], v[172:175], v[84:87], a[156:159]\n" \
"s_add_u32 m0, %[mA], 0xc00\n" \
"s_mov_b32 s84, 0x6d00\n" \
"v_mfma_f32_16x16x32_bf16 a[176:179], v[148:151], v[92:95], 0\n" \
"v_accvgpr_read_b32 v180, a188\n" \
"v_accvgpr_read_b32 v181, a189\n" \
"v_mfma_f32_16x16x32_bf16 a[176:179], v[152:155], v[96:99], a[176:179]\n" \
"buffer_load_dwordx4 v25, s[76:79], s84 offen lds\n" \
"v_mfma_f32_16x16x32_bf16 a[180:183], v[160:163], v[96:99], 0\n" \
"v_accvgpr_read_b32 v182, a190\n" \
"v_accvgpr_read_b32 v183, a191\n" \
"v_mfma_f32_16x16x32_bf16 a[180:183], v[156:159], v[92:95], a[180:183]\n" \
"s_add_u32 m0, %[mB], 0x400\n" \
"s_mov_b32 s85, 0x108000\n" \
"v_mfma_f32_16x16x32_bf16 a[184:187], v[164:167], v[92:95], 0\n" \
"buffer_load_dwordx4 v26, s[68:71], s85 offen lds\n" \
"v_mfma_f32_16x16x32_bf16 a[184:187], v[168:171], v[96:99], a[184:187]\n" \
"v_cvt_pk_bf16_f32 v252, v252, v253\n" \
"v_cvt_pk_bf16_f32 v253, v254, v255\n" \
"v_mfma_f32_16x16x32_bf16 a[188:191], v[176:179], v[96:99], 0\n" \
"v_cvt_pk_bf16_f32 v254, v180, v181\n" \
"v_cvt_pk_bf16_f32 v255, v182, v183\n" \
"v_mfma_f32_16x16x32_bf16 a[188:191], v[172:175], v[92:95], a[188:191]\n" \
"v_accvgpr_write_b32 a76, v252\n" \
"v_accvgpr_write_b32 a77, v253\n" \
"v_accvgpr_write_b32 a78, v254\n" \
"v_accvgpr_write_b32 a79, v255\n" \
"v_accvgpr_read_b32 v252, a208\n" \
"v_accvgpr_read_b32 v253, a209\n" \
"v_accvgpr_read_b32 v254, a210\n" \
"v_accvgpr_read_b32 v255, a211\n" \
"v_mfma_f32_16x16x32_bf16 a[208:211], v[148:151], v[100:103], 0\n" \
"v_cvt_pk_bf16_f32 v252, v252, v253\n" \
"v_accvgpr_read_b32 v180, a240\n" \
"v_mfma_f32_16x16x32_bf16 a[208:211], v[152:155], v[104:107], a[208:211]\n" \
"v_accvgpr_read_b32 v181, a241\n" \
"v_accvgpr_read_b32 v182, a242\n" \
"v_accvgpr_read_b32 v183, a243\n" \
"v_cvt_pk_bf16_f32 v253, v254, v255\n" \
"v_cvt_pk_bf16_f32 v254, v180, v181\n" \
"v_cvt_pk_bf16_f32 v255, v182, v183\n" \
"v_accvgpr_write_b32 a104, v252\n" \
"v_accvgpr_write_b32 a105, v253\n" \
"v_accvgpr_write_b32 a106, v254\n" \
"v_accvgpr_write_b32 a107, v255\n" \
"v_accvgpr_read_b32 v252, a212\n" \
"v_accvgpr_read_b32 v253, a213\n" \
"v_accvgpr_read_b32 v254, a214\n" \
"v_accvgpr_read_b32 v255, a215\n" \
"v_mfma_f32_16x16x32_bf16 a[212:215], v[160:163], v[104:107], 0\n" \
"s_add_u32 m0, %[mA], 0x1000\n" \
"s_mov_b32 s84, 0x9100\n" \
"buffer_load_dwordx4 v24, s[76:79], s84 offen lds\n" \
"ds_read_b64_tr_b16 v[140:141], v39 offset:32768\n" \
"ds_read_b64_tr_b16 v[142:143], v39 offset:33280\n" \
"ds_read_b128 v[84:87], v22 offset:32768\n" \
"v_mfma_f32_16x16x32_bf16 a[212:215], v[156:159], v[100:103], a[212:215]\n" \
"ds_read_b64_tr_b16 v[144:145], v39 offset:36864\n" \
"ds_read_b64_tr_b16 v[146:147], v39 offset:37376\n" \
"ds_read_b128 v[88:91], v23 offset:32768\n" \
"v_accvgpr_read_b32 v184, a216\n" \
"v_accvgpr_read_b32 v185, a217\n" \
"v_accvgpr_read_b32 v186, a218\n" \
"v_accvgpr_read_b32 v187, a219\n" \
"v_mfma_f32_16x16x32_bf16 a[216:219], v[164:167], v[100:103], 0\n" \
"ds_read_b64_tr_b16 v[128:129], v37 offset:36864\n" \
"ds_read_b64_tr_b16 v[130:131], v37 offset:37376\n" \
"v_mfma_f32_16x16x32_bf16 a[216:219], v[168:171], v[104:107], a[216:219]\n" \
"v_cvt_pk_bf16_f32 v252, v252, v253\n" \
"v_accvgpr_read_b32 v180, a244\n" \
"v_accvgpr_read_b32 v181, a245\n" \
"v_accvgpr_read_b32 v182, a246\n" \
"v_accvgpr_read_b32 v183, a247\n" \
"v_cvt_pk_bf16_f32 v253, v254, v255\n" \
"v_cvt_pk_bf16_f32 v254, v180, v181\n" \
"v_cvt_pk_bf16_f32 v255, v182, v183\n" \
"ds_read_b64_tr_b16 v[124:125], v37 offset:32768\n" \
"ds_read_b64_tr_b16 v[126:127], v37 offset:33280\n" \
"v_accvgpr_write_b32 a108, v252\n" \
"v_accvgpr_write_b32 a109, v253\n" \
"v_accvgpr_write_b32 a110, v254\n" \
"v_accvgpr_write_b32 a111, v255\n" \
"v_accvgpr_read_b32 v252, a220\n" \
"v_accvgpr_read_b32 v253, a221\n" \
"v_accvgpr_read_b32 v254, a222\n" \
"v_accvgpr_read_b32 v255, a223\n" \
"v_mfma_f32_16x16x32_bf16 a[220:223], v[176:179], v[104:107], 0\n" \
"s_add_u32 m0, %[mA], 0x1400\n" \
"s_mov_b32 s85, 0xb500\n" \
"buffer_load_dwordx4 v25, s[76:79], s85 offen lds\n" \
"ds_read_b64_tr_b16 v[132:133], v38 offset:32768\n" \
"ds_read_b64_tr_b16 v[134:135], v38 offset:33280\n" \
"v_mfma_f32_16x16x32_bf16 a[220:223], v[172:175], v[100:103], a[220:223]\n" \
"ds_read_b64_tr_b16 v[136:137], v38 offset:36864\n" \
"ds_read_b64_tr_b16 v[138:139], v38 offset:37376\n" \
"v_mfma_f32_16x16x32_bf16 a[240:243], v[148:151], v[108:111], 0\n" \
"ds_read_b128 v[56:59], v29 offset:32768\n" \
"ds_read_b128 v[52:55], v28 offset:32768\n" \
"v_mfma_f32_16x16x32_bf16 a[240:243], v[152:155], v[112:115], a[240:243]\n" \
"s_add_u32 m0, %[mB], 0x800\n" \
"s_mov_b32 s84, 0x110000\n" \
"v_mfma_f32_16x16x32_bf16 a[244:247], v[160:163], v[112:115], 0\n" \
"buffer_load_dwordx4 v27, s[68:71], s84 offen lds\n" \
"v_mfma_f32_16x16x32_bf16 a[244:247], v[156:159], v[108:111], a[244:247]\n" \
"v_accvgpr_read_b32 v180, a248\n" \
"v_accvgpr_read_b32 v181, a249\n" \
"v_accvgpr_read_b32 v182, a250\n" \
"v_accvgpr_read_b32 v183, a251\n" \
"v_mfma_f32_16x16x32_bf16 a[248:251], v[164:167], v[108:111], 0\n" \
"ds_read_b64_tr_b16 v[120:121], v36 offset:36864\n" \
"ds_read_b64_tr_b16 v[122:123], v36 offset:37376\n" \
"v_mfma_f32_16x16x32_bf16 a[248:251], v[168:171], v[112:115], a[248:251]\n" \
"v_cvt_pk_bf16_f32 v184, v184, v185\n" \
"v_cvt_pk_bf16_f32 v185, v186, v187\n" \
"ds_read_b64_tr_b16 v[116:117], v36 offset:32768\n" \
"ds_read_b64_tr_b16 v[118:119], v36 offset:33280\n" \
"v_cvt_pk_bf16_f32 v186, v180, v181\n" \
"v_cvt_pk_bf16_f32 v187, v182, v183\n" \
"v_accvgpr_read_b32 v180, a252\n" \
"v_accvgpr_read_b32 v181, a253\n" \
"v_accvgpr_read_b32 v182, a254\n" \
"v_accvgpr_read_b32 v183, a255\n" \
"v_mfma_f32_16x16x32_bf16 a[252:255], v[176:179], v[112:115], 0\n" \
"ds_read_b128 v[64:67], v29 offset:34816\n" \
"ds_read_b128 v[60:63], v28 offset:34816\n" \
"v_mfma_f32_16x16x32_bf16 a[252:255], v[172:175], v[108:111], a[252:255]\n" \
"s_add_u32 m0, %[mA], 0x1800\n" \
"s_waitcnt lgkmcnt(12)\n" \
"v_mfma_f32_16x16x32_bf16 a[16:19], v[140:143], v[84:87], a[16:19]\n" \
"s_mov_b32 s84, 0xd900\n" \
"ds_read_b128 v[72:75], v29 offset:36864\n" \
"v_mfma_f32_16x16x32_bf16 a[16:19], v[144:147], v[88:91], a[16:19]\n" \
"buffer_load_dwordx4 v24, s[76:79], s84 offen lds\n" \
"v_mfma_f32_16x16x32_bf16 a[48:51], v[128:131], v[88:91], a[48:51]\n" \
"s_add_u32 m0, %[mA], 0x1c00\n" \
"s_mov_b32 s85, 0xfd00\n" \
"v_mfma_f32_16x16x32_bf16 a[48:51], v[124:127], v[84:87], a[48:51]\n" \
"ds_read_b128 v[68:71], v28 offset:36864\n" \
"s_waitcnt lgkmcnt(10)\n" \
"v_mfma_f32_16x16x32_bf16 a[80:83], v[84:87], v[132:135], a[80:83]\n" \
"buffer_load_dwordx4 v25, s[76:79], s85 offen lds\n" \
"v_mfma_f32_16x16x32_bf16 a[80:83], v[88:91], v[136:139], a[80:83]\n" \
"s_add_u32 m0, %[mB], 0xc00\n" \
"s_mov_b32 s84, 0x118000\n" \
"s_waitcnt lgkmcnt(8)\n" \
"s_nop 0\n" \
"v_mfma_f32_16x16x32_bf16 a[112:115], v[144:147], v[56:59], a[112:115]\n" \
"ds_read_b128 v[80:83], v29 offset:38912\n" \
"ds_read_b128 v[76:79], v28 offset:38912\n" \
"buffer_load_dwordx4 v27, s[68:71], s84 offen lds\n" \
"v_mfma_f32_16x16x32_bf16 a[112:115], v[140:143], v[52:55], a[112:115]\n" \
"ds_read_b64_tr_b16 v[152:153], v32 offset:40960\n" \
"ds_read_b64_tr_b16 v[154:155], v32 offset:41984\n" \
"v_mfma_f32_16x16x32_bf16 a[20:23], v[124:127], v[52:55], a[20:23]\n" \
"ds_read_b64_tr_b16 v[148:149], v32 offset:32768\n" \
"ds_read_b64_tr_b16 v[150:151], v32 offset:33792\n" \
"v_mfma_f32_16x16x32_bf16 a[20:23], v[128:131], v[56:59], a[20:23]\n" \
"s_add_u32 s88, %[oVA], 0x0\n" \
"s_waitcnt lgkmcnt(10)\n" \
"v_mfma_f32_16x16x32_bf16 a[52:55], v[120:123], v[56:59], a[52:55]\n" \
"buffer_store_dwordx4 v[200:203], v20, s[80:83], s88 offen offset:2048 sc1\n" \
"v_mfma_f32_16x16x32_bf16 a[52:55], v[116:119], v[52:55], a[52:55]\n" \
"ds_read_b64_tr_b16 v[156:157], v33 offset:32768\n" \
"ds_read_b64_tr_b16 v[158:159], v33 offset:33792\n" \
"v_mfma_f32_16x16x32_bf16 a[84:87], v[52:55], v[132:135], a[84:87]\n" \
"ds_read_b64_tr_b16 v[160:161], v33 offset:40960\n" \
"ds_read_b64_tr_b16 v[162:163], v33 offset:41984\n" \
"v_mfma_f32_16x16x32_bf16 a[84:87], v[56:59], v[136:139], a[84:87]\n" \
"ds_read_b64_tr_b16 v[168:169], v34 offset:40960\n" \
"s_waitcnt lgkmcnt(13)\n" \
"s_nop 0\n" \
"v_mfma_f32_16x16x32_bf16 a[116:119], v[144:147], v[64:67], a[116:119]\n" \
"ds_read_b64_tr_b16 v[170:171], v34 offset:41984\n" \
"ds_read_b64_tr_b16 v[164:165], v34 offset:32768\n" \
"v_mfma_f32_16x16x32_bf16 a[116:119], v[140:143], v[60:63], a[116:119]\n" \
"ds_read_b64_tr_b16 v[166:167], v34 offset:33792\n" \
"ds_read_b64_tr_b16 v[172:173], v35 offset:32768\n" \
"v_mfma_f32_16x16x32_bf16 a[24:27], v[124:127], v[60:63], a[24:27]\n" \
"ds_read_b64_tr_b16 v[174:175], v35 offset:33792\n" \
"ds_read_b64_tr_b16 v[176:177], v35 offset:40960\n" \
"v_mfma_f32_16x16x32_bf16 a[24:27], v[128:131], v[64:67], a[24:27]\n" \
"ds_read_b64_tr_b16 v[178:179], v35 offset:41984\n" \
"ds_read_b128 v[84:87], v48 offset:32768\n" \
"v_mfma_f32_16x16x32_bf16 a[56:59], v[120:123], v[64:67], a[56:59]\n" \
"ds_read_b128 v[88:91], v49 offset:32768\n" \
"ds_read_b128 v[92:95], v48 offset:34816\n" \
"v_mfma_f32_16x16x32_bf16 a[56:59], v[116:119], v[60:63], a[56:59]\n" \
"s_add_u32 s89, %[oVA], 0x2000\n" \
"ds_read_b128 v[96:99], v49 offset:34816\n" \
"v_mfma_f32_16x16x32_bf16 a[88:91], v[60:63], v[132:135], a[88:91]\n" \
"buffer_store_dwordx4 v[204:207], v20, s[80:83], s89 offen offset:2048 sc1\n" \
"v_mfma_f32_16x16x32_bf16 a[88:91], v[64:67], v[136:139], a[88:91]\n" \
"ds_read_b128 v[100:103], v48 offset:36864\n" \
"s_waitcnt lgkmcnt(15)\n" \
"s_nop 0\n" \
"v_mfma_f32_16x16x32_bf16 a[120:123], v[144:147], v[72:75], a[120:123]\n" \
"ds_read_b128 v[104:107], v49 offset:36864\n" \
"ds_read_b128 v[108:111], v48 offset:38912\n" \
"v_mfma_f32_16x16x32_bf16 a[120:123], v[140:143], v[68:71], a[120:123]\n" \
"ds_read_b128 v[112:115], v49 offset:38912\n" \
"v_cvt_pk_bf16_f32 v252, v252, v253\n" \
"v_mfma_f32_16x16x32_bf16 a[28:31], v[124:127], v[68:71], a[28:31]\n" \
"v_cvt_pk_bf16_f32 v253, v254, v255\n" \
"v_cvt_pk_bf16_f32 v254, v180, v181\n" \
"v_mfma_f32_16x16x32_bf16 a[28:31], v[128:131], v[72:75], a[28:31]\n" \
"v_cvt_pk_bf16_f32 v255, v182, v183\n" \
"v_mfma_f32_16x16x32_bf16 a[60:63], v[120:123], v[72:75], a[60:63]\n" \
"v_mfma_f32_16x16x32_bf16 a[60:63], v[116:119], v[68:71], a[60:63]\n" \
"v_mfma_f32_16x16x32_bf16 a[92:95], v[68:71], v[132:135], a[92:95]\n" \
"v_mfma_f32_16x16x32_bf16 a[92:95], v[72:75], v[136:139], a[92:95]\n" \
"s_add_u32 s88, %[oVA], 0x0\n" \
"s_nop 0\n" \
"v_mfma_f32_16x16x32_bf16 a[124:127], v[144:147], v[80:83], a[124:127]\n" \
"buffer_store_dwordx4 v[208:211], v20, s[80:83], s88 offen offset:3072 sc1\n" \
"v_mfma_f32_16x16x32_bf16 a[124:127], v[140:143], v[76:79], a[124:127]\n" \
"v_mfma_f32_16x16x32_bf16 a[0:3], v[124:127], v[76:79], a[0:3]\n" \
"v_mfma_f32_16x16x32_bf16 a[0:3], v[128:131], v[80:83], a[0:3]\n" \
"v_mfma_f32_16x16x32_bf16 a[4:7], v[120:123], v[80:83], a[4:7]\n" \
"v_mfma_f32_16x16x32_bf16 a[4:7], v[116:119], v[76:79], a[4:7]\n" \
"v_mfma_f32_16x16x32_bf16 a[32:35], v[76:79], v[132:135], a[32:35]\n" \
"v_mfma_f32_16x16x32_bf16 a[32:35], v[80:83], v[136:139], a[32:35]\n" \
"v_mfma_f32_16x16x32_bf16 a[128:131], v[152:155], v[56:59], a[128:131]\n" \
"v_mfma_f32_16x16x32_bf16 a[128:131], v[148:151], v[52:55], a[128:131]\n" \
"v_mfma_f32_16x16x32_bf16 a[132:135], v[156:159], v[52:55], a[132:135]\n" \
"s_waitcnt lgkmcnt(0)\n" \
"v_mfma_f32_16x16x32_bf16 a[132:135], v[160:163], v[56:59], a[132:135]\n" \
"v_mfma_f32_16x16x32_bf16 a[136:139], v[168:171], v[56:59], a[136:139]\n" \
"v_mfma_f32_16x16x32_bf16 a[136:139], v[164:167], v[52:55], a[136:139]\n" \
"v_mfma_f32_16x16x32_bf16 a[140:143], v[172:175], v[52:55], a[140:143]\n" \
"v_mfma_f32_16x16x32_bf16 a[140:143], v[176:179], v[56:59], a[140:143]\n" \
"v_mfma_f32_16x16x32_bf16 a[160:163], v[152:155], v[64:67], a[160:163]\n" \
"v_mfma_f32_16x16x32_bf16 a[160:163], v[148:151], v[60:63], a[160:163]\n" \
"s_waitcnt vmcnt(3)\n" \
"s_barrier\n" \
"s_nop 0\n" \
"v_mfma_f32_16x16x32_bf16 a[164:167], v[156:159], v[60:63], a[164:167]\n" \
"s_add_u32 m0, %[mL], 0x8000\n" \
"s_add_u32 s85, %[oL], 0x6000\n" \
"v_mfma_f32_16x16x32_bf16 a[164:167], v[160:163], v[64:67], a[164:167]\n" \
"buffer_load_dwordx4 v20, s[80:83], s85 offen sc1 lds\n" \
"v_mfma_f32_16x16x32_bf16 a[168:171], v[168:171], v[64:67], a[168:171]\n" \
"ds_read_b64_tr_b16 v[144:145], v39 offset:4096\n" \
"ds_read_b64_tr_b16 v[146:147], v39 offset:4608\n" \
"v_mfma_f32_16x16x32_bf16 a[168:171], v[164:167], v[60:63], a[168:171]\n" \
"s_add_u32 m0, %[mL], 0x9000\n" \
"s_add_u32 s84, %[oL], 0x7000\n" \
"v_mfma_f32_16x16x32_bf16 a[172:175], v[172:175], v[60:63], a[172:175]\n" \
"buffer_load_dwordx4 v20, s[80:83], s84 offen sc1 lds\n" \
"v_mfma_f32_16x16x32_bf16 a[172:175], v[176:179], v[64:67], a[172:175]\n" \
"ds_read_b64_tr_b16 v[140:141], v39 offset:0\n" \
"ds_read_b64_tr_b16 v[142:143], v39 offset:512\n" \
"v_mfma_f32_16x16x32_bf16 a[192:195], v[152:155], v[72:75], a[192:195]\n" \
"ds_read_b64_tr_b16 v[124:125], v37 offset:0\n" \
"ds_read_b64_tr_b16 v[126:127], v37 offset:512\n" \
"v_mfma_f32_16x16x32_bf16 a[192:195], v[148:151], v[68:71], a[192:195]\n" \
"s_add_u32 m0, %[mL], 0xb000\n" \
"s_mov_b32 s85, 0x180\n" \
"v_mfma_f32_16x16x32_bf16 a[196:199], v[156:159], v[68:71], a[196:199]\n" \
"buffer_load_dwordx4 v21, s[76:79], s85 offen lds\n" \
"v_mfma_f32_16x16x32_bf16 a[196:199], v[160:163], v[72:75], a[196:199]\n" \
"ds_read_b64_tr_b16 v[128:129], v37 offset:4096\n" \
"ds_read_b64_tr_b16 v[130:131], v37 offset:4608\n" \
"v_mfma_f32_16x16x32_bf16 a[200:203], v[168:171], v[72:75], a[200:203]\n" \
"ds_read_b64_tr_b16 v[136:137], v38 offset:4096\n" \
"ds_read_b64_tr_b16 v[138:139], v38 offset:4608\n" \
"v_mfma_f32_16x16x32_bf16 a[200:203], v[164:167], v[68:71], a[200:203]\n" \
"s_add_u32 m0, %[mA], 0x8000\n" \
"s_mov_b32 s84, 0x180\n" \
"v_mfma_f32_16x16x32_bf16 a[204:207], v[172:175], v[68:71], a[204:207]\n" \
"buffer_load_dwordx4 v24, s[76:79], s84 offen lds\n" \
"v_mfma_f32_16x16x32_bf16 a[204:207], v[176:179], v[72:75], a[204:207]\n" \
"ds_read_b64_tr_b16 v[132:133], v38 offset:0\n" \
"ds_read_b64_tr_b16 v[134:135], v38 offset:512\n" \
"v_mfma_f32_16x16x32_bf16 a[224:227], v[152:155], v[80:83], a[224:227]\n" \
"ds_read_b128 v[52:55], v28 offset:0\n" \
"ds_read_b128 v[56:59], v29 offset:0\n" \
"v_mfma_f32_16x16x32_bf16 a[224:227], v[148:151], v[76:79], a[224:227]\n" \
"s_add_u32 m0, %[mA], 0x8400\n" \
"s_mov_b32 s85, 0x2580\n" \
"v_mfma_f32_16x16x32_bf16 a[228:231], v[156:159], v[76:79], a[228:231]\n" \
"buffer_load_dwordx4 v25, s[76:79], s85 offen lds\n" \
"v_mfma_f32_16x16x32_bf16 a[228:231], v[160:163], v[80:83], a[228:231]\n" \
"ds_read_b64_tr_b16 v[116:117], v36 offset:0\n" \
"ds_read_b64_tr_b16 v[118:119], v36 offset:512\n" \
"v_mfma_f32_16x16x32_bf16 a[232:235], v[168:171], v[80:83], a[232:235]\n" \
"ds_read_b64_tr_b16 v[120:121], v36 offset:4096\n" \
"ds_read_b64_tr_b16 v[122:123], v36 offset:4608\n" \
"v_mfma_f32_16x16x32_bf16 a[232:235], v[164:167], v[76:79], a[232:235]\n" \
"s_add_u32 m0, %[mB], 0x8000\n" \
"s_mov_b32 s84, 0x180000\n" \
"v_mfma_f32_16x16x32_bf16 a[236:239], v[172:175], v[76:79], a[236:239]\n" \
"buffer_load_dwordx4 v26, s[68:71], s84 offen lds\n" \
"v_mfma_f32_16x16x32_bf16 a[236:239], v[176:179], v[80:83], a[236:239]\n" \
"ds_read_b128 v[60:63], v28 offset:2048\n" \
"ds_read_b128 v[64:67], v29 offset:2048\n" \
"v_mfma_f32_16x16x32_bf16 a[144:147], v[152:155], v[88:91], a[144:147]\n" \
"ds_read_b128 v[68:71], v28 offset:4096\n" \
"ds_read_b128 v[72:75], v29 offset:4096\n" \
"v_mfma_f32_16x16x32_bf16 a[144:147], v[148:151], v[84:87], a[144:147]\n" \
"ds_read_b128 v[76:79], v28 offset:6144\n" \
"ds_read_b128 v[80:83], v29 offset:6144\n" \
"v_mfma_f32_16x16x32_bf16 a[148:151], v[156:159], v[84:87], a[148:151]\n" \
"s_add_u32 m0, %[mA], 0x8800\n" \
"s_mov_b32 s85, 0x4980\n" \
"v_mfma_f32_16x16x32_bf16 a[148:151], v[160:163], v[88:91], a[148:151]\n" \
"buffer_load_dwordx4 v24, s[76:79], s85 offen lds\n" \
"v_mfma_f32_16x16x32_bf16 a[152:155], v[168:171], v[88:91], a[152:155]\n" \
"v_mfma_f32_16x16x32_bf16 a[152:155], v[164:167], v[84:87], a[152:155]\n" \
"v_mfma_f32_16x16x32_bf16 a[156:159], v[172:175], v[84:87], a[156:159]\n" \
"s_add_u32 m0, %[mA], 0x8c00\n" \
"s_mov_b32 s84, 0x6d80\n" \
"v_mfma_f32_16x16x32_bf16 a[156:159], v[176:179], v[88:91], a[156:159]\n" \
"buffer_load_dwordx4 v25, s[76:79], s84 offen lds\n" \
"v_mfma_f32_16x16x32_bf16 a[176:179], v[152:155], v[96:99], a[176:179]\n" \
"ds_read_b128 v[88:91], v23 offset:0\n" \
"ds_read_b128 v[84:87], v22 offset:0\n" \
"v_mfma_f32_16x16x32_bf16 a[176:179], v[148:151], v[92:95], a[176:179]\n" \
"v_mfma_f32_16x16x32_bf16 a[180:183], v[156:159], v[92:95], a[180:183]\n" \
"s_add_u32 m0, %[mB], 0x8400\n" \
"s_mov_b32 s85, 0x188000\n" \
"v_mfma_f32_16x16x32_bf16 a[180:183], v[160:163], v[96:99], a[180:183]\n" \
"buffer_load_dwordx4 v26, s[68:71], s85 offen lds\n" \
"v_mfma_f32_16x16x32_bf16 a[184:187], v[168:171], v[96:99], a[184:187]\n" \
"v_mfma_f32_16x16x32_bf16 a[184:187], v[164:167], v[92:95], a[184:187]\n" \
"v_mfma_f32_16x16x32_bf16 a[188:191], v[172:175], v[92:95], a[188:191]\n" \
"s_add_u32 m0, %[mA], 0x9000\n" \
"s_mov_b32 s84, 0x9180\n" \
"v_mfma_f32_16x16x32_bf16 a[188:191], v[176:179], v[96:99], a[188:191]\n" \
"buffer_load_dwordx4 v24, s[76:79], s84 offen lds\n" \
"v_mfma_f32_16x16x32_bf16 a[208:211], v[152:155], v[104:107], a[208:211]\n" \
"ds_read_b128 v[92:95], v48 offset:2048\n" \
"ds_read_b128 v[96:99], v49 offset:2048\n" \
"v_mfma_f32_16x16x32_bf16 a[208:211], v[148:151], v[100:103], a[208:211]\n" \
"v_mfma_f32_16x16x32_bf16 a[212:215], v[156:159], v[100:103], a[212:215]\n" \
"s_add_u32 m0, %[mA], 0x9400\n" \
"s_mov_b32 s85, 0xb580\n" \
"v_mfma_f32_16x16x32_bf16 a[212:215], v[160:163], v[104:107], a[212:215]\n" \
"buffer_load_dwordx4 v25, s[76:79], s85 offen lds\n" \
"v_mfma_f32_16x16x32_bf16 a[216:219], v[168:171], v[104:107], a[216:219]\n" \
"v_mfma_f32_16x16x32_bf16 a[216:219], v[164:167], v[100:103], a[216:219]\n" \
"v_mfma_f32_16x16x32_bf16 a[220:223], v[172:175], v[100:103], a[220:223]\n" \
"s_add_u32 m0, %[mB], 0x8800\n" \
"s_mov_b32 s84, 0x190000\n" \
"v_mfma_f32_16x16x32_bf16 a[220:223], v[176:179], v[104:107], a[220:223]\n" \
"buffer_load_dwordx4 v27, s[68:71], s84 offen lds\n" \
"v_mfma_f32_16x16x32_bf16 a[240:243], v[152:155], v[112:115], a[240:243]\n" \
"ds_read_b64_tr_b16 v[152:153], v32 offset:8192\n" \
"ds_read_b64_tr_b16 v[154:155], v32 offset:9216\n" \
"v_mfma_f32_16x16x32_bf16 a[240:243], v[148:151], v[108:111], a[240:243]\n" \
"ds_read_b64_tr_b16 v[148:149], v32 offset:0\n" \
"ds_read_b64_tr_b16 v[150:151], v32 offset:1024\n" \
"v_mfma_f32_16x16x32_bf16 a[244:247], v[156:159], v[108:111], a[244:247]\n" \
"ds_read_b64_tr_b16 v[156:157], v33 offset:0\n" \
"ds_read_b64_tr_b16 v[158:159], v33 offset:1024\n" \
"v_mfma_f32_16x16x32_bf16 a[244:247], v[160:163], v[112:115], a[244:247]\n" \
"ds_read_b64_tr_b16 v[160:161], v33 offset:8192\n" \
"ds_read_b64_tr_b16 v[162:163], v33 offset:9216\n" \
"v_mfma_f32_16x16x32_bf16 a[248:251], v[168:171], v[112:115], a[248:251]\n" \
"ds_read_b64_tr_b16 v[168:169], v34 offset:8192\n" \
"ds_read_b64_tr_b16 v[170:171], v34 offset:9216\n" \
"v_mfma_f32_16x16x32_bf16 a[248:251], v[164:167], v[108:111], a[248:251]\n" \
"ds_read_b64_tr_b16 v[164:165], v34 offset:0\n" \
"ds_read_b64_tr_b16 v[166:167], v34 offset:1024\n" \
"v_mfma_f32_16x16x32_bf16 a[252:255], v[172:175], v[108:111], a[252:255]\n" \
"ds_read_b64_tr_b16 v[172:173], v35 offset:0\n" \
"ds_read_b64_tr_b16 v[174:175], v35 offset:1024\n" \
"v_mfma_f32_16x16x32_bf16 a[252:255], v[176:179], v[112:115], a[252:255]\n" \
"s_add_u32 m0, %[mA], 0x9800\n" \
"s_waitcnt lgkmcnt(14)\n" \
"s_nop 0\n" \
"v_mfma_f32_16x16x32_bf16 a[16:19], v[144:147], v[88:91], a[16:19]\n" \
"s_mov_b32 s84, 0xd980\n" \
"ds_read_b64_tr_b16 v[176:177], v35 offset:8192\n" \
"v_mfma_f32_16x16x32_bf16 a[16:19], v[140:143], v[84:87], a[16:19]\n" \
"buffer_load_dwordx4 v24, s[76:79], s84 offen lds\n" \
"v_mfma_f32_16x16x32_bf16 a[48:51], v[124:127], v[84:87], a[48:51]\n" \
"s_add_u32 m0, %[mA], 0x9c00\n" \
"s_mov_b32 s85, 0xfd80\n" \
"v_mfma_f32_16x16x32_bf16 a[48:51], v[128:131], v[88:91], a[48:51]\n" \
"ds_read_b64_tr_b16 v[178:179], v35 offset:9216\n" \
"ds_read_b128 v[100:103], v48 offset:4096\n" \
"v_mfma_f32_16x16x32_bf16 a[80:83], v[88:91], v[136:139], a[80:83]\n" \
"buffer_load_dwordx4 v25, s[76:79], s85 offen lds\n" \
"v_mfma_f32_16x16x32_bf16 a[80:83], v[84:87], v[132:135], a[80:83]\n" \
"s_add_u32 m0, %[mB], 0x8c00\n" \
"s_mov_b32 s84, 0x198000\n" \
"v_mfma_f32_16x16x32_bf16 a[112:115], v[140:143], v[52:55], a[112:115]\n" \
"ds_read_b128 v[84:87], v48 offset:0\n" \
"ds_read_b128 v[88:91], v49 offset:0\n" \
"buffer_load_dwordx4 v27, s[68:71], s84 offen lds\n" \
"v_mfma_f32_16x16x32_bf16 a[112:115], v[144:147], v[56:59], a[112:115]\n" \
"ds_read_b128 v[104:107], v49 offset:4096\n" \
"ds_read_b128 v[108:111], v48 offset:6144\n" \
"v_mfma_f32_16x16x32_bf16 a[20:23], v[128:131], v[56:59], a[20:23]\n" \
"ds_read_b128 v[112:115], v49 offset:6144\n" \
"v_mfma_f32_16x16x32_bf16 a[20:23], v[124:127], v[52:55], a[20:23]\n" \
"s_add_u32 s89, %[oVA], 0x2000\n" \
"v_mfma_f32_16x16x32_bf16 a[52:55], v[116:119], v[52:55], a[52:55]\n" \
"buffer_store_dwordx4 v[212:215], v20, s[80:83], s89 offen offset:3072 sc1\n" \
"v_mfma_f32_16x16x32_bf16 a[52:55], v[120:123], v[56:59], a[52:55]\n" \
"v_mfma_f32_16x16x32_bf16 a[84:87], v[56:59], v[136:139], a[84:87]\n" \
"v_mfma_f32_16x16x32_bf16 a[84:87], v[52:55], v[132:135], a[84:87]\n" \
"v_mfma_f32_16x16x32_bf16 a[116:119], v[140:143], v[60:63], a[116:119]\n" \
"v_mfma_f32_16x16x32_bf16 a[116:119], v[144:147], v[64:67], a[116:119]\n" \
"v_mfma_f32_16x16x32_bf16 a[24:27], v[128:131], v[64:67], a[24:27]\n" \
"v_mfma_f32_16x16x32_bf16 a[24:27], v[124:127], v[60:63], a[24:27]\n" \
"v_mfma_f32_16x16x32_bf16 a[56:59], v[116:119], v[60:63], a[56:59]\n" \
"v_mfma_f32_16x16x32_bf16 a[56:59], v[120:123], v[64:67], a[56:59]\n" \
"s_add_u32 s88, %[oKA], 0x0\n" \
"s_nop 0\n" \
"v_mfma_f32_16x16x32_bf16 a[88:91], v[64:67], v[136:139], a[88:91]\n" \
"buffer_store_dwordx4 v[216:219], v20, s[80:83], s88 offen offset:0 sc1\n" \
"v_mfma_f32_16x16x32_bf16 a[88:91], v[60:63], v[132:135], a[88:91]\n" \
"v_mfma_f32_16x16x32_bf16 a[120:123], v[140:143], v[68:71], a[120:123]\n" \
"v_mfma_f32_16x16x32_bf16 a[120:123], v[144:147], v[72:75], a[120:123]\n" \
"v_mfma_f32_16x16x32_bf16 a[28:31], v[128:131], v[72:75], a[28:31]\n" \
"v_mfma_f32_16x16x32_bf16 a[28:31], v[124:127], v[68:71], a[28:31]\n" \
"v_mfma_f32_16x16x32_bf16 a[60:63], v[116:119], v[68:71], a[60:63]\n" \
"v_mfma_f32_16x16x32_bf16 a[60:63], v[120:123], v[72:75], a[60:63]\n" \
"v_mfma_f32_16x16x32_bf16 a[92:95], v[72:75], v[136:139], a[92:95]\n" \
"v_mfma_f32_16x16x32_bf16 a[92:95], v[68:71], v[132:135], a[92:95]\n" \
"s_add_u32 s89, %[oKA], 0x1000\n" \
"v_mfma_f32_16x16x32_bf16 a[124:127], v[140:143], v[76:79], a[124:127]\n" \
"buffer_store_dwordx4 v[220:223], v20, s[80:83], s89 offen offset:0 sc1\n" \
"v_mfma_f32_16x16x32_bf16 a[124:127], v[144:147], v[80:83], a[124:127]\n" \
"v_mfma_f32_16x16x32_bf16 a[0:3], v[128:131], v[80:83], a[0:3]\n" \
"v_mfma_f32_16x16x32_bf16 a[0:3], v[124:127], v[76:79], a[0:3]\n" \
"v_mfma_f32_16x16x32_bf16 a[4:7], v[116:119], v[76:79], a[4:7]\n" \
"v_mfma_f32_16x16x32_bf16 a[4:7], v[120:123], v[80:83], a[4:7]\n" \
"v_mfma_f32_16x16x32_bf16 a[32:35], v[80:83], v[136:139], a[32:35]\n" \
"v_mfma_f32_16x16x32_bf16 a[32:35], v[76:79], v[132:135], a[32:35]\n" \
"s_waitcnt lgkmcnt(0)\n" \
"v_mfma_f32_16x16x32_bf16 a[128:131], v[148:151], v[52:55], a[128:131]\n" \
"v_mfma_f32_16x16x32_bf16 a[128:131], v[152:155], v[56:59], a[128:131]\n" \
"v_mfma_f32_16x16x32_bf16 a[132:135], v[160:163], v[56:59], a[132:135]\n" \
"v_mfma_f32_16x16x32_bf16 a[132:135], v[156:159], v[52:55], a[132:135]\n" \
"v_mfma_f32_16x16x32_bf16 a[136:139], v[164:167], v[52:55], a[136:139]\n" \
"v_mfma_f32_16x16x32_bf16 a[136:139], v[168:171], v[56:59], a[136:139]\n" \
"v_mfma_f32_16x16x32_bf16 a[140:143], v[176:179], v[56:59], a[140:143]\n" \
"v_mfma_f32_16x16x32_bf16 a[140:143], v[172:175], v[52:55], a[140:143]\n" \
"L_pf5:\n" \
"s_cbranch_execz L_pf9\n" \
"s_nop 0\n" \
"v_mfma_f32_16x16x32_bf16 a[160:163], v[148:151], v[60:63], a[160:163]\n" \
"v_mfma_f32_16x16x32_bf16 a[160:163], v[152:155], v[64:67], a[160:163]\n" \
"s_waitcnt vmcnt(3)\n" \
"s_barrier\n" \
"v_mfma_f32_16x16x32_bf16 a[164:167], v[160:163], v[64:67], a[164:167]\n" \
"s_add_u32 m0, %[mL], 0x0\n" \
"s_add_u32 s85, %[oL], 0x8000\n" \
"v_mfma_f32_16x16x32_bf16 a[164:167], v[156:159], v[60:63], a[164:167]\n" \
"buffer_load_dwordx4 v20, s[80:83], s85 offen sc1 lds\n" \
"v_mfma_f32_16x16x32_bf16 a[168:171], v[164:167], v[60:63], a[168:171]\n" \
"ds_read_b64_tr_b16 v[140:141], v39 offset:32768\n" \
"ds_read_b64_tr_b16 v[142:143], v39 offset:33280\n" \
"v_mfma_f32_16x16x32_bf16 a[168:171], v[168:171], v[64:67], a[168:171]\n" \
"s_add_u32 m0, %[mL], 0x1000\n" \
"s_add_u32 s84, %[oL], 0x9000\n" \
"v_mfma_f32_16x16x32_bf16 a[172:175], v[176:179], v[64:67], a[172:175]\n" \
"buffer_load_dwordx4 v20, s[80:83], s84 offen sc1 lds\n" \
"v_mfma_f32_16x16x32_bf16 a[172:175], v[172:175], v[60:63], a[172:175]\n" \
"ds_read_b64_tr_b16 v[144:145], v39 offset:36864\n" \
"ds_read_b64_tr_b16 v[146:147], v39 offset:37376\n" \
"v_mfma_f32_16x16x32_bf16 a[192:195], v[148:151], v[68:71], a[192:195]\n" \
"ds_read_b64_tr_b16 v[128:129], v37 offset:36864\n" \
"ds_read_b64_tr_b16 v[130:131], v37 offset:37376\n" \
"v_mfma_f32_16x16x32_bf16 a[192:195], v[152:155], v[72:75], a[192:195]\n" \
"s_add_u32 m0, %[mL], 0x3000\n" \
"s_mov_b32 s85, 0x200\n" \
"v_mfma_f32_16x16x32_bf16 a[196:199], v[160:163], v[72:75], a[196:199]\n" \
"buffer_load_dwordx4 v21, s[76:79], s85 offen lds\n" \
"v_mfma_f32_16x16x32_bf16 a[196:199], v[156:159], v[68:71], a[196:199]\n" \
"ds_read_b64_tr_b16 v[124:125], v37 offset:32768\n" \
"ds_read_b64_tr_b16 v[126:127], v37 offset:33280\n" \
"v_mfma_f32_16x16x32_bf16 a[200:203], v[164:167], v[68:71], a[200:203]\n" \
"ds_read_b64_tr_b16 v[132:133], v38 offset:32768\n" \
"ds_read_b64_tr_b16 v[134:135], v38 offset:33280\n" \
"v_mfma_f32_16x16x32_bf16 a[200:203], v[168:171], v[72:75], a[200:203]\n" \
"s_add_u32 m0, %[mA], 0x0\n" \
"s_nop 0\n" \
"s_mov_b32 s84, 0x200\n" \
"v_mfma_f32_16x16x32_bf16 a[204:207], v[176:179], v[72:75], a[204:207]\n" \
"buffer_load_dwordx4 v24, s[76:79], s84 offen lds\n" \
"v_mfma_f32_16x16x32_bf16 a[204:207], v[172:175], v[68:71], a[204:207]\n" \
"ds_read_b64_tr_b16 v[136:137], v38 offset:36864\n" \
"ds_read_b64_tr_b16 v[138:139], v38 offset:37376\n" \
"v_mfma_f32_16x16x32_bf16 a[224:227], v[148:151], v[76:79], a[224:227]\n" \
"ds_read_b128 v[56:59], v29 offset:32768\n" \
"ds_read_b128 v[52:55], v28 offset:32768\n" \
"v_mfma_f32_16x16x32_bf16 a[224:227], v[152:155], v[80:83], a[224:227]\n" \
"s_add_u32 m0, %[mA], 0x400\n" \
"s_mov_b32 s85, 0x2600\n" \
"v_mfma_f32_16x16x32_bf16 a[228:231], v[160:163], v[80:83], a[228:231]\n" \
"buffer_load_dwordx4 v25, s[76:79], s85 offen lds\n" \
"v_mfma_f32_16x16x32_bf16 a[228:231], v[156:159], v[76:79], a[228:231]\n" \
"ds_read_b64_tr_b16 v[120:121], v36 offset:36864\n" \
"ds_read_b64_tr_b16 v[122:123], v36 offset:37376\n" \
"v_mfma_f32_16x16x32_bf16 a[232:235], v[164:167], v[76:79], a[232:235]\n" \
"ds_read_b64_tr_b16 v[116:117], v36 offset:32768\n" \
"ds_read_b64_tr_b16 v[118:119], v36 offset:33280\n" \
"v_mfma_f32_16x16x32_bf16 a[232:235], v[168:171], v[80:83], a[232:235]\n" \
"s_add_u32 m0, %[mB], 0x0\n" \
"s_nop 0\n" \
"s_mov_b32 s84, 0x200000\n" \
"v_mfma_f32_16x16x32_bf16 a[236:239], v[176:179], v[80:83], a[236:239]\n" \
"buffer_load_dwordx4 v26, s[68:71], s84 offen lds\n" \
"v_mfma_f32_16x16x32_bf16 a[236:239], v[172:175], v[76:79], a[236:239]\n" \
"ds_read_b128 v[64:67], v29 offset:34816\n" \
"ds_read_b128 v[60:63], v28 offset:34816\n" \
"v_mfma_f32_16x16x32_bf16 a[144:147], v[148:151], v[84:87], a[144:147]\n" \
"ds_read_b128 v[72:75], v29 offset:36864\n" \
"ds_read_b128 v[68:71], v28 offset:36864\n" \
"v_mfma_f32_16x16x32_bf16 a[144:147], v[152:155], v[88:91], a[144:147]\n" \
"ds_read_b128 v[80:83], v29 offset:38912\n" \
"ds_read_b128 v[76:79], v28 offset:38912\n" \
"v_mfma_f32_16x16x32_bf16 a[148:151], v[160:163], v[88:91], a[148:151]\n" \
"s_add_u32 m0, %[mA], 0x800\n" \
"s_mov_b32 s85, 0x4a00\n" \
"v_mfma_f32_16x16x32_bf16 a[148:151], v[156:159], v[84:87], a[148:151]\n" \
"buffer_load_dwordx4 v24, s[76:79], s85 offen lds\n" \
"v_mfma_f32_16x16x32_bf16 a[152:155], v[164:167], v[84:87], a[152:155]\n" \
"v_mfma_f32_16x16x32_bf16 a[152:155], v[168:171], v[88:91], a[152:155]\n" \
"v_mfma_f32_16x16x32_bf16 a[156:159], v[176:179], v[88:91], a[156:159]\n" \
"s_add_u32 m0, %[mA], 0xc00\n" \
"s_mov_b32 s84, 0x6e00\n" \
"v_mfma_f32_16x16x32_bf16 a[156:159], v[172:175], v[84:87], a[156:159]\n" \
"buffer_load_dwordx4 v25, s[76:79], s84 offen lds\n" \
"v_mfma_f32_16x16x32_bf16 a[176:179], v[148:151], v[92:95], a[176:179]\n" \
"ds_read_b128 v[84:87], v22 offset:32768\n" \
"ds_read_b128 v[88:91], v23 offset:32768\n" \
"v_mfma_f32_16x16x32_bf16 a[176:179], v[152:155], v[96:99], a[176:179]\n" \
"v_mfma_f32_16x16x32_bf16 a[180:183], v[160:163], v[96:99], a[180:183]\n" \
"s_add_u32 m0, %[mB], 0x400\n" \
"s_mov_b32 s85, 0x208000\n" \
"v_mfma_f32_16x16x32_bf16 a[180:183], v[156:159], v[92:95], a[180:183]\n" \
"buffer_load_dwordx4 v26, s[68:71], s85 offen lds\n" \
"v_mfma_f32_16x16x32_bf16 a[184:187], v[164:167], v[92:95], a[184:187]\n" \
"v_mfma_f32_16x16x32_bf16 a[184:187], v[168:171], v[96:99], a[184:187]\n" \
"v_mfma_f32_16x16x32_bf16 a[188:191], v[176:179], v[96:99], a[188:191]\n" \
"s_add_u32 m0, %[mA], 0x1000\n" \
"s_mov_b32 s84, 0x9200\n" \
"v_mfma_f32_16x16x32_bf16 a[188:191], v[172:175], v[92:95], a[188:191]\n" \
"buffer_load_dwordx4 v24, s[76:79], s84 offen lds\n" \
"v_mfma_f32_16x16x32_bf16 a[208:211], v[148:151], v[100:103], a[208:211]\n" \
"ds_read_b128 v[92:95], v48 offset:34816\n" \
"ds_read_b128 v[96:99], v49 offset:34816\n" \
"v_mfma_f32_16x16x32_bf16 a[208:211], v[152:155], v[104:107], a[208:211]\n" \
"v_mfma_f32_16x16x32_bf16 a[212:215], v[160:163], v[104:107], a[212:215]\n" \
"s_add_u32 m0, %[mA], 0x1400\n" \
"s_mov_b32 s85, 0xb600\n" \
"v_mfma_f32_16x16x32_bf16 a[212:215], v[156:159], v[100:103], a[212:215]\n" \
"buffer_load_dwordx4 v25, s[76:79], s85 offen lds\n" \
"v_mfma_f32_16x16x32_bf16 a[216:219], v[164:167], v[100:103], a[216:219]\n" \
"v_mfma_f32_16x16x32_bf16 a[216:219], v[168:171], v[104:107], a[216:219]\n" \
"v_mfma_f32_16x16x32_bf16 a[220:223], v[176:179], v[104:107], a[220:223]\n" \
"s_add_u32 m0, %[mB], 0x800\n" \
"s_mov_b32 s84, 0x210000\n" \
"v_mfma_f32_16x16x32_bf16 a[220:223], v[172:175], v[100:103], a[220:223]\n" \
"buffer_load_dwordx4 v27, s[68:71], s84 offen lds\n" \
"v_mfma_f32_16x16x32_bf16 a[240:243], v[148:151], v[108:111], a[240:243]\n" \
"ds_read_b64_tr_b16 v[148:149], v32 offset:32768\n" \
"ds_read_b64_tr_b16 v[150:151], v32 offset:33792\n" \
"v_mfma_f32_16x16x32_bf16 a[240:243], v[152:155], v[112:115], a[240:243]\n" \
"ds_read_b64_tr_b16 v[152:153], v32 offset:40960\n" \
"ds_read_b64_tr_b16 v[154:155], v32 offset:41984\n" \
"v_mfma_f32_16x16x32_bf16 a[244:247], v[160:163], v[112:115], a[244:247]\n" \
"ds_read_b64_tr_b16 v[160:161], v33 offset:40960\n" \
"ds_read_b64_tr_b16 v[162:163], v33 offset:41984\n" \
"v_mfma_f32_16x16x32_bf16 a[244:247], v[156:159], v[108:111], a[244:247]\n" \
"ds_read_b64_tr_b16 v[156:157], v33 offset:32768\n" \
"ds_read_b64_tr_b16 v[158:159], v33 offset:33792\n" \
"v_mfma_f32_16x16x32_bf16 a[248:251], v[164:167], v[108:111], a[248:251]\n" \
"ds_read_b64_tr_b16 v[164:165], v34 offset:32768\n" \
"ds_read_b64_tr_b16 v[166:167], v34 offset:33792\n" \
"v_mfma_f32_16x16x32_bf16 a[248:251], v[168:171], v[112:115], a[248:251]\n" \
"ds_read_b64_tr_b16 v[168:169], v34 offset:40960\n" \
"ds_read_b64_tr_b16 v[170:171], v34 offset:41984\n" \
"v_mfma_f32_16x16x32_bf16 a[252:255], v[176:179], v[112:115], a[252:255]\n" \
"ds_read_b64_tr_b16 v[176:177], v35 offset:40960\n" \
"ds_read_b64_tr_b16 v[178:179], v35 offset:41984\n" \
"v_mfma_f32_16x16x32_bf16 a[252:255], v[172:175], v[108:111], a[252:255]\n" \
"s_add_u32 m0, %[mA], 0x1800\n" \
"s_waitcnt lgkmcnt(14)\n" \
"s_nop 0\n" \
"v_mfma_f32_16x16x32_bf16 a[16:19], v[140:143], v[84:87], a[16:19]\n" \
"s_mov_b32 s84, 0xda00\n" \
"ds_read_b64_tr_b16 v[172:173], v35 offset:32768\n" \
"v_mfma_f32_16x16x32_bf16 a[16:19], v[144:147], v[88:91], a[16:19]\n" \
"buffer_load_dwordx4 v24, s[76:79], s84 offen lds\n" \
"v_mfma_f32_16x16x32_bf16 a[48:51], v[128:131], v[88:91], a[48:51]\n" \
"s_add_u32 m0, %[mA], 0x1c00\n" \
"s_mov_b32 s85, 0xfe00\n" \
"v_mfma_f32_16x16x32_bf16 a[48:51], v[124:127], v[84:87], a[48:51]\n" \
"ds_read_b64_tr_b16 v[174:175], v35 offset:33792\n" \
"ds_read_b128 v[100:103], v48 offset:36864\n" \
"v_mfma_f32_16x16x32_bf16 a[80:83], v[84:87], v[132:135], a[80:83]\n" \
"buffer_load_dwordx4 v25, s[76:79], s85 offen lds\n" \
"v_mfma_f32_16x16x32_bf16 a[80:83], v[88:91], v[136:139], a[80:83]\n" \
"s_add_u32 m0, %[mB], 0xc00\n" \
"s_mov_b32 s84, 0x218000\n" \
"v_mfma_f32_16x16x32_bf16 a[112:115], v[144:147], v[56:59], a[112:115]\n" \
"ds_read_b128 v[84:87], v48 offset:32768\n" \
"ds_read_b128 v[88:91], v49 offset:32768\n" \
"buffer_load_dwordx4 v27, s[68:71], s84 offen lds\n" \
"v_mfma_f32_16x16x32_bf16 a[112:115], v[140:143], v[52:55], a[112:115]\n" \
"ds_read_b128 v[104:107], v49 offset:36864\n" \
"ds_read_b128 v[108:111], v48 offset:38912\n" \
"v_mfma_f32_16x16x32_bf16 a[20:23], v[124:127], v[52:55], a[20:23]\n" \
"ds_read_b128 v[112:115], v49 offset:38912\n" \
"v_mfma_f32_16x16x32_bf16 a[20:23], v[128:131], v[56:59], a[20:23]\n" \
"s_add_u32 s88, %[oKA], 0x2000\n" \
"v_mfma_f32_16x16x32_bf16 a[52:55], v[120:123], v[56:59], a[52:55]\n" \
"buffer_store_dwordx4 v[224:227], v20, s[80:83], s88 offen offset:0 sc1\n" \
"v_mfma_f32_16x16x32_bf16 a[52:55], v[116:119], v[52:55], a[52:55]\n" \
"v_mfma_f32_16x16x32_bf16 a[84:87], v[52:55], v[132:135], a[84:87]\n" \
"v_mfma_f32_16x16x32_bf16 a[84:87], v[56:59], v[136:139], a[84:87]\n" \
"v_mfma_f32_16x16x32_bf16 a[116:119], v[144:147], v[64:67], a[116:119]\n" \
"v_mfma_f32_16x16x32_bf16 a[116:119], v[140:143], v[60:63], a[116:119]\n" \
"v_mfma_f32_16x16x32_bf16 a[24:27], v[124:127], v[60:63], a[24:27]\n" \
"v_mfma_f32_16x16x32_bf16 a[24:27], v[128:131], v[64:67], a[24:27]\n" \
"v_mfma_f32_16x16x32_bf16 a[56:59], v[120:123], v[64:67], a[56:59]\n" \
"v_mfma_f32_16x16x32_bf16 a[56:59], v[116:119], v[60:63], a[56:59]\n" \
"s_add_u32 s89, %[oKA], 0x3000\n" \
"v_mfma_f32_16x16x32_bf16 a[88:91], v[60:63], v[132:135], a[88:91]\n" \
"buffer_store_dwordx4 v[228:231], v20, s[80:83], s89 offen offset:0 sc1\n" \
"v_mfma_f32_16x16x32_bf16 a[88:91], v[64:67], v[136:139], a[88:91]\n" \
"v_mfma_f32_16x16x32_bf16 a[120:123], v[144:147], v[72:75], a[120:123]\n" \
"v_mfma_f32_16x16x32_bf16 a[120:123], v[140:143], v[68:71], a[120:123]\n" \
"v_mfma_f32_16x16x32_bf16 a[28:31], v[124:127], v[68:71], a[28:31]\n" \
"v_mfma_f32_16x16x32_bf16 a[28:31], v[128:131], v[72:75], a[28:31]\n" \
"v_mfma_f32_16x16x32_bf16 a[60:63], v[120:123], v[72:75], a[60:63]\n" \
"v_mfma_f32_16x16x32_bf16 a[60:63], v[116:119], v[68:71], a[60:63]\n" \
"v_mfma_f32_16x16x32_bf16 a[92:95], v[68:71], v[132:135], a[92:95]\n" \
"v_mfma_f32_16x16x32_bf16 a[92:95], v[72:75], v[136:139], a[92:95]\n" \
"s_add_u32 s88, %[oKA], 0x0\n" \
"v_mfma_f32_16x16x32_bf16 a[124:127], v[144:147], v[80:83], a[124:127]\n" \
"buffer_store_dwordx4 v[232:235], v20, s[80:83], s88 offen offset:1024 sc1\n" \
"v_mfma_f32_16x16x32_bf16 a[124:127], v[140:143], v[76:79], a[124:127]\n" \
"v_mfma_f32_16x16x32_bf16 a[0:3], v[124:127], v[76:79], a[0:3]\n" \
"v_mfma_f32_16x16x32_bf16 a[0:3], v[128:131], v[80:83], a[0:3]\n" \
"v_mfma_f32_16x16x32_bf16 a[4:7], v[120:123], v[80:83], a[4:7]\n" \
"v_mfma_f32_16x16x32_bf16 a[4:7], v[116:119], v[76:79], a[4:7]\n" \
"v_mfma_f32_16x16x32_bf16 a[32:35], v[76:79], v[132:135], a[32:35]\n" \
"v_mfma_f32_16x16x32_bf16 a[32:35], v[80:83], v[136:139], a[32:35]\n" \
"s_waitcnt lgkmcnt(0)\n" \
"v_mfma_f32_16x16x32_bf16 a[128:131], v[152:155], v[56:59], a[128:131]\n" \
"v_mfma_f32_16x16x32_bf16 a[128:131], v[148:151], v[52:55], a[128:131]\n" \
"v_mfma_f32_16x16x32_bf16 a[132:135], v[156:159], v[52:55], a[132:135]\n" \
"v_mfma_f32_16x16x32_bf16 a[132:135], v[160:163], v[56:59], a[132:135]\n" \
"v_mfma_f32_16x16x32_bf16 a[136:139], v[168:171], v[56:59], a[136:139]\n" \
"v_mfma_f32_16x16x32_bf16 a[136:139], v[164:167], v[52:55], a[136:139]\n" \
"v_mfma_f32_16x16x32_bf16 a[140:143], v[172:175], v[52:55], a[140:143]\n" \
"v_mfma_f32_16x16x32_bf16 a[140:143], v[176:179], v[56:59], a[140:143]\n" \
"v_mfma_f32_16x16x32_bf16 a[160:163], v[152:155], v[64:67], a[160:163]\n" \
"v_mfma_f32_16x16x32_bf16 a[160:163], v[148:151], v[60:63], a[160:163]\n" \
"s_waitcnt vmcnt(3)\n" \
"s_barrier\n" \
"v_mfma_f32_16x16x32_bf16 a[164:167], v[156:159], v[60:63], a[164:167]\n" \
"s_add_u32 m0, %[mL], 0x8000\n" \
"s_add_u32 s85, %[oL], 0xa000\n" \
"v_mfma_f32_16x16x32_bf16 a[164:167], v[160:163], v[64:67], a[164:167]\n" \
"buffer_load_dwordx4 v20, s[80:83], s85 offen sc1 lds\n" \
"v_mfma_f32_16x16x32_bf16 a[168:171], v[168:171], v[64:67], a[168:171]\n" \
"ds_read_b64_tr_b16 v[144:145], v39 offset:4096\n" \
"ds_read_b64_tr_b16 v[146:147], v39 offset:4608\n" \
"v_mfma_f32_16x16x32_bf16 a[168:171], v[164:167], v[60:63], a[168:171]\n" \
"s_add_u32 m0, %[mL], 0x9000\n" \
"s_add_u32 s84, %[oL], 0xb000\n" \
"v_mfma_f32_16x16x32_bf16 a[172:175], v[172:175], v[60:63], a[172:175]\n" \
"buffer_load_dwordx4 v20, s[80:83], s84 offen sc1 lds\n" \
"v_mfma_f32_16x16x32_bf16 a[172:175], v[176:179], v[64:67], a[172:175]\n" \
"ds_read_b64_tr_b16 v[140:141], v39 offset:0\n" \
"ds_read_b64_tr_b16 v[142:143], v39 offset:512\n" \
"v_mfma_f32_16x16x32_bf16 a[192:195], v[152:155], v[72:75], a[192:195]\n" \
"ds_read_b64_tr_b16 v[124:125], v37 offset:0\n" \
"ds_read_b64_tr_b16 v[126:127], v37 offset:512\n" \
"v_mfma_f32_16x16x32_bf16 a[192:195], v[148:151], v[68:71], a[192:195]\n" \
"s_add_u32 m0, %[mL], 0xb000\n" \
"s_mov_b32 s85, 0x280\n" \
"v_mfma_f32_16x16x32_bf16 a[196:199], v[156:159], v[68:71], a[196:199]\n" \
"buffer_load_dwordx4 v21, s[76:79], s85 offen lds\n" \
"v_mfma_f32_16x16x32_bf16 a[196:199], v[160:163], v[72:75], a[196:199]\n" \
"ds_read_b64_tr_b16 v[128:129], v37 offset:4096\n" \
"ds_read_b64_tr_b16 v[130:131], v37 offset:4608\n" \
"v_mfma_f32_16x16x32_bf16 a[200:203], v[168:171], v[72:75], a[200:203]\n" \
"ds_read_b64_tr_b16 v[136:137], v38 offset:4096\n" \
"ds_read_b64_tr_b16 v[138:139], v38 offset:4608\n" \
"v_mfma_f32_16x16x32_bf16 a[200:203], v[164:167], v[68:71], a[200:203]\n" \
"s_add_u32 m0, %[mA], 0x8000\n" \
"s_mov_b32 s84, 0x280\n" \
"v_mfma_f32_16x16x32_bf16 a[204:207], v[172:175], v[68:71], a[204:207]\n" \
"buffer_load_dwordx4 v24, s[76:79], s84 offen lds\n" \
"v_mfma_f32_16x16x32_bf16 a[204:207], v[176:179], v[72:75], a[204:207]\n" \
"ds_read_b64_tr_b16 v[132:133], v38 offset:0\n" \
"ds_read_b64_tr_b16 v[134:135], v38 offset:512\n" \
"v_mfma_f32_16x16x32_bf16 a[224:227], v[152:155], v[80:83], a[224:227]\n" \
"ds_read_b128 v[52:55], v28 offset:0\n" \
"ds_read_b128 v[56:59], v29 offset:0\n" \
"v_mfma_f32_16x16x32_bf16 a[224:227], v[148:151], v[76:79], a[224:227]\n" \
"s_add_u32 m0, %[mA], 0x8400\n" \
"s_mov_b32 s85, 0x2680\n" \
"v_mfma_f32_16x16x32_bf16 a[228:231], v[156:159], v[76:79], a[228:231]\n" \
"buffer_load_dwordx4 v25, s[76:79], s85 offen lds\n" \
"v_mfma_f32_16x16x32_bf16 a[228:231], v[160:163], v[80:83], a[228:231]\n" \
"ds_read_b64_tr_b16 v[116:117], v36 offset:0\n" \
"ds_read_b64_tr_b16 v[118:119], v36 offset:512\n" \
"v_mfma_f32_16x16x32_bf16 a[232:235], v[168:171], v[80:83], a[232:235]\n" \
"ds_read_b64_tr_b16 v[120:121], v36 offset:4096\n" \
"ds_read_b64_tr_b16 v[122:123], v36 offset:4608\n" \
"v_mfma_f32_16x16x32_bf16 a[232:235], v[164:167], v[76:79], a[232:235]\n" \
"s_add_u32 m0, %[mB], 0x8000\n" \
"s_mov_b32 s84, 0x280000\n" \
"v_mfma_f32_16x16x32_bf16 a[236:239], v[172:175], v[76:79], a[236:239]\n" \
"buffer_load_dwordx4 v26, s[68:71], s84 offen lds\n" \
"v_mfma_f32_16x16x32_bf16 a[236:239], v[176:179], v[80:83], a[236:239]\n" \
"ds_read_b128 v[60:63], v28 offset:2048\n" \
"ds_read_b128 v[64:67], v29 offset:2048\n" \
"v_mfma_f32_16x16x32_bf16 a[144:147], v[152:155], v[88:91], a[144:147]\n" \
"ds_read_b128 v[68:71], v28 offset:4096\n" \
"ds_read_b128 v[72:75], v29 offset:4096\n" \
"v_mfma_f32_16x16x32_bf16 a[144:147], v[148:151], v[84:87], a[144:147]\n" \
"ds_read_b128 v[76:79], v28 offset:6144\n" \
"ds_read_b128 v[80:83], v29 offset:6144\n" \
"v_mfma_f32_16x16x32_bf16 a[148:151], v[156:159], v[84:87], a[148:151]\n" \
"s_add_u32 m0, %[mA], 0x8800\n" \
"s_mov_b32 s85, 0x4a80\n" \
"v_mfma_f32_16x16x32_bf16 a[148:151], v[160:163], v[88:91], a[148:151]\n" \
"buffer_load_dwordx4 v24, s[76:79], s85 offen lds\n" \
"v_mfma_f32_16x16x32_bf16 a[152:155], v[168:171], v[88:91], a[152:155]\n" \
"v_mfma_f32_16x16x32_bf16 a[152:155], v[164:167], v[84:87], a[152:155]\n" \
"v_mfma_f32_16x16x32_bf16 a[156:159], v[172:175], v[84:87], a[156:159]\n" \
"s_add_u32 m0, %[mA], 0x8c00\n" \
"s_mov_b32 s84, 0x6e80\n" \
"v_mfma_f32_16x16x32_bf16 a[156:159], v[176:179], v[88:91], a[156:159]\n" \
"buffer_load_dwordx4 v25, s[76:79], s84 offen lds\n" \
"v_mfma_f32_16x16x32_bf16 a[176:179], v[152:155], v[96:99], a[176:179]\n" \
"ds_read_b128 v[88:91], v23 offset:0\n" \
"ds_read_b128 v[84:87], v22 offset:0\n" \
"v_mfma_f32_16x16x32_bf16 a[176:179], v[148:151], v[92:95], a[176:179]\n" \
"v_mfma_f32_16x16x32_bf16 a[180:183], v[156:159], v[92:95], a[180:183]\n" \
"s_add_u32 m0, %[mB], 0x8400\n" \
"s_mov_b32 s85, 0x288000\n" \
"v_mfma_f32_16x16x32_bf16 a[180:183], v[160:163], v[96:99], a[180:183]\n" \
"buffer_load_dwordx4 v26, s[68:71], s85 offen lds\n" \
"v_mfma_f32_16x16x32_bf16 a[184:187], v[168:171], v[96:99], a[184:187]\n" \
"v_mfma_f32_16x16x32_bf16 a[184:187], v[164:167], v[92:95], a[184:187]\n" \
"v_mfma_f32_16x16x32_bf16 a[188:191], v[172:175], v[92:95], a[188:191]\n" \
"s_add_u32 m0, %[mA], 0x9000\n" \
"s_mov_b32 s84, 0x9280\n" \
"v_mfma_f32_16x16x32_bf16 a[188:191], v[176:179], v[96:99], a[188:191]\n" \
"buffer_load_dwordx4 v24, s[76:79], s84 offen lds\n" \
"v_mfma_f32_16x16x32_bf16 a[208:211], v[152:155], v[104:107], a[208:211]\n" \
"ds_read_b128 v[92:95], v48 offset:2048\n" \
"ds_read_b128 v[96:99], v49 offset:2048\n" \
"v_mfma_f32_16x16x32_bf16 a[208:211], v[148:151], v[100:103], a[208:211]\n" \
"v_mfma_f32_16x16x32_bf16 a[212:215], v[156:159], v[100:103], a[212:215]\n" \
"s_add_u32 m0, %[mA], 0x9400\n" \
"s_mov_b32 s85, 0xb680\n" \
"v_mfma_f32_16x16x32_bf16 a[212:215], v[160:163], v[104:107], a[212:215]\n" \
"buffer_load_dwordx4 v25, s[76:79], s85 offen lds\n" \
"v_mfma_f32_16x16x32_bf16 a[216:219], v[168:171], v[104:107], a[216:219]\n" \
"v_mfma_f32_16x16x32_bf16 a[216:219], v[164:167], v[100:103], a[216:219]\n" \
"v_mfma_f32_16x16x32_bf16 a[220:223], v[172:175], v[100:103], a[220:223]\n" \
"s_add_u32 m0, %[mB], 0x8800\n" \
"s_mov_b32 s84, 0x290000\n" \
"v_mfma_f32_16x16x32_bf16 a[220:223], v[176:179], v[104:107], a[220:223]\n" \
"buffer_load_dwordx4 v27, s[68:71], s84 offen lds\n" \
"v_mfma_f32_16x16x32_bf16 a[240:243], v[152:155], v[112:115], a[240:243]\n" \
"ds_read_b64_tr_b16 v[152:153], v32 offset:8192\n" \
"ds_read_b64_tr_b16 v[154:155], v32 offset:9216\n" \
"v_mfma_f32_16x16x32_bf16 a[240:243], v[148:151], v[108:111], a[240:243]\n" \
"ds_read_b64_tr_b16 v[148:149], v32 offset:0\n" \
"ds_read_b64_tr_b16 v[150:151], v32 offset:1024\n" \
"v_mfma_f32_16x16x32_bf16 a[244:247], v[156:159], v[108:111], a[244:247]\n" \
"ds_read_b64_tr_b16 v[156:157], v33 offset:0\n" \
"ds_read_b64_tr_b16 v[158:159], v33 offset:1024\n" \
"v_mfma_f32_16x16x32_bf16 a[244:247], v[160:163], v[112:115], a[244:247]\n" \
"ds_read_b64_tr_b16 v[160:161], v33 offset:8192\n" \
"ds_read_b64_tr_b16 v[162:163], v33 offset:9216\n" \
"v_mfma_f32_16x16x32_bf16 a[248:251], v[168:171], v[112:115], a[248:251]\n" \
"ds_read_b64_tr_b16 v[168:169], v34 offset:8192\n" \
"ds_read_b64_tr_b16 v[170:171], v34 offset:9216\n" \
"v_mfma_f32_16x16x32_bf16 a[248:251], v[164:167], v[108:111], a[248:251]\n" \
"ds_read_b64_tr_b16 v[164:165], v34 offset:0\n" \
"ds_read_b64_tr_b16 v[166:167], v34 offset:1024\n" \
"v_mfma_f32_16x16x32_bf16 a[252:255], v[172:175], v[108:111], a[252:255]\n" \
"ds_read_b64_tr_b16 v[172:173], v35 offset:0\n" \
"ds_read_b64_tr_b16 v[174:175], v35 offset:1024\n" \
"v_mfma_f32_16x16x32_bf16 a[252:255], v[176:179], v[112:115], a[252:255]\n" \
"s_add_u32 m0, %[mA], 0x9800\n" \
"s_waitcnt lgkmcnt(14)\n" \
"s_nop 0\n" \
"v_mfma_f32_16x16x32_bf16 a[16:19], v[144:147], v[88:91], a[16:19]\n" \
"s_mov_b32 s84, 0xda80\n" \
"ds_read_b64_tr_b16 v[176:177], v35 offset:8192\n" \
"v_mfma_f32_16x16x32_bf16 a[16:19], v[140:143], v[84:87], a[16:19]\n" \
"buffer_load_dwordx4 v24, s[76:79], s84 offen lds\n" \
"v_mfma_f32_16x16x32_bf16 a[48:51], v[124:127], v[84:87], a[48:51]\n" \
"s_add_u32 m0, %[mA], 0x9c00\n" \
"s_mov_b32 s85, 0xfe80\n" \
"v_mfma_f32_16x16x32_bf16 a[48:51], v[128:131], v[88:91], a[48:51]\n" \
"ds_read_b64_tr_b16 v[178:179], v35 offset:9216\n" \
"ds_read_b128 v[100:103], v48 offset:4096\n" \
"v_mfma_f32_16x16x32_bf16 a[80:83], v[88:91], v[136:139], a[80:83]\n" \
"buffer_load_dwordx4 v25, s[76:79], s85 offen lds\n" \
"v_mfma_f32_16x16x32_bf16 a[80:83], v[84:87], v[132:135], a[80:83]\n" \
"s_add_u32 m0, %[mB], 0x8c00\n" \
"s_mov_b32 s84, 0x298000\n" \
"v_mfma_f32_16x16x32_bf16 a[112:115], v[140:143], v[52:55], a[112:115]\n" \
"ds_read_b128 v[84:87], v48 offset:0\n" \
"ds_read_b128 v[88:91], v49 offset:0\n" \
"buffer_load_dwordx4 v27, s[68:71], s84 offen lds\n" \
"v_mfma_f32_16x16x32_bf16 a[112:115], v[144:147], v[56:59], a[112:115]\n" \
"ds_read_b128 v[104:107], v49 offset:4096\n" \
"ds_read_b128 v[108:111], v48 offset:6144\n" \
"v_mfma_f32_16x16x32_bf16 a[20:23], v[128:131], v[56:59], a[20:23]\n" \
"ds_read_b128 v[112:115], v49 offset:6144\n" \
"v_mfma_f32_16x16x32_bf16 a[20:23], v[124:127], v[52:55], a[20:23]\n" \
"s_add_u32 s89, %[oKA], 0x1000\n" \
"v_mfma_f32_16x16x32_bf16 a[52:55], v[116:119], v[52:55], a[52:55]\n" \
"buffer_store_dwordx4 v[236:239], v20, s[80:83], s89 offen offset:1024 sc1\n" \
"v_mfma_f32_16x16x32_bf16 a[52:55], v[120:123], v[56:59], a[52:55]\n" \
"v_mfma_f32_16x16x32_bf16 a[84:87], v[56:59], v[136:139], a[84:87]\n" \
"v_mfma_f32_16x16x32_bf16 a[84:87], v[52:55], v[132:135], a[84:87]\n" \
"v_mfma_f32_16x16x32_bf16 a[116:119], v[140:143], v[60:63], a[116:119]\n" \
"v_mfma_f32_16x16x32_bf16 a[116:119], v[144:147], v[64:67], a[116:119]\n" \
"v_mfma_f32_16x16x32_bf16 a[24:27], v[128:131], v[64:67], a[24:27]\n" \
"v_mfma_f32_16x16x32_bf16 a[24:27], v[124:127], v[60:63], a[24:27]\n" \
"v_mfma_f32_16x16x32_bf16 a[56:59], v[116:119], v[60:63], a[56:59]\n" \
"v_mfma_f32_16x16x32_bf16 a[56:59], v[120:123], v[64:67], a[56:59]\n" \
"s_add_u32 s88, %[oKA], 0x2000\n" \
"v_mfma_f32_16x16x32_bf16 a[88:91], v[64:67], v[136:139], a[88:91]\n" \
"buffer_store_dwordx4 v[240:243], v20, s[80:83], s88 offen offset:1024 sc1\n" \
"v_mfma_f32_16x16x32_bf16 a[88:91], v[60:63], v[132:135], a[88:91]\n" \
"v_mfma_f32_16x16x32_bf16 a[120:123], v[140:143], v[68:71], a[120:123]\n" \
"v_mfma_f32_16x16x32_bf16 a[120:123], v[144:147], v[72:75], a[120:123]\n" \
"v_mfma_f32_16x16x32_bf16 a[28:31], v[128:131], v[72:75], a[28:31]\n" \
"v_mfma_f32_16x16x32_bf16 a[28:31], v[124:127], v[68:71], a[28:31]\n" \
"v_mfma_f32_16x16x32_bf16 a[60:63], v[116:119], v[68:71], a[60:63]\n" \
"v_mfma_f32_16x16x32_bf16 a[60:63], v[120:123], v[72:75], a[60:63]\n" \
"v_mfma_f32_16x16x32_bf16 a[92:95], v[72:75], v[136:139], a[92:95]\n" \
"v_mfma_f32_16x16x32_bf16 a[92:95], v[68:71], v[132:135], a[92:95]\n" \
"s_add_u32 s89, %[oKA], 0x3000\n" \
"v_mfma_f32_16x16x32_bf16 a[124:127], v[140:143], v[76:79], a[124:127]\n" \
"buffer_store_dwordx4 v[244:247], v20, s[80:83], s89 offen offset:1024 sc1\n" \
"v_mfma_f32_16x16x32_bf16 a[124:127], v[144:147], v[80:83], a[124:127]\n" \
"v_mfma_f32_16x16x32_bf16 a[0:3], v[128:131], v[80:83], a[0:3]\n" \
"v_mfma_f32_16x16x32_bf16 a[0:3], v[124:127], v[76:79], a[0:3]\n" \
"v_mfma_f32_16x16x32_bf16 a[4:7], v[116:119], v[76:79], a[4:7]\n" \
"v_mfma_f32_16x16x32_bf16 a[4:7], v[120:123], v[80:83], a[4:7]\n" \
"v_mfma_f32_16x16x32_bf16 a[32:35], v[80:83], v[136:139], a[32:35]\n" \
"v_mfma_f32_16x16x32_bf16 a[32:35], v[76:79], v[132:135], a[32:35]\n" \
"s_waitcnt lgkmcnt(0)\n" \
"v_mfma_f32_16x16x32_bf16 a[128:131], v[148:151], v[52:55], a[128:131]\n" \
"v_mfma_f32_16x16x32_bf16 a[128:131], v[152:155], v[56:59], a[128:131]\n" \
"v_mfma_f32_16x16x32_bf16 a[132:135], v[160:163], v[56:59], a[132:135]\n" \
"v_mfma_f32_16x16x32_bf16 a[132:135], v[156:159], v[52:55], a[132:135]\n" \
"v_mfma_f32_16x16x32_bf16 a[136:139], v[164:167], v[52:55], a[136:139]\n" \
"v_mfma_f32_16x16x32_bf16 a[136:139], v[168:171], v[56:59], a[136:139]\n" \
"v_mfma_f32_16x16x32_bf16 a[140:143], v[176:179], v[56:59], a[140:143]\n" \
"v_mfma_f32_16x16x32_bf16 a[140:143], v[172:175], v[52:55], a[140:143]\n" \
"v_mfma_f32_16x16x32_bf16 a[160:163], v[148:151], v[60:63], a[160:163]\n" \
"v_mfma_f32_16x16x32_bf16 a[160:163], v[152:155], v[64:67], a[160:163]\n" \
"s_waitcnt vmcnt(3)\n" \
"s_barrier\n" \
"v_mfma_f32_16x16x32_bf16 a[164:167], v[160:163], v[64:67], a[164:167]\n" \
"s_add_u32 m0, %[mL], 0x0\n" \
"s_add_u32 s85, %[oL], 0xc000\n" \
"v_mfma_f32_16x16x32_bf16 a[164:167], v[156:159], v[60:63], a[164:167]\n" \
"buffer_load_dwordx4 v20, s[80:83], s85 offen sc1 lds\n" \
"v_mfma_f32_16x16x32_bf16 a[168:171], v[164:167], v[60:63], a[168:171]\n" \
"ds_read_b64_tr_b16 v[140:141], v39 offset:32768\n" \
"ds_read_b64_tr_b16 v[142:143], v39 offset:33280\n" \
"v_mfma_f32_16x16x32_bf16 a[168:171], v[168:171], v[64:67], a[168:171]\n" \
"s_add_u32 m0, %[mL], 0x1000\n" \
"s_add_u32 s84, %[oL], 0xd000\n" \
"v_mfma_f32_16x16x32_bf16 a[172:175], v[176:179], v[64:67], a[172:175]\n" \
"buffer_load_dwordx4 v20, s[80:83], s84 offen sc1 lds\n" \
"v_mfma_f32_16x16x32_bf16 a[172:175], v[172:175], v[60:63], a[172:175]\n" \
"ds_read_b64_tr_b16 v[144:145], v39 offset:36864\n" \
"ds_read_b64_tr_b16 v[146:147], v39 offset:37376\n" \
"v_mfma_f32_16x16x32_bf16 a[192:195], v[148:151], v[68:71], a[192:195]\n" \
"ds_read_b64_tr_b16 v[128:129], v37 offset:36864\n" \
"ds_read_b64_tr_b16 v[130:131], v37 offset:37376\n" \
"v_mfma_f32_16x16x32_bf16 a[192:195], v[152:155], v[72:75], a[192:195]\n" \
"s_add_u32 m0, %[mL], 0x3000\n" \
"s_mov_b32 s85, 0x300\n" \
"v_mfma_f32_16x16x32_bf16 a[196:199], v[160:163], v[72:75], a[196:199]\n" \
"buffer_load_dwordx4 v21, s[76:79], s85 offen lds\n" \
"v_mfma_f32_16x16x32_bf16 a[196:199], v[156:159], v[68:71], a[196:199]\n" \
"ds_read_b64_tr_b16 v[124:125], v37 offset:32768\n" \
"ds_read_b64_tr_b16 v[126:127], v37 offset:33280\n" \
"v_mfma_f32_16x16x32_bf16 a[200:203], v[164:167], v[68:71], a[200:203]\n" \
"ds_read_b64_tr_b16 v[132:133], v38 offset:32768\n" \
"ds_read_b64_tr_b16 v[134:135], v38 offset:33280\n" \
"v_mfma_f32_16x16x32_bf16 a[200:203], v[168:171], v[72:75], a[200:203]\n" \
"s_add_u32 m0, %[mA], 0x0\n" \
"s_nop 0\n" \
"s_mov_b32 s84, 0x300\n" \
"v_mfma_f32_16x16x32_bf16 a[204:207], v[176:179], v[72:75], a[204:207]\n" \
"buffer_load_dwordx4 v24, s[76:79], s84 offen lds\n" \
"v_mfma_f32_16x16x32_bf16 a[204:207], v[172:175], v[68:71], a[204:207]\n" \
"ds_read_b64_tr_b16 v[136:137], v38 offset:36864\n" \
"ds_read_b64_tr_b16 v[138:139], v38 offset:37376\n" \
"v_mfma_f32_16x16x32_bf16 a[224:227], v[148:151], v[76:79], a[224:227]\n" \
"ds_read_b128 v[56:59], v29 offset:32768\n" \
"ds_read_b128 v[52:55], v28 offset:32768\n" \
"v_mfma_f32_16x16x32_bf16 a[224:227], v[152:155], v[80:83], a[224:227]\n" \
"s_add_u32 m0, %[mA], 0x400\n" \
"s_mov_b32 s85, 0x2700\n" \
"v_mfma_f32_16x16x32_bf16 a[228:231], v[160:163], v[80:83], a[228:231]\n" \
"buffer_load_dwordx4 v25, s[76:79], s85 offen lds\n" \
"v_mfma_f32_16x16x32_bf16 a[228:231], v[156:159], v[76:79], a[228:231]\n" \
"ds_read_b64_tr_b16 v[120:121], v36 offset:36864\n" \
"ds_read_b64_tr_b16 v[122:123], v36 offset:37376\n" \
"v_mfma_f32_16x16x32_bf16 a[232:235], v[164:167], v[76:79], a[232:235]\n" \
"ds_read_b64_tr_b16 v[116:117], v36 offset:32768\n" \
"ds_read_b64_tr_b16 v[118:119], v36 offset:33280\n" \
"v_mfma_f32_16x16x32_bf16 a[232:235], v[168:171], v[80:83], a[232:235]\n" \
"s_add_u32 m0, %[mB], 0x0\n" \
"s_nop 0\n" \
"s_mov_b32 s84, 0x300000\n" \
"v_mfma_f32_16x16x32_bf16 a[236:239], v[176:179], v[80:83], a[236:239]\n" \
"buffer_load_dwordx4 v26, s[68:71], s84 offen lds\n" \
"v_mfma_f32_16x16x32_bf16 a[236:239], v[172:175], v[76:79], a[236:239]\n" \
"ds_read_b128 v[64:67], v29 offset:34816\n" \
"ds_read_b128 v[60:63], v28 offset:34816\n" \
"v_mfma_f32_16x16x32_bf16 a[144:147], v[148:151], v[84:87], a[144:147]\n" \
"ds_read_b128 v[72:75], v29 offset:36864\n" \
"ds_read_b128 v[68:71], v28 offset:36864\n" \
"v_mfma_f32_16x16x32_bf16 a[144:147], v[152:155], v[88:91], a[144:147]\n" \
"ds_read_b128 v[80:83], v29 offset:38912\n" \
"ds_read_b128 v[76:79], v28 offset:38912\n" \
"v_mfma_f32_16x16x32_bf16 a[148:151], v[160:163], v[88:91], a[148:151]\n" \
"s_add_u32 m0, %[mA], 0x800\n" \
"s_mov_b32 s85, 0x4b00\n" \
"v_mfma_f32_16x16x32_bf16 a[148:151], v[156:159], v[84:87], a[148:151]\n" \
"buffer_load_dwordx4 v24, s[76:79], s85 offen lds\n" \
"v_mfma_f32_16x16x32_bf16 a[152:155], v[164:167], v[84:87], a[152:155]\n" \
"v_mfma_f32_16x16x32_bf16 a[152:155], v[168:171], v[88:91], a[152:155]\n" \
"v_mfma_f32_16x16x32_bf16 a[156:159], v[176:179], v[88:91], a[156:159]\n" \
"s_add_u32 m0, %[mA], 0xc00\n" \
"s_mov_b32 s84, 0x6f00\n" \
"v_mfma_f32_16x16x32_bf16 a[156:159], v[172:175], v[84:87], a[156:159]\n" \
"buffer_load_dwordx4 v25, s[76:79], s84 offen lds\n" \
"v_mfma_f32_16x16x32_bf16 a[176:179], v[148:151], v[92:95], a[176:179]\n" \
"ds_read_b128 v[84:87], v22 offset:32768\n" \
"ds_read_b128 v[88:91], v23 offset:32768\n" \
"v_mfma_f32_16x16x32_bf16 a[176:179], v[152:155], v[96:99], a[176:179]\n" \
"v_mfma_f32_16x16x32_bf16 a[180:183], v[160:163], v[96:99], a[180:183]\n" \
"s_add_u32 m0, %[mB], 0x400\n" \
"s_mov_b32 s85, 0x308000\n" \
"v_mfma_f32_16x16x32_bf16 a[180:183], v[156:159], v[92:95], a[180:183]\n" \
"buffer_load_dwordx4 v26, s[68:71], s85 offen lds\n" \
"v_mfma_f32_16x16x32_bf16 a[184:187], v[164:167], v[92:95], a[184:187]\n" \
"v_mfma_f32_16x16x32_bf16 a[184:187], v[168:171], v[96:99], a[184:187]\n" \
"v_mfma_f32_16x16x32_bf16 a[188:191], v[176:179], v[96:99], a[188:191]\n" \
"s_add_u32 m0, %[mA], 0x1000\n" \
"s_mov_b32 s84, 0x9300\n" \
"v_mfma_f32_16x16x32_bf16 a[188:191], v[172:175], v[92:95], a[188:191]\n" \
"buffer_load_dwordx4 v24, s[76:79], s84 offen lds\n" \
"v_mfma_f32_16x16x32_bf16 a[208:211], v[148:151], v[100:103], a[208:211]\n" \
"ds_read_b128 v[92:95], v48 offset:34816\n" \
"ds_read_b128 v[96:99], v49 offset:34816\n" \
"v_mfma_f32_16x16x32_bf16 a[208:211], v[152:155], v[104:107], a[208:211]\n" \
"L_pf6:\n" \
"s_cbranch_execz L_pf10\n" \
"s_nop 0\n" \
"v_mfma_f32_16x16x32_bf16 a[212:215], v[160:163], v[104:107], a[212:215]\n" \
"s_add_u32 m0, %[mA], 0x1400\n" \
"s_mov_b32 s85, 0xb700\n" \
"v_mfma_f32_16x16x32_bf16 a[212:215], v[156:159], v[100:103], a[212:215]\n" \
"buffer_load_dwordx4 v25, s[76:79], s85 offen lds\n" \
"v_mfma_f32_16x16x32_bf16 a[216:219], v[164:167], v[100:103], a[216:219]\n" \
"v_mfma_f32_16x16x32_bf16 a[216:219], v[168:171], v[104:107], a[216:219]\n" \
"v_mfma_f32_16x16x32_bf16 a[220:223], v[176:179], v[104:107], a[220:223]\n" \
"s_add_u32 m0, %[mB], 0x800\n" \
"s_mov_b32 s84, 0x310000\n" \
"v_mfma_f32_16x16x32_bf16 a[220:223], v[172:175], v[100:103], a[220:223]\n" \
"buffer_load_dwordx4 v27, s[68:71], s84 offen lds\n" \
"v_mfma_f32_16x16x32_bf16 a[240:243], v[148:151], v[108:111], a[240:243]\n" \
"ds_read_b64_tr_b16 v[148:149], v32 offset:32768\n" \
"ds_read_b64_tr_b16 v[150:151], v32 offset:33792\n" \
"v_mfma_f32_16x16x32_bf16 a[240:243], v[152:155], v[112:115], a[240:243]\n" \
"ds_read_b64_tr_b16 v[152:153], v32 offset:40960\n" \
"ds_read_b64_tr_b16 v[154:155], v32 offset:41984\n" \
"v_mfma_f32_16x16x32_bf16 a[244:247], v[160:163], v[112:115], a[244:247]\n" \
"ds_read_b64_tr_b16 v[160:161], v33 offset:40960\n" \
"ds_read_b64_tr_b16 v[162:163], v33 offset:41984\n" \
"v_mfma_f32_16x16x32_bf16 a[244:247], v[156:159], v[108:111], a[244:247]\n" \
"ds_read_b64_tr_b16 v[156:157], v33 offset:32768\n" \
"ds_read_b64_tr_b16 v[158:159], v33 offset:33792\n" \
"v_mfma_f32_16x16x32_bf16 a[248:251], v[164:167], v[108:111], a[248:251]\n" \
"ds_read_b64_tr_b16 v[164:165], v34 offset:32768\n" \
"ds_read_b64_tr_b16 v[166:167], v34 offset:33792\n" \
"v_mfma_f32_16x16x32_bf16 a[248:251], v[168:171], v[112:115], a[248:251]\n" \
"ds_read_b64_tr_b16 v[168:169], v34 offset:40960\n" \
"ds_read_b64_tr_b16 v[170:171], v34 offset:41984\n" \
"v_mfma_f32_16x16x32_bf16 a[252:255], v[176:179], v[112:115], a[252:255]\n" \
"ds_read_b64_tr_b16 v[176:177], v35 offset:40960\n" \
"ds_read_b64_tr_b16 v[178:179], v35 offset:41984\n" \
"v_mfma_f32_16x16x32_bf16 a[252:255], v[172:175], v[108:111], a[252:255]\n" \
"s_add_u32 m0, %[mA], 0x1800\n" \
"s_waitcnt lgkmcnt(14)\n" \
"s_nop 0\n" \
"v_mfma_f32_16x16x32_bf16 a[16:19], v[140:143], v[84:87], a[16:19]\n" \
"s_mov_b32 s84, 0xdb00\n" \
"ds_read_b64_tr_b16 v[172:173], v35 offset:32768\n" \
"v_mfma_f32_16x16x32_bf16 a[16:19], v[144:147], v[88:91], a[16:19]\n" \
"buffer_load_dwordx4 v24, s[76:79], s84 offen lds\n" \
"v_mfma_f32_16x16x32_bf16 a[48:51], v[128:131], v[88:91], a[48:51]\n" \
"s_add_u32 m0, %[mA], 0x1c00\n" \
"s_mov_b32 s85, 0xff00\n" \
"v_mfma_f32_16x16x32_bf16 a[48:51], v[124:127], v[84:87], a[48:51]\n" \
"ds_read_b64_tr_b16 v[174:175], v35 offset:33792\n" \
"ds_read_b128 v[100:103], v48 offset:36864\n" \
"v_mfma_f32_16x16x32_bf16 a[80:83], v[84:87], v[132:135], a[80:83]\n" \
"buffer_load_dwordx4 v25, s[76:79], s85 offen lds\n" \
"v_mfma_f32_16x16x32_bf16 a[80:83], v[88:91], v[136:139], a[80:83]\n" \
"s_add_u32 m0, %[mB], 0xc00\n" \
"s_mov_b32 s84, 0x318000\n" \
"v_mfma_f32_16x16x32_bf16 a[112:115], v[144:147], v[56:59], a[112:115]\n" \
"ds_read_b128 v[84:87], v48 offset:32768\n" \
"ds_read_b128 v[88:91], v49 offset:32768\n" \
"buffer_load_dwordx4 v27, s[68:71], s84 offen lds\n" \
"v_mfma_f32_16x16x32_bf16 a[112:115], v[140:143], v[52:55], a[112:115]\n" \
"ds_read_b128 v[104:107], v49 offset:36864\n" \
"ds_read_b128 v[108:111], v48 offset:38912\n" \
"v_mfma_f32_16x16x32_bf16 a[20:23], v[124:127], v[52:55], a[20:23]\n" \
"ds_read_b128 v[112:115], v49 offset:38912\n" \
"v_mfma_f32_16x16x32_bf16 a[20:23], v[128:131], v[56:59], a[20:23]\n" \
"s_add_u32 s88, %[oKB], 0x0\n" \
"s_nop 0\n" \
"v_mfma_f32_16x16x32_bf16 a[52:55], v[120:123], v[56:59], a[52:55]\n" \
"buffer_store_dwordx4 v[248:251], v20, s[80:83], s88 offen offset:0 sc1\n" \
"v_mfma_f32_16x16x32_bf16 a[52:55], v[116:119], v[52:55], a[52:55]\n" \
"v_mfma_f32_16x16x32_bf16 a[84:87], v[52:55], v[132:135], a[84:87]\n" \
"v_mfma_f32_16x16x32_bf16 a[84:87], v[56:59], v[136:139], a[84:87]\n" \
"v_mfma_f32_16x16x32_bf16 a[116:119], v[144:147], v[64:67], a[116:119]\n" \
"v_mfma_f32_16x16x32_bf16 a[116:119], v[140:143], v[60:63], a[116:119]\n" \
"v_mfma_f32_16x16x32_bf16 a[24:27], v[124:127], v[60:63], a[24:27]\n" \
"v_mfma_f32_16x16x32_bf16 a[24:27], v[128:131], v[64:67], a[24:27]\n" \
"v_mfma_f32_16x16x32_bf16 a[56:59], v[120:123], v[64:67], a[56:59]\n" \
"v_mfma_f32_16x16x32_bf16 a[56:59], v[116:119], v[60:63], a[56:59]\n" \
"s_add_u32 s89, %[oVB], 0x2000\n" \
"v_mfma_f32_16x16x32_bf16 a[88:91], v[60:63], v[132:135], a[88:91]\n" \
"buffer_store_dwordx4 v[184:187], v20, s[80:83], s89 offen offset:2048 sc1\n" \
"v_mfma_f32_16x16x32_bf16 a[88:91], v[64:67], v[136:139], a[88:91]\n" \
"v_mfma_f32_16x16x32_bf16 a[120:123], v[144:147], v[72:75], a[120:123]\n" \
"v_mfma_f32_16x16x32_bf16 a[120:123], v[140:143], v[68:71], a[120:123]\n" \
"v_mfma_f32_16x16x32_bf16 a[28:31], v[124:127], v[68:71], a[28:31]\n" \
"v_mfma_f32_16x16x32_bf16 a[28:31], v[128:131], v[72:75], a[28:31]\n" \
"v_mfma_f32_16x16x32_bf16 a[60:63], v[120:123], v[72:75], a[60:63]\n" \
"v_mfma_f32_16x16x32_bf16 a[60:63], v[116:119], v[68:71], a[60:63]\n" \
"v_mfma_f32_16x16x32_bf16 a[92:95], v[68:71], v[132:135], a[92:95]\n" \
"v_mfma_f32_16x16x32_bf16 a[92:95], v[72:75], v[136:139], a[92:95]\n" \
"s_add_u32 s88, %[oVB], 0x2000\n" \
"v_mfma_f32_16x16x32_bf16 a[124:127], v[144:147], v[80:83], a[124:127]\n" \
"buffer_store_dwordx4 v[252:255], v20, s[80:83], s88 offen offset:3072 sc1\n" \
"v_mfma_f32_16x16x32_bf16 a[124:127], v[140:143], v[76:79], a[124:127]\n" \
"v_mfma_f32_16x16x32_bf16 a[0:3], v[124:127], v[76:79], a[0:3]\n" \
"v_mfma_f32_16x16x32_bf16 a[0:3], v[128:131], v[80:83], a[0:3]\n" \
"v_mfma_f32_16x16x32_bf16 a[4:7], v[120:123], v[80:83], a[4:7]\n" \
"v_mfma_f32_16x16x32_bf16 a[4:7], v[116:119], v[76:79], a[4:7]\n" \
"v_mfma_f32_16x16x32_bf16 a[32:35], v[76:79], v[132:135], a[32:35]\n" \
"v_mfma_f32_16x16x32_bf16 a[32:35], v[80:83], v[136:139], a[32:35]\n" \
"s_waitcnt lgkmcnt(0)\n" \
"v_mfma_f32_16x16x32_bf16 a[128:131], v[152:155], v[56:59], a[128:131]\n" \
"v_mfma_f32_16x16x32_bf16 a[128:131], v[148:151], v[52:55], a[128:131]\n" \
"v_mfma_f32_16x16x32_bf16 a[132:135], v[156:159], v[52:55], a[132:135]\n" \
"v_mfma_f32_16x16x32_bf16 a[132:135], v[160:163], v[56:59], a[132:135]\n" \
"v_mfma_f32_16x16x32_bf16 a[136:139], v[168:171], v[56:59], a[136:139]\n" \
"v_mfma_f32_16x16x32_bf16 a[136:139], v[164:167], v[52:55], a[136:139]\n" \
"v_mfma_f32_16x16x32_bf16 a[140:143], v[172:175], v[52:55], a[140:143]\n" \
"v_mfma_f32_16x16x32_bf16 a[140:143], v[176:179], v[56:59], a[140:143]\n" \
"v_mfma_f32_16x16x32_bf16 a[160:163], v[152:155], v[64:67], a[160:163]\n" \
"v_mfma_f32_16x16x32_bf16 a[160:163], v[148:151], v[60:63], a[160:163]\n" \
"s_waitcnt vmcnt(3)\n" \
"s_barrier\n" \
"s_mov_b64 exec, 1\n" \
"buffer_store_dword v45, off, s[80:83], %[oFs] offset:0 sc1\n" \
"s_mov_b64 exec, -1\n" \
"s_nop 0\n" \
"v_mfma_f32_16x16x32_bf16 a[164:167], v[156:159], v[60:63], a[164:167]\n" \
"s_add_u32 m0, %[mL], 0x8000\n" \
"s_add_u32 s85, %[oL], 0xe000\n" \
"v_mfma_f32_16x16x32_bf16 a[164:167], v[160:163], v[64:67], a[164:167]\n" \
"ds_read_b64_tr_b16 v[144:145], v39 offset:4096\n" \
"ds_read_b64_tr_b16 v[146:147], v39 offset:4608\n" \
"v_mfma_f32_16x16x32_bf16 a[168:171], v[168:171], v[64:67], a[168:171]\n" \
"buffer_load_dwordx4 v20, s[80:83], s85 offen sc1 lds\n" \
"v_mfma_f32_16x16x32_bf16 a[168:171], v[164:167], v[60:63], a[168:171]\n" \
"s_add_u32 m0, %[mL], 0x9000\n" \
"s_add_u32 s84, %[oL], 0xf000\n" \
"v_mfma_f32_16x16x32_bf16 a[172:175], v[172:175], v[60:63], a[172:175]\n" \
"ds_read_b64_tr_b16 v[140:141], v39 offset:0\n" \
"ds_read_b64_tr_b16 v[142:143], v39 offset:512\n" \
"v_mfma_f32_16x16x32_bf16 a[172:175], v[176:179], v[64:67], a[172:175]\n" \
"buffer_load_dwordx4 v20, s[80:83], s84 offen sc1 lds\n" \
"v_mfma_f32_16x16x32_bf16 a[192:195], v[152:155], v[72:75], a[192:195]\n" \
"ds_read_b64_tr_b16 v[124:125], v37 offset:0\n" \
"ds_read_b64_tr_b16 v[126:127], v37 offset:512\n" \
"v_mfma_f32_16x16x32_bf16 a[192:195], v[148:151], v[68:71], a[192:195]\n" \
"s_add_u32 m0, %[mL], 0xb000\n" \
"s_mov_b32 s85, 0x380\n" \
"v_mfma_f32_16x16x32_bf16 a[196:199], v[156:159], v[68:71], a[196:199]\n" \
"buffer_load_dwordx4 v21, s[76:79], s85 offen lds\n" \
"v_mfma_f32_16x16x32_bf16 a[196:199], v[160:163], v[72:75], a[196:199]\n" \
"ds_read_b64_tr_b16 v[128:129], v37 offset:4096\n" \
"ds_read_b64_tr_b16 v[130:131], v37 offset:4608\n" \
"v_mfma_f32_16x16x32_bf16 a[200:203], v[168:171], v[72:75], a[200:203]\n" \
"ds_read_b64_tr_b16 v[136:137], v38 offset:4096\n" \
"ds_read_b64_tr_b16 v[138:139], v38 offset:4608\n" \
"v_mfma_f32_16x16x32_bf16 a[200:203], v[164:167], v[68:71], a[200:203]\n" \
"s_add_u32 m0, %[mA], 0x8000\n" \
"s_mov_b32 s84, 0x380\n" \
"v_mfma_f32_16x16x32_bf16 a[204:207], v[172:175], v[68:71], a[204:207]\n" \
"buffer_load_dwordx4 v24, s[76:79], s84 offen lds\n" \
"v_mfma_f32_16x16x32_bf16 a[204:207], v[176:179], v[72:75], a[204:207]\n" \
"ds_read_b64_tr_b16 v[132:133], v38 offset:0\n" \
"ds_read_b64_tr_b16 v[134:135], v38 offset:512\n" \
"v_mfma_f32_16x16x32_bf16 a[224:227], v[152:155], v[80:83], a[224:227]\n" \
"ds_read_b128 v[52:55], v28 offset:0\n" \
"ds_read_b128 v[56:59], v29 offset:0\n" \
"v_mfma_f32_16x16x32_bf16 a[224:227], v[148:151], v[76:79], a[224:227]\n" \
"s_add_u32 m0, %[mA], 0x8400\n" \
"s_mov_b32 s85, 0x2780\n" \
"v_mfma_f32_16x16x32_bf16 a[228:231], v[156:159], v[76:79], a[228:231]\n" \
"buffer_load_dwordx4 v25, s[76:79], s85 offen lds\n" \
"v_mfma_f32_16x16x32_bf16 a[228:231], v[160:163], v[80:83], a[228:231]\n" \
"ds_read_b64_tr_b16 v[116:117], v36 offset:0\n" \
"ds_read_b64_tr_b16 v[118:119], v36 offset:512\n" \
"v_mfma_f32_16x16x32_bf16 a[232:235], v[168:171], v[80:83], a[232:235]\n" \
"ds_read_b64_tr_b16 v[120:121], v36 offset:4096\n" \
"ds_read_b64_tr_b16 v[122:123], v36 offset:4608\n" \
"v_mfma_f32_16x16x32_bf16 a[232:235], v[164:167], v[76:79], a[232:235]\n" \
"s_add_u32 m0, %[mB], 0x8000\n" \
"s_mov_b32 s84, 0x380000\n" \
"v_mfma_f32_16x16x32_bf16 a[236:239], v[172:175], v[76:79], a[236:239]\n" \
"buffer_load_dwordx4 v26, s[68:71], s84 offen lds\n" \
"v_mfma_f32_16x16x32_bf16 a[236:239], v[176:179], v[80:83], a[236:239]\n" \
"ds_read_b128 v[60:63], v28 offset:2048\n" \
"ds_read_b128 v[64:67], v29 offset:2048\n" \
"v_mfma_f32_16x16x32_bf16 a[144:147], v[152:155], v[88:91], a[144:147]\n" \
"ds_read_b128 v[68:71], v28 offset:4096\n" \
"ds_read_b128 v[72:75], v29 offset:4096\n" \
"v_mfma_f32_16x16x32_bf16 a[144:147], v[148:151], v[84:87], a[144:147]\n" \
"ds_read_b128 v[76:79], v28 offset:6144\n" \
"ds_read_b128 v[80:83], v29 offset:6144\n" \
"v_mfma_f32_16x16x32_bf16 a[148:151], v[156:159], v[84:87], a[148:151]\n" \
"s_add_u32 m0, %[mA], 0x8800\n" \
"s_mov_b32 s85, 0x4b80\n" \
"v_mfma_f32_16x16x32_bf16 a[148:151], v[160:163], v[88:91], a[148:151]\n" \
"buffer_load_dwordx4 v24, s[76:79], s85 offen lds\n" \
"v_mfma_f32_16x16x32_bf16 a[152:155], v[168:171], v[88:91], a[152:155]\n" \
"v_mfma_f32_16x16x32_bf16 a[152:155], v[164:167], v[84:87], a[152:155]\n" \
"v_mfma_f32_16x16x32_bf16 a[156:159], v[172:175], v[84:87], a[156:159]\n" \
"s_add_u32 m0, %[mA], 0x8c00\n" \
"s_mov_b32 s84, 0x6f80\n" \
"v_mfma_f32_16x16x32_bf16 a[156:159], v[176:179], v[88:91], a[156:159]\n" \
"buffer_load_dwordx4 v25, s[76:79], s84 offen lds\n" \
"v_mfma_f32_16x16x32_bf16 a[176:179], v[152:155], v[96:99], a[176:179]\n" \
"ds_read_b128 v[88:91], v23 offset:0\n" \
"ds_read_b128 v[84:87], v22 offset:0\n" \
"v_mfma_f32_16x16x32_bf16 a[176:179], v[148:151], v[92:95], a[176:179]\n" \
"v_mfma_f32_16x16x32_bf16 a[180:183], v[156:159], v[92:95], a[180:183]\n" \
"s_add_u32 m0, %[mB], 0x8400\n" \
"s_mov_b32 s85, 0x388000\n" \
"v_mfma_f32_16x16x32_bf16 a[180:183], v[160:163], v[96:99], a[180:183]\n" \
"buffer_load_dwordx4 v26, s[68:71], s85 offen lds\n" \
"v_mfma_f32_16x16x32_bf16 a[184:187], v[168:171], v[96:99], a[184:187]\n" \
"v_mfma_f32_16x16x32_bf16 a[184:187], v[164:167], v[92:95], a[184:187]\n" \
"v_mfma_f32_16x16x32_bf16 a[188:191], v[172:175], v[92:95], a[188:191]\n" \
"s_add_u32 m0, %[mA], 0x9000\n" \
"s_mov_b32 s84, 0x9380\n" \
"v_mfma_f32_16x16x32_bf16 a[188:191], v[176:179], v[96:99], a[188:191]\n" \
"buffer_load_dwordx4 v24, s[76:79], s84 offen lds\n" \
"v_mfma_f32_16x16x32_bf16 a[208:211], v[152:155], v[104:107], a[208:211]\n" \
"ds_read_b128 v[92:95], v48 offset:2048\n" \
"ds_read_b128 v[96:99], v49 offset:2048\n" \
"v_mfma_f32_16x16x32_bf16 a[208:211], v[148:151], v[100:103], a[208:211]\n" \
"v_mfma_f32_16x16x32_bf16 a[212:215], v[156:159], v[100:103], a[212:215]\n" \
"s_add_u32 m0, %[mA], 0x9400\n" \
"s_mov_b32 s85, 0xb780\n" \
"v_mfma_f32_16x16x32_bf16 a[212:215], v[160:163], v[104:107], a[212:215]\n" \
"buffer_load_dwordx4 v25, s[76:79], s85 offen lds\n" \
"v_mfma_f32_16x16x32_bf16 a[216:219], v[168:171], v[104:107], a[216:219]\n" \
"v_mfma_f32_16x16x32_bf16 a[216:219], v[164:167], v[100:103], a[216:219]\n" \
"v_mfma_f32_16x16x32_bf16 a[220:223], v[172:175], v[100:103], a[220:223]\n" \
"s_add_u32 m0, %[mB], 0x8800\n" \
"s_mov_b32 s84, 0x390000\n" \
"v_mfma_f32_16x16x32_bf16 a[220:223], v[176:179], v[104:107], a[220:223]\n" \
"buffer_load_dwordx4 v27, s[68:71], s84 offen lds\n" \
"v_mfma_f32_16x16x32_bf16 a[240:243], v[152:155], v[112:115], a[240:243]\n" \
"ds_read_b64_tr_b16 v[152:153], v32 offset:8192\n" \
"ds_read_b64_tr_b16 v[154:155], v32 offset:9216\n" \
"v_mfma_f32_16x16x32_bf16 a[240:243], v[148:151], v[108:111], a[240:243]\n" \
"ds_read_b64_tr_b16 v[148:149], v32 offset:0\n" \
"ds_read_b64_tr_b16 v[150:151], v32 offset:1024\n" \
"v_mfma_f32_16x16x32_bf16 a[244:247], v[156:159], v[108:111], a[244:247]\n" \
"ds_read_b64_tr_b16 v[156:157], v33 offset:0\n" \
"ds_read_b64_tr_b16 v[158:159], v33 offset:1024\n" \
"v_mfma_f32_16x16x32_bf16 a[244:247], v[160:163], v[112:115], a[244:247]\n" \
"ds_read_b64_tr_b16 v[160:161], v33 offset:8192\n" \
"ds_read_b64_tr_b16 v[162:163], v33 offset:9216\n" \
"v_mfma_f32_16x16x32_bf16 a[248:251], v[168:171], v[112:115], a[248:251]\n" \
"ds_read_b64_tr_b16 v[168:169], v34 offset:8192\n" \
"ds_read_b64_tr_b16 v[170:171], v34 offset:9216\n" \
"v_mfma_f32_16x16x32_bf16 a[248:251], v[164:167], v[108:111], a[248:251]\n" \
"ds_read_b64_tr_b16 v[164:165], v34 offset:0\n" \
"ds_read_b64_tr_b16 v[166:167], v34 offset:1024\n" \
"v_mfma_f32_16x16x32_bf16 a[252:255], v[172:175], v[108:111], a[252:255]\n" \
"ds_read_b64_tr_b16 v[172:173], v35 offset:0\n" \
"ds_read_b64_tr_b16 v[174:175], v35 offset:1024\n" \
"v_mfma_f32_16x16x32_bf16 a[252:255], v[176:179], v[112:115], a[252:255]\n" \
"s_add_u32 m0, %[mA], 0x9800\n" \
"s_waitcnt lgkmcnt(14)\n" \
"s_nop 0\n" \
"v_mfma_f32_16x16x32_bf16 a[16:19], v[144:147], v[88:91], a[16:19]\n" \
"s_mov_b32 s84, 0xdb80\n" \
"ds_read_b64_tr_b16 v[176:177], v35 offset:8192\n" \
"v_mfma_f32_16x16x32_bf16 a[16:19], v[140:143], v[84:87], a[16:19]\n" \
"buffer_load_dwordx4 v24, s[76:79], s84 offen lds\n" \
"v_mfma_f32_16x16x32_bf16 a[48:51], v[124:127], v[84:87], a[48:51]\n" \
"s_add_u32 m0, %[mA], 0x9c00\n" \
"s_mov_b32 s85, 0xff80\n" \
"v_mfma_f32_16x16x32_bf16 a[48:51], v[128:131], v[88:91], a[48:51]\n" \
"ds_read_b64_tr_b16 v[178:179], v35 offset:9216\n" \
"ds_read_b128 v[100:103], v48 offset:4096\n" \
"v_mfma_f32_16x16x32_bf16 a[80:83], v[88:91], v[136:139], a[80:83]\n" \
"buffer_load_dwordx4 v25, s[76:79], s85 offen lds\n" \
"v_mfma_f32_16x16x32_bf16 a[80:83], v[84:87], v[132:135], a[80:83]\n" \
"s_add_u32 m0, %[mB], 0x8c00\n" \
"s_mov_b32 s84, 0x398000\n" \
"v_mfma_f32_16x16x32_bf16 a[112:115], v[140:143], v[52:55], a[112:115]\n" \
"ds_read_b128 v[84:87], v48 offset:0\n" \
"ds_read_b128 v[88:91], v49 offset:0\n" \
"buffer_load_dwordx4 v27, s[68:71], s84 offen lds\n" \
"v_mfma_f32_16x16x32_bf16 a[112:115], v[144:147], v[56:59], a[112:115]\n" \
"ds_read_b128 v[104:107], v49 offset:4096\n" \
"ds_read_b128 v[108:111], v48 offset:6144\n" \
"v_mfma_f32_16x16x32_bf16 a[20:23], v[128:131], v[56:59], a[20:23]\n" \
"ds_read_b128 v[112:115], v49 offset:6144\n" \
"v_mfma_f32_16x16x32_bf16 a[20:23], v[124:127], v[52:55], a[20:23]\n" \
"s_add_u32 s89, %[oKB], 0x1000\n" \
"v_mfma_f32_16x16x32_bf16 a[52:55], v[116:119], v[52:55], a[52:55]\n" \
"buffer_store_dwordx4 a[36:39], v20, s[80:83], s89 offen offset:0 sc1\n" \
"v_mfma_f32_16x16x32_bf16 a[52:55], v[120:123], v[56:59], a[52:55]\n" \
"v_mfma_f32_16x16x32_bf16 a[84:87], v[56:59], v[136:139], a[84:87]\n" \
"v_mfma_f32_16x16x32_bf16 a[84:87], v[52:55], v[132:135], a[84:87]\n" \
"v_mfma_f32_16x16x32_bf16 a[116:119], v[140:143], v[60:63], a[116:119]\n" \
"v_mfma_f32_16x16x32_bf16 a[116:119], v[144:147], v[64:67], a[116:119]\n" \
"v_mfma_f32_16x16x32_bf16 a[24:27], v[128:131], v[64:67], a[24:27]\n" \
"v_mfma_f32_16x16x32_bf16 a[24:27], v[124:127], v[60:63], a[24:27]\n" \
"v_mfma_f32_16x16x32_bf16 a[56:59], v[116:119], v[60:63], a[56:59]\n" \
"v_mfma_f32_16x16x32_bf16 a[56:59], v[120:123], v[64:67], a[56:59]\n" \
"s_add_u32 s88, %[oKB], 0x2000\n" \
"v_mfma_f32_16x16x32_bf16 a[88:91], v[64:67], v[136:139], a[88:91]\n" \
"buffer_store_dwordx4 a[64:67], v20, s[80:83], s88 offen offset:0 sc1\n" \
"v_mfma_f32_16x16x32_bf16 a[88:91], v[60:63], v[132:135], a[88:91]\n" \
"v_mfma_f32_16x16x32_bf16 a[120:123], v[140:143], v[68:71], a[120:123]\n" \
"v_mfma_f32_16x16x32_bf16 a[120:123], v[144:147], v[72:75], a[120:123]\n" \
"v_mfma_f32_16x16x32_bf16 a[28:31], v[128:131], v[72:75], a[28:31]\n" \
"v_mfma_f32_16x16x32_bf16 a[28:31], v[124:127], v[68:71], a[28:31]\n" \
"v_mfma_f32_16x16x32_bf16 a[60:63], v[116:119], v[68:71], a[60:63]\n" \
"v_mfma_f32_16x16x32_bf16 a[60:63], v[120:123], v[72:75], a[60:63]\n" \
"v_mfma_f32_16x16x32_bf16 a[92:95], v[72:75], v[136:139], a[92:95]\n" \
"v_mfma_f32_16x16x32_bf16 a[92:95], v[68:71], v[132:135], a[92:95]\n" \
"s_add_u32 s89, %[oKB], 0x3000\n" \
"v_mfma_f32_16x16x32_bf16 a[124:127], v[140:143], v[76:79], a[124:127]\n" \
"buffer_store_dwordx4 a[68:71], v20, s[80:83], s89 offen offset:0 sc1\n" \
"v_mfma_f32_16x16x32_bf16 a[124:127], v[144:147], v[80:83], a[124:127]\n" \
"v_mfma_f32_16x16x32_bf16 a[0:3], v[128:131], v[80:83], a[0:3]\n" \
"v_mfma_f32_16x16x32_bf16 a[0:3], v[124:127], v[76:79], a[0:3]\n" \
"v_mfma_f32_16x16x32_bf16 a[4:7], v[116:119], v[76:79], a[4:7]\n" \
"v_mfma_f32_16x16x32_bf16 a[4:7], v[120:123], v[80:83], a[4:7]\n" \
"v_mfma_f32_16x16x32_bf16 a[32:35], v[80:83], v[136:139], a[32:35]\n" \
"v_mfma_f32_16x16x32_bf16 a[32:35], v[76:79], v[132:135], a[32:35]\n" \
"s_waitcnt lgkmcnt(0)\n" \
"v_mfma_f32_16x16x32_bf16 a[128:131], v[148:151], v[52:55], a[128:131]\n" \
"v_mfma_f32_16x16x32_bf16 a[128:131], v[152:155], v[56:59], a[128:131]\n" \
"v_mfma_f32_16x16x32_bf16 a[132:135], v[160:163], v[56:59], a[132:135]\n" \
"v_mfma_f32_16x16x32_bf16 a[132:135], v[156:159], v[52:55], a[132:135]\n" \
"v_mfma_f32_16x16x32_bf16 a[136:139], v[164:167], v[52:55], a[136:139]\n" \
"v_mfma_f32_16x16x32_bf16 a[136:139], v[168:171], v[56:59], a[136:139]\n" \
"v_mfma_f32_16x16x32_bf16 a[140:143], v[176:179], v[56:59], a[140:143]\n" \
"v_mfma_f32_16x16x32_bf16 a[140:143], v[172:175], v[52:55], a[140:143]\n" \
"v_mfma_f32_16x16x32_bf16 a[160:163], v[148:151], v[60:63], a[160:163]\n" \
"v_mfma_f32_16x16x32_bf16 a[160:163], v[152:155], v[64:67], a[160:163]\n" \
"s_waitcnt vmcnt(3)\n" \
"s_barrier\n" \
"v_mfma_f32_16x16x32_bf16 a[164:167], v[160:163], v[64:67], a[164:167]\n" \
"s_add_u32 m0, %[mL], 0x0\n" \
"s_add_u32 s85, %[oL], 0x10000\n" \
"v_mfma_f32_16x16x32_bf16 a[164:167], v[156:159], v[60:63], a[164:167]\n" \
"buffer_load_dwordx4 v20, s[80:83], s85 offen sc1 lds\n" \
"v_mfma_f32_16x16x32_bf16 a[168:171], v[164:167], v[60:63], a[168:171]\n" \
"ds_read_b64_tr_b16 v[140:141], v39 offset:32768\n" \
"ds_read_b64_tr_b16 v[142:143], v39 offset:33280\n" \
"v_mfma_f32_16x16x32_bf16 a[168:171], v[168:171], v[64:67], a[168:171]\n" \
"s_add_u32 m0, %[mL], 0x1000\n" \
"s_add_u32 s84, %[oL], 0x11000\n" \
"v_mfma_f32_16x16x32_bf16 a[172:175], v[176:179], v[64:67], a[172:175]\n" \
"buffer_load_dwordx4 v20, s[80:83], s84 offen sc1 lds\n" \
"v_mfma_f32_16x16x32_bf16 a[172:175], v[172:175], v[60:63], a[172:175]\n" \
"ds_read_b64_tr_b16 v[144:145], v39 offset:36864\n" \
"ds_read_b64_tr_b16 v[146:147], v39 offset:37376\n" \
"v_mfma_f32_16x16x32_bf16 a[192:195], v[148:151], v[68:71], a[192:195]\n" \
"ds_read_b64_tr_b16 v[128:129], v37 offset:36864\n" \
"ds_read_b64_tr_b16 v[130:131], v37 offset:37376\n" \
"v_mfma_f32_16x16x32_bf16 a[192:195], v[152:155], v[72:75], a[192:195]\n" \
"s_add_u32 m0, %[mL], 0x3000\n" \
"s_mov_b32 s85, 0x400\n" \
"v_mfma_f32_16x16x32_bf16 a[196:199], v[160:163], v[72:75], a[196:199]\n" \
"buffer_load_dwordx4 v21, s[76:79], s85 offen lds\n" \
"v_mfma_f32_16x16x32_bf16 a[196:199], v[156:159], v[68:71], a[196:199]\n" \
"ds_read_b64_tr_b16 v[124:125], v37 offset:32768\n" \
"ds_read_b64_tr_b16 v[126:127], v37 offset:33280\n" \
"v_mfma_f32_16x16x32_bf16 a[200:203], v[164:167], v[68:71], a[200:203]\n" \
"ds_read_b64_tr_b16 v[132:133], v38 offset:32768\n" \
"ds_read_b64_tr_b16 v[134:135], v38 offset:33280\n" \
"v_mfma_f32_16x16x32_bf16 a[200:203], v[168:171], v[72:75], a[200:203]\n" \
"s_add_u32 m0, %[mA], 0x0\n" \
"s_nop 0\n" \
"s_mov_b32 s84, 0x400\n" \
"v_mfma_f32_16x16x32_bf16 a[204:207], v[176:179], v[72:75], a[204:207]\n" \
"buffer_load_dwordx4 v24, s[76:79], s84 offen lds\n" \
"v_mfma_f32_16x16x32_bf16 a[204:207], v[172:175], v[68:71], a[204:207]\n" \
"ds_read_b64_tr_b16 v[136:137], v38 offset:36864\n" \
"ds_read_b64_tr_b16 v[138:139], v38 offset:37376\n" \
"v_mfma_f32_16x16x32_bf16 a[224:227], v[148:151], v[76:79], a[224:227]\n" \
"ds_read_b128 v[56:59], v29 offset:32768\n" \
"ds_read_b128 v[52:55], v28 offset:32768\n" \
"v_mfma_f32_16x16x32_bf16 a[224:227], v[152:155], v[80:83], a[224:227]\n" \
"s_add_u32 m0, %[mA], 0x400\n" \
"s_mov_b32 s85, 0x2800\n" \
"v_mfma_f32_16x16x32_bf16 a[228:231], v[160:163], v[80:83], a[228:231]\n" \
"buffer_load_dwordx4 v25, s[76:79], s85 offen lds\n" \
"v_mfma_f32_16x16x32_bf16 a[228:231], v[156:159], v[76:79], a[228:231]\n" \
"ds_read_b64_tr_b16 v[120:121], v36 offset:36864\n" \
"ds_read_b64_tr_b16 v[122:123], v36 offset:37376\n" \
"v_mfma_f32_16x16x32_bf16 a[232:235], v[164:167], v[76:79], a[232:235]\n" \
"ds_read_b64_tr_b16 v[116:117], v36 offset:32768\n" \
"ds_read_b64_tr_b16 v[118:119], v36 offset:33280\n" \
"v_mfma_f32_16x16x32_bf16 a[232:235], v[168:171], v[80:83], a[232:235]\n" \
"s_add_u32 m0, %[mB], 0x0\n" \
"s_nop 0\n" \
"s_mov_b32 s84, 0x400000\n" \
"v_mfma_f32_16x16x32_bf16 a[236:239], v[176:179], v[80:83], a[236:239]\n" \
"buffer_load_dwordx4 v26, s[68:71], s84 offen lds\n" \
"v_mfma_f32_16x16x32_bf16 a[236:239], v[172:175], v[76:79], a[236:239]\n" \
"ds_read_b128 v[64:67], v29 offset:34816\n" \
"ds_read_b128 v[60:63], v28 offset:34816\n" \
"v_mfma_f32_16x16x32_bf16 a[144:147], v[148:151], v[84:87], a[144:147]\n" \
"ds_read_b128 v[72:75], v29 offset:36864\n" \
"ds_read_b128 v[68:71], v28 offset:36864\n" \
"v_mfma_f32_16x16x32_bf16 a[144:147], v[152:155], v[88:91], a[144:147]\n" \
"ds_read_b128 v[80:83], v29 offset:38912\n" \
"ds_read_b128 v[76:79], v28 offset:38912\n" \
"v_mfma_f32_16x16x32_bf16 a[148:151], v[160:163], v[88:91], a[148:151]\n" \
"s_add_u32 m0, %[mA], 0x800\n" \
"s_mov_b32 s85, 0x4c00\n" \
"v_mfma_f32_16x16x32_bf16 a[148:151], v[156:159], v[84:87], a[148:151]\n" \
"buffer_load_dwordx4 v24, s[76:79], s85 offen lds\n" \
"v_mfma_f32_16x16x32_bf16 a[152:155], v[164:167], v[84:87], a[152:155]\n" \
"v_add_u32_e64 v21, %[sQ2], v20\n" \
"v_mfma_f32_16x16x32_bf16 a[152:155], v[168:171], v[88:91], a[152:155]\n" \
"v_mfma_f32_16x16x32_bf16 a[156:159], v[176:179], v[88:91], a[156:159]\n" \
"s_add_u32 m0, %[mA], 0xc00\n" \
"s_mov_b32 s84, 0x7000\n" \
"v_mfma_f32_16x16x32_bf16 a[156:159], v[172:175], v[84:87], a[156:159]\n" \
"buffer_load_dwordx4 v25, s[76:79], s84 offen lds\n" \
"v_mfma_f32_16x16x32_bf16 a[176:179], v[148:151], v[92:95], a[176:179]\n" \
"ds_read_b128 v[84:87], v22 offset:32768\n" \
"ds_read_b128 v[88:91], v23 offset:32768\n" \
"v_mfma_f32_16x16x32_bf16 a[176:179], v[152:155], v[96:99], a[176:179]\n" \
"v_mfma_f32_16x16x32_bf16 a[180:183], v[160:163], v[96:99], a[180:183]\n" \
"s_add_u32 m0, %[mB], 0x400\n" \
"s_mov_b32 s85, 0x408000\n" \
"v_mfma_f32_16x16x32_bf16 a[180:183], v[156:159], v[92:95], a[180:183]\n" \
"buffer_load_dwordx4 v26, s[68:71], s85 offen lds\n" \
"v_mfma_f32_16x16x32_bf16 a[184:187], v[164:167], v[92:95], a[184:187]\n" \
"v_and_b32_e32 v26, 0x1ff, v20\n" \
"v_mfma_f32_16x16x32_bf16 a[184:187], v[168:171], v[96:99], a[184:187]\n" \
"v_mfma_f32_16x16x32_bf16 a[188:191], v[176:179], v[96:99], a[188:191]\n" \
"s_add_u32 m0, %[mA], 0x1000\n" \
"s_mov_b32 s84, 0x9400\n" \
"v_mfma_f32_16x16x32_bf16 a[188:191], v[172:175], v[92:95], a[188:191]\n" \
"buffer_load_dwordx4 v24, s[76:79], s84 offen lds\n" \
"v_mfma_f32_16x16x32_bf16 a[208:211], v[148:151], v[100:103], a[208:211]\n" \
"ds_read_b128 v[92:95], v48 offset:34816\n" \
"ds_read_b128 v[96:99], v49 offset:34816\n" \
"v_mfma_f32_16x16x32_bf16 a[208:211], v[152:155], v[104:107], a[208:211]\n" \
"v_mfma_f32_16x16x32_bf16 a[212:215], v[160:163], v[104:107], a[212:215]\n" \
"s_add_u32 m0, %[mA], 0x1400\n" \
"s_mov_b32 s85, 0xb800\n" \
"v_mfma_f32_16x16x32_bf16 a[212:215], v[156:159], v[100:103], a[212:215]\n" \
"buffer_load_dwordx4 v25, s[76:79], s85 offen lds\n" \
"v_mfma_f32_16x16x32_bf16 a[216:219], v[164:167], v[100:103], a[216:219]\n" \
"v_mfma_f32_16x16x32_bf16 a[216:219], v[168:171], v[104:107], a[216:219]\n" \
"v_mfma_f32_16x16x32_bf16 a[220:223], v[176:179], v[104:107], a[220:223]\n" \
"s_add_u32 m0, %[mB], 0x800\n" \
"s_mov_b32 s84, 0x410000\n" \
"v_mfma_f32_16x16x32_bf16 a[220:223], v[172:175], v[100:103], a[220:223]\n" \
"buffer_load_dwordx4 v27, s[68:71], s84 offen lds\n" \
"v_mfma_f32_16x16x32_bf16 a[240:243], v[148:151], v[108:111], a[240:243]\n" \
"ds_read_b64_tr_b16 v[148:149], v32 offset:32768\n" \
"ds_read_b64_tr_b16 v[150:151], v32 offset:33792\n" \
"v_mfma_f32_16x16x32_bf16 a[240:243], v[152:155], v[112:115], a[240:243]\n" \
"ds_read_b64_tr_b16 v[152:153], v32 offset:40960\n" \
"ds_read_b64_tr_b16 v[154:155], v32 offset:41984\n" \
"v_mfma_f32_16x16x32_bf16 a[244:247], v[160:163], v[112:115], a[244:247]\n" \
"ds_read_b64_tr_b16 v[160:161], v33 offset:40960\n" \
"ds_read_b64_tr_b16 v[162:163], v33 offset:41984\n" \
"v_mfma_f32_16x16x32_bf16 a[244:247], v[156:159], v[108:111], a[244:247]\n" \
"ds_read_b64_tr_b16 v[156:157], v33 offset:32768\n" \
"ds_read_b64_tr_b16 v[158:159], v33 offset:33792\n" \
"v_mfma_f32_16x16x32_bf16 a[248:251], v[164:167], v[108:111], a[248:251]\n" \
"ds_read_b64_tr_b16 v[164:165], v34 offset:32768\n" \
"ds_read_b64_tr_b16 v[166:167], v34 offset:33792\n" \
"v_mfma_f32_16x16x32_bf16 a[248:251], v[168:171], v[112:115], a[248:251]\n" \
"ds_read_b64_tr_b16 v[168:169], v34 offset:40960\n" \
"ds_read_b64_tr_b16 v[170:171], v34 offset:41984\n" \
"v_mfma_f32_16x16x32_bf16 a[252:255], v[176:179], v[112:115], a[252:255]\n" \
"ds_read_b64_tr_b16 v[176:177], v35 offset:40960\n" \
"ds_read_b64_tr_b16 v[178:179], v35 offset:41984\n" \
"v_mfma_f32_16x16x32_bf16 a[252:255], v[172:175], v[108:111], a[252:255]\n" \
"s_add_u32 m0, %[mA], 0x1800\n" \
"s_waitcnt lgkmcnt(14)\n" \
"s_nop 0\n" \
"v_mfma_f32_16x16x32_bf16 a[16:19], v[140:143], v[84:87], a[16:19]\n" \
"s_mov_b32 s84, 0xdc00\n" \
"ds_read_b64_tr_b16 v[172:173], v35 offset:32768\n" \
"v_mfma_f32_16x16x32_bf16 a[16:19], v[144:147], v[88:91], a[16:19]\n" \
"buffer_load_dwordx4 v24, s[76:79], s84 offen lds\n" \
"v_mfma_f32_16x16x32_bf16 a[48:51], v[128:131], v[88:91], a[48:51]\n" \
"s_add_u32 m0, %[mA], 0x1c00\n" \
"s_mov_b32 s85, 0x10000\n" \
"v_mfma_f32_16x16x32_bf16 a[48:51], v[124:127], v[84:87], a[48:51]\n" \
"ds_read_b64_tr_b16 v[174:175], v35 offset:33792\n" \
"ds_read_b128 v[100:103], v48 offset:36864\n" \
"v_mfma_f32_16x16x32_bf16 a[80:83], v[84:87], v[132:135], a[80:83]\n" \
"buffer_load_dwordx4 v25, s[76:79], s85 offen lds\n" \
"s_mov_b32 s76, %[oP2]\n" \
"s_nop 0\n" \
"v_mfma_f32_16x16x32_bf16 a[80:83], v[88:91], v[136:139], a[80:83]\n" \
"s_add_u32 m0, %[mB], 0xc00\n" \
"s_mov_b32 s84, 0x418000\n" \
"v_mfma_f32_16x16x32_bf16 a[112:115], v[144:147], v[56:59], a[112:115]\n" \
"ds_read_b128 v[84:87], v48 offset:32768\n" \
"ds_read_b128 v[88:91], v49 offset:32768\n" \
"buffer_load_dwordx4 v27, s[68:71], s84 offen lds\n" \
"v_mfma_f32_16x16x32_bf16 a[112:115], v[140:143], v[52:55], a[112:115]\n" \
"ds_read_b128 v[104:107], v49 offset:36864\n" \
"ds_read_b128 v[108:111], v48 offset:38912\n" \
"v_mfma_f32_16x16x32_bf16 a[20:23], v[124:127], v[52:55], a[20:23]\n" \
"ds_read_b128 v[112:115], v49 offset:38912\n" \
"v_accvgpr_read_b32 v248, a16\n" \
"v_mfma_f32_16x16x32_bf16 a[20:23], v[128:131], v[56:59], a[20:23]\n" \
"s_add_u32 s88, %[oKB], 0x0\n" \
"s_nop 0\n" \
"v_accvgpr_read_b32 v249, a17\n" \
"v_mfma_f32_16x16x32_bf16 a[52:55], v[120:123], v[56:59], a[52:55]\n" \
"buffer_store_dwordx4 a[96:99], v20, s[80:83], s88 offen offset:1024 sc1\n" \
"v_mfma_f32_16x16x32_bf16 a[52:55], v[116:119], v[52:55], a[52:55]\n" \
"v_accvgpr_read_b32 v250, a18\n" \
"v_accvgpr_read_b32 v251, a19\n" \
"v_mfma_f32_16x16x32_bf16 a[84:87], v[52:55], v[132:135], a[84:87]\n" \
"v_accvgpr_read_b32 v180, a48\n" \
"v_accvgpr_read_b32 v181, a49\n" \
"v_mfma_f32_16x16x32_bf16 a[84:87], v[56:59], v[136:139], a[84:87]\n" \
"v_accvgpr_read_b32 v182, a50\n" \
"v_accvgpr_read_b32 v183, a51\n" \
"v_mfma_f32_16x16x32_bf16 a[116:119], v[144:147], v[64:67], a[116:119]\n" \
"v_accvgpr_read_b32 v252, a80\n" \
"v_accvgpr_read_b32 v253, a81\n" \
"v_mfma_f32_16x16x32_bf16 a[116:119], v[140:143], v[60:63], a[116:119]\n" \
"v_accvgpr_read_b32 v254, a82\n" \
"v_accvgpr_read_b32 v255, a83\n" \
"v_mfma_f32_16x16x32_bf16 a[24:27], v[124:127], v[60:63], a[24:27]\n" \
"v_accvgpr_read_b32 v196, a112\n" \
"v_accvgpr_read_b32 v197, a113\n" \
"v_mfma_f32_16x16x32_bf16 a[24:27], v[128:131], v[64:67], a[24:27]\n" \
"v_accvgpr_read_b32 v198, a114\n" \
"v_accvgpr_read_b32 v199, a115\n" \
"v_mfma_f32_16x16x32_bf16 a[56:59], v[120:123], v[64:67], a[56:59]\n" \
"v_accvgpr_read_b32 v188, a20\n" \
"v_accvgpr_read_b32 v189, a21\n" \
"v_mfma_f32_16x16x32_bf16 a[56:59], v[116:119], v[60:63], a[56:59]\n" \
"s_add_u32 s89, %[oKB], 0x1000\n" \
"v_accvgpr_read_b32 v190, a22\n" \
"v_mfma_f32_16x16x32_bf16 a[88:91], v[60:63], v[132:135], a[88:91]\n" \
"L_pf7:\n" \
"s_cbranch_execz L_pf11\n" \
"s_nop 0\n" \
"buffer_store_dwordx4 a[100:103], v20, s[80:83], s89 offen offset:1024 sc1\n" \
"v_mfma_f32_16x16x32_bf16 a[88:91], v[64:67], v[136:139], a[88:91]\n" \
"v_accvgpr_read_b32 v191, a23\n" \
"v_accvgpr_read_b32 v184, a52\n" \
"v_mfma_f32_16x16x32_bf16 a[120:123], v[144:147], v[72:75], a[120:123]\n" \
"v_accvgpr_read_b32 v185, a53\n" \
"v_accvgpr_read_b32 v186, a54\n" \
"v_mfma_f32_16x16x32_bf16 a[120:123], v[140:143], v[68:71], a[120:123]\n" \
"v_accvgpr_read_b32 v187, a55\n" \
"v_accvgpr_read_b32 v192, a84\n" \
"v_mfma_f32_16x16x32_bf16 a[28:31], v[124:127], v[68:71], a[28:31]\n" \
"v_accvgpr_read_b32 v193, a85\n" \
"v_accvgpr_read_b32 v194, a86\n" \
"v_mfma_f32_16x16x32_bf16 a[28:31], v[128:131], v[72:75], a[28:31]\n" \
"v_accvgpr_read_b32 v195, a87\n" \
"v_accvgpr_read_b32 v212, a116\n" \
"v_mfma_f32_16x16x32_bf16 a[60:63], v[120:123], v[72:75], a[60:63]\n" \
"v_accvgpr_read_b32 v213, a117\n" \
"v_accvgpr_read_b32 v214, a118\n" \
"v_mfma_f32_16x16x32_bf16 a[60:63], v[116:119], v[68:71], a[60:63]\n" \
"v_accvgpr_read_b32 v215, a119\n" \
"v_accvgpr_read_b32 v204, a24\n" \
"v_mfma_f32_16x16x32_bf16 a[92:95], v[68:71], v[132:135], a[92:95]\n" \
"v_accvgpr_read_b32 v205, a25\n" \
"v_accvgpr_read_b32 v206, a26\n" \
"v_mfma_f32_16x16x32_bf16 a[92:95], v[72:75], v[136:139], a[92:95]\n" \
"s_add_u32 s88, %[oKB], 0x2000\n" \
"v_accvgpr_read_b32 v207, a27\n" \
"v_mfma_f32_16x16x32_bf16 a[124:127], v[144:147], v[80:83], a[124:127]\n" \
"buffer_store_dwordx4 a[8:11], v20, s[80:83], s88 offen offset:1024 sc1\n" \
"v_mfma_f32_16x16x32_bf16 a[124:127], v[140:143], v[76:79], a[124:127]\n" \
"v_accvgpr_read_b32 v200, a56\n" \
"v_accvgpr_read_b32 v201, a57\n" \
"v_mfma_f32_16x16x32_bf16 a[0:3], v[124:127], v[76:79], a[0:3]\n" \
"v_accvgpr_read_b32 v202, a58\n" \
"v_accvgpr_read_b32 v203, a59\n" \
"v_mfma_f32_16x16x32_bf16 a[0:3], v[128:131], v[80:83], a[0:3]\n" \
"v_accvgpr_read_b32 v208, a88\n" \
"v_accvgpr_read_b32 v209, a89\n" \
"v_mfma_f32_16x16x32_bf16 a[4:7], v[120:123], v[80:83], a[4:7]\n" \
"v_accvgpr_read_b32 v210, a90\n" \
"v_accvgpr_read_b32 v211, a91\n" \
"v_mfma_f32_16x16x32_bf16 a[4:7], v[116:119], v[76:79], a[4:7]\n" \
"v_accvgpr_read_b32 v228, a120\n" \
"v_accvgpr_read_b32 v229, a121\n" \
"v_mfma_f32_16x16x32_bf16 a[32:35], v[76:79], v[132:135], a[32:35]\n" \
"v_accvgpr_read_b32 v230, a122\n" \
"v_accvgpr_read_b32 v231, a123\n" \
"v_mfma_f32_16x16x32_bf16 a[32:35], v[80:83], v[136:139], a[32:35]\n" \
"v_accvgpr_read_b32 v220, a28\n" \
"s_waitcnt lgkmcnt(0)\n" \
"v_mfma_f32_16x16x32_bf16 a[128:131], v[152:155], v[56:59], a[128:131]\n" \
"v_accvgpr_read_b32 v221, a29\n" \
"v_accvgpr_read_b32 v222, a30\n" \
"v_mfma_f32_16x16x32_bf16 a[128:131], v[148:151], v[52:55], a[128:131]\n" \
"v_lshlrev_b32_e32 v45, 10, v20\n" \
"v_add_u32_e64 v45, %[wl], v45\n" \
"v_mfma_f32_16x16x32_bf16 a[132:135], v[156:159], v[52:55], a[132:135]\n" \
"s_mov_b32 m0, %[mS]\n" \
"s_nop 0\n" \
"v_accvgpr_read_b32 v223, a31\n" \
"v_mfma_f32_16x16x32_bf16 a[132:135], v[160:163], v[56:59], a[132:135]\n" \
"v_accvgpr_read_b32 v216, a60\n" \
"v_accvgpr_read_b32 v217, a61\n" \
"v_mfma_f32_16x16x32_bf16 a[136:139], v[168:171], v[56:59], a[136:139]\n" \
"v_accvgpr_read_b32 v218, a62\n" \
"v_accvgpr_read_b32 v219, a63\n" \
"v_mfma_f32_16x16x32_bf16 a[136:139], v[164:167], v[52:55], a[136:139]\n" \
"v_accvgpr_read_b32 v224, a92\n" \
"v_accvgpr_read_b32 v225, a93\n" \
"v_mfma_f32_16x16x32_bf16 a[140:143], v[172:175], v[52:55], a[140:143]\n" \
"v_accvgpr_read_b32 v226, a94\n" \
"v_accvgpr_read_b32 v227, a95\n" \
"s_getpc_b64 s[86:87]\n" \
"L_ct15_%=:\n" \
"s_add_u32 s86, s86, L_end_%=-L_ct15_%=\n" \
"s_addc_u32 s87, s87, 0\n" \
"s_add_u32 s74, %[oP], 0x4000\n" \
"s_add_u32 s75, %[oP], 0x8000\n" \
"s_add_u32 s88, %[oVB], 0x0\n" \
"v_mfma_f32_16x16x32_bf16 a[140:143], v[176:179], v[56:59], a[140:143]\n" \
"v_accvgpr_read_b32 v244, a124\n" \
"v_accvgpr_read_b32 v245, a125\n" \
"s_mov_b64 exec, 3\n" \
"global_load_lds_dword v45, s[86:87]\n" \
"s_mov_b64 exec, -1\n" \
"s_sub_u32 m0, %[mB], 0x10000\n" \
"v_mfma_f32_16x16x32_bf16 a[160:163], v[152:155], v[64:67], a[160:163]\n" \
"v_accvgpr_read_b32 v246, a126\n" \
"v_accvgpr_read_b32 v247, a127\n" \
"v_mfma_f32_16x16x32_bf16 a[160:163], v[148:151], v[60:63], a[160:163]\n" \
"v_accvgpr_read_b32 v236, a0\n" \
"v_accvgpr_read_b32 v237, a1\n" \
"s_waitcnt vmcnt(4)\n" \
"s_barrier\n" \
"s_nop 0\n" \
"v_mfma_f32_16x16x32_bf16 a[164:167], v[156:159], v[60:63], a[164:167]\n" \
"ds_read_b64_tr_b16 v[144:145], v39 offset:4096\n" \
"ds_read_b64_tr_b16 v[146:147], v39 offset:4608\n" \
"v_mfma_f32_16x16x32_bf16 a[164:167], v[160:163], v[64:67], a[164:167]\n" \
"ds_read_b64_tr_b16 v[140:141], v39 offset:0\n" \
"ds_read_b64_tr_b16 v[142:143], v39 offset:512\n" \
"v_mfma_f32_16x16x32_bf16 a[168:171], v[168:171], v[64:67], a[168:171]\n" \
"ds_read_b64_tr_b16 v[124:125], v37 offset:0\n" \
"ds_read_b64_tr_b16 v[126:127], v37 offset:512\n" \
"v_mfma_f32_16x16x32_bf16 a[168:171], v[164:167], v[60:63], a[168:171]\n" \
"ds_read_b64_tr_b16 v[128:129], v37 offset:4096\n" \
"ds_read_b64_tr_b16 v[130:131], v37 offset:4608\n" \
"v_mfma_f32_16x16x32_bf16 a[172:175], v[172:175], v[60:63], a[172:175]\n" \
"ds_read_b64_tr_b16 v[136:137], v38 offset:4096\n" \
"ds_read_b64_tr_b16 v[138:139], v38 offset:4608\n" \
"v_mfma_f32_16x16x32_bf16 a[172:175], v[176:179], v[64:67], a[172:175]\n" \
"ds_read_b64_tr_b16 v[132:133], v38 offset:0\n" \
"ds_read_b64_tr_b16 v[134:135], v38 offset:512\n" \
"v_mfma_f32_16x16x32_bf16 a[192:195], v[152:155], v[72:75], a[192:195]\n" \
"ds_read_b128 v[52:55], v28 offset:0\n" \
"ds_read_b128 v[56:59], v29 offset:0\n" \
"v_mfma_f32_16x16x32_bf16 a[192:195], v[148:151], v[68:71], a[192:195]\n" \
"ds_read_b64_tr_b16 v[116:117], v36 offset:0\n" \
"ds_read_b64_tr_b16 v[118:119], v36 offset:512\n" \
"v_mfma_f32_16x16x32_bf16 a[196:199], v[156:159], v[68:71], a[196:199]\n" \
"ds_read_b64_tr_b16 v[120:121], v36 offset:4096\n" \
"ds_read_b64_tr_b16 v[122:123], v36 offset:4608\n" \
"v_mfma_f32_16x16x32_bf16 a[196:199], v[160:163], v[72:75], a[196:199]\n" \
"ds_read_b128 v[60:63], v28 offset:2048\n" \
"ds_read_b128 v[64:67], v29 offset:2048\n" \
"v_mfma_f32_16x16x32_bf16 a[200:203], v[168:171], v[72:75], a[200:203]\n" \
"v_accvgpr_read_b32 v238, a2\n" \
"v_accvgpr_read_b32 v239, a3\n" \
"v_mfma_f32_16x16x32_bf16 a[200:203], v[164:167], v[68:71], a[200:203]\n" \
"v_accvgpr_read_b32 v232, a4\n" \
"v_accvgpr_read_b32 v233, a5\n" \
"v_mfma_f32_16x16x32_bf16 a[204:207], v[172:175], v[68:71], a[204:207]\n" \
"ds_read_b128 v[68:71], v28 offset:4096\n" \
"v_accvgpr_read_b32 v234, a6\n" \
"v_mfma_f32_16x16x32_bf16 a[204:207], v[176:179], v[72:75], a[204:207]\n" \
"ds_read_b128 v[72:75], v29 offset:4096\n" \
"v_accvgpr_read_b32 v235, a7\n" \
"v_mfma_f32_16x16x32_bf16 a[224:227], v[152:155], v[80:83], a[224:227]\n" \
"v_accvgpr_read_b32 v240, a32\n" \
"v_accvgpr_read_b32 v241, a33\n" \
"v_mfma_f32_16x16x32_bf16 a[224:227], v[148:151], v[76:79], a[224:227]\n" \
"v_accvgpr_read_b32 v242, a34\n" \
"v_accvgpr_read_b32 v243, a35\n" \
"v_mfma_f32_16x16x32_bf16 a[228:231], v[156:159], v[76:79], a[228:231]\n" \
"s_mov_b32 s68, 0xffc0fffc\n" \
"s_mov_b32 s69, 0xc000fc00\n" \
"v_mfma_f32_16x16x32_bf16 a[228:231], v[160:163], v[80:83], a[228:231]\n" \
"v_lshrrev_b32_e32 v25, 8, v20\n" \
"v_lshrrev_b32_e32 v24, 9, v20\n" \
"v_mfma_f32_16x16x32_bf16 a[232:235], v[168:171], v[80:83], a[232:235]\n" \
"v_mul_u32_u24_e32 v25, 0x24700, v25\n" \
"v_lshl_add_u32 v24, v24, 3, v26\n" \
"v_mfma_f32_16x16x32_bf16 a[232:235], v[164:167], v[76:79], a[232:235]\n" \
"v_add_u32_e32 v25, v25, v20\n" \
"s_add_u32 s84, %[sOA], 0x0\n" \
"v_mfma_f32_16x16x32_bf16 a[236:239], v[172:175], v[76:79], a[236:239]\n" \
"ds_read_b128 v[76:79], v28 offset:6144\n" \
"s_add_u32 s85, %[sOA], 0x24800\n" \
"v_mfma_f32_16x16x32_bf16 a[236:239], v[176:179], v[80:83], a[236:239]\n" \
"ds_read_b128 v[80:83], v29 offset:6144\n" \
"s_add_u32 s86, %[sOA], 0x49000\n" \
"v_mfma_f32_16x16x32_bf16 a[144:147], v[152:155], v[88:91], a[144:147]\n" \
"s_add_u32 s87, %[sOA], 0x6d800\n" \
"s_mov_b32 s70, 0x3e0293ee\n" \
"v_mfma_f32_16x16x32_bf16 a[144:147], v[148:151], v[84:87], a[144:147]\n" \
"v_add_u32_e64 v27, %[sQ], v20\n" \
"s_mov_b32 s71, 0x3e0293ee\n" \
"v_mfma_f32_16x16x32_bf16 a[148:151], v[156:159], v[84:87], a[148:151]\n" \
"v_lshrrev_b32_e32 v26, 2, v20\n" \
"v_add_u32_e32 v26, %[sG], v26\n" \
"v_mfma_f32_16x16x32_bf16 a[148:151], v[160:163], v[88:91], a[148:151]\n" \
"v_mfma_f32_16x16x32_bf16 a[152:155], v[168:171], v[88:91], a[152:155]\n" \
"v_mfma_f32_16x16x32_bf16 a[152:155], v[164:167], v[84:87], a[152:155]\n" \
"v_mfma_f32_16x16x32_bf16 a[156:159], v[172:175], v[84:87], a[156:159]\n" \
"ds_read_b128 v[84:87], v22 offset:0\n" \
"v_mfma_f32_16x16x32_bf16 a[156:159], v[176:179], v[88:91], a[156:159]\n" \
"ds_read_b128 v[88:91], v23 offset:0\n" \
"v_mfma_f32_16x16x32_bf16 a[176:179], v[152:155], v[96:99], a[176:179]\n" \
"v_mfma_f32_16x16x32_bf16 a[176:179], v[148:151], v[92:95], a[176:179]\n" \
"v_mfma_f32_16x16x32_bf16 a[180:183], v[156:159], v[92:95], a[180:183]\n" \
"v_mfma_f32_16x16x32_bf16 a[180:183], v[160:163], v[96:99], a[180:183]\n" \
"v_mfma_f32_16x16x32_bf16 a[184:187], v[168:171], v[96:99], a[184:187]\n" \
"v_mfma_f32_16x16x32_bf16 a[184:187], v[164:167], v[92:95], a[184:187]\n" \
"v_mfma_f32_16x16x32_bf16 a[188:191], v[172:175], v[92:95], a[188:191]\n" \
"ds_read_b128 v[92:95], v48 offset:2048\n" \
"v_mfma_f32_16x16x32_bf16 a[188:191], v[176:179], v[96:99], a[188:191]\n" \
"ds_read_b128 v[96:99], v49 offset:2048\n" \
"v_mfma_f32_16x16x32_bf16 a[208:211], v[152:155], v[104:107], a[208:211]\n" \
"v_mfma_f32_16x16x32_bf16 a[208:211], v[148:151], v[100:103], a[208:211]\n" \
"v_mfma_f32_16x16x32_bf16 a[212:215], v[156:159], v[100:103], a[212:215]\n" \
"v_mfma_f32_16x16x32_bf16 a[212:215], v[160:163], v[104:107], a[212:215]\n" \
"v_mfma_f32_16x16x32_bf16 a[216:219], v[168:171], v[104:107], a[216:219]\n" \
"v_mfma_f32_16x16x32_bf16 a[216:219], v[164:167], v[100:103], a[216:219]\n" \
"v_mfma_f32_16x16x32_bf16 a[220:223], v[172:175], v[100:103], a[220:223]\n" \
"ds_read_b128 v[100:103], v48 offset:4096\n" \
"v_mfma_f32_16x16x32_bf16 a[220:223], v[176:179], v[104:107], a[220:223]\n" \
"ds_read_b128 v[104:107], v49 offset:4096\n" \
"v_mfma_f32_16x16x32_bf16 a[240:243], v[152:155], v[112:115], a[240:243]\n" \
"ds_read_b64_tr_b16 v[152:153], v32 offset:8192\n" \
"ds_read_b64_tr_b16 v[154:155], v32 offset:9216\n" \
"v_mfma_f32_16x16x32_bf16 a[240:243], v[148:151], v[108:111], a[240:243]\n" \
"ds_read_b64_tr_b16 v[148:149], v32 offset:0\n" \
"ds_read_b64_tr_b16 v[150:151], v32 offset:1024\n" \
"v_mfma_f32_16x16x32_bf16 a[244:247], v[156:159], v[108:111], a[244:247]\n" \
"ds_read_b64_tr_b16 v[156:157], v33 offset:0\n" \
"ds_read_b64_tr_b16 v[158:159], v33 offset:1024\n" \
"v_mfma_f32_16x16x32_bf16 a[244:247], v[160:163], v[112:115], a[244:247]\n" \
"ds_read_b64_tr_b16 v[160:161], v33 offset:8192\n" \
"ds_read_b64_tr_b16 v[162:163], v33 offset:9216\n" \
"v_mfma_f32_16x16x32_bf16 a[248:251], v[168:171], v[112:115], a[248:251]\n" \
"ds_read_b64_tr_b16 v[168:169], v34 offset:8192\n" \
"ds_read_b64_tr_b16 v[170:171], v34 offset:9216\n" \
"v_mfma_f32_16x16x32_bf16 a[248:251], v[164:167], v[108:111], a[248:251]\n" \
"ds_read_b64_tr_b16 v[164:165], v34 offset:0\n" \
"ds_read_b64_tr_b16 v[166:167], v34 offset:1024\n" \
"v_mfma_f32_16x16x32_bf16 a[252:255], v[172:175], v[108:111], a[252:255]\n" \
"ds_read_b64_tr_b16 v[172:173], v35 offset:0\n" \
"ds_read_b64_tr_b16 v[174:175], v35 offset:1024\n" \
"v_mfma_f32_16x16x32_bf16 a[252:255], v[176:179], v[112:115], a[252:255]\n" \
"ds_read_b64_tr_b16 v[176:177], v35 offset:8192\n" \
"s_waitcnt lgkmcnt(15)\n" \
"s_nop 0\n" \
"v_mfma_f32_16x16x32_bf16 v[248:251], v[144:147], v[88:91], v[248:251]\n" \
"v_bfe_u32 v44, v20, 2, 5\n" \
"ds_read_b64_tr_b16 v[178:179], v35 offset:9216\n" \
"v_mfma_f32_16x16x32_bf16 v[248:251], v[140:143], v[84:87], v[248:251]\n" \
"buffer_load_dword v44, v44, s[80:83], %[oFp] offen offset:0 sc1\n" \
"v_mfma_f32_16x16x32_bf16 v[180:183], v[124:127], v[84:87], v[180:183]\n" \
"ds_read_b128 v[108:111], v48 offset:6144\n" \
"ds_read_b128 v[112:115], v49 offset:6144\n" \
"v_mfma_f32_16x16x32_bf16 v[180:183], v[128:131], v[88:91], v[180:183]\n" \
"v_mfma_f32_16x16x32_bf16 v[252:255], v[88:91], v[136:139], v[252:255]\n" \
"ds_read_b128 v[88:91], v49 offset:0\n" \
"v_mfma_f32_16x16x32_bf16 v[252:255], v[84:87], v[132:135], v[252:255]\n" \
"ds_read_b128 v[84:87], v48 offset:0\n" \
"v_mfma_f32_16x16x32_bf16 v[196:199], v[140:143], v[52:55], v[196:199]\n" \
"v_mfma_f32_16x16x32_bf16 v[196:199], v[144:147], v[56:59], v[196:199]\n" \
"v_mfma_f32_16x16x32_bf16 v[188:191], v[128:131], v[56:59], v[188:191]\n" \
"v_mfma_f32_16x16x32_bf16 v[188:191], v[124:127], v[52:55], v[188:191]\n" \
"v_cvt_pk_bf16_f32 v180, v180, v181\n" \
"v_cvt_pk_bf16_f32 v181, v182, v183\n" \
"v_mfma_f32_16x16x32_bf16 v[184:187], v[116:119], v[52:55], v[184:187]\n" \
"v_cvt_pk_bf16_f32 v182, v250, v251\n" \
"v_mov_b32_e32 v183, 0x3f80\n" \
"v_mfma_f32_16x16x32_bf16 v[184:187], v[120:123], v[56:59], v[184:187]\n" \
"v_mfma_f32_16x16x32_bf16 v[192:195], v[56:59], v[136:139], v[192:195]\n" \
"v_mfma_f32_16x16x16_bf16 v[248:251], v[182:183], v[46:47], 0\n" \
"v_cvt_pk_bf16_f32 v188, v188, v189\n" \
"v_cvt_pk_bf16_f32 v189, v190, v191\n" \
"v_mfma_f32_16x16x32_bf16 v[192:195], v[52:55], v[132:135], v[192:195]\n" \
"v_cvt_pk_bf16_f32 v190, v198, v199\n" \
"v_mov_b32_e32 v191, 0x3f80\n" \
"v_mfma_f32_16x16x32_bf16 v[212:215], v[140:143], v[60:63], v[212:215]\n" \
"v_cvt_pk_bf16_f32 v184, v184, v185\n" \
"v_cvt_pk_bf16_f32 v185, v186, v187\n" \
"v_mfma_f32_16x16x32_bf16 v[212:215], v[144:147], v[64:67], v[212:215]\n" \
"v_cvt_pk_bf16_f32 v186, v196, v197\n" \
"v_mov_b32_e64 v187, 0\n" \
"v_mfma_f32_16x16x32_bf16 v[204:207], v[128:131], v[64:67], v[204:207]\n" \
"v_mfma_f32_16x16x32_bf16 v[204:207], v[124:127], v[60:63], v[204:207]\n" \
"v_mfma_f32_16x16x32_bf16 v[200:203], v[116:119], v[60:63], v[200:203]\n" \
"v_mfma_f32_16x16x32_bf16 v[200:203], v[120:123], v[64:67], v[200:203]\n" \
"v_mfma_f32_16x16x32_bf16 v[208:211], v[64:67], v[136:139], v[208:211]\n" \
"v_mfma_f32_16x16x32_bf16 v[208:211], v[60:63], v[132:135], v[208:211]\n" \
"v_mfma_f32_16x16x32_bf16 v[228:231], v[140:143], v[68:71], v[228:231]\n" \
"v_mfma_f32_16x16x32_bf16 v[228:231], v[144:147], v[72:75], v[228:231]\n" \
"v_mfma_f32_16x16x32_bf16 v[220:223], v[128:131], v[72:75], v[220:223]\n" \
"v_mfma_f32_16x16x32_bf16 v[220:223], v[124:127], v[68:71], v[220:223]\n" \
"v_cvt_pk_bf16_f32 v204, v204, v205\n" \
"v_cvt_pk_bf16_f32 v205, v206, v207\n" \
"v_mfma_f32_16x16x32_bf16 v[216:219], v[116:119], v[68:71], v[216:219]\n" \
"v_cvt_pk_bf16_f32 v200, v200, v201\n" \
"v_cvt_pk_bf16_f32 v201, v202, v203\n" \
"v_mfma_f32_16x16x32_bf16 v[216:219], v[120:123], v[72:75], v[216:219]\n" \
"v_cvt_pk_bf16_f32 v206, v214, v215\n" \
"v_mov_b32_e32 v207, 0x3f80\n" \
"v_mfma_f32_16x16x32_bf16 v[224:227], v[72:75], v[136:139], v[224:227]\n" \
"v_cvt_pk_bf16_f32 v220, v220, v221\n" \
"v_cvt_pk_bf16_f32 v221, v222, v223\n" \
"v_mfma_f32_16x16x32_bf16 v[224:227], v[68:71], v[132:135], v[224:227]\n" \
"v_cvt_pk_bf16_f32 v202, v212, v213\n" \
"v_mov_b32_e64 v203, 0\n" \
"v_mfma_f32_16x16x32_bf16 v[244:247], v[140:143], v[76:79], v[244:247]\n" \
"v_cvt_pk_bf16_f32 v216, v216, v217\n" \
"v_cvt_pk_bf16_f32 v217, v218, v219\n" \
"v_mfma_f32_16x16x32_bf16 v[244:247], v[144:147], v[80:83], v[244:247]\n" \
"v_cvt_pk_bf16_f32 v222, v230, v231\n" \
"v_mov_b32_e32 v223, 0x3f80\n" \
"v_mfma_f32_16x16x32_bf16 v[236:239], v[128:131], v[80:83], v[236:239]\n" \
"v_cvt_pk_bf16_f32 v128, v208, v209\n" \
"v_cvt_pk_bf16_f32 v129, v210, v211\n" \
"v_mfma_f32_16x16x32_bf16 v[236:239], v[124:127], v[76:79], v[236:239]\n" \
"v_cvt_pk_bf16_f32 v126, v192, v193\n" \
"v_cvt_pk_bf16_f32 v127, v194, v195\n" \
"v_mfma_f32_16x16x32_bf16 v[232:235], v[116:119], v[76:79], v[232:235]\n" \
"v_cvt_pk_bf16_f32 v218, v228, v229\n" \
"v_mov_b32_e64 v219, 0\n" \
"v_mfma_f32_16x16x32_bf16 v[232:235], v[120:123], v[80:83], v[232:235]\n" \
"v_cvt_pk_bf16_f32 v130, v224, v225\n" \
"v_cvt_pk_bf16_f32 v131, v226, v227\n" \
"v_mfma_f32_16x16x32_bf16 v[240:243], v[80:83], v[136:139], v[240:243]\n" \
"v_cvt_pk_bf16_f32 v236, v236, v237\n" \
"v_cvt_pk_bf16_f32 v237, v238, v239\n" \
"v_mfma_f32_16x16x32_bf16 v[240:243], v[76:79], v[132:135], v[240:243]\n" \
"v_cvt_pk_bf16_f32 v238, v246, v247\n" \
"v_mov_b32_e32 v239, 0x3f80\n" \
"s_waitcnt lgkmcnt(0)\n" \
"s_barrier\n" \
"v_mfma_f32_16x16x32_bf16 a[128:131], v[148:151], v[52:55], a[128:131]\n" \
"v_cvt_pk_bf16_f32 v232, v232, v233\n" \
"v_cvt_pk_bf16_f32 v233, v234, v235\n" \
"v_mfma_f32_16x16x32_bf16 a[128:131], v[152:155], v[56:59], a[128:131]\n" \
"v_cvt_pk_bf16_f32 v234, v244, v245\n" \
"v_mov_b32_e64 v235, 0\n" \
"v_mfma_f32_16x16x32_bf16 a[132:135], v[160:163], v[56:59], a[132:135]\n" \
"v_cvt_pk_bf16_f32 v132, v240, v241\n" \
"v_cvt_pk_bf16_f32 v133, v242, v243\n" \
"v_mfma_f32_16x16x32_bf16 a[132:135], v[156:159], v[52:55], a[132:135]\n" \
"v_mfma_f32_16x16x32_bf16 v[116:119], v[180:183], v[184:187], 0\n" \
"v_mfma_f32_16x16x32_bf16 v[192:195], v[188:191], v[184:187], 0\n" \
"s_waitcnt vmcnt(0)\n" \
"v_cmp_ne_u32_e32 vcc, %[tag], v44\n" \
"v_mfma_f32_16x16x32_bf16 a[136:139], v[164:167], v[52:55], a[136:139]\n" \
"v_mfma_f32_16x16x32_bf16 v[208:211], v[204:207], v[200:203], 0\n" \
"v_mfma_f32_16x16x32_bf16 v[120:123], v[188:191], v[200:203], 0\n" \
"v_mfma_f32_16x16x32_bf16 v[224:227], v[220:223], v[216:219], 0\n" \
"v_mfma_f32_16x16x32_bf16 v[36:39], v[204:207], v[216:219], 0\n" \
"s_cbranch_vccnz L_pa_%=\n" \
"s_nop 0\n" \
"L_ra_%=:\n" \
"s_add_u32 s89, %[oKB], 0x3000\n" \
"buffer_load_dwordx4 v20, s[80:83], s73 offen offset:0 sc1 lds\n" \
"v_mfma_f32_16x16x32_bf16 a[136:139], v[168:171], v[56:59], a[136:139]\n" \
"v_mfma_f32_16x16x32_bf16 v[240:243], v[236:239], v[232:235], 0\n" \
"v_mfma_f32_16x16x32_bf16 v[48:51], v[220:223], v[232:235], 0\n" \
"v_cndmask_b32_e64 v116, v116, v117, s[64:65]\n" \
"v_cndmask_b32_e64 v118, v118, v119, s[64:65]\n" \
"v_mfma_f32_16x16x32_bf16 a[140:143], v[176:179], v[56:59], a[140:143]\n" \
"buffer_load_dwordx4 v20, s[80:83], s73 offen offset:1024 sc1 lds\n" \
"v_mfma_f32_16x16x32_bf16 a[140:143], v[172:175], v[52:55], a[140:143]\n" \
"v_cndmask_b32_e64 v192, v192, v193, s[64:65]\n" \
"v_add_f32_e64 v116, %[sBias], v116\n" \
"v_mfma_f32_16x16x32_bf16 a[160:163], v[148:151], v[60:63], a[160:163]\n" \
"v_cndmask_b32_e64 v194, v194, v195, s[64:65]\n" \
"v_add_f32_e64 v118, %[sBias], v118\n" \
"v_mfma_f32_16x16x32_bf16 a[160:163], v[152:155], v[64:67], a[160:163]\n" \
"v_cndmask_b32_e64 v208, v208, v209, s[64:65]\n" \
"v_cndmask_b32_e64 v120, v120, v121, s[64:65]\n" \
"v_mfma_f32_16x16x32_bf16 a[164:167], v[160:163], v[64:67], a[164:167]\n" \
"buffer_load_dwordx4 v20, s[80:83], s73 offen offset:2048 sc1 lds\n" \
"v_mfma_f32_16x16x32_bf16 a[164:167], v[156:159], v[60:63], a[164:167]\n" \
"v_cndmask_b32_e64 v210, v210, v211, s[64:65]\n" \
"v_cndmask_b32_e64 v122, v122, v123, s[64:65]\n" \
"v_mfma_f32_16x16x32_bf16 a[168:171], v[164:167], v[60:63], a[168:171]\n" \
"v_cndmask_b32_e64 v224, v224, v225, s[64:65]\n" \
"v_cndmask_b32_e64 v36, v36, v37, s[64:65]\n" \
"v_mfma_f32_16x16x32_bf16 a[168:171], v[168:171], v[64:67], a[168:171]\n" \
"v_cndmask_b32_e64 v226, v226, v227, s[64:65]\n" \
"v_cndmask_b32_e64 v38, v38, v39, s[64:65]\n" \
"v_mfma_f32_16x16x32_bf16 a[172:175], v[176:179], v[64:67], a[172:175]\n" \
"buffer_load_dwordx4 v20, s[80:83], s73 offen offset:3072 sc1 lds\n" \
"s_sub_u32 m0, %[mB], %[mV]\n" \
"s_nop 0\n" \
"v_mfma_f32_16x16x32_bf16 a[172:175], v[172:175], v[60:63], a[172:175]\n" \
"v_cndmask_b32_e64 v240, v240, v241, s[64:65]\n" \
"v_cndmask_b32_e64 v48, v48, v49, s[64:65]\n" \
"v_mfma_f32_16x16x32_bf16 a[192:195], v[148:151], v[68:71], a[192:195]\n" \
"v_cndmask_b32_e64 v242, v242, v243, s[64:65]\n" \
"v_cndmask_b32_e64 v50, v50, v51, s[64:65]\n" \
"v_mfma_f32_16x16x32_bf16 a[192:195], v[152:155], v[72:75], a[192:195]\n" \
"v_cndmask_b32_e64 v192, v116, v192, s[66:67]\n" \
"v_cndmask_b32_e64 v194, v118, v194, s[68:69]\n" \
"buffer_load_dwordx4 v20, s[80:83], s74 offen offset:0 sc1 lds\n" \
"v_mfma_f32_16x16x32_bf16 a[196:199], v[160:163], v[72:75], a[196:199]\n" \
"v_cndmask_b32_e64 v208, v120, v208, s[66:67]\n" \
"v_cndmask_b32_e64 v210, v122, v210, s[68:69]\n" \
"v_mfma_f32_16x16x32_bf16 a[196:199], v[156:159], v[68:71], a[196:199]\n" \
"v_cndmask_b32_e64 v224, v36, v224, s[66:67]\n" \
"v_cndmask_b32_e64 v226, v38, v226, s[68:69]\n" \
"v_mfma_f32_16x16x32_bf16 a[200:203], v[164:167], v[68:71], a[200:203]\n" \
"v_cndmask_b32_e64 v240, v48, v240, s[66:67]\n" \
"v_cndmask_b32_e64 v242, v50, v242, s[68:69]\n" \
"v_mfma_f32_16x16x32_bf16 a[200:203], v[168:171], v[72:75], a[200:203]\n" \
"buffer_load_dwordx4 v20, s[80:83], s74 offen offset:1024 sc1 lds\n" \
"v_mfma_f32_16x16x32_bf16 a[204:207], v[176:179], v[72:75], a[204:207]\n" \
"v_max_f32_e32 v193, v192, v194\n" \
"v_max_f32_e32 v209, v208, v210\n" \
"v_mfma_f32_16x16x32_bf16 a[204:207], v[172:175], v[68:71], a[204:207]\n" \
"v_max_f32_e32 v225, v224, v226\n" \
"v_max_f32_e32 v241, v240, v242\n" \
"v_mfma_f32_16x16x32_bf16 a[224:227], v[148:151], v[76:79], a[224:227]\n" \
"v_permlane32_swap_b32_e32 v193, v225\n" \
"v_permlane32_swap_b32_e32 v209, v241\n" \
"v_mfma_f32_16x16x32_bf16 a[224:227], v[152:155], v[80:83], a[224:227]\n" \
"buffer_load_dwordx4 v20, s[80:83], s74 offen offset:2048 sc1 lds\n" \
"v_mfma_f32_16x16x32_bf16 a[228:231], v[160:163], v[80:83], a[228:231]\n" \
"v_permlane32_swap_b32_e32 v196, v228\n" \
"v_permlane32_swap_b32_e32 v212, v244\n" \
"v_mfma_f32_16x16x32_bf16 a[228:231], v[156:159], v[76:79], a[228:231]\n" \
"v_max_f32_e32 v193, v193, v225\n" \
"v_max_f32_e32 v209, v209, v241\n" \
"v_mfma_f32_16x16x32_bf16 a[232:235], v[164:167], v[76:79], a[232:235]\n" \
"v_permlane16_swap_b32_e32 v228, v244\n" \
"v_permlane16_swap_b32_e32 v193, v209\n" \
"v_mfma_f32_16x16x32_bf16 a[232:235], v[168:171], v[80:83], a[232:235]\n" \
"buffer_load_dwordx4 v20, s[80:83], s74 offen offset:3072 sc1 lds\n" \
"s_sub_u32 m0, %[mB], 0x4000\n" \
"v_mfma_f32_16x16x32_bf16 a[236:239], v[176:179], v[80:83], a[236:239]\n" \
"v_mul_f32_e32 v45, 0xbfb8aa3b, v228\n" \
"v_max_f32_e64 v193, v193, v209\n" \
"v_mfma_f32_16x16x32_bf16 a[236:239], v[172:175], v[76:79], a[236:239]\n" \
"v_mfma_f32_16x16x16_bf16 v[196:199], v[190:191], v[46:47], 0\n" \
"v_mfma_f32_16x16x16_bf16 v[212:215], v[206:207], v[46:47], 0\n" \
"buffer_load_dwordx4 v20, s[80:83], s75 offen offset:0 sc1 lds\n" \
"v_mfma_f32_16x16x32_bf16 a[144:147], v[148:151], v[84:87], a[144:147]\n" \
"v_mfma_f32_16x16x16_bf16 v[228:231], v[222:223], v[46:47], 0\n" \
"v_mul_f32_e32 v193, s72, v193\n" \
"v_mov_b32_e32 v209, v193\n" \
"v_mfma_f32_16x16x16_bf16 v[244:247], v[238:239], v[46:47], 0\n" \
"v_cvt_pk_bf16_f32 v134, v248, v249\n" \
"v_permlane16_swap_b32_e32 v193, v209\n" \
"v_mfma_f32_16x16x32_bf16 a[144:147], v[152:155], v[88:91], a[144:147]\n" \
"buffer_load_dwordx4 v20, s[80:83], s75 offen offset:1024 sc1 lds\n" \
"v_mov_b32_e32 v225, v193\n" \
"v_mov_b32_e64 v241, v209\n" \
"v_mfma_f32_16x16x32_bf16 a[148:151], v[160:163], v[88:91], a[148:151]\n" \
"v_permlane32_swap_b32_e32 v193, v225\n" \
"v_permlane32_swap_b32_e32 v209, v241\n" \
"v_fma_f32 v192, v192, s72, -v193\n" \
"v_fma_f32 v194, v194, s72, -v193\n" \
"v_fma_f32 v208, v208, s72, -v209\n" \
"v_fma_f32 v210, v210, s72, -v209\n" \
"v_exp_f32_e32 v192, v192\n" \
"v_exp_f32_e32 v194, v194\n" \
"buffer_load_dwordx4 v20, s[80:83], s75 offen offset:2048 sc1 lds\n" \
"v_exp_f32_e32 v208, v208\n" \
"v_exp_f32_e32 v210, v210\n" \
"v_fma_f32 v224, v224, s72, -v225\n" \
"v_fma_f32 v226, v226, s72, -v225\n" \
"v_exp_f32_e32 v224, v224\n" \
"v_exp_f32_e32 v226, v226\n" \
"v_fma_f32 v240, v240, s72, -v241\n" \
"v_fma_f32 v242, v242, s72, -v241\n" \
"v_cvt_pk_bf16_f32 v192, v192, v194\n" \
"buffer_load_dwordx4 v20, s[80:83], s75 offen offset:3072 sc1 lds\n" \
"s_mov_b32 m0, %[mK2]\n" \
"v_cvt_pk_bf16_f32 v208, v208, v210\n" \
"v_exp_f32_e32 v240, v240\n" \
"v_exp_f32_e64 v242, v242\n" \
"v_cvt_pk_bf16_f32 v135, v250, v251\n" \
"v_cvt_pk_bf16_f32 v136, v196, v197\n" \
"v_cvt_pk_bf16_f32 v137, v198, v199\n" \
"v_cvt_pk_bf16_f32 v138, v212, v213\n" \
"v_cvt_pk_bf16_f32 v139, v214, v215\n" \
"v_perm_b32 v184, v192, v192, v40\n" \
"v_perm_b32 v185, v192, v192, v41\n" \
"buffer_load_dwordx4 v20, s[80:83], s76 offen offset:0 sc1 lds\n" \
"v_perm_b32 v186, v192, v192, v42\n" \
"v_perm_b32 v187, v192, v192, v43\n" \
"v_perm_b32 v200, v208, v208, v40\n" \
"v_perm_b32 v201, v208, v208, v41\n" \
"v_perm_b32 v202, v208, v208, v42\n" \
"v_perm_b32 v203, v208, v208, v43\n" \
"v_cvt_pk_bf16_f32 v224, v224, v226\n" \
"v_cvt_pk_bf16_f32 v140, v228, v229\n" \
"v_cvt_pk_bf16_f32 v141, v230, v231\n" \
"v_mfma_f32_16x16x32_bf16 v[116:119], v[134:137], v[184:187], 0\n" \
"v_mfma_f32_16x16x32_bf16 v[120:123], v[136:139], v[200:203], 0\n" \
"buffer_load_dwordx4 v20, s[80:83], s76 offen offset:1024 sc1 lds\n" \
"v_perm_b32 v216, v224, v224, v40\n" \
"v_perm_b32 v217, v224, v224, v41\n" \
"v_perm_b32 v218, v224, v224, v42\n" \
"v_perm_b32 v219, v224, v224, v43\n" \
"v_cvt_pk_bf16_f32 v240, v240, v242\n" \
"v_mfma_f32_16x16x32_bf16 a[148:151], v[156:159], v[84:87], a[148:151]\n" \
"v_cvt_pk_bf16_f32 v142, v244, v245\n" \
"v_cvt_pk_bf16_f32 v143, v246, v247\n" \
"v_mfma_f32_16x16x32_bf16 v[36:39], v[138:141], v[216:219], 0\n" \
"v_perm_b32 v232, v240, v240, v40\n" \
"v_perm_b32 v233, v240, v240, v41\n" \
"v_perm_b32 v234, v240, v240, v42\n" \
"v_perm_b32 v235, v240, v240, v43\n" \
"v_mfma_f32_16x16x32_bf16 a[152:155], v[164:167], v[84:87], a[152:155]\n" \
"v_cvt_pk_bf16_f32 v124, v252, v253\n" \
"v_cvt_pk_bf16_f32 v125, v254, v255\n" \
"v_mfma_f32_16x16x32_bf16 v[48:51], v[140:143], v[232:235], 0\n" \
"v_exp_f32_e64 v45, v45\n" \
"v_mfma_f32_16x16x32_bf16 a[152:155], v[168:171], v[88:91], a[152:155]\n" \
"v_mfma_f32_16x16x32_bf16 v[192:195], v[124:127], v[184:187], 0\n" \
"v_mfma_f32_16x16x32_bf16 v[208:211], v[126:129], v[200:203], 0\n" \
"v_mfma_f32_16x16x32_bf16 v[224:227], v[128:131], v[216:219], 0\n" \
"v_mfma_f32_16x16x32_bf16 v[240:243], v[130:133], v[232:235], 0\n" \
"v_mov_b32_dpp v118, v122 quad_perm:[0,1,2,3] row_mask:0x2 bank_mask:0xf\n" \
"v_add_f32_e64 v45, 1.0, v45\n" \
"v_mfma_f32_16x16x32_bf16 a[156:159], v[176:179], v[88:91], a[156:159]\n" \
"v_mov_b32_dpp v118, v38 quad_perm:[0,1,2,3] row_mask:0x4 bank_mask:0xf\n" \
"v_mov_b32_dpp v116, v120 quad_perm:[0,1,2,3] row_mask:0x2 bank_mask:0xf\n" \
"v_rcp_f32_e64 v45, v45\n" \
"v_mov_b32_dpp v118, v50 quad_perm:[0,1,2,3] row_mask:0x8 bank_mask:0xf\n" \
"v_mov_b32_dpp v117, v121 quad_perm:[0,1,2,3] row_mask:0x2 bank_mask:0xf\n" \
"v_rcp_f32_e64 v118, v118\n" \
"v_mov_b32_dpp v116, v36 quad_perm:[0,1,2,3] row_mask:0x4 bank_mask:0xf\n" \
"v_mov_b32_dpp v117, v37 quad_perm:[0,1,2,3] row_mask:0x4 bank_mask:0xf\n" \
"v_mul_f32_e64 v118, v45, v118\n" \
"v_mov_b32_dpp v116, v48 quad_perm:[0,1,2,3] row_mask:0x8 bank_mask:0xf\n" \
"v_mov_b32_dpp v117, v49 quad_perm:[0,1,2,3] row_mask:0x8 bank_mask:0xf\n" \
"v_mul_f32_e32 v116, v118, v116\n" \
"v_mul_f32_e32 v117, v118, v117\n" \
"v_mov_b32_e64 v122, v118\n" \
"buffer_store_dwordx4 a[12:15], v20, s[80:83], s89 offen offset:1024 sc1\n" \
"s_add_u32 s89, %[oVB], 0x0\n" \
"v_mfma_f32_16x16x32_bf16 a[156:159], v[172:175], v[84:87], a[156:159]\n" \
"v_permlane16_swap_b32_e32 v118, v122\n" \
"v_mov_b32_e32 v38, v118\n" \
"v_mov_b32_e32 v50, v122\n" \
"v_mfma_f32_16x16x32_bf16 a[176:179], v[148:151], v[92:95], a[176:179]\n" \
"v_permlane32_swap_b32_e32 v118, v38\n" \
"v_permlane32_swap_b32_e32 v122, v50\n" \
"v_mul_f32_e32 v192, v118, v192\n" \
"v_mul_f32_e32 v193, v118, v193\n" \
"v_mul_f32_e32 v208, v122, v208\n" \
"v_mul_f32_e32 v209, v122, v209\n" \
"v_mul_f32_e32 v224, v38, v224\n" \
"v_mul_f32_e32 v225, v38, v225\n" \
"v_mul_f32_e32 v240, v50, v240\n" \
"v_mul_f32_e32 v241, v50, v241\n" \
"v_mul_f32_e32 v194, v118, v194\n" \
"v_mul_f32_e32 v195, v118, v195\n" \
"v_cvt_pk_bf16_f32 v192, v192, v193\n" \
"v_mul_f32_e32 v210, v122, v210\n" \
"v_mul_f32_e32 v211, v122, v211\n" \
"v_cvt_pk_bf16_f32 v208, v208, v209\n" \
"v_mul_f32_e32 v226, v38, v226\n" \
"v_mul_f32_e32 v227, v38, v227\n" \
"v_cvt_pk_bf16_f32 v224, v224, v225\n" \
"v_mul_f32_e32 v242, v50, v242\n" \
"v_mul_f32_e32 v243, v50, v243\n" \
"v_cvt_pk_bf16_f32 v240, v240, v241\n" \
"v_cvt_pk_bf16_f32 v116, v116, v117\n" \
"v_cvt_pk_bf16_f32 v193, v194, v195\n" \
"v_cvt_pk_bf16_f32 v209, v210, v211\n" \
"v_cvt_pk_bf16_f32 v225, v226, v227\n" \
"v_cvt_pk_bf16_f32 v241, v242, v243\n" \
"buffer_store_dword v116, v25, s[80:83], %[sO2] offen sc1\n" \
"buffer_store_dwordx2 v[192:193], v24, s[80:83], s84 offen sc1\n" \
"buffer_store_dwordx2 v[208:209], v24, s[80:83], s85 offen sc1\n" \
"buffer_store_dwordx2 v[224:225], v24, s[80:83], s86 offen sc1\n" \
"buffer_store_dwordx2 v[240:241], v24, s[80:83], s87 offen sc1\n" \
"v_mfma_f32_16x16x32_bf16 a[176:179], v[152:155], v[96:99], a[176:179]\n" \
"v_accvgpr_read_b32 v52, a128\n" \
"v_accvgpr_read_b32 v53, a129\n" \
"L_pf8:\n" \
"s_cbranch_execz L_pf12\n" \
"s_nop 0\n" \
"v_mfma_f32_16x16x32_bf16 a[180:183], v[160:163], v[96:99], a[180:183]\n" \
"v_accvgpr_read_b32 v54, a130\n" \
"v_accvgpr_read_b32 v55, a131\n" \
"v_mfma_f32_16x16x32_bf16 a[180:183], v[156:159], v[92:95], a[180:183]\n" \
"v_mul_f32_e32 v52, s70, v52\n" \
"v_mul_f32_e32 v53, s70, v53\n" \
"v_mfma_f32_16x16x32_bf16 a[184:187], v[164:167], v[92:95], a[184:187]\n" \
"v_accvgpr_read_b32 v56, a132\n" \
"v_accvgpr_read_b32 v57, a133\n" \
"v_mfma_f32_16x16x32_bf16 a[184:187], v[168:171], v[96:99], a[184:187]\n" \
"v_accvgpr_read_b32 v58, a134\n" \
"v_accvgpr_read_b32 v59, a135\n" \
"v_mfma_f32_16x16x32_bf16 a[188:191], v[176:179], v[96:99], a[188:191]\n" \
"v_mul_f32_e32 v54, s70, v54\n" \
"v_mul_f32_e32 v55, s70, v55\n" \
"v_mfma_f32_16x16x32_bf16 a[188:191], v[172:175], v[92:95], a[188:191]\n" \
"v_cvt_pk_bf16_f32 v52, v52, v53\n" \
"v_mul_f32_e64 v56, s70, v56\n" \
"v_mfma_f32_16x16x32_bf16 a[208:211], v[148:151], v[100:103], a[208:211]\n" \
"v_mul_f32_e32 v57, s70, v57\n" \
"v_mul_f32_e32 v58, s70, v58\n" \
"v_mfma_f32_16x16x32_bf16 a[208:211], v[152:155], v[104:107], a[208:211]\n" \
"v_mul_f32_e64 v59, s70, v59\n" \
"v_cvt_pk_bf16_f32 v53, v54, v55\n" \
"v_cvt_pk_bf16_f32 v54, v56, v57\n" \
"v_cvt_pk_bf16_f32 v55, v58, v59\n" \
"v_mfma_f32_16x16x32_bf16 a[212:215], v[160:163], v[104:107], a[212:215]\n" \
"ds_write_b128 v27, v[52:55] offset:0\n" \
"v_accvgpr_read_b32 v52, a136\n" \
"v_accvgpr_read_b32 v53, a137\n" \
"v_accvgpr_read_b32 v54, a138\n" \
"v_accvgpr_read_b32 v55, a139\n" \
"v_mul_f32_e32 v52, s70, v52\n" \
"v_mul_f32_e32 v53, s70, v53\n" \
"v_mfma_f32_16x16x32_bf16 a[212:215], v[156:159], v[100:103], a[212:215]\n" \
"v_accvgpr_read_b32 v56, a140\n" \
"v_accvgpr_read_b32 v57, a141\n" \
"v_accvgpr_read_b32 v58, a142\n" \
"v_accvgpr_read_b32 v59, a143\n" \
"v_mul_f32_e32 v54, s70, v54\n" \
"v_mul_f32_e32 v55, s70, v55\n" \
"v_cvt_pk_bf16_f32 v52, v52, v53\n" \
"v_mul_f32_e32 v56, s70, v56\n" \
"v_mul_f32_e32 v57, s70, v57\n" \
"v_mul_f32_e32 v58, s70, v58\n" \
"v_mul_f32_e32 v59, s70, v59\n" \
"buffer_store_dwordx4 a[40:43], v20, s[80:83], s88 offen offset:0 sc1\n" \
"s_add_u32 s88, %[oVB], 0x0\n" \
"v_cvt_pk_bf16_f32 v53, v54, v55\n" \
"v_cvt_pk_bf16_f32 v54, v56, v57\n" \
"v_cvt_pk_bf16_f32 v55, v58, v59\n" \
"ds_write_b128 v27, v[52:55] offset:1024\n" \
"v_mfma_f32_16x16x32_bf16 a[216:219], v[164:167], v[100:103], a[216:219]\n" \
"v_accvgpr_read_b32 v52, a168\n" \
"v_accvgpr_read_b32 v53, a169\n" \
"v_accvgpr_read_b32 v54, a170\n" \
"v_accvgpr_read_b32 v55, a171\n" \
"v_mul_f32_e32 v52, s70, v52\n" \
"v_mul_f32_e64 v53, s70, v53\n" \
"v_accvgpr_read_b32 v56, a172\n" \
"v_accvgpr_read_b32 v57, a173\n" \
"v_accvgpr_read_b32 v58, a174\n" \
"v_accvgpr_read_b32 v59, a175\n" \
"v_mul_f32_e32 v54, s70, v54\n" \
"v_mul_f32_e32 v55, s70, v55\n" \
"v_cvt_pk_bf16_f32 v52, v52, v53\n" \
"v_mfma_f32_16x16x32_bf16 a[216:219], v[168:171], v[104:107], a[216:219]\n" \
"v_mul_f32_e32 v56, s70, v56\n" \
"v_mul_f32_e32 v57, s70, v57\n" \
"v_mul_f32_e32 v58, s70, v58\n" \
"v_mul_f32_e32 v59, s70, v59\n" \
"v_cvt_pk_bf16_f32 v53, v54, v55\n" \
"v_cvt_pk_bf16_f32 v54, v56, v57\n" \
"v_cvt_pk_bf16_f32 v55, v58, v59\n" \
"v_accvgpr_read_b32 v60, a160\n" \
"v_accvgpr_read_b32 v61, a161\n" \
"ds_write_b128 v27, v[52:55] offset:5120\n" \
"v_accvgpr_read_b32 v62, a162\n" \
"v_accvgpr_read_b32 v63, a163\n" \
"v_mul_f32_e32 v60, s70, v60\n" \
"v_mul_f32_e32 v61, s70, v61\n" \
"v_mfma_f32_16x16x32_bf16 a[220:223], v[176:179], v[104:107], a[220:223]\n" \
"v_accvgpr_read_b32 v64, a164\n" \
"v_accvgpr_read_b32 v65, a165\n" \
"v_accvgpr_read_b32 v66, a166\n" \
"v_accvgpr_read_b32 v67, a167\n" \
"v_mul_f32_e32 v62, s70, v62\n" \
"v_mul_f32_e32 v63, s70, v63\n" \
"v_cvt_pk_bf16_f32 v60, v60, v61\n" \
"v_accvgpr_read_b32 v52, a232\n" \
"v_accvgpr_read_b32 v53, a233\n" \
"v_mul_f32_e32 v64, s70, v64\n" \
"v_mul_f32_e32 v65, s70, v65\n" \
"v_mul_f32_e32 v66, s70, v66\n" \
"v_mul_f32_e32 v67, s70, v67\n" \
"v_cvt_pk_bf16_f32 v61, v62, v63\n" \
"v_accvgpr_read_b32 v54, a234\n" \
"v_accvgpr_read_b32 v55, a235\n" \
"v_mul_f32_e32 v52, s70, v52\n" \
"v_mul_f32_e32 v53, s70, v53\n" \
"v_cvt_pk_bf16_f32 v62, v64, v65\n" \
"v_cvt_pk_bf16_f32 v63, v66, v67\n" \
"v_accvgpr_read_b32 v56, a236\n" \
"buffer_store_dwordx4 a[44:47], v20, s[80:83], s89 offen offset:1024 sc1\n" \
"s_add_u32 s89, %[oVB], 0x0\n" \
"v_accvgpr_read_b32 v57, a237\n" \
"v_accvgpr_read_b32 v58, a238\n" \
"v_accvgpr_read_b32 v59, a239\n" \
"v_mul_f32_e32 v54, s70, v54\n" \
"v_mul_f32_e64 v55, s70, v55\n" \
"v_cvt_pk_bf16_f32 v52, v52, v53\n" \
"ds_write_b128 v27, v[60:63] offset:4096\n" \
"v_mul_f32_e32 v56, s70, v56\n" \
"v_mul_f32_e32 v57, s70, v57\n" \
"v_mul_f32_e32 v58, s70, v58\n" \
"v_mul_f32_e32 v59, s70, v59\n" \
"v_cvt_pk_bf16_f32 v53, v54, v55\n" \
"v_mfma_f32_16x16x32_bf16 a[220:223], v[172:175], v[100:103], a[220:223]\n" \
"v_cvt_pk_bf16_f32 v54, v56, v57\n" \
"v_cvt_pk_bf16_f32 v55, v58, v59\n" \
"v_accvgpr_read_b32 v68, a192\n" \
"v_accvgpr_read_b32 v69, a193\n" \
"v_accvgpr_read_b32 v60, a200\n" \
"v_accvgpr_read_b32 v61, a201\n" \
"v_accvgpr_read_b32 v76, a224\n" \
"v_accvgpr_read_b32 v77, a225\n" \
"ds_write_b128 v27, v[52:55] offset:13312\n" \
"v_accvgpr_read_b32 v70, a194\n" \
"v_accvgpr_read_b32 v71, a195\n" \
"v_mul_f32_e32 v68, s70, v68\n" \
"v_mul_f32_e32 v69, s70, v69\n" \
"v_accvgpr_read_b32 v62, a202\n" \
"v_accvgpr_read_b32 v63, a203\n" \
"v_mul_f32_e32 v60, s70, v60\n" \
"v_mul_f32_e32 v61, s70, v61\n" \
"v_accvgpr_read_b32 v78, a226\n" \
"v_accvgpr_read_b32 v79, a227\n" \
"v_mul_f32_e32 v76, s70, v76\n" \
"v_mul_f32_e32 v77, s70, v77\n" \
"v_accvgpr_read_b32 v72, a196\n" \
"v_accvgpr_read_b32 v73, a197\n" \
"v_accvgpr_read_b32 v74, a198\n" \
"v_accvgpr_read_b32 v75, a199\n" \
"v_mul_f32_e32 v70, s70, v70\n" \
"v_mul_f32_e32 v71, s70, v71\n" \
"v_cvt_pk_bf16_f32 v68, v68, v69\n" \
"v_accvgpr_read_b32 v64, a204\n" \
"v_accvgpr_read_b32 v65, a205\n" \
"v_accvgpr_read_b32 v66, a206\n" \
"v_accvgpr_read_b32 v67, a207\n" \
"v_mul_f32_e32 v62, s70, v62\n" \
"v_mul_f32_e32 v63, s70, v63\n" \
"v_cvt_pk_bf16_f32 v60, v60, v61\n" \
"v_accvgpr_read_b32 v80, a228\n" \
"v_accvgpr_read_b32 v81, a229\n" \
"v_accvgpr_read_b32 v82, a230\n" \
"v_accvgpr_read_b32 v83, a231\n" \
"v_mul_f32_e32 v78, s70, v78\n" \
"v_mul_f32_e32 v79, s70, v79\n" \
"v_cvt_pk_bf16_f32 v76, v76, v77\n" \
"v_accvgpr_read_b32 v52, a240\n" \
"v_accvgpr_read_b32 v53, a241\n" \
"buffer_store_dwordx4 a[72:75], v20, s[80:83], s88 offen offset:2048 sc1\n" \
"s_add_u32 s88, %[oVB], 0x2000\n" \
"v_accvgpr_read_b32 v54, a242\n" \
"v_accvgpr_read_b32 v55, a243\n" \
"v_mul_f32_e32 v72, s70, v72\n" \
"v_mul_f32_e32 v73, s70, v73\n" \
"v_mfma_f32_16x16x32_bf16 v[52:55], v[148:151], v[108:111], v[52:55]\n" \
"v_mul_f32_e32 v74, s70, v74\n" \
"v_mul_f32_e32 v75, s70, v75\n" \
"v_cvt_pk_bf16_f32 v69, v70, v71\n" \
"v_mul_f32_e32 v64, s70, v64\n" \
"v_mul_f32_e32 v65, s70, v65\n" \
"v_mul_f32_e32 v66, s70, v66\n" \
"v_mul_f32_e32 v67, s70, v67\n" \
"v_cvt_pk_bf16_f32 v61, v62, v63\n" \
"v_mul_f32_e32 v80, s70, v80\n" \
"v_mul_f32_e32 v81, s70, v81\n" \
"v_mul_f32_e32 v82, s70, v82\n" \
"v_mul_f32_e32 v83, s70, v83\n" \
"v_cvt_pk_bf16_f32 v77, v78, v79\n" \
"v_cvt_pk_bf16_f32 v70, v72, v73\n" \
"v_cvt_pk_bf16_f32 v71, v74, v75\n" \
"v_cvt_pk_bf16_f32 v62, v64, v65\n" \
"v_cvt_pk_bf16_f32 v63, v66, v67\n" \
"v_cvt_pk_bf16_f32 v78, v80, v81\n" \
"v_cvt_pk_bf16_f32 v79, v82, v83\n" \
"ds_write_b128 v27, v[68:71] offset:8192\n" \
"ds_write_b128 v27, v[60:63] offset:9216\n" \
"ds_write_b128 v27, v[76:79] offset:12288\n" \
"v_mfma_f32_16x16x32_bf16 v[52:55], v[152:155], v[112:115], v[52:55]\n" \
"v_accvgpr_read_b32 v84, a144\n" \
"v_accvgpr_read_b32 v85, a145\n" \
"v_accvgpr_read_b32 v68, a152\n" \
"v_accvgpr_read_b32 v69, a153\n" \
"v_accvgpr_read_b32 v92, a176\n" \
"v_accvgpr_read_b32 v93, a177\n" \
"v_accvgpr_read_b32 v60, a184\n" \
"v_accvgpr_read_b32 v61, a185\n" \
"v_accvgpr_read_b32 v100, a208\n" \
"v_accvgpr_read_b32 v101, a209\n" \
"v_accvgpr_read_b32 v76, a216\n" \
"v_accvgpr_read_b32 v77, a217\n" \
"v_accvgpr_read_b32 v86, a146\n" \
"v_accvgpr_read_b32 v87, a147\n" \
"v_pk_mul_f32 v[84:85], v[84:85], s[70:71]\n" \
"v_accvgpr_read_b32 v70, a154\n" \
"v_accvgpr_read_b32 v71, a155\n" \
"v_pk_mul_f32 v[68:69], v[68:69], s[70:71]\n" \
"v_accvgpr_read_b32 v94, a178\n" \
"v_accvgpr_read_b32 v95, a179\n" \
"v_pk_mul_f32 v[92:93], v[92:93], s[70:71]\n" \
"v_accvgpr_read_b32 v62, a186\n" \
"v_accvgpr_read_b32 v63, a187\n" \
"v_pk_mul_f32 v[60:61], v[60:61], s[70:71]\n" \
"v_accvgpr_read_b32 v102, a210\n" \
"v_accvgpr_read_b32 v103, a211\n" \
"v_pk_mul_f32 v[100:101], v[100:101], s[70:71]\n" \
"buffer_store_dwordx4 a[76:79], v20, s[80:83], s89 offen offset:3072 sc1\n" \
"s_add_u32 s89, %[oVB], 0x2000\n" \
"v_accvgpr_read_b32 v78, a218\n" \
"v_accvgpr_read_b32 v79, a219\n" \
"v_pk_mul_f32 v[76:77], v[76:77], s[70:71]\n" \
"v_pk_mul_f32 v[52:53], v[52:53], s[70:71]\n" \
"v_accvgpr_read_b32 v88, a148\n" \
"v_accvgpr_read_b32 v89, a149\n" \
"v_accvgpr_read_b32 v90, a150\n" \
"v_accvgpr_read_b32 v91, a151\n" \
"v_pk_mul_f32 v[86:87], v[86:87], s[70:71]\n" \
"v_cvt_pk_bf16_f32 v84, v84, v85\n" \
"v_accvgpr_read_b32 v72, a156\n" \
"v_accvgpr_read_b32 v73, a157\n" \
"v_accvgpr_read_b32 v74, a158\n" \
"v_accvgpr_read_b32 v75, a159\n" \
"v_pk_mul_f32 v[70:71], v[70:71], s[70:71]\n" \
"v_cvt_pk_bf16_f32 v68, v68, v69\n" \
"v_accvgpr_read_b32 v96, a180\n" \
"v_accvgpr_read_b32 v97, a181\n" \
"v_accvgpr_read_b32 v98, a182\n" \
"v_accvgpr_read_b32 v99, a183\n" \
"v_pk_mul_f32 v[94:95], v[94:95], s[70:71]\n" \
"v_cvt_pk_bf16_f32 v92, v92, v93\n" \
"v_accvgpr_read_b32 v64, a188\n" \
"v_accvgpr_read_b32 v65, a189\n" \
"v_accvgpr_read_b32 v66, a190\n" \
"v_accvgpr_read_b32 v67, a191\n" \
"v_pk_mul_f32 v[62:63], v[62:63], s[70:71]\n" \
"v_cvt_pk_bf16_f32 v60, v60, v61\n" \
"v_accvgpr_read_b32 v104, a212\n" \
"v_accvgpr_read_b32 v105, a213\n" \
"v_accvgpr_read_b32 v106, a214\n" \
"v_accvgpr_read_b32 v107, a215\n" \
"v_pk_mul_f32 v[102:103], v[102:103], s[70:71]\n" \
"v_cvt_pk_bf16_f32 v100, v100, v101\n" \
"v_accvgpr_read_b32 v80, a220\n" \
"v_accvgpr_read_b32 v81, a221\n" \
"v_accvgpr_read_b32 v82, a222\n" \
"v_accvgpr_read_b32 v83, a223\n" \
"v_pk_mul_f32 v[78:79], v[78:79], s[70:71]\n" \
"v_cvt_pk_bf16_f32 v76, v76, v77\n" \
"v_pk_mul_f32 v[54:55], v[54:55], s[70:71]\n" \
"v_cvt_pk_bf16_f32 v52, v52, v53\n" \
"v_pk_mul_f32 v[88:89], v[88:89], s[70:71]\n" \
"v_pk_mul_f32 v[90:91], v[90:91], s[70:71]\n" \
"v_cvt_pk_bf16_f32 v85, v86, v87\n" \
"v_pk_mul_f32 v[72:73], v[72:73], s[70:71]\n" \
"v_pk_mul_f32 v[74:75], v[74:75], s[70:71]\n" \
"v_cvt_pk_bf16_f32 v69, v70, v71\n" \
"v_pk_mul_f32 v[96:97], v[96:97], s[70:71]\n" \
"v_pk_mul_f32 v[98:99], v[98:99], s[70:71]\n" \
"v_cvt_pk_bf16_f32 v93, v94, v95\n" \
"v_pk_mul_f32 v[64:65], v[64:65], s[70:71]\n" \
"v_pk_mul_f32 v[66:67], v[66:67], s[70:71]\n" \
"v_cvt_pk_bf16_f32 v61, v62, v63\n" \
"v_pk_mul_f32 v[104:105], v[104:105], s[70:71]\n" \
"v_pk_mul_f32 v[106:107], v[106:107], s[70:71]\n" \
"v_cvt_pk_bf16_f32 v101, v102, v103\n" \
"v_pk_mul_f32 v[80:81], v[80:81], s[70:71]\n" \
"v_pk_mul_f32 v[82:83], v[82:83], s[70:71]\n" \
"v_cvt_pk_bf16_f32 v77, v78, v79\n" \
"buffer_store_dwordx4 a[104:107], v20, s[80:83], s88 offen offset:0 sc1\n" \
"v_accvgpr_read_b32 v56, a244\n" \
"v_accvgpr_read_b32 v57, a245\n" \
"v_accvgpr_read_b32 v58, a246\n" \
"v_accvgpr_read_b32 v59, a247\n" \
"v_cvt_pk_bf16_f32 v53, v54, v55\n" \
"v_cvt_pk_bf16_f32 v86, v88, v89\n" \
"v_mfma_f32_16x16x32_bf16 v[56:59], v[160:163], v[112:115], v[56:59]\n" \
"v_cvt_pk_bf16_f32 v87, v90, v91\n" \
"v_cvt_pk_bf16_f32 v70, v72, v73\n" \
"v_cvt_pk_bf16_f32 v71, v74, v75\n" \
"v_cvt_pk_bf16_f32 v94, v96, v97\n" \
"v_cvt_pk_bf16_f32 v95, v98, v99\n" \
"v_cvt_pk_bf16_f32 v62, v64, v65\n" \
"v_cvt_pk_bf16_f32 v63, v66, v67\n" \
"v_cvt_pk_bf16_f32 v102, v104, v105\n" \
"v_cvt_pk_bf16_f32 v103, v106, v107\n" \
"v_cvt_pk_bf16_f32 v78, v80, v81\n" \
"v_cvt_pk_bf16_f32 v79, v82, v83\n" \
"ds_write_b128 v21, v[84:87] offset:16384\n" \
"ds_write_b128 v21, v[68:71] offset:17408\n" \
"ds_write_b128 v21, v[92:95] offset:20480\n" \
"ds_write_b128 v21, v[60:63] offset:21504\n" \
"ds_write_b128 v21, v[100:103] offset:24576\n" \
"ds_write_b128 v21, v[76:79] offset:25600\n" \
"v_mfma_f32_16x16x32_bf16 v[56:59], v[156:159], v[108:111], v[56:59]\n" \
"v_accvgpr_read_b32 v84, a248\n" \
"v_accvgpr_read_b32 v85, a249\n" \
"v_accvgpr_read_b32 v86, a250\n" \
"v_accvgpr_read_b32 v87, a251\n" \
"v_accvgpr_read_b32 v88, a252\n" \
"v_accvgpr_read_b32 v89, a253\n" \
"v_mfma_f32_16x16x32_bf16 v[84:87], v[164:167], v[108:111], v[84:87]\n" \
"v_accvgpr_read_b32 v90, a254\n" \
"v_pk_mul_f32 v[56:57], v[56:57], s[70:71]\n" \
"v_pk_mul_f32 v[58:59], v[58:59], s[70:71]\n" \
"v_cvt_pk_bf16_f32 v54, v56, v57\n" \
"v_cvt_pk_bf16_f32 v55, v58, v59\n" \
"ds_write_b128 v21, v[52:55] offset:28672\n" \
"v_mfma_f32_16x16x32_bf16 v[84:87], v[168:171], v[112:115], v[84:87]\n" \
"v_accvgpr_read_b32 v91, a255\n" \
"v_sub_f32_e32 v44, 1.0, v45\n" \
"ds_write_b32 v26, v44\n" \
"v_mfma_f32_16x16x32_bf16 v[88:91], v[176:179], v[112:115], v[88:91]\n" \
"v_mfma_f32_16x16x32_bf16 v[88:91], v[172:175], v[108:111], v[88:91]\n" \
"buffer_store_dwordx4 a[108:111], v20, s[80:83], s89 offen offset:1024 sc1\n" \
"s_nop 1\n" \
"v_pk_mul_f32 v[84:85], v[84:85], s[70:71]\n" \
"v_pk_mul_f32 v[86:87], v[86:87], s[70:71]\n" \
"v_cvt_pk_bf16_f32 v84, v84, v85\n" \
"v_cvt_pk_bf16_f32 v85, v86, v87\n" \
"s_nop 0\n" \
"v_pk_mul_f32 v[88:89], v[88:89], s[70:71]\n" \
"v_pk_mul_f32 v[90:91], v[90:91], s[70:71]\n" \
"v_cvt_pk_bf16_f32 v86, v88, v89\n" \
"v_cvt_pk_bf16_f32 v87, v90, v91\n" \
"ds_write_b128 v21, v[84:87] offset:29696\n" \
"L_end_%=:\n"
#define PROJ_VCLOB "v33","v34","v35","v36","v37","v38","v39","v44","v45","v48","v49","v50","v51","v52","v53","v54","v55","v56","v57","v58","v59","v60","v61","v62","v63","v64","v65","v66","v67","v68","v69","v70","v71","v72","v73","v74","v75","v76","v77","v78","v79","v80","v81","v82","v83","v84","v85","v86","v87","v88","v89","v90","v91","v92","v93","v94","v95","v96","v97","v98","v99","v100","v101","v102","v103","v104","v105","v106","v107","v108","v109","v110","v111","v112","v113","v114","v115","v116","v117","v118","v119","v120","v121","v122","v123","v124","v125","v126","v127","v128","v129","v130","v131","v132","v133","v134","v135","v136","v137","v138","v139","v140","v141","v142","v143","v144","v145","v146","v147","v148","v149","v150","v151","v152","v153","v154","v155","v156","v157","v158","v159","v160","v161","v162","v163","v164","v165","v166","v167","v168","v169","v170","v171","v172","v173","v174","v175","v176","v177","v178","v179","v180","v181","v182","v183","v200","v201","v202","v203","v204","v205","v206","v207","v208","v209","v210","v211","v212","v213","v214","v215","v216","v217","v218","v219","v220","v221","v222","v223","v224","v225","v226","v227","v228","v229","v230","v231","v232","v233","v234","v235","v236","v237","v238","v239","v240","v241","v242","v243","v244","v245","v246","v247","v248","v249","v250","v251","v252","v253","v254","v255"
#define PROJ_SCLOB "s62","s63","s76","s77","s78","s79","s80","s81","s82","s83","s84","s85","s86","s87","s88","s89"

#define PLO(p) ((unsigned)(size_t)(p))
#define PHI(p) ((unsigned)((size_t)(p) >> 32))
template <class ERL> __device__ __forceinline__ unsigned proj_phase(const u16* __restrict__ x, const u16* __restrict__ ckv, const u16* __restrict__ wq, const u16* __restrict__ wuk,
                                                       const u16* __restrict__ wuv, const u16* __restrict__ wqkv, const u16* __restrict__ gw,
                                                       char* __restrict__ ws, char* lds, const unsigned wid, unsigned& tag, u32x4 (&vp)[5], ERL&& early) {
    typedef unsigned u32x2 __attribute__((ext_vector_type(2)));
    unsigned* const flags = (unsigned*)(ws + WS_F);
    const int b = blockIdx.x, h = (b & 7) * 4 + ((b >> 3) & 3), mt = b >> 5;
    const unsigned L0 = (unsigned)(size_t)lds, rb = (wid < 2u ? 15u - mt : mt) * 128u;
    RTS(0);
#define PST() do {} while (0)
    const unsigned lane = threadIdx.x & 63u, wr = wid >> 1, wc = wid & 1u;
    PST();
    unsigned vAc[2], vAx[2], vB[2];
    const unsigned sp = b & 7, rot = (PROJ_ROT) & 7u;
#pragma unroll
    for (int par = 0; par < 2; ++par) {
        const unsigned lr = lane >> 3, cc = (lane & 7u) ^ (4u * par + (lr >> 1)), r0 = rb + wc * 64u + lr;
        vAc[par] = r0 * (DC * 2) + cc * 16u; vAx[par] = r0 * (DM * 2) + cc * 16u;
        const unsigned br = lane >> 4, p16 = lane & 15u, sc = (((p16 >> 1) ^ (br | (par << 2))) << 1) | (p16 & 1u);
        vB[par] = (16u * wid + br) * (NBIG * 2) + h * (HD * 2) + sc * 16u;
    }
#define SRD(p, n) (u32x4){PLO(p), PHI(p) & 0xffffu, n, 0x00020000u}
    u32x4 sC = SRD(ckv, S * DC * 2), sK = SRD(wuk, DC * NBIG * 2), sV = SRD(wuv, DC * NBIG * 2);
#define PRE(n) do { SBAR(); asm volatile(PROJ_PRE##n :: "{v22}"(vAc[0]), "{v23}"(vAc[1]), "{v26}"(vB[0]), "{v27}"(vB[1]), "{s[64:67]}"(sC), "{s[68:71]}"(sK), "{s[72:75]}"(sV), \
                 [mA] "s"(L0 + wid * 8192u), [mB] "s"(L0 + 65536u + wid * 4096u), [rc] "s"(rot * 128u) : "memory", "m0", "scc", "s62", "s63", "s84", "s85"); SBAR(); } while (0)
    const unsigned wi = (unsigned)(b >> 3) * 4u + wid, mS = L0 + 151552u + wid * 512u;
    PRE(0);
    u32x4 r192, r196;
    {
        const unsigned OOR = 0x40000000u, w3 = L0 + 139264u - mt * 9216u, dum = L0 + 148480u + lane * 16u;
#define LSW(k) ((((k) >> 1) & 1u) | (((k) >> 2) & 2u))
#pragma unroll
        for (int i = 0; i < 2; ++i) {
            const unsigned q = wid * 64u + lane + 256u * i, r6 = __umul24(q, 10923u) >> 16, c6 = q - r6 * 6u, k = mt * 72u + (q < 432u ? r6 : q - 432u);
            r192[i] = q < 432u ? __umul24(k, NQKV * 2) + (c6 >> 1) * (DM * 2) + (c6 & 1u) * 16u : OOR;
            if (i) r192[3] = q - 432u < 72u ? k * 2u : OOR;
            r196[i] = q < 504u ? k * 128u + (((q < 432u ? c6 : 7u) ^ (LSW(k) << 1)) << 4) + w3 : dum;
        }
        const unsigned k = mt * 72u + wid * 18u + lane;
        r192[2] = lane < 18u ? __umul24(k, NQKV * 2) : OOR;
        r196[2] = lane < 18u ? k * 128u + ((3u ^ LSW(k)) << 5) + w3 : dum; r196[3] = L0 + 139264u + wid * 1024u + lane * 16u;
    }
    const unsigned q27 = wi * 27u + min(lane & 31u, 26u), r27 = __umul24(q27, 43691u) >> 18, s27 = q27 - r27 * 6u;
    const char* const tk2 = (const char*)wqkv + (r27 * 27u + (s27 >> 1) * 9u + (s27 & 1u) + (b & 7)) * 128u;
    const unsigned vH = ((rb ? rb - 16u : 0u) + wc * 8u + (lane >> 3)) * (DM * 2) + (((lane & 7u) ^ (4u * wc + (lane >> 4))) << 4);
    const unsigned q36 = wi * 36u + lane;
    const char* const tp0 = lane < 36u ? (const char*)wq + ((q36 >> 3) * 64u + (b & 7) * 8u + (q36 & 7u)) * 128u : (const char*)x + (((unsigned)b * 4u + wid) * 18u + min(lane, 53u) - 36u) * 128u;
    const unsigned fr = lane & 15u, fg = lane >> 4;
    const unsigned aA0 = L0 + (wr * 64u + fr) * 128u + ((fg ^ (fr >> 1)) << 4), r4 = (lane >> 2) & 3u;
    const unsigned sB = r4 | ((fg & 1u) << 2), aB = L0 + 65536u + (8u * fg + r4) * 256u + (lane & 3u) * 8u + ((sB ^ (4u * wc)) << 5);
    const unsigned aL = L0 + 65536u + 16384u + (8u * fg + r4) * 128u + (lane & 3u) * 8u + (((r4 >> 1) | ((fg & 1u) << 1)) << 5);
    const unsigned pl = (fr & 1u) ? 0x01000c0cu : 0x0c0c0100u, ph = (fr & 1u) ? 0x03020c0cu : 0x0c0c0302u, pz = 0x0c0c0c0cu, kk = 4u * fg + (fr & 1u);
    u32x4 ps = {kk <= fr ? pz : pl, kk + 2u <= fr ? pz : ph, kk <= fr ? pl : pz, kk + 2u <= fr ? ph : pz};
    u32x2 sel = {fg == 1u && !(lane & 2u) ? 0x3f80u << ((lane & 1u) * 16u) : 0u, fg == 0u && (lane & 3u) == 2u ? 0x3f80u : 0u};
    const unsigned oH = (unsigned)WS_K + (unsigned)h * 1048576u, oK = oH + wr * 32768u + wc * 2048u, oV = oK + 16384u + wc * 2048u, sQ = L0 + 65536u + wr * 32768u + wc * 2048u, oA = (unsigned)WS_AF + ((rb >> 4) + 4u * wc) * (KS_TOT * 1024u), oF = (unsigned)WS_F + (unsigned)(F_KV + h * 64) * 4u;
    u32x4 r20 = {lane * 16u, vH, vAc[0], vAc[1]}, r24 = {vAx[0], vAx[1], vB[0], vB[1]}, r184 = {aL, aL, PLO(tk2), PHI(tk2)}, r188 = {PLO(tp0), PHI(tp0), aL, aL};
    u32x2 r28 = {aA0, aA0 ^ 64u}; unsigned r32 = aB;
    asm volatile("" : "+v"(r20), "+v"(r24), "+v"(r184), "+v"(r188), "+v"(r28), "+v"(r32), "+v"(ps), "+v"(sel), "+v"(r192), "+v"(r196));
    PST();
    PRE(1);
    early([&]() { PST(); PRE(2); });
    PST();
    PRE(3);
    asm volatile("s_mov_b64 exec, 0\n s_cmp_lt_u32 %0, 2\n s_cbranch_scc1 L_chA\n s_cmp_eq_u32 %0, 2\n s_cbranch_scc1 L_pf2\n s_branch L_pf3\n"
                 "L_chA: s_cmp_eq_u32 %0, 0\n s_cbranch_scc1 L_pf0\n s_branch L_pf1\n L_retA: s_mov_b64 exec, -1" :: "s"(wid) : "scc", "memory");
    PST();
    asm volatile(PROJ_ASM
                 : [tag] "=&s"(tag), "+{s[64:67]}"(sC), "+{s[68:71]}"(sK), "+{s[72:75]}"(sV), "+{v[20:23]}"(r20), "+{v[24:27]}"(r24), "+{v[28:29]}"(r28), "+{v32}"(r32), "+{v[184:187]}"(r184), "+{v[188:191]}"(r188), "+{v[192:195]}"(r192), "+{v[196:199]}"(r196)
                 : "{v[0:3]}"(vp[0]), "{v[4:7]}"(vp[1]), "{v[8:11]}"(vp[2]), "{v[12:15]}"(vp[3]), "{v[16:19]}"(vp[4]), "{v[40:43]}"(ps), "{v[46:47]}"(sel),
                   [xl] "s"(PLO(x)), [xh] "s"(PHI(x)), [wql] "s"(PLO(wq)), [wqh] "s"(PHI(wq)),
                   [wsl] "s"(PLO(ws)), [wsh] "s"(PHI(ws)), [wvl] "s"(PLO(wqkv)), [wvh] "s"(PHI(wqkv)), [gwl] "s"(PLO(gw)), [gwh] "s"(PHI(gw)), [oW] "s"(h * (DHA * 2)),
                   [oL] "s"((unsigned)WS_L + (unsigned)h * 73728u + wid * 1024u), [oLs] "s"((unsigned)WS_L + (unsigned)h * 73728u + wid * 1024u + mt * 9216u), [oL3] "s"(wid ? 0x40000000u : (unsigned)WS_L + (unsigned)h * 73728u + mt * 9216u + 8192u), [oFs] "s"(wid != 3u ? 0x40000000u : oF + mt * 4u), [oFp] "s"(oF), [mA] "s"(L0 + wid * 8192u), [mB] "s"(L0 + 65536u + wid * 4096u),
                   [mL] "s"(L0 + 65536u + 16384u + wid * 1024u), [mS] "s"(mS), [wl] "s"(wi * 128u), [rc] "s"(rot * 128u), [oKA] "s"(oK + mt * 65536u), [oKB] "s"(oK + (15u - mt) * 65536u), [oVA] "s"(oV + mt * 65536u), [oVB] "s"(oV + (15u - mt) * 65536u),
                   [dLo] "s"((wr + wc) * 8192u), [dHi] "s"((wr + 1u - wc) * 8192u), [dH] "s"(wc ? wr * 8192u + 6144u : 94208u - wr * 6144u), [sG] "s"(L0 + 159744u + wid * 256u), [sQ] "s"(wc ? sQ + 16384u + wr * 8192u : sQ),
                   [sQ2] "s"(wc ? sQ - 16384u : sQ + wr * 8192u), [sBias] "s"(rb + wc ? 0u : 0xff800000u),
                   [sOA] "s"(oA + h * 512u), [sO2] "s"(oA + 16384u + (h >> 2) * 256u + (h & 3) * 4u),
                   [oP] "s"(oH + wid * 4096u), [mV] "s"(mt ? 49152u : 32768u), [oP2] "s"(oH + 65536u + wid * 2048u), [mK2] "s"((mt ? 32768u : 16384u) + wid * 2048u)
                 : "memory", "m0", "vcc", "scc", PROJ_VCLOB, PROJ_SCLOB, ACLOB_LO, ACLOB_HI);
    RTS(1);
    return r20[0];
}

namespace att {
#define DI __device__ __forceinline__
constexpr int LDS_BYTES = 163840;
constexpr unsigned LDS_G = 159744u, LDS_L = 161792u;
typedef __bf16 bfx8 __attribute__((ext_vector_type(8)));
DI unsigned rfl(unsigned x) { return __builtin_amdgcn_readfirstlane(x); }
DI f32x4 mfma(u32x4 a, u32x4 b, f32x4 c) { return __builtin_amdgcn_mfma_f32_16x16x32_bf16(__builtin_bit_cast(bfx8, a), __builtin_bit_cast(bfx8, b), c, 0, 0, 0); }
typedef __bf16 bfx2 __attribute__((ext_vector_type(2)));
typedef float f32x2 __attribute__((ext_vector_type(2)));
DI unsigned cvtpk2(float lo, float hi) { f32x2 v = {lo, hi}; bfx2 b = __builtin_convertvector(v, bfx2); return __builtin_bit_cast(unsigned, b); }
DI void att_exact(const u16* __restrict__ x, const u16* __restrict__ wq, char* __restrict__ ws, unsigned h, unsigned r0, float g1) {
    const unsigned lane = lane_id(), g = lane >> 4, c = lane & 15u;
    f32x4 o[8];
#pragma unroll
    for (int n = 0; n < 8; ++n) o[n] = (f32x4){0.f, 0.f, 0.f, 0.f};
#pragma unroll 1
    for (unsigned kk = 0; kk < (unsigned)DM; kk += 32u) {
        const unsigned k = kk + 8u * g;
        const u32x4 xf = *reinterpret_cast<const u32x4*>(x + (r0 + c) * DM + k);
#pragma unroll
        for (int n = 0; n < 8; ++n) {
            const u16* w = wq + k * NBIG + h * HD + n * 16 + c; u32x4 wf;
#pragma unroll
            for (int e = 0; e < 4; ++e) wf[e] = (unsigned)w[2 * e * NBIG] | ((unsigned)w[(2 * e + 1) * NBIG] << 16);
            o[n] = mfma(wf, xf, o[n]);
        }
    }
    u32x4 q[4];
#pragma unroll
    for (int p = 0; p < 4; ++p) {
        const f32x4 a = o[2 * p] * 0.12751743082f, b = o[2 * p + 1] * 0.12751743082f;
        q[p] = (u32x4){cvtpk2(a[0], a[1]), cvtpk2(a[2], a[3]), cvtpk2(b[0], b[1]), cvtpk2(b[2], b[3])};
    }
#pragma unroll
    for (int n = 0; n < 8; ++n) o[n] = (f32x4){0.f, 0.f, 0.f, 0.f};
    const __amdgpu_buffer_rsrc_t srd = __builtin_amdgcn_make_buffer_rsrc((void*)(ws + WS_K + (size_t)h * 1048576), (short)0, 1048576, 0x00020000);
    float m = -3.0e38f, l = 0.f;
#pragma unroll 1
    for (unsigned k0 = 0; k0 <= r0 + 15u; k0 += 32u) {
        const unsigned tb = (k0 >> 6) * 32768u + (k0 & 32u) * 256u;
        f32x4 s[2]; float mx = m;
#pragma unroll
        for (int kt = 0; kt < 2; ++kt) {
            s[kt] = (f32x4){0.f, 0.f, 0.f, 0.f};
#pragma unroll
            for (int p = 0; p < 4; ++p) s[kt] = mfma(__builtin_amdgcn_raw_buffer_load_b128(srd, lane * 16u + (kt * 4 + p) * 1024u, tb, 16), q[p], s[kt]);
#pragma unroll
            for (int e = 0; e < 4; ++e) { if (k0 + 16u * kt + 4u * g + e > r0 + c) s[kt][e] = -3.0e38f; mx = fmaxf(mx, s[kt][e]); }
        }
        mx = fmaxf(mx, __shfl_xor(mx, 16)); mx = fmaxf(mx, __shfl_xor(mx, 32));
        const float al = __builtin_amdgcn_exp2f(m - mx); m = mx;
        u32x4 pf;
#pragma unroll
        for (int e = 0; e < 4; ++e) pf[e] = cvtpk2(__builtin_amdgcn_exp2f(s[e >> 1][2 * (e & 1)] - m), __builtin_amdgcn_exp2f(s[e >> 1][2 * (e & 1) + 1] - m));
        l *= al;
#pragma unroll
        for (int e = 0; e < 4; ++e) l += __uint_as_float(pf[e] << 16) + __uint_as_float(pf[e] & 0xffff0000u);
#pragma unroll
        for (int n = 0; n < 8; ++n) o[n] = mfma(__builtin_amdgcn_raw_buffer_load_b128(srd, lane * 16u + 16384u + n * 1024u, tb, 16), pf, o[n] * al);
    }
    l += __shfl_xor(l, 16); l += __shfl_xor(l, 32);
    const float f = g1 / l;
    const __amdgpu_buffer_rsrc_t srdA = __builtin_amdgcn_make_buffer_rsrc((void*)(ws + WS_AF), (short)0, 128 * KS_TOT * 1024, 0x00020000);
#pragma unroll
    for (int p = 0; p < 4; ++p) {
        const f32x4 a = o[2 * p] * f, b = o[2 * p + 1] * f;
        __builtin_amdgcn_raw_buffer_store_b128((u32x4){cvtpk2(a[0], a[1]), cvtpk2(a[2], a[3]), cvtpk2(b[0], b[1]), cvtpk2(b[2], b[3])}, srdA, lane * 16u + p * 1024u, ((r0 >> 4) * KS_TOT + 18u + 4u * h) * 1024u, 16);
    }
}
}

#define ATT_ASM \
"L_att0:\n" \
"s_mov_b32 s101, 0\n" \
"s_waitcnt vmcnt(22) lgkmcnt(0)\n" \
"s_barrier\n" \
"v_accvgpr_write_b32 a216, 0\n" \
"v_accvgpr_write_b32 a217, 0\n" \
"v_accvgpr_write_b32 a218, 0\n" \
"v_accvgpr_write_b32 a219, 0\n" \
"v_and_b32_e32 v63, 0x300, v20\n" \
"ds_read_b128 v[96:99], v63 offset:0\n" \
"v_mfma_f32_32x32x16_bf16 v[128:143], a[216:219], a[216:219], 0\n" \
"v_add_u32_e32 v58, %[qa], v20\n" \
"ds_read_b128 a[0:3], v58 offset:0\n" \
"ds_read_b128 a[16:19], v58 offset:8192\n" \
"v_add_u32_e32 v59, %[qa2], v20\n" \
"ds_read_b128 a[32:35], v59 offset:0\n" \
"ds_read_b128 a[48:51], v59 offset:8192\n" \
"v_mfma_f32_32x32x16_bf16 v[144:159], a[216:219], a[216:219], 0\n" \
"ds_read_b128 v[100:103], v63 offset:1024\n" \
"ds_read_b128 a[4:7], v58 offset:1024\n" \
"ds_read_b128 a[20:23], v58 offset:9216\n" \
"s_sub_u32 s67, %[m1], 0xc000\n" \
"s_mov_b32 s65, %[m1]\n" \
"v_add_u32_e32 v55, s67, v20\n" \
"v_mfma_f32_32x32x16_bf16 v[160:175], a[216:219], a[216:219], 0\n" \
"v_add_u32_e32 v52, 0xc000, v55\n" \
"s_xor_b32 m0, s65, 0x10000\n" \
"ds_read_b128 a[36:39], v59 offset:1024\n" \
"buffer_load_dwordx4 v52, %[srd], 0 offen offset:0 sc1 lds\n" \
"ds_read_b128 a[52:55], v59 offset:9216\n" \
"v_mfma_f32_32x32x16_bf16 v[176:191], a[216:219], a[216:219], 0\n" \
"ds_read_b128 v[104:107], v63 offset:2048\n" \
"ds_read_b128 a[8:11], v58 offset:2048\n" \
"ds_read_b128 a[24:27], v58 offset:10240\n" \
"buffer_load_dwordx4 v52, %[srd], 0 offen offset:1024 sc1 lds\n" \
"ds_read_b128 a[40:43], v59 offset:2048\n" \
"v_mfma_f32_32x32x16_bf16 v[192:207], a[216:219], a[216:219], 0\n" \
"ds_read_b128 a[56:59], v59 offset:10240\n" \
"ds_read_b128 v[108:111], v63 offset:3072\n" \
"ds_read_b128 a[12:15], v58 offset:3072\n" \
"ds_read_b128 a[28:31], v58 offset:11264\n" \
"ds_read_b128 a[44:47], v59 offset:3072\n" \
"ds_read_b128 a[60:63], v59 offset:11264\n" \
"v_mfma_f32_32x32x16_bf16 v[208:223], a[216:219], a[216:219], 0\n" \
"s_waitcnt lgkmcnt(15)\n" \
"v_xor_b32_e32 v96, 0x80008000, v96\n" \
"v_mfma_f32_32x32x16_bf16 v[224:239], a[216:219], a[216:219], 0\n" \
"v_xor_b32_e32 v97, 0x80008000, v97\n" \
"v_xor_b32_e32 v98, 0x80008000, v98\n" \
"v_xor_b32_e32 v99, 0x80008000, v99\n" \
"ds_read_b128 a[160:163], v20 offset:0\n" \
"ds_read_b128 a[164:167], v20 offset:1024\n" \
"s_waitcnt lgkmcnt(15)\n" \
"v_xor_b32_e32 v100, 0x80008000, v100\n" \
"v_mfma_f32_32x32x16_bf16 v[240:255], a[216:219], a[216:219], 0\n" \
"v_xor_b32_e32 v101, 0x80008000, v101\n" \
"v_xor_b32_e32 v102, 0x80008000, v102\n" \
"v_xor_b32_e32 v103, 0x80008000, v103\n" \
"ds_read_b128 a[168:171], v20 offset:2048\n" \
"ds_read_b128 a[172:175], v20 offset:3072\n" \
"ds_read_b128 a[176:179], v20 offset:4096\n" \
"v_mfma_f32_32x32x16_bf16 a[192:207], a[216:219], a[216:219], 0\n" \
"s_waitcnt lgkmcnt(14)\n" \
"v_xor_b32_e32 v104, 0x80008000, v104\n" \
"v_xor_b32_e32 v105, 0x80008000, v105\n" \
"v_xor_b32_e32 v106, 0x80008000, v106\n" \
"v_xor_b32_e32 v107, 0x80008000, v107\n" \
"ds_read_b128 a[180:183], v20 offset:5120\n" \
"v_mfma_f32_16x16x32_bf16 v[32:35], v[96:99], a[0:3], 0\n" \
"s_waitcnt lgkmcnt(10)\n" \
"v_xor_b32_e32 v108, 0x80008000, v108\n" \
"v_mfma_f32_16x16x32_bf16 v[36:39], v[96:99], a[16:19], 0\n" \
"v_xor_b32_e32 v109, 0x80008000, v109\n" \
"v_xor_b32_e32 v110, 0x80008000, v110\n" \
"v_mfma_f32_16x16x32_bf16 v[40:43], v[96:99], a[32:35], 0\n" \
"v_xor_b32_e32 v111, 0x80008000, v111\n" \
"ds_read_b128 a[184:187], v20 offset:6144\n" \
"v_mfma_f32_16x16x32_bf16 v[44:47], v[96:99], a[48:51], 0\n" \
"ds_read_b128 a[188:191], v20 offset:7168\n" \
"v_add_u32_e32 v60, %[p4], v20\n" \
"v_mfma_f32_16x16x32_bf16 v[32:35], v[100:103], a[4:7], v[32:35]\n" \
"v_add_u32_e32 v60, 0x10000, v60\n" \
"ds_read_b128 a[128:131], v20 offset:8192\n" \
"v_mfma_f32_16x16x32_bf16 v[36:39], v[100:103], a[20:23], v[36:39]\n" \
"ds_read_b128 a[132:135], v20 offset:9216\n" \
"ds_read_b128 a[136:139], v20 offset:10240\n" \
"v_mfma_f32_16x16x32_bf16 v[40:43], v[100:103], a[36:39], v[40:43]\n" \
"ds_read_b128 a[140:143], v20 offset:11264\n" \
"ds_read_b128 a[144:147], v20 offset:12288\n" \
"v_mfma_f32_16x16x32_bf16 v[44:47], v[100:103], a[52:55], v[44:47]\n" \
"ds_read_b128 a[148:151], v20 offset:13312\n" \
"ds_read_b128 a[152:155], v20 offset:14336\n" \
"v_mfma_f32_16x16x32_bf16 v[32:35], v[104:107], a[8:11], v[32:35]\n" \
"ds_read_b128 a[156:159], v20 offset:15360\n" \
"ds_read_b128 a[64:67], v60 offset:16384\n" \
"v_mfma_f32_16x16x32_bf16 v[36:39], v[104:107], a[24:27], v[36:39]\n" \
"ds_read_b128 a[68:71], v60 offset:17408\n" \
"ds_read_b128 a[72:75], v60 offset:18432\n" \
"v_mfma_f32_16x16x32_bf16 v[40:43], v[104:107], a[40:43], v[40:43]\n" \
"ds_read_b128 a[76:79], v60 offset:19456\n" \
"ds_read_b128 a[80:83], v60 offset:24576\n" \
"v_mfma_f32_16x16x32_bf16 v[44:47], v[104:107], a[56:59], v[44:47]\n" \
"ds_read_b128 a[84:87], v60 offset:25600\n" \
"s_waitcnt lgkmcnt(15)\n" \
"v_mfma_f32_16x16x32_bf16 v[32:35], v[108:111], a[12:15], v[32:35]\n" \
"ds_read_b128 a[88:91], v60 offset:26624\n" \
"ds_read_b128 a[92:95], v60 offset:27648\n" \
"v_mfma_f32_16x16x32_bf16 v[36:39], v[108:111], a[28:31], v[36:39]\n" \
"ds_read_b128 a[96:99], v60 offset:0\n" \
"ds_read_b128 a[100:103], v60 offset:1024\n" \
"v_mfma_f32_16x16x32_bf16 v[40:43], v[108:111], a[44:47], v[40:43]\n" \
"ds_read_b128 a[104:107], v60 offset:2048\n" \
"ds_read_b128 a[108:111], v60 offset:3072\n" \
"v_mfma_f32_16x16x32_bf16 v[44:47], v[108:111], a[60:63], v[44:47]\n" \
"ds_read_b128 a[112:115], v60 offset:8192\n" \
"ds_read_b128 a[116:119], v60 offset:9216\n" \
"v_mfma_f32_16x16x32_bf16 v[64:67], a[160:163], a[0:3], v[32:35]\n" \
"ds_read_b128 a[120:123], v60 offset:10240\n" \
"ds_read_b128 a[124:127], v60 offset:11264\n" \
"v_mfma_f32_16x16x32_bf16 v[64:67], a[164:167], a[4:7], v[64:67]\n" \
"v_mov_b32_e32 v56, s62\n" \
"v_readlane_b32 s68, v11, 0\n" \
"v_mfma_f32_16x16x32_bf16 v[64:67], a[168:171], a[8:11], v[64:67]\n" \
"v_mov_b32_e32 v54, 0x3f803f80\n" \
"v_bfe_u32 v21, v20, 2, 5\n" \
"v_mfma_f32_16x16x32_bf16 v[64:67], a[172:175], a[12:15], v[64:67]\n" \
"s_mov_b32 s63, 0x30000000\n" \
"v_mov_b32_e32 v62, 0\n" \
"v_mfma_f32_16x16x32_bf16 v[72:75], a[172:175], a[28:31], v[36:39]\n" \
"v_accvgpr_write_b32 a212, v54\n" \
"v_accvgpr_write_b32 a208, 0\n" \
"v_mfma_f32_16x16x32_bf16 v[72:75], a[168:171], a[24:27], v[72:75]\n" \
"v_accvgpr_write_b32 a213, v54\n" \
"v_accvgpr_write_b32 a209, 0\n" \
"v_mfma_f32_16x16x32_bf16 v[72:75], a[164:167], a[20:23], v[72:75]\n" \
"v_exp_f32_e32 v64, v64\n" \
"v_mfma_f32_16x16x32_bf16 v[72:75], a[160:163], a[16:19], v[72:75]\n" \
"v_exp_f32_e32 v65, v65\n" \
"v_mfma_f32_16x16x32_bf16 v[68:71], a[176:179], a[0:3], v[32:35]\n" \
"v_exp_f32_e32 v66, v66\n" \
"v_mfma_f32_16x16x32_bf16 v[68:71], a[180:183], a[4:7], v[68:71]\n" \
"v_exp_f32_e32 v67, v67\n" \
"v_mfma_f32_16x16x32_bf16 v[68:71], a[184:187], a[8:11], v[68:71]\n" \
"v_cvt_pk_bf16_f32 v64, v64, v65\n" \
"s_waitcnt lgkmcnt(8)\n" \
"v_mfma_f32_16x16x32_bf16 v[68:71], a[188:191], a[12:15], v[68:71]\n" \
"v_exp_f32_e32 v72, v72\n" \
"v_mfma_f32_16x16x32_bf16 v[76:79], a[188:191], a[28:31], v[36:39]\n" \
"v_exp_f32_e32 v73, v73\n" \
"v_mfma_f32_16x16x32_bf16 v[76:79], a[184:187], a[24:27], v[76:79]\n" \
"v_exp_f32_e32 v74, v74\n" \
"v_mfma_f32_16x16x32_bf16 v[76:79], a[180:183], a[20:23], v[76:79]\n" \
"v_exp_f32_e32 v75, v75\n" \
"v_mfma_f32_16x16x32_bf16 v[76:79], a[176:179], a[16:19], v[76:79]\n" \
"v_exp_f32_e32 v68, v68\n" \
"v_mfma_f32_16x16x32_bf16 v[80:83], a[160:163], a[32:35], v[40:43]\n" \
"v_exp_f32_e32 v69, v69\n" \
"v_mfma_f32_16x16x32_bf16 v[80:83], a[164:167], a[36:39], v[80:83]\n" \
"v_exp_f32_e32 v70, v70\n" \
"v_mfma_f32_16x16x32_bf16 v[80:83], a[168:171], a[40:43], v[80:83]\n" \
"v_exp_f32_e32 v71, v71\n" \
"v_mfma_f32_16x16x32_bf16 v[80:83], a[172:175], a[44:47], v[80:83]\n" \
"v_cvt_pk_bf16_f32 v72, v72, v73\n" \
"v_cvt_pk_bf16_f32 v65, v66, v67\n" \
"v_mfma_f32_16x16x32_bf16 v[88:91], a[172:175], a[60:63], v[44:47]\n" \
"v_exp_f32_e32 v76, v76\n" \
"v_mfma_f32_16x16x32_bf16 v[88:91], a[168:171], a[56:59], v[88:91]\n" \
"v_exp_f32_e32 v77, v77\n" \
"v_mfma_f32_16x16x32_bf16 v[88:91], a[164:167], a[52:55], v[88:91]\n" \
"v_exp_f32_e32 v78, v78\n" \
"v_mfma_f32_16x16x32_bf16 v[88:91], a[160:163], a[48:51], v[88:91]\n" \
"v_exp_f32_e32 v79, v79\n" \
"v_mfma_f32_16x16x32_bf16 v[84:87], a[176:179], a[32:35], v[40:43]\n" \
"v_cvt_pk_bf16_f32 v73, v74, v75\n" \
"v_accvgpr_write_b32 a214, v54\n" \
"v_mfma_f32_16x16x32_bf16 v[84:87], a[180:183], a[36:39], v[84:87]\n" \
"v_accvgpr_write_b32 a210, 0\n" \
"v_accvgpr_write_b32 a215, v54\n" \
"v_mfma_f32_16x16x32_bf16 v[84:87], a[184:187], a[40:43], v[84:87]\n" \
"v_accvgpr_write_b32 a211, 0\n" \
"s_mov_b32 s64, 0x8000\n" \
"v_mfma_f32_16x16x32_bf16 v[84:87], a[188:191], a[44:47], v[84:87]\n" \
"v_add_u32_e32 v50, 0x6000, v55\n" \
"v_add_u32_e32 v51, 0xa000, v55\n" \
"v_mfma_f32_16x16x32_bf16 v[92:95], a[188:191], a[60:63], v[44:47]\n" \
"v_add_u32_e32 v53, 0x10000, v55\n" \
"v_add_u32_e32 v48, 0xc000, v20\n" \
"v_mfma_f32_16x16x32_bf16 v[92:95], a[184:187], a[56:59], v[92:95]\n" \
"v_add_u32_e32 v49, %[v0], v20\n" \
"v_accvgpr_write_b32 a220, v96\n" \
"v_mfma_f32_16x16x32_bf16 v[92:95], a[180:183], a[52:55], v[92:95]\n" \
"v_accvgpr_write_b32 a221, v97\n" \
"v_accvgpr_write_b32 a222, v98\n" \
"L_pf9:\n" \
"s_cbranch_execz L_pf13\n" \
"s_nop 0\n" \
"v_mfma_f32_16x16x32_bf16 v[92:95], a[176:179], a[48:51], v[92:95]\n" \
"v_accvgpr_write_b32 a223, v99\n" \
"v_accvgpr_write_b32 a224, v100\n" \
"s_waitcnt vmcnt(2) lgkmcnt(0)\n" \
"s_barrier\n" \
"s_mov_b64 exec, 1\n" \
"buffer_store_dword v56, off, s[56:59], s68 offset:0 sc1\n" \
"buffer_store_dword v56, off, s[56:59], %[fks] sc1\n" \
"s_mov_b64 exec, -1\n" \
"v_accvgpr_write_b32 a225, v101\n" \
"v_accvgpr_write_b32 a226, v102\n" \
"v_accvgpr_write_b32 a227, v103\n" \
"v_accvgpr_write_b32 a228, v104\n" \
"v_accvgpr_write_b32 a229, v105\n" \
"v_accvgpr_write_b32 a230, v106\n" \
"v_accvgpr_write_b32 a231, v107\n" \
"v_accvgpr_write_b32 a232, v108\n" \
"v_accvgpr_write_b32 a233, v109\n" \
"v_accvgpr_write_b32 a234, v110\n" \
"v_accvgpr_write_b32 a235, v111\n" \
"v_cvt_pk_bf16_f32 v66, v68, v69\n" \
"v_cvt_pk_bf16_f32 v67, v70, v71\n" \
"v_cvt_pk_bf16_f32 v74, v76, v77\n" \
"v_cvt_pk_bf16_f32 v75, v78, v79\n" \
"s_sub_u32 s66, %[n1], 1\n" \
"s_cbranch_scc1 L_tr_%=\n" \
"L_1_%=:\n" \
"v_mfma_f32_16x16x32_bf16 v[96:99], a[128:131], a[0:3], v[32:35]\n" \
"s_add_u32 s63, s63, s63\n" \
"s_cbranch_scc1 L_h1_%=\n" \
"L_hr1_%=:\n" \
"v_mfma_f32_16x16x32_bf16 v[96:99], a[132:135], a[4:7], v[96:99]\n" \
"s_xor_b32 m0, s65, 0x12000\n" \
"ds_read_b128 a[160:163], v49 offset:0\n" \
"v_mfma_f32_16x16x32_bf16 v[96:99], a[136:139], a[8:11], v[96:99]\n" \
"buffer_load_dwordx4 v50, %[srd], s64 offen offset:0 sc1 lds\n" \
"v_mfma_f32_16x16x32_bf16 v[96:99], a[140:143], a[12:15], v[96:99]\n" \
"ds_read_b128 a[164:167], v49 offset:1024\n" \
"ds_read_b128 a[168:171], v49 offset:2048\n" \
"v_mfma_f32_16x16x32_bf16 v[104:107], a[140:143], a[28:31], v[36:39]\n" \
"ds_read_b128 a[172:175], v49 offset:3072\n" \
"ds_read_b128 a[176:179], v49 offset:4096\n" \
"v_mfma_f32_16x16x32_bf16 v[104:107], a[136:139], a[24:27], v[104:107]\n" \
"ds_read_b128 a[180:183], v49 offset:5120\n" \
"ds_read_b128 a[184:187], v49 offset:6144\n" \
"v_mfma_f32_16x16x32_bf16 v[104:107], a[132:135], a[20:23], v[104:107]\n" \
"v_exp_f32_e32 v80, v80\n" \
"v_mfma_f32_16x16x32_bf16 v[104:107], a[128:131], a[16:19], v[104:107]\n" \
"v_exp_f32_e32 v81, v81\n" \
"v_mfma_f32_16x16x32_bf16 v[100:103], a[144:147], a[0:3], v[32:35]\n" \
"ds_read_b128 a[188:191], v49 offset:7168\n" \
"v_cvt_pk_bf16_f32 v80, v80, v81\n" \
"v_mfma_f32_16x16x32_bf16 v[100:103], a[148:151], a[4:7], v[100:103]\n" \
"v_exp_f32_e32 v82, v82\n" \
"v_mfma_f32_16x16x32_bf16 v[100:103], a[152:155], a[8:11], v[100:103]\n" \
"v_exp_f32_e32 v83, v83\n" \
"v_mfma_f32_16x16x32_bf16 v[100:103], a[156:159], a[12:15], v[100:103]\n" \
"v_exp_f32_e32 v84, v84\n" \
"v_mfma_f32_16x16x32_bf16 v[108:111], a[156:159], a[28:31], v[36:39]\n" \
"v_exp_f32_e32 v85, v85\n" \
"v_mfma_f32_16x16x32_bf16 v[108:111], a[152:155], a[24:27], v[108:111]\n" \
"v_exp_f32_e32 v86, v86\n" \
"v_mfma_f32_16x16x32_bf16 v[108:111], a[148:151], a[20:23], v[108:111]\n" \
"v_exp_f32_e32 v87, v87\n" \
"v_mfma_f32_16x16x32_bf16 v[108:111], a[144:147], a[16:19], v[108:111]\n" \
"buffer_load_dwordx4 v50, %[srd], s64 offen offset:1024 sc1 lds\n" \
"v_mfma_f32_16x16x32_bf16 v[112:115], a[128:131], a[32:35], v[40:43]\n" \
"s_xor_b32 m0, s65, 0x6000\n" \
"v_cvt_pk_bf16_f32 v81, v82, v83\n" \
"v_mfma_f32_16x16x32_bf16 v[112:115], a[132:135], a[36:39], v[112:115]\n" \
"v_cvt_pk_bf16_f32 v82, v84, v85\n" \
"v_cvt_pk_bf16_f32 v83, v86, v87\n" \
"v_mfma_f32_16x16x32_bf16 v[112:115], a[136:139], a[40:43], v[112:115]\n" \
"v_exp_f32_e32 v88, v88\n" \
"v_mfma_f32_16x16x32_bf16 v[112:115], a[140:143], a[44:47], v[112:115]\n" \
"v_exp_f32_e32 v89, v89\n" \
"v_mfma_f32_16x16x32_bf16 v[120:123], a[140:143], a[60:63], v[44:47]\n" \
"ds_read_b128 a[140:143], v48 offset:3072\n" \
"v_cvt_pk_bf16_f32 v88, v88, v89\n" \
"v_mfma_f32_16x16x32_bf16 v[120:123], a[136:139], a[56:59], v[120:123]\n" \
"v_exp_f32_e32 v90, v90\n" \
"v_mfma_f32_16x16x32_bf16 v[120:123], a[132:135], a[52:55], v[120:123]\n" \
"ds_read_b128 a[132:135], v48 offset:1024\n" \
"ds_read_b128 a[136:139], v48 offset:2048\n" \
"v_mfma_f32_16x16x32_bf16 v[120:123], a[128:131], a[48:51], v[120:123]\n" \
"v_exp_f32_e32 v91, v91\n" \
"v_mfma_f32_16x16x32_bf16 v[116:119], a[144:147], a[32:35], v[40:43]\n" \
"ds_read_b128 a[128:131], v48 offset:0\n" \
"v_cvt_pk_bf16_f32 v89, v90, v91\n" \
"v_mfma_f32_16x16x32_bf16 v[116:119], a[148:151], a[36:39], v[116:119]\n" \
"v_exp_f32_e32 v92, v92\n" \
"v_mfma_f32_16x16x32_bf16 v[116:119], a[152:155], a[40:43], v[116:119]\n" \
"v_exp_f32_e32 v93, v93\n" \
"v_mfma_f32_16x16x32_bf16 v[116:119], a[156:159], a[44:47], v[116:119]\n" \
"v_exp_f32_e32 v94, v94\n" \
"v_mfma_f32_16x16x32_bf16 v[124:127], a[156:159], a[60:63], v[44:47]\n" \
"ds_read_b128 a[156:159], v48 offset:7168\n" \
"v_cvt_pk_bf16_f32 v90, v92, v93\n" \
"v_mfma_f32_16x16x32_bf16 v[124:127], a[152:155], a[56:59], v[124:127]\n" \
"v_exp_f32_e32 v95, v95\n" \
"v_mfma_f32_16x16x32_bf16 v[124:127], a[148:151], a[52:55], v[124:127]\n" \
"ds_read_b128 a[148:151], v48 offset:5120\n" \
"ds_read_b128 a[152:155], v48 offset:6144\n" \
"v_mfma_f32_16x16x32_bf16 v[124:127], a[144:147], a[48:51], v[124:127]\n" \
"ds_read_b128 a[144:147], v48 offset:4096\n" \
"s_waitcnt lgkmcnt(8)\n" \
"v_mfma_f32_16x16x32_bf16 v[128:131], a[160:163], v[64:67], v[128:131]\n" \
"buffer_load_dwordx4 v51, %[srd], s64 offen offset:0 sc1 lds\n" \
"v_mfma_f32_16x16x32_bf16 v[132:135], a[164:167], v[64:67], v[132:135]\n" \
"v_exp_f32_e32 v96, v96\n" \
"v_mfma_f32_16x16x32_bf16 v[136:139], a[168:171], v[64:67], v[136:139]\n" \
"v_exp_f32_e32 v97, v97\n" \
"v_mfma_f32_16x16x32_bf16 v[140:143], a[172:175], v[64:67], v[140:143]\n" \
"v_cvt_pk_bf16_f32 v91, v94, v95\n" \
"v_cvt_pk_bf16_f32 v96, v96, v97\n" \
"v_mfma_f32_16x16x32_bf16 v[144:147], a[176:179], v[64:67], v[144:147]\n" \
"v_exp_f32_e32 v98, v98\n" \
"v_mfma_f32_16x16x32_bf16 v[148:151], a[180:183], v[64:67], v[148:151]\n" \
"v_exp_f32_e32 v99, v99\n" \
"v_mfma_f32_16x16x32_bf16 v[152:155], a[184:187], v[64:67], v[152:155]\n" \
"v_exp_f32_e32 v100, v100\n" \
"v_mfma_f32_16x16x32_bf16 v[156:159], a[188:191], v[64:67], v[156:159]\n" \
"v_exp_f32_e32 v101, v101\n" \
"v_mfma_f32_16x16x32_bf16 a[192:195], a[212:215], v[64:67], a[192:195]\n" \
"v_exp_f32_e32 v102, v102\n" \
"v_mfma_f32_16x16x32_bf16 v[160:163], a[160:163], v[72:75], v[160:163]\n" \
"v_exp_f32_e32 v103, v103\n" \
"v_mfma_f32_16x16x32_bf16 v[164:167], a[164:167], v[72:75], v[164:167]\n" \
"v_exp_f32_e32 v104, v104\n" \
"v_mfma_f32_16x16x32_bf16 v[168:171], a[168:171], v[72:75], v[168:171]\n" \
"v_exp_f32_e32 v105, v105\n" \
"v_mfma_f32_16x16x32_bf16 v[172:175], a[172:175], v[72:75], v[172:175]\n" \
"v_cvt_pk_bf16_f32 v97, v98, v99\n" \
"v_cvt_pk_bf16_f32 v98, v100, v101\n" \
"v_mfma_f32_16x16x32_bf16 v[176:179], a[176:179], v[72:75], v[176:179]\n" \
"v_cvt_pk_bf16_f32 v99, v102, v103\n" \
"v_cvt_pk_bf16_f32 v104, v104, v105\n" \
"v_mfma_f32_16x16x32_bf16 v[180:183], a[180:183], v[72:75], v[180:183]\n" \
"buffer_load_dwordx4 v51, %[srd], s64 offen offset:1024 sc1 lds\n" \
"v_mfma_f32_16x16x32_bf16 v[184:187], a[184:187], v[72:75], v[184:187]\n" \
"v_exp_f32_e32 v106, v106\n" \
"v_mfma_f32_16x16x32_bf16 v[188:191], a[188:191], v[72:75], v[188:191]\n" \
"v_exp_f32_e32 v107, v107\n" \
"v_mfma_f32_16x16x32_bf16 a[196:199], a[212:215], v[72:75], a[196:199]\n" \
"v_exp_f32_e32 v108, v108\n" \
"v_mfma_f32_16x16x32_bf16 v[192:195], a[160:163], v[80:83], v[192:195]\n" \
"v_exp_f32_e32 v109, v109\n" \
"v_mfma_f32_16x16x32_bf16 v[196:199], a[164:167], v[80:83], v[196:199]\n" \
"v_exp_f32_e32 v110, v110\n" \
"v_mfma_f32_16x16x32_bf16 v[200:203], a[168:171], v[80:83], v[200:203]\n" \
"v_exp_f32_e32 v111, v111\n" \
"v_mfma_f32_16x16x32_bf16 v[204:207], a[172:175], v[80:83], v[204:207]\n" \
"v_exp_f32_e32 v112, v112\n" \
"v_mfma_f32_16x16x32_bf16 v[208:211], a[176:179], v[80:83], v[208:211]\n" \
"v_exp_f32_e32 v113, v113\n" \
"v_mfma_f32_16x16x32_bf16 v[212:215], a[180:183], v[80:83], v[212:215]\n" \
"v_cvt_pk_bf16_f32 v105, v106, v107\n" \
"v_cvt_pk_bf16_f32 v106, v108, v109\n" \
"v_mfma_f32_16x16x32_bf16 v[216:219], a[184:187], v[80:83], v[216:219]\n" \
"v_cvt_pk_bf16_f32 v107, v110, v111\n" \
"v_cvt_pk_bf16_f32 v112, v112, v113\n" \
"v_mfma_f32_16x16x32_bf16 v[220:223], a[188:191], v[80:83], v[220:223]\n" \
"v_exp_f32_e32 v114, v114\n" \
"v_mfma_f32_16x16x32_bf16 a[200:203], a[212:215], v[80:83], a[200:203]\n" \
"v_exp_f32_e32 v115, v115\n" \
"v_mfma_f32_16x16x32_bf16 v[224:227], a[160:163], v[88:91], v[224:227]\n" \
"v_exp_f32_e32 v116, v116\n" \
"v_mfma_f32_16x16x32_bf16 v[228:231], a[164:167], v[88:91], v[228:231]\n" \
"v_exp_f32_e32 v117, v117\n" \
"v_mfma_f32_16x16x32_bf16 v[232:235], a[168:171], v[88:91], v[232:235]\n" \
"v_exp_f32_e32 v118, v118\n" \
"v_mfma_f32_16x16x32_bf16 v[236:239], a[172:175], v[88:91], v[236:239]\n" \
"v_exp_f32_e32 v119, v119\n" \
"v_mfma_f32_16x16x32_bf16 v[240:243], a[176:179], v[88:91], v[240:243]\n" \
"v_exp_f32_e32 v120, v120\n" \
"v_mfma_f32_16x16x32_bf16 v[244:247], a[180:183], v[88:91], v[244:247]\n" \
"v_exp_f32_e32 v121, v121\n" \
"v_mfma_f32_16x16x32_bf16 v[248:251], a[184:187], v[88:91], v[248:251]\n" \
"v_cvt_pk_bf16_f32 v113, v114, v115\n" \
"v_cvt_pk_bf16_f32 v114, v116, v117\n" \
"v_mfma_f32_16x16x32_bf16 v[252:255], a[188:191], v[88:91], v[252:255]\n" \
"v_cvt_pk_bf16_f32 v115, v118, v119\n" \
"v_cvt_pk_bf16_f32 v120, v120, v121\n" \
"v_mfma_f32_16x16x32_bf16 a[204:207], a[212:215], v[88:91], a[204:207]\n" \
"v_exp_f32_e32 v122, v122\n" \
"s_waitcnt vmcnt(4) lgkmcnt(0)\n" \
"s_barrier\n" \
"v_mfma_f32_16x16x32_bf16 v[64:67], a[128:131], a[0:3], v[32:35]\n" \
"s_xor_b32 m0, s65, 0x14000\n" \
"ds_read_b128 a[160:163], v49 offset:8192\n" \
"v_mfma_f32_16x16x32_bf16 v[64:67], a[132:135], a[4:7], v[64:67]\n" \
"buffer_load_dwordx4 v52, %[srd], s64 offen offset:0 sc1 lds\n" \
"v_mfma_f32_16x16x32_bf16 v[64:67], a[136:139], a[8:11], v[64:67]\n" \
"ds_read_b128 a[164:167], v49 offset:9216\n" \
"ds_read_b128 a[168:171], v49 offset:10240\n" \
"v_mfma_f32_16x16x32_bf16 v[64:67], a[140:143], a[12:15], v[64:67]\n" \
"ds_read_b128 a[172:175], v49 offset:11264\n" \
"ds_read_b128 a[176:179], v49 offset:12288\n" \
"v_mfma_f32_16x16x32_bf16 v[72:75], a[140:143], a[28:31], v[36:39]\n" \
"ds_read_b128 a[180:183], v49 offset:13312\n" \
"ds_read_b128 a[184:187], v49 offset:14336\n" \
"v_mfma_f32_16x16x32_bf16 v[72:75], a[136:139], a[24:27], v[72:75]\n" \
"v_exp_f32_e32 v123, v123\n" \
"v_mfma_f32_16x16x32_bf16 v[72:75], a[132:135], a[20:23], v[72:75]\n" \
"ds_read_b128 a[188:191], v49 offset:15360\n" \
"v_cvt_pk_bf16_f32 v121, v122, v123\n" \
"v_mfma_f32_16x16x32_bf16 v[72:75], a[128:131], a[16:19], v[72:75]\n" \
"v_exp_f32_e32 v124, v124\n" \
"v_mfma_f32_16x16x32_bf16 v[68:71], a[144:147], a[0:3], v[32:35]\n" \
"v_exp_f32_e32 v125, v125\n" \
"v_mfma_f32_16x16x32_bf16 v[68:71], a[148:151], a[4:7], v[68:71]\n" \
"v_exp_f32_e32 v126, v126\n" \
"v_mfma_f32_16x16x32_bf16 v[68:71], a[152:155], a[8:11], v[68:71]\n" \
"v_exp_f32_e32 v127, v127\n" \
"v_mfma_f32_16x16x32_bf16 v[68:71], a[156:159], a[12:15], v[68:71]\n" \
"v_cvt_pk_bf16_f32 v122, v124, v125\n" \
"v_cvt_pk_bf16_f32 v123, v126, v127\n" \
"v_mfma_f32_16x16x32_bf16 v[76:79], a[156:159], a[28:31], v[36:39]\n" \
"v_exp_f32_e32 v64, v64\n" \
"v_mfma_f32_16x16x32_bf16 v[76:79], a[152:155], a[24:27], v[76:79]\n" \
"v_exp_f32_e32 v65, v65\n" \
"v_mfma_f32_16x16x32_bf16 v[76:79], a[148:151], a[20:23], v[76:79]\n" \
"v_exp_f32_e32 v72, v72\n" \
"v_mfma_f32_16x16x32_bf16 v[76:79], a[144:147], a[16:19], v[76:79]\n" \
"buffer_load_dwordx4 v52, %[srd], s64 offen offset:1024 sc1 lds\n" \
"v_mfma_f32_16x16x32_bf16 v[80:83], a[128:131], a[32:35], v[40:43]\n" \
"s_mov_b32 m0, s65\n" \
"v_cvt_pk_bf16_f32 v64, v64, v65\n" \
"v_mfma_f32_16x16x32_bf16 v[80:83], a[132:135], a[36:39], v[80:83]\n" \
"v_exp_f32_e32 v73, v73\n" \
"v_mfma_f32_16x16x32_bf16 v[80:83], a[136:139], a[40:43], v[80:83]\n" \
"v_exp_f32_e32 v66, v66\n" \
"v_mfma_f32_16x16x32_bf16 v[80:83], a[140:143], a[44:47], v[80:83]\n" \
"v_exp_f32_e32 v67, v67\n" \
"v_mfma_f32_16x16x32_bf16 v[88:91], a[140:143], a[60:63], v[44:47]\n" \
"ds_read_b128 a[140:143], v48 offset:11264\n" \
"v_cvt_pk_bf16_f32 v72, v72, v73\n" \
"v_mfma_f32_16x16x32_bf16 v[88:91], a[136:139], a[56:59], v[88:91]\n" \
"ds_read_b128 a[136:139], v48 offset:10240\n" \
"v_cvt_pk_bf16_f32 v65, v66, v67\n" \
"v_mfma_f32_16x16x32_bf16 v[88:91], a[132:135], a[52:55], v[88:91]\n" \
"ds_read_b128 a[132:135], v48 offset:9216\n" \
"v_add_u32_e32 v49, 0x10000, v48\n" \
"v_mfma_f32_16x16x32_bf16 v[88:91], a[128:131], a[48:51], v[88:91]\n" \
"ds_read_b128 a[128:131], v48 offset:8192\n" \
"s_xor_b32 s65, s65, 0x4000\n" \
"v_mfma_f32_16x16x32_bf16 v[84:87], a[144:147], a[32:35], v[40:43]\n" \
"v_exp_f32_e32 v74, v74\n" \
"v_mfma_f32_16x16x32_bf16 v[84:87], a[148:151], a[36:39], v[84:87]\n" \
"v_exp_f32_e32 v75, v75\n" \
"v_mfma_f32_16x16x32_bf16 v[84:87], a[152:155], a[40:43], v[84:87]\n" \
"v_exp_f32_e32 v68, v68\n" \
"v_mfma_f32_16x16x32_bf16 v[84:87], a[156:159], a[44:47], v[84:87]\n" \
"v_exp_f32_e32 v69, v69\n" \
"v_mfma_f32_16x16x32_bf16 v[92:95], a[156:159], a[60:63], v[44:47]\n" \
"ds_read_b128 a[156:159], v48 offset:15360\n" \
"v_cvt_pk_bf16_f32 v73, v74, v75\n" \
"v_mfma_f32_16x16x32_bf16 v[92:95], a[152:155], a[56:59], v[92:95]\n" \
"ds_read_b128 a[152:155], v48 offset:14336\n" \
"v_cvt_pk_bf16_f32 v66, v68, v69\n" \
"v_mfma_f32_16x16x32_bf16 v[92:95], a[148:151], a[52:55], v[92:95]\n" \
"v_exp_f32_e32 v70, v70\n" \
"v_mfma_f32_16x16x32_bf16 v[92:95], a[144:147], a[48:51], v[92:95]\n" \
"ds_read_b128 a[144:147], v48 offset:12288\n" \
"s_waitcnt lgkmcnt(7)\n" \
"v_mfma_f32_16x16x32_bf16 v[128:131], a[160:163], v[96:99], v[128:131]\n" \
"buffer_load_dwordx4 v53, %[srd], s64 offen offset:0 sc1 lds\n" \
"v_mfma_f32_16x16x32_bf16 v[132:135], a[164:167], v[96:99], v[132:135]\n" \
"v_exp_f32_e32 v71, v71\n" \
"v_mfma_f32_16x16x32_bf16 v[136:139], a[168:171], v[96:99], v[136:139]\n" \
"ds_read_b128 a[148:151], v48 offset:13312\n" \
"v_cvt_pk_bf16_f32 v67, v70, v71\n" \
"v_mfma_f32_16x16x32_bf16 v[140:143], a[172:175], v[96:99], v[140:143]\n" \
"v_exp_f32_e32 v76, v76\n" \
"v_mfma_f32_16x16x32_bf16 v[144:147], a[176:179], v[96:99], v[144:147]\n" \
"v_exp_f32_e32 v77, v77\n" \
"v_mfma_f32_16x16x32_bf16 v[148:151], a[180:183], v[96:99], v[148:151]\n" \
"v_exp_f32_e32 v78, v78\n" \
"v_mfma_f32_16x16x32_bf16 v[152:155], a[184:187], v[96:99], v[152:155]\n" \
"v_exp_f32_e32 v79, v79\n" \
"v_mfma_f32_16x16x32_bf16 v[156:159], a[188:191], v[96:99], v[156:159]\n" \
"v_cvt_pk_bf16_f32 v74, v76, v77\n" \
"v_cvt_pk_bf16_f32 v75, v78, v79\n" \
"v_mfma_f32_16x16x32_bf16 a[192:195], a[212:215], v[96:99], a[192:195]\n" \
"v_xor_b32_e32 v48, 0x4000, v48\n" \
"v_mfma_f32_16x16x32_bf16 v[160:163], a[160:163], v[104:107], v[160:163]\n" \
"v_mfma_f32_16x16x32_bf16 v[164:167], a[164:167], v[104:107], v[164:167]\n" \
"v_mfma_f32_16x16x32_bf16 v[168:171], a[168:171], v[104:107], v[168:171]\n" \
"v_mfma_f32_16x16x32_bf16 v[172:175], a[172:175], v[104:107], v[172:175]\n" \
"v_mfma_f32_16x16x32_bf16 v[176:179], a[176:179], v[104:107], v[176:179]\n" \
"v_mfma_f32_16x16x32_bf16 v[180:183], a[180:183], v[104:107], v[180:183]\n" \
"buffer_load_dwordx4 v53, %[srd], s64 offen offset:1024 sc1 lds\n" \
"v_mfma_f32_16x16x32_bf16 v[184:187], a[184:187], v[104:107], v[184:187]\n" \
"s_add_u32 s64, s64, 0x8000\n" \
"s_sub_u32 s66, s66, 1\n" \
"v_mfma_f32_16x16x32_bf16 v[188:191], a[188:191], v[104:107], v[188:191]\n" \
"v_mfma_f32_16x16x32_bf16 a[196:199], a[212:215], v[104:107], a[196:199]\n" \
"v_mfma_f32_16x16x32_bf16 v[192:195], a[160:163], v[112:115], v[192:195]\n" \
"v_mfma_f32_16x16x32_bf16 v[196:199], a[164:167], v[112:115], v[196:199]\n" \
"v_mfma_f32_16x16x32_bf16 v[200:203], a[168:171], v[112:115], v[200:203]\n" \
"v_mfma_f32_16x16x32_bf16 v[204:207], a[172:175], v[112:115], v[204:207]\n" \
"v_mfma_f32_16x16x32_bf16 v[208:211], a[176:179], v[112:115], v[208:211]\n" \
"v_mfma_f32_16x16x32_bf16 v[212:215], a[180:183], v[112:115], v[212:215]\n" \
"v_mfma_f32_16x16x32_bf16 v[216:219], a[184:187], v[112:115], v[216:219]\n" \
"v_mfma_f32_16x16x32_bf16 v[220:223], a[188:191], v[112:115], v[220:223]\n" \
"v_mfma_f32_16x16x32_bf16 a[200:203], a[212:215], v[112:115], a[200:203]\n" \
"v_mfma_f32_16x16x32_bf16 v[224:227], a[160:163], v[120:123], v[224:227]\n" \
"v_mfma_f32_16x16x32_bf16 v[228:231], a[164:167], v[120:123], v[228:231]\n" \
"v_mfma_f32_16x16x32_bf16 v[232:235], a[168:171], v[120:123], v[232:235]\n" \
"v_mfma_f32_16x16x32_bf16 v[236:239], a[172:175], v[120:123], v[236:239]\n" \
"v_mfma_f32_16x16x32_bf16 v[240:243], a[176:179], v[120:123], v[240:243]\n" \
"v_mfma_f32_16x16x32_bf16 v[244:247], a[180:183], v[120:123], v[244:247]\n" \
"v_mfma_f32_16x16x32_bf16 v[248:251], a[184:187], v[120:123], v[248:251]\n" \
"v_mfma_f32_16x16x32_bf16 v[252:255], a[188:191], v[120:123], v[252:255]\n" \
"v_mfma_f32_16x16x32_bf16 a[204:207], a[212:215], v[120:123], a[204:207]\n" \
"s_waitcnt vmcnt(4) lgkmcnt(0)\n" \
"s_barrier\n" \
"s_cbranch_scc0 L_1_%=\n" \
"L_tr_%=:\n" \
"L_pf10:\n" \
"s_cbranch_execz L_pf14\n" \
"s_nop 0\n" \
"s_cmp_lg_u32 %[k2], 0\n" \
"s_cbranch_scc1 L_sw_%=\n" \
"v_mfma_f32_16x16x32_bf16 v[96:99], a[128:131], a[0:3], v[32:35]\n" \
"s_xor_b32 m0, s65, 0x12000\n" \
"s_mov_b32 s65, %[m2]\n" \
"v_mfma_f32_16x16x32_bf16 v[96:99], a[132:135], a[4:7], v[96:99]\n" \
"buffer_load_dwordx4 v50, %[srd], s64 offen offset:0 sc1 lds\n" \
"v_mfma_f32_16x16x32_bf16 v[96:99], a[136:139], a[8:11], v[96:99]\n" \
"v_add_u32_e32 v63, %[p4], v20\n" \
"s_add_u32 s67, %[t2], 40960\n" \
"v_mfma_f32_16x16x32_bf16 v[96:99], a[140:143], a[12:15], v[96:99]\n" \
"ds_read_b128 a[160:163], v49 offset:0\n" \
"ds_read_b128 a[164:167], v49 offset:1024\n" \
"v_mfma_f32_16x16x32_bf16 v[104:107], a[140:143], a[28:31], v[36:39]\n" \
"buffer_load_dwordx4 v50, %[srd], s64 offen offset:1024 sc1 lds\n" \
"v_mfma_f32_16x16x32_bf16 v[104:107], a[136:139], a[24:27], v[104:107]\n" \
"v_add_u32_e32 v50, 0x6000, v63\n" \
"s_xor_b32 m0, s65, 0x8000\n" \
"v_mfma_f32_16x16x32_bf16 v[104:107], a[132:135], a[20:23], v[104:107]\n" \
"ds_read_b128 a[168:171], v49 offset:2048\n" \
"ds_read_b128 a[172:175], v49 offset:3072\n" \
"v_mfma_f32_16x16x32_bf16 v[104:107], a[128:131], a[16:19], v[104:107]\n" \
"ds_read_b128 a[176:179], v49 offset:4096\n" \
"ds_read_b128 a[180:183], v49 offset:5120\n" \
"v_mfma_f32_16x16x32_bf16 v[100:103], a[144:147], a[0:3], v[32:35]\n" \
"buffer_load_dwordx4 v50, %[srd], s67 offen offset:0 sc1 lds\n" \
"v_mfma_f32_16x16x32_bf16 v[100:103], a[148:151], a[4:7], v[100:103]\n" \
"ds_read_b128 a[184:187], v49 offset:6144\n" \
"ds_read_b128 a[188:191], v49 offset:7168\n" \
"v_mfma_f32_16x16x32_bf16 v[100:103], a[152:155], a[8:11], v[100:103]\n" \
"s_mov_b32 s64, %[t2]\n" \
"v_add_u32_e32 v51, 0x12000, v63\n" \
"v_mfma_f32_16x16x32_bf16 v[100:103], a[156:159], a[12:15], v[100:103]\n" \
"v_exp_f32_e32 v80, v80\n" \
"v_mfma_f32_16x16x32_bf16 v[108:111], a[156:159], a[28:31], v[36:39]\n" \
"v_exp_f32_e32 v81, v81\n" \
"v_mfma_f32_16x16x32_bf16 v[108:111], a[152:155], a[24:27], v[108:111]\n" \
"buffer_load_dwordx4 v50, %[srd], s67 offen offset:1024 sc1 lds\n" \
"v_mfma_f32_16x16x32_bf16 v[108:111], a[148:151], a[20:23], v[108:111]\n" \
"v_exp_f32_e32 v82, v82\n" \
"v_mfma_f32_16x16x32_bf16 v[108:111], a[144:147], a[16:19], v[108:111]\n" \
"v_exp_f32_e32 v83, v83\n" \
"v_mfma_f32_16x16x32_bf16 v[112:115], a[128:131], a[32:35], v[40:43]\n" \
"v_cvt_pk_bf16_f32 v80, v80, v81\n" \
"v_cvt_pk_bf16_f32 v81, v82, v83\n" \
"v_mfma_f32_16x16x32_bf16 v[112:115], a[132:135], a[36:39], v[112:115]\n" \
"buffer_load_dwordx4 v50, %[srd], s67 offen offset:2048 sc1 lds\n" \
"v_mfma_f32_16x16x32_bf16 v[112:115], a[136:139], a[40:43], v[112:115]\n" \
"v_exp_f32_e32 v84, v84\n" \
"v_mfma_f32_16x16x32_bf16 v[112:115], a[140:143], a[44:47], v[112:115]\n" \
"v_exp_f32_e32 v85, v85\n" \
"v_mfma_f32_16x16x32_bf16 v[120:123], a[140:143], a[60:63], v[44:47]\n" \
"v_exp_f32_e32 v86, v86\n" \
"v_mfma_f32_16x16x32_bf16 v[120:123], a[136:139], a[56:59], v[120:123]\n" \
"v_exp_f32_e32 v87, v87\n" \
"v_mfma_f32_16x16x32_bf16 v[120:123], a[132:135], a[52:55], v[120:123]\n" \
"buffer_load_dwordx4 v50, %[srd], s67 offen offset:3072 sc1 lds\n" \
"v_mfma_f32_16x16x32_bf16 v[120:123], a[128:131], a[48:51], v[120:123]\n" \
"s_xor_b32 m0, s65, 0xa000\n" \
"v_cvt_pk_bf16_f32 v82, v84, v85\n" \
"v_mfma_f32_16x16x32_bf16 v[116:119], a[144:147], a[32:35], v[40:43]\n" \
"v_cvt_pk_bf16_f32 v83, v86, v87\n" \
"v_readlane_b32 s93, v11, 19\n" \
"v_mfma_f32_16x16x32_bf16 v[116:119], a[148:151], a[36:39], v[116:119]\n" \
"v_readlane_b32 s80, v11, 11\n" \
"v_readlane_b32 s81, v11, 12\n" \
"v_mfma_f32_16x16x32_bf16 v[116:119], a[152:155], a[40:43], v[116:119]\n" \
"buffer_load_dwordx4 v51, %[srd], s64 offen offset:0 sc1 lds\n" \
"v_mfma_f32_16x16x32_bf16 v[116:119], a[156:159], a[44:47], v[116:119]\n" \
"s_mov_b32 s82, 0x47fca0\n" \
"s_mov_b32 s83, 0x20000\n" \
"v_mfma_f32_16x16x32_bf16 v[124:127], a[156:159], a[60:63], v[44:47]\n" \
"v_readlane_b32 s92, v11, 24\n" \
"ds_read_b128 a[128:131], v48 offset:0\n" \
"v_mfma_f32_16x16x32_bf16 v[124:127], a[152:155], a[56:59], v[124:127]\n" \
"ds_read_b128 a[132:135], v48 offset:1024\n" \
"ds_read_b128 a[136:139], v48 offset:2048\n" \
"v_mfma_f32_16x16x32_bf16 v[124:127], a[148:151], a[52:55], v[124:127]\n" \
"ds_read_b128 a[140:143], v48 offset:3072\n" \
"ds_read_b128 a[148:151], v48 offset:5120\n" \
"v_mfma_f32_16x16x32_bf16 v[124:127], a[144:147], a[48:51], v[124:127]\n" \
"ds_read_b128 a[144:147], v48 offset:4096\n" \
"s_waitcnt lgkmcnt(6)\n" \
"v_mfma_f32_16x16x32_bf16 v[128:131], a[160:163], v[64:67], v[128:131]\n" \
"buffer_load_dwordx4 v51, %[srd], s64 offen offset:1024 sc1 lds\n" \
"v_mfma_f32_16x16x32_bf16 v[132:135], a[164:167], v[64:67], v[132:135]\n" \
"ds_read_b128 a[152:155], v48 offset:6144\n" \
"ds_read_b128 a[156:159], v48 offset:7168\n" \
"v_mfma_f32_16x16x32_bf16 v[136:139], a[168:171], v[64:67], v[136:139]\n" \
"v_exp_f32_e32 v88, v88\n" \
"v_mfma_f32_16x16x32_bf16 v[140:143], a[172:175], v[64:67], v[140:143]\n" \
"buffer_load_dwordx4 v51, %[srd], s64 offen offset:2048 sc1 lds\n" \
"v_mfma_f32_16x16x32_bf16 v[144:147], a[176:179], v[64:67], v[144:147]\n" \
"v_exp_f32_e32 v89, v89\n" \
"v_mfma_f32_16x16x32_bf16 v[148:151], a[180:183], v[64:67], v[148:151]\n" \
"v_exp_f32_e32 v90, v90\n" \
"v_mfma_f32_16x16x32_bf16 v[152:155], a[184:187], v[64:67], v[152:155]\n" \
"v_exp_f32_e32 v91, v91\n" \
"v_mfma_f32_16x16x32_bf16 v[156:159], a[188:191], v[64:67], v[156:159]\n" \
"buffer_load_dwordx4 v51, %[srd], s64 offen offset:3072 sc1 lds\n" \
"v_mfma_f32_16x16x32_bf16 a[192:195], a[212:215], v[64:67], a[192:195]\n" \
"s_add_u32 m0, s93, 0x0\n" \
"v_cvt_pk_bf16_f32 v88, v88, v89\n" \
"v_mfma_f32_16x16x32_bf16 v[160:163], a[160:163], v[72:75], v[160:163]\n" \
"v_exp_f32_e32 v92, v92\n" \
"v_mfma_f32_16x16x32_bf16 v[164:167], a[164:167], v[72:75], v[164:167]\n" \
"v_exp_f32_e32 v93, v93\n" \
"v_mfma_f32_16x16x32_bf16 v[168:171], a[168:171], v[72:75], v[168:171]\n" \
"v_exp_f32_e32 v94, v94\n" \
"v_mfma_f32_16x16x32_bf16 v[172:175], a[172:175], v[72:75], v[172:175]\n" \
"buffer_load_dwordx4 v0, s[80:83], s92 offen offset:0 lds\n" \
"v_mfma_f32_16x16x32_bf16 v[176:179], a[176:179], v[72:75], v[176:179]\n" \
"v_exp_f32_e32 v95, v95\n" \
"v_mfma_f32_16x16x32_bf16 v[180:183], a[180:183], v[72:75], v[180:183]\n" \
"v_cvt_pk_bf16_f32 v89, v90, v91\n" \
"v_cvt_pk_bf16_f32 v90, v92, v93\n" \
"v_mfma_f32_16x16x32_bf16 v[184:187], a[184:187], v[72:75], v[184:187]\n" \
"v_cvt_pk_bf16_f32 v91, v94, v95\n" \
"v_add_u32_e32 v52, 0x14000, v63\n" \
"v_mfma_f32_16x16x32_bf16 v[188:191], a[188:191], v[72:75], v[188:191]\n" \
"buffer_load_dwordx4 v1, s[80:83], s92 offen offset:1024 lds\n" \
"v_mfma_f32_16x16x32_bf16 a[196:199], a[212:215], v[72:75], a[196:199]\n" \
"v_add_u32_e32 v53, 0x20000, v63\n" \
"s_mov_b32 s66, %[n2]\n" \
"v_mfma_f32_16x16x32_bf16 v[192:195], a[160:163], v[80:83], v[192:195]\n" \
"v_exp_f32_e32 v96, v96\n" \
"v_mfma_f32_16x16x32_bf16 v[196:199], a[164:167], v[80:83], v[196:199]\n" \
"v_exp_f32_e32 v97, v97\n" \
"v_mfma_f32_16x16x32_bf16 v[200:203], a[168:171], v[80:83], v[200:203]\n" \
"v_exp_f32_e32 v98, v98\n" \
"v_mfma_f32_16x16x32_bf16 v[204:207], a[172:175], v[80:83], v[204:207]\n" \
"buffer_load_dwordx4 v2, s[80:83], s92 offen offset:2048 lds\n" \
"v_mfma_f32_16x16x32_bf16 v[208:211], a[176:179], v[80:83], v[208:211]\n" \
"v_exp_f32_e32 v99, v99\n" \
"v_mfma_f32_16x16x32_bf16 v[212:215], a[180:183], v[80:83], v[212:215]\n" \
"v_exp_f32_e32 v100, v100\n" \
"v_mfma_f32_16x16x32_bf16 v[216:219], a[184:187], v[80:83], v[216:219]\n" \
"v_exp_f32_e32 v101, v101\n" \
"v_mfma_f32_16x16x32_bf16 v[220:223], a[188:191], v[80:83], v[220:223]\n" \
"buffer_load_dwordx4 v3, s[80:83], s92 offen offset:3072 lds\n" \
"v_mfma_f32_16x16x32_bf16 a[200:203], a[212:215], v[80:83], a[200:203]\n" \
"v_exp_f32_e32 v102, v102\n" \
"v_mfma_f32_16x16x32_bf16 v[224:227], a[160:163], v[88:91], v[224:227]\n" \
"v_exp_f32_e32 v103, v103\n" \
"v_mfma_f32_16x16x32_bf16 v[228:231], a[164:167], v[88:91], v[228:231]\n" \
"v_exp_f32_e32 v104, v104\n" \
"v_mfma_f32_16x16x32_bf16 v[232:235], a[168:171], v[88:91], v[232:235]\n" \
"v_exp_f32_e32 v105, v105\n" \
"v_mfma_f32_16x16x32_bf16 v[236:239], a[172:175], v[88:91], v[236:239]\n" \
"v_cvt_pk_bf16_f32 v96, v96, v97\n" \
"v_cvt_pk_bf16_f32 v97, v98, v99\n" \
"v_mfma_f32_16x16x32_bf16 v[240:243], a[176:179], v[88:91], v[240:243]\n" \
"v_cvt_pk_bf16_f32 v98, v100, v101\n" \
"v_cvt_pk_bf16_f32 v99, v102, v103\n" \
"v_mfma_f32_16x16x32_bf16 v[244:247], a[180:183], v[88:91], v[244:247]\n" \
"v_exp_f32_e32 v106, v106\n" \
"v_mfma_f32_16x16x32_bf16 v[248:251], a[184:187], v[88:91], v[248:251]\n" \
"v_exp_f32_e32 v107, v107\n" \
"v_mfma_f32_16x16x32_bf16 v[252:255], a[188:191], v[88:91], v[252:255]\n" \
"v_exp_f32_e32 v108, v108\n" \
"v_mfma_f32_16x16x32_bf16 a[204:207], a[212:215], v[88:91], a[204:207]\n" \
"v_cvt_pk_bf16_f32 v104, v104, v105\n" \
"v_cvt_pk_bf16_f32 v105, v106, v107\n" \
"s_waitcnt vmcnt(14) lgkmcnt(0)\n" \
"s_mov_b64 exec, 0\n" \
"s_cmp_lg_u32 %[p4], 0\n" \
"s_cbranch_scc1 L_pf20\n" \
"s_branch L_pf19\n" \
"L_retB:\n" \
"s_mov_b64 exec, -1\n" \
"s_barrier\n" \
"v_mfma_f32_16x16x32_bf16 v[64:67], a[128:131], a[0:3], v[32:35]\n" \
"s_xor_b32 m0, s65, 0x18000\n" \
"ds_read_b128 a[160:163], v49 offset:8192\n" \
"v_mfma_f32_16x16x32_bf16 v[64:67], a[132:135], a[4:7], v[64:67]\n" \
"buffer_load_dwordx4 v52, %[srd], s64 offen offset:0 sc1 lds\n" \
"v_mfma_f32_16x16x32_bf16 v[64:67], a[136:139], a[8:11], v[64:67]\n" \
"ds_read_b128 a[164:167], v49 offset:9216\n" \
"ds_read_b128 a[168:171], v49 offset:10240\n" \
"v_mfma_f32_16x16x32_bf16 v[64:67], a[140:143], a[12:15], v[64:67]\n" \
"ds_read_b128 a[172:175], v49 offset:11264\n" \
"ds_read_b128 a[176:179], v49 offset:12288\n" \
"v_mfma_f32_16x16x32_bf16 v[72:75], a[140:143], a[28:31], v[36:39]\n" \
"buffer_load_dwordx4 v52, %[srd], s64 offen offset:1024 sc1 lds\n" \
"v_mfma_f32_16x16x32_bf16 v[72:75], a[136:139], a[24:27], v[72:75]\n" \
"ds_read_b128 a[180:183], v49 offset:13312\n" \
"ds_read_b128 a[184:187], v49 offset:14336\n" \
"v_mfma_f32_16x16x32_bf16 v[72:75], a[132:135], a[20:23], v[72:75]\n" \
"v_exp_f32_e32 v109, v109\n" \
"v_mfma_f32_16x16x32_bf16 v[72:75], a[128:131], a[16:19], v[72:75]\n" \
"ds_read_b128 a[188:191], v49 offset:15360\n" \
"v_cvt_pk_bf16_f32 v106, v108, v109\n" \
"v_mfma_f32_16x16x32_bf16 v[68:71], a[144:147], a[0:3], v[32:35]\n" \
"v_exp_f32_e32 v110, v110\n" \
"v_mfma_f32_16x16x32_bf16 v[68:71], a[148:151], a[4:7], v[68:71]\n" \
"buffer_load_dwordx4 v52, %[srd], s64 offen offset:2048 sc1 lds\n" \
"v_mfma_f32_16x16x32_bf16 v[68:71], a[152:155], a[8:11], v[68:71]\n" \
"v_exp_f32_e32 v111, v111\n" \
"v_mfma_f32_16x16x32_bf16 v[68:71], a[156:159], a[12:15], v[68:71]\n" \
"v_cvt_pk_bf16_f32 v107, v110, v111\n" \
"v_add_u32_e32 v49, 0x10000, v48\n" \
"v_mfma_f32_16x16x32_bf16 v[76:79], a[156:159], a[28:31], v[36:39]\n" \
"v_exp_f32_e32 v112, v112\n" \
"v_mfma_f32_16x16x32_bf16 v[76:79], a[152:155], a[24:27], v[76:79]\n" \
"v_exp_f32_e32 v113, v113\n" \
"v_mfma_f32_16x16x32_bf16 v[76:79], a[148:151], a[20:23], v[76:79]\n" \
"buffer_load_dwordx4 v52, %[srd], s64 offen offset:3072 sc1 lds\n" \
"v_mfma_f32_16x16x32_bf16 v[76:79], a[144:147], a[16:19], v[76:79]\n" \
"s_mov_b32 m0, s65\n" \
"v_cvt_pk_bf16_f32 v112, v112, v113\n" \
"v_mfma_f32_16x16x32_bf16 v[80:83], a[128:131], a[32:35], v[40:43]\n" \
"v_exp_f32_e32 v114, v114\n" \
"v_mfma_f32_16x16x32_bf16 v[80:83], a[132:135], a[36:39], v[80:83]\n" \
"v_exp_f32_e32 v115, v115\n" \
"v_mfma_f32_16x16x32_bf16 v[80:83], a[136:139], a[40:43], v[80:83]\n" \
"v_exp_f32_e32 v116, v116\n" \
"v_mfma_f32_16x16x32_bf16 v[80:83], a[140:143], a[44:47], v[80:83]\n" \
"buffer_load_dwordx4 v53, %[srd], s64 offen offset:0 sc1 lds\n" \
"v_mfma_f32_16x16x32_bf16 v[88:91], a[140:143], a[60:63], v[44:47]\n" \
"v_exp_f32_e32 v117, v117\n" \
"v_mfma_f32_16x16x32_bf16 v[88:91], a[136:139], a[56:59], v[88:91]\n" \
"v_exp_f32_e32 v118, v118\n" \
"v_mfma_f32_16x16x32_bf16 v[88:91], a[132:135], a[52:55], v[88:91]\n" \
"v_exp_f32_e32 v119, v119\n" \
"v_mfma_f32_16x16x32_bf16 v[88:91], a[128:131], a[48:51], v[88:91]\n" \
"buffer_load_dwordx4 v53, %[srd], s64 offen offset:1024 sc1 lds\n" \
"v_mfma_f32_16x16x32_bf16 v[84:87], a[144:147], a[32:35], v[40:43]\n" \
"v_cvt_pk_bf16_f32 v113, v114, v115\n" \
"v_cvt_pk_bf16_f32 v114, v116, v117\n" \
"v_mfma_f32_16x16x32_bf16 v[84:87], a[148:151], a[36:39], v[84:87]\n" \
"v_cvt_pk_bf16_f32 v115, v118, v119\n" \
"ds_read_b128 a[128:131], v48 offset:8192\n" \
"v_mfma_f32_16x16x32_bf16 v[84:87], a[152:155], a[40:43], v[84:87]\n" \
"ds_read_b128 a[132:135], v48 offset:9216\n" \
"ds_read_b128 a[136:139], v48 offset:10240\n" \
"v_mfma_f32_16x16x32_bf16 v[84:87], a[156:159], a[44:47], v[84:87]\n" \
"ds_read_b128 a[140:143], v48 offset:11264\n" \
"s_xor_b32 s65, s65, 0x8000\n" \
"v_mfma_f32_16x16x32_bf16 v[92:95], a[156:159], a[60:63], v[44:47]\n" \
"buffer_load_dwordx4 v53, %[srd], s64 offen offset:2048 sc1 lds\n" \
"v_mfma_f32_16x16x32_bf16 v[92:95], a[152:155], a[56:59], v[92:95]\n" \
"ds_read_b128 a[152:155], v48 offset:14336\n" \
"ds_read_b128 a[156:159], v48 offset:15360\n" \
"v_mfma_f32_16x16x32_bf16 v[92:95], a[148:151], a[52:55], v[92:95]\n" \
"v_exp_f32_e32 v120, v120\n" \
"v_mfma_f32_16x16x32_bf16 v[92:95], a[144:147], a[48:51], v[92:95]\n" \
"ds_read_b128 a[144:147], v48 offset:12288\n" \
"s_waitcnt lgkmcnt(7)\n" \
"v_mfma_f32_16x16x32_bf16 v[128:131], a[160:163], v[96:99], v[128:131]\n" \
"v_exp_f32_e32 v121, v121\n" \
"v_mfma_f32_16x16x32_bf16 v[132:135], a[164:167], v[96:99], v[132:135]\n" \
"buffer_load_dwordx4 v53, %[srd], s64 offen offset:3072 sc1 lds\n" \
"v_mfma_f32_16x16x32_bf16 v[136:139], a[168:171], v[96:99], v[136:139]\n" \
"s_add_u32 m0, s93, 0x1000\n" \
"ds_read_b128 a[148:151], v48 offset:13312\n" \
"v_mfma_f32_16x16x32_bf16 v[140:143], a[172:175], v[96:99], v[140:143]\n" \
"v_exp_f32_e32 v122, v122\n" \
"v_mfma_f32_16x16x32_bf16 v[144:147], a[176:179], v[96:99], v[144:147]\n" \
"v_exp_f32_e32 v123, v123\n" \
"v_mfma_f32_16x16x32_bf16 v[148:151], a[180:183], v[96:99], v[148:151]\n" \
"v_cvt_pk_bf16_f32 v120, v120, v121\n" \
"v_cvt_pk_bf16_f32 v121, v122, v123\n" \
"v_mfma_f32_16x16x32_bf16 v[152:155], a[184:187], v[96:99], v[152:155]\n" \
"buffer_load_dwordx4 v4, s[80:83], s92 offen offset:0 lds\n" \
"v_mfma_f32_16x16x32_bf16 v[156:159], a[188:191], v[96:99], v[156:159]\n" \
"v_exp_f32_e32 v124, v124\n" \
"L_pf11:\n" \
"s_cbranch_execz L_pf15\n" \
"s_nop 0\n" \
"v_mfma_f32_16x16x32_bf16 a[192:195], a[212:215], v[96:99], a[192:195]\n" \
"v_exp_f32_e32 v125, v125\n" \
"v_mfma_f32_16x16x32_bf16 v[160:163], a[160:163], v[104:107], v[160:163]\n" \
"v_exp_f32_e32 v126, v126\n" \
"v_mfma_f32_16x16x32_bf16 v[164:167], a[164:167], v[104:107], v[164:167]\n" \
"buffer_load_dwordx4 v5, s[80:83], s92 offen offset:1024 lds\n" \
"v_mfma_f32_16x16x32_bf16 v[168:171], a[168:171], v[104:107], v[168:171]\n" \
"v_exp_f32_e32 v127, v127\n" \
"v_mfma_f32_16x16x32_bf16 v[172:175], a[172:175], v[104:107], v[172:175]\n" \
"v_cvt_pk_bf16_f32 v122, v124, v125\n" \
"v_cvt_pk_bf16_f32 v123, v126, v127\n" \
"v_mfma_f32_16x16x32_bf16 v[176:179], a[176:179], v[104:107], v[176:179]\n" \
"v_exp_f32_e32 v64, v64\n" \
"v_mfma_f32_16x16x32_bf16 v[180:183], a[180:183], v[104:107], v[180:183]\n" \
"v_exp_f32_e32 v65, v65\n" \
"v_mfma_f32_16x16x32_bf16 v[184:187], a[184:187], v[104:107], v[184:187]\n" \
"buffer_load_dwordx4 v6, s[80:83], s92 offen offset:2048 lds\n" \
"v_mfma_f32_16x16x32_bf16 v[188:191], a[188:191], v[104:107], v[188:191]\n" \
"v_exp_f32_e32 v72, v72\n" \
"v_mfma_f32_16x16x32_bf16 a[196:199], a[212:215], v[104:107], a[196:199]\n" \
"v_exp_f32_e32 v73, v73\n" \
"v_mfma_f32_16x16x32_bf16 v[192:195], a[160:163], v[112:115], v[192:195]\n" \
"v_exp_f32_e32 v66, v66\n" \
"v_mfma_f32_16x16x32_bf16 v[196:199], a[164:167], v[112:115], v[196:199]\n" \
"v_exp_f32_e32 v67, v67\n" \
"v_mfma_f32_16x16x32_bf16 v[200:203], a[168:171], v[112:115], v[200:203]\n" \
"buffer_load_dwordx4 v7, s[80:83], s92 offen offset:3072 lds\n" \
"v_mfma_f32_16x16x32_bf16 v[204:207], a[172:175], v[112:115], v[204:207]\n" \
"s_add_u32 m0, s93, 0x2000\n" \
"v_cvt_pk_bf16_f32 v64, v64, v65\n" \
"v_mfma_f32_16x16x32_bf16 v[208:211], a[176:179], v[112:115], v[208:211]\n" \
"v_exp_f32_e32 v74, v74\n" \
"v_mfma_f32_16x16x32_bf16 v[212:215], a[180:183], v[112:115], v[212:215]\n" \
"v_exp_f32_e32 v75, v75\n" \
"v_mfma_f32_16x16x32_bf16 v[216:219], a[184:187], v[112:115], v[216:219]\n" \
"v_exp_f32_e32 v68, v68\n" \
"v_mfma_f32_16x16x32_bf16 v[220:223], a[188:191], v[112:115], v[220:223]\n" \
"buffer_load_dwordx4 v8, s[80:83], s92 offen offset:0 lds\n" \
"v_mfma_f32_16x16x32_bf16 a[200:203], a[212:215], v[112:115], a[200:203]\n" \
"v_exp_f32_e32 v69, v69\n" \
"v_mfma_f32_16x16x32_bf16 v[224:227], a[160:163], v[120:123], v[224:227]\n" \
"v_exp_f32_e32 v70, v70\n" \
"v_mfma_f32_16x16x32_bf16 v[228:231], a[164:167], v[120:123], v[228:231]\n" \
"v_exp_f32_e32 v71, v71\n" \
"v_mfma_f32_16x16x32_bf16 v[232:235], a[168:171], v[120:123], v[232:235]\n" \
"v_cvt_pk_bf16_f32 v72, v72, v73\n" \
"v_cvt_pk_bf16_f32 v65, v66, v67\n" \
"v_mfma_f32_16x16x32_bf16 v[236:239], a[172:175], v[120:123], v[236:239]\n" \
"v_exp_f32_e32 v76, v76\n" \
"v_mfma_f32_16x16x32_bf16 v[240:243], a[176:179], v[120:123], v[240:243]\n" \
"v_exp_f32_e32 v77, v77\n" \
"v_mfma_f32_16x16x32_bf16 v[244:247], a[180:183], v[120:123], v[244:247]\n" \
"v_exp_f32_e32 v78, v78\n" \
"v_mfma_f32_16x16x32_bf16 v[248:251], a[184:187], v[120:123], v[248:251]\n" \
"v_exp_f32_e32 v79, v79\n" \
"v_mfma_f32_16x16x32_bf16 v[252:255], a[188:191], v[120:123], v[252:255]\n" \
"v_cvt_pk_bf16_f32 v73, v74, v75\n" \
"v_cvt_pk_bf16_f32 v66, v68, v69\n" \
"v_mfma_f32_16x16x32_bf16 a[204:207], a[212:215], v[120:123], a[204:207]\n" \
"v_cvt_pk_bf16_f32 v67, v70, v71\n" \
"v_cvt_pk_bf16_f32 v74, v76, v77\n" \
"s_waitcnt vmcnt(13) lgkmcnt(0)\n" \
"s_barrier\n" \
"v_cvt_pk_bf16_f32 v75, v78, v79\n" \
"v_add_u32_e32 v48, %[k2], v20\n" \
"s_add_u32 s64, s64, 0x10000\n" \
"L_2_%=:\n" \
"v_mfma_f32_16x16x32_bf16 v[96:99], a[128:131], a[64:67], v[32:35]\n" \
"s_add_u32 s63, s63, s63\n" \
"s_cbranch_scc1 L_h2_%=\n" \
"L_hr2_%=:\n" \
"v_mfma_f32_16x16x32_bf16 v[96:99], a[132:135], a[68:71], v[96:99]\n" \
"s_xor_b32 m0, s65, 0x12000\n" \
"ds_read_b128 a[160:163], v49 offset:0\n" \
"v_mfma_f32_16x16x32_bf16 v[96:99], a[136:139], a[72:75], v[96:99]\n" \
"buffer_load_dwordx4 v50, %[srd], s64 offen offset:0 sc1 lds\n" \
"v_mfma_f32_16x16x32_bf16 v[96:99], a[140:143], a[76:79], v[96:99]\n" \
"ds_read_b128 a[164:167], v49 offset:1024\n" \
"ds_read_b128 a[168:171], v49 offset:2048\n" \
"v_mfma_f32_16x16x32_bf16 v[104:107], a[140:143], a[92:95], v[36:39]\n" \
"ds_read_b128 a[172:175], v49 offset:3072\n" \
"ds_read_b128 a[176:179], v49 offset:4096\n" \
"v_mfma_f32_16x16x32_bf16 v[104:107], a[136:139], a[88:91], v[104:107]\n" \
"ds_read_b128 a[180:183], v49 offset:5120\n" \
"ds_read_b128 a[184:187], v49 offset:6144\n" \
"v_mfma_f32_16x16x32_bf16 v[104:107], a[132:135], a[84:87], v[104:107]\n" \
"v_exp_f32_e32 v80, v80\n" \
"v_mfma_f32_16x16x32_bf16 v[104:107], a[128:131], a[80:83], v[104:107]\n" \
"buffer_load_dwordx4 v50, %[srd], s64 offen offset:1024 sc1 lds\n" \
"v_mfma_f32_16x16x32_bf16 v[100:103], a[144:147], a[64:67], v[32:35]\n" \
"v_exp_f32_e32 v81, v81\n" \
"v_mfma_f32_16x16x32_bf16 v[100:103], a[148:151], a[68:71], v[100:103]\n" \
"ds_read_b128 a[188:191], v49 offset:7168\n" \
"v_cvt_pk_bf16_f32 v80, v80, v81\n" \
"v_mfma_f32_16x16x32_bf16 v[100:103], a[152:155], a[72:75], v[100:103]\n" \
"v_exp_f32_e32 v82, v82\n" \
"v_mfma_f32_16x16x32_bf16 v[100:103], a[156:159], a[76:79], v[100:103]\n" \
"v_exp_f32_e32 v83, v83\n" \
"v_mfma_f32_16x16x32_bf16 v[108:111], a[156:159], a[92:95], v[36:39]\n" \
"v_exp_f32_e32 v84, v84\n" \
"v_mfma_f32_16x16x32_bf16 v[108:111], a[152:155], a[88:91], v[108:111]\n" \
"v_exp_f32_e32 v85, v85\n" \
"v_mfma_f32_16x16x32_bf16 v[108:111], a[148:151], a[84:87], v[108:111]\n" \
"v_exp_f32_e32 v86, v86\n" \
"v_mfma_f32_16x16x32_bf16 v[108:111], a[144:147], a[80:83], v[108:111]\n" \
"buffer_load_dwordx4 v50, %[srd], s64 offen offset:2048 sc1 lds\n" \
"v_mfma_f32_16x16x32_bf16 v[112:115], a[128:131], a[96:99], v[40:43]\n" \
"v_exp_f32_e32 v87, v87\n" \
"v_mfma_f32_16x16x32_bf16 v[112:115], a[132:135], a[100:103], v[112:115]\n" \
"v_exp_f32_e32 v88, v88\n" \
"v_mfma_f32_16x16x32_bf16 v[112:115], a[136:139], a[104:107], v[112:115]\n" \
"v_exp_f32_e32 v89, v89\n" \
"v_mfma_f32_16x16x32_bf16 v[112:115], a[140:143], a[108:111], v[112:115]\n" \
"v_cvt_pk_bf16_f32 v81, v82, v83\n" \
"v_cvt_pk_bf16_f32 v82, v84, v85\n" \
"v_mfma_f32_16x16x32_bf16 v[120:123], a[140:143], a[124:127], v[44:47]\n" \
"v_cvt_pk_bf16_f32 v83, v86, v87\n" \
"ds_read_b128 a[140:143], v48 offset:3072\n" \
"v_mfma_f32_16x16x32_bf16 v[120:123], a[136:139], a[120:123], v[120:123]\n" \
"ds_read_b128 a[136:139], v48 offset:2048\n" \
"v_cvt_pk_bf16_f32 v88, v88, v89\n" \
"v_mfma_f32_16x16x32_bf16 v[120:123], a[132:135], a[116:119], v[120:123]\n" \
"v_exp_f32_e32 v90, v90\n" \
"v_mfma_f32_16x16x32_bf16 v[120:123], a[128:131], a[112:115], v[120:123]\n" \
"buffer_load_dwordx4 v50, %[srd], s64 offen offset:3072 sc1 lds\n" \
"v_mfma_f32_16x16x32_bf16 v[116:119], a[144:147], a[96:99], v[40:43]\n" \
"s_xor_b32 m0, s65, 0xa000\n" \
"ds_read_b128 a[128:131], v48 offset:0\n" \
"v_mfma_f32_16x16x32_bf16 v[116:119], a[148:151], a[100:103], v[116:119]\n" \
"v_exp_f32_e32 v91, v91\n" \
"v_mfma_f32_16x16x32_bf16 v[116:119], a[152:155], a[104:107], v[116:119]\n" \
"ds_read_b128 a[132:135], v48 offset:1024\n" \
"v_cvt_pk_bf16_f32 v89, v90, v91\n" \
"v_mfma_f32_16x16x32_bf16 v[116:119], a[156:159], a[108:111], v[116:119]\n" \
"v_exp_f32_e32 v92, v92\n" \
"v_mfma_f32_16x16x32_bf16 v[124:127], a[156:159], a[124:127], v[44:47]\n" \
"v_exp_f32_e32 v93, v93\n" \
"v_mfma_f32_16x16x32_bf16 v[124:127], a[152:155], a[120:123], v[124:127]\n" \
"ds_read_b128 a[152:155], v48 offset:6144\n" \
"ds_read_b128 a[156:159], v48 offset:7168\n" \
"v_mfma_f32_16x16x32_bf16 v[124:127], a[148:151], a[116:119], v[124:127]\n" \
"ds_read_b128 a[148:151], v48 offset:5120\n" \
"v_cvt_pk_bf16_f32 v90, v92, v93\n" \
"v_mfma_f32_16x16x32_bf16 v[124:127], a[144:147], a[112:115], v[124:127]\n" \
"ds_read_b128 a[144:147], v48 offset:4096\n" \
"s_waitcnt lgkmcnt(8)\n" \
"v_mfma_f32_16x16x32_bf16 v[128:131], a[160:163], v[64:67], v[128:131]\n" \
"buffer_load_dwordx4 v51, %[srd], s64 offen offset:0 sc1 lds\n" \
"v_mfma_f32_16x16x32_bf16 v[132:135], a[164:167], v[64:67], v[132:135]\n" \
"v_exp_f32_e32 v94, v94\n" \
"v_mfma_f32_16x16x32_bf16 v[136:139], a[168:171], v[64:67], v[136:139]\n" \
"v_exp_f32_e32 v95, v95\n" \
"v_mfma_f32_16x16x32_bf16 v[140:143], a[172:175], v[64:67], v[140:143]\n" \
"v_exp_f32_e32 v96, v96\n" \
"v_mfma_f32_16x16x32_bf16 v[144:147], a[176:179], v[64:67], v[144:147]\n" \
"v_exp_f32_e32 v97, v97\n" \
"v_mfma_f32_16x16x32_bf16 v[148:151], a[180:183], v[64:67], v[148:151]\n" \
"v_cvt_pk_bf16_f32 v91, v94, v95\n" \
"v_cvt_pk_bf16_f32 v96, v96, v97\n" \
"v_mfma_f32_16x16x32_bf16 v[152:155], a[184:187], v[64:67], v[152:155]\n" \
"buffer_load_dwordx4 v51, %[srd], s64 offen offset:1024 sc1 lds\n" \
"v_mfma_f32_16x16x32_bf16 v[156:159], a[188:191], v[64:67], v[156:159]\n" \
"v_exp_f32_e32 v98, v98\n" \
"v_mfma_f32_16x16x32_bf16 a[192:195], a[212:215], v[64:67], a[192:195]\n" \
"v_exp_f32_e32 v99, v99\n" \
"v_mfma_f32_16x16x32_bf16 v[160:163], a[160:163], v[72:75], v[160:163]\n" \
"v_exp_f32_e32 v100, v100\n" \
"v_mfma_f32_16x16x32_bf16 v[164:167], a[164:167], v[72:75], v[164:167]\n" \
"v_exp_f32_e32 v101, v101\n" \
"v_mfma_f32_16x16x32_bf16 v[168:171], a[168:171], v[72:75], v[168:171]\n" \
"v_exp_f32_e32 v102, v102\n" \
"v_mfma_f32_16x16x32_bf16 v[172:175], a[172:175], v[72:75], v[172:175]\n" \
"v_exp_f32_e32 v103, v103\n" \
"v_mfma_f32_16x16x32_bf16 v[176:179], a[176:179], v[72:75], v[176:179]\n" \
"v_exp_f32_e32 v104, v104\n" \
"v_mfma_f32_16x16x32_bf16 v[180:183], a[180:183], v[72:75], v[180:183]\n" \
"buffer_load_dwordx4 v51, %[srd], s64 offen offset:2048 sc1 lds\n" \
"v_mfma_f32_16x16x32_bf16 v[184:187], a[184:187], v[72:75], v[184:187]\n" \
"v_exp_f32_e32 v105, v105\n" \
"v_mfma_f32_16x16x32_bf16 v[188:191], a[188:191], v[72:75], v[188:191]\n" \
"v_cvt_pk_bf16_f32 v97, v98, v99\n" \
"v_cvt_pk_bf16_f32 v98, v100, v101\n" \
"v_mfma_f32_16x16x32_bf16 a[196:199], a[212:215], v[72:75], a[196:199]\n" \
"v_cvt_pk_bf16_f32 v99, v102, v103\n" \
"v_cvt_pk_bf16_f32 v104, v104, v105\n" \
"v_mfma_f32_16x16x32_bf16 v[192:195], a[160:163], v[80:83], v[192:195]\n" \
"v_exp_f32_e32 v106, v106\n" \
"v_mfma_f32_16x16x32_bf16 v[196:199], a[164:167], v[80:83], v[196:199]\n" \
"v_exp_f32_e32 v107, v107\n" \
"v_mfma_f32_16x16x32_bf16 v[200:203], a[168:171], v[80:83], v[200:203]\n" \
"v_exp_f32_e32 v108, v108\n" \
"v_mfma_f32_16x16x32_bf16 v[204:207], a[172:175], v[80:83], v[204:207]\n" \
"v_exp_f32_e32 v109, v109\n" \
"v_mfma_f32_16x16x32_bf16 v[208:211], a[176:179], v[80:83], v[208:211]\n" \
"buffer_load_dwordx4 v51, %[srd], s64 offen offset:3072 sc1 lds\n" \
"v_mfma_f32_16x16x32_bf16 v[212:215], a[180:183], v[80:83], v[212:215]\n" \
"v_exp_f32_e32 v110, v110\n" \
"v_mfma_f32_16x16x32_bf16 v[216:219], a[184:187], v[80:83], v[216:219]\n" \
"v_exp_f32_e32 v111, v111\n" \
"v_mfma_f32_16x16x32_bf16 v[220:223], a[188:191], v[80:83], v[220:223]\n" \
"v_exp_f32_e32 v112, v112\n" \
"v_mfma_f32_16x16x32_bf16 a[200:203], a[212:215], v[80:83], a[200:203]\n" \
"v_exp_f32_e32 v113, v113\n" \
"v_mfma_f32_16x16x32_bf16 v[224:227], a[160:163], v[88:91], v[224:227]\n" \
"v_cvt_pk_bf16_f32 v105, v106, v107\n" \
"v_cvt_pk_bf16_f32 v106, v108, v109\n" \
"v_mfma_f32_16x16x32_bf16 v[228:231], a[164:167], v[88:91], v[228:231]\n" \
"v_cvt_pk_bf16_f32 v107, v110, v111\n" \
"v_cvt_pk_bf16_f32 v112, v112, v113\n" \
"v_mfma_f32_16x16x32_bf16 v[232:235], a[168:171], v[88:91], v[232:235]\n" \
"v_exp_f32_e32 v114, v114\n" \
"v_mfma_f32_16x16x32_bf16 v[236:239], a[172:175], v[88:91], v[236:239]\n" \
"v_exp_f32_e32 v115, v115\n" \
"v_mfma_f32_16x16x32_bf16 v[240:243], a[176:179], v[88:91], v[240:243]\n" \
"v_exp_f32_e32 v116, v116\n" \
"v_mfma_f32_16x16x32_bf16 v[244:247], a[180:183], v[88:91], v[244:247]\n" \
"v_exp_f32_e32 v117, v117\n" \
"v_mfma_f32_16x16x32_bf16 v[248:251], a[184:187], v[88:91], v[248:251]\n" \
"v_exp_f32_e32 v118, v118\n" \
"v_mfma_f32_16x16x32_bf16 v[252:255], a[188:191], v[88:91], v[252:255]\n" \
"v_exp_f32_e32 v119, v119\n" \
"v_mfma_f32_16x16x32_bf16 a[204:207], a[212:215], v[88:91], a[204:207]\n" \
"v_cvt_pk_bf16_f32 v113, v114, v115\n" \
"v_cvt_pk_bf16_f32 v114, v116, v117\n" \
"s_waitcnt vmcnt(8) lgkmcnt(0)\n" \
"s_barrier\n" \
"v_mfma_f32_16x16x32_bf16 v[64:67], a[128:131], a[64:67], v[32:35]\n" \
"s_xor_b32 m0, s65, 0x18000\n" \
"ds_read_b128 a[160:163], v49 offset:8192\n" \
"v_mfma_f32_16x16x32_bf16 v[64:67], a[132:135], a[68:71], v[64:67]\n" \
"buffer_load_dwordx4 v52, %[srd], s64 offen offset:0 sc1 lds\n" \
"v_mfma_f32_16x16x32_bf16 v[64:67], a[136:139], a[72:75], v[64:67]\n" \
"ds_read_b128 a[164:167], v49 offset:9216\n" \
"ds_read_b128 a[168:171], v49 offset:10240\n" \
"v_mfma_f32_16x16x32_bf16 v[64:67], a[140:143], a[76:79], v[64:67]\n" \
"ds_read_b128 a[172:175], v49 offset:11264\n" \
"ds_read_b128 a[176:179], v49 offset:12288\n" \
"v_mfma_f32_16x16x32_bf16 v[72:75], a[140:143], a[92:95], v[36:39]\n" \
"ds_read_b128 a[180:183], v49 offset:13312\n" \
"ds_read_b128 a[184:187], v49 offset:14336\n" \
"v_mfma_f32_16x16x32_bf16 v[72:75], a[136:139], a[88:91], v[72:75]\n" \
"ds_read_b128 a[188:191], v49 offset:15360\n" \
"v_cvt_pk_bf16_f32 v115, v118, v119\n" \
"v_mfma_f32_16x16x32_bf16 v[72:75], a[132:135], a[84:87], v[72:75]\n" \
"v_exp_f32_e32 v120, v120\n" \
"v_mfma_f32_16x16x32_bf16 v[72:75], a[128:131], a[80:83], v[72:75]\n" \
"buffer_load_dwordx4 v52, %[srd], s64 offen offset:1024 sc1 lds\n" \
"v_mfma_f32_16x16x32_bf16 v[68:71], a[144:147], a[64:67], v[32:35]\n" \
"v_exp_f32_e32 v121, v121\n" \
"v_mfma_f32_16x16x32_bf16 v[68:71], a[148:151], a[68:71], v[68:71]\n" \
"v_exp_f32_e32 v122, v122\n" \
"v_mfma_f32_16x16x32_bf16 v[68:71], a[152:155], a[72:75], v[68:71]\n" \
"v_exp_f32_e32 v123, v123\n" \
"v_mfma_f32_16x16x32_bf16 v[68:71], a[156:159], a[76:79], v[68:71]\n" \
"v_cvt_pk_bf16_f32 v120, v120, v121\n" \
"v_cvt_pk_bf16_f32 v121, v122, v123\n" \
"v_mfma_f32_16x16x32_bf16 v[76:79], a[156:159], a[92:95], v[36:39]\n" \
"v_exp_f32_e32 v124, v124\n" \
"v_mfma_f32_16x16x32_bf16 v[76:79], a[152:155], a[88:91], v[76:79]\n" \
"v_exp_f32_e32 v125, v125\n" \
"v_mfma_f32_16x16x32_bf16 v[76:79], a[148:151], a[84:87], v[76:79]\n" \
"v_exp_f32_e32 v126, v126\n" \
"v_mfma_f32_16x16x32_bf16 v[76:79], a[144:147], a[80:83], v[76:79]\n" \
"buffer_load_dwordx4 v52, %[srd], s64 offen offset:2048 sc1 lds\n" \
"v_mfma_f32_16x16x32_bf16 v[80:83], a[128:131], a[96:99], v[40:43]\n" \
"v_exp_f32_e32 v127, v127\n" \
"v_mfma_f32_16x16x32_bf16 v[80:83], a[132:135], a[100:103], v[80:83]\n" \
"v_cvt_pk_bf16_f32 v122, v124, v125\n" \
"v_cvt_pk_bf16_f32 v123, v126, v127\n" \
"v_mfma_f32_16x16x32_bf16 v[80:83], a[136:139], a[104:107], v[80:83]\n" \
"v_exp_f32_e32 v64, v64\n" \
"v_mfma_f32_16x16x32_bf16 v[80:83], a[140:143], a[108:111], v[80:83]\n" \
"v_exp_f32_e32 v65, v65\n" \
"v_mfma_f32_16x16x32_bf16 v[88:91], a[140:143], a[124:127], v[44:47]\n" \
"ds_read_b128 a[140:143], v48 offset:11264\n" \
"v_cvt_pk_bf16_f32 v64, v64, v65\n" \
"v_mfma_f32_16x16x32_bf16 v[88:91], a[136:139], a[120:123], v[88:91]\n" \
"ds_read_b128 a[136:139], v48 offset:10240\n" \
"v_add_u32_e32 v49, 0x10000, v48\n" \
"v_mfma_f32_16x16x32_bf16 v[88:91], a[132:135], a[116:119], v[88:91]\n" \
"v_exp_f32_e32 v72, v72\n" \
"v_mfma_f32_16x16x32_bf16 v[88:91], a[128:131], a[112:115], v[88:91]\n" \
"buffer_load_dwordx4 v52, %[srd], s64 offen offset:3072 sc1 lds\n" \
"v_mfma_f32_16x16x32_bf16 v[84:87], a[144:147], a[96:99], v[40:43]\n" \
"s_mov_b32 m0, s65\n" \
"ds_read_b128 a[128:131], v48 offset:8192\n" \
"v_mfma_f32_16x16x32_bf16 v[84:87], a[148:151], a[100:103], v[84:87]\n" \
"ds_read_b128 a[132:135], v48 offset:9216\n" \
"s_xor_b32 s65, s65, 0x8000\n" \
"v_mfma_f32_16x16x32_bf16 v[84:87], a[152:155], a[104:107], v[84:87]\n" \
"v_exp_f32_e32 v73, v73\n" \
"v_mfma_f32_16x16x32_bf16 v[84:87], a[156:159], a[108:111], v[84:87]\n" \
"v_exp_f32_e32 v66, v66\n" \
"v_mfma_f32_16x16x32_bf16 v[92:95], a[156:159], a[124:127], v[44:47]\n" \
"ds_read_b128 a[156:159], v48 offset:15360\n" \
"v_cvt_pk_bf16_f32 v72, v72, v73\n" \
"v_mfma_f32_16x16x32_bf16 v[92:95], a[152:155], a[120:123], v[92:95]\n" \
"v_exp_f32_e32 v67, v67\n" \
"v_mfma_f32_16x16x32_bf16 v[92:95], a[148:151], a[116:119], v[92:95]\n" \
"ds_read_b128 a[148:151], v48 offset:13312\n" \
"ds_read_b128 a[152:155], v48 offset:14336\n" \
"v_mfma_f32_16x16x32_bf16 v[92:95], a[144:147], a[112:115], v[92:95]\n" \
"ds_read_b128 a[144:147], v48 offset:12288\n" \
"s_waitcnt lgkmcnt(8)\n" \
"v_mfma_f32_16x16x32_bf16 v[128:131], a[160:163], v[96:99], v[128:131]\n" \
"buffer_load_dwordx4 v53, %[srd], s64 offen offset:0 sc1 lds\n" \
"v_mfma_f32_16x16x32_bf16 v[132:135], a[164:167], v[96:99], v[132:135]\n" \
"v_exp_f32_e32 v74, v74\n" \
"v_mfma_f32_16x16x32_bf16 v[136:139], a[168:171], v[96:99], v[136:139]\n" \
"v_exp_f32_e32 v75, v75\n" \
"v_mfma_f32_16x16x32_bf16 v[140:143], a[172:175], v[96:99], v[140:143]\n" \
"v_exp_f32_e32 v68, v68\n" \
"v_mfma_f32_16x16x32_bf16 v[144:147], a[176:179], v[96:99], v[144:147]\n" \
"v_exp_f32_e32 v69, v69\n" \
"v_mfma_f32_16x16x32_bf16 v[148:151], a[180:183], v[96:99], v[148:151]\n" \
"v_exp_f32_e32 v70, v70\n" \
"v_mfma_f32_16x16x32_bf16 v[152:155], a[184:187], v[96:99], v[152:155]\n" \
"buffer_load_dwordx4 v53, %[srd], s64 offen offset:1024 sc1 lds\n" \
"v_mfma_f32_16x16x32_bf16 v[156:159], a[188:191], v[96:99], v[156:159]\n" \
"v_exp_f32_e32 v71, v71\n" \
"v_mfma_f32_16x16x32_bf16 a[192:195], a[212:215], v[96:99], a[192:195]\n" \
"v_exp_f32_e32 v76, v76\n" \
"v_mfma_f32_16x16x32_bf16 v[160:163], a[160:163], v[104:107], v[160:163]\n" \
"v_exp_f32_e32 v77, v77\n" \
"v_mfma_f32_16x16x32_bf16 v[164:167], a[164:167], v[104:107], v[164:167]\n" \
"v_exp_f32_e32 v78, v78\n" \
"v_mfma_f32_16x16x32_bf16 v[168:171], a[168:171], v[104:107], v[168:171]\n" \
"v_exp_f32_e32 v79, v79\n" \
"v_mfma_f32_16x16x32_bf16 v[172:175], a[172:175], v[104:107], v[172:175]\n" \
"v_cvt_pk_bf16_f32 v65, v66, v67\n" \
"v_cvt_pk_bf16_f32 v73, v74, v75\n" \
"v_mfma_f32_16x16x32_bf16 v[176:179], a[176:179], v[104:107], v[176:179]\n" \
"v_cvt_pk_bf16_f32 v66, v68, v69\n" \
"v_cvt_pk_bf16_f32 v67, v70, v71\n" \
"v_mfma_f32_16x16x32_bf16 v[180:183], a[180:183], v[104:107], v[180:183]\n" \
"buffer_load_dwordx4 v53, %[srd], s64 offen offset:2048 sc1 lds\n" \
"v_mfma_f32_16x16x32_bf16 v[184:187], a[184:187], v[104:107], v[184:187]\n" \
"v_cvt_pk_bf16_f32 v74, v76, v77\n" \
"v_cvt_pk_bf16_f32 v75, v78, v79\n" \
"v_mfma_f32_16x16x32_bf16 v[188:191], a[188:191], v[104:107], v[188:191]\n" \
"v_xor_b32_e32 v48, 0x8000, v48\n" \
"v_mfma_f32_16x16x32_bf16 a[196:199], a[212:215], v[104:107], a[196:199]\n" \
"v_mfma_f32_16x16x32_bf16 v[192:195], a[160:163], v[112:115], v[192:195]\n" \
"v_mfma_f32_16x16x32_bf16 v[196:199], a[164:167], v[112:115], v[196:199]\n" \
"v_mfma_f32_16x16x32_bf16 v[200:203], a[168:171], v[112:115], v[200:203]\n" \
"v_mfma_f32_16x16x32_bf16 v[204:207], a[172:175], v[112:115], v[204:207]\n" \
"v_mfma_f32_16x16x32_bf16 v[208:211], a[176:179], v[112:115], v[208:211]\n" \
"buffer_load_dwordx4 v53, %[srd], s64 offen offset:3072 sc1 lds\n" \
"v_mfma_f32_16x16x32_bf16 v[212:215], a[180:183], v[112:115], v[212:215]\n" \
"s_add_u32 s64, s64, 0x10000\n" \
"s_sub_u32 s66, s66, 1\n" \
"v_mfma_f32_16x16x32_bf16 v[216:219], a[184:187], v[112:115], v[216:219]\n" \
"v_mfma_f32_16x16x32_bf16 v[220:223], a[188:191], v[112:115], v[220:223]\n" \
"v_mfma_f32_16x16x32_bf16 a[200:203], a[212:215], v[112:115], a[200:203]\n" \
"v_mfma_f32_16x16x32_bf16 v[224:227], a[160:163], v[120:123], v[224:227]\n" \
"v_mfma_f32_16x16x32_bf16 v[228:231], a[164:167], v[120:123], v[228:231]\n" \
"v_mfma_f32_16x16x32_bf16 v[232:235], a[168:171], v[120:123], v[232:235]\n" \
"v_mfma_f32_16x16x32_bf16 v[236:239], a[172:175], v[120:123], v[236:239]\n" \
"v_mfma_f32_16x16x32_bf16 v[240:243], a[176:179], v[120:123], v[240:243]\n" \
"v_mfma_f32_16x16x32_bf16 v[244:247], a[180:183], v[120:123], v[244:247]\n" \
"v_mfma_f32_16x16x32_bf16 v[248:251], a[184:187], v[120:123], v[248:251]\n" \
"v_mfma_f32_16x16x32_bf16 v[252:255], a[188:191], v[120:123], v[252:255]\n" \
"v_mfma_f32_16x16x32_bf16 a[204:207], a[212:215], v[120:123], a[204:207]\n" \
"s_waitcnt vmcnt(8) lgkmcnt(0)\n" \
"s_barrier\n" \
"s_cbranch_scc0 L_2_%=\n" \
"s_cmp_lg_u32 %[k2], 0\n" \
"s_cbranch_scc1 L_f1_%=\n" \
"v_bfe_u32 v58, v20, 2, 7\n" \
"v_readlane_b32 s96, v11, 1\n" \
"ds_read_b128 a[160:163], v49 offset:0\n" \
"ds_read_b128 a[164:167], v49 offset:1024\n" \
"ds_read_b128 a[168:171], v49 offset:2048\n" \
"ds_read_b128 a[172:175], v49 offset:3072\n" \
"ds_read_b128 a[176:179], v49 offset:4096\n" \
"buffer_load_dword v55, v58, s[56:59], s96 offen sc1\n" \
"ds_read_b128 a[180:183], v49 offset:5120\n" \
"v_mfma_f32_16x16x32_bf16 v[120:123], a[128:131], a[112:115], v[44:47]\n" \
"s_add_u32 s70, s61, 0x0\n" \
"s_add_u32 s71, s61, 0x49000\n" \
"ds_read_b128 a[184:187], v49 offset:6144\n" \
"v_mfma_f32_16x16x32_bf16 v[120:123], a[132:135], a[116:119], v[120:123]\n" \
"ds_read_b128 a[188:191], v49 offset:7168\n" \
"v_add_u32_e32 v54, 0x49000, v20\n" \
"v_mfma_f32_16x16x32_bf16 v[120:123], a[136:139], a[120:123], v[120:123]\n" \
"v_exp_f32_e32 v80, v80\n" \
"v_mfma_f32_16x16x32_bf16 v[120:123], a[140:143], a[124:127], v[120:123]\n" \
"buffer_load_dwordx4 a[220:223], v20, s[56:59], s60 offen offset:0 sc1\n" \
"v_mfma_f32_16x16x32_bf16 v[124:127], a[156:159], a[124:127], v[44:47]\n" \
"v_exp_f32_e32 v81, v81\n" \
"v_mfma_f32_16x16x32_bf16 v[124:127], a[152:155], a[120:123], v[124:127]\n" \
"v_exp_f32_e32 v82, v82\n" \
"v_mfma_f32_16x16x32_bf16 v[124:127], a[148:151], a[116:119], v[124:127]\n" \
"v_exp_f32_e32 v83, v83\n" \
"v_mfma_f32_16x16x32_bf16 v[124:127], a[144:147], a[112:115], v[124:127]\n" \
"buffer_load_dwordx4 a[236:239], v54, s[56:59], s60 offen offset:0 sc1\n" \
"s_waitcnt lgkmcnt(7)\n" \
"v_mfma_f32_16x16x32_bf16 v[128:131], a[160:163], v[64:67], v[128:131]\n" \
"v_cvt_pk_bf16_f32 v80, v80, v81\n" \
"s_waitcnt lgkmcnt(6)\n" \
"v_mfma_f32_16x16x32_bf16 v[132:135], a[164:167], v[64:67], v[132:135]\n" \
"v_cvt_pk_bf16_f32 v81, v82, v83\n" \
"s_waitcnt lgkmcnt(5)\n" \
"v_mfma_f32_16x16x32_bf16 v[136:139], a[168:171], v[64:67], v[136:139]\n" \
"v_and_b32_e32 v80, v12, v80\n" \
"s_waitcnt lgkmcnt(4)\n" \
"v_mfma_f32_16x16x32_bf16 v[140:143], a[172:175], v[64:67], v[140:143]\n" \
"v_and_b32_e32 v81, v13, v81\n" \
"s_waitcnt lgkmcnt(3)\n" \
"v_mfma_f32_16x16x32_bf16 v[144:147], a[176:179], v[64:67], v[144:147]\n" \
"ds_read_b32 v60, v18 offset:0\n" \
"buffer_load_dwordx4 a[224:227], v20, s[56:59], s60 offen offset:1024 sc1\n" \
"s_waitcnt lgkmcnt(3)\n" \
"v_mfma_f32_16x16x32_bf16 v[148:151], a[180:183], v[64:67], v[148:151]\n" \
"ds_read_b32 v61, v18 offset:128\n" \
"s_waitcnt lgkmcnt(3)\n" \
"v_mfma_f32_16x16x32_bf16 v[152:155], a[184:187], v[64:67], v[152:155]\n" \
"ds_read_b128 a[0:3], v49 offset:8192\n" \
"s_waitcnt lgkmcnt(3)\n" \
"v_mfma_f32_16x16x32_bf16 v[156:159], a[188:191], v[64:67], v[156:159]\n" \
"v_exp_f32_e32 v84, v84\n" \
"buffer_load_dwordx4 a[240:243], v54, s[56:59], s60 offen offset:1024 sc1\n" \
"v_mfma_f32_16x16x32_bf16 a[192:195], a[212:215], v[64:67], a[192:195]\n" \
"v_exp_f32_e32 v85, v85\n" \
"v_mfma_f32_16x16x32_bf16 v[160:163], a[160:163], v[72:75], v[160:163]\n" \
"v_exp_f32_e32 v86, v86\n" \
"v_mfma_f32_16x16x32_bf16 v[164:167], a[164:167], v[72:75], v[164:167]\n" \
"v_exp_f32_e32 v87, v87\n" \
"v_mfma_f32_16x16x32_bf16 v[168:171], a[168:171], v[72:75], v[168:171]\n" \
"buffer_load_dwordx4 a[228:231], v20, s[56:59], s60 offen offset:2048 sc1\n" \
"v_mfma_f32_16x16x32_bf16 v[172:175], a[172:175], v[72:75], v[172:175]\n" \
"v_cvt_pk_bf16_f32 v82, v84, v85\n" \
"v_cvt_pk_bf16_f32 v83, v86, v87\n" \
"v_mfma_f32_16x16x32_bf16 v[176:179], a[176:179], v[72:75], v[176:179]\n" \
"v_and_b32_e32 v82, v14, v82\n" \
"v_and_b32_e32 v83, v15, v83\n" \
"v_mfma_f32_16x16x32_bf16 v[180:183], a[180:183], v[72:75], v[180:183]\n" \
"ds_read_b128 a[4:7], v49 offset:9216\n" \
"ds_read_b128 a[8:11], v49 offset:10240\n" \
"v_mfma_f32_16x16x32_bf16 v[184:187], a[184:187], v[72:75], v[184:187]\n" \
"buffer_load_dwordx4 a[244:247], v54, s[56:59], s60 offen offset:2048 sc1\n" \
"v_mfma_f32_16x16x32_bf16 v[188:191], a[188:191], v[72:75], v[188:191]\n" \
"ds_read_b128 a[12:15], v49 offset:11264\n" \
"ds_read_b128 a[16:19], v49 offset:12288\n" \
"v_mfma_f32_16x16x32_bf16 a[196:199], a[212:215], v[72:75], a[196:199]\n" \
"ds_read_b128 a[20:23], v49 offset:13312\n" \
"ds_read_b128 a[24:27], v49 offset:14336\n" \
"v_mfma_f32_16x16x32_bf16 v[192:195], a[160:163], v[80:83], v[192:195]\n" \
"ds_read_b128 a[28:31], v49 offset:15360\n" \
"v_add_u32_e32 v50, %[p4], v48\n" \
"v_mfma_f32_16x16x32_bf16 v[196:199], a[164:167], v[80:83], v[196:199]\n" \
"buffer_load_dwordx4 a[232:235], v20, s[56:59], s60 offen offset:3072 sc1\n" \
"v_mfma_f32_16x16x32_bf16 v[200:203], a[168:171], v[80:83], v[200:203]\n" \
"v_add_u32_e32 v51, 0x10000, v50\n" \
"v_accvgpr_read_b32 v40, a192\n" \
"v_mfma_f32_16x16x32_bf16 v[204:207], a[172:175], v[80:83], v[204:207]\n" \
"v_accvgpr_read_b32 v41, a196\n" \
"ds_write_b64 v19, v[40:41]\n" \
"v_mfma_f32_16x16x32_bf16 v[208:211], a[176:179], v[80:83], v[208:211]\n" \
"s_waitcnt vmcnt(8)\n" \
"ds_write_b128 v50, v[128:131] offset:0\n" \
"buffer_load_dwordx4 a[248:251], v54, s[56:59], s60 offen offset:3072 sc1\n" \
"v_mfma_f32_16x16x32_bf16 v[212:215], a[180:183], v[80:83], v[212:215]\n" \
"ds_write_b128 v50, v[132:135] offset:1024\n" \
"v_mfma_f32_16x16x32_bf16 v[216:219], a[184:187], v[80:83], v[216:219]\n" \
"ds_write_b128 v50, v[136:139] offset:2048\n" \
"v_mfma_f32_16x16x32_bf16 v[220:223], a[188:191], v[80:83], v[220:223]\n" \
"ds_write_b128 v50, v[140:143] offset:3072\n" \
"v_mfma_f32_16x16x32_bf16 a[200:203], a[212:215], v[80:83], a[200:203]\n" \
"ds_write_b128 v50, v[144:147] offset:8192\n" \
"ds_write_b128 v50, v[148:151] offset:9216\n" \
"ds_write_b128 v50, v[152:155] offset:10240\n" \
"ds_write_b128 v50, v[156:159] offset:11264\n" \
"ds_write_b128 v51, v[160:163] offset:0\n" \
"ds_write_b128 v51, v[164:167] offset:1024\n" \
"ds_write_b128 v51, v[168:171] offset:2048\n" \
"ds_write_b128 v51, v[172:175] offset:3072\n" \
"ds_write_b128 v51, v[176:179] offset:8192\n" \
"ds_write_b128 v51, v[180:183] offset:9216\n" \
"ds_write_b128 v51, v[184:187] offset:10240\n" \
"ds_write_b128 v51, v[188:191] offset:11264\n" \
"v_xor_b32_e32 v52, 0x4000, v50\n" \
"v_xor_b32_e32 v53, 0x4000, v51\n" \
"v_exp_f32_e32 v88, v88\n" \
"v_exp_f32_e32 v89, v89\n" \
"v_exp_f32_e32 v90, v90\n" \
"v_exp_f32_e32 v91, v91\n" \
"v_cvt_pk_bf16_f32 v88, v88, v89\n" \
"v_exp_f32_e32 v92, v92\n" \
"v_exp_f32_e32 v93, v93\n" \
"v_exp_f32_e32 v94, v94\n" \
"v_exp_f32_e32 v95, v95\n" \
"v_xor_b32_e32 v63, 0x400, v19\n" \
"v_cvt_pk_bf16_f32 v89, v90, v91\n" \
"v_cvt_pk_bf16_f32 v90, v92, v93\n" \
"v_cvt_pk_bf16_f32 v91, v94, v95\n" \
"v_readlane_b32 s93, v11, 20\n" \
"v_readlane_b32 s84, v11, 15\n" \
"v_readlane_b32 s85, v11, 16\n" \
"v_readlane_b32 s86, v11, 17\n" \
"s_mov_b32 s87, 0x20000\n" \
"v_readlane_b32 s92, v11, 18\n" \
"s_add_u32 m0, s93, 0x0\n" \
"v_readlane_b32 s97, v11, 34\n" \
"v_readlane_b32 s98, v11, 35\n" \
"v_exp_f32_e32 v120, v120\n" \
"v_exp_f32_e32 v121, v121\n" \
"v_exp_f32_e32 v122, v122\n" \
"v_exp_f32_e32 v123, v123\n" \
"s_waitcnt lgkmcnt(0)\n" \
"s_barrier\n" \
"ds_read_b128 v[128:131], v52 offset:0\n" \
"ds_read_b128 v[132:135], v52 offset:1024\n" \
"ds_read_b128 v[136:139], v52 offset:2048\n" \
"ds_read_b128 v[140:143], v52 offset:3072\n" \
"ds_read_b128 v[144:147], v52 offset:8192\n" \
"ds_read_b128 v[148:151], v52 offset:9216\n" \
"ds_read_b128 v[152:155], v52 offset:10240\n" \
"ds_read_b128 v[156:159], v52 offset:11264\n" \
"ds_read_b128 v[160:163], v53 offset:0\n" \
"ds_read_b128 v[164:167], v53 offset:1024\n" \
"ds_read_b128 v[168:171], v53 offset:2048\n" \
"ds_read_b128 v[172:175], v53 offset:3072\n" \
"ds_read_b128 v[176:179], v53 offset:8192\n" \
"ds_read_b128 v[180:183], v53 offset:9216\n" \
"ds_read_b128 v[184:187], v53 offset:10240\n" \
"ds_read_b128 v[188:191], v53 offset:11264\n" \
"ds_read_b64 v[42:43], v63\n" \
"v_mfma_f32_16x16x32_bf16 v[224:227], a[160:163], v[88:91], v[224:227]\n" \
"buffer_load_dwordx4 v0, s[84:87], s92 offen offset:0 lds\n" \
"v_mfma_f32_16x16x32_bf16 v[228:231], a[164:167], v[88:91], v[228:231]\n" \
"s_waitcnt vmcnt(9)\n" \
"v_cmp_ne_u32_e32 vcc, s62, v55\n" \
"v_mfma_f32_16x16x32_bf16 v[232:235], a[168:171], v[88:91], v[232:235]\n" \
"v_cvt_pk_bf16_f32 v120, v120, v121\n" \
"v_cvt_pk_bf16_f32 v121, v122, v123\n" \
"v_mfma_f32_16x16x32_bf16 v[236:239], a[172:175], v[88:91], v[236:239]\n" \
"v_exp_f32_e32 v124, v124\n" \
"s_cbranch_vccnz L_so0_%=\n" \
"L_ro0_%=:\n" \
"buffer_load_dwordx4 v1, s[84:87], s92 offen offset:1024 lds\n" \
"v_mfma_f32_16x16x32_bf16 v[240:243], a[176:179], v[88:91], v[240:243]\n" \
"v_exp_f32_e32 v125, v125\n" \
"v_mfma_f32_16x16x32_bf16 v[244:247], a[180:183], v[88:91], v[244:247]\n" \
"v_exp_f32_e32 v126, v126\n" \
"v_mfma_f32_16x16x32_bf16 v[248:251], a[184:187], v[88:91], v[248:251]\n" \
"v_exp_f32_e32 v127, v127\n" \
"v_mfma_f32_16x16x32_bf16 v[252:255], a[188:191], v[88:91], v[252:255]\n" \
"buffer_load_dwordx4 v2, s[84:87], s92 offen offset:2048 lds\n" \
"v_mfma_f32_16x16x32_bf16 a[204:207], a[212:215], v[88:91], a[204:207]\n" \
"v_cvt_pk_bf16_f32 v122, v124, v125\n" \
"v_cvt_pk_bf16_f32 v123, v126, v127\n" \
"s_waitcnt lgkmcnt(1)\n" \
"s_barrier\n" \
"v_and_b32_e32 v120, v12, v120\n" \
"v_and_b32_e32 v121, v13, v121\n" \
"v_and_b32_e32 v122, v14, v122\n" \
"v_and_b32_e32 v123, v15, v123\n" \
"buffer_load_dwordx4 v3, s[84:87], s92 offen offset:3072 lds\n" \
"s_add_u32 m0, s93, 0x1000\n" \
"v_mfma_f32_16x16x32_bf16 v[224:227], a[0:3], v[120:123], v[224:227]\n" \
"v_accvgpr_read_b32 v56, a200\n" \
"s_waitcnt lgkmcnt(0)\n" \
"v_add_f32_e32 v56, v56, v42\n" \
"v_mfma_f32_16x16x32_bf16 v[228:231], a[4:7], v[120:123], v[228:231]\n" \
"v_max_f32_e32 v62, v62, v56\n" \
"v_add_f32_e32 v192, v192, v128\n" \
"v_mfma_f32_16x16x32_bf16 v[232:235], a[8:11], v[120:123], v[232:235]\n" \
"v_rcp_f32_e32 v56, v56\n" \
"buffer_load_dwordx4 v4, s[84:87], s92 offen offset:0 lds\n" \
"v_mfma_f32_16x16x32_bf16 v[236:239], a[12:15], v[120:123], v[236:239]\n" \
"v_add_f32_e32 v193, v193, v129\n" \
"v_add_f32_e32 v194, v194, v130\n" \
"v_mfma_f32_16x16x32_bf16 v[240:243], a[16:19], v[120:123], v[240:243]\n" \
"v_add_f32_e32 v195, v195, v131\n" \
"v_add_f32_e32 v196, v196, v132\n" \
"v_add_f32_e32 v197, v197, v133\n" \
"v_add_f32_e32 v198, v198, v134\n" \
"v_add_f32_e32 v199, v199, v135\n" \
"v_mul_f32_e32 v56, v56, v60\n" \
"v_mul_f32_e32 v192, v56, v192\n" \
"buffer_load_dwordx4 v5, s[84:87], s92 offen offset:1024 lds\n" \
"v_mul_f32_e32 v193, v56, v193\n" \
"v_mul_f32_e32 v194, v56, v194\n" \
"v_mul_f32_e32 v195, v56, v195\n" \
"v_mul_f32_e32 v196, v56, v196\n" \
"v_mul_f32_e32 v197, v56, v197\n" \
"v_mul_f32_e32 v198, v56, v198\n" \
"v_mul_f32_e32 v199, v56, v199\n" \
"v_add_f32_e32 v200, v200, v136\n" \
"v_add_f32_e32 v201, v201, v137\n" \
"v_add_f32_e32 v202, v202, v138\n" \
"v_add_f32_e32 v203, v203, v139\n" \
"v_add_f32_e32 v204, v204, v140\n" \
"v_add_f32_e32 v205, v205, v141\n" \
"buffer_load_dwordx4 v6, s[84:87], s92 offen offset:2048 lds\n" \
"v_add_f32_e32 v206, v206, v142\n" \
"v_add_f32_e32 v207, v207, v143\n" \
"v_cvt_pk_bf16_f32 v64, v192, v193\n" \
"v_cvt_pk_bf16_f32 v65, v194, v195\n" \
"v_cvt_pk_bf16_f32 v66, v196, v197\n" \
"v_cvt_pk_bf16_f32 v67, v198, v199\n" \
"v_mfma_f32_16x16x32_bf16 v[244:247], a[20:23], v[120:123], v[244:247]\n" \
"v_mul_f32_e32 v200, v56, v200\n" \
"L_pf12:\n" \
"s_cbranch_execz L_pf16\n" \
"s_nop 0\n" \
"v_mul_f32_e32 v201, v56, v201\n" \
"v_mfma_f32_16x16x32_bf16 a[208:211], a[216:219], v[64:67], a[208:211]\n" \
"v_mul_f32_e32 v202, v56, v202\n" \
"v_mul_f32_e32 v203, v56, v203\n" \
"buffer_store_dwordx4 v[64:67], v20, s[56:59], s70 offen offset:0 sc1\n" \
"v_mul_f32_e32 v204, v56, v204\n" \
"v_mul_f32_e32 v205, v56, v205\n" \
"v_mul_f32_e32 v206, v56, v206\n" \
"v_mul_f32_e32 v207, v56, v207\n" \
"v_add_f32_e32 v208, v208, v144\n" \
"v_add_f32_e32 v209, v209, v145\n" \
"v_add_f32_e32 v210, v210, v146\n" \
"v_add_f32_e32 v211, v211, v147\n" \
"v_add_f32_e32 v212, v212, v148\n" \
"v_add_f32_e32 v213, v213, v149\n" \
"v_add_f32_e32 v214, v214, v150\n" \
"v_add_f32_e32 v215, v215, v151\n" \
"v_cvt_pk_bf16_f32 v68, v200, v201\n" \
"buffer_load_dwordx4 v7, s[84:87], s92 offen offset:3072 lds\n" \
"s_add_u32 m0, s93, 0x2000\n" \
"v_cvt_pk_bf16_f32 v69, v202, v203\n" \
"v_cvt_pk_bf16_f32 v70, v204, v205\n" \
"v_cvt_pk_bf16_f32 v71, v206, v207\n" \
"v_mul_f32_e32 v208, v56, v208\n" \
"v_mul_f32_e32 v209, v56, v209\n" \
"v_mfma_f32_16x16x32_bf16 a[208:211], a[216:219], v[68:71], a[208:211]\n" \
"v_mul_f32_e32 v210, v56, v210\n" \
"v_mul_f32_e32 v211, v56, v211\n" \
"v_mul_f32_e32 v212, v56, v212\n" \
"v_mul_f32_e32 v213, v56, v213\n" \
"v_mul_f32_e32 v214, v56, v214\n" \
"buffer_store_dwordx4 v[68:71], v20, s[56:59], s70 offen offset:1024 sc1\n" \
"v_mul_f32_e32 v215, v56, v215\n" \
"v_add_f32_e32 v216, v216, v152\n" \
"v_add_f32_e32 v217, v217, v153\n" \
"v_add_f32_e32 v218, v218, v154\n" \
"v_add_f32_e32 v219, v219, v155\n" \
"v_add_f32_e32 v220, v220, v156\n" \
"v_add_f32_e32 v221, v221, v157\n" \
"v_add_f32_e32 v222, v222, v158\n" \
"v_add_f32_e32 v223, v223, v159\n" \
"v_cvt_pk_bf16_f32 v72, v208, v209\n" \
"v_cvt_pk_bf16_f32 v73, v210, v211\n" \
"v_cvt_pk_bf16_f32 v74, v212, v213\n" \
"v_cvt_pk_bf16_f32 v75, v214, v215\n" \
"buffer_store_dwordx4 v[72:75], v20, s[56:59], s70 offen offset:2048 sc1\n" \
"v_mfma_f32_16x16x32_bf16 v[248:251], a[24:27], v[120:123], v[248:251]\n" \
"v_mfma_f32_16x16x32_bf16 a[208:211], a[216:219], v[72:75], a[208:211]\n" \
"v_mul_f32_e32 v216, v56, v216\n" \
"v_mul_f32_e32 v217, v56, v217\n" \
"v_mul_f32_e32 v218, v56, v218\n" \
"v_mul_f32_e32 v219, v56, v219\n" \
"v_mul_f32_e32 v220, v56, v220\n" \
"v_mul_f32_e32 v221, v56, v221\n" \
"v_mul_f32_e32 v222, v56, v222\n" \
"buffer_load_dwordx4 v8, s[84:87], s92 offen offset:0 lds\n" \
"v_mul_f32_e32 v223, v56, v223\n" \
"v_cvt_pk_bf16_f32 v76, v216, v217\n" \
"v_cvt_pk_bf16_f32 v77, v218, v219\n" \
"v_cvt_pk_bf16_f32 v78, v220, v221\n" \
"v_cvt_pk_bf16_f32 v79, v222, v223\n" \
"v_add_f32_e32 v224, v224, v160\n" \
"v_add_f32_e32 v225, v225, v161\n" \
"v_add_f32_e32 v226, v226, v162\n" \
"v_add_f32_e32 v227, v227, v163\n" \
"v_add_f32_e32 v228, v228, v164\n" \
"v_add_f32_e32 v229, v229, v165\n" \
"v_add_f32_e32 v230, v230, v166\n" \
"v_add_f32_e32 v231, v231, v167\n" \
"buffer_store_dwordx4 v[76:79], v20, s[56:59], s70 offen offset:3072 sc1\n" \
"v_mfma_f32_16x16x32_bf16 a[208:211], a[216:219], v[76:79], a[208:211]\n" \
"v_add_f32_e32 v232, v232, v168\n" \
"v_add_f32_e32 v233, v233, v169\n" \
"v_add_f32_e32 v234, v234, v170\n" \
"v_add_f32_e32 v235, v235, v171\n" \
"v_add_f32_e32 v236, v236, v172\n" \
"v_add_f32_e32 v237, v237, v173\n" \
"v_add_f32_e32 v238, v238, v174\n" \
"v_add_f32_e32 v239, v239, v175\n" \
"v_mfma_f32_16x16x32_bf16 v[252:255], a[28:31], v[120:123], v[252:255]\n" \
"v_add_f32_e32 v240, v240, v176\n" \
"v_add_f32_e32 v241, v241, v177\n" \
"buffer_load_dwordx4 a[96:99], v20, s[56:59], s97 offen offset:0 sc1\n" \
"v_add_f32_e32 v242, v242, v178\n" \
"v_add_f32_e32 v243, v243, v179\n" \
"v_add_f32_e32 v244, v244, v180\n" \
"v_add_f32_e32 v245, v245, v181\n" \
"v_add_f32_e32 v246, v246, v182\n" \
"v_add_f32_e32 v247, v247, v183\n" \
"v_add_f32_e32 v248, v248, v184\n" \
"v_add_f32_e32 v249, v249, v185\n" \
"v_add_f32_e32 v250, v250, v186\n" \
"v_add_f32_e32 v251, v251, v187\n" \
"v_add_f32_e32 v252, v252, v188\n" \
"v_add_f32_e32 v253, v253, v189\n" \
"v_add_f32_e32 v254, v254, v190\n" \
"buffer_load_dwordx4 a[104:107], v54, s[56:59], s97 offen offset:0 sc1\n" \
"v_add_f32_e32 v255, v255, v191\n" \
"v_mfma_f32_16x16x32_bf16 a[204:207], a[212:215], v[120:123], a[204:207]\n" \
"buffer_load_dwordx4 a[112:115], v20, s[56:59], s98 offen offset:0 sc1\n" \
"buffer_load_dwordx4 a[120:123], v54, s[56:59], s98 offen offset:0 sc1\n" \
"buffer_load_dwordx4 a[100:103], v20, s[56:59], s97 offen offset:1024 sc1\n" \
"buffer_load_dwordx4 a[108:111], v54, s[56:59], s97 offen offset:1024 sc1\n" \
"buffer_load_dwordx4 a[116:119], v20, s[56:59], s98 offen offset:1024 sc1\n" \
"buffer_load_dwordx4 a[124:127], v54, s[56:59], s98 offen offset:1024 sc1\n" \
"s_nop 1\n" \
"v_accvgpr_read_b32 v57, a204\n" \
"v_add_f32_e32 v57, v57, v43\n" \
"v_max_f32_e32 v62, v62, v57\n" \
"v_rcp_f32_e32 v57, v57\n" \
"v_cmp_lt_f32_e64 s[72:73], %[lim], v62\n" \
"v_mul_f32_e32 v57, v57, v61\n" \
"v_mul_f32_e32 v224, v57, v224\n" \
"v_mul_f32_e32 v225, v57, v225\n" \
"v_mul_f32_e32 v226, v57, v226\n" \
"v_mul_f32_e32 v227, v57, v227\n" \
"v_mul_f32_e32 v228, v57, v228\n" \
"v_mul_f32_e32 v229, v57, v229\n" \
"v_mul_f32_e32 v230, v57, v230\n" \
"v_mul_f32_e32 v231, v57, v231\n" \
"v_cvt_pk_bf16_f32 v80, v224, v225\n" \
"v_cvt_pk_bf16_f32 v81, v226, v227\n" \
"v_cvt_pk_bf16_f32 v82, v228, v229\n" \
"v_cvt_pk_bf16_f32 v83, v230, v231\n" \
"v_mul_f32_e32 v232, v57, v232\n" \
"v_mul_f32_e32 v233, v57, v233\n" \
"v_mfma_f32_16x16x32_bf16 a[208:211], a[216:219], v[80:83], a[208:211]\n" \
"v_mul_f32_e32 v234, v57, v234\n" \
"v_mul_f32_e32 v235, v57, v235\n" \
"v_mul_f32_e32 v236, v57, v236\n" \
"v_mul_f32_e32 v237, v57, v237\n" \
"v_mul_f32_e32 v238, v57, v238\n" \
"v_mul_f32_e32 v239, v57, v239\n" \
"v_cvt_pk_bf16_f32 v84, v232, v233\n" \
"v_cvt_pk_bf16_f32 v85, v234, v235\n" \
"v_cvt_pk_bf16_f32 v86, v236, v237\n" \
"v_cvt_pk_bf16_f32 v87, v238, v239\n" \
"v_mul_f32_e32 v240, v57, v240\n" \
"v_mul_f32_e32 v241, v57, v241\n" \
"v_mfma_f32_16x16x32_bf16 a[208:211], a[216:219], v[84:87], a[208:211]\n" \
"v_mul_f32_e32 v242, v57, v242\n" \
"v_mul_f32_e32 v243, v57, v243\n" \
"v_mul_f32_e32 v244, v57, v244\n" \
"v_mul_f32_e32 v245, v57, v245\n" \
"v_mul_f32_e32 v246, v57, v246\n" \
"v_mul_f32_e32 v247, v57, v247\n" \
"v_cvt_pk_bf16_f32 v96, v240, v241\n" \
"v_cvt_pk_bf16_f32 v97, v242, v243\n" \
"v_cvt_pk_bf16_f32 v98, v244, v245\n" \
"v_cvt_pk_bf16_f32 v99, v246, v247\n" \
"v_mul_f32_e32 v248, v57, v248\n" \
"v_mul_f32_e32 v249, v57, v249\n" \
"v_mfma_f32_16x16x32_bf16 a[208:211], a[216:219], v[96:99], a[208:211]\n" \
"v_mul_f32_e32 v250, v57, v250\n" \
"v_mul_f32_e32 v251, v57, v251\n" \
"v_mul_f32_e32 v252, v57, v252\n" \
"v_mul_f32_e32 v253, v57, v253\n" \
"v_mul_f32_e32 v254, v57, v254\n" \
"v_mul_f32_e32 v255, v57, v255\n" \
"v_cvt_pk_bf16_f32 v100, v248, v249\n" \
"v_cvt_pk_bf16_f32 v101, v250, v251\n" \
"v_cvt_pk_bf16_f32 v102, v252, v253\n" \
"v_cvt_pk_bf16_f32 v103, v254, v255\n" \
"buffer_store_dwordx4 v[80:83], v20, s[56:59], s71 offen offset:0 sc1\n" \
"buffer_store_dwordx4 v[84:87], v20, s[56:59], s71 offen offset:1024 sc1\n" \
"v_mfma_f32_16x16x32_bf16 a[208:211], a[216:219], v[100:103], a[208:211]\n" \
"buffer_store_dwordx4 v[96:99], v20, s[56:59], s71 offen offset:2048 sc1\n" \
"buffer_store_dwordx4 v[100:103], v20, s[56:59], s71 offen offset:3072 sc1\n" \
"s_nop 5\n" \
"v_accvgpr_read_b32 v63, a208\n" \
"v_cmp_u_f32_e32 vcc, v63, v63\n" \
"s_nop 0\n" \
"s_or_b64 vcc, s[72:73], vcc\n" \
"s_cbranch_scc0 L_out_%=\n" \
"s_branch L_end_%=\n" \
"L_h1_%=:\n" \
"L_pf13:\n" \
"s_cbranch_execz L_pf17\n" \
"s_nop 0\n" \
"s_cmp_eq_u32 s63, 0\n" \
"s_cbranch_scc1 L_hc1_%=\n" \
"buffer_load_dword v22, v21, s[56:59], %[fkb] offen sc1\n" \
"s_branch L_hr1_%=\n" \
"L_hs1_%=:\n" \
"s_sleep 1\n" \
"buffer_load_dword v22, v21, s[56:59], %[fkb] offen sc1\n" \
"s_add_u32 s101, s101, 1\n" \
"s_waitcnt vmcnt(0)\n" \
"s_cmp_gt_u32 s101, 0x100000\n" \
"s_cbranch_scc1 L_hr1_%=\n" \
"L_hc1_%=:\n" \
"v_cmp_ne_u32_e32 vcc, s62, v22\n" \
"s_nop 4\n" \
"s_cbranch_vccnz L_hs1_%=\n" \
"s_branch L_hr1_%=\n" \
"L_h2_%=:\n" \
"s_cmp_eq_u32 s63, 0\n" \
"s_cbranch_scc1 L_hc2_%=\n" \
"buffer_load_dword v22, v21, s[56:59], %[fkb] offen sc1\n" \
"s_branch L_hr2_%=\n" \
"L_hs2_%=:\n" \
"s_sleep 1\n" \
"buffer_load_dword v22, v21, s[56:59], %[fkb] offen sc1\n" \
"s_add_u32 s101, s101, 1\n" \
"s_waitcnt vmcnt(0)\n" \
"s_cmp_gt_u32 s101, 0x100000\n" \
"s_cbranch_scc1 L_hr2_%=\n" \
"L_hc2_%=:\n" \
"v_cmp_ne_u32_e32 vcc, s62, v22\n" \
"s_nop 4\n" \
"s_cbranch_vccnz L_hs2_%=\n" \
"s_branch L_hr2_%=\n" \
"L_so0_%=:\n" \
"s_sleep 1\n" \
"buffer_load_dword v55, v58, s[56:59], s96 offen sc1\n" \
"s_add_u32 s101, s101, 1\n" \
"s_waitcnt vmcnt(0)\n" \
"v_cmp_ne_u32_e32 vcc, s62, v55\n" \
"s_cmp_gt_u32 s101, 0x100000\n" \
"s_cbranch_scc1 L_ro0_%=\n" \
"s_nop 2\n" \
"s_cbranch_vccnz L_so0_%=\n" \
"s_branch L_ro0_%=\n" \
"L_so1_%=:\n" \
"s_sleep 1\n" \
"buffer_load_dword v57, v60, s[56:59], s96 offen sc1\n" \
"s_add_u32 s101, s101, 1\n" \
"s_waitcnt vmcnt(0)\n" \
"v_cmp_ne_u32_e32 vcc, s62, v57\n" \
"s_cmp_gt_u32 s101, 0x100000\n" \
"s_cbranch_scc1 L_ro1_%=\n" \
"s_nop 2\n" \
"s_cbranch_vccnz L_so1_%=\n" \
"s_branch L_ro1_%=\n" \
"L_sa_%=:\n" \
"s_sleep 1\n" \
"buffer_load_dword v37, v38, s[56:59], s64 offen sc1\n" \
"s_add_u32 s101, s101, 1\n" \
"s_waitcnt vmcnt(0)\n" \
"v_cmp_ne_u32_e32 vcc, s62, v37\n" \
"s_cmp_gt_u32 s101, 0x100000\n" \
"s_cbranch_scc1 L_ra_%=\n" \
"s_nop 2\n" \
"s_cbranch_vccnz L_sa_%=\n" \
"s_branch L_ra_%=\n" \
"L_sb_%=:\n" \
"s_sleep 1\n" \
"buffer_load_dword v47, v38, s[56:59], s71 offen sc1\n" \
"s_add_u32 s101, s101, 1\n" \
"s_waitcnt vmcnt(0)\n" \
"v_cmp_ne_u32_e32 vcc, s62, v47\n" \
"s_cmp_gt_u32 s101, 0x100000\n" \
"s_cbranch_scc1 L_rb_%=\n" \
"s_nop 2\n" \
"s_cbranch_vccnz L_sb_%=\n" \
"s_branch L_rb_%=\n" \
"L_sx0_%=:\n" \
"s_sleep 1\n" \
"buffer_load_dword v37, v40, s[56:59], s74 offen sc1\n" \
"s_add_u32 s101, s101, 1\n" \
"s_waitcnt vmcnt(0)\n" \
"v_cmp_eq_u32_e64 s[98:99], s62, v37\n" \
"s_cmp_gt_u32 s101, 0x100000\n" \
"s_cbranch_scc1 L_rx0_%=\n" \
"s_andn2_b32 s75, 0x1, s98\n" \
"s_cbranch_scc1 L_sx0_%=\n" \
"s_branch L_rx0_%=\n" \
"L_sx1_%=:\n" \
"s_sleep 1\n" \
"buffer_load_dword v37, v40, s[56:59], s74 offen sc1\n" \
"s_add_u32 s101, s101, 1\n" \
"s_waitcnt vmcnt(0)\n" \
"v_cmp_eq_u32_e64 s[98:99], s62, v37\n" \
"s_cmp_gt_u32 s101, 0x100000\n" \
"s_cbranch_scc1 L_rx1_%=\n" \
"s_andn2_b32 s75, 0x2, s98\n" \
"s_cbranch_scc1 L_sx1_%=\n" \
"s_branch L_rx1_%=\n" \
"L_sx2_%=:\n" \
"s_sleep 1\n" \
"buffer_load_dword v37, v40, s[56:59], s74 offen sc1\n" \
"s_add_u32 s101, s101, 1\n" \
"s_waitcnt vmcnt(0)\n" \
"v_cmp_eq_u32_e64 s[98:99], s62, v37\n" \
"s_cmp_gt_u32 s101, 0x100000\n" \
"s_cbranch_scc1 L_rx2_%=\n" \
"s_andn2_b32 s75, 0x4, s98\n" \
"s_cbranch_scc1 L_sx2_%=\n" \
"s_branch L_rx2_%=\n" \
"L_sx3_%=:\n" \
"s_sleep 1\n" \
"buffer_load_dword v37, v40, s[56:59], s74 offen sc1\n" \
"s_add_u32 s101, s101, 1\n" \
"s_waitcnt vmcnt(0)\n" \
"v_cmp_eq_u32_e64 s[98:99], s62, v37\n" \
"s_cmp_gt_u32 s101, 0x100000\n" \
"s_cbranch_scc1 L_rx3_%=\n" \
"s_andn2_b32 s75, s100, s98\n" \
"s_cbranch_scc1 L_sx3_%=\n" \
"s_branch L_rx3_%=\n" \
"L_sx4_%=:\n" \
"s_sleep 1\n" \
"buffer_load_dword v37, v40, s[56:59], s74 offen sc1\n" \
"s_add_u32 s101, s101, 1\n" \
"s_waitcnt vmcnt(0)\n" \
"v_cmp_eq_u32_e64 s[98:99], s62, v37\n" \
"s_cmp_gt_u32 s101, 0x100000\n" \
"s_cbranch_scc1 L_rx4_%=\n" \
"s_andn2_b32 s75, 0x10, s98\n" \
"s_cbranch_scc1 L_sx4_%=\n" \
"s_branch L_rx4_%=\n" \
"L_sx5_%=:\n" \
"s_sleep 1\n" \
"buffer_load_dword v37, v40, s[56:59], s74 offen sc1\n" \
"s_add_u32 s101, s101, 1\n" \
"s_waitcnt vmcnt(0)\n" \
"v_cmp_eq_u32_e64 s[98:99], s62, v37\n" \
"s_cmp_gt_u32 s101, 0x100000\n" \
"s_cbranch_scc1 L_rx5_%=\n" \
"s_andn2_b32 s75, 0x20, s98\n" \
"s_cbranch_scc1 L_sx5_%=\n" \
"s_branch L_rx5_%=\n" \
"L_sx6_%=:\n" \
"s_sleep 1\n" \
"buffer_load_dword v37, v40, s[56:59], s74 offen sc1\n" \
"s_add_u32 s101, s101, 1\n" \
"s_waitcnt vmcnt(0)\n" \
"v_cmp_eq_u32_e64 s[98:99], s62, v37\n" \
"s_cmp_gt_u32 s101, 0x100000\n" \
"s_cbranch_scc1 L_rx6_%=\n" \
"s_andn2_b32 s75, 0x40, s98\n" \
"s_cbranch_scc1 L_sx6_%=\n" \
"s_branch L_rx6_%=\n" \
"L_sx7_%=:\n" \
"s_sleep 1\n" \
"buffer_load_dword v37, v40, s[56:59], s74 offen sc1\n" \
"s_add_u32 s101, s101, 1\n" \
"s_waitcnt vmcnt(0)\n" \
"v_cmp_eq_u32_e64 s[98:99], s62, v37\n" \
"s_cmp_gt_u32 s101, 0x100000\n" \
"s_cbranch_scc1 L_rx7_%=\n" \
"s_andn2_b32 s75, 0x80, s98\n" \
"s_cbranch_scc1 L_sx7_%=\n" \
"s_branch L_rx7_%=\n" \
"L_sx8_%=:\n" \
"s_sleep 1\n" \
"buffer_load_dword v37, v40, s[56:59], s74 offen sc1\n" \
"s_add_u32 s101, s101, 1\n" \
"s_waitcnt vmcnt(0)\n" \
"v_cmp_eq_u32_e64 s[98:99], s62, v37\n" \
"s_cmp_gt_u32 s101, 0x100000\n" \
"s_cbranch_scc1 L_rx8_%=\n" \
"s_andn2_b32 s75, 0x100, s98\n" \
"s_cbranch_scc1 L_sx8_%=\n" \
"s_branch L_rx8_%=\n" \
"L_sx9_%=:\n" \
"s_sleep 1\n" \
"buffer_load_dword v37, v40, s[56:59], s74 offen sc1\n" \
"s_add_u32 s101, s101, 1\n" \
"s_waitcnt vmcnt(0)\n" \
"v_cmp_eq_u32_e64 s[98:99], s62, v37\n" \
"s_cmp_gt_u32 s101, 0x100000\n" \
"s_cbranch_scc1 L_rx9_%=\n" \
"s_andn2_b32 s75, 0x200, s98\n" \
"s_cbranch_scc1 L_sx9_%=\n" \
"s_branch L_rx9_%=\n" \
"L_sx10_%=:\n" \
"s_sleep 1\n" \
"buffer_load_dword v37, v40, s[56:59], s74 offen sc1\n" \
"s_add_u32 s101, s101, 1\n" \
"s_waitcnt vmcnt(0)\n" \
"v_cmp_eq_u32_e64 s[98:99], s62, v37\n" \
"s_cmp_gt_u32 s101, 0x100000\n" \
"s_cbranch_scc1 L_rx10_%=\n" \
"s_andn2_b32 s75, 0x400, s98\n" \
"s_cbranch_scc1 L_sx10_%=\n" \
"s_branch L_rx10_%=\n" \
"L_att_stub:\n" \
"s_mov_b32 s101, 0\n" \
"v_add_u32_e32 v32, 0x49000, v20\n" \
"v_readlane_b32 s96, v11, 34\n" \
"v_readlane_b32 s97, v11, 35\n" \
"buffer_load_dwordx4 a[220:223], v20, s[56:59], s60 offen offset:0 sc1\n" \
"buffer_load_dwordx4 a[236:239], v32, s[56:59], s60 offen offset:0 sc1\n" \
"buffer_load_dwordx4 a[224:227], v20, s[56:59], s60 offen offset:1024 sc1\n" \
"buffer_load_dwordx4 a[240:243], v32, s[56:59], s60 offen offset:1024 sc1\n" \
"buffer_load_dwordx4 a[228:231], v20, s[56:59], s60 offen offset:2048 sc1\n" \
"buffer_load_dwordx4 a[244:247], v32, s[56:59], s60 offen offset:2048 sc1\n" \
"buffer_load_dwordx4 a[232:235], v20, s[56:59], s60 offen offset:3072 sc1\n" \
"buffer_load_dwordx4 a[248:251], v32, s[56:59], s60 offen offset:3072 sc1\n" \
"buffer_load_dwordx4 a[96:99], v20, s[56:59], s96 offen offset:0 sc1\n" \
"buffer_load_dwordx4 a[104:107], v32, s[56:59], s96 offen offset:0 sc1\n" \
"buffer_load_dwordx4 a[112:115], v20, s[56:59], s97 offen offset:0 sc1\n" \
"buffer_load_dwordx4 a[120:123], v32, s[56:59], s97 offen offset:0 sc1\n" \
"buffer_load_dwordx4 a[100:103], v20, s[56:59], s96 offen offset:1024 sc1\n" \
"buffer_load_dwordx4 a[108:111], v32, s[56:59], s96 offen offset:1024 sc1\n" \
"buffer_load_dwordx4 a[116:119], v20, s[56:59], s97 offen offset:1024 sc1\n" \
"buffer_load_dwordx4 a[124:127], v32, s[56:59], s97 offen offset:1024 sc1\n" \
"buffer_load_dwordx4 v[64:67], v20, s[56:59], s61 offen offset:0 sc1\n" \
"buffer_load_dwordx4 v[68:71], v20, s[56:59], s61 offen offset:1024 sc1\n" \
"buffer_load_dwordx4 v[72:75], v20, s[56:59], s61 offen offset:2048 sc1\n" \
"buffer_load_dwordx4 v[76:79], v20, s[56:59], s61 offen offset:3072 sc1\n" \
"buffer_load_dwordx4 v[80:83], v32, s[56:59], s61 offen offset:0 sc1\n" \
"buffer_load_dwordx4 v[84:87], v32, s[56:59], s61 offen offset:1024 sc1\n" \
"buffer_load_dwordx4 v[96:99], v32, s[56:59], s61 offen offset:2048 sc1\n" \
"buffer_load_dwordx4 v[100:103], v32, s[56:59], s61 offen offset:3072 sc1\n" \
"s_waitcnt vmcnt(0)\n" \
"s_branch L_out_%=\n" \
"L_sw_%=:\n" \
"L_pf14:\n" \
"s_cbranch_execz L_pf18\n" \
"s_nop 0\n" \
"v_mfma_f32_16x16x32_bf16 v[104:107], a[128:131], a[16:19], v[36:39]\n" \
"s_add_u32 s68, s60, 0xfff6e000\n" \
"s_add_u32 s69, s60, 0xfffb7000\n" \
"s_add_u32 s70, s60, 0x0\n" \
"s_add_u32 s71, s60, 0x49000\n" \
"v_mfma_f32_16x16x32_bf16 v[104:107], a[132:135], a[20:23], v[104:107]\n" \
"s_xor_b32 m0, s65, 0x12000\n" \
"ds_read_b128 a[160:163], v49 offset:0\n" \
"v_mfma_f32_16x16x32_bf16 v[104:107], a[136:139], a[24:27], v[104:107]\n" \
"buffer_load_dwordx4 v50, %[srd], s64 offen offset:0 sc1 lds\n" \
"v_mfma_f32_16x16x32_bf16 v[104:107], a[140:143], a[28:31], v[104:107]\n" \
"ds_read_b128 a[164:167], v49 offset:1024\n" \
"ds_read_b128 a[168:171], v49 offset:2048\n" \
"v_mfma_f32_16x16x32_bf16 v[108:111], a[156:159], a[28:31], v[36:39]\n" \
"ds_read_b128 a[172:175], v49 offset:3072\n" \
"ds_read_b128 a[176:179], v49 offset:4096\n" \
"v_mfma_f32_16x16x32_bf16 v[108:111], a[152:155], a[24:27], v[108:111]\n" \
"buffer_load_dwordx4 v50, %[srd], s64 offen offset:1024 sc1 lds\n" \
"v_mfma_f32_16x16x32_bf16 v[108:111], a[148:151], a[20:23], v[108:111]\n" \
"ds_read_b128 a[180:183], v49 offset:5120\n" \
"ds_read_b128 a[184:187], v49 offset:6144\n" \
"v_mfma_f32_16x16x32_bf16 v[108:111], a[144:147], a[16:19], v[108:111]\n" \
"ds_read_b128 a[188:191], v49 offset:7168\n" \
"s_mov_b32 s65, %[m2]\n" \
"v_mfma_f32_16x16x32_bf16 v[112:115], a[128:131], a[32:35], v[40:43]\n" \
"v_add_u32_e32 v63, %[p4], v20\n" \
"s_add_u32 s67, %[t2], 40960\n" \
"v_mfma_f32_16x16x32_bf16 v[112:115], a[132:135], a[36:39], v[112:115]\n" \
"v_add_u32_e32 v50, 0x6000, v63\n" \
"s_xor_b32 m0, s65, 0x8000\n" \
"v_mfma_f32_16x16x32_bf16 v[112:115], a[136:139], a[40:43], v[112:115]\n" \
"v_and_b32_e32 v64, v12, v64\n" \
"v_and_b32_e32 v65, v13, v65\n" \
"v_mfma_f32_16x16x32_bf16 v[112:115], a[140:143], a[44:47], v[112:115]\n" \
"buffer_load_dwordx4 v50, %[srd], s67 offen offset:0 sc1 lds\n" \
"v_mfma_f32_16x16x32_bf16 v[120:123], a[140:143], a[60:63], v[44:47]\n" \
"v_and_b32_e32 v66, v14, v66\n" \
"v_and_b32_e32 v67, v15, v67\n" \
"v_mfma_f32_16x16x32_bf16 v[120:123], a[136:139], a[56:59], v[120:123]\n" \
"v_exp_f32_e32 v80, v80\n" \
"v_mfma_f32_16x16x32_bf16 v[120:123], a[132:135], a[52:55], v[120:123]\n" \
"v_exp_f32_e32 v81, v81\n" \
"v_mfma_f32_16x16x32_bf16 v[120:123], a[128:131], a[48:51], v[120:123]\n" \
"v_exp_f32_e32 v82, v82\n" \
"v_mfma_f32_16x16x32_bf16 v[116:119], a[144:147], a[32:35], v[40:43]\n" \
"buffer_load_dwordx4 v50, %[srd], s67 offen offset:1024 sc1 lds\n" \
"v_mfma_f32_16x16x32_bf16 v[116:119], a[148:151], a[36:39], v[116:119]\n" \
"v_exp_f32_e32 v83, v83\n" \
"v_mfma_f32_16x16x32_bf16 v[116:119], a[152:155], a[40:43], v[116:119]\n" \
"v_cvt_pk_bf16_f32 v80, v80, v81\n" \
"v_cvt_pk_bf16_f32 v81, v82, v83\n" \
"v_mfma_f32_16x16x32_bf16 v[116:119], a[156:159], a[44:47], v[116:119]\n" \
"v_exp_f32_e32 v84, v84\n" \
"v_mfma_f32_16x16x32_bf16 v[124:127], a[156:159], a[60:63], v[44:47]\n" \
"v_exp_f32_e32 v85, v85\n" \
"v_mfma_f32_16x16x32_bf16 v[124:127], a[152:155], a[56:59], v[124:127]\n" \
"v_exp_f32_e32 v86, v86\n" \
"v_mfma_f32_16x16x32_bf16 v[124:127], a[148:151], a[52:55], v[124:127]\n" \
"buffer_load_dwordx4 v50, %[srd], s67 offen offset:2048 sc1 lds\n" \
"v_mfma_f32_16x16x32_bf16 v[124:127], a[144:147], a[48:51], v[124:127]\n" \
"v_cvt_pk_bf16_f32 v82, v84, v85\n" \
"s_waitcnt lgkmcnt(0)\n" \
"v_mfma_f32_16x16x32_bf16 v[128:131], a[160:163], v[64:67], v[128:131]\n" \
"v_exp_f32_e32 v87, v87\n" \
"v_mfma_f32_16x16x32_bf16 v[132:135], a[164:167], v[64:67], v[132:135]\n" \
"v_cvt_pk_bf16_f32 v83, v86, v87\n" \
"s_mov_b32 s64, %[t2]\n" \
"v_mfma_f32_16x16x32_bf16 v[136:139], a[168:171], v[64:67], v[136:139]\n" \
"v_add_u32_e32 v51, 0x12000, v63\n" \
"ds_read_b32 v58, v18 offset:512\n" \
"v_mfma_f32_16x16x32_bf16 v[140:143], a[172:175], v[64:67], v[140:143]\n" \
"v_exp_f32_e32 v88, v88\n" \
"v_mfma_f32_16x16x32_bf16 v[144:147], a[176:179], v[64:67], v[144:147]\n" \
"buffer_load_dwordx4 v50, %[srd], s67 offen offset:3072 sc1 lds\n" \
"v_mfma_f32_16x16x32_bf16 v[148:151], a[180:183], v[64:67], v[148:151]\n" \
"s_xor_b32 m0, s65, 0xa000\n" \
"ds_read_b32 v59, v18 offset:640\n" \
"v_mfma_f32_16x16x32_bf16 v[152:155], a[184:187], v[64:67], v[152:155]\n" \
"v_exp_f32_e32 v89, v89\n" \
"v_mfma_f32_16x16x32_bf16 v[156:159], a[188:191], v[64:67], v[156:159]\n" \
"v_exp_f32_e32 v90, v90\n" \
"v_mfma_f32_16x16x32_bf16 a[192:195], a[212:215], v[64:67], a[192:195]\n" \
"v_exp_f32_e32 v91, v91\n" \
"v_mfma_f32_16x16x32_bf16 v[160:163], a[160:163], v[72:75], v[160:163]\n" \
"buffer_load_dwordx4 v51, %[srd], s64 offen offset:0 sc1 lds\n" \
"v_mfma_f32_16x16x32_bf16 v[164:167], a[164:167], v[72:75], v[164:167]\n" \
"v_cvt_pk_bf16_f32 v88, v88, v89\n" \
"v_cvt_pk_bf16_f32 v89, v90, v91\n" \
"v_mfma_f32_16x16x32_bf16 v[168:171], a[168:171], v[72:75], v[168:171]\n" \
"v_exp_f32_e32 v92, v92\n" \
"v_mfma_f32_16x16x32_bf16 v[172:175], a[172:175], v[72:75], v[172:175]\n" \
"v_exp_f32_e32 v93, v93\n" \
"v_mfma_f32_16x16x32_bf16 v[176:179], a[176:179], v[72:75], v[176:179]\n" \
"v_exp_f32_e32 v94, v94\n" \
"v_mfma_f32_16x16x32_bf16 v[180:183], a[180:183], v[72:75], v[180:183]\n" \
"v_exp_f32_e32 v95, v95\n" \
"v_mfma_f32_16x16x32_bf16 v[184:187], a[184:187], v[72:75], v[184:187]\n" \
"buffer_load_dwordx4 v51, %[srd], s64 offen offset:1024 sc1 lds\n" \
"v_mfma_f32_16x16x32_bf16 v[188:191], a[188:191], v[72:75], v[188:191]\n" \
"v_cvt_pk_bf16_f32 v90, v92, v93\n" \
"v_cvt_pk_bf16_f32 v91, v94, v95\n" \
"v_mfma_f32_16x16x32_bf16 a[196:199], a[212:215], v[72:75], a[196:199]\n" \
"ds_read_b32 v60, v18 offset:768\n" \
"ds_read_b32 v61, v18 offset:896\n" \
"v_mfma_f32_16x16x32_bf16 v[192:195], a[160:163], v[80:83], v[192:195]\n" \
"ds_read_b128 a[128:131], v48 offset:0\n" \
"ds_read_b128 a[132:135], v48 offset:1024\n" \
"v_mfma_f32_16x16x32_bf16 v[196:199], a[164:167], v[80:83], v[196:199]\n" \
"ds_read_b128 a[136:139], v48 offset:2048\n" \
"ds_read_b128 a[140:143], v48 offset:3072\n" \
"v_mfma_f32_16x16x32_bf16 v[200:203], a[168:171], v[80:83], v[200:203]\n" \
"buffer_load_dwordx4 v51, %[srd], s64 offen offset:2048 sc1 lds\n" \
"v_mfma_f32_16x16x32_bf16 v[204:207], a[172:175], v[80:83], v[204:207]\n" \
"ds_read_b128 a[144:147], v48 offset:4096\n" \
"ds_read_b128 a[148:151], v48 offset:5120\n" \
"v_mfma_f32_16x16x32_bf16 v[208:211], a[176:179], v[80:83], v[208:211]\n" \
"ds_read_b128 a[152:155], v48 offset:6144\n" \
"ds_read_b128 a[156:159], v48 offset:7168\n" \
"v_mfma_f32_16x16x32_bf16 v[212:215], a[180:183], v[80:83], v[212:215]\n" \
"v_add_u32_e32 v52, 0x14000, v63\n" \
"v_add_u32_e32 v53, 0x20000, v63\n" \
"v_mfma_f32_16x16x32_bf16 v[216:219], a[184:187], v[80:83], v[216:219]\n" \
"v_exp_f32_e32 v104, v104\n" \
"v_mfma_f32_16x16x32_bf16 v[220:223], a[188:191], v[80:83], v[220:223]\n" \
"v_exp_f32_e32 v105, v105\n" \
"v_mfma_f32_16x16x32_bf16 a[200:203], a[212:215], v[80:83], a[200:203]\n" \
"buffer_load_dwordx4 v51, %[srd], s64 offen offset:3072 sc1 lds\n" \
"v_mfma_f32_16x16x32_bf16 v[224:227], a[160:163], v[88:91], v[224:227]\n" \
"v_exp_f32_e32 v106, v106\n" \
"v_mfma_f32_16x16x32_bf16 v[228:231], a[164:167], v[88:91], v[228:231]\n" \
"v_exp_f32_e32 v107, v107\n" \
"v_mfma_f32_16x16x32_bf16 v[232:235], a[168:171], v[88:91], v[232:235]\n" \
"v_cvt_pk_bf16_f32 v104, v104, v105\n" \
"v_cvt_pk_bf16_f32 v105, v106, v107\n" \
"v_mfma_f32_16x16x32_bf16 v[236:239], a[172:175], v[88:91], v[236:239]\n" \
"v_exp_f32_e32 v108, v108\n" \
"v_mfma_f32_16x16x32_bf16 v[240:243], a[176:179], v[88:91], v[240:243]\n" \
"v_exp_f32_e32 v109, v109\n" \
"v_mfma_f32_16x16x32_bf16 v[244:247], a[180:183], v[88:91], v[244:247]\n" \
"v_exp_f32_e32 v110, v110\n" \
"v_mfma_f32_16x16x32_bf16 v[248:251], a[184:187], v[88:91], v[248:251]\n" \
"v_exp_f32_e32 v111, v111\n" \
"v_mfma_f32_16x16x32_bf16 v[252:255], a[188:191], v[88:91], v[252:255]\n" \
"v_cvt_pk_bf16_f32 v106, v108, v109\n" \
"v_cvt_pk_bf16_f32 v107, v110, v111\n" \
"v_mfma_f32_16x16x32_bf16 a[204:207], a[212:215], v[88:91], a[204:207]\n" \
"v_and_b32_e32 v104, v12, v104\n" \
"v_and_b32_e32 v105, v13, v105\n" \
"v_and_b32_e32 v106, v14, v106\n" \
"v_and_b32_e32 v107, v15, v107\n" \
"v_exp_f32_e32 v112, v112\n" \
"v_exp_f32_e32 v113, v113\n" \
"v_exp_f32_e32 v114, v114\n" \
"v_exp_f32_e32 v115, v115\n" \
"v_cvt_pk_bf16_f32 v112, v112, v113\n" \
"v_exp_f32_e32 v116, v116\n" \
"v_exp_f32_e32 v117, v117\n" \
"v_exp_f32_e32 v118, v118\n" \
"v_exp_f32_e32 v119, v119\n" \
"v_cvt_pk_bf16_f32 v113, v114, v115\n" \
"v_cvt_pk_bf16_f32 v114, v116, v117\n" \
"v_cvt_pk_bf16_f32 v115, v118, v119\n" \
"v_exp_f32_e32 v120, v120\n" \
"v_exp_f32_e32 v121, v121\n" \
"v_exp_f32_e32 v122, v122\n" \
"v_exp_f32_e32 v123, v123\n" \
"s_waitcnt vmcnt(10) lgkmcnt(0)\n" \
"s_barrier\n" \
"v_mfma_f32_16x16x32_bf16 v[80:83], a[128:131], a[32:35], v[40:43]\n" \
"ds_read_b128 a[160:163], v49 offset:8192\n" \
"ds_read_b128 a[164:167], v49 offset:9216\n" \
"v_mfma_f32_16x16x32_bf16 v[80:83], a[132:135], a[36:39], v[80:83]\n" \
"ds_read_b128 a[168:171], v49 offset:10240\n" \
"ds_read_b128 a[172:175], v49 offset:11264\n" \
"v_mfma_f32_16x16x32_bf16 v[80:83], a[136:139], a[40:43], v[80:83]\n" \
"s_xor_b32 m0, s65, 0x18000\n" \
"ds_read_b128 a[176:179], v49 offset:12288\n" \
"v_mfma_f32_16x16x32_bf16 v[80:83], a[140:143], a[44:47], v[80:83]\n" \
"buffer_load_dwordx4 v52, %[srd], s64 offen offset:0 sc1 lds\n" \
"v_mfma_f32_16x16x32_bf16 v[88:91], a[140:143], a[60:63], v[44:47]\n" \
"ds_read_b128 a[180:183], v49 offset:13312\n" \
"ds_read_b128 a[184:187], v49 offset:14336\n" \
"v_mfma_f32_16x16x32_bf16 v[88:91], a[136:139], a[56:59], v[88:91]\n" \
"buffer_load_dwordx4 v52, %[srd], s64 offen offset:1024 sc1 lds\n" \
"v_mfma_f32_16x16x32_bf16 v[88:91], a[132:135], a[52:55], v[88:91]\n" \
"ds_read_b128 a[188:191], v49 offset:15360\n" \
"v_readlane_b32 s93, v11, 19\n" \
"v_mfma_f32_16x16x32_bf16 v[88:91], a[128:131], a[48:51], v[88:91]\n" \
"buffer_load_dwordx4 v52, %[srd], s64 offen offset:2048 sc1 lds\n" \
"v_mfma_f32_16x16x32_bf16 v[84:87], a[144:147], a[32:35], v[40:43]\n" \
"v_cvt_pk_bf16_f32 v120, v120, v121\n" \
"v_readlane_b32 s80, v11, 11\n" \
"v_mfma_f32_16x16x32_bf16 v[84:87], a[148:151], a[36:39], v[84:87]\n" \
"v_exp_f32_e32 v124, v124\n" \
"v_mfma_f32_16x16x32_bf16 v[84:87], a[152:155], a[40:43], v[84:87]\n" \
"buffer_load_dwordx4 v52, %[srd], s64 offen offset:3072 sc1 lds\n" \
"v_mfma_f32_16x16x32_bf16 v[84:87], a[156:159], a[44:47], v[84:87]\n" \
"s_mov_b32 m0, s65\n" \
"v_readlane_b32 s81, v11, 12\n" \
"v_mfma_f32_16x16x32_bf16 v[92:95], a[156:159], a[60:63], v[44:47]\n" \
"v_exp_f32_e32 v125, v125\n" \
"v_mfma_f32_16x16x32_bf16 v[92:95], a[152:155], a[56:59], v[92:95]\n" \
"buffer_load_dwordx4 v53, %[srd], s64 offen offset:0 sc1 lds\n" \
"v_mfma_f32_16x16x32_bf16 v[92:95], a[148:151], a[52:55], v[92:95]\n" \
"v_exp_f32_e32 v126, v126\n" \
"v_mfma_f32_16x16x32_bf16 v[92:95], a[144:147], a[48:51], v[92:95]\n" \
"s_mov_b32 s82, 0x47fca0\n" \
"s_waitcnt lgkmcnt(4)\n" \
"v_mfma_f32_16x16x32_bf16 v[160:163], a[160:163], v[104:107], v[160:163]\n" \
"buffer_load_dwordx4 v53, %[srd], s64 offen offset:1024 sc1 lds\n" \
"v_mfma_f32_16x16x32_bf16 v[164:167], a[164:167], v[104:107], v[164:167]\n" \
"v_exp_f32_e32 v127, v127\n" \
"v_mfma_f32_16x16x32_bf16 v[168:171], a[168:171], v[104:107], v[168:171]\n" \
"s_mov_b32 s83, 0x20000\n" \
"v_readlane_b32 s92, v11, 24\n" \
"v_mfma_f32_16x16x32_bf16 v[172:175], a[172:175], v[104:107], v[172:175]\n" \
"v_cvt_pk_bf16_f32 v121, v122, v123\n" \
"s_waitcnt lgkmcnt(1)\n" \
"v_mfma_f32_16x16x32_bf16 v[176:179], a[176:179], v[104:107], v[176:179]\n" \
"buffer_load_dwordx4 v53, %[srd], s64 offen offset:2048 sc1 lds\n" \
"v_mfma_f32_16x16x32_bf16 v[180:183], a[180:183], v[104:107], v[180:183]\n" \
"v_cvt_pk_bf16_f32 v122, v124, v125\n" \
"v_cvt_pk_bf16_f32 v123, v126, v127\n" \
"v_mfma_f32_16x16x32_bf16 v[184:187], a[184:187], v[104:107], v[184:187]\n" \
"ds_read_b128 a[128:131], v48 offset:8192\n" \
"s_waitcnt lgkmcnt(1)\n" \
"v_mfma_f32_16x16x32_bf16 v[188:191], a[188:191], v[104:107], v[188:191]\n" \
"buffer_load_dwordx4 v53, %[srd], s64 offen offset:3072 sc1 lds\n" \
"v_mfma_f32_16x16x32_bf16 a[196:199], a[212:215], v[104:107], a[196:199]\n" \
"s_add_u32 m0, s93, 0x0\n" \
"ds_read_b128 a[132:135], v48 offset:9216\n" \
"v_mfma_f32_16x16x32_bf16 v[192:195], a[160:163], v[112:115], v[192:195]\n" \
"ds_read_b128 a[136:139], v48 offset:10240\n" \
"ds_read_b128 a[140:143], v48 offset:11264\n" \
"v_mfma_f32_16x16x32_bf16 v[196:199], a[164:167], v[112:115], v[196:199]\n" \
"buffer_load_dwordx4 v0, s[80:83], s92 offen offset:0 lds\n" \
"v_mfma_f32_16x16x32_bf16 v[200:203], a[168:171], v[112:115], v[200:203]\n" \
"ds_read_b128 a[144:147], v48 offset:12288\n" \
"ds_read_b128 a[148:151], v48 offset:13312\n" \
"v_mfma_f32_16x16x32_bf16 v[204:207], a[172:175], v[112:115], v[204:207]\n" \
"ds_read_b128 a[152:155], v48 offset:14336\n" \
"ds_read_b128 a[156:159], v48 offset:15360\n" \
"v_mfma_f32_16x16x32_bf16 v[208:211], a[176:179], v[112:115], v[208:211]\n" \
"s_xor_b32 s65, s65, 0x8000\n" \
"s_add_u32 s64, s64, 0x10000\n" \
"v_mfma_f32_16x16x32_bf16 v[212:215], a[180:183], v[112:115], v[212:215]\n" \
"buffer_load_dwordx4 v1, s[80:83], s92 offen offset:1024 lds\n" \
"v_mfma_f32_16x16x32_bf16 v[216:219], a[184:187], v[112:115], v[216:219]\n" \
"v_add_u32_e32 v49, 0x10000, v48\n" \
"v_add_u32_e32 v48, %[k2], v20\n" \
"v_mfma_f32_16x16x32_bf16 v[220:223], a[188:191], v[112:115], v[220:223]\n" \
"v_exp_f32_e32 v80, v80\n" \
"v_mfma_f32_16x16x32_bf16 a[200:203], a[212:215], v[112:115], a[200:203]\n" \
"buffer_load_dwordx4 v2, s[80:83], s92 offen offset:2048 lds\n" \
"v_mfma_f32_16x16x32_bf16 v[224:227], a[160:163], v[120:123], v[224:227]\n" \
"v_exp_f32_e32 v81, v81\n" \
"v_mfma_f32_16x16x32_bf16 v[228:231], a[164:167], v[120:123], v[228:231]\n" \
"v_exp_f32_e32 v82, v82\n" \
"v_mfma_f32_16x16x32_bf16 v[232:235], a[168:171], v[120:123], v[232:235]\n" \
"buffer_load_dwordx4 v3, s[80:83], s92 offen offset:3072 lds\n" \
"v_mfma_f32_16x16x32_bf16 v[236:239], a[172:175], v[120:123], v[236:239]\n" \
"v_exp_f32_e32 v83, v83\n" \
"v_mfma_f32_16x16x32_bf16 v[240:243], a[176:179], v[120:123], v[240:243]\n" \
"v_cvt_pk_bf16_f32 v80, v80, v81\n" \
"v_cvt_pk_bf16_f32 v81, v82, v83\n" \
"v_mfma_f32_16x16x32_bf16 v[244:247], a[180:183], v[120:123], v[244:247]\n" \
"v_exp_f32_e32 v84, v84\n" \
"v_mfma_f32_16x16x32_bf16 v[248:251], a[184:187], v[120:123], v[248:251]\n" \
"v_exp_f32_e32 v85, v85\n" \
"v_mfma_f32_16x16x32_bf16 v[252:255], a[188:191], v[120:123], v[252:255]\n" \
"v_exp_f32_e32 v86, v86\n" \
"v_mfma_f32_16x16x32_bf16 a[204:207], a[212:215], v[120:123], a[204:207]\n" \
"v_exp_f32_e32 v87, v87\n" \
"v_cvt_pk_bf16_f32 v82, v84, v85\n" \
"v_cvt_pk_bf16_f32 v83, v86, v87\n" \
"v_and_b32_e32 v80, v12, v80\n" \
"v_and_b32_e32 v81, v13, v81\n" \
"v_and_b32_e32 v82, v14, v82\n" \
"v_and_b32_e32 v83, v15, v83\n" \
"v_exp_f32_e32 v88, v88\n" \
"v_exp_f32_e32 v89, v89\n" \
"v_exp_f32_e32 v90, v90\n" \
"v_exp_f32_e32 v91, v91\n" \
"v_cvt_pk_bf16_f32 v88, v88, v89\n" \
"v_exp_f32_e32 v92, v92\n" \
"v_exp_f32_e32 v93, v93\n" \
"v_exp_f32_e32 v94, v94\n" \
"v_exp_f32_e32 v95, v95\n" \
"v_cvt_pk_bf16_f32 v89, v90, v91\n" \
"L_pf15:\n" \
"s_cbranch_execz L_retA\n" \
"s_nop 0\n" \
"v_cvt_pk_bf16_f32 v90, v92, v93\n" \
"v_cvt_pk_bf16_f32 v91, v94, v95\n" \
"v_accvgpr_read_b32 v54, a192\n" \
"v_max_f32_e32 v62, v62, v54\n" \
"v_accvgpr_read_b32 v55, a196\n" \
"v_max_f32_e32 v62, v62, v55\n" \
"v_rcp_f32_e32 v54, v54\n" \
"v_rcp_f32_e32 v55, v55\n" \
"v_mul_f32_e32 v54, v54, v58\n" \
"v_mul_f32_e32 v55, v55, v59\n" \
"v_mul_f32_e32 v128, v54, v128\n" \
"v_mul_f32_e32 v129, v54, v129\n" \
"v_mul_f32_e32 v136, v54, v136\n" \
"v_mul_f32_e32 v137, v54, v137\n" \
"v_mul_f32_e32 v144, v54, v144\n" \
"v_mul_f32_e32 v145, v54, v145\n" \
"v_mul_f32_e32 v152, v54, v152\n" \
"v_mul_f32_e32 v153, v54, v153\n" \
"v_mul_f32_e32 v160, v55, v160\n" \
"v_mul_f32_e32 v161, v55, v161\n" \
"v_mul_f32_e32 v168, v55, v168\n" \
"v_mul_f32_e32 v169, v55, v169\n" \
"v_mul_f32_e32 v176, v55, v176\n" \
"v_mul_f32_e32 v177, v55, v177\n" \
"v_mul_f32_e32 v184, v55, v184\n" \
"v_mul_f32_e32 v185, v55, v185\n" \
"v_cvt_pk_bf16_f32 v128, v128, v129\n" \
"v_mul_f32_e32 v130, v54, v130\n" \
"v_mul_f32_e32 v131, v54, v131\n" \
"v_cvt_pk_bf16_f32 v136, v136, v137\n" \
"v_mul_f32_e32 v138, v54, v138\n" \
"v_mul_f32_e32 v139, v54, v139\n" \
"v_cvt_pk_bf16_f32 v144, v144, v145\n" \
"v_mul_f32_e32 v146, v54, v146\n" \
"v_mul_f32_e32 v147, v54, v147\n" \
"v_cvt_pk_bf16_f32 v152, v152, v153\n" \
"v_mul_f32_e32 v154, v54, v154\n" \
"v_mul_f32_e32 v155, v54, v155\n" \
"v_cvt_pk_bf16_f32 v160, v160, v161\n" \
"v_mul_f32_e32 v162, v55, v162\n" \
"v_mul_f32_e32 v163, v55, v163\n" \
"v_cvt_pk_bf16_f32 v168, v168, v169\n" \
"v_mul_f32_e32 v170, v55, v170\n" \
"v_mul_f32_e32 v171, v55, v171\n" \
"v_cvt_pk_bf16_f32 v176, v176, v177\n" \
"v_mul_f32_e32 v178, v55, v178\n" \
"v_mul_f32_e32 v179, v55, v179\n" \
"v_cvt_pk_bf16_f32 v184, v184, v185\n" \
"v_mul_f32_e32 v186, v55, v186\n" \
"v_mul_f32_e32 v187, v55, v187\n" \
"v_cvt_pk_bf16_f32 v129, v130, v131\n" \
"v_mul_f32_e32 v132, v54, v132\n" \
"v_mul_f32_e32 v133, v54, v133\n" \
"v_mul_f32_e32 v134, v54, v134\n" \
"v_mul_f32_e32 v135, v54, v135\n" \
"v_cvt_pk_bf16_f32 v137, v138, v139\n" \
"v_mul_f32_e32 v140, v54, v140\n" \
"v_mul_f32_e32 v141, v54, v141\n" \
"v_mul_f32_e32 v142, v54, v142\n" \
"v_mul_f32_e32 v143, v54, v143\n" \
"v_cvt_pk_bf16_f32 v145, v146, v147\n" \
"v_mul_f32_e32 v148, v54, v148\n" \
"v_mul_f32_e32 v149, v54, v149\n" \
"v_mul_f32_e32 v150, v54, v150\n" \
"v_mul_f32_e32 v151, v54, v151\n" \
"v_cvt_pk_bf16_f32 v153, v154, v155\n" \
"v_mul_f32_e32 v156, v54, v156\n" \
"v_mul_f32_e32 v157, v54, v157\n" \
"v_mul_f32_e32 v158, v54, v158\n" \
"v_mul_f32_e32 v159, v54, v159\n" \
"v_cvt_pk_bf16_f32 v161, v162, v163\n" \
"v_mul_f32_e32 v164, v55, v164\n" \
"v_mul_f32_e32 v165, v55, v165\n" \
"v_mul_f32_e32 v166, v55, v166\n" \
"s_waitcnt vmcnt(12) lgkmcnt(0)\n" \
"s_barrier\n" \
"v_mfma_f32_16x16x32_bf16 v[120:123], a[128:131], a[48:51], v[44:47]\n" \
"s_xor_b32 m0, s65, 0x12000\n" \
"ds_read_b128 a[160:163], v49 offset:0\n" \
"v_mfma_f32_16x16x32_bf16 v[120:123], a[132:135], a[52:55], v[120:123]\n" \
"buffer_load_dwordx4 v50, %[srd], s64 offen offset:0 sc1 lds\n" \
"v_mfma_f32_16x16x32_bf16 v[120:123], a[136:139], a[56:59], v[120:123]\n" \
"ds_read_b128 a[164:167], v49 offset:1024\n" \
"ds_read_b128 a[168:171], v49 offset:2048\n" \
"v_mfma_f32_16x16x32_bf16 v[120:123], a[140:143], a[60:63], v[120:123]\n" \
"buffer_load_dwordx4 v50, %[srd], s64 offen offset:1024 sc1 lds\n" \
"v_mfma_f32_16x16x32_bf16 v[124:127], a[156:159], a[60:63], v[44:47]\n" \
"ds_read_b128 a[172:175], v49 offset:3072\n" \
"ds_read_b128 a[176:179], v49 offset:4096\n" \
"v_mfma_f32_16x16x32_bf16 v[124:127], a[152:155], a[56:59], v[124:127]\n" \
"ds_read_b128 a[180:183], v49 offset:5120\n" \
"ds_read_b128 a[184:187], v49 offset:6144\n" \
"v_mfma_f32_16x16x32_bf16 v[124:127], a[148:151], a[52:55], v[124:127]\n" \
"ds_read_b128 a[188:191], v49 offset:7168\n" \
"ds_read_b128 a[0:3], v49 offset:8192\n" \
"v_mfma_f32_16x16x32_bf16 v[124:127], a[144:147], a[48:51], v[124:127]\n" \
"buffer_load_dwordx4 v50, %[srd], s64 offen offset:2048 sc1 lds\n" \
"v_mfma_f32_16x16x32_bf16 v[32:35], a[220:223], a[64:67], 0\n" \
"ds_read_b128 a[4:7], v49 offset:9216\n" \
"ds_read_b128 a[8:11], v49 offset:10240\n" \
"v_mfma_f32_16x16x32_bf16 v[36:39], a[220:223], a[80:83], 0\n" \
"ds_read_b128 a[12:15], v49 offset:11264\n" \
"ds_read_b128 a[16:19], v49 offset:12288\n" \
"v_mfma_f32_16x16x32_bf16 v[40:43], a[220:223], a[96:99], 0\n" \
"ds_read_b128 a[20:23], v49 offset:13312\n" \
"ds_read_b128 a[24:27], v49 offset:14336\n" \
"v_mfma_f32_16x16x32_bf16 v[44:47], a[220:223], a[112:115], 0\n" \
"buffer_load_dwordx4 v50, %[srd], s64 offen offset:3072 sc1 lds\n" \
"v_mfma_f32_16x16x32_bf16 v[32:35], a[224:227], a[68:71], v[32:35]\n" \
"s_xor_b32 m0, s65, 0xa000\n" \
"ds_read_b128 a[28:31], v49 offset:15360\n" \
"v_mfma_f32_16x16x32_bf16 v[36:39], a[224:227], a[84:87], v[36:39]\n" \
"v_exp_f32_e32 v120, v120\n" \
"v_mfma_f32_16x16x32_bf16 v[40:43], a[224:227], a[100:103], v[40:43]\n" \
"buffer_load_dwordx4 v51, %[srd], s64 offen offset:0 sc1 lds\n" \
"v_mfma_f32_16x16x32_bf16 v[44:47], a[224:227], a[116:119], v[44:47]\n" \
"v_exp_f32_e32 v121, v121\n" \
"v_mfma_f32_16x16x32_bf16 v[32:35], a[228:231], a[72:75], v[32:35]\n" \
"v_exp_f32_e32 v122, v122\n" \
"v_mfma_f32_16x16x32_bf16 v[36:39], a[228:231], a[88:91], v[36:39]\n" \
"v_exp_f32_e32 v123, v123\n" \
"v_mfma_f32_16x16x32_bf16 v[40:43], a[228:231], a[104:107], v[40:43]\n" \
"buffer_load_dwordx4 v51, %[srd], s64 offen offset:1024 sc1 lds\n" \
"v_mfma_f32_16x16x32_bf16 v[44:47], a[228:231], a[120:123], v[44:47]\n" \
"v_cvt_pk_bf16_f32 v120, v120, v121\n" \
"v_cvt_pk_bf16_f32 v121, v122, v123\n" \
"v_mfma_f32_16x16x32_bf16 v[32:35], a[232:235], a[76:79], v[32:35]\n" \
"v_exp_f32_e32 v124, v124\n" \
"v_mfma_f32_16x16x32_bf16 v[36:39], a[232:235], a[92:95], v[36:39]\n" \
"v_exp_f32_e32 v125, v125\n" \
"v_mfma_f32_16x16x32_bf16 v[40:43], a[232:235], a[108:111], v[40:43]\n" \
"buffer_load_dwordx4 v51, %[srd], s64 offen offset:2048 sc1 lds\n" \
"v_mfma_f32_16x16x32_bf16 v[44:47], a[232:235], a[124:127], v[44:47]\n" \
"v_cvt_pk_bf16_f32 v122, v124, v125\n" \
"s_waitcnt lgkmcnt(3)\n" \
"v_mfma_f32_16x16x32_bf16 v[192:195], a[160:163], v[80:83], v[192:195]\n" \
"v_exp_f32_e32 v126, v126\n" \
"v_mfma_f32_16x16x32_bf16 v[196:199], a[164:167], v[80:83], v[196:199]\n" \
"buffer_load_dwordx4 v51, %[srd], s64 offen offset:3072 sc1 lds\n" \
"v_mfma_f32_16x16x32_bf16 v[200:203], a[168:171], v[80:83], v[200:203]\n" \
"s_add_u32 m0, s93, 0x1000\n" \
"v_and_b32_e32 v120, v12, v120\n" \
"v_mfma_f32_16x16x32_bf16 v[204:207], a[172:175], v[80:83], v[204:207]\n" \
"v_exp_f32_e32 v127, v127\n" \
"v_mfma_f32_16x16x32_bf16 v[208:211], a[176:179], v[80:83], v[208:211]\n" \
"v_cvt_pk_bf16_f32 v123, v126, v127\n" \
"v_and_b32_e32 v121, v13, v121\n" \
"v_mfma_f32_16x16x32_bf16 v[212:215], a[180:183], v[80:83], v[212:215]\n" \
"buffer_load_dwordx4 v4, s[80:83], s92 offen offset:0 lds\n" \
"v_mfma_f32_16x16x32_bf16 v[216:219], a[184:187], v[80:83], v[216:219]\n" \
"v_and_b32_e32 v122, v14, v122\n" \
"v_and_b32_e32 v123, v15, v123\n" \
"v_mfma_f32_16x16x32_bf16 v[220:223], a[188:191], v[80:83], v[220:223]\n" \
"ds_read_b128 a[128:131], v48 offset:0\n" \
"ds_read_b128 a[132:135], v48 offset:1024\n" \
"v_mfma_f32_16x16x32_bf16 a[200:203], a[212:215], v[80:83], a[200:203]\n" \
"ds_read_b128 a[136:139], v48 offset:2048\n" \
"ds_read_b128 a[140:143], v48 offset:3072\n" \
"v_mfma_f32_16x16x32_bf16 v[224:227], a[160:163], v[88:91], v[224:227]\n" \
"buffer_load_dwordx4 v5, s[80:83], s92 offen offset:1024 lds\n" \
"v_mfma_f32_16x16x32_bf16 v[228:231], a[164:167], v[88:91], v[228:231]\n" \
"ds_read_b128 a[144:147], v48 offset:4096\n" \
"ds_read_b128 a[148:151], v48 offset:5120\n" \
"v_mfma_f32_16x16x32_bf16 v[232:235], a[168:171], v[88:91], v[232:235]\n" \
"ds_read_b128 a[152:155], v48 offset:6144\n" \
"ds_read_b128 a[156:159], v48 offset:7168\n" \
"v_mfma_f32_16x16x32_bf16 v[236:239], a[172:175], v[88:91], v[236:239]\n" \
"buffer_load_dwordx4 v6, s[80:83], s92 offen offset:2048 lds\n" \
"v_mfma_f32_16x16x32_bf16 v[240:243], a[176:179], v[88:91], v[240:243]\n" \
"v_mul_f32_e32 v167, v55, v167\n" \
"v_cvt_pk_bf16_f32 v169, v170, v171\n" \
"v_mfma_f32_16x16x32_bf16 v[244:247], a[180:183], v[88:91], v[244:247]\n" \
"v_mul_f32_e32 v172, v55, v172\n" \
"v_mul_f32_e32 v173, v55, v173\n" \
"v_mfma_f32_16x16x32_bf16 v[248:251], a[184:187], v[88:91], v[248:251]\n" \
"v_mul_f32_e32 v174, v55, v174\n" \
"v_mul_f32_e32 v175, v55, v175\n" \
"v_mfma_f32_16x16x32_bf16 v[252:255], a[188:191], v[88:91], v[252:255]\n" \
"buffer_load_dwordx4 v7, s[80:83], s92 offen offset:3072 lds\n" \
"v_mfma_f32_16x16x32_bf16 a[204:207], a[212:215], v[88:91], a[204:207]\n" \
"v_cvt_pk_bf16_f32 v177, v178, v179\n" \
"v_mul_f32_e32 v180, v55, v180\n" \
"v_mfma_f32_16x16x32_bf16 v[224:227], a[0:3], v[120:123], v[224:227]\n" \
"v_mul_f32_e32 v181, v55, v181\n" \
"v_mul_f32_e32 v182, v55, v182\n" \
"v_mfma_f32_16x16x32_bf16 v[228:231], a[4:7], v[120:123], v[228:231]\n" \
"v_mul_f32_e32 v183, v55, v183\n" \
"v_cvt_pk_bf16_f32 v185, v186, v187\n" \
"v_mfma_f32_16x16x32_bf16 v[232:235], a[8:11], v[120:123], v[232:235]\n" \
"v_mul_f32_e32 v188, v55, v188\n" \
"v_mul_f32_e32 v189, v55, v189\n" \
"v_mfma_f32_16x16x32_bf16 v[236:239], a[12:15], v[120:123], v[236:239]\n" \
"v_mul_f32_e32 v190, v55, v190\n" \
"v_mul_f32_e32 v191, v55, v191\n" \
"v_mfma_f32_16x16x32_bf16 v[240:243], a[16:19], v[120:123], v[240:243]\n" \
"v_cvt_pk_bf16_f32 v130, v132, v133\n" \
"s_waitcnt lgkmcnt(4)\n" \
"v_mfma_f32_16x16x32_bf16 v[244:247], a[20:23], v[120:123], v[244:247]\n" \
"v_cvt_pk_bf16_f32 v131, v134, v135\n" \
"v_cvt_pk_bf16_f32 v138, v140, v141\n" \
"buffer_store_dwordx4 v[128:131], v20, s[56:59], s68 offen offset:0 sc1\n" \
"v_mfma_f32_16x16x32_bf16 v[248:251], a[24:27], v[120:123], v[248:251]\n" \
"v_cvt_pk_bf16_f32 v139, v142, v143\n" \
"v_cvt_pk_bf16_f32 v146, v148, v149\n" \
"buffer_store_dwordx4 v[136:139], v20, s[56:59], s68 offen offset:1024 sc1\n" \
"v_mfma_f32_16x16x32_bf16 v[252:255], a[28:31], v[120:123], v[252:255]\n" \
"v_cvt_pk_bf16_f32 v147, v150, v151\n" \
"v_cvt_pk_bf16_f32 v154, v156, v157\n" \
"buffer_store_dwordx4 v[144:147], v20, s[56:59], s68 offen offset:2048 sc1\n" \
"v_mfma_f32_16x16x32_bf16 a[204:207], a[212:215], v[120:123], a[204:207]\n" \
"v_cvt_pk_bf16_f32 v155, v158, v159\n" \
"buffer_store_dwordx4 v[152:155], v20, s[56:59], s68 offen offset:3072 sc1\n" \
"v_cvt_pk_bf16_f32 v162, v164, v165\n" \
"v_cvt_pk_bf16_f32 v163, v166, v167\n" \
"buffer_store_dwordx4 v[160:163], v20, s[56:59], s69 offen offset:0 sc1\n" \
"v_cvt_pk_bf16_f32 v170, v172, v173\n" \
"v_cvt_pk_bf16_f32 v171, v174, v175\n" \
"buffer_store_dwordx4 v[168:171], v20, s[56:59], s69 offen offset:1024 sc1\n" \
"v_cvt_pk_bf16_f32 v178, v180, v181\n" \
"v_cvt_pk_bf16_f32 v179, v182, v183\n" \
"buffer_store_dwordx4 v[176:179], v20, s[56:59], s69 offen offset:2048 sc1\n" \
"v_cvt_pk_bf16_f32 v186, v188, v189\n" \
"v_cvt_pk_bf16_f32 v187, v190, v191\n" \
"buffer_store_dwordx4 v[184:187], v20, s[56:59], s69 offen offset:3072 sc1\n" \
"s_add_u32 m0, s93, 0x2000\n" \
"v_mfma_f32_16x16x32_bf16 a[208:211], a[216:219], v[128:131], a[208:211]\n" \
"buffer_load_dwordx4 v8, s[80:83], s92 offen offset:0 lds\n" \
"v_mfma_f32_16x16x32_bf16 a[208:211], a[216:219], v[136:139], a[208:211]\n" \
"v_mfma_f32_32x32x16_bf16 v[128:143], a[216:219], a[216:219], 0\n" \
"v_mfma_f32_16x16x32_bf16 a[208:211], a[216:219], v[144:147], a[208:211]\n" \
"v_mfma_f32_16x16x32_bf16 a[208:211], a[216:219], v[152:155], a[208:211]\n" \
"v_mfma_f32_32x32x16_bf16 v[144:159], a[216:219], a[216:219], 0\n" \
"v_accvgpr_read_b32 v56, a200\n" \
"v_mfma_f32_16x16x32_bf16 a[208:211], a[216:219], v[160:163], a[208:211]\n" \
"v_max_f32_e32 v62, v62, v56\n" \
"v_rcp_f32_e32 v56, v56\n" \
"v_mfma_f32_16x16x32_bf16 a[208:211], a[216:219], v[168:171], a[208:211]\n" \
"v_mfma_f32_32x32x16_bf16 v[160:175], a[216:219], a[216:219], 0\n" \
"v_mul_f32_e32 v56, v56, v60\n" \
"v_mfma_f32_16x16x32_bf16 a[208:211], a[216:219], v[176:179], a[208:211]\n" \
"v_mul_f32_e32 v192, v56, v192\n" \
"v_mul_f32_e32 v193, v56, v193\n" \
"s_waitcnt vmcnt(21) lgkmcnt(0)\n" \
"s_barrier\n" \
"v_mfma_f32_16x16x32_bf16 v[64:67], a[128:131], a[64:67], v[32:35]\n" \
"s_xor_b32 m0, s65, 0x18000\n" \
"v_cvt_pk_bf16_f32 v192, v192, v193\n" \
"v_mfma_f32_16x16x32_bf16 v[64:67], a[132:135], a[68:71], v[64:67]\n" \
"buffer_load_dwordx4 v52, %[srd], s64 offen offset:0 sc1 lds\n" \
"v_mfma_f32_16x16x32_bf16 v[64:67], a[136:139], a[72:75], v[64:67]\n" \
"v_mul_f32_e32 v194, v56, v194\n" \
"v_mul_f32_e32 v195, v56, v195\n" \
"v_mfma_f32_16x16x32_bf16 v[64:67], a[140:143], a[76:79], v[64:67]\n" \
"buffer_load_dwordx4 v52, %[srd], s64 offen offset:1024 sc1 lds\n" \
"v_mfma_f32_16x16x32_bf16 v[72:75], a[140:143], a[92:95], v[36:39]\n" \
"v_mfma_f32_16x16x32_bf16 a[208:211], a[216:219], v[184:187], a[208:211]\n" \
"v_mfma_f32_32x32x16_bf16 v[176:191], a[216:219], a[216:219], 0\n" \
"v_cvt_pk_bf16_f32 v193, v194, v195\n" \
"v_mul_f32_e32 v196, v56, v196\n" \
"v_mul_f32_e32 v197, v56, v197\n" \
"v_mul_f32_e32 v198, v56, v198\n" \
"v_mul_f32_e32 v199, v56, v199\n" \
"v_mul_f32_e32 v200, v56, v200\n" \
"v_mfma_f32_16x16x32_bf16 v[72:75], a[136:139], a[88:91], v[72:75]\n" \
"v_mul_f32_e32 v201, v56, v201\n" \
"v_cvt_pk_bf16_f32 v194, v196, v197\n" \
"v_mfma_f32_16x16x32_bf16 v[72:75], a[132:135], a[84:87], v[72:75]\n" \
"v_cvt_pk_bf16_f32 v195, v198, v199\n" \
"v_cvt_pk_bf16_f32 v200, v200, v201\n" \
"buffer_store_dwordx4 v[192:195], v20, s[56:59], s70 offen offset:0 sc1\n" \
"v_mfma_f32_16x16x32_bf16 v[72:75], a[128:131], a[80:83], v[72:75]\n" \
"buffer_load_dwordx4 v52, %[srd], s64 offen offset:2048 sc1 lds\n" \
"v_mfma_f32_16x16x32_bf16 v[68:71], a[144:147], a[64:67], v[32:35]\n" \
"v_mul_f32_e32 v202, v56, v202\n" \
"v_mul_f32_e32 v203, v56, v203\n" \
"v_mfma_f32_16x16x32_bf16 v[68:71], a[148:151], a[68:71], v[68:71]\n" \
"v_accvgpr_read_b32 v57, a204\n" \
"v_cvt_pk_bf16_f32 v201, v202, v203\n" \
"v_mfma_f32_32x32x16_bf16 a[192:207], a[216:219], a[216:219], 0\n" \
"v_mfma_f32_16x16x32_bf16 a[208:211], a[216:219], v[192:195], a[208:211]\n" \
"v_mul_f32_e32 v204, v56, v204\n" \
"v_mul_f32_e32 v205, v56, v205\n" \
"v_mfma_f32_16x16x32_bf16 v[68:71], a[152:155], a[72:75], v[68:71]\n" \
"v_mul_f32_e32 v206, v56, v206\n" \
"v_mul_f32_e32 v207, v56, v207\n" \
"v_mfma_f32_16x16x32_bf16 v[68:71], a[156:159], a[76:79], v[68:71]\n" \
"buffer_load_dwordx4 v52, %[srd], s64 offen offset:3072 sc1 lds\n" \
"v_mfma_f32_16x16x32_bf16 v[76:79], a[156:159], a[92:95], v[36:39]\n" \
"s_mov_b32 m0, s65\n" \
"v_mul_f32_e32 v208, v56, v208\n" \
"v_mfma_f32_16x16x32_bf16 v[76:79], a[152:155], a[88:91], v[76:79]\n" \
"v_mul_f32_e32 v209, v56, v209\n" \
"v_max_f32_e32 v62, v62, v57\n" \
"v_mfma_f32_16x16x32_bf16 v[76:79], a[148:151], a[84:87], v[76:79]\n" \
"v_cvt_pk_bf16_f32 v202, v204, v205\n" \
"v_cvt_pk_bf16_f32 v203, v206, v207\n" \
"buffer_store_dwordx4 v[200:203], v20, s[56:59], s70 offen offset:1024 sc1\n" \
"v_mfma_f32_16x16x32_bf16 v[76:79], a[144:147], a[80:83], v[76:79]\n" \
"buffer_load_dwordx4 v53, %[srd], s64 offen offset:0 sc1 lds\n" \
"v_mfma_f32_16x16x32_bf16 v[80:83], a[128:131], a[96:99], v[40:43]\n" \
"v_cvt_pk_bf16_f32 v208, v208, v209\n" \
"v_mul_f32_e32 v210, v56, v210\n" \
"v_mfma_f32_16x16x32_bf16 v[80:83], a[132:135], a[100:103], v[80:83]\n" \
"v_mul_f32_e32 v211, v56, v211\n" \
"v_cvt_pk_bf16_f32 v209, v210, v211\n" \
"v_mfma_f32_16x16x32_bf16 v[80:83], a[136:139], a[104:107], v[80:83]\n" \
"buffer_load_dwordx4 v53, %[srd], s64 offen offset:1024 sc1 lds\n" \
"v_mfma_f32_16x16x32_bf16 v[80:83], a[140:143], a[108:111], v[80:83]\n" \
"v_rcp_f32_e32 v57, v57\n" \
"v_mfma_f32_16x16x32_bf16 v[88:91], a[140:143], a[124:127], v[44:47]\n" \
"ds_read_b128 a[140:143], v48 offset:11264\n" \
"v_mul_f32_e32 v212, v56, v212\n" \
"v_mfma_f32_16x16x32_bf16 v[88:91], a[136:139], a[120:123], v[88:91]\n" \
"ds_read_b128 a[136:139], v48 offset:10240\n" \
"v_mul_f32_e32 v213, v56, v213\n" \
"v_mfma_f32_16x16x32_bf16 v[88:91], a[132:135], a[116:119], v[88:91]\n" \
"buffer_load_dwordx4 v53, %[srd], s64 offen offset:2048 sc1 lds\n" \
"v_mfma_f32_16x16x32_bf16 v[88:91], a[128:131], a[112:115], v[88:91]\n" \
"ds_read_b128 a[128:131], v48 offset:8192\n" \
"ds_read_b128 a[132:135], v48 offset:9216\n" \
"v_mfma_f32_16x16x32_bf16 v[84:87], a[144:147], a[96:99], v[40:43]\n" \
"v_mfma_f32_16x16x32_bf16 a[208:211], a[216:219], v[200:203], a[208:211]\n" \
"v_mfma_f32_32x32x16_bf16 v[192:207], a[216:219], a[216:219], 0\n" \
"v_mul_f32_e32 v214, v56, v214\n" \
"v_mul_f32_e32 v215, v56, v215\n" \
"v_mul_f32_e32 v216, v56, v216\n" \
"v_mul_f32_e32 v217, v56, v217\n" \
"v_cvt_pk_bf16_f32 v210, v212, v213\n" \
"v_cvt_pk_bf16_f32 v211, v214, v215\n" \
"buffer_store_dwordx4 v[208:211], v20, s[56:59], s70 offen offset:2048 sc1\n" \
"v_mfma_f32_16x16x32_bf16 v[84:87], a[148:151], a[100:103], v[84:87]\n" \
"v_cvt_pk_bf16_f32 v216, v216, v217\n" \
"v_mul_f32_e32 v218, v56, v218\n" \
"v_mfma_f32_16x16x32_bf16 v[84:87], a[152:155], a[104:107], v[84:87]\n" \
"v_mul_f32_e32 v219, v56, v219\n" \
"v_mul_f32_e32 v57, v57, v61\n" \
"v_mfma_f32_16x16x32_bf16 v[84:87], a[156:159], a[108:111], v[84:87]\n" \
"v_mfma_f32_16x16x32_bf16 a[208:211], a[216:219], v[208:211], a[208:211]\n" \
"v_cvt_pk_bf16_f32 v217, v218, v219\n" \
"v_mul_f32_e32 v220, v56, v220\n" \
"v_mfma_f32_16x16x32_bf16 v[92:95], a[156:159], a[124:127], v[44:47]\n" \
"ds_read_b128 a[156:159], v48 offset:15360\n" \
"v_mul_f32_e32 v221, v56, v221\n" \
"v_mfma_f32_16x16x32_bf16 v[92:95], a[152:155], a[120:123], v[92:95]\n" \
"ds_read_b128 a[152:155], v48 offset:14336\n" \
"v_mul_f32_e32 v222, v56, v222\n" \
"v_mfma_f32_16x16x32_bf16 v[92:95], a[148:151], a[116:119], v[92:95]\n" \
"ds_read_b128 a[148:151], v48 offset:13312\n" \
"v_mul_f32_e32 v223, v56, v223\n" \
"v_mfma_f32_16x16x32_bf16 v[92:95], a[144:147], a[112:115], v[92:95]\n" \
"ds_read_b128 a[144:147], v48 offset:12288\n" \
"v_mul_f32_e32 v224, v57, v224\n" \
"v_mul_f32_e32 v225, v57, v225\n" \
"v_cvt_pk_bf16_f32 v218, v220, v221\n" \
"v_cvt_pk_bf16_f32 v219, v222, v223\n" \
"buffer_store_dwordx4 v[216:219], v20, s[56:59], s70 offen offset:3072 sc1\n" \
"v_cvt_pk_bf16_f32 v224, v224, v225\n" \
"v_mul_f32_e32 v226, v57, v226\n" \
"v_mul_f32_e32 v227, v57, v227\n" \
"v_mfma_f32_16x16x32_bf16 a[208:211], a[216:219], v[216:219], a[208:211]\n" \
"v_mfma_f32_32x32x16_bf16 v[208:223], a[216:219], a[216:219], 0\n" \
"v_cvt_pk_bf16_f32 v225, v226, v227\n" \
"v_mul_f32_e32 v228, v57, v228\n" \
"v_mul_f32_e32 v229, v57, v229\n" \
"v_mul_f32_e32 v230, v57, v230\n" \
"v_mul_f32_e32 v231, v57, v231\n" \
"v_mul_f32_e32 v232, v57, v232\n" \
"v_mul_f32_e32 v233, v57, v233\n" \
"v_cvt_pk_bf16_f32 v226, v228, v229\n" \
"v_cvt_pk_bf16_f32 v227, v230, v231\n" \
"buffer_store_dwordx4 v[224:227], v20, s[56:59], s71 offen offset:0 sc1\n" \
"v_cvt_pk_bf16_f32 v232, v232, v233\n" \
"v_mul_f32_e32 v234, v57, v234\n" \
"v_mul_f32_e32 v235, v57, v235\n" \
"v_mfma_f32_16x16x32_bf16 a[208:211], a[216:219], v[224:227], a[208:211]\n" \
"v_cvt_pk_bf16_f32 v233, v234, v235\n" \
"v_mul_f32_e32 v236, v57, v236\n" \
"v_mul_f32_e32 v237, v57, v237\n" \
"v_mul_f32_e32 v238, v57, v238\n" \
"v_mul_f32_e32 v239, v57, v239\n" \
"v_mul_f32_e32 v240, v57, v240\n" \
"v_mul_f32_e32 v241, v57, v241\n" \
"v_cvt_pk_bf16_f32 v234, v236, v237\n" \
"v_cvt_pk_bf16_f32 v235, v238, v239\n" \
"buffer_store_dwordx4 v[232:235], v20, s[56:59], s71 offen offset:1024 sc1\n" \
"v_cvt_pk_bf16_f32 v240, v240, v241\n" \
"v_mul_f32_e32 v242, v57, v242\n" \
"v_mul_f32_e32 v243, v57, v243\n" \
"v_mfma_f32_16x16x32_bf16 a[208:211], a[216:219], v[232:235], a[208:211]\n" \
"v_mfma_f32_32x32x16_bf16 v[224:239], a[216:219], a[216:219], 0\n" \
"v_cvt_pk_bf16_f32 v241, v242, v243\n" \
"v_mul_f32_e32 v244, v57, v244\n" \
"v_mul_f32_e32 v245, v57, v245\n" \
"v_mul_f32_e32 v246, v57, v246\n" \
"v_mul_f32_e32 v247, v57, v247\n" \
"v_mul_f32_e32 v248, v57, v248\n" \
"v_mul_f32_e32 v249, v57, v249\n" \
"v_cvt_pk_bf16_f32 v242, v244, v245\n" \
"v_cvt_pk_bf16_f32 v243, v246, v247\n" \
"buffer_store_dwordx4 v[240:243], v20, s[56:59], s71 offen offset:2048 sc1\n" \
"v_cvt_pk_bf16_f32 v248, v248, v249\n" \
"v_mul_f32_e32 v250, v57, v250\n" \
"v_mul_f32_e32 v251, v57, v251\n" \
"v_mfma_f32_16x16x32_bf16 a[208:211], a[216:219], v[240:243], a[208:211]\n" \
"v_cvt_pk_bf16_f32 v249, v250, v251\n" \
"v_mul_f32_e32 v252, v57, v252\n" \
"v_mul_f32_e32 v253, v57, v253\n" \
"v_mul_f32_e32 v254, v57, v254\n" \
"v_mul_f32_e32 v255, v57, v255\n" \
"v_cvt_pk_bf16_f32 v250, v252, v253\n" \
"v_cvt_pk_bf16_f32 v251, v254, v255\n" \
"buffer_store_dwordx4 v[248:251], v20, s[56:59], s71 offen offset:3072 sc1\n" \
"buffer_load_dwordx4 v53, %[srd], s64 offen offset:3072 sc1 lds\n" \
"v_mfma_f32_16x16x32_bf16 a[208:211], a[216:219], v[248:251], a[208:211]\n" \
"v_mfma_f32_32x32x16_bf16 v[240:255], a[216:219], a[216:219], 0\n" \
"v_exp_f32_e32 v64, v64\n" \
"v_exp_f32_e32 v65, v65\n" \
"v_exp_f32_e32 v72, v72\n" \
"v_exp_f32_e32 v73, v73\n" \
"s_add_u32 s64, s64, 0x10000\n" \
"v_cmp_lt_f32_e64 s[72:73], %[lim], v62\n" \
"v_readlane_b32 s95, v11, 3\n" \
"v_accvgpr_read_b32 v63, a208\n" \
"v_cmp_u_f32_e32 vcc, v63, v63\n" \
"v_exp_f32_e32 v66, v66\n" \
"s_waitcnt vmcnt(16) lgkmcnt(0)\n" \
"s_barrier\n" \
"v_exp_f32_e32 v67, v67\n" \
"v_exp_f32_e32 v74, v74\n" \
"v_exp_f32_e32 v75, v75\n" \
"s_or_b64 vcc, s[72:73], vcc\n" \
"s_cselect_b32 s95, 0x40000000, s95\n" \
"s_cselect_b32 s94, 0x7f800000, 0\n" \
"v_cvt_pk_bf16_f32 v64, v64, v65\n" \
"v_exp_f32_e32 v68, v68\n" \
"v_exp_f32_e32 v69, v69\n" \
"v_exp_f32_e32 v70, v70\n" \
"v_exp_f32_e32 v71, v71\n" \
"v_cvt_pk_bf16_f32 v72, v72, v73\n" \
"v_exp_f32_e32 v76, v76\n" \
"v_exp_f32_e32 v77, v77\n" \
"v_exp_f32_e32 v78, v78\n" \
"v_exp_f32_e32 v79, v79\n" \
"v_cvt_pk_bf16_f32 v65, v66, v67\n" \
"v_cvt_pk_bf16_f32 v73, v74, v75\n" \
"v_add_u32_e32 v49, 0x10000, v48\n" \
"s_xor_b32 s65, s65, 0x8000\n" \
"v_cvt_pk_bf16_f32 v66, v68, v69\n" \
"v_cvt_pk_bf16_f32 v67, v70, v71\n" \
"v_cvt_pk_bf16_f32 v74, v76, v77\n" \
"v_cvt_pk_bf16_f32 v75, v78, v79\n" \
"v_xor_b32_e32 v48, 0x8000, v48\n" \
"s_sub_u32 s66, %[n2], 1\n" \
"s_cbranch_scc0 L_2_%=\n" \
"L_f1_%=:\n" \
"L_pf16:\n" \
"s_cbranch_execz L_retA\n" \
"s_nop 0\n" \
"v_bfe_u32 v60, v20, 2, 7\n" \
"v_readlane_b32 s96, v11, 1\n" \
"ds_read_b128 a[160:163], v49 offset:0\n" \
"ds_read_b128 a[164:167], v49 offset:1024\n" \
"ds_read_b128 a[168:171], v49 offset:2048\n" \
"ds_read_b128 a[172:175], v49 offset:3072\n" \
"ds_read_b128 a[176:179], v49 offset:4096\n" \
"buffer_load_dword v57, v60, s[56:59], s96 offen sc1\n" \
"ds_read_b128 a[180:183], v49 offset:5120\n" \
"v_mfma_f32_16x16x32_bf16 v[104:107], a[128:131], a[80:83], v[36:39]\n" \
"s_add_u32 s68, s61, 0x0\n" \
"s_add_u32 s69, s61, 0x49000\n" \
"ds_read_b128 a[184:187], v49 offset:6144\n" \
"v_mfma_f32_16x16x32_bf16 v[104:107], a[132:135], a[84:87], v[104:107]\n" \
"ds_read_b128 a[188:191], v49 offset:7168\n" \
"v_and_b32_e32 v64, v12, v64\n" \
"v_mfma_f32_16x16x32_bf16 v[104:107], a[136:139], a[88:91], v[104:107]\n" \
"v_and_b32_e32 v65, v13, v65\n" \
"v_and_b32_e32 v66, v14, v66\n" \
"v_mfma_f32_16x16x32_bf16 v[104:107], a[140:143], a[92:95], v[104:107]\n" \
"v_and_b32_e32 v67, v15, v67\n" \
"v_mov_b32_e32 v61, s62\n" \
"v_mfma_f32_16x16x32_bf16 v[108:111], a[156:159], a[92:95], v[36:39]\n" \
"v_add_u32_e32 v56, 0x49000, v20\n" \
"ds_read_b128 a[0:3], v20 offset:8192\n" \
"v_mfma_f32_16x16x32_bf16 v[108:111], a[152:155], a[88:91], v[108:111]\n" \
"ds_read_b128 a[4:7], v20 offset:9216\n" \
"ds_read_b128 a[8:11], v20 offset:10240\n" \
"v_mfma_f32_16x16x32_bf16 v[108:111], a[148:151], a[84:87], v[108:111]\n" \
"ds_read_b128 a[12:15], v20 offset:11264\n" \
"ds_read_b128 a[16:19], v20 offset:12288\n" \
"v_mfma_f32_16x16x32_bf16 v[108:111], a[144:147], a[80:83], v[108:111]\n" \
"ds_read_b128 a[20:23], v20 offset:13312\n" \
"ds_read_b128 a[24:27], v20 offset:14336\n" \
"s_waitcnt lgkmcnt(14)\n" \
"v_mfma_f32_16x16x32_bf16 v[128:131], a[160:163], v[64:67], v[128:131]\n" \
"ds_read_b128 a[28:31], v20 offset:15360\n" \
"s_waitcnt lgkmcnt(14)\n" \
"v_mfma_f32_16x16x32_bf16 v[132:135], a[164:167], v[64:67], v[132:135]\n" \
"v_add_u32_e32 v50, %[p4], v48\n" \
"s_waitcnt lgkmcnt(13)\n" \
"v_mfma_f32_16x16x32_bf16 v[136:139], a[168:171], v[64:67], v[136:139]\n" \
"v_xor_b32_e32 v48, 0x4000, v49\n" \
"s_waitcnt lgkmcnt(12)\n" \
"v_mfma_f32_16x16x32_bf16 v[140:143], a[172:175], v[64:67], v[140:143]\n" \
"ds_read_b32 v58, v18 offset:256\n" \
"s_waitcnt lgkmcnt(12)\n" \
"v_mfma_f32_16x16x32_bf16 v[144:147], a[176:179], v[64:67], v[144:147]\n" \
"ds_read_b32 v59, v18 offset:384\n" \
"s_waitcnt lgkmcnt(12)\n" \
"v_mfma_f32_16x16x32_bf16 v[148:151], a[180:183], v[64:67], v[148:151]\n" \
"ds_read_b128 a[32:35], v48 offset:8192\n" \
"s_waitcnt lgkmcnt(12)\n" \
"v_mfma_f32_16x16x32_bf16 v[152:155], a[184:187], v[64:67], v[152:155]\n" \
"ds_read_b128 a[36:39], v48 offset:9216\n" \
"s_waitcnt lgkmcnt(12)\n" \
"v_mfma_f32_16x16x32_bf16 v[156:159], a[188:191], v[64:67], v[156:159]\n" \
"ds_read_b128 a[40:43], v48 offset:10240\n" \
"ds_read_b128 a[44:47], v48 offset:11264\n" \
"s_waitcnt vmcnt(1)\n" \
"s_mov_b64 exec, 1\n" \
"buffer_store_dword v61, off, s[56:59], s95 offset:0 sc1\n" \
"s_mov_b64 exec, -1\n" \
"v_mfma_f32_16x16x32_bf16 a[192:195], a[212:215], v[64:67], a[192:195]\n" \
"ds_read_b128 a[48:51], v48 offset:12288\n" \
"ds_read_b128 a[52:55], v48 offset:13312\n" \
"v_mfma_f32_16x16x32_bf16 v[160:163], a[160:163], v[72:75], v[160:163]\n" \
"ds_read_b128 a[56:59], v48 offset:14336\n" \
"ds_read_b128 a[60:63], v48 offset:15360\n" \
"v_mfma_f32_16x16x32_bf16 v[164:167], a[164:167], v[72:75], v[164:167]\n" \
"ds_read_b128 a[128:131], v49 offset:8192\n" \
"ds_read_b128 a[132:135], v49 offset:9216\n" \
"buffer_load_dwordx4 a[220:223], v20, s[56:59], s60 offen offset:0 sc1\n" \
"v_mfma_f32_16x16x32_bf16 v[168:171], a[168:171], v[72:75], v[168:171]\n" \
"ds_read_b128 a[136:139], v49 offset:10240\n" \
"ds_read_b128 a[140:143], v49 offset:11264\n" \
"v_mfma_f32_16x16x32_bf16 v[172:175], a[172:175], v[72:75], v[172:175]\n" \
"ds_read_b128 a[144:147], v49 offset:12288\n" \
"ds_read_b128 a[148:151], v49 offset:13312\n" \
"v_mfma_f32_16x16x32_bf16 v[176:179], a[176:179], v[72:75], v[176:179]\n" \
"ds_read_b128 a[152:155], v49 offset:14336\n" \
"ds_read_b128 a[156:159], v49 offset:15360\n" \
"v_mfma_f32_16x16x32_bf16 v[180:183], a[180:183], v[72:75], v[180:183]\n" \
"buffer_load_dwordx4 a[236:239], v56, s[56:59], s60 offen offset:0 sc1\n" \
"v_mfma_f32_16x16x32_bf16 v[184:187], a[184:187], v[72:75], v[184:187]\n" \
"v_accvgpr_read_b32 v40, a200\n" \
"v_accvgpr_read_b32 v41, a204\n" \
"v_mfma_f32_16x16x32_bf16 v[188:191], a[188:191], v[72:75], v[188:191]\n" \
"v_add_u32_e32 v51, 0x10000, v50\n" \
"v_add_f32_e32 v40, s94, v40\n" \
"v_mfma_f32_16x16x32_bf16 a[196:199], a[212:215], v[72:75], a[196:199]\n" \
"v_add_f32_e32 v41, s94, v41\n" \
"s_waitcnt lgkmcnt(15)\n" \
"v_mfma_f32_16x16x32_bf16 v[112:115], a[0:3], a[64:67], v[32:35]\n" \
"buffer_load_dwordx4 a[224:227], v20, s[56:59], s60 offen offset:1024 sc1\n" \
"v_mfma_f32_16x16x32_bf16 v[112:115], a[4:7], a[68:71], v[112:115]\n" \
"ds_write_b128 v50, v[192:195] offset:0\n" \
"v_mfma_f32_16x16x32_bf16 v[112:115], a[8:11], a[72:75], v[112:115]\n" \
"ds_write_b128 v50, v[196:199] offset:1024\n" \
"v_mfma_f32_16x16x32_bf16 v[112:115], a[12:15], a[76:79], v[112:115]\n" \
"ds_write_b128 v50, v[200:203] offset:2048\n" \
"v_mfma_f32_16x16x32_bf16 v[120:123], a[12:15], a[92:95], v[36:39]\n" \
"buffer_load_dwordx4 a[240:243], v56, s[56:59], s60 offen offset:1024 sc1\n" \
"v_mfma_f32_16x16x32_bf16 v[120:123], a[8:11], a[88:91], v[120:123]\n" \
"ds_write_b128 v50, v[204:207] offset:3072\n" \
"v_mfma_f32_16x16x32_bf16 v[120:123], a[4:7], a[84:87], v[120:123]\n" \
"ds_write_b128 v50, v[208:211] offset:8192\n" \
"v_mfma_f32_16x16x32_bf16 v[120:123], a[0:3], a[80:83], v[120:123]\n" \
"ds_write_b128 v50, v[212:215] offset:9216\n" \
"v_mfma_f32_16x16x32_bf16 v[116:119], a[16:19], a[64:67], v[32:35]\n" \
"buffer_load_dwordx4 a[228:231], v20, s[56:59], s60 offen offset:2048 sc1\n" \
"v_mfma_f32_16x16x32_bf16 v[116:119], a[20:23], a[68:71], v[116:119]\n" \
"ds_write_b128 v50, v[216:219] offset:10240\n" \
"v_mfma_f32_16x16x32_bf16 v[116:119], a[24:27], a[72:75], v[116:119]\n" \
"ds_write_b128 v50, v[220:223] offset:11264\n" \
"v_mfma_f32_16x16x32_bf16 v[116:119], a[28:31], a[76:79], v[116:119]\n" \
"ds_write_b128 v51, v[224:227] offset:0\n" \
"v_mfma_f32_16x16x32_bf16 v[124:127], a[28:31], a[92:95], v[36:39]\n" \
"buffer_load_dwordx4 a[244:247], v56, s[56:59], s60 offen offset:2048 sc1\n" \
"ds_write_b128 v51, v[228:231] offset:1024\n" \
"ds_write_b128 v51, v[232:235] offset:2048\n" \
"ds_write_b128 v51, v[236:239] offset:3072\n" \
"ds_write_b128 v51, v[240:243] offset:8192\n" \
"ds_write_b128 v51, v[244:247] offset:9216\n" \
"ds_write_b128 v51, v[248:251] offset:10240\n" \
"ds_write_b128 v51, v[252:255] offset:11264\n" \
"buffer_load_dwordx4 a[232:235], v20, s[56:59], s60 offen offset:3072 sc1\n" \
"ds_write_b64 v19, v[40:41]\n" \
"v_mfma_f32_16x16x32_bf16 v[124:127], a[24:27], a[88:91], v[124:127]\n" \
"v_xor_b32_e32 v52, 0x4000, v50\n" \
"v_xor_b32_e32 v53, 0x4000, v51\n" \
"v_mfma_f32_16x16x32_bf16 v[124:127], a[20:23], a[84:87], v[124:127]\n" \
"v_exp_f32_e32 v104, v104\n" \
"v_mfma_f32_16x16x32_bf16 v[124:127], a[16:19], a[80:83], v[124:127]\n" \
"v_exp_f32_e32 v105, v105\n" \
"buffer_load_dwordx4 a[248:251], v56, s[56:59], s60 offen offset:3072 sc1\n" \
"v_exp_f32_e32 v106, v106\n" \
"v_exp_f32_e32 v107, v107\n" \
"v_cvt_pk_bf16_f32 v104, v104, v105\n" \
"v_exp_f32_e32 v108, v108\n" \
"v_exp_f32_e32 v109, v109\n" \
"v_exp_f32_e32 v110, v110\n" \
"v_exp_f32_e32 v111, v111\n" \
"v_cvt_pk_bf16_f32 v105, v106, v107\n" \
"v_cvt_pk_bf16_f32 v106, v108, v109\n" \
"v_cvt_pk_bf16_f32 v107, v110, v111\n" \
"v_and_b32_e32 v104, v12, v104\n" \
"v_and_b32_e32 v105, v13, v105\n" \
"v_and_b32_e32 v106, v14, v106\n" \
"v_and_b32_e32 v107, v15, v107\n" \
"v_readlane_b32 s93, v11, 20\n" \
"v_readlane_b32 s84, v11, 15\n" \
"v_readlane_b32 s85, v11, 16\n" \
"v_readlane_b32 s86, v11, 17\n" \
"s_mov_b32 s87, 0x20000\n" \
"v_readlane_b32 s92, v11, 18\n" \
"s_waitcnt lgkmcnt(0)\n" \
"s_barrier\n" \
"ds_read_b128 v[192:195], v52 offset:0\n" \
"ds_read_b128 v[196:199], v52 offset:1024\n" \
"ds_read_b128 v[200:203], v52 offset:2048\n" \
"ds_read_b128 v[204:207], v52 offset:3072\n" \
"ds_read_b128 v[208:211], v52 offset:8192\n" \
"ds_read_b128 v[212:215], v52 offset:9216\n" \
"L_pf17:\n" \
"s_cbranch_execz L_retA\n" \
"s_nop 0\n" \
"ds_read_b128 v[216:219], v52 offset:10240\n" \
"ds_read_b128 v[220:223], v52 offset:11264\n" \
"ds_read_b128 v[224:227], v53 offset:0\n" \
"ds_read_b128 v[228:231], v53 offset:1024\n" \
"ds_read_b128 v[232:235], v53 offset:2048\n" \
"ds_read_b128 v[236:239], v53 offset:3072\n" \
"ds_read_b128 v[240:243], v53 offset:8192\n" \
"ds_read_b128 v[244:247], v53 offset:9216\n" \
"ds_read_b128 v[248:251], v53 offset:10240\n" \
"ds_read_b128 v[252:255], v53 offset:11264\n" \
"v_mfma_f32_16x16x32_bf16 v[160:163], a[128:131], v[104:107], v[160:163]\n" \
"s_add_u32 m0, s93, 0x0\n" \
"s_waitcnt vmcnt(9)\n" \
"v_cmp_ne_u32_e32 vcc, s62, v57\n" \
"v_mfma_f32_16x16x32_bf16 v[164:167], a[132:135], v[104:107], v[164:167]\n" \
"buffer_load_dwordx4 v0, s[84:87], s92 offen offset:0 lds\n" \
"v_mfma_f32_16x16x32_bf16 v[168:171], a[136:139], v[104:107], v[168:171]\n" \
"v_readlane_b32 s97, v11, 34\n" \
"v_readlane_b32 s98, v11, 35\n" \
"v_mfma_f32_16x16x32_bf16 v[172:175], a[140:143], v[104:107], v[172:175]\n" \
"s_cbranch_vccnz L_so1_%=\n" \
"L_ro1_%=:\n" \
"v_xor_b32_e32 v63, 0x400, v19\n" \
"v_mfma_f32_16x16x32_bf16 v[176:179], a[144:147], v[104:107], v[176:179]\n" \
"ds_read_b64 v[42:43], v63\n" \
"v_mfma_f32_16x16x32_bf16 v[180:183], a[148:151], v[104:107], v[180:183]\n" \
"buffer_load_dwordx4 v1, s[84:87], s92 offen offset:1024 lds\n" \
"v_mfma_f32_16x16x32_bf16 v[184:187], a[152:155], v[104:107], v[184:187]\n" \
"v_exp_f32_e32 v112, v112\n" \
"v_mfma_f32_16x16x32_bf16 v[188:191], a[156:159], v[104:107], v[188:191]\n" \
"v_exp_f32_e32 v113, v113\n" \
"v_mfma_f32_16x16x32_bf16 a[196:199], a[212:215], v[104:107], a[196:199]\n" \
"v_exp_f32_e32 v114, v114\n" \
"v_exp_f32_e32 v115, v115\n" \
"buffer_load_dwordx4 v2, s[84:87], s92 offen offset:2048 lds\n" \
"s_waitcnt lgkmcnt(1)\n" \
"s_barrier\n" \
"v_cvt_pk_bf16_f32 v112, v112, v113\n" \
"v_exp_f32_e32 v116, v116\n" \
"v_exp_f32_e32 v117, v117\n" \
"v_exp_f32_e32 v118, v118\n" \
"v_exp_f32_e32 v119, v119\n" \
"buffer_load_dwordx4 v3, s[84:87], s92 offen offset:3072 lds\n" \
"s_add_u32 m0, s93, 0x1000\n" \
"v_cvt_pk_bf16_f32 v113, v114, v115\n" \
"v_cvt_pk_bf16_f32 v114, v116, v117\n" \
"v_cvt_pk_bf16_f32 v115, v118, v119\n" \
"v_exp_f32_e32 v120, v120\n" \
"v_exp_f32_e32 v121, v121\n" \
"v_mfma_f32_16x16x32_bf16 v[128:131], a[32:35], v[112:115], v[128:131]\n" \
"v_exp_f32_e32 v122, v122\n" \
"v_mfma_f32_16x16x32_bf16 v[132:135], a[36:39], v[112:115], v[132:135]\n" \
"buffer_load_dwordx4 v4, s[84:87], s92 offen offset:0 lds\n" \
"v_mfma_f32_16x16x32_bf16 v[136:139], a[40:43], v[112:115], v[136:139]\n" \
"v_exp_f32_e32 v123, v123\n" \
"v_mfma_f32_16x16x32_bf16 v[140:143], a[44:47], v[112:115], v[140:143]\n" \
"v_cvt_pk_bf16_f32 v120, v120, v121\n" \
"v_cvt_pk_bf16_f32 v121, v122, v123\n" \
"v_mfma_f32_16x16x32_bf16 v[144:147], a[48:51], v[112:115], v[144:147]\n" \
"v_exp_f32_e32 v124, v124\n" \
"v_mfma_f32_16x16x32_bf16 v[148:151], a[52:55], v[112:115], v[148:151]\n" \
"buffer_load_dwordx4 v5, s[84:87], s92 offen offset:1024 lds\n" \
"v_mfma_f32_16x16x32_bf16 v[152:155], a[56:59], v[112:115], v[152:155]\n" \
"v_exp_f32_e32 v125, v125\n" \
"v_mfma_f32_16x16x32_bf16 v[156:159], a[60:63], v[112:115], v[156:159]\n" \
"v_exp_f32_e32 v126, v126\n" \
"v_mfma_f32_16x16x32_bf16 a[192:195], a[212:215], v[112:115], a[192:195]\n" \
"v_exp_f32_e32 v127, v127\n" \
"v_cvt_pk_bf16_f32 v122, v124, v125\n" \
"buffer_load_dwordx4 v6, s[84:87], s92 offen offset:2048 lds\n" \
"v_cvt_pk_bf16_f32 v123, v126, v127\n" \
"v_add_f32_e32 v160, v160, v224\n" \
"v_add_f32_e32 v161, v161, v225\n" \
"v_add_f32_e32 v162, v162, v226\n" \
"v_add_f32_e32 v163, v163, v227\n" \
"v_add_f32_e32 v164, v164, v228\n" \
"v_add_f32_e32 v165, v165, v229\n" \
"v_mfma_f32_16x16x32_bf16 v[160:163], a[32:35], v[120:123], v[160:163]\n" \
"v_add_f32_e32 v166, v166, v230\n" \
"v_add_f32_e32 v167, v167, v231\n" \
"v_add_f32_e32 v168, v168, v232\n" \
"v_add_f32_e32 v169, v169, v233\n" \
"buffer_load_dwordx4 v7, s[84:87], s92 offen offset:3072 lds\n" \
"s_add_u32 m0, s93, 0x2000\n" \
"v_mfma_f32_16x16x32_bf16 v[164:167], a[36:39], v[120:123], v[164:167]\n" \
"v_add_f32_e32 v170, v170, v234\n" \
"v_add_f32_e32 v171, v171, v235\n" \
"v_add_f32_e32 v172, v172, v236\n" \
"v_add_f32_e32 v173, v173, v237\n" \
"v_mfma_f32_16x16x32_bf16 v[168:171], a[40:43], v[120:123], v[168:171]\n" \
"v_add_f32_e32 v174, v174, v238\n" \
"v_add_f32_e32 v175, v175, v239\n" \
"v_accvgpr_read_b32 v54, a192\n" \
"s_waitcnt lgkmcnt(0)\n" \
"v_add_f32_e32 v54, v54, v42\n" \
"buffer_load_dwordx4 v8, s[84:87], s92 offen offset:0 lds\n" \
"v_mfma_f32_16x16x32_bf16 v[172:175], a[44:47], v[120:123], v[172:175]\n" \
"v_max_f32_e32 v62, v62, v54\n" \
"v_add_f32_e32 v176, v176, v240\n" \
"v_add_f32_e32 v177, v177, v241\n" \
"v_add_f32_e32 v178, v178, v242\n" \
"v_add_f32_e32 v179, v179, v243\n" \
"v_rcp_f32_e32 v54, v54\n" \
"v_add_f32_e32 v128, v128, v192\n" \
"v_mfma_f32_16x16x32_bf16 v[176:179], a[48:51], v[120:123], v[176:179]\n" \
"v_add_f32_e32 v129, v129, v193\n" \
"buffer_load_dwordx4 a[96:99], v20, s[56:59], s97 offen offset:0 sc1\n" \
"v_add_f32_e32 v130, v130, v194\n" \
"v_add_f32_e32 v131, v131, v195\n" \
"v_add_f32_e32 v132, v132, v196\n" \
"v_add_f32_e32 v133, v133, v197\n" \
"v_add_f32_e32 v134, v134, v198\n" \
"v_add_f32_e32 v135, v135, v199\n" \
"v_mul_f32_e32 v54, v54, v58\n" \
"v_mul_f32_e32 v128, v54, v128\n" \
"v_mul_f32_e32 v129, v54, v129\n" \
"v_mul_f32_e32 v130, v54, v130\n" \
"v_mul_f32_e32 v131, v54, v131\n" \
"v_mul_f32_e32 v132, v54, v132\n" \
"v_mul_f32_e32 v133, v54, v133\n" \
"buffer_load_dwordx4 a[104:107], v56, s[56:59], s97 offen offset:0 sc1\n" \
"v_mul_f32_e32 v134, v54, v134\n" \
"v_mul_f32_e32 v135, v54, v135\n" \
"v_add_f32_e32 v180, v180, v244\n" \
"v_add_f32_e32 v181, v181, v245\n" \
"v_add_f32_e32 v182, v182, v246\n" \
"v_add_f32_e32 v183, v183, v247\n" \
"v_add_f32_e32 v136, v136, v200\n" \
"v_add_f32_e32 v137, v137, v201\n" \
"v_add_f32_e32 v138, v138, v202\n" \
"v_add_f32_e32 v139, v139, v203\n" \
"v_add_f32_e32 v140, v140, v204\n" \
"v_add_f32_e32 v141, v141, v205\n" \
"v_add_f32_e32 v142, v142, v206\n" \
"buffer_load_dwordx4 a[112:115], v20, s[56:59], s98 offen offset:0 sc1\n" \
"v_add_f32_e32 v143, v143, v207\n" \
"v_cvt_pk_bf16_f32 v64, v128, v129\n" \
"v_cvt_pk_bf16_f32 v65, v130, v131\n" \
"v_cvt_pk_bf16_f32 v66, v132, v133\n" \
"v_cvt_pk_bf16_f32 v67, v134, v135\n" \
"v_mfma_f32_16x16x32_bf16 v[180:183], a[52:55], v[120:123], v[180:183]\n" \
"v_mul_f32_e32 v136, v54, v136\n" \
"v_mfma_f32_16x16x32_bf16 a[208:211], a[216:219], v[64:67], a[208:211]\n" \
"v_mul_f32_e32 v137, v54, v137\n" \
"v_mul_f32_e32 v138, v54, v138\n" \
"buffer_store_dwordx4 v[64:67], v20, s[56:59], s68 offen offset:0 sc1\n" \
"v_mul_f32_e32 v139, v54, v139\n" \
"v_mul_f32_e32 v140, v54, v140\n" \
"v_mul_f32_e32 v141, v54, v141\n" \
"v_mul_f32_e32 v142, v54, v142\n" \
"v_mul_f32_e32 v143, v54, v143\n" \
"v_add_f32_e32 v144, v144, v208\n" \
"v_add_f32_e32 v145, v145, v209\n" \
"v_add_f32_e32 v146, v146, v210\n" \
"v_add_f32_e32 v147, v147, v211\n" \
"v_add_f32_e32 v148, v148, v212\n" \
"v_add_f32_e32 v149, v149, v213\n" \
"v_add_f32_e32 v150, v150, v214\n" \
"v_add_f32_e32 v151, v151, v215\n" \
"buffer_load_dwordx4 a[120:123], v56, s[56:59], s98 offen offset:0 sc1\n" \
"v_cvt_pk_bf16_f32 v68, v136, v137\n" \
"v_cvt_pk_bf16_f32 v69, v138, v139\n" \
"v_cvt_pk_bf16_f32 v70, v140, v141\n" \
"v_cvt_pk_bf16_f32 v71, v142, v143\n" \
"v_mul_f32_e32 v144, v54, v144\n" \
"v_mul_f32_e32 v145, v54, v145\n" \
"v_mfma_f32_16x16x32_bf16 a[208:211], a[216:219], v[68:71], a[208:211]\n" \
"v_mul_f32_e32 v146, v54, v146\n" \
"v_mul_f32_e32 v147, v54, v147\n" \
"v_mul_f32_e32 v148, v54, v148\n" \
"v_mul_f32_e32 v149, v54, v149\n" \
"v_mul_f32_e32 v150, v54, v150\n" \
"buffer_store_dwordx4 v[68:71], v20, s[56:59], s68 offen offset:1024 sc1\n" \
"v_mul_f32_e32 v151, v54, v151\n" \
"v_add_f32_e32 v184, v184, v248\n" \
"v_add_f32_e32 v185, v185, v249\n" \
"v_add_f32_e32 v186, v186, v250\n" \
"v_add_f32_e32 v187, v187, v251\n" \
"v_add_f32_e32 v152, v152, v216\n" \
"v_add_f32_e32 v153, v153, v217\n" \
"v_add_f32_e32 v154, v154, v218\n" \
"v_add_f32_e32 v155, v155, v219\n" \
"v_add_f32_e32 v156, v156, v220\n" \
"v_add_f32_e32 v157, v157, v221\n" \
"v_add_f32_e32 v158, v158, v222\n" \
"v_add_f32_e32 v159, v159, v223\n" \
"buffer_load_dwordx4 a[100:103], v20, s[56:59], s97 offen offset:1024 sc1\n" \
"v_cvt_pk_bf16_f32 v72, v144, v145\n" \
"v_cvt_pk_bf16_f32 v73, v146, v147\n" \
"v_cvt_pk_bf16_f32 v74, v148, v149\n" \
"v_cvt_pk_bf16_f32 v75, v150, v151\n" \
"v_mfma_f32_16x16x32_bf16 v[184:187], a[56:59], v[120:123], v[184:187]\n" \
"v_mul_f32_e32 v152, v54, v152\n" \
"v_mfma_f32_16x16x32_bf16 a[208:211], a[216:219], v[72:75], a[208:211]\n" \
"v_mul_f32_e32 v153, v54, v153\n" \
"v_mul_f32_e32 v154, v54, v154\n" \
"v_mul_f32_e32 v155, v54, v155\n" \
"buffer_store_dwordx4 v[72:75], v20, s[56:59], s68 offen offset:2048 sc1\n" \
"v_mul_f32_e32 v156, v54, v156\n" \
"v_mul_f32_e32 v157, v54, v157\n" \
"v_mul_f32_e32 v158, v54, v158\n" \
"v_mul_f32_e32 v159, v54, v159\n" \
"v_cvt_pk_bf16_f32 v76, v152, v153\n" \
"v_cvt_pk_bf16_f32 v77, v154, v155\n" \
"v_cvt_pk_bf16_f32 v78, v156, v157\n" \
"v_cvt_pk_bf16_f32 v79, v158, v159\n" \
"v_add_f32_e32 v188, v188, v252\n" \
"v_add_f32_e32 v189, v189, v253\n" \
"v_mfma_f32_16x16x32_bf16 a[208:211], a[216:219], v[76:79], a[208:211]\n" \
"v_add_f32_e32 v190, v190, v254\n" \
"buffer_store_dwordx4 v[76:79], v20, s[56:59], s68 offen offset:3072 sc1\n" \
"v_add_f32_e32 v191, v191, v255\n" \
"s_nop 1\n" \
"v_mfma_f32_16x16x32_bf16 v[188:191], a[60:63], v[120:123], v[188:191]\n" \
"v_mfma_f32_16x16x32_bf16 a[196:199], a[212:215], v[120:123], a[196:199]\n" \
"buffer_load_dwordx4 a[108:111], v56, s[56:59], s97 offen offset:1024 sc1\n" \
"buffer_load_dwordx4 a[116:119], v20, s[56:59], s98 offen offset:1024 sc1\n" \
"buffer_load_dwordx4 a[124:127], v56, s[56:59], s98 offen offset:1024 sc1\n" \
"s_nop 4\n" \
"v_accvgpr_read_b32 v55, a196\n" \
"v_add_f32_e32 v55, v55, v43\n" \
"v_max_f32_e32 v62, v62, v55\n" \
"v_rcp_f32_e32 v55, v55\n" \
"v_cmp_lt_f32_e64 s[72:73], %[lim], v62\n" \
"v_mul_f32_e32 v55, v55, v59\n" \
"v_mul_f32_e32 v160, v55, v160\n" \
"v_mul_f32_e32 v161, v55, v161\n" \
"v_mul_f32_e32 v162, v55, v162\n" \
"v_mul_f32_e32 v163, v55, v163\n" \
"v_mul_f32_e32 v164, v55, v164\n" \
"v_mul_f32_e32 v165, v55, v165\n" \
"v_mul_f32_e32 v166, v55, v166\n" \
"v_mul_f32_e32 v167, v55, v167\n" \
"v_cvt_pk_bf16_f32 v80, v160, v161\n" \
"v_cvt_pk_bf16_f32 v81, v162, v163\n" \
"v_cvt_pk_bf16_f32 v82, v164, v165\n" \
"v_cvt_pk_bf16_f32 v83, v166, v167\n" \
"v_mul_f32_e32 v168, v55, v168\n" \
"v_mul_f32_e32 v169, v55, v169\n" \
"v_mfma_f32_16x16x32_bf16 a[208:211], a[216:219], v[80:83], a[208:211]\n" \
"v_mul_f32_e32 v170, v55, v170\n" \
"v_mul_f32_e32 v171, v55, v171\n" \
"v_mul_f32_e32 v172, v55, v172\n" \
"v_mul_f32_e32 v173, v55, v173\n" \
"v_mul_f32_e32 v174, v55, v174\n" \
"v_mul_f32_e32 v175, v55, v175\n" \
"v_cvt_pk_bf16_f32 v84, v168, v169\n" \
"v_cvt_pk_bf16_f32 v85, v170, v171\n" \
"v_cvt_pk_bf16_f32 v86, v172, v173\n" \
"v_cvt_pk_bf16_f32 v87, v174, v175\n" \
"v_mul_f32_e32 v176, v55, v176\n" \
"v_mul_f32_e32 v177, v55, v177\n" \
"v_mfma_f32_16x16x32_bf16 a[208:211], a[216:219], v[84:87], a[208:211]\n" \
"v_mul_f32_e32 v178, v55, v178\n" \
"buffer_store_dwordx4 v[80:83], v20, s[56:59], s69 offen offset:0 sc1\n" \
"v_mul_f32_e32 v179, v55, v179\n" \
"v_mul_f32_e32 v180, v55, v180\n" \
"v_mul_f32_e32 v181, v55, v181\n" \
"v_mul_f32_e32 v182, v55, v182\n" \
"v_mul_f32_e32 v183, v55, v183\n" \
"v_cvt_pk_bf16_f32 v96, v176, v177\n" \
"v_cvt_pk_bf16_f32 v97, v178, v179\n" \
"v_cvt_pk_bf16_f32 v98, v180, v181\n" \
"v_cvt_pk_bf16_f32 v99, v182, v183\n" \
"v_mul_f32_e32 v184, v55, v184\n" \
"v_mul_f32_e32 v185, v55, v185\n" \
"v_mfma_f32_16x16x32_bf16 a[208:211], a[216:219], v[96:99], a[208:211]\n" \
"buffer_store_dwordx4 v[84:87], v20, s[56:59], s69 offen offset:1024 sc1\n" \
"v_mul_f32_e32 v186, v55, v186\n" \
"v_mul_f32_e32 v187, v55, v187\n" \
"v_mul_f32_e32 v188, v55, v188\n" \
"v_mul_f32_e32 v189, v55, v189\n" \
"v_mul_f32_e32 v190, v55, v190\n" \
"v_mul_f32_e32 v191, v55, v191\n" \
"v_cvt_pk_bf16_f32 v100, v184, v185\n" \
"v_cvt_pk_bf16_f32 v101, v186, v187\n" \
"v_cvt_pk_bf16_f32 v102, v188, v189\n" \
"v_cvt_pk_bf16_f32 v103, v190, v191\n" \
"buffer_store_dwordx4 v[96:99], v20, s[56:59], s69 offen offset:2048 sc1\n" \
"buffer_store_dwordx4 v[100:103], v20, s[56:59], s69 offen offset:3072 sc1\n" \
"v_mfma_f32_16x16x32_bf16 a[208:211], a[216:219], v[100:103], a[208:211]\n" \
"s_nop 7\n" \
"v_accvgpr_read_b32 v63, a208\n" \
"v_cmp_u_f32_e32 vcc, v63, v63\n" \
"s_nop 0\n" \
"s_or_b64 vcc, s[72:73], vcc\n" \
"s_cbranch_scc1 L_end_%=\n" \
"L_out_%=:\n" \
"L_pf18:\n" \
"s_cbranch_execz L_retA\n" \
"s_nop 0\n" \
"v_add_u32_e32 v34, 0x20000, v10\n" \
"ds_read_b64_tr_b16 a[0:1], v34 offset:0\n" \
"ds_read_b64_tr_b16 a[2:3], v34 offset:4608\n" \
"ds_read_b64_tr_b16 a[4:5], v34 offset:32\n" \
"ds_read_b64_tr_b16 a[6:7], v34 offset:4640\n" \
"ds_read_b64_tr_b16 a[8:9], v34 offset:64\n" \
"ds_read_b64_tr_b16 a[10:11], v34 offset:4672\n" \
"ds_read_b64_tr_b16 a[12:13], v34 offset:96\n" \
"ds_read_b64_tr_b16 a[14:15], v34 offset:4704\n" \
"v_mov_b32_e32 v39, s62\n" \
"v_readlane_b32 s70, v11, 5\n" \
"v_bfe_u32 v38, v20, 2, 4\n" \
"v_readlane_b32 s64, v11, 2\n" \
"v_readlane_b32 s71, v11, 4\n" \
"ds_read_b64_tr_b16 a[16:17], v34 offset:128\n" \
"ds_read_b64_tr_b16 a[18:19], v34 offset:4736\n" \
"ds_read_b64_tr_b16 a[20:21], v34 offset:160\n" \
"ds_read_b64_tr_b16 a[22:23], v34 offset:4768\n" \
"buffer_load_dword v37, v38, s[56:59], s64 offen sc1\n" \
"ds_read_b64_tr_b16 a[24:25], v34 offset:192\n" \
"ds_read_b64_tr_b16 a[26:27], v34 offset:4800\n" \
"ds_read_b64_tr_b16 a[28:29], v34 offset:224\n" \
"ds_read_b64_tr_b16 a[30:31], v34 offset:4832\n" \
"ds_read_b64_tr_b16 a[192:193], v34 offset:256\n" \
"ds_read_b64_tr_b16 a[194:195], v34 offset:4864\n" \
"ds_read_b64_tr_b16 a[196:197], v34 offset:9216\n" \
"ds_read_b64_tr_b16 a[198:199], v34 offset:13824\n" \
"ds_read_b64_tr_b16 a[200:201], v34 offset:9248\n" \
"ds_read_b64_tr_b16 a[202:203], v34 offset:13856\n" \
"ds_read_b64_tr_b16 a[204:205], v34 offset:9280\n" \
"ds_read_b64_tr_b16 a[206:207], v34 offset:13888\n" \
"ds_read_b64_tr_b16 a[208:209], v34 offset:9312\n" \
"ds_read_b64_tr_b16 a[210:211], v34 offset:13920\n" \
"ds_read_b64_tr_b16 a[212:213], v34 offset:9344\n" \
"ds_read_b64_tr_b16 a[214:215], v34 offset:13952\n" \
"ds_read_b64_tr_b16 a[216:217], v34 offset:9376\n" \
"ds_read_b64_tr_b16 a[218:219], v34 offset:13984\n" \
"v_add_u32_e32 v33, 0xfc00, v10\n" \
"v_readlane_b32 s99, v11, 21\n" \
"v_readlane_b32 s80, v11, 11\n" \
"v_readlane_b32 s81, v11, 12\n" \
"s_mov_b32 s82, 0x47fca0\n" \
"s_waitcnt vmcnt(26) lgkmcnt(15)\n" \
"v_mfma_f32_16x16x32_bf16 v[112:115], a[0:3], a[220:223], 0\n" \
"s_mov_b32 s83, 0x20000\n" \
"v_readlane_b32 s65, v11, 25\n" \
"v_mfma_f32_16x16x32_bf16 v[116:119], a[0:3], a[236:239], 0\n" \
"s_add_u32 m0, s99, 0x6c00\n" \
"v_readlane_b32 s66, v11, 28\n" \
"v_mfma_f32_16x16x32_bf16 v[184:187], a[0:3], v[64:67], 0\n" \
"v_add_u32_e32 v32, 0x49000, v20\n" \
"v_lshrrev_b32_e32 v44, 9, v20\n" \
"v_mfma_f32_16x16x32_bf16 v[188:191], a[0:3], v[80:83], 0\n" \
"ds_read_b64_tr_b16 a[0:1], v34 offset:9408\n" \
"ds_read_b64_tr_b16 a[2:3], v34 offset:14016\n" \
"v_mfma_f32_16x16x32_bf16 v[120:123], a[4:7], a[220:223], 0\n" \
"buffer_load_dwordx4 v0, s[80:83], s65 offen offset:0 lds\n" \
"v_mfma_f32_16x16x32_bf16 v[124:127], a[4:7], a[236:239], 0\n" \
"v_mul_u32_u24_e32 v44, 0xb40, v44\n" \
"v_add_u32_e32 v35, v44, v10\n" \
"v_mfma_f32_16x16x32_bf16 v[192:195], a[4:7], v[64:67], 0\n" \
"v_readlane_b32 s67, v11, 26\n" \
"v_readlane_b32 s92, v11, 22\n" \
"v_mfma_f32_16x16x32_bf16 v[196:199], a[4:7], v[80:83], 0\n" \
"ds_read_b64_tr_b16 a[4:5], v34 offset:9440\n" \
"ds_read_b64_tr_b16 a[6:7], v34 offset:14048\n" \
"v_mfma_f32_16x16x32_bf16 v[128:131], a[8:11], a[220:223], 0\n" \
"v_readlane_b32 s73, v11, 27\n" \
"v_readlane_b32 s94, v11, 23\n" \
"v_mfma_f32_16x16x32_bf16 v[132:135], a[8:11], a[236:239], 0\n" \
"buffer_load_dwordx4 v1, s[80:83], s65 offen offset:1024 lds\n" \
"v_mfma_f32_16x16x32_bf16 v[200:203], a[8:11], v[64:67], 0\n" \
"v_readlane_b32 s84, v11, 9\n" \
"v_readlane_b32 s85, v11, 10\n" \
"v_mfma_f32_16x16x32_bf16 v[204:207], a[8:11], v[80:83], 0\n" \
"ds_read_b64_tr_b16 a[8:9], v34 offset:9472\n" \
"ds_read_b64_tr_b16 a[10:11], v34 offset:14080\n" \
"v_mfma_f32_16x16x32_bf16 v[136:139], a[12:15], a[220:223], 0\n" \
"s_mov_b32 s86, 0xa1ca0\n" \
"s_mov_b32 s87, 0x20000\n" \
"v_mfma_f32_16x16x32_bf16 v[140:143], a[12:15], a[236:239], 0\n" \
"v_readlane_b32 s68, v11, 29\n" \
"v_readlane_b32 s69, v11, 30\n" \
"v_mfma_f32_16x16x32_bf16 v[208:211], a[12:15], v[64:67], 0\n" \
"buffer_load_dwordx4 v2, s[80:83], s65 offen offset:2048 lds\n" \
"v_mfma_f32_16x16x32_bf16 v[212:215], a[12:15], v[80:83], 0\n" \
"ds_read_b64_tr_b16 a[12:13], v34 offset:18432\n" \
"ds_read_b64_tr_b16 a[14:15], v34 offset:23040\n" \
"v_mfma_f32_16x16x32_bf16 v[144:147], a[16:19], a[220:223], 0\n" \
"v_readlane_b32 s72, v11, 32\n" \
"v_readlane_b32 s93, v11, 33\n" \
"v_mfma_f32_16x16x32_bf16 v[148:151], a[16:19], a[236:239], 0\n" \
"v_lshlrev_b32_e32 v41, 12, v20\n" \
"v_readlane_b32 s63, v11, 39\n" \
"v_mfma_f32_16x16x32_bf16 v[216:219], a[16:19], v[64:67], 0\n" \
"v_bfe_u32 v40, v20, 2, 7\n" \
"s_add_u32 s74, s63, 0x3800\n" \
"v_mfma_f32_16x16x32_bf16 v[220:223], a[16:19], v[80:83], 0\n" \
"ds_read_b64_tr_b16 a[16:17], v34 offset:18464\n" \
"ds_read_b64_tr_b16 a[18:19], v34 offset:23072\n" \
"v_mfma_f32_16x16x32_bf16 v[152:155], a[20:23], a[220:223], 0\n" \
"buffer_load_dwordx4 v3, s[80:83], s65 offen offset:3072 lds\n" \
"v_mfma_f32_16x16x32_bf16 v[156:159], a[20:23], a[236:239], 0\n" \
"s_add_u32 m0, s99, 0x7c00\n" \
"v_readlane_b32 s95, v11, 36\n" \
"v_mfma_f32_16x16x32_bf16 v[224:227], a[20:23], v[64:67], 0\n" \
"v_readlane_b32 s96, v11, 37\n" \
"v_readlane_b32 s97, v11, 38\n" \
"v_mfma_f32_16x16x32_bf16 v[228:231], a[20:23], v[80:83], 0\n" \
"ds_read_b64_tr_b16 a[20:21], v34 offset:18496\n" \
"ds_read_b64_tr_b16 a[22:23], v34 offset:23104\n" \
"v_mfma_f32_16x16x32_bf16 v[160:163], a[24:27], a[220:223], 0\n" \
"v_readlane_b32 s100, v11, 7\n" \
"v_bfe_u32 v44, v20, 4, 4\n" \
"v_mfma_f32_16x16x32_bf16 v[164:167], a[24:27], a[236:239], 0\n" \
"buffer_load_dwordx4 v4, s[80:83], s65 offen offset:0 lds\n" \
"v_mfma_f32_16x16x32_bf16 v[232:235], a[24:27], v[64:67], 0\n" \
"v_bfe_u32 v45, v20, 8, 2\n" \
"v_mul_u32_u24_e32 v42, 0x480, v44\n" \
"v_mfma_f32_16x16x32_bf16 v[236:239], a[24:27], v[80:83], 0\n" \
"ds_read_b64_tr_b16 a[24:25], v34 offset:18528\n" \
"s_waitcnt lgkmcnt(7)\n" \
"v_mfma_f32_16x16x32_bf16 v[168:171], a[28:31], a[220:223], 0\n" \
"ds_read_b64_tr_b16 a[26:27], v34 offset:23136\n" \
"v_lshl_add_u32 v42, v45, 3, v42\n" \
"v_mfma_f32_16x16x32_bf16 v[172:175], a[28:31], a[236:239], 0\n" \
"v_lshrrev_b32_e32 v44, 1, v44\n" \
"v_lshlrev_b32_e32 v45, 1, v45\n" \
"v_mfma_f32_16x16x32_bf16 v[240:243], a[28:31], v[64:67], 0\n" \
"buffer_load_dwordx4 v5, s[80:83], s65 offen offset:1024 lds\n" \
"v_mfma_f32_16x16x32_bf16 v[244:247], a[28:31], v[80:83], 0\n" \
"ds_read_b64_tr_b16 a[28:29], v34 offset:18560\n" \
"ds_read_b64_tr_b16 a[30:31], v34 offset:23168\n" \
"v_mfma_f32_16x16x32_bf16 v[176:179], a[192:195], a[220:223], 0\n" \
"v_sub_u32_e32 v44, v44, v45\n" \
"v_and_b32_e32 v45, 16, v20\n" \
"v_mfma_f32_16x16x32_bf16 v[180:183], a[192:195], a[236:239], 0\n" \
"v_mov_b32_e32 v46, 0x3f80\n" \
"v_lshlrev_b32_e32 v45, v45, v46\n" \
"v_mfma_f32_16x16x32_bf16 v[248:251], a[192:195], v[64:67], 0\n" \
"v_mov_b32_e32 v52, 0\n" \
"v_mov_b32_e32 v53, 0\n" \
"v_mfma_f32_16x16x32_bf16 v[252:255], a[192:195], v[80:83], 0\n" \
"ds_read_b64_tr_b16 a[192:193], v34 offset:18592\n" \
"ds_read_b64_tr_b16 a[194:195], v34 offset:23200\n" \
"v_mfma_f32_16x16x32_bf16 v[112:115], a[196:199], a[224:227], v[112:115]\n" \
"buffer_load_dwordx4 v6, s[80:83], s65 offen offset:2048 lds\n" \
"v_mfma_f32_16x16x32_bf16 v[116:119], a[196:199], a[240:243], v[116:119]\n" \
"v_mov_b32_e32 v48, 0\n" \
"v_mov_b32_e32 v49, 0\n" \
"v_mfma_f32_16x16x32_bf16 v[184:187], a[196:199], v[68:71], v[184:187]\n" \
"v_add_u32_e32 v43, 0x9000, v42\n" \
"v_readlane_b32 s88, v11, 13\n" \
"v_mfma_f32_16x16x32_bf16 v[188:191], a[196:199], v[84:87], v[188:191]\n" \
"ds_read_b64_tr_b16 a[196:197], v34 offset:18624\n" \
"ds_read_b64_tr_b16 a[198:199], v34 offset:23232\n" \
"v_mfma_f32_16x16x32_bf16 v[120:123], a[200:203], a[224:227], v[120:123]\n" \
"v_readlane_b32 s89, v11, 14\n" \
"s_mov_b32 s90, 0x240000\n" \
"v_mfma_f32_16x16x32_bf16 v[124:127], a[200:203], a[240:243], v[124:127]\n" \
"buffer_load_dwordx4 v7, s[80:83], s65 offen offset:3072 lds\n" \
"v_mfma_f32_16x16x32_bf16 v[192:195], a[200:203], v[68:71], v[192:195]\n" \
"s_add_u32 m0, s99, 0x8c00\n" \
"s_mov_b32 s91, 0x20000\n" \
"v_mfma_f32_16x16x32_bf16 v[196:199], a[200:203], v[84:87], v[196:199]\n" \
"ds_read_b64_tr_b16 a[200:201], v34 offset:18656\n" \
"ds_read_b64_tr_b16 a[202:203], v34 offset:23264\n" \
"v_mfma_f32_16x16x32_bf16 v[128:131], a[204:207], a[224:227], v[128:131]\n" \
"v_mov_b32_e32 v46, s62\n" \
"v_mfma_f32_16x16x32_bf16 v[132:135], a[204:207], a[240:243], v[132:135]\n" \
"s_waitcnt vmcnt(8)\n" \
"v_cmp_ne_u32_e32 vcc, s62, v37\n" \
"s_mov_b64 exec, 1\n" \
"buffer_store_dword v39, off, s[56:59], s70 offset:0 sc1\n" \
"s_mov_b64 exec, -1\n" \
"v_mfma_f32_16x16x32_bf16 v[200:203], a[204:207], v[68:71], v[200:203]\n" \
"v_readlane_b32 s70, v11, 47\n" \
"s_cbranch_vccnz L_sa_%=\n" \
"L_ra_%=:\n" \
"v_mfma_f32_16x16x32_bf16 v[204:207], a[204:207], v[84:87], v[204:207]\n" \
"ds_read_b64_tr_b16 a[204:205], v34 offset:18688\n" \
"ds_read_b64_tr_b16 a[206:207], v34 offset:23296\n" \
"v_mfma_f32_16x16x32_bf16 v[136:139], a[208:211], a[224:227], v[136:139]\n" \
"v_readlane_b32 s64, v11, 31\n" \
"v_mfma_f32_16x16x32_bf16 v[140:143], a[208:211], a[240:243], v[140:143]\n" \
"buffer_load_dwordx4 v8, s[80:83], s65 offen offset:0 lds\n" \
"v_mfma_f32_16x16x32_bf16 v[208:211], a[208:211], v[68:71], v[208:211]\n" \
"s_add_u32 m0, s99, 0x14400\n" \
"v_readlane_b32 s65, v11, 44\n" \
"v_mfma_f32_16x16x32_bf16 v[212:215], a[208:211], v[84:87], v[212:215]\n" \
"v_mfma_f32_16x16x32_bf16 v[144:147], a[212:215], a[224:227], v[144:147]\n" \
"v_mfma_f32_16x16x32_bf16 v[148:151], a[212:215], a[240:243], v[148:151]\n" \
"v_mfma_f32_16x16x32_bf16 v[216:219], a[212:215], v[68:71], v[216:219]\n" \
"buffer_load_dwordx4 a[64:67], v20, s[56:59], s66 offen offset:0 sc1\n" \
"v_mfma_f32_16x16x32_bf16 v[220:223], a[212:215], v[84:87], v[220:223]\n" \
"v_mfma_f32_16x16x32_bf16 v[152:155], a[216:219], a[224:227], v[152:155]\n" \
"v_mfma_f32_16x16x32_bf16 v[156:159], a[216:219], a[240:243], v[156:159]\n" \
"v_mfma_f32_16x16x32_bf16 v[224:227], a[216:219], v[68:71], v[224:227]\n" \
"v_mfma_f32_16x16x32_bf16 v[228:231], a[216:219], v[84:87], v[228:231]\n" \
"buffer_load_dwordx4 a[80:83], v32, s[56:59], s66 offen offset:0 sc1\n" \
"v_mfma_f32_16x16x32_bf16 v[160:163], a[0:3], a[224:227], v[160:163]\n" \
"v_mfma_f32_16x16x32_bf16 v[164:167], a[0:3], a[240:243], v[164:167]\n" \
"v_mfma_f32_16x16x32_bf16 v[232:235], a[0:3], v[68:71], v[232:235]\n" \
"v_mfma_f32_16x16x32_bf16 v[236:239], a[0:3], v[84:87], v[236:239]\n" \
"v_mfma_f32_16x16x32_bf16 v[168:171], a[4:7], a[224:227], v[168:171]\n" \
"buffer_load_dwordx4 a[68:71], v20, s[56:59], s66 offen offset:1024 sc1\n" \
"v_mfma_f32_16x16x32_bf16 v[172:175], a[4:7], a[240:243], v[172:175]\n" \
"v_mfma_f32_16x16x32_bf16 v[240:243], a[4:7], v[68:71], v[240:243]\n" \
"v_mfma_f32_16x16x32_bf16 v[244:247], a[4:7], v[84:87], v[244:247]\n" \
"v_mfma_f32_16x16x32_bf16 v[176:179], a[8:11], a[224:227], v[176:179]\n" \
"v_mfma_f32_16x16x32_bf16 v[180:183], a[8:11], a[240:243], v[180:183]\n" \
"buffer_load_dwordx4 a[84:87], v32, s[56:59], s66 offen offset:1024 sc1\n" \
"v_mfma_f32_16x16x32_bf16 v[248:251], a[8:11], v[68:71], v[248:251]\n" \
"v_mfma_f32_16x16x32_bf16 v[252:255], a[8:11], v[84:87], v[252:255]\n" \
"s_waitcnt lgkmcnt(0)\n" \
"v_mfma_f32_16x16x32_bf16 v[112:115], a[12:15], a[228:231], v[112:115]\n" \
"v_mfma_f32_16x16x32_bf16 v[116:119], a[12:15], a[244:247], v[116:119]\n" \
"v_mfma_f32_16x16x32_bf16 v[184:187], a[12:15], v[72:75], v[184:187]\n" \
"buffer_load_dwordx4 a[72:75], v20, s[56:59], s66 offen offset:2048 sc1\n" \
"v_mfma_f32_16x16x32_bf16 v[188:191], a[12:15], v[96:99], v[188:191]\n" \
"v_mfma_f32_16x16x32_bf16 v[120:123], a[16:19], a[228:231], v[120:123]\n" \
"v_mfma_f32_16x16x32_bf16 v[124:127], a[16:19], a[244:247], v[124:127]\n" \
"v_mfma_f32_16x16x32_bf16 v[192:195], a[16:19], v[72:75], v[192:195]\n" \
"v_mfma_f32_16x16x32_bf16 v[196:199], a[16:19], v[96:99], v[196:199]\n" \
"buffer_load_dwordx4 a[88:91], v32, s[56:59], s66 offen offset:2048 sc1\n" \
"v_mfma_f32_16x16x32_bf16 v[128:131], a[20:23], a[228:231], v[128:131]\n" \
"v_mfma_f32_16x16x32_bf16 v[132:135], a[20:23], a[244:247], v[132:135]\n" \
"v_mfma_f32_16x16x32_bf16 v[200:203], a[20:23], v[72:75], v[200:203]\n" \
"v_mfma_f32_16x16x32_bf16 v[204:207], a[20:23], v[96:99], v[204:207]\n" \
"s_barrier\n" \
"v_mfma_f32_16x16x32_bf16 v[136:139], a[24:27], a[228:231], v[136:139]\n" \
"ds_read_b64_tr_b16 a[208:209], v33 offset:9216\n" \
"ds_read_b64_tr_b16 a[210:211], v33 offset:13824\n" \
"v_mfma_f32_16x16x32_bf16 v[140:143], a[24:27], a[244:247], v[140:143]\n" \
"ds_read_b64_tr_b16 a[212:213], v33 offset:9248\n" \
"ds_read_b64_tr_b16 a[214:215], v33 offset:13856\n" \
"v_mfma_f32_16x16x32_bf16 v[208:211], a[24:27], v[72:75], v[208:211]\n" \
"ds_read_b64_tr_b16 a[216:217], v33 offset:9280\n" \
"ds_read_b64_tr_b16 a[218:219], v33 offset:13888\n" \
"v_mfma_f32_16x16x32_bf16 v[212:215], a[24:27], v[96:99], v[212:215]\n" \
"ds_read_b64_tr_b16 a[0:1], v33 offset:9312\n" \
"ds_read_b64_tr_b16 a[2:3], v33 offset:13920\n" \
"v_mfma_f32_16x16x32_bf16 v[144:147], a[28:31], a[228:231], v[144:147]\n" \
"ds_read_b64_tr_b16 a[4:5], v33 offset:9344\n" \
"ds_read_b64_tr_b16 a[6:7], v33 offset:13952\n" \
"v_mfma_f32_16x16x32_bf16 v[148:151], a[28:31], a[244:247], v[148:151]\n" \
"ds_read_b64_tr_b16 a[8:9], v33 offset:9376\n" \
"ds_read_b64_tr_b16 a[10:11], v33 offset:13984\n" \
"v_mfma_f32_16x16x32_bf16 v[216:219], a[28:31], v[72:75], v[216:219]\n" \
"ds_read_b64_tr_b16 a[12:13], v33 offset:9408\n" \
"ds_read_b64_tr_b16 a[14:15], v33 offset:14016\n" \
"v_mfma_f32_16x16x32_bf16 v[220:223], a[28:31], v[96:99], v[220:223]\n" \
"ds_read_b64_tr_b16 a[16:17], v33 offset:9440\n" \
"ds_read_b64_tr_b16 a[18:19], v33 offset:14048\n" \
"v_mfma_f32_16x16x32_bf16 v[152:155], a[192:195], a[228:231], v[152:155]\n" \
"ds_read_b64_tr_b16 a[20:21], v33 offset:9472\n" \
"ds_read_b64_tr_b16 a[22:23], v33 offset:14080\n" \
"v_mfma_f32_16x16x32_bf16 v[156:159], a[192:195], a[244:247], v[156:159]\n" \
"ds_read_b64_tr_b16 a[24:25], v35 offset:0\n" \
"ds_read_b64_tr_b16 a[26:27], v35 offset:2304\n" \
"v_mfma_f32_16x16x32_bf16 v[224:227], a[192:195], v[72:75], v[224:227]\n" \
"ds_read_b64_tr_b16 a[28:29], v35 offset:32\n" \
"ds_read_b64_tr_b16 a[30:31], v35 offset:2336\n" \
"v_mfma_f32_16x16x32_bf16 v[228:231], a[192:195], v[96:99], v[228:231]\n" \
"ds_read_b64_tr_b16 a[192:193], v35 offset:64\n" \
"ds_read_b64_tr_b16 a[194:195], v35 offset:2368\n" \
"v_mfma_f32_16x16x32_bf16 v[160:163], a[196:199], a[228:231], v[160:163]\n" \
"buffer_load_dwordx4 v0, s[80:83], s67 offen offset:0 lds\n" \
"v_mfma_f32_16x16x32_bf16 v[164:167], a[196:199], a[244:247], v[164:167]\n" \
"v_mfma_f32_16x16x32_bf16 v[232:235], a[196:199], v[72:75], v[232:235]\n" \
"v_mfma_f32_16x16x32_bf16 v[236:239], a[196:199], v[96:99], v[236:239]\n" \
"ds_read_b64_tr_b16 a[196:197], v35 offset:96\n" \
"ds_read_b64_tr_b16 a[198:199], v35 offset:2400\n" \
"v_mfma_f32_16x16x32_bf16 v[168:171], a[200:203], a[228:231], v[168:171]\n" \
"v_mfma_f32_16x16x32_bf16 v[172:175], a[200:203], a[244:247], v[172:175]\n" \
"buffer_load_dwordx4 v1, s[80:83], s67 offen offset:1024 lds\n" \
"v_mfma_f32_16x16x32_bf16 v[240:243], a[200:203], v[72:75], v[240:243]\n" \
"v_mfma_f32_16x16x32_bf16 v[244:247], a[200:203], v[96:99], v[244:247]\n" \
"ds_read_b64_tr_b16 a[200:201], v35 offset:128\n" \
"ds_read_b64_tr_b16 a[202:203], v35 offset:2432\n" \
"v_mfma_f32_16x16x32_bf16 v[176:179], a[204:207], a[228:231], v[176:179]\n" \
"v_mfma_f32_16x16x32_bf16 v[180:183], a[204:207], a[244:247], v[180:183]\n" \
"v_mfma_f32_16x16x32_bf16 v[248:251], a[204:207], v[72:75], v[248:251]\n" \
"buffer_load_dwordx4 v2, s[80:83], s67 offen offset:2048 lds\n" \
"v_mfma_f32_16x16x32_bf16 v[252:255], a[204:207], v[96:99], v[252:255]\n" \
"ds_read_b64_tr_b16 a[204:205], v35 offset:160\n" \
"s_waitcnt lgkmcnt(9)\n" \
"v_mfma_f32_16x16x32_bf16 v[112:115], a[208:211], a[232:235], v[112:115]\n" \
"ds_read_b64_tr_b16 a[206:207], v35 offset:2464\n" \
"v_mfma_f32_16x16x32_bf16 v[116:119], a[208:211], a[248:251], v[116:119]\n" \
"v_mfma_f32_16x16x32_bf16 v[184:187], a[208:211], v[76:79], v[184:187]\n" \
"v_mfma_f32_16x16x32_bf16 v[188:191], a[208:211], v[100:103], v[188:191]\n" \
"ds_read_b64_tr_b16 a[208:209], v35 offset:192\n" \
"ds_read_b64_tr_b16 a[210:211], v35 offset:2496\n" \
"v_mfma_f32_16x16x32_bf16 v[120:123], a[212:215], a[232:235], v[120:123]\n" \
"buffer_load_dwordx4 v3, s[80:83], s67 offen offset:3072 lds\n" \
"v_mfma_f32_16x16x32_bf16 v[124:127], a[212:215], a[248:251], v[124:127]\n" \
"s_add_u32 m0, s99, 0x15400\n" \
"v_mfma_f32_16x16x32_bf16 v[192:195], a[212:215], v[76:79], v[192:195]\n" \
"v_mfma_f32_16x16x32_bf16 v[196:199], a[212:215], v[100:103], v[196:199]\n" \
"ds_read_b64_tr_b16 a[212:213], v35 offset:224\n" \
"ds_read_b64_tr_b16 a[214:215], v35 offset:2528\n" \
"v_mfma_f32_16x16x32_bf16 v[128:131], a[216:219], a[232:235], v[128:131]\n" \
"v_mfma_f32_16x16x32_bf16 v[132:135], a[216:219], a[248:251], v[132:135]\n" \
"buffer_load_dwordx4 v4, s[80:83], s67 offen offset:0 lds\n" \
"v_mfma_f32_16x16x32_bf16 v[200:203], a[216:219], v[76:79], v[200:203]\n" \
"v_mfma_f32_16x16x32_bf16 v[204:207], a[216:219], v[100:103], v[204:207]\n" \
"ds_read_b64_tr_b16 a[216:217], v35 offset:256\n" \
"ds_read_b64_tr_b16 a[218:219], v35 offset:2560\n" \
"v_mfma_f32_16x16x32_bf16 v[136:139], a[0:3], a[232:235], v[136:139]\n" \
"v_mfma_f32_16x16x32_bf16 v[140:143], a[0:3], a[248:251], v[140:143]\n" \
"v_mfma_f32_16x16x32_bf16 v[208:211], a[0:3], v[76:79], v[208:211]\n" \
"buffer_load_dwordx4 v5, s[80:83], s67 offen offset:1024 lds\n" \
"v_mfma_f32_16x16x32_bf16 v[212:215], a[0:3], v[100:103], v[212:215]\n" \
"ds_read_b64_tr_b16 a[0:1], v35 offset:10368\n" \
"ds_read_b64_tr_b16 a[2:3], v35 offset:12672\n" \
"v_mfma_f32_16x16x32_bf16 v[144:147], a[4:7], a[232:235], v[144:147]\n" \
"v_mfma_f32_16x16x32_bf16 v[148:151], a[4:7], a[248:251], v[148:151]\n" \
"v_mfma_f32_16x16x32_bf16 v[216:219], a[4:7], v[76:79], v[216:219]\n" \
"v_mfma_f32_16x16x32_bf16 v[220:223], a[4:7], v[100:103], v[220:223]\n" \
"L_pf19:\n" \
"s_cbranch_execz L_pf21\n" \
"s_nop 0\n" \
"ds_read_b64_tr_b16 a[4:5], v35 offset:10400\n" \
"ds_read_b64_tr_b16 a[6:7], v35 offset:12704\n" \
"v_mfma_f32_16x16x32_bf16 v[152:155], a[8:11], a[232:235], v[152:155]\n" \
"buffer_load_dwordx4 v6, s[80:83], s67 offen offset:2048 lds\n" \
"v_mfma_f32_16x16x32_bf16 v[156:159], a[8:11], a[248:251], v[156:159]\n" \
"v_mfma_f32_16x16x32_bf16 v[224:227], a[8:11], v[76:79], v[224:227]\n" \
"v_mfma_f32_16x16x32_bf16 v[228:231], a[8:11], v[100:103], v[228:231]\n" \
"ds_read_b64_tr_b16 a[8:9], v35 offset:10432\n" \
"ds_read_b64_tr_b16 a[10:11], v35 offset:12736\n" \
"v_mfma_f32_16x16x32_bf16 v[160:163], a[12:15], a[232:235], v[160:163]\n" \
"v_mfma_f32_16x16x32_bf16 v[164:167], a[12:15], a[248:251], v[164:167]\n" \
"buffer_load_dwordx4 v7, s[80:83], s67 offen offset:3072 lds\n" \
"v_mfma_f32_16x16x32_bf16 v[232:235], a[12:15], v[76:79], v[232:235]\n" \
"s_add_u32 m0, s99, 0x16400\n" \
"v_mfma_f32_16x16x32_bf16 v[236:239], a[12:15], v[100:103], v[236:239]\n" \
"ds_read_b64_tr_b16 a[12:13], v35 offset:10464\n" \
"ds_read_b64_tr_b16 a[14:15], v35 offset:12768\n" \
"v_mfma_f32_16x16x32_bf16 v[168:171], a[16:19], a[232:235], v[168:171]\n" \
"v_mfma_f32_16x16x32_bf16 v[172:175], a[16:19], a[248:251], v[172:175]\n" \
"v_mfma_f32_16x16x32_bf16 v[240:243], a[16:19], v[76:79], v[240:243]\n" \
"buffer_load_dwordx4 v8, s[80:83], s67 offen offset:0 lds\n" \
"v_mfma_f32_16x16x32_bf16 v[244:247], a[16:19], v[100:103], v[244:247]\n" \
"s_add_u32 m0, s92, 0x0\n" \
"ds_read_b64_tr_b16 a[16:17], v35 offset:10496\n" \
"v_mfma_f32_16x16x32_bf16 v[176:179], a[20:23], a[232:235], v[176:179]\n" \
"ds_read_b64_tr_b16 a[18:19], v35 offset:12800\n" \
"v_readlane_b32 s67, v11, 46\n" \
"v_mfma_f32_16x16x32_bf16 v[180:183], a[20:23], a[248:251], v[180:183]\n" \
"v_mfma_f32_16x16x32_bf16 v[248:251], a[20:23], v[76:79], v[248:251]\n" \
"v_mfma_f32_16x16x32_bf16 v[252:255], a[20:23], v[100:103], v[252:255]\n" \
"buffer_load_dwordx4 v0, s[80:83], s73 offen offset:0 lds\n" \
"v_mfma_f32_16x16x32_bf16 v[112:115], a[24:27], a[96:99], v[112:115]\n" \
"ds_read_b64_tr_b16 a[20:21], v35 offset:10528\n" \
"ds_read_b64_tr_b16 a[22:23], v35 offset:12832\n" \
"v_mfma_f32_16x16x32_bf16 v[116:119], a[24:27], a[104:107], v[116:119]\n" \
"v_mfma_f32_16x16x32_bf16 v[184:187], a[24:27], a[112:115], v[184:187]\n" \
"v_mfma_f32_16x16x32_bf16 v[188:191], a[24:27], a[120:123], v[188:191]\n" \
"ds_read_b64_tr_b16 a[24:25], v35 offset:10560\n" \
"s_waitcnt lgkmcnt(7)\n" \
"v_mfma_f32_16x16x32_bf16 v[120:123], a[28:31], a[96:99], v[120:123]\n" \
"buffer_load_dword v47, v38, s[56:59], s71 offen sc1\n" \
"v_mfma_f32_16x16x32_bf16 v[124:127], a[28:31], a[104:107], v[124:127]\n" \
"ds_read_b64_tr_b16 a[26:27], v35 offset:12864\n" \
"v_mfma_f32_16x16x32_bf16 v[192:195], a[28:31], a[112:115], v[192:195]\n" \
"v_mfma_f32_16x16x32_bf16 v[196:199], a[28:31], a[120:123], v[196:199]\n" \
"ds_read_b64_tr_b16 a[28:29], v35 offset:10592\n" \
"ds_read_b64_tr_b16 a[30:31], v35 offset:12896\n" \
"v_mfma_f32_16x16x32_bf16 v[128:131], a[192:195], a[96:99], v[128:131]\n" \
"v_mfma_f32_16x16x32_bf16 v[132:135], a[192:195], a[104:107], v[132:135]\n" \
"buffer_load_dwordx4 v1, s[80:83], s73 offen offset:1024 lds\n" \
"v_mfma_f32_16x16x32_bf16 v[200:203], a[192:195], a[112:115], v[200:203]\n" \
"v_mfma_f32_16x16x32_bf16 v[204:207], a[192:195], a[120:123], v[204:207]\n" \
"ds_read_b64_tr_b16 a[192:193], v35 offset:10624\n" \
"ds_read_b64_tr_b16 a[194:195], v35 offset:12928\n" \
"v_mfma_f32_16x16x32_bf16 v[136:139], a[196:199], a[96:99], v[136:139]\n" \
"v_mfma_f32_16x16x32_bf16 v[140:143], a[196:199], a[104:107], v[140:143]\n" \
"v_mfma_f32_16x16x32_bf16 v[208:211], a[196:199], a[112:115], v[208:211]\n" \
"buffer_load_dwordx4 v2, s[80:83], s73 offen offset:2048 lds\n" \
"v_mfma_f32_16x16x32_bf16 v[212:215], a[196:199], a[120:123], v[212:215]\n" \
"v_mfma_f32_16x16x32_bf16 v[144:147], a[200:203], a[96:99], v[144:147]\n" \
"v_mfma_f32_16x16x32_bf16 v[148:151], a[200:203], a[104:107], v[148:151]\n" \
"v_mfma_f32_16x16x32_bf16 v[216:219], a[200:203], a[112:115], v[216:219]\n" \
"v_mfma_f32_16x16x32_bf16 v[220:223], a[200:203], a[120:123], v[220:223]\n" \
"buffer_load_dwordx4 v3, s[80:83], s73 offen offset:3072 lds\n" \
"v_mfma_f32_16x16x32_bf16 v[152:155], a[204:207], a[96:99], v[152:155]\n" \
"s_add_u32 m0, s92, 0x1000\n" \
"v_mfma_f32_16x16x32_bf16 v[156:159], a[204:207], a[104:107], v[156:159]\n" \
"v_mfma_f32_16x16x32_bf16 v[224:227], a[204:207], a[112:115], v[224:227]\n" \
"v_mfma_f32_16x16x32_bf16 v[228:231], a[204:207], a[120:123], v[228:231]\n" \
"v_mfma_f32_16x16x32_bf16 v[160:163], a[208:211], a[96:99], v[160:163]\n" \
"buffer_load_dwordx4 v4, s[80:83], s73 offen offset:0 lds\n" \
"v_mfma_f32_16x16x32_bf16 v[164:167], a[208:211], a[104:107], v[164:167]\n" \
"v_mfma_f32_16x16x32_bf16 v[232:235], a[208:211], a[112:115], v[232:235]\n" \
"v_mfma_f32_16x16x32_bf16 v[236:239], a[208:211], a[120:123], v[236:239]\n" \
"v_mfma_f32_16x16x32_bf16 v[168:171], a[212:215], a[96:99], v[168:171]\n" \
"v_mfma_f32_16x16x32_bf16 v[172:175], a[212:215], a[104:107], v[172:175]\n" \
"buffer_load_dwordx4 v5, s[80:83], s73 offen offset:1024 lds\n" \
"v_mfma_f32_16x16x32_bf16 v[240:243], a[212:215], a[112:115], v[240:243]\n" \
"v_mfma_f32_16x16x32_bf16 v[244:247], a[212:215], a[120:123], v[244:247]\n" \
"v_mfma_f32_16x16x32_bf16 v[176:179], a[216:219], a[96:99], v[176:179]\n" \
"v_mfma_f32_16x16x32_bf16 v[180:183], a[216:219], a[104:107], v[180:183]\n" \
"v_mfma_f32_16x16x32_bf16 v[248:251], a[216:219], a[112:115], v[248:251]\n" \
"buffer_load_dwordx4 v6, s[80:83], s73 offen offset:2048 lds\n" \
"v_mfma_f32_16x16x32_bf16 v[252:255], a[216:219], a[120:123], v[252:255]\n" \
"v_mfma_f32_16x16x32_bf16 v[112:115], a[0:3], a[100:103], v[112:115]\n" \
"v_mfma_f32_16x16x32_bf16 v[116:119], a[0:3], a[108:111], v[116:119]\n" \
"v_mfma_f32_16x16x32_bf16 v[184:187], a[0:3], a[116:119], v[184:187]\n" \
"v_mfma_f32_16x16x32_bf16 v[188:191], a[0:3], a[124:127], v[188:191]\n" \
"buffer_load_dwordx4 v7, s[80:83], s73 offen offset:3072 lds\n" \
"s_add_u32 m0, s92, 0x2000\n" \
"v_mfma_f32_16x16x32_bf16 v[120:123], a[4:7], a[100:103], v[120:123]\n" \
"v_readlane_b32 s92, v11, 44\n" \
"v_mfma_f32_16x16x32_bf16 v[124:127], a[4:7], a[108:111], v[124:127]\n" \
"v_mfma_f32_16x16x32_bf16 v[192:195], a[4:7], a[116:119], v[192:195]\n" \
"v_mfma_f32_16x16x32_bf16 v[196:199], a[4:7], a[124:127], v[196:199]\n" \
"buffer_load_dwordx4 v8, s[80:83], s73 offen offset:0 lds\n" \
"s_mov_b32 m0, s94\n" \
"v_mfma_f32_16x16x32_bf16 v[128:131], a[8:11], a[100:103], v[128:131]\n" \
"v_readlane_b32 s73, v11, 50\n" \
"v_readlane_b32 s94, v11, 46\n" \
"v_mfma_f32_16x16x32_bf16 v[132:135], a[8:11], a[108:111], v[132:135]\n" \
"v_mfma_f32_16x16x32_bf16 v[200:203], a[8:11], a[116:119], v[200:203]\n" \
"v_mfma_f32_16x16x32_bf16 v[204:207], a[8:11], a[124:127], v[204:207]\n" \
"buffer_load_dwordx4 v9, s[84:87], 0 offen lds\n" \
"s_waitcnt lgkmcnt(0)\n" \
"v_mfma_f32_16x16x32_bf16 v[136:139], a[12:15], a[100:103], v[136:139]\n" \
"v_mfma_f32_16x16x32_bf16 v[140:143], a[12:15], a[108:111], v[140:143]\n" \
"v_mfma_f32_16x16x32_bf16 v[208:211], a[12:15], a[116:119], v[208:211]\n" \
"v_mfma_f32_16x16x32_bf16 v[212:215], a[12:15], a[124:127], v[212:215]\n" \
"buffer_load_dwordx4 a[76:79], v20, s[56:59], s66 offen offset:3072 sc1\n" \
"s_waitcnt vmcnt(23)\n" \
"s_barrier\n" \
"v_mfma_f32_16x16x32_bf16 v[144:147], a[16:19], a[100:103], v[144:147]\n" \
"ds_read_b64_tr_b16 a[196:197], v10 offset:27648\n" \
"ds_read_b64_tr_b16 a[198:199], v10 offset:32256\n" \
"v_mfma_f32_16x16x32_bf16 v[148:151], a[16:19], a[108:111], v[148:151]\n" \
"ds_read_b64_tr_b16 a[200:201], v10 offset:27680\n" \
"ds_read_b64_tr_b16 a[202:203], v10 offset:32288\n" \
"v_mfma_f32_16x16x32_bf16 v[216:219], a[16:19], a[116:119], v[216:219]\n" \
"buffer_load_dwordx4 a[92:95], v32, s[56:59], s66 offen offset:3072 sc1\n" \
"v_mfma_f32_16x16x32_bf16 v[220:223], a[16:19], a[124:127], v[220:223]\n" \
"ds_read_b64_tr_b16 a[204:205], v10 offset:27712\n" \
"ds_read_b64_tr_b16 a[206:207], v10 offset:32320\n" \
"v_mfma_f32_16x16x32_bf16 v[152:155], a[20:23], a[100:103], v[152:155]\n" \
"ds_read_b64_tr_b16 a[208:209], v10 offset:27744\n" \
"ds_read_b64_tr_b16 a[210:211], v10 offset:32352\n" \
"v_mfma_f32_16x16x32_bf16 v[156:159], a[20:23], a[108:111], v[156:159]\n" \
"ds_read_b64_tr_b16 a[212:213], v10 offset:27776\n" \
"ds_read_b64_tr_b16 a[214:215], v10 offset:32384\n" \
"v_mfma_f32_16x16x32_bf16 v[224:227], a[20:23], a[116:119], v[224:227]\n" \
"ds_read_b64_tr_b16 a[216:217], v10 offset:27808\n" \
"ds_read_b64_tr_b16 a[218:219], v10 offset:32416\n" \
"buffer_load_dwordx4 a[128:131], v20, s[56:59], s68 offen offset:0 sc1\n" \
"v_mfma_f32_16x16x32_bf16 v[228:231], a[20:23], a[124:127], v[228:231]\n" \
"ds_read_b64_tr_b16 a[0:1], v10 offset:27840\n" \
"ds_read_b64_tr_b16 a[2:3], v10 offset:32448\n" \
"v_mfma_f32_16x16x32_bf16 v[160:163], a[24:27], a[100:103], v[160:163]\n" \
"ds_read_b64_tr_b16 a[4:5], v10 offset:27872\n" \
"ds_read_b64_tr_b16 a[6:7], v10 offset:32480\n" \
"v_mfma_f32_16x16x32_bf16 v[164:167], a[24:27], a[108:111], v[164:167]\n" \
"ds_read_b64_tr_b16 a[8:9], v10 offset:27904\n" \
"ds_read_b64_tr_b16 a[10:11], v10 offset:32512\n" \
"v_mfma_f32_16x16x32_bf16 v[232:235], a[24:27], a[116:119], v[232:235]\n" \
"ds_read_b64_tr_b16 a[12:13], v10 offset:36864\n" \
"ds_read_b64_tr_b16 a[14:15], v10 offset:41472\n" \
"buffer_load_dwordx4 a[144:147], v32, s[56:59], s68 offen offset:0 sc1\n" \
"v_mfma_f32_16x16x32_bf16 v[236:239], a[24:27], a[124:127], v[236:239]\n" \
"ds_read_b64_tr_b16 a[16:17], v10 offset:36896\n" \
"ds_read_b64_tr_b16 a[18:19], v10 offset:41504\n" \
"v_mfma_f32_16x16x32_bf16 v[168:171], a[28:31], a[100:103], v[168:171]\n" \
"ds_read_b64_tr_b16 a[20:21], v10 offset:36928\n" \
"ds_read_b64_tr_b16 a[22:23], v10 offset:41536\n" \
"v_mfma_f32_16x16x32_bf16 v[172:175], a[28:31], a[108:111], v[172:175]\n" \
"ds_read_b64_tr_b16 a[24:25], v10 offset:36960\n" \
"ds_read_b64_tr_b16 a[26:27], v10 offset:41568\n" \
"v_mfma_f32_16x16x32_bf16 v[240:243], a[28:31], a[116:119], v[240:243]\n" \
"v_readlane_b32 s66, v11, 45\n" \
"s_waitcnt vmcnt(13)\n" \
"v_cmp_ne_u32_e32 vcc, s62, v47\n" \
"buffer_load_dwordx4 a[132:135], v20, s[56:59], s68 offen offset:1024 sc1\n" \
"v_mfma_f32_16x16x32_bf16 v[244:247], a[28:31], a[124:127], v[244:247]\n" \
"ds_read_b64_tr_b16 a[28:29], v10 offset:36992\n" \
"ds_read_b64_tr_b16 a[30:31], v10 offset:41600\n" \
"v_mfma_f32_16x16x32_bf16 v[176:179], a[192:195], a[100:103], v[176:179]\n" \
"s_cbranch_vccnz L_sb_%=\n" \
"L_rb_%=:\n" \
"v_readlane_b32 s71, v11, 48\n" \
"v_mfma_f32_16x16x32_bf16 v[180:183], a[192:195], a[108:111], v[180:183]\n" \
"v_cmp_eq_u32_e32 vcc, 0, v44\n" \
"v_mfma_f32_16x16x32_bf16 v[248:251], a[192:195], a[116:119], v[248:251]\n" \
"v_mfma_f32_16x16x32_bf16 v[252:255], a[192:195], a[124:127], v[252:255]\n" \
"ds_read_b64_tr_b16 a[192:193], v10 offset:37024\n" \
"buffer_load_dwordx4 a[148:151], v32, s[56:59], s68 offen offset:1024 sc1\n" \
"s_waitcnt lgkmcnt(15)\n" \
"v_mfma_f32_16x16x32_bf16 v[112:115], a[196:199], a[64:67], v[112:115]\n" \
"ds_read_b64_tr_b16 a[194:195], v10 offset:41632\n" \
"v_cndmask_b32_e32 v50, 0, v45, vcc\n" \
"v_mfma_f32_16x16x32_bf16 v[116:119], a[196:199], a[80:83], v[116:119]\n" \
"ds_read_b64_tr_b16 a[196:197], v10 offset:37056\n" \
"ds_read_b64_tr_b16 a[198:199], v10 offset:41664\n" \
"v_mfma_f32_16x16x32_bf16 v[120:123], a[200:203], a[64:67], v[120:123]\n" \
"v_cmp_eq_u32_e32 vcc, 1, v44\n" \
"v_and_b32_e32 v44, 0x3c0, v20\n" \
"v_mfma_f32_16x16x32_bf16 v[124:127], a[200:203], a[80:83], v[124:127]\n" \
"buffer_load_dwordx4 a[136:139], v20, s[56:59], s68 offen offset:2048 sc1\n" \
"v_mfma_f32_16x16x32_bf16 v[128:131], a[204:207], a[64:67], v[128:131]\n" \
"ds_read_b64_tr_b16 a[200:201], v10 offset:37088\n" \
"ds_read_b64_tr_b16 a[202:203], v10 offset:41696\n" \
"v_mfma_f32_16x16x32_bf16 v[132:135], a[204:207], a[80:83], v[132:135]\n" \
"ds_read_b64_tr_b16 a[204:205], v10 offset:37120\n" \
"ds_read_b64_tr_b16 a[206:207], v10 offset:41728\n" \
"v_mfma_f32_16x16x32_bf16 v[136:139], a[208:211], a[64:67], v[136:139]\n" \
"v_cndmask_b32_e32 v51, 0, v45, vcc\n" \
"v_bfe_u32 v45, v20, 1, 5\n" \
"v_mfma_f32_16x16x32_bf16 v[140:143], a[208:211], a[80:83], v[140:143]\n" \
"ds_read_b64_tr_b16 a[208:209], v10 offset:46080\n" \
"ds_read_b64_tr_b16 a[210:211], v10 offset:50688\n" \
"buffer_load_dwordx4 a[152:155], v32, s[56:59], s68 offen offset:2048 sc1\n" \
"v_mfma_f32_16x16x32_bf16 v[144:147], a[212:215], a[64:67], v[144:147]\n" \
"v_and_b32_e32 v45, 24, v45\n" \
"v_add_u32_e32 v45, 0x1d400, v45\n" \
"v_mfma_f32_16x16x32_bf16 v[148:151], a[212:215], a[80:83], v[148:151]\n" \
"ds_read_b64_tr_b16 a[212:213], v10 offset:46112\n" \
"ds_read_b64_tr_b16 a[214:215], v10 offset:50720\n" \
"v_mfma_f32_16x16x32_bf16 v[152:155], a[216:219], a[64:67], v[152:155]\n" \
"v_add_u32_e32 v36, v44, v45\n" \
"v_mfma_f32_16x16x32_bf16 v[156:159], a[216:219], a[80:83], v[156:159]\n" \
"ds_read_b64_tr_b16 a[216:217], v10 offset:46144\n" \
"ds_read_b64_tr_b16 a[218:219], v10 offset:50752\n" \
"buffer_load_dwordx4 a[140:143], v20, s[56:59], s68 offen offset:3072 sc1\n" \
"v_mfma_f32_16x16x32_bf16 v[160:163], a[0:3], a[64:67], v[160:163]\n" \
"v_mfma_f32_16x16x32_bf16 v[164:167], a[0:3], a[80:83], v[164:167]\n" \
"ds_read_b64_tr_b16 a[0:1], v10 offset:46176\n" \
"s_waitcnt lgkmcnt(13)\n" \
"v_mfma_f32_16x16x32_bf16 v[168:171], a[4:7], a[64:67], v[168:171]\n" \
"ds_read_b64_tr_b16 a[2:3], v10 offset:50784\n" \
"v_mfma_f32_16x16x32_bf16 v[172:175], a[4:7], a[80:83], v[172:175]\n" \
"ds_read_b64_tr_b16 a[4:5], v10 offset:46208\n" \
"ds_read_b64_tr_b16 a[6:7], v10 offset:50816\n" \
"buffer_load_dwordx4 a[156:159], v32, s[56:59], s68 offen offset:3072 sc1\n" \
"v_mfma_f32_16x16x32_bf16 v[176:179], a[8:11], a[64:67], v[176:179]\n" \
"v_readlane_b32 s68, v11, 51\n" \
"v_mfma_f32_16x16x32_bf16 v[180:183], a[8:11], a[80:83], v[180:183]\n" \
"ds_read_b64_tr_b16 a[8:9], v10 offset:46240\n" \
"ds_read_b64_tr_b16 a[10:11], v10 offset:50848\n" \
"v_mfma_f32_16x16x32_bf16 v[112:115], a[12:15], a[68:71], v[112:115]\n" \
"v_mfma_f32_16x16x32_bf16 v[116:119], a[12:15], a[84:87], v[116:119]\n" \
"ds_read_b64_tr_b16 a[12:13], v10 offset:46272\n" \
"ds_read_b64_tr_b16 a[14:15], v10 offset:50880\n" \
"buffer_load_dwordx4 a[32:35], v20, s[56:59], s69 offen offset:0 sc1\n" \
"v_mfma_f32_16x16x32_bf16 v[120:123], a[16:19], a[68:71], v[120:123]\n" \
"v_mfma_f32_16x16x32_bf16 v[124:127], a[16:19], a[84:87], v[124:127]\n" \
"ds_read_b64_tr_b16 a[16:17], v10 offset:46304\n" \
"ds_read_b64_tr_b16 a[18:19], v10 offset:50912\n" \
"v_mfma_f32_16x16x32_bf16 v[128:131], a[20:23], a[68:71], v[128:131]\n" \
"v_mfma_f32_16x16x32_bf16 v[132:135], a[20:23], a[84:87], v[132:135]\n" \
"ds_read_b64_tr_b16 a[20:21], v10 offset:46336\n" \
"ds_read_b64_tr_b16 a[22:23], v10 offset:50944\n" \
"buffer_load_dwordx4 a[48:51], v32, s[56:59], s69 offen offset:0 sc1\n" \
"v_mfma_f32_16x16x32_bf16 v[136:139], a[24:27], a[68:71], v[136:139]\n" \
"v_mfma_f32_16x16x32_bf16 v[140:143], a[24:27], a[84:87], v[140:143]\n" \
"ds_read_b64_tr_b16 a[24:25], v10 offset:55296\n" \
"ds_read_b64_tr_b16 a[26:27], v10 offset:59904\n" \
"v_mfma_f32_16x16x32_bf16 v[144:147], a[28:31], a[68:71], v[144:147]\n" \
"v_mfma_f32_16x16x32_bf16 v[148:151], a[28:31], a[84:87], v[148:151]\n" \
"ds_read_b64_tr_b16 a[28:29], v10 offset:55328\n" \
"ds_read_b64_tr_b16 a[30:31], v10 offset:59936\n" \
"buffer_load_dwordx4 a[36:39], v20, s[56:59], s69 offen offset:1024 sc1\n" \
"v_mfma_f32_16x16x32_bf16 v[152:155], a[192:195], a[68:71], v[152:155]\n" \
"v_mfma_f32_16x16x32_bf16 v[156:159], a[192:195], a[84:87], v[156:159]\n" \
"ds_read_b64_tr_b16 a[192:193], v10 offset:55360\n" \
"s_waitcnt lgkmcnt(13)\n" \
"v_mfma_f32_16x16x32_bf16 v[160:163], a[196:199], a[68:71], v[160:163]\n" \
"ds_read_b64_tr_b16 a[194:195], v10 offset:59968\n" \
"v_mfma_f32_16x16x32_bf16 v[164:167], a[196:199], a[84:87], v[164:167]\n" \
"ds_read_b64_tr_b16 a[196:197], v10 offset:55392\n" \
"ds_read_b64_tr_b16 a[198:199], v10 offset:60000\n" \
"buffer_load_dwordx4 a[52:55], v32, s[56:59], s69 offen offset:1024 sc1\n" \
"v_mfma_f32_16x16x32_bf16 v[168:171], a[200:203], a[68:71], v[168:171]\n" \
"v_mfma_f32_16x16x32_bf16 v[172:175], a[200:203], a[84:87], v[172:175]\n" \
"ds_read_b64_tr_b16 a[200:201], v10 offset:55424\n" \
"ds_read_b64_tr_b16 a[202:203], v10 offset:60032\n" \
"v_mfma_f32_16x16x32_bf16 v[176:179], a[204:207], a[68:71], v[176:179]\n" \
"v_mfma_f32_16x16x32_bf16 v[180:183], a[204:207], a[84:87], v[180:183]\n" \
"ds_read_b64_tr_b16 a[204:205], v10 offset:55456\n" \
"ds_read_b64_tr_b16 a[206:207], v10 offset:60064\n" \
"buffer_load_dwordx4 a[40:43], v20, s[56:59], s69 offen offset:2048 sc1\n" \
"v_mfma_f32_16x16x32_bf16 v[112:115], a[208:211], a[72:75], v[112:115]\n" \
"v_mfma_f32_16x16x32_bf16 v[116:119], a[208:211], a[88:91], v[116:119]\n" \
"ds_read_b64_tr_b16 a[208:209], v10 offset:55488\n" \
"ds_read_b64_tr_b16 a[210:211], v10 offset:60096\n" \
"v_mfma_f32_16x16x32_bf16 v[120:123], a[212:215], a[72:75], v[120:123]\n" \
"v_mfma_f32_16x16x32_bf16 v[124:127], a[212:215], a[88:91], v[124:127]\n" \
"ds_read_b64_tr_b16 a[212:213], v10 offset:55520\n" \
"ds_read_b64_tr_b16 a[214:215], v10 offset:60128\n" \
"buffer_load_dwordx4 a[56:59], v32, s[56:59], s69 offen offset:2048 sc1\n" \
"v_mfma_f32_16x16x32_bf16 v[128:131], a[216:219], a[72:75], v[128:131]\n" \
"v_mfma_f32_16x16x32_bf16 v[132:135], a[216:219], a[88:91], v[132:135]\n" \
"ds_read_b64_tr_b16 a[216:217], v10 offset:55552\n" \
"ds_read_b64_tr_b16 a[218:219], v10 offset:60160\n" \
"v_mfma_f32_16x16x32_bf16 v[136:139], a[0:3], a[72:75], v[136:139]\n" \
"v_mfma_f32_16x16x32_bf16 v[140:143], a[0:3], a[88:91], v[140:143]\n" \
"v_mfma_f32_16x16x32_bf16 v[144:147], a[4:7], a[72:75], v[144:147]\n" \
"buffer_load_dwordx4 a[44:47], v20, s[56:59], s69 offen offset:3072 sc1\n" \
"v_mfma_f32_16x16x32_bf16 v[148:151], a[4:7], a[88:91], v[148:151]\n" \
"s_waitcnt lgkmcnt(10)\n" \
"v_mfma_f32_16x16x32_bf16 v[152:155], a[8:11], a[72:75], v[152:155]\n" \
"v_mfma_f32_16x16x32_bf16 v[156:159], a[8:11], a[88:91], v[156:159]\n" \
"v_mfma_f32_16x16x32_bf16 v[160:163], a[12:15], a[72:75], v[160:163]\n" \
"v_mfma_f32_16x16x32_bf16 v[164:167], a[12:15], a[88:91], v[164:167]\n" \
"buffer_load_dwordx4 a[60:63], v32, s[56:59], s69 offen offset:3072 sc1\n" \
"v_mfma_f32_16x16x32_bf16 v[168:171], a[16:19], a[72:75], v[168:171]\n" \
"v_readlane_b32 s69, v11, 6\n" \
"v_mfma_f32_16x16x32_bf16 v[172:175], a[16:19], a[88:91], v[172:175]\n" \
"v_mfma_f32_16x16x32_bf16 v[176:179], a[20:23], a[72:75], v[176:179]\n" \
"v_mfma_f32_16x16x32_bf16 v[180:183], a[20:23], a[88:91], v[180:183]\n" \
"s_waitcnt vmcnt(18)\n" \
"s_barrier\n" \
"buffer_load_dwordx4 a[96:99], v20, s[56:59], s64 offen offset:0 sc1\n" \
"ds_read_b64_tr_b16 a[0:1], v33 offset:18432\n" \
"ds_read_b64_tr_b16 a[2:3], v33 offset:23040\n" \
"ds_read_b64_tr_b16 a[4:5], v33 offset:27648\n" \
"ds_read_b64_tr_b16 a[6:7], v33 offset:32256\n" \
"ds_read_b64_tr_b16 a[8:9], v33 offset:36864\n" \
"ds_read_b64_tr_b16 a[10:11], v33 offset:41472\n" \
"ds_read_b64_tr_b16 a[12:13], v33 offset:46080\n" \
"ds_read_b64_tr_b16 a[14:15], v33 offset:50688\n" \
"ds_read_b64_tr_b16 a[16:17], v34 offset:0\n" \
"ds_read_b64_tr_b16 a[18:19], v34 offset:4608\n" \
"s_waitcnt vmcnt(18)\n" \
"v_mfma_f32_16x16x32_bf16 v[112:115], a[24:27], a[76:79], v[112:115]\n" \
"ds_read_b64_tr_b16 a[20:21], v34 offset:9216\n" \
"ds_read_b64_tr_b16 a[22:23], v34 offset:13824\n" \
"buffer_load_dwordx4 a[112:115], v32, s[56:59], s64 offen offset:0 sc1\n" \
"s_waitcnt vmcnt(18)\n" \
"v_mfma_f32_16x16x32_bf16 v[116:119], a[24:27], a[92:95], v[116:119]\n" \
"ds_read_b64_tr_b16 a[24:25], v34 offset:18432\n" \
"ds_read_b64_tr_b16 a[26:27], v34 offset:23040\n" \
"buffer_load_dwordx4 a[100:103], v20, s[56:59], s64 offen offset:1024 sc1\n" \
"v_mfma_f32_16x16x32_bf16 v[120:123], a[28:31], a[76:79], v[120:123]\n" \
"v_mfma_f32_16x16x32_bf16 v[124:127], a[28:31], a[92:95], v[124:127]\n" \
"ds_read_b64_tr_b16 a[28:29], v33 offset:0\n" \
"ds_read_b64_tr_b16 a[30:31], v33 offset:4608\n" \
"v_mfma_f32_16x16x32_bf16 v[128:131], a[192:195], a[76:79], v[128:131]\n" \
"v_mfma_f32_16x16x32_bf16 v[132:135], a[192:195], a[92:95], v[132:135]\n" \
"ds_read_b64_tr_b16 a[192:193], v33 offset:18464\n" \
"ds_read_b64_tr_b16 a[194:195], v33 offset:23072\n" \
"buffer_load_dwordx4 a[116:119], v32, s[56:59], s64 offen offset:1024 sc1\n" \
"v_mfma_f32_16x16x32_bf16 v[136:139], a[196:199], a[76:79], v[136:139]\n" \
"v_mfma_f32_16x16x32_bf16 v[140:143], a[196:199], a[92:95], v[140:143]\n" \
"ds_read_b64_tr_b16 a[196:197], v33 offset:27680\n" \
"s_waitcnt lgkmcnt(15)\n" \
"v_mfma_f32_16x16x32_bf16 v[144:147], a[200:203], a[76:79], v[144:147]\n" \
"ds_read_b64_tr_b16 a[198:199], v33 offset:32288\n" \
"v_mfma_f32_16x16x32_bf16 v[148:151], a[200:203], a[92:95], v[148:151]\n" \
"ds_read_b64_tr_b16 a[200:201], v33 offset:36896\n" \
"ds_read_b64_tr_b16 a[202:203], v33 offset:41504\n" \
"buffer_load_dwordx4 a[104:107], v20, s[56:59], s64 offen offset:2048 sc1\n" \
"v_mfma_f32_16x16x32_bf16 v[152:155], a[204:207], a[76:79], v[152:155]\n" \
"v_mfma_f32_16x16x32_bf16 v[156:159], a[204:207], a[92:95], v[156:159]\n" \
"ds_read_b64_tr_b16 a[204:205], v33 offset:46112\n" \
"ds_read_b64_tr_b16 a[206:207], v33 offset:50720\n" \
"v_mfma_f32_16x16x32_bf16 v[160:163], a[208:211], a[76:79], v[160:163]\n" \
"v_mfma_f32_16x16x32_bf16 v[164:167], a[208:211], a[92:95], v[164:167]\n" \
"ds_read_b64_tr_b16 a[208:209], v34 offset:32\n" \
"ds_read_b64_tr_b16 a[210:211], v34 offset:4640\n" \
"buffer_load_dwordx4 a[120:123], v32, s[56:59], s64 offen offset:2048 sc1\n" \
"v_mfma_f32_16x16x32_bf16 v[168:171], a[212:215], a[76:79], v[168:171]\n" \
"v_mfma_f32_16x16x32_bf16 v[172:175], a[212:215], a[92:95], v[172:175]\n" \
"ds_read_b64_tr_b16 a[212:213], v34 offset:9248\n" \
"ds_read_b64_tr_b16 a[214:215], v34 offset:13856\n" \
"v_mfma_f32_16x16x32_bf16 v[176:179], a[216:219], a[76:79], v[176:179]\n" \
"v_mfma_f32_16x16x32_bf16 v[180:183], a[216:219], a[92:95], v[180:183]\n" \
"ds_read_b64_tr_b16 a[216:217], v34 offset:18464\n" \
"buffer_load_dwordx4 a[108:111], v20, s[56:59], s64 offen offset:3072 sc1\n" \
"s_waitcnt vmcnt(22)\n" \
"v_mfma_f32_16x16x32_bf16 v[112:115], a[0:3], a[128:131], v[112:115]\n" \
"ds_read_b64_tr_b16 a[218:219], v34 offset:23072\n" \
"s_waitcnt vmcnt(21)\n" \
"v_mfma_f32_16x16x32_bf16 v[116:119], a[0:3], a[144:147], v[116:119]\n" \
"ds_read_b64_tr_b16 a[0:1], v33 offset:32\n" \
"s_waitcnt vmcnt(20)\n" \
"v_mfma_f32_16x16x32_bf16 v[112:115], a[4:7], a[132:135], v[112:115]\n" \
"ds_read_b64_tr_b16 a[2:3], v33 offset:4640\n" \
"s_waitcnt vmcnt(19)\n" \
"v_mfma_f32_16x16x32_bf16 v[116:119], a[4:7], a[148:151], v[116:119]\n" \
"ds_read_b64_tr_b16 a[4:5], v33 offset:18496\n" \
"buffer_load_dwordx4 a[124:127], v32, s[56:59], s64 offen offset:3072 sc1\n" \
"s_waitcnt vmcnt(19) lgkmcnt(11)\n" \
"v_mfma_f32_16x16x32_bf16 v[112:115], a[8:11], a[136:139], v[112:115]\n" \
"ds_read_b64_tr_b16 a[6:7], v33 offset:23104\n" \
"v_readlane_b32 s64, v11, 40\n" \
"s_waitcnt vmcnt(18)\n" \
"v_mfma_f32_16x16x32_bf16 v[116:119], a[8:11], a[152:155], v[116:119]\n" \
"buffer_load_dwordx4 a[160:163], v20, s[56:59], s72 offen offset:0 sc1\n" \
"ds_read_b64_tr_b16 a[8:9], v33 offset:27712\n" \
"ds_read_b64_tr_b16 a[10:11], v33 offset:32320\n" \
"s_waitcnt vmcnt(18)\n" \
"v_mfma_f32_16x16x32_bf16 v[112:115], a[12:15], a[140:143], v[112:115]\n" \
"buffer_load_dwordx4 a[176:179], v32, s[56:59], s72 offen offset:0 sc1\n" \
"s_waitcnt vmcnt(18)\n" \
"v_mfma_f32_16x16x32_bf16 v[116:119], a[12:15], a[156:159], v[116:119]\n" \
"buffer_load_dwordx4 a[164:167], v20, s[56:59], s72 offen offset:1024 sc1\n" \
"ds_read_b64_tr_b16 a[12:13], v33 offset:36928\n" \
"ds_read_b64_tr_b16 a[14:15], v33 offset:41536\n" \
"s_waitcnt vmcnt(18)\n" \
"v_mfma_f32_16x16x32_bf16 v[112:115], a[16:19], a[32:35], v[112:115]\n" \
"buffer_load_dwordx4 a[180:183], v32, s[56:59], s72 offen offset:1024 sc1\n" \
"s_waitcnt vmcnt(18)\n" \
"v_mfma_f32_16x16x32_bf16 v[116:119], a[16:19], a[48:51], v[116:119]\n" \
"buffer_load_dwordx4 a[168:171], v20, s[56:59], s72 offen offset:2048 sc1\n" \
"ds_read_b64_tr_b16 a[16:17], v33 offset:46144\n" \
"ds_read_b64_tr_b16 a[18:19], v33 offset:50752\n" \
"s_waitcnt vmcnt(18)\n" \
"v_mfma_f32_16x16x32_bf16 v[112:115], a[20:23], a[36:39], v[112:115]\n" \
"buffer_load_dwordx4 a[184:187], v32, s[56:59], s72 offen offset:2048 sc1\n" \
"s_waitcnt vmcnt(18)\n" \
"v_mfma_f32_16x16x32_bf16 v[116:119], a[20:23], a[52:55], v[116:119]\n" \
"buffer_load_dwordx4 a[172:175], v20, s[56:59], s72 offen offset:3072 sc1\n" \
"ds_read_b64_tr_b16 a[20:21], v34 offset:64\n" \
"ds_read_b64_tr_b16 a[22:23], v34 offset:4672\n" \
"s_waitcnt vmcnt(18)\n" \
"v_mfma_f32_16x16x32_bf16 v[112:115], a[24:27], a[40:43], v[112:115]\n" \
"buffer_load_dwordx4 a[188:191], v32, s[56:59], s72 offen offset:3072 sc1\n" \
"v_readlane_b32 s72, v11, 49\n" \
"s_waitcnt vmcnt(18)\n" \
"v_mfma_f32_16x16x32_bf16 v[116:119], a[24:27], a[56:59], v[116:119]\n" \
"buffer_load_dwordx4 a[220:223], v20, s[56:59], s93 offen offset:0 sc1\n" \
"ds_read_b64_tr_b16 a[24:25], v34 offset:9280\n" \
"ds_read_b64_tr_b16 a[26:27], v34 offset:13888\n" \
"s_waitcnt vmcnt(18)\n" \
"v_mfma_f32_16x16x32_bf16 v[112:115], a[28:31], a[44:47], v[112:115]\n" \
"buffer_load_dwordx4 a[236:239], v32, s[56:59], s93 offen offset:0 sc1\n" \
"s_waitcnt vmcnt(18)\n" \
"v_mfma_f32_16x16x32_bf16 v[116:119], a[28:31], a[60:63], v[116:119]\n" \
"buffer_load_dwordx4 a[224:227], v20, s[56:59], s93 offen offset:1024 sc1\n" \
"v_mfma_f32_16x16x32_bf16 v[120:123], a[192:195], a[128:131], v[120:123]\n" \
"ds_read_b64_tr_b16 a[28:29], v34 offset:18496\n" \
"ds_read_b64_tr_b16 a[30:31], v34 offset:23104\n" \
"v_mfma_f32_16x16x32_bf16 v[124:127], a[192:195], a[144:147], v[124:127]\n" \
"v_cvt_pk_bf16_f32 v112, v112, v113\n" \
"v_cvt_pk_bf16_f32 v113, v114, v115\n" \
"v_mfma_f32_16x16x32_bf16 v[120:123], a[196:199], a[132:135], v[120:123]\n" \
"v_cvt_pk_bf16_f32 v114, v116, v117\n" \
"v_cvt_pk_bf16_f32 v115, v118, v119\n" \
"buffer_store_dwordx4 v[112:115], v20, s[56:59], s65 offen offset:0 sc1\n" \
"s_add_u32 s65, s65, 0x3800\n" \
"v_mfma_f32_16x16x32_bf16 v[124:127], a[196:199], a[148:151], v[124:127]\n" \
"ds_read_b64_tr_b16 a[192:193], v33 offset:64\n" \
"ds_read_b64_tr_b16 a[194:195], v33 offset:4672\n" \
"v_mfma_f32_16x16x32_bf16 v[120:123], a[200:203], a[136:139], v[120:123]\n" \
"ds_read_b64_tr_b16 a[196:197], v10 offset:27904\n" \
"ds_read_b64_tr_b16 a[198:199], v10 offset:32512\n" \
"v_mfma_f32_16x16x32_bf16 v[124:127], a[200:203], a[152:155], v[124:127]\n" \
"ds_read_b64_tr_b16 a[200:201], v10 offset:37120\n" \
"s_waitcnt lgkmcnt(7)\n" \
"v_mfma_f32_16x16x32_bf16 v[120:123], a[204:207], a[140:143], v[120:123]\n" \
"buffer_load_dwordx4 a[240:243], v32, s[56:59], s93 offen offset:1024 sc1\n" \
"v_mfma_f32_16x16x32_bf16 v[124:127], a[204:207], a[156:159], v[124:127]\n" \
"ds_read_b64_tr_b16 a[202:203], v10 offset:41728\n" \
"ds_read_b64_tr_b16 a[204:205], v10 offset:46336\n" \
"v_mfma_f32_16x16x32_bf16 v[120:123], a[208:211], a[32:35], v[120:123]\n" \
"ds_read_b64_tr_b16 a[206:207], v10 offset:50944\n" \
"v_mfma_f32_16x16x32_bf16 v[124:127], a[208:211], a[48:51], v[124:127]\n" \
"ds_read_b64_tr_b16 a[208:209], v10 offset:55552\n" \
"ds_read_b64_tr_b16 a[210:211], v10 offset:60160\n" \
"v_mfma_f32_16x16x32_bf16 v[120:123], a[212:215], a[36:39], v[120:123]\n" \
"v_mfma_f32_16x16x32_bf16 v[124:127], a[212:215], a[52:55], v[124:127]\n" \
"buffer_load_dwordx4 a[228:231], v20, s[56:59], s93 offen offset:2048 sc1\n" \
"v_mfma_f32_16x16x32_bf16 v[120:123], a[216:219], a[40:43], v[120:123]\n" \
"ds_read_b64_tr_b16 a[212:213], v33 offset:18688\n" \
"ds_read_b64_tr_b16 a[214:215], v33 offset:23296\n" \
"v_mfma_f32_16x16x32_bf16 v[124:127], a[216:219], a[56:59], v[124:127]\n" \
"ds_read_b64_tr_b16 a[216:217], v33 offset:27904\n" \
"ds_read_b64_tr_b16 a[218:219], v33 offset:32512\n" \
"v_mfma_f32_16x16x32_bf16 v[120:123], a[0:3], a[44:47], v[120:123]\n" \
"v_mfma_f32_16x16x32_bf16 v[124:127], a[0:3], a[60:63], v[124:127]\n" \
"ds_read_b64_tr_b16 a[0:1], v33 offset:37120\n" \
"ds_read_b64_tr_b16 a[2:3], v33 offset:41728\n" \
"buffer_load_dwordx4 a[244:247], v32, s[56:59], s93 offen offset:2048 sc1\n" \
"v_mfma_f32_16x16x32_bf16 v[128:131], a[4:7], a[128:131], v[128:131]\n" \
"v_mfma_f32_16x16x32_bf16 v[132:135], a[4:7], a[144:147], v[132:135]\n" \
"ds_read_b64_tr_b16 a[4:5], v33 offset:46336\n" \
"ds_read_b64_tr_b16 a[6:7], v33 offset:50944\n" \
"v_cvt_pk_bf16_f32 v120, v120, v121\n" \
"v_cvt_pk_bf16_f32 v121, v122, v123\n" \
"v_cvt_pk_bf16_f32 v122, v124, v125\n" \
"v_cvt_pk_bf16_f32 v123, v126, v127\n" \
"buffer_store_dwordx4 v[120:123], v20, s[56:59], s66 offen offset:0 sc1\n" \
"s_add_u32 s66, s66, 0x3000\n" \
"v_mfma_f32_16x16x32_bf16 v[128:131], a[8:11], a[132:135], v[128:131]\n" \
"v_mfma_f32_16x16x32_bf16 v[132:135], a[8:11], a[148:151], v[132:135]\n" \
"ds_read_b64_tr_b16 a[8:9], v34 offset:256\n" \
"ds_read_b64_tr_b16 a[10:11], v34 offset:4864\n" \
"v_mfma_f32_16x16x32_bf16 v[128:131], a[12:15], a[136:139], v[128:131]\n" \
"v_mfma_f32_16x16x32_bf16 v[132:135], a[12:15], a[152:155], v[132:135]\n" \
"buffer_load_dwordx4 a[232:235], v20, s[56:59], s93 offen offset:3072 sc1\n" \
"v_mfma_f32_16x16x32_bf16 v[128:131], a[16:19], a[140:143], v[128:131]\n" \
"ds_read_b64_tr_b16 a[12:13], v34 offset:9472\n" \
"ds_read_b64_tr_b16 a[14:15], v34 offset:14080\n" \
"v_mfma_f32_16x16x32_bf16 v[132:135], a[16:19], a[156:159], v[132:135]\n" \
"ds_read_b64_tr_b16 a[16:17], v34 offset:18688\n" \
"ds_read_b64_tr_b16 a[18:19], v34 offset:23296\n" \
"v_mfma_f32_16x16x32_bf16 v[128:131], a[20:23], a[32:35], v[128:131]\n" \
"v_mfma_f32_16x16x32_bf16 v[132:135], a[20:23], a[48:51], v[132:135]\n" \
"ds_read_b64_tr_b16 a[20:21], v33 offset:256\n" \
"ds_read_b64_tr_b16 a[22:23], v33 offset:4864\n" \
"buffer_load_dwordx4 a[248:251], v32, s[56:59], s93 offen offset:3072 sc1\n" \
"v_mfma_f32_16x16x32_bf16 v[128:131], a[24:27], a[36:39], v[128:131]\n" \
"v_readlane_b32 s93, v11, 45\n" \
"v_mfma_f32_16x16x32_bf16 v[132:135], a[24:27], a[52:55], v[132:135]\n" \
"ds_read_b64_tr_b16 a[24:25], v10 offset:27744\n" \
"s_waitcnt lgkmcnt(11)\n" \
"v_mfma_f32_16x16x32_bf16 v[128:131], a[28:31], a[40:43], v[128:131]\n" \
"ds_read_b64_tr_b16 a[26:27], v10 offset:32352\n" \
"v_mfma_f32_16x16x32_bf16 v[132:135], a[28:31], a[56:59], v[132:135]\n" \
"ds_read_b64_tr_b16 a[28:29], v10 offset:36960\n" \
"ds_read_b64_tr_b16 a[30:31], v10 offset:41568\n" \
"v_mfma_f32_16x16x32_bf16 v[128:131], a[192:195], a[44:47], v[128:131]\n" \
"buffer_load_dwordx4 v[64:67], v20, s[56:59], s95 offen offset:0 sc1\n" \
"v_mfma_f32_16x16x32_bf16 v[132:135], a[192:195], a[60:63], v[132:135]\n" \
"ds_read_b64_tr_b16 a[192:193], v10 offset:46176\n" \
"s_waitcnt vmcnt(26)\n" \
"v_mfma_f32_16x16x32_bf16 v[248:251], a[196:199], a[96:99], v[248:251]\n" \
"ds_read_b64_tr_b16 a[194:195], v10 offset:50784\n" \
"s_waitcnt vmcnt(25)\n" \
"v_mfma_f32_16x16x32_bf16 v[252:255], a[196:199], a[112:115], v[252:255]\n" \
"v_cvt_pk_bf16_f32 v128, v128, v129\n" \
"v_cvt_pk_bf16_f32 v129, v130, v131\n" \
"v_cvt_pk_bf16_f32 v130, v132, v133\n" \
"v_cvt_pk_bf16_f32 v131, v134, v135\n" \
"buffer_store_dwordx4 v[128:131], v20, s[56:59], s67 offen offset:0 sc1\n" \
"s_add_u32 s67, s67, 0x2800\n" \
"s_waitcnt vmcnt(25)\n" \
"v_mfma_f32_16x16x32_bf16 v[248:251], a[200:203], a[100:103], v[248:251]\n" \
"ds_read_b64_tr_b16 a[196:197], v10 offset:55392\n" \
"s_waitcnt vmcnt(24)\n" \
"v_mfma_f32_16x16x32_bf16 v[252:255], a[200:203], a[116:119], v[252:255]\n" \
"ds_read_b64_tr_b16 a[198:199], v10 offset:60000\n" \
"s_waitcnt vmcnt(23)\n" \
"v_mfma_f32_16x16x32_bf16 v[248:251], a[204:207], a[104:107], v[248:251]\n" \
"ds_read_b64_tr_b16 a[200:201], v33 offset:18528\n" \
"s_waitcnt vmcnt(22)\n" \
"v_mfma_f32_16x16x32_bf16 v[252:255], a[204:207], a[120:123], v[252:255]\n" \
"ds_read_b64_tr_b16 a[202:203], v33 offset:23136\n" \
"s_waitcnt vmcnt(21)\n" \
"v_mfma_f32_16x16x32_bf16 v[248:251], a[208:211], a[108:111], v[248:251]\n" \
"ds_read_b64_tr_b16 a[204:205], v33 offset:27744\n" \
"s_waitcnt vmcnt(20)\n" \
"v_mfma_f32_16x16x32_bf16 v[252:255], a[208:211], a[124:127], v[252:255]\n" \
"ds_read_b64_tr_b16 a[206:207], v33 offset:32352\n" \
"ds_read_b64_tr_b16 a[208:209], v33 offset:36960\n" \
"v_mfma_f32_16x16x32_bf16 v[176:179], a[212:215], a[128:131], v[176:179]\n" \
"buffer_load_dwordx4 v[72:75], v32, s[56:59], s95 offen offset:0 sc1\n" \
"v_mfma_f32_16x16x32_bf16 v[180:183], a[212:215], a[144:147], v[180:183]\n" \
"ds_read_b64_tr_b16 a[210:211], v33 offset:41568\n" \
"s_waitcnt vmcnt(20)\n" \
"v_mfma_f32_16x16x32_bf16 v[248:251], a[212:215], a[160:163], v[248:251]\n" \
"L_pf20:\n" \
"s_cbranch_execz L_retB\n" \
"s_nop 0\n" \
"s_waitcnt vmcnt(19)\n" \
"v_mfma_f32_16x16x32_bf16 v[252:255], a[212:215], a[176:179], v[252:255]\n" \
"ds_read_b64_tr_b16 a[212:213], v33 offset:46176\n" \
"ds_read_b64_tr_b16 a[214:215], v33 offset:50784\n" \
"v_mfma_f32_16x16x32_bf16 v[176:179], a[216:219], a[132:135], v[176:179]\n" \
"buffer_load_dwordx4 v[80:83], v20, s[56:59], s96 offen offset:0 sc1\n" \
"v_mfma_f32_16x16x32_bf16 v[180:183], a[216:219], a[148:151], v[180:183]\n" \
"s_waitcnt vmcnt(19)\n" \
"v_mfma_f32_16x16x32_bf16 v[248:251], a[216:219], a[164:167], v[248:251]\n" \
"s_waitcnt vmcnt(18)\n" \
"v_mfma_f32_16x16x32_bf16 v[252:255], a[216:219], a[180:183], v[252:255]\n" \
"ds_read_b64_tr_b16 a[216:217], v34 offset:96\n" \
"ds_read_b64_tr_b16 a[218:219], v34 offset:4704\n" \
"v_mfma_f32_16x16x32_bf16 v[176:179], a[0:3], a[136:139], v[176:179]\n" \
"buffer_load_dwordx4 v[88:91], v32, s[56:59], s96 offen offset:0 sc1\n" \
"v_mfma_f32_16x16x32_bf16 v[180:183], a[0:3], a[152:155], v[180:183]\n" \
"s_waitcnt vmcnt(18)\n" \
"v_mfma_f32_16x16x32_bf16 v[248:251], a[0:3], a[168:171], v[248:251]\n" \
"s_waitcnt vmcnt(17)\n" \
"v_mfma_f32_16x16x32_bf16 v[252:255], a[0:3], a[184:187], v[252:255]\n" \
"ds_read_b64_tr_b16 a[0:1], v34 offset:9312\n" \
"s_waitcnt lgkmcnt(3)\n" \
"v_mfma_f32_16x16x32_bf16 v[176:179], a[4:7], a[140:143], v[176:179]\n" \
"buffer_load_dwordx4 v[68:71], v20, s[56:59], s95 offen offset:1024 sc1\n" \
"v_mfma_f32_16x16x32_bf16 v[180:183], a[4:7], a[156:159], v[180:183]\n" \
"ds_read_b64_tr_b16 a[2:3], v34 offset:13920\n" \
"s_waitcnt vmcnt(17)\n" \
"v_mfma_f32_16x16x32_bf16 v[248:251], a[4:7], a[172:175], v[248:251]\n" \
"s_waitcnt vmcnt(16)\n" \
"v_mfma_f32_16x16x32_bf16 v[252:255], a[4:7], a[188:191], v[252:255]\n" \
"ds_read_b64_tr_b16 a[4:5], v34 offset:18528\n" \
"ds_read_b64_tr_b16 a[6:7], v34 offset:23136\n" \
"v_mfma_f32_16x16x32_bf16 v[176:179], a[8:11], a[32:35], v[176:179]\n" \
"buffer_load_dwordx4 v[76:79], v32, s[56:59], s95 offen offset:1024 sc1\n" \
"v_mfma_f32_16x16x32_bf16 v[180:183], a[8:11], a[48:51], v[180:183]\n" \
"s_waitcnt vmcnt(16)\n" \
"v_mfma_f32_16x16x32_bf16 v[248:251], a[8:11], a[220:223], v[248:251]\n" \
"s_waitcnt vmcnt(15)\n" \
"v_mfma_f32_16x16x32_bf16 v[252:255], a[8:11], a[236:239], v[252:255]\n" \
"ds_read_b64_tr_b16 a[8:9], v33 offset:96\n" \
"ds_read_b64_tr_b16 a[10:11], v33 offset:4704\n" \
"v_mfma_f32_16x16x32_bf16 v[176:179], a[12:15], a[36:39], v[176:179]\n" \
"buffer_load_dwordx4 v[84:87], v20, s[56:59], s96 offen offset:1024 sc1\n" \
"v_mfma_f32_16x16x32_bf16 v[180:183], a[12:15], a[52:55], v[180:183]\n" \
"s_waitcnt vmcnt(15)\n" \
"v_mfma_f32_16x16x32_bf16 v[248:251], a[12:15], a[224:227], v[248:251]\n" \
"s_waitcnt vmcnt(13)\n" \
"v_mfma_f32_16x16x32_bf16 v[252:255], a[12:15], a[240:243], v[252:255]\n" \
"ds_read_b64_tr_b16 a[12:13], v10 offset:27776\n" \
"ds_read_b64_tr_b16 a[14:15], v10 offset:32384\n" \
"s_mov_b64 exec, 1\n" \
"buffer_store_dword v39, off, s[56:59], s65 offset:0 sc1\n" \
"s_mov_b64 exec, -1\n" \
"v_mfma_f32_16x16x32_bf16 v[176:179], a[16:19], a[40:43], v[176:179]\n" \
"v_mfma_f32_16x16x32_bf16 v[180:183], a[16:19], a[56:59], v[180:183]\n" \
"s_waitcnt vmcnt(13)\n" \
"v_mfma_f32_16x16x32_bf16 v[248:251], a[16:19], a[228:231], v[248:251]\n" \
"buffer_load_dwordx4 v[92:95], v32, s[56:59], s96 offen offset:1024 sc1\n" \
"s_waitcnt vmcnt(13)\n" \
"v_mfma_f32_16x16x32_bf16 v[252:255], a[16:19], a[244:247], v[252:255]\n" \
"ds_read_b64_tr_b16 a[16:17], v10 offset:36992\n" \
"ds_read_b64_tr_b16 a[18:19], v10 offset:41600\n" \
"v_mfma_f32_16x16x32_bf16 v[176:179], a[20:23], a[44:47], v[176:179]\n" \
"buffer_load_dwordx4 v[96:99], v20, s[56:59], s97 offen offset:0 sc1\n" \
"v_mfma_f32_16x16x32_bf16 v[180:183], a[20:23], a[60:63], v[180:183]\n" \
"s_waitcnt vmcnt(12)\n" \
"v_mfma_f32_16x16x32_bf16 v[248:251], a[20:23], a[232:235], v[248:251]\n" \
"s_mov_b64 exec, 1\n" \
"buffer_store_dword v39, off, s[56:59], s66 offset:4 sc1\n" \
"s_mov_b64 exec, -1\n" \
"s_waitcnt vmcnt(12)\n" \
"v_mfma_f32_16x16x32_bf16 v[252:255], a[20:23], a[248:251], v[252:255]\n" \
"v_cvt_pk_bf16_f32 v176, v176, v177\n" \
"v_cvt_pk_bf16_f32 v177, v178, v179\n" \
"v_cvt_pk_bf16_f32 v178, v180, v181\n" \
"v_cvt_pk_bf16_f32 v248, v248, v249\n" \
"v_cvt_pk_bf16_f32 v249, v250, v251\n" \
"v_cvt_pk_bf16_f32 v179, v182, v183\n" \
"buffer_store_dwordx4 v[176:179], v20, s[56:59], s68 offen offset:0 sc1\n" \
"v_mfma_f32_16x16x32_bf16 v[208:211], a[24:27], a[96:99], v[208:211]\n" \
"v_cvt_pk_bf16_f32 v250, v252, v253\n" \
"v_cvt_pk_bf16_f32 v251, v254, v255\n" \
"v_mfma_f32_16x16x32_bf16 v[212:215], a[24:27], a[112:115], v[212:215]\n" \
"ds_read_b64_tr_b16 a[20:21], v10 offset:46208\n" \
"ds_read_b64_tr_b16 a[22:23], v10 offset:50816\n" \
"buffer_store_dwordx4 v[248:251], v20, s[56:59], s68 offen offset:1024 sc1\n" \
"v_mfma_f32_16x16x32_bf16 v[208:211], a[28:31], a[100:103], v[208:211]\n" \
"ds_read_b64_tr_b16 a[24:25], v10 offset:55424\n" \
"ds_read_b64_tr_b16 a[26:27], v10 offset:60032\n" \
"v_mfma_f32_16x16x32_bf16 v[212:215], a[28:31], a[116:119], v[212:215]\n" \
"ds_read_b64_tr_b16 a[28:29], v33 offset:18560\n" \
"ds_read_b64_tr_b16 a[30:31], v33 offset:23168\n" \
"v_mfma_f32_16x16x32_bf16 v[208:211], a[192:195], a[104:107], v[208:211]\n" \
"v_mfma_f32_16x16x32_bf16 v[212:215], a[192:195], a[120:123], v[212:215]\n" \
"ds_read_b64_tr_b16 a[192:193], v33 offset:27776\n" \
"ds_read_b64_tr_b16 a[194:195], v33 offset:32384\n" \
"s_waitcnt vmcnt(12)\n" \
"s_mov_b64 exec, 1\n" \
"buffer_store_dword v39, off, s[56:59], s67 offset:8 sc1\n" \
"s_mov_b64 exec, -1\n" \
"v_mfma_f32_16x16x32_bf16 v[208:211], a[196:199], a[108:111], v[208:211]\n" \
"v_mfma_f32_16x16x32_bf16 v[212:215], a[196:199], a[124:127], v[212:215]\n" \
"ds_read_b64_tr_b16 a[196:197], v33 offset:36992\n" \
"ds_read_b64_tr_b16 a[198:199], v33 offset:41600\n" \
"v_mfma_f32_16x16x32_bf16 v[136:139], a[200:203], a[128:131], v[136:139]\n" \
"v_mfma_f32_16x16x32_bf16 v[140:143], a[200:203], a[144:147], v[140:143]\n" \
"buffer_load_dwordx4 v[104:107], v32, s[56:59], s97 offen offset:0 sc1\n" \
"v_mfma_f32_16x16x32_bf16 v[208:211], a[200:203], a[160:163], v[208:211]\n" \
"v_mfma_f32_16x16x32_bf16 v[212:215], a[200:203], a[176:179], v[212:215]\n" \
"ds_read_b64_tr_b16 a[200:201], v33 offset:46208\n" \
"ds_read_b64_tr_b16 a[202:203], v33 offset:50816\n" \
"v_mfma_f32_16x16x32_bf16 v[136:139], a[204:207], a[132:135], v[136:139]\n" \
"v_mfma_f32_16x16x32_bf16 v[140:143], a[204:207], a[148:151], v[140:143]\n" \
"v_mfma_f32_16x16x32_bf16 v[208:211], a[204:207], a[164:167], v[208:211]\n" \
"buffer_load_dwordx4 v[100:103], v20, s[56:59], s97 offen offset:1024 sc1\n" \
"v_mfma_f32_16x16x32_bf16 v[212:215], a[204:207], a[180:183], v[212:215]\n" \
"ds_read_b64_tr_b16 a[204:205], v34 offset:128\n" \
"ds_read_b64_tr_b16 a[206:207], v34 offset:4736\n" \
"v_mfma_f32_16x16x32_bf16 v[136:139], a[208:211], a[136:139], v[136:139]\n" \
"v_mfma_f32_16x16x32_bf16 v[140:143], a[208:211], a[152:155], v[140:143]\n" \
"v_mfma_f32_16x16x32_bf16 v[208:211], a[208:211], a[168:171], v[208:211]\n" \
"v_mfma_f32_16x16x32_bf16 v[212:215], a[208:211], a[184:187], v[212:215]\n" \
"ds_read_b64_tr_b16 a[208:209], v34 offset:9344\n" \
"ds_read_b64_tr_b16 a[210:211], v34 offset:13952\n" \
"v_mfma_f32_16x16x32_bf16 v[136:139], a[212:215], a[140:143], v[136:139]\n" \
"buffer_load_dwordx4 v[108:111], v32, s[56:59], s97 offen offset:1024 sc1\n" \
"v_mfma_f32_16x16x32_bf16 v[140:143], a[212:215], a[156:159], v[140:143]\n" \
"v_mfma_f32_16x16x32_bf16 v[208:211], a[212:215], a[172:175], v[208:211]\n" \
"v_mfma_f32_16x16x32_bf16 v[212:215], a[212:215], a[188:191], v[212:215]\n" \
"ds_read_b64_tr_b16 a[212:213], v34 offset:18560\n" \
"s_waitcnt lgkmcnt(7)\n" \
"v_mfma_f32_16x16x32_bf16 v[136:139], a[216:219], a[32:35], v[136:139]\n" \
"ds_read_b64_tr_b16 a[214:215], v34 offset:23168\n" \
"v_mfma_f32_16x16x32_bf16 v[140:143], a[216:219], a[48:51], v[140:143]\n" \
"v_mfma_f32_16x16x32_bf16 v[208:211], a[216:219], a[220:223], v[208:211]\n" \
"v_mfma_f32_16x16x32_bf16 v[212:215], a[216:219], a[236:239], v[212:215]\n" \
"ds_read_b64_tr_b16 a[216:217], v33 offset:128\n" \
"ds_read_b64_tr_b16 a[218:219], v33 offset:4736\n" \
"v_mfma_f32_16x16x32_bf16 v[136:139], a[0:3], a[36:39], v[136:139]\n" \
"v_mfma_f32_16x16x32_bf16 v[140:143], a[0:3], a[52:55], v[140:143]\n" \
"v_mfma_f32_16x16x32_bf16 v[208:211], a[0:3], a[224:227], v[208:211]\n" \
"v_mfma_f32_16x16x32_bf16 v[212:215], a[0:3], a[240:243], v[212:215]\n" \
"ds_read_b64_tr_b16 a[0:1], v10 offset:27808\n" \
"ds_read_b64_tr_b16 a[2:3], v10 offset:32416\n" \
"v_mfma_f32_16x16x32_bf16 v[136:139], a[4:7], a[40:43], v[136:139]\n" \
"v_mfma_f32_16x16x32_bf16 v[140:143], a[4:7], a[56:59], v[140:143]\n" \
"v_mfma_f32_16x16x32_bf16 v[208:211], a[4:7], a[228:231], v[208:211]\n" \
"v_mfma_f32_16x16x32_bf16 v[212:215], a[4:7], a[244:247], v[212:215]\n" \
"ds_read_b64_tr_b16 a[4:5], v10 offset:37024\n" \
"ds_read_b64_tr_b16 a[6:7], v10 offset:41632\n" \
"v_mfma_f32_16x16x32_bf16 v[136:139], a[8:11], a[44:47], v[136:139]\n" \
"v_mfma_f32_16x16x32_bf16 v[140:143], a[8:11], a[60:63], v[140:143]\n" \
"v_mfma_f32_16x16x32_bf16 v[208:211], a[8:11], a[232:235], v[208:211]\n" \
"v_mfma_f32_16x16x32_bf16 v[212:215], a[8:11], a[248:251], v[212:215]\n" \
"ds_read_b64_tr_b16 a[8:9], v10 offset:46240\n" \
"ds_read_b64_tr_b16 a[10:11], v10 offset:50848\n" \
"v_mfma_f32_16x16x32_bf16 v[216:219], a[12:15], a[96:99], v[216:219]\n" \
"v_mfma_f32_16x16x32_bf16 v[220:223], a[12:15], a[112:115], v[220:223]\n" \
"ds_read_b64_tr_b16 a[12:13], v10 offset:55456\n" \
"v_cvt_pk_bf16_f32 v136, v136, v137\n" \
"v_cvt_pk_bf16_f32 v137, v138, v139\n" \
"v_cvt_pk_bf16_f32 v208, v208, v209\n" \
"v_cvt_pk_bf16_f32 v209, v210, v211\n" \
"v_cvt_pk_bf16_f32 v138, v140, v141\n" \
"v_cvt_pk_bf16_f32 v139, v142, v143\n" \
"v_cvt_pk_bf16_f32 v210, v212, v213\n" \
"v_cvt_pk_bf16_f32 v211, v214, v215\n" \
"buffer_store_dwordx4 v[136:139], v20, s[56:59], s70 offen offset:0 sc1\n" \
"v_mfma_f32_16x16x32_bf16 v[216:219], a[16:19], a[100:103], v[216:219]\n" \
"ds_read_b64_tr_b16 a[14:15], v10 offset:60064\n" \
"v_mfma_f32_16x16x32_bf16 v[220:223], a[16:19], a[116:119], v[220:223]\n" \
"ds_read_b64_tr_b16 a[16:17], v33 offset:18592\n" \
"ds_read_b64_tr_b16 a[18:19], v33 offset:23200\n" \
"buffer_store_dwordx4 v[208:211], v20, s[56:59], s70 offen offset:1024 sc1\n" \
"s_add_u32 s70, s70, 0x2000\n" \
"v_mfma_f32_16x16x32_bf16 v[216:219], a[20:23], a[104:107], v[216:219]\n" \
"v_mfma_f32_16x16x32_bf16 v[220:223], a[20:23], a[120:123], v[220:223]\n" \
"ds_read_b64_tr_b16 a[20:21], v33 offset:27808\n" \
"ds_read_b64_tr_b16 a[22:23], v33 offset:32416\n" \
"v_mfma_f32_16x16x32_bf16 v[216:219], a[24:27], a[108:111], v[216:219]\n" \
"v_mfma_f32_16x16x32_bf16 v[220:223], a[24:27], a[124:127], v[220:223]\n" \
"ds_read_b64_tr_b16 a[24:25], v33 offset:37024\n" \
"ds_read_b64_tr_b16 a[26:27], v33 offset:41632\n" \
"v_mfma_f32_16x16x32_bf16 v[144:147], a[28:31], a[128:131], v[144:147]\n" \
"v_mfma_f32_16x16x32_bf16 v[148:151], a[28:31], a[144:147], v[148:151]\n" \
"v_mfma_f32_16x16x32_bf16 v[216:219], a[28:31], a[160:163], v[216:219]\n" \
"v_mfma_f32_16x16x32_bf16 v[220:223], a[28:31], a[176:179], v[220:223]\n" \
"ds_read_b64_tr_b16 a[28:29], v33 offset:46240\n" \
"ds_read_b64_tr_b16 a[30:31], v33 offset:50848\n" \
"v_mfma_f32_16x16x32_bf16 v[144:147], a[192:195], a[132:135], v[144:147]\n" \
"v_mfma_f32_16x16x32_bf16 v[148:151], a[192:195], a[148:151], v[148:151]\n" \
"v_mfma_f32_16x16x32_bf16 v[216:219], a[192:195], a[164:167], v[216:219]\n" \
"v_mfma_f32_16x16x32_bf16 v[220:223], a[192:195], a[180:183], v[220:223]\n" \
"ds_read_b64_tr_b16 a[192:193], v34 offset:160\n" \
"ds_read_b64_tr_b16 a[194:195], v34 offset:4768\n" \
"v_mfma_f32_16x16x32_bf16 v[144:147], a[196:199], a[136:139], v[144:147]\n" \
"v_mfma_f32_16x16x32_bf16 v[148:151], a[196:199], a[152:155], v[148:151]\n" \
"v_mfma_f32_16x16x32_bf16 v[216:219], a[196:199], a[168:171], v[216:219]\n" \
"v_mfma_f32_16x16x32_bf16 v[220:223], a[196:199], a[184:187], v[220:223]\n" \
"ds_read_b64_tr_b16 a[196:197], v34 offset:9376\n" \
"s_waitcnt lgkmcnt(7)\n" \
"v_mfma_f32_16x16x32_bf16 v[144:147], a[200:203], a[140:143], v[144:147]\n" \
"ds_read_b64_tr_b16 a[198:199], v34 offset:13984\n" \
"v_mfma_f32_16x16x32_bf16 v[148:151], a[200:203], a[156:159], v[148:151]\n" \
"v_mfma_f32_16x16x32_bf16 v[216:219], a[200:203], a[172:175], v[216:219]\n" \
"v_mfma_f32_16x16x32_bf16 v[220:223], a[200:203], a[188:191], v[220:223]\n" \
"ds_read_b64_tr_b16 a[200:201], v34 offset:18592\n" \
"ds_read_b64_tr_b16 a[202:203], v34 offset:23200\n" \
"v_mfma_f32_16x16x32_bf16 v[144:147], a[204:207], a[32:35], v[144:147]\n" \
"v_mfma_f32_16x16x32_bf16 v[148:151], a[204:207], a[48:51], v[148:151]\n" \
"v_mfma_f32_16x16x32_bf16 v[216:219], a[204:207], a[220:223], v[216:219]\n" \
"v_mfma_f32_16x16x32_bf16 v[220:223], a[204:207], a[236:239], v[220:223]\n" \
"ds_read_b64_tr_b16 a[204:205], v33 offset:160\n" \
"ds_read_b64_tr_b16 a[206:207], v33 offset:4768\n" \
"v_mfma_f32_16x16x32_bf16 v[144:147], a[208:211], a[36:39], v[144:147]\n" \
"v_mfma_f32_16x16x32_bf16 v[148:151], a[208:211], a[52:55], v[148:151]\n" \
"v_mfma_f32_16x16x32_bf16 v[216:219], a[208:211], a[224:227], v[216:219]\n" \
"v_mfma_f32_16x16x32_bf16 v[220:223], a[208:211], a[240:243], v[220:223]\n" \
"ds_read_b64_tr_b16 a[208:209], v10 offset:27840\n" \
"ds_read_b64_tr_b16 a[210:211], v10 offset:32448\n" \
"v_mfma_f32_16x16x32_bf16 v[144:147], a[212:215], a[40:43], v[144:147]\n" \
"v_mfma_f32_16x16x32_bf16 v[148:151], a[212:215], a[56:59], v[148:151]\n" \
"v_mfma_f32_16x16x32_bf16 v[216:219], a[212:215], a[228:231], v[216:219]\n" \
"v_mfma_f32_16x16x32_bf16 v[220:223], a[212:215], a[244:247], v[220:223]\n" \
"ds_read_b64_tr_b16 a[212:213], v10 offset:37056\n" \
"ds_read_b64_tr_b16 a[214:215], v10 offset:41664\n" \
"v_mfma_f32_16x16x32_bf16 v[144:147], a[216:219], a[44:47], v[144:147]\n" \
"v_mfma_f32_16x16x32_bf16 v[148:151], a[216:219], a[60:63], v[148:151]\n" \
"v_mfma_f32_16x16x32_bf16 v[216:219], a[216:219], a[232:235], v[216:219]\n" \
"v_mfma_f32_16x16x32_bf16 v[220:223], a[216:219], a[248:251], v[220:223]\n" \
"ds_read_b64_tr_b16 a[216:217], v10 offset:46272\n" \
"ds_read_b64_tr_b16 a[218:219], v10 offset:50880\n" \
"v_mfma_f32_16x16x32_bf16 v[224:227], a[0:3], a[96:99], v[224:227]\n" \
"v_mfma_f32_16x16x32_bf16 v[228:231], a[0:3], a[112:115], v[228:231]\n" \
"ds_read_b64_tr_b16 a[0:1], v10 offset:55488\n" \
"v_cvt_pk_bf16_f32 v144, v144, v145\n" \
"v_cvt_pk_bf16_f32 v145, v146, v147\n" \
"v_cvt_pk_bf16_f32 v216, v216, v217\n" \
"v_cvt_pk_bf16_f32 v217, v218, v219\n" \
"v_cvt_pk_bf16_f32 v146, v148, v149\n" \
"v_cvt_pk_bf16_f32 v147, v150, v151\n" \
"v_cvt_pk_bf16_f32 v218, v220, v221\n" \
"v_cvt_pk_bf16_f32 v219, v222, v223\n" \
"buffer_store_dwordx4 v[144:147], v20, s[56:59], s71 offen offset:0 sc1\n" \
"v_mfma_f32_16x16x32_bf16 v[224:227], a[4:7], a[100:103], v[224:227]\n" \
"ds_read_b64_tr_b16 a[2:3], v10 offset:60096\n" \
"v_mfma_f32_16x16x32_bf16 v[228:231], a[4:7], a[116:119], v[228:231]\n" \
"ds_read_b64_tr_b16 a[4:5], v33 offset:18624\n" \
"ds_read_b64_tr_b16 a[6:7], v33 offset:23232\n" \
"buffer_store_dwordx4 v[216:219], v20, s[56:59], s71 offen offset:1024 sc1\n" \
"s_add_u32 s71, s71, 0x1800\n" \
"v_mfma_f32_16x16x32_bf16 v[224:227], a[8:11], a[104:107], v[224:227]\n" \
"v_mfma_f32_16x16x32_bf16 v[228:231], a[8:11], a[120:123], v[228:231]\n" \
"ds_read_b64_tr_b16 a[8:9], v33 offset:27840\n" \
"ds_read_b64_tr_b16 a[10:11], v33 offset:32448\n" \
"v_mfma_f32_16x16x32_bf16 v[224:227], a[12:15], a[108:111], v[224:227]\n" \
"v_mfma_f32_16x16x32_bf16 v[228:231], a[12:15], a[124:127], v[228:231]\n" \
"ds_read_b64_tr_b16 a[12:13], v33 offset:37056\n" \
"ds_read_b64_tr_b16 a[14:15], v33 offset:41664\n" \
"v_mfma_f32_16x16x32_bf16 v[152:155], a[16:19], a[128:131], v[152:155]\n" \
"s_waitcnt vmcnt(8)\n" \
"s_mov_b64 exec, 3\n" \
"buffer_store_dword v39, v41, s[56:59], s69 offen sc1\n" \
"s_mov_b64 exec, -1\n" \
"v_mfma_f32_16x16x32_bf16 v[156:159], a[16:19], a[144:147], v[156:159]\n" \
"v_mfma_f32_16x16x32_bf16 v[224:227], a[16:19], a[160:163], v[224:227]\n" \
"v_mfma_f32_16x16x32_bf16 v[228:231], a[16:19], a[176:179], v[228:231]\n" \
"ds_read_b64_tr_b16 a[16:17], v33 offset:46272\n" \
"ds_read_b64_tr_b16 a[18:19], v33 offset:50880\n" \
"v_mfma_f32_16x16x32_bf16 v[152:155], a[20:23], a[132:135], v[152:155]\n" \
"v_mfma_f32_16x16x32_bf16 v[156:159], a[20:23], a[148:151], v[156:159]\n" \
"v_mfma_f32_16x16x32_bf16 v[224:227], a[20:23], a[164:167], v[224:227]\n" \
"v_mfma_f32_16x16x32_bf16 v[228:231], a[20:23], a[180:183], v[228:231]\n" \
"ds_read_b64_tr_b16 a[20:21], v34 offset:192\n" \
"s_waitcnt lgkmcnt(9)\n" \
"v_mfma_f32_16x16x32_bf16 v[152:155], a[24:27], a[136:139], v[152:155]\n" \
"ds_read_b64_tr_b16 a[22:23], v34 offset:4800\n" \
"v_mfma_f32_16x16x32_bf16 v[156:159], a[24:27], a[152:155], v[156:159]\n" \
"v_mfma_f32_16x16x32_bf16 v[224:227], a[24:27], a[168:171], v[224:227]\n" \
"v_mfma_f32_16x16x32_bf16 v[228:231], a[24:27], a[184:187], v[228:231]\n" \
"ds_read_b64_tr_b16 a[24:25], v34 offset:9408\n" \
"ds_read_b64_tr_b16 a[26:27], v34 offset:14016\n" \
"v_mfma_f32_16x16x32_bf16 v[152:155], a[28:31], a[140:143], v[152:155]\n" \
"v_mfma_f32_16x16x32_bf16 v[156:159], a[28:31], a[156:159], v[156:159]\n" \
"v_mfma_f32_16x16x32_bf16 v[224:227], a[28:31], a[172:175], v[224:227]\n" \
"v_mfma_f32_16x16x32_bf16 v[228:231], a[28:31], a[188:191], v[228:231]\n" \
"ds_read_b64_tr_b16 a[28:29], v34 offset:18624\n" \
"ds_read_b64_tr_b16 a[30:31], v34 offset:23232\n" \
"v_mfma_f32_16x16x32_bf16 v[152:155], a[192:195], a[32:35], v[152:155]\n" \
"v_mfma_f32_16x16x32_bf16 v[156:159], a[192:195], a[48:51], v[156:159]\n" \
"v_mfma_f32_16x16x32_bf16 v[224:227], a[192:195], a[220:223], v[224:227]\n" \
"v_mfma_f32_16x16x32_bf16 v[228:231], a[192:195], a[236:239], v[228:231]\n" \
"ds_read_b64_tr_b16 a[192:193], v33 offset:192\n" \
"ds_read_b64_tr_b16 a[194:195], v33 offset:4800\n" \
"v_mfma_f32_16x16x32_bf16 v[152:155], a[196:199], a[36:39], v[152:155]\n" \
"v_mfma_f32_16x16x32_bf16 v[156:159], a[196:199], a[52:55], v[156:159]\n" \
"v_mfma_f32_16x16x32_bf16 v[224:227], a[196:199], a[224:227], v[224:227]\n" \
"v_mfma_f32_16x16x32_bf16 v[228:231], a[196:199], a[240:243], v[228:231]\n" \
"ds_read_b64_tr_b16 a[196:197], v10 offset:27648\n" \
"ds_read_b64_tr_b16 a[198:199], v10 offset:32256\n" \
"v_mfma_f32_16x16x32_bf16 v[152:155], a[200:203], a[40:43], v[152:155]\n" \
"v_mfma_f32_16x16x32_bf16 v[156:159], a[200:203], a[56:59], v[156:159]\n" \
"v_mfma_f32_16x16x32_bf16 v[224:227], a[200:203], a[228:231], v[224:227]\n" \
"v_mfma_f32_16x16x32_bf16 v[228:231], a[200:203], a[244:247], v[228:231]\n" \
"ds_read_b64_tr_b16 a[200:201], v10 offset:36864\n" \
"ds_read_b64_tr_b16 a[202:203], v10 offset:41472\n" \
"v_mfma_f32_16x16x32_bf16 v[152:155], a[204:207], a[44:47], v[152:155]\n" \
"v_mfma_f32_16x16x32_bf16 v[156:159], a[204:207], a[60:63], v[156:159]\n" \
"v_mfma_f32_16x16x32_bf16 v[224:227], a[204:207], a[232:235], v[224:227]\n" \
"v_mfma_f32_16x16x32_bf16 v[228:231], a[204:207], a[248:251], v[228:231]\n" \
"ds_read_b64_tr_b16 a[204:205], v10 offset:46080\n" \
"ds_read_b64_tr_b16 a[206:207], v10 offset:50688\n" \
"v_mfma_f32_16x16x32_bf16 v[232:235], a[208:211], a[96:99], v[232:235]\n" \
"v_mfma_f32_16x16x32_bf16 v[236:239], a[208:211], a[112:115], v[236:239]\n" \
"ds_read_b64_tr_b16 a[208:209], v10 offset:55296\n" \
"v_cvt_pk_bf16_f32 v152, v152, v153\n" \
"v_cvt_pk_bf16_f32 v153, v154, v155\n" \
"v_cvt_pk_bf16_f32 v224, v224, v225\n" \
"v_cvt_pk_bf16_f32 v225, v226, v227\n" \
"v_cvt_pk_bf16_f32 v154, v156, v157\n" \
"v_cvt_pk_bf16_f32 v155, v158, v159\n" \
"v_cvt_pk_bf16_f32 v226, v228, v229\n" \
"v_cvt_pk_bf16_f32 v227, v230, v231\n" \
"buffer_store_dwordx4 v[152:155], v20, s[56:59], s72 offen offset:0 sc1\n" \
"v_mfma_f32_16x16x32_bf16 v[232:235], a[212:215], a[100:103], v[232:235]\n" \
"ds_read_b64_tr_b16 a[210:211], v10 offset:59904\n" \
"v_mfma_f32_16x16x32_bf16 v[236:239], a[212:215], a[116:119], v[236:239]\n" \
"ds_read_b64_tr_b16 a[212:213], v33 offset:18432\n" \
"ds_read_b64_tr_b16 a[214:215], v33 offset:23040\n" \
"buffer_store_dwordx4 v[224:227], v20, s[56:59], s72 offen offset:1024 sc1\n" \
"s_add_u32 s72, s72, 0x1000\n" \
"v_mfma_f32_16x16x32_bf16 v[232:235], a[216:219], a[104:107], v[232:235]\n" \
"v_mfma_f32_16x16x32_bf16 v[236:239], a[216:219], a[120:123], v[236:239]\n" \
"ds_read_b64_tr_b16 a[216:217], v33 offset:27648\n" \
"ds_read_b64_tr_b16 a[218:219], v33 offset:32256\n" \
"v_mfma_f32_16x16x32_bf16 v[232:235], a[0:3], a[108:111], v[232:235]\n" \
"v_mfma_f32_16x16x32_bf16 v[236:239], a[0:3], a[124:127], v[236:239]\n" \
"ds_read_b64_tr_b16 a[0:1], v33 offset:36864\n" \
"s_waitcnt lgkmcnt(9)\n" \
"v_mfma_f32_16x16x32_bf16 v[160:163], a[4:7], a[128:131], v[160:163]\n" \
"ds_read_b64_tr_b16 a[2:3], v33 offset:41472\n" \
"v_mfma_f32_16x16x32_bf16 v[164:167], a[4:7], a[144:147], v[164:167]\n" \
"v_mfma_f32_16x16x32_bf16 v[232:235], a[4:7], a[160:163], v[232:235]\n" \
"v_mfma_f32_16x16x32_bf16 v[236:239], a[4:7], a[176:179], v[236:239]\n" \
"ds_read_b64_tr_b16 a[4:5], v33 offset:46080\n" \
"ds_read_b64_tr_b16 a[6:7], v33 offset:50688\n" \
"s_waitcnt vmcnt(5)\n" \
"s_mov_b64 exec, 1\n" \
"buffer_store_dword v39, off, s[56:59], s70 offset:16 sc1\n" \
"s_mov_b64 exec, -1\n" \
"v_mfma_f32_16x16x32_bf16 v[160:163], a[8:11], a[132:135], v[160:163]\n" \
"v_mfma_f32_16x16x32_bf16 v[164:167], a[8:11], a[148:151], v[164:167]\n" \
"v_mfma_f32_16x16x32_bf16 v[232:235], a[8:11], a[164:167], v[232:235]\n" \
"v_mfma_f32_16x16x32_bf16 v[236:239], a[8:11], a[180:183], v[236:239]\n" \
"ds_read_b64_tr_b16 a[8:9], v34 offset:0\n" \
"ds_read_b64_tr_b16 a[10:11], v34 offset:4608\n" \
"v_mfma_f32_16x16x32_bf16 v[160:163], a[12:15], a[136:139], v[160:163]\n" \
"v_mfma_f32_16x16x32_bf16 v[164:167], a[12:15], a[152:155], v[164:167]\n" \
"v_mfma_f32_16x16x32_bf16 v[232:235], a[12:15], a[168:171], v[232:235]\n" \
"v_mfma_f32_16x16x32_bf16 v[236:239], a[12:15], a[184:187], v[236:239]\n" \
"ds_read_b64_tr_b16 a[12:13], v34 offset:9216\n" \
"ds_read_b64_tr_b16 a[14:15], v34 offset:13824\n" \
"v_mfma_f32_16x16x32_bf16 v[160:163], a[16:19], a[140:143], v[160:163]\n" \
"v_mfma_f32_16x16x32_bf16 v[164:167], a[16:19], a[156:159], v[164:167]\n" \
"v_mfma_f32_16x16x32_bf16 v[232:235], a[16:19], a[172:175], v[232:235]\n" \
"v_mfma_f32_16x16x32_bf16 v[236:239], a[16:19], a[188:191], v[236:239]\n" \
"ds_read_b64_tr_b16 a[16:17], v34 offset:18432\n" \
"ds_read_b64_tr_b16 a[18:19], v34 offset:23040\n" \
"v_mfma_f32_16x16x32_bf16 v[160:163], a[20:23], a[32:35], v[160:163]\n" \
"v_mfma_f32_16x16x32_bf16 v[164:167], a[20:23], a[48:51], v[164:167]\n" \
"v_mfma_f32_16x16x32_bf16 v[232:235], a[20:23], a[220:223], v[232:235]\n" \
"v_mfma_f32_16x16x32_bf16 v[236:239], a[20:23], a[236:239], v[236:239]\n" \
"ds_read_b64_tr_b16 a[20:21], v33 offset:0\n" \
"ds_read_b64_tr_b16 a[22:23], v33 offset:4608\n" \
"v_mfma_f32_16x16x32_bf16 v[160:163], a[24:27], a[36:39], v[160:163]\n" \
"v_mfma_f32_16x16x32_bf16 v[164:167], a[24:27], a[52:55], v[164:167]\n" \
"v_mfma_f32_16x16x32_bf16 v[232:235], a[24:27], a[224:227], v[232:235]\n" \
"v_mfma_f32_16x16x32_bf16 v[236:239], a[24:27], a[240:243], v[236:239]\n" \
"ds_read_b64_tr_b16 a[24:25], v10 offset:27680\n" \
"ds_read_b64_tr_b16 a[26:27], v10 offset:32288\n" \
"v_mfma_f32_16x16x32_bf16 v[160:163], a[28:31], a[40:43], v[160:163]\n" \
"v_mfma_f32_16x16x32_bf16 v[164:167], a[28:31], a[56:59], v[164:167]\n" \
"v_mfma_f32_16x16x32_bf16 v[232:235], a[28:31], a[228:231], v[232:235]\n" \
"v_mfma_f32_16x16x32_bf16 v[236:239], a[28:31], a[244:247], v[236:239]\n" \
"ds_read_b64_tr_b16 a[28:29], v10 offset:36896\n" \
"ds_read_b64_tr_b16 a[30:31], v10 offset:41504\n" \
"v_mfma_f32_16x16x32_bf16 v[160:163], a[192:195], a[44:47], v[160:163]\n" \
"v_mfma_f32_16x16x32_bf16 v[164:167], a[192:195], a[60:63], v[164:167]\n" \
"v_mfma_f32_16x16x32_bf16 v[232:235], a[192:195], a[232:235], v[232:235]\n" \
"v_mfma_f32_16x16x32_bf16 v[236:239], a[192:195], a[248:251], v[236:239]\n" \
"ds_read_b64_tr_b16 a[192:193], v10 offset:46112\n" \
"ds_read_b64_tr_b16 a[194:195], v10 offset:50720\n" \
"v_mfma_f32_16x16x32_bf16 v[184:187], a[196:199], a[96:99], v[184:187]\n" \
"v_mfma_f32_16x16x32_bf16 v[188:191], a[196:199], a[112:115], v[188:191]\n" \
"ds_read_b64_tr_b16 a[196:197], v10 offset:55328\n" \
"v_cvt_pk_bf16_f32 v160, v160, v161\n" \
"v_cvt_pk_bf16_f32 v161, v162, v163\n" \
"v_cvt_pk_bf16_f32 v232, v232, v233\n" \
"v_cvt_pk_bf16_f32 v233, v234, v235\n" \
"v_cvt_pk_bf16_f32 v162, v164, v165\n" \
"v_cvt_pk_bf16_f32 v163, v166, v167\n" \
"v_cvt_pk_bf16_f32 v234, v236, v237\n" \
"v_cvt_pk_bf16_f32 v235, v238, v239\n" \
"buffer_store_dwordx4 v[160:163], v20, s[56:59], s73 offen offset:0 sc1\n" \
"v_mfma_f32_16x16x32_bf16 v[184:187], a[200:203], a[100:103], v[184:187]\n" \
"ds_read_b64_tr_b16 a[198:199], v10 offset:59936\n" \
"v_mfma_f32_16x16x32_bf16 v[188:191], a[200:203], a[116:119], v[188:191]\n" \
"ds_read_b64_tr_b16 a[200:201], v33 offset:18464\n" \
"s_waitcnt lgkmcnt(7)\n" \
"v_mfma_f32_16x16x32_bf16 v[184:187], a[204:207], a[104:107], v[184:187]\n" \
"buffer_store_dwordx4 v[232:235], v20, s[56:59], s73 offen offset:1024 sc1\n" \
"s_add_u32 s73, s73, 0x800\n" \
"v_mfma_f32_16x16x32_bf16 v[188:191], a[204:207], a[120:123], v[188:191]\n" \
"ds_read_b64_tr_b16 a[202:203], v33 offset:23072\n" \
"ds_read_b64_tr_b16 a[204:205], v33 offset:27680\n" \
"v_mfma_f32_16x16x32_bf16 v[184:187], a[208:211], a[108:111], v[184:187]\n" \
"ds_read_b64_tr_b16 a[206:207], v33 offset:32288\n" \
"v_mfma_f32_16x16x32_bf16 v[188:191], a[208:211], a[124:127], v[188:191]\n" \
"ds_read_b64_tr_b16 a[208:209], v33 offset:36896\n" \
"ds_read_b64_tr_b16 a[210:211], v33 offset:41504\n" \
"v_mfma_f32_16x16x32_bf16 v[184:187], a[212:215], a[160:163], v[184:187]\n" \
"v_mfma_f32_16x16x32_bf16 v[188:191], a[212:215], a[176:179], v[188:191]\n" \
"ds_read_b64_tr_b16 a[212:213], v33 offset:46112\n" \
"ds_read_b64_tr_b16 a[214:215], v33 offset:50720\n" \
"s_waitcnt vmcnt(6)\n" \
"s_mov_b64 exec, 1\n" \
"buffer_store_dword v39, off, s[56:59], s71 offset:20 sc1\n" \
"s_mov_b64 exec, -1\n" \
"v_mfma_f32_16x16x32_bf16 v[184:187], a[216:219], a[164:167], v[184:187]\n" \
"v_mfma_f32_16x16x32_bf16 v[188:191], a[216:219], a[180:183], v[188:191]\n" \
"ds_read_b64_tr_b16 a[216:217], v34 offset:32\n" \
"ds_read_b64_tr_b16 a[218:219], v34 offset:4640\n" \
"v_mfma_f32_16x16x32_bf16 v[184:187], a[0:3], a[168:171], v[184:187]\n" \
"v_mfma_f32_16x16x32_bf16 v[188:191], a[0:3], a[184:187], v[188:191]\n" \
"ds_read_b64_tr_b16 a[0:1], v34 offset:9248\n" \
"ds_read_b64_tr_b16 a[2:3], v34 offset:13856\n" \
"v_mfma_f32_16x16x32_bf16 v[184:187], a[4:7], a[172:175], v[184:187]\n" \
"v_mfma_f32_16x16x32_bf16 v[188:191], a[4:7], a[188:191], v[188:191]\n" \
"ds_read_b64_tr_b16 a[4:5], v34 offset:18464\n" \
"ds_read_b64_tr_b16 a[6:7], v34 offset:23072\n" \
"v_mfma_f32_16x16x32_bf16 v[184:187], a[8:11], a[220:223], v[184:187]\n" \
"v_mfma_f32_16x16x32_bf16 v[188:191], a[8:11], a[236:239], v[188:191]\n" \
"ds_read_b64_tr_b16 a[8:9], v33 offset:32\n" \
"ds_read_b64_tr_b16 a[10:11], v33 offset:4640\n" \
"v_mfma_f32_16x16x32_bf16 v[184:187], a[12:15], a[224:227], v[184:187]\n" \
"v_mfma_f32_16x16x32_bf16 v[188:191], a[12:15], a[240:243], v[188:191]\n" \
"ds_read_b64_tr_b16 a[12:13], v10 offset:27712\n" \
"ds_read_b64_tr_b16 a[14:15], v10 offset:32320\n" \
"v_mfma_f32_16x16x32_bf16 v[184:187], a[16:19], a[228:231], v[184:187]\n" \
"v_mfma_f32_16x16x32_bf16 v[188:191], a[16:19], a[244:247], v[188:191]\n" \
"ds_read_b64_tr_b16 a[16:17], v10 offset:36928\n" \
"ds_read_b64_tr_b16 a[18:19], v10 offset:41536\n" \
"v_mfma_f32_16x16x32_bf16 v[184:187], a[20:23], a[232:235], v[184:187]\n" \
"v_mfma_f32_16x16x32_bf16 v[188:191], a[20:23], a[248:251], v[188:191]\n" \
"ds_read_b64_tr_b16 a[20:21], v10 offset:46144\n" \
"ds_read_b64_tr_b16 a[22:23], v10 offset:50752\n" \
"v_mfma_f32_16x16x32_bf16 v[192:195], a[24:27], a[96:99], v[192:195]\n" \
"v_mfma_f32_16x16x32_bf16 v[196:199], a[24:27], a[112:115], v[196:199]\n" \
"ds_read_b64_tr_b16 a[24:25], v10 offset:55360\n" \
"s_waitcnt lgkmcnt(13)\n" \
"v_mfma_f32_16x16x32_bf16 v[192:195], a[28:31], a[100:103], v[192:195]\n" \
"v_cvt_pk_bf16_f32 v184, v184, v185\n" \
"v_cvt_pk_bf16_f32 v185, v186, v187\n" \
"v_cvt_pk_bf16_f32 v186, v188, v189\n" \
"v_cvt_pk_bf16_f32 v187, v190, v191\n" \
"buffer_store_dwordx4 v[184:187], v20, s[56:59], s92 offen offset:1024 sc1\n" \
"s_add_u32 s92, s92, 0x3800\n" \
"v_mfma_f32_16x16x32_bf16 v[196:199], a[28:31], a[116:119], v[196:199]\n" \
"ds_read_b64_tr_b16 a[26:27], v10 offset:59968\n" \
"ds_read_b64_tr_b16 a[28:29], v33 offset:18496\n" \
"v_mfma_f32_16x16x32_bf16 v[192:195], a[192:195], a[104:107], v[192:195]\n" \
"ds_read_b64_tr_b16 a[30:31], v33 offset:23104\n" \
"v_mfma_f32_16x16x32_bf16 v[196:199], a[192:195], a[120:123], v[196:199]\n" \
"ds_read_b64_tr_b16 a[192:193], v33 offset:27712\n" \
"ds_read_b64_tr_b16 a[194:195], v33 offset:32320\n" \
"v_mfma_f32_16x16x32_bf16 v[192:195], a[196:199], a[108:111], v[192:195]\n" \
"v_mfma_f32_16x16x32_bf16 v[196:199], a[196:199], a[124:127], v[196:199]\n" \
"ds_read_b64_tr_b16 a[196:197], v33 offset:36928\n" \
"ds_read_b64_tr_b16 a[198:199], v33 offset:41536\n" \
"v_mfma_f32_16x16x32_bf16 v[192:195], a[200:203], a[160:163], v[192:195]\n" \
"v_mfma_f32_16x16x32_bf16 v[196:199], a[200:203], a[176:179], v[196:199]\n" \
"ds_read_b64_tr_b16 a[200:201], v33 offset:46144\n" \
"ds_read_b64_tr_b16 a[202:203], v33 offset:50752\n" \
"v_mfma_f32_16x16x32_bf16 v[192:195], a[204:207], a[164:167], v[192:195]\n" \
"v_mfma_f32_16x16x32_bf16 v[196:199], a[204:207], a[180:183], v[196:199]\n" \
"ds_read_b64_tr_b16 a[204:205], v34 offset:64\n" \
"ds_read_b64_tr_b16 a[206:207], v34 offset:4672\n" \
"v_mfma_f32_16x16x32_bf16 v[192:195], a[208:211], a[168:171], v[192:195]\n" \
"v_mfma_f32_16x16x32_bf16 v[196:199], a[208:211], a[184:187], v[196:199]\n" \
"ds_read_b64_tr_b16 a[208:209], v34 offset:9280\n" \
"ds_read_b64_tr_b16 a[210:211], v34 offset:13888\n" \
"v_mfma_f32_16x16x32_bf16 v[192:195], a[212:215], a[172:175], v[192:195]\n" \
"v_mfma_f32_16x16x32_bf16 v[196:199], a[212:215], a[188:191], v[196:199]\n" \
"ds_read_b64_tr_b16 a[212:213], v34 offset:18496\n" \
"ds_read_b64_tr_b16 a[214:215], v34 offset:23104\n" \
"v_mfma_f32_16x16x32_bf16 v[192:195], a[216:219], a[220:223], v[192:195]\n" \
"v_mfma_f32_16x16x32_bf16 v[196:199], a[216:219], a[236:239], v[196:199]\n" \
"ds_read_b64_tr_b16 a[216:217], v33 offset:64\n" \
"s_waitcnt lgkmcnt(14)\n" \
"v_mfma_f32_16x16x32_bf16 v[192:195], a[0:3], a[224:227], v[192:195]\n" \
"ds_read_b64_tr_b16 a[218:219], v33 offset:4672\n" \
"v_mfma_f32_16x16x32_bf16 v[196:199], a[0:3], a[240:243], v[196:199]\n" \
"ds_read_b64_tr_b16 a[0:1], v10 offset:27872\n" \
"ds_read_b64_tr_b16 a[2:3], v10 offset:32480\n" \
"v_mfma_f32_16x16x32_bf16 v[192:195], a[4:7], a[228:231], v[192:195]\n" \
"v_mfma_f32_16x16x32_bf16 v[196:199], a[4:7], a[244:247], v[196:199]\n" \
"ds_read_b64_tr_b16 a[4:5], v10 offset:37088\n" \
"ds_read_b64_tr_b16 a[6:7], v10 offset:41696\n" \
"v_mfma_f32_16x16x32_bf16 v[192:195], a[8:11], a[232:235], v[192:195]\n" \
"v_mfma_f32_16x16x32_bf16 v[196:199], a[8:11], a[248:251], v[196:199]\n" \
"ds_read_b64_tr_b16 a[8:9], v10 offset:46304\n" \
"ds_read_b64_tr_b16 a[10:11], v10 offset:50912\n" \
"v_mfma_f32_16x16x32_bf16 v[200:203], a[12:15], a[96:99], v[200:203]\n" \
"v_mfma_f32_16x16x32_bf16 v[204:207], a[12:15], a[112:115], v[204:207]\n" \
"ds_read_b64_tr_b16 a[12:13], v10 offset:55520\n" \
"ds_read_b64_tr_b16 a[14:15], v10 offset:60128\n" \
"v_mfma_f32_16x16x32_bf16 v[200:203], a[16:19], a[100:103], v[200:203]\n" \
"v_cvt_pk_bf16_f32 v192, v192, v193\n" \
"v_cvt_pk_bf16_f32 v193, v194, v195\n" \
"v_cvt_pk_bf16_f32 v194, v196, v197\n" \
"v_cvt_pk_bf16_f32 v195, v198, v199\n" \
"buffer_store_dwordx4 v[192:195], v20, s[56:59], s93 offen offset:1024 sc1\n" \
"s_waitcnt vmcnt(6)\n" \
"s_mov_b64 exec, 1\n" \
"buffer_store_dword v39, off, s[56:59], s72 offset:24 sc1\n" \
"s_mov_b64 exec, -1\n" \
"s_add_u32 s93, s93, 0x3000\n" \
"v_mfma_f32_16x16x32_bf16 v[204:207], a[16:19], a[116:119], v[204:207]\n" \
"ds_read_b64_tr_b16 a[16:17], v33 offset:18656\n" \
"ds_read_b64_tr_b16 a[18:19], v33 offset:23264\n" \
"v_mfma_f32_16x16x32_bf16 v[200:203], a[20:23], a[104:107], v[200:203]\n" \
"v_readlane_b32 s72, v11, 52\n" \
"L_pf21:\n" \
"s_cbranch_execz L_retB\n" \
"s_nop 0\n" \
"v_mfma_f32_16x16x32_bf16 v[204:207], a[20:23], a[120:123], v[204:207]\n" \
"ds_read_b64_tr_b16 a[20:21], v33 offset:27872\n" \
"ds_read_b64_tr_b16 a[22:23], v33 offset:32480\n" \
"v_mfma_f32_16x16x32_bf16 v[200:203], a[24:27], a[108:111], v[200:203]\n" \
"v_mfma_f32_16x16x32_bf16 v[204:207], a[24:27], a[124:127], v[204:207]\n" \
"ds_read_b64_tr_b16 a[24:25], v33 offset:37088\n" \
"s_waitcnt lgkmcnt(11)\n" \
"v_mfma_f32_16x16x32_bf16 v[200:203], a[28:31], a[160:163], v[200:203]\n" \
"ds_read_b64_tr_b16 a[26:27], v33 offset:41696\n" \
"v_mfma_f32_16x16x32_bf16 v[204:207], a[28:31], a[176:179], v[204:207]\n" \
"ds_read_b64_tr_b16 a[28:29], v33 offset:46304\n" \
"ds_read_b64_tr_b16 a[30:31], v33 offset:50912\n" \
"v_mfma_f32_16x16x32_bf16 v[200:203], a[192:195], a[164:167], v[200:203]\n" \
"v_mfma_f32_16x16x32_bf16 v[204:207], a[192:195], a[180:183], v[204:207]\n" \
"ds_read_b64_tr_b16 a[192:193], v34 offset:224\n" \
"ds_read_b64_tr_b16 a[194:195], v34 offset:4832\n" \
"v_mfma_f32_16x16x32_bf16 v[200:203], a[196:199], a[168:171], v[200:203]\n" \
"v_mfma_f32_16x16x32_bf16 v[204:207], a[196:199], a[184:187], v[204:207]\n" \
"ds_read_b64_tr_b16 a[196:197], v34 offset:9440\n" \
"ds_read_b64_tr_b16 a[198:199], v34 offset:14048\n" \
"v_mfma_f32_16x16x32_bf16 v[200:203], a[200:203], a[172:175], v[200:203]\n" \
"v_mfma_f32_16x16x32_bf16 v[204:207], a[200:203], a[188:191], v[204:207]\n" \
"ds_read_b64_tr_b16 a[200:201], v34 offset:18656\n" \
"ds_read_b64_tr_b16 a[202:203], v34 offset:23264\n" \
"v_mfma_f32_16x16x32_bf16 v[200:203], a[204:207], a[220:223], v[200:203]\n" \
"v_mfma_f32_16x16x32_bf16 v[204:207], a[204:207], a[236:239], v[204:207]\n" \
"ds_read_b64_tr_b16 a[204:205], v33 offset:224\n" \
"ds_read_b64_tr_b16 a[206:207], v33 offset:4832\n" \
"v_mfma_f32_16x16x32_bf16 v[200:203], a[208:211], a[224:227], v[200:203]\n" \
"v_mfma_f32_16x16x32_bf16 v[204:207], a[208:211], a[240:243], v[204:207]\n" \
"ds_read_b64_tr_b16 a[208:209], v36 offset:0\n" \
"ds_read_b64_tr_b16 a[210:211], v36 offset:1024\n" \
"v_mfma_f32_16x16x32_bf16 v[200:203], a[212:215], a[228:231], v[200:203]\n" \
"v_mfma_f32_16x16x32_bf16 v[204:207], a[212:215], a[244:247], v[204:207]\n" \
"ds_read_b64_tr_b16 a[212:213], v36 offset:2048\n" \
"ds_read_b64_tr_b16 a[214:215], v36 offset:3072\n" \
"v_mfma_f32_16x16x32_bf16 v[200:203], a[216:219], a[232:235], v[200:203]\n" \
"v_mfma_f32_16x16x32_bf16 v[204:207], a[216:219], a[248:251], v[204:207]\n" \
"ds_read_b64_tr_b16 a[216:217], v36 offset:32\n" \
"ds_read_b64_tr_b16 a[218:219], v36 offset:1056\n" \
"v_mfma_f32_16x16x32_bf16 v[240:243], a[0:3], a[96:99], v[240:243]\n" \
"v_mfma_f32_16x16x32_bf16 v[244:247], a[0:3], a[112:115], v[244:247]\n" \
"ds_read_b64_tr_b16 a[0:1], v36 offset:2080\n" \
"s_waitcnt lgkmcnt(13)\n" \
"v_mfma_f32_16x16x32_bf16 v[240:243], a[4:7], a[100:103], v[240:243]\n" \
"v_cvt_pk_bf16_f32 v200, v200, v201\n" \
"v_cvt_pk_bf16_f32 v201, v202, v203\n" \
"v_cvt_pk_bf16_f32 v202, v204, v205\n" \
"v_cvt_pk_bf16_f32 v203, v206, v207\n" \
"buffer_store_dwordx4 v[200:203], v20, s[56:59], s94 offen offset:1024 sc1\n" \
"s_add_u32 s94, s94, 0x2800\n" \
"v_mfma_f32_16x16x32_bf16 v[244:247], a[4:7], a[116:119], v[244:247]\n" \
"ds_read_b64_tr_b16 a[2:3], v36 offset:3104\n" \
"v_mfma_f32_16x16x32_bf16 v[240:243], a[8:11], a[104:107], v[240:243]\n" \
"v_mfma_f32_16x16x32_bf16 v[244:247], a[8:11], a[120:123], v[244:247]\n" \
"v_mfma_f32_16x16x32_bf16 v[240:243], a[12:15], a[108:111], v[240:243]\n" \
"v_mfma_f32_16x16x32_bf16 v[244:247], a[12:15], a[124:127], v[244:247]\n" \
"v_mfma_f32_16x16x32_bf16 v[168:171], a[16:19], a[128:131], v[168:171]\n" \
"v_mfma_f32_16x16x32_bf16 v[172:175], a[16:19], a[144:147], v[172:175]\n" \
"v_mfma_f32_16x16x32_bf16 v[240:243], a[16:19], a[160:163], v[240:243]\n" \
"v_mfma_f32_16x16x32_bf16 v[244:247], a[16:19], a[176:179], v[244:247]\n" \
"v_mfma_f32_16x16x32_bf16 v[168:171], a[20:23], a[132:135], v[168:171]\n" \
"v_mfma_f32_16x16x32_bf16 v[172:175], a[20:23], a[148:151], v[172:175]\n" \
"v_mfma_f32_16x16x32_bf16 v[240:243], a[20:23], a[164:167], v[240:243]\n" \
"v_mfma_f32_16x16x32_bf16 v[244:247], a[20:23], a[180:183], v[244:247]\n" \
"v_mfma_f32_16x16x32_bf16 v[168:171], a[24:27], a[136:139], v[168:171]\n" \
"v_mfma_f32_16x16x32_bf16 v[172:175], a[24:27], a[152:155], v[172:175]\n" \
"v_mfma_f32_16x16x32_bf16 v[240:243], a[24:27], a[168:171], v[240:243]\n" \
"v_mfma_f32_16x16x32_bf16 v[244:247], a[24:27], a[184:187], v[244:247]\n" \
"v_mfma_f32_16x16x32_bf16 v[168:171], a[28:31], a[140:143], v[168:171]\n" \
"s_waitcnt vmcnt(5)\n" \
"s_mov_b64 exec, 1\n" \
"buffer_store_dword v39, off, s[56:59], s73 offset:28 sc1\n" \
"s_mov_b64 exec, -1\n" \
"v_mfma_f32_16x16x32_bf16 v[172:175], a[28:31], a[156:159], v[172:175]\n" \
"v_readlane_b32 s73, v11, 43\n" \
"v_mfma_f32_16x16x32_bf16 v[240:243], a[28:31], a[172:175], v[240:243]\n" \
"v_mfma_f32_16x16x32_bf16 v[244:247], a[28:31], a[188:191], v[244:247]\n" \
"v_mfma_f32_16x16x32_bf16 v[168:171], a[192:195], a[32:35], v[168:171]\n" \
"v_mfma_f32_16x16x32_bf16 v[172:175], a[192:195], a[48:51], v[172:175]\n" \
"v_mfma_f32_16x16x32_bf16 v[240:243], a[192:195], a[220:223], v[240:243]\n" \
"v_mfma_f32_16x16x32_bf16 v[244:247], a[192:195], a[236:239], v[244:247]\n" \
"s_waitcnt lgkmcnt(0)\n" \
"v_mfma_f32_16x16x32_bf16 v[168:171], a[196:199], a[36:39], v[168:171]\n" \
"v_mfma_f32_16x16x32_bf16 v[172:175], a[196:199], a[52:55], v[172:175]\n" \
"v_mfma_f32_16x16x32_bf16 v[240:243], a[196:199], a[224:227], v[240:243]\n" \
"v_mfma_f32_16x16x32_bf16 v[244:247], a[196:199], a[240:243], v[244:247]\n" \
"v_mfma_f32_16x16x32_bf16 v[168:171], a[200:203], a[40:43], v[168:171]\n" \
"v_mfma_f32_16x16x32_bf16 v[172:175], a[200:203], a[56:59], v[172:175]\n" \
"v_mfma_f32_16x16x32_bf16 v[240:243], a[200:203], a[228:231], v[240:243]\n" \
"v_mfma_f32_16x16x32_bf16 v[244:247], a[200:203], a[244:247], v[244:247]\n" \
"v_mfma_f32_16x16x32_bf16 v[168:171], a[204:207], a[44:47], v[168:171]\n" \
"v_mfma_f32_16x16x32_bf16 v[172:175], a[204:207], a[60:63], v[172:175]\n" \
"v_mfma_f32_16x16x32_bf16 v[240:243], a[204:207], a[232:235], v[240:243]\n" \
"v_mfma_f32_16x16x32_bf16 v[244:247], a[204:207], a[248:251], v[244:247]\n" \
"v_mfma_f32_16x16x32_bf16 v[168:171], a[208:211], v[64:67], v[168:171]\n" \
"v_mfma_f32_16x16x32_bf16 v[172:175], a[208:211], v[72:75], v[172:175]\n" \
"v_mfma_f32_16x16x32_bf16 v[240:243], a[208:211], v[80:83], v[240:243]\n" \
"v_mfma_f32_16x16x32_bf16 v[244:247], a[208:211], v[88:91], v[244:247]\n" \
"v_mfma_f32_16x16x32_bf16 v[168:171], a[212:215], v[68:71], v[168:171]\n" \
"s_waitcnt vmcnt(4)\n" \
"s_mov_b64 exec, 1\n" \
"buffer_store_dword v39, off, s[56:59], s92 offset:32 sc1\n" \
"s_mov_b64 exec, -1\n" \
"v_mfma_f32_16x16x32_bf16 v[172:175], a[212:215], v[76:79], v[172:175]\n" \
"v_readlane_b32 s92, v11, 41\n" \
"v_mfma_f32_16x16x32_bf16 v[240:243], a[212:215], v[84:87], v[240:243]\n" \
"v_mfma_f32_16x16x32_bf16 v[244:247], a[212:215], v[92:95], v[244:247]\n" \
"v_mfma_f32_16x16x32_bf16 v[56:59], a[216:219], v[96:99], 0\n" \
"v_mfma_f32_16x16x32_bf16 v[60:63], a[216:219], v[104:107], 0\n" \
"v_mfma_f32_16x16x32_bf16 v[56:59], a[0:3], v[100:103], v[56:59]\n" \
"v_mfma_f32_16x16x32_bf16 v[60:63], a[0:3], v[108:111], v[60:63]\n" \
"s_waitcnt vmcnt(4)\n" \
"s_mov_b64 exec, 1\n" \
"buffer_store_dword v39, off, s[56:59], s93 offset:36 sc1\n" \
"s_mov_b64 exec, -1\n" \
"v_readlane_b32 s93, v11, 42\n" \
"s_waitcnt vmcnt(3)\n" \
"s_mov_b64 exec, 1\n" \
"buffer_store_dword v39, off, s[56:59], s94 offset:40 sc1\n" \
"s_mov_b64 exec, -1\n" \
"buffer_load_dword v37, v40, s[56:59], s74 offen sc1\n" \
"s_waitcnt vmcnt(0)\n" \
"v_cmp_eq_u32_e64 s[98:99], s62, v37\n" \
"s_nop 0\n" \
"s_andn2_b32 s75, 0x1, s98\n" \
"s_cbranch_scc1 L_sx0_%=\n" \
"L_rx0_%=:\n" \
"s_add_u32 s95, s63, 0x0\n" \
"buffer_load_dwordx4 v[116:119], v20, s[56:59], s95 offen offset:0 sc1\n" \
"s_andn2_b32 s75, 0x2, s98\n" \
"s_cbranch_scc1 L_sx1_%=\n" \
"L_rx1_%=:\n" \
"s_add_u32 s96, s63, 0x800\n" \
"buffer_load_dwordx4 v[124:127], v20, s[56:59], s96 offen offset:0 sc1\n" \
"s_andn2_b32 s75, 0x4, s98\n" \
"s_cbranch_scc1 L_sx2_%=\n" \
"L_rx2_%=:\n" \
"s_add_u32 s97, s63, 0x1000\n" \
"buffer_load_dwordx4 v[132:135], v20, s[56:59], s97 offen offset:0 sc1\n" \
"s_andn2_b32 s75, s100, s98\n" \
"s_cbranch_scc1 L_sx3_%=\n" \
"L_rx3_%=:\n" \
"buffer_load_dwordx4 v[136:139], v20, s[56:59], s64 offen offset:0 sc1\n" \
"buffer_load_dwordx4 v[208:211], v20, s[56:59], s64 offen offset:2048 sc1\n" \
"s_add_u32 s64, s64, 0x1000\n" \
"buffer_load_dwordx4 v[144:147], v20, s[56:59], s64 offen offset:0 sc1\n" \
"buffer_load_dwordx4 v[216:219], v20, s[56:59], s64 offen offset:2048 sc1\n" \
"s_add_u32 s64, s64, 0x1000\n" \
"buffer_load_dwordx4 v[152:155], v20, s[56:59], s64 offen offset:0 sc1\n" \
"buffer_load_dwordx4 v[224:227], v20, s[56:59], s64 offen offset:2048 sc1\n" \
"s_add_u32 s64, s64, 0x1000\n" \
"buffer_load_dwordx4 v[176:179], v20, s[56:59], s64 offen offset:0 sc1\n" \
"buffer_load_dwordx4 v[248:251], v20, s[56:59], s64 offen offset:2048 sc1\n" \
"s_andn2_b32 s75, 0x10, s98\n" \
"s_cbranch_scc1 L_sx4_%=\n" \
"L_rx4_%=:\n" \
"s_add_u32 s65, s63, 0x1800\n" \
"buffer_load_dwordx4 v[140:143], v20, s[56:59], s65 offen offset:0 sc1\n" \
"buffer_load_dwordx4 v[212:215], v20, s[56:59], s65 offen offset:1024 sc1\n" \
"s_andn2_b32 s75, 0x20, s98\n" \
"s_cbranch_scc1 L_sx5_%=\n" \
"L_rx5_%=:\n" \
"s_add_u32 s66, s63, 0x2000\n" \
"buffer_load_dwordx4 v[148:151], v20, s[56:59], s66 offen offset:0 sc1\n" \
"buffer_load_dwordx4 v[220:223], v20, s[56:59], s66 offen offset:1024 sc1\n" \
"s_andn2_b32 s75, 0x40, s98\n" \
"s_cbranch_scc1 L_sx6_%=\n" \
"L_rx6_%=:\n" \
"s_add_u32 s67, s63, 0x2800\n" \
"buffer_load_dwordx4 v[156:159], v20, s[56:59], s67 offen offset:0 sc1\n" \
"buffer_load_dwordx4 v[228:231], v20, s[56:59], s67 offen offset:1024 sc1\n" \
"s_andn2_b32 s75, 0x80, s98\n" \
"s_cbranch_scc1 L_sx7_%=\n" \
"L_rx7_%=:\n" \
"s_add_u32 s68, s63, 0x3000\n" \
"buffer_load_dwordx4 v[164:167], v20, s[56:59], s68 offen offset:0 sc1\n" \
"buffer_load_dwordx4 v[236:239], v20, s[56:59], s68 offen offset:1024 sc1\n" \
"s_andn2_b32 s75, 0x100, s98\n" \
"s_cbranch_scc1 L_sx8_%=\n" \
"L_rx8_%=:\n" \
"s_add_u32 s69, s63, 0x0\n" \
"buffer_load_dwordx4 v[188:191], v20, s[56:59], s69 offen offset:1024 sc1\n" \
"s_andn2_b32 s75, 0x200, s98\n" \
"s_cbranch_scc1 L_sx9_%=\n" \
"L_rx9_%=:\n" \
"s_add_u32 s70, s63, 0x800\n" \
"buffer_load_dwordx4 v[196:199], v20, s[56:59], s70 offen offset:1024 sc1\n" \
"s_andn2_b32 s75, 0x400, s98\n" \
"s_cbranch_scc1 L_sx10_%=\n" \
"L_rx10_%=:\n" \
"s_add_u32 s71, s63, 0x1000\n" \
"buffer_load_dwordx4 v[204:207], v20, s[56:59], s71 offen offset:1024 sc1\n" \
"s_waitcnt vmcnt(21)\n" \
"v_mfma_f32_16x16x32_bf16 v[168:171], v[50:53], v[116:119], v[168:171]\n" \
"v_mfma_f32_16x16x32_bf16 v[172:175], v[48:51], v[116:119], v[172:175]\n" \
"s_waitcnt vmcnt(20)\n" \
"v_mfma_f32_16x16x32_bf16 v[168:171], v[50:53], v[124:127], v[168:171]\n" \
"v_mfma_f32_16x16x32_bf16 v[172:175], v[48:51], v[124:127], v[172:175]\n" \
"s_waitcnt vmcnt(19)\n" \
"v_mfma_f32_16x16x32_bf16 v[168:171], v[50:53], v[132:135], v[168:171]\n" \
"v_mfma_f32_16x16x32_bf16 v[172:175], v[48:51], v[132:135], v[172:175]\n" \
"s_waitcnt vmcnt(18)\n" \
"v_mfma_f32_16x16x32_bf16 v[56:59], v[50:53], v[136:139], v[56:59]\n" \
"v_mfma_f32_16x16x32_bf16 v[60:63], v[48:51], v[136:139], v[60:63]\n" \
"s_waitcnt vmcnt(17)\n" \
"v_mfma_f32_16x16x32_bf16 v[56:59], v[50:53], v[208:211], v[56:59]\n" \
"v_mfma_f32_16x16x32_bf16 v[60:63], v[48:51], v[208:211], v[60:63]\n" \
"s_waitcnt vmcnt(16)\n" \
"v_mfma_f32_16x16x32_bf16 v[56:59], v[50:53], v[144:147], v[56:59]\n" \
"v_mfma_f32_16x16x32_bf16 v[60:63], v[48:51], v[144:147], v[60:63]\n" \
"s_waitcnt vmcnt(15)\n" \
"v_mfma_f32_16x16x32_bf16 v[56:59], v[50:53], v[216:219], v[56:59]\n" \
"v_mfma_f32_16x16x32_bf16 v[60:63], v[48:51], v[216:219], v[60:63]\n" \
"s_waitcnt vmcnt(14)\n" \
"v_mfma_f32_16x16x32_bf16 v[56:59], v[50:53], v[152:155], v[56:59]\n" \
"v_mfma_f32_16x16x32_bf16 v[60:63], v[48:51], v[152:155], v[60:63]\n" \
"s_waitcnt vmcnt(13)\n" \
"v_mfma_f32_16x16x32_bf16 v[56:59], v[50:53], v[224:227], v[56:59]\n" \
"v_mfma_f32_16x16x32_bf16 v[60:63], v[48:51], v[224:227], v[60:63]\n" \
"s_waitcnt vmcnt(12)\n" \
"v_mfma_f32_16x16x32_bf16 v[56:59], v[50:53], v[176:179], v[56:59]\n" \
"v_mfma_f32_16x16x32_bf16 v[60:63], v[48:51], v[176:179], v[60:63]\n" \
"s_waitcnt vmcnt(11)\n" \
"v_mfma_f32_16x16x32_bf16 v[56:59], v[50:53], v[248:251], v[56:59]\n" \
"v_mfma_f32_16x16x32_bf16 v[60:63], v[48:51], v[248:251], v[60:63]\n" \
"s_waitcnt vmcnt(10)\n" \
"v_mfma_f32_16x16x32_bf16 v[168:171], v[50:53], v[140:143], v[168:171]\n" \
"v_mfma_f32_16x16x32_bf16 v[172:175], v[48:51], v[140:143], v[172:175]\n" \
"s_waitcnt vmcnt(9)\n" \
"v_mfma_f32_16x16x32_bf16 v[240:243], v[50:53], v[212:215], v[240:243]\n" \
"v_mfma_f32_16x16x32_bf16 v[244:247], v[48:51], v[212:215], v[244:247]\n" \
"s_waitcnt vmcnt(8)\n" \
"v_mfma_f32_16x16x32_bf16 v[168:171], v[50:53], v[148:151], v[168:171]\n" \
"v_cvt_pk_bf16_f32 v56, v56, v57\n" \
"v_cvt_pk_bf16_f32 v60, v60, v61\n" \
"v_mfma_f32_16x16x32_bf16 v[172:175], v[48:51], v[148:151], v[172:175]\n" \
"v_cvt_pk_bf16_f32 v57, v58, v59\n" \
"s_waitcnt vmcnt(7)\n" \
"v_mfma_f32_16x16x32_bf16 v[240:243], v[50:53], v[220:223], v[240:243]\n" \
"buffer_store_dwordx2 v[56:57], v42, s[88:91], s73 offen sc0 sc1\n" \
"v_mfma_f32_16x16x32_bf16 v[244:247], v[48:51], v[220:223], v[244:247]\n" \
"v_cvt_pk_bf16_f32 v61, v62, v63\n" \
"s_waitcnt vmcnt(7)\n" \
"v_mfma_f32_16x16x32_bf16 v[168:171], v[50:53], v[156:159], v[168:171]\n" \
"buffer_store_dwordx2 v[60:61], v43, s[88:91], s73 offen sc0 sc1\n" \
"v_mfma_f32_16x16x32_bf16 v[172:175], v[48:51], v[156:159], v[172:175]\n" \
"s_waitcnt vmcnt(7)\n" \
"v_mfma_f32_16x16x32_bf16 v[240:243], v[50:53], v[228:231], v[240:243]\n" \
"v_mfma_f32_16x16x32_bf16 v[244:247], v[48:51], v[228:231], v[244:247]\n" \
"s_waitcnt vmcnt(6)\n" \
"v_mfma_f32_16x16x32_bf16 v[168:171], v[50:53], v[164:167], v[168:171]\n" \
"v_mfma_f32_16x16x32_bf16 v[172:175], v[48:51], v[164:167], v[172:175]\n" \
"s_waitcnt vmcnt(5)\n" \
"v_mfma_f32_16x16x32_bf16 v[240:243], v[50:53], v[236:239], v[240:243]\n" \
"v_mfma_f32_16x16x32_bf16 v[244:247], v[48:51], v[236:239], v[244:247]\n" \
"s_waitcnt vmcnt(4)\n" \
"v_mfma_f32_16x16x32_bf16 v[240:243], v[50:53], v[188:191], v[240:243]\n" \
"v_mfma_f32_16x16x32_bf16 v[244:247], v[48:51], v[188:191], v[244:247]\n" \
"s_waitcnt vmcnt(3)\n" \
"v_mfma_f32_16x16x32_bf16 v[240:243], v[50:53], v[196:199], v[240:243]\n" \
"v_cvt_pk_bf16_f32 v168, v168, v169\n" \
"v_cvt_pk_bf16_f32 v172, v172, v173\n" \
"v_mfma_f32_16x16x32_bf16 v[244:247], v[48:51], v[196:199], v[244:247]\n" \
"v_cvt_pk_bf16_f32 v169, v170, v171\n" \
"s_waitcnt vmcnt(2)\n" \
"v_mfma_f32_16x16x32_bf16 v[240:243], v[50:53], v[204:207], v[240:243]\n" \
"v_cvt_pk_bf16_f32 v173, v174, v175\n" \
"v_mfma_f32_16x16x32_bf16 v[244:247], v[48:51], v[204:207], v[244:247]\n" \
"s_mov_b64 exec, 1\n" \
"buffer_store_dword v46, off, s[56:59], s72 offset:0 sc1\n" \
"s_mov_b64 exec, -1\n" \
"buffer_store_dwordx2 v[168:169], v42, s[88:91], s92 offen sc0 sc1\n" \
"buffer_store_dwordx2 v[172:173], v43, s[88:91], s92 offen sc0 sc1\n" \
"s_nop 0\n" \
"v_cvt_pk_bf16_f32 v240, v240, v241\n" \
"v_cvt_pk_bf16_f32 v241, v242, v243\n" \
"v_cvt_pk_bf16_f32 v244, v244, v245\n" \
"v_cvt_pk_bf16_f32 v245, v246, v247\n" \
"buffer_store_dwordx2 v[240:241], v42, s[88:91], s93 offen sc0 sc1\n" \
"buffer_store_dwordx2 v[244:245], v43, s[88:91], s93 offen sc0 sc1\n"
#define ATT_VCLOB "v21","v22","v32","v33","v34","v35","v36","v37","v38","v39","v40","v41","v42","v43","v44","v45","v46","v47","v48","v49","v50","v51","v52","v53","v54","v55","v56","v57","v58","v59","v60","v61","v62","v63","v64","v65","v66","v67","v68","v69","v70","v71","v72","v73","v74","v75","v76","v77","v78","v79","v80","v81","v82","v83","v84","v85","v86","v87","v88","v89","v90","v91","v92","v93","v94","v95","v96","v97","v98","v99","v100","v101","v102","v103","v104","v105","v106","v107","v108","v109","v110","v111","v112","v113","v114","v115","v116","v117","v118","v119","v120","v121","v122","v123","v124","v125","v126","v127","v128","v129","v130","v131","v132","v133","v134","v135","v136","v137","v138","v139","v140","v141","v142","v143","v144","v145","v146","v147","v148","v149","v150","v151","v152","v153","v154","v155","v156","v157","v158","v159","v160","v161","v162","v163","v164","v165","v166","v167","v168","v169","v170","v171","v172","v173","v174","v175","v176","v177","v178","v179","v180","v181","v182","v183","v184","v185","v186","v187","v188","v189","v190","v191","v192","v193","v194","v195","v196","v197","v198","v199","v200","v201","v202","v203","v204","v205","v206","v207","v208","v209","v210","v211","v212","v213","v214","v215","v216","v217","v218","v219","v220","v221","v222","v223","v224","v225","v226","v227","v228","v229","v230","v231","v232","v233","v234","v235","v236","v237","v238","v239","v240","v241","v242","v243","v244","v245","v246","v247","v248","v249","v250","v251","v252","v253","v254","v255"
#define ATT_SCLOB "s63","s64","s65","s66","s67","s68","s69","s70","s71","s72","s73","s74","s75","s76","s77","s78","s79","s80","s81","s82","s83","s84","s85","s86","s87","s88","s89","s90","s91","s92","s93","s94","s95","s96","s97","s98","s99","s100","s101"
enum { T_fo, T_fj, T_fa, T_fas, T_fb, T_fbs, T_f8, T_mk8, T_wt, T_wol, T_woh, T_wbl, T_wbh, T_outl, T_outh, T_r1l, T_r1h, T_r1n, T_r1s, T_m0, T_m1, T_mw, T_m4, T_ml, T_w0, T_w1, T_w2, T_w3, T_A1, T_A2, T_A3, T_B1, T_B2, T_B3, T_Ao, T_Bo, T_Al, T_Bl, T_l8, T_xr, T_x8r, T_oA, T_oB, T_o8, T_x0, T_x1, T_x2, T_x3, T_x4, T_x5, T_x6, T_x8, T_ep, T_xf, T_kf, T_o2a, T_o2b };

template <class MID> __device__ __forceinline__ void out_consts(MID&& mid, u32x4 (&vp)[3], const u16* wo, const u16* wob, const u16* out, const unsigned b, const unsigned w, const unsigned lane) {
    const unsigned sp = b & 7u, nt = (b >> 3) & 3u, j = b >> 5, head = 4u * sp + nt, OOR = 0x40000000u;
    unsigned vw[9];
#pragma unroll
    for (unsigned i = 0; i < 9u; ++i) {
        const unsigned q = 64u * i + lane, row = __umul24(q, 3641u) >> 16, c = q - __umul24(row, 18u);
        vw[i] = __umul24(row, DM * 2) + (c < 16u ? (c * 16u + sp * 32u + 32u) & 255u : c * 16u) - (i & 3u) * 1024u;
    }
    const unsigned row = 16u * w + (lane >> 2), r9 = __umul24(16u * (row >> 5) + (row & 12u) + 2u * ((row >> 4) & 1u) + ((row >> 1) & 1u), 18u) + 16u + (row & 1u);
    unsigned vwl = __umul24(r9, DM * 2) + ((lane & 2u) ? 256u : sp * 32u) + (lane & 1u) * 16u, vrd = __umul24(lane >> 2, 288u) + (lane & 3u) * 8u;
    asm volatile("" : "+v"(vw[0]), "+v"(vw[1]), "+v"(vw[2]), "+v"(vw[3]), "+v"(vw[4]), "+v"(vw[5]), "+v"(vw[6]), "+v"(vw[7]), "+v"(vw[8]), "+v"(vwl), "+v"(vrd));
    mid();
    unsigned tab = 0;
#define TB(n, v) asm("v_writelane_b32 %0, %1, %2" : "+v"(tab) : "s"((unsigned)(v)), "n"((int)T_##n))
    const unsigned FJ = (unsigned)WS_F + (F_J + 640u * j) * 4u, mt = 2u * (w & 2u) + (w & 1u), w2 = w < 2u ? w : 2u, hw = head * 147456u;
    const unsigned FR = (unsigned)WS_F + F_R * 4u + (8u * j + sp) * 1024u;
    TB(fo, FJ + 4u * head); TB(fj, FJ); TB(fa, FR + (w | 2u) * 256u); TB(fas, FR + w * 256u + nt * 4u);
    TB(fb, FR + 128u + w * 256u); TB(fbs, FR + 128u + w * 256u + nt * 4u);
    TB(ep, b + w ? OOR : (unsigned)WS_F + F_EPOCH * 4u);
    const u16* pwo = wo + nt * 144u; const u16* pwb = wob + nt * 144u; const u16* pr1 = w < 3u ? pwo : pwb;
    TB(wol, PLO(pwo)); TB(woh, PHI(pwo) & 0xffffu); TB(wbl, PLO(pwb)); TB(wbh, PHI(pwb) & 0xffffu); TB(outl, PLO(out)); TB(outh, PHI(out) & 0xffffu); TB(r1l, PLO(pr1)); TB(r1h, PHI(pr1) & 0xffffu);
    TB(r1n, (w < 3u ? 72u * sp + 71u : 4095u) * 1152u + 288u); TB(r1s, w < 3u ? (72u * sp + 32u * w) * 1152u : hw + 110592u);
    TB(m0, 131072u + 9216u * w2); TB(w0, hw + 36864u * w2); TB(m1, w < 3u ? 9216u * w : 73728u); TB(mw, 9216u * w); TB(m4, w < 3u ? 131072u + 9216u * w : 64512u); TB(ml, 119808u + 1024u * w);
    const unsigned tA = (unsigned)WS_AF + (8u * j + mt) * (KS_TOT * 1024u), tB = (unsigned)WS_AF + (120u - 8u * j + mt) * (KS_TOT * 1024u);
#define HK(k) (4u * sp + ((nt + k) & 3u))
    TB(w1, HK(1u) * 147456u + w * 36864u); TB(w2, HK(2u) * 147456u + w * 36864u); TB(w3, HK(3u) * 147456u + w * 36864u);
    TB(A1, tA + 18432u + HK(1u) * 4096u); TB(A2, tA + 18432u + HK(2u) * 4096u); TB(A3, tA + 18432u + HK(3u) * 4096u);
    TB(B1, tB + 18432u + HK(1u) * 4096u); TB(B2, tB + 18432u + HK(2u) * 4096u); TB(B3, tB + 18432u + HK(3u) * 4096u);
    TB(Ao, tA + sp * 2048u); TB(Bo, tB + sp * 2048u); TB(Al, tA + 16384u); TB(Bl, tB + 16384u);
    const bool red = w == (sp >> 1), blk = sp & 1u;
    const unsigned xg = (unsigned)WS_X + (4u * j + nt) * 589824u + w * 16384u, oA = (128u * j + 16u * mt) * 1152u + nt * 288u, oB = (1920u - 128u * j + 16u * mt) * 1152u + nt * 288u;
    TB(l8, red ? (blk ? tB : tA) + 16384u : OOR); TB(xr, xg + sp * 65536u); TB(x8r, red ? xg + 524288u + (sp & 1u) * 1024u : OOR);
    TB(oA, oA + sp * 32u); TB(oB, oB + sp * 32u); TB(o8, red ? (blk ? oB : oA) + 256u : OOR); TB(x8, xg + 524288u + sp * 2048u);
    TB(f8, xg + w * 131072u + 14400u + sp * 4u); TB(mk8, red ? 0xff0000u : 0u); TB(wt, hw + j * 18432u + w * 4608u);
    const unsigned o2 = (unsigned)WS_AF + (b >> 1) * (KS_TOT * 1024u) + 16384u + ((b & 1u) * 4u + w) * 256u;
    TB(xf, xg + sp * 65536u + 14336u); TB(kf, (unsigned)WS_F + (F_KV + head * 64u) * 4u); TB(o2a, o2); TB(o2b, o2 + 128u);
#define XI(i) TB(x##i, xg + ((sp + 1u + i) & 7u) * 65536u + i * 2048u)
    XI(0); XI(1); XI(2); XI(3); XI(4); XI(5); XI(6);
#undef XI
#undef HK
#undef TB
    vp[0] = (u32x4){vw[0], vw[1], vw[2], vw[3]}; vp[1] = (u32x4){vw[4], vw[5], vw[6], vw[7]}; vp[2] = (u32x4){vw[8], vwl, vrd, tab};
}
__global__ __launch_bounds__(256, 1) __attribute__((aligned(4096))) void fused_kernel(const u16* __restrict__ x, const u16* __restrict__ ckv, const u16* __restrict__ wq, const u16* __restrict__ wuk,
                                                       const u16* __restrict__ wuv, const u16* __restrict__ wqkv, const u16* __restrict__ gw,
                                                       const u16* __restrict__ wo, const u16* __restrict__ wob, char* __restrict__ ws, u16* __restrict__ out) {
    using namespace att;
    extern __shared__ __attribute__((aligned(16))) char lds[];
    const unsigned wid = rfl(threadIdx.x >> 6);
    code_touch((blockIdx.x >> 3) * 4u + wid, (unsigned)(size_t)lds + 151552u + wid * 512u);
    SBAR();
    asm volatile("" :: "s"(x), "s"(ckv), "s"(wq), "s"(wuk), "s"(wuv), "s"(wqkv), "s"(gw), "s"(wo), "s"(wob), "s"(ws), "s"(out));
    unsigned tag;
    const int b = blockIdx.x, head = (b & 7) * 4 + ((b >> 3) & 3), jj = b >> 5;
    u32x4 vp[5];
    const unsigned vl = proj_phase(x, ckv, wq, wuk, wuv, wqkv, gw, ws, lds, wid, tag, vp, [&](auto&& mid) {
        const unsigned lane = threadIdx.x & 63u, p = wid & 1u, gw_ = (unsigned)b * 4u + wid;
        u32x4 mk;
#pragma unroll
        for (int k = 0; k < 4; ++k) { const unsigned key = 16u * (k >> 1) + 4u * (lane >> 4) + 2u * (k & 1), q = 16u * p + (lane & 15u); mk[k] = (key <= q ? 0xffffu : 0u) | (key < q ? 0xffff0000u : 0u); }
        const unsigned tl = min(gw_ * 42u + lane, 42047u);
        const char* tw = lane < 42u ? (tl < 5184u ? (const char*)wo : (const char*)wob - 5184u * 128u) + tl * 128u : (const char*)out + (gw_ * 18u + min(lane, 59u) - 42u) * 128u;
        vp[3] = mk; vp[4] = (u32x4){PLO(tw), PHI(tw), LDS_G + (16u * p + (lane & 15u)) * 4u, LDS_L + wid * 512u + lane * 8u};
        asm volatile("" : "+v"(vp[3]), "+v"(vp[4]));
        u32x4 c[3]; out_consts(mid, c, wo, wob, out, b, wid, lane);
        vp[0] = c[0]; vp[1] = c[1]; vp[2] = c[2];
        asm volatile("" : "+v"(vp[0]), "+v"(vp[1]), "+v"(vp[2]));
    });
    asm volatile("" : "+s"(ws));
    unsigned* const flags = (unsigned*)(ws + WS_F);
    RTS(2);
#define AST(L) do {} while (0)
    const unsigned pair = wid >> 1, p = wid & 1u, sw = 2u * jj + 2u;
    const __amdgpu_buffer_rsrc_t srd = __builtin_amdgcn_make_buffer_rsrc((void*)(ws + WS_K + (size_t)head * 1048576), (short)0, (32 - 2 * jj) * 32768, 0x00020000);
#define SD(J8, H, W) ((unsigned)WS_AF + (((J8) + 2u * ((W) & 2u) + ((W) & 1u)) * KS_TOT + 18u + 4u * (H)) * 1024u)
#define SRDA (u32x4){PLO(ws), PHI(ws) & 0xffffu, (unsigned)WS_L, 0x00020000u}
#define ATT_TAIL "s_endpgm\n"
    asm volatile(ATT_ASM ATT_TAIL "L_end_%=:\n s_nop 1"
                 :
                 : "{v[0:3]}"(vp[0]), "{v[4:7]}"(vp[1]), "{v[8:11]}"(vp[2]), "{v[12:15]}"(vp[3]), "{v[16:19]}"(vp[4]), "{v20}"(vl), "{s[56:59]}"(SRDA), "{s60}"(SD(8u * jj, head, wid)),
                   "{s61}"(SD(120u - 8u * jj, head, wid)), "{s62}"(tag), [srd] "s"(srd), [m1] "s"(49152u + wid * 2048u),
                   [m2] "s"((pair * 16384u + p * 4096u) ^ 32768u), [t2] "s"((sw + pair - 2u) * 32768u), [p4] "s"(p * 4096u), [k2] "s"(pair * 16384u), [v0] "s"(jj ? 16384u : 32768u),
                   [n1] "s"(sw - 2u), [n2] "s"(16u - sw), [m0s] "s"(158720u + wid * 256u), [lim] "s"(0x71800000u), [qa] "s"(81920u + pair * 16384u + p * 4096u), [qa2] "s"(65536u + pair * 57344u + p * 4096u),
                   [fkb] "s"((unsigned)WS_F + (unsigned)(F_KV + head * 64 + 8) * 4u), [fks] "s"(wid != 3u ? 0x40000000u : (unsigned)WS_F + (unsigned)(F_KV + head * 64 + 8 + jj) * 4u)
                 : "memory", "m0", "vcc", "scc", ATT_VCLOB, ATT_SCLOB, ACLOB_LO, ACLOB_HI);
    {
        const u16* const __attribute__((address_space(4)))* ka = (const u16* const __attribute__((address_space(4)))*)__builtin_amdgcn_kernarg_segment_ptr();
        unsigned b2 = b, w2 = wid;
        asm volatile("s_waitcnt vmcnt(0)" : "+s"(ka), "+s"(b2), "+s"(w2), "+s"(ws) :: "memory");
        const unsigned j2 = b2 >> 5, p2 = w2 & 1u, h2 = (b2 & 7u) * 4u + ((b2 >> 3) & 3u);
#pragma unroll 1
        for (unsigned i = w2 & 2u; i < 2u + 3u * (w2 & 2u); ++i)
            att_exact(ka[0], ka[2], ws, h2, (i < 4u ? 1920u - 128u * j2 : 128u * j2 - 128u) + 32u * i + 16u * p2, *reinterpret_cast<const float*>(lds + LDS_G + (32u * i + 16u * p2 + (lane_id() & 15u)) * 4u));
        asm volatile("s_waitcnt vmcnt(0)" ::: "memory");
        unsigned* fa = (unsigned*)(ws + WS_F) + F_R + (8u * j2 + (b2 & 7u)) * 256u + (2u + p2) * 64u + ((b2 >> 3) & 3u);
        if (w2 & 2u) st_flag(fa, tag);
        wait_flags(fa, ld_flag(fa), tag);
        const unsigned l2 = lane_id(); u32x4 c[3]; out_consts([]() {}, c, ka[7], ka[8], ka[10], b2, w2, l2);
        asm volatile("s_waitcnt vmcnt(0) lgkmcnt(0)\n\ts_nop 7\n\ts_branch L_att_stub" :: "{v[0:3]}"(c[0]), "{v[4:7]}"(c[1]), "{v[8:11]}"(c[2]), "{v20}"(l2 * 16u), "{s[56:59]}"(SRDA), "{s60}"(SD(8u * j2, h2, w2)),
                     "{s61}"(SD(120u - 8u * j2, h2, w2)), "{s62}"(tag) : "memory");
    }
    RTS(5);
}

extern "C" void kernel_launch(void* const* d_in, const int* in_sizes, int n_in, void* d_out, int out_size, void* d_ws, size_t ws_size,
                              hipStream_t stream) {
    (void)hipFuncSetAttribute((const void*)fused_kernel, hipFuncAttributeMaxDynamicSharedMemorySize, att::LDS_BYTES);
    fused_kernel<<<dim3(256), 256, att::LDS_BYTES, stream>>>((const u16*)d_in[0], (const u16*)d_in[4], (const u16*)d_in[5], (const u16*)d_in[6], (const u16*)d_in[7], (const u16*)d_in[1],
                                                             (const u16*)d_in[9], (const u16*)d_in[2], (const u16*)d_in[8], (char*)d_ws, (u16*)d_out);
}
```

```cpp
#include <hip/hip_runtime.h>
#include <hip/hip_bf16.h>
#include <stdint.h>
#include <utility>

typedef short bf16x8 __attribute__((ext_vector_type(8)));
typedef short s16x4 __attribute__((ext_vector_type(4)));
typedef float f32x16 __attribute__((ext_vector_type(16)));
typedef float f32x4 __attribute__((ext_vector_type(4)));
typedef unsigned u32x4 __attribute__((ext_vector_type(4)));
typedef unsigned short u16;

constexpr int S = 2048, DM = 576, NH = 32, HD = 128, DC = 512, NQKV = 1728, DHA = 18;
constexpr int NBIG = NH * HD;
constexpr int KS_TOT = 146;

#define SBAR() __builtin_amdgcn_sched_barrier(0)
constexpr size_t WS_Q = 0, WS_K = WS_Q + (size_t)S * NBIG * 2, WS_V = WS_K + (size_t)S * NBIG * 2, WS_AF = WS_V + (size_t)S * NBIG * 2, WS_X = WS_AF + (size_t)128 * KS_TOT * 1024,
                 WS_P = WS_X + (size_t)64 * 16 * 9216, WS_F = WS_P + (size_t)128 * (4 * 32 * 256 + 1024) * 4;
constexpr size_t WS_L = (size_t)96 << 20;
static_assert(WS_F == 96206848, "workspace map is baked into the generated asm");
constexpr int F_EPOCH = 0, F_J = 1024, F_ST = 8192, F_KV = 40960, F_R = 65536;
constexpr unsigned TAG_STEP = 0x9E3779B1u;
__device__ __forceinline__ unsigned ld_flag(const unsigned* p) { return __hip_atomic_load(p, __ATOMIC_RELAXED, __HIP_MEMORY_SCOPE_AGENT); }
__device__ __forceinline__ void st_flag(unsigned* p, unsigned v) { __hip_atomic_store(p, v, __ATOMIC_RELAXED, __HIP_MEMORY_SCOPE_AGENT); }
__device__ __forceinline__ unsigned ld_flag_async(const unsigned* p) { unsigned v; asm volatile("global_load_dword %0, %1, off sc1" : "=v"(v) : "v"(p) : "memory"); return v; }
#define WAITV(v, N) asm volatile("s_waitcnt vmcnt(" #N ")" : "+v"(v) :: "memory")
__device__ __forceinline__ void wait_flags(const unsigned* p, unsigned v, unsigned tag) {
    if (__builtin_expect(__builtin_amdgcn_ballot_w64(v != tag) != 0ull, 0)) {
        unsigned spins = 0;
        do { __builtin_amdgcn_s_sleep(1); } while (__builtin_amdgcn_ballot_w64(ld_flag(p) != tag) != 0ull && ++spins < (1u << 22));
    }
    asm volatile("" ::: "memory");
}

__device__ __forceinline__ unsigned cvtpk(float lo, float hi) {
    unsigned r; asm volatile("v_cvt_pk_bf16_f32 %0, %1, %2" : "=v"(r) : "v"(lo), "v"(hi)); return r;
}
__device__ __forceinline__ float bf2f(u16 v) { return __uint_as_float(((unsigned)v) << 16); }
__device__ __forceinline__ int crow(int r, int hi) { return (r & 3) + 8 * (r >> 2) + 4 * hi; }

typedef __attribute__((address_space(3))) s16x4* lds_s16x4_p;
__device__ __forceinline__ s16x4 trrd(unsigned addr) { return __builtin_amdgcn_ds_read_tr16_b64_v4i16((lds_s16x4_p)addr); }

__device__ __forceinline__ unsigned lane_id() { unsigned l; asm volatile("v_mbcnt_lo_u32_b32 %0, -1, 0\n\tv_mbcnt_hi_u32_b32 %0, -1, %0" : "=v"(l)); return l; }
#define RTS(slot) do {} while (0)
#define CT_L 5u
__device__ __forceinline__ void code_touch(unsigned wi, unsigned lds_scratch) {
    unsigned long long pc; asm volatile("s_getpc_b64 %0" : "=s"(pc));
    asm volatile("s_mov_b32 m0, %0\n\ts_mov_b64 exec, %3\n\tglobal_load_lds_dword %1, %2\n\ts_mov_b64 exec, -1" :: "s"(lds_scratch), "v"(((threadIdx.x & 63u) * 128u + wi) * 128u), "s"(pc & ~127ull), "n"((1u << CT_L) - 1u) : "m0", "memory");
}
#define ACLOB_LO "a0","a1","a2","a3","a4","a5","a6","a7","a8","a9","a10","a11","a12","a13","a14","a15","a16","a17","a18","a19","a20","a21","a22","a23","a24","a25","a26","a27","a28","a29","a30","a31","a32","a33","a34","a35","a36","a37","a38","a39","a40","a41","a42","a43","a44","a45","a46","a47","a48","a49","a50","a51","a52","a53","a54","a55","a56","a57","a58","a59","a60","a61","a62","a63","a64","a65","a66","a67","a68","a69","a70","a71","a72","a73","a74","a75","a76","a77","a78","a79","a80","a81","a82","a83","a84","a85","a86","a87","a88","a89","a90","a91","a92","a93","a94","a95","a96","a97","a98","a99","a100","a101","a102","a103","a104","a105","a106","a107","a108","a109","a110","a111","a112","a113","a114","a115","a116","a117","a118","a119","a120","a121","a122","a123","a124","a125","a126","a127"
#define ACLOB_HI "a128","a129","a130","a131","a132","a133","a134","a135","a136","a137","a138","a139","a140","a141","a142","a143","a144","a145","a146","a147","a148","a149","a150","a151","a152","a153","a154","a155","a156","a157","a158","a159","a160","a161","a162","a163","a164","a165","a166","a167","a168","a169","a170","a171","a172","a173","a174","a175","a176","a177","a178","a179","a180","a181","a182","a183","a184","a185","a186","a187","a188","a189","a190","a191","a192","a193","a194","a195","a196","a197","a198","a199","a200","a201","a202","a203","a204","a205","a206","a207","a208","a209","a210","a211","a212","a213","a214","a215","a216","a217","a218","a219","a220","a221","a222","a223","a224","a225","a226","a227","a228","a229","a230","a231","a232","a233","a234","a235","a236","a237","a238","a239","a240","a241","a242","a243","a244","a245","a246","a247","a248","a249","a250","a251","a252","a253","a254","a255"
#define PROJ_PRE0 \
"s_add_u32 s62, %[rc], 0x0\n" \
"s_and_b32 s62, s62, 0x3ff\n" \
"s_lshl_b32 s63, s62, 12\n" \
"s_add_u32 m0, %[mA], 0x0\n" \
"s_add_u32 s85, s62, 0x0\n" \
"buffer_load_dwordx4 v22, s[64:67], s85 offen lds\n" \
"s_add_u32 m0, %[mA], 0x400\n" \
"s_add_u32 s84, s62, 0x2000\n" \
"buffer_load_dwordx4 v23, s[64:67], s84 offen lds\n" \
"s_add_u32 m0, %[mB], 0x0\n" \
"s_add_u32 s85, s63, 0x0\n" \
"buffer_load_dwordx4 v26, s[68:71], s85 offen lds\n" \
"s_add_u32 m0, %[mB], 0x4000\n" \
"s_add_u32 s84, s63, 0x0\n" \
"buffer_load_dwordx4 v26, s[72:75], s84 offen lds\n" \
"s_add_u32 m0, %[mA], 0x800\n" \
"s_add_u32 s85, s62, 0x4000\n" \
"buffer_load_dwordx4 v22, s[64:67], s85 offen lds\n" \
"s_add_u32 m0, %[mA], 0xc00\n" \
"s_add_u32 s84, s62, 0x6000\n" \
"buffer_load_dwordx4 v23, s[64:67], s84 offen lds\n" \
"s_add_u32 m0, %[mB], 0x400\n" \
"s_add_u32 s85, s63, 0x8000\n" \
"buffer_load_dwordx4 v26, s[68:71], s85 offen lds\n" \
"s_add_u32 m0, %[mB], 0x4400\n" \
"s_add_u32 s84, s63, 0x8000\n" \
"buffer_load_dwordx4 v26, s[72:75], s84 offen lds\n"
#define PROJ_PRE1 \
"s_add_u32 s62, %[rc], 0x0\n" \
"s_and_b32 s62, s62, 0x3ff\n" \
"s_lshl_b32 s63, s62, 12\n" \
"s_add_u32 m0, %[mA], 0x1000\n" \
"s_add_u32 s85, s62, 0x8000\n" \
"buffer_load_dwordx4 v22, s[64:67], s85 offen lds\n" \
"s_add_u32 m0, %[mA], 0x1400\n" \
"s_add_u32 s84, s62, 0xa000\n" \
"buffer_load_dwordx4 v23, s[64:67], s84 offen lds\n" \
"s_add_u32 m0, %[mB], 0x800\n" \
"s_add_u32 s85, s63, 0x10000\n" \
"buffer_load_dwordx4 v27, s[68:71], s85 offen lds\n" \
"s_add_u32 m0, %[mB], 0x4800\n" \
"s_add_u32 s84, s63, 0x10000\n" \
"buffer_load_dwordx4 v27, s[72:75], s84 offen lds\n" \
"s_add_u32 m0, %[mA], 0x1800\n" \
"s_add_u32 s85, s62, 0xc000\n" \
"buffer_load_dwordx4 v22, s[64:67], s85 offen lds\n" \
"s_add_u32 m0, %[mA], 0x1c00\n" \
"s_add_u32 s84, s62, 0xe000\n" \
"buffer_load_dwordx4 v23, s[64:67], s84 offen lds\n" \
"s_add_u32 m0, %[mB], 0xc00\n" \
"s_add_u32 s85, s63, 0x18000\n" \
"buffer_load_dwordx4 v27, s[68:71], s85 offen lds\n" \
"s_add_u32 m0, %[mB], 0x4c00\n" \
"s_add_u32 s84, s63, 0x18000\n" \
"buffer_load_dwordx4 v27, s[72:75], s84 offen lds\n"
#define PROJ_PRE2 \
"s_add_u32 s62, %[rc], 0x80\n" \
"s_and_b32 s62, s62, 0x3ff\n" \
"s_lshl_b32 s63, s62, 12\n" \
"s_add_u32 m0, %[mA], 0x8000\n" \
"s_add_u32 s85, s62, 0x0\n" \
"buffer_load_dwordx4 v22, s[64:67], s85 offen lds\n" \
"s_add_u32 m0, %[mA], 0x8400\n" \
"s_add_u32 s84, s62, 0x2000\n" \
"buffer_load_dwordx4 v23, s[64:67], s84 offen lds\n"
#define PROJ_PRE3 \
"s_add_u32 s62, %[rc], 0x80\n" \
"s_and_b32 s62, s62, 0x3ff\n" \
"s_lshl_b32 s63, s62, 12\n" \
"s_add_u32 m0, %[mB], 0x8000\n" \
"s_add_u32 s85, s63, 0x0\n" \
"buffer_load_dwordx4 v26, s[68:71], s85 offen lds\n" \
"s_add_u32 m0, %[mB], 0xc000\n" \
"s_add_u32 s84, s63, 0x0\n" \
"buffer_load_dwordx4 v26, s[72:75], s84 offen lds\n" \
"s_add_u32 m0, %[mA], 0x8800\n" \
"s_add_u32 s85, s62, 0x4000\n" \
"buffer_load_dwordx4 v22, s[64:67], s85 offen lds\n" \
"s_add_u32 m0, %[mA], 0x8c00\n" \
"s_add_u32 s84, s62, 0x6000\n" \
"buffer_load_dwordx4 v23, s[64:67], s84 offen lds\n" \
"s_add_u32 m0, %[mB], 0x8400\n" \
"s_add_u32 s85, s63, 0x8000\n" \
"buffer_load_dwordx4 v26, s[68:71], s85 offen lds\n" \
"s_add_u32 m0, %[mB], 0xc400\n" \
"s_add_u32 s84, s63, 0x8000\n" \
"buffer_load_dwordx4 v26, s[72:75], s84 offen lds\n" \
"s_add_u32 m0, %[mA], 0x9000\n" \
"s_add_u32 s85, s62, 0x8000\n" \
"buffer_load_dwordx4 v22, s[64:67], s85 offen lds\n" \
"s_add_u32 m0, %[mA], 0x9400\n" \
"s_add_u32 s84, s62, 0xa000\n" \
"buffer_load_dwordx4 v23, s[64:67], s84 offen lds\n"
#define PROJ_TKE 0
#define PROJ_T0 0
#define PROJ_ROT 2*(sp+mt)
#define PROJ_TC 0
#define PROJ_ASM \
".p2align 3\n" \
"s_branch L_go_%=\n" \
"L_pl_%=:\n" \
"s_mov_b32 s63, 0\n" \
"L_ql_%=:\n" \
"s_sleep 1\n" \
"v_bfe_u32 v44, v20, 2, 5\n" \
"buffer_load_dword v44, v44, s[80:83], s42 offen offset:64 sc1\n" \
"s_waitcnt vmcnt(0)\n" \
"v_cmp_ne_u32_e32 vcc, %[tag], v44\n" \
"s_add_u32 s63, s63, 1\n" \
"s_nop 3\n" \
"s_cbranch_vccz L_rl_%=\n" \
"s_cmp_gt_u32 s63, 0x400000\n" \
"s_cbranch_scc0 L_ql_%=\n" \
"s_branch L_rl_%=\n" \
"L_pa_%=:\n" \
"s_mov_b32 s63, 0\n" \
"L_qa_%=:\n" \
"s_sleep 1\n" \
"v_bfe_u32 v44, v20, 2, 5\n" \
"buffer_load_dword v44, v44, s[80:83], s42 offen offset:0 sc1\n" \
"s_waitcnt vmcnt(0)\n" \
"v_cmp_ne_u32_e32 vcc, %[tag], v44\n" \
"s_add_u32 s63, s63, 1\n" \
"s_nop 3\n" \
"s_cbranch_vccz L_ra_%=\n" \
"s_cmp_gt_u32 s63, 0x400000\n" \
"s_cbranch_scc0 L_qa_%=\n" \
"s_branch L_ra_%=\n" \
"L_go_%=:\n" \
"v_xor_b32_e32 v33, 0x20, v32\n" \
"s_mov_b32 s80, %[wsl]\n" \
"s_and_b32 s81, %[wsh], 0xffff\n" \
"s_mov_b32 s82, 0x10000000\n" \
"s_mov_b32 s83, 0x20000\n" \
"s_mov_b32 s84, 0x5bc0000\n" \
"s_add_u32 s62, %[rc], 0x80\n" \
"buffer_load_dword v180, off, s[80:83], s84 sc1\n" \
"s_and_b32 s62, s62, 0x3ff\n" \
"s_lshl_b32 s63, s62, 12\n" \
"v_xor_b32_e64 v34, 0x40, v32\n" \
"v_xor_b32_e32 v35, 0x60, v32\n" \
"s_waitcnt vmcnt(11)\n" \
"s_barrier\n" \
"ds_read_b64_tr_b16 v[120:121], v32 offset:8192\n" \
"ds_read_b64_tr_b16 v[122:123], v32 offset:9216\n" \
"ds_read_b128 v[56:59], v29 offset:16384\n" \
"ds_read_b64_tr_b16 v[116:117], v32 offset:0\n" \
"ds_read_b64_tr_b16 v[118:119], v32 offset:1024\n" \
"ds_read_b128 v[52:55], v28 offset:16384\n" \
"ds_read_b128 v[60:63], v28 offset:18432\n" \
"ds_read_b128 v[64:67], v29 offset:18432\n" \
"ds_read_b128 v[72:75], v29 offset:20480\n" \
"ds_read_b128 v[68:71], v28 offset:20480\n" \
"ds_read_b128 v[76:79], v28 offset:22528\n" \
"ds_read_b128 v[80:83], v29 offset:22528\n" \
"ds_read_b64_tr_b16 v[128:129], v33 offset:8192\n" \
"ds_read_b64_tr_b16 v[130:131], v33 offset:9216\n" \
"ds_read_b64_tr_b16 v[124:125], v33 offset:0\n" \
"ds_read_b64_tr_b16 v[126:127], v33 offset:1024\n" \
"ds_read_b64_tr_b16 v[136:137], v34 offset:8192\n" \
"ds_read_b64_tr_b16 v[138:139], v34 offset:9216\n" \
"s_add_u32 m0, %[mB], 0x8800\n" \
"s_add_u32 s85, s63, 0x10000\n" \
"ds_read_b64_tr_b16 v[132:133], v34 offset:0\n" \
"ds_read_b64_tr_b16 v[134:135], v34 offset:1024\n" \
"ds_read_b64_tr_b16 v[144:145], v35 offset:8192\n" \
"ds_read_b64_tr_b16 v[146:147], v35 offset:9216\n" \
"ds_read_b64_tr_b16 v[140:141], v35 offset:0\n" \
"ds_read_b64_tr_b16 v[142:143], v35 offset:1024\n" \
"s_mov_b32 s76, %[wvl]\n" \
"s_and_b32 s77, %[wvh], 0xffff\n" \
"s_mov_b32 s78, 0x1e6000\n" \
"s_mov_b32 s79, 0x20000\n" \
"s_waitcnt lgkmcnt(15)\n" \
"v_mfma_f32_16x16x32_bf16 a[0:3], v[120:123], v[56:59], 0\n" \
"buffer_load_dwordx4 v27, s[68:71], s85 offen lds\n" \
"v_mfma_f32_16x16x32_bf16 a[0:3], v[116:119], v[52:55], a[0:3]\n" \
"ds_read_b64_tr_b16 v[152:153], v32 offset:24576\n" \
"ds_read_b64_tr_b16 v[154:155], v32 offset:25600\n" \
"v_mfma_f32_16x16x32_bf16 a[32:35], v[116:119], v[60:63], 0\n" \
"s_add_u32 m0, %[mB], 0xc800\n" \
"s_add_u32 s84, s63, 0x10000\n" \
"v_mfma_f32_16x16x32_bf16 a[32:35], v[120:123], v[64:67], a[32:35]\n" \
"buffer_load_dwordx4 v27, s[72:75], s84 offen lds\n" \
"v_mfma_f32_16x16x32_bf16 a[64:67], v[120:123], v[72:75], 0\n" \
"ds_read_b64_tr_b16 v[148:149], v32 offset:16384\n" \
"s_waitcnt lgkmcnt(13)\n" \
"v_mfma_f32_16x16x32_bf16 a[64:67], v[116:119], v[68:71], a[64:67]\n" \
"s_add_u32 m0, %[mA], 0x9800\n" \
"s_add_u32 s85, s62, 0xc000\n" \
"v_mfma_f32_16x16x32_bf16 a[96:99], v[116:119], v[76:79], 0\n" \
"buffer_load_dwordx4 v22, s[64:67], s85 offen lds\n" \
"v_mfma_f32_16x16x32_bf16 a[96:99], v[120:123], v[80:83], a[96:99]\n" \
"ds_read_b64_tr_b16 v[150:151], v32 offset:17408\n" \
"ds_read_b64_tr_b16 v[160:161], v33 offset:24576\n" \
"v_mfma_f32_16x16x32_bf16 a[4:7], v[128:131], v[56:59], 0\n" \
"s_add_u32 m0, %[mA], 0x9c00\n" \
"s_waitcnt lgkmcnt(5)\n" \
"v_mfma_f32_16x16x32_bf16 a[4:7], v[124:127], v[52:55], a[4:7]\n" \
"s_add_u32 s84, s62, 0xe000\n" \
"ds_read_b64_tr_b16 v[162:163], v33 offset:25600\n" \
"v_mfma_f32_16x16x32_bf16 a[36:39], v[124:127], v[60:63], 0\n" \
"buffer_load_dwordx4 v23, s[64:67], s84 offen lds\n" \
"v_mfma_f32_16x16x32_bf16 a[36:39], v[128:131], v[64:67], a[36:39]\n" \
"s_add_u32 m0, %[mB], 0x8c00\n" \
"s_add_u32 s85, s63, 0x18000\n" \
"v_mfma_f32_16x16x32_bf16 a[68:71], v[128:131], v[72:75], 0\n" \
"ds_read_b64_tr_b16 v[156:157], v33 offset:16384\n" \
"ds_read_b64_tr_b16 v[158:159], v33 offset:17408\n" \
"v_mfma_f32_16x16x32_bf16 a[68:71], v[124:127], v[68:71], a[68:71]\n" \
"buffer_load_dwordx4 v27, s[68:71], s85 offen lds\n" \
"v_mfma_f32_16x16x32_bf16 a[100:103], v[124:127], v[76:79], 0\n" \
"s_add_u32 m0, %[mB], 0xcc00\n" \
"s_add_u32 s84, s63, 0x18000\n" \
"v_mfma_f32_16x16x32_bf16 a[100:103], v[128:131], v[80:83], a[100:103]\n" \
"ds_read_b64_tr_b16 v[168:169], v34 offset:24576\n" \
"ds_read_b64_tr_b16 v[170:171], v34 offset:25600\n" \
"v_mfma_f32_16x16x32_bf16 a[8:11], v[136:139], v[56:59], 0\n" \
"buffer_load_dwordx4 v27, s[72:75], s84 offen lds\n" \
"v_mfma_f32_16x16x32_bf16 a[8:11], v[132:135], v[52:55], a[8:11]\n" \
"ds_read_b64_tr_b16 v[164:165], v34 offset:16384\n" \
"ds_read_b64_tr_b16 v[166:167], v34 offset:17408\n" \
"v_mfma_f32_16x16x32_bf16 a[40:43], v[132:135], v[60:63], 0\n" \
"s_lshr_b32 s95, %[b], 3\n" \
"s_lshl_b32 s96, s95, 2\n" \
"v_mfma_f32_16x16x32_bf16 a[40:43], v[136:139], v[64:67], a[40:43]\n" \
"s_add_u32 s97, s96, %[w]\n" \
"v_lshrrev_b32_e32 v212, 4, v20\n" \
"v_mfma_f32_16x16x32_bf16 a[72:75], v[136:139], v[72:75], 0\n" \
"s_mul_i32 s98, 27, s97\n" \
"v_min_u32_e32 v213, 26, v212\n" \
"v_mfma_f32_16x16x32_bf16 a[72:75], v[132:135], v[68:71], a[72:75]\n" \
"v_add_u32_e64 v214, s98, v213\n" \
"v_mul_u32_u24_e32 v215, 0xaaab, v214\n" \
"v_mfma_f32_16x16x32_bf16 a[104:107], v[132:135], v[76:79], 0\n" \
"v_lshrrev_b32_e32 v216, 18, v215\n" \
"v_mul_u32_u24_e32 v218, 6, v216\n" \
"v_mfma_f32_16x16x32_bf16 a[104:107], v[136:139], v[80:83], a[104:107]\n" \
"v_sub_u32_e32 v219, v214, v218\n" \
"v_lshrrev_b32_e32 v220, 1, v219\n" \
"v_mfma_f32_16x16x32_bf16 a[12:15], v[144:147], v[56:59], 0\n" \
"v_mul_u32_u24_e32 v217, 27, v216\n" \
"v_mul_u32_u24_e32 v221, 9, v220\n" \
"v_mfma_f32_16x16x32_bf16 a[12:15], v[140:143], v[52:55], a[12:15]\n" \
"v_add_u32_e32 v222, v217, v221\n" \
"v_and_b32_e32 v223, 1, v219\n" \
"v_mfma_f32_16x16x32_bf16 a[44:47], v[140:143], v[60:63], 0\n" \
"s_and_b32 s94, 7, %[b]\n" \
"v_add_u32_e32 v224, v222, v223\n" \
"v_mfma_f32_16x16x32_bf16 a[44:47], v[144:147], v[64:67], a[44:47]\n" \
"v_add_u32_e32 v225, s94, v224\n" \
"v_lshlrev_b32_e32 v186, 7, v225\n" \
"v_mfma_f32_16x16x32_bf16 a[76:79], v[144:147], v[72:75], 0\n" \
"ds_read_b64_tr_b16 v[176:177], v35 offset:24576\n" \
"ds_read_b64_tr_b16 v[178:179], v35 offset:25600\n" \
"v_mfma_f32_16x16x32_bf16 a[76:79], v[140:143], v[68:71], a[76:79]\n" \
"ds_read_b64_tr_b16 v[172:173], v35 offset:16384\n" \
"ds_read_b64_tr_b16 v[174:175], v35 offset:17408\n" \
"v_mfma_f32_16x16x32_bf16 a[108:111], v[140:143], v[76:79], 0\n" \
"ds_read_b128 v[84:87], v28 offset:0\n" \
"ds_read_b128 v[88:91], v29 offset:0\n" \
"v_mfma_f32_16x16x32_bf16 a[108:111], v[144:147], v[80:83], a[108:111]\n" \
"ds_read_b128 v[92:95], v28 offset:2048\n" \
"s_waitcnt lgkmcnt(7)\n" \
"v_mfma_f32_16x16x32_bf16 a[16:19], v[56:59], v[152:155], 0\n" \
"ds_read_b128 v[96:99], v29 offset:2048\n" \
"ds_read_b128 v[100:103], v28 offset:4096\n" \
"v_mfma_f32_16x16x32_bf16 a[16:19], v[52:55], v[148:151], a[16:19]\n" \
"ds_read_b128 v[104:107], v29 offset:4096\n" \
"ds_read_b128 v[108:111], v28 offset:6144\n" \
"v_mfma_f32_16x16x32_bf16 a[48:51], v[60:63], v[148:151], 0\n" \
"ds_read_b128 v[112:115], v29 offset:6144\n" \
"s_mul_i32 s89, 6, s97\n" \
"v_mfma_f32_16x16x32_bf16 a[48:51], v[64:67], v[152:155], a[48:51]\n" \
"s_mov_b32 m0, %[mS]\n" \
"s_mov_b32 s86, %[wvl]\n" \
"v_mfma_f32_16x16x32_bf16 a[80:83], v[72:75], v[152:155], 0\n" \
"s_mov_b32 s87, %[wvh]\n" \
"s_add_u32 s95, 0xffffffdc, s89\n" \
"v_mfma_f32_16x16x32_bf16 a[80:83], v[68:71], v[148:151], a[80:83]\n" \
"v_min_u32_e32 v227, 41, v212\n" \
"s_mul_i32 s99, 0x1200, s94\n" \
"v_mfma_f32_16x16x32_bf16 a[112:115], v[76:79], v[148:151], 0\n" \
"s_mul_i32 s100, 36, s97\n" \
"v_add_u32_e32 v228, s95, v227\n" \
"v_mfma_f32_16x16x32_bf16 a[112:115], v[80:83], v[152:155], a[112:115]\n" \
"s_add_u32 s101, s99, s100\n" \
"s_mul_i32 s88, 0x288, s94\n" \
"v_mfma_f32_16x16x32_bf16 a[20:23], v[56:59], v[160:163], 0\n" \
"v_min_u32_e32 v229, 0x287, v228\n" \
"v_add_u32_e32 v226, s101, v212\n" \
"v_mfma_f32_16x16x32_bf16 a[20:23], v[52:55], v[156:159], a[20:23]\n" \
"v_add_u32_e32 v230, s88, v229\n" \
"v_cmp_gt_u32_e32 vcc, 36, v212\n" \
"s_mov_b32 exec_lo, 0x7ffffff\n" \
"s_mov_b32 exec_hi, 0x0\n" \
"global_load_lds_dword v186, s[86:87]\n" \
"s_mov_b64 exec, -1\n" \
"v_mfma_f32_16x16x32_bf16 a[52:55], v[60:63], v[156:159], 0\n" \
"v_cndmask_b32_e32 v231, v230, v226, vcc\n" \
"v_lshlrev_b32_e32 v16, 7, v231\n" \
"v_mfma_f32_16x16x32_bf16 a[52:55], v[64:67], v[160:163], a[52:55]\n" \
"s_lshl_b32 s94, %[w], 6\n" \
"v_lshrrev_b32_e32 v212, 4, v20\n" \
"v_mfma_f32_16x16x32_bf16 a[84:87], v[72:75], v[160:163], 0\n" \
"v_add_u32_e64 v213, s94, v212\n" \
"v_mul_u32_u24_e32 v214, 0x2aab, v213\n" \
"v_mfma_f32_16x16x32_bf16 a[84:87], v[68:71], v[156:159], a[84:87]\n" \
"v_lshrrev_b32_e32 v215, 16, v214\n" \
"s_add_u32 s97, 0xfffffe50, s94\n" \
"v_mfma_f32_16x16x32_bf16 a[116:119], v[76:79], v[156:159], 0\n" \
"s_lshr_b32 s95, %[b], 5\n" \
"v_add_u32_e64 v216, s97, v212\n" \
"v_mfma_f32_16x16x32_bf16 a[116:119], v[80:83], v[160:163], a[116:119]\n" \
"v_cmp_gt_u32_e32 vcc, 0x1b0, v213\n" \
"v_mul_u32_u24_e32 v220, 6, v215\n" \
"v_mfma_f32_16x16x32_bf16 a[24:27], v[56:59], v[168:171], 0\n" \
"s_mul_i32 s96, 0x48, s95\n" \
"v_cndmask_b32_e32 v217, v216, v215, vcc\n" \
"v_mfma_f32_16x16x32_bf16 a[24:27], v[52:55], v[164:167], a[24:27]\n" \
"v_sub_u32_e32 v221, v213, v220\n" \
"v_add_u32_e32 v218, s96, v217\n" \
"v_mfma_f32_16x16x32_bf16 a[56:59], v[60:63], v[164:167], 0\n" \
"v_lshrrev_b32_e32 v222, 1, v221\n" \
"s_add_u32 s98, 0x100, s94\n" \
"v_mfma_f32_16x16x32_bf16 a[56:59], v[64:67], v[168:171], a[56:59]\n" \
"v_mul_u32_u24_e32 v219, 0xd80, v218\n" \
"v_mul_u32_u24_e32 v223, 0x480, v222\n" \
"v_mfma_f32_16x16x32_bf16 a[88:91], v[72:75], v[168:171], 0\n" \
"v_and_b32_e32 v225, 1, v221\n" \
"v_add_u32_e64 v228, s98, v212\n" \
"v_mfma_f32_16x16x32_bf16 a[88:91], v[68:71], v[164:167], a[88:91]\n" \
"s_and_b32 s88, 7, %[b]\n" \
"s_lshr_b32 s97, %[b], 3\n" \
"v_mfma_f32_16x16x32_bf16 a[120:123], v[76:79], v[164:167], 0\n" \
"v_add_u32_e32 v224, v219, v223\n" \
"v_lshlrev_b32_e32 v226, 4, v225\n" \
"v_mfma_f32_16x16x32_bf16 a[120:123], v[80:83], v[168:171], a[120:123]\n" \
"s_add_u32 s99, 0xffffff50, s94\n" \
"s_waitcnt lgkmcnt(0)\n" \
"v_mfma_f32_16x16x32_bf16 a[28:31], v[56:59], v[176:179], 0\n" \
"s_add_u32 s62, %[rc], 0x100\n" \
"s_and_b32 s62, s62, 0x3ff\n" \
"v_mfma_f32_16x16x32_bf16 a[28:31], v[52:55], v[172:175], a[28:31]\n" \
"s_lshl_b32 s63, s62, 12\n" \
"s_lshl_b32 s89, s88, 2\n" \
"v_mfma_f32_16x16x32_bf16 a[60:63], v[60:63], v[172:175], 0\n" \
"s_and_b32 s98, 3, s97\n" \
"v_add_u32_e64 v227, v224, v226\n" \
"v_mfma_f32_16x16x32_bf16 a[60:63], v[64:67], v[176:179], a[60:63]\n" \
"v_cmp_gt_u32_e32 vcc, 0x1b0, v213\n" \
"s_add_u32 s94, s89, s98\n" \
"v_mfma_f32_16x16x32_bf16 a[92:95], v[72:75], v[176:179], 0\n" \
"v_cndmask_b32_e32 v192, 0x40000000, v227, vcc\n" \
"s_mul_i32 s37, 36, s94\n" \
"v_mfma_f32_16x16x32_bf16 a[92:95], v[68:71], v[172:175], a[92:95]\n" \
"v_mul_u32_u24_e32 v229, 0x2aab, v228\n" \
"v_lshrrev_b32_e32 v230, 16, v229\n" \
"v_mfma_f32_16x16x32_bf16 a[124:127], v[76:79], v[172:175], 0\n" \
"v_add_u32_e64 v231, s99, v212\n" \
"v_cmp_gt_u32_e32 vcc, 0x1b0, v228\n" \
"v_mfma_f32_16x16x32_bf16 a[124:127], v[80:83], v[176:179], a[124:127]\n" \
"v_mul_u32_u24_e32 v235, 6, v230\n" \
"v_cndmask_b32_e32 v232, v231, v230, vcc\n" \
"s_waitcnt vmcnt(1)\n" \
"s_barrier\n" \
"v_mfma_f32_16x16x32_bf16 a[144:147], v[88:91], v[152:155], 0\n" \
"s_add_u32 m0, %[mA], 0x0\n" \
"s_add_u32 s85, s62, 0x0\n" \
"v_mfma_f32_16x16x32_bf16 a[144:147], v[84:87], v[148:151], a[144:147]\n" \
"buffer_load_dwordx4 v22, s[64:67], s85 offen lds\n" \
"v_mfma_f32_16x16x32_bf16 a[176:179], v[92:95], v[148:151], 0\n" \
"ds_read_b128 v[56:59], v29 offset:49152\n" \
"ds_read_b128 v[52:55], v28 offset:49152\n" \
"v_mfma_f32_16x16x32_bf16 a[176:179], v[96:99], v[152:155], a[176:179]\n" \
"s_add_u32 m0, %[mA], 0x400\n" \
"s_add_u32 s84, s62, 0x2000\n" \
"v_mfma_f32_16x16x32_bf16 a[208:211], v[104:107], v[152:155], 0\n" \
"buffer_load_dwordx4 v23, s[64:67], s84 offen lds\n" \
"v_mfma_f32_16x16x32_bf16 a[208:211], v[100:103], v[148:151], a[208:211]\n" \
"ds_read_b128 v[60:63], v28 offset:51200\n" \
"ds_read_b128 v[64:67], v29 offset:51200\n" \
"v_mfma_f32_16x16x32_bf16 a[240:243], v[108:111], v[148:151], 0\n" \
"ds_read_b64_tr_b16 v[148:149], v32 offset:49152\n" \
"ds_read_b64_tr_b16 v[150:151], v32 offset:50176\n" \
"v_mfma_f32_16x16x32_bf16 a[240:243], v[112:115], v[152:155], a[240:243]\n" \
"s_add_u32 m0, %[mB], 0x0\n" \
"s_add_u32 s85, s63, 0x0\n" \
"v_mfma_f32_16x16x32_bf16 a[148:151], v[88:91], v[160:163], 0\n" \
"buffer_load_dwordx4 v26, s[68:71], s85 offen lds\n" \
"v_mfma_f32_16x16x32_bf16 a[148:151], v[84:87], v[156:159], a[148:151]\n" \
"ds_read_b64_tr_b16 v[152:153], v32 offset:57344\n" \
"ds_read_b64_tr_b16 v[154:155], v32 offset:58368\n" \
"v_mfma_f32_16x16x32_bf16 a[180:183], v[92:95], v[156:159], 0\n" \
"ds_read_b128 v[72:75], v29 offset:53248\n" \
"ds_read_b128 v[68:71], v28 offset:53248\n" \
"v_mfma_f32_16x16x32_bf16 a[180:183], v[96:99], v[160:163], a[180:183]\n" \
"s_add_u32 m0, %[mB], 0x4000\n" \
"s_add_u32 s84, s63, 0x0\n" \
"s_nop 0\n" \
"v_mfma_f32_16x16x32_bf16 a[212:215], v[104:107], v[160:163], 0\n" \
"buffer_load_dwordx4 v26, s[72:75], s84 offen lds\n" \
"v_mfma_f32_16x16x32_bf16 a[212:215], v[100:103], v[156:159], a[212:215]\n" \
"ds_read_b128 v[76:79], v28 offset:55296\n" \
"ds_read_b128 v[80:83], v29 offset:55296\n" \
"v_mfma_f32_16x16x32_bf16 a[244:247], v[108:111], v[156:159], 0\n" \
"ds_read_b64_tr_b16 v[156:157], v33 offset:49152\n" \
"ds_read_b64_tr_b16 v[158:159], v33 offset:50176\n" \
"v_mfma_f32_16x16x32_bf16 a[244:247], v[112:115], v[160:163], a[244:247]\n" \
"s_add_u32 m0, %[mA], 0x800\n" \
"s_add_u32 s85, s62, 0x4000\n" \
"v_mfma_f32_16x16x32_bf16 a[152:155], v[88:91], v[168:171], 0\n" \
"buffer_load_dwordx4 v22, s[64:67], s85 offen lds\n" \
"v_mfma_f32_16x16x32_bf16 a[152:155], v[84:87], v[164:167], a[152:155]\n" \
"ds_read_b64_tr_b16 v[160:161], v33 offset:57344\n" \
"ds_read_b64_tr_b16 v[162:163], v33 offset:58368\n" \
"v_mfma_f32_16x16x32_bf16 a[184:187], v[92:95], v[164:167], 0\n" \
"v_sub_u32_e32 v236, v228, v235\n" \
"v_add_u32_e32 v233, s96, v232\n" \
"v_mfma_f32_16x16x32_bf16 a[184:187], v[96:99], v[168:171], a[184:187]\n" \
"s_add_u32 m0, %[mA], 0xc00\n" \
"s_add_u32 s84, s62, 0x6000\n" \
"v_mfma_f32_16x16x32_bf16 a[216:219], v[104:107], v[168:171], 0\n" \
"buffer_load_dwordx4 v23, s[64:67], s84 offen lds\n" \
"v_mfma_f32_16x16x32_bf16 a[216:219], v[100:103], v[164:167], a[216:219]\n" \
"v_lshrrev_b32_e32 v237, 1, v236\n" \
"s_nop 0\n" \
"v_mul_u32_u24_e32 v234, 0xd80, v233\n" \
"v_mfma_f32_16x16x32_bf16 a[248:251], v[108:111], v[164:167], 0\n" \
"ds_read_b64_tr_b16 v[164:165], v34 offset:49152\n" \
"ds_read_b64_tr_b16 v[166:167], v34 offset:50176\n" \
"v_mfma_f32_16x16x32_bf16 a[248:251], v[112:115], v[168:171], a[248:251]\n" \
"s_add_u32 m0, %[mB], 0x400\n" \
"s_add_u32 s85, s63, 0x8000\n" \
"v_mfma_f32_16x16x32_bf16 a[156:159], v[88:91], v[176:179], 0\n" \
"buffer_load_dwordx4 v26, s[68:71], s85 offen lds\n" \
"v_mfma_f32_16x16x32_bf16 a[156:159], v[84:87], v[172:175], a[156:159]\n" \
"ds_read_b64_tr_b16 v[168:169], v34 offset:57344\n" \
"ds_read_b64_tr_b16 v[170:171], v34 offset:58368\n" \
"v_mfma_f32_16x16x32_bf16 a[188:191], v[92:95], v[172:175], 0\n" \
"v_mul_u32_u24_e32 v238, 0x480, v237\n" \
"v_and_b32_e64 v240, 1, v236\n" \
"v_mfma_f32_16x16x32_bf16 a[188:191], v[96:99], v[176:179], a[188:191]\n" \
"s_add_u32 m0, %[mB], 0x4400\n" \
"s_add_u32 s84, s63, 0x8000\n" \
"v_mfma_f32_16x16x32_bf16 a[220:223], v[104:107], v[176:179], 0\n" \
"buffer_load_dwordx4 v26, s[72:75], s84 offen lds\n" \
"v_mfma_f32_16x16x32_bf16 a[220:223], v[100:103], v[172:175], a[220:223]\n" \
"v_add_u32_e32 v239, v234, v238\n" \
"v_lshlrev_b32_e32 v241, 4, v240\n" \
"v_mfma_f32_16x16x32_bf16 a[252:255], v[108:111], v[172:175], 0\n" \
"ds_read_b64_tr_b16 v[172:173], v35 offset:49152\n" \
"ds_read_b64_tr_b16 v[174:175], v35 offset:50176\n" \
"v_mfma_f32_16x16x32_bf16 a[252:255], v[112:115], v[176:179], a[252:255]\n" \
"s_add_u32 m0, %[mA], 0x1000\n" \
"s_add_u32 s85, s62, 0x8000\n" \
"v_mfma_f32_16x16x32_bf16 a[128:131], v[120:123], v[88:91], 0\n" \
"buffer_load_dwordx4 v22, s[64:67], s85 offen lds\n" \
"v_mfma_f32_16x16x32_bf16 a[128:131], v[116:119], v[84:87], a[128:131]\n" \
"ds_read_b64_tr_b16 v[176:177], v35 offset:57344\n" \
"ds_read_b64_tr_b16 v[178:179], v35 offset:58368\n" \
"v_mfma_f32_16x16x32_bf16 a[160:163], v[116:119], v[92:95], 0\n" \
"v_add_u32_e64 v242, v239, v241\n" \
"v_cmp_gt_u32_e32 vcc, 0x1b0, v228\n" \
"v_mfma_f32_16x16x32_bf16 a[160:163], v[120:123], v[96:99], a[160:163]\n" \
"s_add_u32 m0, %[mA], 0x1400\n" \
"s_add_u32 s84, s62, 0xa000\n" \
"v_mfma_f32_16x16x32_bf16 a[192:195], v[120:123], v[104:107], 0\n" \
"buffer_load_dwordx4 v23, s[64:67], s84 offen lds\n" \
"v_mfma_f32_16x16x32_bf16 a[192:195], v[116:119], v[100:103], a[192:195]\n" \
"v_cndmask_b32_e32 v193, 0x40000000, v242, vcc\n" \
"s_mul_i32 s100, 18, %[w]\n" \
"v_mfma_f32_16x16x32_bf16 a[224:227], v[116:119], v[108:111], 0\n" \
"ds_read_b64_tr_b16 v[116:117], v32 offset:32768\n" \
"ds_read_b64_tr_b16 v[118:119], v32 offset:33792\n" \
"v_mfma_f32_16x16x32_bf16 a[224:227], v[120:123], v[112:115], a[224:227]\n" \
"s_add_u32 m0, %[mB], 0x800\n" \
"s_add_u32 s85, s63, 0x10000\n" \
"v_mfma_f32_16x16x32_bf16 a[132:135], v[128:131], v[88:91], 0\n" \
"buffer_load_dwordx4 v27, s[68:71], s85 offen lds\n" \
"v_mfma_f32_16x16x32_bf16 a[132:135], v[124:127], v[84:87], a[132:135]\n" \
"ds_read_b64_tr_b16 v[120:121], v32 offset:40960\n" \
"ds_read_b64_tr_b16 v[122:123], v32 offset:41984\n" \
"v_mfma_f32_16x16x32_bf16 a[164:167], v[124:127], v[92:95], 0\n" \
"v_lshlrev_b32_e32 v243, 1, v233\n" \
"v_cmp_gt_u32_e32 vcc, 0x48, v231\n" \
"v_mfma_f32_16x16x32_bf16 a[164:167], v[128:131], v[96:99], a[164:167]\n" \
"s_add_u32 m0, %[mB], 0x4800\n" \
"s_add_u32 s84, s63, 0x10000\n" \
"v_mfma_f32_16x16x32_bf16 a[196:199], v[128:131], v[104:107], 0\n" \
"buffer_load_dwordx4 v27, s[72:75], s84 offen lds\n" \
"v_mfma_f32_16x16x32_bf16 a[196:199], v[124:127], v[100:103], a[196:199]\n" \
"s_add_u32 s101, s96, s100\n" \
"v_cndmask_b32_e32 v195, 0x40000000, v243, vcc\n" \
"v_mfma_f32_16x16x32_bf16 a[228:231], v[124:127], v[108:111], 0\n" \
"ds_read_b64_tr_b16 v[124:125], v33 offset:32768\n" \
"ds_read_b64_tr_b16 v[126:127], v33 offset:33792\n" \
"v_mfma_f32_16x16x32_bf16 a[228:231], v[128:131], v[112:115], a[228:231]\n" \
"ds_read_b64_tr_b16 v[128:129], v33 offset:40960\n" \
"ds_read_b64_tr_b16 v[130:131], v33 offset:41984\n" \
"v_mfma_f32_16x16x32_bf16 a[136:139], v[136:139], v[88:91], 0\n" \
"v_add_u32_e32 v244, s101, v212\n" \
"v_mul_u32_u24_e32 v245, 0xd80, v244\n" \
"v_mfma_f32_16x16x32_bf16 a[136:139], v[132:135], v[84:87], a[136:139]\n" \
"v_cmp_gt_u32_e32 vcc, 18, v212\n" \
"v_lshrrev_b32_e32 v248, 1, v218\n" \
"v_mfma_f32_16x16x32_bf16 a[168:171], v[132:135], v[92:95], 0\n" \
"v_cndmask_b32_e32 v194, 0x40000000, v245, vcc\n" \
"v_lshrrev_b32_e32 v250, 2, v218\n" \
"v_mfma_f32_16x16x32_bf16 a[168:171], v[136:139], v[96:99], a[168:171]\n" \
"L_pf0:\n" \
"s_cbranch_execz L_pf4\n" \
"s_nop 0\n" \
"v_and_b32_e32 v249, 1, v248\n" \
"v_and_b32_e32 v251, 2, v250\n" \
"v_mfma_f32_16x16x32_bf16 a[200:203], v[136:139], v[104:107], 0\n" \
"v_cmp_gt_u32_e32 vcc, 0x1b0, v213\n" \
"v_or_b32_e32 v214, v249, v251\n" \
"v_mfma_f32_16x16x32_bf16 a[200:203], v[132:135], v[100:103], a[200:203]\n" \
"v_cndmask_b32_e32 v247, 7, v221, vcc\n" \
"v_lshlrev_b32_e32 v216, 1, v214\n" \
"v_mfma_f32_16x16x32_bf16 a[232:235], v[132:135], v[108:111], 0\n" \
"ds_read_b64_tr_b16 v[132:133], v34 offset:32768\n" \
"ds_read_b64_tr_b16 v[134:135], v34 offset:33792\n" \
"v_mfma_f32_16x16x32_bf16 a[232:235], v[136:139], v[112:115], a[232:235]\n" \
"ds_read_b64_tr_b16 v[136:137], v34 offset:40960\n" \
"ds_read_b64_tr_b16 v[138:139], v34 offset:41984\n" \
"v_mfma_f32_16x16x32_bf16 a[140:143], v[144:147], v[88:91], 0\n" \
"ds_read_b128 v[88:91], v29 offset:32768\n" \
"v_xor_b32_e32 v217, v247, v216\n" \
"v_mfma_f32_16x16x32_bf16 a[140:143], v[140:143], v[84:87], a[140:143]\n" \
"ds_read_b128 v[84:87], v28 offset:32768\n" \
"s_mul_i32 s99, 0x2400, s95\n" \
"v_mfma_f32_16x16x32_bf16 a[172:175], v[140:143], v[92:95], 0\n" \
"ds_read_b128 v[92:95], v28 offset:34816\n" \
"v_lshlrev_b32_e32 v246, 7, v218\n" \
"v_mfma_f32_16x16x32_bf16 a[172:175], v[144:147], v[96:99], a[172:175]\n" \
"ds_read_b128 v[96:99], v29 offset:34816\n" \
"v_lshlrev_b32_e32 v215, 4, v217\n" \
"v_mfma_f32_16x16x32_bf16 a[204:207], v[144:147], v[104:107], 0\n" \
"ds_read_b128 v[104:107], v29 offset:36864\n" \
"s_sub_u32 s96, 0x22000, s99\n" \
"v_mfma_f32_16x16x32_bf16 a[204:207], v[140:143], v[100:103], a[204:207]\n" \
"ds_read_b128 v[100:103], v28 offset:36864\n" \
"v_add_u32_e64 v220, v246, v215\n" \
"v_mfma_f32_16x16x32_bf16 a[236:239], v[140:143], v[108:111], 0\n" \
"ds_read_b64_tr_b16 v[140:141], v35 offset:32768\n" \
"ds_read_b64_tr_b16 v[142:143], v35 offset:33792\n" \
"v_mfma_f32_16x16x32_bf16 a[236:239], v[144:147], v[112:115], a[236:239]\n" \
"s_add_u32 m0, %[mA], 0x1800\n" \
"s_waitcnt lgkmcnt(12)\n" \
"s_nop 0\n" \
"v_mfma_f32_16x16x32_bf16 a[16:19], v[56:59], v[152:155], a[16:19]\n" \
"s_add_u32 s85, s62, 0xc000\n" \
"ds_read_b64_tr_b16 v[144:145], v35 offset:40960\n" \
"v_mfma_f32_16x16x32_bf16 a[16:19], v[52:55], v[148:151], a[16:19]\n" \
"buffer_load_dwordx4 v22, s[64:67], s85 offen lds\n" \
"v_mfma_f32_16x16x32_bf16 a[48:51], v[60:63], v[148:151], a[48:51]\n" \
"s_add_u32 m0, %[mA], 0x1c00\n" \
"s_add_u32 s84, s62, 0xe000\n" \
"v_mfma_f32_16x16x32_bf16 a[48:51], v[64:67], v[152:155], a[48:51]\n" \
"ds_read_b64_tr_b16 v[146:147], v35 offset:41984\n" \
"ds_read_b128 v[108:111], v28 offset:38912\n" \
"v_mfma_f32_16x16x32_bf16 a[80:83], v[72:75], v[152:155], a[80:83]\n" \
"buffer_load_dwordx4 v23, s[64:67], s84 offen lds\n" \
"v_mfma_f32_16x16x32_bf16 a[80:83], v[68:71], v[148:151], a[80:83]\n" \
"s_add_u32 m0, %[mB], 0xc00\n" \
"s_add_u32 s85, s63, 0x18000\n" \
"v_mfma_f32_16x16x32_bf16 a[112:115], v[76:79], v[148:151], a[112:115]\n" \
"ds_read_b128 v[112:115], v29 offset:38912\n" \
"s_mul_i32 s89, 0x12000, s94\n" \
"v_mfma_f32_16x16x32_bf16 a[112:115], v[80:83], v[152:155], a[112:115]\n" \
"buffer_load_dwordx4 v27, s[68:71], s85 offen lds\n" \
"v_mfma_f32_16x16x32_bf16 a[20:23], v[56:59], v[160:163], a[20:23]\n" \
"s_add_u32 m0, %[mB], 0x4c00\n" \
"s_add_u32 s84, s63, 0x18000\n" \
"v_mfma_f32_16x16x32_bf16 a[20:23], v[52:55], v[156:159], a[20:23]\n" \
"v_add_u32_e64 v222, s96, v220\n" \
"v_add_u32_e32 v219, 0x24400, v20\n" \
"v_mfma_f32_16x16x32_bf16 a[52:55], v[60:63], v[156:159], a[52:55]\n" \
"buffer_load_dwordx4 v27, s[72:75], s84 offen lds\n" \
"v_mfma_f32_16x16x32_bf16 a[52:55], v[64:67], v[160:163], a[52:55]\n" \
"v_cmp_gt_u32_e32 vcc, 0x1f8, v213\n" \
"v_lshrrev_b32_e32 v224, 1, v233\n" \
"v_mfma_f32_16x16x32_bf16 a[84:87], v[72:75], v[160:163], a[84:87]\n" \
"v_lshrrev_b32_e32 v227, 2, v233\n" \
"s_lshl_b32 s100, %[w], 10\n" \
"v_mfma_f32_16x16x32_bf16 a[84:87], v[68:71], v[156:159], a[84:87]\n" \
"s_add_u32 s98, 0x6000000, s89\n" \
"v_cndmask_b32_e32 v196, v219, v222, vcc\n" \
"v_mfma_f32_16x16x32_bf16 a[116:119], v[76:79], v[156:159], a[116:119]\n" \
"v_and_b32_e32 v226, 1, v224\n" \
"v_and_b32_e32 v229, 2, v227\n" \
"v_mfma_f32_16x16x32_bf16 a[116:119], v[80:83], v[160:163], a[116:119]\n" \
"s_mov_b32 m0, %[mS]\n" \
"s_mov_b32 s86, %[wbl]\n" \
"v_mfma_f32_16x16x32_bf16 a[24:27], v[56:59], v[168:171], a[24:27]\n" \
"s_mov_b32 s87, %[wbh]\n" \
"v_lshrrev_b32_e32 v239, 1, v244\n" \
"v_mfma_f32_16x16x32_bf16 a[24:27], v[52:55], v[164:167], a[24:27]\n" \
"v_lshrrev_b32_e32 v242, 2, v244\n" \
"s_add_u32 s101, 0x22000, s100\n" \
"v_mfma_f32_16x16x32_bf16 a[56:59], v[60:63], v[164:167], a[56:59]\n" \
"s_add_u32 s38, s98, s100\n" \
"v_cmp_gt_u32_e32 vcc, 0x1b0, v228\n" \
"v_mfma_f32_16x16x32_bf16 a[56:59], v[64:67], v[168:171], a[56:59]\n" \
"v_or_b32_e32 v232, v226, v229\n" \
"v_and_b32_e32 v241, 1, v239\n" \
"v_mfma_f32_16x16x32_bf16 a[88:91], v[72:75], v[168:171], a[88:91]\n" \
"v_and_b32_e32 v231, 2, v242\n" \
"v_add_u32_e32 v199, s101, v20\n" \
"v_mfma_f32_16x16x32_bf16 a[88:91], v[68:71], v[164:167], a[88:91]\n" \
"s_add_u32 s39, s38, s99\n" \
"v_cndmask_b32_e32 v225, 7, v236, vcc\n" \
"s_mov_b32 exec_lo, 0x3ffff\n" \
"s_mov_b32 exec_hi, 0x0\n" \
"global_load_lds_dword v16, s[86:87]\n" \
"s_mov_b64 exec, -1\n" \
"v_mfma_f32_16x16x32_bf16 a[120:123], v[76:79], v[164:167], a[120:123]\n" \
"v_lshlrev_b32_e32 v230, 1, v232\n" \
"v_or_b32_e32 v243, v241, v231\n" \
"v_mfma_f32_16x16x32_bf16 a[120:123], v[80:83], v[168:171], a[120:123]\n" \
"s_add_u32 s95, 0x2000, s39\n" \
"s_lshl_b32 s101, s97, 2\n" \
"v_mfma_f32_16x16x32_bf16 a[28:31], v[56:59], v[176:179], a[28:31]\n" \
"s_lshl_b32 s98, %[b], 2\n" \
"v_xor_b32_e64 v235, v225, v230\n" \
"v_mfma_f32_16x16x32_bf16 a[28:31], v[52:55], v[172:175], a[28:31]\n" \
"v_xor_b32_e32 v245, 3, v243\n" \
"s_add_u32 s94, s101, %[w]\n" \
"v_mfma_f32_16x16x32_bf16 a[60:63], v[60:63], v[172:175], a[60:63]\n" \
"s_cmp_eq_u32 %[w], 0\n" \
"s_cselect_b32 s40, s95, 0x40000000\n" \
"v_mfma_f32_16x16x32_bf16 a[60:63], v[64:67], v[176:179], a[60:63]\n" \
"s_add_u32 s100, s98, %[w]\n" \
"v_lshlrev_b32_e32 v223, 7, v233\n" \
"v_mfma_f32_16x16x32_bf16 a[92:95], v[72:75], v[176:179], a[92:95]\n" \
"v_lshlrev_b32_e32 v237, 4, v235\n" \
"v_lshlrev_b32_e32 v240, 7, v244\n" \
"v_mfma_f32_16x16x32_bf16 a[92:95], v[68:71], v[172:175], a[92:95]\n" \
"v_lshlrev_b32_e32 v221, 5, v245\n" \
"s_mul_i32 s89, 36, s94\n" \
"v_mfma_f32_16x16x32_bf16 a[124:127], v[76:79], v[172:175], a[124:127]\n" \
"s_mul_i32 s99, 18, s100\n" \
"v_add_u32_e32 v234, v223, v237\n" \
"v_mfma_f32_16x16x32_bf16 a[124:127], v[80:83], v[176:179], a[124:127]\n" \
"v_add_u32_e32 v248, v240, v221\n" \
"v_add_u32_e32 v250, s89, v212\n" \
"v_mfma_f32_16x16x32_bf16 a[0:3], v[120:123], v[56:59], a[0:3]\n" \
"s_add_u32 s95, 0xffffffdc, s99\n" \
"v_add_u32_e64 v238, s96, v234\n" \
"v_mfma_f32_16x16x32_bf16 a[0:3], v[116:119], v[52:55], a[0:3]\n" \
"v_add_u32_e32 v218, s96, v248\n" \
"v_lshrrev_b32_e32 v249, 3, v250\n" \
"v_mfma_f32_16x16x32_bf16 a[32:35], v[116:119], v[60:63], a[32:35]\n" \
"v_min_u32_e32 v217, 53, v212\n" \
"s_and_b32 s97, 7, %[b]\n" \
"v_mfma_f32_16x16x32_bf16 a[32:35], v[120:123], v[64:67], a[32:35]\n" \
"s_lshr_b32 s99, %[b], 3\n" \
"s_lshl_b32 s96, s88, 3\n" \
"v_mfma_f32_16x16x32_bf16 a[64:67], v[120:123], v[72:75], a[64:67]\n" \
"v_lshlrev_b32_e32 v251, 6, v249\n" \
"v_add_u32_e32 v246, s95, v217\n" \
"v_mfma_f32_16x16x32_bf16 a[64:67], v[116:119], v[68:71], a[64:67]\n" \
"s_lshr_b32 s94, %[b], 5\n" \
"s_lshl_b32 s98, s97, 2\n" \
"v_mfma_f32_16x16x32_bf16 a[96:99], v[116:119], v[76:79], a[96:99]\n" \
"s_and_b32 s100, 3, s99\n" \
"v_add_u32_e32 v214, s96, v251\n" \
"v_mfma_f32_16x16x32_bf16 a[96:99], v[120:123], v[80:83], a[96:99]\n" \
"s_mul_i32 s95, 0xa00, s94\n" \
"s_add_u32 s101, s98, s100\n" \
"v_mfma_f32_16x16x32_bf16 a[4:7], v[128:131], v[56:59], a[4:7]\n" \
"s_add_u32 s96, 0x5bc1000, s95\n" \
"s_lshl_b32 s88, s101, 2\n" \
"v_mfma_f32_16x16x32_bf16 a[4:7], v[124:127], v[52:55], a[4:7]\n" \
"s_add_u32 s89, s96, s88\n" \
"s_lshl_b32 s95, s94, 3\n" \
"v_mfma_f32_16x16x32_bf16 a[36:39], v[124:127], v[60:63], a[36:39]\n" \
"v_writelane_b32 v11, s89, 0\n" \
"s_add_u32 s99, s95, s97\n" \
"v_mfma_f32_16x16x32_bf16 a[36:39], v[128:131], v[64:67], a[36:39]\n" \
"v_writelane_b32 v11, s96, 1\n" \
"s_lshl_b32 s88, s99, 10\n" \
"v_mfma_f32_16x16x32_bf16 a[68:71], v[128:131], v[72:75], a[68:71]\n" \
"s_add_u32 s89, 0x5c00000, s88\n" \
"s_lshl_b32 s96, %[w], 8\n" \
"v_mfma_f32_16x16x32_bf16 a[68:71], v[124:127], v[68:71], a[68:71]\n" \
"s_add_u32 s95, s89, s96\n" \
"s_or_b32 s99, 0x200, s95\n" \
"v_mfma_f32_16x16x32_bf16 a[100:103], v[124:127], v[76:79], a[100:103]\n" \
"s_lshl_b32 s88, s100, 2\n" \
"v_writelane_b32 v11, s99, 2\n" \
"v_mfma_f32_16x16x32_bf16 a[100:103], v[128:131], v[80:83], a[100:103]\n" \
"s_add_u32 s89, s95, s88\n" \
"s_waitcnt lgkmcnt(0)\n" \
"v_mfma_f32_16x16x32_bf16 a[8:11], v[136:139], v[56:59], a[8:11]\n" \
"v_writelane_b32 v11, s89, 3\n" \
"s_add_u32 s96, 0x80, s95\n" \
"v_mfma_f32_16x16x32_bf16 a[8:11], v[132:135], v[52:55], a[8:11]\n" \
"s_lshl_b32 s88, s101, 8\n" \
"v_writelane_b32 v11, s96, 4\n" \
"v_mfma_f32_16x16x32_bf16 a[40:43], v[132:135], v[60:63], a[40:43]\n" \
"s_add_u32 s99, 0x80, s89\n" \
"s_add_u32 s42, 0x5be8000, s88\n" \
"v_mfma_f32_16x16x32_bf16 a[40:43], v[136:139], v[64:67], a[40:43]\n" \
"s_lshl_b32 s95, s94, 2\n" \
"v_writelane_b32 v11, s99, 5\n" \
"v_mfma_f32_16x16x32_bf16 a[72:75], v[136:139], v[72:75], a[72:75]\n" \
"s_add_u32 s96, s42, s95\n" \
"s_add_u32 s89, s95, s100\n" \
"v_mfma_f32_16x16x32_bf16 a[72:75], v[132:135], v[68:71], a[72:75]\n" \
"s_cmp_eq_u32 %[w], 3\n" \
"s_cselect_b32 s41, s96, 0x40000000\n" \
"v_mfma_f32_16x16x32_bf16 a[104:107], v[132:135], v[76:79], a[104:107]\n" \
"s_mul_i32 s99, 0x90000, s89\n" \
"s_add_u32 s101, 0x4240000, s99\n" \
"v_mfma_f32_16x16x32_bf16 a[104:107], v[136:139], v[80:83], a[104:107]\n" \
"s_lshl_b32 s88, %[w], 14\n" \
"s_add_u32 s94, s101, s88\n" \
"v_mfma_f32_16x16x32_bf16 a[12:15], v[144:147], v[56:59], a[12:15]\n" \
"s_add_u32 s62, %[rc], 0x180\n" \
"s_and_b32 s62, s62, 0x3ff\n" \
"v_mfma_f32_16x16x32_bf16 a[12:15], v[140:143], v[52:55], a[12:15]\n" \
"s_lshl_b32 s63, s62, 12\n" \
"s_lshl_b32 s96, s97, 16\n" \
"v_mfma_f32_16x16x32_bf16 a[44:47], v[140:143], v[60:63], a[44:47]\n" \
"v_writelane_b32 v11, s42, 53\n" \
"s_add_u32 s95, s94, s96\n" \
"v_mfma_f32_16x16x32_bf16 a[44:47], v[144:147], v[64:67], a[44:47]\n" \
"s_lshl_b32 s89, s97, 11\n" \
"v_writelane_b32 v11, s95, 38\n" \
"v_mfma_f32_16x16x32_bf16 a[76:79], v[144:147], v[72:75], a[76:79]\n" \
"s_add_u32 s100, 0x3800, s95\n" \
"s_add_u32 s99, s94, s89\n" \
"v_mfma_f32_16x16x32_bf16 a[76:79], v[140:143], v[68:71], a[76:79]\n" \
"v_writelane_b32 v11, s100, 52\n" \
"s_add_u32 s101, 0x80000, s99\n" \
"v_mfma_f32_16x16x32_bf16 a[108:111], v[140:143], v[76:79], a[108:111]\n" \
"s_lshl_b32 s88, %[w], 17\n" \
"v_writelane_b32 v11, s101, 50\n" \
"v_mfma_f32_16x16x32_bf16 a[108:111], v[144:147], v[80:83], a[108:111]\n" \
"s_add_u32 s96, s94, s88\n" \
"s_and_b32 s99, 1, s97\n" \
"s_waitcnt vmcnt(1)\n" \
"s_barrier\n" \
"v_mfma_f32_16x16x32_bf16 a[128:131], v[120:123], v[88:91], a[128:131]\n" \
"s_add_u32 m0, %[mA], 0x8000\n" \
"s_add_u32 s85, s62, 0x0\n" \
"v_mfma_f32_16x16x32_bf16 a[128:131], v[116:119], v[84:87], a[128:131]\n" \
"buffer_load_dwordx4 v22, s[64:67], s85 offen lds\n" \
"v_mfma_f32_16x16x32_bf16 a[160:163], v[116:119], v[92:95], a[160:163]\n" \
"ds_read_b128 v[56:59], v29 offset:16384\n" \
"ds_read_b128 v[52:55], v28 offset:16384\n" \
"v_mfma_f32_16x16x32_bf16 a[160:163], v[120:123], v[96:99], a[160:163]\n" \
"s_add_u32 m0, %[mA], 0x8400\n" \
"s_add_u32 s84, s62, 0x2000\n" \
"v_mfma_f32_16x16x32_bf16 a[192:195], v[120:123], v[104:107], a[192:195]\n" \
"buffer_load_dwordx4 v23, s[64:67], s84 offen lds\n" \
"v_mfma_f32_16x16x32_bf16 a[192:195], v[116:119], v[100:103], a[192:195]\n" \
"ds_read_b128 v[60:63], v28 offset:18432\n" \
"ds_read_b128 v[64:67], v29 offset:18432\n" \
"v_mfma_f32_16x16x32_bf16 a[224:227], v[116:119], v[108:111], a[224:227]\n" \
"ds_read_b64_tr_b16 v[116:117], v32 offset:0\n" \
"ds_read_b64_tr_b16 v[118:119], v32 offset:1024\n" \
"v_mfma_f32_16x16x32_bf16 a[224:227], v[120:123], v[112:115], a[224:227]\n" \
"s_add_u32 m0, %[mB], 0x8000\n" \
"s_add_u32 s85, s63, 0x0\n" \
"v_mfma_f32_16x16x32_bf16 a[132:135], v[128:131], v[88:91], a[132:135]\n" \
"buffer_load_dwordx4 v26, s[68:71], s85 offen lds\n" \
"v_mfma_f32_16x16x32_bf16 a[132:135], v[124:127], v[84:87], a[132:135]\n" \
"ds_read_b64_tr_b16 v[120:121], v32 offset:8192\n" \
"ds_read_b64_tr_b16 v[122:123], v32 offset:9216\n" \
"v_mfma_f32_16x16x32_bf16 a[164:167], v[124:127], v[92:95], a[164:167]\n" \
"ds_read_b128 v[72:75], v29 offset:20480\n" \
"ds_read_b128 v[68:71], v28 offset:20480\n" \
"v_mfma_f32_16x16x32_bf16 a[164:167], v[128:131], v[96:99], a[164:167]\n" \
"s_add_u32 m0, %[mB], 0xc000\n" \
"s_add_u32 s84, s63, 0x0\n" \
"v_mfma_f32_16x16x32_bf16 a[196:199], v[128:131], v[104:107], a[196:199]\n" \
"buffer_load_dwordx4 v26, s[72:75], s84 offen lds\n" \
"v_mfma_f32_16x16x32_bf16 a[196:199], v[124:127], v[100:103], a[196:199]\n" \
"ds_read_b128 v[76:79], v28 offset:22528\n" \
"ds_read_b128 v[80:83], v29 offset:22528\n" \
"v_mfma_f32_16x16x32_bf16 a[228:231], v[124:127], v[108:111], a[228:231]\n" \
"ds_read_b64_tr_b16 v[124:125], v33 offset:0\n" \
"ds_read_b64_tr_b16 v[126:127], v33 offset:1024\n" \
"v_mfma_f32_16x16x32_bf16 a[228:231], v[128:131], v[112:115], a[228:231]\n" \
"s_add_u32 m0, %[mA], 0x8800\n" \
"s_add_u32 s85, s62, 0x4000\n" \
"v_mfma_f32_16x16x32_bf16 a[136:139], v[136:139], v[88:91], a[136:139]\n" \
"buffer_load_dwordx4 v22, s[64:67], s85 offen lds\n" \
"v_mfma_f32_16x16x32_bf16 a[136:139], v[132:135], v[84:87], a[136:139]\n" \
"ds_read_b64_tr_b16 v[128:129], v33 offset:8192\n" \
"ds_read_b64_tr_b16 v[130:131], v33 offset:9216\n" \
"v_mfma_f32_16x16x32_bf16 a[168:171], v[132:135], v[92:95], a[168:171]\n" \
"s_add_u32 s95, s96, s98\n" \
"s_lshl_b32 s101, s99, 10\n" \
"v_mfma_f32_16x16x32_bf16 a[168:171], v[136:139], v[96:99], a[168:171]\n" \
"s_add_u32 m0, %[mA], 0x8c00\n" \
"s_add_u32 s84, s62, 0x6000\n" \
"v_mfma_f32_16x16x32_bf16 a[200:203], v[136:139], v[104:107], a[200:203]\n" \
"buffer_load_dwordx4 v23, s[64:67], s84 offen lds\n" \
"v_mfma_f32_16x16x32_bf16 a[200:203], v[132:135], v[100:103], a[200:203]\n" \
"s_add_u32 s100, 0x3840, s95\n" \
"s_add_u32 s88, s94, s101\n" \
"s_nop 0\n" \
"v_mfma_f32_16x16x32_bf16 a[232:235], v[132:135], v[108:111], a[232:235]\n" \
"ds_read_b64_tr_b16 v[132:133], v34 offset:0\n" \
"ds_read_b64_tr_b16 v[134:135], v34 offset:1024\n" \
"v_mfma_f32_16x16x32_bf16 a[232:235], v[136:139], v[112:115], a[232:235]\n" \
"s_add_u32 m0, %[mB], 0x8400\n" \
"s_add_u32 s85, s63, 0x8000\n" \
"v_mfma_f32_16x16x32_bf16 a[140:143], v[144:147], v[88:91], a[140:143]\n" \
"buffer_load_dwordx4 v26, s[68:71], s85 offen lds\n" \
"v_mfma_f32_16x16x32_bf16 a[140:143], v[140:143], v[84:87], a[140:143]\n" \
"ds_read_b64_tr_b16 v[136:137], v34 offset:8192\n" \
"ds_read_b64_tr_b16 v[138:139], v34 offset:9216\n" \
"v_mfma_f32_16x16x32_bf16 a[172:175], v[140:143], v[92:95], a[172:175]\n" \
"v_writelane_b32 v11, s100, 6\n" \
"s_lshr_b32 s89, s97, 1\n" \
"v_mfma_f32_16x16x32_bf16 a[172:175], v[144:147], v[96:99], a[172:175]\n" \
"s_add_u32 m0, %[mB], 0xc400\n" \
"s_add_u32 s84, s63, 0x8000\n" \
"v_mfma_f32_16x16x32_bf16 a[204:207], v[144:147], v[104:107], a[204:207]\n" \
"buffer_load_dwordx4 v26, s[72:75], s84 offen lds\n" \
"v_mfma_f32_16x16x32_bf16 a[204:207], v[140:143], v[100:103], a[204:207]\n" \
"s_add_u32 s96, 0x80000, s88\n" \
"s_add_u32 s95, 1, s97\n" \
"v_mfma_f32_16x16x32_bf16 a[236:239], v[140:143], v[108:111], a[236:239]\n" \
"ds_read_b64_tr_b16 v[140:141], v35 offset:0\n" \
"ds_read_b64_tr_b16 v[142:143], v35 offset:1024\n" \
"v_mfma_f32_16x16x32_bf16 a[236:239], v[144:147], v[112:115], a[236:239]\n" \
"s_add_u32 m0, %[mA], 0x9000\n" \
"s_add_u32 s85, s62, 0x8000\n" \
"v_mfma_f32_16x16x32_bf16 a[144:147], v[88:91], v[152:155], a[144:147]\n" \
"buffer_load_dwordx4 v22, s[64:67], s85 offen lds\n" \
"v_mfma_f32_16x16x32_bf16 a[144:147], v[84:87], v[148:151], a[144:147]\n" \
"ds_read_b64_tr_b16 v[144:145], v35 offset:8192\n" \
"ds_read_b64_tr_b16 v[146:147], v35 offset:9216\n" \
"v_mfma_f32_16x16x32_bf16 a[176:179], v[92:95], v[148:151], a[176:179]\n" \
"s_cmp_eq_u32 %[w], s89\n" \
"s_cselect_b32 s98, s96, 0x40000000\n" \
"v_mfma_f32_16x16x32_bf16 a[176:179], v[96:99], v[152:155], a[176:179]\n" \
"s_add_u32 m0, %[mA], 0x9400\n" \
"s_add_u32 s84, s62, 0xa000\n" \
"v_mfma_f32_16x16x32_bf16 a[208:211], v[104:107], v[152:155], a[208:211]\n" \
"buffer_load_dwordx4 v23, s[64:67], s84 offen lds\n" \
"v_mfma_f32_16x16x32_bf16 a[208:211], v[100:103], v[148:151], a[208:211]\n" \
"s_and_b32 s100, 7, s95\n" \
"s_lshl_b32 s99, s100, 16\n" \
"v_mfma_f32_16x16x32_bf16 a[240:243], v[108:111], v[148:151], a[240:243]\n" \
"ds_read_b64_tr_b16 v[148:149], v32 offset:16384\n" \
"ds_read_b64_tr_b16 v[150:151], v32 offset:17408\n" \
"v_mfma_f32_16x16x32_bf16 a[240:243], v[112:115], v[152:155], a[240:243]\n" \
"s_add_u32 m0, %[mB], 0x8800\n" \
"s_add_u32 s85, s63, 0x10000\n" \
"v_mfma_f32_16x16x32_bf16 a[148:151], v[88:91], v[160:163], a[148:151]\n" \
"buffer_load_dwordx4 v27, s[68:71], s85 offen lds\n" \
"v_mfma_f32_16x16x32_bf16 a[148:151], v[84:87], v[156:159], a[148:151]\n" \
"ds_read_b64_tr_b16 v[152:153], v32 offset:24576\n" \
"ds_read_b64_tr_b16 v[154:155], v32 offset:25600\n" \
"v_mfma_f32_16x16x32_bf16 a[180:183], v[92:95], v[156:159], a[180:183]\n" \
"s_add_u32 s88, 2, s97\n" \
"v_writelane_b32 v11, s98, 39\n" \
"v_mfma_f32_16x16x32_bf16 a[180:183], v[96:99], v[160:163], a[180:183]\n" \
"s_add_u32 m0, %[mB], 0xc800\n" \
"s_add_u32 s84, s63, 0x10000\n" \
"v_mfma_f32_16x16x32_bf16 a[212:215], v[104:107], v[160:163], a[212:215]\n" \
"buffer_load_dwordx4 v27, s[72:75], s84 offen lds\n" \
"v_mfma_f32_16x16x32_bf16 a[212:215], v[100:103], v[156:159], a[212:215]\n" \
"s_add_u32 s101, s94, s99\n" \
"s_and_b32 s89, 7, s88\n" \
"v_mfma_f32_16x16x32_bf16 a[244:247], v[108:111], v[156:159], a[244:247]\n" \
"ds_read_b64_tr_b16 v[156:157], v33 offset:16384\n" \
"ds_read_b64_tr_b16 v[158:159], v33 offset:17408\n" \
"v_mfma_f32_16x16x32_bf16 a[244:247], v[112:115], v[160:163], a[244:247]\n" \
"ds_read_b64_tr_b16 v[160:161], v33 offset:24576\n" \
"ds_read_b64_tr_b16 v[162:163], v33 offset:25600\n" \
"v_mfma_f32_16x16x32_bf16 a[152:155], v[88:91], v[168:171], a[152:155]\n" \
"s_add_u32 s100, 3, s97\n" \
"v_writelane_b32 v11, s101, 43\n" \
"v_mfma_f32_16x16x32_bf16 a[152:155], v[84:87], v[164:167], a[152:155]\n" \
"s_lshl_b32 s96, s89, 16\n" \
"s_and_b32 s99, 7, s100\n" \
"v_mfma_f32_16x16x32_bf16 a[184:187], v[92:95], v[164:167], a[184:187]\n" \
"s_add_u32 s98, s94, s96\n" \
"s_lshl_b32 s101, s99, 16\n" \
"v_mfma_f32_16x16x32_bf16 a[184:187], v[96:99], v[168:171], a[184:187]\n" \
"s_add_u32 s95, 0x800, s98\n" \
"s_add_u32 s88, s94, s101\n" \
"v_mfma_f32_16x16x32_bf16 a[216:219], v[104:107], v[168:171], a[216:219]\n" \
"s_add_u32 s96, 4, s97\n" \
"v_writelane_b32 v11, s95, 44\n" \
"v_mfma_f32_16x16x32_bf16 a[216:219], v[100:103], v[164:167], a[216:219]\n" \
"s_add_u32 s89, 0x1000, s88\n" \
"s_and_b32 s98, 7, s96\n" \
"v_mfma_f32_16x16x32_bf16 a[248:251], v[108:111], v[164:167], a[248:251]\n" \
"ds_read_b64_tr_b16 v[164:165], v34 offset:16384\n" \
"ds_read_b64_tr_b16 v[166:167], v34 offset:17408\n" \
"v_mfma_f32_16x16x32_bf16 a[248:251], v[112:115], v[168:171], a[248:251]\n" \
"ds_read_b64_tr_b16 v[168:169], v34 offset:24576\n" \
"ds_read_b64_tr_b16 v[170:171], v34 offset:25600\n" \
"v_mfma_f32_16x16x32_bf16 a[156:159], v[88:91], v[176:179], a[156:159]\n" \
"ds_read_b128 v[88:91], v29 offset:0\n" \
"s_add_u32 s101, 5, s97\n" \
"v_mfma_f32_16x16x32_bf16 a[156:159], v[84:87], v[172:175], a[156:159]\n" \
"ds_read_b128 v[84:87], v28 offset:0\n" \
"v_writelane_b32 v11, s89, 45\n" \
"v_mfma_f32_16x16x32_bf16 a[188:191], v[92:95], v[172:175], a[188:191]\n" \
"ds_read_b128 v[92:95], v28 offset:2048\n" \
"s_lshl_b32 s95, s98, 16\n" \
"v_mfma_f32_16x16x32_bf16 a[188:191], v[96:99], v[176:179], a[188:191]\n" \
"ds_read_b128 v[96:99], v29 offset:2048\n" \
"s_and_b32 s88, 7, s101\n" \
"v_mfma_f32_16x16x32_bf16 a[220:223], v[104:107], v[176:179], a[220:223]\n" \
"ds_read_b128 v[104:107], v29 offset:4096\n" \
"s_add_u32 s100, s94, s95\n" \
"v_mfma_f32_16x16x32_bf16 a[220:223], v[100:103], v[172:175], a[220:223]\n" \
"ds_read_b128 v[100:103], v28 offset:4096\n" \
"s_lshl_b32 s89, s88, 16\n" \
"v_mfma_f32_16x16x32_bf16 a[252:255], v[108:111], v[172:175], a[252:255]\n" \
"ds_read_b64_tr_b16 v[172:173], v35 offset:16384\n" \
"ds_read_b64_tr_b16 v[174:175], v35 offset:17408\n" \
"v_mfma_f32_16x16x32_bf16 a[252:255], v[112:115], v[176:179], a[252:255]\n" \
"s_add_u32 m0, %[mA], 0x9800\n" \
"s_waitcnt lgkmcnt(12)\n" \
"s_nop 0\n" \
"v_mfma_f32_16x16x32_bf16 a[0:3], v[120:123], v[56:59], a[0:3]\n" \
"s_add_u32 s85, s62, 0xc000\n" \
"ds_read_b64_tr_b16 v[176:177], v35 offset:24576\n" \
"v_mfma_f32_16x16x32_bf16 a[0:3], v[116:119], v[52:55], a[0:3]\n" \
"buffer_load_dwordx4 v22, s[64:67], s85 offen lds\n" \
"v_mfma_f32_16x16x32_bf16 a[32:35], v[116:119], v[60:63], a[32:35]\n" \
"s_add_u32 m0, %[mA], 0x9c00\n" \
"s_add_u32 s84, s62, 0xe000\n" \
"v_mfma_f32_16x16x32_bf16 a[32:35], v[120:123], v[64:67], a[32:35]\n" \
"ds_read_b64_tr_b16 v[178:179], v35 offset:25600\n" \
"ds_read_b128 v[108:111], v28 offset:6144\n" \
"v_mfma_f32_16x16x32_bf16 a[64:67], v[120:123], v[72:75], a[64:67]\n" \
"buffer_load_dwordx4 v23, s[64:67], s84 offen lds\n" \
"v_mfma_f32_16x16x32_bf16 a[64:67], v[116:119], v[68:71], a[64:67]\n" \
"s_add_u32 m0, %[mB], 0x8c00\n" \
"s_add_u32 s85, s63, 0x18000\n" \
"v_mfma_f32_16x16x32_bf16 a[96:99], v[116:119], v[76:79], a[96:99]\n" \
"ds_read_b128 v[112:115], v29 offset:6144\n" \
"s_add_u32 s99, 0x1800, s100\n" \
"v_mfma_f32_16x16x32_bf16 a[96:99], v[120:123], v[80:83], a[96:99]\n" \
"buffer_load_dwordx4 v27, s[68:71], s85 offen lds\n" \
"v_mfma_f32_16x16x32_bf16 a[4:7], v[128:131], v[56:59], a[4:7]\n" \
"s_add_u32 m0, %[mB], 0xcc00\n" \
"s_add_u32 s84, s63, 0x18000\n" \
"v_mfma_f32_16x16x32_bf16 a[4:7], v[124:127], v[52:55], a[4:7]\n" \
"s_add_u32 s96, s94, s89\n" \
"s_add_u32 s95, 6, s97\n" \
"v_mfma_f32_16x16x32_bf16 a[36:39], v[124:127], v[60:63], a[36:39]\n" \
"buffer_load_dwordx4 v27, s[72:75], s84 offen lds\n" \
"v_mfma_f32_16x16x32_bf16 a[36:39], v[128:131], v[64:67], a[36:39]\n" \
"v_writelane_b32 v11, s99, 46\n" \
"s_add_u32 s98, 0x2000, s96\n" \
"v_mfma_f32_16x16x32_bf16 a[68:71], v[128:131], v[72:75], a[68:71]\n" \
"s_and_b32 s100, 7, s95\n" \
"s_add_u32 s89, 7, s97\n" \
"v_mfma_f32_16x16x32_bf16 a[68:71], v[124:127], v[68:71], a[68:71]\n" \
"buffer_load_dwordx4 v[200:203], v192, s[76:79], s37 offen offset:0\n" \
"v_mfma_f32_16x16x32_bf16 a[100:103], v[124:127], v[76:79], a[100:103]\n" \
"v_writelane_b32 v11, s98, 47\n" \
"s_lshl_b32 s99, s100, 16\n" \
"v_mfma_f32_16x16x32_bf16 a[100:103], v[128:131], v[80:83], a[100:103]\n" \
"s_and_b32 s96, 7, s89\n" \
"s_add_u32 s101, s94, s99\n" \
"v_mfma_f32_16x16x32_bf16 a[8:11], v[136:139], v[56:59], a[8:11]\n" \
"s_lshl_b32 s98, s96, 16\n" \
"s_add_u32 s88, 0x2800, s101\n" \
"v_mfma_f32_16x16x32_bf16 a[8:11], v[132:135], v[52:55], a[8:11]\n" \
"s_add_u32 s95, s94, s98\n" \
"v_writelane_b32 v11, s88, 48\n" \
"v_mfma_f32_16x16x32_bf16 a[40:43], v[132:135], v[60:63], a[40:43]\n" \
"buffer_load_dwordx4 v[204:207], v193, s[76:79], s37 offen offset:0\n" \
"v_mfma_f32_16x16x32_bf16 a[40:43], v[136:139], v[64:67], a[40:43]\n" \
"s_add_u32 s100, 0x3000, s95\n" \
"v_writelane_b32 v11, s100, 49\n" \
"v_mfma_f32_16x16x32_bf16 a[72:75], v[136:139], v[72:75], a[72:75]\n" \
"v_cmp_gt_u32_e32 vcc, 0x1f8, v228\n" \
"v_and_b32_e32 v247, 7, v250\n" \
"v_mfma_f32_16x16x32_bf16 a[72:75], v[132:135], v[68:71], a[72:75]\n" \
"v_cndmask_b32_e32 v197, v219, v238, vcc\n" \
"v_cmp_gt_u32_e32 vcc, 18, v212\n" \
"v_mfma_f32_16x16x32_bf16 a[104:107], v[132:135], v[76:79], a[104:107]\n" \
"v_add_u32_e32 v216, v214, v247\n" \
"v_cndmask_b32_e32 v198, v219, v218, vcc\n" \
"v_mfma_f32_16x16x32_bf16 a[104:107], v[136:139], v[80:83], a[104:107]\n" \
"v_cmp_gt_u32_e32 vcc, 36, v212\n" \
"v_readfirstlane_b32 %[tag], v180\n" \
"v_mfma_f32_16x16x32_bf16 a[12:15], v[144:147], v[56:59], a[12:15]\n" \
"buffer_load_dword v208, v194, s[76:79], s37 offen offset:32\n" \
"v_mfma_f32_16x16x32_bf16 a[12:15], v[140:143], v[52:55], a[12:15]\n" \
"v_cndmask_b32_e32 v215, v246, v216, vcc\n" \
"v_lshlrev_b32_e32 v188, 7, v215\n" \
"v_mfma_f32_16x16x32_bf16 a[44:47], v[140:143], v[60:63], a[44:47]\n" \
"s_add_u32 %[tag], %[tag], 0x9E3779B1\n" \
"v_mov_b32_e64 v45, %[tag]\n" \
"v_mfma_f32_16x16x32_bf16 a[44:47], v[144:147], v[64:67], a[44:47]\n" \
"s_lshr_b32 s94, %[b], 5\n" \
"s_sub_u32 s95, 15, s94\n" \
"v_mfma_f32_16x16x32_bf16 a[76:79], v[144:147], v[72:75], a[76:79]\n" \
"s_cmp_lt_u32 %[w], 2\n" \
"s_cselect_b32 s96, s95, s94\n" \
"v_mfma_f32_16x16x32_bf16 a[76:79], v[140:143], v[68:71], a[76:79]\n" \
"s_lshl_b32 s97, s96, 7\n" \
"s_add_u32 s98, 0xfffffff0, s97\n" \
"v_mfma_f32_16x16x32_bf16 a[108:111], v[140:143], v[76:79], a[108:111]\n" \
"buffer_load_dword v209, v194, s[76:79], s37 offen offset:1184\n" \
"v_mfma_f32_16x16x32_bf16 a[108:111], v[144:147], v[80:83], a[108:111]\n" \
"s_cmp_eq_u32 s97, 0\n" \
"s_cselect_b32 s99, 0, s98\n" \
"v_mfma_f32_16x16x32_bf16 a[16:19], v[56:59], v[152:155], a[16:19]\n" \
"s_and_b32 s100, 1, %[w]\n" \
"s_lshl_b32 s101, s100, 3\n" \
"v_mfma_f32_16x16x32_bf16 a[16:19], v[52:55], v[148:151], a[16:19]\n" \
"s_add_u32 s88, s99, s101\n" \
"v_lshrrev_b32_e32 v212, 4, v20\n" \
"v_mfma_f32_16x16x32_bf16 a[48:51], v[60:63], v[148:151], a[48:51]\n" \
"v_lshrrev_b32_e32 v213, 3, v212\n" \
"s_add_u32 s99, s97, s100\n" \
"v_mfma_f32_16x16x32_bf16 a[48:51], v[64:67], v[152:155], a[48:51]\n" \
"v_add_u32_e32 v214, s88, v213\n" \
"s_lshl_b32 s89, s100, 2\n" \
"v_mfma_f32_16x16x32_bf16 a[80:83], v[72:75], v[152:155], a[80:83]\n" \
"buffer_load_dword v211, v194, s[76:79], s37 offen offset:2336\n" \
"v_mfma_f32_16x16x32_bf16 a[80:83], v[68:71], v[148:151], a[80:83]\n" \
"s_mov_b32 s76, %[gwl]\n" \
"s_and_b32 s77, %[gwh], 0xffff\n" \
"v_mfma_f32_16x16x32_bf16 a[112:115], v[76:79], v[148:151], a[112:115]\n" \
"s_mov_b32 s78, 0x480\n" \
"s_add_u32 s37, s39, 0x1000\n" \
"v_mfma_f32_16x16x32_bf16 a[112:115], v[80:83], v[152:155], a[112:115]\n" \
"v_lshrrev_b32_e32 v217, 4, v212\n" \
"s_lshl_b32 s96, s100, 6\n" \
"v_mfma_f32_16x16x32_bf16 a[20:23], v[56:59], v[160:163], a[20:23]\n" \
"s_cmp_eq_u32 s99, 0\n" \
"s_cselect_b32 s55, 0xff800000, 0\n" \
"v_mfma_f32_16x16x32_bf16 a[20:23], v[52:55], v[156:159], a[20:23]\n" \
"s_lshr_b32 s101, s97, 4\n" \
"v_add_u32_e32 v218, s89, v217\n" \
"v_mfma_f32_16x16x32_bf16 a[52:55], v[60:63], v[156:159], a[52:55]\n" \
"buffer_load_ushort v210, v195, s[76:79], 0 offen\n" \
"v_mfma_f32_16x16x32_bf16 a[52:55], v[64:67], v[160:163], a[52:55]\n" \
"s_add_u32 s98, s97, s96\n" \
"s_add_u32 s88, s101, s89\n" \
"v_mfma_f32_16x16x32_bf16 a[84:87], v[72:75], v[160:163], a[84:87]\n" \
"v_add_u32_e64 v221, s98, v213\n" \
"s_mul_i32 s96, 0x24800, s88\n" \
"v_mfma_f32_16x16x32_bf16 a[84:87], v[68:71], v[156:159], a[84:87]\n" \
"s_and_b32 s99, 7, %[b]\n" \
"s_lshr_b32 s89, %[b], 3\n" \
"v_mfma_f32_16x16x32_bf16 a[116:119], v[76:79], v[156:159], a[116:119]\n" \
"s_add_u32 s98, 0x3000000, s96\n" \
"s_lshl_b32 s97, s99, 8\n" \
"v_mfma_f32_16x16x32_bf16 a[116:119], v[80:83], v[160:163], a[116:119]\n" \
"s_and_b32 s88, 3, s89\n" \
"s_waitcnt lgkmcnt(0)\n" \
"v_mfma_f32_16x16x32_bf16 a[24:27], v[56:59], v[168:171], a[24:27]\n" \
"s_add_u32 s101, s98, s97\n" \
"s_lshl_b32 s96, s88, 2\n" \
"v_mfma_f32_16x16x32_bf16 a[24:27], v[52:55], v[164:167], a[24:27]\n" \
"s_add_u32 s97, s101, s96\n" \
"s_lshl_b32 s101, s99, 2\n" \
"v_mfma_f32_16x16x32_bf16 a[56:59], v[60:63], v[164:167], a[56:59]\n" \
"v_and_b32_e32 v216, 7, v212\n" \
"s_add_u32 s57, 0x4000, s97\n" \
"v_mfma_f32_16x16x32_bf16 a[56:59], v[64:67], v[168:171], a[56:59]\n" \
"s_add_u32 s96, s101, s88\n" \
"v_xor_b32_e32 v219, v216, v218\n" \
"v_mfma_f32_16x16x32_bf16 a[88:91], v[72:75], v[168:171], a[88:91]\n" \
"s_lshl_b32 s97, s96, 9\n" \
"s_lshl_b32 s101, s89, 2\n" \
"v_mfma_f32_16x16x32_bf16 a[88:91], v[68:71], v[164:167], a[88:91]\n" \
"v_mul_u32_u24_e32 v215, 0x480, v214\n" \
"v_lshlrev_b32_e32 v220, 4, v219\n" \
"v_mfma_f32_16x16x32_bf16 a[120:123], v[76:79], v[164:167], a[120:123]\n" \
"v_lshrrev_b32_e32 v229, 2, v212\n" \
"v_and_b32_e64 v234, 3, v212\n" \
"v_mfma_f32_16x16x32_bf16 a[120:123], v[80:83], v[168:171], a[120:123]\n" \
"s_add_u32 s56, s98, s97\n" \
"s_lshl_b32 s99, %[w], 10\n" \
"v_mfma_f32_16x16x32_bf16 a[28:31], v[56:59], v[176:179], a[28:31]\n" \
"s_add_u32 s62, %[rc], 0x200\n" \
"s_and_b32 s62, s62, 0x3ff\n" \
"v_mfma_f32_16x16x32_bf16 a[28:31], v[52:55], v[172:175], a[28:31]\n" \
"s_lshl_b32 s63, s62, 12\n" \
"s_add_u32 s88, s101, %[w]\n" \
"v_mfma_f32_16x16x32_bf16 a[60:63], v[60:63], v[172:175], a[60:63]\n" \
"v_add_u32_e32 v21, v215, v220\n" \
"v_add_u32_e32 v225, 4, v217\n" \
"v_mfma_f32_16x16x32_bf16 a[60:63], v[64:67], v[176:179], a[60:63]\n" \
"s_add_u32 s43, 0x14000, s99\n" \
"s_lshl_b32 s44, s88, 7\n" \
"v_mfma_f32_16x16x32_bf16 a[92:95], v[72:75], v[176:179], a[92:95]\n" \
"s_lshl_b32 s98, s96, 20\n" \
"v_lshrrev_b32_e32 v212, 4, v20\n" \
"v_mfma_f32_16x16x32_bf16 a[92:95], v[68:71], v[172:175], a[92:95]\n" \
"v_xor_b32_e32 v223, v216, v217\n" \
"v_xor_b32_e64 v226, v216, v225\n" \
"v_mfma_f32_16x16x32_bf16 a[124:127], v[76:79], v[172:175], a[124:127]\n" \
"s_add_u32 s97, 0x1000000, s98\n" \
"s_lshl_b32 s99, %[w], 12\n" \
"v_mfma_f32_16x16x32_bf16 a[124:127], v[80:83], v[176:179], a[124:127]\n" \
"s_lshl_b32 s89, %[w], 11\n" \
"s_lshr_b32 s88, %[w], 1\n" \
"s_waitcnt vmcnt(6)\n" \
"s_barrier\n" \
"v_mfma_f32_16x16x32_bf16 a[144:147], v[88:91], v[152:155], a[144:147]\n" \
"s_add_u32 m0, %[mA], 0x0\n" \
"s_add_u32 s85, s62, 0x0\n" \
"s_nop 0\n" \
"v_mfma_f32_16x16x32_bf16 a[144:147], v[84:87], v[148:151], a[144:147]\n" \
"buffer_load_dwordx4 v22, s[64:67], s85 offen lds\n" \
"v_mfma_f32_16x16x32_bf16 a[176:179], v[92:95], v[148:151], a[176:179]\n" \
"ds_read_b128 v[56:59], v29 offset:49152\n" \
"ds_read_b128 v[52:55], v28 offset:49152\n" \
"v_mfma_f32_16x16x32_bf16 a[176:179], v[96:99], v[152:155], a[176:179]\n" \
"L_pf1:\n" \
"s_cbranch_execz L_pf5\n" \
"s_nop 0\n" \
"s_add_u32 m0, %[mA], 0x400\n" \
"s_add_u32 s84, s62, 0x2000\n" \
"v_mfma_f32_16x16x32_bf16 a[208:211], v[104:107], v[152:155], a[208:211]\n" \
"buffer_load_dwordx4 v23, s[64:67], s84 offen lds\n" \
"v_mfma_f32_16x16x32_bf16 a[208:211], v[100:103], v[148:151], a[208:211]\n" \
"ds_read_b128 v[60:63], v28 offset:51200\n" \
"ds_read_b128 v[64:67], v29 offset:51200\n" \
"v_mfma_f32_16x16x32_bf16 a[240:243], v[108:111], v[148:151], a[240:243]\n" \
"ds_read_b64_tr_b16 v[148:149], v32 offset:49152\n" \
"ds_read_b64_tr_b16 v[150:151], v32 offset:50176\n" \
"v_mfma_f32_16x16x32_bf16 a[240:243], v[112:115], v[152:155], a[240:243]\n" \
"s_add_u32 m0, %[mB], 0x0\n" \
"s_add_u32 s85, s63, 0x0\n" \
"v_mfma_f32_16x16x32_bf16 a[148:151], v[88:91], v[160:163], a[148:151]\n" \
"buffer_load_dwordx4 v26, s[68:71], s85 offen lds\n" \
"v_mfma_f32_16x16x32_bf16 a[148:151], v[84:87], v[156:159], a[148:151]\n" \
"ds_read_b64_tr_b16 v[152:153], v32 offset:57344\n" \
"ds_read_b64_tr_b16 v[154:155], v32 offset:58368\n" \
"v_mfma_f32_16x16x32_bf16 a[180:183], v[92:95], v[156:159], a[180:183]\n" \
"ds_read_b128 v[72:75], v29 offset:53248\n" \
"ds_read_b128 v[68:71], v28 offset:53248\n" \
"v_mfma_f32_16x16x32_bf16 a[180:183], v[96:99], v[160:163], a[180:183]\n" \
"s_add_u32 m0, %[mB], 0x4000\n" \
"s_add_u32 s84, s63, 0x0\n" \
"s_nop 0\n" \
"v_mfma_f32_16x16x32_bf16 a[212:215], v[104:107], v[160:163], a[212:215]\n" \
"buffer_load_dwordx4 v26, s[72:75], s84 offen lds\n" \
"v_mfma_f32_16x16x32_bf16 a[212:215], v[100:103], v[156:159], a[212:215]\n" \
"ds_read_b128 v[76:79], v28 offset:55296\n" \
"ds_read_b128 v[80:83], v29 offset:55296\n" \
"v_mfma_f32_16x16x32_bf16 a[244:247], v[108:111], v[156:159], a[244:247]\n" \
"ds_read_b64_tr_b16 v[156:157], v33 offset:49152\n" \
"ds_read_b64_tr_b16 v[158:159], v33 offset:50176\n" \
"v_mfma_f32_16x16x32_bf16 a[244:247], v[112:115], v[160:163], a[244:247]\n" \
"s_add_u32 m0, %[mA], 0x800\n" \
"s_add_u32 s85, s62, 0x4000\n" \
"v_mfma_f32_16x16x32_bf16 a[152:155], v[88:91], v[168:171], a[152:155]\n" \
"buffer_load_dwordx4 v22, s[64:67], s85 offen lds\n" \
"v_mfma_f32_16x16x32_bf16 a[152:155], v[84:87], v[164:167], a[152:155]\n" \
"ds_read_b64_tr_b16 v[160:161], v33 offset:57344\n" \
"ds_read_b64_tr_b16 v[162:163], v33 offset:58368\n" \
"v_mfma_f32_16x16x32_bf16 a[184:187], v[92:95], v[164:167], a[184:187]\n" \
"v_and_b32_e32 v215, 15, v212\n" \
"s_add_u32 s58, s97, s99\n" \
"v_mfma_f32_16x16x32_bf16 a[184:187], v[96:99], v[168:171], a[184:187]\n" \
"s_add_u32 m0, %[mA], 0xc00\n" \
"s_add_u32 s84, s62, 0x6000\n" \
"v_mfma_f32_16x16x32_bf16 a[216:219], v[104:107], v[168:171], a[216:219]\n" \
"buffer_load_dwordx4 v23, s[64:67], s84 offen lds\n" \
"v_mfma_f32_16x16x32_bf16 a[216:219], v[100:103], v[164:167], a[216:219]\n" \
"s_add_u32 s101, s97, s89\n" \
"s_lshl_b32 s96, s88, 15\n" \
"v_mfma_f32_16x16x32_bf16 a[248:251], v[108:111], v[164:167], a[248:251]\n" \
"ds_read_b64_tr_b16 v[164:165], v34 offset:49152\n" \
"ds_read_b64_tr_b16 v[166:167], v34 offset:50176\n" \
"v_mfma_f32_16x16x32_bf16 a[248:251], v[112:115], v[168:171], a[248:251]\n" \
"s_add_u32 m0, %[mB], 0x400\n" \
"s_add_u32 s85, s63, 0x8000\n" \
"v_mfma_f32_16x16x32_bf16 a[156:159], v[88:91], v[176:179], a[156:159]\n" \
"buffer_load_dwordx4 v26, s[68:71], s85 offen lds\n" \
"v_mfma_f32_16x16x32_bf16 a[156:159], v[84:87], v[172:175], a[156:159]\n" \
"ds_read_b64_tr_b16 v[168:169], v34 offset:57344\n" \
"ds_read_b64_tr_b16 v[170:171], v34 offset:58368\n" \
"v_mfma_f32_16x16x32_bf16 a[188:191], v[92:95], v[172:175], a[188:191]\n" \
"v_lshrrev_b32_e32 v213, 4, v212\n" \
"v_and_b32_e32 v216, 1, v215\n" \
"v_mfma_f32_16x16x32_bf16 a[188:191], v[96:99], v[176:179], a[188:191]\n" \
"s_add_u32 m0, %[mB], 0x4400\n" \
"s_add_u32 s84, s63, 0x8000\n" \
"v_mfma_f32_16x16x32_bf16 a[220:223], v[104:107], v[176:179], a[220:223]\n" \
"buffer_load_dwordx4 v26, s[72:75], s84 offen lds\n" \
"v_mfma_f32_16x16x32_bf16 a[220:223], v[100:103], v[172:175], a[220:223]\n" \
"v_lshlrev_b32_e32 v228, 3, v217\n" \
"v_and_b32_e32 v238, 1, v217\n" \
"v_mfma_f32_16x16x32_bf16 a[252:255], v[108:111], v[172:175], a[252:255]\n" \
"ds_read_b64_tr_b16 v[172:173], v35 offset:49152\n" \
"ds_read_b64_tr_b16 v[174:175], v35 offset:50176\n" \
"v_mfma_f32_16x16x32_bf16 a[252:255], v[112:115], v[176:179], a[252:255]\n" \
"s_add_u32 m0, %[mA], 0x1000\n" \
"s_add_u32 s85, s62, 0x8000\n" \
"v_mfma_f32_16x16x32_bf16 a[128:131], v[120:123], v[88:91], a[128:131]\n" \
"buffer_load_dwordx4 v22, s[64:67], s85 offen lds\n" \
"v_mfma_f32_16x16x32_bf16 a[128:131], v[116:119], v[84:87], a[128:131]\n" \
"ds_read_b64_tr_b16 v[176:177], v35 offset:57344\n" \
"ds_read_b64_tr_b16 v[178:179], v35 offset:58368\n" \
"v_mfma_f32_16x16x32_bf16 a[160:163], v[116:119], v[92:95], a[160:163]\n" \
"s_add_u32 s60, 0x10000, s101\n" \
"s_add_u32 s98, s97, s96\n" \
"v_mfma_f32_16x16x32_bf16 a[160:163], v[120:123], v[96:99], a[160:163]\n" \
"s_add_u32 m0, %[mA], 0x1400\n" \
"s_add_u32 s84, s62, 0xa000\n" \
"v_mfma_f32_16x16x32_bf16 a[192:195], v[120:123], v[104:107], a[192:195]\n" \
"buffer_load_dwordx4 v23, s[64:67], s84 offen lds\n" \
"v_mfma_f32_16x16x32_bf16 a[192:195], v[116:119], v[100:103], a[192:195]\n" \
"s_lshl_b32 s99, s100, 11\n" \
"v_lshlrev_b32_e32 v214, 2, v213\n" \
"s_nop 0\n" \
"v_mfma_f32_16x16x32_bf16 a[224:227], v[116:119], v[108:111], a[224:227]\n" \
"ds_read_b64_tr_b16 v[116:117], v32 offset:32768\n" \
"ds_read_b64_tr_b16 v[118:119], v32 offset:33792\n" \
"v_mfma_f32_16x16x32_bf16 a[224:227], v[120:123], v[112:115], a[224:227]\n" \
"s_add_u32 m0, %[mB], 0x800\n" \
"s_add_u32 s85, s63, 0x10000\n" \
"v_mfma_f32_16x16x32_bf16 a[132:135], v[128:131], v[88:91], a[132:135]\n" \
"buffer_load_dwordx4 v27, s[68:71], s85 offen lds\n" \
"v_mfma_f32_16x16x32_bf16 a[132:135], v[124:127], v[84:87], a[132:135]\n" \
"ds_read_b64_tr_b16 v[120:121], v32 offset:40960\n" \
"ds_read_b64_tr_b16 v[122:123], v32 offset:41984\n" \
"v_mfma_f32_16x16x32_bf16 a[164:167], v[124:127], v[92:95], a[164:167]\n" \
"v_mov_b32_e32 v218, 0x1000c0c\n" \
"v_mov_b32_e32 v219, 0xc0c0100\n" \
"v_mfma_f32_16x16x32_bf16 a[164:167], v[128:131], v[96:99], a[164:167]\n" \
"s_add_u32 m0, %[mB], 0x4800\n" \
"s_add_u32 s84, s63, 0x10000\n" \
"v_mfma_f32_16x16x32_bf16 a[196:199], v[128:131], v[104:107], a[196:199]\n" \
"buffer_load_dwordx4 v27, s[72:75], s84 offen lds\n" \
"v_mfma_f32_16x16x32_bf16 a[196:199], v[124:127], v[100:103], a[196:199]\n" \
"v_cmp_ne_u32_e32 vcc, 0, v216\n" \
"v_mul_u32_u24_e32 v222, 0x480, v221\n" \
"v_mfma_f32_16x16x32_bf16 a[228:231], v[124:127], v[108:111], a[228:231]\n" \
"ds_read_b64_tr_b16 v[124:125], v33 offset:32768\n" \
"ds_read_b64_tr_b16 v[126:127], v33 offset:33792\n" \
"v_mfma_f32_16x16x32_bf16 a[228:231], v[128:131], v[112:115], a[228:231]\n" \
"ds_read_b64_tr_b16 v[128:129], v33 offset:40960\n" \
"ds_read_b64_tr_b16 v[130:131], v33 offset:41984\n" \
"v_mfma_f32_16x16x32_bf16 a[136:139], v[136:139], v[88:91], a[136:139]\n" \
"s_add_u32 s89, s98, s99\n" \
"s_lshl_b32 s101, s94, 16\n" \
"v_mfma_f32_16x16x32_bf16 a[136:139], v[132:135], v[84:87], a[136:139]\n" \
"s_lshl_b32 s88, s95, 16\n" \
"v_add_u32_e64 v217, v214, v216\n" \
"v_mfma_f32_16x16x32_bf16 a[168:171], v[132:135], v[92:95], a[168:171]\n" \
"v_cndmask_b32_e32 v220, v219, v218, vcc\n" \
"v_lshlrev_b32_e32 v224, 4, v223\n" \
"v_mfma_f32_16x16x32_bf16 a[168:171], v[136:139], v[96:99], a[168:171]\n" \
"s_add_u32 s45, s89, s101\n" \
"s_add_u32 s46, s89, s88\n" \
"v_mfma_f32_16x16x32_bf16 a[200:203], v[136:139], v[104:107], a[200:203]\n" \
"v_mov_b32_e32 v221, 0xc0c0c0c\n" \
"v_cmp_le_u32_e32 vcc, v217, v215\n" \
"v_mfma_f32_16x16x32_bf16 a[200:203], v[132:135], v[100:103], a[200:203]\n" \
"v_add_u32_e32 v24, v222, v224\n" \
"v_lshlrev_b32_e32 v227, 4, v226\n" \
"v_mfma_f32_16x16x32_bf16 a[232:235], v[132:135], v[108:111], a[232:235]\n" \
"ds_read_b64_tr_b16 v[132:133], v34 offset:32768\n" \
"ds_read_b64_tr_b16 v[134:135], v34 offset:33792\n" \
"v_mfma_f32_16x16x32_bf16 a[232:235], v[136:139], v[112:115], a[232:235]\n" \
"ds_read_b64_tr_b16 v[136:137], v34 offset:40960\n" \
"ds_read_b64_tr_b16 v[138:139], v34 offset:41984\n" \
"v_mfma_f32_16x16x32_bf16 a[140:143], v[144:147], v[88:91], a[140:143]\n" \
"ds_read_b128 v[88:91], v29 offset:32768\n" \
"s_add_u32 s97, s45, s99\n" \
"v_mfma_f32_16x16x32_bf16 a[140:143], v[140:143], v[84:87], a[140:143]\n" \
"ds_read_b128 v[84:87], v28 offset:32768\n" \
"s_add_u32 s96, s46, s99\n" \
"v_mfma_f32_16x16x32_bf16 a[172:175], v[140:143], v[92:95], a[172:175]\n" \
"ds_read_b128 v[92:95], v28 offset:34816\n" \
"v_cndmask_b32_e32 v40, v220, v221, vcc\n" \
"v_mfma_f32_16x16x32_bf16 a[172:175], v[144:147], v[96:99], a[172:175]\n" \
"ds_read_b128 v[96:99], v29 offset:34816\n" \
"v_add_u32_e64 v25, v222, v227\n" \
"v_mfma_f32_16x16x32_bf16 a[204:207], v[144:147], v[104:107], a[204:207]\n" \
"ds_read_b128 v[104:107], v29 offset:36864\n" \
"s_add_u32 s47, 0x4000, s97\n" \
"v_mfma_f32_16x16x32_bf16 a[204:207], v[140:143], v[100:103], a[204:207]\n" \
"ds_read_b128 v[100:103], v28 offset:36864\n" \
"s_add_u32 s48, 0x4000, s96\n" \
"v_mfma_f32_16x16x32_bf16 a[236:239], v[140:143], v[108:111], a[236:239]\n" \
"ds_read_b64_tr_b16 v[140:141], v35 offset:32768\n" \
"ds_read_b64_tr_b16 v[142:143], v35 offset:33792\n" \
"v_mfma_f32_16x16x32_bf16 a[236:239], v[144:147], v[112:115], a[236:239]\n" \
"s_add_u32 m0, %[mA], 0x1800\n" \
"s_waitcnt lgkmcnt(12)\n" \
"s_nop 0\n" \
"v_mfma_f32_16x16x32_bf16 a[16:19], v[56:59], v[152:155], a[16:19]\n" \
"s_add_u32 s85, s62, 0xc000\n" \
"ds_read_b64_tr_b16 v[144:145], v35 offset:40960\n" \
"v_mfma_f32_16x16x32_bf16 a[16:19], v[52:55], v[148:151], a[16:19]\n" \
"buffer_load_dwordx4 v22, s[64:67], s85 offen lds\n" \
"v_mfma_f32_16x16x32_bf16 a[48:51], v[60:63], v[148:151], a[48:51]\n" \
"s_add_u32 m0, %[mA], 0x1c00\n" \
"s_add_u32 s84, s62, 0xe000\n" \
"v_mfma_f32_16x16x32_bf16 a[48:51], v[64:67], v[152:155], a[48:51]\n" \
"ds_read_b64_tr_b16 v[146:147], v35 offset:41984\n" \
"ds_read_b128 v[108:111], v28 offset:38912\n" \
"v_mfma_f32_16x16x32_bf16 a[80:83], v[72:75], v[152:155], a[80:83]\n" \
"buffer_load_dwordx4 v23, s[64:67], s84 offen lds\n" \
"v_mfma_f32_16x16x32_bf16 a[80:83], v[68:71], v[148:151], a[80:83]\n" \
"s_add_u32 m0, %[mB], 0xc00\n" \
"s_add_u32 s85, s63, 0x18000\n" \
"v_mfma_f32_16x16x32_bf16 a[112:115], v[76:79], v[148:151], a[112:115]\n" \
"ds_read_b128 v[112:115], v29 offset:38912\n" \
"s_lshr_b32 s94, %[w], 1\n" \
"v_mfma_f32_16x16x32_bf16 a[112:115], v[80:83], v[152:155], a[112:115]\n" \
"buffer_load_dwordx4 v27, s[68:71], s85 offen lds\n" \
"v_mfma_f32_16x16x32_bf16 a[20:23], v[56:59], v[160:163], a[20:23]\n" \
"s_add_u32 m0, %[mB], 0x4c00\n" \
"s_add_u32 s84, s63, 0x18000\n" \
"v_mfma_f32_16x16x32_bf16 a[20:23], v[52:55], v[156:159], a[20:23]\n" \
"s_waitcnt vmcnt(15)\n" \
"v_or_b32_e32 v206, v210, v206\n" \
"v_mfma_f32_16x16x32_bf16 a[52:55], v[60:63], v[156:159], a[52:55]\n" \
"buffer_load_dwordx4 v27, s[72:75], s84 offen lds\n" \
"v_mfma_f32_16x16x32_bf16 a[52:55], v[64:67], v[160:163], a[52:55]\n" \
"s_mov_b32 m0, %[mS]\n" \
"s_and_b32 s95, 1, %[w]\n" \
"v_mfma_f32_16x16x32_bf16 a[84:87], v[72:75], v[160:163], a[84:87]\n" \
"v_mov_b32_e32 v223, 0x3020c0c\n" \
"v_mov_b32_e32 v224, 0xc0c0302\n" \
"v_mfma_f32_16x16x32_bf16 a[84:87], v[68:71], v[156:159], a[84:87]\n" \
"v_cmp_ne_u32_e32 vcc, 0, v216\n" \
"s_add_u32 s96, s94, s95\n" \
"v_mfma_f32_16x16x32_bf16 a[116:119], v[76:79], v[156:159], a[116:119]\n" \
"s_add_u32 s97, 1, s94\n" \
"v_add_u32_e64 v222, 2, v217\n" \
"v_mfma_f32_16x16x32_bf16 a[116:119], v[80:83], v[160:163], a[116:119]\n" \
"v_cndmask_b32_e32 v225, v224, v223, vcc\n" \
"s_lshl_b32 s49, s96, 13\n" \
"v_mfma_f32_16x16x32_bf16 a[24:27], v[56:59], v[168:171], a[24:27]\n" \
"s_sub_u32 s98, s97, s95\n" \
"s_lshl_b32 s99, s94, 13\n" \
"v_mfma_f32_16x16x32_bf16 a[24:27], v[52:55], v[164:167], a[24:27]\n" \
"s_mul_i32 s101, 0x1800, s94\n" \
"v_mov_b32_e32 v226, 0xc0c0c0c\n" \
"v_mfma_f32_16x16x32_bf16 a[56:59], v[60:63], v[164:167], a[56:59]\n" \
"v_cmp_le_u32_e32 vcc, v222, v215\n" \
"s_lshl_b32 s50, s98, 13\n" \
"v_mfma_f32_16x16x32_bf16 a[56:59], v[64:67], v[168:171], a[56:59]\n" \
"v_or_b32_e32 v204, v210, v204\n" \
"v_mov_b32_e32 v210, 0\n" \
"v_mfma_f32_16x16x32_bf16 a[88:91], v[72:75], v[168:171], a[88:91]\n" \
"s_mov_b32 s86, %[wql]\n" \
"s_mov_b32 s87, %[wqh]\n" \
"v_mfma_f32_16x16x32_bf16 a[88:91], v[68:71], v[164:167], a[88:91]\n" \
"ds_write_b128 v196, v[200:203]\n" \
"s_mov_b32 exec_lo, 0x29\n" \
"s_mov_b32 exec_hi, 0x300000\n" \
"buffer_load_dword v11, s[80:83], 0 offen sc1 lds\n" \
"s_mov_b64 exec, -1\n" \
"v_mfma_f32_16x16x32_bf16 a[120:123], v[76:79], v[164:167], a[120:123]\n" \
"s_mov_b32 m0, %[mS]\n" \
"s_add_u32 s100, 0x1800, s99\n" \
"v_mfma_f32_16x16x32_bf16 a[120:123], v[80:83], v[168:171], a[120:123]\n" \
"ds_write_b128 v197, v[204:207]\n" \
"v_mfma_f32_16x16x32_bf16 a[28:31], v[56:59], v[176:179], a[28:31]\n" \
"ds_write_b128 v198, v[208:211]\n" \
"v_mfma_f32_16x16x32_bf16 a[28:31], v[52:55], v[172:175], a[28:31]\n" \
"s_sub_u32 s88, 0x17000, s101\n" \
"s_lshl_b32 s96, s94, 15\n" \
"v_mfma_f32_16x16x32_bf16 a[60:63], v[60:63], v[172:175], a[60:63]\n" \
"v_cndmask_b32_e32 v41, v225, v226, vcc\n" \
"v_and_b32_e32 v230, 3, v229\n" \
"s_mov_b32 exec_lo, 0x3ffff\n" \
"s_mov_b32 exec_hi, 0x0\n" \
"global_load_lds_dword v188, s[86:87]\n" \
"s_mov_b64 exec, -1\n" \
"v_mfma_f32_16x16x32_bf16 a[60:63], v[64:67], v[176:179], a[60:63]\n" \
"s_cmp_lg_u32 s95, 0\n" \
"s_cselect_b32 s51, s100, s88\n" \
"v_mfma_f32_16x16x32_bf16 a[92:95], v[72:75], v[176:179], a[92:95]\n" \
"s_add_u32 s97, 0x10000, s96\n" \
"s_lshl_b32 s98, s95, 11\n" \
"v_mfma_f32_16x16x32_bf16 a[92:95], v[68:71], v[172:175], a[92:95]\n" \
"v_mov_b32_e32 v227, 0xc0c0c0c\n" \
"v_cmp_le_u32_e32 vcc, v217, v215\n" \
"v_mfma_f32_16x16x32_bf16 a[124:127], v[76:79], v[172:175], a[124:127]\n" \
"v_add_u32_e32 v231, v228, v230\n" \
"s_add_u32 s101, s97, s98\n" \
"v_mfma_f32_16x16x32_bf16 a[124:127], v[80:83], v[176:179], a[124:127]\n" \
"v_cndmask_b32_e32 v42, v227, v220, vcc\n" \
"v_lshlrev_b32_e32 v232, 7, v231\n" \
"v_mfma_f32_16x16x32_bf16 a[0:3], v[120:123], v[56:59], a[0:3]\n" \
"v_lshrrev_b32_e32 v237, 1, v230\n" \
"s_add_u32 s100, s101, s99\n" \
"v_mfma_f32_16x16x32_bf16 a[0:3], v[116:119], v[52:55], a[0:3]\n" \
"v_mov_b32_e32 v228, 0xc0c0c0c\n" \
"v_cmp_le_u32_e32 vcc, v222, v215\n" \
"v_mfma_f32_16x16x32_bf16 a[32:35], v[116:119], v[60:63], a[32:35]\n" \
"v_add_u32_e32 v233, 0x14000, v232\n" \
"v_lshlrev_b32_e32 v235, 3, v234\n" \
"v_mfma_f32_16x16x32_bf16 a[32:35], v[120:123], v[64:67], a[32:35]\n" \
"s_add_u32 s88, 0x4000, s100\n" \
"v_cndmask_b32_e32 v43, v228, v225, vcc\n" \
"v_mfma_f32_16x16x32_bf16 a[64:67], v[120:123], v[72:75], a[64:67]\n" \
"v_and_b32_e32 v229, 50, v212\n" \
"v_and_b32_e64 v230, 16, v20\n" \
"v_mfma_f32_16x16x32_bf16 a[64:67], v[116:119], v[68:71], a[64:67]\n" \
"v_mov_b32_e32 v231, 0x3f80\n" \
"v_add_u32_e64 v236, v233, v235\n" \
"v_mfma_f32_16x16x32_bf16 a[96:99], v[116:119], v[76:79], a[96:99]\n" \
"v_lshlrev_b32_e32 v239, 1, v238\n" \
"v_lshlrev_b32_e32 v232, v230, v231\n" \
"v_mfma_f32_16x16x32_bf16 a[96:99], v[120:123], v[80:83], a[96:99]\n" \
"s_cmp_lg_u32 s95, 0\n" \
"s_cselect_b32 s53, s88, s101\n" \
"v_mfma_f32_16x16x32_bf16 a[4:7], v[128:131], v[56:59], a[4:7]\n" \
"v_cmp_eq_u32_e32 vcc, 16, v229\n" \
"v_or_b32_e32 v240, v237, v239\n" \
"v_mfma_f32_16x16x32_bf16 a[4:7], v[124:127], v[52:55], a[4:7]\n" \
"v_cndmask_b32_e32 v46, 0, v232, vcc\n" \
"v_and_b32_e32 v233, 51, v212\n" \
"v_mfma_f32_16x16x32_bf16 a[36:39], v[124:127], v[60:63], a[36:39]\n" \
"v_lshlrev_b32_e32 v241, 5, v240\n" \
"v_mov_b32_e32 v234, 0x3f80\n" \
"v_mfma_f32_16x16x32_bf16 a[36:39], v[128:131], v[64:67], a[36:39]\n" \
"v_cmp_eq_u32_e32 vcc, 2, v233\n" \
"s_lshl_b32 s88, s95, 4\n" \
"v_mfma_f32_16x16x32_bf16 a[68:71], v[128:131], v[72:75], a[68:71]\n" \
"v_add_u32_e32 v190, v236, v241\n" \
"v_cndmask_b32_e32 v47, 0, v234, vcc\n" \
"v_mfma_f32_16x16x32_bf16 a[68:71], v[124:127], v[68:71], a[68:71]\n" \
"v_add_u32_e32 v235, s88, v215\n" \
"v_mov_b32_e32 v236, 0xffff\n" \
"v_mfma_f32_16x16x32_bf16 a[100:103], v[124:127], v[76:79], a[100:103]\n" \
"v_cmp_le_u32_e32 vcc, v214, v235\n" \
"v_mov_b32_e32 v238, 0xffff0000\n" \
"v_mfma_f32_16x16x32_bf16 a[100:103], v[128:131], v[80:83], a[100:103]\n" \
"v_cndmask_b32_e32 v237, 0, v236, vcc\n" \
"s_waitcnt lgkmcnt(0)\n" \
"v_mfma_f32_16x16x32_bf16 a[8:11], v[136:139], v[56:59], a[8:11]\n" \
"v_cmp_lt_u32_e32 vcc, v214, v235\n" \
"v_add_u32_e64 v240, 2, v214\n" \
"v_mfma_f32_16x16x32_bf16 a[8:11], v[132:135], v[52:55], a[8:11]\n" \
"v_cndmask_b32_e32 v239, 0, v238, vcc\n" \
"v_mov_b32_e32 v241, 0xffff\n" \
"v_mfma_f32_16x16x32_bf16 a[40:43], v[132:135], v[60:63], a[40:43]\n" \
"v_cmp_le_u32_e32 vcc, v240, v235\n" \
"v_mov_b32_e32 v243, 0xffff0000\n" \
"v_mfma_f32_16x16x32_bf16 a[40:43], v[136:139], v[64:67], a[40:43]\n" \
"v_cndmask_b32_e32 v242, 0, v241, vcc\n" \
"v_cmp_lt_u32_e32 vcc, v240, v235\n" \
"v_mfma_f32_16x16x32_bf16 a[72:75], v[136:139], v[72:75], a[72:75]\n" \
"v_add_u32_e32 v245, 16, v214\n" \
"v_cndmask_b32_e32 v244, 0, v243, vcc\n" \
"v_mfma_f32_16x16x32_bf16 a[72:75], v[132:135], v[68:71], a[72:75]\n" \
"v_mov_b32_e32 v246, 0xffff\n" \
"v_cmp_le_u32_e32 vcc, v245, v235\n" \
"v_mfma_f32_16x16x32_bf16 a[104:107], v[132:135], v[76:79], a[104:107]\n" \
"v_mov_b32_e32 v248, 0xffff0000\n" \
"v_cndmask_b32_e32 v247, 0, v246, vcc\n" \
"v_mfma_f32_16x16x32_bf16 a[104:107], v[136:139], v[80:83], a[104:107]\n" \
"v_cmp_lt_u32_e32 vcc, v245, v235\n" \
"v_add_u32_e32 v250, 18, v214\n" \
"v_mfma_f32_16x16x32_bf16 a[12:15], v[144:147], v[56:59], a[12:15]\n" \
"s_add_u32 s62, %[rc], 0x280\n" \
"s_and_b32 s62, s62, 0x3ff\n" \
"v_mfma_f32_16x16x32_bf16 a[12:15], v[140:143], v[52:55], a[12:15]\n" \
"s_lshl_b32 s63, s62, 12\n" \
"v_cndmask_b32_e32 v249, 0, v248, vcc\n" \
"v_mfma_f32_16x16x32_bf16 a[44:47], v[140:143], v[60:63], a[44:47]\n" \
"s_lshl_b32 s89, %[w], 8\n" \
"v_mov_b32_e32 v251, 0xffff\n" \
"v_mfma_f32_16x16x32_bf16 a[44:47], v[144:147], v[64:67], a[44:47]\n" \
"v_cmp_le_u32_e32 vcc, v250, v235\n" \
"s_add_u32 s52, 0x27000, s89\n" \
"v_mfma_f32_16x16x32_bf16 a[76:79], v[144:147], v[72:75], a[76:79]\n" \
"v_cndmask_b32_e32 v213, 0, v251, vcc\n" \
"s_add_u32 s89, 0xffffc000, s101\n" \
"v_mfma_f32_16x16x32_bf16 a[76:79], v[140:143], v[68:71], a[76:79]\n" \
"v_mov_b32_e32 v218, 0xffff0000\n" \
"v_cmp_lt_u32_e32 vcc, v250, v235\n" \
"v_mfma_f32_16x16x32_bf16 a[108:111], v[140:143], v[76:79], a[108:111]\n" \
"s_cmp_lg_u32 s95, 0\n" \
"s_cselect_b32 s54, s89, s100\n" \
"v_mfma_f32_16x16x32_bf16 a[108:111], v[144:147], v[80:83], a[108:111]\n" \
"v_cndmask_b32_e32 v219, 0, v218, vcc\n" \
"v_lshlrev_b32_e32 v216, 3, v212\n" \
"s_waitcnt vmcnt(2)\n" \
"s_barrier\n" \
"v_mfma_f32_16x16x32_bf16 a[128:131], v[120:123], v[88:91], a[128:131]\n" \
"s_add_u32 m0, %[mA], 0x8000\n" \
"s_add_u32 s85, s62, 0x0\n" \
"s_nop 0\n" \
"v_mfma_f32_16x16x32_bf16 a[128:131], v[116:119], v[84:87], a[128:131]\n" \
"buffer_load_dwordx4 v22, s[64:67], s85 offen lds\n" \
"v_mfma_f32_16x16x32_bf16 a[160:163], v[116:119], v[92:95], a[160:163]\n" \
"ds_read_b128 v[56:59], v29 offset:16384\n" \
"ds_read_b128 v[52:55], v28 offset:16384\n" \
"v_mfma_f32_16x16x32_bf16 a[160:163], v[120:123], v[96:99], a[160:163]\n" \
"s_add_u32 m0, %[mA], 0x8400\n" \
"s_add_u32 s84, s62, 0x2000\n" \
"v_mfma_f32_16x16x32_bf16 a[192:195], v[120:123], v[104:107], a[192:195]\n" \
"buffer_load_dwordx4 v23, s[64:67], s84 offen lds\n" \
"v_mfma_f32_16x16x32_bf16 a[192:195], v[116:119], v[100:103], a[192:195]\n" \
"ds_read_b128 v[60:63], v28 offset:18432\n" \
"ds_read_b128 v[64:67], v29 offset:18432\n" \
"v_mfma_f32_16x16x32_bf16 a[224:227], v[116:119], v[108:111], a[224:227]\n" \
"ds_read_b64_tr_b16 v[116:117], v32 offset:0\n" \
"ds_read_b64_tr_b16 v[118:119], v32 offset:1024\n" \
"v_mfma_f32_16x16x32_bf16 a[224:227], v[120:123], v[112:115], a[224:227]\n" \
"s_add_u32 m0, %[mB], 0x8000\n" \
"s_add_u32 s85, s63, 0x0\n" \
"v_mfma_f32_16x16x32_bf16 a[132:135], v[128:131], v[88:91], a[132:135]\n" \
"buffer_load_dwordx4 v26, s[68:71], s85 offen lds\n" \
"v_mfma_f32_16x16x32_bf16 a[132:135], v[124:127], v[84:87], a[132:135]\n" \
"ds_read_b64_tr_b16 v[120:121], v32 offset:8192\n" \
"ds_read_b64_tr_b16 v[122:123], v32 offset:9216\n" \
"v_mfma_f32_16x16x32_bf16 a[164:167], v[124:127], v[92:95], a[164:167]\n" \
"ds_read_b128 v[72:75], v29 offset:20480\n" \
"ds_read_b128 v[68:71], v28 offset:20480\n" \
"v_mfma_f32_16x16x32_bf16 a[164:167], v[128:131], v[96:99], a[164:167]\n" \
"s_add_u32 m0, %[mB], 0xc000\n" \
"s_add_u32 s84, s63, 0x0\n" \
"v_mfma_f32_16x16x32_bf16 a[196:199], v[128:131], v[104:107], a[196:199]\n" \
"buffer_load_dwordx4 v26, s[72:75], s84 offen lds\n" \
"v_mfma_f32_16x16x32_bf16 a[196:199], v[124:127], v[100:103], a[196:199]\n" \
"ds_read_b128 v[76:79], v28 offset:22528\n" \
"ds_read_b128 v[80:83], v29 offset:22528\n" \
"v_mfma_f32_16x16x32_bf16 a[228:231], v[124:127], v[108:111], a[228:231]\n" \
"ds_read_b64_tr_b16 v[124:125], v33 offset:0\n" \
"ds_read_b64_tr_b16 v[126:127], v33 offset:1024\n" \
"v_mfma_f32_16x16x32_bf16 a[228:231], v[128:131], v[112:115], a[228:231]\n" \
"s_add_u32 m0, %[mA], 0x8800\n" \
"s_add_u32 s85, s62, 0x4000\n" \
"v_mfma_f32_16x16x32_bf16 a[136:139], v[136:139], v[88:91], a[136:139]\n" \
"buffer_load_dwordx4 v22, s[64:67], s85 offen lds\n" \
"v_mfma_f32_16x16x32_bf16 a[136:139], v[132:135], v[84:87], a[136:139]\n" \
"ds_read_b64_tr_b16 v[128:129], v33 offset:8192\n" \
"ds_read_b64_tr_b16 v[130:131], v33 offset:9216\n" \
"v_mfma_f32_16x16x32_bf16 a[168:171], v[132:135], v[92:95], a[168:171]\n" \
"ds_read_b128 v[200:203], v199 offset:0\n" \
"ds_read_b128 v[204:207], v199 offset:4096\n" \
"v_mfma_f32_16x16x32_bf16 a[168:171], v[136:139], v[96:99], a[168:171]\n" \
"s_add_u32 m0, %[mA], 0x8c00\n" \
"s_add_u32 s84, s62, 0x6000\n" \
"v_mfma_f32_16x16x32_bf16 a[200:203], v[136:139], v[104:107], a[200:203]\n" \
"buffer_load_dwordx4 v23, s[64:67], s84 offen lds\n" \
"v_mfma_f32_16x16x32_bf16 a[200:203], v[132:135], v[100:103], a[200:203]\n" \
"ds_read_b128 v[208:211], v199 offset:8192\n" \
"s_lshr_b32 s94, %[b], 5\n" \
"s_nop 0\n" \
"v_mfma_f32_16x16x32_bf16 a[232:235], v[132:135], v[108:111], a[232:235]\n" \
"ds_read_b64_tr_b16 v[132:133], v34 offset:0\n" \
"ds_read_b64_tr_b16 v[134:135], v34 offset:1024\n" \
"v_mfma_f32_16x16x32_bf16 a[232:235], v[136:139], v[112:115], a[232:235]\n" \
"s_add_u32 m0, %[mB], 0x8400\n" \
"s_add_u32 s85, s63, 0x8000\n" \
"v_mfma_f32_16x16x32_bf16 a[140:143], v[144:147], v[88:91], a[140:143]\n" \
"buffer_load_dwordx4 v26, s[68:71], s85 offen lds\n" \
"v_mfma_f32_16x16x32_bf16 a[140:143], v[140:143], v[84:87], a[140:143]\n" \
"ds_read_b64_tr_b16 v[136:137], v34 offset:8192\n" \
"ds_read_b64_tr_b16 v[138:139], v34 offset:9216\n" \
"v_mfma_f32_16x16x32_bf16 a[172:175], v[140:143], v[92:95], a[172:175]\n" \
"s_mov_b32 s96, 0xc000\n" \
"s_mov_b32 s97, 0x8000\n" \
"v_mfma_f32_16x16x32_bf16 a[172:175], v[144:147], v[96:99], a[172:175]\n" \
"s_add_u32 m0, %[mB], 0xc400\n" \
"s_add_u32 s84, s63, 0x8000\n" \
"v_mfma_f32_16x16x32_bf16 a[204:207], v[144:147], v[104:107], a[204:207]\n" \
"buffer_load_dwordx4 v26, s[72:75], s84 offen lds\n" \
"v_mfma_f32_16x16x32_bf16 a[204:207], v[140:143], v[100:103], a[204:207]\n" \
"v_or_b32_e32 v15, v213, v219\n" \
"s_lshl_b32 s101, %[w], 9\n" \
"v_mfma_f32_16x16x32_bf16 a[236:239], v[140:143], v[108:111], a[236:239]\n" \
"ds_read_b64_tr_b16 v[140:141], v35 offset:0\n" \
"ds_read_b64_tr_b16 v[142:143], v35 offset:1024\n" \
"v_mfma_f32_16x16x32_bf16 a[236:239], v[144:147], v[112:115], a[236:239]\n" \
"s_add_u32 m0, %[mA], 0x9000\n" \
"s_add_u32 s85, s62, 0x8000\n" \
"v_mfma_f32_16x16x32_bf16 a[144:147], v[88:91], v[152:155], a[144:147]\n" \
"buffer_load_dwordx4 v22, s[64:67], s85 offen lds\n" \
"v_mfma_f32_16x16x32_bf16 a[144:147], v[84:87], v[148:151], a[144:147]\n" \
"ds_read_b64_tr_b16 v[144:145], v35 offset:8192\n" \
"ds_read_b64_tr_b16 v[146:147], v35 offset:9216\n" \
"v_mfma_f32_16x16x32_bf16 a[176:179], v[92:95], v[148:151], a[176:179]\n" \
"v_lshrrev_b32_e32 v212, 4, v20\n" \
"s_add_u32 s89, 0x27800, s101\n" \
"v_mfma_f32_16x16x32_bf16 a[176:179], v[96:99], v[152:155], a[176:179]\n" \
"s_add_u32 m0, %[mA], 0x9400\n" \
"s_add_u32 s84, s62, 0xa000\n" \
"v_mfma_f32_16x16x32_bf16 a[208:211], v[104:107], v[152:155], a[208:211]\n" \
"buffer_load_dwordx4 v23, s[64:67], s84 offen lds\n" \
"v_mfma_f32_16x16x32_bf16 a[208:211], v[100:103], v[148:151], a[208:211]\n" \
"s_cmp_lg_u32 s94, 0\n" \
"s_cselect_b32 s59, s96, 0x8000\n" \
"v_mfma_f32_16x16x32_bf16 a[240:243], v[108:111], v[148:151], a[240:243]\n" \
"ds_read_b64_tr_b16 v[148:149], v32 offset:16384\n" \
"ds_read_b64_tr_b16 v[150:151], v32 offset:17408\n" \
"v_mfma_f32_16x16x32_bf16 a[240:243], v[112:115], v[152:155], a[240:243]\n" \
"s_add_u32 m0, %[mB], 0x8800\n" \
"s_add_u32 s85, s63, 0x10000\n" \
"v_mfma_f32_16x16x32_bf16 a[148:151], v[88:91], v[160:163], a[148:151]\n" \
"buffer_load_dwordx4 v27, s[68:71], s85 offen lds\n" \
"v_mfma_f32_16x16x32_bf16 a[148:151], v[84:87], v[156:159], a[148:151]\n" \
"ds_read_b64_tr_b16 v[152:153], v32 offset:24576\n" \
"ds_read_b64_tr_b16 v[154:155], v32 offset:25600\n" \
"v_mfma_f32_16x16x32_bf16 a[180:183], v[92:95], v[156:159], a[180:183]\n" \
"s_cmp_lg_u32 s94, 0\n" \
"s_cselect_b32 s98, s97, 0x4000\n" \
"v_mfma_f32_16x16x32_bf16 a[180:183], v[96:99], v[160:163], a[180:183]\n" \
"s_add_u32 m0, %[mB], 0xc800\n" \
"s_add_u32 s84, s63, 0x10000\n" \
"v_mfma_f32_16x16x32_bf16 a[212:215], v[104:107], v[160:163], a[212:215]\n" \
"buffer_load_dwordx4 v27, s[72:75], s84 offen lds\n" \
"v_mfma_f32_16x16x32_bf16 a[212:215], v[100:103], v[156:159], a[212:215]\n" \
"v_mul_u32_u24_e32 v213, 0xe39, v212\n" \
"v_add_u32_e32 v19, s89, v216\n" \
"v_mfma_f32_16x16x32_bf16 a[244:247], v[108:111], v[156:159], a[244:247]\n" \
"ds_read_b64_tr_b16 v[156:157], v33 offset:16384\n" \
"ds_read_b64_tr_b16 v[158:159], v33 offset:17408\n" \
"v_mfma_f32_16x16x32_bf16 a[244:247], v[112:115], v[160:163], a[244:247]\n" \
"ds_read_b64_tr_b16 v[160:161], v33 offset:24576\n" \
"ds_read_b64_tr_b16 v[162:163], v33 offset:25600\n" \
"v_mfma_f32_16x16x32_bf16 a[152:155], v[88:91], v[168:171], a[152:155]\n" \
"v_lshrrev_b32_e32 v214, 16, v213\n" \
"v_add_u32_e32 v222, 64, v212\n" \
"v_mfma_f32_16x16x32_bf16 a[152:155], v[84:87], v[164:167], a[152:155]\n" \
"v_mul_u32_u24_e32 v216, 18, v214\n" \
"s_and_b32 s94, 7, %[b]\n" \
"v_mfma_f32_16x16x32_bf16 a[184:187], v[92:95], v[164:167], a[184:187]\n" \
"v_mul_u32_u24_e32 v223, 0xe39, v222\n" \
"v_sub_u32_e32 v217, v212, v216\n" \
"v_mfma_f32_16x16x32_bf16 a[184:187], v[96:99], v[168:171], a[184:187]\n" \
"s_lshl_b32 s95, s94, 5\n" \
"v_lshrrev_b32_e32 v224, 16, v223\n" \
"v_mfma_f32_16x16x32_bf16 a[216:219], v[104:107], v[168:171], a[216:219]\n" \
"v_add_u32_e32 v233, 0x80, v212\n" \
"v_or_b32_e32 v13, v242, v244\n" \
"v_mfma_f32_16x16x32_bf16 a[216:219], v[100:103], v[164:167], a[216:219]\n" \
"v_lshlrev_b32_e32 v221, 2, v235\n" \
"s_add_u32 s96, 32, s95\n" \
"v_mfma_f32_16x16x32_bf16 a[248:251], v[108:111], v[164:167], a[248:251]\n" \
"ds_read_b64_tr_b16 v[164:165], v34 offset:16384\n" \
"ds_read_b64_tr_b16 v[166:167], v34 offset:17408\n" \
"v_mfma_f32_16x16x32_bf16 a[248:251], v[112:115], v[168:171], a[248:251]\n" \
"ds_read_b64_tr_b16 v[168:169], v34 offset:24576\n" \
"ds_read_b64_tr_b16 v[170:171], v34 offset:25600\n" \
"v_mfma_f32_16x16x32_bf16 a[156:159], v[88:91], v[176:179], a[156:159]\n" \
"ds_read_b128 v[88:91], v29 offset:0\n" \
"v_lshlrev_b32_e32 v218, 4, v217\n" \
"v_mfma_f32_16x16x32_bf16 a[156:159], v[84:87], v[172:175], a[156:159]\n" \
"ds_read_b128 v[84:87], v28 offset:0\n" \
"v_mul_u32_u24_e32 v226, 18, v224\n" \
"v_mfma_f32_16x16x32_bf16 a[188:191], v[92:95], v[172:175], a[188:191]\n" \
"ds_read_b128 v[92:95], v28 offset:2048\n" \
"v_mul_u32_u24_e32 v234, 0xe39, v233\n" \
"v_mfma_f32_16x16x32_bf16 a[188:191], v[96:99], v[176:179], a[188:191]\n" \
"ds_read_b128 v[96:99], v29 offset:2048\n" \
"v_or_b32_e32 v12, v237, v239\n" \
"v_mfma_f32_16x16x32_bf16 a[220:223], v[104:107], v[176:179], a[220:223]\n" \
"ds_read_b128 v[104:107], v29 offset:4096\n" \
"v_add_u32_e32 v219, s96, v218\n" \
"v_mfma_f32_16x16x32_bf16 a[220:223], v[100:103], v[172:175], a[220:223]\n" \
"ds_read_b128 v[100:103], v28 offset:4096\n" \
"v_sub_u32_e32 v227, v222, v226\n" \
"v_mfma_f32_16x16x32_bf16 a[252:255], v[108:111], v[172:175], a[252:255]\n" \
"ds_read_b64_tr_b16 v[172:173], v35 offset:16384\n" \
"ds_read_b64_tr_b16 v[174:175], v35 offset:17408\n" \
"v_mfma_f32_16x16x32_bf16 a[252:255], v[112:115], v[176:179], a[252:255]\n" \
"s_add_u32 m0, %[mA], 0x9800\n" \
"s_waitcnt lgkmcnt(12)\n" \
"v_mfma_f32_16x16x32_bf16 a[0:3], v[120:123], v[56:59], a[0:3]\n" \
"s_add_u32 s85, s62, 0xc000\n" \
"ds_read_b64_tr_b16 v[176:177], v35 offset:24576\n" \
"v_mfma_f32_16x16x32_bf16 a[0:3], v[116:119], v[52:55], a[0:3]\n" \
"buffer_load_dwordx4 v22, s[64:67], s85 offen lds\n" \
"v_mfma_f32_16x16x32_bf16 a[32:35], v[116:119], v[60:63], a[32:35]\n" \
"s_add_u32 m0, %[mA], 0x9c00\n" \
"s_add_u32 s84, s62, 0xe000\n" \
"v_mfma_f32_16x16x32_bf16 a[32:35], v[120:123], v[64:67], a[32:35]\n" \
"ds_read_b64_tr_b16 v[178:179], v35 offset:25600\n" \
"ds_read_b128 v[108:111], v28 offset:6144\n" \
"v_mfma_f32_16x16x32_bf16 a[64:67], v[120:123], v[72:75], a[64:67]\n" \
"buffer_load_dwordx4 v23, s[64:67], s84 offen lds\n" \
"v_mfma_f32_16x16x32_bf16 a[64:67], v[116:119], v[68:71], a[64:67]\n" \
"s_add_u32 m0, %[mB], 0x8c00\n" \
"s_add_u32 s85, s63, 0x18000\n" \
"v_mfma_f32_16x16x32_bf16 a[96:99], v[116:119], v[76:79], a[96:99]\n" \
"ds_read_b128 v[112:115], v29 offset:6144\n" \
"v_lshrrev_b32_e32 v235, 16, v234\n" \
"s_nop 0\n" \
"v_mfma_f32_16x16x32_bf16 a[96:99], v[120:123], v[80:83], a[96:99]\n" \
"buffer_load_dwordx4 v27, s[68:71], s85 offen lds\n" \
"v_mfma_f32_16x16x32_bf16 a[4:7], v[128:131], v[56:59], a[4:7]\n" \
"s_add_u32 m0, %[mB], 0xcc00\n" \
"s_add_u32 s84, s63, 0x18000\n" \
"v_mfma_f32_16x16x32_bf16 a[4:7], v[124:127], v[52:55], a[4:7]\n" \
"v_add_u32_e32 v244, 0xc0, v212\n" \
"v_add_u32_e32 v18, 0x27000, v221\n" \
"v_mfma_f32_16x16x32_bf16 a[36:39], v[124:127], v[60:63], a[36:39]\n" \
"buffer_load_dwordx4 v27, s[72:75], s84 offen lds\n" \
"v_mfma_f32_16x16x32_bf16 a[36:39], v[128:131], v[64:67], a[36:39]\n" \
"L_pf2:\n" \
"s_cbranch_execz L_pf6\n" \
"s_nop 0\n" \
"v_and_b32_e32 v220, 0xff, v219\n" \
"v_cmp_gt_u32_e32 vcc, 16, v217\n" \
"v_mfma_f32_16x16x32_bf16 a[68:71], v[128:131], v[72:75], a[68:71]\n" \
"v_lshlrev_b32_e32 v228, 4, v227\n" \
"v_mul_u32_u24_e32 v237, 18, v235\n" \
"v_mfma_f32_16x16x32_bf16 a[68:71], v[124:127], v[68:71], a[68:71]\n" \
"buffer_store_dwordx4 v[200:203], v20, s[80:83], s39 offen sc1\n" \
"v_mfma_f32_16x16x32_bf16 a[100:103], v[124:127], v[76:79], a[100:103]\n" \
"v_mul_u32_u24_e32 v245, 0xe39, v244\n" \
"v_cndmask_b32_e32 v221, v218, v220, vcc\n" \
"v_mfma_f32_16x16x32_bf16 a[100:103], v[128:131], v[80:83], a[100:103]\n" \
"v_add_u32_e32 v229, s96, v228\n" \
"v_sub_u32_e32 v238, v233, v237\n" \
"v_mfma_f32_16x16x32_bf16 a[8:11], v[136:139], v[56:59], a[8:11]\n" \
"v_lshrrev_b32_e32 v246, 16, v245\n" \
"v_add_u32_e32 v219, 0x100, v212\n" \
"v_mfma_f32_16x16x32_bf16 a[8:11], v[132:135], v[52:55], a[8:11]\n" \
"v_or_b32_e32 v14, v247, v249\n" \
"v_mul_u32_u24_e32 v215, 0x480, v214\n" \
"v_mfma_f32_16x16x32_bf16 a[40:43], v[132:135], v[60:63], a[40:43]\n" \
"buffer_store_dwordx4 v[204:207], v20, s[80:83], s37 offen sc1\n" \
"v_mfma_f32_16x16x32_bf16 a[40:43], v[136:139], v[64:67], a[40:43]\n" \
"v_and_b32_e32 v230, 0xff, v229\n" \
"v_cmp_gt_u32_e32 vcc, 16, v227\n" \
"v_mfma_f32_16x16x32_bf16 a[72:75], v[136:139], v[72:75], a[72:75]\n" \
"v_lshlrev_b32_e32 v239, 4, v238\n" \
"v_mul_u32_u24_e32 v248, 18, v246\n" \
"v_mfma_f32_16x16x32_bf16 a[72:75], v[132:135], v[68:71], a[72:75]\n" \
"v_mul_u32_u24_e32 v217, 0xe39, v219\n" \
"v_add_u32_e32 v0, v215, v221\n" \
"v_mfma_f32_16x16x32_bf16 a[104:107], v[132:135], v[76:79], a[104:107]\n" \
"v_cndmask_b32_e32 v231, v228, v230, vcc\n" \
"v_add_u32_e32 v240, s96, v239\n" \
"v_mfma_f32_16x16x32_bf16 a[104:107], v[136:139], v[80:83], a[104:107]\n" \
"v_sub_u32_e32 v249, v244, v248\n" \
"v_lshrrev_b32_e32 v220, 16, v217\n" \
"v_mfma_f32_16x16x32_bf16 a[12:15], v[144:147], v[56:59], a[12:15]\n" \
"buffer_store_dwordx4 v[208:211], v20, s[80:83], s40 offen sc1\n" \
"v_mfma_f32_16x16x32_bf16 a[12:15], v[140:143], v[52:55], a[12:15]\n" \
"v_add_u32_e32 v229, 0x140, v212\n" \
"v_mul_u32_u24_e32 v225, 0x480, v224\n" \
"v_mfma_f32_16x16x32_bf16 a[44:47], v[140:143], v[60:63], a[44:47]\n" \
"v_and_b32_e32 v241, 0xff, v240\n" \
"v_cmp_gt_u32_e32 vcc, 16, v238\n" \
"v_mfma_f32_16x16x32_bf16 a[44:47], v[144:147], v[64:67], a[44:47]\n" \
"v_lshlrev_b32_e32 v250, 4, v249\n" \
"v_mul_u32_u24_e32 v215, 18, v220\n" \
"v_mfma_f32_16x16x32_bf16 a[76:79], v[144:147], v[72:75], a[76:79]\n" \
"v_mul_u32_u24_e32 v227, 0xe39, v229\n" \
"v_add_u32_e32 v232, v225, v231\n" \
"v_mfma_f32_16x16x32_bf16 a[76:79], v[140:143], v[68:71], a[76:79]\n" \
"v_cndmask_b32_e32 v242, v239, v241, vcc\n" \
"v_add_u32_e32 v251, s96, v250\n" \
"v_mfma_f32_16x16x32_bf16 a[108:111], v[140:143], v[76:79], a[108:111]\n" \
"s_mov_b32 m0, %[mS]\n" \
"s_mov_b32 s86, %[wql]\n" \
"v_mfma_f32_16x16x32_bf16 a[108:111], v[144:147], v[80:83], a[108:111]\n" \
"s_mov_b32 s87, %[wqh]\n" \
"v_sub_u32_e32 v221, v219, v215\n" \
"v_mfma_f32_16x16x32_bf16 a[16:19], v[56:59], v[152:155], a[16:19]\n" \
"v_lshrrev_b32_e32 v230, 16, v227\n" \
"v_add_u32_e32 v240, 0x180, v212\n" \
"v_mfma_f32_16x16x32_bf16 a[16:19], v[52:55], v[148:151], a[16:19]\n" \
"v_mul_u32_u24_e32 v236, 0x480, v235\n" \
"v_and_b32_e32 v213, 0xff, v251\n" \
"v_mfma_f32_16x16x32_bf16 a[48:51], v[60:63], v[148:151], a[48:51]\n" \
"v_cmp_gt_u32_e32 vcc, 16, v249\n" \
"v_lshlrev_b32_e32 v223, 4, v221\n" \
"v_mfma_f32_16x16x32_bf16 a[48:51], v[64:67], v[152:155], a[48:51]\n" \
"v_mul_u32_u24_e32 v225, 18, v230\n" \
"v_mul_u32_u24_e32 v238, 0xe39, v240\n" \
"v_mfma_f32_16x16x32_bf16 a[80:83], v[72:75], v[152:155], a[80:83]\n" \
"v_add_u32_e32 v1, 0xfffffc00, v232\n" \
"v_add_u32_e64 v243, v236, v242\n" \
"s_mov_b32 exec_lo, 0xfffc0000\n" \
"s_mov_b32 exec_hi, 0xf\n" \
"global_load_lds_dword v188, s[86:87]\n" \
"s_mov_b64 exec, -1\n" \
"v_mfma_f32_16x16x32_bf16 a[80:83], v[68:71], v[148:151], a[80:83]\n" \
"v_cndmask_b32_e32 v214, v250, v213, vcc\n" \
"v_add_u32_e32 v224, s96, v223\n" \
"v_mfma_f32_16x16x32_bf16 a[112:115], v[76:79], v[148:151], a[112:115]\n" \
"v_sub_u32_e32 v231, v229, v225\n" \
"v_lshrrev_b32_e32 v241, 16, v238\n" \
"v_mfma_f32_16x16x32_bf16 a[112:115], v[80:83], v[152:155], a[112:115]\n" \
"v_add_u32_e32 v251, 0x1c0, v212\n" \
"v_mul_u32_u24_e32 v247, 0x480, v246\n" \
"v_mfma_f32_16x16x32_bf16 a[20:23], v[56:59], v[160:163], a[20:23]\n" \
"v_and_b32_e32 v222, 0xff, v224\n" \
"v_cmp_gt_u32_e32 vcc, 16, v221\n" \
"v_mfma_f32_16x16x32_bf16 a[20:23], v[52:55], v[156:159], a[20:23]\n" \
"v_lshlrev_b32_e32 v232, 4, v231\n" \
"v_mul_u32_u24_e32 v236, 18, v241\n" \
"v_mfma_f32_16x16x32_bf16 a[52:55], v[60:63], v[156:159], a[52:55]\n" \
"v_mul_u32_u24_e32 v249, 0xe39, v251\n" \
"v_add_u32_e32 v2, 0xfffff800, v243\n" \
"v_mfma_f32_16x16x32_bf16 a[52:55], v[64:67], v[160:163], a[52:55]\n" \
"v_add_u32_e32 v216, v247, v214\n" \
"v_cndmask_b32_e32 v226, v223, v222, vcc\n" \
"v_mfma_f32_16x16x32_bf16 a[84:87], v[72:75], v[160:163], a[84:87]\n" \
"v_add_u32_e32 v234, s96, v232\n" \
"v_sub_u32_e32 v242, v240, v236\n" \
"v_mfma_f32_16x16x32_bf16 a[84:87], v[68:71], v[156:159], a[84:87]\n" \
"v_lshrrev_b32_e32 v213, 16, v249\n" \
"v_add_u32_e32 v224, 0x200, v212\n" \
"v_mfma_f32_16x16x32_bf16 a[116:119], v[76:79], v[156:159], a[116:119]\n" \
"v_mul_u32_u24_e32 v218, 0x480, v220\n" \
"v_and_b32_e32 v235, 0xff, v234\n" \
"v_mfma_f32_16x16x32_bf16 a[116:119], v[80:83], v[160:163], a[116:119]\n" \
"v_cmp_gt_u32_e32 vcc, 16, v231\n" \
"s_waitcnt lgkmcnt(0)\n" \
"v_mfma_f32_16x16x32_bf16 a[24:27], v[56:59], v[168:171], a[24:27]\n" \
"v_lshlrev_b32_e32 v243, 4, v242\n" \
"v_mul_u32_u24_e32 v247, 18, v213\n" \
"v_mfma_f32_16x16x32_bf16 a[24:27], v[52:55], v[164:167], a[24:27]\n" \
"v_mul_u32_u24_e32 v221, 0xe39, v224\n" \
"v_add_u32_e32 v3, 0xfffff400, v216\n" \
"v_mfma_f32_16x16x32_bf16 a[56:59], v[60:63], v[164:167], a[56:59]\n" \
"v_add_u32_e32 v4, v218, v226\n" \
"v_cndmask_b32_e32 v233, v232, v235, vcc\n" \
"v_mfma_f32_16x16x32_bf16 a[56:59], v[64:67], v[168:171], a[56:59]\n" \
"v_add_u32_e32 v245, s96, v243\n" \
"v_sub_u32_e32 v214, v251, v247\n" \
"v_mfma_f32_16x16x32_bf16 a[88:91], v[72:75], v[168:171], a[88:91]\n" \
"v_lshrrev_b32_e32 v222, 16, v221\n" \
"v_and_b32_e32 v246, 0xff, v245\n" \
"v_mfma_f32_16x16x32_bf16 a[88:91], v[68:71], v[164:167], a[88:91]\n" \
"v_cmp_gt_u32_e32 vcc, 16, v242\n" \
"v_lshlrev_b32_e32 v216, 4, v214\n" \
"v_mfma_f32_16x16x32_bf16 a[120:123], v[76:79], v[164:167], a[120:123]\n" \
"v_mul_u32_u24_e32 v218, 18, v222\n" \
"v_cndmask_b32_e32 v244, v243, v246, vcc\n" \
"v_mfma_f32_16x16x32_bf16 a[120:123], v[80:83], v[168:171], a[120:123]\n" \
"v_add_u32_e32 v217, s96, v216\n" \
"v_sub_u32_e32 v226, v224, v218\n" \
"v_mfma_f32_16x16x32_bf16 a[28:31], v[56:59], v[176:179], a[28:31]\n" \
"s_add_u32 s62, %[rc], 0x300\n" \
"s_and_b32 s62, s62, 0x3ff\n" \
"v_mfma_f32_16x16x32_bf16 a[28:31], v[52:55], v[172:175], a[28:31]\n" \
"s_lshl_b32 s63, s62, 12\n" \
"v_mul_u32_u24_e32 v228, 0x480, v230\n" \
"v_mfma_f32_16x16x32_bf16 a[60:63], v[60:63], v[172:175], a[60:63]\n" \
"v_and_b32_e32 v220, 0xff, v217\n" \
"v_cmp_gt_u32_e32 vcc, 16, v214\n" \
"v_mfma_f32_16x16x32_bf16 a[60:63], v[64:67], v[176:179], a[60:63]\n" \
"v_lshlrev_b32_e32 v227, 4, v226\n" \
"v_cndmask_b32_e32 v219, v216, v220, vcc\n" \
"v_mfma_f32_16x16x32_bf16 a[92:95], v[72:75], v[176:179], a[92:95]\n" \
"v_add_u32_e32 v230, s96, v227\n" \
"v_and_b32_e32 v229, 0xff, v230\n" \
"v_mfma_f32_16x16x32_bf16 a[92:95], v[68:71], v[172:175], a[92:95]\n" \
"v_cmp_gt_u32_e32 vcc, 16, v226\n" \
"v_mul_u32_u24_e32 v250, 0x480, v213\n" \
"v_mfma_f32_16x16x32_bf16 a[124:127], v[76:79], v[172:175], a[124:127]\n" \
"v_cndmask_b32_e32 v225, v227, v229, vcc\n" \
"v_and_b32_e64 v249, 2, v212\n" \
"v_mfma_f32_16x16x32_bf16 a[124:127], v[80:83], v[176:179], a[124:127]\n" \
"v_mov_b32_e32 v213, 0x100\n" \
"v_mov_b32_e32 v251, s95\n" \
"s_waitcnt vmcnt(4)\n" \
"s_barrier\n" \
"v_mfma_f32_16x16x32_bf16 a[144:147], v[88:91], v[152:155], a[144:147]\n" \
"s_add_u32 m0, %[mA], 0x0\n" \
"s_add_u32 s85, s62, 0x0\n" \
"s_nop 0\n" \
"v_mfma_f32_16x16x32_bf16 a[144:147], v[84:87], v[148:151], a[144:147]\n" \
"buffer_load_dwordx4 v22, s[64:67], s85 offen lds\n" \
"v_mfma_f32_16x16x32_bf16 a[176:179], v[92:95], v[148:151], a[176:179]\n" \
"ds_read_b128 v[56:59], v29 offset:49152\n" \
"ds_read_b128 v[52:55], v28 offset:49152\n" \
"v_mfma_f32_16x16x32_bf16 a[176:179], v[96:99], v[152:155], a[176:179]\n" \
"s_add_u32 m0, %[mA], 0x400\n" \
"s_add_u32 s84, s62, 0x2000\n" \
"v_mfma_f32_16x16x32_bf16 a[208:211], v[104:107], v[152:155], a[208:211]\n" \
"buffer_load_dwordx4 v23, s[64:67], s84 offen lds\n" \
"v_mfma_f32_16x16x32_bf16 a[208:211], v[100:103], v[148:151], a[208:211]\n" \
"ds_read_b128 v[60:63], v28 offset:51200\n" \
"ds_read_b128 v[64:67], v29 offset:51200\n" \
"v_mfma_f32_16x16x32_bf16 a[240:243], v[108:111], v[148:151], a[240:243]\n" \
"ds_read_b64_tr_b16 v[148:149], v32 offset:49152\n" \
"ds_read_b64_tr_b16 v[150:151], v32 offset:50176\n" \
"v_mfma_f32_16x16x32_bf16 a[240:243], v[112:115], v[152:155], a[240:243]\n" \
"s_add_u32 m0, %[mB], 0x0\n" \
"s_add_u32 s85, s63, 0x0\n" \
"v_mfma_f32_16x16x32_bf16 a[148:151], v[88:91], v[160:163], a[148:151]\n" \
"buffer_load_dwordx4 v26, s[68:71], s85 offen lds\n" \
"v_mfma_f32_16x16x32_bf16 a[148:151], v[84:87], v[156:159], a[148:151]\n" \
"ds_read_b64_tr_b16 v[152:153], v32 offset:57344\n" \
"ds_read_b64_tr_b16 v[154:155], v32 offset:58368\n" \
"v_mfma_f32_16x16x32_bf16 a[180:183], v[92:95], v[156:159], a[180:183]\n" \
"ds_read_b128 v[72:75], v29 offset:53248\n" \
"ds_read_b128 v[68:71], v28 offset:53248\n" \
"v_mfma_f32_16x16x32_bf16 a[180:183], v[96:99], v[160:163], a[180:183]\n" \
"s_add_u32 m0, %[mB], 0x4000\n" \
"s_add_u32 s84, s63, 0x0\n" \
"s_nop 0\n" \
"v_mfma_f32_16x16x32_bf16 a[212:215], v[104:107], v[160:163], a[212:215]\n" \
"buffer_load_dwordx4 v26, s[72:75], s84 offen lds\n" \
"v_mfma_f32_16x16x32_bf16 a[212:215], v[100:103], v[156:159], a[212:215]\n" \
"ds_read_b128 v[76:79], v28 offset:55296\n" \
"ds_read_b128 v[80:83], v29 offset:55296\n" \
"v_mfma_f32_16x16x32_bf16 a[244:247], v[108:111], v[156:159], a[244:247]\n" \
"ds_read_b64_tr_b16 v[156:157], v33 offset:49152\n" \
"ds_read_b64_tr_b16 v[158:159], v33 offset:50176\n" \
"v_mfma_f32_16x16x32_bf16 a[244:247], v[112:115], v[160:163], a[244:247]\n" \
"s_add_u32 m0, %[mA], 0x800\n" \
"s_add_u32 s85, s62, 0x4000\n" \
"v_mfma_f32_16x16x32_bf16 a[152:155], v[88:91], v[168:171], a[152:155]\n" \
"buffer_load_dwordx4 v22, s[64:67], s85 offen lds\n" \
"v_mfma_f32_16x16x32_bf16 a[152:155], v[84:87], v[164:167], a[152:155]\n" \
"ds_read_b64_tr_b16 v[160:161], v33 offset:57344\n" \
"ds_read_b64_tr_b16 v[162:163], v33 offset:58368\n" \
"v_mfma_f32_16x16x32_bf16 a[184:187], v[92:95], v[164:167], a[184:187]\n" \
"v_cmp_ne_u32_e32 vcc, 0, v249\n" \
"s_mov_b32 s76, %[xl]\n" \
"v_mfma_f32_16x16x32_bf16 a[184:187], v[96:99], v[168:171], a[184:187]\n" \
"s_add_u32 m0, %[mA], 0xc00\n" \
"s_add_u32 s84, s62, 0x6000\n" \
"v_mfma_f32_16x16x32_bf16 a[216:219], v[104:107], v[168:171], a[216:219]\n" \
"buffer_load_dwordx4 v23, s[64:67], s84 offen lds\n" \
"v_mfma_f32_16x16x32_bf16 a[216:219], v[100:103], v[164:167], a[216:219]\n" \
"v_cndmask_b32_e32 v247, v251, v213, vcc\n" \
"s_nop 0\n" \
"s_and_b32 s77, %[xh], 0xffff\n" \
"v_mfma_f32_16x16x32_bf16 a[248:251], v[108:111], v[164:167], a[248:251]\n" \
"ds_read_b64_tr_b16 v[164:165], v34 offset:49152\n" \
"ds_read_b64_tr_b16 v[166:167], v34 offset:50176\n" \
"v_mfma_f32_16x16x32_bf16 a[248:251], v[112:115], v[168:171], a[248:251]\n" \
"s_add_u32 m0, %[mB], 0x400\n" \
"s_add_u32 s85, s63, 0x8000\n" \
"v_mfma_f32_16x16x32_bf16 a[156:159], v[88:91], v[176:179], a[156:159]\n" \
"buffer_load_dwordx4 v26, s[68:71], s85 offen lds\n" \
"v_mfma_f32_16x16x32_bf16 a[156:159], v[84:87], v[172:175], a[156:159]\n" \
"ds_read_b64_tr_b16 v[168:169], v34 offset:57344\n" \
"ds_read_b64_tr_b16 v[170:171], v34 offset:58368\n" \
"v_mfma_f32_16x16x32_bf16 a[188:191], v[92:95], v[172:175], a[188:191]\n" \
"s_mov_b32 s78, 0x240000\n" \
"v_xor_b32_e32 v39, 0x60, v190\n" \
"v_mfma_f32_16x16x32_bf16 a[188:191], v[96:99], v[176:179], a[188:191]\n" \
"s_add_u32 m0, %[mB], 0x4400\n" \
"s_add_u32 s84, s63, 0x8000\n" \
"v_mfma_f32_16x16x32_bf16 a[220:223], v[104:107], v[176:179], a[220:223]\n" \
"buffer_load_dwordx4 v26, s[72:75], s84 offen lds\n" \
"v_mfma_f32_16x16x32_bf16 a[220:223], v[100:103], v[172:175], a[220:223]\n" \
"v_xor_b32_e32 v37, 0x20, v190\n" \
"v_xor_b32_e32 v38, 0x40, v190\n" \
"v_mfma_f32_16x16x32_bf16 a[252:255], v[108:111], v[172:175], a[252:255]\n" \
"ds_read_b64_tr_b16 v[172:173], v35 offset:49152\n" \
"ds_read_b64_tr_b16 v[174:175], v35 offset:50176\n" \
"v_mfma_f32_16x16x32_bf16 a[252:255], v[112:115], v[176:179], a[252:255]\n" \
"s_add_u32 m0, %[mA], 0x1000\n" \
"s_add_u32 s85, s62, 0x8000\n" \
"v_mfma_f32_16x16x32_bf16 a[128:131], v[120:123], v[88:91], a[128:131]\n" \
"buffer_load_dwordx4 v22, s[64:67], s85 offen lds\n" \
"v_mfma_f32_16x16x32_bf16 a[128:131], v[116:119], v[84:87], a[128:131]\n" \
"ds_read_b64_tr_b16 v[176:177], v35 offset:57344\n" \
"ds_read_b64_tr_b16 v[178:179], v35 offset:58368\n" \
"v_mfma_f32_16x16x32_bf16 a[160:163], v[116:119], v[92:95], a[160:163]\n" \
"v_add_u32_e32 v48, s50, v28\n" \
"v_add_u32_e32 v49, s50, v29\n" \
"v_mfma_f32_16x16x32_bf16 a[160:163], v[120:123], v[96:99], a[160:163]\n" \
"s_add_u32 m0, %[mA], 0x1400\n" \
"s_add_u32 s84, s62, 0xa000\n" \
"v_mfma_f32_16x16x32_bf16 a[192:195], v[120:123], v[104:107], a[192:195]\n" \
"buffer_load_dwordx4 v23, s[64:67], s84 offen lds\n" \
"v_mfma_f32_16x16x32_bf16 a[192:195], v[116:119], v[100:103], a[192:195]\n" \
"v_xor_b32_e32 v36, 0x0, v190\n" \
"v_add_u32_e32 v237, v228, v233\n" \
"v_mfma_f32_16x16x32_bf16 a[224:227], v[116:119], v[108:111], a[224:227]\n" \
"ds_read_b64_tr_b16 v[116:117], v32 offset:32768\n" \
"ds_read_b64_tr_b16 v[118:119], v32 offset:33792\n" \
"v_mfma_f32_16x16x32_bf16 a[224:227], v[120:123], v[112:115], a[224:227]\n" \
"s_add_u32 m0, %[mB], 0x800\n" \
"s_add_u32 s85, s63, 0x10000\n" \
"v_mfma_f32_16x16x32_bf16 a[132:135], v[128:131], v[88:91], a[132:135]\n" \
"buffer_load_dwordx4 v27, s[68:71], s85 offen lds\n" \
"v_mfma_f32_16x16x32_bf16 a[132:135], v[124:127], v[84:87], a[132:135]\n" \
"ds_read_b64_tr_b16 v[120:121], v32 offset:40960\n" \
"ds_read_b64_tr_b16 v[122:123], v32 offset:41984\n" \
"v_mfma_f32_16x16x32_bf16 a[164:167], v[124:127], v[92:95], a[164:167]\n" \
"s_lshl_b32 s97, %[w], 4\n" \
"v_lshrrev_b32_e32 v234, 2, v212\n" \
"v_mfma_f32_16x16x32_bf16 a[164:167], v[128:131], v[96:99], a[164:167]\n" \
"s_add_u32 m0, %[mB], 0x4800\n" \
"s_add_u32 s84, s63, 0x10000\n" \
"v_mfma_f32_16x16x32_bf16 a[196:199], v[128:131], v[104:107], a[196:199]\n" \
"buffer_load_dwordx4 v27, s[72:75], s84 offen lds\n" \
"v_mfma_f32_16x16x32_bf16 a[196:199], v[124:127], v[100:103], a[196:199]\n" \
"v_add_u32_e32 v5, 0xfffffc00, v237\n" \
"v_add_u32_e64 v231, s97, v234\n" \
"v_mfma_f32_16x16x32_bf16 a[228:231], v[124:127], v[108:111], a[228:231]\n" \
"ds_read_b64_tr_b16 v[124:125], v33 offset:32768\n" \
"ds_read_b64_tr_b16 v[126:127], v33 offset:33792\n" \
"v_mfma_f32_16x16x32_bf16 a[228:231], v[128:131], v[112:115], a[228:231]\n" \
"ds_read_b64_tr_b16 v[128:129], v33 offset:40960\n" \
"ds_read_b64_tr_b16 v[130:131], v33 offset:41984\n" \
"v_mfma_f32_16x16x32_bf16 a[136:139], v[136:139], v[88:91], a[136:139]\n" \
"v_lshrrev_b32_e32 v235, 5, v231\n" \
"v_lshrrev_b32_e32 v237, 4, v231\n" \
"v_mfma_f32_16x16x32_bf16 a[136:139], v[132:135], v[84:87], a[136:139]\n" \
"v_mul_u32_u24_e32 v239, 0x480, v241\n" \
"v_lshlrev_b32_e32 v232, 4, v235\n" \
"v_mfma_f32_16x16x32_bf16 a[168:171], v[132:135], v[92:95], a[168:171]\n" \
"v_and_b32_e32 v228, 12, v231\n" \
"v_and_b32_e64 v238, 1, v237\n" \
"v_mfma_f32_16x16x32_bf16 a[168:171], v[136:139], v[96:99], a[168:171]\n" \
"v_add_u32_e32 v233, v232, v228\n" \
"v_lshlrev_b32_e32 v241, 1, v238\n" \
"v_mfma_f32_16x16x32_bf16 a[200:203], v[136:139], v[104:107], a[200:203]\n" \
"v_lshrrev_b32_e32 v236, 1, v231\n" \
"v_add_u32_e32 v240, v233, v241\n" \
"v_mfma_f32_16x16x32_bf16 a[200:203], v[132:135], v[100:103], a[200:203]\n" \
"v_and_b32_e32 v245, 1, v236\n" \
"v_add_u32_e32 v242, v240, v245\n" \
"v_mfma_f32_16x16x32_bf16 a[232:235], v[132:135], v[108:111], a[232:235]\n" \
"ds_read_b64_tr_b16 v[132:133], v34 offset:32768\n" \
"ds_read_b64_tr_b16 v[134:135], v34 offset:33792\n" \
"v_mfma_f32_16x16x32_bf16 a[232:235], v[136:139], v[112:115], a[232:235]\n" \
"ds_read_b64_tr_b16 v[136:137], v34 offset:40960\n" \
"ds_read_b64_tr_b16 v[138:139], v34 offset:41984\n" \
"v_mfma_f32_16x16x32_bf16 a[140:143], v[144:147], v[88:91], a[140:143]\n" \
"ds_read_b128 v[88:91], v29 offset:32768\n" \
"v_add_u32_e64 v248, v239, v244\n" \
"v_mfma_f32_16x16x32_bf16 a[140:143], v[140:143], v[84:87], a[140:143]\n" \
"ds_read_b128 v[84:87], v28 offset:32768\n" \
"v_mul_u32_u24_e32 v246, 18, v242\n" \
"v_mfma_f32_16x16x32_bf16 a[172:175], v[140:143], v[92:95], a[172:175]\n" \
"ds_read_b128 v[92:95], v28 offset:34816\n" \
"v_add_u32_e32 v243, 16, v246\n" \
"v_mfma_f32_16x16x32_bf16 a[172:175], v[144:147], v[96:99], a[172:175]\n" \
"ds_read_b128 v[96:99], v29 offset:34816\n" \
"v_and_b32_e64 v239, 1, v231\n" \
"v_mfma_f32_16x16x32_bf16 a[204:207], v[144:147], v[104:107], a[204:207]\n" \
"ds_read_b128 v[104:107], v29 offset:36864\n" \
"v_add_u32_e32 v6, 0xfffff800, v248\n" \
"v_mfma_f32_16x16x32_bf16 a[204:207], v[140:143], v[100:103], a[204:207]\n" \
"ds_read_b128 v[100:103], v28 offset:36864\n" \
"v_add_u32_e64 v244, v243, v239\n" \
"v_mfma_f32_16x16x32_bf16 a[236:239], v[140:143], v[108:111], a[236:239]\n" \
"ds_read_b64_tr_b16 v[140:141], v35 offset:32768\n" \
"ds_read_b64_tr_b16 v[142:143], v35 offset:33792\n" \
"v_mfma_f32_16x16x32_bf16 a[236:239], v[144:147], v[112:115], a[236:239]\n" \
"s_add_u32 m0, %[mA], 0x1800\n" \
"s_waitcnt lgkmcnt(12)\n" \
"s_nop 0\n" \
"v_mfma_f32_16x16x32_bf16 a[16:19], v[56:59], v[152:155], a[16:19]\n" \
"s_add_u32 s85, s62, 0xc000\n" \
"ds_read_b64_tr_b16 v[144:145], v35 offset:40960\n" \
"v_mfma_f32_16x16x32_bf16 a[16:19], v[52:55], v[148:151], a[16:19]\n" \
"buffer_load_dwordx4 v22, s[64:67], s85 offen lds\n" \
"v_mfma_f32_16x16x32_bf16 a[48:51], v[60:63], v[148:151], a[48:51]\n" \
"s_add_u32 m0, %[mA], 0x1c00\n" \
"s_add_u32 s84, s62, 0xe000\n" \
"v_mfma_f32_16x16x32_bf16 a[48:51], v[64:67], v[152:155], a[48:51]\n" \
"ds_read_b64_tr_b16 v[146:147], v35 offset:41984\n" \
"ds_read_b128 v[108:111], v28 offset:38912\n" \
"v_mfma_f32_16x16x32_bf16 a[80:83], v[72:75], v[152:155], a[80:83]\n" \
"buffer_load_dwordx4 v23, s[64:67], s84 offen lds\n" \
"v_mfma_f32_16x16x32_bf16 a[80:83], v[68:71], v[148:151], a[80:83]\n" \
"s_add_u32 m0, %[mB], 0xc00\n" \
"s_add_u32 s85, s63, 0x18000\n" \
"v_mfma_f32_16x16x32_bf16 a[112:115], v[76:79], v[148:151], a[112:115]\n" \
"ds_read_b128 v[112:115], v29 offset:38912\n" \
"v_mul_u32_u24_e32 v248, 0x480, v244\n" \
"v_mfma_f32_16x16x32_bf16 a[112:115], v[80:83], v[152:155], a[112:115]\n" \
"buffer_load_dwordx4 v27, s[68:71], s85 offen lds\n" \
"v_mfma_f32_16x16x32_bf16 a[20:23], v[56:59], v[160:163], a[20:23]\n" \
"s_add_u32 m0, %[mB], 0x4c00\n" \
"s_add_u32 s84, s63, 0x18000\n" \
"v_mfma_f32_16x16x32_bf16 a[20:23], v[52:55], v[156:159], a[20:23]\n" \
"v_add_u32_e32 v215, v250, v219\n" \
"v_add_u32_e32 v217, v248, v247\n" \
"v_mfma_f32_16x16x32_bf16 a[52:55], v[60:63], v[156:159], a[52:55]\n" \
"buffer_load_dwordx4 v27, s[72:75], s84 offen lds\n" \
"v_mfma_f32_16x16x32_bf16 a[52:55], v[64:67], v[160:163], a[52:55]\n" \
"v_and_b32_e64 v214, 16, v20\n" \
"v_add_u32_e32 v7, 0xfffff400, v215\n" \
"v_mfma_f32_16x16x32_bf16 a[84:87], v[72:75], v[160:163], a[84:87]\n" \
"v_add_u32_e32 v9, v217, v214\n" \
"v_and_b32_e32 v216, 3, v212\n" \
"v_mfma_f32_16x16x32_bf16 a[84:87], v[68:71], v[156:159], a[84:87]\n" \
"v_lshlrev_b32_e32 v250, 3, v216\n" \
"v_mul_u32_u24_e32 v220, 0x120, v234\n" \
"v_mfma_f32_16x16x32_bf16 a[116:119], v[76:79], v[156:159], a[116:119]\n" \
"v_mul_u32_u24_e32 v223, 0x480, v222\n" \
"v_add_u32_e32 v10, v220, v250\n" \
"v_mfma_f32_16x16x32_bf16 a[116:119], v[80:83], v[160:163], a[116:119]\n" \
"s_mov_b32 m0, %[mS]\n" \
"s_mov_b32 s86, %[xl]\n" \
"v_mfma_f32_16x16x32_bf16 a[24:27], v[56:59], v[168:171], a[24:27]\n" \
"s_mov_b32 s87, %[xh]\n" \
"v_add_u32_e32 v8, v223, v225\n" \
"v_mfma_f32_16x16x32_bf16 a[24:27], v[52:55], v[164:167], a[24:27]\n" \
"s_lshl_b32 s99, %[w], 11\n" \
"s_add_u32 s61, s98, s99\n" \
"v_mfma_f32_16x16x32_bf16 a[56:59], v[60:63], v[164:167], a[56:59]\n" \
"v_mfma_f32_16x16x32_bf16 a[56:59], v[64:67], v[168:171], a[56:59]\n" \
"v_mfma_f32_16x16x32_bf16 a[88:91], v[72:75], v[168:171], a[88:91]\n" \
"v_mfma_f32_16x16x32_bf16 a[88:91], v[68:71], v[164:167], a[88:91]\n" \
"s_mov_b32 exec_lo, 0x0\n" \
"s_mov_b32 exec_hi, 0x3ffff0\n" \
"global_load_lds_dword v188, s[86:87]\n" \
"s_mov_b64 exec, -1\n" \
"v_mfma_f32_16x16x32_bf16 a[120:123], v[76:79], v[164:167], a[120:123]\n" \
"v_mfma_f32_16x16x32_bf16 a[120:123], v[80:83], v[168:171], a[120:123]\n" \
"v_mfma_f32_16x16x32_bf16 a[28:31], v[56:59], v[176:179], a[28:31]\n" \
"v_mfma_f32_16x16x32_bf16 a[28:31], v[52:55], v[172:175], a[28:31]\n" \
"v_mfma_f32_16x16x32_bf16 a[60:63], v[60:63], v[172:175], a[60:63]\n" \
"v_mfma_f32_16x16x32_bf16 a[60:63], v[64:67], v[176:179], a[60:63]\n" \
"v_mfma_f32_16x16x32_bf16 a[92:95], v[72:75], v[176:179], a[92:95]\n" \
"v_mfma_f32_16x16x32_bf16 a[92:95], v[68:71], v[172:175], a[92:95]\n" \
"v_mfma_f32_16x16x32_bf16 a[124:127], v[76:79], v[172:175], a[124:127]\n" \
"v_mfma_f32_16x16x32_bf16 a[124:127], v[80:83], v[176:179], a[124:127]\n" \
"v_mfma_f32_16x16x32_bf16 a[0:3], v[120:123], v[56:59], a[0:3]\n" \
"v_mfma_f32_16x16x32_bf16 a[0:3], v[116:119], v[52:55], a[0:3]\n" \
"v_mfma_f32_16x16x32_bf16 a[32:35], v[116:119], v[60:63], a[32:35]\n" \
"v_mfma_f32_16x16x32_bf16 a[32:35], v[120:123], v[64:67], a[32:35]\n" \
"v_mfma_f32_16x16x32_bf16 a[64:67], v[120:123], v[72:75], a[64:67]\n" \
"v_mfma_f32_16x16x32_bf16 a[64:67], v[116:119], v[68:71], a[64:67]\n" \
"v_mfma_f32_16x16x32_bf16 a[96:99], v[116:119], v[76:79], a[96:99]\n" \
"v_mfma_f32_16x16x32_bf16 a[96:99], v[120:123], v[80:83], a[96:99]\n" \
"v_mfma_f32_16x16x32_bf16 a[4:7], v[128:131], v[56:59], a[4:7]\n" \
"v_mfma_f32_16x16x32_bf16 a[4:7], v[124:127], v[52:55], a[4:7]\n" \
"v_mfma_f32_16x16x32_bf16 a[36:39], v[124:127], v[60:63], a[36:39]\n" \
"v_mfma_f32_16x16x32_bf16 a[36:39], v[128:131], v[64:67], a[36:39]\n" \
"v_mfma_f32_16x16x32_bf16 a[68:71], v[128:131], v[72:75], a[68:71]\n" \
"v_mfma_f32_16x16x32_bf16 a[68:71], v[124:127], v[68:71], a[68:71]\n" \
"v_mfma_f32_16x16x32_bf16 a[100:103], v[124:127], v[76:79], a[100:103]\n" \
"v_mfma_f32_16x16x32_bf16 a[100:103], v[128:131], v[80:83], a[100:103]\n" \
"s_waitcnt lgkmcnt(0)\n" \
"s_nop 0\n" \
"v_mfma_f32_16x16x32_bf16 a[8:11], v[136:139], v[56:59], a[8:11]\n" \
"v_mfma_f32_16x16x32_bf16 a[8:11], v[132:135], v[52:55], a[8:11]\n" \
"v_mfma_f32_16x16x32_bf16 a[40:43], v[132:135], v[60:63], a[40:43]\n" \
"v_mfma_f32_16x16x32_bf16 a[40:43], v[136:139], v[64:67], a[40:43]\n" \
"v_mfma_f32_16x16x32_bf16 a[72:75], v[136:139], v[72:75], a[72:75]\n" \
"v_mfma_f32_16x16x32_bf16 a[72:75], v[132:135], v[68:71], a[72:75]\n" \
"v_mfma_f32_16x16x32_bf16 a[104:107], v[132:135], v[76:79], a[104:107]\n" \
"v_mfma_f32_16x16x32_bf16 a[104:107], v[136:139], v[80:83], a[104:107]\n" \
"v_mfma_f32_16x16x32_bf16 a[12:15], v[144:147], v[56:59], a[12:15]\n" \
"s_add_u32 s62, %[rc], 0x380\n" \
"s_and_b32 s62, s62, 0x3ff\n" \
"v_mfma_f32_16x16x32_bf16 a[12:15], v[140:143], v[52:55], a[12:15]\n" \
"s_lshl_b32 s63, s62, 12\n" \
"v_mfma_f32_16x16x32_bf16 a[44:47], v[140:143], v[60:63], a[44:47]\n" \
"v_mfma_f32_16x16x32_bf16 a[44:47], v[144:147], v[64:67], a[44:47]\n" \
"v_mfma_f32_16x16x32_bf16 a[76:79], v[144:147], v[72:75], a[76:79]\n" \
"v_mfma_f32_16x16x32_bf16 a[76:79], v[140:143], v[68:71], a[76:79]\n" \
"v_mfma_f32_16x16x32_bf16 a[108:111], v[140:143], v[76:79], a[108:111]\n" \
"v_mfma_f32_16x16x32_bf16 a[108:111], v[144:147], v[80:83], a[108:111]\n" \
"s_waitcnt vmcnt(1)\n" \
"s_barrier\n" \
"s_mov_b64 exec, 1\n" \
"buffer_store_dword v45, off, s[80:83], s41 offset:64 sc1\n" \
"s_mov_b64 exec, -1\n" \
"v_mfma_f32_16x16x32_bf16 a[128:131], v[120:123], v[88:91], a[128:131]\n" \
"s_add_u32 m0, %[mA], 0x8000\n" \
"s_add_u32 s85, s62, 0x0\n" \
"v_mfma_f32_16x16x32_bf16 a[128:131], v[116:119], v[84:87], a[128:131]\n" \
"ds_read_b128 v[56:59], v29 offset:16384\n" \
"ds_read_b128 v[52:55], v28 offset:16384\n" \
"v_mfma_f32_16x16x32_bf16 a[160:163], v[116:119], v[92:95], a[160:163]\n" \
"buffer_load_dwordx4 v22, s[64:67], s85 offen lds\n" \
"v_mfma_f32_16x16x32_bf16 a[160:163], v[120:123], v[96:99], a[160:163]\n" \
"s_add_u32 m0, %[mA], 0x8400\n" \
"s_add_u32 s84, s62, 0x2000\n" \
"v_mfma_f32_16x16x32_bf16 a[192:195], v[120:123], v[104:107], a[192:195]\n" \
"ds_read_b128 v[60:63], v28 offset:18432\n" \
"ds_read_b128 v[64:67], v29 offset:18432\n" \
"v_mfma_f32_16x16x32_bf16 a[192:195], v[116:119], v[100:103], a[192:195]\n" \
"buffer_load_dwordx4 v23, s[64:67], s84 offen lds\n" \
"v_mfma_f32_16x16x32_bf16 a[224:227], v[116:119], v[108:111], a[224:227]\n" \
"ds_read_b64_tr_b16 v[116:117], v32 offset:0\n" \
"ds_read_b64_tr_b16 v[118:119], v32 offset:1024\n" \
"v_mfma_f32_16x16x32_bf16 a[224:227], v[120:123], v[112:115], a[224:227]\n" \
"s_add_u32 m0, %[mB], 0x8000\n" \
"s_add_u32 s85, s63, 0x0\n" \
"v_mfma_f32_16x16x32_bf16 a[132:135], v[128:131], v[88:91], a[132:135]\n" \
"buffer_load_dwordx4 v26, s[68:71], s85 offen lds\n" \
"v_mfma_f32_16x16x32_bf16 a[132:135], v[124:127], v[84:87], a[132:135]\n" \
"ds_read_b64_tr_b16 v[120:121], v32 offset:8192\n" \
"ds_read_b64_tr_b16 v[122:123], v32 offset:9216\n" \
"v_mfma_f32_16x16x32_bf16 a[164:167], v[124:127], v[92:95], a[164:167]\n" \
"ds_read_b128 v[72:75], v29 offset:20480\n" \
"ds_read_b128 v[68:71], v28 offset:20480\n" \
"v_mfma_f32_16x16x32_bf16 a[164:167], v[128:131], v[96:99], a[164:167]\n" \
"s_add_u32 m0, %[mB], 0xc000\n" \
"s_add_u32 s84, s63, 0x0\n" \
"v_mfma_f32_16x16x32_bf16 a[196:199], v[128:131], v[104:107], a[196:199]\n" \
"buffer_load_dwordx4 v26, s[72:75], s84 offen lds\n" \
"v_mfma_f32_16x16x32_bf16 a[196:199], v[124:127], v[100:103], a[196:199]\n" \
"ds_read_b128 v[76:79], v28 offset:22528\n" \
"ds_read_b128 v[80:83], v29 offset:22528\n" \
"v_mfma_f32_16x16x32_bf16 a[228:231], v[124:127], v[108:111], a[228:231]\n" \
"ds_read_b64_tr_b16 v[124:125], v33 offset:0\n" \
"ds_read_b64_tr_b16 v[126:127], v33 offset:1024\n" \
"v_mfma_f32_16x16x32_bf16 a[228:231], v[128:131], v[112:115], a[228:231]\n" \
"s_add_u32 m0, %[mA], 0x8800\n" \
"s_add_u32 s85, s62, 0x4000\n" \
"v_mfma_f32_16x16x32_bf16 a[136:139], v[136:139], v[88:91], a[136:139]\n" \
"buffer_load_dwordx4 v22, s[64:67], s85 offen lds\n" \
"v_mfma_f32_16x16x32_bf16 a[136:139], v[132:135], v[84:87], a[136:139]\n" \
"ds_read_b64_tr_b16 v[128:129], v33 offset:8192\n" \
"ds_read_b64_tr_b16 v[130:131], v33 offset:9216\n" \
"v_mfma_f32_16x16x32_bf16 a[168:171], v[132:135], v[92:95], a[168:171]\n" \
"v_mov_b32_e64 v45, %[tag]\n" \
"v_mfma_f32_16x16x32_bf16 a[168:171], v[136:139], v[96:99], a[168:171]\n" \
"s_add_u32 m0, %[mA], 0x8c00\n" \
"s_add_u32 s84, s62, 0x6000\n" \
"v_mfma_f32_16x16x32_bf16 a[200:203], v[136:139], v[104:107], a[200:203]\n" \
"buffer_load_dwordx4 v23, s[64:67], s84 offen lds\n" \
"v_mfma_f32_16x16x32_bf16 a[200:203], v[132:135], v[100:103], a[200:203]\n" \
"v_mfma_f32_16x16x32_bf16 a[232:235], v[132:135], v[108:111], a[232:235]\n" \
"ds_read_b64_tr_b16 v[132:133], v34 offset:0\n" \
"ds_read_b64_tr_b16 v[134:135], v34 offset:1024\n" \
"v_mfma_f32_16x16x32_bf16 a[232:235], v[136:139], v[112:115], a[232:235]\n" \
"s_add_u32 m0, %[mB], 0x8400\n" \
"s_add_u32 s85, s63, 0x8000\n" \
"v_mfma_f32_16x16x32_bf16 a[140:143], v[144:147], v[88:91], a[140:143]\n" \
"buffer_load_dwordx4 v26, s[68:71], s85 offen lds\n" \
"v_mfma_f32_16x16x32_bf16 a[140:143], v[140:143], v[84:87], a[140:143]\n" \
"ds_read_b64_tr_b16 v[136:137], v34 offset:8192\n" \
"ds_read_b64_tr_b16 v[138:139], v34 offset:9216\n" \
"v_mfma_f32_16x16x32_bf16 a[172:175], v[140:143], v[92:95], a[172:175]\n" \
"v_mfma_f32_16x16x32_bf16 a[172:175], v[144:147], v[96:99], a[172:175]\n" \
"s_add_u32 m0, %[mB], 0xc400\n" \
"s_add_u32 s84, s63, 0x8000\n" \
"v_mfma_f32_16x16x32_bf16 a[204:207], v[144:147], v[104:107], a[204:207]\n" \
"buffer_load_dwordx4 v26, s[72:75], s84 offen lds\n" \
"v_mfma_f32_16x16x32_bf16 a[204:207], v[140:143], v[100:103], a[204:207]\n" \
"v_mfma_f32_16x16x32_bf16 a[236:239], v[140:143], v[108:111], a[236:239]\n" \
"ds_read_b64_tr_b16 v[140:141], v35 offset:0\n" \
"ds_read_b64_tr_b16 v[142:143], v35 offset:1024\n" \
"v_mfma_f32_16x16x32_bf16 a[236:239], v[144:147], v[112:115], a[236:239]\n" \
"s_add_u32 m0, %[mA], 0x9000\n" \
"s_add_u32 s85, s62, 0x8000\n" \
"v_mfma_f32_16x16x32_bf16 a[144:147], v[88:91], v[152:155], a[144:147]\n" \
"buffer_load_dwordx4 v22, s[64:67], s85 offen lds\n" \
"v_mfma_f32_16x16x32_bf16 a[144:147], v[84:87], v[148:151], a[144:147]\n" \
"ds_read_b64_tr_b16 v[144:145], v35 offset:8192\n" \
"ds_read_b64_tr_b16 v[146:147], v35 offset:9216\n" \
"v_mfma_f32_16x16x32_bf16 a[176:179], v[92:95], v[148:151], a[176:179]\n" \
"v_mfma_f32_16x16x32_bf16 a[176:179], v[96:99], v[152:155], a[176:179]\n" \
"s_add_u32 m0, %[mA], 0x9400\n" \
"s_add_u32 s84, s62, 0xa000\n" \
"v_mfma_f32_16x16x32_bf16 a[208:211], v[104:107], v[152:155], a[208:211]\n" \
"buffer_load_dwordx4 v23, s[64:67], s84 offen lds\n" \
"v_mfma_f32_16x16x32_bf16 a[208:211], v[100:103], v[148:151], a[208:211]\n" \
"v_mfma_f32_16x16x32_bf16 a[240:243], v[108:111], v[148:151], a[240:243]\n" \
"ds_read_b64_tr_b16 v[148:149], v32 offset:16384\n" \
"ds_read_b64_tr_b16 v[150:151], v32 offset:17408\n" \
"v_mfma_f32_16x16x32_bf16 a[240:243], v[112:115], v[152:155], a[240:243]\n" \
"s_add_u32 m0, %[mB], 0x8800\n" \
"s_add_u32 s85, s63, 0x10000\n" \
"v_mfma_f32_16x16x32_bf16 a[148:151], v[88:91], v[160:163], a[148:151]\n" \
"buffer_load_dwordx4 v27, s[68:71], s85 offen lds\n" \
"v_mfma_f32_16x16x32_bf16 a[148:151], v[84:87], v[156:159], a[148:151]\n" \
"ds_read_b64_tr_b16 v[152:153], v32 offset:24576\n" \
"ds_read_b64_tr_b16 v[154:155], v32 offset:25600\n" \
"v_mfma_f32_16x16x32_bf16 a[180:183], v[92:95], v[156:159], a[180:183]\n" \
"v_mfma_f32_16x16x32_bf16 a[180:183], v[96:99], v[160:163], a[180:183]\n" \
"s_add_u32 m0, %[mB], 0xc800\n" \
"s_add_u32 s84, s63, 0x10000\n" \
"v_mfma_f32_16x16x32_bf16 a[212:215], v[104:107], v[160:163], a[212:215]\n" \
"buffer_load_dwordx4 v27, s[72:75], s84 offen lds\n" \
"v_mfma_f32_16x16x32_bf16 a[212:215], v[100:103], v[156:159], a[212:215]\n" \
"v_mfma_f32_16x16x32_bf16 a[244:247], v[108:111], v[156:159], a[244:247]\n" \
"ds_read_b64_tr_b16 v[156:157], v33 offset:16384\n" \
"ds_read_b64_tr_b16 v[158:159], v33 offset:17408\n" \
"v_mfma_f32_16x16x32_bf16 a[244:247], v[112:115], v[160:163], a[244:247]\n" \
"ds_read_b64_tr_b16 v[160:161], v33 offset:24576\n" \
"ds_read_b64_tr_b16 v[162:163], v33 offset:25600\n" \
"v_mfma_f32_16x16x32_bf16 a[152:155], v[88:91], v[168:171], a[152:155]\n" \
"v_mfma_f32_16x16x32_bf16 a[152:155], v[84:87], v[164:167], a[152:155]\n" \
"L_pf3:\n" \
"s_cbranch_execz L_pf7\n" \
"s_nop 0\n" \
"v_mfma_f32_16x16x32_bf16 a[184:187], v[92:95], v[164:167], a[184:187]\n" \
"v_mfma_f32_16x16x32_bf16 a[184:187], v[96:99], v[168:171], a[184:187]\n" \
"v_mfma_f32_16x16x32_bf16 a[216:219], v[104:107], v[168:171], a[216:219]\n" \
"v_mfma_f32_16x16x32_bf16 a[216:219], v[100:103], v[164:167], a[216:219]\n" \
"v_mfma_f32_16x16x32_bf16 a[248:251], v[108:111], v[164:167], a[248:251]\n" \
"ds_read_b64_tr_b16 v[164:165], v34 offset:16384\n" \
"ds_read_b64_tr_b16 v[166:167], v34 offset:17408\n" \
"v_mfma_f32_16x16x32_bf16 a[248:251], v[112:115], v[168:171], a[248:251]\n" \
"ds_read_b64_tr_b16 v[168:169], v34 offset:24576\n" \
"ds_read_b64_tr_b16 v[170:171], v34 offset:25600\n" \
"v_mfma_f32_16x16x32_bf16 a[156:159], v[88:91], v[176:179], a[156:159]\n" \
"ds_read_b128 v[88:91], v29 offset:0\n" \
"v_mfma_f32_16x16x32_bf16 a[156:159], v[84:87], v[172:175], a[156:159]\n" \
"ds_read_b128 v[84:87], v28 offset:0\n" \
"v_mfma_f32_16x16x32_bf16 a[188:191], v[92:95], v[172:175], a[188:191]\n" \
"ds_read_b128 v[92:95], v28 offset:2048\n" \
"v_mfma_f32_16x16x32_bf16 a[188:191], v[96:99], v[176:179], a[188:191]\n" \
"ds_read_b128 v[96:99], v29 offset:2048\n" \
"v_mfma_f32_16x16x32_bf16 a[220:223], v[104:107], v[176:179], a[220:223]\n" \
"ds_read_b128 v[104:107], v29 offset:4096\n" \
"v_mfma_f32_16x16x32_bf16 a[220:223], v[100:103], v[172:175], a[220:223]\n" \
"ds_read_b128 v[100:103], v28 offset:4096\n" \
"v_mfma_f32_16x16x32_bf16 a[252:255], v[108:111], v[172:175], a[252:255]\n" \
"ds_read_b64_tr_b16 v[172:173], v35 offset:16384\n" \
"ds_read_b64_tr_b16 v[174:175], v35 offset:17408\n" \
"v_mfma_f32_16x16x32_bf16 a[252:255], v[112:115], v[176:179], a[252:255]\n" \
"s_add_u32 m0, %[mA], 0x9800\n" \
"s_waitcnt lgkmcnt(12)\n" \
"s_nop 0\n" \
"v_mfma_f32_16x16x32_bf16 a[0:3], v[120:123], v[56:59], a[0:3]\n" \
"s_add_u32 s85, s62, 0xc000\n" \
"ds_read_b64_tr_b16 v[176:177], v35 offset:24576\n" \
"v_mfma_f32_16x16x32_bf16 a[0:3], v[116:119], v[52:55], a[0:3]\n" \
"buffer_load_dwordx4 v22, s[64:67], s85 offen lds\n" \
"v_mfma_f32_16x16x32_bf16 a[32:35], v[116:119], v[60:63], a[32:35]\n" \
"s_add_u32 m0, %[mA], 0x9c00\n" \
"s_add_u32 s84, s62, 0xe000\n" \
"v_mfma_f32_16x16x32_bf16 a[32:35], v[120:123], v[64:67], a[32:35]\n" \
"ds_read_b64_tr_b16 v[178:179], v35 offset:25600\n" \
"ds_read_b128 v[108:111], v28 offset:6144\n" \
"v_mfma_f32_16x16x32_bf16 a[64:67], v[120:123], v[72:75], a[64:67]\n" \
"buffer_load_dwordx4 v23, s[64:67], s84 offen lds\n" \
"v_mfma_f32_16x16x32_bf16 a[64:67], v[116:119], v[68:71], a[64:67]\n" \
"s_add_u32 m0, %[mB], 0x8c00\n" \
"s_add_u32 s85, s63, 0x18000\n" \
"v_mfma_f32_16x16x32_bf16 a[96:99], v[116:119], v[76:79], a[96:99]\n" \
"ds_read_b128 v[112:115], v29 offset:6144\n" \
"v_add_u32_e64 v23, s51, v29\n" \
"v_mfma_f32_16x16x32_bf16 a[96:99], v[120:123], v[80:83], a[96:99]\n" \
"buffer_load_dwordx4 v27, s[68:71], s85 offen lds\n" \
"v_mfma_f32_16x16x32_bf16 a[4:7], v[128:131], v[56:59], a[4:7]\n" \
"s_add_u32 m0, %[mB], 0xcc00\n" \
"s_add_u32 s84, s63, 0x18000\n" \
"v_mfma_f32_16x16x32_bf16 a[4:7], v[124:127], v[52:55], a[4:7]\n" \
"s_mov_b32 s68, %[wql]\n" \
"s_and_b32 s69, %[wqh], 0xffff\n" \
"v_mfma_f32_16x16x32_bf16 a[36:39], v[124:127], v[60:63], a[36:39]\n" \
"buffer_load_dwordx4 v27, s[72:75], s84 offen lds\n" \
"s_mov_b32 s73, s58\n" \
"s_add_u32 s74, s58, 0x4000\n" \
"s_add_u32 s75, s58, 0x8000\n" \
"v_mfma_f32_16x16x32_bf16 a[36:39], v[128:131], v[64:67], a[36:39]\n" \
"s_mov_b32 s70, 0x480000\n" \
"v_add_u32_e64 v22, s51, v28\n" \
"v_mfma_f32_16x16x32_bf16 a[68:71], v[128:131], v[72:75], a[68:71]\n" \
"v_bfe_u32 v44, v20, 2, 5\n" \
"s_mov_b32 s72, 0x3eae1a93\n" \
"v_mfma_f32_16x16x32_bf16 a[68:71], v[124:127], v[68:71], a[68:71]\n" \
"buffer_load_dword v44, v44, s[80:83], s42 offen offset:64 sc1\n" \
"v_mfma_f32_16x16x32_bf16 a[100:103], v[124:127], v[76:79], a[100:103]\n" \
"v_mfma_f32_16x16x32_bf16 a[100:103], v[128:131], v[80:83], a[100:103]\n" \
"s_mov_b32 m0, %[mS]\n" \
"s_mov_b32 s86, %[wbl]\n" \
"v_mfma_f32_16x16x32_bf16 a[8:11], v[136:139], v[56:59], a[8:11]\n" \
"s_mov_b32 s87, %[wbh]\n" \
"v_mfma_f32_16x16x32_bf16 a[8:11], v[132:135], v[52:55], a[8:11]\n" \
"v_mfma_f32_16x16x32_bf16 a[40:43], v[132:135], v[60:63], a[40:43]\n" \
"v_mfma_f32_16x16x32_bf16 a[40:43], v[136:139], v[64:67], a[40:43]\n" \
"v_mfma_f32_16x16x32_bf16 a[72:75], v[136:139], v[72:75], a[72:75]\n" \
"v_mfma_f32_16x16x32_bf16 a[72:75], v[132:135], v[68:71], a[72:75]\n" \
"s_mov_b32 exec_lo, 0xfffc0000\n" \
"s_mov_b32 exec_hi, 0xf\n" \
"global_load_lds_dword v16, s[86:87]\n" \
"s_mov_b64 exec, -1\n" \
"v_mfma_f32_16x16x32_bf16 a[104:107], v[132:135], v[76:79], a[104:107]\n" \
"s_mov_b32 m0, %[mS]\n" \
"s_mov_b32 s86, %[wol]\n" \
"v_mfma_f32_16x16x32_bf16 a[104:107], v[136:139], v[80:83], a[104:107]\n" \
"s_mov_b32 s87, %[woh]\n" \
"v_mfma_f32_16x16x32_bf16 a[12:15], v[144:147], v[56:59], a[12:15]\n" \
"v_mfma_f32_16x16x32_bf16 a[12:15], v[140:143], v[52:55], a[12:15]\n" \
"v_mfma_f32_16x16x32_bf16 a[44:47], v[140:143], v[60:63], a[44:47]\n" \
"s_mov_b32 exec_lo, 0x0\n" \
"s_mov_b32 exec_hi, 0x3f0\n" \
"global_load_lds_dword v16, s[86:87]\n" \
"s_mov_b64 exec, -1\n" \
"v_mfma_f32_16x16x32_bf16 a[44:47], v[144:147], v[64:67], a[44:47]\n" \
"v_mfma_f32_16x16x32_bf16 a[76:79], v[144:147], v[72:75], a[76:79]\n" \
"v_mfma_f32_16x16x32_bf16 a[76:79], v[140:143], v[68:71], a[76:79]\n" \
"v_mfma_f32_16x16x32_bf16 a[108:111], v[140:143], v[76:79], a[108:111]\n" \
"v_mfma_f32_16x16x32_bf16 a[108:111], v[144:147], v[80:83], a[108:111]\n" \
"v_mfma_f32_16x16x32_bf16 a[16:19], v[56:59], v[152:155], a[16:19]\n" \
"v_mfma_f32_16x16x32_bf16 a[16:19], v[52:55], v[148:151], a[16:19]\n" \
"v_mfma_f32_16x16x32_bf16 a[48:51], v[60:63], v[148:151], a[48:51]\n" \
"v_mfma_f32_16x16x32_bf16 a[48:51], v[64:67], v[152:155], a[48:51]\n" \
"v_mfma_f32_16x16x32_bf16 a[80:83], v[72:75], v[152:155], a[80:83]\n" \
"v_mfma_f32_16x16x32_bf16 a[80:83], v[68:71], v[148:151], a[80:83]\n" \
"v_mfma_f32_16x16x32_bf16 a[112:115], v[76:79], v[148:151], a[112:115]\n" \
"v_mfma_f32_16x16x32_bf16 a[112:115], v[80:83], v[152:155], a[112:115]\n" \
"v_mfma_f32_16x16x32_bf16 a[20:23], v[56:59], v[160:163], a[20:23]\n" \
"v_mfma_f32_16x16x32_bf16 a[20:23], v[52:55], v[156:159], a[20:23]\n" \
"v_mfma_f32_16x16x32_bf16 a[52:55], v[60:63], v[156:159], a[52:55]\n" \
"v_mfma_f32_16x16x32_bf16 a[52:55], v[64:67], v[160:163], a[52:55]\n" \
"v_mfma_f32_16x16x32_bf16 a[84:87], v[72:75], v[160:163], a[84:87]\n" \
"v_mfma_f32_16x16x32_bf16 a[84:87], v[68:71], v[156:159], a[84:87]\n" \
"v_mfma_f32_16x16x32_bf16 a[116:119], v[76:79], v[156:159], a[116:119]\n" \
"v_mfma_f32_16x16x32_bf16 a[116:119], v[80:83], v[160:163], a[116:119]\n" \
"s_waitcnt lgkmcnt(0)\n" \
"v_mfma_f32_16x16x32_bf16 a[24:27], v[56:59], v[168:171], a[24:27]\n" \
"v_mfma_f32_16x16x32_bf16 a[24:27], v[52:55], v[164:167], a[24:27]\n" \
"v_mfma_f32_16x16x32_bf16 a[56:59], v[60:63], v[164:167], a[56:59]\n" \
"v_mfma_f32_16x16x32_bf16 a[56:59], v[64:67], v[168:171], a[56:59]\n" \
"v_mfma_f32_16x16x32_bf16 a[88:91], v[72:75], v[168:171], a[88:91]\n" \
"v_mfma_f32_16x16x32_bf16 a[88:91], v[68:71], v[164:167], a[88:91]\n" \
"v_mfma_f32_16x16x32_bf16 a[120:123], v[76:79], v[164:167], a[120:123]\n" \
"v_mfma_f32_16x16x32_bf16 a[120:123], v[80:83], v[168:171], a[120:123]\n" \
"v_mfma_f32_16x16x32_bf16 a[28:31], v[56:59], v[176:179], a[28:31]\n" \
"s_waitcnt vmcnt(2)\n" \
"v_cmp_ne_u32_e32 vcc, %[tag], v44\n" \
"v_mfma_f32_16x16x32_bf16 a[28:31], v[52:55], v[172:175], a[28:31]\n" \
"v_mfma_f32_16x16x32_bf16 a[60:63], v[60:63], v[172:175], a[60:63]\n" \
"v_mfma_f32_16x16x32_bf16 a[60:63], v[64:67], v[176:179], a[60:63]\n" \
"v_mfma_f32_16x16x32_bf16 a[92:95], v[72:75], v[176:179], a[92:95]\n" \
"v_mfma_f32_16x16x32_bf16 a[92:95], v[68:71], v[172:175], a[92:95]\n" \
"s_cbranch_vccnz L_pl_%=\n" \
"L_rl_%=:\n" \
"v_mfma_f32_16x16x32_bf16 a[124:127], v[76:79], v[172:175], a[124:127]\n" \
"v_mfma_f32_16x16x32_bf16 a[124:127], v[80:83], v[176:179], a[124:127]\n" \
"s_barrier\n" \
"v_mfma_f32_16x16x32_bf16 a[144:147], v[88:91], v[152:155], a[144:147]\n" \
"s_add_u32 m0, s43, 0x0\n" \
"s_add_u32 s85, s38, 0x0\n" \
"s_nop 0\n" \
"v_mfma_f32_16x16x32_bf16 a[144:147], v[84:87], v[148:151], a[144:147]\n" \
"buffer_load_dwordx4 v20, s[80:83], s85 offen sc1 lds\n" \
"v_mfma_f32_16x16x32_bf16 a[176:179], v[92:95], v[148:151], a[176:179]\n" \
"ds_read_b128 v[56:59], v29 offset:49152\n" \
"ds_read_b128 v[52:55], v28 offset:49152\n" \
"v_mfma_f32_16x16x32_bf16 a[176:179], v[96:99], v[152:155], a[176:179]\n" \
"s_add_u32 m0, s43, 0x1000\n" \
"s_add_u32 s84, s38, 0x1000\n" \
"v_mfma_f32_16x16x32_bf16 a[208:211], v[104:107], v[152:155], a[208:211]\n" \
"buffer_load_dwordx4 v20, s[80:83], s84 offen sc1 lds\n" \
"v_mfma_f32_16x16x32_bf16 a[208:211], v[100:103], v[148:151], a[208:211]\n" \
"ds_read_b128 v[60:63], v28 offset:51200\n" \
"ds_read_b128 v[64:67], v29 offset:51200\n" \
"v_mfma_f32_16x16x32_bf16 a[240:243], v[108:111], v[148:151], a[240:243]\n" \
"ds_read_b64_tr_b16 v[148:149], v32 offset:49152\n" \
"ds_read_b64_tr_b16 v[150:151], v32 offset:50176\n" \
"v_mfma_f32_16x16x32_bf16 a[240:243], v[112:115], v[152:155], a[240:243]\n" \
"s_add_u32 m0, s43, 0x3000\n" \
"s_mov_b32 s85, 0x0\n" \
"v_mfma_f32_16x16x32_bf16 a[148:151], v[88:91], v[160:163], a[148:151]\n" \
"buffer_load_dwordx4 v21, s[76:79], s85 offen lds\n" \
"v_mfma_f32_16x16x32_bf16 a[148:151], v[84:87], v[156:159], a[148:151]\n" \
"ds_read_b64_tr_b16 v[152:153], v32 offset:57344\n" \
"ds_read_b64_tr_b16 v[154:155], v32 offset:58368\n" \
"v_mfma_f32_16x16x32_bf16 a[180:183], v[92:95], v[156:159], a[180:183]\n" \
"ds_read_b128 v[72:75], v29 offset:53248\n" \
"ds_read_b128 v[68:71], v28 offset:53248\n" \
"v_mfma_f32_16x16x32_bf16 a[180:183], v[96:99], v[160:163], a[180:183]\n" \
"s_add_u32 m0, %[mA], 0x0\n" \
"s_mov_b32 s84, 0x0\n" \
"s_nop 0\n" \
"v_mfma_f32_16x16x32_bf16 a[212:215], v[104:107], v[160:163], a[212:215]\n" \
"buffer_load_dwordx4 v24, s[76:79], s84 offen lds\n" \
"v_mfma_f32_16x16x32_bf16 a[212:215], v[100:103], v[156:159], a[212:215]\n" \
"ds_read_b128 v[76:79], v28 offset:55296\n" \
"ds_read_b128 v[80:83], v29 offset:55296\n" \
"v_mfma_f32_16x16x32_bf16 a[244:247], v[108:111], v[156:159], a[244:247]\n" \
"ds_read_b64_tr_b16 v[156:157], v33 offset:49152\n" \
"ds_read_b64_tr_b16 v[158:159], v33 offset:50176\n" \
"v_mfma_f32_16x16x32_bf16 a[244:247], v[112:115], v[160:163], a[244:247]\n" \
"s_add_u32 m0, %[mA], 0x400\n" \
"s_mov_b32 s85, 0x2400\n" \
"v_mfma_f32_16x16x32_bf16 a[152:155], v[88:91], v[168:171], a[152:155]\n" \
"buffer_load_dwordx4 v25, s[76:79], s85 offen lds\n" \
"v_mfma_f32_16x16x32_bf16 a[152:155], v[84:87], v[164:167], a[152:155]\n" \
"ds_read_b64_tr_b16 v[160:161], v33 offset:57344\n" \
"ds_read_b64_tr_b16 v[162:163], v33 offset:58368\n" \
"v_mfma_f32_16x16x32_bf16 a[184:187], v[92:95], v[164:167], a[184:187]\n" \
"v_mfma_f32_16x16x32_bf16 a[184:187], v[96:99], v[168:171], a[184:187]\n" \
"s_add_u32 m0, %[mB], 0x0\n" \
"s_mov_b32 s84, 0x0\n" \
"v_mfma_f32_16x16x32_bf16 a[216:219], v[104:107], v[168:171], a[216:219]\n" \
"buffer_load_dwordx4 v26, s[68:71], s84 offen lds\n" \
"v_mfma_f32_16x16x32_bf16 a[216:219], v[100:103], v[164:167], a[216:219]\n" \
"v_mfma_f32_16x16x32_bf16 a[248:251], v[108:111], v[164:167], a[248:251]\n" \
"ds_read_b64_tr_b16 v[164:165], v34 offset:49152\n" \
"ds_read_b64_tr_b16 v[166:167], v34 offset:50176\n" \
"v_mfma_f32_16x16x32_bf16 a[248:251], v[112:115], v[168:171], a[248:251]\n" \
"s_add_u32 m0, %[mA], 0x800\n" \
"s_mov_b32 s85, 0x4800\n" \
"v_mfma_f32_16x16x32_bf16 a[156:159], v[88:91], v[176:179], a[156:159]\n" \
"buffer_load_dwordx4 v24, s[76:79], s85 offen lds\n" \
"v_mfma_f32_16x16x32_bf16 a[156:159], v[84:87], v[172:175], a[156:159]\n" \
"ds_read_b64_tr_b16 v[168:169], v34 offset:57344\n" \
"ds_read_b64_tr_b16 v[170:171], v34 offset:58368\n" \
"v_mfma_f32_16x16x32_bf16 a[188:191], v[92:95], v[172:175], a[188:191]\n" \
"v_mfma_f32_16x16x32_bf16 a[188:191], v[96:99], v[176:179], a[188:191]\n" \
"s_add_u32 m0, %[mA], 0xc00\n" \
"s_mov_b32 s84, 0x6c00\n" \
"v_mfma_f32_16x16x32_bf16 a[220:223], v[104:107], v[176:179], a[220:223]\n" \
"buffer_load_dwordx4 v25, s[76:79], s84 offen lds\n" \
"v_mfma_f32_16x16x32_bf16 a[220:223], v[100:103], v[172:175], a[220:223]\n" \
"v_mfma_f32_16x16x32_bf16 a[252:255], v[108:111], v[172:175], a[252:255]\n" \
"ds_read_b64_tr_b16 v[172:173], v35 offset:49152\n" \
"ds_read_b64_tr_b16 v[174:175], v35 offset:50176\n" \
"v_mfma_f32_16x16x32_bf16 a[252:255], v[112:115], v[176:179], a[252:255]\n" \
"s_add_u32 m0, %[mB], 0x400\n" \
"s_mov_b32 s85, 0x8000\n" \
"v_mfma_f32_16x16x32_bf16 a[128:131], v[120:123], v[88:91], a[128:131]\n" \
"buffer_load_dwordx4 v26, s[68:71], s85 offen lds\n" \
"v_mfma_f32_16x16x32_bf16 a[128:131], v[116:119], v[84:87], a[128:131]\n" \
"ds_read_b64_tr_b16 v[176:177], v35 offset:57344\n" \
"ds_read_b64_tr_b16 v[178:179], v35 offset:58368\n" \
"v_mfma_f32_16x16x32_bf16 a[160:163], v[116:119], v[92:95], a[160:163]\n" \
"v_mfma_f32_16x16x32_bf16 a[160:163], v[120:123], v[96:99], a[160:163]\n" \
"s_add_u32 m0, %[mA], 0x1000\n" \
"s_mov_b32 s84, 0x9000\n" \
"v_mfma_f32_16x16x32_bf16 a[192:195], v[120:123], v[104:107], a[192:195]\n" \
"buffer_load_dwordx4 v24, s[76:79], s84 offen lds\n" \
"v_mfma_f32_16x16x32_bf16 a[192:195], v[116:119], v[100:103], a[192:195]\n" \
"v_mfma_f32_16x16x32_bf16 a[224:227], v[116:119], v[108:111], a[224:227]\n" \
"ds_read_b64_tr_b16 v[116:117], v32 offset:32768\n" \
"ds_read_b64_tr_b16 v[118:119], v32 offset:33792\n" \
"v_mfma_f32_16x16x32_bf16 a[224:227], v[120:123], v[112:115], a[224:227]\n" \
"s_add_u32 m0, %[mA], 0x1400\n" \
"s_mov_b32 s85, 0xb400\n" \
"v_mfma_f32_16x16x32_bf16 a[132:135], v[128:131], v[88:91], a[132:135]\n" \
"buffer_load_dwordx4 v25, s[76:79], s85 offen lds\n" \
"v_mfma_f32_16x16x32_bf16 a[132:135], v[124:127], v[84:87], a[132:135]\n" \
"ds_read_b64_tr_b16 v[120:121], v32 offset:40960\n" \
"ds_read_b64_tr_b16 v[122:123], v32 offset:41984\n" \
"v_mfma_f32_16x16x32_bf16 a[164:167], v[124:127], v[92:95], a[164:167]\n" \
"v_mfma_f32_16x16x32_bf16 a[164:167], v[128:131], v[96:99], a[164:167]\n" \
"s_add_u32 m0, %[mB], 0x800\n" \
"s_mov_b32 s84, 0x10000\n" \
"v_mfma_f32_16x16x32_bf16 a[196:199], v[128:131], v[104:107], a[196:199]\n" \
"buffer_load_dwordx4 v27, s[68:71], s84 offen lds\n" \
"v_mfma_f32_16x16x32_bf16 a[196:199], v[124:127], v[100:103], a[196:199]\n" \
"v_mfma_f32_16x16x32_bf16 a[228:231], v[124:127], v[108:111], a[228:231]\n" \
"ds_read_b64_tr_b16 v[124:125], v33 offset:32768\n" \
"ds_read_b64_tr_b16 v[126:127], v33 offset:33792\n" \
"v_mfma_f32_16x16x32_bf16 a[228:231], v[128:131], v[112:115], a[228:231]\n" \
"ds_read_b64_tr_b16 v[128:129], v33 offset:40960\n" \
"ds_read_b64_tr_b16 v[130:131], v33 offset:41984\n" \
"v_mfma_f32_16x16x32_bf16 a[136:139], v[136:139], v[88:91], a[136:139]\n" \
"v_mfma_f32_16x16x32_bf16 a[136:139], v[132:135], v[84:87], a[136:139]\n" \
"v_mfma_f32_16x16x32_bf16 a[168:171], v[132:135], v[92:95], a[168:171]\n" \
"v_mfma_f32_16x16x32_bf16 a[168:171], v[136:139], v[96:99], a[168:171]\n" \
"v_mfma_f32_16x16x32_bf16 a[200:203], v[136:139], v[104:107], a[200:203]\n" \
"v_mfma_f32_16x16x32_bf16 a[200:203], v[132:135], v[100:103], a[200:203]\n" \
"v_mfma_f32_16x16x32_bf16 a[232:235], v[132:135], v[108:111], a[232:235]\n" \
"ds_read_b64_tr_b16 v[132:133], v34 offset:32768\n" \
"ds_read_b64_tr_b16 v[134:135], v34 offset:33792\n" \
"v_mfma_f32_16x16x32_bf16 a[232:235], v[136:139], v[112:115], a[232:235]\n" \
"ds_read_b64_tr_b16 v[136:137], v34 offset:40960\n" \
"ds_read_b64_tr_b16 v[138:139], v34 offset:41984\n" \
"v_mfma_f32_16x16x32_bf16 a[140:143], v[144:147], v[88:91], a[140:143]\n" \
"ds_read_b128 v[88:91], v29 offset:32768\n" \
"v_mfma_f32_16x16x32_bf16 a[140:143], v[140:143], v[84:87], a[140:143]\n" \
"ds_read_b128 v[84:87], v28 offset:32768\n" \
"v_mfma_f32_16x16x32_bf16 a[172:175], v[140:143], v[92:95], a[172:175]\n" \
"ds_read_b128 v[92:95], v28 offset:34816\n" \
"v_mfma_f32_16x16x32_bf16 a[172:175], v[144:147], v[96:99], a[172:175]\n" \
"ds_read_b128 v[96:99], v29 offset:34816\n" \
"v_mfma_f32_16x16x32_bf16 a[204:207], v[144:147], v[104:107], a[204:207]\n" \
"ds_read_b128 v[104:107], v29 offset:36864\n" \
"v_mfma_f32_16x16x32_bf16 a[204:207], v[140:143], v[100:103], a[204:207]\n" \
"ds_read_b128 v[100:103], v28 offset:36864\n" \
"v_mfma_f32_16x16x32_bf16 a[236:239], v[140:143], v[108:111], a[236:239]\n" \
"ds_read_b64_tr_b16 v[140:141], v35 offset:32768\n" \
"ds_read_b64_tr_b16 v[142:143], v35 offset:33792\n" \
"v_mfma_f32_16x16x32_bf16 a[236:239], v[144:147], v[112:115], a[236:239]\n" \
"s_add_u32 m0, %[mA], 0x1800\n" \
"s_waitcnt lgkmcnt(12)\n" \
"s_nop 0\n" \
"v_mfma_f32_16x16x32_bf16 a[16:19], v[56:59], v[152:155], a[16:19]\n" \
"s_mov_b32 s84, 0xd800\n" \
"ds_read_b64_tr_b16 v[144:145], v35 offset:40960\n" \
"v_mfma_f32_16x16x32_bf16 a[16:19], v[52:55], v[148:151], a[16:19]\n" \
"buffer_load_dwordx4 v24, s[76:79], s84 offen lds\n" \
"v_mfma_f32_16x16x32_bf16 a[48:51], v[60:63], v[148:151], a[48:51]\n" \
"s_add_u32 m0, %[mA], 0x1c00\n" \
"s_mov_b32 s85, 0xfc00\n" \
"v_mfma_f32_16x16x32_bf16 a[48:51], v[64:67], v[152:155], a[48:51]\n" \
"ds_read_b64_tr_b16 v[146:147], v35 offset:41984\n" \
"ds_read_b128 v[108:111], v28 offset:38912\n" \
"v_mfma_f32_16x16x32_bf16 a[80:83], v[72:75], v[152:155], a[80:83]\n" \
"buffer_load_dwordx4 v25, s[76:79], s85 offen lds\n" \
"v_mfma_f32_16x16x32_bf16 a[80:83], v[68:71], v[148:151], a[80:83]\n" \
"s_add_u32 m0, %[mB], 0xc00\n" \
"s_mov_b32 s84, 0x18000\n" \
"v_mfma_f32_16x16x32_bf16 a[112:115], v[76:79], v[148:151], a[112:115]\n" \
"ds_read_b128 v[112:115], v29 offset:38912\n" \
"v_add_u32_e64 v28, s49, v28\n" \
"v_mfma_f32_16x16x32_bf16 a[112:115], v[80:83], v[152:155], a[112:115]\n" \
"buffer_load_dwordx4 v27, s[68:71], s84 offen lds\n" \
"v_mfma_f32_16x16x32_bf16 a[20:23], v[56:59], v[160:163], a[20:23]\n" \
"v_add_u32_e64 v29, s49, v29\n" \
"v_accvgpr_read_b32 v184, a16\n" \
"v_mfma_f32_16x16x32_bf16 a[20:23], v[52:55], v[156:159], a[20:23]\n" \
"v_accvgpr_read_b32 v185, a17\n" \
"v_accvgpr_read_b32 v186, a18\n" \
"v_mfma_f32_16x16x32_bf16 a[52:55], v[60:63], v[156:159], a[52:55]\n" \
"v_accvgpr_read_b32 v187, a19\n" \
"v_accvgpr_read_b32 v180, a48\n" \
"v_mfma_f32_16x16x32_bf16 a[52:55], v[64:67], v[160:163], a[52:55]\n" \
"v_accvgpr_read_b32 v181, a49\n" \
"v_accvgpr_read_b32 v182, a50\n" \
"v_mfma_f32_16x16x32_bf16 a[84:87], v[72:75], v[160:163], a[84:87]\n" \
"v_accvgpr_read_b32 v183, a51\n" \
"v_accvgpr_read_b32 v188, a80\n" \
"v_mfma_f32_16x16x32_bf16 a[84:87], v[68:71], v[156:159], a[84:87]\n" \
"v_accvgpr_read_b32 v189, a81\n" \
"v_accvgpr_read_b32 v190, a82\n" \
"v_mfma_f32_16x16x32_bf16 a[116:119], v[76:79], v[156:159], a[116:119]\n" \
"v_accvgpr_read_b32 v191, a83\n" \
"v_cvt_pk_bf16_f32 v184, v184, v185\n" \
"v_mfma_f32_16x16x32_bf16 a[116:119], v[80:83], v[160:163], a[116:119]\n" \
"v_cvt_pk_bf16_f32 v185, v186, v187\n" \
"v_cvt_pk_bf16_f32 v186, v180, v181\n" \
"v_mfma_f32_16x16x32_bf16 a[24:27], v[56:59], v[168:171], a[24:27]\n" \
"v_cvt_pk_bf16_f32 v187, v182, v183\n" \
"v_accvgpr_read_b32 v180, a112\n" \
"v_mfma_f32_16x16x32_bf16 a[24:27], v[52:55], v[164:167], a[24:27]\n" \
"v_accvgpr_read_b32 v181, a113\n" \
"v_accvgpr_read_b32 v182, a114\n" \
"v_mfma_f32_16x16x32_bf16 a[56:59], v[60:63], v[164:167], a[56:59]\n" \
"v_accvgpr_read_b32 v183, a115\n" \
"v_accvgpr_read_b32 v192, a20\n" \
"v_mfma_f32_16x16x32_bf16 a[56:59], v[64:67], v[168:171], a[56:59]\n" \
"v_accvgpr_read_b32 v193, a21\n" \
"v_accvgpr_read_b32 v194, a22\n" \
"v_mfma_f32_16x16x32_bf16 a[88:91], v[72:75], v[168:171], a[88:91]\n" \
"v_accvgpr_read_b32 v195, a23\n" \
"v_cvt_pk_bf16_f32 v188, v188, v189\n" \
"v_mfma_f32_16x16x32_bf16 a[88:91], v[68:71], v[164:167], a[88:91]\n" \
"v_cvt_pk_bf16_f32 v189, v190, v191\n" \
"v_cvt_pk_bf16_f32 v190, v180, v181\n" \
"v_mfma_f32_16x16x32_bf16 a[120:123], v[76:79], v[164:167], a[120:123]\n" \
"v_cvt_pk_bf16_f32 v191, v182, v183\n" \
"v_accvgpr_read_b32 v180, a52\n" \
"v_mfma_f32_16x16x32_bf16 a[120:123], v[80:83], v[168:171], a[120:123]\n" \
"v_accvgpr_read_b32 v181, a53\n" \
"v_accvgpr_read_b32 v182, a54\n" \
"v_mfma_f32_16x16x32_bf16 a[28:31], v[56:59], v[176:179], a[28:31]\n" \
"v_accvgpr_read_b32 v183, a55\n" \
"v_accvgpr_read_b32 v196, a84\n" \
"v_mfma_f32_16x16x32_bf16 a[28:31], v[52:55], v[172:175], a[28:31]\n" \
"v_accvgpr_read_b32 v197, a85\n" \
"v_accvgpr_read_b32 v198, a86\n" \
"v_mfma_f32_16x16x32_bf16 a[60:63], v[60:63], v[172:175], a[60:63]\n" \
"v_accvgpr_read_b32 v199, a87\n" \
"v_cvt_pk_bf16_f32 v192, v192, v193\n" \
"v_mfma_f32_16x16x32_bf16 a[60:63], v[64:67], v[176:179], a[60:63]\n" \
"v_cvt_pk_bf16_f32 v193, v194, v195\n" \
"v_cvt_pk_bf16_f32 v194, v180, v181\n" \
"v_mfma_f32_16x16x32_bf16 a[92:95], v[72:75], v[176:179], a[92:95]\n" \
"v_cvt_pk_bf16_f32 v195, v182, v183\n" \
"v_accvgpr_read_b32 v180, a116\n" \
"v_mfma_f32_16x16x32_bf16 a[92:95], v[68:71], v[172:175], a[92:95]\n" \
"v_accvgpr_read_b32 v181, a117\n" \
"v_accvgpr_read_b32 v182, a118\n" \
"v_mfma_f32_16x16x32_bf16 a[124:127], v[76:79], v[172:175], a[124:127]\n" \
"v_accvgpr_read_b32 v183, a119\n" \
"v_accvgpr_read_b32 v200, a24\n" \
"v_mfma_f32_16x16x32_bf16 a[124:127], v[80:83], v[176:179], a[124:127]\n" \
"v_accvgpr_read_b32 v201, a25\n" \
"v_accvgpr_read_b32 v202, a26\n" \
"v_mfma_f32_16x16x32_bf16 a[0:3], v[120:123], v[56:59], a[0:3]\n" \
"v_accvgpr_read_b32 v203, a27\n" \
"v_cvt_pk_bf16_f32 v196, v196, v197\n" \
"v_mfma_f32_16x16x32_bf16 a[0:3], v[116:119], v[52:55], a[0:3]\n" \
"v_cvt_pk_bf16_f32 v197, v198, v199\n" \
"v_cvt_pk_bf16_f32 v198, v180, v181\n" \
"v_mfma_f32_16x16x32_bf16 a[32:35], v[116:119], v[60:63], a[32:35]\n" \
"v_cvt_pk_bf16_f32 v199, v182, v183\n" \
"v_accvgpr_read_b32 v180, a56\n" \
"v_mfma_f32_16x16x32_bf16 a[32:35], v[120:123], v[64:67], a[32:35]\n" \
"v_accvgpr_read_b32 v181, a57\n" \
"v_accvgpr_read_b32 v182, a58\n" \
"v_mfma_f32_16x16x32_bf16 a[64:67], v[120:123], v[72:75], a[64:67]\n" \
"v_accvgpr_read_b32 v183, a59\n" \
"v_accvgpr_read_b32 v204, a88\n" \
"v_mfma_f32_16x16x32_bf16 a[64:67], v[116:119], v[68:71], a[64:67]\n" \
"v_accvgpr_read_b32 v205, a89\n" \
"v_accvgpr_read_b32 v206, a90\n" \
"v_mfma_f32_16x16x32_bf16 a[96:99], v[116:119], v[76:79], a[96:99]\n" \
"v_accvgpr_read_b32 v207, a91\n" \
"v_cvt_pk_bf16_f32 v200, v200, v201\n" \
"v_mfma_f32_16x16x32_bf16 a[96:99], v[120:123], v[80:83], a[96:99]\n" \
"v_cvt_pk_bf16_f32 v201, v202, v203\n" \
"v_cvt_pk_bf16_f32 v202, v180, v181\n" \
"v_mfma_f32_16x16x32_bf16 a[4:7], v[128:131], v[56:59], a[4:7]\n" \
"v_cvt_pk_bf16_f32 v203, v182, v183\n" \
"v_accvgpr_read_b32 v180, a120\n" \
"v_mfma_f32_16x16x32_bf16 a[4:7], v[124:127], v[52:55], a[4:7]\n" \
"v_accvgpr_read_b32 v181, a121\n" \
"v_accvgpr_read_b32 v182, a122\n" \
"v_mfma_f32_16x16x32_bf16 a[36:39], v[124:127], v[60:63], a[36:39]\n" \
"v_accvgpr_read_b32 v183, a123\n" \
"v_accvgpr_read_b32 v208, a28\n" \
"v_mfma_f32_16x16x32_bf16 a[36:39], v[128:131], v[64:67], a[36:39]\n" \
"v_accvgpr_read_b32 v209, a29\n" \
"v_accvgpr_read_b32 v210, a30\n" \
"v_mfma_f32_16x16x32_bf16 a[68:71], v[128:131], v[72:75], a[68:71]\n" \
"v_accvgpr_read_b32 v211, a31\n" \
"v_cvt_pk_bf16_f32 v204, v204, v205\n" \
"v_mfma_f32_16x16x32_bf16 a[68:71], v[124:127], v[68:71], a[68:71]\n" \
"v_cvt_pk_bf16_f32 v205, v206, v207\n" \
"v_cvt_pk_bf16_f32 v206, v180, v181\n" \
"v_mfma_f32_16x16x32_bf16 a[100:103], v[124:127], v[76:79], a[100:103]\n" \
"v_cvt_pk_bf16_f32 v207, v182, v183\n" \
"v_accvgpr_read_b32 v180, a60\n" \
"v_mfma_f32_16x16x32_bf16 a[100:103], v[128:131], v[80:83], a[100:103]\n" \
"v_accvgpr_read_b32 v181, a61\n" \
"s_waitcnt lgkmcnt(0)\n" \
"s_nop 0\n" \
"v_mfma_f32_16x16x32_bf16 a[8:11], v[136:139], v[56:59], a[8:11]\n" \
"v_accvgpr_read_b32 v182, a62\n" \
"v_accvgpr_read_b32 v183, a63\n" \
"v_mfma_f32_16x16x32_bf16 a[8:11], v[132:135], v[52:55], a[8:11]\n" \
"v_accvgpr_read_b32 v212, a92\n" \
"v_accvgpr_read_b32 v213, a93\n" \
"v_mfma_f32_16x16x32_bf16 a[40:43], v[132:135], v[60:63], a[40:43]\n" \
"v_accvgpr_read_b32 v214, a94\n" \
"v_accvgpr_read_b32 v215, a95\n" \
"v_mfma_f32_16x16x32_bf16 a[40:43], v[136:139], v[64:67], a[40:43]\n" \
"v_cvt_pk_bf16_f32 v208, v208, v209\n" \
"v_cvt_pk_bf16_f32 v209, v210, v211\n" \
"v_mfma_f32_16x16x32_bf16 a[72:75], v[136:139], v[72:75], a[72:75]\n" \
"v_cvt_pk_bf16_f32 v210, v180, v181\n" \
"v_cvt_pk_bf16_f32 v211, v182, v183\n" \
"v_mfma_f32_16x16x32_bf16 a[72:75], v[132:135], v[68:71], a[72:75]\n" \
"v_accvgpr_read_b32 v180, a124\n" \
"v_accvgpr_read_b32 v181, a125\n" \
"v_mfma_f32_16x16x32_bf16 a[104:107], v[132:135], v[76:79], a[104:107]\n" \
"v_accvgpr_read_b32 v182, a126\n" \
"v_accvgpr_read_b32 v183, a127\n" \
"v_mfma_f32_16x16x32_bf16 a[104:107], v[136:139], v[80:83], a[104:107]\n" \
"v_accvgpr_read_b32 v216, a0\n" \
"v_accvgpr_read_b32 v217, a1\n" \
"v_mfma_f32_16x16x32_bf16 a[12:15], v[144:147], v[56:59], a[12:15]\n" \
"v_accvgpr_read_b32 v218, a2\n" \
"v_accvgpr_read_b32 v219, a3\n" \
"v_mfma_f32_16x16x32_bf16 a[12:15], v[140:143], v[52:55], a[12:15]\n" \
"v_cvt_pk_bf16_f32 v212, v212, v213\n" \
"v_cvt_pk_bf16_f32 v213, v214, v215\n" \
"v_mfma_f32_16x16x32_bf16 a[44:47], v[140:143], v[60:63], a[44:47]\n" \
"v_cvt_pk_bf16_f32 v214, v180, v181\n" \
"v_cvt_pk_bf16_f32 v215, v182, v183\n" \
"v_mfma_f32_16x16x32_bf16 a[44:47], v[144:147], v[64:67], a[44:47]\n" \
"v_accvgpr_read_b32 v180, a4\n" \
"v_accvgpr_read_b32 v181, a5\n" \
"v_mfma_f32_16x16x32_bf16 a[76:79], v[144:147], v[72:75], a[76:79]\n" \
"v_accvgpr_read_b32 v182, a6\n" \
"v_accvgpr_read_b32 v183, a7\n" \
"v_mfma_f32_16x16x32_bf16 a[76:79], v[140:143], v[68:71], a[76:79]\n" \
"v_cvt_pk_bf16_f32 v216, v216, v217\n" \
"v_cvt_pk_bf16_f32 v217, v218, v219\n" \
"v_mfma_f32_16x16x32_bf16 a[108:111], v[140:143], v[76:79], a[108:111]\n" \
"v_accvgpr_read_b32 v220, a32\n" \
"v_accvgpr_read_b32 v221, a33\n" \
"v_mfma_f32_16x16x32_bf16 a[108:111], v[144:147], v[80:83], a[108:111]\n" \
"v_cvt_pk_bf16_f32 v218, v180, v181\n" \
"v_cvt_pk_bf16_f32 v219, v182, v183\n" \
"s_waitcnt vmcnt(0)\n" \
"s_barrier\n" \
"v_mfma_f32_16x16x32_bf16 a[128:131], v[120:123], v[88:91], a[128:131]\n" \
"s_add_u32 m0, s43, 0x8000\n" \
"s_add_u32 s85, s38, 0x2000\n" \
"v_mfma_f32_16x16x32_bf16 a[128:131], v[116:119], v[84:87], a[128:131]\n" \
"buffer_load_dwordx4 v20, s[80:83], s85 offen sc1 lds\n" \
"v_mfma_f32_16x16x32_bf16 a[160:163], v[116:119], v[92:95], a[160:163]\n" \
"ds_read_b128 v[52:55], v28 offset:0\n" \
"ds_read_b128 v[56:59], v29 offset:0\n" \
"v_mfma_f32_16x16x32_bf16 a[160:163], v[120:123], v[96:99], a[160:163]\n" \
"s_add_u32 m0, s43, 0x9000\n" \
"s_add_u32 s84, s38, 0x3000\n" \
"v_mfma_f32_16x16x32_bf16 a[192:195], v[120:123], v[104:107], a[192:195]\n" \
"buffer_load_dwordx4 v20, s[80:83], s84 offen sc1 lds\n" \
"v_mfma_f32_16x16x32_bf16 a[192:195], v[116:119], v[100:103], a[192:195]\n" \
"ds_read_b128 v[60:63], v28 offset:2048\n" \
"ds_read_b128 v[64:67], v29 offset:2048\n" \
"v_mfma_f32_16x16x32_bf16 a[224:227], v[116:119], v[108:111], a[224:227]\n" \
"ds_read_b64_tr_b16 v[116:117], v36 offset:0\n" \
"ds_read_b64_tr_b16 v[118:119], v36 offset:512\n" \
"v_mfma_f32_16x16x32_bf16 a[224:227], v[120:123], v[112:115], a[224:227]\n" \
"s_add_u32 m0, s43, 0xb000\n" \
"s_mov_b32 s85, 0x80\n" \
"v_mfma_f32_16x16x32_bf16 a[132:135], v[128:131], v[88:91], a[132:135]\n" \
"buffer_load_dwordx4 v21, s[76:79], s85 offen lds\n" \
"v_mfma_f32_16x16x32_bf16 a[132:135], v[124:127], v[84:87], a[132:135]\n" \
"ds_read_b64_tr_b16 v[120:121], v36 offset:4096\n" \
"ds_read_b64_tr_b16 v[122:123], v36 offset:4608\n" \
"v_mfma_f32_16x16x32_bf16 a[164:167], v[124:127], v[92:95], a[164:167]\n" \
"ds_read_b128 v[68:71], v28 offset:4096\n" \
"ds_read_b128 v[72:75], v29 offset:4096\n" \
"v_mfma_f32_16x16x32_bf16 a[164:167], v[128:131], v[96:99], a[164:167]\n" \
"s_add_u32 m0, %[mA], 0x8000\n" \
"s_mov_b32 s84, 0x80\n" \
"v_mfma_f32_16x16x32_bf16 a[196:199], v[128:131], v[104:107], a[196:199]\n" \
"buffer_load_dwordx4 v24, s[76:79], s84 offen lds\n" \
"v_mfma_f32_16x16x32_bf16 a[196:199], v[124:127], v[100:103], a[196:199]\n" \
"ds_read_b128 v[76:79], v28 offset:6144\n" \
"ds_read_b128 v[80:83], v29 offset:6144\n" \
"v_mfma_f32_16x16x32_bf16 a[228:231], v[124:127], v[108:111], a[228:231]\n" \
"ds_read_b64_tr_b16 v[124:125], v37 offset:0\n" \
"ds_read_b64_tr_b16 v[126:127], v37 offset:512\n" \
"v_mfma_f32_16x16x32_bf16 a[228:231], v[128:131], v[112:115], a[228:231]\n" \
"s_add_u32 m0, %[mA], 0x8400\n" \
"s_mov_b32 s85, 0x2480\n" \
"v_mfma_f32_16x16x32_bf16 a[136:139], v[136:139], v[88:91], a[136:139]\n" \
"buffer_load_dwordx4 v25, s[76:79], s85 offen lds\n" \
"v_mfma_f32_16x16x32_bf16 a[136:139], v[132:135], v[84:87], a[136:139]\n" \
"ds_read_b64_tr_b16 v[128:129], v37 offset:4096\n" \
"ds_read_b64_tr_b16 v[130:131], v37 offset:4608\n" \
"L_pf4:\n" \
"s_cbranch_execz L_pf8\n" \
"s_nop 0\n" \
"v_mfma_f32_16x16x32_bf16 a[168:171], v[132:135], v[92:95], a[168:171]\n" \
"v_accvgpr_read_b32 v222, a34\n" \
"v_accvgpr_read_b32 v223, a35\n" \
"v_mfma_f32_16x16x32_bf16 a[168:171], v[136:139], v[96:99], a[168:171]\n" \
"s_add_u32 m0, %[mB], 0x8000\n" \
"s_mov_b32 s84, 0x80000\n" \
"v_mfma_f32_16x16x32_bf16 a[200:203], v[136:139], v[104:107], a[200:203]\n" \
"buffer_load_dwordx4 v26, s[68:71], s84 offen lds\n" \
"v_mfma_f32_16x16x32_bf16 a[200:203], v[132:135], v[100:103], a[200:203]\n" \
"v_cvt_pk_bf16_f32 v220, v220, v221\n" \
"v_accvgpr_read_b32 v180, a36\n" \
"v_mfma_f32_16x16x32_bf16 a[232:235], v[132:135], v[108:111], a[232:235]\n" \
"ds_read_b64_tr_b16 v[132:133], v38 offset:0\n" \
"ds_read_b64_tr_b16 v[134:135], v38 offset:512\n" \
"v_mfma_f32_16x16x32_bf16 a[232:235], v[136:139], v[112:115], a[232:235]\n" \
"s_add_u32 m0, %[mA], 0x8800\n" \
"s_mov_b32 s85, 0x4880\n" \
"v_mfma_f32_16x16x32_bf16 a[140:143], v[144:147], v[88:91], a[140:143]\n" \
"buffer_load_dwordx4 v24, s[76:79], s85 offen lds\n" \
"v_mfma_f32_16x16x32_bf16 a[140:143], v[140:143], v[84:87], a[140:143]\n" \
"ds_read_b64_tr_b16 v[136:137], v38 offset:4096\n" \
"ds_read_b64_tr_b16 v[138:139], v38 offset:4608\n" \
"v_mfma_f32_16x16x32_bf16 a[172:175], v[140:143], v[92:95], a[172:175]\n" \
"v_accvgpr_read_b32 v181, a37\n" \
"v_accvgpr_read_b32 v182, a38\n" \
"v_mfma_f32_16x16x32_bf16 a[172:175], v[144:147], v[96:99], a[172:175]\n" \
"s_add_u32 m0, %[mA], 0x8c00\n" \
"s_mov_b32 s84, 0x6c80\n" \
"v_mfma_f32_16x16x32_bf16 a[204:207], v[144:147], v[104:107], a[204:207]\n" \
"buffer_load_dwordx4 v25, s[76:79], s84 offen lds\n" \
"v_mfma_f32_16x16x32_bf16 a[204:207], v[140:143], v[100:103], a[204:207]\n" \
"v_accvgpr_read_b32 v183, a39\n" \
"v_cvt_pk_bf16_f32 v221, v222, v223\n" \
"v_mfma_f32_16x16x32_bf16 a[236:239], v[140:143], v[108:111], a[236:239]\n" \
"ds_read_b64_tr_b16 v[140:141], v39 offset:0\n" \
"ds_read_b64_tr_b16 v[142:143], v39 offset:512\n" \
"v_mfma_f32_16x16x32_bf16 a[236:239], v[144:147], v[112:115], a[236:239]\n" \
"s_add_u32 m0, %[mB], 0x8400\n" \
"s_mov_b32 s85, 0x88000\n" \
"v_mfma_f32_16x16x32_bf16 a[144:147], v[88:91], v[152:155], a[144:147]\n" \
"buffer_load_dwordx4 v26, s[68:71], s85 offen lds\n" \
"v_mfma_f32_16x16x32_bf16 a[144:147], v[84:87], v[148:151], a[144:147]\n" \
"ds_read_b64_tr_b16 v[144:145], v39 offset:4096\n" \
"ds_read_b64_tr_b16 v[146:147], v39 offset:4608\n" \
"v_mfma_f32_16x16x32_bf16 a[148:151], v[84:87], v[156:159], a[148:151]\n" \
"v_accvgpr_read_b32 v224, a64\n" \
"v_accvgpr_read_b32 v225, a65\n" \
"v_mfma_f32_16x16x32_bf16 a[148:151], v[88:91], v[160:163], a[148:151]\n" \
"s_add_u32 m0, %[mA], 0x9000\n" \
"s_mov_b32 s84, 0x9080\n" \
"v_mfma_f32_16x16x32_bf16 a[152:155], v[88:91], v[168:171], a[152:155]\n" \
"buffer_load_dwordx4 v24, s[76:79], s84 offen lds\n" \
"v_mfma_f32_16x16x32_bf16 a[152:155], v[84:87], v[164:167], a[152:155]\n" \
"v_cvt_pk_bf16_f32 v222, v180, v181\n" \
"v_cvt_pk_bf16_f32 v223, v182, v183\n" \
"v_mfma_f32_16x16x32_bf16 a[156:159], v[84:87], v[172:175], a[156:159]\n" \
"ds_read_b128 v[84:87], v22 offset:0\n" \
"v_accvgpr_read_b32 v226, a66\n" \
"v_mfma_f32_16x16x32_bf16 a[156:159], v[88:91], v[176:179], a[156:159]\n" \
"s_add_u32 m0, %[mA], 0x9400\n" \
"s_mov_b32 s85, 0xb480\n" \
"v_mfma_f32_16x16x32_bf16 a[176:179], v[96:99], v[152:155], a[176:179]\n" \
"buffer_load_dwordx4 v25, s[76:79], s85 offen lds\n" \
"v_mfma_f32_16x16x32_bf16 a[176:179], v[92:95], v[148:151], a[176:179]\n" \
"ds_read_b128 v[88:91], v23 offset:0\n" \
"v_accvgpr_read_b32 v227, a67\n" \
"v_mfma_f32_16x16x32_bf16 a[180:183], v[92:95], v[156:159], a[180:183]\n" \
"v_cvt_pk_bf16_f32 v224, v224, v225\n" \
"v_accvgpr_read_b32 v180, a68\n" \
"v_mfma_f32_16x16x32_bf16 a[180:183], v[96:99], v[160:163], a[180:183]\n" \
"s_add_u32 m0, %[mB], 0x8800\n" \
"s_mov_b32 s84, 0x90000\n" \
"v_mfma_f32_16x16x32_bf16 a[184:187], v[96:99], v[168:171], a[184:187]\n" \
"buffer_load_dwordx4 v27, s[68:71], s84 offen lds\n" \
"v_mfma_f32_16x16x32_bf16 a[184:187], v[92:95], v[164:167], a[184:187]\n" \
"v_accvgpr_read_b32 v181, a69\n" \
"v_accvgpr_read_b32 v182, a70\n" \
"v_mfma_f32_16x16x32_bf16 a[188:191], v[92:95], v[172:175], a[188:191]\n" \
"v_accvgpr_read_b32 v183, a71\n" \
"v_cvt_pk_bf16_f32 v225, v226, v227\n" \
"v_mfma_f32_16x16x32_bf16 a[188:191], v[96:99], v[176:179], a[188:191]\n" \
"v_accvgpr_read_b32 v228, a96\n" \
"v_accvgpr_read_b32 v229, a97\n" \
"v_mfma_f32_16x16x32_bf16 a[208:211], v[104:107], v[152:155], a[208:211]\n" \
"v_cvt_pk_bf16_f32 v226, v180, v181\n" \
"v_cvt_pk_bf16_f32 v227, v182, v183\n" \
"v_mfma_f32_16x16x32_bf16 a[208:211], v[100:103], v[148:151], a[208:211]\n" \
"v_accvgpr_read_b32 v230, a98\n" \
"v_accvgpr_read_b32 v231, a99\n" \
"v_mfma_f32_16x16x32_bf16 a[212:215], v[100:103], v[156:159], a[212:215]\n" \
"v_cvt_pk_bf16_f32 v228, v228, v229\n" \
"v_accvgpr_read_b32 v180, a100\n" \
"v_mfma_f32_16x16x32_bf16 a[212:215], v[104:107], v[160:163], a[212:215]\n" \
"v_accvgpr_read_b32 v181, a101\n" \
"v_accvgpr_read_b32 v182, a102\n" \
"v_mfma_f32_16x16x32_bf16 a[216:219], v[104:107], v[168:171], a[216:219]\n" \
"v_accvgpr_read_b32 v183, a103\n" \
"v_cvt_pk_bf16_f32 v229, v230, v231\n" \
"v_mfma_f32_16x16x32_bf16 a[216:219], v[100:103], v[164:167], a[216:219]\n" \
"v_accvgpr_read_b32 v232, a8\n" \
"v_accvgpr_read_b32 v233, a9\n" \
"v_mfma_f32_16x16x32_bf16 a[220:223], v[100:103], v[172:175], a[220:223]\n" \
"v_cvt_pk_bf16_f32 v230, v180, v181\n" \
"v_cvt_pk_bf16_f32 v231, v182, v183\n" \
"v_mfma_f32_16x16x32_bf16 a[220:223], v[104:107], v[176:179], a[220:223]\n" \
"v_accvgpr_read_b32 v234, a10\n" \
"v_accvgpr_read_b32 v235, a11\n" \
"v_mfma_f32_16x16x32_bf16 a[240:243], v[112:115], v[152:155], a[240:243]\n" \
"ds_read_b64_tr_b16 v[152:153], v32 offset:8192\n" \
"ds_read_b64_tr_b16 v[154:155], v32 offset:9216\n" \
"v_mfma_f32_16x16x32_bf16 a[240:243], v[108:111], v[148:151], a[240:243]\n" \
"ds_read_b64_tr_b16 v[148:149], v32 offset:0\n" \
"ds_read_b64_tr_b16 v[150:151], v32 offset:1024\n" \
"v_mfma_f32_16x16x32_bf16 a[244:247], v[108:111], v[156:159], a[244:247]\n" \
"ds_read_b64_tr_b16 v[156:157], v33 offset:0\n" \
"ds_read_b64_tr_b16 v[158:159], v33 offset:1024\n" \
"v_mfma_f32_16x16x32_bf16 a[244:247], v[112:115], v[160:163], a[244:247]\n" \
"ds_read_b64_tr_b16 v[160:161], v33 offset:8192\n" \
"ds_read_b64_tr_b16 v[162:163], v33 offset:9216\n" \
"v_mfma_f32_16x16x32_bf16 a[248:251], v[112:115], v[168:171], a[248:251]\n" \
"ds_read_b64_tr_b16 v[168:169], v34 offset:8192\n" \
"ds_read_b64_tr_b16 v[170:171], v34 offset:9216\n" \
"v_mfma_f32_16x16x32_bf16 a[248:251], v[108:111], v[164:167], a[248:251]\n" \
"ds_read_b64_tr_b16 v[164:165], v34 offset:0\n" \
"ds_read_b64_tr_b16 v[166:167], v34 offset:1024\n" \
"v_mfma_f32_16x16x32_bf16 a[252:255], v[108:111], v[172:175], a[252:255]\n" \
"ds_read_b64_tr_b16 v[172:173], v35 offset:0\n" \
"ds_read_b64_tr_b16 v[174:175], v35 offset:1024\n" \
"v_mfma_f32_16x16x32_bf16 a[252:255], v[112:115], v[176:179], a[252:255]\n" \
"s_add_u32 m0, %[mA], 0x9800\n" \
"s_waitcnt lgkmcnt(14)\n" \
"s_nop 0\n" \
"v_mfma_f32_16x16x32_bf16 a[16:19], v[144:147], v[88:91], 0\n" \
"s_mov_b32 s84, 0xd880\n" \
"ds_read_b64_tr_b16 v[176:177], v35 offset:8192\n" \
"v_mfma_f32_16x16x32_bf16 a[16:19], v[140:143], v[84:87], a[16:19]\n" \
"buffer_load_dwordx4 v24, s[76:79], s84 offen lds\n" \
"v_mfma_f32_16x16x32_bf16 a[48:51], v[124:127], v[84:87], 0\n" \
"s_add_u32 m0, %[mA], 0x9c00\n" \
"s_mov_b32 s85, 0xfc80\n" \
"v_mfma_f32_16x16x32_bf16 a[48:51], v[128:131], v[88:91], a[48:51]\n" \
"ds_read_b64_tr_b16 v[178:179], v35 offset:9216\n" \
"v_cvt_pk_bf16_f32 v232, v232, v233\n" \
"v_mfma_f32_16x16x32_bf16 a[80:83], v[88:91], v[136:139], 0\n" \
"buffer_load_dwordx4 v25, s[76:79], s85 offen lds\n" \
"v_mfma_f32_16x16x32_bf16 a[80:83], v[84:87], v[132:135], a[80:83]\n" \
"s_add_u32 m0, %[mB], 0x8c00\n" \
"s_mov_b32 s84, 0x98000\n" \
"v_mfma_f32_16x16x32_bf16 a[112:115], v[140:143], v[52:55], 0\n" \
"v_accvgpr_read_b32 v180, a12\n" \
"v_accvgpr_read_b32 v181, a13\n" \
"v_mfma_f32_16x16x32_bf16 a[112:115], v[144:147], v[56:59], a[112:115]\n" \
"buffer_load_dwordx4 v27, s[68:71], s84 offen lds\n" \
"v_mfma_f32_16x16x32_bf16 a[20:23], v[128:131], v[56:59], 0\n" \
"v_accvgpr_read_b32 v182, a14\n" \
"v_accvgpr_read_b32 v183, a15\n" \
"v_mfma_f32_16x16x32_bf16 a[20:23], v[124:127], v[52:55], a[20:23]\n" \
"s_add_u32 s88, s47, 0x0\n" \
"s_nop 0\n" \
"v_cvt_pk_bf16_f32 v233, v234, v235\n" \
"v_mfma_f32_16x16x32_bf16 a[52:55], v[116:119], v[52:55], 0\n" \
"buffer_store_dwordx4 v[184:187], v20, s[80:83], s88 offen offset:0 sc1\n" \
"v_mfma_f32_16x16x32_bf16 a[52:55], v[120:123], v[56:59], a[52:55]\n" \
"v_accvgpr_read_b32 v236, a40\n" \
"v_accvgpr_read_b32 v237, a41\n" \
"v_mfma_f32_16x16x32_bf16 a[84:87], v[56:59], v[136:139], 0\n" \
"v_cvt_pk_bf16_f32 v234, v180, v181\n" \
"v_cvt_pk_bf16_f32 v235, v182, v183\n" \
"v_mfma_f32_16x16x32_bf16 a[84:87], v[52:55], v[132:135], a[84:87]\n" \
"v_accvgpr_read_b32 v238, a42\n" \
"v_accvgpr_read_b32 v239, a43\n" \
"v_mfma_f32_16x16x32_bf16 a[116:119], v[140:143], v[60:63], 0\n" \
"v_cvt_pk_bf16_f32 v236, v236, v237\n" \
"v_accvgpr_read_b32 v180, a44\n" \
"v_mfma_f32_16x16x32_bf16 a[116:119], v[144:147], v[64:67], a[116:119]\n" \
"v_accvgpr_read_b32 v181, a45\n" \
"v_accvgpr_read_b32 v182, a46\n" \
"v_mfma_f32_16x16x32_bf16 a[24:27], v[128:131], v[64:67], 0\n" \
"v_accvgpr_read_b32 v183, a47\n" \
"v_cvt_pk_bf16_f32 v237, v238, v239\n" \
"v_mfma_f32_16x16x32_bf16 a[24:27], v[124:127], v[60:63], a[24:27]\n" \
"v_accvgpr_read_b32 v240, a72\n" \
"v_accvgpr_read_b32 v241, a73\n" \
"v_mfma_f32_16x16x32_bf16 a[56:59], v[116:119], v[60:63], 0\n" \
"v_cvt_pk_bf16_f32 v238, v180, v181\n" \
"v_cvt_pk_bf16_f32 v239, v182, v183\n" \
"v_mfma_f32_16x16x32_bf16 a[56:59], v[120:123], v[64:67], a[56:59]\n" \
"s_add_u32 s89, s47, 0x2000\n" \
"v_accvgpr_read_b32 v242, a74\n" \
"v_mfma_f32_16x16x32_bf16 a[88:91], v[64:67], v[136:139], 0\n" \
"buffer_store_dwordx4 v[188:191], v20, s[80:83], s89 offen offset:0 sc1\n" \
"v_mfma_f32_16x16x32_bf16 a[88:91], v[60:63], v[132:135], a[88:91]\n" \
"v_accvgpr_read_b32 v243, a75\n" \
"v_cvt_pk_bf16_f32 v240, v240, v241\n" \
"v_mfma_f32_16x16x32_bf16 a[120:123], v[140:143], v[68:71], 0\n" \
"v_accvgpr_read_b32 v180, a76\n" \
"v_accvgpr_read_b32 v181, a77\n" \
"v_mfma_f32_16x16x32_bf16 a[120:123], v[144:147], v[72:75], a[120:123]\n" \
"v_accvgpr_read_b32 v182, a78\n" \
"v_accvgpr_read_b32 v183, a79\n" \
"v_mfma_f32_16x16x32_bf16 a[28:31], v[128:131], v[72:75], 0\n" \
"v_cvt_pk_bf16_f32 v241, v242, v243\n" \
"v_accvgpr_read_b32 v244, a104\n" \
"v_mfma_f32_16x16x32_bf16 a[28:31], v[124:127], v[68:71], a[28:31]\n" \
"v_accvgpr_read_b32 v245, a105\n" \
"v_cvt_pk_bf16_f32 v242, v180, v181\n" \
"v_mfma_f32_16x16x32_bf16 a[60:63], v[116:119], v[68:71], 0\n" \
"v_cvt_pk_bf16_f32 v243, v182, v183\n" \
"v_accvgpr_read_b32 v246, a106\n" \
"v_mfma_f32_16x16x32_bf16 a[60:63], v[120:123], v[72:75], a[60:63]\n" \
"v_accvgpr_read_b32 v247, a107\n" \
"v_cvt_pk_bf16_f32 v244, v244, v245\n" \
"v_mfma_f32_16x16x32_bf16 a[92:95], v[72:75], v[136:139], 0\n" \
"v_accvgpr_read_b32 v248, a128\n" \
"v_accvgpr_read_b32 v249, a129\n" \
"v_mfma_f32_16x16x32_bf16 a[92:95], v[68:71], v[132:135], a[92:95]\n" \
"v_accvgpr_read_b32 v250, a130\n" \
"v_accvgpr_read_b32 v251, a131\n" \
"v_mfma_f32_16x16x32_bf16 a[124:127], v[140:143], v[76:79], 0\n" \
"v_accvgpr_read_b32 v180, a108\n" \
"v_accvgpr_read_b32 v181, a109\n" \
"v_mfma_f32_16x16x32_bf16 a[124:127], v[144:147], v[80:83], a[124:127]\n" \
"v_accvgpr_read_b32 v182, a110\n" \
"v_accvgpr_read_b32 v183, a111\n" \
"v_mfma_f32_16x16x32_bf16 a[0:3], v[128:131], v[80:83], 0\n" \
"v_cvt_pk_bf16_f32 v245, v246, v247\n" \
"v_cvt_pk_bf16_f32 v246, v180, v181\n" \
"v_mfma_f32_16x16x32_bf16 a[0:3], v[124:127], v[76:79], a[0:3]\n" \
"v_cvt_pk_bf16_f32 v247, v182, v183\n" \
"v_cvt_pk_bf16_f32 v248, v248, v249\n" \
"v_mfma_f32_16x16x32_bf16 a[4:7], v[116:119], v[76:79], 0\n" \
"v_accvgpr_read_b32 v180, a132\n" \
"v_accvgpr_read_b32 v181, a133\n" \
"v_mfma_f32_16x16x32_bf16 a[4:7], v[120:123], v[80:83], a[4:7]\n" \
"v_accvgpr_read_b32 v182, a134\n" \
"v_accvgpr_read_b32 v183, a135\n" \
"v_mfma_f32_16x16x32_bf16 a[32:35], v[80:83], v[136:139], 0\n" \
"v_cvt_pk_bf16_f32 v249, v250, v251\n" \
"v_accvgpr_read_b32 v252, a160\n" \
"v_mfma_f32_16x16x32_bf16 a[32:35], v[76:79], v[132:135], a[32:35]\n" \
"v_accvgpr_read_b32 v253, a161\n" \
"s_waitcnt lgkmcnt(0)\n" \
"s_nop 0\n" \
"v_mfma_f32_16x16x32_bf16 a[128:131], v[148:151], v[52:55], 0\n" \
"v_cvt_pk_bf16_f32 v250, v180, v181\n" \
"v_cvt_pk_bf16_f32 v251, v182, v183\n" \
"v_mfma_f32_16x16x32_bf16 a[128:131], v[152:155], v[56:59], a[128:131]\n" \
"v_accvgpr_read_b32 v254, a162\n" \
"v_accvgpr_read_b32 v255, a163\n" \
"v_cvt_pk_bf16_f32 v252, v252, v253\n" \
"v_accvgpr_read_b32 v180, a164\n" \
"v_accvgpr_read_b32 v181, a165\n" \
"v_accvgpr_read_b32 v182, a166\n" \
"v_accvgpr_read_b32 v183, a167\n" \
"v_cvt_pk_bf16_f32 v253, v254, v255\n" \
"v_cvt_pk_bf16_f32 v254, v180, v181\n" \
"v_cvt_pk_bf16_f32 v255, v182, v183\n" \
"v_accvgpr_write_b32 a36, v252\n" \
"v_accvgpr_write_b32 a37, v253\n" \
"v_mfma_f32_16x16x32_bf16 a[132:135], v[160:163], v[56:59], 0\n" \
"v_accvgpr_write_b32 a38, v254\n" \
"v_accvgpr_write_b32 a39, v255\n" \
"v_accvgpr_read_b32 v252, a192\n" \
"v_accvgpr_read_b32 v253, a193\n" \
"v_accvgpr_read_b32 v254, a194\n" \
"v_accvgpr_read_b32 v255, a195\n" \
"v_cvt_pk_bf16_f32 v252, v252, v253\n" \
"s_add_u32 s88, s47, 0x0\n" \
"s_nop 0\n" \
"v_accvgpr_read_b32 v180, a196\n" \
"v_accvgpr_read_b32 v181, a197\n" \
"v_accvgpr_read_b32 v182, a198\n" \
"v_accvgpr_read_b32 v183, a199\n" \
"v_cvt_pk_bf16_f32 v253, v254, v255\n" \
"buffer_store_dwordx4 v[192:195], v20, s[80:83], s88 offen offset:1024 sc1\n" \
"v_cvt_pk_bf16_f32 v254, v180, v181\n" \
"v_cvt_pk_bf16_f32 v255, v182, v183\n" \
"v_accvgpr_write_b32 a64, v252\n" \
"v_accvgpr_write_b32 a65, v253\n" \
"v_mfma_f32_16x16x32_bf16 a[132:135], v[156:159], v[52:55], a[132:135]\n" \
"v_accvgpr_write_b32 a66, v254\n" \
"v_accvgpr_write_b32 a67, v255\n" \
"v_accvgpr_read_b32 v252, a224\n" \
"v_accvgpr_read_b32 v253, a225\n" \
"v_accvgpr_read_b32 v254, a226\n" \
"v_accvgpr_read_b32 v255, a227\n" \
"v_cvt_pk_bf16_f32 v252, v252, v253\n" \
"v_accvgpr_read_b32 v180, a228\n" \
"v_accvgpr_read_b32 v181, a229\n" \
"v_accvgpr_read_b32 v182, a230\n" \
"v_accvgpr_read_b32 v183, a231\n" \
"v_cvt_pk_bf16_f32 v253, v254, v255\n" \
"v_cvt_pk_bf16_f32 v254, v180, v181\n" \
"v_cvt_pk_bf16_f32 v255, v182, v183\n" \
"v_accvgpr_write_b32 a68, v252\n" \
"v_accvgpr_write_b32 a69, v253\n" \
"v_accvgpr_write_b32 a70, v254\n" \
"v_accvgpr_write_b32 a71, v255\n" \
"v_accvgpr_read_b32 v252, a136\n" \
"v_accvgpr_read_b32 v253, a137\n" \
"v_accvgpr_read_b32 v254, a138\n" \
"v_accvgpr_read_b32 v255, a139\n" \
"v_mfma_f32_16x16x32_bf16 a[136:139], v[164:167], v[52:55], 0\n" \
"ds_read_b128 v[84:87], v48 offset:0\n" \
"ds_read_b128 v[88:91], v49 offset:0\n" \
"ds_read_b128 v[92:95], v48 offset:2048\n" \
"ds_read_b128 v[96:99], v49 offset:2048\n" \
"ds_read_b128 v[100:103], v48 offset:4096\n" \
"ds_read_b128 v[104:107], v49 offset:4096\n" \
"ds_read_b128 v[108:111], v48 offset:6144\n" \
"ds_read_b128 v[112:115], v49 offset:6144\n" \
"v_mfma_f32_16x16x32_bf16 a[136:139], v[168:171], v[56:59], a[136:139]\n" \
"v_accvgpr_read_b32 v180, a140\n" \
"v_accvgpr_read_b32 v181, a141\n" \
"v_accvgpr_read_b32 v182, a142\n" \
"v_accvgpr_read_b32 v183, a143\n" \
"v_mfma_f32_16x16x32_bf16 a[140:143], v[176:179], v[56:59], 0\n" \
"v_cvt_pk_bf16_f32 v252, v252, v253\n" \
"v_cvt_pk_bf16_f32 v253, v254, v255\n" \
"v_mfma_f32_16x16x32_bf16 a[140:143], v[172:175], v[52:55], a[140:143]\n" \
"v_cvt_pk_bf16_f32 v254, v180, v181\n" \
"v_cvt_pk_bf16_f32 v255, v182, v183\n" \
"v_mfma_f32_16x16x32_bf16 a[160:163], v[148:151], v[60:63], 0\n" \
"v_accvgpr_write_b32 a96, v252\n" \
"v_accvgpr_write_b32 a97, v253\n" \
"v_mfma_f32_16x16x32_bf16 a[160:163], v[152:155], v[64:67], a[160:163]\n" \
"v_accvgpr_write_b32 a98, v254\n" \
"v_accvgpr_write_b32 a99, v255\n" \
"v_mfma_f32_16x16x32_bf16 a[164:167], v[160:163], v[64:67], 0\n" \
"v_accvgpr_read_b32 v252, a168\n" \
"v_accvgpr_read_b32 v253, a169\n" \
"v_mfma_f32_16x16x32_bf16 a[164:167], v[156:159], v[60:63], a[164:167]\n" \
"v_accvgpr_read_b32 v254, a170\n" \
"v_accvgpr_read_b32 v255, a171\n" \
"v_mfma_f32_16x16x32_bf16 a[168:171], v[164:167], v[60:63], 0\n" \
"s_add_u32 s89, s47, 0x2000\n" \
"v_accvgpr_read_b32 v180, a172\n" \
"buffer_store_dwordx4 v[196:199], v20, s[80:83], s89 offen offset:1024 sc1\n" \
"v_mfma_f32_16x16x32_bf16 a[168:171], v[168:171], v[64:67], a[168:171]\n" \
"v_accvgpr_read_b32 v181, a173\n" \
"v_accvgpr_read_b32 v182, a174\n" \
"v_accvgpr_read_b32 v183, a175\n" \
"v_mfma_f32_16x16x32_bf16 a[172:175], v[176:179], v[64:67], 0\n" \
"v_cvt_pk_bf16_f32 v252, v252, v253\n" \
"v_cvt_pk_bf16_f32 v253, v254, v255\n" \
"v_mfma_f32_16x16x32_bf16 a[172:175], v[172:175], v[60:63], a[172:175]\n" \
"v_cvt_pk_bf16_f32 v254, v180, v181\n" \
"v_cvt_pk_bf16_f32 v255, v182, v183\n" \
"v_mfma_f32_16x16x32_bf16 a[192:195], v[148:151], v[68:71], 0\n" \
"v_accvgpr_write_b32 a100, v252\n" \
"v_accvgpr_write_b32 a101, v253\n" \
"v_mfma_f32_16x16x32_bf16 a[192:195], v[152:155], v[72:75], a[192:195]\n" \
"v_accvgpr_write_b32 a102, v254\n" \
"v_accvgpr_write_b32 a103, v255\n" \
"s_waitcnt vmcnt(4) lgkmcnt(0)\n" \
"s_barrier\n" \
"v_mfma_f32_16x16x32_bf16 a[196:199], v[160:163], v[72:75], 0\n" \
"v_accvgpr_read_b32 v252, a200\n" \
"v_accvgpr_read_b32 v253, a201\n" \
"v_mfma_f32_16x16x32_bf16 a[196:199], v[156:159], v[68:71], a[196:199]\n" \
"v_accvgpr_read_b32 v254, a202\n" \
"v_accvgpr_read_b32 v255, a203\n" \
"v_mfma_f32_16x16x32_bf16 a[200:203], v[164:167], v[68:71], 0\n" \
"s_add_u32 m0, s43, 0x0\n" \
"s_nop 0\n" \
"s_add_u32 s85, s38, 0x4000\n" \
"v_mfma_f32_16x16x32_bf16 a[200:203], v[168:171], v[72:75], a[200:203]\n" \
"buffer_load_dwordx4 v20, s[80:83], s85 offen sc1 lds\n" \
"v_accvgpr_read_b32 v180, a204\n" \
"v_accvgpr_read_b32 v181, a205\n" \
"v_accvgpr_read_b32 v182, a206\n" \
"v_accvgpr_read_b32 v183, a207\n" \
"v_mfma_f32_16x16x32_bf16 a[204:207], v[176:179], v[72:75], 0\n" \
"s_add_u32 m0, s43, 0x1000\n" \
"s_add_u32 s84, s38, 0x5000\n" \
"v_mfma_f32_16x16x32_bf16 a[204:207], v[172:175], v[68:71], a[204:207]\n" \
"buffer_load_dwordx4 v20, s[80:83], s84 offen sc1 lds\n" \
"v_mfma_f32_16x16x32_bf16 a[224:227], v[148:151], v[76:79], 0\n" \
"v_cvt_pk_bf16_f32 v252, v252, v253\n" \
"v_cvt_pk_bf16_f32 v253, v254, v255\n" \
"v_mfma_f32_16x16x32_bf16 a[224:227], v[152:155], v[80:83], a[224:227]\n" \
"v_cvt_pk_bf16_f32 v254, v180, v181\n" \
"v_cvt_pk_bf16_f32 v255, v182, v183\n" \
"v_mfma_f32_16x16x32_bf16 a[228:231], v[160:163], v[80:83], 0\n" \
"v_accvgpr_write_b32 a8, v252\n" \
"v_accvgpr_write_b32 a9, v253\n" \
"v_mfma_f32_16x16x32_bf16 a[228:231], v[156:159], v[76:79], a[228:231]\n" \
"v_accvgpr_write_b32 a10, v254\n" \
"v_accvgpr_write_b32 a11, v255\n" \
"v_accvgpr_read_b32 v252, a232\n" \
"v_accvgpr_read_b32 v253, a233\n" \
"v_accvgpr_read_b32 v254, a234\n" \
"v_accvgpr_read_b32 v255, a235\n" \
"v_mfma_f32_16x16x32_bf16 a[232:235], v[164:167], v[76:79], 0\n" \
"s_add_u32 m0, s43, 0x3000\n" \
"s_mov_b32 s85, 0x100\n" \
"buffer_load_dwordx4 v21, s[76:79], s85 offen lds\n" \
"v_mfma_f32_16x16x32_bf16 a[232:235], v[168:171], v[80:83], a[232:235]\n" \
"v_accvgpr_read_b32 v180, a236\n" \
"v_accvgpr_read_b32 v181, a237\n" \
"v_accvgpr_read_b32 v182, a238\n" \
"v_accvgpr_read_b32 v183, a239\n" \
"v_mfma_f32_16x16x32_bf16 a[236:239], v[176:179], v[80:83], 0\n" \
"v_cvt_pk_bf16_f32 v252, v252, v253\n" \
"v_cvt_pk_bf16_f32 v253, v254, v255\n" \
"v_mfma_f32_16x16x32_bf16 a[236:239], v[172:175], v[76:79], a[236:239]\n" \
"v_cvt_pk_bf16_f32 v254, v180, v181\n" \
"v_cvt_pk_bf16_f32 v255, v182, v183\n" \
"v_accvgpr_write_b32 a12, v252\n" \
"v_accvgpr_write_b32 a13, v253\n" \
"v_accvgpr_write_b32 a14, v254\n" \
"v_accvgpr_write_b32 a15, v255\n" \
"v_accvgpr_read_b32 v252, a144\n" \
"v_accvgpr_read_b32 v253, a145\n" \
"v_accvgpr_read_b32 v254, a146\n" \
"v_accvgpr_read_b32 v255, a147\n" \
"v_mfma_f32_16x16x32_bf16 a[144:147], v[148:151], v[84:87], 0\n" \
"s_add_u32 m0, %[mA], 0x0\n" \
"s_nop 0\n" \
"s_mov_b32 s84, 0x100\n" \
"buffer_load_dwordx4 v24, s[76:79], s84 offen lds\n" \
"v_mfma_f32_16x16x32_bf16 a[144:147], v[152:155], v[88:91], a[144:147]\n" \
"v_cvt_pk_bf16_f32 v252, v252, v253\n" \
"v_accvgpr_read_b32 v180, a176\n" \
"v_accvgpr_read_b32 v181, a177\n" \
"v_accvgpr_read_b32 v182, a178\n" \
"v_accvgpr_read_b32 v183, a179\n" \
"v_cvt_pk_bf16_f32 v253, v254, v255\n" \
"v_cvt_pk_bf16_f32 v254, v180, v181\n" \
"v_cvt_pk_bf16_f32 v255, v182, v183\n" \
"v_accvgpr_write_b32 a40, v252\n" \
"v_accvgpr_write_b32 a41, v253\n" \
"v_accvgpr_write_b32 a42, v254\n" \
"v_accvgpr_write_b32 a43, v255\n" \
"v_accvgpr_read_b32 v252, a148\n" \
"v_accvgpr_read_b32 v253, a149\n" \
"v_accvgpr_read_b32 v254, a150\n" \
"v_accvgpr_read_b32 v255, a151\n" \
"v_mfma_f32_16x16x32_bf16 a[148:151], v[160:163], v[88:91], 0\n" \
"v_cvt_pk_bf16_f32 v252, v252, v253\n" \
"s_add_u32 m0, %[mA], 0x400\n" \
"v_mfma_f32_16x16x32_bf16 a[148:151], v[156:159], v[84:87], a[148:151]\n" \
"s_mov_b32 s85, 0x2500\n" \
"v_accvgpr_read_b32 v180, a180\n" \
"v_accvgpr_read_b32 v181, a181\n" \
"v_accvgpr_read_b32 v182, a182\n" \
"v_accvgpr_read_b32 v183, a183\n" \
"v_cvt_pk_bf16_f32 v253, v254, v255\n" \
"buffer_load_dwordx4 v25, s[76:79], s85 offen lds\n" \
"v_cvt_pk_bf16_f32 v254, v180, v181\n" \
"v_cvt_pk_bf16_f32 v255, v182, v183\n" \
"v_accvgpr_write_b32 a44, v252\n" \
"v_accvgpr_write_b32 a45, v253\n" \
"v_accvgpr_write_b32 a46, v254\n" \
"v_accvgpr_write_b32 a47, v255\n" \
"v_accvgpr_read_b32 v252, a152\n" \
"v_accvgpr_read_b32 v253, a153\n" \
"v_accvgpr_read_b32 v254, a154\n" \
"v_accvgpr_read_b32 v255, a155\n" \
"v_mfma_f32_16x16x32_bf16 a[152:155], v[164:167], v[84:87], 0\n" \
"v_cvt_pk_bf16_f32 v252, v252, v253\n" \
"v_accvgpr_read_b32 v180, a184\n" \
"v_mfma_f32_16x16x32_bf16 a[152:155], v[168:171], v[88:91], a[152:155]\n" \
"v_accvgpr_read_b32 v181, a185\n" \
"v_accvgpr_read_b32 v182, a186\n" \
"v_accvgpr_read_b32 v183, a187\n" \
"v_cvt_pk_bf16_f32 v253, v254, v255\n" \
"v_cvt_pk_bf16_f32 v254, v180, v181\n" \
"v_cvt_pk_bf16_f32 v255, v182, v183\n" \
"v_accvgpr_write_b32 a72, v252\n" \
"v_accvgpr_write_b32 a73, v253\n" \
"v_accvgpr_write_b32 a74, v254\n" \
"v_accvgpr_write_b32 a75, v255\n" \
"v_accvgpr_read_b32 v252, a156\n" \
"v_accvgpr_read_b32 v253, a157\n" \
"v_accvgpr_read_b32 v254, a158\n" \
"v_accvgpr_read_b32 v255, a159\n" \
"v_mfma_f32_16x16x32_bf16 a[156:159], v[176:179], v[88:91], 0\n" \
"s_add_u32 m0, %[mB], 0x0\n" \
"s_nop 0\n" \
"s_mov_b32 s84, 0x100000\n" \
"v_mfma_f32_16x16x32_bf16 a[156:159], v[172:175], v[84:87], a[156:159]\n" \
"buffer_load_dwordx4 v26, s[68:71], s84 offen lds\n" \
"v_mfma_f32_16x16x32_bf16 a[176:179], v[148:151], v[92:95], 0\n" \
"s_add_u32 m0, %[mA], 0x800\n" \
"s_mov_b32 s85, 0x4900\n" \
"v_mfma_f32_16x16x32_bf16 a[176:179], v[152:155], v[96:99], a[176:179]\n" \
"v_accvgpr_read_b32 v180, a188\n" \
"v_accvgpr_read_b32 v181, a189\n" \
"v_mfma_f32_16x16x32_bf16 a[180:183], v[160:163], v[96:99], 0\n" \
"buffer_load_dwordx4 v24, s[76:79], s85 offen lds\n" \
"v_mfma_f32_16x16x32_bf16 a[180:183], v[156:159], v[92:95], a[180:183]\n" \
"v_accvgpr_read_b32 v182, a190\n" \
"v_accvgpr_read_b32 v183, a191\n" \
"v_mfma_f32_16x16x32_bf16 a[184:187], v[164:167], v[92:95], 0\n" \
"s_add_u32 m0, %[mA], 0xc00\n" \
"s_mov_b32 s84, 0x6d00\n" \
"v_mfma_f32_16x16x32_bf16 a[184:187], v[168:171], v[96:99], a[184:187]\n" \
"buffer_load_dwordx4 v25, s[76:79], s84 offen lds\n" \
"v_mfma_f32_16x16x32_bf16 a[188:191], v[176:179], v[96:99], 0\n" \
"v_cvt_pk_bf16_f32 v252, v252, v253\n" \
"v_cvt_pk_bf16_f32 v253, v254, v255\n" \
"v_mfma_f32_16x16x32_bf16 a[188:191], v[172:175], v[92:95], a[188:191]\n" \
"v_cvt_pk_bf16_f32 v254, v180, v181\n" \
"v_cvt_pk_bf16_f32 v255, v182, v183\n" \
"v_accvgpr_write_b32 a76, v252\n" \
"v_accvgpr_write_b32 a77, v253\n" \
"v_accvgpr_write_b32 a78, v254\n" \
"v_accvgpr_write_b32 a79, v255\n" \
"v_accvgpr_read_b32 v252, a208\n" \
"v_accvgpr_read_b32 v253, a209\n" \
"v_accvgpr_read_b32 v254, a210\n" \
"v_accvgpr_read_b32 v255, a211\n" \
"v_mfma_f32_16x16x32_bf16 a[208:211], v[148:151], v[100:103], 0\n" \
"v_cvt_pk_bf16_f32 v252, v252, v253\n" \
"v_accvgpr_read_b32 v180, a240\n" \
"v_mfma_f32_16x16x32_bf16 a[208:211], v[152:155], v[104:107], a[208:211]\n" \
"v_accvgpr_read_b32 v181, a241\n" \
"v_accvgpr_read_b32 v182, a242\n" \
"L_pf5:\n" \
"s_cbranch_execz L_pf9\n" \
"s_nop 0\n" \
"v_accvgpr_read_b32 v183, a243\n" \
"v_cvt_pk_bf16_f32 v253, v254, v255\n" \
"v_cvt_pk_bf16_f32 v254, v180, v181\n" \
"v_cvt_pk_bf16_f32 v255, v182, v183\n" \
"v_accvgpr_write_b32 a104, v252\n" \
"v_accvgpr_write_b32 a105, v253\n" \
"v_accvgpr_write_b32 a106, v254\n" \
"v_accvgpr_write_b32 a107, v255\n" \
"v_accvgpr_read_b32 v252, a212\n" \
"v_accvgpr_read_b32 v253, a213\n" \
"v_accvgpr_read_b32 v254, a214\n" \
"v_accvgpr_read_b32 v255, a215\n" \
"v_mfma_f32_16x16x32_bf16 a[212:215], v[160:163], v[104:107], 0\n" \
"s_add_u32 m0, %[mB], 0x400\n" \
"s_mov_b32 s85, 0x108000\n" \
"buffer_load_dwordx4 v26, s[68:71], s85 offen lds\n" \
"ds_read_b64_tr_b16 v[140:141], v39 offset:32768\n" \
"ds_read_b64_tr_b16 v[142:143], v39 offset:33280\n" \
"ds_read_b128 v[84:87], v22 offset:32768\n" \
"v_mfma_f32_16x16x32_bf16 a[212:215], v[156:159], v[100:103], a[212:215]\n" \
"ds_read_b64_tr_b16 v[144:145], v39 offset:36864\n" \
"ds_read_b64_tr_b16 v[146:147], v39 offset:37376\n" \
"ds_read_b128 v[88:91], v23 offset:32768\n" \
"v_accvgpr_read_b32 v184, a216\n" \
"v_accvgpr_read_b32 v185, a217\n" \
"v_accvgpr_read_b32 v186, a218\n" \
"v_accvgpr_read_b32 v187, a219\n" \
"v_mfma_f32_16x16x32_bf16 a[216:219], v[164:167], v[100:103], 0\n" \
"ds_read_b64_tr_b16 v[128:129], v37 offset:36864\n" \
"ds_read_b64_tr_b16 v[130:131], v37 offset:37376\n" \
"v_mfma_f32_16x16x32_bf16 a[216:219], v[168:171], v[104:107], a[216:219]\n" \
"v_cvt_pk_bf16_f32 v252, v252, v253\n" \
"v_accvgpr_read_b32 v180, a244\n" \
"v_accvgpr_read_b32 v181, a245\n" \
"v_accvgpr_read_b32 v182, a246\n" \
"v_accvgpr_read_b32 v183, a247\n" \
"v_cvt_pk_bf16_f32 v253, v254, v255\n" \
"v_cvt_pk_bf16_f32 v254, v180, v181\n" \
"v_cvt_pk_bf16_f32 v255, v182, v183\n" \
"ds_read_b64_tr_b16 v[124:125], v37 offset:32768\n" \
"ds_read_b64_tr_b16 v[126:127], v37 offset:33280\n" \
"v_accvgpr_write_b32 a108, v252\n" \
"v_accvgpr_write_b32 a109, v253\n" \
"v_accvgpr_write_b32 a110, v254\n" \
"v_accvgpr_write_b32 a111, v255\n" \
"v_accvgpr_read_b32 v252, a220\n" \
"v_accvgpr_read_b32 v253, a221\n" \
"v_accvgpr_read_b32 v254, a222\n" \
"v_accvgpr_read_b32 v255, a223\n" \
"v_mfma_f32_16x16x32_bf16 a[220:223], v[176:179], v[104:107], 0\n" \
"s_add_u32 m0, %[mA], 0x1000\n" \
"s_mov_b32 s84, 0x9100\n" \
"buffer_load_dwordx4 v24, s[76:79], s84 offen lds\n" \
"ds_read_b64_tr_b16 v[132:133], v38 offset:32768\n" \
"ds_read_b64_tr_b16 v[134:135], v38 offset:33280\n" \
"v_mfma_f32_16x16x32_bf16 a[220:223], v[172:175], v[100:103], a[220:223]\n" \
"ds_read_b64_tr_b16 v[136:137], v38 offset:36864\n" \
"ds_read_b64_tr_b16 v[138:139], v38 offset:37376\n" \
"v_mfma_f32_16x16x32_bf16 a[240:243], v[148:151], v[108:111], 0\n" \
"ds_read_b128 v[56:59], v29 offset:32768\n" \
"s_add_u32 m0, %[mA], 0x1400\n" \
"v_mfma_f32_16x16x32_bf16 a[240:243], v[152:155], v[112:115], a[240:243]\n" \
"s_mov_b32 s85, 0xb500\n" \
"ds_read_b128 v[52:55], v28 offset:32768\n" \
"buffer_load_dwordx4 v25, s[76:79], s85 offen lds\n" \
"v_mfma_f32_16x16x32_bf16 a[244:247], v[160:163], v[112:115], 0\n" \
"v_accvgpr_read_b32 v180, a248\n" \
"v_accvgpr_read_b32 v181, a249\n" \
"v_mfma_f32_16x16x32_bf16 a[244:247], v[156:159], v[108:111], a[244:247]\n" \
"v_accvgpr_read_b32 v182, a250\n" \
"v_accvgpr_read_b32 v183, a251\n" \
"v_mfma_f32_16x16x32_bf16 a[248:251], v[164:167], v[108:111], 0\n" \
"ds_read_b64_tr_b16 v[120:121], v36 offset:36864\n" \
"ds_read_b64_tr_b16 v[122:123], v36 offset:37376\n" \
"v_mfma_f32_16x16x32_bf16 a[248:251], v[168:171], v[112:115], a[248:251]\n" \
"s_add_u32 m0, %[mB], 0x800\n" \
"s_mov_b32 s84, 0x110000\n" \
"v_cvt_pk_bf16_f32 v184, v184, v185\n" \
"buffer_load_dwordx4 v27, s[68:71], s84 offen lds\n" \
"v_cvt_pk_bf16_f32 v185, v186, v187\n" \
"ds_read_b64_tr_b16 v[116:117], v36 offset:32768\n" \
"ds_read_b64_tr_b16 v[118:119], v36 offset:33280\n" \
"v_cvt_pk_bf16_f32 v186, v180, v181\n" \
"v_cvt_pk_bf16_f32 v187, v182, v183\n" \
"v_accvgpr_read_b32 v180, a252\n" \
"v_accvgpr_read_b32 v181, a253\n" \
"v_accvgpr_read_b32 v182, a254\n" \
"v_accvgpr_read_b32 v183, a255\n" \
"v_mfma_f32_16x16x32_bf16 a[252:255], v[176:179], v[112:115], 0\n" \
"ds_read_b128 v[64:67], v29 offset:34816\n" \
"ds_read_b128 v[60:63], v28 offset:34816\n" \
"v_mfma_f32_16x16x32_bf16 a[252:255], v[172:175], v[108:111], a[252:255]\n" \
"s_add_u32 m0, %[mA], 0x1800\n" \
"s_waitcnt lgkmcnt(12)\n" \
"v_mfma_f32_16x16x32_bf16 a[16:19], v[140:143], v[84:87], a[16:19]\n" \
"s_mov_b32 s84, 0xd900\n" \
"ds_read_b128 v[72:75], v29 offset:36864\n" \
"v_mfma_f32_16x16x32_bf16 a[16:19], v[144:147], v[88:91], a[16:19]\n" \
"buffer_load_dwordx4 v24, s[76:79], s84 offen lds\n" \
"v_mfma_f32_16x16x32_bf16 a[48:51], v[128:131], v[88:91], a[48:51]\n" \
"s_add_u32 m0, %[mA], 0x1c00\n" \
"s_mov_b32 s85, 0xfd00\n" \
"v_mfma_f32_16x16x32_bf16 a[48:51], v[124:127], v[84:87], a[48:51]\n" \
"ds_read_b128 v[68:71], v28 offset:36864\n" \
"s_waitcnt lgkmcnt(9)\n" \
"v_mfma_f32_16x16x32_bf16 a[80:83], v[84:87], v[132:135], a[80:83]\n" \
"buffer_load_dwordx4 v25, s[76:79], s85 offen lds\n" \
"v_mfma_f32_16x16x32_bf16 a[80:83], v[88:91], v[136:139], a[80:83]\n" \
"s_add_u32 m0, %[mB], 0xc00\n" \
"s_mov_b32 s84, 0x118000\n" \
"v_mfma_f32_16x16x32_bf16 a[112:115], v[144:147], v[56:59], a[112:115]\n" \
"ds_read_b128 v[80:83], v29 offset:38912\n" \
"s_waitcnt lgkmcnt(9)\n" \
"v_mfma_f32_16x16x32_bf16 a[112:115], v[140:143], v[52:55], a[112:115]\n" \
"buffer_load_dwordx4 v27, s[68:71], s84 offen lds\n" \
"v_mfma_f32_16x16x32_bf16 a[20:23], v[124:127], v[52:55], a[20:23]\n" \
"ds_read_b128 v[76:79], v28 offset:38912\n" \
"ds_read_b64_tr_b16 v[152:153], v32 offset:40960\n" \
"v_mfma_f32_16x16x32_bf16 a[20:23], v[128:131], v[56:59], a[20:23]\n" \
"s_add_u32 s88, s47, 0x0\n" \
"s_waitcnt lgkmcnt(9)\n" \
"v_mfma_f32_16x16x32_bf16 a[52:55], v[120:123], v[56:59], a[52:55]\n" \
"ds_read_b64_tr_b16 v[154:155], v32 offset:41984\n" \
"s_waitcnt lgkmcnt(8)\n" \
"v_mfma_f32_16x16x32_bf16 a[52:55], v[116:119], v[52:55], a[52:55]\n" \
"buffer_store_dwordx4 v[200:203], v20, s[80:83], s88 offen offset:2048 sc1\n" \
"v_mfma_f32_16x16x32_bf16 a[84:87], v[52:55], v[132:135], a[84:87]\n" \
"ds_read_b64_tr_b16 v[148:149], v32 offset:32768\n" \
"ds_read_b64_tr_b16 v[150:151], v32 offset:33792\n" \
"v_mfma_f32_16x16x32_bf16 a[84:87], v[56:59], v[136:139], a[84:87]\n" \
"ds_read_b64_tr_b16 v[156:157], v33 offset:32768\n" \
"s_waitcnt lgkmcnt(9)\n" \
"s_nop 0\n" \
"v_mfma_f32_16x16x32_bf16 a[116:119], v[144:147], v[64:67], a[116:119]\n" \
"ds_read_b64_tr_b16 v[158:159], v33 offset:33792\n" \
"ds_read_b64_tr_b16 v[160:161], v33 offset:40960\n" \
"v_mfma_f32_16x16x32_bf16 a[116:119], v[140:143], v[60:63], a[116:119]\n" \
"ds_read_b64_tr_b16 v[162:163], v33 offset:41984\n" \
"ds_read_b64_tr_b16 v[168:169], v34 offset:40960\n" \
"v_mfma_f32_16x16x32_bf16 a[24:27], v[124:127], v[60:63], a[24:27]\n" \
"ds_read_b64_tr_b16 v[170:171], v34 offset:41984\n" \
"ds_read_b64_tr_b16 v[164:165], v34 offset:32768\n" \
"v_mfma_f32_16x16x32_bf16 a[24:27], v[128:131], v[64:67], a[24:27]\n" \
"ds_read_b64_tr_b16 v[166:167], v34 offset:33792\n" \
"ds_read_b64_tr_b16 v[172:173], v35 offset:32768\n" \
"v_mfma_f32_16x16x32_bf16 a[56:59], v[120:123], v[64:67], a[56:59]\n" \
"ds_read_b64_tr_b16 v[174:175], v35 offset:33792\n" \
"ds_read_b64_tr_b16 v[176:177], v35 offset:40960\n" \
"v_mfma_f32_16x16x32_bf16 a[56:59], v[116:119], v[60:63], a[56:59]\n" \
"s_add_u32 s89, s47, 0x2000\n" \
"ds_read_b64_tr_b16 v[178:179], v35 offset:41984\n" \
"v_mfma_f32_16x16x32_bf16 a[88:91], v[60:63], v[132:135], a[88:91]\n" \
"buffer_store_dwordx4 v[204:207], v20, s[80:83], s89 offen offset:2048 sc1\n" \
"v_mfma_f32_16x16x32_bf16 a[88:91], v[64:67], v[136:139], a[88:91]\n" \
"ds_read_b128 v[84:87], v48 offset:32768\n" \
"s_waitcnt lgkmcnt(15)\n" \
"s_nop 0\n" \
"v_mfma_f32_16x16x32_bf16 a[120:123], v[144:147], v[72:75], a[120:123]\n" \
"ds_read_b128 v[88:91], v49 offset:32768\n" \
"ds_read_b128 v[92:95], v48 offset:34816\n" \
"v_mfma_f32_16x16x32_bf16 a[120:123], v[140:143], v[68:71], a[120:123]\n" \
"ds_read_b128 v[96:99], v49 offset:34816\n" \
"ds_read_b128 v[100:103], v48 offset:36864\n" \
"v_mfma_f32_16x16x32_bf16 a[28:31], v[124:127], v[68:71], a[28:31]\n" \
"ds_read_b128 v[104:107], v49 offset:36864\n" \
"ds_read_b128 v[108:111], v48 offset:38912\n" \
"v_mfma_f32_16x16x32_bf16 a[28:31], v[128:131], v[72:75], a[28:31]\n" \
"ds_read_b128 v[112:115], v49 offset:38912\n" \
"v_cvt_pk_bf16_f32 v252, v252, v253\n" \
"v_mfma_f32_16x16x32_bf16 a[60:63], v[120:123], v[72:75], a[60:63]\n" \
"v_cvt_pk_bf16_f32 v253, v254, v255\n" \
"v_cvt_pk_bf16_f32 v254, v180, v181\n" \
"v_mfma_f32_16x16x32_bf16 a[60:63], v[116:119], v[68:71], a[60:63]\n" \
"v_cvt_pk_bf16_f32 v255, v182, v183\n" \
"v_mfma_f32_16x16x32_bf16 a[92:95], v[68:71], v[132:135], a[92:95]\n" \
"v_mfma_f32_16x16x32_bf16 a[92:95], v[72:75], v[136:139], a[92:95]\n" \
"s_add_u32 s88, s47, 0x0\n" \
"v_mfma_f32_16x16x32_bf16 a[124:127], v[144:147], v[80:83], a[124:127]\n" \
"buffer_store_dwordx4 v[208:211], v20, s[80:83], s88 offen offset:3072 sc1\n" \
"v_mfma_f32_16x16x32_bf16 a[124:127], v[140:143], v[76:79], a[124:127]\n" \
"v_mfma_f32_16x16x32_bf16 a[0:3], v[124:127], v[76:79], a[0:3]\n" \
"v_mfma_f32_16x16x32_bf16 a[0:3], v[128:131], v[80:83], a[0:3]\n" \
"v_mfma_f32_16x16x32_bf16 a[4:7], v[120:123], v[80:83], a[4:7]\n" \
"v_mfma_f32_16x16x32_bf16 a[4:7], v[116:119], v[76:79], a[4:7]\n" \
"v_mfma_f32_16x16x32_bf16 a[32:35], v[76:79], v[132:135], a[32:35]\n" \
"v_mfma_f32_16x16x32_bf16 a[32:35], v[80:83], v[136:139], a[32:35]\n" \
"v_mfma_f32_16x16x32_bf16 a[128:131], v[152:155], v[56:59], a[128:131]\n" \
"s_waitcnt lgkmcnt(2)\n" \
"v_mfma_f32_16x16x32_bf16 a[128:131], v[148:151], v[52:55], a[128:131]\n" \
"v_mfma_f32_16x16x32_bf16 a[132:135], v[156:159], v[52:55], a[132:135]\n" \
"v_mfma_f32_16x16x32_bf16 a[132:135], v[160:163], v[56:59], a[132:135]\n" \
"v_mfma_f32_16x16x32_bf16 a[136:139], v[168:171], v[56:59], a[136:139]\n" \
"v_mfma_f32_16x16x32_bf16 a[136:139], v[164:167], v[52:55], a[136:139]\n" \
"v_mfma_f32_16x16x32_bf16 a[140:143], v[172:175], v[52:55], a[140:143]\n" \
"v_mfma_f32_16x16x32_bf16 a[140:143], v[176:179], v[56:59], a[140:143]\n" \
"v_mfma_f32_16x16x32_bf16 a[160:163], v[152:155], v[64:67], a[160:163]\n" \
"v_mfma_f32_16x16x32_bf16 a[160:163], v[148:151], v[60:63], a[160:163]\n" \
"v_mfma_f32_16x16x32_bf16 a[164:167], v[156:159], v[60:63], a[164:167]\n" \
"v_mfma_f32_16x16x32_bf16 a[164:167], v[160:163], v[64:67], a[164:167]\n" \
"v_mfma_f32_16x16x32_bf16 a[168:171], v[168:171], v[64:67], a[168:171]\n" \
"v_mfma_f32_16x16x32_bf16 a[168:171], v[164:167], v[60:63], a[168:171]\n" \
"v_mfma_f32_16x16x32_bf16 a[172:175], v[172:175], v[60:63], a[172:175]\n" \
"v_mfma_f32_16x16x32_bf16 a[172:175], v[176:179], v[64:67], a[172:175]\n" \
"v_mfma_f32_16x16x32_bf16 a[192:195], v[152:155], v[72:75], a[192:195]\n" \
"v_mfma_f32_16x16x32_bf16 a[192:195], v[148:151], v[68:71], a[192:195]\n" \
"s_waitcnt vmcnt(3) lgkmcnt(0)\n" \
"s_barrier\n" \
"v_mfma_f32_16x16x32_bf16 a[196:199], v[156:159], v[68:71], a[196:199]\n" \
"s_add_u32 m0, s43, 0x8000\n" \
"s_add_u32 s85, s38, 0x6000\n" \
"v_mfma_f32_16x16x32_bf16 a[196:199], v[160:163], v[72:75], a[196:199]\n" \
"buffer_load_dwordx4 v20, s[80:83], s85 offen sc1 lds\n" \
"v_mfma_f32_16x16x32_bf16 a[200:203], v[168:171], v[72:75], a[200:203]\n" \
"s_add_u32 m0, s43, 0x9000\n" \
"s_add_u32 s84, s38, 0x7000\n" \
"v_mfma_f32_16x16x32_bf16 a[200:203], v[164:167], v[68:71], a[200:203]\n" \
"ds_read_b64_tr_b16 v[144:145], v39 offset:4096\n" \
"ds_read_b64_tr_b16 v[146:147], v39 offset:4608\n" \
"v_mfma_f32_16x16x32_bf16 a[204:207], v[172:175], v[68:71], a[204:207]\n" \
"buffer_load_dwordx4 v20, s[80:83], s84 offen sc1 lds\n" \
"v_mfma_f32_16x16x32_bf16 a[204:207], v[176:179], v[72:75], a[204:207]\n" \
"ds_read_b64_tr_b16 v[140:141], v39 offset:0\n" \
"ds_read_b64_tr_b16 v[142:143], v39 offset:512\n" \
"v_mfma_f32_16x16x32_bf16 a[224:227], v[152:155], v[80:83], a[224:227]\n" \
"s_add_u32 m0, s43, 0xb000\n" \
"s_mov_b32 s85, 0x180\n" \
"v_mfma_f32_16x16x32_bf16 a[224:227], v[148:151], v[76:79], a[224:227]\n" \
"buffer_load_dwordx4 v21, s[76:79], s85 offen lds\n" \
"v_mfma_f32_16x16x32_bf16 a[228:231], v[156:159], v[76:79], a[228:231]\n" \
"ds_read_b64_tr_b16 v[124:125], v37 offset:0\n" \
"ds_read_b64_tr_b16 v[126:127], v37 offset:512\n" \
"v_mfma_f32_16x16x32_bf16 a[228:231], v[160:163], v[80:83], a[228:231]\n" \
"ds_read_b64_tr_b16 v[128:129], v37 offset:4096\n" \
"ds_read_b64_tr_b16 v[130:131], v37 offset:4608\n" \
"v_mfma_f32_16x16x32_bf16 a[232:235], v[168:171], v[80:83], a[232:235]\n" \
"s_add_u32 m0, %[mA], 0x8000\n" \
"s_mov_b32 s84, 0x180\n" \
"v_mfma_f32_16x16x32_bf16 a[232:235], v[164:167], v[76:79], a[232:235]\n" \
"buffer_load_dwordx4 v24, s[76:79], s84 offen lds\n" \
"v_mfma_f32_16x16x32_bf16 a[236:239], v[172:175], v[76:79], a[236:239]\n" \
"ds_read_b64_tr_b16 v[136:137], v38 offset:4096\n" \
"ds_read_b64_tr_b16 v[138:139], v38 offset:4608\n" \
"v_mfma_f32_16x16x32_bf16 a[236:239], v[176:179], v[80:83], a[236:239]\n" \
"s_add_u32 m0, %[mA], 0x8400\n" \
"s_mov_b32 s85, 0x2580\n" \
"v_mfma_f32_16x16x32_bf16 a[144:147], v[152:155], v[88:91], a[144:147]\n" \
"buffer_load_dwordx4 v25, s[76:79], s85 offen lds\n" \
"v_mfma_f32_16x16x32_bf16 a[144:147], v[148:151], v[84:87], a[144:147]\n" \
"ds_read_b64_tr_b16 v[132:133], v38 offset:0\n" \
"ds_read_b64_tr_b16 v[134:135], v38 offset:512\n" \
"v_mfma_f32_16x16x32_bf16 a[148:151], v[156:159], v[84:87], a[148:151]\n" \
"ds_read_b128 v[52:55], v28 offset:0\n" \
"ds_read_b128 v[56:59], v29 offset:0\n" \
"v_mfma_f32_16x16x32_bf16 a[148:151], v[160:163], v[88:91], a[148:151]\n" \
"s_add_u32 m0, %[mB], 0x8000\n" \
"s_mov_b32 s84, 0x180000\n" \
"v_mfma_f32_16x16x32_bf16 a[152:155], v[168:171], v[88:91], a[152:155]\n" \
"buffer_load_dwordx4 v26, s[68:71], s84 offen lds\n" \
"v_mfma_f32_16x16x32_bf16 a[152:155], v[164:167], v[84:87], a[152:155]\n" \
"ds_read_b64_tr_b16 v[116:117], v36 offset:0\n" \
"ds_read_b64_tr_b16 v[118:119], v36 offset:512\n" \
"v_mfma_f32_16x16x32_bf16 a[156:159], v[172:175], v[84:87], a[156:159]\n" \
"ds_read_b128 v[84:87], v22 offset:0\n" \
"ds_read_b64_tr_b16 v[120:121], v36 offset:4096\n" \
"v_mfma_f32_16x16x32_bf16 a[156:159], v[176:179], v[88:91], a[156:159]\n" \
"s_add_u32 m0, %[mA], 0x8800\n" \
"s_mov_b32 s85, 0x4980\n" \
"v_mfma_f32_16x16x32_bf16 a[176:179], v[152:155], v[96:99], a[176:179]\n" \
"buffer_load_dwordx4 v24, s[76:79], s85 offen lds\n" \
"v_mfma_f32_16x16x32_bf16 a[176:179], v[148:151], v[92:95], a[176:179]\n" \
"ds_read_b128 v[88:91], v23 offset:0\n" \
"ds_read_b64_tr_b16 v[122:123], v36 offset:4608\n" \
"v_mfma_f32_16x16x32_bf16 a[180:183], v[156:159], v[92:95], a[180:183]\n" \
"s_add_u32 m0, %[mA], 0x8c00\n" \
"s_mov_b32 s84, 0x6d80\n" \
"v_mfma_f32_16x16x32_bf16 a[180:183], v[160:163], v[96:99], a[180:183]\n" \
"buffer_load_dwordx4 v25, s[76:79], s84 offen lds\n" \
"v_mfma_f32_16x16x32_bf16 a[184:187], v[168:171], v[96:99], a[184:187]\n" \
"ds_read_b128 v[60:63], v28 offset:2048\n" \
"ds_read_b128 v[64:67], v29 offset:2048\n" \
"v_mfma_f32_16x16x32_bf16 a[184:187], v[164:167], v[92:95], a[184:187]\n" \
"ds_read_b128 v[68:71], v28 offset:4096\n" \
"ds_read_b128 v[72:75], v29 offset:4096\n" \
"v_mfma_f32_16x16x32_bf16 a[188:191], v[172:175], v[92:95], a[188:191]\n" \
"s_add_u32 m0, %[mB], 0x8400\n" \
"s_mov_b32 s85, 0x188000\n" \
"v_mfma_f32_16x16x32_bf16 a[188:191], v[176:179], v[96:99], a[188:191]\n" \
"buffer_load_dwordx4 v26, s[68:71], s85 offen lds\n" \
"v_mfma_f32_16x16x32_bf16 a[208:211], v[152:155], v[104:107], a[208:211]\n" \
"ds_read_b128 v[76:79], v28 offset:6144\n" \
"ds_read_b128 v[80:83], v29 offset:6144\n" \
"v_mfma_f32_16x16x32_bf16 a[208:211], v[148:151], v[100:103], a[208:211]\n" \
"ds_read_b128 v[92:95], v48 offset:2048\n" \
"ds_read_b128 v[96:99], v49 offset:2048\n" \
"v_mfma_f32_16x16x32_bf16 a[212:215], v[156:159], v[100:103], a[212:215]\n" \
"s_add_u32 m0, %[mA], 0x9000\n" \
"s_mov_b32 s84, 0x9180\n" \
"v_mfma_f32_16x16x32_bf16 a[212:215], v[160:163], v[104:107], a[212:215]\n" \
"buffer_load_dwordx4 v24, s[76:79], s84 offen lds\n" \
"v_mfma_f32_16x16x32_bf16 a[216:219], v[168:171], v[104:107], a[216:219]\n" \
"v_mfma_f32_16x16x32_bf16 a[216:219], v[164:167], v[100:103], a[216:219]\n" \
"s_add_u32 m0, %[mA], 0x9400\n" \
"s_mov_b32 s85, 0xb580\n" \
"v_mfma_f32_16x16x32_bf16 a[220:223], v[172:175], v[100:103], a[220:223]\n" \
"buffer_load_dwordx4 v25, s[76:79], s85 offen lds\n" \
"v_mfma_f32_16x16x32_bf16 a[220:223], v[176:179], v[104:107], a[220:223]\n" \
"ds_read_b128 v[100:103], v48 offset:4096\n" \
"ds_read_b128 v[104:107], v49 offset:4096\n" \
"v_mfma_f32_16x16x32_bf16 a[240:243], v[152:155], v[112:115], a[240:243]\n" \
"ds_read_b64_tr_b16 v[152:153], v32 offset:8192\n" \
"ds_read_b64_tr_b16 v[154:155], v32 offset:9216\n" \
"v_mfma_f32_16x16x32_bf16 a[240:243], v[148:151], v[108:111], a[240:243]\n" \
"s_add_u32 m0, %[mB], 0x8800\n" \
"s_mov_b32 s84, 0x190000\n" \
"v_mfma_f32_16x16x32_bf16 a[244:247], v[156:159], v[108:111], a[244:247]\n" \
"buffer_load_dwordx4 v27, s[68:71], s84 offen lds\n" \
"v_mfma_f32_16x16x32_bf16 a[244:247], v[160:163], v[112:115], a[244:247]\n" \
"ds_read_b64_tr_b16 v[148:149], v32 offset:0\n" \
"ds_read_b64_tr_b16 v[150:151], v32 offset:1024\n" \
"v_mfma_f32_16x16x32_bf16 a[248:251], v[168:171], v[112:115], a[248:251]\n" \
"ds_read_b64_tr_b16 v[160:161], v33 offset:8192\n" \
"ds_read_b64_tr_b16 v[162:163], v33 offset:9216\n" \
"v_mfma_f32_16x16x32_bf16 a[248:251], v[164:167], v[108:111], a[248:251]\n" \
"ds_read_b64_tr_b16 v[156:157], v33 offset:0\n" \
"ds_read_b64_tr_b16 v[158:159], v33 offset:1024\n" \
"v_mfma_f32_16x16x32_bf16 a[252:255], v[172:175], v[108:111], a[252:255]\n" \
"ds_read_b64_tr_b16 v[164:165], v34 offset:0\n" \
"ds_read_b64_tr_b16 v[166:167], v34 offset:1024\n" \
"v_mfma_f32_16x16x32_bf16 a[252:255], v[176:179], v[112:115], a[252:255]\n" \
"s_add_u32 m0, %[mA], 0x9800\n" \
"s_waitcnt lgkmcnt(12)\n" \
"s_nop 0\n" \
"v_mfma_f32_16x16x32_bf16 a[16:19], v[144:147], v[88:91], a[16:19]\n" \
"s_mov_b32 s84, 0xd980\n" \
"ds_read_b64_tr_b16 v[168:169], v34 offset:8192\n" \
"v_mfma_f32_16x16x32_bf16 a[16:19], v[140:143], v[84:87], a[16:19]\n" \
"buffer_load_dwordx4 v24, s[76:79], s84 offen lds\n" \
"v_mfma_f32_16x16x32_bf16 a[48:51], v[124:127], v[84:87], a[48:51]\n" \
"s_add_u32 m0, %[mA], 0x9c00\n" \
"s_mov_b32 s85, 0xfd80\n" \
"v_mfma_f32_16x16x32_bf16 a[48:51], v[128:131], v[88:91], a[48:51]\n" \
"ds_read_b64_tr_b16 v[170:171], v34 offset:9216\n" \
"ds_read_b64_tr_b16 v[176:177], v35 offset:8192\n" \
"v_mfma_f32_16x16x32_bf16 a[80:83], v[88:91], v[136:139], a[80:83]\n" \
"buffer_load_dwordx4 v25, s[76:79], s85 offen lds\n" \
"v_mfma_f32_16x16x32_bf16 a[80:83], v[84:87], v[132:135], a[80:83]\n" \
"s_add_u32 m0, %[mB], 0x8c00\n" \
"s_mov_b32 s84, 0x198000\n" \
"v_mfma_f32_16x16x32_bf16 a[112:115], v[140:143], v[52:55], a[112:115]\n" \
"ds_read_b64_tr_b16 v[178:179], v35 offset:9216\n" \
"ds_read_b64_tr_b16 v[172:173], v35 offset:0\n" \
"v_mfma_f32_16x16x32_bf16 a[112:115], v[144:147], v[56:59], a[112:115]\n" \
"buffer_load_dwordx4 v27, s[68:71], s84 offen lds\n" \
"v_mfma_f32_16x16x32_bf16 a[20:23], v[128:131], v[56:59], a[20:23]\n" \
"ds_read_b64_tr_b16 v[174:175], v35 offset:1024\n" \
"ds_read_b128 v[84:87], v48 offset:0\n" \
"v_mfma_f32_16x16x32_bf16 a[20:23], v[124:127], v[52:55], a[20:23]\n" \
"s_add_u32 s89, s47, 0x2000\n" \
"ds_read_b128 v[88:91], v49 offset:0\n" \
"v_mfma_f32_16x16x32_bf16 a[52:55], v[116:119], v[52:55], a[52:55]\n" \
"buffer_store_dwordx4 v[212:215], v20, s[80:83], s89 offen offset:3072 sc1\n" \
"v_mfma_f32_16x16x32_bf16 a[52:55], v[120:123], v[56:59], a[52:55]\n" \
"ds_read_b128 v[108:111], v48 offset:6144\n" \
"ds_read_b128 v[112:115], v49 offset:6144\n" \
"v_mfma_f32_16x16x32_bf16 a[84:87], v[56:59], v[136:139], a[84:87]\n" \
"s_add_u32 s94, %[b], %[w]\n" \
"s_and_b32 s96, 7, %[b]\n" \
"v_mfma_f32_16x16x32_bf16 a[84:87], v[52:55], v[132:135], a[84:87]\n" \
"s_cmp_lg_u32 s94, 0\n" \
"s_cselect_b32 s95, 0x40000000, 0x5bc0000\n" \
"v_mfma_f32_16x16x32_bf16 a[116:119], v[140:143], v[60:63], a[116:119]\n" \
"s_mul_i32 s97, 0x48, s96\n" \
"s_add_u32 s98, 0x47, s97\n" \
"v_mfma_f32_16x16x32_bf16 a[116:119], v[144:147], v[64:67], a[116:119]\n" \
"v_writelane_b32 v11, s95, 51\n" \
"s_lshr_b32 s94, %[b], 3\n" \
"v_mfma_f32_16x16x32_bf16 a[24:27], v[128:131], v[64:67], a[24:27]\n" \
"s_cmp_lt_u32 %[w], 3\n" \
"s_cselect_b32 s99, s98, 0xfff\n" \
"v_mfma_f32_16x16x32_bf16 a[24:27], v[124:127], v[60:63], a[24:27]\n" \
"s_lshl_b32 s67, s96, 2\n" \
"s_and_b32 s95, 3, s94\n" \
"v_mfma_f32_16x16x32_bf16 a[56:59], v[116:119], v[60:63], a[56:59]\n" \
"s_mul_i32 s100, 0x480, s99\n" \
"s_lshl_b32 s64, %[w], 5\n" \
"v_mfma_f32_16x16x32_bf16 a[56:59], v[120:123], v[64:67], a[56:59]\n" \
"s_add_u32 s88, s45, 0x0\n" \
"s_add_u32 s98, s67, s95\n" \
"v_mfma_f32_16x16x32_bf16 a[88:91], v[64:67], v[136:139], a[88:91]\n" \
"buffer_store_dwordx4 v[216:219], v20, s[80:83], s88 offen offset:0 sc1\n" \
"v_mfma_f32_16x16x32_bf16 a[88:91], v[60:63], v[132:135], a[88:91]\n" \
"s_add_u32 s101, 0x120, s100\n" \
"s_add_u32 s65, s97, s64\n" \
"v_mfma_f32_16x16x32_bf16 a[120:123], v[140:143], v[68:71], a[120:123]\n" \
"s_mul_i32 s99, 0x24000, s98\n" \
"v_writelane_b32 v11, s101, 16\n" \
"v_mfma_f32_16x16x32_bf16 a[120:123], v[144:147], v[72:75], a[120:123]\n" \
"s_mul_i32 s66, 0x480, s65\n" \
"s_add_u32 s100, 0x1b000, s99\n" \
"v_mfma_f32_16x16x32_bf16 a[28:31], v[128:131], v[72:75], a[28:31]\n" \
"s_cmp_lt_u32 %[w], 3\n" \
"s_cselect_b32 s101, s66, s100\n" \
"v_mfma_f32_16x16x32_bf16 a[28:31], v[124:127], v[68:71], a[28:31]\n" \
"s_min_u32 s97, 2, %[w]\n" \
"s_mul_i32 s64, 0x2400, s97\n" \
"v_mfma_f32_16x16x32_bf16 a[60:63], v[116:119], v[68:71], a[60:63]\n" \
"v_writelane_b32 v11, s101, 17\n" \
"s_add_u32 s65, 0x20000, s64\n" \
"v_mfma_f32_16x16x32_bf16 a[60:63], v[120:123], v[72:75], a[60:63]\n" \
"s_mul_i32 s94, 0x9000, s97\n" \
"s_mul_i32 s95, 0x2400, %[w]\n" \
"v_mfma_f32_16x16x32_bf16 a[92:95], v[72:75], v[136:139], a[92:95]\n" \
"v_writelane_b32 v11, s65, 18\n" \
"s_add_u32 s67, s99, s94\n" \
"v_mfma_f32_16x16x32_bf16 a[92:95], v[68:71], v[132:135], a[92:95]\n" \
"s_add_u32 s89, s45, 0x1000\n" \
"v_writelane_b32 v11, s67, 23\n" \
"v_mfma_f32_16x16x32_bf16 a[124:127], v[140:143], v[76:79], a[124:127]\n" \
"buffer_store_dwordx4 v[220:223], v20, s[80:83], s89 offen offset:0 sc1\n" \
"v_mfma_f32_16x16x32_bf16 a[124:127], v[144:147], v[80:83], a[124:127]\n" \
"s_cmp_lt_u32 %[w], 3\n" \
"s_cselect_b32 s98, s95, 0x12000\n" \
"v_mfma_f32_16x16x32_bf16 a[0:3], v[128:131], v[80:83], a[0:3]\n" \
"s_add_u32 s66, 0x20000, s95\n" \
"v_writelane_b32 v11, s98, 19\n" \
"v_mfma_f32_16x16x32_bf16 a[0:3], v[124:127], v[76:79], a[0:3]\n" \
"s_cmp_lt_u32 %[w], 3\n" \
"s_cselect_b32 s100, s66, 0xfc00\n" \
"v_mfma_f32_16x16x32_bf16 a[4:7], v[116:119], v[76:79], a[4:7]\n" \
"v_writelane_b32 v11, s95, 20\n" \
"s_lshl_b32 s101, %[w], 10\n" \
"v_mfma_f32_16x16x32_bf16 a[4:7], v[120:123], v[80:83], a[4:7]\n" \
"s_lshr_b32 s65, s96, 1\n" \
"v_writelane_b32 v11, s100, 21\n" \
"v_mfma_f32_16x16x32_bf16 a[32:35], v[80:83], v[136:139], a[32:35]\n" \
"s_cmp_eq_u32 %[w], s65\n" \
"s_cselect_b32 s97, 0xff0000, 0\n" \
"v_mfma_f32_16x16x32_bf16 a[32:35], v[76:79], v[132:135], a[32:35]\n" \
"s_add_u32 s64, 0x1d400, s101\n" \
"s_waitcnt lgkmcnt(0)\n" \
"s_nop 0\n" \
"v_mfma_f32_16x16x32_bf16 a[128:131], v[148:151], v[52:55], a[128:131]\n" \
"v_writelane_b32 v11, s64, 22\n" \
"v_writelane_b32 v11, s97, 7\n" \
"v_mfma_f32_16x16x32_bf16 a[128:131], v[152:155], v[56:59], a[128:131]\n" \
"v_mfma_f32_16x16x32_bf16 a[132:135], v[160:163], v[56:59], a[132:135]\n" \
"v_mfma_f32_16x16x32_bf16 a[132:135], v[156:159], v[52:55], a[132:135]\n" \
"v_mfma_f32_16x16x32_bf16 a[136:139], v[164:167], v[52:55], a[136:139]\n" \
"v_mfma_f32_16x16x32_bf16 a[136:139], v[168:171], v[56:59], a[136:139]\n" \
"v_mfma_f32_16x16x32_bf16 a[140:143], v[176:179], v[56:59], a[140:143]\n" \
"v_mfma_f32_16x16x32_bf16 a[140:143], v[172:175], v[52:55], a[140:143]\n" \
"v_mfma_f32_16x16x32_bf16 a[160:163], v[148:151], v[60:63], a[160:163]\n" \
"v_mfma_f32_16x16x32_bf16 a[160:163], v[152:155], v[64:67], a[160:163]\n" \
"v_mfma_f32_16x16x32_bf16 a[164:167], v[160:163], v[64:67], a[164:167]\n" \
"v_mfma_f32_16x16x32_bf16 a[164:167], v[156:159], v[60:63], a[164:167]\n" \
"v_mfma_f32_16x16x32_bf16 a[168:171], v[164:167], v[60:63], a[168:171]\n" \
"v_mfma_f32_16x16x32_bf16 a[168:171], v[168:171], v[64:67], a[168:171]\n" \
"v_mfma_f32_16x16x32_bf16 a[172:175], v[176:179], v[64:67], a[172:175]\n" \
"v_mfma_f32_16x16x32_bf16 a[172:175], v[172:175], v[60:63], a[172:175]\n" \
"v_mfma_f32_16x16x32_bf16 a[192:195], v[148:151], v[68:71], a[192:195]\n" \
"v_mfma_f32_16x16x32_bf16 a[192:195], v[152:155], v[72:75], a[192:195]\n" \
"s_waitcnt vmcnt(3)\n" \
"s_barrier\n" \
"v_mfma_f32_16x16x32_bf16 a[196:199], v[160:163], v[72:75], a[196:199]\n" \
"s_add_u32 m0, s43, 0x0\n" \
"s_nop 0\n" \
"s_add_u32 s85, s38, 0x8000\n" \
"v_mfma_f32_16x16x32_bf16 a[196:199], v[156:159], v[68:71], a[196:199]\n" \
"buffer_load_dwordx4 v20, s[80:83], s85 offen sc1 lds\n" \
"v_mfma_f32_16x16x32_bf16 a[200:203], v[164:167], v[68:71], a[200:203]\n" \
"s_add_u32 m0, s43, 0x1000\n" \
"s_add_u32 s84, s38, 0x9000\n" \
"v_mfma_f32_16x16x32_bf16 a[200:203], v[168:171], v[72:75], a[200:203]\n" \
"ds_read_b64_tr_b16 v[140:141], v39 offset:32768\n" \
"ds_read_b64_tr_b16 v[142:143], v39 offset:33280\n" \
"v_mfma_f32_16x16x32_bf16 a[204:207], v[176:179], v[72:75], a[204:207]\n" \
"buffer_load_dwordx4 v20, s[80:83], s84 offen sc1 lds\n" \
"v_mfma_f32_16x16x32_bf16 a[204:207], v[172:175], v[68:71], a[204:207]\n" \
"ds_read_b64_tr_b16 v[144:145], v39 offset:36864\n" \
"ds_read_b64_tr_b16 v[146:147], v39 offset:37376\n" \
"v_mfma_f32_16x16x32_bf16 a[224:227], v[148:151], v[76:79], a[224:227]\n" \
"s_add_u32 m0, s43, 0x3000\n" \
"s_mov_b32 s85, 0x200\n" \
"v_mfma_f32_16x16x32_bf16 a[224:227], v[152:155], v[80:83], a[224:227]\n" \
"buffer_load_dwordx4 v21, s[76:79], s85 offen lds\n" \
"v_mfma_f32_16x16x32_bf16 a[228:231], v[160:163], v[80:83], a[228:231]\n" \
"ds_read_b64_tr_b16 v[128:129], v37 offset:36864\n" \
"ds_read_b64_tr_b16 v[130:131], v37 offset:37376\n" \
"v_mfma_f32_16x16x32_bf16 a[228:231], v[156:159], v[76:79], a[228:231]\n" \
"ds_read_b64_tr_b16 v[124:125], v37 offset:32768\n" \
"ds_read_b64_tr_b16 v[126:127], v37 offset:33280\n" \
"v_mfma_f32_16x16x32_bf16 a[232:235], v[164:167], v[76:79], a[232:235]\n" \
"s_add_u32 m0, %[mA], 0x0\n" \
"s_nop 0\n" \
"s_mov_b32 s84, 0x200\n" \
"v_mfma_f32_16x16x32_bf16 a[232:235], v[168:171], v[80:83], a[232:235]\n" \
"buffer_load_dwordx4 v24, s[76:79], s84 offen lds\n" \
"v_mfma_f32_16x16x32_bf16 a[236:239], v[176:179], v[80:83], a[236:239]\n" \
"ds_read_b64_tr_b16 v[132:133], v38 offset:32768\n" \
"ds_read_b64_tr_b16 v[134:135], v38 offset:33280\n" \
"v_mfma_f32_16x16x32_bf16 a[236:239], v[172:175], v[76:79], a[236:239]\n" \
"s_add_u32 m0, %[mA], 0x400\n" \
"s_mov_b32 s85, 0x2600\n" \
"v_mfma_f32_16x16x32_bf16 a[144:147], v[148:151], v[84:87], a[144:147]\n" \
"buffer_load_dwordx4 v25, s[76:79], s85 offen lds\n" \
"v_mfma_f32_16x16x32_bf16 a[144:147], v[152:155], v[88:91], a[144:147]\n" \
"ds_read_b64_tr_b16 v[136:137], v38 offset:36864\n" \
"ds_read_b64_tr_b16 v[138:139], v38 offset:37376\n" \
"v_mfma_f32_16x16x32_bf16 a[148:151], v[160:163], v[88:91], a[148:151]\n" \
"ds_read_b128 v[56:59], v29 offset:32768\n" \
"ds_read_b128 v[52:55], v28 offset:32768\n" \
"v_mfma_f32_16x16x32_bf16 a[148:151], v[156:159], v[84:87], a[148:151]\n" \
"s_add_u32 m0, %[mB], 0x0\n" \
"s_nop 0\n" \
"s_mov_b32 s84, 0x200000\n" \
"v_mfma_f32_16x16x32_bf16 a[152:155], v[164:167], v[84:87], a[152:155]\n" \
"buffer_load_dwordx4 v26, s[68:71], s84 offen lds\n" \
"v_mfma_f32_16x16x32_bf16 a[152:155], v[168:171], v[88:91], a[152:155]\n" \
"ds_read_b64_tr_b16 v[120:121], v36 offset:36864\n" \
"ds_read_b64_tr_b16 v[122:123], v36 offset:37376\n" \
"v_mfma_f32_16x16x32_bf16 a[156:159], v[176:179], v[88:91], a[156:159]\n" \
"ds_read_b128 v[88:91], v23 offset:32768\n" \
"ds_read_b64_tr_b16 v[116:117], v36 offset:32768\n" \
"v_mfma_f32_16x16x32_bf16 a[156:159], v[172:175], v[84:87], a[156:159]\n" \
"s_add_u32 m0, %[mA], 0x800\n" \
"s_mov_b32 s85, 0x4a00\n" \
"v_mfma_f32_16x16x32_bf16 a[176:179], v[148:151], v[92:95], a[176:179]\n" \
"buffer_load_dwordx4 v24, s[76:79], s85 offen lds\n" \
"v_mfma_f32_16x16x32_bf16 a[176:179], v[152:155], v[96:99], a[176:179]\n" \
"ds_read_b128 v[84:87], v22 offset:32768\n" \
"ds_read_b64_tr_b16 v[118:119], v36 offset:33280\n" \
"v_mfma_f32_16x16x32_bf16 a[180:183], v[160:163], v[96:99], a[180:183]\n" \
"s_add_u32 m0, %[mA], 0xc00\n" \
"s_mov_b32 s84, 0x6e00\n" \
"L_pf6:\n" \
"s_cbranch_execz L_pf10\n" \
"s_nop 0\n" \
"v_mfma_f32_16x16x32_bf16 a[180:183], v[156:159], v[92:95], a[180:183]\n" \
"buffer_load_dwordx4 v25, s[76:79], s84 offen lds\n" \
"v_mfma_f32_16x16x32_bf16 a[184:187], v[164:167], v[92:95], a[184:187]\n" \
"ds_read_b128 v[64:67], v29 offset:34816\n" \
"ds_read_b128 v[60:63], v28 offset:34816\n" \
"v_mfma_f32_16x16x32_bf16 a[184:187], v[168:171], v[96:99], a[184:187]\n" \
"ds_read_b128 v[72:75], v29 offset:36864\n" \
"ds_read_b128 v[68:71], v28 offset:36864\n" \
"v_mfma_f32_16x16x32_bf16 a[188:191], v[176:179], v[96:99], a[188:191]\n" \
"s_add_u32 m0, %[mB], 0x400\n" \
"s_mov_b32 s85, 0x208000\n" \
"v_mfma_f32_16x16x32_bf16 a[188:191], v[172:175], v[92:95], a[188:191]\n" \
"buffer_load_dwordx4 v26, s[68:71], s85 offen lds\n" \
"v_mfma_f32_16x16x32_bf16 a[208:211], v[148:151], v[100:103], a[208:211]\n" \
"ds_read_b128 v[80:83], v29 offset:38912\n" \
"ds_read_b128 v[76:79], v28 offset:38912\n" \
"v_mfma_f32_16x16x32_bf16 a[208:211], v[152:155], v[104:107], a[208:211]\n" \
"ds_read_b128 v[92:95], v48 offset:34816\n" \
"ds_read_b128 v[96:99], v49 offset:34816\n" \
"v_mfma_f32_16x16x32_bf16 a[212:215], v[160:163], v[104:107], a[212:215]\n" \
"s_add_u32 m0, %[mA], 0x1000\n" \
"s_mov_b32 s84, 0x9200\n" \
"v_mfma_f32_16x16x32_bf16 a[212:215], v[156:159], v[100:103], a[212:215]\n" \
"buffer_load_dwordx4 v24, s[76:79], s84 offen lds\n" \
"v_mfma_f32_16x16x32_bf16 a[216:219], v[164:167], v[100:103], a[216:219]\n" \
"v_mfma_f32_16x16x32_bf16 a[216:219], v[168:171], v[104:107], a[216:219]\n" \
"s_add_u32 m0, %[mA], 0x1400\n" \
"s_mov_b32 s85, 0xb600\n" \
"v_mfma_f32_16x16x32_bf16 a[220:223], v[176:179], v[104:107], a[220:223]\n" \
"buffer_load_dwordx4 v25, s[76:79], s85 offen lds\n" \
"v_mfma_f32_16x16x32_bf16 a[220:223], v[172:175], v[100:103], a[220:223]\n" \
"ds_read_b128 v[100:103], v48 offset:36864\n" \
"ds_read_b128 v[104:107], v49 offset:36864\n" \
"v_mfma_f32_16x16x32_bf16 a[240:243], v[148:151], v[108:111], a[240:243]\n" \
"ds_read_b64_tr_b16 v[148:149], v32 offset:32768\n" \
"ds_read_b64_tr_b16 v[150:151], v32 offset:33792\n" \
"v_mfma_f32_16x16x32_bf16 a[240:243], v[152:155], v[112:115], a[240:243]\n" \
"s_add_u32 m0, %[mB], 0x800\n" \
"s_mov_b32 s84, 0x210000\n" \
"v_mfma_f32_16x16x32_bf16 a[244:247], v[160:163], v[112:115], a[244:247]\n" \
"buffer_load_dwordx4 v27, s[68:71], s84 offen lds\n" \
"v_mfma_f32_16x16x32_bf16 a[244:247], v[156:159], v[108:111], a[244:247]\n" \
"ds_read_b64_tr_b16 v[152:153], v32 offset:40960\n" \
"ds_read_b64_tr_b16 v[154:155], v32 offset:41984\n" \
"v_mfma_f32_16x16x32_bf16 a[248:251], v[164:167], v[108:111], a[248:251]\n" \
"ds_read_b64_tr_b16 v[156:157], v33 offset:32768\n" \
"ds_read_b64_tr_b16 v[158:159], v33 offset:33792\n" \
"v_mfma_f32_16x16x32_bf16 a[248:251], v[168:171], v[112:115], a[248:251]\n" \
"ds_read_b64_tr_b16 v[160:161], v33 offset:40960\n" \
"ds_read_b64_tr_b16 v[162:163], v33 offset:41984\n" \
"v_mfma_f32_16x16x32_bf16 a[252:255], v[176:179], v[112:115], a[252:255]\n" \
"ds_read_b64_tr_b16 v[168:169], v34 offset:40960\n" \
"ds_read_b64_tr_b16 v[170:171], v34 offset:41984\n" \
"v_mfma_f32_16x16x32_bf16 a[252:255], v[172:175], v[108:111], a[252:255]\n" \
"s_add_u32 m0, %[mA], 0x1800\n" \
"s_waitcnt lgkmcnt(12)\n" \
"s_nop 0\n" \
"v_mfma_f32_16x16x32_bf16 a[16:19], v[140:143], v[84:87], a[16:19]\n" \
"s_mov_b32 s84, 0xda00\n" \
"ds_read_b64_tr_b16 v[164:165], v34 offset:32768\n" \
"v_mfma_f32_16x16x32_bf16 a[16:19], v[144:147], v[88:91], a[16:19]\n" \
"buffer_load_dwordx4 v24, s[76:79], s84 offen lds\n" \
"v_mfma_f32_16x16x32_bf16 a[48:51], v[128:131], v[88:91], a[48:51]\n" \
"s_add_u32 m0, %[mA], 0x1c00\n" \
"s_mov_b32 s85, 0xfe00\n" \
"v_mfma_f32_16x16x32_bf16 a[48:51], v[124:127], v[84:87], a[48:51]\n" \
"ds_read_b64_tr_b16 v[166:167], v34 offset:33792\n" \
"ds_read_b64_tr_b16 v[172:173], v35 offset:32768\n" \
"v_mfma_f32_16x16x32_bf16 a[80:83], v[84:87], v[132:135], a[80:83]\n" \
"buffer_load_dwordx4 v25, s[76:79], s85 offen lds\n" \
"v_mfma_f32_16x16x32_bf16 a[80:83], v[88:91], v[136:139], a[80:83]\n" \
"s_add_u32 m0, %[mB], 0xc00\n" \
"s_mov_b32 s84, 0x218000\n" \
"v_mfma_f32_16x16x32_bf16 a[112:115], v[144:147], v[56:59], a[112:115]\n" \
"ds_read_b64_tr_b16 v[174:175], v35 offset:33792\n" \
"ds_read_b64_tr_b16 v[176:177], v35 offset:40960\n" \
"v_mfma_f32_16x16x32_bf16 a[112:115], v[140:143], v[52:55], a[112:115]\n" \
"buffer_load_dwordx4 v27, s[68:71], s84 offen lds\n" \
"v_mfma_f32_16x16x32_bf16 a[20:23], v[124:127], v[52:55], a[20:23]\n" \
"ds_read_b64_tr_b16 v[178:179], v35 offset:41984\n" \
"ds_read_b128 v[84:87], v48 offset:32768\n" \
"v_mfma_f32_16x16x32_bf16 a[20:23], v[128:131], v[56:59], a[20:23]\n" \
"s_add_u32 s88, s45, 0x2000\n" \
"ds_read_b128 v[88:91], v49 offset:32768\n" \
"v_mfma_f32_16x16x32_bf16 a[52:55], v[120:123], v[56:59], a[52:55]\n" \
"buffer_store_dwordx4 v[224:227], v20, s[80:83], s88 offen offset:0 sc1\n" \
"v_mfma_f32_16x16x32_bf16 a[52:55], v[116:119], v[52:55], a[52:55]\n" \
"ds_read_b128 v[108:111], v48 offset:38912\n" \
"ds_read_b128 v[112:115], v49 offset:38912\n" \
"v_mfma_f32_16x16x32_bf16 a[84:87], v[52:55], v[132:135], a[84:87]\n" \
"s_lshr_b32 s96, %[b], 3\n" \
"s_and_b32 s97, 3, s96\n" \
"v_mfma_f32_16x16x32_bf16 a[84:87], v[56:59], v[136:139], a[84:87]\n" \
"s_and_b32 s94, 7, %[b]\n" \
"s_add_u32 s98, 1, s97\n" \
"v_mfma_f32_16x16x32_bf16 a[116:119], v[144:147], v[64:67], a[116:119]\n" \
"s_lshl_b32 s95, s94, 2\n" \
"s_and_b32 s99, 3, s98\n" \
"v_mfma_f32_16x16x32_bf16 a[116:119], v[140:143], v[60:63], a[116:119]\n" \
"s_add_u32 s100, s95, s99\n" \
"s_mul_i32 s101, 0x24000, s100\n" \
"v_mfma_f32_16x16x32_bf16 a[24:27], v[124:127], v[60:63], a[24:27]\n" \
"s_mul_i32 s64, 0x9000, %[w]\n" \
"s_and_b32 s96, 2, %[w]\n" \
"v_mfma_f32_16x16x32_bf16 a[24:27], v[128:131], v[64:67], a[24:27]\n" \
"s_add_u32 s65, s101, s64\n" \
"s_lshr_b32 s66, %[b], 5\n" \
"v_mfma_f32_16x16x32_bf16 a[56:59], v[120:123], v[64:67], a[56:59]\n" \
"s_lshl_b32 s98, s96, 1\n" \
"s_and_b32 s99, 1, %[w]\n" \
"v_mfma_f32_16x16x32_bf16 a[56:59], v[116:119], v[60:63], a[56:59]\n" \
"s_add_u32 s89, s45, 0x3000\n" \
"v_writelane_b32 v11, s65, 24\n" \
"v_mfma_f32_16x16x32_bf16 a[88:91], v[60:63], v[132:135], a[88:91]\n" \
"buffer_store_dwordx4 v[228:231], v20, s[80:83], s89 offen offset:0 sc1\n" \
"v_mfma_f32_16x16x32_bf16 a[88:91], v[64:67], v[136:139], a[88:91]\n" \
"s_lshl_b32 s67, s66, 3\n" \
"s_add_u32 s101, s98, s99\n" \
"v_mfma_f32_16x16x32_bf16 a[120:123], v[144:147], v[72:75], a[120:123]\n" \
"s_add_u32 s65, s67, s101\n" \
"s_mul_i32 s66, 0x24800, s65\n" \
"v_mfma_f32_16x16x32_bf16 a[120:123], v[140:143], v[68:71], a[120:123]\n" \
"s_lshl_b32 s98, s100, 12\n" \
"s_add_u32 s96, 0x3000000, s66\n" \
"v_mfma_f32_16x16x32_bf16 a[28:31], v[124:127], v[68:71], a[28:31]\n" \
"s_add_u32 s99, 0x4800, s98\n" \
"s_add_u32 s65, s96, s99\n" \
"v_mfma_f32_16x16x32_bf16 a[28:31], v[128:131], v[72:75], a[28:31]\n" \
"s_sub_u32 s100, 0x78, s67\n" \
"v_writelane_b32 v11, s65, 27\n" \
"v_mfma_f32_16x16x32_bf16 a[60:63], v[120:123], v[72:75], a[60:63]\n" \
"s_add_u32 s98, s100, s101\n" \
"s_mul_i32 s65, 0x24800, s98\n" \
"v_mfma_f32_16x16x32_bf16 a[60:63], v[116:119], v[68:71], a[60:63]\n" \
"s_add_u32 s67, 0x3000000, s65\n" \
"s_add_u32 s101, 2, s97\n" \
"v_mfma_f32_16x16x32_bf16 a[92:95], v[68:71], v[132:135], a[92:95]\n" \
"s_add_u32 s100, s67, s99\n" \
"s_and_b32 s98, 3, s101\n" \
"v_mfma_f32_16x16x32_bf16 a[92:95], v[72:75], v[136:139], a[92:95]\n" \
"s_add_u32 s88, s45, 0x0\n" \
"v_writelane_b32 v11, s100, 30\n" \
"v_mfma_f32_16x16x32_bf16 a[124:127], v[144:147], v[80:83], a[124:127]\n" \
"buffer_store_dwordx4 v[232:235], v20, s[80:83], s88 offen offset:1024 sc1\n" \
"v_mfma_f32_16x16x32_bf16 a[124:127], v[140:143], v[76:79], a[124:127]\n" \
"s_add_u32 s99, s95, s98\n" \
"s_mul_i32 s100, 0x24000, s99\n" \
"v_mfma_f32_16x16x32_bf16 a[0:3], v[124:127], v[76:79], a[0:3]\n" \
"s_add_u32 s101, s100, s64\n" \
"s_lshl_b32 s98, s99, 12\n" \
"v_mfma_f32_16x16x32_bf16 a[0:3], v[128:131], v[80:83], a[0:3]\n" \
"v_writelane_b32 v11, s101, 25\n" \
"s_add_u32 s100, 0x4800, s98\n" \
"v_mfma_f32_16x16x32_bf16 a[4:7], v[120:123], v[80:83], a[4:7]\n" \
"s_add_u32 s101, s96, s100\n" \
"v_writelane_b32 v11, s101, 28\n" \
"v_mfma_f32_16x16x32_bf16 a[4:7], v[116:119], v[76:79], a[4:7]\n" \
"s_add_u32 s98, 3, s97\n" \
"s_add_u32 s99, s67, s100\n" \
"v_mfma_f32_16x16x32_bf16 a[32:35], v[76:79], v[132:135], a[32:35]\n" \
"s_and_b32 s101, 3, s98\n" \
"v_writelane_b32 v11, s99, 31\n" \
"v_mfma_f32_16x16x32_bf16 a[32:35], v[80:83], v[136:139], a[32:35]\n" \
"s_add_u32 s100, s95, s101\n" \
"s_waitcnt lgkmcnt(0)\n" \
"v_mfma_f32_16x16x32_bf16 a[128:131], v[152:155], v[56:59], a[128:131]\n" \
"s_mul_i32 s99, 0x24000, s100\n" \
"s_lshl_b32 s98, s100, 12\n" \
"v_mfma_f32_16x16x32_bf16 a[128:131], v[148:151], v[52:55], a[128:131]\n" \
"s_add_u32 s97, s99, s64\n" \
"s_add_u32 s95, 0x4800, s98\n" \
"v_mfma_f32_16x16x32_bf16 a[132:135], v[156:159], v[52:55], a[132:135]\n" \
"v_writelane_b32 v11, s97, 26\n" \
"s_add_u32 s101, s96, s95\n" \
"v_mfma_f32_16x16x32_bf16 a[132:135], v[160:163], v[56:59], a[132:135]\n" \
"v_writelane_b32 v11, s101, 29\n" \
"s_add_u32 s99, s67, s95\n" \
"v_mfma_f32_16x16x32_bf16 a[136:139], v[168:171], v[56:59], a[136:139]\n" \
"s_lshl_b32 s64, s94, 11\n" \
"v_writelane_b32 v11, s99, 32\n" \
"v_mfma_f32_16x16x32_bf16 a[136:139], v[164:167], v[52:55], a[136:139]\n" \
"s_add_u32 s97, s96, s64\n" \
"v_writelane_b32 v11, s97, 33\n" \
"v_mfma_f32_16x16x32_bf16 a[140:143], v[172:175], v[52:55], a[140:143]\n" \
"s_and_b32 s99, 1, s94\n" \
"s_add_u32 s100, s67, s64\n" \
"v_mfma_f32_16x16x32_bf16 a[140:143], v[176:179], v[56:59], a[140:143]\n" \
"s_cmp_lg_u32 s99, 0\n" \
"s_cselect_b32 s97, s67, s96\n" \
"v_mfma_f32_16x16x32_bf16 a[160:163], v[152:155], v[64:67], a[160:163]\n" \
"v_writelane_b32 v11, s100, 34\n" \
"s_add_u32 s98, 0x3004000, s66\n" \
"v_mfma_f32_16x16x32_bf16 a[160:163], v[148:151], v[60:63], a[160:163]\n" \
"s_lshr_b32 s95, s94, 1\n" \
"s_add_u32 s64, 0x4000, s97\n" \
"v_mfma_f32_16x16x32_bf16 a[164:167], v[156:159], v[60:63], a[164:167]\n" \
"v_writelane_b32 v11, s98, 35\n" \
"s_add_u32 s101, 0x3004000, s65\n" \
"v_mfma_f32_16x16x32_bf16 a[164:167], v[160:163], v[64:67], a[164:167]\n" \
"s_cmp_eq_u32 %[w], s95\n" \
"s_cselect_b32 s100, s64, 0x40000000\n" \
"v_mfma_f32_16x16x32_bf16 a[168:171], v[168:171], v[64:67], a[168:171]\n" \
"v_writelane_b32 v11, s101, 36\n" \
"v_writelane_b32 v11, s100, 37\n" \
"v_mfma_f32_16x16x32_bf16 a[168:171], v[164:167], v[60:63], a[168:171]\n" \
"v_mfma_f32_16x16x32_bf16 a[172:175], v[172:175], v[60:63], a[172:175]\n" \
"v_mfma_f32_16x16x32_bf16 a[172:175], v[176:179], v[64:67], a[172:175]\n" \
"v_mfma_f32_16x16x32_bf16 a[192:195], v[152:155], v[72:75], a[192:195]\n" \
"v_mfma_f32_16x16x32_bf16 a[192:195], v[148:151], v[68:71], a[192:195]\n" \
"s_waitcnt vmcnt(3)\n" \
"s_barrier\n" \
"v_mfma_f32_16x16x32_bf16 a[196:199], v[156:159], v[68:71], a[196:199]\n" \
"s_add_u32 m0, s43, 0x8000\n" \
"s_add_u32 s85, s38, 0xa000\n" \
"v_mfma_f32_16x16x32_bf16 a[196:199], v[160:163], v[72:75], a[196:199]\n" \
"buffer_load_dwordx4 v20, s[80:83], s85 offen sc1 lds\n" \
"v_mfma_f32_16x16x32_bf16 a[200:203], v[168:171], v[72:75], a[200:203]\n" \
"s_add_u32 m0, s43, 0x9000\n" \
"s_add_u32 s84, s38, 0xb000\n" \
"v_mfma_f32_16x16x32_bf16 a[200:203], v[164:167], v[68:71], a[200:203]\n" \
"ds_read_b64_tr_b16 v[144:145], v39 offset:4096\n" \
"ds_read_b64_tr_b16 v[146:147], v39 offset:4608\n" \
"v_mfma_f32_16x16x32_bf16 a[204:207], v[172:175], v[68:71], a[204:207]\n" \
"buffer_load_dwordx4 v20, s[80:83], s84 offen sc1 lds\n" \
"v_mfma_f32_16x16x32_bf16 a[204:207], v[176:179], v[72:75], a[204:207]\n" \
"ds_read_b64_tr_b16 v[140:141], v39 offset:0\n" \
"ds_read_b64_tr_b16 v[142:143], v39 offset:512\n" \
"v_mfma_f32_16x16x32_bf16 a[224:227], v[152:155], v[80:83], a[224:227]\n" \
"s_add_u32 m0, s43, 0xb000\n" \
"s_mov_b32 s85, 0x280\n" \
"v_mfma_f32_16x16x32_bf16 a[224:227], v[148:151], v[76:79], a[224:227]\n" \
"buffer_load_dwordx4 v21, s[76:79], s85 offen lds\n" \
"v_mfma_f32_16x16x32_bf16 a[228:231], v[156:159], v[76:79], a[228:231]\n" \
"ds_read_b64_tr_b16 v[124:125], v37 offset:0\n" \
"ds_read_b64_tr_b16 v[126:127], v37 offset:512\n" \
"v_mfma_f32_16x16x32_bf16 a[228:231], v[160:163], v[80:83], a[228:231]\n" \
"ds_read_b64_tr_b16 v[128:129], v37 offset:4096\n" \
"ds_read_b64_tr_b16 v[130:131], v37 offset:4608\n" \
"v_mfma_f32_16x16x32_bf16 a[232:235], v[168:171], v[80:83], a[232:235]\n" \
"s_add_u32 m0, %[mA], 0x8000\n" \
"s_mov_b32 s84, 0x280\n" \
"v_mfma_f32_16x16x32_bf16 a[232:235], v[164:167], v[76:79], a[232:235]\n" \
"buffer_load_dwordx4 v24, s[76:79], s84 offen lds\n" \
"v_mfma_f32_16x16x32_bf16 a[236:239], v[172:175], v[76:79], a[236:239]\n" \
"ds_read_b64_tr_b16 v[136:137], v38 offset:4096\n" \
"ds_read_b64_tr_b16 v[138:139], v38 offset:4608\n" \
"v_mfma_f32_16x16x32_bf16 a[236:239], v[176:179], v[80:83], a[236:239]\n" \
"s_add_u32 m0, %[mA], 0x8400\n" \
"s_mov_b32 s85, 0x2680\n" \
"v_mfma_f32_16x16x32_bf16 a[144:147], v[152:155], v[88:91], a[144:147]\n" \
"buffer_load_dwordx4 v25, s[76:79], s85 offen lds\n" \
"v_mfma_f32_16x16x32_bf16 a[144:147], v[148:151], v[84:87], a[144:147]\n" \
"ds_read_b64_tr_b16 v[132:133], v38 offset:0\n" \
"ds_read_b64_tr_b16 v[134:135], v38 offset:512\n" \
"v_mfma_f32_16x16x32_bf16 a[148:151], v[156:159], v[84:87], a[148:151]\n" \
"ds_read_b128 v[52:55], v28 offset:0\n" \
"ds_read_b128 v[56:59], v29 offset:0\n" \
"v_mfma_f32_16x16x32_bf16 a[148:151], v[160:163], v[88:91], a[148:151]\n" \
"s_add_u32 m0, %[mB], 0x8000\n" \
"s_mov_b32 s84, 0x280000\n" \
"v_mfma_f32_16x16x32_bf16 a[152:155], v[168:171], v[88:91], a[152:155]\n" \
"buffer_load_dwordx4 v26, s[68:71], s84 offen lds\n" \
"v_mfma_f32_16x16x32_bf16 a[152:155], v[164:167], v[84:87], a[152:155]\n" \
"ds_read_b64_tr_b16 v[116:117], v36 offset:0\n" \
"ds_read_b64_tr_b16 v[118:119], v36 offset:512\n" \
"v_mfma_f32_16x16x32_bf16 a[156:159], v[172:175], v[84:87], a[156:159]\n" \
"ds_read_b128 v[84:87], v22 offset:0\n" \
"ds_read_b64_tr_b16 v[120:121], v36 offset:4096\n" \
"v_mfma_f32_16x16x32_bf16 a[156:159], v[176:179], v[88:91], a[156:159]\n" \
"s_add_u32 m0, %[mA], 0x8800\n" \
"s_mov_b32 s85, 0x4a80\n" \
"v_mfma_f32_16x16x32_bf16 a[176:179], v[152:155], v[96:99], a[176:179]\n" \
"buffer_load_dwordx4 v24, s[76:79], s85 offen lds\n" \
"v_mfma_f32_16x16x32_bf16 a[176:179], v[148:151], v[92:95], a[176:179]\n" \
"ds_read_b128 v[88:91], v23 offset:0\n" \
"ds_read_b64_tr_b16 v[122:123], v36 offset:4608\n" \
"v_mfma_f32_16x16x32_bf16 a[180:183], v[156:159], v[92:95], a[180:183]\n" \
"s_add_u32 m0, %[mA], 0x8c00\n" \
"s_mov_b32 s84, 0x6e80\n" \
"v_mfma_f32_16x16x32_bf16 a[180:183], v[160:163], v[96:99], a[180:183]\n" \
"buffer_load_dwordx4 v25, s[76:79], s84 offen lds\n" \
"v_mfma_f32_16x16x32_bf16 a[184:187], v[168:171], v[96:99], a[184:187]\n" \
"ds_read_b128 v[60:63], v28 offset:2048\n" \
"ds_read_b128 v[64:67], v29 offset:2048\n" \
"v_mfma_f32_16x16x32_bf16 a[184:187], v[164:167], v[92:95], a[184:187]\n" \
"ds_read_b128 v[68:71], v28 offset:4096\n" \
"ds_read_b128 v[72:75], v29 offset:4096\n" \
"v_mfma_f32_16x16x32_bf16 a[188:191], v[172:175], v[92:95], a[188:191]\n" \
"s_add_u32 m0, %[mB], 0x8400\n" \
"s_mov_b32 s85, 0x288000\n" \
"v_mfma_f32_16x16x32_bf16 a[188:191], v[176:179], v[96:99], a[188:191]\n" \
"buffer_load_dwordx4 v26, s[68:71], s85 offen lds\n" \
"v_mfma_f32_16x16x32_bf16 a[208:211], v[152:155], v[104:107], a[208:211]\n" \
"ds_read_b128 v[76:79], v28 offset:6144\n" \
"ds_read_b128 v[80:83], v29 offset:6144\n" \
"v_mfma_f32_16x16x32_bf16 a[208:211], v[148:151], v[100:103], a[208:211]\n" \
"ds_read_b128 v[92:95], v48 offset:2048\n" \
"ds_read_b128 v[96:99], v49 offset:2048\n" \
"v_mfma_f32_16x16x32_bf16 a[212:215], v[156:159], v[100:103], a[212:215]\n" \
"s_add_u32 m0, %[mA], 0x9000\n" \
"s_mov_b32 s84, 0x9280\n" \
"v_mfma_f32_16x16x32_bf16 a[212:215], v[160:163], v[104:107], a[212:215]\n" \
"buffer_load_dwordx4 v24, s[76:79], s84 offen lds\n" \
"v_mfma_f32_16x16x32_bf16 a[216:219], v[168:171], v[104:107], a[216:219]\n" \
"v_mfma_f32_16x16x32_bf16 a[216:219], v[164:167], v[100:103], a[216:219]\n" \
"s_add_u32 m0, %[mA], 0x9400\n" \
"s_mov_b32 s85, 0xb680\n" \
"v_mfma_f32_16x16x32_bf16 a[220:223], v[172:175], v[100:103], a[220:223]\n" \
"buffer_load_dwordx4 v25, s[76:79], s85 offen lds\n" \
"v_mfma_f32_16x16x32_bf16 a[220:223], v[176:179], v[104:107], a[220:223]\n" \
"ds_read_b128 v[100:103], v48 offset:4096\n" \
"ds_read_b128 v[104:107], v49 offset:4096\n" \
"v_mfma_f32_16x16x32_bf16 a[240:243], v[152:155], v[112:115], a[240:243]\n" \
"ds_read_b64_tr_b16 v[152:153], v32 offset:8192\n" \
"ds_read_b64_tr_b16 v[154:155], v32 offset:9216\n" \
"v_mfma_f32_16x16x32_bf16 a[240:243], v[148:151], v[108:111], a[240:243]\n" \
"s_add_u32 m0, %[mB], 0x8800\n" \
"s_mov_b32 s84, 0x290000\n" \
"v_mfma_f32_16x16x32_bf16 a[244:247], v[156:159], v[108:111], a[244:247]\n" \
"buffer_load_dwordx4 v27, s[68:71], s84 offen lds\n" \
"v_mfma_f32_16x16x32_bf16 a[244:247], v[160:163], v[112:115], a[244:247]\n" \
"ds_read_b64_tr_b16 v[148:149], v32 offset:0\n" \
"ds_read_b64_tr_b16 v[150:151], v32 offset:1024\n" \
"v_mfma_f32_16x16x32_bf16 a[248:251], v[168:171], v[112:115], a[248:251]\n" \
"ds_read_b64_tr_b16 v[160:161], v33 offset:8192\n" \
"ds_read_b64_tr_b16 v[162:163], v33 offset:9216\n" \
"v_mfma_f32_16x16x32_bf16 a[248:251], v[164:167], v[108:111], a[248:251]\n" \
"ds_read_b64_tr_b16 v[156:157], v33 offset:0\n" \
"ds_read_b64_tr_b16 v[158:159], v33 offset:1024\n" \
"v_mfma_f32_16x16x32_bf16 a[252:255], v[172:175], v[108:111], a[252:255]\n" \
"ds_read_b64_tr_b16 v[164:165], v34 offset:0\n" \
"ds_read_b64_tr_b16 v[166:167], v34 offset:1024\n" \
"v_mfma_f32_16x16x32_bf16 a[252:255], v[176:179], v[112:115], a[252:255]\n" \
"s_add_u32 m0, %[mA], 0x9800\n" \
"s_waitcnt lgkmcnt(12)\n" \
"s_nop 0\n" \
"v_mfma_f32_16x16x32_bf16 a[16:19], v[144:147], v[88:91], a[16:19]\n" \
"s_mov_b32 s84, 0xda80\n" \
"ds_read_b64_tr_b16 v[168:169], v34 offset:8192\n" \
"v_mfma_f32_16x16x32_bf16 a[16:19], v[140:143], v[84:87], a[16:19]\n" \
"buffer_load_dwordx4 v24, s[76:79], s84 offen lds\n" \
"v_mfma_f32_16x16x32_bf16 a[48:51], v[124:127], v[84:87], a[48:51]\n" \
"s_add_u32 m0, %[mA], 0x9c00\n" \
"s_mov_b32 s85, 0xfe80\n" \
"v_mfma_f32_16x16x32_bf16 a[48:51], v[128:131], v[88:91], a[48:51]\n" \
"ds_read_b64_tr_b16 v[170:171], v34 offset:9216\n" \
"ds_read_b64_tr_b16 v[176:177], v35 offset:8192\n" \
"v_mfma_f32_16x16x32_bf16 a[80:83], v[88:91], v[136:139], a[80:83]\n" \
"buffer_load_dwordx4 v25, s[76:79], s85 offen lds\n" \
"v_mfma_f32_16x16x32_bf16 a[80:83], v[84:87], v[132:135], a[80:83]\n" \
"s_add_u32 m0, %[mB], 0x8c00\n" \
"s_mov_b32 s84, 0x298000\n" \
"v_mfma_f32_16x16x32_bf16 a[112:115], v[140:143], v[52:55], a[112:115]\n" \
"ds_read_b64_tr_b16 v[178:179], v35 offset:9216\n" \
"ds_read_b64_tr_b16 v[172:173], v35 offset:0\n" \
"v_mfma_f32_16x16x32_bf16 a[112:115], v[144:147], v[56:59], a[112:115]\n" \
"buffer_load_dwordx4 v27, s[68:71], s84 offen lds\n" \
"v_mfma_f32_16x16x32_bf16 a[20:23], v[128:131], v[56:59], a[20:23]\n" \
"ds_read_b64_tr_b16 v[174:175], v35 offset:1024\n" \
"ds_read_b128 v[84:87], v48 offset:0\n" \
"v_mfma_f32_16x16x32_bf16 a[20:23], v[124:127], v[52:55], a[20:23]\n" \
"s_add_u32 s89, s45, 0x1000\n" \
"ds_read_b128 v[88:91], v49 offset:0\n" \
"v_mfma_f32_16x16x32_bf16 a[52:55], v[116:119], v[52:55], a[52:55]\n" \
"buffer_store_dwordx4 v[236:239], v20, s[80:83], s89 offen offset:1024 sc1\n" \
"v_mfma_f32_16x16x32_bf16 a[52:55], v[120:123], v[56:59], a[52:55]\n" \
"ds_read_b128 v[108:111], v48 offset:6144\n" \
"ds_read_b128 v[112:115], v49 offset:6144\n" \
"v_mfma_f32_16x16x32_bf16 a[84:87], v[56:59], v[136:139], a[84:87]\n" \
"s_and_b32 s96, 2, %[w]\n" \
"s_lshl_b32 s97, s96, 1\n" \
"v_mfma_f32_16x16x32_bf16 a[84:87], v[52:55], v[132:135], a[84:87]\n" \
"s_and_b32 s98, 1, %[w]\n" \
"s_lshr_b32 s94, %[b], 5\n" \
"v_mfma_f32_16x16x32_bf16 a[116:119], v[140:143], v[60:63], a[116:119]\n" \
"s_add_u32 s99, s97, s98\n" \
"s_lshl_b32 s95, s94, 7\n" \
"v_mfma_f32_16x16x32_bf16 a[116:119], v[144:147], v[64:67], a[116:119]\n" \
"s_lshl_b32 s100, s99, 4\n" \
"s_add_u32 s101, s95, s100\n" \
"v_mfma_f32_16x16x32_bf16 a[24:27], v[128:131], v[64:67], a[24:27]\n" \
"s_lshr_b32 s65, %[b], 3\n" \
"s_mul_i32 s64, 0x480, s101\n" \
"v_mfma_f32_16x16x32_bf16 a[24:27], v[124:127], v[60:63], a[24:27]\n" \
"s_and_b32 s66, 3, s65\n" \
"s_sub_u32 s99, 0x780, s95\n" \
"v_mfma_f32_16x16x32_bf16 a[56:59], v[116:119], v[60:63], a[56:59]\n" \
"s_mul_i32 s67, 0x120, s66\n" \
"s_and_b32 s96, 7, %[b]\n" \
"v_mfma_f32_16x16x32_bf16 a[56:59], v[120:123], v[64:67], a[56:59]\n" \
"s_add_u32 s88, s45, 0x2000\n" \
"s_add_u32 s101, s99, s100\n" \
"v_mfma_f32_16x16x32_bf16 a[88:91], v[64:67], v[136:139], a[88:91]\n" \
"buffer_store_dwordx4 v[240:243], v20, s[80:83], s88 offen offset:1024 sc1\n" \
"v_mfma_f32_16x16x32_bf16 a[88:91], v[60:63], v[132:135], a[88:91]\n" \
"s_add_u32 s94, s64, s67\n" \
"s_lshl_b32 s97, s96, 5\n" \
"v_mfma_f32_16x16x32_bf16 a[120:123], v[140:143], v[68:71], a[120:123]\n" \
"s_mul_i32 s65, 0x480, s101\n" \
"s_add_u32 s98, s94, s97\n" \
"v_mfma_f32_16x16x32_bf16 a[120:123], v[144:147], v[72:75], a[120:123]\n" \
"s_add_u32 s66, s65, s67\n" \
"v_writelane_b32 v11, s98, 40\n" \
"v_mfma_f32_16x16x32_bf16 a[28:31], v[128:131], v[72:75], a[28:31]\n" \
"s_add_u32 s64, s66, s97\n" \
"s_and_b32 s95, 1, s96\n" \
"v_mfma_f32_16x16x32_bf16 a[28:31], v[124:127], v[68:71], a[28:31]\n" \
"v_writelane_b32 v11, s64, 41\n" \
"s_mov_b32 s64, 0xaaaaaaaa\n" \
"v_mfma_f32_16x16x32_bf16 a[60:63], v[116:119], v[68:71], a[60:63]\n" \
"s_cmp_lg_u32 s95, 0\n" \
"s_cselect_b32 s99, s66, s94\n" \
"v_mfma_f32_16x16x32_bf16 a[60:63], v[120:123], v[72:75], a[60:63]\n" \
"s_mov_b32 s65, 0xaaaaaaaa\n" \
"s_mov_b32 s94, s67\n" \
"v_mfma_f32_16x16x32_bf16 a[92:95], v[72:75], v[136:139], a[92:95]\n" \
"s_mov_b32 s66, 0xfff0ffff\n" \
"s_mov_b32 s67, 0xf000ff00\n" \
"v_mfma_f32_16x16x32_bf16 a[92:95], v[68:71], v[132:135], a[92:95]\n" \
"s_add_u32 s89, s45, 0x3000\n" \
"s_lshr_b32 s98, s96, 1\n" \
"v_mfma_f32_16x16x32_bf16 a[124:127], v[140:143], v[76:79], a[124:127]\n" \
"buffer_store_dwordx4 v[244:247], v20, s[80:83], s89 offen offset:1024 sc1\n" \
"v_mfma_f32_16x16x32_bf16 a[124:127], v[144:147], v[80:83], a[124:127]\n" \
"s_add_u32 s100, 0x100, s99\n" \
"s_and_b32 s95, %[outh], 0xffff\n" \
"v_mfma_f32_16x16x32_bf16 a[0:3], v[128:131], v[80:83], a[0:3]\n" \
"s_cmp_eq_u32 %[w], s98\n" \
"s_cselect_b32 s101, s100, 0x40000000\n" \
"v_mfma_f32_16x16x32_bf16 a[0:3], v[124:127], v[76:79], a[0:3]\n" \
"s_add_u32 s96, %[wol], s94\n" \
"s_addc_u32 s97, %[woh], 0\n" \
"v_mfma_f32_16x16x32_bf16 a[4:7], v[116:119], v[76:79], a[4:7]\n" \
"v_writelane_b32 v11, s101, 42\n" \
"s_and_b32 s97, s97, 0xffff\n" \
"v_mfma_f32_16x16x32_bf16 a[4:7], v[120:123], v[80:83], a[4:7]\n" \
"s_add_u32 s98, %[wbl], s94\n" \
"s_addc_u32 s99, %[wbh], 0\n" \
"v_mfma_f32_16x16x32_bf16 a[32:35], v[80:83], v[136:139], a[32:35]\n" \
"v_writelane_b32 v11, s96, 8\n" \
"v_writelane_b32 v11, s97, 9\n" \
"v_mfma_f32_16x16x32_bf16 a[32:35], v[76:79], v[132:135], a[32:35]\n" \
"s_and_b32 s99, s99, 0xffff\n" \
"s_waitcnt lgkmcnt(0)\n" \
"v_mfma_f32_16x16x32_bf16 a[128:131], v[148:151], v[52:55], a[128:131]\n" \
"s_cmp_lt_u32 %[w], 3\n" \
"s_cselect_b64 s[96:97], s[96:97], s[98:99]\n" \
"s_nop 0\n" \
"v_mfma_f32_16x16x32_bf16 a[128:131], v[152:155], v[56:59], a[128:131]\n" \
"v_writelane_b32 v11, s98, 10\n" \
"v_writelane_b32 v11, s99, 11\n" \
"v_mfma_f32_16x16x32_bf16 a[132:135], v[160:163], v[56:59], a[132:135]\n" \
"v_writelane_b32 v11, s96, 14\n" \
"v_writelane_b32 v11, s97, 15\n" \
"v_mfma_f32_16x16x32_bf16 a[132:135], v[156:159], v[52:55], a[132:135]\n" \
"v_writelane_b32 v11, %[outl], 12\n" \
"v_writelane_b32 v11, s95, 13\n" \
"v_mfma_f32_16x16x32_bf16 a[136:139], v[164:167], v[52:55], a[136:139]\n" \
"v_mfma_f32_16x16x32_bf16 a[136:139], v[168:171], v[56:59], a[136:139]\n" \
"v_mfma_f32_16x16x32_bf16 a[140:143], v[176:179], v[56:59], a[140:143]\n" \
"v_mfma_f32_16x16x32_bf16 a[140:143], v[172:175], v[52:55], a[140:143]\n" \
"v_mfma_f32_16x16x32_bf16 a[160:163], v[148:151], v[60:63], a[160:163]\n" \
"v_mfma_f32_16x16x32_bf16 a[160:163], v[152:155], v[64:67], a[160:163]\n" \
"v_mfma_f32_16x16x32_bf16 a[164:167], v[160:163], v[64:67], a[164:167]\n" \
"v_mfma_f32_16x16x32_bf16 a[164:167], v[156:159], v[60:63], a[164:167]\n" \
"v_mfma_f32_16x16x32_bf16 a[168:171], v[164:167], v[60:63], a[168:171]\n" \
"v_mfma_f32_16x16x32_bf16 a[168:171], v[168:171], v[64:67], a[168:171]\n" \
"v_mfma_f32_16x16x32_bf16 a[172:175], v[176:179], v[64:67], a[172:175]\n" \
"v_mfma_f32_16x16x32_bf16 a[172:175], v[172:175], v[60:63], a[172:175]\n" \
"v_mfma_f32_16x16x32_bf16 a[192:195], v[148:151], v[68:71], a[192:195]\n" \
"v_mfma_f32_16x16x32_bf16 a[192:195], v[152:155], v[72:75], a[192:195]\n" \
"s_waitcnt vmcnt(3)\n" \
"s_barrier\n" \
"v_mfma_f32_16x16x32_bf16 a[196:199], v[160:163], v[72:75], a[196:199]\n" \
"s_add_u32 m0, s43, 0x0\n" \
"s_nop 0\n" \
"s_add_u32 s85, s38, 0xc000\n" \
"v_mfma_f32_16x16x32_bf16 a[196:199], v[156:159], v[68:71], a[196:199]\n" \
"buffer_load_dwordx4 v20, s[80:83], s85 offen sc1 lds\n" \
"v_mfma_f32_16x16x32_bf16 a[200:203], v[164:167], v[68:71], a[200:203]\n" \
"s_add_u32 m0, s43, 0x1000\n" \
"s_add_u32 s84, s38, 0xd000\n" \
"v_mfma_f32_16x16x32_bf16 a[200:203], v[168:171], v[72:75], a[200:203]\n" \
"ds_read_b64_tr_b16 v[140:141], v39 offset:32768\n" \
"ds_read_b64_tr_b16 v[142:143], v39 offset:33280\n" \
"v_mfma_f32_16x16x32_bf16 a[204:207], v[176:179], v[72:75], a[204:207]\n" \
"buffer_load_dwordx4 v20, s[80:83], s84 offen sc1 lds\n" \
"v_mfma_f32_16x16x32_bf16 a[204:207], v[172:175], v[68:71], a[204:207]\n" \
"ds_read_b64_tr_b16 v[144:145], v39 offset:36864\n" \
"ds_read_b64_tr_b16 v[146:147], v39 offset:37376\n" \
"v_mfma_f32_16x16x32_bf16 a[224:227], v[148:151], v[76:79], a[224:227]\n" \
"s_add_u32 m0, s43, 0x3000\n" \
"s_mov_b32 s85, 0x300\n" \
"v_mfma_f32_16x16x32_bf16 a[224:227], v[152:155], v[80:83], a[224:227]\n" \
"buffer_load_dwordx4 v21, s[76:79], s85 offen lds\n" \
"v_mfma_f32_16x16x32_bf16 a[228:231], v[160:163], v[80:83], a[228:231]\n" \
"ds_read_b64_tr_b16 v[128:129], v37 offset:36864\n" \
"ds_read_b64_tr_b16 v[130:131], v37 offset:37376\n" \
"v_mfma_f32_16x16x32_bf16 a[228:231], v[156:159], v[76:79], a[228:231]\n" \
"ds_read_b64_tr_b16 v[124:125], v37 offset:32768\n" \
"ds_read_b64_tr_b16 v[126:127], v37 offset:33280\n" \
"v_mfma_f32_16x16x32_bf16 a[232:235], v[164:167], v[76:79], a[232:235]\n" \
"s_add_u32 m0, %[mA], 0x0\n" \
"s_nop 0\n" \
"s_mov_b32 s84, 0x300\n" \
"v_mfma_f32_16x16x32_bf16 a[232:235], v[168:171], v[80:83], a[232:235]\n" \
"buffer_load_dwordx4 v24, s[76:79], s84 offen lds\n" \
"v_mfma_f32_16x16x32_bf16 a[236:239], v[176:179], v[80:83], a[236:239]\n" \
"ds_read_b64_tr_b16 v[132:133], v38 offset:32768\n" \
"ds_read_b64_tr_b16 v[134:135], v38 offset:33280\n" \
"v_mfma_f32_16x16x32_bf16 a[236:239], v[172:175], v[76:79], a[236:239]\n" \
"s_add_u32 m0, %[mA], 0x400\n" \
"s_mov_b32 s85, 0x2700\n" \
"v_mfma_f32_16x16x32_bf16 a[144:147], v[148:151], v[84:87], a[144:147]\n" \
"buffer_load_dwordx4 v25, s[76:79], s85 offen lds\n" \
"v_mfma_f32_16x16x32_bf16 a[144:147], v[152:155], v[88:91], a[144:147]\n" \
"ds_read_b64_tr_b16 v[136:137], v38 offset:36864\n" \
"ds_read_b64_tr_b16 v[138:139], v38 offset:37376\n" \
"v_mfma_f32_16x16x32_bf16 a[148:151], v[160:163], v[88:91], a[148:151]\n" \
"ds_read_b128 v[56:59], v29 offset:32768\n" \
"ds_read_b128 v[52:55], v28 offset:32768\n" \
"v_mfma_f32_16x16x32_bf16 a[148:151], v[156:159], v[84:87], a[148:151]\n" \
"s_add_u32 m0, %[mB], 0x0\n" \
"s_nop 0\n" \
"s_mov_b32 s84, 0x300000\n" \
"v_mfma_f32_16x16x32_bf16 a[152:155], v[164:167], v[84:87], a[152:155]\n" \
"buffer_load_dwordx4 v26, s[68:71], s84 offen lds\n" \
"v_mfma_f32_16x16x32_bf16 a[152:155], v[168:171], v[88:91], a[152:155]\n" \
"ds_read_b64_tr_b16 v[120:121], v36 offset:36864\n" \
"ds_read_b64_tr_b16 v[122:123], v36 offset:37376\n" \
"v_mfma_f32_16x16x32_bf16 a[156:159], v[176:179], v[88:91], a[156:159]\n" \
"ds_read_b128 v[88:91], v23 offset:32768\n" \
"ds_read_b64_tr_b16 v[116:117], v36 offset:32768\n" \
"v_mfma_f32_16x16x32_bf16 a[156:159], v[172:175], v[84:87], a[156:159]\n" \
"s_add_u32 m0, %[mA], 0x800\n" \
"s_mov_b32 s85, 0x4b00\n" \
"v_mfma_f32_16x16x32_bf16 a[176:179], v[148:151], v[92:95], a[176:179]\n" \
"buffer_load_dwordx4 v24, s[76:79], s85 offen lds\n" \
"v_mfma_f32_16x16x32_bf16 a[176:179], v[152:155], v[96:99], a[176:179]\n" \
"ds_read_b128 v[84:87], v22 offset:32768\n" \
"ds_read_b64_tr_b16 v[118:119], v36 offset:33280\n" \
"v_mfma_f32_16x16x32_bf16 a[180:183], v[160:163], v[96:99], a[180:183]\n" \
"s_add_u32 m0, %[mA], 0xc00\n" \
"s_mov_b32 s84, 0x6f00\n" \
"v_mfma_f32_16x16x32_bf16 a[180:183], v[156:159], v[92:95], a[180:183]\n" \
"buffer_load_dwordx4 v25, s[76:79], s84 offen lds\n" \
"v_mfma_f32_16x16x32_bf16 a[184:187], v[164:167], v[92:95], a[184:187]\n" \
"ds_read_b128 v[64:67], v29 offset:34816\n" \
"ds_read_b128 v[60:63], v28 offset:34816\n" \
"v_mfma_f32_16x16x32_bf16 a[184:187], v[168:171], v[96:99], a[184:187]\n" \
"ds_read_b128 v[72:75], v29 offset:36864\n" \
"ds_read_b128 v[68:71], v28 offset:36864\n" \
"v_mfma_f32_16x16x32_bf16 a[188:191], v[176:179], v[96:99], a[188:191]\n" \
"s_add_u32 m0, %[mB], 0x400\n" \
"s_mov_b32 s85, 0x308000\n" \
"v_mfma_f32_16x16x32_bf16 a[188:191], v[172:175], v[92:95], a[188:191]\n" \
"L_pf7:\n" \
"s_cbranch_execz L_pf11\n" \
"s_nop 0\n" \
"buffer_load_dwordx4 v26, s[68:71], s85 offen lds\n" \
"v_mfma_f32_16x16x32_bf16 a[208:211], v[148:151], v[100:103], a[208:211]\n" \
"ds_read_b128 v[80:83], v29 offset:38912\n" \
"ds_read_b128 v[76:79], v28 offset:38912\n" \
"v_mfma_f32_16x16x32_bf16 a[208:211], v[152:155], v[104:107], a[208:211]\n" \
"ds_read_b128 v[92:95], v48 offset:34816\n" \
"ds_read_b128 v[96:99], v49 offset:34816\n" \
"v_mfma_f32_16x16x32_bf16 a[212:215], v[160:163], v[104:107], a[212:215]\n" \
"s_add_u32 m0, %[mA], 0x1000\n" \
"s_mov_b32 s84, 0x9300\n" \
"v_mfma_f32_16x16x32_bf16 a[212:215], v[156:159], v[100:103], a[212:215]\n" \
"buffer_load_dwordx4 v24, s[76:79], s84 offen lds\n" \
"v_mfma_f32_16x16x32_bf16 a[216:219], v[164:167], v[100:103], a[216:219]\n" \
"v_mfma_f32_16x16x32_bf16 a[216:219], v[168:171], v[104:107], a[216:219]\n" \
"s_add_u32 m0, %[mA], 0x1400\n" \
"s_mov_b32 s85, 0xb700\n" \
"v_mfma_f32_16x16x32_bf16 a[220:223], v[176:179], v[104:107], a[220:223]\n" \
"buffer_load_dwordx4 v25, s[76:79], s85 offen lds\n" \
"v_mfma_f32_16x16x32_bf16 a[220:223], v[172:175], v[100:103], a[220:223]\n" \
"ds_read_b128 v[100:103], v48 offset:36864\n" \
"ds_read_b128 v[104:107], v49 offset:36864\n" \
"v_mfma_f32_16x16x32_bf16 a[240:243], v[148:151], v[108:111], a[240:243]\n" \
"ds_read_b64_tr_b16 v[148:149], v32 offset:32768\n" \
"ds_read_b64_tr_b16 v[150:151], v32 offset:33792\n" \
"v_mfma_f32_16x16x32_bf16 a[240:243], v[152:155], v[112:115], a[240:243]\n" \
"s_add_u32 m0, %[mB], 0x800\n" \
"s_mov_b32 s84, 0x310000\n" \
"v_mfma_f32_16x16x32_bf16 a[244:247], v[160:163], v[112:115], a[244:247]\n" \
"buffer_load_dwordx4 v27, s[68:71], s84 offen lds\n" \
"v_mfma_f32_16x16x32_bf16 a[244:247], v[156:159], v[108:111], a[244:247]\n" \
"ds_read_b64_tr_b16 v[152:153], v32 offset:40960\n" \
"ds_read_b64_tr_b16 v[154:155], v32 offset:41984\n" \
"v_mfma_f32_16x16x32_bf16 a[248:251], v[164:167], v[108:111], a[248:251]\n" \
"ds_read_b64_tr_b16 v[156:157], v33 offset:32768\n" \
"ds_read_b64_tr_b16 v[158:159], v33 offset:33792\n" \
"v_mfma_f32_16x16x32_bf16 a[248:251], v[168:171], v[112:115], a[248:251]\n" \
"ds_read_b64_tr_b16 v[160:161], v33 offset:40960\n" \
"ds_read_b64_tr_b16 v[162:163], v33 offset:41984\n" \
"v_mfma_f32_16x16x32_bf16 a[252:255], v[176:179], v[112:115], a[252:255]\n" \
"ds_read_b64_tr_b16 v[168:169], v34 offset:40960\n" \
"ds_read_b64_tr_b16 v[170:171], v34 offset:41984\n" \
"v_mfma_f32_16x16x32_bf16 a[252:255], v[172:175], v[108:111], a[252:255]\n" \
"s_add_u32 m0, %[mA], 0x1800\n" \
"s_waitcnt lgkmcnt(12)\n" \
"s_nop 0\n" \
"v_mfma_f32_16x16x32_bf16 a[16:19], v[140:143], v[84:87], a[16:19]\n" \
"s_mov_b32 s84, 0xdb00\n" \
"ds_read_b64_tr_b16 v[164:165], v34 offset:32768\n" \
"v_mfma_f32_16x16x32_bf16 a[16:19], v[144:147], v[88:91], a[16:19]\n" \
"buffer_load_dwordx4 v24, s[76:79], s84 offen lds\n" \
"v_mfma_f32_16x16x32_bf16 a[48:51], v[128:131], v[88:91], a[48:51]\n" \
"s_add_u32 m0, %[mA], 0x1c00\n" \
"s_mov_b32 s85, 0xff00\n" \
"v_mfma_f32_16x16x32_bf16 a[48:51], v[124:127], v[84:87], a[48:51]\n" \
"ds_read_b64_tr_b16 v[166:167], v34 offset:33792\n" \
"ds_read_b64_tr_b16 v[172:173], v35 offset:32768\n" \
"v_mfma_f32_16x16x32_bf16 a[80:83], v[84:87], v[132:135], a[80:83]\n" \
"buffer_load_dwordx4 v25, s[76:79], s85 offen lds\n" \
"v_mfma_f32_16x16x32_bf16 a[80:83], v[88:91], v[136:139], a[80:83]\n" \
"s_add_u32 m0, %[mB], 0xc00\n" \
"s_mov_b32 s84, 0x318000\n" \
"v_mfma_f32_16x16x32_bf16 a[112:115], v[144:147], v[56:59], a[112:115]\n" \
"ds_read_b64_tr_b16 v[174:175], v35 offset:33792\n" \
"ds_read_b64_tr_b16 v[176:177], v35 offset:40960\n" \
"v_mfma_f32_16x16x32_bf16 a[112:115], v[140:143], v[52:55], a[112:115]\n" \
"buffer_load_dwordx4 v27, s[68:71], s84 offen lds\n" \
"v_mfma_f32_16x16x32_bf16 a[20:23], v[124:127], v[52:55], a[20:23]\n" \
"ds_read_b64_tr_b16 v[178:179], v35 offset:41984\n" \
"ds_read_b128 v[84:87], v48 offset:32768\n" \
"v_mfma_f32_16x16x32_bf16 a[20:23], v[128:131], v[56:59], a[20:23]\n" \
"s_add_u32 s88, s46, 0x0\n" \
"s_nop 0\n" \
"ds_read_b128 v[88:91], v49 offset:32768\n" \
"v_mfma_f32_16x16x32_bf16 a[52:55], v[120:123], v[56:59], a[52:55]\n" \
"buffer_store_dwordx4 v[248:251], v20, s[80:83], s88 offen offset:0 sc1\n" \
"v_mfma_f32_16x16x32_bf16 a[52:55], v[116:119], v[52:55], a[52:55]\n" \
"ds_read_b128 v[108:111], v48 offset:38912\n" \
"ds_read_b128 v[112:115], v49 offset:38912\n" \
"v_mfma_f32_16x16x32_bf16 a[84:87], v[52:55], v[132:135], a[84:87]\n" \
"v_mfma_f32_16x16x32_bf16 a[84:87], v[56:59], v[136:139], a[84:87]\n" \
"v_mfma_f32_16x16x32_bf16 a[116:119], v[144:147], v[64:67], a[116:119]\n" \
"v_mfma_f32_16x16x32_bf16 a[116:119], v[140:143], v[60:63], a[116:119]\n" \
"v_mfma_f32_16x16x32_bf16 a[24:27], v[124:127], v[60:63], a[24:27]\n" \
"v_mfma_f32_16x16x32_bf16 a[24:27], v[128:131], v[64:67], a[24:27]\n" \
"v_mfma_f32_16x16x32_bf16 a[56:59], v[120:123], v[64:67], a[56:59]\n" \
"v_mfma_f32_16x16x32_bf16 a[56:59], v[116:119], v[60:63], a[56:59]\n" \
"s_add_u32 s89, s48, 0x2000\n" \
"v_mfma_f32_16x16x32_bf16 a[88:91], v[60:63], v[132:135], a[88:91]\n" \
"buffer_store_dwordx4 v[184:187], v20, s[80:83], s89 offen offset:2048 sc1\n" \
"v_mfma_f32_16x16x32_bf16 a[88:91], v[64:67], v[136:139], a[88:91]\n" \
"v_mfma_f32_16x16x32_bf16 a[120:123], v[144:147], v[72:75], a[120:123]\n" \
"v_mfma_f32_16x16x32_bf16 a[120:123], v[140:143], v[68:71], a[120:123]\n" \
"v_mfma_f32_16x16x32_bf16 a[28:31], v[124:127], v[68:71], a[28:31]\n" \
"v_mfma_f32_16x16x32_bf16 a[28:31], v[128:131], v[72:75], a[28:31]\n" \
"v_mfma_f32_16x16x32_bf16 a[60:63], v[120:123], v[72:75], a[60:63]\n" \
"v_mfma_f32_16x16x32_bf16 a[60:63], v[116:119], v[68:71], a[60:63]\n" \
"v_mfma_f32_16x16x32_bf16 a[92:95], v[68:71], v[132:135], a[92:95]\n" \
"v_mfma_f32_16x16x32_bf16 a[92:95], v[72:75], v[136:139], a[92:95]\n" \
"s_add_u32 s88, s48, 0x2000\n" \
"v_mfma_f32_16x16x32_bf16 a[124:127], v[144:147], v[80:83], a[124:127]\n" \
"buffer_store_dwordx4 v[252:255], v20, s[80:83], s88 offen offset:3072 sc1\n" \
"v_mfma_f32_16x16x32_bf16 a[124:127], v[140:143], v[76:79], a[124:127]\n" \
"v_mfma_f32_16x16x32_bf16 a[0:3], v[124:127], v[76:79], a[0:3]\n" \
"v_mfma_f32_16x16x32_bf16 a[0:3], v[128:131], v[80:83], a[0:3]\n" \
"v_mfma_f32_16x16x32_bf16 a[4:7], v[120:123], v[80:83], a[4:7]\n" \
"v_mfma_f32_16x16x32_bf16 a[4:7], v[116:119], v[76:79], a[4:7]\n" \
"v_mfma_f32_16x16x32_bf16 a[32:35], v[76:79], v[132:135], a[32:35]\n" \
"v_mfma_f32_16x16x32_bf16 a[32:35], v[80:83], v[136:139], a[32:35]\n" \
"s_waitcnt lgkmcnt(0)\n" \
"s_nop 0\n" \
"v_mfma_f32_16x16x32_bf16 a[128:131], v[152:155], v[56:59], a[128:131]\n" \
"v_mfma_f32_16x16x32_bf16 a[128:131], v[148:151], v[52:55], a[128:131]\n" \
"v_mfma_f32_16x16x32_bf16 a[132:135], v[156:159], v[52:55], a[132:135]\n" \
"v_mfma_f32_16x16x32_bf16 a[132:135], v[160:163], v[56:59], a[132:135]\n" \
"v_mfma_f32_16x16x32_bf16 a[136:139], v[168:171], v[56:59], a[136:139]\n" \
"v_mfma_f32_16x16x32_bf16 a[136:139], v[164:167], v[52:55], a[136:139]\n" \
"v_mfma_f32_16x16x32_bf16 a[140:143], v[172:175], v[52:55], a[140:143]\n" \
"v_mfma_f32_16x16x32_bf16 a[140:143], v[176:179], v[56:59], a[140:143]\n" \
"v_mfma_f32_16x16x32_bf16 a[160:163], v[152:155], v[64:67], a[160:163]\n" \
"v_mfma_f32_16x16x32_bf16 a[160:163], v[148:151], v[60:63], a[160:163]\n" \
"v_mfma_f32_16x16x32_bf16 a[164:167], v[156:159], v[60:63], a[164:167]\n" \
"v_mfma_f32_16x16x32_bf16 a[164:167], v[160:163], v[64:67], a[164:167]\n" \
"v_mfma_f32_16x16x32_bf16 a[168:171], v[168:171], v[64:67], a[168:171]\n" \
"v_mfma_f32_16x16x32_bf16 a[168:171], v[164:167], v[60:63], a[168:171]\n" \
"v_mfma_f32_16x16x32_bf16 a[172:175], v[172:175], v[60:63], a[172:175]\n" \
"v_mfma_f32_16x16x32_bf16 a[172:175], v[176:179], v[64:67], a[172:175]\n" \
"v_mfma_f32_16x16x32_bf16 a[192:195], v[152:155], v[72:75], a[192:195]\n" \
"v_mfma_f32_16x16x32_bf16 a[192:195], v[148:151], v[68:71], a[192:195]\n" \
"s_waitcnt vmcnt(3)\n" \
"s_barrier\n" \
"s_mov_b64 exec, 1\n" \
"buffer_store_dword v45, off, s[80:83], s41 offset:0 sc1\n" \
"s_mov_b64 exec, -1\n" \
"v_mfma_f32_16x16x32_bf16 a[196:199], v[156:159], v[68:71], a[196:199]\n" \
"s_add_u32 m0, s43, 0x8000\n" \
"s_add_u32 s85, s38, 0xe000\n" \
"v_mfma_f32_16x16x32_bf16 a[196:199], v[160:163], v[72:75], a[196:199]\n" \
"ds_read_b64_tr_b16 v[144:145], v39 offset:4096\n" \
"ds_read_b64_tr_b16 v[146:147], v39 offset:4608\n" \
"v_mfma_f32_16x16x32_bf16 a[200:203], v[168:171], v[72:75], a[200:203]\n" \
"buffer_load_dwordx4 v20, s[80:83], s85 offen sc1 lds\n" \
"v_mfma_f32_16x16x32_bf16 a[200:203], v[164:167], v[68:71], a[200:203]\n" \
"s_add_u32 m0, s43, 0x9000\n" \
"s_add_u32 s84, s38, 0xf000\n" \
"v_mfma_f32_16x16x32_bf16 a[204:207], v[172:175], v[68:71], a[204:207]\n" \
"ds_read_b64_tr_b16 v[140:141], v39 offset:0\n" \
"ds_read_b64_tr_b16 v[142:143], v39 offset:512\n" \
"v_mfma_f32_16x16x32_bf16 a[204:207], v[176:179], v[72:75], a[204:207]\n" \
"buffer_load_dwordx4 v20, s[80:83], s84 offen sc1 lds\n" \
"v_mfma_f32_16x16x32_bf16 a[224:227], v[152:155], v[80:83], a[224:227]\n" \
"s_add_u32 m0, s43, 0xb000\n" \
"s_mov_b32 s85, 0x380\n" \
"v_mfma_f32_16x16x32_bf16 a[224:227], v[148:151], v[76:79], a[224:227]\n" \
"ds_read_b64_tr_b16 v[124:125], v37 offset:0\n" \
"ds_read_b64_tr_b16 v[126:127], v37 offset:512\n" \
"v_mfma_f32_16x16x32_bf16 a[228:231], v[156:159], v[76:79], a[228:231]\n" \
"buffer_load_dwordx4 v21, s[76:79], s85 offen lds\n" \
"v_mfma_f32_16x16x32_bf16 a[228:231], v[160:163], v[80:83], a[228:231]\n" \
"ds_read_b64_tr_b16 v[128:129], v37 offset:4096\n" \
"ds_read_b64_tr_b16 v[130:131], v37 offset:4608\n" \
"v_mfma_f32_16x16x32_bf16 a[232:235], v[168:171], v[80:83], a[232:235]\n" \
"s_add_u32 m0, %[mA], 0x8000\n" \
"s_mov_b32 s84, 0x380\n" \
"v_mfma_f32_16x16x32_bf16 a[232:235], v[164:167], v[76:79], a[232:235]\n" \
"buffer_load_dwordx4 v24, s[76:79], s84 offen lds\n" \
"v_mfma_f32_16x16x32_bf16 a[236:239], v[172:175], v[76:79], a[236:239]\n" \
"ds_read_b64_tr_b16 v[136:137], v38 offset:4096\n" \
"ds_read_b64_tr_b16 v[138:139], v38 offset:4608\n" \
"v_mfma_f32_16x16x32_bf16 a[236:239], v[176:179], v[80:83], a[236:239]\n" \
"s_add_u32 m0, %[mA], 0x8400\n" \
"s_mov_b32 s85, 0x2780\n" \
"v_mfma_f32_16x16x32_bf16 a[144:147], v[152:155], v[88:91], a[144:147]\n" \
"buffer_load_dwordx4 v25, s[76:79], s85 offen lds\n" \
"v_mfma_f32_16x16x32_bf16 a[144:147], v[148:151], v[84:87], a[144:147]\n" \
"ds_read_b64_tr_b16 v[132:133], v38 offset:0\n" \
"ds_read_b64_tr_b16 v[134:135], v38 offset:512\n" \
"v_mfma_f32_16x16x32_bf16 a[148:151], v[156:159], v[84:87], a[148:151]\n" \
"ds_read_b128 v[52:55], v28 offset:0\n" \
"ds_read_b128 v[56:59], v29 offset:0\n" \
"v_mfma_f32_16x16x32_bf16 a[148:151], v[160:163], v[88:91], a[148:151]\n" \
"s_add_u32 m0, %[mB], 0x8000\n" \
"s_mov_b32 s84, 0x380000\n" \
"v_mfma_f32_16x16x32_bf16 a[152:155], v[168:171], v[88:91], a[152:155]\n" \
"buffer_load_dwordx4 v26, s[68:71], s84 offen lds\n" \
"v_mfma_f32_16x16x32_bf16 a[152:155], v[164:167], v[84:87], a[152:155]\n" \
"ds_read_b64_tr_b16 v[116:117], v36 offset:0\n" \
"ds_read_b64_tr_b16 v[118:119], v36 offset:512\n" \
"v_mfma_f32_16x16x32_bf16 a[156:159], v[172:175], v[84:87], a[156:159]\n" \
"ds_read_b128 v[84:87], v22 offset:0\n" \
"ds_read_b64_tr_b16 v[120:121], v36 offset:4096\n" \
"v_mfma_f32_16x16x32_bf16 a[156:159], v[176:179], v[88:91], a[156:159]\n" \
"s_add_u32 m0, %[mA], 0x8800\n" \
"s_mov_b32 s85, 0x4b80\n" \
"v_mfma_f32_16x16x32_bf16 a[176:179], v[152:155], v[96:99], a[176:179]\n" \
"buffer_load_dwordx4 v24, s[76:79], s85 offen lds\n" \
"v_mfma_f32_16x16x32_bf16 a[176:179], v[148:151], v[92:95], a[176:179]\n" \
"ds_read_b128 v[88:91], v23 offset:0\n" \
"ds_read_b64_tr_b16 v[122:123], v36 offset:4608\n" \
"v_mfma_f32_16x16x32_bf16 a[180:183], v[156:159], v[92:95], a[180:183]\n" \
"s_add_u32 m0, %[mA], 0x8c00\n" \
"s_mov_b32 s84, 0x6f80\n" \
"v_mfma_f32_16x16x32_bf16 a[180:183], v[160:163], v[96:99], a[180:183]\n" \
"buffer_load_dwordx4 v25, s[76:79], s84 offen lds\n" \
"v_mfma_f32_16x16x32_bf16 a[184:187], v[168:171], v[96:99], a[184:187]\n" \
"ds_read_b128 v[60:63], v28 offset:2048\n" \
"ds_read_b128 v[64:67], v29 offset:2048\n" \
"v_mfma_f32_16x16x32_bf16 a[184:187], v[164:167], v[92:95], a[184:187]\n" \
"ds_read_b128 v[68:71], v28 offset:4096\n" \
"ds_read_b128 v[72:75], v29 offset:4096\n" \
"v_mfma_f32_16x16x32_bf16 a[188:191], v[172:175], v[92:95], a[188:191]\n" \
"s_add_u32 m0, %[mB], 0x8400\n" \
"s_mov_b32 s85, 0x388000\n" \
"v_mfma_f32_16x16x32_bf16 a[188:191], v[176:179], v[96:99], a[188:191]\n" \
"buffer_load_dwordx4 v26, s[68:71], s85 offen lds\n" \
"v_mfma_f32_16x16x32_bf16 a[208:211], v[152:155], v[104:107], a[208:211]\n" \
"ds_read_b128 v[76:79], v28 offset:6144\n" \
"ds_read_b128 v[80:83], v29 offset:6144\n" \
"v_mfma_f32_16x16x32_bf16 a[208:211], v[148:151], v[100:103], a[208:211]\n" \
"ds_read_b128 v[92:95], v48 offset:2048\n" \
"ds_read_b128 v[96:99], v49 offset:2048\n" \
"v_mfma_f32_16x16x32_bf16 a[212:215], v[156:159], v[100:103], a[212:215]\n" \
"s_add_u32 m0, %[mA], 0x9000\n" \
"s_mov_b32 s84, 0x9380\n" \
"v_mfma_f32_16x16x32_bf16 a[212:215], v[160:163], v[104:107], a[212:215]\n" \
"buffer_load_dwordx4 v24, s[76:79], s84 offen lds\n" \
"v_mfma_f32_16x16x32_bf16 a[216:219], v[168:171], v[104:107], a[216:219]\n" \
"v_mfma_f32_16x16x32_bf16 a[216:219], v[164:167], v[100:103], a[216:219]\n" \
"s_add_u32 m0, %[mA], 0x9400\n" \
"s_mov_b32 s85, 0xb780\n" \
"v_mfma_f32_16x16x32_bf16 a[220:223], v[172:175], v[100:103], a[220:223]\n" \
"buffer_load_dwordx4 v25, s[76:79], s85 offen lds\n" \
"v_mfma_f32_16x16x32_bf16 a[220:223], v[176:179], v[104:107], a[220:223]\n" \
"ds_read_b128 v[100:103], v48 offset:4096\n" \
"ds_read_b128 v[104:107], v49 offset:4096\n" \
"v_mfma_f32_16x16x32_bf16 a[240:243], v[152:155], v[112:115], a[240:243]\n" \
"ds_read_b64_tr_b16 v[152:153], v32 offset:8192\n" \
"ds_read_b64_tr_b16 v[154:155], v32 offset:9216\n" \
"v_mfma_f32_16x16x32_bf16 a[240:243], v[148:151], v[108:111], a[240:243]\n" \
"s_add_u32 m0, %[mB], 0x8800\n" \
"s_mov_b32 s84, 0x390000\n" \
"v_mfma_f32_16x16x32_bf16 a[244:247], v[156:159], v[108:111], a[244:247]\n" \
"buffer_load_dwordx4 v27, s[68:71], s84 offen lds\n" \
"v_mfma_f32_16x16x32_bf16 a[244:247], v[160:163], v[112:115], a[244:247]\n" \
"ds_read_b64_tr_b16 v[148:149], v32 offset:0\n" \
"ds_read_b64_tr_b16 v[150:151], v32 offset:1024\n" \
"v_mfma_f32_16x16x32_bf16 a[248:251], v[168:171], v[112:115], a[248:251]\n" \
"ds_read_b64_tr_b16 v[160:161], v33 offset:8192\n" \
"ds_read_b64_tr_b16 v[162:163], v33 offset:9216\n" \
"v_mfma_f32_16x16x32_bf16 a[248:251], v[164:167], v[108:111], a[248:251]\n" \
"ds_read_b64_tr_b16 v[156:157], v33 offset:0\n" \
"ds_read_b64_tr_b16 v[158:159], v33 offset:1024\n" \
"v_mfma_f32_16x16x32_bf16 a[252:255], v[172:175], v[108:111], a[252:255]\n" \
"ds_read_b64_tr_b16 v[164:165], v34 offset:0\n" \
"ds_read_b64_tr_b16 v[166:167], v34 offset:1024\n" \
"v_mfma_f32_16x16x32_bf16 a[252:255], v[176:179], v[112:115], a[252:255]\n" \
"s_add_u32 m0, %[mA], 0x9800\n" \
"s_waitcnt lgkmcnt(12)\n" \
"s_nop 0\n" \
"v_mfma_f32_16x16x32_bf16 a[16:19], v[144:147], v[88:91], a[16:19]\n" \
"s_mov_b32 s84, 0xdb80\n" \
"ds_read_b64_tr_b16 v[168:169], v34 offset:8192\n" \
"v_mfma_f32_16x16x32_bf16 a[16:19], v[140:143], v[84:87], a[16:19]\n" \
"buffer_load_dwordx4 v24, s[76:79], s84 offen lds\n" \
"v_mfma_f32_16x16x32_bf16 a[48:51], v[124:127], v[84:87], a[48:51]\n" \
"s_add_u32 m0, %[mA], 0x9c00\n" \
"s_mov_b32 s85, 0xff80\n" \
"v_mfma_f32_16x16x32_bf16 a[48:51], v[128:131], v[88:91], a[48:51]\n" \
"ds_read_b64_tr_b16 v[170:171], v34 offset:9216\n" \
"ds_read_b64_tr_b16 v[176:177], v35 offset:8192\n" \
"v_mfma_f32_16x16x32_bf16 a[80:83], v[88:91], v[136:139], a[80:83]\n" \
"buffer_load_dwordx4 v25, s[76:79], s85 offen lds\n" \
"v_mfma_f32_16x16x32_bf16 a[80:83], v[84:87], v[132:135], a[80:83]\n" \
"s_add_u32 m0, %[mB], 0x8c00\n" \
"s_mov_b32 s84, 0x398000\n" \
"v_mfma_f32_16x16x32_bf16 a[112:115], v[140:143], v[52:55], a[112:115]\n" \
"ds_read_b64_tr_b16 v[178:179], v35 offset:9216\n" \
"ds_read_b64_tr_b16 v[172:173], v35 offset:0\n" \
"v_mfma_f32_16x16x32_bf16 a[112:115], v[144:147], v[56:59], a[112:115]\n" \
"buffer_load_dwordx4 v27, s[68:71], s84 offen lds\n" \
"v_mfma_f32_16x16x32_bf16 a[20:23], v[128:131], v[56:59], a[20:23]\n" \
"ds_read_b64_tr_b16 v[174:175], v35 offset:1024\n" \
"ds_read_b128 v[84:87], v48 offset:0\n" \
"v_mfma_f32_16x16x32_bf16 a[20:23], v[124:127], v[52:55], a[20:23]\n" \
"s_add_u32 s89, s46, 0x1000\n" \
"ds_read_b128 v[88:91], v49 offset:0\n" \
"v_mfma_f32_16x16x32_bf16 a[52:55], v[116:119], v[52:55], a[52:55]\n" \
"buffer_store_dwordx4 a[36:39], v20, s[80:83], s89 offen offset:0 sc1\n" \
"v_mfma_f32_16x16x32_bf16 a[52:55], v[120:123], v[56:59], a[52:55]\n" \
"ds_read_b128 v[108:111], v48 offset:6144\n" \
"ds_read_b128 v[112:115], v49 offset:6144\n" \
"v_mfma_f32_16x16x32_bf16 a[84:87], v[56:59], v[136:139], a[84:87]\n" \
"v_mfma_f32_16x16x32_bf16 a[84:87], v[52:55], v[132:135], a[84:87]\n" \
"v_mfma_f32_16x16x32_bf16 a[116:119], v[140:143], v[60:63], a[116:119]\n" \
"v_mfma_f32_16x16x32_bf16 a[116:119], v[144:147], v[64:67], a[116:119]\n" \
"v_mfma_f32_16x16x32_bf16 a[24:27], v[128:131], v[64:67], a[24:27]\n" \
"v_mfma_f32_16x16x32_bf16 a[24:27], v[124:127], v[60:63], a[24:27]\n" \
"v_mfma_f32_16x16x32_bf16 a[56:59], v[116:119], v[60:63], a[56:59]\n" \
"v_mfma_f32_16x16x32_bf16 a[56:59], v[120:123], v[64:67], a[56:59]\n" \
"s_add_u32 s88, s46, 0x2000\n" \
"v_mfma_f32_16x16x32_bf16 a[88:91], v[64:67], v[136:139], a[88:91]\n" \
"buffer_store_dwordx4 a[64:67], v20, s[80:83], s88 offen offset:0 sc1\n" \
"v_mfma_f32_16x16x32_bf16 a[88:91], v[60:63], v[132:135], a[88:91]\n" \
"v_mfma_f32_16x16x32_bf16 a[120:123], v[140:143], v[68:71], a[120:123]\n" \
"v_mfma_f32_16x16x32_bf16 a[120:123], v[144:147], v[72:75], a[120:123]\n" \
"v_mfma_f32_16x16x32_bf16 a[28:31], v[128:131], v[72:75], a[28:31]\n" \
"v_mfma_f32_16x16x32_bf16 a[28:31], v[124:127], v[68:71], a[28:31]\n" \
"v_mfma_f32_16x16x32_bf16 a[60:63], v[116:119], v[68:71], a[60:63]\n" \
"v_mfma_f32_16x16x32_bf16 a[60:63], v[120:123], v[72:75], a[60:63]\n" \
"v_mfma_f32_16x16x32_bf16 a[92:95], v[72:75], v[136:139], a[92:95]\n" \
"v_mfma_f32_16x16x32_bf16 a[92:95], v[68:71], v[132:135], a[92:95]\n" \
"s_add_u32 s89, s46, 0x3000\n" \
"v_mfma_f32_16x16x32_bf16 a[124:127], v[140:143], v[76:79], a[124:127]\n" \
"buffer_store_dwordx4 a[68:71], v20, s[80:83], s89 offen offset:0 sc1\n" \
"v_mfma_f32_16x16x32_bf16 a[124:127], v[144:147], v[80:83], a[124:127]\n" \
"v_mfma_f32_16x16x32_bf16 a[0:3], v[128:131], v[80:83], a[0:3]\n" \
"v_mfma_f32_16x16x32_bf16 a[0:3], v[124:127], v[76:79], a[0:3]\n" \
"v_mfma_f32_16x16x32_bf16 a[4:7], v[116:119], v[76:79], a[4:7]\n" \
"v_mfma_f32_16x16x32_bf16 a[4:7], v[120:123], v[80:83], a[4:7]\n" \
"v_mfma_f32_16x16x32_bf16 a[32:35], v[80:83], v[136:139], a[32:35]\n" \
"v_mfma_f32_16x16x32_bf16 a[32:35], v[76:79], v[132:135], a[32:35]\n" \
"s_waitcnt lgkmcnt(0)\n" \
"s_nop 0\n" \
"v_mfma_f32_16x16x32_bf16 a[128:131], v[148:151], v[52:55], a[128:131]\n" \
"v_mfma_f32_16x16x32_bf16 a[128:131], v[152:155], v[56:59], a[128:131]\n" \
"v_mfma_f32_16x16x32_bf16 a[132:135], v[160:163], v[56:59], a[132:135]\n" \
"v_mfma_f32_16x16x32_bf16 a[132:135], v[156:159], v[52:55], a[132:135]\n" \
"v_mfma_f32_16x16x32_bf16 a[136:139], v[164:167], v[52:55], a[136:139]\n" \
"v_mfma_f32_16x16x32_bf16 a[136:139], v[168:171], v[56:59], a[136:139]\n" \
"v_mfma_f32_16x16x32_bf16 a[140:143], v[176:179], v[56:59], a[140:143]\n" \
"v_mfma_f32_16x16x32_bf16 a[140:143], v[172:175], v[52:55], a[140:143]\n" \
"v_mfma_f32_16x16x32_bf16 a[160:163], v[148:151], v[60:63], a[160:163]\n" \
"v_mfma_f32_16x16x32_bf16 a[160:163], v[152:155], v[64:67], a[160:163]\n" \
"v_mfma_f32_16x16x32_bf16 a[164:167], v[160:163], v[64:67], a[164:167]\n" \
"v_mfma_f32_16x16x32_bf16 a[164:167], v[156:159], v[60:63], a[164:167]\n" \
"v_mfma_f32_16x16x32_bf16 a[168:171], v[164:167], v[60:63], a[168:171]\n" \
"v_mfma_f32_16x16x32_bf16 a[168:171], v[168:171], v[64:67], a[168:171]\n" \
"v_mfma_f32_16x16x32_bf16 a[172:175], v[176:179], v[64:67], a[172:175]\n" \
"v_mfma_f32_16x16x32_bf16 a[172:175], v[172:175], v[60:63], a[172:175]\n" \
"v_mfma_f32_16x16x32_bf16 a[192:195], v[148:151], v[68:71], a[192:195]\n" \
"v_mfma_f32_16x16x32_bf16 a[192:195], v[152:155], v[72:75], a[192:195]\n" \
"s_waitcnt vmcnt(3)\n" \
"s_barrier\n" \
"v_mfma_f32_16x16x32_bf16 a[196:199], v[160:163], v[72:75], a[196:199]\n" \
"s_add_u32 m0, s43, 0x0\n" \
"s_nop 0\n" \
"s_add_u32 s85, s38, 0x10000\n" \
"v_mfma_f32_16x16x32_bf16 a[196:199], v[156:159], v[68:71], a[196:199]\n" \
"buffer_load_dwordx4 v20, s[80:83], s85 offen sc1 lds\n" \
"v_mfma_f32_16x16x32_bf16 a[200:203], v[164:167], v[68:71], a[200:203]\n" \
"s_add_u32 m0, s43, 0x1000\n" \
"s_add_u32 s84, s38, 0x11000\n" \
"v_mfma_f32_16x16x32_bf16 a[200:203], v[168:171], v[72:75], a[200:203]\n" \
"ds_read_b64_tr_b16 v[140:141], v39 offset:32768\n" \
"ds_read_b64_tr_b16 v[142:143], v39 offset:33280\n" \
"v_mfma_f32_16x16x32_bf16 a[204:207], v[176:179], v[72:75], a[204:207]\n" \
"buffer_load_dwordx4 v20, s[80:83], s84 offen sc1 lds\n" \
"v_mfma_f32_16x16x32_bf16 a[204:207], v[172:175], v[68:71], a[204:207]\n" \
"ds_read_b64_tr_b16 v[144:145], v39 offset:36864\n" \
"ds_read_b64_tr_b16 v[146:147], v39 offset:37376\n" \
"v_mfma_f32_16x16x32_bf16 a[224:227], v[148:151], v[76:79], a[224:227]\n" \
"s_add_u32 m0, s43, 0x3000\n" \
"s_mov_b32 s85, 0x400\n" \
"v_mfma_f32_16x16x32_bf16 a[224:227], v[152:155], v[80:83], a[224:227]\n" \
"buffer_load_dwordx4 v21, s[76:79], s85 offen lds\n" \
"v_mfma_f32_16x16x32_bf16 a[228:231], v[160:163], v[80:83], a[228:231]\n" \
"ds_read_b64_tr_b16 v[128:129], v37 offset:36864\n" \
"ds_read_b64_tr_b16 v[130:131], v37 offset:37376\n" \
"v_mfma_f32_16x16x32_bf16 a[228:231], v[156:159], v[76:79], a[228:231]\n" \
"ds_read_b64_tr_b16 v[124:125], v37 offset:32768\n" \
"ds_read_b64_tr_b16 v[126:127], v37 offset:33280\n" \
"v_mfma_f32_16x16x32_bf16 a[232:235], v[164:167], v[76:79], a[232:235]\n" \
"s_add_u32 m0, %[mA], 0x0\n" \
"s_nop 0\n" \
"s_mov_b32 s84, 0x400\n" \
"v_mfma_f32_16x16x32_bf16 a[232:235], v[168:171], v[80:83], a[232:235]\n" \
"buffer_load_dwordx4 v24, s[76:79], s84 offen lds\n" \
"v_mfma_f32_16x16x32_bf16 a[236:239], v[176:179], v[80:83], a[236:239]\n" \
"ds_read_b64_tr_b16 v[132:133], v38 offset:32768\n" \
"ds_read_b64_tr_b16 v[134:135], v38 offset:33280\n" \
"v_mfma_f32_16x16x32_bf16 a[236:239], v[172:175], v[76:79], a[236:239]\n" \
"s_add_u32 m0, %[mA], 0x400\n" \
"s_mov_b32 s85, 0x2800\n" \
"v_mfma_f32_16x16x32_bf16 a[144:147], v[148:151], v[84:87], a[144:147]\n" \
"buffer_load_dwordx4 v25, s[76:79], s85 offen lds\n" \
"v_mfma_f32_16x16x32_bf16 a[144:147], v[152:155], v[88:91], a[144:147]\n" \
"ds_read_b64_tr_b16 v[136:137], v38 offset:36864\n" \
"ds_read_b64_tr_b16 v[138:139], v38 offset:37376\n" \
"v_mfma_f32_16x16x32_bf16 a[148:151], v[160:163], v[88:91], a[148:151]\n" \
"ds_read_b128 v[56:59], v29 offset:32768\n" \
"ds_read_b128 v[52:55], v28 offset:32768\n" \
"v_mfma_f32_16x16x32_bf16 a[148:151], v[156:159], v[84:87], a[148:151]\n" \
"s_add_u32 m0, %[mB], 0x0\n" \
"s_nop 0\n" \
"s_mov_b32 s84, 0x400000\n" \
"v_mfma_f32_16x16x32_bf16 a[152:155], v[164:167], v[84:87], a[152:155]\n" \
"buffer_load_dwordx4 v26, s[68:71], s84 offen lds\n" \
"v_mfma_f32_16x16x32_bf16 a[152:155], v[168:171], v[88:91], a[152:155]\n" \
"ds_read_b64_tr_b16 v[120:121], v36 offset:36864\n" \
"ds_read_b64_tr_b16 v[122:123], v36 offset:37376\n" \
"v_mfma_f32_16x16x32_bf16 a[156:159], v[176:179], v[88:91], a[156:159]\n" \
"ds_read_b128 v[88:91], v23 offset:32768\n" \
"ds_read_b64_tr_b16 v[116:117], v36 offset:32768\n" \
"v_mfma_f32_16x16x32_bf16 a[156:159], v[172:175], v[84:87], a[156:159]\n" \
"s_add_u32 m0, %[mA], 0x800\n" \
"s_mov_b32 s85, 0x4c00\n" \
"v_mfma_f32_16x16x32_bf16 a[176:179], v[148:151], v[92:95], a[176:179]\n" \
"buffer_load_dwordx4 v24, s[76:79], s85 offen lds\n" \
"v_mfma_f32_16x16x32_bf16 a[176:179], v[152:155], v[96:99], a[176:179]\n" \
"ds_read_b128 v[84:87], v22 offset:32768\n" \
"ds_read_b64_tr_b16 v[118:119], v36 offset:33280\n" \
"v_mfma_f32_16x16x32_bf16 a[180:183], v[160:163], v[96:99], a[180:183]\n" \
"s_add_u32 m0, %[mA], 0xc00\n" \
"s_mov_b32 s84, 0x7000\n" \
"v_mfma_f32_16x16x32_bf16 a[180:183], v[156:159], v[92:95], a[180:183]\n" \
"buffer_load_dwordx4 v25, s[76:79], s84 offen lds\n" \
"v_mfma_f32_16x16x32_bf16 a[184:187], v[164:167], v[92:95], a[184:187]\n" \
"ds_read_b128 v[64:67], v29 offset:34816\n" \
"ds_read_b128 v[60:63], v28 offset:34816\n" \
"v_mfma_f32_16x16x32_bf16 a[184:187], v[168:171], v[96:99], a[184:187]\n" \
"ds_read_b128 v[72:75], v29 offset:36864\n" \
"ds_read_b128 v[68:71], v28 offset:36864\n" \
"v_mfma_f32_16x16x32_bf16 a[188:191], v[176:179], v[96:99], a[188:191]\n" \
"s_add_u32 m0, %[mB], 0x400\n" \
"s_mov_b32 s85, 0x408000\n" \
"v_mfma_f32_16x16x32_bf16 a[188:191], v[172:175], v[92:95], a[188:191]\n" \
"buffer_load_dwordx4 v26, s[68:71], s85 offen lds\n" \
"v_mfma_f32_16x16x32_bf16 a[208:211], v[148:151], v[100:103], a[208:211]\n" \
"ds_read_b128 v[80:83], v29 offset:38912\n" \
"ds_read_b128 v[76:79], v28 offset:38912\n" \
"v_mfma_f32_16x16x32_bf16 a[208:211], v[152:155], v[104:107], a[208:211]\n" \
"ds_read_b128 v[92:95], v48 offset:34816\n" \
"ds_read_b128 v[96:99], v49 offset:34816\n" \
"v_mfma_f32_16x16x32_bf16 a[212:215], v[160:163], v[104:107], a[212:215]\n" \
"s_add_u32 m0, %[mA], 0x1000\n" \
"s_mov_b32 s84, 0x9400\n" \
"v_mfma_f32_16x16x32_bf16 a[212:215], v[156:159], v[100:103], a[212:215]\n" \
"buffer_load_dwordx4 v24, s[76:79], s84 offen lds\n" \
"v_mfma_f32_16x16x32_bf16 a[216:219], v[164:167], v[100:103], a[216:219]\n" \
"v_and_b32_e32 v26, 0x1ff, v20\n" \
"v_add_u32_e64 v21, s54, v20\n" \
"v_mfma_f32_16x16x32_bf16 a[216:219], v[168:171], v[104:107], a[216:219]\n" \
"s_add_u32 m0, %[mA], 0x1400\n" \
"s_mov_b32 s85, 0xb800\n" \
"v_mfma_f32_16x16x32_bf16 a[220:223], v[176:179], v[104:107], a[220:223]\n" \
"buffer_load_dwordx4 v25, s[76:79], s85 offen lds\n" \
"v_mfma_f32_16x16x32_bf16 a[220:223], v[172:175], v[100:103], a[220:223]\n" \
"ds_read_b128 v[100:103], v48 offset:36864\n" \
"ds_read_b128 v[104:107], v49 offset:36864\n" \
"v_mfma_f32_16x16x32_bf16 a[240:243], v[148:151], v[108:111], a[240:243]\n" \
"ds_read_b64_tr_b16 v[148:149], v32 offset:32768\n" \
"ds_read_b64_tr_b16 v[150:151], v32 offset:33792\n" \
"v_mfma_f32_16x16x32_bf16 a[240:243], v[152:155], v[112:115], a[240:243]\n" \
"s_add_u32 m0, %[mB], 0x800\n" \
"s_mov_b32 s84, 0x410000\n" \
"v_mfma_f32_16x16x32_bf16 a[244:247], v[160:163], v[112:115], a[244:247]\n" \
"buffer_load_dwordx4 v27, s[68:71], s84 offen lds\n" \
"v_mfma_f32_16x16x32_bf16 a[244:247], v[156:159], v[108:111], a[244:247]\n" \
"ds_read_b64_tr_b16 v[152:153], v32 offset:40960\n" \
"ds_read_b64_tr_b16 v[154:155], v32 offset:41984\n" \
"v_mfma_f32_16x16x32_bf16 a[248:251], v[164:167], v[108:111], a[248:251]\n" \
"ds_read_b64_tr_b16 v[156:157], v33 offset:32768\n" \
"ds_read_b64_tr_b16 v[158:159], v33 offset:33792\n" \
"v_mfma_f32_16x16x32_bf16 a[248:251], v[168:171], v[112:115], a[248:251]\n" \
"ds_read_b64_tr_b16 v[160:161], v33 offset:40960\n" \
"ds_read_b64_tr_b16 v[162:163], v33 offset:41984\n" \
"v_mfma_f32_16x16x32_bf16 a[252:255], v[176:179], v[112:115], a[252:255]\n" \
"ds_read_b64_tr_b16 v[168:169], v34 offset:40960\n" \
"ds_read_b64_tr_b16 v[170:171], v34 offset:41984\n" \
"v_mfma_f32_16x16x32_bf16 a[252:255], v[172:175], v[108:111], a[252:255]\n" \
"s_add_u32 m0, %[mA], 0x1800\n" \
"s_waitcnt lgkmcnt(12)\n" \
"s_nop 0\n" \
"v_mfma_f32_16x16x32_bf16 a[16:19], v[140:143], v[84:87], a[16:19]\n" \
"s_mov_b32 s84, 0xdc00\n" \
"ds_read_b64_tr_b16 v[164:165], v34 offset:32768\n" \
"v_mfma_f32_16x16x32_bf16 a[16:19], v[144:147], v[88:91], a[16:19]\n" \
"buffer_load_dwordx4 v24, s[76:79], s84 offen lds\n" \
"v_mfma_f32_16x16x32_bf16 a[48:51], v[128:131], v[88:91], a[48:51]\n" \
"s_add_u32 m0, %[mA], 0x1c00\n" \
"s_mov_b32 s85, 0x10000\n" \
"v_mfma_f32_16x16x32_bf16 a[48:51], v[124:127], v[84:87], a[48:51]\n" \
"ds_read_b64_tr_b16 v[166:167], v34 offset:33792\n" \
"ds_read_b64_tr_b16 v[172:173], v35 offset:32768\n" \
"v_mfma_f32_16x16x32_bf16 a[80:83], v[84:87], v[132:135], a[80:83]\n" \
"buffer_load_dwordx4 v25, s[76:79], s85 offen lds\n" \
"s_mov_b32 s76, s60\n" \
"s_nop 0\n" \
"v_mfma_f32_16x16x32_bf16 a[80:83], v[88:91], v[136:139], a[80:83]\n" \
"s_add_u32 m0, %[mB], 0xc00\n" \
"s_mov_b32 s84, 0x418000\n" \
"v_mfma_f32_16x16x32_bf16 a[112:115], v[144:147], v[56:59], a[112:115]\n" \
"ds_read_b64_tr_b16 v[174:175], v35 offset:33792\n" \
"ds_read_b64_tr_b16 v[176:177], v35 offset:40960\n" \
"v_mfma_f32_16x16x32_bf16 a[112:115], v[140:143], v[52:55], a[112:115]\n" \
"buffer_load_dwordx4 v27, s[68:71], s84 offen lds\n" \
"v_mfma_f32_16x16x32_bf16 a[20:23], v[124:127], v[52:55], a[20:23]\n" \
"ds_read_b64_tr_b16 v[178:179], v35 offset:41984\n" \
"ds_read_b128 v[84:87], v48 offset:32768\n" \
"v_mfma_f32_16x16x32_bf16 a[20:23], v[128:131], v[56:59], a[20:23]\n" \
"s_add_u32 s88, s46, 0x0\n" \
"s_nop 0\n" \
"ds_read_b128 v[88:91], v49 offset:32768\n" \
"v_mfma_f32_16x16x32_bf16 a[52:55], v[120:123], v[56:59], a[52:55]\n" \
"buffer_store_dwordx4 a[96:99], v20, s[80:83], s88 offen offset:1024 sc1\n" \
"v_mfma_f32_16x16x32_bf16 a[52:55], v[116:119], v[52:55], a[52:55]\n" \
"ds_read_b128 v[108:111], v48 offset:38912\n" \
"ds_read_b128 v[112:115], v49 offset:38912\n" \
"v_mfma_f32_16x16x32_bf16 a[84:87], v[52:55], v[132:135], a[84:87]\n" \
"v_accvgpr_read_b32 v248, a16\n" \
"v_accvgpr_read_b32 v249, a17\n" \
"v_mfma_f32_16x16x32_bf16 a[84:87], v[56:59], v[136:139], a[84:87]\n" \
"v_accvgpr_read_b32 v250, a18\n" \
"v_accvgpr_read_b32 v251, a19\n" \
"v_mfma_f32_16x16x32_bf16 a[116:119], v[144:147], v[64:67], a[116:119]\n" \
"v_accvgpr_read_b32 v180, a48\n" \
"v_accvgpr_read_b32 v181, a49\n" \
"v_mfma_f32_16x16x32_bf16 a[116:119], v[140:143], v[60:63], a[116:119]\n" \
"v_accvgpr_read_b32 v182, a50\n" \
"v_accvgpr_read_b32 v183, a51\n" \
"v_mfma_f32_16x16x32_bf16 a[24:27], v[124:127], v[60:63], a[24:27]\n" \
"v_accvgpr_read_b32 v252, a80\n" \
"v_accvgpr_read_b32 v253, a81\n" \
"v_mfma_f32_16x16x32_bf16 a[24:27], v[128:131], v[64:67], a[24:27]\n" \
"v_accvgpr_read_b32 v254, a82\n" \
"v_accvgpr_read_b32 v255, a83\n" \
"L_pf8:\n" \
"s_cbranch_execz L_pf12\n" \
"s_nop 0\n" \
"v_mfma_f32_16x16x32_bf16 a[56:59], v[120:123], v[64:67], a[56:59]\n" \
"v_accvgpr_read_b32 v196, a112\n" \
"v_accvgpr_read_b32 v197, a113\n" \
"v_mfma_f32_16x16x32_bf16 a[56:59], v[116:119], v[60:63], a[56:59]\n" \
"s_add_u32 s89, s46, 0x1000\n" \
"v_accvgpr_read_b32 v198, a114\n" \
"v_mfma_f32_16x16x32_bf16 a[88:91], v[60:63], v[132:135], a[88:91]\n" \
"buffer_store_dwordx4 a[100:103], v20, s[80:83], s89 offen offset:1024 sc1\n" \
"s_add_u32 s89, s46, 0x3000\n" \
"v_mfma_f32_16x16x32_bf16 a[88:91], v[64:67], v[136:139], a[88:91]\n" \
"v_accvgpr_read_b32 v199, a115\n" \
"v_accvgpr_read_b32 v188, a20\n" \
"v_mfma_f32_16x16x32_bf16 a[120:123], v[144:147], v[72:75], a[120:123]\n" \
"v_accvgpr_read_b32 v189, a21\n" \
"v_accvgpr_read_b32 v190, a22\n" \
"v_mfma_f32_16x16x32_bf16 a[120:123], v[140:143], v[68:71], a[120:123]\n" \
"v_accvgpr_read_b32 v191, a23\n" \
"v_accvgpr_read_b32 v184, a52\n" \
"v_mfma_f32_16x16x32_bf16 a[28:31], v[124:127], v[68:71], a[28:31]\n" \
"v_accvgpr_read_b32 v185, a53\n" \
"v_accvgpr_read_b32 v186, a54\n" \
"v_mfma_f32_16x16x32_bf16 a[28:31], v[128:131], v[72:75], a[28:31]\n" \
"v_accvgpr_read_b32 v187, a55\n" \
"v_accvgpr_read_b32 v192, a84\n" \
"v_mfma_f32_16x16x32_bf16 a[60:63], v[120:123], v[72:75], a[60:63]\n" \
"v_accvgpr_read_b32 v193, a85\n" \
"v_accvgpr_read_b32 v194, a86\n" \
"v_mfma_f32_16x16x32_bf16 a[60:63], v[116:119], v[68:71], a[60:63]\n" \
"v_accvgpr_read_b32 v195, a87\n" \
"v_accvgpr_read_b32 v212, a116\n" \
"v_mfma_f32_16x16x32_bf16 a[92:95], v[68:71], v[132:135], a[92:95]\n" \
"v_accvgpr_read_b32 v213, a117\n" \
"v_accvgpr_read_b32 v214, a118\n" \
"v_mfma_f32_16x16x32_bf16 a[92:95], v[72:75], v[136:139], a[92:95]\n" \
"s_add_u32 s88, s46, 0x2000\n" \
"v_accvgpr_read_b32 v215, a119\n" \
"v_mfma_f32_16x16x32_bf16 a[124:127], v[144:147], v[80:83], a[124:127]\n" \
"buffer_store_dwordx4 a[8:11], v20, s[80:83], s88 offen offset:1024 sc1\n" \
"s_add_u32 s88, s48, 0x0\n" \
"s_nop 0\n" \
"v_mfma_f32_16x16x32_bf16 a[124:127], v[140:143], v[76:79], a[124:127]\n" \
"v_accvgpr_read_b32 v204, a24\n" \
"v_accvgpr_read_b32 v205, a25\n" \
"v_mfma_f32_16x16x32_bf16 a[0:3], v[124:127], v[76:79], a[0:3]\n" \
"v_accvgpr_read_b32 v206, a26\n" \
"v_accvgpr_read_b32 v207, a27\n" \
"v_mfma_f32_16x16x32_bf16 a[0:3], v[128:131], v[80:83], a[0:3]\n" \
"v_accvgpr_read_b32 v200, a56\n" \
"v_accvgpr_read_b32 v201, a57\n" \
"v_mfma_f32_16x16x32_bf16 a[4:7], v[120:123], v[80:83], a[4:7]\n" \
"v_accvgpr_read_b32 v202, a58\n" \
"v_accvgpr_read_b32 v203, a59\n" \
"v_mfma_f32_16x16x32_bf16 a[4:7], v[116:119], v[76:79], a[4:7]\n" \
"v_accvgpr_read_b32 v208, a88\n" \
"v_accvgpr_read_b32 v209, a89\n" \
"v_mfma_f32_16x16x32_bf16 a[32:35], v[76:79], v[132:135], a[32:35]\n" \
"v_accvgpr_read_b32 v210, a90\n" \
"v_accvgpr_read_b32 v211, a91\n" \
"v_mfma_f32_16x16x32_bf16 a[32:35], v[80:83], v[136:139], a[32:35]\n" \
"v_accvgpr_read_b32 v228, a120\n" \
"s_waitcnt lgkmcnt(0)\n" \
"v_mfma_f32_16x16x32_bf16 a[128:131], v[152:155], v[56:59], a[128:131]\n" \
"v_accvgpr_read_b32 v229, a121\n" \
"v_accvgpr_read_b32 v230, a122\n" \
"v_mfma_f32_16x16x32_bf16 a[128:131], v[148:151], v[52:55], a[128:131]\n" \
"v_lshlrev_b32_e32 v45, 10, v20\n" \
"v_add_u32_e64 v45, s44, v45\n" \
"v_mfma_f32_16x16x32_bf16 a[132:135], v[156:159], v[52:55], a[132:135]\n" \
"s_mov_b32 m0, %[mS]\n" \
"s_nop 0\n" \
"v_accvgpr_read_b32 v231, a123\n" \
"v_mfma_f32_16x16x32_bf16 a[132:135], v[160:163], v[56:59], a[132:135]\n" \
"v_accvgpr_read_b32 v220, a28\n" \
"v_accvgpr_read_b32 v221, a29\n" \
"v_mfma_f32_16x16x32_bf16 a[136:139], v[168:171], v[56:59], a[136:139]\n" \
"v_accvgpr_read_b32 v222, a30\n" \
"v_accvgpr_read_b32 v223, a31\n" \
"v_mfma_f32_16x16x32_bf16 a[136:139], v[164:167], v[52:55], a[136:139]\n" \
"v_accvgpr_read_b32 v216, a60\n" \
"v_accvgpr_read_b32 v217, a61\n" \
"v_mfma_f32_16x16x32_bf16 a[140:143], v[172:175], v[52:55], a[140:143]\n" \
"v_accvgpr_read_b32 v218, a62\n" \
"v_accvgpr_read_b32 v219, a63\n" \
"s_getpc_b64 s[86:87]\n" \
"L_ct15_%=:\n" \
"s_add_u32 s86, s86, L_end_%=-L_ct15_%=\n" \
"s_addc_u32 s87, s87, 0\n" \
"v_mfma_f32_16x16x32_bf16 a[140:143], v[176:179], v[56:59], a[140:143]\n" \
"v_accvgpr_read_b32 v224, a92\n" \
"v_accvgpr_read_b32 v225, a93\n" \
"s_mov_b64 exec, 3\n" \
"global_load_lds_dword v45, s[86:87]\n" \
"s_mov_b64 exec, -1\n" \
"s_sub_u32 m0, %[mB], 0x10000\n" \
"v_mfma_f32_16x16x32_bf16 a[160:163], v[152:155], v[64:67], a[160:163]\n" \
"v_accvgpr_read_b32 v226, a94\n" \
"v_accvgpr_read_b32 v227, a95\n" \
"v_mfma_f32_16x16x32_bf16 a[160:163], v[148:151], v[60:63], a[160:163]\n" \
"v_accvgpr_read_b32 v244, a124\n" \
"v_accvgpr_read_b32 v245, a125\n" \
"v_mfma_f32_16x16x32_bf16 a[164:167], v[156:159], v[60:63], a[164:167]\n" \
"v_accvgpr_read_b32 v246, a126\n" \
"v_accvgpr_read_b32 v247, a127\n" \
"v_mfma_f32_16x16x32_bf16 a[164:167], v[160:163], v[64:67], a[164:167]\n" \
"v_accvgpr_read_b32 v236, a0\n" \
"v_accvgpr_read_b32 v237, a1\n" \
"v_mfma_f32_16x16x32_bf16 a[168:171], v[168:171], v[64:67], a[168:171]\n" \
"v_accvgpr_read_b32 v238, a2\n" \
"v_accvgpr_read_b32 v239, a3\n" \
"v_mfma_f32_16x16x32_bf16 a[168:171], v[164:167], v[60:63], a[168:171]\n" \
"v_accvgpr_read_b32 v232, a4\n" \
"v_accvgpr_read_b32 v233, a5\n" \
"v_mfma_f32_16x16x32_bf16 a[172:175], v[172:175], v[60:63], a[172:175]\n" \
"v_accvgpr_read_b32 v234, a6\n" \
"v_accvgpr_read_b32 v235, a7\n" \
"v_mfma_f32_16x16x32_bf16 a[172:175], v[176:179], v[64:67], a[172:175]\n" \
"v_accvgpr_read_b32 v240, a32\n" \
"v_accvgpr_read_b32 v241, a33\n" \
"v_mfma_f32_16x16x32_bf16 a[192:195], v[152:155], v[72:75], a[192:195]\n" \
"v_accvgpr_read_b32 v242, a34\n" \
"v_accvgpr_read_b32 v243, a35\n" \
"v_mfma_f32_16x16x32_bf16 a[192:195], v[148:151], v[68:71], a[192:195]\n" \
"s_mov_b32 s68, 0xffc0fffc\n" \
"s_mov_b32 s69, 0xc000fc00\n" \
"s_waitcnt vmcnt(4)\n" \
"s_barrier\n" \
"v_mfma_f32_16x16x32_bf16 a[196:199], v[156:159], v[68:71], a[196:199]\n" \
"ds_read_b64_tr_b16 v[144:145], v39 offset:4096\n" \
"ds_read_b64_tr_b16 v[146:147], v39 offset:4608\n" \
"v_mfma_f32_16x16x32_bf16 a[196:199], v[160:163], v[72:75], a[196:199]\n" \
"ds_read_b64_tr_b16 v[140:141], v39 offset:0\n" \
"ds_read_b64_tr_b16 v[142:143], v39 offset:512\n" \
"v_mfma_f32_16x16x32_bf16 a[200:203], v[168:171], v[72:75], a[200:203]\n" \
"ds_read_b64_tr_b16 v[124:125], v37 offset:0\n" \
"ds_read_b64_tr_b16 v[126:127], v37 offset:512\n" \
"v_mfma_f32_16x16x32_bf16 a[200:203], v[164:167], v[68:71], a[200:203]\n" \
"ds_read_b64_tr_b16 v[128:129], v37 offset:4096\n" \
"ds_read_b64_tr_b16 v[130:131], v37 offset:4608\n" \
"v_mfma_f32_16x16x32_bf16 a[204:207], v[172:175], v[68:71], a[204:207]\n" \
"ds_read_b64_tr_b16 v[136:137], v38 offset:4096\n" \
"ds_read_b64_tr_b16 v[138:139], v38 offset:4608\n" \
"v_mfma_f32_16x16x32_bf16 a[204:207], v[176:179], v[72:75], a[204:207]\n" \
"ds_read_b64_tr_b16 v[132:133], v38 offset:0\n" \
"ds_read_b64_tr_b16 v[134:135], v38 offset:512\n" \
"v_mfma_f32_16x16x32_bf16 a[224:227], v[152:155], v[80:83], a[224:227]\n" \
"ds_read_b128 v[52:55], v28 offset:0\n" \
"ds_read_b128 v[56:59], v29 offset:0\n" \
"v_mfma_f32_16x16x32_bf16 a[224:227], v[148:151], v[76:79], a[224:227]\n" \
"ds_read_b64_tr_b16 v[116:117], v36 offset:0\n" \
"ds_read_b64_tr_b16 v[118:119], v36 offset:512\n" \
"v_mfma_f32_16x16x32_bf16 a[228:231], v[156:159], v[76:79], a[228:231]\n" \
"ds_read_b64_tr_b16 v[120:121], v36 offset:4096\n" \
"ds_read_b64_tr_b16 v[122:123], v36 offset:4608\n" \
"v_mfma_f32_16x16x32_bf16 a[228:231], v[160:163], v[80:83], a[228:231]\n" \
"ds_read_b128 v[60:63], v28 offset:2048\n" \
"ds_read_b128 v[64:67], v29 offset:2048\n" \
"v_mfma_f32_16x16x32_bf16 a[232:235], v[168:171], v[80:83], a[232:235]\n" \
"ds_read_b128 v[68:71], v28 offset:4096\n" \
"ds_read_b128 v[72:75], v29 offset:4096\n" \
"v_mfma_f32_16x16x32_bf16 a[232:235], v[164:167], v[76:79], a[232:235]\n" \
"v_lshrrev_b32_e32 v25, 8, v20\n" \
"v_lshrrev_b32_e32 v24, 9, v20\n" \
"v_mfma_f32_16x16x32_bf16 a[236:239], v[172:175], v[76:79], a[236:239]\n" \
"ds_read_b128 v[76:79], v28 offset:6144\n" \
"v_mul_u32_u24_e32 v25, 0x24700, v25\n" \
"v_mfma_f32_16x16x32_bf16 a[236:239], v[176:179], v[80:83], a[236:239]\n" \
"ds_read_b128 v[80:83], v29 offset:6144\n" \
"v_lshl_add_u32 v24, v24, 3, v26\n" \
"v_mfma_f32_16x16x32_bf16 a[144:147], v[152:155], v[88:91], a[144:147]\n" \
"v_add_u32_e32 v25, v25, v20\n" \
"s_add_u32 s84, s56, 0x0\n" \
"v_mfma_f32_16x16x32_bf16 a[144:147], v[148:151], v[84:87], a[144:147]\n" \
"s_add_u32 s85, s56, 0x24800\n" \
"s_add_u32 s86, s56, 0x49000\n" \
"v_mfma_f32_16x16x32_bf16 a[148:151], v[156:159], v[84:87], a[148:151]\n" \
"s_add_u32 s87, s56, 0x6d800\n" \
"s_mov_b32 s70, 0x3e0293ee\n" \
"v_mfma_f32_16x16x32_bf16 a[148:151], v[160:163], v[88:91], a[148:151]\n" \
"s_mov_b32 s71, 0x3e0293ee\n" \
"v_add_u32_e64 v27, s53, v20\n" \
"v_mfma_f32_16x16x32_bf16 a[152:155], v[168:171], v[88:91], a[152:155]\n" \
"v_lshrrev_b32_e32 v26, 2, v20\n" \
"v_add_u32_e32 v26, s52, v26\n" \
"v_mfma_f32_16x16x32_bf16 a[152:155], v[164:167], v[84:87], a[152:155]\n" \
"v_mfma_f32_16x16x32_bf16 a[156:159], v[172:175], v[84:87], a[156:159]\n" \
"ds_read_b128 v[84:87], v22 offset:0\n" \
"v_mfma_f32_16x16x32_bf16 a[156:159], v[176:179], v[88:91], a[156:159]\n" \
"ds_read_b128 v[88:91], v23 offset:0\n" \
"v_mfma_f32_16x16x32_bf16 a[176:179], v[152:155], v[96:99], a[176:179]\n" \
"v_mfma_f32_16x16x32_bf16 a[176:179], v[148:151], v[92:95], a[176:179]\n" \
"v_mfma_f32_16x16x32_bf16 a[180:183], v[156:159], v[92:95], a[180:183]\n" \
"v_mfma_f32_16x16x32_bf16 a[180:183], v[160:163], v[96:99], a[180:183]\n" \
"v_mfma_f32_16x16x32_bf16 a[184:187], v[168:171], v[96:99], a[184:187]\n" \
"v_mfma_f32_16x16x32_bf16 a[184:187], v[164:167], v[92:95], a[184:187]\n" \
"v_mfma_f32_16x16x32_bf16 a[188:191], v[172:175], v[92:95], a[188:191]\n" \
"ds_read_b128 v[92:95], v48 offset:2048\n" \
"v_mfma_f32_16x16x32_bf16 a[188:191], v[176:179], v[96:99], a[188:191]\n" \
"ds_read_b128 v[96:99], v49 offset:2048\n" \
"v_mfma_f32_16x16x32_bf16 a[208:211], v[152:155], v[104:107], a[208:211]\n" \
"v_mfma_f32_16x16x32_bf16 a[208:211], v[148:151], v[100:103], a[208:211]\n" \
"v_mfma_f32_16x16x32_bf16 a[212:215], v[156:159], v[100:103], a[212:215]\n" \
"v_mfma_f32_16x16x32_bf16 a[212:215], v[160:163], v[104:107], a[212:215]\n" \
"v_mfma_f32_16x16x32_bf16 a[216:219], v[168:171], v[104:107], a[216:219]\n" \
"v_mfma_f32_16x16x32_bf16 a[216:219], v[164:167], v[100:103], a[216:219]\n" \
"v_mfma_f32_16x16x32_bf16 a[220:223], v[172:175], v[100:103], a[220:223]\n" \
"ds_read_b128 v[100:103], v48 offset:4096\n" \
"v_mfma_f32_16x16x32_bf16 a[220:223], v[176:179], v[104:107], a[220:223]\n" \
"ds_read_b128 v[104:107], v49 offset:4096\n" \
"v_mfma_f32_16x16x32_bf16 a[240:243], v[152:155], v[112:115], a[240:243]\n" \
"ds_read_b64_tr_b16 v[152:153], v32 offset:8192\n" \
"ds_read_b64_tr_b16 v[154:155], v32 offset:9216\n" \
"v_mfma_f32_16x16x32_bf16 a[240:243], v[148:151], v[108:111], a[240:243]\n" \
"ds_read_b64_tr_b16 v[148:149], v32 offset:0\n" \
"ds_read_b64_tr_b16 v[150:151], v32 offset:1024\n" \
"v_mfma_f32_16x16x32_bf16 a[244:247], v[156:159], v[108:111], a[244:247]\n" \
"ds_read_b64_tr_b16 v[156:157], v33 offset:0\n" \
"ds_read_b64_tr_b16 v[158:159], v33 offset:1024\n" \
"v_mfma_f32_16x16x32_bf16 a[244:247], v[160:163], v[112:115], a[244:247]\n" \
"ds_read_b64_tr_b16 v[160:161], v33 offset:8192\n" \
"ds_read_b64_tr_b16 v[162:163], v33 offset:9216\n" \
"v_mfma_f32_16x16x32_bf16 a[248:251], v[168:171], v[112:115], a[248:251]\n" \
"ds_read_b64_tr_b16 v[168:169], v34 offset:8192\n" \
"ds_read_b64_tr_b16 v[170:171], v34 offset:9216\n" \
"v_mfma_f32_16x16x32_bf16 a[248:251], v[164:167], v[108:111], a[248:251]\n" \
"ds_read_b64_tr_b16 v[164:165], v34 offset:0\n" \
"ds_read_b64_tr_b16 v[166:167], v34 offset:1024\n" \
"v_mfma_f32_16x16x32_bf16 a[252:255], v[172:175], v[108:111], a[252:255]\n" \
"ds_read_b64_tr_b16 v[172:173], v35 offset:0\n" \
"ds_read_b64_tr_b16 v[174:175], v35 offset:1024\n" \
"v_mfma_f32_16x16x32_bf16 a[252:255], v[176:179], v[112:115], a[252:255]\n" \
"ds_read_b64_tr_b16 v[176:177], v35 offset:8192\n" \
"s_waitcnt lgkmcnt(15)\n" \
"s_nop 0\n" \
"v_mfma_f32_16x16x32_bf16 v[248:251], v[144:147], v[88:91], v[248:251]\n" \
"v_bfe_u32 v44, v20, 2, 5\n" \
"ds_read_b64_tr_b16 v[178:179], v35 offset:9216\n" \
"v_mfma_f32_16x16x32_bf16 v[248:251], v[140:143], v[84:87], v[248:251]\n" \
"buffer_load_dword v44, v44, s[80:83], s42 offen offset:0 sc1\n" \
"v_mfma_f32_16x16x32_bf16 v[180:183], v[124:127], v[84:87], v[180:183]\n" \
"ds_read_b128 v[108:111], v48 offset:6144\n" \
"ds_read_b128 v[112:115], v49 offset:6144\n" \
"v_mfma_f32_16x16x32_bf16 v[180:183], v[128:131], v[88:91], v[180:183]\n" \
"v_mfma_f32_16x16x32_bf16 v[252:255], v[88:91], v[136:139], v[252:255]\n" \
"ds_read_b128 v[88:91], v49 offset:0\n" \
"v_mfma_f32_16x16x32_bf16 v[252:255], v[84:87], v[132:135], v[252:255]\n" \
"ds_read_b128 v[84:87], v48 offset:0\n" \
"v_mfma_f32_16x16x32_bf16 v[196:199], v[140:143], v[52:55], v[196:199]\n" \
"v_mfma_f32_16x16x32_bf16 v[196:199], v[144:147], v[56:59], v[196:199]\n" \
"v_mfma_f32_16x16x32_bf16 v[188:191], v[128:131], v[56:59], v[188:191]\n" \
"v_mfma_f32_16x16x32_bf16 v[188:191], v[124:127], v[52:55], v[188:191]\n" \
"v_cvt_pk_bf16_f32 v180, v180, v181\n" \
"v_cvt_pk_bf16_f32 v181, v182, v183\n" \
"v_mfma_f32_16x16x32_bf16 v[184:187], v[116:119], v[52:55], v[184:187]\n" \
"v_cvt_pk_bf16_f32 v182, v250, v251\n" \
"v_mov_b32_e32 v183, 0x3f80\n" \
"v_mfma_f32_16x16x32_bf16 v[184:187], v[120:123], v[56:59], v[184:187]\n" \
"v_mfma_f32_16x16x32_bf16 v[192:195], v[56:59], v[136:139], v[192:195]\n" \
"v_mfma_f32_16x16x16_bf16 v[248:251], v[182:183], v[46:47], 0\n" \
"v_cvt_pk_bf16_f32 v188, v188, v189\n" \
"v_cvt_pk_bf16_f32 v189, v190, v191\n" \
"v_mfma_f32_16x16x32_bf16 v[192:195], v[52:55], v[132:135], v[192:195]\n" \
"v_cvt_pk_bf16_f32 v190, v198, v199\n" \
"v_mov_b32_e32 v191, 0x3f80\n" \
"v_mfma_f32_16x16x32_bf16 v[212:215], v[140:143], v[60:63], v[212:215]\n" \
"v_cvt_pk_bf16_f32 v184, v184, v185\n" \
"v_cvt_pk_bf16_f32 v185, v186, v187\n" \
"v_mfma_f32_16x16x32_bf16 v[212:215], v[144:147], v[64:67], v[212:215]\n" \
"v_cvt_pk_bf16_f32 v186, v196, v197\n" \
"v_mov_b32_e64 v187, 0\n" \
"v_mfma_f32_16x16x32_bf16 v[204:207], v[128:131], v[64:67], v[204:207]\n" \
"v_mfma_f32_16x16x32_bf16 v[204:207], v[124:127], v[60:63], v[204:207]\n" \
"v_mfma_f32_16x16x32_bf16 v[200:203], v[116:119], v[60:63], v[200:203]\n" \
"v_mfma_f32_16x16x32_bf16 v[200:203], v[120:123], v[64:67], v[200:203]\n" \
"v_mfma_f32_16x16x32_bf16 v[208:211], v[64:67], v[136:139], v[208:211]\n" \
"v_mfma_f32_16x16x32_bf16 v[208:211], v[60:63], v[132:135], v[208:211]\n" \
"v_mfma_f32_16x16x32_bf16 v[228:231], v[140:143], v[68:71], v[228:231]\n" \
"v_mfma_f32_16x16x32_bf16 v[228:231], v[144:147], v[72:75], v[228:231]\n" \
"v_mfma_f32_16x16x32_bf16 v[220:223], v[128:131], v[72:75], v[220:223]\n" \
"v_mfma_f32_16x16x32_bf16 v[220:223], v[124:127], v[68:71], v[220:223]\n" \
"v_cvt_pk_bf16_f32 v204, v204, v205\n" \
"v_cvt_pk_bf16_f32 v205, v206, v207\n" \
"v_mfma_f32_16x16x32_bf16 v[216:219], v[116:119], v[68:71], v[216:219]\n" \
"v_cvt_pk_bf16_f32 v200, v200, v201\n" \
"v_cvt_pk_bf16_f32 v201, v202, v203\n" \
"v_mfma_f32_16x16x32_bf16 v[216:219], v[120:123], v[72:75], v[216:219]\n" \
"v_cvt_pk_bf16_f32 v206, v214, v215\n" \
"v_mov_b32_e32 v207, 0x3f80\n" \
"v_mfma_f32_16x16x32_bf16 v[224:227], v[72:75], v[136:139], v[224:227]\n" \
"v_cvt_pk_bf16_f32 v220, v220, v221\n" \
"v_cvt_pk_bf16_f32 v221, v222, v223\n" \
"v_mfma_f32_16x16x32_bf16 v[224:227], v[68:71], v[132:135], v[224:227]\n" \
"v_cvt_pk_bf16_f32 v202, v212, v213\n" \
"v_mov_b32_e64 v203, 0\n" \
"v_mfma_f32_16x16x32_bf16 v[244:247], v[140:143], v[76:79], v[244:247]\n" \
"v_cvt_pk_bf16_f32 v216, v216, v217\n" \
"v_cvt_pk_bf16_f32 v217, v218, v219\n" \
"v_mfma_f32_16x16x32_bf16 v[244:247], v[144:147], v[80:83], v[244:247]\n" \
"v_cvt_pk_bf16_f32 v222, v230, v231\n" \
"v_mov_b32_e32 v223, 0x3f80\n" \
"v_mfma_f32_16x16x32_bf16 v[236:239], v[128:131], v[80:83], v[236:239]\n" \
"v_cvt_pk_bf16_f32 v128, v208, v209\n" \
"v_cvt_pk_bf16_f32 v129, v210, v211\n" \
"v_mfma_f32_16x16x32_bf16 v[236:239], v[124:127], v[76:79], v[236:239]\n" \
"v_cvt_pk_bf16_f32 v126, v192, v193\n" \
"v_cvt_pk_bf16_f32 v127, v194, v195\n" \
"v_mfma_f32_16x16x32_bf16 v[232:235], v[116:119], v[76:79], v[232:235]\n" \
"v_cvt_pk_bf16_f32 v218, v228, v229\n" \
"v_mov_b32_e64 v219, 0\n" \
"v_mfma_f32_16x16x32_bf16 v[232:235], v[120:123], v[80:83], v[232:235]\n" \
"v_cvt_pk_bf16_f32 v130, v224, v225\n" \
"v_cvt_pk_bf16_f32 v131, v226, v227\n" \
"v_mfma_f32_16x16x32_bf16 v[240:243], v[80:83], v[136:139], v[240:243]\n" \
"v_cvt_pk_bf16_f32 v236, v236, v237\n" \
"v_cvt_pk_bf16_f32 v237, v238, v239\n" \
"v_mfma_f32_16x16x32_bf16 v[240:243], v[76:79], v[132:135], v[240:243]\n" \
"v_cvt_pk_bf16_f32 v238, v246, v247\n" \
"v_mov_b32_e32 v239, 0x3f80\n" \
"s_waitcnt lgkmcnt(0)\n" \
"s_barrier\n" \
"v_mfma_f32_16x16x32_bf16 a[128:131], v[148:151], v[52:55], a[128:131]\n" \
"v_cvt_pk_bf16_f32 v232, v232, v233\n" \
"v_cvt_pk_bf16_f32 v233, v234, v235\n" \
"v_mfma_f32_16x16x32_bf16 a[128:131], v[152:155], v[56:59], a[128:131]\n" \
"v_cvt_pk_bf16_f32 v234, v244, v245\n" \
"v_mov_b32_e64 v235, 0\n" \
"v_mfma_f32_16x16x32_bf16 a[132:135], v[160:163], v[56:59], a[132:135]\n" \
"v_cvt_pk_bf16_f32 v132, v240, v241\n" \
"v_cvt_pk_bf16_f32 v133, v242, v243\n" \
"v_mfma_f32_16x16x32_bf16 a[132:135], v[156:159], v[52:55], a[132:135]\n" \
"v_mfma_f32_16x16x32_bf16 v[116:119], v[180:183], v[184:187], 0\n" \
"v_mfma_f32_16x16x32_bf16 v[192:195], v[188:191], v[184:187], 0\n" \
"s_waitcnt vmcnt(0)\n" \
"v_cmp_ne_u32_e32 vcc, %[tag], v44\n" \
"v_mfma_f32_16x16x32_bf16 a[136:139], v[164:167], v[52:55], a[136:139]\n" \
"v_mfma_f32_16x16x32_bf16 v[208:211], v[204:207], v[200:203], 0\n" \
"v_mfma_f32_16x16x32_bf16 v[120:123], v[188:191], v[200:203], 0\n" \
"v_mfma_f32_16x16x32_bf16 v[224:227], v[220:223], v[216:219], 0\n" \
"v_mfma_f32_16x16x32_bf16 v[36:39], v[204:207], v[216:219], 0\n" \
"s_cbranch_vccnz L_pa_%=\n" \
"L_ra_%=:\n" \
"v_cndmask_b32_e64 v116, v116, v117, s[64:65]\n" \
"buffer_load_dwordx4 v20, s[80:83], s73 offen offset:0 sc1 lds\n" \
"v_mfma_f32_16x16x32_bf16 a[136:139], v[168:171], v[56:59], a[136:139]\n" \
"v_mfma_f32_16x16x32_bf16 v[240:243], v[236:239], v[232:235], 0\n" \
"v_mfma_f32_16x16x32_bf16 v[48:51], v[220:223], v[232:235], 0\n" \
"v_cndmask_b32_e64 v118, v118, v119, s[64:65]\n" \
"v_cndmask_b32_e64 v192, v192, v193, s[64:65]\n" \
"v_mfma_f32_16x16x32_bf16 a[140:143], v[176:179], v[56:59], a[140:143]\n" \
"buffer_load_dwordx4 v20, s[80:83], s73 offen offset:1024 sc1 lds\n" \
"v_mfma_f32_16x16x32_bf16 a[140:143], v[172:175], v[52:55], a[140:143]\n" \
"v_add_f32_e32 v116, s55, v116\n" \
"v_cndmask_b32_e64 v194, v194, v195, s[64:65]\n" \
"v_mfma_f32_16x16x32_bf16 a[160:163], v[148:151], v[60:63], a[160:163]\n" \
"v_add_f32_e64 v118, s55, v118\n" \
"v_cndmask_b32_e64 v208, v208, v209, s[64:65]\n" \
"v_mfma_f32_16x16x32_bf16 a[160:163], v[152:155], v[64:67], a[160:163]\n" \
"v_cndmask_b32_e64 v120, v120, v121, s[64:65]\n" \
"v_cndmask_b32_e64 v210, v210, v211, s[64:65]\n" \
"v_mfma_f32_16x16x32_bf16 a[164:167], v[160:163], v[64:67], a[164:167]\n" \
"buffer_load_dwordx4 v20, s[80:83], s73 offen offset:2048 sc1 lds\n" \
"v_mfma_f32_16x16x32_bf16 a[164:167], v[156:159], v[60:63], a[164:167]\n" \
"v_cndmask_b32_e64 v122, v122, v123, s[64:65]\n" \
"v_cndmask_b32_e64 v224, v224, v225, s[64:65]\n" \
"v_mfma_f32_16x16x32_bf16 a[168:171], v[164:167], v[60:63], a[168:171]\n" \
"v_cndmask_b32_e64 v36, v36, v37, s[64:65]\n" \
"v_cndmask_b32_e64 v226, v226, v227, s[64:65]\n" \
"v_mfma_f32_16x16x32_bf16 a[168:171], v[168:171], v[64:67], a[168:171]\n" \
"v_cndmask_b32_e64 v38, v38, v39, s[64:65]\n" \
"v_cndmask_b32_e64 v240, v240, v241, s[64:65]\n" \
"v_mfma_f32_16x16x32_bf16 a[172:175], v[176:179], v[64:67], a[172:175]\n" \
"buffer_load_dwordx4 v20, s[80:83], s73 offen offset:3072 sc1 lds\n" \
"v_mfma_f32_16x16x32_bf16 a[172:175], v[172:175], v[60:63], a[172:175]\n" \
"s_sub_u32 m0, %[mB], s59\n" \
"s_nop 0\n" \
"v_cndmask_b32_e64 v48, v48, v49, s[64:65]\n" \
"v_mfma_f32_16x16x32_bf16 a[192:195], v[148:151], v[68:71], a[192:195]\n" \
"v_cndmask_b32_e64 v242, v242, v243, s[64:65]\n" \
"v_cndmask_b32_e64 v50, v50, v51, s[64:65]\n" \
"v_mfma_f32_16x16x32_bf16 a[192:195], v[152:155], v[72:75], a[192:195]\n" \
"v_cndmask_b32_e64 v192, v116, v192, s[66:67]\n" \
"v_cndmask_b32_e64 v194, v118, v194, s[68:69]\n" \
"v_mfma_f32_16x16x32_bf16 a[196:199], v[160:163], v[72:75], a[196:199]\n" \
"buffer_load_dwordx4 v20, s[80:83], s74 offen offset:0 sc1 lds\n" \
"v_mfma_f32_16x16x32_bf16 a[196:199], v[156:159], v[68:71], a[196:199]\n" \
"v_cndmask_b32_e64 v208, v120, v208, s[66:67]\n" \
"v_cndmask_b32_e64 v210, v122, v210, s[68:69]\n" \
"v_mfma_f32_16x16x32_bf16 a[200:203], v[164:167], v[68:71], a[200:203]\n" \
"v_cndmask_b32_e64 v224, v36, v224, s[66:67]\n" \
"v_cndmask_b32_e64 v226, v38, v226, s[68:69]\n" \
"v_mfma_f32_16x16x32_bf16 a[200:203], v[168:171], v[72:75], a[200:203]\n" \
"v_cndmask_b32_e64 v240, v48, v240, s[66:67]\n" \
"v_cndmask_b32_e64 v242, v50, v242, s[68:69]\n" \
"v_mfma_f32_16x16x32_bf16 a[204:207], v[176:179], v[72:75], a[204:207]\n" \
"buffer_load_dwordx4 v20, s[80:83], s74 offen offset:1024 sc1 lds\n" \
"v_mfma_f32_16x16x32_bf16 a[204:207], v[172:175], v[68:71], a[204:207]\n" \
"v_max_f32_e32 v193, v192, v194\n" \
"v_max_f32_e32 v209, v208, v210\n" \
"v_mfma_f32_16x16x32_bf16 a[224:227], v[148:151], v[76:79], a[224:227]\n" \
"v_max_f32_e32 v225, v224, v226\n" \
"v_max_f32_e32 v241, v240, v242\n" \
"v_mfma_f32_16x16x32_bf16 a[224:227], v[152:155], v[80:83], a[224:227]\n" \
"v_permlane32_swap_b32_e32 v193, v225\n" \
"v_mfma_f32_16x16x32_bf16 a[228:231], v[160:163], v[80:83], a[228:231]\n" \
"buffer_load_dwordx4 v20, s[80:83], s74 offen offset:2048 sc1 lds\n" \
"v_mfma_f32_16x16x32_bf16 a[228:231], v[156:159], v[76:79], a[228:231]\n" \
"v_permlane32_swap_b32_e32 v209, v241\n" \
"v_mfma_f32_16x16x32_bf16 a[232:235], v[164:167], v[76:79], a[232:235]\n" \
"v_max_f32_e32 v193, v193, v225\n" \
"v_max_f32_e32 v209, v209, v241\n" \
"v_mfma_f32_16x16x32_bf16 a[232:235], v[168:171], v[80:83], a[232:235]\n" \
"v_permlane32_swap_b32_e32 v196, v228\n" \
"v_mfma_f32_16x16x32_bf16 a[236:239], v[176:179], v[80:83], a[236:239]\n" \
"buffer_load_dwordx4 v20, s[80:83], s74 offen offset:3072 sc1 lds\n" \
"v_mfma_f32_16x16x32_bf16 a[236:239], v[172:175], v[76:79], a[236:239]\n" \
"s_sub_u32 m0, %[mB], 0x4000\n" \
"v_cvt_pk_bf16_f32 v134, v248, v249\n" \
"v_permlane32_swap_b32_e32 v212, v244\n" \
"v_permlane16_swap_b32_e32 v193, v209\n" \
"v_mfma_f32_16x16x32_bf16 a[144:147], v[148:151], v[84:87], a[144:147]\n" \
"v_permlane16_swap_b32_e32 v228, v244\n" \
"v_mfma_f32_16x16x32_bf16 a[144:147], v[152:155], v[88:91], a[144:147]\n" \
"buffer_load_dwordx4 v20, s[80:83], s75 offen offset:0 sc1 lds\n" \
"v_mfma_f32_16x16x32_bf16 a[148:151], v[160:163], v[88:91], a[148:151]\n" \
"v_max_f32_e32 v193, v193, v209\n" \
"v_mul_f32_e32 v193, s72, v193\n" \
"v_mfma_f32_16x16x32_bf16 a[148:151], v[156:159], v[84:87], a[148:151]\n" \
"v_mul_f32_e32 v45, 0xbfb8aa3b, v228\n" \
"v_mov_b32_e64 v209, v193\n" \
"v_mfma_f32_16x16x16_bf16 v[196:199], v[190:191], v[46:47], 0\n" \
"v_mfma_f32_16x16x16_bf16 v[212:215], v[206:207], v[46:47], 0\n" \
"buffer_load_dwordx4 v20, s[80:83], s75 offen offset:1024 sc1 lds\n" \
"v_mfma_f32_16x16x16_bf16 v[228:231], v[222:223], v[46:47], 0\n" \
"v_permlane16_swap_b32_e32 v193, v209\n" \
"v_mfma_f32_16x16x16_bf16 v[244:247], v[238:239], v[46:47], 0\n" \
"v_mov_b32_e32 v225, v193\n" \
"v_mov_b32_e64 v241, v209\n" \
"v_mfma_f32_16x16x32_bf16 a[152:155], v[164:167], v[84:87], a[152:155]\n" \
"v_permlane32_swap_b32_e32 v193, v225\n" \
"v_permlane32_swap_b32_e32 v209, v241\n" \
"buffer_load_dwordx4 v20, s[80:83], s75 offen offset:2048 sc1 lds\n" \
"v_mfma_f32_16x16x32_bf16 a[152:155], v[168:171], v[88:91], a[152:155]\n" \
"v_fma_f32 v192, v192, s72, -v193\n" \
"v_fma_f32 v194, v194, s72, -v193\n" \
"v_mfma_f32_16x16x32_bf16 a[156:159], v[176:179], v[88:91], a[156:159]\n" \
"v_fma_f32 v208, v208, s72, -v209\n" \
"v_fma_f32 v210, v210, s72, -v209\n" \
"v_exp_f32_e32 v192, v192\n" \
"v_exp_f32_e32 v194, v194\n" \
"v_exp_f32_e64 v208, v208\n" \
"buffer_load_dwordx4 v20, s[80:83], s75 offen offset:3072 sc1 lds\n" \
"v_mfma_f32_16x16x32_bf16 a[156:159], v[172:175], v[84:87], a[156:159]\n" \
"s_mov_b32 m0, s61\n" \
"v_fma_f32 v224, v224, s72, -v225\n" \
"v_exp_f32_e32 v210, v210\n" \
"v_mfma_f32_16x16x32_bf16 a[176:179], v[148:151], v[92:95], a[176:179]\n" \
"v_fma_f32 v226, v226, s72, -v225\n" \
"v_fma_f32 v240, v240, s72, -v241\n" \
"v_exp_f32_e32 v224, v224\n" \
"v_exp_f32_e32 v226, v226\n" \
"buffer_load_dwordx4 v20, s[80:83], s76 offen offset:0 sc1 lds\n" \
"v_mfma_f32_16x16x32_bf16 a[176:179], v[152:155], v[96:99], a[176:179]\n" \
"v_fma_f32 v242, v242, s72, -v241\n" \
"v_cvt_pk_bf16_f32 v192, v192, v194\n" \
"v_mfma_f32_16x16x32_bf16 a[180:183], v[160:163], v[96:99], a[180:183]\n" \
"v_cvt_pk_bf16_f32 v208, v208, v210\n" \
"v_cvt_pk_bf16_f32 v135, v250, v251\n" \
"v_exp_f32_e32 v240, v240\n" \
"v_exp_f32_e32 v242, v242\n" \
"v_mfma_f32_16x16x32_bf16 a[180:183], v[156:159], v[92:95], a[180:183]\n" \
"buffer_load_dwordx4 v20, s[80:83], s76 offen offset:1024 sc1 lds\n" \
"v_mfma_f32_16x16x32_bf16 a[184:187], v[164:167], v[92:95], a[184:187]\n" \
"v_cvt_pk_bf16_f32 v136, v196, v197\n" \
"v_cvt_pk_bf16_f32 v137, v198, v199\n" \
"v_mfma_f32_16x16x32_bf16 a[184:187], v[168:171], v[96:99], a[184:187]\n" \
"v_cvt_pk_bf16_f32 v138, v212, v213\n" \
"v_cvt_pk_bf16_f32 v139, v214, v215\n" \
"v_mfma_f32_16x16x32_bf16 a[188:191], v[176:179], v[96:99], a[188:191]\n" \
"v_perm_b32 v184, v192, v192, v40\n" \
"v_perm_b32 v185, v192, v192, v41\n" \
"v_mfma_f32_16x16x32_bf16 a[188:191], v[172:175], v[92:95], a[188:191]\n" \
"v_perm_b32 v186, v192, v192, v42\n" \
"v_perm_b32 v187, v192, v192, v43\n" \
"v_perm_b32 v200, v208, v208, v40\n" \
"v_perm_b32 v201, v208, v208, v41\n" \
"v_perm_b32 v202, v208, v208, v42\n" \
"v_perm_b32 v203, v208, v208, v43\n" \
"v_cvt_pk_bf16_f32 v224, v224, v226\n" \
"v_cvt_pk_bf16_f32 v140, v228, v229\n" \
"v_cvt_pk_bf16_f32 v141, v230, v231\n" \
"v_mfma_f32_16x16x32_bf16 v[116:119], v[134:137], v[184:187], 0\n" \
"v_mfma_f32_16x16x32_bf16 v[120:123], v[136:139], v[200:203], 0\n" \
"v_perm_b32 v216, v224, v224, v40\n" \
"v_perm_b32 v217, v224, v224, v41\n" \
"v_perm_b32 v218, v224, v224, v42\n" \
"v_perm_b32 v219, v224, v224, v43\n" \
"v_cvt_pk_bf16_f32 v240, v240, v242\n" \
"v_cvt_pk_bf16_f32 v142, v244, v245\n" \
"v_cvt_pk_bf16_f32 v143, v246, v247\n" \
"v_mfma_f32_16x16x32_bf16 v[36:39], v[138:141], v[216:219], 0\n" \
"v_perm_b32 v232, v240, v240, v40\n" \
"v_perm_b32 v233, v240, v240, v41\n" \
"v_perm_b32 v234, v240, v240, v42\n" \
"v_perm_b32 v235, v240, v240, v43\n" \
"v_exp_f32_e64 v45, v45\n" \
"v_mov_b32_dpp v118, v122 quad_perm:[0,1,2,3] row_mask:0x2 bank_mask:0xf\n" \
"v_mfma_f32_16x16x32_bf16 v[48:51], v[140:143], v[232:235], 0\n" \
"v_cvt_pk_bf16_f32 v124, v252, v253\n" \
"v_cvt_pk_bf16_f32 v125, v254, v255\n" \
"v_mov_b32_dpp v118, v38 quad_perm:[0,1,2,3] row_mask:0x4 bank_mask:0xf\n" \
"v_add_f32_e64 v45, 1.0, v45\n" \
"v_mfma_f32_16x16x32_bf16 v[192:195], v[124:127], v[184:187], 0\n" \
"v_mfma_f32_16x16x32_bf16 v[208:211], v[126:129], v[200:203], 0\n" \
"v_mfma_f32_16x16x32_bf16 v[224:227], v[128:131], v[216:219], 0\n" \
"buffer_store_dwordx4 a[12:15], v20, s[80:83], s89 offen offset:1024 sc1\n" \
"s_add_u32 s89, s48, 0x0\n" \
"v_mfma_f32_16x16x32_bf16 v[240:243], v[130:133], v[232:235], 0\n" \
"v_mov_b32_dpp v118, v50 quad_perm:[0,1,2,3] row_mask:0x8 bank_mask:0xf\n" \
"v_rcp_f32_e32 v45, v45\n" \
"v_mov_b32_dpp v116, v120 quad_perm:[0,1,2,3] row_mask:0x2 bank_mask:0xf\n" \
"v_mov_b32_dpp v117, v121 quad_perm:[0,1,2,3] row_mask:0x2 bank_mask:0xf\n" \
"v_rcp_f32_e64 v118, v118\n" \
"v_mov_b32_dpp v116, v36 quad_perm:[0,1,2,3] row_mask:0x4 bank_mask:0xf\n" \
"v_mov_b32_dpp v117, v37 quad_perm:[0,1,2,3] row_mask:0x4 bank_mask:0xf\n" \
"v_mul_f32_e64 v118, v45, v118\n" \
"v_mov_b32_dpp v116, v48 quad_perm:[0,1,2,3] row_mask:0x8 bank_mask:0xf\n" \
"v_mov_b32_dpp v117, v49 quad_perm:[0,1,2,3] row_mask:0x8 bank_mask:0xf\n" \
"v_mul_f32_e32 v116, v118, v116\n" \
"v_mul_f32_e32 v117, v118, v117\n" \
"v_mov_b32_e64 v122, v118\n" \
"v_cvt_pk_bf16_f32 v116, v116, v117\n" \
"buffer_store_dword v116, v25, s[80:83], s57 offen sc1\n" \
"v_permlane16_swap_b32_e32 v118, v122\n" \
"v_mov_b32_e32 v38, v118\n" \
"v_mov_b32_e64 v50, v122\n" \
"v_accvgpr_read_b32 v52, a128\n" \
"v_permlane32_swap_b32_e32 v118, v38\n" \
"v_permlane32_swap_b32_e32 v122, v50\n" \
"v_mul_f32_e32 v192, v118, v192\n" \
"v_mul_f32_e32 v193, v118, v193\n" \
"v_mul_f32_e32 v208, v122, v208\n" \
"v_mul_f32_e32 v209, v122, v209\n" \
"v_mul_f32_e32 v224, v38, v224\n" \
"v_mul_f32_e32 v225, v38, v225\n" \
"v_mul_f32_e32 v240, v50, v240\n" \
"v_mul_f32_e32 v241, v50, v241\n" \
"v_mul_f32_e32 v194, v118, v194\n" \
"v_mul_f32_e32 v195, v118, v195\n" \
"v_cvt_pk_bf16_f32 v192, v192, v193\n" \
"v_mul_f32_e32 v210, v122, v210\n" \
"v_mul_f32_e32 v211, v122, v211\n" \
"L_pf9:\n" \
"s_cbranch_execz L_pf13\n" \
"s_nop 0\n" \
"v_cvt_pk_bf16_f32 v208, v208, v209\n" \
"v_mul_f32_e32 v226, v38, v226\n" \
"v_mul_f32_e32 v227, v38, v227\n" \
"v_cvt_pk_bf16_f32 v224, v224, v225\n" \
"v_mul_f32_e32 v242, v50, v242\n" \
"v_mul_f32_e32 v243, v50, v243\n" \
"v_cvt_pk_bf16_f32 v240, v240, v241\n" \
"v_cvt_pk_bf16_f32 v193, v194, v195\n" \
"v_cvt_pk_bf16_f32 v209, v210, v211\n" \
"v_cvt_pk_bf16_f32 v225, v226, v227\n" \
"v_cvt_pk_bf16_f32 v241, v242, v243\n" \
"buffer_store_dwordx2 v[192:193], v24, s[80:83], s84 offen sc1\n" \
"buffer_store_dwordx2 v[208:209], v24, s[80:83], s85 offen sc1\n" \
"buffer_store_dwordx2 v[224:225], v24, s[80:83], s86 offen sc1\n" \
"buffer_store_dwordx2 v[240:241], v24, s[80:83], s87 offen sc1\n" \
"v_accvgpr_read_b32 v53, a129\n" \
"v_accvgpr_read_b32 v54, a130\n" \
"v_accvgpr_read_b32 v55, a131\n" \
"v_pk_mul_f32 v[52:53], v[52:53], s[70:71]\n" \
"v_accvgpr_read_b32 v56, a132\n" \
"v_accvgpr_read_b32 v57, a133\n" \
"v_accvgpr_read_b32 v58, a134\n" \
"v_accvgpr_read_b32 v59, a135\n" \
"v_pk_mul_f32 v[54:55], v[54:55], s[70:71]\n" \
"v_cvt_pk_bf16_f32 v52, v52, v53\n" \
"v_pk_mul_f32 v[56:57], v[56:57], s[70:71]\n" \
"v_pk_mul_f32 v[58:59], v[58:59], s[70:71]\n" \
"v_cvt_pk_bf16_f32 v53, v54, v55\n" \
"v_cvt_pk_bf16_f32 v54, v56, v57\n" \
"v_cvt_pk_bf16_f32 v55, v58, v59\n" \
"ds_write_b128 v27, v[52:55] offset:0\n" \
"v_accvgpr_read_b32 v76, a224\n" \
"v_accvgpr_read_b32 v77, a225\n" \
"v_accvgpr_read_b32 v78, a226\n" \
"v_accvgpr_read_b32 v79, a227\n" \
"v_pk_mul_f32 v[76:77], v[76:77], s[70:71]\n" \
"v_accvgpr_read_b32 v52, a136\n" \
"v_accvgpr_read_b32 v53, a137\n" \
"v_accvgpr_read_b32 v80, a228\n" \
"v_accvgpr_read_b32 v81, a229\n" \
"v_accvgpr_read_b32 v82, a230\n" \
"v_accvgpr_read_b32 v83, a231\n" \
"v_pk_mul_f32 v[78:79], v[78:79], s[70:71]\n" \
"v_cvt_pk_bf16_f32 v76, v76, v77\n" \
"v_accvgpr_read_b32 v54, a138\n" \
"v_accvgpr_read_b32 v55, a139\n" \
"v_pk_mul_f32 v[52:53], v[52:53], s[70:71]\n" \
"v_pk_mul_f32 v[80:81], v[80:81], s[70:71]\n" \
"v_pk_mul_f32 v[82:83], v[82:83], s[70:71]\n" \
"v_cvt_pk_bf16_f32 v77, v78, v79\n" \
"v_accvgpr_read_b32 v56, a140\n" \
"v_accvgpr_read_b32 v57, a141\n" \
"v_accvgpr_read_b32 v58, a142\n" \
"v_accvgpr_read_b32 v59, a143\n" \
"v_pk_mul_f32 v[54:55], v[54:55], s[70:71]\n" \
"v_cvt_pk_bf16_f32 v52, v52, v53\n" \
"v_cvt_pk_bf16_f32 v78, v80, v81\n" \
"v_cvt_pk_bf16_f32 v79, v82, v83\n" \
"ds_write_b128 v27, v[76:79] offset:12288\n" \
"v_pk_mul_f32 v[56:57], v[56:57], s[70:71]\n" \
"v_pk_mul_f32 v[58:59], v[58:59], s[70:71]\n" \
"v_cvt_pk_bf16_f32 v53, v54, v55\n" \
"v_cvt_pk_bf16_f32 v54, v56, v57\n" \
"v_cvt_pk_bf16_f32 v55, v58, v59\n" \
"ds_write_b128 v27, v[52:55] offset:1024\n" \
"v_accvgpr_read_b32 v76, a208\n" \
"v_accvgpr_read_b32 v77, a209\n" \
"v_accvgpr_read_b32 v78, a210\n" \
"v_accvgpr_read_b32 v79, a211\n" \
"v_accvgpr_read_b32 v52, a168\n" \
"v_accvgpr_read_b32 v53, a169\n" \
"v_mfma_f32_16x16x32_bf16 v[76:79], v[148:151], v[100:103], v[76:79]\n" \
"buffer_store_dwordx4 a[40:43], v20, s[80:83], s88 offen offset:0 sc1\n" \
"v_mfma_f32_16x16x32_bf16 v[76:79], v[152:155], v[104:107], v[76:79]\n" \
"s_add_u32 s88, s48, 0x0\n" \
"s_nop 0\n" \
"v_accvgpr_read_b32 v54, a170\n" \
"v_accvgpr_read_b32 v55, a171\n" \
"v_accvgpr_read_b32 v56, a172\n" \
"v_pk_mul_f32 v[52:53], v[52:53], s[70:71]\n" \
"v_accvgpr_read_b32 v57, a173\n" \
"v_accvgpr_read_b32 v58, a174\n" \
"v_accvgpr_read_b32 v59, a175\n" \
"v_pk_mul_f32 v[54:55], v[54:55], s[70:71]\n" \
"v_cvt_pk_bf16_f32 v52, v52, v53\n" \
"v_pk_mul_f32 v[56:57], v[56:57], s[70:71]\n" \
"v_pk_mul_f32 v[58:59], v[58:59], s[70:71]\n" \
"v_cvt_pk_bf16_f32 v53, v54, v55\n" \
"v_cvt_pk_bf16_f32 v54, v56, v57\n" \
"v_cvt_pk_bf16_f32 v55, v58, v59\n" \
"ds_write_b128 v27, v[52:55] offset:5120\n" \
"v_accvgpr_read_b32 v80, a212\n" \
"v_accvgpr_read_b32 v81, a213\n" \
"v_accvgpr_read_b32 v82, a214\n" \
"v_accvgpr_read_b32 v83, a215\n" \
"v_accvgpr_read_b32 v52, a232\n" \
"v_accvgpr_read_b32 v53, a233\n" \
"v_mfma_f32_16x16x32_bf16 v[80:83], v[160:163], v[104:107], v[80:83]\n" \
"v_accvgpr_read_b32 v54, a234\n" \
"v_accvgpr_read_b32 v55, a235\n" \
"v_mfma_f32_16x16x32_bf16 v[80:83], v[156:159], v[100:103], v[80:83]\n" \
"v_accvgpr_read_b32 v56, a236\n" \
"v_accvgpr_read_b32 v57, a237\n" \
"v_accvgpr_read_b32 v58, a238\n" \
"v_accvgpr_read_b32 v59, a239\n" \
"v_pk_mul_f32 v[52:53], v[52:53], s[70:71]\n" \
"v_pk_mul_f32 v[54:55], v[54:55], s[70:71]\n" \
"v_cvt_pk_bf16_f32 v52, v52, v53\n" \
"v_pk_mul_f32 v[56:57], v[56:57], s[70:71]\n" \
"v_pk_mul_f32 v[58:59], v[58:59], s[70:71]\n" \
"v_cvt_pk_bf16_f32 v53, v54, v55\n" \
"v_cvt_pk_bf16_f32 v54, v56, v57\n" \
"v_cvt_pk_bf16_f32 v55, v58, v59\n" \
"ds_write_b128 v27, v[52:55] offset:13312\n" \
"v_accvgpr_read_b32 v52, a216\n" \
"v_accvgpr_read_b32 v53, a217\n" \
"v_accvgpr_read_b32 v54, a218\n" \
"v_accvgpr_read_b32 v55, a219\n" \
"v_accvgpr_read_b32 v60, a160\n" \
"v_accvgpr_read_b32 v61, a161\n" \
"v_mfma_f32_16x16x32_bf16 v[52:55], v[164:167], v[100:103], v[52:55]\n" \
"v_accvgpr_read_b32 v62, a162\n" \
"v_accvgpr_read_b32 v63, a163\n" \
"v_mfma_f32_16x16x32_bf16 v[52:55], v[168:171], v[104:107], v[52:55]\n" \
"v_accvgpr_read_b32 v56, a220\n" \
"v_accvgpr_read_b32 v57, a221\n" \
"v_accvgpr_read_b32 v58, a222\n" \
"v_accvgpr_read_b32 v59, a223\n" \
"buffer_store_dwordx4 a[44:47], v20, s[80:83], s89 offen offset:1024 sc1\n" \
"s_add_u32 s89, s48, 0x0\n" \
"s_nop 0\n" \
"v_mfma_f32_16x16x32_bf16 v[56:59], v[176:179], v[104:107], v[56:59]\n" \
"v_accvgpr_read_b32 v64, a164\n" \
"v_accvgpr_read_b32 v65, a165\n" \
"v_mfma_f32_16x16x32_bf16 v[56:59], v[172:175], v[100:103], v[56:59]\n" \
"v_accvgpr_read_b32 v66, a166\n" \
"v_accvgpr_read_b32 v67, a167\n" \
"v_accvgpr_read_b32 v68, a192\n" \
"v_accvgpr_read_b32 v69, a193\n" \
"v_pk_mul_f32 v[60:61], v[60:61], s[70:71]\n" \
"v_pk_mul_f32 v[62:63], v[62:63], s[70:71]\n" \
"v_cvt_pk_bf16_f32 v60, v60, v61\n" \
"v_pk_mul_f32 v[64:65], v[64:65], s[70:71]\n" \
"v_pk_mul_f32 v[66:67], v[66:67], s[70:71]\n" \
"v_cvt_pk_bf16_f32 v61, v62, v63\n" \
"v_cvt_pk_bf16_f32 v62, v64, v65\n" \
"v_cvt_pk_bf16_f32 v63, v66, v67\n" \
"ds_write_b128 v27, v[60:63] offset:4096\n" \
"v_accvgpr_read_b32 v60, a200\n" \
"v_accvgpr_read_b32 v61, a201\n" \
"v_accvgpr_read_b32 v70, a194\n" \
"v_accvgpr_read_b32 v71, a195\n" \
"v_pk_mul_f32 v[68:69], v[68:69], s[70:71]\n" \
"v_accvgpr_read_b32 v62, a202\n" \
"v_accvgpr_read_b32 v63, a203\n" \
"v_pk_mul_f32 v[60:61], v[60:61], s[70:71]\n" \
"v_accvgpr_read_b32 v72, a196\n" \
"v_accvgpr_read_b32 v73, a197\n" \
"v_accvgpr_read_b32 v74, a198\n" \
"v_accvgpr_read_b32 v75, a199\n" \
"v_pk_mul_f32 v[70:71], v[70:71], s[70:71]\n" \
"v_cvt_pk_bf16_f32 v68, v68, v69\n" \
"v_accvgpr_read_b32 v64, a204\n" \
"v_accvgpr_read_b32 v65, a205\n" \
"v_accvgpr_read_b32 v66, a206\n" \
"v_accvgpr_read_b32 v67, a207\n" \
"v_pk_mul_f32 v[62:63], v[62:63], s[70:71]\n" \
"v_cvt_pk_bf16_f32 v60, v60, v61\n" \
"v_accvgpr_read_b32 v100, a240\n" \
"v_accvgpr_read_b32 v101, a241\n" \
"v_accvgpr_read_b32 v102, a242\n" \
"v_accvgpr_read_b32 v103, a243\n" \
"v_pk_mul_f32 v[72:73], v[72:73], s[70:71]\n" \
"v_pk_mul_f32 v[74:75], v[74:75], s[70:71]\n" \
"v_mfma_f32_16x16x32_bf16 v[100:103], v[148:151], v[108:111], v[100:103]\n" \
"v_cvt_pk_bf16_f32 v69, v70, v71\n" \
"v_cvt_pk_bf16_f32 v61, v62, v63\n" \
"v_mfma_f32_16x16x32_bf16 v[100:103], v[152:155], v[112:115], v[100:103]\n" \
"v_cvt_pk_bf16_f32 v70, v72, v73\n" \
"v_cvt_pk_bf16_f32 v71, v74, v75\n" \
"ds_write_b128 v27, v[68:71] offset:8192\n" \
"v_pk_mul_f32 v[64:65], v[64:65], s[70:71]\n" \
"v_pk_mul_f32 v[66:67], v[66:67], s[70:71]\n" \
"v_cvt_pk_bf16_f32 v62, v64, v65\n" \
"v_cvt_pk_bf16_f32 v63, v66, v67\n" \
"buffer_store_dwordx4 a[72:75], v20, s[80:83], s88 offen offset:2048 sc1\n" \
"s_add_u32 s88, s48, 0x2000\n" \
"ds_write_b128 v27, v[60:63] offset:9216\n" \
"v_accvgpr_read_b32 v84, a144\n" \
"v_accvgpr_read_b32 v85, a145\n" \
"v_accvgpr_read_b32 v68, a152\n" \
"v_accvgpr_read_b32 v69, a153\n" \
"v_accvgpr_read_b32 v92, a176\n" \
"v_accvgpr_read_b32 v93, a177\n" \
"v_accvgpr_read_b32 v60, a184\n" \
"v_accvgpr_read_b32 v61, a185\n" \
"v_accvgpr_read_b32 v86, a146\n" \
"v_accvgpr_read_b32 v87, a147\n" \
"v_pk_mul_f32 v[84:85], v[84:85], s[70:71]\n" \
"v_accvgpr_read_b32 v70, a154\n" \
"v_accvgpr_read_b32 v71, a155\n" \
"v_pk_mul_f32 v[68:69], v[68:69], s[70:71]\n" \
"v_accvgpr_read_b32 v94, a178\n" \
"v_accvgpr_read_b32 v95, a179\n" \
"v_pk_mul_f32 v[92:93], v[92:93], s[70:71]\n" \
"v_accvgpr_read_b32 v62, a186\n" \
"v_accvgpr_read_b32 v63, a187\n" \
"v_pk_mul_f32 v[60:61], v[60:61], s[70:71]\n" \
"v_pk_mul_f32 v[76:77], v[76:77], s[70:71]\n" \
"v_pk_mul_f32 v[52:53], v[52:53], s[70:71]\n" \
"v_pk_mul_f32 v[100:101], v[100:101], s[70:71]\n" \
"v_accvgpr_read_b32 v88, a148\n" \
"v_accvgpr_read_b32 v89, a149\n" \
"v_accvgpr_read_b32 v90, a150\n" \
"v_accvgpr_read_b32 v91, a151\n" \
"v_pk_mul_f32 v[86:87], v[86:87], s[70:71]\n" \
"v_cvt_pk_bf16_f32 v84, v84, v85\n" \
"v_accvgpr_read_b32 v72, a156\n" \
"v_accvgpr_read_b32 v73, a157\n" \
"v_accvgpr_read_b32 v74, a158\n" \
"v_accvgpr_read_b32 v75, a159\n" \
"v_pk_mul_f32 v[70:71], v[70:71], s[70:71]\n" \
"v_cvt_pk_bf16_f32 v68, v68, v69\n" \
"v_accvgpr_read_b32 v96, a180\n" \
"v_accvgpr_read_b32 v97, a181\n" \
"v_accvgpr_read_b32 v98, a182\n" \
"v_accvgpr_read_b32 v99, a183\n" \
"v_pk_mul_f32 v[94:95], v[94:95], s[70:71]\n" \
"v_cvt_pk_bf16_f32 v92, v92, v93\n" \
"v_accvgpr_read_b32 v64, a188\n" \
"v_accvgpr_read_b32 v65, a189\n" \
"v_accvgpr_read_b32 v66, a190\n" \
"v_accvgpr_read_b32 v67, a191\n" \
"v_pk_mul_f32 v[62:63], v[62:63], s[70:71]\n" \
"v_cvt_pk_bf16_f32 v60, v60, v61\n" \
"v_pk_mul_f32 v[78:79], v[78:79], s[70:71]\n" \
"v_cvt_pk_bf16_f32 v76, v76, v77\n" \
"v_pk_mul_f32 v[54:55], v[54:55], s[70:71]\n" \
"v_cvt_pk_bf16_f32 v52, v52, v53\n" \
"v_pk_mul_f32 v[102:103], v[102:103], s[70:71]\n" \
"v_cvt_pk_bf16_f32 v100, v100, v101\n" \
"v_pk_mul_f32 v[88:89], v[88:89], s[70:71]\n" \
"v_pk_mul_f32 v[90:91], v[90:91], s[70:71]\n" \
"v_cvt_pk_bf16_f32 v85, v86, v87\n" \
"v_pk_mul_f32 v[72:73], v[72:73], s[70:71]\n" \
"v_pk_mul_f32 v[74:75], v[74:75], s[70:71]\n" \
"buffer_store_dwordx4 a[76:79], v20, s[80:83], s89 offen offset:3072 sc1\n" \
"s_add_u32 s89, s48, 0x2000\n" \
"v_cvt_pk_bf16_f32 v69, v70, v71\n" \
"v_pk_mul_f32 v[96:97], v[96:97], s[70:71]\n" \
"v_pk_mul_f32 v[98:99], v[98:99], s[70:71]\n" \
"v_cvt_pk_bf16_f32 v93, v94, v95\n" \
"v_pk_mul_f32 v[64:65], v[64:65], s[70:71]\n" \
"v_pk_mul_f32 v[66:67], v[66:67], s[70:71]\n" \
"v_cvt_pk_bf16_f32 v61, v62, v63\n" \
"v_pk_mul_f32 v[80:81], v[80:81], s[70:71]\n" \
"v_pk_mul_f32 v[82:83], v[82:83], s[70:71]\n" \
"v_cvt_pk_bf16_f32 v77, v78, v79\n" \
"v_pk_mul_f32 v[56:57], v[56:57], s[70:71]\n" \
"v_pk_mul_f32 v[58:59], v[58:59], s[70:71]\n" \
"v_cvt_pk_bf16_f32 v53, v54, v55\n" \
"v_accvgpr_read_b32 v104, a244\n" \
"v_accvgpr_read_b32 v105, a245\n" \
"v_accvgpr_read_b32 v106, a246\n" \
"v_accvgpr_read_b32 v107, a247\n" \
"v_cvt_pk_bf16_f32 v101, v102, v103\n" \
"v_cvt_pk_bf16_f32 v86, v88, v89\n" \
"v_mfma_f32_16x16x32_bf16 v[104:107], v[160:163], v[112:115], v[104:107]\n" \
"v_cvt_pk_bf16_f32 v87, v90, v91\n" \
"v_cvt_pk_bf16_f32 v70, v72, v73\n" \
"ds_write_b128 v21, v[84:87] offset:16384\n" \
"v_mfma_f32_16x16x32_bf16 v[104:107], v[156:159], v[108:111], v[104:107]\n" \
"v_cvt_pk_bf16_f32 v71, v74, v75\n" \
"ds_write_b128 v21, v[68:71] offset:17408\n" \
"v_cvt_pk_bf16_f32 v94, v96, v97\n" \
"v_cvt_pk_bf16_f32 v95, v98, v99\n" \
"ds_write_b128 v21, v[92:95] offset:20480\n" \
"v_cvt_pk_bf16_f32 v62, v64, v65\n" \
"v_cvt_pk_bf16_f32 v63, v66, v67\n" \
"ds_write_b128 v21, v[60:63] offset:21504\n" \
"v_pk_mul_f32 v[104:105], v[104:105], s[70:71]\n" \
"v_pk_mul_f32 v[106:107], v[106:107], s[70:71]\n" \
"v_cvt_pk_bf16_f32 v78, v80, v81\n" \
"v_cvt_pk_bf16_f32 v79, v82, v83\n" \
"ds_write_b128 v21, v[76:79] offset:24576\n" \
"v_cvt_pk_bf16_f32 v54, v56, v57\n" \
"v_cvt_pk_bf16_f32 v55, v58, v59\n" \
"ds_write_b128 v21, v[52:55] offset:25600\n" \
"v_cvt_pk_bf16_f32 v102, v104, v105\n" \
"v_cvt_pk_bf16_f32 v103, v106, v107\n" \
"ds_write_b128 v21, v[100:103] offset:28672\n" \
"v_accvgpr_read_b32 v84, a248\n" \
"v_accvgpr_read_b32 v85, a249\n" \
"v_accvgpr_read_b32 v86, a250\n" \
"v_accvgpr_read_b32 v87, a251\n" \
"v_accvgpr_read_b32 v88, a252\n" \
"v_accvgpr_read_b32 v89, a253\n" \
"v_mfma_f32_16x16x32_bf16 v[84:87], v[164:167], v[108:111], v[84:87]\n" \
"buffer_store_dwordx4 a[104:107], v20, s[80:83], s88 offen offset:0 sc1\n" \
"v_mfma_f32_16x16x32_bf16 v[84:87], v[168:171], v[112:115], v[84:87]\n" \
"v_accvgpr_read_b32 v90, a254\n" \
"v_accvgpr_read_b32 v91, a255\n" \
"v_sub_f32_e32 v44, 1.0, v45\n" \
"ds_write_b32 v26, v44\n" \
"v_mfma_f32_16x16x32_bf16 v[88:91], v[176:179], v[112:115], v[88:91]\n" \
"v_mfma_f32_16x16x32_bf16 v[88:91], v[172:175], v[108:111], v[88:91]\n" \
"buffer_store_dwordx4 a[108:111], v20, s[80:83], s89 offen offset:1024 sc1\n" \
"s_nop 0\n" \
"v_pk_mul_f32 v[84:85], v[84:85], s[70:71]\n" \
"v_pk_mul_f32 v[86:87], v[86:87], s[70:71]\n" \
"v_cvt_pk_bf16_f32 v84, v84, v85\n" \
"v_cvt_pk_bf16_f32 v85, v86, v87\n" \
"s_nop 1\n" \
"v_pk_mul_f32 v[88:89], v[88:89], s[70:71]\n" \
"v_pk_mul_f32 v[90:91], v[90:91], s[70:71]\n" \
"v_cvt_pk_bf16_f32 v86, v88, v89\n" \
"v_cvt_pk_bf16_f32 v87, v90, v91\n" \
"ds_write_b128 v21, v[84:87] offset:29696\n" \
"L_end_%=:\n"
#define PROJ_VCLOB "v21","v24","v25","v33","v34","v35","v36","v37","v38","v39","v40","v41","v42","v43","v44","v45","v46","v47","v48","v49","v50","v51","v52","v53","v54","v55","v56","v57","v58","v59","v60","v61","v62","v63","v64","v65","v66","v67","v68","v69","v70","v71","v72","v73","v74","v75","v76","v77","v78","v79","v80","v81","v82","v83","v84","v85","v86","v87","v88","v89","v90","v91","v92","v93","v94","v95","v96","v97","v98","v99","v100","v101","v102","v103","v104","v105","v106","v107","v108","v109","v110","v111","v112","v113","v114","v115","v116","v117","v118","v119","v120","v121","v122","v123","v124","v125","v126","v127","v128","v129","v130","v131","v132","v133","v134","v135","v136","v137","v138","v139","v140","v141","v142","v143","v144","v145","v146","v147","v148","v149","v150","v151","v152","v153","v154","v155","v156","v157","v158","v159","v160","v161","v162","v163","v164","v165","v166","v167","v168","v169","v170","v171","v172","v173","v174","v175","v176","v177","v178","v179","v180","v181","v182","v183","v184","v185","v186","v187","v188","v189","v190","v191","v192","v193","v194","v195","v196","v197","v198","v199","v200","v201","v202","v203","v204","v205","v206","v207","v208","v209","v210","v211","v212","v213","v214","v215","v216","v217","v218","v219","v220","v221","v222","v223","v224","v225","v226","v227","v228","v229","v230","v231","v232","v233","v234","v235","v236","v237","v238","v239","v240","v241","v242","v243","v244","v245","v246","v247","v248","v249","v250","v251","v252","v253","v254","v255"
#define PROJ_SCLOB "s37","s38","s39","s40","s41","s42","s43","s44","s45","s46","s47","s48","s49","s50","s51","s52","s53","s54","s55","s56","s57","s58","s59","s60","s61","s62","s63","s76","s77","s78","s79","s80","s81","s82","s83","s84","s85","s86","s87","s88","s89","s94","s95","s96","s97","s98","s99","s100","s101"

#define PLO(p) ((unsigned)(size_t)(p))
#define PHI(p) ((unsigned)((size_t)(p) >> 32))
__device__ __forceinline__ unsigned proj_phase(const u16* __restrict__ x, const u16* __restrict__ ckv, const u16* __restrict__ wq, const u16* __restrict__ wuk,
                                                       const u16* __restrict__ wuv, const u16* __restrict__ wqkv, const u16* __restrict__ gw, const u16* wo, const u16* wob, const u16* out,
                                                       char* __restrict__ ws, char* lds, const unsigned wid, unsigned& tag, u32x4 (&vp)[5]) {
    typedef unsigned u32x2 __attribute__((ext_vector_type(2)));
    unsigned* const flags = (unsigned*)(ws + WS_F);
    const int b = blockIdx.x, h = (b & 7) * 4 + ((b >> 3) & 3), mt = b >> 5;
    const unsigned L0 = (unsigned)(size_t)lds, rb = (wid < 2u ? 15u - mt : mt) * 128u;
    RTS(0);
#define PST() do {} while (0)
    const unsigned lane = threadIdx.x & 63u, wr = wid >> 1, wc = wid & 1u;
    PST();
    unsigned vAc[2], vB[2];
    const unsigned sp = b & 7, rot = (PROJ_ROT) & 7u;
#pragma unroll
    for (int par = 0; par < 2; ++par) {
        const unsigned lr = lane >> 3, cc = (lane & 7u) ^ (4u * par + (lr >> 1)), r0 = rb + wc * 64u + lr;
        vAc[par] = r0 * (DC * 2) + cc * 16u;
        const unsigned br = lane >> 4, p16 = lane & 15u, sc = (((p16 >> 1) ^ (br | (par << 2))) << 1) | (p16 & 1u);
        vB[par] = (16u * wid + br) * (NBIG * 2) + h * (HD * 2) + sc * 16u;
    }
#define SRD(p, n) (u32x4){PLO(p), PHI(p) & 0xffffu, n, 0x00020000u}
    u32x4 sC = SRD(ckv, S * DC * 2), sK = SRD(wuk, DC * NBIG * 2), sV = SRD(wuv, DC * NBIG * 2);
#define PRE(n) do { SBAR(); asm volatile(PROJ_PRE##n :: "{v22}"(vAc[0]), "{v23}"(vAc[1]), "{v26}"(vB[0]), "{v27}"(vB[1]), "{s[64:67]}"(sC), "{s[68:71]}"(sK), "{s[72:75]}"(sV), \
                 [mA] "s"(L0 + wid * 8192u), [mB] "s"(L0 + 65536u + wid * 4096u), [rc] "s"(rot * 128u) : "memory", "m0", "scc", "s62", "s63", "s84", "s85"); SBAR(); } while (0)
    PRE(0);
    const unsigned fr = lane & 15u, fg = lane >> 4, r4 = (lane >> 2) & 3u, sB = r4 | ((fg & 1u) << 2);
    const unsigned aA0 = L0 + (wr * 64u + fr) * 128u + ((fg ^ (fr >> 1)) << 4);
    unsigned vl = lane * 16u, r32 = L0 + 65536u + (8u * fg + r4) * 256u + (lane & 3u) * 8u + ((sB ^ (4u * wc)) << 5);
    u32x2 r28 = {aA0, aA0 ^ 64u};
    asm volatile("" : "+v"(vl), "+v"(r28), "+v"(r32));
    PST();
    PRE(1);
    PST();
    PRE(2);
    PST();
    asm volatile("s_mov_b64 exec, 0\n s_cmp_lt_u32 %0, 2\n s_cbranch_scc1 L_chA\n s_cmp_eq_u32 %0, 2\n s_cbranch_scc1 L_pf2\n s_branch L_pf3\n"
                 "L_chA: s_cmp_eq_u32 %0, 0\n s_cbranch_scc1 L_pf0\n s_branch L_pf1\n L_retA: s_mov_b64 exec, -1" :: "s"(wid) : "scc", "memory");
    PRE(3);
    PST();
    asm volatile(PROJ_ASM
                 : [tag] "=&s"(tag), "+{s[64:67]}"(sC), "+{s[68:71]}"(sK), "+{s[72:75]}"(sV), "+{v22}"(vAc[0]), "+{v23}"(vAc[1]), "+{v26}"(vB[0]), "+{v27}"(vB[1]), "+{v[28:29]}"(r28), "+{v32}"(r32),
                   "={v[0:3]}"(vp[0]), "={v[4:7]}"(vp[1]), "={v[8:11]}"(vp[2]), "={v[12:15]}"(vp[3]), "={v[16:19]}"(vp[4])
                 : "{v20}"(vl), [xl] "s"(PLO(x)), [xh] "s"(PHI(x)), [wql] "s"(PLO(wq)), [wqh] "s"(PHI(wq)), [wsl] "s"(PLO(ws)), [wsh] "s"(PHI(ws)), [wvl] "s"(PLO(wqkv)), [wvh] "s"(PHI(wqkv)),
                   [gwl] "s"(PLO(gw)), [gwh] "s"(PHI(gw)), [wol] "s"(PLO(wo)), [woh] "s"(PHI(wo)), [wbl] "s"(PLO(wob)), [wbh] "s"(PHI(wob)), [outl] "s"(PLO(out)), [outh] "s"(PHI(out)),
                   [b] "s"(b), [w] "s"(wid), [mA] "s"(L0 + wid * 8192u), [mB] "s"(L0 + 65536u + wid * 4096u), [mS] "s"(L0 + 151552u + wid * 512u), [rc] "s"(rot * 128u)
                 : "memory", "m0", "vcc", "scc", PROJ_VCLOB, PROJ_SCLOB, ACLOB_LO, ACLOB_HI);
    RTS(1);
    return vl;
}

namespace att {
#define DI __device__ __forceinline__
constexpr int LDS_BYTES = 163840;
constexpr unsigned LDS_G = 159744u, LDS_L = 161792u;
typedef __bf16 bfx8 __attribute__((ext_vector_type(8)));
DI unsigned rfl(unsigned x) { return __builtin_amdgcn_readfirstlane(x); }
DI f32x4 mfma(u32x4 a, u32x4 b, f32x4 c) { return __builtin_amdgcn_mfma_f32_16x16x32_bf16(__builtin_bit_cast(bfx8, a), __builtin_bit_cast(bfx8, b), c, 0, 0, 0); }
typedef __bf16 bfx2 __attribute__((ext_vector_type(2)));
typedef float f32x2 __attribute__((ext_vector_type(2)));
DI unsigned cvtpk2(float lo, float hi) { f32x2 v = {lo, hi}; bfx2 b = __builtin_convertvector(v, bfx2); return __builtin_bit_cast(unsigned, b); }
DI void att_exact(const u16* __restrict__ x, const u16* __restrict__ wq, char* __restrict__ ws, unsigned h, unsigned r0, float g1) {
    const unsigned lane = lane_id(), g = lane >> 4, c = lane & 15u;
    f32x4 o[8];
#pragma unroll
    for (int n = 0; n < 8; ++n) o[n] = (f32x4){0.f, 0.f, 0.f, 0.f};
#pragma unroll 1
    for (unsigned kk = 0; kk < (unsigned)DM; kk += 32u) {
        const unsigned k = kk + 8u * g;
        const u32x4 xf = *reinterpret_cast<const u32x4*>(x + (r0 + c) * DM + k);
#pragma unroll
        for (int n = 0; n < 8; ++n) {
            const u16* w = wq + k * NBIG + h * HD + n * 16 + c; u32x4 wf;
#pragma unroll
            for (int e = 0; e < 4; ++e) wf[e] = (unsigned)w[2 * e * NBIG] | ((unsigned)w[(2 * e + 1) * NBIG] << 16);
            o[n] = mfma(wf, xf, o[n]);
        }
    }
    u32x4 q[4];
#pragma unroll
    for (int p = 0; p < 4; ++p) {
        const f32x4 a = o[2 * p] * 0.12751743082f, b = o[2 * p + 1] * 0.12751743082f;
        q[p] = (u32x4){cvtpk2(a[0], a[1]), cvtpk2(a[2], a[3]), cvtpk2(b[0], b[1]), cvtpk2(b[2], b[3])};
    }
#pragma unroll
    for (int n = 0; n < 8; ++n) o[n] = (f32x4){0.f, 0.f, 0.f, 0.f};
    const __amdgpu_buffer_rsrc_t srd = __builtin_amdgcn_make_buffer_rsrc((void*)(ws + WS_K + (size_t)h * 1048576), (short)0, 1048576, 0x00020000);
    float m = -3.0e38f, l = 0.f;
#pragma unroll 1
    for (unsigned k0 = 0; k0 <= r0 + 15u; k0 += 32u) {
        const unsigned tb = (k0 >> 6) * 32768u + (k0 & 32u) * 256u;
        f32x4 s[2]; float mx = m;
#pragma unroll
        for (int kt = 0; kt < 2; ++kt) {
            s[kt] = (f32x4){0.f, 0.f, 0.f, 0.f};
#pragma unroll
            for (int p = 0; p < 4; ++p) s[kt] = mfma(__builtin_amdgcn_raw_buffer_load_b128(srd, lane * 16u + (kt * 4 + p) * 1024u, tb, 16), q[p], s[kt]);
#pragma unroll
            for (int e = 0; e < 4; ++e) { if (k0 + 16u * kt + 4u * g + e > r0 + c) s[kt][e] = -3.0e38f; mx = fmaxf(mx, s[kt][e]); }
        }
        mx = fmaxf(mx, __shfl_xor(mx, 16)); mx = fmaxf(mx, __shfl_xor(mx, 32));
        const float al = __builtin_amdgcn_exp2f(m - mx); m = mx;
        u32x4 pf;
#pragma unroll
        for (int e = 0; e < 4; ++e) pf[e] = cvtpk2(__builtin_amdgcn_exp2f(s[e >> 1][2 * (e & 1)] - m), __builtin_amdgcn_exp2f(s[e >> 1][2 * (e & 1) + 1] - m));
        l *= al;
#pragma unroll
        for (int e = 0; e < 4; ++e) l += __uint_as_float(pf[e] << 16) + __uint_as_float(pf[e] & 0xffff0000u);
#pragma unroll
        for (int n = 0; n < 8; ++n) o[n] = mfma(__builtin_amdgcn_raw_buffer_load_b128(srd, lane * 16u + 16384u + n * 1024u, tb, 16), pf, o[n] * al);
    }
    l += __shfl_xor(l, 16); l += __shfl_xor(l, 32);
    const float f = g1 / l;
    const __amdgpu_buffer_rsrc_t srdA = __builtin_amdgcn_make_buffer_rsrc((void*)(ws + WS_AF), (short)0, 128 * KS_TOT * 1024, 0x00020000);
#pragma unroll
    for (int p = 0; p < 4; ++p) {
        const f32x4 a = o[2 * p] * f, b = o[2 * p + 1] * f;
        __builtin_amdgcn_raw_buffer_store_b128((u32x4){cvtpk2(a[0], a[1]), cvtpk2(a[2], a[3]), cvtpk2(b[0], b[1]), cvtpk2(b[2], b[3])}, srdA, lane * 16u + p * 1024u, ((r0 >> 4) * KS_TOT + 18u + 4u * h) * 1024u, 16);
    }
}
}

#define ATT_ASM \
"L_att0:\n" \
"s_mov_b32 s101, 0\n" \
"s_waitcnt vmcnt(22) lgkmcnt(0)\n" \
"s_barrier\n" \
"v_accvgpr_write_b32 a216, 0\n" \
"v_accvgpr_write_b32 a217, 0\n" \
"v_accvgpr_write_b32 a218, 0\n" \
"v_accvgpr_write_b32 a219, 0\n" \
"v_and_b32_e32 v63, 0x300, v20\n" \
"ds_read_b128 v[96:99], v63 offset:0\n" \
"v_mfma_f32_32x32x16_bf16 v[128:143], a[216:219], a[216:219], 0\n" \
"v_add_u32_e32 v58, %[qa], v20\n" \
"ds_read_b128 a[0:3], v58 offset:0\n" \
"ds_read_b128 a[16:19], v58 offset:8192\n" \
"v_add_u32_e32 v59, %[qa2], v20\n" \
"ds_read_b128 a[32:35], v59 offset:0\n" \
"ds_read_b128 a[48:51], v59 offset:8192\n" \
"v_mfma_f32_32x32x16_bf16 v[144:159], a[216:219], a[216:219], 0\n" \
"ds_read_b128 v[100:103], v63 offset:1024\n" \
"ds_read_b128 a[4:7], v58 offset:1024\n" \
"ds_read_b128 a[20:23], v58 offset:9216\n" \
"s_sub_u32 s67, %[m1], 0xc000\n" \
"s_mov_b32 s65, %[m1]\n" \
"v_add_u32_e32 v55, s67, v20\n" \
"v_mfma_f32_32x32x16_bf16 v[160:175], a[216:219], a[216:219], 0\n" \
"v_add_u32_e32 v52, 0xc000, v55\n" \
"s_xor_b32 m0, s65, 0x10000\n" \
"ds_read_b128 a[36:39], v59 offset:1024\n" \
"buffer_load_dwordx4 v52, %[srd], 0 offen offset:0 sc1 lds\n" \
"ds_read_b128 a[52:55], v59 offset:9216\n" \
"v_mfma_f32_32x32x16_bf16 v[176:191], a[216:219], a[216:219], 0\n" \
"ds_read_b128 v[104:107], v63 offset:2048\n" \
"ds_read_b128 a[8:11], v58 offset:2048\n" \
"ds_read_b128 a[24:27], v58 offset:10240\n" \
"buffer_load_dwordx4 v52, %[srd], 0 offen offset:1024 sc1 lds\n" \
"ds_read_b128 a[40:43], v59 offset:2048\n" \
"v_mfma_f32_32x32x16_bf16 v[192:207], a[216:219], a[216:219], 0\n" \
"ds_read_b128 a[56:59], v59 offset:10240\n" \
"ds_read_b128 v[108:111], v63 offset:3072\n" \
"ds_read_b128 a[12:15], v58 offset:3072\n" \
"ds_read_b128 a[28:31], v58 offset:11264\n" \
"ds_read_b128 a[44:47], v59 offset:3072\n" \
"ds_read_b128 a[60:63], v59 offset:11264\n" \
"v_mfma_f32_32x32x16_bf16 v[208:223], a[216:219], a[216:219], 0\n" \
"s_waitcnt lgkmcnt(15)\n" \
"v_xor_b32_e32 v96, 0x80008000, v96\n" \
"v_mfma_f32_32x32x16_bf16 v[224:239], a[216:219], a[216:219], 0\n" \
"v_xor_b32_e32 v97, 0x80008000, v97\n" \
"v_xor_b32_e32 v98, 0x80008000, v98\n" \
"v_xor_b32_e32 v99, 0x80008000, v99\n" \
"ds_read_b128 a[160:163], v20 offset:0\n" \
"ds_read_b128 a[164:167], v20 offset:1024\n" \
"s_waitcnt lgkmcnt(15)\n" \
"v_xor_b32_e32 v100, 0x80008000, v100\n" \
"v_mfma_f32_32x32x16_bf16 v[240:255], a[216:219], a[216:219], 0\n" \
"v_xor_b32_e32 v101, 0x80008000, v101\n" \
"v_xor_b32_e32 v102, 0x80008000, v102\n" \
"v_xor_b32_e32 v103, 0x80008000, v103\n" \
"ds_read_b128 a[168:171], v20 offset:2048\n" \
"ds_read_b128 a[172:175], v20 offset:3072\n" \
"ds_read_b128 a[176:179], v20 offset:4096\n" \
"v_mfma_f32_32x32x16_bf16 a[192:207], a[216:219], a[216:219], 0\n" \
"s_waitcnt lgkmcnt(14)\n" \
"v_xor_b32_e32 v104, 0x80008000, v104\n" \
"v_xor_b32_e32 v105, 0x80008000, v105\n" \
"v_xor_b32_e32 v106, 0x80008000, v106\n" \
"v_xor_b32_e32 v107, 0x80008000, v107\n" \
"ds_read_b128 a[180:183], v20 offset:5120\n" \
"v_mfma_f32_16x16x32_bf16 v[32:35], v[96:99], a[0:3], 0\n" \
"s_waitcnt lgkmcnt(10)\n" \
"v_xor_b32_e32 v108, 0x80008000, v108\n" \
"v_mfma_f32_16x16x32_bf16 v[36:39], v[96:99], a[16:19], 0\n" \
"v_xor_b32_e32 v109, 0x80008000, v109\n" \
"v_xor_b32_e32 v110, 0x80008000, v110\n" \
"v_mfma_f32_16x16x32_bf16 v[40:43], v[96:99], a[32:35], 0\n" \
"v_xor_b32_e32 v111, 0x80008000, v111\n" \
"ds_read_b128 a[184:187], v20 offset:6144\n" \
"v_mfma_f32_16x16x32_bf16 v[44:47], v[96:99], a[48:51], 0\n" \
"ds_read_b128 a[188:191], v20 offset:7168\n" \
"v_add_u32_e32 v60, %[p4], v20\n" \
"v_mfma_f32_16x16x32_bf16 v[32:35], v[100:103], a[4:7], v[32:35]\n" \
"v_add_u32_e32 v60, 0x10000, v60\n" \
"ds_read_b128 a[128:131], v20 offset:8192\n" \
"v_mfma_f32_16x16x32_bf16 v[36:39], v[100:103], a[20:23], v[36:39]\n" \
"ds_read_b128 a[132:135], v20 offset:9216\n" \
"ds_read_b128 a[136:139], v20 offset:10240\n" \
"v_mfma_f32_16x16x32_bf16 v[40:43], v[100:103], a[36:39], v[40:43]\n" \
"ds_read_b128 a[140:143], v20 offset:11264\n" \
"ds_read_b128 a[144:147], v20 offset:12288\n" \
"v_mfma_f32_16x16x32_bf16 v[44:47], v[100:103], a[52:55], v[44:47]\n" \
"ds_read_b128 a[148:151], v20 offset:13312\n" \
"ds_read_b128 a[152:155], v20 offset:14336\n" \
"v_mfma_f32_16x16x32_bf16 v[32:35], v[104:107], a[8:11], v[32:35]\n" \
"ds_read_b128 a[156:159], v20 offset:15360\n" \
"ds_read_b128 a[64:67], v60 offset:16384\n" \
"v_mfma_f32_16x16x32_bf16 v[36:39], v[104:107], a[24:27], v[36:39]\n" \
"ds_read_b128 a[68:71], v60 offset:17408\n" \
"ds_read_b128 a[72:75], v60 offset:18432\n" \
"v_mfma_f32_16x16x32_bf16 v[40:43], v[104:107], a[40:43], v[40:43]\n" \
"ds_read_b128 a[76:79], v60 offset:19456\n" \
"ds_read_b128 a[80:83], v60 offset:24576\n" \
"v_mfma_f32_16x16x32_bf16 v[44:47], v[104:107], a[56:59], v[44:47]\n" \
"ds_read_b128 a[84:87], v60 offset:25600\n" \
"s_waitcnt lgkmcnt(15)\n" \
"v_mfma_f32_16x16x32_bf16 v[32:35], v[108:111], a[12:15], v[32:35]\n" \
"ds_read_b128 a[88:91], v60 offset:26624\n" \
"ds_read_b128 a[92:95], v60 offset:27648\n" \
"v_mfma_f32_16x16x32_bf16 v[36:39], v[108:111], a[28:31], v[36:39]\n" \
"ds_read_b128 a[96:99], v60 offset:0\n" \
"ds_read_b128 a[100:103], v60 offset:1024\n" \
"v_mfma_f32_16x16x32_bf16 v[40:43], v[108:111], a[44:47], v[40:43]\n" \
"ds_read_b128 a[104:107], v60 offset:2048\n" \
"ds_read_b128 a[108:111], v60 offset:3072\n" \
"v_mfma_f32_16x16x32_bf16 v[44:47], v[108:111], a[60:63], v[44:47]\n" \
"ds_read_b128 a[112:115], v60 offset:8192\n" \
"ds_read_b128 a[116:119], v60 offset:9216\n" \
"v_mfma_f32_16x16x32_bf16 v[64:67], a[160:163], a[0:3], v[32:35]\n" \
"ds_read_b128 a[120:123], v60 offset:10240\n" \
"ds_read_b128 a[124:127], v60 offset:11264\n" \
"v_mfma_f32_16x16x32_bf16 v[64:67], a[164:167], a[4:7], v[64:67]\n" \
"v_mov_b32_e32 v56, s62\n" \
"v_readlane_b32 s68, v11, 0\n" \
"v_mfma_f32_16x16x32_bf16 v[64:67], a[168:171], a[8:11], v[64:67]\n" \
"v_mov_b32_e32 v54, 0x3f803f80\n" \
"v_readlane_b32 s80, v11, 10\n" \
"v_mfma_f32_16x16x32_bf16 v[64:67], a[172:175], a[12:15], v[64:67]\n" \
"v_readlane_b32 s81, v11, 11\n" \
"s_mov_b32 s82, 0x47fca0\n" \
"v_mfma_f32_16x16x32_bf16 v[72:75], a[172:175], a[28:31], v[36:39]\n" \
"s_mov_b32 s83, 0x20000\n" \
"v_bfe_u32 v21, v20, 2, 5\n" \
"v_mfma_f32_16x16x32_bf16 v[72:75], a[168:171], a[24:27], v[72:75]\n" \
"s_mov_b32 s63, 0x30040000\n" \
"v_mov_b32_e32 v62, 0\n" \
"v_mfma_f32_16x16x32_bf16 v[72:75], a[164:167], a[20:23], v[72:75]\n" \
"v_exp_f32_e32 v64, v64\n" \
"v_mfma_f32_16x16x32_bf16 v[72:75], a[160:163], a[16:19], v[72:75]\n" \
"v_exp_f32_e32 v65, v65\n" \
"v_mfma_f32_16x16x32_bf16 v[68:71], a[176:179], a[0:3], v[32:35]\n" \
"v_exp_f32_e32 v66, v66\n" \
"v_mfma_f32_16x16x32_bf16 v[68:71], a[180:183], a[4:7], v[68:71]\n" \
"v_exp_f32_e32 v67, v67\n" \
"v_mfma_f32_16x16x32_bf16 v[68:71], a[184:187], a[8:11], v[68:71]\n" \
"v_cvt_pk_bf16_f32 v64, v64, v65\n" \
"s_waitcnt lgkmcnt(8)\n" \
"v_mfma_f32_16x16x32_bf16 v[68:71], a[188:191], a[12:15], v[68:71]\n" \
"v_exp_f32_e32 v72, v72\n" \
"v_mfma_f32_16x16x32_bf16 v[76:79], a[188:191], a[28:31], v[36:39]\n" \
"v_exp_f32_e32 v73, v73\n" \
"v_mfma_f32_16x16x32_bf16 v[76:79], a[184:187], a[24:27], v[76:79]\n" \
"v_exp_f32_e32 v74, v74\n" \
"v_mfma_f32_16x16x32_bf16 v[76:79], a[180:183], a[20:23], v[76:79]\n" \
"v_exp_f32_e32 v75, v75\n" \
"v_mfma_f32_16x16x32_bf16 v[76:79], a[176:179], a[16:19], v[76:79]\n" \
"v_exp_f32_e32 v68, v68\n" \
"v_mfma_f32_16x16x32_bf16 v[80:83], a[160:163], a[32:35], v[40:43]\n" \
"v_exp_f32_e32 v69, v69\n" \
"v_mfma_f32_16x16x32_bf16 v[80:83], a[164:167], a[36:39], v[80:83]\n" \
"v_exp_f32_e32 v70, v70\n" \
"v_mfma_f32_16x16x32_bf16 v[80:83], a[168:171], a[40:43], v[80:83]\n" \
"v_exp_f32_e32 v71, v71\n" \
"v_mfma_f32_16x16x32_bf16 v[80:83], a[172:175], a[44:47], v[80:83]\n" \
"v_cvt_pk_bf16_f32 v72, v72, v73\n" \
"v_cvt_pk_bf16_f32 v65, v66, v67\n" \
"v_mfma_f32_16x16x32_bf16 v[88:91], a[172:175], a[60:63], v[44:47]\n" \
"v_exp_f32_e32 v76, v76\n" \
"v_mfma_f32_16x16x32_bf16 v[88:91], a[168:171], a[56:59], v[88:91]\n" \
"v_exp_f32_e32 v77, v77\n" \
"v_mfma_f32_16x16x32_bf16 v[88:91], a[164:167], a[52:55], v[88:91]\n" \
"v_exp_f32_e32 v78, v78\n" \
"v_mfma_f32_16x16x32_bf16 v[88:91], a[160:163], a[48:51], v[88:91]\n" \
"v_exp_f32_e32 v79, v79\n" \
"v_mfma_f32_16x16x32_bf16 v[84:87], a[176:179], a[32:35], v[40:43]\n" \
"v_cvt_pk_bf16_f32 v73, v74, v75\n" \
"v_accvgpr_write_b32 a212, v54\n" \
"v_mfma_f32_16x16x32_bf16 v[84:87], a[180:183], a[36:39], v[84:87]\n" \
"v_accvgpr_write_b32 a208, 0\n" \
"L_pf10:\n" \
"s_cbranch_execz L_pf14\n" \
"s_nop 0\n" \
"v_accvgpr_write_b32 a213, v54\n" \
"v_mfma_f32_16x16x32_bf16 v[84:87], a[184:187], a[40:43], v[84:87]\n" \
"v_accvgpr_write_b32 a209, 0\n" \
"v_accvgpr_write_b32 a214, v54\n" \
"v_mfma_f32_16x16x32_bf16 v[84:87], a[188:191], a[44:47], v[84:87]\n" \
"v_accvgpr_write_b32 a210, 0\n" \
"v_accvgpr_write_b32 a215, v54\n" \
"v_mfma_f32_16x16x32_bf16 v[92:95], a[188:191], a[60:63], v[44:47]\n" \
"v_accvgpr_write_b32 a211, 0\n" \
"s_mov_b32 s64, 0x8000\n" \
"v_mfma_f32_16x16x32_bf16 v[92:95], a[184:187], a[56:59], v[92:95]\n" \
"v_add_u32_e32 v50, 0x6000, v55\n" \
"v_add_u32_e32 v51, 0xa000, v55\n" \
"v_mfma_f32_16x16x32_bf16 v[92:95], a[180:183], a[52:55], v[92:95]\n" \
"v_add_u32_e32 v53, 0x10000, v55\n" \
"v_add_u32_e32 v48, 0xc000, v20\n" \
"v_mfma_f32_16x16x32_bf16 v[92:95], a[176:179], a[48:51], v[92:95]\n" \
"v_add_u32_e32 v49, %[v0], v20\n" \
"v_accvgpr_write_b32 a220, v96\n" \
"s_waitcnt vmcnt(2) lgkmcnt(0)\n" \
"s_barrier\n" \
"s_mov_b64 exec, 1\n" \
"buffer_store_dword v56, off, s[56:59], s68 offset:0 sc1\n" \
"buffer_store_dword v56, off, s[56:59], %[fks] sc1\n" \
"s_mov_b64 exec, -1\n" \
"v_accvgpr_write_b32 a221, v97\n" \
"v_accvgpr_write_b32 a222, v98\n" \
"v_accvgpr_write_b32 a223, v99\n" \
"v_accvgpr_write_b32 a224, v100\n" \
"v_accvgpr_write_b32 a225, v101\n" \
"v_accvgpr_write_b32 a226, v102\n" \
"v_accvgpr_write_b32 a227, v103\n" \
"v_accvgpr_write_b32 a228, v104\n" \
"v_accvgpr_write_b32 a229, v105\n" \
"v_accvgpr_write_b32 a230, v106\n" \
"v_accvgpr_write_b32 a231, v107\n" \
"v_accvgpr_write_b32 a232, v108\n" \
"v_accvgpr_write_b32 a233, v109\n" \
"v_accvgpr_write_b32 a234, v110\n" \
"v_accvgpr_write_b32 a235, v111\n" \
"v_cvt_pk_bf16_f32 v66, v68, v69\n" \
"v_cvt_pk_bf16_f32 v67, v70, v71\n" \
"v_cvt_pk_bf16_f32 v74, v76, v77\n" \
"v_cvt_pk_bf16_f32 v75, v78, v79\n" \
"s_sub_u32 s66, %[n1], 1\n" \
"s_cbranch_scc1 L_tr_%=\n" \
"L_1_%=:\n" \
"v_mfma_f32_16x16x32_bf16 v[96:99], a[128:131], a[0:3], v[32:35]\n" \
"s_add_u32 s63, s63, s63\n" \
"s_cbranch_scc1 L_h1_%=\n" \
"L_hr1_%=:\n" \
"v_mfma_f32_16x16x32_bf16 v[96:99], a[132:135], a[4:7], v[96:99]\n" \
"s_xor_b32 m0, s65, 0x12000\n" \
"ds_read_b128 a[160:163], v49 offset:0\n" \
"v_mfma_f32_16x16x32_bf16 v[96:99], a[136:139], a[8:11], v[96:99]\n" \
"buffer_load_dwordx4 v50, %[srd], s64 offen offset:0 sc1 lds\n" \
"v_mfma_f32_16x16x32_bf16 v[96:99], a[140:143], a[12:15], v[96:99]\n" \
"ds_read_b128 a[164:167], v49 offset:1024\n" \
"ds_read_b128 a[168:171], v49 offset:2048\n" \
"v_mfma_f32_16x16x32_bf16 v[104:107], a[140:143], a[28:31], v[36:39]\n" \
"ds_read_b128 a[172:175], v49 offset:3072\n" \
"ds_read_b128 a[176:179], v49 offset:4096\n" \
"v_mfma_f32_16x16x32_bf16 v[104:107], a[136:139], a[24:27], v[104:107]\n" \
"ds_read_b128 a[180:183], v49 offset:5120\n" \
"ds_read_b128 a[184:187], v49 offset:6144\n" \
"v_mfma_f32_16x16x32_bf16 v[104:107], a[132:135], a[20:23], v[104:107]\n" \
"v_exp_f32_e32 v80, v80\n" \
"v_mfma_f32_16x16x32_bf16 v[104:107], a[128:131], a[16:19], v[104:107]\n" \
"v_exp_f32_e32 v81, v81\n" \
"v_mfma_f32_16x16x32_bf16 v[100:103], a[144:147], a[0:3], v[32:35]\n" \
"ds_read_b128 a[188:191], v49 offset:7168\n" \
"v_cvt_pk_bf16_f32 v80, v80, v81\n" \
"v_mfma_f32_16x16x32_bf16 v[100:103], a[148:151], a[4:7], v[100:103]\n" \
"v_exp_f32_e32 v82, v82\n" \
"v_mfma_f32_16x16x32_bf16 v[100:103], a[152:155], a[8:11], v[100:103]\n" \
"v_exp_f32_e32 v83, v83\n" \
"v_mfma_f32_16x16x32_bf16 v[100:103], a[156:159], a[12:15], v[100:103]\n" \
"v_exp_f32_e32 v84, v84\n" \
"v_mfma_f32_16x16x32_bf16 v[108:111], a[156:159], a[28:31], v[36:39]\n" \
"v_exp_f32_e32 v85, v85\n" \
"v_mfma_f32_16x16x32_bf16 v[108:111], a[152:155], a[24:27], v[108:111]\n" \
"v_exp_f32_e32 v86, v86\n" \
"v_mfma_f32_16x16x32_bf16 v[108:111], a[148:151], a[20:23], v[108:111]\n" \
"v_exp_f32_e32 v87, v87\n" \
"v_mfma_f32_16x16x32_bf16 v[108:111], a[144:147], a[16:19], v[108:111]\n" \
"buffer_load_dwordx4 v50, %[srd], s64 offen offset:1024 sc1 lds\n" \
"v_mfma_f32_16x16x32_bf16 v[112:115], a[128:131], a[32:35], v[40:43]\n" \
"s_xor_b32 m0, s65, 0x6000\n" \
"v_cvt_pk_bf16_f32 v81, v82, v83\n" \
"v_mfma_f32_16x16x32_bf16 v[112:115], a[132:135], a[36:39], v[112:115]\n" \
"v_cvt_pk_bf16_f32 v82, v84, v85\n" \
"v_cvt_pk_bf16_f32 v83, v86, v87\n" \
"v_mfma_f32_16x16x32_bf16 v[112:115], a[136:139], a[40:43], v[112:115]\n" \
"v_exp_f32_e32 v88, v88\n" \
"v_mfma_f32_16x16x32_bf16 v[112:115], a[140:143], a[44:47], v[112:115]\n" \
"v_exp_f32_e32 v89, v89\n" \
"v_mfma_f32_16x16x32_bf16 v[120:123], a[140:143], a[60:63], v[44:47]\n" \
"ds_read_b128 a[140:143], v48 offset:3072\n" \
"v_cvt_pk_bf16_f32 v88, v88, v89\n" \
"v_mfma_f32_16x16x32_bf16 v[120:123], a[136:139], a[56:59], v[120:123]\n" \
"v_exp_f32_e32 v90, v90\n" \
"v_mfma_f32_16x16x32_bf16 v[120:123], a[132:135], a[52:55], v[120:123]\n" \
"ds_read_b128 a[132:135], v48 offset:1024\n" \
"ds_read_b128 a[136:139], v48 offset:2048\n" \
"v_mfma_f32_16x16x32_bf16 v[120:123], a[128:131], a[48:51], v[120:123]\n" \
"v_exp_f32_e32 v91, v91\n" \
"v_mfma_f32_16x16x32_bf16 v[116:119], a[144:147], a[32:35], v[40:43]\n" \
"ds_read_b128 a[128:131], v48 offset:0\n" \
"v_cvt_pk_bf16_f32 v89, v90, v91\n" \
"v_mfma_f32_16x16x32_bf16 v[116:119], a[148:151], a[36:39], v[116:119]\n" \
"v_exp_f32_e32 v92, v92\n" \
"v_mfma_f32_16x16x32_bf16 v[116:119], a[152:155], a[40:43], v[116:119]\n" \
"v_exp_f32_e32 v93, v93\n" \
"v_mfma_f32_16x16x32_bf16 v[116:119], a[156:159], a[44:47], v[116:119]\n" \
"v_exp_f32_e32 v94, v94\n" \
"v_mfma_f32_16x16x32_bf16 v[124:127], a[156:159], a[60:63], v[44:47]\n" \
"ds_read_b128 a[156:159], v48 offset:7168\n" \
"v_cvt_pk_bf16_f32 v90, v92, v93\n" \
"v_mfma_f32_16x16x32_bf16 v[124:127], a[152:155], a[56:59], v[124:127]\n" \
"v_exp_f32_e32 v95, v95\n" \
"v_mfma_f32_16x16x32_bf16 v[124:127], a[148:151], a[52:55], v[124:127]\n" \
"ds_read_b128 a[148:151], v48 offset:5120\n" \
"ds_read_b128 a[152:155], v48 offset:6144\n" \
"v_mfma_f32_16x16x32_bf16 v[124:127], a[144:147], a[48:51], v[124:127]\n" \
"ds_read_b128 a[144:147], v48 offset:4096\n" \
"s_waitcnt lgkmcnt(8)\n" \
"v_mfma_f32_16x16x32_bf16 v[128:131], a[160:163], v[64:67], v[128:131]\n" \
"buffer_load_dwordx4 v51, %[srd], s64 offen offset:0 sc1 lds\n" \
"v_mfma_f32_16x16x32_bf16 v[132:135], a[164:167], v[64:67], v[132:135]\n" \
"v_exp_f32_e32 v96, v96\n" \
"v_mfma_f32_16x16x32_bf16 v[136:139], a[168:171], v[64:67], v[136:139]\n" \
"v_exp_f32_e32 v97, v97\n" \
"v_mfma_f32_16x16x32_bf16 v[140:143], a[172:175], v[64:67], v[140:143]\n" \
"v_cvt_pk_bf16_f32 v91, v94, v95\n" \
"v_cvt_pk_bf16_f32 v96, v96, v97\n" \
"v_mfma_f32_16x16x32_bf16 v[144:147], a[176:179], v[64:67], v[144:147]\n" \
"v_exp_f32_e32 v98, v98\n" \
"v_mfma_f32_16x16x32_bf16 v[148:151], a[180:183], v[64:67], v[148:151]\n" \
"v_exp_f32_e32 v99, v99\n" \
"v_mfma_f32_16x16x32_bf16 v[152:155], a[184:187], v[64:67], v[152:155]\n" \
"v_exp_f32_e32 v100, v100\n" \
"v_mfma_f32_16x16x32_bf16 v[156:159], a[188:191], v[64:67], v[156:159]\n" \
"v_exp_f32_e32 v101, v101\n" \
"v_mfma_f32_16x16x32_bf16 a[192:195], a[212:215], v[64:67], a[192:195]\n" \
"v_exp_f32_e32 v102, v102\n" \
"v_mfma_f32_16x16x32_bf16 v[160:163], a[160:163], v[72:75], v[160:163]\n" \
"v_exp_f32_e32 v103, v103\n" \
"v_mfma_f32_16x16x32_bf16 v[164:167], a[164:167], v[72:75], v[164:167]\n" \
"v_exp_f32_e32 v104, v104\n" \
"v_mfma_f32_16x16x32_bf16 v[168:171], a[168:171], v[72:75], v[168:171]\n" \
"v_exp_f32_e32 v105, v105\n" \
"v_mfma_f32_16x16x32_bf16 v[172:175], a[172:175], v[72:75], v[172:175]\n" \
"v_cvt_pk_bf16_f32 v97, v98, v99\n" \
"v_cvt_pk_bf16_f32 v98, v100, v101\n" \
"v_mfma_f32_16x16x32_bf16 v[176:179], a[176:179], v[72:75], v[176:179]\n" \
"v_cvt_pk_bf16_f32 v99, v102, v103\n" \
"v_cvt_pk_bf16_f32 v104, v104, v105\n" \
"v_mfma_f32_16x16x32_bf16 v[180:183], a[180:183], v[72:75], v[180:183]\n" \
"buffer_load_dwordx4 v51, %[srd], s64 offen offset:1024 sc1 lds\n" \
"v_mfma_f32_16x16x32_bf16 v[184:187], a[184:187], v[72:75], v[184:187]\n" \
"v_exp_f32_e32 v106, v106\n" \
"v_mfma_f32_16x16x32_bf16 v[188:191], a[188:191], v[72:75], v[188:191]\n" \
"v_exp_f32_e32 v107, v107\n" \
"v_mfma_f32_16x16x32_bf16 a[196:199], a[212:215], v[72:75], a[196:199]\n" \
"v_exp_f32_e32 v108, v108\n" \
"v_mfma_f32_16x16x32_bf16 v[192:195], a[160:163], v[80:83], v[192:195]\n" \
"v_exp_f32_e32 v109, v109\n" \
"v_mfma_f32_16x16x32_bf16 v[196:199], a[164:167], v[80:83], v[196:199]\n" \
"v_exp_f32_e32 v110, v110\n" \
"v_mfma_f32_16x16x32_bf16 v[200:203], a[168:171], v[80:83], v[200:203]\n" \
"v_exp_f32_e32 v111, v111\n" \
"v_mfma_f32_16x16x32_bf16 v[204:207], a[172:175], v[80:83], v[204:207]\n" \
"v_exp_f32_e32 v112, v112\n" \
"v_mfma_f32_16x16x32_bf16 v[208:211], a[176:179], v[80:83], v[208:211]\n" \
"v_exp_f32_e32 v113, v113\n" \
"v_mfma_f32_16x16x32_bf16 v[212:215], a[180:183], v[80:83], v[212:215]\n" \
"v_cvt_pk_bf16_f32 v105, v106, v107\n" \
"v_cvt_pk_bf16_f32 v106, v108, v109\n" \
"v_mfma_f32_16x16x32_bf16 v[216:219], a[184:187], v[80:83], v[216:219]\n" \
"v_cvt_pk_bf16_f32 v107, v110, v111\n" \
"v_cvt_pk_bf16_f32 v112, v112, v113\n" \
"v_mfma_f32_16x16x32_bf16 v[220:223], a[188:191], v[80:83], v[220:223]\n" \
"v_exp_f32_e32 v114, v114\n" \
"v_mfma_f32_16x16x32_bf16 a[200:203], a[212:215], v[80:83], a[200:203]\n" \
"v_exp_f32_e32 v115, v115\n" \
"v_mfma_f32_16x16x32_bf16 v[224:227], a[160:163], v[88:91], v[224:227]\n" \
"v_exp_f32_e32 v116, v116\n" \
"v_mfma_f32_16x16x32_bf16 v[228:231], a[164:167], v[88:91], v[228:231]\n" \
"v_exp_f32_e32 v117, v117\n" \
"v_mfma_f32_16x16x32_bf16 v[232:235], a[168:171], v[88:91], v[232:235]\n" \
"v_exp_f32_e32 v118, v118\n" \
"v_mfma_f32_16x16x32_bf16 v[236:239], a[172:175], v[88:91], v[236:239]\n" \
"v_exp_f32_e32 v119, v119\n" \
"v_mfma_f32_16x16x32_bf16 v[240:243], a[176:179], v[88:91], v[240:243]\n" \
"v_exp_f32_e32 v120, v120\n" \
"v_mfma_f32_16x16x32_bf16 v[244:247], a[180:183], v[88:91], v[244:247]\n" \
"v_exp_f32_e32 v121, v121\n" \
"v_mfma_f32_16x16x32_bf16 v[248:251], a[184:187], v[88:91], v[248:251]\n" \
"v_cvt_pk_bf16_f32 v113, v114, v115\n" \
"v_cvt_pk_bf16_f32 v114, v116, v117\n" \
"v_mfma_f32_16x16x32_bf16 v[252:255], a[188:191], v[88:91], v[252:255]\n" \
"v_cvt_pk_bf16_f32 v115, v118, v119\n" \
"v_cvt_pk_bf16_f32 v120, v120, v121\n" \
"v_mfma_f32_16x16x32_bf16 a[204:207], a[212:215], v[88:91], a[204:207]\n" \
"v_exp_f32_e32 v122, v122\n" \
"s_waitcnt vmcnt(4) lgkmcnt(0)\n" \
"s_barrier\n" \
"v_mfma_f32_16x16x32_bf16 v[64:67], a[128:131], a[0:3], v[32:35]\n" \
"s_xor_b32 m0, s65, 0x14000\n" \
"ds_read_b128 a[160:163], v49 offset:8192\n" \
"v_mfma_f32_16x16x32_bf16 v[64:67], a[132:135], a[4:7], v[64:67]\n" \
"buffer_load_dwordx4 v52, %[srd], s64 offen offset:0 sc1 lds\n" \
"v_mfma_f32_16x16x32_bf16 v[64:67], a[136:139], a[8:11], v[64:67]\n" \
"ds_read_b128 a[164:167], v49 offset:9216\n" \
"ds_read_b128 a[168:171], v49 offset:10240\n" \
"v_mfma_f32_16x16x32_bf16 v[64:67], a[140:143], a[12:15], v[64:67]\n" \
"ds_read_b128 a[172:175], v49 offset:11264\n" \
"ds_read_b128 a[176:179], v49 offset:12288\n" \
"v_mfma_f32_16x16x32_bf16 v[72:75], a[140:143], a[28:31], v[36:39]\n" \
"ds_read_b128 a[180:183], v49 offset:13312\n" \
"ds_read_b128 a[184:187], v49 offset:14336\n" \
"v_mfma_f32_16x16x32_bf16 v[72:75], a[136:139], a[24:27], v[72:75]\n" \
"v_exp_f32_e32 v123, v123\n" \
"v_mfma_f32_16x16x32_bf16 v[72:75], a[132:135], a[20:23], v[72:75]\n" \
"ds_read_b128 a[188:191], v49 offset:15360\n" \
"v_cvt_pk_bf16_f32 v121, v122, v123\n" \
"v_mfma_f32_16x16x32_bf16 v[72:75], a[128:131], a[16:19], v[72:75]\n" \
"v_exp_f32_e32 v124, v124\n" \
"v_mfma_f32_16x16x32_bf16 v[68:71], a[144:147], a[0:3], v[32:35]\n" \
"v_exp_f32_e32 v125, v125\n" \
"v_mfma_f32_16x16x32_bf16 v[68:71], a[148:151], a[4:7], v[68:71]\n" \
"v_exp_f32_e32 v126, v126\n" \
"v_mfma_f32_16x16x32_bf16 v[68:71], a[152:155], a[8:11], v[68:71]\n" \
"v_exp_f32_e32 v127, v127\n" \
"v_mfma_f32_16x16x32_bf16 v[68:71], a[156:159], a[12:15], v[68:71]\n" \
"v_cvt_pk_bf16_f32 v122, v124, v125\n" \
"v_cvt_pk_bf16_f32 v123, v126, v127\n" \
"v_mfma_f32_16x16x32_bf16 v[76:79], a[156:159], a[28:31], v[36:39]\n" \
"v_exp_f32_e32 v64, v64\n" \
"v_mfma_f32_16x16x32_bf16 v[76:79], a[152:155], a[24:27], v[76:79]\n" \
"v_exp_f32_e32 v65, v65\n" \
"v_mfma_f32_16x16x32_bf16 v[76:79], a[148:151], a[20:23], v[76:79]\n" \
"v_exp_f32_e32 v72, v72\n" \
"v_mfma_f32_16x16x32_bf16 v[76:79], a[144:147], a[16:19], v[76:79]\n" \
"buffer_load_dwordx4 v52, %[srd], s64 offen offset:1024 sc1 lds\n" \
"v_mfma_f32_16x16x32_bf16 v[80:83], a[128:131], a[32:35], v[40:43]\n" \
"s_mov_b32 m0, s65\n" \
"v_cvt_pk_bf16_f32 v64, v64, v65\n" \
"v_mfma_f32_16x16x32_bf16 v[80:83], a[132:135], a[36:39], v[80:83]\n" \
"v_exp_f32_e32 v73, v73\n" \
"v_mfma_f32_16x16x32_bf16 v[80:83], a[136:139], a[40:43], v[80:83]\n" \
"v_exp_f32_e32 v66, v66\n" \
"v_mfma_f32_16x16x32_bf16 v[80:83], a[140:143], a[44:47], v[80:83]\n" \
"v_exp_f32_e32 v67, v67\n" \
"v_mfma_f32_16x16x32_bf16 v[88:91], a[140:143], a[60:63], v[44:47]\n" \
"ds_read_b128 a[140:143], v48 offset:11264\n" \
"v_cvt_pk_bf16_f32 v72, v72, v73\n" \
"v_mfma_f32_16x16x32_bf16 v[88:91], a[136:139], a[56:59], v[88:91]\n" \
"ds_read_b128 a[136:139], v48 offset:10240\n" \
"v_cvt_pk_bf16_f32 v65, v66, v67\n" \
"v_mfma_f32_16x16x32_bf16 v[88:91], a[132:135], a[52:55], v[88:91]\n" \
"ds_read_b128 a[132:135], v48 offset:9216\n" \
"v_add_u32_e32 v49, 0x10000, v48\n" \
"v_mfma_f32_16x16x32_bf16 v[88:91], a[128:131], a[48:51], v[88:91]\n" \
"ds_read_b128 a[128:131], v48 offset:8192\n" \
"s_xor_b32 s65, s65, 0x4000\n" \
"v_mfma_f32_16x16x32_bf16 v[84:87], a[144:147], a[32:35], v[40:43]\n" \
"v_exp_f32_e32 v74, v74\n" \
"v_mfma_f32_16x16x32_bf16 v[84:87], a[148:151], a[36:39], v[84:87]\n" \
"v_exp_f32_e32 v75, v75\n" \
"v_mfma_f32_16x16x32_bf16 v[84:87], a[152:155], a[40:43], v[84:87]\n" \
"v_exp_f32_e32 v68, v68\n" \
"v_mfma_f32_16x16x32_bf16 v[84:87], a[156:159], a[44:47], v[84:87]\n" \
"v_exp_f32_e32 v69, v69\n" \
"v_mfma_f32_16x16x32_bf16 v[92:95], a[156:159], a[60:63], v[44:47]\n" \
"ds_read_b128 a[156:159], v48 offset:15360\n" \
"v_cvt_pk_bf16_f32 v73, v74, v75\n" \
"v_mfma_f32_16x16x32_bf16 v[92:95], a[152:155], a[56:59], v[92:95]\n" \
"ds_read_b128 a[152:155], v48 offset:14336\n" \
"v_cvt_pk_bf16_f32 v66, v68, v69\n" \
"v_mfma_f32_16x16x32_bf16 v[92:95], a[148:151], a[52:55], v[92:95]\n" \
"v_exp_f32_e32 v70, v70\n" \
"v_mfma_f32_16x16x32_bf16 v[92:95], a[144:147], a[48:51], v[92:95]\n" \
"ds_read_b128 a[144:147], v48 offset:12288\n" \
"s_waitcnt lgkmcnt(7)\n" \
"v_mfma_f32_16x16x32_bf16 v[128:131], a[160:163], v[96:99], v[128:131]\n" \
"buffer_load_dwordx4 v53, %[srd], s64 offen offset:0 sc1 lds\n" \
"v_mfma_f32_16x16x32_bf16 v[132:135], a[164:167], v[96:99], v[132:135]\n" \
"v_exp_f32_e32 v71, v71\n" \
"v_mfma_f32_16x16x32_bf16 v[136:139], a[168:171], v[96:99], v[136:139]\n" \
"ds_read_b128 a[148:151], v48 offset:13312\n" \
"v_cvt_pk_bf16_f32 v67, v70, v71\n" \
"v_mfma_f32_16x16x32_bf16 v[140:143], a[172:175], v[96:99], v[140:143]\n" \
"v_exp_f32_e32 v76, v76\n" \
"v_mfma_f32_16x16x32_bf16 v[144:147], a[176:179], v[96:99], v[144:147]\n" \
"v_exp_f32_e32 v77, v77\n" \
"v_mfma_f32_16x16x32_bf16 v[148:151], a[180:183], v[96:99], v[148:151]\n" \
"v_exp_f32_e32 v78, v78\n" \
"v_mfma_f32_16x16x32_bf16 v[152:155], a[184:187], v[96:99], v[152:155]\n" \
"v_exp_f32_e32 v79, v79\n" \
"v_mfma_f32_16x16x32_bf16 v[156:159], a[188:191], v[96:99], v[156:159]\n" \
"v_cvt_pk_bf16_f32 v74, v76, v77\n" \
"v_cvt_pk_bf16_f32 v75, v78, v79\n" \
"v_mfma_f32_16x16x32_bf16 a[192:195], a[212:215], v[96:99], a[192:195]\n" \
"v_xor_b32_e32 v48, 0x4000, v48\n" \
"v_mfma_f32_16x16x32_bf16 v[160:163], a[160:163], v[104:107], v[160:163]\n" \
"v_mfma_f32_16x16x32_bf16 v[164:167], a[164:167], v[104:107], v[164:167]\n" \
"v_mfma_f32_16x16x32_bf16 v[168:171], a[168:171], v[104:107], v[168:171]\n" \
"v_mfma_f32_16x16x32_bf16 v[172:175], a[172:175], v[104:107], v[172:175]\n" \
"v_mfma_f32_16x16x32_bf16 v[176:179], a[176:179], v[104:107], v[176:179]\n" \
"v_mfma_f32_16x16x32_bf16 v[180:183], a[180:183], v[104:107], v[180:183]\n" \
"buffer_load_dwordx4 v53, %[srd], s64 offen offset:1024 sc1 lds\n" \
"v_mfma_f32_16x16x32_bf16 v[184:187], a[184:187], v[104:107], v[184:187]\n" \
"s_add_u32 s64, s64, 0x8000\n" \
"s_sub_u32 s66, s66, 1\n" \
"v_mfma_f32_16x16x32_bf16 v[188:191], a[188:191], v[104:107], v[188:191]\n" \
"v_mfma_f32_16x16x32_bf16 a[196:199], a[212:215], v[104:107], a[196:199]\n" \
"v_mfma_f32_16x16x32_bf16 v[192:195], a[160:163], v[112:115], v[192:195]\n" \
"v_mfma_f32_16x16x32_bf16 v[196:199], a[164:167], v[112:115], v[196:199]\n" \
"v_mfma_f32_16x16x32_bf16 v[200:203], a[168:171], v[112:115], v[200:203]\n" \
"v_mfma_f32_16x16x32_bf16 v[204:207], a[172:175], v[112:115], v[204:207]\n" \
"v_mfma_f32_16x16x32_bf16 v[208:211], a[176:179], v[112:115], v[208:211]\n" \
"v_mfma_f32_16x16x32_bf16 v[212:215], a[180:183], v[112:115], v[212:215]\n" \
"v_mfma_f32_16x16x32_bf16 v[216:219], a[184:187], v[112:115], v[216:219]\n" \
"v_mfma_f32_16x16x32_bf16 v[220:223], a[188:191], v[112:115], v[220:223]\n" \
"v_mfma_f32_16x16x32_bf16 a[200:203], a[212:215], v[112:115], a[200:203]\n" \
"v_mfma_f32_16x16x32_bf16 v[224:227], a[160:163], v[120:123], v[224:227]\n" \
"v_mfma_f32_16x16x32_bf16 v[228:231], a[164:167], v[120:123], v[228:231]\n" \
"v_mfma_f32_16x16x32_bf16 v[232:235], a[168:171], v[120:123], v[232:235]\n" \
"v_mfma_f32_16x16x32_bf16 v[236:239], a[172:175], v[120:123], v[236:239]\n" \
"v_mfma_f32_16x16x32_bf16 v[240:243], a[176:179], v[120:123], v[240:243]\n" \
"v_mfma_f32_16x16x32_bf16 v[244:247], a[180:183], v[120:123], v[244:247]\n" \
"v_mfma_f32_16x16x32_bf16 v[248:251], a[184:187], v[120:123], v[248:251]\n" \
"v_mfma_f32_16x16x32_bf16 v[252:255], a[188:191], v[120:123], v[252:255]\n" \
"v_mfma_f32_16x16x32_bf16 a[204:207], a[212:215], v[120:123], a[204:207]\n" \
"s_waitcnt vmcnt(4) lgkmcnt(0)\n" \
"s_barrier\n" \
"s_cbranch_scc0 L_1_%=\n" \
"L_tr_%=:\n" \
"L_pf11:\n" \
"s_cbranch_execz L_pf15\n" \
"s_nop 0\n" \
"s_cmp_lg_u32 %[k2], 0\n" \
"s_cbranch_scc1 L_sw_%=\n" \
"v_mfma_f32_16x16x32_bf16 v[96:99], a[128:131], a[0:3], v[32:35]\n" \
"s_xor_b32 m0, s65, 0x12000\n" \
"s_mov_b32 s65, %[m2]\n" \
"v_mfma_f32_16x16x32_bf16 v[96:99], a[132:135], a[4:7], v[96:99]\n" \
"buffer_load_dwordx4 v50, %[srd], s64 offen offset:0 sc1 lds\n" \
"v_mfma_f32_16x16x32_bf16 v[96:99], a[136:139], a[8:11], v[96:99]\n" \
"v_add_u32_e32 v63, %[p4], v20\n" \
"s_add_u32 s67, %[t2], 40960\n" \
"v_mfma_f32_16x16x32_bf16 v[96:99], a[140:143], a[12:15], v[96:99]\n" \
"ds_read_b128 a[160:163], v49 offset:0\n" \
"ds_read_b128 a[164:167], v49 offset:1024\n" \
"v_mfma_f32_16x16x32_bf16 v[104:107], a[140:143], a[28:31], v[36:39]\n" \
"buffer_load_dwordx4 v50, %[srd], s64 offen offset:1024 sc1 lds\n" \
"v_mfma_f32_16x16x32_bf16 v[104:107], a[136:139], a[24:27], v[104:107]\n" \
"v_add_u32_e32 v50, 0x6000, v63\n" \
"s_xor_b32 m0, s65, 0x8000\n" \
"v_mfma_f32_16x16x32_bf16 v[104:107], a[132:135], a[20:23], v[104:107]\n" \
"ds_read_b128 a[168:171], v49 offset:2048\n" \
"ds_read_b128 a[172:175], v49 offset:3072\n" \
"v_mfma_f32_16x16x32_bf16 v[104:107], a[128:131], a[16:19], v[104:107]\n" \
"ds_read_b128 a[176:179], v49 offset:4096\n" \
"ds_read_b128 a[180:183], v49 offset:5120\n" \
"v_mfma_f32_16x16x32_bf16 v[100:103], a[144:147], a[0:3], v[32:35]\n" \
"buffer_load_dwordx4 v50, %[srd], s67 offen offset:0 sc1 lds\n" \
"v_mfma_f32_16x16x32_bf16 v[100:103], a[148:151], a[4:7], v[100:103]\n" \
"ds_read_b128 a[184:187], v49 offset:6144\n" \
"ds_read_b128 a[188:191], v49 offset:7168\n" \
"v_mfma_f32_16x16x32_bf16 v[100:103], a[152:155], a[8:11], v[100:103]\n" \
"s_mov_b32 s64, %[t2]\n" \
"v_add_u32_e32 v51, 0x12000, v63\n" \
"v_mfma_f32_16x16x32_bf16 v[100:103], a[156:159], a[12:15], v[100:103]\n" \
"v_exp_f32_e32 v80, v80\n" \
"v_mfma_f32_16x16x32_bf16 v[108:111], a[156:159], a[28:31], v[36:39]\n" \
"v_exp_f32_e32 v81, v81\n" \
"v_mfma_f32_16x16x32_bf16 v[108:111], a[152:155], a[24:27], v[108:111]\n" \
"buffer_load_dwordx4 v50, %[srd], s67 offen offset:1024 sc1 lds\n" \
"v_mfma_f32_16x16x32_bf16 v[108:111], a[148:151], a[20:23], v[108:111]\n" \
"v_exp_f32_e32 v82, v82\n" \
"v_mfma_f32_16x16x32_bf16 v[108:111], a[144:147], a[16:19], v[108:111]\n" \
"v_exp_f32_e32 v83, v83\n" \
"v_mfma_f32_16x16x32_bf16 v[112:115], a[128:131], a[32:35], v[40:43]\n" \
"v_cvt_pk_bf16_f32 v80, v80, v81\n" \
"v_cvt_pk_bf16_f32 v81, v82, v83\n" \
"v_mfma_f32_16x16x32_bf16 v[112:115], a[132:135], a[36:39], v[112:115]\n" \
"buffer_load_dwordx4 v50, %[srd], s67 offen offset:2048 sc1 lds\n" \
"v_mfma_f32_16x16x32_bf16 v[112:115], a[136:139], a[40:43], v[112:115]\n" \
"v_exp_f32_e32 v84, v84\n" \
"v_mfma_f32_16x16x32_bf16 v[112:115], a[140:143], a[44:47], v[112:115]\n" \
"v_exp_f32_e32 v85, v85\n" \
"v_mfma_f32_16x16x32_bf16 v[120:123], a[140:143], a[60:63], v[44:47]\n" \
"v_exp_f32_e32 v86, v86\n" \
"v_mfma_f32_16x16x32_bf16 v[120:123], a[136:139], a[56:59], v[120:123]\n" \
"v_exp_f32_e32 v87, v87\n" \
"v_mfma_f32_16x16x32_bf16 v[120:123], a[132:135], a[52:55], v[120:123]\n" \
"buffer_load_dwordx4 v50, %[srd], s67 offen offset:3072 sc1 lds\n" \
"v_mfma_f32_16x16x32_bf16 v[120:123], a[128:131], a[48:51], v[120:123]\n" \
"s_xor_b32 m0, s65, 0xa000\n" \
"v_cvt_pk_bf16_f32 v82, v84, v85\n" \
"v_mfma_f32_16x16x32_bf16 v[116:119], a[144:147], a[32:35], v[40:43]\n" \
"v_cvt_pk_bf16_f32 v83, v86, v87\n" \
"v_readlane_b32 s93, v11, 18\n" \
"v_mfma_f32_16x16x32_bf16 v[116:119], a[148:151], a[36:39], v[116:119]\n" \
"v_readlane_b32 s92, v11, 23\n" \
"ds_read_b128 a[128:131], v48 offset:0\n" \
"v_mfma_f32_16x16x32_bf16 v[116:119], a[152:155], a[40:43], v[116:119]\n" \
"buffer_load_dwordx4 v51, %[srd], s64 offen offset:0 sc1 lds\n" \
"v_mfma_f32_16x16x32_bf16 v[116:119], a[156:159], a[44:47], v[116:119]\n" \
"ds_read_b128 a[132:135], v48 offset:1024\n" \
"ds_read_b128 a[136:139], v48 offset:2048\n" \
"v_mfma_f32_16x16x32_bf16 v[124:127], a[156:159], a[60:63], v[44:47]\n" \
"ds_read_b128 a[140:143], v48 offset:3072\n" \
"ds_read_b128 a[156:159], v48 offset:7168\n" \
"v_mfma_f32_16x16x32_bf16 v[124:127], a[152:155], a[56:59], v[124:127]\n" \
"ds_read_b128 a[152:155], v48 offset:6144\n" \
"v_add_u32_e32 v52, 0x14000, v63\n" \
"v_mfma_f32_16x16x32_bf16 v[124:127], a[148:151], a[52:55], v[124:127]\n" \
"ds_read_b128 a[148:151], v48 offset:5120\n" \
"v_add_u32_e32 v53, 0x20000, v63\n" \
"v_mfma_f32_16x16x32_bf16 v[124:127], a[144:147], a[48:51], v[124:127]\n" \
"ds_read_b128 a[144:147], v48 offset:4096\n" \
"s_waitcnt lgkmcnt(8)\n" \
"v_mfma_f32_16x16x32_bf16 v[128:131], a[160:163], v[64:67], v[128:131]\n" \
"buffer_load_dwordx4 v51, %[srd], s64 offen offset:1024 sc1 lds\n" \
"v_mfma_f32_16x16x32_bf16 v[132:135], a[164:167], v[64:67], v[132:135]\n" \
"v_exp_f32_e32 v88, v88\n" \
"v_mfma_f32_16x16x32_bf16 v[136:139], a[168:171], v[64:67], v[136:139]\n" \
"v_exp_f32_e32 v89, v89\n" \
"v_mfma_f32_16x16x32_bf16 v[140:143], a[172:175], v[64:67], v[140:143]\n" \
"buffer_load_dwordx4 v51, %[srd], s64 offen offset:2048 sc1 lds\n" \
"v_mfma_f32_16x16x32_bf16 v[144:147], a[176:179], v[64:67], v[144:147]\n" \
"v_exp_f32_e32 v90, v90\n" \
"v_mfma_f32_16x16x32_bf16 v[148:151], a[180:183], v[64:67], v[148:151]\n" \
"v_exp_f32_e32 v91, v91\n" \
"v_mfma_f32_16x16x32_bf16 v[152:155], a[184:187], v[64:67], v[152:155]\n" \
"v_cvt_pk_bf16_f32 v88, v88, v89\n" \
"v_cvt_pk_bf16_f32 v89, v90, v91\n" \
"v_mfma_f32_16x16x32_bf16 v[156:159], a[188:191], v[64:67], v[156:159]\n" \
"buffer_load_dwordx4 v51, %[srd], s64 offen offset:3072 sc1 lds\n" \
"v_mfma_f32_16x16x32_bf16 a[192:195], a[212:215], v[64:67], a[192:195]\n" \
"s_add_u32 m0, s93, 0x0\n" \
"s_mov_b32 s66, %[n2]\n" \
"v_mfma_f32_16x16x32_bf16 v[160:163], a[160:163], v[72:75], v[160:163]\n" \
"v_exp_f32_e32 v92, v92\n" \
"v_mfma_f32_16x16x32_bf16 v[164:167], a[164:167], v[72:75], v[164:167]\n" \
"v_exp_f32_e32 v93, v93\n" \
"v_mfma_f32_16x16x32_bf16 v[168:171], a[168:171], v[72:75], v[168:171]\n" \
"v_exp_f32_e32 v94, v94\n" \
"v_mfma_f32_16x16x32_bf16 v[172:175], a[172:175], v[72:75], v[172:175]\n" \
"buffer_load_dwordx4 v0, s[80:83], s92 offen offset:0 lds\n" \
"v_mfma_f32_16x16x32_bf16 v[176:179], a[176:179], v[72:75], v[176:179]\n" \
"v_exp_f32_e32 v95, v95\n" \
"v_mfma_f32_16x16x32_bf16 v[180:183], a[180:183], v[72:75], v[180:183]\n" \
"v_cvt_pk_bf16_f32 v90, v92, v93\n" \
"v_cvt_pk_bf16_f32 v91, v94, v95\n" \
"v_mfma_f32_16x16x32_bf16 v[184:187], a[184:187], v[72:75], v[184:187]\n" \
"v_exp_f32_e32 v96, v96\n" \
"v_mfma_f32_16x16x32_bf16 v[188:191], a[188:191], v[72:75], v[188:191]\n" \
"buffer_load_dwordx4 v1, s[80:83], s92 offen offset:1024 lds\n" \
"v_mfma_f32_16x16x32_bf16 a[196:199], a[212:215], v[72:75], a[196:199]\n" \
"v_exp_f32_e32 v97, v97\n" \
"v_mfma_f32_16x16x32_bf16 v[192:195], a[160:163], v[80:83], v[192:195]\n" \
"v_exp_f32_e32 v98, v98\n" \
"v_mfma_f32_16x16x32_bf16 v[196:199], a[164:167], v[80:83], v[196:199]\n" \
"v_exp_f32_e32 v99, v99\n" \
"v_mfma_f32_16x16x32_bf16 v[200:203], a[168:171], v[80:83], v[200:203]\n" \
"v_exp_f32_e32 v100, v100\n" \
"v_mfma_f32_16x16x32_bf16 v[204:207], a[172:175], v[80:83], v[204:207]\n" \
"buffer_load_dwordx4 v2, s[80:83], s92 offen offset:2048 lds\n" \
"v_mfma_f32_16x16x32_bf16 v[208:211], a[176:179], v[80:83], v[208:211]\n" \
"v_exp_f32_e32 v101, v101\n" \
"v_mfma_f32_16x16x32_bf16 v[212:215], a[180:183], v[80:83], v[212:215]\n" \
"v_exp_f32_e32 v102, v102\n" \
"v_mfma_f32_16x16x32_bf16 v[216:219], a[184:187], v[80:83], v[216:219]\n" \
"v_exp_f32_e32 v103, v103\n" \
"v_mfma_f32_16x16x32_bf16 v[220:223], a[188:191], v[80:83], v[220:223]\n" \
"buffer_load_dwordx4 v3, s[80:83], s92 offen offset:3072 lds\n" \
"v_mfma_f32_16x16x32_bf16 a[200:203], a[212:215], v[80:83], a[200:203]\n" \
"v_exp_f32_e32 v104, v104\n" \
"v_mfma_f32_16x16x32_bf16 v[224:227], a[160:163], v[88:91], v[224:227]\n" \
"v_exp_f32_e32 v105, v105\n" \
"v_mfma_f32_16x16x32_bf16 v[228:231], a[164:167], v[88:91], v[228:231]\n" \
"v_cvt_pk_bf16_f32 v96, v96, v97\n" \
"v_cvt_pk_bf16_f32 v97, v98, v99\n" \
"v_mfma_f32_16x16x32_bf16 v[232:235], a[168:171], v[88:91], v[232:235]\n" \
"v_cvt_pk_bf16_f32 v98, v100, v101\n" \
"v_cvt_pk_bf16_f32 v99, v102, v103\n" \
"v_mfma_f32_16x16x32_bf16 v[236:239], a[172:175], v[88:91], v[236:239]\n" \
"v_exp_f32_e32 v106, v106\n" \
"v_mfma_f32_16x16x32_bf16 v[240:243], a[176:179], v[88:91], v[240:243]\n" \
"v_exp_f32_e32 v107, v107\n" \
"v_mfma_f32_16x16x32_bf16 v[244:247], a[180:183], v[88:91], v[244:247]\n" \
"v_exp_f32_e32 v108, v108\n" \
"v_mfma_f32_16x16x32_bf16 v[248:251], a[184:187], v[88:91], v[248:251]\n" \
"v_exp_f32_e32 v109, v109\n" \
"v_mfma_f32_16x16x32_bf16 v[252:255], a[188:191], v[88:91], v[252:255]\n" \
"v_exp_f32_e32 v110, v110\n" \
"v_mfma_f32_16x16x32_bf16 a[204:207], a[212:215], v[88:91], a[204:207]\n" \
"v_cvt_pk_bf16_f32 v104, v104, v105\n" \
"v_cvt_pk_bf16_f32 v105, v106, v107\n" \
"s_waitcnt vmcnt(14) lgkmcnt(0)\n" \
"s_mov_b64 exec, 0\n" \
"s_cmp_lg_u32 %[p4], 0\n" \
"s_cbranch_scc1 L_pf21\n" \
"s_branch L_pf20\n" \
"L_retB:\n" \
"s_mov_b64 exec, -1\n" \
"s_barrier\n" \
"v_mfma_f32_16x16x32_bf16 v[64:67], a[128:131], a[0:3], v[32:35]\n" \
"s_xor_b32 m0, s65, 0x18000\n" \
"ds_read_b128 a[160:163], v49 offset:8192\n" \
"v_mfma_f32_16x16x32_bf16 v[64:67], a[132:135], a[4:7], v[64:67]\n" \
"buffer_load_dwordx4 v52, %[srd], s64 offen offset:0 sc1 lds\n" \
"v_mfma_f32_16x16x32_bf16 v[64:67], a[136:139], a[8:11], v[64:67]\n" \
"ds_read_b128 a[164:167], v49 offset:9216\n" \
"ds_read_b128 a[168:171], v49 offset:10240\n" \
"v_mfma_f32_16x16x32_bf16 v[64:67], a[140:143], a[12:15], v[64:67]\n" \
"ds_read_b128 a[172:175], v49 offset:11264\n" \
"ds_read_b128 a[176:179], v49 offset:12288\n" \
"v_mfma_f32_16x16x32_bf16 v[72:75], a[140:143], a[28:31], v[36:39]\n" \
"buffer_load_dwordx4 v52, %[srd], s64 offen offset:1024 sc1 lds\n" \
"v_mfma_f32_16x16x32_bf16 v[72:75], a[136:139], a[24:27], v[72:75]\n" \
"ds_read_b128 a[180:183], v49 offset:13312\n" \
"ds_read_b128 a[184:187], v49 offset:14336\n" \
"v_mfma_f32_16x16x32_bf16 v[72:75], a[132:135], a[20:23], v[72:75]\n" \
"ds_read_b128 a[188:191], v49 offset:15360\n" \
"v_cvt_pk_bf16_f32 v106, v108, v109\n" \
"v_mfma_f32_16x16x32_bf16 v[72:75], a[128:131], a[16:19], v[72:75]\n" \
"v_exp_f32_e32 v111, v111\n" \
"v_mfma_f32_16x16x32_bf16 v[68:71], a[144:147], a[0:3], v[32:35]\n" \
"v_cvt_pk_bf16_f32 v107, v110, v111\n" \
"v_add_u32_e32 v49, 0x10000, v48\n" \
"v_mfma_f32_16x16x32_bf16 v[68:71], a[148:151], a[4:7], v[68:71]\n" \
"buffer_load_dwordx4 v52, %[srd], s64 offen offset:2048 sc1 lds\n" \
"v_mfma_f32_16x16x32_bf16 v[68:71], a[152:155], a[8:11], v[68:71]\n" \
"v_exp_f32_e32 v112, v112\n" \
"v_mfma_f32_16x16x32_bf16 v[68:71], a[156:159], a[12:15], v[68:71]\n" \
"v_exp_f32_e32 v113, v113\n" \
"v_mfma_f32_16x16x32_bf16 v[76:79], a[156:159], a[28:31], v[36:39]\n" \
"v_exp_f32_e32 v114, v114\n" \
"v_mfma_f32_16x16x32_bf16 v[76:79], a[152:155], a[24:27], v[76:79]\n" \
"v_exp_f32_e32 v115, v115\n" \
"v_mfma_f32_16x16x32_bf16 v[76:79], a[148:151], a[20:23], v[76:79]\n" \
"buffer_load_dwordx4 v52, %[srd], s64 offen offset:3072 sc1 lds\n" \
"v_mfma_f32_16x16x32_bf16 v[76:79], a[144:147], a[16:19], v[76:79]\n" \
"s_mov_b32 m0, s65\n" \
"v_cvt_pk_bf16_f32 v112, v112, v113\n" \
"v_mfma_f32_16x16x32_bf16 v[80:83], a[128:131], a[32:35], v[40:43]\n" \
"v_exp_f32_e32 v116, v116\n" \
"v_mfma_f32_16x16x32_bf16 v[80:83], a[132:135], a[36:39], v[80:83]\n" \
"v_exp_f32_e32 v117, v117\n" \
"v_mfma_f32_16x16x32_bf16 v[80:83], a[136:139], a[40:43], v[80:83]\n" \
"v_exp_f32_e32 v118, v118\n" \
"v_mfma_f32_16x16x32_bf16 v[80:83], a[140:143], a[44:47], v[80:83]\n" \
"buffer_load_dwordx4 v53, %[srd], s64 offen offset:0 sc1 lds\n" \
"v_mfma_f32_16x16x32_bf16 v[88:91], a[140:143], a[60:63], v[44:47]\n" \
"v_exp_f32_e32 v119, v119\n" \
"v_mfma_f32_16x16x32_bf16 v[88:91], a[136:139], a[56:59], v[88:91]\n" \
"v_cvt_pk_bf16_f32 v113, v114, v115\n" \
"v_cvt_pk_bf16_f32 v114, v116, v117\n" \
"v_mfma_f32_16x16x32_bf16 v[88:91], a[132:135], a[52:55], v[88:91]\n" \
"v_cvt_pk_bf16_f32 v115, v118, v119\n" \
"ds_read_b128 a[132:135], v48 offset:9216\n" \
"v_mfma_f32_16x16x32_bf16 v[88:91], a[128:131], a[48:51], v[88:91]\n" \
"buffer_load_dwordx4 v53, %[srd], s64 offen offset:1024 sc1 lds\n" \
"v_mfma_f32_16x16x32_bf16 v[84:87], a[144:147], a[32:35], v[40:43]\n" \
"ds_read_b128 a[128:131], v48 offset:8192\n" \
"ds_read_b128 a[136:139], v48 offset:10240\n" \
"v_mfma_f32_16x16x32_bf16 v[84:87], a[148:151], a[36:39], v[84:87]\n" \
"ds_read_b128 a[140:143], v48 offset:11264\n" \
"s_xor_b32 s65, s65, 0x8000\n" \
"v_mfma_f32_16x16x32_bf16 v[84:87], a[152:155], a[40:43], v[84:87]\n" \
"v_exp_f32_e32 v120, v120\n" \
"v_mfma_f32_16x16x32_bf16 v[84:87], a[156:159], a[44:47], v[84:87]\n" \
"v_exp_f32_e32 v121, v121\n" \
"v_mfma_f32_16x16x32_bf16 v[92:95], a[156:159], a[60:63], v[44:47]\n" \
"buffer_load_dwordx4 v53, %[srd], s64 offen offset:2048 sc1 lds\n" \
"v_mfma_f32_16x16x32_bf16 v[92:95], a[152:155], a[56:59], v[92:95]\n" \
"ds_read_b128 a[152:155], v48 offset:14336\n" \
"ds_read_b128 a[156:159], v48 offset:15360\n" \
"v_mfma_f32_16x16x32_bf16 v[92:95], a[148:151], a[52:55], v[92:95]\n" \
"ds_read_b128 a[148:151], v48 offset:13312\n" \
"v_cvt_pk_bf16_f32 v120, v120, v121\n" \
"v_mfma_f32_16x16x32_bf16 v[92:95], a[144:147], a[48:51], v[92:95]\n" \
"L_pf12:\n" \
"s_cbranch_execz L_pf16\n" \
"s_nop 0\n" \
"ds_read_b128 a[144:147], v48 offset:12288\n" \
"s_waitcnt lgkmcnt(8)\n" \
"v_mfma_f32_16x16x32_bf16 v[128:131], a[160:163], v[96:99], v[128:131]\n" \
"v_exp_f32_e32 v122, v122\n" \
"v_mfma_f32_16x16x32_bf16 v[132:135], a[164:167], v[96:99], v[132:135]\n" \
"buffer_load_dwordx4 v53, %[srd], s64 offen offset:3072 sc1 lds\n" \
"v_mfma_f32_16x16x32_bf16 v[136:139], a[168:171], v[96:99], v[136:139]\n" \
"s_add_u32 m0, s93, 0x1000\n" \
"v_add_u32_e32 v48, %[k2], v20\n" \
"v_mfma_f32_16x16x32_bf16 v[140:143], a[172:175], v[96:99], v[140:143]\n" \
"v_exp_f32_e32 v123, v123\n" \
"v_mfma_f32_16x16x32_bf16 v[144:147], a[176:179], v[96:99], v[144:147]\n" \
"v_exp_f32_e32 v124, v124\n" \
"v_mfma_f32_16x16x32_bf16 v[148:151], a[180:183], v[96:99], v[148:151]\n" \
"v_exp_f32_e32 v125, v125\n" \
"v_mfma_f32_16x16x32_bf16 v[152:155], a[184:187], v[96:99], v[152:155]\n" \
"buffer_load_dwordx4 v4, s[80:83], s92 offen offset:0 lds\n" \
"v_mfma_f32_16x16x32_bf16 v[156:159], a[188:191], v[96:99], v[156:159]\n" \
"v_exp_f32_e32 v126, v126\n" \
"v_mfma_f32_16x16x32_bf16 a[192:195], a[212:215], v[96:99], a[192:195]\n" \
"v_exp_f32_e32 v127, v127\n" \
"v_mfma_f32_16x16x32_bf16 v[160:163], a[160:163], v[104:107], v[160:163]\n" \
"v_cvt_pk_bf16_f32 v121, v122, v123\n" \
"v_cvt_pk_bf16_f32 v122, v124, v125\n" \
"v_mfma_f32_16x16x32_bf16 v[164:167], a[164:167], v[104:107], v[164:167]\n" \
"buffer_load_dwordx4 v5, s[80:83], s92 offen offset:1024 lds\n" \
"v_mfma_f32_16x16x32_bf16 v[168:171], a[168:171], v[104:107], v[168:171]\n" \
"v_cvt_pk_bf16_f32 v123, v126, v127\n" \
"s_add_u32 s64, s64, 0x10000\n" \
"v_mfma_f32_16x16x32_bf16 v[172:175], a[172:175], v[104:107], v[172:175]\n" \
"v_exp_f32_e32 v64, v64\n" \
"v_mfma_f32_16x16x32_bf16 v[176:179], a[176:179], v[104:107], v[176:179]\n" \
"v_exp_f32_e32 v65, v65\n" \
"v_mfma_f32_16x16x32_bf16 v[180:183], a[180:183], v[104:107], v[180:183]\n" \
"v_exp_f32_e32 v72, v72\n" \
"v_mfma_f32_16x16x32_bf16 v[184:187], a[184:187], v[104:107], v[184:187]\n" \
"buffer_load_dwordx4 v6, s[80:83], s92 offen offset:2048 lds\n" \
"v_mfma_f32_16x16x32_bf16 v[188:191], a[188:191], v[104:107], v[188:191]\n" \
"v_exp_f32_e32 v73, v73\n" \
"v_mfma_f32_16x16x32_bf16 a[196:199], a[212:215], v[104:107], a[196:199]\n" \
"v_exp_f32_e32 v66, v66\n" \
"v_mfma_f32_16x16x32_bf16 v[192:195], a[160:163], v[112:115], v[192:195]\n" \
"v_exp_f32_e32 v67, v67\n" \
"v_mfma_f32_16x16x32_bf16 v[196:199], a[164:167], v[112:115], v[196:199]\n" \
"v_exp_f32_e32 v74, v74\n" \
"v_mfma_f32_16x16x32_bf16 v[200:203], a[168:171], v[112:115], v[200:203]\n" \
"buffer_load_dwordx4 v7, s[80:83], s92 offen offset:3072 lds\n" \
"v_mfma_f32_16x16x32_bf16 v[204:207], a[172:175], v[112:115], v[204:207]\n" \
"s_add_u32 m0, s93, 0x2000\n" \
"v_cvt_pk_bf16_f32 v64, v64, v65\n" \
"v_mfma_f32_16x16x32_bf16 v[208:211], a[176:179], v[112:115], v[208:211]\n" \
"v_exp_f32_e32 v75, v75\n" \
"v_mfma_f32_16x16x32_bf16 v[212:215], a[180:183], v[112:115], v[212:215]\n" \
"v_exp_f32_e32 v68, v68\n" \
"v_mfma_f32_16x16x32_bf16 v[216:219], a[184:187], v[112:115], v[216:219]\n" \
"v_exp_f32_e32 v69, v69\n" \
"v_mfma_f32_16x16x32_bf16 v[220:223], a[188:191], v[112:115], v[220:223]\n" \
"buffer_load_dwordx4 v8, s[80:83], s92 offen offset:0 lds\n" \
"v_mfma_f32_16x16x32_bf16 a[200:203], a[212:215], v[112:115], a[200:203]\n" \
"v_exp_f32_e32 v70, v70\n" \
"v_mfma_f32_16x16x32_bf16 v[224:227], a[160:163], v[120:123], v[224:227]\n" \
"v_exp_f32_e32 v71, v71\n" \
"v_mfma_f32_16x16x32_bf16 v[228:231], a[164:167], v[120:123], v[228:231]\n" \
"v_cvt_pk_bf16_f32 v72, v72, v73\n" \
"v_cvt_pk_bf16_f32 v65, v66, v67\n" \
"v_mfma_f32_16x16x32_bf16 v[232:235], a[168:171], v[120:123], v[232:235]\n" \
"v_exp_f32_e32 v76, v76\n" \
"v_mfma_f32_16x16x32_bf16 v[236:239], a[172:175], v[120:123], v[236:239]\n" \
"v_exp_f32_e32 v77, v77\n" \
"v_mfma_f32_16x16x32_bf16 v[240:243], a[176:179], v[120:123], v[240:243]\n" \
"v_exp_f32_e32 v78, v78\n" \
"v_mfma_f32_16x16x32_bf16 v[244:247], a[180:183], v[120:123], v[244:247]\n" \
"v_exp_f32_e32 v79, v79\n" \
"v_mfma_f32_16x16x32_bf16 v[248:251], a[184:187], v[120:123], v[248:251]\n" \
"v_cvt_pk_bf16_f32 v73, v74, v75\n" \
"v_cvt_pk_bf16_f32 v66, v68, v69\n" \
"v_mfma_f32_16x16x32_bf16 v[252:255], a[188:191], v[120:123], v[252:255]\n" \
"v_cvt_pk_bf16_f32 v67, v70, v71\n" \
"v_cvt_pk_bf16_f32 v74, v76, v77\n" \
"v_mfma_f32_16x16x32_bf16 a[204:207], a[212:215], v[120:123], a[204:207]\n" \
"v_cvt_pk_bf16_f32 v75, v78, v79\n" \
"s_waitcnt vmcnt(13) lgkmcnt(0)\n" \
"s_barrier\n" \
"L_2_%=:\n" \
"v_mfma_f32_16x16x32_bf16 v[96:99], a[128:131], a[64:67], v[32:35]\n" \
"s_add_u32 s63, s63, s63\n" \
"s_cbranch_scc1 L_h2_%=\n" \
"L_hr2_%=:\n" \
"v_mfma_f32_16x16x32_bf16 v[96:99], a[132:135], a[68:71], v[96:99]\n" \
"s_xor_b32 m0, s65, 0x12000\n" \
"ds_read_b128 a[160:163], v49 offset:0\n" \
"v_mfma_f32_16x16x32_bf16 v[96:99], a[136:139], a[72:75], v[96:99]\n" \
"buffer_load_dwordx4 v50, %[srd], s64 offen offset:0 sc1 lds\n" \
"v_mfma_f32_16x16x32_bf16 v[96:99], a[140:143], a[76:79], v[96:99]\n" \
"ds_read_b128 a[164:167], v49 offset:1024\n" \
"ds_read_b128 a[168:171], v49 offset:2048\n" \
"v_mfma_f32_16x16x32_bf16 v[104:107], a[140:143], a[92:95], v[36:39]\n" \
"ds_read_b128 a[172:175], v49 offset:3072\n" \
"ds_read_b128 a[176:179], v49 offset:4096\n" \
"v_mfma_f32_16x16x32_bf16 v[104:107], a[136:139], a[88:91], v[104:107]\n" \
"ds_read_b128 a[180:183], v49 offset:5120\n" \
"ds_read_b128 a[184:187], v49 offset:6144\n" \
"v_mfma_f32_16x16x32_bf16 v[104:107], a[132:135], a[84:87], v[104:107]\n" \
"v_exp_f32_e32 v80, v80\n" \
"v_mfma_f32_16x16x32_bf16 v[104:107], a[128:131], a[80:83], v[104:107]\n" \
"buffer_load_dwordx4 v50, %[srd], s64 offen offset:1024 sc1 lds\n" \
"v_mfma_f32_16x16x32_bf16 v[100:103], a[144:147], a[64:67], v[32:35]\n" \
"v_exp_f32_e32 v81, v81\n" \
"v_mfma_f32_16x16x32_bf16 v[100:103], a[148:151], a[68:71], v[100:103]\n" \
"ds_read_b128 a[188:191], v49 offset:7168\n" \
"v_cvt_pk_bf16_f32 v80, v80, v81\n" \
"v_mfma_f32_16x16x32_bf16 v[100:103], a[152:155], a[72:75], v[100:103]\n" \
"v_exp_f32_e32 v82, v82\n" \
"v_mfma_f32_16x16x32_bf16 v[100:103], a[156:159], a[76:79], v[100:103]\n" \
"v_exp_f32_e32 v83, v83\n" \
"v_mfma_f32_16x16x32_bf16 v[108:111], a[156:159], a[92:95], v[36:39]\n" \
"v_exp_f32_e32 v84, v84\n" \
"v_mfma_f32_16x16x32_bf16 v[108:111], a[152:155], a[88:91], v[108:111]\n" \
"v_exp_f32_e32 v85, v85\n" \
"v_mfma_f32_16x16x32_bf16 v[108:111], a[148:151], a[84:87], v[108:111]\n" \
"v_exp_f32_e32 v86, v86\n" \
"v_mfma_f32_16x16x32_bf16 v[108:111], a[144:147], a[80:83], v[108:111]\n" \
"buffer_load_dwordx4 v50, %[srd], s64 offen offset:2048 sc1 lds\n" \
"v_mfma_f32_16x16x32_bf16 v[112:115], a[128:131], a[96:99], v[40:43]\n" \
"v_exp_f32_e32 v87, v87\n" \
"v_mfma_f32_16x16x32_bf16 v[112:115], a[132:135], a[100:103], v[112:115]\n" \
"v_exp_f32_e32 v88, v88\n" \
"v_mfma_f32_16x16x32_bf16 v[112:115], a[136:139], a[104:107], v[112:115]\n" \
"v_exp_f32_e32 v89, v89\n" \
"v_mfma_f32_16x16x32_bf16 v[112:115], a[140:143], a[108:111], v[112:115]\n" \
"v_cvt_pk_bf16_f32 v81, v82, v83\n" \
"v_cvt_pk_bf16_f32 v82, v84, v85\n" \
"v_mfma_f32_16x16x32_bf16 v[120:123], a[140:143], a[124:127], v[44:47]\n" \
"v_cvt_pk_bf16_f32 v83, v86, v87\n" \
"ds_read_b128 a[140:143], v48 offset:3072\n" \
"v_mfma_f32_16x16x32_bf16 v[120:123], a[136:139], a[120:123], v[120:123]\n" \
"ds_read_b128 a[136:139], v48 offset:2048\n" \
"v_cvt_pk_bf16_f32 v88, v88, v89\n" \
"v_mfma_f32_16x16x32_bf16 v[120:123], a[132:135], a[116:119], v[120:123]\n" \
"v_exp_f32_e32 v90, v90\n" \
"v_mfma_f32_16x16x32_bf16 v[120:123], a[128:131], a[112:115], v[120:123]\n" \
"buffer_load_dwordx4 v50, %[srd], s64 offen offset:3072 sc1 lds\n" \
"v_mfma_f32_16x16x32_bf16 v[116:119], a[144:147], a[96:99], v[40:43]\n" \
"s_xor_b32 m0, s65, 0xa000\n" \
"ds_read_b128 a[128:131], v48 offset:0\n" \
"v_mfma_f32_16x16x32_bf16 v[116:119], a[148:151], a[100:103], v[116:119]\n" \
"v_exp_f32_e32 v91, v91\n" \
"v_mfma_f32_16x16x32_bf16 v[116:119], a[152:155], a[104:107], v[116:119]\n" \
"ds_read_b128 a[132:135], v48 offset:1024\n" \
"v_cvt_pk_bf16_f32 v89, v90, v91\n" \
"v_mfma_f32_16x16x32_bf16 v[116:119], a[156:159], a[108:111], v[116:119]\n" \
"v_exp_f32_e32 v92, v92\n" \
"v_mfma_f32_16x16x32_bf16 v[124:127], a[156:159], a[124:127], v[44:47]\n" \
"v_exp_f32_e32 v93, v93\n" \
"v_mfma_f32_16x16x32_bf16 v[124:127], a[152:155], a[120:123], v[124:127]\n" \
"ds_read_b128 a[152:155], v48 offset:6144\n" \
"ds_read_b128 a[156:159], v48 offset:7168\n" \
"v_mfma_f32_16x16x32_bf16 v[124:127], a[148:151], a[116:119], v[124:127]\n" \
"ds_read_b128 a[148:151], v48 offset:5120\n" \
"v_cvt_pk_bf16_f32 v90, v92, v93\n" \
"v_mfma_f32_16x16x32_bf16 v[124:127], a[144:147], a[112:115], v[124:127]\n" \
"ds_read_b128 a[144:147], v48 offset:4096\n" \
"s_waitcnt lgkmcnt(8)\n" \
"v_mfma_f32_16x16x32_bf16 v[128:131], a[160:163], v[64:67], v[128:131]\n" \
"buffer_load_dwordx4 v51, %[srd], s64 offen offset:0 sc1 lds\n" \
"v_mfma_f32_16x16x32_bf16 v[132:135], a[164:167], v[64:67], v[132:135]\n" \
"v_exp_f32_e32 v94, v94\n" \
"v_mfma_f32_16x16x32_bf16 v[136:139], a[168:171], v[64:67], v[136:139]\n" \
"v_exp_f32_e32 v95, v95\n" \
"v_mfma_f32_16x16x32_bf16 v[140:143], a[172:175], v[64:67], v[140:143]\n" \
"v_exp_f32_e32 v96, v96\n" \
"v_mfma_f32_16x16x32_bf16 v[144:147], a[176:179], v[64:67], v[144:147]\n" \
"v_exp_f32_e32 v97, v97\n" \
"v_mfma_f32_16x16x32_bf16 v[148:151], a[180:183], v[64:67], v[148:151]\n" \
"v_cvt_pk_bf16_f32 v91, v94, v95\n" \
"v_cvt_pk_bf16_f32 v96, v96, v97\n" \
"v_mfma_f32_16x16x32_bf16 v[152:155], a[184:187], v[64:67], v[152:155]\n" \
"buffer_load_dwordx4 v51, %[srd], s64 offen offset:1024 sc1 lds\n" \
"v_mfma_f32_16x16x32_bf16 v[156:159], a[188:191], v[64:67], v[156:159]\n" \
"v_exp_f32_e32 v98, v98\n" \
"v_mfma_f32_16x16x32_bf16 a[192:195], a[212:215], v[64:67], a[192:195]\n" \
"v_exp_f32_e32 v99, v99\n" \
"v_mfma_f32_16x16x32_bf16 v[160:163], a[160:163], v[72:75], v[160:163]\n" \
"v_exp_f32_e32 v100, v100\n" \
"v_mfma_f32_16x16x32_bf16 v[164:167], a[164:167], v[72:75], v[164:167]\n" \
"v_exp_f32_e32 v101, v101\n" \
"v_mfma_f32_16x16x32_bf16 v[168:171], a[168:171], v[72:75], v[168:171]\n" \
"v_exp_f32_e32 v102, v102\n" \
"v_mfma_f32_16x16x32_bf16 v[172:175], a[172:175], v[72:75], v[172:175]\n" \
"v_exp_f32_e32 v103, v103\n" \
"v_mfma_f32_16x16x32_bf16 v[176:179], a[176:179], v[72:75], v[176:179]\n" \
"v_exp_f32_e32 v104, v104\n" \
"v_mfma_f32_16x16x32_bf16 v[180:183], a[180:183], v[72:75], v[180:183]\n" \
"buffer_load_dwordx4 v51, %[srd], s64 offen offset:2048 sc1 lds\n" \
"v_mfma_f32_16x16x32_bf16 v[184:187], a[184:187], v[72:75], v[184:187]\n" \
"v_exp_f32_e32 v105, v105\n" \
"v_mfma_f32_16x16x32_bf16 v[188:191], a[188:191], v[72:75], v[188:191]\n" \
"v_cvt_pk_bf16_f32 v97, v98, v99\n" \
"v_cvt_pk_bf16_f32 v98, v100, v101\n" \
"v_mfma_f32_16x16x32_bf16 a[196:199], a[212:215], v[72:75], a[196:199]\n" \
"v_cvt_pk_bf16_f32 v99, v102, v103\n" \
"v_cvt_pk_bf16_f32 v104, v104, v105\n" \
"v_mfma_f32_16x16x32_bf16 v[192:195], a[160:163], v[80:83], v[192:195]\n" \
"v_exp_f32_e32 v106, v106\n" \
"v_mfma_f32_16x16x32_bf16 v[196:199], a[164:167], v[80:83], v[196:199]\n" \
"v_exp_f32_e32 v107, v107\n" \
"v_mfma_f32_16x16x32_bf16 v[200:203], a[168:171], v[80:83], v[200:203]\n" \
"v_exp_f32_e32 v108, v108\n" \
"v_mfma_f32_16x16x32_bf16 v[204:207], a[172:175], v[80:83], v[204:207]\n" \
"v_exp_f32_e32 v109, v109\n" \
"v_mfma_f32_16x16x32_bf16 v[208:211], a[176:179], v[80:83], v[208:211]\n" \
"buffer_load_dwordx4 v51, %[srd], s64 offen offset:3072 sc1 lds\n" \
"v_mfma_f32_16x16x32_bf16 v[212:215], a[180:183], v[80:83], v[212:215]\n" \
"v_exp_f32_e32 v110, v110\n" \
"v_mfma_f32_16x16x32_bf16 v[216:219], a[184:187], v[80:83], v[216:219]\n" \
"v_exp_f32_e32 v111, v111\n" \
"v_mfma_f32_16x16x32_bf16 v[220:223], a[188:191], v[80:83], v[220:223]\n" \
"v_exp_f32_e32 v112, v112\n" \
"v_mfma_f32_16x16x32_bf16 a[200:203], a[212:215], v[80:83], a[200:203]\n" \
"v_exp_f32_e32 v113, v113\n" \
"v_mfma_f32_16x16x32_bf16 v[224:227], a[160:163], v[88:91], v[224:227]\n" \
"v_cvt_pk_bf16_f32 v105, v106, v107\n" \
"v_cvt_pk_bf16_f32 v106, v108, v109\n" \
"v_mfma_f32_16x16x32_bf16 v[228:231], a[164:167], v[88:91], v[228:231]\n" \
"v_cvt_pk_bf16_f32 v107, v110, v111\n" \
"v_cvt_pk_bf16_f32 v112, v112, v113\n" \
"v_mfma_f32_16x16x32_bf16 v[232:235], a[168:171], v[88:91], v[232:235]\n" \
"v_exp_f32_e32 v114, v114\n" \
"v_mfma_f32_16x16x32_bf16 v[236:239], a[172:175], v[88:91], v[236:239]\n" \
"v_exp_f32_e32 v115, v115\n" \
"v_mfma_f32_16x16x32_bf16 v[240:243], a[176:179], v[88:91], v[240:243]\n" \
"v_exp_f32_e32 v116, v116\n" \
"v_mfma_f32_16x16x32_bf16 v[244:247], a[180:183], v[88:91], v[244:247]\n" \
"v_exp_f32_e32 v117, v117\n" \
"v_mfma_f32_16x16x32_bf16 v[248:251], a[184:187], v[88:91], v[248:251]\n" \
"v_exp_f32_e32 v118, v118\n" \
"v_mfma_f32_16x16x32_bf16 v[252:255], a[188:191], v[88:91], v[252:255]\n" \
"v_exp_f32_e32 v119, v119\n" \
"v_mfma_f32_16x16x32_bf16 a[204:207], a[212:215], v[88:91], a[204:207]\n" \
"v_cvt_pk_bf16_f32 v113, v114, v115\n" \
"v_cvt_pk_bf16_f32 v114, v116, v117\n" \
"s_waitcnt vmcnt(8) lgkmcnt(0)\n" \
"s_barrier\n" \
"v_mfma_f32_16x16x32_bf16 v[64:67], a[128:131], a[64:67], v[32:35]\n" \
"s_xor_b32 m0, s65, 0x18000\n" \
"ds_read_b128 a[160:163], v49 offset:8192\n" \
"v_mfma_f32_16x16x32_bf16 v[64:67], a[132:135], a[68:71], v[64:67]\n" \
"buffer_load_dwordx4 v52, %[srd], s64 offen offset:0 sc1 lds\n" \
"v_mfma_f32_16x16x32_bf16 v[64:67], a[136:139], a[72:75], v[64:67]\n" \
"ds_read_b128 a[164:167], v49 offset:9216\n" \
"ds_read_b128 a[168:171], v49 offset:10240\n" \
"v_mfma_f32_16x16x32_bf16 v[64:67], a[140:143], a[76:79], v[64:67]\n" \
"ds_read_b128 a[172:175], v49 offset:11264\n" \
"ds_read_b128 a[176:179], v49 offset:12288\n" \
"v_mfma_f32_16x16x32_bf16 v[72:75], a[140:143], a[92:95], v[36:39]\n" \
"ds_read_b128 a[180:183], v49 offset:13312\n" \
"ds_read_b128 a[184:187], v49 offset:14336\n" \
"v_mfma_f32_16x16x32_bf16 v[72:75], a[136:139], a[88:91], v[72:75]\n" \
"ds_read_b128 a[188:191], v49 offset:15360\n" \
"v_cvt_pk_bf16_f32 v115, v118, v119\n" \
"v_mfma_f32_16x16x32_bf16 v[72:75], a[132:135], a[84:87], v[72:75]\n" \
"v_exp_f32_e32 v120, v120\n" \
"v_mfma_f32_16x16x32_bf16 v[72:75], a[128:131], a[80:83], v[72:75]\n" \
"buffer_load_dwordx4 v52, %[srd], s64 offen offset:1024 sc1 lds\n" \
"v_mfma_f32_16x16x32_bf16 v[68:71], a[144:147], a[64:67], v[32:35]\n" \
"v_exp_f32_e32 v121, v121\n" \
"v_mfma_f32_16x16x32_bf16 v[68:71], a[148:151], a[68:71], v[68:71]\n" \
"v_exp_f32_e32 v122, v122\n" \
"v_mfma_f32_16x16x32_bf16 v[68:71], a[152:155], a[72:75], v[68:71]\n" \
"v_exp_f32_e32 v123, v123\n" \
"v_mfma_f32_16x16x32_bf16 v[68:71], a[156:159], a[76:79], v[68:71]\n" \
"v_cvt_pk_bf16_f32 v120, v120, v121\n" \
"v_cvt_pk_bf16_f32 v121, v122, v123\n" \
"v_mfma_f32_16x16x32_bf16 v[76:79], a[156:159], a[92:95], v[36:39]\n" \
"v_exp_f32_e32 v124, v124\n" \
"v_mfma_f32_16x16x32_bf16 v[76:79], a[152:155], a[88:91], v[76:79]\n" \
"v_exp_f32_e32 v125, v125\n" \
"v_mfma_f32_16x16x32_bf16 v[76:79], a[148:151], a[84:87], v[76:79]\n" \
"v_exp_f32_e32 v126, v126\n" \
"v_mfma_f32_16x16x32_bf16 v[76:79], a[144:147], a[80:83], v[76:79]\n" \
"buffer_load_dwordx4 v52, %[srd], s64 offen offset:2048 sc1 lds\n" \
"v_mfma_f32_16x16x32_bf16 v[80:83], a[128:131], a[96:99], v[40:43]\n" \
"v_exp_f32_e32 v127, v127\n" \
"v_mfma_f32_16x16x32_bf16 v[80:83], a[132:135], a[100:103], v[80:83]\n" \
"v_cvt_pk_bf16_f32 v122, v124, v125\n" \
"v_cvt_pk_bf16_f32 v123, v126, v127\n" \
"v_mfma_f32_16x16x32_bf16 v[80:83], a[136:139], a[104:107], v[80:83]\n" \
"v_exp_f32_e32 v64, v64\n" \
"v_mfma_f32_16x16x32_bf16 v[80:83], a[140:143], a[108:111], v[80:83]\n" \
"v_exp_f32_e32 v65, v65\n" \
"v_mfma_f32_16x16x32_bf16 v[88:91], a[140:143], a[124:127], v[44:47]\n" \
"ds_read_b128 a[140:143], v48 offset:11264\n" \
"v_cvt_pk_bf16_f32 v64, v64, v65\n" \
"v_mfma_f32_16x16x32_bf16 v[88:91], a[136:139], a[120:123], v[88:91]\n" \
"ds_read_b128 a[136:139], v48 offset:10240\n" \
"v_add_u32_e32 v49, 0x10000, v48\n" \
"v_mfma_f32_16x16x32_bf16 v[88:91], a[132:135], a[116:119], v[88:91]\n" \
"v_exp_f32_e32 v72, v72\n" \
"v_mfma_f32_16x16x32_bf16 v[88:91], a[128:131], a[112:115], v[88:91]\n" \
"buffer_load_dwordx4 v52, %[srd], s64 offen offset:3072 sc1 lds\n" \
"v_mfma_f32_16x16x32_bf16 v[84:87], a[144:147], a[96:99], v[40:43]\n" \
"s_mov_b32 m0, s65\n" \
"ds_read_b128 a[128:131], v48 offset:8192\n" \
"v_mfma_f32_16x16x32_bf16 v[84:87], a[148:151], a[100:103], v[84:87]\n" \
"ds_read_b128 a[132:135], v48 offset:9216\n" \
"s_xor_b32 s65, s65, 0x8000\n" \
"v_mfma_f32_16x16x32_bf16 v[84:87], a[152:155], a[104:107], v[84:87]\n" \
"v_exp_f32_e32 v73, v73\n" \
"v_mfma_f32_16x16x32_bf16 v[84:87], a[156:159], a[108:111], v[84:87]\n" \
"v_exp_f32_e32 v66, v66\n" \
"v_mfma_f32_16x16x32_bf16 v[92:95], a[156:159], a[124:127], v[44:47]\n" \
"ds_read_b128 a[156:159], v48 offset:15360\n" \
"v_cvt_pk_bf16_f32 v72, v72, v73\n" \
"v_mfma_f32_16x16x32_bf16 v[92:95], a[152:155], a[120:123], v[92:95]\n" \
"v_exp_f32_e32 v67, v67\n" \
"v_mfma_f32_16x16x32_bf16 v[92:95], a[148:151], a[116:119], v[92:95]\n" \
"ds_read_b128 a[148:151], v48 offset:13312\n" \
"ds_read_b128 a[152:155], v48 offset:14336\n" \
"v_mfma_f32_16x16x32_bf16 v[92:95], a[144:147], a[112:115], v[92:95]\n" \
"ds_read_b128 a[144:147], v48 offset:12288\n" \
"s_waitcnt lgkmcnt(8)\n" \
"v_mfma_f32_16x16x32_bf16 v[128:131], a[160:163], v[96:99], v[128:131]\n" \
"buffer_load_dwordx4 v53, %[srd], s64 offen offset:0 sc1 lds\n" \
"v_mfma_f32_16x16x32_bf16 v[132:135], a[164:167], v[96:99], v[132:135]\n" \
"v_exp_f32_e32 v74, v74\n" \
"v_mfma_f32_16x16x32_bf16 v[136:139], a[168:171], v[96:99], v[136:139]\n" \
"v_exp_f32_e32 v75, v75\n" \
"v_mfma_f32_16x16x32_bf16 v[140:143], a[172:175], v[96:99], v[140:143]\n" \
"v_exp_f32_e32 v68, v68\n" \
"v_mfma_f32_16x16x32_bf16 v[144:147], a[176:179], v[96:99], v[144:147]\n" \
"v_exp_f32_e32 v69, v69\n" \
"v_mfma_f32_16x16x32_bf16 v[148:151], a[180:183], v[96:99], v[148:151]\n" \
"v_exp_f32_e32 v70, v70\n" \
"v_mfma_f32_16x16x32_bf16 v[152:155], a[184:187], v[96:99], v[152:155]\n" \
"buffer_load_dwordx4 v53, %[srd], s64 offen offset:1024 sc1 lds\n" \
"v_mfma_f32_16x16x32_bf16 v[156:159], a[188:191], v[96:99], v[156:159]\n" \
"v_exp_f32_e32 v71, v71\n" \
"v_mfma_f32_16x16x32_bf16 a[192:195], a[212:215], v[96:99], a[192:195]\n" \
"v_exp_f32_e32 v76, v76\n" \
"v_mfma_f32_16x16x32_bf16 v[160:163], a[160:163], v[104:107], v[160:163]\n" \
"v_exp_f32_e32 v77, v77\n" \
"v_mfma_f32_16x16x32_bf16 v[164:167], a[164:167], v[104:107], v[164:167]\n" \
"v_exp_f32_e32 v78, v78\n" \
"v_mfma_f32_16x16x32_bf16 v[168:171], a[168:171], v[104:107], v[168:171]\n" \
"v_exp_f32_e32 v79, v79\n" \
"v_mfma_f32_16x16x32_bf16 v[172:175], a[172:175], v[104:107], v[172:175]\n" \
"v_cvt_pk_bf16_f32 v65, v66, v67\n" \
"v_cvt_pk_bf16_f32 v73, v74, v75\n" \
"v_mfma_f32_16x16x32_bf16 v[176:179], a[176:179], v[104:107], v[176:179]\n" \
"v_cvt_pk_bf16_f32 v66, v68, v69\n" \
"v_cvt_pk_bf16_f32 v67, v70, v71\n" \
"v_mfma_f32_16x16x32_bf16 v[180:183], a[180:183], v[104:107], v[180:183]\n" \
"buffer_load_dwordx4 v53, %[srd], s64 offen offset:2048 sc1 lds\n" \
"v_mfma_f32_16x16x32_bf16 v[184:187], a[184:187], v[104:107], v[184:187]\n" \
"v_cvt_pk_bf16_f32 v74, v76, v77\n" \
"v_cvt_pk_bf16_f32 v75, v78, v79\n" \
"v_mfma_f32_16x16x32_bf16 v[188:191], a[188:191], v[104:107], v[188:191]\n" \
"v_xor_b32_e32 v48, 0x8000, v48\n" \
"v_mfma_f32_16x16x32_bf16 a[196:199], a[212:215], v[104:107], a[196:199]\n" \
"v_mfma_f32_16x16x32_bf16 v[192:195], a[160:163], v[112:115], v[192:195]\n" \
"v_mfma_f32_16x16x32_bf16 v[196:199], a[164:167], v[112:115], v[196:199]\n" \
"v_mfma_f32_16x16x32_bf16 v[200:203], a[168:171], v[112:115], v[200:203]\n" \
"v_mfma_f32_16x16x32_bf16 v[204:207], a[172:175], v[112:115], v[204:207]\n" \
"v_mfma_f32_16x16x32_bf16 v[208:211], a[176:179], v[112:115], v[208:211]\n" \
"buffer_load_dwordx4 v53, %[srd], s64 offen offset:3072 sc1 lds\n" \
"v_mfma_f32_16x16x32_bf16 v[212:215], a[180:183], v[112:115], v[212:215]\n" \
"s_add_u32 s64, s64, 0x10000\n" \
"s_sub_u32 s66, s66, 1\n" \
"v_mfma_f32_16x16x32_bf16 v[216:219], a[184:187], v[112:115], v[216:219]\n" \
"v_mfma_f32_16x16x32_bf16 v[220:223], a[188:191], v[112:115], v[220:223]\n" \
"v_mfma_f32_16x16x32_bf16 a[200:203], a[212:215], v[112:115], a[200:203]\n" \
"v_mfma_f32_16x16x32_bf16 v[224:227], a[160:163], v[120:123], v[224:227]\n" \
"v_mfma_f32_16x16x32_bf16 v[228:231], a[164:167], v[120:123], v[228:231]\n" \
"v_mfma_f32_16x16x32_bf16 v[232:235], a[168:171], v[120:123], v[232:235]\n" \
"v_mfma_f32_16x16x32_bf16 v[236:239], a[172:175], v[120:123], v[236:239]\n" \
"v_mfma_f32_16x16x32_bf16 v[240:243], a[176:179], v[120:123], v[240:243]\n" \
"v_mfma_f32_16x16x32_bf16 v[244:247], a[180:183], v[120:123], v[244:247]\n" \
"v_mfma_f32_16x16x32_bf16 v[248:251], a[184:187], v[120:123], v[248:251]\n" \
"v_mfma_f32_16x16x32_bf16 v[252:255], a[188:191], v[120:123], v[252:255]\n" \
"v_mfma_f32_16x16x32_bf16 a[204:207], a[212:215], v[120:123], a[204:207]\n" \
"s_waitcnt vmcnt(8) lgkmcnt(0)\n" \
"s_barrier\n" \
"s_cbranch_scc0 L_2_%=\n" \
"s_cmp_lg_u32 %[k2], 0\n" \
"s_cbranch_scc1 L_f1_%=\n" \
"v_bfe_u32 v58, v20, 2, 7\n" \
"v_readlane_b32 s96, v11, 1\n" \
"ds_read_b128 a[160:163], v49 offset:0\n" \
"ds_read_b128 a[164:167], v49 offset:1024\n" \
"ds_read_b128 a[168:171], v49 offset:2048\n" \
"ds_read_b128 a[172:175], v49 offset:3072\n" \
"ds_read_b128 a[176:179], v49 offset:4096\n" \
"buffer_load_dword v55, v58, s[56:59], s96 offen sc1\n" \
"ds_read_b128 a[180:183], v49 offset:5120\n" \
"v_mfma_f32_16x16x32_bf16 v[120:123], a[128:131], a[112:115], v[44:47]\n" \
"s_add_u32 s70, s61, 0x0\n" \
"s_add_u32 s71, s61, 0x49000\n" \
"ds_read_b128 a[184:187], v49 offset:6144\n" \
"v_mfma_f32_16x16x32_bf16 v[120:123], a[132:135], a[116:119], v[120:123]\n" \
"ds_read_b128 a[188:191], v49 offset:7168\n" \
"v_add_u32_e32 v54, 0x49000, v20\n" \
"v_mfma_f32_16x16x32_bf16 v[120:123], a[136:139], a[120:123], v[120:123]\n" \
"v_exp_f32_e32 v80, v80\n" \
"v_mfma_f32_16x16x32_bf16 v[120:123], a[140:143], a[124:127], v[120:123]\n" \
"buffer_load_dwordx4 a[220:223], v20, s[56:59], s60 offen offset:0 sc1\n" \
"v_mfma_f32_16x16x32_bf16 v[124:127], a[156:159], a[124:127], v[44:47]\n" \
"v_exp_f32_e32 v81, v81\n" \
"v_mfma_f32_16x16x32_bf16 v[124:127], a[152:155], a[120:123], v[124:127]\n" \
"v_exp_f32_e32 v82, v82\n" \
"v_mfma_f32_16x16x32_bf16 v[124:127], a[148:151], a[116:119], v[124:127]\n" \
"v_exp_f32_e32 v83, v83\n" \
"v_mfma_f32_16x16x32_bf16 v[124:127], a[144:147], a[112:115], v[124:127]\n" \
"buffer_load_dwordx4 a[236:239], v54, s[56:59], s60 offen offset:0 sc1\n" \
"s_waitcnt lgkmcnt(7)\n" \
"v_mfma_f32_16x16x32_bf16 v[128:131], a[160:163], v[64:67], v[128:131]\n" \
"v_cvt_pk_bf16_f32 v80, v80, v81\n" \
"s_waitcnt lgkmcnt(6)\n" \
"v_mfma_f32_16x16x32_bf16 v[132:135], a[164:167], v[64:67], v[132:135]\n" \
"v_cvt_pk_bf16_f32 v81, v82, v83\n" \
"s_waitcnt lgkmcnt(5)\n" \
"v_mfma_f32_16x16x32_bf16 v[136:139], a[168:171], v[64:67], v[136:139]\n" \
"v_and_b32_e32 v80, v12, v80\n" \
"s_waitcnt lgkmcnt(4)\n" \
"v_mfma_f32_16x16x32_bf16 v[140:143], a[172:175], v[64:67], v[140:143]\n" \
"v_and_b32_e32 v81, v13, v81\n" \
"s_waitcnt lgkmcnt(3)\n" \
"v_mfma_f32_16x16x32_bf16 v[144:147], a[176:179], v[64:67], v[144:147]\n" \
"ds_read_b32 v60, v18 offset:0\n" \
"buffer_load_dwordx4 a[224:227], v20, s[56:59], s60 offen offset:1024 sc1\n" \
"s_waitcnt lgkmcnt(3)\n" \
"v_mfma_f32_16x16x32_bf16 v[148:151], a[180:183], v[64:67], v[148:151]\n" \
"ds_read_b32 v61, v18 offset:128\n" \
"s_waitcnt lgkmcnt(3)\n" \
"v_mfma_f32_16x16x32_bf16 v[152:155], a[184:187], v[64:67], v[152:155]\n" \
"ds_read_b128 a[0:3], v49 offset:8192\n" \
"s_waitcnt lgkmcnt(3)\n" \
"v_mfma_f32_16x16x32_bf16 v[156:159], a[188:191], v[64:67], v[156:159]\n" \
"v_exp_f32_e32 v84, v84\n" \
"buffer_load_dwordx4 a[240:243], v54, s[56:59], s60 offen offset:1024 sc1\n" \
"v_mfma_f32_16x16x32_bf16 a[192:195], a[212:215], v[64:67], a[192:195]\n" \
"v_exp_f32_e32 v85, v85\n" \
"v_mfma_f32_16x16x32_bf16 v[160:163], a[160:163], v[72:75], v[160:163]\n" \
"v_exp_f32_e32 v86, v86\n" \
"v_mfma_f32_16x16x32_bf16 v[164:167], a[164:167], v[72:75], v[164:167]\n" \
"v_exp_f32_e32 v87, v87\n" \
"v_mfma_f32_16x16x32_bf16 v[168:171], a[168:171], v[72:75], v[168:171]\n" \
"buffer_load_dwordx4 a[228:231], v20, s[56:59], s60 offen offset:2048 sc1\n" \
"v_mfma_f32_16x16x32_bf16 v[172:175], a[172:175], v[72:75], v[172:175]\n" \
"v_cvt_pk_bf16_f32 v82, v84, v85\n" \
"v_cvt_pk_bf16_f32 v83, v86, v87\n" \
"v_mfma_f32_16x16x32_bf16 v[176:179], a[176:179], v[72:75], v[176:179]\n" \
"v_and_b32_e32 v82, v14, v82\n" \
"v_and_b32_e32 v83, v15, v83\n" \
"v_mfma_f32_16x16x32_bf16 v[180:183], a[180:183], v[72:75], v[180:183]\n" \
"ds_read_b128 a[4:7], v49 offset:9216\n" \
"ds_read_b128 a[8:11], v49 offset:10240\n" \
"v_mfma_f32_16x16x32_bf16 v[184:187], a[184:187], v[72:75], v[184:187]\n" \
"buffer_load_dwordx4 a[244:247], v54, s[56:59], s60 offen offset:2048 sc1\n" \
"v_mfma_f32_16x16x32_bf16 v[188:191], a[188:191], v[72:75], v[188:191]\n" \
"ds_read_b128 a[12:15], v49 offset:11264\n" \
"ds_read_b128 a[16:19], v49 offset:12288\n" \
"v_mfma_f32_16x16x32_bf16 a[196:199], a[212:215], v[72:75], a[196:199]\n" \
"ds_read_b128 a[20:23], v49 offset:13312\n" \
"ds_read_b128 a[24:27], v49 offset:14336\n" \
"v_mfma_f32_16x16x32_bf16 v[192:195], a[160:163], v[80:83], v[192:195]\n" \
"ds_read_b128 a[28:31], v49 offset:15360\n" \
"v_add_u32_e32 v50, %[p4], v48\n" \
"v_mfma_f32_16x16x32_bf16 v[196:199], a[164:167], v[80:83], v[196:199]\n" \
"buffer_load_dwordx4 a[232:235], v20, s[56:59], s60 offen offset:3072 sc1\n" \
"v_mfma_f32_16x16x32_bf16 v[200:203], a[168:171], v[80:83], v[200:203]\n" \
"v_add_u32_e32 v51, 0x10000, v50\n" \
"v_accvgpr_read_b32 v40, a192\n" \
"v_mfma_f32_16x16x32_bf16 v[204:207], a[172:175], v[80:83], v[204:207]\n" \
"v_accvgpr_read_b32 v41, a196\n" \
"ds_write_b64 v19, v[40:41]\n" \
"v_mfma_f32_16x16x32_bf16 v[208:211], a[176:179], v[80:83], v[208:211]\n" \
"s_waitcnt vmcnt(8)\n" \
"ds_write_b128 v50, v[128:131] offset:0\n" \
"buffer_load_dwordx4 a[248:251], v54, s[56:59], s60 offen offset:3072 sc1\n" \
"v_mfma_f32_16x16x32_bf16 v[212:215], a[180:183], v[80:83], v[212:215]\n" \
"ds_write_b128 v50, v[132:135] offset:1024\n" \
"v_mfma_f32_16x16x32_bf16 v[216:219], a[184:187], v[80:83], v[216:219]\n" \
"ds_write_b128 v50, v[136:139] offset:2048\n" \
"v_mfma_f32_16x16x32_bf16 v[220:223], a[188:191], v[80:83], v[220:223]\n" \
"ds_write_b128 v50, v[140:143] offset:3072\n" \
"v_mfma_f32_16x16x32_bf16 a[200:203], a[212:215], v[80:83], a[200:203]\n" \
"ds_write_b128 v50, v[144:147] offset:8192\n" \
"ds_write_b128 v50, v[148:151] offset:9216\n" \
"ds_write_b128 v50, v[152:155] offset:10240\n" \
"ds_write_b128 v50, v[156:159] offset:11264\n" \
"ds_write_b128 v51, v[160:163] offset:0\n" \
"ds_write_b128 v51, v[164:167] offset:1024\n" \
"ds_write_b128 v51, v[168:171] offset:2048\n" \
"ds_write_b128 v51, v[172:175] offset:3072\n" \
"ds_write_b128 v51, v[176:179] offset:8192\n" \
"ds_write_b128 v51, v[180:183] offset:9216\n" \
"ds_write_b128 v51, v[184:187] offset:10240\n" \
"ds_write_b128 v51, v[188:191] offset:11264\n" \
"v_xor_b32_e32 v52, 0x4000, v50\n" \
"v_xor_b32_e32 v53, 0x4000, v51\n" \
"v_exp_f32_e32 v88, v88\n" \
"v_exp_f32_e32 v89, v89\n" \
"v_exp_f32_e32 v90, v90\n" \
"v_exp_f32_e32 v91, v91\n" \
"v_cvt_pk_bf16_f32 v88, v88, v89\n" \
"v_exp_f32_e32 v92, v92\n" \
"v_exp_f32_e32 v93, v93\n" \
"v_exp_f32_e32 v94, v94\n" \
"v_exp_f32_e32 v95, v95\n" \
"v_xor_b32_e32 v63, 0x400, v19\n" \
"v_cvt_pk_bf16_f32 v89, v90, v91\n" \
"v_cvt_pk_bf16_f32 v90, v92, v93\n" \
"v_cvt_pk_bf16_f32 v91, v94, v95\n" \
"v_readlane_b32 s93, v11, 19\n" \
"v_readlane_b32 s84, v11, 14\n" \
"v_readlane_b32 s85, v11, 15\n" \
"v_readlane_b32 s86, v11, 16\n" \
"s_mov_b32 s87, 0x20000\n" \
"v_readlane_b32 s92, v11, 17\n" \
"s_add_u32 m0, s93, 0x0\n" \
"v_readlane_b32 s97, v11, 33\n" \
"v_readlane_b32 s98, v11, 34\n" \
"v_exp_f32_e32 v120, v120\n" \
"v_exp_f32_e32 v121, v121\n" \
"v_exp_f32_e32 v122, v122\n" \
"v_exp_f32_e32 v123, v123\n" \
"s_waitcnt lgkmcnt(0)\n" \
"s_barrier\n" \
"ds_read_b128 v[128:131], v52 offset:0\n" \
"ds_read_b128 v[132:135], v52 offset:1024\n" \
"ds_read_b128 v[136:139], v52 offset:2048\n" \
"ds_read_b128 v[140:143], v52 offset:3072\n" \
"ds_read_b128 v[144:147], v52 offset:8192\n" \
"ds_read_b128 v[148:151], v52 offset:9216\n" \
"ds_read_b128 v[152:155], v52 offset:10240\n" \
"ds_read_b128 v[156:159], v52 offset:11264\n" \
"ds_read_b128 v[160:163], v53 offset:0\n" \
"ds_read_b128 v[164:167], v53 offset:1024\n" \
"ds_read_b128 v[168:171], v53 offset:2048\n" \
"ds_read_b128 v[172:175], v53 offset:3072\n" \
"ds_read_b128 v[176:179], v53 offset:8192\n" \
"ds_read_b128 v[180:183], v53 offset:9216\n" \
"ds_read_b128 v[184:187], v53 offset:10240\n" \
"ds_read_b128 v[188:191], v53 offset:11264\n" \
"ds_read_b64 v[42:43], v63\n" \
"v_mfma_f32_16x16x32_bf16 v[224:227], a[160:163], v[88:91], v[224:227]\n" \
"buffer_load_dwordx4 v0, s[84:87], s92 offen offset:0 lds\n" \
"v_mfma_f32_16x16x32_bf16 v[228:231], a[164:167], v[88:91], v[228:231]\n" \
"s_waitcnt vmcnt(9)\n" \
"v_cmp_ne_u32_e32 vcc, s62, v55\n" \
"v_mfma_f32_16x16x32_bf16 v[232:235], a[168:171], v[88:91], v[232:235]\n" \
"v_cvt_pk_bf16_f32 v120, v120, v121\n" \
"v_cvt_pk_bf16_f32 v121, v122, v123\n" \
"v_mfma_f32_16x16x32_bf16 v[236:239], a[172:175], v[88:91], v[236:239]\n" \
"v_exp_f32_e32 v124, v124\n" \
"s_cbranch_vccnz L_so0_%=\n" \
"L_ro0_%=:\n" \
"buffer_load_dwordx4 v1, s[84:87], s92 offen offset:1024 lds\n" \
"v_mfma_f32_16x16x32_bf16 v[240:243], a[176:179], v[88:91], v[240:243]\n" \
"v_exp_f32_e32 v125, v125\n" \
"v_mfma_f32_16x16x32_bf16 v[244:247], a[180:183], v[88:91], v[244:247]\n" \
"v_exp_f32_e32 v126, v126\n" \
"v_mfma_f32_16x16x32_bf16 v[248:251], a[184:187], v[88:91], v[248:251]\n" \
"v_exp_f32_e32 v127, v127\n" \
"v_mfma_f32_16x16x32_bf16 v[252:255], a[188:191], v[88:91], v[252:255]\n" \
"buffer_load_dwordx4 v2, s[84:87], s92 offen offset:2048 lds\n" \
"v_mfma_f32_16x16x32_bf16 a[204:207], a[212:215], v[88:91], a[204:207]\n" \
"v_cvt_pk_bf16_f32 v122, v124, v125\n" \
"v_cvt_pk_bf16_f32 v123, v126, v127\n" \
"s_waitcnt lgkmcnt(1)\n" \
"s_barrier\n" \
"v_and_b32_e32 v120, v12, v120\n" \
"v_and_b32_e32 v121, v13, v121\n" \
"v_and_b32_e32 v122, v14, v122\n" \
"v_and_b32_e32 v123, v15, v123\n" \
"buffer_load_dwordx4 v3, s[84:87], s92 offen offset:3072 lds\n" \
"s_add_u32 m0, s93, 0x1000\n" \
"v_mfma_f32_16x16x32_bf16 v[224:227], a[0:3], v[120:123], v[224:227]\n" \
"v_accvgpr_read_b32 v56, a200\n" \
"s_waitcnt lgkmcnt(0)\n" \
"v_add_f32_e32 v56, v56, v42\n" \
"v_mfma_f32_16x16x32_bf16 v[228:231], a[4:7], v[120:123], v[228:231]\n" \
"v_max_f32_e32 v62, v62, v56\n" \
"v_add_f32_e32 v192, v192, v128\n" \
"v_mfma_f32_16x16x32_bf16 v[232:235], a[8:11], v[120:123], v[232:235]\n" \
"v_rcp_f32_e32 v56, v56\n" \
"buffer_load_dwordx4 v4, s[84:87], s92 offen offset:0 lds\n" \
"v_mfma_f32_16x16x32_bf16 v[236:239], a[12:15], v[120:123], v[236:239]\n" \
"v_add_f32_e32 v193, v193, v129\n" \
"v_add_f32_e32 v194, v194, v130\n" \
"v_mfma_f32_16x16x32_bf16 v[240:243], a[16:19], v[120:123], v[240:243]\n" \
"v_add_f32_e32 v195, v195, v131\n" \
"v_add_f32_e32 v196, v196, v132\n" \
"v_add_f32_e32 v197, v197, v133\n" \
"v_add_f32_e32 v198, v198, v134\n" \
"v_add_f32_e32 v199, v199, v135\n" \
"v_mul_f32_e32 v56, v56, v60\n" \
"L_pf13:\n" \
"s_cbranch_execz L_pf17\n" \
"s_nop 0\n" \
"v_mul_f32_e32 v192, v56, v192\n" \
"buffer_load_dwordx4 v5, s[84:87], s92 offen offset:1024 lds\n" \
"v_mul_f32_e32 v193, v56, v193\n" \
"v_mul_f32_e32 v194, v56, v194\n" \
"v_mul_f32_e32 v195, v56, v195\n" \
"v_mul_f32_e32 v196, v56, v196\n" \
"v_mul_f32_e32 v197, v56, v197\n" \
"v_mul_f32_e32 v198, v56, v198\n" \
"v_mul_f32_e32 v199, v56, v199\n" \
"v_add_f32_e32 v200, v200, v136\n" \
"v_add_f32_e32 v201, v201, v137\n" \
"v_add_f32_e32 v202, v202, v138\n" \
"v_add_f32_e32 v203, v203, v139\n" \
"v_add_f32_e32 v204, v204, v140\n" \
"v_add_f32_e32 v205, v205, v141\n" \
"buffer_load_dwordx4 v6, s[84:87], s92 offen offset:2048 lds\n" \
"v_add_f32_e32 v206, v206, v142\n" \
"v_add_f32_e32 v207, v207, v143\n" \
"v_cvt_pk_bf16_f32 v64, v192, v193\n" \
"v_cvt_pk_bf16_f32 v65, v194, v195\n" \
"v_cvt_pk_bf16_f32 v66, v196, v197\n" \
"v_cvt_pk_bf16_f32 v67, v198, v199\n" \
"v_mfma_f32_16x16x32_bf16 v[244:247], a[20:23], v[120:123], v[244:247]\n" \
"v_mul_f32_e32 v200, v56, v200\n" \
"v_mul_f32_e32 v201, v56, v201\n" \
"v_mfma_f32_16x16x32_bf16 a[208:211], a[216:219], v[64:67], a[208:211]\n" \
"v_mul_f32_e32 v202, v56, v202\n" \
"v_mul_f32_e32 v203, v56, v203\n" \
"buffer_store_dwordx4 v[64:67], v20, s[56:59], s70 offen offset:0 sc1\n" \
"v_mul_f32_e32 v204, v56, v204\n" \
"v_mul_f32_e32 v205, v56, v205\n" \
"v_mul_f32_e32 v206, v56, v206\n" \
"v_mul_f32_e32 v207, v56, v207\n" \
"v_add_f32_e32 v208, v208, v144\n" \
"v_add_f32_e32 v209, v209, v145\n" \
"v_add_f32_e32 v210, v210, v146\n" \
"v_add_f32_e32 v211, v211, v147\n" \
"v_add_f32_e32 v212, v212, v148\n" \
"v_add_f32_e32 v213, v213, v149\n" \
"v_add_f32_e32 v214, v214, v150\n" \
"v_add_f32_e32 v215, v215, v151\n" \
"v_cvt_pk_bf16_f32 v68, v200, v201\n" \
"buffer_load_dwordx4 v7, s[84:87], s92 offen offset:3072 lds\n" \
"s_add_u32 m0, s93, 0x2000\n" \
"v_cvt_pk_bf16_f32 v69, v202, v203\n" \
"v_cvt_pk_bf16_f32 v70, v204, v205\n" \
"v_cvt_pk_bf16_f32 v71, v206, v207\n" \
"v_mul_f32_e32 v208, v56, v208\n" \
"v_mul_f32_e32 v209, v56, v209\n" \
"v_mfma_f32_16x16x32_bf16 a[208:211], a[216:219], v[68:71], a[208:211]\n" \
"v_mul_f32_e32 v210, v56, v210\n" \
"v_mul_f32_e32 v211, v56, v211\n" \
"v_mul_f32_e32 v212, v56, v212\n" \
"v_mul_f32_e32 v213, v56, v213\n" \
"v_mul_f32_e32 v214, v56, v214\n" \
"buffer_store_dwordx4 v[68:71], v20, s[56:59], s70 offen offset:1024 sc1\n" \
"v_mul_f32_e32 v215, v56, v215\n" \
"v_add_f32_e32 v216, v216, v152\n" \
"v_add_f32_e32 v217, v217, v153\n" \
"v_add_f32_e32 v218, v218, v154\n" \
"v_add_f32_e32 v219, v219, v155\n" \
"v_add_f32_e32 v220, v220, v156\n" \
"v_add_f32_e32 v221, v221, v157\n" \
"v_add_f32_e32 v222, v222, v158\n" \
"v_add_f32_e32 v223, v223, v159\n" \
"v_cvt_pk_bf16_f32 v72, v208, v209\n" \
"v_cvt_pk_bf16_f32 v73, v210, v211\n" \
"v_cvt_pk_bf16_f32 v74, v212, v213\n" \
"v_cvt_pk_bf16_f32 v75, v214, v215\n" \
"buffer_store_dwordx4 v[72:75], v20, s[56:59], s70 offen offset:2048 sc1\n" \
"v_mfma_f32_16x16x32_bf16 v[248:251], a[24:27], v[120:123], v[248:251]\n" \
"v_mfma_f32_16x16x32_bf16 a[208:211], a[216:219], v[72:75], a[208:211]\n" \
"v_mul_f32_e32 v216, v56, v216\n" \
"v_mul_f32_e32 v217, v56, v217\n" \
"v_mul_f32_e32 v218, v56, v218\n" \
"v_mul_f32_e32 v219, v56, v219\n" \
"v_mul_f32_e32 v220, v56, v220\n" \
"v_mul_f32_e32 v221, v56, v221\n" \
"v_mul_f32_e32 v222, v56, v222\n" \
"buffer_load_dwordx4 v8, s[84:87], s92 offen offset:0 lds\n" \
"v_mul_f32_e32 v223, v56, v223\n" \
"v_cvt_pk_bf16_f32 v76, v216, v217\n" \
"v_cvt_pk_bf16_f32 v77, v218, v219\n" \
"v_cvt_pk_bf16_f32 v78, v220, v221\n" \
"v_cvt_pk_bf16_f32 v79, v222, v223\n" \
"v_add_f32_e32 v224, v224, v160\n" \
"v_add_f32_e32 v225, v225, v161\n" \
"v_add_f32_e32 v226, v226, v162\n" \
"v_add_f32_e32 v227, v227, v163\n" \
"v_add_f32_e32 v228, v228, v164\n" \
"v_add_f32_e32 v229, v229, v165\n" \
"v_add_f32_e32 v230, v230, v166\n" \
"v_add_f32_e32 v231, v231, v167\n" \
"buffer_store_dwordx4 v[76:79], v20, s[56:59], s70 offen offset:3072 sc1\n" \
"v_mfma_f32_16x16x32_bf16 a[208:211], a[216:219], v[76:79], a[208:211]\n" \
"v_add_f32_e32 v232, v232, v168\n" \
"v_add_f32_e32 v233, v233, v169\n" \
"v_add_f32_e32 v234, v234, v170\n" \
"v_add_f32_e32 v235, v235, v171\n" \
"v_add_f32_e32 v236, v236, v172\n" \
"v_add_f32_e32 v237, v237, v173\n" \
"v_add_f32_e32 v238, v238, v174\n" \
"v_add_f32_e32 v239, v239, v175\n" \
"v_mfma_f32_16x16x32_bf16 v[252:255], a[28:31], v[120:123], v[252:255]\n" \
"v_add_f32_e32 v240, v240, v176\n" \
"v_add_f32_e32 v241, v241, v177\n" \
"buffer_load_dwordx4 a[96:99], v20, s[56:59], s97 offen offset:0 sc1\n" \
"v_add_f32_e32 v242, v242, v178\n" \
"v_add_f32_e32 v243, v243, v179\n" \
"v_add_f32_e32 v244, v244, v180\n" \
"v_add_f32_e32 v245, v245, v181\n" \
"v_add_f32_e32 v246, v246, v182\n" \
"v_add_f32_e32 v247, v247, v183\n" \
"v_add_f32_e32 v248, v248, v184\n" \
"v_add_f32_e32 v249, v249, v185\n" \
"v_add_f32_e32 v250, v250, v186\n" \
"v_add_f32_e32 v251, v251, v187\n" \
"v_add_f32_e32 v252, v252, v188\n" \
"v_add_f32_e32 v253, v253, v189\n" \
"v_add_f32_e32 v254, v254, v190\n" \
"buffer_load_dwordx4 a[104:107], v54, s[56:59], s97 offen offset:0 sc1\n" \
"v_add_f32_e32 v255, v255, v191\n" \
"v_mfma_f32_16x16x32_bf16 a[204:207], a[212:215], v[120:123], a[204:207]\n" \
"buffer_load_dwordx4 a[112:115], v20, s[56:59], s98 offen offset:0 sc1\n" \
"buffer_load_dwordx4 a[120:123], v54, s[56:59], s98 offen offset:0 sc1\n" \
"buffer_load_dwordx4 a[100:103], v20, s[56:59], s97 offen offset:1024 sc1\n" \
"buffer_load_dwordx4 a[108:111], v54, s[56:59], s97 offen offset:1024 sc1\n" \
"buffer_load_dwordx4 a[116:119], v20, s[56:59], s98 offen offset:1024 sc1\n" \
"buffer_load_dwordx4 a[124:127], v54, s[56:59], s98 offen offset:1024 sc1\n" \
"s_nop 1\n" \
"v_accvgpr_read_b32 v57, a204\n" \
"v_add_f32_e32 v57, v57, v43\n" \
"v_max_f32_e32 v62, v62, v57\n" \
"v_rcp_f32_e32 v57, v57\n" \
"v_cmp_lt_f32_e64 s[72:73], %[lim], v62\n" \
"v_mul_f32_e32 v57, v57, v61\n" \
"v_mul_f32_e32 v224, v57, v224\n" \
"v_mul_f32_e32 v225, v57, v225\n" \
"v_mul_f32_e32 v226, v57, v226\n" \
"v_mul_f32_e32 v227, v57, v227\n" \
"v_mul_f32_e32 v228, v57, v228\n" \
"v_mul_f32_e32 v229, v57, v229\n" \
"v_mul_f32_e32 v230, v57, v230\n" \
"v_mul_f32_e32 v231, v57, v231\n" \
"v_cvt_pk_bf16_f32 v80, v224, v225\n" \
"v_cvt_pk_bf16_f32 v81, v226, v227\n" \
"v_cvt_pk_bf16_f32 v82, v228, v229\n" \
"v_cvt_pk_bf16_f32 v83, v230, v231\n" \
"v_mul_f32_e32 v232, v57, v232\n" \
"v_mul_f32_e32 v233, v57, v233\n" \
"v_mfma_f32_16x16x32_bf16 a[208:211], a[216:219], v[80:83], a[208:211]\n" \
"v_mul_f32_e32 v234, v57, v234\n" \
"v_mul_f32_e32 v235, v57, v235\n" \
"v_mul_f32_e32 v236, v57, v236\n" \
"v_mul_f32_e32 v237, v57, v237\n" \
"v_mul_f32_e32 v238, v57, v238\n" \
"v_mul_f32_e32 v239, v57, v239\n" \
"v_cvt_pk_bf16_f32 v84, v232, v233\n" \
"v_cvt_pk_bf16_f32 v85, v234, v235\n" \
"v_cvt_pk_bf16_f32 v86, v236, v237\n" \
"v_cvt_pk_bf16_f32 v87, v238, v239\n" \
"v_mul_f32_e32 v240, v57, v240\n" \
"v_mul_f32_e32 v241, v57, v241\n" \
"v_mfma_f32_16x16x32_bf16 a[208:211], a[216:219], v[84:87], a[208:211]\n" \
"v_mul_f32_e32 v242, v57, v242\n" \
"v_mul_f32_e32 v243, v57, v243\n" \
"v_mul_f32_e32 v244, v57, v244\n" \
"v_mul_f32_e32 v245, v57, v245\n" \
"v_mul_f32_e32 v246, v57, v246\n" \
"v_mul_f32_e32 v247, v57, v247\n" \
"v_cvt_pk_bf16_f32 v96, v240, v241\n" \
"v_cvt_pk_bf16_f32 v97, v242, v243\n" \
"v_cvt_pk_bf16_f32 v98, v244, v245\n" \
"v_cvt_pk_bf16_f32 v99, v246, v247\n" \
"v_mul_f32_e32 v248, v57, v248\n" \
"v_mul_f32_e32 v249, v57, v249\n" \
"v_mfma_f32_16x16x32_bf16 a[208:211], a[216:219], v[96:99], a[208:211]\n" \
"v_mul_f32_e32 v250, v57, v250\n" \
"v_mul_f32_e32 v251, v57, v251\n" \
"v_mul_f32_e32 v252, v57, v252\n" \
"v_mul_f32_e32 v253, v57, v253\n" \
"v_mul_f32_e32 v254, v57, v254\n" \
"v_mul_f32_e32 v255, v57, v255\n" \
"v_cvt_pk_bf16_f32 v100, v248, v249\n" \
"v_cvt_pk_bf16_f32 v101, v250, v251\n" \
"v_cvt_pk_bf16_f32 v102, v252, v253\n" \
"v_cvt_pk_bf16_f32 v103, v254, v255\n" \
"buffer_store_dwordx4 v[80:83], v20, s[56:59], s71 offen offset:0 sc1\n" \
"buffer_store_dwordx4 v[84:87], v20, s[56:59], s71 offen offset:1024 sc1\n" \
"v_mfma_f32_16x16x32_bf16 a[208:211], a[216:219], v[100:103], a[208:211]\n" \
"buffer_store_dwordx4 v[96:99], v20, s[56:59], s71 offen offset:2048 sc1\n" \
"buffer_store_dwordx4 v[100:103], v20, s[56:59], s71 offen offset:3072 sc1\n" \
"s_nop 5\n" \
"v_accvgpr_read_b32 v63, a208\n" \
"v_cmp_u_f32_e32 vcc, v63, v63\n" \
"s_nop 0\n" \
"s_or_b64 vcc, s[72:73], vcc\n" \
"s_cbranch_scc0 L_out_%=\n" \
"s_branch L_end_%=\n" \
"L_h1_%=:\n" \
"L_pf14:\n" \
"s_cbranch_execz L_pf18\n" \
"s_nop 0\n" \
"s_cmp_eq_u32 s63, 0x400000\n" \
"s_cbranch_scc1 L_hc1_%=\n" \
"s_cmp_lg_u32 s63, 0x0\n" \
"s_cbranch_scc1 L_hn01_%=\n" \
"s_mov_b32 exec_lo, 0xffffffff\n" \
"s_mov_b32 exec_hi, 0xf\n" \
"buffer_load_dword v23, v16, s[80:83], 0 offen\n" \
"s_mov_b64 exec, -1\n" \
"s_branch L_hr1_%=\n" \
"L_hn01_%=:\n" \
"buffer_load_dword v22, v21, s[56:59], %[fkb] offen sc1\n" \
"s_branch L_hr1_%=\n" \
"L_hs1_%=:\n" \
"s_sleep 1\n" \
"buffer_load_dword v22, v21, s[56:59], %[fkb] offen sc1\n" \
"s_add_u32 s101, s101, 1\n" \
"s_waitcnt vmcnt(0)\n" \
"s_cmp_gt_u32 s101, 0x100000\n" \
"s_cbranch_scc1 L_hr1_%=\n" \
"L_hc1_%=:\n" \
"v_cmp_ne_u32_e32 vcc, s62, v22\n" \
"s_nop 4\n" \
"s_cbranch_vccnz L_hs1_%=\n" \
"s_branch L_hr1_%=\n" \
"L_h2_%=:\n" \
"s_cmp_eq_u32 s63, 0x400000\n" \
"s_cbranch_scc1 L_hc2_%=\n" \
"s_cmp_lg_u32 s63, 0x0\n" \
"s_cbranch_scc1 L_hn02_%=\n" \
"s_mov_b32 exec_lo, 0xffffffff\n" \
"s_mov_b32 exec_hi, 0xf\n" \
"buffer_load_dword v23, v16, s[80:83], 0 offen\n" \
"s_mov_b64 exec, -1\n" \
"s_branch L_hr2_%=\n" \
"L_hn02_%=:\n" \
"buffer_load_dword v22, v21, s[56:59], %[fkb] offen sc1\n" \
"s_branch L_hr2_%=\n" \
"L_hs2_%=:\n" \
"s_sleep 1\n" \
"buffer_load_dword v22, v21, s[56:59], %[fkb] offen sc1\n" \
"s_add_u32 s101, s101, 1\n" \
"s_waitcnt vmcnt(0)\n" \
"s_cmp_gt_u32 s101, 0x100000\n" \
"s_cbranch_scc1 L_hr2_%=\n" \
"L_hc2_%=:\n" \
"v_cmp_ne_u32_e32 vcc, s62, v22\n" \
"s_nop 4\n" \
"s_cbranch_vccnz L_hs2_%=\n" \
"s_branch L_hr2_%=\n" \
"L_so0_%=:\n" \
"s_sleep 1\n" \
"buffer_load_dword v55, v58, s[56:59], s96 offen sc1\n" \
"s_add_u32 s101, s101, 1\n" \
"s_waitcnt vmcnt(0)\n" \
"v_cmp_ne_u32_e32 vcc, s62, v55\n" \
"s_cmp_gt_u32 s101, 0x100000\n" \
"s_cbranch_scc1 L_ro0_%=\n" \
"s_nop 2\n" \
"s_cbranch_vccnz L_so0_%=\n" \
"s_branch L_ro0_%=\n" \
"L_so1_%=:\n" \
"s_sleep 1\n" \
"buffer_load_dword v57, v60, s[56:59], s96 offen sc1\n" \
"s_add_u32 s101, s101, 1\n" \
"s_waitcnt vmcnt(0)\n" \
"v_cmp_ne_u32_e32 vcc, s62, v57\n" \
"s_cmp_gt_u32 s101, 0x100000\n" \
"s_cbranch_scc1 L_ro1_%=\n" \
"s_nop 2\n" \
"s_cbranch_vccnz L_so1_%=\n" \
"s_branch L_ro1_%=\n" \
"L_sa_%=:\n" \
"s_sleep 1\n" \
"buffer_load_dword v37, v38, s[56:59], s64 offen sc1\n" \
"s_add_u32 s101, s101, 1\n" \
"s_waitcnt vmcnt(0)\n" \
"v_cmp_ne_u32_e32 vcc, s62, v37\n" \
"s_cmp_gt_u32 s101, 0x100000\n" \
"s_cbranch_scc1 L_ra_%=\n" \
"s_nop 2\n" \
"s_cbranch_vccnz L_sa_%=\n" \
"s_branch L_ra_%=\n" \
"L_sb_%=:\n" \
"s_sleep 1\n" \
"buffer_load_dword v47, v38, s[56:59], s71 offen sc1\n" \
"s_add_u32 s101, s101, 1\n" \
"s_waitcnt vmcnt(0)\n" \
"v_cmp_ne_u32_e32 vcc, s62, v47\n" \
"s_cmp_gt_u32 s101, 0x100000\n" \
"s_cbranch_scc1 L_rb_%=\n" \
"s_nop 2\n" \
"s_cbranch_vccnz L_sb_%=\n" \
"s_branch L_rb_%=\n" \
"L_sx0_%=:\n" \
"s_sleep 1\n" \
"buffer_load_dword v37, v40, s[56:59], s74 offen sc1\n" \
"s_add_u32 s101, s101, 1\n" \
"s_waitcnt vmcnt(0)\n" \
"v_cmp_eq_u32_e64 s[98:99], s62, v37\n" \
"s_cmp_gt_u32 s101, 0x100000\n" \
"s_cbranch_scc1 L_rx0_%=\n" \
"s_andn2_b32 s75, 0x1, s98\n" \
"s_cbranch_scc1 L_sx0_%=\n" \
"s_branch L_rx0_%=\n" \
"L_sx1_%=:\n" \
"s_sleep 1\n" \
"buffer_load_dword v37, v40, s[56:59], s74 offen sc1\n" \
"s_add_u32 s101, s101, 1\n" \
"s_waitcnt vmcnt(0)\n" \
"v_cmp_eq_u32_e64 s[98:99], s62, v37\n" \
"s_cmp_gt_u32 s101, 0x100000\n" \
"s_cbranch_scc1 L_rx1_%=\n" \
"s_andn2_b32 s75, 0x2, s98\n" \
"s_cbranch_scc1 L_sx1_%=\n" \
"s_branch L_rx1_%=\n" \
"L_sx2_%=:\n" \
"s_sleep 1\n" \
"buffer_load_dword v37, v40, s[56:59], s74 offen sc1\n" \
"s_add_u32 s101, s101, 1\n" \
"s_waitcnt vmcnt(0)\n" \
"v_cmp_eq_u32_e64 s[98:99], s62, v37\n" \
"s_cmp_gt_u32 s101, 0x100000\n" \
"s_cbranch_scc1 L_rx2_%=\n" \
"s_andn2_b32 s75, 0x4, s98\n" \
"s_cbranch_scc1 L_sx2_%=\n" \
"s_branch L_rx2_%=\n" \
"L_sx3_%=:\n" \
"s_sleep 1\n" \
"buffer_load_dword v37, v40, s[56:59], s74 offen sc1\n" \
"s_add_u32 s101, s101, 1\n" \
"s_waitcnt vmcnt(0)\n" \
"v_cmp_eq_u32_e64 s[98:99], s62, v37\n" \
"s_cmp_gt_u32 s101, 0x100000\n" \
"s_cbranch_scc1 L_rx3_%=\n" \
"s_andn2_b32 s75, s100, s98\n" \
"s_cbranch_scc1 L_sx3_%=\n" \
"s_branch L_rx3_%=\n" \
"L_sx4_%=:\n" \
"s_sleep 1\n" \
"buffer_load_dword v37, v40, s[56:59], s74 offen sc1\n" \
"s_add_u32 s101, s101, 1\n" \
"s_waitcnt vmcnt(0)\n" \
"v_cmp_eq_u32_e64 s[98:99], s62, v37\n" \
"s_cmp_gt_u32 s101, 0x100000\n" \
"s_cbranch_scc1 L_rx4_%=\n" \
"s_andn2_b32 s75, 0x10, s98\n" \
"s_cbranch_scc1 L_sx4_%=\n" \
"s_branch L_rx4_%=\n" \
"L_sx5_%=:\n" \
"s_sleep 1\n" \
"buffer_load_dword v37, v40, s[56:59], s74 offen sc1\n" \
"s_add_u32 s101, s101, 1\n" \
"s_waitcnt vmcnt(0)\n" \
"v_cmp_eq_u32_e64 s[98:99], s62, v37\n" \
"s_cmp_gt_u32 s101, 0x100000\n" \
"s_cbranch_scc1 L_rx5_%=\n" \
"s_andn2_b32 s75, 0x20, s98\n" \
"s_cbranch_scc1 L_sx5_%=\n" \
"s_branch L_rx5_%=\n" \
"L_sx6_%=:\n" \
"s_sleep 1\n" \
"buffer_load_dword v37, v40, s[56:59], s74 offen sc1\n" \
"s_add_u32 s101, s101, 1\n" \
"s_waitcnt vmcnt(0)\n" \
"v_cmp_eq_u32_e64 s[98:99], s62, v37\n" \
"s_cmp_gt_u32 s101, 0x100000\n" \
"s_cbranch_scc1 L_rx6_%=\n" \
"s_andn2_b32 s75, 0x40, s98\n" \
"s_cbranch_scc1 L_sx6_%=\n" \
"s_branch L_rx6_%=\n" \
"L_sx7_%=:\n" \
"s_sleep 1\n" \
"buffer_load_dword v37, v40, s[56:59], s74 offen sc1\n" \
"s_add_u32 s101, s101, 1\n" \
"s_waitcnt vmcnt(0)\n" \
"v_cmp_eq_u32_e64 s[98:99], s62, v37\n" \
"s_cmp_gt_u32 s101, 0x100000\n" \
"s_cbranch_scc1 L_rx7_%=\n" \
"s_andn2_b32 s75, 0x80, s98\n" \
"s_cbranch_scc1 L_sx7_%=\n" \
"s_branch L_rx7_%=\n" \
"L_sx8_%=:\n" \
"s_sleep 1\n" \
"buffer_load_dword v37, v40, s[56:59], s74 offen sc1\n" \
"s_add_u32 s101, s101, 1\n" \
"s_waitcnt vmcnt(0)\n" \
"v_cmp_eq_u32_e64 s[98:99], s62, v37\n" \
"s_cmp_gt_u32 s101, 0x100000\n" \
"s_cbranch_scc1 L_rx8_%=\n" \
"s_andn2_b32 s75, 0x100, s98\n" \
"s_cbranch_scc1 L_sx8_%=\n" \
"s_branch L_rx8_%=\n" \
"L_sx9_%=:\n" \
"s_sleep 1\n" \
"buffer_load_dword v37, v40, s[56:59], s74 offen sc1\n" \
"s_add_u32 s101, s101, 1\n" \
"s_waitcnt vmcnt(0)\n" \
"v_cmp_eq_u32_e64 s[98:99], s62, v37\n" \
"s_cmp_gt_u32 s101, 0x100000\n" \
"s_cbranch_scc1 L_rx9_%=\n" \
"s_andn2_b32 s75, 0x200, s98\n" \
"s_cbranch_scc1 L_sx9_%=\n" \
"s_branch L_rx9_%=\n" \
"L_sx10_%=:\n" \
"s_sleep 1\n" \
"buffer_load_dword v37, v40, s[56:59], s74 offen sc1\n" \
"s_add_u32 s101, s101, 1\n" \
"s_waitcnt vmcnt(0)\n" \
"v_cmp_eq_u32_e64 s[98:99], s62, v37\n" \
"s_cmp_gt_u32 s101, 0x100000\n" \
"s_cbranch_scc1 L_rx10_%=\n" \
"s_andn2_b32 s75, 0x400, s98\n" \
"s_cbranch_scc1 L_sx10_%=\n" \
"s_branch L_rx10_%=\n" \
"L_att_stub:\n" \
"s_mov_b32 s101, 0\n" \
"v_add_u32_e32 v32, 0x49000, v20\n" \
"v_readlane_b32 s96, v11, 33\n" \
"v_readlane_b32 s97, v11, 34\n" \
"buffer_load_dwordx4 a[220:223], v20, s[56:59], s60 offen offset:0 sc1\n" \
"buffer_load_dwordx4 a[236:239], v32, s[56:59], s60 offen offset:0 sc1\n" \
"buffer_load_dwordx4 a[224:227], v20, s[56:59], s60 offen offset:1024 sc1\n" \
"buffer_load_dwordx4 a[240:243], v32, s[56:59], s60 offen offset:1024 sc1\n" \
"buffer_load_dwordx4 a[228:231], v20, s[56:59], s60 offen offset:2048 sc1\n" \
"buffer_load_dwordx4 a[244:247], v32, s[56:59], s60 offen offset:2048 sc1\n" \
"buffer_load_dwordx4 a[232:235], v20, s[56:59], s60 offen offset:3072 sc1\n" \
"buffer_load_dwordx4 a[248:251], v32, s[56:59], s60 offen offset:3072 sc1\n" \
"buffer_load_dwordx4 a[96:99], v20, s[56:59], s96 offen offset:0 sc1\n" \
"buffer_load_dwordx4 a[104:107], v32, s[56:59], s96 offen offset:0 sc1\n" \
"buffer_load_dwordx4 a[112:115], v20, s[56:59], s97 offen offset:0 sc1\n" \
"buffer_load_dwordx4 a[120:123], v32, s[56:59], s97 offen offset:0 sc1\n" \
"buffer_load_dwordx4 a[100:103], v20, s[56:59], s96 offen offset:1024 sc1\n" \
"buffer_load_dwordx4 a[108:111], v32, s[56:59], s96 offen offset:1024 sc1\n" \
"buffer_load_dwordx4 a[116:119], v20, s[56:59], s97 offen offset:1024 sc1\n" \
"buffer_load_dwordx4 a[124:127], v32, s[56:59], s97 offen offset:1024 sc1\n" \
"buffer_load_dwordx4 v[64:67], v20, s[56:59], s61 offen offset:0 sc1\n" \
"buffer_load_dwordx4 v[68:71], v20, s[56:59], s61 offen offset:1024 sc1\n" \
"buffer_load_dwordx4 v[72:75], v20, s[56:59], s61 offen offset:2048 sc1\n" \
"buffer_load_dwordx4 v[76:79], v20, s[56:59], s61 offen offset:3072 sc1\n" \
"buffer_load_dwordx4 v[80:83], v32, s[56:59], s61 offen offset:0 sc1\n" \
"buffer_load_dwordx4 v[84:87], v32, s[56:59], s61 offen offset:1024 sc1\n" \
"buffer_load_dwordx4 v[96:99], v32, s[56:59], s61 offen offset:2048 sc1\n" \
"buffer_load_dwordx4 v[100:103], v32, s[56:59], s61 offen offset:3072 sc1\n" \
"s_waitcnt vmcnt(0)\n" \
"s_branch L_out_%=\n" \
"L_sw_%=:\n" \
"L_pf15:\n" \
"s_cbranch_execz L_pf19\n" \
"s_nop 0\n" \
"v_mfma_f32_16x16x32_bf16 v[104:107], a[128:131], a[16:19], v[36:39]\n" \
"s_add_u32 s68, s60, 0xfff6e000\n" \
"s_add_u32 s69, s60, 0xfffb7000\n" \
"s_add_u32 s70, s60, 0x0\n" \
"s_add_u32 s71, s60, 0x49000\n" \
"v_mfma_f32_16x16x32_bf16 v[104:107], a[132:135], a[20:23], v[104:107]\n" \
"s_xor_b32 m0, s65, 0x12000\n" \
"ds_read_b128 a[160:163], v49 offset:0\n" \
"v_mfma_f32_16x16x32_bf16 v[104:107], a[136:139], a[24:27], v[104:107]\n" \
"buffer_load_dwordx4 v50, %[srd], s64 offen offset:0 sc1 lds\n" \
"v_mfma_f32_16x16x32_bf16 v[104:107], a[140:143], a[28:31], v[104:107]\n" \
"ds_read_b128 a[164:167], v49 offset:1024\n" \
"ds_read_b128 a[168:171], v49 offset:2048\n" \
"v_mfma_f32_16x16x32_bf16 v[108:111], a[156:159], a[28:31], v[36:39]\n" \
"ds_read_b128 a[172:175], v49 offset:3072\n" \
"ds_read_b128 a[176:179], v49 offset:4096\n" \
"v_mfma_f32_16x16x32_bf16 v[108:111], a[152:155], a[24:27], v[108:111]\n" \
"buffer_load_dwordx4 v50, %[srd], s64 offen offset:1024 sc1 lds\n" \
"v_mfma_f32_16x16x32_bf16 v[108:111], a[148:151], a[20:23], v[108:111]\n" \
"ds_read_b128 a[180:183], v49 offset:5120\n" \
"ds_read_b128 a[184:187], v49 offset:6144\n" \
"v_mfma_f32_16x16x32_bf16 v[108:111], a[144:147], a[16:19], v[108:111]\n" \
"ds_read_b128 a[188:191], v49 offset:7168\n" \
"s_mov_b32 s65, %[m2]\n" \
"v_mfma_f32_16x16x32_bf16 v[112:115], a[128:131], a[32:35], v[40:43]\n" \
"v_add_u32_e32 v63, %[p4], v20\n" \
"s_add_u32 s67, %[t2], 40960\n" \
"v_mfma_f32_16x16x32_bf16 v[112:115], a[132:135], a[36:39], v[112:115]\n" \
"v_add_u32_e32 v50, 0x6000, v63\n" \
"s_xor_b32 m0, s65, 0x8000\n" \
"v_mfma_f32_16x16x32_bf16 v[112:115], a[136:139], a[40:43], v[112:115]\n" \
"v_and_b32_e32 v64, v12, v64\n" \
"v_and_b32_e32 v65, v13, v65\n" \
"v_mfma_f32_16x16x32_bf16 v[112:115], a[140:143], a[44:47], v[112:115]\n" \
"buffer_load_dwordx4 v50, %[srd], s67 offen offset:0 sc1 lds\n" \
"v_mfma_f32_16x16x32_bf16 v[120:123], a[140:143], a[60:63], v[44:47]\n" \
"v_and_b32_e32 v66, v14, v66\n" \
"v_and_b32_e32 v67, v15, v67\n" \
"v_mfma_f32_16x16x32_bf16 v[120:123], a[136:139], a[56:59], v[120:123]\n" \
"v_exp_f32_e32 v80, v80\n" \
"v_mfma_f32_16x16x32_bf16 v[120:123], a[132:135], a[52:55], v[120:123]\n" \
"v_exp_f32_e32 v81, v81\n" \
"v_mfma_f32_16x16x32_bf16 v[120:123], a[128:131], a[48:51], v[120:123]\n" \
"v_exp_f32_e32 v82, v82\n" \
"v_mfma_f32_16x16x32_bf16 v[116:119], a[144:147], a[32:35], v[40:43]\n" \
"buffer_load_dwordx4 v50, %[srd], s67 offen offset:1024 sc1 lds\n" \
"v_mfma_f32_16x16x32_bf16 v[116:119], a[148:151], a[36:39], v[116:119]\n" \
"v_exp_f32_e32 v83, v83\n" \
"v_mfma_f32_16x16x32_bf16 v[116:119], a[152:155], a[40:43], v[116:119]\n" \
"v_cvt_pk_bf16_f32 v80, v80, v81\n" \
"v_cvt_pk_bf16_f32 v81, v82, v83\n" \
"v_mfma_f32_16x16x32_bf16 v[116:119], a[156:159], a[44:47], v[116:119]\n" \
"v_exp_f32_e32 v84, v84\n" \
"v_mfma_f32_16x16x32_bf16 v[124:127], a[156:159], a[60:63], v[44:47]\n" \
"v_exp_f32_e32 v85, v85\n" \
"v_mfma_f32_16x16x32_bf16 v[124:127], a[152:155], a[56:59], v[124:127]\n" \
"v_exp_f32_e32 v86, v86\n" \
"v_mfma_f32_16x16x32_bf16 v[124:127], a[148:151], a[52:55], v[124:127]\n" \
"buffer_load_dwordx4 v50, %[srd], s67 offen offset:2048 sc1 lds\n" \
"v_mfma_f32_16x16x32_bf16 v[124:127], a[144:147], a[48:51], v[124:127]\n" \
"v_cvt_pk_bf16_f32 v82, v84, v85\n" \
"s_waitcnt lgkmcnt(0)\n" \
"v_mfma_f32_16x16x32_bf16 v[128:131], a[160:163], v[64:67], v[128:131]\n" \
"v_exp_f32_e32 v87, v87\n" \
"v_mfma_f32_16x16x32_bf16 v[132:135], a[164:167], v[64:67], v[132:135]\n" \
"v_cvt_pk_bf16_f32 v83, v86, v87\n" \
"s_mov_b32 s64, %[t2]\n" \
"v_mfma_f32_16x16x32_bf16 v[136:139], a[168:171], v[64:67], v[136:139]\n" \
"v_add_u32_e32 v51, 0x12000, v63\n" \
"ds_read_b32 v58, v18 offset:512\n" \
"v_mfma_f32_16x16x32_bf16 v[140:143], a[172:175], v[64:67], v[140:143]\n" \
"v_exp_f32_e32 v88, v88\n" \
"v_mfma_f32_16x16x32_bf16 v[144:147], a[176:179], v[64:67], v[144:147]\n" \
"buffer_load_dwordx4 v50, %[srd], s67 offen offset:3072 sc1 lds\n" \
"v_mfma_f32_16x16x32_bf16 v[148:151], a[180:183], v[64:67], v[148:151]\n" \
"s_xor_b32 m0, s65, 0xa000\n" \
"ds_read_b32 v59, v18 offset:640\n" \
"v_mfma_f32_16x16x32_bf16 v[152:155], a[184:187], v[64:67], v[152:155]\n" \
"v_exp_f32_e32 v89, v89\n" \
"v_mfma_f32_16x16x32_bf16 v[156:159], a[188:191], v[64:67], v[156:159]\n" \
"v_exp_f32_e32 v90, v90\n" \
"v_mfma_f32_16x16x32_bf16 a[192:195], a[212:215], v[64:67], a[192:195]\n" \
"v_exp_f32_e32 v91, v91\n" \
"v_mfma_f32_16x16x32_bf16 v[160:163], a[160:163], v[72:75], v[160:163]\n" \
"buffer_load_dwordx4 v51, %[srd], s64 offen offset:0 sc1 lds\n" \
"v_mfma_f32_16x16x32_bf16 v[164:167], a[164:167], v[72:75], v[164:167]\n" \
"v_cvt_pk_bf16_f32 v88, v88, v89\n" \
"v_cvt_pk_bf16_f32 v89, v90, v91\n" \
"v_mfma_f32_16x16x32_bf16 v[168:171], a[168:171], v[72:75], v[168:171]\n" \
"v_exp_f32_e32 v92, v92\n" \
"v_mfma_f32_16x16x32_bf16 v[172:175], a[172:175], v[72:75], v[172:175]\n" \
"v_exp_f32_e32 v93, v93\n" \
"v_mfma_f32_16x16x32_bf16 v[176:179], a[176:179], v[72:75], v[176:179]\n" \
"v_exp_f32_e32 v94, v94\n" \
"v_mfma_f32_16x16x32_bf16 v[180:183], a[180:183], v[72:75], v[180:183]\n" \
"v_exp_f32_e32 v95, v95\n" \
"v_mfma_f32_16x16x32_bf16 v[184:187], a[184:187], v[72:75], v[184:187]\n" \
"buffer_load_dwordx4 v51, %[srd], s64 offen offset:1024 sc1 lds\n" \
"v_mfma_f32_16x16x32_bf16 v[188:191], a[188:191], v[72:75], v[188:191]\n" \
"v_cvt_pk_bf16_f32 v90, v92, v93\n" \
"v_cvt_pk_bf16_f32 v91, v94, v95\n" \
"v_mfma_f32_16x16x32_bf16 a[196:199], a[212:215], v[72:75], a[196:199]\n" \
"ds_read_b32 v60, v18 offset:768\n" \
"ds_read_b32 v61, v18 offset:896\n" \
"v_mfma_f32_16x16x32_bf16 v[192:195], a[160:163], v[80:83], v[192:195]\n" \
"ds_read_b128 a[128:131], v48 offset:0\n" \
"ds_read_b128 a[132:135], v48 offset:1024\n" \
"v_mfma_f32_16x16x32_bf16 v[196:199], a[164:167], v[80:83], v[196:199]\n" \
"ds_read_b128 a[136:139], v48 offset:2048\n" \
"ds_read_b128 a[140:143], v48 offset:3072\n" \
"v_mfma_f32_16x16x32_bf16 v[200:203], a[168:171], v[80:83], v[200:203]\n" \
"buffer_load_dwordx4 v51, %[srd], s64 offen offset:2048 sc1 lds\n" \
"v_mfma_f32_16x16x32_bf16 v[204:207], a[172:175], v[80:83], v[204:207]\n" \
"ds_read_b128 a[144:147], v48 offset:4096\n" \
"ds_read_b128 a[148:151], v48 offset:5120\n" \
"v_mfma_f32_16x16x32_bf16 v[208:211], a[176:179], v[80:83], v[208:211]\n" \
"ds_read_b128 a[152:155], v48 offset:6144\n" \
"ds_read_b128 a[156:159], v48 offset:7168\n" \
"v_mfma_f32_16x16x32_bf16 v[212:215], a[180:183], v[80:83], v[212:215]\n" \
"v_add_u32_e32 v52, 0x14000, v63\n" \
"v_add_u32_e32 v53, 0x20000, v63\n" \
"v_mfma_f32_16x16x32_bf16 v[216:219], a[184:187], v[80:83], v[216:219]\n" \
"v_exp_f32_e32 v104, v104\n" \
"v_mfma_f32_16x16x32_bf16 v[220:223], a[188:191], v[80:83], v[220:223]\n" \
"v_exp_f32_e32 v105, v105\n" \
"v_mfma_f32_16x16x32_bf16 a[200:203], a[212:215], v[80:83], a[200:203]\n" \
"buffer_load_dwordx4 v51, %[srd], s64 offen offset:3072 sc1 lds\n" \
"v_mfma_f32_16x16x32_bf16 v[224:227], a[160:163], v[88:91], v[224:227]\n" \
"v_exp_f32_e32 v106, v106\n" \
"v_mfma_f32_16x16x32_bf16 v[228:231], a[164:167], v[88:91], v[228:231]\n" \
"v_exp_f32_e32 v107, v107\n" \
"v_mfma_f32_16x16x32_bf16 v[232:235], a[168:171], v[88:91], v[232:235]\n" \
"v_cvt_pk_bf16_f32 v104, v104, v105\n" \
"v_cvt_pk_bf16_f32 v105, v106, v107\n" \
"v_mfma_f32_16x16x32_bf16 v[236:239], a[172:175], v[88:91], v[236:239]\n" \
"v_exp_f32_e32 v108, v108\n" \
"v_mfma_f32_16x16x32_bf16 v[240:243], a[176:179], v[88:91], v[240:243]\n" \
"v_exp_f32_e32 v109, v109\n" \
"v_mfma_f32_16x16x32_bf16 v[244:247], a[180:183], v[88:91], v[244:247]\n" \
"v_exp_f32_e32 v110, v110\n" \
"v_mfma_f32_16x16x32_bf16 v[248:251], a[184:187], v[88:91], v[248:251]\n" \
"v_exp_f32_e32 v111, v111\n" \
"v_mfma_f32_16x16x32_bf16 v[252:255], a[188:191], v[88:91], v[252:255]\n" \
"v_cvt_pk_bf16_f32 v106, v108, v109\n" \
"v_cvt_pk_bf16_f32 v107, v110, v111\n" \
"v_mfma_f32_16x16x32_bf16 a[204:207], a[212:215], v[88:91], a[204:207]\n" \
"v_and_b32_e32 v104, v12, v104\n" \
"v_and_b32_e32 v105, v13, v105\n" \
"v_and_b32_e32 v106, v14, v106\n" \
"v_and_b32_e32 v107, v15, v107\n" \
"v_exp_f32_e32 v112, v112\n" \
"v_exp_f32_e32 v113, v113\n" \
"v_exp_f32_e32 v114, v114\n" \
"v_exp_f32_e32 v115, v115\n" \
"v_cvt_pk_bf16_f32 v112, v112, v113\n" \
"v_exp_f32_e32 v116, v116\n" \
"v_exp_f32_e32 v117, v117\n" \
"v_exp_f32_e32 v118, v118\n" \
"v_exp_f32_e32 v119, v119\n" \
"v_cvt_pk_bf16_f32 v113, v114, v115\n" \
"v_cvt_pk_bf16_f32 v114, v116, v117\n" \
"v_cvt_pk_bf16_f32 v115, v118, v119\n" \
"v_exp_f32_e32 v120, v120\n" \
"v_exp_f32_e32 v121, v121\n" \
"v_exp_f32_e32 v122, v122\n" \
"v_exp_f32_e32 v123, v123\n" \
"s_waitcnt vmcnt(10) lgkmcnt(0)\n" \
"s_barrier\n" \
"v_mfma_f32_16x16x32_bf16 v[80:83], a[128:131], a[32:35], v[40:43]\n" \
"ds_read_b128 a[160:163], v49 offset:8192\n" \
"ds_read_b128 a[164:167], v49 offset:9216\n" \
"v_mfma_f32_16x16x32_bf16 v[80:83], a[132:135], a[36:39], v[80:83]\n" \
"ds_read_b128 a[168:171], v49 offset:10240\n" \
"ds_read_b128 a[172:175], v49 offset:11264\n" \
"v_mfma_f32_16x16x32_bf16 v[80:83], a[136:139], a[40:43], v[80:83]\n" \
"s_xor_b32 m0, s65, 0x18000\n" \
"ds_read_b128 a[176:179], v49 offset:12288\n" \
"v_mfma_f32_16x16x32_bf16 v[80:83], a[140:143], a[44:47], v[80:83]\n" \
"buffer_load_dwordx4 v52, %[srd], s64 offen offset:0 sc1 lds\n" \
"v_mfma_f32_16x16x32_bf16 v[88:91], a[140:143], a[60:63], v[44:47]\n" \
"ds_read_b128 a[180:183], v49 offset:13312\n" \
"ds_read_b128 a[184:187], v49 offset:14336\n" \
"v_mfma_f32_16x16x32_bf16 v[88:91], a[136:139], a[56:59], v[88:91]\n" \
"buffer_load_dwordx4 v52, %[srd], s64 offen offset:1024 sc1 lds\n" \
"v_mfma_f32_16x16x32_bf16 v[88:91], a[132:135], a[52:55], v[88:91]\n" \
"ds_read_b128 a[188:191], v49 offset:15360\n" \
"v_readlane_b32 s93, v11, 18\n" \
"v_mfma_f32_16x16x32_bf16 v[88:91], a[128:131], a[48:51], v[88:91]\n" \
"buffer_load_dwordx4 v52, %[srd], s64 offen offset:2048 sc1 lds\n" \
"v_mfma_f32_16x16x32_bf16 v[84:87], a[144:147], a[32:35], v[40:43]\n" \
"v_cvt_pk_bf16_f32 v120, v120, v121\n" \
"v_readlane_b32 s92, v11, 23\n" \
"v_mfma_f32_16x16x32_bf16 v[84:87], a[148:151], a[36:39], v[84:87]\n" \
"v_exp_f32_e32 v124, v124\n" \
"v_mfma_f32_16x16x32_bf16 v[84:87], a[152:155], a[40:43], v[84:87]\n" \
"buffer_load_dwordx4 v52, %[srd], s64 offen offset:3072 sc1 lds\n" \
"v_mfma_f32_16x16x32_bf16 v[84:87], a[156:159], a[44:47], v[84:87]\n" \
"s_mov_b32 m0, s65\n" \
"v_cvt_pk_bf16_f32 v121, v122, v123\n" \
"v_mfma_f32_16x16x32_bf16 v[92:95], a[156:159], a[60:63], v[44:47]\n" \
"v_exp_f32_e32 v125, v125\n" \
"v_mfma_f32_16x16x32_bf16 v[92:95], a[152:155], a[56:59], v[92:95]\n" \
"buffer_load_dwordx4 v53, %[srd], s64 offen offset:0 sc1 lds\n" \
"v_mfma_f32_16x16x32_bf16 v[92:95], a[148:151], a[52:55], v[92:95]\n" \
"v_exp_f32_e32 v126, v126\n" \
"v_mfma_f32_16x16x32_bf16 v[92:95], a[144:147], a[48:51], v[92:95]\n" \
"v_cvt_pk_bf16_f32 v122, v124, v125\n" \
"s_waitcnt lgkmcnt(4)\n" \
"v_mfma_f32_16x16x32_bf16 v[160:163], a[160:163], v[104:107], v[160:163]\n" \
"buffer_load_dwordx4 v53, %[srd], s64 offen offset:1024 sc1 lds\n" \
"v_mfma_f32_16x16x32_bf16 v[164:167], a[164:167], v[104:107], v[164:167]\n" \
"v_exp_f32_e32 v127, v127\n" \
"v_mfma_f32_16x16x32_bf16 v[168:171], a[168:171], v[104:107], v[168:171]\n" \
"v_cvt_pk_bf16_f32 v123, v126, v127\n" \
"ds_read_b128 a[128:131], v48 offset:8192\n" \
"v_mfma_f32_16x16x32_bf16 v[172:175], a[172:175], v[104:107], v[172:175]\n" \
"ds_read_b128 a[132:135], v48 offset:9216\n" \
"s_waitcnt lgkmcnt(3)\n" \
"v_mfma_f32_16x16x32_bf16 v[176:179], a[176:179], v[104:107], v[176:179]\n" \
"buffer_load_dwordx4 v53, %[srd], s64 offen offset:2048 sc1 lds\n" \
"v_mfma_f32_16x16x32_bf16 v[180:183], a[180:183], v[104:107], v[180:183]\n" \
"ds_read_b128 a[136:139], v48 offset:10240\n" \
"ds_read_b128 a[140:143], v48 offset:11264\n" \
"v_mfma_f32_16x16x32_bf16 v[184:187], a[184:187], v[104:107], v[184:187]\n" \
"ds_read_b128 a[144:147], v48 offset:12288\n" \
"s_waitcnt lgkmcnt(5)\n" \
"v_mfma_f32_16x16x32_bf16 v[188:191], a[188:191], v[104:107], v[188:191]\n" \
"buffer_load_dwordx4 v53, %[srd], s64 offen offset:3072 sc1 lds\n" \
"v_mfma_f32_16x16x32_bf16 a[196:199], a[212:215], v[104:107], a[196:199]\n" \
"s_add_u32 m0, s93, 0x0\n" \
"ds_read_b128 a[148:151], v48 offset:13312\n" \
"v_mfma_f32_16x16x32_bf16 v[192:195], a[160:163], v[112:115], v[192:195]\n" \
"ds_read_b128 a[152:155], v48 offset:14336\n" \
"ds_read_b128 a[156:159], v48 offset:15360\n" \
"v_mfma_f32_16x16x32_bf16 v[196:199], a[164:167], v[112:115], v[196:199]\n" \
"buffer_load_dwordx4 v0, s[80:83], s92 offen offset:0 lds\n" \
"v_mfma_f32_16x16x32_bf16 v[200:203], a[168:171], v[112:115], v[200:203]\n" \
"s_xor_b32 s65, s65, 0x8000\n" \
"s_add_u32 s64, s64, 0x10000\n" \
"v_mfma_f32_16x16x32_bf16 v[204:207], a[172:175], v[112:115], v[204:207]\n" \
"v_add_u32_e32 v49, 0x10000, v48\n" \
"v_add_u32_e32 v48, %[k2], v20\n" \
"v_mfma_f32_16x16x32_bf16 v[208:211], a[176:179], v[112:115], v[208:211]\n" \
"v_exp_f32_e32 v80, v80\n" \
"v_mfma_f32_16x16x32_bf16 v[212:215], a[180:183], v[112:115], v[212:215]\n" \
"buffer_load_dwordx4 v1, s[80:83], s92 offen offset:1024 lds\n" \
"v_mfma_f32_16x16x32_bf16 v[216:219], a[184:187], v[112:115], v[216:219]\n" \
"v_exp_f32_e32 v81, v81\n" \
"v_mfma_f32_16x16x32_bf16 v[220:223], a[188:191], v[112:115], v[220:223]\n" \
"v_exp_f32_e32 v82, v82\n" \
"v_mfma_f32_16x16x32_bf16 a[200:203], a[212:215], v[112:115], a[200:203]\n" \
"buffer_load_dwordx4 v2, s[80:83], s92 offen offset:2048 lds\n" \
"v_mfma_f32_16x16x32_bf16 v[224:227], a[160:163], v[120:123], v[224:227]\n" \
"v_exp_f32_e32 v83, v83\n" \
"v_mfma_f32_16x16x32_bf16 v[228:231], a[164:167], v[120:123], v[228:231]\n" \
"v_cvt_pk_bf16_f32 v80, v80, v81\n" \
"L_pf16:\n" \
"s_cbranch_execz L_retA\n" \
"s_nop 0\n" \
"v_cvt_pk_bf16_f32 v81, v82, v83\n" \
"v_mfma_f32_16x16x32_bf16 v[232:235], a[168:171], v[120:123], v[232:235]\n" \
"buffer_load_dwordx4 v3, s[80:83], s92 offen offset:3072 lds\n" \
"v_mfma_f32_16x16x32_bf16 v[236:239], a[172:175], v[120:123], v[236:239]\n" \
"v_exp_f32_e32 v84, v84\n" \
"v_mfma_f32_16x16x32_bf16 v[240:243], a[176:179], v[120:123], v[240:243]\n" \
"v_exp_f32_e32 v85, v85\n" \
"v_mfma_f32_16x16x32_bf16 v[244:247], a[180:183], v[120:123], v[244:247]\n" \
"v_exp_f32_e32 v86, v86\n" \
"v_mfma_f32_16x16x32_bf16 v[248:251], a[184:187], v[120:123], v[248:251]\n" \
"v_exp_f32_e32 v87, v87\n" \
"v_mfma_f32_16x16x32_bf16 v[252:255], a[188:191], v[120:123], v[252:255]\n" \
"v_cvt_pk_bf16_f32 v82, v84, v85\n" \
"v_cvt_pk_bf16_f32 v83, v86, v87\n" \
"v_mfma_f32_16x16x32_bf16 a[204:207], a[212:215], v[120:123], a[204:207]\n" \
"v_and_b32_e32 v80, v12, v80\n" \
"v_and_b32_e32 v81, v13, v81\n" \
"v_and_b32_e32 v82, v14, v82\n" \
"v_and_b32_e32 v83, v15, v83\n" \
"v_exp_f32_e32 v88, v88\n" \
"v_exp_f32_e32 v89, v89\n" \
"v_exp_f32_e32 v90, v90\n" \
"v_exp_f32_e32 v91, v91\n" \
"v_cvt_pk_bf16_f32 v88, v88, v89\n" \
"v_exp_f32_e32 v92, v92\n" \
"v_exp_f32_e32 v93, v93\n" \
"v_exp_f32_e32 v94, v94\n" \
"v_exp_f32_e32 v95, v95\n" \
"v_cvt_pk_bf16_f32 v89, v90, v91\n" \
"v_cvt_pk_bf16_f32 v90, v92, v93\n" \
"v_cvt_pk_bf16_f32 v91, v94, v95\n" \
"v_accvgpr_read_b32 v54, a192\n" \
"v_max_f32_e32 v62, v62, v54\n" \
"v_accvgpr_read_b32 v55, a196\n" \
"v_max_f32_e32 v62, v62, v55\n" \
"v_rcp_f32_e32 v54, v54\n" \
"v_rcp_f32_e32 v55, v55\n" \
"v_mul_f32_e32 v54, v54, v58\n" \
"v_mul_f32_e32 v55, v55, v59\n" \
"v_mul_f32_e32 v128, v54, v128\n" \
"v_mul_f32_e32 v129, v54, v129\n" \
"v_mul_f32_e32 v136, v54, v136\n" \
"v_mul_f32_e32 v137, v54, v137\n" \
"v_mul_f32_e32 v144, v54, v144\n" \
"v_mul_f32_e32 v145, v54, v145\n" \
"v_mul_f32_e32 v152, v54, v152\n" \
"v_mul_f32_e32 v153, v54, v153\n" \
"v_mul_f32_e32 v160, v55, v160\n" \
"v_mul_f32_e32 v161, v55, v161\n" \
"v_mul_f32_e32 v168, v55, v168\n" \
"v_mul_f32_e32 v169, v55, v169\n" \
"v_mul_f32_e32 v176, v55, v176\n" \
"v_mul_f32_e32 v177, v55, v177\n" \
"v_mul_f32_e32 v184, v55, v184\n" \
"v_mul_f32_e32 v185, v55, v185\n" \
"v_cvt_pk_bf16_f32 v128, v128, v129\n" \
"v_mul_f32_e32 v130, v54, v130\n" \
"v_mul_f32_e32 v131, v54, v131\n" \
"v_cvt_pk_bf16_f32 v136, v136, v137\n" \
"v_mul_f32_e32 v138, v54, v138\n" \
"v_mul_f32_e32 v139, v54, v139\n" \
"v_cvt_pk_bf16_f32 v144, v144, v145\n" \
"v_mul_f32_e32 v146, v54, v146\n" \
"v_mul_f32_e32 v147, v54, v147\n" \
"v_cvt_pk_bf16_f32 v152, v152, v153\n" \
"v_mul_f32_e32 v154, v54, v154\n" \
"v_mul_f32_e32 v155, v54, v155\n" \
"v_cvt_pk_bf16_f32 v160, v160, v161\n" \
"v_mul_f32_e32 v162, v55, v162\n" \
"v_mul_f32_e32 v163, v55, v163\n" \
"v_cvt_pk_bf16_f32 v168, v168, v169\n" \
"v_mul_f32_e32 v170, v55, v170\n" \
"v_mul_f32_e32 v171, v55, v171\n" \
"v_cvt_pk_bf16_f32 v176, v176, v177\n" \
"v_mul_f32_e32 v178, v55, v178\n" \
"v_mul_f32_e32 v179, v55, v179\n" \
"v_cvt_pk_bf16_f32 v184, v184, v185\n" \
"v_mul_f32_e32 v186, v55, v186\n" \
"v_mul_f32_e32 v187, v55, v187\n" \
"v_cvt_pk_bf16_f32 v129, v130, v131\n" \
"v_mul_f32_e32 v132, v54, v132\n" \
"v_mul_f32_e32 v133, v54, v133\n" \
"v_mul_f32_e32 v134, v54, v134\n" \
"v_mul_f32_e32 v135, v54, v135\n" \
"v_cvt_pk_bf16_f32 v137, v138, v139\n" \
"v_mul_f32_e32 v140, v54, v140\n" \
"v_mul_f32_e32 v141, v54, v141\n" \
"v_mul_f32_e32 v142, v54, v142\n" \
"v_mul_f32_e32 v143, v54, v143\n" \
"v_cvt_pk_bf16_f32 v145, v146, v147\n" \
"v_mul_f32_e32 v148, v54, v148\n" \
"v_mul_f32_e32 v149, v54, v149\n" \
"v_mul_f32_e32 v150, v54, v150\n" \
"v_mul_f32_e32 v151, v54, v151\n" \
"v_cvt_pk_bf16_f32 v153, v154, v155\n" \
"v_mul_f32_e32 v156, v54, v156\n" \
"v_mul_f32_e32 v157, v54, v157\n" \
"v_mul_f32_e32 v158, v54, v158\n" \
"v_mul_f32_e32 v159, v54, v159\n" \
"v_cvt_pk_bf16_f32 v161, v162, v163\n" \
"v_mul_f32_e32 v164, v55, v164\n" \
"v_mul_f32_e32 v165, v55, v165\n" \
"v_mul_f32_e32 v166, v55, v166\n" \
"v_mul_f32_e32 v167, v55, v167\n" \
"v_cvt_pk_bf16_f32 v169, v170, v171\n" \
"v_mul_f32_e32 v172, v55, v172\n" \
"v_mul_f32_e32 v173, v55, v173\n" \
"s_waitcnt vmcnt(12) lgkmcnt(0)\n" \
"s_barrier\n" \
"v_mfma_f32_16x16x32_bf16 v[120:123], a[128:131], a[48:51], v[44:47]\n" \
"s_xor_b32 m0, s65, 0x12000\n" \
"ds_read_b128 a[160:163], v49 offset:0\n" \
"v_mfma_f32_16x16x32_bf16 v[120:123], a[132:135], a[52:55], v[120:123]\n" \
"buffer_load_dwordx4 v50, %[srd], s64 offen offset:0 sc1 lds\n" \
"v_mfma_f32_16x16x32_bf16 v[120:123], a[136:139], a[56:59], v[120:123]\n" \
"ds_read_b128 a[164:167], v49 offset:1024\n" \
"ds_read_b128 a[168:171], v49 offset:2048\n" \
"v_mfma_f32_16x16x32_bf16 v[120:123], a[140:143], a[60:63], v[120:123]\n" \
"buffer_load_dwordx4 v50, %[srd], s64 offen offset:1024 sc1 lds\n" \
"v_mfma_f32_16x16x32_bf16 v[124:127], a[156:159], a[60:63], v[44:47]\n" \
"ds_read_b128 a[172:175], v49 offset:3072\n" \
"ds_read_b128 a[176:179], v49 offset:4096\n" \
"v_mfma_f32_16x16x32_bf16 v[124:127], a[152:155], a[56:59], v[124:127]\n" \
"ds_read_b128 a[180:183], v49 offset:5120\n" \
"ds_read_b128 a[184:187], v49 offset:6144\n" \
"v_mfma_f32_16x16x32_bf16 v[124:127], a[148:151], a[52:55], v[124:127]\n" \
"ds_read_b128 a[188:191], v49 offset:7168\n" \
"ds_read_b128 a[0:3], v49 offset:8192\n" \
"v_mfma_f32_16x16x32_bf16 v[124:127], a[144:147], a[48:51], v[124:127]\n" \
"buffer_load_dwordx4 v50, %[srd], s64 offen offset:2048 sc1 lds\n" \
"v_mfma_f32_16x16x32_bf16 v[32:35], a[220:223], a[64:67], 0\n" \
"ds_read_b128 a[4:7], v49 offset:9216\n" \
"ds_read_b128 a[8:11], v49 offset:10240\n" \
"v_mfma_f32_16x16x32_bf16 v[36:39], a[220:223], a[80:83], 0\n" \
"ds_read_b128 a[12:15], v49 offset:11264\n" \
"ds_read_b128 a[16:19], v49 offset:12288\n" \
"v_mfma_f32_16x16x32_bf16 v[40:43], a[220:223], a[96:99], 0\n" \
"ds_read_b128 a[20:23], v49 offset:13312\n" \
"ds_read_b128 a[24:27], v49 offset:14336\n" \
"v_mfma_f32_16x16x32_bf16 v[44:47], a[220:223], a[112:115], 0\n" \
"buffer_load_dwordx4 v50, %[srd], s64 offen offset:3072 sc1 lds\n" \
"v_mfma_f32_16x16x32_bf16 v[32:35], a[224:227], a[68:71], v[32:35]\n" \
"s_xor_b32 m0, s65, 0xa000\n" \
"ds_read_b128 a[28:31], v49 offset:15360\n" \
"v_mfma_f32_16x16x32_bf16 v[36:39], a[224:227], a[84:87], v[36:39]\n" \
"v_exp_f32_e32 v120, v120\n" \
"v_mfma_f32_16x16x32_bf16 v[40:43], a[224:227], a[100:103], v[40:43]\n" \
"buffer_load_dwordx4 v51, %[srd], s64 offen offset:0 sc1 lds\n" \
"v_mfma_f32_16x16x32_bf16 v[44:47], a[224:227], a[116:119], v[44:47]\n" \
"v_exp_f32_e32 v121, v121\n" \
"v_mfma_f32_16x16x32_bf16 v[32:35], a[228:231], a[72:75], v[32:35]\n" \
"v_exp_f32_e32 v122, v122\n" \
"v_mfma_f32_16x16x32_bf16 v[36:39], a[228:231], a[88:91], v[36:39]\n" \
"v_exp_f32_e32 v123, v123\n" \
"v_mfma_f32_16x16x32_bf16 v[40:43], a[228:231], a[104:107], v[40:43]\n" \
"buffer_load_dwordx4 v51, %[srd], s64 offen offset:1024 sc1 lds\n" \
"v_mfma_f32_16x16x32_bf16 v[44:47], a[228:231], a[120:123], v[44:47]\n" \
"v_cvt_pk_bf16_f32 v120, v120, v121\n" \
"v_cvt_pk_bf16_f32 v121, v122, v123\n" \
"v_mfma_f32_16x16x32_bf16 v[32:35], a[232:235], a[76:79], v[32:35]\n" \
"v_exp_f32_e32 v124, v124\n" \
"v_mfma_f32_16x16x32_bf16 v[36:39], a[232:235], a[92:95], v[36:39]\n" \
"v_exp_f32_e32 v125, v125\n" \
"v_mfma_f32_16x16x32_bf16 v[40:43], a[232:235], a[108:111], v[40:43]\n" \
"buffer_load_dwordx4 v51, %[srd], s64 offen offset:2048 sc1 lds\n" \
"v_mfma_f32_16x16x32_bf16 v[44:47], a[232:235], a[124:127], v[44:47]\n" \
"v_cvt_pk_bf16_f32 v122, v124, v125\n" \
"s_waitcnt lgkmcnt(3)\n" \
"v_mfma_f32_16x16x32_bf16 v[192:195], a[160:163], v[80:83], v[192:195]\n" \
"v_exp_f32_e32 v126, v126\n" \
"v_mfma_f32_16x16x32_bf16 v[196:199], a[164:167], v[80:83], v[196:199]\n" \
"buffer_load_dwordx4 v51, %[srd], s64 offen offset:3072 sc1 lds\n" \
"v_mfma_f32_16x16x32_bf16 v[200:203], a[168:171], v[80:83], v[200:203]\n" \
"s_add_u32 m0, s93, 0x1000\n" \
"v_and_b32_e32 v120, v12, v120\n" \
"v_mfma_f32_16x16x32_bf16 v[204:207], a[172:175], v[80:83], v[204:207]\n" \
"v_exp_f32_e32 v127, v127\n" \
"v_mfma_f32_16x16x32_bf16 v[208:211], a[176:179], v[80:83], v[208:211]\n" \
"v_cvt_pk_bf16_f32 v123, v126, v127\n" \
"v_and_b32_e32 v121, v13, v121\n" \
"v_mfma_f32_16x16x32_bf16 v[212:215], a[180:183], v[80:83], v[212:215]\n" \
"buffer_load_dwordx4 v4, s[80:83], s92 offen offset:0 lds\n" \
"v_mfma_f32_16x16x32_bf16 v[216:219], a[184:187], v[80:83], v[216:219]\n" \
"v_and_b32_e32 v122, v14, v122\n" \
"v_and_b32_e32 v123, v15, v123\n" \
"v_mfma_f32_16x16x32_bf16 v[220:223], a[188:191], v[80:83], v[220:223]\n" \
"ds_read_b128 a[128:131], v48 offset:0\n" \
"ds_read_b128 a[132:135], v48 offset:1024\n" \
"v_mfma_f32_16x16x32_bf16 a[200:203], a[212:215], v[80:83], a[200:203]\n" \
"ds_read_b128 a[136:139], v48 offset:2048\n" \
"ds_read_b128 a[140:143], v48 offset:3072\n" \
"v_mfma_f32_16x16x32_bf16 v[224:227], a[160:163], v[88:91], v[224:227]\n" \
"buffer_load_dwordx4 v5, s[80:83], s92 offen offset:1024 lds\n" \
"v_mfma_f32_16x16x32_bf16 v[228:231], a[164:167], v[88:91], v[228:231]\n" \
"ds_read_b128 a[144:147], v48 offset:4096\n" \
"ds_read_b128 a[148:151], v48 offset:5120\n" \
"v_mfma_f32_16x16x32_bf16 v[232:235], a[168:171], v[88:91], v[232:235]\n" \
"ds_read_b128 a[152:155], v48 offset:6144\n" \
"ds_read_b128 a[156:159], v48 offset:7168\n" \
"v_mfma_f32_16x16x32_bf16 v[236:239], a[172:175], v[88:91], v[236:239]\n" \
"buffer_load_dwordx4 v6, s[80:83], s92 offen offset:2048 lds\n" \
"v_mfma_f32_16x16x32_bf16 v[240:243], a[176:179], v[88:91], v[240:243]\n" \
"v_mul_f32_e32 v174, v55, v174\n" \
"v_mul_f32_e32 v175, v55, v175\n" \
"v_mfma_f32_16x16x32_bf16 v[244:247], a[180:183], v[88:91], v[244:247]\n" \
"v_cvt_pk_bf16_f32 v177, v178, v179\n" \
"v_mul_f32_e32 v180, v55, v180\n" \
"v_mfma_f32_16x16x32_bf16 v[248:251], a[184:187], v[88:91], v[248:251]\n" \
"v_mul_f32_e32 v181, v55, v181\n" \
"v_mul_f32_e32 v182, v55, v182\n" \
"v_mfma_f32_16x16x32_bf16 v[252:255], a[188:191], v[88:91], v[252:255]\n" \
"buffer_load_dwordx4 v7, s[80:83], s92 offen offset:3072 lds\n" \
"v_mfma_f32_16x16x32_bf16 a[204:207], a[212:215], v[88:91], a[204:207]\n" \
"v_mul_f32_e32 v183, v55, v183\n" \
"v_cvt_pk_bf16_f32 v185, v186, v187\n" \
"v_mfma_f32_16x16x32_bf16 v[224:227], a[0:3], v[120:123], v[224:227]\n" \
"v_mul_f32_e32 v188, v55, v188\n" \
"v_mul_f32_e32 v189, v55, v189\n" \
"v_mfma_f32_16x16x32_bf16 v[228:231], a[4:7], v[120:123], v[228:231]\n" \
"v_mul_f32_e32 v190, v55, v190\n" \
"v_mul_f32_e32 v191, v55, v191\n" \
"v_mfma_f32_16x16x32_bf16 v[232:235], a[8:11], v[120:123], v[232:235]\n" \
"v_cvt_pk_bf16_f32 v130, v132, v133\n" \
"v_cvt_pk_bf16_f32 v131, v134, v135\n" \
"buffer_store_dwordx4 v[128:131], v20, s[56:59], s68 offen offset:0 sc1\n" \
"v_mfma_f32_16x16x32_bf16 v[236:239], a[12:15], v[120:123], v[236:239]\n" \
"v_cvt_pk_bf16_f32 v138, v140, v141\n" \
"v_cvt_pk_bf16_f32 v139, v142, v143\n" \
"buffer_store_dwordx4 v[136:139], v20, s[56:59], s68 offen offset:1024 sc1\n" \
"v_mfma_f32_16x16x32_bf16 v[240:243], a[16:19], v[120:123], v[240:243]\n" \
"v_cvt_pk_bf16_f32 v146, v148, v149\n" \
"s_waitcnt lgkmcnt(4)\n" \
"v_mfma_f32_16x16x32_bf16 v[244:247], a[20:23], v[120:123], v[244:247]\n" \
"v_cvt_pk_bf16_f32 v147, v150, v151\n" \
"v_cvt_pk_bf16_f32 v154, v156, v157\n" \
"buffer_store_dwordx4 v[144:147], v20, s[56:59], s68 offen offset:2048 sc1\n" \
"v_mfma_f32_16x16x32_bf16 v[248:251], a[24:27], v[120:123], v[248:251]\n" \
"v_cvt_pk_bf16_f32 v155, v158, v159\n" \
"v_cvt_pk_bf16_f32 v162, v164, v165\n" \
"buffer_store_dwordx4 v[152:155], v20, s[56:59], s68 offen offset:3072 sc1\n" \
"v_mfma_f32_16x16x32_bf16 v[252:255], a[28:31], v[120:123], v[252:255]\n" \
"v_cvt_pk_bf16_f32 v163, v166, v167\n" \
"v_cvt_pk_bf16_f32 v170, v172, v173\n" \
"buffer_store_dwordx4 v[160:163], v20, s[56:59], s69 offen offset:0 sc1\n" \
"v_mfma_f32_16x16x32_bf16 a[204:207], a[212:215], v[120:123], a[204:207]\n" \
"v_cvt_pk_bf16_f32 v171, v174, v175\n" \
"buffer_store_dwordx4 v[168:171], v20, s[56:59], s69 offen offset:1024 sc1\n" \
"v_cvt_pk_bf16_f32 v178, v180, v181\n" \
"v_cvt_pk_bf16_f32 v179, v182, v183\n" \
"buffer_store_dwordx4 v[176:179], v20, s[56:59], s69 offen offset:2048 sc1\n" \
"v_cvt_pk_bf16_f32 v186, v188, v189\n" \
"v_cvt_pk_bf16_f32 v187, v190, v191\n" \
"buffer_store_dwordx4 v[184:187], v20, s[56:59], s69 offen offset:3072 sc1\n" \
"s_add_u32 m0, s93, 0x2000\n" \
"v_mfma_f32_16x16x32_bf16 a[208:211], a[216:219], v[128:131], a[208:211]\n" \
"buffer_load_dwordx4 v8, s[80:83], s92 offen offset:0 lds\n" \
"v_mfma_f32_16x16x32_bf16 a[208:211], a[216:219], v[136:139], a[208:211]\n" \
"v_mfma_f32_32x32x16_bf16 v[128:143], a[216:219], a[216:219], 0\n" \
"v_mfma_f32_16x16x32_bf16 a[208:211], a[216:219], v[144:147], a[208:211]\n" \
"v_mfma_f32_16x16x32_bf16 a[208:211], a[216:219], v[152:155], a[208:211]\n" \
"v_mfma_f32_32x32x16_bf16 v[144:159], a[216:219], a[216:219], 0\n" \
"v_accvgpr_read_b32 v56, a200\n" \
"v_mfma_f32_16x16x32_bf16 a[208:211], a[216:219], v[160:163], a[208:211]\n" \
"v_max_f32_e32 v62, v62, v56\n" \
"v_rcp_f32_e32 v56, v56\n" \
"v_mfma_f32_16x16x32_bf16 a[208:211], a[216:219], v[168:171], a[208:211]\n" \
"v_mfma_f32_32x32x16_bf16 v[160:175], a[216:219], a[216:219], 0\n" \
"v_mul_f32_e32 v56, v56, v60\n" \
"v_mfma_f32_16x16x32_bf16 a[208:211], a[216:219], v[176:179], a[208:211]\n" \
"v_mul_f32_e32 v192, v56, v192\n" \
"v_mul_f32_e32 v193, v56, v193\n" \
"s_waitcnt vmcnt(21) lgkmcnt(0)\n" \
"s_barrier\n" \
"v_mfma_f32_16x16x32_bf16 v[64:67], a[128:131], a[64:67], v[32:35]\n" \
"s_xor_b32 m0, s65, 0x18000\n" \
"v_cvt_pk_bf16_f32 v192, v192, v193\n" \
"v_mfma_f32_16x16x32_bf16 v[64:67], a[132:135], a[68:71], v[64:67]\n" \
"buffer_load_dwordx4 v52, %[srd], s64 offen offset:0 sc1 lds\n" \
"v_mfma_f32_16x16x32_bf16 v[64:67], a[136:139], a[72:75], v[64:67]\n" \
"v_mul_f32_e32 v194, v56, v194\n" \
"v_mul_f32_e32 v195, v56, v195\n" \
"v_mfma_f32_16x16x32_bf16 v[64:67], a[140:143], a[76:79], v[64:67]\n" \
"buffer_load_dwordx4 v52, %[srd], s64 offen offset:1024 sc1 lds\n" \
"v_mfma_f32_16x16x32_bf16 v[72:75], a[140:143], a[92:95], v[36:39]\n" \
"v_mfma_f32_16x16x32_bf16 a[208:211], a[216:219], v[184:187], a[208:211]\n" \
"v_mfma_f32_32x32x16_bf16 v[176:191], a[216:219], a[216:219], 0\n" \
"v_cvt_pk_bf16_f32 v193, v194, v195\n" \
"v_mul_f32_e32 v196, v56, v196\n" \
"v_mul_f32_e32 v197, v56, v197\n" \
"v_mul_f32_e32 v198, v56, v198\n" \
"v_mul_f32_e32 v199, v56, v199\n" \
"v_mul_f32_e32 v200, v56, v200\n" \
"v_mfma_f32_16x16x32_bf16 v[72:75], a[136:139], a[88:91], v[72:75]\n" \
"v_mul_f32_e32 v201, v56, v201\n" \
"v_cvt_pk_bf16_f32 v194, v196, v197\n" \
"v_mfma_f32_16x16x32_bf16 v[72:75], a[132:135], a[84:87], v[72:75]\n" \
"v_cvt_pk_bf16_f32 v195, v198, v199\n" \
"v_cvt_pk_bf16_f32 v200, v200, v201\n" \
"buffer_store_dwordx4 v[192:195], v20, s[56:59], s70 offen offset:0 sc1\n" \
"v_mfma_f32_16x16x32_bf16 v[72:75], a[128:131], a[80:83], v[72:75]\n" \
"buffer_load_dwordx4 v52, %[srd], s64 offen offset:2048 sc1 lds\n" \
"v_mfma_f32_16x16x32_bf16 v[68:71], a[144:147], a[64:67], v[32:35]\n" \
"v_mul_f32_e32 v202, v56, v202\n" \
"v_mul_f32_e32 v203, v56, v203\n" \
"v_mfma_f32_16x16x32_bf16 v[68:71], a[148:151], a[68:71], v[68:71]\n" \
"v_accvgpr_read_b32 v57, a204\n" \
"v_cvt_pk_bf16_f32 v201, v202, v203\n" \
"v_mfma_f32_32x32x16_bf16 a[192:207], a[216:219], a[216:219], 0\n" \
"v_mfma_f32_16x16x32_bf16 a[208:211], a[216:219], v[192:195], a[208:211]\n" \
"v_mul_f32_e32 v204, v56, v204\n" \
"v_mul_f32_e32 v205, v56, v205\n" \
"v_mfma_f32_16x16x32_bf16 v[68:71], a[152:155], a[72:75], v[68:71]\n" \
"v_mul_f32_e32 v206, v56, v206\n" \
"v_mul_f32_e32 v207, v56, v207\n" \
"v_mfma_f32_16x16x32_bf16 v[68:71], a[156:159], a[76:79], v[68:71]\n" \
"buffer_load_dwordx4 v52, %[srd], s64 offen offset:3072 sc1 lds\n" \
"v_mfma_f32_16x16x32_bf16 v[76:79], a[156:159], a[92:95], v[36:39]\n" \
"s_mov_b32 m0, s65\n" \
"v_mul_f32_e32 v208, v56, v208\n" \
"v_mfma_f32_16x16x32_bf16 v[76:79], a[152:155], a[88:91], v[76:79]\n" \
"v_mul_f32_e32 v209, v56, v209\n" \
"v_max_f32_e32 v62, v62, v57\n" \
"v_mfma_f32_16x16x32_bf16 v[76:79], a[148:151], a[84:87], v[76:79]\n" \
"v_cvt_pk_bf16_f32 v202, v204, v205\n" \
"v_cvt_pk_bf16_f32 v203, v206, v207\n" \
"buffer_store_dwordx4 v[200:203], v20, s[56:59], s70 offen offset:1024 sc1\n" \
"v_mfma_f32_16x16x32_bf16 v[76:79], a[144:147], a[80:83], v[76:79]\n" \
"buffer_load_dwordx4 v53, %[srd], s64 offen offset:0 sc1 lds\n" \
"v_mfma_f32_16x16x32_bf16 v[80:83], a[128:131], a[96:99], v[40:43]\n" \
"v_cvt_pk_bf16_f32 v208, v208, v209\n" \
"v_mul_f32_e32 v210, v56, v210\n" \
"v_mfma_f32_16x16x32_bf16 v[80:83], a[132:135], a[100:103], v[80:83]\n" \
"v_mul_f32_e32 v211, v56, v211\n" \
"v_cvt_pk_bf16_f32 v209, v210, v211\n" \
"v_mfma_f32_16x16x32_bf16 v[80:83], a[136:139], a[104:107], v[80:83]\n" \
"buffer_load_dwordx4 v53, %[srd], s64 offen offset:1024 sc1 lds\n" \
"v_mfma_f32_16x16x32_bf16 v[80:83], a[140:143], a[108:111], v[80:83]\n" \
"v_rcp_f32_e32 v57, v57\n" \
"v_mfma_f32_16x16x32_bf16 v[88:91], a[140:143], a[124:127], v[44:47]\n" \
"ds_read_b128 a[140:143], v48 offset:11264\n" \
"v_mul_f32_e32 v212, v56, v212\n" \
"v_mfma_f32_16x16x32_bf16 v[88:91], a[136:139], a[120:123], v[88:91]\n" \
"ds_read_b128 a[136:139], v48 offset:10240\n" \
"v_mul_f32_e32 v213, v56, v213\n" \
"v_mfma_f32_16x16x32_bf16 v[88:91], a[132:135], a[116:119], v[88:91]\n" \
"buffer_load_dwordx4 v53, %[srd], s64 offen offset:2048 sc1 lds\n" \
"v_mfma_f32_16x16x32_bf16 v[88:91], a[128:131], a[112:115], v[88:91]\n" \
"ds_read_b128 a[128:131], v48 offset:8192\n" \
"ds_read_b128 a[132:135], v48 offset:9216\n" \
"v_mfma_f32_16x16x32_bf16 v[84:87], a[144:147], a[96:99], v[40:43]\n" \
"v_mfma_f32_16x16x32_bf16 a[208:211], a[216:219], v[200:203], a[208:211]\n" \
"v_mfma_f32_32x32x16_bf16 v[192:207], a[216:219], a[216:219], 0\n" \
"v_mul_f32_e32 v214, v56, v214\n" \
"v_mul_f32_e32 v215, v56, v215\n" \
"v_mul_f32_e32 v216, v56, v216\n" \
"v_mul_f32_e32 v217, v56, v217\n" \
"v_cvt_pk_bf16_f32 v210, v212, v213\n" \
"v_cvt_pk_bf16_f32 v211, v214, v215\n" \
"buffer_store_dwordx4 v[208:211], v20, s[56:59], s70 offen offset:2048 sc1\n" \
"v_mfma_f32_16x16x32_bf16 v[84:87], a[148:151], a[100:103], v[84:87]\n" \
"v_cvt_pk_bf16_f32 v216, v216, v217\n" \
"v_mul_f32_e32 v218, v56, v218\n" \
"v_mfma_f32_16x16x32_bf16 v[84:87], a[152:155], a[104:107], v[84:87]\n" \
"v_mul_f32_e32 v219, v56, v219\n" \
"v_mul_f32_e32 v57, v57, v61\n" \
"v_mfma_f32_16x16x32_bf16 v[84:87], a[156:159], a[108:111], v[84:87]\n" \
"v_mfma_f32_16x16x32_bf16 a[208:211], a[216:219], v[208:211], a[208:211]\n" \
"v_cvt_pk_bf16_f32 v217, v218, v219\n" \
"v_mul_f32_e32 v220, v56, v220\n" \
"v_mfma_f32_16x16x32_bf16 v[92:95], a[156:159], a[124:127], v[44:47]\n" \
"ds_read_b128 a[156:159], v48 offset:15360\n" \
"v_mul_f32_e32 v221, v56, v221\n" \
"v_mfma_f32_16x16x32_bf16 v[92:95], a[152:155], a[120:123], v[92:95]\n" \
"ds_read_b128 a[152:155], v48 offset:14336\n" \
"v_mul_f32_e32 v222, v56, v222\n" \
"v_mfma_f32_16x16x32_bf16 v[92:95], a[148:151], a[116:119], v[92:95]\n" \
"ds_read_b128 a[148:151], v48 offset:13312\n" \
"v_mul_f32_e32 v223, v56, v223\n" \
"v_mfma_f32_16x16x32_bf16 v[92:95], a[144:147], a[112:115], v[92:95]\n" \
"ds_read_b128 a[144:147], v48 offset:12288\n" \
"v_mul_f32_e32 v224, v57, v224\n" \
"v_mul_f32_e32 v225, v57, v225\n" \
"v_cvt_pk_bf16_f32 v218, v220, v221\n" \
"v_cvt_pk_bf16_f32 v219, v222, v223\n" \
"buffer_store_dwordx4 v[216:219], v20, s[56:59], s70 offen offset:3072 sc1\n" \
"v_cvt_pk_bf16_f32 v224, v224, v225\n" \
"v_mul_f32_e32 v226, v57, v226\n" \
"v_mul_f32_e32 v227, v57, v227\n" \
"v_mfma_f32_16x16x32_bf16 a[208:211], a[216:219], v[216:219], a[208:211]\n" \
"v_mfma_f32_32x32x16_bf16 v[208:223], a[216:219], a[216:219], 0\n" \
"v_cvt_pk_bf16_f32 v225, v226, v227\n" \
"v_mul_f32_e32 v228, v57, v228\n" \
"v_mul_f32_e32 v229, v57, v229\n" \
"v_mul_f32_e32 v230, v57, v230\n" \
"v_mul_f32_e32 v231, v57, v231\n" \
"v_mul_f32_e32 v232, v57, v232\n" \
"v_mul_f32_e32 v233, v57, v233\n" \
"v_cvt_pk_bf16_f32 v226, v228, v229\n" \
"v_cvt_pk_bf16_f32 v227, v230, v231\n" \
"buffer_store_dwordx4 v[224:227], v20, s[56:59], s71 offen offset:0 sc1\n" \
"v_cvt_pk_bf16_f32 v232, v232, v233\n" \
"v_mul_f32_e32 v234, v57, v234\n" \
"v_mul_f32_e32 v235, v57, v235\n" \
"v_mfma_f32_16x16x32_bf16 a[208:211], a[216:219], v[224:227], a[208:211]\n" \
"v_cvt_pk_bf16_f32 v233, v234, v235\n" \
"v_mul_f32_e32 v236, v57, v236\n" \
"v_mul_f32_e32 v237, v57, v237\n" \
"v_mul_f32_e32 v238, v57, v238\n" \
"v_mul_f32_e32 v239, v57, v239\n" \
"v_mul_f32_e32 v240, v57, v240\n" \
"v_mul_f32_e32 v241, v57, v241\n" \
"v_cvt_pk_bf16_f32 v234, v236, v237\n" \
"v_cvt_pk_bf16_f32 v235, v238, v239\n" \
"buffer_store_dwordx4 v[232:235], v20, s[56:59], s71 offen offset:1024 sc1\n" \
"v_cvt_pk_bf16_f32 v240, v240, v241\n" \
"v_mul_f32_e32 v242, v57, v242\n" \
"v_mul_f32_e32 v243, v57, v243\n" \
"v_mfma_f32_16x16x32_bf16 a[208:211], a[216:219], v[232:235], a[208:211]\n" \
"v_mfma_f32_32x32x16_bf16 v[224:239], a[216:219], a[216:219], 0\n" \
"v_cvt_pk_bf16_f32 v241, v242, v243\n" \
"v_mul_f32_e32 v244, v57, v244\n" \
"v_mul_f32_e32 v245, v57, v245\n" \
"v_mul_f32_e32 v246, v57, v246\n" \
"v_mul_f32_e32 v247, v57, v247\n" \
"v_mul_f32_e32 v248, v57, v248\n" \
"v_mul_f32_e32 v249, v57, v249\n" \
"v_cvt_pk_bf16_f32 v242, v244, v245\n" \
"v_cvt_pk_bf16_f32 v243, v246, v247\n" \
"buffer_store_dwordx4 v[240:243], v20, s[56:59], s71 offen offset:2048 sc1\n" \
"v_cvt_pk_bf16_f32 v248, v248, v249\n" \
"v_mul_f32_e32 v250, v57, v250\n" \
"v_mul_f32_e32 v251, v57, v251\n" \
"v_mfma_f32_16x16x32_bf16 a[208:211], a[216:219], v[240:243], a[208:211]\n" \
"v_cvt_pk_bf16_f32 v249, v250, v251\n" \
"v_mul_f32_e32 v252, v57, v252\n" \
"v_mul_f32_e32 v253, v57, v253\n" \
"v_mul_f32_e32 v254, v57, v254\n" \
"v_mul_f32_e32 v255, v57, v255\n" \
"v_cvt_pk_bf16_f32 v250, v252, v253\n" \
"v_cvt_pk_bf16_f32 v251, v254, v255\n" \
"buffer_store_dwordx4 v[248:251], v20, s[56:59], s71 offen offset:3072 sc1\n" \
"buffer_load_dwordx4 v53, %[srd], s64 offen offset:3072 sc1 lds\n" \
"v_mfma_f32_16x16x32_bf16 a[208:211], a[216:219], v[248:251], a[208:211]\n" \
"v_mfma_f32_32x32x16_bf16 v[240:255], a[216:219], a[216:219], 0\n" \
"v_exp_f32_e32 v64, v64\n" \
"v_exp_f32_e32 v65, v65\n" \
"v_exp_f32_e32 v72, v72\n" \
"v_exp_f32_e32 v73, v73\n" \
"v_cmp_lt_f32_e64 s[72:73], %[lim], v62\n" \
"v_readlane_b32 s95, v11, 3\n" \
"v_exp_f32_e32 v66, v66\n" \
"v_accvgpr_read_b32 v63, a208\n" \
"v_cmp_u_f32_e32 vcc, v63, v63\n" \
"v_cvt_pk_bf16_f32 v64, v64, v65\n" \
"s_waitcnt vmcnt(16) lgkmcnt(0)\n" \
"s_barrier\n" \
"v_exp_f32_e32 v67, v67\n" \
"v_exp_f32_e32 v74, v74\n" \
"v_exp_f32_e32 v75, v75\n" \
"s_or_b64 vcc, s[72:73], vcc\n" \
"s_cselect_b32 s95, 0x40000000, s95\n" \
"s_cselect_b32 s94, 0x7f800000, 0\n" \
"v_exp_f32_e32 v68, v68\n" \
"v_exp_f32_e32 v69, v69\n" \
"v_exp_f32_e32 v70, v70\n" \
"v_exp_f32_e32 v71, v71\n" \
"v_cvt_pk_bf16_f32 v72, v72, v73\n" \
"v_exp_f32_e32 v76, v76\n" \
"v_exp_f32_e32 v77, v77\n" \
"v_exp_f32_e32 v78, v78\n" \
"v_exp_f32_e32 v79, v79\n" \
"v_cvt_pk_bf16_f32 v65, v66, v67\n" \
"v_cvt_pk_bf16_f32 v73, v74, v75\n" \
"v_add_u32_e32 v49, 0x10000, v48\n" \
"s_xor_b32 s65, s65, 0x8000\n" \
"s_add_u32 s64, s64, 0x10000\n" \
"v_cvt_pk_bf16_f32 v66, v68, v69\n" \
"v_cvt_pk_bf16_f32 v67, v70, v71\n" \
"v_cvt_pk_bf16_f32 v74, v76, v77\n" \
"v_cvt_pk_bf16_f32 v75, v78, v79\n" \
"v_xor_b32_e32 v48, 0x8000, v48\n" \
"s_sub_u32 s66, %[n2], 1\n" \
"s_cbranch_scc0 L_2_%=\n" \
"L_f1_%=:\n" \
"L_pf17:\n" \
"s_cbranch_execz L_retA\n" \
"s_nop 0\n" \
"v_bfe_u32 v60, v20, 2, 7\n" \
"v_readlane_b32 s96, v11, 1\n" \
"ds_read_b128 a[160:163], v49 offset:0\n" \
"ds_read_b128 a[164:167], v49 offset:1024\n" \
"ds_read_b128 a[168:171], v49 offset:2048\n" \
"ds_read_b128 a[172:175], v49 offset:3072\n" \
"ds_read_b128 a[176:179], v49 offset:4096\n" \
"buffer_load_dword v57, v60, s[56:59], s96 offen sc1\n" \
"ds_read_b128 a[180:183], v49 offset:5120\n" \
"v_mfma_f32_16x16x32_bf16 v[104:107], a[128:131], a[80:83], v[36:39]\n" \
"s_add_u32 s68, s61, 0x0\n" \
"s_add_u32 s69, s61, 0x49000\n" \
"ds_read_b128 a[184:187], v49 offset:6144\n" \
"v_mfma_f32_16x16x32_bf16 v[104:107], a[132:135], a[84:87], v[104:107]\n" \
"ds_read_b128 a[188:191], v49 offset:7168\n" \
"v_and_b32_e32 v64, v12, v64\n" \
"v_mfma_f32_16x16x32_bf16 v[104:107], a[136:139], a[88:91], v[104:107]\n" \
"v_and_b32_e32 v65, v13, v65\n" \
"v_and_b32_e32 v66, v14, v66\n" \
"v_mfma_f32_16x16x32_bf16 v[104:107], a[140:143], a[92:95], v[104:107]\n" \
"v_and_b32_e32 v67, v15, v67\n" \
"v_mov_b32_e32 v61, s62\n" \
"v_mfma_f32_16x16x32_bf16 v[108:111], a[156:159], a[92:95], v[36:39]\n" \
"v_add_u32_e32 v56, 0x49000, v20\n" \
"ds_read_b128 a[0:3], v20 offset:8192\n" \
"v_mfma_f32_16x16x32_bf16 v[108:111], a[152:155], a[88:91], v[108:111]\n" \
"ds_read_b128 a[4:7], v20 offset:9216\n" \
"ds_read_b128 a[8:11], v20 offset:10240\n" \
"v_mfma_f32_16x16x32_bf16 v[108:111], a[148:151], a[84:87], v[108:111]\n" \
"ds_read_b128 a[12:15], v20 offset:11264\n" \
"ds_read_b128 a[16:19], v20 offset:12288\n" \
"v_mfma_f32_16x16x32_bf16 v[108:111], a[144:147], a[80:83], v[108:111]\n" \
"ds_read_b128 a[20:23], v20 offset:13312\n" \
"ds_read_b128 a[24:27], v20 offset:14336\n" \
"s_waitcnt lgkmcnt(14)\n" \
"v_mfma_f32_16x16x32_bf16 v[128:131], a[160:163], v[64:67], v[128:131]\n" \
"ds_read_b128 a[28:31], v20 offset:15360\n" \
"s_waitcnt lgkmcnt(14)\n" \
"v_mfma_f32_16x16x32_bf16 v[132:135], a[164:167], v[64:67], v[132:135]\n" \
"v_add_u32_e32 v50, %[p4], v48\n" \
"s_waitcnt lgkmcnt(13)\n" \
"v_mfma_f32_16x16x32_bf16 v[136:139], a[168:171], v[64:67], v[136:139]\n" \
"v_xor_b32_e32 v48, 0x4000, v49\n" \
"s_waitcnt lgkmcnt(12)\n" \
"v_mfma_f32_16x16x32_bf16 v[140:143], a[172:175], v[64:67], v[140:143]\n" \
"ds_read_b32 v58, v18 offset:256\n" \
"s_waitcnt lgkmcnt(12)\n" \
"v_mfma_f32_16x16x32_bf16 v[144:147], a[176:179], v[64:67], v[144:147]\n" \
"ds_read_b32 v59, v18 offset:384\n" \
"s_waitcnt lgkmcnt(12)\n" \
"v_mfma_f32_16x16x32_bf16 v[148:151], a[180:183], v[64:67], v[148:151]\n" \
"ds_read_b128 a[32:35], v48 offset:8192\n" \
"s_waitcnt lgkmcnt(12)\n" \
"v_mfma_f32_16x16x32_bf16 v[152:155], a[184:187], v[64:67], v[152:155]\n" \
"ds_read_b128 a[36:39], v48 offset:9216\n" \
"s_waitcnt lgkmcnt(12)\n" \
"v_mfma_f32_16x16x32_bf16 v[156:159], a[188:191], v[64:67], v[156:159]\n" \
"ds_read_b128 a[40:43], v48 offset:10240\n" \
"ds_read_b128 a[44:47], v48 offset:11264\n" \
"s_waitcnt vmcnt(1)\n" \
"s_mov_b64 exec, 1\n" \
"buffer_store_dword v61, off, s[56:59], s95 offset:0 sc1\n" \
"s_mov_b64 exec, -1\n" \
"v_mfma_f32_16x16x32_bf16 a[192:195], a[212:215], v[64:67], a[192:195]\n" \
"ds_read_b128 a[48:51], v48 offset:12288\n" \
"ds_read_b128 a[52:55], v48 offset:13312\n" \
"v_mfma_f32_16x16x32_bf16 v[160:163], a[160:163], v[72:75], v[160:163]\n" \
"ds_read_b128 a[56:59], v48 offset:14336\n" \
"ds_read_b128 a[60:63], v48 offset:15360\n" \
"v_mfma_f32_16x16x32_bf16 v[164:167], a[164:167], v[72:75], v[164:167]\n" \
"ds_read_b128 a[128:131], v49 offset:8192\n" \
"ds_read_b128 a[132:135], v49 offset:9216\n" \
"buffer_load_dwordx4 a[220:223], v20, s[56:59], s60 offen offset:0 sc1\n" \
"v_mfma_f32_16x16x32_bf16 v[168:171], a[168:171], v[72:75], v[168:171]\n" \
"ds_read_b128 a[136:139], v49 offset:10240\n" \
"ds_read_b128 a[140:143], v49 offset:11264\n" \
"v_mfma_f32_16x16x32_bf16 v[172:175], a[172:175], v[72:75], v[172:175]\n" \
"ds_read_b128 a[144:147], v49 offset:12288\n" \
"ds_read_b128 a[148:151], v49 offset:13312\n" \
"v_mfma_f32_16x16x32_bf16 v[176:179], a[176:179], v[72:75], v[176:179]\n" \
"ds_read_b128 a[152:155], v49 offset:14336\n" \
"ds_read_b128 a[156:159], v49 offset:15360\n" \
"v_mfma_f32_16x16x32_bf16 v[180:183], a[180:183], v[72:75], v[180:183]\n" \
"buffer_load_dwordx4 a[236:239], v56, s[56:59], s60 offen offset:0 sc1\n" \
"v_mfma_f32_16x16x32_bf16 v[184:187], a[184:187], v[72:75], v[184:187]\n" \
"v_accvgpr_read_b32 v40, a200\n" \
"v_accvgpr_read_b32 v41, a204\n" \
"v_mfma_f32_16x16x32_bf16 v[188:191], a[188:191], v[72:75], v[188:191]\n" \
"v_add_u32_e32 v51, 0x10000, v50\n" \
"v_add_f32_e32 v40, s94, v40\n" \
"v_mfma_f32_16x16x32_bf16 a[196:199], a[212:215], v[72:75], a[196:199]\n" \
"v_add_f32_e32 v41, s94, v41\n" \
"s_waitcnt lgkmcnt(15)\n" \
"v_mfma_f32_16x16x32_bf16 v[112:115], a[0:3], a[64:67], v[32:35]\n" \
"buffer_load_dwordx4 a[224:227], v20, s[56:59], s60 offen offset:1024 sc1\n" \
"v_mfma_f32_16x16x32_bf16 v[112:115], a[4:7], a[68:71], v[112:115]\n" \
"ds_write_b128 v50, v[192:195] offset:0\n" \
"v_mfma_f32_16x16x32_bf16 v[112:115], a[8:11], a[72:75], v[112:115]\n" \
"ds_write_b128 v50, v[196:199] offset:1024\n" \
"v_mfma_f32_16x16x32_bf16 v[112:115], a[12:15], a[76:79], v[112:115]\n" \
"ds_write_b128 v50, v[200:203] offset:2048\n" \
"v_mfma_f32_16x16x32_bf16 v[120:123], a[12:15], a[92:95], v[36:39]\n" \
"buffer_load_dwordx4 a[240:243], v56, s[56:59], s60 offen offset:1024 sc1\n" \
"v_mfma_f32_16x16x32_bf16 v[120:123], a[8:11], a[88:91], v[120:123]\n" \
"ds_write_b128 v50, v[204:207] offset:3072\n" \
"v_mfma_f32_16x16x32_bf16 v[120:123], a[4:7], a[84:87], v[120:123]\n" \
"ds_write_b128 v50, v[208:211] offset:8192\n" \
"v_mfma_f32_16x16x32_bf16 v[120:123], a[0:3], a[80:83], v[120:123]\n" \
"ds_write_b128 v50, v[212:215] offset:9216\n" \
"v_mfma_f32_16x16x32_bf16 v[116:119], a[16:19], a[64:67], v[32:35]\n" \
"buffer_load_dwordx4 a[228:231], v20, s[56:59], s60 offen offset:2048 sc1\n" \
"v_mfma_f32_16x16x32_bf16 v[116:119], a[20:23], a[68:71], v[116:119]\n" \
"ds_write_b128 v50, v[216:219] offset:10240\n" \
"v_mfma_f32_16x16x32_bf16 v[116:119], a[24:27], a[72:75], v[116:119]\n" \
"ds_write_b128 v50, v[220:223] offset:11264\n" \
"v_mfma_f32_16x16x32_bf16 v[116:119], a[28:31], a[76:79], v[116:119]\n" \
"ds_write_b128 v51, v[224:227] offset:0\n" \
"v_mfma_f32_16x16x32_bf16 v[124:127], a[28:31], a[92:95], v[36:39]\n" \
"buffer_load_dwordx4 a[244:247], v56, s[56:59], s60 offen offset:2048 sc1\n" \
"ds_write_b128 v51, v[228:231] offset:1024\n" \
"ds_write_b128 v51, v[232:235] offset:2048\n" \
"ds_write_b128 v51, v[236:239] offset:3072\n" \
"ds_write_b128 v51, v[240:243] offset:8192\n" \
"ds_write_b128 v51, v[244:247] offset:9216\n" \
"ds_write_b128 v51, v[248:251] offset:10240\n" \
"ds_write_b128 v51, v[252:255] offset:11264\n" \
"buffer_load_dwordx4 a[232:235], v20, s[56:59], s60 offen offset:3072 sc1\n" \
"ds_write_b64 v19, v[40:41]\n" \
"v_mfma_f32_16x16x32_bf16 v[124:127], a[24:27], a[88:91], v[124:127]\n" \
"v_xor_b32_e32 v52, 0x4000, v50\n" \
"v_xor_b32_e32 v53, 0x4000, v51\n" \
"v_mfma_f32_16x16x32_bf16 v[124:127], a[20:23], a[84:87], v[124:127]\n" \
"v_exp_f32_e32 v104, v104\n" \
"v_mfma_f32_16x16x32_bf16 v[124:127], a[16:19], a[80:83], v[124:127]\n" \
"v_exp_f32_e32 v105, v105\n" \
"buffer_load_dwordx4 a[248:251], v56, s[56:59], s60 offen offset:3072 sc1\n" \
"v_exp_f32_e32 v106, v106\n" \
"v_exp_f32_e32 v107, v107\n" \
"v_cvt_pk_bf16_f32 v104, v104, v105\n" \
"L_pf18:\n" \
"s_cbranch_execz L_retA\n" \
"s_nop 0\n" \
"v_exp_f32_e32 v108, v108\n" \
"v_exp_f32_e32 v109, v109\n" \
"v_exp_f32_e32 v110, v110\n" \
"v_exp_f32_e32 v111, v111\n" \
"v_cvt_pk_bf16_f32 v105, v106, v107\n" \
"v_cvt_pk_bf16_f32 v106, v108, v109\n" \
"v_cvt_pk_bf16_f32 v107, v110, v111\n" \
"v_and_b32_e32 v104, v12, v104\n" \
"v_and_b32_e32 v105, v13, v105\n" \
"v_and_b32_e32 v106, v14, v106\n" \
"v_and_b32_e32 v107, v15, v107\n" \
"v_readlane_b32 s93, v11, 19\n" \
"v_readlane_b32 s84, v11, 14\n" \
"v_readlane_b32 s85, v11, 15\n" \
"v_readlane_b32 s86, v11, 16\n" \
"s_mov_b32 s87, 0x20000\n" \
"v_readlane_b32 s92, v11, 17\n" \
"s_waitcnt lgkmcnt(0)\n" \
"s_barrier\n" \
"ds_read_b128 v[192:195], v52 offset:0\n" \
"ds_read_b128 v[196:199], v52 offset:1024\n" \
"ds_read_b128 v[200:203], v52 offset:2048\n" \
"ds_read_b128 v[204:207], v52 offset:3072\n" \
"ds_read_b128 v[208:211], v52 offset:8192\n" \
"ds_read_b128 v[212:215], v52 offset:9216\n" \
"ds_read_b128 v[216:219], v52 offset:10240\n" \
"ds_read_b128 v[220:223], v52 offset:11264\n" \
"ds_read_b128 v[224:227], v53 offset:0\n" \
"ds_read_b128 v[228:231], v53 offset:1024\n" \
"ds_read_b128 v[232:235], v53 offset:2048\n" \
"ds_read_b128 v[236:239], v53 offset:3072\n" \
"ds_read_b128 v[240:243], v53 offset:8192\n" \
"ds_read_b128 v[244:247], v53 offset:9216\n" \
"ds_read_b128 v[248:251], v53 offset:10240\n" \
"ds_read_b128 v[252:255], v53 offset:11264\n" \
"v_mfma_f32_16x16x32_bf16 v[160:163], a[128:131], v[104:107], v[160:163]\n" \
"s_add_u32 m0, s93, 0x0\n" \
"s_waitcnt vmcnt(9)\n" \
"v_cmp_ne_u32_e32 vcc, s62, v57\n" \
"v_mfma_f32_16x16x32_bf16 v[164:167], a[132:135], v[104:107], v[164:167]\n" \
"buffer_load_dwordx4 v0, s[84:87], s92 offen offset:0 lds\n" \
"v_mfma_f32_16x16x32_bf16 v[168:171], a[136:139], v[104:107], v[168:171]\n" \
"v_readlane_b32 s97, v11, 33\n" \
"v_readlane_b32 s98, v11, 34\n" \
"v_mfma_f32_16x16x32_bf16 v[172:175], a[140:143], v[104:107], v[172:175]\n" \
"s_cbranch_vccnz L_so1_%=\n" \
"L_ro1_%=:\n" \
"v_xor_b32_e32 v63, 0x400, v19\n" \
"v_mfma_f32_16x16x32_bf16 v[176:179], a[144:147], v[104:107], v[176:179]\n" \
"ds_read_b64 v[42:43], v63\n" \
"v_mfma_f32_16x16x32_bf16 v[180:183], a[148:151], v[104:107], v[180:183]\n" \
"buffer_load_dwordx4 v1, s[84:87], s92 offen offset:1024 lds\n" \
"v_mfma_f32_16x16x32_bf16 v[184:187], a[152:155], v[104:107], v[184:187]\n" \
"v_exp_f32_e32 v112, v112\n" \
"v_mfma_f32_16x16x32_bf16 v[188:191], a[156:159], v[104:107], v[188:191]\n" \
"v_exp_f32_e32 v113, v113\n" \
"v_mfma_f32_16x16x32_bf16 a[196:199], a[212:215], v[104:107], a[196:199]\n" \
"v_exp_f32_e32 v114, v114\n" \
"v_exp_f32_e32 v115, v115\n" \
"buffer_load_dwordx4 v2, s[84:87], s92 offen offset:2048 lds\n" \
"s_waitcnt lgkmcnt(1)\n" \
"s_barrier\n" \
"v_cvt_pk_bf16_f32 v112, v112, v113\n" \
"v_exp_f32_e32 v116, v116\n" \
"v_exp_f32_e32 v117, v117\n" \
"v_exp_f32_e32 v118, v118\n" \
"v_exp_f32_e32 v119, v119\n" \
"buffer_load_dwordx4 v3, s[84:87], s92 offen offset:3072 lds\n" \
"s_add_u32 m0, s93, 0x1000\n" \
"v_cvt_pk_bf16_f32 v113, v114, v115\n" \
"v_cvt_pk_bf16_f32 v114, v116, v117\n" \
"v_cvt_pk_bf16_f32 v115, v118, v119\n" \
"v_exp_f32_e32 v120, v120\n" \
"v_exp_f32_e32 v121, v121\n" \
"v_mfma_f32_16x16x32_bf16 v[128:131], a[32:35], v[112:115], v[128:131]\n" \
"v_exp_f32_e32 v122, v122\n" \
"v_mfma_f32_16x16x32_bf16 v[132:135], a[36:39], v[112:115], v[132:135]\n" \
"buffer_load_dwordx4 v4, s[84:87], s92 offen offset:0 lds\n" \
"v_mfma_f32_16x16x32_bf16 v[136:139], a[40:43], v[112:115], v[136:139]\n" \
"v_exp_f32_e32 v123, v123\n" \
"v_mfma_f32_16x16x32_bf16 v[140:143], a[44:47], v[112:115], v[140:143]\n" \
"v_cvt_pk_bf16_f32 v120, v120, v121\n" \
"v_cvt_pk_bf16_f32 v121, v122, v123\n" \
"v_mfma_f32_16x16x32_bf16 v[144:147], a[48:51], v[112:115], v[144:147]\n" \
"v_exp_f32_e32 v124, v124\n" \
"v_mfma_f32_16x16x32_bf16 v[148:151], a[52:55], v[112:115], v[148:151]\n" \
"buffer_load_dwordx4 v5, s[84:87], s92 offen offset:1024 lds\n" \
"v_mfma_f32_16x16x32_bf16 v[152:155], a[56:59], v[112:115], v[152:155]\n" \
"v_exp_f32_e32 v125, v125\n" \
"v_mfma_f32_16x16x32_bf16 v[156:159], a[60:63], v[112:115], v[156:159]\n" \
"v_exp_f32_e32 v126, v126\n" \
"v_mfma_f32_16x16x32_bf16 a[192:195], a[212:215], v[112:115], a[192:195]\n" \
"v_exp_f32_e32 v127, v127\n" \
"v_cvt_pk_bf16_f32 v122, v124, v125\n" \
"buffer_load_dwordx4 v6, s[84:87], s92 offen offset:2048 lds\n" \
"v_cvt_pk_bf16_f32 v123, v126, v127\n" \
"v_add_f32_e32 v160, v160, v224\n" \
"v_add_f32_e32 v161, v161, v225\n" \
"v_add_f32_e32 v162, v162, v226\n" \
"v_add_f32_e32 v163, v163, v227\n" \
"v_add_f32_e32 v164, v164, v228\n" \
"v_add_f32_e32 v165, v165, v229\n" \
"v_mfma_f32_16x16x32_bf16 v[160:163], a[32:35], v[120:123], v[160:163]\n" \
"v_add_f32_e32 v166, v166, v230\n" \
"v_add_f32_e32 v167, v167, v231\n" \
"v_add_f32_e32 v168, v168, v232\n" \
"v_add_f32_e32 v169, v169, v233\n" \
"buffer_load_dwordx4 v7, s[84:87], s92 offen offset:3072 lds\n" \
"s_add_u32 m0, s93, 0x2000\n" \
"v_mfma_f32_16x16x32_bf16 v[164:167], a[36:39], v[120:123], v[164:167]\n" \
"v_add_f32_e32 v170, v170, v234\n" \
"v_add_f32_e32 v171, v171, v235\n" \
"v_add_f32_e32 v172, v172, v236\n" \
"v_add_f32_e32 v173, v173, v237\n" \
"v_mfma_f32_16x16x32_bf16 v[168:171], a[40:43], v[120:123], v[168:171]\n" \
"v_add_f32_e32 v174, v174, v238\n" \
"v_add_f32_e32 v175, v175, v239\n" \
"v_accvgpr_read_b32 v54, a192\n" \
"s_waitcnt lgkmcnt(0)\n" \
"v_add_f32_e32 v54, v54, v42\n" \
"buffer_load_dwordx4 v8, s[84:87], s92 offen offset:0 lds\n" \
"v_mfma_f32_16x16x32_bf16 v[172:175], a[44:47], v[120:123], v[172:175]\n" \
"v_max_f32_e32 v62, v62, v54\n" \
"v_add_f32_e32 v176, v176, v240\n" \
"v_add_f32_e32 v177, v177, v241\n" \
"v_add_f32_e32 v178, v178, v242\n" \
"v_add_f32_e32 v179, v179, v243\n" \
"v_rcp_f32_e32 v54, v54\n" \
"v_add_f32_e32 v128, v128, v192\n" \
"v_mfma_f32_16x16x32_bf16 v[176:179], a[48:51], v[120:123], v[176:179]\n" \
"v_add_f32_e32 v129, v129, v193\n" \
"buffer_load_dwordx4 a[96:99], v20, s[56:59], s97 offen offset:0 sc1\n" \
"v_add_f32_e32 v130, v130, v194\n" \
"v_add_f32_e32 v131, v131, v195\n" \
"v_add_f32_e32 v132, v132, v196\n" \
"v_add_f32_e32 v133, v133, v197\n" \
"v_add_f32_e32 v134, v134, v198\n" \
"v_add_f32_e32 v135, v135, v199\n" \
"v_mul_f32_e32 v54, v54, v58\n" \
"v_mul_f32_e32 v128, v54, v128\n" \
"v_mul_f32_e32 v129, v54, v129\n" \
"v_mul_f32_e32 v130, v54, v130\n" \
"v_mul_f32_e32 v131, v54, v131\n" \
"v_mul_f32_e32 v132, v54, v132\n" \
"v_mul_f32_e32 v133, v54, v133\n" \
"buffer_load_dwordx4 a[104:107], v56, s[56:59], s97 offen offset:0 sc1\n" \
"v_mul_f32_e32 v134, v54, v134\n" \
"v_mul_f32_e32 v135, v54, v135\n" \
"v_add_f32_e32 v180, v180, v244\n" \
"v_add_f32_e32 v181, v181, v245\n" \
"v_add_f32_e32 v182, v182, v246\n" \
"v_add_f32_e32 v183, v183, v247\n" \
"v_add_f32_e32 v136, v136, v200\n" \
"v_add_f32_e32 v137, v137, v201\n" \
"v_add_f32_e32 v138, v138, v202\n" \
"v_add_f32_e32 v139, v139, v203\n" \
"v_add_f32_e32 v140, v140, v204\n" \
"v_add_f32_e32 v141, v141, v205\n" \
"v_add_f32_e32 v142, v142, v206\n" \
"buffer_load_dwordx4 a[112:115], v20, s[56:59], s98 offen offset:0 sc1\n" \
"v_add_f32_e32 v143, v143, v207\n" \
"v_cvt_pk_bf16_f32 v64, v128, v129\n" \
"v_cvt_pk_bf16_f32 v65, v130, v131\n" \
"v_cvt_pk_bf16_f32 v66, v132, v133\n" \
"v_cvt_pk_bf16_f32 v67, v134, v135\n" \
"v_mfma_f32_16x16x32_bf16 v[180:183], a[52:55], v[120:123], v[180:183]\n" \
"v_mul_f32_e32 v136, v54, v136\n" \
"v_mfma_f32_16x16x32_bf16 a[208:211], a[216:219], v[64:67], a[208:211]\n" \
"v_mul_f32_e32 v137, v54, v137\n" \
"v_mul_f32_e32 v138, v54, v138\n" \
"buffer_store_dwordx4 v[64:67], v20, s[56:59], s68 offen offset:0 sc1\n" \
"v_mul_f32_e32 v139, v54, v139\n" \
"v_mul_f32_e32 v140, v54, v140\n" \
"v_mul_f32_e32 v141, v54, v141\n" \
"v_mul_f32_e32 v142, v54, v142\n" \
"v_mul_f32_e32 v143, v54, v143\n" \
"v_add_f32_e32 v144, v144, v208\n" \
"v_add_f32_e32 v145, v145, v209\n" \
"v_add_f32_e32 v146, v146, v210\n" \
"v_add_f32_e32 v147, v147, v211\n" \
"v_add_f32_e32 v148, v148, v212\n" \
"v_add_f32_e32 v149, v149, v213\n" \
"v_add_f32_e32 v150, v150, v214\n" \
"v_add_f32_e32 v151, v151, v215\n" \
"buffer_load_dwordx4 a[120:123], v56, s[56:59], s98 offen offset:0 sc1\n" \
"v_cvt_pk_bf16_f32 v68, v136, v137\n" \
"v_cvt_pk_bf16_f32 v69, v138, v139\n" \
"v_cvt_pk_bf16_f32 v70, v140, v141\n" \
"v_cvt_pk_bf16_f32 v71, v142, v143\n" \
"v_mul_f32_e32 v144, v54, v144\n" \
"v_mul_f32_e32 v145, v54, v145\n" \
"v_mfma_f32_16x16x32_bf16 a[208:211], a[216:219], v[68:71], a[208:211]\n" \
"v_mul_f32_e32 v146, v54, v146\n" \
"v_mul_f32_e32 v147, v54, v147\n" \
"v_mul_f32_e32 v148, v54, v148\n" \
"v_mul_f32_e32 v149, v54, v149\n" \
"v_mul_f32_e32 v150, v54, v150\n" \
"buffer_store_dwordx4 v[68:71], v20, s[56:59], s68 offen offset:1024 sc1\n" \
"v_mul_f32_e32 v151, v54, v151\n" \
"v_add_f32_e32 v184, v184, v248\n" \
"v_add_f32_e32 v185, v185, v249\n" \
"v_add_f32_e32 v186, v186, v250\n" \
"v_add_f32_e32 v187, v187, v251\n" \
"v_add_f32_e32 v152, v152, v216\n" \
"v_add_f32_e32 v153, v153, v217\n" \
"v_add_f32_e32 v154, v154, v218\n" \
"v_add_f32_e32 v155, v155, v219\n" \
"v_add_f32_e32 v156, v156, v220\n" \
"v_add_f32_e32 v157, v157, v221\n" \
"v_add_f32_e32 v158, v158, v222\n" \
"v_add_f32_e32 v159, v159, v223\n" \
"buffer_load_dwordx4 a[100:103], v20, s[56:59], s97 offen offset:1024 sc1\n" \
"v_cvt_pk_bf16_f32 v72, v144, v145\n" \
"v_cvt_pk_bf16_f32 v73, v146, v147\n" \
"v_cvt_pk_bf16_f32 v74, v148, v149\n" \
"v_cvt_pk_bf16_f32 v75, v150, v151\n" \
"v_mfma_f32_16x16x32_bf16 v[184:187], a[56:59], v[120:123], v[184:187]\n" \
"v_mul_f32_e32 v152, v54, v152\n" \
"v_mfma_f32_16x16x32_bf16 a[208:211], a[216:219], v[72:75], a[208:211]\n" \
"v_mul_f32_e32 v153, v54, v153\n" \
"v_mul_f32_e32 v154, v54, v154\n" \
"v_mul_f32_e32 v155, v54, v155\n" \
"buffer_store_dwordx4 v[72:75], v20, s[56:59], s68 offen offset:2048 sc1\n" \
"v_mul_f32_e32 v156, v54, v156\n" \
"v_mul_f32_e32 v157, v54, v157\n" \
"v_mul_f32_e32 v158, v54, v158\n" \
"v_mul_f32_e32 v159, v54, v159\n" \
"v_cvt_pk_bf16_f32 v76, v152, v153\n" \
"v_cvt_pk_bf16_f32 v77, v154, v155\n" \
"v_cvt_pk_bf16_f32 v78, v156, v157\n" \
"v_cvt_pk_bf16_f32 v79, v158, v159\n" \
"v_add_f32_e32 v188, v188, v252\n" \
"v_add_f32_e32 v189, v189, v253\n" \
"v_mfma_f32_16x16x32_bf16 a[208:211], a[216:219], v[76:79], a[208:211]\n" \
"v_add_f32_e32 v190, v190, v254\n" \
"buffer_store_dwordx4 v[76:79], v20, s[56:59], s68 offen offset:3072 sc1\n" \
"v_add_f32_e32 v191, v191, v255\n" \
"s_nop 1\n" \
"v_mfma_f32_16x16x32_bf16 v[188:191], a[60:63], v[120:123], v[188:191]\n" \
"v_mfma_f32_16x16x32_bf16 a[196:199], a[212:215], v[120:123], a[196:199]\n" \
"buffer_load_dwordx4 a[108:111], v56, s[56:59], s97 offen offset:1024 sc1\n" \
"buffer_load_dwordx4 a[116:119], v20, s[56:59], s98 offen offset:1024 sc1\n" \
"buffer_load_dwordx4 a[124:127], v56, s[56:59], s98 offen offset:1024 sc1\n" \
"s_nop 4\n" \
"v_accvgpr_read_b32 v55, a196\n" \
"v_add_f32_e32 v55, v55, v43\n" \
"v_max_f32_e32 v62, v62, v55\n" \
"v_rcp_f32_e32 v55, v55\n" \
"v_cmp_lt_f32_e64 s[72:73], %[lim], v62\n" \
"v_mul_f32_e32 v55, v55, v59\n" \
"v_mul_f32_e32 v160, v55, v160\n" \
"v_mul_f32_e32 v161, v55, v161\n" \
"v_mul_f32_e32 v162, v55, v162\n" \
"v_mul_f32_e32 v163, v55, v163\n" \
"v_mul_f32_e32 v164, v55, v164\n" \
"v_mul_f32_e32 v165, v55, v165\n" \
"v_mul_f32_e32 v166, v55, v166\n" \
"v_mul_f32_e32 v167, v55, v167\n" \
"v_cvt_pk_bf16_f32 v80, v160, v161\n" \
"v_cvt_pk_bf16_f32 v81, v162, v163\n" \
"v_cvt_pk_bf16_f32 v82, v164, v165\n" \
"v_cvt_pk_bf16_f32 v83, v166, v167\n" \
"v_mul_f32_e32 v168, v55, v168\n" \
"v_mul_f32_e32 v169, v55, v169\n" \
"v_mfma_f32_16x16x32_bf16 a[208:211], a[216:219], v[80:83], a[208:211]\n" \
"v_mul_f32_e32 v170, v55, v170\n" \
"v_mul_f32_e32 v171, v55, v171\n" \
"v_mul_f32_e32 v172, v55, v172\n" \
"v_mul_f32_e32 v173, v55, v173\n" \
"v_mul_f32_e32 v174, v55, v174\n" \
"v_mul_f32_e32 v175, v55, v175\n" \
"v_cvt_pk_bf16_f32 v84, v168, v169\n" \
"v_cvt_pk_bf16_f32 v85, v170, v171\n" \
"v_cvt_pk_bf16_f32 v86, v172, v173\n" \
"v_cvt_pk_bf16_f32 v87, v174, v175\n" \
"v_mul_f32_e32 v176, v55, v176\n" \
"v_mul_f32_e32 v177, v55, v177\n" \
"v_mfma_f32_16x16x32_bf16 a[208:211], a[216:219], v[84:87], a[208:211]\n" \
"v_mul_f32_e32 v178, v55, v178\n" \
"buffer_store_dwordx4 v[80:83], v20, s[56:59], s69 offen offset:0 sc1\n" \
"v_mul_f32_e32 v179, v55, v179\n" \
"v_mul_f32_e32 v180, v55, v180\n" \
"v_mul_f32_e32 v181, v55, v181\n" \
"v_mul_f32_e32 v182, v55, v182\n" \
"v_mul_f32_e32 v183, v55, v183\n" \
"v_cvt_pk_bf16_f32 v96, v176, v177\n" \
"v_cvt_pk_bf16_f32 v97, v178, v179\n" \
"v_cvt_pk_bf16_f32 v98, v180, v181\n" \
"v_cvt_pk_bf16_f32 v99, v182, v183\n" \
"v_mul_f32_e32 v184, v55, v184\n" \
"v_mul_f32_e32 v185, v55, v185\n" \
"v_mfma_f32_16x16x32_bf16 a[208:211], a[216:219], v[96:99], a[208:211]\n" \
"buffer_store_dwordx4 v[84:87], v20, s[56:59], s69 offen offset:1024 sc1\n" \
"v_mul_f32_e32 v186, v55, v186\n" \
"v_mul_f32_e32 v187, v55, v187\n" \
"v_mul_f32_e32 v188, v55, v188\n" \
"v_mul_f32_e32 v189, v55, v189\n" \
"v_mul_f32_e32 v190, v55, v190\n" \
"v_mul_f32_e32 v191, v55, v191\n" \
"v_cvt_pk_bf16_f32 v100, v184, v185\n" \
"v_cvt_pk_bf16_f32 v101, v186, v187\n" \
"v_cvt_pk_bf16_f32 v102, v188, v189\n" \
"v_cvt_pk_bf16_f32 v103, v190, v191\n" \
"buffer_store_dwordx4 v[96:99], v20, s[56:59], s69 offen offset:2048 sc1\n" \
"buffer_store_dwordx4 v[100:103], v20, s[56:59], s69 offen offset:3072 sc1\n" \
"v_mfma_f32_16x16x32_bf16 a[208:211], a[216:219], v[100:103], a[208:211]\n" \
"s_nop 7\n" \
"v_accvgpr_read_b32 v63, a208\n" \
"v_cmp_u_f32_e32 vcc, v63, v63\n" \
"s_nop 0\n" \
"s_or_b64 vcc, s[72:73], vcc\n" \
"s_cbranch_scc1 L_end_%=\n" \
"L_out_%=:\n" \
"L_pf19:\n" \
"s_cbranch_execz L_retA\n" \
"s_nop 0\n" \
"v_add_u32_e32 v34, 0x20000, v10\n" \
"ds_read_b64_tr_b16 a[0:1], v34 offset:0\n" \
"ds_read_b64_tr_b16 a[2:3], v34 offset:4608\n" \
"ds_read_b64_tr_b16 a[4:5], v34 offset:9216\n" \
"ds_read_b64_tr_b16 a[6:7], v34 offset:13824\n" \
"ds_read_b64_tr_b16 a[8:9], v34 offset:18432\n" \
"ds_read_b64_tr_b16 a[10:11], v34 offset:23040\n" \
"ds_read_b64_tr_b16 a[12:13], v34 offset:32\n" \
"ds_read_b64_tr_b16 a[14:15], v34 offset:4640\n" \
"v_mov_b32_e32 v39, s62\n" \
"v_readlane_b32 s70, v11, 5\n" \
"ds_read_b64_tr_b16 a[16:17], v34 offset:9248\n" \
"ds_read_b64_tr_b16 a[18:19], v34 offset:13856\n" \
"v_bfe_u32 v38, v20, 2, 4\n" \
"v_readlane_b32 s64, v11, 2\n" \
"v_readlane_b32 s71, v11, 4\n" \
"ds_read_b64_tr_b16 a[20:21], v34 offset:18464\n" \
"ds_read_b64_tr_b16 a[22:23], v34 offset:23072\n" \
"ds_read_b64_tr_b16 a[24:25], v34 offset:64\n" \
"ds_read_b64_tr_b16 a[26:27], v34 offset:4672\n" \
"buffer_load_dword v37, v38, s[56:59], s64 offen sc1\n" \
"ds_read_b64_tr_b16 a[28:29], v34 offset:9280\n" \
"ds_read_b64_tr_b16 a[30:31], v34 offset:13888\n" \
"ds_read_b64_tr_b16 a[192:193], v34 offset:18496\n" \
"ds_read_b64_tr_b16 a[194:195], v34 offset:23104\n" \
"ds_read_b64_tr_b16 a[196:197], v34 offset:96\n" \
"ds_read_b64_tr_b16 a[198:199], v34 offset:4704\n" \
"ds_read_b64_tr_b16 a[200:201], v34 offset:9312\n" \
"ds_read_b64_tr_b16 a[202:203], v34 offset:13920\n" \
"ds_read_b64_tr_b16 a[204:205], v34 offset:18528\n" \
"ds_read_b64_tr_b16 a[206:207], v34 offset:23136\n" \
"ds_read_b64_tr_b16 a[208:209], v34 offset:128\n" \
"ds_read_b64_tr_b16 a[210:211], v34 offset:4736\n" \
"ds_read_b64_tr_b16 a[212:213], v34 offset:9344\n" \
"ds_read_b64_tr_b16 a[214:215], v34 offset:13952\n" \
"ds_read_b64_tr_b16 a[216:217], v34 offset:18560\n" \
"ds_read_b64_tr_b16 a[218:219], v34 offset:23168\n" \
"v_add_u32_e32 v33, 0xfc00, v10\n" \
"v_readlane_b32 s99, v11, 20\n" \
"v_readlane_b32 s80, v11, 10\n" \
"v_readlane_b32 s81, v11, 11\n" \
"s_mov_b32 s82, 0x47fca0\n" \
"s_waitcnt vmcnt(26) lgkmcnt(15)\n" \
"v_mfma_f32_16x16x32_bf16 v[112:115], a[0:3], a[220:223], 0\n" \
"s_mov_b32 s83, 0x20000\n" \
"v_readlane_b32 s65, v11, 24\n" \
"v_mfma_f32_16x16x32_bf16 v[112:115], a[4:7], a[224:227], v[112:115]\n" \
"s_add_u32 m0, s99, 0x6c00\n" \
"v_readlane_b32 s66, v11, 27\n" \
"v_mfma_f32_16x16x32_bf16 v[112:115], a[8:11], a[228:231], v[112:115]\n" \
"v_add_u32_e32 v32, 0x49000, v20\n" \
"v_lshrrev_b32_e32 v44, 9, v20\n" \
"v_mfma_f32_16x16x32_bf16 v[116:119], a[8:11], a[244:247], 0\n" \
"buffer_load_dwordx4 v0, s[80:83], s65 offen offset:0 lds\n" \
"v_mfma_f32_16x16x32_bf16 v[116:119], a[4:7], a[240:243], v[116:119]\n" \
"v_mul_u32_u24_e32 v44, 0xb40, v44\n" \
"v_add_u32_e32 v35, v44, v10\n" \
"v_mfma_f32_16x16x32_bf16 v[116:119], a[0:3], a[236:239], v[116:119]\n" \
"v_readlane_b32 s67, v11, 25\n" \
"v_readlane_b32 s92, v11, 21\n" \
"v_mfma_f32_16x16x32_bf16 v[184:187], a[0:3], v[64:67], 0\n" \
"v_readlane_b32 s73, v11, 26\n" \
"v_readlane_b32 s94, v11, 22\n" \
"v_mfma_f32_16x16x32_bf16 v[184:187], a[4:7], v[68:71], v[184:187]\n" \
"v_readlane_b32 s84, v11, 8\n" \
"v_readlane_b32 s85, v11, 9\n" \
"v_mfma_f32_16x16x32_bf16 v[184:187], a[8:11], v[72:75], v[184:187]\n" \
"buffer_load_dwordx4 v1, s[80:83], s65 offen offset:1024 lds\n" \
"v_mfma_f32_16x16x32_bf16 v[188:191], a[8:11], v[96:99], 0\n" \
"ds_read_b64_tr_b16 a[8:9], v34 offset:18592\n" \
"ds_read_b64_tr_b16 a[10:11], v34 offset:23200\n" \
"v_mfma_f32_16x16x32_bf16 v[188:191], a[4:7], v[84:87], v[188:191]\n" \
"ds_read_b64_tr_b16 a[4:5], v34 offset:9376\n" \
"ds_read_b64_tr_b16 a[6:7], v34 offset:13984\n" \
"v_mfma_f32_16x16x32_bf16 v[188:191], a[0:3], v[80:83], v[188:191]\n" \
"ds_read_b64_tr_b16 a[0:1], v34 offset:160\n" \
"ds_read_b64_tr_b16 a[2:3], v34 offset:4768\n" \
"v_mfma_f32_16x16x32_bf16 v[120:123], a[12:15], a[220:223], 0\n" \
"s_mov_b32 s86, 0xa1ca0\n" \
"s_mov_b32 s87, 0x20000\n" \
"v_mfma_f32_16x16x32_bf16 v[120:123], a[16:19], a[224:227], v[120:123]\n" \
"buffer_load_dwordx4 v2, s[80:83], s65 offen offset:2048 lds\n" \
"v_mfma_f32_16x16x32_bf16 v[120:123], a[20:23], a[228:231], v[120:123]\n" \
"v_readlane_b32 s68, v11, 28\n" \
"v_readlane_b32 s69, v11, 29\n" \
"v_mfma_f32_16x16x32_bf16 v[124:127], a[20:23], a[244:247], 0\n" \
"v_readlane_b32 s72, v11, 31\n" \
"v_readlane_b32 s93, v11, 32\n" \
"v_mfma_f32_16x16x32_bf16 v[124:127], a[16:19], a[240:243], v[124:127]\n" \
"v_lshlrev_b32_e32 v41, 12, v20\n" \
"v_readlane_b32 s63, v11, 38\n" \
"v_mfma_f32_16x16x32_bf16 v[124:127], a[12:15], a[236:239], v[124:127]\n" \
"v_bfe_u32 v40, v20, 2, 7\n" \
"s_add_u32 s74, s63, 0x3800\n" \
"v_mfma_f32_16x16x32_bf16 v[192:195], a[12:15], v[64:67], 0\n" \
"buffer_load_dwordx4 v3, s[80:83], s65 offen offset:3072 lds\n" \
"v_mfma_f32_16x16x32_bf16 v[192:195], a[16:19], v[68:71], v[192:195]\n" \
"s_add_u32 m0, s99, 0x7c00\n" \
"v_readlane_b32 s95, v11, 35\n" \
"v_mfma_f32_16x16x32_bf16 v[192:195], a[20:23], v[72:75], v[192:195]\n" \
"v_readlane_b32 s96, v11, 36\n" \
"v_readlane_b32 s97, v11, 37\n" \
"v_mfma_f32_16x16x32_bf16 v[196:199], a[20:23], v[96:99], 0\n" \
"ds_read_b64_tr_b16 a[20:21], v34 offset:18624\n" \
"ds_read_b64_tr_b16 a[22:23], v34 offset:23232\n" \
"v_mfma_f32_16x16x32_bf16 v[196:199], a[16:19], v[84:87], v[196:199]\n" \
"ds_read_b64_tr_b16 a[16:17], v34 offset:9408\n" \
"ds_read_b64_tr_b16 a[18:19], v34 offset:14016\n" \
"v_mfma_f32_16x16x32_bf16 v[196:199], a[12:15], v[80:83], v[196:199]\n" \
"ds_read_b64_tr_b16 a[12:13], v34 offset:192\n" \
"ds_read_b64_tr_b16 a[14:15], v34 offset:4800\n" \
"v_mfma_f32_16x16x32_bf16 v[128:131], a[24:27], a[220:223], 0\n" \
"v_readlane_b32 s100, v11, 7\n" \
"s_waitcnt lgkmcnt(8)\n" \
"v_mfma_f32_16x16x32_bf16 v[128:131], a[28:31], a[224:227], v[128:131]\n" \
"buffer_load_dwordx4 v4, s[80:83], s65 offen offset:0 lds\n" \
"v_mfma_f32_16x16x32_bf16 v[128:131], a[192:195], a[228:231], v[128:131]\n" \
"v_bfe_u32 v44, v20, 4, 4\n" \
"v_bfe_u32 v45, v20, 8, 2\n" \
"v_mfma_f32_16x16x32_bf16 v[132:135], a[192:195], a[244:247], 0\n" \
"v_mul_u32_u24_e32 v42, 0x480, v44\n" \
"v_lshl_add_u32 v42, v45, 3, v42\n" \
"v_mfma_f32_16x16x32_bf16 v[132:135], a[28:31], a[240:243], v[132:135]\n" \
"v_lshrrev_b32_e32 v44, 1, v44\n" \
"v_lshlrev_b32_e32 v45, 1, v45\n" \
"v_mfma_f32_16x16x32_bf16 v[132:135], a[24:27], a[236:239], v[132:135]\n" \
"v_sub_u32_e32 v44, v44, v45\n" \
"v_and_b32_e32 v45, 16, v20\n" \
"v_mfma_f32_16x16x32_bf16 v[200:203], a[24:27], v[64:67], 0\n" \
"buffer_load_dwordx4 v5, s[80:83], s65 offen offset:1024 lds\n" \
"v_mfma_f32_16x16x32_bf16 v[200:203], a[28:31], v[68:71], v[200:203]\n" \
"v_mov_b32_e32 v46, 0x3f80\n" \
"v_lshlrev_b32_e32 v45, v45, v46\n" \
"v_mfma_f32_16x16x32_bf16 v[200:203], a[192:195], v[72:75], v[200:203]\n" \
"v_mov_b32_e32 v52, 0\n" \
"v_mov_b32_e32 v53, 0\n" \
"v_mfma_f32_16x16x32_bf16 v[204:207], a[192:195], v[96:99], 0\n" \
"ds_read_b64_tr_b16 a[192:193], v34 offset:18656\n" \
"ds_read_b64_tr_b16 a[194:195], v34 offset:23264\n" \
"v_mfma_f32_16x16x32_bf16 v[204:207], a[28:31], v[84:87], v[204:207]\n" \
"ds_read_b64_tr_b16 a[28:29], v34 offset:9440\n" \
"ds_read_b64_tr_b16 a[30:31], v34 offset:14048\n" \
"v_mfma_f32_16x16x32_bf16 v[204:207], a[24:27], v[80:83], v[204:207]\n" \
"ds_read_b64_tr_b16 a[24:25], v34 offset:224\n" \
"ds_read_b64_tr_b16 a[26:27], v34 offset:4832\n" \
"v_mfma_f32_16x16x32_bf16 v[136:139], a[196:199], a[220:223], 0\n" \
"buffer_load_dwordx4 v6, s[80:83], s65 offen offset:2048 lds\n" \
"v_mfma_f32_16x16x32_bf16 v[136:139], a[200:203], a[224:227], v[136:139]\n" \
"v_mov_b32_e32 v48, 0\n" \
"v_mov_b32_e32 v49, 0\n" \
"v_mfma_f32_16x16x32_bf16 v[136:139], a[204:207], a[228:231], v[136:139]\n" \
"v_add_u32_e32 v43, 0x9000, v42\n" \
"v_readlane_b32 s88, v11, 12\n" \
"v_mfma_f32_16x16x32_bf16 v[140:143], a[204:207], a[244:247], 0\n" \
"v_readlane_b32 s89, v11, 13\n" \
"s_mov_b32 s90, 0x240000\n" \
"v_mfma_f32_16x16x32_bf16 v[140:143], a[200:203], a[240:243], v[140:143]\n" \
"s_mov_b32 s91, 0x20000\n" \
"v_mov_b32_e32 v46, s62\n" \
"v_mfma_f32_16x16x32_bf16 v[140:143], a[196:199], a[236:239], v[140:143]\n" \
"buffer_load_dwordx4 v7, s[80:83], s65 offen offset:3072 lds\n" \
"v_mfma_f32_16x16x32_bf16 v[208:211], a[196:199], v[64:67], 0\n" \
"s_add_u32 m0, s99, 0x8c00\n" \
"v_mfma_f32_16x16x32_bf16 v[208:211], a[200:203], v[68:71], v[208:211]\n" \
"v_mfma_f32_16x16x32_bf16 v[208:211], a[204:207], v[72:75], v[208:211]\n" \
"v_mfma_f32_16x16x32_bf16 v[212:215], a[204:207], v[96:99], 0\n" \
"ds_read_b64_tr_b16 a[204:205], v34 offset:18688\n" \
"ds_read_b64_tr_b16 a[206:207], v34 offset:23296\n" \
"v_mfma_f32_16x16x32_bf16 v[212:215], a[200:203], v[84:87], v[212:215]\n" \
"ds_read_b64_tr_b16 a[200:201], v34 offset:9472\n" \
"ds_read_b64_tr_b16 a[202:203], v34 offset:14080\n" \
"v_mfma_f32_16x16x32_bf16 v[212:215], a[196:199], v[80:83], v[212:215]\n" \
"ds_read_b64_tr_b16 a[196:197], v34 offset:256\n" \
"ds_read_b64_tr_b16 a[198:199], v34 offset:4864\n" \
"v_mfma_f32_16x16x32_bf16 v[144:147], a[208:211], a[220:223], 0\n" \
"buffer_load_dwordx4 v8, s[80:83], s65 offen offset:0 lds\n" \
"v_mfma_f32_16x16x32_bf16 v[144:147], a[212:215], a[224:227], v[144:147]\n" \
"s_waitcnt vmcnt(9)\n" \
"v_cmp_ne_u32_e32 vcc, s62, v37\n" \
"v_mfma_f32_16x16x32_bf16 v[144:147], a[216:219], a[228:231], v[144:147]\n" \
"s_add_u32 m0, s99, 0x14400\n" \
"v_readlane_b32 s65, v11, 43\n" \
"v_mfma_f32_16x16x32_bf16 v[148:151], a[216:219], a[244:247], 0\n" \
"v_mfma_f32_16x16x32_bf16 v[148:151], a[212:215], a[240:243], v[148:151]\n" \
"s_cbranch_vccnz L_sa_%=\n" \
"L_ra_%=:\n" \
"v_readlane_b32 s64, v11, 30\n" \
"s_mov_b64 exec, 1\n" \
"buffer_store_dword v39, off, s[56:59], s70 offset:0 sc1\n" \
"s_mov_b64 exec, -1\n" \
"v_mfma_f32_16x16x32_bf16 v[148:151], a[208:211], a[236:239], v[148:151]\n" \
"v_readlane_b32 s70, v11, 46\n" \
"v_mfma_f32_16x16x32_bf16 v[216:219], a[208:211], v[64:67], 0\n" \
"v_mfma_f32_16x16x32_bf16 v[216:219], a[212:215], v[68:71], v[216:219]\n" \
"v_mfma_f32_16x16x32_bf16 v[216:219], a[216:219], v[72:75], v[216:219]\n" \
"buffer_load_dwordx4 a[64:67], v20, s[56:59], s66 offen offset:0 sc1\n" \
"v_mfma_f32_16x16x32_bf16 v[220:223], a[216:219], v[96:99], 0\n" \
"v_mfma_f32_16x16x32_bf16 v[220:223], a[212:215], v[84:87], v[220:223]\n" \
"v_mfma_f32_16x16x32_bf16 v[220:223], a[208:211], v[80:83], v[220:223]\n" \
"s_waitcnt lgkmcnt(3)\n" \
"v_mfma_f32_16x16x32_bf16 v[152:155], a[0:3], a[220:223], 0\n" \
"v_mfma_f32_16x16x32_bf16 v[152:155], a[4:7], a[224:227], v[152:155]\n" \
"buffer_load_dwordx4 a[68:71], v20, s[56:59], s66 offen offset:1024 sc1\n" \
"v_mfma_f32_16x16x32_bf16 v[152:155], a[8:11], a[228:231], v[152:155]\n" \
"v_mfma_f32_16x16x32_bf16 v[156:159], a[8:11], a[244:247], 0\n" \
"v_mfma_f32_16x16x32_bf16 v[156:159], a[4:7], a[240:243], v[156:159]\n" \
"v_mfma_f32_16x16x32_bf16 v[156:159], a[0:3], a[236:239], v[156:159]\n" \
"v_mfma_f32_16x16x32_bf16 v[224:227], a[0:3], v[64:67], 0\n" \
"buffer_load_dwordx4 a[84:87], v32, s[56:59], s66 offen offset:1024 sc1\n" \
"v_mfma_f32_16x16x32_bf16 v[224:227], a[4:7], v[68:71], v[224:227]\n" \
"v_mfma_f32_16x16x32_bf16 v[224:227], a[8:11], v[72:75], v[224:227]\n" \
"v_mfma_f32_16x16x32_bf16 v[228:231], a[8:11], v[96:99], 0\n" \
"v_mfma_f32_16x16x32_bf16 v[228:231], a[4:7], v[84:87], v[228:231]\n" \
"v_mfma_f32_16x16x32_bf16 v[228:231], a[0:3], v[80:83], v[228:231]\n" \
"buffer_load_dwordx4 a[80:83], v32, s[56:59], s66 offen offset:0 sc1\n" \
"v_mfma_f32_16x16x32_bf16 v[160:163], a[12:15], a[220:223], 0\n" \
"v_mfma_f32_16x16x32_bf16 v[160:163], a[16:19], a[224:227], v[160:163]\n" \
"v_mfma_f32_16x16x32_bf16 v[160:163], a[20:23], a[228:231], v[160:163]\n" \
"v_mfma_f32_16x16x32_bf16 v[164:167], a[20:23], a[244:247], 0\n" \
"v_mfma_f32_16x16x32_bf16 v[164:167], a[16:19], a[240:243], v[164:167]\n" \
"buffer_load_dwordx4 a[72:75], v20, s[56:59], s66 offen offset:2048 sc1\n" \
"v_mfma_f32_16x16x32_bf16 v[164:167], a[12:15], a[236:239], v[164:167]\n" \
"v_mfma_f32_16x16x32_bf16 v[232:235], a[12:15], v[64:67], 0\n" \
"v_mfma_f32_16x16x32_bf16 v[232:235], a[16:19], v[68:71], v[232:235]\n" \
"v_mfma_f32_16x16x32_bf16 v[232:235], a[20:23], v[72:75], v[232:235]\n" \
"v_mfma_f32_16x16x32_bf16 v[236:239], a[20:23], v[96:99], 0\n" \
"buffer_load_dwordx4 a[76:79], v20, s[56:59], s66 offen offset:3072 sc1\n" \
"v_mfma_f32_16x16x32_bf16 v[236:239], a[16:19], v[84:87], v[236:239]\n" \
"v_mfma_f32_16x16x32_bf16 v[236:239], a[12:15], v[80:83], v[236:239]\n" \
"s_waitcnt lgkmcnt(0)\n" \
"s_barrier\n" \
"v_mfma_f32_16x16x32_bf16 v[168:171], a[24:27], a[220:223], 0\n" \
"ds_read_b64_tr_b16 a[208:209], v33 offset:9216\n" \
"ds_read_b64_tr_b16 a[210:211], v33 offset:13824\n" \
"v_mfma_f32_16x16x32_bf16 v[168:171], a[28:31], a[224:227], v[168:171]\n" \
"ds_read_b64_tr_b16 a[212:213], v33 offset:9248\n" \
"ds_read_b64_tr_b16 a[214:215], v33 offset:13856\n" \
"v_mfma_f32_16x16x32_bf16 v[168:171], a[192:195], a[228:231], v[168:171]\n" \
"ds_read_b64_tr_b16 a[216:217], v33 offset:9280\n" \
"ds_read_b64_tr_b16 a[218:219], v33 offset:13888\n" \
"v_mfma_f32_16x16x32_bf16 v[172:175], a[192:195], a[244:247], 0\n" \
"ds_read_b64_tr_b16 a[0:1], v33 offset:9312\n" \
"ds_read_b64_tr_b16 a[2:3], v33 offset:13920\n" \
"v_mfma_f32_16x16x32_bf16 v[172:175], a[28:31], a[240:243], v[172:175]\n" \
"ds_read_b64_tr_b16 a[4:5], v33 offset:9344\n" \
"ds_read_b64_tr_b16 a[6:7], v33 offset:13952\n" \
"v_mfma_f32_16x16x32_bf16 v[172:175], a[24:27], a[236:239], v[172:175]\n" \
"ds_read_b64_tr_b16 a[8:9], v33 offset:9376\n" \
"ds_read_b64_tr_b16 a[10:11], v33 offset:13984\n" \
"v_mfma_f32_16x16x32_bf16 v[240:243], a[24:27], v[64:67], 0\n" \
"ds_read_b64_tr_b16 a[12:13], v33 offset:9408\n" \
"ds_read_b64_tr_b16 a[14:15], v33 offset:14016\n" \
"v_mfma_f32_16x16x32_bf16 v[240:243], a[28:31], v[68:71], v[240:243]\n" \
"ds_read_b64_tr_b16 a[16:17], v33 offset:9440\n" \
"ds_read_b64_tr_b16 a[18:19], v33 offset:14048\n" \
"v_mfma_f32_16x16x32_bf16 v[240:243], a[192:195], v[72:75], v[240:243]\n" \
"ds_read_b64_tr_b16 a[20:21], v33 offset:9472\n" \
"ds_read_b64_tr_b16 a[22:23], v33 offset:14080\n" \
"v_mfma_f32_16x16x32_bf16 v[244:247], a[192:195], v[96:99], 0\n" \
"ds_read_b64_tr_b16 a[192:193], v35 offset:32\n" \
"ds_read_b64_tr_b16 a[194:195], v35 offset:2336\n" \
"v_mfma_f32_16x16x32_bf16 v[244:247], a[28:31], v[84:87], v[244:247]\n" \
"ds_read_b64_tr_b16 a[28:29], v35 offset:10368\n" \
"ds_read_b64_tr_b16 a[30:31], v35 offset:12672\n" \
"v_mfma_f32_16x16x32_bf16 v[244:247], a[24:27], v[80:83], v[244:247]\n" \
"ds_read_b64_tr_b16 a[24:25], v35 offset:0\n" \
"ds_read_b64_tr_b16 a[26:27], v35 offset:2304\n" \
"v_mfma_f32_16x16x32_bf16 v[176:179], a[196:199], a[220:223], 0\n" \
"buffer_load_dwordx4 a[92:95], v32, s[56:59], s66 offen offset:3072 sc1\n" \
"v_mfma_f32_16x16x32_bf16 v[176:179], a[200:203], a[224:227], v[176:179]\n" \
"v_mfma_f32_16x16x32_bf16 v[176:179], a[204:207], a[228:231], v[176:179]\n" \
"v_mfma_f32_16x16x32_bf16 v[180:183], a[204:207], a[244:247], 0\n" \
"v_mfma_f32_16x16x32_bf16 v[180:183], a[200:203], a[240:243], v[180:183]\n" \
"v_mfma_f32_16x16x32_bf16 v[180:183], a[196:199], a[236:239], v[180:183]\n" \
"buffer_load_dwordx4 a[88:91], v32, s[56:59], s66 offen offset:2048 sc1\n" \
"v_mfma_f32_16x16x32_bf16 v[248:251], a[196:199], v[64:67], 0\n" \
"v_readlane_b32 s66, v11, 44\n" \
"v_mfma_f32_16x16x32_bf16 v[248:251], a[200:203], v[68:71], v[248:251]\n" \
"v_mfma_f32_16x16x32_bf16 v[248:251], a[204:207], v[72:75], v[248:251]\n" \
"v_mfma_f32_16x16x32_bf16 v[252:255], a[204:207], v[96:99], 0\n" \
"ds_read_b64_tr_b16 a[204:205], v35 offset:10432\n" \
"ds_read_b64_tr_b16 a[206:207], v35 offset:12736\n" \
"v_mfma_f32_16x16x32_bf16 v[252:255], a[200:203], v[84:87], v[252:255]\n" \
"ds_read_b64_tr_b16 a[200:201], v35 offset:64\n" \
"ds_read_b64_tr_b16 a[202:203], v35 offset:2368\n" \
"v_mfma_f32_16x16x32_bf16 v[252:255], a[196:199], v[80:83], v[252:255]\n" \
"ds_read_b64_tr_b16 a[196:197], v35 offset:10400\n" \
"s_waitcnt lgkmcnt(9)\n" \
"v_mfma_f32_16x16x32_bf16 v[112:115], a[208:211], a[232:235], v[112:115]\n" \
"buffer_load_dwordx4 v0, s[80:83], s67 offen offset:0 lds\n" \
"v_mfma_f32_16x16x32_bf16 v[116:119], a[208:211], a[248:251], v[116:119]\n" \
"ds_read_b64_tr_b16 a[198:199], v35 offset:12704\n" \
"v_mfma_f32_16x16x32_bf16 v[184:187], a[208:211], v[76:79], v[184:187]\n" \
"v_mfma_f32_16x16x32_bf16 v[188:191], a[208:211], v[100:103], v[188:191]\n" \
"ds_read_b64_tr_b16 a[208:209], v35 offset:96\n" \
"ds_read_b64_tr_b16 a[210:211], v35 offset:2400\n" \
"v_mfma_f32_16x16x32_bf16 v[120:123], a[212:215], a[232:235], v[120:123]\n" \
"v_mfma_f32_16x16x32_bf16 v[124:127], a[212:215], a[248:251], v[124:127]\n" \
"buffer_load_dwordx4 v1, s[80:83], s67 offen offset:1024 lds\n" \
"v_mfma_f32_16x16x32_bf16 v[192:195], a[212:215], v[76:79], v[192:195]\n" \
"L_pf20:\n" \
"s_cbranch_execz L_pf22\n" \
"s_nop 0\n" \
"v_mfma_f32_16x16x32_bf16 v[196:199], a[212:215], v[100:103], v[196:199]\n" \
"ds_read_b64_tr_b16 a[212:213], v35 offset:10464\n" \
"ds_read_b64_tr_b16 a[214:215], v35 offset:12768\n" \
"v_mfma_f32_16x16x32_bf16 v[128:131], a[216:219], a[232:235], v[128:131]\n" \
"v_mfma_f32_16x16x32_bf16 v[132:135], a[216:219], a[248:251], v[132:135]\n" \
"v_mfma_f32_16x16x32_bf16 v[200:203], a[216:219], v[76:79], v[200:203]\n" \
"buffer_load_dwordx4 v2, s[80:83], s67 offen offset:2048 lds\n" \
"v_mfma_f32_16x16x32_bf16 v[204:207], a[216:219], v[100:103], v[204:207]\n" \
"ds_read_b64_tr_b16 a[216:217], v35 offset:128\n" \
"ds_read_b64_tr_b16 a[218:219], v35 offset:2432\n" \
"v_mfma_f32_16x16x32_bf16 v[136:139], a[0:3], a[232:235], v[136:139]\n" \
"v_mfma_f32_16x16x32_bf16 v[140:143], a[0:3], a[248:251], v[140:143]\n" \
"v_mfma_f32_16x16x32_bf16 v[208:211], a[0:3], v[76:79], v[208:211]\n" \
"v_mfma_f32_16x16x32_bf16 v[212:215], a[0:3], v[100:103], v[212:215]\n" \
"ds_read_b64_tr_b16 a[0:1], v35 offset:10496\n" \
"ds_read_b64_tr_b16 a[2:3], v35 offset:12800\n" \
"v_mfma_f32_16x16x32_bf16 v[144:147], a[4:7], a[232:235], v[144:147]\n" \
"buffer_load_dwordx4 v3, s[80:83], s67 offen offset:3072 lds\n" \
"v_mfma_f32_16x16x32_bf16 v[148:151], a[4:7], a[248:251], v[148:151]\n" \
"s_add_u32 m0, s99, 0x15400\n" \
"v_mfma_f32_16x16x32_bf16 v[216:219], a[4:7], v[76:79], v[216:219]\n" \
"v_mfma_f32_16x16x32_bf16 v[220:223], a[4:7], v[100:103], v[220:223]\n" \
"ds_read_b64_tr_b16 a[4:5], v35 offset:160\n" \
"ds_read_b64_tr_b16 a[6:7], v35 offset:2464\n" \
"v_mfma_f32_16x16x32_bf16 v[152:155], a[8:11], a[232:235], v[152:155]\n" \
"v_mfma_f32_16x16x32_bf16 v[156:159], a[8:11], a[248:251], v[156:159]\n" \
"buffer_load_dwordx4 v4, s[80:83], s67 offen offset:0 lds\n" \
"v_mfma_f32_16x16x32_bf16 v[224:227], a[8:11], v[76:79], v[224:227]\n" \
"v_mfma_f32_16x16x32_bf16 v[228:231], a[8:11], v[100:103], v[228:231]\n" \
"ds_read_b64_tr_b16 a[8:9], v35 offset:10528\n" \
"ds_read_b64_tr_b16 a[10:11], v35 offset:12832\n" \
"v_mfma_f32_16x16x32_bf16 v[160:163], a[12:15], a[232:235], v[160:163]\n" \
"v_mfma_f32_16x16x32_bf16 v[164:167], a[12:15], a[248:251], v[164:167]\n" \
"v_mfma_f32_16x16x32_bf16 v[232:235], a[12:15], v[76:79], v[232:235]\n" \
"buffer_load_dwordx4 v5, s[80:83], s67 offen offset:1024 lds\n" \
"v_mfma_f32_16x16x32_bf16 v[236:239], a[12:15], v[100:103], v[236:239]\n" \
"ds_read_b64_tr_b16 a[12:13], v35 offset:192\n" \
"ds_read_b64_tr_b16 a[14:15], v35 offset:2496\n" \
"v_mfma_f32_16x16x32_bf16 v[168:171], a[16:19], a[232:235], v[168:171]\n" \
"v_mfma_f32_16x16x32_bf16 v[172:175], a[16:19], a[248:251], v[172:175]\n" \
"v_mfma_f32_16x16x32_bf16 v[240:243], a[16:19], v[76:79], v[240:243]\n" \
"v_mfma_f32_16x16x32_bf16 v[244:247], a[16:19], v[100:103], v[244:247]\n" \
"buffer_load_dwordx4 v6, s[80:83], s67 offen offset:2048 lds\n" \
"v_mfma_f32_16x16x32_bf16 v[176:179], a[20:23], a[232:235], v[176:179]\n" \
"ds_read_b64_tr_b16 a[16:17], v35 offset:10560\n" \
"ds_read_b64_tr_b16 a[18:19], v35 offset:12864\n" \
"v_mfma_f32_16x16x32_bf16 v[180:183], a[20:23], a[248:251], v[180:183]\n" \
"v_mfma_f32_16x16x32_bf16 v[248:251], a[20:23], v[76:79], v[248:251]\n" \
"v_mfma_f32_16x16x32_bf16 v[252:255], a[20:23], v[100:103], v[252:255]\n" \
"ds_read_b64_tr_b16 a[20:21], v35 offset:224\n" \
"s_waitcnt lgkmcnt(7)\n" \
"v_mfma_f32_16x16x32_bf16 v[112:115], a[24:27], a[96:99], v[112:115]\n" \
"buffer_load_dwordx4 v7, s[80:83], s67 offen offset:3072 lds\n" \
"v_mfma_f32_16x16x32_bf16 v[112:115], a[28:31], a[100:103], v[112:115]\n" \
"s_add_u32 m0, s99, 0x16400\n" \
"ds_read_b64_tr_b16 a[22:23], v35 offset:2528\n" \
"v_mfma_f32_16x16x32_bf16 v[116:119], a[28:31], a[108:111], v[116:119]\n" \
"v_mfma_f32_16x16x32_bf16 v[116:119], a[24:27], a[104:107], v[116:119]\n" \
"v_mfma_f32_16x16x32_bf16 v[184:187], a[24:27], a[112:115], v[184:187]\n" \
"v_mfma_f32_16x16x32_bf16 v[184:187], a[28:31], a[116:119], v[184:187]\n" \
"buffer_load_dword v47, v38, s[56:59], s71 offen sc1\n" \
"v_mfma_f32_16x16x32_bf16 v[188:191], a[28:31], a[124:127], v[188:191]\n" \
"ds_read_b64_tr_b16 a[28:29], v35 offset:256\n" \
"ds_read_b64_tr_b16 a[30:31], v35 offset:2560\n" \
"v_mfma_f32_16x16x32_bf16 v[188:191], a[24:27], a[120:123], v[188:191]\n" \
"ds_read_b64_tr_b16 a[24:25], v35 offset:10592\n" \
"ds_read_b64_tr_b16 a[26:27], v35 offset:12896\n" \
"v_mfma_f32_16x16x32_bf16 v[120:123], a[192:195], a[96:99], v[120:123]\n" \
"v_mfma_f32_16x16x32_bf16 v[120:123], a[196:199], a[100:103], v[120:123]\n" \
"v_mfma_f32_16x16x32_bf16 v[124:127], a[196:199], a[108:111], v[124:127]\n" \
"buffer_load_dwordx4 v8, s[80:83], s67 offen offset:0 lds\n" \
"v_mfma_f32_16x16x32_bf16 v[124:127], a[192:195], a[104:107], v[124:127]\n" \
"s_add_u32 m0, s92, 0x0\n" \
"v_readlane_b32 s67, v11, 45\n" \
"v_mfma_f32_16x16x32_bf16 v[192:195], a[192:195], a[112:115], v[192:195]\n" \
"v_mfma_f32_16x16x32_bf16 v[192:195], a[196:199], a[116:119], v[192:195]\n" \
"v_mfma_f32_16x16x32_bf16 v[196:199], a[196:199], a[124:127], v[196:199]\n" \
"v_mfma_f32_16x16x32_bf16 v[196:199], a[192:195], a[120:123], v[196:199]\n" \
"buffer_load_dwordx4 v0, s[80:83], s73 offen offset:0 lds\n" \
"v_mfma_f32_16x16x32_bf16 v[128:131], a[200:203], a[96:99], v[128:131]\n" \
"ds_read_b64_tr_b16 a[192:193], v35 offset:10624\n" \
"ds_read_b64_tr_b16 a[194:195], v35 offset:12928\n" \
"v_mfma_f32_16x16x32_bf16 v[128:131], a[204:207], a[100:103], v[128:131]\n" \
"v_mfma_f32_16x16x32_bf16 v[132:135], a[204:207], a[108:111], v[132:135]\n" \
"v_mfma_f32_16x16x32_bf16 v[132:135], a[200:203], a[104:107], v[132:135]\n" \
"v_mfma_f32_16x16x32_bf16 v[200:203], a[200:203], a[112:115], v[200:203]\n" \
"buffer_load_dwordx4 v1, s[80:83], s73 offen offset:1024 lds\n" \
"v_mfma_f32_16x16x32_bf16 v[200:203], a[204:207], a[116:119], v[200:203]\n" \
"v_mfma_f32_16x16x32_bf16 v[204:207], a[204:207], a[124:127], v[204:207]\n" \
"v_mfma_f32_16x16x32_bf16 v[204:207], a[200:203], a[120:123], v[204:207]\n" \
"v_mfma_f32_16x16x32_bf16 v[136:139], a[208:211], a[96:99], v[136:139]\n" \
"v_mfma_f32_16x16x32_bf16 v[136:139], a[212:215], a[100:103], v[136:139]\n" \
"buffer_load_dwordx4 v2, s[80:83], s73 offen offset:2048 lds\n" \
"v_mfma_f32_16x16x32_bf16 v[140:143], a[212:215], a[108:111], v[140:143]\n" \
"v_mfma_f32_16x16x32_bf16 v[140:143], a[208:211], a[104:107], v[140:143]\n" \
"v_mfma_f32_16x16x32_bf16 v[208:211], a[208:211], a[112:115], v[208:211]\n" \
"v_mfma_f32_16x16x32_bf16 v[208:211], a[212:215], a[116:119], v[208:211]\n" \
"v_mfma_f32_16x16x32_bf16 v[212:215], a[212:215], a[124:127], v[212:215]\n" \
"buffer_load_dwordx4 v3, s[80:83], s73 offen offset:3072 lds\n" \
"s_add_u32 m0, s92, 0x1000\n" \
"v_mfma_f32_16x16x32_bf16 v[212:215], a[208:211], a[120:123], v[212:215]\n" \
"v_mfma_f32_16x16x32_bf16 v[144:147], a[216:219], a[96:99], v[144:147]\n" \
"v_mfma_f32_16x16x32_bf16 v[144:147], a[0:3], a[100:103], v[144:147]\n" \
"v_mfma_f32_16x16x32_bf16 v[148:151], a[0:3], a[108:111], v[148:151]\n" \
"buffer_load_dwordx4 v4, s[80:83], s73 offen offset:0 lds\n" \
"v_mfma_f32_16x16x32_bf16 v[148:151], a[216:219], a[104:107], v[148:151]\n" \
"v_mfma_f32_16x16x32_bf16 v[216:219], a[216:219], a[112:115], v[216:219]\n" \
"v_mfma_f32_16x16x32_bf16 v[216:219], a[0:3], a[116:119], v[216:219]\n" \
"v_mfma_f32_16x16x32_bf16 v[220:223], a[0:3], a[124:127], v[220:223]\n" \
"v_mfma_f32_16x16x32_bf16 v[220:223], a[216:219], a[120:123], v[220:223]\n" \
"buffer_load_dwordx4 v5, s[80:83], s73 offen offset:1024 lds\n" \
"v_mfma_f32_16x16x32_bf16 v[152:155], a[4:7], a[96:99], v[152:155]\n" \
"s_waitcnt lgkmcnt(0)\n" \
"v_mfma_f32_16x16x32_bf16 v[152:155], a[8:11], a[100:103], v[152:155]\n" \
"v_mfma_f32_16x16x32_bf16 v[156:159], a[8:11], a[108:111], v[156:159]\n" \
"v_mfma_f32_16x16x32_bf16 v[156:159], a[4:7], a[104:107], v[156:159]\n" \
"v_mfma_f32_16x16x32_bf16 v[224:227], a[4:7], a[112:115], v[224:227]\n" \
"buffer_load_dwordx4 v6, s[80:83], s73 offen offset:2048 lds\n" \
"v_mfma_f32_16x16x32_bf16 v[224:227], a[8:11], a[116:119], v[224:227]\n" \
"v_mfma_f32_16x16x32_bf16 v[228:231], a[8:11], a[124:127], v[228:231]\n" \
"v_mfma_f32_16x16x32_bf16 v[228:231], a[4:7], a[120:123], v[228:231]\n" \
"s_waitcnt vmcnt(22)\n" \
"s_barrier\n" \
"buffer_load_dwordx4 v7, s[80:83], s73 offen offset:3072 lds\n" \
"s_add_u32 m0, s92, 0x2000\n" \
"v_mfma_f32_16x16x32_bf16 v[160:163], a[12:15], a[96:99], v[160:163]\n" \
"ds_read_b64_tr_b16 a[196:197], v10 offset:27648\n" \
"ds_read_b64_tr_b16 a[198:199], v10 offset:32256\n" \
"v_mfma_f32_16x16x32_bf16 v[160:163], a[16:19], a[100:103], v[160:163]\n" \
"ds_read_b64_tr_b16 a[200:201], v10 offset:36864\n" \
"ds_read_b64_tr_b16 a[202:203], v10 offset:41472\n" \
"v_mfma_f32_16x16x32_bf16 v[164:167], a[16:19], a[108:111], v[164:167]\n" \
"ds_read_b64_tr_b16 a[204:205], v10 offset:27680\n" \
"ds_read_b64_tr_b16 a[206:207], v10 offset:32288\n" \
"v_mfma_f32_16x16x32_bf16 v[164:167], a[12:15], a[104:107], v[164:167]\n" \
"buffer_load_dwordx4 v8, s[80:83], s73 offen offset:0 lds\n" \
"s_mov_b32 m0, s94\n" \
"v_mfma_f32_16x16x32_bf16 v[232:235], a[12:15], a[112:115], v[232:235]\n" \
"ds_read_b64_tr_b16 a[208:209], v10 offset:36896\n" \
"ds_read_b64_tr_b16 a[210:211], v10 offset:41504\n" \
"v_mfma_f32_16x16x32_bf16 v[232:235], a[16:19], a[116:119], v[232:235]\n" \
"ds_read_b64_tr_b16 a[212:213], v10 offset:27712\n" \
"ds_read_b64_tr_b16 a[214:215], v10 offset:32320\n" \
"v_mfma_f32_16x16x32_bf16 v[236:239], a[16:19], a[124:127], v[236:239]\n" \
"ds_read_b64_tr_b16 a[216:217], v10 offset:36928\n" \
"ds_read_b64_tr_b16 a[218:219], v10 offset:41536\n" \
"v_mfma_f32_16x16x32_bf16 v[236:239], a[12:15], a[120:123], v[236:239]\n" \
"buffer_load_dwordx4 v9, s[84:87], 0 offen lds\n" \
"v_mfma_f32_16x16x32_bf16 v[168:171], a[20:23], a[96:99], v[168:171]\n" \
"ds_read_b64_tr_b16 a[0:1], v10 offset:27744\n" \
"ds_read_b64_tr_b16 a[2:3], v10 offset:32352\n" \
"v_mfma_f32_16x16x32_bf16 v[168:171], a[24:27], a[100:103], v[168:171]\n" \
"ds_read_b64_tr_b16 a[4:5], v10 offset:36960\n" \
"ds_read_b64_tr_b16 a[6:7], v10 offset:41568\n" \
"v_mfma_f32_16x16x32_bf16 v[172:175], a[24:27], a[108:111], v[172:175]\n" \
"ds_read_b64_tr_b16 a[8:9], v10 offset:27776\n" \
"ds_read_b64_tr_b16 a[10:11], v10 offset:32384\n" \
"v_mfma_f32_16x16x32_bf16 v[172:175], a[20:23], a[104:107], v[172:175]\n" \
"ds_read_b64_tr_b16 a[12:13], v10 offset:36992\n" \
"ds_read_b64_tr_b16 a[14:15], v10 offset:41600\n" \
"buffer_load_dwordx4 a[128:131], v20, s[56:59], s68 offen offset:0 sc1\n" \
"v_mfma_f32_16x16x32_bf16 v[240:243], a[20:23], a[112:115], v[240:243]\n" \
"ds_read_b64_tr_b16 a[16:17], v10 offset:27808\n" \
"ds_read_b64_tr_b16 a[18:19], v10 offset:32416\n" \
"v_mfma_f32_16x16x32_bf16 v[240:243], a[24:27], a[116:119], v[240:243]\n" \
"s_waitcnt vmcnt(12)\n" \
"v_cmp_ne_u32_e32 vcc, s62, v47\n" \
"v_mfma_f32_16x16x32_bf16 v[244:247], a[24:27], a[124:127], v[244:247]\n" \
"ds_read_b64_tr_b16 a[24:25], v10 offset:27840\n" \
"ds_read_b64_tr_b16 a[26:27], v10 offset:32448\n" \
"v_mfma_f32_16x16x32_bf16 v[244:247], a[20:23], a[120:123], v[244:247]\n" \
"ds_read_b64_tr_b16 a[20:21], v10 offset:37024\n" \
"ds_read_b64_tr_b16 a[22:23], v10 offset:41632\n" \
"buffer_load_dwordx4 a[132:135], v20, s[56:59], s68 offen offset:1024 sc1\n" \
"v_mfma_f32_16x16x32_bf16 v[176:179], a[28:31], a[96:99], v[176:179]\n" \
"s_cbranch_vccnz L_sb_%=\n" \
"L_rb_%=:\n" \
"v_readlane_b32 s71, v11, 47\n" \
"v_mfma_f32_16x16x32_bf16 v[176:179], a[192:195], a[100:103], v[176:179]\n" \
"v_readlane_b32 s73, v11, 49\n" \
"v_readlane_b32 s92, v11, 43\n" \
"v_mfma_f32_16x16x32_bf16 v[180:183], a[192:195], a[108:111], v[180:183]\n" \
"v_readlane_b32 s94, v11, 45\n" \
"v_cmp_eq_u32_e32 vcc, 0, v44\n" \
"v_mfma_f32_16x16x32_bf16 v[180:183], a[28:31], a[104:107], v[180:183]\n" \
"v_mfma_f32_16x16x32_bf16 v[248:251], a[28:31], a[112:115], v[248:251]\n" \
"buffer_load_dwordx4 a[136:139], v20, s[56:59], s68 offen offset:2048 sc1\n" \
"v_mfma_f32_16x16x32_bf16 v[248:251], a[192:195], a[116:119], v[248:251]\n" \
"v_cndmask_b32_e32 v50, 0, v45, vcc\n" \
"v_cmp_eq_u32_e32 vcc, 1, v44\n" \
"v_mfma_f32_16x16x32_bf16 v[252:255], a[192:195], a[124:127], v[252:255]\n" \
"ds_read_b64_tr_b16 a[192:193], v10 offset:27872\n" \
"ds_read_b64_tr_b16 a[194:195], v10 offset:32480\n" \
"v_mfma_f32_16x16x32_bf16 v[252:255], a[28:31], a[120:123], v[252:255]\n" \
"ds_read_b64_tr_b16 a[28:29], v10 offset:37056\n" \
"s_waitcnt lgkmcnt(11)\n" \
"v_mfma_f32_16x16x32_bf16 v[112:115], a[196:199], a[64:67], v[112:115]\n" \
"ds_read_b64_tr_b16 a[30:31], v10 offset:41664\n" \
"v_cndmask_b32_e32 v51, 0, v45, vcc\n" \
"buffer_load_dwordx4 a[140:143], v20, s[56:59], s68 offen offset:3072 sc1\n" \
"v_mfma_f32_16x16x32_bf16 v[112:115], a[200:203], a[68:71], v[112:115]\n" \
"v_bfe_u32 v45, v20, 1, 5\n" \
"v_and_b32_e32 v45, 24, v45\n" \
"v_mfma_f32_16x16x32_bf16 v[116:119], a[200:203], a[84:87], v[116:119]\n" \
"ds_read_b64_tr_b16 a[200:201], v10 offset:27904\n" \
"ds_read_b64_tr_b16 a[202:203], v10 offset:32512\n" \
"v_mfma_f32_16x16x32_bf16 v[116:119], a[196:199], a[80:83], v[116:119]\n" \
"ds_read_b64_tr_b16 a[196:197], v10 offset:37088\n" \
"ds_read_b64_tr_b16 a[198:199], v10 offset:41696\n" \
"v_mfma_f32_16x16x32_bf16 v[120:123], a[204:207], a[64:67], v[120:123]\n" \
"v_and_b32_e32 v44, 0x3c0, v20\n" \
"v_add_u32_e32 v45, 0x1d400, v45\n" \
"buffer_load_dwordx4 a[156:159], v32, s[56:59], s68 offen offset:3072 sc1\n" \
"v_mfma_f32_16x16x32_bf16 v[120:123], a[208:211], a[68:71], v[120:123]\n" \
"v_add_u32_e32 v36, v44, v45\n" \
"v_mfma_f32_16x16x32_bf16 v[124:127], a[208:211], a[84:87], v[124:127]\n" \
"ds_read_b64_tr_b16 a[208:209], v10 offset:46080\n" \
"ds_read_b64_tr_b16 a[210:211], v10 offset:50688\n" \
"v_mfma_f32_16x16x32_bf16 v[124:127], a[204:207], a[80:83], v[124:127]\n" \
"ds_read_b64_tr_b16 a[204:205], v10 offset:37120\n" \
"ds_read_b64_tr_b16 a[206:207], v10 offset:41728\n" \
"v_mfma_f32_16x16x32_bf16 v[128:131], a[212:215], a[64:67], v[128:131]\n" \
"v_mfma_f32_16x16x32_bf16 v[128:131], a[216:219], a[68:71], v[128:131]\n" \
"buffer_load_dwordx4 a[152:155], v32, s[56:59], s68 offen offset:2048 sc1\n" \
"v_mfma_f32_16x16x32_bf16 v[132:135], a[216:219], a[84:87], v[132:135]\n" \
"ds_read_b64_tr_b16 a[216:217], v10 offset:46112\n" \
"ds_read_b64_tr_b16 a[218:219], v10 offset:50720\n" \
"v_mfma_f32_16x16x32_bf16 v[132:135], a[212:215], a[80:83], v[132:135]\n" \
"ds_read_b64_tr_b16 a[212:213], v10 offset:55296\n" \
"ds_read_b64_tr_b16 a[214:215], v10 offset:59904\n" \
"v_mfma_f32_16x16x32_bf16 v[136:139], a[0:3], a[64:67], v[136:139]\n" \
"v_mfma_f32_16x16x32_bf16 v[136:139], a[4:7], a[68:71], v[136:139]\n" \
"v_mfma_f32_16x16x32_bf16 v[140:143], a[4:7], a[84:87], v[140:143]\n" \
"buffer_load_dwordx4 a[148:151], v32, s[56:59], s68 offen offset:1024 sc1\n" \
"v_mfma_f32_16x16x32_bf16 v[140:143], a[0:3], a[80:83], v[140:143]\n" \
"ds_read_b64_tr_b16 a[0:1], v10 offset:55328\n" \
"ds_read_b64_tr_b16 a[2:3], v10 offset:59936\n" \
"v_mfma_f32_16x16x32_bf16 v[144:147], a[8:11], a[64:67], v[144:147]\n" \
"ds_read_b64_tr_b16 a[4:5], v10 offset:46144\n" \
"s_waitcnt lgkmcnt(13)\n" \
"v_mfma_f32_16x16x32_bf16 v[144:147], a[12:15], a[68:71], v[144:147]\n" \
"ds_read_b64_tr_b16 a[6:7], v10 offset:50752\n" \
"v_mfma_f32_16x16x32_bf16 v[148:151], a[12:15], a[84:87], v[148:151]\n" \
"ds_read_b64_tr_b16 a[12:13], v10 offset:46176\n" \
"ds_read_b64_tr_b16 a[14:15], v10 offset:50784\n" \
"buffer_load_dwordx4 a[144:147], v32, s[56:59], s68 offen offset:0 sc1\n" \
"v_mfma_f32_16x16x32_bf16 v[148:151], a[8:11], a[80:83], v[148:151]\n" \
"ds_read_b64_tr_b16 a[8:9], v10 offset:55360\n" \
"ds_read_b64_tr_b16 a[10:11], v10 offset:59968\n" \
"v_mfma_f32_16x16x32_bf16 v[152:155], a[16:19], a[64:67], v[152:155]\n" \
"v_readlane_b32 s68, v11, 50\n" \
"v_mfma_f32_16x16x32_bf16 v[152:155], a[20:23], a[68:71], v[152:155]\n" \
"v_mfma_f32_16x16x32_bf16 v[156:159], a[20:23], a[84:87], v[156:159]\n" \
"ds_read_b64_tr_b16 a[20:21], v10 offset:46208\n" \
"ds_read_b64_tr_b16 a[22:23], v10 offset:50816\n" \
"buffer_load_dwordx4 a[32:35], v20, s[56:59], s69 offen offset:0 sc1\n" \
"v_mfma_f32_16x16x32_bf16 v[156:159], a[16:19], a[80:83], v[156:159]\n" \
"ds_read_b64_tr_b16 a[16:17], v10 offset:55392\n" \
"ds_read_b64_tr_b16 a[18:19], v10 offset:60000\n" \
"v_mfma_f32_16x16x32_bf16 v[160:163], a[24:27], a[64:67], v[160:163]\n" \
"v_mfma_f32_16x16x32_bf16 v[160:163], a[28:31], a[68:71], v[160:163]\n" \
"v_mfma_f32_16x16x32_bf16 v[164:167], a[28:31], a[84:87], v[164:167]\n" \
"ds_read_b64_tr_b16 a[28:29], v10 offset:46240\n" \
"ds_read_b64_tr_b16 a[30:31], v10 offset:50848\n" \
"buffer_load_dwordx4 a[36:39], v20, s[56:59], s69 offen offset:1024 sc1\n" \
"v_mfma_f32_16x16x32_bf16 v[164:167], a[24:27], a[80:83], v[164:167]\n" \
"ds_read_b64_tr_b16 a[24:25], v10 offset:55424\n" \
"ds_read_b64_tr_b16 a[26:27], v10 offset:60032\n" \
"v_mfma_f32_16x16x32_bf16 v[168:171], a[192:195], a[64:67], v[168:171]\n" \
"s_waitcnt lgkmcnt(14)\n" \
"v_mfma_f32_16x16x32_bf16 v[168:171], a[196:199], a[68:71], v[168:171]\n" \
"v_mfma_f32_16x16x32_bf16 v[172:175], a[196:199], a[84:87], v[172:175]\n" \
"ds_read_b64_tr_b16 a[196:197], v10 offset:46272\n" \
"ds_read_b64_tr_b16 a[198:199], v10 offset:50880\n" \
"buffer_load_dwordx4 a[40:43], v20, s[56:59], s69 offen offset:2048 sc1\n" \
"v_mfma_f32_16x16x32_bf16 v[172:175], a[192:195], a[80:83], v[172:175]\n" \
"ds_read_b64_tr_b16 a[192:193], v10 offset:55456\n" \
"ds_read_b64_tr_b16 a[194:195], v10 offset:60064\n" \
"v_mfma_f32_16x16x32_bf16 v[176:179], a[200:203], a[64:67], v[176:179]\n" \
"v_mfma_f32_16x16x32_bf16 v[176:179], a[204:207], a[68:71], v[176:179]\n" \
"v_mfma_f32_16x16x32_bf16 v[180:183], a[204:207], a[84:87], v[180:183]\n" \
"ds_read_b64_tr_b16 a[204:205], v10 offset:46304\n" \
"ds_read_b64_tr_b16 a[206:207], v10 offset:50912\n" \
"buffer_load_dwordx4 a[44:47], v20, s[56:59], s69 offen offset:3072 sc1\n" \
"v_mfma_f32_16x16x32_bf16 v[180:183], a[200:203], a[80:83], v[180:183]\n" \
"ds_read_b64_tr_b16 a[200:201], v10 offset:55488\n" \
"ds_read_b64_tr_b16 a[202:203], v10 offset:60096\n" \
"v_mfma_f32_16x16x32_bf16 v[112:115], a[208:211], a[72:75], v[112:115]\n" \
"v_mfma_f32_16x16x32_bf16 v[112:115], a[212:215], a[76:79], v[112:115]\n" \
"v_mfma_f32_16x16x32_bf16 v[116:119], a[212:215], a[92:95], v[116:119]\n" \
"ds_read_b64_tr_b16 a[212:213], v10 offset:46336\n" \
"ds_read_b64_tr_b16 a[214:215], v10 offset:50944\n" \
"buffer_load_dwordx4 a[60:63], v32, s[56:59], s69 offen offset:3072 sc1\n" \
"v_mfma_f32_16x16x32_bf16 v[116:119], a[208:211], a[88:91], v[116:119]\n" \
"ds_read_b64_tr_b16 a[208:209], v10 offset:55520\n" \
"ds_read_b64_tr_b16 a[210:211], v10 offset:60128\n" \
"v_mfma_f32_16x16x32_bf16 v[120:123], a[216:219], a[72:75], v[120:123]\n" \
"v_mfma_f32_16x16x32_bf16 v[120:123], a[0:3], a[76:79], v[120:123]\n" \
"v_mfma_f32_16x16x32_bf16 v[124:127], a[0:3], a[92:95], v[124:127]\n" \
"v_mfma_f32_16x16x32_bf16 v[124:127], a[216:219], a[88:91], v[124:127]\n" \
"ds_read_b64_tr_b16 a[216:217], v10 offset:55552\n" \
"buffer_load_dwordx4 a[56:59], v32, s[56:59], s69 offen offset:2048 sc1\n" \
"s_waitcnt lgkmcnt(11)\n" \
"v_mfma_f32_16x16x32_bf16 v[128:131], a[4:7], a[72:75], v[128:131]\n" \
"ds_read_b64_tr_b16 a[218:219], v10 offset:60160\n" \
"v_mfma_f32_16x16x32_bf16 v[128:131], a[8:11], a[76:79], v[128:131]\n" \
"v_mfma_f32_16x16x32_bf16 v[132:135], a[8:11], a[92:95], v[132:135]\n" \
"v_mfma_f32_16x16x32_bf16 v[132:135], a[4:7], a[88:91], v[132:135]\n" \
"buffer_load_dwordx4 a[52:55], v32, s[56:59], s69 offen offset:1024 sc1\n" \
"v_mfma_f32_16x16x32_bf16 v[136:139], a[12:15], a[72:75], v[136:139]\n" \
"v_mfma_f32_16x16x32_bf16 v[136:139], a[16:19], a[76:79], v[136:139]\n" \
"v_mfma_f32_16x16x32_bf16 v[140:143], a[16:19], a[92:95], v[140:143]\n" \
"v_mfma_f32_16x16x32_bf16 v[140:143], a[12:15], a[88:91], v[140:143]\n" \
"s_waitcnt vmcnt(15)\n" \
"s_barrier\n" \
"buffer_load_dwordx4 a[48:51], v32, s[56:59], s69 offen offset:0 sc1\n" \
"v_mfma_f32_16x16x32_bf16 v[144:147], a[20:23], a[72:75], v[144:147]\n" \
"ds_read_b64_tr_b16 a[0:1], v33 offset:18432\n" \
"ds_read_b64_tr_b16 a[2:3], v33 offset:23040\n" \
"v_mfma_f32_16x16x32_bf16 v[144:147], a[24:27], a[76:79], v[144:147]\n" \
"ds_read_b64_tr_b16 a[4:5], v33 offset:27648\n" \
"ds_read_b64_tr_b16 a[6:7], v33 offset:32256\n" \
"v_mfma_f32_16x16x32_bf16 v[148:151], a[24:27], a[92:95], v[148:151]\n" \
"ds_read_b64_tr_b16 a[8:9], v33 offset:36864\n" \
"ds_read_b64_tr_b16 a[10:11], v33 offset:41472\n" \
"v_mfma_f32_16x16x32_bf16 v[148:151], a[20:23], a[88:91], v[148:151]\n" \
"ds_read_b64_tr_b16 a[12:13], v33 offset:46080\n" \
"ds_read_b64_tr_b16 a[14:15], v33 offset:50688\n" \
"buffer_load_dwordx4 a[96:99], v20, s[56:59], s64 offen offset:0 sc1\n" \
"v_mfma_f32_16x16x32_bf16 v[152:155], a[28:31], a[72:75], v[152:155]\n" \
"ds_read_b64_tr_b16 a[16:17], v34 offset:0\n" \
"s_waitcnt lgkmcnt(9)\n" \
"v_mfma_f32_16x16x32_bf16 v[152:155], a[192:195], a[76:79], v[152:155]\n" \
"ds_read_b64_tr_b16 a[18:19], v34 offset:4608\n" \
"ds_read_b64_tr_b16 a[20:21], v34 offset:9216\n" \
"v_mfma_f32_16x16x32_bf16 v[156:159], a[192:195], a[92:95], v[156:159]\n" \
"ds_read_b64_tr_b16 a[22:23], v34 offset:13824\n" \
"ds_read_b64_tr_b16 a[24:25], v34 offset:18432\n" \
"v_mfma_f32_16x16x32_bf16 v[156:159], a[28:31], a[88:91], v[156:159]\n" \
"ds_read_b64_tr_b16 a[26:27], v34 offset:23040\n" \
"ds_read_b64_tr_b16 a[28:29], v33 offset:0\n" \
"buffer_load_dwordx4 a[100:103], v20, s[56:59], s64 offen offset:1024 sc1\n" \
"v_mfma_f32_16x16x32_bf16 v[160:163], a[196:199], a[72:75], v[160:163]\n" \
"ds_read_b64_tr_b16 a[30:31], v33 offset:4608\n" \
"ds_read_b64_tr_b16 a[192:193], v33 offset:18464\n" \
"v_mfma_f32_16x16x32_bf16 v[160:163], a[200:203], a[76:79], v[160:163]\n" \
"ds_read_b64_tr_b16 a[194:195], v33 offset:23072\n" \
"v_readlane_b32 s69, v11, 6\n" \
"v_mfma_f32_16x16x32_bf16 v[164:167], a[200:203], a[92:95], v[164:167]\n" \
"ds_read_b64_tr_b16 a[200:201], v33 offset:36896\n" \
"ds_read_b64_tr_b16 a[202:203], v33 offset:41504\n" \
"v_mfma_f32_16x16x32_bf16 v[164:167], a[196:199], a[88:91], v[164:167]\n" \
"ds_read_b64_tr_b16 a[196:197], v33 offset:27680\n" \
"ds_read_b64_tr_b16 a[198:199], v33 offset:32288\n" \
"buffer_load_dwordx4 a[104:107], v20, s[56:59], s64 offen offset:2048 sc1\n" \
"v_mfma_f32_16x16x32_bf16 v[168:171], a[204:207], a[72:75], v[168:171]\n" \
"v_mfma_f32_16x16x32_bf16 v[168:171], a[208:211], a[76:79], v[168:171]\n" \
"v_mfma_f32_16x16x32_bf16 v[172:175], a[208:211], a[92:95], v[172:175]\n" \
"ds_read_b64_tr_b16 a[208:209], v34 offset:32\n" \
"ds_read_b64_tr_b16 a[210:211], v34 offset:4640\n" \
"v_mfma_f32_16x16x32_bf16 v[172:175], a[204:207], a[88:91], v[172:175]\n" \
"ds_read_b64_tr_b16 a[204:205], v33 offset:46112\n" \
"ds_read_b64_tr_b16 a[206:207], v33 offset:50720\n" \
"buffer_load_dwordx4 a[108:111], v20, s[56:59], s64 offen offset:3072 sc1\n" \
"v_mfma_f32_16x16x32_bf16 v[176:179], a[212:215], a[72:75], v[176:179]\n" \
"v_mfma_f32_16x16x32_bf16 v[176:179], a[216:219], a[76:79], v[176:179]\n" \
"v_mfma_f32_16x16x32_bf16 v[180:183], a[216:219], a[92:95], v[180:183]\n" \
"ds_read_b64_tr_b16 a[216:217], v34 offset:18464\n" \
"ds_read_b64_tr_b16 a[218:219], v34 offset:23072\n" \
"v_mfma_f32_16x16x32_bf16 v[180:183], a[212:215], a[88:91], v[180:183]\n" \
"ds_read_b64_tr_b16 a[212:213], v34 offset:9248\n" \
"buffer_load_dwordx4 a[124:127], v32, s[56:59], s64 offen offset:3072 sc1\n" \
"s_waitcnt vmcnt(20) lgkmcnt(14)\n" \
"v_mfma_f32_16x16x32_bf16 v[112:115], a[0:3], a[128:131], v[112:115]\n" \
"ds_read_b64_tr_b16 a[214:215], v34 offset:13856\n" \
"s_waitcnt vmcnt(19)\n" \
"v_mfma_f32_16x16x32_bf16 v[112:115], a[4:7], a[132:135], v[112:115]\n" \
"s_waitcnt vmcnt(18)\n" \
"v_mfma_f32_16x16x32_bf16 v[112:115], a[8:11], a[136:139], v[112:115]\n" \
"s_waitcnt vmcnt(17)\n" \
"v_mfma_f32_16x16x32_bf16 v[112:115], a[12:15], a[140:143], v[112:115]\n" \
"buffer_load_dwordx4 a[120:123], v32, s[56:59], s64 offen offset:2048 sc1\n" \
"s_waitcnt vmcnt(17)\n" \
"v_mfma_f32_16x16x32_bf16 v[116:119], a[12:15], a[156:159], v[116:119]\n" \
"ds_read_b64_tr_b16 a[12:13], v33 offset:36928\n" \
"s_waitcnt vmcnt(16)\n" \
"v_mfma_f32_16x16x32_bf16 v[116:119], a[8:11], a[152:155], v[116:119]\n" \
"ds_read_b64_tr_b16 a[8:9], v33 offset:27712\n" \
"ds_read_b64_tr_b16 a[10:11], v33 offset:32320\n" \
"ds_read_b64_tr_b16 a[14:15], v33 offset:41536\n" \
"s_waitcnt vmcnt(15)\n" \
"v_mfma_f32_16x16x32_bf16 v[116:119], a[4:7], a[148:151], v[116:119]\n" \
"buffer_load_dwordx4 a[116:119], v32, s[56:59], s64 offen offset:1024 sc1\n" \
"ds_read_b64_tr_b16 a[4:5], v33 offset:18496\n" \
"ds_read_b64_tr_b16 a[6:7], v33 offset:23104\n" \
"s_waitcnt vmcnt(15)\n" \
"v_mfma_f32_16x16x32_bf16 v[116:119], a[0:3], a[144:147], v[116:119]\n" \
"buffer_load_dwordx4 a[112:115], v32, s[56:59], s64 offen offset:0 sc1\n" \
"ds_read_b64_tr_b16 a[0:1], v33 offset:32\n" \
"ds_read_b64_tr_b16 a[2:3], v33 offset:4640\n" \
"v_readlane_b32 s64, v11, 39\n" \
"s_waitcnt vmcnt(15)\n" \
"v_mfma_f32_16x16x32_bf16 v[112:115], a[16:19], a[32:35], v[112:115]\n" \
"buffer_load_dwordx4 a[160:163], v20, s[56:59], s72 offen offset:0 sc1\n" \
"s_waitcnt vmcnt(15)\n" \
"v_mfma_f32_16x16x32_bf16 v[112:115], a[20:23], a[36:39], v[112:115]\n" \
"buffer_load_dwordx4 a[164:167], v20, s[56:59], s72 offen offset:1024 sc1\n" \
"s_waitcnt vmcnt(15)\n" \
"v_mfma_f32_16x16x32_bf16 v[112:115], a[24:27], a[40:43], v[112:115]\n" \
"buffer_load_dwordx4 a[168:171], v20, s[56:59], s72 offen offset:2048 sc1\n" \
"s_waitcnt vmcnt(15) lgkmcnt(0)\n" \
"v_mfma_f32_16x16x32_bf16 v[112:115], a[28:31], a[44:47], v[112:115]\n" \
"buffer_load_dwordx4 a[172:175], v20, s[56:59], s72 offen offset:3072 sc1\n" \
"s_waitcnt vmcnt(15)\n" \
"v_mfma_f32_16x16x32_bf16 v[116:119], a[28:31], a[60:63], v[116:119]\n" \
"buffer_load_dwordx4 a[188:191], v32, s[56:59], s72 offen offset:3072 sc1\n" \
"ds_read_b64_tr_b16 a[28:29], v34 offset:18496\n" \
"ds_read_b64_tr_b16 a[30:31], v34 offset:23104\n" \
"s_waitcnt vmcnt(15)\n" \
"v_mfma_f32_16x16x32_bf16 v[116:119], a[24:27], a[56:59], v[116:119]\n" \
"v_cvt_pk_bf16_f32 v112, v112, v113\n" \
"v_cvt_pk_bf16_f32 v113, v114, v115\n" \
"buffer_load_dwordx4 a[184:187], v32, s[56:59], s72 offen offset:2048 sc1\n" \
"ds_read_b64_tr_b16 a[24:25], v34 offset:9280\n" \
"ds_read_b64_tr_b16 a[26:27], v34 offset:13888\n" \
"s_waitcnt vmcnt(15)\n" \
"v_mfma_f32_16x16x32_bf16 v[116:119], a[20:23], a[52:55], v[116:119]\n" \
"ds_read_b64_tr_b16 a[20:21], v34 offset:64\n" \
"ds_read_b64_tr_b16 a[22:23], v34 offset:4672\n" \
"buffer_load_dwordx4 a[180:183], v32, s[56:59], s72 offen offset:1024 sc1\n" \
"s_waitcnt vmcnt(15)\n" \
"v_mfma_f32_16x16x32_bf16 v[116:119], a[16:19], a[48:51], v[116:119]\n" \
"buffer_load_dwordx4 a[176:179], v32, s[56:59], s72 offen offset:0 sc1\n" \
"v_mfma_f32_16x16x32_bf16 v[120:123], a[192:195], a[128:131], v[120:123]\n" \
"ds_read_b64_tr_b16 a[16:17], v33 offset:46144\n" \
"ds_read_b64_tr_b16 a[18:19], v33 offset:50752\n" \
"v_mfma_f32_16x16x32_bf16 v[120:123], a[196:199], a[132:135], v[120:123]\n" \
"v_readlane_b32 s72, v11, 48\n" \
"v_mfma_f32_16x16x32_bf16 v[120:123], a[200:203], a[136:139], v[120:123]\n" \
"v_mfma_f32_16x16x32_bf16 v[120:123], a[204:207], a[140:143], v[120:123]\n" \
"v_cvt_pk_bf16_f32 v114, v116, v117\n" \
"v_cvt_pk_bf16_f32 v115, v118, v119\n" \
"buffer_store_dwordx4 v[112:115], v20, s[56:59], s65 offen offset:0 sc1\n" \
"s_add_u32 s65, s65, 0x3800\n" \
"v_mfma_f32_16x16x32_bf16 v[124:127], a[204:207], a[156:159], v[124:127]\n" \
"ds_read_b64_tr_b16 a[204:205], v10 offset:46336\n" \
"ds_read_b64_tr_b16 a[206:207], v10 offset:50944\n" \
"v_mfma_f32_16x16x32_bf16 v[124:127], a[200:203], a[152:155], v[124:127]\n" \
"ds_read_b64_tr_b16 a[200:201], v10 offset:37120\n" \
"ds_read_b64_tr_b16 a[202:203], v10 offset:41728\n" \
"v_mfma_f32_16x16x32_bf16 v[124:127], a[196:199], a[148:151], v[124:127]\n" \
"ds_read_b64_tr_b16 a[196:197], v10 offset:27904\n" \
"ds_read_b64_tr_b16 a[198:199], v10 offset:32512\n" \
"v_mfma_f32_16x16x32_bf16 v[124:127], a[192:195], a[144:147], v[124:127]\n" \
"buffer_load_dwordx4 a[220:223], v20, s[56:59], s93 offen offset:0 sc1\n" \
"v_mfma_f32_16x16x32_bf16 v[120:123], a[208:211], a[32:35], v[120:123]\n" \
"ds_read_b64_tr_b16 a[192:193], v33 offset:64\n" \
"ds_read_b64_tr_b16 a[194:195], v33 offset:4672\n" \
"v_mfma_f32_16x16x32_bf16 v[120:123], a[212:215], a[36:39], v[120:123]\n" \
"v_mfma_f32_16x16x32_bf16 v[120:123], a[216:219], a[40:43], v[120:123]\n" \
"v_mfma_f32_16x16x32_bf16 v[120:123], a[0:3], a[44:47], v[120:123]\n" \
"v_mfma_f32_16x16x32_bf16 v[124:127], a[0:3], a[60:63], v[124:127]\n" \
"buffer_load_dwordx4 a[224:227], v20, s[56:59], s93 offen offset:1024 sc1\n" \
"v_mfma_f32_16x16x32_bf16 v[124:127], a[216:219], a[56:59], v[124:127]\n" \
"ds_read_b64_tr_b16 a[216:217], v33 offset:27904\n" \
"ds_read_b64_tr_b16 a[218:219], v33 offset:32512\n" \
"v_mfma_f32_16x16x32_bf16 v[124:127], a[212:215], a[52:55], v[124:127]\n" \
"ds_read_b64_tr_b16 a[212:213], v33 offset:18688\n" \
"ds_read_b64_tr_b16 a[214:215], v33 offset:23296\n" \
"v_cvt_pk_bf16_f32 v120, v120, v121\n" \
"v_cvt_pk_bf16_f32 v121, v122, v123\n" \
"v_mfma_f32_16x16x32_bf16 v[124:127], a[208:211], a[48:51], v[124:127]\n" \
"ds_read_b64_tr_b16 a[208:209], v10 offset:55552\n" \
"ds_read_b64_tr_b16 a[210:211], v10 offset:60160\n" \
"v_mfma_f32_16x16x32_bf16 v[128:131], a[4:7], a[128:131], v[128:131]\n" \
"buffer_load_dwordx4 a[228:231], v20, s[56:59], s93 offen offset:2048 sc1\n" \
"v_mfma_f32_16x16x32_bf16 v[128:131], a[8:11], a[132:135], v[128:131]\n" \
"ds_read_b64_tr_b16 a[0:1], v33 offset:37120\n" \
"ds_read_b64_tr_b16 a[2:3], v33 offset:41728\n" \
"v_mfma_f32_16x16x32_bf16 v[128:131], a[12:15], a[136:139], v[128:131]\n" \
"v_cvt_pk_bf16_f32 v122, v124, v125\n" \
"v_cvt_pk_bf16_f32 v123, v126, v127\n" \
"buffer_store_dwordx4 v[120:123], v20, s[56:59], s66 offen offset:0 sc1\n" \
"s_add_u32 s66, s66, 0x3000\n" \
"s_waitcnt lgkmcnt(12)\n" \
"v_mfma_f32_16x16x32_bf16 v[128:131], a[16:19], a[140:143], v[128:131]\n" \
"v_mfma_f32_16x16x32_bf16 v[132:135], a[16:19], a[156:159], v[132:135]\n" \
"ds_read_b64_tr_b16 a[16:17], v34 offset:18688\n" \
"ds_read_b64_tr_b16 a[18:19], v34 offset:23296\n" \
"v_mfma_f32_16x16x32_bf16 v[132:135], a[12:15], a[152:155], v[132:135]\n" \
"ds_read_b64_tr_b16 a[12:13], v34 offset:9472\n" \
"ds_read_b64_tr_b16 a[14:15], v34 offset:14080\n" \
"v_mfma_f32_16x16x32_bf16 v[132:135], a[8:11], a[148:151], v[132:135]\n" \
"buffer_load_dwordx4 a[232:235], v20, s[56:59], s93 offen offset:3072 sc1\n" \
"v_mfma_f32_16x16x32_bf16 v[132:135], a[4:7], a[144:147], v[132:135]\n" \
"ds_read_b64_tr_b16 a[4:5], v33 offset:46336\n" \
"ds_read_b64_tr_b16 a[6:7], v33 offset:50944\n" \
"v_mfma_f32_16x16x32_bf16 v[128:131], a[20:23], a[32:35], v[128:131]\n" \
"ds_read_b64_tr_b16 a[8:9], v34 offset:256\n" \
"ds_read_b64_tr_b16 a[10:11], v34 offset:4864\n" \
"v_mfma_f32_16x16x32_bf16 v[128:131], a[24:27], a[36:39], v[128:131]\n" \
"v_mfma_f32_16x16x32_bf16 v[128:131], a[28:31], a[40:43], v[128:131]\n" \
"s_waitcnt lgkmcnt(14)\n" \
"v_mfma_f32_16x16x32_bf16 v[128:131], a[192:195], a[44:47], v[128:131]\n" \
"buffer_load_dwordx4 a[248:251], v32, s[56:59], s93 offen offset:3072 sc1\n" \
"v_mfma_f32_16x16x32_bf16 v[132:135], a[192:195], a[60:63], v[132:135]\n" \
"ds_read_b64_tr_b16 a[192:193], v10 offset:46176\n" \
"ds_read_b64_tr_b16 a[194:195], v10 offset:50784\n" \
"v_mfma_f32_16x16x32_bf16 v[132:135], a[28:31], a[56:59], v[132:135]\n" \
"ds_read_b64_tr_b16 a[28:29], v10 offset:36960\n" \
"ds_read_b64_tr_b16 a[30:31], v10 offset:41568\n" \
"v_mfma_f32_16x16x32_bf16 v[132:135], a[24:27], a[52:55], v[132:135]\n" \
"v_cvt_pk_bf16_f32 v128, v128, v129\n" \
"v_cvt_pk_bf16_f32 v129, v130, v131\n" \
"v_mfma_f32_16x16x32_bf16 v[132:135], a[20:23], a[48:51], v[132:135]\n" \
"ds_read_b64_tr_b16 a[20:21], v33 offset:256\n" \
"buffer_load_dwordx4 a[244:247], v32, s[56:59], s93 offen offset:2048 sc1\n" \
"s_waitcnt vmcnt(23)\n" \
"v_mfma_f32_16x16x32_bf16 v[248:251], a[196:199], a[96:99], v[248:251]\n" \
"ds_read_b64_tr_b16 a[22:23], v33 offset:4864\n" \
"s_waitcnt vmcnt(22)\n" \
"v_mfma_f32_16x16x32_bf16 v[248:251], a[200:203], a[100:103], v[248:251]\n" \
"ds_read_b64_tr_b16 a[24:25], v10 offset:27744\n" \
"v_cvt_pk_bf16_f32 v130, v132, v133\n" \
"v_cvt_pk_bf16_f32 v131, v134, v135\n" \
"buffer_store_dwordx4 v[128:131], v20, s[56:59], s67 offen offset:0 sc1\n" \
"s_add_u32 s67, s67, 0x2800\n" \
"s_waitcnt vmcnt(22)\n" \
"v_mfma_f32_16x16x32_bf16 v[248:251], a[204:207], a[104:107], v[248:251]\n" \
"ds_read_b64_tr_b16 a[26:27], v10 offset:32352\n" \
"s_waitcnt vmcnt(21) lgkmcnt(12)\n" \
"v_mfma_f32_16x16x32_bf16 v[248:251], a[208:211], a[108:111], v[248:251]\n" \
"s_waitcnt vmcnt(20)\n" \
"v_mfma_f32_16x16x32_bf16 v[252:255], a[208:211], a[124:127], v[252:255]\n" \
"ds_read_b64_tr_b16 a[208:209], v33 offset:36960\n" \
"L_pf21:\n" \
"s_cbranch_execz L_retB\n" \
"s_nop 0\n" \
"s_waitcnt vmcnt(19)\n" \
"v_mfma_f32_16x16x32_bf16 v[252:255], a[204:207], a[120:123], v[252:255]\n" \
"ds_read_b64_tr_b16 a[204:205], v33 offset:27744\n" \
"buffer_load_dwordx4 a[240:243], v32, s[56:59], s93 offen offset:1024 sc1\n" \
"s_waitcnt vmcnt(19)\n" \
"v_mfma_f32_16x16x32_bf16 v[252:255], a[200:203], a[116:119], v[252:255]\n" \
"ds_read_b64_tr_b16 a[200:201], v33 offset:18528\n" \
"s_waitcnt vmcnt(18)\n" \
"v_mfma_f32_16x16x32_bf16 v[252:255], a[196:199], a[112:115], v[252:255]\n" \
"ds_read_b64_tr_b16 a[196:197], v10 offset:55392\n" \
"ds_read_b64_tr_b16 a[198:199], v10 offset:60000\n" \
"v_mfma_f32_16x16x32_bf16 v[176:179], a[212:215], a[128:131], v[176:179]\n" \
"ds_read_b64_tr_b16 a[202:203], v33 offset:23136\n" \
"ds_read_b64_tr_b16 a[206:207], v33 offset:32352\n" \
"v_mfma_f32_16x16x32_bf16 v[176:179], a[216:219], a[132:135], v[176:179]\n" \
"buffer_load_dwordx4 a[236:239], v32, s[56:59], s93 offen offset:0 sc1\n" \
"v_mfma_f32_16x16x32_bf16 v[176:179], a[0:3], a[136:139], v[176:179]\n" \
"ds_read_b64_tr_b16 a[210:211], v33 offset:41568\n" \
"s_waitcnt lgkmcnt(12)\n" \
"v_mfma_f32_16x16x32_bf16 v[176:179], a[4:7], a[140:143], v[176:179]\n" \
"v_readlane_b32 s93, v11, 44\n" \
"v_mfma_f32_16x16x32_bf16 v[180:183], a[4:7], a[156:159], v[180:183]\n" \
"v_mfma_f32_16x16x32_bf16 v[180:183], a[0:3], a[152:155], v[180:183]\n" \
"v_mfma_f32_16x16x32_bf16 v[180:183], a[216:219], a[148:151], v[180:183]\n" \
"buffer_load_dwordx4 v[64:67], v20, s[56:59], s95 offen offset:0 sc1\n" \
"v_mfma_f32_16x16x32_bf16 v[180:183], a[212:215], a[144:147], v[180:183]\n" \
"s_waitcnt vmcnt(19)\n" \
"v_mfma_f32_16x16x32_bf16 v[248:251], a[212:215], a[160:163], v[248:251]\n" \
"s_waitcnt vmcnt(18)\n" \
"v_mfma_f32_16x16x32_bf16 v[248:251], a[216:219], a[164:167], v[248:251]\n" \
"s_waitcnt vmcnt(17)\n" \
"v_mfma_f32_16x16x32_bf16 v[248:251], a[0:3], a[168:171], v[248:251]\n" \
"s_waitcnt vmcnt(16)\n" \
"v_mfma_f32_16x16x32_bf16 v[248:251], a[4:7], a[172:175], v[248:251]\n" \
"buffer_load_dwordx4 v[72:75], v32, s[56:59], s95 offen offset:0 sc1\n" \
"s_waitcnt vmcnt(16)\n" \
"v_mfma_f32_16x16x32_bf16 v[252:255], a[4:7], a[188:191], v[252:255]\n" \
"ds_read_b64_tr_b16 a[4:5], v34 offset:18528\n" \
"ds_read_b64_tr_b16 a[6:7], v34 offset:23136\n" \
"s_waitcnt vmcnt(15)\n" \
"v_mfma_f32_16x16x32_bf16 v[252:255], a[0:3], a[184:187], v[252:255]\n" \
"ds_read_b64_tr_b16 a[0:1], v34 offset:9312\n" \
"ds_read_b64_tr_b16 a[2:3], v34 offset:13920\n" \
"buffer_load_dwordx4 v[80:83], v20, s[56:59], s96 offen offset:0 sc1\n" \
"s_waitcnt vmcnt(15)\n" \
"v_mfma_f32_16x16x32_bf16 v[252:255], a[216:219], a[180:183], v[252:255]\n" \
"ds_read_b64_tr_b16 a[216:217], v34 offset:96\n" \
"ds_read_b64_tr_b16 a[218:219], v34 offset:4704\n" \
"buffer_load_dwordx4 v[88:91], v32, s[56:59], s96 offen offset:0 sc1\n" \
"s_waitcnt vmcnt(15)\n" \
"v_mfma_f32_16x16x32_bf16 v[252:255], a[212:215], a[176:179], v[252:255]\n" \
"ds_read_b64_tr_b16 a[212:213], v33 offset:46176\n" \
"ds_read_b64_tr_b16 a[214:215], v33 offset:50784\n" \
"v_mfma_f32_16x16x32_bf16 v[176:179], a[8:11], a[32:35], v[176:179]\n" \
"buffer_load_dwordx4 v[68:71], v20, s[56:59], s95 offen offset:1024 sc1\n" \
"v_mfma_f32_16x16x32_bf16 v[176:179], a[12:15], a[36:39], v[176:179]\n" \
"v_mfma_f32_16x16x32_bf16 v[176:179], a[16:19], a[40:43], v[176:179]\n" \
"s_waitcnt lgkmcnt(2)\n" \
"v_mfma_f32_16x16x32_bf16 v[176:179], a[20:23], a[44:47], v[176:179]\n" \
"s_waitcnt vmcnt(15)\n" \
"s_mov_b64 exec, 1\n" \
"buffer_store_dword v39, off, s[56:59], s65 offset:0 sc1\n" \
"s_mov_b64 exec, -1\n" \
"v_mfma_f32_16x16x32_bf16 v[180:183], a[20:23], a[60:63], v[180:183]\n" \
"s_add_u32 s65, s63, 0x1800\n" \
"v_mfma_f32_16x16x32_bf16 v[180:183], a[16:19], a[56:59], v[180:183]\n" \
"v_mfma_f32_16x16x32_bf16 v[180:183], a[12:15], a[52:55], v[180:183]\n" \
"v_cvt_pk_bf16_f32 v176, v176, v177\n" \
"v_cvt_pk_bf16_f32 v177, v178, v179\n" \
"v_mfma_f32_16x16x32_bf16 v[180:183], a[8:11], a[48:51], v[180:183]\n" \
"s_waitcnt vmcnt(15)\n" \
"v_mfma_f32_16x16x32_bf16 v[248:251], a[8:11], a[220:223], v[248:251]\n" \
"buffer_load_dwordx4 v[76:79], v32, s[56:59], s95 offen offset:1024 sc1\n" \
"s_add_u32 s95, s63, 0x0\n" \
"s_waitcnt vmcnt(15)\n" \
"v_mfma_f32_16x16x32_bf16 v[248:251], a[12:15], a[224:227], v[248:251]\n" \
"s_waitcnt vmcnt(14)\n" \
"v_mfma_f32_16x16x32_bf16 v[248:251], a[16:19], a[228:231], v[248:251]\n" \
"v_cvt_pk_bf16_f32 v178, v180, v181\n" \
"v_cvt_pk_bf16_f32 v179, v182, v183\n" \
"buffer_store_dwordx4 v[176:179], v20, s[56:59], s68 offen offset:0 sc1\n" \
"s_waitcnt vmcnt(13)\n" \
"v_mfma_f32_16x16x32_bf16 v[248:251], a[20:23], a[232:235], v[248:251]\n" \
"s_mov_b64 exec, 1\n" \
"buffer_store_dword v39, off, s[56:59], s66 offset:4 sc1\n" \
"s_mov_b64 exec, -1\n" \
"s_add_u32 s66, s63, 0x2000\n" \
"s_waitcnt vmcnt(13)\n" \
"v_mfma_f32_16x16x32_bf16 v[252:255], a[20:23], a[248:251], v[252:255]\n" \
"ds_read_b64_tr_b16 a[20:21], v10 offset:46208\n" \
"ds_read_b64_tr_b16 a[22:23], v10 offset:50816\n" \
"v_cvt_pk_bf16_f32 v248, v248, v249\n" \
"v_cvt_pk_bf16_f32 v249, v250, v251\n" \
"buffer_load_dwordx4 v[84:87], v20, s[56:59], s96 offen offset:1024 sc1\n" \
"s_waitcnt vmcnt(13)\n" \
"v_mfma_f32_16x16x32_bf16 v[252:255], a[16:19], a[244:247], v[252:255]\n" \
"ds_read_b64_tr_b16 a[16:17], v10 offset:36992\n" \
"ds_read_b64_tr_b16 a[18:19], v10 offset:41600\n" \
"s_waitcnt vmcnt(11)\n" \
"v_mfma_f32_16x16x32_bf16 v[252:255], a[12:15], a[240:243], v[252:255]\n" \
"s_mov_b64 exec, 1\n" \
"buffer_store_dword v39, off, s[56:59], s67 offset:8 sc1\n" \
"s_mov_b64 exec, -1\n" \
"ds_read_b64_tr_b16 a[12:13], v10 offset:27776\n" \
"ds_read_b64_tr_b16 a[14:15], v10 offset:32384\n" \
"s_add_u32 s67, s63, 0x2800\n" \
"s_waitcnt vmcnt(11)\n" \
"v_mfma_f32_16x16x32_bf16 v[252:255], a[8:11], a[236:239], v[252:255]\n" \
"ds_read_b64_tr_b16 a[8:9], v33 offset:96\n" \
"ds_read_b64_tr_b16 a[10:11], v33 offset:4704\n" \
"v_mfma_f32_16x16x32_bf16 v[208:211], a[24:27], a[96:99], v[208:211]\n" \
"buffer_load_dwordx4 v[92:95], v32, s[56:59], s96 offen offset:1024 sc1\n" \
"v_mfma_f32_16x16x32_bf16 v[208:211], a[28:31], a[100:103], v[208:211]\n" \
"s_add_u32 s96, s63, 0x800\n" \
"v_mfma_f32_16x16x32_bf16 v[208:211], a[192:195], a[104:107], v[208:211]\n" \
"v_mfma_f32_16x16x32_bf16 v[208:211], a[196:199], a[108:111], v[208:211]\n" \
"v_cvt_pk_bf16_f32 v250, v252, v253\n" \
"v_cvt_pk_bf16_f32 v251, v254, v255\n" \
"buffer_store_dwordx4 v[248:251], v20, s[56:59], s68 offen offset:1024 sc1\n" \
"v_mfma_f32_16x16x32_bf16 v[212:215], a[196:199], a[124:127], v[212:215]\n" \
"ds_read_b64_tr_b16 a[196:197], v33 offset:36992\n" \
"ds_read_b64_tr_b16 a[198:199], v33 offset:41600\n" \
"v_mfma_f32_16x16x32_bf16 v[212:215], a[192:195], a[120:123], v[212:215]\n" \
"ds_read_b64_tr_b16 a[192:193], v33 offset:27776\n" \
"ds_read_b64_tr_b16 a[194:195], v33 offset:32384\n" \
"v_mfma_f32_16x16x32_bf16 v[212:215], a[28:31], a[116:119], v[212:215]\n" \
"ds_read_b64_tr_b16 a[28:29], v33 offset:18560\n" \
"ds_read_b64_tr_b16 a[30:31], v33 offset:23168\n" \
"v_mfma_f32_16x16x32_bf16 v[212:215], a[24:27], a[112:115], v[212:215]\n" \
"ds_read_b64_tr_b16 a[24:25], v10 offset:55424\n" \
"ds_read_b64_tr_b16 a[26:27], v10 offset:60032\n" \
"v_mfma_f32_16x16x32_bf16 v[136:139], a[200:203], a[128:131], v[136:139]\n" \
"buffer_load_dwordx4 v[96:99], v20, s[56:59], s97 offen offset:0 sc1\n" \
"v_mfma_f32_16x16x32_bf16 v[136:139], a[204:207], a[132:135], v[136:139]\n" \
"s_add_u32 s68, s63, 0x3000\n" \
"v_mfma_f32_16x16x32_bf16 v[136:139], a[208:211], a[136:139], v[136:139]\n" \
"s_waitcnt lgkmcnt(10)\n" \
"v_mfma_f32_16x16x32_bf16 v[136:139], a[212:215], a[140:143], v[136:139]\n" \
"v_mfma_f32_16x16x32_bf16 v[140:143], a[212:215], a[156:159], v[140:143]\n" \
"v_mfma_f32_16x16x32_bf16 v[140:143], a[208:211], a[152:155], v[140:143]\n" \
"buffer_load_dwordx4 v[104:107], v32, s[56:59], s97 offen offset:0 sc1\n" \
"v_mfma_f32_16x16x32_bf16 v[140:143], a[204:207], a[148:151], v[140:143]\n" \
"v_mfma_f32_16x16x32_bf16 v[140:143], a[200:203], a[144:147], v[140:143]\n" \
"v_mfma_f32_16x16x32_bf16 v[208:211], a[200:203], a[160:163], v[208:211]\n" \
"v_mfma_f32_16x16x32_bf16 v[208:211], a[204:207], a[164:167], v[208:211]\n" \
"v_mfma_f32_16x16x32_bf16 v[208:211], a[208:211], a[168:171], v[208:211]\n" \
"buffer_load_dwordx4 v[100:103], v20, s[56:59], s97 offen offset:1024 sc1\n" \
"v_mfma_f32_16x16x32_bf16 v[208:211], a[212:215], a[172:175], v[208:211]\n" \
"v_mfma_f32_16x16x32_bf16 v[212:215], a[212:215], a[188:191], v[212:215]\n" \
"ds_read_b64_tr_b16 a[212:213], v34 offset:18560\n" \
"ds_read_b64_tr_b16 a[214:215], v34 offset:23168\n" \
"v_mfma_f32_16x16x32_bf16 v[212:215], a[208:211], a[184:187], v[212:215]\n" \
"ds_read_b64_tr_b16 a[208:209], v34 offset:9344\n" \
"ds_read_b64_tr_b16 a[210:211], v34 offset:13952\n" \
"v_mfma_f32_16x16x32_bf16 v[212:215], a[204:207], a[180:183], v[212:215]\n" \
"ds_read_b64_tr_b16 a[204:205], v34 offset:128\n" \
"ds_read_b64_tr_b16 a[206:207], v34 offset:4736\n" \
"v_mfma_f32_16x16x32_bf16 v[212:215], a[200:203], a[176:179], v[212:215]\n" \
"ds_read_b64_tr_b16 a[200:201], v33 offset:46208\n" \
"ds_read_b64_tr_b16 a[202:203], v33 offset:50816\n" \
"v_mfma_f32_16x16x32_bf16 v[136:139], a[216:219], a[32:35], v[136:139]\n" \
"buffer_load_dwordx4 v[108:111], v32, s[56:59], s97 offen offset:1024 sc1\n" \
"v_mfma_f32_16x16x32_bf16 v[136:139], a[0:3], a[36:39], v[136:139]\n" \
"s_add_u32 s97, s63, 0x1000\n" \
"v_mfma_f32_16x16x32_bf16 v[136:139], a[4:7], a[40:43], v[136:139]\n" \
"s_waitcnt lgkmcnt(8)\n" \
"v_mfma_f32_16x16x32_bf16 v[136:139], a[8:11], a[44:47], v[136:139]\n" \
"v_mfma_f32_16x16x32_bf16 v[140:143], a[8:11], a[60:63], v[140:143]\n" \
"v_mfma_f32_16x16x32_bf16 v[140:143], a[4:7], a[56:59], v[140:143]\n" \
"v_mfma_f32_16x16x32_bf16 v[140:143], a[0:3], a[52:55], v[140:143]\n" \
"v_mfma_f32_16x16x32_bf16 v[140:143], a[216:219], a[48:51], v[140:143]\n" \
"v_mfma_f32_16x16x32_bf16 v[208:211], a[216:219], a[220:223], v[208:211]\n" \
"v_mfma_f32_16x16x32_bf16 v[208:211], a[0:3], a[224:227], v[208:211]\n" \
"v_mfma_f32_16x16x32_bf16 v[208:211], a[4:7], a[228:231], v[208:211]\n" \
"v_mfma_f32_16x16x32_bf16 v[208:211], a[8:11], a[232:235], v[208:211]\n" \
"v_cvt_pk_bf16_f32 v136, v136, v137\n" \
"v_cvt_pk_bf16_f32 v137, v138, v139\n" \
"v_mfma_f32_16x16x32_bf16 v[212:215], a[8:11], a[248:251], v[212:215]\n" \
"ds_read_b64_tr_b16 a[8:9], v10 offset:46240\n" \
"v_cvt_pk_bf16_f32 v138, v140, v141\n" \
"v_cvt_pk_bf16_f32 v139, v142, v143\n" \
"buffer_store_dwordx4 v[136:139], v20, s[56:59], s70 offen offset:0 sc1\n" \
"v_mfma_f32_16x16x32_bf16 v[212:215], a[4:7], a[244:247], v[212:215]\n" \
"v_cvt_pk_bf16_f32 v208, v208, v209\n" \
"v_cvt_pk_bf16_f32 v209, v210, v211\n" \
"v_mfma_f32_16x16x32_bf16 v[212:215], a[0:3], a[240:243], v[212:215]\n" \
"ds_read_b64_tr_b16 a[0:1], v10 offset:27808\n" \
"ds_read_b64_tr_b16 a[2:3], v10 offset:32416\n" \
"v_mfma_f32_16x16x32_bf16 v[212:215], a[216:219], a[236:239], v[212:215]\n" \
"ds_read_b64_tr_b16 a[216:217], v33 offset:128\n" \
"ds_read_b64_tr_b16 a[218:219], v33 offset:4736\n" \
"v_mfma_f32_16x16x32_bf16 v[216:219], a[12:15], a[96:99], v[216:219]\n" \
"ds_read_b64_tr_b16 a[4:5], v10 offset:37024\n" \
"ds_read_b64_tr_b16 a[6:7], v10 offset:41632\n" \
"v_mfma_f32_16x16x32_bf16 v[216:219], a[16:19], a[100:103], v[216:219]\n" \
"ds_read_b64_tr_b16 a[10:11], v10 offset:50848\n" \
"v_mfma_f32_16x16x32_bf16 v[216:219], a[20:23], a[104:107], v[216:219]\n" \
"v_cvt_pk_bf16_f32 v210, v212, v213\n" \
"v_cvt_pk_bf16_f32 v211, v214, v215\n" \
"buffer_store_dwordx4 v[208:211], v20, s[56:59], s70 offen offset:1024 sc1\n" \
"s_add_u32 s70, s70, 0x2000\n" \
"v_mfma_f32_16x16x32_bf16 v[216:219], a[24:27], a[108:111], v[216:219]\n" \
"v_mfma_f32_16x16x32_bf16 v[220:223], a[24:27], a[124:127], v[220:223]\n" \
"ds_read_b64_tr_b16 a[24:25], v33 offset:37024\n" \
"ds_read_b64_tr_b16 a[26:27], v33 offset:41632\n" \
"v_mfma_f32_16x16x32_bf16 v[220:223], a[20:23], a[120:123], v[220:223]\n" \
"ds_read_b64_tr_b16 a[20:21], v33 offset:27808\n" \
"ds_read_b64_tr_b16 a[22:23], v33 offset:32416\n" \
"v_mfma_f32_16x16x32_bf16 v[220:223], a[16:19], a[116:119], v[220:223]\n" \
"ds_read_b64_tr_b16 a[16:17], v33 offset:18592\n" \
"ds_read_b64_tr_b16 a[18:19], v33 offset:23200\n" \
"v_mfma_f32_16x16x32_bf16 v[220:223], a[12:15], a[112:115], v[220:223]\n" \
"ds_read_b64_tr_b16 a[12:13], v10 offset:55456\n" \
"ds_read_b64_tr_b16 a[14:15], v10 offset:60064\n" \
"v_mfma_f32_16x16x32_bf16 v[144:147], a[28:31], a[128:131], v[144:147]\n" \
"v_mfma_f32_16x16x32_bf16 v[144:147], a[192:195], a[132:135], v[144:147]\n" \
"v_mfma_f32_16x16x32_bf16 v[144:147], a[196:199], a[136:139], v[144:147]\n" \
"s_waitcnt lgkmcnt(15)\n" \
"v_mfma_f32_16x16x32_bf16 v[144:147], a[200:203], a[140:143], v[144:147]\n" \
"v_mfma_f32_16x16x32_bf16 v[148:151], a[200:203], a[156:159], v[148:151]\n" \
"v_mfma_f32_16x16x32_bf16 v[148:151], a[196:199], a[152:155], v[148:151]\n" \
"v_mfma_f32_16x16x32_bf16 v[148:151], a[192:195], a[148:151], v[148:151]\n" \
"v_mfma_f32_16x16x32_bf16 v[148:151], a[28:31], a[144:147], v[148:151]\n" \
"v_mfma_f32_16x16x32_bf16 v[216:219], a[28:31], a[160:163], v[216:219]\n" \
"v_mfma_f32_16x16x32_bf16 v[216:219], a[192:195], a[164:167], v[216:219]\n" \
"v_mfma_f32_16x16x32_bf16 v[216:219], a[196:199], a[168:171], v[216:219]\n" \
"v_mfma_f32_16x16x32_bf16 v[216:219], a[200:203], a[172:175], v[216:219]\n" \
"v_mfma_f32_16x16x32_bf16 v[220:223], a[200:203], a[188:191], v[220:223]\n" \
"ds_read_b64_tr_b16 a[200:201], v34 offset:18592\n" \
"ds_read_b64_tr_b16 a[202:203], v34 offset:23200\n" \
"v_mfma_f32_16x16x32_bf16 v[220:223], a[196:199], a[184:187], v[220:223]\n" \
"ds_read_b64_tr_b16 a[196:197], v34 offset:9376\n" \
"ds_read_b64_tr_b16 a[198:199], v34 offset:13984\n" \
"v_mfma_f32_16x16x32_bf16 v[220:223], a[192:195], a[180:183], v[220:223]\n" \
"ds_read_b64_tr_b16 a[192:193], v34 offset:160\n" \
"ds_read_b64_tr_b16 a[194:195], v34 offset:4768\n" \
"v_mfma_f32_16x16x32_bf16 v[220:223], a[28:31], a[176:179], v[220:223]\n" \
"ds_read_b64_tr_b16 a[28:29], v33 offset:46240\n" \
"ds_read_b64_tr_b16 a[30:31], v33 offset:50848\n" \
"v_mfma_f32_16x16x32_bf16 v[144:147], a[204:207], a[32:35], v[144:147]\n" \
"v_mfma_f32_16x16x32_bf16 v[144:147], a[208:211], a[36:39], v[144:147]\n" \
"v_mfma_f32_16x16x32_bf16 v[144:147], a[212:215], a[40:43], v[144:147]\n" \
"s_waitcnt lgkmcnt(8)\n" \
"v_mfma_f32_16x16x32_bf16 v[144:147], a[216:219], a[44:47], v[144:147]\n" \
"v_mfma_f32_16x16x32_bf16 v[148:151], a[216:219], a[60:63], v[148:151]\n" \
"v_mfma_f32_16x16x32_bf16 v[148:151], a[212:215], a[56:59], v[148:151]\n" \
"v_mfma_f32_16x16x32_bf16 v[148:151], a[208:211], a[52:55], v[148:151]\n" \
"v_mfma_f32_16x16x32_bf16 v[148:151], a[204:207], a[48:51], v[148:151]\n" \
"v_mfma_f32_16x16x32_bf16 v[216:219], a[204:207], a[220:223], v[216:219]\n" \
"v_mfma_f32_16x16x32_bf16 v[216:219], a[208:211], a[224:227], v[216:219]\n" \
"v_mfma_f32_16x16x32_bf16 v[216:219], a[212:215], a[228:231], v[216:219]\n" \
"v_mfma_f32_16x16x32_bf16 v[216:219], a[216:219], a[232:235], v[216:219]\n" \
"v_cvt_pk_bf16_f32 v144, v144, v145\n" \
"v_cvt_pk_bf16_f32 v145, v146, v147\n" \
"v_mfma_f32_16x16x32_bf16 v[220:223], a[216:219], a[248:251], v[220:223]\n" \
"ds_read_b64_tr_b16 a[216:217], v10 offset:46272\n" \
"v_cvt_pk_bf16_f32 v146, v148, v149\n" \
"v_cvt_pk_bf16_f32 v147, v150, v151\n" \
"buffer_store_dwordx4 v[144:147], v20, s[56:59], s71 offen offset:0 sc1\n" \
"v_mfma_f32_16x16x32_bf16 v[220:223], a[212:215], a[244:247], v[220:223]\n" \
"v_cvt_pk_bf16_f32 v216, v216, v217\n" \
"v_cvt_pk_bf16_f32 v217, v218, v219\n" \
"v_mfma_f32_16x16x32_bf16 v[220:223], a[208:211], a[240:243], v[220:223]\n" \
"ds_read_b64_tr_b16 a[208:209], v10 offset:27840\n" \
"ds_read_b64_tr_b16 a[210:211], v10 offset:32448\n" \
"v_mfma_f32_16x16x32_bf16 v[220:223], a[204:207], a[236:239], v[220:223]\n" \
"ds_read_b64_tr_b16 a[204:205], v33 offset:160\n" \
"ds_read_b64_tr_b16 a[206:207], v33 offset:4768\n" \
"v_mfma_f32_16x16x32_bf16 v[224:227], a[0:3], a[96:99], v[224:227]\n" \
"ds_read_b64_tr_b16 a[212:213], v10 offset:37056\n" \
"ds_read_b64_tr_b16 a[214:215], v10 offset:41664\n" \
"v_mfma_f32_16x16x32_bf16 v[224:227], a[4:7], a[100:103], v[224:227]\n" \
"ds_read_b64_tr_b16 a[218:219], v10 offset:50880\n" \
"v_mfma_f32_16x16x32_bf16 v[224:227], a[8:11], a[104:107], v[224:227]\n" \
"v_cvt_pk_bf16_f32 v218, v220, v221\n" \
"v_cvt_pk_bf16_f32 v219, v222, v223\n" \
"buffer_store_dwordx4 v[216:219], v20, s[56:59], s71 offen offset:1024 sc1\n" \
"s_add_u32 s71, s71, 0x1800\n" \
"v_mfma_f32_16x16x32_bf16 v[224:227], a[12:15], a[108:111], v[224:227]\n" \
"v_mfma_f32_16x16x32_bf16 v[228:231], a[12:15], a[124:127], v[228:231]\n" \
"ds_read_b64_tr_b16 a[12:13], v33 offset:37056\n" \
"ds_read_b64_tr_b16 a[14:15], v33 offset:41664\n" \
"v_mfma_f32_16x16x32_bf16 v[228:231], a[8:11], a[120:123], v[228:231]\n" \
"ds_read_b64_tr_b16 a[8:9], v33 offset:27840\n" \
"ds_read_b64_tr_b16 a[10:11], v33 offset:32448\n" \
"v_mfma_f32_16x16x32_bf16 v[228:231], a[4:7], a[116:119], v[228:231]\n" \
"ds_read_b64_tr_b16 a[4:5], v33 offset:18624\n" \
"ds_read_b64_tr_b16 a[6:7], v33 offset:23232\n" \
"v_mfma_f32_16x16x32_bf16 v[228:231], a[0:3], a[112:115], v[228:231]\n" \
"ds_read_b64_tr_b16 a[0:1], v10 offset:55488\n" \
"ds_read_b64_tr_b16 a[2:3], v10 offset:60096\n" \
"v_mfma_f32_16x16x32_bf16 v[152:155], a[16:19], a[128:131], v[152:155]\n" \
"v_mfma_f32_16x16x32_bf16 v[152:155], a[20:23], a[132:135], v[152:155]\n" \
"v_mfma_f32_16x16x32_bf16 v[152:155], a[24:27], a[136:139], v[152:155]\n" \
"s_waitcnt lgkmcnt(15)\n" \
"v_mfma_f32_16x16x32_bf16 v[152:155], a[28:31], a[140:143], v[152:155]\n" \
"v_mfma_f32_16x16x32_bf16 v[156:159], a[28:31], a[156:159], v[156:159]\n" \
"v_mfma_f32_16x16x32_bf16 v[156:159], a[24:27], a[152:155], v[156:159]\n" \
"v_mfma_f32_16x16x32_bf16 v[156:159], a[20:23], a[148:151], v[156:159]\n" \
"v_mfma_f32_16x16x32_bf16 v[156:159], a[16:19], a[144:147], v[156:159]\n" \
"s_waitcnt vmcnt(8)\n" \
"s_mov_b64 exec, 3\n" \
"buffer_store_dword v39, v41, s[56:59], s69 offen sc1\n" \
"s_mov_b64 exec, -1\n" \
"v_mfma_f32_16x16x32_bf16 v[224:227], a[16:19], a[160:163], v[224:227]\n" \
"s_add_u32 s69, s63, 0x0\n" \
"v_mfma_f32_16x16x32_bf16 v[224:227], a[20:23], a[164:167], v[224:227]\n" \
"v_mfma_f32_16x16x32_bf16 v[224:227], a[24:27], a[168:171], v[224:227]\n" \
"v_mfma_f32_16x16x32_bf16 v[224:227], a[28:31], a[172:175], v[224:227]\n" \
"v_mfma_f32_16x16x32_bf16 v[228:231], a[28:31], a[188:191], v[228:231]\n" \
"ds_read_b64_tr_b16 a[28:29], v34 offset:18624\n" \
"ds_read_b64_tr_b16 a[30:31], v34 offset:23232\n" \
"v_mfma_f32_16x16x32_bf16 v[228:231], a[24:27], a[184:187], v[228:231]\n" \
"ds_read_b64_tr_b16 a[24:25], v34 offset:9408\n" \
"ds_read_b64_tr_b16 a[26:27], v34 offset:14016\n" \
"v_mfma_f32_16x16x32_bf16 v[228:231], a[20:23], a[180:183], v[228:231]\n" \
"ds_read_b64_tr_b16 a[20:21], v34 offset:192\n" \
"ds_read_b64_tr_b16 a[22:23], v34 offset:4800\n" \
"v_mfma_f32_16x16x32_bf16 v[228:231], a[16:19], a[176:179], v[228:231]\n" \
"ds_read_b64_tr_b16 a[16:17], v33 offset:46272\n" \
"ds_read_b64_tr_b16 a[18:19], v33 offset:50880\n" \
"v_mfma_f32_16x16x32_bf16 v[152:155], a[192:195], a[32:35], v[152:155]\n" \
"v_mfma_f32_16x16x32_bf16 v[152:155], a[196:199], a[36:39], v[152:155]\n" \
"v_mfma_f32_16x16x32_bf16 v[152:155], a[200:203], a[40:43], v[152:155]\n" \
"s_waitcnt lgkmcnt(8)\n" \
"v_mfma_f32_16x16x32_bf16 v[152:155], a[204:207], a[44:47], v[152:155]\n" \
"v_mfma_f32_16x16x32_bf16 v[156:159], a[204:207], a[60:63], v[156:159]\n" \
"v_mfma_f32_16x16x32_bf16 v[156:159], a[200:203], a[56:59], v[156:159]\n" \
"v_mfma_f32_16x16x32_bf16 v[156:159], a[196:199], a[52:55], v[156:159]\n" \
"v_mfma_f32_16x16x32_bf16 v[156:159], a[192:195], a[48:51], v[156:159]\n" \
"v_mfma_f32_16x16x32_bf16 v[224:227], a[192:195], a[220:223], v[224:227]\n" \
"v_mfma_f32_16x16x32_bf16 v[224:227], a[196:199], a[224:227], v[224:227]\n" \
"v_mfma_f32_16x16x32_bf16 v[224:227], a[200:203], a[228:231], v[224:227]\n" \
"v_mfma_f32_16x16x32_bf16 v[224:227], a[204:207], a[232:235], v[224:227]\n" \
"v_cvt_pk_bf16_f32 v152, v152, v153\n" \
"v_cvt_pk_bf16_f32 v153, v154, v155\n" \
"v_mfma_f32_16x16x32_bf16 v[228:231], a[204:207], a[248:251], v[228:231]\n" \
"ds_read_b64_tr_b16 a[204:205], v10 offset:46080\n" \
"v_cvt_pk_bf16_f32 v154, v156, v157\n" \
"v_cvt_pk_bf16_f32 v155, v158, v159\n" \
"buffer_store_dwordx4 v[152:155], v20, s[56:59], s72 offen offset:0 sc1\n" \
"v_mfma_f32_16x16x32_bf16 v[228:231], a[200:203], a[244:247], v[228:231]\n" \
"v_cvt_pk_bf16_f32 v224, v224, v225\n" \
"v_cvt_pk_bf16_f32 v225, v226, v227\n" \
"v_mfma_f32_16x16x32_bf16 v[228:231], a[196:199], a[240:243], v[228:231]\n" \
"ds_read_b64_tr_b16 a[196:197], v10 offset:27648\n" \
"ds_read_b64_tr_b16 a[198:199], v10 offset:32256\n" \
"v_mfma_f32_16x16x32_bf16 v[228:231], a[192:195], a[236:239], v[228:231]\n" \
"ds_read_b64_tr_b16 a[192:193], v33 offset:192\n" \
"ds_read_b64_tr_b16 a[194:195], v33 offset:4800\n" \
"v_mfma_f32_16x16x32_bf16 v[232:235], a[208:211], a[96:99], v[232:235]\n" \
"ds_read_b64_tr_b16 a[200:201], v10 offset:36864\n" \
"ds_read_b64_tr_b16 a[202:203], v10 offset:41472\n" \
"v_mfma_f32_16x16x32_bf16 v[232:235], a[212:215], a[100:103], v[232:235]\n" \
"ds_read_b64_tr_b16 a[206:207], v10 offset:50688\n" \
"v_mfma_f32_16x16x32_bf16 v[232:235], a[216:219], a[104:107], v[232:235]\n" \
"v_cvt_pk_bf16_f32 v226, v228, v229\n" \
"v_cvt_pk_bf16_f32 v227, v230, v231\n" \
"buffer_store_dwordx4 v[224:227], v20, s[56:59], s72 offen offset:1024 sc1\n" \
"s_add_u32 s72, s72, 0x1000\n" \
"v_mfma_f32_16x16x32_bf16 v[232:235], a[0:3], a[108:111], v[232:235]\n" \
"v_mfma_f32_16x16x32_bf16 v[236:239], a[0:3], a[124:127], v[236:239]\n" \
"ds_read_b64_tr_b16 a[0:1], v33 offset:36864\n" \
"ds_read_b64_tr_b16 a[2:3], v33 offset:41472\n" \
"v_mfma_f32_16x16x32_bf16 v[236:239], a[216:219], a[120:123], v[236:239]\n" \
"ds_read_b64_tr_b16 a[216:217], v33 offset:27648\n" \
"ds_read_b64_tr_b16 a[218:219], v33 offset:32256\n" \
"v_mfma_f32_16x16x32_bf16 v[236:239], a[212:215], a[116:119], v[236:239]\n" \
"ds_read_b64_tr_b16 a[212:213], v33 offset:18432\n" \
"ds_read_b64_tr_b16 a[214:215], v33 offset:23040\n" \
"v_mfma_f32_16x16x32_bf16 v[236:239], a[208:211], a[112:115], v[236:239]\n" \
"ds_read_b64_tr_b16 a[208:209], v10 offset:55296\n" \
"ds_read_b64_tr_b16 a[210:211], v10 offset:59904\n" \
"v_mfma_f32_16x16x32_bf16 v[160:163], a[4:7], a[128:131], v[160:163]\n" \
"v_mfma_f32_16x16x32_bf16 v[160:163], a[8:11], a[132:135], v[160:163]\n" \
"v_mfma_f32_16x16x32_bf16 v[160:163], a[12:15], a[136:139], v[160:163]\n" \
"s_waitcnt lgkmcnt(15)\n" \
"v_mfma_f32_16x16x32_bf16 v[160:163], a[16:19], a[140:143], v[160:163]\n" \
"v_mfma_f32_16x16x32_bf16 v[164:167], a[16:19], a[156:159], v[164:167]\n" \
"v_mfma_f32_16x16x32_bf16 v[164:167], a[12:15], a[152:155], v[164:167]\n" \
"s_waitcnt vmcnt(5)\n" \
"s_mov_b64 exec, 1\n" \
"buffer_store_dword v39, off, s[56:59], s70 offset:16 sc1\n" \
"s_mov_b64 exec, -1\n" \
"v_mfma_f32_16x16x32_bf16 v[164:167], a[8:11], a[148:151], v[164:167]\n" \
"s_add_u32 s70, s63, 0x800\n" \
"v_mfma_f32_16x16x32_bf16 v[164:167], a[4:7], a[144:147], v[164:167]\n" \
"v_mfma_f32_16x16x32_bf16 v[232:235], a[4:7], a[160:163], v[232:235]\n" \
"v_mfma_f32_16x16x32_bf16 v[232:235], a[8:11], a[164:167], v[232:235]\n" \
"v_mfma_f32_16x16x32_bf16 v[232:235], a[12:15], a[168:171], v[232:235]\n" \
"v_mfma_f32_16x16x32_bf16 v[232:235], a[16:19], a[172:175], v[232:235]\n" \
"v_mfma_f32_16x16x32_bf16 v[236:239], a[16:19], a[188:191], v[236:239]\n" \
"ds_read_b64_tr_b16 a[16:17], v34 offset:18432\n" \
"ds_read_b64_tr_b16 a[18:19], v34 offset:23040\n" \
"v_mfma_f32_16x16x32_bf16 v[236:239], a[12:15], a[184:187], v[236:239]\n" \
"ds_read_b64_tr_b16 a[12:13], v34 offset:9216\n" \
"ds_read_b64_tr_b16 a[14:15], v34 offset:13824\n" \
"v_mfma_f32_16x16x32_bf16 v[236:239], a[8:11], a[180:183], v[236:239]\n" \
"ds_read_b64_tr_b16 a[8:9], v34 offset:0\n" \
"ds_read_b64_tr_b16 a[10:11], v34 offset:4608\n" \
"v_mfma_f32_16x16x32_bf16 v[236:239], a[4:7], a[176:179], v[236:239]\n" \
"ds_read_b64_tr_b16 a[4:5], v33 offset:46080\n" \
"ds_read_b64_tr_b16 a[6:7], v33 offset:50688\n" \
"v_mfma_f32_16x16x32_bf16 v[160:163], a[20:23], a[32:35], v[160:163]\n" \
"v_mfma_f32_16x16x32_bf16 v[160:163], a[24:27], a[36:39], v[160:163]\n" \
"v_mfma_f32_16x16x32_bf16 v[160:163], a[28:31], a[40:43], v[160:163]\n" \
"s_waitcnt lgkmcnt(8)\n" \
"v_mfma_f32_16x16x32_bf16 v[160:163], a[192:195], a[44:47], v[160:163]\n" \
"v_mfma_f32_16x16x32_bf16 v[164:167], a[192:195], a[60:63], v[164:167]\n" \
"v_mfma_f32_16x16x32_bf16 v[164:167], a[28:31], a[56:59], v[164:167]\n" \
"v_mfma_f32_16x16x32_bf16 v[164:167], a[24:27], a[52:55], v[164:167]\n" \
"v_mfma_f32_16x16x32_bf16 v[164:167], a[20:23], a[48:51], v[164:167]\n" \
"v_mfma_f32_16x16x32_bf16 v[232:235], a[20:23], a[220:223], v[232:235]\n" \
"v_mfma_f32_16x16x32_bf16 v[232:235], a[24:27], a[224:227], v[232:235]\n" \
"v_mfma_f32_16x16x32_bf16 v[232:235], a[28:31], a[228:231], v[232:235]\n" \
"v_mfma_f32_16x16x32_bf16 v[232:235], a[192:195], a[232:235], v[232:235]\n" \
"v_cvt_pk_bf16_f32 v160, v160, v161\n" \
"v_cvt_pk_bf16_f32 v161, v162, v163\n" \
"v_mfma_f32_16x16x32_bf16 v[236:239], a[192:195], a[248:251], v[236:239]\n" \
"ds_read_b64_tr_b16 a[192:193], v10 offset:46112\n" \
"v_cvt_pk_bf16_f32 v162, v164, v165\n" \
"v_cvt_pk_bf16_f32 v163, v166, v167\n" \
"buffer_store_dwordx4 v[160:163], v20, s[56:59], s73 offen offset:0 sc1\n" \
"v_mfma_f32_16x16x32_bf16 v[236:239], a[28:31], a[244:247], v[236:239]\n" \
"v_cvt_pk_bf16_f32 v232, v232, v233\n" \
"v_cvt_pk_bf16_f32 v233, v234, v235\n" \
"v_mfma_f32_16x16x32_bf16 v[236:239], a[24:27], a[240:243], v[236:239]\n" \
"ds_read_b64_tr_b16 a[24:25], v10 offset:27680\n" \
"ds_read_b64_tr_b16 a[26:27], v10 offset:32288\n" \
"v_mfma_f32_16x16x32_bf16 v[236:239], a[20:23], a[236:239], v[236:239]\n" \
"ds_read_b64_tr_b16 a[20:21], v33 offset:0\n" \
"ds_read_b64_tr_b16 a[22:23], v33 offset:4608\n" \
"v_mfma_f32_16x16x32_bf16 v[184:187], a[196:199], a[96:99], v[184:187]\n" \
"ds_read_b64_tr_b16 a[28:29], v10 offset:36896\n" \
"ds_read_b64_tr_b16 a[30:31], v10 offset:41504\n" \
"v_mfma_f32_16x16x32_bf16 v[184:187], a[200:203], a[100:103], v[184:187]\n" \
"ds_read_b64_tr_b16 a[194:195], v10 offset:50720\n" \
"v_mfma_f32_16x16x32_bf16 v[184:187], a[204:207], a[104:107], v[184:187]\n" \
"v_cvt_pk_bf16_f32 v234, v236, v237\n" \
"v_cvt_pk_bf16_f32 v235, v238, v239\n" \
"buffer_store_dwordx4 v[232:235], v20, s[56:59], s73 offen offset:1024 sc1\n" \
"s_add_u32 s73, s73, 0x800\n" \
"v_mfma_f32_16x16x32_bf16 v[184:187], a[208:211], a[108:111], v[184:187]\n" \
"v_mfma_f32_16x16x32_bf16 v[188:191], a[208:211], a[124:127], v[188:191]\n" \
"ds_read_b64_tr_b16 a[208:209], v33 offset:36896\n" \
"ds_read_b64_tr_b16 a[210:211], v33 offset:41504\n" \
"v_mfma_f32_16x16x32_bf16 v[188:191], a[204:207], a[120:123], v[188:191]\n" \
"ds_read_b64_tr_b16 a[204:205], v33 offset:27680\n" \
"ds_read_b64_tr_b16 a[206:207], v33 offset:32288\n" \
"v_mfma_f32_16x16x32_bf16 v[188:191], a[200:203], a[116:119], v[188:191]\n" \
"ds_read_b64_tr_b16 a[200:201], v33 offset:18464\n" \
"ds_read_b64_tr_b16 a[202:203], v33 offset:23072\n" \
"v_mfma_f32_16x16x32_bf16 v[188:191], a[196:199], a[112:115], v[188:191]\n" \
"ds_read_b64_tr_b16 a[196:197], v10 offset:55328\n" \
"ds_read_b64_tr_b16 a[198:199], v10 offset:59936\n" \
"v_mfma_f32_16x16x32_bf16 v[184:187], a[212:215], a[160:163], v[184:187]\n" \
"v_mfma_f32_16x16x32_bf16 v[184:187], a[216:219], a[164:167], v[184:187]\n" \
"v_mfma_f32_16x16x32_bf16 v[184:187], a[0:3], a[168:171], v[184:187]\n" \
"s_waitcnt lgkmcnt(15)\n" \
"v_mfma_f32_16x16x32_bf16 v[184:187], a[4:7], a[172:175], v[184:187]\n" \
"v_mfma_f32_16x16x32_bf16 v[188:191], a[4:7], a[188:191], v[188:191]\n" \
"ds_read_b64_tr_b16 a[4:5], v34 offset:18464\n" \
"ds_read_b64_tr_b16 a[6:7], v34 offset:23072\n" \
"s_waitcnt vmcnt(6)\n" \
"s_mov_b64 exec, 1\n" \
"buffer_store_dword v39, off, s[56:59], s71 offset:20 sc1\n" \
"s_mov_b64 exec, -1\n" \
"v_mfma_f32_16x16x32_bf16 v[188:191], a[0:3], a[184:187], v[188:191]\n" \
"ds_read_b64_tr_b16 a[0:1], v34 offset:9248\n" \
"ds_read_b64_tr_b16 a[2:3], v34 offset:13856\n" \
"v_mfma_f32_16x16x32_bf16 v[188:191], a[216:219], a[180:183], v[188:191]\n" \
"ds_read_b64_tr_b16 a[216:217], v34 offset:32\n" \
"ds_read_b64_tr_b16 a[218:219], v34 offset:4640\n" \
"v_mfma_f32_16x16x32_bf16 v[188:191], a[212:215], a[176:179], v[188:191]\n" \
"ds_read_b64_tr_b16 a[212:213], v33 offset:46112\n" \
"ds_read_b64_tr_b16 a[214:215], v33 offset:50720\n" \
"v_mfma_f32_16x16x32_bf16 v[184:187], a[8:11], a[220:223], v[184:187]\n" \
"s_add_u32 s71, s63, 0x1000\n" \
"v_mfma_f32_16x16x32_bf16 v[184:187], a[12:15], a[224:227], v[184:187]\n" \
"v_mfma_f32_16x16x32_bf16 v[184:187], a[16:19], a[228:231], v[184:187]\n" \
"s_waitcnt lgkmcnt(12)\n" \
"v_mfma_f32_16x16x32_bf16 v[184:187], a[20:23], a[232:235], v[184:187]\n" \
"v_mfma_f32_16x16x32_bf16 v[188:191], a[20:23], a[248:251], v[188:191]\n" \
"ds_read_b64_tr_b16 a[20:21], v10 offset:46144\n" \
"ds_read_b64_tr_b16 a[22:23], v10 offset:50752\n" \
"v_mfma_f32_16x16x32_bf16 v[188:191], a[16:19], a[244:247], v[188:191]\n" \
"ds_read_b64_tr_b16 a[16:17], v10 offset:36928\n" \
"ds_read_b64_tr_b16 a[18:19], v10 offset:41536\n" \
"v_mfma_f32_16x16x32_bf16 v[188:191], a[12:15], a[240:243], v[188:191]\n" \
"ds_read_b64_tr_b16 a[12:13], v10 offset:27712\n" \
"v_cvt_pk_bf16_f32 v184, v184, v185\n" \
"v_cvt_pk_bf16_f32 v185, v186, v187\n" \
"v_mfma_f32_16x16x32_bf16 v[188:191], a[8:11], a[236:239], v[188:191]\n" \
"ds_read_b64_tr_b16 a[8:9], v33 offset:32\n" \
"ds_read_b64_tr_b16 a[10:11], v33 offset:4640\n" \
"v_mfma_f32_16x16x32_bf16 v[192:195], a[24:27], a[96:99], v[192:195]\n" \
"ds_read_b64_tr_b16 a[14:15], v10 offset:32320\n" \
"v_mfma_f32_16x16x32_bf16 v[192:195], a[28:31], a[100:103], v[192:195]\n" \
"v_mfma_f32_16x16x32_bf16 v[192:195], a[192:195], a[104:107], v[192:195]\n" \
"s_waitcnt lgkmcnt(14)\n" \
"v_mfma_f32_16x16x32_bf16 v[192:195], a[196:199], a[108:111], v[192:195]\n" \
"v_cvt_pk_bf16_f32 v186, v188, v189\n" \
"v_cvt_pk_bf16_f32 v187, v190, v191\n" \
"buffer_store_dwordx4 v[184:187], v20, s[56:59], s92 offen offset:1024 sc1\n" \
"s_add_u32 s92, s92, 0x3800\n" \
"v_mfma_f32_16x16x32_bf16 v[196:199], a[196:199], a[124:127], v[196:199]\n" \
"ds_read_b64_tr_b16 a[196:197], v33 offset:36928\n" \
"ds_read_b64_tr_b16 a[198:199], v33 offset:41536\n" \
"v_mfma_f32_16x16x32_bf16 v[196:199], a[192:195], a[120:123], v[196:199]\n" \
"ds_read_b64_tr_b16 a[192:193], v33 offset:27712\n" \
"ds_read_b64_tr_b16 a[194:195], v33 offset:32320\n" \
"v_mfma_f32_16x16x32_bf16 v[196:199], a[28:31], a[116:119], v[196:199]\n" \
"ds_read_b64_tr_b16 a[28:29], v33 offset:18496\n" \
"ds_read_b64_tr_b16 a[30:31], v33 offset:23104\n" \
"v_mfma_f32_16x16x32_bf16 v[196:199], a[24:27], a[112:115], v[196:199]\n" \
"ds_read_b64_tr_b16 a[24:25], v10 offset:55360\n" \
"ds_read_b64_tr_b16 a[26:27], v10 offset:59968\n" \
"v_mfma_f32_16x16x32_bf16 v[192:195], a[200:203], a[160:163], v[192:195]\n" \
"v_mfma_f32_16x16x32_bf16 v[192:195], a[204:207], a[164:167], v[192:195]\n" \
"v_mfma_f32_16x16x32_bf16 v[192:195], a[208:211], a[168:171], v[192:195]\n" \
"s_waitcnt lgkmcnt(13)\n" \
"v_mfma_f32_16x16x32_bf16 v[192:195], a[212:215], a[172:175], v[192:195]\n" \
"v_mfma_f32_16x16x32_bf16 v[196:199], a[212:215], a[188:191], v[196:199]\n" \
"ds_read_b64_tr_b16 a[212:213], v34 offset:18496\n" \
"ds_read_b64_tr_b16 a[214:215], v34 offset:23104\n" \
"v_mfma_f32_16x16x32_bf16 v[196:199], a[208:211], a[184:187], v[196:199]\n" \
"ds_read_b64_tr_b16 a[208:209], v34 offset:9280\n" \
"ds_read_b64_tr_b16 a[210:211], v34 offset:13888\n" \
"v_mfma_f32_16x16x32_bf16 v[196:199], a[204:207], a[180:183], v[196:199]\n" \
"ds_read_b64_tr_b16 a[204:205], v34 offset:64\n" \
"ds_read_b64_tr_b16 a[206:207], v34 offset:4672\n" \
"v_mfma_f32_16x16x32_bf16 v[196:199], a[200:203], a[176:179], v[196:199]\n" \
"ds_read_b64_tr_b16 a[200:201], v33 offset:46144\n" \
"ds_read_b64_tr_b16 a[202:203], v33 offset:50752\n" \
"v_mfma_f32_16x16x32_bf16 v[192:195], a[216:219], a[220:223], v[192:195]\n" \
"v_mfma_f32_16x16x32_bf16 v[192:195], a[0:3], a[224:227], v[192:195]\n" \
"L_pf22:\n" \
"s_cbranch_execz L_retB\n" \
"s_nop 0\n" \
"v_mfma_f32_16x16x32_bf16 v[192:195], a[4:7], a[228:231], v[192:195]\n" \
"s_waitcnt lgkmcnt(15)\n" \
"v_mfma_f32_16x16x32_bf16 v[192:195], a[8:11], a[232:235], v[192:195]\n" \
"v_mfma_f32_16x16x32_bf16 v[196:199], a[8:11], a[248:251], v[196:199]\n" \
"ds_read_b64_tr_b16 a[8:9], v10 offset:46304\n" \
"ds_read_b64_tr_b16 a[10:11], v10 offset:50912\n" \
"v_mfma_f32_16x16x32_bf16 v[196:199], a[4:7], a[244:247], v[196:199]\n" \
"ds_read_b64_tr_b16 a[4:5], v10 offset:37088\n" \
"ds_read_b64_tr_b16 a[6:7], v10 offset:41696\n" \
"v_mfma_f32_16x16x32_bf16 v[196:199], a[0:3], a[240:243], v[196:199]\n" \
"ds_read_b64_tr_b16 a[0:1], v10 offset:27872\n" \
"v_cvt_pk_bf16_f32 v192, v192, v193\n" \
"v_cvt_pk_bf16_f32 v193, v194, v195\n" \
"v_mfma_f32_16x16x32_bf16 v[196:199], a[216:219], a[236:239], v[196:199]\n" \
"ds_read_b64_tr_b16 a[216:217], v33 offset:64\n" \
"ds_read_b64_tr_b16 a[218:219], v33 offset:4672\n" \
"v_mfma_f32_16x16x32_bf16 v[200:203], a[12:15], a[96:99], v[200:203]\n" \
"ds_read_b64_tr_b16 a[2:3], v10 offset:32480\n" \
"v_mfma_f32_16x16x32_bf16 v[200:203], a[16:19], a[100:103], v[200:203]\n" \
"v_mfma_f32_16x16x32_bf16 v[200:203], a[20:23], a[104:107], v[200:203]\n" \
"s_waitcnt lgkmcnt(14)\n" \
"v_mfma_f32_16x16x32_bf16 v[200:203], a[24:27], a[108:111], v[200:203]\n" \
"v_cvt_pk_bf16_f32 v194, v196, v197\n" \
"v_cvt_pk_bf16_f32 v195, v198, v199\n" \
"buffer_store_dwordx4 v[192:195], v20, s[56:59], s93 offen offset:1024 sc1\n" \
"s_waitcnt vmcnt(6)\n" \
"s_mov_b64 exec, 1\n" \
"buffer_store_dword v39, off, s[56:59], s72 offset:24 sc1\n" \
"s_mov_b64 exec, -1\n" \
"s_add_u32 s93, s93, 0x3000\n" \
"v_mfma_f32_16x16x32_bf16 v[204:207], a[24:27], a[124:127], v[204:207]\n" \
"ds_read_b64_tr_b16 a[24:25], v33 offset:37088\n" \
"ds_read_b64_tr_b16 a[26:27], v33 offset:41696\n" \
"v_mfma_f32_16x16x32_bf16 v[204:207], a[20:23], a[120:123], v[204:207]\n" \
"ds_read_b64_tr_b16 a[20:21], v33 offset:27872\n" \
"ds_read_b64_tr_b16 a[22:23], v33 offset:32480\n" \
"v_mfma_f32_16x16x32_bf16 v[204:207], a[16:19], a[116:119], v[204:207]\n" \
"ds_read_b64_tr_b16 a[16:17], v33 offset:18656\n" \
"ds_read_b64_tr_b16 a[18:19], v33 offset:23264\n" \
"v_mfma_f32_16x16x32_bf16 v[204:207], a[12:15], a[112:115], v[204:207]\n" \
"ds_read_b64_tr_b16 a[12:13], v10 offset:55520\n" \
"ds_read_b64_tr_b16 a[14:15], v10 offset:60128\n" \
"v_mfma_f32_16x16x32_bf16 v[200:203], a[28:31], a[160:163], v[200:203]\n" \
"v_readlane_b32 s72, v11, 51\n" \
"v_mfma_f32_16x16x32_bf16 v[200:203], a[192:195], a[164:167], v[200:203]\n" \
"v_mfma_f32_16x16x32_bf16 v[200:203], a[196:199], a[168:171], v[200:203]\n" \
"s_waitcnt lgkmcnt(11)\n" \
"v_mfma_f32_16x16x32_bf16 v[200:203], a[200:203], a[172:175], v[200:203]\n" \
"v_mfma_f32_16x16x32_bf16 v[204:207], a[200:203], a[188:191], v[204:207]\n" \
"ds_read_b64_tr_b16 a[200:201], v34 offset:18656\n" \
"ds_read_b64_tr_b16 a[202:203], v34 offset:23264\n" \
"v_mfma_f32_16x16x32_bf16 v[204:207], a[196:199], a[184:187], v[204:207]\n" \
"ds_read_b64_tr_b16 a[196:197], v34 offset:9440\n" \
"ds_read_b64_tr_b16 a[198:199], v34 offset:14048\n" \
"v_mfma_f32_16x16x32_bf16 v[204:207], a[192:195], a[180:183], v[204:207]\n" \
"ds_read_b64_tr_b16 a[192:193], v34 offset:224\n" \
"ds_read_b64_tr_b16 a[194:195], v34 offset:4832\n" \
"v_mfma_f32_16x16x32_bf16 v[204:207], a[28:31], a[176:179], v[204:207]\n" \
"ds_read_b64_tr_b16 a[28:29], v33 offset:46304\n" \
"ds_read_b64_tr_b16 a[30:31], v33 offset:50912\n" \
"v_mfma_f32_16x16x32_bf16 v[200:203], a[204:207], a[220:223], v[200:203]\n" \
"v_mfma_f32_16x16x32_bf16 v[200:203], a[208:211], a[224:227], v[200:203]\n" \
"v_mfma_f32_16x16x32_bf16 v[200:203], a[212:215], a[228:231], v[200:203]\n" \
"s_waitcnt lgkmcnt(15)\n" \
"v_mfma_f32_16x16x32_bf16 v[200:203], a[216:219], a[232:235], v[200:203]\n" \
"v_mfma_f32_16x16x32_bf16 v[204:207], a[216:219], a[248:251], v[204:207]\n" \
"ds_read_b64_tr_b16 a[216:217], v36 offset:32\n" \
"ds_read_b64_tr_b16 a[218:219], v36 offset:1056\n" \
"v_mfma_f32_16x16x32_bf16 v[204:207], a[212:215], a[244:247], v[204:207]\n" \
"ds_read_b64_tr_b16 a[212:213], v36 offset:2048\n" \
"ds_read_b64_tr_b16 a[214:215], v36 offset:3072\n" \
"v_mfma_f32_16x16x32_bf16 v[204:207], a[208:211], a[240:243], v[204:207]\n" \
"ds_read_b64_tr_b16 a[208:209], v36 offset:0\n" \
"v_cvt_pk_bf16_f32 v200, v200, v201\n" \
"v_cvt_pk_bf16_f32 v201, v202, v203\n" \
"v_mfma_f32_16x16x32_bf16 v[204:207], a[204:207], a[236:239], v[204:207]\n" \
"ds_read_b64_tr_b16 a[204:205], v33 offset:224\n" \
"ds_read_b64_tr_b16 a[206:207], v33 offset:4832\n" \
"v_mfma_f32_16x16x32_bf16 v[240:243], a[0:3], a[96:99], v[240:243]\n" \
"ds_read_b64_tr_b16 a[210:211], v36 offset:1024\n" \
"v_mfma_f32_16x16x32_bf16 v[240:243], a[4:7], a[100:103], v[240:243]\n" \
"v_mfma_f32_16x16x32_bf16 v[240:243], a[8:11], a[104:107], v[240:243]\n" \
"s_waitcnt lgkmcnt(14)\n" \
"v_mfma_f32_16x16x32_bf16 v[240:243], a[12:15], a[108:111], v[240:243]\n" \
"v_cvt_pk_bf16_f32 v202, v204, v205\n" \
"v_cvt_pk_bf16_f32 v203, v206, v207\n" \
"buffer_store_dwordx4 v[200:203], v20, s[56:59], s94 offen offset:1024 sc1\n" \
"s_add_u32 s94, s94, 0x2800\n" \
"v_mfma_f32_16x16x32_bf16 v[244:247], a[12:15], a[124:127], v[244:247]\n" \
"v_mfma_f32_16x16x32_bf16 v[244:247], a[8:11], a[120:123], v[244:247]\n" \
"v_mfma_f32_16x16x32_bf16 v[244:247], a[4:7], a[116:119], v[244:247]\n" \
"v_mfma_f32_16x16x32_bf16 v[244:247], a[0:3], a[112:115], v[244:247]\n" \
"ds_read_b64_tr_b16 a[0:1], v36 offset:2080\n" \
"ds_read_b64_tr_b16 a[2:3], v36 offset:3104\n" \
"v_mfma_f32_16x16x32_bf16 v[168:171], a[16:19], a[128:131], v[168:171]\n" \
"v_mfma_f32_16x16x32_bf16 v[168:171], a[20:23], a[132:135], v[168:171]\n" \
"v_mfma_f32_16x16x32_bf16 v[168:171], a[24:27], a[136:139], v[168:171]\n" \
"s_waitcnt lgkmcnt(7)\n" \
"v_mfma_f32_16x16x32_bf16 v[168:171], a[28:31], a[140:143], v[168:171]\n" \
"v_mfma_f32_16x16x32_bf16 v[172:175], a[28:31], a[156:159], v[172:175]\n" \
"v_mfma_f32_16x16x32_bf16 v[172:175], a[24:27], a[152:155], v[172:175]\n" \
"v_mfma_f32_16x16x32_bf16 v[172:175], a[20:23], a[148:151], v[172:175]\n" \
"v_mfma_f32_16x16x32_bf16 v[172:175], a[16:19], a[144:147], v[172:175]\n" \
"v_mfma_f32_16x16x32_bf16 v[240:243], a[16:19], a[160:163], v[240:243]\n" \
"v_mfma_f32_16x16x32_bf16 v[240:243], a[20:23], a[164:167], v[240:243]\n" \
"v_mfma_f32_16x16x32_bf16 v[240:243], a[24:27], a[168:171], v[240:243]\n" \
"v_mfma_f32_16x16x32_bf16 v[240:243], a[28:31], a[172:175], v[240:243]\n" \
"s_waitcnt vmcnt(5)\n" \
"s_mov_b64 exec, 1\n" \
"buffer_store_dword v39, off, s[56:59], s73 offset:28 sc1\n" \
"s_mov_b64 exec, -1\n" \
"v_mfma_f32_16x16x32_bf16 v[244:247], a[28:31], a[188:191], v[244:247]\n" \
"v_readlane_b32 s73, v11, 42\n" \
"v_mfma_f32_16x16x32_bf16 v[244:247], a[24:27], a[184:187], v[244:247]\n" \
"v_mfma_f32_16x16x32_bf16 v[244:247], a[20:23], a[180:183], v[244:247]\n" \
"v_mfma_f32_16x16x32_bf16 v[244:247], a[16:19], a[176:179], v[244:247]\n" \
"v_mfma_f32_16x16x32_bf16 v[168:171], a[192:195], a[32:35], v[168:171]\n" \
"v_mfma_f32_16x16x32_bf16 v[168:171], a[196:199], a[36:39], v[168:171]\n" \
"v_mfma_f32_16x16x32_bf16 v[168:171], a[200:203], a[40:43], v[168:171]\n" \
"s_waitcnt lgkmcnt(0)\n" \
"v_mfma_f32_16x16x32_bf16 v[168:171], a[204:207], a[44:47], v[168:171]\n" \
"v_mfma_f32_16x16x32_bf16 v[172:175], a[204:207], a[60:63], v[172:175]\n" \
"v_mfma_f32_16x16x32_bf16 v[172:175], a[200:203], a[56:59], v[172:175]\n" \
"v_mfma_f32_16x16x32_bf16 v[172:175], a[196:199], a[52:55], v[172:175]\n" \
"v_mfma_f32_16x16x32_bf16 v[172:175], a[192:195], a[48:51], v[172:175]\n" \
"v_mfma_f32_16x16x32_bf16 v[240:243], a[192:195], a[220:223], v[240:243]\n" \
"v_mfma_f32_16x16x32_bf16 v[240:243], a[196:199], a[224:227], v[240:243]\n" \
"v_mfma_f32_16x16x32_bf16 v[240:243], a[200:203], a[228:231], v[240:243]\n" \
"v_mfma_f32_16x16x32_bf16 v[240:243], a[204:207], a[232:235], v[240:243]\n" \
"v_mfma_f32_16x16x32_bf16 v[244:247], a[204:207], a[248:251], v[244:247]\n" \
"v_mfma_f32_16x16x32_bf16 v[244:247], a[200:203], a[244:247], v[244:247]\n" \
"v_mfma_f32_16x16x32_bf16 v[244:247], a[196:199], a[240:243], v[244:247]\n" \
"v_mfma_f32_16x16x32_bf16 v[244:247], a[192:195], a[236:239], v[244:247]\n" \
"v_mfma_f32_16x16x32_bf16 v[168:171], a[208:211], v[64:67], v[168:171]\n" \
"v_mfma_f32_16x16x32_bf16 v[172:175], a[208:211], v[72:75], v[172:175]\n" \
"v_mfma_f32_16x16x32_bf16 v[240:243], a[208:211], v[80:83], v[240:243]\n" \
"v_mfma_f32_16x16x32_bf16 v[244:247], a[208:211], v[88:91], v[244:247]\n" \
"v_mfma_f32_16x16x32_bf16 v[168:171], a[212:215], v[68:71], v[168:171]\n" \
"v_mfma_f32_16x16x32_bf16 v[172:175], a[212:215], v[76:79], v[172:175]\n" \
"s_waitcnt vmcnt(4)\n" \
"s_mov_b64 exec, 1\n" \
"buffer_store_dword v39, off, s[56:59], s92 offset:32 sc1\n" \
"s_mov_b64 exec, -1\n" \
"v_mfma_f32_16x16x32_bf16 v[240:243], a[212:215], v[84:87], v[240:243]\n" \
"v_readlane_b32 s92, v11, 40\n" \
"v_mfma_f32_16x16x32_bf16 v[244:247], a[212:215], v[92:95], v[244:247]\n" \
"v_mfma_f32_16x16x32_bf16 v[56:59], a[216:219], v[96:99], 0\n" \
"v_mfma_f32_16x16x32_bf16 v[60:63], a[216:219], v[104:107], 0\n" \
"v_mfma_f32_16x16x32_bf16 v[56:59], a[0:3], v[100:103], v[56:59]\n" \
"v_mfma_f32_16x16x32_bf16 v[60:63], a[0:3], v[108:111], v[60:63]\n" \
"s_waitcnt vmcnt(4)\n" \
"s_mov_b64 exec, 1\n" \
"buffer_store_dword v39, off, s[56:59], s93 offset:36 sc1\n" \
"s_mov_b64 exec, -1\n" \
"v_readlane_b32 s93, v11, 41\n" \
"s_waitcnt vmcnt(3)\n" \
"s_mov_b64 exec, 1\n" \
"buffer_store_dword v39, off, s[56:59], s94 offset:40 sc1\n" \
"s_mov_b64 exec, -1\n" \
"buffer_load_dword v37, v40, s[56:59], s74 offen sc1\n" \
"s_waitcnt vmcnt(0)\n" \
"v_cmp_eq_u32_e64 s[98:99], s62, v37\n" \
"s_nop 0\n" \
"s_andn2_b32 s75, 0x1, s98\n" \
"s_cbranch_scc1 L_sx0_%=\n" \
"L_rx0_%=:\n" \
"buffer_load_dwordx4 v[116:119], v20, s[56:59], s95 offen offset:0 sc1\n" \
"s_andn2_b32 s75, 0x2, s98\n" \
"s_cbranch_scc1 L_sx1_%=\n" \
"L_rx1_%=:\n" \
"buffer_load_dwordx4 v[124:127], v20, s[56:59], s96 offen offset:0 sc1\n" \
"s_andn2_b32 s75, 0x4, s98\n" \
"s_cbranch_scc1 L_sx2_%=\n" \
"L_rx2_%=:\n" \
"buffer_load_dwordx4 v[132:135], v20, s[56:59], s97 offen offset:0 sc1\n" \
"s_andn2_b32 s75, s100, s98\n" \
"s_cbranch_scc1 L_sx3_%=\n" \
"L_rx3_%=:\n" \
"buffer_load_dwordx4 v[136:139], v20, s[56:59], s64 offen offset:0 sc1\n" \
"buffer_load_dwordx4 v[208:211], v20, s[56:59], s64 offen offset:2048 sc1\n" \
"s_add_u32 s64, s64, 0x1000\n" \
"buffer_load_dwordx4 v[144:147], v20, s[56:59], s64 offen offset:0 sc1\n" \
"buffer_load_dwordx4 v[216:219], v20, s[56:59], s64 offen offset:2048 sc1\n" \
"s_add_u32 s64, s64, 0x1000\n" \
"buffer_load_dwordx4 v[152:155], v20, s[56:59], s64 offen offset:0 sc1\n" \
"buffer_load_dwordx4 v[224:227], v20, s[56:59], s64 offen offset:2048 sc1\n" \
"s_add_u32 s64, s64, 0x1000\n" \
"buffer_load_dwordx4 v[176:179], v20, s[56:59], s64 offen offset:0 sc1\n" \
"buffer_load_dwordx4 v[248:251], v20, s[56:59], s64 offen offset:2048 sc1\n" \
"s_andn2_b32 s75, 0x10, s98\n" \
"s_cbranch_scc1 L_sx4_%=\n" \
"L_rx4_%=:\n" \
"buffer_load_dwordx4 v[140:143], v20, s[56:59], s65 offen offset:0 sc1\n" \
"buffer_load_dwordx4 v[212:215], v20, s[56:59], s65 offen offset:1024 sc1\n" \
"s_andn2_b32 s75, 0x20, s98\n" \
"s_cbranch_scc1 L_sx5_%=\n" \
"L_rx5_%=:\n" \
"buffer_load_dwordx4 v[148:151], v20, s[56:59], s66 offen offset:0 sc1\n" \
"buffer_load_dwordx4 v[220:223], v20, s[56:59], s66 offen offset:1024 sc1\n" \
"s_andn2_b32 s75, 0x40, s98\n" \
"s_cbranch_scc1 L_sx6_%=\n" \
"L_rx6_%=:\n" \
"buffer_load_dwordx4 v[156:159], v20, s[56:59], s67 offen offset:0 sc1\n" \
"buffer_load_dwordx4 v[228:231], v20, s[56:59], s67 offen offset:1024 sc1\n" \
"s_andn2_b32 s75, 0x80, s98\n" \
"s_cbranch_scc1 L_sx7_%=\n" \
"L_rx7_%=:\n" \
"buffer_load_dwordx4 v[164:167], v20, s[56:59], s68 offen offset:0 sc1\n" \
"buffer_load_dwordx4 v[236:239], v20, s[56:59], s68 offen offset:1024 sc1\n" \
"s_andn2_b32 s75, 0x100, s98\n" \
"s_cbranch_scc1 L_sx8_%=\n" \
"L_rx8_%=:\n" \
"buffer_load_dwordx4 v[188:191], v20, s[56:59], s69 offen offset:1024 sc1\n" \
"s_andn2_b32 s75, 0x200, s98\n" \
"s_cbranch_scc1 L_sx9_%=\n" \
"L_rx9_%=:\n" \
"buffer_load_dwordx4 v[196:199], v20, s[56:59], s70 offen offset:1024 sc1\n" \
"s_andn2_b32 s75, 0x400, s98\n" \
"s_cbranch_scc1 L_sx10_%=\n" \
"L_rx10_%=:\n" \
"buffer_load_dwordx4 v[204:207], v20, s[56:59], s71 offen offset:1024 sc1\n" \
"s_waitcnt vmcnt(21)\n" \
"v_mfma_f32_16x16x32_bf16 v[168:171], v[50:53], v[116:119], v[168:171]\n" \
"v_mfma_f32_16x16x32_bf16 v[172:175], v[48:51], v[116:119], v[172:175]\n" \
"s_waitcnt vmcnt(20)\n" \
"v_mfma_f32_16x16x32_bf16 v[168:171], v[50:53], v[124:127], v[168:171]\n" \
"v_mfma_f32_16x16x32_bf16 v[172:175], v[48:51], v[124:127], v[172:175]\n" \
"s_waitcnt vmcnt(19)\n" \
"v_mfma_f32_16x16x32_bf16 v[168:171], v[50:53], v[132:135], v[168:171]\n" \
"v_mfma_f32_16x16x32_bf16 v[172:175], v[48:51], v[132:135], v[172:175]\n" \
"s_waitcnt vmcnt(18)\n" \
"v_mfma_f32_16x16x32_bf16 v[56:59], v[50:53], v[136:139], v[56:59]\n" \
"v_mfma_f32_16x16x32_bf16 v[60:63], v[48:51], v[136:139], v[60:63]\n" \
"s_waitcnt vmcnt(17)\n" \
"v_mfma_f32_16x16x32_bf16 v[56:59], v[50:53], v[208:211], v[56:59]\n" \
"v_mfma_f32_16x16x32_bf16 v[60:63], v[48:51], v[208:211], v[60:63]\n" \
"s_waitcnt vmcnt(16)\n" \
"v_mfma_f32_16x16x32_bf16 v[56:59], v[50:53], v[144:147], v[56:59]\n" \
"v_mfma_f32_16x16x32_bf16 v[60:63], v[48:51], v[144:147], v[60:63]\n" \
"s_waitcnt vmcnt(15)\n" \
"v_mfma_f32_16x16x32_bf16 v[56:59], v[50:53], v[216:219], v[56:59]\n" \
"v_mfma_f32_16x16x32_bf16 v[60:63], v[48:51], v[216:219], v[60:63]\n" \
"s_waitcnt vmcnt(14)\n" \
"v_mfma_f32_16x16x32_bf16 v[56:59], v[50:53], v[152:155], v[56:59]\n" \
"v_mfma_f32_16x16x32_bf16 v[60:63], v[48:51], v[152:155], v[60:63]\n" \
"s_waitcnt vmcnt(13)\n" \
"v_mfma_f32_16x16x32_bf16 v[56:59], v[50:53], v[224:227], v[56:59]\n" \
"v_mfma_f32_16x16x32_bf16 v[60:63], v[48:51], v[224:227], v[60:63]\n" \
"s_waitcnt vmcnt(12)\n" \
"v_mfma_f32_16x16x32_bf16 v[56:59], v[50:53], v[176:179], v[56:59]\n" \
"v_mfma_f32_16x16x32_bf16 v[60:63], v[48:51], v[176:179], v[60:63]\n" \
"s_waitcnt vmcnt(11)\n" \
"v_mfma_f32_16x16x32_bf16 v[56:59], v[50:53], v[248:251], v[56:59]\n" \
"v_mfma_f32_16x16x32_bf16 v[60:63], v[48:51], v[248:251], v[60:63]\n" \
"s_waitcnt vmcnt(10)\n" \
"v_mfma_f32_16x16x32_bf16 v[168:171], v[50:53], v[140:143], v[168:171]\n" \
"v_mfma_f32_16x16x32_bf16 v[172:175], v[48:51], v[140:143], v[172:175]\n" \
"s_waitcnt vmcnt(9)\n" \
"v_mfma_f32_16x16x32_bf16 v[240:243], v[50:53], v[212:215], v[240:243]\n" \
"v_mfma_f32_16x16x32_bf16 v[244:247], v[48:51], v[212:215], v[244:247]\n" \
"s_waitcnt vmcnt(8)\n" \
"v_mfma_f32_16x16x32_bf16 v[168:171], v[50:53], v[148:151], v[168:171]\n" \
"v_cvt_pk_bf16_f32 v56, v56, v57\n" \
"v_cvt_pk_bf16_f32 v60, v60, v61\n" \
"v_mfma_f32_16x16x32_bf16 v[172:175], v[48:51], v[148:151], v[172:175]\n" \
"v_cvt_pk_bf16_f32 v57, v58, v59\n" \
"s_waitcnt vmcnt(7)\n" \
"v_mfma_f32_16x16x32_bf16 v[240:243], v[50:53], v[220:223], v[240:243]\n" \
"buffer_store_dwordx2 v[56:57], v42, s[88:91], s73 offen sc0 sc1\n" \
"v_mfma_f32_16x16x32_bf16 v[244:247], v[48:51], v[220:223], v[244:247]\n" \
"v_cvt_pk_bf16_f32 v61, v62, v63\n" \
"s_waitcnt vmcnt(7)\n" \
"v_mfma_f32_16x16x32_bf16 v[168:171], v[50:53], v[156:159], v[168:171]\n" \
"buffer_store_dwordx2 v[60:61], v43, s[88:91], s73 offen sc0 sc1\n" \
"v_mfma_f32_16x16x32_bf16 v[172:175], v[48:51], v[156:159], v[172:175]\n" \
"s_waitcnt vmcnt(7)\n" \
"v_mfma_f32_16x16x32_bf16 v[240:243], v[50:53], v[228:231], v[240:243]\n" \
"v_mfma_f32_16x16x32_bf16 v[244:247], v[48:51], v[228:231], v[244:247]\n" \
"s_waitcnt vmcnt(6)\n" \
"v_mfma_f32_16x16x32_bf16 v[168:171], v[50:53], v[164:167], v[168:171]\n" \
"v_mfma_f32_16x16x32_bf16 v[172:175], v[48:51], v[164:167], v[172:175]\n" \
"s_waitcnt vmcnt(5)\n" \
"v_mfma_f32_16x16x32_bf16 v[240:243], v[50:53], v[236:239], v[240:243]\n" \
"v_mfma_f32_16x16x32_bf16 v[244:247], v[48:51], v[236:239], v[244:247]\n" \
"s_waitcnt vmcnt(4)\n" \
"v_mfma_f32_16x16x32_bf16 v[240:243], v[50:53], v[188:191], v[240:243]\n" \
"v_mfma_f32_16x16x32_bf16 v[244:247], v[48:51], v[188:191], v[244:247]\n" \
"s_waitcnt vmcnt(3)\n" \
"v_mfma_f32_16x16x32_bf16 v[240:243], v[50:53], v[196:199], v[240:243]\n" \
"v_cvt_pk_bf16_f32 v168, v168, v169\n" \
"v_cvt_pk_bf16_f32 v172, v172, v173\n" \
"v_mfma_f32_16x16x32_bf16 v[244:247], v[48:51], v[196:199], v[244:247]\n" \
"v_cvt_pk_bf16_f32 v169, v170, v171\n" \
"s_waitcnt vmcnt(2)\n" \
"v_mfma_f32_16x16x32_bf16 v[240:243], v[50:53], v[204:207], v[240:243]\n" \
"v_cvt_pk_bf16_f32 v173, v174, v175\n" \
"v_mfma_f32_16x16x32_bf16 v[244:247], v[48:51], v[204:207], v[244:247]\n" \
"s_mov_b64 exec, 1\n" \
"buffer_store_dword v46, off, s[56:59], s72 offset:0 sc1\n" \
"s_mov_b64 exec, -1\n" \
"buffer_store_dwordx2 v[168:169], v42, s[88:91], s92 offen sc0 sc1\n" \
"buffer_store_dwordx2 v[172:173], v43, s[88:91], s92 offen sc0 sc1\n" \
"s_nop 0\n" \
"v_cvt_pk_bf16_f32 v240, v240, v241\n" \
"v_cvt_pk_bf16_f32 v241, v242, v243\n" \
"v_cvt_pk_bf16_f32 v244, v244, v245\n" \
"v_cvt_pk_bf16_f32 v245, v246, v247\n" \
"buffer_store_dwordx2 v[240:241], v42, s[88:91], s93 offen sc0 sc1\n" \
"buffer_store_dwordx2 v[244:245], v43, s[88:91], s93 offen sc0 sc1\n"
#define ATT_VCLOB "v21","v22","v23","v32","v33","v34","v35","v36","v37","v38","v39","v40","v41","v42","v43","v44","v45","v46","v47","v48","v49","v50","v51","v52","v53","v54","v55","v56","v57","v58","v59","v60","v61","v62","v63","v64","v65","v66","v67","v68","v69","v70","v71","v72","v73","v74","v75","v76","v77","v78","v79","v80","v81","v82","v83","v84","v85","v86","v87","v88","v89","v90","v91","v92","v93","v94","v95","v96","v97","v98","v99","v100","v101","v102","v103","v104","v105","v106","v107","v108","v109","v110","v111","v112","v113","v114","v115","v116","v117","v118","v119","v120","v121","v122","v123","v124","v125","v126","v127","v128","v129","v130","v131","v132","v133","v134","v135","v136","v137","v138","v139","v140","v141","v142","v143","v144","v145","v146","v147","v148","v149","v150","v151","v152","v153","v154","v155","v156","v157","v158","v159","v160","v161","v162","v163","v164","v165","v166","v167","v168","v169","v170","v171","v172","v173","v174","v175","v176","v177","v178","v179","v180","v181","v182","v183","v184","v185","v186","v187","v188","v189","v190","v191","v192","v193","v194","v195","v196","v197","v198","v199","v200","v201","v202","v203","v204","v205","v206","v207","v208","v209","v210","v211","v212","v213","v214","v215","v216","v217","v218","v219","v220","v221","v222","v223","v224","v225","v226","v227","v228","v229","v230","v231","v232","v233","v234","v235","v236","v237","v238","v239","v240","v241","v242","v243","v244","v245","v246","v247","v248","v249","v250","v251","v252","v253","v254","v255"
#define ATT_SCLOB "s63","s64","s65","s66","s67","s68","s69","s70","s71","s72","s73","s74","s75","s76","s77","s78","s79","s80","s81","s82","s83","s84","s85","s86","s87","s88","s89","s90","s91","s92","s93","s94","s95","s96","s97","s98","s99","s100","s101"
enum { T_fo, T_fj, T_fa, T_fas, T_fb, T_fbs, T_f8, T_mk8, T_wol, T_woh, T_wbl, T_wbh, T_outl, T_outh, T_r1l, T_r1h, T_r1n, T_r1s, T_m0, T_m1, T_mw, T_m4, T_ml, T_w0, T_w1, T_w2, T_w3, T_A1, T_A2, T_A3, T_B1, T_B2, T_B3, T_Ao, T_Bo, T_Al, T_Bl, T_l8, T_xr, T_x8r, T_oA, T_oB, T_o8, T_x0, T_x1, T_x2, T_x3, T_x4, T_x5, T_x6, T_x8, T_ep, T_xf, T_kf };

__device__ __forceinline__ void out_consts(u32x4 (&vp)[3], const u16* wo, const u16* wob, const u16* out, const unsigned b, const unsigned w, const unsigned lane) {
    const unsigned sp = b & 7u, nt = (b >> 3) & 3u, j = b >> 5, head = 4u * sp + nt, OOR = 0x40000000u;
    unsigned vw[9];
#pragma unroll
    for (unsigned i = 0; i < 9u; ++i) {
        const unsigned q = 64u * i + lane, row = __umul24(q, 3641u) >> 16, c = q - __umul24(row, 18u);
        vw[i] = __umul24(row, DM * 2) + (c < 16u ? (c * 16u + sp * 32u + 32u) & 255u : c * 16u) - (i & 3u) * 1024u;
    }
    const unsigned row = 16u * w + (lane >> 2), r9 = __umul24(16u * (row >> 5) + (row & 12u) + 2u * ((row >> 4) & 1u) + ((row >> 1) & 1u), 18u) + 16u + (row & 1u);
    unsigned vwl = __umul24(r9, DM * 2) + ((lane & 2u) ? 256u : sp * 32u) + (lane & 1u) * 16u, vrd = __umul24(lane >> 2, 288u) + (lane & 3u) * 8u;
    asm volatile("" : "+v"(vw[0]), "+v"(vw[1]), "+v"(vw[2]), "+v"(vw[3]), "+v"(vw[4]), "+v"(vw[5]), "+v"(vw[6]), "+v"(vw[7]), "+v"(vw[8]), "+v"(vwl), "+v"(vrd));
    unsigned tab = 0;
#define TB(n, v) asm("v_writelane_b32 %0, %1, %2" : "+v"(tab) : "s"((unsigned)(v)), "n"((int)T_##n))
    const unsigned FJ = (unsigned)WS_F + (F_J + 640u * j) * 4u, mt = 2u * (w & 2u) + (w & 1u), w2 = w < 2u ? w : 2u, hw = head * 147456u;
    const unsigned FR = (unsigned)WS_F + F_R * 4u + (8u * j + sp) * 1024u;
    TB(fo, FJ + 4u * head); TB(fj, FJ); TB(fa, FR + (w | 2u) * 256u); TB(fas, FR + w * 256u + nt * 4u);
    TB(fb, FR + 128u + w * 256u); TB(fbs, FR + 128u + w * 256u + nt * 4u);
    TB(ep, b + w ? OOR : (unsigned)WS_F + F_EPOCH * 4u);
    const u16* pwo = wo + nt * 144u; const u16* pwb = wob + nt * 144u; const u16* pr1 = w < 3u ? pwo : pwb;
    TB(wol, PLO(pwo)); TB(woh, PHI(pwo) & 0xffffu); TB(wbl, PLO(pwb)); TB(wbh, PHI(pwb) & 0xffffu); TB(outl, PLO(out)); TB(outh, PHI(out) & 0xffffu); TB(r1l, PLO(pr1)); TB(r1h, PHI(pr1) & 0xffffu);
    TB(r1n, (w < 3u ? 72u * sp + 71u : 4095u) * 1152u + 288u); TB(r1s, w < 3u ? (72u * sp + 32u * w) * 1152u : hw + 110592u);
    TB(m0, 131072u + 9216u * w2); TB(w0, hw + 36864u * w2); TB(m1, w < 3u ? 9216u * w : 73728u); TB(mw, 9216u * w); TB(m4, w < 3u ? 131072u + 9216u * w : 64512u); TB(ml, 119808u + 1024u * w);
    const unsigned tA = (unsigned)WS_AF + (8u * j + mt) * (KS_TOT * 1024u), tB = (unsigned)WS_AF + (120u - 8u * j + mt) * (KS_TOT * 1024u);
#define HK(k) (4u * sp + ((nt + k) & 3u))
    TB(w1, HK(1u) * 147456u + w * 36864u); TB(w2, HK(2u) * 147456u + w * 36864u); TB(w3, HK(3u) * 147456u + w * 36864u);
    TB(A1, tA + 18432u + HK(1u) * 4096u); TB(A2, tA + 18432u + HK(2u) * 4096u); TB(A3, tA + 18432u + HK(3u) * 4096u);
    TB(B1, tB + 18432u + HK(1u) * 4096u); TB(B2, tB + 18432u + HK(2u) * 4096u); TB(B3, tB + 18432u + HK(3u) * 4096u);
    TB(Ao, tA + sp * 2048u); TB(Bo, tB + sp * 2048u); TB(Al, tA + 16384u); TB(Bl, tB + 16384u);
    const bool red = w == (sp >> 1), blk = sp & 1u;
    const unsigned xg = (unsigned)WS_X + (4u * j + nt) * 589824u + w * 16384u, oA = (128u * j + 16u * mt) * 1152u + nt * 288u, oB = (1920u - 128u * j + 16u * mt) * 1152u + nt * 288u;
    TB(l8, red ? (blk ? tB : tA) + 16384u : OOR); TB(xr, xg + sp * 65536u); TB(x8r, red ? xg + 524288u + (sp & 1u) * 1024u : OOR);
    TB(oA, oA + sp * 32u); TB(oB, oB + sp * 32u); TB(o8, red ? (blk ? oB : oA) + 256u : OOR); TB(x8, xg + 524288u + sp * 2048u);
    TB(f8, xg + w * 131072u + 14400u + sp * 4u); TB(mk8, red ? 0xff0000u : 0u); TB(xf, xg + sp * 65536u + 14336u);
    TB(kf, (unsigned)WS_F + (F_KV + head * 64u) * 4u);
#define XI(i) TB(x##i, xg + ((sp + 1u + i) & 7u) * 65536u + i * 2048u)
    XI(0); XI(1); XI(2); XI(3); XI(4); XI(5); XI(6);
#undef XI
#undef HK
#undef TB
    vp[0] = (u32x4){vw[0], vw[1], vw[2], vw[3]}; vp[1] = (u32x4){vw[4], vw[5], vw[6], vw[7]}; vp[2] = (u32x4){vw[8], vwl, vrd, tab};
}
__global__ __launch_bounds__(256, 1) __attribute__((aligned(4096))) void fused_kernel(const u16* __restrict__ x, const u16* __restrict__ ckv, const u16* __restrict__ wq, const u16* __restrict__ wuk,
                                                       const u16* __restrict__ wuv, const u16* __restrict__ wqkv, const u16* __restrict__ gw,
                                                       const u16* __restrict__ wo, const u16* __restrict__ wob, char* __restrict__ ws, u16* __restrict__ out) {
    using namespace att;
    extern __shared__ __attribute__((aligned(16))) char lds[];
    unsigned wid = rfl(threadIdx.x >> 6);
    code_touch((blockIdx.x >> 3) * 4u + wid, (unsigned)(size_t)lds + 151552u + wid * 512u);
    SBAR();
    asm volatile("" :: "s"(x), "s"(ckv), "s"(wq), "s"(wuk), "s"(wuv), "s"(wqkv), "s"(gw), "s"(wo), "s"(wob), "s"(ws), "s"(out));
    unsigned tag;
    int b = blockIdx.x;
    u32x4 vp[5];
    const unsigned vl = proj_phase(x, ckv, wq, wuk, wuv, wqkv, gw, wo, wob, out, ws, lds, wid, tag, vp);
    asm volatile("" : "+s"(ws));
    const int head = (b & 7) * 4 + ((b >> 3) & 3), jj = b >> 5;
    unsigned* const flags = (unsigned*)(ws + WS_F);
    RTS(2);
#define AST(L) do {} while (0)
    const unsigned pair = wid >> 1, p = wid & 1u, sw = 2u * jj + 2u;
    const __amdgpu_buffer_rsrc_t srd = __builtin_amdgcn_make_buffer_rsrc((void*)(ws + WS_K + (size_t)head * 1048576), (short)0, (32 - 2 * jj) * 32768, 0x00020000);
#define SD(J8, H, W) ((unsigned)WS_AF + (((J8) + 2u * ((W) & 2u) + ((W) & 1u)) * KS_TOT + 18u + 4u * (H)) * 1024u)
#define SRDA (u32x4){PLO(ws), PHI(ws) & 0xffffu, (unsigned)WS_L, 0x00020000u}
#define ATT_TAIL "s_endpgm\n"
    asm volatile(ATT_ASM ATT_TAIL "L_end_%=:\n s_nop 1"
                 :
                 : "{v[0:3]}"(vp[0]), "{v[4:7]}"(vp[1]), "{v[8:11]}"(vp[2]), "{v[12:15]}"(vp[3]), "{v[16:19]}"(vp[4]), "{v20}"(vl), "{s[56:59]}"(SRDA), "{s60}"(SD(8u * jj, head, wid)),
                   "{s61}"(SD(120u - 8u * jj, head, wid)), "{s62}"(tag), [srd] "s"(srd), [m1] "s"(49152u + wid * 2048u),
                   [m2] "s"((pair * 16384u + p * 4096u) ^ 32768u), [t2] "s"((sw + pair - 2u) * 32768u), [p4] "s"(p * 4096u), [k2] "s"(pair * 16384u), [v0] "s"(jj ? 16384u : 32768u),
                   [n1] "s"(sw - 2u), [n2] "s"(16u - sw), [m0s] "s"(158720u + wid * 256u), [lim] "s"(0x71800000u), [qa] "s"(81920u + pair * 16384u + p * 4096u), [qa2] "s"(65536u + pair * 57344u + p * 4096u),
                   [fkb] "s"((unsigned)WS_F + (unsigned)(F_KV + head * 64 + 8) * 4u), [fks] "s"(wid != 3u ? 0x40000000u : (unsigned)WS_F + (unsigned)(F_KV + head * 64 + 8 + jj) * 4u)
                 : "memory", "m0", "vcc", "scc", ATT_VCLOB, ATT_SCLOB, ACLOB_LO, ACLOB_HI);
    {
        const u16* const __attribute__((address_space(4)))* ka = (const u16* const __attribute__((address_space(4)))*)__builtin_amdgcn_kernarg_segment_ptr();
        unsigned b2 = b, w2 = wid;
        asm volatile("s_waitcnt vmcnt(0)" : "+s"(ka), "+s"(b2), "+s"(w2), "+s"(ws) :: "memory");
        const unsigned j2 = b2 >> 5, p2 = w2 & 1u, h2 = (b2 & 7u) * 4u + ((b2 >> 3) & 3u);
#pragma unroll 1
        for (unsigned i = w2 & 2u; i < 2u + 3u * (w2 & 2u); ++i)
            att_exact(ka[0], ka[2], ws, h2, (i < 4u ? 1920u - 128u * j2 : 128u * j2 - 128u) + 32u * i + 16u * p2, *reinterpret_cast<const float*>(lds + LDS_G + (32u * i + 16u * p2 + (lane_id() & 15u)) * 4u));
        asm volatile("s_waitcnt vmcnt(0)" ::: "memory");
        unsigned* fa = (unsigned*)(ws + WS_F) + F_R + (8u * j2 + (b2 & 7u)) * 256u + (2u + p2) * 64u + ((b2 >> 3) & 3u);
        if (w2 & 2u) st_flag(fa, tag);
        wait_flags(fa, ld_flag(fa), tag);
        const unsigned l2 = lane_id(); u32x4 c[3]; out_consts(c, ka[7], ka[8], ka[10], b2, w2, l2);
        asm volatile("s_waitcnt vmcnt(0) lgkmcnt(0)\n\ts_nop 7\n\ts_branch L_att_stub" :: "{v[0:3]}"(c[0]), "{v[4:7]}"(c[1]), "{v[8:11]}"(c[2]), "{v20}"(l2 * 16u), "{s[56:59]}"(SRDA), "{s60}"(SD(8u * j2, h2, w2)),
                     "{s61}"(SD(120u - 8u * j2, h2, w2)), "{s62}"(tag) : "memory");
    }
    RTS(5);
}

extern "C" void kernel_launch(void* const* d_in, const int* in_sizes, int n_in, void* d_out, int out_size, void* d_ws, size_t ws_size,
                              hipStream_t stream) {
    (void)hipFuncSetAttribute((const void*)fused_kernel, hipFuncAttributeMaxDynamicSharedMemorySize, att::LDS_BYTES);
    fused_kernel<<<dim3(256), 256, att::LDS_BYTES, stream>>>((const u16*)d_in[0], (const u16*)d_in[4], (const u16*)d_in[5], (const u16*)d_in[6], (const u16*)d_in[7], (const u16*)d_in[1],
                                                             (const u16*)d_in[9], (const u16*)d_in[2], (const u16*)d_in[8], (char*)d_ws, (u16*)d_out);
}
```

```cpp
#include <hip/hip_runtime.h>
#include <hip/hip_bf16.h>
#include <stdint.h>
#include <utility>

typedef short bf16x8 __attribute__((ext_vector_type(8)));
typedef short s16x4 __attribute__((ext_vector_type(4)));
typedef float f32x16 __attribute__((ext_vector_type(16)));
typedef float f32x4 __attribute__((ext_vector_type(4)));
typedef unsigned u32x4 __attribute__((ext_vector_type(4)));
typedef unsigned short u16;

constexpr int S = 2048, DM = 576, NH = 32, HD = 128, DC = 512, NQKV = 1728, DHA = 18;
constexpr int NBIG = NH * HD;
constexpr int KS_TOT = 146;

#define SBAR() __builtin_amdgcn_sched_barrier(0)
constexpr size_t WS_Q = 0, WS_K = WS_Q + (size_t)S * NBIG * 2, WS_V = WS_K + (size_t)S * NBIG * 2, WS_AF = WS_V + (size_t)S * NBIG * 2, WS_X = WS_AF + (size_t)128 * KS_TOT * 1024,
                 WS_P = WS_X + (size_t)64 * 16 * 9216, WS_F = WS_P + (size_t)128 * (4 * 32 * 256 + 1024) * 4;
constexpr size_t WS_L = (size_t)96 << 20;
static_assert(WS_F == 96206848, "workspace map is baked into the generated asm");
constexpr int F_EPOCH = 0, F_J = 1024, F_ST = 8192, F_KV = 40960, F_R = 65536;
constexpr unsigned TAG_STEP = 0x9E3779B1u;
__device__ __forceinline__ unsigned ld_flag(const unsigned* p) { return __hip_atomic_load(p, __ATOMIC_RELAXED, __HIP_MEMORY_SCOPE_AGENT); }
__device__ __forceinline__ void st_flag(unsigned* p, unsigned v) { __hip_atomic_store(p, v, __ATOMIC_RELAXED, __HIP_MEMORY_SCOPE_AGENT); }
__device__ __forceinline__ unsigned ld_flag_async(const unsigned* p) { unsigned v; asm volatile("global_load_dword %0, %1, off sc1" : "=v"(v) : "v"(p) : "memory"); return v; }
#define WAITV(v, N) asm volatile("s_waitcnt vmcnt(" #N ")" : "+v"(v) :: "memory")
__device__ __forceinline__ void wait_flags(const unsigned* p, unsigned v, unsigned tag) {
    if (__builtin_expect(__builtin_amdgcn_ballot_w64(v != tag) != 0ull, 0)) {
        unsigned spins = 0;
        do { __builtin_amdgcn_s_sleep(1); } while (__builtin_amdgcn_ballot_w64(ld_flag(p) != tag) != 0ull && ++spins < (1u << 22));
    }
    asm volatile("" ::: "memory");
}

__device__ __forceinline__ unsigned cvtpk(float lo, float hi) {
    unsigned r; asm volatile("v_cvt_pk_bf16_f32 %0, %1, %2" : "=v"(r) : "v"(lo), "v"(hi)); return r;
}
__device__ __forceinline__ float bf2f(u16 v) { return __uint_as_float(((unsigned)v) << 16); }
__device__ __forceinline__ int crow(int r, int hi) { return (r & 3) + 8 * (r >> 2) + 4 * hi; }

typedef __attribute__((address_space(3))) s16x4* lds_s16x4_p;
__device__ __forceinline__ s16x4 trrd(unsigned addr) { return __builtin_amdgcn_ds_read_tr16_b64_v4i16((lds_s16x4_p)addr); }

__device__ __forceinline__ unsigned lane_id() { unsigned l; asm volatile("v_mbcnt_lo_u32_b32 %0, -1, 0\n\tv_mbcnt_hi_u32_b32 %0, -1, %0" : "=v"(l)); return l; }
#define RTS(slot) do {} while (0)
#define CT_L 5u
__device__ __forceinline__ void code_touch(unsigned wi, unsigned lds_scratch) {
    unsigned long long pc; asm volatile("s_getpc_b64 %0" : "=s"(pc));
    asm volatile("s_mov_b32 m0, %0\n\ts_mov_b64 exec, %3\n\tglobal_load_lds_dword %1, %2\n\ts_mov_b64 exec, -1" :: "s"(lds_scratch), "v"(((threadIdx.x & 63u) * 128u + wi) * 128u), "s"(pc & ~127ull), "n"((1u << CT_L) - 1u) : "m0", "memory");
}
#define ACLOB_LO "a0","a1","a2","a3","a4","a5","a6","a7","a8","a9","a10","a11","a12","a13","a14","a15","a16","a17","a18","a19","a20","a21","a22","a23","a24","a25","a26","a27","a28","a29","a30","a31","a32","a33","a34","a35","a36","a37","a38","a39","a40","a41","a42","a43","a44","a45","a46","a47","a48","a49","a50","a51","a52","a53","a54","a55","a56","a57","a58","a59","a60","a61","a62","a63","a64","a65","a66","a67","a68","a69","a70","a71","a72","a73","a74","a75","a76","a77","a78","a79","a80","a81","a82","a83","a84","a85","a86","a87","a88","a89","a90","a91","a92","a93","a94","a95","a96","a97","a98","a99","a100","a101","a102","a103","a104","a105","a106","a107","a108","a109","a110","a111","a112","a113","a114","a115","a116","a117","a118","a119","a120","a121","a122","a123","a124","a125","a126","a127"
#define ACLOB_HI "a128","a129","a130","a131","a132","a133","a134","a135","a136","a137","a138","a139","a140","a141","a142","a143","a144","a145","a146","a147","a148","a149","a150","a151","a152","a153","a154","a155","a156","a157","a158","a159","a160","a161","a162","a163","a164","a165","a166","a167","a168","a169","a170","a171","a172","a173","a174","a175","a176","a177","a178","a179","a180","a181","a182","a183","a184","a185","a186","a187","a188","a189","a190","a191","a192","a193","a194","a195","a196","a197","a198","a199","a200","a201","a202","a203","a204","a205","a206","a207","a208","a209","a210","a211","a212","a213","a214","a215","a216","a217","a218","a219","a220","a221","a222","a223","a224","a225","a226","a227","a228","a229","a230","a231","a232","a233","a234","a235","a236","a237","a238","a239","a240","a241","a242","a243","a244","a245","a246","a247","a248","a249","a250","a251","a252","a253","a254","a255"
#define PROJ_PRE0 \
"s_add_u32 s62, %[rc], 0x0\n" \
"s_and_b32 s62, s62, 0x3ff\n" \
"s_lshl_b32 s63, s62, 12\n" \
"s_add_u32 m0, %[mA], 0x0\n" \
"s_add_u32 s85, s62, 0x0\n" \
"buffer_load_dwordx4 v22, s[64:67], s85 offen lds\n" \
"s_add_u32 m0, %[mA], 0x400\n" \
"s_add_u32 s84, s62, 0x2000\n" \
"buffer_load_dwordx4 v23, s[64:67], s84 offen lds\n" \
"s_add_u32 m0, %[mB], 0x0\n" \
"s_add_u32 s85, s63, 0x0\n" \
"buffer_load_dwordx4 v26, s[68:71], s85 offen lds\n" \
"s_add_u32 m0, %[mB], 0x4000\n" \
"s_add_u32 s84, s63, 0x0\n" \
"buffer_load_dwordx4 v26, s[72:75], s84 offen lds\n" \
"s_add_u32 m0, %[mA], 0x800\n" \
"s_add_u32 s85, s62, 0x4000\n" \
"buffer_load_dwordx4 v22, s[64:67], s85 offen lds\n" \
"s_add_u32 m0, %[mA], 0xc00\n" \
"s_add_u32 s84, s62, 0x6000\n" \
"buffer_load_dwordx4 v23, s[64:67], s84 offen lds\n" \
"s_add_u32 m0, %[mB], 0x400\n" \
"s_add_u32 s85, s63, 0x8000\n" \
"buffer_load_dwordx4 v26, s[68:71], s85 offen lds\n" \
"s_add_u32 m0, %[mB], 0x4400\n" \
"s_add_u32 s84, s63, 0x8000\n" \
"buffer_load_dwordx4 v26, s[72:75], s84 offen lds\n"
#define PROJ_PRE1 \
"s_add_u32 s62, %[rc], 0x0\n" \
"s_and_b32 s62, s62, 0x3ff\n" \
"s_lshl_b32 s63, s62, 12\n" \
"s_add_u32 m0, %[mA], 0x1000\n" \
"s_add_u32 s85, s62, 0x8000\n" \
"buffer_load_dwordx4 v22, s[64:67], s85 offen lds\n" \
"s_add_u32 m0, %[mA], 0x1400\n" \
"s_add_u32 s84, s62, 0xa000\n" \
"buffer_load_dwordx4 v23, s[64:67], s84 offen lds\n" \
"s_add_u32 m0, %[mB], 0x800\n" \
"s_add_u32 s85, s63, 0x10000\n" \
"buffer_load_dwordx4 v27, s[68:71], s85 offen lds\n" \
"s_add_u32 m0, %[mB], 0x4800\n" \
"s_add_u32 s84, s63, 0x10000\n" \
"buffer_load_dwordx4 v27, s[72:75], s84 offen lds\n" \
"s_add_u32 m0, %[mA], 0x1800\n" \
"s_add_u32 s85, s62, 0xc000\n" \
"buffer_load_dwordx4 v22, s[64:67], s85 offen lds\n" \
"s_add_u32 m0, %[mA], 0x1c00\n" \
"s_add_u32 s84, s62, 0xe000\n" \
"buffer_load_dwordx4 v23, s[64:67], s84 offen lds\n" \
"s_add_u32 m0, %[mB], 0xc00\n" \
"s_add_u32 s85, s63, 0x18000\n" \
"buffer_load_dwordx4 v27, s[68:71], s85 offen lds\n" \
"s_add_u32 m0, %[mB], 0x4c00\n" \
"s_add_u32 s84, s63, 0x18000\n" \
"buffer_load_dwordx4 v27, s[72:75], s84 offen lds\n"
#define PROJ_PRE2 \
"s_add_u32 s62, %[rc], 0x80\n" \
"s_and_b32 s62, s62, 0x3ff\n" \
"s_lshl_b32 s63, s62, 12\n" \
"s_add_u32 m0, %[mA], 0x8000\n" \
"s_add_u32 s85, s62, 0x0\n" \
"buffer_load_dwordx4 v22, s[64:67], s85 offen lds\n" \
"s_add_u32 m0, %[mA], 0x8400\n" \
"s_add_u32 s84, s62, 0x2000\n" \
"buffer_load_dwordx4 v23, s[64:67], s84 offen lds\n"
#define PROJ_PRE3 \
"s_add_u32 s62, %[rc], 0x80\n" \
"s_and_b32 s62, s62, 0x3ff\n" \
"s_lshl_b32 s63, s62, 12\n" \
"s_add_u32 m0, %[mB], 0x8000\n" \
"s_add_u32 s85, s63, 0x0\n" \
"buffer_load_dwordx4 v26, s[68:71], s85 offen lds\n" \
"s_add_u32 m0, %[mB], 0xc000\n" \
"s_add_u32 s84, s63, 0x0\n" \
"buffer_load_dwordx4 v26, s[72:75], s84 offen lds\n" \
"s_add_u32 m0, %[mA], 0x8800\n" \
"s_add_u32 s85, s62, 0x4000\n" \
"buffer_load_dwordx4 v22, s[64:67], s85 offen lds\n" \
"s_add_u32 m0, %[mA], 0x8c00\n" \
"s_add_u32 s84, s62, 0x6000\n" \
"buffer_load_dwordx4 v23, s[64:67], s84 offen lds\n" \
"s_add_u32 m0, %[mB], 0x8400\n" \
"s_add_u32 s85, s63, 0x8000\n" \
"buffer_load_dwordx4 v26, s[68:71], s85 offen lds\n" \
"s_add_u32 m0, %[mB], 0xc400\n" \
"s_add_u32 s84, s63, 0x8000\n" \
"buffer_load_dwordx4 v26, s[72:75], s84 offen lds\n" \
"s_add_u32 m0, %[mA], 0x9000\n" \
"s_add_u32 s85, s62, 0x8000\n" \
"buffer_load_dwordx4 v22, s[64:67], s85 offen lds\n" \
"s_add_u32 m0, %[mA], 0x9400\n" \
"s_add_u32 s84, s62, 0xa000\n" \
"buffer_load_dwordx4 v23, s[64:67], s84 offen lds\n"
#define PROJ_TKE 0
#define PROJ_T0 0
#define PROJ_ROT 2*(sp+mt)
#define PROJ_TC 0
#define PROJ_ASM \
".p2align 3\n" \
"s_branch L_go_%=\n" \
"L_pl_%=:\n" \
"s_mov_b32 s63, 0\n" \
"L_ql_%=:\n" \
"s_sleep 1\n" \
"v_bfe_u32 v44, v20, 2, 5\n" \
"buffer_load_dword v44, v44, s[80:83], s39 offen offset:64 sc1\n" \
"s_waitcnt vmcnt(0)\n" \
"v_cmp_ne_u32_e32 vcc, %[tag], v44\n" \
"s_add_u32 s63, s63, 1\n" \
"s_nop 3\n" \
"s_cbranch_vccz L_rl_%=\n" \
"s_cmp_gt_u32 s63, 0x400000\n" \
"s_cbranch_scc0 L_ql_%=\n" \
"s_branch L_rl_%=\n" \
"L_pa_%=:\n" \
"s_mov_b32 s63, 0\n" \
"L_qa_%=:\n" \
"s_sleep 1\n" \
"v_bfe_u32 v44, v20, 2, 5\n" \
"buffer_load_dword v44, v44, s[80:83], s39 offen offset:0 sc1\n" \
"s_waitcnt vmcnt(0)\n" \
"v_cmp_ne_u32_e32 vcc, %[tag], v44\n" \
"s_add_u32 s63, s63, 1\n" \
"s_nop 3\n" \
"s_cbranch_vccz L_ra_%=\n" \
"s_cmp_gt_u32 s63, 0x400000\n" \
"s_cbranch_scc0 L_qa_%=\n" \
"s_branch L_ra_%=\n" \
"L_go_%=:\n" \
"v_xor_b32_e32 v33, 0x20, v32\n" \
"s_mov_b32 s80, %[wsl]\n" \
"s_and_b32 s81, %[wsh], 0xffff\n" \
"s_mov_b32 s82, 0x10000000\n" \
"s_mov_b32 s83, 0x20000\n" \
"s_mov_b32 s84, 0x5bc0000\n" \
"s_add_u32 s62, s22, 0x80\n" \
"buffer_load_dword v180, off, s[80:83], s84 sc1\n" \
"s_and_b32 s62, s62, 0x3ff\n" \
"s_lshl_b32 s63, s62, 12\n" \
"v_xor_b32_e64 v34, 0x40, v32\n" \
"v_xor_b32_e32 v35, 0x60, v32\n" \
"s_waitcnt vmcnt(11)\n" \
"s_barrier\n" \
"ds_read_b64_tr_b16 v[120:121], v32 offset:8192\n" \
"ds_read_b64_tr_b16 v[122:123], v32 offset:9216\n" \
"ds_read_b128 v[56:59], v29 offset:16384\n" \
"ds_read_b64_tr_b16 v[116:117], v32 offset:0\n" \
"ds_read_b64_tr_b16 v[118:119], v32 offset:1024\n" \
"ds_read_b128 v[52:55], v28 offset:16384\n" \
"s_add_u32 m0, %[mB], 0x8800\n" \
"s_add_u32 s85, s63, 0x10000\n" \
"s_mov_b32 s76, s12\n" \
"ds_read_b128 v[60:63], v28 offset:18432\n" \
"s_and_b32 s77, s13, 0xffff\n" \
"s_mov_b32 s78, 0x1e6000\n" \
"s_mov_b32 s79, 0x20000\n" \
"ds_read_b128 v[64:67], v29 offset:18432\n" \
"s_waitcnt lgkmcnt(5)\n" \
"v_mfma_f32_16x16x32_bf16 a[0:3], v[120:123], v[56:59], 0\n" \
"ds_read_b128 v[72:75], v29 offset:20480\n" \
"ds_read_b128 v[68:71], v28 offset:20480\n" \
"ds_read_b128 v[76:79], v28 offset:22528\n" \
"buffer_load_dwordx4 v27, s[68:71], s85 offen lds\n" \
"ds_read_b128 v[80:83], v29 offset:22528\n" \
"s_waitcnt lgkmcnt(6)\n" \
"v_mfma_f32_16x16x32_bf16 a[0:3], v[116:119], v[52:55], a[0:3]\n" \
"ds_read_b64_tr_b16 v[128:129], v33 offset:8192\n" \
"s_waitcnt lgkmcnt(6)\n" \
"v_mfma_f32_16x16x32_bf16 a[32:35], v[116:119], v[60:63], 0\n" \
"ds_read_b64_tr_b16 v[130:131], v33 offset:9216\n" \
"ds_read_b64_tr_b16 v[124:125], v33 offset:0\n" \
"ds_read_b64_tr_b16 v[126:127], v33 offset:1024\n" \
"s_waitcnt lgkmcnt(8)\n" \
"s_nop 0\n" \
"v_mfma_f32_16x16x32_bf16 a[32:35], v[120:123], v[64:67], a[32:35]\n" \
"ds_read_b64_tr_b16 v[136:137], v34 offset:8192\n" \
"ds_read_b64_tr_b16 v[138:139], v34 offset:9216\n" \
"ds_read_b64_tr_b16 v[132:133], v34 offset:0\n" \
"s_add_u32 m0, %[mB], 0xc800\n" \
"ds_read_b64_tr_b16 v[134:135], v34 offset:1024\n" \
"s_add_u32 s84, s63, 0x10000\n" \
"buffer_load_dwordx4 v27, s[72:75], s84 offen lds\n" \
"ds_read_b64_tr_b16 v[144:145], v35 offset:8192\n" \
"ds_read_b64_tr_b16 v[146:147], v35 offset:9216\n" \
"ds_read_b64_tr_b16 v[140:141], v35 offset:0\n" \
"s_lshr_b32 s95, %[b], 3\n" \
"ds_read_b64_tr_b16 v[142:143], v35 offset:1024\n" \
"s_lshl_b32 s96, s95, 2\n" \
"ds_read_b64_tr_b16 v[152:153], v32 offset:24576\n" \
"s_add_u32 s97, s96, %[w]\n" \
"ds_read_b64_tr_b16 v[154:155], v32 offset:25600\n" \
"v_lshrrev_b32_e32 v212, 4, v20\n" \
"ds_read_b64_tr_b16 v[148:149], v32 offset:16384\n" \
"s_mul_i32 s98, 27, s97\n" \
"ds_read_b64_tr_b16 v[150:151], v32 offset:17408\n" \
"v_min_u32_e32 v213, 26, v212\n" \
"s_waitcnt lgkmcnt(15)\n" \
"v_mfma_f32_16x16x32_bf16 a[64:67], v[120:123], v[72:75], 0\n" \
"ds_read_b64_tr_b16 v[160:161], v33 offset:24576\n" \
"ds_read_b64_tr_b16 v[162:163], v33 offset:25600\n" \
"v_mfma_f32_16x16x32_bf16 a[64:67], v[116:119], v[68:71], a[64:67]\n" \
"s_add_u32 m0, %[mA], 0x9800\n" \
"s_add_u32 s85, s62, 0xc000\n" \
"v_mfma_f32_16x16x32_bf16 a[96:99], v[116:119], v[76:79], 0\n" \
"buffer_load_dwordx4 v22, s[64:67], s85 offen lds\n" \
"v_mfma_f32_16x16x32_bf16 a[96:99], v[120:123], v[80:83], a[96:99]\n" \
"ds_read_b64_tr_b16 v[156:157], v33 offset:16384\n" \
"s_waitcnt lgkmcnt(15)\n" \
"v_mfma_f32_16x16x32_bf16 a[4:7], v[128:131], v[56:59], 0\n" \
"s_add_u32 m0, %[mA], 0x9c00\n" \
"s_add_u32 s84, s62, 0xe000\n" \
"v_mfma_f32_16x16x32_bf16 a[4:7], v[124:127], v[52:55], a[4:7]\n" \
"buffer_load_dwordx4 v23, s[64:67], s84 offen lds\n" \
"v_mfma_f32_16x16x32_bf16 a[36:39], v[124:127], v[60:63], 0\n" \
"ds_read_b64_tr_b16 v[158:159], v33 offset:17408\n" \
"ds_read_b64_tr_b16 v[168:169], v34 offset:24576\n" \
"v_mfma_f32_16x16x32_bf16 a[36:39], v[128:131], v[64:67], a[36:39]\n" \
"s_add_u32 m0, %[mB], 0x8c00\n" \
"s_add_u32 s85, s63, 0x18000\n" \
"v_mfma_f32_16x16x32_bf16 a[68:71], v[128:131], v[72:75], 0\n" \
"buffer_load_dwordx4 v27, s[68:71], s85 offen lds\n" \
"v_mfma_f32_16x16x32_bf16 a[68:71], v[124:127], v[68:71], a[68:71]\n" \
"ds_read_b64_tr_b16 v[170:171], v34 offset:25600\n" \
"ds_read_b64_tr_b16 v[164:165], v34 offset:16384\n" \
"v_mfma_f32_16x16x32_bf16 a[100:103], v[124:127], v[76:79], 0\n" \
"s_add_u32 m0, %[mB], 0xcc00\n" \
"s_add_u32 s84, s63, 0x18000\n" \
"v_mfma_f32_16x16x32_bf16 a[100:103], v[128:131], v[80:83], a[100:103]\n" \
"ds_read_b64_tr_b16 v[166:167], v34 offset:17408\n" \
"s_waitcnt lgkmcnt(8)\n" \
"v_mfma_f32_16x16x32_bf16 a[8:11], v[136:139], v[56:59], 0\n" \
"buffer_load_dwordx4 v27, s[72:75], s84 offen lds\n" \
"v_mfma_f32_16x16x32_bf16 a[8:11], v[132:135], v[52:55], a[8:11]\n" \
"v_add_u32_e32 v214, s98, v213\n" \
"v_mul_u32_u24_e32 v215, 0xaaab, v214\n" \
"v_mfma_f32_16x16x32_bf16 a[40:43], v[132:135], v[60:63], 0\n" \
"v_lshrrev_b32_e32 v216, 18, v215\n" \
"v_mul_u32_u24_e32 v218, 6, v216\n" \
"v_mfma_f32_16x16x32_bf16 a[40:43], v[136:139], v[64:67], a[40:43]\n" \
"v_sub_u32_e32 v219, v214, v218\n" \
"v_lshrrev_b32_e32 v220, 1, v219\n" \
"v_mfma_f32_16x16x32_bf16 a[72:75], v[136:139], v[72:75], 0\n" \
"v_mul_u32_u24_e32 v217, 27, v216\n" \
"v_mul_u32_u24_e32 v221, 9, v220\n" \
"v_mfma_f32_16x16x32_bf16 a[72:75], v[132:135], v[68:71], a[72:75]\n" \
"v_add_u32_e32 v222, v217, v221\n" \
"v_and_b32_e32 v223, 1, v219\n" \
"v_mfma_f32_16x16x32_bf16 a[104:107], v[132:135], v[76:79], 0\n" \
"s_and_b32 s94, 7, %[b]\n" \
"v_add_u32_e32 v224, v222, v223\n" \
"v_mfma_f32_16x16x32_bf16 a[104:107], v[136:139], v[80:83], a[104:107]\n" \
"v_add_u32_e32 v225, s94, v224\n" \
"v_lshlrev_b32_e32 v186, 7, v225\n" \
"v_mfma_f32_16x16x32_bf16 a[12:15], v[144:147], v[56:59], 0\n" \
"ds_read_b64_tr_b16 v[176:177], v35 offset:24576\n" \
"ds_read_b64_tr_b16 v[178:179], v35 offset:25600\n" \
"v_mfma_f32_16x16x32_bf16 a[12:15], v[140:143], v[52:55], a[12:15]\n" \
"ds_read_b64_tr_b16 v[172:173], v35 offset:16384\n" \
"ds_read_b64_tr_b16 v[174:175], v35 offset:17408\n" \
"v_mfma_f32_16x16x32_bf16 a[44:47], v[140:143], v[60:63], 0\n" \
"ds_read_b128 v[84:87], v28 offset:0\n" \
"ds_read_b128 v[88:91], v29 offset:0\n" \
"v_mfma_f32_16x16x32_bf16 a[44:47], v[144:147], v[64:67], a[44:47]\n" \
"ds_read_b128 v[92:95], v28 offset:2048\n" \
"ds_read_b128 v[96:99], v29 offset:2048\n" \
"v_mfma_f32_16x16x32_bf16 a[76:79], v[144:147], v[72:75], 0\n" \
"s_mul_i32 s89, 6, s97\n" \
"ds_read_b128 v[100:103], v28 offset:4096\n" \
"v_mfma_f32_16x16x32_bf16 a[76:79], v[140:143], v[68:71], a[76:79]\n" \
"s_add_u32 s95, 0xffffffdc, s89\n" \
"ds_read_b128 v[104:107], v29 offset:4096\n" \
"v_mfma_f32_16x16x32_bf16 a[108:111], v[140:143], v[76:79], 0\n" \
"v_min_u32_e32 v227, 41, v212\n" \
"ds_read_b128 v[108:111], v28 offset:6144\n" \
"v_mfma_f32_16x16x32_bf16 a[108:111], v[144:147], v[80:83], a[108:111]\n" \
"s_mul_i32 s99, 0x1200, s94\n" \
"ds_read_b128 v[112:115], v29 offset:6144\n" \
"v_mfma_f32_16x16x32_bf16 a[16:19], v[56:59], v[152:155], 0\n" \
"s_mul_i32 s100, 36, s97\n" \
"v_add_u32_e32 v228, s95, v227\n" \
"v_mfma_f32_16x16x32_bf16 a[16:19], v[52:55], v[148:151], a[16:19]\n" \
"s_add_u32 s101, s99, s100\n" \
"s_mul_i32 s88, 0x288, s94\n" \
"v_mfma_f32_16x16x32_bf16 a[48:51], v[60:63], v[148:151], 0\n" \
"v_min_u32_e32 v229, 0x287, v228\n" \
"v_add_u32_e32 v226, s101, v212\n" \
"v_mfma_f32_16x16x32_bf16 a[48:51], v[64:67], v[152:155], a[48:51]\n" \
"s_mov_b32 m0, %[mS]\n" \
"s_mov_b32 s86, s12\n" \
"v_mfma_f32_16x16x32_bf16 a[80:83], v[72:75], v[152:155], 0\n" \
"s_mov_b32 s87, s13\n" \
"v_add_u32_e32 v230, s88, v229\n" \
"v_mfma_f32_16x16x32_bf16 a[80:83], v[68:71], v[148:151], a[80:83]\n" \
"v_cmp_gt_u32_e32 vcc, 36, v212\n" \
"s_lshl_b32 s94, %[w], 6\n" \
"v_mfma_f32_16x16x32_bf16 a[112:115], v[76:79], v[148:151], 0\n" \
"v_cndmask_b32_e32 v231, v230, v226, vcc\n" \
"v_lshlrev_b32_e32 v16, 7, v231\n" \
"v_mfma_f32_16x16x32_bf16 a[112:115], v[80:83], v[152:155], a[112:115]\n" \
"v_lshrrev_b32_e32 v212, 4, v20\n" \
"s_waitcnt lgkmcnt(8)\n" \
"v_mfma_f32_16x16x32_bf16 a[20:23], v[56:59], v[160:163], 0\n" \
"v_add_u32_e64 v213, s94, v212\n" \
"v_mul_u32_u24_e32 v214, 0x2aab, v213\n" \
"v_mfma_f32_16x16x32_bf16 a[20:23], v[52:55], v[156:159], a[20:23]\n" \
"v_lshrrev_b32_e32 v215, 16, v214\n" \
"s_add_u32 s97, 0xfffffe50, s94\n" \
"s_mov_b32 exec_lo, 0x7ffffff\n" \
"s_mov_b32 exec_hi, 0x0\n" \
"global_load_lds_dword v186, s[86:87]\n" \
"s_mov_b64 exec, -1\n" \
"v_mfma_f32_16x16x32_bf16 a[52:55], v[60:63], v[156:159], 0\n" \
"s_lshr_b32 s95, %[b], 5\n" \
"v_add_u32_e64 v216, s97, v212\n" \
"v_mfma_f32_16x16x32_bf16 a[52:55], v[64:67], v[160:163], a[52:55]\n" \
"v_cmp_gt_u32_e32 vcc, 0x1b0, v213\n" \
"v_mul_u32_u24_e32 v220, 6, v215\n" \
"v_mfma_f32_16x16x32_bf16 a[84:87], v[72:75], v[160:163], 0\n" \
"s_mul_i32 s96, 0x48, s95\n" \
"v_cndmask_b32_e32 v217, v216, v215, vcc\n" \
"v_mfma_f32_16x16x32_bf16 a[84:87], v[68:71], v[156:159], a[84:87]\n" \
"v_sub_u32_e32 v221, v213, v220\n" \
"v_add_u32_e32 v218, s96, v217\n" \
"v_mfma_f32_16x16x32_bf16 a[116:119], v[76:79], v[156:159], 0\n" \
"v_lshrrev_b32_e32 v222, 1, v221\n" \
"s_add_u32 s98, 0x100, s94\n" \
"v_mfma_f32_16x16x32_bf16 a[116:119], v[80:83], v[160:163], a[116:119]\n" \
"v_mul_u32_u24_e32 v219, 0xd80, v218\n" \
"v_mul_u32_u24_e32 v223, 0x480, v222\n" \
"v_mfma_f32_16x16x32_bf16 a[24:27], v[56:59], v[168:171], 0\n" \
"v_and_b32_e32 v225, 1, v221\n" \
"v_add_u32_e64 v228, s98, v212\n" \
"v_mfma_f32_16x16x32_bf16 a[24:27], v[52:55], v[164:167], a[24:27]\n" \
"s_and_b32 s88, 7, %[b]\n" \
"s_lshr_b32 s97, %[b], 3\n" \
"v_mfma_f32_16x16x32_bf16 a[56:59], v[60:63], v[164:167], 0\n" \
"v_add_u32_e32 v224, v219, v223\n" \
"v_lshlrev_b32_e32 v226, 4, v225\n" \
"v_mfma_f32_16x16x32_bf16 a[56:59], v[64:67], v[168:171], a[56:59]\n" \
"s_add_u32 s99, 0xffffff50, s94\n" \
"s_lshl_b32 s89, s88, 2\n" \
"v_mfma_f32_16x16x32_bf16 a[88:91], v[72:75], v[168:171], 0\n" \
"s_and_b32 s98, 3, s97\n" \
"v_add_u32_e64 v227, v224, v226\n" \
"v_mfma_f32_16x16x32_bf16 a[88:91], v[68:71], v[164:167], a[88:91]\n" \
"v_cmp_gt_u32_e32 vcc, 0x1b0, v213\n" \
"s_add_u32 s94, s89, s98\n" \
"v_mfma_f32_16x16x32_bf16 a[120:123], v[76:79], v[164:167], 0\n" \
"v_cndmask_b32_e32 v192, 0x40000000, v227, vcc\n" \
"s_mul_i32 s56, 36, s94\n" \
"v_mfma_f32_16x16x32_bf16 a[120:123], v[80:83], v[168:171], a[120:123]\n" \
"v_mul_u32_u24_e32 v229, 0x2aab, v228\n" \
"v_lshrrev_b32_e32 v230, 16, v229\n" \
"v_mfma_f32_16x16x32_bf16 a[28:31], v[56:59], v[176:179], 0\n" \
"s_add_u32 s62, s22, 0x100\n" \
"s_and_b32 s62, s62, 0x3ff\n" \
"v_mfma_f32_16x16x32_bf16 a[28:31], v[52:55], v[172:175], a[28:31]\n" \
"s_lshl_b32 s63, s62, 12\n" \
"v_add_u32_e32 v231, s99, v212\n" \
"v_mfma_f32_16x16x32_bf16 a[60:63], v[60:63], v[172:175], 0\n" \
"v_cmp_gt_u32_e32 vcc, 0x1b0, v228\n" \
"v_mul_u32_u24_e32 v235, 6, v230\n" \
"v_mfma_f32_16x16x32_bf16 a[60:63], v[64:67], v[176:179], a[60:63]\n" \
"v_cndmask_b32_e32 v232, v231, v230, vcc\n" \
"v_sub_u32_e32 v236, v228, v235\n" \
"v_mfma_f32_16x16x32_bf16 a[92:95], v[72:75], v[176:179], 0\n" \
"v_add_u32_e32 v233, s96, v232\n" \
"v_lshrrev_b32_e32 v237, 1, v236\n" \
"v_mfma_f32_16x16x32_bf16 a[92:95], v[68:71], v[172:175], a[92:95]\n" \
"v_mul_u32_u24_e32 v234, 0xd80, v233\n" \
"v_mul_u32_u24_e32 v238, 0x480, v237\n" \
"v_mfma_f32_16x16x32_bf16 a[124:127], v[76:79], v[172:175], 0\n" \
"v_and_b32_e32 v240, 1, v236\n" \
"v_add_u32_e64 v239, v234, v238\n" \
"v_mfma_f32_16x16x32_bf16 a[124:127], v[80:83], v[176:179], a[124:127]\n" \
"v_lshlrev_b32_e32 v241, 4, v240\n" \
"v_add_u32_e32 v242, v239, v241\n" \
"s_waitcnt vmcnt(1) lgkmcnt(0)\n" \
"s_barrier\n" \
"v_mfma_f32_16x16x32_bf16 a[144:147], v[88:91], v[152:155], 0\n" \
"s_add_u32 m0, %[mA], 0x0\n" \
"s_add_u32 s85, s62, 0x0\n" \
"v_mfma_f32_16x16x32_bf16 a[144:147], v[84:87], v[148:151], a[144:147]\n" \
"buffer_load_dwordx4 v22, s[64:67], s85 offen lds\n" \
"v_mfma_f32_16x16x32_bf16 a[176:179], v[92:95], v[148:151], 0\n" \
"ds_read_b128 v[56:59], v29 offset:49152\n" \
"ds_read_b128 v[52:55], v28 offset:49152\n" \
"v_mfma_f32_16x16x32_bf16 a[176:179], v[96:99], v[152:155], a[176:179]\n" \
"s_add_u32 m0, %[mA], 0x400\n" \
"s_add_u32 s84, s62, 0x2000\n" \
"v_mfma_f32_16x16x32_bf16 a[208:211], v[104:107], v[152:155], 0\n" \
"buffer_load_dwordx4 v23, s[64:67], s84 offen lds\n" \
"v_mfma_f32_16x16x32_bf16 a[208:211], v[100:103], v[148:151], a[208:211]\n" \
"ds_read_b128 v[60:63], v28 offset:51200\n" \
"ds_read_b128 v[64:67], v29 offset:51200\n" \
"v_mfma_f32_16x16x32_bf16 a[240:243], v[108:111], v[148:151], 0\n" \
"ds_read_b64_tr_b16 v[148:149], v32 offset:49152\n" \
"ds_read_b64_tr_b16 v[150:151], v32 offset:50176\n" \
"v_mfma_f32_16x16x32_bf16 a[240:243], v[112:115], v[152:155], a[240:243]\n" \
"s_add_u32 m0, %[mB], 0x0\n" \
"s_add_u32 s85, s63, 0x0\n" \
"v_mfma_f32_16x16x32_bf16 a[148:151], v[88:91], v[160:163], 0\n" \
"buffer_load_dwordx4 v26, s[68:71], s85 offen lds\n" \
"v_mfma_f32_16x16x32_bf16 a[148:151], v[84:87], v[156:159], a[148:151]\n" \
"ds_read_b64_tr_b16 v[152:153], v32 offset:57344\n" \
"ds_read_b64_tr_b16 v[154:155], v32 offset:58368\n" \
"v_mfma_f32_16x16x32_bf16 a[180:183], v[92:95], v[156:159], 0\n" \
"ds_read_b128 v[72:75], v29 offset:53248\n" \
"ds_read_b128 v[68:71], v28 offset:53248\n" \
"v_mfma_f32_16x16x32_bf16 a[180:183], v[96:99], v[160:163], a[180:183]\n" \
"s_add_u32 m0, %[mB], 0x4000\n" \
"s_add_u32 s84, s63, 0x0\n" \
"s_nop 0\n" \
"v_mfma_f32_16x16x32_bf16 a[212:215], v[104:107], v[160:163], 0\n" \
"buffer_load_dwordx4 v26, s[72:75], s84 offen lds\n" \
"v_mfma_f32_16x16x32_bf16 a[212:215], v[100:103], v[156:159], a[212:215]\n" \
"ds_read_b128 v[76:79], v28 offset:55296\n" \
"ds_read_b128 v[80:83], v29 offset:55296\n" \
"v_mfma_f32_16x16x32_bf16 a[244:247], v[108:111], v[156:159], 0\n" \
"ds_read_b64_tr_b16 v[156:157], v33 offset:49152\n" \
"ds_read_b64_tr_b16 v[158:159], v33 offset:50176\n" \
"v_mfma_f32_16x16x32_bf16 a[244:247], v[112:115], v[160:163], a[244:247]\n" \
"s_add_u32 m0, %[mA], 0x800\n" \
"s_add_u32 s85, s62, 0x4000\n" \
"v_mfma_f32_16x16x32_bf16 a[152:155], v[88:91], v[168:171], 0\n" \
"buffer_load_dwordx4 v22, s[64:67], s85 offen lds\n" \
"v_mfma_f32_16x16x32_bf16 a[152:155], v[84:87], v[164:167], a[152:155]\n" \
"ds_read_b64_tr_b16 v[160:161], v33 offset:57344\n" \
"ds_read_b64_tr_b16 v[162:163], v33 offset:58368\n" \
"v_mfma_f32_16x16x32_bf16 a[184:187], v[92:95], v[164:167], 0\n" \
"v_cmp_gt_u32_e32 vcc, 0x1b0, v228\n" \
"s_mul_i32 s100, 18, %[w]\n" \
"v_mfma_f32_16x16x32_bf16 a[184:187], v[96:99], v[168:171], a[184:187]\n" \
"s_add_u32 m0, %[mA], 0xc00\n" \
"s_add_u32 s84, s62, 0x6000\n" \
"v_mfma_f32_16x16x32_bf16 a[216:219], v[104:107], v[168:171], 0\n" \
"buffer_load_dwordx4 v23, s[64:67], s84 offen lds\n" \
"v_mfma_f32_16x16x32_bf16 a[216:219], v[100:103], v[164:167], a[216:219]\n" \
"v_cndmask_b32_e32 v193, 0x40000000, v242, vcc\n" \
"v_lshlrev_b32_e32 v243, 1, v233\n" \
"s_nop 0\n" \
"v_mfma_f32_16x16x32_bf16 a[248:251], v[108:111], v[164:167], 0\n" \
"ds_read_b64_tr_b16 v[164:165], v34 offset:49152\n" \
"ds_read_b64_tr_b16 v[166:167], v34 offset:50176\n" \
"v_mfma_f32_16x16x32_bf16 a[248:251], v[112:115], v[168:171], a[248:251]\n" \
"s_add_u32 m0, %[mB], 0x400\n" \
"s_add_u32 s85, s63, 0x8000\n" \
"v_mfma_f32_16x16x32_bf16 a[156:159], v[88:91], v[176:179], 0\n" \
"buffer_load_dwordx4 v26, s[68:71], s85 offen lds\n" \
"v_mfma_f32_16x16x32_bf16 a[156:159], v[84:87], v[172:175], a[156:159]\n" \
"ds_read_b64_tr_b16 v[168:169], v34 offset:57344\n" \
"ds_read_b64_tr_b16 v[170:171], v34 offset:58368\n" \
"v_mfma_f32_16x16x32_bf16 a[188:191], v[92:95], v[172:175], 0\n" \
"v_cmp_gt_u32_e32 vcc, 0x48, v231\n" \
"s_add_u32 s101, s96, s100\n" \
"v_mfma_f32_16x16x32_bf16 a[188:191], v[96:99], v[176:179], a[188:191]\n" \
"s_add_u32 m0, %[mB], 0x4400\n" \
"s_add_u32 s84, s63, 0x8000\n" \
"v_mfma_f32_16x16x32_bf16 a[220:223], v[104:107], v[176:179], 0\n" \
"buffer_load_dwordx4 v26, s[72:75], s84 offen lds\n" \
"v_mfma_f32_16x16x32_bf16 a[220:223], v[100:103], v[172:175], a[220:223]\n" \
"v_cndmask_b32_e32 v195, 0x40000000, v243, vcc\n" \
"v_add_u32_e64 v244, s101, v212\n" \
"v_mfma_f32_16x16x32_bf16 a[252:255], v[108:111], v[172:175], 0\n" \
"ds_read_b64_tr_b16 v[172:173], v35 offset:49152\n" \
"ds_read_b64_tr_b16 v[174:175], v35 offset:50176\n" \
"v_mfma_f32_16x16x32_bf16 a[252:255], v[112:115], v[176:179], a[252:255]\n" \
"s_add_u32 m0, %[mA], 0x1000\n" \
"s_add_u32 s85, s62, 0x8000\n" \
"v_mfma_f32_16x16x32_bf16 a[128:131], v[120:123], v[88:91], 0\n" \
"buffer_load_dwordx4 v22, s[64:67], s85 offen lds\n" \
"v_mfma_f32_16x16x32_bf16 a[128:131], v[116:119], v[84:87], a[128:131]\n" \
"ds_read_b64_tr_b16 v[176:177], v35 offset:57344\n" \
"ds_read_b64_tr_b16 v[178:179], v35 offset:58368\n" \
"v_mfma_f32_16x16x32_bf16 a[160:163], v[116:119], v[92:95], 0\n" \
"v_mul_u32_u24_e32 v245, 0xd80, v244\n" \
"v_cmp_gt_u32_e32 vcc, 18, v212\n" \
"v_mfma_f32_16x16x32_bf16 a[160:163], v[120:123], v[96:99], a[160:163]\n" \
"s_add_u32 m0, %[mA], 0x1400\n" \
"s_add_u32 s84, s62, 0xa000\n" \
"v_mfma_f32_16x16x32_bf16 a[192:195], v[120:123], v[104:107], 0\n" \
"buffer_load_dwordx4 v23, s[64:67], s84 offen lds\n" \
"v_mfma_f32_16x16x32_bf16 a[192:195], v[116:119], v[100:103], a[192:195]\n" \
"v_cndmask_b32_e32 v194, 0x40000000, v245, vcc\n" \
"v_lshrrev_b32_e32 v248, 1, v218\n" \
"s_nop 0\n" \
"v_mfma_f32_16x16x32_bf16 a[224:227], v[116:119], v[108:111], 0\n" \
"ds_read_b64_tr_b16 v[116:117], v32 offset:32768\n" \
"ds_read_b64_tr_b16 v[118:119], v32 offset:33792\n" \
"v_mfma_f32_16x16x32_bf16 a[224:227], v[120:123], v[112:115], a[224:227]\n" \
"s_add_u32 m0, %[mB], 0x800\n" \
"s_add_u32 s85, s63, 0x10000\n" \
"v_mfma_f32_16x16x32_bf16 a[132:135], v[128:131], v[88:91], 0\n" \
"buffer_load_dwordx4 v27, s[68:71], s85 offen lds\n" \
"v_mfma_f32_16x16x32_bf16 a[132:135], v[124:127], v[84:87], a[132:135]\n" \
"ds_read_b64_tr_b16 v[120:121], v32 offset:40960\n" \
"ds_read_b64_tr_b16 v[122:123], v32 offset:41984\n" \
"v_mfma_f32_16x16x32_bf16 a[164:167], v[124:127], v[92:95], 0\n" \
"v_lshrrev_b32_e32 v250, 2, v218\n" \
"v_and_b32_e32 v249, 1, v248\n" \
"v_mfma_f32_16x16x32_bf16 a[164:167], v[128:131], v[96:99], a[164:167]\n" \
"s_add_u32 m0, %[mB], 0x4800\n" \
"s_add_u32 s84, s63, 0x10000\n" \
"v_mfma_f32_16x16x32_bf16 a[196:199], v[128:131], v[104:107], 0\n" \
"buffer_load_dwordx4 v27, s[72:75], s84 offen lds\n" \
"v_mfma_f32_16x16x32_bf16 a[196:199], v[124:127], v[100:103], a[196:199]\n" \
"v_and_b32_e64 v251, 2, v250\n" \
"v_cmp_gt_u32_e32 vcc, 0x1b0, v213\n" \
"v_mfma_f32_16x16x32_bf16 a[228:231], v[124:127], v[108:111], 0\n" \
"L_pf0:\n" \
"s_cbranch_execz L_pf4\n" \
"s_nop 0\n" \
"ds_read_b64_tr_b16 v[124:125], v33 offset:32768\n" \
"ds_read_b64_tr_b16 v[126:127], v33 offset:33792\n" \
"v_mfma_f32_16x16x32_bf16 a[228:231], v[128:131], v[112:115], a[228:231]\n" \
"ds_read_b64_tr_b16 v[128:129], v33 offset:40960\n" \
"ds_read_b64_tr_b16 v[130:131], v33 offset:41984\n" \
"v_mfma_f32_16x16x32_bf16 a[136:139], v[136:139], v[88:91], 0\n" \
"v_or_b32_e32 v214, v249, v251\n" \
"v_cndmask_b32_e32 v247, 7, v221, vcc\n" \
"v_mfma_f32_16x16x32_bf16 a[136:139], v[132:135], v[84:87], a[136:139]\n" \
"v_lshlrev_b32_e32 v216, 1, v214\n" \
"v_xor_b32_e32 v217, v247, v216\n" \
"v_mfma_f32_16x16x32_bf16 a[168:171], v[132:135], v[92:95], 0\n" \
"s_mul_i32 s99, 0x2400, s95\n" \
"v_lshlrev_b32_e32 v246, 7, v218\n" \
"v_mfma_f32_16x16x32_bf16 a[168:171], v[136:139], v[96:99], a[168:171]\n" \
"v_lshlrev_b32_e32 v215, 4, v217\n" \
"s_sub_u32 s96, 0x22000, s99\n" \
"v_mfma_f32_16x16x32_bf16 a[200:203], v[136:139], v[104:107], 0\n" \
"v_add_u32_e32 v220, v246, v215\n" \
"v_add_u32_e32 v222, s96, v220\n" \
"v_mfma_f32_16x16x32_bf16 a[200:203], v[132:135], v[100:103], a[200:203]\n" \
"v_add_u32_e32 v219, 0x24400, v20\n" \
"v_cmp_gt_u32_e32 vcc, 0x1f8, v213\n" \
"v_mfma_f32_16x16x32_bf16 a[232:235], v[132:135], v[108:111], 0\n" \
"ds_read_b64_tr_b16 v[132:133], v34 offset:32768\n" \
"ds_read_b64_tr_b16 v[134:135], v34 offset:33792\n" \
"v_mfma_f32_16x16x32_bf16 a[232:235], v[136:139], v[112:115], a[232:235]\n" \
"ds_read_b64_tr_b16 v[136:137], v34 offset:40960\n" \
"ds_read_b64_tr_b16 v[138:139], v34 offset:41984\n" \
"v_mfma_f32_16x16x32_bf16 a[140:143], v[144:147], v[88:91], 0\n" \
"ds_read_b128 v[88:91], v29 offset:32768\n" \
"v_lshrrev_b32_e32 v224, 1, v233\n" \
"v_mfma_f32_16x16x32_bf16 a[140:143], v[140:143], v[84:87], a[140:143]\n" \
"ds_read_b128 v[84:87], v28 offset:32768\n" \
"v_lshrrev_b32_e32 v227, 2, v233\n" \
"v_mfma_f32_16x16x32_bf16 a[172:175], v[140:143], v[92:95], 0\n" \
"ds_read_b128 v[92:95], v28 offset:34816\n" \
"s_lshl_b32 s100, %[w], 10\n" \
"v_mfma_f32_16x16x32_bf16 a[172:175], v[144:147], v[96:99], a[172:175]\n" \
"ds_read_b128 v[96:99], v29 offset:34816\n" \
"s_mul_i32 s89, 0x12000, s94\n" \
"v_mfma_f32_16x16x32_bf16 a[204:207], v[144:147], v[104:107], 0\n" \
"ds_read_b128 v[104:107], v29 offset:36864\n" \
"v_cndmask_b32_e32 v196, v219, v222, vcc\n" \
"v_mfma_f32_16x16x32_bf16 a[204:207], v[140:143], v[100:103], a[204:207]\n" \
"ds_read_b128 v[100:103], v28 offset:36864\n" \
"v_and_b32_e64 v226, 1, v224\n" \
"v_mfma_f32_16x16x32_bf16 a[236:239], v[140:143], v[108:111], 0\n" \
"ds_read_b64_tr_b16 v[140:141], v35 offset:32768\n" \
"ds_read_b64_tr_b16 v[142:143], v35 offset:33792\n" \
"v_mfma_f32_16x16x32_bf16 a[236:239], v[144:147], v[112:115], a[236:239]\n" \
"s_add_u32 m0, %[mA], 0x1800\n" \
"s_waitcnt lgkmcnt(12)\n" \
"s_nop 0\n" \
"v_mfma_f32_16x16x32_bf16 a[16:19], v[56:59], v[152:155], a[16:19]\n" \
"s_add_u32 s85, s62, 0xc000\n" \
"ds_read_b64_tr_b16 v[144:145], v35 offset:40960\n" \
"v_mfma_f32_16x16x32_bf16 a[16:19], v[52:55], v[148:151], a[16:19]\n" \
"buffer_load_dwordx4 v22, s[64:67], s85 offen lds\n" \
"v_mfma_f32_16x16x32_bf16 a[48:51], v[60:63], v[148:151], a[48:51]\n" \
"s_add_u32 m0, %[mA], 0x1c00\n" \
"s_add_u32 s84, s62, 0xe000\n" \
"v_mfma_f32_16x16x32_bf16 a[48:51], v[64:67], v[152:155], a[48:51]\n" \
"ds_read_b64_tr_b16 v[146:147], v35 offset:41984\n" \
"ds_read_b128 v[108:111], v28 offset:38912\n" \
"v_mfma_f32_16x16x32_bf16 a[80:83], v[72:75], v[152:155], a[80:83]\n" \
"buffer_load_dwordx4 v23, s[64:67], s84 offen lds\n" \
"v_mfma_f32_16x16x32_bf16 a[80:83], v[68:71], v[148:151], a[80:83]\n" \
"s_add_u32 m0, %[mB], 0xc00\n" \
"s_add_u32 s85, s63, 0x18000\n" \
"v_mfma_f32_16x16x32_bf16 a[112:115], v[76:79], v[148:151], a[112:115]\n" \
"ds_read_b128 v[112:115], v29 offset:38912\n" \
"v_and_b32_e64 v229, 2, v227\n" \
"v_mfma_f32_16x16x32_bf16 a[112:115], v[80:83], v[152:155], a[112:115]\n" \
"buffer_load_dwordx4 v27, s[68:71], s85 offen lds\n" \
"v_mfma_f32_16x16x32_bf16 a[20:23], v[56:59], v[160:163], a[20:23]\n" \
"s_add_u32 m0, %[mB], 0x4c00\n" \
"s_add_u32 s84, s63, 0x18000\n" \
"v_mfma_f32_16x16x32_bf16 a[20:23], v[52:55], v[156:159], a[20:23]\n" \
"v_lshrrev_b32_e32 v239, 1, v244\n" \
"v_lshrrev_b32_e32 v242, 2, v244\n" \
"v_mfma_f32_16x16x32_bf16 a[52:55], v[60:63], v[156:159], a[52:55]\n" \
"buffer_load_dwordx4 v27, s[72:75], s84 offen lds\n" \
"v_mfma_f32_16x16x32_bf16 a[52:55], v[64:67], v[160:163], a[52:55]\n" \
"s_add_u32 s101, 0x22000, s100\n" \
"s_add_u32 s98, 0x6000000, s89\n" \
"v_mfma_f32_16x16x32_bf16 a[84:87], v[72:75], v[160:163], a[84:87]\n" \
"v_cmp_gt_u32_e32 vcc, 0x1b0, v228\n" \
"v_or_b32_e32 v232, v226, v229\n" \
"v_mfma_f32_16x16x32_bf16 a[84:87], v[68:71], v[156:159], a[84:87]\n" \
"v_and_b32_e32 v241, 1, v239\n" \
"v_and_b32_e64 v231, 2, v242\n" \
"v_mfma_f32_16x16x32_bf16 a[116:119], v[76:79], v[156:159], a[116:119]\n" \
"v_add_u32_e32 v199, s101, v20\n" \
"s_add_u32 s37, s98, s100\n" \
"v_mfma_f32_16x16x32_bf16 a[116:119], v[80:83], v[160:163], a[116:119]\n" \
"s_mov_b32 m0, %[mS]\n" \
"s_mov_b32 s86, s18\n" \
"v_mfma_f32_16x16x32_bf16 a[24:27], v[56:59], v[168:171], a[24:27]\n" \
"s_mov_b32 s87, s19\n" \
"v_cndmask_b32_e32 v225, 7, v236, vcc\n" \
"v_mfma_f32_16x16x32_bf16 a[24:27], v[52:55], v[164:167], a[24:27]\n" \
"v_lshlrev_b32_e32 v230, 1, v232\n" \
"v_or_b32_e32 v243, v241, v231\n" \
"v_mfma_f32_16x16x32_bf16 a[56:59], v[60:63], v[164:167], a[56:59]\n" \
"s_add_u32 s57, s37, s99\n" \
"s_lshl_b32 s101, s97, 2\n" \
"v_mfma_f32_16x16x32_bf16 a[56:59], v[64:67], v[168:171], a[56:59]\n" \
"v_xor_b32_e32 v235, v225, v230\n" \
"v_xor_b32_e32 v245, 3, v243\n" \
"v_mfma_f32_16x16x32_bf16 a[88:91], v[72:75], v[168:171], a[88:91]\n" \
"s_add_u32 s95, 0x2000, s57\n" \
"s_add_u32 s94, s101, %[w]\n" \
"v_mfma_f32_16x16x32_bf16 a[88:91], v[68:71], v[164:167], a[88:91]\n" \
"s_lshl_b32 s98, %[b], 2\n" \
"v_lshlrev_b32_e32 v223, 7, v233\n" \
"s_mov_b32 exec_lo, 0x3ffff\n" \
"s_mov_b32 exec_hi, 0x0\n" \
"global_load_lds_dword v16, s[86:87]\n" \
"s_mov_b64 exec, -1\n" \
"v_mfma_f32_16x16x32_bf16 a[120:123], v[76:79], v[164:167], a[120:123]\n" \
"v_lshlrev_b32_e32 v237, 4, v235\n" \
"v_lshlrev_b32_e32 v240, 7, v244\n" \
"v_mfma_f32_16x16x32_bf16 a[120:123], v[80:83], v[168:171], a[120:123]\n" \
"v_lshlrev_b32_e32 v221, 5, v245\n" \
"s_mul_i32 s89, 36, s94\n" \
"v_mfma_f32_16x16x32_bf16 a[28:31], v[56:59], v[176:179], a[28:31]\n" \
"s_cmp_eq_u32 %[w], 0\n" \
"s_cselect_b32 s58, s95, 0x40000000\n" \
"v_mfma_f32_16x16x32_bf16 a[28:31], v[52:55], v[172:175], a[28:31]\n" \
"s_add_u32 s100, s98, %[w]\n" \
"v_add_u32_e64 v234, v223, v237\n" \
"v_mfma_f32_16x16x32_bf16 a[60:63], v[60:63], v[172:175], a[60:63]\n" \
"v_add_u32_e32 v248, v240, v221\n" \
"v_add_u32_e32 v250, s89, v212\n" \
"v_mfma_f32_16x16x32_bf16 a[60:63], v[64:67], v[176:179], a[60:63]\n" \
"s_mul_i32 s99, 18, s100\n" \
"v_add_u32_e32 v238, s96, v234\n" \
"v_mfma_f32_16x16x32_bf16 a[92:95], v[72:75], v[176:179], a[92:95]\n" \
"v_add_u32_e32 v218, s96, v248\n" \
"v_lshrrev_b32_e32 v249, 3, v250\n" \
"v_mfma_f32_16x16x32_bf16 a[92:95], v[68:71], v[172:175], a[92:95]\n" \
"s_add_u32 s95, 0xffffffdc, s99\n" \
"v_min_u32_e32 v217, 53, v212\n" \
"v_mfma_f32_16x16x32_bf16 a[124:127], v[76:79], v[172:175], a[124:127]\n" \
"s_lshl_b32 s96, s88, 3\n" \
"v_lshlrev_b32_e32 v251, 6, v249\n" \
"v_mfma_f32_16x16x32_bf16 a[124:127], v[80:83], v[176:179], a[124:127]\n" \
"v_add_u32_e32 v246, s95, v217\n" \
"s_lshr_b32 s94, %[b], 5\n" \
"v_mfma_f32_16x16x32_bf16 a[0:3], v[120:123], v[56:59], a[0:3]\n" \
"s_and_b32 s97, 7, %[b]\n" \
"s_lshr_b32 s99, %[b], 3\n" \
"v_mfma_f32_16x16x32_bf16 a[0:3], v[116:119], v[52:55], a[0:3]\n" \
"v_add_u32_e32 v214, s96, v251\n" \
"s_mul_i32 s95, 0xa00, s94\n" \
"v_mfma_f32_16x16x32_bf16 a[32:35], v[116:119], v[60:63], a[32:35]\n" \
"s_lshl_b32 s98, s97, 2\n" \
"s_and_b32 s100, 3, s99\n" \
"v_mfma_f32_16x16x32_bf16 a[32:35], v[120:123], v[64:67], a[32:35]\n" \
"s_add_u32 s96, 0x5bc1000, s95\n" \
"s_add_u32 s101, s98, s100\n" \
"v_mfma_f32_16x16x32_bf16 a[64:67], v[120:123], v[72:75], a[64:67]\n" \
"s_lshl_b32 s88, s101, 2\n" \
"s_lshl_b32 s95, s94, 3\n" \
"v_mfma_f32_16x16x32_bf16 a[64:67], v[116:119], v[68:71], a[64:67]\n" \
"s_add_u32 s89, s96, s88\n" \
"s_add_u32 s99, s95, s97\n" \
"v_mfma_f32_16x16x32_bf16 a[96:99], v[116:119], v[76:79], a[96:99]\n" \
"v_writelane_b32 v11, s89, 0\n" \
"s_lshl_b32 s88, s99, 10\n" \
"v_mfma_f32_16x16x32_bf16 a[96:99], v[120:123], v[80:83], a[96:99]\n" \
"s_add_u32 s89, 0x5c00000, s88\n" \
"s_lshl_b32 s95, %[w], 8\n" \
"v_mfma_f32_16x16x32_bf16 a[4:7], v[128:131], v[56:59], a[4:7]\n" \
"v_cmp_gt_u32_e32 vcc, 0x1f8, v228\n" \
"s_add_u32 s99, s89, s95\n" \
"v_mfma_f32_16x16x32_bf16 a[4:7], v[124:127], v[52:55], a[4:7]\n" \
"v_cndmask_b32_e32 v197, v219, v238, vcc\n" \
"s_or_b32 s88, 0x200, s99\n" \
"v_mfma_f32_16x16x32_bf16 a[36:39], v[124:127], v[60:63], a[36:39]\n" \
"s_lshl_b32 s89, s100, 2\n" \
"v_cmp_gt_u32_e32 vcc, 18, v212\n" \
"v_mfma_f32_16x16x32_bf16 a[36:39], v[128:131], v[64:67], a[36:39]\n" \
"v_writelane_b32 v11, s88, 1\n" \
"s_add_u32 s95, s99, s89\n" \
"v_mfma_f32_16x16x32_bf16 a[68:71], v[128:131], v[72:75], a[68:71]\n" \
"v_cndmask_b32_e32 v198, v219, v218, vcc\n" \
"v_and_b32_e32 v247, 7, v250\n" \
"v_mfma_f32_16x16x32_bf16 a[68:71], v[124:127], v[68:71], a[68:71]\n" \
"v_writelane_b32 v11, s95, 2\n" \
"s_add_u32 s88, 0x80, s99\n" \
"v_mfma_f32_16x16x32_bf16 a[100:103], v[124:127], v[76:79], a[100:103]\n" \
"v_add_u32_e32 v216, v214, v247\n" \
"v_cmp_gt_u32_e32 vcc, 36, v212\n" \
"v_mfma_f32_16x16x32_bf16 a[100:103], v[128:131], v[80:83], a[100:103]\n" \
"v_writelane_b32 v11, s88, 3\n" \
"s_waitcnt lgkmcnt(0)\n" \
"v_mfma_f32_16x16x32_bf16 a[8:11], v[136:139], v[56:59], a[8:11]\n" \
"s_lshl_b32 s99, s101, 8\n" \
"v_cndmask_b32_e32 v215, v246, v216, vcc\n" \
"v_mfma_f32_16x16x32_bf16 a[8:11], v[132:135], v[52:55], a[8:11]\n" \
"s_add_u32 s89, 0x80, s95\n" \
"s_add_u32 s39, 0x5be8000, s99\n" \
"v_mfma_f32_16x16x32_bf16 a[40:43], v[132:135], v[60:63], a[40:43]\n" \
"s_lshl_b32 s88, s94, 2\n" \
"v_lshlrev_b32_e32 v188, 7, v215\n" \
"v_mfma_f32_16x16x32_bf16 a[40:43], v[136:139], v[64:67], a[40:43]\n" \
"s_add_u32 s95, s39, s88\n" \
"v_lshrrev_b32_e32 v212, 4, v20\n" \
"v_mfma_f32_16x16x32_bf16 a[72:75], v[136:139], v[72:75], a[72:75]\n" \
"v_writelane_b32 v11, s89, 4\n" \
"v_lshlrev_b32_e32 v213, 2, v212\n" \
"v_mfma_f32_16x16x32_bf16 a[72:75], v[132:135], v[68:71], a[72:75]\n" \
"s_cmp_eq_u32 %[w], 3\n" \
"s_cselect_b32 s38, s95, 0x40000000\n" \
"v_mfma_f32_16x16x32_bf16 a[104:107], v[132:135], v[76:79], a[104:107]\n" \
"v_and_b32_e32 v215, 7, v212\n" \
"s_add_u32 s101, s88, s100\n" \
"v_mfma_f32_16x16x32_bf16 a[104:107], v[136:139], v[80:83], a[104:107]\n" \
"v_add_u32_e64 v214, s96, v213\n" \
"s_add_u32 s89, 0x5be8020, s99\n" \
"v_mfma_f32_16x16x32_bf16 a[12:15], v[144:147], v[56:59], a[12:15]\n" \
"s_add_u32 s62, s22, 0x180\n" \
"s_and_b32 s62, s62, 0x3ff\n" \
"v_mfma_f32_16x16x32_bf16 a[12:15], v[140:143], v[52:55], a[12:15]\n" \
"s_lshl_b32 s63, s62, 12\n" \
"v_lshlrev_b32_e32 v216, 2, v215\n" \
"v_mfma_f32_16x16x32_bf16 a[44:47], v[140:143], v[60:63], a[44:47]\n" \
"s_mul_i32 s94, 0x90000, s101\n" \
"v_add_u32_e64 v217, s89, v216\n" \
"v_mfma_f32_16x16x32_bf16 a[44:47], v[144:147], v[64:67], a[44:47]\n" \
"s_add_u32 s95, 0x4240000, s94\n" \
"s_lshl_b32 s96, %[w], 14\n" \
"v_mfma_f32_16x16x32_bf16 a[76:79], v[144:147], v[72:75], a[76:79]\n" \
"s_add_u32 s99, s95, s96\n" \
"s_lshl_b32 s89, s97, 16\n" \
"v_mfma_f32_16x16x32_bf16 a[76:79], v[140:143], v[68:71], a[76:79]\n" \
"v_writelane_b32 v11, s39, 52\n" \
"s_add_u32 s88, s99, s89\n" \
"v_mfma_f32_16x16x32_bf16 a[108:111], v[140:143], v[76:79], a[108:111]\n" \
"s_lshl_b32 s101, s97, 11\n" \
"v_writelane_b32 v11, s88, 37\n" \
"v_mfma_f32_16x16x32_bf16 a[108:111], v[144:147], v[80:83], a[108:111]\n" \
"s_add_u32 s100, 0x3800, s88\n" \
"s_add_u32 s94, s99, s101\n" \
"s_waitcnt vmcnt(1)\n" \
"s_barrier\n" \
"v_mfma_f32_16x16x32_bf16 a[128:131], v[120:123], v[88:91], a[128:131]\n" \
"s_add_u32 m0, %[mA], 0x8000\n" \
"s_add_u32 s85, s62, 0x0\n" \
"s_nop 0\n" \
"v_mfma_f32_16x16x32_bf16 a[128:131], v[116:119], v[84:87], a[128:131]\n" \
"buffer_load_dwordx4 v22, s[64:67], s85 offen lds\n" \
"v_mfma_f32_16x16x32_bf16 a[160:163], v[116:119], v[92:95], a[160:163]\n" \
"ds_read_b128 v[56:59], v29 offset:16384\n" \
"ds_read_b128 v[52:55], v28 offset:16384\n" \
"v_mfma_f32_16x16x32_bf16 a[160:163], v[120:123], v[96:99], a[160:163]\n" \
"s_add_u32 m0, %[mA], 0x8400\n" \
"s_add_u32 s84, s62, 0x2000\n" \
"v_mfma_f32_16x16x32_bf16 a[192:195], v[120:123], v[104:107], a[192:195]\n" \
"buffer_load_dwordx4 v23, s[64:67], s84 offen lds\n" \
"v_mfma_f32_16x16x32_bf16 a[192:195], v[116:119], v[100:103], a[192:195]\n" \
"ds_read_b128 v[60:63], v28 offset:18432\n" \
"ds_read_b128 v[64:67], v29 offset:18432\n" \
"v_mfma_f32_16x16x32_bf16 a[224:227], v[116:119], v[108:111], a[224:227]\n" \
"ds_read_b64_tr_b16 v[116:117], v32 offset:0\n" \
"ds_read_b64_tr_b16 v[118:119], v32 offset:1024\n" \
"v_mfma_f32_16x16x32_bf16 a[224:227], v[120:123], v[112:115], a[224:227]\n" \
"s_add_u32 m0, %[mB], 0x8000\n" \
"s_add_u32 s85, s63, 0x0\n" \
"v_mfma_f32_16x16x32_bf16 a[132:135], v[128:131], v[88:91], a[132:135]\n" \
"buffer_load_dwordx4 v26, s[68:71], s85 offen lds\n" \
"v_mfma_f32_16x16x32_bf16 a[132:135], v[124:127], v[84:87], a[132:135]\n" \
"ds_read_b64_tr_b16 v[120:121], v32 offset:8192\n" \
"ds_read_b64_tr_b16 v[122:123], v32 offset:9216\n" \
"v_mfma_f32_16x16x32_bf16 a[164:167], v[124:127], v[92:95], a[164:167]\n" \
"ds_read_b128 v[72:75], v29 offset:20480\n" \
"ds_read_b128 v[68:71], v28 offset:20480\n" \
"v_mfma_f32_16x16x32_bf16 a[164:167], v[128:131], v[96:99], a[164:167]\n" \
"s_add_u32 m0, %[mB], 0xc000\n" \
"s_add_u32 s84, s63, 0x0\n" \
"v_mfma_f32_16x16x32_bf16 a[196:199], v[128:131], v[104:107], a[196:199]\n" \
"buffer_load_dwordx4 v26, s[72:75], s84 offen lds\n" \
"v_mfma_f32_16x16x32_bf16 a[196:199], v[124:127], v[100:103], a[196:199]\n" \
"ds_read_b128 v[76:79], v28 offset:22528\n" \
"ds_read_b128 v[80:83], v29 offset:22528\n" \
"v_mfma_f32_16x16x32_bf16 a[228:231], v[124:127], v[108:111], a[228:231]\n" \
"ds_read_b64_tr_b16 v[124:125], v33 offset:0\n" \
"ds_read_b64_tr_b16 v[126:127], v33 offset:1024\n" \
"v_mfma_f32_16x16x32_bf16 a[228:231], v[128:131], v[112:115], a[228:231]\n" \
"s_add_u32 m0, %[mA], 0x8800\n" \
"s_add_u32 s85, s62, 0x4000\n" \
"v_mfma_f32_16x16x32_bf16 a[136:139], v[136:139], v[88:91], a[136:139]\n" \
"buffer_load_dwordx4 v22, s[64:67], s85 offen lds\n" \
"v_mfma_f32_16x16x32_bf16 a[136:139], v[132:135], v[84:87], a[136:139]\n" \
"ds_read_b64_tr_b16 v[128:129], v33 offset:8192\n" \
"ds_read_b64_tr_b16 v[130:131], v33 offset:9216\n" \
"v_mfma_f32_16x16x32_bf16 a[168:171], v[132:135], v[92:95], a[168:171]\n" \
"v_writelane_b32 v11, s100, 51\n" \
"s_add_u32 s95, 0x80000, s94\n" \
"v_mfma_f32_16x16x32_bf16 a[168:171], v[136:139], v[96:99], a[168:171]\n" \
"s_add_u32 m0, %[mA], 0x8c00\n" \
"s_add_u32 s84, s62, 0x6000\n" \
"v_mfma_f32_16x16x32_bf16 a[200:203], v[136:139], v[104:107], a[200:203]\n" \
"buffer_load_dwordx4 v23, s[64:67], s84 offen lds\n" \
"v_mfma_f32_16x16x32_bf16 a[200:203], v[132:135], v[100:103], a[200:203]\n" \
"s_lshl_b32 s96, %[w], 17\n" \
"s_nop 0\n" \
"v_writelane_b32 v11, s95, 49\n" \
"v_mfma_f32_16x16x32_bf16 a[232:235], v[132:135], v[108:111], a[232:235]\n" \
"ds_read_b64_tr_b16 v[132:133], v34 offset:0\n" \
"ds_read_b64_tr_b16 v[134:135], v34 offset:1024\n" \
"v_mfma_f32_16x16x32_bf16 a[232:235], v[136:139], v[112:115], a[232:235]\n" \
"s_add_u32 m0, %[mB], 0x8400\n" \
"s_add_u32 s85, s63, 0x8000\n" \
"v_mfma_f32_16x16x32_bf16 a[140:143], v[144:147], v[88:91], a[140:143]\n" \
"buffer_load_dwordx4 v26, s[68:71], s85 offen lds\n" \
"v_mfma_f32_16x16x32_bf16 a[140:143], v[140:143], v[84:87], a[140:143]\n" \
"ds_read_b64_tr_b16 v[136:137], v34 offset:8192\n" \
"ds_read_b64_tr_b16 v[138:139], v34 offset:9216\n" \
"v_mfma_f32_16x16x32_bf16 a[172:175], v[140:143], v[92:95], a[172:175]\n" \
"s_add_u32 s89, s99, s96\n" \
"s_and_b32 s94, 1, s97\n" \
"v_mfma_f32_16x16x32_bf16 a[172:175], v[144:147], v[96:99], a[172:175]\n" \
"s_add_u32 m0, %[mB], 0xc400\n" \
"s_add_u32 s84, s63, 0x8000\n" \
"v_mfma_f32_16x16x32_bf16 a[204:207], v[144:147], v[104:107], a[204:207]\n" \
"buffer_load_dwordx4 v26, s[72:75], s84 offen lds\n" \
"v_mfma_f32_16x16x32_bf16 a[204:207], v[140:143], v[100:103], a[204:207]\n" \
"s_add_u32 s88, s89, s98\n" \
"s_lshl_b32 s95, s94, 10\n" \
"v_mfma_f32_16x16x32_bf16 a[236:239], v[140:143], v[108:111], a[236:239]\n" \
"ds_read_b64_tr_b16 v[140:141], v35 offset:0\n" \
"ds_read_b64_tr_b16 v[142:143], v35 offset:1024\n" \
"v_mfma_f32_16x16x32_bf16 a[236:239], v[144:147], v[112:115], a[236:239]\n" \
"s_add_u32 m0, %[mA], 0x9000\n" \
"s_add_u32 s85, s62, 0x8000\n" \
"v_mfma_f32_16x16x32_bf16 a[144:147], v[88:91], v[152:155], a[144:147]\n" \
"buffer_load_dwordx4 v22, s[64:67], s85 offen lds\n" \
"v_mfma_f32_16x16x32_bf16 a[144:147], v[84:87], v[148:151], a[144:147]\n" \
"ds_read_b64_tr_b16 v[144:145], v35 offset:8192\n" \
"ds_read_b64_tr_b16 v[146:147], v35 offset:9216\n" \
"v_mfma_f32_16x16x32_bf16 a[176:179], v[92:95], v[148:151], a[176:179]\n" \
"s_add_u32 s100, 0x3840, s88\n" \
"s_add_u32 s96, s99, s95\n" \
"v_mfma_f32_16x16x32_bf16 a[176:179], v[96:99], v[152:155], a[176:179]\n" \
"s_add_u32 m0, %[mA], 0x9400\n" \
"s_add_u32 s84, s62, 0xa000\n" \
"v_mfma_f32_16x16x32_bf16 a[208:211], v[104:107], v[152:155], a[208:211]\n" \
"buffer_load_dwordx4 v23, s[64:67], s84 offen lds\n" \
"v_mfma_f32_16x16x32_bf16 a[208:211], v[100:103], v[148:151], a[208:211]\n" \
"v_writelane_b32 v11, s100, 5\n" \
"s_lshr_b32 s101, s97, 1\n" \
"v_mfma_f32_16x16x32_bf16 a[240:243], v[108:111], v[148:151], a[240:243]\n" \
"ds_read_b64_tr_b16 v[148:149], v32 offset:16384\n" \
"ds_read_b64_tr_b16 v[150:151], v32 offset:17408\n" \
"v_mfma_f32_16x16x32_bf16 a[240:243], v[112:115], v[152:155], a[240:243]\n" \
"s_add_u32 m0, %[mB], 0x8800\n" \
"s_add_u32 s85, s63, 0x10000\n" \
"v_mfma_f32_16x16x32_bf16 a[148:151], v[88:91], v[160:163], a[148:151]\n" \
"buffer_load_dwordx4 v27, s[68:71], s85 offen lds\n" \
"v_mfma_f32_16x16x32_bf16 a[148:151], v[84:87], v[156:159], a[148:151]\n" \
"ds_read_b64_tr_b16 v[152:153], v32 offset:24576\n" \
"ds_read_b64_tr_b16 v[154:155], v32 offset:25600\n" \
"v_mfma_f32_16x16x32_bf16 a[180:183], v[92:95], v[156:159], a[180:183]\n" \
"s_add_u32 s89, 0x80000, s96\n" \
"s_add_u32 s88, 1, s97\n" \
"v_mfma_f32_16x16x32_bf16 a[180:183], v[96:99], v[160:163], a[180:183]\n" \
"s_add_u32 m0, %[mB], 0xc800\n" \
"s_add_u32 s84, s63, 0x10000\n" \
"v_mfma_f32_16x16x32_bf16 a[212:215], v[104:107], v[160:163], a[212:215]\n" \
"buffer_load_dwordx4 v27, s[72:75], s84 offen lds\n" \
"v_mfma_f32_16x16x32_bf16 a[212:215], v[100:103], v[156:159], a[212:215]\n" \
"s_cmp_eq_u32 %[w], s101\n" \
"s_cselect_b32 s98, s89, 0x40000000\n" \
"v_mfma_f32_16x16x32_bf16 a[244:247], v[108:111], v[156:159], a[244:247]\n" \
"ds_read_b64_tr_b16 v[156:157], v33 offset:16384\n" \
"ds_read_b64_tr_b16 v[158:159], v33 offset:17408\n" \
"v_mfma_f32_16x16x32_bf16 a[244:247], v[112:115], v[160:163], a[244:247]\n" \
"ds_read_b64_tr_b16 v[160:161], v33 offset:24576\n" \
"ds_read_b64_tr_b16 v[162:163], v33 offset:25600\n" \
"v_mfma_f32_16x16x32_bf16 a[152:155], v[88:91], v[168:171], a[152:155]\n" \
"s_and_b32 s100, 7, s88\n" \
"s_lshl_b32 s94, s100, 16\n" \
"v_mfma_f32_16x16x32_bf16 a[152:155], v[84:87], v[164:167], a[152:155]\n" \
"s_add_u32 s96, 2, s97\n" \
"v_writelane_b32 v11, s98, 38\n" \
"v_mfma_f32_16x16x32_bf16 a[184:187], v[92:95], v[164:167], a[184:187]\n" \
"s_add_u32 s95, s99, s94\n" \
"s_and_b32 s101, 7, s96\n" \
"v_mfma_f32_16x16x32_bf16 a[184:187], v[96:99], v[168:171], a[184:187]\n" \
"s_add_u32 s100, 3, s97\n" \
"v_writelane_b32 v11, s95, 42\n" \
"v_mfma_f32_16x16x32_bf16 a[216:219], v[104:107], v[168:171], a[216:219]\n" \
"s_lshl_b32 s89, s101, 16\n" \
"s_and_b32 s94, 7, s100\n" \
"v_mfma_f32_16x16x32_bf16 a[216:219], v[100:103], v[164:167], a[216:219]\n" \
"s_add_u32 s98, s99, s89\n" \
"s_lshl_b32 s95, s94, 16\n" \
"s_nop 0\n" \
"v_mfma_f32_16x16x32_bf16 a[248:251], v[108:111], v[164:167], a[248:251]\n" \
"ds_read_b64_tr_b16 v[164:165], v34 offset:16384\n" \
"ds_read_b64_tr_b16 v[166:167], v34 offset:17408\n" \
"v_mfma_f32_16x16x32_bf16 a[248:251], v[112:115], v[168:171], a[248:251]\n" \
"ds_read_b64_tr_b16 v[168:169], v34 offset:24576\n" \
"ds_read_b64_tr_b16 v[170:171], v34 offset:25600\n" \
"v_mfma_f32_16x16x32_bf16 a[156:159], v[88:91], v[176:179], a[156:159]\n" \
"ds_read_b128 v[88:91], v29 offset:0\n" \
"s_add_u32 s88, 0x800, s98\n" \
"v_mfma_f32_16x16x32_bf16 a[156:159], v[84:87], v[172:175], a[156:159]\n" \
"ds_read_b128 v[84:87], v28 offset:0\n" \
"s_add_u32 s96, s99, s95\n" \
"v_mfma_f32_16x16x32_bf16 a[188:191], v[92:95], v[172:175], a[188:191]\n" \
"ds_read_b128 v[92:95], v28 offset:2048\n" \
"s_add_u32 s89, 4, s97\n" \
"v_mfma_f32_16x16x32_bf16 a[188:191], v[96:99], v[176:179], a[188:191]\n" \
"ds_read_b128 v[96:99], v29 offset:2048\n" \
"v_writelane_b32 v11, s88, 43\n" \
"v_mfma_f32_16x16x32_bf16 a[220:223], v[104:107], v[176:179], a[220:223]\n" \
"ds_read_b128 v[104:107], v29 offset:4096\n" \
"s_add_u32 s101, 0x1000, s96\n" \
"v_mfma_f32_16x16x32_bf16 a[220:223], v[100:103], v[172:175], a[220:223]\n" \
"ds_read_b128 v[100:103], v28 offset:4096\n" \
"s_and_b32 s98, 7, s89\n" \
"v_mfma_f32_16x16x32_bf16 a[252:255], v[108:111], v[172:175], a[252:255]\n" \
"ds_read_b64_tr_b16 v[172:173], v35 offset:16384\n" \
"ds_read_b64_tr_b16 v[174:175], v35 offset:17408\n" \
"v_mfma_f32_16x16x32_bf16 a[252:255], v[112:115], v[176:179], a[252:255]\n" \
"s_add_u32 m0, %[mA], 0x9800\n" \
"s_waitcnt lgkmcnt(12)\n" \
"v_mfma_f32_16x16x32_bf16 a[0:3], v[120:123], v[56:59], a[0:3]\n" \
"s_add_u32 s85, s62, 0xc000\n" \
"ds_read_b64_tr_b16 v[176:177], v35 offset:24576\n" \
"v_mfma_f32_16x16x32_bf16 a[0:3], v[116:119], v[52:55], a[0:3]\n" \
"buffer_load_dwordx4 v22, s[64:67], s85 offen lds\n" \
"v_mfma_f32_16x16x32_bf16 a[32:35], v[116:119], v[60:63], a[32:35]\n" \
"s_add_u32 m0, %[mA], 0x9c00\n" \
"s_add_u32 s84, s62, 0xe000\n" \
"v_mfma_f32_16x16x32_bf16 a[32:35], v[120:123], v[64:67], a[32:35]\n" \
"ds_read_b64_tr_b16 v[178:179], v35 offset:25600\n" \
"ds_read_b128 v[108:111], v28 offset:6144\n" \
"v_mfma_f32_16x16x32_bf16 a[64:67], v[120:123], v[72:75], a[64:67]\n" \
"buffer_load_dwordx4 v23, s[64:67], s84 offen lds\n" \
"v_mfma_f32_16x16x32_bf16 a[64:67], v[116:119], v[68:71], a[64:67]\n" \
"s_add_u32 m0, %[mB], 0x8c00\n" \
"s_add_u32 s85, s63, 0x18000\n" \
"v_mfma_f32_16x16x32_bf16 a[96:99], v[116:119], v[76:79], a[96:99]\n" \
"ds_read_b128 v[112:115], v29 offset:6144\n" \
"s_add_u32 s95, 5, s97\n" \
"v_mfma_f32_16x16x32_bf16 a[96:99], v[120:123], v[80:83], a[96:99]\n" \
"buffer_load_dwordx4 v27, s[68:71], s85 offen lds\n" \
"v_mfma_f32_16x16x32_bf16 a[4:7], v[128:131], v[56:59], a[4:7]\n" \
"s_add_u32 m0, %[mB], 0xcc00\n" \
"s_add_u32 s84, s63, 0x18000\n" \
"v_mfma_f32_16x16x32_bf16 a[4:7], v[124:127], v[52:55], a[4:7]\n" \
"v_writelane_b32 v11, s101, 44\n" \
"s_lshl_b32 s88, s98, 16\n" \
"v_mfma_f32_16x16x32_bf16 a[36:39], v[124:127], v[60:63], a[36:39]\n" \
"buffer_load_dwordx4 v27, s[72:75], s84 offen lds\n" \
"v_mfma_f32_16x16x32_bf16 a[36:39], v[128:131], v[64:67], a[36:39]\n" \
"s_and_b32 s96, 7, s95\n" \
"s_add_u32 s100, s99, s88\n" \
"v_mfma_f32_16x16x32_bf16 a[68:71], v[128:131], v[72:75], a[68:71]\n" \
"s_lshl_b32 s101, s96, 16\n" \
"s_add_u32 s94, 0x1800, s100\n" \
"v_mfma_f32_16x16x32_bf16 a[68:71], v[124:127], v[68:71], a[68:71]\n" \
"buffer_load_dwordx4 v[200:203], v192, s[76:79], s56 offen offset:0\n" \
"v_mfma_f32_16x16x32_bf16 a[100:103], v[124:127], v[76:79], a[100:103]\n" \
"s_add_u32 s89, s99, s101\n" \
"s_add_u32 s88, 6, s97\n" \
"v_mfma_f32_16x16x32_bf16 a[100:103], v[128:131], v[80:83], a[100:103]\n" \
"v_writelane_b32 v11, s94, 45\n" \
"s_add_u32 s98, 0x2000, s89\n" \
"v_mfma_f32_16x16x32_bf16 a[8:11], v[136:139], v[56:59], a[8:11]\n" \
"s_and_b32 s100, 7, s88\n" \
"s_add_u32 s101, 7, s97\n" \
"v_mfma_f32_16x16x32_bf16 a[8:11], v[132:135], v[52:55], a[8:11]\n" \
"v_writelane_b32 v11, s98, 46\n" \
"s_lshl_b32 s94, s100, 16\n" \
"v_mfma_f32_16x16x32_bf16 a[40:43], v[132:135], v[60:63], a[40:43]\n" \
"buffer_load_dwordx4 v[204:207], v193, s[76:79], s56 offen offset:0\n" \
"v_mfma_f32_16x16x32_bf16 a[40:43], v[136:139], v[64:67], a[40:43]\n" \
"s_and_b32 s89, 7, s101\n" \
"s_add_u32 s95, s99, s94\n" \
"v_mfma_f32_16x16x32_bf16 a[72:75], v[136:139], v[72:75], a[72:75]\n" \
"s_lshl_b32 s98, s89, 16\n" \
"s_add_u32 s96, 0x2800, s95\n" \
"v_mfma_f32_16x16x32_bf16 a[72:75], v[132:135], v[68:71], a[72:75]\n" \
"s_add_u32 s88, s99, s98\n" \
"v_writelane_b32 v11, s96, 47\n" \
"v_mfma_f32_16x16x32_bf16 a[104:107], v[132:135], v[76:79], a[104:107]\n" \
"s_add_u32 s100, 0x3000, s88\n" \
"v_writelane_b32 v11, s100, 48\n" \
"v_mfma_f32_16x16x32_bf16 a[104:107], v[136:139], v[80:83], a[104:107]\n" \
"v_readfirstlane_b32 %[tag], v180\n" \
"s_lshr_b32 s94, %[b], 5\n" \
"v_mfma_f32_16x16x32_bf16 a[12:15], v[144:147], v[56:59], a[12:15]\n" \
"buffer_load_dword v208, v194, s[76:79], s56 offen offset:32\n" \
"v_mfma_f32_16x16x32_bf16 a[12:15], v[140:143], v[52:55], a[12:15]\n" \
"s_add_u32 %[tag], %[tag], 0x9E3779B1\n" \
"v_mov_b32_e64 v45, %[tag]\n" \
"v_mfma_f32_16x16x32_bf16 a[44:47], v[140:143], v[60:63], a[44:47]\n" \
"s_sub_u32 s95, 15, s94\n" \
"s_and_b32 s100, 1, %[w]\n" \
"v_mfma_f32_16x16x32_bf16 a[44:47], v[144:147], v[64:67], a[44:47]\n" \
"s_cmp_lt_u32 %[w], 2\n" \
"s_cselect_b32 s96, s95, s94\n" \
"v_mfma_f32_16x16x32_bf16 a[76:79], v[144:147], v[72:75], a[76:79]\n" \
"s_lshl_b32 s97, s96, 7\n" \
"s_add_u32 s98, 0xfffffff0, s97\n" \
"v_mfma_f32_16x16x32_bf16 a[76:79], v[140:143], v[68:71], a[76:79]\n" \
"s_cmp_eq_u32 s97, 0\n" \
"s_cselect_b32 s99, 0, s98\n" \
"v_mfma_f32_16x16x32_bf16 a[108:111], v[140:143], v[76:79], a[108:111]\n" \
"buffer_load_dword v209, v194, s[76:79], s56 offen offset:1184\n" \
"v_mfma_f32_16x16x32_bf16 a[108:111], v[144:147], v[80:83], a[108:111]\n" \
"v_cmp_gt_u32_e32 vcc, 32, v212\n" \
"s_lshl_b32 s101, s100, 3\n" \
"v_mfma_f32_16x16x32_bf16 a[16:19], v[56:59], v[152:155], a[16:19]\n" \
"s_add_u32 s88, s99, s101\n" \
"v_lshrrev_b32_e32 v212, 4, v20\n" \
"v_mfma_f32_16x16x32_bf16 a[16:19], v[52:55], v[148:151], a[16:19]\n" \
"v_cndmask_b32_e32 v17, v217, v214, vcc\n" \
"v_lshrrev_b32_e32 v213, 3, v212\n" \
"v_mfma_f32_16x16x32_bf16 a[48:51], v[60:63], v[148:151], a[48:51]\n" \
"s_add_u32 s99, s97, s100\n" \
"v_add_u32_e32 v214, s88, v213\n" \
"v_mfma_f32_16x16x32_bf16 a[48:51], v[64:67], v[152:155], a[48:51]\n" \
"s_lshl_b32 s89, s100, 2\n" \
"v_lshrrev_b32_e32 v217, 4, v212\n" \
"v_mfma_f32_16x16x32_bf16 a[80:83], v[72:75], v[152:155], a[80:83]\n" \
"buffer_load_dword v211, v194, s[76:79], s56 offen offset:2336\n" \
"v_mfma_f32_16x16x32_bf16 a[80:83], v[68:71], v[148:151], a[80:83]\n" \
"s_mov_b32 s76, s14\n" \
"s_and_b32 s77, s15, 0xffff\n" \
"v_mfma_f32_16x16x32_bf16 a[112:115], v[76:79], v[148:151], a[112:115]\n" \
"s_mov_b32 s78, 0x480\n" \
"s_add_u32 s56, s57, 0x1000\n" \
"v_mfma_f32_16x16x32_bf16 a[112:115], v[80:83], v[152:155], a[112:115]\n" \
"s_lshl_b32 s96, s100, 6\n" \
"s_lshr_b32 s101, s97, 4\n" \
"v_mfma_f32_16x16x32_bf16 a[20:23], v[56:59], v[160:163], a[20:23]\n" \
"s_cmp_eq_u32 s99, 0\n" \
"s_cselect_b32 s49, 0xff800000, 0\n" \
"v_mfma_f32_16x16x32_bf16 a[20:23], v[52:55], v[156:159], a[20:23]\n" \
"v_add_u32_e32 v218, s89, v217\n" \
"s_add_u32 s98, s97, s96\n" \
"v_mfma_f32_16x16x32_bf16 a[52:55], v[60:63], v[156:159], a[52:55]\n" \
"buffer_load_ushort v210, v195, s[76:79], 0 offen\n" \
"v_mfma_f32_16x16x32_bf16 a[52:55], v[64:67], v[160:163], a[52:55]\n" \
"s_add_u32 s88, s101, s89\n" \
"v_add_u32_e32 v221, s98, v213\n" \
"v_mfma_f32_16x16x32_bf16 a[84:87], v[72:75], v[160:163], a[84:87]\n" \
"s_mul_i32 s96, 0x24800, s88\n" \
"s_and_b32 s99, 7, %[b]\n" \
"v_mfma_f32_16x16x32_bf16 a[84:87], v[68:71], v[156:159], a[84:87]\n" \
"s_lshr_b32 s89, %[b], 3\n" \
"s_add_u32 s98, 0x3000000, s96\n" \
"v_mfma_f32_16x16x32_bf16 a[116:119], v[76:79], v[156:159], a[116:119]\n" \
"s_lshl_b32 s97, s99, 8\n" \
"s_and_b32 s88, 3, s89\n" \
"v_mfma_f32_16x16x32_bf16 a[116:119], v[80:83], v[160:163], a[116:119]\n" \
"s_add_u32 s101, s98, s97\n" \
"s_waitcnt lgkmcnt(0)\n" \
"v_mfma_f32_16x16x32_bf16 a[24:27], v[56:59], v[168:171], a[24:27]\n" \
"s_lshl_b32 s96, s88, 2\n" \
"s_add_u32 s97, s101, s96\n" \
"v_mfma_f32_16x16x32_bf16 a[24:27], v[52:55], v[164:167], a[24:27]\n" \
"s_lshl_b32 s101, s99, 2\n" \
"v_and_b32_e32 v216, 7, v212\n" \
"v_mfma_f32_16x16x32_bf16 a[56:59], v[60:63], v[164:167], a[56:59]\n" \
"s_add_u32 s51, 0x4000, s97\n" \
"s_add_u32 s96, s101, s88\n" \
"v_mfma_f32_16x16x32_bf16 a[56:59], v[64:67], v[168:171], a[56:59]\n" \
"v_xor_b32_e32 v219, v216, v218\n" \
"s_lshl_b32 s97, s96, 9\n" \
"v_mfma_f32_16x16x32_bf16 a[88:91], v[72:75], v[168:171], a[88:91]\n" \
"s_lshl_b32 s101, s89, 2\n" \
"v_mul_u32_u24_e32 v215, 0x480, v214\n" \
"v_mfma_f32_16x16x32_bf16 a[88:91], v[68:71], v[164:167], a[88:91]\n" \
"v_lshlrev_b32_e32 v220, 4, v219\n" \
"v_lshrrev_b32_e32 v229, 2, v212\n" \
"v_mfma_f32_16x16x32_bf16 a[120:123], v[76:79], v[164:167], a[120:123]\n" \
"v_and_b32_e32 v234, 3, v212\n" \
"s_add_u32 s50, s98, s97\n" \
"v_mfma_f32_16x16x32_bf16 a[120:123], v[80:83], v[168:171], a[120:123]\n" \
"s_lshl_b32 s99, %[w], 10\n" \
"s_add_u32 s88, s101, %[w]\n" \
"v_mfma_f32_16x16x32_bf16 a[28:31], v[56:59], v[176:179], a[28:31]\n" \
"s_add_u32 s62, s22, 0x200\n" \
"s_and_b32 s62, s62, 0x3ff\n" \
"v_mfma_f32_16x16x32_bf16 a[28:31], v[52:55], v[172:175], a[28:31]\n" \
"s_lshl_b32 s63, s62, 12\n" \
"v_add_u32_e32 v21, v215, v220\n" \
"v_mfma_f32_16x16x32_bf16 a[60:63], v[60:63], v[172:175], a[60:63]\n" \
"v_add_u32_e64 v225, 4, v217\n" \
"s_add_u32 s40, 0x14000, s99\n" \
"v_mfma_f32_16x16x32_bf16 a[60:63], v[64:67], v[176:179], a[60:63]\n" \
"s_lshl_b32 s41, s88, 7\n" \
"s_lshl_b32 s98, s96, 20\n" \
"v_mfma_f32_16x16x32_bf16 a[92:95], v[72:75], v[176:179], a[92:95]\n" \
"v_lshrrev_b32_e32 v212, 4, v20\n" \
"v_xor_b32_e32 v223, v216, v217\n" \
"v_mfma_f32_16x16x32_bf16 a[92:95], v[68:71], v[172:175], a[92:95]\n" \
"v_xor_b32_e64 v226, v216, v225\n" \
"s_add_u32 s97, 0x1000000, s98\n" \
"v_mfma_f32_16x16x32_bf16 a[124:127], v[76:79], v[172:175], a[124:127]\n" \
"L_pf1:\n" \
"s_cbranch_execz L_pf5\n" \
"s_nop 0\n" \
"s_lshl_b32 s99, %[w], 12\n" \
"s_lshl_b32 s89, %[w], 11\n" \
"v_mfma_f32_16x16x32_bf16 a[124:127], v[80:83], v[176:179], a[124:127]\n" \
"s_lshr_b32 s88, %[w], 1\n" \
"v_and_b32_e32 v215, 15, v212\n" \
"s_waitcnt vmcnt(6)\n" \
"s_barrier\n" \
"v_mfma_f32_16x16x32_bf16 a[144:147], v[88:91], v[152:155], a[144:147]\n" \
"s_add_u32 m0, %[mA], 0x0\n" \
"s_add_u32 s85, s62, 0x0\n" \
"v_mfma_f32_16x16x32_bf16 a[144:147], v[84:87], v[148:151], a[144:147]\n" \
"buffer_load_dwordx4 v22, s[64:67], s85 offen lds\n" \
"v_mfma_f32_16x16x32_bf16 a[176:179], v[92:95], v[148:151], a[176:179]\n" \
"ds_read_b128 v[56:59], v29 offset:49152\n" \
"ds_read_b128 v[52:55], v28 offset:49152\n" \
"v_mfma_f32_16x16x32_bf16 a[176:179], v[96:99], v[152:155], a[176:179]\n" \
"s_add_u32 m0, %[mA], 0x400\n" \
"s_add_u32 s84, s62, 0x2000\n" \
"v_mfma_f32_16x16x32_bf16 a[208:211], v[104:107], v[152:155], a[208:211]\n" \
"buffer_load_dwordx4 v23, s[64:67], s84 offen lds\n" \
"v_mfma_f32_16x16x32_bf16 a[208:211], v[100:103], v[148:151], a[208:211]\n" \
"ds_read_b128 v[60:63], v28 offset:51200\n" \
"ds_read_b128 v[64:67], v29 offset:51200\n" \
"v_mfma_f32_16x16x32_bf16 a[240:243], v[108:111], v[148:151], a[240:243]\n" \
"ds_read_b64_tr_b16 v[148:149], v32 offset:49152\n" \
"ds_read_b64_tr_b16 v[150:151], v32 offset:50176\n" \
"v_mfma_f32_16x16x32_bf16 a[240:243], v[112:115], v[152:155], a[240:243]\n" \
"s_add_u32 m0, %[mB], 0x0\n" \
"s_add_u32 s85, s63, 0x0\n" \
"v_mfma_f32_16x16x32_bf16 a[148:151], v[88:91], v[160:163], a[148:151]\n" \
"buffer_load_dwordx4 v26, s[68:71], s85 offen lds\n" \
"v_mfma_f32_16x16x32_bf16 a[148:151], v[84:87], v[156:159], a[148:151]\n" \
"ds_read_b64_tr_b16 v[152:153], v32 offset:57344\n" \
"ds_read_b64_tr_b16 v[154:155], v32 offset:58368\n" \
"v_mfma_f32_16x16x32_bf16 a[180:183], v[92:95], v[156:159], a[180:183]\n" \
"ds_read_b128 v[72:75], v29 offset:53248\n" \
"ds_read_b128 v[68:71], v28 offset:53248\n" \
"v_mfma_f32_16x16x32_bf16 a[180:183], v[96:99], v[160:163], a[180:183]\n" \
"s_add_u32 m0, %[mB], 0x4000\n" \
"s_add_u32 s84, s63, 0x0\n" \
"s_nop 0\n" \
"v_mfma_f32_16x16x32_bf16 a[212:215], v[104:107], v[160:163], a[212:215]\n" \
"buffer_load_dwordx4 v26, s[72:75], s84 offen lds\n" \
"v_mfma_f32_16x16x32_bf16 a[212:215], v[100:103], v[156:159], a[212:215]\n" \
"ds_read_b128 v[76:79], v28 offset:55296\n" \
"ds_read_b128 v[80:83], v29 offset:55296\n" \
"v_mfma_f32_16x16x32_bf16 a[244:247], v[108:111], v[156:159], a[244:247]\n" \
"ds_read_b64_tr_b16 v[156:157], v33 offset:49152\n" \
"ds_read_b64_tr_b16 v[158:159], v33 offset:50176\n" \
"v_mfma_f32_16x16x32_bf16 a[244:247], v[112:115], v[160:163], a[244:247]\n" \
"s_add_u32 m0, %[mA], 0x800\n" \
"s_add_u32 s85, s62, 0x4000\n" \
"v_mfma_f32_16x16x32_bf16 a[152:155], v[88:91], v[168:171], a[152:155]\n" \
"buffer_load_dwordx4 v22, s[64:67], s85 offen lds\n" \
"v_mfma_f32_16x16x32_bf16 a[152:155], v[84:87], v[164:167], a[152:155]\n" \
"ds_read_b64_tr_b16 v[160:161], v33 offset:57344\n" \
"ds_read_b64_tr_b16 v[162:163], v33 offset:58368\n" \
"v_mfma_f32_16x16x32_bf16 a[184:187], v[92:95], v[164:167], a[184:187]\n" \
"s_add_u32 s52, s97, s99\n" \
"s_add_u32 s101, s97, s89\n" \
"v_mfma_f32_16x16x32_bf16 a[184:187], v[96:99], v[168:171], a[184:187]\n" \
"s_add_u32 m0, %[mA], 0xc00\n" \
"s_add_u32 s84, s62, 0x6000\n" \
"v_mfma_f32_16x16x32_bf16 a[216:219], v[104:107], v[168:171], a[216:219]\n" \
"buffer_load_dwordx4 v23, s[64:67], s84 offen lds\n" \
"v_mfma_f32_16x16x32_bf16 a[216:219], v[100:103], v[164:167], a[216:219]\n" \
"s_lshl_b32 s96, s88, 15\n" \
"v_lshrrev_b32_e32 v213, 4, v212\n" \
"v_mfma_f32_16x16x32_bf16 a[248:251], v[108:111], v[164:167], a[248:251]\n" \
"ds_read_b64_tr_b16 v[164:165], v34 offset:49152\n" \
"ds_read_b64_tr_b16 v[166:167], v34 offset:50176\n" \
"v_mfma_f32_16x16x32_bf16 a[248:251], v[112:115], v[168:171], a[248:251]\n" \
"s_add_u32 m0, %[mB], 0x400\n" \
"s_add_u32 s85, s63, 0x8000\n" \
"v_mfma_f32_16x16x32_bf16 a[156:159], v[88:91], v[176:179], a[156:159]\n" \
"buffer_load_dwordx4 v26, s[68:71], s85 offen lds\n" \
"v_mfma_f32_16x16x32_bf16 a[156:159], v[84:87], v[172:175], a[156:159]\n" \
"ds_read_b64_tr_b16 v[168:169], v34 offset:57344\n" \
"ds_read_b64_tr_b16 v[170:171], v34 offset:58368\n" \
"v_mfma_f32_16x16x32_bf16 a[188:191], v[92:95], v[172:175], a[188:191]\n" \
"v_and_b32_e32 v216, 1, v215\n" \
"v_lshlrev_b32_e32 v228, 3, v217\n" \
"v_mfma_f32_16x16x32_bf16 a[188:191], v[96:99], v[176:179], a[188:191]\n" \
"s_add_u32 m0, %[mB], 0x4400\n" \
"s_add_u32 s84, s63, 0x8000\n" \
"v_mfma_f32_16x16x32_bf16 a[220:223], v[104:107], v[176:179], a[220:223]\n" \
"buffer_load_dwordx4 v26, s[72:75], s84 offen lds\n" \
"v_mfma_f32_16x16x32_bf16 a[220:223], v[100:103], v[172:175], a[220:223]\n" \
"v_and_b32_e64 v238, 1, v217\n" \
"s_add_u32 s54, 0x10000, s101\n" \
"v_mfma_f32_16x16x32_bf16 a[252:255], v[108:111], v[172:175], a[252:255]\n" \
"ds_read_b64_tr_b16 v[172:173], v35 offset:49152\n" \
"ds_read_b64_tr_b16 v[174:175], v35 offset:50176\n" \
"v_mfma_f32_16x16x32_bf16 a[252:255], v[112:115], v[176:179], a[252:255]\n" \
"s_add_u32 m0, %[mA], 0x1000\n" \
"s_add_u32 s85, s62, 0x8000\n" \
"v_mfma_f32_16x16x32_bf16 a[128:131], v[120:123], v[88:91], a[128:131]\n" \
"buffer_load_dwordx4 v22, s[64:67], s85 offen lds\n" \
"v_mfma_f32_16x16x32_bf16 a[128:131], v[116:119], v[84:87], a[128:131]\n" \
"ds_read_b64_tr_b16 v[176:177], v35 offset:57344\n" \
"ds_read_b64_tr_b16 v[178:179], v35 offset:58368\n" \
"v_mfma_f32_16x16x32_bf16 a[160:163], v[116:119], v[92:95], a[160:163]\n" \
"s_add_u32 s98, s97, s96\n" \
"s_lshl_b32 s99, s100, 11\n" \
"v_mfma_f32_16x16x32_bf16 a[160:163], v[120:123], v[96:99], a[160:163]\n" \
"s_add_u32 m0, %[mA], 0x1400\n" \
"s_add_u32 s84, s62, 0xa000\n" \
"v_mfma_f32_16x16x32_bf16 a[192:195], v[120:123], v[104:107], a[192:195]\n" \
"buffer_load_dwordx4 v23, s[64:67], s84 offen lds\n" \
"v_mfma_f32_16x16x32_bf16 a[192:195], v[116:119], v[100:103], a[192:195]\n" \
"v_lshlrev_b32_e32 v214, 2, v213\n" \
"s_nop 0\n" \
"v_mov_b32_e32 v218, 0x1000c0c\n" \
"v_mfma_f32_16x16x32_bf16 a[224:227], v[116:119], v[108:111], a[224:227]\n" \
"ds_read_b64_tr_b16 v[116:117], v32 offset:32768\n" \
"ds_read_b64_tr_b16 v[118:119], v32 offset:33792\n" \
"v_mfma_f32_16x16x32_bf16 a[224:227], v[120:123], v[112:115], a[224:227]\n" \
"s_add_u32 m0, %[mB], 0x800\n" \
"s_add_u32 s85, s63, 0x10000\n" \
"v_mfma_f32_16x16x32_bf16 a[132:135], v[128:131], v[88:91], a[132:135]\n" \
"buffer_load_dwordx4 v27, s[68:71], s85 offen lds\n" \
"v_mfma_f32_16x16x32_bf16 a[132:135], v[124:127], v[84:87], a[132:135]\n" \
"ds_read_b64_tr_b16 v[120:121], v32 offset:40960\n" \
"ds_read_b64_tr_b16 v[122:123], v32 offset:41984\n" \
"v_mfma_f32_16x16x32_bf16 a[164:167], v[124:127], v[92:95], a[164:167]\n" \
"v_mov_b32_e32 v219, 0xc0c0100\n" \
"v_cmp_ne_u32_e32 vcc, 0, v216\n" \
"v_mfma_f32_16x16x32_bf16 a[164:167], v[128:131], v[96:99], a[164:167]\n" \
"s_add_u32 m0, %[mB], 0x4800\n" \
"s_add_u32 s84, s63, 0x10000\n" \
"v_mfma_f32_16x16x32_bf16 a[196:199], v[128:131], v[104:107], a[196:199]\n" \
"buffer_load_dwordx4 v27, s[72:75], s84 offen lds\n" \
"v_mfma_f32_16x16x32_bf16 a[196:199], v[124:127], v[100:103], a[196:199]\n" \
"v_mul_u32_u24_e32 v222, 0x480, v221\n" \
"s_add_u32 s89, s98, s99\n" \
"v_mfma_f32_16x16x32_bf16 a[228:231], v[124:127], v[108:111], a[228:231]\n" \
"ds_read_b64_tr_b16 v[124:125], v33 offset:32768\n" \
"ds_read_b64_tr_b16 v[126:127], v33 offset:33792\n" \
"v_mfma_f32_16x16x32_bf16 a[228:231], v[128:131], v[112:115], a[228:231]\n" \
"ds_read_b64_tr_b16 v[128:129], v33 offset:40960\n" \
"ds_read_b64_tr_b16 v[130:131], v33 offset:41984\n" \
"v_mfma_f32_16x16x32_bf16 a[136:139], v[136:139], v[88:91], a[136:139]\n" \
"s_lshl_b32 s101, s94, 16\n" \
"v_add_u32_e32 v217, v214, v216\n" \
"v_mfma_f32_16x16x32_bf16 a[136:139], v[132:135], v[84:87], a[136:139]\n" \
"v_cndmask_b32_e32 v220, v219, v218, vcc\n" \
"v_lshlrev_b32_e32 v224, 4, v223\n" \
"v_mfma_f32_16x16x32_bf16 a[168:171], v[132:135], v[92:95], a[168:171]\n" \
"s_add_u32 s42, s89, s101\n" \
"s_lshl_b32 s88, s95, 16\n" \
"v_mfma_f32_16x16x32_bf16 a[168:171], v[136:139], v[96:99], a[168:171]\n" \
"s_mul_i32 s97, 0x1800, s100\n" \
"v_mov_b32_e32 v221, 0xc0c0c0c\n" \
"v_mfma_f32_16x16x32_bf16 a[200:203], v[136:139], v[104:107], a[200:203]\n" \
"v_cmp_le_u32_e32 vcc, v217, v215\n" \
"v_add_u32_e32 v24, v222, v224\n" \
"v_mfma_f32_16x16x32_bf16 a[200:203], v[132:135], v[100:103], a[200:203]\n" \
"v_lshlrev_b32_e32 v227, 4, v226\n" \
"s_add_u32 s43, s89, s88\n" \
"v_mfma_f32_16x16x32_bf16 a[232:235], v[132:135], v[108:111], a[232:235]\n" \
"ds_read_b64_tr_b16 v[132:133], v34 offset:32768\n" \
"ds_read_b64_tr_b16 v[134:135], v34 offset:33792\n" \
"v_mfma_f32_16x16x32_bf16 a[232:235], v[136:139], v[112:115], a[232:235]\n" \
"ds_read_b64_tr_b16 v[136:137], v34 offset:40960\n" \
"ds_read_b64_tr_b16 v[138:139], v34 offset:41984\n" \
"v_mfma_f32_16x16x32_bf16 a[140:143], v[144:147], v[88:91], a[140:143]\n" \
"ds_read_b128 v[88:91], v29 offset:32768\n" \
"s_add_u32 s96, s42, s97\n" \
"v_mfma_f32_16x16x32_bf16 a[140:143], v[140:143], v[84:87], a[140:143]\n" \
"ds_read_b128 v[84:87], v28 offset:32768\n" \
"v_cndmask_b32_e32 v40, v220, v221, vcc\n" \
"v_mfma_f32_16x16x32_bf16 a[172:175], v[140:143], v[92:95], a[172:175]\n" \
"ds_read_b128 v[92:95], v28 offset:34816\n" \
"v_add_u32_e64 v25, v222, v227\n" \
"v_mfma_f32_16x16x32_bf16 a[172:175], v[144:147], v[96:99], a[172:175]\n" \
"ds_read_b128 v[96:99], v29 offset:34816\n" \
"s_add_u32 s44, 0x4000, s96\n" \
"v_mfma_f32_16x16x32_bf16 a[204:207], v[144:147], v[104:107], a[204:207]\n" \
"ds_read_b128 v[104:107], v29 offset:36864\n" \
"s_add_u32 s98, s43, s97\n" \
"v_mfma_f32_16x16x32_bf16 a[204:207], v[140:143], v[100:103], a[204:207]\n" \
"ds_read_b128 v[100:103], v28 offset:36864\n" \
"s_lshr_b32 s94, %[w], 1\n" \
"v_mfma_f32_16x16x32_bf16 a[236:239], v[140:143], v[108:111], a[236:239]\n" \
"ds_read_b64_tr_b16 v[140:141], v35 offset:32768\n" \
"ds_read_b64_tr_b16 v[142:143], v35 offset:33792\n" \
"v_mfma_f32_16x16x32_bf16 a[236:239], v[144:147], v[112:115], a[236:239]\n" \
"s_add_u32 m0, %[mA], 0x1800\n" \
"s_waitcnt lgkmcnt(12)\n" \
"s_nop 0\n" \
"v_mfma_f32_16x16x32_bf16 a[16:19], v[56:59], v[152:155], a[16:19]\n" \
"s_add_u32 s85, s62, 0xc000\n" \
"ds_read_b64_tr_b16 v[144:145], v35 offset:40960\n" \
"v_mfma_f32_16x16x32_bf16 a[16:19], v[52:55], v[148:151], a[16:19]\n" \
"buffer_load_dwordx4 v22, s[64:67], s85 offen lds\n" \
"v_mfma_f32_16x16x32_bf16 a[48:51], v[60:63], v[148:151], a[48:51]\n" \
"s_add_u32 m0, %[mA], 0x1c00\n" \
"s_add_u32 s84, s62, 0xe000\n" \
"v_mfma_f32_16x16x32_bf16 a[48:51], v[64:67], v[152:155], a[48:51]\n" \
"ds_read_b64_tr_b16 v[146:147], v35 offset:41984\n" \
"ds_read_b128 v[108:111], v28 offset:38912\n" \
"v_mfma_f32_16x16x32_bf16 a[80:83], v[72:75], v[152:155], a[80:83]\n" \
"buffer_load_dwordx4 v23, s[64:67], s84 offen lds\n" \
"v_mfma_f32_16x16x32_bf16 a[80:83], v[68:71], v[148:151], a[80:83]\n" \
"s_add_u32 m0, %[mB], 0xc00\n" \
"s_add_u32 s85, s63, 0x18000\n" \
"v_mfma_f32_16x16x32_bf16 a[112:115], v[76:79], v[148:151], a[112:115]\n" \
"ds_read_b128 v[112:115], v29 offset:38912\n" \
"s_and_b32 s95, 1, %[w]\n" \
"v_mfma_f32_16x16x32_bf16 a[112:115], v[80:83], v[152:155], a[112:115]\n" \
"buffer_load_dwordx4 v27, s[68:71], s85 offen lds\n" \
"v_mfma_f32_16x16x32_bf16 a[20:23], v[56:59], v[160:163], a[20:23]\n" \
"s_add_u32 m0, %[mB], 0x4c00\n" \
"s_add_u32 s84, s63, 0x18000\n" \
"v_mfma_f32_16x16x32_bf16 a[20:23], v[52:55], v[156:159], a[20:23]\n" \
"s_waitcnt vmcnt(15)\n" \
"v_or_b32_e32 v206, v210, v206\n" \
"v_mfma_f32_16x16x32_bf16 a[52:55], v[60:63], v[156:159], a[52:55]\n" \
"buffer_load_dwordx4 v27, s[72:75], s84 offen lds\n" \
"v_mfma_f32_16x16x32_bf16 a[52:55], v[64:67], v[160:163], a[52:55]\n" \
"s_mov_b32 m0, %[mS]\n" \
"v_mov_b32_e32 v223, 0x3020c0c\n" \
"v_mfma_f32_16x16x32_bf16 a[84:87], v[72:75], v[160:163], a[84:87]\n" \
"v_mov_b32_e32 v224, 0xc0c0302\n" \
"v_cmp_ne_u32_e32 vcc, 0, v216\n" \
"v_mfma_f32_16x16x32_bf16 a[84:87], v[68:71], v[156:159], a[84:87]\n" \
"s_add_u32 s45, 0x4000, s98\n" \
"s_add_u32 s96, s94, s95\n" \
"v_mfma_f32_16x16x32_bf16 a[116:119], v[76:79], v[156:159], a[116:119]\n" \
"s_add_u32 s97, 1, s94\n" \
"v_add_u32_e32 v222, 2, v217\n" \
"v_mfma_f32_16x16x32_bf16 a[116:119], v[80:83], v[160:163], a[116:119]\n" \
"v_cndmask_b32_e32 v225, v224, v223, vcc\n" \
"s_lshl_b32 s59, s96, 13\n" \
"v_mfma_f32_16x16x32_bf16 a[24:27], v[56:59], v[168:171], a[24:27]\n" \
"s_sub_u32 s98, s97, s95\n" \
"s_lshl_b32 s99, s94, 13\n" \
"v_mfma_f32_16x16x32_bf16 a[24:27], v[52:55], v[164:167], a[24:27]\n" \
"s_mul_i32 s101, 0x1800, s94\n" \
"v_mov_b32_e32 v226, 0xc0c0c0c\n" \
"v_mfma_f32_16x16x32_bf16 a[56:59], v[60:63], v[164:167], a[56:59]\n" \
"v_cmp_le_u32_e32 vcc, v222, v215\n" \
"s_lshl_b32 s60, s98, 13\n" \
"v_mfma_f32_16x16x32_bf16 a[56:59], v[64:67], v[168:171], a[56:59]\n" \
"v_or_b32_e32 v204, v210, v204\n" \
"v_mov_b32_e32 v210, 0\n" \
"v_mfma_f32_16x16x32_bf16 a[88:91], v[72:75], v[168:171], a[88:91]\n" \
"s_mov_b32 s86, s10\n" \
"s_mov_b32 s87, s11\n" \
"v_mfma_f32_16x16x32_bf16 a[88:91], v[68:71], v[164:167], a[88:91]\n" \
"ds_write_b128 v196, v[200:203]\n" \
"s_mov_b32 exec_lo, 0x15\n" \
"s_mov_b32 exec_hi, 0x180000\n" \
"buffer_load_dword v11, s[80:83], 0 offen sc1 lds\n" \
"s_mov_b64 exec, -1\n" \
"v_mfma_f32_16x16x32_bf16 a[120:123], v[76:79], v[164:167], a[120:123]\n" \
"s_mov_b32 m0, %[mS]\n" \
"s_add_u32 s100, 0x1800, s99\n" \
"v_mfma_f32_16x16x32_bf16 a[120:123], v[80:83], v[168:171], a[120:123]\n" \
"ds_write_b128 v197, v[204:207]\n" \
"v_mfma_f32_16x16x32_bf16 a[28:31], v[56:59], v[176:179], a[28:31]\n" \
"ds_write_b128 v198, v[208:211]\n" \
"v_mfma_f32_16x16x32_bf16 a[28:31], v[52:55], v[172:175], a[28:31]\n" \
"s_sub_u32 s88, 0x17000, s101\n" \
"s_lshl_b32 s96, s94, 15\n" \
"v_mfma_f32_16x16x32_bf16 a[60:63], v[60:63], v[172:175], a[60:63]\n" \
"v_cndmask_b32_e32 v41, v225, v226, vcc\n" \
"v_and_b32_e32 v230, 3, v229\n" \
"s_mov_b32 exec_lo, 0x3ffff\n" \
"s_mov_b32 exec_hi, 0x0\n" \
"global_load_lds_dword v188, s[86:87]\n" \
"s_mov_b64 exec, -1\n" \
"v_mfma_f32_16x16x32_bf16 a[60:63], v[64:67], v[176:179], a[60:63]\n" \
"s_cmp_lg_u32 s95, 0\n" \
"s_cselect_b32 s61, s100, s88\n" \
"v_mfma_f32_16x16x32_bf16 a[92:95], v[72:75], v[176:179], a[92:95]\n" \
"s_add_u32 s97, 0x10000, s96\n" \
"s_lshl_b32 s98, s95, 11\n" \
"v_mfma_f32_16x16x32_bf16 a[92:95], v[68:71], v[172:175], a[92:95]\n" \
"v_mov_b32_e32 v227, 0xc0c0c0c\n" \
"v_cmp_le_u32_e32 vcc, v217, v215\n" \
"v_mfma_f32_16x16x32_bf16 a[124:127], v[76:79], v[172:175], a[124:127]\n" \
"v_add_u32_e32 v231, v228, v230\n" \
"s_add_u32 s101, s97, s98\n" \
"v_mfma_f32_16x16x32_bf16 a[124:127], v[80:83], v[176:179], a[124:127]\n" \
"v_cndmask_b32_e32 v42, v227, v220, vcc\n" \
"v_lshlrev_b32_e32 v232, 7, v231\n" \
"v_mfma_f32_16x16x32_bf16 a[0:3], v[120:123], v[56:59], a[0:3]\n" \
"v_lshrrev_b32_e32 v237, 1, v230\n" \
"s_add_u32 s100, s101, s99\n" \
"v_mfma_f32_16x16x32_bf16 a[0:3], v[116:119], v[52:55], a[0:3]\n" \
"v_mov_b32_e32 v228, 0xc0c0c0c\n" \
"v_cmp_le_u32_e32 vcc, v222, v215\n" \
"v_mfma_f32_16x16x32_bf16 a[32:35], v[116:119], v[60:63], a[32:35]\n" \
"v_add_u32_e32 v233, 0x14000, v232\n" \
"v_lshlrev_b32_e32 v235, 3, v234\n" \
"v_mfma_f32_16x16x32_bf16 a[32:35], v[120:123], v[64:67], a[32:35]\n" \
"s_add_u32 s88, 0x4000, s100\n" \
"v_cndmask_b32_e32 v43, v228, v225, vcc\n" \
"v_mfma_f32_16x16x32_bf16 a[64:67], v[120:123], v[72:75], a[64:67]\n" \
"v_and_b32_e32 v229, 50, v212\n" \
"v_and_b32_e64 v230, 16, v20\n" \
"v_mfma_f32_16x16x32_bf16 a[64:67], v[116:119], v[68:71], a[64:67]\n" \
"v_mov_b32_e32 v231, 0x3f80\n" \
"v_add_u32_e64 v236, v233, v235\n" \
"v_mfma_f32_16x16x32_bf16 a[96:99], v[116:119], v[76:79], a[96:99]\n" \
"v_lshlrev_b32_e32 v239, 1, v238\n" \
"v_lshlrev_b32_e32 v232, v230, v231\n" \
"v_mfma_f32_16x16x32_bf16 a[96:99], v[120:123], v[80:83], a[96:99]\n" \
"s_cmp_lg_u32 s95, 0\n" \
"s_cselect_b32 s47, s88, s101\n" \
"v_mfma_f32_16x16x32_bf16 a[4:7], v[128:131], v[56:59], a[4:7]\n" \
"v_cmp_eq_u32_e32 vcc, 16, v229\n" \
"v_or_b32_e32 v240, v237, v239\n" \
"v_mfma_f32_16x16x32_bf16 a[4:7], v[124:127], v[52:55], a[4:7]\n" \
"v_cndmask_b32_e32 v46, 0, v232, vcc\n" \
"v_and_b32_e32 v233, 51, v212\n" \
"v_mfma_f32_16x16x32_bf16 a[36:39], v[124:127], v[60:63], a[36:39]\n" \
"v_lshlrev_b32_e32 v241, 5, v240\n" \
"v_mov_b32_e32 v234, 0x3f80\n" \
"v_mfma_f32_16x16x32_bf16 a[36:39], v[128:131], v[64:67], a[36:39]\n" \
"v_cmp_eq_u32_e32 vcc, 2, v233\n" \
"s_lshl_b32 s88, s95, 4\n" \
"v_mfma_f32_16x16x32_bf16 a[68:71], v[128:131], v[72:75], a[68:71]\n" \
"v_add_u32_e32 v190, v236, v241\n" \
"v_cndmask_b32_e32 v47, 0, v234, vcc\n" \
"v_mfma_f32_16x16x32_bf16 a[68:71], v[124:127], v[68:71], a[68:71]\n" \
"v_add_u32_e32 v235, s88, v215\n" \
"v_mov_b32_e32 v236, 0xffff\n" \
"v_mfma_f32_16x16x32_bf16 a[100:103], v[124:127], v[76:79], a[100:103]\n" \
"v_cmp_le_u32_e32 vcc, v214, v235\n" \
"v_mov_b32_e32 v238, 0xffff0000\n" \
"v_mfma_f32_16x16x32_bf16 a[100:103], v[128:131], v[80:83], a[100:103]\n" \
"v_cndmask_b32_e32 v237, 0, v236, vcc\n" \
"s_waitcnt lgkmcnt(0)\n" \
"v_mfma_f32_16x16x32_bf16 a[8:11], v[136:139], v[56:59], a[8:11]\n" \
"v_cmp_lt_u32_e32 vcc, v214, v235\n" \
"v_add_u32_e64 v240, 2, v214\n" \
"v_mfma_f32_16x16x32_bf16 a[8:11], v[132:135], v[52:55], a[8:11]\n" \
"v_cndmask_b32_e32 v239, 0, v238, vcc\n" \
"v_mov_b32_e32 v241, 0xffff\n" \
"v_mfma_f32_16x16x32_bf16 a[40:43], v[132:135], v[60:63], a[40:43]\n" \
"v_cmp_le_u32_e32 vcc, v240, v235\n" \
"v_mov_b32_e32 v243, 0xffff0000\n" \
"v_mfma_f32_16x16x32_bf16 a[40:43], v[136:139], v[64:67], a[40:43]\n" \
"v_cndmask_b32_e32 v242, 0, v241, vcc\n" \
"v_cmp_lt_u32_e32 vcc, v240, v235\n" \
"v_mfma_f32_16x16x32_bf16 a[72:75], v[136:139], v[72:75], a[72:75]\n" \
"v_add_u32_e32 v245, 16, v214\n" \
"v_cndmask_b32_e32 v244, 0, v243, vcc\n" \
"v_mfma_f32_16x16x32_bf16 a[72:75], v[132:135], v[68:71], a[72:75]\n" \
"v_mov_b32_e32 v246, 0xffff\n" \
"v_cmp_le_u32_e32 vcc, v245, v235\n" \
"v_mfma_f32_16x16x32_bf16 a[104:107], v[132:135], v[76:79], a[104:107]\n" \
"v_mov_b32_e32 v248, 0xffff0000\n" \
"v_cndmask_b32_e32 v247, 0, v246, vcc\n" \
"v_mfma_f32_16x16x32_bf16 a[104:107], v[136:139], v[80:83], a[104:107]\n" \
"v_cmp_lt_u32_e32 vcc, v245, v235\n" \
"v_add_u32_e32 v250, 18, v214\n" \
"v_mfma_f32_16x16x32_bf16 a[12:15], v[144:147], v[56:59], a[12:15]\n" \
"s_add_u32 s62, s22, 0x280\n" \
"s_and_b32 s62, s62, 0x3ff\n" \
"v_mfma_f32_16x16x32_bf16 a[12:15], v[140:143], v[52:55], a[12:15]\n" \
"s_lshl_b32 s63, s62, 12\n" \
"v_cndmask_b32_e32 v249, 0, v248, vcc\n" \
"v_mfma_f32_16x16x32_bf16 a[44:47], v[140:143], v[60:63], a[44:47]\n" \
"s_lshl_b32 s89, %[w], 8\n" \
"v_mov_b32_e32 v251, 0xffff\n" \
"v_mfma_f32_16x16x32_bf16 a[44:47], v[144:147], v[64:67], a[44:47]\n" \
"v_cmp_le_u32_e32 vcc, v250, v235\n" \
"s_add_u32 s46, 0x27000, s89\n" \
"v_mfma_f32_16x16x32_bf16 a[76:79], v[144:147], v[72:75], a[76:79]\n" \
"v_cndmask_b32_e32 v213, 0, v251, vcc\n" \
"s_add_u32 s89, 0xffffc000, s101\n" \
"v_mfma_f32_16x16x32_bf16 a[76:79], v[140:143], v[68:71], a[76:79]\n" \
"v_mov_b32_e32 v218, 0xffff0000\n" \
"v_cmp_lt_u32_e32 vcc, v250, v235\n" \
"v_mfma_f32_16x16x32_bf16 a[108:111], v[140:143], v[76:79], a[108:111]\n" \
"s_cmp_lg_u32 s95, 0\n" \
"s_cselect_b32 s48, s89, s100\n" \
"v_mfma_f32_16x16x32_bf16 a[108:111], v[144:147], v[80:83], a[108:111]\n" \
"v_cndmask_b32_e32 v219, 0, v218, vcc\n" \
"v_lshlrev_b32_e32 v216, 3, v212\n" \
"s_waitcnt vmcnt(2)\n" \
"s_barrier\n" \
"v_mfma_f32_16x16x32_bf16 a[128:131], v[120:123], v[88:91], a[128:131]\n" \
"s_add_u32 m0, %[mA], 0x8000\n" \
"s_add_u32 s85, s62, 0x0\n" \
"s_nop 0\n" \
"v_mfma_f32_16x16x32_bf16 a[128:131], v[116:119], v[84:87], a[128:131]\n" \
"buffer_load_dwordx4 v22, s[64:67], s85 offen lds\n" \
"v_mfma_f32_16x16x32_bf16 a[160:163], v[116:119], v[92:95], a[160:163]\n" \
"ds_read_b128 v[56:59], v29 offset:16384\n" \
"ds_read_b128 v[52:55], v28 offset:16384\n" \
"v_mfma_f32_16x16x32_bf16 a[160:163], v[120:123], v[96:99], a[160:163]\n" \
"s_add_u32 m0, %[mA], 0x8400\n" \
"s_add_u32 s84, s62, 0x2000\n" \
"v_mfma_f32_16x16x32_bf16 a[192:195], v[120:123], v[104:107], a[192:195]\n" \
"buffer_load_dwordx4 v23, s[64:67], s84 offen lds\n" \
"v_mfma_f32_16x16x32_bf16 a[192:195], v[116:119], v[100:103], a[192:195]\n" \
"ds_read_b128 v[60:63], v28 offset:18432\n" \
"ds_read_b128 v[64:67], v29 offset:18432\n" \
"v_mfma_f32_16x16x32_bf16 a[224:227], v[116:119], v[108:111], a[224:227]\n" \
"ds_read_b64_tr_b16 v[116:117], v32 offset:0\n" \
"ds_read_b64_tr_b16 v[118:119], v32 offset:1024\n" \
"v_mfma_f32_16x16x32_bf16 a[224:227], v[120:123], v[112:115], a[224:227]\n" \
"s_add_u32 m0, %[mB], 0x8000\n" \
"s_add_u32 s85, s63, 0x0\n" \
"v_mfma_f32_16x16x32_bf16 a[132:135], v[128:131], v[88:91], a[132:135]\n" \
"buffer_load_dwordx4 v26, s[68:71], s85 offen lds\n" \
"v_mfma_f32_16x16x32_bf16 a[132:135], v[124:127], v[84:87], a[132:135]\n" \
"ds_read_b64_tr_b16 v[120:121], v32 offset:8192\n" \
"ds_read_b64_tr_b16 v[122:123], v32 offset:9216\n" \
"v_mfma_f32_16x16x32_bf16 a[164:167], v[124:127], v[92:95], a[164:167]\n" \
"ds_read_b128 v[72:75], v29 offset:20480\n" \
"ds_read_b128 v[68:71], v28 offset:20480\n" \
"v_mfma_f32_16x16x32_bf16 a[164:167], v[128:131], v[96:99], a[164:167]\n" \
"s_add_u32 m0, %[mB], 0xc000\n" \
"s_add_u32 s84, s63, 0x0\n" \
"v_mfma_f32_16x16x32_bf16 a[196:199], v[128:131], v[104:107], a[196:199]\n" \
"buffer_load_dwordx4 v26, s[72:75], s84 offen lds\n" \
"v_mfma_f32_16x16x32_bf16 a[196:199], v[124:127], v[100:103], a[196:199]\n" \
"ds_read_b128 v[76:79], v28 offset:22528\n" \
"ds_read_b128 v[80:83], v29 offset:22528\n" \
"v_mfma_f32_16x16x32_bf16 a[228:231], v[124:127], v[108:111], a[228:231]\n" \
"ds_read_b64_tr_b16 v[124:125], v33 offset:0\n" \
"ds_read_b64_tr_b16 v[126:127], v33 offset:1024\n" \
"v_mfma_f32_16x16x32_bf16 a[228:231], v[128:131], v[112:115], a[228:231]\n" \
"s_add_u32 m0, %[mA], 0x8800\n" \
"s_add_u32 s85, s62, 0x4000\n" \
"v_mfma_f32_16x16x32_bf16 a[136:139], v[136:139], v[88:91], a[136:139]\n" \
"buffer_load_dwordx4 v22, s[64:67], s85 offen lds\n" \
"v_mfma_f32_16x16x32_bf16 a[136:139], v[132:135], v[84:87], a[136:139]\n" \
"ds_read_b64_tr_b16 v[128:129], v33 offset:8192\n" \
"ds_read_b64_tr_b16 v[130:131], v33 offset:9216\n" \
"v_mfma_f32_16x16x32_bf16 a[168:171], v[132:135], v[92:95], a[168:171]\n" \
"ds_read_b128 v[200:203], v199 offset:0\n" \
"ds_read_b128 v[204:207], v199 offset:4096\n" \
"v_mfma_f32_16x16x32_bf16 a[168:171], v[136:139], v[96:99], a[168:171]\n" \
"s_add_u32 m0, %[mA], 0x8c00\n" \
"s_add_u32 s84, s62, 0x6000\n" \
"v_mfma_f32_16x16x32_bf16 a[200:203], v[136:139], v[104:107], a[200:203]\n" \
"buffer_load_dwordx4 v23, s[64:67], s84 offen lds\n" \
"v_mfma_f32_16x16x32_bf16 a[200:203], v[132:135], v[100:103], a[200:203]\n" \
"ds_read_b128 v[208:211], v199 offset:8192\n" \
"s_lshr_b32 s94, %[b], 5\n" \
"s_nop 0\n" \
"v_mfma_f32_16x16x32_bf16 a[232:235], v[132:135], v[108:111], a[232:235]\n" \
"ds_read_b64_tr_b16 v[132:133], v34 offset:0\n" \
"ds_read_b64_tr_b16 v[134:135], v34 offset:1024\n" \
"v_mfma_f32_16x16x32_bf16 a[232:235], v[136:139], v[112:115], a[232:235]\n" \
"s_add_u32 m0, %[mB], 0x8400\n" \
"s_add_u32 s85, s63, 0x8000\n" \
"v_mfma_f32_16x16x32_bf16 a[140:143], v[144:147], v[88:91], a[140:143]\n" \
"buffer_load_dwordx4 v26, s[68:71], s85 offen lds\n" \
"v_mfma_f32_16x16x32_bf16 a[140:143], v[140:143], v[84:87], a[140:143]\n" \
"ds_read_b64_tr_b16 v[136:137], v34 offset:8192\n" \
"ds_read_b64_tr_b16 v[138:139], v34 offset:9216\n" \
"v_mfma_f32_16x16x32_bf16 a[172:175], v[140:143], v[92:95], a[172:175]\n" \
"s_mov_b32 s96, 0xc000\n" \
"s_mov_b32 s97, 0x8000\n" \
"v_mfma_f32_16x16x32_bf16 a[172:175], v[144:147], v[96:99], a[172:175]\n" \
"s_add_u32 m0, %[mB], 0xc400\n" \
"s_add_u32 s84, s63, 0x8000\n" \
"v_mfma_f32_16x16x32_bf16 a[204:207], v[144:147], v[104:107], a[204:207]\n" \
"buffer_load_dwordx4 v26, s[72:75], s84 offen lds\n" \
"v_mfma_f32_16x16x32_bf16 a[204:207], v[140:143], v[100:103], a[204:207]\n" \
"v_or_b32_e32 v15, v213, v219\n" \
"s_lshl_b32 s101, %[w], 9\n" \
"v_mfma_f32_16x16x32_bf16 a[236:239], v[140:143], v[108:111], a[236:239]\n" \
"ds_read_b64_tr_b16 v[140:141], v35 offset:0\n" \
"ds_read_b64_tr_b16 v[142:143], v35 offset:1024\n" \
"v_mfma_f32_16x16x32_bf16 a[236:239], v[144:147], v[112:115], a[236:239]\n" \
"s_add_u32 m0, %[mA], 0x9000\n" \
"s_add_u32 s85, s62, 0x8000\n" \
"v_mfma_f32_16x16x32_bf16 a[144:147], v[88:91], v[152:155], a[144:147]\n" \
"buffer_load_dwordx4 v22, s[64:67], s85 offen lds\n" \
"v_mfma_f32_16x16x32_bf16 a[144:147], v[84:87], v[148:151], a[144:147]\n" \
"ds_read_b64_tr_b16 v[144:145], v35 offset:8192\n" \
"ds_read_b64_tr_b16 v[146:147], v35 offset:9216\n" \
"v_mfma_f32_16x16x32_bf16 a[176:179], v[92:95], v[148:151], a[176:179]\n" \
"v_lshrrev_b32_e32 v212, 4, v20\n" \
"s_add_u32 s89, 0x27800, s101\n" \
"v_mfma_f32_16x16x32_bf16 a[176:179], v[96:99], v[152:155], a[176:179]\n" \
"s_add_u32 m0, %[mA], 0x9400\n" \
"s_add_u32 s84, s62, 0xa000\n" \
"v_mfma_f32_16x16x32_bf16 a[208:211], v[104:107], v[152:155], a[208:211]\n" \
"buffer_load_dwordx4 v23, s[64:67], s84 offen lds\n" \
"v_mfma_f32_16x16x32_bf16 a[208:211], v[100:103], v[148:151], a[208:211]\n" \
"s_cmp_lg_u32 s94, 0\n" \
"s_cselect_b32 s53, s96, 0x8000\n" \
"v_mfma_f32_16x16x32_bf16 a[240:243], v[108:111], v[148:151], a[240:243]\n" \
"ds_read_b64_tr_b16 v[148:149], v32 offset:16384\n" \
"ds_read_b64_tr_b16 v[150:151], v32 offset:17408\n" \
"v_mfma_f32_16x16x32_bf16 a[240:243], v[112:115], v[152:155], a[240:243]\n" \
"s_add_u32 m0, %[mB], 0x8800\n" \
"s_add_u32 s85, s63, 0x10000\n" \
"v_mfma_f32_16x16x32_bf16 a[148:151], v[88:91], v[160:163], a[148:151]\n" \
"buffer_load_dwordx4 v27, s[68:71], s85 offen lds\n" \
"v_mfma_f32_16x16x32_bf16 a[148:151], v[84:87], v[156:159], a[148:151]\n" \
"ds_read_b64_tr_b16 v[152:153], v32 offset:24576\n" \
"ds_read_b64_tr_b16 v[154:155], v32 offset:25600\n" \
"v_mfma_f32_16x16x32_bf16 a[180:183], v[92:95], v[156:159], a[180:183]\n" \
"s_cmp_lg_u32 s94, 0\n" \
"s_cselect_b32 s98, s97, 0x4000\n" \
"v_mfma_f32_16x16x32_bf16 a[180:183], v[96:99], v[160:163], a[180:183]\n" \
"s_add_u32 m0, %[mB], 0xc800\n" \
"s_add_u32 s84, s63, 0x10000\n" \
"v_mfma_f32_16x16x32_bf16 a[212:215], v[104:107], v[160:163], a[212:215]\n" \
"buffer_load_dwordx4 v27, s[72:75], s84 offen lds\n" \
"v_mfma_f32_16x16x32_bf16 a[212:215], v[100:103], v[156:159], a[212:215]\n" \
"v_mul_u32_u24_e32 v213, 0xe39, v212\n" \
"v_add_u32_e32 v19, s89, v216\n" \
"v_mfma_f32_16x16x32_bf16 a[244:247], v[108:111], v[156:159], a[244:247]\n" \
"ds_read_b64_tr_b16 v[156:157], v33 offset:16384\n" \
"ds_read_b64_tr_b16 v[158:159], v33 offset:17408\n" \
"v_mfma_f32_16x16x32_bf16 a[244:247], v[112:115], v[160:163], a[244:247]\n" \
"ds_read_b64_tr_b16 v[160:161], v33 offset:24576\n" \
"ds_read_b64_tr_b16 v[162:163], v33 offset:25600\n" \
"v_mfma_f32_16x16x32_bf16 a[152:155], v[88:91], v[168:171], a[152:155]\n" \
"v_lshrrev_b32_e32 v214, 16, v213\n" \
"v_add_u32_e32 v222, 64, v212\n" \
"v_mfma_f32_16x16x32_bf16 a[152:155], v[84:87], v[164:167], a[152:155]\n" \
"v_mul_u32_u24_e32 v216, 18, v214\n" \
"s_and_b32 s94, 7, %[b]\n" \
"v_mfma_f32_16x16x32_bf16 a[184:187], v[92:95], v[164:167], a[184:187]\n" \
"v_mul_u32_u24_e32 v223, 0xe39, v222\n" \
"v_sub_u32_e32 v217, v212, v216\n" \
"v_mfma_f32_16x16x32_bf16 a[184:187], v[96:99], v[168:171], a[184:187]\n" \
"s_lshl_b32 s95, s94, 5\n" \
"v_lshrrev_b32_e32 v224, 16, v223\n" \
"v_mfma_f32_16x16x32_bf16 a[216:219], v[104:107], v[168:171], a[216:219]\n" \
"v_add_u32_e32 v233, 0x80, v212\n" \
"v_or_b32_e32 v13, v242, v244\n" \
"v_mfma_f32_16x16x32_bf16 a[216:219], v[100:103], v[164:167], a[216:219]\n" \
"v_lshlrev_b32_e32 v221, 2, v235\n" \
"s_add_u32 s96, 32, s95\n" \
"v_mfma_f32_16x16x32_bf16 a[248:251], v[108:111], v[164:167], a[248:251]\n" \
"ds_read_b64_tr_b16 v[164:165], v34 offset:16384\n" \
"ds_read_b64_tr_b16 v[166:167], v34 offset:17408\n" \
"v_mfma_f32_16x16x32_bf16 a[248:251], v[112:115], v[168:171], a[248:251]\n" \
"ds_read_b64_tr_b16 v[168:169], v34 offset:24576\n" \
"ds_read_b64_tr_b16 v[170:171], v34 offset:25600\n" \
"v_mfma_f32_16x16x32_bf16 a[156:159], v[88:91], v[176:179], a[156:159]\n" \
"ds_read_b128 v[88:91], v29 offset:0\n" \
"v_lshlrev_b32_e32 v218, 4, v217\n" \
"v_mfma_f32_16x16x32_bf16 a[156:159], v[84:87], v[172:175], a[156:159]\n" \
"ds_read_b128 v[84:87], v28 offset:0\n" \
"v_mul_u32_u24_e32 v226, 18, v224\n" \
"v_mfma_f32_16x16x32_bf16 a[188:191], v[92:95], v[172:175], a[188:191]\n" \
"ds_read_b128 v[92:95], v28 offset:2048\n" \
"v_mul_u32_u24_e32 v234, 0xe39, v233\n" \
"v_mfma_f32_16x16x32_bf16 a[188:191], v[96:99], v[176:179], a[188:191]\n" \
"ds_read_b128 v[96:99], v29 offset:2048\n" \
"v_or_b32_e32 v12, v237, v239\n" \
"v_mfma_f32_16x16x32_bf16 a[220:223], v[104:107], v[176:179], a[220:223]\n" \
"ds_read_b128 v[104:107], v29 offset:4096\n" \
"v_add_u32_e32 v219, s96, v218\n" \
"v_mfma_f32_16x16x32_bf16 a[220:223], v[100:103], v[172:175], a[220:223]\n" \
"ds_read_b128 v[100:103], v28 offset:4096\n" \
"v_sub_u32_e32 v227, v222, v226\n" \
"v_mfma_f32_16x16x32_bf16 a[252:255], v[108:111], v[172:175], a[252:255]\n" \
"ds_read_b64_tr_b16 v[172:173], v35 offset:16384\n" \
"ds_read_b64_tr_b16 v[174:175], v35 offset:17408\n" \
"v_mfma_f32_16x16x32_bf16 a[252:255], v[112:115], v[176:179], a[252:255]\n" \
"s_add_u32 m0, %[mA], 0x9800\n" \
"s_waitcnt lgkmcnt(12)\n" \
"v_mfma_f32_16x16x32_bf16 a[0:3], v[120:123], v[56:59], a[0:3]\n" \
"s_add_u32 s85, s62, 0xc000\n" \
"ds_read_b64_tr_b16 v[176:177], v35 offset:24576\n" \
"v_mfma_f32_16x16x32_bf16 a[0:3], v[116:119], v[52:55], a[0:3]\n" \
"buffer_load_dwordx4 v22, s[64:67], s85 offen lds\n" \
"v_mfma_f32_16x16x32_bf16 a[32:35], v[116:119], v[60:63], a[32:35]\n" \
"s_add_u32 m0, %[mA], 0x9c00\n" \
"s_add_u32 s84, s62, 0xe000\n" \
"v_mfma_f32_16x16x32_bf16 a[32:35], v[120:123], v[64:67], a[32:35]\n" \
"ds_read_b64_tr_b16 v[178:179], v35 offset:25600\n" \
"ds_read_b128 v[108:111], v28 offset:6144\n" \
"v_mfma_f32_16x16x32_bf16 a[64:67], v[120:123], v[72:75], a[64:67]\n" \
"buffer_load_dwordx4 v23, s[64:67], s84 offen lds\n" \
"v_mfma_f32_16x16x32_bf16 a[64:67], v[116:119], v[68:71], a[64:67]\n" \
"s_add_u32 m0, %[mB], 0x8c00\n" \
"s_add_u32 s85, s63, 0x18000\n" \
"v_mfma_f32_16x16x32_bf16 a[96:99], v[116:119], v[76:79], a[96:99]\n" \
"ds_read_b128 v[112:115], v29 offset:6144\n" \
"L_pf2:\n" \
"s_cbranch_execz L_pf6\n" \
"s_nop 0\n" \
"v_lshrrev_b32_e32 v235, 16, v234\n" \
"s_nop 0\n" \
"v_mfma_f32_16x16x32_bf16 a[96:99], v[120:123], v[80:83], a[96:99]\n" \
"buffer_load_dwordx4 v27, s[68:71], s85 offen lds\n" \
"v_mfma_f32_16x16x32_bf16 a[4:7], v[128:131], v[56:59], a[4:7]\n" \
"s_add_u32 m0, %[mB], 0xcc00\n" \
"s_add_u32 s84, s63, 0x18000\n" \
"v_mfma_f32_16x16x32_bf16 a[4:7], v[124:127], v[52:55], a[4:7]\n" \
"v_add_u32_e32 v244, 0xc0, v212\n" \
"v_add_u32_e32 v18, 0x27000, v221\n" \
"v_mfma_f32_16x16x32_bf16 a[36:39], v[124:127], v[60:63], a[36:39]\n" \
"buffer_load_dwordx4 v27, s[72:75], s84 offen lds\n" \
"v_mfma_f32_16x16x32_bf16 a[36:39], v[128:131], v[64:67], a[36:39]\n" \
"v_and_b32_e32 v220, 0xff, v219\n" \
"v_cmp_gt_u32_e32 vcc, 16, v217\n" \
"v_mfma_f32_16x16x32_bf16 a[68:71], v[128:131], v[72:75], a[68:71]\n" \
"v_lshlrev_b32_e32 v228, 4, v227\n" \
"v_mul_u32_u24_e32 v237, 18, v235\n" \
"v_mfma_f32_16x16x32_bf16 a[68:71], v[124:127], v[68:71], a[68:71]\n" \
"buffer_store_dwordx4 v[200:203], v20, s[80:83], s57 offen sc1\n" \
"v_mfma_f32_16x16x32_bf16 a[100:103], v[124:127], v[76:79], a[100:103]\n" \
"v_mul_u32_u24_e32 v245, 0xe39, v244\n" \
"v_cndmask_b32_e32 v221, v218, v220, vcc\n" \
"v_mfma_f32_16x16x32_bf16 a[100:103], v[128:131], v[80:83], a[100:103]\n" \
"v_add_u32_e32 v229, s96, v228\n" \
"v_sub_u32_e32 v238, v233, v237\n" \
"v_mfma_f32_16x16x32_bf16 a[8:11], v[136:139], v[56:59], a[8:11]\n" \
"v_lshrrev_b32_e32 v246, 16, v245\n" \
"v_add_u32_e32 v219, 0x100, v212\n" \
"v_mfma_f32_16x16x32_bf16 a[8:11], v[132:135], v[52:55], a[8:11]\n" \
"v_or_b32_e32 v14, v247, v249\n" \
"v_mul_u32_u24_e32 v215, 0x480, v214\n" \
"v_mfma_f32_16x16x32_bf16 a[40:43], v[132:135], v[60:63], a[40:43]\n" \
"buffer_store_dwordx4 v[204:207], v20, s[80:83], s56 offen sc1\n" \
"v_mfma_f32_16x16x32_bf16 a[40:43], v[136:139], v[64:67], a[40:43]\n" \
"v_and_b32_e32 v230, 0xff, v229\n" \
"v_cmp_gt_u32_e32 vcc, 16, v227\n" \
"v_mfma_f32_16x16x32_bf16 a[72:75], v[136:139], v[72:75], a[72:75]\n" \
"v_lshlrev_b32_e32 v239, 4, v238\n" \
"v_mul_u32_u24_e32 v248, 18, v246\n" \
"v_mfma_f32_16x16x32_bf16 a[72:75], v[132:135], v[68:71], a[72:75]\n" \
"v_mul_u32_u24_e32 v217, 0xe39, v219\n" \
"v_add_u32_e32 v0, v215, v221\n" \
"v_mfma_f32_16x16x32_bf16 a[104:107], v[132:135], v[76:79], a[104:107]\n" \
"v_cndmask_b32_e32 v231, v228, v230, vcc\n" \
"v_add_u32_e32 v240, s96, v239\n" \
"v_mfma_f32_16x16x32_bf16 a[104:107], v[136:139], v[80:83], a[104:107]\n" \
"v_sub_u32_e32 v249, v244, v248\n" \
"v_lshrrev_b32_e32 v220, 16, v217\n" \
"v_mfma_f32_16x16x32_bf16 a[12:15], v[144:147], v[56:59], a[12:15]\n" \
"buffer_store_dwordx4 v[208:211], v20, s[80:83], s58 offen sc1\n" \
"v_mfma_f32_16x16x32_bf16 a[12:15], v[140:143], v[52:55], a[12:15]\n" \
"v_add_u32_e32 v229, 0x140, v212\n" \
"v_mul_u32_u24_e32 v225, 0x480, v224\n" \
"v_mfma_f32_16x16x32_bf16 a[44:47], v[140:143], v[60:63], a[44:47]\n" \
"v_and_b32_e32 v241, 0xff, v240\n" \
"v_cmp_gt_u32_e32 vcc, 16, v238\n" \
"v_mfma_f32_16x16x32_bf16 a[44:47], v[144:147], v[64:67], a[44:47]\n" \
"v_lshlrev_b32_e32 v250, 4, v249\n" \
"v_mul_u32_u24_e32 v215, 18, v220\n" \
"v_mfma_f32_16x16x32_bf16 a[76:79], v[144:147], v[72:75], a[76:79]\n" \
"v_mul_u32_u24_e32 v227, 0xe39, v229\n" \
"v_add_u32_e32 v232, v225, v231\n" \
"v_mfma_f32_16x16x32_bf16 a[76:79], v[140:143], v[68:71], a[76:79]\n" \
"v_cndmask_b32_e32 v242, v239, v241, vcc\n" \
"v_add_u32_e32 v251, s96, v250\n" \
"v_mfma_f32_16x16x32_bf16 a[108:111], v[140:143], v[76:79], a[108:111]\n" \
"s_mov_b32 m0, %[mS]\n" \
"s_mov_b32 s86, s10\n" \
"v_mfma_f32_16x16x32_bf16 a[108:111], v[144:147], v[80:83], a[108:111]\n" \
"s_mov_b32 s87, s11\n" \
"v_sub_u32_e32 v221, v219, v215\n" \
"v_mfma_f32_16x16x32_bf16 a[16:19], v[56:59], v[152:155], a[16:19]\n" \
"v_lshrrev_b32_e32 v230, 16, v227\n" \
"v_add_u32_e32 v240, 0x180, v212\n" \
"v_mfma_f32_16x16x32_bf16 a[16:19], v[52:55], v[148:151], a[16:19]\n" \
"v_mul_u32_u24_e32 v236, 0x480, v235\n" \
"v_and_b32_e32 v213, 0xff, v251\n" \
"v_mfma_f32_16x16x32_bf16 a[48:51], v[60:63], v[148:151], a[48:51]\n" \
"v_cmp_gt_u32_e32 vcc, 16, v249\n" \
"v_lshlrev_b32_e32 v223, 4, v221\n" \
"v_mfma_f32_16x16x32_bf16 a[48:51], v[64:67], v[152:155], a[48:51]\n" \
"v_mul_u32_u24_e32 v225, 18, v230\n" \
"v_mul_u32_u24_e32 v238, 0xe39, v240\n" \
"v_mfma_f32_16x16x32_bf16 a[80:83], v[72:75], v[152:155], a[80:83]\n" \
"v_add_u32_e32 v1, 0xfffffc00, v232\n" \
"v_add_u32_e64 v243, v236, v242\n" \
"s_mov_b32 exec_lo, 0xfffc0000\n" \
"s_mov_b32 exec_hi, 0xf\n" \
"global_load_lds_dword v188, s[86:87]\n" \
"s_mov_b64 exec, -1\n" \
"v_mfma_f32_16x16x32_bf16 a[80:83], v[68:71], v[148:151], a[80:83]\n" \
"v_cndmask_b32_e32 v214, v250, v213, vcc\n" \
"v_add_u32_e32 v224, s96, v223\n" \
"v_mfma_f32_16x16x32_bf16 a[112:115], v[76:79], v[148:151], a[112:115]\n" \
"v_sub_u32_e32 v231, v229, v225\n" \
"v_lshrrev_b32_e32 v241, 16, v238\n" \
"v_mfma_f32_16x16x32_bf16 a[112:115], v[80:83], v[152:155], a[112:115]\n" \
"v_add_u32_e32 v251, 0x1c0, v212\n" \
"v_mul_u32_u24_e32 v247, 0x480, v246\n" \
"v_mfma_f32_16x16x32_bf16 a[20:23], v[56:59], v[160:163], a[20:23]\n" \
"v_and_b32_e32 v222, 0xff, v224\n" \
"v_cmp_gt_u32_e32 vcc, 16, v221\n" \
"v_mfma_f32_16x16x32_bf16 a[20:23], v[52:55], v[156:159], a[20:23]\n" \
"v_lshlrev_b32_e32 v232, 4, v231\n" \
"v_mul_u32_u24_e32 v236, 18, v241\n" \
"v_mfma_f32_16x16x32_bf16 a[52:55], v[60:63], v[156:159], a[52:55]\n" \
"v_mul_u32_u24_e32 v249, 0xe39, v251\n" \
"v_add_u32_e32 v2, 0xfffff800, v243\n" \
"v_mfma_f32_16x16x32_bf16 a[52:55], v[64:67], v[160:163], a[52:55]\n" \
"v_add_u32_e32 v216, v247, v214\n" \
"v_cndmask_b32_e32 v226, v223, v222, vcc\n" \
"v_mfma_f32_16x16x32_bf16 a[84:87], v[72:75], v[160:163], a[84:87]\n" \
"v_add_u32_e32 v234, s96, v232\n" \
"v_sub_u32_e32 v242, v240, v236\n" \
"v_mfma_f32_16x16x32_bf16 a[84:87], v[68:71], v[156:159], a[84:87]\n" \
"v_lshrrev_b32_e32 v213, 16, v249\n" \
"v_add_u32_e32 v224, 0x200, v212\n" \
"v_mfma_f32_16x16x32_bf16 a[116:119], v[76:79], v[156:159], a[116:119]\n" \
"v_mul_u32_u24_e32 v218, 0x480, v220\n" \
"v_and_b32_e32 v235, 0xff, v234\n" \
"v_mfma_f32_16x16x32_bf16 a[116:119], v[80:83], v[160:163], a[116:119]\n" \
"v_cmp_gt_u32_e32 vcc, 16, v231\n" \
"s_waitcnt lgkmcnt(0)\n" \
"v_mfma_f32_16x16x32_bf16 a[24:27], v[56:59], v[168:171], a[24:27]\n" \
"v_lshlrev_b32_e32 v243, 4, v242\n" \
"v_mul_u32_u24_e32 v247, 18, v213\n" \
"v_mfma_f32_16x16x32_bf16 a[24:27], v[52:55], v[164:167], a[24:27]\n" \
"v_mul_u32_u24_e32 v221, 0xe39, v224\n" \
"v_add_u32_e32 v3, 0xfffff400, v216\n" \
"v_mfma_f32_16x16x32_bf16 a[56:59], v[60:63], v[164:167], a[56:59]\n" \
"v_add_u32_e32 v4, v218, v226\n" \
"v_cndmask_b32_e32 v233, v232, v235, vcc\n" \
"v_mfma_f32_16x16x32_bf16 a[56:59], v[64:67], v[168:171], a[56:59]\n" \
"v_add_u32_e32 v245, s96, v243\n" \
"v_sub_u32_e32 v214, v251, v247\n" \
"v_mfma_f32_16x16x32_bf16 a[88:91], v[72:75], v[168:171], a[88:91]\n" \
"v_lshrrev_b32_e32 v222, 16, v221\n" \
"v_and_b32_e32 v246, 0xff, v245\n" \
"v_mfma_f32_16x16x32_bf16 a[88:91], v[68:71], v[164:167], a[88:91]\n" \
"v_cmp_gt_u32_e32 vcc, 16, v242\n" \
"v_lshlrev_b32_e32 v216, 4, v214\n" \
"v_mfma_f32_16x16x32_bf16 a[120:123], v[76:79], v[164:167], a[120:123]\n" \
"v_mul_u32_u24_e32 v218, 18, v222\n" \
"v_cndmask_b32_e32 v244, v243, v246, vcc\n" \
"v_mfma_f32_16x16x32_bf16 a[120:123], v[80:83], v[168:171], a[120:123]\n" \
"v_add_u32_e32 v217, s96, v216\n" \
"v_sub_u32_e32 v226, v224, v218\n" \
"v_mfma_f32_16x16x32_bf16 a[28:31], v[56:59], v[176:179], a[28:31]\n" \
"s_add_u32 s62, s22, 0x300\n" \
"s_and_b32 s62, s62, 0x3ff\n" \
"v_mfma_f32_16x16x32_bf16 a[28:31], v[52:55], v[172:175], a[28:31]\n" \
"s_lshl_b32 s63, s62, 12\n" \
"v_mul_u32_u24_e32 v228, 0x480, v230\n" \
"v_mfma_f32_16x16x32_bf16 a[60:63], v[60:63], v[172:175], a[60:63]\n" \
"v_and_b32_e32 v220, 0xff, v217\n" \
"v_cmp_gt_u32_e32 vcc, 16, v214\n" \
"v_mfma_f32_16x16x32_bf16 a[60:63], v[64:67], v[176:179], a[60:63]\n" \
"v_lshlrev_b32_e32 v227, 4, v226\n" \
"v_cndmask_b32_e32 v219, v216, v220, vcc\n" \
"v_mfma_f32_16x16x32_bf16 a[92:95], v[72:75], v[176:179], a[92:95]\n" \
"v_add_u32_e32 v230, s96, v227\n" \
"v_and_b32_e32 v229, 0xff, v230\n" \
"v_mfma_f32_16x16x32_bf16 a[92:95], v[68:71], v[172:175], a[92:95]\n" \
"v_cmp_gt_u32_e32 vcc, 16, v226\n" \
"v_mul_u32_u24_e32 v250, 0x480, v213\n" \
"v_mfma_f32_16x16x32_bf16 a[124:127], v[76:79], v[172:175], a[124:127]\n" \
"v_cndmask_b32_e32 v225, v227, v229, vcc\n" \
"v_and_b32_e64 v249, 2, v212\n" \
"v_mfma_f32_16x16x32_bf16 a[124:127], v[80:83], v[176:179], a[124:127]\n" \
"v_mov_b32_e32 v213, 0x100\n" \
"v_mov_b32_e32 v251, s95\n" \
"s_waitcnt vmcnt(4)\n" \
"s_barrier\n" \
"v_mfma_f32_16x16x32_bf16 a[144:147], v[88:91], v[152:155], a[144:147]\n" \
"s_add_u32 m0, %[mA], 0x0\n" \
"s_add_u32 s85, s62, 0x0\n" \
"s_nop 0\n" \
"v_mfma_f32_16x16x32_bf16 a[144:147], v[84:87], v[148:151], a[144:147]\n" \
"buffer_load_dwordx4 v22, s[64:67], s85 offen lds\n" \
"v_mfma_f32_16x16x32_bf16 a[176:179], v[92:95], v[148:151], a[176:179]\n" \
"ds_read_b128 v[56:59], v29 offset:49152\n" \
"ds_read_b128 v[52:55], v28 offset:49152\n" \
"v_mfma_f32_16x16x32_bf16 a[176:179], v[96:99], v[152:155], a[176:179]\n" \
"s_add_u32 m0, %[mA], 0x400\n" \
"s_add_u32 s84, s62, 0x2000\n" \
"v_mfma_f32_16x16x32_bf16 a[208:211], v[104:107], v[152:155], a[208:211]\n" \
"buffer_load_dwordx4 v23, s[64:67], s84 offen lds\n" \
"v_mfma_f32_16x16x32_bf16 a[208:211], v[100:103], v[148:151], a[208:211]\n" \
"ds_read_b128 v[60:63], v28 offset:51200\n" \
"ds_read_b128 v[64:67], v29 offset:51200\n" \
"v_mfma_f32_16x16x32_bf16 a[240:243], v[108:111], v[148:151], a[240:243]\n" \
"ds_read_b64_tr_b16 v[148:149], v32 offset:49152\n" \
"ds_read_b64_tr_b16 v[150:151], v32 offset:50176\n" \
"v_mfma_f32_16x16x32_bf16 a[240:243], v[112:115], v[152:155], a[240:243]\n" \
"s_add_u32 m0, %[mB], 0x0\n" \
"s_add_u32 s85, s63, 0x0\n" \
"v_mfma_f32_16x16x32_bf16 a[148:151], v[88:91], v[160:163], a[148:151]\n" \
"buffer_load_dwordx4 v26, s[68:71], s85 offen lds\n" \
"v_mfma_f32_16x16x32_bf16 a[148:151], v[84:87], v[156:159], a[148:151]\n" \
"ds_read_b64_tr_b16 v[152:153], v32 offset:57344\n" \
"ds_read_b64_tr_b16 v[154:155], v32 offset:58368\n" \
"v_mfma_f32_16x16x32_bf16 a[180:183], v[92:95], v[156:159], a[180:183]\n" \
"ds_read_b128 v[72:75], v29 offset:53248\n" \
"ds_read_b128 v[68:71], v28 offset:53248\n" \
"v_mfma_f32_16x16x32_bf16 a[180:183], v[96:99], v[160:163], a[180:183]\n" \
"s_add_u32 m0, %[mB], 0x4000\n" \
"s_add_u32 s84, s63, 0x0\n" \
"s_nop 0\n" \
"v_mfma_f32_16x16x32_bf16 a[212:215], v[104:107], v[160:163], a[212:215]\n" \
"buffer_load_dwordx4 v26, s[72:75], s84 offen lds\n" \
"v_mfma_f32_16x16x32_bf16 a[212:215], v[100:103], v[156:159], a[212:215]\n" \
"ds_read_b128 v[76:79], v28 offset:55296\n" \
"ds_read_b128 v[80:83], v29 offset:55296\n" \
"v_mfma_f32_16x16x32_bf16 a[244:247], v[108:111], v[156:159], a[244:247]\n" \
"ds_read_b64_tr_b16 v[156:157], v33 offset:49152\n" \
"ds_read_b64_tr_b16 v[158:159], v33 offset:50176\n" \
"v_mfma_f32_16x16x32_bf16 a[244:247], v[112:115], v[160:163], a[244:247]\n" \
"s_add_u32 m0, %[mA], 0x800\n" \
"s_add_u32 s85, s62, 0x4000\n" \
"v_mfma_f32_16x16x32_bf16 a[152:155], v[88:91], v[168:171], a[152:155]\n" \
"buffer_load_dwordx4 v22, s[64:67], s85 offen lds\n" \
"v_mfma_f32_16x16x32_bf16 a[152:155], v[84:87], v[164:167], a[152:155]\n" \
"ds_read_b64_tr_b16 v[160:161], v33 offset:57344\n" \
"ds_read_b64_tr_b16 v[162:163], v33 offset:58368\n" \
"v_mfma_f32_16x16x32_bf16 a[184:187], v[92:95], v[164:167], a[184:187]\n" \
"v_cmp_ne_u32_e32 vcc, 0, v249\n" \
"s_mov_b32 s76, s8\n" \
"v_mfma_f32_16x16x32_bf16 a[184:187], v[96:99], v[168:171], a[184:187]\n" \
"s_add_u32 m0, %[mA], 0xc00\n" \
"s_add_u32 s84, s62, 0x6000\n" \
"v_mfma_f32_16x16x32_bf16 a[216:219], v[104:107], v[168:171], a[216:219]\n" \
"buffer_load_dwordx4 v23, s[64:67], s84 offen lds\n" \
"v_mfma_f32_16x16x32_bf16 a[216:219], v[100:103], v[164:167], a[216:219]\n" \
"v_cndmask_b32_e32 v247, v251, v213, vcc\n" \
"s_nop 0\n" \
"s_and_b32 s77, s9, 0xffff\n" \
"v_mfma_f32_16x16x32_bf16 a[248:251], v[108:111], v[164:167], a[248:251]\n" \
"ds_read_b64_tr_b16 v[164:165], v34 offset:49152\n" \
"ds_read_b64_tr_b16 v[166:167], v34 offset:50176\n" \
"v_mfma_f32_16x16x32_bf16 a[248:251], v[112:115], v[168:171], a[248:251]\n" \
"s_add_u32 m0, %[mB], 0x400\n" \
"s_add_u32 s85, s63, 0x8000\n" \
"v_mfma_f32_16x16x32_bf16 a[156:159], v[88:91], v[176:179], a[156:159]\n" \
"buffer_load_dwordx4 v26, s[68:71], s85 offen lds\n" \
"v_mfma_f32_16x16x32_bf16 a[156:159], v[84:87], v[172:175], a[156:159]\n" \
"ds_read_b64_tr_b16 v[168:169], v34 offset:57344\n" \
"ds_read_b64_tr_b16 v[170:171], v34 offset:58368\n" \
"v_mfma_f32_16x16x32_bf16 a[188:191], v[92:95], v[172:175], a[188:191]\n" \
"s_mov_b32 s78, 0x240000\n" \
"v_xor_b32_e32 v39, 0x60, v190\n" \
"v_mfma_f32_16x16x32_bf16 a[188:191], v[96:99], v[176:179], a[188:191]\n" \
"s_add_u32 m0, %[mB], 0x4400\n" \
"s_add_u32 s84, s63, 0x8000\n" \
"v_mfma_f32_16x16x32_bf16 a[220:223], v[104:107], v[176:179], a[220:223]\n" \
"buffer_load_dwordx4 v26, s[72:75], s84 offen lds\n" \
"v_mfma_f32_16x16x32_bf16 a[220:223], v[100:103], v[172:175], a[220:223]\n" \
"v_xor_b32_e32 v37, 0x20, v190\n" \
"v_xor_b32_e32 v38, 0x40, v190\n" \
"v_mfma_f32_16x16x32_bf16 a[252:255], v[108:111], v[172:175], a[252:255]\n" \
"ds_read_b64_tr_b16 v[172:173], v35 offset:49152\n" \
"ds_read_b64_tr_b16 v[174:175], v35 offset:50176\n" \
"v_mfma_f32_16x16x32_bf16 a[252:255], v[112:115], v[176:179], a[252:255]\n" \
"s_add_u32 m0, %[mA], 0x1000\n" \
"s_add_u32 s85, s62, 0x8000\n" \
"v_mfma_f32_16x16x32_bf16 a[128:131], v[120:123], v[88:91], a[128:131]\n" \
"buffer_load_dwordx4 v22, s[64:67], s85 offen lds\n" \
"v_mfma_f32_16x16x32_bf16 a[128:131], v[116:119], v[84:87], a[128:131]\n" \
"ds_read_b64_tr_b16 v[176:177], v35 offset:57344\n" \
"ds_read_b64_tr_b16 v[178:179], v35 offset:58368\n" \
"v_mfma_f32_16x16x32_bf16 a[160:163], v[116:119], v[92:95], a[160:163]\n" \
"v_add_u32_e32 v48, s60, v28\n" \
"v_add_u32_e32 v49, s60, v29\n" \
"v_mfma_f32_16x16x32_bf16 a[160:163], v[120:123], v[96:99], a[160:163]\n" \
"s_add_u32 m0, %[mA], 0x1400\n" \
"s_add_u32 s84, s62, 0xa000\n" \
"v_mfma_f32_16x16x32_bf16 a[192:195], v[120:123], v[104:107], a[192:195]\n" \
"buffer_load_dwordx4 v23, s[64:67], s84 offen lds\n" \
"v_mfma_f32_16x16x32_bf16 a[192:195], v[116:119], v[100:103], a[192:195]\n" \
"v_xor_b32_e32 v36, 0x0, v190\n" \
"v_add_u32_e32 v237, v228, v233\n" \
"v_mfma_f32_16x16x32_bf16 a[224:227], v[116:119], v[108:111], a[224:227]\n" \
"ds_read_b64_tr_b16 v[116:117], v32 offset:32768\n" \
"ds_read_b64_tr_b16 v[118:119], v32 offset:33792\n" \
"v_mfma_f32_16x16x32_bf16 a[224:227], v[120:123], v[112:115], a[224:227]\n" \
"s_add_u32 m0, %[mB], 0x800\n" \
"s_add_u32 s85, s63, 0x10000\n" \
"v_mfma_f32_16x16x32_bf16 a[132:135], v[128:131], v[88:91], a[132:135]\n" \
"buffer_load_dwordx4 v27, s[68:71], s85 offen lds\n" \
"v_mfma_f32_16x16x32_bf16 a[132:135], v[124:127], v[84:87], a[132:135]\n" \
"ds_read_b64_tr_b16 v[120:121], v32 offset:40960\n" \
"ds_read_b64_tr_b16 v[122:123], v32 offset:41984\n" \
"v_mfma_f32_16x16x32_bf16 a[164:167], v[124:127], v[92:95], a[164:167]\n" \
"s_lshl_b32 s97, %[w], 4\n" \
"v_lshrrev_b32_e32 v234, 2, v212\n" \
"v_mfma_f32_16x16x32_bf16 a[164:167], v[128:131], v[96:99], a[164:167]\n" \
"s_add_u32 m0, %[mB], 0x4800\n" \
"s_add_u32 s84, s63, 0x10000\n" \
"v_mfma_f32_16x16x32_bf16 a[196:199], v[128:131], v[104:107], a[196:199]\n" \
"buffer_load_dwordx4 v27, s[72:75], s84 offen lds\n" \
"v_mfma_f32_16x16x32_bf16 a[196:199], v[124:127], v[100:103], a[196:199]\n" \
"v_add_u32_e32 v5, 0xfffffc00, v237\n" \
"v_add_u32_e64 v231, s97, v234\n" \
"v_mfma_f32_16x16x32_bf16 a[228:231], v[124:127], v[108:111], a[228:231]\n" \
"ds_read_b64_tr_b16 v[124:125], v33 offset:32768\n" \
"ds_read_b64_tr_b16 v[126:127], v33 offset:33792\n" \
"v_mfma_f32_16x16x32_bf16 a[228:231], v[128:131], v[112:115], a[228:231]\n" \
"ds_read_b64_tr_b16 v[128:129], v33 offset:40960\n" \
"ds_read_b64_tr_b16 v[130:131], v33 offset:41984\n" \
"v_mfma_f32_16x16x32_bf16 a[136:139], v[136:139], v[88:91], a[136:139]\n" \
"v_lshrrev_b32_e32 v235, 5, v231\n" \
"v_lshrrev_b32_e32 v237, 4, v231\n" \
"v_mfma_f32_16x16x32_bf16 a[136:139], v[132:135], v[84:87], a[136:139]\n" \
"v_mul_u32_u24_e32 v239, 0x480, v241\n" \
"v_lshlrev_b32_e32 v232, 4, v235\n" \
"v_mfma_f32_16x16x32_bf16 a[168:171], v[132:135], v[92:95], a[168:171]\n" \
"v_and_b32_e32 v228, 12, v231\n" \
"v_and_b32_e64 v238, 1, v237\n" \
"v_mfma_f32_16x16x32_bf16 a[168:171], v[136:139], v[96:99], a[168:171]\n" \
"v_add_u32_e32 v233, v232, v228\n" \
"v_lshlrev_b32_e32 v241, 1, v238\n" \
"v_mfma_f32_16x16x32_bf16 a[200:203], v[136:139], v[104:107], a[200:203]\n" \
"v_lshrrev_b32_e32 v236, 1, v231\n" \
"v_add_u32_e32 v240, v233, v241\n" \
"v_mfma_f32_16x16x32_bf16 a[200:203], v[132:135], v[100:103], a[200:203]\n" \
"v_and_b32_e32 v245, 1, v236\n" \
"v_add_u32_e32 v242, v240, v245\n" \
"v_mfma_f32_16x16x32_bf16 a[232:235], v[132:135], v[108:111], a[232:235]\n" \
"ds_read_b64_tr_b16 v[132:133], v34 offset:32768\n" \
"ds_read_b64_tr_b16 v[134:135], v34 offset:33792\n" \
"v_mfma_f32_16x16x32_bf16 a[232:235], v[136:139], v[112:115], a[232:235]\n" \
"ds_read_b64_tr_b16 v[136:137], v34 offset:40960\n" \
"ds_read_b64_tr_b16 v[138:139], v34 offset:41984\n" \
"v_mfma_f32_16x16x32_bf16 a[140:143], v[144:147], v[88:91], a[140:143]\n" \
"ds_read_b128 v[88:91], v29 offset:32768\n" \
"v_add_u32_e64 v248, v239, v244\n" \
"v_mfma_f32_16x16x32_bf16 a[140:143], v[140:143], v[84:87], a[140:143]\n" \
"ds_read_b128 v[84:87], v28 offset:32768\n" \
"v_mul_u32_u24_e32 v246, 18, v242\n" \
"v_mfma_f32_16x16x32_bf16 a[172:175], v[140:143], v[92:95], a[172:175]\n" \
"ds_read_b128 v[92:95], v28 offset:34816\n" \
"v_add_u32_e32 v243, 16, v246\n" \
"v_mfma_f32_16x16x32_bf16 a[172:175], v[144:147], v[96:99], a[172:175]\n" \
"ds_read_b128 v[96:99], v29 offset:34816\n" \
"v_and_b32_e64 v239, 1, v231\n" \
"v_mfma_f32_16x16x32_bf16 a[204:207], v[144:147], v[104:107], a[204:207]\n" \
"ds_read_b128 v[104:107], v29 offset:36864\n" \
"v_add_u32_e32 v6, 0xfffff800, v248\n" \
"v_mfma_f32_16x16x32_bf16 a[204:207], v[140:143], v[100:103], a[204:207]\n" \
"ds_read_b128 v[100:103], v28 offset:36864\n" \
"v_add_u32_e64 v244, v243, v239\n" \
"v_mfma_f32_16x16x32_bf16 a[236:239], v[140:143], v[108:111], a[236:239]\n" \
"ds_read_b64_tr_b16 v[140:141], v35 offset:32768\n" \
"ds_read_b64_tr_b16 v[142:143], v35 offset:33792\n" \
"v_mfma_f32_16x16x32_bf16 a[236:239], v[144:147], v[112:115], a[236:239]\n" \
"s_add_u32 m0, %[mA], 0x1800\n" \
"s_waitcnt lgkmcnt(12)\n" \
"s_nop 0\n" \
"v_mfma_f32_16x16x32_bf16 a[16:19], v[56:59], v[152:155], a[16:19]\n" \
"s_add_u32 s85, s62, 0xc000\n" \
"ds_read_b64_tr_b16 v[144:145], v35 offset:40960\n" \
"v_mfma_f32_16x16x32_bf16 a[16:19], v[52:55], v[148:151], a[16:19]\n" \
"buffer_load_dwordx4 v22, s[64:67], s85 offen lds\n" \
"v_mfma_f32_16x16x32_bf16 a[48:51], v[60:63], v[148:151], a[48:51]\n" \
"s_add_u32 m0, %[mA], 0x1c00\n" \
"s_add_u32 s84, s62, 0xe000\n" \
"v_mfma_f32_16x16x32_bf16 a[48:51], v[64:67], v[152:155], a[48:51]\n" \
"ds_read_b64_tr_b16 v[146:147], v35 offset:41984\n" \
"ds_read_b128 v[108:111], v28 offset:38912\n" \
"v_mfma_f32_16x16x32_bf16 a[80:83], v[72:75], v[152:155], a[80:83]\n" \
"buffer_load_dwordx4 v23, s[64:67], s84 offen lds\n" \
"v_mfma_f32_16x16x32_bf16 a[80:83], v[68:71], v[148:151], a[80:83]\n" \
"s_add_u32 m0, %[mB], 0xc00\n" \
"s_add_u32 s85, s63, 0x18000\n" \
"v_mfma_f32_16x16x32_bf16 a[112:115], v[76:79], v[148:151], a[112:115]\n" \
"ds_read_b128 v[112:115], v29 offset:38912\n" \
"v_mul_u32_u24_e32 v248, 0x480, v244\n" \
"v_mfma_f32_16x16x32_bf16 a[112:115], v[80:83], v[152:155], a[112:115]\n" \
"buffer_load_dwordx4 v27, s[68:71], s85 offen lds\n" \
"v_mfma_f32_16x16x32_bf16 a[20:23], v[56:59], v[160:163], a[20:23]\n" \
"s_add_u32 m0, %[mB], 0x4c00\n" \
"s_add_u32 s84, s63, 0x18000\n" \
"v_mfma_f32_16x16x32_bf16 a[20:23], v[52:55], v[156:159], a[20:23]\n" \
"v_add_u32_e32 v215, v250, v219\n" \
"v_add_u32_e32 v217, v248, v247\n" \
"v_mfma_f32_16x16x32_bf16 a[52:55], v[60:63], v[156:159], a[52:55]\n" \
"buffer_load_dwordx4 v27, s[72:75], s84 offen lds\n" \
"v_mfma_f32_16x16x32_bf16 a[52:55], v[64:67], v[160:163], a[52:55]\n" \
"v_and_b32_e64 v214, 16, v20\n" \
"v_add_u32_e32 v7, 0xfffff400, v215\n" \
"v_mfma_f32_16x16x32_bf16 a[84:87], v[72:75], v[160:163], a[84:87]\n" \
"v_add_u32_e32 v9, v217, v214\n" \
"v_and_b32_e32 v216, 3, v212\n" \
"v_mfma_f32_16x16x32_bf16 a[84:87], v[68:71], v[156:159], a[84:87]\n" \
"v_lshlrev_b32_e32 v250, 3, v216\n" \
"v_mul_u32_u24_e32 v220, 0x120, v234\n" \
"v_mfma_f32_16x16x32_bf16 a[116:119], v[76:79], v[156:159], a[116:119]\n" \
"v_mul_u32_u24_e32 v223, 0x480, v222\n" \
"v_add_u32_e32 v10, v220, v250\n" \
"v_mfma_f32_16x16x32_bf16 a[116:119], v[80:83], v[160:163], a[116:119]\n" \
"s_mov_b32 m0, %[mS]\n" \
"s_mov_b32 s86, s8\n" \
"v_mfma_f32_16x16x32_bf16 a[24:27], v[56:59], v[168:171], a[24:27]\n" \
"s_mov_b32 s87, s9\n" \
"v_add_u32_e32 v8, v223, v225\n" \
"v_mfma_f32_16x16x32_bf16 a[24:27], v[52:55], v[164:167], a[24:27]\n" \
"s_lshl_b32 s99, %[w], 11\n" \
"s_add_u32 s55, s98, s99\n" \
"v_mfma_f32_16x16x32_bf16 a[56:59], v[60:63], v[164:167], a[56:59]\n" \
"v_mfma_f32_16x16x32_bf16 a[56:59], v[64:67], v[168:171], a[56:59]\n" \
"v_mfma_f32_16x16x32_bf16 a[88:91], v[72:75], v[168:171], a[88:91]\n" \
"v_mfma_f32_16x16x32_bf16 a[88:91], v[68:71], v[164:167], a[88:91]\n" \
"s_mov_b32 exec_lo, 0x0\n" \
"s_mov_b32 exec_hi, 0x3ffff0\n" \
"global_load_lds_dword v188, s[86:87]\n" \
"s_mov_b64 exec, -1\n" \
"v_mfma_f32_16x16x32_bf16 a[120:123], v[76:79], v[164:167], a[120:123]\n" \
"v_mfma_f32_16x16x32_bf16 a[120:123], v[80:83], v[168:171], a[120:123]\n" \
"v_mfma_f32_16x16x32_bf16 a[28:31], v[56:59], v[176:179], a[28:31]\n" \
"v_mfma_f32_16x16x32_bf16 a[28:31], v[52:55], v[172:175], a[28:31]\n" \
"v_mfma_f32_16x16x32_bf16 a[60:63], v[60:63], v[172:175], a[60:63]\n" \
"v_mfma_f32_16x16x32_bf16 a[60:63], v[64:67], v[176:179], a[60:63]\n" \
"v_mfma_f32_16x16x32_bf16 a[92:95], v[72:75], v[176:179], a[92:95]\n" \
"v_mfma_f32_16x16x32_bf16 a[92:95], v[68:71], v[172:175], a[92:95]\n" \
"v_mfma_f32_16x16x32_bf16 a[124:127], v[76:79], v[172:175], a[124:127]\n" \
"v_mfma_f32_16x16x32_bf16 a[124:127], v[80:83], v[176:179], a[124:127]\n" \
"v_mfma_f32_16x16x32_bf16 a[0:3], v[120:123], v[56:59], a[0:3]\n" \
"v_mfma_f32_16x16x32_bf16 a[0:3], v[116:119], v[52:55], a[0:3]\n" \
"v_mfma_f32_16x16x32_bf16 a[32:35], v[116:119], v[60:63], a[32:35]\n" \
"v_mfma_f32_16x16x32_bf16 a[32:35], v[120:123], v[64:67], a[32:35]\n" \
"v_mfma_f32_16x16x32_bf16 a[64:67], v[120:123], v[72:75], a[64:67]\n" \
"v_mfma_f32_16x16x32_bf16 a[64:67], v[116:119], v[68:71], a[64:67]\n" \
"v_mfma_f32_16x16x32_bf16 a[96:99], v[116:119], v[76:79], a[96:99]\n" \
"v_mfma_f32_16x16x32_bf16 a[96:99], v[120:123], v[80:83], a[96:99]\n" \
"v_mfma_f32_16x16x32_bf16 a[4:7], v[128:131], v[56:59], a[4:7]\n" \
"v_mfma_f32_16x16x32_bf16 a[4:7], v[124:127], v[52:55], a[4:7]\n" \
"v_mfma_f32_16x16x32_bf16 a[36:39], v[124:127], v[60:63], a[36:39]\n" \
"v_mfma_f32_16x16x32_bf16 a[36:39], v[128:131], v[64:67], a[36:39]\n" \
"v_mfma_f32_16x16x32_bf16 a[68:71], v[128:131], v[72:75], a[68:71]\n" \
"v_mfma_f32_16x16x32_bf16 a[68:71], v[124:127], v[68:71], a[68:71]\n" \
"v_mfma_f32_16x16x32_bf16 a[100:103], v[124:127], v[76:79], a[100:103]\n" \
"v_mfma_f32_16x16x32_bf16 a[100:103], v[128:131], v[80:83], a[100:103]\n" \
"s_waitcnt lgkmcnt(0)\n" \
"s_nop 0\n" \
"v_mfma_f32_16x16x32_bf16 a[8:11], v[136:139], v[56:59], a[8:11]\n" \
"v_mfma_f32_16x16x32_bf16 a[8:11], v[132:135], v[52:55], a[8:11]\n" \
"v_mfma_f32_16x16x32_bf16 a[40:43], v[132:135], v[60:63], a[40:43]\n" \
"v_mfma_f32_16x16x32_bf16 a[40:43], v[136:139], v[64:67], a[40:43]\n" \
"v_mfma_f32_16x16x32_bf16 a[72:75], v[136:139], v[72:75], a[72:75]\n" \
"v_mfma_f32_16x16x32_bf16 a[72:75], v[132:135], v[68:71], a[72:75]\n" \
"v_mfma_f32_16x16x32_bf16 a[104:107], v[132:135], v[76:79], a[104:107]\n" \
"v_mfma_f32_16x16x32_bf16 a[104:107], v[136:139], v[80:83], a[104:107]\n" \
"v_mfma_f32_16x16x32_bf16 a[12:15], v[144:147], v[56:59], a[12:15]\n" \
"s_add_u32 s62, s22, 0x380\n" \
"s_and_b32 s62, s62, 0x3ff\n" \
"v_mfma_f32_16x16x32_bf16 a[12:15], v[140:143], v[52:55], a[12:15]\n" \
"s_lshl_b32 s63, s62, 12\n" \
"v_mfma_f32_16x16x32_bf16 a[44:47], v[140:143], v[60:63], a[44:47]\n" \
"v_mfma_f32_16x16x32_bf16 a[44:47], v[144:147], v[64:67], a[44:47]\n" \
"v_mfma_f32_16x16x32_bf16 a[76:79], v[144:147], v[72:75], a[76:79]\n" \
"v_mfma_f32_16x16x32_bf16 a[76:79], v[140:143], v[68:71], a[76:79]\n" \
"v_mfma_f32_16x16x32_bf16 a[108:111], v[140:143], v[76:79], a[108:111]\n" \
"v_mfma_f32_16x16x32_bf16 a[108:111], v[144:147], v[80:83], a[108:111]\n" \
"s_waitcnt vmcnt(1)\n" \
"s_barrier\n" \
"s_mov_b64 exec, 1\n" \
"buffer_store_dword v45, off, s[80:83], s38 offset:64 sc1\n" \
"s_mov_b64 exec, -1\n" \
"v_mfma_f32_16x16x32_bf16 a[128:131], v[120:123], v[88:91], a[128:131]\n" \
"s_add_u32 m0, %[mA], 0x8000\n" \
"s_add_u32 s85, s62, 0x0\n" \
"v_mfma_f32_16x16x32_bf16 a[128:131], v[116:119], v[84:87], a[128:131]\n" \
"ds_read_b128 v[56:59], v29 offset:16384\n" \
"ds_read_b128 v[52:55], v28 offset:16384\n" \
"v_mfma_f32_16x16x32_bf16 a[160:163], v[116:119], v[92:95], a[160:163]\n" \
"buffer_load_dwordx4 v22, s[64:67], s85 offen lds\n" \
"v_mfma_f32_16x16x32_bf16 a[160:163], v[120:123], v[96:99], a[160:163]\n" \
"s_add_u32 m0, %[mA], 0x8400\n" \
"s_add_u32 s84, s62, 0x2000\n" \
"v_mfma_f32_16x16x32_bf16 a[192:195], v[120:123], v[104:107], a[192:195]\n" \
"ds_read_b128 v[60:63], v28 offset:18432\n" \
"ds_read_b128 v[64:67], v29 offset:18432\n" \
"v_mfma_f32_16x16x32_bf16 a[192:195], v[116:119], v[100:103], a[192:195]\n" \
"buffer_load_dwordx4 v23, s[64:67], s84 offen lds\n" \
"v_mfma_f32_16x16x32_bf16 a[224:227], v[116:119], v[108:111], a[224:227]\n" \
"ds_read_b64_tr_b16 v[116:117], v32 offset:0\n" \
"ds_read_b64_tr_b16 v[118:119], v32 offset:1024\n" \
"v_mfma_f32_16x16x32_bf16 a[224:227], v[120:123], v[112:115], a[224:227]\n" \
"s_add_u32 m0, %[mB], 0x8000\n" \
"s_add_u32 s85, s63, 0x0\n" \
"v_mfma_f32_16x16x32_bf16 a[132:135], v[128:131], v[88:91], a[132:135]\n" \
"buffer_load_dwordx4 v26, s[68:71], s85 offen lds\n" \
"v_mfma_f32_16x16x32_bf16 a[132:135], v[124:127], v[84:87], a[132:135]\n" \
"ds_read_b64_tr_b16 v[120:121], v32 offset:8192\n" \
"ds_read_b64_tr_b16 v[122:123], v32 offset:9216\n" \
"v_mfma_f32_16x16x32_bf16 a[164:167], v[124:127], v[92:95], a[164:167]\n" \
"ds_read_b128 v[72:75], v29 offset:20480\n" \
"ds_read_b128 v[68:71], v28 offset:20480\n" \
"v_mfma_f32_16x16x32_bf16 a[164:167], v[128:131], v[96:99], a[164:167]\n" \
"s_add_u32 m0, %[mB], 0xc000\n" \
"s_add_u32 s84, s63, 0x0\n" \
"v_mfma_f32_16x16x32_bf16 a[196:199], v[128:131], v[104:107], a[196:199]\n" \
"buffer_load_dwordx4 v26, s[72:75], s84 offen lds\n" \
"v_mfma_f32_16x16x32_bf16 a[196:199], v[124:127], v[100:103], a[196:199]\n" \
"ds_read_b128 v[76:79], v28 offset:22528\n" \
"ds_read_b128 v[80:83], v29 offset:22528\n" \
"v_mfma_f32_16x16x32_bf16 a[228:231], v[124:127], v[108:111], a[228:231]\n" \
"ds_read_b64_tr_b16 v[124:125], v33 offset:0\n" \
"ds_read_b64_tr_b16 v[126:127], v33 offset:1024\n" \
"v_mfma_f32_16x16x32_bf16 a[228:231], v[128:131], v[112:115], a[228:231]\n" \
"s_add_u32 m0, %[mA], 0x8800\n" \
"s_add_u32 s85, s62, 0x4000\n" \
"v_mfma_f32_16x16x32_bf16 a[136:139], v[136:139], v[88:91], a[136:139]\n" \
"buffer_load_dwordx4 v22, s[64:67], s85 offen lds\n" \
"v_mfma_f32_16x16x32_bf16 a[136:139], v[132:135], v[84:87], a[136:139]\n" \
"ds_read_b64_tr_b16 v[128:129], v33 offset:8192\n" \
"ds_read_b64_tr_b16 v[130:131], v33 offset:9216\n" \
"v_mfma_f32_16x16x32_bf16 a[168:171], v[132:135], v[92:95], a[168:171]\n" \
"v_mov_b32_e64 v45, %[tag]\n" \
"v_mfma_f32_16x16x32_bf16 a[168:171], v[136:139], v[96:99], a[168:171]\n" \
"s_add_u32 m0, %[mA], 0x8c00\n" \
"s_add_u32 s84, s62, 0x6000\n" \
"v_mfma_f32_16x16x32_bf16 a[200:203], v[136:139], v[104:107], a[200:203]\n" \
"buffer_load_dwordx4 v23, s[64:67], s84 offen lds\n" \
"v_mfma_f32_16x16x32_bf16 a[200:203], v[132:135], v[100:103], a[200:203]\n" \
"v_mfma_f32_16x16x32_bf16 a[232:235], v[132:135], v[108:111], a[232:235]\n" \
"ds_read_b64_tr_b16 v[132:133], v34 offset:0\n" \
"ds_read_b64_tr_b16 v[134:135], v34 offset:1024\n" \
"v_mfma_f32_16x16x32_bf16 a[232:235], v[136:139], v[112:115], a[232:235]\n" \
"s_add_u32 m0, %[mB], 0x8400\n" \
"s_add_u32 s85, s63, 0x8000\n" \
"v_mfma_f32_16x16x32_bf16 a[140:143], v[144:147], v[88:91], a[140:143]\n" \
"buffer_load_dwordx4 v26, s[68:71], s85 offen lds\n" \
"v_mfma_f32_16x16x32_bf16 a[140:143], v[140:143], v[84:87], a[140:143]\n" \
"ds_read_b64_tr_b16 v[136:137], v34 offset:8192\n" \
"ds_read_b64_tr_b16 v[138:139], v34 offset:9216\n" \
"v_mfma_f32_16x16x32_bf16 a[172:175], v[140:143], v[92:95], a[172:175]\n" \
"v_mfma_f32_16x16x32_bf16 a[172:175], v[144:147], v[96:99], a[172:175]\n" \
"s_add_u32 m0, %[mB], 0xc400\n" \
"s_add_u32 s84, s63, 0x8000\n" \
"v_mfma_f32_16x16x32_bf16 a[204:207], v[144:147], v[104:107], a[204:207]\n" \
"buffer_load_dwordx4 v26, s[72:75], s84 offen lds\n" \
"v_mfma_f32_16x16x32_bf16 a[204:207], v[140:143], v[100:103], a[204:207]\n" \
"v_mfma_f32_16x16x32_bf16 a[236:239], v[140:143], v[108:111], a[236:239]\n" \
"ds_read_b64_tr_b16 v[140:141], v35 offset:0\n" \
"ds_read_b64_tr_b16 v[142:143], v35 offset:1024\n" \
"v_mfma_f32_16x16x32_bf16 a[236:239], v[144:147], v[112:115], a[236:239]\n" \
"s_add_u32 m0, %[mA], 0x9000\n" \
"s_add_u32 s85, s62, 0x8000\n" \
"v_mfma_f32_16x16x32_bf16 a[144:147], v[88:91], v[152:155], a[144:147]\n" \
"buffer_load_dwordx4 v22, s[64:67], s85 offen lds\n" \
"v_mfma_f32_16x16x32_bf16 a[144:147], v[84:87], v[148:151], a[144:147]\n" \
"ds_read_b64_tr_b16 v[144:145], v35 offset:8192\n" \
"ds_read_b64_tr_b16 v[146:147], v35 offset:9216\n" \
"v_mfma_f32_16x16x32_bf16 a[176:179], v[92:95], v[148:151], a[176:179]\n" \
"v_mfma_f32_16x16x32_bf16 a[176:179], v[96:99], v[152:155], a[176:179]\n" \
"s_add_u32 m0, %[mA], 0x9400\n" \
"s_add_u32 s84, s62, 0xa000\n" \
"v_mfma_f32_16x16x32_bf16 a[208:211], v[104:107], v[152:155], a[208:211]\n" \
"buffer_load_dwordx4 v23, s[64:67], s84 offen lds\n" \
"v_mfma_f32_16x16x32_bf16 a[208:211], v[100:103], v[148:151], a[208:211]\n" \
"v_mfma_f32_16x16x32_bf16 a[240:243], v[108:111], v[148:151], a[240:243]\n" \
"ds_read_b64_tr_b16 v[148:149], v32 offset:16384\n" \
"ds_read_b64_tr_b16 v[150:151], v32 offset:17408\n" \
"v_mfma_f32_16x16x32_bf16 a[240:243], v[112:115], v[152:155], a[240:243]\n" \
"s_add_u32 m0, %[mB], 0x8800\n" \
"s_add_u32 s85, s63, 0x10000\n" \
"v_mfma_f32_16x16x32_bf16 a[148:151], v[88:91], v[160:163], a[148:151]\n" \
"buffer_load_dwordx4 v27, s[68:71], s85 offen lds\n" \
"v_mfma_f32_16x16x32_bf16 a[148:151], v[84:87], v[156:159], a[148:151]\n" \
"ds_read_b64_tr_b16 v[152:153], v32 offset:24576\n" \
"ds_read_b64_tr_b16 v[154:155], v32 offset:25600\n" \
"v_mfma_f32_16x16x32_bf16 a[180:183], v[92:95], v[156:159], a[180:183]\n" \
"v_mfma_f32_16x16x32_bf16 a[180:183], v[96:99], v[160:163], a[180:183]\n" \
"s_add_u32 m0, %[mB], 0xc800\n" \
"L_pf3:\n" \
"s_cbranch_execz L_pf7\n" \
"s_nop 0\n" \
"s_add_u32 s84, s63, 0x10000\n" \
"v_mfma_f32_16x16x32_bf16 a[212:215], v[104:107], v[160:163], a[212:215]\n" \
"buffer_load_dwordx4 v27, s[72:75], s84 offen lds\n" \
"v_mfma_f32_16x16x32_bf16 a[212:215], v[100:103], v[156:159], a[212:215]\n" \
"v_mfma_f32_16x16x32_bf16 a[244:247], v[108:111], v[156:159], a[244:247]\n" \
"ds_read_b64_tr_b16 v[156:157], v33 offset:16384\n" \
"ds_read_b64_tr_b16 v[158:159], v33 offset:17408\n" \
"v_mfma_f32_16x16x32_bf16 a[244:247], v[112:115], v[160:163], a[244:247]\n" \
"ds_read_b64_tr_b16 v[160:161], v33 offset:24576\n" \
"ds_read_b64_tr_b16 v[162:163], v33 offset:25600\n" \
"v_mfma_f32_16x16x32_bf16 a[152:155], v[88:91], v[168:171], a[152:155]\n" \
"v_mfma_f32_16x16x32_bf16 a[152:155], v[84:87], v[164:167], a[152:155]\n" \
"v_mfma_f32_16x16x32_bf16 a[184:187], v[92:95], v[164:167], a[184:187]\n" \
"v_mfma_f32_16x16x32_bf16 a[184:187], v[96:99], v[168:171], a[184:187]\n" \
"v_mfma_f32_16x16x32_bf16 a[216:219], v[104:107], v[168:171], a[216:219]\n" \
"v_mfma_f32_16x16x32_bf16 a[216:219], v[100:103], v[164:167], a[216:219]\n" \
"v_mfma_f32_16x16x32_bf16 a[248:251], v[108:111], v[164:167], a[248:251]\n" \
"ds_read_b64_tr_b16 v[164:165], v34 offset:16384\n" \
"ds_read_b64_tr_b16 v[166:167], v34 offset:17408\n" \
"v_mfma_f32_16x16x32_bf16 a[248:251], v[112:115], v[168:171], a[248:251]\n" \
"ds_read_b64_tr_b16 v[168:169], v34 offset:24576\n" \
"ds_read_b64_tr_b16 v[170:171], v34 offset:25600\n" \
"v_mfma_f32_16x16x32_bf16 a[156:159], v[88:91], v[176:179], a[156:159]\n" \
"ds_read_b128 v[88:91], v29 offset:0\n" \
"v_mfma_f32_16x16x32_bf16 a[156:159], v[84:87], v[172:175], a[156:159]\n" \
"ds_read_b128 v[84:87], v28 offset:0\n" \
"v_mfma_f32_16x16x32_bf16 a[188:191], v[92:95], v[172:175], a[188:191]\n" \
"ds_read_b128 v[92:95], v28 offset:2048\n" \
"v_mfma_f32_16x16x32_bf16 a[188:191], v[96:99], v[176:179], a[188:191]\n" \
"ds_read_b128 v[96:99], v29 offset:2048\n" \
"v_mfma_f32_16x16x32_bf16 a[220:223], v[104:107], v[176:179], a[220:223]\n" \
"ds_read_b128 v[104:107], v29 offset:4096\n" \
"v_mfma_f32_16x16x32_bf16 a[220:223], v[100:103], v[172:175], a[220:223]\n" \
"ds_read_b128 v[100:103], v28 offset:4096\n" \
"v_mfma_f32_16x16x32_bf16 a[252:255], v[108:111], v[172:175], a[252:255]\n" \
"ds_read_b64_tr_b16 v[172:173], v35 offset:16384\n" \
"ds_read_b64_tr_b16 v[174:175], v35 offset:17408\n" \
"v_mfma_f32_16x16x32_bf16 a[252:255], v[112:115], v[176:179], a[252:255]\n" \
"s_add_u32 m0, %[mA], 0x9800\n" \
"s_waitcnt lgkmcnt(12)\n" \
"s_nop 0\n" \
"v_mfma_f32_16x16x32_bf16 a[0:3], v[120:123], v[56:59], a[0:3]\n" \
"s_add_u32 s85, s62, 0xc000\n" \
"ds_read_b64_tr_b16 v[176:177], v35 offset:24576\n" \
"v_mfma_f32_16x16x32_bf16 a[0:3], v[116:119], v[52:55], a[0:3]\n" \
"buffer_load_dwordx4 v22, s[64:67], s85 offen lds\n" \
"v_mfma_f32_16x16x32_bf16 a[32:35], v[116:119], v[60:63], a[32:35]\n" \
"s_add_u32 m0, %[mA], 0x9c00\n" \
"s_add_u32 s84, s62, 0xe000\n" \
"v_mfma_f32_16x16x32_bf16 a[32:35], v[120:123], v[64:67], a[32:35]\n" \
"ds_read_b64_tr_b16 v[178:179], v35 offset:25600\n" \
"ds_read_b128 v[108:111], v28 offset:6144\n" \
"v_mfma_f32_16x16x32_bf16 a[64:67], v[120:123], v[72:75], a[64:67]\n" \
"buffer_load_dwordx4 v23, s[64:67], s84 offen lds\n" \
"v_mfma_f32_16x16x32_bf16 a[64:67], v[116:119], v[68:71], a[64:67]\n" \
"s_add_u32 m0, %[mB], 0x8c00\n" \
"s_add_u32 s85, s63, 0x18000\n" \
"v_mfma_f32_16x16x32_bf16 a[96:99], v[116:119], v[76:79], a[96:99]\n" \
"ds_read_b128 v[112:115], v29 offset:6144\n" \
"v_add_u32_e64 v23, s61, v29\n" \
"v_mfma_f32_16x16x32_bf16 a[96:99], v[120:123], v[80:83], a[96:99]\n" \
"buffer_load_dwordx4 v27, s[68:71], s85 offen lds\n" \
"v_mfma_f32_16x16x32_bf16 a[4:7], v[128:131], v[56:59], a[4:7]\n" \
"s_add_u32 m0, %[mB], 0xcc00\n" \
"s_add_u32 s84, s63, 0x18000\n" \
"v_mfma_f32_16x16x32_bf16 a[4:7], v[124:127], v[52:55], a[4:7]\n" \
"s_mov_b32 s68, s10\n" \
"s_and_b32 s69, s11, 0xffff\n" \
"v_mfma_f32_16x16x32_bf16 a[36:39], v[124:127], v[60:63], a[36:39]\n" \
"buffer_load_dwordx4 v27, s[72:75], s84 offen lds\n" \
"s_mov_b32 s73, s52\n" \
"s_add_u32 s74, s52, 0x4000\n" \
"s_add_u32 s75, s52, 0x8000\n" \
"v_mfma_f32_16x16x32_bf16 a[36:39], v[128:131], v[64:67], a[36:39]\n" \
"s_mov_b32 s70, 0x480000\n" \
"v_add_u32_e64 v22, s61, v28\n" \
"v_mfma_f32_16x16x32_bf16 a[68:71], v[128:131], v[72:75], a[68:71]\n" \
"v_bfe_u32 v44, v20, 2, 5\n" \
"s_mov_b32 s72, 0x3eae1a93\n" \
"v_mfma_f32_16x16x32_bf16 a[68:71], v[124:127], v[68:71], a[68:71]\n" \
"buffer_load_dword v44, v44, s[80:83], s39 offen offset:64 sc1\n" \
"v_mfma_f32_16x16x32_bf16 a[100:103], v[124:127], v[76:79], a[100:103]\n" \
"v_mfma_f32_16x16x32_bf16 a[100:103], v[128:131], v[80:83], a[100:103]\n" \
"s_mov_b32 m0, %[mS]\n" \
"s_mov_b32 s86, s18\n" \
"v_mfma_f32_16x16x32_bf16 a[8:11], v[136:139], v[56:59], a[8:11]\n" \
"s_mov_b32 s87, s19\n" \
"v_mfma_f32_16x16x32_bf16 a[8:11], v[132:135], v[52:55], a[8:11]\n" \
"v_mfma_f32_16x16x32_bf16 a[40:43], v[132:135], v[60:63], a[40:43]\n" \
"v_mfma_f32_16x16x32_bf16 a[40:43], v[136:139], v[64:67], a[40:43]\n" \
"v_mfma_f32_16x16x32_bf16 a[72:75], v[136:139], v[72:75], a[72:75]\n" \
"v_mfma_f32_16x16x32_bf16 a[72:75], v[132:135], v[68:71], a[72:75]\n" \
"s_mov_b32 exec_lo, 0xfffc0000\n" \
"s_mov_b32 exec_hi, 0xf\n" \
"global_load_lds_dword v16, s[86:87]\n" \
"s_mov_b64 exec, -1\n" \
"v_mfma_f32_16x16x32_bf16 a[104:107], v[132:135], v[76:79], a[104:107]\n" \
"s_mov_b32 m0, %[mS]\n" \
"s_mov_b32 s86, s16\n" \
"v_mfma_f32_16x16x32_bf16 a[104:107], v[136:139], v[80:83], a[104:107]\n" \
"s_mov_b32 s87, s17\n" \
"v_mfma_f32_16x16x32_bf16 a[12:15], v[144:147], v[56:59], a[12:15]\n" \
"v_mfma_f32_16x16x32_bf16 a[12:15], v[140:143], v[52:55], a[12:15]\n" \
"v_mfma_f32_16x16x32_bf16 a[44:47], v[140:143], v[60:63], a[44:47]\n" \
"s_mov_b32 exec_lo, 0x0\n" \
"s_mov_b32 exec_hi, 0x3f0\n" \
"global_load_lds_dword v16, s[86:87]\n" \
"s_mov_b64 exec, -1\n" \
"v_mfma_f32_16x16x32_bf16 a[44:47], v[144:147], v[64:67], a[44:47]\n" \
"v_mfma_f32_16x16x32_bf16 a[76:79], v[144:147], v[72:75], a[76:79]\n" \
"v_mfma_f32_16x16x32_bf16 a[76:79], v[140:143], v[68:71], a[76:79]\n" \
"v_mfma_f32_16x16x32_bf16 a[108:111], v[140:143], v[76:79], a[108:111]\n" \
"v_mfma_f32_16x16x32_bf16 a[108:111], v[144:147], v[80:83], a[108:111]\n" \
"v_mfma_f32_16x16x32_bf16 a[16:19], v[56:59], v[152:155], a[16:19]\n" \
"v_mfma_f32_16x16x32_bf16 a[16:19], v[52:55], v[148:151], a[16:19]\n" \
"v_mfma_f32_16x16x32_bf16 a[48:51], v[60:63], v[148:151], a[48:51]\n" \
"v_mfma_f32_16x16x32_bf16 a[48:51], v[64:67], v[152:155], a[48:51]\n" \
"v_mfma_f32_16x16x32_bf16 a[80:83], v[72:75], v[152:155], a[80:83]\n" \
"v_mfma_f32_16x16x32_bf16 a[80:83], v[68:71], v[148:151], a[80:83]\n" \
"v_mfma_f32_16x16x32_bf16 a[112:115], v[76:79], v[148:151], a[112:115]\n" \
"v_mfma_f32_16x16x32_bf16 a[112:115], v[80:83], v[152:155], a[112:115]\n" \
"v_mfma_f32_16x16x32_bf16 a[20:23], v[56:59], v[160:163], a[20:23]\n" \
"v_mfma_f32_16x16x32_bf16 a[20:23], v[52:55], v[156:159], a[20:23]\n" \
"v_mfma_f32_16x16x32_bf16 a[52:55], v[60:63], v[156:159], a[52:55]\n" \
"v_mfma_f32_16x16x32_bf16 a[52:55], v[64:67], v[160:163], a[52:55]\n" \
"v_mfma_f32_16x16x32_bf16 a[84:87], v[72:75], v[160:163], a[84:87]\n" \
"v_mfma_f32_16x16x32_bf16 a[84:87], v[68:71], v[156:159], a[84:87]\n" \
"v_mfma_f32_16x16x32_bf16 a[116:119], v[76:79], v[156:159], a[116:119]\n" \
"v_mfma_f32_16x16x32_bf16 a[116:119], v[80:83], v[160:163], a[116:119]\n" \
"s_waitcnt lgkmcnt(0)\n" \
"v_mfma_f32_16x16x32_bf16 a[24:27], v[56:59], v[168:171], a[24:27]\n" \
"v_mfma_f32_16x16x32_bf16 a[24:27], v[52:55], v[164:167], a[24:27]\n" \
"v_mfma_f32_16x16x32_bf16 a[56:59], v[60:63], v[164:167], a[56:59]\n" \
"v_mfma_f32_16x16x32_bf16 a[56:59], v[64:67], v[168:171], a[56:59]\n" \
"v_mfma_f32_16x16x32_bf16 a[88:91], v[72:75], v[168:171], a[88:91]\n" \
"v_mfma_f32_16x16x32_bf16 a[88:91], v[68:71], v[164:167], a[88:91]\n" \
"v_mfma_f32_16x16x32_bf16 a[120:123], v[76:79], v[164:167], a[120:123]\n" \
"v_mfma_f32_16x16x32_bf16 a[120:123], v[80:83], v[168:171], a[120:123]\n" \
"v_mfma_f32_16x16x32_bf16 a[28:31], v[56:59], v[176:179], a[28:31]\n" \
"s_waitcnt vmcnt(2)\n" \
"v_cmp_ne_u32_e32 vcc, %[tag], v44\n" \
"v_mfma_f32_16x16x32_bf16 a[28:31], v[52:55], v[172:175], a[28:31]\n" \
"v_mfma_f32_16x16x32_bf16 a[60:63], v[60:63], v[172:175], a[60:63]\n" \
"v_mfma_f32_16x16x32_bf16 a[60:63], v[64:67], v[176:179], a[60:63]\n" \
"v_mfma_f32_16x16x32_bf16 a[92:95], v[72:75], v[176:179], a[92:95]\n" \
"v_mfma_f32_16x16x32_bf16 a[92:95], v[68:71], v[172:175], a[92:95]\n" \
"s_cbranch_vccnz L_pl_%=\n" \
"L_rl_%=:\n" \
"v_mfma_f32_16x16x32_bf16 a[124:127], v[76:79], v[172:175], a[124:127]\n" \
"v_mfma_f32_16x16x32_bf16 a[124:127], v[80:83], v[176:179], a[124:127]\n" \
"s_barrier\n" \
"v_mfma_f32_16x16x32_bf16 a[144:147], v[88:91], v[152:155], a[144:147]\n" \
"s_add_u32 m0, s40, 0x0\n" \
"s_add_u32 s85, s37, 0x0\n" \
"s_nop 0\n" \
"v_mfma_f32_16x16x32_bf16 a[144:147], v[84:87], v[148:151], a[144:147]\n" \
"buffer_load_dwordx4 v20, s[80:83], s85 offen sc1 lds\n" \
"v_mfma_f32_16x16x32_bf16 a[176:179], v[92:95], v[148:151], a[176:179]\n" \
"ds_read_b128 v[56:59], v29 offset:49152\n" \
"ds_read_b128 v[52:55], v28 offset:49152\n" \
"v_mfma_f32_16x16x32_bf16 a[176:179], v[96:99], v[152:155], a[176:179]\n" \
"s_add_u32 m0, s40, 0x1000\n" \
"s_add_u32 s84, s37, 0x1000\n" \
"v_mfma_f32_16x16x32_bf16 a[208:211], v[104:107], v[152:155], a[208:211]\n" \
"buffer_load_dwordx4 v20, s[80:83], s84 offen sc1 lds\n" \
"v_mfma_f32_16x16x32_bf16 a[208:211], v[100:103], v[148:151], a[208:211]\n" \
"ds_read_b128 v[60:63], v28 offset:51200\n" \
"ds_read_b128 v[64:67], v29 offset:51200\n" \
"v_mfma_f32_16x16x32_bf16 a[240:243], v[108:111], v[148:151], a[240:243]\n" \
"ds_read_b64_tr_b16 v[148:149], v32 offset:49152\n" \
"ds_read_b64_tr_b16 v[150:151], v32 offset:50176\n" \
"v_mfma_f32_16x16x32_bf16 a[240:243], v[112:115], v[152:155], a[240:243]\n" \
"s_add_u32 m0, s40, 0x3000\n" \
"s_mov_b32 s85, 0x0\n" \
"v_mfma_f32_16x16x32_bf16 a[148:151], v[88:91], v[160:163], a[148:151]\n" \
"buffer_load_dwordx4 v21, s[76:79], s85 offen lds\n" \
"v_mfma_f32_16x16x32_bf16 a[148:151], v[84:87], v[156:159], a[148:151]\n" \
"ds_read_b64_tr_b16 v[152:153], v32 offset:57344\n" \
"ds_read_b64_tr_b16 v[154:155], v32 offset:58368\n" \
"v_mfma_f32_16x16x32_bf16 a[180:183], v[92:95], v[156:159], a[180:183]\n" \
"ds_read_b128 v[72:75], v29 offset:53248\n" \
"ds_read_b128 v[68:71], v28 offset:53248\n" \
"v_mfma_f32_16x16x32_bf16 a[180:183], v[96:99], v[160:163], a[180:183]\n" \
"s_add_u32 m0, %[mA], 0x0\n" \
"s_mov_b32 s84, 0x0\n" \
"s_nop 0\n" \
"v_mfma_f32_16x16x32_bf16 a[212:215], v[104:107], v[160:163], a[212:215]\n" \
"buffer_load_dwordx4 v24, s[76:79], s84 offen lds\n" \
"v_mfma_f32_16x16x32_bf16 a[212:215], v[100:103], v[156:159], a[212:215]\n" \
"ds_read_b128 v[76:79], v28 offset:55296\n" \
"ds_read_b128 v[80:83], v29 offset:55296\n" \
"v_mfma_f32_16x16x32_bf16 a[244:247], v[108:111], v[156:159], a[244:247]\n" \
"ds_read_b64_tr_b16 v[156:157], v33 offset:49152\n" \
"ds_read_b64_tr_b16 v[158:159], v33 offset:50176\n" \
"v_mfma_f32_16x16x32_bf16 a[244:247], v[112:115], v[160:163], a[244:247]\n" \
"s_add_u32 m0, %[mA], 0x400\n" \
"s_mov_b32 s85, 0x2400\n" \
"v_mfma_f32_16x16x32_bf16 a[152:155], v[88:91], v[168:171], a[152:155]\n" \
"buffer_load_dwordx4 v25, s[76:79], s85 offen lds\n" \
"v_mfma_f32_16x16x32_bf16 a[152:155], v[84:87], v[164:167], a[152:155]\n" \
"ds_read_b64_tr_b16 v[160:161], v33 offset:57344\n" \
"ds_read_b64_tr_b16 v[162:163], v33 offset:58368\n" \
"v_mfma_f32_16x16x32_bf16 a[184:187], v[92:95], v[164:167], a[184:187]\n" \
"v_mfma_f32_16x16x32_bf16 a[184:187], v[96:99], v[168:171], a[184:187]\n" \
"s_add_u32 m0, %[mB], 0x0\n" \
"s_mov_b32 s84, 0x0\n" \
"v_mfma_f32_16x16x32_bf16 a[216:219], v[104:107], v[168:171], a[216:219]\n" \
"buffer_load_dwordx4 v26, s[68:71], s84 offen lds\n" \
"v_mfma_f32_16x16x32_bf16 a[216:219], v[100:103], v[164:167], a[216:219]\n" \
"v_mfma_f32_16x16x32_bf16 a[248:251], v[108:111], v[164:167], a[248:251]\n" \
"ds_read_b64_tr_b16 v[164:165], v34 offset:49152\n" \
"ds_read_b64_tr_b16 v[166:167], v34 offset:50176\n" \
"v_mfma_f32_16x16x32_bf16 a[248:251], v[112:115], v[168:171], a[248:251]\n" \
"s_add_u32 m0, %[mA], 0x800\n" \
"s_mov_b32 s85, 0x4800\n" \
"v_mfma_f32_16x16x32_bf16 a[156:159], v[88:91], v[176:179], a[156:159]\n" \
"buffer_load_dwordx4 v24, s[76:79], s85 offen lds\n" \
"v_mfma_f32_16x16x32_bf16 a[156:159], v[84:87], v[172:175], a[156:159]\n" \
"ds_read_b64_tr_b16 v[168:169], v34 offset:57344\n" \
"ds_read_b64_tr_b16 v[170:171], v34 offset:58368\n" \
"v_mfma_f32_16x16x32_bf16 a[188:191], v[92:95], v[172:175], a[188:191]\n" \
"v_mfma_f32_16x16x32_bf16 a[188:191], v[96:99], v[176:179], a[188:191]\n" \
"s_add_u32 m0, %[mA], 0xc00\n" \
"s_mov_b32 s84, 0x6c00\n" \
"v_mfma_f32_16x16x32_bf16 a[220:223], v[104:107], v[176:179], a[220:223]\n" \
"buffer_load_dwordx4 v25, s[76:79], s84 offen lds\n" \
"v_mfma_f32_16x16x32_bf16 a[220:223], v[100:103], v[172:175], a[220:223]\n" \
"v_mfma_f32_16x16x32_bf16 a[252:255], v[108:111], v[172:175], a[252:255]\n" \
"ds_read_b64_tr_b16 v[172:173], v35 offset:49152\n" \
"ds_read_b64_tr_b16 v[174:175], v35 offset:50176\n" \
"v_mfma_f32_16x16x32_bf16 a[252:255], v[112:115], v[176:179], a[252:255]\n" \
"s_add_u32 m0, %[mB], 0x400\n" \
"s_mov_b32 s85, 0x8000\n" \
"v_mfma_f32_16x16x32_bf16 a[128:131], v[120:123], v[88:91], a[128:131]\n" \
"buffer_load_dwordx4 v26, s[68:71], s85 offen lds\n" \
"v_mfma_f32_16x16x32_bf16 a[128:131], v[116:119], v[84:87], a[128:131]\n" \
"ds_read_b64_tr_b16 v[176:177], v35 offset:57344\n" \
"ds_read_b64_tr_b16 v[178:179], v35 offset:58368\n" \
"v_mfma_f32_16x16x32_bf16 a[160:163], v[116:119], v[92:95], a[160:163]\n" \
"v_mfma_f32_16x16x32_bf16 a[160:163], v[120:123], v[96:99], a[160:163]\n" \
"s_add_u32 m0, %[mA], 0x1000\n" \
"s_mov_b32 s84, 0x9000\n" \
"v_mfma_f32_16x16x32_bf16 a[192:195], v[120:123], v[104:107], a[192:195]\n" \
"buffer_load_dwordx4 v24, s[76:79], s84 offen lds\n" \
"v_mfma_f32_16x16x32_bf16 a[192:195], v[116:119], v[100:103], a[192:195]\n" \
"v_mfma_f32_16x16x32_bf16 a[224:227], v[116:119], v[108:111], a[224:227]\n" \
"ds_read_b64_tr_b16 v[116:117], v32 offset:32768\n" \
"ds_read_b64_tr_b16 v[118:119], v32 offset:33792\n" \
"v_mfma_f32_16x16x32_bf16 a[224:227], v[120:123], v[112:115], a[224:227]\n" \
"s_add_u32 m0, %[mA], 0x1400\n" \
"s_mov_b32 s85, 0xb400\n" \
"v_mfma_f32_16x16x32_bf16 a[132:135], v[128:131], v[88:91], a[132:135]\n" \
"buffer_load_dwordx4 v25, s[76:79], s85 offen lds\n" \
"v_mfma_f32_16x16x32_bf16 a[132:135], v[124:127], v[84:87], a[132:135]\n" \
"ds_read_b64_tr_b16 v[120:121], v32 offset:40960\n" \
"ds_read_b64_tr_b16 v[122:123], v32 offset:41984\n" \
"v_mfma_f32_16x16x32_bf16 a[164:167], v[124:127], v[92:95], a[164:167]\n" \
"v_mfma_f32_16x16x32_bf16 a[164:167], v[128:131], v[96:99], a[164:167]\n" \
"s_add_u32 m0, %[mB], 0x800\n" \
"s_mov_b32 s84, 0x10000\n" \
"v_mfma_f32_16x16x32_bf16 a[196:199], v[128:131], v[104:107], a[196:199]\n" \
"buffer_load_dwordx4 v27, s[68:71], s84 offen lds\n" \
"v_mfma_f32_16x16x32_bf16 a[196:199], v[124:127], v[100:103], a[196:199]\n" \
"v_mfma_f32_16x16x32_bf16 a[228:231], v[124:127], v[108:111], a[228:231]\n" \
"ds_read_b64_tr_b16 v[124:125], v33 offset:32768\n" \
"ds_read_b64_tr_b16 v[126:127], v33 offset:33792\n" \
"v_mfma_f32_16x16x32_bf16 a[228:231], v[128:131], v[112:115], a[228:231]\n" \
"ds_read_b64_tr_b16 v[128:129], v33 offset:40960\n" \
"ds_read_b64_tr_b16 v[130:131], v33 offset:41984\n" \
"v_mfma_f32_16x16x32_bf16 a[136:139], v[136:139], v[88:91], a[136:139]\n" \
"v_mfma_f32_16x16x32_bf16 a[136:139], v[132:135], v[84:87], a[136:139]\n" \
"v_mfma_f32_16x16x32_bf16 a[168:171], v[132:135], v[92:95], a[168:171]\n" \
"v_mfma_f32_16x16x32_bf16 a[168:171], v[136:139], v[96:99], a[168:171]\n" \
"v_mfma_f32_16x16x32_bf16 a[200:203], v[136:139], v[104:107], a[200:203]\n" \
"v_mfma_f32_16x16x32_bf16 a[200:203], v[132:135], v[100:103], a[200:203]\n" \
"v_mfma_f32_16x16x32_bf16 a[232:235], v[132:135], v[108:111], a[232:235]\n" \
"ds_read_b64_tr_b16 v[132:133], v34 offset:32768\n" \
"ds_read_b64_tr_b16 v[134:135], v34 offset:33792\n" \
"v_mfma_f32_16x16x32_bf16 a[232:235], v[136:139], v[112:115], a[232:235]\n" \
"ds_read_b64_tr_b16 v[136:137], v34 offset:40960\n" \
"ds_read_b64_tr_b16 v[138:139], v34 offset:41984\n" \
"v_mfma_f32_16x16x32_bf16 a[140:143], v[144:147], v[88:91], a[140:143]\n" \
"ds_read_b128 v[88:91], v29 offset:32768\n" \
"v_mfma_f32_16x16x32_bf16 a[140:143], v[140:143], v[84:87], a[140:143]\n" \
"ds_read_b128 v[84:87], v28 offset:32768\n" \
"v_mfma_f32_16x16x32_bf16 a[172:175], v[140:143], v[92:95], a[172:175]\n" \
"ds_read_b128 v[92:95], v28 offset:34816\n" \
"v_mfma_f32_16x16x32_bf16 a[172:175], v[144:147], v[96:99], a[172:175]\n" \
"ds_read_b128 v[96:99], v29 offset:34816\n" \
"v_mfma_f32_16x16x32_bf16 a[204:207], v[144:147], v[104:107], a[204:207]\n" \
"ds_read_b128 v[104:107], v29 offset:36864\n" \
"v_mfma_f32_16x16x32_bf16 a[204:207], v[140:143], v[100:103], a[204:207]\n" \
"ds_read_b128 v[100:103], v28 offset:36864\n" \
"v_mfma_f32_16x16x32_bf16 a[236:239], v[140:143], v[108:111], a[236:239]\n" \
"ds_read_b64_tr_b16 v[140:141], v35 offset:32768\n" \
"ds_read_b64_tr_b16 v[142:143], v35 offset:33792\n" \
"v_mfma_f32_16x16x32_bf16 a[236:239], v[144:147], v[112:115], a[236:239]\n" \
"s_add_u32 m0, %[mA], 0x1800\n" \
"s_waitcnt lgkmcnt(12)\n" \
"s_nop 0\n" \
"v_mfma_f32_16x16x32_bf16 a[16:19], v[56:59], v[152:155], a[16:19]\n" \
"s_mov_b32 s84, 0xd800\n" \
"ds_read_b64_tr_b16 v[144:145], v35 offset:40960\n" \
"v_mfma_f32_16x16x32_bf16 a[16:19], v[52:55], v[148:151], a[16:19]\n" \
"buffer_load_dwordx4 v24, s[76:79], s84 offen lds\n" \
"v_mfma_f32_16x16x32_bf16 a[48:51], v[60:63], v[148:151], a[48:51]\n" \
"s_add_u32 m0, %[mA], 0x1c00\n" \
"s_mov_b32 s85, 0xfc00\n" \
"v_mfma_f32_16x16x32_bf16 a[48:51], v[64:67], v[152:155], a[48:51]\n" \
"ds_read_b64_tr_b16 v[146:147], v35 offset:41984\n" \
"ds_read_b128 v[108:111], v28 offset:38912\n" \
"v_mfma_f32_16x16x32_bf16 a[80:83], v[72:75], v[152:155], a[80:83]\n" \
"buffer_load_dwordx4 v25, s[76:79], s85 offen lds\n" \
"v_mfma_f32_16x16x32_bf16 a[80:83], v[68:71], v[148:151], a[80:83]\n" \
"s_add_u32 m0, %[mB], 0xc00\n" \
"s_mov_b32 s84, 0x18000\n" \
"v_mfma_f32_16x16x32_bf16 a[112:115], v[76:79], v[148:151], a[112:115]\n" \
"ds_read_b128 v[112:115], v29 offset:38912\n" \
"v_add_u32_e64 v28, s59, v28\n" \
"v_mfma_f32_16x16x32_bf16 a[112:115], v[80:83], v[152:155], a[112:115]\n" \
"buffer_load_dwordx4 v27, s[68:71], s84 offen lds\n" \
"v_mfma_f32_16x16x32_bf16 a[20:23], v[56:59], v[160:163], a[20:23]\n" \
"v_add_u32_e64 v29, s59, v29\n" \
"v_accvgpr_read_b32 v184, a16\n" \
"v_mfma_f32_16x16x32_bf16 a[20:23], v[52:55], v[156:159], a[20:23]\n" \
"v_accvgpr_read_b32 v185, a17\n" \
"v_accvgpr_read_b32 v186, a18\n" \
"v_mfma_f32_16x16x32_bf16 a[52:55], v[60:63], v[156:159], a[52:55]\n" \
"v_accvgpr_read_b32 v187, a19\n" \
"v_accvgpr_read_b32 v180, a48\n" \
"v_mfma_f32_16x16x32_bf16 a[52:55], v[64:67], v[160:163], a[52:55]\n" \
"v_accvgpr_read_b32 v181, a49\n" \
"v_accvgpr_read_b32 v182, a50\n" \
"v_mfma_f32_16x16x32_bf16 a[84:87], v[72:75], v[160:163], a[84:87]\n" \
"v_accvgpr_read_b32 v183, a51\n" \
"v_accvgpr_read_b32 v188, a80\n" \
"v_mfma_f32_16x16x32_bf16 a[84:87], v[68:71], v[156:159], a[84:87]\n" \
"v_accvgpr_read_b32 v189, a81\n" \
"v_accvgpr_read_b32 v190, a82\n" \
"v_mfma_f32_16x16x32_bf16 a[116:119], v[76:79], v[156:159], a[116:119]\n" \
"v_accvgpr_read_b32 v191, a83\n" \
"v_cvt_pk_bf16_f32 v184, v184, v185\n" \
"v_mfma_f32_16x16x32_bf16 a[116:119], v[80:83], v[160:163], a[116:119]\n" \
"v_cvt_pk_bf16_f32 v185, v186, v187\n" \
"v_cvt_pk_bf16_f32 v186, v180, v181\n" \
"v_mfma_f32_16x16x32_bf16 a[24:27], v[56:59], v[168:171], a[24:27]\n" \
"v_cvt_pk_bf16_f32 v187, v182, v183\n" \
"v_accvgpr_read_b32 v180, a112\n" \
"v_mfma_f32_16x16x32_bf16 a[24:27], v[52:55], v[164:167], a[24:27]\n" \
"v_accvgpr_read_b32 v181, a113\n" \
"v_accvgpr_read_b32 v182, a114\n" \
"v_mfma_f32_16x16x32_bf16 a[56:59], v[60:63], v[164:167], a[56:59]\n" \
"v_accvgpr_read_b32 v183, a115\n" \
"v_accvgpr_read_b32 v192, a20\n" \
"v_mfma_f32_16x16x32_bf16 a[56:59], v[64:67], v[168:171], a[56:59]\n" \
"v_accvgpr_read_b32 v193, a21\n" \
"v_accvgpr_read_b32 v194, a22\n" \
"v_mfma_f32_16x16x32_bf16 a[88:91], v[72:75], v[168:171], a[88:91]\n" \
"v_accvgpr_read_b32 v195, a23\n" \
"v_cvt_pk_bf16_f32 v188, v188, v189\n" \
"v_mfma_f32_16x16x32_bf16 a[88:91], v[68:71], v[164:167], a[88:91]\n" \
"v_cvt_pk_bf16_f32 v189, v190, v191\n" \
"v_cvt_pk_bf16_f32 v190, v180, v181\n" \
"v_mfma_f32_16x16x32_bf16 a[120:123], v[76:79], v[164:167], a[120:123]\n" \
"v_cvt_pk_bf16_f32 v191, v182, v183\n" \
"v_accvgpr_read_b32 v180, a52\n" \
"v_mfma_f32_16x16x32_bf16 a[120:123], v[80:83], v[168:171], a[120:123]\n" \
"v_accvgpr_read_b32 v181, a53\n" \
"v_accvgpr_read_b32 v182, a54\n" \
"v_mfma_f32_16x16x32_bf16 a[28:31], v[56:59], v[176:179], a[28:31]\n" \
"v_accvgpr_read_b32 v183, a55\n" \
"v_accvgpr_read_b32 v196, a84\n" \
"v_mfma_f32_16x16x32_bf16 a[28:31], v[52:55], v[172:175], a[28:31]\n" \
"v_accvgpr_read_b32 v197, a85\n" \
"v_accvgpr_read_b32 v198, a86\n" \
"v_mfma_f32_16x16x32_bf16 a[60:63], v[60:63], v[172:175], a[60:63]\n" \
"v_accvgpr_read_b32 v199, a87\n" \
"v_cvt_pk_bf16_f32 v192, v192, v193\n" \
"v_mfma_f32_16x16x32_bf16 a[60:63], v[64:67], v[176:179], a[60:63]\n" \
"v_cvt_pk_bf16_f32 v193, v194, v195\n" \
"v_cvt_pk_bf16_f32 v194, v180, v181\n" \
"v_mfma_f32_16x16x32_bf16 a[92:95], v[72:75], v[176:179], a[92:95]\n" \
"v_cvt_pk_bf16_f32 v195, v182, v183\n" \
"v_accvgpr_read_b32 v180, a116\n" \
"v_mfma_f32_16x16x32_bf16 a[92:95], v[68:71], v[172:175], a[92:95]\n" \
"v_accvgpr_read_b32 v181, a117\n" \
"v_accvgpr_read_b32 v182, a118\n" \
"v_mfma_f32_16x16x32_bf16 a[124:127], v[76:79], v[172:175], a[124:127]\n" \
"v_accvgpr_read_b32 v183, a119\n" \
"v_accvgpr_read_b32 v200, a24\n" \
"v_mfma_f32_16x16x32_bf16 a[124:127], v[80:83], v[176:179], a[124:127]\n" \
"v_accvgpr_read_b32 v201, a25\n" \
"v_accvgpr_read_b32 v202, a26\n" \
"v_mfma_f32_16x16x32_bf16 a[0:3], v[120:123], v[56:59], a[0:3]\n" \
"v_accvgpr_read_b32 v203, a27\n" \
"v_cvt_pk_bf16_f32 v196, v196, v197\n" \
"v_mfma_f32_16x16x32_bf16 a[0:3], v[116:119], v[52:55], a[0:3]\n" \
"v_cvt_pk_bf16_f32 v197, v198, v199\n" \
"v_cvt_pk_bf16_f32 v198, v180, v181\n" \
"v_mfma_f32_16x16x32_bf16 a[32:35], v[116:119], v[60:63], a[32:35]\n" \
"v_cvt_pk_bf16_f32 v199, v182, v183\n" \
"v_accvgpr_read_b32 v180, a56\n" \
"v_mfma_f32_16x16x32_bf16 a[32:35], v[120:123], v[64:67], a[32:35]\n" \
"v_accvgpr_read_b32 v181, a57\n" \
"v_accvgpr_read_b32 v182, a58\n" \
"v_mfma_f32_16x16x32_bf16 a[64:67], v[120:123], v[72:75], a[64:67]\n" \
"v_accvgpr_read_b32 v183, a59\n" \
"v_accvgpr_read_b32 v204, a88\n" \
"v_mfma_f32_16x16x32_bf16 a[64:67], v[116:119], v[68:71], a[64:67]\n" \
"v_accvgpr_read_b32 v205, a89\n" \
"v_accvgpr_read_b32 v206, a90\n" \
"v_mfma_f32_16x16x32_bf16 a[96:99], v[116:119], v[76:79], a[96:99]\n" \
"v_accvgpr_read_b32 v207, a91\n" \
"v_cvt_pk_bf16_f32 v200, v200, v201\n" \
"v_mfma_f32_16x16x32_bf16 a[96:99], v[120:123], v[80:83], a[96:99]\n" \
"v_cvt_pk_bf16_f32 v201, v202, v203\n" \
"v_cvt_pk_bf16_f32 v202, v180, v181\n" \
"v_mfma_f32_16x16x32_bf16 a[4:7], v[128:131], v[56:59], a[4:7]\n" \
"v_cvt_pk_bf16_f32 v203, v182, v183\n" \
"v_accvgpr_read_b32 v180, a120\n" \
"v_mfma_f32_16x16x32_bf16 a[4:7], v[124:127], v[52:55], a[4:7]\n" \
"v_accvgpr_read_b32 v181, a121\n" \
"v_accvgpr_read_b32 v182, a122\n" \
"v_mfma_f32_16x16x32_bf16 a[36:39], v[124:127], v[60:63], a[36:39]\n" \
"v_accvgpr_read_b32 v183, a123\n" \
"v_accvgpr_read_b32 v208, a28\n" \
"v_mfma_f32_16x16x32_bf16 a[36:39], v[128:131], v[64:67], a[36:39]\n" \
"v_accvgpr_read_b32 v209, a29\n" \
"v_accvgpr_read_b32 v210, a30\n" \
"v_mfma_f32_16x16x32_bf16 a[68:71], v[128:131], v[72:75], a[68:71]\n" \
"v_accvgpr_read_b32 v211, a31\n" \
"v_cvt_pk_bf16_f32 v204, v204, v205\n" \
"v_mfma_f32_16x16x32_bf16 a[68:71], v[124:127], v[68:71], a[68:71]\n" \
"v_cvt_pk_bf16_f32 v205, v206, v207\n" \
"v_cvt_pk_bf16_f32 v206, v180, v181\n" \
"v_mfma_f32_16x16x32_bf16 a[100:103], v[124:127], v[76:79], a[100:103]\n" \
"v_cvt_pk_bf16_f32 v207, v182, v183\n" \
"v_accvgpr_read_b32 v180, a60\n" \
"v_mfma_f32_16x16x32_bf16 a[100:103], v[128:131], v[80:83], a[100:103]\n" \
"v_accvgpr_read_b32 v181, a61\n" \
"s_waitcnt lgkmcnt(0)\n" \
"s_nop 0\n" \
"v_mfma_f32_16x16x32_bf16 a[8:11], v[136:139], v[56:59], a[8:11]\n" \
"v_accvgpr_read_b32 v182, a62\n" \
"v_accvgpr_read_b32 v183, a63\n" \
"v_mfma_f32_16x16x32_bf16 a[8:11], v[132:135], v[52:55], a[8:11]\n" \
"v_accvgpr_read_b32 v212, a92\n" \
"v_accvgpr_read_b32 v213, a93\n" \
"v_mfma_f32_16x16x32_bf16 a[40:43], v[132:135], v[60:63], a[40:43]\n" \
"v_accvgpr_read_b32 v214, a94\n" \
"v_accvgpr_read_b32 v215, a95\n" \
"v_mfma_f32_16x16x32_bf16 a[40:43], v[136:139], v[64:67], a[40:43]\n" \
"v_cvt_pk_bf16_f32 v208, v208, v209\n" \
"v_cvt_pk_bf16_f32 v209, v210, v211\n" \
"v_mfma_f32_16x16x32_bf16 a[72:75], v[136:139], v[72:75], a[72:75]\n" \
"v_cvt_pk_bf16_f32 v210, v180, v181\n" \
"v_cvt_pk_bf16_f32 v211, v182, v183\n" \
"v_mfma_f32_16x16x32_bf16 a[72:75], v[132:135], v[68:71], a[72:75]\n" \
"v_accvgpr_read_b32 v180, a124\n" \
"v_accvgpr_read_b32 v181, a125\n" \
"v_mfma_f32_16x16x32_bf16 a[104:107], v[132:135], v[76:79], a[104:107]\n" \
"v_accvgpr_read_b32 v182, a126\n" \
"v_accvgpr_read_b32 v183, a127\n" \
"v_mfma_f32_16x16x32_bf16 a[104:107], v[136:139], v[80:83], a[104:107]\n" \
"v_accvgpr_read_b32 v216, a0\n" \
"v_accvgpr_read_b32 v217, a1\n" \
"v_mfma_f32_16x16x32_bf16 a[12:15], v[144:147], v[56:59], a[12:15]\n" \
"v_accvgpr_read_b32 v218, a2\n" \
"v_accvgpr_read_b32 v219, a3\n" \
"v_mfma_f32_16x16x32_bf16 a[12:15], v[140:143], v[52:55], a[12:15]\n" \
"v_cvt_pk_bf16_f32 v212, v212, v213\n" \
"v_cvt_pk_bf16_f32 v213, v214, v215\n" \
"v_mfma_f32_16x16x32_bf16 a[44:47], v[140:143], v[60:63], a[44:47]\n" \
"v_cvt_pk_bf16_f32 v214, v180, v181\n" \
"v_cvt_pk_bf16_f32 v215, v182, v183\n" \
"v_mfma_f32_16x16x32_bf16 a[44:47], v[144:147], v[64:67], a[44:47]\n" \
"v_accvgpr_read_b32 v180, a4\n" \
"v_accvgpr_read_b32 v181, a5\n" \
"v_mfma_f32_16x16x32_bf16 a[76:79], v[144:147], v[72:75], a[76:79]\n" \
"v_accvgpr_read_b32 v182, a6\n" \
"v_accvgpr_read_b32 v183, a7\n" \
"v_mfma_f32_16x16x32_bf16 a[76:79], v[140:143], v[68:71], a[76:79]\n" \
"v_cvt_pk_bf16_f32 v216, v216, v217\n" \
"v_cvt_pk_bf16_f32 v217, v218, v219\n" \
"v_mfma_f32_16x16x32_bf16 a[108:111], v[140:143], v[76:79], a[108:111]\n" \
"v_accvgpr_read_b32 v220, a32\n" \
"v_accvgpr_read_b32 v221, a33\n" \
"v_mfma_f32_16x16x32_bf16 a[108:111], v[144:147], v[80:83], a[108:111]\n" \
"v_cvt_pk_bf16_f32 v218, v180, v181\n" \
"v_cvt_pk_bf16_f32 v219, v182, v183\n" \
"s_waitcnt vmcnt(0)\n" \
"s_barrier\n" \
"v_mfma_f32_16x16x32_bf16 a[128:131], v[120:123], v[88:91], a[128:131]\n" \
"s_add_u32 m0, s40, 0x8000\n" \
"s_add_u32 s85, s37, 0x2000\n" \
"v_mfma_f32_16x16x32_bf16 a[128:131], v[116:119], v[84:87], a[128:131]\n" \
"buffer_load_dwordx4 v20, s[80:83], s85 offen sc1 lds\n" \
"v_mfma_f32_16x16x32_bf16 a[160:163], v[116:119], v[92:95], a[160:163]\n" \
"ds_read_b128 v[52:55], v28 offset:0\n" \
"ds_read_b128 v[56:59], v29 offset:0\n" \
"v_mfma_f32_16x16x32_bf16 a[160:163], v[120:123], v[96:99], a[160:163]\n" \
"s_add_u32 m0, s40, 0x9000\n" \
"s_add_u32 s84, s37, 0x3000\n" \
"v_mfma_f32_16x16x32_bf16 a[192:195], v[120:123], v[104:107], a[192:195]\n" \
"buffer_load_dwordx4 v20, s[80:83], s84 offen sc1 lds\n" \
"v_mfma_f32_16x16x32_bf16 a[192:195], v[116:119], v[100:103], a[192:195]\n" \
"ds_read_b128 v[60:63], v28 offset:2048\n" \
"ds_read_b128 v[64:67], v29 offset:2048\n" \
"v_mfma_f32_16x16x32_bf16 a[224:227], v[116:119], v[108:111], a[224:227]\n" \
"ds_read_b64_tr_b16 v[116:117], v36 offset:0\n" \
"ds_read_b64_tr_b16 v[118:119], v36 offset:512\n" \
"v_mfma_f32_16x16x32_bf16 a[224:227], v[120:123], v[112:115], a[224:227]\n" \
"s_add_u32 m0, s40, 0xb000\n" \
"s_mov_b32 s85, 0x80\n" \
"v_mfma_f32_16x16x32_bf16 a[132:135], v[128:131], v[88:91], a[132:135]\n" \
"buffer_load_dwordx4 v21, s[76:79], s85 offen lds\n" \
"v_mfma_f32_16x16x32_bf16 a[132:135], v[124:127], v[84:87], a[132:135]\n" \
"ds_read_b64_tr_b16 v[120:121], v36 offset:4096\n" \
"ds_read_b64_tr_b16 v[122:123], v36 offset:4608\n" \
"v_mfma_f32_16x16x32_bf16 a[164:167], v[124:127], v[92:95], a[164:167]\n" \
"ds_read_b128 v[68:71], v28 offset:4096\n" \
"ds_read_b128 v[72:75], v29 offset:4096\n" \
"v_mfma_f32_16x16x32_bf16 a[164:167], v[128:131], v[96:99], a[164:167]\n" \
"s_add_u32 m0, %[mA], 0x8000\n" \
"s_mov_b32 s84, 0x80\n" \
"v_mfma_f32_16x16x32_bf16 a[196:199], v[128:131], v[104:107], a[196:199]\n" \
"buffer_load_dwordx4 v24, s[76:79], s84 offen lds\n" \
"v_mfma_f32_16x16x32_bf16 a[196:199], v[124:127], v[100:103], a[196:199]\n" \
"ds_read_b128 v[76:79], v28 offset:6144\n" \
"L_pf4:\n" \
"s_cbranch_execz L_pf8\n" \
"s_nop 0\n" \
"ds_read_b128 v[80:83], v29 offset:6144\n" \
"v_mfma_f32_16x16x32_bf16 a[228:231], v[124:127], v[108:111], a[228:231]\n" \
"ds_read_b64_tr_b16 v[124:125], v37 offset:0\n" \
"ds_read_b64_tr_b16 v[126:127], v37 offset:512\n" \
"v_mfma_f32_16x16x32_bf16 a[228:231], v[128:131], v[112:115], a[228:231]\n" \
"s_add_u32 m0, %[mA], 0x8400\n" \
"s_mov_b32 s85, 0x2480\n" \
"v_mfma_f32_16x16x32_bf16 a[136:139], v[136:139], v[88:91], a[136:139]\n" \
"buffer_load_dwordx4 v25, s[76:79], s85 offen lds\n" \
"v_mfma_f32_16x16x32_bf16 a[136:139], v[132:135], v[84:87], a[136:139]\n" \
"ds_read_b64_tr_b16 v[128:129], v37 offset:4096\n" \
"ds_read_b64_tr_b16 v[130:131], v37 offset:4608\n" \
"v_mfma_f32_16x16x32_bf16 a[168:171], v[132:135], v[92:95], a[168:171]\n" \
"v_accvgpr_read_b32 v222, a34\n" \
"v_accvgpr_read_b32 v223, a35\n" \
"v_mfma_f32_16x16x32_bf16 a[168:171], v[136:139], v[96:99], a[168:171]\n" \
"s_add_u32 m0, %[mB], 0x8000\n" \
"s_mov_b32 s84, 0x80000\n" \
"v_mfma_f32_16x16x32_bf16 a[200:203], v[136:139], v[104:107], a[200:203]\n" \
"buffer_load_dwordx4 v26, s[68:71], s84 offen lds\n" \
"v_mfma_f32_16x16x32_bf16 a[200:203], v[132:135], v[100:103], a[200:203]\n" \
"v_cvt_pk_bf16_f32 v220, v220, v221\n" \
"v_accvgpr_read_b32 v180, a36\n" \
"v_mfma_f32_16x16x32_bf16 a[232:235], v[132:135], v[108:111], a[232:235]\n" \
"ds_read_b64_tr_b16 v[132:133], v38 offset:0\n" \
"ds_read_b64_tr_b16 v[134:135], v38 offset:512\n" \
"v_mfma_f32_16x16x32_bf16 a[232:235], v[136:139], v[112:115], a[232:235]\n" \
"s_add_u32 m0, %[mA], 0x8800\n" \
"s_mov_b32 s85, 0x4880\n" \
"v_mfma_f32_16x16x32_bf16 a[140:143], v[144:147], v[88:91], a[140:143]\n" \
"buffer_load_dwordx4 v24, s[76:79], s85 offen lds\n" \
"v_mfma_f32_16x16x32_bf16 a[140:143], v[140:143], v[84:87], a[140:143]\n" \
"ds_read_b64_tr_b16 v[136:137], v38 offset:4096\n" \
"ds_read_b64_tr_b16 v[138:139], v38 offset:4608\n" \
"v_mfma_f32_16x16x32_bf16 a[172:175], v[140:143], v[92:95], a[172:175]\n" \
"v_accvgpr_read_b32 v181, a37\n" \
"v_accvgpr_read_b32 v182, a38\n" \
"v_mfma_f32_16x16x32_bf16 a[172:175], v[144:147], v[96:99], a[172:175]\n" \
"s_add_u32 m0, %[mA], 0x8c00\n" \
"s_mov_b32 s84, 0x6c80\n" \
"v_mfma_f32_16x16x32_bf16 a[204:207], v[144:147], v[104:107], a[204:207]\n" \
"buffer_load_dwordx4 v25, s[76:79], s84 offen lds\n" \
"v_mfma_f32_16x16x32_bf16 a[204:207], v[140:143], v[100:103], a[204:207]\n" \
"v_accvgpr_read_b32 v183, a39\n" \
"v_cvt_pk_bf16_f32 v221, v222, v223\n" \
"v_mfma_f32_16x16x32_bf16 a[236:239], v[140:143], v[108:111], a[236:239]\n" \
"ds_read_b64_tr_b16 v[140:141], v39 offset:0\n" \
"ds_read_b64_tr_b16 v[142:143], v39 offset:512\n" \
"v_mfma_f32_16x16x32_bf16 a[236:239], v[144:147], v[112:115], a[236:239]\n" \
"s_add_u32 m0, %[mB], 0x8400\n" \
"s_mov_b32 s85, 0x88000\n" \
"v_mfma_f32_16x16x32_bf16 a[144:147], v[88:91], v[152:155], a[144:147]\n" \
"buffer_load_dwordx4 v26, s[68:71], s85 offen lds\n" \
"v_mfma_f32_16x16x32_bf16 a[144:147], v[84:87], v[148:151], a[144:147]\n" \
"ds_read_b64_tr_b16 v[144:145], v39 offset:4096\n" \
"ds_read_b64_tr_b16 v[146:147], v39 offset:4608\n" \
"v_mfma_f32_16x16x32_bf16 a[148:151], v[84:87], v[156:159], a[148:151]\n" \
"v_accvgpr_read_b32 v224, a64\n" \
"v_accvgpr_read_b32 v225, a65\n" \
"v_mfma_f32_16x16x32_bf16 a[148:151], v[88:91], v[160:163], a[148:151]\n" \
"s_add_u32 m0, %[mA], 0x9000\n" \
"s_mov_b32 s84, 0x9080\n" \
"v_mfma_f32_16x16x32_bf16 a[152:155], v[88:91], v[168:171], a[152:155]\n" \
"buffer_load_dwordx4 v24, s[76:79], s84 offen lds\n" \
"v_mfma_f32_16x16x32_bf16 a[152:155], v[84:87], v[164:167], a[152:155]\n" \
"v_cvt_pk_bf16_f32 v222, v180, v181\n" \
"v_cvt_pk_bf16_f32 v223, v182, v183\n" \
"v_mfma_f32_16x16x32_bf16 a[156:159], v[84:87], v[172:175], a[156:159]\n" \
"ds_read_b128 v[84:87], v22 offset:0\n" \
"v_accvgpr_read_b32 v226, a66\n" \
"v_mfma_f32_16x16x32_bf16 a[156:159], v[88:91], v[176:179], a[156:159]\n" \
"s_add_u32 m0, %[mA], 0x9400\n" \
"s_mov_b32 s85, 0xb480\n" \
"v_mfma_f32_16x16x32_bf16 a[176:179], v[96:99], v[152:155], a[176:179]\n" \
"buffer_load_dwordx4 v25, s[76:79], s85 offen lds\n" \
"v_mfma_f32_16x16x32_bf16 a[176:179], v[92:95], v[148:151], a[176:179]\n" \
"ds_read_b128 v[88:91], v23 offset:0\n" \
"v_accvgpr_read_b32 v227, a67\n" \
"v_mfma_f32_16x16x32_bf16 a[180:183], v[92:95], v[156:159], a[180:183]\n" \
"v_cvt_pk_bf16_f32 v224, v224, v225\n" \
"v_accvgpr_read_b32 v180, a68\n" \
"v_mfma_f32_16x16x32_bf16 a[180:183], v[96:99], v[160:163], a[180:183]\n" \
"s_add_u32 m0, %[mB], 0x8800\n" \
"s_mov_b32 s84, 0x90000\n" \
"v_mfma_f32_16x16x32_bf16 a[184:187], v[96:99], v[168:171], a[184:187]\n" \
"buffer_load_dwordx4 v27, s[68:71], s84 offen lds\n" \
"v_mfma_f32_16x16x32_bf16 a[184:187], v[92:95], v[164:167], a[184:187]\n" \
"v_accvgpr_read_b32 v181, a69\n" \
"v_accvgpr_read_b32 v182, a70\n" \
"v_mfma_f32_16x16x32_bf16 a[188:191], v[92:95], v[172:175], a[188:191]\n" \
"v_accvgpr_read_b32 v183, a71\n" \
"v_cvt_pk_bf16_f32 v225, v226, v227\n" \
"v_mfma_f32_16x16x32_bf16 a[188:191], v[96:99], v[176:179], a[188:191]\n" \
"v_accvgpr_read_b32 v228, a96\n" \
"v_accvgpr_read_b32 v229, a97\n" \
"v_mfma_f32_16x16x32_bf16 a[208:211], v[104:107], v[152:155], a[208:211]\n" \
"v_cvt_pk_bf16_f32 v226, v180, v181\n" \
"v_cvt_pk_bf16_f32 v227, v182, v183\n" \
"v_mfma_f32_16x16x32_bf16 a[208:211], v[100:103], v[148:151], a[208:211]\n" \
"v_accvgpr_read_b32 v230, a98\n" \
"v_accvgpr_read_b32 v231, a99\n" \
"v_mfma_f32_16x16x32_bf16 a[212:215], v[100:103], v[156:159], a[212:215]\n" \
"v_cvt_pk_bf16_f32 v228, v228, v229\n" \
"v_accvgpr_read_b32 v180, a100\n" \
"v_mfma_f32_16x16x32_bf16 a[212:215], v[104:107], v[160:163], a[212:215]\n" \
"v_accvgpr_read_b32 v181, a101\n" \
"v_accvgpr_read_b32 v182, a102\n" \
"v_mfma_f32_16x16x32_bf16 a[216:219], v[104:107], v[168:171], a[216:219]\n" \
"v_accvgpr_read_b32 v183, a103\n" \
"v_cvt_pk_bf16_f32 v229, v230, v231\n" \
"v_mfma_f32_16x16x32_bf16 a[216:219], v[100:103], v[164:167], a[216:219]\n" \
"v_accvgpr_read_b32 v232, a8\n" \
"v_accvgpr_read_b32 v233, a9\n" \
"v_mfma_f32_16x16x32_bf16 a[220:223], v[100:103], v[172:175], a[220:223]\n" \
"v_cvt_pk_bf16_f32 v230, v180, v181\n" \
"v_cvt_pk_bf16_f32 v231, v182, v183\n" \
"v_mfma_f32_16x16x32_bf16 a[220:223], v[104:107], v[176:179], a[220:223]\n" \
"v_accvgpr_read_b32 v234, a10\n" \
"v_accvgpr_read_b32 v235, a11\n" \
"v_mfma_f32_16x16x32_bf16 a[240:243], v[112:115], v[152:155], a[240:243]\n" \
"ds_read_b64_tr_b16 v[152:153], v32 offset:8192\n" \
"ds_read_b64_tr_b16 v[154:155], v32 offset:9216\n" \
"v_mfma_f32_16x16x32_bf16 a[240:243], v[108:111], v[148:151], a[240:243]\n" \
"ds_read_b64_tr_b16 v[148:149], v32 offset:0\n" \
"ds_read_b64_tr_b16 v[150:151], v32 offset:1024\n" \
"v_mfma_f32_16x16x32_bf16 a[244:247], v[108:111], v[156:159], a[244:247]\n" \
"ds_read_b64_tr_b16 v[156:157], v33 offset:0\n" \
"ds_read_b64_tr_b16 v[158:159], v33 offset:1024\n" \
"v_mfma_f32_16x16x32_bf16 a[244:247], v[112:115], v[160:163], a[244:247]\n" \
"ds_read_b64_tr_b16 v[160:161], v33 offset:8192\n" \
"ds_read_b64_tr_b16 v[162:163], v33 offset:9216\n" \
"v_mfma_f32_16x16x32_bf16 a[248:251], v[112:115], v[168:171], a[248:251]\n" \
"ds_read_b64_tr_b16 v[168:169], v34 offset:8192\n" \
"ds_read_b64_tr_b16 v[170:171], v34 offset:9216\n" \
"v_mfma_f32_16x16x32_bf16 a[248:251], v[108:111], v[164:167], a[248:251]\n" \
"ds_read_b64_tr_b16 v[164:165], v34 offset:0\n" \
"ds_read_b64_tr_b16 v[166:167], v34 offset:1024\n" \
"v_mfma_f32_16x16x32_bf16 a[252:255], v[108:111], v[172:175], a[252:255]\n" \
"ds_read_b64_tr_b16 v[172:173], v35 offset:0\n" \
"ds_read_b64_tr_b16 v[174:175], v35 offset:1024\n" \
"v_mfma_f32_16x16x32_bf16 a[252:255], v[112:115], v[176:179], a[252:255]\n" \
"s_add_u32 m0, %[mA], 0x9800\n" \
"s_waitcnt lgkmcnt(14)\n" \
"s_nop 0\n" \
"v_mfma_f32_16x16x32_bf16 a[16:19], v[144:147], v[88:91], 0\n" \
"s_mov_b32 s84, 0xd880\n" \
"ds_read_b64_tr_b16 v[176:177], v35 offset:8192\n" \
"v_mfma_f32_16x16x32_bf16 a[16:19], v[140:143], v[84:87], a[16:19]\n" \
"buffer_load_dwordx4 v24, s[76:79], s84 offen lds\n" \
"v_mfma_f32_16x16x32_bf16 a[48:51], v[124:127], v[84:87], 0\n" \
"s_add_u32 m0, %[mA], 0x9c00\n" \
"s_mov_b32 s85, 0xfc80\n" \
"v_mfma_f32_16x16x32_bf16 a[48:51], v[128:131], v[88:91], a[48:51]\n" \
"ds_read_b64_tr_b16 v[178:179], v35 offset:9216\n" \
"v_cvt_pk_bf16_f32 v232, v232, v233\n" \
"v_mfma_f32_16x16x32_bf16 a[80:83], v[88:91], v[136:139], 0\n" \
"buffer_load_dwordx4 v25, s[76:79], s85 offen lds\n" \
"v_mfma_f32_16x16x32_bf16 a[80:83], v[84:87], v[132:135], a[80:83]\n" \
"s_add_u32 m0, %[mB], 0x8c00\n" \
"s_mov_b32 s84, 0x98000\n" \
"v_mfma_f32_16x16x32_bf16 a[112:115], v[140:143], v[52:55], 0\n" \
"v_accvgpr_read_b32 v180, a12\n" \
"v_accvgpr_read_b32 v181, a13\n" \
"v_mfma_f32_16x16x32_bf16 a[112:115], v[144:147], v[56:59], a[112:115]\n" \
"buffer_load_dwordx4 v27, s[68:71], s84 offen lds\n" \
"v_mfma_f32_16x16x32_bf16 a[20:23], v[128:131], v[56:59], 0\n" \
"v_accvgpr_read_b32 v182, a14\n" \
"v_accvgpr_read_b32 v183, a15\n" \
"v_mfma_f32_16x16x32_bf16 a[20:23], v[124:127], v[52:55], a[20:23]\n" \
"s_add_u32 s88, s44, 0x0\n" \
"s_nop 0\n" \
"v_cvt_pk_bf16_f32 v233, v234, v235\n" \
"v_mfma_f32_16x16x32_bf16 a[52:55], v[116:119], v[52:55], 0\n" \
"buffer_store_dwordx4 v[184:187], v20, s[80:83], s88 offen offset:0 sc1\n" \
"v_mfma_f32_16x16x32_bf16 a[52:55], v[120:123], v[56:59], a[52:55]\n" \
"v_accvgpr_read_b32 v236, a40\n" \
"v_accvgpr_read_b32 v237, a41\n" \
"v_mfma_f32_16x16x32_bf16 a[84:87], v[56:59], v[136:139], 0\n" \
"v_cvt_pk_bf16_f32 v234, v180, v181\n" \
"v_cvt_pk_bf16_f32 v235, v182, v183\n" \
"v_mfma_f32_16x16x32_bf16 a[84:87], v[52:55], v[132:135], a[84:87]\n" \
"v_accvgpr_read_b32 v238, a42\n" \
"v_accvgpr_read_b32 v239, a43\n" \
"v_mfma_f32_16x16x32_bf16 a[116:119], v[140:143], v[60:63], 0\n" \
"v_cvt_pk_bf16_f32 v236, v236, v237\n" \
"v_accvgpr_read_b32 v180, a44\n" \
"v_mfma_f32_16x16x32_bf16 a[116:119], v[144:147], v[64:67], a[116:119]\n" \
"v_accvgpr_read_b32 v181, a45\n" \
"v_accvgpr_read_b32 v182, a46\n" \
"v_mfma_f32_16x16x32_bf16 a[24:27], v[128:131], v[64:67], 0\n" \
"v_accvgpr_read_b32 v183, a47\n" \
"v_cvt_pk_bf16_f32 v237, v238, v239\n" \
"v_mfma_f32_16x16x32_bf16 a[24:27], v[124:127], v[60:63], a[24:27]\n" \
"v_accvgpr_read_b32 v240, a72\n" \
"v_accvgpr_read_b32 v241, a73\n" \
"v_mfma_f32_16x16x32_bf16 a[56:59], v[116:119], v[60:63], 0\n" \
"v_cvt_pk_bf16_f32 v238, v180, v181\n" \
"v_cvt_pk_bf16_f32 v239, v182, v183\n" \
"v_mfma_f32_16x16x32_bf16 a[56:59], v[120:123], v[64:67], a[56:59]\n" \
"s_add_u32 s89, s44, 0x1000\n" \
"v_accvgpr_read_b32 v242, a74\n" \
"v_mfma_f32_16x16x32_bf16 a[88:91], v[64:67], v[136:139], 0\n" \
"buffer_store_dwordx4 v[188:191], v20, s[80:83], s89 offen offset:0 sc1\n" \
"v_mfma_f32_16x16x32_bf16 a[88:91], v[60:63], v[132:135], a[88:91]\n" \
"v_accvgpr_read_b32 v243, a75\n" \
"v_cvt_pk_bf16_f32 v240, v240, v241\n" \
"v_mfma_f32_16x16x32_bf16 a[120:123], v[140:143], v[68:71], 0\n" \
"v_accvgpr_read_b32 v180, a76\n" \
"v_accvgpr_read_b32 v181, a77\n" \
"v_mfma_f32_16x16x32_bf16 a[120:123], v[144:147], v[72:75], a[120:123]\n" \
"v_accvgpr_read_b32 v182, a78\n" \
"v_accvgpr_read_b32 v183, a79\n" \
"v_mfma_f32_16x16x32_bf16 a[28:31], v[128:131], v[72:75], 0\n" \
"v_cvt_pk_bf16_f32 v241, v242, v243\n" \
"v_accvgpr_read_b32 v244, a104\n" \
"v_mfma_f32_16x16x32_bf16 a[28:31], v[124:127], v[68:71], a[28:31]\n" \
"v_accvgpr_read_b32 v245, a105\n" \
"v_cvt_pk_bf16_f32 v242, v180, v181\n" \
"v_mfma_f32_16x16x32_bf16 a[60:63], v[116:119], v[68:71], 0\n" \
"v_cvt_pk_bf16_f32 v243, v182, v183\n" \
"v_accvgpr_read_b32 v246, a106\n" \
"v_mfma_f32_16x16x32_bf16 a[60:63], v[120:123], v[72:75], a[60:63]\n" \
"v_accvgpr_read_b32 v247, a107\n" \
"v_cvt_pk_bf16_f32 v244, v244, v245\n" \
"v_mfma_f32_16x16x32_bf16 a[92:95], v[72:75], v[136:139], 0\n" \
"v_accvgpr_read_b32 v248, a128\n" \
"v_accvgpr_read_b32 v249, a129\n" \
"v_mfma_f32_16x16x32_bf16 a[92:95], v[68:71], v[132:135], a[92:95]\n" \
"v_accvgpr_read_b32 v250, a130\n" \
"v_accvgpr_read_b32 v251, a131\n" \
"v_mfma_f32_16x16x32_bf16 a[124:127], v[140:143], v[76:79], 0\n" \
"v_accvgpr_read_b32 v180, a108\n" \
"v_accvgpr_read_b32 v181, a109\n" \
"v_mfma_f32_16x16x32_bf16 a[124:127], v[144:147], v[80:83], a[124:127]\n" \
"v_accvgpr_read_b32 v182, a110\n" \
"v_accvgpr_read_b32 v183, a111\n" \
"v_mfma_f32_16x16x32_bf16 a[0:3], v[128:131], v[80:83], 0\n" \
"v_cvt_pk_bf16_f32 v245, v246, v247\n" \
"v_cvt_pk_bf16_f32 v246, v180, v181\n" \
"v_mfma_f32_16x16x32_bf16 a[0:3], v[124:127], v[76:79], a[0:3]\n" \
"v_cvt_pk_bf16_f32 v247, v182, v183\n" \
"v_cvt_pk_bf16_f32 v248, v248, v249\n" \
"v_mfma_f32_16x16x32_bf16 a[4:7], v[116:119], v[76:79], 0\n" \
"v_accvgpr_read_b32 v180, a132\n" \
"v_accvgpr_read_b32 v181, a133\n" \
"v_mfma_f32_16x16x32_bf16 a[4:7], v[120:123], v[80:83], a[4:7]\n" \
"v_accvgpr_read_b32 v182, a134\n" \
"v_accvgpr_read_b32 v183, a135\n" \
"v_mfma_f32_16x16x32_bf16 a[32:35], v[80:83], v[136:139], 0\n" \
"v_cvt_pk_bf16_f32 v249, v250, v251\n" \
"v_accvgpr_read_b32 v252, a160\n" \
"v_mfma_f32_16x16x32_bf16 a[32:35], v[76:79], v[132:135], a[32:35]\n" \
"v_accvgpr_read_b32 v253, a161\n" \
"s_waitcnt lgkmcnt(0)\n" \
"s_nop 0\n" \
"v_mfma_f32_16x16x32_bf16 a[128:131], v[148:151], v[52:55], 0\n" \
"v_cvt_pk_bf16_f32 v250, v180, v181\n" \
"v_cvt_pk_bf16_f32 v251, v182, v183\n" \
"v_mfma_f32_16x16x32_bf16 a[128:131], v[152:155], v[56:59], a[128:131]\n" \
"v_accvgpr_read_b32 v254, a162\n" \
"v_accvgpr_read_b32 v255, a163\n" \
"v_cvt_pk_bf16_f32 v252, v252, v253\n" \
"v_accvgpr_read_b32 v180, a164\n" \
"v_accvgpr_read_b32 v181, a165\n" \
"v_accvgpr_read_b32 v182, a166\n" \
"v_accvgpr_read_b32 v183, a167\n" \
"v_cvt_pk_bf16_f32 v253, v254, v255\n" \
"v_cvt_pk_bf16_f32 v254, v180, v181\n" \
"v_cvt_pk_bf16_f32 v255, v182, v183\n" \
"v_accvgpr_write_b32 a36, v252\n" \
"v_accvgpr_write_b32 a37, v253\n" \
"v_mfma_f32_16x16x32_bf16 a[132:135], v[160:163], v[56:59], 0\n" \
"v_accvgpr_write_b32 a38, v254\n" \
"v_accvgpr_write_b32 a39, v255\n" \
"v_accvgpr_read_b32 v252, a192\n" \
"v_accvgpr_read_b32 v253, a193\n" \
"v_accvgpr_read_b32 v254, a194\n" \
"v_accvgpr_read_b32 v255, a195\n" \
"v_cvt_pk_bf16_f32 v252, v252, v253\n" \
"s_add_u32 s88, s44, 0x0\n" \
"s_nop 0\n" \
"v_accvgpr_read_b32 v180, a196\n" \
"v_accvgpr_read_b32 v181, a197\n" \
"v_accvgpr_read_b32 v182, a198\n" \
"v_accvgpr_read_b32 v183, a199\n" \
"v_cvt_pk_bf16_f32 v253, v254, v255\n" \
"buffer_store_dwordx4 v[192:195], v20, s[80:83], s88 offen offset:1024 sc1\n" \
"v_cvt_pk_bf16_f32 v254, v180, v181\n" \
"v_cvt_pk_bf16_f32 v255, v182, v183\n" \
"v_accvgpr_write_b32 a64, v252\n" \
"v_accvgpr_write_b32 a65, v253\n" \
"v_mfma_f32_16x16x32_bf16 a[132:135], v[156:159], v[52:55], a[132:135]\n" \
"v_accvgpr_write_b32 a66, v254\n" \
"v_accvgpr_write_b32 a67, v255\n" \
"v_accvgpr_read_b32 v252, a224\n" \
"v_accvgpr_read_b32 v253, a225\n" \
"v_accvgpr_read_b32 v254, a226\n" \
"v_accvgpr_read_b32 v255, a227\n" \
"v_cvt_pk_bf16_f32 v252, v252, v253\n" \
"v_accvgpr_read_b32 v180, a228\n" \
"v_accvgpr_read_b32 v181, a229\n" \
"v_accvgpr_read_b32 v182, a230\n" \
"v_accvgpr_read_b32 v183, a231\n" \
"v_cvt_pk_bf16_f32 v253, v254, v255\n" \
"v_cvt_pk_bf16_f32 v254, v180, v181\n" \
"v_cvt_pk_bf16_f32 v255, v182, v183\n" \
"v_accvgpr_write_b32 a68, v252\n" \
"v_accvgpr_write_b32 a69, v253\n" \
"v_accvgpr_write_b32 a70, v254\n" \
"v_accvgpr_write_b32 a71, v255\n" \
"v_accvgpr_read_b32 v252, a136\n" \
"v_accvgpr_read_b32 v253, a137\n" \
"v_accvgpr_read_b32 v254, a138\n" \
"v_accvgpr_read_b32 v255, a139\n" \
"v_mfma_f32_16x16x32_bf16 a[136:139], v[164:167], v[52:55], 0\n" \
"ds_read_b128 v[84:87], v48 offset:0\n" \
"ds_read_b128 v[88:91], v49 offset:0\n" \
"ds_read_b128 v[92:95], v48 offset:2048\n" \
"v_mfma_f32_16x16x32_bf16 a[136:139], v[168:171], v[56:59], a[136:139]\n" \
"ds_read_b128 v[96:99], v49 offset:2048\n" \
"v_accvgpr_read_b32 v180, a140\n" \
"v_accvgpr_read_b32 v181, a141\n" \
"v_accvgpr_read_b32 v182, a142\n" \
"ds_read_b128 v[100:103], v48 offset:4096\n" \
"v_accvgpr_read_b32 v183, a143\n" \
"v_mfma_f32_16x16x32_bf16 a[140:143], v[176:179], v[56:59], 0\n" \
"ds_read_b128 v[104:107], v49 offset:4096\n" \
"v_cvt_pk_bf16_f32 v252, v252, v253\n" \
"v_mfma_f32_16x16x32_bf16 a[140:143], v[172:175], v[52:55], a[140:143]\n" \
"ds_read_b128 v[108:111], v48 offset:6144\n" \
"v_cvt_pk_bf16_f32 v253, v254, v255\n" \
"v_mfma_f32_16x16x32_bf16 a[160:163], v[148:151], v[60:63], 0\n" \
"ds_read_b128 v[112:115], v49 offset:6144\n" \
"v_cvt_pk_bf16_f32 v254, v180, v181\n" \
"v_mfma_f32_16x16x32_bf16 a[160:163], v[152:155], v[64:67], a[160:163]\n" \
"v_cvt_pk_bf16_f32 v255, v182, v183\n" \
"v_accvgpr_write_b32 a96, v252\n" \
"v_mfma_f32_16x16x32_bf16 a[164:167], v[160:163], v[64:67], 0\n" \
"v_accvgpr_write_b32 a97, v253\n" \
"v_accvgpr_write_b32 a98, v254\n" \
"v_mfma_f32_16x16x32_bf16 a[164:167], v[156:159], v[60:63], a[164:167]\n" \
"v_accvgpr_write_b32 a99, v255\n" \
"v_accvgpr_read_b32 v252, a168\n" \
"v_accvgpr_read_b32 v253, a169\n" \
"v_accvgpr_read_b32 v254, a170\n" \
"v_accvgpr_read_b32 v255, a171\n" \
"v_mfma_f32_16x16x32_bf16 a[168:171], v[164:167], v[60:63], 0\n" \
"s_add_u32 s89, s44, 0x1000\n" \
"v_accvgpr_read_b32 v180, a172\n" \
"buffer_store_dwordx4 v[196:199], v20, s[80:83], s89 offen offset:1024 sc1\n" \
"v_mfma_f32_16x16x32_bf16 a[168:171], v[168:171], v[64:67], a[168:171]\n" \
"v_accvgpr_read_b32 v181, a173\n" \
"v_accvgpr_read_b32 v182, a174\n" \
"v_accvgpr_read_b32 v183, a175\n" \
"v_mfma_f32_16x16x32_bf16 a[172:175], v[176:179], v[64:67], 0\n" \
"v_cvt_pk_bf16_f32 v252, v252, v253\n" \
"v_cvt_pk_bf16_f32 v253, v254, v255\n" \
"v_mfma_f32_16x16x32_bf16 a[172:175], v[172:175], v[60:63], a[172:175]\n" \
"v_cvt_pk_bf16_f32 v254, v180, v181\n" \
"v_cvt_pk_bf16_f32 v255, v182, v183\n" \
"v_mfma_f32_16x16x32_bf16 a[192:195], v[148:151], v[68:71], 0\n" \
"v_accvgpr_write_b32 a100, v252\n" \
"v_accvgpr_write_b32 a101, v253\n" \
"v_mfma_f32_16x16x32_bf16 a[192:195], v[152:155], v[72:75], a[192:195]\n" \
"v_accvgpr_write_b32 a102, v254\n" \
"v_accvgpr_write_b32 a103, v255\n" \
"v_accvgpr_read_b32 v252, a200\n" \
"v_accvgpr_read_b32 v253, a201\n" \
"v_accvgpr_read_b32 v254, a202\n" \
"v_accvgpr_read_b32 v255, a203\n" \
"s_waitcnt vmcnt(4) lgkmcnt(0)\n" \
"s_barrier\n" \
"v_mfma_f32_16x16x32_bf16 a[196:199], v[160:163], v[72:75], 0\n" \
"s_add_u32 m0, s40, 0x0\n" \
"s_nop 0\n" \
"s_add_u32 s85, s37, 0x4000\n" \
"v_mfma_f32_16x16x32_bf16 a[196:199], v[156:159], v[68:71], a[196:199]\n" \
"buffer_load_dwordx4 v20, s[80:83], s85 offen sc1 lds\n" \
"v_mfma_f32_16x16x32_bf16 a[200:203], v[164:167], v[68:71], 0\n" \
"v_accvgpr_read_b32 v180, a204\n" \
"v_accvgpr_read_b32 v181, a205\n" \
"v_mfma_f32_16x16x32_bf16 a[200:203], v[168:171], v[72:75], a[200:203]\n" \
"v_accvgpr_read_b32 v182, a206\n" \
"v_accvgpr_read_b32 v183, a207\n" \
"v_mfma_f32_16x16x32_bf16 a[204:207], v[176:179], v[72:75], 0\n" \
"s_add_u32 m0, s40, 0x1000\n" \
"s_add_u32 s84, s37, 0x5000\n" \
"v_mfma_f32_16x16x32_bf16 a[204:207], v[172:175], v[68:71], a[204:207]\n" \
"buffer_load_dwordx4 v20, s[80:83], s84 offen sc1 lds\n" \
"v_mfma_f32_16x16x32_bf16 a[224:227], v[148:151], v[76:79], 0\n" \
"v_cvt_pk_bf16_f32 v252, v252, v253\n" \
"v_cvt_pk_bf16_f32 v253, v254, v255\n" \
"v_mfma_f32_16x16x32_bf16 a[224:227], v[152:155], v[80:83], a[224:227]\n" \
"v_cvt_pk_bf16_f32 v254, v180, v181\n" \
"v_cvt_pk_bf16_f32 v255, v182, v183\n" \
"v_mfma_f32_16x16x32_bf16 a[228:231], v[160:163], v[80:83], 0\n" \
"v_accvgpr_write_b32 a8, v252\n" \
"v_accvgpr_write_b32 a9, v253\n" \
"v_mfma_f32_16x16x32_bf16 a[228:231], v[156:159], v[76:79], a[228:231]\n" \
"v_accvgpr_write_b32 a10, v254\n" \
"v_accvgpr_write_b32 a11, v255\n" \
"v_accvgpr_read_b32 v252, a232\n" \
"v_accvgpr_read_b32 v253, a233\n" \
"v_accvgpr_read_b32 v254, a234\n" \
"v_accvgpr_read_b32 v255, a235\n" \
"v_mfma_f32_16x16x32_bf16 a[232:235], v[164:167], v[76:79], 0\n" \
"s_add_u32 m0, s40, 0x3000\n" \
"s_mov_b32 s85, 0x100\n" \
"buffer_load_dwordx4 v21, s[76:79], s85 offen lds\n" \
"v_mfma_f32_16x16x32_bf16 a[232:235], v[168:171], v[80:83], a[232:235]\n" \
"v_accvgpr_read_b32 v180, a236\n" \
"v_accvgpr_read_b32 v181, a237\n" \
"v_accvgpr_read_b32 v182, a238\n" \
"v_accvgpr_read_b32 v183, a239\n" \
"v_mfma_f32_16x16x32_bf16 a[236:239], v[176:179], v[80:83], 0\n" \
"v_cvt_pk_bf16_f32 v252, v252, v253\n" \
"v_cvt_pk_bf16_f32 v253, v254, v255\n" \
"v_mfma_f32_16x16x32_bf16 a[236:239], v[172:175], v[76:79], a[236:239]\n" \
"v_cvt_pk_bf16_f32 v254, v180, v181\n" \
"v_cvt_pk_bf16_f32 v255, v182, v183\n" \
"v_accvgpr_write_b32 a12, v252\n" \
"v_accvgpr_write_b32 a13, v253\n" \
"v_accvgpr_write_b32 a14, v254\n" \
"v_accvgpr_write_b32 a15, v255\n" \
"v_accvgpr_read_b32 v252, a144\n" \
"v_accvgpr_read_b32 v253, a145\n" \
"v_accvgpr_read_b32 v254, a146\n" \
"v_accvgpr_read_b32 v255, a147\n" \
"v_mfma_f32_16x16x32_bf16 a[144:147], v[148:151], v[84:87], 0\n" \
"s_add_u32 m0, %[mA], 0x0\n" \
"s_nop 0\n" \
"s_mov_b32 s84, 0x100\n" \
"buffer_load_dwordx4 v24, s[76:79], s84 offen lds\n" \
"v_mfma_f32_16x16x32_bf16 a[144:147], v[152:155], v[88:91], a[144:147]\n" \
"v_cvt_pk_bf16_f32 v252, v252, v253\n" \
"v_accvgpr_read_b32 v180, a176\n" \
"v_accvgpr_read_b32 v181, a177\n" \
"v_accvgpr_read_b32 v182, a178\n" \
"v_accvgpr_read_b32 v183, a179\n" \
"v_cvt_pk_bf16_f32 v253, v254, v255\n" \
"v_cvt_pk_bf16_f32 v254, v180, v181\n" \
"v_cvt_pk_bf16_f32 v255, v182, v183\n" \
"v_accvgpr_write_b32 a40, v252\n" \
"v_accvgpr_write_b32 a41, v253\n" \
"v_accvgpr_write_b32 a42, v254\n" \
"v_accvgpr_write_b32 a43, v255\n" \
"v_accvgpr_read_b32 v252, a148\n" \
"v_accvgpr_read_b32 v253, a149\n" \
"v_accvgpr_read_b32 v254, a150\n" \
"v_accvgpr_read_b32 v255, a151\n" \
"v_mfma_f32_16x16x32_bf16 a[148:151], v[160:163], v[88:91], 0\n" \
"v_cvt_pk_bf16_f32 v252, v252, v253\n" \
"s_add_u32 m0, %[mA], 0x400\n" \
"v_mfma_f32_16x16x32_bf16 a[148:151], v[156:159], v[84:87], a[148:151]\n" \
"s_mov_b32 s85, 0x2500\n" \
"v_accvgpr_read_b32 v180, a180\n" \
"v_accvgpr_read_b32 v181, a181\n" \
"v_accvgpr_read_b32 v182, a182\n" \
"v_accvgpr_read_b32 v183, a183\n" \
"v_cvt_pk_bf16_f32 v253, v254, v255\n" \
"buffer_load_dwordx4 v25, s[76:79], s85 offen lds\n" \
"v_cvt_pk_bf16_f32 v254, v180, v181\n" \
"v_cvt_pk_bf16_f32 v255, v182, v183\n" \
"v_accvgpr_write_b32 a44, v252\n" \
"v_accvgpr_write_b32 a45, v253\n" \
"v_accvgpr_write_b32 a46, v254\n" \
"v_accvgpr_write_b32 a47, v255\n" \
"v_accvgpr_read_b32 v252, a152\n" \
"v_accvgpr_read_b32 v253, a153\n" \
"v_accvgpr_read_b32 v254, a154\n" \
"v_accvgpr_read_b32 v255, a155\n" \
"v_mfma_f32_16x16x32_bf16 a[152:155], v[164:167], v[84:87], 0\n" \
"v_cvt_pk_bf16_f32 v252, v252, v253\n" \
"v_accvgpr_read_b32 v180, a184\n" \
"v_mfma_f32_16x16x32_bf16 a[152:155], v[168:171], v[88:91], a[152:155]\n" \
"v_accvgpr_read_b32 v181, a185\n" \
"v_accvgpr_read_b32 v182, a186\n" \
"v_accvgpr_read_b32 v183, a187\n" \
"v_cvt_pk_bf16_f32 v253, v254, v255\n" \
"v_cvt_pk_bf16_f32 v254, v180, v181\n" \
"v_cvt_pk_bf16_f32 v255, v182, v183\n" \
"v_accvgpr_write_b32 a72, v252\n" \
"v_accvgpr_write_b32 a73, v253\n" \
"v_accvgpr_write_b32 a74, v254\n" \
"v_accvgpr_write_b32 a75, v255\n" \
"v_accvgpr_read_b32 v252, a156\n" \
"v_accvgpr_read_b32 v253, a157\n" \
"v_accvgpr_read_b32 v254, a158\n" \
"v_accvgpr_read_b32 v255, a159\n" \
"v_mfma_f32_16x16x32_bf16 a[156:159], v[176:179], v[88:91], 0\n" \
"s_add_u32 m0, %[mB], 0x0\n" \
"s_nop 0\n" \
"s_mov_b32 s84, 0x100000\n" \
"v_mfma_f32_16x16x32_bf16 a[156:159], v[172:175], v[84:87], a[156:159]\n" \
"buffer_load_dwordx4 v26, s[68:71], s84 offen lds\n" \
"v_mfma_f32_16x16x32_bf16 a[176:179], v[148:151], v[92:95], 0\n" \
"s_add_u32 m0, %[mA], 0x800\n" \
"s_mov_b32 s85, 0x4900\n" \
"v_mfma_f32_16x16x32_bf16 a[176:179], v[152:155], v[96:99], a[176:179]\n" \
"v_accvgpr_read_b32 v180, a188\n" \
"v_accvgpr_read_b32 v181, a189\n" \
"v_mfma_f32_16x16x32_bf16 a[180:183], v[160:163], v[96:99], 0\n" \
"buffer_load_dwordx4 v24, s[76:79], s85 offen lds\n" \
"v_mfma_f32_16x16x32_bf16 a[180:183], v[156:159], v[92:95], a[180:183]\n" \
"v_accvgpr_read_b32 v182, a190\n" \
"v_accvgpr_read_b32 v183, a191\n" \
"v_mfma_f32_16x16x32_bf16 a[184:187], v[164:167], v[92:95], 0\n" \
"s_add_u32 m0, %[mA], 0xc00\n" \
"s_mov_b32 s84, 0x6d00\n" \
"v_mfma_f32_16x16x32_bf16 a[184:187], v[168:171], v[96:99], a[184:187]\n" \
"buffer_load_dwordx4 v25, s[76:79], s84 offen lds\n" \
"v_mfma_f32_16x16x32_bf16 a[188:191], v[176:179], v[96:99], 0\n" \
"v_cvt_pk_bf16_f32 v252, v252, v253\n" \
"v_cvt_pk_bf16_f32 v253, v254, v255\n" \
"v_mfma_f32_16x16x32_bf16 a[188:191], v[172:175], v[92:95], a[188:191]\n" \
"v_cvt_pk_bf16_f32 v254, v180, v181\n" \
"v_cvt_pk_bf16_f32 v255, v182, v183\n" \
"v_accvgpr_write_b32 a76, v252\n" \
"v_accvgpr_write_b32 a77, v253\n" \
"L_pf5:\n" \
"s_cbranch_execz L_pf9\n" \
"s_nop 0\n" \
"v_accvgpr_write_b32 a78, v254\n" \
"v_accvgpr_write_b32 a79, v255\n" \
"v_accvgpr_read_b32 v252, a208\n" \
"v_accvgpr_read_b32 v253, a209\n" \
"v_accvgpr_read_b32 v254, a210\n" \
"v_accvgpr_read_b32 v255, a211\n" \
"v_mfma_f32_16x16x32_bf16 a[208:211], v[148:151], v[100:103], 0\n" \
"v_cvt_pk_bf16_f32 v252, v252, v253\n" \
"v_accvgpr_read_b32 v180, a240\n" \
"v_mfma_f32_16x16x32_bf16 a[208:211], v[152:155], v[104:107], a[208:211]\n" \
"v_accvgpr_read_b32 v181, a241\n" \
"v_accvgpr_read_b32 v182, a242\n" \
"v_accvgpr_read_b32 v183, a243\n" \
"v_cvt_pk_bf16_f32 v253, v254, v255\n" \
"v_cvt_pk_bf16_f32 v254, v180, v181\n" \
"v_cvt_pk_bf16_f32 v255, v182, v183\n" \
"v_accvgpr_write_b32 a104, v252\n" \
"v_accvgpr_write_b32 a105, v253\n" \
"v_accvgpr_write_b32 a106, v254\n" \
"v_accvgpr_write_b32 a107, v255\n" \
"v_accvgpr_read_b32 v252, a212\n" \
"v_accvgpr_read_b32 v253, a213\n" \
"v_accvgpr_read_b32 v254, a214\n" \
"v_accvgpr_read_b32 v255, a215\n" \
"v_mfma_f32_16x16x32_bf16 a[212:215], v[160:163], v[104:107], 0\n" \
"s_add_u32 m0, %[mB], 0x400\n" \
"s_mov_b32 s85, 0x108000\n" \
"buffer_load_dwordx4 v26, s[68:71], s85 offen lds\n" \
"ds_read_b64_tr_b16 v[140:141], v39 offset:32768\n" \
"ds_read_b64_tr_b16 v[142:143], v39 offset:33280\n" \
"ds_read_b128 v[84:87], v22 offset:32768\n" \
"v_mfma_f32_16x16x32_bf16 a[212:215], v[156:159], v[100:103], a[212:215]\n" \
"ds_read_b64_tr_b16 v[144:145], v39 offset:36864\n" \
"ds_read_b64_tr_b16 v[146:147], v39 offset:37376\n" \
"ds_read_b128 v[88:91], v23 offset:32768\n" \
"v_accvgpr_read_b32 v184, a216\n" \
"v_accvgpr_read_b32 v185, a217\n" \
"v_accvgpr_read_b32 v186, a218\n" \
"v_accvgpr_read_b32 v187, a219\n" \
"v_mfma_f32_16x16x32_bf16 a[216:219], v[164:167], v[100:103], 0\n" \
"ds_read_b64_tr_b16 v[128:129], v37 offset:36864\n" \
"ds_read_b64_tr_b16 v[130:131], v37 offset:37376\n" \
"v_mfma_f32_16x16x32_bf16 a[216:219], v[168:171], v[104:107], a[216:219]\n" \
"v_cvt_pk_bf16_f32 v252, v252, v253\n" \
"v_accvgpr_read_b32 v180, a244\n" \
"v_accvgpr_read_b32 v181, a245\n" \
"v_accvgpr_read_b32 v182, a246\n" \
"v_accvgpr_read_b32 v183, a247\n" \
"v_cvt_pk_bf16_f32 v253, v254, v255\n" \
"v_cvt_pk_bf16_f32 v254, v180, v181\n" \
"v_cvt_pk_bf16_f32 v255, v182, v183\n" \
"ds_read_b64_tr_b16 v[124:125], v37 offset:32768\n" \
"ds_read_b64_tr_b16 v[126:127], v37 offset:33280\n" \
"v_accvgpr_write_b32 a108, v252\n" \
"v_accvgpr_write_b32 a109, v253\n" \
"v_accvgpr_write_b32 a110, v254\n" \
"v_accvgpr_write_b32 a111, v255\n" \
"v_accvgpr_read_b32 v252, a220\n" \
"v_accvgpr_read_b32 v253, a221\n" \
"v_accvgpr_read_b32 v254, a222\n" \
"v_accvgpr_read_b32 v255, a223\n" \
"v_mfma_f32_16x16x32_bf16 a[220:223], v[176:179], v[104:107], 0\n" \
"s_add_u32 m0, %[mA], 0x1000\n" \
"s_mov_b32 s84, 0x9100\n" \
"buffer_load_dwordx4 v24, s[76:79], s84 offen lds\n" \
"ds_read_b64_tr_b16 v[132:133], v38 offset:32768\n" \
"ds_read_b64_tr_b16 v[134:135], v38 offset:33280\n" \
"v_mfma_f32_16x16x32_bf16 a[220:223], v[172:175], v[100:103], a[220:223]\n" \
"ds_read_b64_tr_b16 v[136:137], v38 offset:36864\n" \
"ds_read_b64_tr_b16 v[138:139], v38 offset:37376\n" \
"v_mfma_f32_16x16x32_bf16 a[240:243], v[148:151], v[108:111], 0\n" \
"ds_read_b128 v[56:59], v29 offset:32768\n" \
"s_add_u32 m0, %[mA], 0x1400\n" \
"v_mfma_f32_16x16x32_bf16 a[240:243], v[152:155], v[112:115], a[240:243]\n" \
"s_mov_b32 s85, 0xb500\n" \
"ds_read_b128 v[52:55], v28 offset:32768\n" \
"buffer_load_dwordx4 v25, s[76:79], s85 offen lds\n" \
"v_mfma_f32_16x16x32_bf16 a[244:247], v[160:163], v[112:115], 0\n" \
"v_accvgpr_read_b32 v180, a248\n" \
"v_accvgpr_read_b32 v181, a249\n" \
"v_mfma_f32_16x16x32_bf16 a[244:247], v[156:159], v[108:111], a[244:247]\n" \
"v_accvgpr_read_b32 v182, a250\n" \
"v_accvgpr_read_b32 v183, a251\n" \
"v_mfma_f32_16x16x32_bf16 a[248:251], v[164:167], v[108:111], 0\n" \
"ds_read_b64_tr_b16 v[120:121], v36 offset:36864\n" \
"ds_read_b64_tr_b16 v[122:123], v36 offset:37376\n" \
"v_mfma_f32_16x16x32_bf16 a[248:251], v[168:171], v[112:115], a[248:251]\n" \
"s_add_u32 m0, %[mB], 0x800\n" \
"s_mov_b32 s84, 0x110000\n" \
"v_cvt_pk_bf16_f32 v184, v184, v185\n" \
"buffer_load_dwordx4 v27, s[68:71], s84 offen lds\n" \
"v_cvt_pk_bf16_f32 v185, v186, v187\n" \
"ds_read_b64_tr_b16 v[116:117], v36 offset:32768\n" \
"ds_read_b64_tr_b16 v[118:119], v36 offset:33280\n" \
"v_cvt_pk_bf16_f32 v186, v180, v181\n" \
"v_cvt_pk_bf16_f32 v187, v182, v183\n" \
"v_accvgpr_read_b32 v180, a252\n" \
"v_accvgpr_read_b32 v181, a253\n" \
"v_accvgpr_read_b32 v182, a254\n" \
"v_accvgpr_read_b32 v183, a255\n" \
"v_mfma_f32_16x16x32_bf16 a[252:255], v[176:179], v[112:115], 0\n" \
"ds_read_b128 v[64:67], v29 offset:34816\n" \
"ds_read_b128 v[60:63], v28 offset:34816\n" \
"v_mfma_f32_16x16x32_bf16 a[252:255], v[172:175], v[108:111], a[252:255]\n" \
"s_add_u32 m0, %[mA], 0x1800\n" \
"s_waitcnt lgkmcnt(12)\n" \
"v_mfma_f32_16x16x32_bf16 a[16:19], v[140:143], v[84:87], a[16:19]\n" \
"s_mov_b32 s84, 0xd900\n" \
"ds_read_b128 v[72:75], v29 offset:36864\n" \
"v_mfma_f32_16x16x32_bf16 a[16:19], v[144:147], v[88:91], a[16:19]\n" \
"buffer_load_dwordx4 v24, s[76:79], s84 offen lds\n" \
"v_mfma_f32_16x16x32_bf16 a[48:51], v[128:131], v[88:91], a[48:51]\n" \
"s_add_u32 m0, %[mA], 0x1c00\n" \
"s_mov_b32 s85, 0xfd00\n" \
"v_mfma_f32_16x16x32_bf16 a[48:51], v[124:127], v[84:87], a[48:51]\n" \
"ds_read_b128 v[68:71], v28 offset:36864\n" \
"s_waitcnt lgkmcnt(9)\n" \
"v_mfma_f32_16x16x32_bf16 a[80:83], v[84:87], v[132:135], a[80:83]\n" \
"buffer_load_dwordx4 v25, s[76:79], s85 offen lds\n" \
"v_mfma_f32_16x16x32_bf16 a[80:83], v[88:91], v[136:139], a[80:83]\n" \
"s_add_u32 m0, %[mB], 0xc00\n" \
"s_mov_b32 s84, 0x118000\n" \
"v_mfma_f32_16x16x32_bf16 a[112:115], v[144:147], v[56:59], a[112:115]\n" \
"ds_read_b128 v[80:83], v29 offset:38912\n" \
"s_waitcnt lgkmcnt(9)\n" \
"v_mfma_f32_16x16x32_bf16 a[112:115], v[140:143], v[52:55], a[112:115]\n" \
"buffer_load_dwordx4 v27, s[68:71], s84 offen lds\n" \
"v_mfma_f32_16x16x32_bf16 a[20:23], v[124:127], v[52:55], a[20:23]\n" \
"ds_read_b128 v[76:79], v28 offset:38912\n" \
"ds_read_b64_tr_b16 v[152:153], v32 offset:40960\n" \
"v_mfma_f32_16x16x32_bf16 a[20:23], v[128:131], v[56:59], a[20:23]\n" \
"s_add_u32 s88, s44, 0x0\n" \
"s_waitcnt lgkmcnt(9)\n" \
"v_mfma_f32_16x16x32_bf16 a[52:55], v[120:123], v[56:59], a[52:55]\n" \
"ds_read_b64_tr_b16 v[154:155], v32 offset:41984\n" \
"s_waitcnt lgkmcnt(8)\n" \
"v_mfma_f32_16x16x32_bf16 a[52:55], v[116:119], v[52:55], a[52:55]\n" \
"buffer_store_dwordx4 v[200:203], v20, s[80:83], s88 offen offset:2048 sc1\n" \
"v_mfma_f32_16x16x32_bf16 a[84:87], v[52:55], v[132:135], a[84:87]\n" \
"ds_read_b64_tr_b16 v[148:149], v32 offset:32768\n" \
"ds_read_b64_tr_b16 v[150:151], v32 offset:33792\n" \
"v_mfma_f32_16x16x32_bf16 a[84:87], v[56:59], v[136:139], a[84:87]\n" \
"ds_read_b64_tr_b16 v[156:157], v33 offset:32768\n" \
"s_waitcnt lgkmcnt(9)\n" \
"s_nop 0\n" \
"v_mfma_f32_16x16x32_bf16 a[116:119], v[144:147], v[64:67], a[116:119]\n" \
"ds_read_b64_tr_b16 v[158:159], v33 offset:33792\n" \
"ds_read_b64_tr_b16 v[160:161], v33 offset:40960\n" \
"v_mfma_f32_16x16x32_bf16 a[116:119], v[140:143], v[60:63], a[116:119]\n" \
"ds_read_b64_tr_b16 v[162:163], v33 offset:41984\n" \
"ds_read_b64_tr_b16 v[168:169], v34 offset:40960\n" \
"v_mfma_f32_16x16x32_bf16 a[24:27], v[124:127], v[60:63], a[24:27]\n" \
"ds_read_b64_tr_b16 v[170:171], v34 offset:41984\n" \
"ds_read_b64_tr_b16 v[164:165], v34 offset:32768\n" \
"v_mfma_f32_16x16x32_bf16 a[24:27], v[128:131], v[64:67], a[24:27]\n" \
"ds_read_b64_tr_b16 v[166:167], v34 offset:33792\n" \
"ds_read_b64_tr_b16 v[172:173], v35 offset:32768\n" \
"v_mfma_f32_16x16x32_bf16 a[56:59], v[120:123], v[64:67], a[56:59]\n" \
"ds_read_b64_tr_b16 v[174:175], v35 offset:33792\n" \
"ds_read_b64_tr_b16 v[176:177], v35 offset:40960\n" \
"v_mfma_f32_16x16x32_bf16 a[56:59], v[116:119], v[60:63], a[56:59]\n" \
"s_add_u32 s89, s44, 0x1000\n" \
"ds_read_b64_tr_b16 v[178:179], v35 offset:41984\n" \
"v_mfma_f32_16x16x32_bf16 a[88:91], v[60:63], v[132:135], a[88:91]\n" \
"buffer_store_dwordx4 v[204:207], v20, s[80:83], s89 offen offset:2048 sc1\n" \
"v_mfma_f32_16x16x32_bf16 a[88:91], v[64:67], v[136:139], a[88:91]\n" \
"ds_read_b128 v[84:87], v48 offset:32768\n" \
"s_waitcnt lgkmcnt(15)\n" \
"s_nop 0\n" \
"v_mfma_f32_16x16x32_bf16 a[120:123], v[144:147], v[72:75], a[120:123]\n" \
"ds_read_b128 v[88:91], v49 offset:32768\n" \
"ds_read_b128 v[92:95], v48 offset:34816\n" \
"v_mfma_f32_16x16x32_bf16 a[120:123], v[140:143], v[68:71], a[120:123]\n" \
"ds_read_b128 v[96:99], v49 offset:34816\n" \
"ds_read_b128 v[100:103], v48 offset:36864\n" \
"v_mfma_f32_16x16x32_bf16 a[28:31], v[124:127], v[68:71], a[28:31]\n" \
"v_cvt_pk_bf16_f32 v252, v252, v253\n" \
"ds_read_b128 v[104:107], v49 offset:36864\n" \
"v_mfma_f32_16x16x32_bf16 a[28:31], v[128:131], v[72:75], a[28:31]\n" \
"v_cvt_pk_bf16_f32 v253, v254, v255\n" \
"ds_read_b128 v[108:111], v48 offset:38912\n" \
"v_mfma_f32_16x16x32_bf16 a[60:63], v[120:123], v[72:75], a[60:63]\n" \
"v_cvt_pk_bf16_f32 v254, v180, v181\n" \
"ds_read_b128 v[112:115], v49 offset:38912\n" \
"v_mfma_f32_16x16x32_bf16 a[60:63], v[116:119], v[68:71], a[60:63]\n" \
"v_cvt_pk_bf16_f32 v255, v182, v183\n" \
"v_mfma_f32_16x16x32_bf16 a[92:95], v[68:71], v[132:135], a[92:95]\n" \
"v_mfma_f32_16x16x32_bf16 a[92:95], v[72:75], v[136:139], a[92:95]\n" \
"s_add_u32 s88, s44, 0x0\n" \
"v_mfma_f32_16x16x32_bf16 a[124:127], v[144:147], v[80:83], a[124:127]\n" \
"buffer_store_dwordx4 v[208:211], v20, s[80:83], s88 offen offset:3072 sc1\n" \
"v_mfma_f32_16x16x32_bf16 a[124:127], v[140:143], v[76:79], a[124:127]\n" \
"v_mfma_f32_16x16x32_bf16 a[0:3], v[124:127], v[76:79], a[0:3]\n" \
"v_mfma_f32_16x16x32_bf16 a[0:3], v[128:131], v[80:83], a[0:3]\n" \
"v_mfma_f32_16x16x32_bf16 a[4:7], v[120:123], v[80:83], a[4:7]\n" \
"v_mfma_f32_16x16x32_bf16 a[4:7], v[116:119], v[76:79], a[4:7]\n" \
"v_mfma_f32_16x16x32_bf16 a[32:35], v[76:79], v[132:135], a[32:35]\n" \
"v_mfma_f32_16x16x32_bf16 a[32:35], v[80:83], v[136:139], a[32:35]\n" \
"v_mfma_f32_16x16x32_bf16 a[128:131], v[152:155], v[56:59], a[128:131]\n" \
"s_waitcnt lgkmcnt(4)\n" \
"v_mfma_f32_16x16x32_bf16 a[128:131], v[148:151], v[52:55], a[128:131]\n" \
"v_mfma_f32_16x16x32_bf16 a[132:135], v[156:159], v[52:55], a[132:135]\n" \
"v_mfma_f32_16x16x32_bf16 a[132:135], v[160:163], v[56:59], a[132:135]\n" \
"v_mfma_f32_16x16x32_bf16 a[136:139], v[168:171], v[56:59], a[136:139]\n" \
"v_mfma_f32_16x16x32_bf16 a[136:139], v[164:167], v[52:55], a[136:139]\n" \
"v_mfma_f32_16x16x32_bf16 a[140:143], v[172:175], v[52:55], a[140:143]\n" \
"v_mfma_f32_16x16x32_bf16 a[140:143], v[176:179], v[56:59], a[140:143]\n" \
"v_mfma_f32_16x16x32_bf16 a[160:163], v[152:155], v[64:67], a[160:163]\n" \
"v_mfma_f32_16x16x32_bf16 a[160:163], v[148:151], v[60:63], a[160:163]\n" \
"v_mfma_f32_16x16x32_bf16 a[164:167], v[156:159], v[60:63], a[164:167]\n" \
"v_mfma_f32_16x16x32_bf16 a[164:167], v[160:163], v[64:67], a[164:167]\n" \
"v_mfma_f32_16x16x32_bf16 a[168:171], v[168:171], v[64:67], a[168:171]\n" \
"v_mfma_f32_16x16x32_bf16 a[168:171], v[164:167], v[60:63], a[168:171]\n" \
"v_mfma_f32_16x16x32_bf16 a[172:175], v[172:175], v[60:63], a[172:175]\n" \
"v_mfma_f32_16x16x32_bf16 a[172:175], v[176:179], v[64:67], a[172:175]\n" \
"v_mfma_f32_16x16x32_bf16 a[192:195], v[152:155], v[72:75], a[192:195]\n" \
"v_mfma_f32_16x16x32_bf16 a[192:195], v[148:151], v[68:71], a[192:195]\n" \
"s_waitcnt vmcnt(3) lgkmcnt(0)\n" \
"s_barrier\n" \
"v_mfma_f32_16x16x32_bf16 a[196:199], v[156:159], v[68:71], a[196:199]\n" \
"s_add_u32 m0, s40, 0x8000\n" \
"s_add_u32 s85, s37, 0x6000\n" \
"v_mfma_f32_16x16x32_bf16 a[196:199], v[160:163], v[72:75], a[196:199]\n" \
"buffer_load_dwordx4 v20, s[80:83], s85 offen sc1 lds\n" \
"v_mfma_f32_16x16x32_bf16 a[200:203], v[168:171], v[72:75], a[200:203]\n" \
"s_add_u32 m0, s40, 0x9000\n" \
"s_add_u32 s84, s37, 0x7000\n" \
"v_mfma_f32_16x16x32_bf16 a[200:203], v[164:167], v[68:71], a[200:203]\n" \
"ds_read_b64_tr_b16 v[144:145], v39 offset:4096\n" \
"ds_read_b64_tr_b16 v[146:147], v39 offset:4608\n" \
"v_mfma_f32_16x16x32_bf16 a[204:207], v[172:175], v[68:71], a[204:207]\n" \
"buffer_load_dwordx4 v20, s[80:83], s84 offen sc1 lds\n" \
"v_mfma_f32_16x16x32_bf16 a[204:207], v[176:179], v[72:75], a[204:207]\n" \
"ds_read_b64_tr_b16 v[140:141], v39 offset:0\n" \
"ds_read_b64_tr_b16 v[142:143], v39 offset:512\n" \
"v_mfma_f32_16x16x32_bf16 a[224:227], v[152:155], v[80:83], a[224:227]\n" \
"s_add_u32 m0, s40, 0xb000\n" \
"s_mov_b32 s85, 0x180\n" \
"v_mfma_f32_16x16x32_bf16 a[224:227], v[148:151], v[76:79], a[224:227]\n" \
"buffer_load_dwordx4 v21, s[76:79], s85 offen lds\n" \
"v_mfma_f32_16x16x32_bf16 a[228:231], v[156:159], v[76:79], a[228:231]\n" \
"ds_read_b64_tr_b16 v[124:125], v37 offset:0\n" \
"ds_read_b64_tr_b16 v[126:127], v37 offset:512\n" \
"v_mfma_f32_16x16x32_bf16 a[228:231], v[160:163], v[80:83], a[228:231]\n" \
"ds_read_b64_tr_b16 v[128:129], v37 offset:4096\n" \
"ds_read_b64_tr_b16 v[130:131], v37 offset:4608\n" \
"v_mfma_f32_16x16x32_bf16 a[232:235], v[168:171], v[80:83], a[232:235]\n" \
"s_add_u32 m0, %[mA], 0x8000\n" \
"s_mov_b32 s84, 0x180\n" \
"v_mfma_f32_16x16x32_bf16 a[232:235], v[164:167], v[76:79], a[232:235]\n" \
"buffer_load_dwordx4 v24, s[76:79], s84 offen lds\n" \
"v_mfma_f32_16x16x32_bf16 a[236:239], v[172:175], v[76:79], a[236:239]\n" \
"ds_read_b64_tr_b16 v[136:137], v38 offset:4096\n" \
"ds_read_b64_tr_b16 v[138:139], v38 offset:4608\n" \
"v_mfma_f32_16x16x32_bf16 a[236:239], v[176:179], v[80:83], a[236:239]\n" \
"s_add_u32 m0, %[mA], 0x8400\n" \
"s_mov_b32 s85, 0x2580\n" \
"v_mfma_f32_16x16x32_bf16 a[144:147], v[152:155], v[88:91], a[144:147]\n" \
"buffer_load_dwordx4 v25, s[76:79], s85 offen lds\n" \
"v_mfma_f32_16x16x32_bf16 a[144:147], v[148:151], v[84:87], a[144:147]\n" \
"ds_read_b64_tr_b16 v[132:133], v38 offset:0\n" \
"ds_read_b64_tr_b16 v[134:135], v38 offset:512\n" \
"v_mfma_f32_16x16x32_bf16 a[148:151], v[156:159], v[84:87], a[148:151]\n" \
"ds_read_b128 v[52:55], v28 offset:0\n" \
"ds_read_b128 v[56:59], v29 offset:0\n" \
"v_mfma_f32_16x16x32_bf16 a[148:151], v[160:163], v[88:91], a[148:151]\n" \
"s_add_u32 m0, %[mB], 0x8000\n" \
"s_mov_b32 s84, 0x180000\n" \
"v_mfma_f32_16x16x32_bf16 a[152:155], v[168:171], v[88:91], a[152:155]\n" \
"buffer_load_dwordx4 v26, s[68:71], s84 offen lds\n" \
"v_mfma_f32_16x16x32_bf16 a[152:155], v[164:167], v[84:87], a[152:155]\n" \
"ds_read_b64_tr_b16 v[116:117], v36 offset:0\n" \
"ds_read_b64_tr_b16 v[118:119], v36 offset:512\n" \
"v_mfma_f32_16x16x32_bf16 a[156:159], v[172:175], v[84:87], a[156:159]\n" \
"ds_read_b128 v[84:87], v22 offset:0\n" \
"ds_read_b64_tr_b16 v[120:121], v36 offset:4096\n" \
"v_mfma_f32_16x16x32_bf16 a[156:159], v[176:179], v[88:91], a[156:159]\n" \
"s_add_u32 m0, %[mA], 0x8800\n" \
"s_mov_b32 s85, 0x4980\n" \
"v_mfma_f32_16x16x32_bf16 a[176:179], v[152:155], v[96:99], a[176:179]\n" \
"buffer_load_dwordx4 v24, s[76:79], s85 offen lds\n" \
"v_mfma_f32_16x16x32_bf16 a[176:179], v[148:151], v[92:95], a[176:179]\n" \
"ds_read_b128 v[88:91], v23 offset:0\n" \
"ds_read_b64_tr_b16 v[122:123], v36 offset:4608\n" \
"v_mfma_f32_16x16x32_bf16 a[180:183], v[156:159], v[92:95], a[180:183]\n" \
"s_add_u32 m0, %[mA], 0x8c00\n" \
"s_mov_b32 s84, 0x6d80\n" \
"v_mfma_f32_16x16x32_bf16 a[180:183], v[160:163], v[96:99], a[180:183]\n" \
"buffer_load_dwordx4 v25, s[76:79], s84 offen lds\n" \
"v_mfma_f32_16x16x32_bf16 a[184:187], v[168:171], v[96:99], a[184:187]\n" \
"ds_read_b128 v[60:63], v28 offset:2048\n" \
"ds_read_b128 v[64:67], v29 offset:2048\n" \
"v_mfma_f32_16x16x32_bf16 a[184:187], v[164:167], v[92:95], a[184:187]\n" \
"ds_read_b128 v[68:71], v28 offset:4096\n" \
"ds_read_b128 v[72:75], v29 offset:4096\n" \
"v_mfma_f32_16x16x32_bf16 a[188:191], v[172:175], v[92:95], a[188:191]\n" \
"s_add_u32 m0, %[mB], 0x8400\n" \
"s_mov_b32 s85, 0x188000\n" \
"v_mfma_f32_16x16x32_bf16 a[188:191], v[176:179], v[96:99], a[188:191]\n" \
"buffer_load_dwordx4 v26, s[68:71], s85 offen lds\n" \
"v_mfma_f32_16x16x32_bf16 a[208:211], v[152:155], v[104:107], a[208:211]\n" \
"ds_read_b128 v[76:79], v28 offset:6144\n" \
"ds_read_b128 v[80:83], v29 offset:6144\n" \
"v_mfma_f32_16x16x32_bf16 a[208:211], v[148:151], v[100:103], a[208:211]\n" \
"ds_read_b128 v[92:95], v48 offset:2048\n" \
"ds_read_b128 v[96:99], v49 offset:2048\n" \
"v_mfma_f32_16x16x32_bf16 a[212:215], v[156:159], v[100:103], a[212:215]\n" \
"s_add_u32 m0, %[mA], 0x9000\n" \
"s_mov_b32 s84, 0x9180\n" \
"v_mfma_f32_16x16x32_bf16 a[212:215], v[160:163], v[104:107], a[212:215]\n" \
"buffer_load_dwordx4 v24, s[76:79], s84 offen lds\n" \
"v_mfma_f32_16x16x32_bf16 a[216:219], v[168:171], v[104:107], a[216:219]\n" \
"v_mfma_f32_16x16x32_bf16 a[216:219], v[164:167], v[100:103], a[216:219]\n" \
"s_add_u32 m0, %[mA], 0x9400\n" \
"s_mov_b32 s85, 0xb580\n" \
"v_mfma_f32_16x16x32_bf16 a[220:223], v[172:175], v[100:103], a[220:223]\n" \
"buffer_load_dwordx4 v25, s[76:79], s85 offen lds\n" \
"v_mfma_f32_16x16x32_bf16 a[220:223], v[176:179], v[104:107], a[220:223]\n" \
"ds_read_b128 v[100:103], v48 offset:4096\n" \
"ds_read_b128 v[104:107], v49 offset:4096\n" \
"v_mfma_f32_16x16x32_bf16 a[240:243], v[152:155], v[112:115], a[240:243]\n" \
"ds_read_b64_tr_b16 v[152:153], v32 offset:8192\n" \
"ds_read_b64_tr_b16 v[154:155], v32 offset:9216\n" \
"v_mfma_f32_16x16x32_bf16 a[240:243], v[148:151], v[108:111], a[240:243]\n" \
"s_add_u32 m0, %[mB], 0x8800\n" \
"s_mov_b32 s84, 0x190000\n" \
"v_mfma_f32_16x16x32_bf16 a[244:247], v[156:159], v[108:111], a[244:247]\n" \
"buffer_load_dwordx4 v27, s[68:71], s84 offen lds\n" \
"v_mfma_f32_16x16x32_bf16 a[244:247], v[160:163], v[112:115], a[244:247]\n" \
"ds_read_b64_tr_b16 v[148:149], v32 offset:0\n" \
"ds_read_b64_tr_b16 v[150:151], v32 offset:1024\n" \
"v_mfma_f32_16x16x32_bf16 a[248:251], v[168:171], v[112:115], a[248:251]\n" \
"ds_read_b64_tr_b16 v[160:161], v33 offset:8192\n" \
"ds_read_b64_tr_b16 v[162:163], v33 offset:9216\n" \
"v_mfma_f32_16x16x32_bf16 a[248:251], v[164:167], v[108:111], a[248:251]\n" \
"ds_read_b64_tr_b16 v[156:157], v33 offset:0\n" \
"ds_read_b64_tr_b16 v[158:159], v33 offset:1024\n" \
"v_mfma_f32_16x16x32_bf16 a[252:255], v[172:175], v[108:111], a[252:255]\n" \
"ds_read_b64_tr_b16 v[164:165], v34 offset:0\n" \
"ds_read_b64_tr_b16 v[166:167], v34 offset:1024\n" \
"v_mfma_f32_16x16x32_bf16 a[252:255], v[176:179], v[112:115], a[252:255]\n" \
"s_add_u32 m0, %[mA], 0x9800\n" \
"s_waitcnt lgkmcnt(14)\n" \
"s_nop 0\n" \
"v_mfma_f32_16x16x32_bf16 a[16:19], v[144:147], v[88:91], a[16:19]\n" \
"s_mov_b32 s84, 0xd980\n" \
"ds_read_b64_tr_b16 v[168:169], v34 offset:8192\n" \
"v_mfma_f32_16x16x32_bf16 a[16:19], v[140:143], v[84:87], a[16:19]\n" \
"buffer_load_dwordx4 v24, s[76:79], s84 offen lds\n" \
"v_mfma_f32_16x16x32_bf16 a[48:51], v[124:127], v[84:87], a[48:51]\n" \
"s_add_u32 m0, %[mA], 0x9c00\n" \
"s_mov_b32 s85, 0xfd80\n" \
"v_mfma_f32_16x16x32_bf16 a[48:51], v[128:131], v[88:91], a[48:51]\n" \
"ds_read_b64_tr_b16 v[170:171], v34 offset:9216\n" \
"ds_read_b64_tr_b16 v[176:177], v35 offset:8192\n" \
"v_mfma_f32_16x16x32_bf16 a[80:83], v[88:91], v[136:139], a[80:83]\n" \
"buffer_load_dwordx4 v25, s[76:79], s85 offen lds\n" \
"v_mfma_f32_16x16x32_bf16 a[80:83], v[84:87], v[132:135], a[80:83]\n" \
"s_add_u32 m0, %[mB], 0x8c00\n" \
"s_mov_b32 s84, 0x198000\n" \
"v_mfma_f32_16x16x32_bf16 a[112:115], v[140:143], v[52:55], a[112:115]\n" \
"ds_read_b64_tr_b16 v[178:179], v35 offset:9216\n" \
"ds_read_b64_tr_b16 v[172:173], v35 offset:0\n" \
"v_mfma_f32_16x16x32_bf16 a[112:115], v[144:147], v[56:59], a[112:115]\n" \
"buffer_load_dwordx4 v27, s[68:71], s84 offen lds\n" \
"v_mfma_f32_16x16x32_bf16 a[20:23], v[128:131], v[56:59], a[20:23]\n" \
"ds_read_b64_tr_b16 v[174:175], v35 offset:1024\n" \
"ds_read_b128 v[84:87], v48 offset:0\n" \
"v_mfma_f32_16x16x32_bf16 a[20:23], v[124:127], v[52:55], a[20:23]\n" \
"s_add_u32 s89, s44, 0x1000\n" \
"ds_read_b128 v[88:91], v49 offset:0\n" \
"v_mfma_f32_16x16x32_bf16 a[52:55], v[116:119], v[52:55], a[52:55]\n" \
"buffer_store_dwordx4 v[212:215], v20, s[80:83], s89 offen offset:3072 sc1\n" \
"v_mfma_f32_16x16x32_bf16 a[52:55], v[120:123], v[56:59], a[52:55]\n" \
"ds_read_b128 v[108:111], v48 offset:6144\n" \
"ds_read_b128 v[112:115], v49 offset:6144\n" \
"v_mfma_f32_16x16x32_bf16 a[84:87], v[56:59], v[136:139], a[84:87]\n" \
"s_add_u32 s94, %[b], %[w]\n" \
"s_and_b32 s96, 7, %[b]\n" \
"v_mfma_f32_16x16x32_bf16 a[84:87], v[52:55], v[132:135], a[84:87]\n" \
"s_cmp_lg_u32 s94, 0\n" \
"s_cselect_b32 s95, 0x40000000, 0x5bc0000\n" \
"v_mfma_f32_16x16x32_bf16 a[116:119], v[140:143], v[60:63], a[116:119]\n" \
"s_mul_i32 s97, 0x48, s96\n" \
"s_add_u32 s98, 0x47, s97\n" \
"v_mfma_f32_16x16x32_bf16 a[116:119], v[144:147], v[64:67], a[116:119]\n" \
"v_writelane_b32 v11, s95, 50\n" \
"s_lshr_b32 s94, %[b], 3\n" \
"v_mfma_f32_16x16x32_bf16 a[24:27], v[128:131], v[64:67], a[24:27]\n" \
"s_cmp_lt_u32 %[w], 3\n" \
"s_cselect_b32 s99, s98, 0xfff\n" \
"v_mfma_f32_16x16x32_bf16 a[24:27], v[124:127], v[60:63], a[24:27]\n" \
"s_lshl_b32 s67, s96, 2\n" \
"s_and_b32 s95, 3, s94\n" \
"v_mfma_f32_16x16x32_bf16 a[56:59], v[116:119], v[60:63], a[56:59]\n" \
"s_mul_i32 s100, 0x480, s99\n" \
"s_lshl_b32 s64, %[w], 5\n" \
"v_mfma_f32_16x16x32_bf16 a[56:59], v[120:123], v[64:67], a[56:59]\n" \
"s_add_u32 s88, s42, 0x0\n" \
"s_add_u32 s98, s67, s95\n" \
"v_mfma_f32_16x16x32_bf16 a[88:91], v[64:67], v[136:139], a[88:91]\n" \
"buffer_store_dwordx4 v[216:219], v20, s[80:83], s88 offen offset:0 sc1\n" \
"v_mfma_f32_16x16x32_bf16 a[88:91], v[60:63], v[132:135], a[88:91]\n" \
"s_add_u32 s101, 0x120, s100\n" \
"s_add_u32 s65, s97, s64\n" \
"v_mfma_f32_16x16x32_bf16 a[120:123], v[140:143], v[68:71], a[120:123]\n" \
"s_mul_i32 s99, 0x24000, s98\n" \
"v_writelane_b32 v11, s101, 15\n" \
"v_mfma_f32_16x16x32_bf16 a[120:123], v[144:147], v[72:75], a[120:123]\n" \
"s_mul_i32 s66, 0x480, s65\n" \
"s_add_u32 s100, 0x1b000, s99\n" \
"v_mfma_f32_16x16x32_bf16 a[28:31], v[128:131], v[72:75], a[28:31]\n" \
"s_cmp_lt_u32 %[w], 3\n" \
"s_cselect_b32 s101, s66, s100\n" \
"v_mfma_f32_16x16x32_bf16 a[28:31], v[124:127], v[68:71], a[28:31]\n" \
"s_min_u32 s97, 2, %[w]\n" \
"s_mul_i32 s64, 0x2400, s97\n" \
"v_mfma_f32_16x16x32_bf16 a[60:63], v[116:119], v[68:71], a[60:63]\n" \
"v_writelane_b32 v11, s101, 16\n" \
"s_add_u32 s65, 0x20000, s64\n" \
"v_mfma_f32_16x16x32_bf16 a[60:63], v[120:123], v[72:75], a[60:63]\n" \
"s_mul_i32 s94, 0x9000, s97\n" \
"s_mul_i32 s95, 0x2400, %[w]\n" \
"v_mfma_f32_16x16x32_bf16 a[92:95], v[72:75], v[136:139], a[92:95]\n" \
"v_writelane_b32 v11, s65, 17\n" \
"s_add_u32 s67, s99, s94\n" \
"v_mfma_f32_16x16x32_bf16 a[92:95], v[68:71], v[132:135], a[92:95]\n" \
"s_add_u32 s89, s42, 0x1000\n" \
"v_writelane_b32 v11, s67, 22\n" \
"v_mfma_f32_16x16x32_bf16 a[124:127], v[140:143], v[76:79], a[124:127]\n" \
"buffer_store_dwordx4 v[220:223], v20, s[80:83], s89 offen offset:0 sc1\n" \
"v_mfma_f32_16x16x32_bf16 a[124:127], v[144:147], v[80:83], a[124:127]\n" \
"s_cmp_lt_u32 %[w], 3\n" \
"s_cselect_b32 s98, s95, 0x12000\n" \
"v_mfma_f32_16x16x32_bf16 a[0:3], v[128:131], v[80:83], a[0:3]\n" \
"s_add_u32 s66, 0x20000, s95\n" \
"v_writelane_b32 v11, s98, 18\n" \
"v_mfma_f32_16x16x32_bf16 a[0:3], v[124:127], v[76:79], a[0:3]\n" \
"s_cmp_lt_u32 %[w], 3\n" \
"s_cselect_b32 s100, s66, 0xfc00\n" \
"v_mfma_f32_16x16x32_bf16 a[4:7], v[116:119], v[76:79], a[4:7]\n" \
"v_writelane_b32 v11, s95, 19\n" \
"s_lshl_b32 s101, %[w], 10\n" \
"v_mfma_f32_16x16x32_bf16 a[4:7], v[120:123], v[80:83], a[4:7]\n" \
"s_lshr_b32 s65, s96, 1\n" \
"v_writelane_b32 v11, s100, 20\n" \
"v_mfma_f32_16x16x32_bf16 a[32:35], v[80:83], v[136:139], a[32:35]\n" \
"s_cmp_eq_u32 %[w], s65\n" \
"s_cselect_b32 s97, 0xff0000, 0\n" \
"v_mfma_f32_16x16x32_bf16 a[32:35], v[76:79], v[132:135], a[32:35]\n" \
"s_add_u32 s64, 0x1d400, s101\n" \
"s_waitcnt lgkmcnt(0)\n" \
"s_nop 0\n" \
"v_mfma_f32_16x16x32_bf16 a[128:131], v[148:151], v[52:55], a[128:131]\n" \
"v_writelane_b32 v11, s64, 21\n" \
"v_writelane_b32 v11, s97, 6\n" \
"v_mfma_f32_16x16x32_bf16 a[128:131], v[152:155], v[56:59], a[128:131]\n" \
"v_mfma_f32_16x16x32_bf16 a[132:135], v[160:163], v[56:59], a[132:135]\n" \
"v_mfma_f32_16x16x32_bf16 a[132:135], v[156:159], v[52:55], a[132:135]\n" \
"v_mfma_f32_16x16x32_bf16 a[136:139], v[164:167], v[52:55], a[136:139]\n" \
"v_mfma_f32_16x16x32_bf16 a[136:139], v[168:171], v[56:59], a[136:139]\n" \
"v_mfma_f32_16x16x32_bf16 a[140:143], v[176:179], v[56:59], a[140:143]\n" \
"v_mfma_f32_16x16x32_bf16 a[140:143], v[172:175], v[52:55], a[140:143]\n" \
"v_mfma_f32_16x16x32_bf16 a[160:163], v[148:151], v[60:63], a[160:163]\n" \
"v_mfma_f32_16x16x32_bf16 a[160:163], v[152:155], v[64:67], a[160:163]\n" \
"v_mfma_f32_16x16x32_bf16 a[164:167], v[160:163], v[64:67], a[164:167]\n" \
"v_mfma_f32_16x16x32_bf16 a[164:167], v[156:159], v[60:63], a[164:167]\n" \
"v_mfma_f32_16x16x32_bf16 a[168:171], v[164:167], v[60:63], a[168:171]\n" \
"v_mfma_f32_16x16x32_bf16 a[168:171], v[168:171], v[64:67], a[168:171]\n" \
"v_mfma_f32_16x16x32_bf16 a[172:175], v[176:179], v[64:67], a[172:175]\n" \
"v_mfma_f32_16x16x32_bf16 a[172:175], v[172:175], v[60:63], a[172:175]\n" \
"v_mfma_f32_16x16x32_bf16 a[192:195], v[148:151], v[68:71], a[192:195]\n" \
"v_mfma_f32_16x16x32_bf16 a[192:195], v[152:155], v[72:75], a[192:195]\n" \
"s_waitcnt vmcnt(3)\n" \
"s_barrier\n" \
"v_mfma_f32_16x16x32_bf16 a[196:199], v[160:163], v[72:75], a[196:199]\n" \
"s_add_u32 m0, s40, 0x0\n" \
"s_nop 0\n" \
"s_add_u32 s85, s37, 0x8000\n" \
"v_mfma_f32_16x16x32_bf16 a[196:199], v[156:159], v[68:71], a[196:199]\n" \
"buffer_load_dwordx4 v20, s[80:83], s85 offen sc1 lds\n" \
"v_mfma_f32_16x16x32_bf16 a[200:203], v[164:167], v[68:71], a[200:203]\n" \
"s_add_u32 m0, s40, 0x1000\n" \
"s_add_u32 s84, s37, 0x9000\n" \
"v_mfma_f32_16x16x32_bf16 a[200:203], v[168:171], v[72:75], a[200:203]\n" \
"ds_read_b64_tr_b16 v[140:141], v39 offset:32768\n" \
"ds_read_b64_tr_b16 v[142:143], v39 offset:33280\n" \
"v_mfma_f32_16x16x32_bf16 a[204:207], v[176:179], v[72:75], a[204:207]\n" \
"buffer_load_dwordx4 v20, s[80:83], s84 offen sc1 lds\n" \
"v_mfma_f32_16x16x32_bf16 a[204:207], v[172:175], v[68:71], a[204:207]\n" \
"ds_read_b64_tr_b16 v[144:145], v39 offset:36864\n" \
"ds_read_b64_tr_b16 v[146:147], v39 offset:37376\n" \
"v_mfma_f32_16x16x32_bf16 a[224:227], v[148:151], v[76:79], a[224:227]\n" \
"s_add_u32 m0, s40, 0x3000\n" \
"s_mov_b32 s85, 0x200\n" \
"v_mfma_f32_16x16x32_bf16 a[224:227], v[152:155], v[80:83], a[224:227]\n" \
"buffer_load_dwordx4 v21, s[76:79], s85 offen lds\n" \
"v_mfma_f32_16x16x32_bf16 a[228:231], v[160:163], v[80:83], a[228:231]\n" \
"ds_read_b64_tr_b16 v[128:129], v37 offset:36864\n" \
"ds_read_b64_tr_b16 v[130:131], v37 offset:37376\n" \
"v_mfma_f32_16x16x32_bf16 a[228:231], v[156:159], v[76:79], a[228:231]\n" \
"ds_read_b64_tr_b16 v[124:125], v37 offset:32768\n" \
"ds_read_b64_tr_b16 v[126:127], v37 offset:33280\n" \
"v_mfma_f32_16x16x32_bf16 a[232:235], v[164:167], v[76:79], a[232:235]\n" \
"s_add_u32 m0, %[mA], 0x0\n" \
"s_nop 0\n" \
"s_mov_b32 s84, 0x200\n" \
"v_mfma_f32_16x16x32_bf16 a[232:235], v[168:171], v[80:83], a[232:235]\n" \
"buffer_load_dwordx4 v24, s[76:79], s84 offen lds\n" \
"v_mfma_f32_16x16x32_bf16 a[236:239], v[176:179], v[80:83], a[236:239]\n" \
"ds_read_b64_tr_b16 v[132:133], v38 offset:32768\n" \
"ds_read_b64_tr_b16 v[134:135], v38 offset:33280\n" \
"v_mfma_f32_16x16x32_bf16 a[236:239], v[172:175], v[76:79], a[236:239]\n" \
"s_add_u32 m0, %[mA], 0x400\n" \
"s_mov_b32 s85, 0x2600\n" \
"v_mfma_f32_16x16x32_bf16 a[144:147], v[148:151], v[84:87], a[144:147]\n" \
"buffer_load_dwordx4 v25, s[76:79], s85 offen lds\n" \
"v_mfma_f32_16x16x32_bf16 a[144:147], v[152:155], v[88:91], a[144:147]\n" \
"ds_read_b64_tr_b16 v[136:137], v38 offset:36864\n" \
"ds_read_b64_tr_b16 v[138:139], v38 offset:37376\n" \
"v_mfma_f32_16x16x32_bf16 a[148:151], v[160:163], v[88:91], a[148:151]\n" \
"ds_read_b128 v[56:59], v29 offset:32768\n" \
"ds_read_b128 v[52:55], v28 offset:32768\n" \
"v_mfma_f32_16x16x32_bf16 a[148:151], v[156:159], v[84:87], a[148:151]\n" \
"s_add_u32 m0, %[mB], 0x0\n" \
"s_nop 0\n" \
"s_mov_b32 s84, 0x200000\n" \
"v_mfma_f32_16x16x32_bf16 a[152:155], v[164:167], v[84:87], a[152:155]\n" \
"buffer_load_dwordx4 v26, s[68:71], s84 offen lds\n" \
"v_mfma_f32_16x16x32_bf16 a[152:155], v[168:171], v[88:91], a[152:155]\n" \
"ds_read_b64_tr_b16 v[120:121], v36 offset:36864\n" \
"ds_read_b64_tr_b16 v[122:123], v36 offset:37376\n" \
"v_mfma_f32_16x16x32_bf16 a[156:159], v[176:179], v[88:91], a[156:159]\n" \
"ds_read_b128 v[88:91], v23 offset:32768\n" \
"L_pf6:\n" \
"s_cbranch_execz L_pf10\n" \
"s_nop 0\n" \
"ds_read_b64_tr_b16 v[116:117], v36 offset:32768\n" \
"v_mfma_f32_16x16x32_bf16 a[156:159], v[172:175], v[84:87], a[156:159]\n" \
"s_add_u32 m0, %[mA], 0x800\n" \
"s_mov_b32 s85, 0x4a00\n" \
"v_mfma_f32_16x16x32_bf16 a[176:179], v[148:151], v[92:95], a[176:179]\n" \
"buffer_load_dwordx4 v24, s[76:79], s85 offen lds\n" \
"v_mfma_f32_16x16x32_bf16 a[176:179], v[152:155], v[96:99], a[176:179]\n" \
"ds_read_b128 v[84:87], v22 offset:32768\n" \
"ds_read_b64_tr_b16 v[118:119], v36 offset:33280\n" \
"v_mfma_f32_16x16x32_bf16 a[180:183], v[160:163], v[96:99], a[180:183]\n" \
"s_add_u32 m0, %[mA], 0xc00\n" \
"s_mov_b32 s84, 0x6e00\n" \
"v_mfma_f32_16x16x32_bf16 a[180:183], v[156:159], v[92:95], a[180:183]\n" \
"buffer_load_dwordx4 v25, s[76:79], s84 offen lds\n" \
"v_mfma_f32_16x16x32_bf16 a[184:187], v[164:167], v[92:95], a[184:187]\n" \
"ds_read_b128 v[64:67], v29 offset:34816\n" \
"ds_read_b128 v[60:63], v28 offset:34816\n" \
"v_mfma_f32_16x16x32_bf16 a[184:187], v[168:171], v[96:99], a[184:187]\n" \
"ds_read_b128 v[72:75], v29 offset:36864\n" \
"ds_read_b128 v[68:71], v28 offset:36864\n" \
"v_mfma_f32_16x16x32_bf16 a[188:191], v[176:179], v[96:99], a[188:191]\n" \
"s_add_u32 m0, %[mB], 0x400\n" \
"s_mov_b32 s85, 0x208000\n" \
"v_mfma_f32_16x16x32_bf16 a[188:191], v[172:175], v[92:95], a[188:191]\n" \
"buffer_load_dwordx4 v26, s[68:71], s85 offen lds\n" \
"v_mfma_f32_16x16x32_bf16 a[208:211], v[148:151], v[100:103], a[208:211]\n" \
"ds_read_b128 v[80:83], v29 offset:38912\n" \
"ds_read_b128 v[76:79], v28 offset:38912\n" \
"v_mfma_f32_16x16x32_bf16 a[208:211], v[152:155], v[104:107], a[208:211]\n" \
"ds_read_b128 v[92:95], v48 offset:34816\n" \
"ds_read_b128 v[96:99], v49 offset:34816\n" \
"v_mfma_f32_16x16x32_bf16 a[212:215], v[160:163], v[104:107], a[212:215]\n" \
"s_add_u32 m0, %[mA], 0x1000\n" \
"s_mov_b32 s84, 0x9200\n" \
"v_mfma_f32_16x16x32_bf16 a[212:215], v[156:159], v[100:103], a[212:215]\n" \
"buffer_load_dwordx4 v24, s[76:79], s84 offen lds\n" \
"v_mfma_f32_16x16x32_bf16 a[216:219], v[164:167], v[100:103], a[216:219]\n" \
"v_mfma_f32_16x16x32_bf16 a[216:219], v[168:171], v[104:107], a[216:219]\n" \
"s_add_u32 m0, %[mA], 0x1400\n" \
"s_mov_b32 s85, 0xb600\n" \
"v_mfma_f32_16x16x32_bf16 a[220:223], v[176:179], v[104:107], a[220:223]\n" \
"buffer_load_dwordx4 v25, s[76:79], s85 offen lds\n" \
"v_mfma_f32_16x16x32_bf16 a[220:223], v[172:175], v[100:103], a[220:223]\n" \
"ds_read_b128 v[100:103], v48 offset:36864\n" \
"ds_read_b128 v[104:107], v49 offset:36864\n" \
"v_mfma_f32_16x16x32_bf16 a[240:243], v[148:151], v[108:111], a[240:243]\n" \
"ds_read_b64_tr_b16 v[148:149], v32 offset:32768\n" \
"ds_read_b64_tr_b16 v[150:151], v32 offset:33792\n" \
"v_mfma_f32_16x16x32_bf16 a[240:243], v[152:155], v[112:115], a[240:243]\n" \
"s_add_u32 m0, %[mB], 0x800\n" \
"s_mov_b32 s84, 0x210000\n" \
"v_mfma_f32_16x16x32_bf16 a[244:247], v[160:163], v[112:115], a[244:247]\n" \
"buffer_load_dwordx4 v27, s[68:71], s84 offen lds\n" \
"v_mfma_f32_16x16x32_bf16 a[244:247], v[156:159], v[108:111], a[244:247]\n" \
"ds_read_b64_tr_b16 v[152:153], v32 offset:40960\n" \
"ds_read_b64_tr_b16 v[154:155], v32 offset:41984\n" \
"v_mfma_f32_16x16x32_bf16 a[248:251], v[164:167], v[108:111], a[248:251]\n" \
"ds_read_b64_tr_b16 v[156:157], v33 offset:32768\n" \
"ds_read_b64_tr_b16 v[158:159], v33 offset:33792\n" \
"v_mfma_f32_16x16x32_bf16 a[248:251], v[168:171], v[112:115], a[248:251]\n" \
"ds_read_b64_tr_b16 v[160:161], v33 offset:40960\n" \
"ds_read_b64_tr_b16 v[162:163], v33 offset:41984\n" \
"v_mfma_f32_16x16x32_bf16 a[252:255], v[176:179], v[112:115], a[252:255]\n" \
"ds_read_b64_tr_b16 v[168:169], v34 offset:40960\n" \
"ds_read_b64_tr_b16 v[170:171], v34 offset:41984\n" \
"v_mfma_f32_16x16x32_bf16 a[252:255], v[172:175], v[108:111], a[252:255]\n" \
"s_add_u32 m0, %[mA], 0x1800\n" \
"s_waitcnt lgkmcnt(14)\n" \
"s_nop 0\n" \
"v_mfma_f32_16x16x32_bf16 a[16:19], v[140:143], v[84:87], a[16:19]\n" \
"s_mov_b32 s84, 0xda00\n" \
"ds_read_b64_tr_b16 v[164:165], v34 offset:32768\n" \
"v_mfma_f32_16x16x32_bf16 a[16:19], v[144:147], v[88:91], a[16:19]\n" \
"buffer_load_dwordx4 v24, s[76:79], s84 offen lds\n" \
"v_mfma_f32_16x16x32_bf16 a[48:51], v[128:131], v[88:91], a[48:51]\n" \
"s_add_u32 m0, %[mA], 0x1c00\n" \
"s_mov_b32 s85, 0xfe00\n" \
"v_mfma_f32_16x16x32_bf16 a[48:51], v[124:127], v[84:87], a[48:51]\n" \
"ds_read_b64_tr_b16 v[166:167], v34 offset:33792\n" \
"ds_read_b64_tr_b16 v[172:173], v35 offset:32768\n" \
"v_mfma_f32_16x16x32_bf16 a[80:83], v[84:87], v[132:135], a[80:83]\n" \
"buffer_load_dwordx4 v25, s[76:79], s85 offen lds\n" \
"v_mfma_f32_16x16x32_bf16 a[80:83], v[88:91], v[136:139], a[80:83]\n" \
"s_add_u32 m0, %[mB], 0xc00\n" \
"s_mov_b32 s84, 0x218000\n" \
"v_mfma_f32_16x16x32_bf16 a[112:115], v[144:147], v[56:59], a[112:115]\n" \
"ds_read_b64_tr_b16 v[174:175], v35 offset:33792\n" \
"ds_read_b64_tr_b16 v[176:177], v35 offset:40960\n" \
"v_mfma_f32_16x16x32_bf16 a[112:115], v[140:143], v[52:55], a[112:115]\n" \
"buffer_load_dwordx4 v27, s[68:71], s84 offen lds\n" \
"v_mfma_f32_16x16x32_bf16 a[20:23], v[124:127], v[52:55], a[20:23]\n" \
"ds_read_b64_tr_b16 v[178:179], v35 offset:41984\n" \
"ds_read_b128 v[84:87], v48 offset:32768\n" \
"v_mfma_f32_16x16x32_bf16 a[20:23], v[128:131], v[56:59], a[20:23]\n" \
"s_add_u32 s88, s42, 0x2000\n" \
"ds_read_b128 v[88:91], v49 offset:32768\n" \
"v_mfma_f32_16x16x32_bf16 a[52:55], v[120:123], v[56:59], a[52:55]\n" \
"buffer_store_dwordx4 v[224:227], v20, s[80:83], s88 offen offset:0 sc1\n" \
"v_mfma_f32_16x16x32_bf16 a[52:55], v[116:119], v[52:55], a[52:55]\n" \
"ds_read_b128 v[108:111], v48 offset:38912\n" \
"ds_read_b128 v[112:115], v49 offset:38912\n" \
"v_mfma_f32_16x16x32_bf16 a[84:87], v[52:55], v[132:135], a[84:87]\n" \
"s_lshr_b32 s96, %[b], 3\n" \
"s_and_b32 s97, 3, s96\n" \
"v_mfma_f32_16x16x32_bf16 a[84:87], v[56:59], v[136:139], a[84:87]\n" \
"s_and_b32 s94, 7, %[b]\n" \
"s_add_u32 s98, 1, s97\n" \
"v_mfma_f32_16x16x32_bf16 a[116:119], v[144:147], v[64:67], a[116:119]\n" \
"s_lshl_b32 s95, s94, 2\n" \
"s_and_b32 s99, 3, s98\n" \
"v_mfma_f32_16x16x32_bf16 a[116:119], v[140:143], v[60:63], a[116:119]\n" \
"s_add_u32 s100, s95, s99\n" \
"s_mul_i32 s101, 0x24000, s100\n" \
"v_mfma_f32_16x16x32_bf16 a[24:27], v[124:127], v[60:63], a[24:27]\n" \
"s_mul_i32 s64, 0x9000, %[w]\n" \
"s_and_b32 s96, 2, %[w]\n" \
"v_mfma_f32_16x16x32_bf16 a[24:27], v[128:131], v[64:67], a[24:27]\n" \
"s_add_u32 s65, s101, s64\n" \
"s_lshr_b32 s66, %[b], 5\n" \
"v_mfma_f32_16x16x32_bf16 a[56:59], v[120:123], v[64:67], a[56:59]\n" \
"s_lshl_b32 s98, s96, 1\n" \
"s_and_b32 s99, 1, %[w]\n" \
"v_mfma_f32_16x16x32_bf16 a[56:59], v[116:119], v[60:63], a[56:59]\n" \
"s_add_u32 s89, s42, 0x3000\n" \
"v_writelane_b32 v11, s65, 23\n" \
"v_mfma_f32_16x16x32_bf16 a[88:91], v[60:63], v[132:135], a[88:91]\n" \
"buffer_store_dwordx4 v[228:231], v20, s[80:83], s89 offen offset:0 sc1\n" \
"v_mfma_f32_16x16x32_bf16 a[88:91], v[64:67], v[136:139], a[88:91]\n" \
"s_lshl_b32 s67, s66, 3\n" \
"s_add_u32 s101, s98, s99\n" \
"v_mfma_f32_16x16x32_bf16 a[120:123], v[144:147], v[72:75], a[120:123]\n" \
"s_add_u32 s65, s67, s101\n" \
"s_mul_i32 s66, 0x24800, s65\n" \
"v_mfma_f32_16x16x32_bf16 a[120:123], v[140:143], v[68:71], a[120:123]\n" \
"s_lshl_b32 s98, s100, 12\n" \
"s_add_u32 s96, 0x3000000, s66\n" \
"v_mfma_f32_16x16x32_bf16 a[28:31], v[124:127], v[68:71], a[28:31]\n" \
"s_add_u32 s99, 0x4800, s98\n" \
"s_add_u32 s65, s96, s99\n" \
"v_mfma_f32_16x16x32_bf16 a[28:31], v[128:131], v[72:75], a[28:31]\n" \
"s_sub_u32 s100, 0x78, s67\n" \
"v_writelane_b32 v11, s65, 26\n" \
"v_mfma_f32_16x16x32_bf16 a[60:63], v[120:123], v[72:75], a[60:63]\n" \
"s_add_u32 s98, s100, s101\n" \
"s_mul_i32 s65, 0x24800, s98\n" \
"v_mfma_f32_16x16x32_bf16 a[60:63], v[116:119], v[68:71], a[60:63]\n" \
"s_add_u32 s67, 0x3000000, s65\n" \
"s_add_u32 s101, 2, s97\n" \
"v_mfma_f32_16x16x32_bf16 a[92:95], v[68:71], v[132:135], a[92:95]\n" \
"s_add_u32 s100, s67, s99\n" \
"s_and_b32 s98, 3, s101\n" \
"v_mfma_f32_16x16x32_bf16 a[92:95], v[72:75], v[136:139], a[92:95]\n" \
"s_add_u32 s88, s42, 0x0\n" \
"v_writelane_b32 v11, s100, 29\n" \
"v_mfma_f32_16x16x32_bf16 a[124:127], v[144:147], v[80:83], a[124:127]\n" \
"buffer_store_dwordx4 v[232:235], v20, s[80:83], s88 offen offset:1024 sc1\n" \
"v_mfma_f32_16x16x32_bf16 a[124:127], v[140:143], v[76:79], a[124:127]\n" \
"s_add_u32 s99, s95, s98\n" \
"s_mul_i32 s100, 0x24000, s99\n" \
"v_mfma_f32_16x16x32_bf16 a[0:3], v[124:127], v[76:79], a[0:3]\n" \
"s_add_u32 s101, s100, s64\n" \
"s_lshl_b32 s98, s99, 12\n" \
"v_mfma_f32_16x16x32_bf16 a[0:3], v[128:131], v[80:83], a[0:3]\n" \
"v_writelane_b32 v11, s101, 24\n" \
"s_add_u32 s100, 0x4800, s98\n" \
"v_mfma_f32_16x16x32_bf16 a[4:7], v[120:123], v[80:83], a[4:7]\n" \
"s_add_u32 s101, s96, s100\n" \
"v_writelane_b32 v11, s101, 27\n" \
"v_mfma_f32_16x16x32_bf16 a[4:7], v[116:119], v[76:79], a[4:7]\n" \
"s_add_u32 s98, 3, s97\n" \
"s_add_u32 s99, s67, s100\n" \
"v_mfma_f32_16x16x32_bf16 a[32:35], v[76:79], v[132:135], a[32:35]\n" \
"s_and_b32 s101, 3, s98\n" \
"v_writelane_b32 v11, s99, 30\n" \
"v_mfma_f32_16x16x32_bf16 a[32:35], v[80:83], v[136:139], a[32:35]\n" \
"s_add_u32 s100, s95, s101\n" \
"s_waitcnt lgkmcnt(0)\n" \
"v_mfma_f32_16x16x32_bf16 a[128:131], v[152:155], v[56:59], a[128:131]\n" \
"s_mul_i32 s99, 0x24000, s100\n" \
"s_lshl_b32 s98, s100, 12\n" \
"v_mfma_f32_16x16x32_bf16 a[128:131], v[148:151], v[52:55], a[128:131]\n" \
"s_add_u32 s97, s99, s64\n" \
"s_add_u32 s95, 0x4800, s98\n" \
"v_mfma_f32_16x16x32_bf16 a[132:135], v[156:159], v[52:55], a[132:135]\n" \
"v_writelane_b32 v11, s97, 25\n" \
"s_add_u32 s101, s96, s95\n" \
"v_mfma_f32_16x16x32_bf16 a[132:135], v[160:163], v[56:59], a[132:135]\n" \
"v_writelane_b32 v11, s101, 28\n" \
"s_add_u32 s99, s67, s95\n" \
"v_mfma_f32_16x16x32_bf16 a[136:139], v[168:171], v[56:59], a[136:139]\n" \
"s_lshl_b32 s64, s94, 11\n" \
"v_writelane_b32 v11, s99, 31\n" \
"v_mfma_f32_16x16x32_bf16 a[136:139], v[164:167], v[52:55], a[136:139]\n" \
"s_add_u32 s97, s96, s64\n" \
"v_writelane_b32 v11, s97, 32\n" \
"v_mfma_f32_16x16x32_bf16 a[140:143], v[172:175], v[52:55], a[140:143]\n" \
"s_and_b32 s99, 1, s94\n" \
"s_add_u32 s100, s67, s64\n" \
"v_mfma_f32_16x16x32_bf16 a[140:143], v[176:179], v[56:59], a[140:143]\n" \
"s_cmp_lg_u32 s99, 0\n" \
"s_cselect_b32 s97, s67, s96\n" \
"v_mfma_f32_16x16x32_bf16 a[160:163], v[152:155], v[64:67], a[160:163]\n" \
"v_writelane_b32 v11, s100, 33\n" \
"s_add_u32 s98, 0x3004000, s66\n" \
"v_mfma_f32_16x16x32_bf16 a[160:163], v[148:151], v[60:63], a[160:163]\n" \
"s_lshr_b32 s95, s94, 1\n" \
"s_add_u32 s64, 0x4000, s97\n" \
"v_mfma_f32_16x16x32_bf16 a[164:167], v[156:159], v[60:63], a[164:167]\n" \
"v_writelane_b32 v11, s98, 34\n" \
"s_add_u32 s101, 0x3004000, s65\n" \
"v_mfma_f32_16x16x32_bf16 a[164:167], v[160:163], v[64:67], a[164:167]\n" \
"s_cmp_eq_u32 %[w], s95\n" \
"s_cselect_b32 s100, s64, 0x40000000\n" \
"v_mfma_f32_16x16x32_bf16 a[168:171], v[168:171], v[64:67], a[168:171]\n" \
"v_writelane_b32 v11, s101, 35\n" \
"v_writelane_b32 v11, s100, 36\n" \
"v_mfma_f32_16x16x32_bf16 a[168:171], v[164:167], v[60:63], a[168:171]\n" \
"v_mfma_f32_16x16x32_bf16 a[172:175], v[172:175], v[60:63], a[172:175]\n" \
"v_mfma_f32_16x16x32_bf16 a[172:175], v[176:179], v[64:67], a[172:175]\n" \
"v_mfma_f32_16x16x32_bf16 a[192:195], v[152:155], v[72:75], a[192:195]\n" \
"v_mfma_f32_16x16x32_bf16 a[192:195], v[148:151], v[68:71], a[192:195]\n" \
"s_waitcnt vmcnt(3)\n" \
"s_barrier\n" \
"v_mfma_f32_16x16x32_bf16 a[196:199], v[156:159], v[68:71], a[196:199]\n" \
"s_add_u32 m0, s40, 0x8000\n" \
"s_add_u32 s85, s37, 0xa000\n" \
"v_mfma_f32_16x16x32_bf16 a[196:199], v[160:163], v[72:75], a[196:199]\n" \
"buffer_load_dwordx4 v20, s[80:83], s85 offen sc1 lds\n" \
"v_mfma_f32_16x16x32_bf16 a[200:203], v[168:171], v[72:75], a[200:203]\n" \
"s_add_u32 m0, s40, 0x9000\n" \
"s_add_u32 s84, s37, 0xb000\n" \
"v_mfma_f32_16x16x32_bf16 a[200:203], v[164:167], v[68:71], a[200:203]\n" \
"ds_read_b64_tr_b16 v[144:145], v39 offset:4096\n" \
"ds_read_b64_tr_b16 v[146:147], v39 offset:4608\n" \
"v_mfma_f32_16x16x32_bf16 a[204:207], v[172:175], v[68:71], a[204:207]\n" \
"buffer_load_dwordx4 v20, s[80:83], s84 offen sc1 lds\n" \
"v_mfma_f32_16x16x32_bf16 a[204:207], v[176:179], v[72:75], a[204:207]\n" \
"ds_read_b64_tr_b16 v[140:141], v39 offset:0\n" \
"ds_read_b64_tr_b16 v[142:143], v39 offset:512\n" \
"v_mfma_f32_16x16x32_bf16 a[224:227], v[152:155], v[80:83], a[224:227]\n" \
"s_add_u32 m0, s40, 0xb000\n" \
"s_mov_b32 s85, 0x280\n" \
"v_mfma_f32_16x16x32_bf16 a[224:227], v[148:151], v[76:79], a[224:227]\n" \
"buffer_load_dwordx4 v21, s[76:79], s85 offen lds\n" \
"v_mfma_f32_16x16x32_bf16 a[228:231], v[156:159], v[76:79], a[228:231]\n" \
"ds_read_b64_tr_b16 v[124:125], v37 offset:0\n" \
"ds_read_b64_tr_b16 v[126:127], v37 offset:512\n" \
"v_mfma_f32_16x16x32_bf16 a[228:231], v[160:163], v[80:83], a[228:231]\n" \
"ds_read_b64_tr_b16 v[128:129], v37 offset:4096\n" \
"ds_read_b64_tr_b16 v[130:131], v37 offset:4608\n" \
"v_mfma_f32_16x16x32_bf16 a[232:235], v[168:171], v[80:83], a[232:235]\n" \
"s_add_u32 m0, %[mA], 0x8000\n" \
"s_mov_b32 s84, 0x280\n" \
"v_mfma_f32_16x16x32_bf16 a[232:235], v[164:167], v[76:79], a[232:235]\n" \
"buffer_load_dwordx4 v24, s[76:79], s84 offen lds\n" \
"v_mfma_f32_16x16x32_bf16 a[236:239], v[172:175], v[76:79], a[236:239]\n" \
"ds_read_b64_tr_b16 v[136:137], v38 offset:4096\n" \
"ds_read_b64_tr_b16 v[138:139], v38 offset:4608\n" \
"v_mfma_f32_16x16x32_bf16 a[236:239], v[176:179], v[80:83], a[236:239]\n" \
"s_add_u32 m0, %[mA], 0x8400\n" \
"s_mov_b32 s85, 0x2680\n" \
"v_mfma_f32_16x16x32_bf16 a[144:147], v[152:155], v[88:91], a[144:147]\n" \
"buffer_load_dwordx4 v25, s[76:79], s85 offen lds\n" \
"v_mfma_f32_16x16x32_bf16 a[144:147], v[148:151], v[84:87], a[144:147]\n" \
"ds_read_b64_tr_b16 v[132:133], v38 offset:0\n" \
"ds_read_b64_tr_b16 v[134:135], v38 offset:512\n" \
"v_mfma_f32_16x16x32_bf16 a[148:151], v[156:159], v[84:87], a[148:151]\n" \
"ds_read_b128 v[52:55], v28 offset:0\n" \
"ds_read_b128 v[56:59], v29 offset:0\n" \
"v_mfma_f32_16x16x32_bf16 a[148:151], v[160:163], v[88:91], a[148:151]\n" \
"s_add_u32 m0, %[mB], 0x8000\n" \
"s_mov_b32 s84, 0x280000\n" \
"v_mfma_f32_16x16x32_bf16 a[152:155], v[168:171], v[88:91], a[152:155]\n" \
"buffer_load_dwordx4 v26, s[68:71], s84 offen lds\n" \
"v_mfma_f32_16x16x32_bf16 a[152:155], v[164:167], v[84:87], a[152:155]\n" \
"ds_read_b64_tr_b16 v[116:117], v36 offset:0\n" \
"ds_read_b64_tr_b16 v[118:119], v36 offset:512\n" \
"v_mfma_f32_16x16x32_bf16 a[156:159], v[172:175], v[84:87], a[156:159]\n" \
"ds_read_b128 v[84:87], v22 offset:0\n" \
"ds_read_b64_tr_b16 v[120:121], v36 offset:4096\n" \
"v_mfma_f32_16x16x32_bf16 a[156:159], v[176:179], v[88:91], a[156:159]\n" \
"s_add_u32 m0, %[mA], 0x8800\n" \
"s_mov_b32 s85, 0x4a80\n" \
"v_mfma_f32_16x16x32_bf16 a[176:179], v[152:155], v[96:99], a[176:179]\n" \
"buffer_load_dwordx4 v24, s[76:79], s85 offen lds\n" \
"v_mfma_f32_16x16x32_bf16 a[176:179], v[148:151], v[92:95], a[176:179]\n" \
"ds_read_b128 v[88:91], v23 offset:0\n" \
"ds_read_b64_tr_b16 v[122:123], v36 offset:4608\n" \
"v_mfma_f32_16x16x32_bf16 a[180:183], v[156:159], v[92:95], a[180:183]\n" \
"s_add_u32 m0, %[mA], 0x8c00\n" \
"s_mov_b32 s84, 0x6e80\n" \
"v_mfma_f32_16x16x32_bf16 a[180:183], v[160:163], v[96:99], a[180:183]\n" \
"buffer_load_dwordx4 v25, s[76:79], s84 offen lds\n" \
"v_mfma_f32_16x16x32_bf16 a[184:187], v[168:171], v[96:99], a[184:187]\n" \
"ds_read_b128 v[60:63], v28 offset:2048\n" \
"ds_read_b128 v[64:67], v29 offset:2048\n" \
"v_mfma_f32_16x16x32_bf16 a[184:187], v[164:167], v[92:95], a[184:187]\n" \
"ds_read_b128 v[68:71], v28 offset:4096\n" \
"ds_read_b128 v[72:75], v29 offset:4096\n" \
"v_mfma_f32_16x16x32_bf16 a[188:191], v[172:175], v[92:95], a[188:191]\n" \
"s_add_u32 m0, %[mB], 0x8400\n" \
"s_mov_b32 s85, 0x288000\n" \
"v_mfma_f32_16x16x32_bf16 a[188:191], v[176:179], v[96:99], a[188:191]\n" \
"buffer_load_dwordx4 v26, s[68:71], s85 offen lds\n" \
"v_mfma_f32_16x16x32_bf16 a[208:211], v[152:155], v[104:107], a[208:211]\n" \
"ds_read_b128 v[76:79], v28 offset:6144\n" \
"ds_read_b128 v[80:83], v29 offset:6144\n" \
"v_mfma_f32_16x16x32_bf16 a[208:211], v[148:151], v[100:103], a[208:211]\n" \
"ds_read_b128 v[92:95], v48 offset:2048\n" \
"ds_read_b128 v[96:99], v49 offset:2048\n" \
"v_mfma_f32_16x16x32_bf16 a[212:215], v[156:159], v[100:103], a[212:215]\n" \
"s_add_u32 m0, %[mA], 0x9000\n" \
"s_mov_b32 s84, 0x9280\n" \
"v_mfma_f32_16x16x32_bf16 a[212:215], v[160:163], v[104:107], a[212:215]\n" \
"buffer_load_dwordx4 v24, s[76:79], s84 offen lds\n" \
"v_mfma_f32_16x16x32_bf16 a[216:219], v[168:171], v[104:107], a[216:219]\n" \
"v_mfma_f32_16x16x32_bf16 a[216:219], v[164:167], v[100:103], a[216:219]\n" \
"s_add_u32 m0, %[mA], 0x9400\n" \
"s_mov_b32 s85, 0xb680\n" \
"v_mfma_f32_16x16x32_bf16 a[220:223], v[172:175], v[100:103], a[220:223]\n" \
"buffer_load_dwordx4 v25, s[76:79], s85 offen lds\n" \
"v_mfma_f32_16x16x32_bf16 a[220:223], v[176:179], v[104:107], a[220:223]\n" \
"ds_read_b128 v[100:103], v48 offset:4096\n" \
"ds_read_b128 v[104:107], v49 offset:4096\n" \
"v_mfma_f32_16x16x32_bf16 a[240:243], v[152:155], v[112:115], a[240:243]\n" \
"ds_read_b64_tr_b16 v[152:153], v32 offset:8192\n" \
"ds_read_b64_tr_b16 v[154:155], v32 offset:9216\n" \
"v_mfma_f32_16x16x32_bf16 a[240:243], v[148:151], v[108:111], a[240:243]\n" \
"s_add_u32 m0, %[mB], 0x8800\n" \
"s_mov_b32 s84, 0x290000\n" \
"v_mfma_f32_16x16x32_bf16 a[244:247], v[156:159], v[108:111], a[244:247]\n" \
"buffer_load_dwordx4 v27, s[68:71], s84 offen lds\n" \
"v_mfma_f32_16x16x32_bf16 a[244:247], v[160:163], v[112:115], a[244:247]\n" \
"ds_read_b64_tr_b16 v[148:149], v32 offset:0\n" \
"ds_read_b64_tr_b16 v[150:151], v32 offset:1024\n" \
"v_mfma_f32_16x16x32_bf16 a[248:251], v[168:171], v[112:115], a[248:251]\n" \
"ds_read_b64_tr_b16 v[160:161], v33 offset:8192\n" \
"ds_read_b64_tr_b16 v[162:163], v33 offset:9216\n" \
"v_mfma_f32_16x16x32_bf16 a[248:251], v[164:167], v[108:111], a[248:251]\n" \
"ds_read_b64_tr_b16 v[156:157], v33 offset:0\n" \
"ds_read_b64_tr_b16 v[158:159], v33 offset:1024\n" \
"v_mfma_f32_16x16x32_bf16 a[252:255], v[172:175], v[108:111], a[252:255]\n" \
"ds_read_b64_tr_b16 v[164:165], v34 offset:0\n" \
"ds_read_b64_tr_b16 v[166:167], v34 offset:1024\n" \
"v_mfma_f32_16x16x32_bf16 a[252:255], v[176:179], v[112:115], a[252:255]\n" \
"s_add_u32 m0, %[mA], 0x9800\n" \
"s_waitcnt lgkmcnt(14)\n" \
"s_nop 0\n" \
"v_mfma_f32_16x16x32_bf16 a[16:19], v[144:147], v[88:91], a[16:19]\n" \
"s_mov_b32 s84, 0xda80\n" \
"ds_read_b64_tr_b16 v[168:169], v34 offset:8192\n" \
"v_mfma_f32_16x16x32_bf16 a[16:19], v[140:143], v[84:87], a[16:19]\n" \
"buffer_load_dwordx4 v24, s[76:79], s84 offen lds\n" \
"v_mfma_f32_16x16x32_bf16 a[48:51], v[124:127], v[84:87], a[48:51]\n" \
"s_add_u32 m0, %[mA], 0x9c00\n" \
"s_mov_b32 s85, 0xfe80\n" \
"v_mfma_f32_16x16x32_bf16 a[48:51], v[128:131], v[88:91], a[48:51]\n" \
"ds_read_b64_tr_b16 v[170:171], v34 offset:9216\n" \
"ds_read_b64_tr_b16 v[176:177], v35 offset:8192\n" \
"v_mfma_f32_16x16x32_bf16 a[80:83], v[88:91], v[136:139], a[80:83]\n" \
"buffer_load_dwordx4 v25, s[76:79], s85 offen lds\n" \
"v_mfma_f32_16x16x32_bf16 a[80:83], v[84:87], v[132:135], a[80:83]\n" \
"s_add_u32 m0, %[mB], 0x8c00\n" \
"s_mov_b32 s84, 0x298000\n" \
"v_mfma_f32_16x16x32_bf16 a[112:115], v[140:143], v[52:55], a[112:115]\n" \
"ds_read_b64_tr_b16 v[178:179], v35 offset:9216\n" \
"ds_read_b64_tr_b16 v[172:173], v35 offset:0\n" \
"v_mfma_f32_16x16x32_bf16 a[112:115], v[144:147], v[56:59], a[112:115]\n" \
"buffer_load_dwordx4 v27, s[68:71], s84 offen lds\n" \
"v_mfma_f32_16x16x32_bf16 a[20:23], v[128:131], v[56:59], a[20:23]\n" \
"ds_read_b64_tr_b16 v[174:175], v35 offset:1024\n" \
"ds_read_b128 v[84:87], v48 offset:0\n" \
"v_mfma_f32_16x16x32_bf16 a[20:23], v[124:127], v[52:55], a[20:23]\n" \
"s_add_u32 s89, s42, 0x1000\n" \
"ds_read_b128 v[88:91], v49 offset:0\n" \
"v_mfma_f32_16x16x32_bf16 a[52:55], v[116:119], v[52:55], a[52:55]\n" \
"buffer_store_dwordx4 v[236:239], v20, s[80:83], s89 offen offset:1024 sc1\n" \
"v_mfma_f32_16x16x32_bf16 a[52:55], v[120:123], v[56:59], a[52:55]\n" \
"ds_read_b128 v[108:111], v48 offset:6144\n" \
"ds_read_b128 v[112:115], v49 offset:6144\n" \
"v_mfma_f32_16x16x32_bf16 a[84:87], v[56:59], v[136:139], a[84:87]\n" \
"s_and_b32 s96, 2, %[w]\n" \
"s_lshl_b32 s97, s96, 1\n" \
"v_mfma_f32_16x16x32_bf16 a[84:87], v[52:55], v[132:135], a[84:87]\n" \
"s_and_b32 s98, 1, %[w]\n" \
"s_lshr_b32 s94, %[b], 5\n" \
"v_mfma_f32_16x16x32_bf16 a[116:119], v[140:143], v[60:63], a[116:119]\n" \
"s_add_u32 s99, s97, s98\n" \
"s_lshl_b32 s95, s94, 7\n" \
"v_mfma_f32_16x16x32_bf16 a[116:119], v[144:147], v[64:67], a[116:119]\n" \
"s_lshl_b32 s100, s99, 4\n" \
"s_add_u32 s101, s95, s100\n" \
"v_mfma_f32_16x16x32_bf16 a[24:27], v[128:131], v[64:67], a[24:27]\n" \
"s_mul_i32 s64, 0x480, s101\n" \
"s_lshr_b32 s65, %[b], 3\n" \
"v_mfma_f32_16x16x32_bf16 a[24:27], v[124:127], v[60:63], a[24:27]\n" \
"s_sub_u32 s99, 0x780, s95\n" \
"s_and_b32 s66, 3, s65\n" \
"v_mfma_f32_16x16x32_bf16 a[56:59], v[116:119], v[60:63], a[56:59]\n" \
"s_add_u32 s101, s99, s100\n" \
"s_mul_i32 s67, 0x120, s66\n" \
"v_mfma_f32_16x16x32_bf16 a[56:59], v[120:123], v[64:67], a[56:59]\n" \
"s_add_u32 s88, s42, 0x2000\n" \
"s_and_b32 s96, 7, %[b]\n" \
"v_mfma_f32_16x16x32_bf16 a[88:91], v[64:67], v[136:139], a[88:91]\n" \
"buffer_store_dwordx4 v[240:243], v20, s[80:83], s88 offen offset:1024 sc1\n" \
"v_mfma_f32_16x16x32_bf16 a[88:91], v[60:63], v[132:135], a[88:91]\n" \
"s_mul_i32 s65, 0x480, s101\n" \
"s_add_u32 s94, s64, s67\n" \
"v_mfma_f32_16x16x32_bf16 a[120:123], v[140:143], v[68:71], a[120:123]\n" \
"s_lshl_b32 s97, s96, 5\n" \
"s_add_u32 s66, s65, s67\n" \
"v_mfma_f32_16x16x32_bf16 a[120:123], v[144:147], v[72:75], a[120:123]\n" \
"s_and_b32 s95, 1, s96\n" \
"s_add_u32 s98, s94, s97\n" \
"v_mfma_f32_16x16x32_bf16 a[28:31], v[128:131], v[72:75], a[28:31]\n" \
"s_cmp_lg_u32 s95, 0\n" \
"s_cselect_b32 s99, s66, s94\n" \
"v_mfma_f32_16x16x32_bf16 a[28:31], v[124:127], v[68:71], a[28:31]\n" \
"v_writelane_b32 v11, s98, 39\n" \
"s_lshr_b32 s98, s96, 1\n" \
"v_mfma_f32_16x16x32_bf16 a[60:63], v[116:119], v[68:71], a[60:63]\n" \
"s_add_u32 s100, 0x100, s99\n" \
"s_add_u32 s64, s66, s97\n" \
"v_mfma_f32_16x16x32_bf16 a[60:63], v[120:123], v[72:75], a[60:63]\n" \
"s_cmp_eq_u32 %[w], s98\n" \
"s_cselect_b32 s101, s100, 0x40000000\n" \
"v_mfma_f32_16x16x32_bf16 a[92:95], v[72:75], v[136:139], a[92:95]\n" \
"s_mov_b32 s94, s67\n" \
"v_writelane_b32 v11, s64, 40\n" \
"v_mfma_f32_16x16x32_bf16 a[92:95], v[68:71], v[132:135], a[92:95]\n" \
"s_add_u32 s89, s42, 0x3000\n" \
"v_writelane_b32 v11, s101, 41\n" \
"v_mfma_f32_16x16x32_bf16 a[124:127], v[140:143], v[76:79], a[124:127]\n" \
"buffer_store_dwordx4 v[244:247], v20, s[80:83], s89 offen offset:1024 sc1\n" \
"v_mfma_f32_16x16x32_bf16 a[124:127], v[144:147], v[80:83], a[124:127]\n" \
"s_add_u32 s96, s16, s94\n" \
"s_addc_u32 s97, s17, 0\n" \
"v_mfma_f32_16x16x32_bf16 a[0:3], v[128:131], v[80:83], a[0:3]\n" \
"s_add_u32 s98, s18, s94\n" \
"s_addc_u32 s99, s19, 0\n" \
"v_mfma_f32_16x16x32_bf16 a[0:3], v[124:127], v[76:79], a[0:3]\n" \
"s_and_b32 s97, s97, 0xffff\n" \
"v_writelane_b32 v11, s96, 7\n" \
"v_mfma_f32_16x16x32_bf16 a[4:7], v[116:119], v[76:79], a[4:7]\n" \
"v_writelane_b32 v11, s97, 8\n" \
"s_and_b32 s99, s99, 0xffff\n" \
"v_mfma_f32_16x16x32_bf16 a[4:7], v[120:123], v[80:83], a[4:7]\n" \
"v_writelane_b32 v11, s98, 9\n" \
"v_writelane_b32 v11, s99, 10\n" \
"v_mfma_f32_16x16x32_bf16 a[32:35], v[80:83], v[136:139], a[32:35]\n" \
"s_cmp_lt_u32 %[w], 3\n" \
"s_cselect_b64 s[96:97], s[96:97], s[98:99]\n" \
"v_mfma_f32_16x16x32_bf16 a[32:35], v[76:79], v[132:135], a[32:35]\n" \
"v_writelane_b32 v11, s96, 13\n" \
"s_waitcnt lgkmcnt(0)\n" \
"s_nop 0\n" \
"v_mfma_f32_16x16x32_bf16 a[128:131], v[148:151], v[52:55], a[128:131]\n" \
"v_writelane_b32 v11, s97, 14\n" \
"v_writelane_b32 v11, s20, 11\n" \
"v_mfma_f32_16x16x32_bf16 a[128:131], v[152:155], v[56:59], a[128:131]\n" \
"s_and_b32 s95, s21, 0xffff\n" \
"v_writelane_b32 v11, s95, 12\n" \
"v_mfma_f32_16x16x32_bf16 a[132:135], v[160:163], v[56:59], a[132:135]\n" \
"v_mfma_f32_16x16x32_bf16 a[132:135], v[156:159], v[52:55], a[132:135]\n" \
"v_mfma_f32_16x16x32_bf16 a[136:139], v[164:167], v[52:55], a[136:139]\n" \
"v_mfma_f32_16x16x32_bf16 a[136:139], v[168:171], v[56:59], a[136:139]\n" \
"v_mfma_f32_16x16x32_bf16 a[140:143], v[176:179], v[56:59], a[140:143]\n" \
"v_mfma_f32_16x16x32_bf16 a[140:143], v[172:175], v[52:55], a[140:143]\n" \
"v_mfma_f32_16x16x32_bf16 a[160:163], v[148:151], v[60:63], a[160:163]\n" \
"v_mfma_f32_16x16x32_bf16 a[160:163], v[152:155], v[64:67], a[160:163]\n" \
"v_mfma_f32_16x16x32_bf16 a[164:167], v[160:163], v[64:67], a[164:167]\n" \
"v_mfma_f32_16x16x32_bf16 a[164:167], v[156:159], v[60:63], a[164:167]\n" \
"v_mfma_f32_16x16x32_bf16 a[168:171], v[164:167], v[60:63], a[168:171]\n" \
"v_mfma_f32_16x16x32_bf16 a[168:171], v[168:171], v[64:67], a[168:171]\n" \
"v_mfma_f32_16x16x32_bf16 a[172:175], v[176:179], v[64:67], a[172:175]\n" \
"v_mfma_f32_16x16x32_bf16 a[172:175], v[172:175], v[60:63], a[172:175]\n" \
"v_mfma_f32_16x16x32_bf16 a[192:195], v[148:151], v[68:71], a[192:195]\n" \
"v_mfma_f32_16x16x32_bf16 a[192:195], v[152:155], v[72:75], a[192:195]\n" \
"s_waitcnt vmcnt(3)\n" \
"s_barrier\n" \
"v_mfma_f32_16x16x32_bf16 a[196:199], v[160:163], v[72:75], a[196:199]\n" \
"s_add_u32 m0, s40, 0x0\n" \
"s_nop 0\n" \
"s_add_u32 s85, s37, 0xc000\n" \
"v_mfma_f32_16x16x32_bf16 a[196:199], v[156:159], v[68:71], a[196:199]\n" \
"buffer_load_dwordx4 v20, s[80:83], s85 offen sc1 lds\n" \
"v_mfma_f32_16x16x32_bf16 a[200:203], v[164:167], v[68:71], a[200:203]\n" \
"s_add_u32 m0, s40, 0x1000\n" \
"s_add_u32 s84, s37, 0xd000\n" \
"v_mfma_f32_16x16x32_bf16 a[200:203], v[168:171], v[72:75], a[200:203]\n" \
"ds_read_b64_tr_b16 v[140:141], v39 offset:32768\n" \
"ds_read_b64_tr_b16 v[142:143], v39 offset:33280\n" \
"v_mfma_f32_16x16x32_bf16 a[204:207], v[176:179], v[72:75], a[204:207]\n" \
"buffer_load_dwordx4 v20, s[80:83], s84 offen sc1 lds\n" \
"v_mfma_f32_16x16x32_bf16 a[204:207], v[172:175], v[68:71], a[204:207]\n" \
"ds_read_b64_tr_b16 v[144:145], v39 offset:36864\n" \
"ds_read_b64_tr_b16 v[146:147], v39 offset:37376\n" \
"v_mfma_f32_16x16x32_bf16 a[224:227], v[148:151], v[76:79], a[224:227]\n" \
"s_add_u32 m0, s40, 0x3000\n" \
"s_mov_b32 s85, 0x300\n" \
"v_mfma_f32_16x16x32_bf16 a[224:227], v[152:155], v[80:83], a[224:227]\n" \
"buffer_load_dwordx4 v21, s[76:79], s85 offen lds\n" \
"v_mfma_f32_16x16x32_bf16 a[228:231], v[160:163], v[80:83], a[228:231]\n" \
"ds_read_b64_tr_b16 v[128:129], v37 offset:36864\n" \
"ds_read_b64_tr_b16 v[130:131], v37 offset:37376\n" \
"v_mfma_f32_16x16x32_bf16 a[228:231], v[156:159], v[76:79], a[228:231]\n" \
"ds_read_b64_tr_b16 v[124:125], v37 offset:32768\n" \
"ds_read_b64_tr_b16 v[126:127], v37 offset:33280\n" \
"v_mfma_f32_16x16x32_bf16 a[232:235], v[164:167], v[76:79], a[232:235]\n" \
"s_add_u32 m0, %[mA], 0x0\n" \
"s_nop 0\n" \
"s_mov_b32 s84, 0x300\n" \
"v_mfma_f32_16x16x32_bf16 a[232:235], v[168:171], v[80:83], a[232:235]\n" \
"buffer_load_dwordx4 v24, s[76:79], s84 offen lds\n" \
"v_mfma_f32_16x16x32_bf16 a[236:239], v[176:179], v[80:83], a[236:239]\n" \
"ds_read_b64_tr_b16 v[132:133], v38 offset:32768\n" \
"ds_read_b64_tr_b16 v[134:135], v38 offset:33280\n" \
"v_mfma_f32_16x16x32_bf16 a[236:239], v[172:175], v[76:79], a[236:239]\n" \
"s_add_u32 m0, %[mA], 0x400\n" \
"s_mov_b32 s85, 0x2700\n" \
"v_mfma_f32_16x16x32_bf16 a[144:147], v[148:151], v[84:87], a[144:147]\n" \
"buffer_load_dwordx4 v25, s[76:79], s85 offen lds\n" \
"v_mfma_f32_16x16x32_bf16 a[144:147], v[152:155], v[88:91], a[144:147]\n" \
"ds_read_b64_tr_b16 v[136:137], v38 offset:36864\n" \
"ds_read_b64_tr_b16 v[138:139], v38 offset:37376\n" \
"v_mfma_f32_16x16x32_bf16 a[148:151], v[160:163], v[88:91], a[148:151]\n" \
"ds_read_b128 v[56:59], v29 offset:32768\n" \
"ds_read_b128 v[52:55], v28 offset:32768\n" \
"v_mfma_f32_16x16x32_bf16 a[148:151], v[156:159], v[84:87], a[148:151]\n" \
"s_add_u32 m0, %[mB], 0x0\n" \
"s_nop 0\n" \
"s_mov_b32 s84, 0x300000\n" \
"v_mfma_f32_16x16x32_bf16 a[152:155], v[164:167], v[84:87], a[152:155]\n" \
"buffer_load_dwordx4 v26, s[68:71], s84 offen lds\n" \
"v_mfma_f32_16x16x32_bf16 a[152:155], v[168:171], v[88:91], a[152:155]\n" \
"ds_read_b64_tr_b16 v[120:121], v36 offset:36864\n" \
"ds_read_b64_tr_b16 v[122:123], v36 offset:37376\n" \
"v_mfma_f32_16x16x32_bf16 a[156:159], v[176:179], v[88:91], a[156:159]\n" \
"ds_read_b128 v[88:91], v23 offset:32768\n" \
"ds_read_b64_tr_b16 v[116:117], v36 offset:32768\n" \
"v_mfma_f32_16x16x32_bf16 a[156:159], v[172:175], v[84:87], a[156:159]\n" \
"s_add_u32 m0, %[mA], 0x800\n" \
"s_mov_b32 s85, 0x4b00\n" \
"v_mfma_f32_16x16x32_bf16 a[176:179], v[148:151], v[92:95], a[176:179]\n" \
"buffer_load_dwordx4 v24, s[76:79], s85 offen lds\n" \
"v_mfma_f32_16x16x32_bf16 a[176:179], v[152:155], v[96:99], a[176:179]\n" \
"ds_read_b128 v[84:87], v22 offset:32768\n" \
"ds_read_b64_tr_b16 v[118:119], v36 offset:33280\n" \
"v_mfma_f32_16x16x32_bf16 a[180:183], v[160:163], v[96:99], a[180:183]\n" \
"s_add_u32 m0, %[mA], 0xc00\n" \
"s_mov_b32 s84, 0x6f00\n" \
"v_mfma_f32_16x16x32_bf16 a[180:183], v[156:159], v[92:95], a[180:183]\n" \
"buffer_load_dwordx4 v25, s[76:79], s84 offen lds\n" \
"v_mfma_f32_16x16x32_bf16 a[184:187], v[164:167], v[92:95], a[184:187]\n" \
"ds_read_b128 v[64:67], v29 offset:34816\n" \
"L_pf7:\n" \
"s_cbranch_execz L_pf11\n" \
"s_nop 0\n" \
"ds_read_b128 v[60:63], v28 offset:34816\n" \
"v_mfma_f32_16x16x32_bf16 a[184:187], v[168:171], v[96:99], a[184:187]\n" \
"ds_read_b128 v[72:75], v29 offset:36864\n" \
"ds_read_b128 v[68:71], v28 offset:36864\n" \
"v_mfma_f32_16x16x32_bf16 a[188:191], v[176:179], v[96:99], a[188:191]\n" \
"s_add_u32 m0, %[mB], 0x400\n" \
"s_mov_b32 s85, 0x308000\n" \
"v_mfma_f32_16x16x32_bf16 a[188:191], v[172:175], v[92:95], a[188:191]\n" \
"buffer_load_dwordx4 v26, s[68:71], s85 offen lds\n" \
"v_mfma_f32_16x16x32_bf16 a[208:211], v[148:151], v[100:103], a[208:211]\n" \
"ds_read_b128 v[80:83], v29 offset:38912\n" \
"ds_read_b128 v[76:79], v28 offset:38912\n" \
"v_mfma_f32_16x16x32_bf16 a[208:211], v[152:155], v[104:107], a[208:211]\n" \
"ds_read_b128 v[92:95], v48 offset:34816\n" \
"ds_read_b128 v[96:99], v49 offset:34816\n" \
"v_mfma_f32_16x16x32_bf16 a[212:215], v[160:163], v[104:107], a[212:215]\n" \
"s_add_u32 m0, %[mA], 0x1000\n" \
"s_mov_b32 s84, 0x9300\n" \
"v_mfma_f32_16x16x32_bf16 a[212:215], v[156:159], v[100:103], a[212:215]\n" \
"buffer_load_dwordx4 v24, s[76:79], s84 offen lds\n" \
"v_mfma_f32_16x16x32_bf16 a[216:219], v[164:167], v[100:103], a[216:219]\n" \
"v_mfma_f32_16x16x32_bf16 a[216:219], v[168:171], v[104:107], a[216:219]\n" \
"s_add_u32 m0, %[mA], 0x1400\n" \
"s_mov_b32 s85, 0xb700\n" \
"v_mfma_f32_16x16x32_bf16 a[220:223], v[176:179], v[104:107], a[220:223]\n" \
"buffer_load_dwordx4 v25, s[76:79], s85 offen lds\n" \
"v_mfma_f32_16x16x32_bf16 a[220:223], v[172:175], v[100:103], a[220:223]\n" \
"ds_read_b128 v[100:103], v48 offset:36864\n" \
"ds_read_b128 v[104:107], v49 offset:36864\n" \
"v_mfma_f32_16x16x32_bf16 a[240:243], v[148:151], v[108:111], a[240:243]\n" \
"ds_read_b64_tr_b16 v[148:149], v32 offset:32768\n" \
"ds_read_b64_tr_b16 v[150:151], v32 offset:33792\n" \
"v_mfma_f32_16x16x32_bf16 a[240:243], v[152:155], v[112:115], a[240:243]\n" \
"s_add_u32 m0, %[mB], 0x800\n" \
"s_mov_b32 s84, 0x310000\n" \
"v_mfma_f32_16x16x32_bf16 a[244:247], v[160:163], v[112:115], a[244:247]\n" \
"buffer_load_dwordx4 v27, s[68:71], s84 offen lds\n" \
"v_mfma_f32_16x16x32_bf16 a[244:247], v[156:159], v[108:111], a[244:247]\n" \
"ds_read_b64_tr_b16 v[152:153], v32 offset:40960\n" \
"ds_read_b64_tr_b16 v[154:155], v32 offset:41984\n" \
"v_mfma_f32_16x16x32_bf16 a[248:251], v[164:167], v[108:111], a[248:251]\n" \
"ds_read_b64_tr_b16 v[156:157], v33 offset:32768\n" \
"ds_read_b64_tr_b16 v[158:159], v33 offset:33792\n" \
"v_mfma_f32_16x16x32_bf16 a[248:251], v[168:171], v[112:115], a[248:251]\n" \
"ds_read_b64_tr_b16 v[160:161], v33 offset:40960\n" \
"ds_read_b64_tr_b16 v[162:163], v33 offset:41984\n" \
"v_mfma_f32_16x16x32_bf16 a[252:255], v[176:179], v[112:115], a[252:255]\n" \
"ds_read_b64_tr_b16 v[168:169], v34 offset:40960\n" \
"ds_read_b64_tr_b16 v[170:171], v34 offset:41984\n" \
"v_mfma_f32_16x16x32_bf16 a[252:255], v[172:175], v[108:111], a[252:255]\n" \
"s_add_u32 m0, %[mA], 0x1800\n" \
"s_waitcnt lgkmcnt(14)\n" \
"s_nop 0\n" \
"v_mfma_f32_16x16x32_bf16 a[16:19], v[140:143], v[84:87], a[16:19]\n" \
"s_mov_b32 s84, 0xdb00\n" \
"ds_read_b64_tr_b16 v[164:165], v34 offset:32768\n" \
"v_mfma_f32_16x16x32_bf16 a[16:19], v[144:147], v[88:91], a[16:19]\n" \
"buffer_load_dwordx4 v24, s[76:79], s84 offen lds\n" \
"v_mfma_f32_16x16x32_bf16 a[48:51], v[128:131], v[88:91], a[48:51]\n" \
"s_add_u32 m0, %[mA], 0x1c00\n" \
"s_mov_b32 s85, 0xff00\n" \
"v_mfma_f32_16x16x32_bf16 a[48:51], v[124:127], v[84:87], a[48:51]\n" \
"ds_read_b64_tr_b16 v[166:167], v34 offset:33792\n" \
"ds_read_b64_tr_b16 v[172:173], v35 offset:32768\n" \
"v_mfma_f32_16x16x32_bf16 a[80:83], v[84:87], v[132:135], a[80:83]\n" \
"buffer_load_dwordx4 v25, s[76:79], s85 offen lds\n" \
"v_mfma_f32_16x16x32_bf16 a[80:83], v[88:91], v[136:139], a[80:83]\n" \
"s_add_u32 m0, %[mB], 0xc00\n" \
"s_mov_b32 s84, 0x318000\n" \
"v_mfma_f32_16x16x32_bf16 a[112:115], v[144:147], v[56:59], a[112:115]\n" \
"ds_read_b64_tr_b16 v[174:175], v35 offset:33792\n" \
"ds_read_b64_tr_b16 v[176:177], v35 offset:40960\n" \
"v_mfma_f32_16x16x32_bf16 a[112:115], v[140:143], v[52:55], a[112:115]\n" \
"buffer_load_dwordx4 v27, s[68:71], s84 offen lds\n" \
"v_mfma_f32_16x16x32_bf16 a[20:23], v[124:127], v[52:55], a[20:23]\n" \
"ds_read_b64_tr_b16 v[178:179], v35 offset:41984\n" \
"ds_read_b128 v[84:87], v48 offset:32768\n" \
"v_mfma_f32_16x16x32_bf16 a[20:23], v[128:131], v[56:59], a[20:23]\n" \
"s_add_u32 s88, s43, 0x0\n" \
"ds_read_b128 v[88:91], v49 offset:32768\n" \
"v_mfma_f32_16x16x32_bf16 a[52:55], v[120:123], v[56:59], a[52:55]\n" \
"buffer_store_dwordx4 v[248:251], v20, s[80:83], s88 offen offset:0 sc1\n" \
"v_mfma_f32_16x16x32_bf16 a[52:55], v[116:119], v[52:55], a[52:55]\n" \
"ds_read_b128 v[108:111], v48 offset:38912\n" \
"ds_read_b128 v[112:115], v49 offset:38912\n" \
"v_mfma_f32_16x16x32_bf16 a[84:87], v[52:55], v[132:135], a[84:87]\n" \
"s_and_b32 s95, 1, %[w]\n" \
"s_lshr_b32 s96, %[w], 1\n" \
"v_mfma_f32_16x16x32_bf16 a[84:87], v[56:59], v[136:139], a[84:87]\n" \
"s_lshl_b32 s94, %[w], 11\n" \
"s_lshl_b32 s15, s95, 12\n" \
"v_mfma_f32_16x16x32_bf16 a[116:119], v[144:147], v[64:67], a[116:119]\n" \
"s_lshl_b32 s16, s96, 14\n" \
"s_add_u32 s12, 0xc000, s94\n" \
"v_mfma_f32_16x16x32_bf16 a[116:119], v[140:143], v[60:63], a[116:119]\n" \
"s_add_u32 s97, s16, s15\n" \
"s_and_b32 s65, 7, %[b]\n" \
"v_mfma_f32_16x16x32_bf16 a[24:27], v[124:127], v[60:63], a[24:27]\n" \
"s_lshr_b32 s67, %[b], 3\n" \
"s_xor_b32 s13, 0x8000, s97\n" \
"v_mfma_f32_16x16x32_bf16 a[24:27], v[128:131], v[64:67], a[24:27]\n" \
"s_add_u32 s20, 0x14000, s97\n" \
"s_mul_i32 s98, 0xe000, s96\n" \
"v_mfma_f32_16x16x32_bf16 a[56:59], v[120:123], v[64:67], a[56:59]\n" \
"s_lshr_b32 s100, %[b], 5\n" \
"s_lshl_b32 s66, s65, 2\n" \
"v_mfma_f32_16x16x32_bf16 a[56:59], v[116:119], v[60:63], a[56:59]\n" \
"s_add_u32 s89, s45, 0x1000\n" \
"s_and_b32 s94, 3, s67\n" \
"v_mfma_f32_16x16x32_bf16 a[88:91], v[60:63], v[132:135], a[88:91]\n" \
"buffer_store_dwordx4 v[184:187], v20, s[80:83], s89 offen offset:2048 sc1\n" \
"v_mfma_f32_16x16x32_bf16 a[88:91], v[64:67], v[136:139], a[88:91]\n" \
"s_add_u32 s99, s98, s15\n" \
"s_lshl_b32 s18, s100, 1\n" \
"v_mfma_f32_16x16x32_bf16 a[120:123], v[144:147], v[72:75], a[120:123]\n" \
"s_add_u32 s97, s66, s94\n" \
"s_add_u32 s21, 0x10000, s99\n" \
"v_mfma_f32_16x16x32_bf16 a[120:123], v[140:143], v[68:71], a[120:123]\n" \
"s_add_u32 s101, s18, s96\n" \
"s_lshl_b32 s98, s97, 8\n" \
"v_mfma_f32_16x16x32_bf16 a[28:31], v[124:127], v[68:71], a[28:31]\n" \
"s_lshl_b32 s14, s101, 15\n" \
"s_mov_b32 s64, 0x4000\n" \
"v_mfma_f32_16x16x32_bf16 a[28:31], v[128:131], v[72:75], a[28:31]\n" \
"s_add_u32 s99, 0x5be8020, s98\n" \
"s_lshl_b32 s96, s100, 2\n" \
"v_mfma_f32_16x16x32_bf16 a[60:63], v[120:123], v[72:75], a[60:63]\n" \
"s_cmp_lg_u32 s100, 0\n" \
"s_cselect_b32 s17, s64, 0x8000\n" \
"v_mfma_f32_16x16x32_bf16 a[60:63], v[116:119], v[68:71], a[60:63]\n" \
"s_add_u32 s101, s99, s96\n" \
"s_and_b32 s65, 2, %[w]\n" \
"v_mfma_f32_16x16x32_bf16 a[92:95], v[68:71], v[132:135], a[92:95]\n" \
"s_cmp_eq_u32 %[w], 3\n" \
"s_cselect_b32 s22, s101, 0x40000000\n" \
"v_mfma_f32_16x16x32_bf16 a[92:95], v[72:75], v[136:139], a[92:95]\n" \
"s_add_u32 s88, s45, 0x1000\n" \
"s_lshl_b32 s64, s100, 3\n" \
"v_mfma_f32_16x16x32_bf16 a[124:127], v[144:147], v[80:83], a[124:127]\n" \
"buffer_store_dwordx4 v[252:255], v20, s[80:83], s88 offen offset:3072 sc1\n" \
"v_mfma_f32_16x16x32_bf16 a[124:127], v[140:143], v[76:79], a[124:127]\n" \
"s_lshl_b32 s67, s65, 1\n" \
"s_add_u32 s66, s67, s95\n" \
"v_mfma_f32_16x16x32_bf16 a[0:3], v[124:127], v[76:79], a[0:3]\n" \
"s_sub_u32 s101, 0x78, s64\n" \
"s_add_u32 s65, s101, s66\n" \
"v_mfma_f32_16x16x32_bf16 a[0:3], v[128:131], v[80:83], a[0:3]\n" \
"s_add_u32 s94, s64, s66\n" \
"s_mul_i32 s67, 0x24800, s65\n" \
"v_mfma_f32_16x16x32_bf16 a[4:7], v[120:123], v[80:83], a[4:7]\n" \
"s_lshl_b32 s99, s97, 12\n" \
"s_mov_b32 s64, 0xaaaaaaaa\n" \
"v_mfma_f32_16x16x32_bf16 a[4:7], v[116:119], v[76:79], a[4:7]\n" \
"s_mov_b32 s65, 0xaaaaaaaa\n" \
"s_add_u32 s95, s67, s99\n" \
"v_mfma_f32_16x16x32_bf16 a[32:35], v[76:79], v[132:135], a[32:35]\n" \
"s_mov_b32 s66, 0xfff0ffff\n" \
"s_mov_b32 s67, 0xf000ff00\n" \
"v_mfma_f32_16x16x32_bf16 a[32:35], v[80:83], v[136:139], a[32:35]\n" \
"s_mul_i32 s98, 0x24800, s94\n" \
"s_waitcnt lgkmcnt(0)\n" \
"v_mfma_f32_16x16x32_bf16 a[128:131], v[152:155], v[56:59], a[128:131]\n" \
"s_add_u32 s96, s98, s99\n" \
"s_lshl_b32 s98, s97, 20\n" \
"v_mfma_f32_16x16x32_bf16 a[128:131], v[148:151], v[52:55], a[128:131]\n" \
"s_lshl_b32 s94, s100, 16\n" \
"s_add_u32 s8, 0x1000000, s98\n" \
"v_mfma_f32_16x16x32_bf16 a[132:135], v[156:159], v[52:55], a[132:135]\n" \
"s_sub_u32 s19, 14, s18\n" \
"s_add_u32 s60, 0x3004800, s96\n" \
"v_mfma_f32_16x16x32_bf16 a[132:135], v[160:163], v[56:59], a[132:135]\n" \
"s_add_u32 s61, 0x3004800, s95\n" \
"s_sub_u32 s10, 0x100000, s94\n" \
"v_mfma_f32_16x16x32_bf16 a[136:139], v[168:171], v[56:59], a[136:139]\n" \
"s_mov_b32 s11, 0x20000\n" \
"s_mov_b32 s58, 0x6000000\n" \
"v_mfma_f32_16x16x32_bf16 a[136:139], v[164:167], v[52:55], a[136:139]\n" \
"s_mov_b32 s59, 0x20000\n" \
"s_mov_b64 s[56:57], s[80:81]\n" \
"v_mfma_f32_16x16x32_bf16 a[140:143], v[172:175], v[52:55], a[140:143]\n" \
"s_add_u32 s8, s8, s80\n" \
"s_addc_u32 s9, s81, 0\n" \
"s_nop 0\n" \
"v_mfma_f32_16x16x32_bf16 a[140:143], v[176:179], v[56:59], a[140:143]\n" \
"v_mfma_f32_16x16x32_bf16 a[160:163], v[152:155], v[64:67], a[160:163]\n" \
"v_mfma_f32_16x16x32_bf16 a[160:163], v[148:151], v[60:63], a[160:163]\n" \
"v_mfma_f32_16x16x32_bf16 a[164:167], v[156:159], v[60:63], a[164:167]\n" \
"v_mfma_f32_16x16x32_bf16 a[164:167], v[160:163], v[64:67], a[164:167]\n" \
"v_mfma_f32_16x16x32_bf16 a[168:171], v[168:171], v[64:67], a[168:171]\n" \
"v_mfma_f32_16x16x32_bf16 a[168:171], v[164:167], v[60:63], a[168:171]\n" \
"v_mfma_f32_16x16x32_bf16 a[172:175], v[172:175], v[60:63], a[172:175]\n" \
"v_mfma_f32_16x16x32_bf16 a[172:175], v[176:179], v[64:67], a[172:175]\n" \
"v_mfma_f32_16x16x32_bf16 a[192:195], v[152:155], v[72:75], a[192:195]\n" \
"v_mfma_f32_16x16x32_bf16 a[192:195], v[148:151], v[68:71], a[192:195]\n" \
"s_waitcnt vmcnt(3)\n" \
"s_barrier\n" \
"s_mov_b64 exec, 1\n" \
"buffer_store_dword v45, off, s[80:83], s38 offset:0 sc1\n" \
"s_mov_b64 exec, -1\n" \
"v_mfma_f32_16x16x32_bf16 a[196:199], v[156:159], v[68:71], a[196:199]\n" \
"s_add_u32 m0, s40, 0x8000\n" \
"s_add_u32 s85, s37, 0xe000\n" \
"v_mfma_f32_16x16x32_bf16 a[196:199], v[160:163], v[72:75], a[196:199]\n" \
"ds_read_b64_tr_b16 v[144:145], v39 offset:4096\n" \
"ds_read_b64_tr_b16 v[146:147], v39 offset:4608\n" \
"v_mfma_f32_16x16x32_bf16 a[200:203], v[168:171], v[72:75], a[200:203]\n" \
"buffer_load_dwordx4 v20, s[80:83], s85 offen sc1 lds\n" \
"v_mfma_f32_16x16x32_bf16 a[200:203], v[164:167], v[68:71], a[200:203]\n" \
"s_add_u32 m0, s40, 0x9000\n" \
"s_add_u32 s84, s37, 0xf000\n" \
"v_mfma_f32_16x16x32_bf16 a[204:207], v[172:175], v[68:71], a[204:207]\n" \
"ds_read_b64_tr_b16 v[140:141], v39 offset:0\n" \
"ds_read_b64_tr_b16 v[142:143], v39 offset:512\n" \
"v_mfma_f32_16x16x32_bf16 a[204:207], v[176:179], v[72:75], a[204:207]\n" \
"buffer_load_dwordx4 v20, s[80:83], s84 offen sc1 lds\n" \
"v_mfma_f32_16x16x32_bf16 a[224:227], v[152:155], v[80:83], a[224:227]\n" \
"s_add_u32 m0, s40, 0xb000\n" \
"s_mov_b32 s85, 0x380\n" \
"v_mfma_f32_16x16x32_bf16 a[224:227], v[148:151], v[76:79], a[224:227]\n" \
"ds_read_b64_tr_b16 v[124:125], v37 offset:0\n" \
"ds_read_b64_tr_b16 v[126:127], v37 offset:512\n" \
"v_mfma_f32_16x16x32_bf16 a[228:231], v[156:159], v[76:79], a[228:231]\n" \
"buffer_load_dwordx4 v21, s[76:79], s85 offen lds\n" \
"v_mfma_f32_16x16x32_bf16 a[228:231], v[160:163], v[80:83], a[228:231]\n" \
"ds_read_b64_tr_b16 v[128:129], v37 offset:4096\n" \
"ds_read_b64_tr_b16 v[130:131], v37 offset:4608\n" \
"v_mfma_f32_16x16x32_bf16 a[232:235], v[168:171], v[80:83], a[232:235]\n" \
"s_add_u32 m0, %[mA], 0x8000\n" \
"s_mov_b32 s84, 0x380\n" \
"v_mfma_f32_16x16x32_bf16 a[232:235], v[164:167], v[76:79], a[232:235]\n" \
"buffer_load_dwordx4 v24, s[76:79], s84 offen lds\n" \
"v_mfma_f32_16x16x32_bf16 a[236:239], v[172:175], v[76:79], a[236:239]\n" \
"ds_read_b64_tr_b16 v[136:137], v38 offset:4096\n" \
"ds_read_b64_tr_b16 v[138:139], v38 offset:4608\n" \
"v_mfma_f32_16x16x32_bf16 a[236:239], v[176:179], v[80:83], a[236:239]\n" \
"s_add_u32 m0, %[mA], 0x8400\n" \
"s_mov_b32 s85, 0x2780\n" \
"v_mfma_f32_16x16x32_bf16 a[144:147], v[152:155], v[88:91], a[144:147]\n" \
"buffer_load_dwordx4 v25, s[76:79], s85 offen lds\n" \
"v_mfma_f32_16x16x32_bf16 a[144:147], v[148:151], v[84:87], a[144:147]\n" \
"ds_read_b64_tr_b16 v[132:133], v38 offset:0\n" \
"ds_read_b64_tr_b16 v[134:135], v38 offset:512\n" \
"v_mfma_f32_16x16x32_bf16 a[148:151], v[156:159], v[84:87], a[148:151]\n" \
"ds_read_b128 v[52:55], v28 offset:0\n" \
"ds_read_b128 v[56:59], v29 offset:0\n" \
"v_mfma_f32_16x16x32_bf16 a[148:151], v[160:163], v[88:91], a[148:151]\n" \
"s_add_u32 m0, %[mB], 0x8000\n" \
"s_mov_b32 s84, 0x380000\n" \
"v_mfma_f32_16x16x32_bf16 a[152:155], v[168:171], v[88:91], a[152:155]\n" \
"buffer_load_dwordx4 v26, s[68:71], s84 offen lds\n" \
"v_mfma_f32_16x16x32_bf16 a[152:155], v[164:167], v[84:87], a[152:155]\n" \
"ds_read_b64_tr_b16 v[116:117], v36 offset:0\n" \
"ds_read_b64_tr_b16 v[118:119], v36 offset:512\n" \
"v_mfma_f32_16x16x32_bf16 a[156:159], v[172:175], v[84:87], a[156:159]\n" \
"ds_read_b128 v[84:87], v22 offset:0\n" \
"ds_read_b64_tr_b16 v[120:121], v36 offset:4096\n" \
"v_mfma_f32_16x16x32_bf16 a[156:159], v[176:179], v[88:91], a[156:159]\n" \
"s_add_u32 m0, %[mA], 0x8800\n" \
"s_mov_b32 s85, 0x4b80\n" \
"v_mfma_f32_16x16x32_bf16 a[176:179], v[152:155], v[96:99], a[176:179]\n" \
"buffer_load_dwordx4 v24, s[76:79], s85 offen lds\n" \
"v_mfma_f32_16x16x32_bf16 a[176:179], v[148:151], v[92:95], a[176:179]\n" \
"ds_read_b128 v[88:91], v23 offset:0\n" \
"ds_read_b64_tr_b16 v[122:123], v36 offset:4608\n" \
"v_mfma_f32_16x16x32_bf16 a[180:183], v[156:159], v[92:95], a[180:183]\n" \
"s_add_u32 m0, %[mA], 0x8c00\n" \
"s_mov_b32 s84, 0x6f80\n" \
"v_mfma_f32_16x16x32_bf16 a[180:183], v[160:163], v[96:99], a[180:183]\n" \
"buffer_load_dwordx4 v25, s[76:79], s84 offen lds\n" \
"v_mfma_f32_16x16x32_bf16 a[184:187], v[168:171], v[96:99], a[184:187]\n" \
"ds_read_b128 v[60:63], v28 offset:2048\n" \
"ds_read_b128 v[64:67], v29 offset:2048\n" \
"v_mfma_f32_16x16x32_bf16 a[184:187], v[164:167], v[92:95], a[184:187]\n" \
"ds_read_b128 v[68:71], v28 offset:4096\n" \
"ds_read_b128 v[72:75], v29 offset:4096\n" \
"v_mfma_f32_16x16x32_bf16 a[188:191], v[172:175], v[92:95], a[188:191]\n" \
"s_add_u32 m0, %[mB], 0x8400\n" \
"s_mov_b32 s85, 0x388000\n" \
"v_mfma_f32_16x16x32_bf16 a[188:191], v[176:179], v[96:99], a[188:191]\n" \
"buffer_load_dwordx4 v26, s[68:71], s85 offen lds\n" \
"v_mfma_f32_16x16x32_bf16 a[208:211], v[152:155], v[104:107], a[208:211]\n" \
"ds_read_b128 v[76:79], v28 offset:6144\n" \
"ds_read_b128 v[80:83], v29 offset:6144\n" \
"v_mfma_f32_16x16x32_bf16 a[208:211], v[148:151], v[100:103], a[208:211]\n" \
"ds_read_b128 v[92:95], v48 offset:2048\n" \
"ds_read_b128 v[96:99], v49 offset:2048\n" \
"v_mfma_f32_16x16x32_bf16 a[212:215], v[156:159], v[100:103], a[212:215]\n" \
"s_add_u32 m0, %[mA], 0x9000\n" \
"s_mov_b32 s84, 0x9380\n" \
"v_mfma_f32_16x16x32_bf16 a[212:215], v[160:163], v[104:107], a[212:215]\n" \
"buffer_load_dwordx4 v24, s[76:79], s84 offen lds\n" \
"v_mfma_f32_16x16x32_bf16 a[216:219], v[168:171], v[104:107], a[216:219]\n" \
"v_mfma_f32_16x16x32_bf16 a[216:219], v[164:167], v[100:103], a[216:219]\n" \
"s_add_u32 m0, %[mA], 0x9400\n" \
"s_mov_b32 s85, 0xb780\n" \
"v_mfma_f32_16x16x32_bf16 a[220:223], v[172:175], v[100:103], a[220:223]\n" \
"buffer_load_dwordx4 v25, s[76:79], s85 offen lds\n" \
"v_mfma_f32_16x16x32_bf16 a[220:223], v[176:179], v[104:107], a[220:223]\n" \
"ds_read_b128 v[100:103], v48 offset:4096\n" \
"ds_read_b128 v[104:107], v49 offset:4096\n" \
"v_mfma_f32_16x16x32_bf16 a[240:243], v[152:155], v[112:115], a[240:243]\n" \
"ds_read_b64_tr_b16 v[152:153], v32 offset:8192\n" \
"ds_read_b64_tr_b16 v[154:155], v32 offset:9216\n" \
"v_mfma_f32_16x16x32_bf16 a[240:243], v[148:151], v[108:111], a[240:243]\n" \
"s_add_u32 m0, %[mB], 0x8800\n" \
"s_mov_b32 s84, 0x390000\n" \
"v_mfma_f32_16x16x32_bf16 a[244:247], v[156:159], v[108:111], a[244:247]\n" \
"buffer_load_dwordx4 v27, s[68:71], s84 offen lds\n" \
"v_mfma_f32_16x16x32_bf16 a[244:247], v[160:163], v[112:115], a[244:247]\n" \
"ds_read_b64_tr_b16 v[148:149], v32 offset:0\n" \
"ds_read_b64_tr_b16 v[150:151], v32 offset:1024\n" \
"v_mfma_f32_16x16x32_bf16 a[248:251], v[168:171], v[112:115], a[248:251]\n" \
"ds_read_b64_tr_b16 v[160:161], v33 offset:8192\n" \
"ds_read_b64_tr_b16 v[162:163], v33 offset:9216\n" \
"v_mfma_f32_16x16x32_bf16 a[248:251], v[164:167], v[108:111], a[248:251]\n" \
"ds_read_b64_tr_b16 v[156:157], v33 offset:0\n" \
"ds_read_b64_tr_b16 v[158:159], v33 offset:1024\n" \
"v_mfma_f32_16x16x32_bf16 a[252:255], v[172:175], v[108:111], a[252:255]\n" \
"ds_read_b64_tr_b16 v[164:165], v34 offset:0\n" \
"ds_read_b64_tr_b16 v[166:167], v34 offset:1024\n" \
"v_mfma_f32_16x16x32_bf16 a[252:255], v[176:179], v[112:115], a[252:255]\n" \
"s_add_u32 m0, %[mA], 0x9800\n" \
"s_waitcnt lgkmcnt(14)\n" \
"s_nop 0\n" \
"v_mfma_f32_16x16x32_bf16 a[16:19], v[144:147], v[88:91], a[16:19]\n" \
"s_mov_b32 s84, 0xdb80\n" \
"ds_read_b64_tr_b16 v[168:169], v34 offset:8192\n" \
"v_mfma_f32_16x16x32_bf16 a[16:19], v[140:143], v[84:87], a[16:19]\n" \
"buffer_load_dwordx4 v24, s[76:79], s84 offen lds\n" \
"v_mfma_f32_16x16x32_bf16 a[48:51], v[124:127], v[84:87], a[48:51]\n" \
"s_add_u32 m0, %[mA], 0x9c00\n" \
"s_mov_b32 s85, 0xff80\n" \
"v_mfma_f32_16x16x32_bf16 a[48:51], v[128:131], v[88:91], a[48:51]\n" \
"ds_read_b64_tr_b16 v[170:171], v34 offset:9216\n" \
"ds_read_b64_tr_b16 v[176:177], v35 offset:8192\n" \
"v_mfma_f32_16x16x32_bf16 a[80:83], v[88:91], v[136:139], a[80:83]\n" \
"buffer_load_dwordx4 v25, s[76:79], s85 offen lds\n" \
"v_mfma_f32_16x16x32_bf16 a[80:83], v[84:87], v[132:135], a[80:83]\n" \
"s_add_u32 m0, %[mB], 0x8c00\n" \
"s_mov_b32 s84, 0x398000\n" \
"v_mfma_f32_16x16x32_bf16 a[112:115], v[140:143], v[52:55], a[112:115]\n" \
"ds_read_b64_tr_b16 v[178:179], v35 offset:9216\n" \
"ds_read_b64_tr_b16 v[172:173], v35 offset:0\n" \
"v_mfma_f32_16x16x32_bf16 a[112:115], v[144:147], v[56:59], a[112:115]\n" \
"buffer_load_dwordx4 v27, s[68:71], s84 offen lds\n" \
"v_mfma_f32_16x16x32_bf16 a[20:23], v[128:131], v[56:59], a[20:23]\n" \
"ds_read_b64_tr_b16 v[174:175], v35 offset:1024\n" \
"ds_read_b128 v[84:87], v48 offset:0\n" \
"v_mfma_f32_16x16x32_bf16 a[20:23], v[124:127], v[52:55], a[20:23]\n" \
"s_add_u32 s89, s43, 0x1000\n" \
"ds_read_b128 v[88:91], v49 offset:0\n" \
"v_mfma_f32_16x16x32_bf16 a[52:55], v[116:119], v[52:55], a[52:55]\n" \
"buffer_store_dwordx4 a[36:39], v20, s[80:83], s89 offen offset:0 sc1\n" \
"v_mfma_f32_16x16x32_bf16 a[52:55], v[120:123], v[56:59], a[52:55]\n" \
"ds_read_b128 v[108:111], v48 offset:6144\n" \
"ds_read_b128 v[112:115], v49 offset:6144\n" \
"v_mfma_f32_16x16x32_bf16 a[84:87], v[56:59], v[136:139], a[84:87]\n" \
"v_mfma_f32_16x16x32_bf16 a[84:87], v[52:55], v[132:135], a[84:87]\n" \
"v_mfma_f32_16x16x32_bf16 a[116:119], v[140:143], v[60:63], a[116:119]\n" \
"v_mfma_f32_16x16x32_bf16 a[116:119], v[144:147], v[64:67], a[116:119]\n" \
"v_mfma_f32_16x16x32_bf16 a[24:27], v[128:131], v[64:67], a[24:27]\n" \
"v_mfma_f32_16x16x32_bf16 a[24:27], v[124:127], v[60:63], a[24:27]\n" \
"v_mfma_f32_16x16x32_bf16 a[56:59], v[116:119], v[60:63], a[56:59]\n" \
"v_mfma_f32_16x16x32_bf16 a[56:59], v[120:123], v[64:67], a[56:59]\n" \
"s_add_u32 s88, s43, 0x2000\n" \
"v_mfma_f32_16x16x32_bf16 a[88:91], v[64:67], v[136:139], a[88:91]\n" \
"buffer_store_dwordx4 a[64:67], v20, s[80:83], s88 offen offset:0 sc1\n" \
"v_mfma_f32_16x16x32_bf16 a[88:91], v[60:63], v[132:135], a[88:91]\n" \
"v_mfma_f32_16x16x32_bf16 a[120:123], v[140:143], v[68:71], a[120:123]\n" \
"v_mfma_f32_16x16x32_bf16 a[120:123], v[144:147], v[72:75], a[120:123]\n" \
"v_mfma_f32_16x16x32_bf16 a[28:31], v[128:131], v[72:75], a[28:31]\n" \
"v_mfma_f32_16x16x32_bf16 a[28:31], v[124:127], v[68:71], a[28:31]\n" \
"v_mfma_f32_16x16x32_bf16 a[60:63], v[116:119], v[68:71], a[60:63]\n" \
"v_mfma_f32_16x16x32_bf16 a[60:63], v[120:123], v[72:75], a[60:63]\n" \
"v_mfma_f32_16x16x32_bf16 a[92:95], v[72:75], v[136:139], a[92:95]\n" \
"v_mfma_f32_16x16x32_bf16 a[92:95], v[68:71], v[132:135], a[92:95]\n" \
"s_add_u32 s89, s43, 0x3000\n" \
"v_mfma_f32_16x16x32_bf16 a[124:127], v[140:143], v[76:79], a[124:127]\n" \
"buffer_store_dwordx4 a[68:71], v20, s[80:83], s89 offen offset:0 sc1\n" \
"v_mfma_f32_16x16x32_bf16 a[124:127], v[144:147], v[80:83], a[124:127]\n" \
"v_mfma_f32_16x16x32_bf16 a[0:3], v[128:131], v[80:83], a[0:3]\n" \
"v_mfma_f32_16x16x32_bf16 a[0:3], v[124:127], v[76:79], a[0:3]\n" \
"v_mfma_f32_16x16x32_bf16 a[4:7], v[116:119], v[76:79], a[4:7]\n" \
"v_mfma_f32_16x16x32_bf16 a[4:7], v[120:123], v[80:83], a[4:7]\n" \
"v_mfma_f32_16x16x32_bf16 a[32:35], v[80:83], v[136:139], a[32:35]\n" \
"v_mfma_f32_16x16x32_bf16 a[32:35], v[76:79], v[132:135], a[32:35]\n" \
"s_waitcnt lgkmcnt(0)\n" \
"s_nop 0\n" \
"v_mfma_f32_16x16x32_bf16 a[128:131], v[148:151], v[52:55], a[128:131]\n" \
"v_mfma_f32_16x16x32_bf16 a[128:131], v[152:155], v[56:59], a[128:131]\n" \
"v_mfma_f32_16x16x32_bf16 a[132:135], v[160:163], v[56:59], a[132:135]\n" \
"v_mfma_f32_16x16x32_bf16 a[132:135], v[156:159], v[52:55], a[132:135]\n" \
"v_mfma_f32_16x16x32_bf16 a[136:139], v[164:167], v[52:55], a[136:139]\n" \
"v_mfma_f32_16x16x32_bf16 a[136:139], v[168:171], v[56:59], a[136:139]\n" \
"v_mfma_f32_16x16x32_bf16 a[140:143], v[176:179], v[56:59], a[140:143]\n" \
"v_mfma_f32_16x16x32_bf16 a[140:143], v[172:175], v[52:55], a[140:143]\n" \
"v_mfma_f32_16x16x32_bf16 a[160:163], v[148:151], v[60:63], a[160:163]\n" \
"v_mfma_f32_16x16x32_bf16 a[160:163], v[152:155], v[64:67], a[160:163]\n" \
"v_mfma_f32_16x16x32_bf16 a[164:167], v[160:163], v[64:67], a[164:167]\n" \
"v_mfma_f32_16x16x32_bf16 a[164:167], v[156:159], v[60:63], a[164:167]\n" \
"v_mfma_f32_16x16x32_bf16 a[168:171], v[164:167], v[60:63], a[168:171]\n" \
"v_mfma_f32_16x16x32_bf16 a[168:171], v[168:171], v[64:67], a[168:171]\n" \
"v_mfma_f32_16x16x32_bf16 a[172:175], v[176:179], v[64:67], a[172:175]\n" \
"v_mfma_f32_16x16x32_bf16 a[172:175], v[172:175], v[60:63], a[172:175]\n" \
"v_mfma_f32_16x16x32_bf16 a[192:195], v[148:151], v[68:71], a[192:195]\n" \
"v_mfma_f32_16x16x32_bf16 a[192:195], v[152:155], v[72:75], a[192:195]\n" \
"s_waitcnt vmcnt(3)\n" \
"s_barrier\n" \
"v_mfma_f32_16x16x32_bf16 a[196:199], v[160:163], v[72:75], a[196:199]\n" \
"s_add_u32 m0, s40, 0x0\n" \
"s_nop 0\n" \
"s_add_u32 s85, s37, 0x10000\n" \
"v_mfma_f32_16x16x32_bf16 a[196:199], v[156:159], v[68:71], a[196:199]\n" \
"buffer_load_dwordx4 v20, s[80:83], s85 offen sc1 lds\n" \
"v_mfma_f32_16x16x32_bf16 a[200:203], v[164:167], v[68:71], a[200:203]\n" \
"s_add_u32 m0, s40, 0x1000\n" \
"s_add_u32 s84, s37, 0x11000\n" \
"v_mfma_f32_16x16x32_bf16 a[200:203], v[168:171], v[72:75], a[200:203]\n" \
"ds_read_b64_tr_b16 v[140:141], v39 offset:32768\n" \
"ds_read_b64_tr_b16 v[142:143], v39 offset:33280\n" \
"v_mfma_f32_16x16x32_bf16 a[204:207], v[176:179], v[72:75], a[204:207]\n" \
"buffer_load_dwordx4 v20, s[80:83], s84 offen sc1 lds\n" \
"v_mfma_f32_16x16x32_bf16 a[204:207], v[172:175], v[68:71], a[204:207]\n" \
"ds_read_b64_tr_b16 v[144:145], v39 offset:36864\n" \
"ds_read_b64_tr_b16 v[146:147], v39 offset:37376\n" \
"v_mfma_f32_16x16x32_bf16 a[224:227], v[148:151], v[76:79], a[224:227]\n" \
"s_add_u32 m0, s40, 0x3000\n" \
"s_mov_b32 s85, 0x400\n" \
"v_mfma_f32_16x16x32_bf16 a[224:227], v[152:155], v[80:83], a[224:227]\n" \
"buffer_load_dwordx4 v21, s[76:79], s85 offen lds\n" \
"v_mfma_f32_16x16x32_bf16 a[228:231], v[160:163], v[80:83], a[228:231]\n" \
"ds_read_b64_tr_b16 v[128:129], v37 offset:36864\n" \
"ds_read_b64_tr_b16 v[130:131], v37 offset:37376\n" \
"v_mfma_f32_16x16x32_bf16 a[228:231], v[156:159], v[76:79], a[228:231]\n" \
"ds_read_b64_tr_b16 v[124:125], v37 offset:32768\n" \
"ds_read_b64_tr_b16 v[126:127], v37 offset:33280\n" \
"v_mfma_f32_16x16x32_bf16 a[232:235], v[164:167], v[76:79], a[232:235]\n" \
"s_add_u32 m0, %[mA], 0x0\n" \
"s_nop 0\n" \
"s_mov_b32 s84, 0x400\n" \
"v_mfma_f32_16x16x32_bf16 a[232:235], v[168:171], v[80:83], a[232:235]\n" \
"buffer_load_dwordx4 v24, s[76:79], s84 offen lds\n" \
"v_mfma_f32_16x16x32_bf16 a[236:239], v[176:179], v[80:83], a[236:239]\n" \
"ds_read_b64_tr_b16 v[132:133], v38 offset:32768\n" \
"ds_read_b64_tr_b16 v[134:135], v38 offset:33280\n" \
"v_mfma_f32_16x16x32_bf16 a[236:239], v[172:175], v[76:79], a[236:239]\n" \
"s_add_u32 m0, %[mA], 0x400\n" \
"s_mov_b32 s85, 0x2800\n" \
"v_mfma_f32_16x16x32_bf16 a[144:147], v[148:151], v[84:87], a[144:147]\n" \
"buffer_load_dwordx4 v25, s[76:79], s85 offen lds\n" \
"v_mfma_f32_16x16x32_bf16 a[144:147], v[152:155], v[88:91], a[144:147]\n" \
"ds_read_b64_tr_b16 v[136:137], v38 offset:36864\n" \
"ds_read_b64_tr_b16 v[138:139], v38 offset:37376\n" \
"v_mfma_f32_16x16x32_bf16 a[148:151], v[160:163], v[88:91], a[148:151]\n" \
"ds_read_b128 v[56:59], v29 offset:32768\n" \
"ds_read_b128 v[52:55], v28 offset:32768\n" \
"v_mfma_f32_16x16x32_bf16 a[148:151], v[156:159], v[84:87], a[148:151]\n" \
"s_add_u32 m0, %[mB], 0x0\n" \
"s_nop 0\n" \
"s_mov_b32 s84, 0x400000\n" \
"v_mfma_f32_16x16x32_bf16 a[152:155], v[164:167], v[84:87], a[152:155]\n" \
"buffer_load_dwordx4 v26, s[68:71], s84 offen lds\n" \
"v_mfma_f32_16x16x32_bf16 a[152:155], v[168:171], v[88:91], a[152:155]\n" \
"ds_read_b64_tr_b16 v[120:121], v36 offset:36864\n" \
"ds_read_b64_tr_b16 v[122:123], v36 offset:37376\n" \
"v_mfma_f32_16x16x32_bf16 a[156:159], v[176:179], v[88:91], a[156:159]\n" \
"ds_read_b128 v[88:91], v23 offset:32768\n" \
"ds_read_b64_tr_b16 v[116:117], v36 offset:32768\n" \
"v_mfma_f32_16x16x32_bf16 a[156:159], v[172:175], v[84:87], a[156:159]\n" \
"s_add_u32 m0, %[mA], 0x800\n" \
"s_mov_b32 s85, 0x4c00\n" \
"v_mfma_f32_16x16x32_bf16 a[176:179], v[148:151], v[92:95], a[176:179]\n" \
"buffer_load_dwordx4 v24, s[76:79], s85 offen lds\n" \
"v_mfma_f32_16x16x32_bf16 a[176:179], v[152:155], v[96:99], a[176:179]\n" \
"ds_read_b128 v[84:87], v22 offset:32768\n" \
"ds_read_b64_tr_b16 v[118:119], v36 offset:33280\n" \
"v_mfma_f32_16x16x32_bf16 a[180:183], v[160:163], v[96:99], a[180:183]\n" \
"s_add_u32 m0, %[mA], 0xc00\n" \
"s_mov_b32 s84, 0x7000\n" \
"v_mfma_f32_16x16x32_bf16 a[180:183], v[156:159], v[92:95], a[180:183]\n" \
"buffer_load_dwordx4 v25, s[76:79], s84 offen lds\n" \
"v_mfma_f32_16x16x32_bf16 a[184:187], v[164:167], v[92:95], a[184:187]\n" \
"ds_read_b128 v[64:67], v29 offset:34816\n" \
"ds_read_b128 v[60:63], v28 offset:34816\n" \
"v_mfma_f32_16x16x32_bf16 a[184:187], v[168:171], v[96:99], a[184:187]\n" \
"ds_read_b128 v[72:75], v29 offset:36864\n" \
"ds_read_b128 v[68:71], v28 offset:36864\n" \
"v_mfma_f32_16x16x32_bf16 a[188:191], v[176:179], v[96:99], a[188:191]\n" \
"s_add_u32 m0, %[mB], 0x400\n" \
"s_mov_b32 s85, 0x408000\n" \
"v_mfma_f32_16x16x32_bf16 a[188:191], v[172:175], v[92:95], a[188:191]\n" \
"buffer_load_dwordx4 v26, s[68:71], s85 offen lds\n" \
"v_mfma_f32_16x16x32_bf16 a[208:211], v[148:151], v[100:103], a[208:211]\n" \
"ds_read_b128 v[80:83], v29 offset:38912\n" \
"ds_read_b128 v[76:79], v28 offset:38912\n" \
"v_mfma_f32_16x16x32_bf16 a[208:211], v[152:155], v[104:107], a[208:211]\n" \
"ds_read_b128 v[92:95], v48 offset:34816\n" \
"ds_read_b128 v[96:99], v49 offset:34816\n" \
"v_mfma_f32_16x16x32_bf16 a[212:215], v[160:163], v[104:107], a[212:215]\n" \
"s_add_u32 m0, %[mA], 0x1000\n" \
"s_mov_b32 s84, 0x9400\n" \
"v_mfma_f32_16x16x32_bf16 a[212:215], v[156:159], v[100:103], a[212:215]\n" \
"buffer_load_dwordx4 v24, s[76:79], s84 offen lds\n" \
"v_mfma_f32_16x16x32_bf16 a[216:219], v[164:167], v[100:103], a[216:219]\n" \
"v_and_b32_e32 v26, 0x1ff, v20\n" \
"v_add_u32_e64 v21, s48, v20\n" \
"v_mfma_f32_16x16x32_bf16 a[216:219], v[168:171], v[104:107], a[216:219]\n" \
"s_add_u32 m0, %[mA], 0x1400\n" \
"s_mov_b32 s85, 0xb800\n" \
"v_mfma_f32_16x16x32_bf16 a[220:223], v[176:179], v[104:107], a[220:223]\n" \
"buffer_load_dwordx4 v25, s[76:79], s85 offen lds\n" \
"v_mfma_f32_16x16x32_bf16 a[220:223], v[172:175], v[100:103], a[220:223]\n" \
"ds_read_b128 v[100:103], v48 offset:36864\n" \
"ds_read_b128 v[104:107], v49 offset:36864\n" \
"v_mfma_f32_16x16x32_bf16 a[240:243], v[148:151], v[108:111], a[240:243]\n" \
"ds_read_b64_tr_b16 v[148:149], v32 offset:32768\n" \
"ds_read_b64_tr_b16 v[150:151], v32 offset:33792\n" \
"v_mfma_f32_16x16x32_bf16 a[240:243], v[152:155], v[112:115], a[240:243]\n" \
"s_add_u32 m0, %[mB], 0x800\n" \
"s_mov_b32 s84, 0x410000\n" \
"v_mfma_f32_16x16x32_bf16 a[244:247], v[160:163], v[112:115], a[244:247]\n" \
"buffer_load_dwordx4 v27, s[68:71], s84 offen lds\n" \
"v_mfma_f32_16x16x32_bf16 a[244:247], v[156:159], v[108:111], a[244:247]\n" \
"ds_read_b64_tr_b16 v[152:153], v32 offset:40960\n" \
"ds_read_b64_tr_b16 v[154:155], v32 offset:41984\n" \
"v_mfma_f32_16x16x32_bf16 a[248:251], v[164:167], v[108:111], a[248:251]\n" \
"ds_read_b64_tr_b16 v[156:157], v33 offset:32768\n" \
"ds_read_b64_tr_b16 v[158:159], v33 offset:33792\n" \
"v_mfma_f32_16x16x32_bf16 a[248:251], v[168:171], v[112:115], a[248:251]\n" \
"ds_read_b64_tr_b16 v[160:161], v33 offset:40960\n" \
"ds_read_b64_tr_b16 v[162:163], v33 offset:41984\n" \
"v_mfma_f32_16x16x32_bf16 a[252:255], v[176:179], v[112:115], a[252:255]\n" \
"ds_read_b64_tr_b16 v[168:169], v34 offset:40960\n" \
"ds_read_b64_tr_b16 v[170:171], v34 offset:41984\n" \
"v_mfma_f32_16x16x32_bf16 a[252:255], v[172:175], v[108:111], a[252:255]\n" \
"s_add_u32 m0, %[mA], 0x1800\n" \
"s_waitcnt lgkmcnt(14)\n" \
"s_nop 0\n" \
"v_mfma_f32_16x16x32_bf16 a[16:19], v[140:143], v[84:87], a[16:19]\n" \
"s_mov_b32 s84, 0xdc00\n" \
"ds_read_b64_tr_b16 v[164:165], v34 offset:32768\n" \
"L_pf8:\n" \
"s_cbranch_execz L_pf12\n" \
"s_nop 0\n" \
"v_mfma_f32_16x16x32_bf16 a[16:19], v[144:147], v[88:91], a[16:19]\n" \
"buffer_load_dwordx4 v24, s[76:79], s84 offen lds\n" \
"v_mfma_f32_16x16x32_bf16 a[48:51], v[128:131], v[88:91], a[48:51]\n" \
"s_add_u32 m0, %[mA], 0x1c00\n" \
"s_mov_b32 s85, 0x10000\n" \
"v_mfma_f32_16x16x32_bf16 a[48:51], v[124:127], v[84:87], a[48:51]\n" \
"ds_read_b64_tr_b16 v[166:167], v34 offset:33792\n" \
"ds_read_b64_tr_b16 v[172:173], v35 offset:32768\n" \
"v_mfma_f32_16x16x32_bf16 a[80:83], v[84:87], v[132:135], a[80:83]\n" \
"buffer_load_dwordx4 v25, s[76:79], s85 offen lds\n" \
"s_mov_b32 s76, s54\n" \
"s_nop 0\n" \
"v_mfma_f32_16x16x32_bf16 a[80:83], v[88:91], v[136:139], a[80:83]\n" \
"s_add_u32 m0, %[mB], 0xc00\n" \
"s_mov_b32 s84, 0x418000\n" \
"v_mfma_f32_16x16x32_bf16 a[112:115], v[144:147], v[56:59], a[112:115]\n" \
"ds_read_b64_tr_b16 v[174:175], v35 offset:33792\n" \
"ds_read_b64_tr_b16 v[176:177], v35 offset:40960\n" \
"v_mfma_f32_16x16x32_bf16 a[112:115], v[140:143], v[52:55], a[112:115]\n" \
"buffer_load_dwordx4 v27, s[68:71], s84 offen lds\n" \
"v_mfma_f32_16x16x32_bf16 a[20:23], v[124:127], v[52:55], a[20:23]\n" \
"ds_read_b64_tr_b16 v[178:179], v35 offset:41984\n" \
"ds_read_b128 v[84:87], v48 offset:32768\n" \
"v_mfma_f32_16x16x32_bf16 a[20:23], v[128:131], v[56:59], a[20:23]\n" \
"s_add_u32 s88, s43, 0x0\n" \
"s_nop 0\n" \
"ds_read_b128 v[88:91], v49 offset:32768\n" \
"v_mfma_f32_16x16x32_bf16 a[52:55], v[120:123], v[56:59], a[52:55]\n" \
"buffer_store_dwordx4 a[96:99], v20, s[80:83], s88 offen offset:1024 sc1\n" \
"v_mfma_f32_16x16x32_bf16 a[52:55], v[116:119], v[52:55], a[52:55]\n" \
"ds_read_b128 v[108:111], v48 offset:38912\n" \
"ds_read_b128 v[112:115], v49 offset:38912\n" \
"v_mfma_f32_16x16x32_bf16 a[84:87], v[52:55], v[132:135], a[84:87]\n" \
"v_accvgpr_read_b32 v248, a16\n" \
"v_accvgpr_read_b32 v249, a17\n" \
"v_mfma_f32_16x16x32_bf16 a[84:87], v[56:59], v[136:139], a[84:87]\n" \
"v_accvgpr_read_b32 v250, a18\n" \
"v_accvgpr_read_b32 v251, a19\n" \
"v_mfma_f32_16x16x32_bf16 a[116:119], v[144:147], v[64:67], a[116:119]\n" \
"v_accvgpr_read_b32 v180, a48\n" \
"v_accvgpr_read_b32 v181, a49\n" \
"v_mfma_f32_16x16x32_bf16 a[116:119], v[140:143], v[60:63], a[116:119]\n" \
"v_accvgpr_read_b32 v182, a50\n" \
"v_accvgpr_read_b32 v183, a51\n" \
"v_mfma_f32_16x16x32_bf16 a[24:27], v[124:127], v[60:63], a[24:27]\n" \
"v_accvgpr_read_b32 v252, a80\n" \
"v_accvgpr_read_b32 v253, a81\n" \
"v_mfma_f32_16x16x32_bf16 a[24:27], v[128:131], v[64:67], a[24:27]\n" \
"v_accvgpr_read_b32 v254, a82\n" \
"v_accvgpr_read_b32 v255, a83\n" \
"v_mfma_f32_16x16x32_bf16 a[56:59], v[120:123], v[64:67], a[56:59]\n" \
"v_accvgpr_read_b32 v196, a112\n" \
"v_accvgpr_read_b32 v197, a113\n" \
"v_mfma_f32_16x16x32_bf16 a[56:59], v[116:119], v[60:63], a[56:59]\n" \
"s_add_u32 s89, s43, 0x1000\n" \
"v_accvgpr_read_b32 v198, a114\n" \
"v_mfma_f32_16x16x32_bf16 a[88:91], v[60:63], v[132:135], a[88:91]\n" \
"buffer_store_dwordx4 a[100:103], v20, s[80:83], s89 offen offset:1024 sc1\n" \
"s_add_u32 s89, s43, 0x3000\n" \
"v_mfma_f32_16x16x32_bf16 a[88:91], v[64:67], v[136:139], a[88:91]\n" \
"v_accvgpr_read_b32 v199, a115\n" \
"v_accvgpr_read_b32 v188, a20\n" \
"v_mfma_f32_16x16x32_bf16 a[120:123], v[144:147], v[72:75], a[120:123]\n" \
"v_accvgpr_read_b32 v189, a21\n" \
"v_accvgpr_read_b32 v190, a22\n" \
"v_mfma_f32_16x16x32_bf16 a[120:123], v[140:143], v[68:71], a[120:123]\n" \
"v_accvgpr_read_b32 v191, a23\n" \
"v_accvgpr_read_b32 v184, a52\n" \
"v_mfma_f32_16x16x32_bf16 a[28:31], v[124:127], v[68:71], a[28:31]\n" \
"v_accvgpr_read_b32 v185, a53\n" \
"v_accvgpr_read_b32 v186, a54\n" \
"v_mfma_f32_16x16x32_bf16 a[28:31], v[128:131], v[72:75], a[28:31]\n" \
"v_accvgpr_read_b32 v187, a55\n" \
"v_accvgpr_read_b32 v192, a84\n" \
"v_mfma_f32_16x16x32_bf16 a[60:63], v[120:123], v[72:75], a[60:63]\n" \
"v_accvgpr_read_b32 v193, a85\n" \
"v_accvgpr_read_b32 v194, a86\n" \
"v_mfma_f32_16x16x32_bf16 a[60:63], v[116:119], v[68:71], a[60:63]\n" \
"v_accvgpr_read_b32 v195, a87\n" \
"v_accvgpr_read_b32 v212, a116\n" \
"v_mfma_f32_16x16x32_bf16 a[92:95], v[68:71], v[132:135], a[92:95]\n" \
"v_accvgpr_read_b32 v213, a117\n" \
"v_accvgpr_read_b32 v214, a118\n" \
"v_mfma_f32_16x16x32_bf16 a[92:95], v[72:75], v[136:139], a[92:95]\n" \
"s_add_u32 s88, s43, 0x2000\n" \
"v_accvgpr_read_b32 v215, a119\n" \
"v_mfma_f32_16x16x32_bf16 a[124:127], v[144:147], v[80:83], a[124:127]\n" \
"buffer_store_dwordx4 a[8:11], v20, s[80:83], s88 offen offset:1024 sc1\n" \
"s_add_u32 s88, s45, 0x0\n" \
"s_nop 0\n" \
"v_mfma_f32_16x16x32_bf16 a[124:127], v[140:143], v[76:79], a[124:127]\n" \
"v_accvgpr_read_b32 v204, a24\n" \
"v_accvgpr_read_b32 v205, a25\n" \
"v_mfma_f32_16x16x32_bf16 a[0:3], v[124:127], v[76:79], a[0:3]\n" \
"v_accvgpr_read_b32 v206, a26\n" \
"v_accvgpr_read_b32 v207, a27\n" \
"v_mfma_f32_16x16x32_bf16 a[0:3], v[128:131], v[80:83], a[0:3]\n" \
"v_accvgpr_read_b32 v200, a56\n" \
"v_accvgpr_read_b32 v201, a57\n" \
"v_mfma_f32_16x16x32_bf16 a[4:7], v[120:123], v[80:83], a[4:7]\n" \
"v_accvgpr_read_b32 v202, a58\n" \
"v_accvgpr_read_b32 v203, a59\n" \
"v_mfma_f32_16x16x32_bf16 a[4:7], v[116:119], v[76:79], a[4:7]\n" \
"v_accvgpr_read_b32 v208, a88\n" \
"v_accvgpr_read_b32 v209, a89\n" \
"v_mfma_f32_16x16x32_bf16 a[32:35], v[76:79], v[132:135], a[32:35]\n" \
"v_accvgpr_read_b32 v210, a90\n" \
"v_accvgpr_read_b32 v211, a91\n" \
"v_mfma_f32_16x16x32_bf16 a[32:35], v[80:83], v[136:139], a[32:35]\n" \
"v_accvgpr_read_b32 v228, a120\n" \
"s_waitcnt lgkmcnt(0)\n" \
"v_mfma_f32_16x16x32_bf16 a[128:131], v[152:155], v[56:59], a[128:131]\n" \
"v_accvgpr_read_b32 v229, a121\n" \
"v_accvgpr_read_b32 v230, a122\n" \
"v_mfma_f32_16x16x32_bf16 a[128:131], v[148:151], v[52:55], a[128:131]\n" \
"v_lshlrev_b32_e32 v45, 10, v20\n" \
"v_add_u32_e64 v45, s41, v45\n" \
"v_mfma_f32_16x16x32_bf16 a[132:135], v[156:159], v[52:55], a[132:135]\n" \
"s_mov_b32 m0, %[mS]\n" \
"s_nop 0\n" \
"v_accvgpr_read_b32 v231, a123\n" \
"v_mfma_f32_16x16x32_bf16 a[132:135], v[160:163], v[56:59], a[132:135]\n" \
"v_accvgpr_read_b32 v220, a28\n" \
"v_accvgpr_read_b32 v221, a29\n" \
"v_mfma_f32_16x16x32_bf16 a[136:139], v[168:171], v[56:59], a[136:139]\n" \
"v_accvgpr_read_b32 v222, a30\n" \
"v_accvgpr_read_b32 v223, a31\n" \
"v_mfma_f32_16x16x32_bf16 a[136:139], v[164:167], v[52:55], a[136:139]\n" \
"v_accvgpr_read_b32 v216, a60\n" \
"v_accvgpr_read_b32 v217, a61\n" \
"v_mfma_f32_16x16x32_bf16 a[140:143], v[172:175], v[52:55], a[140:143]\n" \
"v_accvgpr_read_b32 v218, a62\n" \
"v_accvgpr_read_b32 v219, a63\n" \
"s_getpc_b64 s[86:87]\n" \
"L_ct15_%=:\n" \
"s_add_u32 s86, s86, L_end_%=-L_ct15_%=\n" \
"s_addc_u32 s87, s87, 0\n" \
"v_mfma_f32_16x16x32_bf16 a[140:143], v[176:179], v[56:59], a[140:143]\n" \
"v_accvgpr_read_b32 v224, a92\n" \
"v_accvgpr_read_b32 v225, a93\n" \
"s_mov_b64 exec, 3\n" \
"global_load_lds_dword v45, s[86:87]\n" \
"s_mov_b64 exec, -1\n" \
"s_sub_u32 m0, %[mB], 0x10000\n" \
"v_mfma_f32_16x16x32_bf16 a[160:163], v[152:155], v[64:67], a[160:163]\n" \
"v_accvgpr_read_b32 v226, a94\n" \
"v_accvgpr_read_b32 v227, a95\n" \
"v_mfma_f32_16x16x32_bf16 a[160:163], v[148:151], v[60:63], a[160:163]\n" \
"v_accvgpr_read_b32 v244, a124\n" \
"v_accvgpr_read_b32 v245, a125\n" \
"v_mfma_f32_16x16x32_bf16 a[164:167], v[156:159], v[60:63], a[164:167]\n" \
"v_accvgpr_read_b32 v246, a126\n" \
"v_accvgpr_read_b32 v247, a127\n" \
"v_mfma_f32_16x16x32_bf16 a[164:167], v[160:163], v[64:67], a[164:167]\n" \
"v_accvgpr_read_b32 v236, a0\n" \
"v_accvgpr_read_b32 v237, a1\n" \
"v_mfma_f32_16x16x32_bf16 a[168:171], v[168:171], v[64:67], a[168:171]\n" \
"v_accvgpr_read_b32 v238, a2\n" \
"v_accvgpr_read_b32 v239, a3\n" \
"v_mfma_f32_16x16x32_bf16 a[168:171], v[164:167], v[60:63], a[168:171]\n" \
"v_accvgpr_read_b32 v232, a4\n" \
"v_accvgpr_read_b32 v233, a5\n" \
"v_mfma_f32_16x16x32_bf16 a[172:175], v[172:175], v[60:63], a[172:175]\n" \
"v_accvgpr_read_b32 v234, a6\n" \
"v_accvgpr_read_b32 v235, a7\n" \
"v_mfma_f32_16x16x32_bf16 a[172:175], v[176:179], v[64:67], a[172:175]\n" \
"v_accvgpr_read_b32 v240, a32\n" \
"v_accvgpr_read_b32 v241, a33\n" \
"v_mfma_f32_16x16x32_bf16 a[192:195], v[152:155], v[72:75], a[192:195]\n" \
"v_accvgpr_read_b32 v242, a34\n" \
"v_accvgpr_read_b32 v243, a35\n" \
"v_mfma_f32_16x16x32_bf16 a[192:195], v[148:151], v[68:71], a[192:195]\n" \
"s_mov_b32 s68, 0xffc0fffc\n" \
"s_mov_b32 s69, 0xc000fc00\n" \
"s_waitcnt vmcnt(4)\n" \
"s_barrier\n" \
"v_mfma_f32_16x16x32_bf16 a[196:199], v[156:159], v[68:71], a[196:199]\n" \
"ds_read_b64_tr_b16 v[144:145], v39 offset:4096\n" \
"ds_read_b64_tr_b16 v[146:147], v39 offset:4608\n" \
"v_mfma_f32_16x16x32_bf16 a[196:199], v[160:163], v[72:75], a[196:199]\n" \
"ds_read_b64_tr_b16 v[140:141], v39 offset:0\n" \
"ds_read_b64_tr_b16 v[142:143], v39 offset:512\n" \
"v_mfma_f32_16x16x32_bf16 a[200:203], v[168:171], v[72:75], a[200:203]\n" \
"ds_read_b64_tr_b16 v[124:125], v37 offset:0\n" \
"ds_read_b64_tr_b16 v[126:127], v37 offset:512\n" \
"v_mfma_f32_16x16x32_bf16 a[200:203], v[164:167], v[68:71], a[200:203]\n" \
"ds_read_b64_tr_b16 v[128:129], v37 offset:4096\n" \
"ds_read_b64_tr_b16 v[130:131], v37 offset:4608\n" \
"v_mfma_f32_16x16x32_bf16 a[204:207], v[172:175], v[68:71], a[204:207]\n" \
"ds_read_b64_tr_b16 v[136:137], v38 offset:4096\n" \
"ds_read_b64_tr_b16 v[138:139], v38 offset:4608\n" \
"v_mfma_f32_16x16x32_bf16 a[204:207], v[176:179], v[72:75], a[204:207]\n" \
"ds_read_b64_tr_b16 v[132:133], v38 offset:0\n" \
"ds_read_b64_tr_b16 v[134:135], v38 offset:512\n" \
"v_mfma_f32_16x16x32_bf16 a[224:227], v[152:155], v[80:83], a[224:227]\n" \
"ds_read_b128 v[52:55], v28 offset:0\n" \
"ds_read_b128 v[56:59], v29 offset:0\n" \
"v_mfma_f32_16x16x32_bf16 a[224:227], v[148:151], v[76:79], a[224:227]\n" \
"ds_read_b64_tr_b16 v[116:117], v36 offset:0\n" \
"ds_read_b64_tr_b16 v[118:119], v36 offset:512\n" \
"v_mfma_f32_16x16x32_bf16 a[228:231], v[156:159], v[76:79], a[228:231]\n" \
"ds_read_b64_tr_b16 v[120:121], v36 offset:4096\n" \
"ds_read_b64_tr_b16 v[122:123], v36 offset:4608\n" \
"v_mfma_f32_16x16x32_bf16 a[228:231], v[160:163], v[80:83], a[228:231]\n" \
"ds_read_b128 v[60:63], v28 offset:2048\n" \
"ds_read_b128 v[64:67], v29 offset:2048\n" \
"v_mfma_f32_16x16x32_bf16 a[232:235], v[168:171], v[80:83], a[232:235]\n" \
"v_lshrrev_b32_e32 v25, 8, v20\n" \
"ds_read_b128 v[68:71], v28 offset:4096\n" \
"v_mfma_f32_16x16x32_bf16 a[232:235], v[164:167], v[76:79], a[232:235]\n" \
"v_lshrrev_b32_e32 v24, 9, v20\n" \
"ds_read_b128 v[72:75], v29 offset:4096\n" \
"v_mfma_f32_16x16x32_bf16 a[236:239], v[172:175], v[76:79], a[236:239]\n" \
"v_mul_u32_u24_e32 v25, 0x24700, v25\n" \
"ds_read_b128 v[76:79], v28 offset:6144\n" \
"v_mfma_f32_16x16x32_bf16 a[236:239], v[176:179], v[80:83], a[236:239]\n" \
"v_lshl_add_u32 v24, v24, 3, v26\n" \
"ds_read_b128 v[80:83], v29 offset:6144\n" \
"v_mfma_f32_16x16x32_bf16 a[144:147], v[152:155], v[88:91], a[144:147]\n" \
"v_add_u32_e32 v25, v25, v20\n" \
"s_add_u32 s84, s50, 0x0\n" \
"v_mfma_f32_16x16x32_bf16 a[144:147], v[148:151], v[84:87], a[144:147]\n" \
"s_add_u32 s85, s50, 0x24800\n" \
"s_add_u32 s86, s50, 0x49000\n" \
"v_mfma_f32_16x16x32_bf16 a[148:151], v[156:159], v[84:87], a[148:151]\n" \
"s_add_u32 s87, s50, 0x6d800\n" \
"s_mov_b32 s70, 0x3e0293ee\n" \
"v_mfma_f32_16x16x32_bf16 a[148:151], v[160:163], v[88:91], a[148:151]\n" \
"s_mov_b32 s71, 0x3e0293ee\n" \
"v_add_u32_e64 v27, s47, v20\n" \
"v_mfma_f32_16x16x32_bf16 a[152:155], v[168:171], v[88:91], a[152:155]\n" \
"v_lshrrev_b32_e32 v26, 2, v20\n" \
"v_add_u32_e32 v26, s46, v26\n" \
"v_mfma_f32_16x16x32_bf16 a[152:155], v[164:167], v[84:87], a[152:155]\n" \
"v_mfma_f32_16x16x32_bf16 a[156:159], v[172:175], v[84:87], a[156:159]\n" \
"ds_read_b128 v[84:87], v22 offset:0\n" \
"v_mfma_f32_16x16x32_bf16 a[156:159], v[176:179], v[88:91], a[156:159]\n" \
"ds_read_b128 v[88:91], v23 offset:0\n" \
"v_mfma_f32_16x16x32_bf16 a[176:179], v[152:155], v[96:99], a[176:179]\n" \
"v_mfma_f32_16x16x32_bf16 a[176:179], v[148:151], v[92:95], a[176:179]\n" \
"v_mfma_f32_16x16x32_bf16 a[180:183], v[156:159], v[92:95], a[180:183]\n" \
"v_mfma_f32_16x16x32_bf16 a[180:183], v[160:163], v[96:99], a[180:183]\n" \
"v_mfma_f32_16x16x32_bf16 a[184:187], v[168:171], v[96:99], a[184:187]\n" \
"v_mfma_f32_16x16x32_bf16 a[184:187], v[164:167], v[92:95], a[184:187]\n" \
"v_mfma_f32_16x16x32_bf16 a[188:191], v[172:175], v[92:95], a[188:191]\n" \
"ds_read_b128 v[92:95], v48 offset:2048\n" \
"v_mfma_f32_16x16x32_bf16 a[188:191], v[176:179], v[96:99], a[188:191]\n" \
"ds_read_b128 v[96:99], v49 offset:2048\n" \
"v_mfma_f32_16x16x32_bf16 a[208:211], v[152:155], v[104:107], a[208:211]\n" \
"v_mfma_f32_16x16x32_bf16 a[208:211], v[148:151], v[100:103], a[208:211]\n" \
"v_mfma_f32_16x16x32_bf16 a[212:215], v[156:159], v[100:103], a[212:215]\n" \
"v_mfma_f32_16x16x32_bf16 a[212:215], v[160:163], v[104:107], a[212:215]\n" \
"v_mfma_f32_16x16x32_bf16 a[216:219], v[168:171], v[104:107], a[216:219]\n" \
"v_mfma_f32_16x16x32_bf16 a[216:219], v[164:167], v[100:103], a[216:219]\n" \
"v_mfma_f32_16x16x32_bf16 a[220:223], v[172:175], v[100:103], a[220:223]\n" \
"ds_read_b128 v[100:103], v48 offset:4096\n" \
"v_mfma_f32_16x16x32_bf16 a[220:223], v[176:179], v[104:107], a[220:223]\n" \
"ds_read_b128 v[104:107], v49 offset:4096\n" \
"v_mfma_f32_16x16x32_bf16 a[240:243], v[152:155], v[112:115], a[240:243]\n" \
"ds_read_b64_tr_b16 v[152:153], v32 offset:8192\n" \
"ds_read_b64_tr_b16 v[154:155], v32 offset:9216\n" \
"v_mfma_f32_16x16x32_bf16 a[240:243], v[148:151], v[108:111], a[240:243]\n" \
"ds_read_b64_tr_b16 v[148:149], v32 offset:0\n" \
"ds_read_b64_tr_b16 v[150:151], v32 offset:1024\n" \
"v_mfma_f32_16x16x32_bf16 a[244:247], v[156:159], v[108:111], a[244:247]\n" \
"ds_read_b64_tr_b16 v[156:157], v33 offset:0\n" \
"ds_read_b64_tr_b16 v[158:159], v33 offset:1024\n" \
"v_mfma_f32_16x16x32_bf16 a[244:247], v[160:163], v[112:115], a[244:247]\n" \
"ds_read_b64_tr_b16 v[160:161], v33 offset:8192\n" \
"ds_read_b64_tr_b16 v[162:163], v33 offset:9216\n" \
"v_mfma_f32_16x16x32_bf16 a[248:251], v[168:171], v[112:115], a[248:251]\n" \
"ds_read_b64_tr_b16 v[168:169], v34 offset:8192\n" \
"ds_read_b64_tr_b16 v[170:171], v34 offset:9216\n" \
"v_mfma_f32_16x16x32_bf16 a[248:251], v[164:167], v[108:111], a[248:251]\n" \
"ds_read_b64_tr_b16 v[164:165], v34 offset:0\n" \
"ds_read_b64_tr_b16 v[166:167], v34 offset:1024\n" \
"v_mfma_f32_16x16x32_bf16 a[252:255], v[172:175], v[108:111], a[252:255]\n" \
"ds_read_b64_tr_b16 v[172:173], v35 offset:0\n" \
"ds_read_b64_tr_b16 v[174:175], v35 offset:1024\n" \
"v_mfma_f32_16x16x32_bf16 a[252:255], v[176:179], v[112:115], a[252:255]\n" \
"ds_read_b64_tr_b16 v[176:177], v35 offset:8192\n" \
"s_waitcnt lgkmcnt(15)\n" \
"s_nop 0\n" \
"v_mfma_f32_16x16x32_bf16 v[248:251], v[144:147], v[88:91], v[248:251]\n" \
"v_bfe_u32 v44, v20, 2, 5\n" \
"ds_read_b64_tr_b16 v[178:179], v35 offset:9216\n" \
"v_mfma_f32_16x16x32_bf16 v[248:251], v[140:143], v[84:87], v[248:251]\n" \
"buffer_load_dword v44, v44, s[80:83], s39 offen offset:0 sc1\n" \
"v_mfma_f32_16x16x32_bf16 v[180:183], v[124:127], v[84:87], v[180:183]\n" \
"ds_read_b128 v[108:111], v48 offset:6144\n" \
"ds_read_b128 v[112:115], v49 offset:6144\n" \
"v_mfma_f32_16x16x32_bf16 v[180:183], v[128:131], v[88:91], v[180:183]\n" \
"v_mfma_f32_16x16x32_bf16 v[252:255], v[88:91], v[136:139], v[252:255]\n" \
"ds_read_b128 v[88:91], v49 offset:0\n" \
"v_mfma_f32_16x16x32_bf16 v[252:255], v[84:87], v[132:135], v[252:255]\n" \
"ds_read_b128 v[84:87], v48 offset:0\n" \
"v_mfma_f32_16x16x32_bf16 v[196:199], v[140:143], v[52:55], v[196:199]\n" \
"v_mfma_f32_16x16x32_bf16 v[196:199], v[144:147], v[56:59], v[196:199]\n" \
"v_mfma_f32_16x16x32_bf16 v[188:191], v[128:131], v[56:59], v[188:191]\n" \
"v_mfma_f32_16x16x32_bf16 v[188:191], v[124:127], v[52:55], v[188:191]\n" \
"v_cvt_pk_bf16_f32 v180, v180, v181\n" \
"v_cvt_pk_bf16_f32 v181, v182, v183\n" \
"v_mfma_f32_16x16x32_bf16 v[184:187], v[116:119], v[52:55], v[184:187]\n" \
"v_cvt_pk_bf16_f32 v182, v250, v251\n" \
"v_mov_b32_e32 v183, 0x3f80\n" \
"v_mfma_f32_16x16x32_bf16 v[184:187], v[120:123], v[56:59], v[184:187]\n" \
"v_mfma_f32_16x16x32_bf16 v[192:195], v[56:59], v[136:139], v[192:195]\n" \
"v_mfma_f32_16x16x16_bf16 v[248:251], v[182:183], v[46:47], 0\n" \
"v_cvt_pk_bf16_f32 v188, v188, v189\n" \
"v_cvt_pk_bf16_f32 v189, v190, v191\n" \
"v_mfma_f32_16x16x32_bf16 v[192:195], v[52:55], v[132:135], v[192:195]\n" \
"v_cvt_pk_bf16_f32 v190, v198, v199\n" \
"v_mov_b32_e32 v191, 0x3f80\n" \
"v_mfma_f32_16x16x32_bf16 v[212:215], v[140:143], v[60:63], v[212:215]\n" \
"v_cvt_pk_bf16_f32 v184, v184, v185\n" \
"v_cvt_pk_bf16_f32 v185, v186, v187\n" \
"v_mfma_f32_16x16x32_bf16 v[212:215], v[144:147], v[64:67], v[212:215]\n" \
"v_cvt_pk_bf16_f32 v186, v196, v197\n" \
"v_mov_b32_e64 v187, 0\n" \
"v_mfma_f32_16x16x32_bf16 v[204:207], v[128:131], v[64:67], v[204:207]\n" \
"v_mfma_f32_16x16x32_bf16 v[204:207], v[124:127], v[60:63], v[204:207]\n" \
"v_mfma_f32_16x16x32_bf16 v[200:203], v[116:119], v[60:63], v[200:203]\n" \
"v_mfma_f32_16x16x32_bf16 v[200:203], v[120:123], v[64:67], v[200:203]\n" \
"v_mfma_f32_16x16x32_bf16 v[208:211], v[64:67], v[136:139], v[208:211]\n" \
"v_mfma_f32_16x16x32_bf16 v[208:211], v[60:63], v[132:135], v[208:211]\n" \
"v_mfma_f32_16x16x32_bf16 v[228:231], v[140:143], v[68:71], v[228:231]\n" \
"v_mfma_f32_16x16x32_bf16 v[228:231], v[144:147], v[72:75], v[228:231]\n" \
"v_mfma_f32_16x16x32_bf16 v[220:223], v[128:131], v[72:75], v[220:223]\n" \
"v_mfma_f32_16x16x32_bf16 v[220:223], v[124:127], v[68:71], v[220:223]\n" \
"v_cvt_pk_bf16_f32 v204, v204, v205\n" \
"v_cvt_pk_bf16_f32 v205, v206, v207\n" \
"v_mfma_f32_16x16x32_bf16 v[216:219], v[116:119], v[68:71], v[216:219]\n" \
"v_cvt_pk_bf16_f32 v200, v200, v201\n" \
"v_cvt_pk_bf16_f32 v201, v202, v203\n" \
"v_mfma_f32_16x16x32_bf16 v[216:219], v[120:123], v[72:75], v[216:219]\n" \
"v_cvt_pk_bf16_f32 v206, v214, v215\n" \
"v_mov_b32_e32 v207, 0x3f80\n" \
"v_mfma_f32_16x16x32_bf16 v[224:227], v[72:75], v[136:139], v[224:227]\n" \
"v_cvt_pk_bf16_f32 v220, v220, v221\n" \
"v_cvt_pk_bf16_f32 v221, v222, v223\n" \
"v_mfma_f32_16x16x32_bf16 v[224:227], v[68:71], v[132:135], v[224:227]\n" \
"v_cvt_pk_bf16_f32 v202, v212, v213\n" \
"v_mov_b32_e64 v203, 0\n" \
"v_mfma_f32_16x16x32_bf16 v[244:247], v[140:143], v[76:79], v[244:247]\n" \
"v_cvt_pk_bf16_f32 v216, v216, v217\n" \
"v_cvt_pk_bf16_f32 v217, v218, v219\n" \
"v_mfma_f32_16x16x32_bf16 v[244:247], v[144:147], v[80:83], v[244:247]\n" \
"v_cvt_pk_bf16_f32 v222, v230, v231\n" \
"v_mov_b32_e32 v223, 0x3f80\n" \
"v_mfma_f32_16x16x32_bf16 v[236:239], v[128:131], v[80:83], v[236:239]\n" \
"v_cvt_pk_bf16_f32 v128, v208, v209\n" \
"v_cvt_pk_bf16_f32 v129, v210, v211\n" \
"v_mfma_f32_16x16x32_bf16 v[236:239], v[124:127], v[76:79], v[236:239]\n" \
"v_cvt_pk_bf16_f32 v126, v192, v193\n" \
"v_cvt_pk_bf16_f32 v127, v194, v195\n" \
"v_mfma_f32_16x16x32_bf16 v[232:235], v[116:119], v[76:79], v[232:235]\n" \
"v_cvt_pk_bf16_f32 v218, v228, v229\n" \
"v_mov_b32_e64 v219, 0\n" \
"v_mfma_f32_16x16x32_bf16 v[232:235], v[120:123], v[80:83], v[232:235]\n" \
"v_cvt_pk_bf16_f32 v130, v224, v225\n" \
"v_cvt_pk_bf16_f32 v131, v226, v227\n" \
"v_mfma_f32_16x16x32_bf16 v[240:243], v[80:83], v[136:139], v[240:243]\n" \
"v_cvt_pk_bf16_f32 v236, v236, v237\n" \
"v_cvt_pk_bf16_f32 v237, v238, v239\n" \
"v_mfma_f32_16x16x32_bf16 v[240:243], v[76:79], v[132:135], v[240:243]\n" \
"v_cvt_pk_bf16_f32 v238, v246, v247\n" \
"v_mov_b32_e32 v239, 0x3f80\n" \
"s_waitcnt lgkmcnt(0)\n" \
"s_barrier\n" \
"v_mfma_f32_16x16x32_bf16 a[128:131], v[148:151], v[52:55], a[128:131]\n" \
"v_cvt_pk_bf16_f32 v232, v232, v233\n" \
"v_cvt_pk_bf16_f32 v233, v234, v235\n" \
"v_mfma_f32_16x16x32_bf16 a[128:131], v[152:155], v[56:59], a[128:131]\n" \
"v_cvt_pk_bf16_f32 v234, v244, v245\n" \
"v_mov_b32_e64 v235, 0\n" \
"v_mfma_f32_16x16x32_bf16 a[132:135], v[160:163], v[56:59], a[132:135]\n" \
"v_cvt_pk_bf16_f32 v132, v240, v241\n" \
"v_cvt_pk_bf16_f32 v133, v242, v243\n" \
"v_mfma_f32_16x16x32_bf16 a[132:135], v[156:159], v[52:55], a[132:135]\n" \
"v_mfma_f32_16x16x32_bf16 v[116:119], v[180:183], v[184:187], 0\n" \
"v_mfma_f32_16x16x32_bf16 v[192:195], v[188:191], v[184:187], 0\n" \
"s_waitcnt vmcnt(0)\n" \
"v_cmp_ne_u32_e32 vcc, %[tag], v44\n" \
"v_mfma_f32_16x16x32_bf16 a[136:139], v[164:167], v[52:55], a[136:139]\n" \
"v_mfma_f32_16x16x32_bf16 v[208:211], v[204:207], v[200:203], 0\n" \
"v_mfma_f32_16x16x32_bf16 v[120:123], v[188:191], v[200:203], 0\n" \
"v_mfma_f32_16x16x32_bf16 v[224:227], v[220:223], v[216:219], 0\n" \
"v_mfma_f32_16x16x32_bf16 v[36:39], v[204:207], v[216:219], 0\n" \
"s_cbranch_vccnz L_pa_%=\n" \
"L_ra_%=:\n" \
"v_cndmask_b32_e64 v116, v116, v117, s[64:65]\n" \
"buffer_load_dwordx4 v20, s[80:83], s73 offen offset:0 sc1 lds\n" \
"v_mfma_f32_16x16x32_bf16 a[136:139], v[168:171], v[56:59], a[136:139]\n" \
"v_mfma_f32_16x16x32_bf16 v[240:243], v[236:239], v[232:235], 0\n" \
"v_mfma_f32_16x16x32_bf16 v[48:51], v[220:223], v[232:235], 0\n" \
"v_cndmask_b32_e64 v118, v118, v119, s[64:65]\n" \
"v_cndmask_b32_e64 v192, v192, v193, s[64:65]\n" \
"v_mfma_f32_16x16x32_bf16 a[140:143], v[176:179], v[56:59], a[140:143]\n" \
"buffer_load_dwordx4 v20, s[80:83], s73 offen offset:1024 sc1 lds\n" \
"v_mfma_f32_16x16x32_bf16 a[140:143], v[172:175], v[52:55], a[140:143]\n" \
"v_add_f32_e32 v116, s49, v116\n" \
"v_cndmask_b32_e64 v194, v194, v195, s[64:65]\n" \
"v_mfma_f32_16x16x32_bf16 a[160:163], v[148:151], v[60:63], a[160:163]\n" \
"v_add_f32_e64 v118, s49, v118\n" \
"v_cndmask_b32_e64 v208, v208, v209, s[64:65]\n" \
"v_mfma_f32_16x16x32_bf16 a[160:163], v[152:155], v[64:67], a[160:163]\n" \
"v_cndmask_b32_e64 v120, v120, v121, s[64:65]\n" \
"v_cndmask_b32_e64 v210, v210, v211, s[64:65]\n" \
"v_mfma_f32_16x16x32_bf16 a[164:167], v[160:163], v[64:67], a[164:167]\n" \
"buffer_load_dwordx4 v20, s[80:83], s73 offen offset:2048 sc1 lds\n" \
"v_mfma_f32_16x16x32_bf16 a[164:167], v[156:159], v[60:63], a[164:167]\n" \
"v_cndmask_b32_e64 v122, v122, v123, s[64:65]\n" \
"v_cndmask_b32_e64 v224, v224, v225, s[64:65]\n" \
"v_mfma_f32_16x16x32_bf16 a[168:171], v[164:167], v[60:63], a[168:171]\n" \
"v_cndmask_b32_e64 v36, v36, v37, s[64:65]\n" \
"v_cndmask_b32_e64 v226, v226, v227, s[64:65]\n" \
"v_mfma_f32_16x16x32_bf16 a[168:171], v[168:171], v[64:67], a[168:171]\n" \
"v_cndmask_b32_e64 v38, v38, v39, s[64:65]\n" \
"v_cndmask_b32_e64 v240, v240, v241, s[64:65]\n" \
"v_mfma_f32_16x16x32_bf16 a[172:175], v[176:179], v[64:67], a[172:175]\n" \
"buffer_load_dwordx4 v20, s[80:83], s73 offen offset:3072 sc1 lds\n" \
"v_mfma_f32_16x16x32_bf16 a[172:175], v[172:175], v[60:63], a[172:175]\n" \
"s_sub_u32 m0, %[mB], s53\n" \
"s_nop 0\n" \
"v_cndmask_b32_e64 v48, v48, v49, s[64:65]\n" \
"v_mfma_f32_16x16x32_bf16 a[192:195], v[148:151], v[68:71], a[192:195]\n" \
"v_cndmask_b32_e64 v242, v242, v243, s[64:65]\n" \
"v_cndmask_b32_e64 v50, v50, v51, s[64:65]\n" \
"v_mfma_f32_16x16x32_bf16 a[192:195], v[152:155], v[72:75], a[192:195]\n" \
"v_cndmask_b32_e64 v192, v116, v192, s[66:67]\n" \
"v_cndmask_b32_e64 v194, v118, v194, s[68:69]\n" \
"v_mfma_f32_16x16x32_bf16 a[196:199], v[160:163], v[72:75], a[196:199]\n" \
"buffer_load_dwordx4 v20, s[80:83], s74 offen offset:0 sc1 lds\n" \
"v_mfma_f32_16x16x32_bf16 a[196:199], v[156:159], v[68:71], a[196:199]\n" \
"v_cndmask_b32_e64 v208, v120, v208, s[66:67]\n" \
"v_cndmask_b32_e64 v210, v122, v210, s[68:69]\n" \
"v_mfma_f32_16x16x32_bf16 a[200:203], v[164:167], v[68:71], a[200:203]\n" \
"v_cndmask_b32_e64 v224, v36, v224, s[66:67]\n" \
"v_cndmask_b32_e64 v226, v38, v226, s[68:69]\n" \
"v_mfma_f32_16x16x32_bf16 a[200:203], v[168:171], v[72:75], a[200:203]\n" \
"v_cndmask_b32_e64 v240, v48, v240, s[66:67]\n" \
"v_cndmask_b32_e64 v242, v50, v242, s[68:69]\n" \
"v_mfma_f32_16x16x32_bf16 a[204:207], v[176:179], v[72:75], a[204:207]\n" \
"buffer_load_dwordx4 v20, s[80:83], s74 offen offset:1024 sc1 lds\n" \
"v_mfma_f32_16x16x32_bf16 a[204:207], v[172:175], v[68:71], a[204:207]\n" \
"v_max_f32_e32 v193, v192, v194\n" \
"v_max_f32_e32 v209, v208, v210\n" \
"v_mfma_f32_16x16x32_bf16 a[224:227], v[148:151], v[76:79], a[224:227]\n" \
"v_max_f32_e32 v225, v224, v226\n" \
"v_max_f32_e32 v241, v240, v242\n" \
"v_mfma_f32_16x16x32_bf16 a[224:227], v[152:155], v[80:83], a[224:227]\n" \
"v_permlane32_swap_b32_e32 v193, v225\n" \
"v_mfma_f32_16x16x32_bf16 a[228:231], v[160:163], v[80:83], a[228:231]\n" \
"buffer_load_dwordx4 v20, s[80:83], s74 offen offset:2048 sc1 lds\n" \
"v_mfma_f32_16x16x32_bf16 a[228:231], v[156:159], v[76:79], a[228:231]\n" \
"v_permlane32_swap_b32_e32 v209, v241\n" \
"v_mfma_f32_16x16x32_bf16 a[232:235], v[164:167], v[76:79], a[232:235]\n" \
"v_max_f32_e32 v193, v193, v225\n" \
"v_max_f32_e32 v209, v209, v241\n" \
"v_mfma_f32_16x16x32_bf16 a[232:235], v[168:171], v[80:83], a[232:235]\n" \
"v_permlane32_swap_b32_e32 v196, v228\n" \
"v_mfma_f32_16x16x32_bf16 a[236:239], v[176:179], v[80:83], a[236:239]\n" \
"buffer_load_dwordx4 v20, s[80:83], s74 offen offset:3072 sc1 lds\n" \
"v_mfma_f32_16x16x32_bf16 a[236:239], v[172:175], v[76:79], a[236:239]\n" \
"s_sub_u32 m0, %[mB], 0x4000\n" \
"v_cvt_pk_bf16_f32 v134, v248, v249\n" \
"v_permlane32_swap_b32_e32 v212, v244\n" \
"v_permlane16_swap_b32_e32 v193, v209\n" \
"v_mfma_f32_16x16x32_bf16 a[144:147], v[148:151], v[84:87], a[144:147]\n" \
"v_permlane16_swap_b32_e32 v228, v244\n" \
"v_mfma_f32_16x16x32_bf16 a[144:147], v[152:155], v[88:91], a[144:147]\n" \
"buffer_load_dwordx4 v20, s[80:83], s75 offen offset:0 sc1 lds\n" \
"v_mfma_f32_16x16x32_bf16 a[148:151], v[160:163], v[88:91], a[148:151]\n" \
"v_max_f32_e32 v193, v193, v209\n" \
"v_mul_f32_e32 v193, s72, v193\n" \
"v_mfma_f32_16x16x32_bf16 a[148:151], v[156:159], v[84:87], a[148:151]\n" \
"v_mul_f32_e32 v45, 0xbfb8aa3b, v228\n" \
"v_mov_b32_e64 v209, v193\n" \
"v_mfma_f32_16x16x16_bf16 v[196:199], v[190:191], v[46:47], 0\n" \
"v_mfma_f32_16x16x16_bf16 v[212:215], v[206:207], v[46:47], 0\n" \
"buffer_load_dwordx4 v20, s[80:83], s75 offen offset:1024 sc1 lds\n" \
"v_mfma_f32_16x16x16_bf16 v[228:231], v[222:223], v[46:47], 0\n" \
"v_permlane16_swap_b32_e32 v193, v209\n" \
"v_mfma_f32_16x16x16_bf16 v[244:247], v[238:239], v[46:47], 0\n" \
"v_mov_b32_e32 v225, v193\n" \
"v_mov_b32_e64 v241, v209\n" \
"v_mfma_f32_16x16x32_bf16 a[152:155], v[164:167], v[84:87], a[152:155]\n" \
"v_permlane32_swap_b32_e32 v193, v225\n" \
"v_permlane32_swap_b32_e32 v209, v241\n" \
"buffer_load_dwordx4 v20, s[80:83], s75 offen offset:2048 sc1 lds\n" \
"v_mfma_f32_16x16x32_bf16 a[152:155], v[168:171], v[88:91], a[152:155]\n" \
"v_fma_f32 v192, v192, s72, -v193\n" \
"v_fma_f32 v194, v194, s72, -v193\n" \
"v_mfma_f32_16x16x32_bf16 a[156:159], v[176:179], v[88:91], a[156:159]\n" \
"v_fma_f32 v208, v208, s72, -v209\n" \
"v_fma_f32 v210, v210, s72, -v209\n" \
"v_exp_f32_e32 v192, v192\n" \
"v_exp_f32_e32 v194, v194\n" \
"v_exp_f32_e64 v208, v208\n" \
"buffer_load_dwordx4 v20, s[80:83], s75 offen offset:3072 sc1 lds\n" \
"v_mfma_f32_16x16x32_bf16 a[156:159], v[172:175], v[84:87], a[156:159]\n" \
"s_mov_b32 m0, s55\n" \
"v_fma_f32 v224, v224, s72, -v225\n" \
"v_exp_f32_e32 v210, v210\n" \
"v_mfma_f32_16x16x32_bf16 a[176:179], v[148:151], v[92:95], a[176:179]\n" \
"v_fma_f32 v226, v226, s72, -v225\n" \
"v_fma_f32 v240, v240, s72, -v241\n" \
"v_exp_f32_e32 v224, v224\n" \
"v_exp_f32_e32 v226, v226\n" \
"buffer_load_dwordx4 v20, s[80:83], s76 offen offset:0 sc1 lds\n" \
"v_mfma_f32_16x16x32_bf16 a[176:179], v[152:155], v[96:99], a[176:179]\n" \
"v_fma_f32 v242, v242, s72, -v241\n" \
"v_cvt_pk_bf16_f32 v192, v192, v194\n" \
"v_mfma_f32_16x16x32_bf16 a[180:183], v[160:163], v[96:99], a[180:183]\n" \
"v_cvt_pk_bf16_f32 v208, v208, v210\n" \
"v_cvt_pk_bf16_f32 v135, v250, v251\n" \
"v_exp_f32_e32 v240, v240\n" \
"v_exp_f32_e32 v242, v242\n" \
"v_mfma_f32_16x16x32_bf16 a[180:183], v[156:159], v[92:95], a[180:183]\n" \
"buffer_load_dwordx4 v20, s[80:83], s76 offen offset:1024 sc1 lds\n" \
"v_mfma_f32_16x16x32_bf16 a[184:187], v[164:167], v[92:95], a[184:187]\n" \
"v_cvt_pk_bf16_f32 v136, v196, v197\n" \
"v_cvt_pk_bf16_f32 v137, v198, v199\n" \
"v_mfma_f32_16x16x32_bf16 a[184:187], v[168:171], v[96:99], a[184:187]\n" \
"v_cvt_pk_bf16_f32 v138, v212, v213\n" \
"v_cvt_pk_bf16_f32 v139, v214, v215\n" \
"v_mfma_f32_16x16x32_bf16 a[188:191], v[176:179], v[96:99], a[188:191]\n" \
"v_perm_b32 v184, v192, v192, v40\n" \
"v_perm_b32 v185, v192, v192, v41\n" \
"v_mfma_f32_16x16x32_bf16 a[188:191], v[172:175], v[92:95], a[188:191]\n" \
"v_perm_b32 v186, v192, v192, v42\n" \
"v_perm_b32 v187, v192, v192, v43\n" \
"v_perm_b32 v200, v208, v208, v40\n" \
"v_perm_b32 v201, v208, v208, v41\n" \
"v_perm_b32 v202, v208, v208, v42\n" \
"v_perm_b32 v203, v208, v208, v43\n" \
"v_cvt_pk_bf16_f32 v224, v224, v226\n" \
"v_cvt_pk_bf16_f32 v140, v228, v229\n" \
"v_cvt_pk_bf16_f32 v141, v230, v231\n" \
"v_mfma_f32_16x16x32_bf16 v[116:119], v[134:137], v[184:187], 0\n" \
"v_mfma_f32_16x16x32_bf16 v[120:123], v[136:139], v[200:203], 0\n" \
"v_perm_b32 v216, v224, v224, v40\n" \
"L_pf9:\n" \
"s_cbranch_execz L_pf13\n" \
"s_nop 0\n" \
"v_perm_b32 v217, v224, v224, v41\n" \
"v_perm_b32 v218, v224, v224, v42\n" \
"v_perm_b32 v219, v224, v224, v43\n" \
"v_cvt_pk_bf16_f32 v240, v240, v242\n" \
"v_cvt_pk_bf16_f32 v142, v244, v245\n" \
"v_cvt_pk_bf16_f32 v143, v246, v247\n" \
"v_mfma_f32_16x16x32_bf16 v[36:39], v[138:141], v[216:219], 0\n" \
"v_perm_b32 v232, v240, v240, v40\n" \
"v_perm_b32 v233, v240, v240, v41\n" \
"v_perm_b32 v234, v240, v240, v42\n" \
"v_perm_b32 v235, v240, v240, v43\n" \
"v_exp_f32_e64 v45, v45\n" \
"v_mov_b32_dpp v118, v122 quad_perm:[0,1,2,3] row_mask:0x2 bank_mask:0xf\n" \
"v_mfma_f32_16x16x32_bf16 v[48:51], v[140:143], v[232:235], 0\n" \
"v_cvt_pk_bf16_f32 v124, v252, v253\n" \
"v_cvt_pk_bf16_f32 v125, v254, v255\n" \
"v_mov_b32_dpp v118, v38 quad_perm:[0,1,2,3] row_mask:0x4 bank_mask:0xf\n" \
"v_add_f32_e64 v45, 1.0, v45\n" \
"v_mfma_f32_16x16x32_bf16 v[192:195], v[124:127], v[184:187], 0\n" \
"v_mfma_f32_16x16x32_bf16 v[208:211], v[126:129], v[200:203], 0\n" \
"v_mfma_f32_16x16x32_bf16 v[224:227], v[128:131], v[216:219], 0\n" \
"buffer_store_dwordx4 a[12:15], v20, s[80:83], s89 offen offset:1024 sc1\n" \
"s_add_u32 s89, s45, 0x0\n" \
"v_mfma_f32_16x16x32_bf16 v[240:243], v[130:133], v[232:235], 0\n" \
"v_mov_b32_dpp v118, v50 quad_perm:[0,1,2,3] row_mask:0x8 bank_mask:0xf\n" \
"v_rcp_f32_e32 v45, v45\n" \
"v_mov_b32_dpp v116, v120 quad_perm:[0,1,2,3] row_mask:0x2 bank_mask:0xf\n" \
"v_mov_b32_dpp v117, v121 quad_perm:[0,1,2,3] row_mask:0x2 bank_mask:0xf\n" \
"v_rcp_f32_e64 v118, v118\n" \
"v_mov_b32_dpp v116, v36 quad_perm:[0,1,2,3] row_mask:0x4 bank_mask:0xf\n" \
"v_mov_b32_dpp v117, v37 quad_perm:[0,1,2,3] row_mask:0x4 bank_mask:0xf\n" \
"v_mul_f32_e64 v118, v45, v118\n" \
"v_mov_b32_dpp v116, v48 quad_perm:[0,1,2,3] row_mask:0x8 bank_mask:0xf\n" \
"v_mov_b32_dpp v117, v49 quad_perm:[0,1,2,3] row_mask:0x8 bank_mask:0xf\n" \
"v_mul_f32_e32 v116, v118, v116\n" \
"v_mul_f32_e32 v117, v118, v117\n" \
"v_mov_b32_e64 v122, v118\n" \
"v_cvt_pk_bf16_f32 v116, v116, v117\n" \
"buffer_store_dword v116, v25, s[80:83], s51 offen sc1\n" \
"v_permlane16_swap_b32_e32 v118, v122\n" \
"v_mov_b32_e32 v38, v118\n" \
"v_mov_b32_e64 v50, v122\n" \
"v_accvgpr_read_b32 v52, a128\n" \
"v_permlane32_swap_b32_e32 v118, v38\n" \
"v_permlane32_swap_b32_e32 v122, v50\n" \
"v_mul_f32_e32 v192, v118, v192\n" \
"v_mul_f32_e32 v193, v118, v193\n" \
"v_mul_f32_e32 v208, v122, v208\n" \
"v_mul_f32_e32 v209, v122, v209\n" \
"v_mul_f32_e32 v224, v38, v224\n" \
"v_mul_f32_e32 v225, v38, v225\n" \
"v_mul_f32_e32 v240, v50, v240\n" \
"v_mul_f32_e32 v241, v50, v241\n" \
"v_mul_f32_e32 v194, v118, v194\n" \
"v_mul_f32_e32 v195, v118, v195\n" \
"v_cvt_pk_bf16_f32 v192, v192, v193\n" \
"v_mul_f32_e32 v210, v122, v210\n" \
"v_mul_f32_e32 v211, v122, v211\n" \
"v_cvt_pk_bf16_f32 v208, v208, v209\n" \
"v_mul_f32_e32 v226, v38, v226\n" \
"v_mul_f32_e32 v227, v38, v227\n" \
"v_cvt_pk_bf16_f32 v224, v224, v225\n" \
"v_mul_f32_e32 v242, v50, v242\n" \
"v_mul_f32_e32 v243, v50, v243\n" \
"v_cvt_pk_bf16_f32 v240, v240, v241\n" \
"v_cvt_pk_bf16_f32 v193, v194, v195\n" \
"v_cvt_pk_bf16_f32 v209, v210, v211\n" \
"v_cvt_pk_bf16_f32 v225, v226, v227\n" \
"v_cvt_pk_bf16_f32 v241, v242, v243\n" \
"buffer_store_dwordx2 v[192:193], v24, s[80:83], s84 offen sc1\n" \
"buffer_store_dwordx2 v[208:209], v24, s[80:83], s85 offen sc1\n" \
"buffer_store_dwordx2 v[224:225], v24, s[80:83], s86 offen sc1\n" \
"buffer_store_dwordx2 v[240:241], v24, s[80:83], s87 offen sc1\n" \
"v_accvgpr_read_b32 v53, a129\n" \
"v_accvgpr_read_b32 v54, a130\n" \
"v_accvgpr_read_b32 v55, a131\n" \
"v_pk_mul_f32 v[52:53], v[52:53], s[70:71]\n" \
"v_accvgpr_read_b32 v56, a132\n" \
"v_accvgpr_read_b32 v57, a133\n" \
"v_accvgpr_read_b32 v58, a134\n" \
"v_accvgpr_read_b32 v59, a135\n" \
"v_pk_mul_f32 v[54:55], v[54:55], s[70:71]\n" \
"v_cvt_pk_bf16_f32 v52, v52, v53\n" \
"v_pk_mul_f32 v[56:57], v[56:57], s[70:71]\n" \
"v_pk_mul_f32 v[58:59], v[58:59], s[70:71]\n" \
"v_cvt_pk_bf16_f32 v53, v54, v55\n" \
"v_cvt_pk_bf16_f32 v54, v56, v57\n" \
"v_cvt_pk_bf16_f32 v55, v58, v59\n" \
"ds_write_b128 v27, v[52:55] offset:0\n" \
"v_accvgpr_read_b32 v76, a224\n" \
"v_accvgpr_read_b32 v77, a225\n" \
"v_accvgpr_read_b32 v78, a226\n" \
"v_accvgpr_read_b32 v79, a227\n" \
"v_pk_mul_f32 v[76:77], v[76:77], s[70:71]\n" \
"v_accvgpr_read_b32 v52, a136\n" \
"v_accvgpr_read_b32 v53, a137\n" \
"v_accvgpr_read_b32 v80, a228\n" \
"v_accvgpr_read_b32 v81, a229\n" \
"v_accvgpr_read_b32 v82, a230\n" \
"v_accvgpr_read_b32 v83, a231\n" \
"v_pk_mul_f32 v[78:79], v[78:79], s[70:71]\n" \
"v_cvt_pk_bf16_f32 v76, v76, v77\n" \
"v_accvgpr_read_b32 v54, a138\n" \
"v_accvgpr_read_b32 v55, a139\n" \
"v_pk_mul_f32 v[52:53], v[52:53], s[70:71]\n" \
"v_pk_mul_f32 v[80:81], v[80:81], s[70:71]\n" \
"v_pk_mul_f32 v[82:83], v[82:83], s[70:71]\n" \
"v_cvt_pk_bf16_f32 v77, v78, v79\n" \
"v_accvgpr_read_b32 v56, a140\n" \
"v_accvgpr_read_b32 v57, a141\n" \
"v_accvgpr_read_b32 v58, a142\n" \
"v_accvgpr_read_b32 v59, a143\n" \
"v_pk_mul_f32 v[54:55], v[54:55], s[70:71]\n" \
"v_cvt_pk_bf16_f32 v52, v52, v53\n" \
"v_cvt_pk_bf16_f32 v78, v80, v81\n" \
"v_cvt_pk_bf16_f32 v79, v82, v83\n" \
"ds_write_b128 v27, v[76:79] offset:12288\n" \
"v_pk_mul_f32 v[56:57], v[56:57], s[70:71]\n" \
"v_pk_mul_f32 v[58:59], v[58:59], s[70:71]\n" \
"v_cvt_pk_bf16_f32 v53, v54, v55\n" \
"v_cvt_pk_bf16_f32 v54, v56, v57\n" \
"v_cvt_pk_bf16_f32 v55, v58, v59\n" \
"ds_write_b128 v27, v[52:55] offset:1024\n" \
"v_accvgpr_read_b32 v76, a208\n" \
"v_accvgpr_read_b32 v77, a209\n" \
"v_accvgpr_read_b32 v78, a210\n" \
"v_accvgpr_read_b32 v79, a211\n" \
"v_accvgpr_read_b32 v52, a168\n" \
"v_accvgpr_read_b32 v53, a169\n" \
"v_mfma_f32_16x16x32_bf16 v[76:79], v[148:151], v[100:103], v[76:79]\n" \
"buffer_store_dwordx4 a[40:43], v20, s[80:83], s88 offen offset:0 sc1\n" \
"v_mfma_f32_16x16x32_bf16 v[76:79], v[152:155], v[104:107], v[76:79]\n" \
"s_add_u32 s88, s45, 0x0\n" \
"s_nop 0\n" \
"v_accvgpr_read_b32 v54, a170\n" \
"v_accvgpr_read_b32 v55, a171\n" \
"v_accvgpr_read_b32 v56, a172\n" \
"v_pk_mul_f32 v[52:53], v[52:53], s[70:71]\n" \
"v_accvgpr_read_b32 v57, a173\n" \
"v_accvgpr_read_b32 v58, a174\n" \
"v_accvgpr_read_b32 v59, a175\n" \
"v_pk_mul_f32 v[54:55], v[54:55], s[70:71]\n" \
"v_cvt_pk_bf16_f32 v52, v52, v53\n" \
"v_pk_mul_f32 v[56:57], v[56:57], s[70:71]\n" \
"v_pk_mul_f32 v[58:59], v[58:59], s[70:71]\n" \
"v_cvt_pk_bf16_f32 v53, v54, v55\n" \
"v_cvt_pk_bf16_f32 v54, v56, v57\n" \
"v_cvt_pk_bf16_f32 v55, v58, v59\n" \
"ds_write_b128 v27, v[52:55] offset:5120\n" \
"v_accvgpr_read_b32 v80, a212\n" \
"v_accvgpr_read_b32 v81, a213\n" \
"v_accvgpr_read_b32 v82, a214\n" \
"v_accvgpr_read_b32 v83, a215\n" \
"v_accvgpr_read_b32 v52, a232\n" \
"v_accvgpr_read_b32 v53, a233\n" \
"v_mfma_f32_16x16x32_bf16 v[80:83], v[160:163], v[104:107], v[80:83]\n" \
"v_accvgpr_read_b32 v54, a234\n" \
"v_accvgpr_read_b32 v55, a235\n" \
"v_mfma_f32_16x16x32_bf16 v[80:83], v[156:159], v[100:103], v[80:83]\n" \
"v_accvgpr_read_b32 v56, a236\n" \
"v_accvgpr_read_b32 v57, a237\n" \
"v_accvgpr_read_b32 v58, a238\n" \
"v_accvgpr_read_b32 v59, a239\n" \
"v_pk_mul_f32 v[52:53], v[52:53], s[70:71]\n" \
"v_pk_mul_f32 v[54:55], v[54:55], s[70:71]\n" \
"v_cvt_pk_bf16_f32 v52, v52, v53\n" \
"v_pk_mul_f32 v[56:57], v[56:57], s[70:71]\n" \
"v_pk_mul_f32 v[58:59], v[58:59], s[70:71]\n" \
"v_cvt_pk_bf16_f32 v53, v54, v55\n" \
"v_cvt_pk_bf16_f32 v54, v56, v57\n" \
"v_cvt_pk_bf16_f32 v55, v58, v59\n" \
"ds_write_b128 v27, v[52:55] offset:13312\n" \
"v_accvgpr_read_b32 v52, a216\n" \
"v_accvgpr_read_b32 v53, a217\n" \
"v_accvgpr_read_b32 v54, a218\n" \
"v_accvgpr_read_b32 v55, a219\n" \
"v_accvgpr_read_b32 v60, a160\n" \
"v_accvgpr_read_b32 v61, a161\n" \
"v_mfma_f32_16x16x32_bf16 v[52:55], v[164:167], v[100:103], v[52:55]\n" \
"v_accvgpr_read_b32 v62, a162\n" \
"v_accvgpr_read_b32 v63, a163\n" \
"v_mfma_f32_16x16x32_bf16 v[52:55], v[168:171], v[104:107], v[52:55]\n" \
"v_accvgpr_read_b32 v56, a220\n" \
"v_accvgpr_read_b32 v57, a221\n" \
"v_accvgpr_read_b32 v58, a222\n" \
"v_accvgpr_read_b32 v59, a223\n" \
"buffer_store_dwordx4 a[44:47], v20, s[80:83], s89 offen offset:1024 sc1\n" \
"s_add_u32 s89, s45, 0x0\n" \
"s_nop 0\n" \
"v_mfma_f32_16x16x32_bf16 v[56:59], v[176:179], v[104:107], v[56:59]\n" \
"v_accvgpr_read_b32 v64, a164\n" \
"v_accvgpr_read_b32 v65, a165\n" \
"v_mfma_f32_16x16x32_bf16 v[56:59], v[172:175], v[100:103], v[56:59]\n" \
"v_accvgpr_read_b32 v66, a166\n" \
"v_accvgpr_read_b32 v67, a167\n" \
"v_accvgpr_read_b32 v68, a192\n" \
"v_accvgpr_read_b32 v69, a193\n" \
"v_pk_mul_f32 v[60:61], v[60:61], s[70:71]\n" \
"v_pk_mul_f32 v[62:63], v[62:63], s[70:71]\n" \
"v_cvt_pk_bf16_f32 v60, v60, v61\n" \
"v_pk_mul_f32 v[64:65], v[64:65], s[70:71]\n" \
"v_pk_mul_f32 v[66:67], v[66:67], s[70:71]\n" \
"v_cvt_pk_bf16_f32 v61, v62, v63\n" \
"v_cvt_pk_bf16_f32 v62, v64, v65\n" \
"v_cvt_pk_bf16_f32 v63, v66, v67\n" \
"ds_write_b128 v27, v[60:63] offset:4096\n" \
"v_accvgpr_read_b32 v60, a200\n" \
"v_accvgpr_read_b32 v61, a201\n" \
"v_accvgpr_read_b32 v70, a194\n" \
"v_accvgpr_read_b32 v71, a195\n" \
"v_pk_mul_f32 v[68:69], v[68:69], s[70:71]\n" \
"v_accvgpr_read_b32 v62, a202\n" \
"v_accvgpr_read_b32 v63, a203\n" \
"v_pk_mul_f32 v[60:61], v[60:61], s[70:71]\n" \
"v_accvgpr_read_b32 v72, a196\n" \
"v_accvgpr_read_b32 v73, a197\n" \
"v_accvgpr_read_b32 v74, a198\n" \
"v_accvgpr_read_b32 v75, a199\n" \
"v_pk_mul_f32 v[70:71], v[70:71], s[70:71]\n" \
"v_cvt_pk_bf16_f32 v68, v68, v69\n" \
"v_accvgpr_read_b32 v64, a204\n" \
"v_accvgpr_read_b32 v65, a205\n" \
"v_accvgpr_read_b32 v66, a206\n" \
"v_accvgpr_read_b32 v67, a207\n" \
"v_pk_mul_f32 v[62:63], v[62:63], s[70:71]\n" \
"v_cvt_pk_bf16_f32 v60, v60, v61\n" \
"v_accvgpr_read_b32 v100, a240\n" \
"v_accvgpr_read_b32 v101, a241\n" \
"v_accvgpr_read_b32 v102, a242\n" \
"v_accvgpr_read_b32 v103, a243\n" \
"v_pk_mul_f32 v[72:73], v[72:73], s[70:71]\n" \
"v_pk_mul_f32 v[74:75], v[74:75], s[70:71]\n" \
"v_mfma_f32_16x16x32_bf16 v[100:103], v[148:151], v[108:111], v[100:103]\n" \
"v_cvt_pk_bf16_f32 v69, v70, v71\n" \
"v_cvt_pk_bf16_f32 v61, v62, v63\n" \
"v_mfma_f32_16x16x32_bf16 v[100:103], v[152:155], v[112:115], v[100:103]\n" \
"v_cvt_pk_bf16_f32 v70, v72, v73\n" \
"v_cvt_pk_bf16_f32 v71, v74, v75\n" \
"ds_write_b128 v27, v[68:71] offset:8192\n" \
"v_pk_mul_f32 v[64:65], v[64:65], s[70:71]\n" \
"v_pk_mul_f32 v[66:67], v[66:67], s[70:71]\n" \
"v_cvt_pk_bf16_f32 v62, v64, v65\n" \
"v_cvt_pk_bf16_f32 v63, v66, v67\n" \
"buffer_store_dwordx4 a[72:75], v20, s[80:83], s88 offen offset:2048 sc1\n" \
"s_add_u32 s88, s45, 0x1000\n" \
"ds_write_b128 v27, v[60:63] offset:9216\n" \
"v_accvgpr_read_b32 v84, a144\n" \
"v_accvgpr_read_b32 v85, a145\n" \
"v_accvgpr_read_b32 v68, a152\n" \
"v_accvgpr_read_b32 v69, a153\n" \
"v_accvgpr_read_b32 v92, a176\n" \
"v_accvgpr_read_b32 v93, a177\n" \
"v_accvgpr_read_b32 v60, a184\n" \
"v_accvgpr_read_b32 v61, a185\n" \
"v_accvgpr_read_b32 v86, a146\n" \
"v_accvgpr_read_b32 v87, a147\n" \
"v_pk_mul_f32 v[84:85], v[84:85], s[70:71]\n" \
"v_accvgpr_read_b32 v70, a154\n" \
"v_accvgpr_read_b32 v71, a155\n" \
"v_pk_mul_f32 v[68:69], v[68:69], s[70:71]\n" \
"v_accvgpr_read_b32 v94, a178\n" \
"v_accvgpr_read_b32 v95, a179\n" \
"v_pk_mul_f32 v[92:93], v[92:93], s[70:71]\n" \
"v_accvgpr_read_b32 v62, a186\n" \
"v_accvgpr_read_b32 v63, a187\n" \
"v_pk_mul_f32 v[60:61], v[60:61], s[70:71]\n" \
"v_pk_mul_f32 v[76:77], v[76:77], s[70:71]\n" \
"v_pk_mul_f32 v[52:53], v[52:53], s[70:71]\n" \
"v_pk_mul_f32 v[100:101], v[100:101], s[70:71]\n" \
"v_accvgpr_read_b32 v88, a148\n" \
"v_accvgpr_read_b32 v89, a149\n" \
"v_accvgpr_read_b32 v90, a150\n" \
"v_accvgpr_read_b32 v91, a151\n" \
"v_pk_mul_f32 v[86:87], v[86:87], s[70:71]\n" \
"v_cvt_pk_bf16_f32 v84, v84, v85\n" \
"v_accvgpr_read_b32 v72, a156\n" \
"v_accvgpr_read_b32 v73, a157\n" \
"v_accvgpr_read_b32 v74, a158\n" \
"v_accvgpr_read_b32 v75, a159\n" \
"v_pk_mul_f32 v[70:71], v[70:71], s[70:71]\n" \
"v_cvt_pk_bf16_f32 v68, v68, v69\n" \
"v_accvgpr_read_b32 v96, a180\n" \
"v_accvgpr_read_b32 v97, a181\n" \
"v_accvgpr_read_b32 v98, a182\n" \
"v_accvgpr_read_b32 v99, a183\n" \
"v_pk_mul_f32 v[94:95], v[94:95], s[70:71]\n" \
"v_cvt_pk_bf16_f32 v92, v92, v93\n" \
"v_accvgpr_read_b32 v64, a188\n" \
"v_accvgpr_read_b32 v65, a189\n" \
"v_accvgpr_read_b32 v66, a190\n" \
"v_accvgpr_read_b32 v67, a191\n" \
"v_pk_mul_f32 v[62:63], v[62:63], s[70:71]\n" \
"v_cvt_pk_bf16_f32 v60, v60, v61\n" \
"v_pk_mul_f32 v[78:79], v[78:79], s[70:71]\n" \
"v_cvt_pk_bf16_f32 v76, v76, v77\n" \
"v_pk_mul_f32 v[54:55], v[54:55], s[70:71]\n" \
"v_cvt_pk_bf16_f32 v52, v52, v53\n" \
"v_pk_mul_f32 v[102:103], v[102:103], s[70:71]\n" \
"v_cvt_pk_bf16_f32 v100, v100, v101\n" \
"v_pk_mul_f32 v[88:89], v[88:89], s[70:71]\n" \
"v_pk_mul_f32 v[90:91], v[90:91], s[70:71]\n" \
"v_cvt_pk_bf16_f32 v85, v86, v87\n" \
"v_pk_mul_f32 v[72:73], v[72:73], s[70:71]\n" \
"v_pk_mul_f32 v[74:75], v[74:75], s[70:71]\n" \
"buffer_store_dwordx4 a[76:79], v20, s[80:83], s89 offen offset:3072 sc1\n" \
"s_add_u32 s89, s45, 0x1000\n" \
"v_cvt_pk_bf16_f32 v69, v70, v71\n" \
"v_pk_mul_f32 v[96:97], v[96:97], s[70:71]\n" \
"v_pk_mul_f32 v[98:99], v[98:99], s[70:71]\n" \
"v_cvt_pk_bf16_f32 v93, v94, v95\n" \
"v_pk_mul_f32 v[64:65], v[64:65], s[70:71]\n" \
"v_pk_mul_f32 v[66:67], v[66:67], s[70:71]\n" \
"v_cvt_pk_bf16_f32 v61, v62, v63\n" \
"v_pk_mul_f32 v[80:81], v[80:81], s[70:71]\n" \
"v_pk_mul_f32 v[82:83], v[82:83], s[70:71]\n" \
"v_cvt_pk_bf16_f32 v77, v78, v79\n" \
"v_pk_mul_f32 v[56:57], v[56:57], s[70:71]\n" \
"v_pk_mul_f32 v[58:59], v[58:59], s[70:71]\n" \
"v_cvt_pk_bf16_f32 v53, v54, v55\n" \
"v_accvgpr_read_b32 v104, a244\n" \
"v_accvgpr_read_b32 v105, a245\n" \
"v_accvgpr_read_b32 v106, a246\n" \
"v_accvgpr_read_b32 v107, a247\n" \
"v_cvt_pk_bf16_f32 v101, v102, v103\n" \
"v_cvt_pk_bf16_f32 v86, v88, v89\n" \
"v_mfma_f32_16x16x32_bf16 v[104:107], v[160:163], v[112:115], v[104:107]\n" \
"v_cvt_pk_bf16_f32 v87, v90, v91\n" \
"v_cvt_pk_bf16_f32 v70, v72, v73\n" \
"ds_write_b128 v21, v[84:87] offset:16384\n" \
"v_mfma_f32_16x16x32_bf16 v[104:107], v[156:159], v[108:111], v[104:107]\n" \
"v_cvt_pk_bf16_f32 v71, v74, v75\n" \
"ds_write_b128 v21, v[68:71] offset:17408\n" \
"v_cvt_pk_bf16_f32 v94, v96, v97\n" \
"v_cvt_pk_bf16_f32 v95, v98, v99\n" \
"ds_write_b128 v21, v[92:95] offset:20480\n" \
"v_cvt_pk_bf16_f32 v62, v64, v65\n" \
"v_cvt_pk_bf16_f32 v63, v66, v67\n" \
"ds_write_b128 v21, v[60:63] offset:21504\n" \
"v_pk_mul_f32 v[104:105], v[104:105], s[70:71]\n" \
"v_pk_mul_f32 v[106:107], v[106:107], s[70:71]\n" \
"v_cvt_pk_bf16_f32 v78, v80, v81\n" \
"v_cvt_pk_bf16_f32 v79, v82, v83\n" \
"ds_write_b128 v21, v[76:79] offset:24576\n" \
"v_cvt_pk_bf16_f32 v54, v56, v57\n" \
"v_cvt_pk_bf16_f32 v55, v58, v59\n" \
"ds_write_b128 v21, v[52:55] offset:25600\n" \
"v_cvt_pk_bf16_f32 v102, v104, v105\n" \
"v_cvt_pk_bf16_f32 v103, v106, v107\n" \
"ds_write_b128 v21, v[100:103] offset:28672\n" \
"v_accvgpr_read_b32 v84, a248\n" \
"v_accvgpr_read_b32 v85, a249\n" \
"v_accvgpr_read_b32 v86, a250\n" \
"v_accvgpr_read_b32 v87, a251\n" \
"v_accvgpr_read_b32 v88, a252\n" \
"v_accvgpr_read_b32 v89, a253\n" \
"v_mfma_f32_16x16x32_bf16 v[84:87], v[164:167], v[108:111], v[84:87]\n" \
"buffer_store_dwordx4 a[104:107], v20, s[80:83], s88 offen offset:0 sc1\n" \
"v_mfma_f32_16x16x32_bf16 v[84:87], v[168:171], v[112:115], v[84:87]\n" \
"v_accvgpr_read_b32 v90, a254\n" \
"v_accvgpr_read_b32 v91, a255\n" \
"v_sub_f32_e32 v44, 1.0, v45\n" \
"ds_write_b32 v26, v44\n" \
"v_mfma_f32_16x16x32_bf16 v[88:91], v[176:179], v[112:115], v[88:91]\n" \
"v_mfma_f32_16x16x32_bf16 v[88:91], v[172:175], v[108:111], v[88:91]\n" \
"buffer_store_dwordx4 a[108:111], v20, s[80:83], s89 offen offset:1024 sc1\n" \
"s_nop 0\n" \
"v_pk_mul_f32 v[84:85], v[84:85], s[70:71]\n" \
"v_pk_mul_f32 v[86:87], v[86:87], s[70:71]\n" \
"v_cvt_pk_bf16_f32 v84, v84, v85\n" \
"v_cvt_pk_bf16_f32 v85, v86, v87\n" \
"s_nop 1\n" \
"v_pk_mul_f32 v[88:89], v[88:89], s[70:71]\n" \
"v_pk_mul_f32 v[90:91], v[90:91], s[70:71]\n" \
"v_cvt_pk_bf16_f32 v86, v88, v89\n" \
"v_cvt_pk_bf16_f32 v87, v90, v91\n" \
"ds_write_b128 v21, v[84:87] offset:29696\n" \
"L_end_%=:\n"
#define PROJ_VCLOB "v21","v24","v25","v33","v34","v35","v36","v37","v38","v39","v40","v41","v42","v43","v44","v45","v46","v47","v48","v49","v50","v51","v52","v53","v54","v55","v56","v57","v58","v59","v60","v61","v62","v63","v64","v65","v66","v67","v68","v69","v70","v71","v72","v73","v74","v75","v76","v77","v78","v79","v80","v81","v82","v83","v84","v85","v86","v87","v88","v89","v90","v91","v92","v93","v94","v95","v96","v97","v98","v99","v100","v101","v102","v103","v104","v105","v106","v107","v108","v109","v110","v111","v112","v113","v114","v115","v116","v117","v118","v119","v120","v121","v122","v123","v124","v125","v126","v127","v128","v129","v130","v131","v132","v133","v134","v135","v136","v137","v138","v139","v140","v141","v142","v143","v144","v145","v146","v147","v148","v149","v150","v151","v152","v153","v154","v155","v156","v157","v158","v159","v160","v161","v162","v163","v164","v165","v166","v167","v168","v169","v170","v171","v172","v173","v174","v175","v176","v177","v178","v179","v180","v181","v182","v183","v184","v185","v186","v187","v188","v189","v190","v191","v192","v193","v194","v195","v196","v197","v198","v199","v200","v201","v202","v203","v204","v205","v206","v207","v208","v209","v210","v211","v212","v213","v214","v215","v216","v217","v218","v219","v220","v221","v222","v223","v224","v225","v226","v227","v228","v229","v230","v231","v232","v233","v234","v235","v236","v237","v238","v239","v240","v241","v242","v243","v244","v245","v246","v247","v248","v249","v250","v251","v252","v253","v254","v255"
#define PROJ_SCLOB "s37","s38","s39","s40","s41","s42","s43","s44","s45","s46","s47","s48","s49","s50","s51","s52","s53","s54","s55","s62","s63","s76","s77","s78","s79","s80","s81","s82","s83","s84","s85","s86","s87","s88","s89","s94","s95","s96","s97","s98","s99","s100","s101"

#define PLO(p) ((unsigned)(size_t)(p))
#define PHI(p) ((unsigned)((size_t)(p) >> 32))
__device__ __forceinline__ unsigned proj_phase(const u16* __restrict__ x, const u16* __restrict__ ckv, const u16* __restrict__ wq, const u16* __restrict__ wuk,
                                                       const u16* __restrict__ wuv, const u16* __restrict__ wqkv, const u16* __restrict__ gw, const u16* wo, const u16* wob, const u16* out,
                                                       char* __restrict__ ws, char* lds, const unsigned wid, unsigned& tag, u32x4 (&vp)[5], u32x4 (&sa)[5], unsigned (&sd)[2]) {
    typedef unsigned u32x2 __attribute__((ext_vector_type(2)));
    unsigned* const flags = (unsigned*)(ws + WS_F);
    const int b = blockIdx.x, h = (b & 7) * 4 + ((b >> 3) & 3), mt = b >> 5;
    const unsigned L0 = (unsigned)(size_t)lds, rb = (wid < 2u ? 15u - mt : mt) * 128u;
    RTS(0);
#define PST() do {} while (0)
    const unsigned lane = threadIdx.x & 63u, wr = wid >> 1, wc = wid & 1u;
    PST();
    unsigned vAc[2], vB[2];
    const unsigned sp = b & 7, rot = (PROJ_ROT) & 7u;
#pragma unroll
    for (int par = 0; par < 2; ++par) {
        const unsigned lr = lane >> 3, cc = (lane & 7u) ^ (4u * par + (lr >> 1)), r0 = rb + wc * 64u + lr;
        vAc[par] = r0 * (DC * 2) + cc * 16u;
        const unsigned br = lane >> 4, p16 = lane & 15u, sc = (((p16 >> 1) ^ (br | (par << 2))) << 1) | (p16 & 1u);
        vB[par] = (16u * wid + br) * (NBIG * 2) + h * (HD * 2) + sc * 16u;
    }
#define SRD(p, n) (u32x4){PLO(p), PHI(p) & 0xffffu, n, 0x00020000u}
    u32x4 sC = SRD(ckv, S * DC * 2), sK = SRD(wuk, DC * NBIG * 2), sV = SRD(wuv, DC * NBIG * 2);
#define PRE(n) do { SBAR(); asm volatile(PROJ_PRE##n :: "{v22}"(vAc[0]), "{v23}"(vAc[1]), "{v26}"(vB[0]), "{v27}"(vB[1]), "{s[64:67]}"(sC), "{s[68:71]}"(sK), "{s[72:75]}"(sV), \
                 [mA] "s"(L0 + wid * 8192u), [mB] "s"(L0 + 65536u + wid * 4096u), [rc] "s"(rot * 128u) : "memory", "m0", "scc", "s62", "s63", "s84", "s85"); SBAR(); } while (0)
    PRE(0);
    const unsigned fr = lane & 15u, fg = lane >> 4, r4 = (lane >> 2) & 3u, sB = r4 | ((fg & 1u) << 2);
    const unsigned aA0 = L0 + (wr * 64u + fr) * 128u + ((fg ^ (fr >> 1)) << 4);
    unsigned vl = lane * 16u, r32 = L0 + 65536u + (8u * fg + r4) * 256u + (lane & 3u) * 8u + ((sB ^ (4u * wc)) << 5);
    u32x2 r28 = {aA0, aA0 ^ 64u};
    asm volatile("" : "+v"(vl), "+v"(r28), "+v"(r32));
    PST();
    PRE(1);
    PST();
    PRE(2);
    PST();
    asm volatile("s_mov_b64 exec, 0\n s_cmp_lt_u32 %0, 2\n s_cbranch_scc1 L_chA\n s_cmp_eq_u32 %0, 2\n s_cbranch_scc1 L_pf2\n s_branch L_pf3\n"
                 "L_chA: s_cmp_eq_u32 %0, 0\n s_cbranch_scc1 L_pf0\n s_branch L_pf1\n L_retA: s_mov_b64 exec, -1" :: "s"(wid) : "scc", "memory");
    PRE(3);
    PST();
    sa[0] = (u32x4){PLO(x), PHI(x), PLO(wq), PHI(wq)}; sa[1] = (u32x4){PLO(wqkv), PHI(wqkv), PLO(gw), PHI(gw)}; sa[2] = (u32x4){PLO(wo), PHI(wo), PLO(wob), PHI(wob)}; sa[3] = (u32x4){PLO(out), PHI(out), rot * 128u, 0u};
    asm volatile(PROJ_ASM
                 : [tag] "=&s"(tag), "+{s[8:11]}"(sa[0]), "+{s[12:15]}"(sa[1]), "+{s[16:19]}"(sa[2]), "+{s[20:23]}"(sa[3]), "=&{s[56:59]}"(sa[4]), "=&{s60}"(sd[0]), "=&{s61}"(sd[1]),
                   "+{s[64:67]}"(sC), "+{s[68:71]}"(sK), "+{s[72:75]}"(sV), "+{v22}"(vAc[0]), "+{v23}"(vAc[1]), "+{v26}"(vB[0]), "+{v27}"(vB[1]), "+{v[28:29]}"(r28), "+{v32}"(r32),
                   "={v[0:3]}"(vp[0]), "={v[4:7]}"(vp[1]), "={v[8:11]}"(vp[2]), "={v[12:15]}"(vp[3]), "={v[16:19]}"(vp[4])
                 : "{v20}"(vl), [wsl] "s"(PLO(ws)), [wsh] "s"(PHI(ws)), [b] "s"(b), [w] "s"(wid), [mA] "s"(L0 + wid * 8192u), [mB] "s"(L0 + 65536u + wid * 4096u), [mS] "s"(L0 + 151552u + wid * 512u)
                 : "memory", "m0", "vcc", "scc", PROJ_VCLOB, PROJ_SCLOB, ACLOB_LO, ACLOB_HI);
    RTS(1);
    return vl;
}

namespace att {
#define DI __device__ __forceinline__
constexpr int LDS_BYTES = 163840;
constexpr unsigned LDS_G = 159744u, LDS_L = 161792u;
typedef __bf16 bfx8 __attribute__((ext_vector_type(8)));
DI unsigned rfl(unsigned x) { return __builtin_amdgcn_readfirstlane(x); }
DI f32x4 mfma(u32x4 a, u32x4 b, f32x4 c) { return __builtin_amdgcn_mfma_f32_16x16x32_bf16(__builtin_bit_cast(bfx8, a), __builtin_bit_cast(bfx8, b), c, 0, 0, 0); }
typedef __bf16 bfx2 __attribute__((ext_vector_type(2)));
typedef float f32x2 __attribute__((ext_vector_type(2)));
DI unsigned cvtpk2(float lo, float hi) { f32x2 v = {lo, hi}; bfx2 b = __builtin_convertvector(v, bfx2); return __builtin_bit_cast(unsigned, b); }
DI void att_exact(const u16* __restrict__ x, const u16* __restrict__ wq, char* __restrict__ ws, unsigned h, unsigned r0, float g1) {
    const unsigned lane = lane_id(), g = lane >> 4, c = lane & 15u;
    f32x4 o[8];
#pragma unroll
    for (int n = 0; n < 8; ++n) o[n] = (f32x4){0.f, 0.f, 0.f, 0.f};
#pragma unroll 1
    for (unsigned kk = 0; kk < (unsigned)DM; kk += 32u) {
        const unsigned k = kk + 8u * g;
        const u32x4 xf = *reinterpret_cast<const u32x4*>(x + (r0 + c) * DM + k);
#pragma unroll
        for (int n = 0; n < 8; ++n) {
            const u16* w = wq + k * NBIG + h * HD + n * 16 + c; u32x4 wf;
#pragma unroll
            for (int e = 0; e < 4; ++e) wf[e] = (unsigned)w[2 * e * NBIG] | ((unsigned)w[(2 * e + 1) * NBIG] << 16);
            o[n] = mfma(wf, xf, o[n]);
        }
    }
    u32x4 q[4];
#pragma unroll
    for (int p = 0; p < 4; ++p) {
        const f32x4 a = o[2 * p] * 0.12751743082f, b = o[2 * p + 1] * 0.12751743082f;
        q[p] = (u32x4){cvtpk2(a[0], a[1]), cvtpk2(a[2], a[3]), cvtpk2(b[0], b[1]), cvtpk2(b[2], b[3])};
    }
#pragma unroll
    for (int n = 0; n < 8; ++n) o[n] = (f32x4){0.f, 0.f, 0.f, 0.f};
    const __amdgpu_buffer_rsrc_t srd = __builtin_amdgcn_make_buffer_rsrc((void*)(ws + WS_K + (size_t)h * 1048576), (short)0, 1048576, 0x00020000);
    float m = -3.0e38f, l = 0.f;
#pragma unroll 1
    for (unsigned k0 = 0; k0 <= r0 + 15u; k0 += 32u) {
        const unsigned tb = (k0 >> 6) * 32768u + (k0 & 32u) * 256u;
        f32x4 s[2]; float mx = m;
#pragma unroll
        for (int kt = 0; kt < 2; ++kt) {
            s[kt] = (f32x4){0.f, 0.f, 0.f, 0.f};
#pragma unroll
            for (int p = 0; p < 4; ++p) s[kt] = mfma(__builtin_amdgcn_raw_buffer_load_b128(srd, lane * 16u + (kt * 4 + p) * 1024u, tb, 16), q[p], s[kt]);
#pragma unroll
            for (int e = 0; e < 4; ++e) { if (k0 + 16u * kt + 4u * g + e > r0 + c) s[kt][e] = -3.0e38f; mx = fmaxf(mx, s[kt][e]); }
        }
        mx = fmaxf(mx, __shfl_xor(mx, 16)); mx = fmaxf(mx, __shfl_xor(mx, 32));
        const float al = __builtin_amdgcn_exp2f(m - mx); m = mx;
        u32x4 pf;
#pragma unroll
        for (int e = 0; e < 4; ++e) pf[e] = cvtpk2(__builtin_amdgcn_exp2f(s[e >> 1][2 * (e & 1)] - m), __builtin_amdgcn_exp2f(s[e >> 1][2 * (e & 1) + 1] - m));
        l *= al;
#pragma unroll
        for (int e = 0; e < 4; ++e) l += __uint_as_float(pf[e] << 16) + __uint_as_float(pf[e] & 0xffff0000u);
#pragma unroll
        for (int n = 0; n < 8; ++n) o[n] = mfma(__builtin_amdgcn_raw_buffer_load_b128(srd, lane * 16u + 16384u + ((n >> 2) * 8 + (n & 3)) * 1024u, tb - (k0 & 32u) * 128u, 16), pf, o[n] * al);
    }
    l += __shfl_xor(l, 16); l += __shfl_xor(l, 32);
    const float f = g1 / l;
    const __amdgpu_buffer_rsrc_t srdA = __builtin_amdgcn_make_buffer_rsrc((void*)(ws + WS_AF), (short)0, 128 * KS_TOT * 1024, 0x00020000);
#pragma unroll
    for (int p = 0; p < 4; ++p) {
        const f32x4 a = o[2 * p] * f, b = o[2 * p + 1] * f;
        __builtin_amdgcn_raw_buffer_store_b128((u32x4){cvtpk2(a[0], a[1]), cvtpk2(a[2], a[3]), cvtpk2(b[0], b[1]), cvtpk2(b[2], b[3])}, srdA, lane * 16u + p * 1024u, ((r0 >> 4) * KS_TOT + 18u + 4u * h) * 1024u, 16);
    }
}
}

#define ATT_ASM \
"L_att0:\n" \
"s_mov_b32 s101, 0\n" \
"s_waitcnt vmcnt(22) lgkmcnt(0)\n" \
"s_barrier\n" \
"v_accvgpr_write_b32 a216, 0\n" \
"v_accvgpr_write_b32 a217, 0\n" \
"v_accvgpr_write_b32 a218, 0\n" \
"v_accvgpr_write_b32 a219, 0\n" \
"v_and_b32_e32 v63, 0x300, v20\n" \
"ds_read_b128 v[112:115], v63 offset:0\n" \
"v_mfma_f32_32x32x16_bf16 v[128:143], a[216:219], a[216:219], 0\n" \
"v_add_u32_e32 v58, s20, v20\n" \
"ds_read_b128 a[0:3], v58 offset:0\n" \
"ds_read_b128 a[16:19], v58 offset:8192\n" \
"v_add_u32_e32 v59, s21, v20\n" \
"ds_read_b128 a[32:35], v59 offset:0\n" \
"ds_read_b128 a[48:51], v59 offset:8192\n" \
"v_mfma_f32_32x32x16_bf16 v[144:159], a[216:219], a[216:219], 0\n" \
"ds_read_b128 v[116:119], v63 offset:1024\n" \
"ds_read_b128 a[4:7], v58 offset:1024\n" \
"ds_read_b128 a[20:23], v58 offset:9216\n" \
"s_sub_u32 s67, s12, 0xc000\n" \
"s_mov_b32 s65, s12\n" \
"v_add_u32_e32 v55, s67, v20\n" \
"v_mfma_f32_32x32x16_bf16 v[160:175], a[216:219], a[216:219], 0\n" \
"v_add_u32_e32 v52, 0xc000, v55\n" \
"s_xor_b32 m0, s65, 0x10000\n" \
"ds_read_b128 a[36:39], v59 offset:1024\n" \
"buffer_load_dwordx4 v52, s[8:11], 0 offen offset:0 sc1 lds\n" \
"ds_read_b128 a[52:55], v59 offset:9216\n" \
"v_mfma_f32_32x32x16_bf16 v[176:191], a[216:219], a[216:219], 0\n" \
"ds_read_b128 v[120:123], v63 offset:2048\n" \
"buffer_load_dwordx4 v52, s[8:11], 0 offen offset:1024 sc1 lds\n" \
"ds_read_b128 a[8:11], v58 offset:2048\n" \
"v_add_u32_e32 v60, s15, v20\n" \
"v_add_u32_e32 v60, 0x10000, v60\n" \
"v_mfma_f32_32x32x16_bf16 v[192:207], a[216:219], a[216:219], 0\n" \
"ds_read_b128 a[24:27], v58 offset:10240\n" \
"v_mov_b32_e32 v56, s62\n" \
"ds_read_b128 a[40:43], v59 offset:2048\n" \
"v_readlane_b32 s68, v11, 0\n" \
"v_mov_b32_e32 v54, 0x3f803f80\n" \
"v_readlane_b32 s80, v11, 9\n" \
"v_mfma_f32_32x32x16_bf16 v[208:223], a[216:219], a[216:219], 0\n" \
"ds_read_b128 a[56:59], v59 offset:10240\n" \
"s_waitcnt lgkmcnt(14)\n" \
"v_xor_b32_e32 v112, 0x80008000, v112\n" \
"ds_read_b128 v[124:127], v63 offset:3072\n" \
"v_xor_b32_e32 v113, 0x80008000, v113\n" \
"v_xor_b32_e32 v114, 0x80008000, v114\n" \
"v_mfma_f32_32x32x16_bf16 v[224:239], a[216:219], a[216:219], 0\n" \
"ds_read_b128 a[12:15], v58 offset:3072\n" \
"v_xor_b32_e32 v115, 0x80008000, v115\n" \
"ds_read_b128 a[28:31], v58 offset:11264\n" \
"v_readlane_b32 s81, v11, 10\n" \
"s_mov_b32 s82, 0x47fca0\n" \
"s_mov_b32 s83, 0x20000\n" \
"v_mfma_f32_32x32x16_bf16 v[240:255], a[216:219], a[216:219], 0\n" \
"ds_read_b128 a[44:47], v59 offset:3072\n" \
"s_mov_b32 s100, 0x71800000\n" \
"ds_read_b128 a[60:63], v59 offset:11264\n" \
"s_waitcnt lgkmcnt(14)\n" \
"v_xor_b32_e32 v116, 0x80008000, v116\n" \
"v_xor_b32_e32 v117, 0x80008000, v117\n" \
"v_mfma_f32_32x32x16_bf16 a[192:207], a[216:219], a[216:219], 0\n" \
"ds_read_b128 a[160:163], v20 offset:0\n" \
"v_xor_b32_e32 v118, 0x80008000, v118\n" \
"ds_read_b128 a[164:167], v20 offset:1024\n" \
"v_xor_b32_e32 v119, 0x80008000, v119\n" \
"s_mov_b32 s63, 0x30040000\n" \
"v_mov_b32_e32 v62, 0\n" \
"v_mfma_f32_16x16x32_bf16 v[32:35], v[112:115], a[0:3], 0\n" \
"ds_read_b128 a[168:171], v20 offset:2048\n" \
"v_accvgpr_write_b32 a212, v54\n" \
"v_mfma_f32_16x16x32_bf16 v[36:39], v[112:115], a[16:19], 0\n" \
"ds_read_b128 a[172:175], v20 offset:3072\n" \
"v_accvgpr_write_b32 a208, 0\n" \
"v_mfma_f32_16x16x32_bf16 v[40:43], v[112:115], a[32:35], 0\n" \
"ds_read_b128 a[188:191], v20 offset:7168\n" \
"s_waitcnt lgkmcnt(14)\n" \
"v_xor_b32_e32 v120, 0x80008000, v120\n" \
"v_mfma_f32_16x16x32_bf16 v[44:47], v[112:115], a[48:51], 0\n" \
"ds_read_b128 a[184:187], v20 offset:6144\n" \
"v_xor_b32_e32 v121, 0x80008000, v121\n" \
"v_mfma_f32_16x16x32_bf16 v[32:35], v[116:119], a[4:7], v[32:35]\n" \
"v_xor_b32_e32 v122, 0x80008000, v122\n" \
"v_xor_b32_e32 v123, 0x80008000, v123\n" \
"v_mfma_f32_16x16x32_bf16 v[36:39], v[116:119], a[20:23], v[36:39]\n" \
"ds_read_b128 a[180:183], v20 offset:5120\n" \
"ds_read_b128 a[176:179], v20 offset:4096\n" \
"v_mfma_f32_16x16x32_bf16 v[40:43], v[116:119], a[36:39], v[40:43]\n" \
"ds_read_b128 a[128:131], v20 offset:8192\n" \
"s_waitcnt lgkmcnt(13)\n" \
"v_xor_b32_e32 v124, 0x80008000, v124\n" \
"v_mfma_f32_16x16x32_bf16 v[44:47], v[116:119], a[52:55], v[44:47]\n" \
"v_xor_b32_e32 v125, 0x80008000, v125\n" \
"v_xor_b32_e32 v126, 0x80008000, v126\n" \
"v_mfma_f32_16x16x32_bf16 v[32:35], v[120:123], a[8:11], v[32:35]\n" \
"v_xor_b32_e32 v127, 0x80008000, v127\n" \
"ds_read_b128 a[132:135], v20 offset:9216\n" \
"v_mfma_f32_16x16x32_bf16 v[36:39], v[120:123], a[24:27], v[36:39]\n" \
"ds_read_b128 a[136:139], v20 offset:10240\n" \
"ds_read_b128 a[140:143], v20 offset:11264\n" \
"v_mfma_f32_16x16x32_bf16 v[40:43], v[120:123], a[40:43], v[40:43]\n" \
"ds_read_b128 a[144:147], v20 offset:12288\n" \
"ds_read_b128 a[148:151], v20 offset:13312\n" \
"v_mfma_f32_16x16x32_bf16 v[44:47], v[120:123], a[56:59], v[44:47]\n" \
"ds_read_b128 a[152:155], v20 offset:14336\n" \
"s_waitcnt lgkmcnt(15)\n" \
"v_mfma_f32_16x16x32_bf16 v[32:35], v[124:127], a[12:15], v[32:35]\n" \
"ds_read_b128 a[156:159], v20 offset:15360\n" \
"v_accvgpr_write_b32 a213, v54\n" \
"v_mfma_f32_16x16x32_bf16 v[36:39], v[124:127], a[28:31], v[36:39]\n" \
"ds_read_b128 a[64:67], v60 offset:16384\n" \
"v_accvgpr_write_b32 a209, 0\n" \
"v_mfma_f32_16x16x32_bf16 v[40:43], v[124:127], a[44:47], v[40:43]\n" \
"ds_read_b128 a[68:71], v60 offset:17408\n" \
"v_accvgpr_write_b32 a214, v54\n" \
"v_mfma_f32_16x16x32_bf16 v[44:47], v[124:127], a[60:63], v[44:47]\n" \
"ds_read_b128 a[72:75], v60 offset:18432\n" \
"s_waitcnt lgkmcnt(15)\n" \
"v_mfma_f32_16x16x32_bf16 v[64:67], a[160:163], a[0:3], v[32:35]\n" \
"ds_read_b128 a[76:79], v60 offset:19456\n" \
"v_accvgpr_write_b32 a210, 0\n" \
"v_mfma_f32_16x16x32_bf16 v[64:67], a[164:167], a[4:7], v[64:67]\n" \
"ds_read_b128 a[80:83], v60 offset:24576\n" \
"v_accvgpr_write_b32 a215, v54\n" \
"v_mfma_f32_16x16x32_bf16 v[64:67], a[168:171], a[8:11], v[64:67]\n" \
"ds_read_b128 a[84:87], v60 offset:25600\n" \
"v_accvgpr_write_b32 a211, 0\n" \
"v_mfma_f32_16x16x32_bf16 v[64:67], a[172:175], a[12:15], v[64:67]\n" \
"ds_read_b128 a[88:91], v60 offset:26624\n" \
"s_waitcnt lgkmcnt(15)\n" \
"v_mfma_f32_16x16x32_bf16 v[68:71], a[188:191], a[12:15], v[32:35]\n" \
"ds_read_b128 a[92:95], v60 offset:27648\n" \
"s_mov_b32 s64, 0x8000\n" \
"v_mfma_f32_16x16x32_bf16 v[68:71], a[184:187], a[8:11], v[68:71]\n" \
"ds_read_b128 a[96:99], v60 offset:0\n" \
"v_add_u32_e32 v50, 0x6000, v55\n" \
"v_mfma_f32_16x16x32_bf16 v[68:71], a[180:183], a[4:7], v[68:71]\n" \
"ds_read_b128 a[100:103], v60 offset:1024\n" \
"v_add_u32_e32 v51, 0xa000, v55\n" \
"v_mfma_f32_16x16x32_bf16 v[68:71], a[176:179], a[0:3], v[68:71]\n" \
"ds_read_b128 a[104:107], v60 offset:2048\n" \
"v_add_u32_e32 v53, 0x10000, v55\n" \
"v_mfma_f32_16x16x32_bf16 v[72:75], a[160:163], a[16:19], v[36:39]\n" \
"ds_read_b128 a[108:111], v60 offset:3072\n" \
"v_add_u32_e32 v48, 0xc000, v20\n" \
"v_mfma_f32_16x16x32_bf16 v[72:75], a[164:167], a[20:23], v[72:75]\n" \
"ds_read_b128 a[112:115], v60 offset:8192\n" \
"v_add_u32_e32 v49, s17, v20\n" \
"v_mfma_f32_16x16x32_bf16 v[72:75], a[168:171], a[24:27], v[72:75]\n" \
"ds_read_b128 a[116:119], v60 offset:9216\n" \
"v_accvgpr_write_b32 a220, v112\n" \
"v_mfma_f32_16x16x32_bf16 v[72:75], a[172:175], a[28:31], v[72:75]\n" \
"ds_read_b128 a[120:123], v60 offset:10240\n" \
"v_accvgpr_write_b32 a221, v113\n" \
"v_mfma_f32_16x16x32_bf16 v[76:79], a[188:191], a[28:31], v[36:39]\n" \
"ds_read_b128 a[124:127], v60 offset:11264\n" \
"v_accvgpr_write_b32 a222, v114\n" \
"L_pf10:\n" \
"s_cbranch_execz L_pf14\n" \
"s_nop 0\n" \
"v_mfma_f32_16x16x32_bf16 v[76:79], a[184:187], a[24:27], v[76:79]\n" \
"v_exp_f32_e32 v64, v64\n" \
"v_mfma_f32_16x16x32_bf16 v[76:79], a[180:183], a[20:23], v[76:79]\n" \
"v_exp_f32_e32 v65, v65\n" \
"v_mfma_f32_16x16x32_bf16 v[76:79], a[176:179], a[16:19], v[76:79]\n" \
"v_exp_f32_e32 v66, v66\n" \
"v_mfma_f32_16x16x32_bf16 v[80:83], a[160:163], a[32:35], v[40:43]\n" \
"v_exp_f32_e32 v67, v67\n" \
"v_mfma_f32_16x16x32_bf16 v[80:83], a[164:167], a[36:39], v[80:83]\n" \
"v_exp_f32_e32 v68, v68\n" \
"v_mfma_f32_16x16x32_bf16 v[80:83], a[168:171], a[40:43], v[80:83]\n" \
"v_exp_f32_e32 v69, v69\n" \
"v_mfma_f32_16x16x32_bf16 v[80:83], a[172:175], a[44:47], v[80:83]\n" \
"v_exp_f32_e32 v70, v70\n" \
"v_mfma_f32_16x16x32_bf16 v[84:87], a[188:191], a[44:47], v[40:43]\n" \
"v_exp_f32_e32 v71, v71\n" \
"v_mfma_f32_16x16x32_bf16 v[84:87], a[184:187], a[40:43], v[84:87]\n" \
"v_exp_f32_e32 v72, v72\n" \
"v_mfma_f32_16x16x32_bf16 v[84:87], a[180:183], a[36:39], v[84:87]\n" \
"v_exp_f32_e32 v73, v73\n" \
"v_mfma_f32_16x16x32_bf16 v[84:87], a[176:179], a[32:35], v[84:87]\n" \
"v_exp_f32_e32 v74, v74\n" \
"v_mfma_f32_16x16x32_bf16 v[88:91], a[160:163], a[48:51], v[44:47]\n" \
"v_exp_f32_e32 v75, v75\n" \
"v_mfma_f32_16x16x32_bf16 v[88:91], a[164:167], a[52:55], v[88:91]\n" \
"v_exp_f32_e32 v76, v76\n" \
"v_mfma_f32_16x16x32_bf16 v[88:91], a[168:171], a[56:59], v[88:91]\n" \
"v_exp_f32_e32 v77, v77\n" \
"v_mfma_f32_16x16x32_bf16 v[88:91], a[172:175], a[60:63], v[88:91]\n" \
"v_exp_f32_e32 v78, v78\n" \
"v_mfma_f32_16x16x32_bf16 v[92:95], a[188:191], a[60:63], v[44:47]\n" \
"v_exp_f32_e32 v79, v79\n" \
"v_mfma_f32_16x16x32_bf16 v[92:95], a[184:187], a[56:59], v[92:95]\n" \
"v_exp_f32_e32 v80, v80\n" \
"v_mfma_f32_16x16x32_bf16 v[92:95], a[180:183], a[52:55], v[92:95]\n" \
"v_exp_f32_e32 v81, v81\n" \
"v_mfma_f32_16x16x32_bf16 v[92:95], a[176:179], a[48:51], v[92:95]\n" \
"v_exp_f32_e32 v82, v82\n" \
"s_waitcnt vmcnt(2) lgkmcnt(0)\n" \
"s_barrier\n" \
"s_mov_b64 exec, 1\n" \
"buffer_store_dword v56, off, s[56:59], s68 offset:0 sc1\n" \
"buffer_store_dword v56, off, s[56:59], s22 sc1\n" \
"s_mov_b64 exec, -1\n" \
"v_exp_f32_e32 v83, v83\n" \
"v_exp_f32_e32 v84, v84\n" \
"v_exp_f32_e32 v85, v85\n" \
"v_exp_f32_e32 v86, v86\n" \
"v_exp_f32_e32 v87, v87\n" \
"v_accvgpr_write_b32 a223, v115\n" \
"v_accvgpr_write_b32 a224, v116\n" \
"v_accvgpr_write_b32 a225, v117\n" \
"v_accvgpr_write_b32 a226, v118\n" \
"v_accvgpr_write_b32 a227, v119\n" \
"v_accvgpr_write_b32 a228, v120\n" \
"v_accvgpr_write_b32 a229, v121\n" \
"v_accvgpr_write_b32 a230, v122\n" \
"v_accvgpr_write_b32 a231, v123\n" \
"v_accvgpr_write_b32 a232, v124\n" \
"v_accvgpr_write_b32 a233, v125\n" \
"v_accvgpr_write_b32 a234, v126\n" \
"v_accvgpr_write_b32 a235, v127\n" \
"v_exp_f32_e32 v88, v88\n" \
"v_exp_f32_e32 v89, v89\n" \
"v_exp_f32_e32 v90, v90\n" \
"v_exp_f32_e32 v91, v91\n" \
"v_exp_f32_e32 v92, v92\n" \
"v_exp_f32_e32 v93, v93\n" \
"v_exp_f32_e32 v94, v94\n" \
"v_exp_f32_e32 v95, v95\n" \
"v_cvt_pk_bf16_f32 v96, v64, v65\n" \
"v_cvt_pk_bf16_f32 v97, v66, v67\n" \
"v_cvt_pk_bf16_f32 v98, v68, v69\n" \
"v_cvt_pk_bf16_f32 v99, v70, v71\n" \
"v_cvt_pk_bf16_f32 v100, v72, v73\n" \
"v_cvt_pk_bf16_f32 v101, v74, v75\n" \
"v_cvt_pk_bf16_f32 v102, v76, v77\n" \
"v_cvt_pk_bf16_f32 v103, v78, v79\n" \
"v_cvt_pk_bf16_f32 v104, v80, v81\n" \
"v_cvt_pk_bf16_f32 v105, v82, v83\n" \
"v_cvt_pk_bf16_f32 v106, v84, v85\n" \
"v_cvt_pk_bf16_f32 v107, v86, v87\n" \
"s_sub_u32 s66, s18, 1\n" \
"s_cbranch_scc1 L_tr_%=\n" \
"L_1_%=:\n" \
"v_mfma_f32_16x16x32_bf16 v[64:67], a[128:131], a[0:3], v[32:35]\n" \
"s_add_u32 s63, s63, s63\n" \
"s_cbranch_scc1 L_h1_%=\n" \
"L_hr1_%=:\n" \
"v_mfma_f32_16x16x32_bf16 v[64:67], a[132:135], a[4:7], v[64:67]\n" \
"s_xor_b32 m0, s65, 0x12000\n" \
"ds_read_b128 a[160:163], v49 offset:0\n" \
"v_mfma_f32_16x16x32_bf16 v[64:67], a[136:139], a[8:11], v[64:67]\n" \
"buffer_load_dwordx4 v50, s[8:11], s64 offen offset:0 sc1 lds\n" \
"v_mfma_f32_16x16x32_bf16 v[64:67], a[140:143], a[12:15], v[64:67]\n" \
"ds_read_b128 a[176:179], v49 offset:4096\n" \
"ds_read_b128 a[180:183], v49 offset:5120\n" \
"v_mfma_f32_16x16x32_bf16 v[68:71], a[156:159], a[12:15], v[32:35]\n" \
"v_cvt_pk_bf16_f32 v108, v88, v89\n" \
"ds_read_b128 a[164:167], v49 offset:1024\n" \
"v_mfma_f32_16x16x32_bf16 v[68:71], a[152:155], a[8:11], v[68:71]\n" \
"v_cvt_pk_bf16_f32 v109, v90, v91\n" \
"ds_read_b128 a[168:171], v49 offset:2048\n" \
"v_mfma_f32_16x16x32_bf16 v[68:71], a[148:151], a[4:7], v[68:71]\n" \
"v_cvt_pk_bf16_f32 v110, v92, v93\n" \
"ds_read_b128 a[184:187], v49 offset:6144\n" \
"v_mfma_f32_16x16x32_bf16 v[68:71], a[144:147], a[0:3], v[68:71]\n" \
"v_exp_f32_e32 v64, v64\n" \
"v_mfma_f32_16x16x32_bf16 v[72:75], a[128:131], a[16:19], v[36:39]\n" \
"ds_read_b128 a[188:191], v49 offset:7168\n" \
"ds_read_b128 a[172:175], v49 offset:3072\n" \
"v_mfma_f32_16x16x32_bf16 v[72:75], a[132:135], a[20:23], v[72:75]\n" \
"v_exp_f32_e32 v65, v65\n" \
"v_mfma_f32_16x16x32_bf16 v[72:75], a[136:139], a[24:27], v[72:75]\n" \
"v_cvt_pk_bf16_f32 v111, v94, v95\n" \
"v_cvt_pk_bf16_f32 v112, v64, v65\n" \
"v_mfma_f32_16x16x32_bf16 v[72:75], a[140:143], a[28:31], v[72:75]\n" \
"v_exp_f32_e32 v66, v66\n" \
"v_mfma_f32_16x16x32_bf16 v[76:79], a[156:159], a[28:31], v[36:39]\n" \
"v_exp_f32_e32 v67, v67\n" \
"v_mfma_f32_16x16x32_bf16 v[76:79], a[152:155], a[24:27], v[76:79]\n" \
"v_exp_f32_e32 v68, v68\n" \
"v_mfma_f32_16x16x32_bf16 v[76:79], a[148:151], a[20:23], v[76:79]\n" \
"v_exp_f32_e32 v69, v69\n" \
"v_mfma_f32_16x16x32_bf16 v[76:79], a[144:147], a[16:19], v[76:79]\n" \
"v_exp_f32_e32 v70, v70\n" \
"v_mfma_f32_16x16x32_bf16 v[80:83], a[128:131], a[32:35], v[40:43]\n" \
"v_exp_f32_e32 v71, v71\n" \
"v_mfma_f32_16x16x32_bf16 v[80:83], a[132:135], a[36:39], v[80:83]\n" \
"v_cvt_pk_bf16_f32 v113, v66, v67\n" \
"v_cvt_pk_bf16_f32 v114, v68, v69\n" \
"v_mfma_f32_16x16x32_bf16 v[80:83], a[136:139], a[40:43], v[80:83]\n" \
"buffer_load_dwordx4 v50, s[8:11], s64 offen offset:1024 sc1 lds\n" \
"v_mfma_f32_16x16x32_bf16 v[80:83], a[140:143], a[44:47], v[80:83]\n" \
"v_cvt_pk_bf16_f32 v115, v70, v71\n" \
"s_xor_b32 m0, s65, 0x6000\n" \
"v_mfma_f32_16x16x32_bf16 v[84:87], a[156:159], a[44:47], v[40:43]\n" \
"v_exp_f32_e32 v72, v72\n" \
"v_mfma_f32_16x16x32_bf16 v[84:87], a[152:155], a[40:43], v[84:87]\n" \
"v_exp_f32_e32 v73, v73\n" \
"v_mfma_f32_16x16x32_bf16 v[84:87], a[148:151], a[36:39], v[84:87]\n" \
"v_exp_f32_e32 v74, v74\n" \
"v_mfma_f32_16x16x32_bf16 v[84:87], a[144:147], a[32:35], v[84:87]\n" \
"v_exp_f32_e32 v75, v75\n" \
"v_mfma_f32_16x16x32_bf16 v[88:91], a[128:131], a[48:51], v[44:47]\n" \
"v_exp_f32_e32 v76, v76\n" \
"v_mfma_f32_16x16x32_bf16 v[88:91], a[132:135], a[52:55], v[88:91]\n" \
"v_exp_f32_e32 v77, v77\n" \
"v_mfma_f32_16x16x32_bf16 v[88:91], a[136:139], a[56:59], v[88:91]\n" \
"v_exp_f32_e32 v78, v78\n" \
"v_mfma_f32_16x16x32_bf16 v[88:91], a[140:143], a[60:63], v[88:91]\n" \
"v_exp_f32_e32 v79, v79\n" \
"v_mfma_f32_16x16x32_bf16 v[92:95], a[156:159], a[60:63], v[44:47]\n" \
"v_cvt_pk_bf16_f32 v116, v72, v73\n" \
"v_cvt_pk_bf16_f32 v117, v74, v75\n" \
"v_mfma_f32_16x16x32_bf16 v[92:95], a[152:155], a[56:59], v[92:95]\n" \
"v_cvt_pk_bf16_f32 v118, v76, v77\n" \
"v_cvt_pk_bf16_f32 v119, v78, v79\n" \
"v_mfma_f32_16x16x32_bf16 v[92:95], a[148:151], a[52:55], v[92:95]\n" \
"v_exp_f32_e32 v80, v80\n" \
"v_mfma_f32_16x16x32_bf16 v[92:95], a[144:147], a[48:51], v[92:95]\n" \
"ds_read_b128 a[128:131], v48 offset:0\n" \
"s_waitcnt lgkmcnt(1)\n" \
"v_mfma_f32_16x16x32_bf16 v[128:131], a[160:163], v[96:99], v[128:131]\n" \
"buffer_load_dwordx4 v51, s[8:11], s64 offen offset:0 sc1 lds\n" \
"v_mfma_f32_16x16x32_bf16 v[128:131], a[176:179], v[112:115], v[128:131]\n" \
"v_exp_f32_e32 v81, v81\n" \
"v_mfma_f32_16x16x32_bf16 v[132:135], a[180:183], v[112:115], v[132:135]\n" \
"v_exp_f32_e32 v82, v82\n" \
"v_mfma_f32_16x16x32_bf16 v[132:135], a[164:167], v[96:99], v[132:135]\n" \
"v_exp_f32_e32 v83, v83\n" \
"v_mfma_f32_16x16x32_bf16 v[136:139], a[168:171], v[96:99], v[136:139]\n" \
"v_exp_f32_e32 v84, v84\n" \
"v_mfma_f32_16x16x32_bf16 v[136:139], a[184:187], v[112:115], v[136:139]\n" \
"v_exp_f32_e32 v85, v85\n" \
"v_mfma_f32_16x16x32_bf16 v[140:143], a[188:191], v[112:115], v[140:143]\n" \
"v_exp_f32_e32 v86, v86\n" \
"v_mfma_f32_16x16x32_bf16 v[140:143], a[172:175], v[96:99], v[140:143]\n" \
"v_exp_f32_e32 v87, v87\n" \
"v_mfma_f32_16x16x32_bf16 a[192:195], a[212:215], v[96:99], a[192:195]\n" \
"v_cvt_pk_bf16_f32 v120, v80, v81\n" \
"v_cvt_pk_bf16_f32 v121, v82, v83\n" \
"v_mfma_f32_16x16x32_bf16 a[192:195], a[212:215], v[112:115], a[192:195]\n" \
"v_cvt_pk_bf16_f32 v122, v84, v85\n" \
"v_cvt_pk_bf16_f32 v123, v86, v87\n" \
"v_mfma_f32_16x16x32_bf16 v[160:163], a[160:163], v[100:103], v[160:163]\n" \
"ds_read_b128 a[132:135], v48 offset:1024\n" \
"ds_read_b128 a[136:139], v48 offset:2048\n" \
"v_mfma_f32_16x16x32_bf16 v[160:163], a[176:179], v[116:119], v[160:163]\n" \
"v_exp_f32_e32 v88, v88\n" \
"v_mfma_f32_16x16x32_bf16 v[164:167], a[180:183], v[116:119], v[164:167]\n" \
"ds_read_b128 a[140:143], v48 offset:3072\n" \
"ds_read_b128 a[144:147], v48 offset:4096\n" \
"v_mfma_f32_16x16x32_bf16 v[164:167], a[164:167], v[100:103], v[164:167]\n" \
"v_exp_f32_e32 v89, v89\n" \
"v_mfma_f32_16x16x32_bf16 v[168:171], a[168:171], v[100:103], v[168:171]\n" \
"ds_read_b128 a[148:151], v48 offset:5120\n" \
"ds_read_b128 a[152:155], v48 offset:6144\n" \
"v_mfma_f32_16x16x32_bf16 v[168:171], a[184:187], v[116:119], v[168:171]\n" \
"v_exp_f32_e32 v90, v90\n" \
"v_mfma_f32_16x16x32_bf16 v[172:175], a[188:191], v[116:119], v[172:175]\n" \
"ds_read_b128 a[156:159], v48 offset:7168\n" \
"v_cvt_pk_bf16_f32 v124, v88, v89\n" \
"v_mfma_f32_16x16x32_bf16 v[172:175], a[172:175], v[100:103], v[172:175]\n" \
"v_exp_f32_e32 v91, v91\n" \
"v_mfma_f32_16x16x32_bf16 a[196:199], a[212:215], v[100:103], a[196:199]\n" \
"v_exp_f32_e32 v92, v92\n" \
"v_mfma_f32_16x16x32_bf16 a[196:199], a[212:215], v[116:119], a[196:199]\n" \
"v_exp_f32_e32 v93, v93\n" \
"v_mfma_f32_16x16x32_bf16 v[192:195], a[160:163], v[104:107], v[192:195]\n" \
"v_exp_f32_e32 v94, v94\n" \
"v_mfma_f32_16x16x32_bf16 v[192:195], a[176:179], v[120:123], v[192:195]\n" \
"v_exp_f32_e32 v95, v95\n" \
"v_mfma_f32_16x16x32_bf16 v[196:199], a[180:183], v[120:123], v[196:199]\n" \
"v_cvt_pk_bf16_f32 v125, v90, v91\n" \
"v_cvt_pk_bf16_f32 v126, v92, v93\n" \
"v_mfma_f32_16x16x32_bf16 v[196:199], a[164:167], v[104:107], v[196:199]\n" \
"buffer_load_dwordx4 v51, s[8:11], s64 offen offset:1024 sc1 lds\n" \
"v_mfma_f32_16x16x32_bf16 v[200:203], a[168:171], v[104:107], v[200:203]\n" \
"v_cvt_pk_bf16_f32 v127, v94, v95\n" \
"v_mfma_f32_16x16x32_bf16 v[200:203], a[184:187], v[120:123], v[200:203]\n" \
"v_mfma_f32_16x16x32_bf16 v[204:207], a[188:191], v[120:123], v[204:207]\n" \
"v_mfma_f32_16x16x32_bf16 v[204:207], a[172:175], v[104:107], v[204:207]\n" \
"v_mfma_f32_16x16x32_bf16 v[224:227], a[160:163], v[108:111], v[224:227]\n" \
"v_mfma_f32_16x16x32_bf16 v[224:227], a[176:179], v[124:127], v[224:227]\n" \
"v_mfma_f32_16x16x32_bf16 v[228:231], a[180:183], v[124:127], v[228:231]\n" \
"v_mfma_f32_16x16x32_bf16 v[228:231], a[164:167], v[108:111], v[228:231]\n" \
"v_mfma_f32_16x16x32_bf16 v[232:235], a[168:171], v[108:111], v[232:235]\n" \
"v_mfma_f32_16x16x32_bf16 v[232:235], a[184:187], v[124:127], v[232:235]\n" \
"v_mfma_f32_16x16x32_bf16 v[236:239], a[188:191], v[124:127], v[236:239]\n" \
"v_mfma_f32_16x16x32_bf16 v[236:239], a[172:175], v[108:111], v[236:239]\n" \
"s_waitcnt vmcnt(4) lgkmcnt(0)\n" \
"s_barrier\n" \
"v_mfma_f32_16x16x32_bf16 v[64:67], a[128:131], a[0:3], v[32:35]\n" \
"s_xor_b32 m0, s65, 0x14000\n" \
"ds_read_b128 a[160:163], v49 offset:8192\n" \
"v_mfma_f32_16x16x32_bf16 v[64:67], a[132:135], a[4:7], v[64:67]\n" \
"buffer_load_dwordx4 v52, s[8:11], s64 offen offset:0 sc1 lds\n" \
"v_mfma_f32_16x16x32_bf16 v[64:67], a[136:139], a[8:11], v[64:67]\n" \
"ds_read_b128 a[176:179], v49 offset:12288\n" \
"ds_read_b128 a[180:183], v49 offset:13312\n" \
"v_mfma_f32_16x16x32_bf16 v[64:67], a[140:143], a[12:15], v[64:67]\n" \
"v_mfma_f32_16x16x32_bf16 v[68:71], a[156:159], a[12:15], v[32:35]\n" \
"ds_read_b128 a[164:167], v49 offset:9216\n" \
"ds_read_b128 a[168:171], v49 offset:10240\n" \
"v_mfma_f32_16x16x32_bf16 v[68:71], a[152:155], a[8:11], v[68:71]\n" \
"v_mfma_f32_16x16x32_bf16 v[68:71], a[148:151], a[4:7], v[68:71]\n" \
"ds_read_b128 a[184:187], v49 offset:14336\n" \
"ds_read_b128 a[188:191], v49 offset:15360\n" \
"v_mfma_f32_16x16x32_bf16 v[68:71], a[144:147], a[0:3], v[68:71]\n" \
"v_exp_f32_e32 v64, v64\n" \
"v_mfma_f32_16x16x32_bf16 v[72:75], a[128:131], a[16:19], v[36:39]\n" \
"v_exp_f32_e32 v65, v65\n" \
"v_mfma_f32_16x16x32_bf16 v[72:75], a[132:135], a[20:23], v[72:75]\n" \
"v_exp_f32_e32 v66, v66\n" \
"v_mfma_f32_16x16x32_bf16 v[72:75], a[136:139], a[24:27], v[72:75]\n" \
"v_exp_f32_e32 v67, v67\n" \
"v_mfma_f32_16x16x32_bf16 v[72:75], a[140:143], a[28:31], v[72:75]\n" \
"v_exp_f32_e32 v68, v68\n" \
"v_mfma_f32_16x16x32_bf16 v[76:79], a[156:159], a[28:31], v[36:39]\n" \
"v_exp_f32_e32 v69, v69\n" \
"v_mfma_f32_16x16x32_bf16 v[76:79], a[152:155], a[24:27], v[76:79]\n" \
"v_exp_f32_e32 v70, v70\n" \
"v_mfma_f32_16x16x32_bf16 v[76:79], a[148:151], a[20:23], v[76:79]\n" \
"v_exp_f32_e32 v71, v71\n" \
"v_mfma_f32_16x16x32_bf16 v[76:79], a[144:147], a[16:19], v[76:79]\n" \
"buffer_load_dwordx4 v52, s[8:11], s64 offen offset:1024 sc1 lds\n" \
"v_mfma_f32_16x16x32_bf16 v[80:83], a[128:131], a[32:35], v[40:43]\n" \
"ds_read_b128 a[172:175], v49 offset:11264\n" \
"s_mov_b32 m0, s65\n" \
"v_mfma_f32_16x16x32_bf16 v[80:83], a[132:135], a[36:39], v[80:83]\n" \
"v_exp_f32_e32 v72, v72\n" \
"v_mfma_f32_16x16x32_bf16 v[80:83], a[136:139], a[40:43], v[80:83]\n" \
"v_exp_f32_e32 v73, v73\n" \
"v_mfma_f32_16x16x32_bf16 v[80:83], a[140:143], a[44:47], v[80:83]\n" \
"v_exp_f32_e32 v74, v74\n" \
"v_mfma_f32_16x16x32_bf16 v[84:87], a[156:159], a[44:47], v[40:43]\n" \
"v_exp_f32_e32 v75, v75\n" \
"v_mfma_f32_16x16x32_bf16 v[84:87], a[152:155], a[40:43], v[84:87]\n" \
"v_exp_f32_e32 v76, v76\n" \
"v_mfma_f32_16x16x32_bf16 v[84:87], a[148:151], a[36:39], v[84:87]\n" \
"v_exp_f32_e32 v77, v77\n" \
"v_mfma_f32_16x16x32_bf16 v[84:87], a[144:147], a[32:35], v[84:87]\n" \
"v_exp_f32_e32 v78, v78\n" \
"v_mfma_f32_16x16x32_bf16 v[88:91], a[128:131], a[48:51], v[44:47]\n" \
"ds_read_b128 a[128:131], v48 offset:8192\n" \
"v_add_u32_e32 v49, 0x10000, v48\n" \
"v_mfma_f32_16x16x32_bf16 v[88:91], a[132:135], a[52:55], v[88:91]\n" \
"ds_read_b128 a[132:135], v48 offset:9216\n" \
"s_xor_b32 s65, s65, 0x4000\n" \
"v_mfma_f32_16x16x32_bf16 v[88:91], a[136:139], a[56:59], v[88:91]\n" \
"v_exp_f32_e32 v79, v79\n" \
"v_mfma_f32_16x16x32_bf16 v[88:91], a[140:143], a[60:63], v[88:91]\n" \
"ds_read_b128 a[136:139], v48 offset:10240\n" \
"ds_read_b128 a[140:143], v48 offset:11264\n" \
"v_mfma_f32_16x16x32_bf16 v[92:95], a[156:159], a[60:63], v[44:47]\n" \
"v_exp_f32_e32 v80, v80\n" \
"v_mfma_f32_16x16x32_bf16 v[92:95], a[152:155], a[56:59], v[92:95]\n" \
"ds_read_b128 a[152:155], v48 offset:14336\n" \
"ds_read_b128 a[156:159], v48 offset:15360\n" \
"v_mfma_f32_16x16x32_bf16 v[92:95], a[148:151], a[52:55], v[92:95]\n" \
"v_exp_f32_e32 v81, v81\n" \
"v_mfma_f32_16x16x32_bf16 v[92:95], a[144:147], a[48:51], v[92:95]\n" \
"ds_read_b128 a[144:147], v48 offset:12288\n" \
"s_waitcnt lgkmcnt(7)\n" \
"v_mfma_f32_16x16x32_bf16 v[144:147], a[160:163], v[96:99], v[144:147]\n" \
"buffer_load_dwordx4 v53, s[8:11], s64 offen offset:0 sc1 lds\n" \
"v_mfma_f32_16x16x32_bf16 v[144:147], a[176:179], v[112:115], v[144:147]\n" \
"v_exp_f32_e32 v82, v82\n" \
"v_mfma_f32_16x16x32_bf16 v[148:151], a[180:183], v[112:115], v[148:151]\n" \
"v_exp_f32_e32 v83, v83\n" \
"v_mfma_f32_16x16x32_bf16 v[148:151], a[164:167], v[96:99], v[148:151]\n" \
"v_exp_f32_e32 v84, v84\n" \
"v_mfma_f32_16x16x32_bf16 v[152:155], a[168:171], v[96:99], v[152:155]\n" \
"v_exp_f32_e32 v85, v85\n" \
"v_mfma_f32_16x16x32_bf16 v[152:155], a[184:187], v[112:115], v[152:155]\n" \
"v_exp_f32_e32 v86, v86\n" \
"v_mfma_f32_16x16x32_bf16 v[156:159], a[188:191], v[112:115], v[156:159]\n" \
"v_exp_f32_e32 v87, v87\n" \
"v_mfma_f32_16x16x32_bf16 v[156:159], a[172:175], v[96:99], v[156:159]\n" \
"ds_read_b128 a[148:151], v48 offset:13312\n" \
"v_cvt_pk_bf16_f32 v96, v64, v65\n" \
"v_mfma_f32_16x16x32_bf16 v[176:179], a[160:163], v[100:103], v[176:179]\n" \
"v_exp_f32_e32 v88, v88\n" \
"v_mfma_f32_16x16x32_bf16 v[176:179], a[176:179], v[116:119], v[176:179]\n" \
"v_exp_f32_e32 v89, v89\n" \
"v_mfma_f32_16x16x32_bf16 v[180:183], a[180:183], v[116:119], v[180:183]\n" \
"v_exp_f32_e32 v90, v90\n" \
"v_mfma_f32_16x16x32_bf16 v[180:183], a[164:167], v[100:103], v[180:183]\n" \
"v_exp_f32_e32 v91, v91\n" \
"v_mfma_f32_16x16x32_bf16 v[184:187], a[168:171], v[100:103], v[184:187]\n" \
"v_exp_f32_e32 v92, v92\n" \
"v_mfma_f32_16x16x32_bf16 v[184:187], a[184:187], v[116:119], v[184:187]\n" \
"v_exp_f32_e32 v93, v93\n" \
"v_mfma_f32_16x16x32_bf16 v[188:191], a[188:191], v[116:119], v[188:191]\n" \
"buffer_load_dwordx4 v53, s[8:11], s64 offen offset:1024 sc1 lds\n" \
"v_mfma_f32_16x16x32_bf16 v[188:191], a[172:175], v[100:103], v[188:191]\n" \
"s_add_u32 s64, s64, 0x8000\n" \
"v_cvt_pk_bf16_f32 v97, v66, v67\n" \
"v_mfma_f32_16x16x32_bf16 v[208:211], a[160:163], v[104:107], v[208:211]\n" \
"v_exp_f32_e32 v94, v94\n" \
"v_mfma_f32_16x16x32_bf16 v[208:211], a[176:179], v[120:123], v[208:211]\n" \
"v_exp_f32_e32 v95, v95\n" \
"v_mfma_f32_16x16x32_bf16 v[212:215], a[180:183], v[120:123], v[212:215]\n" \
"v_cvt_pk_bf16_f32 v98, v68, v69\n" \
"v_cvt_pk_bf16_f32 v99, v70, v71\n" \
"v_mfma_f32_16x16x32_bf16 v[212:215], a[164:167], v[104:107], v[212:215]\n" \
"v_cvt_pk_bf16_f32 v100, v72, v73\n" \
"v_cvt_pk_bf16_f32 v101, v74, v75\n" \
"v_mfma_f32_16x16x32_bf16 v[216:219], a[168:171], v[104:107], v[216:219]\n" \
"v_cvt_pk_bf16_f32 v102, v76, v77\n" \
"v_cvt_pk_bf16_f32 v103, v78, v79\n" \
"v_mfma_f32_16x16x32_bf16 v[216:219], a[184:187], v[120:123], v[216:219]\n" \
"v_xor_b32_e32 v48, 0x4000, v48\n" \
"s_sub_u32 s66, s66, 1\n" \
"v_mfma_f32_16x16x32_bf16 v[220:223], a[188:191], v[120:123], v[220:223]\n" \
"v_mfma_f32_16x16x32_bf16 v[220:223], a[172:175], v[104:107], v[220:223]\n" \
"v_mfma_f32_16x16x32_bf16 a[200:203], a[212:215], v[104:107], a[200:203]\n" \
"v_cvt_pk_bf16_f32 v104, v80, v81\n" \
"v_cvt_pk_bf16_f32 v105, v82, v83\n" \
"v_mfma_f32_16x16x32_bf16 a[200:203], a[212:215], v[120:123], a[200:203]\n" \
"v_cvt_pk_bf16_f32 v106, v84, v85\n" \
"v_cvt_pk_bf16_f32 v107, v86, v87\n" \
"v_mfma_f32_16x16x32_bf16 v[240:243], a[160:163], v[108:111], v[240:243]\n" \
"v_mfma_f32_16x16x32_bf16 v[240:243], a[176:179], v[124:127], v[240:243]\n" \
"v_mfma_f32_16x16x32_bf16 v[244:247], a[180:183], v[124:127], v[244:247]\n" \
"v_mfma_f32_16x16x32_bf16 v[244:247], a[164:167], v[108:111], v[244:247]\n" \
"v_mfma_f32_16x16x32_bf16 v[248:251], a[168:171], v[108:111], v[248:251]\n" \
"v_mfma_f32_16x16x32_bf16 v[248:251], a[184:187], v[124:127], v[248:251]\n" \
"v_mfma_f32_16x16x32_bf16 v[252:255], a[188:191], v[124:127], v[252:255]\n" \
"v_mfma_f32_16x16x32_bf16 v[252:255], a[172:175], v[108:111], v[252:255]\n" \
"v_mfma_f32_16x16x32_bf16 a[204:207], a[212:215], v[108:111], a[204:207]\n" \
"v_mfma_f32_16x16x32_bf16 a[204:207], a[212:215], v[124:127], a[204:207]\n" \
"s_waitcnt vmcnt(4) lgkmcnt(0)\n" \
"s_barrier\n" \
"s_cbranch_scc0 L_1_%=\n" \
"L_tr_%=:\n" \
"L_pf11:\n" \
"s_cbranch_execz L_pf15\n" \
"s_nop 0\n" \
"s_cmp_lg_u32 s16, 0\n" \
"s_cbranch_scc1 L_sw_%=\n" \
"v_mfma_f32_16x16x32_bf16 v[64:67], a[128:131], a[0:3], v[32:35]\n" \
"s_xor_b32 m0, s65, 0x12000\n" \
"s_mov_b32 s65, s13\n" \
"v_mfma_f32_16x16x32_bf16 v[64:67], a[132:135], a[4:7], v[64:67]\n" \
"buffer_load_dwordx4 v50, s[8:11], s64 offen offset:0 sc1 lds\n" \
"v_mfma_f32_16x16x32_bf16 v[64:67], a[136:139], a[8:11], v[64:67]\n" \
"v_add_u32_e32 v63, s15, v20\n" \
"s_add_u32 s67, s14, 40960\n" \
"v_mfma_f32_16x16x32_bf16 v[64:67], a[140:143], a[12:15], v[64:67]\n" \
"ds_read_b128 a[160:163], v49 offset:0\n" \
"ds_read_b128 a[176:179], v49 offset:4096\n" \
"v_mfma_f32_16x16x32_bf16 v[68:71], a[156:159], a[12:15], v[32:35]\n" \
"buffer_load_dwordx4 v50, s[8:11], s64 offen offset:1024 sc1 lds\n" \
"v_mfma_f32_16x16x32_bf16 v[68:71], a[152:155], a[8:11], v[68:71]\n" \
"v_add_u32_e32 v50, 0x6000, v63\n" \
"s_xor_b32 m0, s65, 0x8000\n" \
"v_mfma_f32_16x16x32_bf16 v[68:71], a[148:151], a[4:7], v[68:71]\n" \
"ds_read_b128 a[180:183], v49 offset:5120\n" \
"ds_read_b128 a[164:167], v49 offset:1024\n" \
"v_mfma_f32_16x16x32_bf16 v[68:71], a[144:147], a[0:3], v[68:71]\n" \
"ds_read_b128 a[168:171], v49 offset:2048\n" \
"ds_read_b128 a[184:187], v49 offset:6144\n" \
"v_mfma_f32_16x16x32_bf16 v[72:75], a[128:131], a[16:19], v[36:39]\n" \
"buffer_load_dwordx4 v50, s[8:11], s67 offen offset:0 sc1 lds\n" \
"v_mfma_f32_16x16x32_bf16 v[72:75], a[132:135], a[20:23], v[72:75]\n" \
"v_cvt_pk_bf16_f32 v108, v88, v89\n" \
"v_cvt_pk_bf16_f32 v109, v90, v91\n" \
"v_mfma_f32_16x16x32_bf16 v[72:75], a[136:139], a[24:27], v[72:75]\n" \
"ds_read_b128 a[188:191], v49 offset:7168\n" \
"ds_read_b128 a[172:175], v49 offset:3072\n" \
"v_mfma_f32_16x16x32_bf16 v[72:75], a[140:143], a[28:31], v[72:75]\n" \
"v_cvt_pk_bf16_f32 v110, v92, v93\n" \
"v_cvt_pk_bf16_f32 v111, v94, v95\n" \
"v_mfma_f32_16x16x32_bf16 v[76:79], a[156:159], a[28:31], v[36:39]\n" \
"v_exp_f32_e32 v64, v64\n" \
"v_mfma_f32_16x16x32_bf16 v[76:79], a[152:155], a[24:27], v[76:79]\n" \
"buffer_load_dwordx4 v50, s[8:11], s67 offen offset:1024 sc1 lds\n" \
"v_mfma_f32_16x16x32_bf16 v[76:79], a[148:151], a[20:23], v[76:79]\n" \
"v_exp_f32_e32 v65, v65\n" \
"v_mfma_f32_16x16x32_bf16 v[76:79], a[144:147], a[16:19], v[76:79]\n" \
"v_exp_f32_e32 v66, v66\n" \
"v_mfma_f32_16x16x32_bf16 v[80:83], a[128:131], a[32:35], v[40:43]\n" \
"v_exp_f32_e32 v67, v67\n" \
"v_mfma_f32_16x16x32_bf16 v[80:83], a[132:135], a[36:39], v[80:83]\n" \
"buffer_load_dwordx4 v50, s[8:11], s67 offen offset:2048 sc1 lds\n" \
"v_mfma_f32_16x16x32_bf16 v[80:83], a[136:139], a[40:43], v[80:83]\n" \
"v_exp_f32_e32 v68, v68\n" \
"v_mfma_f32_16x16x32_bf16 v[80:83], a[140:143], a[44:47], v[80:83]\n" \
"v_exp_f32_e32 v69, v69\n" \
"v_mfma_f32_16x16x32_bf16 v[84:87], a[156:159], a[44:47], v[40:43]\n" \
"v_exp_f32_e32 v70, v70\n" \
"v_mfma_f32_16x16x32_bf16 v[84:87], a[152:155], a[40:43], v[84:87]\n" \
"v_exp_f32_e32 v71, v71\n" \
"v_mfma_f32_16x16x32_bf16 v[84:87], a[148:151], a[36:39], v[84:87]\n" \
"buffer_load_dwordx4 v50, s[8:11], s67 offen offset:3072 sc1 lds\n" \
"v_mfma_f32_16x16x32_bf16 v[84:87], a[144:147], a[32:35], v[84:87]\n" \
"v_cvt_pk_bf16_f32 v112, v64, v65\n" \
"v_cvt_pk_bf16_f32 v113, v66, v67\n" \
"v_mfma_f32_16x16x32_bf16 v[88:91], a[128:131], a[48:51], v[44:47]\n" \
"v_cvt_pk_bf16_f32 v114, v68, v69\n" \
"v_cvt_pk_bf16_f32 v115, v70, v71\n" \
"v_mfma_f32_16x16x32_bf16 v[88:91], a[132:135], a[52:55], v[88:91]\n" \
"s_mov_b32 s64, s14\n" \
"v_add_u32_e32 v51, 0x12000, v63\n" \
"v_mfma_f32_16x16x32_bf16 v[88:91], a[136:139], a[56:59], v[88:91]\n" \
"s_xor_b32 m0, s65, 0xa000\n" \
"v_readlane_b32 s93, v11, 17\n" \
"v_mfma_f32_16x16x32_bf16 v[88:91], a[140:143], a[60:63], v[88:91]\n" \
"buffer_load_dwordx4 v51, s[8:11], s64 offen offset:0 sc1 lds\n" \
"v_mfma_f32_16x16x32_bf16 v[92:95], a[156:159], a[60:63], v[44:47]\n" \
"v_exp_f32_e32 v72, v72\n" \
"v_mfma_f32_16x16x32_bf16 v[92:95], a[152:155], a[56:59], v[92:95]\n" \
"v_exp_f32_e32 v73, v73\n" \
"v_mfma_f32_16x16x32_bf16 v[92:95], a[148:151], a[52:55], v[92:95]\n" \
"v_exp_f32_e32 v74, v74\n" \
"v_mfma_f32_16x16x32_bf16 v[92:95], a[144:147], a[48:51], v[92:95]\n" \
"v_cvt_pk_bf16_f32 v116, v72, v73\n" \
"s_waitcnt lgkmcnt(0)\n" \
"v_mfma_f32_16x16x32_bf16 v[128:131], a[160:163], v[96:99], v[128:131]\n" \
"buffer_load_dwordx4 v51, s[8:11], s64 offen offset:1024 sc1 lds\n" \
"v_mfma_f32_16x16x32_bf16 v[128:131], a[176:179], v[112:115], v[128:131]\n" \
"v_exp_f32_e32 v75, v75\n" \
"v_mfma_f32_16x16x32_bf16 v[132:135], a[180:183], v[112:115], v[132:135]\n" \
"v_exp_f32_e32 v76, v76\n" \
"v_mfma_f32_16x16x32_bf16 v[132:135], a[164:167], v[96:99], v[132:135]\n" \
"v_exp_f32_e32 v77, v77\n" \
"v_mfma_f32_16x16x32_bf16 v[136:139], a[168:171], v[96:99], v[136:139]\n" \
"v_exp_f32_e32 v78, v78\n" \
"v_mfma_f32_16x16x32_bf16 v[136:139], a[184:187], v[112:115], v[136:139]\n" \
"v_exp_f32_e32 v79, v79\n" \
"v_mfma_f32_16x16x32_bf16 v[140:143], a[188:191], v[112:115], v[140:143]\n" \
"buffer_load_dwordx4 v51, s[8:11], s64 offen offset:2048 sc1 lds\n" \
"v_mfma_f32_16x16x32_bf16 v[140:143], a[172:175], v[96:99], v[140:143]\n" \
"v_cvt_pk_bf16_f32 v117, v74, v75\n" \
"v_cvt_pk_bf16_f32 v118, v76, v77\n" \
"v_mfma_f32_16x16x32_bf16 a[192:195], a[212:215], v[96:99], a[192:195]\n" \
"v_cvt_pk_bf16_f32 v119, v78, v79\n" \
"v_readlane_b32 s92, v11, 22\n" \
"v_mfma_f32_16x16x32_bf16 a[192:195], a[212:215], v[112:115], a[192:195]\n" \
"buffer_load_dwordx4 v51, s[8:11], s64 offen offset:3072 sc1 lds\n" \
"v_mfma_f32_16x16x32_bf16 v[160:163], a[160:163], v[100:103], v[160:163]\n" \
"s_add_u32 m0, s93, 0x0\n" \
"ds_read_b128 a[128:131], v48 offset:0\n" \
"v_mfma_f32_16x16x32_bf16 v[160:163], a[176:179], v[116:119], v[160:163]\n" \
"v_exp_f32_e32 v80, v80\n" \
"v_mfma_f32_16x16x32_bf16 v[164:167], a[180:183], v[116:119], v[164:167]\n" \
"buffer_load_dwordx4 v0, s[80:83], s92 offen offset:0 lds\n" \
"v_mfma_f32_16x16x32_bf16 v[164:167], a[164:167], v[100:103], v[164:167]\n" \
"v_exp_f32_e32 v81, v81\n" \
"v_mfma_f32_16x16x32_bf16 v[168:171], a[168:171], v[100:103], v[168:171]\n" \
"v_exp_f32_e32 v82, v82\n" \
"v_mfma_f32_16x16x32_bf16 v[168:171], a[184:187], v[116:119], v[168:171]\n" \
"v_exp_f32_e32 v83, v83\n" \
"v_mfma_f32_16x16x32_bf16 v[172:175], a[188:191], v[116:119], v[172:175]\n" \
"v_exp_f32_e32 v84, v84\n" \
"v_mfma_f32_16x16x32_bf16 v[172:175], a[172:175], v[100:103], v[172:175]\n" \
"v_exp_f32_e32 v85, v85\n" \
"v_mfma_f32_16x16x32_bf16 a[196:199], a[212:215], v[100:103], a[196:199]\n" \
"v_exp_f32_e32 v86, v86\n" \
"v_mfma_f32_16x16x32_bf16 a[196:199], a[212:215], v[116:119], a[196:199]\n" \
"v_exp_f32_e32 v87, v87\n" \
"v_mfma_f32_16x16x32_bf16 v[192:195], a[160:163], v[104:107], v[192:195]\n" \
"v_cvt_pk_bf16_f32 v120, v80, v81\n" \
"v_cvt_pk_bf16_f32 v121, v82, v83\n" \
"v_cvt_pk_bf16_f32 v122, v84, v85\n" \
"v_cvt_pk_bf16_f32 v123, v86, v87\n" \
"ds_read_b128 a[132:135], v48 offset:1024\n" \
"ds_read_b128 a[136:139], v48 offset:2048\n" \
"v_mfma_f32_16x16x32_bf16 v[192:195], a[176:179], v[120:123], v[192:195]\n" \
"ds_read_b128 a[140:143], v48 offset:3072\n" \
"ds_read_b128 a[144:147], v48 offset:4096\n" \
"v_mfma_f32_16x16x32_bf16 v[196:199], a[180:183], v[120:123], v[196:199]\n" \
"ds_read_b128 a[148:151], v48 offset:5120\n" \
"ds_read_b128 a[152:155], v48 offset:6144\n" \
"ds_read_b128 a[156:159], v48 offset:7168\n" \
"v_mfma_f32_16x16x32_bf16 v[196:199], a[164:167], v[104:107], v[196:199]\n" \
"buffer_load_dwordx4 v1, s[80:83], s92 offen offset:1024 lds\n" \
"v_mfma_f32_16x16x32_bf16 v[200:203], a[168:171], v[104:107], v[200:203]\n" \
"v_exp_f32_e32 v88, v88\n" \
"v_mfma_f32_16x16x32_bf16 v[200:203], a[184:187], v[120:123], v[200:203]\n" \
"v_exp_f32_e32 v89, v89\n" \
"v_mfma_f32_16x16x32_bf16 v[204:207], a[188:191], v[120:123], v[204:207]\n" \
"v_exp_f32_e32 v90, v90\n" \
"v_mfma_f32_16x16x32_bf16 v[204:207], a[172:175], v[104:107], v[204:207]\n" \
"v_exp_f32_e32 v91, v91\n" \
"v_mfma_f32_16x16x32_bf16 v[224:227], a[160:163], v[108:111], v[224:227]\n" \
"v_exp_f32_e32 v92, v92\n" \
"v_exp_f32_e32 v93, v93\n" \
"v_exp_f32_e32 v94, v94\n" \
"v_exp_f32_e32 v95, v95\n" \
"buffer_load_dwordx4 v2, s[80:83], s92 offen offset:2048 lds\n" \
"v_cvt_pk_bf16_f32 v124, v88, v89\n" \
"v_cvt_pk_bf16_f32 v125, v90, v91\n" \
"v_cvt_pk_bf16_f32 v126, v92, v93\n" \
"v_cvt_pk_bf16_f32 v127, v94, v95\n" \
"buffer_load_dwordx4 v3, s[80:83], s92 offen offset:3072 lds\n" \
"v_add_u32_e32 v52, 0x14000, v63\n" \
"v_mfma_f32_16x16x32_bf16 v[224:227], a[176:179], v[124:127], v[224:227]\n" \
"v_add_u32_e32 v53, 0x20000, v63\n" \
"s_mov_b32 s66, s19\n" \
"v_mfma_f32_16x16x32_bf16 v[228:231], a[180:183], v[124:127], v[228:231]\n" \
"v_mfma_f32_16x16x32_bf16 v[228:231], a[164:167], v[108:111], v[228:231]\n" \
"v_mfma_f32_16x16x32_bf16 v[232:235], a[168:171], v[108:111], v[232:235]\n" \
"v_mfma_f32_16x16x32_bf16 v[232:235], a[184:187], v[124:127], v[232:235]\n" \
"v_mfma_f32_16x16x32_bf16 v[236:239], a[188:191], v[124:127], v[236:239]\n" \
"v_mfma_f32_16x16x32_bf16 v[236:239], a[172:175], v[108:111], v[236:239]\n" \
"s_waitcnt vmcnt(14) lgkmcnt(0)\n" \
"s_barrier\n" \
"v_mfma_f32_16x16x32_bf16 v[64:67], a[128:131], a[0:3], v[32:35]\n" \
"s_xor_b32 m0, s65, 0x18000\n" \
"ds_read_b128 a[160:163], v49 offset:8192\n" \
"v_mfma_f32_16x16x32_bf16 v[64:67], a[132:135], a[4:7], v[64:67]\n" \
"buffer_load_dwordx4 v52, s[8:11], s64 offen offset:0 sc1 lds\n" \
"v_mfma_f32_16x16x32_bf16 v[64:67], a[136:139], a[8:11], v[64:67]\n" \
"ds_read_b128 a[176:179], v49 offset:12288\n" \
"ds_read_b128 a[180:183], v49 offset:13312\n" \
"v_mfma_f32_16x16x32_bf16 v[64:67], a[140:143], a[12:15], v[64:67]\n" \
"ds_read_b128 a[164:167], v49 offset:9216\n" \
"ds_read_b128 a[168:171], v49 offset:10240\n" \
"v_mfma_f32_16x16x32_bf16 v[68:71], a[156:159], a[12:15], v[32:35]\n" \
"buffer_load_dwordx4 v52, s[8:11], s64 offen offset:1024 sc1 lds\n" \
"v_mfma_f32_16x16x32_bf16 v[68:71], a[152:155], a[8:11], v[68:71]\n" \
"ds_read_b128 a[184:187], v49 offset:14336\n" \
"ds_read_b128 a[188:191], v49 offset:15360\n" \
"v_mfma_f32_16x16x32_bf16 v[68:71], a[148:151], a[4:7], v[68:71]\n" \
"v_exp_f32_e32 v64, v64\n" \
"v_mfma_f32_16x16x32_bf16 v[68:71], a[144:147], a[0:3], v[68:71]\n" \
"v_exp_f32_e32 v65, v65\n" \
"v_mfma_f32_16x16x32_bf16 v[72:75], a[128:131], a[16:19], v[36:39]\n" \
"v_exp_f32_e32 v66, v66\n" \
"v_mfma_f32_16x16x32_bf16 v[72:75], a[132:135], a[20:23], v[72:75]\n" \
"buffer_load_dwordx4 v52, s[8:11], s64 offen offset:2048 sc1 lds\n" \
"v_mfma_f32_16x16x32_bf16 v[72:75], a[136:139], a[24:27], v[72:75]\n" \
"v_exp_f32_e32 v67, v67\n" \
"v_mfma_f32_16x16x32_bf16 v[72:75], a[140:143], a[28:31], v[72:75]\n" \
"v_exp_f32_e32 v68, v68\n" \
"v_mfma_f32_16x16x32_bf16 v[76:79], a[156:159], a[28:31], v[36:39]\n" \
"v_exp_f32_e32 v69, v69\n" \
"v_mfma_f32_16x16x32_bf16 v[76:79], a[152:155], a[24:27], v[76:79]\n" \
"v_exp_f32_e32 v70, v70\n" \
"v_mfma_f32_16x16x32_bf16 v[76:79], a[148:151], a[20:23], v[76:79]\n" \
"buffer_load_dwordx4 v52, s[8:11], s64 offen offset:3072 sc1 lds\n" \
"v_mfma_f32_16x16x32_bf16 v[76:79], a[144:147], a[16:19], v[76:79]\n" \
"ds_read_b128 a[172:175], v49 offset:11264\n" \
"s_mov_b32 m0, s65\n" \
"v_mfma_f32_16x16x32_bf16 v[80:83], a[128:131], a[32:35], v[40:43]\n" \
"v_exp_f32_e32 v71, v71\n" \
"v_mfma_f32_16x16x32_bf16 v[80:83], a[132:135], a[36:39], v[80:83]\n" \
"v_exp_f32_e32 v72, v72\n" \
"v_mfma_f32_16x16x32_bf16 v[80:83], a[136:139], a[40:43], v[80:83]\n" \
"v_exp_f32_e32 v73, v73\n" \
"v_mfma_f32_16x16x32_bf16 v[80:83], a[140:143], a[44:47], v[80:83]\n" \
"buffer_load_dwordx4 v53, s[8:11], s64 offen offset:0 sc1 lds\n" \
"v_mfma_f32_16x16x32_bf16 v[84:87], a[156:159], a[44:47], v[40:43]\n" \
"v_exp_f32_e32 v74, v74\n" \
"v_mfma_f32_16x16x32_bf16 v[84:87], a[152:155], a[40:43], v[84:87]\n" \
"v_exp_f32_e32 v75, v75\n" \
"v_mfma_f32_16x16x32_bf16 v[84:87], a[148:151], a[36:39], v[84:87]\n" \
"L_pf12:\n" \
"s_cbranch_execz L_pf16\n" \
"s_nop 0\n" \
"v_exp_f32_e32 v76, v76\n" \
"v_mfma_f32_16x16x32_bf16 v[84:87], a[144:147], a[32:35], v[84:87]\n" \
"buffer_load_dwordx4 v53, s[8:11], s64 offen offset:1024 sc1 lds\n" \
"v_mfma_f32_16x16x32_bf16 v[88:91], a[128:131], a[48:51], v[44:47]\n" \
"ds_read_b128 a[128:131], v48 offset:8192\n" \
"v_add_u32_e32 v49, 0x10000, v48\n" \
"v_mfma_f32_16x16x32_bf16 v[88:91], a[132:135], a[52:55], v[88:91]\n" \
"ds_read_b128 a[132:135], v48 offset:9216\n" \
"s_xor_b32 s65, s65, 0x8000\n" \
"v_mfma_f32_16x16x32_bf16 v[88:91], a[136:139], a[56:59], v[88:91]\n" \
"v_exp_f32_e32 v77, v77\n" \
"v_mfma_f32_16x16x32_bf16 v[88:91], a[140:143], a[60:63], v[88:91]\n" \
"ds_read_b128 a[136:139], v48 offset:10240\n" \
"ds_read_b128 a[140:143], v48 offset:11264\n" \
"v_mfma_f32_16x16x32_bf16 v[92:95], a[156:159], a[60:63], v[44:47]\n" \
"buffer_load_dwordx4 v53, s[8:11], s64 offen offset:2048 sc1 lds\n" \
"v_mfma_f32_16x16x32_bf16 v[92:95], a[152:155], a[56:59], v[92:95]\n" \
"ds_read_b128 a[152:155], v48 offset:14336\n" \
"ds_read_b128 a[156:159], v48 offset:15360\n" \
"v_mfma_f32_16x16x32_bf16 v[92:95], a[148:151], a[52:55], v[92:95]\n" \
"v_exp_f32_e32 v78, v78\n" \
"v_mfma_f32_16x16x32_bf16 v[92:95], a[144:147], a[48:51], v[92:95]\n" \
"ds_read_b128 a[144:147], v48 offset:12288\n" \
"s_waitcnt lgkmcnt(7)\n" \
"v_mfma_f32_16x16x32_bf16 v[144:147], a[160:163], v[96:99], v[144:147]\n" \
"v_exp_f32_e32 v79, v79\n" \
"v_mfma_f32_16x16x32_bf16 v[144:147], a[176:179], v[112:115], v[144:147]\n" \
"buffer_load_dwordx4 v53, s[8:11], s64 offen offset:3072 sc1 lds\n" \
"v_mfma_f32_16x16x32_bf16 v[148:151], a[180:183], v[112:115], v[148:151]\n" \
"s_add_u32 m0, s93, 0x1000\n" \
"ds_read_b128 a[148:151], v48 offset:13312\n" \
"v_mfma_f32_16x16x32_bf16 v[148:151], a[164:167], v[96:99], v[148:151]\n" \
"v_exp_f32_e32 v80, v80\n" \
"v_mfma_f32_16x16x32_bf16 v[152:155], a[168:171], v[96:99], v[152:155]\n" \
"v_exp_f32_e32 v81, v81\n" \
"v_mfma_f32_16x16x32_bf16 v[152:155], a[184:187], v[112:115], v[152:155]\n" \
"v_exp_f32_e32 v82, v82\n" \
"v_mfma_f32_16x16x32_bf16 v[156:159], a[188:191], v[112:115], v[156:159]\n" \
"buffer_load_dwordx4 v4, s[80:83], s92 offen offset:0 lds\n" \
"v_mfma_f32_16x16x32_bf16 v[156:159], a[172:175], v[96:99], v[156:159]\n" \
"v_exp_f32_e32 v83, v83\n" \
"v_mfma_f32_16x16x32_bf16 v[176:179], a[160:163], v[100:103], v[176:179]\n" \
"v_exp_f32_e32 v84, v84\n" \
"v_mfma_f32_16x16x32_bf16 v[176:179], a[176:179], v[116:119], v[176:179]\n" \
"v_exp_f32_e32 v85, v85\n" \
"v_mfma_f32_16x16x32_bf16 v[180:183], a[180:183], v[116:119], v[180:183]\n" \
"buffer_load_dwordx4 v5, s[80:83], s92 offen offset:1024 lds\n" \
"v_mfma_f32_16x16x32_bf16 v[180:183], a[164:167], v[100:103], v[180:183]\n" \
"v_exp_f32_e32 v86, v86\n" \
"v_mfma_f32_16x16x32_bf16 v[184:187], a[168:171], v[100:103], v[184:187]\n" \
"v_exp_f32_e32 v87, v87\n" \
"v_mfma_f32_16x16x32_bf16 v[184:187], a[184:187], v[116:119], v[184:187]\n" \
"v_exp_f32_e32 v88, v88\n" \
"v_mfma_f32_16x16x32_bf16 v[188:191], a[188:191], v[116:119], v[188:191]\n" \
"v_exp_f32_e32 v89, v89\n" \
"v_mfma_f32_16x16x32_bf16 v[188:191], a[172:175], v[100:103], v[188:191]\n" \
"buffer_load_dwordx4 v6, s[80:83], s92 offen offset:2048 lds\n" \
"v_mfma_f32_16x16x32_bf16 v[208:211], a[160:163], v[104:107], v[208:211]\n" \
"v_exp_f32_e32 v90, v90\n" \
"v_mfma_f32_16x16x32_bf16 v[208:211], a[176:179], v[120:123], v[208:211]\n" \
"v_exp_f32_e32 v91, v91\n" \
"v_mfma_f32_16x16x32_bf16 v[212:215], a[180:183], v[120:123], v[212:215]\n" \
"v_exp_f32_e32 v92, v92\n" \
"v_mfma_f32_16x16x32_bf16 v[212:215], a[164:167], v[104:107], v[212:215]\n" \
"v_exp_f32_e32 v93, v93\n" \
"v_mfma_f32_16x16x32_bf16 v[216:219], a[168:171], v[104:107], v[216:219]\n" \
"buffer_load_dwordx4 v7, s[80:83], s92 offen offset:3072 lds\n" \
"v_mfma_f32_16x16x32_bf16 v[216:219], a[184:187], v[120:123], v[216:219]\n" \
"s_add_u32 m0, s93, 0x2000\n" \
"v_cvt_pk_bf16_f32 v96, v64, v65\n" \
"v_mfma_f32_16x16x32_bf16 v[220:223], a[188:191], v[120:123], v[220:223]\n" \
"v_exp_f32_e32 v94, v94\n" \
"v_mfma_f32_16x16x32_bf16 v[220:223], a[172:175], v[104:107], v[220:223]\n" \
"v_exp_f32_e32 v95, v95\n" \
"v_mfma_f32_16x16x32_bf16 a[200:203], a[212:215], v[104:107], a[200:203]\n" \
"v_cvt_pk_bf16_f32 v97, v66, v67\n" \
"v_cvt_pk_bf16_f32 v98, v68, v69\n" \
"v_mfma_f32_16x16x32_bf16 a[200:203], a[212:215], v[120:123], a[200:203]\n" \
"buffer_load_dwordx4 v8, s[80:83], s92 offen offset:0 lds\n" \
"v_mfma_f32_16x16x32_bf16 v[240:243], a[160:163], v[108:111], v[240:243]\n" \
"v_cvt_pk_bf16_f32 v99, v70, v71\n" \
"v_cvt_pk_bf16_f32 v100, v72, v73\n" \
"v_mfma_f32_16x16x32_bf16 v[240:243], a[176:179], v[124:127], v[240:243]\n" \
"v_cvt_pk_bf16_f32 v101, v74, v75\n" \
"v_cvt_pk_bf16_f32 v102, v76, v77\n" \
"v_mfma_f32_16x16x32_bf16 v[244:247], a[180:183], v[124:127], v[244:247]\n" \
"v_cvt_pk_bf16_f32 v103, v78, v79\n" \
"v_cvt_pk_bf16_f32 v104, v80, v81\n" \
"v_mfma_f32_16x16x32_bf16 v[244:247], a[164:167], v[108:111], v[244:247]\n" \
"v_cvt_pk_bf16_f32 v105, v82, v83\n" \
"v_cvt_pk_bf16_f32 v106, v84, v85\n" \
"v_mfma_f32_16x16x32_bf16 v[248:251], a[168:171], v[108:111], v[248:251]\n" \
"v_cvt_pk_bf16_f32 v107, v86, v87\n" \
"v_add_u32_e32 v48, s16, v20\n" \
"v_mfma_f32_16x16x32_bf16 v[248:251], a[184:187], v[124:127], v[248:251]\n" \
"s_add_u32 s64, s64, 0x10000\n" \
"v_mfma_f32_16x16x32_bf16 v[252:255], a[188:191], v[124:127], v[252:255]\n" \
"v_mfma_f32_16x16x32_bf16 v[252:255], a[172:175], v[108:111], v[252:255]\n" \
"v_mfma_f32_16x16x32_bf16 a[204:207], a[212:215], v[108:111], a[204:207]\n" \
"v_mfma_f32_16x16x32_bf16 a[204:207], a[212:215], v[124:127], a[204:207]\n" \
"s_waitcnt vmcnt(13) lgkmcnt(0)\n" \
"s_mov_b64 exec, 0\n" \
"s_cmp_lg_u32 s15, 0\n" \
"s_cbranch_scc1 L_pf21\n" \
"s_branch L_pf20\n" \
"L_retB:\n" \
"s_mov_b64 exec, -1\n" \
"s_barrier\n" \
"L_2_%=:\n" \
"v_mfma_f32_16x16x32_bf16 v[64:67], a[128:131], a[64:67], v[32:35]\n" \
"s_add_u32 s63, s63, s63\n" \
"s_cbranch_scc1 L_h2_%=\n" \
"L_hr2_%=:\n" \
"v_mfma_f32_16x16x32_bf16 v[64:67], a[132:135], a[68:71], v[64:67]\n" \
"s_xor_b32 m0, s65, 0x12000\n" \
"ds_read_b128 a[160:163], v49 offset:0\n" \
"v_mfma_f32_16x16x32_bf16 v[64:67], a[136:139], a[72:75], v[64:67]\n" \
"buffer_load_dwordx4 v50, s[8:11], s64 offen offset:0 sc1 lds\n" \
"v_mfma_f32_16x16x32_bf16 v[64:67], a[140:143], a[76:79], v[64:67]\n" \
"ds_read_b128 a[176:179], v49 offset:4096\n" \
"ds_read_b128 a[180:183], v49 offset:5120\n" \
"v_mfma_f32_16x16x32_bf16 v[68:71], a[156:159], a[76:79], v[32:35]\n" \
"v_cvt_pk_bf16_f32 v108, v88, v89\n" \
"ds_read_b128 a[164:167], v49 offset:1024\n" \
"v_mfma_f32_16x16x32_bf16 v[68:71], a[152:155], a[72:75], v[68:71]\n" \
"v_cvt_pk_bf16_f32 v109, v90, v91\n" \
"ds_read_b128 a[168:171], v49 offset:2048\n" \
"v_mfma_f32_16x16x32_bf16 v[68:71], a[148:151], a[68:71], v[68:71]\n" \
"v_cvt_pk_bf16_f32 v110, v92, v93\n" \
"ds_read_b128 a[184:187], v49 offset:6144\n" \
"v_mfma_f32_16x16x32_bf16 v[68:71], a[144:147], a[64:67], v[68:71]\n" \
"buffer_load_dwordx4 v50, s[8:11], s64 offen offset:1024 sc1 lds\n" \
"v_mfma_f32_16x16x32_bf16 v[72:75], a[128:131], a[80:83], v[36:39]\n" \
"ds_read_b128 a[188:191], v49 offset:7168\n" \
"ds_read_b128 a[172:175], v49 offset:3072\n" \
"v_mfma_f32_16x16x32_bf16 v[72:75], a[132:135], a[84:87], v[72:75]\n" \
"v_exp_f32_e32 v64, v64\n" \
"v_mfma_f32_16x16x32_bf16 v[72:75], a[136:139], a[88:91], v[72:75]\n" \
"v_exp_f32_e32 v65, v65\n" \
"v_mfma_f32_16x16x32_bf16 v[72:75], a[140:143], a[92:95], v[72:75]\n" \
"v_cvt_pk_bf16_f32 v111, v94, v95\n" \
"v_cvt_pk_bf16_f32 v112, v64, v65\n" \
"v_mfma_f32_16x16x32_bf16 v[76:79], a[156:159], a[92:95], v[36:39]\n" \
"v_exp_f32_e32 v66, v66\n" \
"v_mfma_f32_16x16x32_bf16 v[76:79], a[152:155], a[88:91], v[76:79]\n" \
"v_exp_f32_e32 v67, v67\n" \
"v_mfma_f32_16x16x32_bf16 v[76:79], a[148:151], a[84:87], v[76:79]\n" \
"v_exp_f32_e32 v68, v68\n" \
"v_mfma_f32_16x16x32_bf16 v[76:79], a[144:147], a[80:83], v[76:79]\n" \
"buffer_load_dwordx4 v50, s[8:11], s64 offen offset:2048 sc1 lds\n" \
"v_mfma_f32_16x16x32_bf16 v[80:83], a[128:131], a[96:99], v[40:43]\n" \
"v_exp_f32_e32 v69, v69\n" \
"v_mfma_f32_16x16x32_bf16 v[80:83], a[132:135], a[100:103], v[80:83]\n" \
"v_exp_f32_e32 v70, v70\n" \
"v_mfma_f32_16x16x32_bf16 v[80:83], a[136:139], a[104:107], v[80:83]\n" \
"v_exp_f32_e32 v71, v71\n" \
"v_mfma_f32_16x16x32_bf16 v[80:83], a[140:143], a[108:111], v[80:83]\n" \
"v_cvt_pk_bf16_f32 v113, v66, v67\n" \
"v_cvt_pk_bf16_f32 v114, v68, v69\n" \
"v_mfma_f32_16x16x32_bf16 v[84:87], a[156:159], a[108:111], v[40:43]\n" \
"v_exp_f32_e32 v72, v72\n" \
"v_mfma_f32_16x16x32_bf16 v[84:87], a[152:155], a[104:107], v[84:87]\n" \
"v_exp_f32_e32 v73, v73\n" \
"v_mfma_f32_16x16x32_bf16 v[84:87], a[148:151], a[100:103], v[84:87]\n" \
"v_cvt_pk_bf16_f32 v115, v70, v71\n" \
"v_cvt_pk_bf16_f32 v116, v72, v73\n" \
"v_mfma_f32_16x16x32_bf16 v[84:87], a[144:147], a[96:99], v[84:87]\n" \
"buffer_load_dwordx4 v50, s[8:11], s64 offen offset:3072 sc1 lds\n" \
"v_mfma_f32_16x16x32_bf16 v[88:91], a[128:131], a[112:115], v[44:47]\n" \
"s_xor_b32 m0, s65, 0xa000\n" \
"ds_read_b128 a[128:131], v48 offset:0\n" \
"v_mfma_f32_16x16x32_bf16 v[88:91], a[132:135], a[116:119], v[88:91]\n" \
"v_exp_f32_e32 v74, v74\n" \
"v_mfma_f32_16x16x32_bf16 v[88:91], a[136:139], a[120:123], v[88:91]\n" \
"v_exp_f32_e32 v75, v75\n" \
"v_mfma_f32_16x16x32_bf16 v[88:91], a[140:143], a[124:127], v[88:91]\n" \
"v_exp_f32_e32 v76, v76\n" \
"v_mfma_f32_16x16x32_bf16 v[92:95], a[156:159], a[124:127], v[44:47]\n" \
"v_exp_f32_e32 v77, v77\n" \
"v_mfma_f32_16x16x32_bf16 v[92:95], a[152:155], a[120:123], v[92:95]\n" \
"v_exp_f32_e32 v78, v78\n" \
"v_mfma_f32_16x16x32_bf16 v[92:95], a[148:151], a[116:119], v[92:95]\n" \
"v_exp_f32_e32 v79, v79\n" \
"v_mfma_f32_16x16x32_bf16 v[92:95], a[144:147], a[112:115], v[92:95]\n" \
"v_cvt_pk_bf16_f32 v117, v74, v75\n" \
"s_waitcnt lgkmcnt(1)\n" \
"v_mfma_f32_16x16x32_bf16 v[128:131], a[160:163], v[96:99], v[128:131]\n" \
"buffer_load_dwordx4 v51, s[8:11], s64 offen offset:0 sc1 lds\n" \
"v_mfma_f32_16x16x32_bf16 v[128:131], a[176:179], v[112:115], v[128:131]\n" \
"v_cvt_pk_bf16_f32 v118, v76, v77\n" \
"v_cvt_pk_bf16_f32 v119, v78, v79\n" \
"v_mfma_f32_16x16x32_bf16 v[132:135], a[180:183], v[112:115], v[132:135]\n" \
"v_exp_f32_e32 v80, v80\n" \
"v_mfma_f32_16x16x32_bf16 v[132:135], a[164:167], v[96:99], v[132:135]\n" \
"v_exp_f32_e32 v81, v81\n" \
"v_mfma_f32_16x16x32_bf16 v[136:139], a[168:171], v[96:99], v[136:139]\n" \
"v_exp_f32_e32 v82, v82\n" \
"v_mfma_f32_16x16x32_bf16 v[136:139], a[184:187], v[112:115], v[136:139]\n" \
"v_exp_f32_e32 v83, v83\n" \
"v_mfma_f32_16x16x32_bf16 v[140:143], a[188:191], v[112:115], v[140:143]\n" \
"buffer_load_dwordx4 v51, s[8:11], s64 offen offset:1024 sc1 lds\n" \
"v_mfma_f32_16x16x32_bf16 v[140:143], a[172:175], v[96:99], v[140:143]\n" \
"v_exp_f32_e32 v84, v84\n" \
"v_mfma_f32_16x16x32_bf16 a[192:195], a[212:215], v[96:99], a[192:195]\n" \
"v_exp_f32_e32 v85, v85\n" \
"v_mfma_f32_16x16x32_bf16 a[192:195], a[212:215], v[112:115], a[192:195]\n" \
"v_exp_f32_e32 v86, v86\n" \
"v_mfma_f32_16x16x32_bf16 v[160:163], a[160:163], v[100:103], v[160:163]\n" \
"v_exp_f32_e32 v87, v87\n" \
"v_mfma_f32_16x16x32_bf16 v[160:163], a[176:179], v[116:119], v[160:163]\n" \
"v_cvt_pk_bf16_f32 v120, v80, v81\n" \
"v_cvt_pk_bf16_f32 v121, v82, v83\n" \
"v_mfma_f32_16x16x32_bf16 v[164:167], a[180:183], v[116:119], v[164:167]\n" \
"v_cvt_pk_bf16_f32 v122, v84, v85\n" \
"v_cvt_pk_bf16_f32 v123, v86, v87\n" \
"v_mfma_f32_16x16x32_bf16 v[164:167], a[164:167], v[100:103], v[164:167]\n" \
"ds_read_b128 a[132:135], v48 offset:1024\n" \
"ds_read_b128 a[136:139], v48 offset:2048\n" \
"v_mfma_f32_16x16x32_bf16 v[168:171], a[168:171], v[100:103], v[168:171]\n" \
"buffer_load_dwordx4 v51, s[8:11], s64 offen offset:2048 sc1 lds\n" \
"v_mfma_f32_16x16x32_bf16 v[168:171], a[184:187], v[116:119], v[168:171]\n" \
"ds_read_b128 a[140:143], v48 offset:3072\n" \
"ds_read_b128 a[144:147], v48 offset:4096\n" \
"v_mfma_f32_16x16x32_bf16 v[172:175], a[188:191], v[116:119], v[172:175]\n" \
"v_exp_f32_e32 v88, v88\n" \
"v_mfma_f32_16x16x32_bf16 v[172:175], a[172:175], v[100:103], v[172:175]\n" \
"ds_read_b128 a[148:151], v48 offset:5120\n" \
"ds_read_b128 a[152:155], v48 offset:6144\n" \
"v_mfma_f32_16x16x32_bf16 a[196:199], a[212:215], v[100:103], a[196:199]\n" \
"v_exp_f32_e32 v89, v89\n" \
"v_mfma_f32_16x16x32_bf16 a[196:199], a[212:215], v[116:119], a[196:199]\n" \
"ds_read_b128 a[156:159], v48 offset:7168\n" \
"v_cvt_pk_bf16_f32 v124, v88, v89\n" \
"v_mfma_f32_16x16x32_bf16 v[192:195], a[160:163], v[104:107], v[192:195]\n" \
"v_exp_f32_e32 v90, v90\n" \
"v_mfma_f32_16x16x32_bf16 v[192:195], a[176:179], v[120:123], v[192:195]\n" \
"v_exp_f32_e32 v91, v91\n" \
"v_mfma_f32_16x16x32_bf16 v[196:199], a[180:183], v[120:123], v[196:199]\n" \
"v_exp_f32_e32 v92, v92\n" \
"v_mfma_f32_16x16x32_bf16 v[196:199], a[164:167], v[104:107], v[196:199]\n" \
"v_exp_f32_e32 v93, v93\n" \
"v_mfma_f32_16x16x32_bf16 v[200:203], a[168:171], v[104:107], v[200:203]\n" \
"v_exp_f32_e32 v94, v94\n" \
"v_mfma_f32_16x16x32_bf16 v[200:203], a[184:187], v[120:123], v[200:203]\n" \
"v_exp_f32_e32 v95, v95\n" \
"v_mfma_f32_16x16x32_bf16 v[204:207], a[188:191], v[120:123], v[204:207]\n" \
"v_cvt_pk_bf16_f32 v125, v90, v91\n" \
"v_cvt_pk_bf16_f32 v126, v92, v93\n" \
"v_mfma_f32_16x16x32_bf16 v[204:207], a[172:175], v[104:107], v[204:207]\n" \
"v_cvt_pk_bf16_f32 v127, v94, v95\n" \
"v_mfma_f32_16x16x32_bf16 v[224:227], a[160:163], v[108:111], v[224:227]\n" \
"buffer_load_dwordx4 v51, s[8:11], s64 offen offset:3072 sc1 lds\n" \
"v_mfma_f32_16x16x32_bf16 v[224:227], a[176:179], v[124:127], v[224:227]\n" \
"v_mfma_f32_16x16x32_bf16 v[228:231], a[180:183], v[124:127], v[228:231]\n" \
"v_mfma_f32_16x16x32_bf16 v[228:231], a[164:167], v[108:111], v[228:231]\n" \
"v_mfma_f32_16x16x32_bf16 v[232:235], a[168:171], v[108:111], v[232:235]\n" \
"v_mfma_f32_16x16x32_bf16 v[232:235], a[184:187], v[124:127], v[232:235]\n" \
"v_mfma_f32_16x16x32_bf16 v[236:239], a[188:191], v[124:127], v[236:239]\n" \
"v_mfma_f32_16x16x32_bf16 v[236:239], a[172:175], v[108:111], v[236:239]\n" \
"s_waitcnt vmcnt(8) lgkmcnt(0)\n" \
"s_barrier\n" \
"v_mfma_f32_16x16x32_bf16 v[64:67], a[128:131], a[64:67], v[32:35]\n" \
"s_xor_b32 m0, s65, 0x18000\n" \
"ds_read_b128 a[160:163], v49 offset:8192\n" \
"v_mfma_f32_16x16x32_bf16 v[64:67], a[132:135], a[68:71], v[64:67]\n" \
"buffer_load_dwordx4 v52, s[8:11], s64 offen offset:0 sc1 lds\n" \
"v_mfma_f32_16x16x32_bf16 v[64:67], a[136:139], a[72:75], v[64:67]\n" \
"ds_read_b128 a[176:179], v49 offset:12288\n" \
"ds_read_b128 a[180:183], v49 offset:13312\n" \
"v_mfma_f32_16x16x32_bf16 v[64:67], a[140:143], a[76:79], v[64:67]\n" \
"v_mfma_f32_16x16x32_bf16 v[68:71], a[156:159], a[76:79], v[32:35]\n" \
"ds_read_b128 a[164:167], v49 offset:9216\n" \
"ds_read_b128 a[168:171], v49 offset:10240\n" \
"v_mfma_f32_16x16x32_bf16 v[68:71], a[152:155], a[72:75], v[68:71]\n" \
"v_mfma_f32_16x16x32_bf16 v[68:71], a[148:151], a[68:71], v[68:71]\n" \
"ds_read_b128 a[184:187], v49 offset:14336\n" \
"ds_read_b128 a[188:191], v49 offset:15360\n" \
"v_mfma_f32_16x16x32_bf16 v[68:71], a[144:147], a[64:67], v[68:71]\n" \
"buffer_load_dwordx4 v52, s[8:11], s64 offen offset:1024 sc1 lds\n" \
"v_mfma_f32_16x16x32_bf16 v[72:75], a[128:131], a[80:83], v[36:39]\n" \
"v_exp_f32_e32 v64, v64\n" \
"v_mfma_f32_16x16x32_bf16 v[72:75], a[132:135], a[84:87], v[72:75]\n" \
"v_exp_f32_e32 v65, v65\n" \
"v_mfma_f32_16x16x32_bf16 v[72:75], a[136:139], a[88:91], v[72:75]\n" \
"v_exp_f32_e32 v66, v66\n" \
"v_mfma_f32_16x16x32_bf16 v[72:75], a[140:143], a[92:95], v[72:75]\n" \
"v_exp_f32_e32 v67, v67\n" \
"v_mfma_f32_16x16x32_bf16 v[76:79], a[156:159], a[92:95], v[36:39]\n" \
"v_exp_f32_e32 v68, v68\n" \
"v_mfma_f32_16x16x32_bf16 v[76:79], a[152:155], a[88:91], v[76:79]\n" \
"v_exp_f32_e32 v69, v69\n" \
"v_mfma_f32_16x16x32_bf16 v[76:79], a[148:151], a[84:87], v[76:79]\n" \
"v_exp_f32_e32 v70, v70\n" \
"v_mfma_f32_16x16x32_bf16 v[76:79], a[144:147], a[80:83], v[76:79]\n" \
"buffer_load_dwordx4 v52, s[8:11], s64 offen offset:2048 sc1 lds\n" \
"v_mfma_f32_16x16x32_bf16 v[80:83], a[128:131], a[96:99], v[40:43]\n" \
"v_exp_f32_e32 v71, v71\n" \
"v_mfma_f32_16x16x32_bf16 v[80:83], a[132:135], a[100:103], v[80:83]\n" \
"v_exp_f32_e32 v72, v72\n" \
"v_mfma_f32_16x16x32_bf16 v[80:83], a[136:139], a[104:107], v[80:83]\n" \
"v_exp_f32_e32 v73, v73\n" \
"v_mfma_f32_16x16x32_bf16 v[80:83], a[140:143], a[108:111], v[80:83]\n" \
"v_exp_f32_e32 v74, v74\n" \
"v_mfma_f32_16x16x32_bf16 v[84:87], a[156:159], a[108:111], v[40:43]\n" \
"v_exp_f32_e32 v75, v75\n" \
"v_mfma_f32_16x16x32_bf16 v[84:87], a[152:155], a[104:107], v[84:87]\n" \
"v_exp_f32_e32 v76, v76\n" \
"v_mfma_f32_16x16x32_bf16 v[84:87], a[148:151], a[100:103], v[84:87]\n" \
"ds_read_b128 a[172:175], v49 offset:11264\n" \
"v_add_u32_e32 v49, 0x10000, v48\n" \
"v_mfma_f32_16x16x32_bf16 v[84:87], a[144:147], a[96:99], v[84:87]\n" \
"buffer_load_dwordx4 v52, s[8:11], s64 offen offset:3072 sc1 lds\n" \
"v_mfma_f32_16x16x32_bf16 v[88:91], a[128:131], a[112:115], v[44:47]\n" \
"s_mov_b32 m0, s65\n" \
"ds_read_b128 a[128:131], v48 offset:8192\n" \
"v_mfma_f32_16x16x32_bf16 v[88:91], a[132:135], a[116:119], v[88:91]\n" \
"ds_read_b128 a[132:135], v48 offset:9216\n" \
"s_xor_b32 s65, s65, 0x8000\n" \
"v_mfma_f32_16x16x32_bf16 v[88:91], a[136:139], a[120:123], v[88:91]\n" \
"v_exp_f32_e32 v77, v77\n" \
"v_mfma_f32_16x16x32_bf16 v[88:91], a[140:143], a[124:127], v[88:91]\n" \
"ds_read_b128 a[136:139], v48 offset:10240\n" \
"ds_read_b128 a[140:143], v48 offset:11264\n" \
"v_mfma_f32_16x16x32_bf16 v[92:95], a[156:159], a[124:127], v[44:47]\n" \
"v_exp_f32_e32 v78, v78\n" \
"v_mfma_f32_16x16x32_bf16 v[92:95], a[152:155], a[120:123], v[92:95]\n" \
"ds_read_b128 a[152:155], v48 offset:14336\n" \
"ds_read_b128 a[156:159], v48 offset:15360\n" \
"v_mfma_f32_16x16x32_bf16 v[92:95], a[148:151], a[116:119], v[92:95]\n" \
"v_exp_f32_e32 v79, v79\n" \
"v_mfma_f32_16x16x32_bf16 v[92:95], a[144:147], a[112:115], v[92:95]\n" \
"ds_read_b128 a[144:147], v48 offset:12288\n" \
"s_waitcnt lgkmcnt(8)\n" \
"v_mfma_f32_16x16x32_bf16 v[144:147], a[160:163], v[96:99], v[144:147]\n" \
"buffer_load_dwordx4 v53, s[8:11], s64 offen offset:0 sc1 lds\n" \
"v_mfma_f32_16x16x32_bf16 v[144:147], a[176:179], v[112:115], v[144:147]\n" \
"v_exp_f32_e32 v80, v80\n" \
"v_mfma_f32_16x16x32_bf16 v[148:151], a[180:183], v[112:115], v[148:151]\n" \
"v_exp_f32_e32 v81, v81\n" \
"v_mfma_f32_16x16x32_bf16 v[148:151], a[164:167], v[96:99], v[148:151]\n" \
"v_exp_f32_e32 v82, v82\n" \
"v_mfma_f32_16x16x32_bf16 v[152:155], a[168:171], v[96:99], v[152:155]\n" \
"v_exp_f32_e32 v83, v83\n" \
"v_mfma_f32_16x16x32_bf16 v[152:155], a[184:187], v[112:115], v[152:155]\n" \
"v_exp_f32_e32 v84, v84\n" \
"v_mfma_f32_16x16x32_bf16 v[156:159], a[188:191], v[112:115], v[156:159]\n" \
"ds_read_b128 a[148:151], v48 offset:13312\n" \
"s_waitcnt lgkmcnt(7)\n" \
"v_mfma_f32_16x16x32_bf16 v[156:159], a[172:175], v[96:99], v[156:159]\n" \
"buffer_load_dwordx4 v53, s[8:11], s64 offen offset:1024 sc1 lds\n" \
"v_mfma_f32_16x16x32_bf16 v[176:179], a[160:163], v[100:103], v[176:179]\n" \
"v_exp_f32_e32 v85, v85\n" \
"v_mfma_f32_16x16x32_bf16 v[176:179], a[176:179], v[116:119], v[176:179]\n" \
"v_exp_f32_e32 v86, v86\n" \
"v_mfma_f32_16x16x32_bf16 v[180:183], a[180:183], v[116:119], v[180:183]\n" \
"v_exp_f32_e32 v87, v87\n" \
"v_mfma_f32_16x16x32_bf16 v[180:183], a[164:167], v[100:103], v[180:183]\n" \
"v_exp_f32_e32 v88, v88\n" \
"v_mfma_f32_16x16x32_bf16 v[184:187], a[168:171], v[100:103], v[184:187]\n" \
"v_exp_f32_e32 v89, v89\n" \
"v_mfma_f32_16x16x32_bf16 v[184:187], a[184:187], v[116:119], v[184:187]\n" \
"v_exp_f32_e32 v90, v90\n" \
"v_mfma_f32_16x16x32_bf16 v[188:191], a[188:191], v[116:119], v[188:191]\n" \
"buffer_load_dwordx4 v53, s[8:11], s64 offen offset:2048 sc1 lds\n" \
"v_mfma_f32_16x16x32_bf16 v[188:191], a[172:175], v[100:103], v[188:191]\n" \
"v_exp_f32_e32 v91, v91\n" \
"v_mfma_f32_16x16x32_bf16 v[208:211], a[160:163], v[104:107], v[208:211]\n" \
"v_exp_f32_e32 v92, v92\n" \
"v_mfma_f32_16x16x32_bf16 v[208:211], a[176:179], v[120:123], v[208:211]\n" \
"v_exp_f32_e32 v93, v93\n" \
"v_mfma_f32_16x16x32_bf16 v[212:215], a[180:183], v[120:123], v[212:215]\n" \
"v_exp_f32_e32 v94, v94\n" \
"v_mfma_f32_16x16x32_bf16 v[212:215], a[164:167], v[104:107], v[212:215]\n" \
"v_exp_f32_e32 v95, v95\n" \
"v_mfma_f32_16x16x32_bf16 v[216:219], a[168:171], v[104:107], v[216:219]\n" \
"v_cvt_pk_bf16_f32 v96, v64, v65\n" \
"v_cvt_pk_bf16_f32 v97, v66, v67\n" \
"v_mfma_f32_16x16x32_bf16 v[216:219], a[184:187], v[120:123], v[216:219]\n" \
"v_cvt_pk_bf16_f32 v98, v68, v69\n" \
"v_cvt_pk_bf16_f32 v99, v70, v71\n" \
"v_mfma_f32_16x16x32_bf16 v[220:223], a[188:191], v[120:123], v[220:223]\n" \
"buffer_load_dwordx4 v53, s[8:11], s64 offen offset:3072 sc1 lds\n" \
"v_mfma_f32_16x16x32_bf16 v[220:223], a[172:175], v[104:107], v[220:223]\n" \
"s_add_u32 s64, s64, 0x10000\n" \
"v_cvt_pk_bf16_f32 v100, v72, v73\n" \
"v_mfma_f32_16x16x32_bf16 a[200:203], a[212:215], v[104:107], a[200:203]\n" \
"v_cvt_pk_bf16_f32 v101, v74, v75\n" \
"v_cvt_pk_bf16_f32 v102, v76, v77\n" \
"v_mfma_f32_16x16x32_bf16 a[200:203], a[212:215], v[120:123], a[200:203]\n" \
"v_cvt_pk_bf16_f32 v103, v78, v79\n" \
"v_cvt_pk_bf16_f32 v104, v80, v81\n" \
"v_mfma_f32_16x16x32_bf16 v[240:243], a[160:163], v[108:111], v[240:243]\n" \
"v_cvt_pk_bf16_f32 v105, v82, v83\n" \
"v_cvt_pk_bf16_f32 v106, v84, v85\n" \
"v_mfma_f32_16x16x32_bf16 v[240:243], a[176:179], v[124:127], v[240:243]\n" \
"v_cvt_pk_bf16_f32 v107, v86, v87\n" \
"v_xor_b32_e32 v48, 0x8000, v48\n" \
"v_mfma_f32_16x16x32_bf16 v[244:247], a[180:183], v[124:127], v[244:247]\n" \
"s_sub_u32 s66, s66, 1\n" \
"v_mfma_f32_16x16x32_bf16 v[244:247], a[164:167], v[108:111], v[244:247]\n" \
"v_mfma_f32_16x16x32_bf16 v[248:251], a[168:171], v[108:111], v[248:251]\n" \
"v_mfma_f32_16x16x32_bf16 v[248:251], a[184:187], v[124:127], v[248:251]\n" \
"v_mfma_f32_16x16x32_bf16 v[252:255], a[188:191], v[124:127], v[252:255]\n" \
"v_mfma_f32_16x16x32_bf16 v[252:255], a[172:175], v[108:111], v[252:255]\n" \
"v_mfma_f32_16x16x32_bf16 a[204:207], a[212:215], v[108:111], a[204:207]\n" \
"v_mfma_f32_16x16x32_bf16 a[204:207], a[212:215], v[124:127], a[204:207]\n" \
"s_waitcnt vmcnt(8) lgkmcnt(0)\n" \
"s_barrier\n" \
"s_cbranch_scc0 L_2_%=\n" \
"s_cmp_lg_u32 s16, 0\n" \
"s_cbranch_scc1 L_f1_%=\n" \
"ds_read_b128 a[160:163], v49 offset:0\n" \
"ds_read_b128 a[164:167], v49 offset:1024\n" \
"ds_read_b128 a[168:171], v49 offset:2048\n" \
"ds_read_b128 a[172:175], v49 offset:3072\n" \
"ds_read_b128 a[176:179], v49 offset:8192\n" \
"ds_read_b128 a[180:183], v49 offset:9216\n" \
"v_cvt_pk_bf16_f32 v108, v88, v89\n" \
"v_cvt_pk_bf16_f32 v109, v90, v91\n" \
"v_mfma_f32_16x16x32_bf16 v[88:91], a[128:131], a[112:115], v[44:47]\n" \
"s_add_u32 s70, s61, 0x0\n" \
"s_add_u32 s71, s61, 0x49000\n" \
"ds_read_b128 a[184:187], v49 offset:10240\n" \
"v_mfma_f32_16x16x32_bf16 v[88:91], a[132:135], a[116:119], v[88:91]\n" \
"ds_read_b128 a[188:191], v49 offset:11264\n" \
"v_cvt_pk_bf16_f32 v110, v92, v93\n" \
"v_mfma_f32_16x16x32_bf16 v[88:91], a[136:139], a[120:123], v[88:91]\n" \
"v_cvt_pk_bf16_f32 v111, v94, v95\n" \
"v_add_u32_e32 v54, 0x49000, v20\n" \
"v_mfma_f32_16x16x32_bf16 v[88:91], a[140:143], a[124:127], v[88:91]\n" \
"buffer_load_dwordx4 a[220:223], v20, s[56:59], s60 offen offset:0 sc1\n" \
"v_mfma_f32_16x16x32_bf16 v[92:95], a[156:159], a[124:127], v[44:47]\n" \
"v_readlane_b32 s97, v11, 32\n" \
"v_readlane_b32 s98, v11, 33\n" \
"v_mfma_f32_16x16x32_bf16 v[92:95], a[152:155], a[120:123], v[92:95]\n" \
"v_and_b32_e32 v104, v12, v104\n" \
"v_and_b32_e32 v105, v13, v105\n" \
"v_mfma_f32_16x16x32_bf16 v[92:95], a[148:151], a[116:119], v[92:95]\n" \
"buffer_load_dwordx4 a[236:239], v54, s[56:59], s60 offen offset:0 sc1\n" \
"v_mfma_f32_16x16x32_bf16 v[92:95], a[144:147], a[112:115], v[92:95]\n" \
"v_and_b32_e32 v106, v14, v106\n" \
"v_and_b32_e32 v107, v15, v107\n" \
"s_waitcnt lgkmcnt(7)\n" \
"v_mfma_f32_16x16x32_bf16 v[128:131], a[160:163], v[96:99], v[128:131]\n" \
"ds_read_b32 v60, v18 offset:0\n" \
"s_waitcnt lgkmcnt(7)\n" \
"v_mfma_f32_16x16x32_bf16 v[132:135], a[164:167], v[96:99], v[132:135]\n" \
"ds_read_b32 v61, v18 offset:128\n" \
"s_waitcnt lgkmcnt(7)\n" \
"v_mfma_f32_16x16x32_bf16 v[136:139], a[168:171], v[96:99], v[136:139]\n" \
"ds_read_b128 a[0:3], v49 offset:4096\n" \
"s_waitcnt lgkmcnt(7)\n" \
"v_mfma_f32_16x16x32_bf16 v[140:143], a[172:175], v[96:99], v[140:143]\n" \
"ds_read_b128 a[4:7], v49 offset:5120\n" \
"s_waitcnt lgkmcnt(7)\n" \
"v_mfma_f32_16x16x32_bf16 v[144:147], a[176:179], v[96:99], v[144:147]\n" \
"ds_read_b128 a[8:11], v49 offset:6144\n" \
"buffer_load_dwordx4 a[224:227], v20, s[56:59], s60 offen offset:1024 sc1\n" \
"s_waitcnt lgkmcnt(7)\n" \
"v_mfma_f32_16x16x32_bf16 v[148:151], a[180:183], v[96:99], v[148:151]\n" \
"ds_read_b128 a[12:15], v49 offset:7168\n" \
"s_waitcnt lgkmcnt(7)\n" \
"v_mfma_f32_16x16x32_bf16 v[152:155], a[184:187], v[96:99], v[152:155]\n" \
"ds_read_b128 a[16:19], v49 offset:12288\n" \
"buffer_load_dwordx4 a[240:243], v54, s[56:59], s60 offen offset:1024 sc1\n" \
"s_waitcnt lgkmcnt(7)\n" \
"v_mfma_f32_16x16x32_bf16 v[156:159], a[188:191], v[96:99], v[156:159]\n" \
"ds_read_b128 a[20:23], v49 offset:13312\n" \
"ds_read_b128 a[24:27], v49 offset:14336\n" \
"v_mfma_f32_16x16x32_bf16 a[192:195], a[212:215], v[96:99], a[192:195]\n" \
"ds_read_b128 a[28:31], v49 offset:15360\n" \
"v_add_u32_e32 v50, s15, v48\n" \
"buffer_load_dwordx4 a[228:231], v20, s[56:59], s60 offen offset:2048 sc1\n" \
"v_mfma_f32_16x16x32_bf16 v[160:163], a[160:163], v[100:103], v[160:163]\n" \
"v_add_u32_e32 v51, 0x10000, v50\n" \
"v_xor_b32_e32 v52, 0x4000, v50\n" \
"v_mfma_f32_16x16x32_bf16 v[164:167], a[164:167], v[100:103], v[164:167]\n" \
"s_waitcnt vmcnt(5)\n" \
"ds_write_b128 v50, v[128:131] offset:0\n" \
"buffer_load_dwordx4 a[244:247], v54, s[56:59], s60 offen offset:2048 sc1\n" \
"v_mfma_f32_16x16x32_bf16 v[168:171], a[168:171], v[100:103], v[168:171]\n" \
"v_accvgpr_read_b32 v40, a192\n" \
"v_xor_b32_e32 v53, 0x4000, v51\n" \
"v_mfma_f32_16x16x32_bf16 v[172:175], a[172:175], v[100:103], v[172:175]\n" \
"ds_write_b128 v50, v[132:135] offset:1024\n" \
"buffer_load_dwordx4 a[232:235], v20, s[56:59], s60 offen offset:3072 sc1\n" \
"v_mfma_f32_16x16x32_bf16 v[176:179], a[176:179], v[100:103], v[176:179]\n" \
"ds_write_b128 v50, v[136:139] offset:2048\n" \
"v_mfma_f32_16x16x32_bf16 v[180:183], a[180:183], v[100:103], v[180:183]\n" \
"ds_write_b128 v50, v[140:143] offset:3072\n" \
"buffer_load_dwordx4 a[248:251], v54, s[56:59], s60 offen offset:3072 sc1\n" \
"v_mfma_f32_16x16x32_bf16 v[184:187], a[184:187], v[100:103], v[184:187]\n" \
"ds_write_b128 v50, v[144:147] offset:8192\n" \
"v_mfma_f32_16x16x32_bf16 v[188:191], a[188:191], v[100:103], v[188:191]\n" \
"ds_write_b128 v50, v[148:151] offset:9216\n" \
"buffer_load_dwordx4 a[32:35], v20, s[56:59], s97 offen offset:0 sc1\n" \
"v_mfma_f32_16x16x32_bf16 a[196:199], a[212:215], v[100:103], a[196:199]\n" \
"ds_write_b128 v50, v[152:155] offset:10240\n" \
"v_mfma_f32_16x16x32_bf16 v[192:195], a[160:163], v[104:107], v[192:195]\n" \
"ds_write_b128 v50, v[156:159] offset:11264\n" \
"buffer_load_dwordx4 a[40:43], v54, s[56:59], s97 offen offset:0 sc1\n" \
"v_mfma_f32_16x16x32_bf16 v[196:199], a[164:167], v[104:107], v[196:199]\n" \
"ds_write_b128 v51, v[160:163] offset:0\n" \
"v_mfma_f32_16x16x32_bf16 v[200:203], a[168:171], v[104:107], v[200:203]\n" \
"ds_write_b128 v51, v[164:167] offset:1024\n" \
"buffer_load_dwordx4 a[48:51], v20, s[56:59], s98 offen offset:0 sc1\n" \
"v_mfma_f32_16x16x32_bf16 v[204:207], a[172:175], v[104:107], v[204:207]\n" \
"v_accvgpr_read_b32 v41, a196\n" \
"ds_write_b64 v19, v[40:41]\n" \
"v_mfma_f32_16x16x32_bf16 v[208:211], a[176:179], v[104:107], v[208:211]\n" \
"ds_write_b128 v51, v[168:171] offset:2048\n" \
"buffer_load_dwordx4 a[56:59], v54, s[56:59], s98 offen offset:0 sc1\n" \
"v_mfma_f32_16x16x32_bf16 v[212:215], a[180:183], v[104:107], v[212:215]\n" \
"ds_write_b128 v51, v[172:175] offset:3072\n" \
"v_mfma_f32_16x16x32_bf16 v[216:219], a[184:187], v[104:107], v[216:219]\n" \
"ds_write_b128 v51, v[176:179] offset:8192\n" \
"buffer_load_dwordx4 a[36:39], v20, s[56:59], s97 offen offset:1024 sc1\n" \
"v_mfma_f32_16x16x32_bf16 v[220:223], a[188:191], v[104:107], v[220:223]\n" \
"ds_write_b128 v51, v[180:183] offset:9216\n" \
"v_mfma_f32_16x16x32_bf16 a[200:203], a[212:215], v[104:107], a[200:203]\n" \
"ds_write_b128 v51, v[184:187] offset:10240\n" \
"buffer_load_dwordx4 a[44:47], v54, s[56:59], s97 offen offset:1024 sc1\n" \
"ds_write_b128 v51, v[188:191] offset:11264\n" \
"v_xor_b32_e32 v63, 0x400, v19\n" \
"v_readlane_b32 s93, v11, 18\n" \
"v_readlane_b32 s84, v11, 13\n" \
"v_readlane_b32 s85, v11, 14\n" \
"v_readlane_b32 s86, v11, 15\n" \
"s_mov_b32 s87, 0x20000\n" \
"buffer_load_dwordx4 a[52:55], v20, s[56:59], s98 offen offset:1024 sc1\n" \
"v_readlane_b32 s92, v11, 16\n" \
"s_add_u32 m0, s93, 0x0\n" \
"v_exp_f32_e32 v88, v88\n" \
"v_exp_f32_e32 v89, v89\n" \
"v_exp_f32_e32 v90, v90\n" \
"buffer_load_dwordx4 a[60:63], v54, s[56:59], s98 offen offset:1024 sc1\n" \
"v_exp_f32_e32 v91, v91\n" \
"v_exp_f32_e32 v92, v92\n" \
"v_exp_f32_e32 v93, v93\n" \
"v_exp_f32_e32 v94, v94\n" \
"v_exp_f32_e32 v95, v95\n" \
"v_cvt_pk_bf16_f32 v124, v88, v89\n" \
"v_cvt_pk_bf16_f32 v125, v90, v91\n" \
"v_cvt_pk_bf16_f32 v126, v92, v93\n" \
"v_cvt_pk_bf16_f32 v127, v94, v95\n" \
"v_and_b32_e32 v124, v12, v124\n" \
"v_and_b32_e32 v125, v13, v125\n" \
"v_and_b32_e32 v126, v14, v126\n" \
"v_and_b32_e32 v127, v15, v127\n" \
"v_accvgpr_read_b32 v56, a200\n" \
"s_waitcnt lgkmcnt(0)\n" \
"s_barrier\n" \
"ds_read_b128 v[128:131], v52 offset:0\n" \
"ds_read_b128 v[132:135], v52 offset:1024\n" \
"ds_read_b128 v[136:139], v52 offset:2048\n" \
"ds_read_b128 v[140:143], v52 offset:3072\n" \
"ds_read_b128 v[144:147], v52 offset:8192\n" \
"ds_read_b128 v[148:151], v52 offset:9216\n" \
"ds_read_b128 v[152:155], v52 offset:10240\n" \
"ds_read_b128 v[156:159], v52 offset:11264\n" \
"ds_read_b128 v[160:163], v53 offset:0\n" \
"ds_read_b128 v[164:167], v53 offset:1024\n" \
"ds_read_b128 v[168:171], v53 offset:2048\n" \
"ds_read_b128 v[172:175], v53 offset:3072\n" \
"ds_read_b128 v[176:179], v53 offset:8192\n" \
"ds_read_b128 v[180:183], v53 offset:9216\n" \
"ds_read_b128 v[184:187], v53 offset:10240\n" \
"ds_read_b128 v[188:191], v53 offset:11264\n" \
"ds_read_b64 v[42:43], v63\n" \
"v_mfma_f32_16x16x32_bf16 v[224:227], a[160:163], v[108:111], v[224:227]\n" \
"buffer_load_dwordx4 v0, s[84:87], s92 offen offset:0 lds\n" \
"v_mfma_f32_16x16x32_bf16 v[228:231], a[164:167], v[108:111], v[228:231]\n" \
"v_mfma_f32_16x16x32_bf16 v[232:235], a[168:171], v[108:111], v[232:235]\n" \
"v_mfma_f32_16x16x32_bf16 v[236:239], a[172:175], v[108:111], v[236:239]\n" \
"buffer_load_dwordx4 v1, s[84:87], s92 offen offset:1024 lds\n" \
"v_mfma_f32_16x16x32_bf16 v[240:243], a[176:179], v[108:111], v[240:243]\n" \
"v_mfma_f32_16x16x32_bf16 v[244:247], a[180:183], v[108:111], v[244:247]\n" \
"v_mfma_f32_16x16x32_bf16 v[248:251], a[184:187], v[108:111], v[248:251]\n" \
"buffer_load_dwordx4 v2, s[84:87], s92 offen offset:2048 lds\n" \
"v_mfma_f32_16x16x32_bf16 v[252:255], a[188:191], v[108:111], v[252:255]\n" \
"s_waitcnt lgkmcnt(15)\n" \
"v_add_f32_e32 v192, v192, v128\n" \
"v_mfma_f32_16x16x32_bf16 a[204:207], a[212:215], v[108:111], a[204:207]\n" \
"v_add_f32_e32 v193, v193, v129\n" \
"L_pf13:\n" \
"s_cbranch_execz L_pf17\n" \
"s_nop 0\n" \
"v_add_f32_e32 v194, v194, v130\n" \
"buffer_load_dwordx4 v3, s[84:87], s92 offen offset:3072 lds\n" \
"s_add_u32 m0, s93, 0x1000\n" \
"s_waitcnt lgkmcnt(1)\n" \
"s_barrier\n" \
"v_mfma_f32_16x16x32_bf16 v[224:227], a[0:3], v[124:127], v[224:227]\n" \
"buffer_load_dwordx4 v4, s[84:87], s92 offen offset:0 lds\n" \
"v_mfma_f32_16x16x32_bf16 v[228:231], a[4:7], v[124:127], v[228:231]\n" \
"s_waitcnt lgkmcnt(0)\n" \
"v_add_f32_e32 v56, v56, v42\n" \
"v_mfma_f32_16x16x32_bf16 v[232:235], a[8:11], v[124:127], v[232:235]\n" \
"v_max_f32_e32 v62, v62, v56\n" \
"v_add_f32_e32 v195, v195, v131\n" \
"buffer_load_dwordx4 v5, s[84:87], s92 offen offset:1024 lds\n" \
"v_mfma_f32_16x16x32_bf16 v[236:239], a[12:15], v[124:127], v[236:239]\n" \
"v_rcp_f32_e32 v56, v56\n" \
"v_mfma_f32_16x16x32_bf16 v[240:243], a[16:19], v[124:127], v[240:243]\n" \
"v_add_f32_e32 v196, v196, v132\n" \
"v_add_f32_e32 v197, v197, v133\n" \
"buffer_load_dwordx4 v6, s[84:87], s92 offen offset:2048 lds\n" \
"v_add_f32_e32 v198, v198, v134\n" \
"v_add_f32_e32 v199, v199, v135\n" \
"v_mul_f32_e32 v56, v56, v60\n" \
"v_mul_f32_e32 v192, v56, v192\n" \
"v_mul_f32_e32 v193, v56, v193\n" \
"v_mul_f32_e32 v194, v56, v194\n" \
"v_mul_f32_e32 v195, v56, v195\n" \
"v_mul_f32_e32 v196, v56, v196\n" \
"buffer_load_dwordx4 v7, s[84:87], s92 offen offset:3072 lds\n" \
"s_add_u32 m0, s93, 0x2000\n" \
"v_mul_f32_e32 v197, v56, v197\n" \
"v_mul_f32_e32 v198, v56, v198\n" \
"v_mul_f32_e32 v199, v56, v199\n" \
"v_add_f32_e32 v200, v200, v136\n" \
"v_add_f32_e32 v201, v201, v137\n" \
"v_add_f32_e32 v202, v202, v138\n" \
"v_add_f32_e32 v203, v203, v139\n" \
"buffer_load_dwordx4 v8, s[84:87], s92 offen offset:0 lds\n" \
"v_add_f32_e32 v204, v204, v140\n" \
"v_add_f32_e32 v205, v205, v141\n" \
"v_add_f32_e32 v206, v206, v142\n" \
"v_add_f32_e32 v207, v207, v143\n" \
"v_cvt_pk_bf16_f32 v64, v192, v193\n" \
"v_cvt_pk_bf16_f32 v65, v194, v195\n" \
"v_cvt_pk_bf16_f32 v66, v196, v197\n" \
"v_cvt_pk_bf16_f32 v67, v198, v199\n" \
"buffer_store_dwordx4 v[64:67], v20, s[56:59], s70 offen offset:0 sc1\n" \
"v_mfma_f32_16x16x32_bf16 v[244:247], a[20:23], v[124:127], v[244:247]\n" \
"v_mfma_f32_16x16x32_bf16 a[208:211], a[216:219], v[64:67], a[208:211]\n" \
"v_mul_f32_e32 v200, v56, v200\n" \
"v_mul_f32_e32 v201, v56, v201\n" \
"v_mul_f32_e32 v202, v56, v202\n" \
"v_mul_f32_e32 v203, v56, v203\n" \
"v_mul_f32_e32 v204, v56, v204\n" \
"v_mul_f32_e32 v205, v56, v205\n" \
"v_mul_f32_e32 v206, v56, v206\n" \
"v_mul_f32_e32 v207, v56, v207\n" \
"v_add_f32_e32 v208, v208, v144\n" \
"v_add_f32_e32 v209, v209, v145\n" \
"v_add_f32_e32 v210, v210, v146\n" \
"v_add_f32_e32 v211, v211, v147\n" \
"v_add_f32_e32 v212, v212, v148\n" \
"v_add_f32_e32 v213, v213, v149\n" \
"v_add_f32_e32 v214, v214, v150\n" \
"v_add_f32_e32 v215, v215, v151\n" \
"v_cvt_pk_bf16_f32 v68, v200, v201\n" \
"v_cvt_pk_bf16_f32 v69, v202, v203\n" \
"v_cvt_pk_bf16_f32 v70, v204, v205\n" \
"v_cvt_pk_bf16_f32 v71, v206, v207\n" \
"buffer_store_dwordx4 v[68:71], v20, s[56:59], s70 offen offset:1024 sc1\n" \
"v_mul_f32_e32 v208, v56, v208\n" \
"v_mfma_f32_16x16x32_bf16 a[208:211], a[216:219], v[68:71], a[208:211]\n" \
"v_mul_f32_e32 v209, v56, v209\n" \
"v_mul_f32_e32 v210, v56, v210\n" \
"v_mul_f32_e32 v211, v56, v211\n" \
"v_mul_f32_e32 v212, v56, v212\n" \
"v_mul_f32_e32 v213, v56, v213\n" \
"v_mul_f32_e32 v214, v56, v214\n" \
"v_mul_f32_e32 v215, v56, v215\n" \
"v_add_f32_e32 v216, v216, v152\n" \
"v_add_f32_e32 v217, v217, v153\n" \
"v_add_f32_e32 v218, v218, v154\n" \
"v_add_f32_e32 v219, v219, v155\n" \
"v_add_f32_e32 v220, v220, v156\n" \
"v_add_f32_e32 v221, v221, v157\n" \
"v_add_f32_e32 v222, v222, v158\n" \
"v_add_f32_e32 v223, v223, v159\n" \
"v_cvt_pk_bf16_f32 v72, v208, v209\n" \
"v_cvt_pk_bf16_f32 v73, v210, v211\n" \
"v_cvt_pk_bf16_f32 v74, v212, v213\n" \
"v_cvt_pk_bf16_f32 v75, v214, v215\n" \
"buffer_store_dwordx4 v[72:75], v20, s[56:59], s70 offen offset:2048 sc1\n" \
"v_mfma_f32_16x16x32_bf16 v[248:251], a[24:27], v[124:127], v[248:251]\n" \
"v_mfma_f32_16x16x32_bf16 a[208:211], a[216:219], v[72:75], a[208:211]\n" \
"v_mul_f32_e32 v216, v56, v216\n" \
"v_mul_f32_e32 v217, v56, v217\n" \
"v_mul_f32_e32 v218, v56, v218\n" \
"v_mul_f32_e32 v219, v56, v219\n" \
"v_mul_f32_e32 v220, v56, v220\n" \
"v_mul_f32_e32 v221, v56, v221\n" \
"v_mul_f32_e32 v222, v56, v222\n" \
"v_mul_f32_e32 v223, v56, v223\n" \
"v_cvt_pk_bf16_f32 v76, v216, v217\n" \
"v_cvt_pk_bf16_f32 v77, v218, v219\n" \
"v_cvt_pk_bf16_f32 v78, v220, v221\n" \
"v_cvt_pk_bf16_f32 v79, v222, v223\n" \
"buffer_store_dwordx4 v[76:79], v20, s[56:59], s70 offen offset:3072 sc1\n" \
"v_add_f32_e32 v224, v224, v160\n" \
"v_add_f32_e32 v225, v225, v161\n" \
"v_add_f32_e32 v226, v226, v162\n" \
"v_add_f32_e32 v227, v227, v163\n" \
"v_add_f32_e32 v228, v228, v164\n" \
"v_add_f32_e32 v229, v229, v165\n" \
"v_add_f32_e32 v230, v230, v166\n" \
"v_add_f32_e32 v231, v231, v167\n" \
"v_mfma_f32_16x16x32_bf16 a[208:211], a[216:219], v[76:79], a[208:211]\n" \
"v_add_f32_e32 v232, v232, v168\n" \
"v_add_f32_e32 v233, v233, v169\n" \
"v_add_f32_e32 v234, v234, v170\n" \
"v_add_f32_e32 v235, v235, v171\n" \
"v_add_f32_e32 v236, v236, v172\n" \
"v_add_f32_e32 v237, v237, v173\n" \
"v_add_f32_e32 v238, v238, v174\n" \
"v_add_f32_e32 v239, v239, v175\n" \
"v_mfma_f32_16x16x32_bf16 v[252:255], a[28:31], v[124:127], v[252:255]\n" \
"v_add_f32_e32 v240, v240, v176\n" \
"v_add_f32_e32 v241, v241, v177\n" \
"v_add_f32_e32 v242, v242, v178\n" \
"v_add_f32_e32 v243, v243, v179\n" \
"v_add_f32_e32 v244, v244, v180\n" \
"v_add_f32_e32 v245, v245, v181\n" \
"v_add_f32_e32 v246, v246, v182\n" \
"v_add_f32_e32 v247, v247, v183\n" \
"v_add_f32_e32 v248, v248, v184\n" \
"v_add_f32_e32 v249, v249, v185\n" \
"v_add_f32_e32 v250, v250, v186\n" \
"v_add_f32_e32 v251, v251, v187\n" \
"v_add_f32_e32 v252, v252, v188\n" \
"v_add_f32_e32 v253, v253, v189\n" \
"v_add_f32_e32 v254, v254, v190\n" \
"v_add_f32_e32 v255, v255, v191\n" \
"v_mfma_f32_16x16x32_bf16 a[204:207], a[212:215], v[124:127], a[204:207]\n" \
"s_nop 7\n" \
"v_accvgpr_read_b32 v57, a204\n" \
"v_add_f32_e32 v57, v57, v43\n" \
"v_max_f32_e32 v62, v62, v57\n" \
"v_rcp_f32_e32 v57, v57\n" \
"v_cmp_lt_f32_e64 s[72:73], s100, v62\n" \
"v_mul_f32_e32 v57, v57, v61\n" \
"v_mul_f32_e32 v224, v57, v224\n" \
"v_mul_f32_e32 v225, v57, v225\n" \
"v_mul_f32_e32 v226, v57, v226\n" \
"v_mul_f32_e32 v227, v57, v227\n" \
"v_mul_f32_e32 v228, v57, v228\n" \
"v_mul_f32_e32 v229, v57, v229\n" \
"v_mul_f32_e32 v230, v57, v230\n" \
"v_mul_f32_e32 v231, v57, v231\n" \
"v_cvt_pk_bf16_f32 v80, v224, v225\n" \
"v_cvt_pk_bf16_f32 v81, v226, v227\n" \
"v_cvt_pk_bf16_f32 v82, v228, v229\n" \
"v_cvt_pk_bf16_f32 v83, v230, v231\n" \
"buffer_store_dwordx4 v[80:83], v20, s[56:59], s71 offen offset:0 sc1\n" \
"v_mul_f32_e32 v232, v57, v232\n" \
"v_mfma_f32_16x16x32_bf16 a[208:211], a[216:219], v[80:83], a[208:211]\n" \
"v_mul_f32_e32 v233, v57, v233\n" \
"v_mul_f32_e32 v234, v57, v234\n" \
"v_mul_f32_e32 v235, v57, v235\n" \
"v_mul_f32_e32 v236, v57, v236\n" \
"v_mul_f32_e32 v237, v57, v237\n" \
"v_mul_f32_e32 v238, v57, v238\n" \
"v_mul_f32_e32 v239, v57, v239\n" \
"v_cvt_pk_bf16_f32 v84, v232, v233\n" \
"v_cvt_pk_bf16_f32 v85, v234, v235\n" \
"v_cvt_pk_bf16_f32 v86, v236, v237\n" \
"v_cvt_pk_bf16_f32 v87, v238, v239\n" \
"buffer_store_dwordx4 v[84:87], v20, s[56:59], s71 offen offset:1024 sc1\n" \
"v_mul_f32_e32 v240, v57, v240\n" \
"v_mfma_f32_16x16x32_bf16 a[208:211], a[216:219], v[84:87], a[208:211]\n" \
"v_mul_f32_e32 v241, v57, v241\n" \
"v_mul_f32_e32 v242, v57, v242\n" \
"v_mul_f32_e32 v243, v57, v243\n" \
"v_mul_f32_e32 v244, v57, v244\n" \
"v_mul_f32_e32 v245, v57, v245\n" \
"v_mul_f32_e32 v246, v57, v246\n" \
"v_mul_f32_e32 v247, v57, v247\n" \
"v_cvt_pk_bf16_f32 v96, v240, v241\n" \
"v_cvt_pk_bf16_f32 v97, v242, v243\n" \
"v_cvt_pk_bf16_f32 v98, v244, v245\n" \
"v_cvt_pk_bf16_f32 v99, v246, v247\n" \
"buffer_store_dwordx4 v[96:99], v20, s[56:59], s71 offen offset:2048 sc1\n" \
"v_mul_f32_e32 v248, v57, v248\n" \
"v_mfma_f32_16x16x32_bf16 a[208:211], a[216:219], v[96:99], a[208:211]\n" \
"v_mul_f32_e32 v249, v57, v249\n" \
"v_mul_f32_e32 v250, v57, v250\n" \
"v_mul_f32_e32 v251, v57, v251\n" \
"v_mul_f32_e32 v252, v57, v252\n" \
"v_mul_f32_e32 v253, v57, v253\n" \
"v_mul_f32_e32 v254, v57, v254\n" \
"v_mul_f32_e32 v255, v57, v255\n" \
"v_cvt_pk_bf16_f32 v100, v248, v249\n" \
"v_cvt_pk_bf16_f32 v101, v250, v251\n" \
"v_cvt_pk_bf16_f32 v102, v252, v253\n" \
"v_cvt_pk_bf16_f32 v103, v254, v255\n" \
"buffer_store_dwordx4 v[100:103], v20, s[56:59], s71 offen offset:3072 sc1\n" \
"s_nop 0\n" \
"v_mfma_f32_16x16x32_bf16 a[208:211], a[216:219], v[100:103], a[208:211]\n" \
"s_nop 7\n" \
"v_accvgpr_read_b32 v63, a208\n" \
"v_cmp_u_f32_e32 vcc, v63, v63\n" \
"s_nop 0\n" \
"s_or_b64 vcc, s[72:73], vcc\n" \
"s_cbranch_scc0 L_out_%=\n" \
"s_branch L_end_%=\n" \
"L_h1_%=:\n" \
"L_pf14:\n" \
"s_cbranch_execz L_pf18\n" \
"s_nop 0\n" \
"s_cmp_eq_u32 s63, 0x400000\n" \
"s_cbranch_scc1 L_hc1_%=\n" \
"s_cmp_lg_u32 s63, 0x0\n" \
"s_cbranch_scc1 L_hn01_%=\n" \
"s_mov_b32 exec_lo, 0xffffffff\n" \
"s_mov_b32 exec_hi, 0xf\n" \
"buffer_load_dword v23, v16, s[80:83], 0 offen\n" \
"s_mov_b64 exec, -1\n" \
"s_branch L_hr1_%=\n" \
"L_hn01_%=:\n" \
"buffer_load_dword v22, v17, s[56:59], 0 offen sc1\n" \
"s_branch L_hr1_%=\n" \
"L_hs1_%=:\n" \
"s_sleep 1\n" \
"buffer_load_dword v22, v17, s[56:59], 0 offen sc1\n" \
"s_add_u32 s101, s101, 1\n" \
"s_waitcnt vmcnt(0)\n" \
"s_cmp_gt_u32 s101, 0x100000\n" \
"s_cbranch_scc1 L_hr1_%=\n" \
"L_hc1_%=:\n" \
"v_cmp_ne_u32_e32 vcc, s62, v22\n" \
"s_nop 4\n" \
"s_cbranch_vccnz L_hs1_%=\n" \
"s_branch L_hr1_%=\n" \
"L_h2_%=:\n" \
"s_cmp_eq_u32 s63, 0x400000\n" \
"s_cbranch_scc1 L_hc2_%=\n" \
"s_cmp_lg_u32 s63, 0x0\n" \
"s_cbranch_scc1 L_hn02_%=\n" \
"s_mov_b32 exec_lo, 0xffffffff\n" \
"s_mov_b32 exec_hi, 0xf\n" \
"buffer_load_dword v23, v16, s[80:83], 0 offen\n" \
"s_mov_b64 exec, -1\n" \
"s_branch L_hr2_%=\n" \
"L_hn02_%=:\n" \
"buffer_load_dword v22, v17, s[56:59], 0 offen sc1\n" \
"s_branch L_hr2_%=\n" \
"L_hs2_%=:\n" \
"s_sleep 1\n" \
"buffer_load_dword v22, v17, s[56:59], 0 offen sc1\n" \
"s_add_u32 s101, s101, 1\n" \
"s_waitcnt vmcnt(0)\n" \
"s_cmp_gt_u32 s101, 0x100000\n" \
"s_cbranch_scc1 L_hr2_%=\n" \
"L_hc2_%=:\n" \
"v_cmp_ne_u32_e32 vcc, s62, v22\n" \
"s_nop 4\n" \
"s_cbranch_vccnz L_hs2_%=\n" \
"s_branch L_hr2_%=\n" \
"L_sa_%=:\n" \
"s_sleep 1\n" \
"buffer_load_dword v37, v38, s[56:59], s64 offen sc1\n" \
"s_add_u32 s101, s101, 1\n" \
"s_waitcnt vmcnt(0)\n" \
"v_cmp_ne_u32_e32 vcc, s62, v37\n" \
"s_cmp_gt_u32 s101, 0x100000\n" \
"s_cbranch_scc1 L_ra_%=\n" \
"s_nop 2\n" \
"s_cbranch_vccnz L_sa_%=\n" \
"s_branch L_ra_%=\n" \
"L_sb_%=:\n" \
"s_sleep 1\n" \
"buffer_load_dword v47, v38, s[56:59], s71 offen sc1\n" \
"s_add_u32 s101, s101, 1\n" \
"s_waitcnt vmcnt(0)\n" \
"v_cmp_ne_u32_e32 vcc, s62, v47\n" \
"s_cmp_gt_u32 s101, 0x100000\n" \
"s_cbranch_scc1 L_rb_%=\n" \
"s_nop 2\n" \
"s_cbranch_vccnz L_sb_%=\n" \
"s_branch L_rb_%=\n" \
"L_sx0_%=:\n" \
"s_sleep 1\n" \
"buffer_load_dword v37, v40, s[56:59], s74 offen sc1\n" \
"s_add_u32 s101, s101, 1\n" \
"s_waitcnt vmcnt(0)\n" \
"v_cmp_eq_u32_e64 s[98:99], s62, v37\n" \
"s_cmp_gt_u32 s101, 0x100000\n" \
"s_cbranch_scc1 L_rx0_%=\n" \
"s_andn2_b32 s75, 0x1, s98\n" \
"s_cbranch_scc1 L_sx0_%=\n" \
"s_branch L_rx0_%=\n" \
"L_sx1_%=:\n" \
"s_sleep 1\n" \
"buffer_load_dword v37, v40, s[56:59], s74 offen sc1\n" \
"s_add_u32 s101, s101, 1\n" \
"s_waitcnt vmcnt(0)\n" \
"v_cmp_eq_u32_e64 s[98:99], s62, v37\n" \
"s_cmp_gt_u32 s101, 0x100000\n" \
"s_cbranch_scc1 L_rx1_%=\n" \
"s_andn2_b32 s75, 0x2, s98\n" \
"s_cbranch_scc1 L_sx1_%=\n" \
"s_branch L_rx1_%=\n" \
"L_sx2_%=:\n" \
"s_sleep 1\n" \
"buffer_load_dword v37, v40, s[56:59], s74 offen sc1\n" \
"s_add_u32 s101, s101, 1\n" \
"s_waitcnt vmcnt(0)\n" \
"v_cmp_eq_u32_e64 s[98:99], s62, v37\n" \
"s_cmp_gt_u32 s101, 0x100000\n" \
"s_cbranch_scc1 L_rx2_%=\n" \
"s_andn2_b32 s75, 0x4, s98\n" \
"s_cbranch_scc1 L_sx2_%=\n" \
"s_branch L_rx2_%=\n" \
"L_sx3_%=:\n" \
"s_sleep 1\n" \
"buffer_load_dword v37, v40, s[56:59], s74 offen sc1\n" \
"s_add_u32 s101, s101, 1\n" \
"s_waitcnt vmcnt(0)\n" \
"v_cmp_eq_u32_e64 s[98:99], s62, v37\n" \
"s_cmp_gt_u32 s101, 0x100000\n" \
"s_cbranch_scc1 L_rx3_%=\n" \
"s_andn2_b32 s75, s100, s98\n" \
"s_cbranch_scc1 L_sx3_%=\n" \
"s_branch L_rx3_%=\n" \
"L_sx4_%=:\n" \
"s_sleep 1\n" \
"buffer_load_dword v37, v40, s[56:59], s74 offen sc1\n" \
"s_add_u32 s101, s101, 1\n" \
"s_waitcnt vmcnt(0)\n" \
"v_cmp_eq_u32_e64 s[98:99], s62, v37\n" \
"s_cmp_gt_u32 s101, 0x100000\n" \
"s_cbranch_scc1 L_rx4_%=\n" \
"s_andn2_b32 s75, 0x10, s98\n" \
"s_cbranch_scc1 L_sx4_%=\n" \
"s_branch L_rx4_%=\n" \
"L_sx5_%=:\n" \
"s_sleep 1\n" \
"buffer_load_dword v37, v40, s[56:59], s74 offen sc1\n" \
"s_add_u32 s101, s101, 1\n" \
"s_waitcnt vmcnt(0)\n" \
"v_cmp_eq_u32_e64 s[98:99], s62, v37\n" \
"s_cmp_gt_u32 s101, 0x100000\n" \
"s_cbranch_scc1 L_rx5_%=\n" \
"s_andn2_b32 s75, 0x20, s98\n" \
"s_cbranch_scc1 L_sx5_%=\n" \
"s_branch L_rx5_%=\n" \
"L_sx6_%=:\n" \
"s_sleep 1\n" \
"buffer_load_dword v37, v40, s[56:59], s74 offen sc1\n" \
"s_add_u32 s101, s101, 1\n" \
"s_waitcnt vmcnt(0)\n" \
"v_cmp_eq_u32_e64 s[98:99], s62, v37\n" \
"s_cmp_gt_u32 s101, 0x100000\n" \
"s_cbranch_scc1 L_rx6_%=\n" \
"s_andn2_b32 s75, 0x40, s98\n" \
"s_cbranch_scc1 L_sx6_%=\n" \
"s_branch L_rx6_%=\n" \
"L_sx7_%=:\n" \
"s_sleep 1\n" \
"buffer_load_dword v37, v40, s[56:59], s74 offen sc1\n" \
"s_add_u32 s101, s101, 1\n" \
"s_waitcnt vmcnt(0)\n" \
"v_cmp_eq_u32_e64 s[98:99], s62, v37\n" \
"s_cmp_gt_u32 s101, 0x100000\n" \
"s_cbranch_scc1 L_rx7_%=\n" \
"s_andn2_b32 s75, 0x80, s98\n" \
"s_cbranch_scc1 L_sx7_%=\n" \
"s_branch L_rx7_%=\n" \
"L_sx8_%=:\n" \
"s_sleep 1\n" \
"buffer_load_dword v37, v40, s[56:59], s74 offen sc1\n" \
"s_add_u32 s101, s101, 1\n" \
"s_waitcnt vmcnt(0)\n" \
"v_cmp_eq_u32_e64 s[98:99], s62, v37\n" \
"s_cmp_gt_u32 s101, 0x100000\n" \
"s_cbranch_scc1 L_rx8_%=\n" \
"s_andn2_b32 s75, 0x100, s98\n" \
"s_cbranch_scc1 L_sx8_%=\n" \
"s_branch L_rx8_%=\n" \
"L_sx9_%=:\n" \
"s_sleep 1\n" \
"buffer_load_dword v37, v40, s[56:59], s74 offen sc1\n" \
"s_add_u32 s101, s101, 1\n" \
"s_waitcnt vmcnt(0)\n" \
"v_cmp_eq_u32_e64 s[98:99], s62, v37\n" \
"s_cmp_gt_u32 s101, 0x100000\n" \
"s_cbranch_scc1 L_rx9_%=\n" \
"s_andn2_b32 s75, 0x200, s98\n" \
"s_cbranch_scc1 L_sx9_%=\n" \
"s_branch L_rx9_%=\n" \
"L_sx10_%=:\n" \
"s_sleep 1\n" \
"buffer_load_dword v37, v40, s[56:59], s74 offen sc1\n" \
"s_add_u32 s101, s101, 1\n" \
"s_waitcnt vmcnt(0)\n" \
"v_cmp_eq_u32_e64 s[98:99], s62, v37\n" \
"s_cmp_gt_u32 s101, 0x100000\n" \
"s_cbranch_scc1 L_rx10_%=\n" \
"s_andn2_b32 s75, 0x400, s98\n" \
"s_cbranch_scc1 L_sx10_%=\n" \
"s_branch L_rx10_%=\n" \
"L_att_stub:\n" \
"s_mov_b32 s101, 0\n" \
"v_add_u32_e32 v32, 0x49000, v20\n" \
"v_readlane_b32 s96, v11, 32\n" \
"v_readlane_b32 s97, v11, 33\n" \
"buffer_load_dwordx4 a[220:223], v20, s[56:59], s60 offen offset:0 sc1\n" \
"buffer_load_dwordx4 a[236:239], v32, s[56:59], s60 offen offset:0 sc1\n" \
"buffer_load_dwordx4 a[224:227], v20, s[56:59], s60 offen offset:1024 sc1\n" \
"buffer_load_dwordx4 a[240:243], v32, s[56:59], s60 offen offset:1024 sc1\n" \
"buffer_load_dwordx4 a[228:231], v20, s[56:59], s60 offen offset:2048 sc1\n" \
"buffer_load_dwordx4 a[244:247], v32, s[56:59], s60 offen offset:2048 sc1\n" \
"buffer_load_dwordx4 a[232:235], v20, s[56:59], s60 offen offset:3072 sc1\n" \
"buffer_load_dwordx4 a[248:251], v32, s[56:59], s60 offen offset:3072 sc1\n" \
"buffer_load_dwordx4 a[32:35], v20, s[56:59], s96 offen offset:0 sc1\n" \
"buffer_load_dwordx4 a[40:43], v32, s[56:59], s96 offen offset:0 sc1\n" \
"buffer_load_dwordx4 a[48:51], v20, s[56:59], s97 offen offset:0 sc1\n" \
"buffer_load_dwordx4 a[56:59], v32, s[56:59], s97 offen offset:0 sc1\n" \
"buffer_load_dwordx4 a[36:39], v20, s[56:59], s96 offen offset:1024 sc1\n" \
"buffer_load_dwordx4 a[44:47], v32, s[56:59], s96 offen offset:1024 sc1\n" \
"buffer_load_dwordx4 a[52:55], v20, s[56:59], s97 offen offset:1024 sc1\n" \
"buffer_load_dwordx4 a[60:63], v32, s[56:59], s97 offen offset:1024 sc1\n" \
"buffer_load_dwordx4 v[64:67], v20, s[56:59], s61 offen offset:0 sc1\n" \
"buffer_load_dwordx4 v[68:71], v20, s[56:59], s61 offen offset:1024 sc1\n" \
"buffer_load_dwordx4 v[72:75], v20, s[56:59], s61 offen offset:2048 sc1\n" \
"buffer_load_dwordx4 v[76:79], v20, s[56:59], s61 offen offset:3072 sc1\n" \
"buffer_load_dwordx4 v[80:83], v32, s[56:59], s61 offen offset:0 sc1\n" \
"buffer_load_dwordx4 v[84:87], v32, s[56:59], s61 offen offset:1024 sc1\n" \
"buffer_load_dwordx4 v[96:99], v32, s[56:59], s61 offen offset:2048 sc1\n" \
"buffer_load_dwordx4 v[100:103], v32, s[56:59], s61 offen offset:3072 sc1\n" \
"s_waitcnt vmcnt(0)\n" \
"s_branch L_out_%=\n" \
"L_sw_%=:\n" \
"L_pf15:\n" \
"s_cbranch_execz L_pf19\n" \
"s_nop 0\n" \
"v_mfma_f32_16x16x32_bf16 v[72:75], a[128:131], a[16:19], v[36:39]\n" \
"s_add_u32 s68, s60, 0xfff6e000\n" \
"s_add_u32 s69, s60, 0xfffb7000\n" \
"s_add_u32 s70, s60, 0x0\n" \
"s_add_u32 s71, s60, 0x49000\n" \
"v_mfma_f32_16x16x32_bf16 v[72:75], a[132:135], a[20:23], v[72:75]\n" \
"s_xor_b32 m0, s65, 0x12000\n" \
"ds_read_b128 a[160:163], v49 offset:0\n" \
"v_mfma_f32_16x16x32_bf16 v[72:75], a[136:139], a[24:27], v[72:75]\n" \
"buffer_load_dwordx4 v50, s[8:11], s64 offen offset:0 sc1 lds\n" \
"v_mfma_f32_16x16x32_bf16 v[72:75], a[140:143], a[28:31], v[72:75]\n" \
"ds_read_b128 a[164:167], v49 offset:1024\n" \
"ds_read_b128 a[168:171], v49 offset:2048\n" \
"v_mfma_f32_16x16x32_bf16 v[76:79], a[156:159], a[28:31], v[36:39]\n" \
"ds_read_b128 a[172:175], v49 offset:3072\n" \
"v_cvt_pk_bf16_f32 v108, v88, v89\n" \
"v_mfma_f32_16x16x32_bf16 v[76:79], a[152:155], a[24:27], v[76:79]\n" \
"buffer_load_dwordx4 v50, s[8:11], s64 offen offset:1024 sc1 lds\n" \
"v_mfma_f32_16x16x32_bf16 v[76:79], a[148:151], a[20:23], v[76:79]\n" \
"v_cvt_pk_bf16_f32 v109, v90, v91\n" \
"ds_read_b128 a[176:179], v49 offset:4096\n" \
"v_mfma_f32_16x16x32_bf16 v[76:79], a[144:147], a[16:19], v[76:79]\n" \
"ds_read_b128 a[180:183], v49 offset:5120\n" \
"s_mov_b32 s65, s13\n" \
"v_mfma_f32_16x16x32_bf16 v[80:83], a[128:131], a[32:35], v[40:43]\n" \
"v_add_u32_e32 v63, s15, v20\n" \
"s_add_u32 s67, s14, 40960\n" \
"v_mfma_f32_16x16x32_bf16 v[80:83], a[132:135], a[36:39], v[80:83]\n" \
"v_add_u32_e32 v50, 0x6000, v63\n" \
"s_xor_b32 m0, s65, 0x8000\n" \
"v_mfma_f32_16x16x32_bf16 v[80:83], a[136:139], a[40:43], v[80:83]\n" \
"v_cvt_pk_bf16_f32 v110, v92, v93\n" \
"v_cvt_pk_bf16_f32 v111, v94, v95\n" \
"v_mfma_f32_16x16x32_bf16 v[80:83], a[140:143], a[44:47], v[80:83]\n" \
"buffer_load_dwordx4 v50, s[8:11], s67 offen offset:0 sc1 lds\n" \
"v_mfma_f32_16x16x32_bf16 v[84:87], a[156:159], a[44:47], v[40:43]\n" \
"ds_read_b128 a[184:187], v49 offset:6144\n" \
"ds_read_b128 a[188:191], v49 offset:7168\n" \
"v_mfma_f32_16x16x32_bf16 v[84:87], a[152:155], a[40:43], v[84:87]\n" \
"v_and_b32_e32 v96, v12, v96\n" \
"v_and_b32_e32 v97, v13, v97\n" \
"v_mfma_f32_16x16x32_bf16 v[84:87], a[148:151], a[36:39], v[84:87]\n" \
"v_and_b32_e32 v98, v14, v98\n" \
"v_and_b32_e32 v99, v15, v99\n" \
"v_mfma_f32_16x16x32_bf16 v[84:87], a[144:147], a[32:35], v[84:87]\n" \
"v_exp_f32_e32 v72, v72\n" \
"v_mfma_f32_16x16x32_bf16 v[88:91], a[128:131], a[48:51], v[44:47]\n" \
"buffer_load_dwordx4 v50, s[8:11], s67 offen offset:1024 sc1 lds\n" \
"v_mfma_f32_16x16x32_bf16 v[88:91], a[132:135], a[52:55], v[88:91]\n" \
"v_exp_f32_e32 v73, v73\n" \
"v_mfma_f32_16x16x32_bf16 v[88:91], a[136:139], a[56:59], v[88:91]\n" \
"v_exp_f32_e32 v74, v74\n" \
"v_mfma_f32_16x16x32_bf16 v[88:91], a[140:143], a[60:63], v[88:91]\n" \
"v_exp_f32_e32 v75, v75\n" \
"v_mfma_f32_16x16x32_bf16 v[92:95], a[156:159], a[60:63], v[44:47]\n" \
"v_exp_f32_e32 v76, v76\n" \
"v_mfma_f32_16x16x32_bf16 v[92:95], a[152:155], a[56:59], v[92:95]\n" \
"v_exp_f32_e32 v77, v77\n" \
"v_mfma_f32_16x16x32_bf16 v[92:95], a[148:151], a[52:55], v[92:95]\n" \
"v_exp_f32_e32 v78, v78\n" \
"v_mfma_f32_16x16x32_bf16 v[92:95], a[144:147], a[48:51], v[92:95]\n" \
"v_cvt_pk_bf16_f32 v116, v72, v73\n" \
"s_waitcnt lgkmcnt(2)\n" \
"v_mfma_f32_16x16x32_bf16 v[128:131], a[160:163], v[96:99], v[128:131]\n" \
"v_exp_f32_e32 v79, v79\n" \
"v_mfma_f32_16x16x32_bf16 v[132:135], a[164:167], v[96:99], v[132:135]\n" \
"v_cvt_pk_bf16_f32 v117, v74, v75\n" \
"v_cvt_pk_bf16_f32 v118, v76, v77\n" \
"v_mfma_f32_16x16x32_bf16 v[136:139], a[168:171], v[96:99], v[136:139]\n" \
"v_cvt_pk_bf16_f32 v119, v78, v79\n" \
"v_and_b32_e32 v116, v12, v116\n" \
"v_mfma_f32_16x16x32_bf16 v[140:143], a[172:175], v[96:99], v[140:143]\n" \
"v_and_b32_e32 v117, v13, v117\n" \
"v_and_b32_e32 v118, v14, v118\n" \
"v_mfma_f32_16x16x32_bf16 a[192:195], a[212:215], v[96:99], a[192:195]\n" \
"v_and_b32_e32 v119, v15, v119\n" \
"s_mov_b32 s64, s14\n" \
"v_mfma_f32_16x16x32_bf16 v[160:163], a[160:163], v[100:103], v[160:163]\n" \
"buffer_load_dwordx4 v50, s[8:11], s67 offen offset:2048 sc1 lds\n" \
"v_mfma_f32_16x16x32_bf16 v[160:163], a[176:179], v[116:119], v[160:163]\n" \
"buffer_load_dwordx4 v50, s[8:11], s67 offen offset:3072 sc1 lds\n" \
"v_mfma_f32_16x16x32_bf16 v[164:167], a[180:183], v[116:119], v[164:167]\n" \
"v_exp_f32_e32 v80, v80\n" \
"v_mfma_f32_16x16x32_bf16 v[164:167], a[164:167], v[100:103], v[164:167]\n" \
"v_exp_f32_e32 v81, v81\n" \
"v_mfma_f32_16x16x32_bf16 v[168:171], a[168:171], v[100:103], v[168:171]\n" \
"v_cvt_pk_bf16_f32 v120, v80, v81\n" \
"s_waitcnt lgkmcnt(0)\n" \
"v_mfma_f32_16x16x32_bf16 v[168:171], a[184:187], v[116:119], v[168:171]\n" \
"v_exp_f32_e32 v82, v82\n" \
"v_mfma_f32_16x16x32_bf16 v[172:175], a[188:191], v[116:119], v[172:175]\n" \
"v_exp_f32_e32 v83, v83\n" \
"v_mfma_f32_16x16x32_bf16 v[172:175], a[172:175], v[100:103], v[172:175]\n" \
"v_exp_f32_e32 v84, v84\n" \
"v_mfma_f32_16x16x32_bf16 a[196:199], a[212:215], v[100:103], a[196:199]\n" \
"v_exp_f32_e32 v85, v85\n" \
"v_mfma_f32_16x16x32_bf16 a[196:199], a[212:215], v[116:119], a[196:199]\n" \
"v_exp_f32_e32 v86, v86\n" \
"v_mfma_f32_16x16x32_bf16 v[192:195], a[160:163], v[104:107], v[192:195]\n" \
"v_exp_f32_e32 v87, v87\n" \
"v_cvt_pk_bf16_f32 v121, v82, v83\n" \
"v_cvt_pk_bf16_f32 v122, v84, v85\n" \
"v_cvt_pk_bf16_f32 v123, v86, v87\n" \
"v_add_u32_e32 v51, 0x12000, v63\n" \
"s_xor_b32 m0, s65, 0xa000\n" \
"v_mfma_f32_16x16x32_bf16 v[192:195], a[176:179], v[120:123], v[192:195]\n" \
"buffer_load_dwordx4 v51, s[8:11], s64 offen offset:0 sc1 lds\n" \
"v_mfma_f32_16x16x32_bf16 v[196:199], a[180:183], v[120:123], v[196:199]\n" \
"v_exp_f32_e32 v88, v88\n" \
"v_mfma_f32_16x16x32_bf16 v[196:199], a[164:167], v[104:107], v[196:199]\n" \
"v_exp_f32_e32 v89, v89\n" \
"v_mfma_f32_16x16x32_bf16 v[200:203], a[168:171], v[104:107], v[200:203]\n" \
"v_exp_f32_e32 v90, v90\n" \
"v_mfma_f32_16x16x32_bf16 v[200:203], a[184:187], v[120:123], v[200:203]\n" \
"v_exp_f32_e32 v91, v91\n" \
"v_mfma_f32_16x16x32_bf16 v[204:207], a[188:191], v[120:123], v[204:207]\n" \
"v_exp_f32_e32 v92, v92\n" \
"v_mfma_f32_16x16x32_bf16 v[204:207], a[172:175], v[104:107], v[204:207]\n" \
"v_exp_f32_e32 v93, v93\n" \
"v_mfma_f32_16x16x32_bf16 v[224:227], a[160:163], v[108:111], v[224:227]\n" \
"v_exp_f32_e32 v94, v94\n" \
"v_exp_f32_e32 v95, v95\n" \
"buffer_load_dwordx4 v51, s[8:11], s64 offen offset:1024 sc1 lds\n" \
"v_cvt_pk_bf16_f32 v124, v88, v89\n" \
"v_cvt_pk_bf16_f32 v125, v90, v91\n" \
"v_cvt_pk_bf16_f32 v126, v92, v93\n" \
"v_cvt_pk_bf16_f32 v127, v94, v95\n" \
"buffer_load_dwordx4 v51, s[8:11], s64 offen offset:2048 sc1 lds\n" \
"ds_read_b32 v58, v18 offset:512\n" \
"v_mfma_f32_16x16x32_bf16 v[224:227], a[176:179], v[124:127], v[224:227]\n" \
"ds_read_b32 v59, v18 offset:640\n" \
"ds_read_b32 v60, v18 offset:768\n" \
"v_mfma_f32_16x16x32_bf16 v[228:231], a[180:183], v[124:127], v[228:231]\n" \
"ds_read_b32 v61, v18 offset:896\n" \
"ds_read_b128 a[128:131], v48 offset:0\n" \
"v_mfma_f32_16x16x32_bf16 v[228:231], a[164:167], v[108:111], v[228:231]\n" \
"ds_read_b128 a[132:135], v48 offset:1024\n" \
"ds_read_b128 a[136:139], v48 offset:2048\n" \
"v_mfma_f32_16x16x32_bf16 v[232:235], a[168:171], v[108:111], v[232:235]\n" \
"ds_read_b128 a[140:143], v48 offset:3072\n" \
"ds_read_b128 a[144:147], v48 offset:4096\n" \
"v_mfma_f32_16x16x32_bf16 v[232:235], a[184:187], v[124:127], v[232:235]\n" \
"ds_read_b128 a[148:151], v48 offset:5120\n" \
"ds_read_b128 a[152:155], v48 offset:6144\n" \
"v_mfma_f32_16x16x32_bf16 v[236:239], a[188:191], v[124:127], v[236:239]\n" \
"ds_read_b128 a[156:159], v48 offset:7168\n" \
"v_add_u32_e32 v52, 0x14000, v63\n" \
"v_mfma_f32_16x16x32_bf16 v[236:239], a[172:175], v[108:111], v[236:239]\n" \
"buffer_load_dwordx4 v51, s[8:11], s64 offen offset:3072 sc1 lds\n" \
"v_add_u32_e32 v53, 0x20000, v63\n" \
"v_readlane_b32 s93, v11, 17\n" \
"v_readlane_b32 s92, v11, 22\n" \
"v_accvgpr_read_b32 v54, a192\n" \
"v_max_f32_e32 v62, v62, v54\n" \
"v_accvgpr_read_b32 v55, a196\n" \
"v_max_f32_e32 v62, v62, v55\n" \
"v_rcp_f32_e32 v54, v54\n" \
"v_rcp_f32_e32 v55, v55\n" \
"v_readlane_b32 s95, v11, 2\n" \
"s_waitcnt lgkmcnt(11)\n" \
"v_mul_f32_e32 v54, v54, v58\n" \
"v_mul_f32_e32 v128, v54, v128\n" \
"s_waitcnt lgkmcnt(10)\n" \
"v_mul_f32_e32 v55, v55, v59\n" \
"v_mul_f32_e32 v129, v54, v129\n" \
"v_mul_f32_e32 v136, v54, v136\n" \
"v_mul_f32_e32 v137, v54, v137\n" \
"v_mul_f32_e32 v160, v55, v160\n" \
"v_mul_f32_e32 v161, v55, v161\n" \
"v_mul_f32_e32 v168, v55, v168\n" \
"v_mul_f32_e32 v169, v55, v169\n" \
"v_cvt_pk_bf16_f32 v128, v128, v129\n" \
"v_mul_f32_e32 v130, v54, v130\n" \
"v_mul_f32_e32 v131, v54, v131\n" \
"v_cvt_pk_bf16_f32 v136, v136, v137\n" \
"v_mul_f32_e32 v138, v54, v138\n" \
"v_mul_f32_e32 v139, v54, v139\n" \
"v_cvt_pk_bf16_f32 v160, v160, v161\n" \
"v_mul_f32_e32 v162, v55, v162\n" \
"v_mul_f32_e32 v163, v55, v163\n" \
"v_cvt_pk_bf16_f32 v168, v168, v169\n" \
"v_mul_f32_e32 v170, v55, v170\n" \
"s_waitcnt vmcnt(10) lgkmcnt(0)\n" \
"s_barrier\n" \
"v_mfma_f32_16x16x32_bf16 v[80:83], a[128:131], a[32:35], v[40:43]\n" \
"ds_read_b128 a[160:163], v49 offset:8192\n" \
"ds_read_b128 a[164:167], v49 offset:9216\n" \
"v_mfma_f32_16x16x32_bf16 v[80:83], a[132:135], a[36:39], v[80:83]\n" \
"ds_read_b128 a[168:171], v49 offset:10240\n" \
"ds_read_b128 a[172:175], v49 offset:11264\n" \
"v_mfma_f32_16x16x32_bf16 v[80:83], a[136:139], a[40:43], v[80:83]\n" \
"s_xor_b32 m0, s65, 0x18000\n" \
"ds_read_b128 a[176:179], v49 offset:12288\n" \
"v_mfma_f32_16x16x32_bf16 v[80:83], a[140:143], a[44:47], v[80:83]\n" \
"buffer_load_dwordx4 v52, s[8:11], s64 offen offset:0 sc1 lds\n" \
"v_mfma_f32_16x16x32_bf16 v[84:87], a[156:159], a[44:47], v[40:43]\n" \
"ds_read_b128 a[180:183], v49 offset:13312\n" \
"ds_read_b128 a[184:187], v49 offset:14336\n" \
"v_mfma_f32_16x16x32_bf16 v[84:87], a[152:155], a[40:43], v[84:87]\n" \
"buffer_load_dwordx4 v52, s[8:11], s64 offen offset:1024 sc1 lds\n" \
"v_mfma_f32_16x16x32_bf16 v[84:87], a[148:151], a[36:39], v[84:87]\n" \
"ds_read_b128 a[188:191], v49 offset:15360\n" \
"v_add_u32_e32 v49, 0x10000, v48\n" \
"v_mfma_f32_16x16x32_bf16 v[84:87], a[144:147], a[32:35], v[84:87]\n" \
"buffer_load_dwordx4 v52, s[8:11], s64 offen offset:2048 sc1 lds\n" \
"v_mfma_f32_16x16x32_bf16 v[88:91], a[128:131], a[48:51], v[44:47]\n" \
"ds_read_b128 a[128:131], v48 offset:8192\n" \
"v_mul_f32_e32 v171, v55, v171\n" \
"v_mfma_f32_16x16x32_bf16 v[88:91], a[132:135], a[52:55], v[88:91]\n" \
"ds_read_b128 a[132:135], v48 offset:9216\n" \
"v_cvt_pk_bf16_f32 v129, v130, v131\n" \
"v_mfma_f32_16x16x32_bf16 v[88:91], a[136:139], a[56:59], v[88:91]\n" \
"buffer_load_dwordx4 v52, s[8:11], s64 offen offset:3072 sc1 lds\n" \
"v_mfma_f32_16x16x32_bf16 v[88:91], a[140:143], a[60:63], v[88:91]\n" \
"s_mov_b32 m0, s65\n" \
"ds_read_b128 a[136:139], v48 offset:10240\n" \
"v_mfma_f32_16x16x32_bf16 v[92:95], a[156:159], a[60:63], v[44:47]\n" \
"ds_read_b128 a[140:143], v48 offset:11264\n" \
"ds_read_b128 a[156:159], v48 offset:15360\n" \
"v_mfma_f32_16x16x32_bf16 v[92:95], a[152:155], a[56:59], v[92:95]\n" \
"buffer_load_dwordx4 v53, s[8:11], s64 offen offset:0 sc1 lds\n" \
"v_mfma_f32_16x16x32_bf16 v[92:95], a[148:151], a[52:55], v[92:95]\n" \
"ds_read_b128 a[148:151], v48 offset:13312\n" \
"ds_read_b128 a[152:155], v48 offset:14336\n" \
"v_mfma_f32_16x16x32_bf16 v[92:95], a[144:147], a[48:51], v[92:95]\n" \
"ds_read_b128 a[144:147], v48 offset:12288\n" \
"s_waitcnt lgkmcnt(12)\n" \
"v_mfma_f32_16x16x32_bf16 v[144:147], a[160:163], v[96:99], v[144:147]\n" \
"buffer_load_dwordx4 v53, s[8:11], s64 offen offset:1024 sc1 lds\n" \
"v_mfma_f32_16x16x32_bf16 v[148:151], a[164:167], v[96:99], v[148:151]\n" \
"v_exp_f32_e32 v88, v88\n" \
"v_mfma_f32_16x16x32_bf16 v[152:155], a[168:171], v[96:99], v[152:155]\n" \
"v_exp_f32_e32 v89, v89\n" \
"v_mfma_f32_16x16x32_bf16 v[156:159], a[172:175], v[96:99], v[156:159]\n" \
"v_exp_f32_e32 v90, v90\n" \
"v_mfma_f32_16x16x32_bf16 v[176:179], a[160:163], v[100:103], v[176:179]\n" \
"s_xor_b32 s65, s65, 0x8000\n" \
"s_waitcnt lgkmcnt(8)\n" \
"v_mfma_f32_16x16x32_bf16 v[176:179], a[176:179], v[116:119], v[176:179]\n" \
"buffer_load_dwordx4 v53, s[8:11], s64 offen offset:2048 sc1 lds\n" \
"v_mfma_f32_16x16x32_bf16 v[180:183], a[180:183], v[116:119], v[180:183]\n" \
"v_exp_f32_e32 v91, v91\n" \
"v_mfma_f32_16x16x32_bf16 v[180:183], a[164:167], v[100:103], v[180:183]\n" \
"buffer_load_dwordx4 v53, s[8:11], s64 offen offset:3072 sc1 lds\n" \
"v_mfma_f32_16x16x32_bf16 v[184:187], a[168:171], v[100:103], v[184:187]\n" \
"s_add_u32 m0, s93, 0x0\n" \
"s_add_u32 s64, s64, 0x10000\n" \
"v_mfma_f32_16x16x32_bf16 v[184:187], a[184:187], v[116:119], v[184:187]\n" \
"v_exp_f32_e32 v92, v92\n" \
"v_mfma_f32_16x16x32_bf16 v[188:191], a[188:191], v[116:119], v[188:191]\n" \
"buffer_load_dwordx4 v0, s[80:83], s92 offen offset:0 lds\n" \
"v_mfma_f32_16x16x32_bf16 v[188:191], a[172:175], v[100:103], v[188:191]\n" \
"v_exp_f32_e32 v93, v93\n" \
"v_mfma_f32_16x16x32_bf16 v[208:211], a[160:163], v[104:107], v[208:211]\n" \
"v_exp_f32_e32 v94, v94\n" \
"v_mfma_f32_16x16x32_bf16 v[208:211], a[176:179], v[120:123], v[208:211]\n" \
"v_exp_f32_e32 v95, v95\n" \
"v_mfma_f32_16x16x32_bf16 v[212:215], a[180:183], v[120:123], v[212:215]\n" \
"buffer_load_dwordx4 v1, s[80:83], s92 offen offset:1024 lds\n" \
"v_mfma_f32_16x16x32_bf16 v[212:215], a[164:167], v[104:107], v[212:215]\n" \
"v_exp_f32_e32 v80, v80\n" \
"v_mfma_f32_16x16x32_bf16 v[216:219], a[168:171], v[104:107], v[216:219]\n" \
"v_exp_f32_e32 v81, v81\n" \
"v_mfma_f32_16x16x32_bf16 v[216:219], a[184:187], v[120:123], v[216:219]\n" \
"buffer_load_dwordx4 v2, s[80:83], s92 offen offset:2048 lds\n" \
"v_mfma_f32_16x16x32_bf16 v[220:223], a[188:191], v[120:123], v[220:223]\n" \
"v_exp_f32_e32 v82, v82\n" \
"v_mfma_f32_16x16x32_bf16 v[220:223], a[172:175], v[104:107], v[220:223]\n" \
"L_pf16:\n" \
"s_cbranch_execz L_retA\n" \
"s_nop 0\n" \
"v_exp_f32_e32 v83, v83\n" \
"v_mfma_f32_16x16x32_bf16 a[200:203], a[212:215], v[104:107], a[200:203]\n" \
"buffer_load_dwordx4 v3, s[80:83], s92 offen offset:3072 lds\n" \
"v_mfma_f32_16x16x32_bf16 a[200:203], a[212:215], v[120:123], a[200:203]\n" \
"v_exp_f32_e32 v84, v84\n" \
"v_mfma_f32_16x16x32_bf16 v[240:243], a[160:163], v[108:111], v[240:243]\n" \
"v_exp_f32_e32 v85, v85\n" \
"v_mfma_f32_16x16x32_bf16 v[240:243], a[176:179], v[124:127], v[240:243]\n" \
"v_exp_f32_e32 v86, v86\n" \
"v_mfma_f32_16x16x32_bf16 v[244:247], a[180:183], v[124:127], v[244:247]\n" \
"v_exp_f32_e32 v87, v87\n" \
"v_mfma_f32_16x16x32_bf16 v[244:247], a[164:167], v[108:111], v[244:247]\n" \
"v_cvt_pk_bf16_f32 v104, v80, v81\n" \
"v_cvt_pk_bf16_f32 v105, v82, v83\n" \
"v_mfma_f32_16x16x32_bf16 v[248:251], a[168:171], v[108:111], v[248:251]\n" \
"v_cvt_pk_bf16_f32 v106, v84, v85\n" \
"v_cvt_pk_bf16_f32 v107, v86, v87\n" \
"v_mfma_f32_16x16x32_bf16 v[248:251], a[184:187], v[124:127], v[248:251]\n" \
"v_and_b32_e32 v104, v12, v104\n" \
"v_and_b32_e32 v105, v13, v105\n" \
"v_mfma_f32_16x16x32_bf16 v[252:255], a[188:191], v[124:127], v[252:255]\n" \
"v_and_b32_e32 v106, v14, v106\n" \
"v_and_b32_e32 v107, v15, v107\n" \
"v_mfma_f32_16x16x32_bf16 v[252:255], a[172:175], v[108:111], v[252:255]\n" \
"v_add_u32_e32 v48, s16, v20\n" \
"v_mul_f32_e32 v144, v54, v144\n" \
"v_mfma_f32_16x16x32_bf16 a[204:207], a[212:215], v[108:111], a[204:207]\n" \
"v_cvt_pk_bf16_f32 v108, v88, v89\n" \
"v_cvt_pk_bf16_f32 v109, v90, v91\n" \
"v_mfma_f32_16x16x32_bf16 a[204:207], a[212:215], v[124:127], a[204:207]\n" \
"v_cvt_pk_bf16_f32 v110, v92, v93\n" \
"v_cvt_pk_bf16_f32 v111, v94, v95\n" \
"v_mul_f32_e32 v145, v54, v145\n" \
"v_mul_f32_e32 v152, v54, v152\n" \
"v_mul_f32_e32 v153, v54, v153\n" \
"v_mul_f32_e32 v176, v55, v176\n" \
"v_mul_f32_e32 v177, v55, v177\n" \
"v_mul_f32_e32 v184, v55, v184\n" \
"v_mul_f32_e32 v185, v55, v185\n" \
"v_cvt_pk_bf16_f32 v144, v144, v145\n" \
"v_mul_f32_e32 v146, v54, v146\n" \
"v_mul_f32_e32 v147, v54, v147\n" \
"v_cvt_pk_bf16_f32 v152, v152, v153\n" \
"v_mul_f32_e32 v154, v54, v154\n" \
"v_mul_f32_e32 v155, v54, v155\n" \
"v_cvt_pk_bf16_f32 v176, v176, v177\n" \
"v_mul_f32_e32 v178, v55, v178\n" \
"v_mul_f32_e32 v179, v55, v179\n" \
"v_cvt_pk_bf16_f32 v184, v184, v185\n" \
"v_mul_f32_e32 v186, v55, v186\n" \
"v_mul_f32_e32 v187, v55, v187\n" \
"v_mul_f32_e32 v132, v54, v132\n" \
"v_mul_f32_e32 v133, v54, v133\n" \
"v_mul_f32_e32 v134, v54, v134\n" \
"v_mul_f32_e32 v135, v54, v135\n" \
"v_cvt_pk_bf16_f32 v137, v138, v139\n" \
"v_mul_f32_e32 v140, v54, v140\n" \
"v_mul_f32_e32 v141, v54, v141\n" \
"v_mul_f32_e32 v142, v54, v142\n" \
"v_mul_f32_e32 v143, v54, v143\n" \
"v_cvt_pk_bf16_f32 v161, v162, v163\n" \
"v_mul_f32_e32 v164, v55, v164\n" \
"v_mul_f32_e32 v165, v55, v165\n" \
"v_mul_f32_e32 v166, v55, v166\n" \
"v_mul_f32_e32 v167, v55, v167\n" \
"v_cvt_pk_bf16_f32 v169, v170, v171\n" \
"v_mul_f32_e32 v172, v55, v172\n" \
"v_mul_f32_e32 v173, v55, v173\n" \
"v_mul_f32_e32 v174, v55, v174\n" \
"v_mul_f32_e32 v175, v55, v175\n" \
"v_cvt_pk_bf16_f32 v145, v146, v147\n" \
"v_mul_f32_e32 v148, v54, v148\n" \
"v_mul_f32_e32 v149, v54, v149\n" \
"v_mul_f32_e32 v150, v54, v150\n" \
"v_mul_f32_e32 v151, v54, v151\n" \
"v_cvt_pk_bf16_f32 v153, v154, v155\n" \
"v_mul_f32_e32 v156, v54, v156\n" \
"v_mul_f32_e32 v157, v54, v157\n" \
"v_mul_f32_e32 v158, v54, v158\n" \
"v_mul_f32_e32 v159, v54, v159\n" \
"v_cvt_pk_bf16_f32 v177, v178, v179\n" \
"v_mul_f32_e32 v180, v55, v180\n" \
"v_mul_f32_e32 v181, v55, v181\n" \
"v_mul_f32_e32 v182, v55, v182\n" \
"v_mul_f32_e32 v183, v55, v183\n" \
"v_cvt_pk_bf16_f32 v185, v186, v187\n" \
"v_mul_f32_e32 v188, v55, v188\n" \
"v_mul_f32_e32 v189, v55, v189\n" \
"v_mul_f32_e32 v190, v55, v190\n" \
"v_mul_f32_e32 v191, v55, v191\n" \
"v_cvt_pk_bf16_f32 v130, v132, v133\n" \
"v_cvt_pk_bf16_f32 v131, v134, v135\n" \
"buffer_store_dwordx4 v[128:131], v20, s[56:59], s68 offen offset:0 sc1\n" \
"v_cvt_pk_bf16_f32 v138, v140, v141\n" \
"v_cvt_pk_bf16_f32 v139, v142, v143\n" \
"buffer_store_dwordx4 v[136:139], v20, s[56:59], s68 offen offset:1024 sc1\n" \
"v_cvt_pk_bf16_f32 v162, v164, v165\n" \
"v_cvt_pk_bf16_f32 v163, v166, v167\n" \
"buffer_store_dwordx4 v[160:163], v20, s[56:59], s69 offen offset:0 sc1\n" \
"v_cvt_pk_bf16_f32 v170, v172, v173\n" \
"v_cvt_pk_bf16_f32 v171, v174, v175\n" \
"buffer_store_dwordx4 v[168:171], v20, s[56:59], s69 offen offset:1024 sc1\n" \
"v_cvt_pk_bf16_f32 v146, v148, v149\n" \
"v_cvt_pk_bf16_f32 v147, v150, v151\n" \
"buffer_store_dwordx4 v[144:147], v20, s[56:59], s68 offen offset:2048 sc1\n" \
"v_cvt_pk_bf16_f32 v154, v156, v157\n" \
"v_cvt_pk_bf16_f32 v155, v158, v159\n" \
"s_waitcnt vmcnt(17) lgkmcnt(0)\n" \
"s_barrier\n" \
"buffer_store_dwordx4 v[152:155], v20, s[56:59], s68 offen offset:3072 sc1\n" \
"v_mfma_f32_16x16x32_bf16 v[88:91], a[128:131], a[48:51], v[44:47]\n" \
"s_xor_b32 m0, s65, 0x12000\n" \
"ds_read_b128 a[160:163], v49 offset:0\n" \
"v_mfma_f32_16x16x32_bf16 v[88:91], a[132:135], a[52:55], v[88:91]\n" \
"buffer_load_dwordx4 v50, s[8:11], s64 offen offset:0 sc1 lds\n" \
"v_mfma_f32_16x16x32_bf16 v[88:91], a[136:139], a[56:59], v[88:91]\n" \
"ds_read_b128 a[164:167], v49 offset:1024\n" \
"ds_read_b128 a[168:171], v49 offset:2048\n" \
"v_mfma_f32_16x16x32_bf16 v[88:91], a[140:143], a[60:63], v[88:91]\n" \
"buffer_load_dwordx4 v50, s[8:11], s64 offen offset:1024 sc1 lds\n" \
"v_mfma_f32_16x16x32_bf16 v[92:95], a[156:159], a[60:63], v[44:47]\n" \
"ds_read_b128 a[172:175], v49 offset:3072\n" \
"ds_read_b128 a[176:179], v49 offset:8192\n" \
"v_mfma_f32_16x16x32_bf16 v[92:95], a[152:155], a[56:59], v[92:95]\n" \
"ds_read_b128 a[180:183], v49 offset:9216\n" \
"ds_read_b128 a[184:187], v49 offset:10240\n" \
"v_mfma_f32_16x16x32_bf16 v[92:95], a[148:151], a[52:55], v[92:95]\n" \
"ds_read_b128 a[188:191], v49 offset:11264\n" \
"ds_read_b128 a[0:3], v49 offset:4096\n" \
"v_mfma_f32_16x16x32_bf16 v[92:95], a[144:147], a[48:51], v[92:95]\n" \
"buffer_load_dwordx4 v50, s[8:11], s64 offen offset:2048 sc1 lds\n" \
"v_mfma_f32_16x16x32_bf16 v[32:35], a[220:223], a[64:67], 0\n" \
"ds_read_b128 a[4:7], v49 offset:5120\n" \
"ds_read_b128 a[8:11], v49 offset:6144\n" \
"v_mfma_f32_16x16x32_bf16 v[36:39], a[220:223], a[80:83], 0\n" \
"ds_read_b128 a[12:15], v49 offset:7168\n" \
"ds_read_b128 a[16:19], v49 offset:12288\n" \
"v_mfma_f32_16x16x32_bf16 v[40:43], a[220:223], a[96:99], 0\n" \
"ds_read_b128 a[20:23], v49 offset:13312\n" \
"ds_read_b128 a[24:27], v49 offset:14336\n" \
"v_mfma_f32_16x16x32_bf16 v[44:47], a[220:223], a[112:115], 0\n" \
"buffer_load_dwordx4 v50, s[8:11], s64 offen offset:3072 sc1 lds\n" \
"v_mfma_f32_16x16x32_bf16 v[32:35], a[224:227], a[68:71], v[32:35]\n" \
"s_xor_b32 m0, s65, 0xa000\n" \
"ds_read_b128 a[28:31], v49 offset:15360\n" \
"v_mfma_f32_16x16x32_bf16 v[36:39], a[224:227], a[84:87], v[36:39]\n" \
"v_exp_f32_e32 v88, v88\n" \
"v_mfma_f32_16x16x32_bf16 v[40:43], a[224:227], a[100:103], v[40:43]\n" \
"buffer_load_dwordx4 v51, s[8:11], s64 offen offset:0 sc1 lds\n" \
"v_mfma_f32_16x16x32_bf16 v[44:47], a[224:227], a[116:119], v[44:47]\n" \
"v_exp_f32_e32 v89, v89\n" \
"v_mfma_f32_16x16x32_bf16 v[32:35], a[228:231], a[72:75], v[32:35]\n" \
"v_exp_f32_e32 v90, v90\n" \
"v_mfma_f32_16x16x32_bf16 v[36:39], a[228:231], a[88:91], v[36:39]\n" \
"v_exp_f32_e32 v91, v91\n" \
"v_mfma_f32_16x16x32_bf16 v[40:43], a[228:231], a[104:107], v[40:43]\n" \
"buffer_load_dwordx4 v51, s[8:11], s64 offen offset:1024 sc1 lds\n" \
"v_mfma_f32_16x16x32_bf16 v[44:47], a[228:231], a[120:123], v[44:47]\n" \
"v_exp_f32_e32 v92, v92\n" \
"v_mfma_f32_16x16x32_bf16 v[32:35], a[232:235], a[76:79], v[32:35]\n" \
"v_exp_f32_e32 v93, v93\n" \
"v_mfma_f32_16x16x32_bf16 v[36:39], a[232:235], a[92:95], v[36:39]\n" \
"v_exp_f32_e32 v94, v94\n" \
"v_mfma_f32_16x16x32_bf16 v[40:43], a[232:235], a[108:111], v[40:43]\n" \
"buffer_load_dwordx4 v51, s[8:11], s64 offen offset:2048 sc1 lds\n" \
"v_mfma_f32_16x16x32_bf16 v[44:47], a[232:235], a[124:127], v[44:47]\n" \
"v_cvt_pk_bf16_f32 v124, v88, v89\n" \
"s_waitcnt lgkmcnt(3)\n" \
"v_mfma_f32_16x16x32_bf16 v[192:195], a[160:163], v[104:107], v[192:195]\n" \
"v_exp_f32_e32 v95, v95\n" \
"v_mfma_f32_16x16x32_bf16 v[196:199], a[164:167], v[104:107], v[196:199]\n" \
"buffer_load_dwordx4 v51, s[8:11], s64 offen offset:3072 sc1 lds\n" \
"v_mfma_f32_16x16x32_bf16 v[200:203], a[168:171], v[104:107], v[200:203]\n" \
"s_add_u32 m0, s93, 0x1000\n" \
"v_cvt_pk_bf16_f32 v125, v90, v91\n" \
"v_mfma_f32_16x16x32_bf16 v[204:207], a[172:175], v[104:107], v[204:207]\n" \
"v_cvt_pk_bf16_f32 v126, v92, v93\n" \
"v_cvt_pk_bf16_f32 v127, v94, v95\n" \
"v_mfma_f32_16x16x32_bf16 v[208:211], a[176:179], v[104:107], v[208:211]\n" \
"v_and_b32_e32 v124, v12, v124\n" \
"v_and_b32_e32 v125, v13, v125\n" \
"v_mfma_f32_16x16x32_bf16 v[212:215], a[180:183], v[104:107], v[212:215]\n" \
"buffer_load_dwordx4 v4, s[80:83], s92 offen offset:0 lds\n" \
"v_mfma_f32_16x16x32_bf16 v[216:219], a[184:187], v[104:107], v[216:219]\n" \
"v_and_b32_e32 v126, v14, v126\n" \
"v_and_b32_e32 v127, v15, v127\n" \
"v_mfma_f32_16x16x32_bf16 v[220:223], a[188:191], v[104:107], v[220:223]\n" \
"ds_read_b128 a[128:131], v48 offset:0\n" \
"ds_read_b128 a[132:135], v48 offset:1024\n" \
"v_mfma_f32_16x16x32_bf16 a[200:203], a[212:215], v[104:107], a[200:203]\n" \
"ds_read_b128 a[136:139], v48 offset:2048\n" \
"ds_read_b128 a[140:143], v48 offset:3072\n" \
"v_mfma_f32_16x16x32_bf16 v[224:227], a[160:163], v[108:111], v[224:227]\n" \
"buffer_load_dwordx4 v5, s[80:83], s92 offen offset:1024 lds\n" \
"v_mfma_f32_16x16x32_bf16 v[228:231], a[164:167], v[108:111], v[228:231]\n" \
"ds_read_b128 a[144:147], v48 offset:4096\n" \
"ds_read_b128 a[148:151], v48 offset:5120\n" \
"v_mfma_f32_16x16x32_bf16 v[232:235], a[168:171], v[108:111], v[232:235]\n" \
"ds_read_b128 a[152:155], v48 offset:6144\n" \
"ds_read_b128 a[156:159], v48 offset:7168\n" \
"v_mfma_f32_16x16x32_bf16 v[236:239], a[172:175], v[108:111], v[236:239]\n" \
"buffer_load_dwordx4 v6, s[80:83], s92 offen offset:2048 lds\n" \
"v_mfma_f32_16x16x32_bf16 v[240:243], a[176:179], v[108:111], v[240:243]\n" \
"v_cvt_pk_bf16_f32 v178, v180, v181\n" \
"v_cvt_pk_bf16_f32 v179, v182, v183\n" \
"buffer_store_dwordx4 v[176:179], v20, s[56:59], s69 offen offset:2048 sc1\n" \
"v_mfma_f32_16x16x32_bf16 v[244:247], a[180:183], v[108:111], v[244:247]\n" \
"v_cvt_pk_bf16_f32 v186, v188, v189\n" \
"v_cvt_pk_bf16_f32 v187, v190, v191\n" \
"buffer_store_dwordx4 v[184:187], v20, s[56:59], s69 offen offset:3072 sc1\n" \
"v_mfma_f32_16x16x32_bf16 v[248:251], a[184:187], v[108:111], v[248:251]\n" \
"v_mfma_f32_16x16x32_bf16 a[208:211], a[216:219], v[128:131], a[208:211]\n" \
"v_mfma_f32_16x16x32_bf16 a[208:211], a[216:219], v[136:139], a[208:211]\n" \
"v_mfma_f32_32x32x16_bf16 v[128:143], a[216:219], a[216:219], 0\n" \
"v_mfma_f32_16x16x32_bf16 a[208:211], a[216:219], v[160:163], a[208:211]\n" \
"v_mfma_f32_16x16x32_bf16 a[208:211], a[216:219], v[168:171], a[208:211]\n" \
"v_mfma_f32_32x32x16_bf16 v[160:175], a[216:219], a[216:219], 0\n" \
"v_accvgpr_read_b32 v56, a200\n" \
"v_mfma_f32_16x16x32_bf16 a[208:211], a[216:219], v[144:147], a[208:211]\n" \
"v_max_f32_e32 v62, v62, v56\n" \
"v_add_u32_e32 v49, 0x10000, v48\n" \
"v_mfma_f32_16x16x32_bf16 v[252:255], a[188:191], v[108:111], v[252:255]\n" \
"buffer_load_dwordx4 v7, s[80:83], s92 offen offset:3072 lds\n" \
"v_mfma_f32_16x16x32_bf16 a[204:207], a[212:215], v[108:111], a[204:207]\n" \
"v_rcp_f32_e32 v56, v56\n" \
"v_mfma_f32_16x16x32_bf16 v[224:227], a[0:3], v[124:127], v[224:227]\n" \
"s_add_u32 m0, s93, 0x2000\n" \
"v_mul_f32_e32 v56, v56, v60\n" \
"v_mfma_f32_16x16x32_bf16 v[228:231], a[4:7], v[124:127], v[228:231]\n" \
"v_mfma_f32_16x16x32_bf16 a[208:211], a[216:219], v[152:155], a[208:211]\n" \
"v_mfma_f32_32x32x16_bf16 v[144:159], a[216:219], a[216:219], 0\n" \
"v_mfma_f32_16x16x32_bf16 a[208:211], a[216:219], v[176:179], a[208:211]\n" \
"v_mul_f32_e32 v192, v56, v192\n" \
"v_mul_f32_e32 v193, v56, v193\n" \
"v_mfma_f32_16x16x32_bf16 v[232:235], a[8:11], v[124:127], v[232:235]\n" \
"buffer_load_dwordx4 v8, s[80:83], s92 offen offset:0 lds\n" \
"v_mfma_f32_16x16x32_bf16 v[236:239], a[12:15], v[124:127], v[236:239]\n" \
"v_cvt_pk_bf16_f32 v192, v192, v193\n" \
"v_mul_f32_e32 v194, v56, v194\n" \
"v_mfma_f32_16x16x32_bf16 v[240:243], a[16:19], v[124:127], v[240:243]\n" \
"v_mul_f32_e32 v195, v56, v195\n" \
"s_waitcnt lgkmcnt(0)\n" \
"v_mfma_f32_16x16x32_bf16 v[244:247], a[20:23], v[124:127], v[244:247]\n" \
"v_mfma_f32_16x16x32_bf16 a[208:211], a[216:219], v[184:187], a[208:211]\n" \
"v_mfma_f32_32x32x16_bf16 v[176:191], a[216:219], a[216:219], 0\n" \
"v_cvt_pk_bf16_f32 v193, v194, v195\n" \
"v_mul_f32_e32 v196, v56, v196\n" \
"v_mul_f32_e32 v197, v56, v197\n" \
"v_mul_f32_e32 v198, v56, v198\n" \
"v_mul_f32_e32 v199, v56, v199\n" \
"v_mul_f32_e32 v200, v56, v200\n" \
"v_mfma_f32_16x16x32_bf16 v[248:251], a[24:27], v[124:127], v[248:251]\n" \
"v_mul_f32_e32 v201, v56, v201\n" \
"v_cvt_pk_bf16_f32 v194, v196, v197\n" \
"v_mfma_f32_16x16x32_bf16 v[252:255], a[28:31], v[124:127], v[252:255]\n" \
"v_cvt_pk_bf16_f32 v195, v198, v199\n" \
"v_cvt_pk_bf16_f32 v200, v200, v201\n" \
"buffer_store_dwordx4 v[192:195], v20, s[56:59], s70 offen offset:0 sc1\n" \
"v_mfma_f32_16x16x32_bf16 a[204:207], a[212:215], v[124:127], a[204:207]\n" \
"v_mul_f32_e32 v202, v56, v202\n" \
"v_mul_f32_e32 v203, v56, v203\n" \
"s_waitcnt vmcnt(17)\n" \
"s_barrier\n" \
"v_mfma_f32_16x16x32_bf16 v[64:67], a[128:131], a[64:67], v[32:35]\n" \
"s_xor_b32 m0, s65, 0x18000\n" \
"v_cvt_pk_bf16_f32 v201, v202, v203\n" \
"v_mfma_f32_16x16x32_bf16 v[64:67], a[132:135], a[68:71], v[64:67]\n" \
"buffer_load_dwordx4 v52, s[8:11], s64 offen offset:0 sc1 lds\n" \
"v_mfma_f32_16x16x32_bf16 v[64:67], a[136:139], a[72:75], v[64:67]\n" \
"v_accvgpr_read_b32 v57, a204\n" \
"v_mul_f32_e32 v204, v56, v204\n" \
"v_mfma_f32_32x32x16_bf16 a[192:207], a[216:219], a[216:219], 0\n" \
"v_mfma_f32_16x16x32_bf16 a[208:211], a[216:219], v[192:195], a[208:211]\n" \
"v_mul_f32_e32 v205, v56, v205\n" \
"v_mul_f32_e32 v206, v56, v206\n" \
"v_mfma_f32_16x16x32_bf16 v[64:67], a[140:143], a[76:79], v[64:67]\n" \
"buffer_load_dwordx4 v52, s[8:11], s64 offen offset:1024 sc1 lds\n" \
"v_mfma_f32_16x16x32_bf16 v[68:71], a[156:159], a[76:79], v[32:35]\n" \
"v_mul_f32_e32 v207, v56, v207\n" \
"v_mul_f32_e32 v208, v56, v208\n" \
"v_mfma_f32_16x16x32_bf16 v[68:71], a[152:155], a[72:75], v[68:71]\n" \
"v_mul_f32_e32 v209, v56, v209\n" \
"v_max_f32_e32 v62, v62, v57\n" \
"v_mfma_f32_16x16x32_bf16 v[68:71], a[148:151], a[68:71], v[68:71]\n" \
"v_cvt_pk_bf16_f32 v202, v204, v205\n" \
"v_cvt_pk_bf16_f32 v203, v206, v207\n" \
"buffer_store_dwordx4 v[200:203], v20, s[56:59], s70 offen offset:1024 sc1\n" \
"v_mfma_f32_16x16x32_bf16 v[68:71], a[144:147], a[64:67], v[68:71]\n" \
"buffer_load_dwordx4 v52, s[8:11], s64 offen offset:2048 sc1 lds\n" \
"v_mfma_f32_16x16x32_bf16 v[72:75], a[128:131], a[80:83], v[36:39]\n" \
"v_cvt_pk_bf16_f32 v208, v208, v209\n" \
"v_mul_f32_e32 v210, v56, v210\n" \
"v_mfma_f32_16x16x32_bf16 v[72:75], a[132:135], a[84:87], v[72:75]\n" \
"v_mul_f32_e32 v211, v56, v211\n" \
"v_cvt_pk_bf16_f32 v209, v210, v211\n" \
"v_mfma_f32_16x16x32_bf16 v[72:75], a[136:139], a[88:91], v[72:75]\n" \
"v_rcp_f32_e32 v57, v57\n" \
"v_mfma_f32_16x16x32_bf16 v[72:75], a[140:143], a[92:95], v[72:75]\n" \
"buffer_load_dwordx4 v52, s[8:11], s64 offen offset:3072 sc1 lds\n" \
"v_mfma_f32_16x16x32_bf16 v[76:79], a[156:159], a[92:95], v[36:39]\n" \
"s_mov_b32 m0, s65\n" \
"v_mul_f32_e32 v212, v56, v212\n" \
"v_mfma_f32_16x16x32_bf16 v[76:79], a[152:155], a[88:91], v[76:79]\n" \
"v_mfma_f32_16x16x32_bf16 a[208:211], a[216:219], v[200:203], a[208:211]\n" \
"v_mfma_f32_32x32x16_bf16 v[192:207], a[216:219], a[216:219], 0\n" \
"v_mul_f32_e32 v213, v56, v213\n" \
"v_mul_f32_e32 v214, v56, v214\n" \
"v_mul_f32_e32 v215, v56, v215\n" \
"v_mul_f32_e32 v216, v56, v216\n" \
"v_mul_f32_e32 v217, v56, v217\n" \
"v_cvt_pk_bf16_f32 v210, v212, v213\n" \
"v_mfma_f32_16x16x32_bf16 v[76:79], a[148:151], a[84:87], v[76:79]\n" \
"v_cvt_pk_bf16_f32 v211, v214, v215\n" \
"v_cvt_pk_bf16_f32 v216, v216, v217\n" \
"buffer_store_dwordx4 v[208:211], v20, s[56:59], s70 offen offset:2048 sc1\n" \
"v_mfma_f32_16x16x32_bf16 v[76:79], a[144:147], a[80:83], v[76:79]\n" \
"buffer_load_dwordx4 v53, s[8:11], s64 offen offset:0 sc1 lds\n" \
"v_mfma_f32_16x16x32_bf16 v[80:83], a[128:131], a[96:99], v[40:43]\n" \
"v_mul_f32_e32 v218, v56, v218\n" \
"v_mul_f32_e32 v219, v56, v219\n" \
"v_mfma_f32_16x16x32_bf16 v[80:83], a[132:135], a[100:103], v[80:83]\n" \
"v_mul_f32_e32 v57, v57, v61\n" \
"v_cvt_pk_bf16_f32 v217, v218, v219\n" \
"v_mfma_f32_16x16x32_bf16 v[80:83], a[136:139], a[104:107], v[80:83]\n" \
"buffer_load_dwordx4 v53, s[8:11], s64 offen offset:1024 sc1 lds\n" \
"v_mfma_f32_16x16x32_bf16 v[80:83], a[140:143], a[108:111], v[80:83]\n" \
"v_mfma_f32_16x16x32_bf16 a[208:211], a[216:219], v[208:211], a[208:211]\n" \
"v_mul_f32_e32 v220, v56, v220\n" \
"v_mul_f32_e32 v221, v56, v221\n" \
"v_mfma_f32_16x16x32_bf16 v[84:87], a[156:159], a[108:111], v[40:43]\n" \
"v_mul_f32_e32 v222, v56, v222\n" \
"v_mul_f32_e32 v223, v56, v223\n" \
"v_mfma_f32_16x16x32_bf16 v[84:87], a[152:155], a[104:107], v[84:87]\n" \
"v_mul_f32_e32 v224, v57, v224\n" \
"v_mul_f32_e32 v225, v57, v225\n" \
"v_mfma_f32_16x16x32_bf16 v[84:87], a[148:151], a[100:103], v[84:87]\n" \
"buffer_load_dwordx4 v53, s[8:11], s64 offen offset:2048 sc1 lds\n" \
"v_mfma_f32_16x16x32_bf16 v[84:87], a[144:147], a[96:99], v[84:87]\n" \
"v_cvt_pk_bf16_f32 v218, v220, v221\n" \
"v_cvt_pk_bf16_f32 v219, v222, v223\n" \
"buffer_store_dwordx4 v[216:219], v20, s[56:59], s70 offen offset:3072 sc1\n" \
"v_mfma_f32_16x16x32_bf16 v[88:91], a[128:131], a[112:115], v[44:47]\n" \
"ds_read_b128 a[128:131], v48 offset:8192\n" \
"v_cvt_pk_bf16_f32 v224, v224, v225\n" \
"v_mfma_f32_16x16x32_bf16 v[88:91], a[132:135], a[116:119], v[88:91]\n" \
"ds_read_b128 a[132:135], v48 offset:9216\n" \
"v_mul_f32_e32 v226, v57, v226\n" \
"v_mfma_f32_16x16x32_bf16 v[88:91], a[136:139], a[120:123], v[88:91]\n" \
"ds_read_b128 a[136:139], v48 offset:10240\n" \
"v_mul_f32_e32 v227, v57, v227\n" \
"v_mfma_f32_16x16x32_bf16 v[88:91], a[140:143], a[124:127], v[88:91]\n" \
"ds_read_b128 a[140:143], v48 offset:11264\n" \
"v_cvt_pk_bf16_f32 v225, v226, v227\n" \
"v_mfma_f32_16x16x32_bf16 v[92:95], a[156:159], a[124:127], v[44:47]\n" \
"ds_read_b128 a[156:159], v48 offset:15360\n" \
"v_mul_f32_e32 v228, v57, v228\n" \
"v_mfma_f32_16x16x32_bf16 v[92:95], a[152:155], a[120:123], v[92:95]\n" \
"ds_read_b128 a[152:155], v48 offset:14336\n" \
"v_mul_f32_e32 v229, v57, v229\n" \
"v_mfma_f32_16x16x32_bf16 v[92:95], a[148:151], a[116:119], v[92:95]\n" \
"ds_read_b128 a[148:151], v48 offset:13312\n" \
"v_mul_f32_e32 v230, v57, v230\n" \
"v_mfma_f32_16x16x32_bf16 v[92:95], a[144:147], a[112:115], v[92:95]\n" \
"ds_read_b128 a[144:147], v48 offset:12288\n" \
"v_mfma_f32_16x16x32_bf16 a[208:211], a[216:219], v[216:219], a[208:211]\n" \
"v_mfma_f32_32x32x16_bf16 v[208:223], a[216:219], a[216:219], 0\n" \
"v_mul_f32_e32 v231, v57, v231\n" \
"v_mul_f32_e32 v232, v57, v232\n" \
"v_mul_f32_e32 v233, v57, v233\n" \
"v_cvt_pk_bf16_f32 v226, v228, v229\n" \
"v_cvt_pk_bf16_f32 v227, v230, v231\n" \
"buffer_store_dwordx4 v[224:227], v20, s[56:59], s71 offen offset:0 sc1\n" \
"v_cvt_pk_bf16_f32 v232, v232, v233\n" \
"v_mul_f32_e32 v234, v57, v234\n" \
"v_mul_f32_e32 v235, v57, v235\n" \
"v_mfma_f32_16x16x32_bf16 a[208:211], a[216:219], v[224:227], a[208:211]\n" \
"v_cvt_pk_bf16_f32 v233, v234, v235\n" \
"v_mul_f32_e32 v236, v57, v236\n" \
"v_mul_f32_e32 v237, v57, v237\n" \
"v_mul_f32_e32 v238, v57, v238\n" \
"v_mul_f32_e32 v239, v57, v239\n" \
"v_mul_f32_e32 v240, v57, v240\n" \
"v_mul_f32_e32 v241, v57, v241\n" \
"v_cvt_pk_bf16_f32 v234, v236, v237\n" \
"v_cvt_pk_bf16_f32 v235, v238, v239\n" \
"buffer_store_dwordx4 v[232:235], v20, s[56:59], s71 offen offset:1024 sc1\n" \
"v_cvt_pk_bf16_f32 v240, v240, v241\n" \
"v_mul_f32_e32 v242, v57, v242\n" \
"v_mul_f32_e32 v243, v57, v243\n" \
"v_mfma_f32_16x16x32_bf16 a[208:211], a[216:219], v[232:235], a[208:211]\n" \
"v_mfma_f32_32x32x16_bf16 v[224:239], a[216:219], a[216:219], 0\n" \
"v_cvt_pk_bf16_f32 v241, v242, v243\n" \
"v_mul_f32_e32 v244, v57, v244\n" \
"v_mul_f32_e32 v245, v57, v245\n" \
"v_mul_f32_e32 v246, v57, v246\n" \
"v_mul_f32_e32 v247, v57, v247\n" \
"v_mul_f32_e32 v248, v57, v248\n" \
"v_mul_f32_e32 v249, v57, v249\n" \
"v_cvt_pk_bf16_f32 v242, v244, v245\n" \
"v_cvt_pk_bf16_f32 v243, v246, v247\n" \
"buffer_store_dwordx4 v[240:243], v20, s[56:59], s71 offen offset:2048 sc1\n" \
"v_cvt_pk_bf16_f32 v248, v248, v249\n" \
"v_mul_f32_e32 v250, v57, v250\n" \
"v_mul_f32_e32 v251, v57, v251\n" \
"v_mfma_f32_16x16x32_bf16 a[208:211], a[216:219], v[240:243], a[208:211]\n" \
"v_cvt_pk_bf16_f32 v249, v250, v251\n" \
"v_mul_f32_e32 v252, v57, v252\n" \
"v_mul_f32_e32 v253, v57, v253\n" \
"v_mul_f32_e32 v254, v57, v254\n" \
"v_mul_f32_e32 v255, v57, v255\n" \
"v_cvt_pk_bf16_f32 v250, v252, v253\n" \
"v_cvt_pk_bf16_f32 v251, v254, v255\n" \
"buffer_store_dwordx4 v[248:251], v20, s[56:59], s71 offen offset:3072 sc1\n" \
"buffer_load_dwordx4 v53, s[8:11], s64 offen offset:3072 sc1 lds\n" \
"v_mfma_f32_16x16x32_bf16 a[208:211], a[216:219], v[248:251], a[208:211]\n" \
"v_mfma_f32_32x32x16_bf16 v[240:255], a[216:219], a[216:219], 0\n" \
"v_cmp_lt_f32_e64 s[72:73], s100, v62\n" \
"v_exp_f32_e32 v64, v64\n" \
"v_exp_f32_e32 v65, v65\n" \
"v_exp_f32_e32 v66, v66\n" \
"v_exp_f32_e32 v67, v67\n" \
"v_exp_f32_e32 v68, v68\n" \
"v_exp_f32_e32 v69, v69\n" \
"v_accvgpr_read_b32 v63, a208\n" \
"v_cmp_u_f32_e32 vcc, v63, v63\n" \
"v_exp_f32_e32 v70, v70\n" \
"s_or_b64 vcc, s[72:73], vcc\n" \
"s_cselect_b32 s95, 0x40000000, s95\n" \
"s_cselect_b32 s94, 0x7f800000, 0\n" \
"v_exp_f32_e32 v71, v71\n" \
"v_exp_f32_e32 v72, v72\n" \
"v_exp_f32_e32 v73, v73\n" \
"v_exp_f32_e32 v74, v74\n" \
"v_exp_f32_e32 v75, v75\n" \
"v_exp_f32_e32 v76, v76\n" \
"v_exp_f32_e32 v77, v77\n" \
"v_exp_f32_e32 v78, v78\n" \
"v_exp_f32_e32 v79, v79\n" \
"s_xor_b32 s65, s65, 0x8000\n" \
"s_waitcnt vmcnt(15) lgkmcnt(0)\n" \
"s_barrier\n" \
"v_exp_f32_e32 v80, v80\n" \
"v_exp_f32_e32 v81, v81\n" \
"v_exp_f32_e32 v82, v82\n" \
"v_exp_f32_e32 v83, v83\n" \
"v_exp_f32_e32 v84, v84\n" \
"v_exp_f32_e32 v85, v85\n" \
"v_exp_f32_e32 v86, v86\n" \
"v_exp_f32_e32 v87, v87\n" \
"s_add_u32 s64, s64, 0x10000\n" \
"v_exp_f32_e32 v88, v88\n" \
"v_exp_f32_e32 v89, v89\n" \
"v_exp_f32_e32 v90, v90\n" \
"v_exp_f32_e32 v91, v91\n" \
"v_exp_f32_e32 v92, v92\n" \
"v_exp_f32_e32 v93, v93\n" \
"v_exp_f32_e32 v94, v94\n" \
"v_exp_f32_e32 v95, v95\n" \
"v_cvt_pk_bf16_f32 v96, v64, v65\n" \
"v_cvt_pk_bf16_f32 v97, v66, v67\n" \
"v_cvt_pk_bf16_f32 v98, v68, v69\n" \
"v_cvt_pk_bf16_f32 v99, v70, v71\n" \
"v_cvt_pk_bf16_f32 v100, v72, v73\n" \
"v_cvt_pk_bf16_f32 v101, v74, v75\n" \
"v_cvt_pk_bf16_f32 v102, v76, v77\n" \
"v_cvt_pk_bf16_f32 v103, v78, v79\n" \
"v_cvt_pk_bf16_f32 v104, v80, v81\n" \
"v_cvt_pk_bf16_f32 v105, v82, v83\n" \
"v_cvt_pk_bf16_f32 v106, v84, v85\n" \
"v_cvt_pk_bf16_f32 v107, v86, v87\n" \
"v_xor_b32_e32 v48, 0x8000, v48\n" \
"s_sub_u32 s66, s19, 1\n" \
"s_cbranch_scc0 L_2_%=\n" \
"L_f1_%=:\n" \
"L_pf17:\n" \
"s_cbranch_execz L_retA\n" \
"s_nop 0\n" \
"ds_read_b128 a[160:163], v49 offset:0\n" \
"ds_read_b128 a[164:167], v49 offset:1024\n" \
"ds_read_b128 a[168:171], v49 offset:2048\n" \
"ds_read_b128 a[172:175], v49 offset:3072\n" \
"ds_read_b128 a[176:179], v49 offset:8192\n" \
"ds_read_b128 a[180:183], v49 offset:9216\n" \
"v_mfma_f32_16x16x32_bf16 v[72:75], a[128:131], a[80:83], v[36:39]\n" \
"s_add_u32 s68, s61, 0x0\n" \
"s_add_u32 s69, s61, 0x49000\n" \
"ds_read_b128 a[184:187], v49 offset:10240\n" \
"v_mfma_f32_16x16x32_bf16 v[72:75], a[132:135], a[84:87], v[72:75]\n" \
"ds_read_b128 a[188:191], v49 offset:11264\n" \
"v_add_u32_e32 v56, 0x49000, v20\n" \
"v_mfma_f32_16x16x32_bf16 v[72:75], a[136:139], a[88:91], v[72:75]\n" \
"v_readlane_b32 s97, v11, 32\n" \
"v_readlane_b32 s98, v11, 33\n" \
"v_mfma_f32_16x16x32_bf16 v[72:75], a[140:143], a[92:95], v[72:75]\n" \
"v_and_b32_e32 v96, v12, v96\n" \
"v_and_b32_e32 v97, v13, v97\n" \
"v_mfma_f32_16x16x32_bf16 v[76:79], a[156:159], a[92:95], v[36:39]\n" \
"buffer_load_dwordx4 a[32:35], v20, s[56:59], s97 offen offset:0 sc1\n" \
"v_mfma_f32_16x16x32_bf16 v[76:79], a[152:155], a[88:91], v[76:79]\n" \
"v_and_b32_e32 v98, v14, v98\n" \
"v_and_b32_e32 v99, v15, v99\n" \
"v_mfma_f32_16x16x32_bf16 v[76:79], a[148:151], a[84:87], v[76:79]\n" \
"v_mov_b32_e32 v61, s62\n" \
"ds_read_b128 a[0:3], v20 offset:8192\n" \
"buffer_load_dwordx4 a[40:43], v56, s[56:59], s97 offen offset:0 sc1\n" \
"v_mfma_f32_16x16x32_bf16 v[76:79], a[144:147], a[80:83], v[76:79]\n" \
"ds_read_b128 a[4:7], v20 offset:9216\n" \
"ds_read_b128 a[8:11], v20 offset:10240\n" \
"s_waitcnt lgkmcnt(10)\n" \
"v_mfma_f32_16x16x32_bf16 v[128:131], a[160:163], v[96:99], v[128:131]\n" \
"ds_read_b128 a[12:15], v20 offset:11264\n" \
"buffer_load_dwordx4 a[48:51], v20, s[56:59], s98 offen offset:0 sc1\n" \
"s_waitcnt lgkmcnt(10)\n" \
"v_mfma_f32_16x16x32_bf16 v[132:135], a[164:167], v[96:99], v[132:135]\n" \
"ds_read_b128 a[28:31], v20 offset:15360\n" \
"s_waitcnt lgkmcnt(10)\n" \
"v_mfma_f32_16x16x32_bf16 v[136:139], a[168:171], v[96:99], v[136:139]\n" \
"ds_read_b128 a[24:27], v20 offset:14336\n" \
"buffer_load_dwordx4 a[56:59], v56, s[56:59], s98 offen offset:0 sc1\n" \
"s_waitcnt lgkmcnt(10)\n" \
"v_mfma_f32_16x16x32_bf16 v[140:143], a[172:175], v[96:99], v[140:143]\n" \
"ds_read_b128 a[20:23], v20 offset:13312\n" \
"s_waitcnt lgkmcnt(9)\n" \
"v_mfma_f32_16x16x32_bf16 v[144:147], a[176:179], v[96:99], v[144:147]\n" \
"ds_read_b128 a[16:19], v20 offset:12288\n" \
"v_add_u32_e32 v50, s15, v48\n" \
"buffer_load_dwordx4 a[36:39], v20, s[56:59], s97 offen offset:1024 sc1\n" \
"v_mfma_f32_16x16x32_bf16 v[148:151], a[180:183], v[96:99], v[148:151]\n" \
"v_xor_b32_e32 v48, 0x4000, v49\n" \
"s_waitcnt lgkmcnt(8)\n" \
"v_mfma_f32_16x16x32_bf16 v[152:155], a[184:187], v[96:99], v[152:155]\n" \
"ds_read_b32 v58, v18 offset:256\n" \
"ds_read_b32 v59, v18 offset:384\n" \
"buffer_load_dwordx4 a[44:47], v56, s[56:59], s97 offen offset:1024 sc1\n" \
"v_mfma_f32_16x16x32_bf16 v[156:159], a[188:191], v[96:99], v[156:159]\n" \
"ds_read_b128 a[96:99], v48 offset:4096\n" \
"ds_read_b128 a[100:103], v48 offset:5120\n" \
"v_mfma_f32_16x16x32_bf16 a[192:195], a[212:215], v[96:99], a[192:195]\n" \
"ds_read_b128 a[104:107], v48 offset:6144\n" \
"ds_read_b128 a[108:111], v48 offset:7168\n" \
"buffer_load_dwordx4 a[52:55], v20, s[56:59], s98 offen offset:1024 sc1\n" \
"v_mfma_f32_16x16x32_bf16 v[160:163], a[160:163], v[100:103], v[160:163]\n" \
"ds_read_b128 a[112:115], v48 offset:12288\n" \
"ds_read_b128 a[116:119], v48 offset:13312\n" \
"v_mfma_f32_16x16x32_bf16 v[164:167], a[164:167], v[100:103], v[164:167]\n" \
"ds_read_b128 a[120:123], v48 offset:14336\n" \
"ds_read_b128 a[124:127], v48 offset:15360\n" \
"buffer_load_dwordx4 a[60:63], v56, s[56:59], s98 offen offset:1024 sc1\n" \
"v_mfma_f32_16x16x32_bf16 v[168:171], a[168:171], v[100:103], v[168:171]\n" \
"ds_read_b128 a[128:131], v49 offset:4096\n" \
"ds_read_b128 a[132:135], v49 offset:5120\n" \
"v_mfma_f32_16x16x32_bf16 v[172:175], a[172:175], v[100:103], v[172:175]\n" \
"ds_read_b128 a[136:139], v49 offset:6144\n" \
"ds_read_b128 a[140:143], v49 offset:7168\n" \
"s_waitcnt vmcnt(8)\n" \
"s_mov_b64 exec, 1\n" \
"buffer_store_dword v61, off, s[56:59], s95 offset:0 sc1\n" \
"s_mov_b64 exec, -1\n" \
"v_mfma_f32_16x16x32_bf16 v[176:179], a[176:179], v[100:103], v[176:179]\n" \
"ds_read_b128 a[144:147], v49 offset:12288\n" \
"ds_read_b128 a[148:151], v49 offset:13312\n" \
"v_mfma_f32_16x16x32_bf16 v[180:183], a[180:183], v[100:103], v[180:183]\n" \
"ds_read_b128 a[152:155], v49 offset:14336\n" \
"ds_read_b128 a[156:159], v49 offset:15360\n" \
"buffer_load_dwordx4 a[220:223], v20, s[56:59], s60 offen offset:0 sc1\n" \
"v_mfma_f32_16x16x32_bf16 v[184:187], a[184:187], v[100:103], v[184:187]\n" \
"v_accvgpr_read_b32 v40, a200\n" \
"v_accvgpr_read_b32 v41, a204\n" \
"v_mfma_f32_16x16x32_bf16 v[188:191], a[188:191], v[100:103], v[188:191]\n" \
"v_add_u32_e32 v51, 0x10000, v50\n" \
"v_add_f32_e32 v40, s94, v40\n" \
"buffer_load_dwordx4 a[236:239], v56, s[56:59], s60 offen offset:0 sc1\n" \
"v_mfma_f32_16x16x32_bf16 a[196:199], a[212:215], v[100:103], a[196:199]\n" \
"v_add_f32_e32 v41, s94, v41\n" \
"s_waitcnt lgkmcnt(15)\n" \
"v_mfma_f32_16x16x32_bf16 v[80:83], a[0:3], a[64:67], v[32:35]\n" \
"ds_write_b128 v50, v[192:195] offset:0\n" \
"buffer_load_dwordx4 a[224:227], v20, s[56:59], s60 offen offset:1024 sc1\n" \
"v_mfma_f32_16x16x32_bf16 v[80:83], a[4:7], a[68:71], v[80:83]\n" \
"ds_write_b128 v50, v[196:199] offset:1024\n" \
"v_mfma_f32_16x16x32_bf16 v[80:83], a[8:11], a[72:75], v[80:83]\n" \
"ds_write_b128 v50, v[200:203] offset:2048\n" \
"buffer_load_dwordx4 a[240:243], v56, s[56:59], s60 offen offset:1024 sc1\n" \
"v_mfma_f32_16x16x32_bf16 v[80:83], a[12:15], a[76:79], v[80:83]\n" \
"ds_write_b128 v50, v[204:207] offset:3072\n" \
"v_mfma_f32_16x16x32_bf16 v[84:87], a[28:31], a[76:79], v[32:35]\n" \
"ds_write_b128 v50, v[208:211] offset:8192\n" \
"buffer_load_dwordx4 a[228:231], v20, s[56:59], s60 offen offset:2048 sc1\n" \
"v_mfma_f32_16x16x32_bf16 v[84:87], a[24:27], a[72:75], v[84:87]\n" \
"ds_write_b128 v50, v[212:215] offset:9216\n" \
"v_mfma_f32_16x16x32_bf16 v[84:87], a[20:23], a[68:71], v[84:87]\n" \
"ds_write_b128 v50, v[216:219] offset:10240\n" \
"buffer_load_dwordx4 a[244:247], v56, s[56:59], s60 offen offset:2048 sc1\n" \
"v_mfma_f32_16x16x32_bf16 v[84:87], a[16:19], a[64:67], v[84:87]\n" \
"ds_write_b128 v50, v[220:223] offset:11264\n" \
"v_mfma_f32_16x16x32_bf16 v[88:91], a[0:3], a[80:83], v[36:39]\n" \
"ds_write_b128 v51, v[224:227] offset:0\n" \
"buffer_load_dwordx4 a[232:235], v20, s[56:59], s60 offen offset:3072 sc1\n" \
"v_mfma_f32_16x16x32_bf16 v[88:91], a[4:7], a[84:87], v[88:91]\n" \
"ds_write_b128 v51, v[228:231] offset:1024\n" \
"v_mfma_f32_16x16x32_bf16 v[88:91], a[8:11], a[88:91], v[88:91]\n" \
"ds_write_b128 v51, v[232:235] offset:2048\n" \
"buffer_load_dwordx4 a[248:251], v56, s[56:59], s60 offen offset:3072 sc1\n" \
"v_mfma_f32_16x16x32_bf16 v[88:91], a[12:15], a[92:95], v[88:91]\n" \
"ds_write_b128 v51, v[236:239] offset:3072\n" \
"v_mfma_f32_16x16x32_bf16 v[92:95], a[28:31], a[92:95], v[36:39]\n" \
"ds_write_b128 v51, v[240:243] offset:8192\n" \
"ds_write_b128 v51, v[244:247] offset:9216\n" \
"ds_write_b128 v51, v[248:251] offset:10240\n" \
"ds_write_b128 v51, v[252:255] offset:11264\n" \
"ds_write_b64 v19, v[40:41]\n" \
"v_mfma_f32_16x16x32_bf16 v[92:95], a[24:27], a[88:91], v[92:95]\n" \
"v_xor_b32_e32 v52, 0x4000, v50\n" \
"v_xor_b32_e32 v53, 0x4000, v51\n" \
"v_mfma_f32_16x16x32_bf16 v[92:95], a[20:23], a[84:87], v[92:95]\n" \
"v_exp_f32_e32 v72, v72\n" \
"v_mfma_f32_16x16x32_bf16 v[92:95], a[16:19], a[80:83], v[92:95]\n" \
"L_pf18:\n" \
"s_cbranch_execz L_retA\n" \
"s_nop 0\n" \
"v_exp_f32_e32 v73, v73\n" \
"v_exp_f32_e32 v74, v74\n" \
"v_exp_f32_e32 v75, v75\n" \
"v_exp_f32_e32 v76, v76\n" \
"v_exp_f32_e32 v77, v77\n" \
"v_exp_f32_e32 v78, v78\n" \
"v_exp_f32_e32 v79, v79\n" \
"v_cvt_pk_bf16_f32 v116, v72, v73\n" \
"v_cvt_pk_bf16_f32 v117, v74, v75\n" \
"v_cvt_pk_bf16_f32 v118, v76, v77\n" \
"v_cvt_pk_bf16_f32 v119, v78, v79\n" \
"v_and_b32_e32 v116, v12, v116\n" \
"v_and_b32_e32 v117, v13, v117\n" \
"v_and_b32_e32 v118, v14, v118\n" \
"v_and_b32_e32 v119, v15, v119\n" \
"v_readlane_b32 s93, v11, 18\n" \
"v_readlane_b32 s84, v11, 13\n" \
"v_readlane_b32 s85, v11, 14\n" \
"v_readlane_b32 s86, v11, 15\n" \
"s_mov_b32 s87, 0x20000\n" \
"v_readlane_b32 s92, v11, 16\n" \
"s_add_u32 m0, s93, 0x0\n" \
"v_xor_b32_e32 v63, 0x400, v19\n" \
"s_waitcnt lgkmcnt(0)\n" \
"s_barrier\n" \
"ds_read_b128 v[192:195], v52 offset:0\n" \
"ds_read_b128 v[196:199], v52 offset:1024\n" \
"ds_read_b128 v[200:203], v52 offset:2048\n" \
"ds_read_b128 v[204:207], v52 offset:3072\n" \
"ds_read_b128 v[208:211], v52 offset:8192\n" \
"ds_read_b128 v[212:215], v52 offset:9216\n" \
"ds_read_b128 v[216:219], v52 offset:10240\n" \
"ds_read_b128 v[220:223], v52 offset:11264\n" \
"ds_read_b128 v[224:227], v53 offset:0\n" \
"ds_read_b128 v[228:231], v53 offset:1024\n" \
"ds_read_b128 v[232:235], v53 offset:2048\n" \
"ds_read_b128 v[236:239], v53 offset:3072\n" \
"ds_read_b128 v[240:243], v53 offset:8192\n" \
"ds_read_b128 v[244:247], v53 offset:9216\n" \
"ds_read_b128 v[248:251], v53 offset:10240\n" \
"ds_read_b128 v[252:255], v53 offset:11264\n" \
"v_mfma_f32_16x16x32_bf16 v[160:163], a[128:131], v[116:119], v[160:163]\n" \
"buffer_load_dwordx4 v0, s[84:87], s92 offen offset:0 lds\n" \
"v_mfma_f32_16x16x32_bf16 v[164:167], a[132:135], v[116:119], v[164:167]\n" \
"v_exp_f32_e32 v80, v80\n" \
"v_mfma_f32_16x16x32_bf16 v[168:171], a[136:139], v[116:119], v[168:171]\n" \
"v_exp_f32_e32 v81, v81\n" \
"buffer_load_dwordx4 v1, s[84:87], s92 offen offset:1024 lds\n" \
"v_mfma_f32_16x16x32_bf16 v[172:175], a[140:143], v[116:119], v[172:175]\n" \
"ds_read_b64 v[42:43], v63\n" \
"v_cvt_pk_bf16_f32 v120, v80, v81\n" \
"v_mfma_f32_16x16x32_bf16 v[176:179], a[144:147], v[116:119], v[176:179]\n" \
"v_exp_f32_e32 v82, v82\n" \
"buffer_load_dwordx4 v2, s[84:87], s92 offen offset:2048 lds\n" \
"v_mfma_f32_16x16x32_bf16 v[180:183], a[148:151], v[116:119], v[180:183]\n" \
"v_exp_f32_e32 v83, v83\n" \
"v_mfma_f32_16x16x32_bf16 v[184:187], a[152:155], v[116:119], v[184:187]\n" \
"v_exp_f32_e32 v84, v84\n" \
"buffer_load_dwordx4 v3, s[84:87], s92 offen offset:3072 lds\n" \
"s_add_u32 m0, s93, 0x1000\n" \
"v_mfma_f32_16x16x32_bf16 v[188:191], a[156:159], v[116:119], v[188:191]\n" \
"v_exp_f32_e32 v85, v85\n" \
"v_mfma_f32_16x16x32_bf16 a[196:199], a[212:215], v[116:119], a[196:199]\n" \
"v_exp_f32_e32 v86, v86\n" \
"s_waitcnt lgkmcnt(1)\n" \
"s_barrier\n" \
"buffer_load_dwordx4 v4, s[84:87], s92 offen offset:0 lds\n" \
"v_exp_f32_e32 v87, v87\n" \
"v_cvt_pk_bf16_f32 v121, v82, v83\n" \
"v_cvt_pk_bf16_f32 v122, v84, v85\n" \
"v_cvt_pk_bf16_f32 v123, v86, v87\n" \
"v_exp_f32_e32 v88, v88\n" \
"v_exp_f32_e32 v89, v89\n" \
"buffer_load_dwordx4 v5, s[84:87], s92 offen offset:1024 lds\n" \
"v_mfma_f32_16x16x32_bf16 v[128:131], a[96:99], v[120:123], v[128:131]\n" \
"v_exp_f32_e32 v90, v90\n" \
"v_mfma_f32_16x16x32_bf16 v[132:135], a[100:103], v[120:123], v[132:135]\n" \
"v_exp_f32_e32 v91, v91\n" \
"buffer_load_dwordx4 v6, s[84:87], s92 offen offset:2048 lds\n" \
"v_mfma_f32_16x16x32_bf16 v[136:139], a[104:107], v[120:123], v[136:139]\n" \
"v_exp_f32_e32 v92, v92\n" \
"v_mfma_f32_16x16x32_bf16 v[140:143], a[108:111], v[120:123], v[140:143]\n" \
"v_exp_f32_e32 v93, v93\n" \
"buffer_load_dwordx4 v7, s[84:87], s92 offen offset:3072 lds\n" \
"s_add_u32 m0, s93, 0x2000\n" \
"v_mfma_f32_16x16x32_bf16 v[144:147], a[112:115], v[120:123], v[144:147]\n" \
"v_exp_f32_e32 v94, v94\n" \
"v_mfma_f32_16x16x32_bf16 v[148:151], a[116:119], v[120:123], v[148:151]\n" \
"v_exp_f32_e32 v95, v95\n" \
"buffer_load_dwordx4 v8, s[84:87], s92 offen offset:0 lds\n" \
"v_mfma_f32_16x16x32_bf16 v[152:155], a[120:123], v[120:123], v[152:155]\n" \
"v_cvt_pk_bf16_f32 v124, v88, v89\n" \
"v_cvt_pk_bf16_f32 v125, v90, v91\n" \
"v_mfma_f32_16x16x32_bf16 v[156:159], a[124:127], v[120:123], v[156:159]\n" \
"v_cvt_pk_bf16_f32 v126, v92, v93\n" \
"v_cvt_pk_bf16_f32 v127, v94, v95\n" \
"v_mfma_f32_16x16x32_bf16 a[192:195], a[212:215], v[120:123], a[192:195]\n" \
"v_add_f32_e32 v160, v160, v224\n" \
"v_add_f32_e32 v161, v161, v225\n" \
"v_add_f32_e32 v162, v162, v226\n" \
"v_add_f32_e32 v163, v163, v227\n" \
"v_add_f32_e32 v164, v164, v228\n" \
"v_add_f32_e32 v165, v165, v229\n" \
"v_mfma_f32_16x16x32_bf16 v[160:163], a[96:99], v[124:127], v[160:163]\n" \
"v_add_f32_e32 v166, v166, v230\n" \
"v_add_f32_e32 v167, v167, v231\n" \
"v_add_f32_e32 v168, v168, v232\n" \
"v_add_f32_e32 v169, v169, v233\n" \
"v_mfma_f32_16x16x32_bf16 v[164:167], a[100:103], v[124:127], v[164:167]\n" \
"v_add_f32_e32 v170, v170, v234\n" \
"v_add_f32_e32 v171, v171, v235\n" \
"v_add_f32_e32 v172, v172, v236\n" \
"v_add_f32_e32 v173, v173, v237\n" \
"v_mfma_f32_16x16x32_bf16 v[168:171], a[104:107], v[124:127], v[168:171]\n" \
"v_add_f32_e32 v174, v174, v238\n" \
"v_add_f32_e32 v175, v175, v239\n" \
"v_accvgpr_read_b32 v54, a192\n" \
"s_waitcnt lgkmcnt(0)\n" \
"v_add_f32_e32 v54, v54, v42\n" \
"v_mfma_f32_16x16x32_bf16 v[172:175], a[108:111], v[124:127], v[172:175]\n" \
"v_max_f32_e32 v62, v62, v54\n" \
"v_add_f32_e32 v176, v176, v240\n" \
"v_add_f32_e32 v177, v177, v241\n" \
"v_add_f32_e32 v178, v178, v242\n" \
"v_add_f32_e32 v179, v179, v243\n" \
"v_rcp_f32_e32 v54, v54\n" \
"v_add_f32_e32 v128, v128, v192\n" \
"v_mfma_f32_16x16x32_bf16 v[176:179], a[112:115], v[124:127], v[176:179]\n" \
"v_add_f32_e32 v129, v129, v193\n" \
"v_add_f32_e32 v130, v130, v194\n" \
"v_add_f32_e32 v131, v131, v195\n" \
"v_add_f32_e32 v132, v132, v196\n" \
"v_add_f32_e32 v133, v133, v197\n" \
"v_add_f32_e32 v134, v134, v198\n" \
"v_add_f32_e32 v135, v135, v199\n" \
"v_mul_f32_e32 v54, v54, v58\n" \
"v_mul_f32_e32 v128, v54, v128\n" \
"v_mul_f32_e32 v129, v54, v129\n" \
"v_mul_f32_e32 v130, v54, v130\n" \
"v_mul_f32_e32 v131, v54, v131\n" \
"v_mul_f32_e32 v132, v54, v132\n" \
"v_mul_f32_e32 v133, v54, v133\n" \
"v_mul_f32_e32 v134, v54, v134\n" \
"v_mul_f32_e32 v135, v54, v135\n" \
"v_add_f32_e32 v180, v180, v244\n" \
"v_add_f32_e32 v181, v181, v245\n" \
"v_add_f32_e32 v182, v182, v246\n" \
"v_add_f32_e32 v183, v183, v247\n" \
"v_add_f32_e32 v136, v136, v200\n" \
"v_add_f32_e32 v137, v137, v201\n" \
"v_add_f32_e32 v138, v138, v202\n" \
"v_add_f32_e32 v139, v139, v203\n" \
"v_add_f32_e32 v140, v140, v204\n" \
"v_add_f32_e32 v141, v141, v205\n" \
"v_add_f32_e32 v142, v142, v206\n" \
"v_add_f32_e32 v143, v143, v207\n" \
"v_cvt_pk_bf16_f32 v64, v128, v129\n" \
"v_cvt_pk_bf16_f32 v65, v130, v131\n" \
"v_cvt_pk_bf16_f32 v66, v132, v133\n" \
"v_cvt_pk_bf16_f32 v67, v134, v135\n" \
"buffer_store_dwordx4 v[64:67], v20, s[56:59], s68 offen offset:0 sc1\n" \
"v_mfma_f32_16x16x32_bf16 v[180:183], a[116:119], v[124:127], v[180:183]\n" \
"v_mfma_f32_16x16x32_bf16 a[208:211], a[216:219], v[64:67], a[208:211]\n" \
"v_mul_f32_e32 v136, v54, v136\n" \
"v_mul_f32_e32 v137, v54, v137\n" \
"v_mul_f32_e32 v138, v54, v138\n" \
"v_mul_f32_e32 v139, v54, v139\n" \
"v_mul_f32_e32 v140, v54, v140\n" \
"v_mul_f32_e32 v141, v54, v141\n" \
"v_mul_f32_e32 v142, v54, v142\n" \
"v_mul_f32_e32 v143, v54, v143\n" \
"v_add_f32_e32 v144, v144, v208\n" \
"v_add_f32_e32 v145, v145, v209\n" \
"v_add_f32_e32 v146, v146, v210\n" \
"v_add_f32_e32 v147, v147, v211\n" \
"v_add_f32_e32 v148, v148, v212\n" \
"v_add_f32_e32 v149, v149, v213\n" \
"v_add_f32_e32 v150, v150, v214\n" \
"v_add_f32_e32 v151, v151, v215\n" \
"v_cvt_pk_bf16_f32 v68, v136, v137\n" \
"v_cvt_pk_bf16_f32 v69, v138, v139\n" \
"v_cvt_pk_bf16_f32 v70, v140, v141\n" \
"v_cvt_pk_bf16_f32 v71, v142, v143\n" \
"buffer_store_dwordx4 v[68:71], v20, s[56:59], s68 offen offset:1024 sc1\n" \
"v_mul_f32_e32 v144, v54, v144\n" \
"v_mfma_f32_16x16x32_bf16 a[208:211], a[216:219], v[68:71], a[208:211]\n" \
"v_mul_f32_e32 v145, v54, v145\n" \
"v_mul_f32_e32 v146, v54, v146\n" \
"v_mul_f32_e32 v147, v54, v147\n" \
"v_mul_f32_e32 v148, v54, v148\n" \
"v_mul_f32_e32 v149, v54, v149\n" \
"v_mul_f32_e32 v150, v54, v150\n" \
"v_mul_f32_e32 v151, v54, v151\n" \
"v_add_f32_e32 v184, v184, v248\n" \
"v_add_f32_e32 v185, v185, v249\n" \
"v_add_f32_e32 v186, v186, v250\n" \
"v_add_f32_e32 v187, v187, v251\n" \
"v_add_f32_e32 v152, v152, v216\n" \
"v_add_f32_e32 v153, v153, v217\n" \
"v_add_f32_e32 v154, v154, v218\n" \
"v_add_f32_e32 v155, v155, v219\n" \
"v_add_f32_e32 v156, v156, v220\n" \
"v_add_f32_e32 v157, v157, v221\n" \
"v_add_f32_e32 v158, v158, v222\n" \
"v_add_f32_e32 v159, v159, v223\n" \
"v_cvt_pk_bf16_f32 v72, v144, v145\n" \
"v_cvt_pk_bf16_f32 v73, v146, v147\n" \
"v_cvt_pk_bf16_f32 v74, v148, v149\n" \
"v_cvt_pk_bf16_f32 v75, v150, v151\n" \
"buffer_store_dwordx4 v[72:75], v20, s[56:59], s68 offen offset:2048 sc1\n" \
"v_mfma_f32_16x16x32_bf16 v[184:187], a[120:123], v[124:127], v[184:187]\n" \
"v_mfma_f32_16x16x32_bf16 a[208:211], a[216:219], v[72:75], a[208:211]\n" \
"v_mul_f32_e32 v152, v54, v152\n" \
"v_mul_f32_e32 v153, v54, v153\n" \
"v_mul_f32_e32 v154, v54, v154\n" \
"v_mul_f32_e32 v155, v54, v155\n" \
"v_mul_f32_e32 v156, v54, v156\n" \
"v_mul_f32_e32 v157, v54, v157\n" \
"v_mul_f32_e32 v158, v54, v158\n" \
"v_mul_f32_e32 v159, v54, v159\n" \
"v_cvt_pk_bf16_f32 v76, v152, v153\n" \
"v_cvt_pk_bf16_f32 v77, v154, v155\n" \
"v_cvt_pk_bf16_f32 v78, v156, v157\n" \
"v_cvt_pk_bf16_f32 v79, v158, v159\n" \
"buffer_store_dwordx4 v[76:79], v20, s[56:59], s68 offen offset:3072 sc1\n" \
"v_add_f32_e32 v188, v188, v252\n" \
"v_mfma_f32_16x16x32_bf16 a[208:211], a[216:219], v[76:79], a[208:211]\n" \
"v_add_f32_e32 v189, v189, v253\n" \
"v_add_f32_e32 v190, v190, v254\n" \
"v_add_f32_e32 v191, v191, v255\n" \
"s_nop 1\n" \
"v_mfma_f32_16x16x32_bf16 v[188:191], a[124:127], v[124:127], v[188:191]\n" \
"v_mfma_f32_16x16x32_bf16 a[196:199], a[212:215], v[124:127], a[196:199]\n" \
"s_nop 7\n" \
"v_accvgpr_read_b32 v55, a196\n" \
"v_add_f32_e32 v55, v55, v43\n" \
"v_max_f32_e32 v62, v62, v55\n" \
"v_rcp_f32_e32 v55, v55\n" \
"v_cmp_lt_f32_e64 s[72:73], s100, v62\n" \
"v_mul_f32_e32 v55, v55, v59\n" \
"v_mul_f32_e32 v160, v55, v160\n" \
"v_mul_f32_e32 v161, v55, v161\n" \
"v_mul_f32_e32 v162, v55, v162\n" \
"v_mul_f32_e32 v163, v55, v163\n" \
"v_mul_f32_e32 v164, v55, v164\n" \
"v_mul_f32_e32 v165, v55, v165\n" \
"v_mul_f32_e32 v166, v55, v166\n" \
"v_mul_f32_e32 v167, v55, v167\n" \
"v_cvt_pk_bf16_f32 v80, v160, v161\n" \
"v_cvt_pk_bf16_f32 v81, v162, v163\n" \
"v_cvt_pk_bf16_f32 v82, v164, v165\n" \
"v_cvt_pk_bf16_f32 v83, v166, v167\n" \
"buffer_store_dwordx4 v[80:83], v20, s[56:59], s69 offen offset:0 sc1\n" \
"v_mul_f32_e32 v168, v55, v168\n" \
"v_mfma_f32_16x16x32_bf16 a[208:211], a[216:219], v[80:83], a[208:211]\n" \
"v_mul_f32_e32 v169, v55, v169\n" \
"v_mul_f32_e32 v170, v55, v170\n" \
"v_mul_f32_e32 v171, v55, v171\n" \
"v_mul_f32_e32 v172, v55, v172\n" \
"v_mul_f32_e32 v173, v55, v173\n" \
"v_mul_f32_e32 v174, v55, v174\n" \
"v_mul_f32_e32 v175, v55, v175\n" \
"v_cvt_pk_bf16_f32 v84, v168, v169\n" \
"v_cvt_pk_bf16_f32 v85, v170, v171\n" \
"v_cvt_pk_bf16_f32 v86, v172, v173\n" \
"v_cvt_pk_bf16_f32 v87, v174, v175\n" \
"buffer_store_dwordx4 v[84:87], v20, s[56:59], s69 offen offset:1024 sc1\n" \
"v_mul_f32_e32 v176, v55, v176\n" \
"v_mfma_f32_16x16x32_bf16 a[208:211], a[216:219], v[84:87], a[208:211]\n" \
"v_mul_f32_e32 v177, v55, v177\n" \
"v_mul_f32_e32 v178, v55, v178\n" \
"v_mul_f32_e32 v179, v55, v179\n" \
"v_mul_f32_e32 v180, v55, v180\n" \
"v_mul_f32_e32 v181, v55, v181\n" \
"v_mul_f32_e32 v182, v55, v182\n" \
"v_mul_f32_e32 v183, v55, v183\n" \
"v_cvt_pk_bf16_f32 v96, v176, v177\n" \
"v_cvt_pk_bf16_f32 v97, v178, v179\n" \
"v_cvt_pk_bf16_f32 v98, v180, v181\n" \
"v_cvt_pk_bf16_f32 v99, v182, v183\n" \
"buffer_store_dwordx4 v[96:99], v20, s[56:59], s69 offen offset:2048 sc1\n" \
"v_mul_f32_e32 v184, v55, v184\n" \
"v_mfma_f32_16x16x32_bf16 a[208:211], a[216:219], v[96:99], a[208:211]\n" \
"v_mul_f32_e32 v185, v55, v185\n" \
"v_mul_f32_e32 v186, v55, v186\n" \
"v_mul_f32_e32 v187, v55, v187\n" \
"v_mul_f32_e32 v188, v55, v188\n" \
"v_mul_f32_e32 v189, v55, v189\n" \
"v_mul_f32_e32 v190, v55, v190\n" \
"v_mul_f32_e32 v191, v55, v191\n" \
"v_cvt_pk_bf16_f32 v100, v184, v185\n" \
"v_cvt_pk_bf16_f32 v101, v186, v187\n" \
"v_cvt_pk_bf16_f32 v102, v188, v189\n" \
"v_cvt_pk_bf16_f32 v103, v190, v191\n" \
"buffer_store_dwordx4 v[100:103], v20, s[56:59], s69 offen offset:3072 sc1\n" \
"s_nop 0\n" \
"v_mfma_f32_16x16x32_bf16 a[208:211], a[216:219], v[100:103], a[208:211]\n" \
"s_nop 7\n" \
"v_accvgpr_read_b32 v63, a208\n" \
"v_cmp_u_f32_e32 vcc, v63, v63\n" \
"s_nop 0\n" \
"s_or_b64 vcc, s[72:73], vcc\n" \
"s_cbranch_scc1 L_end_%=\n" \
"L_out_%=:\n" \
"L_pf19:\n" \
"s_cbranch_execz L_retA\n" \
"s_nop 0\n" \
"v_add_u32_e32 v34, 0x20000, v10\n" \
"ds_read_b64_tr_b16 a[0:1], v34 offset:0\n" \
"ds_read_b64_tr_b16 a[2:3], v34 offset:4608\n" \
"ds_read_b64_tr_b16 a[4:5], v34 offset:9216\n" \
"ds_read_b64_tr_b16 a[6:7], v34 offset:13824\n" \
"ds_read_b64_tr_b16 a[8:9], v34 offset:18432\n" \
"ds_read_b64_tr_b16 a[10:11], v34 offset:23040\n" \
"ds_read_b64_tr_b16 a[12:13], v34 offset:32\n" \
"ds_read_b64_tr_b16 a[14:15], v34 offset:4640\n" \
"v_mov_b32_e32 v39, s62\n" \
"v_readlane_b32 s70, v11, 4\n" \
"ds_read_b64_tr_b16 a[16:17], v34 offset:9248\n" \
"ds_read_b64_tr_b16 a[18:19], v34 offset:13856\n" \
"v_bfe_u32 v38, v20, 2, 4\n" \
"v_readlane_b32 s64, v11, 1\n" \
"v_readlane_b32 s71, v11, 3\n" \
"ds_read_b64_tr_b16 a[20:21], v34 offset:18464\n" \
"ds_read_b64_tr_b16 a[22:23], v34 offset:23072\n" \
"ds_read_b64_tr_b16 a[24:25], v34 offset:64\n" \
"ds_read_b64_tr_b16 a[26:27], v34 offset:4672\n" \
"buffer_load_dword v37, v38, s[56:59], s64 offen sc1\n" \
"ds_read_b64_tr_b16 a[28:29], v34 offset:9280\n" \
"ds_read_b64_tr_b16 a[30:31], v34 offset:13888\n" \
"ds_read_b64_tr_b16 a[192:193], v34 offset:18496\n" \
"ds_read_b64_tr_b16 a[194:195], v34 offset:23104\n" \
"ds_read_b64_tr_b16 a[196:197], v34 offset:96\n" \
"ds_read_b64_tr_b16 a[198:199], v34 offset:4704\n" \
"ds_read_b64_tr_b16 a[200:201], v34 offset:9312\n" \
"ds_read_b64_tr_b16 a[202:203], v34 offset:13920\n" \
"ds_read_b64_tr_b16 a[204:205], v34 offset:18528\n" \
"ds_read_b64_tr_b16 a[206:207], v34 offset:23136\n" \
"ds_read_b64_tr_b16 a[208:209], v34 offset:128\n" \
"ds_read_b64_tr_b16 a[210:211], v34 offset:4736\n" \
"ds_read_b64_tr_b16 a[212:213], v34 offset:9344\n" \
"ds_read_b64_tr_b16 a[214:215], v34 offset:13952\n" \
"ds_read_b64_tr_b16 a[216:217], v34 offset:18560\n" \
"ds_read_b64_tr_b16 a[218:219], v34 offset:23168\n" \
"v_add_u32_e32 v33, 0xfc00, v10\n" \
"v_readlane_b32 s99, v11, 19\n" \
"v_readlane_b32 s80, v11, 9\n" \
"v_readlane_b32 s81, v11, 10\n" \
"s_mov_b32 s82, 0x47fca0\n" \
"s_waitcnt vmcnt(18) lgkmcnt(15)\n" \
"v_mfma_f32_16x16x32_bf16 v[112:115], a[0:3], a[220:223], 0\n" \
"s_mov_b32 s83, 0x20000\n" \
"v_readlane_b32 s65, v11, 23\n" \
"v_mfma_f32_16x16x32_bf16 v[112:115], a[4:7], a[224:227], v[112:115]\n" \
"s_add_u32 m0, s99, 0x6c00\n" \
"v_readlane_b32 s66, v11, 26\n" \
"v_mfma_f32_16x16x32_bf16 v[112:115], a[8:11], a[228:231], v[112:115]\n" \
"v_add_u32_e32 v32, 0x49000, v20\n" \
"v_lshrrev_b32_e32 v44, 9, v20\n" \
"v_mfma_f32_16x16x32_bf16 v[116:119], a[8:11], a[244:247], 0\n" \
"buffer_load_dwordx4 v0, s[80:83], s65 offen offset:0 lds\n" \
"v_mfma_f32_16x16x32_bf16 v[116:119], a[4:7], a[240:243], v[116:119]\n" \
"v_mul_u32_u24_e32 v44, 0xb40, v44\n" \
"v_add_u32_e32 v35, v44, v10\n" \
"v_mfma_f32_16x16x32_bf16 v[116:119], a[0:3], a[236:239], v[116:119]\n" \
"v_readlane_b32 s67, v11, 24\n" \
"v_readlane_b32 s92, v11, 20\n" \
"v_mfma_f32_16x16x32_bf16 v[184:187], a[0:3], v[64:67], 0\n" \
"v_readlane_b32 s73, v11, 25\n" \
"v_readlane_b32 s94, v11, 21\n" \
"v_mfma_f32_16x16x32_bf16 v[184:187], a[4:7], v[68:71], v[184:187]\n" \
"v_readlane_b32 s84, v11, 7\n" \
"v_readlane_b32 s85, v11, 8\n" \
"v_mfma_f32_16x16x32_bf16 v[184:187], a[8:11], v[72:75], v[184:187]\n" \
"buffer_load_dwordx4 v1, s[80:83], s65 offen offset:1024 lds\n" \
"v_mfma_f32_16x16x32_bf16 v[188:191], a[8:11], v[96:99], 0\n" \
"ds_read_b64_tr_b16 a[8:9], v34 offset:18592\n" \
"ds_read_b64_tr_b16 a[10:11], v34 offset:23200\n" \
"v_mfma_f32_16x16x32_bf16 v[188:191], a[4:7], v[84:87], v[188:191]\n" \
"ds_read_b64_tr_b16 a[4:5], v34 offset:9376\n" \
"ds_read_b64_tr_b16 a[6:7], v34 offset:13984\n" \
"v_mfma_f32_16x16x32_bf16 v[188:191], a[0:3], v[80:83], v[188:191]\n" \
"ds_read_b64_tr_b16 a[0:1], v34 offset:160\n" \
"ds_read_b64_tr_b16 a[2:3], v34 offset:4768\n" \
"v_mfma_f32_16x16x32_bf16 v[120:123], a[12:15], a[220:223], 0\n" \
"s_mov_b32 s86, 0xa1ca0\n" \
"s_mov_b32 s87, 0x20000\n" \
"v_mfma_f32_16x16x32_bf16 v[120:123], a[16:19], a[224:227], v[120:123]\n" \
"buffer_load_dwordx4 v2, s[80:83], s65 offen offset:2048 lds\n" \
"v_mfma_f32_16x16x32_bf16 v[120:123], a[20:23], a[228:231], v[120:123]\n" \
"v_readlane_b32 s68, v11, 27\n" \
"v_readlane_b32 s69, v11, 28\n" \
"v_mfma_f32_16x16x32_bf16 v[124:127], a[20:23], a[244:247], 0\n" \
"v_readlane_b32 s72, v11, 30\n" \
"v_readlane_b32 s93, v11, 31\n" \
"v_mfma_f32_16x16x32_bf16 v[124:127], a[16:19], a[240:243], v[124:127]\n" \
"v_lshlrev_b32_e32 v41, 12, v20\n" \
"v_readlane_b32 s63, v11, 37\n" \
"v_mfma_f32_16x16x32_bf16 v[124:127], a[12:15], a[236:239], v[124:127]\n" \
"v_bfe_u32 v40, v20, 2, 7\n" \
"s_add_u32 s74, s63, 0x3800\n" \
"v_mfma_f32_16x16x32_bf16 v[192:195], a[12:15], v[64:67], 0\n" \
"buffer_load_dwordx4 v3, s[80:83], s65 offen offset:3072 lds\n" \
"v_mfma_f32_16x16x32_bf16 v[192:195], a[16:19], v[68:71], v[192:195]\n" \
"s_add_u32 m0, s99, 0x7c00\n" \
"v_readlane_b32 s95, v11, 34\n" \
"v_mfma_f32_16x16x32_bf16 v[192:195], a[20:23], v[72:75], v[192:195]\n" \
"v_readlane_b32 s96, v11, 35\n" \
"v_readlane_b32 s97, v11, 36\n" \
"v_mfma_f32_16x16x32_bf16 v[196:199], a[20:23], v[96:99], 0\n" \
"ds_read_b64_tr_b16 a[20:21], v34 offset:18624\n" \
"ds_read_b64_tr_b16 a[22:23], v34 offset:23232\n" \
"v_mfma_f32_16x16x32_bf16 v[196:199], a[16:19], v[84:87], v[196:199]\n" \
"ds_read_b64_tr_b16 a[16:17], v34 offset:9408\n" \
"ds_read_b64_tr_b16 a[18:19], v34 offset:14016\n" \
"v_mfma_f32_16x16x32_bf16 v[196:199], a[12:15], v[80:83], v[196:199]\n" \
"ds_read_b64_tr_b16 a[12:13], v34 offset:192\n" \
"ds_read_b64_tr_b16 a[14:15], v34 offset:4800\n" \
"v_mfma_f32_16x16x32_bf16 v[128:131], a[24:27], a[220:223], 0\n" \
"v_readlane_b32 s100, v11, 6\n" \
"s_waitcnt lgkmcnt(8)\n" \
"v_mfma_f32_16x16x32_bf16 v[128:131], a[28:31], a[224:227], v[128:131]\n" \
"buffer_load_dwordx4 v4, s[80:83], s65 offen offset:0 lds\n" \
"v_mfma_f32_16x16x32_bf16 v[128:131], a[192:195], a[228:231], v[128:131]\n" \
"v_bfe_u32 v44, v20, 4, 4\n" \
"v_bfe_u32 v45, v20, 8, 2\n" \
"v_mfma_f32_16x16x32_bf16 v[132:135], a[192:195], a[244:247], 0\n" \
"v_mul_u32_u24_e32 v42, 0x480, v44\n" \
"v_lshl_add_u32 v42, v45, 3, v42\n" \
"v_mfma_f32_16x16x32_bf16 v[132:135], a[28:31], a[240:243], v[132:135]\n" \
"v_lshrrev_b32_e32 v44, 1, v44\n" \
"v_lshlrev_b32_e32 v45, 1, v45\n" \
"v_mfma_f32_16x16x32_bf16 v[132:135], a[24:27], a[236:239], v[132:135]\n" \
"v_sub_u32_e32 v44, v44, v45\n" \
"v_and_b32_e32 v45, 16, v20\n" \
"v_mfma_f32_16x16x32_bf16 v[200:203], a[24:27], v[64:67], 0\n" \
"buffer_load_dwordx4 v5, s[80:83], s65 offen offset:1024 lds\n" \
"v_mfma_f32_16x16x32_bf16 v[200:203], a[28:31], v[68:71], v[200:203]\n" \
"v_mov_b32_e32 v46, 0x3f80\n" \
"v_lshlrev_b32_e32 v45, v45, v46\n" \
"v_mfma_f32_16x16x32_bf16 v[200:203], a[192:195], v[72:75], v[200:203]\n" \
"v_mov_b32_e32 v52, 0\n" \
"v_mov_b32_e32 v53, 0\n" \
"v_mfma_f32_16x16x32_bf16 v[204:207], a[192:195], v[96:99], 0\n" \
"ds_read_b64_tr_b16 a[192:193], v34 offset:18656\n" \
"ds_read_b64_tr_b16 a[194:195], v34 offset:23264\n" \
"v_mfma_f32_16x16x32_bf16 v[204:207], a[28:31], v[84:87], v[204:207]\n" \
"ds_read_b64_tr_b16 a[28:29], v34 offset:9440\n" \
"ds_read_b64_tr_b16 a[30:31], v34 offset:14048\n" \
"v_mfma_f32_16x16x32_bf16 v[204:207], a[24:27], v[80:83], v[204:207]\n" \
"ds_read_b64_tr_b16 a[24:25], v34 offset:224\n" \
"ds_read_b64_tr_b16 a[26:27], v34 offset:4832\n" \
"v_mfma_f32_16x16x32_bf16 v[136:139], a[196:199], a[220:223], 0\n" \
"buffer_load_dwordx4 v6, s[80:83], s65 offen offset:2048 lds\n" \
"v_mfma_f32_16x16x32_bf16 v[136:139], a[200:203], a[224:227], v[136:139]\n" \
"v_mov_b32_e32 v48, 0\n" \
"v_mov_b32_e32 v49, 0\n" \
"v_mfma_f32_16x16x32_bf16 v[136:139], a[204:207], a[228:231], v[136:139]\n" \
"v_add_u32_e32 v43, 0x9000, v42\n" \
"v_readlane_b32 s88, v11, 11\n" \
"v_mfma_f32_16x16x32_bf16 v[140:143], a[204:207], a[244:247], 0\n" \
"v_readlane_b32 s89, v11, 12\n" \
"s_mov_b32 s90, 0x240000\n" \
"v_mfma_f32_16x16x32_bf16 v[140:143], a[200:203], a[240:243], v[140:143]\n" \
"s_mov_b32 s91, 0x20000\n" \
"v_mov_b32_e32 v46, s62\n" \
"v_mfma_f32_16x16x32_bf16 v[140:143], a[196:199], a[236:239], v[140:143]\n" \
"buffer_load_dwordx4 v7, s[80:83], s65 offen offset:3072 lds\n" \
"v_mfma_f32_16x16x32_bf16 v[208:211], a[196:199], v[64:67], 0\n" \
"s_add_u32 m0, s99, 0x8c00\n" \
"v_mfma_f32_16x16x32_bf16 v[208:211], a[200:203], v[68:71], v[208:211]\n" \
"v_mfma_f32_16x16x32_bf16 v[208:211], a[204:207], v[72:75], v[208:211]\n" \
"v_mfma_f32_16x16x32_bf16 v[212:215], a[204:207], v[96:99], 0\n" \
"ds_read_b64_tr_b16 a[204:205], v34 offset:18688\n" \
"ds_read_b64_tr_b16 a[206:207], v34 offset:23296\n" \
"v_mfma_f32_16x16x32_bf16 v[212:215], a[200:203], v[84:87], v[212:215]\n" \
"ds_read_b64_tr_b16 a[200:201], v34 offset:9472\n" \
"ds_read_b64_tr_b16 a[202:203], v34 offset:14080\n" \
"v_mfma_f32_16x16x32_bf16 v[212:215], a[196:199], v[80:83], v[212:215]\n" \
"ds_read_b64_tr_b16 a[196:197], v34 offset:256\n" \
"ds_read_b64_tr_b16 a[198:199], v34 offset:4864\n" \
"v_mfma_f32_16x16x32_bf16 v[144:147], a[208:211], a[220:223], 0\n" \
"buffer_load_dwordx4 v8, s[80:83], s65 offen offset:0 lds\n" \
"v_mfma_f32_16x16x32_bf16 v[144:147], a[212:215], a[224:227], v[144:147]\n" \
"s_waitcnt vmcnt(9)\n" \
"v_cmp_ne_u32_e32 vcc, s62, v37\n" \
"v_mfma_f32_16x16x32_bf16 v[144:147], a[216:219], a[228:231], v[144:147]\n" \
"s_add_u32 m0, s99, 0x14400\n" \
"v_readlane_b32 s65, v11, 42\n" \
"v_mfma_f32_16x16x32_bf16 v[148:151], a[216:219], a[244:247], 0\n" \
"v_mfma_f32_16x16x32_bf16 v[148:151], a[212:215], a[240:243], v[148:151]\n" \
"s_cbranch_vccnz L_sa_%=\n" \
"L_ra_%=:\n" \
"v_readlane_b32 s64, v11, 29\n" \
"s_mov_b64 exec, 1\n" \
"buffer_store_dword v39, off, s[56:59], s70 offset:0 sc1\n" \
"s_mov_b64 exec, -1\n" \
"v_mfma_f32_16x16x32_bf16 v[148:151], a[208:211], a[236:239], v[148:151]\n" \
"v_readlane_b32 s70, v11, 45\n" \
"v_mfma_f32_16x16x32_bf16 v[216:219], a[208:211], v[64:67], 0\n" \
"v_mfma_f32_16x16x32_bf16 v[216:219], a[212:215], v[68:71], v[216:219]\n" \
"v_mfma_f32_16x16x32_bf16 v[216:219], a[216:219], v[72:75], v[216:219]\n" \
"buffer_load_dwordx4 a[64:67], v20, s[56:59], s66 offen offset:0 sc1\n" \
"v_mfma_f32_16x16x32_bf16 v[220:223], a[216:219], v[96:99], 0\n" \
"v_mfma_f32_16x16x32_bf16 v[220:223], a[212:215], v[84:87], v[220:223]\n" \
"v_mfma_f32_16x16x32_bf16 v[220:223], a[208:211], v[80:83], v[220:223]\n" \
"s_waitcnt lgkmcnt(3)\n" \
"v_mfma_f32_16x16x32_bf16 v[152:155], a[0:3], a[220:223], 0\n" \
"v_mfma_f32_16x16x32_bf16 v[152:155], a[4:7], a[224:227], v[152:155]\n" \
"buffer_load_dwordx4 a[68:71], v20, s[56:59], s66 offen offset:1024 sc1\n" \
"v_mfma_f32_16x16x32_bf16 v[152:155], a[8:11], a[228:231], v[152:155]\n" \
"v_mfma_f32_16x16x32_bf16 v[156:159], a[8:11], a[244:247], 0\n" \
"v_mfma_f32_16x16x32_bf16 v[156:159], a[4:7], a[240:243], v[156:159]\n" \
"v_mfma_f32_16x16x32_bf16 v[156:159], a[0:3], a[236:239], v[156:159]\n" \
"v_mfma_f32_16x16x32_bf16 v[224:227], a[0:3], v[64:67], 0\n" \
"buffer_load_dwordx4 a[84:87], v32, s[56:59], s66 offen offset:1024 sc1\n" \
"v_mfma_f32_16x16x32_bf16 v[224:227], a[4:7], v[68:71], v[224:227]\n" \
"v_mfma_f32_16x16x32_bf16 v[224:227], a[8:11], v[72:75], v[224:227]\n" \
"v_mfma_f32_16x16x32_bf16 v[228:231], a[8:11], v[96:99], 0\n" \
"v_mfma_f32_16x16x32_bf16 v[228:231], a[4:7], v[84:87], v[228:231]\n" \
"v_mfma_f32_16x16x32_bf16 v[228:231], a[0:3], v[80:83], v[228:231]\n" \
"buffer_load_dwordx4 a[80:83], v32, s[56:59], s66 offen offset:0 sc1\n" \
"v_mfma_f32_16x16x32_bf16 v[160:163], a[12:15], a[220:223], 0\n" \
"v_mfma_f32_16x16x32_bf16 v[160:163], a[16:19], a[224:227], v[160:163]\n" \
"v_mfma_f32_16x16x32_bf16 v[160:163], a[20:23], a[228:231], v[160:163]\n" \
"v_mfma_f32_16x16x32_bf16 v[164:167], a[20:23], a[244:247], 0\n" \
"v_mfma_f32_16x16x32_bf16 v[164:167], a[16:19], a[240:243], v[164:167]\n" \
"buffer_load_dwordx4 a[72:75], v20, s[56:59], s66 offen offset:2048 sc1\n" \
"v_mfma_f32_16x16x32_bf16 v[164:167], a[12:15], a[236:239], v[164:167]\n" \
"v_mfma_f32_16x16x32_bf16 v[232:235], a[12:15], v[64:67], 0\n" \
"v_mfma_f32_16x16x32_bf16 v[232:235], a[16:19], v[68:71], v[232:235]\n" \
"v_mfma_f32_16x16x32_bf16 v[232:235], a[20:23], v[72:75], v[232:235]\n" \
"v_mfma_f32_16x16x32_bf16 v[236:239], a[20:23], v[96:99], 0\n" \
"buffer_load_dwordx4 a[76:79], v20, s[56:59], s66 offen offset:3072 sc1\n" \
"v_mfma_f32_16x16x32_bf16 v[236:239], a[16:19], v[84:87], v[236:239]\n" \
"v_mfma_f32_16x16x32_bf16 v[236:239], a[12:15], v[80:83], v[236:239]\n" \
"s_waitcnt lgkmcnt(0)\n" \
"s_barrier\n" \
"v_mfma_f32_16x16x32_bf16 v[168:171], a[24:27], a[220:223], 0\n" \
"ds_read_b64_tr_b16 a[208:209], v33 offset:9216\n" \
"ds_read_b64_tr_b16 a[210:211], v33 offset:13824\n" \
"v_mfma_f32_16x16x32_bf16 v[168:171], a[28:31], a[224:227], v[168:171]\n" \
"ds_read_b64_tr_b16 a[212:213], v33 offset:9248\n" \
"ds_read_b64_tr_b16 a[214:215], v33 offset:13856\n" \
"v_mfma_f32_16x16x32_bf16 v[168:171], a[192:195], a[228:231], v[168:171]\n" \
"ds_read_b64_tr_b16 a[216:217], v33 offset:9280\n" \
"ds_read_b64_tr_b16 a[218:219], v33 offset:13888\n" \
"v_mfma_f32_16x16x32_bf16 v[172:175], a[192:195], a[244:247], 0\n" \
"ds_read_b64_tr_b16 a[0:1], v33 offset:9312\n" \
"ds_read_b64_tr_b16 a[2:3], v33 offset:13920\n" \
"v_mfma_f32_16x16x32_bf16 v[172:175], a[28:31], a[240:243], v[172:175]\n" \
"ds_read_b64_tr_b16 a[4:5], v33 offset:9344\n" \
"ds_read_b64_tr_b16 a[6:7], v33 offset:13952\n" \
"v_mfma_f32_16x16x32_bf16 v[172:175], a[24:27], a[236:239], v[172:175]\n" \
"ds_read_b64_tr_b16 a[8:9], v33 offset:9376\n" \
"ds_read_b64_tr_b16 a[10:11], v33 offset:13984\n" \
"v_mfma_f32_16x16x32_bf16 v[240:243], a[24:27], v[64:67], 0\n" \
"ds_read_b64_tr_b16 a[12:13], v33 offset:9408\n" \
"ds_read_b64_tr_b16 a[14:15], v33 offset:14016\n" \
"v_mfma_f32_16x16x32_bf16 v[240:243], a[28:31], v[68:71], v[240:243]\n" \
"ds_read_b64_tr_b16 a[16:17], v33 offset:9440\n" \
"ds_read_b64_tr_b16 a[18:19], v33 offset:14048\n" \
"v_mfma_f32_16x16x32_bf16 v[240:243], a[192:195], v[72:75], v[240:243]\n" \
"ds_read_b64_tr_b16 a[20:21], v33 offset:9472\n" \
"ds_read_b64_tr_b16 a[22:23], v33 offset:14080\n" \
"v_mfma_f32_16x16x32_bf16 v[244:247], a[192:195], v[96:99], 0\n" \
"ds_read_b64_tr_b16 a[192:193], v35 offset:32\n" \
"ds_read_b64_tr_b16 a[194:195], v35 offset:2336\n" \
"v_mfma_f32_16x16x32_bf16 v[244:247], a[28:31], v[84:87], v[244:247]\n" \
"ds_read_b64_tr_b16 a[28:29], v35 offset:10368\n" \
"ds_read_b64_tr_b16 a[30:31], v35 offset:12672\n" \
"v_mfma_f32_16x16x32_bf16 v[244:247], a[24:27], v[80:83], v[244:247]\n" \
"ds_read_b64_tr_b16 a[24:25], v35 offset:0\n" \
"ds_read_b64_tr_b16 a[26:27], v35 offset:2304\n" \
"v_mfma_f32_16x16x32_bf16 v[176:179], a[196:199], a[220:223], 0\n" \
"buffer_load_dwordx4 a[92:95], v32, s[56:59], s66 offen offset:3072 sc1\n" \
"v_mfma_f32_16x16x32_bf16 v[176:179], a[200:203], a[224:227], v[176:179]\n" \
"v_mfma_f32_16x16x32_bf16 v[176:179], a[204:207], a[228:231], v[176:179]\n" \
"v_mfma_f32_16x16x32_bf16 v[180:183], a[204:207], a[244:247], 0\n" \
"v_mfma_f32_16x16x32_bf16 v[180:183], a[200:203], a[240:243], v[180:183]\n" \
"v_mfma_f32_16x16x32_bf16 v[180:183], a[196:199], a[236:239], v[180:183]\n" \
"buffer_load_dwordx4 a[88:91], v32, s[56:59], s66 offen offset:2048 sc1\n" \
"v_mfma_f32_16x16x32_bf16 v[248:251], a[196:199], v[64:67], 0\n" \
"v_readlane_b32 s66, v11, 43\n" \
"v_mfma_f32_16x16x32_bf16 v[248:251], a[200:203], v[68:71], v[248:251]\n" \
"v_mfma_f32_16x16x32_bf16 v[248:251], a[204:207], v[72:75], v[248:251]\n" \
"v_mfma_f32_16x16x32_bf16 v[252:255], a[204:207], v[96:99], 0\n" \
"ds_read_b64_tr_b16 a[204:205], v35 offset:10432\n" \
"ds_read_b64_tr_b16 a[206:207], v35 offset:12736\n" \
"v_mfma_f32_16x16x32_bf16 v[252:255], a[200:203], v[84:87], v[252:255]\n" \
"ds_read_b64_tr_b16 a[200:201], v35 offset:64\n" \
"ds_read_b64_tr_b16 a[202:203], v35 offset:2368\n" \
"v_mfma_f32_16x16x32_bf16 v[252:255], a[196:199], v[80:83], v[252:255]\n" \
"ds_read_b64_tr_b16 a[196:197], v35 offset:10400\n" \
"s_waitcnt lgkmcnt(9)\n" \
"v_mfma_f32_16x16x32_bf16 v[112:115], a[208:211], a[232:235], v[112:115]\n" \
"buffer_load_dwordx4 v0, s[80:83], s67 offen offset:0 lds\n" \
"v_mfma_f32_16x16x32_bf16 v[116:119], a[208:211], a[248:251], v[116:119]\n" \
"ds_read_b64_tr_b16 a[198:199], v35 offset:12704\n" \
"v_mfma_f32_16x16x32_bf16 v[184:187], a[208:211], v[76:79], v[184:187]\n" \
"v_mfma_f32_16x16x32_bf16 v[188:191], a[208:211], v[100:103], v[188:191]\n" \
"ds_read_b64_tr_b16 a[208:209], v35 offset:96\n" \
"ds_read_b64_tr_b16 a[210:211], v35 offset:2400\n" \
"v_mfma_f32_16x16x32_bf16 v[120:123], a[212:215], a[232:235], v[120:123]\n" \
"v_mfma_f32_16x16x32_bf16 v[124:127], a[212:215], a[248:251], v[124:127]\n" \
"buffer_load_dwordx4 v1, s[80:83], s67 offen offset:1024 lds\n" \
"v_mfma_f32_16x16x32_bf16 v[192:195], a[212:215], v[76:79], v[192:195]\n" \
"v_mfma_f32_16x16x32_bf16 v[196:199], a[212:215], v[100:103], v[196:199]\n" \
"ds_read_b64_tr_b16 a[212:213], v35 offset:10464\n" \
"ds_read_b64_tr_b16 a[214:215], v35 offset:12768\n" \
"v_mfma_f32_16x16x32_bf16 v[128:131], a[216:219], a[232:235], v[128:131]\n" \
"v_mfma_f32_16x16x32_bf16 v[132:135], a[216:219], a[248:251], v[132:135]\n" \
"v_mfma_f32_16x16x32_bf16 v[200:203], a[216:219], v[76:79], v[200:203]\n" \
"buffer_load_dwordx4 v2, s[80:83], s67 offen offset:2048 lds\n" \
"v_mfma_f32_16x16x32_bf16 v[204:207], a[216:219], v[100:103], v[204:207]\n" \
"ds_read_b64_tr_b16 a[216:217], v35 offset:128\n" \
"L_pf20:\n" \
"s_cbranch_execz L_pf22\n" \
"s_nop 0\n" \
"ds_read_b64_tr_b16 a[218:219], v35 offset:2432\n" \
"v_mfma_f32_16x16x32_bf16 v[136:139], a[0:3], a[232:235], v[136:139]\n" \
"v_mfma_f32_16x16x32_bf16 v[140:143], a[0:3], a[248:251], v[140:143]\n" \
"v_mfma_f32_16x16x32_bf16 v[208:211], a[0:3], v[76:79], v[208:211]\n" \
"v_mfma_f32_16x16x32_bf16 v[212:215], a[0:3], v[100:103], v[212:215]\n" \
"ds_read_b64_tr_b16 a[0:1], v35 offset:10496\n" \
"ds_read_b64_tr_b16 a[2:3], v35 offset:12800\n" \
"v_mfma_f32_16x16x32_bf16 v[144:147], a[4:7], a[232:235], v[144:147]\n" \
"buffer_load_dwordx4 v3, s[80:83], s67 offen offset:3072 lds\n" \
"v_mfma_f32_16x16x32_bf16 v[148:151], a[4:7], a[248:251], v[148:151]\n" \
"s_add_u32 m0, s99, 0x15400\n" \
"v_mfma_f32_16x16x32_bf16 v[216:219], a[4:7], v[76:79], v[216:219]\n" \
"v_mfma_f32_16x16x32_bf16 v[220:223], a[4:7], v[100:103], v[220:223]\n" \
"ds_read_b64_tr_b16 a[4:5], v35 offset:160\n" \
"ds_read_b64_tr_b16 a[6:7], v35 offset:2464\n" \
"v_mfma_f32_16x16x32_bf16 v[152:155], a[8:11], a[232:235], v[152:155]\n" \
"v_mfma_f32_16x16x32_bf16 v[156:159], a[8:11], a[248:251], v[156:159]\n" \
"buffer_load_dwordx4 v4, s[80:83], s67 offen offset:0 lds\n" \
"v_mfma_f32_16x16x32_bf16 v[224:227], a[8:11], v[76:79], v[224:227]\n" \
"v_mfma_f32_16x16x32_bf16 v[228:231], a[8:11], v[100:103], v[228:231]\n" \
"ds_read_b64_tr_b16 a[8:9], v35 offset:10528\n" \
"ds_read_b64_tr_b16 a[10:11], v35 offset:12832\n" \
"v_mfma_f32_16x16x32_bf16 v[160:163], a[12:15], a[232:235], v[160:163]\n" \
"v_mfma_f32_16x16x32_bf16 v[164:167], a[12:15], a[248:251], v[164:167]\n" \
"v_mfma_f32_16x16x32_bf16 v[232:235], a[12:15], v[76:79], v[232:235]\n" \
"buffer_load_dwordx4 v5, s[80:83], s67 offen offset:1024 lds\n" \
"v_mfma_f32_16x16x32_bf16 v[236:239], a[12:15], v[100:103], v[236:239]\n" \
"ds_read_b64_tr_b16 a[12:13], v35 offset:192\n" \
"ds_read_b64_tr_b16 a[14:15], v35 offset:2496\n" \
"v_mfma_f32_16x16x32_bf16 v[168:171], a[16:19], a[232:235], v[168:171]\n" \
"v_mfma_f32_16x16x32_bf16 v[172:175], a[16:19], a[248:251], v[172:175]\n" \
"v_mfma_f32_16x16x32_bf16 v[240:243], a[16:19], v[76:79], v[240:243]\n" \
"v_mfma_f32_16x16x32_bf16 v[244:247], a[16:19], v[100:103], v[244:247]\n" \
"buffer_load_dwordx4 v6, s[80:83], s67 offen offset:2048 lds\n" \
"v_mfma_f32_16x16x32_bf16 v[176:179], a[20:23], a[232:235], v[176:179]\n" \
"ds_read_b64_tr_b16 a[16:17], v35 offset:10560\n" \
"ds_read_b64_tr_b16 a[18:19], v35 offset:12864\n" \
"v_mfma_f32_16x16x32_bf16 v[180:183], a[20:23], a[248:251], v[180:183]\n" \
"v_mfma_f32_16x16x32_bf16 v[248:251], a[20:23], v[76:79], v[248:251]\n" \
"v_mfma_f32_16x16x32_bf16 v[252:255], a[20:23], v[100:103], v[252:255]\n" \
"ds_read_b64_tr_b16 a[20:21], v35 offset:224\n" \
"s_waitcnt lgkmcnt(7)\n" \
"v_mfma_f32_16x16x32_bf16 v[112:115], a[24:27], a[32:35], v[112:115]\n" \
"buffer_load_dwordx4 v7, s[80:83], s67 offen offset:3072 lds\n" \
"v_mfma_f32_16x16x32_bf16 v[112:115], a[28:31], a[36:39], v[112:115]\n" \
"s_add_u32 m0, s99, 0x16400\n" \
"ds_read_b64_tr_b16 a[22:23], v35 offset:2528\n" \
"v_mfma_f32_16x16x32_bf16 v[116:119], a[28:31], a[44:47], v[116:119]\n" \
"v_mfma_f32_16x16x32_bf16 v[116:119], a[24:27], a[40:43], v[116:119]\n" \
"v_mfma_f32_16x16x32_bf16 v[184:187], a[24:27], a[48:51], v[184:187]\n" \
"v_mfma_f32_16x16x32_bf16 v[184:187], a[28:31], a[52:55], v[184:187]\n" \
"buffer_load_dword v47, v38, s[56:59], s71 offen sc1\n" \
"v_mfma_f32_16x16x32_bf16 v[188:191], a[28:31], a[60:63], v[188:191]\n" \
"ds_read_b64_tr_b16 a[28:29], v35 offset:256\n" \
"ds_read_b64_tr_b16 a[30:31], v35 offset:2560\n" \
"v_mfma_f32_16x16x32_bf16 v[188:191], a[24:27], a[56:59], v[188:191]\n" \
"ds_read_b64_tr_b16 a[24:25], v35 offset:10592\n" \
"ds_read_b64_tr_b16 a[26:27], v35 offset:12896\n" \
"v_mfma_f32_16x16x32_bf16 v[120:123], a[192:195], a[32:35], v[120:123]\n" \
"v_mfma_f32_16x16x32_bf16 v[120:123], a[196:199], a[36:39], v[120:123]\n" \
"v_mfma_f32_16x16x32_bf16 v[124:127], a[196:199], a[44:47], v[124:127]\n" \
"buffer_load_dwordx4 v8, s[80:83], s67 offen offset:0 lds\n" \
"v_mfma_f32_16x16x32_bf16 v[124:127], a[192:195], a[40:43], v[124:127]\n" \
"s_add_u32 m0, s92, 0x0\n" \
"v_readlane_b32 s67, v11, 44\n" \
"v_mfma_f32_16x16x32_bf16 v[192:195], a[192:195], a[48:51], v[192:195]\n" \
"v_mfma_f32_16x16x32_bf16 v[192:195], a[196:199], a[52:55], v[192:195]\n" \
"v_mfma_f32_16x16x32_bf16 v[196:199], a[196:199], a[60:63], v[196:199]\n" \
"v_mfma_f32_16x16x32_bf16 v[196:199], a[192:195], a[56:59], v[196:199]\n" \
"buffer_load_dwordx4 v0, s[80:83], s73 offen offset:0 lds\n" \
"v_mfma_f32_16x16x32_bf16 v[128:131], a[200:203], a[32:35], v[128:131]\n" \
"ds_read_b64_tr_b16 a[192:193], v35 offset:10624\n" \
"ds_read_b64_tr_b16 a[194:195], v35 offset:12928\n" \
"v_mfma_f32_16x16x32_bf16 v[128:131], a[204:207], a[36:39], v[128:131]\n" \
"v_mfma_f32_16x16x32_bf16 v[132:135], a[204:207], a[44:47], v[132:135]\n" \
"v_mfma_f32_16x16x32_bf16 v[132:135], a[200:203], a[40:43], v[132:135]\n" \
"v_mfma_f32_16x16x32_bf16 v[200:203], a[200:203], a[48:51], v[200:203]\n" \
"buffer_load_dwordx4 v1, s[80:83], s73 offen offset:1024 lds\n" \
"v_mfma_f32_16x16x32_bf16 v[200:203], a[204:207], a[52:55], v[200:203]\n" \
"v_mfma_f32_16x16x32_bf16 v[204:207], a[204:207], a[60:63], v[204:207]\n" \
"v_mfma_f32_16x16x32_bf16 v[204:207], a[200:203], a[56:59], v[204:207]\n" \
"v_mfma_f32_16x16x32_bf16 v[136:139], a[208:211], a[32:35], v[136:139]\n" \
"v_mfma_f32_16x16x32_bf16 v[136:139], a[212:215], a[36:39], v[136:139]\n" \
"buffer_load_dwordx4 v2, s[80:83], s73 offen offset:2048 lds\n" \
"v_mfma_f32_16x16x32_bf16 v[140:143], a[212:215], a[44:47], v[140:143]\n" \
"v_mfma_f32_16x16x32_bf16 v[140:143], a[208:211], a[40:43], v[140:143]\n" \
"v_mfma_f32_16x16x32_bf16 v[208:211], a[208:211], a[48:51], v[208:211]\n" \
"v_mfma_f32_16x16x32_bf16 v[208:211], a[212:215], a[52:55], v[208:211]\n" \
"v_mfma_f32_16x16x32_bf16 v[212:215], a[212:215], a[60:63], v[212:215]\n" \
"buffer_load_dwordx4 v3, s[80:83], s73 offen offset:3072 lds\n" \
"s_add_u32 m0, s92, 0x1000\n" \
"v_mfma_f32_16x16x32_bf16 v[212:215], a[208:211], a[56:59], v[212:215]\n" \
"v_mfma_f32_16x16x32_bf16 v[144:147], a[216:219], a[32:35], v[144:147]\n" \
"v_mfma_f32_16x16x32_bf16 v[144:147], a[0:3], a[36:39], v[144:147]\n" \
"v_mfma_f32_16x16x32_bf16 v[148:151], a[0:3], a[44:47], v[148:151]\n" \
"buffer_load_dwordx4 v4, s[80:83], s73 offen offset:0 lds\n" \
"v_mfma_f32_16x16x32_bf16 v[148:151], a[216:219], a[40:43], v[148:151]\n" \
"v_mfma_f32_16x16x32_bf16 v[216:219], a[216:219], a[48:51], v[216:219]\n" \
"v_mfma_f32_16x16x32_bf16 v[216:219], a[0:3], a[52:55], v[216:219]\n" \
"v_mfma_f32_16x16x32_bf16 v[220:223], a[0:3], a[60:63], v[220:223]\n" \
"v_mfma_f32_16x16x32_bf16 v[220:223], a[216:219], a[56:59], v[220:223]\n" \
"buffer_load_dwordx4 v5, s[80:83], s73 offen offset:1024 lds\n" \
"v_mfma_f32_16x16x32_bf16 v[152:155], a[4:7], a[32:35], v[152:155]\n" \
"s_waitcnt lgkmcnt(0)\n" \
"v_mfma_f32_16x16x32_bf16 v[152:155], a[8:11], a[36:39], v[152:155]\n" \
"v_mfma_f32_16x16x32_bf16 v[156:159], a[8:11], a[44:47], v[156:159]\n" \
"v_mfma_f32_16x16x32_bf16 v[156:159], a[4:7], a[40:43], v[156:159]\n" \
"v_mfma_f32_16x16x32_bf16 v[224:227], a[4:7], a[48:51], v[224:227]\n" \
"buffer_load_dwordx4 v6, s[80:83], s73 offen offset:2048 lds\n" \
"v_mfma_f32_16x16x32_bf16 v[224:227], a[8:11], a[52:55], v[224:227]\n" \
"v_mfma_f32_16x16x32_bf16 v[228:231], a[8:11], a[60:63], v[228:231]\n" \
"v_mfma_f32_16x16x32_bf16 v[228:231], a[4:7], a[56:59], v[228:231]\n" \
"s_waitcnt vmcnt(22)\n" \
"s_barrier\n" \
"buffer_load_dwordx4 v7, s[80:83], s73 offen offset:3072 lds\n" \
"s_add_u32 m0, s92, 0x2000\n" \
"v_mfma_f32_16x16x32_bf16 v[160:163], a[12:15], a[32:35], v[160:163]\n" \
"ds_read_b64_tr_b16 a[196:197], v10 offset:27648\n" \
"ds_read_b64_tr_b16 a[198:199], v10 offset:32256\n" \
"v_mfma_f32_16x16x32_bf16 v[160:163], a[16:19], a[36:39], v[160:163]\n" \
"ds_read_b64_tr_b16 a[200:201], v10 offset:36864\n" \
"ds_read_b64_tr_b16 a[202:203], v10 offset:41472\n" \
"v_mfma_f32_16x16x32_bf16 v[164:167], a[16:19], a[44:47], v[164:167]\n" \
"ds_read_b64_tr_b16 a[204:205], v10 offset:27680\n" \
"ds_read_b64_tr_b16 a[206:207], v10 offset:32288\n" \
"v_mfma_f32_16x16x32_bf16 v[164:167], a[12:15], a[40:43], v[164:167]\n" \
"buffer_load_dwordx4 v8, s[80:83], s73 offen offset:0 lds\n" \
"s_mov_b32 m0, s94\n" \
"v_mfma_f32_16x16x32_bf16 v[232:235], a[12:15], a[48:51], v[232:235]\n" \
"ds_read_b64_tr_b16 a[208:209], v10 offset:36896\n" \
"ds_read_b64_tr_b16 a[210:211], v10 offset:41504\n" \
"v_mfma_f32_16x16x32_bf16 v[232:235], a[16:19], a[52:55], v[232:235]\n" \
"ds_read_b64_tr_b16 a[212:213], v10 offset:27712\n" \
"ds_read_b64_tr_b16 a[214:215], v10 offset:32320\n" \
"v_mfma_f32_16x16x32_bf16 v[236:239], a[16:19], a[60:63], v[236:239]\n" \
"ds_read_b64_tr_b16 a[216:217], v10 offset:36928\n" \
"ds_read_b64_tr_b16 a[218:219], v10 offset:41536\n" \
"v_mfma_f32_16x16x32_bf16 v[236:239], a[12:15], a[56:59], v[236:239]\n" \
"buffer_load_dwordx4 v9, s[84:87], 0 offen lds\n" \
"v_mfma_f32_16x16x32_bf16 v[168:171], a[20:23], a[32:35], v[168:171]\n" \
"ds_read_b64_tr_b16 a[0:1], v10 offset:27744\n" \
"ds_read_b64_tr_b16 a[2:3], v10 offset:32352\n" \
"v_mfma_f32_16x16x32_bf16 v[168:171], a[24:27], a[36:39], v[168:171]\n" \
"ds_read_b64_tr_b16 a[4:5], v10 offset:36960\n" \
"ds_read_b64_tr_b16 a[6:7], v10 offset:41568\n" \
"v_mfma_f32_16x16x32_bf16 v[172:175], a[24:27], a[44:47], v[172:175]\n" \
"ds_read_b64_tr_b16 a[8:9], v10 offset:27776\n" \
"ds_read_b64_tr_b16 a[10:11], v10 offset:32384\n" \
"v_mfma_f32_16x16x32_bf16 v[172:175], a[20:23], a[40:43], v[172:175]\n" \
"ds_read_b64_tr_b16 a[12:13], v10 offset:36992\n" \
"ds_read_b64_tr_b16 a[14:15], v10 offset:41600\n" \
"buffer_load_dwordx4 a[128:131], v20, s[56:59], s68 offen offset:0 sc1\n" \
"v_mfma_f32_16x16x32_bf16 v[240:243], a[20:23], a[48:51], v[240:243]\n" \
"ds_read_b64_tr_b16 a[16:17], v10 offset:27808\n" \
"ds_read_b64_tr_b16 a[18:19], v10 offset:32416\n" \
"v_mfma_f32_16x16x32_bf16 v[240:243], a[24:27], a[52:55], v[240:243]\n" \
"s_waitcnt vmcnt(12)\n" \
"v_cmp_ne_u32_e32 vcc, s62, v47\n" \
"v_mfma_f32_16x16x32_bf16 v[244:247], a[24:27], a[60:63], v[244:247]\n" \
"ds_read_b64_tr_b16 a[24:25], v10 offset:27840\n" \
"ds_read_b64_tr_b16 a[26:27], v10 offset:32448\n" \
"v_mfma_f32_16x16x32_bf16 v[244:247], a[20:23], a[56:59], v[244:247]\n" \
"ds_read_b64_tr_b16 a[20:21], v10 offset:37024\n" \
"ds_read_b64_tr_b16 a[22:23], v10 offset:41632\n" \
"buffer_load_dwordx4 a[132:135], v20, s[56:59], s68 offen offset:1024 sc1\n" \
"v_mfma_f32_16x16x32_bf16 v[176:179], a[28:31], a[32:35], v[176:179]\n" \
"s_cbranch_vccnz L_sb_%=\n" \
"L_rb_%=:\n" \
"v_readlane_b32 s71, v11, 46\n" \
"v_mfma_f32_16x16x32_bf16 v[176:179], a[192:195], a[36:39], v[176:179]\n" \
"v_readlane_b32 s73, v11, 48\n" \
"v_readlane_b32 s92, v11, 42\n" \
"v_mfma_f32_16x16x32_bf16 v[180:183], a[192:195], a[44:47], v[180:183]\n" \
"v_readlane_b32 s94, v11, 44\n" \
"v_cmp_eq_u32_e32 vcc, 0, v44\n" \
"v_mfma_f32_16x16x32_bf16 v[180:183], a[28:31], a[40:43], v[180:183]\n" \
"v_mfma_f32_16x16x32_bf16 v[248:251], a[28:31], a[48:51], v[248:251]\n" \
"buffer_load_dwordx4 a[136:139], v20, s[56:59], s68 offen offset:2048 sc1\n" \
"v_mfma_f32_16x16x32_bf16 v[248:251], a[192:195], a[52:55], v[248:251]\n" \
"v_cndmask_b32_e32 v50, 0, v45, vcc\n" \
"v_cmp_eq_u32_e32 vcc, 1, v44\n" \
"v_mfma_f32_16x16x32_bf16 v[252:255], a[192:195], a[60:63], v[252:255]\n" \
"ds_read_b64_tr_b16 a[192:193], v10 offset:27872\n" \
"ds_read_b64_tr_b16 a[194:195], v10 offset:32480\n" \
"v_mfma_f32_16x16x32_bf16 v[252:255], a[28:31], a[56:59], v[252:255]\n" \
"ds_read_b64_tr_b16 a[28:29], v10 offset:37056\n" \
"s_waitcnt lgkmcnt(11)\n" \
"v_mfma_f32_16x16x32_bf16 v[112:115], a[196:199], a[64:67], v[112:115]\n" \
"ds_read_b64_tr_b16 a[30:31], v10 offset:41664\n" \
"v_cndmask_b32_e32 v51, 0, v45, vcc\n" \
"buffer_load_dwordx4 a[140:143], v20, s[56:59], s68 offen offset:3072 sc1\n" \
"v_mfma_f32_16x16x32_bf16 v[112:115], a[200:203], a[68:71], v[112:115]\n" \
"v_bfe_u32 v45, v20, 1, 5\n" \
"v_and_b32_e32 v45, 24, v45\n" \
"v_mfma_f32_16x16x32_bf16 v[116:119], a[200:203], a[84:87], v[116:119]\n" \
"ds_read_b64_tr_b16 a[200:201], v10 offset:27904\n" \
"ds_read_b64_tr_b16 a[202:203], v10 offset:32512\n" \
"v_mfma_f32_16x16x32_bf16 v[116:119], a[196:199], a[80:83], v[116:119]\n" \
"ds_read_b64_tr_b16 a[196:197], v10 offset:37088\n" \
"ds_read_b64_tr_b16 a[198:199], v10 offset:41696\n" \
"v_mfma_f32_16x16x32_bf16 v[120:123], a[204:207], a[64:67], v[120:123]\n" \
"v_and_b32_e32 v44, 0x3c0, v20\n" \
"v_add_u32_e32 v45, 0x1d400, v45\n" \
"buffer_load_dwordx4 a[156:159], v32, s[56:59], s68 offen offset:3072 sc1\n" \
"v_mfma_f32_16x16x32_bf16 v[120:123], a[208:211], a[68:71], v[120:123]\n" \
"v_add_u32_e32 v36, v44, v45\n" \
"v_mfma_f32_16x16x32_bf16 v[124:127], a[208:211], a[84:87], v[124:127]\n" \
"ds_read_b64_tr_b16 a[208:209], v10 offset:46080\n" \
"ds_read_b64_tr_b16 a[210:211], v10 offset:50688\n" \
"v_mfma_f32_16x16x32_bf16 v[124:127], a[204:207], a[80:83], v[124:127]\n" \
"ds_read_b64_tr_b16 a[204:205], v10 offset:37120\n" \
"ds_read_b64_tr_b16 a[206:207], v10 offset:41728\n" \
"v_mfma_f32_16x16x32_bf16 v[128:131], a[212:215], a[64:67], v[128:131]\n" \
"v_mfma_f32_16x16x32_bf16 v[128:131], a[216:219], a[68:71], v[128:131]\n" \
"buffer_load_dwordx4 a[152:155], v32, s[56:59], s68 offen offset:2048 sc1\n" \
"v_mfma_f32_16x16x32_bf16 v[132:135], a[216:219], a[84:87], v[132:135]\n" \
"ds_read_b64_tr_b16 a[216:217], v10 offset:46112\n" \
"ds_read_b64_tr_b16 a[218:219], v10 offset:50720\n" \
"v_mfma_f32_16x16x32_bf16 v[132:135], a[212:215], a[80:83], v[132:135]\n" \
"ds_read_b64_tr_b16 a[212:213], v10 offset:55296\n" \
"ds_read_b64_tr_b16 a[214:215], v10 offset:59904\n" \
"v_mfma_f32_16x16x32_bf16 v[136:139], a[0:3], a[64:67], v[136:139]\n" \
"v_mfma_f32_16x16x32_bf16 v[136:139], a[4:7], a[68:71], v[136:139]\n" \
"v_mfma_f32_16x16x32_bf16 v[140:143], a[4:7], a[84:87], v[140:143]\n" \
"buffer_load_dwordx4 a[148:151], v32, s[56:59], s68 offen offset:1024 sc1\n" \
"v_mfma_f32_16x16x32_bf16 v[140:143], a[0:3], a[80:83], v[140:143]\n" \
"ds_read_b64_tr_b16 a[0:1], v10 offset:55328\n" \
"ds_read_b64_tr_b16 a[2:3], v10 offset:59936\n" \
"v_mfma_f32_16x16x32_bf16 v[144:147], a[8:11], a[64:67], v[144:147]\n" \
"ds_read_b64_tr_b16 a[4:5], v10 offset:46144\n" \
"s_waitcnt lgkmcnt(13)\n" \
"v_mfma_f32_16x16x32_bf16 v[144:147], a[12:15], a[68:71], v[144:147]\n" \
"ds_read_b64_tr_b16 a[6:7], v10 offset:50752\n" \
"v_mfma_f32_16x16x32_bf16 v[148:151], a[12:15], a[84:87], v[148:151]\n" \
"ds_read_b64_tr_b16 a[12:13], v10 offset:46176\n" \
"ds_read_b64_tr_b16 a[14:15], v10 offset:50784\n" \
"buffer_load_dwordx4 a[144:147], v32, s[56:59], s68 offen offset:0 sc1\n" \
"v_mfma_f32_16x16x32_bf16 v[148:151], a[8:11], a[80:83], v[148:151]\n" \
"ds_read_b64_tr_b16 a[8:9], v10 offset:55360\n" \
"ds_read_b64_tr_b16 a[10:11], v10 offset:59968\n" \
"v_mfma_f32_16x16x32_bf16 v[152:155], a[16:19], a[64:67], v[152:155]\n" \
"v_readlane_b32 s68, v11, 49\n" \
"v_mfma_f32_16x16x32_bf16 v[152:155], a[20:23], a[68:71], v[152:155]\n" \
"v_mfma_f32_16x16x32_bf16 v[156:159], a[20:23], a[84:87], v[156:159]\n" \
"ds_read_b64_tr_b16 a[20:21], v10 offset:46208\n" \
"ds_read_b64_tr_b16 a[22:23], v10 offset:50816\n" \
"buffer_load_dwordx4 a[96:99], v20, s[56:59], s69 offen offset:0 sc1\n" \
"v_mfma_f32_16x16x32_bf16 v[156:159], a[16:19], a[80:83], v[156:159]\n" \
"ds_read_b64_tr_b16 a[16:17], v10 offset:55392\n" \
"ds_read_b64_tr_b16 a[18:19], v10 offset:60000\n" \
"v_mfma_f32_16x16x32_bf16 v[160:163], a[24:27], a[64:67], v[160:163]\n" \
"v_mfma_f32_16x16x32_bf16 v[160:163], a[28:31], a[68:71], v[160:163]\n" \
"v_mfma_f32_16x16x32_bf16 v[164:167], a[28:31], a[84:87], v[164:167]\n" \
"ds_read_b64_tr_b16 a[28:29], v10 offset:46240\n" \
"ds_read_b64_tr_b16 a[30:31], v10 offset:50848\n" \
"buffer_load_dwordx4 a[100:103], v20, s[56:59], s69 offen offset:1024 sc1\n" \
"v_mfma_f32_16x16x32_bf16 v[164:167], a[24:27], a[80:83], v[164:167]\n" \
"ds_read_b64_tr_b16 a[24:25], v10 offset:55424\n" \
"ds_read_b64_tr_b16 a[26:27], v10 offset:60032\n" \
"v_mfma_f32_16x16x32_bf16 v[168:171], a[192:195], a[64:67], v[168:171]\n" \
"s_waitcnt lgkmcnt(14)\n" \
"v_mfma_f32_16x16x32_bf16 v[168:171], a[196:199], a[68:71], v[168:171]\n" \
"v_mfma_f32_16x16x32_bf16 v[172:175], a[196:199], a[84:87], v[172:175]\n" \
"ds_read_b64_tr_b16 a[196:197], v10 offset:46272\n" \
"ds_read_b64_tr_b16 a[198:199], v10 offset:50880\n" \
"buffer_load_dwordx4 a[104:107], v20, s[56:59], s69 offen offset:2048 sc1\n" \
"v_mfma_f32_16x16x32_bf16 v[172:175], a[192:195], a[80:83], v[172:175]\n" \
"ds_read_b64_tr_b16 a[192:193], v10 offset:55456\n" \
"ds_read_b64_tr_b16 a[194:195], v10 offset:60064\n" \
"v_mfma_f32_16x16x32_bf16 v[176:179], a[200:203], a[64:67], v[176:179]\n" \
"v_mfma_f32_16x16x32_bf16 v[176:179], a[204:207], a[68:71], v[176:179]\n" \
"v_mfma_f32_16x16x32_bf16 v[180:183], a[204:207], a[84:87], v[180:183]\n" \
"ds_read_b64_tr_b16 a[204:205], v10 offset:46304\n" \
"ds_read_b64_tr_b16 a[206:207], v10 offset:50912\n" \
"buffer_load_dwordx4 a[108:111], v20, s[56:59], s69 offen offset:3072 sc1\n" \
"v_mfma_f32_16x16x32_bf16 v[180:183], a[200:203], a[80:83], v[180:183]\n" \
"ds_read_b64_tr_b16 a[200:201], v10 offset:55488\n" \
"ds_read_b64_tr_b16 a[202:203], v10 offset:60096\n" \
"v_mfma_f32_16x16x32_bf16 v[112:115], a[208:211], a[72:75], v[112:115]\n" \
"v_mfma_f32_16x16x32_bf16 v[112:115], a[212:215], a[76:79], v[112:115]\n" \
"v_mfma_f32_16x16x32_bf16 v[116:119], a[212:215], a[92:95], v[116:119]\n" \
"ds_read_b64_tr_b16 a[212:213], v10 offset:46336\n" \
"ds_read_b64_tr_b16 a[214:215], v10 offset:50944\n" \
"buffer_load_dwordx4 a[124:127], v32, s[56:59], s69 offen offset:3072 sc1\n" \
"v_mfma_f32_16x16x32_bf16 v[116:119], a[208:211], a[88:91], v[116:119]\n" \
"ds_read_b64_tr_b16 a[208:209], v10 offset:55520\n" \
"ds_read_b64_tr_b16 a[210:211], v10 offset:60128\n" \
"v_mfma_f32_16x16x32_bf16 v[120:123], a[216:219], a[72:75], v[120:123]\n" \
"v_mfma_f32_16x16x32_bf16 v[120:123], a[0:3], a[76:79], v[120:123]\n" \
"v_mfma_f32_16x16x32_bf16 v[124:127], a[0:3], a[92:95], v[124:127]\n" \
"v_mfma_f32_16x16x32_bf16 v[124:127], a[216:219], a[88:91], v[124:127]\n" \
"ds_read_b64_tr_b16 a[216:217], v10 offset:55552\n" \
"buffer_load_dwordx4 a[120:123], v32, s[56:59], s69 offen offset:2048 sc1\n" \
"s_waitcnt lgkmcnt(11)\n" \
"v_mfma_f32_16x16x32_bf16 v[128:131], a[4:7], a[72:75], v[128:131]\n" \
"ds_read_b64_tr_b16 a[218:219], v10 offset:60160\n" \
"v_mfma_f32_16x16x32_bf16 v[128:131], a[8:11], a[76:79], v[128:131]\n" \
"v_mfma_f32_16x16x32_bf16 v[132:135], a[8:11], a[92:95], v[132:135]\n" \
"v_mfma_f32_16x16x32_bf16 v[132:135], a[4:7], a[88:91], v[132:135]\n" \
"buffer_load_dwordx4 a[116:119], v32, s[56:59], s69 offen offset:1024 sc1\n" \
"v_mfma_f32_16x16x32_bf16 v[136:139], a[12:15], a[72:75], v[136:139]\n" \
"v_mfma_f32_16x16x32_bf16 v[136:139], a[16:19], a[76:79], v[136:139]\n" \
"v_mfma_f32_16x16x32_bf16 v[140:143], a[16:19], a[92:95], v[140:143]\n" \
"v_mfma_f32_16x16x32_bf16 v[140:143], a[12:15], a[88:91], v[140:143]\n" \
"s_waitcnt vmcnt(15)\n" \
"s_barrier\n" \
"buffer_load_dwordx4 a[112:115], v32, s[56:59], s69 offen offset:0 sc1\n" \
"v_mfma_f32_16x16x32_bf16 v[144:147], a[20:23], a[72:75], v[144:147]\n" \
"ds_read_b64_tr_b16 a[0:1], v33 offset:18432\n" \
"ds_read_b64_tr_b16 a[2:3], v33 offset:23040\n" \
"v_mfma_f32_16x16x32_bf16 v[144:147], a[24:27], a[76:79], v[144:147]\n" \
"ds_read_b64_tr_b16 a[4:5], v33 offset:27648\n" \
"ds_read_b64_tr_b16 a[6:7], v33 offset:32256\n" \
"v_mfma_f32_16x16x32_bf16 v[148:151], a[24:27], a[92:95], v[148:151]\n" \
"ds_read_b64_tr_b16 a[8:9], v33 offset:36864\n" \
"ds_read_b64_tr_b16 a[10:11], v33 offset:41472\n" \
"v_mfma_f32_16x16x32_bf16 v[148:151], a[20:23], a[88:91], v[148:151]\n" \
"ds_read_b64_tr_b16 a[12:13], v33 offset:46080\n" \
"ds_read_b64_tr_b16 a[14:15], v33 offset:50688\n" \
"buffer_load_dwordx4 a[32:35], v20, s[56:59], s64 offen offset:0 sc1\n" \
"v_mfma_f32_16x16x32_bf16 v[152:155], a[28:31], a[72:75], v[152:155]\n" \
"ds_read_b64_tr_b16 a[16:17], v34 offset:0\n" \
"s_waitcnt lgkmcnt(9)\n" \
"v_mfma_f32_16x16x32_bf16 v[152:155], a[192:195], a[76:79], v[152:155]\n" \
"ds_read_b64_tr_b16 a[18:19], v34 offset:4608\n" \
"ds_read_b64_tr_b16 a[20:21], v34 offset:9216\n" \
"v_mfma_f32_16x16x32_bf16 v[156:159], a[192:195], a[92:95], v[156:159]\n" \
"ds_read_b64_tr_b16 a[22:23], v34 offset:13824\n" \
"ds_read_b64_tr_b16 a[24:25], v34 offset:18432\n" \
"v_mfma_f32_16x16x32_bf16 v[156:159], a[28:31], a[88:91], v[156:159]\n" \
"ds_read_b64_tr_b16 a[26:27], v34 offset:23040\n" \
"ds_read_b64_tr_b16 a[28:29], v33 offset:0\n" \
"buffer_load_dwordx4 a[36:39], v20, s[56:59], s64 offen offset:1024 sc1\n" \
"v_mfma_f32_16x16x32_bf16 v[160:163], a[196:199], a[72:75], v[160:163]\n" \
"ds_read_b64_tr_b16 a[30:31], v33 offset:4608\n" \
"ds_read_b64_tr_b16 a[192:193], v33 offset:18464\n" \
"v_mfma_f32_16x16x32_bf16 v[160:163], a[200:203], a[76:79], v[160:163]\n" \
"ds_read_b64_tr_b16 a[194:195], v33 offset:23072\n" \
"v_readlane_b32 s69, v11, 5\n" \
"v_mfma_f32_16x16x32_bf16 v[164:167], a[200:203], a[92:95], v[164:167]\n" \
"ds_read_b64_tr_b16 a[200:201], v33 offset:36896\n" \
"ds_read_b64_tr_b16 a[202:203], v33 offset:41504\n" \
"v_mfma_f32_16x16x32_bf16 v[164:167], a[196:199], a[88:91], v[164:167]\n" \
"ds_read_b64_tr_b16 a[196:197], v33 offset:27680\n" \
"ds_read_b64_tr_b16 a[198:199], v33 offset:32288\n" \
"buffer_load_dwordx4 a[40:43], v20, s[56:59], s64 offen offset:2048 sc1\n" \
"v_mfma_f32_16x16x32_bf16 v[168:171], a[204:207], a[72:75], v[168:171]\n" \
"v_mfma_f32_16x16x32_bf16 v[168:171], a[208:211], a[76:79], v[168:171]\n" \
"v_mfma_f32_16x16x32_bf16 v[172:175], a[208:211], a[92:95], v[172:175]\n" \
"ds_read_b64_tr_b16 a[208:209], v34 offset:32\n" \
"ds_read_b64_tr_b16 a[210:211], v34 offset:4640\n" \
"v_mfma_f32_16x16x32_bf16 v[172:175], a[204:207], a[88:91], v[172:175]\n" \
"ds_read_b64_tr_b16 a[204:205], v33 offset:46112\n" \
"ds_read_b64_tr_b16 a[206:207], v33 offset:50720\n" \
"buffer_load_dwordx4 a[44:47], v20, s[56:59], s64 offen offset:3072 sc1\n" \
"v_mfma_f32_16x16x32_bf16 v[176:179], a[212:215], a[72:75], v[176:179]\n" \
"v_mfma_f32_16x16x32_bf16 v[176:179], a[216:219], a[76:79], v[176:179]\n" \
"v_mfma_f32_16x16x32_bf16 v[180:183], a[216:219], a[92:95], v[180:183]\n" \
"ds_read_b64_tr_b16 a[216:217], v34 offset:18464\n" \
"ds_read_b64_tr_b16 a[218:219], v34 offset:23072\n" \
"v_mfma_f32_16x16x32_bf16 v[180:183], a[212:215], a[88:91], v[180:183]\n" \
"ds_read_b64_tr_b16 a[212:213], v34 offset:9248\n" \
"buffer_load_dwordx4 a[60:63], v32, s[56:59], s64 offen offset:3072 sc1\n" \
"s_waitcnt vmcnt(20) lgkmcnt(14)\n" \
"v_mfma_f32_16x16x32_bf16 v[112:115], a[0:3], a[128:131], v[112:115]\n" \
"ds_read_b64_tr_b16 a[214:215], v34 offset:13856\n" \
"s_waitcnt vmcnt(19)\n" \
"v_mfma_f32_16x16x32_bf16 v[112:115], a[4:7], a[132:135], v[112:115]\n" \
"s_waitcnt vmcnt(18)\n" \
"v_mfma_f32_16x16x32_bf16 v[112:115], a[8:11], a[136:139], v[112:115]\n" \
"s_waitcnt vmcnt(17)\n" \
"v_mfma_f32_16x16x32_bf16 v[112:115], a[12:15], a[140:143], v[112:115]\n" \
"buffer_load_dwordx4 a[56:59], v32, s[56:59], s64 offen offset:2048 sc1\n" \
"s_waitcnt vmcnt(17)\n" \
"v_mfma_f32_16x16x32_bf16 v[116:119], a[12:15], a[156:159], v[116:119]\n" \
"ds_read_b64_tr_b16 a[12:13], v33 offset:36928\n" \
"s_waitcnt vmcnt(16)\n" \
"v_mfma_f32_16x16x32_bf16 v[116:119], a[8:11], a[152:155], v[116:119]\n" \
"ds_read_b64_tr_b16 a[8:9], v33 offset:27712\n" \
"ds_read_b64_tr_b16 a[10:11], v33 offset:32320\n" \
"ds_read_b64_tr_b16 a[14:15], v33 offset:41536\n" \
"s_waitcnt vmcnt(15)\n" \
"v_mfma_f32_16x16x32_bf16 v[116:119], a[4:7], a[148:151], v[116:119]\n" \
"buffer_load_dwordx4 a[52:55], v32, s[56:59], s64 offen offset:1024 sc1\n" \
"ds_read_b64_tr_b16 a[4:5], v33 offset:18496\n" \
"ds_read_b64_tr_b16 a[6:7], v33 offset:23104\n" \
"s_waitcnt vmcnt(15)\n" \
"v_mfma_f32_16x16x32_bf16 v[116:119], a[0:3], a[144:147], v[116:119]\n" \
"buffer_load_dwordx4 a[48:51], v32, s[56:59], s64 offen offset:0 sc1\n" \
"ds_read_b64_tr_b16 a[0:1], v33 offset:32\n" \
"ds_read_b64_tr_b16 a[2:3], v33 offset:4640\n" \
"v_readlane_b32 s64, v11, 38\n" \
"s_waitcnt vmcnt(15)\n" \
"v_mfma_f32_16x16x32_bf16 v[112:115], a[16:19], a[96:99], v[112:115]\n" \
"buffer_load_dwordx4 a[160:163], v20, s[56:59], s72 offen offset:0 sc1\n" \
"s_waitcnt vmcnt(15)\n" \
"v_mfma_f32_16x16x32_bf16 v[112:115], a[20:23], a[100:103], v[112:115]\n" \
"buffer_load_dwordx4 a[164:167], v20, s[56:59], s72 offen offset:1024 sc1\n" \
"s_waitcnt vmcnt(15)\n" \
"v_mfma_f32_16x16x32_bf16 v[112:115], a[24:27], a[104:107], v[112:115]\n" \
"buffer_load_dwordx4 a[168:171], v20, s[56:59], s72 offen offset:2048 sc1\n" \
"s_waitcnt vmcnt(15) lgkmcnt(0)\n" \
"v_mfma_f32_16x16x32_bf16 v[112:115], a[28:31], a[108:111], v[112:115]\n" \
"buffer_load_dwordx4 a[172:175], v20, s[56:59], s72 offen offset:3072 sc1\n" \
"s_waitcnt vmcnt(15)\n" \
"v_mfma_f32_16x16x32_bf16 v[116:119], a[28:31], a[124:127], v[116:119]\n" \
"buffer_load_dwordx4 a[188:191], v32, s[56:59], s72 offen offset:3072 sc1\n" \
"ds_read_b64_tr_b16 a[28:29], v34 offset:18496\n" \
"ds_read_b64_tr_b16 a[30:31], v34 offset:23104\n" \
"s_waitcnt vmcnt(15)\n" \
"v_mfma_f32_16x16x32_bf16 v[116:119], a[24:27], a[120:123], v[116:119]\n" \
"v_cvt_pk_bf16_f32 v112, v112, v113\n" \
"v_cvt_pk_bf16_f32 v113, v114, v115\n" \
"buffer_load_dwordx4 a[184:187], v32, s[56:59], s72 offen offset:2048 sc1\n" \
"ds_read_b64_tr_b16 a[24:25], v34 offset:9280\n" \
"ds_read_b64_tr_b16 a[26:27], v34 offset:13888\n" \
"s_waitcnt vmcnt(15)\n" \
"v_mfma_f32_16x16x32_bf16 v[116:119], a[20:23], a[116:119], v[116:119]\n" \
"ds_read_b64_tr_b16 a[20:21], v34 offset:64\n" \
"ds_read_b64_tr_b16 a[22:23], v34 offset:4672\n" \
"buffer_load_dwordx4 a[180:183], v32, s[56:59], s72 offen offset:1024 sc1\n" \
"s_waitcnt vmcnt(15)\n" \
"v_mfma_f32_16x16x32_bf16 v[116:119], a[16:19], a[112:115], v[116:119]\n" \
"buffer_load_dwordx4 a[176:179], v32, s[56:59], s72 offen offset:0 sc1\n" \
"v_mfma_f32_16x16x32_bf16 v[120:123], a[192:195], a[128:131], v[120:123]\n" \
"ds_read_b64_tr_b16 a[16:17], v33 offset:46144\n" \
"ds_read_b64_tr_b16 a[18:19], v33 offset:50752\n" \
"v_mfma_f32_16x16x32_bf16 v[120:123], a[196:199], a[132:135], v[120:123]\n" \
"v_readlane_b32 s72, v11, 47\n" \
"v_mfma_f32_16x16x32_bf16 v[120:123], a[200:203], a[136:139], v[120:123]\n" \
"v_mfma_f32_16x16x32_bf16 v[120:123], a[204:207], a[140:143], v[120:123]\n" \
"v_cvt_pk_bf16_f32 v114, v116, v117\n" \
"v_cvt_pk_bf16_f32 v115, v118, v119\n" \
"buffer_store_dwordx4 v[112:115], v20, s[56:59], s65 offen offset:0 sc1\n" \
"s_add_u32 s65, s65, 0x3800\n" \
"v_mfma_f32_16x16x32_bf16 v[124:127], a[204:207], a[156:159], v[124:127]\n" \
"ds_read_b64_tr_b16 a[204:205], v10 offset:46336\n" \
"ds_read_b64_tr_b16 a[206:207], v10 offset:50944\n" \
"v_mfma_f32_16x16x32_bf16 v[124:127], a[200:203], a[152:155], v[124:127]\n" \
"ds_read_b64_tr_b16 a[200:201], v10 offset:37120\n" \
"ds_read_b64_tr_b16 a[202:203], v10 offset:41728\n" \
"v_mfma_f32_16x16x32_bf16 v[124:127], a[196:199], a[148:151], v[124:127]\n" \
"ds_read_b64_tr_b16 a[196:197], v10 offset:27904\n" \
"ds_read_b64_tr_b16 a[198:199], v10 offset:32512\n" \
"v_mfma_f32_16x16x32_bf16 v[124:127], a[192:195], a[144:147], v[124:127]\n" \
"buffer_load_dwordx4 a[220:223], v20, s[56:59], s93 offen offset:0 sc1\n" \
"v_mfma_f32_16x16x32_bf16 v[120:123], a[208:211], a[96:99], v[120:123]\n" \
"ds_read_b64_tr_b16 a[192:193], v33 offset:64\n" \
"ds_read_b64_tr_b16 a[194:195], v33 offset:4672\n" \
"v_mfma_f32_16x16x32_bf16 v[120:123], a[212:215], a[100:103], v[120:123]\n" \
"v_mfma_f32_16x16x32_bf16 v[120:123], a[216:219], a[104:107], v[120:123]\n" \
"v_mfma_f32_16x16x32_bf16 v[120:123], a[0:3], a[108:111], v[120:123]\n" \
"v_mfma_f32_16x16x32_bf16 v[124:127], a[0:3], a[124:127], v[124:127]\n" \
"buffer_load_dwordx4 a[224:227], v20, s[56:59], s93 offen offset:1024 sc1\n" \
"v_mfma_f32_16x16x32_bf16 v[124:127], a[216:219], a[120:123], v[124:127]\n" \
"ds_read_b64_tr_b16 a[216:217], v33 offset:27904\n" \
"ds_read_b64_tr_b16 a[218:219], v33 offset:32512\n" \
"v_mfma_f32_16x16x32_bf16 v[124:127], a[212:215], a[116:119], v[124:127]\n" \
"ds_read_b64_tr_b16 a[212:213], v33 offset:18688\n" \
"ds_read_b64_tr_b16 a[214:215], v33 offset:23296\n" \
"v_cvt_pk_bf16_f32 v120, v120, v121\n" \
"v_cvt_pk_bf16_f32 v121, v122, v123\n" \
"v_mfma_f32_16x16x32_bf16 v[124:127], a[208:211], a[112:115], v[124:127]\n" \
"ds_read_b64_tr_b16 a[208:209], v10 offset:55552\n" \
"ds_read_b64_tr_b16 a[210:211], v10 offset:60160\n" \
"v_mfma_f32_16x16x32_bf16 v[128:131], a[4:7], a[128:131], v[128:131]\n" \
"buffer_load_dwordx4 a[228:231], v20, s[56:59], s93 offen offset:2048 sc1\n" \
"v_mfma_f32_16x16x32_bf16 v[128:131], a[8:11], a[132:135], v[128:131]\n" \
"ds_read_b64_tr_b16 a[0:1], v33 offset:37120\n" \
"ds_read_b64_tr_b16 a[2:3], v33 offset:41728\n" \
"v_mfma_f32_16x16x32_bf16 v[128:131], a[12:15], a[136:139], v[128:131]\n" \
"v_cvt_pk_bf16_f32 v122, v124, v125\n" \
"v_cvt_pk_bf16_f32 v123, v126, v127\n" \
"buffer_store_dwordx4 v[120:123], v20, s[56:59], s66 offen offset:0 sc1\n" \
"s_add_u32 s66, s66, 0x3000\n" \
"s_waitcnt lgkmcnt(12)\n" \
"v_mfma_f32_16x16x32_bf16 v[128:131], a[16:19], a[140:143], v[128:131]\n" \
"v_mfma_f32_16x16x32_bf16 v[132:135], a[16:19], a[156:159], v[132:135]\n" \
"ds_read_b64_tr_b16 a[16:17], v34 offset:18688\n" \
"ds_read_b64_tr_b16 a[18:19], v34 offset:23296\n" \
"v_mfma_f32_16x16x32_bf16 v[132:135], a[12:15], a[152:155], v[132:135]\n" \
"ds_read_b64_tr_b16 a[12:13], v34 offset:9472\n" \
"ds_read_b64_tr_b16 a[14:15], v34 offset:14080\n" \
"v_mfma_f32_16x16x32_bf16 v[132:135], a[8:11], a[148:151], v[132:135]\n" \
"buffer_load_dwordx4 a[232:235], v20, s[56:59], s93 offen offset:3072 sc1\n" \
"v_mfma_f32_16x16x32_bf16 v[132:135], a[4:7], a[144:147], v[132:135]\n" \
"ds_read_b64_tr_b16 a[4:5], v33 offset:46336\n" \
"ds_read_b64_tr_b16 a[6:7], v33 offset:50944\n" \
"v_mfma_f32_16x16x32_bf16 v[128:131], a[20:23], a[96:99], v[128:131]\n" \
"ds_read_b64_tr_b16 a[8:9], v34 offset:256\n" \
"ds_read_b64_tr_b16 a[10:11], v34 offset:4864\n" \
"v_mfma_f32_16x16x32_bf16 v[128:131], a[24:27], a[100:103], v[128:131]\n" \
"v_mfma_f32_16x16x32_bf16 v[128:131], a[28:31], a[104:107], v[128:131]\n" \
"s_waitcnt lgkmcnt(14)\n" \
"v_mfma_f32_16x16x32_bf16 v[128:131], a[192:195], a[108:111], v[128:131]\n" \
"buffer_load_dwordx4 a[248:251], v32, s[56:59], s93 offen offset:3072 sc1\n" \
"v_mfma_f32_16x16x32_bf16 v[132:135], a[192:195], a[124:127], v[132:135]\n" \
"ds_read_b64_tr_b16 a[192:193], v10 offset:46176\n" \
"ds_read_b64_tr_b16 a[194:195], v10 offset:50784\n" \
"v_mfma_f32_16x16x32_bf16 v[132:135], a[28:31], a[120:123], v[132:135]\n" \
"ds_read_b64_tr_b16 a[28:29], v10 offset:36960\n" \
"ds_read_b64_tr_b16 a[30:31], v10 offset:41568\n" \
"v_mfma_f32_16x16x32_bf16 v[132:135], a[24:27], a[116:119], v[132:135]\n" \
"v_cvt_pk_bf16_f32 v128, v128, v129\n" \
"v_cvt_pk_bf16_f32 v129, v130, v131\n" \
"v_mfma_f32_16x16x32_bf16 v[132:135], a[20:23], a[112:115], v[132:135]\n" \
"ds_read_b64_tr_b16 a[20:21], v33 offset:256\n" \
"buffer_load_dwordx4 a[244:247], v32, s[56:59], s93 offen offset:2048 sc1\n" \
"s_waitcnt vmcnt(23)\n" \
"v_mfma_f32_16x16x32_bf16 v[248:251], a[196:199], a[32:35], v[248:251]\n" \
"ds_read_b64_tr_b16 a[22:23], v33 offset:4864\n" \
"s_waitcnt vmcnt(22)\n" \
"v_mfma_f32_16x16x32_bf16 v[248:251], a[200:203], a[36:39], v[248:251]\n" \
"ds_read_b64_tr_b16 a[24:25], v10 offset:27744\n" \
"v_cvt_pk_bf16_f32 v130, v132, v133\n" \
"v_cvt_pk_bf16_f32 v131, v134, v135\n" \
"buffer_store_dwordx4 v[128:131], v20, s[56:59], s67 offen offset:0 sc1\n" \
"s_add_u32 s67, s67, 0x2800\n" \
"s_waitcnt vmcnt(22)\n" \
"v_mfma_f32_16x16x32_bf16 v[248:251], a[204:207], a[40:43], v[248:251]\n" \
"ds_read_b64_tr_b16 a[26:27], v10 offset:32352\n" \
"s_waitcnt vmcnt(21) lgkmcnt(12)\n" \
"v_mfma_f32_16x16x32_bf16 v[248:251], a[208:211], a[44:47], v[248:251]\n" \
"s_waitcnt vmcnt(20)\n" \
"v_mfma_f32_16x16x32_bf16 v[252:255], a[208:211], a[60:63], v[252:255]\n" \
"ds_read_b64_tr_b16 a[208:209], v33 offset:36960\n" \
"s_waitcnt vmcnt(19)\n" \
"v_mfma_f32_16x16x32_bf16 v[252:255], a[204:207], a[56:59], v[252:255]\n" \
"ds_read_b64_tr_b16 a[204:205], v33 offset:27744\n" \
"buffer_load_dwordx4 a[240:243], v32, s[56:59], s93 offen offset:1024 sc1\n" \
"s_waitcnt vmcnt(19)\n" \
"v_mfma_f32_16x16x32_bf16 v[252:255], a[200:203], a[52:55], v[252:255]\n" \
"ds_read_b64_tr_b16 a[200:201], v33 offset:18528\n" \
"s_waitcnt vmcnt(18)\n" \
"v_mfma_f32_16x16x32_bf16 v[252:255], a[196:199], a[48:51], v[252:255]\n" \
"ds_read_b64_tr_b16 a[196:197], v10 offset:55392\n" \
"L_pf21:\n" \
"s_cbranch_execz L_retB\n" \
"s_nop 0\n" \
"ds_read_b64_tr_b16 a[198:199], v10 offset:60000\n" \
"v_mfma_f32_16x16x32_bf16 v[176:179], a[212:215], a[128:131], v[176:179]\n" \
"ds_read_b64_tr_b16 a[202:203], v33 offset:23136\n" \
"ds_read_b64_tr_b16 a[206:207], v33 offset:32352\n" \
"v_mfma_f32_16x16x32_bf16 v[176:179], a[216:219], a[132:135], v[176:179]\n" \
"buffer_load_dwordx4 a[236:239], v32, s[56:59], s93 offen offset:0 sc1\n" \
"v_mfma_f32_16x16x32_bf16 v[176:179], a[0:3], a[136:139], v[176:179]\n" \
"ds_read_b64_tr_b16 a[210:211], v33 offset:41568\n" \
"s_waitcnt lgkmcnt(12)\n" \
"v_mfma_f32_16x16x32_bf16 v[176:179], a[4:7], a[140:143], v[176:179]\n" \
"v_readlane_b32 s93, v11, 43\n" \
"v_mfma_f32_16x16x32_bf16 v[180:183], a[4:7], a[156:159], v[180:183]\n" \
"v_mfma_f32_16x16x32_bf16 v[180:183], a[0:3], a[152:155], v[180:183]\n" \
"v_mfma_f32_16x16x32_bf16 v[180:183], a[216:219], a[148:151], v[180:183]\n" \
"buffer_load_dwordx4 v[64:67], v20, s[56:59], s95 offen offset:0 sc1\n" \
"v_mfma_f32_16x16x32_bf16 v[180:183], a[212:215], a[144:147], v[180:183]\n" \
"s_waitcnt vmcnt(19)\n" \
"v_mfma_f32_16x16x32_bf16 v[248:251], a[212:215], a[160:163], v[248:251]\n" \
"s_waitcnt vmcnt(18)\n" \
"v_mfma_f32_16x16x32_bf16 v[248:251], a[216:219], a[164:167], v[248:251]\n" \
"s_waitcnt vmcnt(17)\n" \
"v_mfma_f32_16x16x32_bf16 v[248:251], a[0:3], a[168:171], v[248:251]\n" \
"s_waitcnt vmcnt(16)\n" \
"v_mfma_f32_16x16x32_bf16 v[248:251], a[4:7], a[172:175], v[248:251]\n" \
"buffer_load_dwordx4 v[72:75], v32, s[56:59], s95 offen offset:0 sc1\n" \
"s_waitcnt vmcnt(16)\n" \
"v_mfma_f32_16x16x32_bf16 v[252:255], a[4:7], a[188:191], v[252:255]\n" \
"ds_read_b64_tr_b16 a[4:5], v34 offset:18528\n" \
"ds_read_b64_tr_b16 a[6:7], v34 offset:23136\n" \
"s_waitcnt vmcnt(15)\n" \
"v_mfma_f32_16x16x32_bf16 v[252:255], a[0:3], a[184:187], v[252:255]\n" \
"ds_read_b64_tr_b16 a[0:1], v34 offset:9312\n" \
"ds_read_b64_tr_b16 a[2:3], v34 offset:13920\n" \
"buffer_load_dwordx4 v[80:83], v20, s[56:59], s96 offen offset:0 sc1\n" \
"s_waitcnt vmcnt(15)\n" \
"v_mfma_f32_16x16x32_bf16 v[252:255], a[216:219], a[180:183], v[252:255]\n" \
"ds_read_b64_tr_b16 a[216:217], v34 offset:96\n" \
"ds_read_b64_tr_b16 a[218:219], v34 offset:4704\n" \
"buffer_load_dwordx4 v[88:91], v32, s[56:59], s96 offen offset:0 sc1\n" \
"s_waitcnt vmcnt(15)\n" \
"v_mfma_f32_16x16x32_bf16 v[252:255], a[212:215], a[176:179], v[252:255]\n" \
"ds_read_b64_tr_b16 a[212:213], v33 offset:46176\n" \
"ds_read_b64_tr_b16 a[214:215], v33 offset:50784\n" \
"v_mfma_f32_16x16x32_bf16 v[176:179], a[8:11], a[96:99], v[176:179]\n" \
"buffer_load_dwordx4 v[68:71], v20, s[56:59], s95 offen offset:1024 sc1\n" \
"v_mfma_f32_16x16x32_bf16 v[176:179], a[12:15], a[100:103], v[176:179]\n" \
"v_mfma_f32_16x16x32_bf16 v[176:179], a[16:19], a[104:107], v[176:179]\n" \
"s_waitcnt lgkmcnt(2)\n" \
"v_mfma_f32_16x16x32_bf16 v[176:179], a[20:23], a[108:111], v[176:179]\n" \
"s_waitcnt vmcnt(15)\n" \
"s_mov_b64 exec, 1\n" \
"buffer_store_dword v39, off, s[56:59], s65 offset:0 sc1\n" \
"s_mov_b64 exec, -1\n" \
"v_mfma_f32_16x16x32_bf16 v[180:183], a[20:23], a[124:127], v[180:183]\n" \
"s_add_u32 s65, s63, 0x1800\n" \
"v_mfma_f32_16x16x32_bf16 v[180:183], a[16:19], a[120:123], v[180:183]\n" \
"v_mfma_f32_16x16x32_bf16 v[180:183], a[12:15], a[116:119], v[180:183]\n" \
"v_cvt_pk_bf16_f32 v176, v176, v177\n" \
"v_cvt_pk_bf16_f32 v177, v178, v179\n" \
"v_mfma_f32_16x16x32_bf16 v[180:183], a[8:11], a[112:115], v[180:183]\n" \
"s_waitcnt vmcnt(15)\n" \
"v_mfma_f32_16x16x32_bf16 v[248:251], a[8:11], a[220:223], v[248:251]\n" \
"buffer_load_dwordx4 v[76:79], v32, s[56:59], s95 offen offset:1024 sc1\n" \
"s_add_u32 s95, s63, 0x0\n" \
"s_waitcnt vmcnt(15)\n" \
"v_mfma_f32_16x16x32_bf16 v[248:251], a[12:15], a[224:227], v[248:251]\n" \
"s_waitcnt vmcnt(14)\n" \
"v_mfma_f32_16x16x32_bf16 v[248:251], a[16:19], a[228:231], v[248:251]\n" \
"v_cvt_pk_bf16_f32 v178, v180, v181\n" \
"v_cvt_pk_bf16_f32 v179, v182, v183\n" \
"buffer_store_dwordx4 v[176:179], v20, s[56:59], s68 offen offset:0 sc1\n" \
"s_waitcnt vmcnt(13)\n" \
"v_mfma_f32_16x16x32_bf16 v[248:251], a[20:23], a[232:235], v[248:251]\n" \
"s_mov_b64 exec, 1\n" \
"buffer_store_dword v39, off, s[56:59], s66 offset:4 sc1\n" \
"s_mov_b64 exec, -1\n" \
"s_add_u32 s66, s63, 0x2000\n" \
"s_waitcnt vmcnt(13)\n" \
"v_mfma_f32_16x16x32_bf16 v[252:255], a[20:23], a[248:251], v[252:255]\n" \
"ds_read_b64_tr_b16 a[20:21], v10 offset:46208\n" \
"ds_read_b64_tr_b16 a[22:23], v10 offset:50816\n" \
"v_cvt_pk_bf16_f32 v248, v248, v249\n" \
"v_cvt_pk_bf16_f32 v249, v250, v251\n" \
"buffer_load_dwordx4 v[84:87], v20, s[56:59], s96 offen offset:1024 sc1\n" \
"s_waitcnt vmcnt(13)\n" \
"v_mfma_f32_16x16x32_bf16 v[252:255], a[16:19], a[244:247], v[252:255]\n" \
"ds_read_b64_tr_b16 a[16:17], v10 offset:36992\n" \
"ds_read_b64_tr_b16 a[18:19], v10 offset:41600\n" \
"s_waitcnt vmcnt(11)\n" \
"v_mfma_f32_16x16x32_bf16 v[252:255], a[12:15], a[240:243], v[252:255]\n" \
"s_mov_b64 exec, 1\n" \
"buffer_store_dword v39, off, s[56:59], s67 offset:8 sc1\n" \
"s_mov_b64 exec, -1\n" \
"ds_read_b64_tr_b16 a[12:13], v10 offset:27776\n" \
"ds_read_b64_tr_b16 a[14:15], v10 offset:32384\n" \
"s_add_u32 s67, s63, 0x2800\n" \
"s_waitcnt vmcnt(11)\n" \
"v_mfma_f32_16x16x32_bf16 v[252:255], a[8:11], a[236:239], v[252:255]\n" \
"ds_read_b64_tr_b16 a[8:9], v33 offset:96\n" \
"ds_read_b64_tr_b16 a[10:11], v33 offset:4704\n" \
"v_mfma_f32_16x16x32_bf16 v[208:211], a[24:27], a[32:35], v[208:211]\n" \
"buffer_load_dwordx4 v[92:95], v32, s[56:59], s96 offen offset:1024 sc1\n" \
"v_mfma_f32_16x16x32_bf16 v[208:211], a[28:31], a[36:39], v[208:211]\n" \
"s_add_u32 s96, s63, 0x800\n" \
"v_mfma_f32_16x16x32_bf16 v[208:211], a[192:195], a[40:43], v[208:211]\n" \
"v_mfma_f32_16x16x32_bf16 v[208:211], a[196:199], a[44:47], v[208:211]\n" \
"v_cvt_pk_bf16_f32 v250, v252, v253\n" \
"v_cvt_pk_bf16_f32 v251, v254, v255\n" \
"buffer_store_dwordx4 v[248:251], v20, s[56:59], s68 offen offset:1024 sc1\n" \
"v_mfma_f32_16x16x32_bf16 v[212:215], a[196:199], a[60:63], v[212:215]\n" \
"ds_read_b64_tr_b16 a[196:197], v33 offset:36992\n" \
"ds_read_b64_tr_b16 a[198:199], v33 offset:41600\n" \
"v_mfma_f32_16x16x32_bf16 v[212:215], a[192:195], a[56:59], v[212:215]\n" \
"ds_read_b64_tr_b16 a[192:193], v33 offset:27776\n" \
"ds_read_b64_tr_b16 a[194:195], v33 offset:32384\n" \
"v_mfma_f32_16x16x32_bf16 v[212:215], a[28:31], a[52:55], v[212:215]\n" \
"ds_read_b64_tr_b16 a[28:29], v33 offset:18560\n" \
"ds_read_b64_tr_b16 a[30:31], v33 offset:23168\n" \
"v_mfma_f32_16x16x32_bf16 v[212:215], a[24:27], a[48:51], v[212:215]\n" \
"ds_read_b64_tr_b16 a[24:25], v10 offset:55424\n" \
"ds_read_b64_tr_b16 a[26:27], v10 offset:60032\n" \
"v_mfma_f32_16x16x32_bf16 v[136:139], a[200:203], a[128:131], v[136:139]\n" \
"buffer_load_dwordx4 v[96:99], v20, s[56:59], s97 offen offset:0 sc1\n" \
"v_mfma_f32_16x16x32_bf16 v[136:139], a[204:207], a[132:135], v[136:139]\n" \
"s_add_u32 s68, s63, 0x3000\n" \
"v_mfma_f32_16x16x32_bf16 v[136:139], a[208:211], a[136:139], v[136:139]\n" \
"s_waitcnt lgkmcnt(10)\n" \
"v_mfma_f32_16x16x32_bf16 v[136:139], a[212:215], a[140:143], v[136:139]\n" \
"v_mfma_f32_16x16x32_bf16 v[140:143], a[212:215], a[156:159], v[140:143]\n" \
"v_mfma_f32_16x16x32_bf16 v[140:143], a[208:211], a[152:155], v[140:143]\n" \
"buffer_load_dwordx4 v[104:107], v32, s[56:59], s97 offen offset:0 sc1\n" \
"v_mfma_f32_16x16x32_bf16 v[140:143], a[204:207], a[148:151], v[140:143]\n" \
"v_mfma_f32_16x16x32_bf16 v[140:143], a[200:203], a[144:147], v[140:143]\n" \
"v_mfma_f32_16x16x32_bf16 v[208:211], a[200:203], a[160:163], v[208:211]\n" \
"v_mfma_f32_16x16x32_bf16 v[208:211], a[204:207], a[164:167], v[208:211]\n" \
"v_mfma_f32_16x16x32_bf16 v[208:211], a[208:211], a[168:171], v[208:211]\n" \
"buffer_load_dwordx4 v[100:103], v20, s[56:59], s97 offen offset:1024 sc1\n" \
"v_mfma_f32_16x16x32_bf16 v[208:211], a[212:215], a[172:175], v[208:211]\n" \
"v_mfma_f32_16x16x32_bf16 v[212:215], a[212:215], a[188:191], v[212:215]\n" \
"ds_read_b64_tr_b16 a[212:213], v34 offset:18560\n" \
"ds_read_b64_tr_b16 a[214:215], v34 offset:23168\n" \
"v_mfma_f32_16x16x32_bf16 v[212:215], a[208:211], a[184:187], v[212:215]\n" \
"ds_read_b64_tr_b16 a[208:209], v34 offset:9344\n" \
"ds_read_b64_tr_b16 a[210:211], v34 offset:13952\n" \
"v_mfma_f32_16x16x32_bf16 v[212:215], a[204:207], a[180:183], v[212:215]\n" \
"ds_read_b64_tr_b16 a[204:205], v34 offset:128\n" \
"ds_read_b64_tr_b16 a[206:207], v34 offset:4736\n" \
"v_mfma_f32_16x16x32_bf16 v[212:215], a[200:203], a[176:179], v[212:215]\n" \
"ds_read_b64_tr_b16 a[200:201], v33 offset:46208\n" \
"ds_read_b64_tr_b16 a[202:203], v33 offset:50816\n" \
"v_mfma_f32_16x16x32_bf16 v[136:139], a[216:219], a[96:99], v[136:139]\n" \
"buffer_load_dwordx4 v[108:111], v32, s[56:59], s97 offen offset:1024 sc1\n" \
"v_mfma_f32_16x16x32_bf16 v[136:139], a[0:3], a[100:103], v[136:139]\n" \
"s_add_u32 s97, s63, 0x1000\n" \
"v_mfma_f32_16x16x32_bf16 v[136:139], a[4:7], a[104:107], v[136:139]\n" \
"s_waitcnt lgkmcnt(8)\n" \
"v_mfma_f32_16x16x32_bf16 v[136:139], a[8:11], a[108:111], v[136:139]\n" \
"v_mfma_f32_16x16x32_bf16 v[140:143], a[8:11], a[124:127], v[140:143]\n" \
"v_mfma_f32_16x16x32_bf16 v[140:143], a[4:7], a[120:123], v[140:143]\n" \
"v_mfma_f32_16x16x32_bf16 v[140:143], a[0:3], a[116:119], v[140:143]\n" \
"v_mfma_f32_16x16x32_bf16 v[140:143], a[216:219], a[112:115], v[140:143]\n" \
"v_mfma_f32_16x16x32_bf16 v[208:211], a[216:219], a[220:223], v[208:211]\n" \
"v_mfma_f32_16x16x32_bf16 v[208:211], a[0:3], a[224:227], v[208:211]\n" \
"v_mfma_f32_16x16x32_bf16 v[208:211], a[4:7], a[228:231], v[208:211]\n" \
"v_mfma_f32_16x16x32_bf16 v[208:211], a[8:11], a[232:235], v[208:211]\n" \
"v_cvt_pk_bf16_f32 v136, v136, v137\n" \
"v_cvt_pk_bf16_f32 v137, v138, v139\n" \
"v_mfma_f32_16x16x32_bf16 v[212:215], a[8:11], a[248:251], v[212:215]\n" \
"ds_read_b64_tr_b16 a[8:9], v10 offset:46240\n" \
"v_cvt_pk_bf16_f32 v138, v140, v141\n" \
"v_cvt_pk_bf16_f32 v139, v142, v143\n" \
"buffer_store_dwordx4 v[136:139], v20, s[56:59], s70 offen offset:0 sc1\n" \
"v_mfma_f32_16x16x32_bf16 v[212:215], a[4:7], a[244:247], v[212:215]\n" \
"v_cvt_pk_bf16_f32 v208, v208, v209\n" \
"v_cvt_pk_bf16_f32 v209, v210, v211\n" \
"v_mfma_f32_16x16x32_bf16 v[212:215], a[0:3], a[240:243], v[212:215]\n" \
"ds_read_b64_tr_b16 a[0:1], v10 offset:27808\n" \
"ds_read_b64_tr_b16 a[2:3], v10 offset:32416\n" \
"v_mfma_f32_16x16x32_bf16 v[212:215], a[216:219], a[236:239], v[212:215]\n" \
"ds_read_b64_tr_b16 a[216:217], v33 offset:128\n" \
"ds_read_b64_tr_b16 a[218:219], v33 offset:4736\n" \
"v_mfma_f32_16x16x32_bf16 v[216:219], a[12:15], a[32:35], v[216:219]\n" \
"ds_read_b64_tr_b16 a[4:5], v10 offset:37024\n" \
"ds_read_b64_tr_b16 a[6:7], v10 offset:41632\n" \
"v_mfma_f32_16x16x32_bf16 v[216:219], a[16:19], a[36:39], v[216:219]\n" \
"ds_read_b64_tr_b16 a[10:11], v10 offset:50848\n" \
"v_mfma_f32_16x16x32_bf16 v[216:219], a[20:23], a[40:43], v[216:219]\n" \
"v_cvt_pk_bf16_f32 v210, v212, v213\n" \
"v_cvt_pk_bf16_f32 v211, v214, v215\n" \
"buffer_store_dwordx4 v[208:211], v20, s[56:59], s70 offen offset:1024 sc1\n" \
"s_add_u32 s70, s70, 0x2000\n" \
"v_mfma_f32_16x16x32_bf16 v[216:219], a[24:27], a[44:47], v[216:219]\n" \
"v_mfma_f32_16x16x32_bf16 v[220:223], a[24:27], a[60:63], v[220:223]\n" \
"ds_read_b64_tr_b16 a[24:25], v33 offset:37024\n" \
"ds_read_b64_tr_b16 a[26:27], v33 offset:41632\n" \
"v_mfma_f32_16x16x32_bf16 v[220:223], a[20:23], a[56:59], v[220:223]\n" \
"ds_read_b64_tr_b16 a[20:21], v33 offset:27808\n" \
"ds_read_b64_tr_b16 a[22:23], v33 offset:32416\n" \
"v_mfma_f32_16x16x32_bf16 v[220:223], a[16:19], a[52:55], v[220:223]\n" \
"ds_read_b64_tr_b16 a[16:17], v33 offset:18592\n" \
"ds_read_b64_tr_b16 a[18:19], v33 offset:23200\n" \
"v_mfma_f32_16x16x32_bf16 v[220:223], a[12:15], a[48:51], v[220:223]\n" \
"ds_read_b64_tr_b16 a[12:13], v10 offset:55456\n" \
"ds_read_b64_tr_b16 a[14:15], v10 offset:60064\n" \
"v_mfma_f32_16x16x32_bf16 v[144:147], a[28:31], a[128:131], v[144:147]\n" \
"v_mfma_f32_16x16x32_bf16 v[144:147], a[192:195], a[132:135], v[144:147]\n" \
"v_mfma_f32_16x16x32_bf16 v[144:147], a[196:199], a[136:139], v[144:147]\n" \
"s_waitcnt lgkmcnt(15)\n" \
"v_mfma_f32_16x16x32_bf16 v[144:147], a[200:203], a[140:143], v[144:147]\n" \
"v_mfma_f32_16x16x32_bf16 v[148:151], a[200:203], a[156:159], v[148:151]\n" \
"v_mfma_f32_16x16x32_bf16 v[148:151], a[196:199], a[152:155], v[148:151]\n" \
"v_mfma_f32_16x16x32_bf16 v[148:151], a[192:195], a[148:151], v[148:151]\n" \
"v_mfma_f32_16x16x32_bf16 v[148:151], a[28:31], a[144:147], v[148:151]\n" \
"v_mfma_f32_16x16x32_bf16 v[216:219], a[28:31], a[160:163], v[216:219]\n" \
"v_mfma_f32_16x16x32_bf16 v[216:219], a[192:195], a[164:167], v[216:219]\n" \
"v_mfma_f32_16x16x32_bf16 v[216:219], a[196:199], a[168:171], v[216:219]\n" \
"v_mfma_f32_16x16x32_bf16 v[216:219], a[200:203], a[172:175], v[216:219]\n" \
"v_mfma_f32_16x16x32_bf16 v[220:223], a[200:203], a[188:191], v[220:223]\n" \
"ds_read_b64_tr_b16 a[200:201], v34 offset:18592\n" \
"ds_read_b64_tr_b16 a[202:203], v34 offset:23200\n" \
"v_mfma_f32_16x16x32_bf16 v[220:223], a[196:199], a[184:187], v[220:223]\n" \
"ds_read_b64_tr_b16 a[196:197], v34 offset:9376\n" \
"ds_read_b64_tr_b16 a[198:199], v34 offset:13984\n" \
"v_mfma_f32_16x16x32_bf16 v[220:223], a[192:195], a[180:183], v[220:223]\n" \
"ds_read_b64_tr_b16 a[192:193], v34 offset:160\n" \
"ds_read_b64_tr_b16 a[194:195], v34 offset:4768\n" \
"v_mfma_f32_16x16x32_bf16 v[220:223], a[28:31], a[176:179], v[220:223]\n" \
"ds_read_b64_tr_b16 a[28:29], v33 offset:46240\n" \
"ds_read_b64_tr_b16 a[30:31], v33 offset:50848\n" \
"v_mfma_f32_16x16x32_bf16 v[144:147], a[204:207], a[96:99], v[144:147]\n" \
"v_mfma_f32_16x16x32_bf16 v[144:147], a[208:211], a[100:103], v[144:147]\n" \
"v_mfma_f32_16x16x32_bf16 v[144:147], a[212:215], a[104:107], v[144:147]\n" \
"s_waitcnt lgkmcnt(8)\n" \
"v_mfma_f32_16x16x32_bf16 v[144:147], a[216:219], a[108:111], v[144:147]\n" \
"v_mfma_f32_16x16x32_bf16 v[148:151], a[216:219], a[124:127], v[148:151]\n" \
"v_mfma_f32_16x16x32_bf16 v[148:151], a[212:215], a[120:123], v[148:151]\n" \
"v_mfma_f32_16x16x32_bf16 v[148:151], a[208:211], a[116:119], v[148:151]\n" \
"v_mfma_f32_16x16x32_bf16 v[148:151], a[204:207], a[112:115], v[148:151]\n" \
"v_mfma_f32_16x16x32_bf16 v[216:219], a[204:207], a[220:223], v[216:219]\n" \
"v_mfma_f32_16x16x32_bf16 v[216:219], a[208:211], a[224:227], v[216:219]\n" \
"v_mfma_f32_16x16x32_bf16 v[216:219], a[212:215], a[228:231], v[216:219]\n" \
"v_mfma_f32_16x16x32_bf16 v[216:219], a[216:219], a[232:235], v[216:219]\n" \
"v_cvt_pk_bf16_f32 v144, v144, v145\n" \
"v_cvt_pk_bf16_f32 v145, v146, v147\n" \
"v_mfma_f32_16x16x32_bf16 v[220:223], a[216:219], a[248:251], v[220:223]\n" \
"ds_read_b64_tr_b16 a[216:217], v10 offset:46272\n" \
"v_cvt_pk_bf16_f32 v146, v148, v149\n" \
"v_cvt_pk_bf16_f32 v147, v150, v151\n" \
"buffer_store_dwordx4 v[144:147], v20, s[56:59], s71 offen offset:0 sc1\n" \
"v_mfma_f32_16x16x32_bf16 v[220:223], a[212:215], a[244:247], v[220:223]\n" \
"v_cvt_pk_bf16_f32 v216, v216, v217\n" \
"v_cvt_pk_bf16_f32 v217, v218, v219\n" \
"v_mfma_f32_16x16x32_bf16 v[220:223], a[208:211], a[240:243], v[220:223]\n" \
"ds_read_b64_tr_b16 a[208:209], v10 offset:27840\n" \
"ds_read_b64_tr_b16 a[210:211], v10 offset:32448\n" \
"v_mfma_f32_16x16x32_bf16 v[220:223], a[204:207], a[236:239], v[220:223]\n" \
"ds_read_b64_tr_b16 a[204:205], v33 offset:160\n" \
"ds_read_b64_tr_b16 a[206:207], v33 offset:4768\n" \
"v_mfma_f32_16x16x32_bf16 v[224:227], a[0:3], a[32:35], v[224:227]\n" \
"ds_read_b64_tr_b16 a[212:213], v10 offset:37056\n" \
"ds_read_b64_tr_b16 a[214:215], v10 offset:41664\n" \
"v_mfma_f32_16x16x32_bf16 v[224:227], a[4:7], a[36:39], v[224:227]\n" \
"ds_read_b64_tr_b16 a[218:219], v10 offset:50880\n" \
"v_mfma_f32_16x16x32_bf16 v[224:227], a[8:11], a[40:43], v[224:227]\n" \
"v_cvt_pk_bf16_f32 v218, v220, v221\n" \
"v_cvt_pk_bf16_f32 v219, v222, v223\n" \
"buffer_store_dwordx4 v[216:219], v20, s[56:59], s71 offen offset:1024 sc1\n" \
"s_add_u32 s71, s71, 0x1800\n" \
"v_mfma_f32_16x16x32_bf16 v[224:227], a[12:15], a[44:47], v[224:227]\n" \
"v_mfma_f32_16x16x32_bf16 v[228:231], a[12:15], a[60:63], v[228:231]\n" \
"ds_read_b64_tr_b16 a[12:13], v33 offset:37056\n" \
"ds_read_b64_tr_b16 a[14:15], v33 offset:41664\n" \
"v_mfma_f32_16x16x32_bf16 v[228:231], a[8:11], a[56:59], v[228:231]\n" \
"ds_read_b64_tr_b16 a[8:9], v33 offset:27840\n" \
"ds_read_b64_tr_b16 a[10:11], v33 offset:32448\n" \
"v_mfma_f32_16x16x32_bf16 v[228:231], a[4:7], a[52:55], v[228:231]\n" \
"ds_read_b64_tr_b16 a[4:5], v33 offset:18624\n" \
"ds_read_b64_tr_b16 a[6:7], v33 offset:23232\n" \
"v_mfma_f32_16x16x32_bf16 v[228:231], a[0:3], a[48:51], v[228:231]\n" \
"ds_read_b64_tr_b16 a[0:1], v10 offset:55488\n" \
"ds_read_b64_tr_b16 a[2:3], v10 offset:60096\n" \
"v_mfma_f32_16x16x32_bf16 v[152:155], a[16:19], a[128:131], v[152:155]\n" \
"v_mfma_f32_16x16x32_bf16 v[152:155], a[20:23], a[132:135], v[152:155]\n" \
"v_mfma_f32_16x16x32_bf16 v[152:155], a[24:27], a[136:139], v[152:155]\n" \
"s_waitcnt lgkmcnt(15)\n" \
"v_mfma_f32_16x16x32_bf16 v[152:155], a[28:31], a[140:143], v[152:155]\n" \
"v_mfma_f32_16x16x32_bf16 v[156:159], a[28:31], a[156:159], v[156:159]\n" \
"v_mfma_f32_16x16x32_bf16 v[156:159], a[24:27], a[152:155], v[156:159]\n" \
"v_mfma_f32_16x16x32_bf16 v[156:159], a[20:23], a[148:151], v[156:159]\n" \
"v_mfma_f32_16x16x32_bf16 v[156:159], a[16:19], a[144:147], v[156:159]\n" \
"s_waitcnt vmcnt(8)\n" \
"s_mov_b64 exec, 3\n" \
"buffer_store_dword v39, v41, s[56:59], s69 offen sc1\n" \
"s_mov_b64 exec, -1\n" \
"v_mfma_f32_16x16x32_bf16 v[224:227], a[16:19], a[160:163], v[224:227]\n" \
"s_add_u32 s69, s63, 0x0\n" \
"v_mfma_f32_16x16x32_bf16 v[224:227], a[20:23], a[164:167], v[224:227]\n" \
"v_mfma_f32_16x16x32_bf16 v[224:227], a[24:27], a[168:171], v[224:227]\n" \
"v_mfma_f32_16x16x32_bf16 v[224:227], a[28:31], a[172:175], v[224:227]\n" \
"v_mfma_f32_16x16x32_bf16 v[228:231], a[28:31], a[188:191], v[228:231]\n" \
"ds_read_b64_tr_b16 a[28:29], v34 offset:18624\n" \
"ds_read_b64_tr_b16 a[30:31], v34 offset:23232\n" \
"v_mfma_f32_16x16x32_bf16 v[228:231], a[24:27], a[184:187], v[228:231]\n" \
"ds_read_b64_tr_b16 a[24:25], v34 offset:9408\n" \
"ds_read_b64_tr_b16 a[26:27], v34 offset:14016\n" \
"v_mfma_f32_16x16x32_bf16 v[228:231], a[20:23], a[180:183], v[228:231]\n" \
"ds_read_b64_tr_b16 a[20:21], v34 offset:192\n" \
"ds_read_b64_tr_b16 a[22:23], v34 offset:4800\n" \
"v_mfma_f32_16x16x32_bf16 v[228:231], a[16:19], a[176:179], v[228:231]\n" \
"ds_read_b64_tr_b16 a[16:17], v33 offset:46272\n" \
"ds_read_b64_tr_b16 a[18:19], v33 offset:50880\n" \
"v_mfma_f32_16x16x32_bf16 v[152:155], a[192:195], a[96:99], v[152:155]\n" \
"v_mfma_f32_16x16x32_bf16 v[152:155], a[196:199], a[100:103], v[152:155]\n" \
"v_mfma_f32_16x16x32_bf16 v[152:155], a[200:203], a[104:107], v[152:155]\n" \
"s_waitcnt lgkmcnt(8)\n" \
"v_mfma_f32_16x16x32_bf16 v[152:155], a[204:207], a[108:111], v[152:155]\n" \
"v_mfma_f32_16x16x32_bf16 v[156:159], a[204:207], a[124:127], v[156:159]\n" \
"v_mfma_f32_16x16x32_bf16 v[156:159], a[200:203], a[120:123], v[156:159]\n" \
"v_mfma_f32_16x16x32_bf16 v[156:159], a[196:199], a[116:119], v[156:159]\n" \
"v_mfma_f32_16x16x32_bf16 v[156:159], a[192:195], a[112:115], v[156:159]\n" \
"v_mfma_f32_16x16x32_bf16 v[224:227], a[192:195], a[220:223], v[224:227]\n" \
"v_mfma_f32_16x16x32_bf16 v[224:227], a[196:199], a[224:227], v[224:227]\n" \
"v_mfma_f32_16x16x32_bf16 v[224:227], a[200:203], a[228:231], v[224:227]\n" \
"v_mfma_f32_16x16x32_bf16 v[224:227], a[204:207], a[232:235], v[224:227]\n" \
"v_cvt_pk_bf16_f32 v152, v152, v153\n" \
"v_cvt_pk_bf16_f32 v153, v154, v155\n" \
"v_mfma_f32_16x16x32_bf16 v[228:231], a[204:207], a[248:251], v[228:231]\n" \
"ds_read_b64_tr_b16 a[204:205], v10 offset:46080\n" \
"v_cvt_pk_bf16_f32 v154, v156, v157\n" \
"v_cvt_pk_bf16_f32 v155, v158, v159\n" \
"buffer_store_dwordx4 v[152:155], v20, s[56:59], s72 offen offset:0 sc1\n" \
"v_mfma_f32_16x16x32_bf16 v[228:231], a[200:203], a[244:247], v[228:231]\n" \
"v_cvt_pk_bf16_f32 v224, v224, v225\n" \
"v_cvt_pk_bf16_f32 v225, v226, v227\n" \
"v_mfma_f32_16x16x32_bf16 v[228:231], a[196:199], a[240:243], v[228:231]\n" \
"ds_read_b64_tr_b16 a[196:197], v10 offset:27648\n" \
"ds_read_b64_tr_b16 a[198:199], v10 offset:32256\n" \
"v_mfma_f32_16x16x32_bf16 v[228:231], a[192:195], a[236:239], v[228:231]\n" \
"ds_read_b64_tr_b16 a[192:193], v33 offset:192\n" \
"ds_read_b64_tr_b16 a[194:195], v33 offset:4800\n" \
"v_mfma_f32_16x16x32_bf16 v[232:235], a[208:211], a[32:35], v[232:235]\n" \
"ds_read_b64_tr_b16 a[200:201], v10 offset:36864\n" \
"ds_read_b64_tr_b16 a[202:203], v10 offset:41472\n" \
"v_mfma_f32_16x16x32_bf16 v[232:235], a[212:215], a[36:39], v[232:235]\n" \
"ds_read_b64_tr_b16 a[206:207], v10 offset:50688\n" \
"v_mfma_f32_16x16x32_bf16 v[232:235], a[216:219], a[40:43], v[232:235]\n" \
"v_cvt_pk_bf16_f32 v226, v228, v229\n" \
"v_cvt_pk_bf16_f32 v227, v230, v231\n" \
"buffer_store_dwordx4 v[224:227], v20, s[56:59], s72 offen offset:1024 sc1\n" \
"s_add_u32 s72, s72, 0x1000\n" \
"v_mfma_f32_16x16x32_bf16 v[232:235], a[0:3], a[44:47], v[232:235]\n" \
"v_mfma_f32_16x16x32_bf16 v[236:239], a[0:3], a[60:63], v[236:239]\n" \
"ds_read_b64_tr_b16 a[0:1], v33 offset:36864\n" \
"ds_read_b64_tr_b16 a[2:3], v33 offset:41472\n" \
"v_mfma_f32_16x16x32_bf16 v[236:239], a[216:219], a[56:59], v[236:239]\n" \
"ds_read_b64_tr_b16 a[216:217], v33 offset:27648\n" \
"ds_read_b64_tr_b16 a[218:219], v33 offset:32256\n" \
"v_mfma_f32_16x16x32_bf16 v[236:239], a[212:215], a[52:55], v[236:239]\n" \
"ds_read_b64_tr_b16 a[212:213], v33 offset:18432\n" \
"ds_read_b64_tr_b16 a[214:215], v33 offset:23040\n" \
"v_mfma_f32_16x16x32_bf16 v[236:239], a[208:211], a[48:51], v[236:239]\n" \
"ds_read_b64_tr_b16 a[208:209], v10 offset:55296\n" \
"ds_read_b64_tr_b16 a[210:211], v10 offset:59904\n" \
"v_mfma_f32_16x16x32_bf16 v[160:163], a[4:7], a[128:131], v[160:163]\n" \
"v_mfma_f32_16x16x32_bf16 v[160:163], a[8:11], a[132:135], v[160:163]\n" \
"v_mfma_f32_16x16x32_bf16 v[160:163], a[12:15], a[136:139], v[160:163]\n" \
"s_waitcnt lgkmcnt(15)\n" \
"v_mfma_f32_16x16x32_bf16 v[160:163], a[16:19], a[140:143], v[160:163]\n" \
"v_mfma_f32_16x16x32_bf16 v[164:167], a[16:19], a[156:159], v[164:167]\n" \
"v_mfma_f32_16x16x32_bf16 v[164:167], a[12:15], a[152:155], v[164:167]\n" \
"s_waitcnt vmcnt(5)\n" \
"s_mov_b64 exec, 1\n" \
"buffer_store_dword v39, off, s[56:59], s70 offset:16 sc1\n" \
"s_mov_b64 exec, -1\n" \
"v_mfma_f32_16x16x32_bf16 v[164:167], a[8:11], a[148:151], v[164:167]\n" \
"s_add_u32 s70, s63, 0x800\n" \
"v_mfma_f32_16x16x32_bf16 v[164:167], a[4:7], a[144:147], v[164:167]\n" \
"v_mfma_f32_16x16x32_bf16 v[232:235], a[4:7], a[160:163], v[232:235]\n" \
"v_mfma_f32_16x16x32_bf16 v[232:235], a[8:11], a[164:167], v[232:235]\n" \
"v_mfma_f32_16x16x32_bf16 v[232:235], a[12:15], a[168:171], v[232:235]\n" \
"v_mfma_f32_16x16x32_bf16 v[232:235], a[16:19], a[172:175], v[232:235]\n" \
"v_mfma_f32_16x16x32_bf16 v[236:239], a[16:19], a[188:191], v[236:239]\n" \
"ds_read_b64_tr_b16 a[16:17], v34 offset:18432\n" \
"ds_read_b64_tr_b16 a[18:19], v34 offset:23040\n" \
"v_mfma_f32_16x16x32_bf16 v[236:239], a[12:15], a[184:187], v[236:239]\n" \
"ds_read_b64_tr_b16 a[12:13], v34 offset:9216\n" \
"ds_read_b64_tr_b16 a[14:15], v34 offset:13824\n" \
"v_mfma_f32_16x16x32_bf16 v[236:239], a[8:11], a[180:183], v[236:239]\n" \
"ds_read_b64_tr_b16 a[8:9], v34 offset:0\n" \
"ds_read_b64_tr_b16 a[10:11], v34 offset:4608\n" \
"v_mfma_f32_16x16x32_bf16 v[236:239], a[4:7], a[176:179], v[236:239]\n" \
"ds_read_b64_tr_b16 a[4:5], v33 offset:46080\n" \
"ds_read_b64_tr_b16 a[6:7], v33 offset:50688\n" \
"v_mfma_f32_16x16x32_bf16 v[160:163], a[20:23], a[96:99], v[160:163]\n" \
"v_mfma_f32_16x16x32_bf16 v[160:163], a[24:27], a[100:103], v[160:163]\n" \
"v_mfma_f32_16x16x32_bf16 v[160:163], a[28:31], a[104:107], v[160:163]\n" \
"s_waitcnt lgkmcnt(8)\n" \
"v_mfma_f32_16x16x32_bf16 v[160:163], a[192:195], a[108:111], v[160:163]\n" \
"v_mfma_f32_16x16x32_bf16 v[164:167], a[192:195], a[124:127], v[164:167]\n" \
"v_mfma_f32_16x16x32_bf16 v[164:167], a[28:31], a[120:123], v[164:167]\n" \
"v_mfma_f32_16x16x32_bf16 v[164:167], a[24:27], a[116:119], v[164:167]\n" \
"v_mfma_f32_16x16x32_bf16 v[164:167], a[20:23], a[112:115], v[164:167]\n" \
"v_mfma_f32_16x16x32_bf16 v[232:235], a[20:23], a[220:223], v[232:235]\n" \
"v_mfma_f32_16x16x32_bf16 v[232:235], a[24:27], a[224:227], v[232:235]\n" \
"v_mfma_f32_16x16x32_bf16 v[232:235], a[28:31], a[228:231], v[232:235]\n" \
"v_mfma_f32_16x16x32_bf16 v[232:235], a[192:195], a[232:235], v[232:235]\n" \
"v_cvt_pk_bf16_f32 v160, v160, v161\n" \
"v_cvt_pk_bf16_f32 v161, v162, v163\n" \
"v_mfma_f32_16x16x32_bf16 v[236:239], a[192:195], a[248:251], v[236:239]\n" \
"ds_read_b64_tr_b16 a[192:193], v10 offset:46112\n" \
"v_cvt_pk_bf16_f32 v162, v164, v165\n" \
"v_cvt_pk_bf16_f32 v163, v166, v167\n" \
"buffer_store_dwordx4 v[160:163], v20, s[56:59], s73 offen offset:0 sc1\n" \
"v_mfma_f32_16x16x32_bf16 v[236:239], a[28:31], a[244:247], v[236:239]\n" \
"v_cvt_pk_bf16_f32 v232, v232, v233\n" \
"v_cvt_pk_bf16_f32 v233, v234, v235\n" \
"v_mfma_f32_16x16x32_bf16 v[236:239], a[24:27], a[240:243], v[236:239]\n" \
"ds_read_b64_tr_b16 a[24:25], v10 offset:27680\n" \
"ds_read_b64_tr_b16 a[26:27], v10 offset:32288\n" \
"v_mfma_f32_16x16x32_bf16 v[236:239], a[20:23], a[236:239], v[236:239]\n" \
"ds_read_b64_tr_b16 a[20:21], v33 offset:0\n" \
"ds_read_b64_tr_b16 a[22:23], v33 offset:4608\n" \
"v_mfma_f32_16x16x32_bf16 v[184:187], a[196:199], a[32:35], v[184:187]\n" \
"ds_read_b64_tr_b16 a[28:29], v10 offset:36896\n" \
"ds_read_b64_tr_b16 a[30:31], v10 offset:41504\n" \
"v_mfma_f32_16x16x32_bf16 v[184:187], a[200:203], a[36:39], v[184:187]\n" \
"ds_read_b64_tr_b16 a[194:195], v10 offset:50720\n" \
"v_mfma_f32_16x16x32_bf16 v[184:187], a[204:207], a[40:43], v[184:187]\n" \
"v_cvt_pk_bf16_f32 v234, v236, v237\n" \
"v_cvt_pk_bf16_f32 v235, v238, v239\n" \
"buffer_store_dwordx4 v[232:235], v20, s[56:59], s73 offen offset:1024 sc1\n" \
"s_add_u32 s73, s73, 0x800\n" \
"v_mfma_f32_16x16x32_bf16 v[184:187], a[208:211], a[44:47], v[184:187]\n" \
"v_mfma_f32_16x16x32_bf16 v[188:191], a[208:211], a[60:63], v[188:191]\n" \
"ds_read_b64_tr_b16 a[208:209], v33 offset:36896\n" \
"ds_read_b64_tr_b16 a[210:211], v33 offset:41504\n" \
"v_mfma_f32_16x16x32_bf16 v[188:191], a[204:207], a[56:59], v[188:191]\n" \
"ds_read_b64_tr_b16 a[204:205], v33 offset:27680\n" \
"ds_read_b64_tr_b16 a[206:207], v33 offset:32288\n" \
"v_mfma_f32_16x16x32_bf16 v[188:191], a[200:203], a[52:55], v[188:191]\n" \
"ds_read_b64_tr_b16 a[200:201], v33 offset:18464\n" \
"ds_read_b64_tr_b16 a[202:203], v33 offset:23072\n" \
"v_mfma_f32_16x16x32_bf16 v[188:191], a[196:199], a[48:51], v[188:191]\n" \
"ds_read_b64_tr_b16 a[196:197], v10 offset:55328\n" \
"ds_read_b64_tr_b16 a[198:199], v10 offset:59936\n" \
"v_mfma_f32_16x16x32_bf16 v[184:187], a[212:215], a[160:163], v[184:187]\n" \
"v_mfma_f32_16x16x32_bf16 v[184:187], a[216:219], a[164:167], v[184:187]\n" \
"v_mfma_f32_16x16x32_bf16 v[184:187], a[0:3], a[168:171], v[184:187]\n" \
"s_waitcnt lgkmcnt(15)\n" \
"v_mfma_f32_16x16x32_bf16 v[184:187], a[4:7], a[172:175], v[184:187]\n" \
"v_mfma_f32_16x16x32_bf16 v[188:191], a[4:7], a[188:191], v[188:191]\n" \
"ds_read_b64_tr_b16 a[4:5], v34 offset:18464\n" \
"ds_read_b64_tr_b16 a[6:7], v34 offset:23072\n" \
"s_waitcnt vmcnt(6)\n" \
"s_mov_b64 exec, 1\n" \
"buffer_store_dword v39, off, s[56:59], s71 offset:20 sc1\n" \
"s_mov_b64 exec, -1\n" \
"v_mfma_f32_16x16x32_bf16 v[188:191], a[0:3], a[184:187], v[188:191]\n" \
"ds_read_b64_tr_b16 a[0:1], v34 offset:9248\n" \
"ds_read_b64_tr_b16 a[2:3], v34 offset:13856\n" \
"v_mfma_f32_16x16x32_bf16 v[188:191], a[216:219], a[180:183], v[188:191]\n" \
"ds_read_b64_tr_b16 a[216:217], v34 offset:32\n" \
"ds_read_b64_tr_b16 a[218:219], v34 offset:4640\n" \
"v_mfma_f32_16x16x32_bf16 v[188:191], a[212:215], a[176:179], v[188:191]\n" \
"ds_read_b64_tr_b16 a[212:213], v33 offset:46112\n" \
"ds_read_b64_tr_b16 a[214:215], v33 offset:50720\n" \
"v_mfma_f32_16x16x32_bf16 v[184:187], a[8:11], a[220:223], v[184:187]\n" \
"s_add_u32 s71, s63, 0x1000\n" \
"v_mfma_f32_16x16x32_bf16 v[184:187], a[12:15], a[224:227], v[184:187]\n" \
"v_mfma_f32_16x16x32_bf16 v[184:187], a[16:19], a[228:231], v[184:187]\n" \
"s_waitcnt lgkmcnt(12)\n" \
"v_mfma_f32_16x16x32_bf16 v[184:187], a[20:23], a[232:235], v[184:187]\n" \
"v_mfma_f32_16x16x32_bf16 v[188:191], a[20:23], a[248:251], v[188:191]\n" \
"ds_read_b64_tr_b16 a[20:21], v10 offset:46144\n" \
"ds_read_b64_tr_b16 a[22:23], v10 offset:50752\n" \
"v_mfma_f32_16x16x32_bf16 v[188:191], a[16:19], a[244:247], v[188:191]\n" \
"ds_read_b64_tr_b16 a[16:17], v10 offset:36928\n" \
"ds_read_b64_tr_b16 a[18:19], v10 offset:41536\n" \
"v_mfma_f32_16x16x32_bf16 v[188:191], a[12:15], a[240:243], v[188:191]\n" \
"ds_read_b64_tr_b16 a[12:13], v10 offset:27712\n" \
"v_cvt_pk_bf16_f32 v184, v184, v185\n" \
"v_cvt_pk_bf16_f32 v185, v186, v187\n" \
"v_mfma_f32_16x16x32_bf16 v[188:191], a[8:11], a[236:239], v[188:191]\n" \
"ds_read_b64_tr_b16 a[8:9], v33 offset:32\n" \
"ds_read_b64_tr_b16 a[10:11], v33 offset:4640\n" \
"v_mfma_f32_16x16x32_bf16 v[192:195], a[24:27], a[32:35], v[192:195]\n" \
"ds_read_b64_tr_b16 a[14:15], v10 offset:32320\n" \
"v_mfma_f32_16x16x32_bf16 v[192:195], a[28:31], a[36:39], v[192:195]\n" \
"v_mfma_f32_16x16x32_bf16 v[192:195], a[192:195], a[40:43], v[192:195]\n" \
"s_waitcnt lgkmcnt(14)\n" \
"v_mfma_f32_16x16x32_bf16 v[192:195], a[196:199], a[44:47], v[192:195]\n" \
"v_cvt_pk_bf16_f32 v186, v188, v189\n" \
"v_cvt_pk_bf16_f32 v187, v190, v191\n" \
"buffer_store_dwordx4 v[184:187], v20, s[56:59], s92 offen offset:1024 sc1\n" \
"s_add_u32 s92, s92, 0x3800\n" \
"v_mfma_f32_16x16x32_bf16 v[196:199], a[196:199], a[60:63], v[196:199]\n" \
"ds_read_b64_tr_b16 a[196:197], v33 offset:36928\n" \
"ds_read_b64_tr_b16 a[198:199], v33 offset:41536\n" \
"v_mfma_f32_16x16x32_bf16 v[196:199], a[192:195], a[56:59], v[196:199]\n" \
"ds_read_b64_tr_b16 a[192:193], v33 offset:27712\n" \
"ds_read_b64_tr_b16 a[194:195], v33 offset:32320\n" \
"v_mfma_f32_16x16x32_bf16 v[196:199], a[28:31], a[52:55], v[196:199]\n" \
"ds_read_b64_tr_b16 a[28:29], v33 offset:18496\n" \
"ds_read_b64_tr_b16 a[30:31], v33 offset:23104\n" \
"v_mfma_f32_16x16x32_bf16 v[196:199], a[24:27], a[48:51], v[196:199]\n" \
"ds_read_b64_tr_b16 a[24:25], v10 offset:55360\n" \
"ds_read_b64_tr_b16 a[26:27], v10 offset:59968\n" \
"v_mfma_f32_16x16x32_bf16 v[192:195], a[200:203], a[160:163], v[192:195]\n" \
"v_mfma_f32_16x16x32_bf16 v[192:195], a[204:207], a[164:167], v[192:195]\n" \
"v_mfma_f32_16x16x32_bf16 v[192:195], a[208:211], a[168:171], v[192:195]\n" \
"s_waitcnt lgkmcnt(13)\n" \
"v_mfma_f32_16x16x32_bf16 v[192:195], a[212:215], a[172:175], v[192:195]\n" \
"v_mfma_f32_16x16x32_bf16 v[196:199], a[212:215], a[188:191], v[196:199]\n" \
"ds_read_b64_tr_b16 a[212:213], v34 offset:18496\n" \
"ds_read_b64_tr_b16 a[214:215], v34 offset:23104\n" \
"v_mfma_f32_16x16x32_bf16 v[196:199], a[208:211], a[184:187], v[196:199]\n" \
"ds_read_b64_tr_b16 a[208:209], v34 offset:9280\n" \
"ds_read_b64_tr_b16 a[210:211], v34 offset:13888\n" \
"v_mfma_f32_16x16x32_bf16 v[196:199], a[204:207], a[180:183], v[196:199]\n" \
"ds_read_b64_tr_b16 a[204:205], v34 offset:64\n" \
"ds_read_b64_tr_b16 a[206:207], v34 offset:4672\n" \
"v_mfma_f32_16x16x32_bf16 v[196:199], a[200:203], a[176:179], v[196:199]\n" \
"ds_read_b64_tr_b16 a[200:201], v33 offset:46144\n" \
"ds_read_b64_tr_b16 a[202:203], v33 offset:50752\n" \
"v_mfma_f32_16x16x32_bf16 v[192:195], a[216:219], a[220:223], v[192:195]\n" \
"v_mfma_f32_16x16x32_bf16 v[192:195], a[0:3], a[224:227], v[192:195]\n" \
"v_mfma_f32_16x16x32_bf16 v[192:195], a[4:7], a[228:231], v[192:195]\n" \
"s_waitcnt lgkmcnt(15)\n" \
"v_mfma_f32_16x16x32_bf16 v[192:195], a[8:11], a[232:235], v[192:195]\n" \
"v_mfma_f32_16x16x32_bf16 v[196:199], a[8:11], a[248:251], v[196:199]\n" \
"ds_read_b64_tr_b16 a[8:9], v10 offset:46304\n" \
"ds_read_b64_tr_b16 a[10:11], v10 offset:50912\n" \
"v_mfma_f32_16x16x32_bf16 v[196:199], a[4:7], a[244:247], v[196:199]\n" \
"ds_read_b64_tr_b16 a[4:5], v10 offset:37088\n" \
"ds_read_b64_tr_b16 a[6:7], v10 offset:41696\n" \
"v_mfma_f32_16x16x32_bf16 v[196:199], a[0:3], a[240:243], v[196:199]\n" \
"L_pf22:\n" \
"s_cbranch_execz L_retB\n" \
"s_nop 0\n" \
"ds_read_b64_tr_b16 a[0:1], v10 offset:27872\n" \
"v_cvt_pk_bf16_f32 v192, v192, v193\n" \
"v_cvt_pk_bf16_f32 v193, v194, v195\n" \
"v_mfma_f32_16x16x32_bf16 v[196:199], a[216:219], a[236:239], v[196:199]\n" \
"ds_read_b64_tr_b16 a[216:217], v33 offset:64\n" \
"ds_read_b64_tr_b16 a[218:219], v33 offset:4672\n" \
"v_mfma_f32_16x16x32_bf16 v[200:203], a[12:15], a[32:35], v[200:203]\n" \
"ds_read_b64_tr_b16 a[2:3], v10 offset:32480\n" \
"v_mfma_f32_16x16x32_bf16 v[200:203], a[16:19], a[36:39], v[200:203]\n" \
"v_mfma_f32_16x16x32_bf16 v[200:203], a[20:23], a[40:43], v[200:203]\n" \
"s_waitcnt lgkmcnt(14)\n" \
"v_mfma_f32_16x16x32_bf16 v[200:203], a[24:27], a[44:47], v[200:203]\n" \
"v_cvt_pk_bf16_f32 v194, v196, v197\n" \
"v_cvt_pk_bf16_f32 v195, v198, v199\n" \
"buffer_store_dwordx4 v[192:195], v20, s[56:59], s93 offen offset:1024 sc1\n" \
"s_waitcnt vmcnt(6)\n" \
"s_mov_b64 exec, 1\n" \
"buffer_store_dword v39, off, s[56:59], s72 offset:24 sc1\n" \
"s_mov_b64 exec, -1\n" \
"s_add_u32 s93, s93, 0x3000\n" \
"v_mfma_f32_16x16x32_bf16 v[204:207], a[24:27], a[60:63], v[204:207]\n" \
"ds_read_b64_tr_b16 a[24:25], v33 offset:37088\n" \
"ds_read_b64_tr_b16 a[26:27], v33 offset:41696\n" \
"v_mfma_f32_16x16x32_bf16 v[204:207], a[20:23], a[56:59], v[204:207]\n" \
"ds_read_b64_tr_b16 a[20:21], v33 offset:27872\n" \
"ds_read_b64_tr_b16 a[22:23], v33 offset:32480\n" \
"v_mfma_f32_16x16x32_bf16 v[204:207], a[16:19], a[52:55], v[204:207]\n" \
"ds_read_b64_tr_b16 a[16:17], v33 offset:18656\n" \
"ds_read_b64_tr_b16 a[18:19], v33 offset:23264\n" \
"v_mfma_f32_16x16x32_bf16 v[204:207], a[12:15], a[48:51], v[204:207]\n" \
"ds_read_b64_tr_b16 a[12:13], v10 offset:55520\n" \
"ds_read_b64_tr_b16 a[14:15], v10 offset:60128\n" \
"v_mfma_f32_16x16x32_bf16 v[200:203], a[28:31], a[160:163], v[200:203]\n" \
"v_readlane_b32 s72, v11, 50\n" \
"v_mfma_f32_16x16x32_bf16 v[200:203], a[192:195], a[164:167], v[200:203]\n" \
"v_mfma_f32_16x16x32_bf16 v[200:203], a[196:199], a[168:171], v[200:203]\n" \
"s_waitcnt lgkmcnt(11)\n" \
"v_mfma_f32_16x16x32_bf16 v[200:203], a[200:203], a[172:175], v[200:203]\n" \
"v_mfma_f32_16x16x32_bf16 v[204:207], a[200:203], a[188:191], v[204:207]\n" \
"ds_read_b64_tr_b16 a[200:201], v34 offset:18656\n" \
"ds_read_b64_tr_b16 a[202:203], v34 offset:23264\n" \
"v_mfma_f32_16x16x32_bf16 v[204:207], a[196:199], a[184:187], v[204:207]\n" \
"ds_read_b64_tr_b16 a[196:197], v34 offset:9440\n" \
"ds_read_b64_tr_b16 a[198:199], v34 offset:14048\n" \
"v_mfma_f32_16x16x32_bf16 v[204:207], a[192:195], a[180:183], v[204:207]\n" \
"ds_read_b64_tr_b16 a[192:193], v34 offset:224\n" \
"ds_read_b64_tr_b16 a[194:195], v34 offset:4832\n" \
"v_mfma_f32_16x16x32_bf16 v[204:207], a[28:31], a[176:179], v[204:207]\n" \
"ds_read_b64_tr_b16 a[28:29], v33 offset:46304\n" \
"ds_read_b64_tr_b16 a[30:31], v33 offset:50912\n" \
"v_mfma_f32_16x16x32_bf16 v[200:203], a[204:207], a[220:223], v[200:203]\n" \
"v_mfma_f32_16x16x32_bf16 v[200:203], a[208:211], a[224:227], v[200:203]\n" \
"v_mfma_f32_16x16x32_bf16 v[200:203], a[212:215], a[228:231], v[200:203]\n" \
"s_waitcnt lgkmcnt(15)\n" \
"v_mfma_f32_16x16x32_bf16 v[200:203], a[216:219], a[232:235], v[200:203]\n" \
"v_mfma_f32_16x16x32_bf16 v[204:207], a[216:219], a[248:251], v[204:207]\n" \
"ds_read_b64_tr_b16 a[216:217], v36 offset:32\n" \
"ds_read_b64_tr_b16 a[218:219], v36 offset:1056\n" \
"v_mfma_f32_16x16x32_bf16 v[204:207], a[212:215], a[244:247], v[204:207]\n" \
"ds_read_b64_tr_b16 a[212:213], v36 offset:2048\n" \
"ds_read_b64_tr_b16 a[214:215], v36 offset:3072\n" \
"v_mfma_f32_16x16x32_bf16 v[204:207], a[208:211], a[240:243], v[204:207]\n" \
"ds_read_b64_tr_b16 a[208:209], v36 offset:0\n" \
"v_cvt_pk_bf16_f32 v200, v200, v201\n" \
"v_cvt_pk_bf16_f32 v201, v202, v203\n" \
"v_mfma_f32_16x16x32_bf16 v[204:207], a[204:207], a[236:239], v[204:207]\n" \
"ds_read_b64_tr_b16 a[204:205], v33 offset:224\n" \
"ds_read_b64_tr_b16 a[206:207], v33 offset:4832\n" \
"v_mfma_f32_16x16x32_bf16 v[240:243], a[0:3], a[32:35], v[240:243]\n" \
"ds_read_b64_tr_b16 a[210:211], v36 offset:1024\n" \
"v_mfma_f32_16x16x32_bf16 v[240:243], a[4:7], a[36:39], v[240:243]\n" \
"v_mfma_f32_16x16x32_bf16 v[240:243], a[8:11], a[40:43], v[240:243]\n" \
"s_waitcnt lgkmcnt(14)\n" \
"v_mfma_f32_16x16x32_bf16 v[240:243], a[12:15], a[44:47], v[240:243]\n" \
"v_cvt_pk_bf16_f32 v202, v204, v205\n" \
"v_cvt_pk_bf16_f32 v203, v206, v207\n" \
"buffer_store_dwordx4 v[200:203], v20, s[56:59], s94 offen offset:1024 sc1\n" \
"s_add_u32 s94, s94, 0x2800\n" \
"v_mfma_f32_16x16x32_bf16 v[244:247], a[12:15], a[60:63], v[244:247]\n" \
"v_mfma_f32_16x16x32_bf16 v[244:247], a[8:11], a[56:59], v[244:247]\n" \
"v_mfma_f32_16x16x32_bf16 v[244:247], a[4:7], a[52:55], v[244:247]\n" \
"v_mfma_f32_16x16x32_bf16 v[244:247], a[0:3], a[48:51], v[244:247]\n" \
"ds_read_b64_tr_b16 a[0:1], v36 offset:2080\n" \
"ds_read_b64_tr_b16 a[2:3], v36 offset:3104\n" \
"v_mfma_f32_16x16x32_bf16 v[168:171], a[16:19], a[128:131], v[168:171]\n" \
"v_mfma_f32_16x16x32_bf16 v[168:171], a[20:23], a[132:135], v[168:171]\n" \
"v_mfma_f32_16x16x32_bf16 v[168:171], a[24:27], a[136:139], v[168:171]\n" \
"s_waitcnt lgkmcnt(7)\n" \
"v_mfma_f32_16x16x32_bf16 v[168:171], a[28:31], a[140:143], v[168:171]\n" \
"v_mfma_f32_16x16x32_bf16 v[172:175], a[28:31], a[156:159], v[172:175]\n" \
"v_mfma_f32_16x16x32_bf16 v[172:175], a[24:27], a[152:155], v[172:175]\n" \
"v_mfma_f32_16x16x32_bf16 v[172:175], a[20:23], a[148:151], v[172:175]\n" \
"v_mfma_f32_16x16x32_bf16 v[172:175], a[16:19], a[144:147], v[172:175]\n" \
"v_mfma_f32_16x16x32_bf16 v[240:243], a[16:19], a[160:163], v[240:243]\n" \
"v_mfma_f32_16x16x32_bf16 v[240:243], a[20:23], a[164:167], v[240:243]\n" \
"v_mfma_f32_16x16x32_bf16 v[240:243], a[24:27], a[168:171], v[240:243]\n" \
"v_mfma_f32_16x16x32_bf16 v[240:243], a[28:31], a[172:175], v[240:243]\n" \
"s_waitcnt vmcnt(5)\n" \
"s_mov_b64 exec, 1\n" \
"buffer_store_dword v39, off, s[56:59], s73 offset:28 sc1\n" \
"s_mov_b64 exec, -1\n" \
"v_mfma_f32_16x16x32_bf16 v[244:247], a[28:31], a[188:191], v[244:247]\n" \
"v_readlane_b32 s73, v11, 41\n" \
"v_mfma_f32_16x16x32_bf16 v[244:247], a[24:27], a[184:187], v[244:247]\n" \
"v_mfma_f32_16x16x32_bf16 v[244:247], a[20:23], a[180:183], v[244:247]\n" \
"v_mfma_f32_16x16x32_bf16 v[244:247], a[16:19], a[176:179], v[244:247]\n" \
"v_mfma_f32_16x16x32_bf16 v[168:171], a[192:195], a[96:99], v[168:171]\n" \
"v_mfma_f32_16x16x32_bf16 v[168:171], a[196:199], a[100:103], v[168:171]\n" \
"v_mfma_f32_16x16x32_bf16 v[168:171], a[200:203], a[104:107], v[168:171]\n" \
"s_waitcnt lgkmcnt(0)\n" \
"v_mfma_f32_16x16x32_bf16 v[168:171], a[204:207], a[108:111], v[168:171]\n" \
"v_mfma_f32_16x16x32_bf16 v[172:175], a[204:207], a[124:127], v[172:175]\n" \
"v_mfma_f32_16x16x32_bf16 v[172:175], a[200:203], a[120:123], v[172:175]\n" \
"v_mfma_f32_16x16x32_bf16 v[172:175], a[196:199], a[116:119], v[172:175]\n" \
"v_mfma_f32_16x16x32_bf16 v[172:175], a[192:195], a[112:115], v[172:175]\n" \
"v_mfma_f32_16x16x32_bf16 v[240:243], a[192:195], a[220:223], v[240:243]\n" \
"v_mfma_f32_16x16x32_bf16 v[240:243], a[196:199], a[224:227], v[240:243]\n" \
"v_mfma_f32_16x16x32_bf16 v[240:243], a[200:203], a[228:231], v[240:243]\n" \
"v_mfma_f32_16x16x32_bf16 v[240:243], a[204:207], a[232:235], v[240:243]\n" \
"v_mfma_f32_16x16x32_bf16 v[244:247], a[204:207], a[248:251], v[244:247]\n" \
"v_mfma_f32_16x16x32_bf16 v[244:247], a[200:203], a[244:247], v[244:247]\n" \
"v_mfma_f32_16x16x32_bf16 v[244:247], a[196:199], a[240:243], v[244:247]\n" \
"v_mfma_f32_16x16x32_bf16 v[244:247], a[192:195], a[236:239], v[244:247]\n" \
"v_mfma_f32_16x16x32_bf16 v[168:171], a[208:211], v[64:67], v[168:171]\n" \
"v_mfma_f32_16x16x32_bf16 v[172:175], a[208:211], v[72:75], v[172:175]\n" \
"v_mfma_f32_16x16x32_bf16 v[240:243], a[208:211], v[80:83], v[240:243]\n" \
"v_mfma_f32_16x16x32_bf16 v[244:247], a[208:211], v[88:91], v[244:247]\n" \
"v_mfma_f32_16x16x32_bf16 v[168:171], a[212:215], v[68:71], v[168:171]\n" \
"v_mfma_f32_16x16x32_bf16 v[172:175], a[212:215], v[76:79], v[172:175]\n" \
"s_waitcnt vmcnt(4)\n" \
"s_mov_b64 exec, 1\n" \
"buffer_store_dword v39, off, s[56:59], s92 offset:32 sc1\n" \
"s_mov_b64 exec, -1\n" \
"v_mfma_f32_16x16x32_bf16 v[240:243], a[212:215], v[84:87], v[240:243]\n" \
"v_readlane_b32 s92, v11, 39\n" \
"v_mfma_f32_16x16x32_bf16 v[244:247], a[212:215], v[92:95], v[244:247]\n" \
"v_mfma_f32_16x16x32_bf16 v[56:59], a[216:219], v[96:99], 0\n" \
"v_mfma_f32_16x16x32_bf16 v[60:63], a[216:219], v[104:107], 0\n" \
"v_mfma_f32_16x16x32_bf16 v[56:59], a[0:3], v[100:103], v[56:59]\n" \
"v_mfma_f32_16x16x32_bf16 v[60:63], a[0:3], v[108:111], v[60:63]\n" \
"s_waitcnt vmcnt(4)\n" \
"s_mov_b64 exec, 1\n" \
"buffer_store_dword v39, off, s[56:59], s93 offset:36 sc1\n" \
"s_mov_b64 exec, -1\n" \
"v_readlane_b32 s93, v11, 40\n" \
"s_waitcnt vmcnt(3)\n" \
"s_mov_b64 exec, 1\n" \
"buffer_store_dword v39, off, s[56:59], s94 offset:40 sc1\n" \
"s_mov_b64 exec, -1\n" \
"buffer_load_dword v37, v40, s[56:59], s74 offen sc1\n" \
"s_waitcnt vmcnt(0)\n" \
"v_cmp_eq_u32_e64 s[98:99], s62, v37\n" \
"s_nop 0\n" \
"s_andn2_b32 s75, 0x1, s98\n" \
"s_cbranch_scc1 L_sx0_%=\n" \
"L_rx0_%=:\n" \
"buffer_load_dwordx4 v[116:119], v20, s[56:59], s95 offen offset:0 sc1\n" \
"s_andn2_b32 s75, 0x2, s98\n" \
"s_cbranch_scc1 L_sx1_%=\n" \
"L_rx1_%=:\n" \
"buffer_load_dwordx4 v[124:127], v20, s[56:59], s96 offen offset:0 sc1\n" \
"s_andn2_b32 s75, 0x4, s98\n" \
"s_cbranch_scc1 L_sx2_%=\n" \
"L_rx2_%=:\n" \
"buffer_load_dwordx4 v[132:135], v20, s[56:59], s97 offen offset:0 sc1\n" \
"s_andn2_b32 s75, s100, s98\n" \
"s_cbranch_scc1 L_sx3_%=\n" \
"L_rx3_%=:\n" \
"buffer_load_dwordx4 v[136:139], v20, s[56:59], s64 offen offset:0 sc1\n" \
"buffer_load_dwordx4 v[208:211], v20, s[56:59], s64 offen offset:2048 sc1\n" \
"s_add_u32 s64, s64, 0x1000\n" \
"buffer_load_dwordx4 v[144:147], v20, s[56:59], s64 offen offset:0 sc1\n" \
"buffer_load_dwordx4 v[216:219], v20, s[56:59], s64 offen offset:2048 sc1\n" \
"s_add_u32 s64, s64, 0x1000\n" \
"buffer_load_dwordx4 v[152:155], v20, s[56:59], s64 offen offset:0 sc1\n" \
"buffer_load_dwordx4 v[224:227], v20, s[56:59], s64 offen offset:2048 sc1\n" \
"s_add_u32 s64, s64, 0x1000\n" \
"buffer_load_dwordx4 v[176:179], v20, s[56:59], s64 offen offset:0 sc1\n" \
"buffer_load_dwordx4 v[248:251], v20, s[56:59], s64 offen offset:2048 sc1\n" \
"s_andn2_b32 s75, 0x10, s98\n" \
"s_cbranch_scc1 L_sx4_%=\n" \
"L_rx4_%=:\n" \
"buffer_load_dwordx4 v[140:143], v20, s[56:59], s65 offen offset:0 sc1\n" \
"buffer_load_dwordx4 v[212:215], v20, s[56:59], s65 offen offset:1024 sc1\n" \
"s_andn2_b32 s75, 0x20, s98\n" \
"s_cbranch_scc1 L_sx5_%=\n" \
"L_rx5_%=:\n" \
"buffer_load_dwordx4 v[148:151], v20, s[56:59], s66 offen offset:0 sc1\n" \
"buffer_load_dwordx4 v[220:223], v20, s[56:59], s66 offen offset:1024 sc1\n" \
"s_andn2_b32 s75, 0x40, s98\n" \
"s_cbranch_scc1 L_sx6_%=\n" \
"L_rx6_%=:\n" \
"buffer_load_dwordx4 v[156:159], v20, s[56:59], s67 offen offset:0 sc1\n" \
"buffer_load_dwordx4 v[228:231], v20, s[56:59], s67 offen offset:1024 sc1\n" \
"s_andn2_b32 s75, 0x80, s98\n" \
"s_cbranch_scc1 L_sx7_%=\n" \
"L_rx7_%=:\n" \
"buffer_load_dwordx4 v[164:167], v20, s[56:59], s68 offen offset:0 sc1\n" \
"buffer_load_dwordx4 v[236:239], v20, s[56:59], s68 offen offset:1024 sc1\n" \
"s_andn2_b32 s75, 0x100, s98\n" \
"s_cbranch_scc1 L_sx8_%=\n" \
"L_rx8_%=:\n" \
"buffer_load_dwordx4 v[188:191], v20, s[56:59], s69 offen offset:1024 sc1\n" \
"s_andn2_b32 s75, 0x200, s98\n" \
"s_cbranch_scc1 L_sx9_%=\n" \
"L_rx9_%=:\n" \
"buffer_load_dwordx4 v[196:199], v20, s[56:59], s70 offen offset:1024 sc1\n" \
"s_andn2_b32 s75, 0x400, s98\n" \
"s_cbranch_scc1 L_sx10_%=\n" \
"L_rx10_%=:\n" \
"buffer_load_dwordx4 v[204:207], v20, s[56:59], s71 offen offset:1024 sc1\n" \
"s_waitcnt vmcnt(21)\n" \
"v_mfma_f32_16x16x32_bf16 v[168:171], v[50:53], v[116:119], v[168:171]\n" \
"v_mfma_f32_16x16x32_bf16 v[172:175], v[48:51], v[116:119], v[172:175]\n" \
"s_waitcnt vmcnt(20)\n" \
"v_mfma_f32_16x16x32_bf16 v[168:171], v[50:53], v[124:127], v[168:171]\n" \
"v_mfma_f32_16x16x32_bf16 v[172:175], v[48:51], v[124:127], v[172:175]\n" \
"s_waitcnt vmcnt(19)\n" \
"v_mfma_f32_16x16x32_bf16 v[168:171], v[50:53], v[132:135], v[168:171]\n" \
"v_mfma_f32_16x16x32_bf16 v[172:175], v[48:51], v[132:135], v[172:175]\n" \
"s_waitcnt vmcnt(18)\n" \
"v_mfma_f32_16x16x32_bf16 v[56:59], v[50:53], v[136:139], v[56:59]\n" \
"v_mfma_f32_16x16x32_bf16 v[60:63], v[48:51], v[136:139], v[60:63]\n" \
"s_waitcnt vmcnt(17)\n" \
"v_mfma_f32_16x16x32_bf16 v[56:59], v[50:53], v[208:211], v[56:59]\n" \
"v_mfma_f32_16x16x32_bf16 v[60:63], v[48:51], v[208:211], v[60:63]\n" \
"s_waitcnt vmcnt(16)\n" \
"v_mfma_f32_16x16x32_bf16 v[56:59], v[50:53], v[144:147], v[56:59]\n" \
"v_mfma_f32_16x16x32_bf16 v[60:63], v[48:51], v[144:147], v[60:63]\n" \
"s_waitcnt vmcnt(15)\n" \
"v_mfma_f32_16x16x32_bf16 v[56:59], v[50:53], v[216:219], v[56:59]\n" \
"v_mfma_f32_16x16x32_bf16 v[60:63], v[48:51], v[216:219], v[60:63]\n" \
"s_waitcnt vmcnt(14)\n" \
"v_mfma_f32_16x16x32_bf16 v[56:59], v[50:53], v[152:155], v[56:59]\n" \
"v_mfma_f32_16x16x32_bf16 v[60:63], v[48:51], v[152:155], v[60:63]\n" \
"s_waitcnt vmcnt(13)\n" \
"v_mfma_f32_16x16x32_bf16 v[56:59], v[50:53], v[224:227], v[56:59]\n" \
"v_mfma_f32_16x16x32_bf16 v[60:63], v[48:51], v[224:227], v[60:63]\n" \
"s_waitcnt vmcnt(12)\n" \
"v_mfma_f32_16x16x32_bf16 v[56:59], v[50:53], v[176:179], v[56:59]\n" \
"v_mfma_f32_16x16x32_bf16 v[60:63], v[48:51], v[176:179], v[60:63]\n" \
"s_waitcnt vmcnt(11)\n" \
"v_mfma_f32_16x16x32_bf16 v[56:59], v[50:53], v[248:251], v[56:59]\n" \
"v_mfma_f32_16x16x32_bf16 v[60:63], v[48:51], v[248:251], v[60:63]\n" \
"s_waitcnt vmcnt(10)\n" \
"v_mfma_f32_16x16x32_bf16 v[168:171], v[50:53], v[140:143], v[168:171]\n" \
"v_mfma_f32_16x16x32_bf16 v[172:175], v[48:51], v[140:143], v[172:175]\n" \
"s_waitcnt vmcnt(9)\n" \
"v_mfma_f32_16x16x32_bf16 v[240:243], v[50:53], v[212:215], v[240:243]\n" \
"v_mfma_f32_16x16x32_bf16 v[244:247], v[48:51], v[212:215], v[244:247]\n" \
"s_waitcnt vmcnt(8)\n" \
"v_mfma_f32_16x16x32_bf16 v[168:171], v[50:53], v[148:151], v[168:171]\n" \
"v_cvt_pk_bf16_f32 v56, v56, v57\n" \
"v_cvt_pk_bf16_f32 v60, v60, v61\n" \
"v_mfma_f32_16x16x32_bf16 v[172:175], v[48:51], v[148:151], v[172:175]\n" \
"v_cvt_pk_bf16_f32 v57, v58, v59\n" \
"s_waitcnt vmcnt(7)\n" \
"v_mfma_f32_16x16x32_bf16 v[240:243], v[50:53], v[220:223], v[240:243]\n" \
"buffer_store_dwordx2 v[56:57], v42, s[88:91], s73 offen sc0 sc1\n" \
"v_mfma_f32_16x16x32_bf16 v[244:247], v[48:51], v[220:223], v[244:247]\n" \
"v_cvt_pk_bf16_f32 v61, v62, v63\n" \
"s_waitcnt vmcnt(7)\n" \
"v_mfma_f32_16x16x32_bf16 v[168:171], v[50:53], v[156:159], v[168:171]\n" \
"buffer_store_dwordx2 v[60:61], v43, s[88:91], s73 offen sc0 sc1\n" \
"v_mfma_f32_16x16x32_bf16 v[172:175], v[48:51], v[156:159], v[172:175]\n" \
"s_waitcnt vmcnt(7)\n" \
"v_mfma_f32_16x16x32_bf16 v[240:243], v[50:53], v[228:231], v[240:243]\n" \
"v_mfma_f32_16x16x32_bf16 v[244:247], v[48:51], v[228:231], v[244:247]\n" \
"s_waitcnt vmcnt(6)\n" \
"v_mfma_f32_16x16x32_bf16 v[168:171], v[50:53], v[164:167], v[168:171]\n" \
"v_mfma_f32_16x16x32_bf16 v[172:175], v[48:51], v[164:167], v[172:175]\n" \
"s_waitcnt vmcnt(5)\n" \
"v_mfma_f32_16x16x32_bf16 v[240:243], v[50:53], v[236:239], v[240:243]\n" \
"v_mfma_f32_16x16x32_bf16 v[244:247], v[48:51], v[236:239], v[244:247]\n" \
"s_waitcnt vmcnt(4)\n" \
"v_mfma_f32_16x16x32_bf16 v[240:243], v[50:53], v[188:191], v[240:243]\n" \
"v_mfma_f32_16x16x32_bf16 v[244:247], v[48:51], v[188:191], v[244:247]\n" \
"s_waitcnt vmcnt(3)\n" \
"v_mfma_f32_16x16x32_bf16 v[240:243], v[50:53], v[196:199], v[240:243]\n" \
"v_cvt_pk_bf16_f32 v168, v168, v169\n" \
"v_cvt_pk_bf16_f32 v172, v172, v173\n" \
"v_mfma_f32_16x16x32_bf16 v[244:247], v[48:51], v[196:199], v[244:247]\n" \
"v_cvt_pk_bf16_f32 v169, v170, v171\n" \
"s_waitcnt vmcnt(2)\n" \
"v_mfma_f32_16x16x32_bf16 v[240:243], v[50:53], v[204:207], v[240:243]\n" \
"v_cvt_pk_bf16_f32 v173, v174, v175\n" \
"v_mfma_f32_16x16x32_bf16 v[244:247], v[48:51], v[204:207], v[244:247]\n" \
"s_mov_b64 exec, 1\n" \
"buffer_store_dword v46, off, s[56:59], s72 offset:0 sc1\n" \
"s_mov_b64 exec, -1\n" \
"buffer_store_dwordx2 v[168:169], v42, s[88:91], s92 offen sc0 sc1\n" \
"buffer_store_dwordx2 v[172:173], v43, s[88:91], s92 offen sc0 sc1\n" \
"s_nop 0\n" \
"v_cvt_pk_bf16_f32 v240, v240, v241\n" \
"v_cvt_pk_bf16_f32 v241, v242, v243\n" \
"v_cvt_pk_bf16_f32 v244, v244, v245\n" \
"v_cvt_pk_bf16_f32 v245, v246, v247\n" \
"buffer_store_dwordx2 v[240:241], v42, s[88:91], s93 offen sc0 sc1\n" \
"buffer_store_dwordx2 v[244:245], v43, s[88:91], s93 offen sc0 sc1\n"
#define ATT_VCLOB "v21","v22","v23","v32","v33","v34","v35","v36","v37","v38","v39","v40","v41","v42","v43","v44","v45","v46","v47","v48","v49","v50","v51","v52","v53","v54","v55","v56","v57","v58","v59","v60","v61","v62","v63","v64","v65","v66","v67","v68","v69","v70","v71","v72","v73","v74","v75","v76","v77","v78","v79","v80","v81","v82","v83","v84","v85","v86","v87","v88","v89","v90","v91","v92","v93","v94","v95","v96","v97","v98","v99","v100","v101","v102","v103","v104","v105","v106","v107","v108","v109","v110","v111","v112","v113","v114","v115","v116","v117","v118","v119","v120","v121","v122","v123","v124","v125","v126","v127","v128","v129","v130","v131","v132","v133","v134","v135","v136","v137","v138","v139","v140","v141","v142","v143","v144","v145","v146","v147","v148","v149","v150","v151","v152","v153","v154","v155","v156","v157","v158","v159","v160","v161","v162","v163","v164","v165","v166","v167","v168","v169","v170","v171","v172","v173","v174","v175","v176","v177","v178","v179","v180","v181","v182","v183","v184","v185","v186","v187","v188","v189","v190","v191","v192","v193","v194","v195","v196","v197","v198","v199","v200","v201","v202","v203","v204","v205","v206","v207","v208","v209","v210","v211","v212","v213","v214","v215","v216","v217","v218","v219","v220","v221","v222","v223","v224","v225","v226","v227","v228","v229","v230","v231","v232","v233","v234","v235","v236","v237","v238","v239","v240","v241","v242","v243","v244","v245","v246","v247","v248","v249","v250","v251","v252","v253","v254","v255"
#define ATT_SCLOB "s63","s64","s65","s66","s67","s68","s69","s70","s71","s72","s73","s74","s75","s76","s77","s78","s79","s80","s81","s82","s83","s84","s85","s86","s87","s88","s89","s90","s91","s92","s93","s94","s95","s96","s97","s98","s99","s100","s101"
enum { T_fo, T_fa, T_fas, T_fb, T_fbs, T_f8, T_mk8, T_wol, T_woh, T_wbl, T_wbh, T_outl, T_outh, T_r1l, T_r1h, T_r1n, T_r1s, T_m0, T_m1, T_mw, T_m4, T_ml, T_w0, T_w1, T_w2, T_w3, T_A1, T_A2, T_A3, T_B1, T_B2, T_B3, T_Ao, T_Bo, T_Al, T_Bl, T_l8, T_xr, T_x8r, T_oA, T_oB, T_o8, T_x0, T_x1, T_x2, T_x3, T_x4, T_x5, T_x6, T_x8, T_ep, T_xf, T_kf };

__device__ __forceinline__ void out_consts(u32x4 (&vp)[3], const u16* wo, const u16* wob, const u16* out, const unsigned b, const unsigned w, const unsigned lane) {
    const unsigned sp = b & 7u, nt = (b >> 3) & 3u, j = b >> 5, head = 4u * sp + nt, OOR = 0x40000000u;
    unsigned vw[9];
#pragma unroll
    for (unsigned i = 0; i < 9u; ++i) {
        const unsigned q = 64u * i + lane, row = __umul24(q, 3641u) >> 16, c = q - __umul24(row, 18u);
        vw[i] = __umul24(row, DM * 2) + (c < 16u ? (c * 16u + sp * 32u + 32u) & 255u : c * 16u) - (i & 3u) * 1024u;
    }
    const unsigned row = 16u * w + (lane >> 2), r9 = __umul24(16u * (row >> 5) + (row & 12u) + 2u * ((row >> 4) & 1u) + ((row >> 1) & 1u), 18u) + 16u + (row & 1u);
    unsigned vwl = __umul24(r9, DM * 2) + ((lane & 2u) ? 256u : sp * 32u) + (lane & 1u) * 16u, vrd = __umul24(lane >> 2, 288u) + (lane & 3u) * 8u;
    asm volatile("" : "+v"(vw[0]), "+v"(vw[1]), "+v"(vw[2]), "+v"(vw[3]), "+v"(vw[4]), "+v"(vw[5]), "+v"(vw[6]), "+v"(vw[7]), "+v"(vw[8]), "+v"(vwl), "+v"(vrd));
    unsigned tab = 0;
#define TB(n, v) asm("v_writelane_b32 %0, %1, %2" : "+v"(tab) : "s"((unsigned)(v)), "n"((int)T_##n))
    const unsigned FJ = (unsigned)WS_F + (F_J + 640u * j) * 4u, mt = 2u * (w & 2u) + (w & 1u), w2 = w < 2u ? w : 2u, hw = head * 147456u;
    const unsigned FR = (unsigned)WS_F + F_R * 4u + (8u * j + sp) * 1024u;
    TB(fo, FJ + 4u * head); TB(fa, FR + (w | 2u) * 256u); TB(fas, FR + w * 256u + nt * 4u);
    TB(fb, FR + 128u + w * 256u); TB(fbs, FR + 128u + w * 256u + nt * 4u);
    TB(ep, b + w ? OOR : (unsigned)WS_F + F_EPOCH * 4u);
    const u16* pwo = wo + nt * 144u; const u16* pwb = wob + nt * 144u; const u16* pr1 = w < 3u ? pwo : pwb;
    TB(wol, PLO(pwo)); TB(woh, PHI(pwo) & 0xffffu); TB(wbl, PLO(pwb)); TB(wbh, PHI(pwb) & 0xffffu); TB(outl, PLO(out)); TB(outh, PHI(out) & 0xffffu); TB(r1l, PLO(pr1)); TB(r1h, PHI(pr1) & 0xffffu);
    TB(r1n, (w < 3u ? 72u * sp + 71u : 4095u) * 1152u + 288u); TB(r1s, w < 3u ? (72u * sp + 32u * w) * 1152u : hw + 110592u);
    TB(m0, 131072u + 9216u * w2); TB(w0, hw + 36864u * w2); TB(m1, w < 3u ? 9216u * w : 73728u); TB(mw, 9216u * w); TB(m4, w < 3u ? 131072u + 9216u * w : 64512u); TB(ml, 119808u + 1024u * w);
    const unsigned tA = (unsigned)WS_AF + (8u * j + mt) * (KS_TOT * 1024u), tB = (unsigned)WS_AF + (120u - 8u * j + mt) * (KS_TOT * 1024u);
#define HK(k) (4u * sp + ((nt + k) & 3u))
    TB(w1, HK(1u) * 147456u + w * 36864u); TB(w2, HK(2u) * 147456u + w * 36864u); TB(w3, HK(3u) * 147456u + w * 36864u);
    TB(A1, tA + 18432u + HK(1u) * 4096u); TB(A2, tA + 18432u + HK(2u) * 4096u); TB(A3, tA + 18432u + HK(3u) * 4096u);
    TB(B1, tB + 18432u + HK(1u) * 4096u); TB(B2, tB + 18432u + HK(2u) * 4096u); TB(B3, tB + 18432u + HK(3u) * 4096u);
    TB(Ao, tA + sp * 2048u); TB(Bo, tB + sp * 2048u); TB(Al, tA + 16384u); TB(Bl, tB + 16384u);
    const bool red = w == (sp >> 1), blk = sp & 1u;
    const unsigned xg = (unsigned)WS_X + (4u * j + nt) * 589824u + w * 16384u, oA = (128u * j + 16u * mt) * 1152u + nt * 288u, oB = (1920u - 128u * j + 16u * mt) * 1152u + nt * 288u;
    TB(l8, red ? (blk ? tB : tA) + 16384u : OOR); TB(xr, xg + sp * 65536u); TB(x8r, red ? xg + 524288u + (sp & 1u) * 1024u : OOR);
    TB(oA, oA + sp * 32u); TB(oB, oB + sp * 32u); TB(o8, red ? (blk ? oB : oA) + 256u : OOR); TB(x8, xg + 524288u + sp * 2048u);
    TB(f8, xg + w * 131072u + 14400u + sp * 4u); TB(mk8, red ? 0xff0000u : 0u); TB(xf, xg + sp * 65536u + 14336u);
    TB(kf, (unsigned)WS_F + (F_KV + head * 64u) * 4u);
#define XI(i) TB(x##i, xg + ((sp + 1u + i) & 7u) * 65536u + i * 2048u)
    XI(0); XI(1); XI(2); XI(3); XI(4); XI(5); XI(6);
#undef XI
#undef HK
#undef TB
    vp[0] = (u32x4){vw[0], vw[1], vw[2], vw[3]}; vp[1] = (u32x4){vw[4], vw[5], vw[6], vw[7]}; vp[2] = (u32x4){vw[8], vwl, vrd, tab};
}
__global__ __launch_bounds__(256, 1) __attribute__((aligned(4096))) void fused_kernel(const u16* __restrict__ x, const u16* __restrict__ ckv, const u16* __restrict__ wq, const u16* __restrict__ wuk,
                                                       const u16* __restrict__ wuv, const u16* __restrict__ wqkv, const u16* __restrict__ gw,
                                                       const u16* __restrict__ wo, const u16* __restrict__ wob, char* __restrict__ ws, u16* __restrict__ out) {
    using namespace att;
    extern __shared__ __attribute__((aligned(16))) char lds[];
    unsigned wid = rfl(threadIdx.x >> 6);
    code_touch((blockIdx.x >> 3) * 4u + wid, (unsigned)(size_t)lds + 151552u + wid * 512u);
    SBAR();
    asm volatile("" :: "s"(x), "s"(ckv), "s"(wq), "s"(wuk), "s"(wuv), "s"(wqkv), "s"(gw), "s"(wo), "s"(wob), "s"(ws), "s"(out));
    unsigned tag;
    int b = blockIdx.x;
    u32x4 vp[5], sa[5]; unsigned sd[2];
    const unsigned vl = proj_phase(x, ckv, wq, wuk, wuv, wqkv, gw, wo, wob, out, ws, lds, wid, tag, vp, sa, sd);
    asm volatile("" : "+s"(ws));
    unsigned* const flags = (unsigned*)(ws + WS_F);
    RTS(2);
#define AST(L) do {} while (0)
#define SD(J8, H, W) ((unsigned)WS_AF + (((J8) + 2u * ((W) & 2u) + ((W) & 1u)) * KS_TOT + 18u + 4u * (H)) * 1024u)
#define SRDA (u32x4){PLO(ws), PHI(ws) & 0xffffu, (unsigned)WS_L, 0x00020000u}
#define ATT_TAIL "s_endpgm\n"
    asm volatile(ATT_ASM ATT_TAIL "L_end_%=:\n s_nop 1"
                 :
                 : "{v[0:3]}"(vp[0]), "{v[4:7]}"(vp[1]), "{v[8:11]}"(vp[2]), "{v[12:15]}"(vp[3]), "{v[16:19]}"(vp[4]), "{v20}"(vl), "{s[56:59]}"(sa[4]), "{s60}"(sd[0]), "{s61}"(sd[1]), "{s62}"(tag),
                   "{s[8:11]}"(sa[0]), "{s[12:15]}"(sa[1]), "{s[16:19]}"(sa[2]), "{s[20:23]}"(sa[3])
                 : "memory", "m0", "vcc", "scc", ATT_VCLOB, ATT_SCLOB, ACLOB_LO, ACLOB_HI);
    {
        const u16* const __attribute__((address_space(4)))* ka = (const u16* const __attribute__((address_space(4)))*)__builtin_amdgcn_kernarg_segment_ptr();
        unsigned b2 = b, w2 = wid;
        asm volatile("s_waitcnt vmcnt(0)" : "+s"(ka), "+s"(b2), "+s"(w2), "+s"(ws) :: "memory");
        const unsigned j2 = b2 >> 5, p2 = w2 & 1u, h2 = (b2 & 7u) * 4u + ((b2 >> 3) & 3u);
#pragma unroll 1
        for (unsigned i = w2 & 2u; i < 2u + 3u * (w2 & 2u); ++i)
            att_exact(ka[0], ka[2], ws, h2, (i < 4u ? 1920u - 128u * j2 : 128u * j2 - 128u) + 32u * i + 16u * p2, *reinterpret_cast<const float*>(lds + LDS_G + (32u * i + 16u * p2 + (lane_id() & 15u)) * 4u));
        asm volatile("s_waitcnt vmcnt(0)" ::: "memory");
        unsigned* fa = (unsigned*)(ws + WS_F) + F_R + (8u * j2 + (b2 & 7u)) * 256u + (2u + p2) * 64u + ((b2 >> 3) & 3u);
        if (w2 & 2u) st_flag(fa, tag);
        wait_flags(fa, ld_flag(fa), tag);
        const unsigned l2 = lane_id(); u32x4 c[3]; out_consts(c, ka[7], ka[8], ka[10], b2, w2, l2);
        asm volatile("s_waitcnt vmcnt(0) lgkmcnt(0)\n\ts_nop 7\n\ts_branch L_att_stub" :: "{v[0:3]}"(c[0]), "{v[4:7]}"(c[1]), "{v[8:11]}"(c[2]), "{v20}"(l2 * 16u), "{s[56:59]}"(SRDA), "{s60}"(SD(8u * j2, h2, w2)),
                     "{s61}"(SD(120u - 8u * j2, h2, w2)), "{s62}"(tag) : "memory");
    }
    RTS(5);
}

extern "C" void kernel_launch(void* const* d_in, const int* in_sizes, int n_in, void* d_out, int out_size, void* d_ws, size_t ws_size,
                              hipStream_t stream) {
    (void)hipFuncSetAttribute((const void*)fused_kernel, hipFuncAttributeMaxDynamicSharedMemorySize, att::LDS_BYTES);
    fused_kernel<<<dim3(256), 256, att::LDS_BYTES, stream>>>((const u16*)d_in[0], (const u16*)d_in[4], (const u16*)d_in[5], (const u16*)d_in[6], (const u16*)d_in[7], (const u16*)d_in[1],
                                                             (const u16*)d_in[9], (const u16*)d_in[2], (const u16*)d_in[8], (char*)d_ws, (u16*)d_out);
}
```

```cpp
#include <hip/hip_runtime.h>
#include <hip/hip_bf16.h>
#include <stdint.h>
#include <utility>

typedef short bf16x8 __attribute__((ext_vector_type(8)));
typedef short s16x4 __attribute__((ext_vector_type(4)));
typedef float f32x16 __attribute__((ext_vector_type(16)));
typedef float f32x4 __attribute__((ext_vector_type(4)));
typedef unsigned u32x4 __attribute__((ext_vector_type(4)));
typedef unsigned short u16;

constexpr int S = 2048, DM = 576, NH = 32, HD = 128, DC = 512, NQKV = 1728, DHA = 18;
constexpr int NBIG = NH * HD;
constexpr int KS_TOT = 146;

#define SBAR() __builtin_amdgcn_sched_barrier(0)
constexpr size_t WS_Q = 0, WS_K = WS_Q + (size_t)S * NBIG * 2, WS_V = WS_K + (size_t)S * NBIG * 2, WS_AF = WS_V + (size_t)S * NBIG * 2, WS_X = WS_AF + (size_t)128 * KS_TOT * 1024,
                 WS_P = WS_X + (size_t)64 * 16 * 9216, WS_F = WS_P + (size_t)128 * (4 * 32 * 256 + 1024) * 4;
constexpr size_t WS_L = (size_t)96 << 20;
static_assert(WS_F == 96206848, "workspace map is baked into the generated asm");
constexpr int F_EPOCH = 0, F_J = 1024, F_ST = 8192, F_KV = 40960, F_R = 65536;
constexpr unsigned TAG_STEP = 0x9E3779B1u;
__device__ __forceinline__ unsigned ld_flag(const unsigned* p) { return __hip_atomic_load(p, __ATOMIC_RELAXED, __HIP_MEMORY_SCOPE_AGENT); }
__device__ __forceinline__ void st_flag(unsigned* p, unsigned v) { __hip_atomic_store(p, v, __ATOMIC_RELAXED, __HIP_MEMORY_SCOPE_AGENT); }
__device__ __forceinline__ unsigned ld_flag_async(const unsigned* p) { unsigned v; asm volatile("global_load_dword %0, %1, off sc1" : "=v"(v) : "v"(p) : "memory"); return v; }
#define WAITV(v, N) asm volatile("s_waitcnt vmcnt(" #N ")" : "+v"(v) :: "memory")
__device__ __forceinline__ void wait_flags(const unsigned* p, unsigned v, unsigned tag) {
    if (__builtin_expect(__builtin_amdgcn_ballot_w64(v != tag) != 0ull, 0)) {
        unsigned spins = 0;
        do { __builtin_amdgcn_s_sleep(1); } while (__builtin_amdgcn_ballot_w64(ld_flag(p) != tag) != 0ull && ++spins < (1u << 22));
    }
    asm volatile("" ::: "memory");
}

__device__ __forceinline__ unsigned cvtpk(float lo, float hi) {
    unsigned r; asm volatile("v_cvt_pk_bf16_f32 %0, %1, %2" : "=v"(r) : "v"(lo), "v"(hi)); return r;
}
__device__ __forceinline__ float bf2f(u16 v) { return __uint_as_float(((unsigned)v) << 16); }
__device__ __forceinline__ int crow(int r, int hi) { return (r & 3) + 8 * (r >> 2) + 4 * hi; }

typedef __attribute__((address_space(3))) s16x4* lds_s16x4_p;
__device__ __forceinline__ s16x4 trrd(unsigned addr) { return __builtin_amdgcn_ds_read_tr16_b64_v4i16((lds_s16x4_p)addr); }

__device__ __forceinline__ unsigned lane_id() { unsigned l; asm volatile("v_mbcnt_lo_u32_b32 %0, -1, 0\n\tv_mbcnt_hi_u32_b32 %0, -1, %0" : "=v"(l)); return l; }
#define RTS(slot) do {} while (0)
#define CT_L 5u
__device__ __forceinline__ void code_touch(unsigned wi, unsigned lds_scratch) {
    unsigned long long pc; asm volatile("s_getpc_b64 %0" : "=s"(pc));
    asm volatile("s_mov_b32 m0, %0\n\ts_mov_b64 exec, %3\n\tglobal_load_lds_dword %1, %2\n\ts_mov_b64 exec, -1" :: "s"(lds_scratch), "v"(((threadIdx.x & 63u) * 128u + wi) * 128u), "s"(pc & ~127ull), "n"((1u << CT_L) - 1u) : "m0", "memory");
}
#define ACLOB_LO "a0","a1","a2","a3","a4","a5","a6","a7","a8","a9","a10","a11","a12","a13","a14","a15","a16","a17","a18","a19","a20","a21","a22","a23","a24","a25","a26","a27","a28","a29","a30","a31","a32","a33","a34","a35","a36","a37","a38","a39","a40","a41","a42","a43","a44","a45","a46","a47","a48","a49","a50","a51","a52","a53","a54","a55","a56","a57","a58","a59","a60","a61","a62","a63","a64","a65","a66","a67","a68","a69","a70","a71","a72","a73","a74","a75","a76","a77","a78","a79","a80","a81","a82","a83","a84","a85","a86","a87","a88","a89","a90","a91","a92","a93","a94","a95","a96","a97","a98","a99","a100","a101","a102","a103","a104","a105","a106","a107","a108","a109","a110","a111","a112","a113","a114","a115","a116","a117","a118","a119","a120","a121","a122","a123","a124","a125","a126","a127"
#define ACLOB_HI "a128","a129","a130","a131","a132","a133","a134","a135","a136","a137","a138","a139","a140","a141","a142","a143","a144","a145","a146","a147","a148","a149","a150","a151","a152","a153","a154","a155","a156","a157","a158","a159","a160","a161","a162","a163","a164","a165","a166","a167","a168","a169","a170","a171","a172","a173","a174","a175","a176","a177","a178","a179","a180","a181","a182","a183","a184","a185","a186","a187","a188","a189","a190","a191","a192","a193","a194","a195","a196","a197","a198","a199","a200","a201","a202","a203","a204","a205","a206","a207","a208","a209","a210","a211","a212","a213","a214","a215","a216","a217","a218","a219","a220","a221","a222","a223","a224","a225","a226","a227","a228","a229","a230","a231","a232","a233","a234","a235","a236","a237","a238","a239","a240","a241","a242","a243","a244","a245","a246","a247","a248","a249","a250","a251","a252","a253","a254","a255"
#define PROJ_PRE0 \
"s_add_u32 s62, %[rc], 0x0\n" \
"s_and_b32 s62, s62, 0x3ff\n" \
"s_lshl_b32 s63, s62, 12\n" \
"s_add_u32 m0, %[mA], 0x0\n" \
"s_add_u32 s85, s62, 0x0\n" \
"buffer_load_dwordx4 v22, s[64:67], s85 offen lds\n" \
"s_add_u32 m0, %[mA], 0x400\n" \
"s_add_u32 s84, s62, 0x2000\n" \
"buffer_load_dwordx4 v23, s[64:67], s84 offen lds\n" \
"s_add_u32 m0, %[mB], 0x0\n" \
"s_add_u32 s85, s63, 0x0\n" \
"buffer_load_dwordx4 v26, s[68:71], s85 offen lds\n" \
"s_add_u32 m0, %[mB], 0x4000\n" \
"s_add_u32 s84, s63, 0x0\n" \
"buffer_load_dwordx4 v26, s[72:75], s84 offen lds\n" \
"s_add_u32 m0, %[mA], 0x800\n" \
"s_add_u32 s85, s62, 0x4000\n" \
"buffer_load_dwordx4 v22, s[64:67], s85 offen lds\n" \
"s_add_u32 m0, %[mA], 0xc00\n" \
"s_add_u32 s84, s62, 0x6000\n" \
"buffer_load_dwordx4 v23, s[64:67], s84 offen lds\n" \
"s_add_u32 m0, %[mB], 0x400\n" \
"s_add_u32 s85, s63, 0x8000\n" \
"buffer_load_dwordx4 v26, s[68:71], s85 offen lds\n" \
"s_add_u32 m0, %[mB], 0x4400\n" \
"s_add_u32 s84, s63, 0x8000\n" \
"buffer_load_dwordx4 v26, s[72:75], s84 offen lds\n"
#define PROJ_PRE1 \
"s_add_u32 s62, %[rc], 0x0\n" \
"s_and_b32 s62, s62, 0x3ff\n" \
"s_lshl_b32 s63, s62, 12\n" \
"s_add_u32 m0, %[mA], 0x1000\n" \
"s_add_u32 s85, s62, 0x8000\n" \
"buffer_load_dwordx4 v22, s[64:67], s85 offen lds\n" \
"s_add_u32 m0, %[mA], 0x1400\n" \
"s_add_u32 s84, s62, 0xa000\n" \
"buffer_load_dwordx4 v23, s[64:67], s84 offen lds\n" \
"s_add_u32 m0, %[mB], 0x800\n" \
"s_add_u32 s85, s63, 0x10000\n" \
"buffer_load_dwordx4 v27, s[68:71], s85 offen lds\n" \
"s_add_u32 m0, %[mB], 0x4800\n" \
"s_add_u32 s84, s63, 0x10000\n" \
"buffer_load_dwordx4 v27, s[72:75], s84 offen lds\n" \
"s_add_u32 m0, %[mA], 0x1800\n" \
"s_add_u32 s85, s62, 0xc000\n" \
"buffer_load_dwordx4 v22, s[64:67], s85 offen lds\n" \
"s_add_u32 m0, %[mA], 0x1c00\n" \
"s_add_u32 s84, s62, 0xe000\n" \
"buffer_load_dwordx4 v23, s[64:67], s84 offen lds\n" \
"s_add_u32 m0, %[mB], 0xc00\n" \
"s_add_u32 s85, s63, 0x18000\n" \
"buffer_load_dwordx4 v27, s[68:71], s85 offen lds\n" \
"s_add_u32 m0, %[mB], 0x4c00\n" \
"s_add_u32 s84, s63, 0x18000\n" \
"buffer_load_dwordx4 v27, s[72:75], s84 offen lds\n"
#define PROJ_PRE2 \
"s_add_u32 s62, %[rc], 0x80\n" \
"s_and_b32 s62, s62, 0x3ff\n" \
"s_lshl_b32 s63, s62, 12\n" \
"s_add_u32 m0, %[mA], 0x8000\n" \
"s_add_u32 s85, s62, 0x0\n" \
"buffer_load_dwordx4 v22, s[64:67], s85 offen lds\n" \
"s_add_u32 m0, %[mA], 0x8400\n" \
"s_add_u32 s84, s62, 0x2000\n" \
"buffer_load_dwordx4 v23, s[64:67], s84 offen lds\n"
#define PROJ_PRE3 \
"s_add_u32 s62, %[rc], 0x80\n" \
"s_and_b32 s62, s62, 0x3ff\n" \
"s_lshl_b32 s63, s62, 12\n" \
"s_add_u32 m0, %[mB], 0x8000\n" \
"s_add_u32 s85, s63, 0x0\n" \
"buffer_load_dwordx4 v26, s[68:71], s85 offen lds\n" \
"s_add_u32 m0, %[mB], 0xc000\n" \
"s_add_u32 s84, s63, 0x0\n" \
"buffer_load_dwordx4 v26, s[72:75], s84 offen lds\n" \
"s_add_u32 m0, %[mA], 0x8800\n" \
"s_add_u32 s85, s62, 0x4000\n" \
"buffer_load_dwordx4 v22, s[64:67], s85 offen lds\n" \
"s_add_u32 m0, %[mA], 0x8c00\n" \
"s_add_u32 s84, s62, 0x6000\n" \
"buffer_load_dwordx4 v23, s[64:67], s84 offen lds\n" \
"s_add_u32 m0, %[mB], 0x8400\n" \
"s_add_u32 s85, s63, 0x8000\n" \
"buffer_load_dwordx4 v26, s[68:71], s85 offen lds\n" \
"s_add_u32 m0, %[mB], 0xc400\n" \
"s_add_u32 s84, s63, 0x8000\n" \
"buffer_load_dwordx4 v26, s[72:75], s84 offen lds\n" \
"s_add_u32 m0, %[mA], 0x9000\n" \
"s_add_u32 s85, s62, 0x8000\n" \
"buffer_load_dwordx4 v22, s[64:67], s85 offen lds\n" \
"s_add_u32 m0, %[mA], 0x9400\n" \
"s_add_u32 s84, s62, 0xa000\n" \
"buffer_load_dwordx4 v23, s[64:67], s84 offen lds\n"
#define PROJ_TKE 0
#define PROJ_T0 0
#define PROJ_ROT 2*(sp+mt)
#define PROJ_TC 0
#define PROJ_ASM \
".p2align 3\n" \
"s_branch L_go_%=\n" \
"L_pl_%=:\n" \
"s_mov_b32 s63, 0\n" \
"L_ql_%=:\n" \
"s_sleep 1\n" \
"v_bfe_u32 v44, v20, 2, 5\n" \
"buffer_load_dword v44, v44, s[80:83], s39 offen offset:64 sc1\n" \
"s_waitcnt vmcnt(0)\n" \
"v_cmp_ne_u32_e32 vcc, %[tag], v44\n" \
"s_add_u32 s63, s63, 1\n" \
"s_nop 3\n" \
"s_cbranch_vccz L_rl_%=\n" \
"s_cmp_gt_u32 s63, 0x400000\n" \
"s_cbranch_scc0 L_ql_%=\n" \
"s_branch L_rl_%=\n" \
"L_pa_%=:\n" \
"s_mov_b32 s63, 0\n" \
"L_qa_%=:\n" \
"s_sleep 1\n" \
"v_bfe_u32 v44, v20, 2, 5\n" \
"buffer_load_dword v44, v44, s[80:83], s39 offen offset:0 sc1\n" \
"s_waitcnt vmcnt(0)\n" \
"v_cmp_ne_u32_e32 vcc, %[tag], v44\n" \
"s_add_u32 s63, s63, 1\n" \
"s_nop 3\n" \
"s_cbranch_vccz L_ra_%=\n" \
"s_cmp_gt_u32 s63, 0x400000\n" \
"s_cbranch_scc0 L_qa_%=\n" \
"s_branch L_ra_%=\n" \
"L_go_%=:\n" \
"v_xor_b32_e32 v33, 0x20, v32\n" \
"s_mov_b32 s80, %[wsl]\n" \
"s_and_b32 s81, %[wsh], 0xffff\n" \
"s_mov_b32 s82, 0x10000000\n" \
"s_mov_b32 s83, 0x20000\n" \
"s_mov_b32 s84, 0x5bc0000\n" \
"s_add_u32 s62, s22, 0x80\n" \
"buffer_load_dword v180, off, s[80:83], s84 sc1\n" \
"s_and_b32 s62, s62, 0x3ff\n" \
"s_lshl_b32 s63, s62, 12\n" \
"v_xor_b32_e64 v34, 0x40, v32\n" \
"v_xor_b32_e32 v35, 0x60, v32\n" \
"s_waitcnt vmcnt(11)\n" \
"s_barrier\n" \
"ds_read_b64_tr_b16 v[120:121], v32 offset:8192\n" \
"ds_read_b64_tr_b16 v[122:123], v32 offset:9216\n" \
"ds_read_b128 v[56:59], v29 offset:16384\n" \
"ds_read_b64_tr_b16 v[116:117], v32 offset:0\n" \
"ds_read_b64_tr_b16 v[118:119], v32 offset:1024\n" \
"ds_read_b128 v[52:55], v28 offset:16384\n" \
"s_add_u32 m0, %[mB], 0x8800\n" \
"s_add_u32 s85, s63, 0x10000\n" \
"s_mov_b32 s76, s12\n" \
"ds_read_b128 v[60:63], v28 offset:18432\n" \
"s_and_b32 s77, s13, 0xffff\n" \
"s_mov_b32 s78, 0x1e6000\n" \
"s_mov_b32 s79, 0x20000\n" \
"ds_read_b128 v[64:67], v29 offset:18432\n" \
"s_waitcnt lgkmcnt(5)\n" \
"v_mfma_f32_16x16x32_bf16 a[0:3], v[120:123], v[56:59], 0\n" \
"ds_read_b128 v[72:75], v29 offset:20480\n" \
"ds_read_b128 v[68:71], v28 offset:20480\n" \
"ds_read_b128 v[76:79], v28 offset:22528\n" \
"buffer_load_dwordx4 v27, s[68:71], s85 offen lds\n" \
"ds_read_b128 v[80:83], v29 offset:22528\n" \
"s_waitcnt lgkmcnt(6)\n" \
"v_mfma_f32_16x16x32_bf16 a[0:3], v[116:119], v[52:55], a[0:3]\n" \
"ds_read_b64_tr_b16 v[128:129], v33 offset:8192\n" \
"s_waitcnt lgkmcnt(6)\n" \
"v_mfma_f32_16x16x32_bf16 a[32:35], v[116:119], v[60:63], 0\n" \
"ds_read_b64_tr_b16 v[130:131], v33 offset:9216\n" \
"ds_read_b64_tr_b16 v[124:125], v33 offset:0\n" \
"ds_read_b64_tr_b16 v[126:127], v33 offset:1024\n" \
"s_waitcnt lgkmcnt(8)\n" \
"s_nop 0\n" \
"v_mfma_f32_16x16x32_bf16 a[32:35], v[120:123], v[64:67], a[32:35]\n" \
"ds_read_b64_tr_b16 v[136:137], v34 offset:8192\n" \
"ds_read_b64_tr_b16 v[138:139], v34 offset:9216\n" \
"ds_read_b64_tr_b16 v[132:133], v34 offset:0\n" \
"s_add_u32 m0, %[mB], 0xc800\n" \
"ds_read_b64_tr_b16 v[134:135], v34 offset:1024\n" \
"s_add_u32 s84, s63, 0x10000\n" \
"buffer_load_dwordx4 v27, s[72:75], s84 offen lds\n" \
"ds_read_b64_tr_b16 v[144:145], v35 offset:8192\n" \
"ds_read_b64_tr_b16 v[146:147], v35 offset:9216\n" \
"ds_read_b64_tr_b16 v[140:141], v35 offset:0\n" \
"s_lshr_b32 s95, %[b], 3\n" \
"ds_read_b64_tr_b16 v[142:143], v35 offset:1024\n" \
"s_lshl_b32 s96, s95, 2\n" \
"ds_read_b64_tr_b16 v[152:153], v32 offset:24576\n" \
"s_add_u32 s97, s96, %[w]\n" \
"ds_read_b64_tr_b16 v[154:155], v32 offset:25600\n" \
"v_lshrrev_b32_e32 v212, 4, v20\n" \
"s_mul_i32 s98, 27, s97\n" \
"v_min_u32_e32 v213, 26, v212\n" \
"v_add_u32_e64 v214, s98, v213\n" \
"v_mul_u32_u24_e32 v215, 0xaaab, v214\n" \
"s_waitcnt lgkmcnt(15)\n" \
"v_mfma_f32_16x16x32_bf16 a[64:67], v[120:123], v[72:75], 0\n" \
"ds_read_b64_tr_b16 v[148:149], v32 offset:16384\n" \
"ds_read_b64_tr_b16 v[150:151], v32 offset:17408\n" \
"v_mfma_f32_16x16x32_bf16 a[64:67], v[116:119], v[68:71], a[64:67]\n" \
"s_add_u32 m0, %[mA], 0x9800\n" \
"s_add_u32 s85, s62, 0xc000\n" \
"v_mfma_f32_16x16x32_bf16 a[96:99], v[116:119], v[76:79], 0\n" \
"ds_read_b64_tr_b16 v[160:161], v33 offset:24576\n" \
"s_waitcnt lgkmcnt(15)\n" \
"v_mfma_f32_16x16x32_bf16 a[96:99], v[120:123], v[80:83], a[96:99]\n" \
"buffer_load_dwordx4 v22, s[64:67], s85 offen lds\n" \
"v_mfma_f32_16x16x32_bf16 a[4:7], v[128:131], v[56:59], 0\n" \
"s_add_u32 m0, %[mA], 0x9c00\n" \
"s_waitcnt lgkmcnt(13)\n" \
"s_nop 0\n" \
"v_mfma_f32_16x16x32_bf16 a[4:7], v[124:127], v[52:55], a[4:7]\n" \
"s_add_u32 s84, s62, 0xe000\n" \
"ds_read_b64_tr_b16 v[162:163], v33 offset:25600\n" \
"v_mfma_f32_16x16x32_bf16 a[36:39], v[124:127], v[60:63], 0\n" \
"buffer_load_dwordx4 v23, s[64:67], s84 offen lds\n" \
"v_mfma_f32_16x16x32_bf16 a[36:39], v[128:131], v[64:67], a[36:39]\n" \
"s_add_u32 m0, %[mB], 0x8c00\n" \
"s_add_u32 s85, s63, 0x18000\n" \
"v_mfma_f32_16x16x32_bf16 a[68:71], v[128:131], v[72:75], 0\n" \
"ds_read_b64_tr_b16 v[156:157], v33 offset:16384\n" \
"ds_read_b64_tr_b16 v[158:159], v33 offset:17408\n" \
"v_mfma_f32_16x16x32_bf16 a[68:71], v[124:127], v[68:71], a[68:71]\n" \
"buffer_load_dwordx4 v27, s[68:71], s85 offen lds\n" \
"v_mfma_f32_16x16x32_bf16 a[100:103], v[124:127], v[76:79], 0\n" \
"s_add_u32 m0, %[mB], 0xcc00\n" \
"s_add_u32 s84, s63, 0x18000\n" \
"v_mfma_f32_16x16x32_bf16 a[100:103], v[128:131], v[80:83], a[100:103]\n" \
"ds_read_b64_tr_b16 v[168:169], v34 offset:24576\n" \
"s_waitcnt lgkmcnt(7)\n" \
"v_mfma_f32_16x16x32_bf16 a[8:11], v[136:139], v[56:59], 0\n" \
"buffer_load_dwordx4 v27, s[72:75], s84 offen lds\n" \
"v_mfma_f32_16x16x32_bf16 a[8:11], v[132:135], v[52:55], a[8:11]\n" \
"ds_read_b64_tr_b16 v[170:171], v34 offset:25600\n" \
"ds_read_b64_tr_b16 v[164:165], v34 offset:16384\n" \
"v_mfma_f32_16x16x32_bf16 a[40:43], v[132:135], v[60:63], 0\n" \
"ds_read_b64_tr_b16 v[166:167], v34 offset:17408\n" \
"v_lshrrev_b32_e32 v216, 18, v215\n" \
"v_mfma_f32_16x16x32_bf16 a[40:43], v[136:139], v[64:67], a[40:43]\n" \
"v_mul_u32_u24_e32 v218, 6, v216\n" \
"v_sub_u32_e32 v219, v214, v218\n" \
"v_mfma_f32_16x16x32_bf16 a[72:75], v[136:139], v[72:75], 0\n" \
"v_lshrrev_b32_e32 v220, 1, v219\n" \
"v_mul_u32_u24_e32 v217, 27, v216\n" \
"v_mfma_f32_16x16x32_bf16 a[72:75], v[132:135], v[68:71], a[72:75]\n" \
"v_mul_u32_u24_e32 v221, 9, v220\n" \
"v_add_u32_e32 v222, v217, v221\n" \
"v_mfma_f32_16x16x32_bf16 a[104:107], v[132:135], v[76:79], 0\n" \
"v_and_b32_e32 v223, 1, v219\n" \
"s_and_b32 s94, 7, %[b]\n" \
"v_mfma_f32_16x16x32_bf16 a[104:107], v[136:139], v[80:83], a[104:107]\n" \
"ds_read_b64_tr_b16 v[176:177], v35 offset:24576\n" \
"v_add_u32_e64 v224, v222, v223\n" \
"v_mfma_f32_16x16x32_bf16 a[12:15], v[144:147], v[56:59], 0\n" \
"ds_read_b64_tr_b16 v[178:179], v35 offset:25600\n" \
"v_add_u32_e64 v225, s94, v224\n" \
"v_mfma_f32_16x16x32_bf16 a[12:15], v[140:143], v[52:55], a[12:15]\n" \
"ds_read_b64_tr_b16 v[172:173], v35 offset:16384\n" \
"v_lshlrev_b32_e32 v186, 7, v225\n" \
"v_mfma_f32_16x16x32_bf16 a[44:47], v[140:143], v[60:63], 0\n" \
"ds_read_b64_tr_b16 v[174:175], v35 offset:17408\n" \
"ds_read_b128 v[84:87], v28 offset:0\n" \
"v_mfma_f32_16x16x32_bf16 a[44:47], v[144:147], v[64:67], a[44:47]\n" \
"ds_read_b128 v[88:91], v29 offset:0\n" \
"ds_read_b128 v[92:95], v28 offset:2048\n" \
"v_mfma_f32_16x16x32_bf16 a[76:79], v[144:147], v[72:75], 0\n" \
"ds_read_b128 v[96:99], v29 offset:2048\n" \
"s_mul_i32 s89, 6, s97\n" \
"v_mfma_f32_16x16x32_bf16 a[76:79], v[140:143], v[68:71], a[76:79]\n" \
"ds_read_b128 v[100:103], v28 offset:4096\n" \
"s_add_u32 s95, 0xffffffdc, s89\n" \
"v_mfma_f32_16x16x32_bf16 a[108:111], v[140:143], v[76:79], 0\n" \
"ds_read_b128 v[104:107], v29 offset:4096\n" \
"v_min_u32_e32 v227, 41, v212\n" \
"v_mfma_f32_16x16x32_bf16 a[108:111], v[144:147], v[80:83], a[108:111]\n" \
"ds_read_b128 v[108:111], v28 offset:6144\n" \
"s_mul_i32 s99, 0x1200, s94\n" \
"v_mfma_f32_16x16x32_bf16 a[16:19], v[56:59], v[152:155], 0\n" \
"ds_read_b128 v[112:115], v29 offset:6144\n" \
"s_waitcnt lgkmcnt(12)\n" \
"v_mfma_f32_16x16x32_bf16 a[16:19], v[52:55], v[148:151], a[16:19]\n" \
"s_mul_i32 s100, 36, s97\n" \
"v_add_u32_e32 v228, s95, v227\n" \
"v_mfma_f32_16x16x32_bf16 a[48:51], v[60:63], v[148:151], 0\n" \
"s_add_u32 s101, s99, s100\n" \
"s_mul_i32 s88, 0x288, s94\n" \
"v_mfma_f32_16x16x32_bf16 a[48:51], v[64:67], v[152:155], a[48:51]\n" \
"s_mov_b32 m0, %[mS]\n" \
"s_mov_b32 s86, s12\n" \
"v_mfma_f32_16x16x32_bf16 a[80:83], v[72:75], v[152:155], 0\n" \
"s_mov_b32 s87, s13\n" \
"v_min_u32_e32 v229, 0x287, v228\n" \
"v_mfma_f32_16x16x32_bf16 a[80:83], v[68:71], v[148:151], a[80:83]\n" \
"v_add_u32_e32 v226, s101, v212\n" \
"v_add_u32_e32 v230, s88, v229\n" \
"v_mfma_f32_16x16x32_bf16 a[112:115], v[76:79], v[148:151], 0\n" \
"v_cmp_gt_u32_e32 vcc, 36, v212\n" \
"s_lshl_b32 s94, %[w], 6\n" \
"v_mfma_f32_16x16x32_bf16 a[112:115], v[80:83], v[152:155], a[112:115]\n" \
"v_cndmask_b32_e32 v231, v230, v226, vcc\n" \
"v_lshlrev_b32_e32 v16, 7, v231\n" \
"v_mfma_f32_16x16x32_bf16 a[20:23], v[56:59], v[160:163], 0\n" \
"v_lshrrev_b32_e32 v212, 4, v20\n" \
"v_add_u32_e32 v213, s94, v212\n" \
"v_mfma_f32_16x16x32_bf16 a[20:23], v[52:55], v[156:159], a[20:23]\n" \
"v_mul_u32_u24_e32 v214, 0x2aab, v213\n" \
"v_lshrrev_b32_e32 v215, 16, v214\n" \
"s_mov_b32 exec_lo, 0x7ffffff\n" \
"s_mov_b32 exec_hi, 0x0\n" \
"global_load_lds_dword v186, s[86:87]\n" \
"s_mov_b64 exec, -1\n" \
"v_mfma_f32_16x16x32_bf16 a[52:55], v[60:63], v[156:159], 0\n" \
"s_add_u32 s97, 0xfffffe50, s94\n" \
"s_lshr_b32 s95, %[b], 5\n" \
"v_mfma_f32_16x16x32_bf16 a[52:55], v[64:67], v[160:163], a[52:55]\n" \
"v_add_u32_e64 v216, s97, v212\n" \
"v_cmp_gt_u32_e32 vcc, 0x1b0, v213\n" \
"v_mfma_f32_16x16x32_bf16 a[84:87], v[72:75], v[160:163], 0\n" \
"v_mul_u32_u24_e32 v220, 6, v215\n" \
"s_mul_i32 s96, 0x48, s95\n" \
"v_mfma_f32_16x16x32_bf16 a[84:87], v[68:71], v[156:159], a[84:87]\n" \
"v_cndmask_b32_e32 v217, v216, v215, vcc\n" \
"v_sub_u32_e32 v221, v213, v220\n" \
"v_mfma_f32_16x16x32_bf16 a[116:119], v[76:79], v[156:159], 0\n" \
"v_add_u32_e32 v218, s96, v217\n" \
"v_lshrrev_b32_e32 v222, 1, v221\n" \
"v_mfma_f32_16x16x32_bf16 a[116:119], v[80:83], v[160:163], a[116:119]\n" \
"s_add_u32 s98, 0x100, s94\n" \
"v_mul_u32_u24_e32 v219, 0xd80, v218\n" \
"v_mfma_f32_16x16x32_bf16 a[24:27], v[56:59], v[168:171], 0\n" \
"v_mul_u32_u24_e32 v223, 0x480, v222\n" \
"v_and_b32_e32 v225, 1, v221\n" \
"v_mfma_f32_16x16x32_bf16 a[24:27], v[52:55], v[164:167], a[24:27]\n" \
"v_add_u32_e32 v228, s98, v212\n" \
"s_and_b32 s88, 7, %[b]\n" \
"v_mfma_f32_16x16x32_bf16 a[56:59], v[60:63], v[164:167], 0\n" \
"s_lshr_b32 s97, %[b], 3\n" \
"v_add_u32_e32 v224, v219, v223\n" \
"v_mfma_f32_16x16x32_bf16 a[56:59], v[64:67], v[168:171], a[56:59]\n" \
"v_lshlrev_b32_e32 v226, 4, v225\n" \
"s_add_u32 s99, 0xffffff50, s94\n" \
"v_mfma_f32_16x16x32_bf16 a[88:91], v[72:75], v[168:171], 0\n" \
"s_lshl_b32 s89, s88, 2\n" \
"s_and_b32 s98, 3, s97\n" \
"v_mfma_f32_16x16x32_bf16 a[88:91], v[68:71], v[164:167], a[88:91]\n" \
"v_add_u32_e32 v227, v224, v226\n" \
"v_cmp_gt_u32_e32 vcc, 0x1b0, v213\n" \
"v_mfma_f32_16x16x32_bf16 a[120:123], v[76:79], v[164:167], 0\n" \
"s_add_u32 s94, s89, s98\n" \
"v_cndmask_b32_e32 v192, 0x40000000, v227, vcc\n" \
"v_mfma_f32_16x16x32_bf16 a[120:123], v[80:83], v[168:171], a[120:123]\n" \
"s_mul_i32 s56, 36, s94\n" \
"s_waitcnt lgkmcnt(0)\n" \
"v_mfma_f32_16x16x32_bf16 a[28:31], v[56:59], v[176:179], 0\n" \
"s_add_u32 s62, s22, 0x100\n" \
"s_and_b32 s62, s62, 0x3ff\n" \
"v_mfma_f32_16x16x32_bf16 a[28:31], v[52:55], v[172:175], a[28:31]\n" \
"s_lshl_b32 s63, s62, 12\n" \
"v_mul_u32_u24_e32 v229, 0x2aab, v228\n" \
"v_mfma_f32_16x16x32_bf16 a[60:63], v[60:63], v[172:175], 0\n" \
"v_lshrrev_b32_e32 v230, 16, v229\n" \
"v_add_u32_e64 v231, s99, v212\n" \
"v_mfma_f32_16x16x32_bf16 a[60:63], v[64:67], v[176:179], a[60:63]\n" \
"v_cmp_gt_u32_e32 vcc, 0x1b0, v228\n" \
"v_mul_u32_u24_e32 v235, 6, v230\n" \
"v_mfma_f32_16x16x32_bf16 a[92:95], v[72:75], v[176:179], 0\n" \
"v_cndmask_b32_e32 v232, v231, v230, vcc\n" \
"v_sub_u32_e32 v236, v228, v235\n" \
"v_mfma_f32_16x16x32_bf16 a[92:95], v[68:71], v[172:175], a[92:95]\n" \
"v_add_u32_e32 v233, s96, v232\n" \
"v_lshrrev_b32_e32 v237, 1, v236\n" \
"v_mfma_f32_16x16x32_bf16 a[124:127], v[76:79], v[172:175], 0\n" \
"v_mul_u32_u24_e32 v234, 0xd80, v233\n" \
"v_mul_u32_u24_e32 v238, 0x480, v237\n" \
"v_mfma_f32_16x16x32_bf16 a[124:127], v[80:83], v[176:179], a[124:127]\n" \
"v_and_b32_e32 v240, 1, v236\n" \
"v_add_u32_e32 v239, v234, v238\n" \
"s_waitcnt vmcnt(1)\n" \
"s_barrier\n" \
"v_mfma_f32_16x16x32_bf16 a[144:147], v[88:91], v[152:155], 0\n" \
"s_add_u32 m0, %[mA], 0x0\n" \
"s_add_u32 s85, s62, 0x0\n" \
"s_nop 0\n" \
"v_mfma_f32_16x16x32_bf16 a[144:147], v[84:87], v[148:151], a[144:147]\n" \
"buffer_load_dwordx4 v22, s[64:67], s85 offen lds\n" \
"v_mfma_f32_16x16x32_bf16 a[176:179], v[92:95], v[148:151], 0\n" \
"ds_read_b128 v[56:59], v29 offset:49152\n" \
"ds_read_b128 v[52:55], v28 offset:49152\n" \
"v_mfma_f32_16x16x32_bf16 a[176:179], v[96:99], v[152:155], a[176:179]\n" \
"s_add_u32 m0, %[mA], 0x400\n" \
"s_add_u32 s84, s62, 0x2000\n" \
"v_mfma_f32_16x16x32_bf16 a[208:211], v[104:107], v[152:155], 0\n" \
"buffer_load_dwordx4 v23, s[64:67], s84 offen lds\n" \
"v_mfma_f32_16x16x32_bf16 a[208:211], v[100:103], v[148:151], a[208:211]\n" \
"ds_read_b128 v[60:63], v28 offset:51200\n" \
"ds_read_b128 v[64:67], v29 offset:51200\n" \
"v_mfma_f32_16x16x32_bf16 a[240:243], v[108:111], v[148:151], 0\n" \
"ds_read_b64_tr_b16 v[148:149], v32 offset:49152\n" \
"ds_read_b64_tr_b16 v[150:151], v32 offset:50176\n" \
"v_mfma_f32_16x16x32_bf16 a[240:243], v[112:115], v[152:155], a[240:243]\n" \
"s_add_u32 m0, %[mB], 0x0\n" \
"s_add_u32 s85, s63, 0x0\n" \
"v_mfma_f32_16x16x32_bf16 a[148:151], v[88:91], v[160:163], 0\n" \
"buffer_load_dwordx4 v26, s[68:71], s85 offen lds\n" \
"v_mfma_f32_16x16x32_bf16 a[148:151], v[84:87], v[156:159], a[148:151]\n" \
"ds_read_b64_tr_b16 v[152:153], v32 offset:57344\n" \
"ds_read_b64_tr_b16 v[154:155], v32 offset:58368\n" \
"v_mfma_f32_16x16x32_bf16 a[180:183], v[92:95], v[156:159], 0\n" \
"ds_read_b128 v[72:75], v29 offset:53248\n" \
"ds_read_b128 v[68:71], v28 offset:53248\n" \
"v_mfma_f32_16x16x32_bf16 a[180:183], v[96:99], v[160:163], a[180:183]\n" \
"s_add_u32 m0, %[mB], 0x4000\n" \
"s_add_u32 s84, s63, 0x0\n" \
"s_nop 0\n" \
"v_mfma_f32_16x16x32_bf16 a[212:215], v[104:107], v[160:163], 0\n" \
"buffer_load_dwordx4 v26, s[72:75], s84 offen lds\n" \
"v_mfma_f32_16x16x32_bf16 a[212:215], v[100:103], v[156:159], a[212:215]\n" \
"ds_read_b128 v[76:79], v28 offset:55296\n" \
"ds_read_b128 v[80:83], v29 offset:55296\n" \
"v_mfma_f32_16x16x32_bf16 a[244:247], v[108:111], v[156:159], 0\n" \
"ds_read_b64_tr_b16 v[156:157], v33 offset:49152\n" \
"ds_read_b64_tr_b16 v[158:159], v33 offset:50176\n" \
"v_mfma_f32_16x16x32_bf16 a[244:247], v[112:115], v[160:163], a[244:247]\n" \
"s_add_u32 m0, %[mA], 0x800\n" \
"s_add_u32 s85, s62, 0x4000\n" \
"v_mfma_f32_16x16x32_bf16 a[152:155], v[88:91], v[168:171], 0\n" \
"buffer_load_dwordx4 v22, s[64:67], s85 offen lds\n" \
"v_mfma_f32_16x16x32_bf16 a[152:155], v[84:87], v[164:167], a[152:155]\n" \
"ds_read_b64_tr_b16 v[160:161], v33 offset:57344\n" \
"ds_read_b64_tr_b16 v[162:163], v33 offset:58368\n" \
"v_mfma_f32_16x16x32_bf16 a[184:187], v[92:95], v[164:167], 0\n" \
"v_lshlrev_b32_e32 v241, 4, v240\n" \
"v_add_u32_e32 v242, v239, v241\n" \
"v_mfma_f32_16x16x32_bf16 a[184:187], v[96:99], v[168:171], a[184:187]\n" \
"s_add_u32 m0, %[mA], 0xc00\n" \
"s_add_u32 s84, s62, 0x6000\n" \
"v_mfma_f32_16x16x32_bf16 a[216:219], v[104:107], v[168:171], 0\n" \
"buffer_load_dwordx4 v23, s[64:67], s84 offen lds\n" \
"v_mfma_f32_16x16x32_bf16 a[216:219], v[100:103], v[164:167], a[216:219]\n" \
"v_cmp_gt_u32_e32 vcc, 0x1b0, v228\n" \
"s_mul_i32 s100, 18, %[w]\n" \
"s_nop 0\n" \
"v_mfma_f32_16x16x32_bf16 a[248:251], v[108:111], v[164:167], 0\n" \
"ds_read_b64_tr_b16 v[164:165], v34 offset:49152\n" \
"ds_read_b64_tr_b16 v[166:167], v34 offset:50176\n" \
"v_mfma_f32_16x16x32_bf16 a[248:251], v[112:115], v[168:171], a[248:251]\n" \
"s_add_u32 m0, %[mB], 0x400\n" \
"s_add_u32 s85, s63, 0x8000\n" \
"v_mfma_f32_16x16x32_bf16 a[156:159], v[88:91], v[176:179], 0\n" \
"buffer_load_dwordx4 v26, s[68:71], s85 offen lds\n" \
"v_mfma_f32_16x16x32_bf16 a[156:159], v[84:87], v[172:175], a[156:159]\n" \
"ds_read_b64_tr_b16 v[168:169], v34 offset:57344\n" \
"ds_read_b64_tr_b16 v[170:171], v34 offset:58368\n" \
"v_mfma_f32_16x16x32_bf16 a[188:191], v[92:95], v[172:175], 0\n" \
"v_cndmask_b32_e32 v193, 0x40000000, v242, vcc\n" \
"v_lshlrev_b32_e32 v243, 1, v233\n" \
"v_mfma_f32_16x16x32_bf16 a[188:191], v[96:99], v[176:179], a[188:191]\n" \
"s_add_u32 m0, %[mB], 0x4400\n" \
"s_add_u32 s84, s63, 0x8000\n" \
"v_mfma_f32_16x16x32_bf16 a[220:223], v[104:107], v[176:179], 0\n" \
"buffer_load_dwordx4 v26, s[72:75], s84 offen lds\n" \
"v_mfma_f32_16x16x32_bf16 a[220:223], v[100:103], v[172:175], a[220:223]\n" \
"v_cmp_gt_u32_e32 vcc, 0x48, v231\n" \
"s_add_u32 s101, s96, s100\n" \
"s_nop 0\n" \
"v_mfma_f32_16x16x32_bf16 a[252:255], v[108:111], v[172:175], 0\n" \
"ds_read_b64_tr_b16 v[172:173], v35 offset:49152\n" \
"ds_read_b64_tr_b16 v[174:175], v35 offset:50176\n" \
"v_mfma_f32_16x16x32_bf16 a[252:255], v[112:115], v[176:179], a[252:255]\n" \
"s_add_u32 m0, %[mA], 0x1000\n" \
"s_add_u32 s85, s62, 0x8000\n" \
"v_mfma_f32_16x16x32_bf16 a[128:131], v[120:123], v[88:91], 0\n" \
"buffer_load_dwordx4 v22, s[64:67], s85 offen lds\n" \
"v_mfma_f32_16x16x32_bf16 a[128:131], v[116:119], v[84:87], a[128:131]\n" \
"ds_read_b64_tr_b16 v[176:177], v35 offset:57344\n" \
"ds_read_b64_tr_b16 v[178:179], v35 offset:58368\n" \
"v_mfma_f32_16x16x32_bf16 a[160:163], v[116:119], v[92:95], 0\n" \
"v_cndmask_b32_e32 v195, 0x40000000, v243, vcc\n" \
"v_add_u32_e32 v244, s101, v212\n" \
"v_mfma_f32_16x16x32_bf16 a[160:163], v[120:123], v[96:99], a[160:163]\n" \
"s_add_u32 m0, %[mA], 0x1400\n" \
"s_add_u32 s84, s62, 0xa000\n" \
"v_mfma_f32_16x16x32_bf16 a[192:195], v[120:123], v[104:107], 0\n" \
"buffer_load_dwordx4 v23, s[64:67], s84 offen lds\n" \
"v_mfma_f32_16x16x32_bf16 a[192:195], v[116:119], v[100:103], a[192:195]\n" \
"v_mul_u32_u24_e32 v245, 0xd80, v244\n" \
"v_cmp_gt_u32_e32 vcc, 18, v212\n" \
"s_nop 0\n" \
"v_mfma_f32_16x16x32_bf16 a[224:227], v[116:119], v[108:111], 0\n" \
"ds_read_b64_tr_b16 v[116:117], v32 offset:32768\n" \
"ds_read_b64_tr_b16 v[118:119], v32 offset:33792\n" \
"v_mfma_f32_16x16x32_bf16 a[224:227], v[120:123], v[112:115], a[224:227]\n" \
"s_add_u32 m0, %[mB], 0x800\n" \
"s_add_u32 s85, s63, 0x10000\n" \
"v_mfma_f32_16x16x32_bf16 a[132:135], v[128:131], v[88:91], 0\n" \
"buffer_load_dwordx4 v27, s[68:71], s85 offen lds\n" \
"v_mfma_f32_16x16x32_bf16 a[132:135], v[124:127], v[84:87], a[132:135]\n" \
"ds_read_b64_tr_b16 v[120:121], v32 offset:40960\n" \
"ds_read_b64_tr_b16 v[122:123], v32 offset:41984\n" \
"v_mfma_f32_16x16x32_bf16 a[164:167], v[124:127], v[92:95], 0\n" \
"v_cndmask_b32_e32 v194, 0x40000000, v245, vcc\n" \
"v_lshrrev_b32_e32 v248, 1, v218\n" \
"v_mfma_f32_16x16x32_bf16 a[164:167], v[128:131], v[96:99], a[164:167]\n" \
"s_add_u32 m0, %[mB], 0x4800\n" \
"s_add_u32 s84, s63, 0x10000\n" \
"v_mfma_f32_16x16x32_bf16 a[196:199], v[128:131], v[104:107], 0\n" \
"buffer_load_dwordx4 v27, s[72:75], s84 offen lds\n" \
"v_mfma_f32_16x16x32_bf16 a[196:199], v[124:127], v[100:103], a[196:199]\n" \
"v_lshrrev_b32_e32 v250, 2, v218\n" \
"v_and_b32_e32 v249, 1, v248\n" \
"v_mfma_f32_16x16x32_bf16 a[228:231], v[124:127], v[108:111], 0\n" \
"L_pf0:\n" \
"s_cbranch_execz L_pf4\n" \
"s_nop 0\n" \
"ds_read_b64_tr_b16 v[124:125], v33 offset:32768\n" \
"ds_read_b64_tr_b16 v[126:127], v33 offset:33792\n" \
"v_mfma_f32_16x16x32_bf16 a[228:231], v[128:131], v[112:115], a[228:231]\n" \
"ds_read_b64_tr_b16 v[128:129], v33 offset:40960\n" \
"ds_read_b64_tr_b16 v[130:131], v33 offset:41984\n" \
"v_mfma_f32_16x16x32_bf16 a[136:139], v[136:139], v[88:91], 0\n" \
"v_and_b32_e64 v251, 2, v250\n" \
"v_cmp_gt_u32_e32 vcc, 0x1b0, v213\n" \
"v_mfma_f32_16x16x32_bf16 a[136:139], v[132:135], v[84:87], a[136:139]\n" \
"v_or_b32_e32 v214, v249, v251\n" \
"v_cndmask_b32_e32 v247, 7, v221, vcc\n" \
"v_mfma_f32_16x16x32_bf16 a[168:171], v[132:135], v[92:95], 0\n" \
"v_lshlrev_b32_e32 v216, 1, v214\n" \
"v_xor_b32_e32 v217, v247, v216\n" \
"v_mfma_f32_16x16x32_bf16 a[168:171], v[136:139], v[96:99], a[168:171]\n" \
"s_mul_i32 s99, 0x2400, s95\n" \
"v_lshlrev_b32_e32 v246, 7, v218\n" \
"v_mfma_f32_16x16x32_bf16 a[200:203], v[136:139], v[104:107], 0\n" \
"v_lshlrev_b32_e32 v215, 4, v217\n" \
"s_sub_u32 s96, 0x22000, s99\n" \
"v_mfma_f32_16x16x32_bf16 a[200:203], v[132:135], v[100:103], a[200:203]\n" \
"v_add_u32_e32 v220, v246, v215\n" \
"v_add_u32_e32 v222, s96, v220\n" \
"v_mfma_f32_16x16x32_bf16 a[232:235], v[132:135], v[108:111], 0\n" \
"ds_read_b64_tr_b16 v[132:133], v34 offset:32768\n" \
"ds_read_b64_tr_b16 v[134:135], v34 offset:33792\n" \
"v_mfma_f32_16x16x32_bf16 a[232:235], v[136:139], v[112:115], a[232:235]\n" \
"ds_read_b64_tr_b16 v[136:137], v34 offset:40960\n" \
"ds_read_b64_tr_b16 v[138:139], v34 offset:41984\n" \
"v_mfma_f32_16x16x32_bf16 a[140:143], v[144:147], v[88:91], 0\n" \
"ds_read_b128 v[88:91], v29 offset:32768\n" \
"v_add_u32_e32 v219, 0x24400, v20\n" \
"v_mfma_f32_16x16x32_bf16 a[140:143], v[140:143], v[84:87], a[140:143]\n" \
"ds_read_b128 v[84:87], v28 offset:32768\n" \
"v_cmp_gt_u32_e32 vcc, 0x1f8, v213\n" \
"v_mfma_f32_16x16x32_bf16 a[172:175], v[140:143], v[92:95], 0\n" \
"ds_read_b128 v[92:95], v28 offset:34816\n" \
"v_lshrrev_b32_e32 v224, 1, v233\n" \
"v_mfma_f32_16x16x32_bf16 a[172:175], v[144:147], v[96:99], a[172:175]\n" \
"ds_read_b128 v[96:99], v29 offset:34816\n" \
"v_lshrrev_b32_e32 v227, 2, v233\n" \
"v_mfma_f32_16x16x32_bf16 a[204:207], v[144:147], v[104:107], 0\n" \
"ds_read_b128 v[104:107], v29 offset:36864\n" \
"s_lshl_b32 s100, %[w], 10\n" \
"v_mfma_f32_16x16x32_bf16 a[204:207], v[140:143], v[100:103], a[204:207]\n" \
"ds_read_b128 v[100:103], v28 offset:36864\n" \
"s_mul_i32 s89, 0x12000, s94\n" \
"v_mfma_f32_16x16x32_bf16 a[236:239], v[140:143], v[108:111], 0\n" \
"ds_read_b64_tr_b16 v[140:141], v35 offset:32768\n" \
"ds_read_b64_tr_b16 v[142:143], v35 offset:33792\n" \
"v_mfma_f32_16x16x32_bf16 a[236:239], v[144:147], v[112:115], a[236:239]\n" \
"s_add_u32 m0, %[mA], 0x1800\n" \
"s_waitcnt lgkmcnt(12)\n" \
"v_mfma_f32_16x16x32_bf16 a[16:19], v[56:59], v[152:155], a[16:19]\n" \
"s_add_u32 s85, s62, 0xc000\n" \
"ds_read_b64_tr_b16 v[144:145], v35 offset:40960\n" \
"v_mfma_f32_16x16x32_bf16 a[16:19], v[52:55], v[148:151], a[16:19]\n" \
"buffer_load_dwordx4 v22, s[64:67], s85 offen lds\n" \
"v_mfma_f32_16x16x32_bf16 a[48:51], v[60:63], v[148:151], a[48:51]\n" \
"s_add_u32 m0, %[mA], 0x1c00\n" \
"s_add_u32 s84, s62, 0xe000\n" \
"v_mfma_f32_16x16x32_bf16 a[48:51], v[64:67], v[152:155], a[48:51]\n" \
"ds_read_b64_tr_b16 v[146:147], v35 offset:41984\n" \
"ds_read_b128 v[108:111], v28 offset:38912\n" \
"v_mfma_f32_16x16x32_bf16 a[80:83], v[72:75], v[152:155], a[80:83]\n" \
"buffer_load_dwordx4 v23, s[64:67], s84 offen lds\n" \
"v_mfma_f32_16x16x32_bf16 a[80:83], v[68:71], v[148:151], a[80:83]\n" \
"s_add_u32 m0, %[mB], 0xc00\n" \
"s_add_u32 s85, s63, 0x18000\n" \
"v_mfma_f32_16x16x32_bf16 a[112:115], v[76:79], v[148:151], a[112:115]\n" \
"ds_read_b128 v[112:115], v29 offset:38912\n" \
"v_cndmask_b32_e32 v196, v219, v222, vcc\n" \
"v_mfma_f32_16x16x32_bf16 a[112:115], v[80:83], v[152:155], a[112:115]\n" \
"buffer_load_dwordx4 v27, s[68:71], s85 offen lds\n" \
"v_mfma_f32_16x16x32_bf16 a[20:23], v[56:59], v[160:163], a[20:23]\n" \
"s_add_u32 m0, %[mB], 0x4c00\n" \
"s_add_u32 s84, s63, 0x18000\n" \
"v_mfma_f32_16x16x32_bf16 a[20:23], v[52:55], v[156:159], a[20:23]\n" \
"v_and_b32_e32 v226, 1, v224\n" \
"v_and_b32_e64 v229, 2, v227\n" \
"v_mfma_f32_16x16x32_bf16 a[52:55], v[60:63], v[156:159], a[52:55]\n" \
"buffer_load_dwordx4 v27, s[72:75], s84 offen lds\n" \
"v_mfma_f32_16x16x32_bf16 a[52:55], v[64:67], v[160:163], a[52:55]\n" \
"v_lshrrev_b32_e32 v239, 1, v244\n" \
"v_lshrrev_b32_e32 v242, 2, v244\n" \
"v_mfma_f32_16x16x32_bf16 a[84:87], v[72:75], v[160:163], a[84:87]\n" \
"s_add_u32 s101, 0x22000, s100\n" \
"s_add_u32 s98, 0x6000000, s89\n" \
"v_mfma_f32_16x16x32_bf16 a[84:87], v[68:71], v[156:159], a[84:87]\n" \
"v_cmp_gt_u32_e32 vcc, 0x1b0, v228\n" \
"v_or_b32_e32 v232, v226, v229\n" \
"v_mfma_f32_16x16x32_bf16 a[116:119], v[76:79], v[156:159], a[116:119]\n" \
"v_and_b32_e32 v241, 1, v239\n" \
"v_and_b32_e64 v231, 2, v242\n" \
"v_mfma_f32_16x16x32_bf16 a[116:119], v[80:83], v[160:163], a[116:119]\n" \
"s_mov_b32 m0, %[mS]\n" \
"s_mov_b32 s86, s18\n" \
"v_mfma_f32_16x16x32_bf16 a[24:27], v[56:59], v[168:171], a[24:27]\n" \
"s_mov_b32 s87, s19\n" \
"v_add_u32_e32 v199, s101, v20\n" \
"v_mfma_f32_16x16x32_bf16 a[24:27], v[52:55], v[164:167], a[24:27]\n" \
"s_add_u32 s37, s98, s100\n" \
"v_cndmask_b32_e32 v225, 7, v236, vcc\n" \
"v_mfma_f32_16x16x32_bf16 a[56:59], v[60:63], v[164:167], a[56:59]\n" \
"v_lshlrev_b32_e32 v230, 1, v232\n" \
"v_or_b32_e32 v243, v241, v231\n" \
"v_mfma_f32_16x16x32_bf16 a[56:59], v[64:67], v[168:171], a[56:59]\n" \
"s_add_u32 s57, s37, s99\n" \
"s_lshl_b32 s101, s97, 2\n" \
"v_mfma_f32_16x16x32_bf16 a[88:91], v[72:75], v[168:171], a[88:91]\n" \
"v_xor_b32_e32 v235, v225, v230\n" \
"v_xor_b32_e32 v245, 3, v243\n" \
"v_mfma_f32_16x16x32_bf16 a[88:91], v[68:71], v[164:167], a[88:91]\n" \
"s_add_u32 s95, 0x2000, s57\n" \
"s_add_u32 s94, s101, %[w]\n" \
"s_mov_b32 exec_lo, 0x3ffff\n" \
"s_mov_b32 exec_hi, 0x0\n" \
"global_load_lds_dword v16, s[86:87]\n" \
"s_mov_b64 exec, -1\n" \
"v_mfma_f32_16x16x32_bf16 a[120:123], v[76:79], v[164:167], a[120:123]\n" \
"s_lshl_b32 s98, %[b], 2\n" \
"v_lshlrev_b32_e32 v223, 7, v233\n" \
"v_mfma_f32_16x16x32_bf16 a[120:123], v[80:83], v[168:171], a[120:123]\n" \
"v_lshlrev_b32_e32 v237, 4, v235\n" \
"v_lshlrev_b32_e32 v240, 7, v244\n" \
"v_mfma_f32_16x16x32_bf16 a[28:31], v[56:59], v[176:179], a[28:31]\n" \
"v_lshlrev_b32_e32 v221, 5, v245\n" \
"s_mul_i32 s89, 36, s94\n" \
"v_mfma_f32_16x16x32_bf16 a[28:31], v[52:55], v[172:175], a[28:31]\n" \
"s_cmp_eq_u32 %[w], 0\n" \
"s_cselect_b32 s58, s95, 0x40000000\n" \
"v_mfma_f32_16x16x32_bf16 a[60:63], v[60:63], v[172:175], a[60:63]\n" \
"s_add_u32 s100, s98, %[w]\n" \
"v_add_u32_e64 v234, v223, v237\n" \
"v_mfma_f32_16x16x32_bf16 a[60:63], v[64:67], v[176:179], a[60:63]\n" \
"v_add_u32_e32 v248, v240, v221\n" \
"v_add_u32_e32 v250, s89, v212\n" \
"v_mfma_f32_16x16x32_bf16 a[92:95], v[72:75], v[176:179], a[92:95]\n" \
"s_mul_i32 s99, 18, s100\n" \
"v_add_u32_e32 v238, s96, v234\n" \
"v_mfma_f32_16x16x32_bf16 a[92:95], v[68:71], v[172:175], a[92:95]\n" \
"v_add_u32_e32 v218, s96, v248\n" \
"v_lshrrev_b32_e32 v249, 3, v250\n" \
"v_mfma_f32_16x16x32_bf16 a[124:127], v[76:79], v[172:175], a[124:127]\n" \
"s_add_u32 s95, 0xffffffdc, s99\n" \
"v_min_u32_e32 v217, 53, v212\n" \
"v_mfma_f32_16x16x32_bf16 a[124:127], v[80:83], v[176:179], a[124:127]\n" \
"s_lshl_b32 s96, s88, 3\n" \
"v_lshlrev_b32_e32 v251, 6, v249\n" \
"v_mfma_f32_16x16x32_bf16 a[0:3], v[120:123], v[56:59], a[0:3]\n" \
"v_add_u32_e32 v246, s95, v217\n" \
"s_lshr_b32 s94, %[b], 5\n" \
"v_mfma_f32_16x16x32_bf16 a[0:3], v[116:119], v[52:55], a[0:3]\n" \
"s_and_b32 s97, 7, %[b]\n" \
"s_lshr_b32 s99, %[b], 3\n" \
"v_mfma_f32_16x16x32_bf16 a[32:35], v[116:119], v[60:63], a[32:35]\n" \
"v_add_u32_e32 v214, s96, v251\n" \
"s_mul_i32 s95, 0xa00, s94\n" \
"v_mfma_f32_16x16x32_bf16 a[32:35], v[120:123], v[64:67], a[32:35]\n" \
"s_lshl_b32 s98, s97, 2\n" \
"s_and_b32 s100, 3, s99\n" \
"v_mfma_f32_16x16x32_bf16 a[64:67], v[120:123], v[72:75], a[64:67]\n" \
"s_add_u32 s96, 0x5bc1000, s95\n" \
"s_add_u32 s101, s98, s100\n" \
"v_mfma_f32_16x16x32_bf16 a[64:67], v[116:119], v[68:71], a[64:67]\n" \
"s_lshl_b32 s88, s101, 2\n" \
"s_lshl_b32 s95, s94, 3\n" \
"v_mfma_f32_16x16x32_bf16 a[96:99], v[116:119], v[76:79], a[96:99]\n" \
"s_add_u32 s89, s96, s88\n" \
"s_add_u32 s99, s95, s97\n" \
"v_mfma_f32_16x16x32_bf16 a[96:99], v[120:123], v[80:83], a[96:99]\n" \
"v_writelane_b32 v11, s89, 0\n" \
"s_lshl_b32 s88, s99, 10\n" \
"v_mfma_f32_16x16x32_bf16 a[4:7], v[128:131], v[56:59], a[4:7]\n" \
"s_add_u32 s89, 0x5c00000, s88\n" \
"s_lshl_b32 s95, %[w], 8\n" \
"v_mfma_f32_16x16x32_bf16 a[4:7], v[124:127], v[52:55], a[4:7]\n" \
"v_cmp_gt_u32_e32 vcc, 0x1f8, v228\n" \
"s_add_u32 s99, s89, s95\n" \
"v_mfma_f32_16x16x32_bf16 a[36:39], v[124:127], v[60:63], a[36:39]\n" \
"v_cndmask_b32_e32 v197, v219, v238, vcc\n" \
"s_or_b32 s88, 0x200, s99\n" \
"v_mfma_f32_16x16x32_bf16 a[36:39], v[128:131], v[64:67], a[36:39]\n" \
"s_lshl_b32 s89, s100, 2\n" \
"v_cmp_gt_u32_e32 vcc, 18, v212\n" \
"v_mfma_f32_16x16x32_bf16 a[68:71], v[128:131], v[72:75], a[68:71]\n" \
"v_writelane_b32 v11, s88, 1\n" \
"s_add_u32 s95, s99, s89\n" \
"v_mfma_f32_16x16x32_bf16 a[68:71], v[124:127], v[68:71], a[68:71]\n" \
"v_cndmask_b32_e32 v198, v219, v218, vcc\n" \
"v_and_b32_e32 v247, 7, v250\n" \
"v_mfma_f32_16x16x32_bf16 a[100:103], v[124:127], v[76:79], a[100:103]\n" \
"v_writelane_b32 v11, s95, 2\n" \
"s_add_u32 s88, 0x80, s99\n" \
"v_mfma_f32_16x16x32_bf16 a[100:103], v[128:131], v[80:83], a[100:103]\n" \
"v_add_u32_e32 v216, v214, v247\n" \
"s_waitcnt lgkmcnt(0)\n" \
"v_mfma_f32_16x16x32_bf16 a[8:11], v[136:139], v[56:59], a[8:11]\n" \
"v_cmp_gt_u32_e32 vcc, 36, v212\n" \
"v_writelane_b32 v11, s88, 3\n" \
"v_mfma_f32_16x16x32_bf16 a[8:11], v[132:135], v[52:55], a[8:11]\n" \
"s_lshl_b32 s99, s101, 8\n" \
"v_cndmask_b32_e32 v215, v246, v216, vcc\n" \
"v_mfma_f32_16x16x32_bf16 a[40:43], v[132:135], v[60:63], a[40:43]\n" \
"s_add_u32 s89, 0x80, s95\n" \
"s_add_u32 s39, 0x5be8000, s99\n" \
"v_mfma_f32_16x16x32_bf16 a[40:43], v[136:139], v[64:67], a[40:43]\n" \
"s_lshl_b32 s88, s94, 2\n" \
"v_lshlrev_b32_e32 v188, 7, v215\n" \
"v_mfma_f32_16x16x32_bf16 a[72:75], v[136:139], v[72:75], a[72:75]\n" \
"s_add_u32 s95, s39, s88\n" \
"v_lshrrev_b32_e32 v212, 4, v20\n" \
"v_mfma_f32_16x16x32_bf16 a[72:75], v[132:135], v[68:71], a[72:75]\n" \
"v_writelane_b32 v11, s89, 4\n" \
"v_lshlrev_b32_e32 v213, 2, v212\n" \
"v_mfma_f32_16x16x32_bf16 a[104:107], v[132:135], v[76:79], a[104:107]\n" \
"s_cmp_eq_u32 %[w], 3\n" \
"s_cselect_b32 s38, s95, 0x40000000\n" \
"v_mfma_f32_16x16x32_bf16 a[104:107], v[136:139], v[80:83], a[104:107]\n" \
"v_and_b32_e32 v215, 7, v212\n" \
"s_add_u32 s101, s88, s100\n" \
"v_mfma_f32_16x16x32_bf16 a[12:15], v[144:147], v[56:59], a[12:15]\n" \
"s_add_u32 s62, s22, 0x180\n" \
"s_and_b32 s62, s62, 0x3ff\n" \
"v_mfma_f32_16x16x32_bf16 a[12:15], v[140:143], v[52:55], a[12:15]\n" \
"s_lshl_b32 s63, s62, 12\n" \
"v_add_u32_e32 v214, s96, v213\n" \
"v_mfma_f32_16x16x32_bf16 a[44:47], v[140:143], v[60:63], a[44:47]\n" \
"s_add_u32 s89, 0x5be8020, s99\n" \
"v_lshlrev_b32_e32 v216, 2, v215\n" \
"v_mfma_f32_16x16x32_bf16 a[44:47], v[144:147], v[64:67], a[44:47]\n" \
"s_mul_i32 s94, 0x90000, s101\n" \
"v_add_u32_e32 v217, s89, v216\n" \
"v_mfma_f32_16x16x32_bf16 a[76:79], v[144:147], v[72:75], a[76:79]\n" \
"s_add_u32 s95, 0x4240000, s94\n" \
"s_lshl_b32 s96, %[w], 14\n" \
"v_mfma_f32_16x16x32_bf16 a[76:79], v[140:143], v[68:71], a[76:79]\n" \
"s_add_u32 s99, s95, s96\n" \
"s_lshl_b32 s89, s97, 16\n" \
"v_mfma_f32_16x16x32_bf16 a[108:111], v[140:143], v[76:79], a[108:111]\n" \
"v_writelane_b32 v11, s39, 52\n" \
"s_add_u32 s88, s99, s89\n" \
"v_mfma_f32_16x16x32_bf16 a[108:111], v[144:147], v[80:83], a[108:111]\n" \
"s_lshl_b32 s101, s97, 11\n" \
"v_writelane_b32 v11, s88, 37\n" \
"s_waitcnt vmcnt(1)\n" \
"s_barrier\n" \
"v_mfma_f32_16x16x32_bf16 a[128:131], v[120:123], v[88:91], a[128:131]\n" \
"s_add_u32 m0, %[mA], 0x8000\n" \
"s_add_u32 s85, s62, 0x0\n" \
"v_mfma_f32_16x16x32_bf16 a[128:131], v[116:119], v[84:87], a[128:131]\n" \
"buffer_load_dwordx4 v22, s[64:67], s85 offen lds\n" \
"v_mfma_f32_16x16x32_bf16 a[160:163], v[116:119], v[92:95], a[160:163]\n" \
"ds_read_b128 v[56:59], v29 offset:16384\n" \
"ds_read_b128 v[52:55], v28 offset:16384\n" \
"v_mfma_f32_16x16x32_bf16 a[160:163], v[120:123], v[96:99], a[160:163]\n" \
"s_add_u32 m0, %[mA], 0x8400\n" \
"s_add_u32 s84, s62, 0x2000\n" \
"v_mfma_f32_16x16x32_bf16 a[192:195], v[120:123], v[104:107], a[192:195]\n" \
"buffer_load_dwordx4 v23, s[64:67], s84 offen lds\n" \
"v_mfma_f32_16x16x32_bf16 a[192:195], v[116:119], v[100:103], a[192:195]\n" \
"ds_read_b128 v[60:63], v28 offset:18432\n" \
"ds_read_b128 v[64:67], v29 offset:18432\n" \
"v_mfma_f32_16x16x32_bf16 a[224:227], v[116:119], v[108:111], a[224:227]\n" \
"ds_read_b64_tr_b16 v[116:117], v32 offset:0\n" \
"ds_read_b64_tr_b16 v[118:119], v32 offset:1024\n" \
"v_mfma_f32_16x16x32_bf16 a[224:227], v[120:123], v[112:115], a[224:227]\n" \
"s_add_u32 m0, %[mB], 0x8000\n" \
"s_add_u32 s85, s63, 0x0\n" \
"v_mfma_f32_16x16x32_bf16 a[132:135], v[128:131], v[88:91], a[132:135]\n" \
"buffer_load_dwordx4 v26, s[68:71], s85 offen lds\n" \
"v_mfma_f32_16x16x32_bf16 a[132:135], v[124:127], v[84:87], a[132:135]\n" \
"ds_read_b64_tr_b16 v[120:121], v32 offset:8192\n" \
"ds_read_b64_tr_b16 v[122:123], v32 offset:9216\n" \
"v_mfma_f32_16x16x32_bf16 a[164:167], v[124:127], v[92:95], a[164:167]\n" \
"ds_read_b128 v[72:75], v29 offset:20480\n" \
"ds_read_b128 v[68:71], v28 offset:20480\n" \
"v_mfma_f32_16x16x32_bf16 a[164:167], v[128:131], v[96:99], a[164:167]\n" \
"s_add_u32 m0, %[mB], 0xc000\n" \
"s_add_u32 s84, s63, 0x0\n" \
"v_mfma_f32_16x16x32_bf16 a[196:199], v[128:131], v[104:107], a[196:199]\n" \
"buffer_load_dwordx4 v26, s[72:75], s84 offen lds\n" \
"v_mfma_f32_16x16x32_bf16 a[196:199], v[124:127], v[100:103], a[196:199]\n" \
"ds_read_b128 v[76:79], v28 offset:22528\n" \
"ds_read_b128 v[80:83], v29 offset:22528\n" \
"v_mfma_f32_16x16x32_bf16 a[228:231], v[124:127], v[108:111], a[228:231]\n" \
"ds_read_b64_tr_b16 v[124:125], v33 offset:0\n" \
"ds_read_b64_tr_b16 v[126:127], v33 offset:1024\n" \
"v_mfma_f32_16x16x32_bf16 a[228:231], v[128:131], v[112:115], a[228:231]\n" \
"s_add_u32 m0, %[mA], 0x8800\n" \
"s_add_u32 s85, s62, 0x4000\n" \
"v_mfma_f32_16x16x32_bf16 a[136:139], v[136:139], v[88:91], a[136:139]\n" \
"buffer_load_dwordx4 v22, s[64:67], s85 offen lds\n" \
"v_mfma_f32_16x16x32_bf16 a[136:139], v[132:135], v[84:87], a[136:139]\n" \
"ds_read_b64_tr_b16 v[128:129], v33 offset:8192\n" \
"ds_read_b64_tr_b16 v[130:131], v33 offset:9216\n" \
"v_mfma_f32_16x16x32_bf16 a[168:171], v[132:135], v[92:95], a[168:171]\n" \
"s_add_u32 s100, 0x3800, s88\n" \
"s_add_u32 s94, s99, s101\n" \
"s_nop 0\n" \
"v_mfma_f32_16x16x32_bf16 a[168:171], v[136:139], v[96:99], a[168:171]\n" \
"s_add_u32 m0, %[mA], 0x8c00\n" \
"s_add_u32 s84, s62, 0x6000\n" \
"v_mfma_f32_16x16x32_bf16 a[200:203], v[136:139], v[104:107], a[200:203]\n" \
"buffer_load_dwordx4 v23, s[64:67], s84 offen lds\n" \
"v_mfma_f32_16x16x32_bf16 a[200:203], v[132:135], v[100:103], a[200:203]\n" \
"v_writelane_b32 v11, s100, 51\n" \
"s_add_u32 s95, 0x80000, s94\n" \
"v_mfma_f32_16x16x32_bf16 a[232:235], v[132:135], v[108:111], a[232:235]\n" \
"ds_read_b64_tr_b16 v[132:133], v34 offset:0\n" \
"ds_read_b64_tr_b16 v[134:135], v34 offset:1024\n" \
"v_mfma_f32_16x16x32_bf16 a[232:235], v[136:139], v[112:115], a[232:235]\n" \
"s_add_u32 m0, %[mB], 0x8400\n" \
"s_add_u32 s85, s63, 0x8000\n" \
"v_mfma_f32_16x16x32_bf16 a[140:143], v[144:147], v[88:91], a[140:143]\n" \
"buffer_load_dwordx4 v26, s[68:71], s85 offen lds\n" \
"v_mfma_f32_16x16x32_bf16 a[140:143], v[140:143], v[84:87], a[140:143]\n" \
"ds_read_b64_tr_b16 v[136:137], v34 offset:8192\n" \
"ds_read_b64_tr_b16 v[138:139], v34 offset:9216\n" \
"v_mfma_f32_16x16x32_bf16 a[172:175], v[140:143], v[92:95], a[172:175]\n" \
"s_lshl_b32 s96, %[w], 17\n" \
"v_writelane_b32 v11, s95, 49\n" \
"v_mfma_f32_16x16x32_bf16 a[172:175], v[144:147], v[96:99], a[172:175]\n" \
"s_add_u32 m0, %[mB], 0xc400\n" \
"s_add_u32 s84, s63, 0x8000\n" \
"v_mfma_f32_16x16x32_bf16 a[204:207], v[144:147], v[104:107], a[204:207]\n" \
"buffer_load_dwordx4 v26, s[72:75], s84 offen lds\n" \
"v_mfma_f32_16x16x32_bf16 a[204:207], v[140:143], v[100:103], a[204:207]\n" \
"s_add_u32 s89, s99, s96\n" \
"s_and_b32 s94, 1, s97\n" \
"s_nop 0\n" \
"v_mfma_f32_16x16x32_bf16 a[236:239], v[140:143], v[108:111], a[236:239]\n" \
"ds_read_b64_tr_b16 v[140:141], v35 offset:0\n" \
"ds_read_b64_tr_b16 v[142:143], v35 offset:1024\n" \
"v_mfma_f32_16x16x32_bf16 a[236:239], v[144:147], v[112:115], a[236:239]\n" \
"s_add_u32 m0, %[mA], 0x9000\n" \
"s_add_u32 s85, s62, 0x8000\n" \
"v_mfma_f32_16x16x32_bf16 a[144:147], v[88:91], v[152:155], a[144:147]\n" \
"buffer_load_dwordx4 v22, s[64:67], s85 offen lds\n" \
"v_mfma_f32_16x16x32_bf16 a[144:147], v[84:87], v[148:151], a[144:147]\n" \
"ds_read_b64_tr_b16 v[144:145], v35 offset:8192\n" \
"ds_read_b64_tr_b16 v[146:147], v35 offset:9216\n" \
"v_mfma_f32_16x16x32_bf16 a[176:179], v[92:95], v[148:151], a[176:179]\n" \
"s_add_u32 s88, s89, s98\n" \
"s_lshl_b32 s95, s94, 10\n" \
"v_mfma_f32_16x16x32_bf16 a[176:179], v[96:99], v[152:155], a[176:179]\n" \
"s_add_u32 m0, %[mA], 0x9400\n" \
"s_add_u32 s84, s62, 0xa000\n" \
"v_mfma_f32_16x16x32_bf16 a[208:211], v[104:107], v[152:155], a[208:211]\n" \
"buffer_load_dwordx4 v23, s[64:67], s84 offen lds\n" \
"v_mfma_f32_16x16x32_bf16 a[208:211], v[100:103], v[148:151], a[208:211]\n" \
"s_add_u32 s100, 0x3840, s88\n" \
"s_add_u32 s96, s99, s95\n" \
"s_nop 0\n" \
"v_mfma_f32_16x16x32_bf16 a[240:243], v[108:111], v[148:151], a[240:243]\n" \
"ds_read_b64_tr_b16 v[148:149], v32 offset:16384\n" \
"ds_read_b64_tr_b16 v[150:151], v32 offset:17408\n" \
"v_mfma_f32_16x16x32_bf16 a[240:243], v[112:115], v[152:155], a[240:243]\n" \
"s_add_u32 m0, %[mB], 0x8800\n" \
"s_add_u32 s85, s63, 0x10000\n" \
"v_mfma_f32_16x16x32_bf16 a[148:151], v[88:91], v[160:163], a[148:151]\n" \
"buffer_load_dwordx4 v27, s[68:71], s85 offen lds\n" \
"v_mfma_f32_16x16x32_bf16 a[148:151], v[84:87], v[156:159], a[148:151]\n" \
"ds_read_b64_tr_b16 v[152:153], v32 offset:24576\n" \
"ds_read_b64_tr_b16 v[154:155], v32 offset:25600\n" \
"v_mfma_f32_16x16x32_bf16 a[180:183], v[92:95], v[156:159], a[180:183]\n" \
"v_writelane_b32 v11, s100, 5\n" \
"s_lshr_b32 s101, s97, 1\n" \
"v_mfma_f32_16x16x32_bf16 a[180:183], v[96:99], v[160:163], a[180:183]\n" \
"s_add_u32 m0, %[mB], 0xc800\n" \
"s_add_u32 s84, s63, 0x10000\n" \
"v_mfma_f32_16x16x32_bf16 a[212:215], v[104:107], v[160:163], a[212:215]\n" \
"buffer_load_dwordx4 v27, s[72:75], s84 offen lds\n" \
"v_mfma_f32_16x16x32_bf16 a[212:215], v[100:103], v[156:159], a[212:215]\n" \
"s_add_u32 s89, 0x80000, s96\n" \
"s_add_u32 s88, 1, s97\n" \
"v_mfma_f32_16x16x32_bf16 a[244:247], v[108:111], v[156:159], a[244:247]\n" \
"ds_read_b64_tr_b16 v[156:157], v33 offset:16384\n" \
"ds_read_b64_tr_b16 v[158:159], v33 offset:17408\n" \
"v_mfma_f32_16x16x32_bf16 a[244:247], v[112:115], v[160:163], a[244:247]\n" \
"ds_read_b64_tr_b16 v[160:161], v33 offset:24576\n" \
"ds_read_b64_tr_b16 v[162:163], v33 offset:25600\n" \
"v_mfma_f32_16x16x32_bf16 a[152:155], v[88:91], v[168:171], a[152:155]\n" \
"s_cmp_eq_u32 %[w], s101\n" \
"s_cselect_b32 s98, s89, 0x40000000\n" \
"v_mfma_f32_16x16x32_bf16 a[152:155], v[84:87], v[164:167], a[152:155]\n" \
"s_and_b32 s100, 7, s88\n" \
"s_lshl_b32 s94, s100, 16\n" \
"v_mfma_f32_16x16x32_bf16 a[184:187], v[92:95], v[164:167], a[184:187]\n" \
"s_add_u32 s96, 2, s97\n" \
"v_writelane_b32 v11, s98, 38\n" \
"v_mfma_f32_16x16x32_bf16 a[184:187], v[96:99], v[168:171], a[184:187]\n" \
"s_add_u32 s95, s99, s94\n" \
"s_and_b32 s101, 7, s96\n" \
"v_mfma_f32_16x16x32_bf16 a[216:219], v[104:107], v[168:171], a[216:219]\n" \
"s_add_u32 s100, 3, s97\n" \
"v_writelane_b32 v11, s95, 42\n" \
"v_mfma_f32_16x16x32_bf16 a[216:219], v[100:103], v[164:167], a[216:219]\n" \
"s_lshl_b32 s89, s101, 16\n" \
"s_and_b32 s94, 7, s100\n" \
"v_mfma_f32_16x16x32_bf16 a[248:251], v[108:111], v[164:167], a[248:251]\n" \
"ds_read_b64_tr_b16 v[164:165], v34 offset:16384\n" \
"ds_read_b64_tr_b16 v[166:167], v34 offset:17408\n" \
"v_mfma_f32_16x16x32_bf16 a[248:251], v[112:115], v[168:171], a[248:251]\n" \
"ds_read_b64_tr_b16 v[168:169], v34 offset:24576\n" \
"ds_read_b64_tr_b16 v[170:171], v34 offset:25600\n" \
"v_mfma_f32_16x16x32_bf16 a[156:159], v[88:91], v[176:179], a[156:159]\n" \
"ds_read_b128 v[88:91], v29 offset:0\n" \
"s_add_u32 s98, s99, s89\n" \
"v_mfma_f32_16x16x32_bf16 a[156:159], v[84:87], v[172:175], a[156:159]\n" \
"ds_read_b128 v[84:87], v28 offset:0\n" \
"s_lshl_b32 s95, s94, 16\n" \
"v_mfma_f32_16x16x32_bf16 a[188:191], v[92:95], v[172:175], a[188:191]\n" \
"ds_read_b128 v[92:95], v28 offset:2048\n" \
"s_add_u32 s88, 0x800, s98\n" \
"v_mfma_f32_16x16x32_bf16 a[188:191], v[96:99], v[176:179], a[188:191]\n" \
"ds_read_b128 v[96:99], v29 offset:2048\n" \
"s_add_u32 s96, s99, s95\n" \
"v_mfma_f32_16x16x32_bf16 a[220:223], v[104:107], v[176:179], a[220:223]\n" \
"ds_read_b128 v[104:107], v29 offset:4096\n" \
"s_add_u32 s89, 4, s97\n" \
"v_mfma_f32_16x16x32_bf16 a[220:223], v[100:103], v[172:175], a[220:223]\n" \
"ds_read_b128 v[100:103], v28 offset:4096\n" \
"v_writelane_b32 v11, s88, 43\n" \
"v_mfma_f32_16x16x32_bf16 a[252:255], v[108:111], v[172:175], a[252:255]\n" \
"ds_read_b64_tr_b16 v[172:173], v35 offset:16384\n" \
"ds_read_b64_tr_b16 v[174:175], v35 offset:17408\n" \
"v_mfma_f32_16x16x32_bf16 a[252:255], v[112:115], v[176:179], a[252:255]\n" \
"s_add_u32 m0, %[mA], 0x9800\n" \
"s_waitcnt lgkmcnt(12)\n" \
"s_nop 0\n" \
"v_mfma_f32_16x16x32_bf16 a[0:3], v[120:123], v[56:59], a[0:3]\n" \
"s_add_u32 s85, s62, 0xc000\n" \
"ds_read_b64_tr_b16 v[176:177], v35 offset:24576\n" \
"v_mfma_f32_16x16x32_bf16 a[0:3], v[116:119], v[52:55], a[0:3]\n" \
"buffer_load_dwordx4 v22, s[64:67], s85 offen lds\n" \
"v_mfma_f32_16x16x32_bf16 a[32:35], v[116:119], v[60:63], a[32:35]\n" \
"s_add_u32 m0, %[mA], 0x9c00\n" \
"s_add_u32 s84, s62, 0xe000\n" \
"v_mfma_f32_16x16x32_bf16 a[32:35], v[120:123], v[64:67], a[32:35]\n" \
"ds_read_b64_tr_b16 v[178:179], v35 offset:25600\n" \
"ds_read_b128 v[108:111], v28 offset:6144\n" \
"v_mfma_f32_16x16x32_bf16 a[64:67], v[120:123], v[72:75], a[64:67]\n" \
"buffer_load_dwordx4 v23, s[64:67], s84 offen lds\n" \
"v_mfma_f32_16x16x32_bf16 a[64:67], v[116:119], v[68:71], a[64:67]\n" \
"s_add_u32 m0, %[mB], 0x8c00\n" \
"s_add_u32 s85, s63, 0x18000\n" \
"v_mfma_f32_16x16x32_bf16 a[96:99], v[116:119], v[76:79], a[96:99]\n" \
"ds_read_b128 v[112:115], v29 offset:6144\n" \
"s_add_u32 s101, 0x1000, s96\n" \
"v_mfma_f32_16x16x32_bf16 a[96:99], v[120:123], v[80:83], a[96:99]\n" \
"buffer_load_dwordx4 v27, s[68:71], s85 offen lds\n" \
"v_mfma_f32_16x16x32_bf16 a[4:7], v[128:131], v[56:59], a[4:7]\n" \
"s_add_u32 m0, %[mB], 0xcc00\n" \
"s_add_u32 s84, s63, 0x18000\n" \
"v_mfma_f32_16x16x32_bf16 a[4:7], v[124:127], v[52:55], a[4:7]\n" \
"s_and_b32 s98, 7, s89\n" \
"s_add_u32 s95, 5, s97\n" \
"v_mfma_f32_16x16x32_bf16 a[36:39], v[124:127], v[60:63], a[36:39]\n" \
"buffer_load_dwordx4 v27, s[72:75], s84 offen lds\n" \
"v_mfma_f32_16x16x32_bf16 a[36:39], v[128:131], v[64:67], a[36:39]\n" \
"v_writelane_b32 v11, s101, 44\n" \
"s_lshl_b32 s88, s98, 16\n" \
"v_mfma_f32_16x16x32_bf16 a[68:71], v[128:131], v[72:75], a[68:71]\n" \
"s_and_b32 s96, 7, s95\n" \
"s_add_u32 s100, s99, s88\n" \
"v_mfma_f32_16x16x32_bf16 a[68:71], v[124:127], v[68:71], a[68:71]\n" \
"buffer_load_dwordx4 v[200:203], v192, s[76:79], s56 offen offset:0\n" \
"v_mfma_f32_16x16x32_bf16 a[100:103], v[124:127], v[76:79], a[100:103]\n" \
"s_lshl_b32 s101, s96, 16\n" \
"s_add_u32 s94, 0x1800, s100\n" \
"v_mfma_f32_16x16x32_bf16 a[100:103], v[128:131], v[80:83], a[100:103]\n" \
"s_add_u32 s89, s99, s101\n" \
"s_add_u32 s88, 6, s97\n" \
"v_mfma_f32_16x16x32_bf16 a[8:11], v[136:139], v[56:59], a[8:11]\n" \
"v_writelane_b32 v11, s94, 45\n" \
"s_add_u32 s98, 0x2000, s89\n" \
"v_mfma_f32_16x16x32_bf16 a[8:11], v[132:135], v[52:55], a[8:11]\n" \
"s_and_b32 s100, 7, s88\n" \
"s_add_u32 s101, 7, s97\n" \
"v_mfma_f32_16x16x32_bf16 a[40:43], v[132:135], v[60:63], a[40:43]\n" \
"buffer_load_dwordx4 v[204:207], v193, s[76:79], s56 offen offset:0\n" \
"v_mfma_f32_16x16x32_bf16 a[40:43], v[136:139], v[64:67], a[40:43]\n" \
"v_writelane_b32 v11, s98, 46\n" \
"s_lshl_b32 s94, s100, 16\n" \
"v_mfma_f32_16x16x32_bf16 a[72:75], v[136:139], v[72:75], a[72:75]\n" \
"s_and_b32 s89, 7, s101\n" \
"s_add_u32 s95, s99, s94\n" \
"v_mfma_f32_16x16x32_bf16 a[72:75], v[132:135], v[68:71], a[72:75]\n" \
"s_lshl_b32 s98, s89, 16\n" \
"s_add_u32 s96, 0x2800, s95\n" \
"v_mfma_f32_16x16x32_bf16 a[104:107], v[132:135], v[76:79], a[104:107]\n" \
"s_add_u32 s88, s99, s98\n" \
"v_writelane_b32 v11, s96, 47\n" \
"v_mfma_f32_16x16x32_bf16 a[104:107], v[136:139], v[80:83], a[104:107]\n" \
"s_add_u32 s100, 0x3000, s88\n" \
"v_writelane_b32 v11, s100, 48\n" \
"v_mfma_f32_16x16x32_bf16 a[12:15], v[144:147], v[56:59], a[12:15]\n" \
"buffer_load_dword v208, v194, s[76:79], s56 offen offset:32\n" \
"v_mfma_f32_16x16x32_bf16 a[12:15], v[140:143], v[52:55], a[12:15]\n" \
"v_readfirstlane_b32 %[tag], v180\n" \
"s_lshr_b32 s94, %[b], 5\n" \
"v_mfma_f32_16x16x32_bf16 a[44:47], v[140:143], v[60:63], a[44:47]\n" \
"s_add_u32 %[tag], %[tag], 0x9E3779B1\n" \
"v_mov_b32_e32 v45, %[tag]\n" \
"v_mfma_f32_16x16x32_bf16 a[44:47], v[144:147], v[64:67], a[44:47]\n" \
"s_sub_u32 s95, 15, s94\n" \
"s_and_b32 s100, 1, %[w]\n" \
"v_mfma_f32_16x16x32_bf16 a[76:79], v[144:147], v[72:75], a[76:79]\n" \
"s_cmp_lt_u32 %[w], 2\n" \
"s_cselect_b32 s96, s95, s94\n" \
"v_mfma_f32_16x16x32_bf16 a[76:79], v[140:143], v[68:71], a[76:79]\n" \
"s_lshl_b32 s97, s96, 7\n" \
"s_add_u32 s98, 0xfffffff0, s97\n" \
"v_mfma_f32_16x16x32_bf16 a[108:111], v[140:143], v[76:79], a[108:111]\n" \
"buffer_load_dword v209, v194, s[76:79], s56 offen offset:1184\n" \
"v_mfma_f32_16x16x32_bf16 a[108:111], v[144:147], v[80:83], a[108:111]\n" \
"s_cmp_eq_u32 s97, 0\n" \
"s_cselect_b32 s99, 0, s98\n" \
"v_mfma_f32_16x16x32_bf16 a[16:19], v[56:59], v[152:155], a[16:19]\n" \
"v_cmp_gt_u32_e32 vcc, 32, v212\n" \
"s_lshl_b32 s101, s100, 3\n" \
"v_mfma_f32_16x16x32_bf16 a[16:19], v[52:55], v[148:151], a[16:19]\n" \
"s_add_u32 s88, s99, s101\n" \
"v_lshrrev_b32_e32 v212, 4, v20\n" \
"v_mfma_f32_16x16x32_bf16 a[48:51], v[60:63], v[148:151], a[48:51]\n" \
"v_cndmask_b32_e32 v17, v217, v214, vcc\n" \
"v_lshrrev_b32_e32 v213, 3, v212\n" \
"v_mfma_f32_16x16x32_bf16 a[48:51], v[64:67], v[152:155], a[48:51]\n" \
"s_add_u32 s99, s97, s100\n" \
"v_add_u32_e32 v214, s88, v213\n" \
"v_mfma_f32_16x16x32_bf16 a[80:83], v[72:75], v[152:155], a[80:83]\n" \
"buffer_load_dword v211, v194, s[76:79], s56 offen offset:2336\n" \
"v_mfma_f32_16x16x32_bf16 a[80:83], v[68:71], v[148:151], a[80:83]\n" \
"s_mov_b32 s76, s14\n" \
"s_and_b32 s77, s15, 0xffff\n" \
"v_mfma_f32_16x16x32_bf16 a[112:115], v[76:79], v[148:151], a[112:115]\n" \
"s_mov_b32 s78, 0x480\n" \
"s_add_u32 s56, s57, 0x1000\n" \
"v_mfma_f32_16x16x32_bf16 a[112:115], v[80:83], v[152:155], a[112:115]\n" \
"s_lshl_b32 s89, s100, 2\n" \
"v_lshrrev_b32_e32 v217, 4, v212\n" \
"v_mfma_f32_16x16x32_bf16 a[20:23], v[56:59], v[160:163], a[20:23]\n" \
"s_lshl_b32 s96, s100, 6\n" \
"s_lshr_b32 s101, s97, 4\n" \
"v_mfma_f32_16x16x32_bf16 a[20:23], v[52:55], v[156:159], a[20:23]\n" \
"s_cmp_eq_u32 s99, 0\n" \
"s_cselect_b32 s49, 0xff800000, 0\n" \
"v_mfma_f32_16x16x32_bf16 a[52:55], v[60:63], v[156:159], a[52:55]\n" \
"buffer_load_ushort v210, v195, s[76:79], 0 offen\n" \
"v_mfma_f32_16x16x32_bf16 a[52:55], v[64:67], v[160:163], a[52:55]\n" \
"v_add_u32_e32 v218, s89, v217\n" \
"s_add_u32 s98, s97, s96\n" \
"v_mfma_f32_16x16x32_bf16 a[84:87], v[72:75], v[160:163], a[84:87]\n" \
"s_add_u32 s88, s101, s89\n" \
"v_add_u32_e32 v221, s98, v213\n" \
"v_mfma_f32_16x16x32_bf16 a[84:87], v[68:71], v[156:159], a[84:87]\n" \
"s_mul_i32 s96, 0x24800, s88\n" \
"s_and_b32 s99, 7, %[b]\n" \
"v_mfma_f32_16x16x32_bf16 a[116:119], v[76:79], v[156:159], a[116:119]\n" \
"s_lshr_b32 s89, %[b], 3\n" \
"s_add_u32 s98, 0x3000000, s96\n" \
"v_mfma_f32_16x16x32_bf16 a[116:119], v[80:83], v[160:163], a[116:119]\n" \
"s_lshl_b32 s97, s99, 8\n" \
"s_waitcnt lgkmcnt(0)\n" \
"v_mfma_f32_16x16x32_bf16 a[24:27], v[56:59], v[168:171], a[24:27]\n" \
"s_and_b32 s88, 3, s89\n" \
"s_add_u32 s101, s98, s97\n" \
"v_mfma_f32_16x16x32_bf16 a[24:27], v[52:55], v[164:167], a[24:27]\n" \
"s_lshl_b32 s96, s88, 2\n" \
"s_add_u32 s97, s101, s96\n" \
"v_mfma_f32_16x16x32_bf16 a[56:59], v[60:63], v[164:167], a[56:59]\n" \
"s_lshl_b32 s101, s99, 2\n" \
"v_and_b32_e32 v216, 7, v212\n" \
"v_mfma_f32_16x16x32_bf16 a[56:59], v[64:67], v[168:171], a[56:59]\n" \
"s_add_u32 s51, 0x4000, s97\n" \
"s_add_u32 s96, s101, s88\n" \
"v_mfma_f32_16x16x32_bf16 a[88:91], v[72:75], v[168:171], a[88:91]\n" \
"v_xor_b32_e32 v219, v216, v218\n" \
"s_lshl_b32 s97, s96, 9\n" \
"v_mfma_f32_16x16x32_bf16 a[88:91], v[68:71], v[164:167], a[88:91]\n" \
"s_lshl_b32 s101, s89, 2\n" \
"v_mul_u32_u24_e32 v215, 0x480, v214\n" \
"v_mfma_f32_16x16x32_bf16 a[120:123], v[76:79], v[164:167], a[120:123]\n" \
"v_lshlrev_b32_e32 v220, 4, v219\n" \
"v_lshrrev_b32_e32 v229, 2, v212\n" \
"v_mfma_f32_16x16x32_bf16 a[120:123], v[80:83], v[168:171], a[120:123]\n" \
"v_and_b32_e32 v234, 3, v212\n" \
"s_add_u32 s50, s98, s97\n" \
"v_mfma_f32_16x16x32_bf16 a[28:31], v[56:59], v[176:179], a[28:31]\n" \
"s_add_u32 s62, s22, 0x200\n" \
"s_and_b32 s62, s62, 0x3ff\n" \
"v_mfma_f32_16x16x32_bf16 a[28:31], v[52:55], v[172:175], a[28:31]\n" \
"s_lshl_b32 s63, s62, 12\n" \
"s_lshl_b32 s99, %[w], 10\n" \
"v_mfma_f32_16x16x32_bf16 a[60:63], v[60:63], v[172:175], a[60:63]\n" \
"s_add_u32 s88, s101, %[w]\n" \
"v_add_u32_e32 v21, v215, v220\n" \
"v_mfma_f32_16x16x32_bf16 a[60:63], v[64:67], v[176:179], a[60:63]\n" \
"v_add_u32_e64 v225, 4, v217\n" \
"s_add_u32 s40, 0x14000, s99\n" \
"v_mfma_f32_16x16x32_bf16 a[92:95], v[72:75], v[176:179], a[92:95]\n" \
"s_lshl_b32 s41, s88, 7\n" \
"s_lshl_b32 s98, s96, 20\n" \
"v_mfma_f32_16x16x32_bf16 a[92:95], v[68:71], v[172:175], a[92:95]\n" \
"v_lshrrev_b32_e32 v212, 4, v20\n" \
"v_xor_b32_e32 v223, v216, v217\n" \
"v_mfma_f32_16x16x32_bf16 a[124:127], v[76:79], v[172:175], a[124:127]\n" \
"v_xor_b32_e64 v226, v216, v225\n" \
"s_add_u32 s97, 0x1000000, s98\n" \
"L_pf1:\n" \
"s_cbranch_execz L_pf5\n" \
"s_nop 0\n" \
"v_mfma_f32_16x16x32_bf16 a[124:127], v[80:83], v[176:179], a[124:127]\n" \
"s_lshl_b32 s99, %[w], 12\n" \
"s_lshl_b32 s89, %[w], 11\n" \
"s_waitcnt vmcnt(6)\n" \
"s_barrier\n" \
"v_mfma_f32_16x16x32_bf16 a[144:147], v[88:91], v[152:155], a[144:147]\n" \
"s_add_u32 m0, %[mA], 0x0\n" \
"s_add_u32 s85, s62, 0x0\n" \
"v_mfma_f32_16x16x32_bf16 a[144:147], v[84:87], v[148:151], a[144:147]\n" \
"buffer_load_dwordx4 v22, s[64:67], s85 offen lds\n" \
"v_mfma_f32_16x16x32_bf16 a[176:179], v[92:95], v[148:151], a[176:179]\n" \
"ds_read_b128 v[56:59], v29 offset:49152\n" \
"ds_read_b128 v[52:55], v28 offset:49152\n" \
"v_mfma_f32_16x16x32_bf16 a[176:179], v[96:99], v[152:155], a[176:179]\n" \
"s_add_u32 m0, %[mA], 0x400\n" \
"s_add_u32 s84, s62, 0x2000\n" \
"v_mfma_f32_16x16x32_bf16 a[208:211], v[104:107], v[152:155], a[208:211]\n" \
"buffer_load_dwordx4 v23, s[64:67], s84 offen lds\n" \
"v_mfma_f32_16x16x32_bf16 a[208:211], v[100:103], v[148:151], a[208:211]\n" \
"ds_read_b128 v[60:63], v28 offset:51200\n" \
"ds_read_b128 v[64:67], v29 offset:51200\n" \
"v_mfma_f32_16x16x32_bf16 a[240:243], v[108:111], v[148:151], a[240:243]\n" \
"ds_read_b64_tr_b16 v[148:149], v32 offset:49152\n" \
"ds_read_b64_tr_b16 v[150:151], v32 offset:50176\n" \
"v_mfma_f32_16x16x32_bf16 a[240:243], v[112:115], v[152:155], a[240:243]\n" \
"s_add_u32 m0, %[mB], 0x0\n" \
"s_add_u32 s85, s63, 0x0\n" \
"v_mfma_f32_16x16x32_bf16 a[148:151], v[88:91], v[160:163], a[148:151]\n" \
"buffer_load_dwordx4 v26, s[68:71], s85 offen lds\n" \
"v_mfma_f32_16x16x32_bf16 a[148:151], v[84:87], v[156:159], a[148:151]\n" \
"ds_read_b64_tr_b16 v[152:153], v32 offset:57344\n" \
"ds_read_b64_tr_b16 v[154:155], v32 offset:58368\n" \
"v_mfma_f32_16x16x32_bf16 a[180:183], v[92:95], v[156:159], a[180:183]\n" \
"ds_read_b128 v[72:75], v29 offset:53248\n" \
"ds_read_b128 v[68:71], v28 offset:53248\n" \
"v_mfma_f32_16x16x32_bf16 a[180:183], v[96:99], v[160:163], a[180:183]\n" \
"s_add_u32 m0, %[mB], 0x4000\n" \
"s_add_u32 s84, s63, 0x0\n" \
"s_nop 0\n" \
"v_mfma_f32_16x16x32_bf16 a[212:215], v[104:107], v[160:163], a[212:215]\n" \
"buffer_load_dwordx4 v26, s[72:75], s84 offen lds\n" \
"v_mfma_f32_16x16x32_bf16 a[212:215], v[100:103], v[156:159], a[212:215]\n" \
"ds_read_b128 v[76:79], v28 offset:55296\n" \
"ds_read_b128 v[80:83], v29 offset:55296\n" \
"v_mfma_f32_16x16x32_bf16 a[244:247], v[108:111], v[156:159], a[244:247]\n" \
"ds_read_b64_tr_b16 v[156:157], v33 offset:49152\n" \
"ds_read_b64_tr_b16 v[158:159], v33 offset:50176\n" \
"v_mfma_f32_16x16x32_bf16 a[244:247], v[112:115], v[160:163], a[244:247]\n" \
"s_add_u32 m0, %[mA], 0x800\n" \
"s_add_u32 s85, s62, 0x4000\n" \
"v_mfma_f32_16x16x32_bf16 a[152:155], v[88:91], v[168:171], a[152:155]\n" \
"buffer_load_dwordx4 v22, s[64:67], s85 offen lds\n" \
"v_mfma_f32_16x16x32_bf16 a[152:155], v[84:87], v[164:167], a[152:155]\n" \
"ds_read_b64_tr_b16 v[160:161], v33 offset:57344\n" \
"ds_read_b64_tr_b16 v[162:163], v33 offset:58368\n" \
"v_mfma_f32_16x16x32_bf16 a[184:187], v[92:95], v[164:167], a[184:187]\n" \
"s_lshr_b32 s88, %[w], 1\n" \
"v_and_b32_e32 v215, 15, v212\n" \
"v_mfma_f32_16x16x32_bf16 a[184:187], v[96:99], v[168:171], a[184:187]\n" \
"s_add_u32 m0, %[mA], 0xc00\n" \
"s_add_u32 s84, s62, 0x6000\n" \
"v_mfma_f32_16x16x32_bf16 a[216:219], v[104:107], v[168:171], a[216:219]\n" \
"buffer_load_dwordx4 v23, s[64:67], s84 offen lds\n" \
"v_mfma_f32_16x16x32_bf16 a[216:219], v[100:103], v[164:167], a[216:219]\n" \
"s_add_u32 s52, s97, s99\n" \
"s_add_u32 s101, s97, s89\n" \
"v_mfma_f32_16x16x32_bf16 a[248:251], v[108:111], v[164:167], a[248:251]\n" \
"ds_read_b64_tr_b16 v[164:165], v34 offset:49152\n" \
"ds_read_b64_tr_b16 v[166:167], v34 offset:50176\n" \
"v_mfma_f32_16x16x32_bf16 a[248:251], v[112:115], v[168:171], a[248:251]\n" \
"s_add_u32 m0, %[mB], 0x400\n" \
"s_add_u32 s85, s63, 0x8000\n" \
"v_mfma_f32_16x16x32_bf16 a[156:159], v[88:91], v[176:179], a[156:159]\n" \
"buffer_load_dwordx4 v26, s[68:71], s85 offen lds\n" \
"v_mfma_f32_16x16x32_bf16 a[156:159], v[84:87], v[172:175], a[156:159]\n" \
"ds_read_b64_tr_b16 v[168:169], v34 offset:57344\n" \
"ds_read_b64_tr_b16 v[170:171], v34 offset:58368\n" \
"v_mfma_f32_16x16x32_bf16 a[188:191], v[92:95], v[172:175], a[188:191]\n" \
"s_lshl_b32 s96, s88, 15\n" \
"v_lshrrev_b32_e32 v213, 4, v212\n" \
"v_mfma_f32_16x16x32_bf16 a[188:191], v[96:99], v[176:179], a[188:191]\n" \
"s_add_u32 m0, %[mB], 0x4400\n" \
"s_add_u32 s84, s63, 0x8000\n" \
"v_mfma_f32_16x16x32_bf16 a[220:223], v[104:107], v[176:179], a[220:223]\n" \
"buffer_load_dwordx4 v26, s[72:75], s84 offen lds\n" \
"v_mfma_f32_16x16x32_bf16 a[220:223], v[100:103], v[172:175], a[220:223]\n" \
"v_and_b32_e32 v216, 1, v215\n" \
"v_lshlrev_b32_e32 v228, 3, v217\n" \
"v_mfma_f32_16x16x32_bf16 a[252:255], v[108:111], v[172:175], a[252:255]\n" \
"ds_read_b64_tr_b16 v[172:173], v35 offset:49152\n" \
"ds_read_b64_tr_b16 v[174:175], v35 offset:50176\n" \
"v_mfma_f32_16x16x32_bf16 a[252:255], v[112:115], v[176:179], a[252:255]\n" \
"s_add_u32 m0, %[mA], 0x1000\n" \
"s_add_u32 s85, s62, 0x8000\n" \
"v_mfma_f32_16x16x32_bf16 a[128:131], v[120:123], v[88:91], a[128:131]\n" \
"buffer_load_dwordx4 v22, s[64:67], s85 offen lds\n" \
"v_mfma_f32_16x16x32_bf16 a[128:131], v[116:119], v[84:87], a[128:131]\n" \
"ds_read_b64_tr_b16 v[176:177], v35 offset:57344\n" \
"ds_read_b64_tr_b16 v[178:179], v35 offset:58368\n" \
"v_mfma_f32_16x16x32_bf16 a[160:163], v[116:119], v[92:95], a[160:163]\n" \
"v_and_b32_e64 v238, 1, v217\n" \
"s_add_u32 s54, 0x10000, s101\n" \
"v_mfma_f32_16x16x32_bf16 a[160:163], v[120:123], v[96:99], a[160:163]\n" \
"s_add_u32 m0, %[mA], 0x1400\n" \
"s_add_u32 s84, s62, 0xa000\n" \
"v_mfma_f32_16x16x32_bf16 a[192:195], v[120:123], v[104:107], a[192:195]\n" \
"buffer_load_dwordx4 v23, s[64:67], s84 offen lds\n" \
"v_mfma_f32_16x16x32_bf16 a[192:195], v[116:119], v[100:103], a[192:195]\n" \
"s_add_u32 s98, s97, s96\n" \
"s_lshl_b32 s99, s100, 11\n" \
"v_mfma_f32_16x16x32_bf16 a[224:227], v[116:119], v[108:111], a[224:227]\n" \
"ds_read_b64_tr_b16 v[116:117], v32 offset:32768\n" \
"ds_read_b64_tr_b16 v[118:119], v32 offset:33792\n" \
"v_mfma_f32_16x16x32_bf16 a[224:227], v[120:123], v[112:115], a[224:227]\n" \
"s_add_u32 m0, %[mB], 0x800\n" \
"s_add_u32 s85, s63, 0x10000\n" \
"v_mfma_f32_16x16x32_bf16 a[132:135], v[128:131], v[88:91], a[132:135]\n" \
"buffer_load_dwordx4 v27, s[68:71], s85 offen lds\n" \
"v_mfma_f32_16x16x32_bf16 a[132:135], v[124:127], v[84:87], a[132:135]\n" \
"ds_read_b64_tr_b16 v[120:121], v32 offset:40960\n" \
"ds_read_b64_tr_b16 v[122:123], v32 offset:41984\n" \
"v_mfma_f32_16x16x32_bf16 a[164:167], v[124:127], v[92:95], a[164:167]\n" \
"v_lshlrev_b32_e32 v214, 2, v213\n" \
"v_mov_b32_e32 v218, 0x1000c0c\n" \
"v_mfma_f32_16x16x32_bf16 a[164:167], v[128:131], v[96:99], a[164:167]\n" \
"s_add_u32 m0, %[mB], 0x4800\n" \
"s_add_u32 s84, s63, 0x10000\n" \
"v_mfma_f32_16x16x32_bf16 a[196:199], v[128:131], v[104:107], a[196:199]\n" \
"buffer_load_dwordx4 v27, s[72:75], s84 offen lds\n" \
"v_mfma_f32_16x16x32_bf16 a[196:199], v[124:127], v[100:103], a[196:199]\n" \
"v_mov_b32_e32 v219, 0xc0c0100\n" \
"v_cmp_ne_u32_e32 vcc, 0, v216\n" \
"v_mfma_f32_16x16x32_bf16 a[228:231], v[124:127], v[108:111], a[228:231]\n" \
"ds_read_b64_tr_b16 v[124:125], v33 offset:32768\n" \
"ds_read_b64_tr_b16 v[126:127], v33 offset:33792\n" \
"v_mfma_f32_16x16x32_bf16 a[228:231], v[128:131], v[112:115], a[228:231]\n" \
"ds_read_b64_tr_b16 v[128:129], v33 offset:40960\n" \
"ds_read_b64_tr_b16 v[130:131], v33 offset:41984\n" \
"v_mfma_f32_16x16x32_bf16 a[136:139], v[136:139], v[88:91], a[136:139]\n" \
"v_mul_u32_u24_e32 v222, 0x480, v221\n" \
"s_add_u32 s89, s98, s99\n" \
"v_mfma_f32_16x16x32_bf16 a[136:139], v[132:135], v[84:87], a[136:139]\n" \
"s_lshl_b32 s101, s94, 16\n" \
"v_add_u32_e64 v217, v214, v216\n" \
"v_mfma_f32_16x16x32_bf16 a[168:171], v[132:135], v[92:95], a[168:171]\n" \
"v_cndmask_b32_e32 v220, v219, v218, vcc\n" \
"v_lshlrev_b32_e32 v224, 4, v223\n" \
"v_mfma_f32_16x16x32_bf16 a[168:171], v[136:139], v[96:99], a[168:171]\n" \
"s_add_u32 s42, s89, s101\n" \
"s_lshl_b32 s88, s95, 16\n" \
"v_mfma_f32_16x16x32_bf16 a[200:203], v[136:139], v[104:107], a[200:203]\n" \
"s_mul_i32 s97, 0x1800, s100\n" \
"v_mov_b32_e32 v221, 0xc0c0c0c\n" \
"v_mfma_f32_16x16x32_bf16 a[200:203], v[132:135], v[100:103], a[200:203]\n" \
"v_cmp_le_u32_e32 vcc, v217, v215\n" \
"v_add_u32_e32 v24, v222, v224\n" \
"v_mfma_f32_16x16x32_bf16 a[232:235], v[132:135], v[108:111], a[232:235]\n" \
"ds_read_b64_tr_b16 v[132:133], v34 offset:32768\n" \
"ds_read_b64_tr_b16 v[134:135], v34 offset:33792\n" \
"v_mfma_f32_16x16x32_bf16 a[232:235], v[136:139], v[112:115], a[232:235]\n" \
"ds_read_b64_tr_b16 v[136:137], v34 offset:40960\n" \
"ds_read_b64_tr_b16 v[138:139], v34 offset:41984\n" \
"v_mfma_f32_16x16x32_bf16 a[140:143], v[144:147], v[88:91], a[140:143]\n" \
"ds_read_b128 v[88:91], v29 offset:32768\n" \
"v_lshlrev_b32_e32 v227, 4, v226\n" \
"v_mfma_f32_16x16x32_bf16 a[140:143], v[140:143], v[84:87], a[140:143]\n" \
"ds_read_b128 v[84:87], v28 offset:32768\n" \
"s_add_u32 s43, s89, s88\n" \
"v_mfma_f32_16x16x32_bf16 a[172:175], v[140:143], v[92:95], a[172:175]\n" \
"ds_read_b128 v[92:95], v28 offset:34816\n" \
"s_add_u32 s96, s42, s97\n" \
"v_mfma_f32_16x16x32_bf16 a[172:175], v[144:147], v[96:99], a[172:175]\n" \
"ds_read_b128 v[96:99], v29 offset:34816\n" \
"v_cndmask_b32_e32 v40, v220, v221, vcc\n" \
"v_mfma_f32_16x16x32_bf16 a[204:207], v[144:147], v[104:107], a[204:207]\n" \
"ds_read_b128 v[104:107], v29 offset:36864\n" \
"v_add_u32_e64 v25, v222, v227\n" \
"v_mfma_f32_16x16x32_bf16 a[204:207], v[140:143], v[100:103], a[204:207]\n" \
"ds_read_b128 v[100:103], v28 offset:36864\n" \
"s_add_u32 s44, 0x4000, s96\n" \
"v_mfma_f32_16x16x32_bf16 a[236:239], v[140:143], v[108:111], a[236:239]\n" \
"ds_read_b64_tr_b16 v[140:141], v35 offset:32768\n" \
"ds_read_b64_tr_b16 v[142:143], v35 offset:33792\n" \
"v_mfma_f32_16x16x32_bf16 a[236:239], v[144:147], v[112:115], a[236:239]\n" \
"s_add_u32 m0, %[mA], 0x1800\n" \
"s_waitcnt lgkmcnt(12)\n" \
"s_nop 0\n" \
"v_mfma_f32_16x16x32_bf16 a[16:19], v[56:59], v[152:155], a[16:19]\n" \
"s_add_u32 s85, s62, 0xc000\n" \
"ds_read_b64_tr_b16 v[144:145], v35 offset:40960\n" \
"v_mfma_f32_16x16x32_bf16 a[16:19], v[52:55], v[148:151], a[16:19]\n" \
"buffer_load_dwordx4 v22, s[64:67], s85 offen lds\n" \
"v_mfma_f32_16x16x32_bf16 a[48:51], v[60:63], v[148:151], a[48:51]\n" \
"s_add_u32 m0, %[mA], 0x1c00\n" \
"s_add_u32 s84, s62, 0xe000\n" \
"v_mfma_f32_16x16x32_bf16 a[48:51], v[64:67], v[152:155], a[48:51]\n" \
"ds_read_b64_tr_b16 v[146:147], v35 offset:41984\n" \
"ds_read_b128 v[108:111], v28 offset:38912\n" \
"v_mfma_f32_16x16x32_bf16 a[80:83], v[72:75], v[152:155], a[80:83]\n" \
"buffer_load_dwordx4 v23, s[64:67], s84 offen lds\n" \
"v_mfma_f32_16x16x32_bf16 a[80:83], v[68:71], v[148:151], a[80:83]\n" \
"s_add_u32 m0, %[mB], 0xc00\n" \
"s_add_u32 s85, s63, 0x18000\n" \
"v_mfma_f32_16x16x32_bf16 a[112:115], v[76:79], v[148:151], a[112:115]\n" \
"ds_read_b128 v[112:115], v29 offset:38912\n" \
"s_add_u32 s98, s43, s97\n" \
"v_mfma_f32_16x16x32_bf16 a[112:115], v[80:83], v[152:155], a[112:115]\n" \
"buffer_load_dwordx4 v27, s[68:71], s85 offen lds\n" \
"v_mfma_f32_16x16x32_bf16 a[20:23], v[56:59], v[160:163], a[20:23]\n" \
"s_add_u32 m0, %[mB], 0x4c00\n" \
"s_add_u32 s84, s63, 0x18000\n" \
"v_mfma_f32_16x16x32_bf16 a[20:23], v[52:55], v[156:159], a[20:23]\n" \
"s_waitcnt vmcnt(15)\n" \
"v_or_b32_e32 v206, v210, v206\n" \
"v_mfma_f32_16x16x32_bf16 a[52:55], v[60:63], v[156:159], a[52:55]\n" \
"buffer_load_dwordx4 v27, s[72:75], s84 offen lds\n" \
"v_mfma_f32_16x16x32_bf16 a[52:55], v[64:67], v[160:163], a[52:55]\n" \
"s_mov_b32 m0, %[mS]\n" \
"s_lshr_b32 s94, %[w], 1\n" \
"v_mfma_f32_16x16x32_bf16 a[84:87], v[72:75], v[160:163], a[84:87]\n" \
"s_and_b32 s95, 1, %[w]\n" \
"v_mov_b32_e32 v223, 0x3020c0c\n" \
"v_mfma_f32_16x16x32_bf16 a[84:87], v[68:71], v[156:159], a[84:87]\n" \
"v_mov_b32_e32 v224, 0xc0c0302\n" \
"v_cmp_ne_u32_e32 vcc, 0, v216\n" \
"v_mfma_f32_16x16x32_bf16 a[116:119], v[76:79], v[156:159], a[116:119]\n" \
"s_add_u32 s45, 0x4000, s98\n" \
"s_add_u32 s96, s94, s95\n" \
"v_mfma_f32_16x16x32_bf16 a[116:119], v[80:83], v[160:163], a[116:119]\n" \
"s_add_u32 s97, 1, s94\n" \
"v_add_u32_e32 v222, 2, v217\n" \
"v_mfma_f32_16x16x32_bf16 a[24:27], v[56:59], v[168:171], a[24:27]\n" \
"v_cndmask_b32_e32 v225, v224, v223, vcc\n" \
"s_lshl_b32 s59, s96, 13\n" \
"v_mfma_f32_16x16x32_bf16 a[24:27], v[52:55], v[164:167], a[24:27]\n" \
"s_sub_u32 s98, s97, s95\n" \
"s_lshl_b32 s99, s94, 13\n" \
"v_mfma_f32_16x16x32_bf16 a[56:59], v[60:63], v[164:167], a[56:59]\n" \
"s_mul_i32 s101, 0x1800, s94\n" \
"v_mov_b32_e32 v226, 0xc0c0c0c\n" \
"v_mfma_f32_16x16x32_bf16 a[56:59], v[64:67], v[168:171], a[56:59]\n" \
"v_or_b32_e32 v204, v210, v204\n" \
"v_mov_b32_e32 v210, 0\n" \
"v_mfma_f32_16x16x32_bf16 a[88:91], v[72:75], v[168:171], a[88:91]\n" \
"s_mov_b32 s86, s10\n" \
"s_mov_b32 s87, s11\n" \
"v_mfma_f32_16x16x32_bf16 a[88:91], v[68:71], v[164:167], a[88:91]\n" \
"ds_write_b128 v196, v[200:203]\n" \
"s_mov_b32 exec_lo, 0x15\n" \
"s_mov_b32 exec_hi, 0x180000\n" \
"buffer_load_dword v11, s[80:83], 0 offen sc1 lds\n" \
"s_mov_b64 exec, -1\n" \
"v_mfma_f32_16x16x32_bf16 a[120:123], v[76:79], v[164:167], a[120:123]\n" \
"s_mov_b32 m0, %[mS]\n" \
"v_cmp_le_u32_e32 vcc, v222, v215\n" \
"v_mfma_f32_16x16x32_bf16 a[120:123], v[80:83], v[168:171], a[120:123]\n" \
"ds_write_b128 v197, v[204:207]\n" \
"v_mfma_f32_16x16x32_bf16 a[28:31], v[56:59], v[176:179], a[28:31]\n" \
"ds_write_b128 v198, v[208:211]\n" \
"v_mfma_f32_16x16x32_bf16 a[28:31], v[52:55], v[172:175], a[28:31]\n" \
"s_lshl_b32 s60, s98, 13\n" \
"s_add_u32 s100, 0x1800, s99\n" \
"v_mfma_f32_16x16x32_bf16 a[60:63], v[60:63], v[172:175], a[60:63]\n" \
"s_sub_u32 s88, 0x17000, s101\n" \
"s_lshl_b32 s96, s94, 15\n" \
"s_mov_b32 exec_lo, 0x3ffff\n" \
"s_mov_b32 exec_hi, 0x0\n" \
"global_load_lds_dword v188, s[86:87]\n" \
"s_mov_b64 exec, -1\n" \
"v_mfma_f32_16x16x32_bf16 a[60:63], v[64:67], v[176:179], a[60:63]\n" \
"v_cndmask_b32_e32 v41, v225, v226, vcc\n" \
"v_and_b32_e32 v230, 3, v229\n" \
"v_mfma_f32_16x16x32_bf16 a[92:95], v[72:75], v[176:179], a[92:95]\n" \
"s_cmp_lg_u32 s95, 0\n" \
"s_cselect_b32 s61, s100, s88\n" \
"v_mfma_f32_16x16x32_bf16 a[92:95], v[68:71], v[172:175], a[92:95]\n" \
"s_add_u32 s97, 0x10000, s96\n" \
"s_lshl_b32 s98, s95, 11\n" \
"v_mfma_f32_16x16x32_bf16 a[124:127], v[76:79], v[172:175], a[124:127]\n" \
"v_mov_b32_e32 v227, 0xc0c0c0c\n" \
"v_cmp_le_u32_e32 vcc, v217, v215\n" \
"v_mfma_f32_16x16x32_bf16 a[124:127], v[80:83], v[176:179], a[124:127]\n" \
"v_add_u32_e32 v231, v228, v230\n" \
"s_add_u32 s101, s97, s98\n" \
"v_mfma_f32_16x16x32_bf16 a[0:3], v[120:123], v[56:59], a[0:3]\n" \
"v_cndmask_b32_e32 v42, v227, v220, vcc\n" \
"v_lshlrev_b32_e32 v232, 7, v231\n" \
"v_mfma_f32_16x16x32_bf16 a[0:3], v[116:119], v[52:55], a[0:3]\n" \
"v_lshrrev_b32_e32 v237, 1, v230\n" \
"s_add_u32 s100, s101, s99\n" \
"v_mfma_f32_16x16x32_bf16 a[32:35], v[116:119], v[60:63], a[32:35]\n" \
"v_mov_b32_e32 v228, 0xc0c0c0c\n" \
"v_cmp_le_u32_e32 vcc, v222, v215\n" \
"v_mfma_f32_16x16x32_bf16 a[32:35], v[120:123], v[64:67], a[32:35]\n" \
"v_add_u32_e32 v233, 0x14000, v232\n" \
"v_lshlrev_b32_e32 v235, 3, v234\n" \
"v_mfma_f32_16x16x32_bf16 a[64:67], v[120:123], v[72:75], a[64:67]\n" \
"s_add_u32 s88, 0x4000, s100\n" \
"v_cndmask_b32_e32 v43, v228, v225, vcc\n" \
"v_mfma_f32_16x16x32_bf16 a[64:67], v[116:119], v[68:71], a[64:67]\n" \
"v_and_b32_e32 v229, 50, v212\n" \
"v_and_b32_e64 v230, 16, v20\n" \
"v_mfma_f32_16x16x32_bf16 a[96:99], v[116:119], v[76:79], a[96:99]\n" \
"v_mov_b32_e32 v231, 0x3f80\n" \
"v_add_u32_e64 v236, v233, v235\n" \
"v_mfma_f32_16x16x32_bf16 a[96:99], v[120:123], v[80:83], a[96:99]\n" \
"v_lshlrev_b32_e32 v239, 1, v238\n" \
"v_lshlrev_b32_e32 v232, v230, v231\n" \
"v_mfma_f32_16x16x32_bf16 a[4:7], v[128:131], v[56:59], a[4:7]\n" \
"s_cmp_lg_u32 s95, 0\n" \
"s_cselect_b32 s47, s88, s101\n" \
"v_mfma_f32_16x16x32_bf16 a[4:7], v[124:127], v[52:55], a[4:7]\n" \
"v_cmp_eq_u32_e32 vcc, 16, v229\n" \
"v_or_b32_e32 v240, v237, v239\n" \
"v_mfma_f32_16x16x32_bf16 a[36:39], v[124:127], v[60:63], a[36:39]\n" \
"v_cndmask_b32_e32 v46, 0, v232, vcc\n" \
"v_and_b32_e32 v233, 51, v212\n" \
"v_mfma_f32_16x16x32_bf16 a[36:39], v[128:131], v[64:67], a[36:39]\n" \
"v_lshlrev_b32_e32 v241, 5, v240\n" \
"v_mov_b32_e32 v234, 0x3f80\n" \
"v_mfma_f32_16x16x32_bf16 a[68:71], v[128:131], v[72:75], a[68:71]\n" \
"v_cmp_eq_u32_e32 vcc, 2, v233\n" \
"s_lshl_b32 s88, s95, 4\n" \
"v_mfma_f32_16x16x32_bf16 a[68:71], v[124:127], v[68:71], a[68:71]\n" \
"v_add_u32_e32 v190, v236, v241\n" \
"v_cndmask_b32_e32 v47, 0, v234, vcc\n" \
"v_mfma_f32_16x16x32_bf16 a[100:103], v[124:127], v[76:79], a[100:103]\n" \
"v_add_u32_e32 v235, s88, v215\n" \
"v_mov_b32_e32 v236, 0xffff\n" \
"v_mfma_f32_16x16x32_bf16 a[100:103], v[128:131], v[80:83], a[100:103]\n" \
"v_cmp_le_u32_e32 vcc, v214, v235\n" \
"s_waitcnt lgkmcnt(0)\n" \
"v_mfma_f32_16x16x32_bf16 a[8:11], v[136:139], v[56:59], a[8:11]\n" \
"v_cndmask_b32_e32 v237, 0, v236, vcc\n" \
"v_mov_b32_e32 v238, 0xffff0000\n" \
"v_mfma_f32_16x16x32_bf16 a[8:11], v[132:135], v[52:55], a[8:11]\n" \
"v_cmp_lt_u32_e32 vcc, v214, v235\n" \
"v_add_u32_e64 v240, 2, v214\n" \
"v_mfma_f32_16x16x32_bf16 a[40:43], v[132:135], v[60:63], a[40:43]\n" \
"v_cndmask_b32_e32 v239, 0, v238, vcc\n" \
"v_mov_b32_e32 v241, 0xffff\n" \
"v_mfma_f32_16x16x32_bf16 a[40:43], v[136:139], v[64:67], a[40:43]\n" \
"v_cmp_le_u32_e32 vcc, v240, v235\n" \
"v_mov_b32_e32 v243, 0xffff0000\n" \
"v_mfma_f32_16x16x32_bf16 a[72:75], v[136:139], v[72:75], a[72:75]\n" \
"v_cndmask_b32_e32 v242, 0, v241, vcc\n" \
"v_cmp_lt_u32_e32 vcc, v240, v235\n" \
"v_mfma_f32_16x16x32_bf16 a[72:75], v[132:135], v[68:71], a[72:75]\n" \
"v_add_u32_e32 v245, 16, v214\n" \
"v_cndmask_b32_e32 v244, 0, v243, vcc\n" \
"v_mfma_f32_16x16x32_bf16 a[104:107], v[132:135], v[76:79], a[104:107]\n" \
"v_mov_b32_e32 v246, 0xffff\n" \
"v_cmp_le_u32_e32 vcc, v245, v235\n" \
"v_mfma_f32_16x16x32_bf16 a[104:107], v[136:139], v[80:83], a[104:107]\n" \
"v_mov_b32_e32 v248, 0xffff0000\n" \
"v_cndmask_b32_e32 v247, 0, v246, vcc\n" \
"v_mfma_f32_16x16x32_bf16 a[12:15], v[144:147], v[56:59], a[12:15]\n" \
"s_add_u32 s62, s22, 0x280\n" \
"s_and_b32 s62, s62, 0x3ff\n" \
"v_mfma_f32_16x16x32_bf16 a[12:15], v[140:143], v[52:55], a[12:15]\n" \
"s_lshl_b32 s63, s62, 12\n" \
"v_cmp_lt_u32_e32 vcc, v245, v235\n" \
"v_mfma_f32_16x16x32_bf16 a[44:47], v[140:143], v[60:63], a[44:47]\n" \
"v_add_u32_e32 v250, 18, v214\n" \
"v_cndmask_b32_e32 v249, 0, v248, vcc\n" \
"v_mfma_f32_16x16x32_bf16 a[44:47], v[144:147], v[64:67], a[44:47]\n" \
"s_lshl_b32 s89, %[w], 8\n" \
"v_mov_b32_e32 v251, 0xffff\n" \
"v_mfma_f32_16x16x32_bf16 a[76:79], v[144:147], v[72:75], a[76:79]\n" \
"v_cmp_le_u32_e32 vcc, v250, v235\n" \
"s_add_u32 s46, 0x27000, s89\n" \
"v_mfma_f32_16x16x32_bf16 a[76:79], v[140:143], v[68:71], a[76:79]\n" \
"v_cndmask_b32_e32 v213, 0, v251, vcc\n" \
"s_add_u32 s89, 0xffffc000, s101\n" \
"v_mfma_f32_16x16x32_bf16 a[108:111], v[140:143], v[76:79], a[108:111]\n" \
"v_mov_b32_e32 v218, 0xffff0000\n" \
"v_cmp_lt_u32_e32 vcc, v250, v235\n" \
"v_mfma_f32_16x16x32_bf16 a[108:111], v[144:147], v[80:83], a[108:111]\n" \
"s_cmp_lg_u32 s95, 0\n" \
"s_cselect_b32 s48, s89, s100\n" \
"s_waitcnt vmcnt(2)\n" \
"s_barrier\n" \
"v_mfma_f32_16x16x32_bf16 a[128:131], v[120:123], v[88:91], a[128:131]\n" \
"s_add_u32 m0, %[mA], 0x8000\n" \
"s_add_u32 s85, s62, 0x0\n" \
"s_nop 0\n" \
"v_mfma_f32_16x16x32_bf16 a[128:131], v[116:119], v[84:87], a[128:131]\n" \
"buffer_load_dwordx4 v22, s[64:67], s85 offen lds\n" \
"v_mfma_f32_16x16x32_bf16 a[160:163], v[116:119], v[92:95], a[160:163]\n" \
"ds_read_b128 v[56:59], v29 offset:16384\n" \
"ds_read_b128 v[52:55], v28 offset:16384\n" \
"v_mfma_f32_16x16x32_bf16 a[160:163], v[120:123], v[96:99], a[160:163]\n" \
"s_add_u32 m0, %[mA], 0x8400\n" \
"s_add_u32 s84, s62, 0x2000\n" \
"v_mfma_f32_16x16x32_bf16 a[192:195], v[120:123], v[104:107], a[192:195]\n" \
"buffer_load_dwordx4 v23, s[64:67], s84 offen lds\n" \
"v_mfma_f32_16x16x32_bf16 a[192:195], v[116:119], v[100:103], a[192:195]\n" \
"ds_read_b128 v[60:63], v28 offset:18432\n" \
"ds_read_b128 v[64:67], v29 offset:18432\n" \
"v_mfma_f32_16x16x32_bf16 a[224:227], v[116:119], v[108:111], a[224:227]\n" \
"ds_read_b64_tr_b16 v[116:117], v32 offset:0\n" \
"ds_read_b64_tr_b16 v[118:119], v32 offset:1024\n" \
"v_mfma_f32_16x16x32_bf16 a[224:227], v[120:123], v[112:115], a[224:227]\n" \
"s_add_u32 m0, %[mB], 0x8000\n" \
"s_add_u32 s85, s63, 0x0\n" \
"v_mfma_f32_16x16x32_bf16 a[132:135], v[128:131], v[88:91], a[132:135]\n" \
"buffer_load_dwordx4 v26, s[68:71], s85 offen lds\n" \
"v_mfma_f32_16x16x32_bf16 a[132:135], v[124:127], v[84:87], a[132:135]\n" \
"ds_read_b64_tr_b16 v[120:121], v32 offset:8192\n" \
"ds_read_b64_tr_b16 v[122:123], v32 offset:9216\n" \
"v_mfma_f32_16x16x32_bf16 a[164:167], v[124:127], v[92:95], a[164:167]\n" \
"ds_read_b128 v[72:75], v29 offset:20480\n" \
"ds_read_b128 v[68:71], v28 offset:20480\n" \
"v_mfma_f32_16x16x32_bf16 a[164:167], v[128:131], v[96:99], a[164:167]\n" \
"s_add_u32 m0, %[mB], 0xc000\n" \
"s_add_u32 s84, s63, 0x0\n" \
"v_mfma_f32_16x16x32_bf16 a[196:199], v[128:131], v[104:107], a[196:199]\n" \
"buffer_load_dwordx4 v26, s[72:75], s84 offen lds\n" \
"v_mfma_f32_16x16x32_bf16 a[196:199], v[124:127], v[100:103], a[196:199]\n" \
"ds_read_b128 v[76:79], v28 offset:22528\n" \
"ds_read_b128 v[80:83], v29 offset:22528\n" \
"v_mfma_f32_16x16x32_bf16 a[228:231], v[124:127], v[108:111], a[228:231]\n" \
"ds_read_b64_tr_b16 v[124:125], v33 offset:0\n" \
"ds_read_b64_tr_b16 v[126:127], v33 offset:1024\n" \
"v_mfma_f32_16x16x32_bf16 a[228:231], v[128:131], v[112:115], a[228:231]\n" \
"s_add_u32 m0, %[mA], 0x8800\n" \
"s_add_u32 s85, s62, 0x4000\n" \
"v_mfma_f32_16x16x32_bf16 a[136:139], v[136:139], v[88:91], a[136:139]\n" \
"buffer_load_dwordx4 v22, s[64:67], s85 offen lds\n" \
"v_mfma_f32_16x16x32_bf16 a[136:139], v[132:135], v[84:87], a[136:139]\n" \
"ds_read_b64_tr_b16 v[128:129], v33 offset:8192\n" \
"ds_read_b64_tr_b16 v[130:131], v33 offset:9216\n" \
"v_mfma_f32_16x16x32_bf16 a[168:171], v[132:135], v[92:95], a[168:171]\n" \
"ds_read_b128 v[200:203], v199 offset:0\n" \
"ds_read_b128 v[204:207], v199 offset:4096\n" \
"v_mfma_f32_16x16x32_bf16 a[168:171], v[136:139], v[96:99], a[168:171]\n" \
"s_add_u32 m0, %[mA], 0x8c00\n" \
"s_add_u32 s84, s62, 0x6000\n" \
"v_mfma_f32_16x16x32_bf16 a[200:203], v[136:139], v[104:107], a[200:203]\n" \
"buffer_load_dwordx4 v23, s[64:67], s84 offen lds\n" \
"v_mfma_f32_16x16x32_bf16 a[200:203], v[132:135], v[100:103], a[200:203]\n" \
"ds_read_b128 v[208:211], v199 offset:8192\n" \
"v_cndmask_b32_e32 v219, 0, v218, vcc\n" \
"s_nop 0\n" \
"v_mfma_f32_16x16x32_bf16 a[232:235], v[132:135], v[108:111], a[232:235]\n" \
"ds_read_b64_tr_b16 v[132:133], v34 offset:0\n" \
"ds_read_b64_tr_b16 v[134:135], v34 offset:1024\n" \
"v_mfma_f32_16x16x32_bf16 a[232:235], v[136:139], v[112:115], a[232:235]\n" \
"s_add_u32 m0, %[mB], 0x8400\n" \
"s_add_u32 s85, s63, 0x8000\n" \
"v_mfma_f32_16x16x32_bf16 a[140:143], v[144:147], v[88:91], a[140:143]\n" \
"buffer_load_dwordx4 v26, s[68:71], s85 offen lds\n" \
"v_mfma_f32_16x16x32_bf16 a[140:143], v[140:143], v[84:87], a[140:143]\n" \
"ds_read_b64_tr_b16 v[136:137], v34 offset:8192\n" \
"ds_read_b64_tr_b16 v[138:139], v34 offset:9216\n" \
"v_mfma_f32_16x16x32_bf16 a[172:175], v[140:143], v[92:95], a[172:175]\n" \
"v_lshlrev_b32_e32 v216, 3, v212\n" \
"s_lshr_b32 s94, %[b], 5\n" \
"v_mfma_f32_16x16x32_bf16 a[172:175], v[144:147], v[96:99], a[172:175]\n" \
"s_add_u32 m0, %[mB], 0xc400\n" \
"s_add_u32 s84, s63, 0x8000\n" \
"v_mfma_f32_16x16x32_bf16 a[204:207], v[144:147], v[104:107], a[204:207]\n" \
"buffer_load_dwordx4 v26, s[72:75], s84 offen lds\n" \
"v_mfma_f32_16x16x32_bf16 a[204:207], v[140:143], v[100:103], a[204:207]\n" \
"s_mov_b32 s96, 0xc000\n" \
"s_mov_b32 s97, 0x8000\n" \
"v_mfma_f32_16x16x32_bf16 a[236:239], v[140:143], v[108:111], a[236:239]\n" \
"ds_read_b64_tr_b16 v[140:141], v35 offset:0\n" \
"ds_read_b64_tr_b16 v[142:143], v35 offset:1024\n" \
"v_mfma_f32_16x16x32_bf16 a[236:239], v[144:147], v[112:115], a[236:239]\n" \
"s_add_u32 m0, %[mA], 0x9000\n" \
"s_add_u32 s85, s62, 0x8000\n" \
"v_mfma_f32_16x16x32_bf16 a[144:147], v[88:91], v[152:155], a[144:147]\n" \
"buffer_load_dwordx4 v22, s[64:67], s85 offen lds\n" \
"v_mfma_f32_16x16x32_bf16 a[144:147], v[84:87], v[148:151], a[144:147]\n" \
"ds_read_b64_tr_b16 v[144:145], v35 offset:8192\n" \
"ds_read_b64_tr_b16 v[146:147], v35 offset:9216\n" \
"v_mfma_f32_16x16x32_bf16 a[176:179], v[92:95], v[148:151], a[176:179]\n" \
"v_or_b32_e32 v15, v213, v219\n" \
"s_lshl_b32 s101, %[w], 9\n" \
"v_mfma_f32_16x16x32_bf16 a[176:179], v[96:99], v[152:155], a[176:179]\n" \
"s_add_u32 m0, %[mA], 0x9400\n" \
"s_add_u32 s84, s62, 0xa000\n" \
"v_mfma_f32_16x16x32_bf16 a[208:211], v[104:107], v[152:155], a[208:211]\n" \
"buffer_load_dwordx4 v23, s[64:67], s84 offen lds\n" \
"v_mfma_f32_16x16x32_bf16 a[208:211], v[100:103], v[148:151], a[208:211]\n" \
"v_lshrrev_b32_e32 v212, 4, v20\n" \
"s_nop 0\n" \
"s_add_u32 s89, 0x27800, s101\n" \
"v_mfma_f32_16x16x32_bf16 a[240:243], v[108:111], v[148:151], a[240:243]\n" \
"ds_read_b64_tr_b16 v[148:149], v32 offset:16384\n" \
"ds_read_b64_tr_b16 v[150:151], v32 offset:17408\n" \
"v_mfma_f32_16x16x32_bf16 a[240:243], v[112:115], v[152:155], a[240:243]\n" \
"s_add_u32 m0, %[mB], 0x8800\n" \
"s_add_u32 s85, s63, 0x10000\n" \
"v_mfma_f32_16x16x32_bf16 a[148:151], v[88:91], v[160:163], a[148:151]\n" \
"buffer_load_dwordx4 v27, s[68:71], s85 offen lds\n" \
"v_mfma_f32_16x16x32_bf16 a[148:151], v[84:87], v[156:159], a[148:151]\n" \
"ds_read_b64_tr_b16 v[152:153], v32 offset:24576\n" \
"ds_read_b64_tr_b16 v[154:155], v32 offset:25600\n" \
"v_mfma_f32_16x16x32_bf16 a[180:183], v[92:95], v[156:159], a[180:183]\n" \
"s_cmp_lg_u32 s94, 0\n" \
"s_cselect_b32 s53, s96, 0x8000\n" \
"v_mfma_f32_16x16x32_bf16 a[180:183], v[96:99], v[160:163], a[180:183]\n" \
"s_add_u32 m0, %[mB], 0xc800\n" \
"s_add_u32 s84, s63, 0x10000\n" \
"v_mfma_f32_16x16x32_bf16 a[212:215], v[104:107], v[160:163], a[212:215]\n" \
"buffer_load_dwordx4 v27, s[72:75], s84 offen lds\n" \
"v_mfma_f32_16x16x32_bf16 a[212:215], v[100:103], v[156:159], a[212:215]\n" \
"s_cmp_lg_u32 s94, 0\n" \
"s_cselect_b32 s98, s97, 0x4000\n" \
"v_mfma_f32_16x16x32_bf16 a[244:247], v[108:111], v[156:159], a[244:247]\n" \
"ds_read_b64_tr_b16 v[156:157], v33 offset:16384\n" \
"ds_read_b64_tr_b16 v[158:159], v33 offset:17408\n" \
"v_mfma_f32_16x16x32_bf16 a[244:247], v[112:115], v[160:163], a[244:247]\n" \
"ds_read_b64_tr_b16 v[160:161], v33 offset:24576\n" \
"ds_read_b64_tr_b16 v[162:163], v33 offset:25600\n" \
"v_mfma_f32_16x16x32_bf16 a[152:155], v[88:91], v[168:171], a[152:155]\n" \
"v_mul_u32_u24_e32 v213, 0xe39, v212\n" \
"v_add_u32_e64 v19, s89, v216\n" \
"v_mfma_f32_16x16x32_bf16 a[152:155], v[84:87], v[164:167], a[152:155]\n" \
"v_lshrrev_b32_e32 v214, 16, v213\n" \
"v_add_u32_e32 v222, 64, v212\n" \
"v_mfma_f32_16x16x32_bf16 a[184:187], v[92:95], v[164:167], a[184:187]\n" \
"v_mul_u32_u24_e32 v216, 18, v214\n" \
"s_and_b32 s94, 7, %[b]\n" \
"v_mfma_f32_16x16x32_bf16 a[184:187], v[96:99], v[168:171], a[184:187]\n" \
"v_mul_u32_u24_e32 v223, 0xe39, v222\n" \
"v_sub_u32_e32 v217, v212, v216\n" \
"v_mfma_f32_16x16x32_bf16 a[216:219], v[104:107], v[168:171], a[216:219]\n" \
"s_lshl_b32 s95, s94, 5\n" \
"v_lshrrev_b32_e32 v224, 16, v223\n" \
"v_mfma_f32_16x16x32_bf16 a[216:219], v[100:103], v[164:167], a[216:219]\n" \
"v_add_u32_e32 v233, 0x80, v212\n" \
"v_or_b32_e32 v13, v242, v244\n" \
"v_mfma_f32_16x16x32_bf16 a[248:251], v[108:111], v[164:167], a[248:251]\n" \
"ds_read_b64_tr_b16 v[164:165], v34 offset:16384\n" \
"ds_read_b64_tr_b16 v[166:167], v34 offset:17408\n" \
"v_mfma_f32_16x16x32_bf16 a[248:251], v[112:115], v[168:171], a[248:251]\n" \
"ds_read_b64_tr_b16 v[168:169], v34 offset:24576\n" \
"ds_read_b64_tr_b16 v[170:171], v34 offset:25600\n" \
"v_mfma_f32_16x16x32_bf16 a[156:159], v[88:91], v[176:179], a[156:159]\n" \
"ds_read_b128 v[88:91], v29 offset:0\n" \
"v_lshlrev_b32_e32 v221, 2, v235\n" \
"v_mfma_f32_16x16x32_bf16 a[156:159], v[84:87], v[172:175], a[156:159]\n" \
"ds_read_b128 v[84:87], v28 offset:0\n" \
"s_add_u32 s96, 32, s95\n" \
"v_mfma_f32_16x16x32_bf16 a[188:191], v[92:95], v[172:175], a[188:191]\n" \
"ds_read_b128 v[92:95], v28 offset:2048\n" \
"v_lshlrev_b32_e32 v218, 4, v217\n" \
"v_mfma_f32_16x16x32_bf16 a[188:191], v[96:99], v[176:179], a[188:191]\n" \
"ds_read_b128 v[96:99], v29 offset:2048\n" \
"v_mul_u32_u24_e32 v226, 18, v224\n" \
"v_mfma_f32_16x16x32_bf16 a[220:223], v[104:107], v[176:179], a[220:223]\n" \
"ds_read_b128 v[104:107], v29 offset:4096\n" \
"v_mul_u32_u24_e32 v234, 0xe39, v233\n" \
"v_mfma_f32_16x16x32_bf16 a[220:223], v[100:103], v[172:175], a[220:223]\n" \
"ds_read_b128 v[100:103], v28 offset:4096\n" \
"v_or_b32_e32 v12, v237, v239\n" \
"v_mfma_f32_16x16x32_bf16 a[252:255], v[108:111], v[172:175], a[252:255]\n" \
"ds_read_b64_tr_b16 v[172:173], v35 offset:16384\n" \
"ds_read_b64_tr_b16 v[174:175], v35 offset:17408\n" \
"v_mfma_f32_16x16x32_bf16 a[252:255], v[112:115], v[176:179], a[252:255]\n" \
"s_add_u32 m0, %[mA], 0x9800\n" \
"s_waitcnt lgkmcnt(12)\n" \
"v_mfma_f32_16x16x32_bf16 a[0:3], v[120:123], v[56:59], a[0:3]\n" \
"s_add_u32 s85, s62, 0xc000\n" \
"ds_read_b64_tr_b16 v[176:177], v35 offset:24576\n" \
"v_mfma_f32_16x16x32_bf16 a[0:3], v[116:119], v[52:55], a[0:3]\n" \
"buffer_load_dwordx4 v22, s[64:67], s85 offen lds\n" \
"v_mfma_f32_16x16x32_bf16 a[32:35], v[116:119], v[60:63], a[32:35]\n" \
"s_add_u32 m0, %[mA], 0x9c00\n" \
"s_add_u32 s84, s62, 0xe000\n" \
"v_mfma_f32_16x16x32_bf16 a[32:35], v[120:123], v[64:67], a[32:35]\n" \
"ds_read_b64_tr_b16 v[178:179], v35 offset:25600\n" \
"ds_read_b128 v[108:111], v28 offset:6144\n" \
"v_mfma_f32_16x16x32_bf16 a[64:67], v[120:123], v[72:75], a[64:67]\n" \
"buffer_load_dwordx4 v23, s[64:67], s84 offen lds\n" \
"v_mfma_f32_16x16x32_bf16 a[64:67], v[116:119], v[68:71], a[64:67]\n" \
"s_add_u32 m0, %[mB], 0x8c00\n" \
"s_add_u32 s85, s63, 0x18000\n" \
"v_mfma_f32_16x16x32_bf16 a[96:99], v[116:119], v[76:79], a[96:99]\n" \
"ds_read_b128 v[112:115], v29 offset:6144\n" \
"L_pf2:\n" \
"s_cbranch_execz L_pf6\n" \
"s_nop 0\n" \
"v_add_u32_e64 v219, s96, v218\n" \
"v_mfma_f32_16x16x32_bf16 a[96:99], v[120:123], v[80:83], a[96:99]\n" \
"buffer_load_dwordx4 v27, s[68:71], s85 offen lds\n" \
"v_mfma_f32_16x16x32_bf16 a[4:7], v[128:131], v[56:59], a[4:7]\n" \
"s_add_u32 m0, %[mB], 0xcc00\n" \
"s_add_u32 s84, s63, 0x18000\n" \
"v_mfma_f32_16x16x32_bf16 a[4:7], v[124:127], v[52:55], a[4:7]\n" \
"v_sub_u32_e32 v227, v222, v226\n" \
"v_lshrrev_b32_e32 v235, 16, v234\n" \
"v_mfma_f32_16x16x32_bf16 a[36:39], v[124:127], v[60:63], a[36:39]\n" \
"buffer_load_dwordx4 v27, s[72:75], s84 offen lds\n" \
"v_mfma_f32_16x16x32_bf16 a[36:39], v[128:131], v[64:67], a[36:39]\n" \
"v_add_u32_e32 v244, 0xc0, v212\n" \
"v_add_u32_e32 v18, 0x27000, v221\n" \
"v_mfma_f32_16x16x32_bf16 a[68:71], v[128:131], v[72:75], a[68:71]\n" \
"v_and_b32_e32 v220, 0xff, v219\n" \
"v_cmp_gt_u32_e32 vcc, 16, v217\n" \
"v_mfma_f32_16x16x32_bf16 a[68:71], v[124:127], v[68:71], a[68:71]\n" \
"buffer_store_dwordx4 v[200:203], v20, s[80:83], s57 offen sc1\n" \
"v_mfma_f32_16x16x32_bf16 a[100:103], v[124:127], v[76:79], a[100:103]\n" \
"v_lshlrev_b32_e32 v228, 4, v227\n" \
"v_mul_u32_u24_e32 v237, 18, v235\n" \
"v_mfma_f32_16x16x32_bf16 a[100:103], v[128:131], v[80:83], a[100:103]\n" \
"v_mul_u32_u24_e32 v245, 0xe39, v244\n" \
"v_cndmask_b32_e32 v221, v218, v220, vcc\n" \
"v_mfma_f32_16x16x32_bf16 a[8:11], v[136:139], v[56:59], a[8:11]\n" \
"v_add_u32_e32 v229, s96, v228\n" \
"v_sub_u32_e32 v238, v233, v237\n" \
"v_mfma_f32_16x16x32_bf16 a[8:11], v[132:135], v[52:55], a[8:11]\n" \
"v_lshrrev_b32_e32 v246, 16, v245\n" \
"v_add_u32_e32 v219, 0x100, v212\n" \
"v_mfma_f32_16x16x32_bf16 a[40:43], v[132:135], v[60:63], a[40:43]\n" \
"buffer_store_dwordx4 v[204:207], v20, s[80:83], s56 offen sc1\n" \
"v_mfma_f32_16x16x32_bf16 a[40:43], v[136:139], v[64:67], a[40:43]\n" \
"v_or_b32_e32 v14, v247, v249\n" \
"v_mul_u32_u24_e32 v215, 0x480, v214\n" \
"v_mfma_f32_16x16x32_bf16 a[72:75], v[136:139], v[72:75], a[72:75]\n" \
"v_and_b32_e32 v230, 0xff, v229\n" \
"v_cmp_gt_u32_e32 vcc, 16, v227\n" \
"v_mfma_f32_16x16x32_bf16 a[72:75], v[132:135], v[68:71], a[72:75]\n" \
"v_lshlrev_b32_e32 v239, 4, v238\n" \
"v_mul_u32_u24_e32 v248, 18, v246\n" \
"v_mfma_f32_16x16x32_bf16 a[104:107], v[132:135], v[76:79], a[104:107]\n" \
"v_mul_u32_u24_e32 v217, 0xe39, v219\n" \
"v_add_u32_e32 v0, v215, v221\n" \
"v_mfma_f32_16x16x32_bf16 a[104:107], v[136:139], v[80:83], a[104:107]\n" \
"v_cndmask_b32_e32 v231, v228, v230, vcc\n" \
"v_add_u32_e32 v240, s96, v239\n" \
"v_mfma_f32_16x16x32_bf16 a[12:15], v[144:147], v[56:59], a[12:15]\n" \
"buffer_store_dwordx4 v[208:211], v20, s[80:83], s58 offen sc1\n" \
"v_mfma_f32_16x16x32_bf16 a[12:15], v[140:143], v[52:55], a[12:15]\n" \
"v_sub_u32_e32 v249, v244, v248\n" \
"v_lshrrev_b32_e32 v220, 16, v217\n" \
"v_mfma_f32_16x16x32_bf16 a[44:47], v[140:143], v[60:63], a[44:47]\n" \
"v_add_u32_e32 v229, 0x140, v212\n" \
"v_mul_u32_u24_e32 v225, 0x480, v224\n" \
"v_mfma_f32_16x16x32_bf16 a[44:47], v[144:147], v[64:67], a[44:47]\n" \
"v_and_b32_e32 v241, 0xff, v240\n" \
"v_cmp_gt_u32_e32 vcc, 16, v238\n" \
"v_mfma_f32_16x16x32_bf16 a[76:79], v[144:147], v[72:75], a[76:79]\n" \
"v_lshlrev_b32_e32 v250, 4, v249\n" \
"v_mul_u32_u24_e32 v215, 18, v220\n" \
"v_mfma_f32_16x16x32_bf16 a[76:79], v[140:143], v[68:71], a[76:79]\n" \
"v_mul_u32_u24_e32 v227, 0xe39, v229\n" \
"v_add_u32_e32 v232, v225, v231\n" \
"v_mfma_f32_16x16x32_bf16 a[108:111], v[140:143], v[76:79], a[108:111]\n" \
"s_mov_b32 m0, %[mS]\n" \
"s_mov_b32 s86, s10\n" \
"v_mfma_f32_16x16x32_bf16 a[108:111], v[144:147], v[80:83], a[108:111]\n" \
"s_mov_b32 s87, s11\n" \
"v_cndmask_b32_e32 v242, v239, v241, vcc\n" \
"v_mfma_f32_16x16x32_bf16 a[16:19], v[56:59], v[152:155], a[16:19]\n" \
"v_add_u32_e32 v251, s96, v250\n" \
"v_sub_u32_e32 v221, v219, v215\n" \
"v_mfma_f32_16x16x32_bf16 a[16:19], v[52:55], v[148:151], a[16:19]\n" \
"v_lshrrev_b32_e32 v230, 16, v227\n" \
"v_add_u32_e32 v240, 0x180, v212\n" \
"v_mfma_f32_16x16x32_bf16 a[48:51], v[60:63], v[148:151], a[48:51]\n" \
"v_mul_u32_u24_e32 v236, 0x480, v235\n" \
"v_and_b32_e32 v213, 0xff, v251\n" \
"v_mfma_f32_16x16x32_bf16 a[48:51], v[64:67], v[152:155], a[48:51]\n" \
"v_cmp_gt_u32_e32 vcc, 16, v249\n" \
"v_lshlrev_b32_e32 v223, 4, v221\n" \
"v_mfma_f32_16x16x32_bf16 a[80:83], v[72:75], v[152:155], a[80:83]\n" \
"v_mul_u32_u24_e32 v225, 18, v230\n" \
"v_mul_u32_u24_e32 v238, 0xe39, v240\n" \
"s_mov_b32 exec_lo, 0xfffc0000\n" \
"s_mov_b32 exec_hi, 0xf\n" \
"global_load_lds_dword v188, s[86:87]\n" \
"s_mov_b64 exec, -1\n" \
"v_mfma_f32_16x16x32_bf16 a[80:83], v[68:71], v[148:151], a[80:83]\n" \
"v_add_u32_e32 v1, 0xfffffc00, v232\n" \
"v_add_u32_e64 v243, v236, v242\n" \
"v_mfma_f32_16x16x32_bf16 a[112:115], v[76:79], v[148:151], a[112:115]\n" \
"v_cndmask_b32_e32 v214, v250, v213, vcc\n" \
"v_add_u32_e32 v224, s96, v223\n" \
"v_mfma_f32_16x16x32_bf16 a[112:115], v[80:83], v[152:155], a[112:115]\n" \
"v_sub_u32_e32 v231, v229, v225\n" \
"v_lshrrev_b32_e32 v241, 16, v238\n" \
"v_mfma_f32_16x16x32_bf16 a[20:23], v[56:59], v[160:163], a[20:23]\n" \
"v_add_u32_e32 v251, 0x1c0, v212\n" \
"v_mul_u32_u24_e32 v247, 0x480, v246\n" \
"v_mfma_f32_16x16x32_bf16 a[20:23], v[52:55], v[156:159], a[20:23]\n" \
"v_and_b32_e32 v222, 0xff, v224\n" \
"v_cmp_gt_u32_e32 vcc, 16, v221\n" \
"v_mfma_f32_16x16x32_bf16 a[52:55], v[60:63], v[156:159], a[52:55]\n" \
"v_lshlrev_b32_e32 v232, 4, v231\n" \
"v_mul_u32_u24_e32 v236, 18, v241\n" \
"v_mfma_f32_16x16x32_bf16 a[52:55], v[64:67], v[160:163], a[52:55]\n" \
"v_mul_u32_u24_e32 v249, 0xe39, v251\n" \
"v_add_u32_e32 v2, 0xfffff800, v243\n" \
"v_mfma_f32_16x16x32_bf16 a[84:87], v[72:75], v[160:163], a[84:87]\n" \
"v_add_u32_e32 v216, v247, v214\n" \
"v_cndmask_b32_e32 v226, v223, v222, vcc\n" \
"v_mfma_f32_16x16x32_bf16 a[84:87], v[68:71], v[156:159], a[84:87]\n" \
"v_add_u32_e32 v234, s96, v232\n" \
"v_sub_u32_e32 v242, v240, v236\n" \
"v_mfma_f32_16x16x32_bf16 a[116:119], v[76:79], v[156:159], a[116:119]\n" \
"v_lshrrev_b32_e32 v213, 16, v249\n" \
"v_add_u32_e32 v224, 0x200, v212\n" \
"v_mfma_f32_16x16x32_bf16 a[116:119], v[80:83], v[160:163], a[116:119]\n" \
"v_mul_u32_u24_e32 v218, 0x480, v220\n" \
"s_waitcnt lgkmcnt(0)\n" \
"v_mfma_f32_16x16x32_bf16 a[24:27], v[56:59], v[168:171], a[24:27]\n" \
"v_and_b32_e32 v235, 0xff, v234\n" \
"v_cmp_gt_u32_e32 vcc, 16, v231\n" \
"v_mfma_f32_16x16x32_bf16 a[24:27], v[52:55], v[164:167], a[24:27]\n" \
"v_lshlrev_b32_e32 v243, 4, v242\n" \
"v_mul_u32_u24_e32 v247, 18, v213\n" \
"v_mfma_f32_16x16x32_bf16 a[56:59], v[60:63], v[164:167], a[56:59]\n" \
"v_mul_u32_u24_e32 v221, 0xe39, v224\n" \
"v_add_u32_e32 v3, 0xfffff400, v216\n" \
"v_mfma_f32_16x16x32_bf16 a[56:59], v[64:67], v[168:171], a[56:59]\n" \
"v_add_u32_e32 v4, v218, v226\n" \
"v_cndmask_b32_e32 v233, v232, v235, vcc\n" \
"v_mfma_f32_16x16x32_bf16 a[88:91], v[72:75], v[168:171], a[88:91]\n" \
"v_add_u32_e32 v245, s96, v243\n" \
"v_sub_u32_e32 v214, v251, v247\n" \
"v_mfma_f32_16x16x32_bf16 a[88:91], v[68:71], v[164:167], a[88:91]\n" \
"v_lshrrev_b32_e32 v222, 16, v221\n" \
"v_and_b32_e32 v246, 0xff, v245\n" \
"v_mfma_f32_16x16x32_bf16 a[120:123], v[76:79], v[164:167], a[120:123]\n" \
"v_cmp_gt_u32_e32 vcc, 16, v242\n" \
"v_lshlrev_b32_e32 v216, 4, v214\n" \
"v_mfma_f32_16x16x32_bf16 a[120:123], v[80:83], v[168:171], a[120:123]\n" \
"v_mul_u32_u24_e32 v218, 18, v222\n" \
"v_cndmask_b32_e32 v244, v243, v246, vcc\n" \
"v_mfma_f32_16x16x32_bf16 a[28:31], v[56:59], v[176:179], a[28:31]\n" \
"s_add_u32 s62, s22, 0x300\n" \
"s_and_b32 s62, s62, 0x3ff\n" \
"v_mfma_f32_16x16x32_bf16 a[28:31], v[52:55], v[172:175], a[28:31]\n" \
"s_lshl_b32 s63, s62, 12\n" \
"v_add_u32_e64 v217, s96, v216\n" \
"v_mfma_f32_16x16x32_bf16 a[60:63], v[60:63], v[172:175], a[60:63]\n" \
"v_sub_u32_e32 v226, v224, v218\n" \
"v_mul_u32_u24_e32 v228, 0x480, v230\n" \
"v_mfma_f32_16x16x32_bf16 a[60:63], v[64:67], v[176:179], a[60:63]\n" \
"v_and_b32_e32 v220, 0xff, v217\n" \
"v_cmp_gt_u32_e32 vcc, 16, v214\n" \
"v_mfma_f32_16x16x32_bf16 a[92:95], v[72:75], v[176:179], a[92:95]\n" \
"v_lshlrev_b32_e32 v227, 4, v226\n" \
"v_cndmask_b32_e32 v219, v216, v220, vcc\n" \
"v_mfma_f32_16x16x32_bf16 a[92:95], v[68:71], v[172:175], a[92:95]\n" \
"v_add_u32_e64 v230, s96, v227\n" \
"v_and_b32_e32 v229, 0xff, v230\n" \
"v_mfma_f32_16x16x32_bf16 a[124:127], v[76:79], v[172:175], a[124:127]\n" \
"v_cmp_gt_u32_e32 vcc, 16, v226\n" \
"v_mul_u32_u24_e32 v250, 0x480, v213\n" \
"v_mfma_f32_16x16x32_bf16 a[124:127], v[80:83], v[176:179], a[124:127]\n" \
"v_cndmask_b32_e32 v225, v227, v229, vcc\n" \
"v_and_b32_e32 v249, 2, v212\n" \
"s_waitcnt vmcnt(4)\n" \
"s_barrier\n" \
"v_mfma_f32_16x16x32_bf16 a[144:147], v[88:91], v[152:155], a[144:147]\n" \
"s_add_u32 m0, %[mA], 0x0\n" \
"s_add_u32 s85, s62, 0x0\n" \
"s_nop 0\n" \
"v_mfma_f32_16x16x32_bf16 a[144:147], v[84:87], v[148:151], a[144:147]\n" \
"buffer_load_dwordx4 v22, s[64:67], s85 offen lds\n" \
"v_mfma_f32_16x16x32_bf16 a[176:179], v[92:95], v[148:151], a[176:179]\n" \
"ds_read_b128 v[56:59], v29 offset:49152\n" \
"ds_read_b128 v[52:55], v28 offset:49152\n" \
"v_mfma_f32_16x16x32_bf16 a[176:179], v[96:99], v[152:155], a[176:179]\n" \
"s_add_u32 m0, %[mA], 0x400\n" \
"s_add_u32 s84, s62, 0x2000\n" \
"v_mfma_f32_16x16x32_bf16 a[208:211], v[104:107], v[152:155], a[208:211]\n" \
"buffer_load_dwordx4 v23, s[64:67], s84 offen lds\n" \
"v_mfma_f32_16x16x32_bf16 a[208:211], v[100:103], v[148:151], a[208:211]\n" \
"ds_read_b128 v[60:63], v28 offset:51200\n" \
"ds_read_b128 v[64:67], v29 offset:51200\n" \
"v_mfma_f32_16x16x32_bf16 a[240:243], v[108:111], v[148:151], a[240:243]\n" \
"ds_read_b64_tr_b16 v[148:149], v32 offset:49152\n" \
"ds_read_b64_tr_b16 v[150:151], v32 offset:50176\n" \
"v_mfma_f32_16x16x32_bf16 a[240:243], v[112:115], v[152:155], a[240:243]\n" \
"s_add_u32 m0, %[mB], 0x0\n" \
"s_add_u32 s85, s63, 0x0\n" \
"v_mfma_f32_16x16x32_bf16 a[148:151], v[88:91], v[160:163], a[148:151]\n" \
"buffer_load_dwordx4 v26, s[68:71], s85 offen lds\n" \
"v_mfma_f32_16x16x32_bf16 a[148:151], v[84:87], v[156:159], a[148:151]\n" \
"ds_read_b64_tr_b16 v[152:153], v32 offset:57344\n" \
"ds_read_b64_tr_b16 v[154:155], v32 offset:58368\n" \
"v_mfma_f32_16x16x32_bf16 a[180:183], v[92:95], v[156:159], a[180:183]\n" \
"ds_read_b128 v[72:75], v29 offset:53248\n" \
"ds_read_b128 v[68:71], v28 offset:53248\n" \
"v_mfma_f32_16x16x32_bf16 a[180:183], v[96:99], v[160:163], a[180:183]\n" \
"s_add_u32 m0, %[mB], 0x4000\n" \
"s_add_u32 s84, s63, 0x0\n" \
"s_nop 0\n" \
"v_mfma_f32_16x16x32_bf16 a[212:215], v[104:107], v[160:163], a[212:215]\n" \
"buffer_load_dwordx4 v26, s[72:75], s84 offen lds\n" \
"v_mfma_f32_16x16x32_bf16 a[212:215], v[100:103], v[156:159], a[212:215]\n" \
"ds_read_b128 v[76:79], v28 offset:55296\n" \
"ds_read_b128 v[80:83], v29 offset:55296\n" \
"v_mfma_f32_16x16x32_bf16 a[244:247], v[108:111], v[156:159], a[244:247]\n" \
"ds_read_b64_tr_b16 v[156:157], v33 offset:49152\n" \
"ds_read_b64_tr_b16 v[158:159], v33 offset:50176\n" \
"v_mfma_f32_16x16x32_bf16 a[244:247], v[112:115], v[160:163], a[244:247]\n" \
"s_add_u32 m0, %[mA], 0x800\n" \
"s_add_u32 s85, s62, 0x4000\n" \
"v_mfma_f32_16x16x32_bf16 a[152:155], v[88:91], v[168:171], a[152:155]\n" \
"buffer_load_dwordx4 v22, s[64:67], s85 offen lds\n" \
"v_mfma_f32_16x16x32_bf16 a[152:155], v[84:87], v[164:167], a[152:155]\n" \
"ds_read_b64_tr_b16 v[160:161], v33 offset:57344\n" \
"ds_read_b64_tr_b16 v[162:163], v33 offset:58368\n" \
"v_mfma_f32_16x16x32_bf16 a[184:187], v[92:95], v[164:167], a[184:187]\n" \
"v_mov_b32_e32 v213, 0x100\n" \
"v_mov_b32_e64 v251, s95\n" \
"v_mfma_f32_16x16x32_bf16 a[184:187], v[96:99], v[168:171], a[184:187]\n" \
"s_add_u32 m0, %[mA], 0xc00\n" \
"s_add_u32 s84, s62, 0x6000\n" \
"v_mfma_f32_16x16x32_bf16 a[216:219], v[104:107], v[168:171], a[216:219]\n" \
"buffer_load_dwordx4 v23, s[64:67], s84 offen lds\n" \
"v_mfma_f32_16x16x32_bf16 a[216:219], v[100:103], v[164:167], a[216:219]\n" \
"v_cmp_ne_u32_e32 vcc, 0, v249\n" \
"s_mov_b32 s76, s8\n" \
"v_mfma_f32_16x16x32_bf16 a[248:251], v[108:111], v[164:167], a[248:251]\n" \
"ds_read_b64_tr_b16 v[164:165], v34 offset:49152\n" \
"ds_read_b64_tr_b16 v[166:167], v34 offset:50176\n" \
"v_mfma_f32_16x16x32_bf16 a[248:251], v[112:115], v[168:171], a[248:251]\n" \
"s_add_u32 m0, %[mB], 0x400\n" \
"s_add_u32 s85, s63, 0x8000\n" \
"v_mfma_f32_16x16x32_bf16 a[156:159], v[88:91], v[176:179], a[156:159]\n" \
"buffer_load_dwordx4 v26, s[68:71], s85 offen lds\n" \
"v_mfma_f32_16x16x32_bf16 a[156:159], v[84:87], v[172:175], a[156:159]\n" \
"ds_read_b64_tr_b16 v[168:169], v34 offset:57344\n" \
"ds_read_b64_tr_b16 v[170:171], v34 offset:58368\n" \
"v_mfma_f32_16x16x32_bf16 a[188:191], v[92:95], v[172:175], a[188:191]\n" \
"v_cndmask_b32_e32 v247, v251, v213, vcc\n" \
"s_nop 0\n" \
"s_and_b32 s77, s9, 0xffff\n" \
"v_mfma_f32_16x16x32_bf16 a[188:191], v[96:99], v[176:179], a[188:191]\n" \
"s_add_u32 m0, %[mB], 0x4400\n" \
"s_add_u32 s84, s63, 0x8000\n" \
"v_mfma_f32_16x16x32_bf16 a[220:223], v[104:107], v[176:179], a[220:223]\n" \
"buffer_load_dwordx4 v26, s[72:75], s84 offen lds\n" \
"v_mfma_f32_16x16x32_bf16 a[220:223], v[100:103], v[172:175], a[220:223]\n" \
"s_mov_b32 s78, 0x240000\n" \
"v_xor_b32_e32 v39, 0x60, v190\n" \
"v_mfma_f32_16x16x32_bf16 a[252:255], v[108:111], v[172:175], a[252:255]\n" \
"ds_read_b64_tr_b16 v[172:173], v35 offset:49152\n" \
"ds_read_b64_tr_b16 v[174:175], v35 offset:50176\n" \
"v_mfma_f32_16x16x32_bf16 a[252:255], v[112:115], v[176:179], a[252:255]\n" \
"s_add_u32 m0, %[mA], 0x1000\n" \
"s_add_u32 s85, s62, 0x8000\n" \
"v_mfma_f32_16x16x32_bf16 a[128:131], v[120:123], v[88:91], a[128:131]\n" \
"buffer_load_dwordx4 v22, s[64:67], s85 offen lds\n" \
"v_mfma_f32_16x16x32_bf16 a[128:131], v[116:119], v[84:87], a[128:131]\n" \
"ds_read_b64_tr_b16 v[176:177], v35 offset:57344\n" \
"ds_read_b64_tr_b16 v[178:179], v35 offset:58368\n" \
"v_mfma_f32_16x16x32_bf16 a[160:163], v[116:119], v[92:95], a[160:163]\n" \
"v_xor_b32_e32 v37, 0x20, v190\n" \
"v_xor_b32_e32 v38, 0x40, v190\n" \
"v_mfma_f32_16x16x32_bf16 a[160:163], v[120:123], v[96:99], a[160:163]\n" \
"s_add_u32 m0, %[mA], 0x1400\n" \
"s_add_u32 s84, s62, 0xa000\n" \
"v_mfma_f32_16x16x32_bf16 a[192:195], v[120:123], v[104:107], a[192:195]\n" \
"buffer_load_dwordx4 v23, s[64:67], s84 offen lds\n" \
"v_mfma_f32_16x16x32_bf16 a[192:195], v[116:119], v[100:103], a[192:195]\n" \
"v_add_u32_e32 v48, s60, v28\n" \
"v_add_u32_e32 v49, s60, v29\n" \
"v_mfma_f32_16x16x32_bf16 a[224:227], v[116:119], v[108:111], a[224:227]\n" \
"ds_read_b64_tr_b16 v[116:117], v32 offset:32768\n" \
"ds_read_b64_tr_b16 v[118:119], v32 offset:33792\n" \
"v_mfma_f32_16x16x32_bf16 a[224:227], v[120:123], v[112:115], a[224:227]\n" \
"s_add_u32 m0, %[mB], 0x800\n" \
"s_add_u32 s85, s63, 0x10000\n" \
"v_mfma_f32_16x16x32_bf16 a[132:135], v[128:131], v[88:91], a[132:135]\n" \
"buffer_load_dwordx4 v27, s[68:71], s85 offen lds\n" \
"v_mfma_f32_16x16x32_bf16 a[132:135], v[124:127], v[84:87], a[132:135]\n" \
"ds_read_b64_tr_b16 v[120:121], v32 offset:40960\n" \
"ds_read_b64_tr_b16 v[122:123], v32 offset:41984\n" \
"v_mfma_f32_16x16x32_bf16 a[164:167], v[124:127], v[92:95], a[164:167]\n" \
"v_xor_b32_e32 v36, 0x0, v190\n" \
"v_add_u32_e32 v237, v228, v233\n" \
"v_mfma_f32_16x16x32_bf16 a[164:167], v[128:131], v[96:99], a[164:167]\n" \
"s_add_u32 m0, %[mB], 0x4800\n" \
"s_add_u32 s84, s63, 0x10000\n" \
"v_mfma_f32_16x16x32_bf16 a[196:199], v[128:131], v[104:107], a[196:199]\n" \
"buffer_load_dwordx4 v27, s[72:75], s84 offen lds\n" \
"v_mfma_f32_16x16x32_bf16 a[196:199], v[124:127], v[100:103], a[196:199]\n" \
"s_lshl_b32 s97, %[w], 4\n" \
"v_lshrrev_b32_e32 v234, 2, v212\n" \
"v_mfma_f32_16x16x32_bf16 a[228:231], v[124:127], v[108:111], a[228:231]\n" \
"ds_read_b64_tr_b16 v[124:125], v33 offset:32768\n" \
"ds_read_b64_tr_b16 v[126:127], v33 offset:33792\n" \
"v_mfma_f32_16x16x32_bf16 a[228:231], v[128:131], v[112:115], a[228:231]\n" \
"ds_read_b64_tr_b16 v[128:129], v33 offset:40960\n" \
"ds_read_b64_tr_b16 v[130:131], v33 offset:41984\n" \
"v_mfma_f32_16x16x32_bf16 a[136:139], v[136:139], v[88:91], a[136:139]\n" \
"v_add_u32_e32 v5, 0xfffffc00, v237\n" \
"v_add_u32_e64 v231, s97, v234\n" \
"v_mfma_f32_16x16x32_bf16 a[136:139], v[132:135], v[84:87], a[136:139]\n" \
"v_lshrrev_b32_e32 v235, 5, v231\n" \
"v_lshrrev_b32_e32 v237, 4, v231\n" \
"v_mfma_f32_16x16x32_bf16 a[168:171], v[132:135], v[92:95], a[168:171]\n" \
"v_mul_u32_u24_e32 v239, 0x480, v241\n" \
"v_lshlrev_b32_e32 v232, 4, v235\n" \
"v_mfma_f32_16x16x32_bf16 a[168:171], v[136:139], v[96:99], a[168:171]\n" \
"v_and_b32_e32 v228, 12, v231\n" \
"v_and_b32_e64 v238, 1, v237\n" \
"v_mfma_f32_16x16x32_bf16 a[200:203], v[136:139], v[104:107], a[200:203]\n" \
"v_add_u32_e32 v233, v232, v228\n" \
"v_lshlrev_b32_e32 v241, 1, v238\n" \
"v_mfma_f32_16x16x32_bf16 a[200:203], v[132:135], v[100:103], a[200:203]\n" \
"v_lshrrev_b32_e32 v236, 1, v231\n" \
"v_add_u32_e32 v240, v233, v241\n" \
"v_mfma_f32_16x16x32_bf16 a[232:235], v[132:135], v[108:111], a[232:235]\n" \
"ds_read_b64_tr_b16 v[132:133], v34 offset:32768\n" \
"ds_read_b64_tr_b16 v[134:135], v34 offset:33792\n" \
"v_mfma_f32_16x16x32_bf16 a[232:235], v[136:139], v[112:115], a[232:235]\n" \
"ds_read_b64_tr_b16 v[136:137], v34 offset:40960\n" \
"ds_read_b64_tr_b16 v[138:139], v34 offset:41984\n" \
"v_mfma_f32_16x16x32_bf16 a[140:143], v[144:147], v[88:91], a[140:143]\n" \
"ds_read_b128 v[88:91], v29 offset:32768\n" \
"v_and_b32_e64 v245, 1, v236\n" \
"v_mfma_f32_16x16x32_bf16 a[140:143], v[140:143], v[84:87], a[140:143]\n" \
"ds_read_b128 v[84:87], v28 offset:32768\n" \
"v_add_u32_e64 v242, v240, v245\n" \
"v_mfma_f32_16x16x32_bf16 a[172:175], v[140:143], v[92:95], a[172:175]\n" \
"ds_read_b128 v[92:95], v28 offset:34816\n" \
"v_add_u32_e64 v248, v239, v244\n" \
"v_mfma_f32_16x16x32_bf16 a[172:175], v[144:147], v[96:99], a[172:175]\n" \
"ds_read_b128 v[96:99], v29 offset:34816\n" \
"v_mul_u32_u24_e32 v246, 18, v242\n" \
"v_mfma_f32_16x16x32_bf16 a[204:207], v[144:147], v[104:107], a[204:207]\n" \
"ds_read_b128 v[104:107], v29 offset:36864\n" \
"v_add_u32_e32 v243, 16, v246\n" \
"v_mfma_f32_16x16x32_bf16 a[204:207], v[140:143], v[100:103], a[204:207]\n" \
"ds_read_b128 v[100:103], v28 offset:36864\n" \
"v_and_b32_e64 v239, 1, v231\n" \
"v_mfma_f32_16x16x32_bf16 a[236:239], v[140:143], v[108:111], a[236:239]\n" \
"ds_read_b64_tr_b16 v[140:141], v35 offset:32768\n" \
"ds_read_b64_tr_b16 v[142:143], v35 offset:33792\n" \
"v_mfma_f32_16x16x32_bf16 a[236:239], v[144:147], v[112:115], a[236:239]\n" \
"s_add_u32 m0, %[mA], 0x1800\n" \
"s_waitcnt lgkmcnt(12)\n" \
"s_nop 0\n" \
"v_mfma_f32_16x16x32_bf16 a[16:19], v[56:59], v[152:155], a[16:19]\n" \
"s_add_u32 s85, s62, 0xc000\n" \
"ds_read_b64_tr_b16 v[144:145], v35 offset:40960\n" \
"v_mfma_f32_16x16x32_bf16 a[16:19], v[52:55], v[148:151], a[16:19]\n" \
"buffer_load_dwordx4 v22, s[64:67], s85 offen lds\n" \
"v_mfma_f32_16x16x32_bf16 a[48:51], v[60:63], v[148:151], a[48:51]\n" \
"s_add_u32 m0, %[mA], 0x1c00\n" \
"s_add_u32 s84, s62, 0xe000\n" \
"v_mfma_f32_16x16x32_bf16 a[48:51], v[64:67], v[152:155], a[48:51]\n" \
"ds_read_b64_tr_b16 v[146:147], v35 offset:41984\n" \
"ds_read_b128 v[108:111], v28 offset:38912\n" \
"v_mfma_f32_16x16x32_bf16 a[80:83], v[72:75], v[152:155], a[80:83]\n" \
"buffer_load_dwordx4 v23, s[64:67], s84 offen lds\n" \
"v_mfma_f32_16x16x32_bf16 a[80:83], v[68:71], v[148:151], a[80:83]\n" \
"s_add_u32 m0, %[mB], 0xc00\n" \
"s_add_u32 s85, s63, 0x18000\n" \
"v_mfma_f32_16x16x32_bf16 a[112:115], v[76:79], v[148:151], a[112:115]\n" \
"ds_read_b128 v[112:115], v29 offset:38912\n" \
"v_add_u32_e32 v6, 0xfffff800, v248\n" \
"v_mfma_f32_16x16x32_bf16 a[112:115], v[80:83], v[152:155], a[112:115]\n" \
"buffer_load_dwordx4 v27, s[68:71], s85 offen lds\n" \
"v_mfma_f32_16x16x32_bf16 a[20:23], v[56:59], v[160:163], a[20:23]\n" \
"s_add_u32 m0, %[mB], 0x4c00\n" \
"s_add_u32 s84, s63, 0x18000\n" \
"v_mfma_f32_16x16x32_bf16 a[20:23], v[52:55], v[156:159], a[20:23]\n" \
"v_add_u32_e64 v244, v243, v239\n" \
"v_mul_u32_u24_e32 v248, 0x480, v244\n" \
"v_mfma_f32_16x16x32_bf16 a[52:55], v[60:63], v[156:159], a[52:55]\n" \
"buffer_load_dwordx4 v27, s[72:75], s84 offen lds\n" \
"v_mfma_f32_16x16x32_bf16 a[52:55], v[64:67], v[160:163], a[52:55]\n" \
"v_add_u32_e32 v215, v250, v219\n" \
"v_add_u32_e32 v217, v248, v247\n" \
"v_mfma_f32_16x16x32_bf16 a[84:87], v[72:75], v[160:163], a[84:87]\n" \
"v_and_b32_e64 v214, 16, v20\n" \
"v_add_u32_e32 v7, 0xfffff400, v215\n" \
"v_mfma_f32_16x16x32_bf16 a[84:87], v[68:71], v[156:159], a[84:87]\n" \
"v_add_u32_e32 v9, v217, v214\n" \
"v_and_b32_e32 v216, 3, v212\n" \
"v_mfma_f32_16x16x32_bf16 a[116:119], v[76:79], v[156:159], a[116:119]\n" \
"v_lshlrev_b32_e32 v250, 3, v216\n" \
"v_mul_u32_u24_e32 v220, 0x120, v234\n" \
"v_mfma_f32_16x16x32_bf16 a[116:119], v[80:83], v[160:163], a[116:119]\n" \
"s_mov_b32 m0, %[mS]\n" \
"s_mov_b32 s86, s8\n" \
"v_mfma_f32_16x16x32_bf16 a[24:27], v[56:59], v[168:171], a[24:27]\n" \
"s_mov_b32 s87, s9\n" \
"v_mul_u32_u24_e32 v223, 0x480, v222\n" \
"v_mfma_f32_16x16x32_bf16 a[24:27], v[52:55], v[164:167], a[24:27]\n" \
"v_add_u32_e32 v10, v220, v250\n" \
"v_add_u32_e32 v8, v223, v225\n" \
"v_mfma_f32_16x16x32_bf16 a[56:59], v[60:63], v[164:167], a[56:59]\n" \
"s_lshl_b32 s99, %[w], 11\n" \
"s_add_u32 s55, s98, s99\n" \
"v_mfma_f32_16x16x32_bf16 a[56:59], v[64:67], v[168:171], a[56:59]\n" \
"v_mfma_f32_16x16x32_bf16 a[88:91], v[72:75], v[168:171], a[88:91]\n" \
"v_mfma_f32_16x16x32_bf16 a[88:91], v[68:71], v[164:167], a[88:91]\n" \
"s_mov_b32 exec_lo, 0x0\n" \
"s_mov_b32 exec_hi, 0x3ffff0\n" \
"global_load_lds_dword v188, s[86:87]\n" \
"s_mov_b64 exec, -1\n" \
"v_mfma_f32_16x16x32_bf16 a[120:123], v[76:79], v[164:167], a[120:123]\n" \
"v_mfma_f32_16x16x32_bf16 a[120:123], v[80:83], v[168:171], a[120:123]\n" \
"v_mfma_f32_16x16x32_bf16 a[28:31], v[56:59], v[176:179], a[28:31]\n" \
"v_mfma_f32_16x16x32_bf16 a[28:31], v[52:55], v[172:175], a[28:31]\n" \
"v_mfma_f32_16x16x32_bf16 a[60:63], v[60:63], v[172:175], a[60:63]\n" \
"v_mfma_f32_16x16x32_bf16 a[60:63], v[64:67], v[176:179], a[60:63]\n" \
"v_mfma_f32_16x16x32_bf16 a[92:95], v[72:75], v[176:179], a[92:95]\n" \
"v_mfma_f32_16x16x32_bf16 a[92:95], v[68:71], v[172:175], a[92:95]\n" \
"v_mfma_f32_16x16x32_bf16 a[124:127], v[76:79], v[172:175], a[124:127]\n" \
"v_mfma_f32_16x16x32_bf16 a[124:127], v[80:83], v[176:179], a[124:127]\n" \
"v_mfma_f32_16x16x32_bf16 a[0:3], v[120:123], v[56:59], a[0:3]\n" \
"v_mfma_f32_16x16x32_bf16 a[0:3], v[116:119], v[52:55], a[0:3]\n" \
"v_mfma_f32_16x16x32_bf16 a[32:35], v[116:119], v[60:63], a[32:35]\n" \
"v_mfma_f32_16x16x32_bf16 a[32:35], v[120:123], v[64:67], a[32:35]\n" \
"v_mfma_f32_16x16x32_bf16 a[64:67], v[120:123], v[72:75], a[64:67]\n" \
"v_mfma_f32_16x16x32_bf16 a[64:67], v[116:119], v[68:71], a[64:67]\n" \
"v_mfma_f32_16x16x32_bf16 a[96:99], v[116:119], v[76:79], a[96:99]\n" \
"v_mfma_f32_16x16x32_bf16 a[96:99], v[120:123], v[80:83], a[96:99]\n" \
"v_mfma_f32_16x16x32_bf16 a[4:7], v[128:131], v[56:59], a[4:7]\n" \
"v_mfma_f32_16x16x32_bf16 a[4:7], v[124:127], v[52:55], a[4:7]\n" \
"v_mfma_f32_16x16x32_bf16 a[36:39], v[124:127], v[60:63], a[36:39]\n" \
"v_mfma_f32_16x16x32_bf16 a[36:39], v[128:131], v[64:67], a[36:39]\n" \
"v_mfma_f32_16x16x32_bf16 a[68:71], v[128:131], v[72:75], a[68:71]\n" \
"v_mfma_f32_16x16x32_bf16 a[68:71], v[124:127], v[68:71], a[68:71]\n" \
"v_mfma_f32_16x16x32_bf16 a[100:103], v[124:127], v[76:79], a[100:103]\n" \
"v_mfma_f32_16x16x32_bf16 a[100:103], v[128:131], v[80:83], a[100:103]\n" \
"s_waitcnt lgkmcnt(0)\n" \
"s_nop 0\n" \
"v_mfma_f32_16x16x32_bf16 a[8:11], v[136:139], v[56:59], a[8:11]\n" \
"v_mfma_f32_16x16x32_bf16 a[8:11], v[132:135], v[52:55], a[8:11]\n" \
"v_mfma_f32_16x16x32_bf16 a[40:43], v[132:135], v[60:63], a[40:43]\n" \
"v_mfma_f32_16x16x32_bf16 a[40:43], v[136:139], v[64:67], a[40:43]\n" \
"v_mfma_f32_16x16x32_bf16 a[72:75], v[136:139], v[72:75], a[72:75]\n" \
"v_mfma_f32_16x16x32_bf16 a[72:75], v[132:135], v[68:71], a[72:75]\n" \
"v_mfma_f32_16x16x32_bf16 a[104:107], v[132:135], v[76:79], a[104:107]\n" \
"v_mfma_f32_16x16x32_bf16 a[104:107], v[136:139], v[80:83], a[104:107]\n" \
"v_mfma_f32_16x16x32_bf16 a[12:15], v[144:147], v[56:59], a[12:15]\n" \
"s_add_u32 s62, s22, 0x380\n" \
"s_and_b32 s62, s62, 0x3ff\n" \
"v_mfma_f32_16x16x32_bf16 a[12:15], v[140:143], v[52:55], a[12:15]\n" \
"s_lshl_b32 s63, s62, 12\n" \
"v_mfma_f32_16x16x32_bf16 a[44:47], v[140:143], v[60:63], a[44:47]\n" \
"v_mfma_f32_16x16x32_bf16 a[44:47], v[144:147], v[64:67], a[44:47]\n" \
"v_mfma_f32_16x16x32_bf16 a[76:79], v[144:147], v[72:75], a[76:79]\n" \
"v_mfma_f32_16x16x32_bf16 a[76:79], v[140:143], v[68:71], a[76:79]\n" \
"v_mfma_f32_16x16x32_bf16 a[108:111], v[140:143], v[76:79], a[108:111]\n" \
"v_mfma_f32_16x16x32_bf16 a[108:111], v[144:147], v[80:83], a[108:111]\n" \
"s_waitcnt vmcnt(1)\n" \
"s_barrier\n" \
"s_mov_b64 exec, 1\n" \
"buffer_store_dword v45, off, s[80:83], s38 offset:64 sc1\n" \
"s_mov_b64 exec, -1\n" \
"v_mfma_f32_16x16x32_bf16 a[128:131], v[120:123], v[88:91], a[128:131]\n" \
"s_add_u32 m0, %[mA], 0x8000\n" \
"s_add_u32 s85, s62, 0x0\n" \
"v_mfma_f32_16x16x32_bf16 a[128:131], v[116:119], v[84:87], a[128:131]\n" \
"ds_read_b128 v[56:59], v29 offset:16384\n" \
"ds_read_b128 v[52:55], v28 offset:16384\n" \
"v_mfma_f32_16x16x32_bf16 a[160:163], v[116:119], v[92:95], a[160:163]\n" \
"buffer_load_dwordx4 v22, s[64:67], s85 offen lds\n" \
"v_mfma_f32_16x16x32_bf16 a[160:163], v[120:123], v[96:99], a[160:163]\n" \
"s_add_u32 m0, %[mA], 0x8400\n" \
"s_add_u32 s84, s62, 0x2000\n" \
"v_mfma_f32_16x16x32_bf16 a[192:195], v[120:123], v[104:107], a[192:195]\n" \
"ds_read_b128 v[60:63], v28 offset:18432\n" \
"ds_read_b128 v[64:67], v29 offset:18432\n" \
"v_mfma_f32_16x16x32_bf16 a[192:195], v[116:119], v[100:103], a[192:195]\n" \
"buffer_load_dwordx4 v23, s[64:67], s84 offen lds\n" \
"v_mfma_f32_16x16x32_bf16 a[224:227], v[116:119], v[108:111], a[224:227]\n" \
"ds_read_b64_tr_b16 v[116:117], v32 offset:0\n" \
"ds_read_b64_tr_b16 v[118:119], v32 offset:1024\n" \
"v_mfma_f32_16x16x32_bf16 a[224:227], v[120:123], v[112:115], a[224:227]\n" \
"s_add_u32 m0, %[mB], 0x8000\n" \
"s_add_u32 s85, s63, 0x0\n" \
"v_mfma_f32_16x16x32_bf16 a[132:135], v[128:131], v[88:91], a[132:135]\n" \
"buffer_load_dwordx4 v26, s[68:71], s85 offen lds\n" \
"v_mfma_f32_16x16x32_bf16 a[132:135], v[124:127], v[84:87], a[132:135]\n" \
"ds_read_b64_tr_b16 v[120:121], v32 offset:8192\n" \
"ds_read_b64_tr_b16 v[122:123], v32 offset:9216\n" \
"v_mfma_f32_16x16x32_bf16 a[164:167], v[124:127], v[92:95], a[164:167]\n" \
"ds_read_b128 v[72:75], v29 offset:20480\n" \
"ds_read_b128 v[68:71], v28 offset:20480\n" \
"v_mfma_f32_16x16x32_bf16 a[164:167], v[128:131], v[96:99], a[164:167]\n" \
"s_add_u32 m0, %[mB], 0xc000\n" \
"s_add_u32 s84, s63, 0x0\n" \
"v_mfma_f32_16x16x32_bf16 a[196:199], v[128:131], v[104:107], a[196:199]\n" \
"buffer_load_dwordx4 v26, s[72:75], s84 offen lds\n" \
"v_mfma_f32_16x16x32_bf16 a[196:199], v[124:127], v[100:103], a[196:199]\n" \
"ds_read_b128 v[76:79], v28 offset:22528\n" \
"ds_read_b128 v[80:83], v29 offset:22528\n" \
"v_mfma_f32_16x16x32_bf16 a[228:231], v[124:127], v[108:111], a[228:231]\n" \
"ds_read_b64_tr_b16 v[124:125], v33 offset:0\n" \
"ds_read_b64_tr_b16 v[126:127], v33 offset:1024\n" \
"v_mfma_f32_16x16x32_bf16 a[228:231], v[128:131], v[112:115], a[228:231]\n" \
"s_add_u32 m0, %[mA], 0x8800\n" \
"s_add_u32 s85, s62, 0x4000\n" \
"v_mfma_f32_16x16x32_bf16 a[136:139], v[136:139], v[88:91], a[136:139]\n" \
"buffer_load_dwordx4 v22, s[64:67], s85 offen lds\n" \
"v_mfma_f32_16x16x32_bf16 a[136:139], v[132:135], v[84:87], a[136:139]\n" \
"ds_read_b64_tr_b16 v[128:129], v33 offset:8192\n" \
"ds_read_b64_tr_b16 v[130:131], v33 offset:9216\n" \
"v_mfma_f32_16x16x32_bf16 a[168:171], v[132:135], v[92:95], a[168:171]\n" \
"v_mov_b32_e64 v45, %[tag]\n" \
"v_mfma_f32_16x16x32_bf16 a[168:171], v[136:139], v[96:99], a[168:171]\n" \
"s_add_u32 m0, %[mA], 0x8c00\n" \
"s_add_u32 s84, s62, 0x6000\n" \
"v_mfma_f32_16x16x32_bf16 a[200:203], v[136:139], v[104:107], a[200:203]\n" \
"buffer_load_dwordx4 v23, s[64:67], s84 offen lds\n" \
"v_mfma_f32_16x16x32_bf16 a[200:203], v[132:135], v[100:103], a[200:203]\n" \
"v_mfma_f32_16x16x32_bf16 a[232:235], v[132:135], v[108:111], a[232:235]\n" \
"ds_read_b64_tr_b16 v[132:133], v34 offset:0\n" \
"ds_read_b64_tr_b16 v[134:135], v34 offset:1024\n" \
"v_mfma_f32_16x16x32_bf16 a[232:235], v[136:139], v[112:115], a[232:235]\n" \
"s_add_u32 m0, %[mB], 0x8400\n" \
"s_add_u32 s85, s63, 0x8000\n" \
"v_mfma_f32_16x16x32_bf16 a[140:143], v[144:147], v[88:91], a[140:143]\n" \
"buffer_load_dwordx4 v26, s[68:71], s85 offen lds\n" \
"v_mfma_f32_16x16x32_bf16 a[140:143], v[140:143], v[84:87], a[140:143]\n" \
"ds_read_b64_tr_b16 v[136:137], v34 offset:8192\n" \
"ds_read_b64_tr_b16 v[138:139], v34 offset:9216\n" \
"v_mfma_f32_16x16x32_bf16 a[172:175], v[140:143], v[92:95], a[172:175]\n" \
"v_mfma_f32_16x16x32_bf16 a[172:175], v[144:147], v[96:99], a[172:175]\n" \
"s_add_u32 m0, %[mB], 0xc400\n" \
"s_add_u32 s84, s63, 0x8000\n" \
"v_mfma_f32_16x16x32_bf16 a[204:207], v[144:147], v[104:107], a[204:207]\n" \
"buffer_load_dwordx4 v26, s[72:75], s84 offen lds\n" \
"v_mfma_f32_16x16x32_bf16 a[204:207], v[140:143], v[100:103], a[204:207]\n" \
"v_mfma_f32_16x16x32_bf16 a[236:239], v[140:143], v[108:111], a[236:239]\n" \
"ds_read_b64_tr_b16 v[140:141], v35 offset:0\n" \
"ds_read_b64_tr_b16 v[142:143], v35 offset:1024\n" \
"v_mfma_f32_16x16x32_bf16 a[236:239], v[144:147], v[112:115], a[236:239]\n" \
"s_add_u32 m0, %[mA], 0x9000\n" \
"s_add_u32 s85, s62, 0x8000\n" \
"v_mfma_f32_16x16x32_bf16 a[144:147], v[88:91], v[152:155], a[144:147]\n" \
"buffer_load_dwordx4 v22, s[64:67], s85 offen lds\n" \
"v_mfma_f32_16x16x32_bf16 a[144:147], v[84:87], v[148:151], a[144:147]\n" \
"ds_read_b64_tr_b16 v[144:145], v35 offset:8192\n" \
"ds_read_b64_tr_b16 v[146:147], v35 offset:9216\n" \
"v_mfma_f32_16x16x32_bf16 a[176:179], v[92:95], v[148:151], a[176:179]\n" \
"v_mfma_f32_16x16x32_bf16 a[176:179], v[96:99], v[152:155], a[176:179]\n" \
"s_add_u32 m0, %[mA], 0x9400\n" \
"s_add_u32 s84, s62, 0xa000\n" \
"v_mfma_f32_16x16x32_bf16 a[208:211], v[104:107], v[152:155], a[208:211]\n" \
"buffer_load_dwordx4 v23, s[64:67], s84 offen lds\n" \
"v_mfma_f32_16x16x32_bf16 a[208:211], v[100:103], v[148:151], a[208:211]\n" \
"v_mfma_f32_16x16x32_bf16 a[240:243], v[108:111], v[148:151], a[240:243]\n" \
"ds_read_b64_tr_b16 v[148:149], v32 offset:16384\n" \
"ds_read_b64_tr_b16 v[150:151], v32 offset:17408\n" \
"v_mfma_f32_16x16x32_bf16 a[240:243], v[112:115], v[152:155], a[240:243]\n" \
"s_add_u32 m0, %[mB], 0x8800\n" \
"s_add_u32 s85, s63, 0x10000\n" \
"v_mfma_f32_16x16x32_bf16 a[148:151], v[88:91], v[160:163], a[148:151]\n" \
"buffer_load_dwordx4 v27, s[68:71], s85 offen lds\n" \
"v_mfma_f32_16x16x32_bf16 a[148:151], v[84:87], v[156:159], a[148:151]\n" \
"ds_read_b64_tr_b16 v[152:153], v32 offset:24576\n" \
"ds_read_b64_tr_b16 v[154:155], v32 offset:25600\n" \
"L_pf3:\n" \
"s_cbranch_execz L_pf7\n" \
"s_nop 0\n" \
"v_mfma_f32_16x16x32_bf16 a[180:183], v[92:95], v[156:159], a[180:183]\n" \
"v_mfma_f32_16x16x32_bf16 a[180:183], v[96:99], v[160:163], a[180:183]\n" \
"s_add_u32 m0, %[mB], 0xc800\n" \
"s_add_u32 s84, s63, 0x10000\n" \
"v_mfma_f32_16x16x32_bf16 a[212:215], v[104:107], v[160:163], a[212:215]\n" \
"buffer_load_dwordx4 v27, s[72:75], s84 offen lds\n" \
"v_mfma_f32_16x16x32_bf16 a[212:215], v[100:103], v[156:159], a[212:215]\n" \
"v_mfma_f32_16x16x32_bf16 a[244:247], v[108:111], v[156:159], a[244:247]\n" \
"ds_read_b64_tr_b16 v[156:157], v33 offset:16384\n" \
"ds_read_b64_tr_b16 v[158:159], v33 offset:17408\n" \
"v_mfma_f32_16x16x32_bf16 a[244:247], v[112:115], v[160:163], a[244:247]\n" \
"ds_read_b64_tr_b16 v[160:161], v33 offset:24576\n" \
"ds_read_b64_tr_b16 v[162:163], v33 offset:25600\n" \
"v_mfma_f32_16x16x32_bf16 a[152:155], v[88:91], v[168:171], a[152:155]\n" \
"v_mfma_f32_16x16x32_bf16 a[152:155], v[84:87], v[164:167], a[152:155]\n" \
"v_mfma_f32_16x16x32_bf16 a[184:187], v[92:95], v[164:167], a[184:187]\n" \
"v_mfma_f32_16x16x32_bf16 a[184:187], v[96:99], v[168:171], a[184:187]\n" \
"v_mfma_f32_16x16x32_bf16 a[216:219], v[104:107], v[168:171], a[216:219]\n" \
"v_mfma_f32_16x16x32_bf16 a[216:219], v[100:103], v[164:167], a[216:219]\n" \
"v_mfma_f32_16x16x32_bf16 a[248:251], v[108:111], v[164:167], a[248:251]\n" \
"ds_read_b64_tr_b16 v[164:165], v34 offset:16384\n" \
"ds_read_b64_tr_b16 v[166:167], v34 offset:17408\n" \
"v_mfma_f32_16x16x32_bf16 a[248:251], v[112:115], v[168:171], a[248:251]\n" \
"ds_read_b64_tr_b16 v[168:169], v34 offset:24576\n" \
"ds_read_b64_tr_b16 v[170:171], v34 offset:25600\n" \
"v_mfma_f32_16x16x32_bf16 a[156:159], v[88:91], v[176:179], a[156:159]\n" \
"ds_read_b128 v[88:91], v29 offset:0\n" \
"v_mfma_f32_16x16x32_bf16 a[156:159], v[84:87], v[172:175], a[156:159]\n" \
"ds_read_b128 v[84:87], v28 offset:0\n" \
"v_mfma_f32_16x16x32_bf16 a[188:191], v[92:95], v[172:175], a[188:191]\n" \
"ds_read_b128 v[92:95], v28 offset:2048\n" \
"v_mfma_f32_16x16x32_bf16 a[188:191], v[96:99], v[176:179], a[188:191]\n" \
"ds_read_b128 v[96:99], v29 offset:2048\n" \
"v_mfma_f32_16x16x32_bf16 a[220:223], v[104:107], v[176:179], a[220:223]\n" \
"ds_read_b128 v[104:107], v29 offset:4096\n" \
"v_mfma_f32_16x16x32_bf16 a[220:223], v[100:103], v[172:175], a[220:223]\n" \
"ds_read_b128 v[100:103], v28 offset:4096\n" \
"v_mfma_f32_16x16x32_bf16 a[252:255], v[108:111], v[172:175], a[252:255]\n" \
"ds_read_b64_tr_b16 v[172:173], v35 offset:16384\n" \
"ds_read_b64_tr_b16 v[174:175], v35 offset:17408\n" \
"v_mfma_f32_16x16x32_bf16 a[252:255], v[112:115], v[176:179], a[252:255]\n" \
"s_add_u32 m0, %[mA], 0x9800\n" \
"s_waitcnt lgkmcnt(12)\n" \
"s_nop 0\n" \
"v_mfma_f32_16x16x32_bf16 a[0:3], v[120:123], v[56:59], a[0:3]\n" \
"s_add_u32 s85, s62, 0xc000\n" \
"ds_read_b64_tr_b16 v[176:177], v35 offset:24576\n" \
"v_mfma_f32_16x16x32_bf16 a[0:3], v[116:119], v[52:55], a[0:3]\n" \
"buffer_load_dwordx4 v22, s[64:67], s85 offen lds\n" \
"v_mfma_f32_16x16x32_bf16 a[32:35], v[116:119], v[60:63], a[32:35]\n" \
"s_add_u32 m0, %[mA], 0x9c00\n" \
"s_add_u32 s84, s62, 0xe000\n" \
"v_mfma_f32_16x16x32_bf16 a[32:35], v[120:123], v[64:67], a[32:35]\n" \
"ds_read_b64_tr_b16 v[178:179], v35 offset:25600\n" \
"ds_read_b128 v[108:111], v28 offset:6144\n" \
"v_mfma_f32_16x16x32_bf16 a[64:67], v[120:123], v[72:75], a[64:67]\n" \
"buffer_load_dwordx4 v23, s[64:67], s84 offen lds\n" \
"v_mfma_f32_16x16x32_bf16 a[64:67], v[116:119], v[68:71], a[64:67]\n" \
"s_add_u32 m0, %[mB], 0x8c00\n" \
"s_add_u32 s85, s63, 0x18000\n" \
"v_mfma_f32_16x16x32_bf16 a[96:99], v[116:119], v[76:79], a[96:99]\n" \
"ds_read_b128 v[112:115], v29 offset:6144\n" \
"v_add_u32_e64 v23, s61, v29\n" \
"v_mfma_f32_16x16x32_bf16 a[96:99], v[120:123], v[80:83], a[96:99]\n" \
"buffer_load_dwordx4 v27, s[68:71], s85 offen lds\n" \
"v_mfma_f32_16x16x32_bf16 a[4:7], v[128:131], v[56:59], a[4:7]\n" \
"s_add_u32 m0, %[mB], 0xcc00\n" \
"s_add_u32 s84, s63, 0x18000\n" \
"v_mfma_f32_16x16x32_bf16 a[4:7], v[124:127], v[52:55], a[4:7]\n" \
"s_mov_b32 s68, s10\n" \
"s_and_b32 s69, s11, 0xffff\n" \
"v_mfma_f32_16x16x32_bf16 a[36:39], v[124:127], v[60:63], a[36:39]\n" \
"buffer_load_dwordx4 v27, s[72:75], s84 offen lds\n" \
"s_mov_b32 s73, s52\n" \
"s_add_u32 s74, s52, 0x4000\n" \
"s_add_u32 s75, s52, 0x8000\n" \
"v_mfma_f32_16x16x32_bf16 a[36:39], v[128:131], v[64:67], a[36:39]\n" \
"s_mov_b32 s70, 0x480000\n" \
"v_add_u32_e64 v22, s61, v28\n" \
"v_mfma_f32_16x16x32_bf16 a[68:71], v[128:131], v[72:75], a[68:71]\n" \
"v_bfe_u32 v44, v20, 2, 5\n" \
"s_mov_b32 s72, 0x3eae1a93\n" \
"v_mfma_f32_16x16x32_bf16 a[68:71], v[124:127], v[68:71], a[68:71]\n" \
"buffer_load_dword v44, v44, s[80:83], s39 offen offset:64 sc1\n" \
"v_mfma_f32_16x16x32_bf16 a[100:103], v[124:127], v[76:79], a[100:103]\n" \
"v_mfma_f32_16x16x32_bf16 a[100:103], v[128:131], v[80:83], a[100:103]\n" \
"s_mov_b32 m0, %[mS]\n" \
"s_mov_b32 s86, s18\n" \
"v_mfma_f32_16x16x32_bf16 a[8:11], v[136:139], v[56:59], a[8:11]\n" \
"s_mov_b32 s87, s19\n" \
"v_mfma_f32_16x16x32_bf16 a[8:11], v[132:135], v[52:55], a[8:11]\n" \
"v_mfma_f32_16x16x32_bf16 a[40:43], v[132:135], v[60:63], a[40:43]\n" \
"v_mfma_f32_16x16x32_bf16 a[40:43], v[136:139], v[64:67], a[40:43]\n" \
"v_mfma_f32_16x16x32_bf16 a[72:75], v[136:139], v[72:75], a[72:75]\n" \
"v_mfma_f32_16x16x32_bf16 a[72:75], v[132:135], v[68:71], a[72:75]\n" \
"s_mov_b32 exec_lo, 0xfffc0000\n" \
"s_mov_b32 exec_hi, 0xf\n" \
"global_load_lds_dword v16, s[86:87]\n" \
"s_mov_b64 exec, -1\n" \
"v_mfma_f32_16x16x32_bf16 a[104:107], v[132:135], v[76:79], a[104:107]\n" \
"s_mov_b32 m0, %[mS]\n" \
"s_mov_b32 s86, s16\n" \
"v_mfma_f32_16x16x32_bf16 a[104:107], v[136:139], v[80:83], a[104:107]\n" \
"s_mov_b32 s87, s17\n" \
"v_mfma_f32_16x16x32_bf16 a[12:15], v[144:147], v[56:59], a[12:15]\n" \
"v_mfma_f32_16x16x32_bf16 a[12:15], v[140:143], v[52:55], a[12:15]\n" \
"v_mfma_f32_16x16x32_bf16 a[44:47], v[140:143], v[60:63], a[44:47]\n" \
"s_mov_b32 exec_lo, 0x0\n" \
"s_mov_b32 exec_hi, 0x3f0\n" \
"global_load_lds_dword v16, s[86:87]\n" \
"s_mov_b64 exec, -1\n" \
"v_mfma_f32_16x16x32_bf16 a[44:47], v[144:147], v[64:67], a[44:47]\n" \
"v_mfma_f32_16x16x32_bf16 a[76:79], v[144:147], v[72:75], a[76:79]\n" \
"v_mfma_f32_16x16x32_bf16 a[76:79], v[140:143], v[68:71], a[76:79]\n" \
"v_mfma_f32_16x16x32_bf16 a[108:111], v[140:143], v[76:79], a[108:111]\n" \
"v_mfma_f32_16x16x32_bf16 a[108:111], v[144:147], v[80:83], a[108:111]\n" \
"v_mfma_f32_16x16x32_bf16 a[16:19], v[56:59], v[152:155], a[16:19]\n" \
"v_mfma_f32_16x16x32_bf16 a[16:19], v[52:55], v[148:151], a[16:19]\n" \
"v_mfma_f32_16x16x32_bf16 a[48:51], v[60:63], v[148:151], a[48:51]\n" \
"v_mfma_f32_16x16x32_bf16 a[48:51], v[64:67], v[152:155], a[48:51]\n" \
"v_mfma_f32_16x16x32_bf16 a[80:83], v[72:75], v[152:155], a[80:83]\n" \
"v_mfma_f32_16x16x32_bf16 a[80:83], v[68:71], v[148:151], a[80:83]\n" \
"v_mfma_f32_16x16x32_bf16 a[112:115], v[76:79], v[148:151], a[112:115]\n" \
"v_mfma_f32_16x16x32_bf16 a[112:115], v[80:83], v[152:155], a[112:115]\n" \
"v_mfma_f32_16x16x32_bf16 a[20:23], v[56:59], v[160:163], a[20:23]\n" \
"v_mfma_f32_16x16x32_bf16 a[20:23], v[52:55], v[156:159], a[20:23]\n" \
"v_mfma_f32_16x16x32_bf16 a[52:55], v[60:63], v[156:159], a[52:55]\n" \
"v_mfma_f32_16x16x32_bf16 a[52:55], v[64:67], v[160:163], a[52:55]\n" \
"v_mfma_f32_16x16x32_bf16 a[84:87], v[72:75], v[160:163], a[84:87]\n" \
"v_mfma_f32_16x16x32_bf16 a[84:87], v[68:71], v[156:159], a[84:87]\n" \
"v_mfma_f32_16x16x32_bf16 a[116:119], v[76:79], v[156:159], a[116:119]\n" \
"v_mfma_f32_16x16x32_bf16 a[116:119], v[80:83], v[160:163], a[116:119]\n" \
"s_waitcnt lgkmcnt(0)\n" \
"v_mfma_f32_16x16x32_bf16 a[24:27], v[56:59], v[168:171], a[24:27]\n" \
"v_mfma_f32_16x16x32_bf16 a[24:27], v[52:55], v[164:167], a[24:27]\n" \
"v_mfma_f32_16x16x32_bf16 a[56:59], v[60:63], v[164:167], a[56:59]\n" \
"v_mfma_f32_16x16x32_bf16 a[56:59], v[64:67], v[168:171], a[56:59]\n" \
"v_mfma_f32_16x16x32_bf16 a[88:91], v[72:75], v[168:171], a[88:91]\n" \
"v_mfma_f32_16x16x32_bf16 a[88:91], v[68:71], v[164:167], a[88:91]\n" \
"v_mfma_f32_16x16x32_bf16 a[120:123], v[76:79], v[164:167], a[120:123]\n" \
"v_mfma_f32_16x16x32_bf16 a[120:123], v[80:83], v[168:171], a[120:123]\n" \
"v_mfma_f32_16x16x32_bf16 a[28:31], v[56:59], v[176:179], a[28:31]\n" \
"s_waitcnt vmcnt(2)\n" \
"v_cmp_ne_u32_e32 vcc, %[tag], v44\n" \
"v_mfma_f32_16x16x32_bf16 a[28:31], v[52:55], v[172:175], a[28:31]\n" \
"v_mfma_f32_16x16x32_bf16 a[60:63], v[60:63], v[172:175], a[60:63]\n" \
"v_mfma_f32_16x16x32_bf16 a[60:63], v[64:67], v[176:179], a[60:63]\n" \
"v_mfma_f32_16x16x32_bf16 a[92:95], v[72:75], v[176:179], a[92:95]\n" \
"v_mfma_f32_16x16x32_bf16 a[92:95], v[68:71], v[172:175], a[92:95]\n" \
"s_cbranch_vccnz L_pl_%=\n" \
"L_rl_%=:\n" \
"v_mfma_f32_16x16x32_bf16 a[124:127], v[76:79], v[172:175], a[124:127]\n" \
"v_mfma_f32_16x16x32_bf16 a[124:127], v[80:83], v[176:179], a[124:127]\n" \
"s_barrier\n" \
"v_mfma_f32_16x16x32_bf16 a[144:147], v[88:91], v[152:155], a[144:147]\n" \
"s_add_u32 m0, s40, 0x0\n" \
"s_add_u32 s85, s37, 0x0\n" \
"s_nop 0\n" \
"v_mfma_f32_16x16x32_bf16 a[144:147], v[84:87], v[148:151], a[144:147]\n" \
"buffer_load_dwordx4 v20, s[80:83], s85 offen sc1 lds\n" \
"v_mfma_f32_16x16x32_bf16 a[176:179], v[92:95], v[148:151], a[176:179]\n" \
"ds_read_b128 v[56:59], v29 offset:49152\n" \
"ds_read_b128 v[52:55], v28 offset:49152\n" \
"v_mfma_f32_16x16x32_bf16 a[176:179], v[96:99], v[152:155], a[176:179]\n" \
"s_add_u32 m0, s40, 0x1000\n" \
"s_add_u32 s84, s37, 0x1000\n" \
"v_mfma_f32_16x16x32_bf16 a[208:211], v[104:107], v[152:155], a[208:211]\n" \
"buffer_load_dwordx4 v20, s[80:83], s84 offen sc1 lds\n" \
"v_mfma_f32_16x16x32_bf16 a[208:211], v[100:103], v[148:151], a[208:211]\n" \
"ds_read_b128 v[60:63], v28 offset:51200\n" \
"ds_read_b128 v[64:67], v29 offset:51200\n" \
"v_mfma_f32_16x16x32_bf16 a[240:243], v[108:111], v[148:151], a[240:243]\n" \
"ds_read_b64_tr_b16 v[148:149], v32 offset:49152\n" \
"ds_read_b64_tr_b16 v[150:151], v32 offset:50176\n" \
"v_mfma_f32_16x16x32_bf16 a[240:243], v[112:115], v[152:155], a[240:243]\n" \
"s_add_u32 m0, s40, 0x3000\n" \
"s_mov_b32 s85, 0x0\n" \
"v_mfma_f32_16x16x32_bf16 a[148:151], v[88:91], v[160:163], a[148:151]\n" \
"buffer_load_dwordx4 v21, s[76:79], s85 offen lds\n" \
"v_mfma_f32_16x16x32_bf16 a[148:151], v[84:87], v[156:159], a[148:151]\n" \
"ds_read_b64_tr_b16 v[152:153], v32 offset:57344\n" \
"ds_read_b64_tr_b16 v[154:155], v32 offset:58368\n" \
"v_mfma_f32_16x16x32_bf16 a[180:183], v[92:95], v[156:159], a[180:183]\n" \
"ds_read_b128 v[72:75], v29 offset:53248\n" \
"ds_read_b128 v[68:71], v28 offset:53248\n" \
"v_mfma_f32_16x16x32_bf16 a[180:183], v[96:99], v[160:163], a[180:183]\n" \
"s_add_u32 m0, %[mA], 0x0\n" \
"s_mov_b32 s84, 0x0\n" \
"s_nop 0\n" \
"v_mfma_f32_16x16x32_bf16 a[212:215], v[104:107], v[160:163], a[212:215]\n" \
"buffer_load_dwordx4 v24, s[76:79], s84 offen lds\n" \
"v_mfma_f32_16x16x32_bf16 a[212:215], v[100:103], v[156:159], a[212:215]\n" \
"ds_read_b128 v[76:79], v28 offset:55296\n" \
"ds_read_b128 v[80:83], v29 offset:55296\n" \
"v_mfma_f32_16x16x32_bf16 a[244:247], v[108:111], v[156:159], a[244:247]\n" \
"ds_read_b64_tr_b16 v[156:157], v33 offset:49152\n" \
"ds_read_b64_tr_b16 v[158:159], v33 offset:50176\n" \
"v_mfma_f32_16x16x32_bf16 a[244:247], v[112:115], v[160:163], a[244:247]\n" \
"s_add_u32 m0, %[mA], 0x400\n" \
"s_mov_b32 s85, 0x2400\n" \
"v_mfma_f32_16x16x32_bf16 a[152:155], v[88:91], v[168:171], a[152:155]\n" \
"buffer_load_dwordx4 v25, s[76:79], s85 offen lds\n" \
"v_mfma_f32_16x16x32_bf16 a[152:155], v[84:87], v[164:167], a[152:155]\n" \
"ds_read_b64_tr_b16 v[160:161], v33 offset:57344\n" \
"ds_read_b64_tr_b16 v[162:163], v33 offset:58368\n" \
"v_mfma_f32_16x16x32_bf16 a[184:187], v[92:95], v[164:167], a[184:187]\n" \
"v_mfma_f32_16x16x32_bf16 a[184:187], v[96:99], v[168:171], a[184:187]\n" \
"s_add_u32 m0, %[mB], 0x0\n" \
"s_mov_b32 s84, 0x0\n" \
"v_mfma_f32_16x16x32_bf16 a[216:219], v[104:107], v[168:171], a[216:219]\n" \
"buffer_load_dwordx4 v26, s[68:71], s84 offen lds\n" \
"v_mfma_f32_16x16x32_bf16 a[216:219], v[100:103], v[164:167], a[216:219]\n" \
"v_mfma_f32_16x16x32_bf16 a[248:251], v[108:111], v[164:167], a[248:251]\n" \
"ds_read_b64_tr_b16 v[164:165], v34 offset:49152\n" \
"ds_read_b64_tr_b16 v[166:167], v34 offset:50176\n" \
"v_mfma_f32_16x16x32_bf16 a[248:251], v[112:115], v[168:171], a[248:251]\n" \
"s_add_u32 m0, %[mA], 0x800\n" \
"s_mov_b32 s85, 0x4800\n" \
"v_mfma_f32_16x16x32_bf16 a[156:159], v[88:91], v[176:179], a[156:159]\n" \
"buffer_load_dwordx4 v24, s[76:79], s85 offen lds\n" \
"v_mfma_f32_16x16x32_bf16 a[156:159], v[84:87], v[172:175], a[156:159]\n" \
"ds_read_b64_tr_b16 v[168:169], v34 offset:57344\n" \
"ds_read_b64_tr_b16 v[170:171], v34 offset:58368\n" \
"v_mfma_f32_16x16x32_bf16 a[188:191], v[92:95], v[172:175], a[188:191]\n" \
"v_mfma_f32_16x16x32_bf16 a[188:191], v[96:99], v[176:179], a[188:191]\n" \
"s_add_u32 m0, %[mA], 0xc00\n" \
"s_mov_b32 s84, 0x6c00\n" \
"v_mfma_f32_16x16x32_bf16 a[220:223], v[104:107], v[176:179], a[220:223]\n" \
"buffer_load_dwordx4 v25, s[76:79], s84 offen lds\n" \
"v_mfma_f32_16x16x32_bf16 a[220:223], v[100:103], v[172:175], a[220:223]\n" \
"v_mfma_f32_16x16x32_bf16 a[252:255], v[108:111], v[172:175], a[252:255]\n" \
"ds_read_b64_tr_b16 v[172:173], v35 offset:49152\n" \
"ds_read_b64_tr_b16 v[174:175], v35 offset:50176\n" \
"v_mfma_f32_16x16x32_bf16 a[252:255], v[112:115], v[176:179], a[252:255]\n" \
"s_add_u32 m0, %[mB], 0x400\n" \
"s_mov_b32 s85, 0x8000\n" \
"v_mfma_f32_16x16x32_bf16 a[128:131], v[120:123], v[88:91], a[128:131]\n" \
"buffer_load_dwordx4 v26, s[68:71], s85 offen lds\n" \
"v_mfma_f32_16x16x32_bf16 a[128:131], v[116:119], v[84:87], a[128:131]\n" \
"ds_read_b64_tr_b16 v[176:177], v35 offset:57344\n" \
"ds_read_b64_tr_b16 v[178:179], v35 offset:58368\n" \
"v_mfma_f32_16x16x32_bf16 a[160:163], v[116:119], v[92:95], a[160:163]\n" \
"v_mfma_f32_16x16x32_bf16 a[160:163], v[120:123], v[96:99], a[160:163]\n" \
"s_add_u32 m0, %[mA], 0x1000\n" \
"s_mov_b32 s84, 0x9000\n" \
"v_mfma_f32_16x16x32_bf16 a[192:195], v[120:123], v[104:107], a[192:195]\n" \
"buffer_load_dwordx4 v24, s[76:79], s84 offen lds\n" \
"v_mfma_f32_16x16x32_bf16 a[192:195], v[116:119], v[100:103], a[192:195]\n" \
"v_mfma_f32_16x16x32_bf16 a[224:227], v[116:119], v[108:111], a[224:227]\n" \
"ds_read_b64_tr_b16 v[116:117], v32 offset:32768\n" \
"ds_read_b64_tr_b16 v[118:119], v32 offset:33792\n" \
"v_mfma_f32_16x16x32_bf16 a[224:227], v[120:123], v[112:115], a[224:227]\n" \
"s_add_u32 m0, %[mA], 0x1400\n" \
"s_mov_b32 s85, 0xb400\n" \
"v_mfma_f32_16x16x32_bf16 a[132:135], v[128:131], v[88:91], a[132:135]\n" \
"buffer_load_dwordx4 v25, s[76:79], s85 offen lds\n" \
"v_mfma_f32_16x16x32_bf16 a[132:135], v[124:127], v[84:87], a[132:135]\n" \
"ds_read_b64_tr_b16 v[120:121], v32 offset:40960\n" \
"ds_read_b64_tr_b16 v[122:123], v32 offset:41984\n" \
"v_mfma_f32_16x16x32_bf16 a[164:167], v[124:127], v[92:95], a[164:167]\n" \
"v_mfma_f32_16x16x32_bf16 a[164:167], v[128:131], v[96:99], a[164:167]\n" \
"s_add_u32 m0, %[mB], 0x800\n" \
"s_mov_b32 s84, 0x10000\n" \
"v_mfma_f32_16x16x32_bf16 a[196:199], v[128:131], v[104:107], a[196:199]\n" \
"buffer_load_dwordx4 v27, s[68:71], s84 offen lds\n" \
"v_mfma_f32_16x16x32_bf16 a[196:199], v[124:127], v[100:103], a[196:199]\n" \
"v_mfma_f32_16x16x32_bf16 a[228:231], v[124:127], v[108:111], a[228:231]\n" \
"ds_read_b64_tr_b16 v[124:125], v33 offset:32768\n" \
"ds_read_b64_tr_b16 v[126:127], v33 offset:33792\n" \
"v_mfma_f32_16x16x32_bf16 a[228:231], v[128:131], v[112:115], a[228:231]\n" \
"ds_read_b64_tr_b16 v[128:129], v33 offset:40960\n" \
"ds_read_b64_tr_b16 v[130:131], v33 offset:41984\n" \
"v_mfma_f32_16x16x32_bf16 a[136:139], v[136:139], v[88:91], a[136:139]\n" \
"v_mfma_f32_16x16x32_bf16 a[136:139], v[132:135], v[84:87], a[136:139]\n" \
"v_mfma_f32_16x16x32_bf16 a[168:171], v[132:135], v[92:95], a[168:171]\n" \
"v_mfma_f32_16x16x32_bf16 a[168:171], v[136:139], v[96:99], a[168:171]\n" \
"v_mfma_f32_16x16x32_bf16 a[200:203], v[136:139], v[104:107], a[200:203]\n" \
"v_mfma_f32_16x16x32_bf16 a[200:203], v[132:135], v[100:103], a[200:203]\n" \
"v_mfma_f32_16x16x32_bf16 a[232:235], v[132:135], v[108:111], a[232:235]\n" \
"ds_read_b64_tr_b16 v[132:133], v34 offset:32768\n" \
"ds_read_b64_tr_b16 v[134:135], v34 offset:33792\n" \
"v_mfma_f32_16x16x32_bf16 a[232:235], v[136:139], v[112:115], a[232:235]\n" \
"ds_read_b64_tr_b16 v[136:137], v34 offset:40960\n" \
"ds_read_b64_tr_b16 v[138:139], v34 offset:41984\n" \
"v_mfma_f32_16x16x32_bf16 a[140:143], v[144:147], v[88:91], a[140:143]\n" \
"ds_read_b128 v[88:91], v29 offset:32768\n" \
"v_mfma_f32_16x16x32_bf16 a[140:143], v[140:143], v[84:87], a[140:143]\n" \
"ds_read_b128 v[84:87], v28 offset:32768\n" \
"v_mfma_f32_16x16x32_bf16 a[172:175], v[140:143], v[92:95], a[172:175]\n" \
"ds_read_b128 v[92:95], v28 offset:34816\n" \
"v_mfma_f32_16x16x32_bf16 a[172:175], v[144:147], v[96:99], a[172:175]\n" \
"ds_read_b128 v[96:99], v29 offset:34816\n" \
"v_mfma_f32_16x16x32_bf16 a[204:207], v[144:147], v[104:107], a[204:207]\n" \
"ds_read_b128 v[104:107], v29 offset:36864\n" \
"v_mfma_f32_16x16x32_bf16 a[204:207], v[140:143], v[100:103], a[204:207]\n" \
"ds_read_b128 v[100:103], v28 offset:36864\n" \
"v_mfma_f32_16x16x32_bf16 a[236:239], v[140:143], v[108:111], a[236:239]\n" \
"ds_read_b64_tr_b16 v[140:141], v35 offset:32768\n" \
"ds_read_b64_tr_b16 v[142:143], v35 offset:33792\n" \
"v_mfma_f32_16x16x32_bf16 a[236:239], v[144:147], v[112:115], a[236:239]\n" \
"s_add_u32 m0, %[mA], 0x1800\n" \
"s_waitcnt lgkmcnt(12)\n" \
"s_nop 0\n" \
"v_mfma_f32_16x16x32_bf16 a[16:19], v[56:59], v[152:155], a[16:19]\n" \
"s_mov_b32 s84, 0xd800\n" \
"ds_read_b64_tr_b16 v[144:145], v35 offset:40960\n" \
"v_mfma_f32_16x16x32_bf16 a[16:19], v[52:55], v[148:151], a[16:19]\n" \
"buffer_load_dwordx4 v24, s[76:79], s84 offen lds\n" \
"v_mfma_f32_16x16x32_bf16 a[48:51], v[60:63], v[148:151], a[48:51]\n" \
"s_add_u32 m0, %[mA], 0x1c00\n" \
"s_mov_b32 s85, 0xfc00\n" \
"v_mfma_f32_16x16x32_bf16 a[48:51], v[64:67], v[152:155], a[48:51]\n" \
"ds_read_b64_tr_b16 v[146:147], v35 offset:41984\n" \
"ds_read_b128 v[108:111], v28 offset:38912\n" \
"v_mfma_f32_16x16x32_bf16 a[80:83], v[72:75], v[152:155], a[80:83]\n" \
"buffer_load_dwordx4 v25, s[76:79], s85 offen lds\n" \
"v_mfma_f32_16x16x32_bf16 a[80:83], v[68:71], v[148:151], a[80:83]\n" \
"s_add_u32 m0, %[mB], 0xc00\n" \
"s_mov_b32 s84, 0x18000\n" \
"v_mfma_f32_16x16x32_bf16 a[112:115], v[76:79], v[148:151], a[112:115]\n" \
"ds_read_b128 v[112:115], v29 offset:38912\n" \
"v_add_u32_e64 v28, s59, v28\n" \
"v_mfma_f32_16x16x32_bf16 a[112:115], v[80:83], v[152:155], a[112:115]\n" \
"buffer_load_dwordx4 v27, s[68:71], s84 offen lds\n" \
"v_mfma_f32_16x16x32_bf16 a[20:23], v[56:59], v[160:163], a[20:23]\n" \
"v_add_u32_e64 v29, s59, v29\n" \
"v_accvgpr_read_b32 v184, a16\n" \
"v_mfma_f32_16x16x32_bf16 a[20:23], v[52:55], v[156:159], a[20:23]\n" \
"v_accvgpr_read_b32 v185, a17\n" \
"v_accvgpr_read_b32 v186, a18\n" \
"v_mfma_f32_16x16x32_bf16 a[52:55], v[60:63], v[156:159], a[52:55]\n" \
"v_accvgpr_read_b32 v187, a19\n" \
"v_accvgpr_read_b32 v180, a48\n" \
"v_mfma_f32_16x16x32_bf16 a[52:55], v[64:67], v[160:163], a[52:55]\n" \
"v_accvgpr_read_b32 v181, a49\n" \
"v_accvgpr_read_b32 v182, a50\n" \
"v_mfma_f32_16x16x32_bf16 a[84:87], v[72:75], v[160:163], a[84:87]\n" \
"v_accvgpr_read_b32 v183, a51\n" \
"v_accvgpr_read_b32 v188, a80\n" \
"v_mfma_f32_16x16x32_bf16 a[84:87], v[68:71], v[156:159], a[84:87]\n" \
"v_accvgpr_read_b32 v189, a81\n" \
"v_accvgpr_read_b32 v190, a82\n" \
"v_mfma_f32_16x16x32_bf16 a[116:119], v[76:79], v[156:159], a[116:119]\n" \
"v_accvgpr_read_b32 v191, a83\n" \
"v_cvt_pk_bf16_f32 v184, v184, v185\n" \
"v_mfma_f32_16x16x32_bf16 a[116:119], v[80:83], v[160:163], a[116:119]\n" \
"v_cvt_pk_bf16_f32 v185, v186, v187\n" \
"v_cvt_pk_bf16_f32 v186, v180, v181\n" \
"v_mfma_f32_16x16x32_bf16 a[24:27], v[56:59], v[168:171], a[24:27]\n" \
"v_cvt_pk_bf16_f32 v187, v182, v183\n" \
"v_accvgpr_read_b32 v180, a112\n" \
"v_mfma_f32_16x16x32_bf16 a[24:27], v[52:55], v[164:167], a[24:27]\n" \
"v_accvgpr_read_b32 v181, a113\n" \
"v_accvgpr_read_b32 v182, a114\n" \
"v_mfma_f32_16x16x32_bf16 a[56:59], v[60:63], v[164:167], a[56:59]\n" \
"v_accvgpr_read_b32 v183, a115\n" \
"v_accvgpr_read_b32 v192, a20\n" \
"v_mfma_f32_16x16x32_bf16 a[56:59], v[64:67], v[168:171], a[56:59]\n" \
"v_accvgpr_read_b32 v193, a21\n" \
"v_accvgpr_read_b32 v194, a22\n" \
"v_mfma_f32_16x16x32_bf16 a[88:91], v[72:75], v[168:171], a[88:91]\n" \
"v_accvgpr_read_b32 v195, a23\n" \
"v_cvt_pk_bf16_f32 v188, v188, v189\n" \
"v_mfma_f32_16x16x32_bf16 a[88:91], v[68:71], v[164:167], a[88:91]\n" \
"v_cvt_pk_bf16_f32 v189, v190, v191\n" \
"v_cvt_pk_bf16_f32 v190, v180, v181\n" \
"v_mfma_f32_16x16x32_bf16 a[120:123], v[76:79], v[164:167], a[120:123]\n" \
"v_cvt_pk_bf16_f32 v191, v182, v183\n" \
"v_accvgpr_read_b32 v180, a52\n" \
"v_mfma_f32_16x16x32_bf16 a[120:123], v[80:83], v[168:171], a[120:123]\n" \
"v_accvgpr_read_b32 v181, a53\n" \
"v_accvgpr_read_b32 v182, a54\n" \
"v_mfma_f32_16x16x32_bf16 a[28:31], v[56:59], v[176:179], a[28:31]\n" \
"v_accvgpr_read_b32 v183, a55\n" \
"v_accvgpr_read_b32 v196, a84\n" \
"v_mfma_f32_16x16x32_bf16 a[28:31], v[52:55], v[172:175], a[28:31]\n" \
"v_accvgpr_read_b32 v197, a85\n" \
"v_accvgpr_read_b32 v198, a86\n" \
"v_mfma_f32_16x16x32_bf16 a[60:63], v[60:63], v[172:175], a[60:63]\n" \
"v_accvgpr_read_b32 v199, a87\n" \
"v_cvt_pk_bf16_f32 v192, v192, v193\n" \
"v_mfma_f32_16x16x32_bf16 a[60:63], v[64:67], v[176:179], a[60:63]\n" \
"v_cvt_pk_bf16_f32 v193, v194, v195\n" \
"v_cvt_pk_bf16_f32 v194, v180, v181\n" \
"v_mfma_f32_16x16x32_bf16 a[92:95], v[72:75], v[176:179], a[92:95]\n" \
"v_cvt_pk_bf16_f32 v195, v182, v183\n" \
"v_accvgpr_read_b32 v180, a116\n" \
"v_mfma_f32_16x16x32_bf16 a[92:95], v[68:71], v[172:175], a[92:95]\n" \
"v_accvgpr_read_b32 v181, a117\n" \
"v_accvgpr_read_b32 v182, a118\n" \
"v_mfma_f32_16x16x32_bf16 a[124:127], v[76:79], v[172:175], a[124:127]\n" \
"v_accvgpr_read_b32 v183, a119\n" \
"v_accvgpr_read_b32 v200, a24\n" \
"v_mfma_f32_16x16x32_bf16 a[124:127], v[80:83], v[176:179], a[124:127]\n" \
"v_accvgpr_read_b32 v201, a25\n" \
"v_accvgpr_read_b32 v202, a26\n" \
"v_mfma_f32_16x16x32_bf16 a[0:3], v[120:123], v[56:59], a[0:3]\n" \
"v_accvgpr_read_b32 v203, a27\n" \
"v_cvt_pk_bf16_f32 v196, v196, v197\n" \
"v_mfma_f32_16x16x32_bf16 a[0:3], v[116:119], v[52:55], a[0:3]\n" \
"v_cvt_pk_bf16_f32 v197, v198, v199\n" \
"v_cvt_pk_bf16_f32 v198, v180, v181\n" \
"v_mfma_f32_16x16x32_bf16 a[32:35], v[116:119], v[60:63], a[32:35]\n" \
"v_cvt_pk_bf16_f32 v199, v182, v183\n" \
"v_accvgpr_read_b32 v180, a56\n" \
"v_mfma_f32_16x16x32_bf16 a[32:35], v[120:123], v[64:67], a[32:35]\n" \
"v_accvgpr_read_b32 v181, a57\n" \
"v_accvgpr_read_b32 v182, a58\n" \
"v_mfma_f32_16x16x32_bf16 a[64:67], v[120:123], v[72:75], a[64:67]\n" \
"v_accvgpr_read_b32 v183, a59\n" \
"v_accvgpr_read_b32 v204, a88\n" \
"v_mfma_f32_16x16x32_bf16 a[64:67], v[116:119], v[68:71], a[64:67]\n" \
"v_accvgpr_read_b32 v205, a89\n" \
"v_accvgpr_read_b32 v206, a90\n" \
"v_mfma_f32_16x16x32_bf16 a[96:99], v[116:119], v[76:79], a[96:99]\n" \
"v_accvgpr_read_b32 v207, a91\n" \
"v_cvt_pk_bf16_f32 v200, v200, v201\n" \
"v_mfma_f32_16x16x32_bf16 a[96:99], v[120:123], v[80:83], a[96:99]\n" \
"v_cvt_pk_bf16_f32 v201, v202, v203\n" \
"v_cvt_pk_bf16_f32 v202, v180, v181\n" \
"v_mfma_f32_16x16x32_bf16 a[4:7], v[128:131], v[56:59], a[4:7]\n" \
"v_cvt_pk_bf16_f32 v203, v182, v183\n" \
"v_accvgpr_read_b32 v180, a120\n" \
"v_mfma_f32_16x16x32_bf16 a[4:7], v[124:127], v[52:55], a[4:7]\n" \
"v_accvgpr_read_b32 v181, a121\n" \
"v_accvgpr_read_b32 v182, a122\n" \
"v_mfma_f32_16x16x32_bf16 a[36:39], v[124:127], v[60:63], a[36:39]\n" \
"v_accvgpr_read_b32 v183, a123\n" \
"v_accvgpr_read_b32 v208, a28\n" \
"v_mfma_f32_16x16x32_bf16 a[36:39], v[128:131], v[64:67], a[36:39]\n" \
"v_accvgpr_read_b32 v209, a29\n" \
"v_accvgpr_read_b32 v210, a30\n" \
"v_mfma_f32_16x16x32_bf16 a[68:71], v[128:131], v[72:75], a[68:71]\n" \
"v_accvgpr_read_b32 v211, a31\n" \
"v_cvt_pk_bf16_f32 v204, v204, v205\n" \
"v_mfma_f32_16x16x32_bf16 a[68:71], v[124:127], v[68:71], a[68:71]\n" \
"v_cvt_pk_bf16_f32 v205, v206, v207\n" \
"v_cvt_pk_bf16_f32 v206, v180, v181\n" \
"v_mfma_f32_16x16x32_bf16 a[100:103], v[124:127], v[76:79], a[100:103]\n" \
"v_cvt_pk_bf16_f32 v207, v182, v183\n" \
"v_accvgpr_read_b32 v180, a60\n" \
"v_mfma_f32_16x16x32_bf16 a[100:103], v[128:131], v[80:83], a[100:103]\n" \
"v_accvgpr_read_b32 v181, a61\n" \
"s_waitcnt lgkmcnt(0)\n" \
"s_nop 0\n" \
"v_mfma_f32_16x16x32_bf16 a[8:11], v[136:139], v[56:59], a[8:11]\n" \
"v_accvgpr_read_b32 v182, a62\n" \
"v_accvgpr_read_b32 v183, a63\n" \
"v_mfma_f32_16x16x32_bf16 a[8:11], v[132:135], v[52:55], a[8:11]\n" \
"v_accvgpr_read_b32 v212, a92\n" \
"v_accvgpr_read_b32 v213, a93\n" \
"v_mfma_f32_16x16x32_bf16 a[40:43], v[132:135], v[60:63], a[40:43]\n" \
"v_accvgpr_read_b32 v214, a94\n" \
"v_accvgpr_read_b32 v215, a95\n" \
"v_mfma_f32_16x16x32_bf16 a[40:43], v[136:139], v[64:67], a[40:43]\n" \
"v_cvt_pk_bf16_f32 v208, v208, v209\n" \
"v_cvt_pk_bf16_f32 v209, v210, v211\n" \
"v_mfma_f32_16x16x32_bf16 a[72:75], v[136:139], v[72:75], a[72:75]\n" \
"v_cvt_pk_bf16_f32 v210, v180, v181\n" \
"v_cvt_pk_bf16_f32 v211, v182, v183\n" \
"v_mfma_f32_16x16x32_bf16 a[72:75], v[132:135], v[68:71], a[72:75]\n" \
"v_accvgpr_read_b32 v180, a124\n" \
"v_accvgpr_read_b32 v181, a125\n" \
"v_mfma_f32_16x16x32_bf16 a[104:107], v[132:135], v[76:79], a[104:107]\n" \
"v_accvgpr_read_b32 v182, a126\n" \
"v_accvgpr_read_b32 v183, a127\n" \
"v_mfma_f32_16x16x32_bf16 a[104:107], v[136:139], v[80:83], a[104:107]\n" \
"v_accvgpr_read_b32 v216, a0\n" \
"v_accvgpr_read_b32 v217, a1\n" \
"v_mfma_f32_16x16x32_bf16 a[12:15], v[144:147], v[56:59], a[12:15]\n" \
"v_accvgpr_read_b32 v218, a2\n" \
"v_accvgpr_read_b32 v219, a3\n" \
"v_mfma_f32_16x16x32_bf16 a[12:15], v[140:143], v[52:55], a[12:15]\n" \
"v_cvt_pk_bf16_f32 v212, v212, v213\n" \
"v_cvt_pk_bf16_f32 v213, v214, v215\n" \
"v_mfma_f32_16x16x32_bf16 a[44:47], v[140:143], v[60:63], a[44:47]\n" \
"v_cvt_pk_bf16_f32 v214, v180, v181\n" \
"v_cvt_pk_bf16_f32 v215, v182, v183\n" \
"v_mfma_f32_16x16x32_bf16 a[44:47], v[144:147], v[64:67], a[44:47]\n" \
"v_accvgpr_read_b32 v180, a4\n" \
"v_accvgpr_read_b32 v181, a5\n" \
"v_mfma_f32_16x16x32_bf16 a[76:79], v[144:147], v[72:75], a[76:79]\n" \
"v_accvgpr_read_b32 v182, a6\n" \
"v_accvgpr_read_b32 v183, a7\n" \
"v_mfma_f32_16x16x32_bf16 a[76:79], v[140:143], v[68:71], a[76:79]\n" \
"v_cvt_pk_bf16_f32 v216, v216, v217\n" \
"v_cvt_pk_bf16_f32 v217, v218, v219\n" \
"v_mfma_f32_16x16x32_bf16 a[108:111], v[140:143], v[76:79], a[108:111]\n" \
"v_accvgpr_read_b32 v220, a32\n" \
"v_accvgpr_read_b32 v221, a33\n" \
"v_mfma_f32_16x16x32_bf16 a[108:111], v[144:147], v[80:83], a[108:111]\n" \
"v_cvt_pk_bf16_f32 v218, v180, v181\n" \
"v_cvt_pk_bf16_f32 v219, v182, v183\n" \
"s_waitcnt vmcnt(0)\n" \
"s_barrier\n" \
"v_mfma_f32_16x16x32_bf16 a[128:131], v[120:123], v[88:91], a[128:131]\n" \
"s_add_u32 m0, s40, 0x8000\n" \
"s_add_u32 s85, s37, 0x2000\n" \
"v_mfma_f32_16x16x32_bf16 a[128:131], v[116:119], v[84:87], a[128:131]\n" \
"buffer_load_dwordx4 v20, s[80:83], s85 offen sc1 lds\n" \
"v_mfma_f32_16x16x32_bf16 a[160:163], v[116:119], v[92:95], a[160:163]\n" \
"ds_read_b128 v[52:55], v28 offset:0\n" \
"ds_read_b128 v[56:59], v29 offset:0\n" \
"v_mfma_f32_16x16x32_bf16 a[160:163], v[120:123], v[96:99], a[160:163]\n" \
"s_add_u32 m0, s40, 0x9000\n" \
"s_add_u32 s84, s37, 0x3000\n" \
"v_mfma_f32_16x16x32_bf16 a[192:195], v[120:123], v[104:107], a[192:195]\n" \
"buffer_load_dwordx4 v20, s[80:83], s84 offen sc1 lds\n" \
"v_mfma_f32_16x16x32_bf16 a[192:195], v[116:119], v[100:103], a[192:195]\n" \
"ds_read_b128 v[60:63], v28 offset:2048\n" \
"ds_read_b128 v[64:67], v29 offset:2048\n" \
"v_mfma_f32_16x16x32_bf16 a[224:227], v[116:119], v[108:111], a[224:227]\n" \
"ds_read_b64_tr_b16 v[116:117], v36 offset:0\n" \
"ds_read_b64_tr_b16 v[118:119], v36 offset:512\n" \
"v_mfma_f32_16x16x32_bf16 a[224:227], v[120:123], v[112:115], a[224:227]\n" \
"s_add_u32 m0, s40, 0xb000\n" \
"s_mov_b32 s85, 0x80\n" \
"v_mfma_f32_16x16x32_bf16 a[132:135], v[128:131], v[88:91], a[132:135]\n" \
"buffer_load_dwordx4 v21, s[76:79], s85 offen lds\n" \
"v_mfma_f32_16x16x32_bf16 a[132:135], v[124:127], v[84:87], a[132:135]\n" \
"ds_read_b64_tr_b16 v[120:121], v36 offset:4096\n" \
"ds_read_b64_tr_b16 v[122:123], v36 offset:4608\n" \
"v_mfma_f32_16x16x32_bf16 a[164:167], v[124:127], v[92:95], a[164:167]\n" \
"ds_read_b128 v[68:71], v28 offset:4096\n" \
"ds_read_b128 v[72:75], v29 offset:4096\n" \
"v_mfma_f32_16x16x32_bf16 a[164:167], v[128:131], v[96:99], a[164:167]\n" \
"s_add_u32 m0, %[mA], 0x8000\n" \
"s_mov_b32 s84, 0x80\n" \
"v_mfma_f32_16x16x32_bf16 a[196:199], v[128:131], v[104:107], a[196:199]\n" \
"L_pf4:\n" \
"s_cbranch_execz L_pf8\n" \
"s_nop 0\n" \
"buffer_load_dwordx4 v24, s[76:79], s84 offen lds\n" \
"v_mfma_f32_16x16x32_bf16 a[196:199], v[124:127], v[100:103], a[196:199]\n" \
"ds_read_b128 v[76:79], v28 offset:6144\n" \
"ds_read_b128 v[80:83], v29 offset:6144\n" \
"v_mfma_f32_16x16x32_bf16 a[228:231], v[124:127], v[108:111], a[228:231]\n" \
"ds_read_b64_tr_b16 v[124:125], v37 offset:0\n" \
"ds_read_b64_tr_b16 v[126:127], v37 offset:512\n" \
"v_mfma_f32_16x16x32_bf16 a[228:231], v[128:131], v[112:115], a[228:231]\n" \
"s_add_u32 m0, %[mA], 0x8400\n" \
"s_mov_b32 s85, 0x2480\n" \
"v_mfma_f32_16x16x32_bf16 a[136:139], v[136:139], v[88:91], a[136:139]\n" \
"buffer_load_dwordx4 v25, s[76:79], s85 offen lds\n" \
"v_mfma_f32_16x16x32_bf16 a[136:139], v[132:135], v[84:87], a[136:139]\n" \
"ds_read_b64_tr_b16 v[128:129], v37 offset:4096\n" \
"ds_read_b64_tr_b16 v[130:131], v37 offset:4608\n" \
"v_mfma_f32_16x16x32_bf16 a[168:171], v[132:135], v[92:95], a[168:171]\n" \
"v_accvgpr_read_b32 v222, a34\n" \
"v_accvgpr_read_b32 v223, a35\n" \
"v_mfma_f32_16x16x32_bf16 a[168:171], v[136:139], v[96:99], a[168:171]\n" \
"s_add_u32 m0, %[mB], 0x8000\n" \
"s_mov_b32 s84, 0x80000\n" \
"v_mfma_f32_16x16x32_bf16 a[200:203], v[136:139], v[104:107], a[200:203]\n" \
"buffer_load_dwordx4 v26, s[68:71], s84 offen lds\n" \
"v_mfma_f32_16x16x32_bf16 a[200:203], v[132:135], v[100:103], a[200:203]\n" \
"v_cvt_pk_bf16_f32 v220, v220, v221\n" \
"v_accvgpr_read_b32 v180, a36\n" \
"v_mfma_f32_16x16x32_bf16 a[232:235], v[132:135], v[108:111], a[232:235]\n" \
"ds_read_b64_tr_b16 v[132:133], v38 offset:0\n" \
"ds_read_b64_tr_b16 v[134:135], v38 offset:512\n" \
"v_mfma_f32_16x16x32_bf16 a[232:235], v[136:139], v[112:115], a[232:235]\n" \
"s_add_u32 m0, %[mA], 0x8800\n" \
"s_mov_b32 s85, 0x4880\n" \
"v_mfma_f32_16x16x32_bf16 a[140:143], v[144:147], v[88:91], a[140:143]\n" \
"buffer_load_dwordx4 v24, s[76:79], s85 offen lds\n" \
"v_mfma_f32_16x16x32_bf16 a[140:143], v[140:143], v[84:87], a[140:143]\n" \
"ds_read_b64_tr_b16 v[136:137], v38 offset:4096\n" \
"ds_read_b64_tr_b16 v[138:139], v38 offset:4608\n" \
"v_mfma_f32_16x16x32_bf16 a[172:175], v[140:143], v[92:95], a[172:175]\n" \
"v_accvgpr_read_b32 v181, a37\n" \
"v_accvgpr_read_b32 v182, a38\n" \
"v_mfma_f32_16x16x32_bf16 a[172:175], v[144:147], v[96:99], a[172:175]\n" \
"s_add_u32 m0, %[mA], 0x8c00\n" \
"s_mov_b32 s84, 0x6c80\n" \
"v_mfma_f32_16x16x32_bf16 a[204:207], v[144:147], v[104:107], a[204:207]\n" \
"buffer_load_dwordx4 v25, s[76:79], s84 offen lds\n" \
"v_mfma_f32_16x16x32_bf16 a[204:207], v[140:143], v[100:103], a[204:207]\n" \
"v_accvgpr_read_b32 v183, a39\n" \
"v_cvt_pk_bf16_f32 v221, v222, v223\n" \
"v_mfma_f32_16x16x32_bf16 a[236:239], v[140:143], v[108:111], a[236:239]\n" \
"ds_read_b64_tr_b16 v[140:141], v39 offset:0\n" \
"ds_read_b64_tr_b16 v[142:143], v39 offset:512\n" \
"v_mfma_f32_16x16x32_bf16 a[236:239], v[144:147], v[112:115], a[236:239]\n" \
"s_add_u32 m0, %[mB], 0x8400\n" \
"s_mov_b32 s85, 0x88000\n" \
"v_mfma_f32_16x16x32_bf16 a[144:147], v[88:91], v[152:155], a[144:147]\n" \
"buffer_load_dwordx4 v26, s[68:71], s85 offen lds\n" \
"v_mfma_f32_16x16x32_bf16 a[144:147], v[84:87], v[148:151], a[144:147]\n" \
"ds_read_b64_tr_b16 v[144:145], v39 offset:4096\n" \
"ds_read_b64_tr_b16 v[146:147], v39 offset:4608\n" \
"v_mfma_f32_16x16x32_bf16 a[148:151], v[84:87], v[156:159], a[148:151]\n" \
"v_accvgpr_read_b32 v224, a64\n" \
"v_accvgpr_read_b32 v225, a65\n" \
"v_mfma_f32_16x16x32_bf16 a[148:151], v[88:91], v[160:163], a[148:151]\n" \
"s_add_u32 m0, %[mA], 0x9000\n" \
"s_mov_b32 s84, 0x9080\n" \
"v_mfma_f32_16x16x32_bf16 a[152:155], v[88:91], v[168:171], a[152:155]\n" \
"buffer_load_dwordx4 v24, s[76:79], s84 offen lds\n" \
"v_mfma_f32_16x16x32_bf16 a[152:155], v[84:87], v[164:167], a[152:155]\n" \
"v_cvt_pk_bf16_f32 v222, v180, v181\n" \
"v_cvt_pk_bf16_f32 v223, v182, v183\n" \
"v_mfma_f32_16x16x32_bf16 a[156:159], v[84:87], v[172:175], a[156:159]\n" \
"ds_read_b128 v[84:87], v22 offset:0\n" \
"v_accvgpr_read_b32 v226, a66\n" \
"v_mfma_f32_16x16x32_bf16 a[156:159], v[88:91], v[176:179], a[156:159]\n" \
"s_add_u32 m0, %[mA], 0x9400\n" \
"s_mov_b32 s85, 0xb480\n" \
"v_mfma_f32_16x16x32_bf16 a[176:179], v[96:99], v[152:155], a[176:179]\n" \
"buffer_load_dwordx4 v25, s[76:79], s85 offen lds\n" \
"v_mfma_f32_16x16x32_bf16 a[176:179], v[92:95], v[148:151], a[176:179]\n" \
"ds_read_b128 v[88:91], v23 offset:0\n" \
"v_accvgpr_read_b32 v227, a67\n" \
"v_mfma_f32_16x16x32_bf16 a[180:183], v[92:95], v[156:159], a[180:183]\n" \
"v_cvt_pk_bf16_f32 v224, v224, v225\n" \
"v_accvgpr_read_b32 v180, a68\n" \
"v_mfma_f32_16x16x32_bf16 a[180:183], v[96:99], v[160:163], a[180:183]\n" \
"s_add_u32 m0, %[mB], 0x8800\n" \
"s_mov_b32 s84, 0x90000\n" \
"v_mfma_f32_16x16x32_bf16 a[184:187], v[96:99], v[168:171], a[184:187]\n" \
"buffer_load_dwordx4 v27, s[68:71], s84 offen lds\n" \
"v_mfma_f32_16x16x32_bf16 a[184:187], v[92:95], v[164:167], a[184:187]\n" \
"v_accvgpr_read_b32 v181, a69\n" \
"v_accvgpr_read_b32 v182, a70\n" \
"v_mfma_f32_16x16x32_bf16 a[188:191], v[92:95], v[172:175], a[188:191]\n" \
"v_accvgpr_read_b32 v183, a71\n" \
"v_cvt_pk_bf16_f32 v225, v226, v227\n" \
"v_mfma_f32_16x16x32_bf16 a[188:191], v[96:99], v[176:179], a[188:191]\n" \
"v_accvgpr_read_b32 v228, a96\n" \
"v_accvgpr_read_b32 v229, a97\n" \
"v_mfma_f32_16x16x32_bf16 a[208:211], v[104:107], v[152:155], a[208:211]\n" \
"v_cvt_pk_bf16_f32 v226, v180, v181\n" \
"v_cvt_pk_bf16_f32 v227, v182, v183\n" \
"v_mfma_f32_16x16x32_bf16 a[208:211], v[100:103], v[148:151], a[208:211]\n" \
"v_accvgpr_read_b32 v230, a98\n" \
"v_accvgpr_read_b32 v231, a99\n" \
"v_mfma_f32_16x16x32_bf16 a[212:215], v[100:103], v[156:159], a[212:215]\n" \
"v_cvt_pk_bf16_f32 v228, v228, v229\n" \
"v_accvgpr_read_b32 v180, a100\n" \
"v_mfma_f32_16x16x32_bf16 a[212:215], v[104:107], v[160:163], a[212:215]\n" \
"v_accvgpr_read_b32 v181, a101\n" \
"v_accvgpr_read_b32 v182, a102\n" \
"v_mfma_f32_16x16x32_bf16 a[216:219], v[104:107], v[168:171], a[216:219]\n" \
"v_accvgpr_read_b32 v183, a103\n" \
"v_cvt_pk_bf16_f32 v229, v230, v231\n" \
"v_mfma_f32_16x16x32_bf16 a[216:219], v[100:103], v[164:167], a[216:219]\n" \
"v_accvgpr_read_b32 v232, a8\n" \
"v_accvgpr_read_b32 v233, a9\n" \
"v_mfma_f32_16x16x32_bf16 a[220:223], v[100:103], v[172:175], a[220:223]\n" \
"v_cvt_pk_bf16_f32 v230, v180, v181\n" \
"v_cvt_pk_bf16_f32 v231, v182, v183\n" \
"v_mfma_f32_16x16x32_bf16 a[220:223], v[104:107], v[176:179], a[220:223]\n" \
"v_accvgpr_read_b32 v234, a10\n" \
"v_accvgpr_read_b32 v235, a11\n" \
"v_mfma_f32_16x16x32_bf16 a[240:243], v[112:115], v[152:155], a[240:243]\n" \
"ds_read_b64_tr_b16 v[152:153], v32 offset:8192\n" \
"ds_read_b64_tr_b16 v[154:155], v32 offset:9216\n" \
"v_mfma_f32_16x16x32_bf16 a[240:243], v[108:111], v[148:151], a[240:243]\n" \
"ds_read_b64_tr_b16 v[148:149], v32 offset:0\n" \
"ds_read_b64_tr_b16 v[150:151], v32 offset:1024\n" \
"v_mfma_f32_16x16x32_bf16 a[244:247], v[108:111], v[156:159], a[244:247]\n" \
"ds_read_b64_tr_b16 v[156:157], v33 offset:0\n" \
"ds_read_b64_tr_b16 v[158:159], v33 offset:1024\n" \
"v_mfma_f32_16x16x32_bf16 a[244:247], v[112:115], v[160:163], a[244:247]\n" \
"ds_read_b64_tr_b16 v[160:161], v33 offset:8192\n" \
"ds_read_b64_tr_b16 v[162:163], v33 offset:9216\n" \
"v_mfma_f32_16x16x32_bf16 a[248:251], v[112:115], v[168:171], a[248:251]\n" \
"ds_read_b64_tr_b16 v[168:169], v34 offset:8192\n" \
"ds_read_b64_tr_b16 v[170:171], v34 offset:9216\n" \
"v_mfma_f32_16x16x32_bf16 a[248:251], v[108:111], v[164:167], a[248:251]\n" \
"ds_read_b64_tr_b16 v[164:165], v34 offset:0\n" \
"ds_read_b64_tr_b16 v[166:167], v34 offset:1024\n" \
"v_mfma_f32_16x16x32_bf16 a[252:255], v[108:111], v[172:175], a[252:255]\n" \
"ds_read_b64_tr_b16 v[172:173], v35 offset:0\n" \
"ds_read_b64_tr_b16 v[174:175], v35 offset:1024\n" \
"v_mfma_f32_16x16x32_bf16 a[252:255], v[112:115], v[176:179], a[252:255]\n" \
"s_add_u32 m0, %[mA], 0x9800\n" \
"s_waitcnt lgkmcnt(14)\n" \
"s_nop 0\n" \
"v_mfma_f32_16x16x32_bf16 a[16:19], v[144:147], v[88:91], 0\n" \
"s_mov_b32 s84, 0xd880\n" \
"ds_read_b64_tr_b16 v[176:177], v35 offset:8192\n" \
"v_mfma_f32_16x16x32_bf16 a[16:19], v[140:143], v[84:87], a[16:19]\n" \
"buffer_load_dwordx4 v24, s[76:79], s84 offen lds\n" \
"v_mfma_f32_16x16x32_bf16 a[48:51], v[124:127], v[84:87], 0\n" \
"s_add_u32 m0, %[mA], 0x9c00\n" \
"s_mov_b32 s85, 0xfc80\n" \
"v_mfma_f32_16x16x32_bf16 a[48:51], v[128:131], v[88:91], a[48:51]\n" \
"ds_read_b64_tr_b16 v[178:179], v35 offset:9216\n" \
"v_cvt_pk_bf16_f32 v232, v232, v233\n" \
"v_mfma_f32_16x16x32_bf16 a[80:83], v[88:91], v[136:139], 0\n" \
"buffer_load_dwordx4 v25, s[76:79], s85 offen lds\n" \
"v_mfma_f32_16x16x32_bf16 a[80:83], v[84:87], v[132:135], a[80:83]\n" \
"s_add_u32 m0, %[mB], 0x8c00\n" \
"s_mov_b32 s84, 0x98000\n" \
"v_mfma_f32_16x16x32_bf16 a[112:115], v[140:143], v[52:55], 0\n" \
"v_accvgpr_read_b32 v180, a12\n" \
"v_accvgpr_read_b32 v181, a13\n" \
"v_mfma_f32_16x16x32_bf16 a[112:115], v[144:147], v[56:59], a[112:115]\n" \
"buffer_load_dwordx4 v27, s[68:71], s84 offen lds\n" \
"v_mfma_f32_16x16x32_bf16 a[20:23], v[128:131], v[56:59], 0\n" \
"v_accvgpr_read_b32 v182, a14\n" \
"v_accvgpr_read_b32 v183, a15\n" \
"v_mfma_f32_16x16x32_bf16 a[20:23], v[124:127], v[52:55], a[20:23]\n" \
"s_add_u32 s88, s44, 0x0\n" \
"s_nop 0\n" \
"v_cvt_pk_bf16_f32 v233, v234, v235\n" \
"v_mfma_f32_16x16x32_bf16 a[52:55], v[116:119], v[52:55], 0\n" \
"buffer_store_dwordx4 v[184:187], v20, s[80:83], s88 offen offset:0 sc1\n" \
"v_mfma_f32_16x16x32_bf16 a[52:55], v[120:123], v[56:59], a[52:55]\n" \
"v_accvgpr_read_b32 v236, a40\n" \
"v_accvgpr_read_b32 v237, a41\n" \
"v_mfma_f32_16x16x32_bf16 a[84:87], v[56:59], v[136:139], 0\n" \
"v_cvt_pk_bf16_f32 v234, v180, v181\n" \
"v_cvt_pk_bf16_f32 v235, v182, v183\n" \
"v_mfma_f32_16x16x32_bf16 a[84:87], v[52:55], v[132:135], a[84:87]\n" \
"v_accvgpr_read_b32 v238, a42\n" \
"v_accvgpr_read_b32 v239, a43\n" \
"v_mfma_f32_16x16x32_bf16 a[116:119], v[140:143], v[60:63], 0\n" \
"v_cvt_pk_bf16_f32 v236, v236, v237\n" \
"v_accvgpr_read_b32 v180, a44\n" \
"v_mfma_f32_16x16x32_bf16 a[116:119], v[144:147], v[64:67], a[116:119]\n" \
"v_accvgpr_read_b32 v181, a45\n" \
"v_accvgpr_read_b32 v182, a46\n" \
"v_mfma_f32_16x16x32_bf16 a[24:27], v[128:131], v[64:67], 0\n" \
"v_accvgpr_read_b32 v183, a47\n" \
"v_cvt_pk_bf16_f32 v237, v238, v239\n" \
"v_mfma_f32_16x16x32_bf16 a[24:27], v[124:127], v[60:63], a[24:27]\n" \
"v_accvgpr_read_b32 v240, a72\n" \
"v_accvgpr_read_b32 v241, a73\n" \
"v_mfma_f32_16x16x32_bf16 a[56:59], v[116:119], v[60:63], 0\n" \
"v_cvt_pk_bf16_f32 v238, v180, v181\n" \
"v_cvt_pk_bf16_f32 v239, v182, v183\n" \
"v_mfma_f32_16x16x32_bf16 a[56:59], v[120:123], v[64:67], a[56:59]\n" \
"s_add_u32 s89, s44, 0x1000\n" \
"v_accvgpr_read_b32 v242, a74\n" \
"v_mfma_f32_16x16x32_bf16 a[88:91], v[64:67], v[136:139], 0\n" \
"buffer_store_dwordx4 v[188:191], v20, s[80:83], s89 offen offset:0 sc1\n" \
"v_mfma_f32_16x16x32_bf16 a[88:91], v[60:63], v[132:135], a[88:91]\n" \
"v_accvgpr_read_b32 v243, a75\n" \
"v_cvt_pk_bf16_f32 v240, v240, v241\n" \
"v_mfma_f32_16x16x32_bf16 a[120:123], v[140:143], v[68:71], 0\n" \
"v_accvgpr_read_b32 v180, a76\n" \
"v_accvgpr_read_b32 v181, a77\n" \
"v_mfma_f32_16x16x32_bf16 a[120:123], v[144:147], v[72:75], a[120:123]\n" \
"v_accvgpr_read_b32 v182, a78\n" \
"v_accvgpr_read_b32 v183, a79\n" \
"v_mfma_f32_16x16x32_bf16 a[28:31], v[128:131], v[72:75], 0\n" \
"v_cvt_pk_bf16_f32 v241, v242, v243\n" \
"v_accvgpr_read_b32 v244, a104\n" \
"v_mfma_f32_16x16x32_bf16 a[28:31], v[124:127], v[68:71], a[28:31]\n" \
"v_accvgpr_read_b32 v245, a105\n" \
"v_cvt_pk_bf16_f32 v242, v180, v181\n" \
"v_mfma_f32_16x16x32_bf16 a[60:63], v[116:119], v[68:71], 0\n" \
"v_cvt_pk_bf16_f32 v243, v182, v183\n" \
"v_accvgpr_read_b32 v246, a106\n" \
"v_mfma_f32_16x16x32_bf16 a[60:63], v[120:123], v[72:75], a[60:63]\n" \
"v_accvgpr_read_b32 v247, a107\n" \
"v_cvt_pk_bf16_f32 v244, v244, v245\n" \
"v_mfma_f32_16x16x32_bf16 a[92:95], v[72:75], v[136:139], 0\n" \
"v_accvgpr_read_b32 v248, a128\n" \
"v_accvgpr_read_b32 v249, a129\n" \
"v_mfma_f32_16x16x32_bf16 a[92:95], v[68:71], v[132:135], a[92:95]\n" \
"v_accvgpr_read_b32 v250, a130\n" \
"v_accvgpr_read_b32 v251, a131\n" \
"v_mfma_f32_16x16x32_bf16 a[124:127], v[140:143], v[76:79], 0\n" \
"v_accvgpr_read_b32 v180, a108\n" \
"v_accvgpr_read_b32 v181, a109\n" \
"v_mfma_f32_16x16x32_bf16 a[124:127], v[144:147], v[80:83], a[124:127]\n" \
"v_accvgpr_read_b32 v182, a110\n" \
"v_accvgpr_read_b32 v183, a111\n" \
"v_mfma_f32_16x16x32_bf16 a[0:3], v[128:131], v[80:83], 0\n" \
"v_cvt_pk_bf16_f32 v245, v246, v247\n" \
"v_cvt_pk_bf16_f32 v246, v180, v181\n" \
"v_mfma_f32_16x16x32_bf16 a[0:3], v[124:127], v[76:79], a[0:3]\n" \
"v_cvt_pk_bf16_f32 v247, v182, v183\n" \
"v_cvt_pk_bf16_f32 v248, v248, v249\n" \
"v_mfma_f32_16x16x32_bf16 a[4:7], v[116:119], v[76:79], 0\n" \
"v_accvgpr_read_b32 v180, a132\n" \
"v_accvgpr_read_b32 v181, a133\n" \
"v_mfma_f32_16x16x32_bf16 a[4:7], v[120:123], v[80:83], a[4:7]\n" \
"v_accvgpr_read_b32 v182, a134\n" \
"v_accvgpr_read_b32 v183, a135\n" \
"v_mfma_f32_16x16x32_bf16 a[32:35], v[80:83], v[136:139], 0\n" \
"v_cvt_pk_bf16_f32 v249, v250, v251\n" \
"v_accvgpr_read_b32 v252, a160\n" \
"v_mfma_f32_16x16x32_bf16 a[32:35], v[76:79], v[132:135], a[32:35]\n" \
"v_accvgpr_read_b32 v253, a161\n" \
"s_waitcnt lgkmcnt(0)\n" \
"s_nop 0\n" \
"v_mfma_f32_16x16x32_bf16 a[128:131], v[148:151], v[52:55], 0\n" \
"v_cvt_pk_bf16_f32 v250, v180, v181\n" \
"v_cvt_pk_bf16_f32 v251, v182, v183\n" \
"v_mfma_f32_16x16x32_bf16 a[128:131], v[152:155], v[56:59], a[128:131]\n" \
"v_accvgpr_read_b32 v254, a162\n" \
"v_accvgpr_read_b32 v255, a163\n" \
"v_cvt_pk_bf16_f32 v252, v252, v253\n" \
"v_accvgpr_read_b32 v180, a164\n" \
"v_accvgpr_read_b32 v181, a165\n" \
"v_accvgpr_read_b32 v182, a166\n" \
"v_accvgpr_read_b32 v183, a167\n" \
"v_cvt_pk_bf16_f32 v253, v254, v255\n" \
"v_cvt_pk_bf16_f32 v254, v180, v181\n" \
"v_cvt_pk_bf16_f32 v255, v182, v183\n" \
"v_accvgpr_write_b32 a36, v252\n" \
"v_accvgpr_write_b32 a37, v253\n" \
"v_mfma_f32_16x16x32_bf16 a[132:135], v[160:163], v[56:59], 0\n" \
"v_accvgpr_write_b32 a38, v254\n" \
"v_accvgpr_write_b32 a39, v255\n" \
"v_accvgpr_read_b32 v252, a192\n" \
"v_accvgpr_read_b32 v253, a193\n" \
"v_accvgpr_read_b32 v254, a194\n" \
"v_accvgpr_read_b32 v255, a195\n" \
"v_cvt_pk_bf16_f32 v252, v252, v253\n" \
"s_add_u32 s88, s44, 0x0\n" \
"s_nop 0\n" \
"v_accvgpr_read_b32 v180, a196\n" \
"v_accvgpr_read_b32 v181, a197\n" \
"v_accvgpr_read_b32 v182, a198\n" \
"v_accvgpr_read_b32 v183, a199\n" \
"v_cvt_pk_bf16_f32 v253, v254, v255\n" \
"v_cvt_pk_bf16_f32 v254, v180, v181\n" \
"v_cvt_pk_bf16_f32 v255, v182, v183\n" \
"v_accvgpr_write_b32 a64, v252\n" \
"v_accvgpr_write_b32 a65, v253\n" \
"v_mfma_f32_16x16x32_bf16 a[132:135], v[156:159], v[52:55], a[132:135]\n" \
"buffer_store_dwordx4 v[192:195], v20, s[80:83], s88 offen offset:1024 sc1\n" \
"v_accvgpr_write_b32 a66, v254\n" \
"v_accvgpr_write_b32 a67, v255\n" \
"v_accvgpr_read_b32 v252, a224\n" \
"v_accvgpr_read_b32 v253, a225\n" \
"v_accvgpr_read_b32 v254, a226\n" \
"v_accvgpr_read_b32 v255, a227\n" \
"v_cvt_pk_bf16_f32 v252, v252, v253\n" \
"v_accvgpr_read_b32 v180, a228\n" \
"v_accvgpr_read_b32 v181, a229\n" \
"v_accvgpr_read_b32 v182, a230\n" \
"v_accvgpr_read_b32 v183, a231\n" \
"v_cvt_pk_bf16_f32 v253, v254, v255\n" \
"v_cvt_pk_bf16_f32 v254, v180, v181\n" \
"v_cvt_pk_bf16_f32 v255, v182, v183\n" \
"v_accvgpr_write_b32 a68, v252\n" \
"v_accvgpr_write_b32 a69, v253\n" \
"v_accvgpr_write_b32 a70, v254\n" \
"v_accvgpr_write_b32 a71, v255\n" \
"v_accvgpr_read_b32 v252, a136\n" \
"v_accvgpr_read_b32 v253, a137\n" \
"v_accvgpr_read_b32 v254, a138\n" \
"v_accvgpr_read_b32 v255, a139\n" \
"v_mfma_f32_16x16x32_bf16 a[136:139], v[164:167], v[52:55], 0\n" \
"ds_read_b128 v[84:87], v48 offset:0\n" \
"ds_read_b128 v[88:91], v49 offset:0\n" \
"ds_read_b128 v[92:95], v48 offset:2048\n" \
"v_mfma_f32_16x16x32_bf16 a[136:139], v[168:171], v[56:59], a[136:139]\n" \
"ds_read_b128 v[96:99], v49 offset:2048\n" \
"v_accvgpr_read_b32 v180, a140\n" \
"v_accvgpr_read_b32 v181, a141\n" \
"v_accvgpr_read_b32 v182, a142\n" \
"v_accvgpr_read_b32 v183, a143\n" \
"v_mfma_f32_16x16x32_bf16 a[140:143], v[176:179], v[56:59], 0\n" \
"ds_read_b128 v[100:103], v48 offset:4096\n" \
"ds_read_b128 v[104:107], v49 offset:4096\n" \
"v_mfma_f32_16x16x32_bf16 a[140:143], v[172:175], v[52:55], a[140:143]\n" \
"v_cvt_pk_bf16_f32 v252, v252, v253\n" \
"v_cvt_pk_bf16_f32 v253, v254, v255\n" \
"v_mfma_f32_16x16x32_bf16 a[160:163], v[148:151], v[60:63], 0\n" \
"ds_read_b128 v[108:111], v48 offset:6144\n" \
"ds_read_b128 v[112:115], v49 offset:6144\n" \
"v_mfma_f32_16x16x32_bf16 a[160:163], v[152:155], v[64:67], a[160:163]\n" \
"v_cvt_pk_bf16_f32 v254, v180, v181\n" \
"v_cvt_pk_bf16_f32 v255, v182, v183\n" \
"v_mfma_f32_16x16x32_bf16 a[164:167], v[160:163], v[64:67], 0\n" \
"v_accvgpr_write_b32 a96, v252\n" \
"v_accvgpr_write_b32 a97, v253\n" \
"v_mfma_f32_16x16x32_bf16 a[164:167], v[156:159], v[60:63], a[164:167]\n" \
"v_accvgpr_write_b32 a98, v254\n" \
"v_accvgpr_write_b32 a99, v255\n" \
"v_accvgpr_read_b32 v252, a168\n" \
"v_accvgpr_read_b32 v253, a169\n" \
"v_accvgpr_read_b32 v254, a170\n" \
"v_accvgpr_read_b32 v255, a171\n" \
"v_mfma_f32_16x16x32_bf16 a[168:171], v[164:167], v[60:63], 0\n" \
"s_add_u32 s89, s44, 0x1000\n" \
"v_accvgpr_read_b32 v180, a172\n" \
"v_mfma_f32_16x16x32_bf16 a[168:171], v[168:171], v[64:67], a[168:171]\n" \
"buffer_store_dwordx4 v[196:199], v20, s[80:83], s89 offen offset:1024 sc1\n" \
"v_accvgpr_read_b32 v181, a173\n" \
"v_accvgpr_read_b32 v182, a174\n" \
"v_accvgpr_read_b32 v183, a175\n" \
"v_mfma_f32_16x16x32_bf16 a[172:175], v[176:179], v[64:67], 0\n" \
"v_cvt_pk_bf16_f32 v252, v252, v253\n" \
"v_cvt_pk_bf16_f32 v253, v254, v255\n" \
"v_mfma_f32_16x16x32_bf16 a[172:175], v[172:175], v[60:63], a[172:175]\n" \
"v_cvt_pk_bf16_f32 v254, v180, v181\n" \
"v_cvt_pk_bf16_f32 v255, v182, v183\n" \
"v_mfma_f32_16x16x32_bf16 a[192:195], v[148:151], v[68:71], 0\n" \
"v_accvgpr_write_b32 a100, v252\n" \
"v_accvgpr_write_b32 a101, v253\n" \
"v_mfma_f32_16x16x32_bf16 a[192:195], v[152:155], v[72:75], a[192:195]\n" \
"v_accvgpr_write_b32 a102, v254\n" \
"v_accvgpr_write_b32 a103, v255\n" \
"v_accvgpr_read_b32 v252, a200\n" \
"v_accvgpr_read_b32 v253, a201\n" \
"v_accvgpr_read_b32 v254, a202\n" \
"v_accvgpr_read_b32 v255, a203\n" \
"s_waitcnt vmcnt(4) lgkmcnt(0)\n" \
"s_barrier\n" \
"v_mfma_f32_16x16x32_bf16 a[196:199], v[160:163], v[72:75], 0\n" \
"s_add_u32 m0, s40, 0x0\n" \
"s_nop 0\n" \
"s_add_u32 s85, s37, 0x4000\n" \
"v_mfma_f32_16x16x32_bf16 a[196:199], v[156:159], v[68:71], a[196:199]\n" \
"buffer_load_dwordx4 v20, s[80:83], s85 offen sc1 lds\n" \
"v_mfma_f32_16x16x32_bf16 a[200:203], v[164:167], v[68:71], 0\n" \
"v_accvgpr_read_b32 v180, a204\n" \
"v_accvgpr_read_b32 v181, a205\n" \
"v_mfma_f32_16x16x32_bf16 a[200:203], v[168:171], v[72:75], a[200:203]\n" \
"v_accvgpr_read_b32 v182, a206\n" \
"v_accvgpr_read_b32 v183, a207\n" \
"v_mfma_f32_16x16x32_bf16 a[204:207], v[176:179], v[72:75], 0\n" \
"s_add_u32 m0, s40, 0x1000\n" \
"s_add_u32 s84, s37, 0x5000\n" \
"v_mfma_f32_16x16x32_bf16 a[204:207], v[172:175], v[68:71], a[204:207]\n" \
"buffer_load_dwordx4 v20, s[80:83], s84 offen sc1 lds\n" \
"v_mfma_f32_16x16x32_bf16 a[224:227], v[148:151], v[76:79], 0\n" \
"v_cvt_pk_bf16_f32 v252, v252, v253\n" \
"v_cvt_pk_bf16_f32 v253, v254, v255\n" \
"v_mfma_f32_16x16x32_bf16 a[224:227], v[152:155], v[80:83], a[224:227]\n" \
"v_cvt_pk_bf16_f32 v254, v180, v181\n" \
"v_cvt_pk_bf16_f32 v255, v182, v183\n" \
"v_mfma_f32_16x16x32_bf16 a[228:231], v[160:163], v[80:83], 0\n" \
"v_accvgpr_write_b32 a8, v252\n" \
"v_accvgpr_write_b32 a9, v253\n" \
"v_mfma_f32_16x16x32_bf16 a[228:231], v[156:159], v[76:79], a[228:231]\n" \
"v_accvgpr_write_b32 a10, v254\n" \
"v_accvgpr_write_b32 a11, v255\n" \
"v_accvgpr_read_b32 v252, a232\n" \
"v_accvgpr_read_b32 v253, a233\n" \
"v_accvgpr_read_b32 v254, a234\n" \
"v_accvgpr_read_b32 v255, a235\n" \
"v_mfma_f32_16x16x32_bf16 a[232:235], v[164:167], v[76:79], 0\n" \
"s_add_u32 m0, s40, 0x3000\n" \
"s_mov_b32 s85, 0x100\n" \
"buffer_load_dwordx4 v21, s[76:79], s85 offen lds\n" \
"v_mfma_f32_16x16x32_bf16 a[232:235], v[168:171], v[80:83], a[232:235]\n" \
"v_accvgpr_read_b32 v180, a236\n" \
"v_accvgpr_read_b32 v181, a237\n" \
"v_accvgpr_read_b32 v182, a238\n" \
"v_accvgpr_read_b32 v183, a239\n" \
"v_mfma_f32_16x16x32_bf16 a[236:239], v[176:179], v[80:83], 0\n" \
"v_cvt_pk_bf16_f32 v252, v252, v253\n" \
"v_cvt_pk_bf16_f32 v253, v254, v255\n" \
"v_mfma_f32_16x16x32_bf16 a[236:239], v[172:175], v[76:79], a[236:239]\n" \
"v_cvt_pk_bf16_f32 v254, v180, v181\n" \
"v_cvt_pk_bf16_f32 v255, v182, v183\n" \
"v_accvgpr_write_b32 a12, v252\n" \
"v_accvgpr_write_b32 a13, v253\n" \
"v_accvgpr_write_b32 a14, v254\n" \
"v_accvgpr_write_b32 a15, v255\n" \
"v_accvgpr_read_b32 v252, a144\n" \
"v_accvgpr_read_b32 v253, a145\n" \
"v_accvgpr_read_b32 v254, a146\n" \
"v_accvgpr_read_b32 v255, a147\n" \
"v_mfma_f32_16x16x32_bf16 a[144:147], v[148:151], v[84:87], 0\n" \
"s_add_u32 m0, %[mA], 0x0\n" \
"s_nop 0\n" \
"s_mov_b32 s84, 0x100\n" \
"buffer_load_dwordx4 v24, s[76:79], s84 offen lds\n" \
"v_mfma_f32_16x16x32_bf16 a[144:147], v[152:155], v[88:91], a[144:147]\n" \
"v_cvt_pk_bf16_f32 v252, v252, v253\n" \
"v_accvgpr_read_b32 v180, a176\n" \
"v_accvgpr_read_b32 v181, a177\n" \
"v_accvgpr_read_b32 v182, a178\n" \
"v_accvgpr_read_b32 v183, a179\n" \
"v_cvt_pk_bf16_f32 v253, v254, v255\n" \
"v_cvt_pk_bf16_f32 v254, v180, v181\n" \
"v_cvt_pk_bf16_f32 v255, v182, v183\n" \
"v_accvgpr_write_b32 a40, v252\n" \
"v_accvgpr_write_b32 a41, v253\n" \
"v_accvgpr_write_b32 a42, v254\n" \
"v_accvgpr_write_b32 a43, v255\n" \
"v_accvgpr_read_b32 v252, a148\n" \
"v_accvgpr_read_b32 v253, a149\n" \
"v_accvgpr_read_b32 v254, a150\n" \
"v_accvgpr_read_b32 v255, a151\n" \
"v_mfma_f32_16x16x32_bf16 a[148:151], v[160:163], v[88:91], 0\n" \
"v_cvt_pk_bf16_f32 v252, v252, v253\n" \
"s_add_u32 m0, %[mA], 0x400\n" \
"v_mfma_f32_16x16x32_bf16 a[148:151], v[156:159], v[84:87], a[148:151]\n" \
"s_mov_b32 s85, 0x2500\n" \
"v_accvgpr_read_b32 v180, a180\n" \
"v_accvgpr_read_b32 v181, a181\n" \
"v_accvgpr_read_b32 v182, a182\n" \
"v_accvgpr_read_b32 v183, a183\n" \
"v_cvt_pk_bf16_f32 v253, v254, v255\n" \
"v_cvt_pk_bf16_f32 v254, v180, v181\n" \
"v_cvt_pk_bf16_f32 v255, v182, v183\n" \
"v_accvgpr_write_b32 a44, v252\n" \
"v_accvgpr_write_b32 a45, v253\n" \
"v_accvgpr_write_b32 a46, v254\n" \
"v_accvgpr_write_b32 a47, v255\n" \
"v_accvgpr_read_b32 v252, a152\n" \
"v_accvgpr_read_b32 v253, a153\n" \
"v_accvgpr_read_b32 v254, a154\n" \
"v_accvgpr_read_b32 v255, a155\n" \
"v_mfma_f32_16x16x32_bf16 a[152:155], v[164:167], v[84:87], 0\n" \
"buffer_load_dwordx4 v25, s[76:79], s85 offen lds\n" \
"v_mfma_f32_16x16x32_bf16 a[152:155], v[168:171], v[88:91], a[152:155]\n" \
"v_cvt_pk_bf16_f32 v252, v252, v253\n" \
"v_accvgpr_read_b32 v180, a184\n" \
"v_accvgpr_read_b32 v181, a185\n" \
"v_accvgpr_read_b32 v182, a186\n" \
"v_accvgpr_read_b32 v183, a187\n" \
"v_cvt_pk_bf16_f32 v253, v254, v255\n" \
"v_cvt_pk_bf16_f32 v254, v180, v181\n" \
"v_cvt_pk_bf16_f32 v255, v182, v183\n" \
"v_accvgpr_write_b32 a72, v252\n" \
"v_accvgpr_write_b32 a73, v253\n" \
"v_accvgpr_write_b32 a74, v254\n" \
"v_accvgpr_write_b32 a75, v255\n" \
"v_accvgpr_read_b32 v252, a156\n" \
"v_accvgpr_read_b32 v253, a157\n" \
"v_accvgpr_read_b32 v254, a158\n" \
"v_accvgpr_read_b32 v255, a159\n" \
"v_mfma_f32_16x16x32_bf16 a[156:159], v[176:179], v[88:91], 0\n" \
"s_add_u32 m0, %[mB], 0x0\n" \
"s_nop 0\n" \
"s_mov_b32 s84, 0x100000\n" \
"v_mfma_f32_16x16x32_bf16 a[156:159], v[172:175], v[84:87], a[156:159]\n" \
"buffer_load_dwordx4 v26, s[68:71], s84 offen lds\n" \
"v_mfma_f32_16x16x32_bf16 a[176:179], v[148:151], v[92:95], 0\n" \
"s_add_u32 m0, %[mA], 0x800\n" \
"s_mov_b32 s85, 0x4900\n" \
"v_mfma_f32_16x16x32_bf16 a[176:179], v[152:155], v[96:99], a[176:179]\n" \
"v_accvgpr_read_b32 v180, a188\n" \
"v_accvgpr_read_b32 v181, a189\n" \
"v_mfma_f32_16x16x32_bf16 a[180:183], v[160:163], v[96:99], 0\n" \
"buffer_load_dwordx4 v24, s[76:79], s85 offen lds\n" \
"v_mfma_f32_16x16x32_bf16 a[180:183], v[156:159], v[92:95], a[180:183]\n" \
"v_accvgpr_read_b32 v182, a190\n" \
"v_accvgpr_read_b32 v183, a191\n" \
"v_mfma_f32_16x16x32_bf16 a[184:187], v[164:167], v[92:95], 0\n" \
"s_add_u32 m0, %[mA], 0xc00\n" \
"s_mov_b32 s84, 0x6d00\n" \
"v_mfma_f32_16x16x32_bf16 a[184:187], v[168:171], v[96:99], a[184:187]\n" \
"buffer_load_dwordx4 v25, s[76:79], s84 offen lds\n" \
"v_mfma_f32_16x16x32_bf16 a[188:191], v[176:179], v[96:99], 0\n" \
"v_cvt_pk_bf16_f32 v252, v252, v253\n" \
"v_cvt_pk_bf16_f32 v253, v254, v255\n" \
"v_mfma_f32_16x16x32_bf16 a[188:191], v[172:175], v[92:95], a[188:191]\n" \
"v_cvt_pk_bf16_f32 v254, v180, v181\n" \
"L_pf5:\n" \
"s_cbranch_execz L_pf9\n" \
"s_nop 0\n" \
"v_cvt_pk_bf16_f32 v255, v182, v183\n" \
"v_accvgpr_write_b32 a76, v252\n" \
"v_accvgpr_write_b32 a77, v253\n" \
"v_accvgpr_write_b32 a78, v254\n" \
"v_accvgpr_write_b32 a79, v255\n" \
"v_accvgpr_read_b32 v252, a208\n" \
"v_accvgpr_read_b32 v253, a209\n" \
"v_accvgpr_read_b32 v254, a210\n" \
"v_accvgpr_read_b32 v255, a211\n" \
"v_mfma_f32_16x16x32_bf16 a[208:211], v[148:151], v[100:103], 0\n" \
"v_cvt_pk_bf16_f32 v252, v252, v253\n" \
"v_accvgpr_read_b32 v180, a240\n" \
"v_mfma_f32_16x16x32_bf16 a[208:211], v[152:155], v[104:107], a[208:211]\n" \
"v_accvgpr_read_b32 v181, a241\n" \
"v_accvgpr_read_b32 v182, a242\n" \
"v_accvgpr_read_b32 v183, a243\n" \
"v_cvt_pk_bf16_f32 v253, v254, v255\n" \
"v_cvt_pk_bf16_f32 v254, v180, v181\n" \
"v_cvt_pk_bf16_f32 v255, v182, v183\n" \
"v_accvgpr_write_b32 a104, v252\n" \
"v_accvgpr_write_b32 a105, v253\n" \
"v_accvgpr_write_b32 a106, v254\n" \
"v_accvgpr_write_b32 a107, v255\n" \
"v_accvgpr_read_b32 v252, a212\n" \
"v_accvgpr_read_b32 v253, a213\n" \
"v_accvgpr_read_b32 v254, a214\n" \
"v_accvgpr_read_b32 v255, a215\n" \
"v_mfma_f32_16x16x32_bf16 a[212:215], v[160:163], v[104:107], 0\n" \
"s_add_u32 m0, %[mB], 0x400\n" \
"s_mov_b32 s85, 0x108000\n" \
"buffer_load_dwordx4 v26, s[68:71], s85 offen lds\n" \
"ds_read_b64_tr_b16 v[140:141], v39 offset:32768\n" \
"ds_read_b64_tr_b16 v[142:143], v39 offset:33280\n" \
"ds_read_b128 v[84:87], v22 offset:32768\n" \
"v_mfma_f32_16x16x32_bf16 a[212:215], v[156:159], v[100:103], a[212:215]\n" \
"ds_read_b64_tr_b16 v[144:145], v39 offset:36864\n" \
"ds_read_b64_tr_b16 v[146:147], v39 offset:37376\n" \
"ds_read_b128 v[88:91], v23 offset:32768\n" \
"v_accvgpr_read_b32 v184, a216\n" \
"v_accvgpr_read_b32 v185, a217\n" \
"v_accvgpr_read_b32 v186, a218\n" \
"v_accvgpr_read_b32 v187, a219\n" \
"v_mfma_f32_16x16x32_bf16 a[216:219], v[164:167], v[100:103], 0\n" \
"ds_read_b64_tr_b16 v[128:129], v37 offset:36864\n" \
"ds_read_b64_tr_b16 v[130:131], v37 offset:37376\n" \
"v_mfma_f32_16x16x32_bf16 a[216:219], v[168:171], v[104:107], a[216:219]\n" \
"v_cvt_pk_bf16_f32 v252, v252, v253\n" \
"v_accvgpr_read_b32 v180, a244\n" \
"v_accvgpr_read_b32 v181, a245\n" \
"v_accvgpr_read_b32 v182, a246\n" \
"v_accvgpr_read_b32 v183, a247\n" \
"v_cvt_pk_bf16_f32 v253, v254, v255\n" \
"v_cvt_pk_bf16_f32 v254, v180, v181\n" \
"v_cvt_pk_bf16_f32 v255, v182, v183\n" \
"v_accvgpr_write_b32 a108, v252\n" \
"v_accvgpr_write_b32 a109, v253\n" \
"v_accvgpr_write_b32 a110, v254\n" \
"v_accvgpr_write_b32 a111, v255\n" \
"v_accvgpr_read_b32 v252, a220\n" \
"v_accvgpr_read_b32 v253, a221\n" \
"v_accvgpr_read_b32 v254, a222\n" \
"v_accvgpr_read_b32 v255, a223\n" \
"v_mfma_f32_16x16x32_bf16 a[220:223], v[176:179], v[104:107], 0\n" \
"ds_read_b64_tr_b16 v[124:125], v37 offset:32768\n" \
"ds_read_b64_tr_b16 v[126:127], v37 offset:33280\n" \
"s_add_u32 m0, %[mA], 0x1000\n" \
"s_mov_b32 s84, 0x9100\n" \
"buffer_load_dwordx4 v24, s[76:79], s84 offen lds\n" \
"ds_read_b64_tr_b16 v[132:133], v38 offset:32768\n" \
"ds_read_b64_tr_b16 v[134:135], v38 offset:33280\n" \
"v_mfma_f32_16x16x32_bf16 a[220:223], v[172:175], v[100:103], a[220:223]\n" \
"ds_read_b64_tr_b16 v[136:137], v38 offset:36864\n" \
"ds_read_b64_tr_b16 v[138:139], v38 offset:37376\n" \
"v_mfma_f32_16x16x32_bf16 a[240:243], v[148:151], v[108:111], 0\n" \
"ds_read_b128 v[56:59], v29 offset:32768\n" \
"s_add_u32 m0, %[mA], 0x1400\n" \
"v_mfma_f32_16x16x32_bf16 a[240:243], v[152:155], v[112:115], a[240:243]\n" \
"s_mov_b32 s85, 0xb500\n" \
"ds_read_b128 v[52:55], v28 offset:32768\n" \
"buffer_load_dwordx4 v25, s[76:79], s85 offen lds\n" \
"v_mfma_f32_16x16x32_bf16 a[244:247], v[160:163], v[112:115], 0\n" \
"v_accvgpr_read_b32 v180, a248\n" \
"v_accvgpr_read_b32 v181, a249\n" \
"v_mfma_f32_16x16x32_bf16 a[244:247], v[156:159], v[108:111], a[244:247]\n" \
"v_accvgpr_read_b32 v182, a250\n" \
"v_accvgpr_read_b32 v183, a251\n" \
"v_mfma_f32_16x16x32_bf16 a[248:251], v[164:167], v[108:111], 0\n" \
"ds_read_b64_tr_b16 v[120:121], v36 offset:36864\n" \
"ds_read_b64_tr_b16 v[122:123], v36 offset:37376\n" \
"v_mfma_f32_16x16x32_bf16 a[248:251], v[168:171], v[112:115], a[248:251]\n" \
"s_add_u32 m0, %[mB], 0x800\n" \
"s_mov_b32 s84, 0x110000\n" \
"v_cvt_pk_bf16_f32 v184, v184, v185\n" \
"v_cvt_pk_bf16_f32 v185, v186, v187\n" \
"v_cvt_pk_bf16_f32 v186, v180, v181\n" \
"v_cvt_pk_bf16_f32 v187, v182, v183\n" \
"v_accvgpr_read_b32 v180, a252\n" \
"v_accvgpr_read_b32 v181, a253\n" \
"v_accvgpr_read_b32 v182, a254\n" \
"v_accvgpr_read_b32 v183, a255\n" \
"v_mfma_f32_16x16x32_bf16 a[252:255], v[176:179], v[112:115], 0\n" \
"buffer_load_dwordx4 v27, s[68:71], s84 offen lds\n" \
"ds_read_b64_tr_b16 v[116:117], v36 offset:32768\n" \
"ds_read_b64_tr_b16 v[118:119], v36 offset:33280\n" \
"v_mfma_f32_16x16x32_bf16 a[252:255], v[172:175], v[108:111], a[252:255]\n" \
"ds_read_b128 v[64:67], v29 offset:34816\n" \
"s_waitcnt lgkmcnt(13)\n" \
"v_mfma_f32_16x16x32_bf16 a[16:19], v[140:143], v[84:87], a[16:19]\n" \
"ds_read_b128 v[60:63], v28 offset:34816\n" \
"s_add_u32 m0, %[mA], 0x1800\n" \
"v_mfma_f32_16x16x32_bf16 a[16:19], v[144:147], v[88:91], a[16:19]\n" \
"s_mov_b32 s84, 0xd900\n" \
"ds_read_b128 v[72:75], v29 offset:36864\n" \
"v_mfma_f32_16x16x32_bf16 a[48:51], v[128:131], v[88:91], a[48:51]\n" \
"s_mov_b32 s85, 0xfd00\n" \
"s_waitcnt lgkmcnt(9)\n" \
"v_mfma_f32_16x16x32_bf16 a[48:51], v[124:127], v[84:87], a[48:51]\n" \
"buffer_load_dwordx4 v24, s[76:79], s84 offen lds\n" \
"v_mfma_f32_16x16x32_bf16 a[80:83], v[84:87], v[132:135], a[80:83]\n" \
"s_add_u32 m0, %[mA], 0x1c00\n" \
"ds_read_b128 v[68:71], v28 offset:36864\n" \
"v_mfma_f32_16x16x32_bf16 a[80:83], v[88:91], v[136:139], a[80:83]\n" \
"s_mov_b32 s84, 0x118000\n" \
"s_waitcnt lgkmcnt(8)\n" \
"v_mfma_f32_16x16x32_bf16 a[112:115], v[144:147], v[56:59], a[112:115]\n" \
"buffer_load_dwordx4 v25, s[76:79], s85 offen lds\n" \
"v_mfma_f32_16x16x32_bf16 a[112:115], v[140:143], v[52:55], a[112:115]\n" \
"s_add_u32 m0, %[mB], 0xc00\n" \
"ds_read_b128 v[80:83], v29 offset:38912\n" \
"v_mfma_f32_16x16x32_bf16 a[20:23], v[124:127], v[52:55], a[20:23]\n" \
"ds_read_b128 v[76:79], v28 offset:38912\n" \
"ds_read_b64_tr_b16 v[152:153], v32 offset:40960\n" \
"v_mfma_f32_16x16x32_bf16 a[20:23], v[128:131], v[56:59], a[20:23]\n" \
"s_add_u32 s88, s44, 0x0\n" \
"buffer_load_dwordx4 v27, s[68:71], s84 offen lds\n" \
"s_waitcnt lgkmcnt(9)\n" \
"v_mfma_f32_16x16x32_bf16 a[52:55], v[120:123], v[56:59], a[52:55]\n" \
"ds_read_b64_tr_b16 v[154:155], v32 offset:41984\n" \
"s_waitcnt lgkmcnt(8)\n" \
"v_mfma_f32_16x16x32_bf16 a[52:55], v[116:119], v[52:55], a[52:55]\n" \
"ds_read_b64_tr_b16 v[148:149], v32 offset:32768\n" \
"ds_read_b64_tr_b16 v[150:151], v32 offset:33792\n" \
"v_mfma_f32_16x16x32_bf16 a[84:87], v[52:55], v[132:135], a[84:87]\n" \
"buffer_store_dwordx4 v[200:203], v20, s[80:83], s88 offen offset:2048 sc1\n" \
"v_mfma_f32_16x16x32_bf16 a[84:87], v[56:59], v[136:139], a[84:87]\n" \
"ds_read_b64_tr_b16 v[156:157], v33 offset:32768\n" \
"s_waitcnt lgkmcnt(10)\n" \
"v_mfma_f32_16x16x32_bf16 a[116:119], v[144:147], v[64:67], a[116:119]\n" \
"ds_read_b64_tr_b16 v[158:159], v33 offset:33792\n" \
"s_waitcnt lgkmcnt(10)\n" \
"v_mfma_f32_16x16x32_bf16 a[116:119], v[140:143], v[60:63], a[116:119]\n" \
"ds_read_b64_tr_b16 v[160:161], v33 offset:40960\n" \
"ds_read_b64_tr_b16 v[162:163], v33 offset:41984\n" \
"v_mfma_f32_16x16x32_bf16 a[24:27], v[124:127], v[60:63], a[24:27]\n" \
"ds_read_b64_tr_b16 v[168:169], v34 offset:40960\n" \
"ds_read_b64_tr_b16 v[170:171], v34 offset:41984\n" \
"v_mfma_f32_16x16x32_bf16 a[24:27], v[128:131], v[64:67], a[24:27]\n" \
"ds_read_b64_tr_b16 v[164:165], v34 offset:32768\n" \
"ds_read_b64_tr_b16 v[166:167], v34 offset:33792\n" \
"v_mfma_f32_16x16x32_bf16 a[56:59], v[120:123], v[64:67], a[56:59]\n" \
"ds_read_b64_tr_b16 v[172:173], v35 offset:32768\n" \
"ds_read_b64_tr_b16 v[174:175], v35 offset:33792\n" \
"v_mfma_f32_16x16x32_bf16 a[56:59], v[116:119], v[60:63], a[56:59]\n" \
"s_add_u32 s89, s44, 0x1000\n" \
"ds_read_b64_tr_b16 v[176:177], v35 offset:40960\n" \
"v_mfma_f32_16x16x32_bf16 a[88:91], v[60:63], v[132:135], a[88:91]\n" \
"buffer_store_dwordx4 v[204:207], v20, s[80:83], s89 offen offset:2048 sc1\n" \
"v_mfma_f32_16x16x32_bf16 a[88:91], v[64:67], v[136:139], a[88:91]\n" \
"ds_read_b64_tr_b16 v[178:179], v35 offset:41984\n" \
"s_waitcnt lgkmcnt(15)\n" \
"s_nop 0\n" \
"v_mfma_f32_16x16x32_bf16 a[120:123], v[144:147], v[72:75], a[120:123]\n" \
"ds_read_b128 v[84:87], v48 offset:32768\n" \
"ds_read_b128 v[88:91], v49 offset:32768\n" \
"v_mfma_f32_16x16x32_bf16 a[120:123], v[140:143], v[68:71], a[120:123]\n" \
"ds_read_b128 v[92:95], v48 offset:34816\n" \
"ds_read_b128 v[96:99], v49 offset:34816\n" \
"v_mfma_f32_16x16x32_bf16 a[28:31], v[124:127], v[68:71], a[28:31]\n" \
"v_cvt_pk_bf16_f32 v252, v252, v253\n" \
"v_cvt_pk_bf16_f32 v253, v254, v255\n" \
"v_mfma_f32_16x16x32_bf16 a[28:31], v[128:131], v[72:75], a[28:31]\n" \
"ds_read_b128 v[100:103], v48 offset:36864\n" \
"ds_read_b128 v[104:107], v49 offset:36864\n" \
"v_mfma_f32_16x16x32_bf16 a[60:63], v[120:123], v[72:75], a[60:63]\n" \
"v_cvt_pk_bf16_f32 v254, v180, v181\n" \
"v_cvt_pk_bf16_f32 v255, v182, v183\n" \
"v_mfma_f32_16x16x32_bf16 a[60:63], v[116:119], v[68:71], a[60:63]\n" \
"ds_read_b128 v[108:111], v48 offset:38912\n" \
"ds_read_b128 v[112:115], v49 offset:38912\n" \
"v_mfma_f32_16x16x32_bf16 a[92:95], v[68:71], v[132:135], a[92:95]\n" \
"v_mfma_f32_16x16x32_bf16 a[92:95], v[72:75], v[136:139], a[92:95]\n" \
"s_add_u32 s88, s44, 0x0\n" \
"v_mfma_f32_16x16x32_bf16 a[124:127], v[144:147], v[80:83], a[124:127]\n" \
"buffer_store_dwordx4 v[208:211], v20, s[80:83], s88 offen offset:3072 sc1\n" \
"v_mfma_f32_16x16x32_bf16 a[124:127], v[140:143], v[76:79], a[124:127]\n" \
"v_mfma_f32_16x16x32_bf16 a[0:3], v[124:127], v[76:79], a[0:3]\n" \
"v_mfma_f32_16x16x32_bf16 a[0:3], v[128:131], v[80:83], a[0:3]\n" \
"v_mfma_f32_16x16x32_bf16 a[4:7], v[120:123], v[80:83], a[4:7]\n" \
"v_mfma_f32_16x16x32_bf16 a[4:7], v[116:119], v[76:79], a[4:7]\n" \
"v_mfma_f32_16x16x32_bf16 a[32:35], v[76:79], v[132:135], a[32:35]\n" \
"v_mfma_f32_16x16x32_bf16 a[32:35], v[80:83], v[136:139], a[32:35]\n" \
"s_waitcnt lgkmcnt(6)\n" \
"v_mfma_f32_16x16x32_bf16 a[128:131], v[152:155], v[56:59], a[128:131]\n" \
"v_mfma_f32_16x16x32_bf16 a[128:131], v[148:151], v[52:55], a[128:131]\n" \
"v_mfma_f32_16x16x32_bf16 a[132:135], v[156:159], v[52:55], a[132:135]\n" \
"v_mfma_f32_16x16x32_bf16 a[132:135], v[160:163], v[56:59], a[132:135]\n" \
"v_mfma_f32_16x16x32_bf16 a[136:139], v[168:171], v[56:59], a[136:139]\n" \
"v_mfma_f32_16x16x32_bf16 a[136:139], v[164:167], v[52:55], a[136:139]\n" \
"v_mfma_f32_16x16x32_bf16 a[140:143], v[172:175], v[52:55], a[140:143]\n" \
"v_mfma_f32_16x16x32_bf16 a[140:143], v[176:179], v[56:59], a[140:143]\n" \
"v_mfma_f32_16x16x32_bf16 a[160:163], v[152:155], v[64:67], a[160:163]\n" \
"v_mfma_f32_16x16x32_bf16 a[160:163], v[148:151], v[60:63], a[160:163]\n" \
"v_mfma_f32_16x16x32_bf16 a[164:167], v[156:159], v[60:63], a[164:167]\n" \
"v_mfma_f32_16x16x32_bf16 a[164:167], v[160:163], v[64:67], a[164:167]\n" \
"v_mfma_f32_16x16x32_bf16 a[168:171], v[168:171], v[64:67], a[168:171]\n" \
"v_mfma_f32_16x16x32_bf16 a[168:171], v[164:167], v[60:63], a[168:171]\n" \
"v_mfma_f32_16x16x32_bf16 a[172:175], v[172:175], v[60:63], a[172:175]\n" \
"v_mfma_f32_16x16x32_bf16 a[172:175], v[176:179], v[64:67], a[172:175]\n" \
"v_mfma_f32_16x16x32_bf16 a[192:195], v[152:155], v[72:75], a[192:195]\n" \
"v_mfma_f32_16x16x32_bf16 a[192:195], v[148:151], v[68:71], a[192:195]\n" \
"s_waitcnt vmcnt(3) lgkmcnt(0)\n" \
"s_barrier\n" \
"v_mfma_f32_16x16x32_bf16 a[196:199], v[156:159], v[68:71], a[196:199]\n" \
"s_add_u32 m0, s40, 0x8000\n" \
"s_add_u32 s85, s37, 0x6000\n" \
"v_mfma_f32_16x16x32_bf16 a[196:199], v[160:163], v[72:75], a[196:199]\n" \
"buffer_load_dwordx4 v20, s[80:83], s85 offen sc1 lds\n" \
"v_mfma_f32_16x16x32_bf16 a[200:203], v[168:171], v[72:75], a[200:203]\n" \
"s_add_u32 m0, s40, 0x9000\n" \
"s_add_u32 s84, s37, 0x7000\n" \
"v_mfma_f32_16x16x32_bf16 a[200:203], v[164:167], v[68:71], a[200:203]\n" \
"ds_read_b64_tr_b16 v[144:145], v39 offset:4096\n" \
"ds_read_b64_tr_b16 v[146:147], v39 offset:4608\n" \
"v_mfma_f32_16x16x32_bf16 a[204:207], v[172:175], v[68:71], a[204:207]\n" \
"buffer_load_dwordx4 v20, s[80:83], s84 offen sc1 lds\n" \
"v_mfma_f32_16x16x32_bf16 a[204:207], v[176:179], v[72:75], a[204:207]\n" \
"ds_read_b64_tr_b16 v[140:141], v39 offset:0\n" \
"ds_read_b64_tr_b16 v[142:143], v39 offset:512\n" \
"v_mfma_f32_16x16x32_bf16 a[224:227], v[152:155], v[80:83], a[224:227]\n" \
"s_add_u32 m0, s40, 0xb000\n" \
"s_mov_b32 s85, 0x180\n" \
"v_mfma_f32_16x16x32_bf16 a[224:227], v[148:151], v[76:79], a[224:227]\n" \
"buffer_load_dwordx4 v21, s[76:79], s85 offen lds\n" \
"v_mfma_f32_16x16x32_bf16 a[228:231], v[156:159], v[76:79], a[228:231]\n" \
"ds_read_b64_tr_b16 v[124:125], v37 offset:0\n" \
"ds_read_b64_tr_b16 v[126:127], v37 offset:512\n" \
"v_mfma_f32_16x16x32_bf16 a[228:231], v[160:163], v[80:83], a[228:231]\n" \
"ds_read_b64_tr_b16 v[128:129], v37 offset:4096\n" \
"ds_read_b64_tr_b16 v[130:131], v37 offset:4608\n" \
"v_mfma_f32_16x16x32_bf16 a[232:235], v[168:171], v[80:83], a[232:235]\n" \
"s_add_u32 m0, %[mA], 0x8000\n" \
"s_mov_b32 s84, 0x180\n" \
"v_mfma_f32_16x16x32_bf16 a[232:235], v[164:167], v[76:79], a[232:235]\n" \
"buffer_load_dwordx4 v24, s[76:79], s84 offen lds\n" \
"v_mfma_f32_16x16x32_bf16 a[236:239], v[172:175], v[76:79], a[236:239]\n" \
"ds_read_b64_tr_b16 v[136:137], v38 offset:4096\n" \
"ds_read_b64_tr_b16 v[138:139], v38 offset:4608\n" \
"v_mfma_f32_16x16x32_bf16 a[236:239], v[176:179], v[80:83], a[236:239]\n" \
"s_add_u32 m0, %[mA], 0x8400\n" \
"s_mov_b32 s85, 0x2580\n" \
"v_mfma_f32_16x16x32_bf16 a[144:147], v[152:155], v[88:91], a[144:147]\n" \
"buffer_load_dwordx4 v25, s[76:79], s85 offen lds\n" \
"v_mfma_f32_16x16x32_bf16 a[144:147], v[148:151], v[84:87], a[144:147]\n" \
"ds_read_b64_tr_b16 v[132:133], v38 offset:0\n" \
"ds_read_b64_tr_b16 v[134:135], v38 offset:512\n" \
"v_mfma_f32_16x16x32_bf16 a[148:151], v[156:159], v[84:87], a[148:151]\n" \
"ds_read_b128 v[52:55], v28 offset:0\n" \
"ds_read_b128 v[56:59], v29 offset:0\n" \
"v_mfma_f32_16x16x32_bf16 a[148:151], v[160:163], v[88:91], a[148:151]\n" \
"s_add_u32 m0, %[mB], 0x8000\n" \
"s_mov_b32 s84, 0x180000\n" \
"v_mfma_f32_16x16x32_bf16 a[152:155], v[168:171], v[88:91], a[152:155]\n" \
"buffer_load_dwordx4 v26, s[68:71], s84 offen lds\n" \
"v_mfma_f32_16x16x32_bf16 a[152:155], v[164:167], v[84:87], a[152:155]\n" \
"ds_read_b64_tr_b16 v[116:117], v36 offset:0\n" \
"ds_read_b64_tr_b16 v[118:119], v36 offset:512\n" \
"v_mfma_f32_16x16x32_bf16 a[156:159], v[172:175], v[84:87], a[156:159]\n" \
"ds_read_b128 v[84:87], v22 offset:0\n" \
"ds_read_b64_tr_b16 v[120:121], v36 offset:4096\n" \
"v_mfma_f32_16x16x32_bf16 a[156:159], v[176:179], v[88:91], a[156:159]\n" \
"s_add_u32 m0, %[mA], 0x8800\n" \
"s_mov_b32 s85, 0x4980\n" \
"v_mfma_f32_16x16x32_bf16 a[176:179], v[152:155], v[96:99], a[176:179]\n" \
"buffer_load_dwordx4 v24, s[76:79], s85 offen lds\n" \
"v_mfma_f32_16x16x32_bf16 a[176:179], v[148:151], v[92:95], a[176:179]\n" \
"ds_read_b128 v[88:91], v23 offset:0\n" \
"ds_read_b64_tr_b16 v[122:123], v36 offset:4608\n" \
"v_mfma_f32_16x16x32_bf16 a[180:183], v[156:159], v[92:95], a[180:183]\n" \
"s_add_u32 m0, %[mA], 0x8c00\n" \
"s_mov_b32 s84, 0x6d80\n" \
"v_mfma_f32_16x16x32_bf16 a[180:183], v[160:163], v[96:99], a[180:183]\n" \
"buffer_load_dwordx4 v25, s[76:79], s84 offen lds\n" \
"v_mfma_f32_16x16x32_bf16 a[184:187], v[168:171], v[96:99], a[184:187]\n" \
"ds_read_b128 v[60:63], v28 offset:2048\n" \
"ds_read_b128 v[64:67], v29 offset:2048\n" \
"v_mfma_f32_16x16x32_bf16 a[184:187], v[164:167], v[92:95], a[184:187]\n" \
"ds_read_b128 v[68:71], v28 offset:4096\n" \
"ds_read_b128 v[72:75], v29 offset:4096\n" \
"v_mfma_f32_16x16x32_bf16 a[188:191], v[172:175], v[92:95], a[188:191]\n" \
"s_add_u32 m0, %[mB], 0x8400\n" \
"s_mov_b32 s85, 0x188000\n" \
"v_mfma_f32_16x16x32_bf16 a[188:191], v[176:179], v[96:99], a[188:191]\n" \
"buffer_load_dwordx4 v26, s[68:71], s85 offen lds\n" \
"v_mfma_f32_16x16x32_bf16 a[208:211], v[152:155], v[104:107], a[208:211]\n" \
"ds_read_b128 v[76:79], v28 offset:6144\n" \
"ds_read_b128 v[80:83], v29 offset:6144\n" \
"v_mfma_f32_16x16x32_bf16 a[208:211], v[148:151], v[100:103], a[208:211]\n" \
"ds_read_b128 v[92:95], v48 offset:2048\n" \
"ds_read_b128 v[96:99], v49 offset:2048\n" \
"v_mfma_f32_16x16x32_bf16 a[212:215], v[156:159], v[100:103], a[212:215]\n" \
"s_add_u32 m0, %[mA], 0x9000\n" \
"s_mov_b32 s84, 0x9180\n" \
"v_mfma_f32_16x16x32_bf16 a[212:215], v[160:163], v[104:107], a[212:215]\n" \
"buffer_load_dwordx4 v24, s[76:79], s84 offen lds\n" \
"v_mfma_f32_16x16x32_bf16 a[216:219], v[168:171], v[104:107], a[216:219]\n" \
"v_mfma_f32_16x16x32_bf16 a[216:219], v[164:167], v[100:103], a[216:219]\n" \
"s_add_u32 m0, %[mA], 0x9400\n" \
"s_mov_b32 s85, 0xb580\n" \
"v_mfma_f32_16x16x32_bf16 a[220:223], v[172:175], v[100:103], a[220:223]\n" \
"buffer_load_dwordx4 v25, s[76:79], s85 offen lds\n" \
"v_mfma_f32_16x16x32_bf16 a[220:223], v[176:179], v[104:107], a[220:223]\n" \
"ds_read_b128 v[100:103], v48 offset:4096\n" \
"ds_read_b128 v[104:107], v49 offset:4096\n" \
"v_mfma_f32_16x16x32_bf16 a[240:243], v[152:155], v[112:115], a[240:243]\n" \
"ds_read_b64_tr_b16 v[152:153], v32 offset:8192\n" \
"ds_read_b64_tr_b16 v[154:155], v32 offset:9216\n" \
"v_mfma_f32_16x16x32_bf16 a[240:243], v[148:151], v[108:111], a[240:243]\n" \
"s_add_u32 m0, %[mB], 0x8800\n" \
"s_mov_b32 s84, 0x190000\n" \
"v_mfma_f32_16x16x32_bf16 a[244:247], v[156:159], v[108:111], a[244:247]\n" \
"buffer_load_dwordx4 v27, s[68:71], s84 offen lds\n" \
"v_mfma_f32_16x16x32_bf16 a[244:247], v[160:163], v[112:115], a[244:247]\n" \
"ds_read_b64_tr_b16 v[148:149], v32 offset:0\n" \
"ds_read_b64_tr_b16 v[150:151], v32 offset:1024\n" \
"v_mfma_f32_16x16x32_bf16 a[248:251], v[168:171], v[112:115], a[248:251]\n" \
"ds_read_b64_tr_b16 v[160:161], v33 offset:8192\n" \
"ds_read_b64_tr_b16 v[162:163], v33 offset:9216\n" \
"v_mfma_f32_16x16x32_bf16 a[248:251], v[164:167], v[108:111], a[248:251]\n" \
"ds_read_b64_tr_b16 v[156:157], v33 offset:0\n" \
"ds_read_b64_tr_b16 v[158:159], v33 offset:1024\n" \
"v_mfma_f32_16x16x32_bf16 a[252:255], v[172:175], v[108:111], a[252:255]\n" \
"ds_read_b64_tr_b16 v[164:165], v34 offset:0\n" \
"ds_read_b64_tr_b16 v[166:167], v34 offset:1024\n" \
"v_mfma_f32_16x16x32_bf16 a[252:255], v[176:179], v[112:115], a[252:255]\n" \
"s_add_u32 m0, %[mA], 0x9800\n" \
"s_waitcnt lgkmcnt(14)\n" \
"s_nop 0\n" \
"v_mfma_f32_16x16x32_bf16 a[16:19], v[144:147], v[88:91], a[16:19]\n" \
"s_mov_b32 s84, 0xd980\n" \
"ds_read_b64_tr_b16 v[168:169], v34 offset:8192\n" \
"v_mfma_f32_16x16x32_bf16 a[16:19], v[140:143], v[84:87], a[16:19]\n" \
"buffer_load_dwordx4 v24, s[76:79], s84 offen lds\n" \
"v_mfma_f32_16x16x32_bf16 a[48:51], v[124:127], v[84:87], a[48:51]\n" \
"s_add_u32 m0, %[mA], 0x9c00\n" \
"s_mov_b32 s85, 0xfd80\n" \
"v_mfma_f32_16x16x32_bf16 a[48:51], v[128:131], v[88:91], a[48:51]\n" \
"ds_read_b64_tr_b16 v[170:171], v34 offset:9216\n" \
"ds_read_b64_tr_b16 v[176:177], v35 offset:8192\n" \
"v_mfma_f32_16x16x32_bf16 a[80:83], v[88:91], v[136:139], a[80:83]\n" \
"buffer_load_dwordx4 v25, s[76:79], s85 offen lds\n" \
"v_mfma_f32_16x16x32_bf16 a[80:83], v[84:87], v[132:135], a[80:83]\n" \
"s_add_u32 m0, %[mB], 0x8c00\n" \
"s_mov_b32 s84, 0x198000\n" \
"v_mfma_f32_16x16x32_bf16 a[112:115], v[140:143], v[52:55], a[112:115]\n" \
"ds_read_b64_tr_b16 v[178:179], v35 offset:9216\n" \
"ds_read_b64_tr_b16 v[172:173], v35 offset:0\n" \
"v_mfma_f32_16x16x32_bf16 a[112:115], v[144:147], v[56:59], a[112:115]\n" \
"buffer_load_dwordx4 v27, s[68:71], s84 offen lds\n" \
"v_mfma_f32_16x16x32_bf16 a[20:23], v[128:131], v[56:59], a[20:23]\n" \
"ds_read_b64_tr_b16 v[174:175], v35 offset:1024\n" \
"ds_read_b128 v[84:87], v48 offset:0\n" \
"v_mfma_f32_16x16x32_bf16 a[20:23], v[124:127], v[52:55], a[20:23]\n" \
"s_add_u32 s89, s44, 0x1000\n" \
"ds_read_b128 v[88:91], v49 offset:0\n" \
"v_mfma_f32_16x16x32_bf16 a[52:55], v[116:119], v[52:55], a[52:55]\n" \
"buffer_store_dwordx4 v[212:215], v20, s[80:83], s89 offen offset:3072 sc1\n" \
"v_mfma_f32_16x16x32_bf16 a[52:55], v[120:123], v[56:59], a[52:55]\n" \
"ds_read_b128 v[108:111], v48 offset:6144\n" \
"ds_read_b128 v[112:115], v49 offset:6144\n" \
"v_mfma_f32_16x16x32_bf16 a[84:87], v[56:59], v[136:139], a[84:87]\n" \
"s_add_u32 s94, %[b], %[w]\n" \
"s_and_b32 s96, 7, %[b]\n" \
"v_mfma_f32_16x16x32_bf16 a[84:87], v[52:55], v[132:135], a[84:87]\n" \
"s_cmp_lg_u32 s94, 0\n" \
"s_cselect_b32 s95, 0x40000000, 0x5bc0000\n" \
"v_mfma_f32_16x16x32_bf16 a[116:119], v[140:143], v[60:63], a[116:119]\n" \
"s_mul_i32 s97, 0x48, s96\n" \
"s_add_u32 s98, 0x47, s97\n" \
"v_mfma_f32_16x16x32_bf16 a[116:119], v[144:147], v[64:67], a[116:119]\n" \
"v_writelane_b32 v11, s95, 50\n" \
"s_lshr_b32 s94, %[b], 3\n" \
"v_mfma_f32_16x16x32_bf16 a[24:27], v[128:131], v[64:67], a[24:27]\n" \
"s_cmp_lt_u32 %[w], 3\n" \
"s_cselect_b32 s99, s98, 0xfff\n" \
"v_mfma_f32_16x16x32_bf16 a[24:27], v[124:127], v[60:63], a[24:27]\n" \
"s_lshl_b32 s67, s96, 2\n" \
"s_and_b32 s95, 3, s94\n" \
"v_mfma_f32_16x16x32_bf16 a[56:59], v[116:119], v[60:63], a[56:59]\n" \
"s_mul_i32 s100, 0x480, s99\n" \
"s_lshl_b32 s64, %[w], 5\n" \
"v_mfma_f32_16x16x32_bf16 a[56:59], v[120:123], v[64:67], a[56:59]\n" \
"s_add_u32 s88, s42, 0x0\n" \
"s_add_u32 s98, s67, s95\n" \
"v_mfma_f32_16x16x32_bf16 a[88:91], v[64:67], v[136:139], a[88:91]\n" \
"buffer_store_dwordx4 v[216:219], v20, s[80:83], s88 offen offset:0 sc1\n" \
"v_mfma_f32_16x16x32_bf16 a[88:91], v[60:63], v[132:135], a[88:91]\n" \
"s_add_u32 s101, 0x120, s100\n" \
"s_add_u32 s65, s97, s64\n" \
"v_mfma_f32_16x16x32_bf16 a[120:123], v[140:143], v[68:71], a[120:123]\n" \
"s_mul_i32 s99, 0x24000, s98\n" \
"v_writelane_b32 v11, s101, 15\n" \
"v_mfma_f32_16x16x32_bf16 a[120:123], v[144:147], v[72:75], a[120:123]\n" \
"s_mul_i32 s66, 0x480, s65\n" \
"s_add_u32 s100, 0x1b000, s99\n" \
"v_mfma_f32_16x16x32_bf16 a[28:31], v[128:131], v[72:75], a[28:31]\n" \
"s_cmp_lt_u32 %[w], 3\n" \
"s_cselect_b32 s101, s66, s100\n" \
"v_mfma_f32_16x16x32_bf16 a[28:31], v[124:127], v[68:71], a[28:31]\n" \
"s_min_u32 s97, 2, %[w]\n" \
"s_mul_i32 s64, 0x2400, s97\n" \
"v_mfma_f32_16x16x32_bf16 a[60:63], v[116:119], v[68:71], a[60:63]\n" \
"v_writelane_b32 v11, s101, 16\n" \
"s_add_u32 s65, 0x20000, s64\n" \
"v_mfma_f32_16x16x32_bf16 a[60:63], v[120:123], v[72:75], a[60:63]\n" \
"s_mul_i32 s94, 0x9000, s97\n" \
"s_mul_i32 s95, 0x2400, %[w]\n" \
"v_mfma_f32_16x16x32_bf16 a[92:95], v[72:75], v[136:139], a[92:95]\n" \
"v_writelane_b32 v11, s65, 17\n" \
"s_add_u32 s67, s99, s94\n" \
"v_mfma_f32_16x16x32_bf16 a[92:95], v[68:71], v[132:135], a[92:95]\n" \
"s_add_u32 s89, s42, 0x1000\n" \
"v_writelane_b32 v11, s67, 22\n" \
"v_mfma_f32_16x16x32_bf16 a[124:127], v[140:143], v[76:79], a[124:127]\n" \
"buffer_store_dwordx4 v[220:223], v20, s[80:83], s89 offen offset:0 sc1\n" \
"v_mfma_f32_16x16x32_bf16 a[124:127], v[144:147], v[80:83], a[124:127]\n" \
"s_cmp_lt_u32 %[w], 3\n" \
"s_cselect_b32 s98, s95, 0x12000\n" \
"v_mfma_f32_16x16x32_bf16 a[0:3], v[128:131], v[80:83], a[0:3]\n" \
"s_add_u32 s66, 0x20000, s95\n" \
"v_writelane_b32 v11, s98, 18\n" \
"v_mfma_f32_16x16x32_bf16 a[0:3], v[124:127], v[76:79], a[0:3]\n" \
"s_cmp_lt_u32 %[w], 3\n" \
"s_cselect_b32 s100, s66, 0xfc00\n" \
"v_mfma_f32_16x16x32_bf16 a[4:7], v[116:119], v[76:79], a[4:7]\n" \
"v_writelane_b32 v11, s95, 19\n" \
"s_lshl_b32 s101, %[w], 10\n" \
"v_mfma_f32_16x16x32_bf16 a[4:7], v[120:123], v[80:83], a[4:7]\n" \
"s_lshr_b32 s65, s96, 1\n" \
"v_writelane_b32 v11, s100, 20\n" \
"v_mfma_f32_16x16x32_bf16 a[32:35], v[80:83], v[136:139], a[32:35]\n" \
"s_cmp_eq_u32 %[w], s65\n" \
"s_cselect_b32 s97, 0xff0000, 0\n" \
"v_mfma_f32_16x16x32_bf16 a[32:35], v[76:79], v[132:135], a[32:35]\n" \
"s_add_u32 s64, 0x1d400, s101\n" \
"s_waitcnt lgkmcnt(0)\n" \
"s_nop 0\n" \
"v_mfma_f32_16x16x32_bf16 a[128:131], v[148:151], v[52:55], a[128:131]\n" \
"v_writelane_b32 v11, s64, 21\n" \
"v_writelane_b32 v11, s97, 6\n" \
"v_mfma_f32_16x16x32_bf16 a[128:131], v[152:155], v[56:59], a[128:131]\n" \
"v_mfma_f32_16x16x32_bf16 a[132:135], v[160:163], v[56:59], a[132:135]\n" \
"v_mfma_f32_16x16x32_bf16 a[132:135], v[156:159], v[52:55], a[132:135]\n" \
"v_mfma_f32_16x16x32_bf16 a[136:139], v[164:167], v[52:55], a[136:139]\n" \
"v_mfma_f32_16x16x32_bf16 a[136:139], v[168:171], v[56:59], a[136:139]\n" \
"v_mfma_f32_16x16x32_bf16 a[140:143], v[176:179], v[56:59], a[140:143]\n" \
"v_mfma_f32_16x16x32_bf16 a[140:143], v[172:175], v[52:55], a[140:143]\n" \
"v_mfma_f32_16x16x32_bf16 a[160:163], v[148:151], v[60:63], a[160:163]\n" \
"v_mfma_f32_16x16x32_bf16 a[160:163], v[152:155], v[64:67], a[160:163]\n" \
"v_mfma_f32_16x16x32_bf16 a[164:167], v[160:163], v[64:67], a[164:167]\n" \
"v_mfma_f32_16x16x32_bf16 a[164:167], v[156:159], v[60:63], a[164:167]\n" \
"v_mfma_f32_16x16x32_bf16 a[168:171], v[164:167], v[60:63], a[168:171]\n" \
"v_mfma_f32_16x16x32_bf16 a[168:171], v[168:171], v[64:67], a[168:171]\n" \
"v_mfma_f32_16x16x32_bf16 a[172:175], v[176:179], v[64:67], a[172:175]\n" \
"v_mfma_f32_16x16x32_bf16 a[172:175], v[172:175], v[60:63], a[172:175]\n" \
"v_mfma_f32_16x16x32_bf16 a[192:195], v[148:151], v[68:71], a[192:195]\n" \
"v_mfma_f32_16x16x32_bf16 a[192:195], v[152:155], v[72:75], a[192:195]\n" \
"s_waitcnt vmcnt(3)\n" \
"s_barrier\n" \
"v_mfma_f32_16x16x32_bf16 a[196:199], v[160:163], v[72:75], a[196:199]\n" \
"s_add_u32 m0, s40, 0x0\n" \
"s_nop 0\n" \
"s_add_u32 s85, s37, 0x8000\n" \
"v_mfma_f32_16x16x32_bf16 a[196:199], v[156:159], v[68:71], a[196:199]\n" \
"buffer_load_dwordx4 v20, s[80:83], s85 offen sc1 lds\n" \
"v_mfma_f32_16x16x32_bf16 a[200:203], v[164:167], v[68:71], a[200:203]\n" \
"s_add_u32 m0, s40, 0x1000\n" \
"s_add_u32 s84, s37, 0x9000\n" \
"v_mfma_f32_16x16x32_bf16 a[200:203], v[168:171], v[72:75], a[200:203]\n" \
"ds_read_b64_tr_b16 v[140:141], v39 offset:32768\n" \
"ds_read_b64_tr_b16 v[142:143], v39 offset:33280\n" \
"v_mfma_f32_16x16x32_bf16 a[204:207], v[176:179], v[72:75], a[204:207]\n" \
"buffer_load_dwordx4 v20, s[80:83], s84 offen sc1 lds\n" \
"v_mfma_f32_16x16x32_bf16 a[204:207], v[172:175], v[68:71], a[204:207]\n" \
"ds_read_b64_tr_b16 v[144:145], v39 offset:36864\n" \
"ds_read_b64_tr_b16 v[146:147], v39 offset:37376\n" \
"v_mfma_f32_16x16x32_bf16 a[224:227], v[148:151], v[76:79], a[224:227]\n" \
"s_add_u32 m0, s40, 0x3000\n" \
"s_mov_b32 s85, 0x200\n" \
"v_mfma_f32_16x16x32_bf16 a[224:227], v[152:155], v[80:83], a[224:227]\n" \
"buffer_load_dwordx4 v21, s[76:79], s85 offen lds\n" \
"v_mfma_f32_16x16x32_bf16 a[228:231], v[160:163], v[80:83], a[228:231]\n" \
"ds_read_b64_tr_b16 v[128:129], v37 offset:36864\n" \
"ds_read_b64_tr_b16 v[130:131], v37 offset:37376\n" \
"v_mfma_f32_16x16x32_bf16 a[228:231], v[156:159], v[76:79], a[228:231]\n" \
"ds_read_b64_tr_b16 v[124:125], v37 offset:32768\n" \
"ds_read_b64_tr_b16 v[126:127], v37 offset:33280\n" \
"v_mfma_f32_16x16x32_bf16 a[232:235], v[164:167], v[76:79], a[232:235]\n" \
"s_add_u32 m0, %[mA], 0x0\n" \
"s_nop 0\n" \
"s_mov_b32 s84, 0x200\n" \
"v_mfma_f32_16x16x32_bf16 a[232:235], v[168:171], v[80:83], a[232:235]\n" \
"buffer_load_dwordx4 v24, s[76:79], s84 offen lds\n" \
"v_mfma_f32_16x16x32_bf16 a[236:239], v[176:179], v[80:83], a[236:239]\n" \
"ds_read_b64_tr_b16 v[132:133], v38 offset:32768\n" \
"ds_read_b64_tr_b16 v[134:135], v38 offset:33280\n" \
"v_mfma_f32_16x16x32_bf16 a[236:239], v[172:175], v[76:79], a[236:239]\n" \
"s_add_u32 m0, %[mA], 0x400\n" \
"s_mov_b32 s85, 0x2600\n" \
"v_mfma_f32_16x16x32_bf16 a[144:147], v[148:151], v[84:87], a[144:147]\n" \
"buffer_load_dwordx4 v25, s[76:79], s85 offen lds\n" \
"v_mfma_f32_16x16x32_bf16 a[144:147], v[152:155], v[88:91], a[144:147]\n" \
"ds_read_b64_tr_b16 v[136:137], v38 offset:36864\n" \
"ds_read_b64_tr_b16 v[138:139], v38 offset:37376\n" \
"v_mfma_f32_16x16x32_bf16 a[148:151], v[160:163], v[88:91], a[148:151]\n" \
"ds_read_b128 v[56:59], v29 offset:32768\n" \
"ds_read_b128 v[52:55], v28 offset:32768\n" \
"v_mfma_f32_16x16x32_bf16 a[148:151], v[156:159], v[84:87], a[148:151]\n" \
"s_add_u32 m0, %[mB], 0x0\n" \
"s_nop 0\n" \
"s_mov_b32 s84, 0x200000\n" \
"v_mfma_f32_16x16x32_bf16 a[152:155], v[164:167], v[84:87], a[152:155]\n" \
"buffer_load_dwordx4 v26, s[68:71], s84 offen lds\n" \
"v_mfma_f32_16x16x32_bf16 a[152:155], v[168:171], v[88:91], a[152:155]\n" \
"ds_read_b64_tr_b16 v[120:121], v36 offset:36864\n" \
"L_pf6:\n" \
"s_cbranch_execz L_pf10\n" \
"s_nop 0\n" \
"ds_read_b64_tr_b16 v[122:123], v36 offset:37376\n" \
"v_mfma_f32_16x16x32_bf16 a[156:159], v[176:179], v[88:91], a[156:159]\n" \
"ds_read_b128 v[88:91], v23 offset:32768\n" \
"ds_read_b64_tr_b16 v[116:117], v36 offset:32768\n" \
"v_mfma_f32_16x16x32_bf16 a[156:159], v[172:175], v[84:87], a[156:159]\n" \
"s_add_u32 m0, %[mA], 0x800\n" \
"s_mov_b32 s85, 0x4a00\n" \
"v_mfma_f32_16x16x32_bf16 a[176:179], v[148:151], v[92:95], a[176:179]\n" \
"buffer_load_dwordx4 v24, s[76:79], s85 offen lds\n" \
"v_mfma_f32_16x16x32_bf16 a[176:179], v[152:155], v[96:99], a[176:179]\n" \
"ds_read_b128 v[84:87], v22 offset:32768\n" \
"ds_read_b64_tr_b16 v[118:119], v36 offset:33280\n" \
"v_mfma_f32_16x16x32_bf16 a[180:183], v[160:163], v[96:99], a[180:183]\n" \
"s_add_u32 m0, %[mA], 0xc00\n" \
"s_mov_b32 s84, 0x6e00\n" \
"v_mfma_f32_16x16x32_bf16 a[180:183], v[156:159], v[92:95], a[180:183]\n" \
"buffer_load_dwordx4 v25, s[76:79], s84 offen lds\n" \
"v_mfma_f32_16x16x32_bf16 a[184:187], v[164:167], v[92:95], a[184:187]\n" \
"ds_read_b128 v[64:67], v29 offset:34816\n" \
"ds_read_b128 v[60:63], v28 offset:34816\n" \
"v_mfma_f32_16x16x32_bf16 a[184:187], v[168:171], v[96:99], a[184:187]\n" \
"ds_read_b128 v[72:75], v29 offset:36864\n" \
"ds_read_b128 v[68:71], v28 offset:36864\n" \
"v_mfma_f32_16x16x32_bf16 a[188:191], v[176:179], v[96:99], a[188:191]\n" \
"s_add_u32 m0, %[mB], 0x400\n" \
"s_mov_b32 s85, 0x208000\n" \
"v_mfma_f32_16x16x32_bf16 a[188:191], v[172:175], v[92:95], a[188:191]\n" \
"buffer_load_dwordx4 v26, s[68:71], s85 offen lds\n" \
"v_mfma_f32_16x16x32_bf16 a[208:211], v[148:151], v[100:103], a[208:211]\n" \
"ds_read_b128 v[80:83], v29 offset:38912\n" \
"ds_read_b128 v[76:79], v28 offset:38912\n" \
"v_mfma_f32_16x16x32_bf16 a[208:211], v[152:155], v[104:107], a[208:211]\n" \
"ds_read_b128 v[92:95], v48 offset:34816\n" \
"ds_read_b128 v[96:99], v49 offset:34816\n" \
"v_mfma_f32_16x16x32_bf16 a[212:215], v[160:163], v[104:107], a[212:215]\n" \
"s_add_u32 m0, %[mA], 0x1000\n" \
"s_mov_b32 s84, 0x9200\n" \
"v_mfma_f32_16x16x32_bf16 a[212:215], v[156:159], v[100:103], a[212:215]\n" \
"buffer_load_dwordx4 v24, s[76:79], s84 offen lds\n" \
"v_mfma_f32_16x16x32_bf16 a[216:219], v[164:167], v[100:103], a[216:219]\n" \
"v_mfma_f32_16x16x32_bf16 a[216:219], v[168:171], v[104:107], a[216:219]\n" \
"s_add_u32 m0, %[mA], 0x1400\n" \
"s_mov_b32 s85, 0xb600\n" \
"v_mfma_f32_16x16x32_bf16 a[220:223], v[176:179], v[104:107], a[220:223]\n" \
"buffer_load_dwordx4 v25, s[76:79], s85 offen lds\n" \
"v_mfma_f32_16x16x32_bf16 a[220:223], v[172:175], v[100:103], a[220:223]\n" \
"ds_read_b128 v[100:103], v48 offset:36864\n" \
"ds_read_b128 v[104:107], v49 offset:36864\n" \
"v_mfma_f32_16x16x32_bf16 a[240:243], v[148:151], v[108:111], a[240:243]\n" \
"ds_read_b64_tr_b16 v[148:149], v32 offset:32768\n" \
"ds_read_b64_tr_b16 v[150:151], v32 offset:33792\n" \
"v_mfma_f32_16x16x32_bf16 a[240:243], v[152:155], v[112:115], a[240:243]\n" \
"s_add_u32 m0, %[mB], 0x800\n" \
"s_mov_b32 s84, 0x210000\n" \
"v_mfma_f32_16x16x32_bf16 a[244:247], v[160:163], v[112:115], a[244:247]\n" \
"buffer_load_dwordx4 v27, s[68:71], s84 offen lds\n" \
"v_mfma_f32_16x16x32_bf16 a[244:247], v[156:159], v[108:111], a[244:247]\n" \
"ds_read_b64_tr_b16 v[152:153], v32 offset:40960\n" \
"ds_read_b64_tr_b16 v[154:155], v32 offset:41984\n" \
"v_mfma_f32_16x16x32_bf16 a[248:251], v[164:167], v[108:111], a[248:251]\n" \
"ds_read_b64_tr_b16 v[156:157], v33 offset:32768\n" \
"ds_read_b64_tr_b16 v[158:159], v33 offset:33792\n" \
"v_mfma_f32_16x16x32_bf16 a[248:251], v[168:171], v[112:115], a[248:251]\n" \
"ds_read_b64_tr_b16 v[160:161], v33 offset:40960\n" \
"ds_read_b64_tr_b16 v[162:163], v33 offset:41984\n" \
"v_mfma_f32_16x16x32_bf16 a[252:255], v[176:179], v[112:115], a[252:255]\n" \
"ds_read_b64_tr_b16 v[168:169], v34 offset:40960\n" \
"ds_read_b64_tr_b16 v[170:171], v34 offset:41984\n" \
"v_mfma_f32_16x16x32_bf16 a[252:255], v[172:175], v[108:111], a[252:255]\n" \
"s_add_u32 m0, %[mA], 0x1800\n" \
"s_waitcnt lgkmcnt(14)\n" \
"s_nop 0\n" \
"v_mfma_f32_16x16x32_bf16 a[16:19], v[140:143], v[84:87], a[16:19]\n" \
"s_mov_b32 s84, 0xda00\n" \
"ds_read_b64_tr_b16 v[164:165], v34 offset:32768\n" \
"v_mfma_f32_16x16x32_bf16 a[16:19], v[144:147], v[88:91], a[16:19]\n" \
"buffer_load_dwordx4 v24, s[76:79], s84 offen lds\n" \
"v_mfma_f32_16x16x32_bf16 a[48:51], v[128:131], v[88:91], a[48:51]\n" \
"s_add_u32 m0, %[mA], 0x1c00\n" \
"s_mov_b32 s85, 0xfe00\n" \
"v_mfma_f32_16x16x32_bf16 a[48:51], v[124:127], v[84:87], a[48:51]\n" \
"ds_read_b64_tr_b16 v[166:167], v34 offset:33792\n" \
"ds_read_b64_tr_b16 v[172:173], v35 offset:32768\n" \
"v_mfma_f32_16x16x32_bf16 a[80:83], v[84:87], v[132:135], a[80:83]\n" \
"buffer_load_dwordx4 v25, s[76:79], s85 offen lds\n" \
"v_mfma_f32_16x16x32_bf16 a[80:83], v[88:91], v[136:139], a[80:83]\n" \
"s_add_u32 m0, %[mB], 0xc00\n" \
"s_mov_b32 s84, 0x218000\n" \
"v_mfma_f32_16x16x32_bf16 a[112:115], v[144:147], v[56:59], a[112:115]\n" \
"ds_read_b64_tr_b16 v[174:175], v35 offset:33792\n" \
"ds_read_b64_tr_b16 v[176:177], v35 offset:40960\n" \
"v_mfma_f32_16x16x32_bf16 a[112:115], v[140:143], v[52:55], a[112:115]\n" \
"buffer_load_dwordx4 v27, s[68:71], s84 offen lds\n" \
"v_mfma_f32_16x16x32_bf16 a[20:23], v[124:127], v[52:55], a[20:23]\n" \
"ds_read_b64_tr_b16 v[178:179], v35 offset:41984\n" \
"ds_read_b128 v[84:87], v48 offset:32768\n" \
"v_mfma_f32_16x16x32_bf16 a[20:23], v[128:131], v[56:59], a[20:23]\n" \
"s_add_u32 s88, s42, 0x2000\n" \
"ds_read_b128 v[88:91], v49 offset:32768\n" \
"v_mfma_f32_16x16x32_bf16 a[52:55], v[120:123], v[56:59], a[52:55]\n" \
"buffer_store_dwordx4 v[224:227], v20, s[80:83], s88 offen offset:0 sc1\n" \
"v_mfma_f32_16x16x32_bf16 a[52:55], v[116:119], v[52:55], a[52:55]\n" \
"ds_read_b128 v[108:111], v48 offset:38912\n" \
"ds_read_b128 v[112:115], v49 offset:38912\n" \
"v_mfma_f32_16x16x32_bf16 a[84:87], v[52:55], v[132:135], a[84:87]\n" \
"s_lshr_b32 s96, %[b], 3\n" \
"s_and_b32 s97, 3, s96\n" \
"v_mfma_f32_16x16x32_bf16 a[84:87], v[56:59], v[136:139], a[84:87]\n" \
"s_and_b32 s94, 7, %[b]\n" \
"s_add_u32 s98, 1, s97\n" \
"v_mfma_f32_16x16x32_bf16 a[116:119], v[144:147], v[64:67], a[116:119]\n" \
"s_lshl_b32 s95, s94, 2\n" \
"s_and_b32 s99, 3, s98\n" \
"v_mfma_f32_16x16x32_bf16 a[116:119], v[140:143], v[60:63], a[116:119]\n" \
"s_add_u32 s100, s95, s99\n" \
"s_mul_i32 s101, 0x24000, s100\n" \
"v_mfma_f32_16x16x32_bf16 a[24:27], v[124:127], v[60:63], a[24:27]\n" \
"s_mul_i32 s64, 0x9000, %[w]\n" \
"s_and_b32 s96, 2, %[w]\n" \
"v_mfma_f32_16x16x32_bf16 a[24:27], v[128:131], v[64:67], a[24:27]\n" \
"s_add_u32 s65, s101, s64\n" \
"s_lshr_b32 s66, %[b], 5\n" \
"v_mfma_f32_16x16x32_bf16 a[56:59], v[120:123], v[64:67], a[56:59]\n" \
"s_lshl_b32 s98, s96, 1\n" \
"s_and_b32 s99, 1, %[w]\n" \
"v_mfma_f32_16x16x32_bf16 a[56:59], v[116:119], v[60:63], a[56:59]\n" \
"s_add_u32 s89, s42, 0x3000\n" \
"v_writelane_b32 v11, s65, 23\n" \
"v_mfma_f32_16x16x32_bf16 a[88:91], v[60:63], v[132:135], a[88:91]\n" \
"buffer_store_dwordx4 v[228:231], v20, s[80:83], s89 offen offset:0 sc1\n" \
"v_mfma_f32_16x16x32_bf16 a[88:91], v[64:67], v[136:139], a[88:91]\n" \
"s_lshl_b32 s67, s66, 3\n" \
"s_add_u32 s101, s98, s99\n" \
"v_mfma_f32_16x16x32_bf16 a[120:123], v[144:147], v[72:75], a[120:123]\n" \
"s_add_u32 s65, s67, s101\n" \
"s_mul_i32 s66, 0x24800, s65\n" \
"v_mfma_f32_16x16x32_bf16 a[120:123], v[140:143], v[68:71], a[120:123]\n" \
"s_lshl_b32 s98, s100, 12\n" \
"s_add_u32 s96, 0x3000000, s66\n" \
"v_mfma_f32_16x16x32_bf16 a[28:31], v[124:127], v[68:71], a[28:31]\n" \
"s_add_u32 s99, 0x4800, s98\n" \
"s_add_u32 s65, s96, s99\n" \
"v_mfma_f32_16x16x32_bf16 a[28:31], v[128:131], v[72:75], a[28:31]\n" \
"s_sub_u32 s100, 0x78, s67\n" \
"v_writelane_b32 v11, s65, 26\n" \
"v_mfma_f32_16x16x32_bf16 a[60:63], v[120:123], v[72:75], a[60:63]\n" \
"s_add_u32 s98, s100, s101\n" \
"s_mul_i32 s65, 0x24800, s98\n" \
"v_mfma_f32_16x16x32_bf16 a[60:63], v[116:119], v[68:71], a[60:63]\n" \
"s_add_u32 s67, 0x3000000, s65\n" \
"s_add_u32 s101, 2, s97\n" \
"v_mfma_f32_16x16x32_bf16 a[92:95], v[68:71], v[132:135], a[92:95]\n" \
"s_add_u32 s100, s67, s99\n" \
"s_and_b32 s98, 3, s101\n" \
"v_mfma_f32_16x16x32_bf16 a[92:95], v[72:75], v[136:139], a[92:95]\n" \
"s_add_u32 s88, s42, 0x0\n" \
"v_writelane_b32 v11, s100, 29\n" \
"v_mfma_f32_16x16x32_bf16 a[124:127], v[144:147], v[80:83], a[124:127]\n" \
"buffer_store_dwordx4 v[232:235], v20, s[80:83], s88 offen offset:1024 sc1\n" \
"v_mfma_f32_16x16x32_bf16 a[124:127], v[140:143], v[76:79], a[124:127]\n" \
"s_add_u32 s99, s95, s98\n" \
"s_mul_i32 s100, 0x24000, s99\n" \
"v_mfma_f32_16x16x32_bf16 a[0:3], v[124:127], v[76:79], a[0:3]\n" \
"s_add_u32 s101, s100, s64\n" \
"s_lshl_b32 s98, s99, 12\n" \
"v_mfma_f32_16x16x32_bf16 a[0:3], v[128:131], v[80:83], a[0:3]\n" \
"v_writelane_b32 v11, s101, 24\n" \
"s_add_u32 s100, 0x4800, s98\n" \
"v_mfma_f32_16x16x32_bf16 a[4:7], v[120:123], v[80:83], a[4:7]\n" \
"s_add_u32 s101, s96, s100\n" \
"v_writelane_b32 v11, s101, 27\n" \
"v_mfma_f32_16x16x32_bf16 a[4:7], v[116:119], v[76:79], a[4:7]\n" \
"s_add_u32 s98, 3, s97\n" \
"s_add_u32 s99, s67, s100\n" \
"v_mfma_f32_16x16x32_bf16 a[32:35], v[76:79], v[132:135], a[32:35]\n" \
"s_and_b32 s101, 3, s98\n" \
"v_writelane_b32 v11, s99, 30\n" \
"v_mfma_f32_16x16x32_bf16 a[32:35], v[80:83], v[136:139], a[32:35]\n" \
"s_add_u32 s100, s95, s101\n" \
"s_waitcnt lgkmcnt(0)\n" \
"v_mfma_f32_16x16x32_bf16 a[128:131], v[152:155], v[56:59], a[128:131]\n" \
"s_mul_i32 s99, 0x24000, s100\n" \
"s_lshl_b32 s98, s100, 12\n" \
"v_mfma_f32_16x16x32_bf16 a[128:131], v[148:151], v[52:55], a[128:131]\n" \
"s_add_u32 s97, s99, s64\n" \
"s_add_u32 s95, 0x4800, s98\n" \
"v_mfma_f32_16x16x32_bf16 a[132:135], v[156:159], v[52:55], a[132:135]\n" \
"v_writelane_b32 v11, s97, 25\n" \
"s_add_u32 s101, s96, s95\n" \
"v_mfma_f32_16x16x32_bf16 a[132:135], v[160:163], v[56:59], a[132:135]\n" \
"v_writelane_b32 v11, s101, 28\n" \
"s_add_u32 s99, s67, s95\n" \
"v_mfma_f32_16x16x32_bf16 a[136:139], v[168:171], v[56:59], a[136:139]\n" \
"s_lshl_b32 s64, s94, 11\n" \
"v_writelane_b32 v11, s99, 31\n" \
"v_mfma_f32_16x16x32_bf16 a[136:139], v[164:167], v[52:55], a[136:139]\n" \
"s_add_u32 s97, s96, s64\n" \
"v_writelane_b32 v11, s97, 32\n" \
"v_mfma_f32_16x16x32_bf16 a[140:143], v[172:175], v[52:55], a[140:143]\n" \
"s_and_b32 s99, 1, s94\n" \
"s_add_u32 s100, s67, s64\n" \
"v_mfma_f32_16x16x32_bf16 a[140:143], v[176:179], v[56:59], a[140:143]\n" \
"s_cmp_lg_u32 s99, 0\n" \
"s_cselect_b32 s97, s67, s96\n" \
"v_mfma_f32_16x16x32_bf16 a[160:163], v[152:155], v[64:67], a[160:163]\n" \
"v_writelane_b32 v11, s100, 33\n" \
"s_add_u32 s98, 0x3004000, s66\n" \
"v_mfma_f32_16x16x32_bf16 a[160:163], v[148:151], v[60:63], a[160:163]\n" \
"s_lshr_b32 s95, s94, 1\n" \
"s_add_u32 s64, 0x4000, s97\n" \
"v_mfma_f32_16x16x32_bf16 a[164:167], v[156:159], v[60:63], a[164:167]\n" \
"v_writelane_b32 v11, s98, 34\n" \
"s_add_u32 s101, 0x3004000, s65\n" \
"v_mfma_f32_16x16x32_bf16 a[164:167], v[160:163], v[64:67], a[164:167]\n" \
"s_cmp_eq_u32 %[w], s95\n" \
"s_cselect_b32 s100, s64, 0x40000000\n" \
"v_mfma_f32_16x16x32_bf16 a[168:171], v[168:171], v[64:67], a[168:171]\n" \
"v_writelane_b32 v11, s101, 35\n" \
"v_writelane_b32 v11, s100, 36\n" \
"v_mfma_f32_16x16x32_bf16 a[168:171], v[164:167], v[60:63], a[168:171]\n" \
"v_mfma_f32_16x16x32_bf16 a[172:175], v[172:175], v[60:63], a[172:175]\n" \
"v_mfma_f32_16x16x32_bf16 a[172:175], v[176:179], v[64:67], a[172:175]\n" \
"v_mfma_f32_16x16x32_bf16 a[192:195], v[152:155], v[72:75], a[192:195]\n" \
"v_mfma_f32_16x16x32_bf16 a[192:195], v[148:151], v[68:71], a[192:195]\n" \
"s_waitcnt vmcnt(3)\n" \
"s_barrier\n" \
"v_mfma_f32_16x16x32_bf16 a[196:199], v[156:159], v[68:71], a[196:199]\n" \
"s_add_u32 m0, s40, 0x8000\n" \
"s_add_u32 s85, s37, 0xa000\n" \
"v_mfma_f32_16x16x32_bf16 a[196:199], v[160:163], v[72:75], a[196:199]\n" \
"buffer_load_dwordx4 v20, s[80:83], s85 offen sc1 lds\n" \
"v_mfma_f32_16x16x32_bf16 a[200:203], v[168:171], v[72:75], a[200:203]\n" \
"s_add_u32 m0, s40, 0x9000\n" \
"s_add_u32 s84, s37, 0xb000\n" \
"v_mfma_f32_16x16x32_bf16 a[200:203], v[164:167], v[68:71], a[200:203]\n" \
"ds_read_b64_tr_b16 v[144:145], v39 offset:4096\n" \
"ds_read_b64_tr_b16 v[146:147], v39 offset:4608\n" \
"v_mfma_f32_16x16x32_bf16 a[204:207], v[172:175], v[68:71], a[204:207]\n" \
"buffer_load_dwordx4 v20, s[80:83], s84 offen sc1 lds\n" \
"v_mfma_f32_16x16x32_bf16 a[204:207], v[176:179], v[72:75], a[204:207]\n" \
"ds_read_b64_tr_b16 v[140:141], v39 offset:0\n" \
"ds_read_b64_tr_b16 v[142:143], v39 offset:512\n" \
"v_mfma_f32_16x16x32_bf16 a[224:227], v[152:155], v[80:83], a[224:227]\n" \
"s_add_u32 m0, s40, 0xb000\n" \
"s_mov_b32 s85, 0x280\n" \
"v_mfma_f32_16x16x32_bf16 a[224:227], v[148:151], v[76:79], a[224:227]\n" \
"buffer_load_dwordx4 v21, s[76:79], s85 offen lds\n" \
"v_mfma_f32_16x16x32_bf16 a[228:231], v[156:159], v[76:79], a[228:231]\n" \
"ds_read_b64_tr_b16 v[124:125], v37 offset:0\n" \
"ds_read_b64_tr_b16 v[126:127], v37 offset:512\n" \
"v_mfma_f32_16x16x32_bf16 a[228:231], v[160:163], v[80:83], a[228:231]\n" \
"ds_read_b64_tr_b16 v[128:129], v37 offset:4096\n" \
"ds_read_b64_tr_b16 v[130:131], v37 offset:4608\n" \
"v_mfma_f32_16x16x32_bf16 a[232:235], v[168:171], v[80:83], a[232:235]\n" \
"s_add_u32 m0, %[mA], 0x8000\n" \
"s_mov_b32 s84, 0x280\n" \
"v_mfma_f32_16x16x32_bf16 a[232:235], v[164:167], v[76:79], a[232:235]\n" \
"buffer_load_dwordx4 v24, s[76:79], s84 offen lds\n" \
"v_mfma_f32_16x16x32_bf16 a[236:239], v[172:175], v[76:79], a[236:239]\n" \
"ds_read_b64_tr_b16 v[136:137], v38 offset:4096\n" \
"ds_read_b64_tr_b16 v[138:139], v38 offset:4608\n" \
"v_mfma_f32_16x16x32_bf16 a[236:239], v[176:179], v[80:83], a[236:239]\n" \
"s_add_u32 m0, %[mA], 0x8400\n" \
"s_mov_b32 s85, 0x2680\n" \
"v_mfma_f32_16x16x32_bf16 a[144:147], v[152:155], v[88:91], a[144:147]\n" \
"buffer_load_dwordx4 v25, s[76:79], s85 offen lds\n" \
"v_mfma_f32_16x16x32_bf16 a[144:147], v[148:151], v[84:87], a[144:147]\n" \
"ds_read_b64_tr_b16 v[132:133], v38 offset:0\n" \
"ds_read_b64_tr_b16 v[134:135], v38 offset:512\n" \
"v_mfma_f32_16x16x32_bf16 a[148:151], v[156:159], v[84:87], a[148:151]\n" \
"ds_read_b128 v[52:55], v28 offset:0\n" \
"ds_read_b128 v[56:59], v29 offset:0\n" \
"v_mfma_f32_16x16x32_bf16 a[148:151], v[160:163], v[88:91], a[148:151]\n" \
"s_add_u32 m0, %[mB], 0x8000\n" \
"s_mov_b32 s84, 0x280000\n" \
"v_mfma_f32_16x16x32_bf16 a[152:155], v[168:171], v[88:91], a[152:155]\n" \
"buffer_load_dwordx4 v26, s[68:71], s84 offen lds\n" \
"v_mfma_f32_16x16x32_bf16 a[152:155], v[164:167], v[84:87], a[152:155]\n" \
"ds_read_b64_tr_b16 v[116:117], v36 offset:0\n" \
"ds_read_b64_tr_b16 v[118:119], v36 offset:512\n" \
"v_mfma_f32_16x16x32_bf16 a[156:159], v[172:175], v[84:87], a[156:159]\n" \
"ds_read_b128 v[84:87], v22 offset:0\n" \
"ds_read_b64_tr_b16 v[120:121], v36 offset:4096\n" \
"v_mfma_f32_16x16x32_bf16 a[156:159], v[176:179], v[88:91], a[156:159]\n" \
"s_add_u32 m0, %[mA], 0x8800\n" \
"s_mov_b32 s85, 0x4a80\n" \
"v_mfma_f32_16x16x32_bf16 a[176:179], v[152:155], v[96:99], a[176:179]\n" \
"buffer_load_dwordx4 v24, s[76:79], s85 offen lds\n" \
"v_mfma_f32_16x16x32_bf16 a[176:179], v[148:151], v[92:95], a[176:179]\n" \
"ds_read_b128 v[88:91], v23 offset:0\n" \
"ds_read_b64_tr_b16 v[122:123], v36 offset:4608\n" \
"v_mfma_f32_16x16x32_bf16 a[180:183], v[156:159], v[92:95], a[180:183]\n" \
"s_add_u32 m0, %[mA], 0x8c00\n" \
"s_mov_b32 s84, 0x6e80\n" \
"v_mfma_f32_16x16x32_bf16 a[180:183], v[160:163], v[96:99], a[180:183]\n" \
"buffer_load_dwordx4 v25, s[76:79], s84 offen lds\n" \
"v_mfma_f32_16x16x32_bf16 a[184:187], v[168:171], v[96:99], a[184:187]\n" \
"ds_read_b128 v[60:63], v28 offset:2048\n" \
"ds_read_b128 v[64:67], v29 offset:2048\n" \
"v_mfma_f32_16x16x32_bf16 a[184:187], v[164:167], v[92:95], a[184:187]\n" \
"ds_read_b128 v[68:71], v28 offset:4096\n" \
"ds_read_b128 v[72:75], v29 offset:4096\n" \
"v_mfma_f32_16x16x32_bf16 a[188:191], v[172:175], v[92:95], a[188:191]\n" \
"s_add_u32 m0, %[mB], 0x8400\n" \
"s_mov_b32 s85, 0x288000\n" \
"v_mfma_f32_16x16x32_bf16 a[188:191], v[176:179], v[96:99], a[188:191]\n" \
"buffer_load_dwordx4 v26, s[68:71], s85 offen lds\n" \
"v_mfma_f32_16x16x32_bf16 a[208:211], v[152:155], v[104:107], a[208:211]\n" \
"ds_read_b128 v[76:79], v28 offset:6144\n" \
"ds_read_b128 v[80:83], v29 offset:6144\n" \
"v_mfma_f32_16x16x32_bf16 a[208:211], v[148:151], v[100:103], a[208:211]\n" \
"ds_read_b128 v[92:95], v48 offset:2048\n" \
"ds_read_b128 v[96:99], v49 offset:2048\n" \
"v_mfma_f32_16x16x32_bf16 a[212:215], v[156:159], v[100:103], a[212:215]\n" \
"s_add_u32 m0, %[mA], 0x9000\n" \
"s_mov_b32 s84, 0x9280\n" \
"v_mfma_f32_16x16x32_bf16 a[212:215], v[160:163], v[104:107], a[212:215]\n" \
"buffer_load_dwordx4 v24, s[76:79], s84 offen lds\n" \
"v_mfma_f32_16x16x32_bf16 a[216:219], v[168:171], v[104:107], a[216:219]\n" \
"v_mfma_f32_16x16x32_bf16 a[216:219], v[164:167], v[100:103], a[216:219]\n" \
"s_add_u32 m0, %[mA], 0x9400\n" \
"s_mov_b32 s85, 0xb680\n" \
"v_mfma_f32_16x16x32_bf16 a[220:223], v[172:175], v[100:103], a[220:223]\n" \
"buffer_load_dwordx4 v25, s[76:79], s85 offen lds\n" \
"v_mfma_f32_16x16x32_bf16 a[220:223], v[176:179], v[104:107], a[220:223]\n" \
"ds_read_b128 v[100:103], v48 offset:4096\n" \
"ds_read_b128 v[104:107], v49 offset:4096\n" \
"v_mfma_f32_16x16x32_bf16 a[240:243], v[152:155], v[112:115], a[240:243]\n" \
"ds_read_b64_tr_b16 v[152:153], v32 offset:8192\n" \
"ds_read_b64_tr_b16 v[154:155], v32 offset:9216\n" \
"v_mfma_f32_16x16x32_bf16 a[240:243], v[148:151], v[108:111], a[240:243]\n" \
"s_add_u32 m0, %[mB], 0x8800\n" \
"s_mov_b32 s84, 0x290000\n" \
"v_mfma_f32_16x16x32_bf16 a[244:247], v[156:159], v[108:111], a[244:247]\n" \
"buffer_load_dwordx4 v27, s[68:71], s84 offen lds\n" \
"v_mfma_f32_16x16x32_bf16 a[244:247], v[160:163], v[112:115], a[244:247]\n" \
"ds_read_b64_tr_b16 v[148:149], v32 offset:0\n" \
"ds_read_b64_tr_b16 v[150:151], v32 offset:1024\n" \
"v_mfma_f32_16x16x32_bf16 a[248:251], v[168:171], v[112:115], a[248:251]\n" \
"ds_read_b64_tr_b16 v[160:161], v33 offset:8192\n" \
"ds_read_b64_tr_b16 v[162:163], v33 offset:9216\n" \
"v_mfma_f32_16x16x32_bf16 a[248:251], v[164:167], v[108:111], a[248:251]\n" \
"ds_read_b64_tr_b16 v[156:157], v33 offset:0\n" \
"ds_read_b64_tr_b16 v[158:159], v33 offset:1024\n" \
"v_mfma_f32_16x16x32_bf16 a[252:255], v[172:175], v[108:111], a[252:255]\n" \
"ds_read_b64_tr_b16 v[164:165], v34 offset:0\n" \
"ds_read_b64_tr_b16 v[166:167], v34 offset:1024\n" \
"v_mfma_f32_16x16x32_bf16 a[252:255], v[176:179], v[112:115], a[252:255]\n" \
"s_add_u32 m0, %[mA], 0x9800\n" \
"s_waitcnt lgkmcnt(14)\n" \
"s_nop 0\n" \
"v_mfma_f32_16x16x32_bf16 a[16:19], v[144:147], v[88:91], a[16:19]\n" \
"s_mov_b32 s84, 0xda80\n" \
"ds_read_b64_tr_b16 v[168:169], v34 offset:8192\n" \
"v_mfma_f32_16x16x32_bf16 a[16:19], v[140:143], v[84:87], a[16:19]\n" \
"buffer_load_dwordx4 v24, s[76:79], s84 offen lds\n" \
"v_mfma_f32_16x16x32_bf16 a[48:51], v[124:127], v[84:87], a[48:51]\n" \
"s_add_u32 m0, %[mA], 0x9c00\n" \
"s_mov_b32 s85, 0xfe80\n" \
"v_mfma_f32_16x16x32_bf16 a[48:51], v[128:131], v[88:91], a[48:51]\n" \
"ds_read_b64_tr_b16 v[170:171], v34 offset:9216\n" \
"ds_read_b64_tr_b16 v[176:177], v35 offset:8192\n" \
"v_mfma_f32_16x16x32_bf16 a[80:83], v[88:91], v[136:139], a[80:83]\n" \
"buffer_load_dwordx4 v25, s[76:79], s85 offen lds\n" \
"v_mfma_f32_16x16x32_bf16 a[80:83], v[84:87], v[132:135], a[80:83]\n" \
"s_add_u32 m0, %[mB], 0x8c00\n" \
"s_mov_b32 s84, 0x298000\n" \
"v_mfma_f32_16x16x32_bf16 a[112:115], v[140:143], v[52:55], a[112:115]\n" \
"ds_read_b64_tr_b16 v[178:179], v35 offset:9216\n" \
"ds_read_b64_tr_b16 v[172:173], v35 offset:0\n" \
"v_mfma_f32_16x16x32_bf16 a[112:115], v[144:147], v[56:59], a[112:115]\n" \
"buffer_load_dwordx4 v27, s[68:71], s84 offen lds\n" \
"v_mfma_f32_16x16x32_bf16 a[20:23], v[128:131], v[56:59], a[20:23]\n" \
"ds_read_b64_tr_b16 v[174:175], v35 offset:1024\n" \
"ds_read_b128 v[84:87], v48 offset:0\n" \
"v_mfma_f32_16x16x32_bf16 a[20:23], v[124:127], v[52:55], a[20:23]\n" \
"s_add_u32 s89, s42, 0x1000\n" \
"ds_read_b128 v[88:91], v49 offset:0\n" \
"v_mfma_f32_16x16x32_bf16 a[52:55], v[116:119], v[52:55], a[52:55]\n" \
"buffer_store_dwordx4 v[236:239], v20, s[80:83], s89 offen offset:1024 sc1\n" \
"v_mfma_f32_16x16x32_bf16 a[52:55], v[120:123], v[56:59], a[52:55]\n" \
"ds_read_b128 v[108:111], v48 offset:6144\n" \
"ds_read_b128 v[112:115], v49 offset:6144\n" \
"v_mfma_f32_16x16x32_bf16 a[84:87], v[56:59], v[136:139], a[84:87]\n" \
"s_and_b32 s96, 2, %[w]\n" \
"s_lshl_b32 s97, s96, 1\n" \
"v_mfma_f32_16x16x32_bf16 a[84:87], v[52:55], v[132:135], a[84:87]\n" \
"s_and_b32 s98, 1, %[w]\n" \
"s_lshr_b32 s94, %[b], 5\n" \
"v_mfma_f32_16x16x32_bf16 a[116:119], v[140:143], v[60:63], a[116:119]\n" \
"s_add_u32 s99, s97, s98\n" \
"s_lshl_b32 s95, s94, 7\n" \
"v_mfma_f32_16x16x32_bf16 a[116:119], v[144:147], v[64:67], a[116:119]\n" \
"s_lshl_b32 s100, s99, 4\n" \
"s_add_u32 s101, s95, s100\n" \
"v_mfma_f32_16x16x32_bf16 a[24:27], v[128:131], v[64:67], a[24:27]\n" \
"s_mul_i32 s64, 0x480, s101\n" \
"s_lshr_b32 s65, %[b], 3\n" \
"v_mfma_f32_16x16x32_bf16 a[24:27], v[124:127], v[60:63], a[24:27]\n" \
"s_sub_u32 s99, 0x780, s95\n" \
"s_and_b32 s66, 3, s65\n" \
"v_mfma_f32_16x16x32_bf16 a[56:59], v[116:119], v[60:63], a[56:59]\n" \
"s_add_u32 s101, s99, s100\n" \
"s_mul_i32 s67, 0x120, s66\n" \
"v_mfma_f32_16x16x32_bf16 a[56:59], v[120:123], v[64:67], a[56:59]\n" \
"s_add_u32 s88, s42, 0x2000\n" \
"s_and_b32 s96, 7, %[b]\n" \
"v_mfma_f32_16x16x32_bf16 a[88:91], v[64:67], v[136:139], a[88:91]\n" \
"buffer_store_dwordx4 v[240:243], v20, s[80:83], s88 offen offset:1024 sc1\n" \
"v_mfma_f32_16x16x32_bf16 a[88:91], v[60:63], v[132:135], a[88:91]\n" \
"s_mul_i32 s65, 0x480, s101\n" \
"s_add_u32 s94, s64, s67\n" \
"v_mfma_f32_16x16x32_bf16 a[120:123], v[140:143], v[68:71], a[120:123]\n" \
"s_lshl_b32 s97, s96, 5\n" \
"s_add_u32 s66, s65, s67\n" \
"v_mfma_f32_16x16x32_bf16 a[120:123], v[144:147], v[72:75], a[120:123]\n" \
"s_and_b32 s95, 1, s96\n" \
"s_add_u32 s98, s94, s97\n" \
"v_mfma_f32_16x16x32_bf16 a[28:31], v[128:131], v[72:75], a[28:31]\n" \
"s_cmp_lg_u32 s95, 0\n" \
"s_cselect_b32 s99, s66, s94\n" \
"v_mfma_f32_16x16x32_bf16 a[28:31], v[124:127], v[68:71], a[28:31]\n" \
"v_writelane_b32 v11, s98, 39\n" \
"s_lshr_b32 s98, s96, 1\n" \
"v_mfma_f32_16x16x32_bf16 a[60:63], v[116:119], v[68:71], a[60:63]\n" \
"s_add_u32 s100, 0x100, s99\n" \
"s_add_u32 s64, s66, s97\n" \
"v_mfma_f32_16x16x32_bf16 a[60:63], v[120:123], v[72:75], a[60:63]\n" \
"s_cmp_eq_u32 %[w], s98\n" \
"s_cselect_b32 s101, s100, 0x40000000\n" \
"v_mfma_f32_16x16x32_bf16 a[92:95], v[72:75], v[136:139], a[92:95]\n" \
"s_mov_b32 s94, s67\n" \
"v_writelane_b32 v11, s64, 40\n" \
"v_mfma_f32_16x16x32_bf16 a[92:95], v[68:71], v[132:135], a[92:95]\n" \
"s_add_u32 s89, s42, 0x3000\n" \
"v_writelane_b32 v11, s101, 41\n" \
"v_mfma_f32_16x16x32_bf16 a[124:127], v[140:143], v[76:79], a[124:127]\n" \
"buffer_store_dwordx4 v[244:247], v20, s[80:83], s89 offen offset:1024 sc1\n" \
"v_mfma_f32_16x16x32_bf16 a[124:127], v[144:147], v[80:83], a[124:127]\n" \
"s_add_u32 s96, s16, s94\n" \
"s_addc_u32 s97, s17, 0\n" \
"v_mfma_f32_16x16x32_bf16 a[0:3], v[128:131], v[80:83], a[0:3]\n" \
"s_add_u32 s98, s18, s94\n" \
"s_addc_u32 s99, s19, 0\n" \
"v_mfma_f32_16x16x32_bf16 a[0:3], v[124:127], v[76:79], a[0:3]\n" \
"s_and_b32 s97, s97, 0xffff\n" \
"v_writelane_b32 v11, s96, 7\n" \
"v_mfma_f32_16x16x32_bf16 a[4:7], v[116:119], v[76:79], a[4:7]\n" \
"v_writelane_b32 v11, s97, 8\n" \
"s_and_b32 s99, s99, 0xffff\n" \
"v_mfma_f32_16x16x32_bf16 a[4:7], v[120:123], v[80:83], a[4:7]\n" \
"v_writelane_b32 v11, s98, 9\n" \
"v_writelane_b32 v11, s99, 10\n" \
"v_mfma_f32_16x16x32_bf16 a[32:35], v[80:83], v[136:139], a[32:35]\n" \
"s_cmp_lt_u32 %[w], 3\n" \
"s_cselect_b64 s[96:97], s[96:97], s[98:99]\n" \
"v_mfma_f32_16x16x32_bf16 a[32:35], v[76:79], v[132:135], a[32:35]\n" \
"v_writelane_b32 v11, s96, 13\n" \
"s_waitcnt lgkmcnt(0)\n" \
"s_nop 0\n" \
"v_mfma_f32_16x16x32_bf16 a[128:131], v[148:151], v[52:55], a[128:131]\n" \
"v_writelane_b32 v11, s97, 14\n" \
"v_writelane_b32 v11, s20, 11\n" \
"v_mfma_f32_16x16x32_bf16 a[128:131], v[152:155], v[56:59], a[128:131]\n" \
"s_and_b32 s95, s21, 0xffff\n" \
"v_writelane_b32 v11, s95, 12\n" \
"v_mfma_f32_16x16x32_bf16 a[132:135], v[160:163], v[56:59], a[132:135]\n" \
"v_mfma_f32_16x16x32_bf16 a[132:135], v[156:159], v[52:55], a[132:135]\n" \
"v_mfma_f32_16x16x32_bf16 a[136:139], v[164:167], v[52:55], a[136:139]\n" \
"v_mfma_f32_16x16x32_bf16 a[136:139], v[168:171], v[56:59], a[136:139]\n" \
"v_mfma_f32_16x16x32_bf16 a[140:143], v[176:179], v[56:59], a[140:143]\n" \
"v_mfma_f32_16x16x32_bf16 a[140:143], v[172:175], v[52:55], a[140:143]\n" \
"v_mfma_f32_16x16x32_bf16 a[160:163], v[148:151], v[60:63], a[160:163]\n" \
"v_mfma_f32_16x16x32_bf16 a[160:163], v[152:155], v[64:67], a[160:163]\n" \
"v_mfma_f32_16x16x32_bf16 a[164:167], v[160:163], v[64:67], a[164:167]\n" \
"v_mfma_f32_16x16x32_bf16 a[164:167], v[156:159], v[60:63], a[164:167]\n" \
"v_mfma_f32_16x16x32_bf16 a[168:171], v[164:167], v[60:63], a[168:171]\n" \
"v_mfma_f32_16x16x32_bf16 a[168:171], v[168:171], v[64:67], a[168:171]\n" \
"v_mfma_f32_16x16x32_bf16 a[172:175], v[176:179], v[64:67], a[172:175]\n" \
"v_mfma_f32_16x16x32_bf16 a[172:175], v[172:175], v[60:63], a[172:175]\n" \
"v_mfma_f32_16x16x32_bf16 a[192:195], v[148:151], v[68:71], a[192:195]\n" \
"v_mfma_f32_16x16x32_bf16 a[192:195], v[152:155], v[72:75], a[192:195]\n" \
"s_waitcnt vmcnt(3)\n" \
"s_barrier\n" \
"v_mfma_f32_16x16x32_bf16 a[196:199], v[160:163], v[72:75], a[196:199]\n" \
"s_add_u32 m0, s40, 0x0\n" \
"s_nop 0\n" \
"s_add_u32 s85, s37, 0xc000\n" \
"v_mfma_f32_16x16x32_bf16 a[196:199], v[156:159], v[68:71], a[196:199]\n" \
"buffer_load_dwordx4 v20, s[80:83], s85 offen sc1 lds\n" \
"v_mfma_f32_16x16x32_bf16 a[200:203], v[164:167], v[68:71], a[200:203]\n" \
"s_add_u32 m0, s40, 0x1000\n" \
"s_add_u32 s84, s37, 0xd000\n" \
"v_mfma_f32_16x16x32_bf16 a[200:203], v[168:171], v[72:75], a[200:203]\n" \
"ds_read_b64_tr_b16 v[140:141], v39 offset:32768\n" \
"ds_read_b64_tr_b16 v[142:143], v39 offset:33280\n" \
"v_mfma_f32_16x16x32_bf16 a[204:207], v[176:179], v[72:75], a[204:207]\n" \
"buffer_load_dwordx4 v20, s[80:83], s84 offen sc1 lds\n" \
"v_mfma_f32_16x16x32_bf16 a[204:207], v[172:175], v[68:71], a[204:207]\n" \
"ds_read_b64_tr_b16 v[144:145], v39 offset:36864\n" \
"ds_read_b64_tr_b16 v[146:147], v39 offset:37376\n" \
"v_mfma_f32_16x16x32_bf16 a[224:227], v[148:151], v[76:79], a[224:227]\n" \
"s_add_u32 m0, s40, 0x3000\n" \
"s_mov_b32 s85, 0x300\n" \
"v_mfma_f32_16x16x32_bf16 a[224:227], v[152:155], v[80:83], a[224:227]\n" \
"buffer_load_dwordx4 v21, s[76:79], s85 offen lds\n" \
"v_mfma_f32_16x16x32_bf16 a[228:231], v[160:163], v[80:83], a[228:231]\n" \
"ds_read_b64_tr_b16 v[128:129], v37 offset:36864\n" \
"ds_read_b64_tr_b16 v[130:131], v37 offset:37376\n" \
"v_mfma_f32_16x16x32_bf16 a[228:231], v[156:159], v[76:79], a[228:231]\n" \
"ds_read_b64_tr_b16 v[124:125], v37 offset:32768\n" \
"ds_read_b64_tr_b16 v[126:127], v37 offset:33280\n" \
"v_mfma_f32_16x16x32_bf16 a[232:235], v[164:167], v[76:79], a[232:235]\n" \
"s_add_u32 m0, %[mA], 0x0\n" \
"s_nop 0\n" \
"s_mov_b32 s84, 0x300\n" \
"v_mfma_f32_16x16x32_bf16 a[232:235], v[168:171], v[80:83], a[232:235]\n" \
"buffer_load_dwordx4 v24, s[76:79], s84 offen lds\n" \
"v_mfma_f32_16x16x32_bf16 a[236:239], v[176:179], v[80:83], a[236:239]\n" \
"ds_read_b64_tr_b16 v[132:133], v38 offset:32768\n" \
"ds_read_b64_tr_b16 v[134:135], v38 offset:33280\n" \
"v_mfma_f32_16x16x32_bf16 a[236:239], v[172:175], v[76:79], a[236:239]\n" \
"s_add_u32 m0, %[mA], 0x400\n" \
"s_mov_b32 s85, 0x2700\n" \
"v_mfma_f32_16x16x32_bf16 a[144:147], v[148:151], v[84:87], a[144:147]\n" \
"buffer_load_dwordx4 v25, s[76:79], s85 offen lds\n" \
"v_mfma_f32_16x16x32_bf16 a[144:147], v[152:155], v[88:91], a[144:147]\n" \
"ds_read_b64_tr_b16 v[136:137], v38 offset:36864\n" \
"ds_read_b64_tr_b16 v[138:139], v38 offset:37376\n" \
"v_mfma_f32_16x16x32_bf16 a[148:151], v[160:163], v[88:91], a[148:151]\n" \
"ds_read_b128 v[56:59], v29 offset:32768\n" \
"ds_read_b128 v[52:55], v28 offset:32768\n" \
"v_mfma_f32_16x16x32_bf16 a[148:151], v[156:159], v[84:87], a[148:151]\n" \
"s_add_u32 m0, %[mB], 0x0\n" \
"s_nop 0\n" \
"s_mov_b32 s84, 0x300000\n" \
"v_mfma_f32_16x16x32_bf16 a[152:155], v[164:167], v[84:87], a[152:155]\n" \
"buffer_load_dwordx4 v26, s[68:71], s84 offen lds\n" \
"v_mfma_f32_16x16x32_bf16 a[152:155], v[168:171], v[88:91], a[152:155]\n" \
"ds_read_b64_tr_b16 v[120:121], v36 offset:36864\n" \
"ds_read_b64_tr_b16 v[122:123], v36 offset:37376\n" \
"v_mfma_f32_16x16x32_bf16 a[156:159], v[176:179], v[88:91], a[156:159]\n" \
"ds_read_b128 v[88:91], v23 offset:32768\n" \
"ds_read_b64_tr_b16 v[116:117], v36 offset:32768\n" \
"v_mfma_f32_16x16x32_bf16 a[156:159], v[172:175], v[84:87], a[156:159]\n" \
"s_add_u32 m0, %[mA], 0x800\n" \
"s_mov_b32 s85, 0x4b00\n" \
"v_mfma_f32_16x16x32_bf16 a[176:179], v[148:151], v[92:95], a[176:179]\n" \
"buffer_load_dwordx4 v24, s[76:79], s85 offen lds\n" \
"v_mfma_f32_16x16x32_bf16 a[176:179], v[152:155], v[96:99], a[176:179]\n" \
"ds_read_b128 v[84:87], v22 offset:32768\n" \
"ds_read_b64_tr_b16 v[118:119], v36 offset:33280\n" \
"v_mfma_f32_16x16x32_bf16 a[180:183], v[160:163], v[96:99], a[180:183]\n" \
"s_add_u32 m0, %[mA], 0xc00\n" \
"s_mov_b32 s84, 0x6f00\n" \
"v_mfma_f32_16x16x32_bf16 a[180:183], v[156:159], v[92:95], a[180:183]\n" \
"L_pf7:\n" \
"s_cbranch_execz L_pf11\n" \
"s_nop 0\n" \
"buffer_load_dwordx4 v25, s[76:79], s84 offen lds\n" \
"v_mfma_f32_16x16x32_bf16 a[184:187], v[164:167], v[92:95], a[184:187]\n" \
"ds_read_b128 v[64:67], v29 offset:34816\n" \
"ds_read_b128 v[60:63], v28 offset:34816\n" \
"v_mfma_f32_16x16x32_bf16 a[184:187], v[168:171], v[96:99], a[184:187]\n" \
"ds_read_b128 v[72:75], v29 offset:36864\n" \
"ds_read_b128 v[68:71], v28 offset:36864\n" \
"v_mfma_f32_16x16x32_bf16 a[188:191], v[176:179], v[96:99], a[188:191]\n" \
"s_add_u32 m0, %[mB], 0x400\n" \
"s_mov_b32 s85, 0x308000\n" \
"v_mfma_f32_16x16x32_bf16 a[188:191], v[172:175], v[92:95], a[188:191]\n" \
"buffer_load_dwordx4 v26, s[68:71], s85 offen lds\n" \
"v_mfma_f32_16x16x32_bf16 a[208:211], v[148:151], v[100:103], a[208:211]\n" \
"ds_read_b128 v[80:83], v29 offset:38912\n" \
"ds_read_b128 v[76:79], v28 offset:38912\n" \
"v_mfma_f32_16x16x32_bf16 a[208:211], v[152:155], v[104:107], a[208:211]\n" \
"ds_read_b128 v[92:95], v48 offset:34816\n" \
"ds_read_b128 v[96:99], v49 offset:34816\n" \
"v_mfma_f32_16x16x32_bf16 a[212:215], v[160:163], v[104:107], a[212:215]\n" \
"s_add_u32 m0, %[mA], 0x1000\n" \
"s_mov_b32 s84, 0x9300\n" \
"v_mfma_f32_16x16x32_bf16 a[212:215], v[156:159], v[100:103], a[212:215]\n" \
"buffer_load_dwordx4 v24, s[76:79], s84 offen lds\n" \
"v_mfma_f32_16x16x32_bf16 a[216:219], v[164:167], v[100:103], a[216:219]\n" \
"v_mfma_f32_16x16x32_bf16 a[216:219], v[168:171], v[104:107], a[216:219]\n" \
"s_add_u32 m0, %[mA], 0x1400\n" \
"s_mov_b32 s85, 0xb700\n" \
"v_mfma_f32_16x16x32_bf16 a[220:223], v[176:179], v[104:107], a[220:223]\n" \
"buffer_load_dwordx4 v25, s[76:79], s85 offen lds\n" \
"v_mfma_f32_16x16x32_bf16 a[220:223], v[172:175], v[100:103], a[220:223]\n" \
"ds_read_b128 v[100:103], v48 offset:36864\n" \
"ds_read_b128 v[104:107], v49 offset:36864\n" \
"v_mfma_f32_16x16x32_bf16 a[240:243], v[148:151], v[108:111], a[240:243]\n" \
"ds_read_b64_tr_b16 v[148:149], v32 offset:32768\n" \
"ds_read_b64_tr_b16 v[150:151], v32 offset:33792\n" \
"v_mfma_f32_16x16x32_bf16 a[240:243], v[152:155], v[112:115], a[240:243]\n" \
"s_add_u32 m0, %[mB], 0x800\n" \
"s_mov_b32 s84, 0x310000\n" \
"v_mfma_f32_16x16x32_bf16 a[244:247], v[160:163], v[112:115], a[244:247]\n" \
"buffer_load_dwordx4 v27, s[68:71], s84 offen lds\n" \
"v_mfma_f32_16x16x32_bf16 a[244:247], v[156:159], v[108:111], a[244:247]\n" \
"ds_read_b64_tr_b16 v[152:153], v32 offset:40960\n" \
"ds_read_b64_tr_b16 v[154:155], v32 offset:41984\n" \
"v_mfma_f32_16x16x32_bf16 a[248:251], v[164:167], v[108:111], a[248:251]\n" \
"ds_read_b64_tr_b16 v[156:157], v33 offset:32768\n" \
"ds_read_b64_tr_b16 v[158:159], v33 offset:33792\n" \
"v_mfma_f32_16x16x32_bf16 a[248:251], v[168:171], v[112:115], a[248:251]\n" \
"ds_read_b64_tr_b16 v[160:161], v33 offset:40960\n" \
"ds_read_b64_tr_b16 v[162:163], v33 offset:41984\n" \
"v_mfma_f32_16x16x32_bf16 a[252:255], v[176:179], v[112:115], a[252:255]\n" \
"ds_read_b64_tr_b16 v[168:169], v34 offset:40960\n" \
"ds_read_b64_tr_b16 v[170:171], v34 offset:41984\n" \
"v_mfma_f32_16x16x32_bf16 a[252:255], v[172:175], v[108:111], a[252:255]\n" \
"s_add_u32 m0, %[mA], 0x1800\n" \
"s_waitcnt lgkmcnt(14)\n" \
"s_nop 0\n" \
"v_mfma_f32_16x16x32_bf16 a[16:19], v[140:143], v[84:87], a[16:19]\n" \
"s_mov_b32 s84, 0xdb00\n" \
"ds_read_b64_tr_b16 v[164:165], v34 offset:32768\n" \
"v_mfma_f32_16x16x32_bf16 a[16:19], v[144:147], v[88:91], a[16:19]\n" \
"buffer_load_dwordx4 v24, s[76:79], s84 offen lds\n" \
"v_mfma_f32_16x16x32_bf16 a[48:51], v[128:131], v[88:91], a[48:51]\n" \
"s_add_u32 m0, %[mA], 0x1c00\n" \
"s_mov_b32 s85, 0xff00\n" \
"v_mfma_f32_16x16x32_bf16 a[48:51], v[124:127], v[84:87], a[48:51]\n" \
"ds_read_b64_tr_b16 v[166:167], v34 offset:33792\n" \
"ds_read_b64_tr_b16 v[172:173], v35 offset:32768\n" \
"v_mfma_f32_16x16x32_bf16 a[80:83], v[84:87], v[132:135], a[80:83]\n" \
"buffer_load_dwordx4 v25, s[76:79], s85 offen lds\n" \
"v_mfma_f32_16x16x32_bf16 a[80:83], v[88:91], v[136:139], a[80:83]\n" \
"s_add_u32 m0, %[mB], 0xc00\n" \
"s_mov_b32 s84, 0x318000\n" \
"v_mfma_f32_16x16x32_bf16 a[112:115], v[144:147], v[56:59], a[112:115]\n" \
"ds_read_b64_tr_b16 v[174:175], v35 offset:33792\n" \
"ds_read_b64_tr_b16 v[176:177], v35 offset:40960\n" \
"v_mfma_f32_16x16x32_bf16 a[112:115], v[140:143], v[52:55], a[112:115]\n" \
"buffer_load_dwordx4 v27, s[68:71], s84 offen lds\n" \
"v_mfma_f32_16x16x32_bf16 a[20:23], v[124:127], v[52:55], a[20:23]\n" \
"ds_read_b64_tr_b16 v[178:179], v35 offset:41984\n" \
"ds_read_b128 v[84:87], v48 offset:32768\n" \
"v_mfma_f32_16x16x32_bf16 a[20:23], v[128:131], v[56:59], a[20:23]\n" \
"s_add_u32 s88, s43, 0x0\n" \
"ds_read_b128 v[88:91], v49 offset:32768\n" \
"v_mfma_f32_16x16x32_bf16 a[52:55], v[120:123], v[56:59], a[52:55]\n" \
"buffer_store_dwordx4 v[248:251], v20, s[80:83], s88 offen offset:0 sc1\n" \
"v_mfma_f32_16x16x32_bf16 a[52:55], v[116:119], v[52:55], a[52:55]\n" \
"ds_read_b128 v[108:111], v48 offset:38912\n" \
"ds_read_b128 v[112:115], v49 offset:38912\n" \
"v_mfma_f32_16x16x32_bf16 a[84:87], v[52:55], v[132:135], a[84:87]\n" \
"s_and_b32 s95, 1, %[w]\n" \
"s_lshr_b32 s96, %[w], 1\n" \
"v_mfma_f32_16x16x32_bf16 a[84:87], v[56:59], v[136:139], a[84:87]\n" \
"s_lshl_b32 s94, %[w], 11\n" \
"s_lshl_b32 s15, s95, 12\n" \
"v_mfma_f32_16x16x32_bf16 a[116:119], v[144:147], v[64:67], a[116:119]\n" \
"s_lshl_b32 s16, s96, 14\n" \
"s_add_u32 s12, 0xc000, s94\n" \
"v_mfma_f32_16x16x32_bf16 a[116:119], v[140:143], v[60:63], a[116:119]\n" \
"s_add_u32 s97, s16, s15\n" \
"s_and_b32 s65, 7, %[b]\n" \
"v_mfma_f32_16x16x32_bf16 a[24:27], v[124:127], v[60:63], a[24:27]\n" \
"s_lshr_b32 s67, %[b], 3\n" \
"s_xor_b32 s13, 0x8000, s97\n" \
"v_mfma_f32_16x16x32_bf16 a[24:27], v[128:131], v[64:67], a[24:27]\n" \
"s_add_u32 s20, 0x14000, s97\n" \
"s_mul_i32 s98, 0xe000, s96\n" \
"v_mfma_f32_16x16x32_bf16 a[56:59], v[120:123], v[64:67], a[56:59]\n" \
"s_lshr_b32 s100, %[b], 5\n" \
"s_lshl_b32 s66, s65, 2\n" \
"v_mfma_f32_16x16x32_bf16 a[56:59], v[116:119], v[60:63], a[56:59]\n" \
"s_add_u32 s89, s45, 0x1000\n" \
"s_and_b32 s94, 3, s67\n" \
"v_mfma_f32_16x16x32_bf16 a[88:91], v[60:63], v[132:135], a[88:91]\n" \
"buffer_store_dwordx4 v[184:187], v20, s[80:83], s89 offen offset:2048 sc1\n" \
"v_mfma_f32_16x16x32_bf16 a[88:91], v[64:67], v[136:139], a[88:91]\n" \
"s_add_u32 s99, s98, s15\n" \
"s_lshl_b32 s18, s100, 1\n" \
"v_mfma_f32_16x16x32_bf16 a[120:123], v[144:147], v[72:75], a[120:123]\n" \
"s_add_u32 s97, s66, s94\n" \
"s_add_u32 s21, 0x10000, s99\n" \
"v_mfma_f32_16x16x32_bf16 a[120:123], v[140:143], v[68:71], a[120:123]\n" \
"s_add_u32 s101, s18, s96\n" \
"s_lshl_b32 s98, s97, 8\n" \
"v_mfma_f32_16x16x32_bf16 a[28:31], v[124:127], v[68:71], a[28:31]\n" \
"s_lshl_b32 s14, s101, 15\n" \
"s_mov_b32 s64, 0x4000\n" \
"v_mfma_f32_16x16x32_bf16 a[28:31], v[128:131], v[72:75], a[28:31]\n" \
"s_add_u32 s99, 0x5be8020, s98\n" \
"s_lshl_b32 s96, s100, 2\n" \
"v_mfma_f32_16x16x32_bf16 a[60:63], v[120:123], v[72:75], a[60:63]\n" \
"s_cmp_lg_u32 s100, 0\n" \
"s_cselect_b32 s17, s64, 0x8000\n" \
"v_mfma_f32_16x16x32_bf16 a[60:63], v[116:119], v[68:71], a[60:63]\n" \
"s_add_u32 s101, s99, s96\n" \
"s_and_b32 s65, 2, %[w]\n" \
"v_mfma_f32_16x16x32_bf16 a[92:95], v[68:71], v[132:135], a[92:95]\n" \
"s_cmp_eq_u32 %[w], 3\n" \
"s_cselect_b32 s22, s101, 0x40000000\n" \
"v_mfma_f32_16x16x32_bf16 a[92:95], v[72:75], v[136:139], a[92:95]\n" \
"s_add_u32 s88, s45, 0x1000\n" \
"s_lshl_b32 s64, s100, 3\n" \
"v_mfma_f32_16x16x32_bf16 a[124:127], v[144:147], v[80:83], a[124:127]\n" \
"buffer_store_dwordx4 v[252:255], v20, s[80:83], s88 offen offset:3072 sc1\n" \
"v_mfma_f32_16x16x32_bf16 a[124:127], v[140:143], v[76:79], a[124:127]\n" \
"s_lshl_b32 s67, s65, 1\n" \
"s_add_u32 s66, s67, s95\n" \
"v_mfma_f32_16x16x32_bf16 a[0:3], v[124:127], v[76:79], a[0:3]\n" \
"s_sub_u32 s101, 0x78, s64\n" \
"s_add_u32 s65, s101, s66\n" \
"v_mfma_f32_16x16x32_bf16 a[0:3], v[128:131], v[80:83], a[0:3]\n" \
"s_add_u32 s94, s64, s66\n" \
"s_mul_i32 s67, 0x24800, s65\n" \
"v_mfma_f32_16x16x32_bf16 a[4:7], v[120:123], v[80:83], a[4:7]\n" \
"s_lshl_b32 s99, s97, 12\n" \
"s_mov_b32 s64, 0xaaaaaaaa\n" \
"v_mfma_f32_16x16x32_bf16 a[4:7], v[116:119], v[76:79], a[4:7]\n" \
"s_mov_b32 s65, 0xaaaaaaaa\n" \
"s_add_u32 s95, s67, s99\n" \
"v_mfma_f32_16x16x32_bf16 a[32:35], v[76:79], v[132:135], a[32:35]\n" \
"s_mov_b32 s66, 0xfff0ffff\n" \
"s_mov_b32 s67, 0xf000ff00\n" \
"v_mfma_f32_16x16x32_bf16 a[32:35], v[80:83], v[136:139], a[32:35]\n" \
"s_mul_i32 s98, 0x24800, s94\n" \
"s_waitcnt lgkmcnt(0)\n" \
"v_mfma_f32_16x16x32_bf16 a[128:131], v[152:155], v[56:59], a[128:131]\n" \
"s_add_u32 s96, s98, s99\n" \
"s_lshl_b32 s98, s97, 20\n" \
"v_mfma_f32_16x16x32_bf16 a[128:131], v[148:151], v[52:55], a[128:131]\n" \
"s_lshl_b32 s94, s100, 16\n" \
"s_add_u32 s8, 0x1000000, s98\n" \
"v_mfma_f32_16x16x32_bf16 a[132:135], v[156:159], v[52:55], a[132:135]\n" \
"s_sub_u32 s19, 14, s18\n" \
"s_add_u32 s60, 0x3004800, s96\n" \
"v_mfma_f32_16x16x32_bf16 a[132:135], v[160:163], v[56:59], a[132:135]\n" \
"s_add_u32 s61, 0x3004800, s95\n" \
"s_sub_u32 s10, 0x100000, s94\n" \
"v_mfma_f32_16x16x32_bf16 a[136:139], v[168:171], v[56:59], a[136:139]\n" \
"s_mov_b32 s11, 0x20000\n" \
"s_mov_b32 s58, 0x6000000\n" \
"v_mfma_f32_16x16x32_bf16 a[136:139], v[164:167], v[52:55], a[136:139]\n" \
"s_mov_b32 s59, 0x20000\n" \
"s_mov_b64 s[56:57], s[80:81]\n" \
"v_mfma_f32_16x16x32_bf16 a[140:143], v[172:175], v[52:55], a[140:143]\n" \
"s_add_u32 s8, s8, s80\n" \
"s_addc_u32 s9, s81, 0\n" \
"s_nop 0\n" \
"v_mfma_f32_16x16x32_bf16 a[140:143], v[176:179], v[56:59], a[140:143]\n" \
"v_mfma_f32_16x16x32_bf16 a[160:163], v[152:155], v[64:67], a[160:163]\n" \
"v_mfma_f32_16x16x32_bf16 a[160:163], v[148:151], v[60:63], a[160:163]\n" \
"v_mfma_f32_16x16x32_bf16 a[164:167], v[156:159], v[60:63], a[164:167]\n" \
"v_mfma_f32_16x16x32_bf16 a[164:167], v[160:163], v[64:67], a[164:167]\n" \
"v_mfma_f32_16x16x32_bf16 a[168:171], v[168:171], v[64:67], a[168:171]\n" \
"v_mfma_f32_16x16x32_bf16 a[168:171], v[164:167], v[60:63], a[168:171]\n" \
"v_mfma_f32_16x16x32_bf16 a[172:175], v[172:175], v[60:63], a[172:175]\n" \
"v_mfma_f32_16x16x32_bf16 a[172:175], v[176:179], v[64:67], a[172:175]\n" \
"v_mfma_f32_16x16x32_bf16 a[192:195], v[152:155], v[72:75], a[192:195]\n" \
"v_mfma_f32_16x16x32_bf16 a[192:195], v[148:151], v[68:71], a[192:195]\n" \
"s_waitcnt vmcnt(3)\n" \
"s_barrier\n" \
"s_mov_b64 exec, 1\n" \
"buffer_store_dword v45, off, s[80:83], s38 offset:0 sc1\n" \
"s_mov_b64 exec, -1\n" \
"v_mfma_f32_16x16x32_bf16 a[196:199], v[156:159], v[68:71], a[196:199]\n" \
"s_add_u32 m0, s40, 0x8000\n" \
"s_add_u32 s85, s37, 0xe000\n" \
"v_mfma_f32_16x16x32_bf16 a[196:199], v[160:163], v[72:75], a[196:199]\n" \
"ds_read_b64_tr_b16 v[144:145], v39 offset:4096\n" \
"ds_read_b64_tr_b16 v[146:147], v39 offset:4608\n" \
"v_mfma_f32_16x16x32_bf16 a[200:203], v[168:171], v[72:75], a[200:203]\n" \
"buffer_load_dwordx4 v20, s[80:83], s85 offen sc1 lds\n" \
"v_mfma_f32_16x16x32_bf16 a[200:203], v[164:167], v[68:71], a[200:203]\n" \
"s_add_u32 m0, s40, 0x9000\n" \
"s_add_u32 s84, s37, 0xf000\n" \
"v_mfma_f32_16x16x32_bf16 a[204:207], v[172:175], v[68:71], a[204:207]\n" \
"ds_read_b64_tr_b16 v[140:141], v39 offset:0\n" \
"ds_read_b64_tr_b16 v[142:143], v39 offset:512\n" \
"v_mfma_f32_16x16x32_bf16 a[204:207], v[176:179], v[72:75], a[204:207]\n" \
"buffer_load_dwordx4 v20, s[80:83], s84 offen sc1 lds\n" \
"v_mfma_f32_16x16x32_bf16 a[224:227], v[152:155], v[80:83], a[224:227]\n" \
"s_add_u32 m0, s40, 0xb000\n" \
"s_mov_b32 s85, 0x380\n" \
"v_mfma_f32_16x16x32_bf16 a[224:227], v[148:151], v[76:79], a[224:227]\n" \
"ds_read_b64_tr_b16 v[124:125], v37 offset:0\n" \
"ds_read_b64_tr_b16 v[126:127], v37 offset:512\n" \
"v_mfma_f32_16x16x32_bf16 a[228:231], v[156:159], v[76:79], a[228:231]\n" \
"buffer_load_dwordx4 v21, s[76:79], s85 offen lds\n" \
"v_mfma_f32_16x16x32_bf16 a[228:231], v[160:163], v[80:83], a[228:231]\n" \
"ds_read_b64_tr_b16 v[128:129], v37 offset:4096\n" \
"ds_read_b64_tr_b16 v[130:131], v37 offset:4608\n" \
"v_mfma_f32_16x16x32_bf16 a[232:235], v[168:171], v[80:83], a[232:235]\n" \
"s_add_u32 m0, %[mA], 0x8000\n" \
"s_mov_b32 s84, 0x380\n" \
"v_mfma_f32_16x16x32_bf16 a[232:235], v[164:167], v[76:79], a[232:235]\n" \
"buffer_load_dwordx4 v24, s[76:79], s84 offen lds\n" \
"v_mfma_f32_16x16x32_bf16 a[236:239], v[172:175], v[76:79], a[236:239]\n" \
"ds_read_b64_tr_b16 v[136:137], v38 offset:4096\n" \
"ds_read_b64_tr_b16 v[138:139], v38 offset:4608\n" \
"v_mfma_f32_16x16x32_bf16 a[236:239], v[176:179], v[80:83], a[236:239]\n" \
"s_add_u32 m0, %[mA], 0x8400\n" \
"s_mov_b32 s85, 0x2780\n" \
"v_mfma_f32_16x16x32_bf16 a[144:147], v[152:155], v[88:91], a[144:147]\n" \
"buffer_load_dwordx4 v25, s[76:79], s85 offen lds\n" \
"v_mfma_f32_16x16x32_bf16 a[144:147], v[148:151], v[84:87], a[144:147]\n" \
"ds_read_b64_tr_b16 v[132:133], v38 offset:0\n" \
"ds_read_b64_tr_b16 v[134:135], v38 offset:512\n" \
"v_mfma_f32_16x16x32_bf16 a[148:151], v[156:159], v[84:87], a[148:151]\n" \
"ds_read_b128 v[52:55], v28 offset:0\n" \
"ds_read_b128 v[56:59], v29 offset:0\n" \
"v_mfma_f32_16x16x32_bf16 a[148:151], v[160:163], v[88:91], a[148:151]\n" \
"s_add_u32 m0, %[mB], 0x8000\n" \
"s_mov_b32 s84, 0x380000\n" \
"v_mfma_f32_16x16x32_bf16 a[152:155], v[168:171], v[88:91], a[152:155]\n" \
"buffer_load_dwordx4 v26, s[68:71], s84 offen lds\n" \
"v_mfma_f32_16x16x32_bf16 a[152:155], v[164:167], v[84:87], a[152:155]\n" \
"ds_read_b64_tr_b16 v[116:117], v36 offset:0\n" \
"ds_read_b64_tr_b16 v[118:119], v36 offset:512\n" \
"v_mfma_f32_16x16x32_bf16 a[156:159], v[172:175], v[84:87], a[156:159]\n" \
"ds_read_b128 v[84:87], v22 offset:0\n" \
"ds_read_b64_tr_b16 v[120:121], v36 offset:4096\n" \
"v_mfma_f32_16x16x32_bf16 a[156:159], v[176:179], v[88:91], a[156:159]\n" \
"s_add_u32 m0, %[mA], 0x8800\n" \
"s_mov_b32 s85, 0x4b80\n" \
"v_mfma_f32_16x16x32_bf16 a[176:179], v[152:155], v[96:99], a[176:179]\n" \
"buffer_load_dwordx4 v24, s[76:79], s85 offen lds\n" \
"v_mfma_f32_16x16x32_bf16 a[176:179], v[148:151], v[92:95], a[176:179]\n" \
"ds_read_b128 v[88:91], v23 offset:0\n" \
"ds_read_b64_tr_b16 v[122:123], v36 offset:4608\n" \
"v_mfma_f32_16x16x32_bf16 a[180:183], v[156:159], v[92:95], a[180:183]\n" \
"s_add_u32 m0, %[mA], 0x8c00\n" \
"s_mov_b32 s84, 0x6f80\n" \
"v_mfma_f32_16x16x32_bf16 a[180:183], v[160:163], v[96:99], a[180:183]\n" \
"buffer_load_dwordx4 v25, s[76:79], s84 offen lds\n" \
"v_mfma_f32_16x16x32_bf16 a[184:187], v[168:171], v[96:99], a[184:187]\n" \
"ds_read_b128 v[60:63], v28 offset:2048\n" \
"ds_read_b128 v[64:67], v29 offset:2048\n" \
"v_mfma_f32_16x16x32_bf16 a[184:187], v[164:167], v[92:95], a[184:187]\n" \
"ds_read_b128 v[68:71], v28 offset:4096\n" \
"ds_read_b128 v[72:75], v29 offset:4096\n" \
"v_mfma_f32_16x16x32_bf16 a[188:191], v[172:175], v[92:95], a[188:191]\n" \
"s_add_u32 m0, %[mB], 0x8400\n" \
"s_mov_b32 s85, 0x388000\n" \
"v_mfma_f32_16x16x32_bf16 a[188:191], v[176:179], v[96:99], a[188:191]\n" \
"buffer_load_dwordx4 v26, s[68:71], s85 offen lds\n" \
"v_mfma_f32_16x16x32_bf16 a[208:211], v[152:155], v[104:107], a[208:211]\n" \
"ds_read_b128 v[76:79], v28 offset:6144\n" \
"ds_read_b128 v[80:83], v29 offset:6144\n" \
"v_mfma_f32_16x16x32_bf16 a[208:211], v[148:151], v[100:103], a[208:211]\n" \
"ds_read_b128 v[92:95], v48 offset:2048\n" \
"ds_read_b128 v[96:99], v49 offset:2048\n" \
"v_mfma_f32_16x16x32_bf16 a[212:215], v[156:159], v[100:103], a[212:215]\n" \
"s_add_u32 m0, %[mA], 0x9000\n" \
"s_mov_b32 s84, 0x9380\n" \
"v_mfma_f32_16x16x32_bf16 a[212:215], v[160:163], v[104:107], a[212:215]\n" \
"buffer_load_dwordx4 v24, s[76:79], s84 offen lds\n" \
"v_mfma_f32_16x16x32_bf16 a[216:219], v[168:171], v[104:107], a[216:219]\n" \
"v_mfma_f32_16x16x32_bf16 a[216:219], v[164:167], v[100:103], a[216:219]\n" \
"s_add_u32 m0, %[mA], 0x9400\n" \
"s_mov_b32 s85, 0xb780\n" \
"v_mfma_f32_16x16x32_bf16 a[220:223], v[172:175], v[100:103], a[220:223]\n" \
"buffer_load_dwordx4 v25, s[76:79], s85 offen lds\n" \
"v_mfma_f32_16x16x32_bf16 a[220:223], v[176:179], v[104:107], a[220:223]\n" \
"ds_read_b128 v[100:103], v48 offset:4096\n" \
"ds_read_b128 v[104:107], v49 offset:4096\n" \
"v_mfma_f32_16x16x32_bf16 a[240:243], v[152:155], v[112:115], a[240:243]\n" \
"ds_read_b64_tr_b16 v[152:153], v32 offset:8192\n" \
"ds_read_b64_tr_b16 v[154:155], v32 offset:9216\n" \
"v_mfma_f32_16x16x32_bf16 a[240:243], v[148:151], v[108:111], a[240:243]\n" \
"s_add_u32 m0, %[mB], 0x8800\n" \
"s_mov_b32 s84, 0x390000\n" \
"v_mfma_f32_16x16x32_bf16 a[244:247], v[156:159], v[108:111], a[244:247]\n" \
"buffer_load_dwordx4 v27, s[68:71], s84 offen lds\n" \
"v_mfma_f32_16x16x32_bf16 a[244:247], v[160:163], v[112:115], a[244:247]\n" \
"ds_read_b64_tr_b16 v[148:149], v32 offset:0\n" \
"ds_read_b64_tr_b16 v[150:151], v32 offset:1024\n" \
"v_mfma_f32_16x16x32_bf16 a[248:251], v[168:171], v[112:115], a[248:251]\n" \
"ds_read_b64_tr_b16 v[160:161], v33 offset:8192\n" \
"ds_read_b64_tr_b16 v[162:163], v33 offset:9216\n" \
"v_mfma_f32_16x16x32_bf16 a[248:251], v[164:167], v[108:111], a[248:251]\n" \
"ds_read_b64_tr_b16 v[156:157], v33 offset:0\n" \
"ds_read_b64_tr_b16 v[158:159], v33 offset:1024\n" \
"v_mfma_f32_16x16x32_bf16 a[252:255], v[172:175], v[108:111], a[252:255]\n" \
"ds_read_b64_tr_b16 v[164:165], v34 offset:0\n" \
"ds_read_b64_tr_b16 v[166:167], v34 offset:1024\n" \
"v_mfma_f32_16x16x32_bf16 a[252:255], v[176:179], v[112:115], a[252:255]\n" \
"s_add_u32 m0, %[mA], 0x9800\n" \
"s_waitcnt lgkmcnt(14)\n" \
"s_nop 0\n" \
"v_mfma_f32_16x16x32_bf16 a[16:19], v[144:147], v[88:91], a[16:19]\n" \
"s_mov_b32 s84, 0xdb80\n" \
"ds_read_b64_tr_b16 v[168:169], v34 offset:8192\n" \
"v_mfma_f32_16x16x32_bf16 a[16:19], v[140:143], v[84:87], a[16:19]\n" \
"buffer_load_dwordx4 v24, s[76:79], s84 offen lds\n" \
"v_mfma_f32_16x16x32_bf16 a[48:51], v[124:127], v[84:87], a[48:51]\n" \
"s_add_u32 m0, %[mA], 0x9c00\n" \
"s_mov_b32 s85, 0xff80\n" \
"v_mfma_f32_16x16x32_bf16 a[48:51], v[128:131], v[88:91], a[48:51]\n" \
"ds_read_b64_tr_b16 v[170:171], v34 offset:9216\n" \
"ds_read_b64_tr_b16 v[176:177], v35 offset:8192\n" \
"v_mfma_f32_16x16x32_bf16 a[80:83], v[88:91], v[136:139], a[80:83]\n" \
"buffer_load_dwordx4 v25, s[76:79], s85 offen lds\n" \
"v_mfma_f32_16x16x32_bf16 a[80:83], v[84:87], v[132:135], a[80:83]\n" \
"s_add_u32 m0, %[mB], 0x8c00\n" \
"s_mov_b32 s84, 0x398000\n" \
"v_mfma_f32_16x16x32_bf16 a[112:115], v[140:143], v[52:55], a[112:115]\n" \
"ds_read_b64_tr_b16 v[178:179], v35 offset:9216\n" \
"ds_read_b64_tr_b16 v[172:173], v35 offset:0\n" \
"v_mfma_f32_16x16x32_bf16 a[112:115], v[144:147], v[56:59], a[112:115]\n" \
"buffer_load_dwordx4 v27, s[68:71], s84 offen lds\n" \
"v_mfma_f32_16x16x32_bf16 a[20:23], v[128:131], v[56:59], a[20:23]\n" \
"ds_read_b64_tr_b16 v[174:175], v35 offset:1024\n" \
"ds_read_b128 v[84:87], v48 offset:0\n" \
"v_mfma_f32_16x16x32_bf16 a[20:23], v[124:127], v[52:55], a[20:23]\n" \
"s_add_u32 s89, s43, 0x1000\n" \
"ds_read_b128 v[88:91], v49 offset:0\n" \
"v_mfma_f32_16x16x32_bf16 a[52:55], v[116:119], v[52:55], a[52:55]\n" \
"buffer_store_dwordx4 a[36:39], v20, s[80:83], s89 offen offset:0 sc1\n" \
"v_mfma_f32_16x16x32_bf16 a[52:55], v[120:123], v[56:59], a[52:55]\n" \
"ds_read_b128 v[108:111], v48 offset:6144\n" \
"ds_read_b128 v[112:115], v49 offset:6144\n" \
"v_mfma_f32_16x16x32_bf16 a[84:87], v[56:59], v[136:139], a[84:87]\n" \
"v_mfma_f32_16x16x32_bf16 a[84:87], v[52:55], v[132:135], a[84:87]\n" \
"v_mfma_f32_16x16x32_bf16 a[116:119], v[140:143], v[60:63], a[116:119]\n" \
"v_mfma_f32_16x16x32_bf16 a[116:119], v[144:147], v[64:67], a[116:119]\n" \
"v_mfma_f32_16x16x32_bf16 a[24:27], v[128:131], v[64:67], a[24:27]\n" \
"v_mfma_f32_16x16x32_bf16 a[24:27], v[124:127], v[60:63], a[24:27]\n" \
"v_mfma_f32_16x16x32_bf16 a[56:59], v[116:119], v[60:63], a[56:59]\n" \
"v_mfma_f32_16x16x32_bf16 a[56:59], v[120:123], v[64:67], a[56:59]\n" \
"s_add_u32 s88, s43, 0x2000\n" \
"v_mfma_f32_16x16x32_bf16 a[88:91], v[64:67], v[136:139], a[88:91]\n" \
"buffer_store_dwordx4 a[64:67], v20, s[80:83], s88 offen offset:0 sc1\n" \
"v_mfma_f32_16x16x32_bf16 a[88:91], v[60:63], v[132:135], a[88:91]\n" \
"v_mfma_f32_16x16x32_bf16 a[120:123], v[140:143], v[68:71], a[120:123]\n" \
"v_mfma_f32_16x16x32_bf16 a[120:123], v[144:147], v[72:75], a[120:123]\n" \
"v_mfma_f32_16x16x32_bf16 a[28:31], v[128:131], v[72:75], a[28:31]\n" \
"v_mfma_f32_16x16x32_bf16 a[28:31], v[124:127], v[68:71], a[28:31]\n" \
"v_mfma_f32_16x16x32_bf16 a[60:63], v[116:119], v[68:71], a[60:63]\n" \
"v_mfma_f32_16x16x32_bf16 a[60:63], v[120:123], v[72:75], a[60:63]\n" \
"v_mfma_f32_16x16x32_bf16 a[92:95], v[72:75], v[136:139], a[92:95]\n" \
"v_mfma_f32_16x16x32_bf16 a[92:95], v[68:71], v[132:135], a[92:95]\n" \
"s_add_u32 s89, s43, 0x3000\n" \
"v_mfma_f32_16x16x32_bf16 a[124:127], v[140:143], v[76:79], a[124:127]\n" \
"buffer_store_dwordx4 a[68:71], v20, s[80:83], s89 offen offset:0 sc1\n" \
"v_mfma_f32_16x16x32_bf16 a[124:127], v[144:147], v[80:83], a[124:127]\n" \
"v_mfma_f32_16x16x32_bf16 a[0:3], v[128:131], v[80:83], a[0:3]\n" \
"v_mfma_f32_16x16x32_bf16 a[0:3], v[124:127], v[76:79], a[0:3]\n" \
"v_mfma_f32_16x16x32_bf16 a[4:7], v[116:119], v[76:79], a[4:7]\n" \
"v_mfma_f32_16x16x32_bf16 a[4:7], v[120:123], v[80:83], a[4:7]\n" \
"v_mfma_f32_16x16x32_bf16 a[32:35], v[80:83], v[136:139], a[32:35]\n" \
"v_mfma_f32_16x16x32_bf16 a[32:35], v[76:79], v[132:135], a[32:35]\n" \
"s_waitcnt lgkmcnt(0)\n" \
"s_nop 0\n" \
"v_mfma_f32_16x16x32_bf16 a[128:131], v[148:151], v[52:55], a[128:131]\n" \
"v_mfma_f32_16x16x32_bf16 a[128:131], v[152:155], v[56:59], a[128:131]\n" \
"v_mfma_f32_16x16x32_bf16 a[132:135], v[160:163], v[56:59], a[132:135]\n" \
"v_mfma_f32_16x16x32_bf16 a[132:135], v[156:159], v[52:55], a[132:135]\n" \
"v_mfma_f32_16x16x32_bf16 a[136:139], v[164:167], v[52:55], a[136:139]\n" \
"v_mfma_f32_16x16x32_bf16 a[136:139], v[168:171], v[56:59], a[136:139]\n" \
"v_mfma_f32_16x16x32_bf16 a[140:143], v[176:179], v[56:59], a[140:143]\n" \
"v_mfma_f32_16x16x32_bf16 a[140:143], v[172:175], v[52:55], a[140:143]\n" \
"v_mfma_f32_16x16x32_bf16 a[160:163], v[148:151], v[60:63], a[160:163]\n" \
"v_mfma_f32_16x16x32_bf16 a[160:163], v[152:155], v[64:67], a[160:163]\n" \
"v_mfma_f32_16x16x32_bf16 a[164:167], v[160:163], v[64:67], a[164:167]\n" \
"v_mfma_f32_16x16x32_bf16 a[164:167], v[156:159], v[60:63], a[164:167]\n" \
"v_mfma_f32_16x16x32_bf16 a[168:171], v[164:167], v[60:63], a[168:171]\n" \
"v_mfma_f32_16x16x32_bf16 a[168:171], v[168:171], v[64:67], a[168:171]\n" \
"v_mfma_f32_16x16x32_bf16 a[172:175], v[176:179], v[64:67], a[172:175]\n" \
"v_mfma_f32_16x16x32_bf16 a[172:175], v[172:175], v[60:63], a[172:175]\n" \
"v_mfma_f32_16x16x32_bf16 a[192:195], v[148:151], v[68:71], a[192:195]\n" \
"v_mfma_f32_16x16x32_bf16 a[192:195], v[152:155], v[72:75], a[192:195]\n" \
"s_waitcnt vmcnt(3)\n" \
"s_barrier\n" \
"v_mfma_f32_16x16x32_bf16 a[196:199], v[160:163], v[72:75], a[196:199]\n" \
"s_add_u32 m0, s40, 0x0\n" \
"s_nop 0\n" \
"s_add_u32 s85, s37, 0x10000\n" \
"v_mfma_f32_16x16x32_bf16 a[196:199], v[156:159], v[68:71], a[196:199]\n" \
"buffer_load_dwordx4 v20, s[80:83], s85 offen sc1 lds\n" \
"v_mfma_f32_16x16x32_bf16 a[200:203], v[164:167], v[68:71], a[200:203]\n" \
"s_add_u32 m0, s40, 0x1000\n" \
"s_add_u32 s84, s37, 0x11000\n" \
"v_mfma_f32_16x16x32_bf16 a[200:203], v[168:171], v[72:75], a[200:203]\n" \
"ds_read_b64_tr_b16 v[140:141], v39 offset:32768\n" \
"ds_read_b64_tr_b16 v[142:143], v39 offset:33280\n" \
"v_mfma_f32_16x16x32_bf16 a[204:207], v[176:179], v[72:75], a[204:207]\n" \
"buffer_load_dwordx4 v20, s[80:83], s84 offen sc1 lds\n" \
"v_mfma_f32_16x16x32_bf16 a[204:207], v[172:175], v[68:71], a[204:207]\n" \
"ds_read_b64_tr_b16 v[144:145], v39 offset:36864\n" \
"ds_read_b64_tr_b16 v[146:147], v39 offset:37376\n" \
"v_mfma_f32_16x16x32_bf16 a[224:227], v[148:151], v[76:79], a[224:227]\n" \
"s_add_u32 m0, s40, 0x3000\n" \
"s_mov_b32 s85, 0x400\n" \
"v_mfma_f32_16x16x32_bf16 a[224:227], v[152:155], v[80:83], a[224:227]\n" \
"buffer_load_dwordx4 v21, s[76:79], s85 offen lds\n" \
"v_mfma_f32_16x16x32_bf16 a[228:231], v[160:163], v[80:83], a[228:231]\n" \
"ds_read_b64_tr_b16 v[128:129], v37 offset:36864\n" \
"ds_read_b64_tr_b16 v[130:131], v37 offset:37376\n" \
"v_mfma_f32_16x16x32_bf16 a[228:231], v[156:159], v[76:79], a[228:231]\n" \
"ds_read_b64_tr_b16 v[124:125], v37 offset:32768\n" \
"ds_read_b64_tr_b16 v[126:127], v37 offset:33280\n" \
"v_mfma_f32_16x16x32_bf16 a[232:235], v[164:167], v[76:79], a[232:235]\n" \
"s_add_u32 m0, %[mA], 0x0\n" \
"s_nop 0\n" \
"s_mov_b32 s84, 0x400\n" \
"v_mfma_f32_16x16x32_bf16 a[232:235], v[168:171], v[80:83], a[232:235]\n" \
"buffer_load_dwordx4 v24, s[76:79], s84 offen lds\n" \
"v_mfma_f32_16x16x32_bf16 a[236:239], v[176:179], v[80:83], a[236:239]\n" \
"ds_read_b64_tr_b16 v[132:133], v38 offset:32768\n" \
"ds_read_b64_tr_b16 v[134:135], v38 offset:33280\n" \
"v_mfma_f32_16x16x32_bf16 a[236:239], v[172:175], v[76:79], a[236:239]\n" \
"s_add_u32 m0, %[mA], 0x400\n" \
"s_mov_b32 s85, 0x2800\n" \
"v_mfma_f32_16x16x32_bf16 a[144:147], v[148:151], v[84:87], a[144:147]\n" \
"buffer_load_dwordx4 v25, s[76:79], s85 offen lds\n" \
"v_mfma_f32_16x16x32_bf16 a[144:147], v[152:155], v[88:91], a[144:147]\n" \
"ds_read_b64_tr_b16 v[136:137], v38 offset:36864\n" \
"ds_read_b64_tr_b16 v[138:139], v38 offset:37376\n" \
"v_mfma_f32_16x16x32_bf16 a[148:151], v[160:163], v[88:91], a[148:151]\n" \
"ds_read_b128 v[56:59], v29 offset:32768\n" \
"ds_read_b128 v[52:55], v28 offset:32768\n" \
"v_mfma_f32_16x16x32_bf16 a[148:151], v[156:159], v[84:87], a[148:151]\n" \
"s_add_u32 m0, %[mB], 0x0\n" \
"s_nop 0\n" \
"s_mov_b32 s84, 0x400000\n" \
"v_mfma_f32_16x16x32_bf16 a[152:155], v[164:167], v[84:87], a[152:155]\n" \
"buffer_load_dwordx4 v26, s[68:71], s84 offen lds\n" \
"v_mfma_f32_16x16x32_bf16 a[152:155], v[168:171], v[88:91], a[152:155]\n" \
"ds_read_b64_tr_b16 v[120:121], v36 offset:36864\n" \
"ds_read_b64_tr_b16 v[122:123], v36 offset:37376\n" \
"v_mfma_f32_16x16x32_bf16 a[156:159], v[176:179], v[88:91], a[156:159]\n" \
"ds_read_b128 v[88:91], v23 offset:32768\n" \
"ds_read_b64_tr_b16 v[116:117], v36 offset:32768\n" \
"v_mfma_f32_16x16x32_bf16 a[156:159], v[172:175], v[84:87], a[156:159]\n" \
"s_add_u32 m0, %[mA], 0x800\n" \
"s_mov_b32 s85, 0x4c00\n" \
"v_mfma_f32_16x16x32_bf16 a[176:179], v[148:151], v[92:95], a[176:179]\n" \
"buffer_load_dwordx4 v24, s[76:79], s85 offen lds\n" \
"v_mfma_f32_16x16x32_bf16 a[176:179], v[152:155], v[96:99], a[176:179]\n" \
"ds_read_b128 v[84:87], v22 offset:32768\n" \
"ds_read_b64_tr_b16 v[118:119], v36 offset:33280\n" \
"v_mfma_f32_16x16x32_bf16 a[180:183], v[160:163], v[96:99], a[180:183]\n" \
"s_add_u32 m0, %[mA], 0xc00\n" \
"s_mov_b32 s84, 0x7000\n" \
"v_mfma_f32_16x16x32_bf16 a[180:183], v[156:159], v[92:95], a[180:183]\n" \
"buffer_load_dwordx4 v25, s[76:79], s84 offen lds\n" \
"v_mfma_f32_16x16x32_bf16 a[184:187], v[164:167], v[92:95], a[184:187]\n" \
"ds_read_b128 v[64:67], v29 offset:34816\n" \
"ds_read_b128 v[60:63], v28 offset:34816\n" \
"v_mfma_f32_16x16x32_bf16 a[184:187], v[168:171], v[96:99], a[184:187]\n" \
"ds_read_b128 v[72:75], v29 offset:36864\n" \
"ds_read_b128 v[68:71], v28 offset:36864\n" \
"v_mfma_f32_16x16x32_bf16 a[188:191], v[176:179], v[96:99], a[188:191]\n" \
"s_add_u32 m0, %[mB], 0x400\n" \
"s_mov_b32 s85, 0x408000\n" \
"v_mfma_f32_16x16x32_bf16 a[188:191], v[172:175], v[92:95], a[188:191]\n" \
"buffer_load_dwordx4 v26, s[68:71], s85 offen lds\n" \
"v_mfma_f32_16x16x32_bf16 a[208:211], v[148:151], v[100:103], a[208:211]\n" \
"ds_read_b128 v[80:83], v29 offset:38912\n" \
"ds_read_b128 v[76:79], v28 offset:38912\n" \
"v_mfma_f32_16x16x32_bf16 a[208:211], v[152:155], v[104:107], a[208:211]\n" \
"ds_read_b128 v[92:95], v48 offset:34816\n" \
"ds_read_b128 v[96:99], v49 offset:34816\n" \
"v_mfma_f32_16x16x32_bf16 a[212:215], v[160:163], v[104:107], a[212:215]\n" \
"s_add_u32 m0, %[mA], 0x1000\n" \
"s_mov_b32 s84, 0x9400\n" \
"v_mfma_f32_16x16x32_bf16 a[212:215], v[156:159], v[100:103], a[212:215]\n" \
"buffer_load_dwordx4 v24, s[76:79], s84 offen lds\n" \
"v_mfma_f32_16x16x32_bf16 a[216:219], v[164:167], v[100:103], a[216:219]\n" \
"v_and_b32_e32 v26, 0x1ff, v20\n" \
"v_add_u32_e64 v21, s48, v20\n" \
"v_mfma_f32_16x16x32_bf16 a[216:219], v[168:171], v[104:107], a[216:219]\n" \
"s_add_u32 m0, %[mA], 0x1400\n" \
"s_mov_b32 s85, 0xb800\n" \
"v_mfma_f32_16x16x32_bf16 a[220:223], v[176:179], v[104:107], a[220:223]\n" \
"buffer_load_dwordx4 v25, s[76:79], s85 offen lds\n" \
"v_mfma_f32_16x16x32_bf16 a[220:223], v[172:175], v[100:103], a[220:223]\n" \
"ds_read_b128 v[100:103], v48 offset:36864\n" \
"ds_read_b128 v[104:107], v49 offset:36864\n" \
"v_mfma_f32_16x16x32_bf16 a[240:243], v[148:151], v[108:111], a[240:243]\n" \
"ds_read_b64_tr_b16 v[148:149], v32 offset:32768\n" \
"ds_read_b64_tr_b16 v[150:151], v32 offset:33792\n" \
"v_mfma_f32_16x16x32_bf16 a[240:243], v[152:155], v[112:115], a[240:243]\n" \
"s_add_u32 m0, %[mB], 0x800\n" \
"s_mov_b32 s84, 0x410000\n" \
"v_mfma_f32_16x16x32_bf16 a[244:247], v[160:163], v[112:115], a[244:247]\n" \
"buffer_load_dwordx4 v27, s[68:71], s84 offen lds\n" \
"v_mfma_f32_16x16x32_bf16 a[244:247], v[156:159], v[108:111], a[244:247]\n" \
"ds_read_b64_tr_b16 v[152:153], v32 offset:40960\n" \
"ds_read_b64_tr_b16 v[154:155], v32 offset:41984\n" \
"v_mfma_f32_16x16x32_bf16 a[248:251], v[164:167], v[108:111], a[248:251]\n" \
"ds_read_b64_tr_b16 v[156:157], v33 offset:32768\n" \
"ds_read_b64_tr_b16 v[158:159], v33 offset:33792\n" \
"v_mfma_f32_16x16x32_bf16 a[248:251], v[168:171], v[112:115], a[248:251]\n" \
"ds_read_b64_tr_b16 v[160:161], v33 offset:40960\n" \
"ds_read_b64_tr_b16 v[162:163], v33 offset:41984\n" \
"v_mfma_f32_16x16x32_bf16 a[252:255], v[176:179], v[112:115], a[252:255]\n" \
"ds_read_b64_tr_b16 v[168:169], v34 offset:40960\n" \
"ds_read_b64_tr_b16 v[170:171], v34 offset:41984\n" \
"v_mfma_f32_16x16x32_bf16 a[252:255], v[172:175], v[108:111], a[252:255]\n" \
"s_add_u32 m0, %[mA], 0x1800\n" \
"s_waitcnt lgkmcnt(14)\n" \
"s_nop 0\n" \
"L_pf8:\n" \
"s_cbranch_execz L_pf12\n" \
"s_nop 0\n" \
"v_mfma_f32_16x16x32_bf16 a[16:19], v[140:143], v[84:87], a[16:19]\n" \
"s_mov_b32 s84, 0xdc00\n" \
"ds_read_b64_tr_b16 v[164:165], v34 offset:32768\n" \
"v_mfma_f32_16x16x32_bf16 a[16:19], v[144:147], v[88:91], a[16:19]\n" \
"buffer_load_dwordx4 v24, s[76:79], s84 offen lds\n" \
"v_mfma_f32_16x16x32_bf16 a[48:51], v[128:131], v[88:91], a[48:51]\n" \
"s_add_u32 m0, %[mA], 0x1c00\n" \
"s_mov_b32 s85, 0x10000\n" \
"v_mfma_f32_16x16x32_bf16 a[48:51], v[124:127], v[84:87], a[48:51]\n" \
"ds_read_b64_tr_b16 v[166:167], v34 offset:33792\n" \
"ds_read_b64_tr_b16 v[172:173], v35 offset:32768\n" \
"v_mfma_f32_16x16x32_bf16 a[80:83], v[84:87], v[132:135], a[80:83]\n" \
"buffer_load_dwordx4 v25, s[76:79], s85 offen lds\n" \
"s_mov_b32 s76, s54\n" \
"s_nop 0\n" \
"v_mfma_f32_16x16x32_bf16 a[80:83], v[88:91], v[136:139], a[80:83]\n" \
"s_add_u32 m0, %[mB], 0xc00\n" \
"s_mov_b32 s84, 0x418000\n" \
"v_mfma_f32_16x16x32_bf16 a[112:115], v[144:147], v[56:59], a[112:115]\n" \
"ds_read_b64_tr_b16 v[174:175], v35 offset:33792\n" \
"ds_read_b64_tr_b16 v[176:177], v35 offset:40960\n" \
"v_mfma_f32_16x16x32_bf16 a[112:115], v[140:143], v[52:55], a[112:115]\n" \
"buffer_load_dwordx4 v27, s[68:71], s84 offen lds\n" \
"v_mfma_f32_16x16x32_bf16 a[20:23], v[124:127], v[52:55], a[20:23]\n" \
"ds_read_b64_tr_b16 v[178:179], v35 offset:41984\n" \
"ds_read_b128 v[84:87], v48 offset:32768\n" \
"v_mfma_f32_16x16x32_bf16 a[20:23], v[128:131], v[56:59], a[20:23]\n" \
"s_add_u32 s88, s43, 0x0\n" \
"s_nop 0\n" \
"ds_read_b128 v[88:91], v49 offset:32768\n" \
"v_mfma_f32_16x16x32_bf16 a[52:55], v[120:123], v[56:59], a[52:55]\n" \
"buffer_store_dwordx4 a[96:99], v20, s[80:83], s88 offen offset:1024 sc1\n" \
"v_mfma_f32_16x16x32_bf16 a[52:55], v[116:119], v[52:55], a[52:55]\n" \
"ds_read_b128 v[108:111], v48 offset:38912\n" \
"ds_read_b128 v[112:115], v49 offset:38912\n" \
"v_mfma_f32_16x16x32_bf16 a[84:87], v[52:55], v[132:135], a[84:87]\n" \
"v_accvgpr_read_b32 v248, a16\n" \
"v_accvgpr_read_b32 v249, a17\n" \
"v_mfma_f32_16x16x32_bf16 a[84:87], v[56:59], v[136:139], a[84:87]\n" \
"v_accvgpr_read_b32 v250, a18\n" \
"v_accvgpr_read_b32 v251, a19\n" \
"v_mfma_f32_16x16x32_bf16 a[116:119], v[144:147], v[64:67], a[116:119]\n" \
"v_accvgpr_read_b32 v180, a48\n" \
"v_accvgpr_read_b32 v181, a49\n" \
"v_mfma_f32_16x16x32_bf16 a[116:119], v[140:143], v[60:63], a[116:119]\n" \
"v_accvgpr_read_b32 v182, a50\n" \
"v_accvgpr_read_b32 v183, a51\n" \
"v_mfma_f32_16x16x32_bf16 a[24:27], v[124:127], v[60:63], a[24:27]\n" \
"v_accvgpr_read_b32 v252, a80\n" \
"v_accvgpr_read_b32 v253, a81\n" \
"v_mfma_f32_16x16x32_bf16 a[24:27], v[128:131], v[64:67], a[24:27]\n" \
"v_accvgpr_read_b32 v254, a82\n" \
"v_accvgpr_read_b32 v255, a83\n" \
"v_mfma_f32_16x16x32_bf16 a[56:59], v[120:123], v[64:67], a[56:59]\n" \
"v_accvgpr_read_b32 v196, a112\n" \
"v_accvgpr_read_b32 v197, a113\n" \
"v_mfma_f32_16x16x32_bf16 a[56:59], v[116:119], v[60:63], a[56:59]\n" \
"s_add_u32 s89, s43, 0x1000\n" \
"v_accvgpr_read_b32 v198, a114\n" \
"v_mfma_f32_16x16x32_bf16 a[88:91], v[60:63], v[132:135], a[88:91]\n" \
"buffer_store_dwordx4 a[100:103], v20, s[80:83], s89 offen offset:1024 sc1\n" \
"s_add_u32 s89, s43, 0x3000\n" \
"v_mfma_f32_16x16x32_bf16 a[88:91], v[64:67], v[136:139], a[88:91]\n" \
"v_accvgpr_read_b32 v199, a115\n" \
"v_accvgpr_read_b32 v188, a20\n" \
"v_mfma_f32_16x16x32_bf16 a[120:123], v[144:147], v[72:75], a[120:123]\n" \
"v_accvgpr_read_b32 v189, a21\n" \
"v_accvgpr_read_b32 v190, a22\n" \
"v_mfma_f32_16x16x32_bf16 a[120:123], v[140:143], v[68:71], a[120:123]\n" \
"v_accvgpr_read_b32 v191, a23\n" \
"v_accvgpr_read_b32 v184, a52\n" \
"v_mfma_f32_16x16x32_bf16 a[28:31], v[124:127], v[68:71], a[28:31]\n" \
"v_accvgpr_read_b32 v185, a53\n" \
"v_accvgpr_read_b32 v186, a54\n" \
"v_mfma_f32_16x16x32_bf16 a[28:31], v[128:131], v[72:75], a[28:31]\n" \
"v_accvgpr_read_b32 v187, a55\n" \
"v_accvgpr_read_b32 v192, a84\n" \
"v_mfma_f32_16x16x32_bf16 a[60:63], v[120:123], v[72:75], a[60:63]\n" \
"v_accvgpr_read_b32 v193, a85\n" \
"v_accvgpr_read_b32 v194, a86\n" \
"v_mfma_f32_16x16x32_bf16 a[60:63], v[116:119], v[68:71], a[60:63]\n" \
"v_accvgpr_read_b32 v195, a87\n" \
"v_accvgpr_read_b32 v212, a116\n" \
"v_mfma_f32_16x16x32_bf16 a[92:95], v[68:71], v[132:135], a[92:95]\n" \
"v_accvgpr_read_b32 v213, a117\n" \
"v_accvgpr_read_b32 v214, a118\n" \
"v_mfma_f32_16x16x32_bf16 a[92:95], v[72:75], v[136:139], a[92:95]\n" \
"s_add_u32 s88, s43, 0x2000\n" \
"v_accvgpr_read_b32 v215, a119\n" \
"v_mfma_f32_16x16x32_bf16 a[124:127], v[144:147], v[80:83], a[124:127]\n" \
"buffer_store_dwordx4 a[8:11], v20, s[80:83], s88 offen offset:1024 sc1\n" \
"s_add_u32 s88, s45, 0x0\n" \
"s_nop 0\n" \
"v_mfma_f32_16x16x32_bf16 a[124:127], v[140:143], v[76:79], a[124:127]\n" \
"v_accvgpr_read_b32 v204, a24\n" \
"v_accvgpr_read_b32 v205, a25\n" \
"v_mfma_f32_16x16x32_bf16 a[0:3], v[124:127], v[76:79], a[0:3]\n" \
"v_accvgpr_read_b32 v206, a26\n" \
"v_accvgpr_read_b32 v207, a27\n" \
"v_mfma_f32_16x16x32_bf16 a[0:3], v[128:131], v[80:83], a[0:3]\n" \
"v_accvgpr_read_b32 v200, a56\n" \
"v_accvgpr_read_b32 v201, a57\n" \
"v_mfma_f32_16x16x32_bf16 a[4:7], v[120:123], v[80:83], a[4:7]\n" \
"v_accvgpr_read_b32 v202, a58\n" \
"v_accvgpr_read_b32 v203, a59\n" \
"v_mfma_f32_16x16x32_bf16 a[4:7], v[116:119], v[76:79], a[4:7]\n" \
"v_accvgpr_read_b32 v208, a88\n" \
"v_accvgpr_read_b32 v209, a89\n" \
"v_mfma_f32_16x16x32_bf16 a[32:35], v[76:79], v[132:135], a[32:35]\n" \
"v_accvgpr_read_b32 v210, a90\n" \
"v_accvgpr_read_b32 v211, a91\n" \
"v_mfma_f32_16x16x32_bf16 a[32:35], v[80:83], v[136:139], a[32:35]\n" \
"v_accvgpr_read_b32 v228, a120\n" \
"s_waitcnt lgkmcnt(0)\n" \
"v_mfma_f32_16x16x32_bf16 a[128:131], v[152:155], v[56:59], a[128:131]\n" \
"v_accvgpr_read_b32 v229, a121\n" \
"v_accvgpr_read_b32 v230, a122\n" \
"v_mfma_f32_16x16x32_bf16 a[128:131], v[148:151], v[52:55], a[128:131]\n" \
"v_lshlrev_b32_e32 v45, 10, v20\n" \
"v_add_u32_e64 v45, s41, v45\n" \
"v_mfma_f32_16x16x32_bf16 a[132:135], v[156:159], v[52:55], a[132:135]\n" \
"s_mov_b32 m0, %[mS]\n" \
"s_nop 0\n" \
"v_accvgpr_read_b32 v231, a123\n" \
"v_mfma_f32_16x16x32_bf16 a[132:135], v[160:163], v[56:59], a[132:135]\n" \
"v_accvgpr_read_b32 v220, a28\n" \
"v_accvgpr_read_b32 v221, a29\n" \
"v_mfma_f32_16x16x32_bf16 a[136:139], v[168:171], v[56:59], a[136:139]\n" \
"v_accvgpr_read_b32 v222, a30\n" \
"v_accvgpr_read_b32 v223, a31\n" \
"v_mfma_f32_16x16x32_bf16 a[136:139], v[164:167], v[52:55], a[136:139]\n" \
"v_accvgpr_read_b32 v216, a60\n" \
"v_accvgpr_read_b32 v217, a61\n" \
"v_mfma_f32_16x16x32_bf16 a[140:143], v[172:175], v[52:55], a[140:143]\n" \
"v_accvgpr_read_b32 v218, a62\n" \
"v_accvgpr_read_b32 v219, a63\n" \
"s_getpc_b64 s[86:87]\n" \
"L_ct15_%=:\n" \
"s_add_u32 s86, s86, L_end_%=-L_ct15_%=\n" \
"s_addc_u32 s87, s87, 0\n" \
"v_mfma_f32_16x16x32_bf16 a[140:143], v[176:179], v[56:59], a[140:143]\n" \
"v_accvgpr_read_b32 v224, a92\n" \
"v_accvgpr_read_b32 v225, a93\n" \
"v_mfma_f32_16x16x32_bf16 a[160:163], v[152:155], v[64:67], a[160:163]\n" \
"v_accvgpr_read_b32 v226, a94\n" \
"v_accvgpr_read_b32 v227, a95\n" \
"v_mfma_f32_16x16x32_bf16 a[160:163], v[148:151], v[60:63], a[160:163]\n" \
"v_accvgpr_read_b32 v244, a124\n" \
"v_accvgpr_read_b32 v245, a125\n" \
"v_mfma_f32_16x16x32_bf16 a[164:167], v[156:159], v[60:63], a[164:167]\n" \
"v_accvgpr_read_b32 v246, a126\n" \
"v_accvgpr_read_b32 v247, a127\n" \
"v_mfma_f32_16x16x32_bf16 a[164:167], v[160:163], v[64:67], a[164:167]\n" \
"v_accvgpr_read_b32 v236, a0\n" \
"v_accvgpr_read_b32 v237, a1\n" \
"v_mfma_f32_16x16x32_bf16 a[168:171], v[168:171], v[64:67], a[168:171]\n" \
"v_accvgpr_read_b32 v238, a2\n" \
"v_accvgpr_read_b32 v239, a3\n" \
"v_mfma_f32_16x16x32_bf16 a[168:171], v[164:167], v[60:63], a[168:171]\n" \
"v_accvgpr_read_b32 v232, a4\n" \
"v_accvgpr_read_b32 v233, a5\n" \
"v_mfma_f32_16x16x32_bf16 a[172:175], v[172:175], v[60:63], a[172:175]\n" \
"v_accvgpr_read_b32 v234, a6\n" \
"v_accvgpr_read_b32 v235, a7\n" \
"v_mfma_f32_16x16x32_bf16 a[172:175], v[176:179], v[64:67], a[172:175]\n" \
"v_accvgpr_read_b32 v240, a32\n" \
"v_accvgpr_read_b32 v241, a33\n" \
"v_mfma_f32_16x16x32_bf16 a[192:195], v[152:155], v[72:75], a[192:195]\n" \
"v_accvgpr_read_b32 v242, a34\n" \
"v_accvgpr_read_b32 v243, a35\n" \
"v_mfma_f32_16x16x32_bf16 a[192:195], v[148:151], v[68:71], a[192:195]\n" \
"s_mov_b32 s68, 0xffc0fffc\n" \
"s_mov_b32 s69, 0xc000fc00\n" \
"s_waitcnt vmcnt(3)\n" \
"s_barrier\n" \
"s_mov_b64 exec, 3\n" \
"global_load_lds_dword v45, s[86:87]\n" \
"s_mov_b64 exec, -1\n" \
"s_sub_u32 m0, %[mB], 0x10000\n" \
"v_mfma_f32_16x16x32_bf16 a[196:199], v[156:159], v[68:71], a[196:199]\n" \
"ds_read_b64_tr_b16 v[144:145], v39 offset:4096\n" \
"ds_read_b64_tr_b16 v[146:147], v39 offset:4608\n" \
"v_mfma_f32_16x16x32_bf16 a[196:199], v[160:163], v[72:75], a[196:199]\n" \
"ds_read_b64_tr_b16 v[140:141], v39 offset:0\n" \
"ds_read_b64_tr_b16 v[142:143], v39 offset:512\n" \
"v_mfma_f32_16x16x32_bf16 a[200:203], v[168:171], v[72:75], a[200:203]\n" \
"ds_read_b64_tr_b16 v[124:125], v37 offset:0\n" \
"ds_read_b64_tr_b16 v[126:127], v37 offset:512\n" \
"v_mfma_f32_16x16x32_bf16 a[200:203], v[164:167], v[68:71], a[200:203]\n" \
"ds_read_b64_tr_b16 v[128:129], v37 offset:4096\n" \
"ds_read_b64_tr_b16 v[130:131], v37 offset:4608\n" \
"v_mfma_f32_16x16x32_bf16 a[204:207], v[172:175], v[68:71], a[204:207]\n" \
"ds_read_b64_tr_b16 v[136:137], v38 offset:4096\n" \
"ds_read_b64_tr_b16 v[138:139], v38 offset:4608\n" \
"v_mfma_f32_16x16x32_bf16 a[204:207], v[176:179], v[72:75], a[204:207]\n" \
"ds_read_b64_tr_b16 v[132:133], v38 offset:0\n" \
"ds_read_b64_tr_b16 v[134:135], v38 offset:512\n" \
"v_mfma_f32_16x16x32_bf16 a[224:227], v[152:155], v[80:83], a[224:227]\n" \
"ds_read_b128 v[52:55], v28 offset:0\n" \
"ds_read_b128 v[56:59], v29 offset:0\n" \
"v_mfma_f32_16x16x32_bf16 a[224:227], v[148:151], v[76:79], a[224:227]\n" \
"ds_read_b64_tr_b16 v[116:117], v36 offset:0\n" \
"ds_read_b64_tr_b16 v[118:119], v36 offset:512\n" \
"v_mfma_f32_16x16x32_bf16 a[228:231], v[156:159], v[76:79], a[228:231]\n" \
"ds_read_b64_tr_b16 v[120:121], v36 offset:4096\n" \
"ds_read_b64_tr_b16 v[122:123], v36 offset:4608\n" \
"v_mfma_f32_16x16x32_bf16 a[228:231], v[160:163], v[80:83], a[228:231]\n" \
"ds_read_b128 v[60:63], v28 offset:2048\n" \
"ds_read_b128 v[64:67], v29 offset:2048\n" \
"v_mfma_f32_16x16x32_bf16 a[232:235], v[168:171], v[80:83], a[232:235]\n" \
"v_lshrrev_b32_e32 v25, 8, v20\n" \
"v_lshrrev_b32_e32 v24, 9, v20\n" \
"v_mfma_f32_16x16x32_bf16 a[232:235], v[164:167], v[76:79], a[232:235]\n" \
"ds_read_b128 v[68:71], v28 offset:4096\n" \
"ds_read_b128 v[72:75], v29 offset:4096\n" \
"v_mfma_f32_16x16x32_bf16 a[236:239], v[172:175], v[76:79], a[236:239]\n" \
"v_mul_u32_u24_e32 v25, 0x24700, v25\n" \
"v_lshl_add_u32 v24, v24, 3, v26\n" \
"v_mfma_f32_16x16x32_bf16 a[236:239], v[176:179], v[80:83], a[236:239]\n" \
"ds_read_b128 v[76:79], v28 offset:6144\n" \
"ds_read_b128 v[80:83], v29 offset:6144\n" \
"v_mfma_f32_16x16x32_bf16 a[144:147], v[152:155], v[88:91], a[144:147]\n" \
"v_add_u32_e32 v25, v25, v20\n" \
"s_add_u32 s84, s50, 0x0\n" \
"v_mfma_f32_16x16x32_bf16 a[144:147], v[148:151], v[84:87], a[144:147]\n" \
"s_add_u32 s85, s50, 0x24800\n" \
"s_add_u32 s86, s50, 0x49000\n" \
"v_mfma_f32_16x16x32_bf16 a[148:151], v[156:159], v[84:87], a[148:151]\n" \
"s_add_u32 s87, s50, 0x6d800\n" \
"s_mov_b32 s70, 0x3e0293ee\n" \
"v_mfma_f32_16x16x32_bf16 a[148:151], v[160:163], v[88:91], a[148:151]\n" \
"s_mov_b32 s71, 0x3e0293ee\n" \
"v_add_u32_e64 v27, s47, v20\n" \
"v_mfma_f32_16x16x32_bf16 a[152:155], v[168:171], v[88:91], a[152:155]\n" \
"v_lshrrev_b32_e32 v26, 2, v20\n" \
"v_add_u32_e32 v26, s46, v26\n" \
"v_mfma_f32_16x16x32_bf16 a[152:155], v[164:167], v[84:87], a[152:155]\n" \
"v_mfma_f32_16x16x32_bf16 a[156:159], v[172:175], v[84:87], a[156:159]\n" \
"ds_read_b128 v[84:87], v22 offset:0\n" \
"v_mfma_f32_16x16x32_bf16 a[156:159], v[176:179], v[88:91], a[156:159]\n" \
"ds_read_b128 v[88:91], v23 offset:0\n" \
"v_mfma_f32_16x16x32_bf16 a[176:179], v[152:155], v[96:99], a[176:179]\n" \
"v_mfma_f32_16x16x32_bf16 a[176:179], v[148:151], v[92:95], a[176:179]\n" \
"v_mfma_f32_16x16x32_bf16 a[180:183], v[156:159], v[92:95], a[180:183]\n" \
"v_mfma_f32_16x16x32_bf16 a[180:183], v[160:163], v[96:99], a[180:183]\n" \
"v_mfma_f32_16x16x32_bf16 a[184:187], v[168:171], v[96:99], a[184:187]\n" \
"v_mfma_f32_16x16x32_bf16 a[184:187], v[164:167], v[92:95], a[184:187]\n" \
"v_mfma_f32_16x16x32_bf16 a[188:191], v[172:175], v[92:95], a[188:191]\n" \
"ds_read_b128 v[92:95], v48 offset:2048\n" \
"v_mfma_f32_16x16x32_bf16 a[188:191], v[176:179], v[96:99], a[188:191]\n" \
"ds_read_b128 v[96:99], v49 offset:2048\n" \
"v_mfma_f32_16x16x32_bf16 a[208:211], v[152:155], v[104:107], a[208:211]\n" \
"v_mfma_f32_16x16x32_bf16 a[208:211], v[148:151], v[100:103], a[208:211]\n" \
"v_mfma_f32_16x16x32_bf16 a[212:215], v[156:159], v[100:103], a[212:215]\n" \
"v_mfma_f32_16x16x32_bf16 a[212:215], v[160:163], v[104:107], a[212:215]\n" \
"v_mfma_f32_16x16x32_bf16 a[216:219], v[168:171], v[104:107], a[216:219]\n" \
"v_mfma_f32_16x16x32_bf16 a[216:219], v[164:167], v[100:103], a[216:219]\n" \
"v_mfma_f32_16x16x32_bf16 a[220:223], v[172:175], v[100:103], a[220:223]\n" \
"ds_read_b128 v[100:103], v48 offset:4096\n" \
"v_mfma_f32_16x16x32_bf16 a[220:223], v[176:179], v[104:107], a[220:223]\n" \
"ds_read_b128 v[104:107], v49 offset:4096\n" \
"v_mfma_f32_16x16x32_bf16 a[240:243], v[152:155], v[112:115], a[240:243]\n" \
"ds_read_b64_tr_b16 v[152:153], v32 offset:8192\n" \
"ds_read_b64_tr_b16 v[154:155], v32 offset:9216\n" \
"v_mfma_f32_16x16x32_bf16 a[240:243], v[148:151], v[108:111], a[240:243]\n" \
"ds_read_b64_tr_b16 v[148:149], v32 offset:0\n" \
"ds_read_b64_tr_b16 v[150:151], v32 offset:1024\n" \
"v_mfma_f32_16x16x32_bf16 a[244:247], v[156:159], v[108:111], a[244:247]\n" \
"ds_read_b64_tr_b16 v[156:157], v33 offset:0\n" \
"ds_read_b64_tr_b16 v[158:159], v33 offset:1024\n" \
"v_mfma_f32_16x16x32_bf16 a[244:247], v[160:163], v[112:115], a[244:247]\n" \
"ds_read_b64_tr_b16 v[160:161], v33 offset:8192\n" \
"ds_read_b64_tr_b16 v[162:163], v33 offset:9216\n" \
"v_mfma_f32_16x16x32_bf16 a[248:251], v[168:171], v[112:115], a[248:251]\n" \
"ds_read_b64_tr_b16 v[168:169], v34 offset:8192\n" \
"ds_read_b64_tr_b16 v[170:171], v34 offset:9216\n" \
"v_mfma_f32_16x16x32_bf16 a[248:251], v[164:167], v[108:111], a[248:251]\n" \
"ds_read_b64_tr_b16 v[164:165], v34 offset:0\n" \
"ds_read_b64_tr_b16 v[166:167], v34 offset:1024\n" \
"v_mfma_f32_16x16x32_bf16 a[252:255], v[172:175], v[108:111], a[252:255]\n" \
"ds_read_b64_tr_b16 v[172:173], v35 offset:0\n" \
"ds_read_b64_tr_b16 v[174:175], v35 offset:1024\n" \
"v_mfma_f32_16x16x32_bf16 a[252:255], v[176:179], v[112:115], a[252:255]\n" \
"ds_read_b64_tr_b16 v[176:177], v35 offset:8192\n" \
"s_waitcnt lgkmcnt(15)\n" \
"s_nop 0\n" \
"v_mfma_f32_16x16x32_bf16 v[248:251], v[144:147], v[88:91], v[248:251]\n" \
"ds_read_b64_tr_b16 v[178:179], v35 offset:9216\n" \
"v_bfe_u32 v44, v20, 2, 5\n" \
"v_mfma_f32_16x16x32_bf16 v[248:251], v[140:143], v[84:87], v[248:251]\n" \
"buffer_load_dword v44, v44, s[80:83], s39 offen offset:0 sc1\n" \
"v_mfma_f32_16x16x32_bf16 v[180:183], v[124:127], v[84:87], v[180:183]\n" \
"ds_read_b128 v[108:111], v48 offset:6144\n" \
"ds_read_b128 v[112:115], v49 offset:6144\n" \
"v_mfma_f32_16x16x32_bf16 v[180:183], v[128:131], v[88:91], v[180:183]\n" \
"v_mfma_f32_16x16x32_bf16 v[252:255], v[88:91], v[136:139], v[252:255]\n" \
"ds_read_b128 v[88:91], v49 offset:0\n" \
"v_mfma_f32_16x16x32_bf16 v[252:255], v[84:87], v[132:135], v[252:255]\n" \
"ds_read_b128 v[84:87], v48 offset:0\n" \
"v_mfma_f32_16x16x32_bf16 v[196:199], v[140:143], v[52:55], v[196:199]\n" \
"v_mfma_f32_16x16x32_bf16 v[196:199], v[144:147], v[56:59], v[196:199]\n" \
"v_mfma_f32_16x16x32_bf16 v[188:191], v[128:131], v[56:59], v[188:191]\n" \
"v_mfma_f32_16x16x32_bf16 v[188:191], v[124:127], v[52:55], v[188:191]\n" \
"v_cvt_pk_bf16_f32 v180, v180, v181\n" \
"v_cvt_pk_bf16_f32 v181, v182, v183\n" \
"v_mfma_f32_16x16x32_bf16 v[184:187], v[116:119], v[52:55], v[184:187]\n" \
"v_cvt_pk_bf16_f32 v182, v250, v251\n" \
"v_mov_b32_e32 v183, 0x3f80\n" \
"v_mfma_f32_16x16x32_bf16 v[184:187], v[120:123], v[56:59], v[184:187]\n" \
"v_mfma_f32_16x16x32_bf16 v[192:195], v[56:59], v[136:139], v[192:195]\n" \
"v_mfma_f32_16x16x16_bf16 v[248:251], v[182:183], v[46:47], 0\n" \
"v_cvt_pk_bf16_f32 v188, v188, v189\n" \
"v_cvt_pk_bf16_f32 v189, v190, v191\n" \
"v_mfma_f32_16x16x32_bf16 v[192:195], v[52:55], v[132:135], v[192:195]\n" \
"v_cvt_pk_bf16_f32 v190, v198, v199\n" \
"v_mov_b32_e32 v191, 0x3f80\n" \
"v_mfma_f32_16x16x32_bf16 v[212:215], v[140:143], v[60:63], v[212:215]\n" \
"v_cvt_pk_bf16_f32 v184, v184, v185\n" \
"v_cvt_pk_bf16_f32 v185, v186, v187\n" \
"v_mfma_f32_16x16x32_bf16 v[212:215], v[144:147], v[64:67], v[212:215]\n" \
"v_cvt_pk_bf16_f32 v186, v196, v197\n" \
"v_mov_b32_e64 v187, 0\n" \
"v_mfma_f32_16x16x32_bf16 v[204:207], v[128:131], v[64:67], v[204:207]\n" \
"v_mfma_f32_16x16x32_bf16 v[204:207], v[124:127], v[60:63], v[204:207]\n" \
"v_mfma_f32_16x16x32_bf16 v[200:203], v[116:119], v[60:63], v[200:203]\n" \
"v_mfma_f32_16x16x32_bf16 v[200:203], v[120:123], v[64:67], v[200:203]\n" \
"v_mfma_f32_16x16x32_bf16 v[208:211], v[64:67], v[136:139], v[208:211]\n" \
"v_mfma_f32_16x16x32_bf16 v[208:211], v[60:63], v[132:135], v[208:211]\n" \
"v_mfma_f32_16x16x32_bf16 v[228:231], v[140:143], v[68:71], v[228:231]\n" \
"v_mfma_f32_16x16x32_bf16 v[228:231], v[144:147], v[72:75], v[228:231]\n" \
"v_mfma_f32_16x16x32_bf16 v[220:223], v[128:131], v[72:75], v[220:223]\n" \
"v_mfma_f32_16x16x32_bf16 v[220:223], v[124:127], v[68:71], v[220:223]\n" \
"v_cvt_pk_bf16_f32 v204, v204, v205\n" \
"v_cvt_pk_bf16_f32 v205, v206, v207\n" \
"v_mfma_f32_16x16x32_bf16 v[216:219], v[116:119], v[68:71], v[216:219]\n" \
"v_cvt_pk_bf16_f32 v200, v200, v201\n" \
"v_cvt_pk_bf16_f32 v201, v202, v203\n" \
"v_mfma_f32_16x16x32_bf16 v[216:219], v[120:123], v[72:75], v[216:219]\n" \
"v_cvt_pk_bf16_f32 v206, v214, v215\n" \
"v_mov_b32_e32 v207, 0x3f80\n" \
"v_mfma_f32_16x16x32_bf16 v[224:227], v[72:75], v[136:139], v[224:227]\n" \
"v_cvt_pk_bf16_f32 v220, v220, v221\n" \
"v_cvt_pk_bf16_f32 v221, v222, v223\n" \
"v_mfma_f32_16x16x32_bf16 v[224:227], v[68:71], v[132:135], v[224:227]\n" \
"v_cvt_pk_bf16_f32 v202, v212, v213\n" \
"v_mov_b32_e64 v203, 0\n" \
"v_mfma_f32_16x16x32_bf16 v[244:247], v[140:143], v[76:79], v[244:247]\n" \
"v_cvt_pk_bf16_f32 v216, v216, v217\n" \
"v_cvt_pk_bf16_f32 v217, v218, v219\n" \
"v_mfma_f32_16x16x32_bf16 v[244:247], v[144:147], v[80:83], v[244:247]\n" \
"v_cvt_pk_bf16_f32 v222, v230, v231\n" \
"v_mov_b32_e32 v223, 0x3f80\n" \
"v_mfma_f32_16x16x32_bf16 v[236:239], v[128:131], v[80:83], v[236:239]\n" \
"v_cvt_pk_bf16_f32 v128, v208, v209\n" \
"v_cvt_pk_bf16_f32 v129, v210, v211\n" \
"v_mfma_f32_16x16x32_bf16 v[236:239], v[124:127], v[76:79], v[236:239]\n" \
"v_cvt_pk_bf16_f32 v126, v192, v193\n" \
"v_cvt_pk_bf16_f32 v127, v194, v195\n" \
"v_mfma_f32_16x16x32_bf16 v[232:235], v[116:119], v[76:79], v[232:235]\n" \
"v_cvt_pk_bf16_f32 v218, v228, v229\n" \
"v_mov_b32_e64 v219, 0\n" \
"v_mfma_f32_16x16x32_bf16 v[232:235], v[120:123], v[80:83], v[232:235]\n" \
"v_cvt_pk_bf16_f32 v130, v224, v225\n" \
"v_cvt_pk_bf16_f32 v131, v226, v227\n" \
"v_mfma_f32_16x16x32_bf16 v[240:243], v[80:83], v[136:139], v[240:243]\n" \
"v_cvt_pk_bf16_f32 v236, v236, v237\n" \
"v_cvt_pk_bf16_f32 v237, v238, v239\n" \
"v_mfma_f32_16x16x32_bf16 v[240:243], v[76:79], v[132:135], v[240:243]\n" \
"v_cvt_pk_bf16_f32 v238, v246, v247\n" \
"v_mov_b32_e32 v239, 0x3f80\n" \
"s_waitcnt lgkmcnt(0)\n" \
"s_barrier\n" \
"v_mfma_f32_16x16x32_bf16 a[128:131], v[148:151], v[52:55], a[128:131]\n" \
"v_cvt_pk_bf16_f32 v232, v232, v233\n" \
"v_cvt_pk_bf16_f32 v233, v234, v235\n" \
"v_mfma_f32_16x16x32_bf16 a[128:131], v[152:155], v[56:59], a[128:131]\n" \
"v_cvt_pk_bf16_f32 v234, v244, v245\n" \
"v_mov_b32_e64 v235, 0\n" \
"v_mfma_f32_16x16x32_bf16 a[132:135], v[160:163], v[56:59], a[132:135]\n" \
"v_cvt_pk_bf16_f32 v132, v240, v241\n" \
"v_cvt_pk_bf16_f32 v133, v242, v243\n" \
"v_mfma_f32_16x16x32_bf16 a[132:135], v[156:159], v[52:55], a[132:135]\n" \
"v_mfma_f32_16x16x32_bf16 v[116:119], v[180:183], v[184:187], 0\n" \
"v_mfma_f32_16x16x32_bf16 v[192:195], v[188:191], v[184:187], 0\n" \
"s_waitcnt vmcnt(0)\n" \
"v_cmp_ne_u32_e32 vcc, %[tag], v44\n" \
"v_mfma_f32_16x16x32_bf16 a[136:139], v[164:167], v[52:55], a[136:139]\n" \
"v_mfma_f32_16x16x32_bf16 v[208:211], v[204:207], v[200:203], 0\n" \
"v_mfma_f32_16x16x32_bf16 v[120:123], v[188:191], v[200:203], 0\n" \
"v_mfma_f32_16x16x32_bf16 v[224:227], v[220:223], v[216:219], 0\n" \
"v_mfma_f32_16x16x32_bf16 v[36:39], v[204:207], v[216:219], 0\n" \
"s_cbranch_vccnz L_pa_%=\n" \
"L_ra_%=:\n" \
"v_cndmask_b32_e64 v116, v116, v117, s[64:65]\n" \
"buffer_load_dwordx4 v20, s[80:83], s73 offen offset:0 sc1 lds\n" \
"v_mfma_f32_16x16x32_bf16 a[136:139], v[168:171], v[56:59], a[136:139]\n" \
"v_mfma_f32_16x16x32_bf16 v[240:243], v[236:239], v[232:235], 0\n" \
"v_mfma_f32_16x16x32_bf16 v[48:51], v[220:223], v[232:235], 0\n" \
"v_cndmask_b32_e64 v118, v118, v119, s[64:65]\n" \
"v_cndmask_b32_e64 v192, v192, v193, s[64:65]\n" \
"v_mfma_f32_16x16x32_bf16 a[140:143], v[176:179], v[56:59], a[140:143]\n" \
"buffer_load_dwordx4 v20, s[80:83], s73 offen offset:1024 sc1 lds\n" \
"v_mfma_f32_16x16x32_bf16 a[140:143], v[172:175], v[52:55], a[140:143]\n" \
"v_add_f32_e32 v116, s49, v116\n" \
"v_cndmask_b32_e64 v194, v194, v195, s[64:65]\n" \
"v_mfma_f32_16x16x32_bf16 a[160:163], v[148:151], v[60:63], a[160:163]\n" \
"v_add_f32_e64 v118, s49, v118\n" \
"v_cndmask_b32_e64 v208, v208, v209, s[64:65]\n" \
"v_mfma_f32_16x16x32_bf16 a[160:163], v[152:155], v[64:67], a[160:163]\n" \
"v_cndmask_b32_e64 v120, v120, v121, s[64:65]\n" \
"v_cndmask_b32_e64 v210, v210, v211, s[64:65]\n" \
"v_mfma_f32_16x16x32_bf16 a[164:167], v[160:163], v[64:67], a[164:167]\n" \
"buffer_load_dwordx4 v20, s[80:83], s73 offen offset:2048 sc1 lds\n" \
"v_mfma_f32_16x16x32_bf16 a[164:167], v[156:159], v[60:63], a[164:167]\n" \
"v_cndmask_b32_e64 v122, v122, v123, s[64:65]\n" \
"v_cndmask_b32_e64 v224, v224, v225, s[64:65]\n" \
"v_mfma_f32_16x16x32_bf16 a[168:171], v[164:167], v[60:63], a[168:171]\n" \
"v_cndmask_b32_e64 v36, v36, v37, s[64:65]\n" \
"v_cndmask_b32_e64 v226, v226, v227, s[64:65]\n" \
"v_mfma_f32_16x16x32_bf16 a[168:171], v[168:171], v[64:67], a[168:171]\n" \
"v_cndmask_b32_e64 v38, v38, v39, s[64:65]\n" \
"v_cndmask_b32_e64 v240, v240, v241, s[64:65]\n" \
"v_mfma_f32_16x16x32_bf16 a[172:175], v[176:179], v[64:67], a[172:175]\n" \
"buffer_load_dwordx4 v20, s[80:83], s73 offen offset:3072 sc1 lds\n" \
"v_mfma_f32_16x16x32_bf16 a[172:175], v[172:175], v[60:63], a[172:175]\n" \
"s_sub_u32 m0, %[mB], s53\n" \
"s_nop 0\n" \
"v_cndmask_b32_e64 v48, v48, v49, s[64:65]\n" \
"v_mfma_f32_16x16x32_bf16 a[192:195], v[148:151], v[68:71], a[192:195]\n" \
"v_cndmask_b32_e64 v242, v242, v243, s[64:65]\n" \
"v_cndmask_b32_e64 v50, v50, v51, s[64:65]\n" \
"v_mfma_f32_16x16x32_bf16 a[192:195], v[152:155], v[72:75], a[192:195]\n" \
"v_cndmask_b32_e64 v192, v116, v192, s[66:67]\n" \
"v_cndmask_b32_e64 v194, v118, v194, s[68:69]\n" \
"v_mfma_f32_16x16x32_bf16 a[196:199], v[160:163], v[72:75], a[196:199]\n" \
"buffer_load_dwordx4 v20, s[80:83], s74 offen offset:0 sc1 lds\n" \
"v_mfma_f32_16x16x32_bf16 a[196:199], v[156:159], v[68:71], a[196:199]\n" \
"v_cndmask_b32_e64 v208, v120, v208, s[66:67]\n" \
"v_cndmask_b32_e64 v210, v122, v210, s[68:69]\n" \
"v_mfma_f32_16x16x32_bf16 a[200:203], v[164:167], v[68:71], a[200:203]\n" \
"v_cndmask_b32_e64 v224, v36, v224, s[66:67]\n" \
"v_cndmask_b32_e64 v226, v38, v226, s[68:69]\n" \
"v_mfma_f32_16x16x32_bf16 a[200:203], v[168:171], v[72:75], a[200:203]\n" \
"v_cndmask_b32_e64 v240, v48, v240, s[66:67]\n" \
"v_cndmask_b32_e64 v242, v50, v242, s[68:69]\n" \
"v_mfma_f32_16x16x32_bf16 a[204:207], v[176:179], v[72:75], a[204:207]\n" \
"buffer_load_dwordx4 v20, s[80:83], s74 offen offset:1024 sc1 lds\n" \
"v_mfma_f32_16x16x32_bf16 a[204:207], v[172:175], v[68:71], a[204:207]\n" \
"v_max_f32_e32 v193, v192, v194\n" \
"v_max_f32_e32 v209, v208, v210\n" \
"v_mfma_f32_16x16x32_bf16 a[224:227], v[148:151], v[76:79], a[224:227]\n" \
"v_max_f32_e32 v225, v224, v226\n" \
"v_max_f32_e32 v241, v240, v242\n" \
"v_mfma_f32_16x16x32_bf16 a[224:227], v[152:155], v[80:83], a[224:227]\n" \
"v_permlane32_swap_b32_e32 v193, v225\n" \
"v_mfma_f32_16x16x32_bf16 a[228:231], v[160:163], v[80:83], a[228:231]\n" \
"buffer_load_dwordx4 v20, s[80:83], s74 offen offset:2048 sc1 lds\n" \
"v_mfma_f32_16x16x32_bf16 a[228:231], v[156:159], v[76:79], a[228:231]\n" \
"v_permlane32_swap_b32_e32 v209, v241\n" \
"v_mfma_f32_16x16x32_bf16 a[232:235], v[164:167], v[76:79], a[232:235]\n" \
"v_max_f32_e32 v193, v193, v225\n" \
"v_max_f32_e32 v209, v209, v241\n" \
"v_mfma_f32_16x16x32_bf16 a[232:235], v[168:171], v[80:83], a[232:235]\n" \
"v_permlane32_swap_b32_e32 v196, v228\n" \
"v_mfma_f32_16x16x32_bf16 a[236:239], v[176:179], v[80:83], a[236:239]\n" \
"buffer_load_dwordx4 v20, s[80:83], s74 offen offset:3072 sc1 lds\n" \
"v_mfma_f32_16x16x32_bf16 a[236:239], v[172:175], v[76:79], a[236:239]\n" \
"s_sub_u32 m0, %[mB], 0x4000\n" \
"v_cvt_pk_bf16_f32 v134, v248, v249\n" \
"v_permlane32_swap_b32_e32 v212, v244\n" \
"v_permlane16_swap_b32_e32 v193, v209\n" \
"v_mfma_f32_16x16x32_bf16 a[144:147], v[148:151], v[84:87], a[144:147]\n" \
"v_permlane16_swap_b32_e32 v228, v244\n" \
"v_mfma_f32_16x16x32_bf16 a[144:147], v[152:155], v[88:91], a[144:147]\n" \
"buffer_load_dwordx4 v20, s[80:83], s75 offen offset:0 sc1 lds\n" \
"v_mfma_f32_16x16x32_bf16 a[148:151], v[160:163], v[88:91], a[148:151]\n" \
"v_max_f32_e32 v193, v193, v209\n" \
"v_mul_f32_e32 v193, s72, v193\n" \
"v_mfma_f32_16x16x32_bf16 a[148:151], v[156:159], v[84:87], a[148:151]\n" \
"v_mul_f32_e32 v45, 0xbfb8aa3b, v228\n" \
"v_mov_b32_e64 v209, v193\n" \
"v_mfma_f32_16x16x16_bf16 v[196:199], v[190:191], v[46:47], 0\n" \
"v_mfma_f32_16x16x16_bf16 v[212:215], v[206:207], v[46:47], 0\n" \
"buffer_load_dwordx4 v20, s[80:83], s75 offen offset:1024 sc1 lds\n" \
"v_mfma_f32_16x16x16_bf16 v[228:231], v[222:223], v[46:47], 0\n" \
"v_permlane16_swap_b32_e32 v193, v209\n" \
"v_mfma_f32_16x16x16_bf16 v[244:247], v[238:239], v[46:47], 0\n" \
"v_mov_b32_e32 v225, v193\n" \
"v_mov_b32_e64 v241, v209\n" \
"v_mfma_f32_16x16x32_bf16 a[152:155], v[164:167], v[84:87], a[152:155]\n" \
"v_permlane32_swap_b32_e32 v193, v225\n" \
"v_permlane32_swap_b32_e32 v209, v241\n" \
"buffer_load_dwordx4 v20, s[80:83], s75 offen offset:2048 sc1 lds\n" \
"v_mfma_f32_16x16x32_bf16 a[152:155], v[168:171], v[88:91], a[152:155]\n" \
"v_fma_f32 v192, v192, s72, -v193\n" \
"v_fma_f32 v194, v194, s72, -v193\n" \
"v_mfma_f32_16x16x32_bf16 a[156:159], v[176:179], v[88:91], a[156:159]\n" \
"v_fma_f32 v208, v208, s72, -v209\n" \
"v_fma_f32 v210, v210, s72, -v209\n" \
"v_exp_f32_e32 v192, v192\n" \
"v_exp_f32_e32 v194, v194\n" \
"v_exp_f32_e64 v208, v208\n" \
"buffer_load_dwordx4 v20, s[80:83], s75 offen offset:3072 sc1 lds\n" \
"v_mfma_f32_16x16x32_bf16 a[156:159], v[172:175], v[84:87], a[156:159]\n" \
"s_mov_b32 m0, s55\n" \
"v_fma_f32 v224, v224, s72, -v225\n" \
"v_exp_f32_e32 v210, v210\n" \
"v_mfma_f32_16x16x32_bf16 a[176:179], v[148:151], v[92:95], a[176:179]\n" \
"v_fma_f32 v226, v226, s72, -v225\n" \
"v_fma_f32 v240, v240, s72, -v241\n" \
"v_exp_f32_e32 v224, v224\n" \
"v_exp_f32_e32 v226, v226\n" \
"buffer_load_dwordx4 v20, s[80:83], s76 offen offset:0 sc1 lds\n" \
"v_mfma_f32_16x16x32_bf16 a[176:179], v[152:155], v[96:99], a[176:179]\n" \
"v_fma_f32 v242, v242, s72, -v241\n" \
"v_cvt_pk_bf16_f32 v192, v192, v194\n" \
"v_mfma_f32_16x16x32_bf16 a[180:183], v[160:163], v[96:99], a[180:183]\n" \
"v_cvt_pk_bf16_f32 v208, v208, v210\n" \
"v_cvt_pk_bf16_f32 v135, v250, v251\n" \
"v_exp_f32_e32 v240, v240\n" \
"v_exp_f32_e32 v242, v242\n" \
"v_mfma_f32_16x16x32_bf16 a[180:183], v[156:159], v[92:95], a[180:183]\n" \
"buffer_load_dwordx4 v20, s[80:83], s76 offen offset:1024 sc1 lds\n" \
"v_mfma_f32_16x16x32_bf16 a[184:187], v[164:167], v[92:95], a[184:187]\n" \
"v_cvt_pk_bf16_f32 v136, v196, v197\n" \
"v_cvt_pk_bf16_f32 v137, v198, v199\n" \
"v_mfma_f32_16x16x32_bf16 a[184:187], v[168:171], v[96:99], a[184:187]\n" \
"v_cvt_pk_bf16_f32 v138, v212, v213\n" \
"v_cvt_pk_bf16_f32 v139, v214, v215\n" \
"v_mfma_f32_16x16x32_bf16 a[188:191], v[176:179], v[96:99], a[188:191]\n" \
"v_perm_b32 v184, v192, v192, v40\n" \
"v_perm_b32 v185, v192, v192, v41\n" \
"v_mfma_f32_16x16x32_bf16 a[188:191], v[172:175], v[92:95], a[188:191]\n" \
"v_perm_b32 v186, v192, v192, v42\n" \
"v_perm_b32 v187, v192, v192, v43\n" \
"v_perm_b32 v200, v208, v208, v40\n" \
"v_perm_b32 v201, v208, v208, v41\n" \
"v_perm_b32 v202, v208, v208, v42\n" \
"v_perm_b32 v203, v208, v208, v43\n" \
"v_cvt_pk_bf16_f32 v224, v224, v226\n" \
"v_cvt_pk_bf16_f32 v140, v228, v229\n" \
"v_cvt_pk_bf16_f32 v141, v230, v231\n" \
"L_pf9:\n" \
"s_cbranch_execz L_pf13\n" \
"s_nop 0\n" \
"v_mfma_f32_16x16x32_bf16 v[116:119], v[134:137], v[184:187], 0\n" \
"v_mfma_f32_16x16x32_bf16 v[120:123], v[136:139], v[200:203], 0\n" \
"v_perm_b32 v216, v224, v224, v40\n" \
"v_perm_b32 v217, v224, v224, v41\n" \
"v_perm_b32 v218, v224, v224, v42\n" \
"v_perm_b32 v219, v224, v224, v43\n" \
"v_cvt_pk_bf16_f32 v240, v240, v242\n" \
"v_cvt_pk_bf16_f32 v142, v244, v245\n" \
"v_cvt_pk_bf16_f32 v143, v246, v247\n" \
"v_mfma_f32_16x16x32_bf16 v[36:39], v[138:141], v[216:219], 0\n" \
"v_perm_b32 v232, v240, v240, v40\n" \
"v_perm_b32 v233, v240, v240, v41\n" \
"v_perm_b32 v234, v240, v240, v42\n" \
"v_perm_b32 v235, v240, v240, v43\n" \
"v_exp_f32_e64 v45, v45\n" \
"v_mov_b32_dpp v118, v122 quad_perm:[0,1,2,3] row_mask:0x2 bank_mask:0xf\n" \
"v_mfma_f32_16x16x32_bf16 v[48:51], v[140:143], v[232:235], 0\n" \
"v_cvt_pk_bf16_f32 v124, v252, v253\n" \
"v_cvt_pk_bf16_f32 v125, v254, v255\n" \
"v_mov_b32_dpp v118, v38 quad_perm:[0,1,2,3] row_mask:0x4 bank_mask:0xf\n" \
"v_add_f32_e64 v45, 1.0, v45\n" \
"v_mfma_f32_16x16x32_bf16 v[192:195], v[124:127], v[184:187], 0\n" \
"v_mfma_f32_16x16x32_bf16 v[208:211], v[126:129], v[200:203], 0\n" \
"v_mfma_f32_16x16x32_bf16 v[224:227], v[128:131], v[216:219], 0\n" \
"buffer_store_dwordx4 a[12:15], v20, s[80:83], s89 offen offset:1024 sc1\n" \
"s_add_u32 s89, s45, 0x0\n" \
"v_mfma_f32_16x16x32_bf16 v[240:243], v[130:133], v[232:235], 0\n" \
"v_mov_b32_dpp v118, v50 quad_perm:[0,1,2,3] row_mask:0x8 bank_mask:0xf\n" \
"v_rcp_f32_e32 v45, v45\n" \
"v_mov_b32_dpp v116, v120 quad_perm:[0,1,2,3] row_mask:0x2 bank_mask:0xf\n" \
"v_mov_b32_dpp v117, v121 quad_perm:[0,1,2,3] row_mask:0x2 bank_mask:0xf\n" \
"v_rcp_f32_e64 v118, v118\n" \
"v_mov_b32_dpp v116, v36 quad_perm:[0,1,2,3] row_mask:0x4 bank_mask:0xf\n" \
"v_mov_b32_dpp v117, v37 quad_perm:[0,1,2,3] row_mask:0x4 bank_mask:0xf\n" \
"v_mul_f32_e64 v118, v45, v118\n" \
"v_mov_b32_dpp v116, v48 quad_perm:[0,1,2,3] row_mask:0x8 bank_mask:0xf\n" \
"v_mov_b32_dpp v117, v49 quad_perm:[0,1,2,3] row_mask:0x8 bank_mask:0xf\n" \
"v_mul_f32_e32 v116, v118, v116\n" \
"v_mul_f32_e32 v117, v118, v117\n" \
"v_mov_b32_e64 v122, v118\n" \
"v_cvt_pk_bf16_f32 v116, v116, v117\n" \
"v_accvgpr_read_b32 v52, a128\n" \
"v_permlane16_swap_b32_e32 v118, v122\n" \
"v_mov_b32_e32 v38, v118\n" \
"v_mov_b32_e64 v50, v122\n" \
"v_accvgpr_read_b32 v53, a129\n" \
"v_permlane32_swap_b32_e32 v118, v38\n" \
"v_permlane32_swap_b32_e32 v122, v50\n" \
"v_mul_f32_e32 v192, v118, v192\n" \
"v_mul_f32_e32 v193, v118, v193\n" \
"v_mul_f32_e32 v208, v122, v208\n" \
"v_mul_f32_e32 v209, v122, v209\n" \
"v_mul_f32_e32 v224, v38, v224\n" \
"v_mul_f32_e32 v225, v38, v225\n" \
"v_mul_f32_e32 v240, v50, v240\n" \
"v_mul_f32_e32 v241, v50, v241\n" \
"v_mul_f32_e32 v194, v118, v194\n" \
"v_mul_f32_e32 v195, v118, v195\n" \
"v_cvt_pk_bf16_f32 v192, v192, v193\n" \
"v_mul_f32_e32 v210, v122, v210\n" \
"v_mul_f32_e32 v211, v122, v211\n" \
"v_cvt_pk_bf16_f32 v208, v208, v209\n" \
"v_mul_f32_e32 v226, v38, v226\n" \
"v_mul_f32_e32 v227, v38, v227\n" \
"v_cvt_pk_bf16_f32 v224, v224, v225\n" \
"v_mul_f32_e32 v242, v50, v242\n" \
"v_mul_f32_e32 v243, v50, v243\n" \
"v_cvt_pk_bf16_f32 v240, v240, v241\n" \
"v_cvt_pk_bf16_f32 v193, v194, v195\n" \
"v_cvt_pk_bf16_f32 v209, v210, v211\n" \
"v_cvt_pk_bf16_f32 v225, v226, v227\n" \
"v_cvt_pk_bf16_f32 v241, v242, v243\n" \
"v_accvgpr_read_b32 v54, a130\n" \
"v_accvgpr_read_b32 v55, a131\n" \
"v_pk_mul_f32 v[52:53], v[52:53], s[70:71]\n" \
"v_accvgpr_read_b32 v56, a132\n" \
"v_accvgpr_read_b32 v57, a133\n" \
"v_accvgpr_read_b32 v58, a134\n" \
"v_accvgpr_read_b32 v59, a135\n" \
"v_pk_mul_f32 v[54:55], v[54:55], s[70:71]\n" \
"buffer_store_dwordx4 a[40:43], v20, s[80:83], s88 offen offset:0 sc1\n" \
"s_add_u32 s88, s45, 0x0\n" \
"s_nop 0\n" \
"buffer_store_dword v116, v25, s[80:83], s51 offen sc1\n" \
"buffer_store_dwordx2 v[192:193], v24, s[80:83], s84 offen sc1\n" \
"buffer_store_dwordx2 v[208:209], v24, s[80:83], s85 offen sc1\n" \
"buffer_store_dwordx2 v[224:225], v24, s[80:83], s86 offen sc1\n" \
"buffer_store_dwordx2 v[240:241], v24, s[80:83], s87 offen sc1\n" \
"v_cvt_pk_bf16_f32 v52, v52, v53\n" \
"v_pk_mul_f32 v[56:57], v[56:57], s[70:71]\n" \
"v_pk_mul_f32 v[58:59], v[58:59], s[70:71]\n" \
"v_cvt_pk_bf16_f32 v53, v54, v55\n" \
"v_cvt_pk_bf16_f32 v54, v56, v57\n" \
"v_cvt_pk_bf16_f32 v55, v58, v59\n" \
"ds_write_b128 v27, v[52:55] offset:0\n" \
"v_accvgpr_read_b32 v76, a224\n" \
"v_accvgpr_read_b32 v77, a225\n" \
"v_accvgpr_read_b32 v78, a226\n" \
"v_accvgpr_read_b32 v79, a227\n" \
"v_pk_mul_f32 v[76:77], v[76:77], s[70:71]\n" \
"v_accvgpr_read_b32 v52, a136\n" \
"v_accvgpr_read_b32 v53, a137\n" \
"v_accvgpr_read_b32 v80, a228\n" \
"v_accvgpr_read_b32 v81, a229\n" \
"v_accvgpr_read_b32 v82, a230\n" \
"v_accvgpr_read_b32 v83, a231\n" \
"v_pk_mul_f32 v[78:79], v[78:79], s[70:71]\n" \
"v_cvt_pk_bf16_f32 v76, v76, v77\n" \
"v_accvgpr_read_b32 v54, a138\n" \
"v_accvgpr_read_b32 v55, a139\n" \
"v_pk_mul_f32 v[52:53], v[52:53], s[70:71]\n" \
"v_pk_mul_f32 v[80:81], v[80:81], s[70:71]\n" \
"v_pk_mul_f32 v[82:83], v[82:83], s[70:71]\n" \
"v_cvt_pk_bf16_f32 v77, v78, v79\n" \
"v_accvgpr_read_b32 v56, a140\n" \
"v_accvgpr_read_b32 v57, a141\n" \
"v_accvgpr_read_b32 v58, a142\n" \
"v_accvgpr_read_b32 v59, a143\n" \
"v_pk_mul_f32 v[54:55], v[54:55], s[70:71]\n" \
"v_cvt_pk_bf16_f32 v52, v52, v53\n" \
"v_cvt_pk_bf16_f32 v78, v80, v81\n" \
"v_cvt_pk_bf16_f32 v79, v82, v83\n" \
"ds_write_b128 v27, v[76:79] offset:12288\n" \
"v_pk_mul_f32 v[56:57], v[56:57], s[70:71]\n" \
"v_pk_mul_f32 v[58:59], v[58:59], s[70:71]\n" \
"v_cvt_pk_bf16_f32 v53, v54, v55\n" \
"v_cvt_pk_bf16_f32 v54, v56, v57\n" \
"v_cvt_pk_bf16_f32 v55, v58, v59\n" \
"ds_write_b128 v27, v[52:55] offset:1024\n" \
"v_accvgpr_read_b32 v76, a208\n" \
"v_accvgpr_read_b32 v77, a209\n" \
"v_accvgpr_read_b32 v78, a210\n" \
"v_accvgpr_read_b32 v79, a211\n" \
"v_accvgpr_read_b32 v52, a168\n" \
"v_accvgpr_read_b32 v53, a169\n" \
"v_mfma_f32_16x16x32_bf16 v[76:79], v[148:151], v[100:103], v[76:79]\n" \
"v_accvgpr_read_b32 v54, a170\n" \
"v_accvgpr_read_b32 v55, a171\n" \
"v_mfma_f32_16x16x32_bf16 v[76:79], v[152:155], v[104:107], v[76:79]\n" \
"buffer_store_dwordx4 a[44:47], v20, s[80:83], s89 offen offset:1024 sc1\n" \
"s_add_u32 s89, s45, 0x0\n" \
"s_nop 0\n" \
"v_accvgpr_read_b32 v56, a172\n" \
"v_pk_mul_f32 v[52:53], v[52:53], s[70:71]\n" \
"v_accvgpr_read_b32 v57, a173\n" \
"v_accvgpr_read_b32 v58, a174\n" \
"v_accvgpr_read_b32 v59, a175\n" \
"v_pk_mul_f32 v[54:55], v[54:55], s[70:71]\n" \
"v_cvt_pk_bf16_f32 v52, v52, v53\n" \
"v_pk_mul_f32 v[56:57], v[56:57], s[70:71]\n" \
"v_pk_mul_f32 v[58:59], v[58:59], s[70:71]\n" \
"v_cvt_pk_bf16_f32 v53, v54, v55\n" \
"v_cvt_pk_bf16_f32 v54, v56, v57\n" \
"v_cvt_pk_bf16_f32 v55, v58, v59\n" \
"ds_write_b128 v27, v[52:55] offset:5120\n" \
"v_accvgpr_read_b32 v80, a212\n" \
"v_accvgpr_read_b32 v81, a213\n" \
"v_accvgpr_read_b32 v82, a214\n" \
"v_accvgpr_read_b32 v83, a215\n" \
"v_accvgpr_read_b32 v52, a232\n" \
"v_accvgpr_read_b32 v53, a233\n" \
"v_mfma_f32_16x16x32_bf16 v[80:83], v[160:163], v[104:107], v[80:83]\n" \
"v_accvgpr_read_b32 v54, a234\n" \
"v_accvgpr_read_b32 v55, a235\n" \
"v_mfma_f32_16x16x32_bf16 v[80:83], v[156:159], v[100:103], v[80:83]\n" \
"v_accvgpr_read_b32 v56, a236\n" \
"v_accvgpr_read_b32 v57, a237\n" \
"v_accvgpr_read_b32 v58, a238\n" \
"v_accvgpr_read_b32 v59, a239\n" \
"v_pk_mul_f32 v[52:53], v[52:53], s[70:71]\n" \
"v_pk_mul_f32 v[54:55], v[54:55], s[70:71]\n" \
"v_cvt_pk_bf16_f32 v52, v52, v53\n" \
"v_pk_mul_f32 v[56:57], v[56:57], s[70:71]\n" \
"v_pk_mul_f32 v[58:59], v[58:59], s[70:71]\n" \
"v_cvt_pk_bf16_f32 v53, v54, v55\n" \
"v_cvt_pk_bf16_f32 v54, v56, v57\n" \
"v_cvt_pk_bf16_f32 v55, v58, v59\n" \
"ds_write_b128 v27, v[52:55] offset:13312\n" \
"v_accvgpr_read_b32 v52, a216\n" \
"v_accvgpr_read_b32 v53, a217\n" \
"v_accvgpr_read_b32 v54, a218\n" \
"v_accvgpr_read_b32 v55, a219\n" \
"v_accvgpr_read_b32 v60, a160\n" \
"v_accvgpr_read_b32 v61, a161\n" \
"v_mfma_f32_16x16x32_bf16 v[52:55], v[164:167], v[100:103], v[52:55]\n" \
"v_accvgpr_read_b32 v62, a162\n" \
"v_accvgpr_read_b32 v63, a163\n" \
"v_mfma_f32_16x16x32_bf16 v[52:55], v[168:171], v[104:107], v[52:55]\n" \
"v_accvgpr_read_b32 v56, a220\n" \
"v_accvgpr_read_b32 v57, a221\n" \
"v_accvgpr_read_b32 v58, a222\n" \
"v_accvgpr_read_b32 v59, a223\n" \
"buffer_store_dwordx4 a[72:75], v20, s[80:83], s88 offen offset:2048 sc1\n" \
"s_add_u32 s88, s45, 0x1000\n" \
"v_mfma_f32_16x16x32_bf16 v[56:59], v[176:179], v[104:107], v[56:59]\n" \
"v_accvgpr_read_b32 v64, a164\n" \
"v_accvgpr_read_b32 v65, a165\n" \
"v_mfma_f32_16x16x32_bf16 v[56:59], v[172:175], v[100:103], v[56:59]\n" \
"v_accvgpr_read_b32 v66, a166\n" \
"v_accvgpr_read_b32 v67, a167\n" \
"v_accvgpr_read_b32 v68, a192\n" \
"v_accvgpr_read_b32 v69, a193\n" \
"v_pk_mul_f32 v[60:61], v[60:61], s[70:71]\n" \
"v_pk_mul_f32 v[62:63], v[62:63], s[70:71]\n" \
"v_cvt_pk_bf16_f32 v60, v60, v61\n" \
"v_pk_mul_f32 v[64:65], v[64:65], s[70:71]\n" \
"v_pk_mul_f32 v[66:67], v[66:67], s[70:71]\n" \
"v_cvt_pk_bf16_f32 v61, v62, v63\n" \
"v_cvt_pk_bf16_f32 v62, v64, v65\n" \
"v_cvt_pk_bf16_f32 v63, v66, v67\n" \
"ds_write_b128 v27, v[60:63] offset:4096\n" \
"v_accvgpr_read_b32 v60, a200\n" \
"v_accvgpr_read_b32 v61, a201\n" \
"v_accvgpr_read_b32 v70, a194\n" \
"v_accvgpr_read_b32 v71, a195\n" \
"v_pk_mul_f32 v[68:69], v[68:69], s[70:71]\n" \
"v_accvgpr_read_b32 v62, a202\n" \
"v_accvgpr_read_b32 v63, a203\n" \
"v_pk_mul_f32 v[60:61], v[60:61], s[70:71]\n" \
"v_accvgpr_read_b32 v72, a196\n" \
"v_accvgpr_read_b32 v73, a197\n" \
"v_accvgpr_read_b32 v74, a198\n" \
"v_accvgpr_read_b32 v75, a199\n" \
"v_pk_mul_f32 v[70:71], v[70:71], s[70:71]\n" \
"v_cvt_pk_bf16_f32 v68, v68, v69\n" \
"v_accvgpr_read_b32 v64, a204\n" \
"v_accvgpr_read_b32 v65, a205\n" \
"v_accvgpr_read_b32 v66, a206\n" \
"v_accvgpr_read_b32 v67, a207\n" \
"v_pk_mul_f32 v[62:63], v[62:63], s[70:71]\n" \
"v_cvt_pk_bf16_f32 v60, v60, v61\n" \
"v_accvgpr_read_b32 v100, a240\n" \
"v_accvgpr_read_b32 v101, a241\n" \
"v_accvgpr_read_b32 v102, a242\n" \
"v_accvgpr_read_b32 v103, a243\n" \
"v_pk_mul_f32 v[72:73], v[72:73], s[70:71]\n" \
"v_pk_mul_f32 v[74:75], v[74:75], s[70:71]\n" \
"v_mfma_f32_16x16x32_bf16 v[100:103], v[148:151], v[108:111], v[100:103]\n" \
"v_cvt_pk_bf16_f32 v69, v70, v71\n" \
"v_cvt_pk_bf16_f32 v61, v62, v63\n" \
"v_mfma_f32_16x16x32_bf16 v[100:103], v[152:155], v[112:115], v[100:103]\n" \
"v_cvt_pk_bf16_f32 v70, v72, v73\n" \
"v_cvt_pk_bf16_f32 v71, v74, v75\n" \
"ds_write_b128 v27, v[68:71] offset:8192\n" \
"buffer_store_dwordx4 a[76:79], v20, s[80:83], s89 offen offset:3072 sc1\n" \
"s_add_u32 s89, s45, 0x1000\n" \
"v_pk_mul_f32 v[64:65], v[64:65], s[70:71]\n" \
"v_pk_mul_f32 v[66:67], v[66:67], s[70:71]\n" \
"v_cvt_pk_bf16_f32 v62, v64, v65\n" \
"v_cvt_pk_bf16_f32 v63, v66, v67\n" \
"ds_write_b128 v27, v[60:63] offset:9216\n" \
"v_accvgpr_read_b32 v84, a144\n" \
"v_accvgpr_read_b32 v85, a145\n" \
"v_accvgpr_read_b32 v68, a152\n" \
"v_accvgpr_read_b32 v69, a153\n" \
"v_accvgpr_read_b32 v92, a176\n" \
"v_accvgpr_read_b32 v93, a177\n" \
"v_accvgpr_read_b32 v60, a184\n" \
"v_accvgpr_read_b32 v61, a185\n" \
"v_accvgpr_read_b32 v86, a146\n" \
"v_accvgpr_read_b32 v87, a147\n" \
"v_pk_mul_f32 v[84:85], v[84:85], s[70:71]\n" \
"v_accvgpr_read_b32 v70, a154\n" \
"v_accvgpr_read_b32 v71, a155\n" \
"v_pk_mul_f32 v[68:69], v[68:69], s[70:71]\n" \
"v_accvgpr_read_b32 v94, a178\n" \
"v_accvgpr_read_b32 v95, a179\n" \
"v_pk_mul_f32 v[92:93], v[92:93], s[70:71]\n" \
"v_accvgpr_read_b32 v62, a186\n" \
"v_accvgpr_read_b32 v63, a187\n" \
"v_pk_mul_f32 v[60:61], v[60:61], s[70:71]\n" \
"v_pk_mul_f32 v[76:77], v[76:77], s[70:71]\n" \
"v_pk_mul_f32 v[52:53], v[52:53], s[70:71]\n" \
"v_pk_mul_f32 v[100:101], v[100:101], s[70:71]\n" \
"v_accvgpr_read_b32 v88, a148\n" \
"v_accvgpr_read_b32 v89, a149\n" \
"v_accvgpr_read_b32 v90, a150\n" \
"v_accvgpr_read_b32 v91, a151\n" \
"v_pk_mul_f32 v[86:87], v[86:87], s[70:71]\n" \
"v_cvt_pk_bf16_f32 v84, v84, v85\n" \
"v_accvgpr_read_b32 v72, a156\n" \
"v_accvgpr_read_b32 v73, a157\n" \
"v_accvgpr_read_b32 v74, a158\n" \
"v_accvgpr_read_b32 v75, a159\n" \
"v_pk_mul_f32 v[70:71], v[70:71], s[70:71]\n" \
"v_cvt_pk_bf16_f32 v68, v68, v69\n" \
"v_accvgpr_read_b32 v96, a180\n" \
"v_accvgpr_read_b32 v97, a181\n" \
"v_accvgpr_read_b32 v98, a182\n" \
"v_accvgpr_read_b32 v99, a183\n" \
"v_pk_mul_f32 v[94:95], v[94:95], s[70:71]\n" \
"v_cvt_pk_bf16_f32 v92, v92, v93\n" \
"v_accvgpr_read_b32 v64, a188\n" \
"v_accvgpr_read_b32 v65, a189\n" \
"v_accvgpr_read_b32 v66, a190\n" \
"v_accvgpr_read_b32 v67, a191\n" \
"v_pk_mul_f32 v[62:63], v[62:63], s[70:71]\n" \
"v_cvt_pk_bf16_f32 v60, v60, v61\n" \
"v_pk_mul_f32 v[78:79], v[78:79], s[70:71]\n" \
"v_cvt_pk_bf16_f32 v76, v76, v77\n" \
"v_pk_mul_f32 v[54:55], v[54:55], s[70:71]\n" \
"v_cvt_pk_bf16_f32 v52, v52, v53\n" \
"v_pk_mul_f32 v[102:103], v[102:103], s[70:71]\n" \
"buffer_store_dwordx4 a[104:107], v20, s[80:83], s88 offen offset:0 sc1\n" \
"v_cvt_pk_bf16_f32 v100, v100, v101\n" \
"v_pk_mul_f32 v[88:89], v[88:89], s[70:71]\n" \
"v_pk_mul_f32 v[90:91], v[90:91], s[70:71]\n" \
"v_cvt_pk_bf16_f32 v85, v86, v87\n" \
"v_pk_mul_f32 v[72:73], v[72:73], s[70:71]\n" \
"v_pk_mul_f32 v[74:75], v[74:75], s[70:71]\n" \
"v_cvt_pk_bf16_f32 v69, v70, v71\n" \
"v_pk_mul_f32 v[96:97], v[96:97], s[70:71]\n" \
"v_pk_mul_f32 v[98:99], v[98:99], s[70:71]\n" \
"v_cvt_pk_bf16_f32 v93, v94, v95\n" \
"v_pk_mul_f32 v[64:65], v[64:65], s[70:71]\n" \
"v_pk_mul_f32 v[66:67], v[66:67], s[70:71]\n" \
"v_cvt_pk_bf16_f32 v61, v62, v63\n" \
"v_pk_mul_f32 v[80:81], v[80:81], s[70:71]\n" \
"v_pk_mul_f32 v[82:83], v[82:83], s[70:71]\n" \
"v_cvt_pk_bf16_f32 v77, v78, v79\n" \
"v_pk_mul_f32 v[56:57], v[56:57], s[70:71]\n" \
"v_pk_mul_f32 v[58:59], v[58:59], s[70:71]\n" \
"v_cvt_pk_bf16_f32 v53, v54, v55\n" \
"v_accvgpr_read_b32 v104, a244\n" \
"v_accvgpr_read_b32 v105, a245\n" \
"v_accvgpr_read_b32 v106, a246\n" \
"v_accvgpr_read_b32 v107, a247\n" \
"v_cvt_pk_bf16_f32 v101, v102, v103\n" \
"v_cvt_pk_bf16_f32 v86, v88, v89\n" \
"v_mfma_f32_16x16x32_bf16 v[104:107], v[160:163], v[112:115], v[104:107]\n" \
"v_cvt_pk_bf16_f32 v87, v90, v91\n" \
"v_cvt_pk_bf16_f32 v70, v72, v73\n" \
"ds_write_b128 v21, v[84:87] offset:16384\n" \
"v_mfma_f32_16x16x32_bf16 v[104:107], v[156:159], v[108:111], v[104:107]\n" \
"v_cvt_pk_bf16_f32 v71, v74, v75\n" \
"ds_write_b128 v21, v[68:71] offset:17408\n" \
"v_cvt_pk_bf16_f32 v94, v96, v97\n" \
"v_cvt_pk_bf16_f32 v95, v98, v99\n" \
"ds_write_b128 v21, v[92:95] offset:20480\n" \
"v_cvt_pk_bf16_f32 v62, v64, v65\n" \
"v_cvt_pk_bf16_f32 v63, v66, v67\n" \
"ds_write_b128 v21, v[60:63] offset:21504\n" \
"v_pk_mul_f32 v[104:105], v[104:105], s[70:71]\n" \
"v_pk_mul_f32 v[106:107], v[106:107], s[70:71]\n" \
"v_cvt_pk_bf16_f32 v78, v80, v81\n" \
"v_cvt_pk_bf16_f32 v79, v82, v83\n" \
"ds_write_b128 v21, v[76:79] offset:24576\n" \
"v_cvt_pk_bf16_f32 v54, v56, v57\n" \
"buffer_store_dwordx4 a[108:111], v20, s[80:83], s89 offen offset:1024 sc1\n" \
"v_cvt_pk_bf16_f32 v55, v58, v59\n" \
"ds_write_b128 v21, v[52:55] offset:25600\n" \
"v_cvt_pk_bf16_f32 v102, v104, v105\n" \
"v_cvt_pk_bf16_f32 v103, v106, v107\n" \
"ds_write_b128 v21, v[100:103] offset:28672\n" \
"v_accvgpr_read_b32 v84, a248\n" \
"v_accvgpr_read_b32 v85, a249\n" \
"v_accvgpr_read_b32 v86, a250\n" \
"v_accvgpr_read_b32 v87, a251\n" \
"v_accvgpr_read_b32 v88, a252\n" \
"v_accvgpr_read_b32 v89, a253\n" \
"v_mfma_f32_16x16x32_bf16 v[84:87], v[164:167], v[108:111], v[84:87]\n" \
"v_accvgpr_read_b32 v90, a254\n" \
"v_accvgpr_read_b32 v91, a255\n" \
"v_mfma_f32_16x16x32_bf16 v[84:87], v[168:171], v[112:115], v[84:87]\n" \
"v_sub_f32_e32 v44, 1.0, v45\n" \
"v_mfma_f32_16x16x32_bf16 v[88:91], v[176:179], v[112:115], v[88:91]\n" \
"ds_write_b32 v26, v44\n" \
"v_mfma_f32_16x16x32_bf16 v[88:91], v[172:175], v[108:111], v[88:91]\n" \
"s_nop 3\n" \
"v_pk_mul_f32 v[84:85], v[84:85], s[70:71]\n" \
"v_pk_mul_f32 v[86:87], v[86:87], s[70:71]\n" \
"v_cvt_pk_bf16_f32 v84, v84, v85\n" \
"v_cvt_pk_bf16_f32 v85, v86, v87\n" \
"v_pk_mul_f32 v[88:89], v[88:89], s[70:71]\n" \
"v_pk_mul_f32 v[90:91], v[90:91], s[70:71]\n" \
"v_cvt_pk_bf16_f32 v86, v88, v89\n" \
"v_cvt_pk_bf16_f32 v87, v90, v91\n" \
"ds_write_b128 v21, v[84:87] offset:29696\n" \
"L_end_%=:\n"
#define PROJ_VCLOB "v21","v24","v25","v33","v34","v35","v36","v37","v38","v39","v40","v41","v42","v43","v44","v45","v46","v47","v48","v49","v50","v51","v52","v53","v54","v55","v56","v57","v58","v59","v60","v61","v62","v63","v64","v65","v66","v67","v68","v69","v70","v71","v72","v73","v74","v75","v76","v77","v78","v79","v80","v81","v82","v83","v84","v85","v86","v87","v88","v89","v90","v91","v92","v93","v94","v95","v96","v97","v98","v99","v100","v101","v102","v103","v104","v105","v106","v107","v108","v109","v110","v111","v112","v113","v114","v115","v116","v117","v118","v119","v120","v121","v122","v123","v124","v125","v126","v127","v128","v129","v130","v131","v132","v133","v134","v135","v136","v137","v138","v139","v140","v141","v142","v143","v144","v145","v146","v147","v148","v149","v150","v151","v152","v153","v154","v155","v156","v157","v158","v159","v160","v161","v162","v163","v164","v165","v166","v167","v168","v169","v170","v171","v172","v173","v174","v175","v176","v177","v178","v179","v180","v181","v182","v183","v184","v185","v186","v187","v188","v189","v190","v191","v192","v193","v194","v195","v196","v197","v198","v199","v200","v201","v202","v203","v204","v205","v206","v207","v208","v209","v210","v211","v212","v213","v214","v215","v216","v217","v218","v219","v220","v221","v222","v223","v224","v225","v226","v227","v228","v229","v230","v231","v232","v233","v234","v235","v236","v237","v238","v239","v240","v241","v242","v243","v244","v245","v246","v247","v248","v249","v250","v251","v252","v253","v254","v255"
#define PROJ_SCLOB "s37","s38","s39","s40","s41","s42","s43","s44","s45","s46","s47","s48","s49","s50","s51","s52","s53","s54","s55","s62","s63","s76","s77","s78","s79","s80","s81","s82","s83","s84","s85","s86","s87","s88","s89","s94","s95","s96","s97","s98","s99","s100","s101"

#define PLO(p) ((unsigned)(size_t)(p))
#define PHI(p) ((unsigned)((size_t)(p) >> 32))
__device__ __forceinline__ unsigned proj_phase(const u16* __restrict__ x, const u16* __restrict__ ckv, const u16* __restrict__ wq, const u16* __restrict__ wuk,
                                                       const u16* __restrict__ wuv, const u16* __restrict__ wqkv, const u16* __restrict__ gw, const u16* wo, const u16* wob, const u16* out,
                                                       char* __restrict__ ws, char* lds, const unsigned wid, unsigned& tag, u32x4 (&vp)[5], u32x4 (&sa)[5], unsigned (&sd)[2]) {
    typedef unsigned u32x2 __attribute__((ext_vector_type(2)));
    unsigned* const flags = (unsigned*)(ws + WS_F);
    const int b = blockIdx.x, h = (b & 7) * 4 + ((b >> 3) & 3), mt = b >> 5;
    const unsigned L0 = (unsigned)(size_t)lds, rb = (wid < 2u ? 15u - mt : mt) * 128u;
    RTS(0);
#define PST() do {} while (0)
    const unsigned lane = threadIdx.x & 63u, wr = wid >> 1, wc = wid & 1u;
    PST();
    unsigned vAc[2], vB[2];
    const unsigned sp = b & 7, rot = (PROJ_ROT) & 7u;
#pragma unroll
    for (int par = 0; par < 2; ++par) {
        const unsigned lr = lane >> 3, cc = (lane & 7u) ^ (4u * par + (lr >> 1)), r0 = rb + wc * 64u + lr;
        vAc[par] = r0 * (DC * 2) + cc * 16u;
        const unsigned br = lane >> 4, p16 = lane & 15u, sc = (((p16 >> 1) ^ (br | (par << 2))) << 1) | (p16 & 1u);
        vB[par] = (16u * wid + br) * (NBIG * 2) + h * (HD * 2) + sc * 16u;
    }
#define SRD(p, n) (u32x4){PLO(p), PHI(p) & 0xffffu, n, 0x00020000u}
    u32x4 sC = SRD(ckv, S * DC * 2), sK = SRD(wuk, DC * NBIG * 2), sV = SRD(wuv, DC * NBIG * 2);
#define PRE(n) do { SBAR(); asm volatile(PROJ_PRE##n :: "{v22}"(vAc[0]), "{v23}"(vAc[1]), "{v26}"(vB[0]), "{v27}"(vB[1]), "{s[64:67]}"(sC), "{s[68:71]}"(sK), "{s[72:75]}"(sV), \
                 [mA] "s"(L0 + wid * 8192u), [mB] "s"(L0 + 65536u + wid * 4096u), [rc] "s"(rot * 128u) : "memory", "m0", "scc", "s62", "s63", "s84", "s85"); SBAR(); } while (0)
    PRE(0);
    const unsigned fr = lane & 15u, fg = lane >> 4, r4 = (lane >> 2) & 3u, sB = r4 | ((fg & 1u) << 2);
    const unsigned aA0 = L0 + (wr * 64u + fr) * 128u + ((fg ^ (fr >> 1)) << 4);
    unsigned vl = lane * 16u, r32 = L0 + 65536u + (8u * fg + r4) * 256u + (lane & 3u) * 8u + ((sB ^ (4u * wc)) << 5);
    u32x2 r28 = {aA0, aA0 ^ 64u};
    asm volatile("" : "+v"(vl), "+v"(r28), "+v"(r32));
    PST();
    PRE(1);
    PST();
    PRE(2);
    PST();
    asm volatile("s_mov_b64 exec, 0\n s_cmp_lt_u32 %0, 2\n s_cbranch_scc1 L_chA\n s_cmp_eq_u32 %0, 2\n s_cbranch_scc1 L_pf2\n s_branch L_pf3\n"
                 "L_chA: s_cmp_eq_u32 %0, 0\n s_cbranch_scc1 L_pf0\n s_branch L_pf1\n L_retA: s_mov_b64 exec, -1" :: "s"(wid) : "scc", "memory");
    PRE(3);
    PST();
    sa[0] = (u32x4){PLO(x), PHI(x), PLO(wq), PHI(wq)}; sa[1] = (u32x4){PLO(wqkv), PHI(wqkv), PLO(gw), PHI(gw)}; sa[2] = (u32x4){PLO(wo), PHI(wo), PLO(wob), PHI(wob)}; sa[3] = (u32x4){PLO(out), PHI(out), rot * 128u, 0u};
    asm volatile(PROJ_ASM
                 : [tag] "=&s"(tag), "+{s[8:11]}"(sa[0]), "+{s[12:15]}"(sa[1]), "+{s[16:19]}"(sa[2]), "+{s[20:23]}"(sa[3]), "=&{s[56:59]}"(sa[4]), "=&{s60}"(sd[0]), "=&{s61}"(sd[1]),
                   "+{s[64:67]}"(sC), "+{s[68:71]}"(sK), "+{s[72:75]}"(sV), "+{v22}"(vAc[0]), "+{v23}"(vAc[1]), "+{v26}"(vB[0]), "+{v27}"(vB[1]), "+{v[28:29]}"(r28), "+{v32}"(r32),
                   "={v[0:3]}"(vp[0]), "={v[4:7]}"(vp[1]), "={v[8:11]}"(vp[2]), "={v[12:15]}"(vp[3]), "={v[16:19]}"(vp[4])
                 : "{v20}"(vl), [wsl] "s"(PLO(ws)), [wsh] "s"(PHI(ws)), [b] "s"(b), [w] "s"(wid), [mA] "s"(L0 + wid * 8192u), [mB] "s"(L0 + 65536u + wid * 4096u), [mS] "s"(L0 + 151552u + wid * 512u)
                 : "memory", "m0", "vcc", "scc", PROJ_VCLOB, PROJ_SCLOB, ACLOB_LO, ACLOB_HI);
    RTS(1);
    return vl;
}

namespace att {
#define DI __device__ __forceinline__
constexpr int LDS_BYTES = 163840;
constexpr unsigned LDS_G = 159744u, LDS_L = 161792u;
typedef __bf16 bfx8 __attribute__((ext_vector_type(8)));
DI unsigned rfl(unsigned x) { return __builtin_amdgcn_readfirstlane(x); }
DI f32x4 mfma(u32x4 a, u32x4 b, f32x4 c) { return __builtin_amdgcn_mfma_f32_16x16x32_bf16(__builtin_bit_cast(bfx8, a), __builtin_bit_cast(bfx8, b), c, 0, 0, 0); }
typedef __bf16 bfx2 __attribute__((ext_vector_type(2)));
typedef float f32x2 __attribute__((ext_vector_type(2)));
DI unsigned cvtpk2(float lo, float hi) { f32x2 v = {lo, hi}; bfx2 b = __builtin_convertvector(v, bfx2); return __builtin_bit_cast(unsigned, b); }
DI void att_exact(const u16* __restrict__ x, const u16* __restrict__ wq, char* __restrict__ ws, unsigned h, unsigned r0, float g1) {
    const unsigned lane = lane_id(), g = lane >> 4, c = lane & 15u;
    f32x4 o[8];
#pragma unroll
    for (int n = 0; n < 8; ++n) o[n] = (f32x4){0.f, 0.f, 0.f, 0.f};
#pragma unroll 1
    for (unsigned kk = 0; kk < (unsigned)DM; kk += 32u) {
        const unsigned k = kk + 8u * g;
        const u32x4 xf = *reinterpret_cast<const u32x4*>(x + (r0 + c) * DM + k);
#pragma unroll
        for (int n = 0; n < 8; ++n) {
            const u16* w = wq + k * NBIG + h * HD + n * 16 + c; u32x4 wf;
#pragma unroll
            for (int e = 0; e < 4; ++e) wf[e] = (unsigned)w[2 * e * NBIG] | ((unsigned)w[(2 * e + 1) * NBIG] << 16);
            o[n] = mfma(wf, xf, o[n]);
        }
    }
    u32x4 q[4];
#pragma unroll
    for (int p = 0; p < 4; ++p) {
        const f32x4 a = o[2 * p] * 0.12751743082f, b = o[2 * p + 1] * 0.12751743082f;
        q[p] = (u32x4){cvtpk2(a[0], a[1]), cvtpk2(a[2], a[3]), cvtpk2(b[0], b[1]), cvtpk2(b[2], b[3])};
    }
#pragma unroll
    for (int n = 0; n < 8; ++n) o[n] = (f32x4){0.f, 0.f, 0.f, 0.f};
    const __amdgpu_buffer_rsrc_t srd = __builtin_amdgcn_make_buffer_rsrc((void*)(ws + WS_K + (size_t)h * 1048576), (short)0, 1048576, 0x00020000);
    float m = -3.0e38f, l = 0.f;
#pragma unroll 1
    for (unsigned k0 = 0; k0 <= r0 + 15u; k0 += 32u) {
        const unsigned tb = (k0 >> 6) * 32768u + (k0 & 32u) * 256u;
        f32x4 s[2]; float mx = m;
#pragma unroll
        for (int kt = 0; kt < 2; ++kt) {
            s[kt] = (f32x4){0.f, 0.f, 0.f, 0.f};
#pragma unroll
            for (int p = 0; p < 4; ++p) s[kt] = mfma(__builtin_amdgcn_raw_buffer_load_b128(srd, lane * 16u + (kt * 4 + p) * 1024u, tb, 16), q[p], s[kt]);
#pragma unroll
            for (int e = 0; e < 4; ++e) { if (k0 + 16u * kt + 4u * g + e > r0 + c) s[kt][e] = -3.0e38f; mx = fmaxf(mx, s[kt][e]); }
        }
        mx = fmaxf(mx, __shfl_xor(mx, 16)); mx = fmaxf(mx, __shfl_xor(mx, 32));
        const float al = __builtin_amdgcn_exp2f(m - mx); m = mx;
        u32x4 pf;
#pragma unroll
        for (int e = 0; e < 4; ++e) pf[e] = cvtpk2(__builtin_amdgcn_exp2f(s[e >> 1][2 * (e & 1)] - m), __builtin_amdgcn_exp2f(s[e >> 1][2 * (e & 1) + 1] - m));
        l *= al;
#pragma unroll
        for (int e = 0; e < 4; ++e) l += __uint_as_float(pf[e] << 16) + __uint_as_float(pf[e] & 0xffff0000u);
#pragma unroll
        for (int n = 0; n < 8; ++n) o[n] = mfma(__builtin_amdgcn_raw_buffer_load_b128(srd, lane * 16u + 16384u + ((n >> 2) * 8 + (n & 3)) * 1024u, tb - (k0 & 32u) * 128u, 16), pf, o[n] * al);
    }
    l += __shfl_xor(l, 16); l += __shfl_xor(l, 32);
    const float f = g1 / l;
    const __amdgpu_buffer_rsrc_t srdA = __builtin_amdgcn_make_buffer_rsrc((void*)(ws + WS_AF), (short)0, 128 * KS_TOT * 1024, 0x00020000);
#pragma unroll
    for (int p = 0; p < 4; ++p) {
        const f32x4 a = o[2 * p] * f, b = o[2 * p + 1] * f;
        __builtin_amdgcn_raw_buffer_store_b128((u32x4){cvtpk2(a[0], a[1]), cvtpk2(a[2], a[3]), cvtpk2(b[0], b[1]), cvtpk2(b[2], b[3])}, srdA, lane * 16u + p * 1024u, ((r0 >> 4) * KS_TOT + 18u + 4u * h) * 1024u, 16);
    }
}
}

#define ATT_ASM \
"L_att0:\n" \
"s_mov_b32 s101, 0\n" \
"s_waitcnt vmcnt(22) lgkmcnt(0)\n" \
"s_barrier\n" \
"v_accvgpr_write_b32 a216, 0\n" \
"v_accvgpr_write_b32 a217, 0\n" \
"v_accvgpr_write_b32 a218, 0\n" \
"v_accvgpr_write_b32 a219, 0\n" \
"v_and_b32_e32 v63, 0x300, v20\n" \
"v_add_u32_e32 v58, s20, v20\n" \
"v_mfma_f32_32x32x16_bf16 v[128:143], a[216:219], a[216:219], 0\n" \
"ds_read_b128 v[112:115], v63 offset:0\n" \
"ds_read_b128 a[0:3], v58 offset:0\n" \
"ds_read_b128 a[16:19], v58 offset:8192\n" \
"ds_read_b128 v[116:119], v63 offset:1024\n" \
"ds_read_b128 a[4:7], v58 offset:1024\n" \
"v_add_u32_e32 v59, s21, v20\n" \
"v_mfma_f32_32x32x16_bf16 v[144:159], a[216:219], a[216:219], 0\n" \
"ds_read_b128 a[32:35], v59 offset:0\n" \
"ds_read_b128 a[48:51], v59 offset:8192\n" \
"s_sub_u32 s67, s12, 0xc000\n" \
"s_mov_b32 s65, s12\n" \
"v_add_u32_e32 v55, s67, v20\n" \
"v_add_u32_e32 v52, 0xc000, v55\n" \
"v_mfma_f32_32x32x16_bf16 v[160:175], a[216:219], a[216:219], 0\n" \
"ds_read_b128 a[20:23], v58 offset:9216\n" \
"s_xor_b32 m0, s65, 0x10000\n" \
"ds_read_b128 a[36:39], v59 offset:1024\n" \
"buffer_load_dwordx4 v52, s[8:11], 0 offen offset:0 sc1 lds\n" \
"ds_read_b128 a[52:55], v59 offset:9216\n" \
"v_mfma_f32_32x32x16_bf16 v[176:191], a[216:219], a[216:219], 0\n" \
"buffer_load_dwordx4 v52, s[8:11], 0 offen offset:1024 sc1 lds\n" \
"ds_read_b128 v[120:123], v63 offset:2048\n" \
"v_add_u32_e32 v60, s15, v20\n" \
"v_add_u32_e32 v60, 0x10000, v60\n" \
"v_mov_b32_e32 v56, s62\n" \
"v_mfma_f32_32x32x16_bf16 v[192:207], a[216:219], a[216:219], 0\n" \
"ds_read_b128 a[8:11], v58 offset:2048\n" \
"ds_read_b128 a[24:27], v58 offset:10240\n" \
"v_readlane_b32 s68, v11, 0\n" \
"v_bfe_i32 v21, v20, 4, 1\n" \
"v_mov_b32_e32 v54, 0x3f803f80\n" \
"v_mov_b32_e32 v57, 0x3f003f00\n" \
"v_mfma_f32_32x32x16_bf16 v[208:223], a[216:219], a[216:219], 0\n" \
"ds_read_b128 a[40:43], v59 offset:2048\n" \
"ds_read_b128 a[56:59], v59 offset:10240\n" \
"s_waitcnt lgkmcnt(14)\n" \
"v_xor_b32_e32 v112, 0x80008000, v112\n" \
"v_xor_b32_e32 v113, 0x80008000, v113\n" \
"v_xor_b32_e32 v114, 0x80008000, v114\n" \
"v_mfma_f32_32x32x16_bf16 v[224:239], a[216:219], a[216:219], 0\n" \
"ds_read_b128 v[124:127], v63 offset:3072\n" \
"ds_read_b128 a[12:15], v58 offset:3072\n" \
"v_xor_b32_e32 v115, 0x80008000, v115\n" \
"v_and_b32_e32 v21, 0xaaaaaaaa, v21\n" \
"v_accvgpr_write_b32 a220, v112\n" \
"s_waitcnt lgkmcnt(13)\n" \
"v_xor_b32_e32 v116, 0x80008000, v116\n" \
"v_mfma_f32_32x32x16_bf16 v[240:255], a[216:219], a[216:219], 0\n" \
"ds_read_b128 a[28:31], v58 offset:11264\n" \
"ds_read_b128 a[44:47], v59 offset:3072\n" \
"v_xor_b32_e32 v117, 0x80008000, v117\n" \
"v_xor_b32_e32 v118, 0x80008000, v118\n" \
"v_xor_b32_e32 v119, 0x80008000, v119\n" \
"v_accvgpr_write_b32 a221, v113\n" \
"v_mfma_f32_32x32x16_bf16 a[192:207], a[216:219], a[216:219], 0\n" \
"ds_read_b128 a[60:63], v59 offset:11264\n" \
"ds_read_b128 a[160:163], v20 offset:0\n" \
"v_accvgpr_write_b32 a222, v114\n" \
"v_accvgpr_write_b32 a223, v115\n" \
"v_readlane_b32 s80, v11, 9\n" \
"v_readlane_b32 s81, v11, 10\n" \
"v_mfma_f32_16x16x32_bf16 v[32:35], v[112:115], a[0:3], 0\n" \
"ds_read_b128 a[164:167], v20 offset:1024\n" \
"ds_read_b128 a[168:171], v20 offset:2048\n" \
"v_mfma_f32_16x16x32_bf16 v[36:39], v[112:115], a[16:19], 0\n" \
"ds_read_b128 a[172:175], v20 offset:3072\n" \
"s_waitcnt lgkmcnt(15)\n" \
"v_mfma_f32_16x16x32_bf16 v[40:43], v[112:115], a[32:35], 0\n" \
"ds_read_b128 a[188:191], v20 offset:7168\n" \
"s_waitcnt lgkmcnt(14)\n" \
"v_xor_b32_e32 v120, 0x80008000, v120\n" \
"v_mfma_f32_16x16x32_bf16 v[44:47], v[112:115], a[48:51], 0\n" \
"ds_read_b128 a[184:187], v20 offset:6144\n" \
"v_xor_b32_e32 v121, 0x80008000, v121\n" \
"v_mfma_f32_16x16x32_bf16 v[32:35], v[116:119], a[4:7], v[32:35]\n" \
"v_xor_b32_e32 v122, 0x80008000, v122\n" \
"v_xor_b32_e32 v123, 0x80008000, v123\n" \
"v_mfma_f32_16x16x32_bf16 v[36:39], v[116:119], a[20:23], v[36:39]\n" \
"ds_read_b128 a[180:183], v20 offset:5120\n" \
"ds_read_b128 a[176:179], v20 offset:4096\n" \
"v_mfma_f32_16x16x32_bf16 v[40:43], v[116:119], a[36:39], v[40:43]\n" \
"ds_read_b128 a[128:131], v20 offset:8192\n" \
"s_mov_b32 s82, 0x47fca0\n" \
"v_mfma_f32_16x16x32_bf16 v[44:47], v[116:119], a[52:55], v[44:47]\n" \
"s_waitcnt lgkmcnt(13)\n" \
"v_xor_b32_e32 v124, 0x80008000, v124\n" \
"v_mfma_f32_16x16x32_bf16 v[32:35], v[120:123], a[8:11], v[32:35]\n" \
"v_xor_b32_e32 v125, 0x80008000, v125\n" \
"v_xor_b32_e32 v126, 0x80008000, v126\n" \
"v_mfma_f32_16x16x32_bf16 v[36:39], v[120:123], a[24:27], v[36:39]\n" \
"ds_read_b128 a[132:135], v20 offset:9216\n" \
"v_xor_b32_e32 v127, 0x80008000, v127\n" \
"v_mfma_f32_16x16x32_bf16 v[40:43], v[120:123], a[40:43], v[40:43]\n" \
"ds_read_b128 a[136:139], v20 offset:10240\n" \
"ds_read_b128 a[140:143], v20 offset:11264\n" \
"v_mfma_f32_16x16x32_bf16 v[44:47], v[120:123], a[56:59], v[44:47]\n" \
"ds_read_b128 a[144:147], v20 offset:12288\n" \
"s_waitcnt lgkmcnt(15)\n" \
"v_mfma_f32_16x16x32_bf16 v[32:35], v[124:127], a[12:15], v[32:35]\n" \
"ds_read_b128 a[148:151], v20 offset:13312\n" \
"ds_read_b128 a[152:155], v20 offset:14336\n" \
"v_mfma_f32_16x16x32_bf16 v[36:39], v[124:127], a[28:31], v[36:39]\n" \
"ds_read_b128 a[156:159], v20 offset:15360\n" \
"s_waitcnt lgkmcnt(15)\n" \
"v_mfma_f32_16x16x32_bf16 v[40:43], v[124:127], a[44:47], v[40:43]\n" \
"ds_read_b128 a[64:67], v60 offset:16384\n" \
"s_mov_b32 s83, 0x20000\n" \
"v_mfma_f32_16x16x32_bf16 v[44:47], v[124:127], a[60:63], v[44:47]\n" \
"ds_read_b128 a[68:71], v60 offset:17408\n" \
"s_mov_b32 s100, 0x71800000\n" \
"v_mfma_f32_16x16x32_bf16 v[64:67], a[160:163], a[0:3], v[32:35]\n" \
"ds_read_b128 a[72:75], v60 offset:18432\n" \
"s_waitcnt lgkmcnt(15)\n" \
"v_mfma_f32_16x16x32_bf16 v[64:67], a[164:167], a[4:7], v[64:67]\n" \
"ds_read_b128 a[76:79], v60 offset:19456\n" \
"s_mov_b32 s63, 0x30040000\n" \
"v_mfma_f32_16x16x32_bf16 v[64:67], a[168:171], a[8:11], v[64:67]\n" \
"ds_read_b128 a[80:83], v60 offset:24576\n" \
"v_mov_b32_e32 v62, 0\n" \
"v_mfma_f32_16x16x32_bf16 v[64:67], a[172:175], a[12:15], v[64:67]\n" \
"ds_read_b128 a[84:87], v60 offset:25600\n" \
"s_waitcnt lgkmcnt(15)\n" \
"v_mfma_f32_16x16x32_bf16 v[68:71], a[188:191], a[12:15], v[32:35]\n" \
"ds_read_b128 a[88:91], v60 offset:26624\n" \
"v_accvgpr_write_b32 a212, v54\n" \
"v_mfma_f32_16x16x32_bf16 v[68:71], a[184:187], a[8:11], v[68:71]\n" \
"ds_read_b128 a[92:95], v60 offset:27648\n" \
"v_accvgpr_write_b32 a208, 0\n" \
"v_mfma_f32_16x16x32_bf16 v[68:71], a[180:183], a[4:7], v[68:71]\n" \
"ds_read_b128 a[96:99], v60 offset:0\n" \
"s_waitcnt lgkmcnt(15)\n" \
"v_mfma_f32_16x16x32_bf16 v[68:71], a[176:179], a[0:3], v[68:71]\n" \
"ds_read_b128 a[100:103], v60 offset:1024\n" \
"v_accvgpr_write_b32 a213, v54\n" \
"v_mfma_f32_16x16x32_bf16 v[72:75], a[160:163], a[16:19], v[36:39]\n" \
"ds_read_b128 a[104:107], v60 offset:2048\n" \
"v_accvgpr_write_b32 a209, 0\n" \
"v_mfma_f32_16x16x32_bf16 v[72:75], a[164:167], a[20:23], v[72:75]\n" \
"ds_read_b128 a[108:111], v60 offset:3072\n" \
"v_accvgpr_write_b32 a214, v54\n" \
"v_mfma_f32_16x16x32_bf16 v[72:75], a[168:171], a[24:27], v[72:75]\n" \
"ds_read_b128 a[112:115], v60 offset:8192\n" \
"v_accvgpr_write_b32 a210, 0\n" \
"v_mfma_f32_16x16x32_bf16 v[72:75], a[172:175], a[28:31], v[72:75]\n" \
"ds_read_b128 a[116:119], v60 offset:9216\n" \
"v_accvgpr_write_b32 a215, v54\n" \
"v_mfma_f32_16x16x32_bf16 v[76:79], a[188:191], a[28:31], v[36:39]\n" \
"ds_read_b128 a[120:123], v60 offset:10240\n" \
"L_pf10:\n" \
"s_cbranch_execz L_pf14\n" \
"s_nop 0\n" \
"v_accvgpr_write_b32 a211, 0\n" \
"v_mfma_f32_16x16x32_bf16 v[76:79], a[184:187], a[24:27], v[76:79]\n" \
"ds_read_b128 a[124:127], v60 offset:11264\n" \
"v_or_b32_e32 v21, 0x44444444, v21\n" \
"v_mfma_f32_16x16x32_bf16 v[76:79], a[180:183], a[20:23], v[76:79]\n" \
"v_exp_f32_e32 v64, v64\n" \
"v_mfma_f32_16x16x32_bf16 v[76:79], a[176:179], a[16:19], v[76:79]\n" \
"v_exp_f32_e32 v65, v65\n" \
"v_mfma_f32_16x16x32_bf16 v[80:83], a[160:163], a[32:35], v[40:43]\n" \
"v_exp_f32_e32 v66, v66\n" \
"v_mfma_f32_16x16x32_bf16 v[80:83], a[164:167], a[36:39], v[80:83]\n" \
"v_exp_f32_e32 v67, v67\n" \
"v_mfma_f32_16x16x32_bf16 v[80:83], a[168:171], a[40:43], v[80:83]\n" \
"v_exp_f32_e32 v68, v68\n" \
"v_mfma_f32_16x16x32_bf16 v[80:83], a[172:175], a[44:47], v[80:83]\n" \
"v_exp_f32_e32 v69, v69\n" \
"v_mfma_f32_16x16x32_bf16 v[84:87], a[188:191], a[44:47], v[40:43]\n" \
"v_exp_f32_e32 v70, v70\n" \
"v_mfma_f32_16x16x32_bf16 v[84:87], a[184:187], a[40:43], v[84:87]\n" \
"v_exp_f32_e32 v71, v71\n" \
"v_mfma_f32_16x16x32_bf16 v[84:87], a[180:183], a[36:39], v[84:87]\n" \
"v_exp_f32_e32 v72, v72\n" \
"v_mfma_f32_16x16x32_bf16 v[84:87], a[176:179], a[32:35], v[84:87]\n" \
"v_exp_f32_e32 v73, v73\n" \
"v_mfma_f32_16x16x32_bf16 v[88:91], a[160:163], a[48:51], v[44:47]\n" \
"v_exp_f32_e32 v74, v74\n" \
"v_mfma_f32_16x16x32_bf16 v[88:91], a[164:167], a[52:55], v[88:91]\n" \
"v_exp_f32_e32 v75, v75\n" \
"v_mfma_f32_16x16x32_bf16 v[88:91], a[168:171], a[56:59], v[88:91]\n" \
"v_exp_f32_e32 v76, v76\n" \
"v_mfma_f32_16x16x32_bf16 v[88:91], a[172:175], a[60:63], v[88:91]\n" \
"v_exp_f32_e32 v77, v77\n" \
"v_mfma_f32_16x16x32_bf16 v[92:95], a[188:191], a[60:63], v[44:47]\n" \
"v_exp_f32_e32 v78, v78\n" \
"v_mfma_f32_16x16x32_bf16 v[92:95], a[184:187], a[56:59], v[92:95]\n" \
"v_exp_f32_e32 v79, v79\n" \
"v_mfma_f32_16x16x32_bf16 v[92:95], a[180:183], a[52:55], v[92:95]\n" \
"v_exp_f32_e32 v80, v80\n" \
"v_mfma_f32_16x16x32_bf16 v[92:95], a[176:179], a[48:51], v[92:95]\n" \
"v_exp_f32_e32 v81, v81\n" \
"s_waitcnt vmcnt(2) lgkmcnt(0)\n" \
"s_barrier\n" \
"s_mov_b64 exec, 1\n" \
"buffer_store_dword v56, off, s[56:59], s68 offset:0 sc1\n" \
"buffer_store_dword v56, off, s[56:59], s22 sc1\n" \
"s_mov_b64 exec, -1\n" \
"v_exp_f32_e32 v82, v82\n" \
"v_exp_f32_e32 v83, v83\n" \
"v_exp_f32_e32 v84, v84\n" \
"v_exp_f32_e32 v85, v85\n" \
"v_exp_f32_e32 v86, v86\n" \
"v_exp_f32_e32 v87, v87\n" \
"v_accvgpr_write_b32 a252, v57\n" \
"v_accvgpr_write_b32 a253, v57\n" \
"v_accvgpr_write_b32 a254, v57\n" \
"v_accvgpr_write_b32 a255, v57\n" \
"s_mov_b32 s64, 0x8000\n" \
"v_add_u32_e32 v50, 0x6000, v55\n" \
"v_add_u32_e32 v51, 0xa000, v55\n" \
"v_add_u32_e32 v53, 0x10000, v55\n" \
"v_add_u32_e32 v48, 0xc000, v20\n" \
"v_add_u32_e32 v49, s17, v20\n" \
"v_accvgpr_write_b32 a224, v116\n" \
"v_accvgpr_write_b32 a225, v117\n" \
"v_accvgpr_write_b32 a226, v118\n" \
"v_accvgpr_write_b32 a227, v119\n" \
"v_accvgpr_write_b32 a228, v120\n" \
"v_accvgpr_write_b32 a229, v121\n" \
"v_accvgpr_write_b32 a230, v122\n" \
"v_accvgpr_write_b32 a231, v123\n" \
"v_accvgpr_write_b32 a232, v124\n" \
"v_accvgpr_write_b32 a233, v125\n" \
"v_accvgpr_write_b32 a234, v126\n" \
"v_accvgpr_write_b32 a235, v127\n" \
"v_exp_f32_e32 v88, v88\n" \
"v_exp_f32_e32 v89, v89\n" \
"v_exp_f32_e32 v90, v90\n" \
"v_exp_f32_e32 v91, v91\n" \
"v_cvt_pk_bf16_f32 v96, v64, v65\n" \
"v_cvt_pk_bf16_f32 v97, v66, v67\n" \
"v_cvt_pk_bf16_f32 v98, v68, v69\n" \
"v_cvt_pk_bf16_f32 v99, v70, v71\n" \
"v_cvt_pk_bf16_f32 v104, v72, v73\n" \
"v_cvt_pk_bf16_f32 v105, v74, v75\n" \
"v_cvt_pk_bf16_f32 v106, v76, v77\n" \
"v_cvt_pk_bf16_f32 v107, v78, v79\n" \
"v_cvt_pk_bf16_f32 v112, v80, v81\n" \
"v_cvt_pk_bf16_f32 v113, v82, v83\n" \
"v_cvt_pk_bf16_f32 v114, v84, v85\n" \
"v_cvt_pk_bf16_f32 v115, v86, v87\n" \
"s_sub_u32 s66, s18, 1\n" \
"s_cbranch_scc1 L_tr_%=\n" \
"s_branch L_1_%=\n" \
"L_t1_%=:\n" \
"v_mfma_f32_16x16x32_bf16 v[252:255], a[188:191], v[124:127], v[252:255]\n" \
"v_mfma_f32_16x16x32_bf16 v[252:255], a[172:175], v[120:123], v[252:255]\n" \
"v_smfmac_f32_16x16x64_bf16 a[204:207], a[212:215], v[120:127], v21\n" \
"s_waitcnt lgkmcnt(0)\n" \
"L_1_%=:\n" \
"v_mfma_f32_16x16x32_bf16 v[64:67], a[128:131], a[0:3], v[32:35]\n" \
"ds_read_b128 a[160:163], v49 offset:0\n" \
"ds_read_b128 a[176:179], v49 offset:4096\n" \
"v_mfma_f32_16x16x32_bf16 v[64:67], a[132:135], a[4:7], v[64:67]\n" \
"v_cvt_pk_bf16_f32 v120, v88, v89\n" \
"v_cvt_pk_bf16_f32 v121, v90, v91\n" \
"v_mfma_f32_16x16x32_bf16 v[64:67], a[136:139], a[8:11], v[64:67]\n" \
"ds_read_b128 a[180:183], v49 offset:5120\n" \
"ds_read_b128 a[164:167], v49 offset:1024\n" \
"v_mfma_f32_16x16x32_bf16 v[64:67], a[140:143], a[12:15], v[64:67]\n" \
"v_exp_f32_e32 v92, v92\n" \
"v_mfma_f32_16x16x32_bf16 v[68:71], a[156:159], a[12:15], v[32:35]\n" \
"ds_read_b128 a[168:171], v49 offset:2048\n" \
"ds_read_b128 a[184:187], v49 offset:6144\n" \
"v_mfma_f32_16x16x32_bf16 v[68:71], a[152:155], a[8:11], v[68:71]\n" \
"v_exp_f32_e32 v93, v93\n" \
"v_mfma_f32_16x16x32_bf16 v[68:71], a[148:151], a[4:7], v[68:71]\n" \
"ds_read_b128 a[188:191], v49 offset:7168\n" \
"ds_read_b128 a[172:175], v49 offset:3072\n" \
"v_mfma_f32_16x16x32_bf16 v[68:71], a[144:147], a[0:3], v[68:71]\n" \
"v_exp_f32_e32 v94, v94\n" \
"v_mfma_f32_16x16x32_bf16 v[72:75], a[128:131], a[16:19], v[36:39]\n" \
"v_exp_f32_e32 v95, v95\n" \
"v_mfma_f32_16x16x32_bf16 v[72:75], a[132:135], a[20:23], v[72:75]\n" \
"v_cvt_pk_bf16_f32 v122, v92, v93\n" \
"v_cvt_pk_bf16_f32 v123, v94, v95\n" \
"v_mfma_f32_16x16x32_bf16 v[72:75], a[136:139], a[24:27], v[72:75]\n" \
"v_exp_f32_e32 v64, v64\n" \
"v_mfma_f32_16x16x32_bf16 v[72:75], a[140:143], a[28:31], v[72:75]\n" \
"v_exp_f32_e32 v65, v65\n" \
"v_mfma_f32_16x16x32_bf16 v[76:79], a[156:159], a[28:31], v[36:39]\n" \
"v_exp_f32_e32 v66, v66\n" \
"v_mfma_f32_16x16x32_bf16 v[76:79], a[152:155], a[24:27], v[76:79]\n" \
"v_exp_f32_e32 v67, v67\n" \
"v_mfma_f32_16x16x32_bf16 v[76:79], a[148:151], a[20:23], v[76:79]\n" \
"v_exp_f32_e32 v68, v68\n" \
"v_mfma_f32_16x16x32_bf16 v[76:79], a[144:147], a[16:19], v[76:79]\n" \
"v_exp_f32_e32 v69, v69\n" \
"v_mfma_f32_16x16x32_bf16 v[80:83], a[128:131], a[32:35], v[40:43]\n" \
"v_exp_f32_e32 v70, v70\n" \
"v_mfma_f32_16x16x32_bf16 v[80:83], a[132:135], a[36:39], v[80:83]\n" \
"v_exp_f32_e32 v71, v71\n" \
"v_mfma_f32_16x16x32_bf16 v[80:83], a[136:139], a[40:43], v[80:83]\n" \
"v_cvt_pk_bf16_f32 v100, v64, v65\n" \
"v_cvt_pk_bf16_f32 v101, v66, v67\n" \
"v_mfma_f32_16x16x32_bf16 v[80:83], a[140:143], a[44:47], v[80:83]\n" \
"v_cvt_pk_bf16_f32 v102, v68, v69\n" \
"v_cvt_pk_bf16_f32 v103, v70, v71\n" \
"v_mfma_f32_16x16x32_bf16 v[84:87], a[156:159], a[44:47], v[40:43]\n" \
"v_exp_f32_e32 v72, v72\n" \
"v_mfma_f32_16x16x32_bf16 v[84:87], a[152:155], a[40:43], v[84:87]\n" \
"v_exp_f32_e32 v73, v73\n" \
"v_mfma_f32_16x16x32_bf16 v[84:87], a[148:151], a[36:39], v[84:87]\n" \
"v_exp_f32_e32 v74, v74\n" \
"v_mfma_f32_16x16x32_bf16 v[84:87], a[144:147], a[32:35], v[84:87]\n" \
"v_exp_f32_e32 v75, v75\n" \
"v_mfma_f32_16x16x32_bf16 v[88:91], a[128:131], a[48:51], v[44:47]\n" \
"v_exp_f32_e32 v76, v76\n" \
"v_mfma_f32_16x16x32_bf16 v[88:91], a[132:135], a[52:55], v[88:91]\n" \
"v_exp_f32_e32 v77, v77\n" \
"v_mfma_f32_16x16x32_bf16 v[88:91], a[136:139], a[56:59], v[88:91]\n" \
"v_exp_f32_e32 v78, v78\n" \
"v_mfma_f32_16x16x32_bf16 v[88:91], a[140:143], a[60:63], v[88:91]\n" \
"v_exp_f32_e32 v79, v79\n" \
"v_mfma_f32_16x16x32_bf16 v[92:95], a[156:159], a[60:63], v[44:47]\n" \
"v_cvt_pk_bf16_f32 v108, v72, v73\n" \
"v_cvt_pk_bf16_f32 v109, v74, v75\n" \
"v_mfma_f32_16x16x32_bf16 v[92:95], a[152:155], a[56:59], v[92:95]\n" \
"v_cvt_pk_bf16_f32 v110, v76, v77\n" \
"v_cvt_pk_bf16_f32 v111, v78, v79\n" \
"v_mfma_f32_16x16x32_bf16 v[92:95], a[148:151], a[52:55], v[92:95]\n" \
"ds_read_b128 a[128:131], v48 offset:0\n" \
"ds_read_b128 a[132:135], v48 offset:1024\n" \
"v_mfma_f32_16x16x32_bf16 v[92:95], a[144:147], a[48:51], v[92:95]\n" \
"s_waitcnt lgkmcnt(2)\n" \
"v_mfma_f32_16x16x32_bf16 v[128:131], a[160:163], v[96:99], v[128:131]\n" \
"ds_read_b128 a[136:139], v48 offset:2048\n" \
"ds_read_b128 a[140:143], v48 offset:3072\n" \
"v_mfma_f32_16x16x32_bf16 v[128:131], a[176:179], v[100:103], v[128:131]\n" \
"v_exp_f32_e32 v80, v80\n" \
"v_mfma_f32_16x16x32_bf16 v[132:135], a[180:183], v[100:103], v[132:135]\n" \
"ds_read_b128 a[144:147], v48 offset:4096\n" \
"ds_read_b128 a[148:151], v48 offset:5120\n" \
"v_mfma_f32_16x16x32_bf16 v[132:135], a[164:167], v[96:99], v[132:135]\n" \
"v_exp_f32_e32 v81, v81\n" \
"v_mfma_f32_16x16x32_bf16 v[136:139], a[168:171], v[96:99], v[136:139]\n" \
"ds_read_b128 a[152:155], v48 offset:6144\n" \
"ds_read_b128 a[156:159], v48 offset:7168\n" \
"v_mfma_f32_16x16x32_bf16 v[136:139], a[184:187], v[100:103], v[136:139]\n" \
"v_exp_f32_e32 v82, v82\n" \
"v_mfma_f32_16x16x32_bf16 v[140:143], a[188:191], v[100:103], v[140:143]\n" \
"v_exp_f32_e32 v83, v83\n" \
"v_mfma_f32_16x16x32_bf16 v[140:143], a[172:175], v[96:99], v[140:143]\n" \
"v_exp_f32_e32 v84, v84\n" \
"v_smfmac_f32_16x16x64_bf16 a[192:195], a[212:215], v[96:103], v21\n" \
"v_mfma_f32_16x16x32_bf16 v[160:163], a[160:163], v[104:107], v[160:163]\n" \
"v_exp_f32_e32 v85, v85\n" \
"v_mfma_f32_16x16x32_bf16 v[160:163], a[176:179], v[108:111], v[160:163]\n" \
"v_exp_f32_e32 v86, v86\n" \
"v_mfma_f32_16x16x32_bf16 v[164:167], a[180:183], v[108:111], v[164:167]\n" \
"v_exp_f32_e32 v87, v87\n" \
"v_mfma_f32_16x16x32_bf16 v[164:167], a[164:167], v[104:107], v[164:167]\n" \
"v_cvt_pk_bf16_f32 v116, v80, v81\n" \
"v_cvt_pk_bf16_f32 v117, v82, v83\n" \
"v_mfma_f32_16x16x32_bf16 v[168:171], a[168:171], v[104:107], v[168:171]\n" \
"v_cvt_pk_bf16_f32 v118, v84, v85\n" \
"v_cvt_pk_bf16_f32 v119, v86, v87\n" \
"v_mfma_f32_16x16x32_bf16 v[168:171], a[184:187], v[108:111], v[168:171]\n" \
"v_exp_f32_e32 v88, v88\n" \
"v_mfma_f32_16x16x32_bf16 v[172:175], a[188:191], v[108:111], v[172:175]\n" \
"v_exp_f32_e32 v89, v89\n" \
"v_mfma_f32_16x16x32_bf16 v[172:175], a[172:175], v[104:107], v[172:175]\n" \
"v_exp_f32_e32 v90, v90\n" \
"v_smfmac_f32_16x16x64_bf16 a[196:199], a[212:215], v[104:111], v21\n" \
"v_mfma_f32_16x16x32_bf16 v[192:195], a[160:163], v[112:115], v[192:195]\n" \
"v_exp_f32_e32 v91, v91\n" \
"v_mfma_f32_16x16x32_bf16 v[192:195], a[176:179], v[116:119], v[192:195]\n" \
"v_exp_f32_e32 v92, v92\n" \
"v_mfma_f32_16x16x32_bf16 v[196:199], a[180:183], v[116:119], v[196:199]\n" \
"v_exp_f32_e32 v93, v93\n" \
"v_mfma_f32_16x16x32_bf16 v[196:199], a[164:167], v[112:115], v[196:199]\n" \
"v_exp_f32_e32 v94, v94\n" \
"v_mfma_f32_16x16x32_bf16 v[200:203], a[168:171], v[112:115], v[200:203]\n" \
"v_exp_f32_e32 v95, v95\n" \
"v_mfma_f32_16x16x32_bf16 v[200:203], a[184:187], v[116:119], v[200:203]\n" \
"v_cvt_pk_bf16_f32 v124, v88, v89\n" \
"v_cvt_pk_bf16_f32 v125, v90, v91\n" \
"v_mfma_f32_16x16x32_bf16 v[204:207], a[188:191], v[116:119], v[204:207]\n" \
"v_cvt_pk_bf16_f32 v126, v92, v93\n" \
"v_cvt_pk_bf16_f32 v127, v94, v95\n" \
"v_mfma_f32_16x16x32_bf16 v[204:207], a[172:175], v[112:115], v[204:207]\n" \
"v_smfmac_f32_16x16x64_bf16 a[200:203], a[212:215], v[112:119], v21\n" \
"s_waitcnt vmcnt(0) lgkmcnt(0)\n" \
"s_barrier\n" \
"v_mfma_f32_16x16x32_bf16 v[224:227], a[160:163], v[120:123], v[224:227]\n" \
"s_add_u32 s63, s63, s63\n" \
"s_cbranch_scc1 L_h1_%=\n" \
"L_hr1_%=:\n" \
"v_mfma_f32_16x16x32_bf16 v[224:227], a[176:179], v[124:127], v[224:227]\n" \
"s_xor_b32 m0, s65, 0x12000\n" \
"ds_read_b128 a[160:163], v49 offset:8192\n" \
"v_mfma_f32_16x16x32_bf16 v[228:231], a[180:183], v[124:127], v[228:231]\n" \
"buffer_load_dwordx4 v50, s[8:11], s64 offen offset:0 sc1 lds\n" \
"v_mfma_f32_16x16x32_bf16 v[228:231], a[164:167], v[120:123], v[228:231]\n" \
"ds_read_b128 a[176:179], v49 offset:12288\n" \
"ds_read_b128 a[180:183], v49 offset:13312\n" \
"v_mfma_f32_16x16x32_bf16 v[232:235], a[168:171], v[120:123], v[232:235]\n" \
"v_mfma_f32_16x16x32_bf16 v[232:235], a[184:187], v[124:127], v[232:235]\n" \
"ds_read_b128 a[164:167], v49 offset:9216\n" \
"ds_read_b128 a[168:171], v49 offset:10240\n" \
"v_mfma_f32_16x16x32_bf16 v[236:239], a[188:191], v[124:127], v[236:239]\n" \
"v_mfma_f32_16x16x32_bf16 v[236:239], a[172:175], v[120:123], v[236:239]\n" \
"ds_read_b128 a[184:187], v49 offset:14336\n" \
"ds_read_b128 a[188:191], v49 offset:15360\n" \
"v_mfma_f32_16x16x32_bf16 v[64:67], a[128:131], a[0:3], v[32:35]\n" \
"v_mfma_f32_16x16x32_bf16 v[64:67], a[132:135], a[4:7], v[64:67]\n" \
"buffer_load_dwordx4 v50, s[8:11], s64 offen offset:1024 sc1 lds\n" \
"v_mfma_f32_16x16x32_bf16 v[64:67], a[136:139], a[8:11], v[64:67]\n" \
"s_xor_b32 m0, s65, 0x6000\n" \
"ds_read_b128 a[172:175], v49 offset:11264\n" \
"v_mfma_f32_16x16x32_bf16 v[64:67], a[140:143], a[12:15], v[64:67]\n" \
"v_add_u32_e32 v49, 0x10000, v48\n" \
"v_mfma_f32_16x16x32_bf16 v[68:71], a[156:159], a[12:15], v[32:35]\n" \
"v_mfma_f32_16x16x32_bf16 v[68:71], a[152:155], a[8:11], v[68:71]\n" \
"v_mfma_f32_16x16x32_bf16 v[68:71], a[148:151], a[4:7], v[68:71]\n" \
"v_mfma_f32_16x16x32_bf16 v[68:71], a[144:147], a[0:3], v[68:71]\n" \
"v_mfma_f32_16x16x32_bf16 v[72:75], a[128:131], a[16:19], v[36:39]\n" \
"v_mfma_f32_16x16x32_bf16 v[72:75], a[132:135], a[20:23], v[72:75]\n" \
"v_mfma_f32_16x16x32_bf16 v[72:75], a[136:139], a[24:27], v[72:75]\n" \
"buffer_load_dwordx4 v51, s[8:11], s64 offen offset:0 sc1 lds\n" \
"v_mfma_f32_16x16x32_bf16 v[72:75], a[140:143], a[28:31], v[72:75]\n" \
"v_exp_f32_e32 v64, v64\n" \
"v_mfma_f32_16x16x32_bf16 v[76:79], a[156:159], a[28:31], v[36:39]\n" \
"v_exp_f32_e32 v65, v65\n" \
"v_mfma_f32_16x16x32_bf16 v[76:79], a[152:155], a[24:27], v[76:79]\n" \
"v_exp_f32_e32 v66, v66\n" \
"v_mfma_f32_16x16x32_bf16 v[76:79], a[148:151], a[20:23], v[76:79]\n" \
"v_exp_f32_e32 v67, v67\n" \
"v_mfma_f32_16x16x32_bf16 v[76:79], a[144:147], a[16:19], v[76:79]\n" \
"v_exp_f32_e32 v68, v68\n" \
"v_mfma_f32_16x16x32_bf16 v[80:83], a[128:131], a[32:35], v[40:43]\n" \
"v_exp_f32_e32 v69, v69\n" \
"v_mfma_f32_16x16x32_bf16 v[80:83], a[132:135], a[36:39], v[80:83]\n" \
"v_exp_f32_e32 v70, v70\n" \
"v_mfma_f32_16x16x32_bf16 v[80:83], a[136:139], a[40:43], v[80:83]\n" \
"v_exp_f32_e32 v71, v71\n" \
"v_mfma_f32_16x16x32_bf16 v[80:83], a[140:143], a[44:47], v[80:83]\n" \
"buffer_load_dwordx4 v51, s[8:11], s64 offen offset:1024 sc1 lds\n" \
"v_mfma_f32_16x16x32_bf16 v[84:87], a[156:159], a[44:47], v[40:43]\n" \
"v_exp_f32_e32 v72, v72\n" \
"v_mfma_f32_16x16x32_bf16 v[84:87], a[152:155], a[40:43], v[84:87]\n" \
"v_exp_f32_e32 v73, v73\n" \
"v_mfma_f32_16x16x32_bf16 v[84:87], a[148:151], a[36:39], v[84:87]\n" \
"v_exp_f32_e32 v74, v74\n" \
"v_mfma_f32_16x16x32_bf16 v[84:87], a[144:147], a[32:35], v[84:87]\n" \
"v_exp_f32_e32 v75, v75\n" \
"v_mfma_f32_16x16x32_bf16 v[88:91], a[128:131], a[48:51], v[44:47]\n" \
"s_xor_b32 m0, s65, 0x14000\n" \
"ds_read_b128 a[128:131], v48 offset:8192\n" \
"v_mfma_f32_16x16x32_bf16 v[88:91], a[132:135], a[52:55], v[88:91]\n" \
"v_exp_f32_e32 v76, v76\n" \
"v_mfma_f32_16x16x32_bf16 v[88:91], a[136:139], a[56:59], v[88:91]\n" \
"ds_read_b128 a[132:135], v48 offset:9216\n" \
"ds_read_b128 a[136:139], v48 offset:10240\n" \
"v_mfma_f32_16x16x32_bf16 v[88:91], a[140:143], a[60:63], v[88:91]\n" \
"v_exp_f32_e32 v77, v77\n" \
"v_mfma_f32_16x16x32_bf16 v[92:95], a[156:159], a[60:63], v[44:47]\n" \
"buffer_load_dwordx4 v52, s[8:11], s64 offen offset:0 sc1 lds\n" \
"v_mfma_f32_16x16x32_bf16 v[92:95], a[152:155], a[56:59], v[92:95]\n" \
"ds_read_b128 a[140:143], v48 offset:11264\n" \
"ds_read_b128 a[152:155], v48 offset:14336\n" \
"v_mfma_f32_16x16x32_bf16 v[92:95], a[148:151], a[52:55], v[92:95]\n" \
"v_exp_f32_e32 v78, v78\n" \
"v_mfma_f32_16x16x32_bf16 v[92:95], a[144:147], a[48:51], v[92:95]\n" \
"ds_read_b128 a[144:147], v48 offset:12288\n" \
"s_waitcnt lgkmcnt(6)\n" \
"v_mfma_f32_16x16x32_bf16 v[144:147], a[160:163], v[96:99], v[144:147]\n" \
"ds_read_b128 a[148:151], v48 offset:13312\n" \
"ds_read_b128 a[156:159], v48 offset:15360\n" \
"v_mfma_f32_16x16x32_bf16 v[144:147], a[176:179], v[100:103], v[144:147]\n" \
"v_exp_f32_e32 v79, v79\n" \
"v_mfma_f32_16x16x32_bf16 v[148:151], a[180:183], v[100:103], v[148:151]\n" \
"v_exp_f32_e32 v80, v80\n" \
"v_mfma_f32_16x16x32_bf16 v[148:151], a[164:167], v[96:99], v[148:151]\n" \
"v_exp_f32_e32 v81, v81\n" \
"v_mfma_f32_16x16x32_bf16 v[152:155], a[168:171], v[96:99], v[152:155]\n" \
"buffer_load_dwordx4 v52, s[8:11], s64 offen offset:1024 sc1 lds\n" \
"v_mfma_f32_16x16x32_bf16 v[152:155], a[184:187], v[100:103], v[152:155]\n" \
"s_mov_b32 m0, s65\n" \
"s_xor_b32 s65, s65, 0x4000\n" \
"v_mfma_f32_16x16x32_bf16 v[156:159], a[188:191], v[100:103], v[156:159]\n" \
"v_exp_f32_e32 v82, v82\n" \
"v_mfma_f32_16x16x32_bf16 v[156:159], a[172:175], v[96:99], v[156:159]\n" \
"v_exp_f32_e32 v83, v83\n" \
"v_mfma_f32_16x16x32_bf16 v[176:179], a[160:163], v[104:107], v[176:179]\n" \
"v_exp_f32_e32 v84, v84\n" \
"v_mfma_f32_16x16x32_bf16 v[176:179], a[176:179], v[108:111], v[176:179]\n" \
"v_exp_f32_e32 v85, v85\n" \
"v_mfma_f32_16x16x32_bf16 v[180:183], a[180:183], v[108:111], v[180:183]\n" \
"v_exp_f32_e32 v86, v86\n" \
"v_mfma_f32_16x16x32_bf16 v[180:183], a[164:167], v[104:107], v[180:183]\n" \
"v_exp_f32_e32 v87, v87\n" \
"v_mfma_f32_16x16x32_bf16 v[184:187], a[168:171], v[104:107], v[184:187]\n" \
"v_exp_f32_e32 v88, v88\n" \
"v_mfma_f32_16x16x32_bf16 v[184:187], a[184:187], v[108:111], v[184:187]\n" \
"buffer_load_dwordx4 v53, s[8:11], s64 offen offset:0 sc1 lds\n" \
"v_mfma_f32_16x16x32_bf16 v[188:191], a[188:191], v[108:111], v[188:191]\n" \
"v_exp_f32_e32 v89, v89\n" \
"v_mfma_f32_16x16x32_bf16 v[188:191], a[172:175], v[104:107], v[188:191]\n" \
"v_exp_f32_e32 v90, v90\n" \
"v_mfma_f32_16x16x32_bf16 v[208:211], a[160:163], v[112:115], v[208:211]\n" \
"v_exp_f32_e32 v91, v91\n" \
"v_mfma_f32_16x16x32_bf16 v[208:211], a[176:179], v[116:119], v[208:211]\n" \
"v_cvt_pk_bf16_f32 v96, v64, v65\n" \
"v_cvt_pk_bf16_f32 v97, v66, v67\n" \
"v_mfma_f32_16x16x32_bf16 v[212:215], a[180:183], v[116:119], v[212:215]\n" \
"v_cvt_pk_bf16_f32 v98, v68, v69\n" \
"v_cvt_pk_bf16_f32 v99, v70, v71\n" \
"v_mfma_f32_16x16x32_bf16 v[212:215], a[164:167], v[112:115], v[212:215]\n" \
"v_cvt_pk_bf16_f32 v104, v72, v73\n" \
"v_cvt_pk_bf16_f32 v105, v74, v75\n" \
"v_mfma_f32_16x16x32_bf16 v[216:219], a[168:171], v[112:115], v[216:219]\n" \
"v_cvt_pk_bf16_f32 v106, v76, v77\n" \
"v_cvt_pk_bf16_f32 v107, v78, v79\n" \
"v_mfma_f32_16x16x32_bf16 v[216:219], a[184:187], v[116:119], v[216:219]\n" \
"v_xor_b32_e32 v48, 0x4000, v48\n" \
"v_mfma_f32_16x16x32_bf16 v[220:223], a[188:191], v[116:119], v[220:223]\n" \
"buffer_load_dwordx4 v53, s[8:11], s64 offen offset:1024 sc1 lds\n" \
"v_mfma_f32_16x16x32_bf16 v[220:223], a[172:175], v[112:115], v[220:223]\n" \
"s_add_u32 s64, s64, 0x8000\n" \
"s_sub_u32 s66, s66, 1\n" \
"v_mfma_f32_16x16x32_bf16 v[240:243], a[160:163], v[120:123], v[240:243]\n" \
"v_cvt_pk_bf16_f32 v112, v80, v81\n" \
"v_cvt_pk_bf16_f32 v113, v82, v83\n" \
"v_mfma_f32_16x16x32_bf16 v[240:243], a[176:179], v[124:127], v[240:243]\n" \
"v_cvt_pk_bf16_f32 v114, v84, v85\n" \
"v_cvt_pk_bf16_f32 v115, v86, v87\n" \
"v_mfma_f32_16x16x32_bf16 v[244:247], a[180:183], v[124:127], v[244:247]\n" \
"v_mfma_f32_16x16x32_bf16 v[244:247], a[164:167], v[120:123], v[244:247]\n" \
"v_mfma_f32_16x16x32_bf16 v[248:251], a[168:171], v[120:123], v[248:251]\n" \
"v_mfma_f32_16x16x32_bf16 v[248:251], a[184:187], v[124:127], v[248:251]\n" \
"s_cbranch_scc0 L_t1_%=\n" \
"v_mfma_f32_16x16x32_bf16 v[252:255], a[188:191], v[124:127], v[252:255]\n" \
"v_mfma_f32_16x16x32_bf16 v[252:255], a[172:175], v[120:123], v[252:255]\n" \
"v_smfmac_f32_16x16x64_bf16 a[204:207], a[212:215], v[120:127], v21\n" \
"s_waitcnt lgkmcnt(0)\n" \
"s_barrier\n" \
"L_tr_%=:\n" \
"L_pf11:\n" \
"s_cbranch_execz L_pf15\n" \
"s_nop 0\n" \
"s_cmp_lg_u32 s16, 0\n" \
"s_cbranch_scc1 L_sw_%=\n" \
"v_mfma_f32_16x16x32_bf16 v[64:67], a[128:131], a[0:3], v[32:35]\n" \
"s_xor_b32 m0, s65, 0x12000\n" \
"s_mov_b32 s65, s13\n" \
"v_mfma_f32_16x16x32_bf16 v[64:67], a[132:135], a[4:7], v[64:67]\n" \
"buffer_load_dwordx4 v50, s[8:11], s64 offen offset:0 sc1 lds\n" \
"v_mfma_f32_16x16x32_bf16 v[64:67], a[136:139], a[8:11], v[64:67]\n" \
"v_add_u32_e32 v63, s15, v20\n" \
"s_add_u32 s67, s14, 40960\n" \
"v_mfma_f32_16x16x32_bf16 v[64:67], a[140:143], a[12:15], v[64:67]\n" \
"ds_read_b128 a[160:163], v49 offset:0\n" \
"ds_read_b128 a[176:179], v49 offset:4096\n" \
"v_mfma_f32_16x16x32_bf16 v[68:71], a[156:159], a[12:15], v[32:35]\n" \
"buffer_load_dwordx4 v50, s[8:11], s64 offen offset:1024 sc1 lds\n" \
"v_mfma_f32_16x16x32_bf16 v[68:71], a[152:155], a[8:11], v[68:71]\n" \
"v_add_u32_e32 v50, 0x6000, v63\n" \
"s_xor_b32 m0, s65, 0x8000\n" \
"v_mfma_f32_16x16x32_bf16 v[68:71], a[148:151], a[4:7], v[68:71]\n" \
"ds_read_b128 a[180:183], v49 offset:5120\n" \
"ds_read_b128 a[164:167], v49 offset:1024\n" \
"v_mfma_f32_16x16x32_bf16 v[68:71], a[144:147], a[0:3], v[68:71]\n" \
"ds_read_b128 a[168:171], v49 offset:2048\n" \
"ds_read_b128 a[184:187], v49 offset:6144\n" \
"v_mfma_f32_16x16x32_bf16 v[72:75], a[128:131], a[16:19], v[36:39]\n" \
"v_cvt_pk_bf16_f32 v120, v88, v89\n" \
"v_cvt_pk_bf16_f32 v121, v90, v91\n" \
"v_mfma_f32_16x16x32_bf16 v[72:75], a[132:135], a[20:23], v[72:75]\n" \
"buffer_load_dwordx4 v50, s[8:11], s67 offen offset:0 sc1 lds\n" \
"v_mfma_f32_16x16x32_bf16 v[72:75], a[136:139], a[24:27], v[72:75]\n" \
"ds_read_b128 a[188:191], v49 offset:7168\n" \
"ds_read_b128 a[172:175], v49 offset:3072\n" \
"v_mfma_f32_16x16x32_bf16 v[72:75], a[140:143], a[28:31], v[72:75]\n" \
"v_exp_f32_e32 v92, v92\n" \
"v_mfma_f32_16x16x32_bf16 v[76:79], a[156:159], a[28:31], v[36:39]\n" \
"v_exp_f32_e32 v93, v93\n" \
"v_mfma_f32_16x16x32_bf16 v[76:79], a[152:155], a[24:27], v[76:79]\n" \
"buffer_load_dwordx4 v50, s[8:11], s67 offen offset:1024 sc1 lds\n" \
"v_mfma_f32_16x16x32_bf16 v[76:79], a[148:151], a[20:23], v[76:79]\n" \
"v_exp_f32_e32 v94, v94\n" \
"v_mfma_f32_16x16x32_bf16 v[76:79], a[144:147], a[16:19], v[76:79]\n" \
"v_exp_f32_e32 v95, v95\n" \
"v_mfma_f32_16x16x32_bf16 v[80:83], a[128:131], a[32:35], v[40:43]\n" \
"v_cvt_pk_bf16_f32 v122, v92, v93\n" \
"v_cvt_pk_bf16_f32 v123, v94, v95\n" \
"v_mfma_f32_16x16x32_bf16 v[80:83], a[132:135], a[36:39], v[80:83]\n" \
"v_exp_f32_e32 v64, v64\n" \
"v_mfma_f32_16x16x32_bf16 v[80:83], a[136:139], a[40:43], v[80:83]\n" \
"buffer_load_dwordx4 v50, s[8:11], s67 offen offset:2048 sc1 lds\n" \
"v_mfma_f32_16x16x32_bf16 v[80:83], a[140:143], a[44:47], v[80:83]\n" \
"v_exp_f32_e32 v65, v65\n" \
"v_mfma_f32_16x16x32_bf16 v[84:87], a[156:159], a[44:47], v[40:43]\n" \
"v_exp_f32_e32 v66, v66\n" \
"v_mfma_f32_16x16x32_bf16 v[84:87], a[152:155], a[40:43], v[84:87]\n" \
"v_exp_f32_e32 v67, v67\n" \
"v_mfma_f32_16x16x32_bf16 v[84:87], a[148:151], a[36:39], v[84:87]\n" \
"buffer_load_dwordx4 v50, s[8:11], s67 offen offset:3072 sc1 lds\n" \
"v_mfma_f32_16x16x32_bf16 v[84:87], a[144:147], a[32:35], v[84:87]\n" \
"v_exp_f32_e32 v68, v68\n" \
"v_mfma_f32_16x16x32_bf16 v[88:91], a[128:131], a[48:51], v[44:47]\n" \
"v_exp_f32_e32 v69, v69\n" \
"v_mfma_f32_16x16x32_bf16 v[88:91], a[132:135], a[52:55], v[88:91]\n" \
"v_exp_f32_e32 v70, v70\n" \
"v_mfma_f32_16x16x32_bf16 v[88:91], a[136:139], a[56:59], v[88:91]\n" \
"v_exp_f32_e32 v71, v71\n" \
"v_mfma_f32_16x16x32_bf16 v[88:91], a[140:143], a[60:63], v[88:91]\n" \
"v_cvt_pk_bf16_f32 v100, v64, v65\n" \
"v_cvt_pk_bf16_f32 v101, v66, v67\n" \
"v_mfma_f32_16x16x32_bf16 v[92:95], a[156:159], a[60:63], v[44:47]\n" \
"v_cvt_pk_bf16_f32 v102, v68, v69\n" \
"v_cvt_pk_bf16_f32 v103, v70, v71\n" \
"v_mfma_f32_16x16x32_bf16 v[92:95], a[152:155], a[56:59], v[92:95]\n" \
"s_mov_b32 s64, s14\n" \
"v_add_u32_e32 v51, 0x12000, v63\n" \
"v_mfma_f32_16x16x32_bf16 v[92:95], a[148:151], a[52:55], v[92:95]\n" \
"s_xor_b32 m0, s65, 0xa000\n" \
"v_readlane_b32 s93, v11, 17\n" \
"v_mfma_f32_16x16x32_bf16 v[92:95], a[144:147], a[48:51], v[92:95]\n" \
"v_readlane_b32 s92, v11, 22\n" \
"s_waitcnt lgkmcnt(0)\n" \
"v_mfma_f32_16x16x32_bf16 v[128:131], a[160:163], v[96:99], v[128:131]\n" \
"buffer_load_dwordx4 v51, s[8:11], s64 offen offset:0 sc1 lds\n" \
"v_mfma_f32_16x16x32_bf16 v[128:131], a[176:179], v[100:103], v[128:131]\n" \
"buffer_load_dwordx4 v51, s[8:11], s64 offen offset:1024 sc1 lds\n" \
"v_mfma_f32_16x16x32_bf16 v[132:135], a[180:183], v[100:103], v[132:135]\n" \
"v_exp_f32_e32 v72, v72\n" \
"v_mfma_f32_16x16x32_bf16 v[132:135], a[164:167], v[96:99], v[132:135]\n" \
"v_exp_f32_e32 v73, v73\n" \
"v_mfma_f32_16x16x32_bf16 v[136:139], a[168:171], v[96:99], v[136:139]\n" \
"v_exp_f32_e32 v74, v74\n" \
"v_mfma_f32_16x16x32_bf16 v[136:139], a[184:187], v[100:103], v[136:139]\n" \
"v_exp_f32_e32 v75, v75\n" \
"v_mfma_f32_16x16x32_bf16 v[140:143], a[188:191], v[100:103], v[140:143]\n" \
"v_exp_f32_e32 v76, v76\n" \
"v_mfma_f32_16x16x32_bf16 v[140:143], a[172:175], v[96:99], v[140:143]\n" \
"v_exp_f32_e32 v77, v77\n" \
"v_smfmac_f32_16x16x64_bf16 a[192:195], a[212:215], v[96:103], v21\n" \
"v_mfma_f32_16x16x32_bf16 v[160:163], a[160:163], v[104:107], v[160:163]\n" \
"v_exp_f32_e32 v78, v78\n" \
"v_exp_f32_e32 v79, v79\n" \
"v_cvt_pk_bf16_f32 v108, v72, v73\n" \
"v_cvt_pk_bf16_f32 v109, v74, v75\n" \
"v_cvt_pk_bf16_f32 v110, v76, v77\n" \
"v_cvt_pk_bf16_f32 v111, v78, v79\n" \
"v_exp_f32_e32 v80, v80\n" \
"v_exp_f32_e32 v81, v81\n" \
"v_mfma_f32_16x16x32_bf16 v[160:163], a[176:179], v[108:111], v[160:163]\n" \
"buffer_load_dwordx4 v51, s[8:11], s64 offen offset:2048 sc1 lds\n" \
"v_mfma_f32_16x16x32_bf16 v[164:167], a[180:183], v[108:111], v[164:167]\n" \
"buffer_load_dwordx4 v51, s[8:11], s64 offen offset:3072 sc1 lds\n" \
"v_mfma_f32_16x16x32_bf16 v[164:167], a[164:167], v[104:107], v[164:167]\n" \
"v_exp_f32_e32 v82, v82\n" \
"v_mfma_f32_16x16x32_bf16 v[168:171], a[168:171], v[104:107], v[168:171]\n" \
"v_exp_f32_e32 v83, v83\n" \
"v_mfma_f32_16x16x32_bf16 v[168:171], a[184:187], v[108:111], v[168:171]\n" \
"v_exp_f32_e32 v84, v84\n" \
"v_mfma_f32_16x16x32_bf16 v[172:175], a[188:191], v[108:111], v[172:175]\n" \
"v_exp_f32_e32 v85, v85\n" \
"v_mfma_f32_16x16x32_bf16 v[172:175], a[172:175], v[104:107], v[172:175]\n" \
"v_exp_f32_e32 v86, v86\n" \
"v_smfmac_f32_16x16x64_bf16 a[196:199], a[212:215], v[104:111], v21\n" \
"v_mfma_f32_16x16x32_bf16 v[192:195], a[160:163], v[112:115], v[192:195]\n" \
"v_exp_f32_e32 v87, v87\n" \
"v_cvt_pk_bf16_f32 v116, v80, v81\n" \
"v_cvt_pk_bf16_f32 v117, v82, v83\n" \
"v_cvt_pk_bf16_f32 v118, v84, v85\n" \
"v_cvt_pk_bf16_f32 v119, v86, v87\n" \
"s_add_u32 m0, s93, 0x0\n" \
"v_exp_f32_e32 v88, v88\n" \
"v_mfma_f32_16x16x32_bf16 v[192:195], a[176:179], v[116:119], v[192:195]\n" \
"buffer_load_dwordx4 v0, s[80:83], s92 offen offset:0 lds\n" \
"v_mfma_f32_16x16x32_bf16 v[196:199], a[180:183], v[116:119], v[196:199]\n" \
"ds_read_b128 a[128:131], v48 offset:0\n" \
"ds_read_b128 a[132:135], v48 offset:1024\n" \
"v_mfma_f32_16x16x32_bf16 v[196:199], a[164:167], v[112:115], v[196:199]\n" \
"ds_read_b128 a[136:139], v48 offset:2048\n" \
"ds_read_b128 a[140:143], v48 offset:3072\n" \
"ds_read_b128 a[144:147], v48 offset:4096\n" \
"ds_read_b128 a[148:151], v48 offset:5120\n" \
"ds_read_b128 a[152:155], v48 offset:6144\n" \
"ds_read_b128 a[156:159], v48 offset:7168\n" \
"v_mfma_f32_16x16x32_bf16 v[200:203], a[168:171], v[112:115], v[200:203]\n" \
"buffer_load_dwordx4 v1, s[80:83], s92 offen offset:1024 lds\n" \
"v_mfma_f32_16x16x32_bf16 v[200:203], a[184:187], v[116:119], v[200:203]\n" \
"v_exp_f32_e32 v89, v89\n" \
"v_mfma_f32_16x16x32_bf16 v[204:207], a[188:191], v[116:119], v[204:207]\n" \
"v_exp_f32_e32 v90, v90\n" \
"v_mfma_f32_16x16x32_bf16 v[204:207], a[172:175], v[112:115], v[204:207]\n" \
"v_exp_f32_e32 v91, v91\n" \
"v_smfmac_f32_16x16x64_bf16 a[200:203], a[212:215], v[112:119], v21\n" \
"v_mfma_f32_16x16x32_bf16 v[224:227], a[160:163], v[120:123], v[224:227]\n" \
"v_exp_f32_e32 v92, v92\n" \
"v_exp_f32_e32 v93, v93\n" \
"v_exp_f32_e32 v94, v94\n" \
"v_exp_f32_e32 v95, v95\n" \
"v_cvt_pk_bf16_f32 v124, v88, v89\n" \
"v_cvt_pk_bf16_f32 v125, v90, v91\n" \
"v_cvt_pk_bf16_f32 v126, v92, v93\n" \
"v_cvt_pk_bf16_f32 v127, v94, v95\n" \
"v_add_u32_e32 v52, 0x14000, v63\n" \
"v_add_u32_e32 v53, 0x20000, v63\n" \
"v_mfma_f32_16x16x32_bf16 v[224:227], a[176:179], v[124:127], v[224:227]\n" \
"buffer_load_dwordx4 v2, s[80:83], s92 offen offset:2048 lds\n" \
"v_mfma_f32_16x16x32_bf16 v[228:231], a[180:183], v[124:127], v[228:231]\n" \
"buffer_load_dwordx4 v3, s[80:83], s92 offen offset:3072 lds\n" \
"v_mfma_f32_16x16x32_bf16 v[228:231], a[164:167], v[120:123], v[228:231]\n" \
"s_mov_b32 s66, s19\n" \
"v_mfma_f32_16x16x32_bf16 v[232:235], a[168:171], v[120:123], v[232:235]\n" \
"v_mfma_f32_16x16x32_bf16 v[232:235], a[184:187], v[124:127], v[232:235]\n" \
"v_mfma_f32_16x16x32_bf16 v[236:239], a[188:191], v[124:127], v[236:239]\n" \
"v_mfma_f32_16x16x32_bf16 v[236:239], a[172:175], v[120:123], v[236:239]\n" \
"s_waitcnt vmcnt(14) lgkmcnt(0)\n" \
"s_barrier\n" \
"v_mfma_f32_16x16x32_bf16 v[64:67], a[128:131], a[0:3], v[32:35]\n" \
"s_xor_b32 m0, s65, 0x18000\n" \
"ds_read_b128 a[160:163], v49 offset:8192\n" \
"v_mfma_f32_16x16x32_bf16 v[64:67], a[132:135], a[4:7], v[64:67]\n" \
"buffer_load_dwordx4 v52, s[8:11], s64 offen offset:0 sc1 lds\n" \
"v_mfma_f32_16x16x32_bf16 v[64:67], a[136:139], a[8:11], v[64:67]\n" \
"ds_read_b128 a[176:179], v49 offset:12288\n" \
"ds_read_b128 a[180:183], v49 offset:13312\n" \
"v_mfma_f32_16x16x32_bf16 v[64:67], a[140:143], a[12:15], v[64:67]\n" \
"ds_read_b128 a[164:167], v49 offset:9216\n" \
"ds_read_b128 a[168:171], v49 offset:10240\n" \
"v_mfma_f32_16x16x32_bf16 v[68:71], a[156:159], a[12:15], v[32:35]\n" \
"buffer_load_dwordx4 v52, s[8:11], s64 offen offset:1024 sc1 lds\n" \
"v_mfma_f32_16x16x32_bf16 v[68:71], a[152:155], a[8:11], v[68:71]\n" \
"ds_read_b128 a[184:187], v49 offset:14336\n" \
"ds_read_b128 a[188:191], v49 offset:15360\n" \
"v_mfma_f32_16x16x32_bf16 v[68:71], a[148:151], a[4:7], v[68:71]\n" \
"v_exp_f32_e32 v64, v64\n" \
"v_mfma_f32_16x16x32_bf16 v[68:71], a[144:147], a[0:3], v[68:71]\n" \
"v_exp_f32_e32 v65, v65\n" \
"v_mfma_f32_16x16x32_bf16 v[72:75], a[128:131], a[16:19], v[36:39]\n" \
"v_exp_f32_e32 v66, v66\n" \
"v_mfma_f32_16x16x32_bf16 v[72:75], a[132:135], a[20:23], v[72:75]\n" \
"buffer_load_dwordx4 v52, s[8:11], s64 offen offset:2048 sc1 lds\n" \
"v_mfma_f32_16x16x32_bf16 v[72:75], a[136:139], a[24:27], v[72:75]\n" \
"v_exp_f32_e32 v67, v67\n" \
"v_mfma_f32_16x16x32_bf16 v[72:75], a[140:143], a[28:31], v[72:75]\n" \
"v_exp_f32_e32 v68, v68\n" \
"v_mfma_f32_16x16x32_bf16 v[76:79], a[156:159], a[28:31], v[36:39]\n" \
"v_exp_f32_e32 v69, v69\n" \
"v_mfma_f32_16x16x32_bf16 v[76:79], a[152:155], a[24:27], v[76:79]\n" \
"buffer_load_dwordx4 v52, s[8:11], s64 offen offset:3072 sc1 lds\n" \
"v_mfma_f32_16x16x32_bf16 v[76:79], a[148:151], a[20:23], v[76:79]\n" \
"ds_read_b128 a[172:175], v49 offset:11264\n" \
"s_mov_b32 m0, s65\n" \
"v_mfma_f32_16x16x32_bf16 v[76:79], a[144:147], a[16:19], v[76:79]\n" \
"L_pf12:\n" \
"s_cbranch_execz L_pf16\n" \
"s_nop 0\n" \
"v_exp_f32_e32 v70, v70\n" \
"v_mfma_f32_16x16x32_bf16 v[80:83], a[128:131], a[32:35], v[40:43]\n" \
"v_exp_f32_e32 v71, v71\n" \
"v_mfma_f32_16x16x32_bf16 v[80:83], a[132:135], a[36:39], v[80:83]\n" \
"v_exp_f32_e32 v72, v72\n" \
"v_mfma_f32_16x16x32_bf16 v[80:83], a[136:139], a[40:43], v[80:83]\n" \
"buffer_load_dwordx4 v53, s[8:11], s64 offen offset:0 sc1 lds\n" \
"v_mfma_f32_16x16x32_bf16 v[80:83], a[140:143], a[44:47], v[80:83]\n" \
"v_exp_f32_e32 v73, v73\n" \
"v_mfma_f32_16x16x32_bf16 v[84:87], a[156:159], a[44:47], v[40:43]\n" \
"v_exp_f32_e32 v74, v74\n" \
"v_mfma_f32_16x16x32_bf16 v[84:87], a[152:155], a[40:43], v[84:87]\n" \
"v_exp_f32_e32 v75, v75\n" \
"v_mfma_f32_16x16x32_bf16 v[84:87], a[148:151], a[36:39], v[84:87]\n" \
"v_exp_f32_e32 v76, v76\n" \
"v_mfma_f32_16x16x32_bf16 v[84:87], a[144:147], a[32:35], v[84:87]\n" \
"buffer_load_dwordx4 v53, s[8:11], s64 offen offset:1024 sc1 lds\n" \
"v_mfma_f32_16x16x32_bf16 v[88:91], a[128:131], a[48:51], v[44:47]\n" \
"ds_read_b128 a[128:131], v48 offset:8192\n" \
"v_add_u32_e32 v49, 0x10000, v48\n" \
"v_mfma_f32_16x16x32_bf16 v[88:91], a[132:135], a[52:55], v[88:91]\n" \
"ds_read_b128 a[132:135], v48 offset:9216\n" \
"s_xor_b32 s65, s65, 0x8000\n" \
"v_mfma_f32_16x16x32_bf16 v[88:91], a[136:139], a[56:59], v[88:91]\n" \
"v_exp_f32_e32 v77, v77\n" \
"v_mfma_f32_16x16x32_bf16 v[88:91], a[140:143], a[60:63], v[88:91]\n" \
"buffer_load_dwordx4 v53, s[8:11], s64 offen offset:2048 sc1 lds\n" \
"v_mfma_f32_16x16x32_bf16 v[92:95], a[156:159], a[60:63], v[44:47]\n" \
"ds_read_b128 a[136:139], v48 offset:10240\n" \
"ds_read_b128 a[140:143], v48 offset:11264\n" \
"v_mfma_f32_16x16x32_bf16 v[92:95], a[152:155], a[56:59], v[92:95]\n" \
"ds_read_b128 a[152:155], v48 offset:14336\n" \
"ds_read_b128 a[156:159], v48 offset:15360\n" \
"v_mfma_f32_16x16x32_bf16 v[92:95], a[148:151], a[52:55], v[92:95]\n" \
"v_exp_f32_e32 v78, v78\n" \
"v_mfma_f32_16x16x32_bf16 v[92:95], a[144:147], a[48:51], v[92:95]\n" \
"ds_read_b128 a[144:147], v48 offset:12288\n" \
"s_waitcnt lgkmcnt(7)\n" \
"v_mfma_f32_16x16x32_bf16 v[144:147], a[160:163], v[96:99], v[144:147]\n" \
"buffer_load_dwordx4 v53, s[8:11], s64 offen offset:3072 sc1 lds\n" \
"v_mfma_f32_16x16x32_bf16 v[144:147], a[176:179], v[100:103], v[144:147]\n" \
"s_add_u32 m0, s93, 0x1000\n" \
"ds_read_b128 a[148:151], v48 offset:13312\n" \
"v_mfma_f32_16x16x32_bf16 v[148:151], a[180:183], v[100:103], v[148:151]\n" \
"v_exp_f32_e32 v79, v79\n" \
"v_mfma_f32_16x16x32_bf16 v[148:151], a[164:167], v[96:99], v[148:151]\n" \
"v_exp_f32_e32 v80, v80\n" \
"v_mfma_f32_16x16x32_bf16 v[152:155], a[168:171], v[96:99], v[152:155]\n" \
"buffer_load_dwordx4 v4, s[80:83], s92 offen offset:0 lds\n" \
"v_mfma_f32_16x16x32_bf16 v[152:155], a[184:187], v[100:103], v[152:155]\n" \
"v_exp_f32_e32 v81, v81\n" \
"v_mfma_f32_16x16x32_bf16 v[156:159], a[188:191], v[100:103], v[156:159]\n" \
"v_exp_f32_e32 v82, v82\n" \
"v_mfma_f32_16x16x32_bf16 v[156:159], a[172:175], v[96:99], v[156:159]\n" \
"v_exp_f32_e32 v83, v83\n" \
"v_mfma_f32_16x16x32_bf16 v[176:179], a[160:163], v[104:107], v[176:179]\n" \
"v_exp_f32_e32 v84, v84\n" \
"v_mfma_f32_16x16x32_bf16 v[176:179], a[176:179], v[108:111], v[176:179]\n" \
"buffer_load_dwordx4 v5, s[80:83], s92 offen offset:1024 lds\n" \
"v_mfma_f32_16x16x32_bf16 v[180:183], a[180:183], v[108:111], v[180:183]\n" \
"v_exp_f32_e32 v85, v85\n" \
"v_mfma_f32_16x16x32_bf16 v[180:183], a[164:167], v[104:107], v[180:183]\n" \
"v_exp_f32_e32 v86, v86\n" \
"v_mfma_f32_16x16x32_bf16 v[184:187], a[168:171], v[104:107], v[184:187]\n" \
"v_exp_f32_e32 v87, v87\n" \
"v_mfma_f32_16x16x32_bf16 v[184:187], a[184:187], v[108:111], v[184:187]\n" \
"v_exp_f32_e32 v88, v88\n" \
"v_mfma_f32_16x16x32_bf16 v[188:191], a[188:191], v[108:111], v[188:191]\n" \
"buffer_load_dwordx4 v6, s[80:83], s92 offen offset:2048 lds\n" \
"v_mfma_f32_16x16x32_bf16 v[188:191], a[172:175], v[104:107], v[188:191]\n" \
"v_exp_f32_e32 v89, v89\n" \
"v_mfma_f32_16x16x32_bf16 v[208:211], a[160:163], v[112:115], v[208:211]\n" \
"v_exp_f32_e32 v90, v90\n" \
"v_mfma_f32_16x16x32_bf16 v[208:211], a[176:179], v[116:119], v[208:211]\n" \
"v_exp_f32_e32 v91, v91\n" \
"v_mfma_f32_16x16x32_bf16 v[212:215], a[180:183], v[116:119], v[212:215]\n" \
"buffer_load_dwordx4 v7, s[80:83], s92 offen offset:3072 lds\n" \
"v_mfma_f32_16x16x32_bf16 v[212:215], a[164:167], v[112:115], v[212:215]\n" \
"s_add_u32 m0, s93, 0x2000\n" \
"v_cvt_pk_bf16_f32 v96, v64, v65\n" \
"v_mfma_f32_16x16x32_bf16 v[216:219], a[168:171], v[112:115], v[216:219]\n" \
"v_cvt_pk_bf16_f32 v97, v66, v67\n" \
"v_cvt_pk_bf16_f32 v98, v68, v69\n" \
"v_mfma_f32_16x16x32_bf16 v[216:219], a[184:187], v[116:119], v[216:219]\n" \
"v_cvt_pk_bf16_f32 v99, v70, v71\n" \
"v_cvt_pk_bf16_f32 v104, v72, v73\n" \
"v_mfma_f32_16x16x32_bf16 v[220:223], a[188:191], v[116:119], v[220:223]\n" \
"v_cvt_pk_bf16_f32 v105, v74, v75\n" \
"v_cvt_pk_bf16_f32 v106, v76, v77\n" \
"v_mfma_f32_16x16x32_bf16 v[220:223], a[172:175], v[112:115], v[220:223]\n" \
"buffer_load_dwordx4 v8, s[80:83], s92 offen offset:0 lds\n" \
"v_mfma_f32_16x16x32_bf16 v[240:243], a[160:163], v[120:123], v[240:243]\n" \
"v_cvt_pk_bf16_f32 v107, v78, v79\n" \
"v_cvt_pk_bf16_f32 v112, v80, v81\n" \
"v_mfma_f32_16x16x32_bf16 v[240:243], a[176:179], v[124:127], v[240:243]\n" \
"v_cvt_pk_bf16_f32 v113, v82, v83\n" \
"v_cvt_pk_bf16_f32 v114, v84, v85\n" \
"v_mfma_f32_16x16x32_bf16 v[244:247], a[180:183], v[124:127], v[244:247]\n" \
"v_cvt_pk_bf16_f32 v115, v86, v87\n" \
"v_add_u32_e32 v48, s16, v20\n" \
"v_mfma_f32_16x16x32_bf16 v[244:247], a[164:167], v[120:123], v[244:247]\n" \
"s_add_u32 s64, s64, 0x10000\n" \
"v_mfma_f32_16x16x32_bf16 v[248:251], a[168:171], v[120:123], v[248:251]\n" \
"v_mfma_f32_16x16x32_bf16 v[248:251], a[184:187], v[124:127], v[248:251]\n" \
"v_mfma_f32_16x16x32_bf16 v[252:255], a[188:191], v[124:127], v[252:255]\n" \
"v_mfma_f32_16x16x32_bf16 v[252:255], a[172:175], v[120:123], v[252:255]\n" \
"v_smfmac_f32_16x16x64_bf16 a[204:207], a[212:215], v[120:127], v21\n" \
"s_waitcnt vmcnt(13) lgkmcnt(0)\n" \
"s_mov_b64 exec, 0\n" \
"s_cmp_lg_u32 s15, 0\n" \
"s_cbranch_scc1 L_pf21\n" \
"s_branch L_pf20\n" \
"L_retB:\n" \
"s_mov_b64 exec, -1\n" \
"s_barrier\n" \
"s_branch L_2_%=\n" \
"L_t2_%=:\n" \
"v_mfma_f32_16x16x32_bf16 v[252:255], a[188:191], v[124:127], v[252:255]\n" \
"v_cvt_pk_bf16_f32 v114, v84, v85\n" \
"v_cvt_pk_bf16_f32 v115, v86, v87\n" \
"v_mfma_f32_16x16x32_bf16 v[252:255], a[172:175], v[120:123], v[252:255]\n" \
"v_xor_b32_e32 v48, 0x8000, v48\n" \
"v_smfmac_f32_16x16x64_bf16 a[204:207], a[212:215], v[120:127], v21\n" \
"s_waitcnt lgkmcnt(0)\n" \
"L_2_%=:\n" \
"v_mfma_f32_16x16x32_bf16 v[64:67], a[128:131], a[64:67], v[32:35]\n" \
"ds_read_b128 a[160:163], v49 offset:0\n" \
"ds_read_b128 a[176:179], v49 offset:4096\n" \
"v_mfma_f32_16x16x32_bf16 v[64:67], a[132:135], a[68:71], v[64:67]\n" \
"v_cvt_pk_bf16_f32 v120, v88, v89\n" \
"v_cvt_pk_bf16_f32 v121, v90, v91\n" \
"v_mfma_f32_16x16x32_bf16 v[64:67], a[136:139], a[72:75], v[64:67]\n" \
"ds_read_b128 a[180:183], v49 offset:5120\n" \
"ds_read_b128 a[164:167], v49 offset:1024\n" \
"v_mfma_f32_16x16x32_bf16 v[64:67], a[140:143], a[76:79], v[64:67]\n" \
"v_exp_f32_e32 v92, v92\n" \
"v_mfma_f32_16x16x32_bf16 v[68:71], a[156:159], a[76:79], v[32:35]\n" \
"ds_read_b128 a[168:171], v49 offset:2048\n" \
"ds_read_b128 a[184:187], v49 offset:6144\n" \
"v_mfma_f32_16x16x32_bf16 v[68:71], a[152:155], a[72:75], v[68:71]\n" \
"v_exp_f32_e32 v93, v93\n" \
"v_mfma_f32_16x16x32_bf16 v[68:71], a[148:151], a[68:71], v[68:71]\n" \
"ds_read_b128 a[188:191], v49 offset:7168\n" \
"ds_read_b128 a[172:175], v49 offset:3072\n" \
"v_mfma_f32_16x16x32_bf16 v[68:71], a[144:147], a[64:67], v[68:71]\n" \
"v_exp_f32_e32 v94, v94\n" \
"v_mfma_f32_16x16x32_bf16 v[72:75], a[128:131], a[80:83], v[36:39]\n" \
"v_exp_f32_e32 v95, v95\n" \
"v_mfma_f32_16x16x32_bf16 v[72:75], a[132:135], a[84:87], v[72:75]\n" \
"v_cvt_pk_bf16_f32 v122, v92, v93\n" \
"v_cvt_pk_bf16_f32 v123, v94, v95\n" \
"v_mfma_f32_16x16x32_bf16 v[72:75], a[136:139], a[88:91], v[72:75]\n" \
"v_exp_f32_e32 v64, v64\n" \
"v_mfma_f32_16x16x32_bf16 v[72:75], a[140:143], a[92:95], v[72:75]\n" \
"v_exp_f32_e32 v65, v65\n" \
"v_mfma_f32_16x16x32_bf16 v[76:79], a[156:159], a[92:95], v[36:39]\n" \
"v_exp_f32_e32 v66, v66\n" \
"v_mfma_f32_16x16x32_bf16 v[76:79], a[152:155], a[88:91], v[76:79]\n" \
"v_exp_f32_e32 v67, v67\n" \
"v_mfma_f32_16x16x32_bf16 v[76:79], a[148:151], a[84:87], v[76:79]\n" \
"v_exp_f32_e32 v68, v68\n" \
"v_mfma_f32_16x16x32_bf16 v[76:79], a[144:147], a[80:83], v[76:79]\n" \
"v_exp_f32_e32 v69, v69\n" \
"v_mfma_f32_16x16x32_bf16 v[80:83], a[128:131], a[96:99], v[40:43]\n" \
"v_exp_f32_e32 v70, v70\n" \
"v_mfma_f32_16x16x32_bf16 v[80:83], a[132:135], a[100:103], v[80:83]\n" \
"v_exp_f32_e32 v71, v71\n" \
"v_mfma_f32_16x16x32_bf16 v[80:83], a[136:139], a[104:107], v[80:83]\n" \
"v_cvt_pk_bf16_f32 v100, v64, v65\n" \
"v_cvt_pk_bf16_f32 v101, v66, v67\n" \
"v_mfma_f32_16x16x32_bf16 v[80:83], a[140:143], a[108:111], v[80:83]\n" \
"v_cvt_pk_bf16_f32 v102, v68, v69\n" \
"v_cvt_pk_bf16_f32 v103, v70, v71\n" \
"v_mfma_f32_16x16x32_bf16 v[84:87], a[156:159], a[108:111], v[40:43]\n" \
"v_exp_f32_e32 v72, v72\n" \
"v_mfma_f32_16x16x32_bf16 v[84:87], a[152:155], a[104:107], v[84:87]\n" \
"v_exp_f32_e32 v73, v73\n" \
"v_mfma_f32_16x16x32_bf16 v[84:87], a[148:151], a[100:103], v[84:87]\n" \
"v_exp_f32_e32 v74, v74\n" \
"v_mfma_f32_16x16x32_bf16 v[84:87], a[144:147], a[96:99], v[84:87]\n" \
"v_exp_f32_e32 v75, v75\n" \
"v_mfma_f32_16x16x32_bf16 v[88:91], a[128:131], a[112:115], v[44:47]\n" \
"v_exp_f32_e32 v76, v76\n" \
"v_mfma_f32_16x16x32_bf16 v[88:91], a[132:135], a[116:119], v[88:91]\n" \
"v_exp_f32_e32 v77, v77\n" \
"v_mfma_f32_16x16x32_bf16 v[88:91], a[136:139], a[120:123], v[88:91]\n" \
"v_exp_f32_e32 v78, v78\n" \
"v_mfma_f32_16x16x32_bf16 v[88:91], a[140:143], a[124:127], v[88:91]\n" \
"v_exp_f32_e32 v79, v79\n" \
"v_mfma_f32_16x16x32_bf16 v[92:95], a[156:159], a[124:127], v[44:47]\n" \
"v_cvt_pk_bf16_f32 v108, v72, v73\n" \
"v_cvt_pk_bf16_f32 v109, v74, v75\n" \
"v_mfma_f32_16x16x32_bf16 v[92:95], a[152:155], a[120:123], v[92:95]\n" \
"v_cvt_pk_bf16_f32 v110, v76, v77\n" \
"v_cvt_pk_bf16_f32 v111, v78, v79\n" \
"v_mfma_f32_16x16x32_bf16 v[92:95], a[148:151], a[116:119], v[92:95]\n" \
"ds_read_b128 a[128:131], v48 offset:0\n" \
"ds_read_b128 a[132:135], v48 offset:1024\n" \
"v_mfma_f32_16x16x32_bf16 v[92:95], a[144:147], a[112:115], v[92:95]\n" \
"s_waitcnt lgkmcnt(2)\n" \
"v_mfma_f32_16x16x32_bf16 v[128:131], a[160:163], v[96:99], v[128:131]\n" \
"ds_read_b128 a[136:139], v48 offset:2048\n" \
"ds_read_b128 a[140:143], v48 offset:3072\n" \
"v_mfma_f32_16x16x32_bf16 v[128:131], a[176:179], v[100:103], v[128:131]\n" \
"v_exp_f32_e32 v80, v80\n" \
"v_mfma_f32_16x16x32_bf16 v[132:135], a[180:183], v[100:103], v[132:135]\n" \
"ds_read_b128 a[144:147], v48 offset:4096\n" \
"ds_read_b128 a[148:151], v48 offset:5120\n" \
"v_mfma_f32_16x16x32_bf16 v[132:135], a[164:167], v[96:99], v[132:135]\n" \
"v_exp_f32_e32 v81, v81\n" \
"v_mfma_f32_16x16x32_bf16 v[136:139], a[168:171], v[96:99], v[136:139]\n" \
"ds_read_b128 a[152:155], v48 offset:6144\n" \
"ds_read_b128 a[156:159], v48 offset:7168\n" \
"v_mfma_f32_16x16x32_bf16 v[136:139], a[184:187], v[100:103], v[136:139]\n" \
"v_exp_f32_e32 v82, v82\n" \
"v_mfma_f32_16x16x32_bf16 v[140:143], a[188:191], v[100:103], v[140:143]\n" \
"v_exp_f32_e32 v83, v83\n" \
"v_mfma_f32_16x16x32_bf16 v[140:143], a[172:175], v[96:99], v[140:143]\n" \
"v_exp_f32_e32 v84, v84\n" \
"v_smfmac_f32_16x16x64_bf16 a[192:195], a[212:215], v[96:103], v21\n" \
"v_mfma_f32_16x16x32_bf16 v[160:163], a[160:163], v[104:107], v[160:163]\n" \
"v_exp_f32_e32 v85, v85\n" \
"v_mfma_f32_16x16x32_bf16 v[160:163], a[176:179], v[108:111], v[160:163]\n" \
"v_exp_f32_e32 v86, v86\n" \
"v_mfma_f32_16x16x32_bf16 v[164:167], a[180:183], v[108:111], v[164:167]\n" \
"v_exp_f32_e32 v87, v87\n" \
"v_mfma_f32_16x16x32_bf16 v[164:167], a[164:167], v[104:107], v[164:167]\n" \
"v_cvt_pk_bf16_f32 v116, v80, v81\n" \
"v_cvt_pk_bf16_f32 v117, v82, v83\n" \
"v_mfma_f32_16x16x32_bf16 v[168:171], a[168:171], v[104:107], v[168:171]\n" \
"v_cvt_pk_bf16_f32 v118, v84, v85\n" \
"v_cvt_pk_bf16_f32 v119, v86, v87\n" \
"v_mfma_f32_16x16x32_bf16 v[168:171], a[184:187], v[108:111], v[168:171]\n" \
"v_exp_f32_e32 v88, v88\n" \
"v_mfma_f32_16x16x32_bf16 v[172:175], a[188:191], v[108:111], v[172:175]\n" \
"v_exp_f32_e32 v89, v89\n" \
"v_mfma_f32_16x16x32_bf16 v[172:175], a[172:175], v[104:107], v[172:175]\n" \
"v_exp_f32_e32 v90, v90\n" \
"v_smfmac_f32_16x16x64_bf16 a[196:199], a[212:215], v[104:111], v21\n" \
"v_mfma_f32_16x16x32_bf16 v[192:195], a[160:163], v[112:115], v[192:195]\n" \
"v_exp_f32_e32 v91, v91\n" \
"v_mfma_f32_16x16x32_bf16 v[192:195], a[176:179], v[116:119], v[192:195]\n" \
"v_exp_f32_e32 v92, v92\n" \
"v_mfma_f32_16x16x32_bf16 v[196:199], a[180:183], v[116:119], v[196:199]\n" \
"v_exp_f32_e32 v93, v93\n" \
"v_mfma_f32_16x16x32_bf16 v[196:199], a[164:167], v[112:115], v[196:199]\n" \
"v_exp_f32_e32 v94, v94\n" \
"v_mfma_f32_16x16x32_bf16 v[200:203], a[168:171], v[112:115], v[200:203]\n" \
"v_exp_f32_e32 v95, v95\n" \
"v_mfma_f32_16x16x32_bf16 v[200:203], a[184:187], v[116:119], v[200:203]\n" \
"v_cvt_pk_bf16_f32 v124, v88, v89\n" \
"v_cvt_pk_bf16_f32 v125, v90, v91\n" \
"v_mfma_f32_16x16x32_bf16 v[204:207], a[188:191], v[116:119], v[204:207]\n" \
"v_cvt_pk_bf16_f32 v126, v92, v93\n" \
"v_cvt_pk_bf16_f32 v127, v94, v95\n" \
"v_mfma_f32_16x16x32_bf16 v[204:207], a[172:175], v[112:115], v[204:207]\n" \
"v_smfmac_f32_16x16x64_bf16 a[200:203], a[212:215], v[112:119], v21\n" \
"s_waitcnt vmcnt(0) lgkmcnt(0)\n" \
"s_barrier\n" \
"v_mfma_f32_16x16x32_bf16 v[224:227], a[160:163], v[120:123], v[224:227]\n" \
"s_add_u32 s63, s63, s63\n" \
"s_cbranch_scc1 L_h2_%=\n" \
"L_hr2_%=:\n" \
"v_mfma_f32_16x16x32_bf16 v[224:227], a[176:179], v[124:127], v[224:227]\n" \
"s_xor_b32 m0, s65, 0x12000\n" \
"ds_read_b128 a[160:163], v49 offset:8192\n" \
"v_mfma_f32_16x16x32_bf16 v[228:231], a[180:183], v[124:127], v[228:231]\n" \
"buffer_load_dwordx4 v50, s[8:11], s64 offen offset:0 sc1 lds\n" \
"v_mfma_f32_16x16x32_bf16 v[228:231], a[164:167], v[120:123], v[228:231]\n" \
"ds_read_b128 a[176:179], v49 offset:12288\n" \
"ds_read_b128 a[180:183], v49 offset:13312\n" \
"v_mfma_f32_16x16x32_bf16 v[232:235], a[168:171], v[120:123], v[232:235]\n" \
"v_mfma_f32_16x16x32_bf16 v[232:235], a[184:187], v[124:127], v[232:235]\n" \
"buffer_load_dwordx4 v50, s[8:11], s64 offen offset:1024 sc1 lds\n" \
"v_mfma_f32_16x16x32_bf16 v[236:239], a[188:191], v[124:127], v[236:239]\n" \
"ds_read_b128 a[164:167], v49 offset:9216\n" \
"ds_read_b128 a[168:171], v49 offset:10240\n" \
"v_mfma_f32_16x16x32_bf16 v[236:239], a[172:175], v[120:123], v[236:239]\n" \
"v_mfma_f32_16x16x32_bf16 v[64:67], a[128:131], a[64:67], v[32:35]\n" \
"ds_read_b128 a[184:187], v49 offset:14336\n" \
"ds_read_b128 a[188:191], v49 offset:15360\n" \
"v_mfma_f32_16x16x32_bf16 v[64:67], a[132:135], a[68:71], v[64:67]\n" \
"buffer_load_dwordx4 v50, s[8:11], s64 offen offset:2048 sc1 lds\n" \
"v_mfma_f32_16x16x32_bf16 v[64:67], a[136:139], a[72:75], v[64:67]\n" \
"ds_read_b128 a[172:175], v49 offset:11264\n" \
"v_add_u32_e32 v49, 0x10000, v48\n" \
"v_mfma_f32_16x16x32_bf16 v[64:67], a[140:143], a[76:79], v[64:67]\n" \
"v_mfma_f32_16x16x32_bf16 v[68:71], a[156:159], a[76:79], v[32:35]\n" \
"v_mfma_f32_16x16x32_bf16 v[68:71], a[152:155], a[72:75], v[68:71]\n" \
"v_mfma_f32_16x16x32_bf16 v[68:71], a[148:151], a[68:71], v[68:71]\n" \
"buffer_load_dwordx4 v50, s[8:11], s64 offen offset:3072 sc1 lds\n" \
"v_mfma_f32_16x16x32_bf16 v[68:71], a[144:147], a[64:67], v[68:71]\n" \
"s_xor_b32 m0, s65, 0xa000\n" \
"v_mfma_f32_16x16x32_bf16 v[72:75], a[128:131], a[80:83], v[36:39]\n" \
"v_mfma_f32_16x16x32_bf16 v[72:75], a[132:135], a[84:87], v[72:75]\n" \
"v_exp_f32_e32 v64, v64\n" \
"v_mfma_f32_16x16x32_bf16 v[72:75], a[136:139], a[88:91], v[72:75]\n" \
"buffer_load_dwordx4 v51, s[8:11], s64 offen offset:0 sc1 lds\n" \
"v_mfma_f32_16x16x32_bf16 v[72:75], a[140:143], a[92:95], v[72:75]\n" \
"v_exp_f32_e32 v65, v65\n" \
"v_mfma_f32_16x16x32_bf16 v[76:79], a[156:159], a[92:95], v[36:39]\n" \
"v_exp_f32_e32 v66, v66\n" \
"v_mfma_f32_16x16x32_bf16 v[76:79], a[152:155], a[88:91], v[76:79]\n" \
"v_exp_f32_e32 v67, v67\n" \
"v_mfma_f32_16x16x32_bf16 v[76:79], a[148:151], a[84:87], v[76:79]\n" \
"buffer_load_dwordx4 v51, s[8:11], s64 offen offset:1024 sc1 lds\n" \
"v_mfma_f32_16x16x32_bf16 v[76:79], a[144:147], a[80:83], v[76:79]\n" \
"v_exp_f32_e32 v68, v68\n" \
"v_mfma_f32_16x16x32_bf16 v[80:83], a[128:131], a[96:99], v[40:43]\n" \
"v_exp_f32_e32 v69, v69\n" \
"v_mfma_f32_16x16x32_bf16 v[80:83], a[132:135], a[100:103], v[80:83]\n" \
"v_exp_f32_e32 v70, v70\n" \
"v_mfma_f32_16x16x32_bf16 v[80:83], a[136:139], a[104:107], v[80:83]\n" \
"v_exp_f32_e32 v71, v71\n" \
"v_mfma_f32_16x16x32_bf16 v[80:83], a[140:143], a[108:111], v[80:83]\n" \
"buffer_load_dwordx4 v51, s[8:11], s64 offen offset:2048 sc1 lds\n" \
"v_mfma_f32_16x16x32_bf16 v[84:87], a[156:159], a[108:111], v[40:43]\n" \
"v_exp_f32_e32 v72, v72\n" \
"v_mfma_f32_16x16x32_bf16 v[84:87], a[152:155], a[104:107], v[84:87]\n" \
"v_exp_f32_e32 v73, v73\n" \
"v_mfma_f32_16x16x32_bf16 v[84:87], a[148:151], a[100:103], v[84:87]\n" \
"v_exp_f32_e32 v74, v74\n" \
"v_mfma_f32_16x16x32_bf16 v[84:87], a[144:147], a[96:99], v[84:87]\n" \
"buffer_load_dwordx4 v51, s[8:11], s64 offen offset:3072 sc1 lds\n" \
"v_mfma_f32_16x16x32_bf16 v[88:91], a[128:131], a[112:115], v[44:47]\n" \
"s_xor_b32 m0, s65, 0x18000\n" \
"ds_read_b128 a[128:131], v48 offset:8192\n" \
"v_mfma_f32_16x16x32_bf16 v[88:91], a[132:135], a[116:119], v[88:91]\n" \
"v_exp_f32_e32 v75, v75\n" \
"v_mfma_f32_16x16x32_bf16 v[88:91], a[136:139], a[120:123], v[88:91]\n" \
"ds_read_b128 a[132:135], v48 offset:9216\n" \
"ds_read_b128 a[136:139], v48 offset:10240\n" \
"v_mfma_f32_16x16x32_bf16 v[88:91], a[140:143], a[124:127], v[88:91]\n" \
"v_exp_f32_e32 v76, v76\n" \
"v_mfma_f32_16x16x32_bf16 v[92:95], a[156:159], a[124:127], v[44:47]\n" \
"buffer_load_dwordx4 v52, s[8:11], s64 offen offset:0 sc1 lds\n" \
"v_mfma_f32_16x16x32_bf16 v[92:95], a[152:155], a[120:123], v[92:95]\n" \
"ds_read_b128 a[140:143], v48 offset:11264\n" \
"ds_read_b128 a[152:155], v48 offset:14336\n" \
"v_mfma_f32_16x16x32_bf16 v[92:95], a[148:151], a[116:119], v[92:95]\n" \
"v_exp_f32_e32 v77, v77\n" \
"v_mfma_f32_16x16x32_bf16 v[92:95], a[144:147], a[112:115], v[92:95]\n" \
"ds_read_b128 a[144:147], v48 offset:12288\n" \
"s_waitcnt lgkmcnt(6)\n" \
"v_mfma_f32_16x16x32_bf16 v[144:147], a[160:163], v[96:99], v[144:147]\n" \
"buffer_load_dwordx4 v52, s[8:11], s64 offen offset:1024 sc1 lds\n" \
"v_mfma_f32_16x16x32_bf16 v[144:147], a[176:179], v[100:103], v[144:147]\n" \
"ds_read_b128 a[148:151], v48 offset:13312\n" \
"ds_read_b128 a[156:159], v48 offset:15360\n" \
"v_mfma_f32_16x16x32_bf16 v[148:151], a[180:183], v[100:103], v[148:151]\n" \
"v_exp_f32_e32 v78, v78\n" \
"v_mfma_f32_16x16x32_bf16 v[148:151], a[164:167], v[96:99], v[148:151]\n" \
"v_exp_f32_e32 v79, v79\n" \
"v_mfma_f32_16x16x32_bf16 v[152:155], a[168:171], v[96:99], v[152:155]\n" \
"buffer_load_dwordx4 v52, s[8:11], s64 offen offset:2048 sc1 lds\n" \
"v_mfma_f32_16x16x32_bf16 v[152:155], a[184:187], v[100:103], v[152:155]\n" \
"v_exp_f32_e32 v80, v80\n" \
"v_mfma_f32_16x16x32_bf16 v[156:159], a[188:191], v[100:103], v[156:159]\n" \
"v_exp_f32_e32 v81, v81\n" \
"v_mfma_f32_16x16x32_bf16 v[156:159], a[172:175], v[96:99], v[156:159]\n" \
"v_exp_f32_e32 v82, v82\n" \
"v_mfma_f32_16x16x32_bf16 v[176:179], a[160:163], v[104:107], v[176:179]\n" \
"v_exp_f32_e32 v83, v83\n" \
"v_mfma_f32_16x16x32_bf16 v[176:179], a[176:179], v[108:111], v[176:179]\n" \
"buffer_load_dwordx4 v52, s[8:11], s64 offen offset:3072 sc1 lds\n" \
"v_mfma_f32_16x16x32_bf16 v[180:183], a[180:183], v[108:111], v[180:183]\n" \
"s_mov_b32 m0, s65\n" \
"s_xor_b32 s65, s65, 0x8000\n" \
"v_mfma_f32_16x16x32_bf16 v[180:183], a[164:167], v[104:107], v[180:183]\n" \
"v_exp_f32_e32 v84, v84\n" \
"v_mfma_f32_16x16x32_bf16 v[184:187], a[168:171], v[104:107], v[184:187]\n" \
"v_exp_f32_e32 v85, v85\n" \
"v_mfma_f32_16x16x32_bf16 v[184:187], a[184:187], v[108:111], v[184:187]\n" \
"buffer_load_dwordx4 v53, s[8:11], s64 offen offset:0 sc1 lds\n" \
"v_mfma_f32_16x16x32_bf16 v[188:191], a[188:191], v[108:111], v[188:191]\n" \
"v_exp_f32_e32 v86, v86\n" \
"v_mfma_f32_16x16x32_bf16 v[188:191], a[172:175], v[104:107], v[188:191]\n" \
"v_exp_f32_e32 v87, v87\n" \
"v_mfma_f32_16x16x32_bf16 v[208:211], a[160:163], v[112:115], v[208:211]\n" \
"v_exp_f32_e32 v88, v88\n" \
"v_mfma_f32_16x16x32_bf16 v[208:211], a[176:179], v[116:119], v[208:211]\n" \
"buffer_load_dwordx4 v53, s[8:11], s64 offen offset:1024 sc1 lds\n" \
"v_mfma_f32_16x16x32_bf16 v[212:215], a[180:183], v[116:119], v[212:215]\n" \
"v_exp_f32_e32 v89, v89\n" \
"v_mfma_f32_16x16x32_bf16 v[212:215], a[164:167], v[112:115], v[212:215]\n" \
"v_exp_f32_e32 v90, v90\n" \
"v_mfma_f32_16x16x32_bf16 v[216:219], a[168:171], v[112:115], v[216:219]\n" \
"v_exp_f32_e32 v91, v91\n" \
"v_mfma_f32_16x16x32_bf16 v[216:219], a[184:187], v[116:119], v[216:219]\n" \
"v_cvt_pk_bf16_f32 v96, v64, v65\n" \
"v_cvt_pk_bf16_f32 v97, v66, v67\n" \
"v_mfma_f32_16x16x32_bf16 v[220:223], a[188:191], v[116:119], v[220:223]\n" \
"buffer_load_dwordx4 v53, s[8:11], s64 offen offset:2048 sc1 lds\n" \
"v_mfma_f32_16x16x32_bf16 v[220:223], a[172:175], v[112:115], v[220:223]\n" \
"v_cvt_pk_bf16_f32 v98, v68, v69\n" \
"v_cvt_pk_bf16_f32 v99, v70, v71\n" \
"v_mfma_f32_16x16x32_bf16 v[240:243], a[160:163], v[120:123], v[240:243]\n" \
"v_cvt_pk_bf16_f32 v104, v72, v73\n" \
"v_cvt_pk_bf16_f32 v105, v74, v75\n" \
"v_mfma_f32_16x16x32_bf16 v[240:243], a[176:179], v[124:127], v[240:243]\n" \
"v_cvt_pk_bf16_f32 v106, v76, v77\n" \
"v_cvt_pk_bf16_f32 v107, v78, v79\n" \
"v_mfma_f32_16x16x32_bf16 v[244:247], a[180:183], v[124:127], v[244:247]\n" \
"buffer_load_dwordx4 v53, s[8:11], s64 offen offset:3072 sc1 lds\n" \
"v_mfma_f32_16x16x32_bf16 v[244:247], a[164:167], v[120:123], v[244:247]\n" \
"s_add_u32 s64, s64, 0x10000\n" \
"s_sub_u32 s66, s66, 1\n" \
"v_mfma_f32_16x16x32_bf16 v[248:251], a[168:171], v[120:123], v[248:251]\n" \
"v_cvt_pk_bf16_f32 v112, v80, v81\n" \
"v_cvt_pk_bf16_f32 v113, v82, v83\n" \
"v_mfma_f32_16x16x32_bf16 v[248:251], a[184:187], v[124:127], v[248:251]\n" \
"s_cbranch_scc0 L_t2_%=\n" \
"v_mfma_f32_16x16x32_bf16 v[252:255], a[188:191], v[124:127], v[252:255]\n" \
"v_cvt_pk_bf16_f32 v114, v84, v85\n" \
"v_cvt_pk_bf16_f32 v115, v86, v87\n" \
"v_mfma_f32_16x16x32_bf16 v[252:255], a[172:175], v[120:123], v[252:255]\n" \
"v_xor_b32_e32 v48, 0x8000, v48\n" \
"v_smfmac_f32_16x16x64_bf16 a[204:207], a[212:215], v[120:127], v21\n" \
"s_waitcnt lgkmcnt(0)\n" \
"s_waitcnt vmcnt(12)\n" \
"s_barrier\n" \
"s_cmp_lg_u32 s16, 0\n" \
"s_cbranch_scc1 L_f1_%=\n" \
"ds_read_b128 a[160:163], v49 offset:0\n" \
"ds_read_b128 a[164:167], v49 offset:1024\n" \
"ds_read_b128 a[168:171], v49 offset:2048\n" \
"ds_read_b128 a[172:175], v49 offset:3072\n" \
"ds_read_b128 a[176:179], v49 offset:8192\n" \
"ds_read_b128 a[180:183], v49 offset:9216\n" \
"v_cvt_pk_bf16_f32 v120, v88, v89\n" \
"v_cvt_pk_bf16_f32 v121, v90, v91\n" \
"v_mfma_f32_16x16x32_bf16 v[88:91], a[128:131], a[112:115], v[44:47]\n" \
"s_add_u32 s70, s61, 0x0\n" \
"s_add_u32 s71, s61, 0x49000\n" \
"ds_read_b128 a[184:187], v49 offset:10240\n" \
"v_mfma_f32_16x16x32_bf16 v[88:91], a[132:135], a[116:119], v[88:91]\n" \
"ds_read_b128 a[188:191], v49 offset:11264\n" \
"v_add_u32_e32 v54, 0x49000, v20\n" \
"v_mfma_f32_16x16x32_bf16 v[88:91], a[136:139], a[120:123], v[88:91]\n" \
"v_exp_f32_e32 v92, v92\n" \
"v_mfma_f32_16x16x32_bf16 v[88:91], a[140:143], a[124:127], v[88:91]\n" \
"v_exp_f32_e32 v93, v93\n" \
"v_exp_f32_e32 v94, v94\n" \
"v_exp_f32_e32 v95, v95\n" \
"v_cvt_pk_bf16_f32 v122, v92, v93\n" \
"v_cvt_pk_bf16_f32 v123, v94, v95\n" \
"v_mfma_f32_16x16x32_bf16 v[92:95], a[156:159], a[124:127], v[44:47]\n" \
"buffer_load_dwordx4 a[220:223], v20, s[56:59], s60 offen offset:0 sc1\n" \
"v_mfma_f32_16x16x32_bf16 v[92:95], a[152:155], a[120:123], v[92:95]\n" \
"v_readlane_b32 s97, v11, 32\n" \
"v_readlane_b32 s98, v11, 33\n" \
"v_mfma_f32_16x16x32_bf16 v[92:95], a[148:151], a[116:119], v[92:95]\n" \
"v_and_b32_e32 v112, v12, v112\n" \
"v_and_b32_e32 v113, v13, v113\n" \
"v_mfma_f32_16x16x32_bf16 v[92:95], a[144:147], a[112:115], v[92:95]\n" \
"v_and_b32_e32 v114, v14, v114\n" \
"s_waitcnt lgkmcnt(7)\n" \
"v_mfma_f32_16x16x32_bf16 v[128:131], a[160:163], v[96:99], v[128:131]\n" \
"v_and_b32_e32 v115, v15, v115\n" \
"s_waitcnt lgkmcnt(6)\n" \
"v_mfma_f32_16x16x32_bf16 v[132:135], a[164:167], v[96:99], v[132:135]\n" \
"ds_read_b32 v60, v18 offset:0\n" \
"s_waitcnt lgkmcnt(6)\n" \
"v_mfma_f32_16x16x32_bf16 v[136:139], a[168:171], v[96:99], v[136:139]\n" \
"ds_read_b32 v61, v18 offset:128\n" \
"s_waitcnt lgkmcnt(6)\n" \
"v_mfma_f32_16x16x32_bf16 v[140:143], a[172:175], v[96:99], v[140:143]\n" \
"ds_read_b128 a[0:3], v49 offset:4096\n" \
"s_waitcnt lgkmcnt(5)\n" \
"v_mfma_f32_16x16x32_bf16 v[144:147], a[176:179], v[96:99], v[144:147]\n" \
"buffer_load_dwordx4 a[236:239], v54, s[56:59], s60 offen offset:0 sc1\n" \
"v_mfma_f32_16x16x32_bf16 v[148:151], a[180:183], v[96:99], v[148:151]\n" \
"ds_read_b128 a[4:7], v49 offset:5120\n" \
"s_waitcnt lgkmcnt(5)\n" \
"v_mfma_f32_16x16x32_bf16 v[152:155], a[184:187], v[96:99], v[152:155]\n" \
"ds_read_b128 a[8:11], v49 offset:6144\n" \
"s_waitcnt lgkmcnt(5)\n" \
"v_mfma_f32_16x16x32_bf16 v[156:159], a[188:191], v[96:99], v[156:159]\n" \
"buffer_load_dwordx4 a[224:227], v20, s[56:59], s60 offen offset:1024 sc1\n" \
"v_mfma_f32_16x16x32_bf16 a[192:195], a[252:255], v[96:99], a[192:195]\n" \
"ds_read_b128 a[12:15], v49 offset:7168\n" \
"ds_read_b128 a[16:19], v49 offset:12288\n" \
"v_mfma_f32_16x16x32_bf16 v[160:163], a[160:163], v[104:107], v[160:163]\n" \
"ds_read_b128 a[20:23], v49 offset:13312\n" \
"ds_read_b128 a[24:27], v49 offset:14336\n" \
"buffer_load_dwordx4 a[240:243], v54, s[56:59], s60 offen offset:1024 sc1\n" \
"v_mfma_f32_16x16x32_bf16 v[164:167], a[164:167], v[104:107], v[164:167]\n" \
"ds_read_b128 a[28:31], v49 offset:15360\n" \
"v_accvgpr_read_b32 v40, a192\n" \
"v_mfma_f32_16x16x32_bf16 v[168:171], a[168:171], v[104:107], v[168:171]\n" \
"v_accvgpr_read_b32 v23, a193\n" \
"v_add_f32_e32 v40, v40, v23\n" \
"v_mfma_f32_16x16x32_bf16 v[172:175], a[172:175], v[104:107], v[172:175]\n" \
"buffer_load_dwordx4 a[228:231], v20, s[56:59], s60 offen offset:2048 sc1\n" \
"v_mfma_f32_16x16x32_bf16 v[176:179], a[176:179], v[104:107], v[176:179]\n" \
"v_add_u32_e32 v50, s15, v48\n" \
"v_add_u32_e32 v51, 0x10000, v50\n" \
"v_mfma_f32_16x16x32_bf16 v[180:183], a[180:183], v[104:107], v[180:183]\n" \
"s_waitcnt vmcnt(5)\n" \
"ds_write_b128 v50, v[128:131] offset:0\n" \
"buffer_load_dwordx4 a[244:247], v54, s[56:59], s60 offen offset:2048 sc1\n" \
"v_mfma_f32_16x16x32_bf16 v[184:187], a[184:187], v[104:107], v[184:187]\n" \
"ds_write_b128 v50, v[132:135] offset:1024\n" \
"v_mfma_f32_16x16x32_bf16 v[188:191], a[188:191], v[104:107], v[188:191]\n" \
"ds_write_b128 v50, v[136:139] offset:2048\n" \
"buffer_load_dwordx4 a[232:235], v20, s[56:59], s60 offen offset:3072 sc1\n" \
"v_mfma_f32_16x16x32_bf16 a[196:199], a[252:255], v[104:107], a[196:199]\n" \
"ds_write_b128 v50, v[140:143] offset:3072\n" \
"v_mfma_f32_16x16x32_bf16 v[192:195], a[160:163], v[112:115], v[192:195]\n" \
"ds_write_b128 v50, v[144:147] offset:8192\n" \
"buffer_load_dwordx4 a[248:251], v54, s[56:59], s60 offen offset:3072 sc1\n" \
"v_mfma_f32_16x16x32_bf16 v[196:199], a[164:167], v[112:115], v[196:199]\n" \
"ds_write_b128 v50, v[148:151] offset:9216\n" \
"v_mfma_f32_16x16x32_bf16 v[200:203], a[168:171], v[112:115], v[200:203]\n" \
"ds_write_b128 v50, v[152:155] offset:10240\n" \
"buffer_load_dwordx4 a[32:35], v20, s[56:59], s97 offen offset:0 sc1\n" \
"v_mfma_f32_16x16x32_bf16 v[204:207], a[172:175], v[112:115], v[204:207]\n" \
"v_accvgpr_read_b32 v41, a196\n" \
"v_accvgpr_read_b32 v23, a197\n" \
"v_mfma_f32_16x16x32_bf16 v[208:211], a[176:179], v[112:115], v[208:211]\n" \
"v_add_f32_e32 v41, v41, v23\n" \
"v_xor_b32_e32 v52, 0x4000, v50\n" \
"v_mfma_f32_16x16x32_bf16 v[212:215], a[180:183], v[112:115], v[212:215]\n" \
"buffer_load_dwordx4 a[40:43], v54, s[56:59], s97 offen offset:0 sc1\n" \
"v_mfma_f32_16x16x32_bf16 v[216:219], a[184:187], v[112:115], v[216:219]\n" \
"ds_write_b128 v50, v[156:159] offset:11264\n" \
"v_mfma_f32_16x16x32_bf16 v[220:223], a[188:191], v[112:115], v[220:223]\n" \
"ds_write_b128 v51, v[160:163] offset:0\n" \
"buffer_load_dwordx4 a[48:51], v20, s[56:59], s98 offen offset:0 sc1\n" \
"v_mfma_f32_16x16x32_bf16 a[200:203], a[252:255], v[112:115], a[200:203]\n" \
"ds_write_b128 v51, v[164:167] offset:1024\n" \
"ds_write_b128 v51, v[168:171] offset:2048\n" \
"ds_write_b128 v51, v[172:175] offset:3072\n" \
"buffer_load_dwordx4 a[56:59], v54, s[56:59], s98 offen offset:0 sc1\n" \
"ds_write_b128 v51, v[176:179] offset:8192\n" \
"ds_write_b128 v51, v[180:183] offset:9216\n" \
"ds_write_b128 v51, v[184:187] offset:10240\n" \
"ds_write_b128 v51, v[188:191] offset:11264\n" \
"buffer_load_dwordx4 a[36:39], v20, s[56:59], s97 offen offset:1024 sc1\n" \
"ds_write_b64 v19, v[40:41]\n" \
"v_xor_b32_e32 v53, 0x4000, v51\n" \
"v_xor_b32_e32 v63, 0x400, v19\n" \
"v_readlane_b32 s93, v11, 18\n" \
"v_readlane_b32 s84, v11, 13\n" \
"v_readlane_b32 s85, v11, 14\n" \
"v_readlane_b32 s86, v11, 15\n" \
"s_mov_b32 s87, 0x20000\n" \
"v_readlane_b32 s92, v11, 16\n" \
"s_add_u32 m0, s93, 0x0\n" \
"v_exp_f32_e32 v88, v88\n" \
"v_exp_f32_e32 v89, v89\n" \
"v_exp_f32_e32 v90, v90\n" \
"v_exp_f32_e32 v91, v91\n" \
"v_exp_f32_e32 v92, v92\n" \
"v_exp_f32_e32 v93, v93\n" \
"v_exp_f32_e32 v94, v94\n" \
"v_exp_f32_e32 v95, v95\n" \
"v_cvt_pk_bf16_f32 v124, v88, v89\n" \
"v_cvt_pk_bf16_f32 v125, v90, v91\n" \
"v_cvt_pk_bf16_f32 v126, v92, v93\n" \
"v_cvt_pk_bf16_f32 v127, v94, v95\n" \
"v_and_b32_e32 v124, v12, v124\n" \
"v_and_b32_e32 v125, v13, v125\n" \
"v_and_b32_e32 v126, v14, v126\n" \
"v_and_b32_e32 v127, v15, v127\n" \
"v_accvgpr_read_b32 v56, a200\n" \
"v_accvgpr_read_b32 v23, a201\n" \
"v_add_f32_e32 v56, v56, v23\n" \
"s_waitcnt lgkmcnt(0)\n" \
"s_barrier\n" \
"buffer_load_dwordx4 a[44:47], v54, s[56:59], s97 offen offset:1024 sc1\n" \
"ds_read_b128 v[128:131], v52 offset:0\n" \
"ds_read_b128 v[132:135], v52 offset:1024\n" \
"ds_read_b128 v[136:139], v52 offset:2048\n" \
"ds_read_b128 v[140:143], v52 offset:3072\n" \
"ds_read_b128 v[144:147], v52 offset:8192\n" \
"ds_read_b128 v[148:151], v52 offset:9216\n" \
"ds_read_b128 v[152:155], v52 offset:10240\n" \
"ds_read_b128 v[156:159], v52 offset:11264\n" \
"buffer_load_dwordx4 a[52:55], v20, s[56:59], s98 offen offset:1024 sc1\n" \
"ds_read_b128 v[160:163], v53 offset:0\n" \
"ds_read_b128 v[164:167], v53 offset:1024\n" \
"ds_read_b128 v[168:171], v53 offset:2048\n" \
"ds_read_b128 v[172:175], v53 offset:3072\n" \
"ds_read_b128 v[176:179], v53 offset:8192\n" \
"ds_read_b128 v[180:183], v53 offset:9216\n" \
"ds_read_b128 v[184:187], v53 offset:10240\n" \
"ds_read_b128 v[188:191], v53 offset:11264\n" \
"buffer_load_dwordx4 a[60:63], v54, s[56:59], s98 offen offset:1024 sc1\n" \
"L_pf13:\n" \
"s_cbranch_execz L_pf17\n" \
"s_nop 0\n" \
"ds_read_b64 v[42:43], v63\n" \
"v_mfma_f32_16x16x32_bf16 v[224:227], a[160:163], v[120:123], v[224:227]\n" \
"v_mfma_f32_16x16x32_bf16 v[228:231], a[164:167], v[120:123], v[228:231]\n" \
"v_mfma_f32_16x16x32_bf16 v[232:235], a[168:171], v[120:123], v[232:235]\n" \
"buffer_load_dwordx4 v0, s[84:87], s92 offen offset:0 lds\n" \
"v_mfma_f32_16x16x32_bf16 v[236:239], a[172:175], v[120:123], v[236:239]\n" \
"v_mfma_f32_16x16x32_bf16 v[240:243], a[176:179], v[120:123], v[240:243]\n" \
"v_mfma_f32_16x16x32_bf16 v[244:247], a[180:183], v[120:123], v[244:247]\n" \
"buffer_load_dwordx4 v1, s[84:87], s92 offen offset:1024 lds\n" \
"v_mfma_f32_16x16x32_bf16 v[248:251], a[184:187], v[120:123], v[248:251]\n" \
"s_waitcnt lgkmcnt(15)\n" \
"v_add_f32_e32 v192, v192, v128\n" \
"v_mfma_f32_16x16x32_bf16 v[252:255], a[188:191], v[120:123], v[252:255]\n" \
"v_add_f32_e32 v193, v193, v129\n" \
"v_add_f32_e32 v194, v194, v130\n" \
"v_mfma_f32_16x16x32_bf16 a[204:207], a[252:255], v[120:123], a[204:207]\n" \
"buffer_load_dwordx4 v2, s[84:87], s92 offen offset:2048 lds\n" \
"v_add_f32_e32 v195, v195, v131\n" \
"s_waitcnt lgkmcnt(1)\n" \
"s_barrier\n" \
"v_mfma_f32_16x16x32_bf16 v[224:227], a[0:3], v[124:127], v[224:227]\n" \
"buffer_load_dwordx4 v3, s[84:87], s92 offen offset:3072 lds\n" \
"s_add_u32 m0, s93, 0x1000\n" \
"v_mfma_f32_16x16x32_bf16 v[228:231], a[4:7], v[124:127], v[228:231]\n" \
"s_waitcnt lgkmcnt(0)\n" \
"v_add_f32_e32 v56, v56, v42\n" \
"v_mfma_f32_16x16x32_bf16 v[232:235], a[8:11], v[124:127], v[232:235]\n" \
"v_max_f32_e32 v62, v62, v56\n" \
"v_add_f32_e32 v196, v196, v132\n" \
"v_mfma_f32_16x16x32_bf16 v[236:239], a[12:15], v[124:127], v[236:239]\n" \
"buffer_load_dwordx4 v4, s[84:87], s92 offen offset:0 lds\n" \
"v_rcp_f32_e32 v56, v56\n" \
"v_mfma_f32_16x16x32_bf16 v[240:243], a[16:19], v[124:127], v[240:243]\n" \
"v_add_f32_e32 v197, v197, v133\n" \
"v_add_f32_e32 v198, v198, v134\n" \
"v_add_f32_e32 v199, v199, v135\n" \
"v_mul_f32_e32 v56, v56, v60\n" \
"v_mul_f32_e32 v192, v56, v192\n" \
"v_mul_f32_e32 v193, v56, v193\n" \
"v_mul_f32_e32 v194, v56, v194\n" \
"v_mul_f32_e32 v195, v56, v195\n" \
"v_mul_f32_e32 v196, v56, v196\n" \
"v_mul_f32_e32 v197, v56, v197\n" \
"v_mul_f32_e32 v198, v56, v198\n" \
"v_mul_f32_e32 v199, v56, v199\n" \
"v_add_f32_e32 v200, v200, v136\n" \
"v_add_f32_e32 v201, v201, v137\n" \
"v_add_f32_e32 v202, v202, v138\n" \
"v_add_f32_e32 v203, v203, v139\n" \
"v_add_f32_e32 v204, v204, v140\n" \
"v_add_f32_e32 v205, v205, v141\n" \
"v_add_f32_e32 v206, v206, v142\n" \
"v_add_f32_e32 v207, v207, v143\n" \
"v_cvt_pk_bf16_f32 v64, v192, v193\n" \
"v_cvt_pk_bf16_f32 v65, v194, v195\n" \
"v_cvt_pk_bf16_f32 v66, v196, v197\n" \
"v_cvt_pk_bf16_f32 v67, v198, v199\n" \
"buffer_store_dwordx4 v[64:67], v20, s[56:59], s70 offen offset:0 sc1\n" \
"v_mfma_f32_16x16x32_bf16 v[244:247], a[20:23], v[124:127], v[244:247]\n" \
"v_mfma_f32_16x16x32_bf16 a[208:211], a[216:219], v[64:67], a[208:211]\n" \
"v_mul_f32_e32 v200, v56, v200\n" \
"v_mul_f32_e32 v201, v56, v201\n" \
"v_mul_f32_e32 v202, v56, v202\n" \
"v_mul_f32_e32 v203, v56, v203\n" \
"v_mul_f32_e32 v204, v56, v204\n" \
"v_mul_f32_e32 v205, v56, v205\n" \
"v_mul_f32_e32 v206, v56, v206\n" \
"v_mul_f32_e32 v207, v56, v207\n" \
"v_add_f32_e32 v208, v208, v144\n" \
"v_add_f32_e32 v209, v209, v145\n" \
"v_add_f32_e32 v210, v210, v146\n" \
"v_add_f32_e32 v211, v211, v147\n" \
"v_add_f32_e32 v212, v212, v148\n" \
"v_add_f32_e32 v213, v213, v149\n" \
"v_add_f32_e32 v214, v214, v150\n" \
"v_add_f32_e32 v215, v215, v151\n" \
"v_cvt_pk_bf16_f32 v68, v200, v201\n" \
"v_cvt_pk_bf16_f32 v69, v202, v203\n" \
"v_cvt_pk_bf16_f32 v70, v204, v205\n" \
"v_cvt_pk_bf16_f32 v71, v206, v207\n" \
"buffer_store_dwordx4 v[68:71], v20, s[56:59], s70 offen offset:1024 sc1\n" \
"v_mul_f32_e32 v208, v56, v208\n" \
"v_mfma_f32_16x16x32_bf16 a[208:211], a[216:219], v[68:71], a[208:211]\n" \
"v_mul_f32_e32 v209, v56, v209\n" \
"v_mul_f32_e32 v210, v56, v210\n" \
"v_mul_f32_e32 v211, v56, v211\n" \
"v_mul_f32_e32 v212, v56, v212\n" \
"v_mul_f32_e32 v213, v56, v213\n" \
"v_mul_f32_e32 v214, v56, v214\n" \
"v_mul_f32_e32 v215, v56, v215\n" \
"v_add_f32_e32 v216, v216, v152\n" \
"v_add_f32_e32 v217, v217, v153\n" \
"v_add_f32_e32 v218, v218, v154\n" \
"v_add_f32_e32 v219, v219, v155\n" \
"v_add_f32_e32 v220, v220, v156\n" \
"v_add_f32_e32 v221, v221, v157\n" \
"v_add_f32_e32 v222, v222, v158\n" \
"v_add_f32_e32 v223, v223, v159\n" \
"v_cvt_pk_bf16_f32 v72, v208, v209\n" \
"v_cvt_pk_bf16_f32 v73, v210, v211\n" \
"v_cvt_pk_bf16_f32 v74, v212, v213\n" \
"v_cvt_pk_bf16_f32 v75, v214, v215\n" \
"buffer_store_dwordx4 v[72:75], v20, s[56:59], s70 offen offset:2048 sc1\n" \
"v_mfma_f32_16x16x32_bf16 v[248:251], a[24:27], v[124:127], v[248:251]\n" \
"v_mfma_f32_16x16x32_bf16 a[208:211], a[216:219], v[72:75], a[208:211]\n" \
"v_mul_f32_e32 v216, v56, v216\n" \
"v_mul_f32_e32 v217, v56, v217\n" \
"v_mul_f32_e32 v218, v56, v218\n" \
"v_mul_f32_e32 v219, v56, v219\n" \
"v_mul_f32_e32 v220, v56, v220\n" \
"v_mul_f32_e32 v221, v56, v221\n" \
"v_mul_f32_e32 v222, v56, v222\n" \
"v_mul_f32_e32 v223, v56, v223\n" \
"v_cvt_pk_bf16_f32 v76, v216, v217\n" \
"v_cvt_pk_bf16_f32 v77, v218, v219\n" \
"v_cvt_pk_bf16_f32 v78, v220, v221\n" \
"v_cvt_pk_bf16_f32 v79, v222, v223\n" \
"buffer_store_dwordx4 v[76:79], v20, s[56:59], s70 offen offset:3072 sc1\n" \
"v_add_f32_e32 v224, v224, v160\n" \
"v_add_f32_e32 v225, v225, v161\n" \
"v_add_f32_e32 v226, v226, v162\n" \
"v_add_f32_e32 v227, v227, v163\n" \
"v_add_f32_e32 v228, v228, v164\n" \
"v_add_f32_e32 v229, v229, v165\n" \
"v_add_f32_e32 v230, v230, v166\n" \
"v_add_f32_e32 v231, v231, v167\n" \
"v_mfma_f32_16x16x32_bf16 a[208:211], a[216:219], v[76:79], a[208:211]\n" \
"buffer_load_dwordx4 v5, s[84:87], s92 offen offset:1024 lds\n" \
"v_add_f32_e32 v232, v232, v168\n" \
"v_add_f32_e32 v233, v233, v169\n" \
"v_add_f32_e32 v234, v234, v170\n" \
"v_add_f32_e32 v235, v235, v171\n" \
"v_add_f32_e32 v236, v236, v172\n" \
"v_add_f32_e32 v237, v237, v173\n" \
"v_add_f32_e32 v238, v238, v174\n" \
"v_add_f32_e32 v239, v239, v175\n" \
"v_mfma_f32_16x16x32_bf16 v[252:255], a[28:31], v[124:127], v[252:255]\n" \
"buffer_load_dwordx4 v6, s[84:87], s92 offen offset:2048 lds\n" \
"v_add_f32_e32 v240, v240, v176\n" \
"v_add_f32_e32 v241, v241, v177\n" \
"v_add_f32_e32 v242, v242, v178\n" \
"v_add_f32_e32 v243, v243, v179\n" \
"v_add_f32_e32 v244, v244, v180\n" \
"v_add_f32_e32 v245, v245, v181\n" \
"v_add_f32_e32 v246, v246, v182\n" \
"v_add_f32_e32 v247, v247, v183\n" \
"v_add_f32_e32 v248, v248, v184\n" \
"v_add_f32_e32 v249, v249, v185\n" \
"v_add_f32_e32 v250, v250, v186\n" \
"v_add_f32_e32 v251, v251, v187\n" \
"v_add_f32_e32 v252, v252, v188\n" \
"v_add_f32_e32 v253, v253, v189\n" \
"v_add_f32_e32 v254, v254, v190\n" \
"v_add_f32_e32 v255, v255, v191\n" \
"v_mfma_f32_16x16x32_bf16 a[204:207], a[252:255], v[124:127], a[204:207]\n" \
"buffer_load_dwordx4 v7, s[84:87], s92 offen offset:3072 lds\n" \
"s_add_u32 m0, s93, 0x2000\n" \
"s_nop 0\n" \
"buffer_load_dwordx4 v8, s[84:87], s92 offen offset:0 lds\n" \
"s_nop 3\n" \
"v_accvgpr_read_b32 v57, a204\n" \
"v_accvgpr_read_b32 v23, a205\n" \
"v_add_f32_e32 v57, v57, v23\n" \
"v_add_f32_e32 v57, v57, v43\n" \
"v_max_f32_e32 v62, v62, v57\n" \
"v_rcp_f32_e32 v57, v57\n" \
"v_cmp_lt_f32_e64 s[72:73], s100, v62\n" \
"v_mul_f32_e32 v57, v57, v61\n" \
"v_mul_f32_e32 v224, v57, v224\n" \
"v_mul_f32_e32 v225, v57, v225\n" \
"v_mul_f32_e32 v226, v57, v226\n" \
"v_mul_f32_e32 v227, v57, v227\n" \
"v_mul_f32_e32 v228, v57, v228\n" \
"v_mul_f32_e32 v229, v57, v229\n" \
"v_mul_f32_e32 v230, v57, v230\n" \
"v_mul_f32_e32 v231, v57, v231\n" \
"v_cvt_pk_bf16_f32 v80, v224, v225\n" \
"v_cvt_pk_bf16_f32 v81, v226, v227\n" \
"v_cvt_pk_bf16_f32 v82, v228, v229\n" \
"v_cvt_pk_bf16_f32 v83, v230, v231\n" \
"buffer_store_dwordx4 v[80:83], v20, s[56:59], s71 offen offset:0 sc1\n" \
"v_mul_f32_e32 v232, v57, v232\n" \
"v_mfma_f32_16x16x32_bf16 a[208:211], a[216:219], v[80:83], a[208:211]\n" \
"v_mul_f32_e32 v233, v57, v233\n" \
"v_mul_f32_e32 v234, v57, v234\n" \
"v_mul_f32_e32 v235, v57, v235\n" \
"v_mul_f32_e32 v236, v57, v236\n" \
"v_mul_f32_e32 v237, v57, v237\n" \
"v_mul_f32_e32 v238, v57, v238\n" \
"v_mul_f32_e32 v239, v57, v239\n" \
"v_cvt_pk_bf16_f32 v84, v232, v233\n" \
"v_cvt_pk_bf16_f32 v85, v234, v235\n" \
"v_cvt_pk_bf16_f32 v86, v236, v237\n" \
"v_cvt_pk_bf16_f32 v87, v238, v239\n" \
"buffer_store_dwordx4 v[84:87], v20, s[56:59], s71 offen offset:1024 sc1\n" \
"v_mul_f32_e32 v240, v57, v240\n" \
"v_mfma_f32_16x16x32_bf16 a[208:211], a[216:219], v[84:87], a[208:211]\n" \
"v_mul_f32_e32 v241, v57, v241\n" \
"v_mul_f32_e32 v242, v57, v242\n" \
"v_mul_f32_e32 v243, v57, v243\n" \
"v_mul_f32_e32 v244, v57, v244\n" \
"v_mul_f32_e32 v245, v57, v245\n" \
"v_mul_f32_e32 v246, v57, v246\n" \
"v_mul_f32_e32 v247, v57, v247\n" \
"v_cvt_pk_bf16_f32 v96, v240, v241\n" \
"v_cvt_pk_bf16_f32 v97, v242, v243\n" \
"v_cvt_pk_bf16_f32 v98, v244, v245\n" \
"v_cvt_pk_bf16_f32 v99, v246, v247\n" \
"buffer_store_dwordx4 v[96:99], v20, s[56:59], s71 offen offset:2048 sc1\n" \
"v_mul_f32_e32 v248, v57, v248\n" \
"v_mfma_f32_16x16x32_bf16 a[208:211], a[216:219], v[96:99], a[208:211]\n" \
"v_mul_f32_e32 v249, v57, v249\n" \
"v_mul_f32_e32 v250, v57, v250\n" \
"v_mul_f32_e32 v251, v57, v251\n" \
"v_mul_f32_e32 v252, v57, v252\n" \
"v_mul_f32_e32 v253, v57, v253\n" \
"v_mul_f32_e32 v254, v57, v254\n" \
"v_mul_f32_e32 v255, v57, v255\n" \
"v_cvt_pk_bf16_f32 v100, v248, v249\n" \
"v_cvt_pk_bf16_f32 v101, v250, v251\n" \
"v_cvt_pk_bf16_f32 v102, v252, v253\n" \
"v_cvt_pk_bf16_f32 v103, v254, v255\n" \
"buffer_store_dwordx4 v[100:103], v20, s[56:59], s71 offen offset:3072 sc1\n" \
"s_nop 0\n" \
"v_mfma_f32_16x16x32_bf16 a[208:211], a[216:219], v[100:103], a[208:211]\n" \
"s_nop 7\n" \
"v_accvgpr_read_b32 v63, a208\n" \
"v_cmp_u_f32_e32 vcc, v63, v63\n" \
"s_nop 0\n" \
"s_or_b64 vcc, s[72:73], vcc\n" \
"s_cbranch_scc0 L_out_%=\n" \
"s_branch L_end_%=\n" \
"L_h1_%=:\n" \
"L_pf14:\n" \
"s_cbranch_execz L_pf18\n" \
"s_nop 0\n" \
"s_cmp_eq_u32 s63, 0x400000\n" \
"s_cbranch_scc1 L_hc1_%=\n" \
"s_cmp_lg_u32 s63, 0x0\n" \
"s_cbranch_scc1 L_hn01_%=\n" \
"s_mov_b32 exec_lo, 0xffffffff\n" \
"s_mov_b32 exec_hi, 0xf\n" \
"buffer_load_dword v22, v16, s[80:83], 0 offen\n" \
"s_mov_b64 exec, -1\n" \
"s_branch L_hr1_%=\n" \
"L_hn01_%=:\n" \
"buffer_load_dword v22, v17, s[56:59], 0 offen sc1\n" \
"s_branch L_hr1_%=\n" \
"L_hs1_%=:\n" \
"s_sleep 1\n" \
"buffer_load_dword v22, v17, s[56:59], 0 offen sc1\n" \
"s_add_u32 s101, s101, 1\n" \
"s_waitcnt vmcnt(0)\n" \
"s_cmp_gt_u32 s101, 0x100000\n" \
"s_cbranch_scc1 L_hr1_%=\n" \
"L_hc1_%=:\n" \
"v_cmp_ne_u32_e32 vcc, s62, v22\n" \
"s_nop 4\n" \
"s_cbranch_vccnz L_hs1_%=\n" \
"s_branch L_hr1_%=\n" \
"L_h2_%=:\n" \
"s_cmp_eq_u32 s63, 0x400000\n" \
"s_cbranch_scc1 L_hc2_%=\n" \
"s_cmp_lg_u32 s63, 0x0\n" \
"s_cbranch_scc1 L_hn02_%=\n" \
"s_mov_b32 exec_lo, 0xffffffff\n" \
"s_mov_b32 exec_hi, 0xf\n" \
"buffer_load_dword v22, v16, s[80:83], 0 offen\n" \
"s_mov_b64 exec, -1\n" \
"s_branch L_hr2_%=\n" \
"L_hn02_%=:\n" \
"buffer_load_dword v22, v17, s[56:59], 0 offen sc1\n" \
"s_branch L_hr2_%=\n" \
"L_hs2_%=:\n" \
"s_sleep 1\n" \
"buffer_load_dword v22, v17, s[56:59], 0 offen sc1\n" \
"s_add_u32 s101, s101, 1\n" \
"s_waitcnt vmcnt(0)\n" \
"s_cmp_gt_u32 s101, 0x100000\n" \
"s_cbranch_scc1 L_hr2_%=\n" \
"L_hc2_%=:\n" \
"v_cmp_ne_u32_e32 vcc, s62, v22\n" \
"s_nop 4\n" \
"s_cbranch_vccnz L_hs2_%=\n" \
"s_branch L_hr2_%=\n" \
"L_sa_%=:\n" \
"s_sleep 1\n" \
"buffer_load_dword v37, v38, s[56:59], s64 offen sc1\n" \
"s_add_u32 s101, s101, 1\n" \
"s_waitcnt vmcnt(0)\n" \
"v_cmp_ne_u32_e32 vcc, s62, v37\n" \
"s_cmp_gt_u32 s101, 0x100000\n" \
"s_cbranch_scc1 L_ra_%=\n" \
"s_nop 2\n" \
"s_cbranch_vccnz L_sa_%=\n" \
"s_branch L_ra_%=\n" \
"L_sb_%=:\n" \
"s_sleep 1\n" \
"buffer_load_dword v47, v38, s[56:59], s71 offen sc1\n" \
"s_add_u32 s101, s101, 1\n" \
"s_waitcnt vmcnt(0)\n" \
"v_cmp_ne_u32_e32 vcc, s62, v47\n" \
"s_cmp_gt_u32 s101, 0x100000\n" \
"s_cbranch_scc1 L_rb_%=\n" \
"s_nop 2\n" \
"s_cbranch_vccnz L_sb_%=\n" \
"s_branch L_rb_%=\n" \
"L_sx0_%=:\n" \
"s_sleep 1\n" \
"buffer_load_dword v37, v40, s[56:59], s74 offen sc1\n" \
"s_add_u32 s101, s101, 1\n" \
"s_waitcnt vmcnt(0)\n" \
"v_cmp_eq_u32_e64 s[98:99], s62, v37\n" \
"s_cmp_gt_u32 s101, 0x100000\n" \
"s_cbranch_scc1 L_rx0_%=\n" \
"s_andn2_b32 s75, 0x1, s98\n" \
"s_cbranch_scc1 L_sx0_%=\n" \
"s_branch L_rx0_%=\n" \
"L_sx1_%=:\n" \
"s_sleep 1\n" \
"buffer_load_dword v37, v40, s[56:59], s74 offen sc1\n" \
"s_add_u32 s101, s101, 1\n" \
"s_waitcnt vmcnt(0)\n" \
"v_cmp_eq_u32_e64 s[98:99], s62, v37\n" \
"s_cmp_gt_u32 s101, 0x100000\n" \
"s_cbranch_scc1 L_rx1_%=\n" \
"s_andn2_b32 s75, 0x2, s98\n" \
"s_cbranch_scc1 L_sx1_%=\n" \
"s_branch L_rx1_%=\n" \
"L_sx2_%=:\n" \
"s_sleep 1\n" \
"buffer_load_dword v37, v40, s[56:59], s74 offen sc1\n" \
"s_add_u32 s101, s101, 1\n" \
"s_waitcnt vmcnt(0)\n" \
"v_cmp_eq_u32_e64 s[98:99], s62, v37\n" \
"s_cmp_gt_u32 s101, 0x100000\n" \
"s_cbranch_scc1 L_rx2_%=\n" \
"s_andn2_b32 s75, 0x4, s98\n" \
"s_cbranch_scc1 L_sx2_%=\n" \
"s_branch L_rx2_%=\n" \
"L_sx3_%=:\n" \
"s_sleep 1\n" \
"buffer_load_dword v37, v40, s[56:59], s74 offen sc1\n" \
"s_add_u32 s101, s101, 1\n" \
"s_waitcnt vmcnt(0)\n" \
"v_cmp_eq_u32_e64 s[98:99], s62, v37\n" \
"s_cmp_gt_u32 s101, 0x100000\n" \
"s_cbranch_scc1 L_rx3_%=\n" \
"s_andn2_b32 s75, s100, s98\n" \
"s_cbranch_scc1 L_sx3_%=\n" \
"s_branch L_rx3_%=\n" \
"L_sx4_%=:\n" \
"s_sleep 1\n" \
"buffer_load_dword v37, v40, s[56:59], s74 offen sc1\n" \
"s_add_u32 s101, s101, 1\n" \
"s_waitcnt vmcnt(0)\n" \
"v_cmp_eq_u32_e64 s[98:99], s62, v37\n" \
"s_cmp_gt_u32 s101, 0x100000\n" \
"s_cbranch_scc1 L_rx4_%=\n" \
"s_andn2_b32 s75, 0x10, s98\n" \
"s_cbranch_scc1 L_sx4_%=\n" \
"s_branch L_rx4_%=\n" \
"L_sx5_%=:\n" \
"s_sleep 1\n" \
"buffer_load_dword v37, v40, s[56:59], s74 offen sc1\n" \
"s_add_u32 s101, s101, 1\n" \
"s_waitcnt vmcnt(0)\n" \
"v_cmp_eq_u32_e64 s[98:99], s62, v37\n" \
"s_cmp_gt_u32 s101, 0x100000\n" \
"s_cbranch_scc1 L_rx5_%=\n" \
"s_andn2_b32 s75, 0x20, s98\n" \
"s_cbranch_scc1 L_sx5_%=\n" \
"s_branch L_rx5_%=\n" \
"L_sx6_%=:\n" \
"s_sleep 1\n" \
"buffer_load_dword v37, v40, s[56:59], s74 offen sc1\n" \
"s_add_u32 s101, s101, 1\n" \
"s_waitcnt vmcnt(0)\n" \
"v_cmp_eq_u32_e64 s[98:99], s62, v37\n" \
"s_cmp_gt_u32 s101, 0x100000\n" \
"s_cbranch_scc1 L_rx6_%=\n" \
"s_andn2_b32 s75, 0x40, s98\n" \
"s_cbranch_scc1 L_sx6_%=\n" \
"s_branch L_rx6_%=\n" \
"L_sx7_%=:\n" \
"s_sleep 1\n" \
"buffer_load_dword v37, v40, s[56:59], s74 offen sc1\n" \
"s_add_u32 s101, s101, 1\n" \
"s_waitcnt vmcnt(0)\n" \
"v_cmp_eq_u32_e64 s[98:99], s62, v37\n" \
"s_cmp_gt_u32 s101, 0x100000\n" \
"s_cbranch_scc1 L_rx7_%=\n" \
"s_andn2_b32 s75, 0x80, s98\n" \
"s_cbranch_scc1 L_sx7_%=\n" \
"s_branch L_rx7_%=\n" \
"L_sx8_%=:\n" \
"s_sleep 1\n" \
"buffer_load_dword v37, v40, s[56:59], s74 offen sc1\n" \
"s_add_u32 s101, s101, 1\n" \
"s_waitcnt vmcnt(0)\n" \
"v_cmp_eq_u32_e64 s[98:99], s62, v37\n" \
"s_cmp_gt_u32 s101, 0x100000\n" \
"s_cbranch_scc1 L_rx8_%=\n" \
"s_andn2_b32 s75, 0x100, s98\n" \
"s_cbranch_scc1 L_sx8_%=\n" \
"s_branch L_rx8_%=\n" \
"L_sx9_%=:\n" \
"s_sleep 1\n" \
"buffer_load_dword v37, v40, s[56:59], s74 offen sc1\n" \
"s_add_u32 s101, s101, 1\n" \
"s_waitcnt vmcnt(0)\n" \
"v_cmp_eq_u32_e64 s[98:99], s62, v37\n" \
"s_cmp_gt_u32 s101, 0x100000\n" \
"s_cbranch_scc1 L_rx9_%=\n" \
"s_andn2_b32 s75, 0x200, s98\n" \
"s_cbranch_scc1 L_sx9_%=\n" \
"s_branch L_rx9_%=\n" \
"L_sx10_%=:\n" \
"s_sleep 1\n" \
"buffer_load_dword v37, v40, s[56:59], s74 offen sc1\n" \
"s_add_u32 s101, s101, 1\n" \
"s_waitcnt vmcnt(0)\n" \
"v_cmp_eq_u32_e64 s[98:99], s62, v37\n" \
"s_cmp_gt_u32 s101, 0x100000\n" \
"s_cbranch_scc1 L_rx10_%=\n" \
"s_andn2_b32 s75, 0x400, s98\n" \
"s_cbranch_scc1 L_sx10_%=\n" \
"s_branch L_rx10_%=\n" \
"L_att_stub:\n" \
"s_mov_b32 s101, 0\n" \
"v_add_u32_e32 v32, 0x49000, v20\n" \
"v_readlane_b32 s96, v11, 32\n" \
"v_readlane_b32 s97, v11, 33\n" \
"buffer_load_dwordx4 a[220:223], v20, s[56:59], s60 offen offset:0 sc1\n" \
"buffer_load_dwordx4 a[236:239], v32, s[56:59], s60 offen offset:0 sc1\n" \
"buffer_load_dwordx4 a[224:227], v20, s[56:59], s60 offen offset:1024 sc1\n" \
"buffer_load_dwordx4 a[240:243], v32, s[56:59], s60 offen offset:1024 sc1\n" \
"buffer_load_dwordx4 a[228:231], v20, s[56:59], s60 offen offset:2048 sc1\n" \
"buffer_load_dwordx4 a[244:247], v32, s[56:59], s60 offen offset:2048 sc1\n" \
"buffer_load_dwordx4 a[232:235], v20, s[56:59], s60 offen offset:3072 sc1\n" \
"buffer_load_dwordx4 a[248:251], v32, s[56:59], s60 offen offset:3072 sc1\n" \
"buffer_load_dwordx4 a[32:35], v20, s[56:59], s96 offen offset:0 sc1\n" \
"buffer_load_dwordx4 a[40:43], v32, s[56:59], s96 offen offset:0 sc1\n" \
"buffer_load_dwordx4 a[48:51], v20, s[56:59], s97 offen offset:0 sc1\n" \
"buffer_load_dwordx4 a[56:59], v32, s[56:59], s97 offen offset:0 sc1\n" \
"buffer_load_dwordx4 a[36:39], v20, s[56:59], s96 offen offset:1024 sc1\n" \
"buffer_load_dwordx4 a[44:47], v32, s[56:59], s96 offen offset:1024 sc1\n" \
"buffer_load_dwordx4 a[52:55], v20, s[56:59], s97 offen offset:1024 sc1\n" \
"buffer_load_dwordx4 a[60:63], v32, s[56:59], s97 offen offset:1024 sc1\n" \
"buffer_load_dwordx4 v[64:67], v20, s[56:59], s61 offen offset:0 sc1\n" \
"buffer_load_dwordx4 v[68:71], v20, s[56:59], s61 offen offset:1024 sc1\n" \
"buffer_load_dwordx4 v[72:75], v20, s[56:59], s61 offen offset:2048 sc1\n" \
"buffer_load_dwordx4 v[76:79], v20, s[56:59], s61 offen offset:3072 sc1\n" \
"buffer_load_dwordx4 v[80:83], v32, s[56:59], s61 offen offset:0 sc1\n" \
"buffer_load_dwordx4 v[84:87], v32, s[56:59], s61 offen offset:1024 sc1\n" \
"buffer_load_dwordx4 v[96:99], v32, s[56:59], s61 offen offset:2048 sc1\n" \
"buffer_load_dwordx4 v[100:103], v32, s[56:59], s61 offen offset:3072 sc1\n" \
"s_waitcnt vmcnt(0)\n" \
"s_branch L_out_%=\n" \
"L_sw_%=:\n" \
"L_pf15:\n" \
"s_cbranch_execz L_pf19\n" \
"s_nop 0\n" \
"v_mfma_f32_16x16x32_bf16 v[72:75], a[128:131], a[16:19], v[36:39]\n" \
"s_add_u32 s68, s60, 0xfff6e000\n" \
"s_add_u32 s69, s60, 0xfffb7000\n" \
"s_add_u32 s70, s60, 0x0\n" \
"s_add_u32 s71, s60, 0x49000\n" \
"v_mfma_f32_16x16x32_bf16 v[72:75], a[132:135], a[20:23], v[72:75]\n" \
"s_xor_b32 m0, s65, 0x12000\n" \
"ds_read_b128 a[160:163], v49 offset:0\n" \
"v_mfma_f32_16x16x32_bf16 v[72:75], a[136:139], a[24:27], v[72:75]\n" \
"buffer_load_dwordx4 v50, s[8:11], s64 offen offset:0 sc1 lds\n" \
"v_mfma_f32_16x16x32_bf16 v[72:75], a[140:143], a[28:31], v[72:75]\n" \
"ds_read_b128 a[164:167], v49 offset:1024\n" \
"ds_read_b128 a[168:171], v49 offset:2048\n" \
"v_mfma_f32_16x16x32_bf16 v[76:79], a[156:159], a[28:31], v[36:39]\n" \
"ds_read_b128 a[172:175], v49 offset:3072\n" \
"v_cvt_pk_bf16_f32 v120, v88, v89\n" \
"v_mfma_f32_16x16x32_bf16 v[76:79], a[152:155], a[24:27], v[76:79]\n" \
"buffer_load_dwordx4 v50, s[8:11], s64 offen offset:1024 sc1 lds\n" \
"v_mfma_f32_16x16x32_bf16 v[76:79], a[148:151], a[20:23], v[76:79]\n" \
"ds_read_b128 a[176:179], v49 offset:4096\n" \
"v_cvt_pk_bf16_f32 v121, v90, v91\n" \
"v_mfma_f32_16x16x32_bf16 v[76:79], a[144:147], a[16:19], v[76:79]\n" \
"ds_read_b128 a[180:183], v49 offset:5120\n" \
"s_mov_b32 s65, s13\n" \
"v_mfma_f32_16x16x32_bf16 v[80:83], a[128:131], a[32:35], v[40:43]\n" \
"v_add_u32_e32 v63, s15, v20\n" \
"s_add_u32 s67, s14, 40960\n" \
"v_mfma_f32_16x16x32_bf16 v[80:83], a[132:135], a[36:39], v[80:83]\n" \
"v_add_u32_e32 v50, 0x6000, v63\n" \
"s_xor_b32 m0, s65, 0x8000\n" \
"v_mfma_f32_16x16x32_bf16 v[80:83], a[136:139], a[40:43], v[80:83]\n" \
"v_exp_f32_e32 v92, v92\n" \
"v_mfma_f32_16x16x32_bf16 v[80:83], a[140:143], a[44:47], v[80:83]\n" \
"buffer_load_dwordx4 v50, s[8:11], s67 offen offset:0 sc1 lds\n" \
"v_mfma_f32_16x16x32_bf16 v[84:87], a[156:159], a[44:47], v[40:43]\n" \
"v_exp_f32_e32 v93, v93\n" \
"v_mfma_f32_16x16x32_bf16 v[84:87], a[152:155], a[40:43], v[84:87]\n" \
"v_exp_f32_e32 v94, v94\n" \
"v_mfma_f32_16x16x32_bf16 v[84:87], a[148:151], a[36:39], v[84:87]\n" \
"v_exp_f32_e32 v95, v95\n" \
"v_mfma_f32_16x16x32_bf16 v[84:87], a[144:147], a[32:35], v[84:87]\n" \
"v_cvt_pk_bf16_f32 v122, v92, v93\n" \
"v_cvt_pk_bf16_f32 v123, v94, v95\n" \
"v_mfma_f32_16x16x32_bf16 v[88:91], a[128:131], a[48:51], v[44:47]\n" \
"ds_read_b128 a[184:187], v49 offset:6144\n" \
"ds_read_b128 a[188:191], v49 offset:7168\n" \
"v_mfma_f32_16x16x32_bf16 v[88:91], a[132:135], a[52:55], v[88:91]\n" \
"v_and_b32_e32 v96, v12, v96\n" \
"v_and_b32_e32 v97, v13, v97\n" \
"v_mfma_f32_16x16x32_bf16 v[88:91], a[136:139], a[56:59], v[88:91]\n" \
"v_and_b32_e32 v98, v14, v98\n" \
"v_and_b32_e32 v99, v15, v99\n" \
"v_mfma_f32_16x16x32_bf16 v[88:91], a[140:143], a[60:63], v[88:91]\n" \
"buffer_load_dwordx4 v50, s[8:11], s67 offen offset:1024 sc1 lds\n" \
"v_mfma_f32_16x16x32_bf16 v[92:95], a[156:159], a[60:63], v[44:47]\n" \
"v_exp_f32_e32 v72, v72\n" \
"v_mfma_f32_16x16x32_bf16 v[92:95], a[152:155], a[56:59], v[92:95]\n" \
"v_exp_f32_e32 v73, v73\n" \
"v_mfma_f32_16x16x32_bf16 v[92:95], a[148:151], a[52:55], v[92:95]\n" \
"v_exp_f32_e32 v74, v74\n" \
"v_mfma_f32_16x16x32_bf16 v[92:95], a[144:147], a[48:51], v[92:95]\n" \
"v_cvt_pk_bf16_f32 v108, v72, v73\n" \
"s_waitcnt lgkmcnt(2)\n" \
"v_mfma_f32_16x16x32_bf16 v[128:131], a[160:163], v[96:99], v[128:131]\n" \
"v_exp_f32_e32 v75, v75\n" \
"v_mfma_f32_16x16x32_bf16 v[132:135], a[164:167], v[96:99], v[132:135]\n" \
"v_exp_f32_e32 v76, v76\n" \
"v_mfma_f32_16x16x32_bf16 v[136:139], a[168:171], v[96:99], v[136:139]\n" \
"v_exp_f32_e32 v77, v77\n" \
"v_mfma_f32_16x16x32_bf16 v[140:143], a[172:175], v[96:99], v[140:143]\n" \
"v_exp_f32_e32 v78, v78\n" \
"v_mfma_f32_16x16x32_bf16 a[192:195], a[252:255], v[96:99], a[192:195]\n" \
"v_exp_f32_e32 v79, v79\n" \
"v_mfma_f32_16x16x32_bf16 v[160:163], a[160:163], v[104:107], v[160:163]\n" \
"v_cvt_pk_bf16_f32 v109, v74, v75\n" \
"v_cvt_pk_bf16_f32 v110, v76, v77\n" \
"v_cvt_pk_bf16_f32 v111, v78, v79\n" \
"v_and_b32_e32 v108, v12, v108\n" \
"v_and_b32_e32 v109, v13, v109\n" \
"v_and_b32_e32 v110, v14, v110\n" \
"v_and_b32_e32 v111, v15, v111\n" \
"v_exp_f32_e32 v80, v80\n" \
"v_exp_f32_e32 v81, v81\n" \
"v_mfma_f32_16x16x32_bf16 v[160:163], a[176:179], v[108:111], v[160:163]\n" \
"buffer_load_dwordx4 v50, s[8:11], s67 offen offset:2048 sc1 lds\n" \
"v_mfma_f32_16x16x32_bf16 v[164:167], a[180:183], v[108:111], v[164:167]\n" \
"buffer_load_dwordx4 v50, s[8:11], s67 offen offset:3072 sc1 lds\n" \
"v_mfma_f32_16x16x32_bf16 v[164:167], a[164:167], v[104:107], v[164:167]\n" \
"v_exp_f32_e32 v82, v82\n" \
"v_mfma_f32_16x16x32_bf16 v[168:171], a[168:171], v[104:107], v[168:171]\n" \
"v_cvt_pk_bf16_f32 v116, v80, v81\n" \
"s_waitcnt lgkmcnt(0)\n" \
"v_mfma_f32_16x16x32_bf16 v[168:171], a[184:187], v[108:111], v[168:171]\n" \
"v_exp_f32_e32 v83, v83\n" \
"v_mfma_f32_16x16x32_bf16 v[172:175], a[188:191], v[108:111], v[172:175]\n" \
"v_exp_f32_e32 v84, v84\n" \
"v_mfma_f32_16x16x32_bf16 v[172:175], a[172:175], v[104:107], v[172:175]\n" \
"v_exp_f32_e32 v85, v85\n" \
"v_smfmac_f32_16x16x64_bf16 a[196:199], a[212:215], v[104:111], v21\n" \
"v_mfma_f32_16x16x32_bf16 v[192:195], a[160:163], v[112:115], v[192:195]\n" \
"v_exp_f32_e32 v86, v86\n" \
"v_exp_f32_e32 v87, v87\n" \
"v_cvt_pk_bf16_f32 v117, v82, v83\n" \
"v_cvt_pk_bf16_f32 v118, v84, v85\n" \
"v_cvt_pk_bf16_f32 v119, v86, v87\n" \
"s_mov_b32 s64, s14\n" \
"v_add_u32_e32 v51, 0x12000, v63\n" \
"v_mfma_f32_16x16x32_bf16 v[192:195], a[176:179], v[116:119], v[192:195]\n" \
"s_xor_b32 m0, s65, 0xa000\n" \
"ds_read_b32 v58, v18 offset:512\n" \
"v_mfma_f32_16x16x32_bf16 v[196:199], a[180:183], v[116:119], v[196:199]\n" \
"buffer_load_dwordx4 v51, s[8:11], s64 offen offset:0 sc1 lds\n" \
"v_mfma_f32_16x16x32_bf16 v[196:199], a[164:167], v[112:115], v[196:199]\n" \
"v_exp_f32_e32 v88, v88\n" \
"v_mfma_f32_16x16x32_bf16 v[200:203], a[168:171], v[112:115], v[200:203]\n" \
"v_exp_f32_e32 v89, v89\n" \
"v_mfma_f32_16x16x32_bf16 v[200:203], a[184:187], v[116:119], v[200:203]\n" \
"v_exp_f32_e32 v90, v90\n" \
"v_mfma_f32_16x16x32_bf16 v[204:207], a[188:191], v[116:119], v[204:207]\n" \
"v_exp_f32_e32 v91, v91\n" \
"v_mfma_f32_16x16x32_bf16 v[204:207], a[172:175], v[112:115], v[204:207]\n" \
"v_exp_f32_e32 v92, v92\n" \
"v_smfmac_f32_16x16x64_bf16 a[200:203], a[212:215], v[112:119], v21\n" \
"v_mfma_f32_16x16x32_bf16 v[224:227], a[160:163], v[120:123], v[224:227]\n" \
"v_exp_f32_e32 v93, v93\n" \
"v_exp_f32_e32 v94, v94\n" \
"v_exp_f32_e32 v95, v95\n" \
"v_cvt_pk_bf16_f32 v124, v88, v89\n" \
"v_cvt_pk_bf16_f32 v125, v90, v91\n" \
"v_cvt_pk_bf16_f32 v126, v92, v93\n" \
"v_cvt_pk_bf16_f32 v127, v94, v95\n" \
"v_add_u32_e32 v52, 0x14000, v63\n" \
"v_add_u32_e32 v53, 0x20000, v63\n" \
"v_mfma_f32_16x16x32_bf16 v[224:227], a[176:179], v[124:127], v[224:227]\n" \
"buffer_load_dwordx4 v51, s[8:11], s64 offen offset:1024 sc1 lds\n" \
"v_mfma_f32_16x16x32_bf16 v[228:231], a[180:183], v[124:127], v[228:231]\n" \
"ds_read_b32 v59, v18 offset:640\n" \
"ds_read_b32 v60, v18 offset:768\n" \
"v_mfma_f32_16x16x32_bf16 v[228:231], a[164:167], v[120:123], v[228:231]\n" \
"ds_read_b32 v61, v18 offset:896\n" \
"ds_read_b128 a[128:131], v48 offset:0\n" \
"v_mfma_f32_16x16x32_bf16 v[232:235], a[168:171], v[120:123], v[232:235]\n" \
"ds_read_b128 a[132:135], v48 offset:1024\n" \
"ds_read_b128 a[136:139], v48 offset:2048\n" \
"v_mfma_f32_16x16x32_bf16 v[232:235], a[184:187], v[124:127], v[232:235]\n" \
"ds_read_b128 a[140:143], v48 offset:3072\n" \
"ds_read_b128 a[144:147], v48 offset:4096\n" \
"ds_read_b128 a[148:151], v48 offset:5120\n" \
"ds_read_b128 a[152:155], v48 offset:6144\n" \
"ds_read_b128 a[156:159], v48 offset:7168\n" \
"v_mfma_f32_16x16x32_bf16 v[236:239], a[188:191], v[124:127], v[236:239]\n" \
"buffer_load_dwordx4 v51, s[8:11], s64 offen offset:2048 sc1 lds\n" \
"v_mfma_f32_16x16x32_bf16 v[236:239], a[172:175], v[120:123], v[236:239]\n" \
"buffer_load_dwordx4 v51, s[8:11], s64 offen offset:3072 sc1 lds\n" \
"v_readlane_b32 s93, v11, 17\n" \
"v_readlane_b32 s92, v11, 22\n" \
"v_accvgpr_read_b32 v54, a192\n" \
"v_accvgpr_read_b32 v23, a193\n" \
"v_add_f32_e32 v54, v54, v23\n" \
"v_accvgpr_read_b32 v55, a196\n" \
"v_accvgpr_read_b32 v23, a197\n" \
"v_max_f32_e32 v62, v62, v54\n" \
"v_add_f32_e32 v55, v55, v23\n" \
"v_max_f32_e32 v62, v62, v55\n" \
"v_rcp_f32_e32 v54, v54\n" \
"v_rcp_f32_e32 v55, v55\n" \
"s_waitcnt lgkmcnt(11)\n" \
"v_mul_f32_e32 v54, v54, v58\n" \
"s_waitcnt lgkmcnt(10)\n" \
"v_mul_f32_e32 v55, v55, v59\n" \
"v_mul_f32_e32 v128, v54, v128\n" \
"v_mul_f32_e32 v129, v54, v129\n" \
"v_mul_f32_e32 v136, v54, v136\n" \
"v_mul_f32_e32 v137, v54, v137\n" \
"v_mul_f32_e32 v160, v55, v160\n" \
"v_mul_f32_e32 v161, v55, v161\n" \
"v_mul_f32_e32 v168, v55, v168\n" \
"v_mul_f32_e32 v169, v55, v169\n" \
"v_cvt_pk_bf16_f32 v128, v128, v129\n" \
"v_mul_f32_e32 v130, v54, v130\n" \
"v_mul_f32_e32 v131, v54, v131\n" \
"v_cvt_pk_bf16_f32 v136, v136, v137\n" \
"v_mul_f32_e32 v138, v54, v138\n" \
"v_mul_f32_e32 v139, v54, v139\n" \
"s_waitcnt vmcnt(10) lgkmcnt(0)\n" \
"s_barrier\n" \
"v_mfma_f32_16x16x32_bf16 v[80:83], a[128:131], a[32:35], v[40:43]\n" \
"ds_read_b128 a[160:163], v49 offset:8192\n" \
"ds_read_b128 a[164:167], v49 offset:9216\n" \
"v_mfma_f32_16x16x32_bf16 v[80:83], a[132:135], a[36:39], v[80:83]\n" \
"ds_read_b128 a[168:171], v49 offset:10240\n" \
"ds_read_b128 a[172:175], v49 offset:11264\n" \
"v_mfma_f32_16x16x32_bf16 v[80:83], a[136:139], a[40:43], v[80:83]\n" \
"s_xor_b32 m0, s65, 0x18000\n" \
"ds_read_b128 a[176:179], v49 offset:12288\n" \
"v_mfma_f32_16x16x32_bf16 v[80:83], a[140:143], a[44:47], v[80:83]\n" \
"buffer_load_dwordx4 v52, s[8:11], s64 offen offset:0 sc1 lds\n" \
"v_mfma_f32_16x16x32_bf16 v[84:87], a[156:159], a[44:47], v[40:43]\n" \
"ds_read_b128 a[180:183], v49 offset:13312\n" \
"ds_read_b128 a[184:187], v49 offset:14336\n" \
"v_mfma_f32_16x16x32_bf16 v[84:87], a[152:155], a[40:43], v[84:87]\n" \
"buffer_load_dwordx4 v52, s[8:11], s64 offen offset:1024 sc1 lds\n" \
"v_mfma_f32_16x16x32_bf16 v[84:87], a[148:151], a[36:39], v[84:87]\n" \
"ds_read_b128 a[188:191], v49 offset:15360\n" \
"v_add_u32_e32 v49, 0x10000, v48\n" \
"v_mfma_f32_16x16x32_bf16 v[84:87], a[144:147], a[32:35], v[84:87]\n" \
"buffer_load_dwordx4 v52, s[8:11], s64 offen offset:2048 sc1 lds\n" \
"v_mfma_f32_16x16x32_bf16 v[88:91], a[128:131], a[48:51], v[44:47]\n" \
"ds_read_b128 a[128:131], v48 offset:8192\n" \
"v_cvt_pk_bf16_f32 v160, v160, v161\n" \
"v_mfma_f32_16x16x32_bf16 v[88:91], a[132:135], a[52:55], v[88:91]\n" \
"ds_read_b128 a[132:135], v48 offset:9216\n" \
"v_mul_f32_e32 v162, v55, v162\n" \
"v_mfma_f32_16x16x32_bf16 v[88:91], a[136:139], a[56:59], v[88:91]\n" \
"buffer_load_dwordx4 v52, s[8:11], s64 offen offset:3072 sc1 lds\n" \
"v_mfma_f32_16x16x32_bf16 v[88:91], a[140:143], a[60:63], v[88:91]\n" \
"s_mov_b32 m0, s65\n" \
"ds_read_b128 a[136:139], v48 offset:10240\n" \
"v_mfma_f32_16x16x32_bf16 v[92:95], a[156:159], a[60:63], v[44:47]\n" \
"ds_read_b128 a[140:143], v48 offset:11264\n" \
"ds_read_b128 a[156:159], v48 offset:15360\n" \
"v_mfma_f32_16x16x32_bf16 v[92:95], a[152:155], a[56:59], v[92:95]\n" \
"buffer_load_dwordx4 v53, s[8:11], s64 offen offset:0 sc1 lds\n" \
"v_mfma_f32_16x16x32_bf16 v[92:95], a[148:151], a[52:55], v[92:95]\n" \
"ds_read_b128 a[148:151], v48 offset:13312\n" \
"ds_read_b128 a[152:155], v48 offset:14336\n" \
"v_mfma_f32_16x16x32_bf16 v[92:95], a[144:147], a[48:51], v[92:95]\n" \
"ds_read_b128 a[144:147], v48 offset:12288\n" \
"s_waitcnt lgkmcnt(12)\n" \
"v_mfma_f32_16x16x32_bf16 v[144:147], a[160:163], v[96:99], v[144:147]\n" \
"buffer_load_dwordx4 v53, s[8:11], s64 offen offset:1024 sc1 lds\n" \
"v_mfma_f32_16x16x32_bf16 v[148:151], a[164:167], v[96:99], v[148:151]\n" \
"v_exp_f32_e32 v88, v88\n" \
"v_mfma_f32_16x16x32_bf16 v[152:155], a[168:171], v[96:99], v[152:155]\n" \
"v_exp_f32_e32 v89, v89\n" \
"v_mfma_f32_16x16x32_bf16 v[156:159], a[172:175], v[96:99], v[156:159]\n" \
"v_exp_f32_e32 v90, v90\n" \
"v_mfma_f32_16x16x32_bf16 v[176:179], a[160:163], v[104:107], v[176:179]\n" \
"s_xor_b32 s65, s65, 0x8000\n" \
"s_waitcnt lgkmcnt(8)\n" \
"v_mfma_f32_16x16x32_bf16 v[176:179], a[176:179], v[108:111], v[176:179]\n" \
"buffer_load_dwordx4 v53, s[8:11], s64 offen offset:2048 sc1 lds\n" \
"v_mfma_f32_16x16x32_bf16 v[180:183], a[180:183], v[108:111], v[180:183]\n" \
"v_exp_f32_e32 v91, v91\n" \
"v_mfma_f32_16x16x32_bf16 v[180:183], a[164:167], v[104:107], v[180:183]\n" \
"buffer_load_dwordx4 v53, s[8:11], s64 offen offset:3072 sc1 lds\n" \
"v_mfma_f32_16x16x32_bf16 v[184:187], a[168:171], v[104:107], v[184:187]\n" \
"s_add_u32 m0, s93, 0x0\n" \
"s_add_u32 s64, s64, 0x10000\n" \
"L_pf16:\n" \
"s_cbranch_execz L_retA\n" \
"s_nop 0\n" \
"v_mfma_f32_16x16x32_bf16 v[184:187], a[184:187], v[108:111], v[184:187]\n" \
"v_exp_f32_e32 v92, v92\n" \
"v_mfma_f32_16x16x32_bf16 v[188:191], a[188:191], v[108:111], v[188:191]\n" \
"buffer_load_dwordx4 v0, s[80:83], s92 offen offset:0 lds\n" \
"v_mfma_f32_16x16x32_bf16 v[188:191], a[172:175], v[104:107], v[188:191]\n" \
"v_exp_f32_e32 v93, v93\n" \
"v_mfma_f32_16x16x32_bf16 v[208:211], a[160:163], v[112:115], v[208:211]\n" \
"v_exp_f32_e32 v94, v94\n" \
"v_mfma_f32_16x16x32_bf16 v[208:211], a[176:179], v[116:119], v[208:211]\n" \
"v_exp_f32_e32 v95, v95\n" \
"v_mfma_f32_16x16x32_bf16 v[212:215], a[180:183], v[116:119], v[212:215]\n" \
"buffer_load_dwordx4 v1, s[80:83], s92 offen offset:1024 lds\n" \
"v_mfma_f32_16x16x32_bf16 v[212:215], a[164:167], v[112:115], v[212:215]\n" \
"v_exp_f32_e32 v80, v80\n" \
"v_mfma_f32_16x16x32_bf16 v[216:219], a[168:171], v[112:115], v[216:219]\n" \
"v_exp_f32_e32 v81, v81\n" \
"v_mfma_f32_16x16x32_bf16 v[216:219], a[184:187], v[116:119], v[216:219]\n" \
"buffer_load_dwordx4 v2, s[80:83], s92 offen offset:2048 lds\n" \
"v_mfma_f32_16x16x32_bf16 v[220:223], a[188:191], v[116:119], v[220:223]\n" \
"v_exp_f32_e32 v82, v82\n" \
"v_mfma_f32_16x16x32_bf16 v[220:223], a[172:175], v[112:115], v[220:223]\n" \
"v_exp_f32_e32 v83, v83\n" \
"v_mfma_f32_16x16x32_bf16 v[240:243], a[160:163], v[120:123], v[240:243]\n" \
"buffer_load_dwordx4 v3, s[80:83], s92 offen offset:3072 lds\n" \
"v_mfma_f32_16x16x32_bf16 v[240:243], a[176:179], v[124:127], v[240:243]\n" \
"v_exp_f32_e32 v84, v84\n" \
"v_mfma_f32_16x16x32_bf16 v[244:247], a[180:183], v[124:127], v[244:247]\n" \
"v_exp_f32_e32 v85, v85\n" \
"v_mfma_f32_16x16x32_bf16 v[244:247], a[164:167], v[120:123], v[244:247]\n" \
"v_exp_f32_e32 v86, v86\n" \
"v_mfma_f32_16x16x32_bf16 v[248:251], a[168:171], v[120:123], v[248:251]\n" \
"v_exp_f32_e32 v87, v87\n" \
"v_mfma_f32_16x16x32_bf16 v[248:251], a[184:187], v[124:127], v[248:251]\n" \
"v_cvt_pk_bf16_f32 v112, v80, v81\n" \
"v_cvt_pk_bf16_f32 v113, v82, v83\n" \
"v_mfma_f32_16x16x32_bf16 v[252:255], a[188:191], v[124:127], v[252:255]\n" \
"v_cvt_pk_bf16_f32 v114, v84, v85\n" \
"v_cvt_pk_bf16_f32 v115, v86, v87\n" \
"v_mfma_f32_16x16x32_bf16 v[252:255], a[172:175], v[120:123], v[252:255]\n" \
"v_and_b32_e32 v112, v12, v112\n" \
"v_and_b32_e32 v113, v13, v113\n" \
"v_smfmac_f32_16x16x64_bf16 a[204:207], a[212:215], v[120:127], v21\n" \
"v_cvt_pk_bf16_f32 v120, v88, v89\n" \
"v_cvt_pk_bf16_f32 v121, v90, v91\n" \
"v_cvt_pk_bf16_f32 v122, v92, v93\n" \
"v_cvt_pk_bf16_f32 v123, v94, v95\n" \
"v_and_b32_e32 v114, v14, v114\n" \
"v_and_b32_e32 v115, v15, v115\n" \
"v_add_u32_e32 v48, s16, v20\n" \
"v_mul_f32_e32 v144, v54, v144\n" \
"v_mul_f32_e32 v145, v54, v145\n" \
"v_mul_f32_e32 v152, v54, v152\n" \
"v_mul_f32_e32 v153, v54, v153\n" \
"v_mul_f32_e32 v176, v55, v176\n" \
"v_mul_f32_e32 v177, v55, v177\n" \
"v_mul_f32_e32 v184, v55, v184\n" \
"v_mul_f32_e32 v185, v55, v185\n" \
"v_mul_f32_e32 v163, v55, v163\n" \
"v_cvt_pk_bf16_f32 v168, v168, v169\n" \
"v_mul_f32_e32 v170, v55, v170\n" \
"v_mul_f32_e32 v171, v55, v171\n" \
"v_cvt_pk_bf16_f32 v144, v144, v145\n" \
"v_mul_f32_e32 v146, v54, v146\n" \
"v_mul_f32_e32 v147, v54, v147\n" \
"v_cvt_pk_bf16_f32 v152, v152, v153\n" \
"v_mul_f32_e32 v154, v54, v154\n" \
"v_mul_f32_e32 v155, v54, v155\n" \
"v_cvt_pk_bf16_f32 v176, v176, v177\n" \
"v_mul_f32_e32 v178, v55, v178\n" \
"v_mul_f32_e32 v179, v55, v179\n" \
"v_cvt_pk_bf16_f32 v184, v184, v185\n" \
"v_mul_f32_e32 v186, v55, v186\n" \
"v_mul_f32_e32 v187, v55, v187\n" \
"v_cvt_pk_bf16_f32 v129, v130, v131\n" \
"v_mul_f32_e32 v132, v54, v132\n" \
"v_mul_f32_e32 v133, v54, v133\n" \
"v_mul_f32_e32 v134, v54, v134\n" \
"v_mul_f32_e32 v135, v54, v135\n" \
"v_cvt_pk_bf16_f32 v137, v138, v139\n" \
"v_mul_f32_e32 v140, v54, v140\n" \
"v_mul_f32_e32 v141, v54, v141\n" \
"v_mul_f32_e32 v142, v54, v142\n" \
"v_mul_f32_e32 v143, v54, v143\n" \
"v_cvt_pk_bf16_f32 v161, v162, v163\n" \
"v_mul_f32_e32 v164, v55, v164\n" \
"v_mul_f32_e32 v165, v55, v165\n" \
"v_mul_f32_e32 v166, v55, v166\n" \
"v_mul_f32_e32 v167, v55, v167\n" \
"v_cvt_pk_bf16_f32 v169, v170, v171\n" \
"v_mul_f32_e32 v172, v55, v172\n" \
"v_mul_f32_e32 v173, v55, v173\n" \
"v_mul_f32_e32 v174, v55, v174\n" \
"v_mul_f32_e32 v175, v55, v175\n" \
"v_cvt_pk_bf16_f32 v145, v146, v147\n" \
"v_mul_f32_e32 v148, v54, v148\n" \
"v_mul_f32_e32 v149, v54, v149\n" \
"v_mul_f32_e32 v150, v54, v150\n" \
"v_mul_f32_e32 v151, v54, v151\n" \
"v_cvt_pk_bf16_f32 v153, v154, v155\n" \
"v_mul_f32_e32 v156, v54, v156\n" \
"v_mul_f32_e32 v157, v54, v157\n" \
"v_mul_f32_e32 v158, v54, v158\n" \
"v_mul_f32_e32 v159, v54, v159\n" \
"v_cvt_pk_bf16_f32 v177, v178, v179\n" \
"v_mul_f32_e32 v180, v55, v180\n" \
"v_mul_f32_e32 v181, v55, v181\n" \
"v_mul_f32_e32 v182, v55, v182\n" \
"v_mul_f32_e32 v183, v55, v183\n" \
"v_cvt_pk_bf16_f32 v185, v186, v187\n" \
"v_mul_f32_e32 v188, v55, v188\n" \
"v_mul_f32_e32 v189, v55, v189\n" \
"v_mul_f32_e32 v190, v55, v190\n" \
"v_mul_f32_e32 v191, v55, v191\n" \
"v_cvt_pk_bf16_f32 v130, v132, v133\n" \
"v_cvt_pk_bf16_f32 v131, v134, v135\n" \
"buffer_store_dwordx4 v[128:131], v20, s[56:59], s68 offen offset:0 sc1\n" \
"v_cvt_pk_bf16_f32 v138, v140, v141\n" \
"v_cvt_pk_bf16_f32 v139, v142, v143\n" \
"buffer_store_dwordx4 v[136:139], v20, s[56:59], s68 offen offset:1024 sc1\n" \
"v_cvt_pk_bf16_f32 v162, v164, v165\n" \
"s_waitcnt vmcnt(14) lgkmcnt(0)\n" \
"s_barrier\n" \
"v_mfma_f32_16x16x32_bf16 v[88:91], a[128:131], a[48:51], v[44:47]\n" \
"s_xor_b32 m0, s65, 0x12000\n" \
"ds_read_b128 a[160:163], v49 offset:0\n" \
"v_mfma_f32_16x16x32_bf16 v[88:91], a[132:135], a[52:55], v[88:91]\n" \
"buffer_load_dwordx4 v50, s[8:11], s64 offen offset:0 sc1 lds\n" \
"v_mfma_f32_16x16x32_bf16 v[88:91], a[136:139], a[56:59], v[88:91]\n" \
"ds_read_b128 a[164:167], v49 offset:1024\n" \
"ds_read_b128 a[168:171], v49 offset:2048\n" \
"v_mfma_f32_16x16x32_bf16 v[88:91], a[140:143], a[60:63], v[88:91]\n" \
"buffer_load_dwordx4 v50, s[8:11], s64 offen offset:1024 sc1 lds\n" \
"v_mfma_f32_16x16x32_bf16 v[92:95], a[156:159], a[60:63], v[44:47]\n" \
"ds_read_b128 a[172:175], v49 offset:3072\n" \
"ds_read_b128 a[176:179], v49 offset:8192\n" \
"v_mfma_f32_16x16x32_bf16 v[92:95], a[152:155], a[56:59], v[92:95]\n" \
"ds_read_b128 a[180:183], v49 offset:9216\n" \
"ds_read_b128 a[184:187], v49 offset:10240\n" \
"v_mfma_f32_16x16x32_bf16 v[92:95], a[148:151], a[52:55], v[92:95]\n" \
"ds_read_b128 a[188:191], v49 offset:11264\n" \
"ds_read_b128 a[0:3], v49 offset:4096\n" \
"v_mfma_f32_16x16x32_bf16 v[92:95], a[144:147], a[48:51], v[92:95]\n" \
"buffer_load_dwordx4 v50, s[8:11], s64 offen offset:2048 sc1 lds\n" \
"v_mfma_f32_16x16x32_bf16 v[32:35], a[220:223], a[64:67], 0\n" \
"ds_read_b128 a[4:7], v49 offset:5120\n" \
"ds_read_b128 a[8:11], v49 offset:6144\n" \
"v_mfma_f32_16x16x32_bf16 v[36:39], a[220:223], a[80:83], 0\n" \
"ds_read_b128 a[12:15], v49 offset:7168\n" \
"ds_read_b128 a[16:19], v49 offset:12288\n" \
"v_mfma_f32_16x16x32_bf16 v[40:43], a[220:223], a[96:99], 0\n" \
"ds_read_b128 a[20:23], v49 offset:13312\n" \
"ds_read_b128 a[24:27], v49 offset:14336\n" \
"v_mfma_f32_16x16x32_bf16 v[44:47], a[220:223], a[112:115], 0\n" \
"buffer_load_dwordx4 v50, s[8:11], s64 offen offset:3072 sc1 lds\n" \
"v_mfma_f32_16x16x32_bf16 v[32:35], a[224:227], a[68:71], v[32:35]\n" \
"s_xor_b32 m0, s65, 0xa000\n" \
"ds_read_b128 a[28:31], v49 offset:15360\n" \
"v_mfma_f32_16x16x32_bf16 v[36:39], a[224:227], a[84:87], v[36:39]\n" \
"v_exp_f32_e32 v88, v88\n" \
"v_mfma_f32_16x16x32_bf16 v[40:43], a[224:227], a[100:103], v[40:43]\n" \
"buffer_load_dwordx4 v51, s[8:11], s64 offen offset:0 sc1 lds\n" \
"v_mfma_f32_16x16x32_bf16 v[44:47], a[224:227], a[116:119], v[44:47]\n" \
"v_exp_f32_e32 v89, v89\n" \
"v_mfma_f32_16x16x32_bf16 v[32:35], a[228:231], a[72:75], v[32:35]\n" \
"v_exp_f32_e32 v90, v90\n" \
"v_mfma_f32_16x16x32_bf16 v[36:39], a[228:231], a[88:91], v[36:39]\n" \
"v_exp_f32_e32 v91, v91\n" \
"v_mfma_f32_16x16x32_bf16 v[40:43], a[228:231], a[104:107], v[40:43]\n" \
"buffer_load_dwordx4 v51, s[8:11], s64 offen offset:1024 sc1 lds\n" \
"v_mfma_f32_16x16x32_bf16 v[44:47], a[228:231], a[120:123], v[44:47]\n" \
"v_exp_f32_e32 v92, v92\n" \
"v_mfma_f32_16x16x32_bf16 v[32:35], a[232:235], a[76:79], v[32:35]\n" \
"v_exp_f32_e32 v93, v93\n" \
"v_mfma_f32_16x16x32_bf16 v[36:39], a[232:235], a[92:95], v[36:39]\n" \
"v_exp_f32_e32 v94, v94\n" \
"v_mfma_f32_16x16x32_bf16 v[40:43], a[232:235], a[108:111], v[40:43]\n" \
"buffer_load_dwordx4 v51, s[8:11], s64 offen offset:2048 sc1 lds\n" \
"v_mfma_f32_16x16x32_bf16 v[44:47], a[232:235], a[124:127], v[44:47]\n" \
"v_cvt_pk_bf16_f32 v124, v88, v89\n" \
"s_waitcnt lgkmcnt(3)\n" \
"v_mfma_f32_16x16x32_bf16 v[192:195], a[160:163], v[112:115], v[192:195]\n" \
"v_exp_f32_e32 v95, v95\n" \
"v_mfma_f32_16x16x32_bf16 v[196:199], a[164:167], v[112:115], v[196:199]\n" \
"buffer_load_dwordx4 v51, s[8:11], s64 offen offset:3072 sc1 lds\n" \
"v_mfma_f32_16x16x32_bf16 v[200:203], a[168:171], v[112:115], v[200:203]\n" \
"s_add_u32 m0, s93, 0x1000\n" \
"v_cvt_pk_bf16_f32 v125, v90, v91\n" \
"v_mfma_f32_16x16x32_bf16 v[204:207], a[172:175], v[112:115], v[204:207]\n" \
"v_cvt_pk_bf16_f32 v126, v92, v93\n" \
"v_cvt_pk_bf16_f32 v127, v94, v95\n" \
"v_mfma_f32_16x16x32_bf16 v[208:211], a[176:179], v[112:115], v[208:211]\n" \
"v_and_b32_e32 v124, v12, v124\n" \
"v_and_b32_e32 v125, v13, v125\n" \
"v_mfma_f32_16x16x32_bf16 v[212:215], a[180:183], v[112:115], v[212:215]\n" \
"buffer_load_dwordx4 v4, s[80:83], s92 offen offset:0 lds\n" \
"v_mfma_f32_16x16x32_bf16 v[216:219], a[184:187], v[112:115], v[216:219]\n" \
"v_and_b32_e32 v126, v14, v126\n" \
"v_and_b32_e32 v127, v15, v127\n" \
"v_mfma_f32_16x16x32_bf16 v[220:223], a[188:191], v[112:115], v[220:223]\n" \
"ds_read_b128 a[128:131], v48 offset:0\n" \
"ds_read_b128 a[132:135], v48 offset:1024\n" \
"v_mfma_f32_16x16x32_bf16 a[200:203], a[252:255], v[112:115], a[200:203]\n" \
"ds_read_b128 a[136:139], v48 offset:2048\n" \
"ds_read_b128 a[140:143], v48 offset:3072\n" \
"v_mfma_f32_16x16x32_bf16 v[224:227], a[160:163], v[120:123], v[224:227]\n" \
"buffer_load_dwordx4 v5, s[80:83], s92 offen offset:1024 lds\n" \
"v_mfma_f32_16x16x32_bf16 v[228:231], a[164:167], v[120:123], v[228:231]\n" \
"ds_read_b128 a[144:147], v48 offset:4096\n" \
"ds_read_b128 a[148:151], v48 offset:5120\n" \
"v_mfma_f32_16x16x32_bf16 v[232:235], a[168:171], v[120:123], v[232:235]\n" \
"ds_read_b128 a[152:155], v48 offset:6144\n" \
"ds_read_b128 a[156:159], v48 offset:7168\n" \
"v_mfma_f32_16x16x32_bf16 v[236:239], a[172:175], v[120:123], v[236:239]\n" \
"buffer_load_dwordx4 v6, s[80:83], s92 offen offset:2048 lds\n" \
"v_mfma_f32_16x16x32_bf16 v[240:243], a[176:179], v[120:123], v[240:243]\n" \
"v_cvt_pk_bf16_f32 v163, v166, v167\n" \
"v_cvt_pk_bf16_f32 v170, v172, v173\n" \
"buffer_store_dwordx4 v[160:163], v20, s[56:59], s69 offen offset:0 sc1\n" \
"v_mfma_f32_16x16x32_bf16 v[244:247], a[180:183], v[120:123], v[244:247]\n" \
"v_cvt_pk_bf16_f32 v171, v174, v175\n" \
"v_cvt_pk_bf16_f32 v146, v148, v149\n" \
"buffer_store_dwordx4 v[168:171], v20, s[56:59], s69 offen offset:1024 sc1\n" \
"v_mfma_f32_16x16x32_bf16 v[248:251], a[184:187], v[120:123], v[248:251]\n" \
"v_cvt_pk_bf16_f32 v147, v150, v151\n" \
"v_cvt_pk_bf16_f32 v154, v156, v157\n" \
"buffer_store_dwordx4 v[144:147], v20, s[56:59], s68 offen offset:2048 sc1\n" \
"v_mfma_f32_16x16x32_bf16 v[252:255], a[188:191], v[120:123], v[252:255]\n" \
"buffer_load_dwordx4 v7, s[80:83], s92 offen offset:3072 lds\n" \
"v_mfma_f32_16x16x32_bf16 a[204:207], a[252:255], v[120:123], a[204:207]\n" \
"v_cvt_pk_bf16_f32 v155, v158, v159\n" \
"v_cvt_pk_bf16_f32 v178, v180, v181\n" \
"buffer_store_dwordx4 v[152:155], v20, s[56:59], s68 offen offset:3072 sc1\n" \
"v_mfma_f32_16x16x32_bf16 v[224:227], a[0:3], v[124:127], v[224:227]\n" \
"v_cvt_pk_bf16_f32 v179, v182, v183\n" \
"v_cvt_pk_bf16_f32 v186, v188, v189\n" \
"buffer_store_dwordx4 v[176:179], v20, s[56:59], s69 offen offset:2048 sc1\n" \
"v_mfma_f32_16x16x32_bf16 v[228:231], a[4:7], v[124:127], v[228:231]\n" \
"v_cvt_pk_bf16_f32 v187, v190, v191\n" \
"s_add_u32 m0, s93, 0x2000\n" \
"buffer_store_dwordx4 v[184:187], v20, s[56:59], s69 offen offset:3072 sc1\n" \
"v_mfma_f32_16x16x32_bf16 v[232:235], a[8:11], v[124:127], v[232:235]\n" \
"buffer_load_dwordx4 v8, s[80:83], s92 offen offset:0 lds\n" \
"v_mfma_f32_16x16x32_bf16 v[236:239], a[12:15], v[124:127], v[236:239]\n" \
"v_mfma_f32_16x16x32_bf16 a[208:211], a[216:219], v[128:131], a[208:211]\n" \
"v_mfma_f32_16x16x32_bf16 a[208:211], a[216:219], v[136:139], a[208:211]\n" \
"v_mfma_f32_32x32x16_bf16 v[128:143], a[216:219], a[216:219], 0\n" \
"v_mfma_f32_16x16x32_bf16 a[208:211], a[216:219], v[160:163], a[208:211]\n" \
"v_mfma_f32_16x16x32_bf16 a[208:211], a[216:219], v[168:171], a[208:211]\n" \
"v_mfma_f32_32x32x16_bf16 v[160:175], a[216:219], a[216:219], 0\n" \
"v_accvgpr_read_b32 v56, a200\n" \
"v_accvgpr_read_b32 v23, a201\n" \
"v_add_f32_e32 v56, v56, v23\n" \
"v_mfma_f32_16x16x32_bf16 a[208:211], a[216:219], v[144:147], a[208:211]\n" \
"v_max_f32_e32 v62, v62, v56\n" \
"v_readlane_b32 s95, v11, 2\n" \
"v_mfma_f32_16x16x32_bf16 v[240:243], a[16:19], v[124:127], v[240:243]\n" \
"v_add_u32_e32 v49, 0x10000, v48\n" \
"s_waitcnt lgkmcnt(0)\n" \
"v_mfma_f32_16x16x32_bf16 v[244:247], a[20:23], v[124:127], v[244:247]\n" \
"v_rcp_f32_e32 v56, v56\n" \
"v_mfma_f32_16x16x32_bf16 v[248:251], a[24:27], v[124:127], v[248:251]\n" \
"v_mfma_f32_16x16x32_bf16 a[208:211], a[216:219], v[152:155], a[208:211]\n" \
"v_mfma_f32_32x32x16_bf16 v[144:159], a[216:219], a[216:219], 0\n" \
"v_mul_f32_e32 v56, v56, v60\n" \
"v_mfma_f32_16x16x32_bf16 a[208:211], a[216:219], v[176:179], a[208:211]\n" \
"v_mul_f32_e32 v192, v56, v192\n" \
"v_mul_f32_e32 v193, v56, v193\n" \
"v_mfma_f32_16x16x32_bf16 v[252:255], a[28:31], v[124:127], v[252:255]\n" \
"v_cvt_pk_bf16_f32 v192, v192, v193\n" \
"v_mul_f32_e32 v194, v56, v194\n" \
"v_mfma_f32_16x16x32_bf16 a[204:207], a[252:255], v[124:127], a[204:207]\n" \
"v_mul_f32_e32 v195, v56, v195\n" \
"v_cvt_pk_bf16_f32 v193, v194, v195\n" \
"s_waitcnt vmcnt(19)\n" \
"s_barrier\n" \
"v_mfma_f32_16x16x32_bf16 v[64:67], a[128:131], a[64:67], v[32:35]\n" \
"s_xor_b32 m0, s65, 0x18000\n" \
"v_mul_f32_e32 v196, v56, v196\n" \
"v_mfma_f32_16x16x32_bf16 v[64:67], a[132:135], a[68:71], v[64:67]\n" \
"buffer_load_dwordx4 v52, s[8:11], s64 offen offset:0 sc1 lds\n" \
"v_mfma_f32_16x16x32_bf16 v[64:67], a[136:139], a[72:75], v[64:67]\n" \
"v_mfma_f32_16x16x32_bf16 a[208:211], a[216:219], v[184:187], a[208:211]\n" \
"v_mfma_f32_32x32x16_bf16 v[176:191], a[216:219], a[216:219], 0\n" \
"v_mul_f32_e32 v197, v56, v197\n" \
"v_mul_f32_e32 v198, v56, v198\n" \
"v_mul_f32_e32 v199, v56, v199\n" \
"v_mul_f32_e32 v200, v56, v200\n" \
"v_mul_f32_e32 v201, v56, v201\n" \
"v_accvgpr_read_b32 v57, a204\n" \
"v_mfma_f32_16x16x32_bf16 v[64:67], a[140:143], a[76:79], v[64:67]\n" \
"buffer_load_dwordx4 v52, s[8:11], s64 offen offset:1024 sc1 lds\n" \
"v_mfma_f32_16x16x32_bf16 v[68:71], a[156:159], a[76:79], v[32:35]\n" \
"v_accvgpr_read_b32 v23, a205\n" \
"v_cvt_pk_bf16_f32 v194, v196, v197\n" \
"v_mfma_f32_32x32x16_bf16 a[192:207], a[216:219], a[216:219], 0\n" \
"v_cvt_pk_bf16_f32 v195, v198, v199\n" \
"buffer_store_dwordx4 v[192:195], v20, s[56:59], s70 offen offset:0 sc1\n" \
"v_cvt_pk_bf16_f32 v200, v200, v201\n" \
"v_mfma_f32_16x16x32_bf16 v[68:71], a[152:155], a[72:75], v[68:71]\n" \
"v_mul_f32_e32 v202, v56, v202\n" \
"v_mul_f32_e32 v203, v56, v203\n" \
"v_mfma_f32_16x16x32_bf16 v[68:71], a[148:151], a[68:71], v[68:71]\n" \
"v_add_f32_e32 v57, v57, v23\n" \
"v_cvt_pk_bf16_f32 v201, v202, v203\n" \
"v_mfma_f32_16x16x32_bf16 v[68:71], a[144:147], a[64:67], v[68:71]\n" \
"buffer_load_dwordx4 v52, s[8:11], s64 offen offset:2048 sc1 lds\n" \
"v_mfma_f32_16x16x32_bf16 v[72:75], a[128:131], a[80:83], v[36:39]\n" \
"v_mfma_f32_16x16x32_bf16 a[208:211], a[216:219], v[192:195], a[208:211]\n" \
"v_mul_f32_e32 v204, v56, v204\n" \
"v_mul_f32_e32 v205, v56, v205\n" \
"v_mfma_f32_16x16x32_bf16 v[72:75], a[132:135], a[84:87], v[72:75]\n" \
"v_mul_f32_e32 v206, v56, v206\n" \
"v_mul_f32_e32 v207, v56, v207\n" \
"v_mfma_f32_16x16x32_bf16 v[72:75], a[136:139], a[88:91], v[72:75]\n" \
"v_mul_f32_e32 v208, v56, v208\n" \
"v_mul_f32_e32 v209, v56, v209\n" \
"v_mfma_f32_16x16x32_bf16 v[72:75], a[140:143], a[92:95], v[72:75]\n" \
"buffer_load_dwordx4 v52, s[8:11], s64 offen offset:3072 sc1 lds\n" \
"v_mfma_f32_16x16x32_bf16 v[76:79], a[156:159], a[92:95], v[36:39]\n" \
"s_mov_b32 m0, s65\n" \
"v_max_f32_e32 v62, v62, v57\n" \
"v_mfma_f32_16x16x32_bf16 v[76:79], a[152:155], a[88:91], v[76:79]\n" \
"v_cvt_pk_bf16_f32 v202, v204, v205\n" \
"v_cvt_pk_bf16_f32 v203, v206, v207\n" \
"buffer_store_dwordx4 v[200:203], v20, s[56:59], s70 offen offset:1024 sc1\n" \
"v_mfma_f32_16x16x32_bf16 v[76:79], a[148:151], a[84:87], v[76:79]\n" \
"v_cvt_pk_bf16_f32 v208, v208, v209\n" \
"v_mul_f32_e32 v210, v56, v210\n" \
"v_mfma_f32_16x16x32_bf16 v[76:79], a[144:147], a[80:83], v[76:79]\n" \
"buffer_load_dwordx4 v53, s[8:11], s64 offen offset:0 sc1 lds\n" \
"v_mfma_f32_16x16x32_bf16 v[80:83], a[128:131], a[96:99], v[40:43]\n" \
"v_mul_f32_e32 v211, v56, v211\n" \
"v_cvt_pk_bf16_f32 v209, v210, v211\n" \
"v_mfma_f32_16x16x32_bf16 v[80:83], a[132:135], a[100:103], v[80:83]\n" \
"v_rcp_f32_e32 v57, v57\n" \
"v_mfma_f32_16x16x32_bf16 v[80:83], a[136:139], a[104:107], v[80:83]\n" \
"buffer_load_dwordx4 v53, s[8:11], s64 offen offset:1024 sc1 lds\n" \
"v_mfma_f32_16x16x32_bf16 v[80:83], a[140:143], a[108:111], v[80:83]\n" \
"v_mfma_f32_16x16x32_bf16 a[208:211], a[216:219], v[200:203], a[208:211]\n" \
"v_mfma_f32_32x32x16_bf16 v[192:207], a[216:219], a[216:219], 0\n" \
"v_mul_f32_e32 v212, v56, v212\n" \
"v_mul_f32_e32 v213, v56, v213\n" \
"v_mul_f32_e32 v214, v56, v214\n" \
"v_mul_f32_e32 v215, v56, v215\n" \
"v_mul_f32_e32 v216, v56, v216\n" \
"v_mul_f32_e32 v217, v56, v217\n" \
"v_mfma_f32_16x16x32_bf16 v[84:87], a[156:159], a[108:111], v[40:43]\n" \
"v_cvt_pk_bf16_f32 v210, v212, v213\n" \
"v_cvt_pk_bf16_f32 v211, v214, v215\n" \
"buffer_store_dwordx4 v[208:211], v20, s[56:59], s70 offen offset:2048 sc1\n" \
"v_mfma_f32_16x16x32_bf16 v[84:87], a[152:155], a[104:107], v[84:87]\n" \
"v_cvt_pk_bf16_f32 v216, v216, v217\n" \
"v_mul_f32_e32 v218, v56, v218\n" \
"v_mfma_f32_16x16x32_bf16 v[84:87], a[148:151], a[100:103], v[84:87]\n" \
"buffer_load_dwordx4 v53, s[8:11], s64 offen offset:2048 sc1 lds\n" \
"v_mfma_f32_16x16x32_bf16 v[84:87], a[144:147], a[96:99], v[84:87]\n" \
"v_mul_f32_e32 v219, v56, v219\n" \
"v_mul_f32_e32 v57, v57, v61\n" \
"v_mfma_f32_16x16x32_bf16 v[88:91], a[128:131], a[112:115], v[44:47]\n" \
"ds_read_b128 a[128:131], v48 offset:8192\n" \
"v_cvt_pk_bf16_f32 v217, v218, v219\n" \
"v_mfma_f32_16x16x32_bf16 v[88:91], a[132:135], a[116:119], v[88:91]\n" \
"ds_read_b128 a[132:135], v48 offset:9216\n" \
"v_mul_f32_e32 v220, v56, v220\n" \
"v_mfma_f32_16x16x32_bf16 v[88:91], a[136:139], a[120:123], v[88:91]\n" \
"ds_read_b128 a[136:139], v48 offset:10240\n" \
"v_mul_f32_e32 v221, v56, v221\n" \
"v_mfma_f32_16x16x32_bf16 v[88:91], a[140:143], a[124:127], v[88:91]\n" \
"ds_read_b128 a[140:143], v48 offset:11264\n" \
"v_mul_f32_e32 v222, v56, v222\n" \
"v_mfma_f32_16x16x32_bf16 v[92:95], a[156:159], a[124:127], v[44:47]\n" \
"ds_read_b128 a[156:159], v48 offset:15360\n" \
"v_mul_f32_e32 v223, v56, v223\n" \
"v_mfma_f32_16x16x32_bf16 v[92:95], a[152:155], a[120:123], v[92:95]\n" \
"ds_read_b128 a[152:155], v48 offset:14336\n" \
"v_mul_f32_e32 v224, v57, v224\n" \
"v_mfma_f32_16x16x32_bf16 v[92:95], a[148:151], a[116:119], v[92:95]\n" \
"ds_read_b128 a[148:151], v48 offset:13312\n" \
"v_mul_f32_e32 v225, v57, v225\n" \
"v_mfma_f32_16x16x32_bf16 v[92:95], a[144:147], a[112:115], v[92:95]\n" \
"ds_read_b128 a[144:147], v48 offset:12288\n" \
"v_mfma_f32_16x16x32_bf16 a[208:211], a[216:219], v[208:211], a[208:211]\n" \
"v_cvt_pk_bf16_f32 v218, v220, v221\n" \
"v_cvt_pk_bf16_f32 v219, v222, v223\n" \
"buffer_store_dwordx4 v[216:219], v20, s[56:59], s70 offen offset:3072 sc1\n" \
"v_cvt_pk_bf16_f32 v224, v224, v225\n" \
"v_mul_f32_e32 v226, v57, v226\n" \
"v_mul_f32_e32 v227, v57, v227\n" \
"v_mfma_f32_16x16x32_bf16 a[208:211], a[216:219], v[216:219], a[208:211]\n" \
"v_mfma_f32_32x32x16_bf16 v[208:223], a[216:219], a[216:219], 0\n" \
"v_cvt_pk_bf16_f32 v225, v226, v227\n" \
"v_mul_f32_e32 v228, v57, v228\n" \
"v_mul_f32_e32 v229, v57, v229\n" \
"v_mul_f32_e32 v230, v57, v230\n" \
"v_mul_f32_e32 v231, v57, v231\n" \
"v_mul_f32_e32 v232, v57, v232\n" \
"v_mul_f32_e32 v233, v57, v233\n" \
"v_cvt_pk_bf16_f32 v226, v228, v229\n" \
"v_cvt_pk_bf16_f32 v227, v230, v231\n" \
"buffer_store_dwordx4 v[224:227], v20, s[56:59], s71 offen offset:0 sc1\n" \
"v_cvt_pk_bf16_f32 v232, v232, v233\n" \
"v_mul_f32_e32 v234, v57, v234\n" \
"v_mul_f32_e32 v235, v57, v235\n" \
"v_mfma_f32_16x16x32_bf16 a[208:211], a[216:219], v[224:227], a[208:211]\n" \
"v_cvt_pk_bf16_f32 v233, v234, v235\n" \
"v_mul_f32_e32 v236, v57, v236\n" \
"v_mul_f32_e32 v237, v57, v237\n" \
"v_mul_f32_e32 v238, v57, v238\n" \
"v_mul_f32_e32 v239, v57, v239\n" \
"v_mul_f32_e32 v240, v57, v240\n" \
"v_mul_f32_e32 v241, v57, v241\n" \
"v_cvt_pk_bf16_f32 v234, v236, v237\n" \
"v_cvt_pk_bf16_f32 v235, v238, v239\n" \
"buffer_store_dwordx4 v[232:235], v20, s[56:59], s71 offen offset:1024 sc1\n" \
"v_cvt_pk_bf16_f32 v240, v240, v241\n" \
"v_mul_f32_e32 v242, v57, v242\n" \
"v_mul_f32_e32 v243, v57, v243\n" \
"v_mfma_f32_16x16x32_bf16 a[208:211], a[216:219], v[232:235], a[208:211]\n" \
"v_mfma_f32_32x32x16_bf16 v[224:239], a[216:219], a[216:219], 0\n" \
"v_cvt_pk_bf16_f32 v241, v242, v243\n" \
"v_mul_f32_e32 v244, v57, v244\n" \
"v_mul_f32_e32 v245, v57, v245\n" \
"v_mul_f32_e32 v246, v57, v246\n" \
"v_mul_f32_e32 v247, v57, v247\n" \
"v_mul_f32_e32 v248, v57, v248\n" \
"v_mul_f32_e32 v249, v57, v249\n" \
"v_cvt_pk_bf16_f32 v242, v244, v245\n" \
"v_cvt_pk_bf16_f32 v243, v246, v247\n" \
"buffer_store_dwordx4 v[240:243], v20, s[56:59], s71 offen offset:2048 sc1\n" \
"v_cvt_pk_bf16_f32 v248, v248, v249\n" \
"v_mul_f32_e32 v250, v57, v250\n" \
"v_mul_f32_e32 v251, v57, v251\n" \
"v_mfma_f32_16x16x32_bf16 a[208:211], a[216:219], v[240:243], a[208:211]\n" \
"v_cvt_pk_bf16_f32 v249, v250, v251\n" \
"v_mul_f32_e32 v252, v57, v252\n" \
"v_mul_f32_e32 v253, v57, v253\n" \
"v_mul_f32_e32 v254, v57, v254\n" \
"v_mul_f32_e32 v255, v57, v255\n" \
"v_cvt_pk_bf16_f32 v250, v252, v253\n" \
"v_cvt_pk_bf16_f32 v251, v254, v255\n" \
"buffer_store_dwordx4 v[248:251], v20, s[56:59], s71 offen offset:3072 sc1\n" \
"buffer_load_dwordx4 v53, s[8:11], s64 offen offset:3072 sc1 lds\n" \
"v_mfma_f32_16x16x32_bf16 a[208:211], a[216:219], v[248:251], a[208:211]\n" \
"v_mfma_f32_32x32x16_bf16 v[240:255], a[216:219], a[216:219], 0\n" \
"v_cmp_lt_f32_e64 s[72:73], s100, v62\n" \
"v_exp_f32_e32 v64, v64\n" \
"v_exp_f32_e32 v65, v65\n" \
"s_xor_b32 s65, s65, 0x8000\n" \
"s_waitcnt lgkmcnt(0)\n" \
"v_exp_f32_e32 v66, v66\n" \
"v_exp_f32_e32 v67, v67\n" \
"v_accvgpr_read_b32 v63, a208\n" \
"v_cmp_u_f32_e32 vcc, v63, v63\n" \
"v_exp_f32_e32 v68, v68\n" \
"s_or_b64 vcc, s[72:73], vcc\n" \
"s_cselect_b32 s95, 0x40000000, s95\n" \
"s_cselect_b32 s94, 0x7f800000, 0\n" \
"v_exp_f32_e32 v69, v69\n" \
"v_exp_f32_e32 v70, v70\n" \
"v_exp_f32_e32 v71, v71\n" \
"v_exp_f32_e32 v72, v72\n" \
"v_exp_f32_e32 v73, v73\n" \
"v_exp_f32_e32 v74, v74\n" \
"v_exp_f32_e32 v75, v75\n" \
"v_exp_f32_e32 v76, v76\n" \
"v_exp_f32_e32 v77, v77\n" \
"v_exp_f32_e32 v78, v78\n" \
"v_exp_f32_e32 v79, v79\n" \
"v_exp_f32_e32 v80, v80\n" \
"v_exp_f32_e32 v81, v81\n" \
"v_exp_f32_e32 v82, v82\n" \
"v_exp_f32_e32 v83, v83\n" \
"v_exp_f32_e32 v84, v84\n" \
"v_exp_f32_e32 v85, v85\n" \
"v_exp_f32_e32 v86, v86\n" \
"v_exp_f32_e32 v87, v87\n" \
"s_add_u32 s64, s64, 0x10000\n" \
"v_exp_f32_e32 v88, v88\n" \
"v_exp_f32_e32 v89, v89\n" \
"v_exp_f32_e32 v90, v90\n" \
"v_exp_f32_e32 v91, v91\n" \
"v_cvt_pk_bf16_f32 v96, v64, v65\n" \
"v_cvt_pk_bf16_f32 v97, v66, v67\n" \
"v_cvt_pk_bf16_f32 v98, v68, v69\n" \
"v_cvt_pk_bf16_f32 v99, v70, v71\n" \
"v_cvt_pk_bf16_f32 v104, v72, v73\n" \
"v_cvt_pk_bf16_f32 v105, v74, v75\n" \
"v_cvt_pk_bf16_f32 v106, v76, v77\n" \
"v_cvt_pk_bf16_f32 v107, v78, v79\n" \
"v_cvt_pk_bf16_f32 v112, v80, v81\n" \
"v_cvt_pk_bf16_f32 v113, v82, v83\n" \
"v_cvt_pk_bf16_f32 v114, v84, v85\n" \
"v_cvt_pk_bf16_f32 v115, v86, v87\n" \
"v_xor_b32_e32 v48, 0x8000, v48\n" \
"s_sub_u32 s66, s19, 1\n" \
"s_cbranch_scc0 L_2_%=\n" \
"s_waitcnt vmcnt(16)\n" \
"s_barrier\n" \
"L_f1_%=:\n" \
"L_pf17:\n" \
"s_cbranch_execz L_retA\n" \
"s_nop 0\n" \
"ds_read_b128 a[160:163], v49 offset:0\n" \
"ds_read_b128 a[164:167], v49 offset:1024\n" \
"ds_read_b128 a[168:171], v49 offset:2048\n" \
"ds_read_b128 a[172:175], v49 offset:3072\n" \
"ds_read_b128 a[176:179], v49 offset:8192\n" \
"ds_read_b128 a[180:183], v49 offset:9216\n" \
"v_mfma_f32_16x16x32_bf16 v[72:75], a[128:131], a[80:83], v[36:39]\n" \
"s_add_u32 s68, s61, 0x0\n" \
"s_add_u32 s69, s61, 0x49000\n" \
"ds_read_b128 a[184:187], v49 offset:10240\n" \
"v_mfma_f32_16x16x32_bf16 v[72:75], a[132:135], a[84:87], v[72:75]\n" \
"ds_read_b128 a[188:191], v49 offset:11264\n" \
"v_add_u32_e32 v56, 0x49000, v20\n" \
"v_mfma_f32_16x16x32_bf16 v[72:75], a[136:139], a[88:91], v[72:75]\n" \
"v_readlane_b32 s97, v11, 32\n" \
"v_readlane_b32 s98, v11, 33\n" \
"v_mfma_f32_16x16x32_bf16 v[72:75], a[140:143], a[92:95], v[72:75]\n" \
"v_and_b32_e32 v96, v12, v96\n" \
"v_and_b32_e32 v97, v13, v97\n" \
"v_mfma_f32_16x16x32_bf16 v[76:79], a[156:159], a[92:95], v[36:39]\n" \
"buffer_load_dwordx4 a[32:35], v20, s[56:59], s97 offen offset:0 sc1\n" \
"v_mfma_f32_16x16x32_bf16 v[76:79], a[152:155], a[88:91], v[76:79]\n" \
"v_and_b32_e32 v98, v14, v98\n" \
"v_and_b32_e32 v99, v15, v99\n" \
"v_mfma_f32_16x16x32_bf16 v[76:79], a[148:151], a[84:87], v[76:79]\n" \
"ds_read_b128 a[0:3], v20 offset:8192\n" \
"v_mov_b32_e32 v61, s62\n" \
"v_mfma_f32_16x16x32_bf16 v[76:79], a[144:147], a[80:83], v[76:79]\n" \
"buffer_load_dwordx4 a[40:43], v56, s[56:59], s97 offen offset:0 sc1\n" \
"ds_read_b128 a[4:7], v20 offset:9216\n" \
"ds_read_b128 a[8:11], v20 offset:10240\n" \
"s_waitcnt lgkmcnt(10)\n" \
"v_mfma_f32_16x16x32_bf16 v[128:131], a[160:163], v[96:99], v[128:131]\n" \
"ds_read_b128 a[12:15], v20 offset:11264\n" \
"s_waitcnt lgkmcnt(10)\n" \
"v_mfma_f32_16x16x32_bf16 v[132:135], a[164:167], v[96:99], v[132:135]\n" \
"ds_read_b128 a[28:31], v20 offset:15360\n" \
"buffer_load_dwordx4 a[48:51], v20, s[56:59], s98 offen offset:0 sc1\n" \
"s_waitcnt lgkmcnt(10)\n" \
"v_mfma_f32_16x16x32_bf16 v[136:139], a[168:171], v[96:99], v[136:139]\n" \
"ds_read_b128 a[24:27], v20 offset:14336\n" \
"s_waitcnt lgkmcnt(10)\n" \
"v_mfma_f32_16x16x32_bf16 v[140:143], a[172:175], v[96:99], v[140:143]\n" \
"ds_read_b128 a[20:23], v20 offset:13312\n" \
"buffer_load_dwordx4 a[56:59], v56, s[56:59], s98 offen offset:0 sc1\n" \
"s_waitcnt lgkmcnt(9)\n" \
"v_mfma_f32_16x16x32_bf16 v[144:147], a[176:179], v[96:99], v[144:147]\n" \
"ds_read_b128 a[16:19], v20 offset:12288\n" \
"v_add_u32_e32 v50, s15, v48\n" \
"v_mfma_f32_16x16x32_bf16 v[148:151], a[180:183], v[96:99], v[148:151]\n" \
"v_xor_b32_e32 v48, 0x4000, v49\n" \
"s_waitcnt lgkmcnt(8)\n" \
"v_mfma_f32_16x16x32_bf16 v[152:155], a[184:187], v[96:99], v[152:155]\n" \
"buffer_load_dwordx4 a[36:39], v20, s[56:59], s97 offen offset:1024 sc1\n" \
"v_mfma_f32_16x16x32_bf16 v[156:159], a[188:191], v[96:99], v[156:159]\n" \
"ds_read_b32 v58, v18 offset:256\n" \
"ds_read_b32 v59, v18 offset:384\n" \
"v_mfma_f32_16x16x32_bf16 a[192:195], a[252:255], v[96:99], a[192:195]\n" \
"ds_read_b128 a[96:99], v48 offset:4096\n" \
"ds_read_b128 a[100:103], v48 offset:5120\n" \
"buffer_load_dwordx4 a[44:47], v56, s[56:59], s97 offen offset:1024 sc1\n" \
"v_mfma_f32_16x16x32_bf16 v[160:163], a[160:163], v[104:107], v[160:163]\n" \
"ds_read_b128 a[104:107], v48 offset:6144\n" \
"ds_read_b128 a[108:111], v48 offset:7168\n" \
"v_mfma_f32_16x16x32_bf16 v[164:167], a[164:167], v[104:107], v[164:167]\n" \
"ds_read_b128 a[112:115], v48 offset:12288\n" \
"ds_read_b128 a[116:119], v48 offset:13312\n" \
"buffer_load_dwordx4 a[52:55], v20, s[56:59], s98 offen offset:1024 sc1\n" \
"v_mfma_f32_16x16x32_bf16 v[168:171], a[168:171], v[104:107], v[168:171]\n" \
"ds_read_b128 a[120:123], v48 offset:14336\n" \
"ds_read_b128 a[124:127], v48 offset:15360\n" \
"v_mfma_f32_16x16x32_bf16 v[172:175], a[172:175], v[104:107], v[172:175]\n" \
"ds_read_b128 a[128:131], v49 offset:4096\n" \
"ds_read_b128 a[132:135], v49 offset:5120\n" \
"buffer_load_dwordx4 a[60:63], v56, s[56:59], s98 offen offset:1024 sc1\n" \
"v_mfma_f32_16x16x32_bf16 v[176:179], a[176:179], v[104:107], v[176:179]\n" \
"ds_read_b128 a[136:139], v49 offset:6144\n" \
"ds_read_b128 a[140:143], v49 offset:7168\n" \
"v_mfma_f32_16x16x32_bf16 v[180:183], a[180:183], v[104:107], v[180:183]\n" \
"ds_read_b128 a[144:147], v49 offset:12288\n" \
"ds_read_b128 a[148:151], v49 offset:13312\n" \
"s_waitcnt vmcnt(8)\n" \
"s_mov_b64 exec, 1\n" \
"buffer_store_dword v61, off, s[56:59], s95 offset:0 sc1\n" \
"s_mov_b64 exec, -1\n" \
"v_mfma_f32_16x16x32_bf16 v[184:187], a[184:187], v[104:107], v[184:187]\n" \
"ds_read_b128 a[152:155], v49 offset:14336\n" \
"ds_read_b128 a[156:159], v49 offset:15360\n" \
"v_mfma_f32_16x16x32_bf16 v[188:191], a[188:191], v[104:107], v[188:191]\n" \
"v_accvgpr_read_b32 v40, a200\n" \
"v_accvgpr_read_b32 v23, a201\n" \
"v_mfma_f32_16x16x32_bf16 a[196:199], a[252:255], v[104:107], a[196:199]\n" \
"v_add_f32_e32 v40, v40, v23\n" \
"s_waitcnt lgkmcnt(15)\n" \
"v_mfma_f32_16x16x32_bf16 v[80:83], a[0:3], a[64:67], v[32:35]\n" \
"buffer_load_dwordx4 a[220:223], v20, s[56:59], s60 offen offset:0 sc1\n" \
"v_mfma_f32_16x16x32_bf16 v[80:83], a[4:7], a[68:71], v[80:83]\n" \
"v_accvgpr_read_b32 v41, a204\n" \
"v_accvgpr_read_b32 v23, a205\n" \
"v_mfma_f32_16x16x32_bf16 v[80:83], a[8:11], a[72:75], v[80:83]\n" \
"v_add_f32_e32 v41, v41, v23\n" \
"v_add_u32_e32 v51, 0x10000, v50\n" \
"v_mfma_f32_16x16x32_bf16 v[80:83], a[12:15], a[76:79], v[80:83]\n" \
"buffer_load_dwordx4 a[236:239], v56, s[56:59], s60 offen offset:0 sc1\n" \
"v_mfma_f32_16x16x32_bf16 v[84:87], a[28:31], a[76:79], v[32:35]\n" \
"v_add_f32_e32 v40, s94, v40\n" \
"v_add_f32_e32 v41, s94, v41\n" \
"v_mfma_f32_16x16x32_bf16 v[84:87], a[24:27], a[72:75], v[84:87]\n" \
"ds_write_b128 v50, v[192:195] offset:0\n" \
"buffer_load_dwordx4 a[224:227], v20, s[56:59], s60 offen offset:1024 sc1\n" \
"v_mfma_f32_16x16x32_bf16 v[84:87], a[20:23], a[68:71], v[84:87]\n" \
"ds_write_b128 v50, v[196:199] offset:1024\n" \
"v_mfma_f32_16x16x32_bf16 v[84:87], a[16:19], a[64:67], v[84:87]\n" \
"ds_write_b128 v50, v[200:203] offset:2048\n" \
"buffer_load_dwordx4 a[240:243], v56, s[56:59], s60 offen offset:1024 sc1\n" \
"v_mfma_f32_16x16x32_bf16 v[88:91], a[0:3], a[80:83], v[36:39]\n" \
"ds_write_b128 v50, v[204:207] offset:3072\n" \
"v_mfma_f32_16x16x32_bf16 v[88:91], a[4:7], a[84:87], v[88:91]\n" \
"ds_write_b128 v50, v[208:211] offset:8192\n" \
"buffer_load_dwordx4 a[228:231], v20, s[56:59], s60 offen offset:2048 sc1\n" \
"L_pf18:\n" \
"s_cbranch_execz L_retA\n" \
"s_nop 0\n" \
"v_mfma_f32_16x16x32_bf16 v[88:91], a[8:11], a[88:91], v[88:91]\n" \
"ds_write_b128 v50, v[212:215] offset:9216\n" \
"v_mfma_f32_16x16x32_bf16 v[88:91], a[12:15], a[92:95], v[88:91]\n" \
"ds_write_b128 v50, v[216:219] offset:10240\n" \
"buffer_load_dwordx4 a[244:247], v56, s[56:59], s60 offen offset:2048 sc1\n" \
"v_mfma_f32_16x16x32_bf16 v[92:95], a[28:31], a[92:95], v[36:39]\n" \
"ds_write_b128 v50, v[220:223] offset:11264\n" \
"ds_write_b128 v51, v[224:227] offset:0\n" \
"ds_write_b128 v51, v[228:231] offset:1024\n" \
"buffer_load_dwordx4 a[232:235], v20, s[56:59], s60 offen offset:3072 sc1\n" \
"ds_write_b128 v51, v[232:235] offset:2048\n" \
"ds_write_b128 v51, v[236:239] offset:3072\n" \
"ds_write_b128 v51, v[240:243] offset:8192\n" \
"ds_write_b128 v51, v[244:247] offset:9216\n" \
"buffer_load_dwordx4 a[248:251], v56, s[56:59], s60 offen offset:3072 sc1\n" \
"ds_write_b128 v51, v[248:251] offset:10240\n" \
"ds_write_b128 v51, v[252:255] offset:11264\n" \
"ds_write_b64 v19, v[40:41]\n" \
"v_mfma_f32_16x16x32_bf16 v[92:95], a[24:27], a[88:91], v[92:95]\n" \
"v_xor_b32_e32 v52, 0x4000, v50\n" \
"v_xor_b32_e32 v53, 0x4000, v51\n" \
"v_mfma_f32_16x16x32_bf16 v[92:95], a[20:23], a[84:87], v[92:95]\n" \
"v_exp_f32_e32 v72, v72\n" \
"v_mfma_f32_16x16x32_bf16 v[92:95], a[16:19], a[80:83], v[92:95]\n" \
"v_exp_f32_e32 v73, v73\n" \
"v_exp_f32_e32 v74, v74\n" \
"v_exp_f32_e32 v75, v75\n" \
"v_exp_f32_e32 v76, v76\n" \
"v_exp_f32_e32 v77, v77\n" \
"v_exp_f32_e32 v78, v78\n" \
"v_exp_f32_e32 v79, v79\n" \
"v_cvt_pk_bf16_f32 v108, v72, v73\n" \
"v_cvt_pk_bf16_f32 v109, v74, v75\n" \
"v_cvt_pk_bf16_f32 v110, v76, v77\n" \
"v_cvt_pk_bf16_f32 v111, v78, v79\n" \
"v_and_b32_e32 v108, v12, v108\n" \
"v_and_b32_e32 v109, v13, v109\n" \
"v_and_b32_e32 v110, v14, v110\n" \
"v_and_b32_e32 v111, v15, v111\n" \
"v_readlane_b32 s93, v11, 18\n" \
"v_readlane_b32 s84, v11, 13\n" \
"v_readlane_b32 s85, v11, 14\n" \
"v_readlane_b32 s86, v11, 15\n" \
"s_mov_b32 s87, 0x20000\n" \
"v_readlane_b32 s92, v11, 16\n" \
"s_add_u32 m0, s93, 0x0\n" \
"v_xor_b32_e32 v63, 0x400, v19\n" \
"s_waitcnt lgkmcnt(0)\n" \
"s_barrier\n" \
"ds_read_b128 v[192:195], v52 offset:0\n" \
"ds_read_b128 v[196:199], v52 offset:1024\n" \
"ds_read_b128 v[200:203], v52 offset:2048\n" \
"ds_read_b128 v[204:207], v52 offset:3072\n" \
"ds_read_b128 v[208:211], v52 offset:8192\n" \
"ds_read_b128 v[212:215], v52 offset:9216\n" \
"ds_read_b128 v[216:219], v52 offset:10240\n" \
"ds_read_b128 v[220:223], v52 offset:11264\n" \
"ds_read_b128 v[224:227], v53 offset:0\n" \
"ds_read_b128 v[228:231], v53 offset:1024\n" \
"ds_read_b128 v[232:235], v53 offset:2048\n" \
"ds_read_b128 v[236:239], v53 offset:3072\n" \
"ds_read_b128 v[240:243], v53 offset:8192\n" \
"ds_read_b128 v[244:247], v53 offset:9216\n" \
"ds_read_b128 v[248:251], v53 offset:10240\n" \
"ds_read_b128 v[252:255], v53 offset:11264\n" \
"v_mfma_f32_16x16x32_bf16 v[160:163], a[128:131], v[108:111], v[160:163]\n" \
"buffer_load_dwordx4 v0, s[84:87], s92 offen offset:0 lds\n" \
"v_mfma_f32_16x16x32_bf16 v[164:167], a[132:135], v[108:111], v[164:167]\n" \
"v_exp_f32_e32 v80, v80\n" \
"v_mfma_f32_16x16x32_bf16 v[168:171], a[136:139], v[108:111], v[168:171]\n" \
"v_exp_f32_e32 v81, v81\n" \
"v_mfma_f32_16x16x32_bf16 v[172:175], a[140:143], v[108:111], v[172:175]\n" \
"buffer_load_dwordx4 v1, s[84:87], s92 offen offset:1024 lds\n" \
"v_mfma_f32_16x16x32_bf16 v[176:179], a[144:147], v[108:111], v[176:179]\n" \
"ds_read_b64 v[42:43], v63\n" \
"v_cvt_pk_bf16_f32 v116, v80, v81\n" \
"v_mfma_f32_16x16x32_bf16 v[180:183], a[148:151], v[108:111], v[180:183]\n" \
"v_exp_f32_e32 v82, v82\n" \
"v_mfma_f32_16x16x32_bf16 v[184:187], a[152:155], v[108:111], v[184:187]\n" \
"buffer_load_dwordx4 v2, s[84:87], s92 offen offset:2048 lds\n" \
"v_mfma_f32_16x16x32_bf16 v[188:191], a[156:159], v[108:111], v[188:191]\n" \
"v_exp_f32_e32 v83, v83\n" \
"v_mfma_f32_16x16x32_bf16 a[196:199], a[252:255], v[108:111], a[196:199]\n" \
"v_exp_f32_e32 v84, v84\n" \
"v_exp_f32_e32 v85, v85\n" \
"v_exp_f32_e32 v86, v86\n" \
"s_waitcnt lgkmcnt(1)\n" \
"s_barrier\n" \
"buffer_load_dwordx4 v3, s[84:87], s92 offen offset:3072 lds\n" \
"s_add_u32 m0, s93, 0x1000\n" \
"v_exp_f32_e32 v87, v87\n" \
"v_cvt_pk_bf16_f32 v117, v82, v83\n" \
"v_cvt_pk_bf16_f32 v118, v84, v85\n" \
"v_cvt_pk_bf16_f32 v119, v86, v87\n" \
"v_exp_f32_e32 v88, v88\n" \
"v_exp_f32_e32 v89, v89\n" \
"v_mfma_f32_16x16x32_bf16 v[128:131], a[96:99], v[116:119], v[128:131]\n" \
"buffer_load_dwordx4 v4, s[84:87], s92 offen offset:0 lds\n" \
"v_mfma_f32_16x16x32_bf16 v[132:135], a[100:103], v[116:119], v[132:135]\n" \
"v_exp_f32_e32 v90, v90\n" \
"v_mfma_f32_16x16x32_bf16 v[136:139], a[104:107], v[116:119], v[136:139]\n" \
"v_exp_f32_e32 v91, v91\n" \
"v_mfma_f32_16x16x32_bf16 v[140:143], a[108:111], v[116:119], v[140:143]\n" \
"buffer_load_dwordx4 v5, s[84:87], s92 offen offset:1024 lds\n" \
"v_mfma_f32_16x16x32_bf16 v[144:147], a[112:115], v[116:119], v[144:147]\n" \
"v_exp_f32_e32 v92, v92\n" \
"v_mfma_f32_16x16x32_bf16 v[148:151], a[116:119], v[116:119], v[148:151]\n" \
"v_exp_f32_e32 v93, v93\n" \
"v_mfma_f32_16x16x32_bf16 v[152:155], a[120:123], v[116:119], v[152:155]\n" \
"buffer_load_dwordx4 v6, s[84:87], s92 offen offset:2048 lds\n" \
"v_mfma_f32_16x16x32_bf16 v[156:159], a[124:127], v[116:119], v[156:159]\n" \
"v_exp_f32_e32 v94, v94\n" \
"v_mfma_f32_16x16x32_bf16 a[192:195], a[252:255], v[116:119], a[192:195]\n" \
"v_exp_f32_e32 v95, v95\n" \
"v_cvt_pk_bf16_f32 v124, v88, v89\n" \
"v_cvt_pk_bf16_f32 v125, v90, v91\n" \
"v_cvt_pk_bf16_f32 v126, v92, v93\n" \
"v_cvt_pk_bf16_f32 v127, v94, v95\n" \
"v_add_f32_e32 v160, v160, v224\n" \
"v_add_f32_e32 v161, v161, v225\n" \
"v_add_f32_e32 v162, v162, v226\n" \
"v_add_f32_e32 v163, v163, v227\n" \
"v_add_f32_e32 v164, v164, v228\n" \
"v_add_f32_e32 v165, v165, v229\n" \
"v_mfma_f32_16x16x32_bf16 v[160:163], a[96:99], v[124:127], v[160:163]\n" \
"buffer_load_dwordx4 v7, s[84:87], s92 offen offset:3072 lds\n" \
"s_add_u32 m0, s93, 0x2000\n" \
"v_add_f32_e32 v166, v166, v230\n" \
"v_add_f32_e32 v167, v167, v231\n" \
"v_add_f32_e32 v168, v168, v232\n" \
"v_add_f32_e32 v169, v169, v233\n" \
"v_mfma_f32_16x16x32_bf16 v[164:167], a[100:103], v[124:127], v[164:167]\n" \
"v_add_f32_e32 v170, v170, v234\n" \
"v_add_f32_e32 v171, v171, v235\n" \
"v_add_f32_e32 v172, v172, v236\n" \
"v_add_f32_e32 v173, v173, v237\n" \
"v_mfma_f32_16x16x32_bf16 v[168:171], a[104:107], v[124:127], v[168:171]\n" \
"buffer_load_dwordx4 v8, s[84:87], s92 offen offset:0 lds\n" \
"v_add_f32_e32 v174, v174, v238\n" \
"v_add_f32_e32 v175, v175, v239\n" \
"v_accvgpr_read_b32 v54, a192\n" \
"v_accvgpr_read_b32 v23, a193\n" \
"v_mfma_f32_16x16x32_bf16 v[172:175], a[108:111], v[124:127], v[172:175]\n" \
"v_add_f32_e32 v54, v54, v23\n" \
"s_waitcnt lgkmcnt(0)\n" \
"v_add_f32_e32 v54, v54, v42\n" \
"v_max_f32_e32 v62, v62, v54\n" \
"v_add_f32_e32 v176, v176, v240\n" \
"v_add_f32_e32 v177, v177, v241\n" \
"v_add_f32_e32 v178, v178, v242\n" \
"v_add_f32_e32 v179, v179, v243\n" \
"v_rcp_f32_e32 v54, v54\n" \
"v_add_f32_e32 v128, v128, v192\n" \
"v_mfma_f32_16x16x32_bf16 v[176:179], a[112:115], v[124:127], v[176:179]\n" \
"v_add_f32_e32 v129, v129, v193\n" \
"v_add_f32_e32 v130, v130, v194\n" \
"v_add_f32_e32 v131, v131, v195\n" \
"v_add_f32_e32 v132, v132, v196\n" \
"v_add_f32_e32 v133, v133, v197\n" \
"v_add_f32_e32 v134, v134, v198\n" \
"v_add_f32_e32 v135, v135, v199\n" \
"v_mul_f32_e32 v54, v54, v58\n" \
"v_mul_f32_e32 v128, v54, v128\n" \
"v_mul_f32_e32 v129, v54, v129\n" \
"v_mul_f32_e32 v130, v54, v130\n" \
"v_mul_f32_e32 v131, v54, v131\n" \
"v_mul_f32_e32 v132, v54, v132\n" \
"v_mul_f32_e32 v133, v54, v133\n" \
"v_mul_f32_e32 v134, v54, v134\n" \
"v_mul_f32_e32 v135, v54, v135\n" \
"v_add_f32_e32 v180, v180, v244\n" \
"v_add_f32_e32 v181, v181, v245\n" \
"v_add_f32_e32 v182, v182, v246\n" \
"v_add_f32_e32 v183, v183, v247\n" \
"v_add_f32_e32 v136, v136, v200\n" \
"v_add_f32_e32 v137, v137, v201\n" \
"v_add_f32_e32 v138, v138, v202\n" \
"v_add_f32_e32 v139, v139, v203\n" \
"v_add_f32_e32 v140, v140, v204\n" \
"v_add_f32_e32 v141, v141, v205\n" \
"v_add_f32_e32 v142, v142, v206\n" \
"v_add_f32_e32 v143, v143, v207\n" \
"v_cvt_pk_bf16_f32 v64, v128, v129\n" \
"v_cvt_pk_bf16_f32 v65, v130, v131\n" \
"v_cvt_pk_bf16_f32 v66, v132, v133\n" \
"v_cvt_pk_bf16_f32 v67, v134, v135\n" \
"buffer_store_dwordx4 v[64:67], v20, s[56:59], s68 offen offset:0 sc1\n" \
"v_mfma_f32_16x16x32_bf16 v[180:183], a[116:119], v[124:127], v[180:183]\n" \
"v_mfma_f32_16x16x32_bf16 a[208:211], a[216:219], v[64:67], a[208:211]\n" \
"v_mul_f32_e32 v136, v54, v136\n" \
"v_mul_f32_e32 v137, v54, v137\n" \
"v_mul_f32_e32 v138, v54, v138\n" \
"v_mul_f32_e32 v139, v54, v139\n" \
"v_mul_f32_e32 v140, v54, v140\n" \
"v_mul_f32_e32 v141, v54, v141\n" \
"v_mul_f32_e32 v142, v54, v142\n" \
"v_mul_f32_e32 v143, v54, v143\n" \
"v_add_f32_e32 v144, v144, v208\n" \
"v_add_f32_e32 v145, v145, v209\n" \
"v_add_f32_e32 v146, v146, v210\n" \
"v_add_f32_e32 v147, v147, v211\n" \
"v_add_f32_e32 v148, v148, v212\n" \
"v_add_f32_e32 v149, v149, v213\n" \
"v_add_f32_e32 v150, v150, v214\n" \
"v_add_f32_e32 v151, v151, v215\n" \
"v_cvt_pk_bf16_f32 v68, v136, v137\n" \
"v_cvt_pk_bf16_f32 v69, v138, v139\n" \
"v_cvt_pk_bf16_f32 v70, v140, v141\n" \
"v_cvt_pk_bf16_f32 v71, v142, v143\n" \
"buffer_store_dwordx4 v[68:71], v20, s[56:59], s68 offen offset:1024 sc1\n" \
"v_mul_f32_e32 v144, v54, v144\n" \
"v_mfma_f32_16x16x32_bf16 a[208:211], a[216:219], v[68:71], a[208:211]\n" \
"v_mul_f32_e32 v145, v54, v145\n" \
"v_mul_f32_e32 v146, v54, v146\n" \
"v_mul_f32_e32 v147, v54, v147\n" \
"v_mul_f32_e32 v148, v54, v148\n" \
"v_mul_f32_e32 v149, v54, v149\n" \
"v_mul_f32_e32 v150, v54, v150\n" \
"v_mul_f32_e32 v151, v54, v151\n" \
"v_add_f32_e32 v184, v184, v248\n" \
"v_add_f32_e32 v185, v185, v249\n" \
"v_add_f32_e32 v186, v186, v250\n" \
"v_add_f32_e32 v187, v187, v251\n" \
"v_add_f32_e32 v152, v152, v216\n" \
"v_add_f32_e32 v153, v153, v217\n" \
"v_add_f32_e32 v154, v154, v218\n" \
"v_add_f32_e32 v155, v155, v219\n" \
"v_add_f32_e32 v156, v156, v220\n" \
"v_add_f32_e32 v157, v157, v221\n" \
"v_add_f32_e32 v158, v158, v222\n" \
"v_add_f32_e32 v159, v159, v223\n" \
"v_cvt_pk_bf16_f32 v72, v144, v145\n" \
"v_cvt_pk_bf16_f32 v73, v146, v147\n" \
"v_cvt_pk_bf16_f32 v74, v148, v149\n" \
"v_cvt_pk_bf16_f32 v75, v150, v151\n" \
"buffer_store_dwordx4 v[72:75], v20, s[56:59], s68 offen offset:2048 sc1\n" \
"v_mfma_f32_16x16x32_bf16 v[184:187], a[120:123], v[124:127], v[184:187]\n" \
"v_mfma_f32_16x16x32_bf16 a[208:211], a[216:219], v[72:75], a[208:211]\n" \
"v_mul_f32_e32 v152, v54, v152\n" \
"v_mul_f32_e32 v153, v54, v153\n" \
"v_mul_f32_e32 v154, v54, v154\n" \
"v_mul_f32_e32 v155, v54, v155\n" \
"v_mul_f32_e32 v156, v54, v156\n" \
"v_mul_f32_e32 v157, v54, v157\n" \
"v_mul_f32_e32 v158, v54, v158\n" \
"v_mul_f32_e32 v159, v54, v159\n" \
"v_cvt_pk_bf16_f32 v76, v152, v153\n" \
"v_cvt_pk_bf16_f32 v77, v154, v155\n" \
"v_cvt_pk_bf16_f32 v78, v156, v157\n" \
"v_cvt_pk_bf16_f32 v79, v158, v159\n" \
"buffer_store_dwordx4 v[76:79], v20, s[56:59], s68 offen offset:3072 sc1\n" \
"v_add_f32_e32 v188, v188, v252\n" \
"v_mfma_f32_16x16x32_bf16 a[208:211], a[216:219], v[76:79], a[208:211]\n" \
"v_add_f32_e32 v189, v189, v253\n" \
"v_add_f32_e32 v190, v190, v254\n" \
"v_add_f32_e32 v191, v191, v255\n" \
"s_nop 1\n" \
"v_mfma_f32_16x16x32_bf16 v[188:191], a[124:127], v[124:127], v[188:191]\n" \
"v_mfma_f32_16x16x32_bf16 a[196:199], a[252:255], v[124:127], a[196:199]\n" \
"s_nop 7\n" \
"v_accvgpr_read_b32 v55, a196\n" \
"v_accvgpr_read_b32 v23, a197\n" \
"v_add_f32_e32 v55, v55, v23\n" \
"v_add_f32_e32 v55, v55, v43\n" \
"v_max_f32_e32 v62, v62, v55\n" \
"v_rcp_f32_e32 v55, v55\n" \
"v_cmp_lt_f32_e64 s[72:73], s100, v62\n" \
"v_mul_f32_e32 v55, v55, v59\n" \
"v_mul_f32_e32 v160, v55, v160\n" \
"v_mul_f32_e32 v161, v55, v161\n" \
"v_mul_f32_e32 v162, v55, v162\n" \
"v_mul_f32_e32 v163, v55, v163\n" \
"v_mul_f32_e32 v164, v55, v164\n" \
"v_mul_f32_e32 v165, v55, v165\n" \
"v_mul_f32_e32 v166, v55, v166\n" \
"v_mul_f32_e32 v167, v55, v167\n" \
"v_cvt_pk_bf16_f32 v80, v160, v161\n" \
"v_cvt_pk_bf16_f32 v81, v162, v163\n" \
"v_cvt_pk_bf16_f32 v82, v164, v165\n" \
"v_cvt_pk_bf16_f32 v83, v166, v167\n" \
"buffer_store_dwordx4 v[80:83], v20, s[56:59], s69 offen offset:0 sc1\n" \
"v_mul_f32_e32 v168, v55, v168\n" \
"v_mfma_f32_16x16x32_bf16 a[208:211], a[216:219], v[80:83], a[208:211]\n" \
"v_mul_f32_e32 v169, v55, v169\n" \
"v_mul_f32_e32 v170, v55, v170\n" \
"v_mul_f32_e32 v171, v55, v171\n" \
"v_mul_f32_e32 v172, v55, v172\n" \
"v_mul_f32_e32 v173, v55, v173\n" \
"v_mul_f32_e32 v174, v55, v174\n" \
"v_mul_f32_e32 v175, v55, v175\n" \
"v_cvt_pk_bf16_f32 v84, v168, v169\n" \
"v_cvt_pk_bf16_f32 v85, v170, v171\n" \
"v_cvt_pk_bf16_f32 v86, v172, v173\n" \
"v_cvt_pk_bf16_f32 v87, v174, v175\n" \
"buffer_store_dwordx4 v[84:87], v20, s[56:59], s69 offen offset:1024 sc1\n" \
"v_mul_f32_e32 v176, v55, v176\n" \
"v_mfma_f32_16x16x32_bf16 a[208:211], a[216:219], v[84:87], a[208:211]\n" \
"v_mul_f32_e32 v177, v55, v177\n" \
"v_mul_f32_e32 v178, v55, v178\n" \
"v_mul_f32_e32 v179, v55, v179\n" \
"v_mul_f32_e32 v180, v55, v180\n" \
"v_mul_f32_e32 v181, v55, v181\n" \
"v_mul_f32_e32 v182, v55, v182\n" \
"v_mul_f32_e32 v183, v55, v183\n" \
"v_cvt_pk_bf16_f32 v96, v176, v177\n" \
"v_cvt_pk_bf16_f32 v97, v178, v179\n" \
"v_cvt_pk_bf16_f32 v98, v180, v181\n" \
"v_cvt_pk_bf16_f32 v99, v182, v183\n" \
"buffer_store_dwordx4 v[96:99], v20, s[56:59], s69 offen offset:2048 sc1\n" \
"v_mul_f32_e32 v184, v55, v184\n" \
"v_mfma_f32_16x16x32_bf16 a[208:211], a[216:219], v[96:99], a[208:211]\n" \
"v_mul_f32_e32 v185, v55, v185\n" \
"v_mul_f32_e32 v186, v55, v186\n" \
"v_mul_f32_e32 v187, v55, v187\n" \
"v_mul_f32_e32 v188, v55, v188\n" \
"v_mul_f32_e32 v189, v55, v189\n" \
"v_mul_f32_e32 v190, v55, v190\n" \
"v_mul_f32_e32 v191, v55, v191\n" \
"v_cvt_pk_bf16_f32 v100, v184, v185\n" \
"v_cvt_pk_bf16_f32 v101, v186, v187\n" \
"v_cvt_pk_bf16_f32 v102, v188, v189\n" \
"v_cvt_pk_bf16_f32 v103, v190, v191\n" \
"buffer_store_dwordx4 v[100:103], v20, s[56:59], s69 offen offset:3072 sc1\n" \
"s_nop 0\n" \
"v_mfma_f32_16x16x32_bf16 a[208:211], a[216:219], v[100:103], a[208:211]\n" \
"s_nop 7\n" \
"v_accvgpr_read_b32 v63, a208\n" \
"v_cmp_u_f32_e32 vcc, v63, v63\n" \
"s_nop 0\n" \
"s_or_b64 vcc, s[72:73], vcc\n" \
"s_cbranch_scc1 L_end_%=\n" \
"L_out_%=:\n" \
"L_pf19:\n" \
"s_cbranch_execz L_retA\n" \
"s_nop 0\n" \
"v_add_u32_e32 v34, 0x20000, v10\n" \
"v_mov_b32_e32 v39, s62\n" \
"v_readlane_b32 s70, v11, 4\n" \
"v_bfe_u32 v38, v20, 2, 4\n" \
"v_readlane_b32 s64, v11, 1\n" \
"v_readlane_b32 s71, v11, 3\n" \
"v_add_u32_e32 v33, 0xfc00, v10\n" \
"v_readlane_b32 s99, v11, 19\n" \
"v_readlane_b32 s80, v11, 9\n" \
"v_readlane_b32 s81, v11, 10\n" \
"s_mov_b32 s82, 0x47fca0\n" \
"s_mov_b32 s83, 0x20000\n" \
"ds_read_b64_tr_b16 a[0:1], v34 offset:0\n" \
"ds_read_b64_tr_b16 a[2:3], v34 offset:4608\n" \
"ds_read_b64_tr_b16 a[4:5], v34 offset:9216\n" \
"ds_read_b64_tr_b16 a[6:7], v34 offset:13824\n" \
"ds_read_b64_tr_b16 a[8:9], v34 offset:18432\n" \
"ds_read_b64_tr_b16 a[10:11], v34 offset:23040\n" \
"ds_read_b64_tr_b16 a[12:13], v34 offset:32\n" \
"ds_read_b64_tr_b16 a[14:15], v34 offset:4640\n" \
"ds_read_b64_tr_b16 a[16:17], v34 offset:9248\n" \
"ds_read_b64_tr_b16 a[18:19], v34 offset:13856\n" \
"buffer_load_dword v37, v38, s[56:59], s64 offen sc1\n" \
"ds_read_b64_tr_b16 a[20:21], v34 offset:18464\n" \
"ds_read_b64_tr_b16 a[22:23], v34 offset:23072\n" \
"ds_read_b64_tr_b16 a[24:25], v34 offset:64\n" \
"ds_read_b64_tr_b16 a[26:27], v34 offset:4672\n" \
"ds_read_b64_tr_b16 a[28:29], v34 offset:9280\n" \
"ds_read_b64_tr_b16 a[30:31], v34 offset:13888\n" \
"ds_read_b64_tr_b16 a[192:193], v34 offset:18496\n" \
"ds_read_b64_tr_b16 a[194:195], v34 offset:23104\n" \
"ds_read_b64_tr_b16 a[196:197], v34 offset:96\n" \
"ds_read_b64_tr_b16 a[198:199], v34 offset:4704\n" \
"ds_read_b64_tr_b16 a[200:201], v34 offset:9312\n" \
"ds_read_b64_tr_b16 a[202:203], v34 offset:13920\n" \
"ds_read_b64_tr_b16 a[204:205], v34 offset:18528\n" \
"ds_read_b64_tr_b16 a[206:207], v34 offset:23136\n" \
"ds_read_b64_tr_b16 a[208:209], v34 offset:128\n" \
"ds_read_b64_tr_b16 a[210:211], v34 offset:4736\n" \
"ds_read_b64_tr_b16 a[212:213], v34 offset:9344\n" \
"ds_read_b64_tr_b16 a[214:215], v34 offset:13952\n" \
"ds_read_b64_tr_b16 a[216:217], v34 offset:18560\n" \
"ds_read_b64_tr_b16 a[218:219], v34 offset:23168\n" \
"v_readlane_b32 s65, v11, 23\n" \
"s_add_u32 m0, s99, 0x6c00\n" \
"v_readlane_b32 s66, v11, 26\n" \
"v_add_u32_e32 v32, 0x49000, v20\n" \
"v_lshrrev_b32_e32 v44, 9, v20\n" \
"v_mul_u32_u24_e32 v44, 0xb40, v44\n" \
"v_add_u32_e32 v35, v44, v10\n" \
"v_readlane_b32 s67, v11, 24\n" \
"v_readlane_b32 s92, v11, 20\n" \
"v_readlane_b32 s73, v11, 25\n" \
"s_waitcnt vmcnt(18) lgkmcnt(15)\n" \
"v_mfma_f32_16x16x32_bf16 v[112:115], a[0:3], a[220:223], 0\n" \
"buffer_load_dwordx4 v0, s[80:83], s65 offen offset:0 lds\n" \
"v_mfma_f32_16x16x32_bf16 v[112:115], a[4:7], a[224:227], v[112:115]\n" \
"v_readlane_b32 s94, v11, 21\n" \
"v_readlane_b32 s84, v11, 7\n" \
"v_mfma_f32_16x16x32_bf16 v[112:115], a[8:11], a[228:231], v[112:115]\n" \
"v_readlane_b32 s85, v11, 8\n" \
"s_mov_b32 s86, 0xa1ca0\n" \
"v_mfma_f32_16x16x32_bf16 v[116:119], a[8:11], a[244:247], 0\n" \
"s_mov_b32 s87, 0x20000\n" \
"v_readlane_b32 s68, v11, 27\n" \
"v_mfma_f32_16x16x32_bf16 v[116:119], a[4:7], a[240:243], v[116:119]\n" \
"v_readlane_b32 s69, v11, 28\n" \
"v_readlane_b32 s72, v11, 30\n" \
"v_mfma_f32_16x16x32_bf16 v[116:119], a[0:3], a[236:239], v[116:119]\n" \
"buffer_load_dwordx4 v1, s[80:83], s65 offen offset:1024 lds\n" \
"v_mfma_f32_16x16x32_bf16 v[184:187], a[0:3], v[64:67], 0\n" \
"v_readlane_b32 s93, v11, 31\n" \
"v_lshlrev_b32_e32 v41, 12, v20\n" \
"v_mfma_f32_16x16x32_bf16 v[184:187], a[4:7], v[68:71], v[184:187]\n" \
"v_readlane_b32 s63, v11, 37\n" \
"v_bfe_u32 v40, v20, 2, 7\n" \
"v_mfma_f32_16x16x32_bf16 v[184:187], a[8:11], v[72:75], v[184:187]\n" \
"s_add_u32 s74, s63, 0x3800\n" \
"v_readlane_b32 s95, v11, 34\n" \
"v_mfma_f32_16x16x32_bf16 v[188:191], a[8:11], v[96:99], 0\n" \
"ds_read_b64_tr_b16 a[8:9], v34 offset:18592\n" \
"ds_read_b64_tr_b16 a[10:11], v34 offset:23200\n" \
"v_mfma_f32_16x16x32_bf16 v[188:191], a[4:7], v[84:87], v[188:191]\n" \
"ds_read_b64_tr_b16 a[4:5], v34 offset:9376\n" \
"ds_read_b64_tr_b16 a[6:7], v34 offset:13984\n" \
"v_mfma_f32_16x16x32_bf16 v[188:191], a[0:3], v[80:83], v[188:191]\n" \
"ds_read_b64_tr_b16 a[0:1], v34 offset:160\n" \
"ds_read_b64_tr_b16 a[2:3], v34 offset:4768\n" \
"v_mfma_f32_16x16x32_bf16 v[120:123], a[12:15], a[220:223], 0\n" \
"buffer_load_dwordx4 v2, s[80:83], s65 offen offset:2048 lds\n" \
"v_mfma_f32_16x16x32_bf16 v[120:123], a[16:19], a[224:227], v[120:123]\n" \
"v_readlane_b32 s96, v11, 35\n" \
"v_readlane_b32 s97, v11, 36\n" \
"v_mfma_f32_16x16x32_bf16 v[120:123], a[20:23], a[228:231], v[120:123]\n" \
"v_readlane_b32 s100, v11, 6\n" \
"v_bfe_u32 v44, v20, 4, 4\n" \
"v_mfma_f32_16x16x32_bf16 v[124:127], a[20:23], a[244:247], 0\n" \
"v_bfe_u32 v45, v20, 8, 2\n" \
"v_mul_u32_u24_e32 v42, 0x480, v44\n" \
"v_mfma_f32_16x16x32_bf16 v[124:127], a[16:19], a[240:243], v[124:127]\n" \
"v_lshl_add_u32 v42, v45, 3, v42\n" \
"v_lshrrev_b32_e32 v44, 1, v44\n" \
"v_mfma_f32_16x16x32_bf16 v[124:127], a[12:15], a[236:239], v[124:127]\n" \
"buffer_load_dwordx4 v3, s[80:83], s65 offen offset:3072 lds\n" \
"v_mfma_f32_16x16x32_bf16 v[192:195], a[12:15], v[64:67], 0\n" \
"s_add_u32 m0, s99, 0x7c00\n" \
"v_lshlrev_b32_e32 v45, 1, v45\n" \
"v_mfma_f32_16x16x32_bf16 v[192:195], a[16:19], v[68:71], v[192:195]\n" \
"v_sub_u32_e32 v44, v44, v45\n" \
"v_and_b32_e32 v45, 16, v20\n" \
"v_mfma_f32_16x16x32_bf16 v[192:195], a[20:23], v[72:75], v[192:195]\n" \
"v_mov_b32_e32 v46, 0x3f80\n" \
"v_lshlrev_b32_e32 v45, v45, v46\n" \
"v_mfma_f32_16x16x32_bf16 v[196:199], a[20:23], v[96:99], 0\n" \
"ds_read_b64_tr_b16 a[20:21], v34 offset:18624\n" \
"ds_read_b64_tr_b16 a[22:23], v34 offset:23232\n" \
"v_mfma_f32_16x16x32_bf16 v[196:199], a[16:19], v[84:87], v[196:199]\n" \
"ds_read_b64_tr_b16 a[16:17], v34 offset:9408\n" \
"ds_read_b64_tr_b16 a[18:19], v34 offset:14016\n" \
"v_mfma_f32_16x16x32_bf16 v[196:199], a[12:15], v[80:83], v[196:199]\n" \
"ds_read_b64_tr_b16 a[12:13], v34 offset:192\n" \
"ds_read_b64_tr_b16 a[14:15], v34 offset:4800\n" \
"v_mfma_f32_16x16x32_bf16 v[128:131], a[24:27], a[220:223], 0\n" \
"v_mov_b32_e32 v52, 0\n" \
"s_waitcnt lgkmcnt(8)\n" \
"v_mfma_f32_16x16x32_bf16 v[128:131], a[28:31], a[224:227], v[128:131]\n" \
"buffer_load_dwordx4 v4, s[80:83], s65 offen offset:0 lds\n" \
"v_mfma_f32_16x16x32_bf16 v[128:131], a[192:195], a[228:231], v[128:131]\n" \
"v_mov_b32_e32 v53, 0\n" \
"v_mov_b32_e32 v48, 0\n" \
"v_mfma_f32_16x16x32_bf16 v[132:135], a[192:195], a[244:247], 0\n" \
"v_mov_b32_e32 v49, 0\n" \
"v_add_u32_e32 v43, 0x9000, v42\n" \
"v_mfma_f32_16x16x32_bf16 v[132:135], a[28:31], a[240:243], v[132:135]\n" \
"v_readlane_b32 s88, v11, 11\n" \
"v_readlane_b32 s89, v11, 12\n" \
"v_mfma_f32_16x16x32_bf16 v[132:135], a[24:27], a[236:239], v[132:135]\n" \
"s_mov_b32 s90, 0x240000\n" \
"s_mov_b32 s91, 0x20000\n" \
"v_mfma_f32_16x16x32_bf16 v[200:203], a[24:27], v[64:67], 0\n" \
"buffer_load_dwordx4 v5, s[80:83], s65 offen offset:1024 lds\n" \
"v_mfma_f32_16x16x32_bf16 v[200:203], a[28:31], v[68:71], v[200:203]\n" \
"v_mov_b32_e32 v46, s62\n" \
"v_mfma_f32_16x16x32_bf16 v[200:203], a[192:195], v[72:75], v[200:203]\n" \
"v_mfma_f32_16x16x32_bf16 v[204:207], a[192:195], v[96:99], 0\n" \
"ds_read_b64_tr_b16 a[192:193], v34 offset:18656\n" \
"ds_read_b64_tr_b16 a[194:195], v34 offset:23264\n" \
"v_mfma_f32_16x16x32_bf16 v[204:207], a[28:31], v[84:87], v[204:207]\n" \
"ds_read_b64_tr_b16 a[28:29], v34 offset:9440\n" \
"ds_read_b64_tr_b16 a[30:31], v34 offset:14048\n" \
"v_mfma_f32_16x16x32_bf16 v[204:207], a[24:27], v[80:83], v[204:207]\n" \
"ds_read_b64_tr_b16 a[24:25], v34 offset:224\n" \
"ds_read_b64_tr_b16 a[26:27], v34 offset:4832\n" \
"v_mfma_f32_16x16x32_bf16 v[136:139], a[196:199], a[220:223], 0\n" \
"buffer_load_dwordx4 v6, s[80:83], s65 offen offset:2048 lds\n" \
"v_mfma_f32_16x16x32_bf16 v[136:139], a[200:203], a[224:227], v[136:139]\n" \
"v_mfma_f32_16x16x32_bf16 v[136:139], a[204:207], a[228:231], v[136:139]\n" \
"v_mfma_f32_16x16x32_bf16 v[140:143], a[204:207], a[244:247], 0\n" \
"v_mfma_f32_16x16x32_bf16 v[140:143], a[200:203], a[240:243], v[140:143]\n" \
"v_mfma_f32_16x16x32_bf16 v[140:143], a[196:199], a[236:239], v[140:143]\n" \
"buffer_load_dwordx4 v7, s[80:83], s65 offen offset:3072 lds\n" \
"v_mfma_f32_16x16x32_bf16 v[208:211], a[196:199], v[64:67], 0\n" \
"s_add_u32 m0, s99, 0x8c00\n" \
"v_mfma_f32_16x16x32_bf16 v[208:211], a[200:203], v[68:71], v[208:211]\n" \
"v_mfma_f32_16x16x32_bf16 v[208:211], a[204:207], v[72:75], v[208:211]\n" \
"v_mfma_f32_16x16x32_bf16 v[212:215], a[204:207], v[96:99], 0\n" \
"ds_read_b64_tr_b16 a[204:205], v34 offset:18688\n" \
"ds_read_b64_tr_b16 a[206:207], v34 offset:23296\n" \
"v_mfma_f32_16x16x32_bf16 v[212:215], a[200:203], v[84:87], v[212:215]\n" \
"ds_read_b64_tr_b16 a[200:201], v34 offset:9472\n" \
"ds_read_b64_tr_b16 a[202:203], v34 offset:14080\n" \
"v_mfma_f32_16x16x32_bf16 v[212:215], a[196:199], v[80:83], v[212:215]\n" \
"ds_read_b64_tr_b16 a[196:197], v34 offset:256\n" \
"ds_read_b64_tr_b16 a[198:199], v34 offset:4864\n" \
"v_mfma_f32_16x16x32_bf16 v[144:147], a[208:211], a[220:223], 0\n" \
"buffer_load_dwordx4 v8, s[80:83], s65 offen offset:0 lds\n" \
"v_mfma_f32_16x16x32_bf16 v[144:147], a[212:215], a[224:227], v[144:147]\n" \
"s_waitcnt vmcnt(9)\n" \
"v_cmp_ne_u32_e32 vcc, s62, v37\n" \
"v_mfma_f32_16x16x32_bf16 v[144:147], a[216:219], a[228:231], v[144:147]\n" \
"s_add_u32 m0, s99, 0x14400\n" \
"v_readlane_b32 s65, v11, 42\n" \
"v_mfma_f32_16x16x32_bf16 v[148:151], a[216:219], a[244:247], 0\n" \
"v_mfma_f32_16x16x32_bf16 v[148:151], a[212:215], a[240:243], v[148:151]\n" \
"s_cbranch_vccnz L_sa_%=\n" \
"L_ra_%=:\n" \
"v_readlane_b32 s64, v11, 29\n" \
"v_mfma_f32_16x16x32_bf16 v[148:151], a[208:211], a[236:239], v[148:151]\n" \
"buffer_load_dwordx4 a[64:67], v20, s[56:59], s66 offen offset:0 sc1\n" \
"v_mfma_f32_16x16x32_bf16 v[216:219], a[208:211], v[64:67], 0\n" \
"v_mfma_f32_16x16x32_bf16 v[216:219], a[212:215], v[68:71], v[216:219]\n" \
"v_mfma_f32_16x16x32_bf16 v[216:219], a[216:219], v[72:75], v[216:219]\n" \
"v_mfma_f32_16x16x32_bf16 v[220:223], a[216:219], v[96:99], 0\n" \
"v_mfma_f32_16x16x32_bf16 v[220:223], a[212:215], v[84:87], v[220:223]\n" \
"buffer_load_dwordx4 a[68:71], v20, s[56:59], s66 offen offset:1024 sc1\n" \
"v_mfma_f32_16x16x32_bf16 v[220:223], a[208:211], v[80:83], v[220:223]\n" \
"s_waitcnt lgkmcnt(5)\n" \
"v_mfma_f32_16x16x32_bf16 v[152:155], a[0:3], a[220:223], 0\n" \
"v_mfma_f32_16x16x32_bf16 v[152:155], a[4:7], a[224:227], v[152:155]\n" \
"v_mfma_f32_16x16x32_bf16 v[152:155], a[8:11], a[228:231], v[152:155]\n" \
"v_mfma_f32_16x16x32_bf16 v[156:159], a[8:11], a[244:247], 0\n" \
"buffer_load_dwordx4 a[84:87], v32, s[56:59], s66 offen offset:1024 sc1\n" \
"v_mfma_f32_16x16x32_bf16 v[156:159], a[4:7], a[240:243], v[156:159]\n" \
"v_mfma_f32_16x16x32_bf16 v[156:159], a[0:3], a[236:239], v[156:159]\n" \
"v_mfma_f32_16x16x32_bf16 v[224:227], a[0:3], v[64:67], 0\n" \
"v_mfma_f32_16x16x32_bf16 v[224:227], a[4:7], v[68:71], v[224:227]\n" \
"v_mfma_f32_16x16x32_bf16 v[224:227], a[8:11], v[72:75], v[224:227]\n" \
"buffer_load_dwordx4 a[80:83], v32, s[56:59], s66 offen offset:0 sc1\n" \
"v_mfma_f32_16x16x32_bf16 v[228:231], a[8:11], v[96:99], 0\n" \
"v_mfma_f32_16x16x32_bf16 v[228:231], a[4:7], v[84:87], v[228:231]\n" \
"v_mfma_f32_16x16x32_bf16 v[228:231], a[0:3], v[80:83], v[228:231]\n" \
"v_mfma_f32_16x16x32_bf16 v[160:163], a[12:15], a[220:223], 0\n" \
"v_mfma_f32_16x16x32_bf16 v[160:163], a[16:19], a[224:227], v[160:163]\n" \
"buffer_load_dwordx4 a[72:75], v20, s[56:59], s66 offen offset:2048 sc1\n" \
"v_mfma_f32_16x16x32_bf16 v[160:163], a[20:23], a[228:231], v[160:163]\n" \
"v_mfma_f32_16x16x32_bf16 v[164:167], a[20:23], a[244:247], 0\n" \
"v_mfma_f32_16x16x32_bf16 v[164:167], a[16:19], a[240:243], v[164:167]\n" \
"v_mfma_f32_16x16x32_bf16 v[164:167], a[12:15], a[236:239], v[164:167]\n" \
"v_mfma_f32_16x16x32_bf16 v[232:235], a[12:15], v[64:67], 0\n" \
"buffer_load_dwordx4 a[76:79], v20, s[56:59], s66 offen offset:3072 sc1\n" \
"v_mfma_f32_16x16x32_bf16 v[232:235], a[16:19], v[68:71], v[232:235]\n" \
"v_mfma_f32_16x16x32_bf16 v[232:235], a[20:23], v[72:75], v[232:235]\n" \
"v_mfma_f32_16x16x32_bf16 v[236:239], a[20:23], v[96:99], 0\n" \
"v_mfma_f32_16x16x32_bf16 v[236:239], a[16:19], v[84:87], v[236:239]\n" \
"v_mfma_f32_16x16x32_bf16 v[236:239], a[12:15], v[80:83], v[236:239]\n" \
"buffer_load_dwordx4 a[92:95], v32, s[56:59], s66 offen offset:3072 sc1\n" \
"s_waitcnt lgkmcnt(0)\n" \
"s_barrier\n" \
"v_mfma_f32_16x16x32_bf16 v[168:171], a[24:27], a[220:223], 0\n" \
"ds_read_b64_tr_b16 a[208:209], v33 offset:9216\n" \
"ds_read_b64_tr_b16 a[210:211], v33 offset:13824\n" \
"v_mfma_f32_16x16x32_bf16 v[168:171], a[28:31], a[224:227], v[168:171]\n" \
"ds_read_b64_tr_b16 a[212:213], v33 offset:9248\n" \
"ds_read_b64_tr_b16 a[214:215], v33 offset:13856\n" \
"v_mfma_f32_16x16x32_bf16 v[168:171], a[192:195], a[228:231], v[168:171]\n" \
"ds_read_b64_tr_b16 a[216:217], v33 offset:9280\n" \
"ds_read_b64_tr_b16 a[218:219], v33 offset:13888\n" \
"s_mov_b64 exec, 1\n" \
"buffer_store_dword v39, off, s[56:59], s70 offset:0 sc1\n" \
"s_mov_b64 exec, -1\n" \
"v_mfma_f32_16x16x32_bf16 v[172:175], a[192:195], a[244:247], 0\n" \
"ds_read_b64_tr_b16 a[0:1], v33 offset:9312\n" \
"ds_read_b64_tr_b16 a[2:3], v33 offset:13920\n" \
"v_mfma_f32_16x16x32_bf16 v[172:175], a[28:31], a[240:243], v[172:175]\n" \
"ds_read_b64_tr_b16 a[4:5], v33 offset:9344\n" \
"ds_read_b64_tr_b16 a[6:7], v33 offset:13952\n" \
"v_mfma_f32_16x16x32_bf16 v[172:175], a[24:27], a[236:239], v[172:175]\n" \
"ds_read_b64_tr_b16 a[8:9], v33 offset:9376\n" \
"ds_read_b64_tr_b16 a[10:11], v33 offset:13984\n" \
"v_mfma_f32_16x16x32_bf16 v[240:243], a[24:27], v[64:67], 0\n" \
"ds_read_b64_tr_b16 a[12:13], v33 offset:9408\n" \
"ds_read_b64_tr_b16 a[14:15], v33 offset:14016\n" \
"v_mfma_f32_16x16x32_bf16 v[240:243], a[28:31], v[68:71], v[240:243]\n" \
"ds_read_b64_tr_b16 a[16:17], v33 offset:9440\n" \
"ds_read_b64_tr_b16 a[18:19], v33 offset:14048\n" \
"v_mfma_f32_16x16x32_bf16 v[240:243], a[192:195], v[72:75], v[240:243]\n" \
"ds_read_b64_tr_b16 a[20:21], v33 offset:9472\n" \
"ds_read_b64_tr_b16 a[22:23], v33 offset:14080\n" \
"v_mfma_f32_16x16x32_bf16 v[244:247], a[192:195], v[96:99], 0\n" \
"ds_read_b64_tr_b16 a[192:193], v35 offset:32\n" \
"ds_read_b64_tr_b16 a[194:195], v35 offset:2336\n" \
"v_mfma_f32_16x16x32_bf16 v[244:247], a[28:31], v[84:87], v[244:247]\n" \
"ds_read_b64_tr_b16 a[28:29], v35 offset:10368\n" \
"ds_read_b64_tr_b16 a[30:31], v35 offset:12672\n" \
"v_mfma_f32_16x16x32_bf16 v[244:247], a[24:27], v[80:83], v[244:247]\n" \
"ds_read_b64_tr_b16 a[24:25], v35 offset:0\n" \
"ds_read_b64_tr_b16 a[26:27], v35 offset:2304\n" \
"v_mfma_f32_16x16x32_bf16 v[176:179], a[196:199], a[220:223], 0\n" \
"buffer_load_dwordx4 a[88:91], v32, s[56:59], s66 offen offset:2048 sc1\n" \
"v_mfma_f32_16x16x32_bf16 v[176:179], a[200:203], a[224:227], v[176:179]\n" \
"v_readlane_b32 s66, v11, 43\n" \
"v_readlane_b32 s70, v11, 45\n" \
"v_mfma_f32_16x16x32_bf16 v[176:179], a[204:207], a[228:231], v[176:179]\n" \
"v_mfma_f32_16x16x32_bf16 v[180:183], a[204:207], a[244:247], 0\n" \
"v_mfma_f32_16x16x32_bf16 v[180:183], a[200:203], a[240:243], v[180:183]\n" \
"v_mfma_f32_16x16x32_bf16 v[180:183], a[196:199], a[236:239], v[180:183]\n" \
"buffer_load_dwordx4 v0, s[80:83], s67 offen offset:0 lds\n" \
"v_mfma_f32_16x16x32_bf16 v[248:251], a[196:199], v[64:67], 0\n" \
"v_mfma_f32_16x16x32_bf16 v[248:251], a[200:203], v[68:71], v[248:251]\n" \
"v_mfma_f32_16x16x32_bf16 v[248:251], a[204:207], v[72:75], v[248:251]\n" \
"v_mfma_f32_16x16x32_bf16 v[252:255], a[204:207], v[96:99], 0\n" \
"ds_read_b64_tr_b16 a[204:205], v35 offset:10432\n" \
"L_pf20:\n" \
"s_cbranch_execz L_pf22\n" \
"s_nop 0\n" \
"ds_read_b64_tr_b16 a[206:207], v35 offset:12736\n" \
"v_mfma_f32_16x16x32_bf16 v[252:255], a[200:203], v[84:87], v[252:255]\n" \
"ds_read_b64_tr_b16 a[200:201], v35 offset:64\n" \
"ds_read_b64_tr_b16 a[202:203], v35 offset:2368\n" \
"v_mfma_f32_16x16x32_bf16 v[252:255], a[196:199], v[80:83], v[252:255]\n" \
"ds_read_b64_tr_b16 a[196:197], v35 offset:10400\n" \
"s_waitcnt lgkmcnt(9)\n" \
"v_mfma_f32_16x16x32_bf16 v[112:115], a[208:211], a[232:235], v[112:115]\n" \
"buffer_load_dwordx4 v1, s[80:83], s67 offen offset:1024 lds\n" \
"v_mfma_f32_16x16x32_bf16 v[116:119], a[208:211], a[248:251], v[116:119]\n" \
"ds_read_b64_tr_b16 a[198:199], v35 offset:12704\n" \
"v_mfma_f32_16x16x32_bf16 v[184:187], a[208:211], v[76:79], v[184:187]\n" \
"v_mfma_f32_16x16x32_bf16 v[188:191], a[208:211], v[100:103], v[188:191]\n" \
"ds_read_b64_tr_b16 a[208:209], v35 offset:96\n" \
"ds_read_b64_tr_b16 a[210:211], v35 offset:2400\n" \
"v_mfma_f32_16x16x32_bf16 v[120:123], a[212:215], a[232:235], v[120:123]\n" \
"v_mfma_f32_16x16x32_bf16 v[124:127], a[212:215], a[248:251], v[124:127]\n" \
"buffer_load_dwordx4 v2, s[80:83], s67 offen offset:2048 lds\n" \
"v_mfma_f32_16x16x32_bf16 v[192:195], a[212:215], v[76:79], v[192:195]\n" \
"v_mfma_f32_16x16x32_bf16 v[196:199], a[212:215], v[100:103], v[196:199]\n" \
"ds_read_b64_tr_b16 a[212:213], v35 offset:10464\n" \
"ds_read_b64_tr_b16 a[214:215], v35 offset:12768\n" \
"v_mfma_f32_16x16x32_bf16 v[128:131], a[216:219], a[232:235], v[128:131]\n" \
"v_mfma_f32_16x16x32_bf16 v[132:135], a[216:219], a[248:251], v[132:135]\n" \
"v_mfma_f32_16x16x32_bf16 v[200:203], a[216:219], v[76:79], v[200:203]\n" \
"buffer_load_dwordx4 v3, s[80:83], s67 offen offset:3072 lds\n" \
"v_mfma_f32_16x16x32_bf16 v[204:207], a[216:219], v[100:103], v[204:207]\n" \
"ds_read_b64_tr_b16 a[216:217], v35 offset:128\n" \
"ds_read_b64_tr_b16 a[218:219], v35 offset:2432\n" \
"v_mfma_f32_16x16x32_bf16 v[136:139], a[0:3], a[232:235], v[136:139]\n" \
"s_add_u32 m0, s99, 0x15400\n" \
"v_mfma_f32_16x16x32_bf16 v[140:143], a[0:3], a[248:251], v[140:143]\n" \
"v_mfma_f32_16x16x32_bf16 v[208:211], a[0:3], v[76:79], v[208:211]\n" \
"v_mfma_f32_16x16x32_bf16 v[212:215], a[0:3], v[100:103], v[212:215]\n" \
"ds_read_b64_tr_b16 a[0:1], v35 offset:10496\n" \
"ds_read_b64_tr_b16 a[2:3], v35 offset:12800\n" \
"v_mfma_f32_16x16x32_bf16 v[144:147], a[4:7], a[232:235], v[144:147]\n" \
"buffer_load_dwordx4 v4, s[80:83], s67 offen offset:0 lds\n" \
"v_mfma_f32_16x16x32_bf16 v[148:151], a[4:7], a[248:251], v[148:151]\n" \
"v_mfma_f32_16x16x32_bf16 v[216:219], a[4:7], v[76:79], v[216:219]\n" \
"v_mfma_f32_16x16x32_bf16 v[220:223], a[4:7], v[100:103], v[220:223]\n" \
"ds_read_b64_tr_b16 a[4:5], v35 offset:160\n" \
"ds_read_b64_tr_b16 a[6:7], v35 offset:2464\n" \
"v_mfma_f32_16x16x32_bf16 v[152:155], a[8:11], a[232:235], v[152:155]\n" \
"v_mfma_f32_16x16x32_bf16 v[156:159], a[8:11], a[248:251], v[156:159]\n" \
"buffer_load_dwordx4 v5, s[80:83], s67 offen offset:1024 lds\n" \
"v_mfma_f32_16x16x32_bf16 v[224:227], a[8:11], v[76:79], v[224:227]\n" \
"v_mfma_f32_16x16x32_bf16 v[228:231], a[8:11], v[100:103], v[228:231]\n" \
"ds_read_b64_tr_b16 a[8:9], v35 offset:10528\n" \
"ds_read_b64_tr_b16 a[10:11], v35 offset:12832\n" \
"v_mfma_f32_16x16x32_bf16 v[160:163], a[12:15], a[232:235], v[160:163]\n" \
"v_mfma_f32_16x16x32_bf16 v[164:167], a[12:15], a[248:251], v[164:167]\n" \
"v_mfma_f32_16x16x32_bf16 v[232:235], a[12:15], v[76:79], v[232:235]\n" \
"buffer_load_dwordx4 v6, s[80:83], s67 offen offset:2048 lds\n" \
"v_mfma_f32_16x16x32_bf16 v[236:239], a[12:15], v[100:103], v[236:239]\n" \
"ds_read_b64_tr_b16 a[12:13], v35 offset:192\n" \
"ds_read_b64_tr_b16 a[14:15], v35 offset:2496\n" \
"v_mfma_f32_16x16x32_bf16 v[168:171], a[16:19], a[232:235], v[168:171]\n" \
"v_mfma_f32_16x16x32_bf16 v[172:175], a[16:19], a[248:251], v[172:175]\n" \
"v_mfma_f32_16x16x32_bf16 v[240:243], a[16:19], v[76:79], v[240:243]\n" \
"v_mfma_f32_16x16x32_bf16 v[244:247], a[16:19], v[100:103], v[244:247]\n" \
"buffer_load_dwordx4 v7, s[80:83], s67 offen offset:3072 lds\n" \
"v_mfma_f32_16x16x32_bf16 v[176:179], a[20:23], a[232:235], v[176:179]\n" \
"s_add_u32 m0, s99, 0x16400\n" \
"ds_read_b64_tr_b16 a[16:17], v35 offset:10560\n" \
"v_mfma_f32_16x16x32_bf16 v[180:183], a[20:23], a[248:251], v[180:183]\n" \
"ds_read_b64_tr_b16 a[18:19], v35 offset:12864\n" \
"v_mfma_f32_16x16x32_bf16 v[248:251], a[20:23], v[76:79], v[248:251]\n" \
"v_mfma_f32_16x16x32_bf16 v[252:255], a[20:23], v[100:103], v[252:255]\n" \
"ds_read_b64_tr_b16 a[20:21], v35 offset:224\n" \
"s_waitcnt lgkmcnt(7)\n" \
"v_mfma_f32_16x16x32_bf16 v[112:115], a[24:27], a[32:35], v[112:115]\n" \
"buffer_load_dwordx4 v8, s[80:83], s67 offen offset:0 lds\n" \
"v_mfma_f32_16x16x32_bf16 v[112:115], a[28:31], a[36:39], v[112:115]\n" \
"s_add_u32 m0, s92, 0x0\n" \
"ds_read_b64_tr_b16 a[22:23], v35 offset:2528\n" \
"v_mfma_f32_16x16x32_bf16 v[116:119], a[28:31], a[44:47], v[116:119]\n" \
"v_readlane_b32 s67, v11, 44\n" \
"v_mfma_f32_16x16x32_bf16 v[116:119], a[24:27], a[40:43], v[116:119]\n" \
"v_mfma_f32_16x16x32_bf16 v[184:187], a[24:27], a[48:51], v[184:187]\n" \
"v_mfma_f32_16x16x32_bf16 v[184:187], a[28:31], a[52:55], v[184:187]\n" \
"buffer_load_dword v47, v38, s[56:59], s71 offen sc1\n" \
"v_mfma_f32_16x16x32_bf16 v[188:191], a[28:31], a[60:63], v[188:191]\n" \
"ds_read_b64_tr_b16 a[28:29], v35 offset:256\n" \
"ds_read_b64_tr_b16 a[30:31], v35 offset:2560\n" \
"v_mfma_f32_16x16x32_bf16 v[188:191], a[24:27], a[56:59], v[188:191]\n" \
"ds_read_b64_tr_b16 a[24:25], v35 offset:10592\n" \
"ds_read_b64_tr_b16 a[26:27], v35 offset:12896\n" \
"v_mfma_f32_16x16x32_bf16 v[120:123], a[192:195], a[32:35], v[120:123]\n" \
"v_mfma_f32_16x16x32_bf16 v[120:123], a[196:199], a[36:39], v[120:123]\n" \
"v_mfma_f32_16x16x32_bf16 v[124:127], a[196:199], a[44:47], v[124:127]\n" \
"buffer_load_dwordx4 v0, s[80:83], s73 offen offset:0 lds\n" \
"v_mfma_f32_16x16x32_bf16 v[124:127], a[192:195], a[40:43], v[124:127]\n" \
"v_mfma_f32_16x16x32_bf16 v[192:195], a[192:195], a[48:51], v[192:195]\n" \
"v_mfma_f32_16x16x32_bf16 v[192:195], a[196:199], a[52:55], v[192:195]\n" \
"v_mfma_f32_16x16x32_bf16 v[196:199], a[196:199], a[60:63], v[196:199]\n" \
"v_mfma_f32_16x16x32_bf16 v[196:199], a[192:195], a[56:59], v[196:199]\n" \
"buffer_load_dwordx4 v1, s[80:83], s73 offen offset:1024 lds\n" \
"v_mfma_f32_16x16x32_bf16 v[128:131], a[200:203], a[32:35], v[128:131]\n" \
"ds_read_b64_tr_b16 a[192:193], v35 offset:10624\n" \
"ds_read_b64_tr_b16 a[194:195], v35 offset:12928\n" \
"v_mfma_f32_16x16x32_bf16 v[128:131], a[204:207], a[36:39], v[128:131]\n" \
"v_mfma_f32_16x16x32_bf16 v[132:135], a[204:207], a[44:47], v[132:135]\n" \
"v_mfma_f32_16x16x32_bf16 v[132:135], a[200:203], a[40:43], v[132:135]\n" \
"v_mfma_f32_16x16x32_bf16 v[200:203], a[200:203], a[48:51], v[200:203]\n" \
"buffer_load_dwordx4 v2, s[80:83], s73 offen offset:2048 lds\n" \
"v_mfma_f32_16x16x32_bf16 v[200:203], a[204:207], a[52:55], v[200:203]\n" \
"v_mfma_f32_16x16x32_bf16 v[204:207], a[204:207], a[60:63], v[204:207]\n" \
"v_mfma_f32_16x16x32_bf16 v[204:207], a[200:203], a[56:59], v[204:207]\n" \
"v_mfma_f32_16x16x32_bf16 v[136:139], a[208:211], a[32:35], v[136:139]\n" \
"v_mfma_f32_16x16x32_bf16 v[136:139], a[212:215], a[36:39], v[136:139]\n" \
"buffer_load_dwordx4 v3, s[80:83], s73 offen offset:3072 lds\n" \
"v_mfma_f32_16x16x32_bf16 v[140:143], a[212:215], a[44:47], v[140:143]\n" \
"s_add_u32 m0, s92, 0x1000\n" \
"v_mfma_f32_16x16x32_bf16 v[140:143], a[208:211], a[40:43], v[140:143]\n" \
"v_mfma_f32_16x16x32_bf16 v[208:211], a[208:211], a[48:51], v[208:211]\n" \
"v_mfma_f32_16x16x32_bf16 v[208:211], a[212:215], a[52:55], v[208:211]\n" \
"v_mfma_f32_16x16x32_bf16 v[212:215], a[212:215], a[60:63], v[212:215]\n" \
"buffer_load_dwordx4 v4, s[80:83], s73 offen offset:0 lds\n" \
"v_mfma_f32_16x16x32_bf16 v[212:215], a[208:211], a[56:59], v[212:215]\n" \
"v_mfma_f32_16x16x32_bf16 v[144:147], a[216:219], a[32:35], v[144:147]\n" \
"v_mfma_f32_16x16x32_bf16 v[144:147], a[0:3], a[36:39], v[144:147]\n" \
"v_mfma_f32_16x16x32_bf16 v[148:151], a[0:3], a[44:47], v[148:151]\n" \
"v_mfma_f32_16x16x32_bf16 v[148:151], a[216:219], a[40:43], v[148:151]\n" \
"buffer_load_dwordx4 v5, s[80:83], s73 offen offset:1024 lds\n" \
"v_mfma_f32_16x16x32_bf16 v[216:219], a[216:219], a[48:51], v[216:219]\n" \
"v_mfma_f32_16x16x32_bf16 v[216:219], a[0:3], a[52:55], v[216:219]\n" \
"v_mfma_f32_16x16x32_bf16 v[220:223], a[0:3], a[60:63], v[220:223]\n" \
"v_mfma_f32_16x16x32_bf16 v[220:223], a[216:219], a[56:59], v[220:223]\n" \
"v_mfma_f32_16x16x32_bf16 v[152:155], a[4:7], a[32:35], v[152:155]\n" \
"buffer_load_dwordx4 v6, s[80:83], s73 offen offset:2048 lds\n" \
"s_waitcnt lgkmcnt(0)\n" \
"v_mfma_f32_16x16x32_bf16 v[152:155], a[8:11], a[36:39], v[152:155]\n" \
"v_mfma_f32_16x16x32_bf16 v[156:159], a[8:11], a[44:47], v[156:159]\n" \
"v_mfma_f32_16x16x32_bf16 v[156:159], a[4:7], a[40:43], v[156:159]\n" \
"v_mfma_f32_16x16x32_bf16 v[224:227], a[4:7], a[48:51], v[224:227]\n" \
"buffer_load_dwordx4 v7, s[80:83], s73 offen offset:3072 lds\n" \
"s_add_u32 m0, s92, 0x2000\n" \
"v_mfma_f32_16x16x32_bf16 v[224:227], a[8:11], a[52:55], v[224:227]\n" \
"v_readlane_b32 s92, v11, 42\n" \
"v_mfma_f32_16x16x32_bf16 v[228:231], a[8:11], a[60:63], v[228:231]\n" \
"v_mfma_f32_16x16x32_bf16 v[228:231], a[4:7], a[56:59], v[228:231]\n" \
"s_waitcnt vmcnt(23)\n" \
"s_barrier\n" \
"buffer_load_dwordx4 v8, s[80:83], s73 offen offset:0 lds\n" \
"s_mov_b32 m0, s94\n" \
"v_mfma_f32_16x16x32_bf16 v[160:163], a[12:15], a[32:35], v[160:163]\n" \
"ds_read_b64_tr_b16 a[196:197], v10 offset:27648\n" \
"ds_read_b64_tr_b16 a[198:199], v10 offset:32256\n" \
"v_mfma_f32_16x16x32_bf16 v[160:163], a[16:19], a[36:39], v[160:163]\n" \
"ds_read_b64_tr_b16 a[200:201], v10 offset:36864\n" \
"ds_read_b64_tr_b16 a[202:203], v10 offset:41472\n" \
"v_mfma_f32_16x16x32_bf16 v[164:167], a[16:19], a[44:47], v[164:167]\n" \
"ds_read_b64_tr_b16 a[204:205], v10 offset:27680\n" \
"ds_read_b64_tr_b16 a[206:207], v10 offset:32288\n" \
"v_mfma_f32_16x16x32_bf16 v[164:167], a[12:15], a[40:43], v[164:167]\n" \
"buffer_load_dwordx4 v9, s[84:87], 0 offen lds\n" \
"v_mfma_f32_16x16x32_bf16 v[232:235], a[12:15], a[48:51], v[232:235]\n" \
"ds_read_b64_tr_b16 a[208:209], v10 offset:36896\n" \
"ds_read_b64_tr_b16 a[210:211], v10 offset:41504\n" \
"v_mfma_f32_16x16x32_bf16 v[232:235], a[16:19], a[52:55], v[232:235]\n" \
"ds_read_b64_tr_b16 a[212:213], v10 offset:27712\n" \
"ds_read_b64_tr_b16 a[214:215], v10 offset:32320\n" \
"v_mfma_f32_16x16x32_bf16 v[236:239], a[16:19], a[60:63], v[236:239]\n" \
"ds_read_b64_tr_b16 a[216:217], v10 offset:36928\n" \
"ds_read_b64_tr_b16 a[218:219], v10 offset:41536\n" \
"v_mfma_f32_16x16x32_bf16 v[236:239], a[12:15], a[56:59], v[236:239]\n" \
"ds_read_b64_tr_b16 a[0:1], v10 offset:27744\n" \
"ds_read_b64_tr_b16 a[2:3], v10 offset:32352\n" \
"buffer_load_dwordx4 a[128:131], v20, s[56:59], s68 offen offset:0 sc1\n" \
"v_mfma_f32_16x16x32_bf16 v[168:171], a[20:23], a[32:35], v[168:171]\n" \
"ds_read_b64_tr_b16 a[4:5], v10 offset:36960\n" \
"ds_read_b64_tr_b16 a[6:7], v10 offset:41568\n" \
"v_mfma_f32_16x16x32_bf16 v[168:171], a[24:27], a[36:39], v[168:171]\n" \
"ds_read_b64_tr_b16 a[8:9], v10 offset:27776\n" \
"ds_read_b64_tr_b16 a[10:11], v10 offset:32384\n" \
"v_mfma_f32_16x16x32_bf16 v[172:175], a[24:27], a[44:47], v[172:175]\n" \
"ds_read_b64_tr_b16 a[12:13], v10 offset:36992\n" \
"ds_read_b64_tr_b16 a[14:15], v10 offset:41600\n" \
"v_mfma_f32_16x16x32_bf16 v[172:175], a[20:23], a[40:43], v[172:175]\n" \
"ds_read_b64_tr_b16 a[16:17], v10 offset:27808\n" \
"ds_read_b64_tr_b16 a[18:19], v10 offset:32416\n" \
"buffer_load_dwordx4 a[132:135], v20, s[56:59], s68 offen offset:1024 sc1\n" \
"v_mfma_f32_16x16x32_bf16 v[240:243], a[20:23], a[48:51], v[240:243]\n" \
"s_waitcnt vmcnt(12)\n" \
"v_cmp_ne_u32_e32 vcc, s62, v47\n" \
"v_mfma_f32_16x16x32_bf16 v[240:243], a[24:27], a[52:55], v[240:243]\n" \
"v_readlane_b32 s73, v11, 48\n" \
"v_readlane_b32 s94, v11, 44\n" \
"v_mfma_f32_16x16x32_bf16 v[244:247], a[24:27], a[60:63], v[244:247]\n" \
"ds_read_b64_tr_b16 a[24:25], v10 offset:27840\n" \
"ds_read_b64_tr_b16 a[26:27], v10 offset:32448\n" \
"v_mfma_f32_16x16x32_bf16 v[244:247], a[20:23], a[56:59], v[244:247]\n" \
"ds_read_b64_tr_b16 a[20:21], v10 offset:37024\n" \
"ds_read_b64_tr_b16 a[22:23], v10 offset:41632\n" \
"buffer_load_dwordx4 a[136:139], v20, s[56:59], s68 offen offset:2048 sc1\n" \
"v_mfma_f32_16x16x32_bf16 v[176:179], a[28:31], a[32:35], v[176:179]\n" \
"s_cbranch_vccnz L_sb_%=\n" \
"L_rb_%=:\n" \
"v_readlane_b32 s71, v11, 46\n" \
"v_mfma_f32_16x16x32_bf16 v[176:179], a[192:195], a[36:39], v[176:179]\n" \
"v_cmp_eq_u32_e32 vcc, 0, v44\n" \
"v_mfma_f32_16x16x32_bf16 v[180:183], a[192:195], a[44:47], v[180:183]\n" \
"v_mfma_f32_16x16x32_bf16 v[180:183], a[28:31], a[40:43], v[180:183]\n" \
"v_cndmask_b32_e32 v50, 0, v45, vcc\n" \
"v_cmp_eq_u32_e32 vcc, 1, v44\n" \
"v_mfma_f32_16x16x32_bf16 v[248:251], a[28:31], a[48:51], v[248:251]\n" \
"buffer_load_dwordx4 a[140:143], v20, s[56:59], s68 offen offset:3072 sc1\n" \
"v_mfma_f32_16x16x32_bf16 v[248:251], a[192:195], a[52:55], v[248:251]\n" \
"v_cndmask_b32_e32 v51, 0, v45, vcc\n" \
"v_bfe_u32 v45, v20, 1, 5\n" \
"v_mfma_f32_16x16x32_bf16 v[252:255], a[192:195], a[60:63], v[252:255]\n" \
"ds_read_b64_tr_b16 a[192:193], v10 offset:27872\n" \
"ds_read_b64_tr_b16 a[194:195], v10 offset:32480\n" \
"v_mfma_f32_16x16x32_bf16 v[252:255], a[28:31], a[56:59], v[252:255]\n" \
"ds_read_b64_tr_b16 a[28:29], v10 offset:37056\n" \
"s_waitcnt lgkmcnt(9)\n" \
"v_mfma_f32_16x16x32_bf16 v[112:115], a[196:199], a[64:67], v[112:115]\n" \
"ds_read_b64_tr_b16 a[30:31], v10 offset:41664\n" \
"v_and_b32_e32 v45, 24, v45\n" \
"v_mfma_f32_16x16x32_bf16 v[112:115], a[200:203], a[68:71], v[112:115]\n" \
"buffer_load_dwordx4 a[156:159], v32, s[56:59], s68 offen offset:3072 sc1\n" \
"v_mfma_f32_16x16x32_bf16 v[116:119], a[200:203], a[84:87], v[116:119]\n" \
"ds_read_b64_tr_b16 a[200:201], v10 offset:27904\n" \
"ds_read_b64_tr_b16 a[202:203], v10 offset:32512\n" \
"v_mfma_f32_16x16x32_bf16 v[116:119], a[196:199], a[80:83], v[116:119]\n" \
"ds_read_b64_tr_b16 a[196:197], v10 offset:37088\n" \
"ds_read_b64_tr_b16 a[198:199], v10 offset:41696\n" \
"v_mfma_f32_16x16x32_bf16 v[120:123], a[204:207], a[64:67], v[120:123]\n" \
"v_and_b32_e32 v44, 0x3c0, v20\n" \
"v_add_u32_e32 v45, 0x1d400, v45\n" \
"v_mfma_f32_16x16x32_bf16 v[120:123], a[208:211], a[68:71], v[120:123]\n" \
"v_add_u32_e32 v36, v44, v45\n" \
"v_mfma_f32_16x16x32_bf16 v[124:127], a[208:211], a[84:87], v[124:127]\n" \
"buffer_load_dwordx4 a[152:155], v32, s[56:59], s68 offen offset:2048 sc1\n" \
"v_mfma_f32_16x16x32_bf16 v[124:127], a[204:207], a[80:83], v[124:127]\n" \
"ds_read_b64_tr_b16 a[204:205], v10 offset:37120\n" \
"ds_read_b64_tr_b16 a[206:207], v10 offset:41728\n" \
"v_mfma_f32_16x16x32_bf16 v[128:131], a[212:215], a[64:67], v[128:131]\n" \
"ds_read_b64_tr_b16 a[208:209], v10 offset:46080\n" \
"ds_read_b64_tr_b16 a[210:211], v10 offset:50688\n" \
"v_mfma_f32_16x16x32_bf16 v[128:131], a[216:219], a[68:71], v[128:131]\n" \
"v_mfma_f32_16x16x32_bf16 v[132:135], a[216:219], a[84:87], v[132:135]\n" \
"ds_read_b64_tr_b16 a[216:217], v10 offset:46112\n" \
"ds_read_b64_tr_b16 a[218:219], v10 offset:50720\n" \
"buffer_load_dwordx4 a[148:151], v32, s[56:59], s68 offen offset:1024 sc1\n" \
"v_mfma_f32_16x16x32_bf16 v[132:135], a[212:215], a[80:83], v[132:135]\n" \
"ds_read_b64_tr_b16 a[212:213], v10 offset:55296\n" \
"ds_read_b64_tr_b16 a[214:215], v10 offset:59904\n" \
"v_mfma_f32_16x16x32_bf16 v[136:139], a[0:3], a[64:67], v[136:139]\n" \
"v_mfma_f32_16x16x32_bf16 v[136:139], a[4:7], a[68:71], v[136:139]\n" \
"v_mfma_f32_16x16x32_bf16 v[140:143], a[4:7], a[84:87], v[140:143]\n" \
"ds_read_b64_tr_b16 a[4:5], v10 offset:46144\n" \
"ds_read_b64_tr_b16 a[6:7], v10 offset:50752\n" \
"buffer_load_dwordx4 a[144:147], v32, s[56:59], s68 offen offset:0 sc1\n" \
"v_mfma_f32_16x16x32_bf16 v[140:143], a[0:3], a[80:83], v[140:143]\n" \
"ds_read_b64_tr_b16 a[0:1], v10 offset:55328\n" \
"ds_read_b64_tr_b16 a[2:3], v10 offset:59936\n" \
"v_mfma_f32_16x16x32_bf16 v[144:147], a[8:11], a[64:67], v[144:147]\n" \
"v_readlane_b32 s68, v11, 49\n" \
"v_mfma_f32_16x16x32_bf16 v[144:147], a[12:15], a[68:71], v[144:147]\n" \
"v_mfma_f32_16x16x32_bf16 v[148:151], a[12:15], a[84:87], v[148:151]\n" \
"ds_read_b64_tr_b16 a[12:13], v10 offset:46176\n" \
"ds_read_b64_tr_b16 a[14:15], v10 offset:50784\n" \
"buffer_load_dwordx4 a[96:99], v20, s[56:59], s69 offen offset:0 sc1\n" \
"v_mfma_f32_16x16x32_bf16 v[148:151], a[8:11], a[80:83], v[148:151]\n" \
"ds_read_b64_tr_b16 a[8:9], v10 offset:55360\n" \
"s_waitcnt lgkmcnt(15)\n" \
"v_mfma_f32_16x16x32_bf16 v[152:155], a[16:19], a[64:67], v[152:155]\n" \
"ds_read_b64_tr_b16 a[10:11], v10 offset:59968\n" \
"v_mfma_f32_16x16x32_bf16 v[152:155], a[20:23], a[68:71], v[152:155]\n" \
"v_mfma_f32_16x16x32_bf16 v[156:159], a[20:23], a[84:87], v[156:159]\n" \
"ds_read_b64_tr_b16 a[20:21], v10 offset:46208\n" \
"ds_read_b64_tr_b16 a[22:23], v10 offset:50816\n" \
"buffer_load_dwordx4 a[100:103], v20, s[56:59], s69 offen offset:1024 sc1\n" \
"v_mfma_f32_16x16x32_bf16 v[156:159], a[16:19], a[80:83], v[156:159]\n" \
"ds_read_b64_tr_b16 a[16:17], v10 offset:55392\n" \
"ds_read_b64_tr_b16 a[18:19], v10 offset:60000\n" \
"v_mfma_f32_16x16x32_bf16 v[160:163], a[24:27], a[64:67], v[160:163]\n" \
"v_mfma_f32_16x16x32_bf16 v[160:163], a[28:31], a[68:71], v[160:163]\n" \
"v_mfma_f32_16x16x32_bf16 v[164:167], a[28:31], a[84:87], v[164:167]\n" \
"ds_read_b64_tr_b16 a[28:29], v10 offset:46240\n" \
"ds_read_b64_tr_b16 a[30:31], v10 offset:50848\n" \
"buffer_load_dwordx4 a[104:107], v20, s[56:59], s69 offen offset:2048 sc1\n" \
"v_mfma_f32_16x16x32_bf16 v[164:167], a[24:27], a[80:83], v[164:167]\n" \
"ds_read_b64_tr_b16 a[24:25], v10 offset:55424\n" \
"ds_read_b64_tr_b16 a[26:27], v10 offset:60032\n" \
"v_mfma_f32_16x16x32_bf16 v[168:171], a[192:195], a[64:67], v[168:171]\n" \
"v_mfma_f32_16x16x32_bf16 v[168:171], a[196:199], a[68:71], v[168:171]\n" \
"v_mfma_f32_16x16x32_bf16 v[172:175], a[196:199], a[84:87], v[172:175]\n" \
"ds_read_b64_tr_b16 a[196:197], v10 offset:46272\n" \
"ds_read_b64_tr_b16 a[198:199], v10 offset:50880\n" \
"buffer_load_dwordx4 a[108:111], v20, s[56:59], s69 offen offset:3072 sc1\n" \
"v_mfma_f32_16x16x32_bf16 v[172:175], a[192:195], a[80:83], v[172:175]\n" \
"ds_read_b64_tr_b16 a[192:193], v10 offset:55456\n" \
"ds_read_b64_tr_b16 a[194:195], v10 offset:60064\n" \
"v_mfma_f32_16x16x32_bf16 v[176:179], a[200:203], a[64:67], v[176:179]\n" \
"s_waitcnt lgkmcnt(13)\n" \
"v_mfma_f32_16x16x32_bf16 v[176:179], a[204:207], a[68:71], v[176:179]\n" \
"v_mfma_f32_16x16x32_bf16 v[180:183], a[204:207], a[84:87], v[180:183]\n" \
"ds_read_b64_tr_b16 a[204:205], v10 offset:46304\n" \
"ds_read_b64_tr_b16 a[206:207], v10 offset:50912\n" \
"buffer_load_dwordx4 a[124:127], v32, s[56:59], s69 offen offset:3072 sc1\n" \
"v_mfma_f32_16x16x32_bf16 v[180:183], a[200:203], a[80:83], v[180:183]\n" \
"ds_read_b64_tr_b16 a[200:201], v10 offset:55488\n" \
"ds_read_b64_tr_b16 a[202:203], v10 offset:60096\n" \
"v_mfma_f32_16x16x32_bf16 v[112:115], a[208:211], a[72:75], v[112:115]\n" \
"v_mfma_f32_16x16x32_bf16 v[112:115], a[212:215], a[76:79], v[112:115]\n" \
"v_mfma_f32_16x16x32_bf16 v[116:119], a[212:215], a[92:95], v[116:119]\n" \
"ds_read_b64_tr_b16 a[212:213], v10 offset:46336\n" \
"ds_read_b64_tr_b16 a[214:215], v10 offset:50944\n" \
"buffer_load_dwordx4 a[120:123], v32, s[56:59], s69 offen offset:2048 sc1\n" \
"v_mfma_f32_16x16x32_bf16 v[116:119], a[208:211], a[88:91], v[116:119]\n" \
"ds_read_b64_tr_b16 a[208:209], v10 offset:55520\n" \
"ds_read_b64_tr_b16 a[210:211], v10 offset:60128\n" \
"v_mfma_f32_16x16x32_bf16 v[120:123], a[216:219], a[72:75], v[120:123]\n" \
"v_mfma_f32_16x16x32_bf16 v[120:123], a[0:3], a[76:79], v[120:123]\n" \
"v_mfma_f32_16x16x32_bf16 v[124:127], a[0:3], a[92:95], v[124:127]\n" \
"v_mfma_f32_16x16x32_bf16 v[124:127], a[216:219], a[88:91], v[124:127]\n" \
"buffer_load_dwordx4 a[116:119], v32, s[56:59], s69 offen offset:1024 sc1\n" \
"v_mfma_f32_16x16x32_bf16 v[128:131], a[4:7], a[72:75], v[128:131]\n" \
"ds_read_b64_tr_b16 a[216:217], v10 offset:55552\n" \
"s_waitcnt lgkmcnt(9)\n" \
"v_mfma_f32_16x16x32_bf16 v[128:131], a[8:11], a[76:79], v[128:131]\n" \
"ds_read_b64_tr_b16 a[218:219], v10 offset:60160\n" \
"v_mfma_f32_16x16x32_bf16 v[132:135], a[8:11], a[92:95], v[132:135]\n" \
"v_mfma_f32_16x16x32_bf16 v[132:135], a[4:7], a[88:91], v[132:135]\n" \
"v_mfma_f32_16x16x32_bf16 v[136:139], a[12:15], a[72:75], v[136:139]\n" \
"buffer_load_dwordx4 a[112:115], v32, s[56:59], s69 offen offset:0 sc1\n" \
"v_mfma_f32_16x16x32_bf16 v[136:139], a[16:19], a[76:79], v[136:139]\n" \
"v_readlane_b32 s69, v11, 5\n" \
"v_mfma_f32_16x16x32_bf16 v[140:143], a[16:19], a[92:95], v[140:143]\n" \
"v_mfma_f32_16x16x32_bf16 v[140:143], a[12:15], a[88:91], v[140:143]\n" \
"s_waitcnt vmcnt(16)\n" \
"s_barrier\n" \
"buffer_load_dwordx4 a[32:35], v20, s[56:59], s64 offen offset:0 sc1\n" \
"v_mfma_f32_16x16x32_bf16 v[144:147], a[20:23], a[72:75], v[144:147]\n" \
"ds_read_b64_tr_b16 a[0:1], v33 offset:18432\n" \
"ds_read_b64_tr_b16 a[2:3], v33 offset:23040\n" \
"v_mfma_f32_16x16x32_bf16 v[144:147], a[24:27], a[76:79], v[144:147]\n" \
"ds_read_b64_tr_b16 a[4:5], v33 offset:27648\n" \
"ds_read_b64_tr_b16 a[6:7], v33 offset:32256\n" \
"v_mfma_f32_16x16x32_bf16 v[148:151], a[24:27], a[92:95], v[148:151]\n" \
"ds_read_b64_tr_b16 a[8:9], v33 offset:36864\n" \
"ds_read_b64_tr_b16 a[10:11], v33 offset:41472\n" \
"v_mfma_f32_16x16x32_bf16 v[148:151], a[20:23], a[88:91], v[148:151]\n" \
"ds_read_b64_tr_b16 a[12:13], v33 offset:46080\n" \
"ds_read_b64_tr_b16 a[14:15], v33 offset:50688\n" \
"buffer_load_dwordx4 a[36:39], v20, s[56:59], s64 offen offset:1024 sc1\n" \
"v_mfma_f32_16x16x32_bf16 v[152:155], a[28:31], a[72:75], v[152:155]\n" \
"ds_read_b64_tr_b16 a[16:17], v34 offset:0\n" \
"ds_read_b64_tr_b16 a[18:19], v34 offset:4608\n" \
"v_mfma_f32_16x16x32_bf16 v[152:155], a[192:195], a[76:79], v[152:155]\n" \
"ds_read_b64_tr_b16 a[20:21], v34 offset:9216\n" \
"ds_read_b64_tr_b16 a[22:23], v34 offset:13824\n" \
"v_mfma_f32_16x16x32_bf16 v[156:159], a[192:195], a[92:95], v[156:159]\n" \
"ds_read_b64_tr_b16 a[24:25], v34 offset:18432\n" \
"ds_read_b64_tr_b16 a[26:27], v34 offset:23040\n" \
"v_mfma_f32_16x16x32_bf16 v[156:159], a[28:31], a[88:91], v[156:159]\n" \
"ds_read_b64_tr_b16 a[28:29], v33 offset:0\n" \
"ds_read_b64_tr_b16 a[30:31], v33 offset:4608\n" \
"buffer_load_dwordx4 a[40:43], v20, s[56:59], s64 offen offset:2048 sc1\n" \
"v_mfma_f32_16x16x32_bf16 v[160:163], a[196:199], a[72:75], v[160:163]\n" \
"ds_read_b64_tr_b16 a[192:193], v33 offset:18464\n" \
"s_waitcnt lgkmcnt(15)\n" \
"v_mfma_f32_16x16x32_bf16 v[160:163], a[200:203], a[76:79], v[160:163]\n" \
"ds_read_b64_tr_b16 a[194:195], v33 offset:23072\n" \
"v_mfma_f32_16x16x32_bf16 v[164:167], a[200:203], a[92:95], v[164:167]\n" \
"buffer_load_dwordx4 a[44:47], v20, s[56:59], s64 offen offset:3072 sc1\n" \
"v_mfma_f32_16x16x32_bf16 v[164:167], a[196:199], a[88:91], v[164:167]\n" \
"ds_read_b64_tr_b16 a[196:197], v33 offset:27680\n" \
"ds_read_b64_tr_b16 a[198:199], v33 offset:32288\n" \
"v_mfma_f32_16x16x32_bf16 v[168:171], a[204:207], a[72:75], v[168:171]\n" \
"ds_read_b64_tr_b16 a[200:201], v33 offset:36896\n" \
"ds_read_b64_tr_b16 a[202:203], v33 offset:41504\n" \
"v_mfma_f32_16x16x32_bf16 v[168:171], a[208:211], a[76:79], v[168:171]\n" \
"v_mfma_f32_16x16x32_bf16 v[172:175], a[208:211], a[92:95], v[172:175]\n" \
"ds_read_b64_tr_b16 a[208:209], v34 offset:32\n" \
"ds_read_b64_tr_b16 a[210:211], v34 offset:4640\n" \
"buffer_load_dwordx4 a[60:63], v32, s[56:59], s64 offen offset:3072 sc1\n" \
"v_mfma_f32_16x16x32_bf16 v[172:175], a[204:207], a[88:91], v[172:175]\n" \
"ds_read_b64_tr_b16 a[204:205], v33 offset:46112\n" \
"ds_read_b64_tr_b16 a[206:207], v33 offset:50720\n" \
"v_mfma_f32_16x16x32_bf16 v[176:179], a[212:215], a[72:75], v[176:179]\n" \
"v_mfma_f32_16x16x32_bf16 v[176:179], a[216:219], a[76:79], v[176:179]\n" \
"v_mfma_f32_16x16x32_bf16 v[180:183], a[216:219], a[92:95], v[180:183]\n" \
"ds_read_b64_tr_b16 a[216:217], v34 offset:18464\n" \
"ds_read_b64_tr_b16 a[218:219], v34 offset:23072\n" \
"buffer_load_dwordx4 a[56:59], v32, s[56:59], s64 offen offset:2048 sc1\n" \
"v_mfma_f32_16x16x32_bf16 v[180:183], a[212:215], a[88:91], v[180:183]\n" \
"ds_read_b64_tr_b16 a[212:213], v34 offset:9248\n" \
"s_waitcnt vmcnt(21)\n" \
"v_mfma_f32_16x16x32_bf16 v[112:115], a[0:3], a[128:131], v[112:115]\n" \
"ds_read_b64_tr_b16 a[214:215], v34 offset:13856\n" \
"s_waitcnt vmcnt(20) lgkmcnt(13)\n" \
"v_mfma_f32_16x16x32_bf16 v[112:115], a[4:7], a[132:135], v[112:115]\n" \
"s_waitcnt vmcnt(19)\n" \
"v_mfma_f32_16x16x32_bf16 v[112:115], a[8:11], a[136:139], v[112:115]\n" \
"s_waitcnt vmcnt(18)\n" \
"v_mfma_f32_16x16x32_bf16 v[112:115], a[12:15], a[140:143], v[112:115]\n" \
"buffer_load_dwordx4 a[52:55], v32, s[56:59], s64 offen offset:1024 sc1\n" \
"s_waitcnt vmcnt(18)\n" \
"v_mfma_f32_16x16x32_bf16 v[116:119], a[12:15], a[156:159], v[116:119]\n" \
"ds_read_b64_tr_b16 a[12:13], v33 offset:36928\n" \
"s_waitcnt vmcnt(17)\n" \
"v_mfma_f32_16x16x32_bf16 v[116:119], a[8:11], a[152:155], v[116:119]\n" \
"ds_read_b64_tr_b16 a[8:9], v33 offset:27712\n" \
"ds_read_b64_tr_b16 a[10:11], v33 offset:32320\n" \
"ds_read_b64_tr_b16 a[14:15], v33 offset:41536\n" \
"s_waitcnt vmcnt(16)\n" \
"v_mfma_f32_16x16x32_bf16 v[116:119], a[4:7], a[148:151], v[116:119]\n" \
"buffer_load_dwordx4 a[48:51], v32, s[56:59], s64 offen offset:0 sc1\n" \
"ds_read_b64_tr_b16 a[4:5], v33 offset:18496\n" \
"ds_read_b64_tr_b16 a[6:7], v33 offset:23104\n" \
"v_readlane_b32 s64, v11, 38\n" \
"s_waitcnt vmcnt(16)\n" \
"v_mfma_f32_16x16x32_bf16 v[116:119], a[0:3], a[144:147], v[116:119]\n" \
"buffer_load_dwordx4 a[160:163], v20, s[56:59], s72 offen offset:0 sc1\n" \
"ds_read_b64_tr_b16 a[0:1], v33 offset:32\n" \
"ds_read_b64_tr_b16 a[2:3], v33 offset:4640\n" \
"s_waitcnt vmcnt(16)\n" \
"v_mfma_f32_16x16x32_bf16 v[112:115], a[16:19], a[96:99], v[112:115]\n" \
"buffer_load_dwordx4 a[164:167], v20, s[56:59], s72 offen offset:1024 sc1\n" \
"s_waitcnt vmcnt(16)\n" \
"v_mfma_f32_16x16x32_bf16 v[112:115], a[20:23], a[100:103], v[112:115]\n" \
"buffer_load_dwordx4 a[168:171], v20, s[56:59], s72 offen offset:2048 sc1\n" \
"s_waitcnt vmcnt(16)\n" \
"v_mfma_f32_16x16x32_bf16 v[112:115], a[24:27], a[104:107], v[112:115]\n" \
"buffer_load_dwordx4 a[172:175], v20, s[56:59], s72 offen offset:3072 sc1\n" \
"s_waitcnt vmcnt(16)\n" \
"v_mfma_f32_16x16x32_bf16 v[112:115], a[28:31], a[108:111], v[112:115]\n" \
"buffer_load_dwordx4 a[188:191], v32, s[56:59], s72 offen offset:3072 sc1\n" \
"s_waitcnt vmcnt(16)\n" \
"v_mfma_f32_16x16x32_bf16 v[116:119], a[28:31], a[124:127], v[116:119]\n" \
"buffer_load_dwordx4 a[184:187], v32, s[56:59], s72 offen offset:2048 sc1\n" \
"ds_read_b64_tr_b16 a[28:29], v34 offset:18496\n" \
"ds_read_b64_tr_b16 a[30:31], v34 offset:23104\n" \
"s_waitcnt vmcnt(16)\n" \
"v_mfma_f32_16x16x32_bf16 v[116:119], a[24:27], a[120:123], v[116:119]\n" \
"v_cvt_pk_bf16_f32 v112, v112, v113\n" \
"v_cvt_pk_bf16_f32 v113, v114, v115\n" \
"s_waitcnt vmcnt(15)\n" \
"v_mfma_f32_16x16x32_bf16 v[116:119], a[20:23], a[116:119], v[116:119]\n" \
"buffer_load_dwordx4 a[180:183], v32, s[56:59], s72 offen offset:1024 sc1\n" \
"ds_read_b64_tr_b16 a[20:21], v34 offset:64\n" \
"ds_read_b64_tr_b16 a[22:23], v34 offset:4672\n" \
"ds_read_b64_tr_b16 a[24:25], v34 offset:9280\n" \
"ds_read_b64_tr_b16 a[26:27], v34 offset:13888\n" \
"s_waitcnt vmcnt(15)\n" \
"v_mfma_f32_16x16x32_bf16 v[116:119], a[16:19], a[112:115], v[116:119]\n" \
"ds_read_b64_tr_b16 a[16:17], v33 offset:46144\n" \
"buffer_load_dwordx4 a[176:179], v32, s[56:59], s72 offen offset:0 sc1\n" \
"s_waitcnt lgkmcnt(5)\n" \
"v_mfma_f32_16x16x32_bf16 v[120:123], a[192:195], a[128:131], v[120:123]\n" \
"ds_read_b64_tr_b16 a[18:19], v33 offset:50752\n" \
"v_readlane_b32 s72, v11, 47\n" \
"v_mfma_f32_16x16x32_bf16 v[120:123], a[196:199], a[132:135], v[120:123]\n" \
"v_mfma_f32_16x16x32_bf16 v[120:123], a[200:203], a[136:139], v[120:123]\n" \
"v_cvt_pk_bf16_f32 v114, v116, v117\n" \
"v_cvt_pk_bf16_f32 v115, v118, v119\n" \
"v_mfma_f32_16x16x32_bf16 v[120:123], a[204:207], a[140:143], v[120:123]\n" \
"buffer_store_dwordx4 v[112:115], v20, s[56:59], s65 offen offset:0 sc1\n" \
"s_add_u32 s65, s65, 0x3800\n" \
"v_mfma_f32_16x16x32_bf16 v[124:127], a[204:207], a[156:159], v[124:127]\n" \
"ds_read_b64_tr_b16 a[204:205], v10 offset:46336\n" \
"ds_read_b64_tr_b16 a[206:207], v10 offset:50944\n" \
"v_mfma_f32_16x16x32_bf16 v[124:127], a[200:203], a[152:155], v[124:127]\n" \
"ds_read_b64_tr_b16 a[200:201], v10 offset:37120\n" \
"ds_read_b64_tr_b16 a[202:203], v10 offset:41728\n" \
"v_mfma_f32_16x16x32_bf16 v[124:127], a[196:199], a[148:151], v[124:127]\n" \
"ds_read_b64_tr_b16 a[196:197], v10 offset:27904\n" \
"ds_read_b64_tr_b16 a[198:199], v10 offset:32512\n" \
"v_mfma_f32_16x16x32_bf16 v[124:127], a[192:195], a[144:147], v[124:127]\n" \
"buffer_load_dwordx4 a[220:223], v20, s[56:59], s93 offen offset:0 sc1\n" \
"v_mfma_f32_16x16x32_bf16 v[120:123], a[208:211], a[96:99], v[120:123]\n" \
"ds_read_b64_tr_b16 a[192:193], v33 offset:64\n" \
"ds_read_b64_tr_b16 a[194:195], v33 offset:4672\n" \
"v_mfma_f32_16x16x32_bf16 v[120:123], a[212:215], a[100:103], v[120:123]\n" \
"v_mfma_f32_16x16x32_bf16 v[120:123], a[216:219], a[104:107], v[120:123]\n" \
"v_mfma_f32_16x16x32_bf16 v[120:123], a[0:3], a[108:111], v[120:123]\n" \
"v_mfma_f32_16x16x32_bf16 v[124:127], a[0:3], a[124:127], v[124:127]\n" \
"buffer_load_dwordx4 a[224:227], v20, s[56:59], s93 offen offset:1024 sc1\n" \
"v_mfma_f32_16x16x32_bf16 v[124:127], a[216:219], a[120:123], v[124:127]\n" \
"ds_read_b64_tr_b16 a[216:217], v33 offset:27904\n" \
"ds_read_b64_tr_b16 a[218:219], v33 offset:32512\n" \
"v_mfma_f32_16x16x32_bf16 v[124:127], a[212:215], a[116:119], v[124:127]\n" \
"ds_read_b64_tr_b16 a[212:213], v33 offset:18688\n" \
"ds_read_b64_tr_b16 a[214:215], v33 offset:23296\n" \
"v_cvt_pk_bf16_f32 v120, v120, v121\n" \
"v_cvt_pk_bf16_f32 v121, v122, v123\n" \
"v_mfma_f32_16x16x32_bf16 v[124:127], a[208:211], a[112:115], v[124:127]\n" \
"ds_read_b64_tr_b16 a[208:209], v10 offset:55552\n" \
"ds_read_b64_tr_b16 a[210:211], v10 offset:60160\n" \
"v_mfma_f32_16x16x32_bf16 v[128:131], a[4:7], a[128:131], v[128:131]\n" \
"buffer_load_dwordx4 a[228:231], v20, s[56:59], s93 offen offset:2048 sc1\n" \
"v_mfma_f32_16x16x32_bf16 v[128:131], a[8:11], a[132:135], v[128:131]\n" \
"ds_read_b64_tr_b16 a[0:1], v33 offset:37120\n" \
"ds_read_b64_tr_b16 a[2:3], v33 offset:41728\n" \
"v_mfma_f32_16x16x32_bf16 v[128:131], a[12:15], a[136:139], v[128:131]\n" \
"v_cvt_pk_bf16_f32 v122, v124, v125\n" \
"v_cvt_pk_bf16_f32 v123, v126, v127\n" \
"s_waitcnt lgkmcnt(13)\n" \
"v_mfma_f32_16x16x32_bf16 v[128:131], a[16:19], a[140:143], v[128:131]\n" \
"buffer_store_dwordx4 v[120:123], v20, s[56:59], s66 offen offset:0 sc1\n" \
"s_add_u32 s66, s66, 0x3000\n" \
"v_mfma_f32_16x16x32_bf16 v[132:135], a[16:19], a[156:159], v[132:135]\n" \
"ds_read_b64_tr_b16 a[16:17], v34 offset:18688\n" \
"ds_read_b64_tr_b16 a[18:19], v34 offset:23296\n" \
"v_mfma_f32_16x16x32_bf16 v[132:135], a[12:15], a[152:155], v[132:135]\n" \
"ds_read_b64_tr_b16 a[12:13], v34 offset:9472\n" \
"ds_read_b64_tr_b16 a[14:15], v34 offset:14080\n" \
"v_mfma_f32_16x16x32_bf16 v[132:135], a[8:11], a[148:151], v[132:135]\n" \
"ds_read_b64_tr_b16 a[8:9], v34 offset:256\n" \
"ds_read_b64_tr_b16 a[10:11], v34 offset:4864\n" \
"v_mfma_f32_16x16x32_bf16 v[132:135], a[4:7], a[144:147], v[132:135]\n" \
"buffer_load_dwordx4 a[232:235], v20, s[56:59], s93 offen offset:3072 sc1\n" \
"v_mfma_f32_16x16x32_bf16 v[128:131], a[20:23], a[96:99], v[128:131]\n" \
"ds_read_b64_tr_b16 a[4:5], v33 offset:46336\n" \
"ds_read_b64_tr_b16 a[6:7], v33 offset:50944\n" \
"v_mfma_f32_16x16x32_bf16 v[128:131], a[24:27], a[100:103], v[128:131]\n" \
"v_mfma_f32_16x16x32_bf16 v[128:131], a[28:31], a[104:107], v[128:131]\n" \
"s_waitcnt lgkmcnt(15)\n" \
"v_mfma_f32_16x16x32_bf16 v[128:131], a[192:195], a[108:111], v[128:131]\n" \
"v_mfma_f32_16x16x32_bf16 v[132:135], a[192:195], a[124:127], v[132:135]\n" \
"buffer_load_dwordx4 a[248:251], v32, s[56:59], s93 offen offset:3072 sc1\n" \
"v_mfma_f32_16x16x32_bf16 v[132:135], a[28:31], a[120:123], v[132:135]\n" \
"ds_read_b64_tr_b16 a[28:29], v10 offset:36960\n" \
"ds_read_b64_tr_b16 a[30:31], v10 offset:41568\n" \
"v_mfma_f32_16x16x32_bf16 v[132:135], a[24:27], a[116:119], v[132:135]\n" \
"ds_read_b64_tr_b16 a[24:25], v10 offset:27744\n" \
"ds_read_b64_tr_b16 a[26:27], v10 offset:32352\n" \
"v_cvt_pk_bf16_f32 v128, v128, v129\n" \
"v_cvt_pk_bf16_f32 v129, v130, v131\n" \
"L_pf21:\n" \
"s_cbranch_execz L_retB\n" \
"s_nop 0\n" \
"v_mfma_f32_16x16x32_bf16 v[132:135], a[20:23], a[112:115], v[132:135]\n" \
"ds_read_b64_tr_b16 a[20:21], v33 offset:256\n" \
"s_waitcnt vmcnt(22)\n" \
"v_mfma_f32_16x16x32_bf16 v[248:251], a[196:199], a[32:35], v[248:251]\n" \
"ds_read_b64_tr_b16 a[22:23], v33 offset:4864\n" \
"buffer_load_dwordx4 a[244:247], v32, s[56:59], s93 offen offset:2048 sc1\n" \
"s_waitcnt vmcnt(22)\n" \
"v_mfma_f32_16x16x32_bf16 v[248:251], a[200:203], a[36:39], v[248:251]\n" \
"ds_read_b64_tr_b16 a[192:193], v10 offset:46176\n" \
"v_cvt_pk_bf16_f32 v130, v132, v133\n" \
"v_cvt_pk_bf16_f32 v131, v134, v135\n" \
"s_waitcnt vmcnt(21)\n" \
"v_mfma_f32_16x16x32_bf16 v[248:251], a[204:207], a[40:43], v[248:251]\n" \
"ds_read_b64_tr_b16 a[194:195], v10 offset:50784\n" \
"buffer_store_dwordx4 v[128:131], v20, s[56:59], s67 offen offset:0 sc1\n" \
"s_add_u32 s67, s67, 0x2800\n" \
"s_waitcnt vmcnt(21) lgkmcnt(10)\n" \
"v_mfma_f32_16x16x32_bf16 v[248:251], a[208:211], a[44:47], v[248:251]\n" \
"s_waitcnt vmcnt(20)\n" \
"v_mfma_f32_16x16x32_bf16 v[252:255], a[208:211], a[60:63], v[252:255]\n" \
"ds_read_b64_tr_b16 a[208:209], v33 offset:36960\n" \
"ds_read_b64_tr_b16 a[210:211], v33 offset:41568\n" \
"s_waitcnt vmcnt(19)\n" \
"v_mfma_f32_16x16x32_bf16 v[252:255], a[204:207], a[56:59], v[252:255]\n" \
"buffer_load_dwordx4 a[240:243], v32, s[56:59], s93 offen offset:1024 sc1\n" \
"ds_read_b64_tr_b16 a[204:205], v33 offset:27744\n" \
"ds_read_b64_tr_b16 a[206:207], v33 offset:32352\n" \
"s_waitcnt vmcnt(19)\n" \
"v_mfma_f32_16x16x32_bf16 v[252:255], a[200:203], a[52:55], v[252:255]\n" \
"buffer_load_dwordx4 a[236:239], v32, s[56:59], s93 offen offset:0 sc1\n" \
"ds_read_b64_tr_b16 a[200:201], v33 offset:18528\n" \
"ds_read_b64_tr_b16 a[202:203], v33 offset:23136\n" \
"v_readlane_b32 s93, v11, 43\n" \
"s_waitcnt vmcnt(19)\n" \
"v_mfma_f32_16x16x32_bf16 v[252:255], a[196:199], a[48:51], v[252:255]\n" \
"ds_read_b64_tr_b16 a[196:197], v10 offset:55392\n" \
"ds_read_b64_tr_b16 a[198:199], v10 offset:60000\n" \
"v_mfma_f32_16x16x32_bf16 v[176:179], a[212:215], a[128:131], v[176:179]\n" \
"buffer_load_dwordx4 v[64:67], v20, s[56:59], s95 offen offset:0 sc1\n" \
"v_mfma_f32_16x16x32_bf16 v[176:179], a[216:219], a[132:135], v[176:179]\n" \
"v_mfma_f32_16x16x32_bf16 v[176:179], a[0:3], a[136:139], v[176:179]\n" \
"s_waitcnt lgkmcnt(6)\n" \
"v_mfma_f32_16x16x32_bf16 v[176:179], a[4:7], a[140:143], v[176:179]\n" \
"v_mfma_f32_16x16x32_bf16 v[180:183], a[4:7], a[156:159], v[180:183]\n" \
"v_mfma_f32_16x16x32_bf16 v[180:183], a[0:3], a[152:155], v[180:183]\n" \
"buffer_load_dwordx4 v[72:75], v32, s[56:59], s95 offen offset:0 sc1\n" \
"v_mfma_f32_16x16x32_bf16 v[180:183], a[216:219], a[148:151], v[180:183]\n" \
"v_mfma_f32_16x16x32_bf16 v[180:183], a[212:215], a[144:147], v[180:183]\n" \
"s_waitcnt vmcnt(20)\n" \
"v_mfma_f32_16x16x32_bf16 v[248:251], a[212:215], a[160:163], v[248:251]\n" \
"s_waitcnt vmcnt(19)\n" \
"v_mfma_f32_16x16x32_bf16 v[248:251], a[216:219], a[164:167], v[248:251]\n" \
"s_waitcnt vmcnt(18)\n" \
"v_mfma_f32_16x16x32_bf16 v[248:251], a[0:3], a[168:171], v[248:251]\n" \
"buffer_load_dwordx4 v[80:83], v20, s[56:59], s96 offen offset:0 sc1\n" \
"s_waitcnt vmcnt(18)\n" \
"v_mfma_f32_16x16x32_bf16 v[248:251], a[4:7], a[172:175], v[248:251]\n" \
"buffer_load_dwordx4 v[88:91], v32, s[56:59], s96 offen offset:0 sc1\n" \
"s_waitcnt vmcnt(18)\n" \
"v_mfma_f32_16x16x32_bf16 v[252:255], a[4:7], a[188:191], v[252:255]\n" \
"ds_read_b64_tr_b16 a[4:5], v34 offset:18528\n" \
"ds_read_b64_tr_b16 a[6:7], v34 offset:23136\n" \
"buffer_load_dwordx4 v[68:71], v20, s[56:59], s95 offen offset:1024 sc1\n" \
"s_waitcnt vmcnt(18)\n" \
"v_mfma_f32_16x16x32_bf16 v[252:255], a[0:3], a[184:187], v[252:255]\n" \
"ds_read_b64_tr_b16 a[0:1], v34 offset:9312\n" \
"ds_read_b64_tr_b16 a[2:3], v34 offset:13920\n" \
"buffer_load_dwordx4 v[76:79], v32, s[56:59], s95 offen offset:1024 sc1\n" \
"s_add_u32 s95, s63, 0x0\n" \
"s_waitcnt vmcnt(18)\n" \
"v_mfma_f32_16x16x32_bf16 v[252:255], a[216:219], a[180:183], v[252:255]\n" \
"ds_read_b64_tr_b16 a[216:217], v34 offset:96\n" \
"ds_read_b64_tr_b16 a[218:219], v34 offset:4704\n" \
"buffer_load_dwordx4 v[84:87], v20, s[56:59], s96 offen offset:1024 sc1\n" \
"s_waitcnt vmcnt(18)\n" \
"v_mfma_f32_16x16x32_bf16 v[252:255], a[212:215], a[176:179], v[252:255]\n" \
"ds_read_b64_tr_b16 a[212:213], v33 offset:46176\n" \
"ds_read_b64_tr_b16 a[214:215], v33 offset:50784\n" \
"v_mfma_f32_16x16x32_bf16 v[176:179], a[8:11], a[96:99], v[176:179]\n" \
"buffer_load_dwordx4 v[92:95], v32, s[56:59], s96 offen offset:1024 sc1\n" \
"v_mfma_f32_16x16x32_bf16 v[176:179], a[12:15], a[100:103], v[176:179]\n" \
"s_add_u32 s96, s63, 0x800\n" \
"v_mfma_f32_16x16x32_bf16 v[176:179], a[16:19], a[104:107], v[176:179]\n" \
"v_mfma_f32_16x16x32_bf16 v[176:179], a[20:23], a[108:111], v[176:179]\n" \
"s_waitcnt vmcnt(18)\n" \
"s_mov_b64 exec, 1\n" \
"buffer_store_dword v39, off, s[56:59], s65 offset:0 sc1\n" \
"s_mov_b64 exec, -1\n" \
"v_mfma_f32_16x16x32_bf16 v[180:183], a[20:23], a[124:127], v[180:183]\n" \
"s_add_u32 s65, s63, 0x1800\n" \
"v_mfma_f32_16x16x32_bf16 v[180:183], a[16:19], a[120:123], v[180:183]\n" \
"v_mfma_f32_16x16x32_bf16 v[180:183], a[12:15], a[116:119], v[180:183]\n" \
"v_cvt_pk_bf16_f32 v176, v176, v177\n" \
"v_cvt_pk_bf16_f32 v177, v178, v179\n" \
"v_mfma_f32_16x16x32_bf16 v[180:183], a[8:11], a[112:115], v[180:183]\n" \
"s_waitcnt vmcnt(18)\n" \
"v_mfma_f32_16x16x32_bf16 v[248:251], a[8:11], a[220:223], v[248:251]\n" \
"buffer_load_dwordx4 v[96:99], v20, s[56:59], s97 offen offset:0 sc1\n" \
"s_waitcnt vmcnt(18)\n" \
"v_mfma_f32_16x16x32_bf16 v[248:251], a[12:15], a[224:227], v[248:251]\n" \
"s_waitcnt vmcnt(17)\n" \
"v_mfma_f32_16x16x32_bf16 v[248:251], a[16:19], a[228:231], v[248:251]\n" \
"buffer_load_dwordx4 v[104:107], v32, s[56:59], s97 offen offset:0 sc1\n" \
"v_cvt_pk_bf16_f32 v178, v180, v181\n" \
"v_cvt_pk_bf16_f32 v179, v182, v183\n" \
"s_waitcnt vmcnt(16)\n" \
"v_mfma_f32_16x16x32_bf16 v[248:251], a[20:23], a[232:235], v[248:251]\n" \
"buffer_store_dwordx4 v[176:179], v20, s[56:59], s68 offen offset:0 sc1\n" \
"s_mov_b64 exec, 1\n" \
"buffer_store_dword v39, off, s[56:59], s66 offset:4 sc1\n" \
"s_mov_b64 exec, -1\n" \
"s_add_u32 s66, s63, 0x2000\n" \
"s_waitcnt vmcnt(17)\n" \
"v_mfma_f32_16x16x32_bf16 v[252:255], a[20:23], a[248:251], v[252:255]\n" \
"ds_read_b64_tr_b16 a[20:21], v10 offset:46208\n" \
"v_cvt_pk_bf16_f32 v248, v248, v249\n" \
"v_cvt_pk_bf16_f32 v249, v250, v251\n" \
"s_waitcnt vmcnt(16)\n" \
"v_mfma_f32_16x16x32_bf16 v[252:255], a[16:19], a[244:247], v[252:255]\n" \
"ds_read_b64_tr_b16 a[16:17], v10 offset:36992\n" \
"ds_read_b64_tr_b16 a[18:19], v10 offset:41600\n" \
"ds_read_b64_tr_b16 a[22:23], v10 offset:50816\n" \
"buffer_load_dwordx4 v[100:103], v20, s[56:59], s97 offen offset:1024 sc1\n" \
"s_waitcnt vmcnt(15)\n" \
"v_mfma_f32_16x16x32_bf16 v[252:255], a[12:15], a[240:243], v[252:255]\n" \
"s_mov_b64 exec, 1\n" \
"buffer_store_dword v39, off, s[56:59], s67 offset:8 sc1\n" \
"s_mov_b64 exec, -1\n" \
"ds_read_b64_tr_b16 a[12:13], v10 offset:27776\n" \
"ds_read_b64_tr_b16 a[14:15], v10 offset:32384\n" \
"s_add_u32 s67, s63, 0x2800\n" \
"s_waitcnt vmcnt(15)\n" \
"v_mfma_f32_16x16x32_bf16 v[252:255], a[8:11], a[236:239], v[252:255]\n" \
"ds_read_b64_tr_b16 a[8:9], v33 offset:96\n" \
"ds_read_b64_tr_b16 a[10:11], v33 offset:4704\n" \
"v_mfma_f32_16x16x32_bf16 v[208:211], a[24:27], a[32:35], v[208:211]\n" \
"buffer_load_dwordx4 v[108:111], v32, s[56:59], s97 offen offset:1024 sc1\n" \
"v_mfma_f32_16x16x32_bf16 v[208:211], a[28:31], a[36:39], v[208:211]\n" \
"s_add_u32 s97, s63, 0x1000\n" \
"v_mfma_f32_16x16x32_bf16 v[208:211], a[192:195], a[40:43], v[208:211]\n" \
"s_waitcnt lgkmcnt(4)\n" \
"v_mfma_f32_16x16x32_bf16 v[208:211], a[196:199], a[44:47], v[208:211]\n" \
"v_cvt_pk_bf16_f32 v250, v252, v253\n" \
"v_cvt_pk_bf16_f32 v251, v254, v255\n" \
"v_mfma_f32_16x16x32_bf16 v[212:215], a[196:199], a[60:63], v[212:215]\n" \
"buffer_store_dwordx4 v[248:251], v20, s[56:59], s68 offen offset:1024 sc1\n" \
"v_mfma_f32_16x16x32_bf16 v[212:215], a[192:195], a[56:59], v[212:215]\n" \
"ds_read_b64_tr_b16 a[192:193], v33 offset:27776\n" \
"ds_read_b64_tr_b16 a[194:195], v33 offset:32384\n" \
"v_mfma_f32_16x16x32_bf16 v[212:215], a[28:31], a[52:55], v[212:215]\n" \
"ds_read_b64_tr_b16 a[28:29], v33 offset:18560\n" \
"ds_read_b64_tr_b16 a[30:31], v33 offset:23168\n" \
"v_mfma_f32_16x16x32_bf16 v[212:215], a[24:27], a[48:51], v[212:215]\n" \
"ds_read_b64_tr_b16 a[24:25], v10 offset:55424\n" \
"ds_read_b64_tr_b16 a[26:27], v10 offset:60032\n" \
"v_mfma_f32_16x16x32_bf16 v[136:139], a[200:203], a[128:131], v[136:139]\n" \
"ds_read_b64_tr_b16 a[196:197], v33 offset:36992\n" \
"ds_read_b64_tr_b16 a[198:199], v33 offset:41600\n" \
"v_mfma_f32_16x16x32_bf16 v[136:139], a[204:207], a[132:135], v[136:139]\n" \
"s_add_u32 s68, s63, 0x3000\n" \
"v_mfma_f32_16x16x32_bf16 v[136:139], a[208:211], a[136:139], v[136:139]\n" \
"v_mfma_f32_16x16x32_bf16 v[136:139], a[212:215], a[140:143], v[136:139]\n" \
"v_mfma_f32_16x16x32_bf16 v[140:143], a[212:215], a[156:159], v[140:143]\n" \
"v_mfma_f32_16x16x32_bf16 v[140:143], a[208:211], a[152:155], v[140:143]\n" \
"v_mfma_f32_16x16x32_bf16 v[140:143], a[204:207], a[148:151], v[140:143]\n" \
"v_mfma_f32_16x16x32_bf16 v[140:143], a[200:203], a[144:147], v[140:143]\n" \
"v_mfma_f32_16x16x32_bf16 v[208:211], a[200:203], a[160:163], v[208:211]\n" \
"v_mfma_f32_16x16x32_bf16 v[208:211], a[204:207], a[164:167], v[208:211]\n" \
"v_mfma_f32_16x16x32_bf16 v[208:211], a[208:211], a[168:171], v[208:211]\n" \
"v_mfma_f32_16x16x32_bf16 v[208:211], a[212:215], a[172:175], v[208:211]\n" \
"v_mfma_f32_16x16x32_bf16 v[212:215], a[212:215], a[188:191], v[212:215]\n" \
"ds_read_b64_tr_b16 a[212:213], v34 offset:18560\n" \
"ds_read_b64_tr_b16 a[214:215], v34 offset:23168\n" \
"v_mfma_f32_16x16x32_bf16 v[212:215], a[208:211], a[184:187], v[212:215]\n" \
"ds_read_b64_tr_b16 a[208:209], v34 offset:9344\n" \
"ds_read_b64_tr_b16 a[210:211], v34 offset:13952\n" \
"v_mfma_f32_16x16x32_bf16 v[212:215], a[204:207], a[180:183], v[212:215]\n" \
"ds_read_b64_tr_b16 a[204:205], v34 offset:128\n" \
"ds_read_b64_tr_b16 a[206:207], v34 offset:4736\n" \
"v_mfma_f32_16x16x32_bf16 v[212:215], a[200:203], a[176:179], v[212:215]\n" \
"ds_read_b64_tr_b16 a[200:201], v33 offset:46208\n" \
"ds_read_b64_tr_b16 a[202:203], v33 offset:50816\n" \
"v_mfma_f32_16x16x32_bf16 v[136:139], a[216:219], a[96:99], v[136:139]\n" \
"v_mfma_f32_16x16x32_bf16 v[136:139], a[0:3], a[100:103], v[136:139]\n" \
"v_mfma_f32_16x16x32_bf16 v[136:139], a[4:7], a[104:107], v[136:139]\n" \
"s_waitcnt lgkmcnt(8)\n" \
"v_mfma_f32_16x16x32_bf16 v[136:139], a[8:11], a[108:111], v[136:139]\n" \
"v_mfma_f32_16x16x32_bf16 v[140:143], a[8:11], a[124:127], v[140:143]\n" \
"v_mfma_f32_16x16x32_bf16 v[140:143], a[4:7], a[120:123], v[140:143]\n" \
"v_mfma_f32_16x16x32_bf16 v[140:143], a[0:3], a[116:119], v[140:143]\n" \
"v_mfma_f32_16x16x32_bf16 v[140:143], a[216:219], a[112:115], v[140:143]\n" \
"v_mfma_f32_16x16x32_bf16 v[208:211], a[216:219], a[220:223], v[208:211]\n" \
"v_mfma_f32_16x16x32_bf16 v[208:211], a[0:3], a[224:227], v[208:211]\n" \
"v_mfma_f32_16x16x32_bf16 v[208:211], a[4:7], a[228:231], v[208:211]\n" \
"v_mfma_f32_16x16x32_bf16 v[208:211], a[8:11], a[232:235], v[208:211]\n" \
"v_cvt_pk_bf16_f32 v136, v136, v137\n" \
"v_cvt_pk_bf16_f32 v137, v138, v139\n" \
"v_mfma_f32_16x16x32_bf16 v[212:215], a[8:11], a[248:251], v[212:215]\n" \
"ds_read_b64_tr_b16 a[8:9], v10 offset:46240\n" \
"v_cvt_pk_bf16_f32 v138, v140, v141\n" \
"v_cvt_pk_bf16_f32 v139, v142, v143\n" \
"v_mfma_f32_16x16x32_bf16 v[212:215], a[4:7], a[244:247], v[212:215]\n" \
"buffer_store_dwordx4 v[136:139], v20, s[56:59], s70 offen offset:0 sc1\n" \
"v_cvt_pk_bf16_f32 v208, v208, v209\n" \
"v_cvt_pk_bf16_f32 v209, v210, v211\n" \
"v_mfma_f32_16x16x32_bf16 v[212:215], a[0:3], a[240:243], v[212:215]\n" \
"ds_read_b64_tr_b16 a[0:1], v10 offset:27808\n" \
"ds_read_b64_tr_b16 a[2:3], v10 offset:32416\n" \
"v_mfma_f32_16x16x32_bf16 v[212:215], a[216:219], a[236:239], v[212:215]\n" \
"ds_read_b64_tr_b16 a[216:217], v33 offset:128\n" \
"ds_read_b64_tr_b16 a[218:219], v33 offset:4736\n" \
"v_mfma_f32_16x16x32_bf16 v[216:219], a[12:15], a[32:35], v[216:219]\n" \
"ds_read_b64_tr_b16 a[4:5], v10 offset:37024\n" \
"ds_read_b64_tr_b16 a[6:7], v10 offset:41632\n" \
"v_mfma_f32_16x16x32_bf16 v[216:219], a[16:19], a[36:39], v[216:219]\n" \
"ds_read_b64_tr_b16 a[10:11], v10 offset:50848\n" \
"v_mfma_f32_16x16x32_bf16 v[216:219], a[20:23], a[40:43], v[216:219]\n" \
"v_cvt_pk_bf16_f32 v210, v212, v213\n" \
"v_cvt_pk_bf16_f32 v211, v214, v215\n" \
"v_mfma_f32_16x16x32_bf16 v[216:219], a[24:27], a[44:47], v[216:219]\n" \
"buffer_store_dwordx4 v[208:211], v20, s[56:59], s70 offen offset:1024 sc1\n" \
"s_add_u32 s70, s70, 0x2000\n" \
"v_mfma_f32_16x16x32_bf16 v[220:223], a[24:27], a[60:63], v[220:223]\n" \
"ds_read_b64_tr_b16 a[24:25], v33 offset:37024\n" \
"ds_read_b64_tr_b16 a[26:27], v33 offset:41632\n" \
"v_mfma_f32_16x16x32_bf16 v[220:223], a[20:23], a[56:59], v[220:223]\n" \
"ds_read_b64_tr_b16 a[20:21], v33 offset:27808\n" \
"ds_read_b64_tr_b16 a[22:23], v33 offset:32416\n" \
"v_mfma_f32_16x16x32_bf16 v[220:223], a[16:19], a[52:55], v[220:223]\n" \
"ds_read_b64_tr_b16 a[16:17], v33 offset:18592\n" \
"ds_read_b64_tr_b16 a[18:19], v33 offset:23200\n" \
"v_mfma_f32_16x16x32_bf16 v[220:223], a[12:15], a[48:51], v[220:223]\n" \
"ds_read_b64_tr_b16 a[12:13], v10 offset:55456\n" \
"ds_read_b64_tr_b16 a[14:15], v10 offset:60064\n" \
"v_mfma_f32_16x16x32_bf16 v[144:147], a[28:31], a[128:131], v[144:147]\n" \
"v_mfma_f32_16x16x32_bf16 v[144:147], a[192:195], a[132:135], v[144:147]\n" \
"v_mfma_f32_16x16x32_bf16 v[144:147], a[196:199], a[136:139], v[144:147]\n" \
"s_waitcnt lgkmcnt(15)\n" \
"v_mfma_f32_16x16x32_bf16 v[144:147], a[200:203], a[140:143], v[144:147]\n" \
"v_mfma_f32_16x16x32_bf16 v[148:151], a[200:203], a[156:159], v[148:151]\n" \
"v_mfma_f32_16x16x32_bf16 v[148:151], a[196:199], a[152:155], v[148:151]\n" \
"v_mfma_f32_16x16x32_bf16 v[148:151], a[192:195], a[148:151], v[148:151]\n" \
"v_mfma_f32_16x16x32_bf16 v[148:151], a[28:31], a[144:147], v[148:151]\n" \
"v_mfma_f32_16x16x32_bf16 v[216:219], a[28:31], a[160:163], v[216:219]\n" \
"v_mfma_f32_16x16x32_bf16 v[216:219], a[192:195], a[164:167], v[216:219]\n" \
"v_mfma_f32_16x16x32_bf16 v[216:219], a[196:199], a[168:171], v[216:219]\n" \
"v_mfma_f32_16x16x32_bf16 v[216:219], a[200:203], a[172:175], v[216:219]\n" \
"v_mfma_f32_16x16x32_bf16 v[220:223], a[200:203], a[188:191], v[220:223]\n" \
"ds_read_b64_tr_b16 a[200:201], v34 offset:18592\n" \
"ds_read_b64_tr_b16 a[202:203], v34 offset:23200\n" \
"v_mfma_f32_16x16x32_bf16 v[220:223], a[196:199], a[184:187], v[220:223]\n" \
"ds_read_b64_tr_b16 a[196:197], v34 offset:9376\n" \
"ds_read_b64_tr_b16 a[198:199], v34 offset:13984\n" \
"v_mfma_f32_16x16x32_bf16 v[220:223], a[192:195], a[180:183], v[220:223]\n" \
"ds_read_b64_tr_b16 a[192:193], v34 offset:160\n" \
"ds_read_b64_tr_b16 a[194:195], v34 offset:4768\n" \
"v_mfma_f32_16x16x32_bf16 v[220:223], a[28:31], a[176:179], v[220:223]\n" \
"ds_read_b64_tr_b16 a[28:29], v33 offset:46240\n" \
"ds_read_b64_tr_b16 a[30:31], v33 offset:50848\n" \
"v_mfma_f32_16x16x32_bf16 v[144:147], a[204:207], a[96:99], v[144:147]\n" \
"v_mfma_f32_16x16x32_bf16 v[144:147], a[208:211], a[100:103], v[144:147]\n" \
"v_mfma_f32_16x16x32_bf16 v[144:147], a[212:215], a[104:107], v[144:147]\n" \
"s_waitcnt lgkmcnt(8)\n" \
"v_mfma_f32_16x16x32_bf16 v[144:147], a[216:219], a[108:111], v[144:147]\n" \
"v_mfma_f32_16x16x32_bf16 v[148:151], a[216:219], a[124:127], v[148:151]\n" \
"v_mfma_f32_16x16x32_bf16 v[148:151], a[212:215], a[120:123], v[148:151]\n" \
"v_mfma_f32_16x16x32_bf16 v[148:151], a[208:211], a[116:119], v[148:151]\n" \
"v_mfma_f32_16x16x32_bf16 v[148:151], a[204:207], a[112:115], v[148:151]\n" \
"v_mfma_f32_16x16x32_bf16 v[216:219], a[204:207], a[220:223], v[216:219]\n" \
"v_mfma_f32_16x16x32_bf16 v[216:219], a[208:211], a[224:227], v[216:219]\n" \
"v_mfma_f32_16x16x32_bf16 v[216:219], a[212:215], a[228:231], v[216:219]\n" \
"v_mfma_f32_16x16x32_bf16 v[216:219], a[216:219], a[232:235], v[216:219]\n" \
"v_cvt_pk_bf16_f32 v144, v144, v145\n" \
"v_cvt_pk_bf16_f32 v145, v146, v147\n" \
"v_mfma_f32_16x16x32_bf16 v[220:223], a[216:219], a[248:251], v[220:223]\n" \
"ds_read_b64_tr_b16 a[216:217], v10 offset:46272\n" \
"v_cvt_pk_bf16_f32 v146, v148, v149\n" \
"v_cvt_pk_bf16_f32 v147, v150, v151\n" \
"v_mfma_f32_16x16x32_bf16 v[220:223], a[212:215], a[244:247], v[220:223]\n" \
"buffer_store_dwordx4 v[144:147], v20, s[56:59], s71 offen offset:0 sc1\n" \
"v_cvt_pk_bf16_f32 v216, v216, v217\n" \
"v_cvt_pk_bf16_f32 v217, v218, v219\n" \
"v_mfma_f32_16x16x32_bf16 v[220:223], a[208:211], a[240:243], v[220:223]\n" \
"ds_read_b64_tr_b16 a[208:209], v10 offset:27840\n" \
"ds_read_b64_tr_b16 a[210:211], v10 offset:32448\n" \
"v_mfma_f32_16x16x32_bf16 v[220:223], a[204:207], a[236:239], v[220:223]\n" \
"ds_read_b64_tr_b16 a[204:205], v33 offset:160\n" \
"ds_read_b64_tr_b16 a[206:207], v33 offset:4768\n" \
"v_mfma_f32_16x16x32_bf16 v[224:227], a[0:3], a[32:35], v[224:227]\n" \
"ds_read_b64_tr_b16 a[212:213], v10 offset:37056\n" \
"ds_read_b64_tr_b16 a[214:215], v10 offset:41664\n" \
"v_mfma_f32_16x16x32_bf16 v[224:227], a[4:7], a[36:39], v[224:227]\n" \
"ds_read_b64_tr_b16 a[218:219], v10 offset:50880\n" \
"v_mfma_f32_16x16x32_bf16 v[224:227], a[8:11], a[40:43], v[224:227]\n" \
"v_cvt_pk_bf16_f32 v218, v220, v221\n" \
"v_cvt_pk_bf16_f32 v219, v222, v223\n" \
"v_mfma_f32_16x16x32_bf16 v[224:227], a[12:15], a[44:47], v[224:227]\n" \
"buffer_store_dwordx4 v[216:219], v20, s[56:59], s71 offen offset:1024 sc1\n" \
"s_add_u32 s71, s71, 0x1800\n" \
"v_mfma_f32_16x16x32_bf16 v[228:231], a[12:15], a[60:63], v[228:231]\n" \
"ds_read_b64_tr_b16 a[12:13], v33 offset:37056\n" \
"ds_read_b64_tr_b16 a[14:15], v33 offset:41664\n" \
"v_mfma_f32_16x16x32_bf16 v[228:231], a[8:11], a[56:59], v[228:231]\n" \
"ds_read_b64_tr_b16 a[8:9], v33 offset:27840\n" \
"ds_read_b64_tr_b16 a[10:11], v33 offset:32448\n" \
"v_mfma_f32_16x16x32_bf16 v[228:231], a[4:7], a[52:55], v[228:231]\n" \
"ds_read_b64_tr_b16 a[4:5], v33 offset:18624\n" \
"ds_read_b64_tr_b16 a[6:7], v33 offset:23232\n" \
"v_mfma_f32_16x16x32_bf16 v[228:231], a[0:3], a[48:51], v[228:231]\n" \
"ds_read_b64_tr_b16 a[0:1], v10 offset:55488\n" \
"ds_read_b64_tr_b16 a[2:3], v10 offset:60096\n" \
"v_mfma_f32_16x16x32_bf16 v[152:155], a[16:19], a[128:131], v[152:155]\n" \
"v_mfma_f32_16x16x32_bf16 v[152:155], a[20:23], a[132:135], v[152:155]\n" \
"v_mfma_f32_16x16x32_bf16 v[152:155], a[24:27], a[136:139], v[152:155]\n" \
"s_waitcnt lgkmcnt(15)\n" \
"v_mfma_f32_16x16x32_bf16 v[152:155], a[28:31], a[140:143], v[152:155]\n" \
"v_mfma_f32_16x16x32_bf16 v[156:159], a[28:31], a[156:159], v[156:159]\n" \
"v_mfma_f32_16x16x32_bf16 v[156:159], a[24:27], a[152:155], v[156:159]\n" \
"v_mfma_f32_16x16x32_bf16 v[156:159], a[20:23], a[148:151], v[156:159]\n" \
"v_mfma_f32_16x16x32_bf16 v[156:159], a[16:19], a[144:147], v[156:159]\n" \
"v_mfma_f32_16x16x32_bf16 v[224:227], a[16:19], a[160:163], v[224:227]\n" \
"v_mfma_f32_16x16x32_bf16 v[224:227], a[20:23], a[164:167], v[224:227]\n" \
"s_waitcnt vmcnt(4)\n" \
"s_mov_b64 exec, 3\n" \
"buffer_store_dword v39, v41, s[56:59], s69 offen sc1\n" \
"s_mov_b64 exec, -1\n" \
"v_mfma_f32_16x16x32_bf16 v[224:227], a[24:27], a[168:171], v[224:227]\n" \
"s_add_u32 s69, s63, 0x0\n" \
"v_mfma_f32_16x16x32_bf16 v[224:227], a[28:31], a[172:175], v[224:227]\n" \
"v_mfma_f32_16x16x32_bf16 v[228:231], a[28:31], a[188:191], v[228:231]\n" \
"ds_read_b64_tr_b16 a[28:29], v34 offset:18624\n" \
"ds_read_b64_tr_b16 a[30:31], v34 offset:23232\n" \
"v_mfma_f32_16x16x32_bf16 v[228:231], a[24:27], a[184:187], v[228:231]\n" \
"ds_read_b64_tr_b16 a[24:25], v34 offset:9408\n" \
"ds_read_b64_tr_b16 a[26:27], v34 offset:14016\n" \
"v_mfma_f32_16x16x32_bf16 v[228:231], a[20:23], a[180:183], v[228:231]\n" \
"ds_read_b64_tr_b16 a[20:21], v34 offset:192\n" \
"ds_read_b64_tr_b16 a[22:23], v34 offset:4800\n" \
"v_mfma_f32_16x16x32_bf16 v[228:231], a[16:19], a[176:179], v[228:231]\n" \
"ds_read_b64_tr_b16 a[16:17], v33 offset:46272\n" \
"ds_read_b64_tr_b16 a[18:19], v33 offset:50880\n" \
"v_mfma_f32_16x16x32_bf16 v[152:155], a[192:195], a[96:99], v[152:155]\n" \
"v_mfma_f32_16x16x32_bf16 v[152:155], a[196:199], a[100:103], v[152:155]\n" \
"v_mfma_f32_16x16x32_bf16 v[152:155], a[200:203], a[104:107], v[152:155]\n" \
"s_waitcnt lgkmcnt(8)\n" \
"v_mfma_f32_16x16x32_bf16 v[152:155], a[204:207], a[108:111], v[152:155]\n" \
"v_mfma_f32_16x16x32_bf16 v[156:159], a[204:207], a[124:127], v[156:159]\n" \
"v_mfma_f32_16x16x32_bf16 v[156:159], a[200:203], a[120:123], v[156:159]\n" \
"v_mfma_f32_16x16x32_bf16 v[156:159], a[196:199], a[116:119], v[156:159]\n" \
"v_mfma_f32_16x16x32_bf16 v[156:159], a[192:195], a[112:115], v[156:159]\n" \
"v_mfma_f32_16x16x32_bf16 v[224:227], a[192:195], a[220:223], v[224:227]\n" \
"v_mfma_f32_16x16x32_bf16 v[224:227], a[196:199], a[224:227], v[224:227]\n" \
"v_mfma_f32_16x16x32_bf16 v[224:227], a[200:203], a[228:231], v[224:227]\n" \
"v_mfma_f32_16x16x32_bf16 v[224:227], a[204:207], a[232:235], v[224:227]\n" \
"v_cvt_pk_bf16_f32 v152, v152, v153\n" \
"v_cvt_pk_bf16_f32 v153, v154, v155\n" \
"v_mfma_f32_16x16x32_bf16 v[228:231], a[204:207], a[248:251], v[228:231]\n" \
"ds_read_b64_tr_b16 a[204:205], v10 offset:46080\n" \
"v_cvt_pk_bf16_f32 v154, v156, v157\n" \
"v_cvt_pk_bf16_f32 v155, v158, v159\n" \
"v_mfma_f32_16x16x32_bf16 v[228:231], a[200:203], a[244:247], v[228:231]\n" \
"buffer_store_dwordx4 v[152:155], v20, s[56:59], s72 offen offset:0 sc1\n" \
"v_cvt_pk_bf16_f32 v224, v224, v225\n" \
"v_cvt_pk_bf16_f32 v225, v226, v227\n" \
"v_mfma_f32_16x16x32_bf16 v[228:231], a[196:199], a[240:243], v[228:231]\n" \
"ds_read_b64_tr_b16 a[196:197], v10 offset:27648\n" \
"ds_read_b64_tr_b16 a[198:199], v10 offset:32256\n" \
"v_mfma_f32_16x16x32_bf16 v[228:231], a[192:195], a[236:239], v[228:231]\n" \
"ds_read_b64_tr_b16 a[192:193], v33 offset:192\n" \
"ds_read_b64_tr_b16 a[194:195], v33 offset:4800\n" \
"v_mfma_f32_16x16x32_bf16 v[232:235], a[208:211], a[32:35], v[232:235]\n" \
"ds_read_b64_tr_b16 a[200:201], v10 offset:36864\n" \
"ds_read_b64_tr_b16 a[202:203], v10 offset:41472\n" \
"v_mfma_f32_16x16x32_bf16 v[232:235], a[212:215], a[36:39], v[232:235]\n" \
"ds_read_b64_tr_b16 a[206:207], v10 offset:50688\n" \
"v_mfma_f32_16x16x32_bf16 v[232:235], a[216:219], a[40:43], v[232:235]\n" \
"v_cvt_pk_bf16_f32 v226, v228, v229\n" \
"v_cvt_pk_bf16_f32 v227, v230, v231\n" \
"v_mfma_f32_16x16x32_bf16 v[232:235], a[0:3], a[44:47], v[232:235]\n" \
"buffer_store_dwordx4 v[224:227], v20, s[56:59], s72 offen offset:1024 sc1\n" \
"s_add_u32 s72, s72, 0x1000\n" \
"v_mfma_f32_16x16x32_bf16 v[236:239], a[0:3], a[60:63], v[236:239]\n" \
"ds_read_b64_tr_b16 a[0:1], v33 offset:36864\n" \
"ds_read_b64_tr_b16 a[2:3], v33 offset:41472\n" \
"v_mfma_f32_16x16x32_bf16 v[236:239], a[216:219], a[56:59], v[236:239]\n" \
"ds_read_b64_tr_b16 a[216:217], v33 offset:27648\n" \
"ds_read_b64_tr_b16 a[218:219], v33 offset:32256\n" \
"v_mfma_f32_16x16x32_bf16 v[236:239], a[212:215], a[52:55], v[236:239]\n" \
"ds_read_b64_tr_b16 a[212:213], v33 offset:18432\n" \
"ds_read_b64_tr_b16 a[214:215], v33 offset:23040\n" \
"v_mfma_f32_16x16x32_bf16 v[236:239], a[208:211], a[48:51], v[236:239]\n" \
"ds_read_b64_tr_b16 a[208:209], v10 offset:55296\n" \
"ds_read_b64_tr_b16 a[210:211], v10 offset:59904\n" \
"v_mfma_f32_16x16x32_bf16 v[160:163], a[4:7], a[128:131], v[160:163]\n" \
"v_mfma_f32_16x16x32_bf16 v[160:163], a[8:11], a[132:135], v[160:163]\n" \
"v_mfma_f32_16x16x32_bf16 v[160:163], a[12:15], a[136:139], v[160:163]\n" \
"s_waitcnt lgkmcnt(15)\n" \
"v_mfma_f32_16x16x32_bf16 v[160:163], a[16:19], a[140:143], v[160:163]\n" \
"v_mfma_f32_16x16x32_bf16 v[164:167], a[16:19], a[156:159], v[164:167]\n" \
"v_mfma_f32_16x16x32_bf16 v[164:167], a[12:15], a[152:155], v[164:167]\n" \
"v_mfma_f32_16x16x32_bf16 v[164:167], a[8:11], a[148:151], v[164:167]\n" \
"v_mfma_f32_16x16x32_bf16 v[164:167], a[4:7], a[144:147], v[164:167]\n" \
"s_waitcnt vmcnt(5)\n" \
"s_mov_b64 exec, 1\n" \
"buffer_store_dword v39, off, s[56:59], s70 offset:16 sc1\n" \
"s_mov_b64 exec, -1\n" \
"v_mfma_f32_16x16x32_bf16 v[232:235], a[4:7], a[160:163], v[232:235]\n" \
"s_add_u32 s70, s63, 0x800\n" \
"v_mfma_f32_16x16x32_bf16 v[232:235], a[8:11], a[164:167], v[232:235]\n" \
"v_mfma_f32_16x16x32_bf16 v[232:235], a[12:15], a[168:171], v[232:235]\n" \
"v_mfma_f32_16x16x32_bf16 v[232:235], a[16:19], a[172:175], v[232:235]\n" \
"v_mfma_f32_16x16x32_bf16 v[236:239], a[16:19], a[188:191], v[236:239]\n" \
"ds_read_b64_tr_b16 a[16:17], v34 offset:18432\n" \
"ds_read_b64_tr_b16 a[18:19], v34 offset:23040\n" \
"v_mfma_f32_16x16x32_bf16 v[236:239], a[12:15], a[184:187], v[236:239]\n" \
"ds_read_b64_tr_b16 a[12:13], v34 offset:9216\n" \
"ds_read_b64_tr_b16 a[14:15], v34 offset:13824\n" \
"v_mfma_f32_16x16x32_bf16 v[236:239], a[8:11], a[180:183], v[236:239]\n" \
"ds_read_b64_tr_b16 a[8:9], v34 offset:0\n" \
"ds_read_b64_tr_b16 a[10:11], v34 offset:4608\n" \
"v_mfma_f32_16x16x32_bf16 v[236:239], a[4:7], a[176:179], v[236:239]\n" \
"ds_read_b64_tr_b16 a[4:5], v33 offset:46080\n" \
"ds_read_b64_tr_b16 a[6:7], v33 offset:50688\n" \
"v_mfma_f32_16x16x32_bf16 v[160:163], a[20:23], a[96:99], v[160:163]\n" \
"v_mfma_f32_16x16x32_bf16 v[160:163], a[24:27], a[100:103], v[160:163]\n" \
"v_mfma_f32_16x16x32_bf16 v[160:163], a[28:31], a[104:107], v[160:163]\n" \
"s_waitcnt lgkmcnt(8)\n" \
"v_mfma_f32_16x16x32_bf16 v[160:163], a[192:195], a[108:111], v[160:163]\n" \
"v_mfma_f32_16x16x32_bf16 v[164:167], a[192:195], a[124:127], v[164:167]\n" \
"v_mfma_f32_16x16x32_bf16 v[164:167], a[28:31], a[120:123], v[164:167]\n" \
"v_mfma_f32_16x16x32_bf16 v[164:167], a[24:27], a[116:119], v[164:167]\n" \
"v_mfma_f32_16x16x32_bf16 v[164:167], a[20:23], a[112:115], v[164:167]\n" \
"v_mfma_f32_16x16x32_bf16 v[232:235], a[20:23], a[220:223], v[232:235]\n" \
"v_mfma_f32_16x16x32_bf16 v[232:235], a[24:27], a[224:227], v[232:235]\n" \
"v_mfma_f32_16x16x32_bf16 v[232:235], a[28:31], a[228:231], v[232:235]\n" \
"v_mfma_f32_16x16x32_bf16 v[232:235], a[192:195], a[232:235], v[232:235]\n" \
"v_cvt_pk_bf16_f32 v160, v160, v161\n" \
"v_cvt_pk_bf16_f32 v161, v162, v163\n" \
"v_mfma_f32_16x16x32_bf16 v[236:239], a[192:195], a[248:251], v[236:239]\n" \
"ds_read_b64_tr_b16 a[192:193], v10 offset:46112\n" \
"v_cvt_pk_bf16_f32 v162, v164, v165\n" \
"v_cvt_pk_bf16_f32 v163, v166, v167\n" \
"v_mfma_f32_16x16x32_bf16 v[236:239], a[28:31], a[244:247], v[236:239]\n" \
"buffer_store_dwordx4 v[160:163], v20, s[56:59], s73 offen offset:0 sc1\n" \
"v_cvt_pk_bf16_f32 v232, v232, v233\n" \
"v_cvt_pk_bf16_f32 v233, v234, v235\n" \
"v_mfma_f32_16x16x32_bf16 v[236:239], a[24:27], a[240:243], v[236:239]\n" \
"ds_read_b64_tr_b16 a[24:25], v10 offset:27680\n" \
"ds_read_b64_tr_b16 a[26:27], v10 offset:32288\n" \
"v_mfma_f32_16x16x32_bf16 v[236:239], a[20:23], a[236:239], v[236:239]\n" \
"ds_read_b64_tr_b16 a[20:21], v33 offset:0\n" \
"ds_read_b64_tr_b16 a[22:23], v33 offset:4608\n" \
"v_mfma_f32_16x16x32_bf16 v[184:187], a[196:199], a[32:35], v[184:187]\n" \
"ds_read_b64_tr_b16 a[28:29], v10 offset:36896\n" \
"ds_read_b64_tr_b16 a[30:31], v10 offset:41504\n" \
"v_mfma_f32_16x16x32_bf16 v[184:187], a[200:203], a[36:39], v[184:187]\n" \
"ds_read_b64_tr_b16 a[194:195], v10 offset:50720\n" \
"v_mfma_f32_16x16x32_bf16 v[184:187], a[204:207], a[40:43], v[184:187]\n" \
"v_cvt_pk_bf16_f32 v234, v236, v237\n" \
"v_cvt_pk_bf16_f32 v235, v238, v239\n" \
"v_mfma_f32_16x16x32_bf16 v[184:187], a[208:211], a[44:47], v[184:187]\n" \
"buffer_store_dwordx4 v[232:235], v20, s[56:59], s73 offen offset:1024 sc1\n" \
"s_add_u32 s73, s73, 0x800\n" \
"v_mfma_f32_16x16x32_bf16 v[188:191], a[208:211], a[60:63], v[188:191]\n" \
"ds_read_b64_tr_b16 a[208:209], v33 offset:36896\n" \
"ds_read_b64_tr_b16 a[210:211], v33 offset:41504\n" \
"v_mfma_f32_16x16x32_bf16 v[188:191], a[204:207], a[56:59], v[188:191]\n" \
"ds_read_b64_tr_b16 a[204:205], v33 offset:27680\n" \
"ds_read_b64_tr_b16 a[206:207], v33 offset:32288\n" \
"v_mfma_f32_16x16x32_bf16 v[188:191], a[200:203], a[52:55], v[188:191]\n" \
"ds_read_b64_tr_b16 a[200:201], v33 offset:18464\n" \
"ds_read_b64_tr_b16 a[202:203], v33 offset:23072\n" \
"v_mfma_f32_16x16x32_bf16 v[188:191], a[196:199], a[48:51], v[188:191]\n" \
"ds_read_b64_tr_b16 a[196:197], v10 offset:55328\n" \
"ds_read_b64_tr_b16 a[198:199], v10 offset:59936\n" \
"v_mfma_f32_16x16x32_bf16 v[184:187], a[212:215], a[160:163], v[184:187]\n" \
"v_mfma_f32_16x16x32_bf16 v[184:187], a[216:219], a[164:167], v[184:187]\n" \
"v_mfma_f32_16x16x32_bf16 v[184:187], a[0:3], a[168:171], v[184:187]\n" \
"s_waitcnt lgkmcnt(15)\n" \
"v_mfma_f32_16x16x32_bf16 v[184:187], a[4:7], a[172:175], v[184:187]\n" \
"v_mfma_f32_16x16x32_bf16 v[188:191], a[4:7], a[188:191], v[188:191]\n" \
"ds_read_b64_tr_b16 a[4:5], v34 offset:18464\n" \
"ds_read_b64_tr_b16 a[6:7], v34 offset:23072\n" \
"v_mfma_f32_16x16x32_bf16 v[188:191], a[0:3], a[184:187], v[188:191]\n" \
"ds_read_b64_tr_b16 a[0:1], v34 offset:9248\n" \
"ds_read_b64_tr_b16 a[2:3], v34 offset:13856\n" \
"s_waitcnt vmcnt(6)\n" \
"s_mov_b64 exec, 1\n" \
"buffer_store_dword v39, off, s[56:59], s71 offset:20 sc1\n" \
"s_mov_b64 exec, -1\n" \
"v_mfma_f32_16x16x32_bf16 v[188:191], a[216:219], a[180:183], v[188:191]\n" \
"ds_read_b64_tr_b16 a[216:217], v34 offset:32\n" \
"ds_read_b64_tr_b16 a[218:219], v34 offset:4640\n" \
"v_mfma_f32_16x16x32_bf16 v[188:191], a[212:215], a[176:179], v[188:191]\n" \
"ds_read_b64_tr_b16 a[212:213], v33 offset:46112\n" \
"ds_read_b64_tr_b16 a[214:215], v33 offset:50720\n" \
"v_mfma_f32_16x16x32_bf16 v[184:187], a[8:11], a[220:223], v[184:187]\n" \
"s_add_u32 s71, s63, 0x1000\n" \
"v_mfma_f32_16x16x32_bf16 v[184:187], a[12:15], a[224:227], v[184:187]\n" \
"v_mfma_f32_16x16x32_bf16 v[184:187], a[16:19], a[228:231], v[184:187]\n" \
"s_waitcnt lgkmcnt(12)\n" \
"v_mfma_f32_16x16x32_bf16 v[184:187], a[20:23], a[232:235], v[184:187]\n" \
"v_mfma_f32_16x16x32_bf16 v[188:191], a[20:23], a[248:251], v[188:191]\n" \
"ds_read_b64_tr_b16 a[20:21], v10 offset:46144\n" \
"ds_read_b64_tr_b16 a[22:23], v10 offset:50752\n" \
"v_mfma_f32_16x16x32_bf16 v[188:191], a[16:19], a[244:247], v[188:191]\n" \
"ds_read_b64_tr_b16 a[16:17], v10 offset:36928\n" \
"ds_read_b64_tr_b16 a[18:19], v10 offset:41536\n" \
"v_mfma_f32_16x16x32_bf16 v[188:191], a[12:15], a[240:243], v[188:191]\n" \
"ds_read_b64_tr_b16 a[12:13], v10 offset:27712\n" \
"v_cvt_pk_bf16_f32 v184, v184, v185\n" \
"v_cvt_pk_bf16_f32 v185, v186, v187\n" \
"v_mfma_f32_16x16x32_bf16 v[188:191], a[8:11], a[236:239], v[188:191]\n" \
"ds_read_b64_tr_b16 a[8:9], v33 offset:32\n" \
"ds_read_b64_tr_b16 a[10:11], v33 offset:4640\n" \
"v_mfma_f32_16x16x32_bf16 v[192:195], a[24:27], a[32:35], v[192:195]\n" \
"ds_read_b64_tr_b16 a[14:15], v10 offset:32320\n" \
"v_mfma_f32_16x16x32_bf16 v[192:195], a[28:31], a[36:39], v[192:195]\n" \
"v_mfma_f32_16x16x32_bf16 v[192:195], a[192:195], a[40:43], v[192:195]\n" \
"s_waitcnt lgkmcnt(12)\n" \
"v_mfma_f32_16x16x32_bf16 v[192:195], a[196:199], a[44:47], v[192:195]\n" \
"v_cvt_pk_bf16_f32 v186, v188, v189\n" \
"v_cvt_pk_bf16_f32 v187, v190, v191\n" \
"v_mfma_f32_16x16x32_bf16 v[196:199], a[196:199], a[60:63], v[196:199]\n" \
"buffer_store_dwordx4 v[184:187], v20, s[56:59], s92 offen offset:1024 sc1\n" \
"s_add_u32 s92, s92, 0x3800\n" \
"v_mfma_f32_16x16x32_bf16 v[196:199], a[192:195], a[56:59], v[196:199]\n" \
"ds_read_b64_tr_b16 a[192:193], v33 offset:27712\n" \
"ds_read_b64_tr_b16 a[194:195], v33 offset:32320\n" \
"v_mfma_f32_16x16x32_bf16 v[196:199], a[28:31], a[52:55], v[196:199]\n" \
"ds_read_b64_tr_b16 a[28:29], v33 offset:18496\n" \
"ds_read_b64_tr_b16 a[30:31], v33 offset:23104\n" \
"v_mfma_f32_16x16x32_bf16 v[196:199], a[24:27], a[48:51], v[196:199]\n" \
"ds_read_b64_tr_b16 a[24:25], v10 offset:55360\n" \
"ds_read_b64_tr_b16 a[26:27], v10 offset:59968\n" \
"v_mfma_f32_16x16x32_bf16 v[192:195], a[200:203], a[160:163], v[192:195]\n" \
"ds_read_b64_tr_b16 a[196:197], v33 offset:36928\n" \
"ds_read_b64_tr_b16 a[198:199], v33 offset:41536\n" \
"L_pf22:\n" \
"s_cbranch_execz L_retB\n" \
"s_nop 0\n" \
"v_mfma_f32_16x16x32_bf16 v[192:195], a[204:207], a[164:167], v[192:195]\n" \
"v_mfma_f32_16x16x32_bf16 v[192:195], a[208:211], a[168:171], v[192:195]\n" \
"s_waitcnt lgkmcnt(14)\n" \
"v_mfma_f32_16x16x32_bf16 v[192:195], a[212:215], a[172:175], v[192:195]\n" \
"v_mfma_f32_16x16x32_bf16 v[196:199], a[212:215], a[188:191], v[196:199]\n" \
"ds_read_b64_tr_b16 a[212:213], v34 offset:18496\n" \
"ds_read_b64_tr_b16 a[214:215], v34 offset:23104\n" \
"v_mfma_f32_16x16x32_bf16 v[196:199], a[208:211], a[184:187], v[196:199]\n" \
"ds_read_b64_tr_b16 a[208:209], v34 offset:9280\n" \
"ds_read_b64_tr_b16 a[210:211], v34 offset:13888\n" \
"v_mfma_f32_16x16x32_bf16 v[196:199], a[204:207], a[180:183], v[196:199]\n" \
"ds_read_b64_tr_b16 a[204:205], v34 offset:64\n" \
"ds_read_b64_tr_b16 a[206:207], v34 offset:4672\n" \
"v_mfma_f32_16x16x32_bf16 v[196:199], a[200:203], a[176:179], v[196:199]\n" \
"ds_read_b64_tr_b16 a[200:201], v33 offset:46144\n" \
"ds_read_b64_tr_b16 a[202:203], v33 offset:50752\n" \
"v_mfma_f32_16x16x32_bf16 v[192:195], a[216:219], a[220:223], v[192:195]\n" \
"v_mfma_f32_16x16x32_bf16 v[192:195], a[0:3], a[224:227], v[192:195]\n" \
"v_mfma_f32_16x16x32_bf16 v[192:195], a[4:7], a[228:231], v[192:195]\n" \
"s_waitcnt lgkmcnt(15)\n" \
"v_mfma_f32_16x16x32_bf16 v[192:195], a[8:11], a[232:235], v[192:195]\n" \
"v_mfma_f32_16x16x32_bf16 v[196:199], a[8:11], a[248:251], v[196:199]\n" \
"ds_read_b64_tr_b16 a[8:9], v10 offset:46304\n" \
"ds_read_b64_tr_b16 a[10:11], v10 offset:50912\n" \
"v_mfma_f32_16x16x32_bf16 v[196:199], a[4:7], a[244:247], v[196:199]\n" \
"ds_read_b64_tr_b16 a[4:5], v10 offset:37088\n" \
"ds_read_b64_tr_b16 a[6:7], v10 offset:41696\n" \
"v_mfma_f32_16x16x32_bf16 v[196:199], a[0:3], a[240:243], v[196:199]\n" \
"ds_read_b64_tr_b16 a[0:1], v10 offset:27872\n" \
"v_cvt_pk_bf16_f32 v192, v192, v193\n" \
"v_cvt_pk_bf16_f32 v193, v194, v195\n" \
"v_mfma_f32_16x16x32_bf16 v[196:199], a[216:219], a[236:239], v[196:199]\n" \
"ds_read_b64_tr_b16 a[216:217], v33 offset:64\n" \
"ds_read_b64_tr_b16 a[218:219], v33 offset:4672\n" \
"v_mfma_f32_16x16x32_bf16 v[200:203], a[12:15], a[32:35], v[200:203]\n" \
"ds_read_b64_tr_b16 a[2:3], v10 offset:32480\n" \
"v_mfma_f32_16x16x32_bf16 v[200:203], a[16:19], a[36:39], v[200:203]\n" \
"v_mfma_f32_16x16x32_bf16 v[200:203], a[20:23], a[40:43], v[200:203]\n" \
"s_waitcnt lgkmcnt(14)\n" \
"v_mfma_f32_16x16x32_bf16 v[200:203], a[24:27], a[44:47], v[200:203]\n" \
"v_cvt_pk_bf16_f32 v194, v196, v197\n" \
"v_cvt_pk_bf16_f32 v195, v198, v199\n" \
"v_mfma_f32_16x16x32_bf16 v[204:207], a[24:27], a[60:63], v[204:207]\n" \
"buffer_store_dwordx4 v[192:195], v20, s[56:59], s93 offen offset:1024 sc1\n" \
"s_add_u32 s93, s93, 0x3000\n" \
"v_mfma_f32_16x16x32_bf16 v[204:207], a[20:23], a[56:59], v[204:207]\n" \
"ds_read_b64_tr_b16 a[20:21], v33 offset:27872\n" \
"ds_read_b64_tr_b16 a[22:23], v33 offset:32480\n" \
"s_waitcnt vmcnt(6)\n" \
"s_mov_b64 exec, 1\n" \
"buffer_store_dword v39, off, s[56:59], s72 offset:24 sc1\n" \
"s_mov_b64 exec, -1\n" \
"v_mfma_f32_16x16x32_bf16 v[204:207], a[16:19], a[52:55], v[204:207]\n" \
"ds_read_b64_tr_b16 a[16:17], v33 offset:18656\n" \
"ds_read_b64_tr_b16 a[18:19], v33 offset:23264\n" \
"v_mfma_f32_16x16x32_bf16 v[204:207], a[12:15], a[48:51], v[204:207]\n" \
"ds_read_b64_tr_b16 a[12:13], v10 offset:55520\n" \
"ds_read_b64_tr_b16 a[14:15], v10 offset:60128\n" \
"v_mfma_f32_16x16x32_bf16 v[200:203], a[28:31], a[160:163], v[200:203]\n" \
"ds_read_b64_tr_b16 a[24:25], v33 offset:37088\n" \
"ds_read_b64_tr_b16 a[26:27], v33 offset:41696\n" \
"v_mfma_f32_16x16x32_bf16 v[200:203], a[192:195], a[164:167], v[200:203]\n" \
"v_readlane_b32 s72, v11, 50\n" \
"v_mfma_f32_16x16x32_bf16 v[200:203], a[196:199], a[168:171], v[200:203]\n" \
"s_waitcnt lgkmcnt(12)\n" \
"v_mfma_f32_16x16x32_bf16 v[200:203], a[200:203], a[172:175], v[200:203]\n" \
"v_mfma_f32_16x16x32_bf16 v[204:207], a[200:203], a[188:191], v[204:207]\n" \
"ds_read_b64_tr_b16 a[200:201], v34 offset:18656\n" \
"ds_read_b64_tr_b16 a[202:203], v34 offset:23264\n" \
"v_mfma_f32_16x16x32_bf16 v[204:207], a[196:199], a[184:187], v[204:207]\n" \
"ds_read_b64_tr_b16 a[196:197], v34 offset:9440\n" \
"ds_read_b64_tr_b16 a[198:199], v34 offset:14048\n" \
"v_mfma_f32_16x16x32_bf16 v[204:207], a[192:195], a[180:183], v[204:207]\n" \
"ds_read_b64_tr_b16 a[192:193], v34 offset:224\n" \
"ds_read_b64_tr_b16 a[194:195], v34 offset:4832\n" \
"v_mfma_f32_16x16x32_bf16 v[204:207], a[28:31], a[176:179], v[204:207]\n" \
"ds_read_b64_tr_b16 a[28:29], v33 offset:46304\n" \
"ds_read_b64_tr_b16 a[30:31], v33 offset:50912\n" \
"v_mfma_f32_16x16x32_bf16 v[200:203], a[204:207], a[220:223], v[200:203]\n" \
"v_mfma_f32_16x16x32_bf16 v[200:203], a[208:211], a[224:227], v[200:203]\n" \
"v_mfma_f32_16x16x32_bf16 v[200:203], a[212:215], a[228:231], v[200:203]\n" \
"s_waitcnt lgkmcnt(14)\n" \
"v_mfma_f32_16x16x32_bf16 v[200:203], a[216:219], a[232:235], v[200:203]\n" \
"v_mfma_f32_16x16x32_bf16 v[204:207], a[216:219], a[248:251], v[204:207]\n" \
"ds_read_b64_tr_b16 a[216:217], v36 offset:32\n" \
"ds_read_b64_tr_b16 a[218:219], v36 offset:1056\n" \
"v_mfma_f32_16x16x32_bf16 v[204:207], a[212:215], a[244:247], v[204:207]\n" \
"ds_read_b64_tr_b16 a[212:213], v36 offset:2048\n" \
"ds_read_b64_tr_b16 a[214:215], v36 offset:3072\n" \
"v_mfma_f32_16x16x32_bf16 v[204:207], a[208:211], a[240:243], v[204:207]\n" \
"ds_read_b64_tr_b16 a[208:209], v36 offset:0\n" \
"v_cvt_pk_bf16_f32 v200, v200, v201\n" \
"v_cvt_pk_bf16_f32 v201, v202, v203\n" \
"v_mfma_f32_16x16x32_bf16 v[204:207], a[204:207], a[236:239], v[204:207]\n" \
"ds_read_b64_tr_b16 a[204:205], v33 offset:224\n" \
"ds_read_b64_tr_b16 a[206:207], v33 offset:4832\n" \
"v_mfma_f32_16x16x32_bf16 v[240:243], a[0:3], a[32:35], v[240:243]\n" \
"ds_read_b64_tr_b16 a[210:211], v36 offset:1024\n" \
"v_mfma_f32_16x16x32_bf16 v[240:243], a[4:7], a[36:39], v[240:243]\n" \
"v_mfma_f32_16x16x32_bf16 v[240:243], a[8:11], a[40:43], v[240:243]\n" \
"s_waitcnt lgkmcnt(14)\n" \
"v_mfma_f32_16x16x32_bf16 v[240:243], a[12:15], a[44:47], v[240:243]\n" \
"v_cvt_pk_bf16_f32 v202, v204, v205\n" \
"v_cvt_pk_bf16_f32 v203, v206, v207\n" \
"v_mfma_f32_16x16x32_bf16 v[244:247], a[12:15], a[60:63], v[244:247]\n" \
"buffer_store_dwordx4 v[200:203], v20, s[56:59], s94 offen offset:1024 sc1\n" \
"s_add_u32 s94, s94, 0x2800\n" \
"v_mfma_f32_16x16x32_bf16 v[244:247], a[8:11], a[56:59], v[244:247]\n" \
"v_mfma_f32_16x16x32_bf16 v[244:247], a[4:7], a[52:55], v[244:247]\n" \
"v_mfma_f32_16x16x32_bf16 v[244:247], a[0:3], a[48:51], v[244:247]\n" \
"ds_read_b64_tr_b16 a[0:1], v36 offset:2080\n" \
"ds_read_b64_tr_b16 a[2:3], v36 offset:3104\n" \
"v_mfma_f32_16x16x32_bf16 v[168:171], a[16:19], a[128:131], v[168:171]\n" \
"v_mfma_f32_16x16x32_bf16 v[168:171], a[20:23], a[132:135], v[168:171]\n" \
"v_mfma_f32_16x16x32_bf16 v[168:171], a[24:27], a[136:139], v[168:171]\n" \
"s_waitcnt lgkmcnt(8)\n" \
"v_mfma_f32_16x16x32_bf16 v[168:171], a[28:31], a[140:143], v[168:171]\n" \
"v_mfma_f32_16x16x32_bf16 v[172:175], a[28:31], a[156:159], v[172:175]\n" \
"v_mfma_f32_16x16x32_bf16 v[172:175], a[24:27], a[152:155], v[172:175]\n" \
"v_mfma_f32_16x16x32_bf16 v[172:175], a[20:23], a[148:151], v[172:175]\n" \
"v_mfma_f32_16x16x32_bf16 v[172:175], a[16:19], a[144:147], v[172:175]\n" \
"v_mfma_f32_16x16x32_bf16 v[240:243], a[16:19], a[160:163], v[240:243]\n" \
"v_mfma_f32_16x16x32_bf16 v[240:243], a[20:23], a[164:167], v[240:243]\n" \
"v_mfma_f32_16x16x32_bf16 v[240:243], a[24:27], a[168:171], v[240:243]\n" \
"v_mfma_f32_16x16x32_bf16 v[240:243], a[28:31], a[172:175], v[240:243]\n" \
"v_mfma_f32_16x16x32_bf16 v[244:247], a[28:31], a[188:191], v[244:247]\n" \
"v_mfma_f32_16x16x32_bf16 v[244:247], a[24:27], a[184:187], v[244:247]\n" \
"s_waitcnt vmcnt(5)\n" \
"s_mov_b64 exec, 1\n" \
"buffer_store_dword v39, off, s[56:59], s73 offset:28 sc1\n" \
"s_mov_b64 exec, -1\n" \
"v_mfma_f32_16x16x32_bf16 v[244:247], a[20:23], a[180:183], v[244:247]\n" \
"v_readlane_b32 s73, v11, 41\n" \
"v_mfma_f32_16x16x32_bf16 v[244:247], a[16:19], a[176:179], v[244:247]\n" \
"v_mfma_f32_16x16x32_bf16 v[168:171], a[192:195], a[96:99], v[168:171]\n" \
"v_mfma_f32_16x16x32_bf16 v[168:171], a[196:199], a[100:103], v[168:171]\n" \
"v_mfma_f32_16x16x32_bf16 v[168:171], a[200:203], a[104:107], v[168:171]\n" \
"s_waitcnt lgkmcnt(0)\n" \
"v_mfma_f32_16x16x32_bf16 v[168:171], a[204:207], a[108:111], v[168:171]\n" \
"v_mfma_f32_16x16x32_bf16 v[172:175], a[204:207], a[124:127], v[172:175]\n" \
"v_mfma_f32_16x16x32_bf16 v[172:175], a[200:203], a[120:123], v[172:175]\n" \
"v_mfma_f32_16x16x32_bf16 v[172:175], a[196:199], a[116:119], v[172:175]\n" \
"v_mfma_f32_16x16x32_bf16 v[172:175], a[192:195], a[112:115], v[172:175]\n" \
"v_mfma_f32_16x16x32_bf16 v[240:243], a[192:195], a[220:223], v[240:243]\n" \
"v_mfma_f32_16x16x32_bf16 v[240:243], a[196:199], a[224:227], v[240:243]\n" \
"v_mfma_f32_16x16x32_bf16 v[240:243], a[200:203], a[228:231], v[240:243]\n" \
"v_mfma_f32_16x16x32_bf16 v[240:243], a[204:207], a[232:235], v[240:243]\n" \
"v_mfma_f32_16x16x32_bf16 v[244:247], a[204:207], a[248:251], v[244:247]\n" \
"v_mfma_f32_16x16x32_bf16 v[244:247], a[200:203], a[244:247], v[244:247]\n" \
"v_mfma_f32_16x16x32_bf16 v[244:247], a[196:199], a[240:243], v[244:247]\n" \
"v_mfma_f32_16x16x32_bf16 v[244:247], a[192:195], a[236:239], v[244:247]\n" \
"v_mfma_f32_16x16x32_bf16 v[168:171], a[208:211], v[64:67], v[168:171]\n" \
"v_mfma_f32_16x16x32_bf16 v[172:175], a[208:211], v[72:75], v[172:175]\n" \
"v_mfma_f32_16x16x32_bf16 v[240:243], a[208:211], v[80:83], v[240:243]\n" \
"v_mfma_f32_16x16x32_bf16 v[244:247], a[208:211], v[88:91], v[244:247]\n" \
"v_mfma_f32_16x16x32_bf16 v[168:171], a[212:215], v[68:71], v[168:171]\n" \
"v_mfma_f32_16x16x32_bf16 v[172:175], a[212:215], v[76:79], v[172:175]\n" \
"v_mfma_f32_16x16x32_bf16 v[240:243], a[212:215], v[84:87], v[240:243]\n" \
"v_mfma_f32_16x16x32_bf16 v[244:247], a[212:215], v[92:95], v[244:247]\n" \
"s_waitcnt vmcnt(4)\n" \
"s_mov_b64 exec, 1\n" \
"buffer_store_dword v39, off, s[56:59], s92 offset:32 sc1\n" \
"s_mov_b64 exec, -1\n" \
"v_mfma_f32_16x16x32_bf16 v[56:59], a[216:219], v[96:99], 0\n" \
"v_readlane_b32 s92, v11, 39\n" \
"v_mfma_f32_16x16x32_bf16 v[60:63], a[216:219], v[104:107], 0\n" \
"v_mfma_f32_16x16x32_bf16 v[56:59], a[0:3], v[100:103], v[56:59]\n" \
"v_mfma_f32_16x16x32_bf16 v[60:63], a[0:3], v[108:111], v[60:63]\n" \
"s_waitcnt vmcnt(4)\n" \
"s_mov_b64 exec, 1\n" \
"buffer_store_dword v39, off, s[56:59], s93 offset:36 sc1\n" \
"s_mov_b64 exec, -1\n" \
"v_readlane_b32 s93, v11, 40\n" \
"s_waitcnt vmcnt(3)\n" \
"s_mov_b64 exec, 1\n" \
"buffer_store_dword v39, off, s[56:59], s94 offset:40 sc1\n" \
"s_mov_b64 exec, -1\n" \
"buffer_load_dword v37, v40, s[56:59], s74 offen sc1\n" \
"s_waitcnt vmcnt(0)\n" \
"v_cmp_eq_u32_e64 s[98:99], s62, v37\n" \
"s_nop 0\n" \
"s_andn2_b32 s75, 0x1, s98\n" \
"s_cbranch_scc1 L_sx0_%=\n" \
"L_rx0_%=:\n" \
"buffer_load_dwordx4 v[116:119], v20, s[56:59], s95 offen offset:0 sc1\n" \
"s_andn2_b32 s75, 0x2, s98\n" \
"s_cbranch_scc1 L_sx1_%=\n" \
"L_rx1_%=:\n" \
"buffer_load_dwordx4 v[124:127], v20, s[56:59], s96 offen offset:0 sc1\n" \
"s_andn2_b32 s75, 0x4, s98\n" \
"s_cbranch_scc1 L_sx2_%=\n" \
"L_rx2_%=:\n" \
"buffer_load_dwordx4 v[132:135], v20, s[56:59], s97 offen offset:0 sc1\n" \
"s_andn2_b32 s75, s100, s98\n" \
"s_cbranch_scc1 L_sx3_%=\n" \
"L_rx3_%=:\n" \
"buffer_load_dwordx4 v[136:139], v20, s[56:59], s64 offen offset:0 sc1\n" \
"buffer_load_dwordx4 v[208:211], v20, s[56:59], s64 offen offset:2048 sc1\n" \
"s_add_u32 s64, s64, 0x1000\n" \
"buffer_load_dwordx4 v[144:147], v20, s[56:59], s64 offen offset:0 sc1\n" \
"buffer_load_dwordx4 v[216:219], v20, s[56:59], s64 offen offset:2048 sc1\n" \
"s_add_u32 s64, s64, 0x1000\n" \
"buffer_load_dwordx4 v[152:155], v20, s[56:59], s64 offen offset:0 sc1\n" \
"buffer_load_dwordx4 v[224:227], v20, s[56:59], s64 offen offset:2048 sc1\n" \
"s_add_u32 s64, s64, 0x1000\n" \
"buffer_load_dwordx4 v[176:179], v20, s[56:59], s64 offen offset:0 sc1\n" \
"buffer_load_dwordx4 v[248:251], v20, s[56:59], s64 offen offset:2048 sc1\n" \
"s_andn2_b32 s75, 0x10, s98\n" \
"s_cbranch_scc1 L_sx4_%=\n" \
"L_rx4_%=:\n" \
"buffer_load_dwordx4 v[140:143], v20, s[56:59], s65 offen offset:0 sc1\n" \
"buffer_load_dwordx4 v[212:215], v20, s[56:59], s65 offen offset:1024 sc1\n" \
"s_andn2_b32 s75, 0x20, s98\n" \
"s_cbranch_scc1 L_sx5_%=\n" \
"L_rx5_%=:\n" \
"buffer_load_dwordx4 v[148:151], v20, s[56:59], s66 offen offset:0 sc1\n" \
"buffer_load_dwordx4 v[220:223], v20, s[56:59], s66 offen offset:1024 sc1\n" \
"s_andn2_b32 s75, 0x40, s98\n" \
"s_cbranch_scc1 L_sx6_%=\n" \
"L_rx6_%=:\n" \
"buffer_load_dwordx4 v[156:159], v20, s[56:59], s67 offen offset:0 sc1\n" \
"buffer_load_dwordx4 v[228:231], v20, s[56:59], s67 offen offset:1024 sc1\n" \
"s_andn2_b32 s75, 0x80, s98\n" \
"s_cbranch_scc1 L_sx7_%=\n" \
"L_rx7_%=:\n" \
"buffer_load_dwordx4 v[164:167], v20, s[56:59], s68 offen offset:0 sc1\n" \
"buffer_load_dwordx4 v[236:239], v20, s[56:59], s68 offen offset:1024 sc1\n" \
"s_andn2_b32 s75, 0x100, s98\n" \
"s_cbranch_scc1 L_sx8_%=\n" \
"L_rx8_%=:\n" \
"buffer_load_dwordx4 v[188:191], v20, s[56:59], s69 offen offset:1024 sc1\n" \
"s_andn2_b32 s75, 0x200, s98\n" \
"s_cbranch_scc1 L_sx9_%=\n" \
"L_rx9_%=:\n" \
"buffer_load_dwordx4 v[196:199], v20, s[56:59], s70 offen offset:1024 sc1\n" \
"s_andn2_b32 s75, 0x400, s98\n" \
"s_cbranch_scc1 L_sx10_%=\n" \
"L_rx10_%=:\n" \
"buffer_load_dwordx4 v[204:207], v20, s[56:59], s71 offen offset:1024 sc1\n" \
"s_waitcnt vmcnt(21)\n" \
"v_mfma_f32_16x16x32_bf16 v[168:171], v[50:53], v[116:119], v[168:171]\n" \
"v_mfma_f32_16x16x32_bf16 v[172:175], v[48:51], v[116:119], v[172:175]\n" \
"s_waitcnt vmcnt(20)\n" \
"v_mfma_f32_16x16x32_bf16 v[168:171], v[50:53], v[124:127], v[168:171]\n" \
"v_mfma_f32_16x16x32_bf16 v[172:175], v[48:51], v[124:127], v[172:175]\n" \
"s_waitcnt vmcnt(19)\n" \
"v_mfma_f32_16x16x32_bf16 v[168:171], v[50:53], v[132:135], v[168:171]\n" \
"v_mfma_f32_16x16x32_bf16 v[172:175], v[48:51], v[132:135], v[172:175]\n" \
"s_waitcnt vmcnt(18)\n" \
"v_mfma_f32_16x16x32_bf16 v[56:59], v[50:53], v[136:139], v[56:59]\n" \
"v_mfma_f32_16x16x32_bf16 v[60:63], v[48:51], v[136:139], v[60:63]\n" \
"s_waitcnt vmcnt(17)\n" \
"v_mfma_f32_16x16x32_bf16 v[56:59], v[50:53], v[208:211], v[56:59]\n" \
"v_mfma_f32_16x16x32_bf16 v[60:63], v[48:51], v[208:211], v[60:63]\n" \
"s_waitcnt vmcnt(16)\n" \
"v_mfma_f32_16x16x32_bf16 v[56:59], v[50:53], v[144:147], v[56:59]\n" \
"v_mfma_f32_16x16x32_bf16 v[60:63], v[48:51], v[144:147], v[60:63]\n" \
"s_waitcnt vmcnt(15)\n" \
"v_mfma_f32_16x16x32_bf16 v[56:59], v[50:53], v[216:219], v[56:59]\n" \
"v_mfma_f32_16x16x32_bf16 v[60:63], v[48:51], v[216:219], v[60:63]\n" \
"s_waitcnt vmcnt(14)\n" \
"v_mfma_f32_16x16x32_bf16 v[56:59], v[50:53], v[152:155], v[56:59]\n" \
"v_mfma_f32_16x16x32_bf16 v[60:63], v[48:51], v[152:155], v[60:63]\n" \
"s_waitcnt vmcnt(13)\n" \
"v_mfma_f32_16x16x32_bf16 v[56:59], v[50:53], v[224:227], v[56:59]\n" \
"v_mfma_f32_16x16x32_bf16 v[60:63], v[48:51], v[224:227], v[60:63]\n" \
"s_waitcnt vmcnt(12)\n" \
"v_mfma_f32_16x16x32_bf16 v[56:59], v[50:53], v[176:179], v[56:59]\n" \
"v_mfma_f32_16x16x32_bf16 v[60:63], v[48:51], v[176:179], v[60:63]\n" \
"s_waitcnt vmcnt(11)\n" \
"v_mfma_f32_16x16x32_bf16 v[56:59], v[50:53], v[248:251], v[56:59]\n" \
"v_mfma_f32_16x16x32_bf16 v[60:63], v[48:51], v[248:251], v[60:63]\n" \
"s_waitcnt vmcnt(10)\n" \
"v_mfma_f32_16x16x32_bf16 v[168:171], v[50:53], v[140:143], v[168:171]\n" \
"v_mfma_f32_16x16x32_bf16 v[172:175], v[48:51], v[140:143], v[172:175]\n" \
"s_waitcnt vmcnt(9)\n" \
"v_mfma_f32_16x16x32_bf16 v[240:243], v[50:53], v[212:215], v[240:243]\n" \
"v_mfma_f32_16x16x32_bf16 v[244:247], v[48:51], v[212:215], v[244:247]\n" \
"s_waitcnt vmcnt(8)\n" \
"v_mfma_f32_16x16x32_bf16 v[168:171], v[50:53], v[148:151], v[168:171]\n" \
"v_cvt_pk_bf16_f32 v56, v56, v57\n" \
"v_cvt_pk_bf16_f32 v60, v60, v61\n" \
"v_mfma_f32_16x16x32_bf16 v[172:175], v[48:51], v[148:151], v[172:175]\n" \
"v_cvt_pk_bf16_f32 v57, v58, v59\n" \
"s_waitcnt vmcnt(7)\n" \
"v_mfma_f32_16x16x32_bf16 v[240:243], v[50:53], v[220:223], v[240:243]\n" \
"buffer_store_dwordx2 v[56:57], v42, s[88:91], s73 offen sc0 sc1\n" \
"v_mfma_f32_16x16x32_bf16 v[244:247], v[48:51], v[220:223], v[244:247]\n" \
"v_cvt_pk_bf16_f32 v61, v62, v63\n" \
"s_waitcnt vmcnt(7)\n" \
"v_mfma_f32_16x16x32_bf16 v[168:171], v[50:53], v[156:159], v[168:171]\n" \
"buffer_store_dwordx2 v[60:61], v43, s[88:91], s73 offen sc0 sc1\n" \
"v_mfma_f32_16x16x32_bf16 v[172:175], v[48:51], v[156:159], v[172:175]\n" \
"s_waitcnt vmcnt(7)\n" \
"v_mfma_f32_16x16x32_bf16 v[240:243], v[50:53], v[228:231], v[240:243]\n" \
"v_mfma_f32_16x16x32_bf16 v[244:247], v[48:51], v[228:231], v[244:247]\n" \
"s_waitcnt vmcnt(6)\n" \
"v_mfma_f32_16x16x32_bf16 v[168:171], v[50:53], v[164:167], v[168:171]\n" \
"v_mfma_f32_16x16x32_bf16 v[172:175], v[48:51], v[164:167], v[172:175]\n" \
"s_waitcnt vmcnt(5)\n" \
"v_mfma_f32_16x16x32_bf16 v[240:243], v[50:53], v[236:239], v[240:243]\n" \
"v_mfma_f32_16x16x32_bf16 v[244:247], v[48:51], v[236:239], v[244:247]\n" \
"s_waitcnt vmcnt(4)\n" \
"v_mfma_f32_16x16x32_bf16 v[240:243], v[50:53], v[188:191], v[240:243]\n" \
"v_mfma_f32_16x16x32_bf16 v[244:247], v[48:51], v[188:191], v[244:247]\n" \
"s_waitcnt vmcnt(3)\n" \
"v_mfma_f32_16x16x32_bf16 v[240:243], v[50:53], v[196:199], v[240:243]\n" \
"v_cvt_pk_bf16_f32 v168, v168, v169\n" \
"v_cvt_pk_bf16_f32 v172, v172, v173\n" \
"v_mfma_f32_16x16x32_bf16 v[244:247], v[48:51], v[196:199], v[244:247]\n" \
"v_cvt_pk_bf16_f32 v169, v170, v171\n" \
"s_waitcnt vmcnt(2)\n" \
"v_mfma_f32_16x16x32_bf16 v[240:243], v[50:53], v[204:207], v[240:243]\n" \
"v_cvt_pk_bf16_f32 v173, v174, v175\n" \
"v_mfma_f32_16x16x32_bf16 v[244:247], v[48:51], v[204:207], v[244:247]\n" \
"s_mov_b64 exec, 1\n" \
"buffer_store_dword v46, off, s[56:59], s72 offset:0 sc1\n" \
"s_mov_b64 exec, -1\n" \
"buffer_store_dwordx2 v[168:169], v42, s[88:91], s92 offen sc0 sc1\n" \
"buffer_store_dwordx2 v[172:173], v43, s[88:91], s92 offen sc0 sc1\n" \
"s_nop 0\n" \
"v_cvt_pk_bf16_f32 v240, v240, v241\n" \
"v_cvt_pk_bf16_f32 v241, v242, v243\n" \
"v_cvt_pk_bf16_f32 v244, v244, v245\n" \
"v_cvt_pk_bf16_f32 v245, v246, v247\n" \
"buffer_store_dwordx2 v[240:241], v42, s[88:91], s93 offen sc0 sc1\n" \
"buffer_store_dwordx2 v[244:245], v43, s[88:91], s93 offen sc0 sc1\n"
#define ATT_VCLOB "v21","v22","v23","v32","v33","v34","v35","v36","v37","v38","v39","v40","v41","v42","v43","v44","v45","v46","v47","v48","v49","v50","v51","v52","v53","v54","v55","v56","v57","v58","v59","v60","v61","v62","v63","v64","v65","v66","v67","v68","v69","v70","v71","v72","v73","v74","v75","v76","v77","v78","v79","v80","v81","v82","v83","v84","v85","v86","v87","v88","v89","v90","v91","v92","v93","v94","v95","v96","v97","v98","v99","v100","v101","v102","v103","v104","v105","v106","v107","v108","v109","v110","v111","v112","v113","v114","v115","v116","v117","v118","v119","v120","v121","v122","v123","v124","v125","v126","v127","v128","v129","v130","v131","v132","v133","v134","v135","v136","v137","v138","v139","v140","v141","v142","v143","v144","v145","v146","v147","v148","v149","v150","v151","v152","v153","v154","v155","v156","v157","v158","v159","v160","v161","v162","v163","v164","v165","v166","v167","v168","v169","v170","v171","v172","v173","v174","v175","v176","v177","v178","v179","v180","v181","v182","v183","v184","v185","v186","v187","v188","v189","v190","v191","v192","v193","v194","v195","v196","v197","v198","v199","v200","v201","v202","v203","v204","v205","v206","v207","v208","v209","v210","v211","v212","v213","v214","v215","v216","v217","v218","v219","v220","v221","v222","v223","v224","v225","v226","v227","v228","v229","v230","v231","v232","v233","v234","v235","v236","v237","v238","v239","v240","v241","v242","v243","v244","v245","v246","v247","v248","v249","v250","v251","v252","v253","v254","v255"
#define ATT_SCLOB "s63","s64","s65","s66","s67","s68","s69","s70","s71","s72","s73","s74","s75","s76","s77","s78","s79","s80","s81","s82","s83","s84","s85","s86","s87","s88","s89","s90","s91","s92","s93","s94","s95","s96","s97","s98","s99","s100","s101"
enum { T_fo, T_fa, T_fas, T_fb, T_fbs, T_f8, T_mk8, T_wol, T_woh, T_wbl, T_wbh, T_outl, T_outh, T_r1l, T_r1h, T_r1n, T_r1s, T_m0, T_m1, T_mw, T_m4, T_ml, T_w0, T_w1, T_w2, T_w3, T_A1, T_A2, T_A3, T_B1, T_B2, T_B3, T_Ao, T_Bo, T_Al, T_Bl, T_l8, T_xr, T_x8r, T_oA, T_oB, T_o8, T_x0, T_x1, T_x2, T_x3, T_x4, T_x5, T_x6, T_x8, T_ep, T_xf, T_kf };

__device__ __forceinline__ void out_consts(u32x4 (&vp)[3], const u16* wo, const u16* wob, const u16* out, const unsigned b, const unsigned w, const unsigned lane) {
    const unsigned sp = b & 7u, nt = (b >> 3) & 3u, j = b >> 5, head = 4u * sp + nt, OOR = 0x40000000u;
    unsigned vw[9];
#pragma unroll
    for (unsigned i = 0; i < 9u; ++i) {
        const unsigned q = 64u * i + lane, row = __umul24(q, 3641u) >> 16, c = q - __umul24(row, 18u);
        vw[i] = __umul24(row, DM * 2) + (c < 16u ? (c * 16u + sp * 32u + 32u) & 255u : c * 16u) - (i & 3u) * 1024u;
    }
    const unsigned row = 16u * w + (lane >> 2), r9 = __umul24(16u * (row >> 5) + (row & 12u) + 2u * ((row >> 4) & 1u) + ((row >> 1) & 1u), 18u) + 16u + (row & 1u);
    unsigned vwl = __umul24(r9, DM * 2) + ((lane & 2u) ? 256u : sp * 32u) + (lane & 1u) * 16u, vrd = __umul24(lane >> 2, 288u) + (lane & 3u) * 8u;
    asm volatile("" : "+v"(vw[0]), "+v"(vw[1]), "+v"(vw[2]), "+v"(vw[3]), "+v"(vw[4]), "+v"(vw[5]), "+v"(vw[6]), "+v"(vw[7]), "+v"(vw[8]), "+v"(vwl), "+v"(vrd));
    unsigned tab = 0;
#define TB(n, v) asm("v_writelane_b32 %0, %1, %2" : "+v"(tab) : "s"((unsigned)(v)), "n"((int)T_##n))
    const unsigned FJ = (unsigned)WS_F + (F_J + 640u * j) * 4u, mt = 2u * (w & 2u) + (w & 1u), w2 = w < 2u ? w : 2u, hw = head * 147456u;
    const unsigned FR = (unsigned)WS_F + F_R * 4u + (8u * j + sp) * 1024u;
    TB(fo, FJ + 4u * head); TB(fa, FR + (w | 2u) * 256u); TB(fas, FR + w * 256u + nt * 4u);
    TB(fb, FR + 128u + w * 256u); TB(fbs, FR + 128u + w * 256u + nt * 4u);
    TB(ep, b + w ? OOR : (unsigned)WS_F + F_EPOCH * 4u);
    const u16* pwo = wo + nt * 144u; const u16* pwb = wob + nt * 144u; const u16* pr1 = w < 3u ? pwo : pwb;
    TB(wol, PLO(pwo)); TB(woh, PHI(pwo) & 0xffffu); TB(wbl, PLO(pwb)); TB(wbh, PHI(pwb) & 0xffffu); TB(outl, PLO(out)); TB(outh, PHI(out) & 0xffffu); TB(r1l, PLO(pr1)); TB(r1h, PHI(pr1) & 0xffffu);
    TB(r1n, (w < 3u ? 72u * sp + 71u : 4095u) * 1152u + 288u); TB(r1s, w < 3u ? (72u * sp + 32u * w) * 1152u : hw + 110592u);
    TB(m0, 131072u + 9216u * w2); TB(w0, hw + 36864u * w2); TB(m1, w < 3u ? 9216u * w : 73728u); TB(mw, 9216u * w); TB(m4, w < 3u ? 131072u + 9216u * w : 64512u); TB(ml, 119808u + 1024u * w);
    const unsigned tA = (unsigned)WS_AF + (8u * j + mt) * (KS_TOT * 1024u), tB = (unsigned)WS_AF + (120u - 8u * j + mt) * (KS_TOT * 1024u);
#define HK(k) (4u * sp + ((nt + k) & 3u))
    TB(w1, HK(1u) * 147456u + w * 36864u); TB(w2, HK(2u) * 147456u + w * 36864u); TB(w3, HK(3u) * 147456u + w * 36864u);
    TB(A1, tA + 18432u + HK(1u) * 4096u); TB(A2, tA + 18432u + HK(2u) * 4096u); TB(A3, tA + 18432u + HK(3u) * 4096u);
    TB(B1, tB + 18432u + HK(1u) * 4096u); TB(B2, tB + 18432u + HK(2u) * 4096u); TB(B3, tB + 18432u + HK(3u) * 4096u);
    TB(Ao, tA + sp * 2048u); TB(Bo, tB + sp * 2048u); TB(Al, tA + 16384u); TB(Bl, tB + 16384u);
    const bool red = w == (sp >> 1), blk = sp & 1u;
    const unsigned xg = (unsigned)WS_X + (4u * j + nt) * 589824u + w * 16384u, oA = (128u * j + 16u * mt) * 1152u + nt * 288u, oB = (1920u - 128u * j + 16u * mt) * 1152u + nt * 288u;
    TB(l8, red ? (blk ? tB : tA) + 16384u : OOR); TB(xr, xg + sp * 65536u); TB(x8r, red ? xg + 524288u + (sp & 1u) * 1024u : OOR);
    TB(oA, oA + sp * 32u); TB(oB, oB + sp * 32u); TB(o8, red ? (blk ? oB : oA) + 256u : OOR); TB(x8, xg + 524288u + sp * 2048u);
    TB(f8, xg + w * 131072u + 14400u + sp * 4u); TB(mk8, red ? 0xff0000u : 0u); TB(xf, xg + sp * 65536u + 14336u);
    TB(kf, (unsigned)WS_F + (F_KV + head * 64u) * 4u);
#define XI(i) TB(x##i, xg + ((sp + 1u + i) & 7u) * 65536u + i * 2048u)
    XI(0); XI(1); XI(2); XI(3); XI(4); XI(5); XI(6);
#undef XI
#undef HK
#undef TB
    vp[0] = (u32x4){vw[0], vw[1], vw[2], vw[3]}; vp[1] = (u32x4){vw[4], vw[5], vw[6], vw[7]}; vp[2] = (u32x4){vw[8], vwl, vrd, tab};
}
__global__ __launch_bounds__(256, 1) __attribute__((aligned(4096))) void fused_kernel(const u16* __restrict__ x, const u16* __restrict__ ckv, const u16* __restrict__ wq, const u16* __restrict__ wuk,
                                                       const u16* __restrict__ wuv, const u16* __restrict__ wqkv, const u16* __restrict__ gw,
                                                       const u16* __restrict__ wo, const u16* __restrict__ wob, char* __restrict__ ws, u16* __restrict__ out) {
    using namespace att;
    extern __shared__ __attribute__((aligned(16))) char lds[];
    unsigned wid = rfl(threadIdx.x >> 6);
    code_touch((blockIdx.x >> 3) * 4u + wid, (unsigned)(size_t)lds + 151552u + wid * 512u);
    SBAR();
    asm volatile("" :: "s"(x), "s"(ckv), "s"(wq), "s"(wuk), "s"(wuv), "s"(wqkv), "s"(gw), "s"(wo), "s"(wob), "s"(ws), "s"(out));
    unsigned tag;
    int b = blockIdx.x;
    u32x4 vp[5], sa[5]; unsigned sd[2];
    const unsigned vl = proj_phase(x, ckv, wq, wuk, wuv, wqkv, gw, wo, wob, out, ws, lds, wid, tag, vp, sa, sd);
    asm volatile("" : "+s"(ws));
    unsigned* const flags = (unsigned*)(ws + WS_F);
    RTS(2);
#define AST(L) do {} while (0)
#define SD(J8, H, W) ((unsigned)WS_AF + (((J8) + 2u * ((W) & 2u) + ((W) & 1u)) * KS_TOT + 18u + 4u * (H)) * 1024u)
#define SRDA (u32x4){PLO(ws), PHI(ws) & 0xffffu, (unsigned)WS_L, 0x00020000u}
#define ATT_TAIL "s_endpgm\n"
    asm volatile(ATT_ASM ATT_TAIL "L_end_%=:\n s_nop 1"
                 :
                 : "{v[0:3]}"(vp[0]), "{v[4:7]}"(vp[1]), "{v[8:11]}"(vp[2]), "{v[12:15]}"(vp[3]), "{v[16:19]}"(vp[4]), "{v20}"(vl), "{s[56:59]}"(sa[4]), "{s60}"(sd[0]), "{s61}"(sd[1]), "{s62}"(tag),
                   "{s[8:11]}"(sa[0]), "{s[12:15]}"(sa[1]), "{s[16:19]}"(sa[2]), "{s[20:23]}"(sa[3])
                 : "memory", "m0", "vcc", "scc", ATT_VCLOB, ATT_SCLOB, ACLOB_LO, ACLOB_HI);
    {
        const u16* const __attribute__((address_space(4)))* ka = (const u16* const __attribute__((address_space(4)))*)__builtin_amdgcn_kernarg_segment_ptr();
        unsigned b2 = b, w2 = wid;
        asm volatile("s_waitcnt vmcnt(0)" : "+s"(ka), "+s"(b2), "+s"(w2), "+s"(ws) :: "memory");
        const unsigned j2 = b2 >> 5, p2 = w2 & 1u, h2 = (b2 & 7u) * 4u + ((b2 >> 3) & 3u);
#pragma unroll 1
        for (unsigned i = w2 & 2u; i < 2u + 3u * (w2 & 2u); ++i)
            att_exact(ka[0], ka[2], ws, h2, (i < 4u ? 1920u - 128u * j2 : 128u * j2 - 128u) + 32u * i + 16u * p2, *reinterpret_cast<const float*>(lds + LDS_G + (32u * i + 16u * p2 + (lane_id() & 15u)) * 4u));
        asm volatile("s_waitcnt vmcnt(0)" ::: "memory");
        unsigned* fa = (unsigned*)(ws + WS_F) + F_R + (8u * j2 + (b2 & 7u)) * 256u + (2u + p2) * 64u + ((b2 >> 3) & 3u);
        if (w2 & 2u) st_flag(fa, tag);
        wait_flags(fa, ld_flag(fa), tag);
        const unsigned l2 = lane_id(); u32x4 c[3]; out_consts(c, ka[7], ka[8], ka[10], b2, w2, l2);
        asm volatile("s_waitcnt vmcnt(0) lgkmcnt(0)\n\ts_nop 7\n\ts_branch L_att_stub" :: "{v[0:3]}"(c[0]), "{v[4:7]}"(c[1]), "{v[8:11]}"(c[2]), "{v20}"(l2 * 16u), "{s[56:59]}"(SRDA), "{s60}"(SD(8u * j2, h2, w2)),
                     "{s61}"(SD(120u - 8u * j2, h2, w2)), "{s62}"(tag) : "memory");
    }
    RTS(5);
}

extern "C" void kernel_launch(void* const* d_in, const int* in_sizes, int n_in, void* d_out, int out_size, void* d_ws, size_t ws_size,
                              hipStream_t stream) {
    (void)hipFuncSetAttribute((const void*)fused_kernel, hipFuncAttributeMaxDynamicSharedMemorySize, att::LDS_BYTES);
    fused_kernel<<<dim3(256), 256, att::LDS_BYTES, stream>>>((const u16*)d_in[0], (const u16*)d_in[4], (const u16*)d_in[5], (const u16*)d_in[6], (const u16*)d_in[7], (const u16*)d_in[1],
                                                             (const u16*)d_in[9], (const u16*)d_in[2], (const u16*)d_in[8], (char*)d_ws, (u16*)d_out);
}
```

```cpp
#include <hip/hip_runtime.h>
#include <hip/hip_bf16.h>
#include <stdint.h>
#include <utility>

typedef short bf16x8 __attribute__((ext_vector_type(8)));
typedef short s16x4 __attribute__((ext_vector_type(4)));
typedef float f32x16 __attribute__((ext_vector_type(16)));
typedef float f32x4 __attribute__((ext_vector_type(4)));
typedef unsigned u32x4 __attribute__((ext_vector_type(4)));
typedef unsigned short u16;

constexpr int S = 2048, DM = 576, NH = 32, HD = 128, DC = 512, NQKV = 1728, DHA = 18;
constexpr int NBIG = NH * HD;
constexpr int KS_TOT = 146;

#define SBAR() __builtin_amdgcn_sched_barrier(0)
constexpr size_t WS_Q = 0, WS_K = WS_Q + (size_t)S * NBIG * 2, WS_V = WS_K + (size_t)S * NBIG * 2, WS_AF = WS_V + (size_t)S * NBIG * 2, WS_X = WS_AF + (size_t)128 * KS_TOT * 1024,
                 WS_P = WS_X + (size_t)64 * 16 * 9216, WS_F = WS_P + (size_t)128 * (4 * 32 * 256 + 1024) * 4;
constexpr size_t WS_L = (size_t)96 << 20;
static_assert(WS_F == 96206848, "workspace map is baked into the generated asm");
constexpr int F_EPOCH = 0, F_J = 1024, F_ST = 8192, F_KV = 40960, F_R = 65536;
constexpr unsigned TAG_STEP = 0x9E3779B1u;
__device__ __forceinline__ unsigned ld_flag(const unsigned* p) { return __hip_atomic_load(p, __ATOMIC_RELAXED, __HIP_MEMORY_SCOPE_AGENT); }
__device__ __forceinline__ void st_flag(unsigned* p, unsigned v) { __hip_atomic_store(p, v, __ATOMIC_RELAXED, __HIP_MEMORY_SCOPE_AGENT); }
__device__ __forceinline__ unsigned ld_flag_async(const unsigned* p) { unsigned v; asm volatile("global_load_dword %0, %1, off sc1" : "=v"(v) : "v"(p) : "memory"); return v; }
#define WAITV(v, N) asm volatile("s_waitcnt vmcnt(" #N ")" : "+v"(v) :: "memory")
__device__ __forceinline__ void wait_flags(const unsigned* p, unsigned v, unsigned tag) {
    if (__builtin_expect(__builtin_amdgcn_ballot_w64(v != tag) != 0ull, 0)) {
        unsigned spins = 0;
        do { __builtin_amdgcn_s_sleep(1); } while (__builtin_amdgcn_ballot_w64(ld_flag(p) != tag) != 0ull && ++spins < (1u << 22));
    }
    asm volatile("" ::: "memory");
}

__device__ __forceinline__ unsigned cvtpk(float lo, float hi) {
    unsigned r; asm volatile("v_cvt_pk_bf16_f32 %0, %1, %2" : "=v"(r) : "v"(lo), "v"(hi)); return r;
}
__device__ __forceinline__ float bf2f(u16 v) { return __uint_as_float(((unsigned)v) << 16); }
__device__ __forceinline__ int crow(int r, int hi) { return (r & 3) + 8 * (r >> 2) + 4 * hi; }

typedef __attribute__((address_space(3))) s16x4* lds_s16x4_p;
__device__ __forceinline__ s16x4 trrd(unsigned addr) { return __builtin_amdgcn_ds_read_tr16_b64_v4i16((lds_s16x4_p)addr); }

__device__ __forceinline__ unsigned lane_id() { unsigned l; asm volatile("v_mbcnt_lo_u32_b32 %0, -1, 0\n\tv_mbcnt_hi_u32_b32 %0, -1, %0" : "=v"(l)); return l; }
#define RTS(slot) do {} while (0)
#define CT_L 5u
__device__ __forceinline__ void code_touch(unsigned wi, unsigned lds_scratch) {
    unsigned long long pc; asm volatile("s_getpc_b64 %0" : "=s"(pc));
    asm volatile("s_mov_b32 m0, %0\n\ts_mov_b64 exec, %3\n\tglobal_load_lds_dword %1, %2\n\ts_mov_b64 exec, -1" :: "s"(lds_scratch), "v"(((threadIdx.x & 63u) * 128u + wi) * 128u), "s"(pc & ~127ull), "n"((1u << CT_L) - 1u) : "m0", "memory");
}
#define ACLOB_LO "a0","a1","a2","a3","a4","a5","a6","a7","a8","a9","a10","a11","a12","a13","a14","a15","a16","a17","a18","a19","a20","a21","a22","a23","a24","a25","a26","a27","a28","a29","a30","a31","a32","a33","a34","a35","a36","a37","a38","a39","a40","a41","a42","a43","a44","a45","a46","a47","a48","a49","a50","a51","a52","a53","a54","a55","a56","a57","a58","a59","a60","a61","a62","a63","a64","a65","a66","a67","a68","a69","a70","a71","a72","a73","a74","a75","a76","a77","a78","a79","a80","a81","a82","a83","a84","a85","a86","a87","a88","a89","a90","a91","a92","a93","a94","a95","a96","a97","a98","a99","a100","a101","a102","a103","a104","a105","a106","a107","a108","a109","a110","a111","a112","a113","a114","a115","a116","a117","a118","a119","a120","a121","a122","a123","a124","a125","a126","a127"
#define ACLOB_HI "a128","a129","a130","a131","a132","a133","a134","a135","a136","a137","a138","a139","a140","a141","a142","a143","a144","a145","a146","a147","a148","a149","a150","a151","a152","a153","a154","a155","a156","a157","a158","a159","a160","a161","a162","a163","a164","a165","a166","a167","a168","a169","a170","a171","a172","a173","a174","a175","a176","a177","a178","a179","a180","a181","a182","a183","a184","a185","a186","a187","a188","a189","a190","a191","a192","a193","a194","a195","a196","a197","a198","a199","a200","a201","a202","a203","a204","a205","a206","a207","a208","a209","a210","a211","a212","a213","a214","a215","a216","a217","a218","a219","a220","a221","a222","a223","a224","a225","a226","a227","a228","a229","a230","a231","a232","a233","a234","a235","a236","a237","a238","a239","a240","a241","a242","a243","a244","a245","a246","a247","a248","a249","a250","a251","a252","a253","a254","a255"
#define PROJ_PRE0 \
"s_add_u32 s62, %[rc], 0x0\n" \
"s_and_b32 s62, s62, 0x3ff\n" \
"s_lshl_b32 s63, s62, 12\n" \
"s_add_u32 m0, %[mA], 0x0\n" \
"s_add_u32 s85, s62, 0x0\n" \
"buffer_load_dwordx4 v22, s[64:67], s85 offen lds\n" \
"s_add_u32 m0, %[mA], 0x400\n" \
"s_add_u32 s84, s62, 0x2000\n" \
"buffer_load_dwordx4 v23, s[64:67], s84 offen lds\n" \
"s_add_u32 m0, %[mB], 0x0\n" \
"s_add_u32 s85, s63, 0x0\n" \
"buffer_load_dwordx4 v26, s[68:71], s85 offen lds\n" \
"s_add_u32 m0, %[mB], 0x4000\n" \
"s_add_u32 s84, s63, 0x0\n" \
"buffer_load_dwordx4 v26, s[72:75], s84 offen lds\n" \
"s_add_u32 m0, %[mA], 0x800\n" \
"s_add_u32 s85, s62, 0x4000\n" \
"buffer_load_dwordx4 v22, s[64:67], s85 offen lds\n" \
"s_add_u32 m0, %[mA], 0xc00\n" \
"s_add_u32 s84, s62, 0x6000\n" \
"buffer_load_dwordx4 v23, s[64:67], s84 offen lds\n" \
"s_add_u32 m0, %[mB], 0x400\n" \
"s_add_u32 s85, s63, 0x8000\n" \
"buffer_load_dwordx4 v26, s[68:71], s85 offen lds\n" \
"s_add_u32 m0, %[mB], 0x4400\n" \
"s_add_u32 s84, s63, 0x8000\n" \
"buffer_load_dwordx4 v26, s[72:75], s84 offen lds\n"
#define PROJ_PRE1 \
"s_add_u32 s62, %[rc], 0x0\n" \
"s_and_b32 s62, s62, 0x3ff\n" \
"s_lshl_b32 s63, s62, 12\n" \
"s_add_u32 m0, %[mA], 0x1000\n" \
"s_add_u32 s85, s62, 0x8000\n" \
"buffer_load_dwordx4 v22, s[64:67], s85 offen lds\n" \
"s_add_u32 m0, %[mA], 0x1400\n" \
"s_add_u32 s84, s62, 0xa000\n" \
"buffer_load_dwordx4 v23, s[64:67], s84 offen lds\n" \
"s_add_u32 m0, %[mB], 0x800\n" \
"s_add_u32 s85, s63, 0x10000\n" \
"buffer_load_dwordx4 v27, s[68:71], s85 offen lds\n" \
"s_add_u32 m0, %[mB], 0x4800\n" \
"s_add_u32 s84, s63, 0x10000\n" \
"buffer_load_dwordx4 v27, s[72:75], s84 offen lds\n" \
"s_add_u32 m0, %[mA], 0x1800\n" \
"s_add_u32 s85, s62, 0xc000\n" \
"buffer_load_dwordx4 v22, s[64:67], s85 offen lds\n" \
"s_add_u32 m0, %[mA], 0x1c00\n" \
"s_add_u32 s84, s62, 0xe000\n" \
"buffer_load_dwordx4 v23, s[64:67], s84 offen lds\n" \
"s_add_u32 m0, %[mB], 0xc00\n" \
"s_add_u32 s85, s63, 0x18000\n" \
"buffer_load_dwordx4 v27, s[68:71], s85 offen lds\n" \
"s_add_u32 m0, %[mB], 0x4c00\n" \
"s_add_u32 s84, s63, 0x18000\n" \
"buffer_load_dwordx4 v27, s[72:75], s84 offen lds\n"
#define PROJ_PRE2 \
"s_add_u32 s62, %[rc], 0x80\n" \
"s_and_b32 s62, s62, 0x3ff\n" \
"s_lshl_b32 s63, s62, 12\n" \
"s_add_u32 m0, %[mA], 0x8000\n" \
"s_add_u32 s85, s62, 0x0\n" \
"buffer_load_dwordx4 v22, s[64:67], s85 offen lds\n" \
"s_add_u32 m0, %[mA], 0x8400\n" \
"s_add_u32 s84, s62, 0x2000\n" \
"buffer_load_dwordx4 v23, s[64:67], s84 offen lds\n"
#define PROJ_PRE3 \
"s_add_u32 s62, %[rc], 0x80\n" \
"s_and_b32 s62, s62, 0x3ff\n" \
"s_lshl_b32 s63, s62, 12\n" \
"s_add_u32 m0, %[mB], 0x8000\n" \
"s_add_u32 s85, s63, 0x0\n" \
"buffer_load_dwordx4 v26, s[68:71], s85 offen lds\n" \
"s_add_u32 m0, %[mB], 0xc000\n" \
"s_add_u32 s84, s63, 0x0\n" \
"buffer_load_dwordx4 v26, s[72:75], s84 offen lds\n" \
"s_add_u32 m0, %[mA], 0x8800\n" \
"s_add_u32 s85, s62, 0x4000\n" \
"buffer_load_dwordx4 v22, s[64:67], s85 offen lds\n" \
"s_add_u32 m0, %[mA], 0x8c00\n" \
"s_add_u32 s84, s62, 0x6000\n" \
"buffer_load_dwordx4 v23, s[64:67], s84 offen lds\n" \
"s_add_u32 m0, %[mB], 0x8400\n" \
"s_add_u32 s85, s63, 0x8000\n" \
"buffer_load_dwordx4 v26, s[68:71], s85 offen lds\n" \
"s_add_u32 m0, %[mB], 0xc400\n" \
"s_add_u32 s84, s63, 0x8000\n" \
"buffer_load_dwordx4 v26, s[72:75], s84 offen lds\n" \
"s_add_u32 m0, %[mA], 0x9000\n" \
"s_add_u32 s85, s62, 0x8000\n" \
"buffer_load_dwordx4 v22, s[64:67], s85 offen lds\n" \
"s_add_u32 m0, %[mA], 0x9400\n" \
"s_add_u32 s84, s62, 0xa000\n" \
"buffer_load_dwordx4 v23, s[64:67], s84 offen lds\n"
#define PROJ_TKE 0
#define PROJ_T0 0
#define PROJ_ROT 2*(sp+mt)
#define PROJ_TC 0
#define PROJ_ASM \
".p2align 3\n" \
"s_branch L_go_%=\n" \
"L_pl_%=:\n" \
"s_mov_b32 s63, 0\n" \
"L_ql_%=:\n" \
"s_sleep 1\n" \
"v_bfe_u32 v44, v20, 2, 5\n" \
"buffer_load_dword v44, v44, s[80:83], s39 offen offset:64 sc1\n" \
"s_waitcnt vmcnt(0)\n" \
"v_cmp_ne_u32_e32 vcc, %[tag], v44\n" \
"s_add_u32 s63, s63, 1\n" \
"s_nop 3\n" \
"s_cbranch_vccz L_rl_%=\n" \
"s_cmp_gt_u32 s63, 0x400000\n" \
"s_cbranch_scc0 L_ql_%=\n" \
"s_branch L_rl_%=\n" \
"L_pa_%=:\n" \
"s_mov_b32 s63, 0\n" \
"L_qa_%=:\n" \
"s_sleep 1\n" \
"v_bfe_u32 v44, v20, 2, 5\n" \
"buffer_load_dword v44, v44, s[80:83], s39 offen offset:0 sc1\n" \
"s_waitcnt vmcnt(0)\n" \
"v_cmp_ne_u32_e32 vcc, %[tag], v44\n" \
"s_add_u32 s63, s63, 1\n" \
"s_nop 3\n" \
"s_cbranch_vccz L_ra_%=\n" \
"s_cmp_gt_u32 s63, 0x400000\n" \
"s_cbranch_scc0 L_qa_%=\n" \
"s_branch L_ra_%=\n" \
"L_go_%=:\n" \
"v_xor_b32_e32 v33, 0x20, v32\n" \
"s_mov_b32 s80, %[wsl]\n" \
"s_and_b32 s81, %[wsh], 0xffff\n" \
"s_mov_b32 s82, 0x10000000\n" \
"s_mov_b32 s83, 0x20000\n" \
"s_mov_b32 s84, 0x5bc0000\n" \
"s_add_u32 s62, s22, 0x80\n" \
"buffer_load_dword v180, off, s[80:83], s84 sc1\n" \
"s_and_b32 s62, s62, 0x3ff\n" \
"s_lshl_b32 s63, s62, 12\n" \
"v_xor_b32_e64 v34, 0x40, v32\n" \
"v_xor_b32_e32 v35, 0x60, v32\n" \
"s_waitcnt vmcnt(11)\n" \
"s_barrier\n" \
"ds_read_b64_tr_b16 v[120:121], v32 offset:8192\n" \
"ds_read_b64_tr_b16 v[122:123], v32 offset:9216\n" \
"ds_read_b128 v[56:59], v29 offset:16384\n" \
"ds_read_b64_tr_b16 v[116:117], v32 offset:0\n" \
"ds_read_b64_tr_b16 v[118:119], v32 offset:1024\n" \
"ds_read_b128 v[52:55], v28 offset:16384\n" \
"s_add_u32 m0, %[mB], 0x8800\n" \
"s_add_u32 s85, s63, 0x10000\n" \
"s_mov_b32 s76, s12\n" \
"ds_read_b128 v[60:63], v28 offset:18432\n" \
"s_and_b32 s77, s13, 0xffff\n" \
"s_mov_b32 s78, 0x1e6000\n" \
"s_mov_b32 s79, 0x20000\n" \
"ds_read_b128 v[64:67], v29 offset:18432\n" \
"s_waitcnt lgkmcnt(5)\n" \
"v_mfma_f32_16x16x32_bf16 a[0:3], v[120:123], v[56:59], 0\n" \
"ds_read_b128 v[72:75], v29 offset:20480\n" \
"ds_read_b128 v[68:71], v28 offset:20480\n" \
"ds_read_b128 v[76:79], v28 offset:22528\n" \
"buffer_load_dwordx4 v27, s[68:71], s85 offen lds\n" \
"ds_read_b128 v[80:83], v29 offset:22528\n" \
"s_waitcnt lgkmcnt(6)\n" \
"v_mfma_f32_16x16x32_bf16 a[0:3], v[116:119], v[52:55], a[0:3]\n" \
"ds_read_b64_tr_b16 v[128:129], v33 offset:8192\n" \
"s_waitcnt lgkmcnt(6)\n" \
"v_mfma_f32_16x16x32_bf16 a[32:35], v[116:119], v[60:63], 0\n" \
"ds_read_b64_tr_b16 v[130:131], v33 offset:9216\n" \
"ds_read_b64_tr_b16 v[124:125], v33 offset:0\n" \
"ds_read_b64_tr_b16 v[126:127], v33 offset:1024\n" \
"s_waitcnt lgkmcnt(8)\n" \
"s_nop 0\n" \
"v_mfma_f32_16x16x32_bf16 a[32:35], v[120:123], v[64:67], a[32:35]\n" \
"ds_read_b64_tr_b16 v[136:137], v34 offset:8192\n" \
"ds_read_b64_tr_b16 v[138:139], v34 offset:9216\n" \
"ds_read_b64_tr_b16 v[132:133], v34 offset:0\n" \
"s_add_u32 m0, %[mB], 0xc800\n" \
"ds_read_b64_tr_b16 v[134:135], v34 offset:1024\n" \
"s_add_u32 s84, s63, 0x10000\n" \
"buffer_load_dwordx4 v27, s[72:75], s84 offen lds\n" \
"ds_read_b64_tr_b16 v[144:145], v35 offset:8192\n" \
"ds_read_b64_tr_b16 v[146:147], v35 offset:9216\n" \
"ds_read_b64_tr_b16 v[140:141], v35 offset:0\n" \
"s_lshr_b32 s95, %[b], 3\n" \
"ds_read_b64_tr_b16 v[142:143], v35 offset:1024\n" \
"s_lshl_b32 s96, s95, 2\n" \
"ds_read_b64_tr_b16 v[152:153], v32 offset:24576\n" \
"s_add_u32 s97, s96, %[w]\n" \
"ds_read_b64_tr_b16 v[154:155], v32 offset:25600\n" \
"v_lshrrev_b32_e32 v212, 4, v20\n" \
"s_mul_i32 s98, 27, s97\n" \
"v_min_u32_e32 v213, 26, v212\n" \
"v_add_u32_e64 v214, s98, v213\n" \
"v_mul_u32_u24_e32 v215, 0xaaab, v214\n" \
"s_waitcnt lgkmcnt(15)\n" \
"v_mfma_f32_16x16x32_bf16 a[64:67], v[120:123], v[72:75], 0\n" \
"ds_read_b64_tr_b16 v[148:149], v32 offset:16384\n" \
"ds_read_b64_tr_b16 v[150:151], v32 offset:17408\n" \
"v_mfma_f32_16x16x32_bf16 a[64:67], v[116:119], v[68:71], a[64:67]\n" \
"s_add_u32 m0, %[mA], 0x9800\n" \
"s_add_u32 s85, s62, 0xc000\n" \
"v_mfma_f32_16x16x32_bf16 a[96:99], v[116:119], v[76:79], 0\n" \
"ds_read_b64_tr_b16 v[160:161], v33 offset:24576\n" \
"s_waitcnt lgkmcnt(15)\n" \
"v_mfma_f32_16x16x32_bf16 a[96:99], v[120:123], v[80:83], a[96:99]\n" \
"buffer_load_dwordx4 v22, s[64:67], s85 offen lds\n" \
"v_mfma_f32_16x16x32_bf16 a[4:7], v[128:131], v[56:59], 0\n" \
"s_add_u32 m0, %[mA], 0x9c00\n" \
"s_waitcnt lgkmcnt(13)\n" \
"s_nop 0\n" \
"v_mfma_f32_16x16x32_bf16 a[4:7], v[124:127], v[52:55], a[4:7]\n" \
"s_add_u32 s84, s62, 0xe000\n" \
"ds_read_b64_tr_b16 v[162:163], v33 offset:25600\n" \
"v_mfma_f32_16x16x32_bf16 a[36:39], v[124:127], v[60:63], 0\n" \
"buffer_load_dwordx4 v23, s[64:67], s84 offen lds\n" \
"v_mfma_f32_16x16x32_bf16 a[36:39], v[128:131], v[64:67], a[36:39]\n" \
"s_add_u32 m0, %[mB], 0x8c00\n" \
"s_add_u32 s85, s63, 0x18000\n" \
"v_mfma_f32_16x16x32_bf16 a[68:71], v[128:131], v[72:75], 0\n" \
"ds_read_b64_tr_b16 v[156:157], v33 offset:16384\n" \
"ds_read_b64_tr_b16 v[158:159], v33 offset:17408\n" \
"v_mfma_f32_16x16x32_bf16 a[68:71], v[124:127], v[68:71], a[68:71]\n" \
"buffer_load_dwordx4 v27, s[68:71], s85 offen lds\n" \
"v_mfma_f32_16x16x32_bf16 a[100:103], v[124:127], v[76:79], 0\n" \
"s_add_u32 m0, %[mB], 0xcc00\n" \
"s_add_u32 s84, s63, 0x18000\n" \
"v_mfma_f32_16x16x32_bf16 a[100:103], v[128:131], v[80:83], a[100:103]\n" \
"ds_read_b64_tr_b16 v[168:169], v34 offset:24576\n" \
"s_waitcnt lgkmcnt(7)\n" \
"v_mfma_f32_16x16x32_bf16 a[8:11], v[136:139], v[56:59], 0\n" \
"buffer_load_dwordx4 v27, s[72:75], s84 offen lds\n" \
"v_mfma_f32_16x16x32_bf16 a[8:11], v[132:135], v[52:55], a[8:11]\n" \
"ds_read_b64_tr_b16 v[170:171], v34 offset:25600\n" \
"ds_read_b64_tr_b16 v[164:165], v34 offset:16384\n" \
"v_mfma_f32_16x16x32_bf16 a[40:43], v[132:135], v[60:63], 0\n" \
"ds_read_b64_tr_b16 v[166:167], v34 offset:17408\n" \
"v_lshrrev_b32_e32 v216, 18, v215\n" \
"v_mfma_f32_16x16x32_bf16 a[40:43], v[136:139], v[64:67], a[40:43]\n" \
"v_mul_u32_u24_e32 v218, 6, v216\n" \
"v_sub_u32_e32 v219, v214, v218\n" \
"v_mfma_f32_16x16x32_bf16 a[72:75], v[136:139], v[72:75], 0\n" \
"v_lshrrev_b32_e32 v220, 1, v219\n" \
"v_mul_u32_u24_e32 v217, 27, v216\n" \
"v_mfma_f32_16x16x32_bf16 a[72:75], v[132:135], v[68:71], a[72:75]\n" \
"v_mul_u32_u24_e32 v221, 9, v220\n" \
"v_add_u32_e32 v222, v217, v221\n" \
"v_mfma_f32_16x16x32_bf16 a[104:107], v[132:135], v[76:79], 0\n" \
"v_and_b32_e32 v223, 1, v219\n" \
"s_and_b32 s94, 7, %[b]\n" \
"v_mfma_f32_16x16x32_bf16 a[104:107], v[136:139], v[80:83], a[104:107]\n" \
"ds_read_b64_tr_b16 v[176:177], v35 offset:24576\n" \
"v_add_u32_e64 v224, v222, v223\n" \
"v_mfma_f32_16x16x32_bf16 a[12:15], v[144:147], v[56:59], 0\n" \
"ds_read_b64_tr_b16 v[178:179], v35 offset:25600\n" \
"v_add_u32_e64 v225, s94, v224\n" \
"v_mfma_f32_16x16x32_bf16 a[12:15], v[140:143], v[52:55], a[12:15]\n" \
"ds_read_b64_tr_b16 v[172:173], v35 offset:16384\n" \
"v_lshlrev_b32_e32 v186, 7, v225\n" \
"v_mfma_f32_16x16x32_bf16 a[44:47], v[140:143], v[60:63], 0\n" \
"ds_read_b64_tr_b16 v[174:175], v35 offset:17408\n" \
"ds_read_b128 v[84:87], v28 offset:0\n" \
"v_mfma_f32_16x16x32_bf16 a[44:47], v[144:147], v[64:67], a[44:47]\n" \
"ds_read_b128 v[88:91], v29 offset:0\n" \
"ds_read_b128 v[92:95], v28 offset:2048\n" \
"v_mfma_f32_16x16x32_bf16 a[76:79], v[144:147], v[72:75], 0\n" \
"ds_read_b128 v[96:99], v29 offset:2048\n" \
"s_mul_i32 s89, 6, s97\n" \
"v_mfma_f32_16x16x32_bf16 a[76:79], v[140:143], v[68:71], a[76:79]\n" \
"ds_read_b128 v[100:103], v28 offset:4096\n" \
"s_add_u32 s95, 0xffffffdc, s89\n" \
"v_mfma_f32_16x16x32_bf16 a[108:111], v[140:143], v[76:79], 0\n" \
"ds_read_b128 v[104:107], v29 offset:4096\n" \
"v_min_u32_e32 v227, 41, v212\n" \
"v_mfma_f32_16x16x32_bf16 a[108:111], v[144:147], v[80:83], a[108:111]\n" \
"ds_read_b128 v[108:111], v28 offset:6144\n" \
"s_mul_i32 s99, 0x1200, s94\n" \
"v_mfma_f32_16x16x32_bf16 a[16:19], v[56:59], v[152:155], 0\n" \
"ds_read_b128 v[112:115], v29 offset:6144\n" \
"s_waitcnt lgkmcnt(12)\n" \
"v_mfma_f32_16x16x32_bf16 a[16:19], v[52:55], v[148:151], a[16:19]\n" \
"s_mul_i32 s100, 36, s97\n" \
"v_add_u32_e32 v228, s95, v227\n" \
"v_mfma_f32_16x16x32_bf16 a[48:51], v[60:63], v[148:151], 0\n" \
"s_add_u32 s101, s99, s100\n" \
"s_mul_i32 s88, 0x288, s94\n" \
"v_mfma_f32_16x16x32_bf16 a[48:51], v[64:67], v[152:155], a[48:51]\n" \
"s_mov_b32 m0, %[mS]\n" \
"s_mov_b32 s86, s12\n" \
"v_mfma_f32_16x16x32_bf16 a[80:83], v[72:75], v[152:155], 0\n" \
"s_mov_b32 s87, s13\n" \
"v_min_u32_e32 v229, 0x287, v228\n" \
"v_mfma_f32_16x16x32_bf16 a[80:83], v[68:71], v[148:151], a[80:83]\n" \
"v_add_u32_e32 v226, s101, v212\n" \
"v_add_u32_e32 v230, s88, v229\n" \
"v_mfma_f32_16x16x32_bf16 a[112:115], v[76:79], v[148:151], 0\n" \
"v_cmp_gt_u32_e32 vcc, 36, v212\n" \
"s_lshl_b32 s96, %[w], 6\n" \
"v_mfma_f32_16x16x32_bf16 a[112:115], v[80:83], v[152:155], a[112:115]\n" \
"v_cndmask_b32_e32 v231, v230, v226, vcc\n" \
"v_lshlrev_b32_e32 v16, 7, v231\n" \
"v_mfma_f32_16x16x32_bf16 a[20:23], v[56:59], v[160:163], 0\n" \
"v_lshrrev_b32_e32 v212, 4, v20\n" \
"v_add_u32_e32 v213, s96, v212\n" \
"v_mfma_f32_16x16x32_bf16 a[20:23], v[52:55], v[156:159], a[20:23]\n" \
"v_mul_u32_u24_e32 v214, 0x2aab, v213\n" \
"v_lshrrev_b32_e32 v215, 16, v214\n" \
"s_mov_b32 exec_lo, 0x7ffffff\n" \
"s_mov_b32 exec_hi, 0x0\n" \
"global_load_lds_dword v186, s[86:87]\n" \
"s_mov_b64 exec, -1\n" \
"v_mfma_f32_16x16x32_bf16 a[52:55], v[60:63], v[156:159], 0\n" \
"s_lshr_b32 s94, %[b], 5\n" \
"v_mul_u32_u24_e32 v218, 6, v215\n" \
"v_mfma_f32_16x16x32_bf16 a[52:55], v[64:67], v[160:163], a[52:55]\n" \
"s_mul_i32 s95, 0x48, s94\n" \
"v_sub_u32_e32 v219, v213, v218\n" \
"v_mfma_f32_16x16x32_bf16 a[84:87], v[72:75], v[160:163], 0\n" \
"v_add_u32_e32 v216, s95, v215\n" \
"v_lshrrev_b32_e32 v220, 1, v219\n" \
"v_mfma_f32_16x16x32_bf16 a[84:87], v[68:71], v[156:159], a[84:87]\n" \
"s_and_b32 s100, 7, %[b]\n" \
"s_lshr_b32 s88, %[b], 3\n" \
"v_mfma_f32_16x16x32_bf16 a[116:119], v[76:79], v[156:159], 0\n" \
"v_mul_u32_u24_e32 v217, 0xd80, v216\n" \
"v_mul_u32_u24_e32 v221, 0x480, v220\n" \
"v_mfma_f32_16x16x32_bf16 a[116:119], v[80:83], v[160:163], a[116:119]\n" \
"v_and_b32_e64 v223, 1, v219\n" \
"s_add_u32 s97, 0x100, s96\n" \
"v_mfma_f32_16x16x32_bf16 a[24:27], v[56:59], v[168:171], 0\n" \
"s_lshl_b32 s101, s100, 2\n" \
"s_and_b32 s89, 3, s88\n" \
"v_mfma_f32_16x16x32_bf16 a[24:27], v[52:55], v[164:167], a[24:27]\n" \
"v_add_u32_e32 v222, v217, v221\n" \
"v_lshlrev_b32_e32 v224, 4, v223\n" \
"v_mfma_f32_16x16x32_bf16 a[56:59], v[60:63], v[164:167], 0\n" \
"s_add_u32 s96, s101, s89\n" \
"v_add_u32_e32 v192, v222, v224\n" \
"v_mfma_f32_16x16x32_bf16 a[56:59], v[64:67], v[168:171], a[56:59]\n" \
"s_mul_i32 s56, 36, s96\n" \
"v_add_u32_e32 v225, s97, v212\n" \
"v_mfma_f32_16x16x32_bf16 a[88:91], v[72:75], v[168:171], 0\n" \
"v_mul_u32_u24_e32 v226, 0x2aab, v225\n" \
"v_lshrrev_b32_e32 v227, 16, v226\n" \
"v_mfma_f32_16x16x32_bf16 a[88:91], v[68:71], v[164:167], a[88:91]\n" \
"v_mul_u32_u24_e32 v230, 6, v227\n" \
"v_sub_u32_e32 v231, v225, v230\n" \
"v_mfma_f32_16x16x32_bf16 a[120:123], v[76:79], v[164:167], 0\n" \
"v_add_u32_e32 v228, s95, v227\n" \
"v_lshrrev_b32_e32 v232, 1, v231\n" \
"v_mfma_f32_16x16x32_bf16 a[120:123], v[80:83], v[168:171], a[120:123]\n" \
"v_mul_u32_u24_e32 v229, 0xd80, v228\n" \
"s_waitcnt lgkmcnt(0)\n" \
"v_mfma_f32_16x16x32_bf16 a[28:31], v[56:59], v[176:179], 0\n" \
"s_add_u32 s62, s22, 0x100\n" \
"s_and_b32 s62, s62, 0x3ff\n" \
"v_mfma_f32_16x16x32_bf16 a[28:31], v[52:55], v[172:175], a[28:31]\n" \
"s_lshl_b32 s63, s62, 12\n" \
"v_mul_u32_u24_e32 v233, 0x480, v232\n" \
"v_mfma_f32_16x16x32_bf16 a[60:63], v[60:63], v[172:175], 0\n" \
"v_and_b32_e32 v235, 1, v231\n" \
"v_add_u32_e64 v234, v229, v233\n" \
"v_mfma_f32_16x16x32_bf16 a[60:63], v[64:67], v[176:179], a[60:63]\n" \
"v_lshlrev_b32_e32 v236, 4, v235\n" \
"v_add_u32_e32 v237, v234, v236\n" \
"v_mfma_f32_16x16x32_bf16 a[92:95], v[72:75], v[176:179], 0\n" \
"v_cmp_gt_u32_e32 vcc, 0x1b0, v225\n" \
"s_mul_i32 s98, 18, %[w]\n" \
"v_mfma_f32_16x16x32_bf16 a[92:95], v[68:71], v[172:175], a[92:95]\n" \
"v_cndmask_b32_e32 v193, 0x40000000, v237, vcc\n" \
"s_add_u32 s99, s95, s98\n" \
"v_mfma_f32_16x16x32_bf16 a[124:127], v[76:79], v[172:175], 0\n" \
"v_add_u32_e32 v238, s99, v212\n" \
"v_mul_u32_u24_e32 v239, 0xd80, v238\n" \
"v_mfma_f32_16x16x32_bf16 a[124:127], v[80:83], v[176:179], a[124:127]\n" \
"v_cmp_gt_u32_e32 vcc, 18, v212\n" \
"v_lshlrev_b32_e32 v240, 1, v238\n" \
"s_waitcnt vmcnt(1)\n" \
"s_barrier\n" \
"v_mfma_f32_16x16x32_bf16 a[144:147], v[88:91], v[152:155], 0\n" \
"s_add_u32 m0, %[mA], 0x0\n" \
"s_add_u32 s85, s62, 0x0\n" \
"v_mfma_f32_16x16x32_bf16 a[144:147], v[84:87], v[148:151], a[144:147]\n" \
"buffer_load_dwordx4 v22, s[64:67], s85 offen lds\n" \
"v_mfma_f32_16x16x32_bf16 a[176:179], v[92:95], v[148:151], 0\n" \
"ds_read_b128 v[56:59], v29 offset:49152\n" \
"ds_read_b128 v[52:55], v28 offset:49152\n" \
"v_mfma_f32_16x16x32_bf16 a[176:179], v[96:99], v[152:155], a[176:179]\n" \
"s_add_u32 m0, %[mA], 0x400\n" \
"s_add_u32 s84, s62, 0x2000\n" \
"v_mfma_f32_16x16x32_bf16 a[208:211], v[104:107], v[152:155], 0\n" \
"buffer_load_dwordx4 v23, s[64:67], s84 offen lds\n" \
"v_mfma_f32_16x16x32_bf16 a[208:211], v[100:103], v[148:151], a[208:211]\n" \
"ds_read_b128 v[60:63], v28 offset:51200\n" \
"ds_read_b128 v[64:67], v29 offset:51200\n" \
"v_mfma_f32_16x16x32_bf16 a[240:243], v[108:111], v[148:151], 0\n" \
"ds_read_b64_tr_b16 v[148:149], v32 offset:49152\n" \
"ds_read_b64_tr_b16 v[150:151], v32 offset:50176\n" \
"v_mfma_f32_16x16x32_bf16 a[240:243], v[112:115], v[152:155], a[240:243]\n" \
"s_add_u32 m0, %[mB], 0x0\n" \
"s_add_u32 s85, s63, 0x0\n" \
"v_mfma_f32_16x16x32_bf16 a[148:151], v[88:91], v[160:163], 0\n" \
"buffer_load_dwordx4 v26, s[68:71], s85 offen lds\n" \
"v_mfma_f32_16x16x32_bf16 a[148:151], v[84:87], v[156:159], a[148:151]\n" \
"ds_read_b64_tr_b16 v[152:153], v32 offset:57344\n" \
"ds_read_b64_tr_b16 v[154:155], v32 offset:58368\n" \
"v_mfma_f32_16x16x32_bf16 a[180:183], v[92:95], v[156:159], 0\n" \
"ds_read_b128 v[72:75], v29 offset:53248\n" \
"ds_read_b128 v[68:71], v28 offset:53248\n" \
"v_mfma_f32_16x16x32_bf16 a[180:183], v[96:99], v[160:163], a[180:183]\n" \
"s_add_u32 m0, %[mB], 0x4000\n" \
"s_add_u32 s84, s63, 0x0\n" \
"s_nop 0\n" \
"v_mfma_f32_16x16x32_bf16 a[212:215], v[104:107], v[160:163], 0\n" \
"buffer_load_dwordx4 v26, s[72:75], s84 offen lds\n" \
"v_mfma_f32_16x16x32_bf16 a[212:215], v[100:103], v[156:159], a[212:215]\n" \
"ds_read_b128 v[76:79], v28 offset:55296\n" \
"ds_read_b128 v[80:83], v29 offset:55296\n" \
"v_mfma_f32_16x16x32_bf16 a[244:247], v[108:111], v[156:159], 0\n" \
"ds_read_b64_tr_b16 v[156:157], v33 offset:49152\n" \
"ds_read_b64_tr_b16 v[158:159], v33 offset:50176\n" \
"v_mfma_f32_16x16x32_bf16 a[244:247], v[112:115], v[160:163], a[244:247]\n" \
"s_add_u32 m0, %[mA], 0x800\n" \
"s_add_u32 s85, s62, 0x4000\n" \
"v_mfma_f32_16x16x32_bf16 a[152:155], v[88:91], v[168:171], 0\n" \
"buffer_load_dwordx4 v22, s[64:67], s85 offen lds\n" \
"v_mfma_f32_16x16x32_bf16 a[152:155], v[84:87], v[164:167], a[152:155]\n" \
"ds_read_b64_tr_b16 v[160:161], v33 offset:57344\n" \
"ds_read_b64_tr_b16 v[162:163], v33 offset:58368\n" \
"v_mfma_f32_16x16x32_bf16 a[184:187], v[92:95], v[164:167], 0\n" \
"v_cndmask_b32_e32 v194, 0x40000000, v239, vcc\n" \
"v_cmp_gt_u32_e32 vcc, 18, v212\n" \
"v_mfma_f32_16x16x32_bf16 a[184:187], v[96:99], v[168:171], a[184:187]\n" \
"s_add_u32 m0, %[mA], 0xc00\n" \
"s_add_u32 s84, s62, 0x6000\n" \
"v_mfma_f32_16x16x32_bf16 a[216:219], v[104:107], v[168:171], 0\n" \
"buffer_load_dwordx4 v23, s[64:67], s84 offen lds\n" \
"v_mfma_f32_16x16x32_bf16 a[216:219], v[100:103], v[164:167], a[216:219]\n" \
"v_cndmask_b32_e32 v195, 0x40000000, v240, vcc\n" \
"v_lshrrev_b32_e32 v230, 1, v238\n" \
"v_mfma_f32_16x16x32_bf16 a[248:251], v[108:111], v[164:167], 0\n" \
"ds_read_b64_tr_b16 v[164:165], v34 offset:49152\n" \
"ds_read_b64_tr_b16 v[166:167], v34 offset:50176\n" \
"v_mfma_f32_16x16x32_bf16 a[248:251], v[112:115], v[168:171], a[248:251]\n" \
"s_add_u32 m0, %[mB], 0x400\n" \
"s_add_u32 s85, s63, 0x8000\n" \
"v_mfma_f32_16x16x32_bf16 a[156:159], v[88:91], v[176:179], 0\n" \
"buffer_load_dwordx4 v26, s[68:71], s85 offen lds\n" \
"v_mfma_f32_16x16x32_bf16 a[156:159], v[84:87], v[172:175], a[156:159]\n" \
"ds_read_b64_tr_b16 v[168:169], v34 offset:57344\n" \
"ds_read_b64_tr_b16 v[170:171], v34 offset:58368\n" \
"v_mfma_f32_16x16x32_bf16 a[188:191], v[92:95], v[172:175], 0\n" \
"v_and_b32_e32 v227, 0xff8, v238\n" \
"v_and_b32_e64 v232, 2, v230\n" \
"v_mfma_f32_16x16x32_bf16 a[188:191], v[96:99], v[176:179], a[188:191]\n" \
"s_add_u32 m0, %[mB], 0x4400\n" \
"s_add_u32 s84, s63, 0x8000\n" \
"v_mfma_f32_16x16x32_bf16 a[220:223], v[104:107], v[176:179], 0\n" \
"buffer_load_dwordx4 v26, s[72:75], s84 offen lds\n" \
"v_mfma_f32_16x16x32_bf16 a[220:223], v[100:103], v[172:175], a[220:223]\n" \
"v_add_u32_e32 v229, v227, v232\n" \
"v_and_b32_e32 v233, 1, v238\n" \
"v_mfma_f32_16x16x32_bf16 a[252:255], v[108:111], v[172:175], 0\n" \
"ds_read_b64_tr_b16 v[172:173], v35 offset:49152\n" \
"ds_read_b64_tr_b16 v[174:175], v35 offset:50176\n" \
"v_mfma_f32_16x16x32_bf16 a[252:255], v[112:115], v[176:179], a[252:255]\n" \
"s_add_u32 m0, %[mA], 0x1000\n" \
"s_add_u32 s85, s62, 0x8000\n" \
"v_mfma_f32_16x16x32_bf16 a[128:131], v[120:123], v[88:91], 0\n" \
"buffer_load_dwordx4 v22, s[64:67], s85 offen lds\n" \
"v_mfma_f32_16x16x32_bf16 a[128:131], v[116:119], v[84:87], a[128:131]\n" \
"ds_read_b64_tr_b16 v[176:177], v35 offset:57344\n" \
"ds_read_b64_tr_b16 v[178:179], v35 offset:58368\n" \
"v_mfma_f32_16x16x32_bf16 a[160:163], v[116:119], v[92:95], 0\n" \
"v_add_u32_e32 v235, v229, v233\n" \
"v_lshrrev_b32_e32 v242, 1, v216\n" \
"v_mfma_f32_16x16x32_bf16 a[160:163], v[120:123], v[96:99], a[160:163]\n" \
"s_add_u32 m0, %[mA], 0x1400\n" \
"s_add_u32 s84, s62, 0xa000\n" \
"v_mfma_f32_16x16x32_bf16 a[192:195], v[120:123], v[104:107], 0\n" \
"buffer_load_dwordx4 v23, s[64:67], s84 offen lds\n" \
"v_mfma_f32_16x16x32_bf16 a[192:195], v[116:119], v[100:103], a[192:195]\n" \
"v_lshrrev_b32_e32 v244, 2, v216\n" \
"v_lshrrev_b32_e32 v214, 1, v228\n" \
"v_mfma_f32_16x16x32_bf16 a[224:227], v[116:119], v[108:111], 0\n" \
"ds_read_b64_tr_b16 v[116:117], v32 offset:32768\n" \
"ds_read_b64_tr_b16 v[118:119], v32 offset:33792\n" \
"v_mfma_f32_16x16x32_bf16 a[224:227], v[120:123], v[112:115], a[224:227]\n" \
"s_add_u32 m0, %[mB], 0x800\n" \
"s_add_u32 s85, s63, 0x10000\n" \
"v_mfma_f32_16x16x32_bf16 a[132:135], v[128:131], v[88:91], 0\n" \
"buffer_load_dwordx4 v27, s[68:71], s85 offen lds\n" \
"v_mfma_f32_16x16x32_bf16 a[132:135], v[124:127], v[84:87], a[132:135]\n" \
"ds_read_b64_tr_b16 v[120:121], v32 offset:40960\n" \
"ds_read_b64_tr_b16 v[122:123], v32 offset:41984\n" \
"v_mfma_f32_16x16x32_bf16 a[164:167], v[124:127], v[92:95], 0\n" \
"v_lshrrev_b32_e32 v213, 2, v228\n" \
"v_lshrrev_b32_e32 v236, 1, v235\n" \
"v_mfma_f32_16x16x32_bf16 a[164:167], v[128:131], v[96:99], a[164:167]\n" \
"s_add_u32 m0, %[mB], 0x4800\n" \
"s_add_u32 s84, s63, 0x10000\n" \
"v_mfma_f32_16x16x32_bf16 a[196:199], v[128:131], v[104:107], 0\n" \
"buffer_load_dwordx4 v27, s[72:75], s84 offen lds\n" \
"v_mfma_f32_16x16x32_bf16 a[196:199], v[124:127], v[100:103], a[196:199]\n" \
"v_lshrrev_b32_e32 v239, 2, v235\n" \
"s_lshl_b32 s98, %[w], 10\n" \
"v_mfma_f32_16x16x32_bf16 a[228:231], v[124:127], v[108:111], 0\n" \
"ds_read_b64_tr_b16 v[124:125], v33 offset:32768\n" \
"ds_read_b64_tr_b16 v[126:127], v33 offset:33792\n" \
"v_mfma_f32_16x16x32_bf16 a[228:231], v[128:131], v[112:115], a[228:231]\n" \
"ds_read_b64_tr_b16 v[128:129], v33 offset:40960\n" \
"L_pf0:\n" \
"s_cbranch_execz L_pf4\n" \
"s_nop 0\n" \
"ds_read_b64_tr_b16 v[130:131], v33 offset:41984\n" \
"v_mfma_f32_16x16x32_bf16 a[136:139], v[136:139], v[88:91], 0\n" \
"s_mul_i32 s101, 0x12000, s96\n" \
"v_and_b32_e64 v243, 1, v242\n" \
"v_mfma_f32_16x16x32_bf16 a[136:139], v[132:135], v[84:87], a[136:139]\n" \
"v_and_b32_e32 v245, 2, v244\n" \
"v_and_b32_e32 v215, 1, v214\n" \
"v_mfma_f32_16x16x32_bf16 a[168:171], v[132:135], v[92:95], 0\n" \
"v_and_b32_e32 v218, 2, v213\n" \
"v_and_b32_e32 v237, 1, v236\n" \
"v_mfma_f32_16x16x32_bf16 a[168:171], v[136:139], v[96:99], a[168:171]\n" \
"v_and_b32_e64 v240, 2, v239\n" \
"s_add_u32 s99, 0x22000, s98\n" \
"v_mfma_f32_16x16x32_bf16 a[200:203], v[136:139], v[104:107], 0\n" \
"s_add_u32 s89, 0x6000000, s101\n" \
"s_mul_i32 s97, 0x2400, s94\n" \
"v_mfma_f32_16x16x32_bf16 a[200:203], v[132:135], v[100:103], a[200:203]\n" \
"v_lshlrev_b32_e32 v241, 7, v216\n" \
"v_or_b32_e32 v246, v243, v245\n" \
"v_mfma_f32_16x16x32_bf16 a[232:235], v[132:135], v[108:111], 0\n" \
"ds_read_b64_tr_b16 v[132:133], v34 offset:32768\n" \
"ds_read_b64_tr_b16 v[134:135], v34 offset:33792\n" \
"v_mfma_f32_16x16x32_bf16 a[232:235], v[136:139], v[112:115], a[232:235]\n" \
"ds_read_b64_tr_b16 v[136:137], v34 offset:40960\n" \
"ds_read_b64_tr_b16 v[138:139], v34 offset:41984\n" \
"v_mfma_f32_16x16x32_bf16 a[140:143], v[144:147], v[88:91], 0\n" \
"ds_read_b128 v[88:91], v29 offset:32768\n" \
"v_or_b32_e32 v220, v215, v218\n" \
"v_mfma_f32_16x16x32_bf16 a[140:143], v[140:143], v[84:87], a[140:143]\n" \
"ds_read_b128 v[84:87], v28 offset:32768\n" \
"v_or_b32_e32 v242, v237, v240\n" \
"v_mfma_f32_16x16x32_bf16 a[172:175], v[140:143], v[92:95], 0\n" \
"ds_read_b128 v[92:95], v28 offset:34816\n" \
"v_add_u32_e64 v199, s99, v20\n" \
"v_mfma_f32_16x16x32_bf16 a[172:175], v[144:147], v[96:99], a[172:175]\n" \
"ds_read_b128 v[96:99], v29 offset:34816\n" \
"s_add_u32 s37, s89, s98\n" \
"v_mfma_f32_16x16x32_bf16 a[204:207], v[144:147], v[104:107], 0\n" \
"ds_read_b128 v[104:107], v29 offset:36864\n" \
"v_lshlrev_b32_e32 v247, 1, v246\n" \
"v_mfma_f32_16x16x32_bf16 a[204:207], v[140:143], v[100:103], a[204:207]\n" \
"ds_read_b128 v[100:103], v28 offset:36864\n" \
"v_lshlrev_b32_e32 v217, 1, v220\n" \
"v_mfma_f32_16x16x32_bf16 a[236:239], v[140:143], v[108:111], 0\n" \
"ds_read_b64_tr_b16 v[140:141], v35 offset:32768\n" \
"ds_read_b64_tr_b16 v[142:143], v35 offset:33792\n" \
"v_mfma_f32_16x16x32_bf16 a[236:239], v[144:147], v[112:115], a[236:239]\n" \
"s_add_u32 m0, %[mA], 0x1800\n" \
"s_waitcnt lgkmcnt(12)\n" \
"v_mfma_f32_16x16x32_bf16 a[16:19], v[56:59], v[152:155], a[16:19]\n" \
"s_add_u32 s85, s62, 0xc000\n" \
"ds_read_b64_tr_b16 v[144:145], v35 offset:40960\n" \
"v_mfma_f32_16x16x32_bf16 a[16:19], v[52:55], v[148:151], a[16:19]\n" \
"buffer_load_dwordx4 v22, s[64:67], s85 offen lds\n" \
"v_mfma_f32_16x16x32_bf16 a[48:51], v[60:63], v[148:151], a[48:51]\n" \
"s_add_u32 m0, %[mA], 0x1c00\n" \
"s_add_u32 s84, s62, 0xe000\n" \
"v_mfma_f32_16x16x32_bf16 a[48:51], v[64:67], v[152:155], a[48:51]\n" \
"ds_read_b64_tr_b16 v[146:147], v35 offset:41984\n" \
"ds_read_b128 v[108:111], v28 offset:38912\n" \
"v_mfma_f32_16x16x32_bf16 a[80:83], v[72:75], v[152:155], a[80:83]\n" \
"buffer_load_dwordx4 v23, s[64:67], s84 offen lds\n" \
"v_mfma_f32_16x16x32_bf16 a[80:83], v[68:71], v[148:151], a[80:83]\n" \
"s_add_u32 m0, %[mB], 0xc00\n" \
"s_add_u32 s85, s63, 0x18000\n" \
"v_mfma_f32_16x16x32_bf16 a[112:115], v[76:79], v[148:151], a[112:115]\n" \
"ds_read_b128 v[112:115], v29 offset:38912\n" \
"v_xor_b32_e64 v216, 3, v242\n" \
"v_mfma_f32_16x16x32_bf16 a[112:115], v[80:83], v[152:155], a[112:115]\n" \
"buffer_load_dwordx4 v27, s[68:71], s85 offen lds\n" \
"v_mfma_f32_16x16x32_bf16 a[20:23], v[56:59], v[160:163], a[20:23]\n" \
"s_add_u32 m0, %[mB], 0x4c00\n" \
"s_add_u32 s84, s63, 0x18000\n" \
"v_mfma_f32_16x16x32_bf16 a[20:23], v[52:55], v[156:159], a[20:23]\n" \
"s_add_u32 s57, s37, s97\n" \
"s_lshl_b32 s99, s88, 2\n" \
"v_mfma_f32_16x16x32_bf16 a[52:55], v[60:63], v[156:159], a[52:55]\n" \
"buffer_load_dwordx4 v27, s[72:75], s84 offen lds\n" \
"v_mfma_f32_16x16x32_bf16 a[52:55], v[64:67], v[160:163], a[52:55]\n" \
"v_xor_b32_e32 v248, v219, v247\n" \
"v_xor_b32_e32 v221, v231, v217\n" \
"v_mfma_f32_16x16x32_bf16 a[84:87], v[72:75], v[160:163], a[84:87]\n" \
"v_lshlrev_b32_e32 v234, 7, v235\n" \
"v_lshlrev_b32_e32 v244, 5, v216\n" \
"v_mfma_f32_16x16x32_bf16 a[84:87], v[68:71], v[156:159], a[84:87]\n" \
"v_and_b32_e64 v245, 2, v238\n" \
"s_add_u32 s94, 0x2000, s57\n" \
"v_mfma_f32_16x16x32_bf16 a[116:119], v[76:79], v[156:159], a[116:119]\n" \
"s_add_u32 s96, s99, %[w]\n" \
"s_lshl_b32 s89, %[b], 2\n" \
"v_mfma_f32_16x16x32_bf16 a[116:119], v[80:83], v[160:163], a[116:119]\n" \
"s_mov_b32 m0, %[mS]\n" \
"s_mov_b32 s86, s18\n" \
"v_mfma_f32_16x16x32_bf16 a[24:27], v[56:59], v[168:171], a[24:27]\n" \
"s_mov_b32 s87, s19\n" \
"s_sub_u32 s95, 0x22000, s97\n" \
"v_mfma_f32_16x16x32_bf16 a[24:27], v[52:55], v[164:167], a[24:27]\n" \
"v_lshlrev_b32_e32 v249, 4, v248\n" \
"v_lshlrev_b32_e32 v251, 7, v228\n" \
"v_mfma_f32_16x16x32_bf16 a[56:59], v[60:63], v[164:167], a[56:59]\n" \
"v_lshlrev_b32_e32 v223, 4, v221\n" \
"v_add_u32_e64 v243, v234, v244\n" \
"v_mfma_f32_16x16x32_bf16 a[56:59], v[64:67], v[168:171], a[56:59]\n" \
"v_lshlrev_b32_e32 v246, 1, v245\n" \
"s_mul_i32 s101, 36, s96\n" \
"v_mfma_f32_16x16x32_bf16 a[88:91], v[72:75], v[168:171], a[88:91]\n" \
"s_cmp_eq_u32 %[w], 0\n" \
"s_cselect_b32 s58, s94, 0x40000000\n" \
"v_mfma_f32_16x16x32_bf16 a[88:91], v[68:71], v[164:167], a[88:91]\n" \
"s_add_u32 s98, s89, %[w]\n" \
"v_add_u32_e32 v250, v241, v249\n" \
"s_mov_b32 exec_lo, 0x3ffff\n" \
"s_mov_b32 exec_hi, 0x0\n" \
"global_load_lds_dword v16, s[86:87]\n" \
"s_mov_b64 exec, -1\n" \
"v_mfma_f32_16x16x32_bf16 a[120:123], v[76:79], v[164:167], a[120:123]\n" \
"v_add_u32_e32 v222, v251, v223\n" \
"v_add_u32_e32 v219, v243, v246\n" \
"v_mfma_f32_16x16x32_bf16 a[120:123], v[80:83], v[168:171], a[120:123]\n" \
"v_add_u32_e32 v248, s101, v212\n" \
"s_mul_i32 s97, 18, s98\n" \
"v_mfma_f32_16x16x32_bf16 a[28:31], v[56:59], v[176:179], a[28:31]\n" \
"v_add_u32_e32 v196, s95, v250\n" \
"v_add_u32_e32 v224, s95, v222\n" \
"v_mfma_f32_16x16x32_bf16 a[28:31], v[52:55], v[172:175], a[28:31]\n" \
"v_add_u32_e32 v247, s95, v219\n" \
"v_lshrrev_b32_e32 v241, 3, v248\n" \
"v_mfma_f32_16x16x32_bf16 a[60:63], v[60:63], v[172:175], a[60:63]\n" \
"s_add_u32 s94, 0xffffffdc, s97\n" \
"v_min_u32_e32 v213, 53, v212\n" \
"v_mfma_f32_16x16x32_bf16 a[60:63], v[64:67], v[176:179], a[60:63]\n" \
"s_lshl_b32 s95, s100, 3\n" \
"v_lshlrev_b32_e32 v249, 6, v241\n" \
"v_mfma_f32_16x16x32_bf16 a[92:95], v[72:75], v[176:179], a[92:95]\n" \
"v_add_u32_e32 v215, s94, v213\n" \
"v_add_u32_e64 v250, s95, v249\n" \
"v_mfma_f32_16x16x32_bf16 a[92:95], v[68:71], v[172:175], a[92:95]\n" \
"s_lshr_b32 s94, %[b], 5\n" \
"s_and_b32 s97, 7, %[b]\n" \
"v_mfma_f32_16x16x32_bf16 a[124:127], v[76:79], v[172:175], a[124:127]\n" \
"s_lshr_b32 s99, %[b], 3\n" \
"s_mul_i32 s95, 0xa00, s94\n" \
"v_mfma_f32_16x16x32_bf16 a[124:127], v[80:83], v[176:179], a[124:127]\n" \
"s_lshl_b32 s98, s97, 2\n" \
"s_and_b32 s100, 3, s99\n" \
"v_mfma_f32_16x16x32_bf16 a[0:3], v[120:123], v[56:59], a[0:3]\n" \
"s_add_u32 s96, 0x5bc1000, s95\n" \
"s_add_u32 s101, s98, s100\n" \
"v_mfma_f32_16x16x32_bf16 a[0:3], v[116:119], v[52:55], a[0:3]\n" \
"s_lshl_b32 s88, s101, 2\n" \
"s_lshl_b32 s95, s94, 3\n" \
"v_mfma_f32_16x16x32_bf16 a[32:35], v[116:119], v[60:63], a[32:35]\n" \
"s_add_u32 s89, s96, s88\n" \
"s_add_u32 s99, s95, s97\n" \
"v_mfma_f32_16x16x32_bf16 a[32:35], v[120:123], v[64:67], a[32:35]\n" \
"v_writelane_b32 v11, s89, 0\n" \
"s_lshl_b32 s88, s99, 10\n" \
"v_mfma_f32_16x16x32_bf16 a[64:67], v[120:123], v[72:75], a[64:67]\n" \
"s_add_u32 s89, 0x5c00000, s88\n" \
"s_lshl_b32 s95, %[w], 8\n" \
"v_mfma_f32_16x16x32_bf16 a[64:67], v[116:119], v[68:71], a[64:67]\n" \
"s_add_u32 s99, s89, s95\n" \
"v_add_u32_e32 v226, 0x24400, v20\n" \
"v_mfma_f32_16x16x32_bf16 a[96:99], v[116:119], v[76:79], a[96:99]\n" \
"v_cmp_gt_u32_e32 vcc, 0x1b0, v225\n" \
"s_or_b32 s88, 0x200, s99\n" \
"v_mfma_f32_16x16x32_bf16 a[96:99], v[120:123], v[80:83], a[96:99]\n" \
"s_lshl_b32 s89, s100, 2\n" \
"v_cndmask_b32_e32 v197, v226, v224, vcc\n" \
"v_mfma_f32_16x16x32_bf16 a[4:7], v[128:131], v[56:59], a[4:7]\n" \
"v_writelane_b32 v11, s88, 1\n" \
"s_add_u32 s95, s99, s89\n" \
"v_mfma_f32_16x16x32_bf16 a[4:7], v[124:127], v[52:55], a[4:7]\n" \
"v_cmp_gt_u32_e32 vcc, 18, v212\n" \
"v_writelane_b32 v11, s95, 2\n" \
"v_mfma_f32_16x16x32_bf16 a[36:39], v[124:127], v[60:63], a[36:39]\n" \
"s_add_u32 s88, 0x80, s99\n" \
"v_cndmask_b32_e32 v198, v226, v247, vcc\n" \
"v_mfma_f32_16x16x32_bf16 a[36:39], v[128:131], v[64:67], a[36:39]\n" \
"v_and_b32_e64 v214, 7, v248\n" \
"v_writelane_b32 v11, s88, 3\n" \
"v_mfma_f32_16x16x32_bf16 a[68:71], v[128:131], v[72:75], a[68:71]\n" \
"s_lshl_b32 s99, s101, 8\n" \
"v_add_u32_e32 v228, v250, v214\n" \
"v_mfma_f32_16x16x32_bf16 a[68:71], v[124:127], v[68:71], a[68:71]\n" \
"v_cmp_gt_u32_e32 vcc, 36, v212\n" \
"s_add_u32 s89, 0x80, s95\n" \
"v_mfma_f32_16x16x32_bf16 a[100:103], v[124:127], v[76:79], a[100:103]\n" \
"s_add_u32 s39, 0x5be8000, s99\n" \
"s_lshl_b32 s88, s94, 2\n" \
"v_mfma_f32_16x16x32_bf16 a[100:103], v[128:131], v[80:83], a[100:103]\n" \
"v_cndmask_b32_e32 v218, v215, v228, vcc\n" \
"s_waitcnt lgkmcnt(0)\n" \
"v_mfma_f32_16x16x32_bf16 a[8:11], v[136:139], v[56:59], a[8:11]\n" \
"s_add_u32 s95, s39, s88\n" \
"v_lshrrev_b32_e32 v212, 4, v20\n" \
"v_mfma_f32_16x16x32_bf16 a[8:11], v[132:135], v[52:55], a[8:11]\n" \
"v_writelane_b32 v11, s89, 4\n" \
"v_lshlrev_b32_e32 v213, 2, v212\n" \
"v_mfma_f32_16x16x32_bf16 a[40:43], v[132:135], v[60:63], a[40:43]\n" \
"s_cmp_eq_u32 %[w], 3\n" \
"s_cselect_b32 s38, s95, 0x40000000\n" \
"v_mfma_f32_16x16x32_bf16 a[40:43], v[136:139], v[64:67], a[40:43]\n" \
"v_and_b32_e32 v215, 7, v212\n" \
"s_add_u32 s101, s88, s100\n" \
"v_mfma_f32_16x16x32_bf16 a[72:75], v[136:139], v[72:75], a[72:75]\n" \
"v_add_u32_e32 v214, s96, v213\n" \
"s_add_u32 s89, 0x5be8020, s99\n" \
"v_mfma_f32_16x16x32_bf16 a[72:75], v[132:135], v[68:71], a[72:75]\n" \
"v_lshlrev_b32_e32 v216, 2, v215\n" \
"s_mul_i32 s94, 0x90000, s101\n" \
"v_mfma_f32_16x16x32_bf16 a[104:107], v[132:135], v[76:79], a[104:107]\n" \
"v_add_u32_e32 v217, s89, v216\n" \
"s_add_u32 s95, 0x4240000, s94\n" \
"v_mfma_f32_16x16x32_bf16 a[104:107], v[136:139], v[80:83], a[104:107]\n" \
"s_lshl_b32 s96, %[w], 14\n" \
"s_add_u32 s99, s95, s96\n" \
"v_mfma_f32_16x16x32_bf16 a[12:15], v[144:147], v[56:59], a[12:15]\n" \
"s_add_u32 s62, s22, 0x180\n" \
"s_and_b32 s62, s62, 0x3ff\n" \
"v_mfma_f32_16x16x32_bf16 a[12:15], v[140:143], v[52:55], a[12:15]\n" \
"s_lshl_b32 s63, s62, 12\n" \
"s_lshl_b32 s89, s97, 16\n" \
"v_mfma_f32_16x16x32_bf16 a[44:47], v[140:143], v[60:63], a[44:47]\n" \
"v_writelane_b32 v11, s39, 52\n" \
"s_add_u32 s88, s99, s89\n" \
"v_mfma_f32_16x16x32_bf16 a[44:47], v[144:147], v[64:67], a[44:47]\n" \
"s_lshl_b32 s101, s97, 11\n" \
"v_writelane_b32 v11, s88, 37\n" \
"v_mfma_f32_16x16x32_bf16 a[76:79], v[144:147], v[72:75], a[76:79]\n" \
"s_add_u32 s100, 0x3800, s88\n" \
"s_add_u32 s94, s99, s101\n" \
"v_mfma_f32_16x16x32_bf16 a[76:79], v[140:143], v[68:71], a[76:79]\n" \
"v_writelane_b32 v11, s100, 51\n" \
"s_add_u32 s95, 0x80000, s94\n" \
"v_mfma_f32_16x16x32_bf16 a[108:111], v[140:143], v[76:79], a[108:111]\n" \
"s_lshl_b32 s96, %[w], 17\n" \
"v_writelane_b32 v11, s95, 49\n" \
"v_mfma_f32_16x16x32_bf16 a[108:111], v[144:147], v[80:83], a[108:111]\n" \
"s_add_u32 s89, s99, s96\n" \
"s_and_b32 s94, 1, s97\n" \
"s_waitcnt vmcnt(1)\n" \
"s_barrier\n" \
"v_mfma_f32_16x16x32_bf16 a[128:131], v[120:123], v[88:91], a[128:131]\n" \
"s_add_u32 m0, %[mA], 0x8000\n" \
"s_add_u32 s85, s62, 0x0\n" \
"s_nop 0\n" \
"v_mfma_f32_16x16x32_bf16 a[128:131], v[116:119], v[84:87], a[128:131]\n" \
"buffer_load_dwordx4 v22, s[64:67], s85 offen lds\n" \
"v_mfma_f32_16x16x32_bf16 a[160:163], v[116:119], v[92:95], a[160:163]\n" \
"ds_read_b128 v[56:59], v29 offset:16384\n" \
"ds_read_b128 v[52:55], v28 offset:16384\n" \
"v_mfma_f32_16x16x32_bf16 a[160:163], v[120:123], v[96:99], a[160:163]\n" \
"s_add_u32 m0, %[mA], 0x8400\n" \
"s_add_u32 s84, s62, 0x2000\n" \
"v_mfma_f32_16x16x32_bf16 a[192:195], v[120:123], v[104:107], a[192:195]\n" \
"buffer_load_dwordx4 v23, s[64:67], s84 offen lds\n" \
"v_mfma_f32_16x16x32_bf16 a[192:195], v[116:119], v[100:103], a[192:195]\n" \
"ds_read_b128 v[60:63], v28 offset:18432\n" \
"ds_read_b128 v[64:67], v29 offset:18432\n" \
"v_mfma_f32_16x16x32_bf16 a[224:227], v[116:119], v[108:111], a[224:227]\n" \
"ds_read_b64_tr_b16 v[116:117], v32 offset:0\n" \
"ds_read_b64_tr_b16 v[118:119], v32 offset:1024\n" \
"v_mfma_f32_16x16x32_bf16 a[224:227], v[120:123], v[112:115], a[224:227]\n" \
"s_add_u32 m0, %[mB], 0x8000\n" \
"s_add_u32 s85, s63, 0x0\n" \
"v_mfma_f32_16x16x32_bf16 a[132:135], v[128:131], v[88:91], a[132:135]\n" \
"buffer_load_dwordx4 v26, s[68:71], s85 offen lds\n" \
"v_mfma_f32_16x16x32_bf16 a[132:135], v[124:127], v[84:87], a[132:135]\n" \
"ds_read_b64_tr_b16 v[120:121], v32 offset:8192\n" \
"ds_read_b64_tr_b16 v[122:123], v32 offset:9216\n" \
"v_mfma_f32_16x16x32_bf16 a[164:167], v[124:127], v[92:95], a[164:167]\n" \
"ds_read_b128 v[72:75], v29 offset:20480\n" \
"ds_read_b128 v[68:71], v28 offset:20480\n" \
"v_mfma_f32_16x16x32_bf16 a[164:167], v[128:131], v[96:99], a[164:167]\n" \
"s_add_u32 m0, %[mB], 0xc000\n" \
"s_add_u32 s84, s63, 0x0\n" \
"v_mfma_f32_16x16x32_bf16 a[196:199], v[128:131], v[104:107], a[196:199]\n" \
"buffer_load_dwordx4 v26, s[72:75], s84 offen lds\n" \
"v_mfma_f32_16x16x32_bf16 a[196:199], v[124:127], v[100:103], a[196:199]\n" \
"ds_read_b128 v[76:79], v28 offset:22528\n" \
"ds_read_b128 v[80:83], v29 offset:22528\n" \
"v_mfma_f32_16x16x32_bf16 a[228:231], v[124:127], v[108:111], a[228:231]\n" \
"ds_read_b64_tr_b16 v[124:125], v33 offset:0\n" \
"ds_read_b64_tr_b16 v[126:127], v33 offset:1024\n" \
"v_mfma_f32_16x16x32_bf16 a[228:231], v[128:131], v[112:115], a[228:231]\n" \
"s_add_u32 m0, %[mA], 0x8800\n" \
"s_add_u32 s85, s62, 0x4000\n" \
"v_mfma_f32_16x16x32_bf16 a[136:139], v[136:139], v[88:91], a[136:139]\n" \
"buffer_load_dwordx4 v22, s[64:67], s85 offen lds\n" \
"v_mfma_f32_16x16x32_bf16 a[136:139], v[132:135], v[84:87], a[136:139]\n" \
"ds_read_b64_tr_b16 v[128:129], v33 offset:8192\n" \
"ds_read_b64_tr_b16 v[130:131], v33 offset:9216\n" \
"v_mfma_f32_16x16x32_bf16 a[168:171], v[132:135], v[92:95], a[168:171]\n" \
"s_add_u32 s88, s89, s98\n" \
"s_lshl_b32 s95, s94, 10\n" \
"v_mfma_f32_16x16x32_bf16 a[168:171], v[136:139], v[96:99], a[168:171]\n" \
"s_add_u32 m0, %[mA], 0x8c00\n" \
"s_add_u32 s84, s62, 0x6000\n" \
"v_mfma_f32_16x16x32_bf16 a[200:203], v[136:139], v[104:107], a[200:203]\n" \
"buffer_load_dwordx4 v23, s[64:67], s84 offen lds\n" \
"v_mfma_f32_16x16x32_bf16 a[200:203], v[132:135], v[100:103], a[200:203]\n" \
"s_add_u32 s100, 0x3840, s88\n" \
"s_add_u32 s96, s99, s95\n" \
"s_nop 0\n" \
"v_mfma_f32_16x16x32_bf16 a[232:235], v[132:135], v[108:111], a[232:235]\n" \
"ds_read_b64_tr_b16 v[132:133], v34 offset:0\n" \
"ds_read_b64_tr_b16 v[134:135], v34 offset:1024\n" \
"v_mfma_f32_16x16x32_bf16 a[232:235], v[136:139], v[112:115], a[232:235]\n" \
"s_add_u32 m0, %[mB], 0x8400\n" \
"s_add_u32 s85, s63, 0x8000\n" \
"v_mfma_f32_16x16x32_bf16 a[140:143], v[144:147], v[88:91], a[140:143]\n" \
"buffer_load_dwordx4 v26, s[68:71], s85 offen lds\n" \
"v_mfma_f32_16x16x32_bf16 a[140:143], v[140:143], v[84:87], a[140:143]\n" \
"ds_read_b64_tr_b16 v[136:137], v34 offset:8192\n" \
"ds_read_b64_tr_b16 v[138:139], v34 offset:9216\n" \
"v_mfma_f32_16x16x32_bf16 a[172:175], v[140:143], v[92:95], a[172:175]\n" \
"v_writelane_b32 v11, s100, 5\n" \
"s_lshr_b32 s101, s97, 1\n" \
"v_mfma_f32_16x16x32_bf16 a[172:175], v[144:147], v[96:99], a[172:175]\n" \
"s_add_u32 m0, %[mB], 0xc400\n" \
"s_add_u32 s84, s63, 0x8000\n" \
"v_mfma_f32_16x16x32_bf16 a[204:207], v[144:147], v[104:107], a[204:207]\n" \
"buffer_load_dwordx4 v26, s[72:75], s84 offen lds\n" \
"v_mfma_f32_16x16x32_bf16 a[204:207], v[140:143], v[100:103], a[204:207]\n" \
"s_add_u32 s89, 0x80000, s96\n" \
"s_add_u32 s88, 1, s97\n" \
"v_mfma_f32_16x16x32_bf16 a[236:239], v[140:143], v[108:111], a[236:239]\n" \
"ds_read_b64_tr_b16 v[140:141], v35 offset:0\n" \
"ds_read_b64_tr_b16 v[142:143], v35 offset:1024\n" \
"v_mfma_f32_16x16x32_bf16 a[236:239], v[144:147], v[112:115], a[236:239]\n" \
"s_add_u32 m0, %[mA], 0x9000\n" \
"s_add_u32 s85, s62, 0x8000\n" \
"v_mfma_f32_16x16x32_bf16 a[144:147], v[88:91], v[152:155], a[144:147]\n" \
"buffer_load_dwordx4 v22, s[64:67], s85 offen lds\n" \
"v_mfma_f32_16x16x32_bf16 a[144:147], v[84:87], v[148:151], a[144:147]\n" \
"ds_read_b64_tr_b16 v[144:145], v35 offset:8192\n" \
"ds_read_b64_tr_b16 v[146:147], v35 offset:9216\n" \
"v_mfma_f32_16x16x32_bf16 a[176:179], v[92:95], v[148:151], a[176:179]\n" \
"s_cmp_eq_u32 %[w], s101\n" \
"s_cselect_b32 s98, s89, 0x40000000\n" \
"v_mfma_f32_16x16x32_bf16 a[176:179], v[96:99], v[152:155], a[176:179]\n" \
"s_add_u32 m0, %[mA], 0x9400\n" \
"s_add_u32 s84, s62, 0xa000\n" \
"v_mfma_f32_16x16x32_bf16 a[208:211], v[104:107], v[152:155], a[208:211]\n" \
"buffer_load_dwordx4 v23, s[64:67], s84 offen lds\n" \
"v_mfma_f32_16x16x32_bf16 a[208:211], v[100:103], v[148:151], a[208:211]\n" \
"s_and_b32 s100, 7, s88\n" \
"s_lshl_b32 s94, s100, 16\n" \
"v_mfma_f32_16x16x32_bf16 a[240:243], v[108:111], v[148:151], a[240:243]\n" \
"ds_read_b64_tr_b16 v[148:149], v32 offset:16384\n" \
"ds_read_b64_tr_b16 v[150:151], v32 offset:17408\n" \
"v_mfma_f32_16x16x32_bf16 a[240:243], v[112:115], v[152:155], a[240:243]\n" \
"s_add_u32 m0, %[mB], 0x8800\n" \
"s_add_u32 s85, s63, 0x10000\n" \
"v_mfma_f32_16x16x32_bf16 a[148:151], v[88:91], v[160:163], a[148:151]\n" \
"buffer_load_dwordx4 v27, s[68:71], s85 offen lds\n" \
"v_mfma_f32_16x16x32_bf16 a[148:151], v[84:87], v[156:159], a[148:151]\n" \
"ds_read_b64_tr_b16 v[152:153], v32 offset:24576\n" \
"ds_read_b64_tr_b16 v[154:155], v32 offset:25600\n" \
"v_mfma_f32_16x16x32_bf16 a[180:183], v[92:95], v[156:159], a[180:183]\n" \
"s_add_u32 s96, 2, s97\n" \
"v_writelane_b32 v11, s98, 38\n" \
"v_mfma_f32_16x16x32_bf16 a[180:183], v[96:99], v[160:163], a[180:183]\n" \
"s_add_u32 m0, %[mB], 0xc800\n" \
"s_add_u32 s84, s63, 0x10000\n" \
"v_mfma_f32_16x16x32_bf16 a[212:215], v[104:107], v[160:163], a[212:215]\n" \
"buffer_load_dwordx4 v27, s[72:75], s84 offen lds\n" \
"v_mfma_f32_16x16x32_bf16 a[212:215], v[100:103], v[156:159], a[212:215]\n" \
"s_add_u32 s95, s99, s94\n" \
"s_and_b32 s101, 7, s96\n" \
"v_mfma_f32_16x16x32_bf16 a[244:247], v[108:111], v[156:159], a[244:247]\n" \
"ds_read_b64_tr_b16 v[156:157], v33 offset:16384\n" \
"ds_read_b64_tr_b16 v[158:159], v33 offset:17408\n" \
"v_mfma_f32_16x16x32_bf16 a[244:247], v[112:115], v[160:163], a[244:247]\n" \
"ds_read_b64_tr_b16 v[160:161], v33 offset:24576\n" \
"ds_read_b64_tr_b16 v[162:163], v33 offset:25600\n" \
"v_mfma_f32_16x16x32_bf16 a[152:155], v[88:91], v[168:171], a[152:155]\n" \
"s_add_u32 s100, 3, s97\n" \
"v_writelane_b32 v11, s95, 42\n" \
"v_mfma_f32_16x16x32_bf16 a[152:155], v[84:87], v[164:167], a[152:155]\n" \
"s_lshl_b32 s89, s101, 16\n" \
"s_and_b32 s94, 7, s100\n" \
"v_mfma_f32_16x16x32_bf16 a[184:187], v[92:95], v[164:167], a[184:187]\n" \
"s_add_u32 s98, s99, s89\n" \
"s_lshl_b32 s95, s94, 16\n" \
"v_mfma_f32_16x16x32_bf16 a[184:187], v[96:99], v[168:171], a[184:187]\n" \
"s_add_u32 s88, 0x800, s98\n" \
"s_add_u32 s96, s99, s95\n" \
"v_mfma_f32_16x16x32_bf16 a[216:219], v[104:107], v[168:171], a[216:219]\n" \
"s_add_u32 s89, 4, s97\n" \
"v_writelane_b32 v11, s88, 43\n" \
"v_mfma_f32_16x16x32_bf16 a[216:219], v[100:103], v[164:167], a[216:219]\n" \
"s_add_u32 s101, 0x1000, s96\n" \
"s_and_b32 s98, 7, s89\n" \
"v_mfma_f32_16x16x32_bf16 a[248:251], v[108:111], v[164:167], a[248:251]\n" \
"ds_read_b64_tr_b16 v[164:165], v34 offset:16384\n" \
"ds_read_b64_tr_b16 v[166:167], v34 offset:17408\n" \
"v_mfma_f32_16x16x32_bf16 a[248:251], v[112:115], v[168:171], a[248:251]\n" \
"ds_read_b64_tr_b16 v[168:169], v34 offset:24576\n" \
"ds_read_b64_tr_b16 v[170:171], v34 offset:25600\n" \
"v_mfma_f32_16x16x32_bf16 a[156:159], v[88:91], v[176:179], a[156:159]\n" \
"ds_read_b128 v[88:91], v29 offset:0\n" \
"s_add_u32 s95, 5, s97\n" \
"v_mfma_f32_16x16x32_bf16 a[156:159], v[84:87], v[172:175], a[156:159]\n" \
"ds_read_b128 v[84:87], v28 offset:0\n" \
"v_writelane_b32 v11, s101, 44\n" \
"v_mfma_f32_16x16x32_bf16 a[188:191], v[92:95], v[172:175], a[188:191]\n" \
"ds_read_b128 v[92:95], v28 offset:2048\n" \
"s_lshl_b32 s88, s98, 16\n" \
"v_mfma_f32_16x16x32_bf16 a[188:191], v[96:99], v[176:179], a[188:191]\n" \
"ds_read_b128 v[96:99], v29 offset:2048\n" \
"s_and_b32 s96, 7, s95\n" \
"v_mfma_f32_16x16x32_bf16 a[220:223], v[104:107], v[176:179], a[220:223]\n" \
"ds_read_b128 v[104:107], v29 offset:4096\n" \
"s_add_u32 s100, s99, s88\n" \
"v_mfma_f32_16x16x32_bf16 a[220:223], v[100:103], v[172:175], a[220:223]\n" \
"ds_read_b128 v[100:103], v28 offset:4096\n" \
"s_lshl_b32 s101, s96, 16\n" \
"v_mfma_f32_16x16x32_bf16 a[252:255], v[108:111], v[172:175], a[252:255]\n" \
"ds_read_b64_tr_b16 v[172:173], v35 offset:16384\n" \
"ds_read_b64_tr_b16 v[174:175], v35 offset:17408\n" \
"v_mfma_f32_16x16x32_bf16 a[252:255], v[112:115], v[176:179], a[252:255]\n" \
"s_add_u32 m0, %[mA], 0x9800\n" \
"s_waitcnt lgkmcnt(12)\n" \
"s_nop 0\n" \
"v_mfma_f32_16x16x32_bf16 a[0:3], v[120:123], v[56:59], a[0:3]\n" \
"s_add_u32 s85, s62, 0xc000\n" \
"ds_read_b64_tr_b16 v[176:177], v35 offset:24576\n" \
"v_mfma_f32_16x16x32_bf16 a[0:3], v[116:119], v[52:55], a[0:3]\n" \
"buffer_load_dwordx4 v22, s[64:67], s85 offen lds\n" \
"v_mfma_f32_16x16x32_bf16 a[32:35], v[116:119], v[60:63], a[32:35]\n" \
"s_add_u32 m0, %[mA], 0x9c00\n" \
"s_add_u32 s84, s62, 0xe000\n" \
"v_mfma_f32_16x16x32_bf16 a[32:35], v[120:123], v[64:67], a[32:35]\n" \
"ds_read_b64_tr_b16 v[178:179], v35 offset:25600\n" \
"ds_read_b128 v[108:111], v28 offset:6144\n" \
"v_mfma_f32_16x16x32_bf16 a[64:67], v[120:123], v[72:75], a[64:67]\n" \
"buffer_load_dwordx4 v23, s[64:67], s84 offen lds\n" \
"v_mfma_f32_16x16x32_bf16 a[64:67], v[116:119], v[68:71], a[64:67]\n" \
"s_add_u32 m0, %[mB], 0x8c00\n" \
"s_add_u32 s85, s63, 0x18000\n" \
"v_mfma_f32_16x16x32_bf16 a[96:99], v[116:119], v[76:79], a[96:99]\n" \
"ds_read_b128 v[112:115], v29 offset:6144\n" \
"s_add_u32 s94, 0x1800, s100\n" \
"v_mfma_f32_16x16x32_bf16 a[96:99], v[120:123], v[80:83], a[96:99]\n" \
"buffer_load_dwordx4 v27, s[68:71], s85 offen lds\n" \
"v_mfma_f32_16x16x32_bf16 a[4:7], v[128:131], v[56:59], a[4:7]\n" \
"s_add_u32 m0, %[mB], 0xcc00\n" \
"s_add_u32 s84, s63, 0x18000\n" \
"v_mfma_f32_16x16x32_bf16 a[4:7], v[124:127], v[52:55], a[4:7]\n" \
"s_add_u32 s89, s99, s101\n" \
"s_add_u32 s88, 6, s97\n" \
"v_mfma_f32_16x16x32_bf16 a[36:39], v[124:127], v[60:63], a[36:39]\n" \
"buffer_load_dwordx4 v27, s[72:75], s84 offen lds\n" \
"v_mfma_f32_16x16x32_bf16 a[36:39], v[128:131], v[64:67], a[36:39]\n" \
"v_writelane_b32 v11, s94, 45\n" \
"s_add_u32 s98, 0x2000, s89\n" \
"v_mfma_f32_16x16x32_bf16 a[68:71], v[128:131], v[72:75], a[68:71]\n" \
"s_and_b32 s100, 7, s88\n" \
"s_add_u32 s101, 7, s97\n" \
"v_mfma_f32_16x16x32_bf16 a[68:71], v[124:127], v[68:71], a[68:71]\n" \
"buffer_load_dwordx4 v[200:203], v192, s[76:79], s56 offen offset:0\n" \
"v_mfma_f32_16x16x32_bf16 a[100:103], v[124:127], v[76:79], a[100:103]\n" \
"v_writelane_b32 v11, s98, 46\n" \
"s_lshl_b32 s94, s100, 16\n" \
"v_mfma_f32_16x16x32_bf16 a[100:103], v[128:131], v[80:83], a[100:103]\n" \
"s_and_b32 s89, 7, s101\n" \
"s_add_u32 s95, s99, s94\n" \
"v_mfma_f32_16x16x32_bf16 a[8:11], v[136:139], v[56:59], a[8:11]\n" \
"s_lshl_b32 s98, s89, 16\n" \
"s_add_u32 s96, 0x2800, s95\n" \
"v_mfma_f32_16x16x32_bf16 a[8:11], v[132:135], v[52:55], a[8:11]\n" \
"s_add_u32 s88, s99, s98\n" \
"v_writelane_b32 v11, s96, 47\n" \
"v_mfma_f32_16x16x32_bf16 a[40:43], v[132:135], v[60:63], a[40:43]\n" \
"buffer_load_dwordx4 v[204:207], v193, s[76:79], s56 offen offset:0\n" \
"v_mfma_f32_16x16x32_bf16 a[40:43], v[136:139], v[64:67], a[40:43]\n" \
"s_add_u32 s100, 0x3000, s88\n" \
"v_writelane_b32 v11, s100, 48\n" \
"v_mfma_f32_16x16x32_bf16 a[72:75], v[136:139], v[72:75], a[72:75]\n" \
"s_mov_b32 s87, 0x5040100\n" \
"v_lshlrev_b32_e32 v188, 7, v218\n" \
"v_mfma_f32_16x16x32_bf16 a[72:75], v[132:135], v[68:71], a[72:75]\n" \
"v_readfirstlane_b32 %[tag], v180\n" \
"s_lshr_b32 s94, %[b], 5\n" \
"v_mfma_f32_16x16x32_bf16 a[104:107], v[132:135], v[76:79], a[104:107]\n" \
"s_add_u32 %[tag], %[tag], 0x9E3779B1\n" \
"v_mov_b32_e64 v45, %[tag]\n" \
"v_mfma_f32_16x16x32_bf16 a[104:107], v[136:139], v[80:83], a[104:107]\n" \
"s_sub_u32 s95, 15, s94\n" \
"s_and_b32 s100, 1, %[w]\n" \
"v_mfma_f32_16x16x32_bf16 a[12:15], v[144:147], v[56:59], a[12:15]\n" \
"buffer_load_dword v208, v194, s[76:79], s56 offen offset:32\n" \
"v_mfma_f32_16x16x32_bf16 a[12:15], v[140:143], v[52:55], a[12:15]\n" \
"s_cmp_lt_u32 %[w], 2\n" \
"s_cselect_b32 s96, s95, s94\n" \
"v_mfma_f32_16x16x32_bf16 a[44:47], v[140:143], v[60:63], a[44:47]\n" \
"s_lshl_b32 s97, s96, 7\n" \
"s_add_u32 s98, 0xfffffff0, s97\n" \
"v_mfma_f32_16x16x32_bf16 a[44:47], v[144:147], v[64:67], a[44:47]\n" \
"s_cmp_eq_u32 s97, 0\n" \
"s_cselect_b32 s99, 0, s98\n" \
"v_mfma_f32_16x16x32_bf16 a[76:79], v[144:147], v[72:75], a[76:79]\n" \
"v_cmp_gt_u32_e32 vcc, 32, v212\n" \
"s_lshl_b32 s101, s100, 3\n" \
"v_mfma_f32_16x16x32_bf16 a[76:79], v[140:143], v[68:71], a[76:79]\n" \
"s_add_u32 s88, s99, s101\n" \
"v_lshrrev_b32_e32 v212, 4, v20\n" \
"v_mfma_f32_16x16x32_bf16 a[108:111], v[140:143], v[76:79], a[108:111]\n" \
"buffer_load_dword v209, v194, s[76:79], s56 offen offset:1184\n" \
"v_mfma_f32_16x16x32_bf16 a[108:111], v[144:147], v[80:83], a[108:111]\n" \
"v_cndmask_b32_e32 v17, v217, v214, vcc\n" \
"v_lshrrev_b32_e32 v213, 3, v212\n" \
"v_mfma_f32_16x16x32_bf16 a[16:19], v[56:59], v[152:155], a[16:19]\n" \
"s_lshr_b32 s99, %[b], 3\n" \
"v_add_u32_e32 v214, s88, v213\n" \
"v_mfma_f32_16x16x32_bf16 a[16:19], v[52:55], v[148:151], a[16:19]\n" \
"s_lshl_b32 s96, s100, 6\n" \
"s_and_b32 s101, 3, s99\n" \
"v_mfma_f32_16x16x32_bf16 a[48:51], v[60:63], v[148:151], a[48:51]\n" \
"s_add_u32 s98, s97, s96\n" \
"s_add_u32 s88, s101, s94\n" \
"v_mfma_f32_16x16x32_bf16 a[48:51], v[64:67], v[152:155], a[48:51]\n" \
"v_add_u32_e32 v221, s98, v213\n" \
"s_add_u32 s96, 0xfffffff7, s88\n" \
"v_mfma_f32_16x16x32_bf16 a[80:83], v[72:75], v[152:155], a[80:83]\n" \
"buffer_load_dword v211, v194, s[76:79], s56 offen offset:2336\n" \
"v_mfma_f32_16x16x32_bf16 a[80:83], v[68:71], v[148:151], a[80:83]\n" \
"s_mov_b32 s76, s14\n" \
"s_and_b32 s77, s15, 0xffff\n" \
"v_mfma_f32_16x16x32_bf16 a[112:115], v[76:79], v[148:151], a[112:115]\n" \
"s_mov_b32 s78, 0x480\n" \
"s_add_u32 s56, s57, 0x1000\n" \
"v_mfma_f32_16x16x32_bf16 a[112:115], v[80:83], v[152:155], a[112:115]\n" \
"s_cmp_lt_u32 s88, 9\n" \
"s_cselect_b32 s98, s88, s96\n" \
"v_mfma_f32_16x16x32_bf16 a[20:23], v[56:59], v[160:163], a[20:23]\n" \
"s_add_u32 s88, s97, s100\n" \
"s_lshl_b32 s89, s100, 2\n" \
"v_mfma_f32_16x16x32_bf16 a[20:23], v[52:55], v[156:159], a[20:23]\n" \
"s_lshl_b32 s23, s98, 7\n" \
"s_lshr_b32 s96, s97, 4\n" \
"v_mfma_f32_16x16x32_bf16 a[52:55], v[60:63], v[156:159], a[52:55]\n" \
"buffer_load_ushort v210, v195, s[76:79], 0 offen\n" \
"v_mfma_f32_16x16x32_bf16 a[52:55], v[64:67], v[160:163], a[52:55]\n" \
"s_cmp_eq_u32 s88, 0\n" \
"s_cselect_b32 s49, 0xff800000, 0\n" \
"v_mfma_f32_16x16x32_bf16 a[84:87], v[72:75], v[160:163], a[84:87]\n" \
"v_lshrrev_b32_e32 v217, 4, v212\n" \
"s_add_u32 s98, s96, s89\n" \
"v_mfma_f32_16x16x32_bf16 a[84:87], v[68:71], v[156:159], a[84:87]\n" \
"v_add_u32_e64 v218, s89, v217\n" \
"s_mul_i32 s88, 0x24800, s98\n" \
"v_mfma_f32_16x16x32_bf16 a[116:119], v[76:79], v[156:159], a[116:119]\n" \
"s_and_b32 s96, 7, %[b]\n" \
"s_add_u32 s97, 0x3000000, s88\n" \
"v_mfma_f32_16x16x32_bf16 a[116:119], v[80:83], v[160:163], a[116:119]\n" \
"s_lshl_b32 s89, s96, 8\n" \
"s_waitcnt lgkmcnt(0)\n" \
"v_mfma_f32_16x16x32_bf16 a[24:27], v[56:59], v[168:171], a[24:27]\n" \
"s_add_u32 s98, s97, s89\n" \
"s_lshl_b32 s88, s101, 2\n" \
"v_mfma_f32_16x16x32_bf16 a[24:27], v[52:55], v[164:167], a[24:27]\n" \
"v_and_b32_e32 v216, 7, v212\n" \
"s_add_u32 s89, s98, s88\n" \
"v_mfma_f32_16x16x32_bf16 a[56:59], v[60:63], v[164:167], a[56:59]\n" \
"v_xor_b32_e32 v219, v216, v218\n" \
"s_lshl_b32 s98, s96, 2\n" \
"v_mfma_f32_16x16x32_bf16 a[56:59], v[64:67], v[168:171], a[56:59]\n" \
"v_mul_u32_u24_e32 v215, 0x480, v214\n" \
"v_lshlrev_b32_e32 v220, 4, v219\n" \
"v_mfma_f32_16x16x32_bf16 a[88:91], v[72:75], v[168:171], a[88:91]\n" \
"v_lshrrev_b32_e32 v229, 2, v212\n" \
"v_and_b32_e32 v234, 3, v212\n" \
"v_mfma_f32_16x16x32_bf16 a[88:91], v[68:71], v[164:167], a[88:91]\n" \
"v_and_b32_e64 v249, 2, v212\n" \
"s_add_u32 s51, 0x4000, s89\n" \
"v_mfma_f32_16x16x32_bf16 a[120:123], v[76:79], v[164:167], a[120:123]\n" \
"s_add_u32 s88, s98, s101\n" \
"v_add_u32_e32 v21, v215, v220\n" \
"v_mfma_f32_16x16x32_bf16 a[120:123], v[80:83], v[168:171], a[120:123]\n" \
"v_add_u32_e32 v225, 4, v217\n" \
"s_lshl_b32 s89, s88, 9\n" \
"v_mfma_f32_16x16x32_bf16 a[28:31], v[56:59], v[176:179], a[28:31]\n" \
"s_add_u32 s62, s22, 0x200\n" \
"s_and_b32 s62, s62, 0x3ff\n" \
"v_mfma_f32_16x16x32_bf16 a[28:31], v[52:55], v[172:175], a[28:31]\n" \
"s_lshl_b32 s63, s62, 12\n" \
"s_lshl_b32 s98, s99, 2\n" \
"v_mfma_f32_16x16x32_bf16 a[60:63], v[60:63], v[172:175], a[60:63]\n" \
"v_lshrrev_b32_e32 v212, 4, v20\n" \
"v_xor_b32_e32 v223, v216, v217\n" \
"v_mfma_f32_16x16x32_bf16 a[60:63], v[64:67], v[176:179], a[60:63]\n" \
"v_xor_b32_e64 v226, v216, v225\n" \
"v_mov_b32_e32 v250, 0xeeeeeeee\n" \
"v_mfma_f32_16x16x32_bf16 a[92:95], v[72:75], v[176:179], a[92:95]\n" \
"v_mov_b32_e32 v251, 0x44444444\n" \
"v_cmp_ne_u32_e32 vcc, 0, v249\n" \
"v_mfma_f32_16x16x32_bf16 a[92:95], v[68:71], v[172:175], a[92:95]\n" \
"s_add_u32 s50, s97, s89\n" \
"s_lshl_b32 s96, %[w], 10\n" \
"v_mfma_f32_16x16x32_bf16 a[124:127], v[76:79], v[172:175], a[124:127]\n" \
"s_add_u32 s101, s98, %[w]\n" \
"v_and_b32_e64 v215, 15, v212\n" \
"v_mfma_f32_16x16x32_bf16 a[124:127], v[80:83], v[176:179], a[124:127]\n" \
"v_cndmask_b32_e32 v50, v251, v250, vcc\n" \
"s_add_u32 s40, 0x14000, s96\n" \
"L_pf1:\n" \
"s_cbranch_execz L_pf5\n" \
"s_nop 0\n" \
"s_waitcnt vmcnt(6)\n" \
"s_barrier\n" \
"v_mfma_f32_16x16x32_bf16 a[144:147], v[88:91], v[152:155], a[144:147]\n" \
"s_add_u32 m0, %[mA], 0x0\n" \
"s_add_u32 s85, s62, 0x0\n" \
"s_nop 0\n" \
"v_mfma_f32_16x16x32_bf16 a[144:147], v[84:87], v[148:151], a[144:147]\n" \
"buffer_load_dwordx4 v22, s[64:67], s85 offen lds\n" \
"v_mfma_f32_16x16x32_bf16 a[176:179], v[92:95], v[148:151], a[176:179]\n" \
"ds_read_b128 v[56:59], v29 offset:49152\n" \
"ds_read_b128 v[52:55], v28 offset:49152\n" \
"v_mfma_f32_16x16x32_bf16 a[176:179], v[96:99], v[152:155], a[176:179]\n" \
"s_add_u32 m0, %[mA], 0x400\n" \
"s_add_u32 s84, s62, 0x2000\n" \
"v_mfma_f32_16x16x32_bf16 a[208:211], v[104:107], v[152:155], a[208:211]\n" \
"buffer_load_dwordx4 v23, s[64:67], s84 offen lds\n" \
"v_mfma_f32_16x16x32_bf16 a[208:211], v[100:103], v[148:151], a[208:211]\n" \
"ds_read_b128 v[60:63], v28 offset:51200\n" \
"ds_read_b128 v[64:67], v29 offset:51200\n" \
"v_mfma_f32_16x16x32_bf16 a[240:243], v[108:111], v[148:151], a[240:243]\n" \
"ds_read_b64_tr_b16 v[148:149], v32 offset:49152\n" \
"ds_read_b64_tr_b16 v[150:151], v32 offset:50176\n" \
"v_mfma_f32_16x16x32_bf16 a[240:243], v[112:115], v[152:155], a[240:243]\n" \
"s_add_u32 m0, %[mB], 0x0\n" \
"s_add_u32 s85, s63, 0x0\n" \
"v_mfma_f32_16x16x32_bf16 a[148:151], v[88:91], v[160:163], a[148:151]\n" \
"buffer_load_dwordx4 v26, s[68:71], s85 offen lds\n" \
"v_mfma_f32_16x16x32_bf16 a[148:151], v[84:87], v[156:159], a[148:151]\n" \
"ds_read_b64_tr_b16 v[152:153], v32 offset:57344\n" \
"ds_read_b64_tr_b16 v[154:155], v32 offset:58368\n" \
"v_mfma_f32_16x16x32_bf16 a[180:183], v[92:95], v[156:159], a[180:183]\n" \
"ds_read_b128 v[72:75], v29 offset:53248\n" \
"ds_read_b128 v[68:71], v28 offset:53248\n" \
"v_mfma_f32_16x16x32_bf16 a[180:183], v[96:99], v[160:163], a[180:183]\n" \
"s_add_u32 m0, %[mB], 0x4000\n" \
"s_add_u32 s84, s63, 0x0\n" \
"s_nop 0\n" \
"v_mfma_f32_16x16x32_bf16 a[212:215], v[104:107], v[160:163], a[212:215]\n" \
"buffer_load_dwordx4 v26, s[72:75], s84 offen lds\n" \
"v_mfma_f32_16x16x32_bf16 a[212:215], v[100:103], v[156:159], a[212:215]\n" \
"ds_read_b128 v[76:79], v28 offset:55296\n" \
"ds_read_b128 v[80:83], v29 offset:55296\n" \
"v_mfma_f32_16x16x32_bf16 a[244:247], v[108:111], v[156:159], a[244:247]\n" \
"ds_read_b64_tr_b16 v[156:157], v33 offset:49152\n" \
"ds_read_b64_tr_b16 v[158:159], v33 offset:50176\n" \
"v_mfma_f32_16x16x32_bf16 a[244:247], v[112:115], v[160:163], a[244:247]\n" \
"s_add_u32 m0, %[mA], 0x800\n" \
"s_add_u32 s85, s62, 0x4000\n" \
"v_mfma_f32_16x16x32_bf16 a[152:155], v[88:91], v[168:171], a[152:155]\n" \
"buffer_load_dwordx4 v22, s[64:67], s85 offen lds\n" \
"v_mfma_f32_16x16x32_bf16 a[152:155], v[84:87], v[164:167], a[152:155]\n" \
"ds_read_b64_tr_b16 v[160:161], v33 offset:57344\n" \
"ds_read_b64_tr_b16 v[162:163], v33 offset:58368\n" \
"v_mfma_f32_16x16x32_bf16 a[184:187], v[92:95], v[164:167], a[184:187]\n" \
"s_lshl_b32 s41, s101, 7\n" \
"s_lshl_b32 s97, s88, 20\n" \
"v_mfma_f32_16x16x32_bf16 a[184:187], v[96:99], v[168:171], a[184:187]\n" \
"s_add_u32 m0, %[mA], 0xc00\n" \
"s_add_u32 s84, s62, 0x6000\n" \
"v_mfma_f32_16x16x32_bf16 a[216:219], v[104:107], v[168:171], a[216:219]\n" \
"buffer_load_dwordx4 v23, s[64:67], s84 offen lds\n" \
"v_mfma_f32_16x16x32_bf16 a[216:219], v[100:103], v[164:167], a[216:219]\n" \
"v_lshrrev_b32_e32 v213, 4, v212\n" \
"v_and_b32_e32 v216, 1, v215\n" \
"v_mfma_f32_16x16x32_bf16 a[248:251], v[108:111], v[164:167], a[248:251]\n" \
"ds_read_b64_tr_b16 v[164:165], v34 offset:49152\n" \
"ds_read_b64_tr_b16 v[166:167], v34 offset:50176\n" \
"v_mfma_f32_16x16x32_bf16 a[248:251], v[112:115], v[168:171], a[248:251]\n" \
"s_add_u32 m0, %[mB], 0x400\n" \
"s_add_u32 s85, s63, 0x8000\n" \
"v_mfma_f32_16x16x32_bf16 a[156:159], v[88:91], v[176:179], a[156:159]\n" \
"buffer_load_dwordx4 v26, s[68:71], s85 offen lds\n" \
"v_mfma_f32_16x16x32_bf16 a[156:159], v[84:87], v[172:175], a[156:159]\n" \
"ds_read_b64_tr_b16 v[168:169], v34 offset:57344\n" \
"ds_read_b64_tr_b16 v[170:171], v34 offset:58368\n" \
"v_mfma_f32_16x16x32_bf16 a[188:191], v[92:95], v[172:175], a[188:191]\n" \
"v_lshlrev_b32_e32 v228, 3, v217\n" \
"v_and_b32_e32 v238, 1, v217\n" \
"v_mfma_f32_16x16x32_bf16 a[188:191], v[96:99], v[176:179], a[188:191]\n" \
"s_add_u32 m0, %[mB], 0x4400\n" \
"s_add_u32 s84, s63, 0x8000\n" \
"v_mfma_f32_16x16x32_bf16 a[220:223], v[104:107], v[176:179], a[220:223]\n" \
"buffer_load_dwordx4 v26, s[72:75], s84 offen lds\n" \
"v_mfma_f32_16x16x32_bf16 a[220:223], v[100:103], v[172:175], a[220:223]\n" \
"v_lshlrev_b32_e32 v242, 4, v217\n" \
"s_nop 0\n" \
"s_add_u32 s89, 0x1000000, s97\n" \
"v_mfma_f32_16x16x32_bf16 a[252:255], v[108:111], v[172:175], a[252:255]\n" \
"ds_read_b64_tr_b16 v[172:173], v35 offset:49152\n" \
"ds_read_b64_tr_b16 v[174:175], v35 offset:50176\n" \
"v_mfma_f32_16x16x32_bf16 a[252:255], v[112:115], v[176:179], a[252:255]\n" \
"s_add_u32 m0, %[mA], 0x1000\n" \
"s_add_u32 s85, s62, 0x8000\n" \
"v_mfma_f32_16x16x32_bf16 a[128:131], v[120:123], v[88:91], a[128:131]\n" \
"buffer_load_dwordx4 v22, s[64:67], s85 offen lds\n" \
"v_mfma_f32_16x16x32_bf16 a[128:131], v[116:119], v[84:87], a[128:131]\n" \
"ds_read_b64_tr_b16 v[176:177], v35 offset:57344\n" \
"ds_read_b64_tr_b16 v[178:179], v35 offset:58368\n" \
"v_mfma_f32_16x16x32_bf16 a[160:163], v[116:119], v[92:95], a[160:163]\n" \
"s_lshl_b32 s96, %[w], 12\n" \
"s_lshr_b32 s101, %[w], 1\n" \
"v_mfma_f32_16x16x32_bf16 a[160:163], v[120:123], v[96:99], a[160:163]\n" \
"s_add_u32 m0, %[mA], 0x1400\n" \
"s_add_u32 s84, s62, 0xa000\n" \
"v_mfma_f32_16x16x32_bf16 a[192:195], v[120:123], v[104:107], a[192:195]\n" \
"buffer_load_dwordx4 v23, s[64:67], s84 offen lds\n" \
"v_mfma_f32_16x16x32_bf16 a[192:195], v[116:119], v[100:103], a[192:195]\n" \
"v_lshlrev_b32_e32 v214, 2, v213\n" \
"s_nop 0\n" \
"v_mov_b32_e32 v218, 0x1000c0c\n" \
"v_mfma_f32_16x16x32_bf16 a[224:227], v[116:119], v[108:111], a[224:227]\n" \
"ds_read_b64_tr_b16 v[116:117], v32 offset:32768\n" \
"ds_read_b64_tr_b16 v[118:119], v32 offset:33792\n" \
"v_mfma_f32_16x16x32_bf16 a[224:227], v[120:123], v[112:115], a[224:227]\n" \
"s_add_u32 m0, %[mB], 0x800\n" \
"s_add_u32 s85, s63, 0x10000\n" \
"v_mfma_f32_16x16x32_bf16 a[132:135], v[128:131], v[88:91], a[132:135]\n" \
"buffer_load_dwordx4 v27, s[68:71], s85 offen lds\n" \
"v_mfma_f32_16x16x32_bf16 a[132:135], v[124:127], v[84:87], a[132:135]\n" \
"ds_read_b64_tr_b16 v[120:121], v32 offset:40960\n" \
"ds_read_b64_tr_b16 v[122:123], v32 offset:41984\n" \
"v_mfma_f32_16x16x32_bf16 a[164:167], v[124:127], v[92:95], a[164:167]\n" \
"v_mov_b32_e32 v219, 0xc0c0100\n" \
"v_cmp_ne_u32_e32 vcc, 0, v216\n" \
"v_mfma_f32_16x16x32_bf16 a[164:167], v[128:131], v[96:99], a[164:167]\n" \
"s_add_u32 m0, %[mB], 0x4800\n" \
"s_add_u32 s84, s63, 0x10000\n" \
"v_mfma_f32_16x16x32_bf16 a[196:199], v[128:131], v[104:107], a[196:199]\n" \
"buffer_load_dwordx4 v27, s[72:75], s84 offen lds\n" \
"v_mfma_f32_16x16x32_bf16 a[196:199], v[124:127], v[100:103], a[196:199]\n" \
"v_mul_u32_u24_e32 v222, 0x480, v221\n" \
"s_add_u32 s52, s89, s96\n" \
"v_mfma_f32_16x16x32_bf16 a[228:231], v[124:127], v[108:111], a[228:231]\n" \
"ds_read_b64_tr_b16 v[124:125], v33 offset:32768\n" \
"ds_read_b64_tr_b16 v[126:127], v33 offset:33792\n" \
"v_mfma_f32_16x16x32_bf16 a[228:231], v[128:131], v[112:115], a[228:231]\n" \
"ds_read_b64_tr_b16 v[128:129], v33 offset:40960\n" \
"ds_read_b64_tr_b16 v[130:131], v33 offset:41984\n" \
"v_mfma_f32_16x16x32_bf16 a[136:139], v[136:139], v[88:91], a[136:139]\n" \
"s_lshl_b32 s99, %[w], 11\n" \
"s_lshl_b32 s88, s101, 15\n" \
"v_mfma_f32_16x16x32_bf16 a[136:139], v[132:135], v[84:87], a[136:139]\n" \
"v_add_u32_e32 v217, v214, v216\n" \
"v_cndmask_b32_e32 v220, v219, v218, vcc\n" \
"v_mfma_f32_16x16x32_bf16 a[168:171], v[132:135], v[92:95], a[168:171]\n" \
"v_lshlrev_b32_e32 v224, 4, v223\n" \
"s_add_u32 s98, s89, s99\n" \
"v_mfma_f32_16x16x32_bf16 a[168:171], v[136:139], v[96:99], a[168:171]\n" \
"s_add_u32 s97, s89, s88\n" \
"s_lshl_b32 s96, s100, 11\n" \
"v_mfma_f32_16x16x32_bf16 a[200:203], v[136:139], v[104:107], a[200:203]\n" \
"v_mov_b32_e32 v221, 0xc0c0c0c\n" \
"v_cmp_le_u32_e32 vcc, v217, v215\n" \
"v_mfma_f32_16x16x32_bf16 a[200:203], v[132:135], v[100:103], a[200:203]\n" \
"v_add_u32_e32 v24, v222, v224\n" \
"v_lshlrev_b32_e32 v227, 4, v226\n" \
"v_mfma_f32_16x16x32_bf16 a[232:235], v[132:135], v[108:111], a[232:235]\n" \
"ds_read_b64_tr_b16 v[132:133], v34 offset:32768\n" \
"ds_read_b64_tr_b16 v[134:135], v34 offset:33792\n" \
"v_mfma_f32_16x16x32_bf16 a[232:235], v[136:139], v[112:115], a[232:235]\n" \
"ds_read_b64_tr_b16 v[136:137], v34 offset:40960\n" \
"ds_read_b64_tr_b16 v[138:139], v34 offset:41984\n" \
"v_mfma_f32_16x16x32_bf16 a[140:143], v[144:147], v[88:91], a[140:143]\n" \
"ds_read_b128 v[88:91], v29 offset:32768\n" \
"s_add_u32 s99, s97, s96\n" \
"v_mfma_f32_16x16x32_bf16 a[140:143], v[140:143], v[84:87], a[140:143]\n" \
"ds_read_b128 v[84:87], v28 offset:32768\n" \
"s_lshl_b32 s101, s95, 16\n" \
"v_mfma_f32_16x16x32_bf16 a[172:175], v[140:143], v[92:95], a[172:175]\n" \
"ds_read_b128 v[92:95], v28 offset:34816\n" \
"v_cndmask_b32_e32 v40, v220, v221, vcc\n" \
"v_mfma_f32_16x16x32_bf16 a[172:175], v[144:147], v[96:99], a[172:175]\n" \
"ds_read_b128 v[96:99], v29 offset:34816\n" \
"v_add_u32_e64 v25, v222, v227\n" \
"v_mfma_f32_16x16x32_bf16 a[204:207], v[144:147], v[104:107], a[204:207]\n" \
"ds_read_b128 v[104:107], v29 offset:36864\n" \
"s_add_u32 s54, 0x10000, s98\n" \
"v_mfma_f32_16x16x32_bf16 a[204:207], v[140:143], v[100:103], a[204:207]\n" \
"ds_read_b128 v[100:103], v28 offset:36864\n" \
"s_add_u32 s43, s99, s101\n" \
"v_mfma_f32_16x16x32_bf16 a[236:239], v[140:143], v[108:111], a[236:239]\n" \
"ds_read_b64_tr_b16 v[140:141], v35 offset:32768\n" \
"ds_read_b64_tr_b16 v[142:143], v35 offset:33792\n" \
"v_mfma_f32_16x16x32_bf16 a[236:239], v[144:147], v[112:115], a[236:239]\n" \
"s_add_u32 m0, %[mA], 0x1800\n" \
"s_waitcnt lgkmcnt(12)\n" \
"v_mfma_f32_16x16x32_bf16 a[16:19], v[56:59], v[152:155], a[16:19]\n" \
"s_add_u32 s85, s62, 0xc000\n" \
"ds_read_b64_tr_b16 v[144:145], v35 offset:40960\n" \
"v_mfma_f32_16x16x32_bf16 a[16:19], v[52:55], v[148:151], a[16:19]\n" \
"buffer_load_dwordx4 v22, s[64:67], s85 offen lds\n" \
"v_mfma_f32_16x16x32_bf16 a[48:51], v[60:63], v[148:151], a[48:51]\n" \
"s_add_u32 m0, %[mA], 0x1c00\n" \
"s_add_u32 s84, s62, 0xe000\n" \
"v_mfma_f32_16x16x32_bf16 a[48:51], v[64:67], v[152:155], a[48:51]\n" \
"ds_read_b64_tr_b16 v[146:147], v35 offset:41984\n" \
"ds_read_b128 v[108:111], v28 offset:38912\n" \
"v_mfma_f32_16x16x32_bf16 a[80:83], v[72:75], v[152:155], a[80:83]\n" \
"buffer_load_dwordx4 v23, s[64:67], s84 offen lds\n" \
"v_mfma_f32_16x16x32_bf16 a[80:83], v[68:71], v[148:151], a[80:83]\n" \
"s_add_u32 m0, %[mB], 0xc00\n" \
"s_add_u32 s85, s63, 0x18000\n" \
"v_mfma_f32_16x16x32_bf16 a[112:115], v[76:79], v[148:151], a[112:115]\n" \
"ds_read_b128 v[112:115], v29 offset:38912\n" \
"s_mul_i32 s89, 0x1800, s100\n" \
"v_mfma_f32_16x16x32_bf16 a[112:115], v[80:83], v[152:155], a[112:115]\n" \
"buffer_load_dwordx4 v27, s[68:71], s85 offen lds\n" \
"v_mfma_f32_16x16x32_bf16 a[20:23], v[56:59], v[160:163], a[20:23]\n" \
"s_add_u32 m0, %[mB], 0x4c00\n" \
"s_add_u32 s84, s63, 0x18000\n" \
"v_mfma_f32_16x16x32_bf16 a[20:23], v[52:55], v[156:159], a[20:23]\n" \
"v_mov_b32_e32 v223, 0x3020c0c\n" \
"v_mov_b32_e32 v224, 0xc0c0302\n" \
"v_mfma_f32_16x16x32_bf16 a[52:55], v[60:63], v[156:159], a[52:55]\n" \
"buffer_load_dwordx4 v27, s[72:75], s84 offen lds\n" \
"v_mfma_f32_16x16x32_bf16 a[52:55], v[64:67], v[160:163], a[52:55]\n" \
"s_mov_b32 m0, %[mS]\n" \
"v_cmp_ne_u32_e32 vcc, 0, v216\n" \
"v_mfma_f32_16x16x32_bf16 a[84:87], v[72:75], v[160:163], a[84:87]\n" \
"s_lshl_b32 s98, s94, 16\n" \
"s_add_u32 s97, s43, s89\n" \
"v_mfma_f32_16x16x32_bf16 a[84:87], v[68:71], v[156:159], a[84:87]\n" \
"v_add_u32_e32 v222, 2, v217\n" \
"v_cndmask_b32_e32 v225, v224, v223, vcc\n" \
"v_mfma_f32_16x16x32_bf16 a[116:119], v[76:79], v[156:159], a[116:119]\n" \
"s_add_u32 s42, s99, s98\n" \
"s_add_u32 s45, 0x4000, s97\n" \
"v_mfma_f32_16x16x32_bf16 a[116:119], v[80:83], v[160:163], a[116:119]\n" \
"s_lshr_b32 s94, %[w], 1\n" \
"s_and_b32 s95, 1, %[w]\n" \
"v_mfma_f32_16x16x32_bf16 a[24:27], v[56:59], v[168:171], a[24:27]\n" \
"v_mov_b32_e32 v226, 0xc0c0c0c\n" \
"v_cmp_le_u32_e32 vcc, v222, v215\n" \
"v_mfma_f32_16x16x32_bf16 a[24:27], v[52:55], v[164:167], a[24:27]\n" \
"s_add_u32 s88, s42, s89\n" \
"s_add_u32 s96, s94, s95\n" \
"v_mfma_f32_16x16x32_bf16 a[56:59], v[60:63], v[164:167], a[56:59]\n" \
"s_add_u32 s97, 1, s94\n" \
"v_cndmask_b32_e32 v41, v225, v226, vcc\n" \
"v_mfma_f32_16x16x32_bf16 a[56:59], v[64:67], v[168:171], a[56:59]\n" \
"s_waitcnt vmcnt(18)\n" \
"v_perm_b32 v252, v209, v208, s87\n" \
"v_mfma_f32_16x16x32_bf16 a[88:91], v[72:75], v[168:171], a[88:91]\n" \
"s_mov_b32 s87, 0x7060302\n" \
"v_perm_b32 v253, v209, v208, s87\n" \
"v_mfma_f32_16x16x32_bf16 a[88:91], v[68:71], v[164:167], a[88:91]\n" \
"s_mov_b32 s87, 0x5040100\n" \
"s_waitcnt vmcnt(16)\n" \
"v_perm_b32 v254, v211, v210, s87\n" \
"s_mov_b32 exec_lo, 0x15\n" \
"s_mov_b32 exec_hi, 0x180000\n" \
"buffer_load_dword v11, s[80:83], 0 offen sc1 lds\n" \
"s_mov_b64 exec, -1\n" \
"v_mfma_f32_16x16x32_bf16 a[120:123], v[76:79], v[164:167], a[120:123]\n" \
"s_mov_b32 s87, 0x7060100\n" \
"v_perm_b32 v255, v211, v210, s87\n" \
"v_mfma_f32_16x16x32_bf16 a[120:123], v[80:83], v[168:171], a[120:123]\n" \
"s_mov_b32 m0, %[mS]\n" \
"s_mov_b32 s86, s10\n" \
"v_mfma_f32_16x16x32_bf16 a[28:31], v[56:59], v[176:179], a[28:31]\n" \
"s_mov_b32 s87, s11\n" \
"ds_write2_b32 v198, v252, v253 offset0:0 offset1:2\n" \
"v_mfma_f32_16x16x32_bf16 a[28:31], v[52:55], v[172:175], a[28:31]\n" \
"ds_write_b128 v196, v[200:203]\n" \
"v_mfma_f32_16x16x32_bf16 a[60:63], v[60:63], v[172:175], a[60:63]\n" \
"ds_write2_b32 v198, v254, v255 offset0:4 offset1:6\n" \
"v_and_b32_e32 v230, 3, v229\n" \
"s_mov_b32 exec_lo, 0x3ffff\n" \
"s_mov_b32 exec_hi, 0x0\n" \
"global_load_lds_dword v188, s[86:87]\n" \
"s_mov_b64 exec, -1\n" \
"v_mfma_f32_16x16x32_bf16 a[60:63], v[64:67], v[176:179], a[60:63]\n" \
"ds_write_b128 v197, v[204:207]\n" \
"v_mfma_f32_16x16x32_bf16 a[92:95], v[72:75], v[176:179], a[92:95]\n" \
"s_add_u32 s44, 0x4000, s88\n" \
"s_lshl_b32 s59, s96, 13\n" \
"v_mfma_f32_16x16x32_bf16 a[92:95], v[68:71], v[172:175], a[92:95]\n" \
"s_sub_u32 s98, s97, s95\n" \
"s_lshl_b32 s99, s94, 13\n" \
"v_mfma_f32_16x16x32_bf16 a[124:127], v[76:79], v[172:175], a[124:127]\n" \
"s_mul_i32 s101, 0x1800, s94\n" \
"v_mov_b32_e32 v227, 0xc0c0c0c\n" \
"v_mfma_f32_16x16x32_bf16 a[124:127], v[80:83], v[176:179], a[124:127]\n" \
"v_cmp_le_u32_e32 vcc, v217, v215\n" \
"v_add_u32_e64 v231, v228, v230\n" \
"v_mfma_f32_16x16x32_bf16 a[0:3], v[120:123], v[56:59], a[0:3]\n" \
"s_lshl_b32 s60, s98, 13\n" \
"s_add_u32 s100, 0x1800, s99\n" \
"v_mfma_f32_16x16x32_bf16 a[0:3], v[116:119], v[52:55], a[0:3]\n" \
"s_sub_u32 s88, 0x17000, s101\n" \
"s_lshl_b32 s96, s94, 15\n" \
"v_mfma_f32_16x16x32_bf16 a[32:35], v[116:119], v[60:63], a[32:35]\n" \
"v_cndmask_b32_e32 v42, v227, v220, vcc\n" \
"v_lshlrev_b32_e32 v232, 7, v231\n" \
"v_mfma_f32_16x16x32_bf16 a[32:35], v[120:123], v[64:67], a[32:35]\n" \
"s_cmp_lg_u32 s95, 0\n" \
"s_cselect_b32 s61, s100, s88\n" \
"v_mfma_f32_16x16x32_bf16 a[64:67], v[120:123], v[72:75], a[64:67]\n" \
"s_add_u32 s97, 0x10000, s96\n" \
"s_lshl_b32 s98, s95, 11\n" \
"v_mfma_f32_16x16x32_bf16 a[64:67], v[116:119], v[68:71], a[64:67]\n" \
"v_mov_b32_e32 v228, 0xc0c0c0c\n" \
"v_cmp_le_u32_e32 vcc, v222, v215\n" \
"v_mfma_f32_16x16x32_bf16 a[96:99], v[116:119], v[76:79], a[96:99]\n" \
"v_add_u32_e32 v233, 0x14000, v232\n" \
"v_lshlrev_b32_e32 v235, 3, v234\n" \
"v_mfma_f32_16x16x32_bf16 a[96:99], v[120:123], v[80:83], a[96:99]\n" \
"s_add_u32 s101, s97, s98\n" \
"v_cndmask_b32_e32 v43, v228, v225, vcc\n" \
"v_mfma_f32_16x16x32_bf16 a[4:7], v[128:131], v[56:59], a[4:7]\n" \
"v_and_b32_e32 v231, 2, v212\n" \
"v_add_u32_e64 v236, v233, v235\n" \
"v_mfma_f32_16x16x32_bf16 a[4:7], v[124:127], v[52:55], a[4:7]\n" \
"v_lshrrev_b32_e32 v237, 1, v230\n" \
"v_add_u32_e32 v243, v242, v230\n" \
"v_mfma_f32_16x16x32_bf16 a[36:39], v[124:127], v[60:63], a[36:39]\n" \
"s_add_u32 s100, s101, s99\n" \
"v_and_b32_e32 v229, 3, v212\n" \
"v_mfma_f32_16x16x32_bf16 a[36:39], v[128:131], v[64:67], a[36:39]\n" \
"v_mov_b32_e32 v232, 0x3f80\n" \
"v_cmp_eq_u32_e32 vcc, 0, v231\n" \
"v_mfma_f32_16x16x32_bf16 a[68:71], v[128:131], v[72:75], a[68:71]\n" \
"v_lshlrev_b32_e32 v239, 1, v238\n" \
"v_lshlrev_b32_e32 v244, 7, v243\n" \
"v_mfma_f32_16x16x32_bf16 a[68:71], v[124:127], v[68:71], a[68:71]\n" \
"s_add_u32 s88, 0x4000, s100\n" \
"v_or_b32_e32 v230, 2, v229\n" \
"v_mfma_f32_16x16x32_bf16 a[100:103], v[124:127], v[76:79], a[100:103]\n" \
"v_cndmask_b32_e32 v233, 0, v232, vcc\n" \
"v_or_b32_e32 v240, v237, v239\n" \
"v_mfma_f32_16x16x32_bf16 a[100:103], v[128:131], v[80:83], a[100:103]\n" \
"v_add_u32_e32 v245, 0x14000, v244\n" \
"s_waitcnt lgkmcnt(0)\n" \
"v_mfma_f32_16x16x32_bf16 a[8:11], v[136:139], v[56:59], a[8:11]\n" \
"s_cmp_lg_u32 s95, 0\n" \
"s_cselect_b32 s47, s88, s101\n" \
"v_mfma_f32_16x16x32_bf16 a[8:11], v[132:135], v[52:55], a[8:11]\n" \
"v_cmp_eq_u32_e32 vcc, v230, v213\n" \
"v_lshlrev_b32_e32 v241, 5, v240\n" \
"v_mfma_f32_16x16x32_bf16 a[40:43], v[132:135], v[60:63], a[40:43]\n" \
"v_add_u32_e32 v246, v245, v235\n" \
"v_cndmask_b32_e32 v46, 0, v233, vcc\n" \
"v_mfma_f32_16x16x32_bf16 a[40:43], v[136:139], v[64:67], a[40:43]\n" \
"v_and_b32_e32 v234, 51, v212\n" \
"v_add_u32_e64 v190, v236, v241\n" \
"v_mfma_f32_16x16x32_bf16 a[72:75], v[136:139], v[72:75], a[72:75]\n" \
"v_mov_b32_e32 v235, 0x3f80\n" \
"v_cmp_eq_u32_e32 vcc, 2, v234\n" \
"v_mfma_f32_16x16x32_bf16 a[72:75], v[132:135], v[68:71], a[72:75]\n" \
"s_lshl_b32 s88, s95, 4\n" \
"v_xor_b32_e64 v247, 3, v237\n" \
"v_mfma_f32_16x16x32_bf16 a[104:107], v[132:135], v[76:79], a[104:107]\n" \
"v_cndmask_b32_e32 v47, 0, v235, vcc\n" \
"v_add_u32_e32 v236, s88, v215\n" \
"v_mfma_f32_16x16x32_bf16 a[104:107], v[136:139], v[80:83], a[104:107]\n" \
"v_mov_b32_e32 v237, 0xffff\n" \
"v_cmp_le_u32_e32 vcc, v214, v236\n" \
"v_mfma_f32_16x16x32_bf16 a[12:15], v[144:147], v[56:59], a[12:15]\n" \
"s_add_u32 s62, s22, 0x280\n" \
"s_and_b32 s62, s62, 0x3ff\n" \
"v_mfma_f32_16x16x32_bf16 a[12:15], v[140:143], v[52:55], a[12:15]\n" \
"s_lshl_b32 s63, s62, 12\n" \
"v_cndmask_b32_e32 v238, 0, v237, vcc\n" \
"v_mfma_f32_16x16x32_bf16 a[44:47], v[140:143], v[60:63], a[44:47]\n" \
"v_mov_b32_e32 v239, 0xffff0000\n" \
"v_cmp_lt_u32_e32 vcc, v214, v236\n" \
"v_mfma_f32_16x16x32_bf16 a[44:47], v[144:147], v[64:67], a[44:47]\n" \
"v_add_u32_e32 v241, 2, v214\n" \
"v_cndmask_b32_e32 v240, 0, v239, vcc\n" \
"v_mfma_f32_16x16x32_bf16 a[76:79], v[144:147], v[72:75], a[76:79]\n" \
"v_mov_b32_e32 v242, 0xffff\n" \
"v_cmp_le_u32_e32 vcc, v241, v236\n" \
"v_mfma_f32_16x16x32_bf16 a[76:79], v[140:143], v[68:71], a[76:79]\n" \
"v_lshlrev_b32_e32 v248, 5, v247\n" \
"v_cndmask_b32_e32 v243, 0, v242, vcc\n" \
"v_mfma_f32_16x16x32_bf16 a[108:111], v[140:143], v[76:79], a[108:111]\n" \
"v_add_u32_e32 v39, v246, v248\n" \
"v_mov_b32_e32 v244, 0xffff0000\n" \
"v_mfma_f32_16x16x32_bf16 a[108:111], v[144:147], v[80:83], a[108:111]\n" \
"v_cmp_lt_u32_e32 vcc, v241, v236\n" \
"v_add_u32_e32 v246, 16, v214\n" \
"s_waitcnt vmcnt(2)\n" \
"s_barrier\n" \
"v_mfma_f32_16x16x32_bf16 a[128:131], v[120:123], v[88:91], a[128:131]\n" \
"s_add_u32 m0, %[mA], 0x8000\n" \
"s_add_u32 s85, s62, 0x0\n" \
"s_nop 0\n" \
"v_mfma_f32_16x16x32_bf16 a[128:131], v[116:119], v[84:87], a[128:131]\n" \
"buffer_load_dwordx4 v22, s[64:67], s85 offen lds\n" \
"v_mfma_f32_16x16x32_bf16 a[160:163], v[116:119], v[92:95], a[160:163]\n" \
"ds_read_b128 v[56:59], v29 offset:16384\n" \
"ds_read_b128 v[52:55], v28 offset:16384\n" \
"v_mfma_f32_16x16x32_bf16 a[160:163], v[120:123], v[96:99], a[160:163]\n" \
"s_add_u32 m0, %[mA], 0x8400\n" \
"s_add_u32 s84, s62, 0x2000\n" \
"v_mfma_f32_16x16x32_bf16 a[192:195], v[120:123], v[104:107], a[192:195]\n" \
"buffer_load_dwordx4 v23, s[64:67], s84 offen lds\n" \
"v_mfma_f32_16x16x32_bf16 a[192:195], v[116:119], v[100:103], a[192:195]\n" \
"ds_read_b128 v[60:63], v28 offset:18432\n" \
"ds_read_b128 v[64:67], v29 offset:18432\n" \
"v_mfma_f32_16x16x32_bf16 a[224:227], v[116:119], v[108:111], a[224:227]\n" \
"ds_read_b64_tr_b16 v[116:117], v32 offset:0\n" \
"ds_read_b64_tr_b16 v[118:119], v32 offset:1024\n" \
"v_mfma_f32_16x16x32_bf16 a[224:227], v[120:123], v[112:115], a[224:227]\n" \
"s_add_u32 m0, %[mB], 0x8000\n" \
"s_add_u32 s85, s63, 0x0\n" \
"v_mfma_f32_16x16x32_bf16 a[132:135], v[128:131], v[88:91], a[132:135]\n" \
"buffer_load_dwordx4 v26, s[68:71], s85 offen lds\n" \
"v_mfma_f32_16x16x32_bf16 a[132:135], v[124:127], v[84:87], a[132:135]\n" \
"ds_read_b64_tr_b16 v[120:121], v32 offset:8192\n" \
"ds_read_b64_tr_b16 v[122:123], v32 offset:9216\n" \
"v_mfma_f32_16x16x32_bf16 a[164:167], v[124:127], v[92:95], a[164:167]\n" \
"ds_read_b128 v[72:75], v29 offset:20480\n" \
"ds_read_b128 v[68:71], v28 offset:20480\n" \
"v_mfma_f32_16x16x32_bf16 a[164:167], v[128:131], v[96:99], a[164:167]\n" \
"s_add_u32 m0, %[mB], 0xc000\n" \
"s_add_u32 s84, s63, 0x0\n" \
"v_mfma_f32_16x16x32_bf16 a[196:199], v[128:131], v[104:107], a[196:199]\n" \
"buffer_load_dwordx4 v26, s[72:75], s84 offen lds\n" \
"v_mfma_f32_16x16x32_bf16 a[196:199], v[124:127], v[100:103], a[196:199]\n" \
"ds_read_b128 v[76:79], v28 offset:22528\n" \
"ds_read_b128 v[80:83], v29 offset:22528\n" \
"v_mfma_f32_16x16x32_bf16 a[228:231], v[124:127], v[108:111], a[228:231]\n" \
"ds_read_b64_tr_b16 v[124:125], v33 offset:0\n" \
"ds_read_b64_tr_b16 v[126:127], v33 offset:1024\n" \
"v_mfma_f32_16x16x32_bf16 a[228:231], v[128:131], v[112:115], a[228:231]\n" \
"s_add_u32 m0, %[mA], 0x8800\n" \
"s_add_u32 s85, s62, 0x4000\n" \
"v_mfma_f32_16x16x32_bf16 a[136:139], v[136:139], v[88:91], a[136:139]\n" \
"buffer_load_dwordx4 v22, s[64:67], s85 offen lds\n" \
"v_mfma_f32_16x16x32_bf16 a[136:139], v[132:135], v[84:87], a[136:139]\n" \
"ds_read_b64_tr_b16 v[128:129], v33 offset:8192\n" \
"ds_read_b64_tr_b16 v[130:131], v33 offset:9216\n" \
"v_mfma_f32_16x16x32_bf16 a[168:171], v[132:135], v[92:95], a[168:171]\n" \
"ds_read_b128 v[200:203], v199 offset:0\n" \
"ds_read_b128 v[204:207], v199 offset:4096\n" \
"v_mfma_f32_16x16x32_bf16 a[168:171], v[136:139], v[96:99], a[168:171]\n" \
"s_add_u32 m0, %[mA], 0x8c00\n" \
"s_add_u32 s84, s62, 0x6000\n" \
"v_mfma_f32_16x16x32_bf16 a[200:203], v[136:139], v[104:107], a[200:203]\n" \
"buffer_load_dwordx4 v23, s[64:67], s84 offen lds\n" \
"v_mfma_f32_16x16x32_bf16 a[200:203], v[132:135], v[100:103], a[200:203]\n" \
"ds_read_b128 v[208:211], v199 offset:8192\n" \
"v_cndmask_b32_e32 v245, 0, v244, vcc\n" \
"s_nop 0\n" \
"v_mfma_f32_16x16x32_bf16 a[232:235], v[132:135], v[108:111], a[232:235]\n" \
"ds_read_b64_tr_b16 v[132:133], v34 offset:0\n" \
"ds_read_b64_tr_b16 v[134:135], v34 offset:1024\n" \
"v_mfma_f32_16x16x32_bf16 a[232:235], v[136:139], v[112:115], a[232:235]\n" \
"s_add_u32 m0, %[mB], 0x8400\n" \
"s_add_u32 s85, s63, 0x8000\n" \
"v_mfma_f32_16x16x32_bf16 a[140:143], v[144:147], v[88:91], a[140:143]\n" \
"buffer_load_dwordx4 v26, s[68:71], s85 offen lds\n" \
"v_mfma_f32_16x16x32_bf16 a[140:143], v[140:143], v[84:87], a[140:143]\n" \
"ds_read_b64_tr_b16 v[136:137], v34 offset:8192\n" \
"ds_read_b64_tr_b16 v[138:139], v34 offset:9216\n" \
"v_mfma_f32_16x16x32_bf16 a[172:175], v[140:143], v[92:95], a[172:175]\n" \
"v_mov_b32_e32 v247, 0xffff\n" \
"v_cmp_le_u32_e32 vcc, v246, v236\n" \
"v_mfma_f32_16x16x32_bf16 a[172:175], v[144:147], v[96:99], a[172:175]\n" \
"s_add_u32 m0, %[mB], 0xc400\n" \
"s_add_u32 s84, s63, 0x8000\n" \
"v_mfma_f32_16x16x32_bf16 a[204:207], v[144:147], v[104:107], a[204:207]\n" \
"buffer_load_dwordx4 v26, s[72:75], s84 offen lds\n" \
"v_mfma_f32_16x16x32_bf16 a[204:207], v[140:143], v[100:103], a[204:207]\n" \
"v_cndmask_b32_e32 v248, 0, v247, vcc\n" \
"v_mov_b32_e32 v249, 0xffff0000\n" \
"v_mfma_f32_16x16x32_bf16 a[236:239], v[140:143], v[108:111], a[236:239]\n" \
"ds_read_b64_tr_b16 v[140:141], v35 offset:0\n" \
"ds_read_b64_tr_b16 v[142:143], v35 offset:1024\n" \
"v_mfma_f32_16x16x32_bf16 a[236:239], v[144:147], v[112:115], a[236:239]\n" \
"s_add_u32 m0, %[mA], 0x9000\n" \
"s_add_u32 s85, s62, 0x8000\n" \
"v_mfma_f32_16x16x32_bf16 a[144:147], v[88:91], v[152:155], a[144:147]\n" \
"buffer_load_dwordx4 v22, s[64:67], s85 offen lds\n" \
"v_mfma_f32_16x16x32_bf16 a[144:147], v[84:87], v[148:151], a[144:147]\n" \
"ds_read_b64_tr_b16 v[144:145], v35 offset:8192\n" \
"ds_read_b64_tr_b16 v[146:147], v35 offset:9216\n" \
"v_mfma_f32_16x16x32_bf16 a[176:179], v[92:95], v[148:151], a[176:179]\n" \
"v_cmp_lt_u32_e32 vcc, v246, v236\n" \
"s_lshl_b32 s89, %[w], 8\n" \
"v_mfma_f32_16x16x32_bf16 a[176:179], v[96:99], v[152:155], a[176:179]\n" \
"s_add_u32 m0, %[mA], 0x9400\n" \
"s_add_u32 s84, s62, 0xa000\n" \
"v_mfma_f32_16x16x32_bf16 a[208:211], v[104:107], v[152:155], a[208:211]\n" \
"buffer_load_dwordx4 v23, s[64:67], s84 offen lds\n" \
"v_mfma_f32_16x16x32_bf16 a[208:211], v[100:103], v[148:151], a[208:211]\n" \
"v_cndmask_b32_e32 v250, 0, v249, vcc\n" \
"v_add_u32_e32 v251, 18, v214\n" \
"v_mfma_f32_16x16x32_bf16 a[240:243], v[108:111], v[148:151], a[240:243]\n" \
"ds_read_b64_tr_b16 v[148:149], v32 offset:16384\n" \
"ds_read_b64_tr_b16 v[150:151], v32 offset:17408\n" \
"v_mfma_f32_16x16x32_bf16 a[240:243], v[112:115], v[152:155], a[240:243]\n" \
"s_add_u32 m0, %[mB], 0x8800\n" \
"s_add_u32 s85, s63, 0x10000\n" \
"v_mfma_f32_16x16x32_bf16 a[148:151], v[88:91], v[160:163], a[148:151]\n" \
"buffer_load_dwordx4 v27, s[68:71], s85 offen lds\n" \
"v_mfma_f32_16x16x32_bf16 a[148:151], v[84:87], v[156:159], a[148:151]\n" \
"ds_read_b64_tr_b16 v[152:153], v32 offset:24576\n" \
"ds_read_b64_tr_b16 v[154:155], v32 offset:25600\n" \
"v_mfma_f32_16x16x32_bf16 a[180:183], v[92:95], v[156:159], a[180:183]\n" \
"s_add_u32 s46, 0x27000, s89\n" \
"v_mov_b32_e32 v218, 0xffff\n" \
"v_mfma_f32_16x16x32_bf16 a[180:183], v[96:99], v[160:163], a[180:183]\n" \
"s_add_u32 m0, %[mB], 0xc800\n" \
"s_add_u32 s84, s63, 0x10000\n" \
"v_mfma_f32_16x16x32_bf16 a[212:215], v[104:107], v[160:163], a[212:215]\n" \
"buffer_load_dwordx4 v27, s[72:75], s84 offen lds\n" \
"v_mfma_f32_16x16x32_bf16 a[212:215], v[100:103], v[156:159], a[212:215]\n" \
"v_cmp_le_u32_e32 vcc, v251, v236\n" \
"s_add_u32 s89, 0xffffc000, s101\n" \
"v_mfma_f32_16x16x32_bf16 a[244:247], v[108:111], v[156:159], a[244:247]\n" \
"ds_read_b64_tr_b16 v[156:157], v33 offset:16384\n" \
"ds_read_b64_tr_b16 v[158:159], v33 offset:17408\n" \
"v_mfma_f32_16x16x32_bf16 a[244:247], v[112:115], v[160:163], a[244:247]\n" \
"ds_read_b64_tr_b16 v[160:161], v33 offset:24576\n" \
"ds_read_b64_tr_b16 v[162:163], v33 offset:25600\n" \
"v_mfma_f32_16x16x32_bf16 a[152:155], v[88:91], v[168:171], a[152:155]\n" \
"v_cndmask_b32_e32 v219, 0, v218, vcc\n" \
"v_lshlrev_b32_e32 v224, 3, v212\n" \
"v_mfma_f32_16x16x32_bf16 a[152:155], v[84:87], v[164:167], a[152:155]\n" \
"s_cmp_lg_u32 s95, 0\n" \
"s_cselect_b32 s48, s89, s100\n" \
"v_mfma_f32_16x16x32_bf16 a[184:187], v[92:95], v[164:167], a[184:187]\n" \
"s_lshr_b32 s94, %[b], 5\n" \
"s_mov_b32 s96, 0xc000\n" \
"v_mfma_f32_16x16x32_bf16 a[184:187], v[96:99], v[168:171], a[184:187]\n" \
"s_mov_b32 s97, 0x8000\n" \
"v_mov_b32_e32 v221, 0xffff0000\n" \
"v_mfma_f32_16x16x32_bf16 a[216:219], v[104:107], v[168:171], a[216:219]\n" \
"v_cmp_lt_u32_e32 vcc, v251, v236\n" \
"v_lshrrev_b32_e32 v212, 4, v20\n" \
"v_mfma_f32_16x16x32_bf16 a[216:219], v[100:103], v[164:167], a[216:219]\n" \
"s_cmp_lg_u32 s94, 0\n" \
"s_cselect_b32 s53, s96, 0x8000\n" \
"v_mfma_f32_16x16x32_bf16 a[248:251], v[108:111], v[164:167], a[248:251]\n" \
"ds_read_b64_tr_b16 v[164:165], v34 offset:16384\n" \
"ds_read_b64_tr_b16 v[166:167], v34 offset:17408\n" \
"v_mfma_f32_16x16x32_bf16 a[248:251], v[112:115], v[168:171], a[248:251]\n" \
"ds_read_b64_tr_b16 v[168:169], v34 offset:24576\n" \
"ds_read_b64_tr_b16 v[170:171], v34 offset:25600\n" \
"v_mfma_f32_16x16x32_bf16 a[156:159], v[88:91], v[176:179], a[156:159]\n" \
"ds_read_b128 v[88:91], v29 offset:0\n" \
"v_cndmask_b32_e32 v216, 0, v221, vcc\n" \
"v_mfma_f32_16x16x32_bf16 a[156:159], v[84:87], v[172:175], a[156:159]\n" \
"ds_read_b128 v[84:87], v28 offset:0\n" \
"v_lshlrev_b32_e32 v223, 2, v236\n" \
"v_mfma_f32_16x16x32_bf16 a[188:191], v[92:95], v[172:175], a[188:191]\n" \
"ds_read_b128 v[92:95], v28 offset:2048\n" \
"s_lshl_b32 s101, %[w], 9\n" \
"v_mfma_f32_16x16x32_bf16 a[188:191], v[96:99], v[176:179], a[188:191]\n" \
"ds_read_b128 v[96:99], v29 offset:2048\n" \
"v_mul_u32_u24_e32 v213, 0xe39, v212\n" \
"v_mfma_f32_16x16x32_bf16 a[220:223], v[104:107], v[176:179], a[220:223]\n" \
"ds_read_b128 v[104:107], v29 offset:4096\n" \
"v_or_b32_e32 v15, v219, v216\n" \
"v_mfma_f32_16x16x32_bf16 a[220:223], v[100:103], v[172:175], a[220:223]\n" \
"ds_read_b128 v[100:103], v28 offset:4096\n" \
"v_add_u32_e32 v18, 0x27000, v223\n" \
"v_mfma_f32_16x16x32_bf16 a[252:255], v[108:111], v[172:175], a[252:255]\n" \
"ds_read_b64_tr_b16 v[172:173], v35 offset:16384\n" \
"ds_read_b64_tr_b16 v[174:175], v35 offset:17408\n" \
"v_mfma_f32_16x16x32_bf16 a[252:255], v[112:115], v[176:179], a[252:255]\n" \
"s_add_u32 m0, %[mA], 0x9800\n" \
"s_waitcnt lgkmcnt(12)\n" \
"v_mfma_f32_16x16x32_bf16 a[0:3], v[120:123], v[56:59], a[0:3]\n" \
"s_add_u32 s85, s62, 0xc000\n" \
"ds_read_b64_tr_b16 v[176:177], v35 offset:24576\n" \
"v_mfma_f32_16x16x32_bf16 a[0:3], v[116:119], v[52:55], a[0:3]\n" \
"buffer_load_dwordx4 v22, s[64:67], s85 offen lds\n" \
"v_mfma_f32_16x16x32_bf16 a[32:35], v[116:119], v[60:63], a[32:35]\n" \
"s_add_u32 m0, %[mA], 0x9c00\n" \
"s_add_u32 s84, s62, 0xe000\n" \
"v_mfma_f32_16x16x32_bf16 a[32:35], v[120:123], v[64:67], a[32:35]\n" \
"ds_read_b64_tr_b16 v[178:179], v35 offset:25600\n" \
"ds_read_b128 v[108:111], v28 offset:6144\n" \
"v_mfma_f32_16x16x32_bf16 a[64:67], v[120:123], v[72:75], a[64:67]\n" \
"buffer_load_dwordx4 v23, s[64:67], s84 offen lds\n" \
"v_mfma_f32_16x16x32_bf16 a[64:67], v[116:119], v[68:71], a[64:67]\n" \
"s_add_u32 m0, %[mB], 0x8c00\n" \
"s_add_u32 s85, s63, 0x18000\n" \
"L_pf2:\n" \
"s_cbranch_execz L_pf6\n" \
"s_nop 0\n" \
"v_mfma_f32_16x16x32_bf16 a[96:99], v[116:119], v[76:79], a[96:99]\n" \
"ds_read_b128 v[112:115], v29 offset:6144\n" \
"s_add_u32 s89, 0x27800, s101\n" \
"v_mfma_f32_16x16x32_bf16 a[96:99], v[120:123], v[80:83], a[96:99]\n" \
"buffer_load_dwordx4 v27, s[68:71], s85 offen lds\n" \
"v_mfma_f32_16x16x32_bf16 a[4:7], v[128:131], v[56:59], a[4:7]\n" \
"s_add_u32 m0, %[mB], 0xcc00\n" \
"s_add_u32 s84, s63, 0x18000\n" \
"v_mfma_f32_16x16x32_bf16 a[4:7], v[124:127], v[52:55], a[4:7]\n" \
"s_cmp_lg_u32 s94, 0\n" \
"s_cselect_b32 s98, s97, 0x4000\n" \
"v_mfma_f32_16x16x32_bf16 a[36:39], v[124:127], v[60:63], a[36:39]\n" \
"buffer_load_dwordx4 v27, s[72:75], s84 offen lds\n" \
"v_mfma_f32_16x16x32_bf16 a[36:39], v[128:131], v[64:67], a[36:39]\n" \
"v_lshrrev_b32_e32 v214, 16, v213\n" \
"v_add_u32_e64 v222, 64, v212\n" \
"v_mfma_f32_16x16x32_bf16 a[68:71], v[128:131], v[72:75], a[68:71]\n" \
"v_add_u32_e32 v19, s89, v224\n" \
"v_mul_u32_u24_e32 v216, 18, v214\n" \
"v_mfma_f32_16x16x32_bf16 a[68:71], v[124:127], v[68:71], a[68:71]\n" \
"buffer_store_dwordx4 v[200:203], v20, s[80:83], s57 offen sc1\n" \
"v_mfma_f32_16x16x32_bf16 a[100:103], v[124:127], v[76:79], a[100:103]\n" \
"s_and_b32 s94, 7, %[b]\n" \
"v_mul_u32_u24_e32 v223, 0xe39, v222\n" \
"v_mfma_f32_16x16x32_bf16 a[100:103], v[128:131], v[80:83], a[100:103]\n" \
"v_sub_u32_e32 v217, v212, v216\n" \
"s_lshl_b32 s95, s94, 5\n" \
"v_mfma_f32_16x16x32_bf16 a[8:11], v[136:139], v[56:59], a[8:11]\n" \
"v_lshrrev_b32_e32 v224, 16, v223\n" \
"v_add_u32_e32 v233, 0x80, v212\n" \
"v_mfma_f32_16x16x32_bf16 a[8:11], v[132:135], v[52:55], a[8:11]\n" \
"s_add_u32 s96, 32, s95\n" \
"v_lshlrev_b32_e32 v218, 4, v217\n" \
"v_mfma_f32_16x16x32_bf16 a[40:43], v[132:135], v[60:63], a[40:43]\n" \
"buffer_store_dwordx4 v[204:207], v20, s[80:83], s56 offen sc1\n" \
"v_mfma_f32_16x16x32_bf16 a[40:43], v[136:139], v[64:67], a[40:43]\n" \
"v_mul_u32_u24_e32 v226, 18, v224\n" \
"v_mul_u32_u24_e32 v234, 0xe39, v233\n" \
"v_mfma_f32_16x16x32_bf16 a[72:75], v[136:139], v[72:75], a[72:75]\n" \
"v_or_b32_e32 v13, v243, v245\n" \
"v_add_u32_e64 v219, s96, v218\n" \
"v_mfma_f32_16x16x32_bf16 a[72:75], v[132:135], v[68:71], a[72:75]\n" \
"v_sub_u32_e32 v227, v222, v226\n" \
"v_lshrrev_b32_e32 v235, 16, v234\n" \
"v_mfma_f32_16x16x32_bf16 a[104:107], v[132:135], v[76:79], a[104:107]\n" \
"v_add_u32_e32 v244, 0xc0, v212\n" \
"v_or_b32_e32 v12, v238, v240\n" \
"v_mfma_f32_16x16x32_bf16 a[104:107], v[136:139], v[80:83], a[104:107]\n" \
"v_and_b32_e32 v220, 0xff, v219\n" \
"v_cmp_gt_u32_e32 vcc, 16, v217\n" \
"v_mfma_f32_16x16x32_bf16 a[12:15], v[144:147], v[56:59], a[12:15]\n" \
"buffer_store_dwordx4 v[208:211], v20, s[80:83], s58 offen sc1\n" \
"v_mfma_f32_16x16x32_bf16 a[12:15], v[140:143], v[52:55], a[12:15]\n" \
"v_lshlrev_b32_e32 v228, 4, v227\n" \
"v_mul_u32_u24_e32 v237, 18, v235\n" \
"v_mfma_f32_16x16x32_bf16 a[44:47], v[140:143], v[60:63], a[44:47]\n" \
"v_mul_u32_u24_e32 v245, 0xe39, v244\n" \
"v_or_b32_e32 v14, v248, v250\n" \
"v_mfma_f32_16x16x32_bf16 a[44:47], v[144:147], v[64:67], a[44:47]\n" \
"v_cndmask_b32_e32 v221, v218, v220, vcc\n" \
"v_add_u32_e64 v229, s96, v228\n" \
"v_mfma_f32_16x16x32_bf16 a[76:79], v[144:147], v[72:75], a[76:79]\n" \
"v_sub_u32_e32 v238, v233, v237\n" \
"v_lshrrev_b32_e32 v246, 16, v245\n" \
"v_mfma_f32_16x16x32_bf16 a[76:79], v[140:143], v[68:71], a[76:79]\n" \
"v_add_u32_e32 v219, 0x100, v212\n" \
"v_mul_u32_u24_e32 v215, 0x480, v214\n" \
"v_mfma_f32_16x16x32_bf16 a[108:111], v[140:143], v[76:79], a[108:111]\n" \
"s_mov_b32 m0, %[mS]\n" \
"s_mov_b32 s86, s10\n" \
"v_mfma_f32_16x16x32_bf16 a[108:111], v[144:147], v[80:83], a[108:111]\n" \
"s_mov_b32 s87, s11\n" \
"v_and_b32_e32 v230, 0xff, v229\n" \
"v_mfma_f32_16x16x32_bf16 a[16:19], v[56:59], v[152:155], a[16:19]\n" \
"v_cmp_gt_u32_e32 vcc, 16, v227\n" \
"v_lshlrev_b32_e32 v239, 4, v238\n" \
"v_mfma_f32_16x16x32_bf16 a[16:19], v[52:55], v[148:151], a[16:19]\n" \
"v_mul_u32_u24_e32 v248, 18, v246\n" \
"v_mul_u32_u24_e32 v217, 0xe39, v219\n" \
"v_mfma_f32_16x16x32_bf16 a[48:51], v[60:63], v[148:151], a[48:51]\n" \
"v_add_u32_e32 v0, v215, v221\n" \
"v_cndmask_b32_e32 v231, v228, v230, vcc\n" \
"v_mfma_f32_16x16x32_bf16 a[48:51], v[64:67], v[152:155], a[48:51]\n" \
"v_add_u32_e32 v240, s96, v239\n" \
"v_sub_u32_e32 v249, v244, v248\n" \
"v_mfma_f32_16x16x32_bf16 a[80:83], v[72:75], v[152:155], a[80:83]\n" \
"v_lshrrev_b32_e32 v220, 16, v217\n" \
"v_add_u32_e32 v229, 0x140, v212\n" \
"s_mov_b32 exec_lo, 0xfffc0000\n" \
"s_mov_b32 exec_hi, 0xf\n" \
"global_load_lds_dword v188, s[86:87]\n" \
"s_mov_b64 exec, -1\n" \
"v_mfma_f32_16x16x32_bf16 a[80:83], v[68:71], v[148:151], a[80:83]\n" \
"v_mul_u32_u24_e32 v225, 0x480, v224\n" \
"v_and_b32_e32 v241, 0xff, v240\n" \
"v_mfma_f32_16x16x32_bf16 a[112:115], v[76:79], v[148:151], a[112:115]\n" \
"v_cmp_gt_u32_e32 vcc, 16, v238\n" \
"v_lshlrev_b32_e32 v250, 4, v249\n" \
"v_mfma_f32_16x16x32_bf16 a[112:115], v[80:83], v[152:155], a[112:115]\n" \
"v_mul_u32_u24_e32 v215, 18, v220\n" \
"v_mul_u32_u24_e32 v227, 0xe39, v229\n" \
"v_mfma_f32_16x16x32_bf16 a[20:23], v[56:59], v[160:163], a[20:23]\n" \
"v_add_u32_e32 v232, v225, v231\n" \
"v_cndmask_b32_e32 v242, v239, v241, vcc\n" \
"v_mfma_f32_16x16x32_bf16 a[20:23], v[52:55], v[156:159], a[20:23]\n" \
"v_add_u32_e32 v251, s96, v250\n" \
"v_sub_u32_e32 v221, v219, v215\n" \
"v_mfma_f32_16x16x32_bf16 a[52:55], v[60:63], v[156:159], a[52:55]\n" \
"v_lshrrev_b32_e32 v230, 16, v227\n" \
"v_add_u32_e32 v240, 0x180, v212\n" \
"v_mfma_f32_16x16x32_bf16 a[52:55], v[64:67], v[160:163], a[52:55]\n" \
"v_mul_u32_u24_e32 v236, 0x480, v235\n" \
"v_and_b32_e32 v213, 0xff, v251\n" \
"v_mfma_f32_16x16x32_bf16 a[84:87], v[72:75], v[160:163], a[84:87]\n" \
"v_cmp_gt_u32_e32 vcc, 16, v249\n" \
"v_lshlrev_b32_e32 v223, 4, v221\n" \
"v_mfma_f32_16x16x32_bf16 a[84:87], v[68:71], v[156:159], a[84:87]\n" \
"v_mul_u32_u24_e32 v225, 18, v230\n" \
"v_mul_u32_u24_e32 v238, 0xe39, v240\n" \
"v_mfma_f32_16x16x32_bf16 a[116:119], v[76:79], v[156:159], a[116:119]\n" \
"v_add_u32_e32 v1, 0xfffffc00, v232\n" \
"v_add_u32_e64 v243, v236, v242\n" \
"v_mfma_f32_16x16x32_bf16 a[116:119], v[80:83], v[160:163], a[116:119]\n" \
"v_cndmask_b32_e32 v214, v250, v213, vcc\n" \
"s_waitcnt lgkmcnt(0)\n" \
"v_mfma_f32_16x16x32_bf16 a[24:27], v[56:59], v[168:171], a[24:27]\n" \
"v_add_u32_e32 v224, s96, v223\n" \
"v_sub_u32_e32 v231, v229, v225\n" \
"v_mfma_f32_16x16x32_bf16 a[24:27], v[52:55], v[164:167], a[24:27]\n" \
"v_lshrrev_b32_e32 v241, 16, v238\n" \
"v_add_u32_e32 v251, 0x1c0, v212\n" \
"v_mfma_f32_16x16x32_bf16 a[56:59], v[60:63], v[164:167], a[56:59]\n" \
"v_mul_u32_u24_e32 v247, 0x480, v246\n" \
"v_and_b32_e32 v222, 0xff, v224\n" \
"v_mfma_f32_16x16x32_bf16 a[56:59], v[64:67], v[168:171], a[56:59]\n" \
"v_cmp_gt_u32_e32 vcc, 16, v221\n" \
"v_lshlrev_b32_e32 v232, 4, v231\n" \
"v_mfma_f32_16x16x32_bf16 a[88:91], v[72:75], v[168:171], a[88:91]\n" \
"v_mul_u32_u24_e32 v236, 18, v241\n" \
"v_mul_u32_u24_e32 v249, 0xe39, v251\n" \
"v_mfma_f32_16x16x32_bf16 a[88:91], v[68:71], v[164:167], a[88:91]\n" \
"v_add_u32_e32 v2, 0xfffff800, v243\n" \
"v_add_u32_e64 v216, v247, v214\n" \
"v_mfma_f32_16x16x32_bf16 a[120:123], v[76:79], v[164:167], a[120:123]\n" \
"v_cndmask_b32_e32 v226, v223, v222, vcc\n" \
"v_add_u32_e32 v234, s96, v232\n" \
"v_mfma_f32_16x16x32_bf16 a[120:123], v[80:83], v[168:171], a[120:123]\n" \
"v_sub_u32_e32 v242, v240, v236\n" \
"v_lshrrev_b32_e32 v213, 16, v249\n" \
"v_mfma_f32_16x16x32_bf16 a[28:31], v[56:59], v[176:179], a[28:31]\n" \
"s_add_u32 s62, s22, 0x300\n" \
"s_and_b32 s62, s62, 0x3ff\n" \
"v_mfma_f32_16x16x32_bf16 a[28:31], v[52:55], v[172:175], a[28:31]\n" \
"s_lshl_b32 s63, s62, 12\n" \
"v_add_u32_e32 v224, 0x200, v212\n" \
"v_mfma_f32_16x16x32_bf16 a[60:63], v[60:63], v[172:175], a[60:63]\n" \
"v_mul_u32_u24_e32 v218, 0x480, v220\n" \
"v_and_b32_e32 v235, 0xff, v234\n" \
"v_mfma_f32_16x16x32_bf16 a[60:63], v[64:67], v[176:179], a[60:63]\n" \
"v_cmp_gt_u32_e32 vcc, 16, v231\n" \
"v_lshlrev_b32_e32 v243, 4, v242\n" \
"v_mfma_f32_16x16x32_bf16 a[92:95], v[72:75], v[176:179], a[92:95]\n" \
"v_mul_u32_u24_e32 v247, 18, v213\n" \
"v_mul_u32_u24_e32 v221, 0xe39, v224\n" \
"v_mfma_f32_16x16x32_bf16 a[92:95], v[68:71], v[172:175], a[92:95]\n" \
"v_add_u32_e32 v3, 0xfffff400, v216\n" \
"v_add_u32_e64 v4, v218, v226\n" \
"v_mfma_f32_16x16x32_bf16 a[124:127], v[76:79], v[172:175], a[124:127]\n" \
"v_cndmask_b32_e32 v233, v232, v235, vcc\n" \
"v_add_u32_e32 v245, s96, v243\n" \
"v_mfma_f32_16x16x32_bf16 a[124:127], v[80:83], v[176:179], a[124:127]\n" \
"v_sub_u32_e32 v214, v251, v247\n" \
"v_lshrrev_b32_e32 v222, 16, v221\n" \
"s_waitcnt vmcnt(4)\n" \
"s_barrier\n" \
"v_mfma_f32_16x16x32_bf16 a[144:147], v[88:91], v[152:155], a[144:147]\n" \
"s_add_u32 m0, %[mA], 0x0\n" \
"s_add_u32 s85, s62, 0x0\n" \
"v_mfma_f32_16x16x32_bf16 a[144:147], v[84:87], v[148:151], a[144:147]\n" \
"buffer_load_dwordx4 v22, s[64:67], s85 offen lds\n" \
"v_mfma_f32_16x16x32_bf16 a[176:179], v[92:95], v[148:151], a[176:179]\n" \
"ds_read_b128 v[56:59], v29 offset:49152\n" \
"ds_read_b128 v[52:55], v28 offset:49152\n" \
"v_mfma_f32_16x16x32_bf16 a[176:179], v[96:99], v[152:155], a[176:179]\n" \
"s_add_u32 m0, %[mA], 0x400\n" \
"s_add_u32 s84, s62, 0x2000\n" \
"v_mfma_f32_16x16x32_bf16 a[208:211], v[104:107], v[152:155], a[208:211]\n" \
"buffer_load_dwordx4 v23, s[64:67], s84 offen lds\n" \
"v_mfma_f32_16x16x32_bf16 a[208:211], v[100:103], v[148:151], a[208:211]\n" \
"ds_read_b128 v[60:63], v28 offset:51200\n" \
"ds_read_b128 v[64:67], v29 offset:51200\n" \
"v_mfma_f32_16x16x32_bf16 a[240:243], v[108:111], v[148:151], a[240:243]\n" \
"ds_read_b64_tr_b16 v[148:149], v32 offset:49152\n" \
"ds_read_b64_tr_b16 v[150:151], v32 offset:50176\n" \
"v_mfma_f32_16x16x32_bf16 a[240:243], v[112:115], v[152:155], a[240:243]\n" \
"s_add_u32 m0, %[mB], 0x0\n" \
"s_add_u32 s85, s63, 0x0\n" \
"v_mfma_f32_16x16x32_bf16 a[148:151], v[88:91], v[160:163], a[148:151]\n" \
"buffer_load_dwordx4 v26, s[68:71], s85 offen lds\n" \
"v_mfma_f32_16x16x32_bf16 a[148:151], v[84:87], v[156:159], a[148:151]\n" \
"ds_read_b64_tr_b16 v[152:153], v32 offset:57344\n" \
"ds_read_b64_tr_b16 v[154:155], v32 offset:58368\n" \
"v_mfma_f32_16x16x32_bf16 a[180:183], v[92:95], v[156:159], a[180:183]\n" \
"ds_read_b128 v[72:75], v29 offset:53248\n" \
"ds_read_b128 v[68:71], v28 offset:53248\n" \
"v_mfma_f32_16x16x32_bf16 a[180:183], v[96:99], v[160:163], a[180:183]\n" \
"s_add_u32 m0, %[mB], 0x4000\n" \
"s_add_u32 s84, s63, 0x0\n" \
"s_nop 0\n" \
"v_mfma_f32_16x16x32_bf16 a[212:215], v[104:107], v[160:163], a[212:215]\n" \
"buffer_load_dwordx4 v26, s[72:75], s84 offen lds\n" \
"v_mfma_f32_16x16x32_bf16 a[212:215], v[100:103], v[156:159], a[212:215]\n" \
"ds_read_b128 v[76:79], v28 offset:55296\n" \
"ds_read_b128 v[80:83], v29 offset:55296\n" \
"v_mfma_f32_16x16x32_bf16 a[244:247], v[108:111], v[156:159], a[244:247]\n" \
"ds_read_b64_tr_b16 v[156:157], v33 offset:49152\n" \
"ds_read_b64_tr_b16 v[158:159], v33 offset:50176\n" \
"v_mfma_f32_16x16x32_bf16 a[244:247], v[112:115], v[160:163], a[244:247]\n" \
"s_add_u32 m0, %[mA], 0x800\n" \
"s_add_u32 s85, s62, 0x4000\n" \
"v_mfma_f32_16x16x32_bf16 a[152:155], v[88:91], v[168:171], a[152:155]\n" \
"buffer_load_dwordx4 v22, s[64:67], s85 offen lds\n" \
"v_mfma_f32_16x16x32_bf16 a[152:155], v[84:87], v[164:167], a[152:155]\n" \
"ds_read_b64_tr_b16 v[160:161], v33 offset:57344\n" \
"ds_read_b64_tr_b16 v[162:163], v33 offset:58368\n" \
"v_mfma_f32_16x16x32_bf16 a[184:187], v[92:95], v[164:167], a[184:187]\n" \
"v_and_b32_e32 v246, 0xff, v245\n" \
"v_cmp_gt_u32_e32 vcc, 16, v242\n" \
"v_mfma_f32_16x16x32_bf16 a[184:187], v[96:99], v[168:171], a[184:187]\n" \
"s_add_u32 m0, %[mA], 0xc00\n" \
"s_add_u32 s84, s62, 0x6000\n" \
"v_mfma_f32_16x16x32_bf16 a[216:219], v[104:107], v[168:171], a[216:219]\n" \
"buffer_load_dwordx4 v23, s[64:67], s84 offen lds\n" \
"v_mfma_f32_16x16x32_bf16 a[216:219], v[100:103], v[164:167], a[216:219]\n" \
"v_lshlrev_b32_e32 v216, 4, v214\n" \
"v_mul_u32_u24_e32 v218, 18, v222\n" \
"s_nop 0\n" \
"v_mfma_f32_16x16x32_bf16 a[248:251], v[108:111], v[164:167], a[248:251]\n" \
"ds_read_b64_tr_b16 v[164:165], v34 offset:49152\n" \
"ds_read_b64_tr_b16 v[166:167], v34 offset:50176\n" \
"v_mfma_f32_16x16x32_bf16 a[248:251], v[112:115], v[168:171], a[248:251]\n" \
"s_add_u32 m0, %[mB], 0x400\n" \
"s_add_u32 s85, s63, 0x8000\n" \
"v_mfma_f32_16x16x32_bf16 a[156:159], v[88:91], v[176:179], a[156:159]\n" \
"buffer_load_dwordx4 v26, s[68:71], s85 offen lds\n" \
"v_mfma_f32_16x16x32_bf16 a[156:159], v[84:87], v[172:175], a[156:159]\n" \
"ds_read_b64_tr_b16 v[168:169], v34 offset:57344\n" \
"ds_read_b64_tr_b16 v[170:171], v34 offset:58368\n" \
"v_mfma_f32_16x16x32_bf16 a[188:191], v[92:95], v[172:175], a[188:191]\n" \
"v_cndmask_b32_e32 v244, v243, v246, vcc\n" \
"v_add_u32_e32 v217, s96, v216\n" \
"v_mfma_f32_16x16x32_bf16 a[188:191], v[96:99], v[176:179], a[188:191]\n" \
"s_add_u32 m0, %[mB], 0x4400\n" \
"s_add_u32 s84, s63, 0x8000\n" \
"v_mfma_f32_16x16x32_bf16 a[220:223], v[104:107], v[176:179], a[220:223]\n" \
"buffer_load_dwordx4 v26, s[72:75], s84 offen lds\n" \
"v_mfma_f32_16x16x32_bf16 a[220:223], v[100:103], v[172:175], a[220:223]\n" \
"v_sub_u32_e32 v226, v224, v218\n" \
"s_nop 0\n" \
"v_mul_u32_u24_e32 v228, 0x480, v230\n" \
"v_mfma_f32_16x16x32_bf16 a[252:255], v[108:111], v[172:175], a[252:255]\n" \
"ds_read_b64_tr_b16 v[172:173], v35 offset:49152\n" \
"ds_read_b64_tr_b16 v[174:175], v35 offset:50176\n" \
"v_mfma_f32_16x16x32_bf16 a[252:255], v[112:115], v[176:179], a[252:255]\n" \
"s_add_u32 m0, %[mA], 0x1000\n" \
"s_add_u32 s85, s62, 0x8000\n" \
"v_mfma_f32_16x16x32_bf16 a[128:131], v[120:123], v[88:91], a[128:131]\n" \
"buffer_load_dwordx4 v22, s[64:67], s85 offen lds\n" \
"v_mfma_f32_16x16x32_bf16 a[128:131], v[116:119], v[84:87], a[128:131]\n" \
"ds_read_b64_tr_b16 v[176:177], v35 offset:57344\n" \
"ds_read_b64_tr_b16 v[178:179], v35 offset:58368\n" \
"v_mfma_f32_16x16x32_bf16 a[160:163], v[116:119], v[92:95], a[160:163]\n" \
"v_and_b32_e32 v220, 0xff, v217\n" \
"v_cmp_gt_u32_e32 vcc, 16, v214\n" \
"v_mfma_f32_16x16x32_bf16 a[160:163], v[120:123], v[96:99], a[160:163]\n" \
"s_add_u32 m0, %[mA], 0x1400\n" \
"s_add_u32 s84, s62, 0xa000\n" \
"v_mfma_f32_16x16x32_bf16 a[192:195], v[120:123], v[104:107], a[192:195]\n" \
"buffer_load_dwordx4 v23, s[64:67], s84 offen lds\n" \
"v_mfma_f32_16x16x32_bf16 a[192:195], v[116:119], v[100:103], a[192:195]\n" \
"v_lshlrev_b32_e32 v227, 4, v226\n" \
"v_cndmask_b32_e32 v219, v216, v220, vcc\n" \
"s_nop 0\n" \
"v_mfma_f32_16x16x32_bf16 a[224:227], v[116:119], v[108:111], a[224:227]\n" \
"ds_read_b64_tr_b16 v[116:117], v32 offset:32768\n" \
"ds_read_b64_tr_b16 v[118:119], v32 offset:33792\n" \
"v_mfma_f32_16x16x32_bf16 a[224:227], v[120:123], v[112:115], a[224:227]\n" \
"s_add_u32 m0, %[mB], 0x800\n" \
"s_add_u32 s85, s63, 0x10000\n" \
"v_mfma_f32_16x16x32_bf16 a[132:135], v[128:131], v[88:91], a[132:135]\n" \
"buffer_load_dwordx4 v27, s[68:71], s85 offen lds\n" \
"v_mfma_f32_16x16x32_bf16 a[132:135], v[124:127], v[84:87], a[132:135]\n" \
"ds_read_b64_tr_b16 v[120:121], v32 offset:40960\n" \
"ds_read_b64_tr_b16 v[122:123], v32 offset:41984\n" \
"v_mfma_f32_16x16x32_bf16 a[164:167], v[124:127], v[92:95], a[164:167]\n" \
"v_add_u32_e64 v230, s96, v227\n" \
"v_and_b32_e32 v229, 0xff, v230\n" \
"v_mfma_f32_16x16x32_bf16 a[164:167], v[128:131], v[96:99], a[164:167]\n" \
"s_add_u32 m0, %[mB], 0x4800\n" \
"s_add_u32 s84, s63, 0x10000\n" \
"v_mfma_f32_16x16x32_bf16 a[196:199], v[128:131], v[104:107], a[196:199]\n" \
"buffer_load_dwordx4 v27, s[72:75], s84 offen lds\n" \
"v_mfma_f32_16x16x32_bf16 a[196:199], v[124:127], v[100:103], a[196:199]\n" \
"v_cmp_gt_u32_e32 vcc, 16, v226\n" \
"v_mul_u32_u24_e32 v250, 0x480, v213\n" \
"v_mfma_f32_16x16x32_bf16 a[228:231], v[124:127], v[108:111], a[228:231]\n" \
"ds_read_b64_tr_b16 v[124:125], v33 offset:32768\n" \
"ds_read_b64_tr_b16 v[126:127], v33 offset:33792\n" \
"v_mfma_f32_16x16x32_bf16 a[228:231], v[128:131], v[112:115], a[228:231]\n" \
"ds_read_b64_tr_b16 v[128:129], v33 offset:40960\n" \
"ds_read_b64_tr_b16 v[130:131], v33 offset:41984\n" \
"v_mfma_f32_16x16x32_bf16 a[136:139], v[136:139], v[88:91], a[136:139]\n" \
"v_cndmask_b32_e32 v225, v227, v229, vcc\n" \
"v_and_b32_e64 v249, 2, v212\n" \
"v_mfma_f32_16x16x32_bf16 a[136:139], v[132:135], v[84:87], a[136:139]\n" \
"v_mov_b32_e32 v213, 0x100\n" \
"v_mov_b32_e64 v251, s95\n" \
"v_mfma_f32_16x16x32_bf16 a[168:171], v[132:135], v[92:95], a[168:171]\n" \
"v_cmp_ne_u32_e32 vcc, 0, v249\n" \
"s_mov_b32 s76, s8\n" \
"v_mfma_f32_16x16x32_bf16 a[168:171], v[136:139], v[96:99], a[168:171]\n" \
"v_cndmask_b32_e32 v247, v251, v213, vcc\n" \
"s_and_b32 s77, s9, 0xffff\n" \
"v_mfma_f32_16x16x32_bf16 a[200:203], v[136:139], v[104:107], a[200:203]\n" \
"s_mov_b32 s78, 0x240000\n" \
"v_xor_b32_e32 v37, 0x20, v190\n" \
"v_mfma_f32_16x16x32_bf16 a[200:203], v[132:135], v[100:103], a[200:203]\n" \
"v_xor_b32_e32 v38, 0x40, v190\n" \
"v_add_u32_e32 v48, s60, v28\n" \
"v_mfma_f32_16x16x32_bf16 a[232:235], v[132:135], v[108:111], a[232:235]\n" \
"ds_read_b64_tr_b16 v[132:133], v34 offset:32768\n" \
"ds_read_b64_tr_b16 v[134:135], v34 offset:33792\n" \
"v_mfma_f32_16x16x32_bf16 a[232:235], v[136:139], v[112:115], a[232:235]\n" \
"ds_read_b64_tr_b16 v[136:137], v34 offset:40960\n" \
"ds_read_b64_tr_b16 v[138:139], v34 offset:41984\n" \
"v_mfma_f32_16x16x32_bf16 a[140:143], v[144:147], v[88:91], a[140:143]\n" \
"ds_read_b128 v[88:91], v29 offset:32768\n" \
"v_add_u32_e64 v49, s60, v29\n" \
"v_mfma_f32_16x16x32_bf16 a[140:143], v[140:143], v[84:87], a[140:143]\n" \
"ds_read_b128 v[84:87], v28 offset:32768\n" \
"v_xor_b32_e64 v36, 0x0, v190\n" \
"v_mfma_f32_16x16x32_bf16 a[172:175], v[140:143], v[92:95], a[172:175]\n" \
"ds_read_b128 v[92:95], v28 offset:34816\n" \
"v_add_u32_e64 v237, v228, v233\n" \
"v_mfma_f32_16x16x32_bf16 a[172:175], v[144:147], v[96:99], a[172:175]\n" \
"ds_read_b128 v[96:99], v29 offset:34816\n" \
"s_lshl_b32 s97, %[w], 4\n" \
"v_mfma_f32_16x16x32_bf16 a[204:207], v[144:147], v[104:107], a[204:207]\n" \
"ds_read_b128 v[104:107], v29 offset:36864\n" \
"v_lshrrev_b32_e32 v234, 2, v212\n" \
"v_mfma_f32_16x16x32_bf16 a[204:207], v[140:143], v[100:103], a[204:207]\n" \
"ds_read_b128 v[100:103], v28 offset:36864\n" \
"v_add_u32_e32 v5, 0xfffffc00, v237\n" \
"v_mfma_f32_16x16x32_bf16 a[236:239], v[140:143], v[108:111], a[236:239]\n" \
"ds_read_b64_tr_b16 v[140:141], v35 offset:32768\n" \
"ds_read_b64_tr_b16 v[142:143], v35 offset:33792\n" \
"v_mfma_f32_16x16x32_bf16 a[236:239], v[144:147], v[112:115], a[236:239]\n" \
"s_add_u32 m0, %[mA], 0x1800\n" \
"s_waitcnt lgkmcnt(12)\n" \
"s_nop 0\n" \
"v_mfma_f32_16x16x32_bf16 a[16:19], v[56:59], v[152:155], a[16:19]\n" \
"s_add_u32 s85, s62, 0xc000\n" \
"ds_read_b64_tr_b16 v[144:145], v35 offset:40960\n" \
"v_mfma_f32_16x16x32_bf16 a[16:19], v[52:55], v[148:151], a[16:19]\n" \
"buffer_load_dwordx4 v22, s[64:67], s85 offen lds\n" \
"v_mfma_f32_16x16x32_bf16 a[48:51], v[60:63], v[148:151], a[48:51]\n" \
"s_add_u32 m0, %[mA], 0x1c00\n" \
"s_add_u32 s84, s62, 0xe000\n" \
"v_mfma_f32_16x16x32_bf16 a[48:51], v[64:67], v[152:155], a[48:51]\n" \
"ds_read_b64_tr_b16 v[146:147], v35 offset:41984\n" \
"ds_read_b128 v[108:111], v28 offset:38912\n" \
"v_mfma_f32_16x16x32_bf16 a[80:83], v[72:75], v[152:155], a[80:83]\n" \
"buffer_load_dwordx4 v23, s[64:67], s84 offen lds\n" \
"v_mfma_f32_16x16x32_bf16 a[80:83], v[68:71], v[148:151], a[80:83]\n" \
"s_add_u32 m0, %[mB], 0xc00\n" \
"s_add_u32 s85, s63, 0x18000\n" \
"v_mfma_f32_16x16x32_bf16 a[112:115], v[76:79], v[148:151], a[112:115]\n" \
"ds_read_b128 v[112:115], v29 offset:38912\n" \
"v_add_u32_e64 v231, s97, v234\n" \
"v_mfma_f32_16x16x32_bf16 a[112:115], v[80:83], v[152:155], a[112:115]\n" \
"buffer_load_dwordx4 v27, s[68:71], s85 offen lds\n" \
"v_mfma_f32_16x16x32_bf16 a[20:23], v[56:59], v[160:163], a[20:23]\n" \
"s_add_u32 m0, %[mB], 0x4c00\n" \
"s_add_u32 s84, s63, 0x18000\n" \
"v_mfma_f32_16x16x32_bf16 a[20:23], v[52:55], v[156:159], a[20:23]\n" \
"v_lshrrev_b32_e32 v235, 5, v231\n" \
"v_lshrrev_b32_e32 v237, 4, v231\n" \
"v_mfma_f32_16x16x32_bf16 a[52:55], v[60:63], v[156:159], a[52:55]\n" \
"buffer_load_dwordx4 v27, s[72:75], s84 offen lds\n" \
"v_mfma_f32_16x16x32_bf16 a[52:55], v[64:67], v[160:163], a[52:55]\n" \
"v_mul_u32_u24_e32 v239, 0x480, v241\n" \
"v_lshlrev_b32_e32 v232, 4, v235\n" \
"v_mfma_f32_16x16x32_bf16 a[84:87], v[72:75], v[160:163], a[84:87]\n" \
"v_and_b32_e32 v228, 12, v231\n" \
"v_and_b32_e64 v238, 1, v237\n" \
"v_mfma_f32_16x16x32_bf16 a[84:87], v[68:71], v[156:159], a[84:87]\n" \
"v_add_u32_e32 v233, v232, v228\n" \
"v_lshlrev_b32_e32 v241, 1, v238\n" \
"v_mfma_f32_16x16x32_bf16 a[116:119], v[76:79], v[156:159], a[116:119]\n" \
"v_lshrrev_b32_e32 v236, 1, v231\n" \
"v_add_u32_e32 v240, v233, v241\n" \
"v_mfma_f32_16x16x32_bf16 a[116:119], v[80:83], v[160:163], a[116:119]\n" \
"s_mov_b32 m0, %[mS]\n" \
"s_mov_b32 s86, s8\n" \
"v_mfma_f32_16x16x32_bf16 a[24:27], v[56:59], v[168:171], a[24:27]\n" \
"s_mov_b32 s87, s9\n" \
"v_and_b32_e32 v245, 1, v236\n" \
"v_mfma_f32_16x16x32_bf16 a[24:27], v[52:55], v[164:167], a[24:27]\n" \
"v_add_u32_e32 v242, v240, v245\n" \
"v_add_u32_e32 v248, v239, v244\n" \
"v_mfma_f32_16x16x32_bf16 a[56:59], v[60:63], v[164:167], a[56:59]\n" \
"v_mul_u32_u24_e32 v246, 18, v242\n" \
"v_add_u32_e32 v243, 16, v246\n" \
"v_mfma_f32_16x16x32_bf16 a[56:59], v[64:67], v[168:171], a[56:59]\n" \
"v_and_b32_e64 v239, 1, v231\n" \
"v_add_u32_e32 v6, 0xfffff800, v248\n" \
"v_mfma_f32_16x16x32_bf16 a[88:91], v[72:75], v[168:171], a[88:91]\n" \
"v_add_u32_e64 v244, v243, v239\n" \
"v_mul_u32_u24_e32 v248, 0x480, v244\n" \
"v_mfma_f32_16x16x32_bf16 a[88:91], v[68:71], v[164:167], a[88:91]\n" \
"v_add_u32_e32 v215, v250, v219\n" \
"v_add_u32_e32 v217, v248, v247\n" \
"s_mov_b32 exec_lo, 0x0\n" \
"s_mov_b32 exec_hi, 0x3ffff0\n" \
"global_load_lds_dword v188, s[86:87]\n" \
"s_mov_b64 exec, -1\n" \
"v_mfma_f32_16x16x32_bf16 a[120:123], v[76:79], v[164:167], a[120:123]\n" \
"v_and_b32_e64 v214, 16, v20\n" \
"v_add_u32_e32 v7, 0xfffff400, v215\n" \
"v_mfma_f32_16x16x32_bf16 a[120:123], v[80:83], v[168:171], a[120:123]\n" \
"v_add_u32_e32 v9, v217, v214\n" \
"v_and_b32_e32 v216, 3, v212\n" \
"v_mfma_f32_16x16x32_bf16 a[28:31], v[56:59], v[176:179], a[28:31]\n" \
"v_lshlrev_b32_e32 v250, 3, v216\n" \
"v_mul_u32_u24_e32 v220, 0x120, v234\n" \
"v_mfma_f32_16x16x32_bf16 a[28:31], v[52:55], v[172:175], a[28:31]\n" \
"v_mul_u32_u24_e32 v223, 0x480, v222\n" \
"v_add_u32_e32 v10, v220, v250\n" \
"v_mfma_f32_16x16x32_bf16 a[60:63], v[60:63], v[172:175], a[60:63]\n" \
"v_add_u32_e32 v8, v223, v225\n" \
"s_lshl_b32 s99, %[w], 11\n" \
"v_mfma_f32_16x16x32_bf16 a[60:63], v[64:67], v[176:179], a[60:63]\n" \
"s_add_u32 s55, s98, s99\n" \
"s_nop 0\n" \
"v_mfma_f32_16x16x32_bf16 a[92:95], v[72:75], v[176:179], a[92:95]\n" \
"v_mfma_f32_16x16x32_bf16 a[92:95], v[68:71], v[172:175], a[92:95]\n" \
"v_mfma_f32_16x16x32_bf16 a[124:127], v[76:79], v[172:175], a[124:127]\n" \
"v_mfma_f32_16x16x32_bf16 a[124:127], v[80:83], v[176:179], a[124:127]\n" \
"v_mfma_f32_16x16x32_bf16 a[0:3], v[120:123], v[56:59], a[0:3]\n" \
"v_mfma_f32_16x16x32_bf16 a[0:3], v[116:119], v[52:55], a[0:3]\n" \
"v_mfma_f32_16x16x32_bf16 a[32:35], v[116:119], v[60:63], a[32:35]\n" \
"v_mfma_f32_16x16x32_bf16 a[32:35], v[120:123], v[64:67], a[32:35]\n" \
"v_mfma_f32_16x16x32_bf16 a[64:67], v[120:123], v[72:75], a[64:67]\n" \
"v_mfma_f32_16x16x32_bf16 a[64:67], v[116:119], v[68:71], a[64:67]\n" \
"v_mfma_f32_16x16x32_bf16 a[96:99], v[116:119], v[76:79], a[96:99]\n" \
"v_mfma_f32_16x16x32_bf16 a[96:99], v[120:123], v[80:83], a[96:99]\n" \
"v_mfma_f32_16x16x32_bf16 a[4:7], v[128:131], v[56:59], a[4:7]\n" \
"v_mfma_f32_16x16x32_bf16 a[4:7], v[124:127], v[52:55], a[4:7]\n" \
"v_mfma_f32_16x16x32_bf16 a[36:39], v[124:127], v[60:63], a[36:39]\n" \
"v_mfma_f32_16x16x32_bf16 a[36:39], v[128:131], v[64:67], a[36:39]\n" \
"v_mfma_f32_16x16x32_bf16 a[68:71], v[128:131], v[72:75], a[68:71]\n" \
"v_mfma_f32_16x16x32_bf16 a[68:71], v[124:127], v[68:71], a[68:71]\n" \
"v_mfma_f32_16x16x32_bf16 a[100:103], v[124:127], v[76:79], a[100:103]\n" \
"v_mfma_f32_16x16x32_bf16 a[100:103], v[128:131], v[80:83], a[100:103]\n" \
"s_waitcnt lgkmcnt(0)\n" \
"s_nop 0\n" \
"v_mfma_f32_16x16x32_bf16 a[8:11], v[136:139], v[56:59], a[8:11]\n" \
"v_mfma_f32_16x16x32_bf16 a[8:11], v[132:135], v[52:55], a[8:11]\n" \
"v_mfma_f32_16x16x32_bf16 a[40:43], v[132:135], v[60:63], a[40:43]\n" \
"v_mfma_f32_16x16x32_bf16 a[40:43], v[136:139], v[64:67], a[40:43]\n" \
"v_mfma_f32_16x16x32_bf16 a[72:75], v[136:139], v[72:75], a[72:75]\n" \
"v_mfma_f32_16x16x32_bf16 a[72:75], v[132:135], v[68:71], a[72:75]\n" \
"v_mfma_f32_16x16x32_bf16 a[104:107], v[132:135], v[76:79], a[104:107]\n" \
"v_mfma_f32_16x16x32_bf16 a[104:107], v[136:139], v[80:83], a[104:107]\n" \
"v_mfma_f32_16x16x32_bf16 a[12:15], v[144:147], v[56:59], a[12:15]\n" \
"s_add_u32 s62, s22, 0x380\n" \
"s_and_b32 s62, s62, 0x3ff\n" \
"v_mfma_f32_16x16x32_bf16 a[12:15], v[140:143], v[52:55], a[12:15]\n" \
"s_lshl_b32 s63, s62, 12\n" \
"v_mfma_f32_16x16x32_bf16 a[44:47], v[140:143], v[60:63], a[44:47]\n" \
"v_mfma_f32_16x16x32_bf16 a[44:47], v[144:147], v[64:67], a[44:47]\n" \
"v_mfma_f32_16x16x32_bf16 a[76:79], v[144:147], v[72:75], a[76:79]\n" \
"v_mfma_f32_16x16x32_bf16 a[76:79], v[140:143], v[68:71], a[76:79]\n" \
"v_mfma_f32_16x16x32_bf16 a[108:111], v[140:143], v[76:79], a[108:111]\n" \
"v_mfma_f32_16x16x32_bf16 a[108:111], v[144:147], v[80:83], a[108:111]\n" \
"s_waitcnt vmcnt(1)\n" \
"s_barrier\n" \
"s_mov_b64 exec, 1\n" \
"buffer_store_dword v45, off, s[80:83], s38 offset:64 sc1\n" \
"s_mov_b64 exec, -1\n" \
"v_mfma_f32_16x16x32_bf16 a[128:131], v[120:123], v[88:91], a[128:131]\n" \
"s_add_u32 m0, %[mA], 0x8000\n" \
"s_add_u32 s85, s62, 0x0\n" \
"v_mfma_f32_16x16x32_bf16 a[128:131], v[116:119], v[84:87], a[128:131]\n" \
"ds_read_b128 v[56:59], v29 offset:16384\n" \
"ds_read_b128 v[52:55], v28 offset:16384\n" \
"v_mfma_f32_16x16x32_bf16 a[160:163], v[116:119], v[92:95], a[160:163]\n" \
"buffer_load_dwordx4 v22, s[64:67], s85 offen lds\n" \
"v_mfma_f32_16x16x32_bf16 a[160:163], v[120:123], v[96:99], a[160:163]\n" \
"s_add_u32 m0, %[mA], 0x8400\n" \
"s_add_u32 s84, s62, 0x2000\n" \
"v_mfma_f32_16x16x32_bf16 a[192:195], v[120:123], v[104:107], a[192:195]\n" \
"ds_read_b128 v[60:63], v28 offset:18432\n" \
"ds_read_b128 v[64:67], v29 offset:18432\n" \
"v_mfma_f32_16x16x32_bf16 a[192:195], v[116:119], v[100:103], a[192:195]\n" \
"buffer_load_dwordx4 v23, s[64:67], s84 offen lds\n" \
"v_mfma_f32_16x16x32_bf16 a[224:227], v[116:119], v[108:111], a[224:227]\n" \
"ds_read_b64_tr_b16 v[116:117], v32 offset:0\n" \
"ds_read_b64_tr_b16 v[118:119], v32 offset:1024\n" \
"v_mfma_f32_16x16x32_bf16 a[224:227], v[120:123], v[112:115], a[224:227]\n" \
"s_add_u32 m0, %[mB], 0x8000\n" \
"s_add_u32 s85, s63, 0x0\n" \
"v_mfma_f32_16x16x32_bf16 a[132:135], v[128:131], v[88:91], a[132:135]\n" \
"buffer_load_dwordx4 v26, s[68:71], s85 offen lds\n" \
"v_mfma_f32_16x16x32_bf16 a[132:135], v[124:127], v[84:87], a[132:135]\n" \
"ds_read_b64_tr_b16 v[120:121], v32 offset:8192\n" \
"ds_read_b64_tr_b16 v[122:123], v32 offset:9216\n" \
"v_mfma_f32_16x16x32_bf16 a[164:167], v[124:127], v[92:95], a[164:167]\n" \
"ds_read_b128 v[72:75], v29 offset:20480\n" \
"ds_read_b128 v[68:71], v28 offset:20480\n" \
"v_mfma_f32_16x16x32_bf16 a[164:167], v[128:131], v[96:99], a[164:167]\n" \
"s_add_u32 m0, %[mB], 0xc000\n" \
"s_add_u32 s84, s63, 0x0\n" \
"v_mfma_f32_16x16x32_bf16 a[196:199], v[128:131], v[104:107], a[196:199]\n" \
"buffer_load_dwordx4 v26, s[72:75], s84 offen lds\n" \
"v_mfma_f32_16x16x32_bf16 a[196:199], v[124:127], v[100:103], a[196:199]\n" \
"ds_read_b128 v[76:79], v28 offset:22528\n" \
"ds_read_b128 v[80:83], v29 offset:22528\n" \
"v_mfma_f32_16x16x32_bf16 a[228:231], v[124:127], v[108:111], a[228:231]\n" \
"ds_read_b64_tr_b16 v[124:125], v33 offset:0\n" \
"ds_read_b64_tr_b16 v[126:127], v33 offset:1024\n" \
"v_mfma_f32_16x16x32_bf16 a[228:231], v[128:131], v[112:115], a[228:231]\n" \
"s_add_u32 m0, %[mA], 0x8800\n" \
"s_add_u32 s85, s62, 0x4000\n" \
"v_mfma_f32_16x16x32_bf16 a[136:139], v[136:139], v[88:91], a[136:139]\n" \
"buffer_load_dwordx4 v22, s[64:67], s85 offen lds\n" \
"v_mfma_f32_16x16x32_bf16 a[136:139], v[132:135], v[84:87], a[136:139]\n" \
"ds_read_b64_tr_b16 v[128:129], v33 offset:8192\n" \
"ds_read_b64_tr_b16 v[130:131], v33 offset:9216\n" \
"v_mfma_f32_16x16x32_bf16 a[168:171], v[132:135], v[92:95], a[168:171]\n" \
"v_mov_b32_e64 v45, %[tag]\n" \
"v_mfma_f32_16x16x32_bf16 a[168:171], v[136:139], v[96:99], a[168:171]\n" \
"s_add_u32 m0, %[mA], 0x8c00\n" \
"s_add_u32 s84, s62, 0x6000\n" \
"v_mfma_f32_16x16x32_bf16 a[200:203], v[136:139], v[104:107], a[200:203]\n" \
"buffer_load_dwordx4 v23, s[64:67], s84 offen lds\n" \
"v_mfma_f32_16x16x32_bf16 a[200:203], v[132:135], v[100:103], a[200:203]\n" \
"v_mfma_f32_16x16x32_bf16 a[232:235], v[132:135], v[108:111], a[232:235]\n" \
"ds_read_b64_tr_b16 v[132:133], v34 offset:0\n" \
"ds_read_b64_tr_b16 v[134:135], v34 offset:1024\n" \
"v_mfma_f32_16x16x32_bf16 a[232:235], v[136:139], v[112:115], a[232:235]\n" \
"s_add_u32 m0, %[mB], 0x8400\n" \
"s_add_u32 s85, s63, 0x8000\n" \
"v_mfma_f32_16x16x32_bf16 a[140:143], v[144:147], v[88:91], a[140:143]\n" \
"buffer_load_dwordx4 v26, s[68:71], s85 offen lds\n" \
"v_mfma_f32_16x16x32_bf16 a[140:143], v[140:143], v[84:87], a[140:143]\n" \
"ds_read_b64_tr_b16 v[136:137], v34 offset:8192\n" \
"ds_read_b64_tr_b16 v[138:139], v34 offset:9216\n" \
"v_mfma_f32_16x16x32_bf16 a[172:175], v[140:143], v[92:95], a[172:175]\n" \
"v_mfma_f32_16x16x32_bf16 a[172:175], v[144:147], v[96:99], a[172:175]\n" \
"s_add_u32 m0, %[mB], 0xc400\n" \
"s_add_u32 s84, s63, 0x8000\n" \
"v_mfma_f32_16x16x32_bf16 a[204:207], v[144:147], v[104:107], a[204:207]\n" \
"buffer_load_dwordx4 v26, s[72:75], s84 offen lds\n" \
"v_mfma_f32_16x16x32_bf16 a[204:207], v[140:143], v[100:103], a[204:207]\n" \
"v_mfma_f32_16x16x32_bf16 a[236:239], v[140:143], v[108:111], a[236:239]\n" \
"ds_read_b64_tr_b16 v[140:141], v35 offset:0\n" \
"ds_read_b64_tr_b16 v[142:143], v35 offset:1024\n" \
"v_mfma_f32_16x16x32_bf16 a[236:239], v[144:147], v[112:115], a[236:239]\n" \
"s_add_u32 m0, %[mA], 0x9000\n" \
"s_add_u32 s85, s62, 0x8000\n" \
"v_mfma_f32_16x16x32_bf16 a[144:147], v[88:91], v[152:155], a[144:147]\n" \
"buffer_load_dwordx4 v22, s[64:67], s85 offen lds\n" \
"v_mfma_f32_16x16x32_bf16 a[144:147], v[84:87], v[148:151], a[144:147]\n" \
"ds_read_b64_tr_b16 v[144:145], v35 offset:8192\n" \
"ds_read_b64_tr_b16 v[146:147], v35 offset:9216\n" \
"v_mfma_f32_16x16x32_bf16 a[176:179], v[92:95], v[148:151], a[176:179]\n" \
"v_mfma_f32_16x16x32_bf16 a[176:179], v[96:99], v[152:155], a[176:179]\n" \
"s_add_u32 m0, %[mA], 0x9400\n" \
"L_pf3:\n" \
"s_cbranch_execz L_pf7\n" \
"s_nop 0\n" \
"s_add_u32 s84, s62, 0xa000\n" \
"v_mfma_f32_16x16x32_bf16 a[208:211], v[104:107], v[152:155], a[208:211]\n" \
"buffer_load_dwordx4 v23, s[64:67], s84 offen lds\n" \
"v_mfma_f32_16x16x32_bf16 a[208:211], v[100:103], v[148:151], a[208:211]\n" \
"v_mfma_f32_16x16x32_bf16 a[240:243], v[108:111], v[148:151], a[240:243]\n" \
"ds_read_b64_tr_b16 v[148:149], v32 offset:16384\n" \
"ds_read_b64_tr_b16 v[150:151], v32 offset:17408\n" \
"v_mfma_f32_16x16x32_bf16 a[240:243], v[112:115], v[152:155], a[240:243]\n" \
"s_add_u32 m0, %[mB], 0x8800\n" \
"s_add_u32 s85, s63, 0x10000\n" \
"v_mfma_f32_16x16x32_bf16 a[148:151], v[88:91], v[160:163], a[148:151]\n" \
"buffer_load_dwordx4 v27, s[68:71], s85 offen lds\n" \
"v_mfma_f32_16x16x32_bf16 a[148:151], v[84:87], v[156:159], a[148:151]\n" \
"ds_read_b64_tr_b16 v[152:153], v32 offset:24576\n" \
"ds_read_b64_tr_b16 v[154:155], v32 offset:25600\n" \
"v_mfma_f32_16x16x32_bf16 a[180:183], v[92:95], v[156:159], a[180:183]\n" \
"v_mfma_f32_16x16x32_bf16 a[180:183], v[96:99], v[160:163], a[180:183]\n" \
"s_add_u32 m0, %[mB], 0xc800\n" \
"s_add_u32 s84, s63, 0x10000\n" \
"v_mfma_f32_16x16x32_bf16 a[212:215], v[104:107], v[160:163], a[212:215]\n" \
"buffer_load_dwordx4 v27, s[72:75], s84 offen lds\n" \
"v_mfma_f32_16x16x32_bf16 a[212:215], v[100:103], v[156:159], a[212:215]\n" \
"v_mfma_f32_16x16x32_bf16 a[244:247], v[108:111], v[156:159], a[244:247]\n" \
"ds_read_b64_tr_b16 v[156:157], v33 offset:16384\n" \
"ds_read_b64_tr_b16 v[158:159], v33 offset:17408\n" \
"v_mfma_f32_16x16x32_bf16 a[244:247], v[112:115], v[160:163], a[244:247]\n" \
"ds_read_b64_tr_b16 v[160:161], v33 offset:24576\n" \
"ds_read_b64_tr_b16 v[162:163], v33 offset:25600\n" \
"v_mfma_f32_16x16x32_bf16 a[152:155], v[88:91], v[168:171], a[152:155]\n" \
"v_mfma_f32_16x16x32_bf16 a[152:155], v[84:87], v[164:167], a[152:155]\n" \
"v_mfma_f32_16x16x32_bf16 a[184:187], v[92:95], v[164:167], a[184:187]\n" \
"v_mfma_f32_16x16x32_bf16 a[184:187], v[96:99], v[168:171], a[184:187]\n" \
"v_mfma_f32_16x16x32_bf16 a[216:219], v[104:107], v[168:171], a[216:219]\n" \
"v_mfma_f32_16x16x32_bf16 a[216:219], v[100:103], v[164:167], a[216:219]\n" \
"v_mfma_f32_16x16x32_bf16 a[248:251], v[108:111], v[164:167], a[248:251]\n" \
"ds_read_b64_tr_b16 v[164:165], v34 offset:16384\n" \
"ds_read_b64_tr_b16 v[166:167], v34 offset:17408\n" \
"v_mfma_f32_16x16x32_bf16 a[248:251], v[112:115], v[168:171], a[248:251]\n" \
"ds_read_b64_tr_b16 v[168:169], v34 offset:24576\n" \
"ds_read_b64_tr_b16 v[170:171], v34 offset:25600\n" \
"v_mfma_f32_16x16x32_bf16 a[156:159], v[88:91], v[176:179], a[156:159]\n" \
"ds_read_b128 v[88:91], v29 offset:0\n" \
"v_mfma_f32_16x16x32_bf16 a[156:159], v[84:87], v[172:175], a[156:159]\n" \
"ds_read_b128 v[84:87], v28 offset:0\n" \
"v_mfma_f32_16x16x32_bf16 a[188:191], v[92:95], v[172:175], a[188:191]\n" \
"ds_read_b128 v[92:95], v28 offset:2048\n" \
"v_mfma_f32_16x16x32_bf16 a[188:191], v[96:99], v[176:179], a[188:191]\n" \
"ds_read_b128 v[96:99], v29 offset:2048\n" \
"v_mfma_f32_16x16x32_bf16 a[220:223], v[104:107], v[176:179], a[220:223]\n" \
"ds_read_b128 v[104:107], v29 offset:4096\n" \
"v_mfma_f32_16x16x32_bf16 a[220:223], v[100:103], v[172:175], a[220:223]\n" \
"ds_read_b128 v[100:103], v28 offset:4096\n" \
"v_mfma_f32_16x16x32_bf16 a[252:255], v[108:111], v[172:175], a[252:255]\n" \
"ds_read_b64_tr_b16 v[172:173], v35 offset:16384\n" \
"ds_read_b64_tr_b16 v[174:175], v35 offset:17408\n" \
"v_mfma_f32_16x16x32_bf16 a[252:255], v[112:115], v[176:179], a[252:255]\n" \
"s_add_u32 m0, %[mA], 0x9800\n" \
"s_waitcnt lgkmcnt(12)\n" \
"s_nop 0\n" \
"v_mfma_f32_16x16x32_bf16 a[0:3], v[120:123], v[56:59], a[0:3]\n" \
"s_add_u32 s85, s62, 0xc000\n" \
"ds_read_b64_tr_b16 v[176:177], v35 offset:24576\n" \
"v_mfma_f32_16x16x32_bf16 a[0:3], v[116:119], v[52:55], a[0:3]\n" \
"buffer_load_dwordx4 v22, s[64:67], s85 offen lds\n" \
"v_mfma_f32_16x16x32_bf16 a[32:35], v[116:119], v[60:63], a[32:35]\n" \
"s_add_u32 m0, %[mA], 0x9c00\n" \
"s_add_u32 s84, s62, 0xe000\n" \
"v_mfma_f32_16x16x32_bf16 a[32:35], v[120:123], v[64:67], a[32:35]\n" \
"ds_read_b64_tr_b16 v[178:179], v35 offset:25600\n" \
"ds_read_b128 v[108:111], v28 offset:6144\n" \
"v_mfma_f32_16x16x32_bf16 a[64:67], v[120:123], v[72:75], a[64:67]\n" \
"buffer_load_dwordx4 v23, s[64:67], s84 offen lds\n" \
"v_mfma_f32_16x16x32_bf16 a[64:67], v[116:119], v[68:71], a[64:67]\n" \
"s_add_u32 m0, %[mB], 0x8c00\n" \
"s_add_u32 s85, s63, 0x18000\n" \
"v_mfma_f32_16x16x32_bf16 a[96:99], v[116:119], v[76:79], a[96:99]\n" \
"ds_read_b128 v[112:115], v29 offset:6144\n" \
"s_mov_b32 s62, s23\n" \
"v_mfma_f32_16x16x32_bf16 a[96:99], v[120:123], v[80:83], a[96:99]\n" \
"buffer_load_dwordx4 v27, s[68:71], s85 offen lds\n" \
"v_mfma_f32_16x16x32_bf16 a[4:7], v[128:131], v[56:59], a[4:7]\n" \
"s_add_u32 m0, %[mB], 0xcc00\n" \
"s_add_u32 s84, s63, 0x18000\n" \
"v_mfma_f32_16x16x32_bf16 a[4:7], v[124:127], v[52:55], a[4:7]\n" \
"s_mov_b32 s68, s10\n" \
"s_and_b32 s69, s11, 0xffff\n" \
"v_mfma_f32_16x16x32_bf16 a[36:39], v[124:127], v[60:63], a[36:39]\n" \
"buffer_load_dwordx4 v27, s[72:75], s84 offen lds\n" \
"s_mov_b32 s73, s52\n" \
"s_add_u32 s74, s52, 0x4000\n" \
"s_add_u32 s75, s52, 0x8000\n" \
"v_mfma_f32_16x16x32_bf16 a[36:39], v[128:131], v[64:67], a[36:39]\n" \
"s_lshl_b32 s72, s62, 6\n" \
"s_add_u32 s72, s72, s37\n" \
"v_mfma_f32_16x16x32_bf16 a[68:71], v[128:131], v[72:75], a[68:71]\n" \
"v_bfe_u32 v44, v20, 2, 5\n" \
"s_mov_b32 s70, 0x480000\n" \
"v_mfma_f32_16x16x32_bf16 a[68:71], v[124:127], v[68:71], a[68:71]\n" \
"buffer_load_dword v44, v44, s[80:83], s39 offen offset:64 sc1\n" \
"v_mfma_f32_16x16x32_bf16 a[100:103], v[124:127], v[76:79], a[100:103]\n" \
"v_add_u32_e32 v22, s61, v28\n" \
"v_add_u32_e64 v23, s61, v29\n" \
"v_mfma_f32_16x16x32_bf16 a[100:103], v[128:131], v[80:83], a[100:103]\n" \
"s_mov_b32 m0, %[mS]\n" \
"s_mov_b32 s86, s18\n" \
"v_mfma_f32_16x16x32_bf16 a[8:11], v[136:139], v[56:59], a[8:11]\n" \
"s_mov_b32 s87, s19\n" \
"v_mfma_f32_16x16x32_bf16 a[8:11], v[132:135], v[52:55], a[8:11]\n" \
"v_mfma_f32_16x16x32_bf16 a[40:43], v[132:135], v[60:63], a[40:43]\n" \
"v_mfma_f32_16x16x32_bf16 a[40:43], v[136:139], v[64:67], a[40:43]\n" \
"v_mfma_f32_16x16x32_bf16 a[72:75], v[136:139], v[72:75], a[72:75]\n" \
"v_mfma_f32_16x16x32_bf16 a[72:75], v[132:135], v[68:71], a[72:75]\n" \
"s_mov_b32 exec_lo, 0xfffc0000\n" \
"s_mov_b32 exec_hi, 0xf\n" \
"global_load_lds_dword v16, s[86:87]\n" \
"s_mov_b64 exec, -1\n" \
"v_mfma_f32_16x16x32_bf16 a[104:107], v[132:135], v[76:79], a[104:107]\n" \
"s_mov_b32 m0, %[mS]\n" \
"s_mov_b32 s86, s16\n" \
"v_mfma_f32_16x16x32_bf16 a[104:107], v[136:139], v[80:83], a[104:107]\n" \
"s_mov_b32 s87, s17\n" \
"v_mfma_f32_16x16x32_bf16 a[12:15], v[144:147], v[56:59], a[12:15]\n" \
"v_mfma_f32_16x16x32_bf16 a[12:15], v[140:143], v[52:55], a[12:15]\n" \
"v_mfma_f32_16x16x32_bf16 a[44:47], v[140:143], v[60:63], a[44:47]\n" \
"s_mov_b32 exec_lo, 0x0\n" \
"s_mov_b32 exec_hi, 0x3f0\n" \
"global_load_lds_dword v16, s[86:87]\n" \
"s_mov_b64 exec, -1\n" \
"v_mfma_f32_16x16x32_bf16 a[44:47], v[144:147], v[64:67], a[44:47]\n" \
"v_mfma_f32_16x16x32_bf16 a[76:79], v[144:147], v[72:75], a[76:79]\n" \
"v_mfma_f32_16x16x32_bf16 a[76:79], v[140:143], v[68:71], a[76:79]\n" \
"v_mfma_f32_16x16x32_bf16 a[108:111], v[140:143], v[76:79], a[108:111]\n" \
"v_mfma_f32_16x16x32_bf16 a[108:111], v[144:147], v[80:83], a[108:111]\n" \
"v_mfma_f32_16x16x32_bf16 a[16:19], v[56:59], v[152:155], a[16:19]\n" \
"v_mfma_f32_16x16x32_bf16 a[16:19], v[52:55], v[148:151], a[16:19]\n" \
"v_mfma_f32_16x16x32_bf16 a[48:51], v[60:63], v[148:151], a[48:51]\n" \
"v_mfma_f32_16x16x32_bf16 a[48:51], v[64:67], v[152:155], a[48:51]\n" \
"v_mfma_f32_16x16x32_bf16 a[80:83], v[72:75], v[152:155], a[80:83]\n" \
"v_mfma_f32_16x16x32_bf16 a[80:83], v[68:71], v[148:151], a[80:83]\n" \
"v_mfma_f32_16x16x32_bf16 a[112:115], v[76:79], v[148:151], a[112:115]\n" \
"v_mfma_f32_16x16x32_bf16 a[112:115], v[80:83], v[152:155], a[112:115]\n" \
"v_mfma_f32_16x16x32_bf16 a[20:23], v[56:59], v[160:163], a[20:23]\n" \
"v_mfma_f32_16x16x32_bf16 a[20:23], v[52:55], v[156:159], a[20:23]\n" \
"v_mfma_f32_16x16x32_bf16 a[52:55], v[60:63], v[156:159], a[52:55]\n" \
"v_mfma_f32_16x16x32_bf16 a[52:55], v[64:67], v[160:163], a[52:55]\n" \
"v_mfma_f32_16x16x32_bf16 a[84:87], v[72:75], v[160:163], a[84:87]\n" \
"v_mfma_f32_16x16x32_bf16 a[84:87], v[68:71], v[156:159], a[84:87]\n" \
"v_mfma_f32_16x16x32_bf16 a[116:119], v[76:79], v[156:159], a[116:119]\n" \
"v_mfma_f32_16x16x32_bf16 a[116:119], v[80:83], v[160:163], a[116:119]\n" \
"s_waitcnt lgkmcnt(0)\n" \
"v_mfma_f32_16x16x32_bf16 a[24:27], v[56:59], v[168:171], a[24:27]\n" \
"v_mfma_f32_16x16x32_bf16 a[24:27], v[52:55], v[164:167], a[24:27]\n" \
"v_mfma_f32_16x16x32_bf16 a[56:59], v[60:63], v[164:167], a[56:59]\n" \
"v_mfma_f32_16x16x32_bf16 a[56:59], v[64:67], v[168:171], a[56:59]\n" \
"v_mfma_f32_16x16x32_bf16 a[88:91], v[72:75], v[168:171], a[88:91]\n" \
"v_mfma_f32_16x16x32_bf16 a[88:91], v[68:71], v[164:167], a[88:91]\n" \
"v_mfma_f32_16x16x32_bf16 a[120:123], v[76:79], v[164:167], a[120:123]\n" \
"v_mfma_f32_16x16x32_bf16 a[120:123], v[80:83], v[168:171], a[120:123]\n" \
"v_mfma_f32_16x16x32_bf16 a[28:31], v[56:59], v[176:179], a[28:31]\n" \
"s_waitcnt vmcnt(2)\n" \
"v_cmp_ne_u32_e32 vcc, %[tag], v44\n" \
"v_mfma_f32_16x16x32_bf16 a[28:31], v[52:55], v[172:175], a[28:31]\n" \
"v_mfma_f32_16x16x32_bf16 a[60:63], v[60:63], v[172:175], a[60:63]\n" \
"v_mfma_f32_16x16x32_bf16 a[60:63], v[64:67], v[176:179], a[60:63]\n" \
"v_mfma_f32_16x16x32_bf16 a[92:95], v[72:75], v[176:179], a[92:95]\n" \
"v_mfma_f32_16x16x32_bf16 a[92:95], v[68:71], v[172:175], a[92:95]\n" \
"s_cbranch_vccnz L_pl_%=\n" \
"L_rl_%=:\n" \
"s_lshl_b32 s63, s62, 12\n" \
"v_mfma_f32_16x16x32_bf16 a[124:127], v[76:79], v[172:175], a[124:127]\n" \
"v_mfma_f32_16x16x32_bf16 a[124:127], v[80:83], v[176:179], a[124:127]\n" \
"s_barrier\n" \
"v_mfma_f32_16x16x32_bf16 a[144:147], v[88:91], v[152:155], a[144:147]\n" \
"s_add_u32 m0, s40, 0x0\n" \
"s_add_u32 s85, s72, 0x0\n" \
"v_mfma_f32_16x16x32_bf16 a[144:147], v[84:87], v[148:151], a[144:147]\n" \
"buffer_load_dwordx4 v20, s[80:83], s85 offen sc1 lds\n" \
"v_mfma_f32_16x16x32_bf16 a[176:179], v[92:95], v[148:151], a[176:179]\n" \
"s_add_u32 m0, s40, 0x1000\n" \
"s_add_u32 s84, s72, 0x1000\n" \
"v_mfma_f32_16x16x32_bf16 a[176:179], v[96:99], v[152:155], a[176:179]\n" \
"ds_read_b128 v[56:59], v29 offset:49152\n" \
"ds_read_b128 v[52:55], v28 offset:49152\n" \
"v_mfma_f32_16x16x32_bf16 a[208:211], v[104:107], v[152:155], a[208:211]\n" \
"buffer_load_dwordx4 v20, s[80:83], s84 offen sc1 lds\n" \
"v_mfma_f32_16x16x32_bf16 a[208:211], v[100:103], v[148:151], a[208:211]\n" \
"ds_read_b128 v[60:63], v28 offset:51200\n" \
"ds_read_b128 v[64:67], v29 offset:51200\n" \
"v_mfma_f32_16x16x32_bf16 a[240:243], v[108:111], v[148:151], a[240:243]\n" \
"s_add_u32 m0, s40, 0x3000\n" \
"s_add_u32 s85, s62, 0x0\n" \
"v_mfma_f32_16x16x32_bf16 a[240:243], v[112:115], v[152:155], a[240:243]\n" \
"buffer_load_dwordx4 v21, s[76:79], s85 offen lds\n" \
"v_mfma_f32_16x16x32_bf16 a[148:151], v[88:91], v[160:163], a[148:151]\n" \
"ds_read_b64_tr_b16 v[152:153], v32 offset:57344\n" \
"ds_read_b64_tr_b16 v[154:155], v32 offset:58368\n" \
"v_mfma_f32_16x16x32_bf16 a[148:151], v[84:87], v[156:159], a[148:151]\n" \
"ds_read_b64_tr_b16 v[148:149], v32 offset:49152\n" \
"ds_read_b64_tr_b16 v[150:151], v32 offset:50176\n" \
"v_mfma_f32_16x16x32_bf16 a[180:183], v[92:95], v[156:159], a[180:183]\n" \
"s_add_u32 m0, %[mA], 0x0\n" \
"s_add_u32 s84, s62, 0x0\n" \
"s_nop 0\n" \
"v_mfma_f32_16x16x32_bf16 a[180:183], v[96:99], v[160:163], a[180:183]\n" \
"buffer_load_dwordx4 v24, s[76:79], s84 offen lds\n" \
"v_mfma_f32_16x16x32_bf16 a[212:215], v[104:107], v[160:163], a[212:215]\n" \
"ds_read_b128 v[72:75], v29 offset:53248\n" \
"ds_read_b128 v[68:71], v28 offset:53248\n" \
"v_mfma_f32_16x16x32_bf16 a[212:215], v[100:103], v[156:159], a[212:215]\n" \
"s_add_u32 m0, %[mA], 0x400\n" \
"s_add_u32 s85, s62, 0x2400\n" \
"v_mfma_f32_16x16x32_bf16 a[244:247], v[108:111], v[156:159], a[244:247]\n" \
"buffer_load_dwordx4 v25, s[76:79], s85 offen lds\n" \
"v_mfma_f32_16x16x32_bf16 a[244:247], v[112:115], v[160:163], a[244:247]\n" \
"ds_read_b128 v[76:79], v28 offset:55296\n" \
"ds_read_b128 v[80:83], v29 offset:55296\n" \
"v_mfma_f32_16x16x32_bf16 a[152:155], v[88:91], v[168:171], a[152:155]\n" \
"ds_read_b64_tr_b16 v[160:161], v33 offset:57344\n" \
"ds_read_b64_tr_b16 v[162:163], v33 offset:58368\n" \
"v_mfma_f32_16x16x32_bf16 a[152:155], v[84:87], v[164:167], a[152:155]\n" \
"s_add_u32 m0, %[mB], 0x0\n" \
"s_add_u32 s84, s63, 0x0\n" \
"v_mfma_f32_16x16x32_bf16 a[184:187], v[92:95], v[164:167], a[184:187]\n" \
"buffer_load_dwordx4 v26, s[68:71], s84 offen lds\n" \
"v_mfma_f32_16x16x32_bf16 a[184:187], v[96:99], v[168:171], a[184:187]\n" \
"ds_read_b64_tr_b16 v[156:157], v33 offset:49152\n" \
"ds_read_b64_tr_b16 v[158:159], v33 offset:50176\n" \
"v_mfma_f32_16x16x32_bf16 a[216:219], v[104:107], v[168:171], a[216:219]\n" \
"v_mfma_f32_16x16x32_bf16 a[216:219], v[100:103], v[164:167], a[216:219]\n" \
"s_add_u32 m0, %[mA], 0x800\n" \
"s_add_u32 s85, s62, 0x4800\n" \
"v_mfma_f32_16x16x32_bf16 a[248:251], v[108:111], v[164:167], a[248:251]\n" \
"buffer_load_dwordx4 v24, s[76:79], s85 offen lds\n" \
"v_mfma_f32_16x16x32_bf16 a[248:251], v[112:115], v[168:171], a[248:251]\n" \
"ds_read_b64_tr_b16 v[168:169], v34 offset:57344\n" \
"ds_read_b64_tr_b16 v[170:171], v34 offset:58368\n" \
"v_mfma_f32_16x16x32_bf16 a[156:159], v[88:91], v[176:179], a[156:159]\n" \
"s_add_u32 m0, %[mA], 0xc00\n" \
"s_add_u32 s84, s62, 0x6c00\n" \
"v_mfma_f32_16x16x32_bf16 a[156:159], v[84:87], v[172:175], a[156:159]\n" \
"buffer_load_dwordx4 v25, s[76:79], s84 offen lds\n" \
"v_mfma_f32_16x16x32_bf16 a[188:191], v[92:95], v[172:175], a[188:191]\n" \
"ds_read_b64_tr_b16 v[164:165], v34 offset:49152\n" \
"ds_read_b64_tr_b16 v[166:167], v34 offset:50176\n" \
"v_mfma_f32_16x16x32_bf16 a[188:191], v[96:99], v[176:179], a[188:191]\n" \
"v_mfma_f32_16x16x32_bf16 a[220:223], v[104:107], v[176:179], a[220:223]\n" \
"s_add_u32 m0, %[mB], 0x400\n" \
"s_add_u32 s85, s63, 0x8000\n" \
"v_mfma_f32_16x16x32_bf16 a[220:223], v[100:103], v[172:175], a[220:223]\n" \
"buffer_load_dwordx4 v26, s[68:71], s85 offen lds\n" \
"v_mfma_f32_16x16x32_bf16 a[252:255], v[108:111], v[172:175], a[252:255]\n" \
"ds_read_b64_tr_b16 v[172:173], v35 offset:49152\n" \
"ds_read_b64_tr_b16 v[174:175], v35 offset:50176\n" \
"v_mfma_f32_16x16x32_bf16 a[252:255], v[112:115], v[176:179], a[252:255]\n" \
"ds_read_b64_tr_b16 v[176:177], v35 offset:57344\n" \
"ds_read_b64_tr_b16 v[178:179], v35 offset:58368\n" \
"v_mfma_f32_16x16x32_bf16 a[128:131], v[120:123], v[88:91], a[128:131]\n" \
"s_add_u32 m0, %[mA], 0x1000\n" \
"s_add_u32 s84, s62, 0x9000\n" \
"v_mfma_f32_16x16x32_bf16 a[128:131], v[116:119], v[84:87], a[128:131]\n" \
"buffer_load_dwordx4 v24, s[76:79], s84 offen lds\n" \
"v_mfma_f32_16x16x32_bf16 a[160:163], v[116:119], v[92:95], a[160:163]\n" \
"v_mfma_f32_16x16x32_bf16 a[160:163], v[120:123], v[96:99], a[160:163]\n" \
"s_add_u32 m0, %[mA], 0x1400\n" \
"s_add_u32 s85, s62, 0xb400\n" \
"v_mfma_f32_16x16x32_bf16 a[192:195], v[120:123], v[104:107], a[192:195]\n" \
"buffer_load_dwordx4 v25, s[76:79], s85 offen lds\n" \
"v_mfma_f32_16x16x32_bf16 a[192:195], v[116:119], v[100:103], a[192:195]\n" \
"v_mfma_f32_16x16x32_bf16 a[224:227], v[116:119], v[108:111], a[224:227]\n" \
"ds_read_b64_tr_b16 v[116:117], v32 offset:32768\n" \
"ds_read_b64_tr_b16 v[118:119], v32 offset:33792\n" \
"v_mfma_f32_16x16x32_bf16 a[224:227], v[120:123], v[112:115], a[224:227]\n" \
"s_add_u32 m0, %[mB], 0x800\n" \
"s_add_u32 s84, s63, 0x10000\n" \
"v_mfma_f32_16x16x32_bf16 a[132:135], v[128:131], v[88:91], a[132:135]\n" \
"buffer_load_dwordx4 v27, s[68:71], s84 offen lds\n" \
"v_mfma_f32_16x16x32_bf16 a[132:135], v[124:127], v[84:87], a[132:135]\n" \
"ds_read_b64_tr_b16 v[120:121], v32 offset:40960\n" \
"ds_read_b64_tr_b16 v[122:123], v32 offset:41984\n" \
"v_mfma_f32_16x16x32_bf16 a[164:167], v[124:127], v[92:95], a[164:167]\n" \
"v_mfma_f32_16x16x32_bf16 a[164:167], v[128:131], v[96:99], a[164:167]\n" \
"s_add_u32 m0, %[mA], 0x1800\n" \
"s_add_u32 s85, s62, 0xd800\n" \
"v_mfma_f32_16x16x32_bf16 a[196:199], v[128:131], v[104:107], a[196:199]\n" \
"buffer_load_dwordx4 v24, s[76:79], s85 offen lds\n" \
"v_mfma_f32_16x16x32_bf16 a[196:199], v[124:127], v[100:103], a[196:199]\n" \
"v_mfma_f32_16x16x32_bf16 a[228:231], v[124:127], v[108:111], a[228:231]\n" \
"ds_read_b64_tr_b16 v[124:125], v33 offset:32768\n" \
"ds_read_b64_tr_b16 v[126:127], v33 offset:33792\n" \
"v_mfma_f32_16x16x32_bf16 a[228:231], v[128:131], v[112:115], a[228:231]\n" \
"ds_read_b64_tr_b16 v[128:129], v33 offset:40960\n" \
"ds_read_b64_tr_b16 v[130:131], v33 offset:41984\n" \
"v_mfma_f32_16x16x32_bf16 a[136:139], v[136:139], v[88:91], a[136:139]\n" \
"v_mfma_f32_16x16x32_bf16 a[136:139], v[132:135], v[84:87], a[136:139]\n" \
"v_mfma_f32_16x16x32_bf16 a[168:171], v[132:135], v[92:95], a[168:171]\n" \
"v_mfma_f32_16x16x32_bf16 a[168:171], v[136:139], v[96:99], a[168:171]\n" \
"v_mfma_f32_16x16x32_bf16 a[200:203], v[136:139], v[104:107], a[200:203]\n" \
"v_mfma_f32_16x16x32_bf16 a[200:203], v[132:135], v[100:103], a[200:203]\n" \
"v_mfma_f32_16x16x32_bf16 a[232:235], v[132:135], v[108:111], a[232:235]\n" \
"ds_read_b64_tr_b16 v[132:133], v34 offset:32768\n" \
"ds_read_b64_tr_b16 v[134:135], v34 offset:33792\n" \
"v_mfma_f32_16x16x32_bf16 a[232:235], v[136:139], v[112:115], a[232:235]\n" \
"ds_read_b64_tr_b16 v[136:137], v34 offset:40960\n" \
"ds_read_b64_tr_b16 v[138:139], v34 offset:41984\n" \
"v_mfma_f32_16x16x32_bf16 a[140:143], v[144:147], v[88:91], a[140:143]\n" \
"ds_read_b128 v[88:91], v29 offset:32768\n" \
"v_mfma_f32_16x16x32_bf16 a[140:143], v[140:143], v[84:87], a[140:143]\n" \
"ds_read_b128 v[84:87], v28 offset:32768\n" \
"v_mfma_f32_16x16x32_bf16 a[172:175], v[140:143], v[92:95], a[172:175]\n" \
"ds_read_b128 v[92:95], v28 offset:34816\n" \
"v_mfma_f32_16x16x32_bf16 a[172:175], v[144:147], v[96:99], a[172:175]\n" \
"ds_read_b128 v[96:99], v29 offset:34816\n" \
"v_mfma_f32_16x16x32_bf16 a[204:207], v[144:147], v[104:107], a[204:207]\n" \
"ds_read_b128 v[104:107], v29 offset:36864\n" \
"v_mfma_f32_16x16x32_bf16 a[204:207], v[140:143], v[100:103], a[204:207]\n" \
"ds_read_b128 v[100:103], v28 offset:36864\n" \
"v_mfma_f32_16x16x32_bf16 a[236:239], v[140:143], v[108:111], a[236:239]\n" \
"ds_read_b64_tr_b16 v[140:141], v35 offset:32768\n" \
"ds_read_b64_tr_b16 v[142:143], v35 offset:33792\n" \
"v_mfma_f32_16x16x32_bf16 a[236:239], v[144:147], v[112:115], a[236:239]\n" \
"s_add_u32 m0, %[mA], 0x1c00\n" \
"s_waitcnt lgkmcnt(12)\n" \
"s_nop 0\n" \
"v_mfma_f32_16x16x32_bf16 a[16:19], v[56:59], v[152:155], a[16:19]\n" \
"s_add_u32 s85, s62, 0xfc00\n" \
"ds_read_b64_tr_b16 v[144:145], v35 offset:40960\n" \
"v_mfma_f32_16x16x32_bf16 a[16:19], v[52:55], v[148:151], a[16:19]\n" \
"buffer_load_dwordx4 v25, s[76:79], s85 offen lds\n" \
"v_mfma_f32_16x16x32_bf16 a[48:51], v[60:63], v[148:151], a[48:51]\n" \
"s_add_u32 m0, %[mB], 0xc00\n" \
"s_add_u32 s84, s63, 0x18000\n" \
"v_mfma_f32_16x16x32_bf16 a[48:51], v[64:67], v[152:155], a[48:51]\n" \
"ds_read_b64_tr_b16 v[146:147], v35 offset:41984\n" \
"ds_read_b128 v[108:111], v28 offset:38912\n" \
"v_mfma_f32_16x16x32_bf16 a[80:83], v[72:75], v[152:155], a[80:83]\n" \
"buffer_load_dwordx4 v27, s[68:71], s84 offen lds\n" \
"v_mfma_f32_16x16x32_bf16 a[80:83], v[68:71], v[148:151], a[80:83]\n" \
"ds_read_b128 v[112:115], v29 offset:38912\n" \
"s_add_u32 s62, s62, 0x80\n" \
"v_mfma_f32_16x16x32_bf16 a[112:115], v[76:79], v[148:151], a[112:115]\n" \
"s_cmp_lt_u32 s62, 0x480\n" \
"s_cselect_b32 s62, s62, 0\n" \
"v_mfma_f32_16x16x32_bf16 a[112:115], v[80:83], v[152:155], a[112:115]\n" \
"s_lshl_b32 s72, s62, 6\n" \
"s_add_u32 s72, s72, s37\n" \
"v_mfma_f32_16x16x32_bf16 a[20:23], v[56:59], v[160:163], a[20:23]\n" \
"s_lshl_b32 s63, s62, 12\n" \
"v_add_u32_e64 v28, s59, v28\n" \
"v_mfma_f32_16x16x32_bf16 a[20:23], v[52:55], v[156:159], a[20:23]\n" \
"v_add_u32_e64 v29, s59, v29\n" \
"v_accvgpr_read_b32 v184, a16\n" \
"v_mfma_f32_16x16x32_bf16 a[52:55], v[60:63], v[156:159], a[52:55]\n" \
"v_accvgpr_read_b32 v185, a17\n" \
"v_accvgpr_read_b32 v186, a18\n" \
"v_mfma_f32_16x16x32_bf16 a[52:55], v[64:67], v[160:163], a[52:55]\n" \
"v_accvgpr_read_b32 v187, a19\n" \
"v_accvgpr_write_b32 a16, 0\n" \
"v_mfma_f32_16x16x32_bf16 a[84:87], v[72:75], v[160:163], a[84:87]\n" \
"v_accvgpr_write_b32 a17, 0\n" \
"v_accvgpr_write_b32 a18, 0\n" \
"v_mfma_f32_16x16x32_bf16 a[84:87], v[68:71], v[156:159], a[84:87]\n" \
"v_accvgpr_write_b32 a19, 0\n" \
"v_accvgpr_read_b32 v180, a48\n" \
"v_mfma_f32_16x16x32_bf16 a[116:119], v[76:79], v[156:159], a[116:119]\n" \
"v_accvgpr_read_b32 v181, a49\n" \
"v_accvgpr_read_b32 v182, a50\n" \
"v_mfma_f32_16x16x32_bf16 a[116:119], v[80:83], v[160:163], a[116:119]\n" \
"v_accvgpr_read_b32 v183, a51\n" \
"v_accvgpr_read_b32 v188, a80\n" \
"v_mfma_f32_16x16x32_bf16 a[24:27], v[56:59], v[168:171], a[24:27]\n" \
"v_accvgpr_read_b32 v189, a81\n" \
"v_accvgpr_read_b32 v190, a82\n" \
"v_mfma_f32_16x16x32_bf16 a[24:27], v[52:55], v[164:167], a[24:27]\n" \
"v_accvgpr_read_b32 v191, a83\n" \
"v_cvt_pk_bf16_f32 v184, v184, v185\n" \
"v_mfma_f32_16x16x32_bf16 a[56:59], v[60:63], v[164:167], a[56:59]\n" \
"v_cvt_pk_bf16_f32 v185, v186, v187\n" \
"v_cvt_pk_bf16_f32 v186, v180, v181\n" \
"v_mfma_f32_16x16x32_bf16 a[56:59], v[64:67], v[168:171], a[56:59]\n" \
"v_cvt_pk_bf16_f32 v187, v182, v183\n" \
"v_accvgpr_read_b32 v180, a112\n" \
"v_mfma_f32_16x16x32_bf16 a[88:91], v[72:75], v[168:171], a[88:91]\n" \
"v_accvgpr_read_b32 v181, a113\n" \
"v_accvgpr_read_b32 v182, a114\n" \
"v_mfma_f32_16x16x32_bf16 a[88:91], v[68:71], v[164:167], a[88:91]\n" \
"v_accvgpr_read_b32 v183, a115\n" \
"v_accvgpr_write_b32 a112, 0\n" \
"v_mfma_f32_16x16x32_bf16 a[120:123], v[76:79], v[164:167], a[120:123]\n" \
"v_accvgpr_write_b32 a113, 0\n" \
"v_accvgpr_write_b32 a114, 0\n" \
"v_mfma_f32_16x16x32_bf16 a[120:123], v[80:83], v[168:171], a[120:123]\n" \
"v_accvgpr_write_b32 a115, 0\n" \
"v_accvgpr_read_b32 v192, a20\n" \
"v_mfma_f32_16x16x32_bf16 a[28:31], v[56:59], v[176:179], a[28:31]\n" \
"v_accvgpr_read_b32 v193, a21\n" \
"v_accvgpr_read_b32 v194, a22\n" \
"v_mfma_f32_16x16x32_bf16 a[28:31], v[52:55], v[172:175], a[28:31]\n" \
"v_accvgpr_read_b32 v195, a23\n" \
"v_cvt_pk_bf16_f32 v188, v188, v189\n" \
"v_mfma_f32_16x16x32_bf16 a[60:63], v[60:63], v[172:175], a[60:63]\n" \
"v_cvt_pk_bf16_f32 v189, v190, v191\n" \
"v_cvt_pk_bf16_f32 v190, v180, v181\n" \
"v_mfma_f32_16x16x32_bf16 a[60:63], v[64:67], v[176:179], a[60:63]\n" \
"v_cvt_pk_bf16_f32 v191, v182, v183\n" \
"v_accvgpr_read_b32 v180, a52\n" \
"v_mfma_f32_16x16x32_bf16 a[92:95], v[72:75], v[176:179], a[92:95]\n" \
"v_accvgpr_read_b32 v181, a53\n" \
"v_accvgpr_read_b32 v182, a54\n" \
"v_mfma_f32_16x16x32_bf16 a[92:95], v[68:71], v[172:175], a[92:95]\n" \
"v_accvgpr_read_b32 v183, a55\n" \
"v_accvgpr_read_b32 v196, a84\n" \
"v_mfma_f32_16x16x32_bf16 a[124:127], v[76:79], v[172:175], a[124:127]\n" \
"v_accvgpr_read_b32 v197, a85\n" \
"v_accvgpr_read_b32 v198, a86\n" \
"v_mfma_f32_16x16x32_bf16 a[124:127], v[80:83], v[176:179], a[124:127]\n" \
"v_accvgpr_read_b32 v199, a87\n" \
"v_cvt_pk_bf16_f32 v192, v192, v193\n" \
"v_mfma_f32_16x16x32_bf16 a[0:3], v[120:123], v[56:59], a[0:3]\n" \
"v_cvt_pk_bf16_f32 v193, v194, v195\n" \
"v_cvt_pk_bf16_f32 v194, v180, v181\n" \
"v_mfma_f32_16x16x32_bf16 a[0:3], v[116:119], v[52:55], a[0:3]\n" \
"v_cvt_pk_bf16_f32 v195, v182, v183\n" \
"v_accvgpr_read_b32 v180, a116\n" \
"v_mfma_f32_16x16x32_bf16 a[32:35], v[116:119], v[60:63], a[32:35]\n" \
"v_accvgpr_read_b32 v181, a117\n" \
"v_accvgpr_read_b32 v182, a118\n" \
"v_mfma_f32_16x16x32_bf16 a[32:35], v[120:123], v[64:67], a[32:35]\n" \
"v_accvgpr_read_b32 v183, a119\n" \
"v_accvgpr_write_b32 a116, 0\n" \
"v_mfma_f32_16x16x32_bf16 a[64:67], v[120:123], v[72:75], a[64:67]\n" \
"v_accvgpr_write_b32 a117, 0\n" \
"v_accvgpr_write_b32 a118, 0\n" \
"v_mfma_f32_16x16x32_bf16 a[64:67], v[116:119], v[68:71], a[64:67]\n" \
"v_accvgpr_write_b32 a119, 0\n" \
"v_accvgpr_read_b32 v200, a24\n" \
"v_mfma_f32_16x16x32_bf16 a[96:99], v[116:119], v[76:79], a[96:99]\n" \
"v_accvgpr_read_b32 v201, a25\n" \
"v_accvgpr_read_b32 v202, a26\n" \
"v_mfma_f32_16x16x32_bf16 a[96:99], v[120:123], v[80:83], a[96:99]\n" \
"v_accvgpr_read_b32 v203, a27\n" \
"v_cvt_pk_bf16_f32 v196, v196, v197\n" \
"v_mfma_f32_16x16x32_bf16 a[4:7], v[128:131], v[56:59], a[4:7]\n" \
"v_cvt_pk_bf16_f32 v197, v198, v199\n" \
"v_cvt_pk_bf16_f32 v198, v180, v181\n" \
"v_mfma_f32_16x16x32_bf16 a[4:7], v[124:127], v[52:55], a[4:7]\n" \
"v_cvt_pk_bf16_f32 v199, v182, v183\n" \
"v_accvgpr_read_b32 v180, a56\n" \
"v_mfma_f32_16x16x32_bf16 a[36:39], v[124:127], v[60:63], a[36:39]\n" \
"v_accvgpr_read_b32 v181, a57\n" \
"v_accvgpr_read_b32 v182, a58\n" \
"v_mfma_f32_16x16x32_bf16 a[36:39], v[128:131], v[64:67], a[36:39]\n" \
"v_accvgpr_read_b32 v183, a59\n" \
"v_accvgpr_read_b32 v204, a88\n" \
"v_mfma_f32_16x16x32_bf16 a[68:71], v[128:131], v[72:75], a[68:71]\n" \
"v_accvgpr_read_b32 v205, a89\n" \
"v_accvgpr_read_b32 v206, a90\n" \
"v_mfma_f32_16x16x32_bf16 a[68:71], v[124:127], v[68:71], a[68:71]\n" \
"v_accvgpr_read_b32 v207, a91\n" \
"v_cvt_pk_bf16_f32 v200, v200, v201\n" \
"v_mfma_f32_16x16x32_bf16 a[100:103], v[124:127], v[76:79], a[100:103]\n" \
"v_cvt_pk_bf16_f32 v201, v202, v203\n" \
"v_cvt_pk_bf16_f32 v202, v180, v181\n" \
"v_mfma_f32_16x16x32_bf16 a[100:103], v[128:131], v[80:83], a[100:103]\n" \
"v_cvt_pk_bf16_f32 v203, v182, v183\n" \
"s_waitcnt lgkmcnt(0)\n" \
"s_nop 0\n" \
"v_mfma_f32_16x16x32_bf16 a[8:11], v[136:139], v[56:59], a[8:11]\n" \
"v_accvgpr_read_b32 v180, a120\n" \
"v_accvgpr_read_b32 v181, a121\n" \
"v_mfma_f32_16x16x32_bf16 a[8:11], v[132:135], v[52:55], a[8:11]\n" \
"v_accvgpr_read_b32 v182, a122\n" \
"v_accvgpr_read_b32 v183, a123\n" \
"v_mfma_f32_16x16x32_bf16 a[40:43], v[132:135], v[60:63], a[40:43]\n" \
"v_accvgpr_write_b32 a120, 0\n" \
"v_accvgpr_write_b32 a121, 0\n" \
"v_mfma_f32_16x16x32_bf16 a[40:43], v[136:139], v[64:67], a[40:43]\n" \
"v_accvgpr_write_b32 a122, 0\n" \
"v_accvgpr_write_b32 a123, 0\n" \
"v_mfma_f32_16x16x32_bf16 a[72:75], v[136:139], v[72:75], a[72:75]\n" \
"v_accvgpr_read_b32 v208, a28\n" \
"v_accvgpr_read_b32 v209, a29\n" \
"v_mfma_f32_16x16x32_bf16 a[72:75], v[132:135], v[68:71], a[72:75]\n" \
"v_accvgpr_read_b32 v210, a30\n" \
"v_accvgpr_read_b32 v211, a31\n" \
"v_mfma_f32_16x16x32_bf16 a[104:107], v[132:135], v[76:79], a[104:107]\n" \
"v_cvt_pk_bf16_f32 v204, v204, v205\n" \
"v_cvt_pk_bf16_f32 v205, v206, v207\n" \
"v_mfma_f32_16x16x32_bf16 a[104:107], v[136:139], v[80:83], a[104:107]\n" \
"v_cvt_pk_bf16_f32 v206, v180, v181\n" \
"v_cvt_pk_bf16_f32 v207, v182, v183\n" \
"v_mfma_f32_16x16x32_bf16 a[12:15], v[144:147], v[56:59], a[12:15]\n" \
"v_accvgpr_read_b32 v180, a60\n" \
"v_accvgpr_read_b32 v181, a61\n" \
"v_mfma_f32_16x16x32_bf16 a[12:15], v[140:143], v[52:55], a[12:15]\n" \
"v_accvgpr_read_b32 v182, a62\n" \
"v_accvgpr_read_b32 v183, a63\n" \
"v_mfma_f32_16x16x32_bf16 a[44:47], v[140:143], v[60:63], a[44:47]\n" \
"v_accvgpr_read_b32 v212, a92\n" \
"v_accvgpr_read_b32 v213, a93\n" \
"v_mfma_f32_16x16x32_bf16 a[44:47], v[144:147], v[64:67], a[44:47]\n" \
"v_accvgpr_read_b32 v214, a94\n" \
"v_accvgpr_read_b32 v215, a95\n" \
"v_mfma_f32_16x16x32_bf16 a[76:79], v[144:147], v[72:75], a[76:79]\n" \
"v_cvt_pk_bf16_f32 v208, v208, v209\n" \
"v_cvt_pk_bf16_f32 v209, v210, v211\n" \
"v_mfma_f32_16x16x32_bf16 a[76:79], v[140:143], v[68:71], a[76:79]\n" \
"v_cvt_pk_bf16_f32 v210, v180, v181\n" \
"v_cvt_pk_bf16_f32 v211, v182, v183\n" \
"v_mfma_f32_16x16x32_bf16 a[108:111], v[140:143], v[76:79], a[108:111]\n" \
"v_accvgpr_read_b32 v180, a124\n" \
"v_accvgpr_read_b32 v181, a125\n" \
"v_mfma_f32_16x16x32_bf16 a[108:111], v[144:147], v[80:83], a[108:111]\n" \
"v_accvgpr_read_b32 v182, a126\n" \
"v_accvgpr_read_b32 v183, a127\n" \
"s_waitcnt vmcnt(0)\n" \
"s_barrier\n" \
"v_mfma_f32_16x16x32_bf16 a[128:131], v[120:123], v[88:91], a[128:131]\n" \
"s_add_u32 m0, s40, 0x8000\n" \
"s_add_u32 s85, s72, 0x0\n" \
"s_nop 0\n" \
"v_mfma_f32_16x16x32_bf16 a[128:131], v[116:119], v[84:87], a[128:131]\n" \
"buffer_load_dwordx4 v20, s[80:83], s85 offen sc1 lds\n" \
"v_mfma_f32_16x16x32_bf16 a[160:163], v[116:119], v[92:95], a[160:163]\n" \
"s_add_u32 m0, s40, 0x9000\n" \
"s_add_u32 s84, s72, 0x1000\n" \
"v_mfma_f32_16x16x32_bf16 a[160:163], v[120:123], v[96:99], a[160:163]\n" \
"ds_read_b128 v[52:55], v28 offset:0\n" \
"ds_read_b128 v[56:59], v29 offset:0\n" \
"v_mfma_f32_16x16x32_bf16 a[192:195], v[120:123], v[104:107], a[192:195]\n" \
"buffer_load_dwordx4 v20, s[80:83], s84 offen sc1 lds\n" \
"v_mfma_f32_16x16x32_bf16 a[192:195], v[116:119], v[100:103], a[192:195]\n" \
"ds_read_b128 v[60:63], v28 offset:2048\n" \
"ds_read_b128 v[64:67], v29 offset:2048\n" \
"v_mfma_f32_16x16x32_bf16 a[224:227], v[116:119], v[108:111], a[224:227]\n" \
"L_pf4:\n" \
"s_cbranch_execz L_pf8\n" \
"s_nop 0\n" \
"s_add_u32 m0, s40, 0xb000\n" \
"s_add_u32 s85, s62, 0x0\n" \
"v_mfma_f32_16x16x32_bf16 a[224:227], v[120:123], v[112:115], a[224:227]\n" \
"buffer_load_dwordx4 v21, s[76:79], s85 offen lds\n" \
"v_mfma_f32_16x16x32_bf16 a[132:135], v[128:131], v[88:91], a[132:135]\n" \
"ds_read_b64_tr_b16 v[116:117], v36 offset:0\n" \
"ds_read_b64_tr_b16 v[118:119], v36 offset:512\n" \
"v_mfma_f32_16x16x32_bf16 a[132:135], v[124:127], v[84:87], a[132:135]\n" \
"ds_read_b64_tr_b16 v[120:121], v36 offset:4096\n" \
"ds_read_b64_tr_b16 v[122:123], v36 offset:4608\n" \
"v_mfma_f32_16x16x32_bf16 a[164:167], v[124:127], v[92:95], a[164:167]\n" \
"s_add_u32 m0, %[mA], 0x8000\n" \
"s_add_u32 s84, s62, 0x0\n" \
"v_mfma_f32_16x16x32_bf16 a[164:167], v[128:131], v[96:99], a[164:167]\n" \
"buffer_load_dwordx4 v24, s[76:79], s84 offen lds\n" \
"v_mfma_f32_16x16x32_bf16 a[196:199], v[128:131], v[104:107], a[196:199]\n" \
"ds_read_b128 v[68:71], v28 offset:4096\n" \
"ds_read_b128 v[72:75], v29 offset:4096\n" \
"v_mfma_f32_16x16x32_bf16 a[196:199], v[124:127], v[100:103], a[196:199]\n" \
"s_add_u32 m0, %[mA], 0x8400\n" \
"s_add_u32 s85, s62, 0x2400\n" \
"v_mfma_f32_16x16x32_bf16 a[228:231], v[124:127], v[108:111], a[228:231]\n" \
"buffer_load_dwordx4 v25, s[76:79], s85 offen lds\n" \
"v_mfma_f32_16x16x32_bf16 a[228:231], v[128:131], v[112:115], a[228:231]\n" \
"ds_read_b64_tr_b16 v[128:129], v37 offset:4096\n" \
"ds_read_b64_tr_b16 v[130:131], v37 offset:4608\n" \
"v_mfma_f32_16x16x32_bf16 a[136:139], v[136:139], v[88:91], a[136:139]\n" \
"ds_read_b64_tr_b16 v[124:125], v37 offset:0\n" \
"ds_read_b64_tr_b16 v[126:127], v37 offset:512\n" \
"v_mfma_f32_16x16x32_bf16 a[136:139], v[132:135], v[84:87], a[136:139]\n" \
"s_add_u32 m0, %[mB], 0x8000\n" \
"s_add_u32 s84, s63, 0x0\n" \
"s_nop 0\n" \
"v_mfma_f32_16x16x32_bf16 a[168:171], v[132:135], v[92:95], a[168:171]\n" \
"buffer_load_dwordx4 v26, s[68:71], s84 offen lds\n" \
"v_mfma_f32_16x16x32_bf16 a[168:171], v[136:139], v[96:99], a[168:171]\n" \
"ds_read_b128 v[76:79], v28 offset:6144\n" \
"ds_read_b128 v[80:83], v29 offset:6144\n" \
"v_mfma_f32_16x16x32_bf16 a[200:203], v[136:139], v[104:107], a[200:203]\n" \
"v_accvgpr_write_b32 a124, 0\n" \
"v_accvgpr_write_b32 a125, 0\n" \
"v_mfma_f32_16x16x32_bf16 a[200:203], v[132:135], v[100:103], a[200:203]\n" \
"s_add_u32 m0, %[mA], 0x8800\n" \
"s_add_u32 s85, s62, 0x4800\n" \
"v_mfma_f32_16x16x32_bf16 a[232:235], v[132:135], v[108:111], a[232:235]\n" \
"buffer_load_dwordx4 v24, s[76:79], s85 offen lds\n" \
"v_mfma_f32_16x16x32_bf16 a[232:235], v[136:139], v[112:115], a[232:235]\n" \
"ds_read_b64_tr_b16 v[132:133], v38 offset:0\n" \
"ds_read_b64_tr_b16 v[134:135], v38 offset:512\n" \
"v_mfma_f32_16x16x32_bf16 a[140:143], v[144:147], v[88:91], a[140:143]\n" \
"s_add_u32 m0, %[mA], 0x8c00\n" \
"s_add_u32 s84, s62, 0x6c00\n" \
"v_mfma_f32_16x16x32_bf16 a[140:143], v[140:143], v[84:87], a[140:143]\n" \
"buffer_load_dwordx4 v25, s[76:79], s84 offen lds\n" \
"v_mfma_f32_16x16x32_bf16 a[172:175], v[140:143], v[92:95], a[172:175]\n" \
"ds_read_b64_tr_b16 v[136:137], v38 offset:4096\n" \
"ds_read_b64_tr_b16 v[138:139], v38 offset:4608\n" \
"v_mfma_f32_16x16x32_bf16 a[172:175], v[144:147], v[96:99], a[172:175]\n" \
"v_accvgpr_write_b32 a126, 0\n" \
"v_accvgpr_write_b32 a127, 0\n" \
"v_mfma_f32_16x16x32_bf16 a[204:207], v[144:147], v[104:107], a[204:207]\n" \
"s_add_u32 m0, %[mB], 0x8400\n" \
"s_add_u32 s85, s63, 0x8000\n" \
"v_mfma_f32_16x16x32_bf16 a[204:207], v[140:143], v[100:103], a[204:207]\n" \
"buffer_load_dwordx4 v26, s[68:71], s85 offen lds\n" \
"v_mfma_f32_16x16x32_bf16 a[236:239], v[140:143], v[108:111], a[236:239]\n" \
"ds_read_b64_tr_b16 v[140:141], v39 offset:0\n" \
"ds_read_b64_tr_b16 v[142:143], v39 offset:960\n" \
"v_mfma_f32_16x16x32_bf16 a[236:239], v[144:147], v[112:115], a[236:239]\n" \
"v_accvgpr_read_b32 v216, a0\n" \
"v_accvgpr_read_b32 v217, a1\n" \
"v_mfma_f32_16x16x32_bf16 a[144:147], v[88:91], v[152:155], a[144:147]\n" \
"s_add_u32 m0, %[mA], 0x9000\n" \
"s_add_u32 s84, s62, 0x9000\n" \
"v_mfma_f32_16x16x32_bf16 a[144:147], v[84:87], v[148:151], a[144:147]\n" \
"buffer_load_dwordx4 v24, s[76:79], s84 offen lds\n" \
"v_mfma_f32_16x16x32_bf16 a[148:151], v[84:87], v[156:159], a[148:151]\n" \
"v_accvgpr_read_b32 v218, a2\n" \
"v_accvgpr_read_b32 v219, a3\n" \
"v_mfma_f32_16x16x32_bf16 a[148:151], v[88:91], v[160:163], a[148:151]\n" \
"s_add_u32 m0, %[mA], 0x9400\n" \
"s_add_u32 s85, s62, 0xb400\n" \
"v_mfma_f32_16x16x32_bf16 a[152:155], v[88:91], v[168:171], a[152:155]\n" \
"buffer_load_dwordx4 v25, s[76:79], s85 offen lds\n" \
"v_mfma_f32_16x16x32_bf16 a[152:155], v[84:87], v[164:167], a[152:155]\n" \
"v_cvt_pk_bf16_f32 v212, v212, v213\n" \
"v_cvt_pk_bf16_f32 v213, v214, v215\n" \
"v_mfma_f32_16x16x32_bf16 a[156:159], v[84:87], v[172:175], a[156:159]\n" \
"ds_read_b128 v[84:87], v22 offset:0\n" \
"v_cvt_pk_bf16_f32 v214, v180, v181\n" \
"v_mfma_f32_16x16x32_bf16 a[156:159], v[88:91], v[176:179], a[156:159]\n" \
"s_add_u32 m0, %[mB], 0x8800\n" \
"s_add_u32 s84, s63, 0x10000\n" \
"v_mfma_f32_16x16x32_bf16 a[176:179], v[96:99], v[152:155], a[176:179]\n" \
"buffer_load_dwordx4 v27, s[68:71], s84 offen lds\n" \
"v_mfma_f32_16x16x32_bf16 a[176:179], v[92:95], v[148:151], a[176:179]\n" \
"ds_read_b128 v[88:91], v23 offset:0\n" \
"v_cvt_pk_bf16_f32 v215, v182, v183\n" \
"v_mfma_f32_16x16x32_bf16 a[180:183], v[92:95], v[156:159], a[180:183]\n" \
"v_accvgpr_read_b32 v180, a4\n" \
"v_accvgpr_read_b32 v181, a5\n" \
"v_mfma_f32_16x16x32_bf16 a[180:183], v[96:99], v[160:163], a[180:183]\n" \
"s_add_u32 m0, %[mA], 0x9800\n" \
"s_add_u32 s85, s62, 0xd800\n" \
"v_mfma_f32_16x16x32_bf16 a[184:187], v[96:99], v[168:171], a[184:187]\n" \
"buffer_load_dwordx4 v24, s[76:79], s85 offen lds\n" \
"v_mfma_f32_16x16x32_bf16 a[184:187], v[92:95], v[164:167], a[184:187]\n" \
"v_accvgpr_read_b32 v182, a6\n" \
"v_accvgpr_read_b32 v183, a7\n" \
"v_mfma_f32_16x16x32_bf16 a[188:191], v[92:95], v[172:175], a[188:191]\n" \
"v_cvt_pk_bf16_f32 v216, v216, v217\n" \
"v_cvt_pk_bf16_f32 v217, v218, v219\n" \
"v_mfma_f32_16x16x32_bf16 a[188:191], v[96:99], v[176:179], a[188:191]\n" \
"v_accvgpr_read_b32 v220, a32\n" \
"v_accvgpr_read_b32 v221, a33\n" \
"v_mfma_f32_16x16x32_bf16 a[208:211], v[104:107], v[152:155], a[208:211]\n" \
"v_cvt_pk_bf16_f32 v218, v180, v181\n" \
"v_cvt_pk_bf16_f32 v219, v182, v183\n" \
"v_mfma_f32_16x16x32_bf16 a[208:211], v[100:103], v[148:151], a[208:211]\n" \
"v_accvgpr_read_b32 v222, a34\n" \
"v_accvgpr_read_b32 v223, a35\n" \
"v_mfma_f32_16x16x32_bf16 a[212:215], v[100:103], v[156:159], a[212:215]\n" \
"v_cvt_pk_bf16_f32 v220, v220, v221\n" \
"v_accvgpr_read_b32 v180, a36\n" \
"v_mfma_f32_16x16x32_bf16 a[212:215], v[104:107], v[160:163], a[212:215]\n" \
"v_accvgpr_read_b32 v181, a37\n" \
"v_accvgpr_read_b32 v182, a38\n" \
"v_mfma_f32_16x16x32_bf16 a[216:219], v[104:107], v[168:171], a[216:219]\n" \
"v_accvgpr_read_b32 v183, a39\n" \
"v_cvt_pk_bf16_f32 v221, v222, v223\n" \
"v_mfma_f32_16x16x32_bf16 a[216:219], v[100:103], v[164:167], a[216:219]\n" \
"v_accvgpr_read_b32 v224, a64\n" \
"v_accvgpr_read_b32 v225, a65\n" \
"v_mfma_f32_16x16x32_bf16 a[220:223], v[100:103], v[172:175], a[220:223]\n" \
"v_cvt_pk_bf16_f32 v222, v180, v181\n" \
"v_cvt_pk_bf16_f32 v223, v182, v183\n" \
"v_mfma_f32_16x16x32_bf16 a[220:223], v[104:107], v[176:179], a[220:223]\n" \
"v_accvgpr_read_b32 v226, a66\n" \
"v_accvgpr_read_b32 v227, a67\n" \
"v_mfma_f32_16x16x32_bf16 a[240:243], v[112:115], v[152:155], a[240:243]\n" \
"ds_read_b64_tr_b16 v[152:153], v32 offset:8192\n" \
"ds_read_b64_tr_b16 v[154:155], v32 offset:9216\n" \
"v_mfma_f32_16x16x32_bf16 a[240:243], v[108:111], v[148:151], a[240:243]\n" \
"ds_read_b64_tr_b16 v[148:149], v32 offset:0\n" \
"ds_read_b64_tr_b16 v[150:151], v32 offset:1024\n" \
"v_mfma_f32_16x16x32_bf16 a[244:247], v[108:111], v[156:159], a[244:247]\n" \
"ds_read_b64_tr_b16 v[156:157], v33 offset:0\n" \
"ds_read_b64_tr_b16 v[158:159], v33 offset:1024\n" \
"v_mfma_f32_16x16x32_bf16 a[244:247], v[112:115], v[160:163], a[244:247]\n" \
"ds_read_b64_tr_b16 v[160:161], v33 offset:8192\n" \
"ds_read_b64_tr_b16 v[162:163], v33 offset:9216\n" \
"v_mfma_f32_16x16x32_bf16 a[248:251], v[112:115], v[168:171], a[248:251]\n" \
"ds_read_b64_tr_b16 v[168:169], v34 offset:8192\n" \
"ds_read_b64_tr_b16 v[170:171], v34 offset:9216\n" \
"v_mfma_f32_16x16x32_bf16 a[248:251], v[108:111], v[164:167], a[248:251]\n" \
"ds_read_b64_tr_b16 v[164:165], v34 offset:0\n" \
"ds_read_b64_tr_b16 v[166:167], v34 offset:1024\n" \
"v_mfma_f32_16x16x32_bf16 a[252:255], v[108:111], v[172:175], a[252:255]\n" \
"ds_read_b64_tr_b16 v[172:173], v35 offset:0\n" \
"ds_read_b64_tr_b16 v[174:175], v35 offset:1024\n" \
"v_mfma_f32_16x16x32_bf16 a[252:255], v[112:115], v[176:179], a[252:255]\n" \
"s_add_u32 m0, %[mA], 0x9c00\n" \
"s_waitcnt lgkmcnt(14)\n" \
"s_nop 0\n" \
"v_smfmac_f32_16x16x64_bf16 a[16:19], v[140:143], v[84:91], v50\n" \
"v_mfma_f32_16x16x32_bf16 a[48:51], v[128:131], v[88:91], 0\n" \
"s_add_u32 s85, s62, 0xfc00\n" \
"ds_read_b64_tr_b16 v[176:177], v35 offset:8192\n" \
"v_mfma_f32_16x16x32_bf16 a[48:51], v[124:127], v[84:87], a[48:51]\n" \
"buffer_load_dwordx4 v25, s[76:79], s85 offen lds\n" \
"v_mfma_f32_16x16x32_bf16 a[80:83], v[84:87], v[132:135], 0\n" \
"s_add_u32 m0, %[mB], 0x8c00\n" \
"s_add_u32 s84, s63, 0x18000\n" \
"v_mfma_f32_16x16x32_bf16 a[80:83], v[88:91], v[136:139], a[80:83]\n" \
"ds_read_b64_tr_b16 v[178:179], v35 offset:9216\n" \
"v_cvt_pk_bf16_f32 v224, v224, v225\n" \
"v_smfmac_f32_16x16x64_bf16 a[112:115], v[140:143], v[52:59], v50\n" \
"v_mfma_f32_16x16x32_bf16 a[20:23], v[128:131], v[56:59], 0\n" \
"buffer_load_dwordx4 v27, s[68:71], s84 offen lds\n" \
"v_mfma_f32_16x16x32_bf16 a[20:23], v[124:127], v[52:55], a[20:23]\n" \
"s_add_u32 s88, s44, 0x0\n" \
"s_nop 0\n" \
"v_accvgpr_read_b32 v180, a68\n" \
"v_mfma_f32_16x16x32_bf16 a[52:55], v[116:119], v[52:55], 0\n" \
"v_accvgpr_read_b32 v181, a69\n" \
"v_accvgpr_read_b32 v182, a70\n" \
"v_mfma_f32_16x16x32_bf16 a[52:55], v[120:123], v[56:59], a[52:55]\n" \
"buffer_store_dwordx4 v[184:187], v20, s[80:83], s88 offen offset:0 sc1\n" \
"v_mfma_f32_16x16x32_bf16 a[84:87], v[56:59], v[136:139], 0\n" \
"v_accvgpr_read_b32 v183, a71\n" \
"v_cvt_pk_bf16_f32 v225, v226, v227\n" \
"v_mfma_f32_16x16x32_bf16 a[84:87], v[52:55], v[132:135], a[84:87]\n" \
"v_accvgpr_read_b32 v228, a96\n" \
"v_accvgpr_read_b32 v229, a97\n" \
"v_smfmac_f32_16x16x64_bf16 a[116:119], v[140:143], v[60:67], v50\n" \
"v_mfma_f32_16x16x32_bf16 a[24:27], v[124:127], v[60:63], 0\n" \
"v_cvt_pk_bf16_f32 v226, v180, v181\n" \
"v_cvt_pk_bf16_f32 v227, v182, v183\n" \
"v_mfma_f32_16x16x32_bf16 a[24:27], v[128:131], v[64:67], a[24:27]\n" \
"s_add_u32 s89, s44, 0x1000\n" \
"v_accvgpr_read_b32 v230, a98\n" \
"v_mfma_f32_16x16x32_bf16 a[56:59], v[120:123], v[64:67], 0\n" \
"buffer_store_dwordx4 v[188:191], v20, s[80:83], s89 offen offset:0 sc1\n" \
"v_mfma_f32_16x16x32_bf16 a[56:59], v[116:119], v[60:63], a[56:59]\n" \
"v_accvgpr_read_b32 v231, a99\n" \
"v_cvt_pk_bf16_f32 v228, v228, v229\n" \
"v_mfma_f32_16x16x32_bf16 a[88:91], v[60:63], v[132:135], 0\n" \
"v_accvgpr_read_b32 v180, a100\n" \
"v_accvgpr_read_b32 v181, a101\n" \
"v_mfma_f32_16x16x32_bf16 a[88:91], v[64:67], v[136:139], a[88:91]\n" \
"v_accvgpr_read_b32 v182, a102\n" \
"v_accvgpr_read_b32 v183, a103\n" \
"v_smfmac_f32_16x16x64_bf16 a[120:123], v[140:143], v[68:75], v50\n" \
"v_mfma_f32_16x16x32_bf16 a[28:31], v[128:131], v[72:75], 0\n" \
"v_cvt_pk_bf16_f32 v229, v230, v231\n" \
"v_accvgpr_read_b32 v232, a8\n" \
"v_mfma_f32_16x16x32_bf16 a[28:31], v[124:127], v[68:71], a[28:31]\n" \
"v_accvgpr_read_b32 v233, a9\n" \
"v_cvt_pk_bf16_f32 v230, v180, v181\n" \
"v_mfma_f32_16x16x32_bf16 a[60:63], v[116:119], v[68:71], 0\n" \
"s_add_u32 s88, s44, 0x0\n" \
"s_nop 0\n" \
"v_cvt_pk_bf16_f32 v231, v182, v183\n" \
"v_mfma_f32_16x16x32_bf16 a[60:63], v[120:123], v[72:75], a[60:63]\n" \
"buffer_store_dwordx4 v[192:195], v20, s[80:83], s88 offen offset:1024 sc1\n" \
"v_mfma_f32_16x16x32_bf16 a[92:95], v[72:75], v[136:139], 0\n" \
"v_accvgpr_read_b32 v234, a10\n" \
"v_accvgpr_read_b32 v235, a11\n" \
"v_mfma_f32_16x16x32_bf16 a[92:95], v[68:71], v[132:135], a[92:95]\n" \
"v_cvt_pk_bf16_f32 v232, v232, v233\n" \
"v_accvgpr_read_b32 v180, a12\n" \
"v_smfmac_f32_16x16x64_bf16 a[124:127], v[140:143], v[76:83], v50\n" \
"v_mfma_f32_16x16x32_bf16 a[0:3], v[124:127], v[76:79], 0\n" \
"v_accvgpr_read_b32 v181, a13\n" \
"v_accvgpr_read_b32 v182, a14\n" \
"v_mfma_f32_16x16x32_bf16 a[0:3], v[128:131], v[80:83], a[0:3]\n" \
"v_accvgpr_read_b32 v183, a15\n" \
"v_cvt_pk_bf16_f32 v233, v234, v235\n" \
"v_mfma_f32_16x16x32_bf16 a[4:7], v[120:123], v[80:83], 0\n" \
"v_accvgpr_read_b32 v236, a40\n" \
"v_accvgpr_read_b32 v237, a41\n" \
"v_mfma_f32_16x16x32_bf16 a[4:7], v[116:119], v[76:79], a[4:7]\n" \
"v_cvt_pk_bf16_f32 v234, v180, v181\n" \
"v_cvt_pk_bf16_f32 v235, v182, v183\n" \
"v_mfma_f32_16x16x32_bf16 a[32:35], v[76:79], v[132:135], 0\n" \
"v_accvgpr_read_b32 v238, a42\n" \
"v_accvgpr_read_b32 v239, a43\n" \
"v_mfma_f32_16x16x32_bf16 a[32:35], v[80:83], v[136:139], a[32:35]\n" \
"v_cvt_pk_bf16_f32 v236, v236, v237\n" \
"v_accvgpr_read_b32 v180, a44\n" \
"v_accvgpr_read_b32 v181, a45\n" \
"v_accvgpr_read_b32 v182, a46\n" \
"v_accvgpr_read_b32 v183, a47\n" \
"v_cvt_pk_bf16_f32 v237, v238, v239\n" \
"v_accvgpr_read_b32 v240, a72\n" \
"v_accvgpr_read_b32 v241, a73\n" \
"v_cvt_pk_bf16_f32 v238, v180, v181\n" \
"v_cvt_pk_bf16_f32 v239, v182, v183\n" \
"v_accvgpr_read_b32 v242, a74\n" \
"v_accvgpr_read_b32 v243, a75\n" \
"v_cvt_pk_bf16_f32 v240, v240, v241\n" \
"v_accvgpr_read_b32 v180, a76\n" \
"v_accvgpr_read_b32 v181, a77\n" \
"v_accvgpr_read_b32 v182, a78\n" \
"v_accvgpr_read_b32 v183, a79\n" \
"v_cvt_pk_bf16_f32 v241, v242, v243\n" \
"v_accvgpr_read_b32 v244, a104\n" \
"v_accvgpr_read_b32 v245, a105\n" \
"v_cvt_pk_bf16_f32 v242, v180, v181\n" \
"v_cvt_pk_bf16_f32 v243, v182, v183\n" \
"v_accvgpr_read_b32 v246, a106\n" \
"v_accvgpr_read_b32 v247, a107\n" \
"v_cvt_pk_bf16_f32 v244, v244, v245\n" \
"v_accvgpr_read_b32 v248, a128\n" \
"v_accvgpr_read_b32 v249, a129\n" \
"v_accvgpr_read_b32 v250, a130\n" \
"v_accvgpr_read_b32 v251, a131\n" \
"s_waitcnt lgkmcnt(0)\n" \
"s_nop 0\n" \
"v_mfma_f32_16x16x32_bf16 a[128:131], v[152:155], v[56:59], 0\n" \
"v_accvgpr_read_b32 v180, a108\n" \
"v_accvgpr_read_b32 v181, a109\n" \
"v_accvgpr_read_b32 v182, a110\n" \
"v_accvgpr_read_b32 v183, a111\n" \
"v_cvt_pk_bf16_f32 v245, v246, v247\n" \
"v_cvt_pk_bf16_f32 v246, v180, v181\n" \
"v_cvt_pk_bf16_f32 v247, v182, v183\n" \
"v_cvt_pk_bf16_f32 v248, v248, v249\n" \
"v_mfma_f32_16x16x32_bf16 a[128:131], v[148:151], v[52:55], a[128:131]\n" \
"v_accvgpr_read_b32 v180, a132\n" \
"v_accvgpr_read_b32 v181, a133\n" \
"v_accvgpr_read_b32 v182, a134\n" \
"v_accvgpr_read_b32 v183, a135\n" \
"v_cvt_pk_bf16_f32 v249, v250, v251\n" \
"v_accvgpr_read_b32 v252, a160\n" \
"v_accvgpr_read_b32 v253, a161\n" \
"v_cvt_pk_bf16_f32 v250, v180, v181\n" \
"v_cvt_pk_bf16_f32 v251, v182, v183\n" \
"v_accvgpr_read_b32 v254, a162\n" \
"v_accvgpr_read_b32 v255, a163\n" \
"v_cvt_pk_bf16_f32 v252, v252, v253\n" \
"v_accvgpr_read_b32 v180, a164\n" \
"v_accvgpr_read_b32 v181, a165\n" \
"v_accvgpr_read_b32 v182, a166\n" \
"v_accvgpr_read_b32 v183, a167\n" \
"v_cvt_pk_bf16_f32 v253, v254, v255\n" \
"v_cvt_pk_bf16_f32 v254, v180, v181\n" \
"v_cvt_pk_bf16_f32 v255, v182, v183\n" \
"v_accvgpr_write_b32 a36, v252\n" \
"v_accvgpr_write_b32 a37, v253\n" \
"v_mfma_f32_16x16x32_bf16 a[132:135], v[156:159], v[52:55], 0\n" \
"v_accvgpr_write_b32 a38, v254\n" \
"v_accvgpr_write_b32 a39, v255\n" \
"v_accvgpr_read_b32 v252, a192\n" \
"v_accvgpr_read_b32 v253, a193\n" \
"v_accvgpr_read_b32 v254, a194\n" \
"v_accvgpr_read_b32 v255, a195\n" \
"v_cvt_pk_bf16_f32 v252, v252, v253\n" \
"v_accvgpr_read_b32 v180, a196\n" \
"v_accvgpr_read_b32 v181, a197\n" \
"v_accvgpr_read_b32 v182, a198\n" \
"v_accvgpr_read_b32 v183, a199\n" \
"v_cvt_pk_bf16_f32 v253, v254, v255\n" \
"v_cvt_pk_bf16_f32 v254, v180, v181\n" \
"v_cvt_pk_bf16_f32 v255, v182, v183\n" \
"v_accvgpr_write_b32 a64, v252\n" \
"v_accvgpr_write_b32 a65, v253\n" \
"v_mfma_f32_16x16x32_bf16 a[132:135], v[160:163], v[56:59], a[132:135]\n" \
"v_accvgpr_write_b32 a66, v254\n" \
"v_accvgpr_write_b32 a67, v255\n" \
"v_accvgpr_read_b32 v252, a224\n" \
"v_accvgpr_read_b32 v253, a225\n" \
"v_accvgpr_read_b32 v254, a226\n" \
"v_accvgpr_read_b32 v255, a227\n" \
"v_cvt_pk_bf16_f32 v252, v252, v253\n" \
"v_accvgpr_read_b32 v180, a228\n" \
"v_accvgpr_read_b32 v181, a229\n" \
"v_accvgpr_read_b32 v182, a230\n" \
"v_accvgpr_read_b32 v183, a231\n" \
"v_cvt_pk_bf16_f32 v253, v254, v255\n" \
"v_cvt_pk_bf16_f32 v254, v180, v181\n" \
"v_cvt_pk_bf16_f32 v255, v182, v183\n" \
"v_accvgpr_write_b32 a68, v252\n" \
"v_accvgpr_write_b32 a69, v253\n" \
"v_accvgpr_write_b32 a70, v254\n" \
"v_accvgpr_write_b32 a71, v255\n" \
"v_accvgpr_read_b32 v252, a136\n" \
"v_accvgpr_read_b32 v253, a137\n" \
"v_accvgpr_read_b32 v254, a138\n" \
"v_accvgpr_read_b32 v255, a139\n" \
"v_mfma_f32_16x16x32_bf16 a[136:139], v[168:171], v[56:59], 0\n" \
"ds_read_b128 v[84:87], v48 offset:0\n" \
"ds_read_b128 v[88:91], v49 offset:0\n" \
"ds_read_b128 v[92:95], v48 offset:2048\n" \
"v_mfma_f32_16x16x32_bf16 a[136:139], v[164:167], v[52:55], a[136:139]\n" \
"ds_read_b128 v[96:99], v49 offset:2048\n" \
"v_accvgpr_read_b32 v180, a140\n" \
"v_accvgpr_read_b32 v181, a141\n" \
"v_accvgpr_read_b32 v182, a142\n" \
"v_accvgpr_read_b32 v183, a143\n" \
"v_mfma_f32_16x16x32_bf16 a[140:143], v[172:175], v[52:55], 0\n" \
"ds_read_b128 v[100:103], v48 offset:4096\n" \
"ds_read_b128 v[104:107], v49 offset:4096\n" \
"s_add_u32 s89, s44, 0x1000\n" \
"buffer_store_dwordx4 v[196:199], v20, s[80:83], s89 offen offset:1024 sc1\n" \
"ds_read_b128 v[108:111], v48 offset:6144\n" \
"v_mfma_f32_16x16x32_bf16 a[140:143], v[176:179], v[56:59], a[140:143]\n" \
"v_cvt_pk_bf16_f32 v252, v252, v253\n" \
"v_cvt_pk_bf16_f32 v253, v254, v255\n" \
"v_mfma_f32_16x16x32_bf16 a[160:163], v[152:155], v[64:67], 0\n" \
"ds_read_b128 v[112:115], v49 offset:6144\n" \
"v_cvt_pk_bf16_f32 v254, v180, v181\n" \
"v_mfma_f32_16x16x32_bf16 a[160:163], v[148:151], v[60:63], a[160:163]\n" \
"v_cvt_pk_bf16_f32 v255, v182, v183\n" \
"v_accvgpr_write_b32 a96, v252\n" \
"v_mfma_f32_16x16x32_bf16 a[164:167], v[156:159], v[60:63], 0\n" \
"v_accvgpr_write_b32 a97, v253\n" \
"v_accvgpr_write_b32 a98, v254\n" \
"v_mfma_f32_16x16x32_bf16 a[164:167], v[160:163], v[64:67], a[164:167]\n" \
"v_accvgpr_write_b32 a99, v255\n" \
"v_accvgpr_read_b32 v252, a168\n" \
"v_accvgpr_read_b32 v253, a169\n" \
"v_accvgpr_read_b32 v254, a170\n" \
"v_accvgpr_read_b32 v255, a171\n" \
"v_mfma_f32_16x16x32_bf16 a[168:171], v[168:171], v[64:67], 0\n" \
"v_accvgpr_read_b32 v180, a172\n" \
"v_accvgpr_read_b32 v181, a173\n" \
"v_mfma_f32_16x16x32_bf16 a[168:171], v[164:167], v[60:63], a[168:171]\n" \
"v_accvgpr_read_b32 v182, a174\n" \
"v_accvgpr_read_b32 v183, a175\n" \
"v_mfma_f32_16x16x32_bf16 a[172:175], v[172:175], v[60:63], 0\n" \
"v_cvt_pk_bf16_f32 v252, v252, v253\n" \
"v_cvt_pk_bf16_f32 v253, v254, v255\n" \
"v_mfma_f32_16x16x32_bf16 a[172:175], v[176:179], v[64:67], a[172:175]\n" \
"v_cvt_pk_bf16_f32 v254, v180, v181\n" \
"v_cvt_pk_bf16_f32 v255, v182, v183\n" \
"v_mfma_f32_16x16x32_bf16 a[192:195], v[152:155], v[72:75], 0\n" \
"v_accvgpr_write_b32 a100, v252\n" \
"v_accvgpr_write_b32 a101, v253\n" \
"v_mfma_f32_16x16x32_bf16 a[192:195], v[148:151], v[68:71], a[192:195]\n" \
"v_accvgpr_write_b32 a102, v254\n" \
"v_accvgpr_write_b32 a103, v255\n" \
"s_add_u32 s62, s62, 0x80\n" \
"v_accvgpr_read_b32 v252, a200\n" \
"v_accvgpr_read_b32 v253, a201\n" \
"v_accvgpr_read_b32 v254, a202\n" \
"s_waitcnt vmcnt(4) lgkmcnt(0)\n" \
"s_barrier\n" \
"v_mfma_f32_16x16x32_bf16 a[196:199], v[156:159], v[68:71], 0\n" \
"v_accvgpr_read_b32 v255, a203\n" \
"s_add_u32 m0, s40, 0x0\n" \
"v_mfma_f32_16x16x32_bf16 a[196:199], v[160:163], v[72:75], a[196:199]\n" \
"s_cmp_lt_u32 s62, 0x480\n" \
"s_cselect_b32 s62, s62, 0\n" \
"v_mfma_f32_16x16x32_bf16 a[200:203], v[168:171], v[72:75], 0\n" \
"s_lshl_b32 s72, s62, 6\n" \
"s_add_u32 s72, s72, s37\n" \
"v_mfma_f32_16x16x32_bf16 a[200:203], v[164:167], v[68:71], a[200:203]\n" \
"s_add_u32 s85, s72, 0x0\n" \
"s_nop 0\n" \
"buffer_load_dwordx4 v20, s[80:83], s85 offen sc1 lds\n" \
"v_accvgpr_read_b32 v180, a204\n" \
"v_accvgpr_read_b32 v181, a205\n" \
"v_accvgpr_read_b32 v182, a206\n" \
"v_accvgpr_read_b32 v183, a207\n" \
"v_mfma_f32_16x16x32_bf16 a[204:207], v[172:175], v[68:71], 0\n" \
"s_add_u32 m0, s40, 0x1000\n" \
"s_add_u32 s84, s72, 0x1000\n" \
"v_mfma_f32_16x16x32_bf16 a[204:207], v[176:179], v[72:75], a[204:207]\n" \
"buffer_load_dwordx4 v20, s[80:83], s84 offen sc1 lds\n" \
"v_mfma_f32_16x16x32_bf16 a[224:227], v[152:155], v[80:83], 0\n" \
"v_cvt_pk_bf16_f32 v252, v252, v253\n" \
"v_cvt_pk_bf16_f32 v253, v254, v255\n" \
"v_mfma_f32_16x16x32_bf16 a[224:227], v[148:151], v[76:79], a[224:227]\n" \
"v_cvt_pk_bf16_f32 v254, v180, v181\n" \
"v_cvt_pk_bf16_f32 v255, v182, v183\n" \
"v_mfma_f32_16x16x32_bf16 a[228:231], v[156:159], v[76:79], 0\n" \
"v_accvgpr_write_b32 a8, v252\n" \
"v_accvgpr_write_b32 a9, v253\n" \
"v_mfma_f32_16x16x32_bf16 a[228:231], v[160:163], v[80:83], a[228:231]\n" \
"v_accvgpr_write_b32 a10, v254\n" \
"v_accvgpr_write_b32 a11, v255\n" \
"v_accvgpr_read_b32 v252, a232\n" \
"v_accvgpr_read_b32 v253, a233\n" \
"v_accvgpr_read_b32 v254, a234\n" \
"v_accvgpr_read_b32 v255, a235\n" \
"v_mfma_f32_16x16x32_bf16 a[232:235], v[168:171], v[80:83], 0\n" \
"s_add_u32 m0, s40, 0x3000\n" \
"s_add_u32 s85, s62, 0x0\n" \
"buffer_load_dwordx4 v21, s[76:79], s85 offen lds\n" \
"v_mfma_f32_16x16x32_bf16 a[232:235], v[164:167], v[76:79], a[232:235]\n" \
"v_accvgpr_read_b32 v180, a236\n" \
"v_accvgpr_read_b32 v181, a237\n" \
"v_accvgpr_read_b32 v182, a238\n" \
"v_accvgpr_read_b32 v183, a239\n" \
"v_mfma_f32_16x16x32_bf16 a[236:239], v[172:175], v[76:79], 0\n" \
"v_cvt_pk_bf16_f32 v252, v252, v253\n" \
"s_add_u32 m0, %[mA], 0x0\n" \
"v_mfma_f32_16x16x32_bf16 a[236:239], v[176:179], v[80:83], a[236:239]\n" \
"s_add_u32 s84, s62, 0x0\n" \
"s_nop 0\n" \
"v_cvt_pk_bf16_f32 v253, v254, v255\n" \
"v_cvt_pk_bf16_f32 v254, v180, v181\n" \
"v_cvt_pk_bf16_f32 v255, v182, v183\n" \
"v_accvgpr_write_b32 a12, v252\n" \
"v_accvgpr_write_b32 a13, v253\n" \
"v_accvgpr_write_b32 a14, v254\n" \
"v_accvgpr_write_b32 a15, v255\n" \
"v_accvgpr_read_b32 v252, a144\n" \
"v_accvgpr_read_b32 v253, a145\n" \
"v_accvgpr_read_b32 v254, a146\n" \
"v_accvgpr_read_b32 v255, a147\n" \
"v_mfma_f32_16x16x32_bf16 a[144:147], v[152:155], v[88:91], 0\n" \
"buffer_load_dwordx4 v24, s[76:79], s84 offen lds\n" \
"v_mfma_f32_16x16x32_bf16 a[144:147], v[148:151], v[84:87], a[144:147]\n" \
"v_cvt_pk_bf16_f32 v252, v252, v253\n" \
"v_accvgpr_read_b32 v180, a176\n" \
"v_accvgpr_read_b32 v181, a177\n" \
"v_accvgpr_read_b32 v182, a178\n" \
"v_accvgpr_read_b32 v183, a179\n" \
"v_cvt_pk_bf16_f32 v253, v254, v255\n" \
"v_cvt_pk_bf16_f32 v254, v180, v181\n" \
"v_cvt_pk_bf16_f32 v255, v182, v183\n" \
"v_accvgpr_write_b32 a40, v252\n" \
"v_accvgpr_write_b32 a41, v253\n" \
"v_accvgpr_write_b32 a42, v254\n" \
"v_accvgpr_write_b32 a43, v255\n" \
"v_accvgpr_read_b32 v252, a148\n" \
"v_accvgpr_read_b32 v253, a149\n" \
"v_accvgpr_read_b32 v254, a150\n" \
"v_accvgpr_read_b32 v255, a151\n" \
"v_mfma_f32_16x16x32_bf16 a[148:151], v[156:159], v[84:87], 0\n" \
"v_cvt_pk_bf16_f32 v252, v252, v253\n" \
"s_add_u32 m0, %[mA], 0x400\n" \
"v_mfma_f32_16x16x32_bf16 a[148:151], v[160:163], v[88:91], a[148:151]\n" \
"s_add_u32 s85, s62, 0x2400\n" \
"v_accvgpr_read_b32 v180, a180\n" \
"v_accvgpr_read_b32 v181, a181\n" \
"v_accvgpr_read_b32 v182, a182\n" \
"v_accvgpr_read_b32 v183, a183\n" \
"v_cvt_pk_bf16_f32 v253, v254, v255\n" \
"v_cvt_pk_bf16_f32 v254, v180, v181\n" \
"v_cvt_pk_bf16_f32 v255, v182, v183\n" \
"v_accvgpr_write_b32 a44, v252\n" \
"v_accvgpr_write_b32 a45, v253\n" \
"v_accvgpr_write_b32 a46, v254\n" \
"v_accvgpr_write_b32 a47, v255\n" \
"v_accvgpr_read_b32 v252, a152\n" \
"v_accvgpr_read_b32 v253, a153\n" \
"v_accvgpr_read_b32 v254, a154\n" \
"v_accvgpr_read_b32 v255, a155\n" \
"v_mfma_f32_16x16x32_bf16 a[152:155], v[168:171], v[88:91], 0\n" \
"buffer_load_dwordx4 v25, s[76:79], s85 offen lds\n" \
"v_mfma_f32_16x16x32_bf16 a[152:155], v[164:167], v[84:87], a[152:155]\n" \
"v_cvt_pk_bf16_f32 v252, v252, v253\n" \
"v_accvgpr_read_b32 v180, a184\n" \
"v_accvgpr_read_b32 v181, a185\n" \
"v_accvgpr_read_b32 v182, a186\n" \
"v_accvgpr_read_b32 v183, a187\n" \
"v_cvt_pk_bf16_f32 v253, v254, v255\n" \
"v_cvt_pk_bf16_f32 v254, v180, v181\n" \
"v_cvt_pk_bf16_f32 v255, v182, v183\n" \
"v_accvgpr_write_b32 a72, v252\n" \
"L_pf5:\n" \
"s_cbranch_execz L_pf9\n" \
"s_nop 0\n" \
"v_accvgpr_write_b32 a73, v253\n" \
"v_accvgpr_write_b32 a74, v254\n" \
"v_accvgpr_write_b32 a75, v255\n" \
"s_lshl_b32 s63, s62, 12\n" \
"v_accvgpr_read_b32 v252, a156\n" \
"v_accvgpr_read_b32 v253, a157\n" \
"v_accvgpr_read_b32 v254, a158\n" \
"v_accvgpr_read_b32 v255, a159\n" \
"v_mfma_f32_16x16x32_bf16 a[156:159], v[172:175], v[84:87], 0\n" \
"s_add_u32 m0, %[mB], 0x0\n" \
"s_add_u32 s84, s63, 0x0\n" \
"s_nop 0\n" \
"buffer_load_dwordx4 v26, s[68:71], s84 offen lds\n" \
"v_mfma_f32_16x16x32_bf16 a[156:159], v[176:179], v[88:91], a[156:159]\n" \
"s_add_u32 m0, %[mA], 0x800\n" \
"s_add_u32 s85, s62, 0x4800\n" \
"v_mfma_f32_16x16x32_bf16 a[176:179], v[152:155], v[96:99], 0\n" \
"v_accvgpr_read_b32 v180, a188\n" \
"v_accvgpr_read_b32 v181, a189\n" \
"v_mfma_f32_16x16x32_bf16 a[176:179], v[148:151], v[92:95], a[176:179]\n" \
"buffer_load_dwordx4 v24, s[76:79], s85 offen lds\n" \
"v_mfma_f32_16x16x32_bf16 a[180:183], v[156:159], v[92:95], 0\n" \
"s_add_u32 m0, %[mA], 0xc00\n" \
"s_add_u32 s84, s62, 0x6c00\n" \
"v_mfma_f32_16x16x32_bf16 a[180:183], v[160:163], v[96:99], a[180:183]\n" \
"v_accvgpr_read_b32 v182, a190\n" \
"v_accvgpr_read_b32 v183, a191\n" \
"v_mfma_f32_16x16x32_bf16 a[184:187], v[168:171], v[96:99], 0\n" \
"buffer_load_dwordx4 v25, s[76:79], s84 offen lds\n" \
"v_mfma_f32_16x16x32_bf16 a[184:187], v[164:167], v[92:95], a[184:187]\n" \
"v_cvt_pk_bf16_f32 v252, v252, v253\n" \
"v_cvt_pk_bf16_f32 v253, v254, v255\n" \
"v_mfma_f32_16x16x32_bf16 a[188:191], v[172:175], v[92:95], 0\n" \
"v_cvt_pk_bf16_f32 v254, v180, v181\n" \
"v_cvt_pk_bf16_f32 v255, v182, v183\n" \
"v_mfma_f32_16x16x32_bf16 a[188:191], v[176:179], v[96:99], a[188:191]\n" \
"v_accvgpr_write_b32 a76, v252\n" \
"v_accvgpr_write_b32 a77, v253\n" \
"v_accvgpr_write_b32 a78, v254\n" \
"v_accvgpr_write_b32 a79, v255\n" \
"v_accvgpr_read_b32 v252, a208\n" \
"v_accvgpr_read_b32 v253, a209\n" \
"v_accvgpr_read_b32 v254, a210\n" \
"v_accvgpr_read_b32 v255, a211\n" \
"v_mfma_f32_16x16x32_bf16 a[208:211], v[152:155], v[104:107], 0\n" \
"v_cvt_pk_bf16_f32 v252, v252, v253\n" \
"s_add_u32 m0, %[mB], 0x400\n" \
"v_mfma_f32_16x16x32_bf16 a[208:211], v[148:151], v[100:103], a[208:211]\n" \
"s_add_u32 s85, s63, 0x8000\n" \
"v_accvgpr_read_b32 v180, a240\n" \
"v_accvgpr_read_b32 v181, a241\n" \
"v_accvgpr_read_b32 v182, a242\n" \
"v_accvgpr_read_b32 v183, a243\n" \
"v_cvt_pk_bf16_f32 v253, v254, v255\n" \
"v_cvt_pk_bf16_f32 v254, v180, v181\n" \
"v_cvt_pk_bf16_f32 v255, v182, v183\n" \
"v_accvgpr_write_b32 a104, v252\n" \
"v_accvgpr_write_b32 a105, v253\n" \
"v_accvgpr_write_b32 a106, v254\n" \
"v_accvgpr_write_b32 a107, v255\n" \
"v_accvgpr_read_b32 v252, a212\n" \
"v_accvgpr_read_b32 v253, a213\n" \
"v_accvgpr_read_b32 v254, a214\n" \
"v_accvgpr_read_b32 v255, a215\n" \
"v_mfma_f32_16x16x32_bf16 a[212:215], v[156:159], v[100:103], 0\n" \
"buffer_load_dwordx4 v26, s[68:71], s85 offen lds\n" \
"ds_read_b64_tr_b16 v[140:141], v39 offset:32768\n" \
"ds_read_b64_tr_b16 v[142:143], v39 offset:33728\n" \
"ds_read_b128 v[84:87], v22 offset:32768\n" \
"ds_read_b128 v[88:91], v23 offset:32768\n" \
"v_mfma_f32_16x16x32_bf16 a[212:215], v[160:163], v[104:107], a[212:215]\n" \
"ds_read_b64_tr_b16 v[128:129], v37 offset:36864\n" \
"ds_read_b64_tr_b16 v[130:131], v37 offset:37376\n" \
"v_accvgpr_read_b32 v184, a216\n" \
"v_accvgpr_read_b32 v185, a217\n" \
"v_accvgpr_read_b32 v186, a218\n" \
"v_accvgpr_read_b32 v187, a219\n" \
"v_mfma_f32_16x16x32_bf16 a[216:219], v[168:171], v[104:107], 0\n" \
"s_add_u32 m0, %[mA], 0x1000\n" \
"s_add_u32 s84, s62, 0x9000\n" \
"buffer_load_dwordx4 v24, s[76:79], s84 offen lds\n" \
"ds_read_b64_tr_b16 v[124:125], v37 offset:32768\n" \
"ds_read_b64_tr_b16 v[126:127], v37 offset:33280\n" \
"v_mfma_f32_16x16x32_bf16 a[216:219], v[164:167], v[100:103], a[216:219]\n" \
"v_cvt_pk_bf16_f32 v252, v252, v253\n" \
"v_accvgpr_read_b32 v180, a244\n" \
"v_accvgpr_read_b32 v181, a245\n" \
"v_accvgpr_read_b32 v182, a246\n" \
"v_accvgpr_read_b32 v183, a247\n" \
"v_cvt_pk_bf16_f32 v253, v254, v255\n" \
"v_cvt_pk_bf16_f32 v254, v180, v181\n" \
"v_cvt_pk_bf16_f32 v255, v182, v183\n" \
"v_accvgpr_write_b32 a108, v252\n" \
"v_accvgpr_write_b32 a109, v253\n" \
"v_accvgpr_write_b32 a110, v254\n" \
"v_accvgpr_write_b32 a111, v255\n" \
"v_accvgpr_read_b32 v252, a220\n" \
"v_accvgpr_read_b32 v253, a221\n" \
"v_accvgpr_read_b32 v254, a222\n" \
"v_accvgpr_read_b32 v255, a223\n" \
"v_mfma_f32_16x16x32_bf16 a[220:223], v[172:175], v[100:103], 0\n" \
"ds_read_b64_tr_b16 v[132:133], v38 offset:32768\n" \
"ds_read_b64_tr_b16 v[134:135], v38 offset:33280\n" \
"ds_read_b64_tr_b16 v[136:137], v38 offset:36864\n" \
"ds_read_b64_tr_b16 v[138:139], v38 offset:37376\n" \
"v_mfma_f32_16x16x32_bf16 a[220:223], v[176:179], v[104:107], a[220:223]\n" \
"ds_read_b128 v[52:55], v28 offset:32768\n" \
"ds_read_b128 v[56:59], v29 offset:32768\n" \
"v_mfma_f32_16x16x32_bf16 a[240:243], v[152:155], v[112:115], 0\n" \
"s_add_u32 m0, %[mA], 0x1400\n" \
"s_add_u32 s85, s62, 0xb400\n" \
"buffer_load_dwordx4 v25, s[76:79], s85 offen lds\n" \
"v_mfma_f32_16x16x32_bf16 a[240:243], v[148:151], v[108:111], a[240:243]\n" \
"ds_read_b64_tr_b16 v[116:117], v36 offset:32768\n" \
"ds_read_b64_tr_b16 v[118:119], v36 offset:33280\n" \
"v_mfma_f32_16x16x32_bf16 a[244:247], v[156:159], v[108:111], 0\n" \
"s_add_u32 m0, %[mB], 0x800\n" \
"s_add_u32 s84, s63, 0x10000\n" \
"v_mfma_f32_16x16x32_bf16 a[244:247], v[160:163], v[112:115], a[244:247]\n" \
"buffer_load_dwordx4 v27, s[68:71], s84 offen lds\n" \
"ds_read_b64_tr_b16 v[120:121], v36 offset:36864\n" \
"ds_read_b64_tr_b16 v[122:123], v36 offset:37376\n" \
"v_accvgpr_read_b32 v180, a248\n" \
"v_accvgpr_read_b32 v181, a249\n" \
"v_accvgpr_read_b32 v182, a250\n" \
"v_accvgpr_read_b32 v183, a251\n" \
"v_mfma_f32_16x16x32_bf16 a[248:251], v[168:171], v[112:115], 0\n" \
"ds_read_b128 v[60:63], v28 offset:34816\n" \
"v_cvt_pk_bf16_f32 v184, v184, v185\n" \
"v_mfma_f32_16x16x32_bf16 a[248:251], v[164:167], v[108:111], a[248:251]\n" \
"v_cvt_pk_bf16_f32 v185, v186, v187\n" \
"v_cvt_pk_bf16_f32 v186, v180, v181\n" \
"v_cvt_pk_bf16_f32 v187, v182, v183\n" \
"v_accvgpr_read_b32 v180, a252\n" \
"v_accvgpr_read_b32 v181, a253\n" \
"v_accvgpr_read_b32 v182, a254\n" \
"v_accvgpr_read_b32 v183, a255\n" \
"v_mfma_f32_16x16x32_bf16 a[252:255], v[172:175], v[108:111], 0\n" \
"ds_read_b128 v[64:67], v29 offset:34816\n" \
"s_add_u32 m0, %[mA], 0x1800\n" \
"v_mfma_f32_16x16x32_bf16 a[252:255], v[176:179], v[112:115], a[252:255]\n" \
"s_add_u32 s85, s62, 0xd800\n" \
"buffer_load_dwordx4 v24, s[76:79], s85 offen lds\n" \
"s_waitcnt lgkmcnt(12)\n" \
"v_smfmac_f32_16x16x64_bf16 a[16:19], v[140:143], v[84:91], v50\n" \
"v_mfma_f32_16x16x32_bf16 a[48:51], v[128:131], v[88:91], a[48:51]\n" \
"s_add_u32 m0, %[mA], 0x1c00\n" \
"s_add_u32 s85, s62, 0xfc00\n" \
"v_mfma_f32_16x16x32_bf16 a[48:51], v[124:127], v[84:87], a[48:51]\n" \
"ds_read_b128 v[68:71], v28 offset:36864\n" \
"s_waitcnt lgkmcnt(8)\n" \
"v_mfma_f32_16x16x32_bf16 a[80:83], v[84:87], v[132:135], a[80:83]\n" \
"buffer_load_dwordx4 v25, s[76:79], s85 offen lds\n" \
"v_mfma_f32_16x16x32_bf16 a[80:83], v[88:91], v[136:139], a[80:83]\n" \
"ds_read_b128 v[72:75], v29 offset:36864\n" \
"s_waitcnt lgkmcnt(6)\n" \
"v_smfmac_f32_16x16x64_bf16 a[112:115], v[140:143], v[52:59], v50\n" \
"v_mfma_f32_16x16x32_bf16 a[20:23], v[128:131], v[56:59], a[20:23]\n" \
"s_add_u32 m0, %[mB], 0xc00\n" \
"s_add_u32 s84, s63, 0x18000\n" \
"buffer_load_dwordx4 v27, s[68:71], s84 offen lds\n" \
"v_mfma_f32_16x16x32_bf16 a[20:23], v[124:127], v[52:55], a[20:23]\n" \
"ds_read_b128 v[76:79], v28 offset:38912\n" \
"ds_read_b128 v[80:83], v29 offset:38912\n" \
"v_mfma_f32_16x16x32_bf16 a[52:55], v[116:119], v[52:55], a[52:55]\n" \
"s_add_u32 s88, s44, 0x0\n" \
"s_waitcnt lgkmcnt(5)\n" \
"v_mfma_f32_16x16x32_bf16 a[52:55], v[120:123], v[56:59], a[52:55]\n" \
"buffer_store_dwordx4 v[200:203], v20, s[80:83], s88 offen offset:2048 sc1\n" \
"v_mfma_f32_16x16x32_bf16 a[84:87], v[56:59], v[136:139], a[84:87]\n" \
"ds_read_b64_tr_b16 v[152:153], v32 offset:40960\n" \
"ds_read_b64_tr_b16 v[154:155], v32 offset:41984\n" \
"v_mfma_f32_16x16x32_bf16 a[84:87], v[52:55], v[132:135], a[84:87]\n" \
"ds_read_b64_tr_b16 v[148:149], v32 offset:32768\n" \
"s_waitcnt lgkmcnt(7)\n" \
"v_smfmac_f32_16x16x64_bf16 a[116:119], v[140:143], v[60:67], v50\n" \
"v_mfma_f32_16x16x32_bf16 a[24:27], v[124:127], v[60:63], a[24:27]\n" \
"ds_read_b64_tr_b16 v[150:151], v32 offset:33792\n" \
"ds_read_b64_tr_b16 v[156:157], v33 offset:32768\n" \
"v_mfma_f32_16x16x32_bf16 a[24:27], v[128:131], v[64:67], a[24:27]\n" \
"ds_read_b64_tr_b16 v[158:159], v33 offset:33792\n" \
"ds_read_b64_tr_b16 v[160:161], v33 offset:40960\n" \
"v_mfma_f32_16x16x32_bf16 a[56:59], v[120:123], v[64:67], a[56:59]\n" \
"ds_read_b64_tr_b16 v[162:163], v33 offset:41984\n" \
"s_add_u32 s89, s44, 0x1000\n" \
"v_mfma_f32_16x16x32_bf16 a[56:59], v[116:119], v[60:63], a[56:59]\n" \
"buffer_store_dwordx4 v[204:207], v20, s[80:83], s89 offen offset:2048 sc1\n" \
"v_mfma_f32_16x16x32_bf16 a[88:91], v[60:63], v[132:135], a[88:91]\n" \
"ds_read_b64_tr_b16 v[168:169], v34 offset:40960\n" \
"ds_read_b64_tr_b16 v[170:171], v34 offset:41984\n" \
"v_mfma_f32_16x16x32_bf16 a[88:91], v[64:67], v[136:139], a[88:91]\n" \
"ds_read_b64_tr_b16 v[164:165], v34 offset:32768\n" \
"s_waitcnt lgkmcnt(13)\n" \
"v_smfmac_f32_16x16x64_bf16 a[120:123], v[140:143], v[68:75], v50\n" \
"v_mfma_f32_16x16x32_bf16 a[28:31], v[128:131], v[72:75], a[28:31]\n" \
"ds_read_b64_tr_b16 v[166:167], v34 offset:33792\n" \
"ds_read_b64_tr_b16 v[172:173], v35 offset:32768\n" \
"v_mfma_f32_16x16x32_bf16 a[28:31], v[124:127], v[68:71], a[28:31]\n" \
"ds_read_b64_tr_b16 v[174:175], v35 offset:33792\n" \
"ds_read_b64_tr_b16 v[176:177], v35 offset:40960\n" \
"v_mfma_f32_16x16x32_bf16 a[60:63], v[116:119], v[68:71], a[60:63]\n" \
"ds_read_b64_tr_b16 v[178:179], v35 offset:41984\n" \
"s_add_u32 s88, s44, 0x0\n" \
"v_mfma_f32_16x16x32_bf16 a[60:63], v[120:123], v[72:75], a[60:63]\n" \
"buffer_store_dwordx4 v[208:211], v20, s[80:83], s88 offen offset:3072 sc1\n" \
"v_mfma_f32_16x16x32_bf16 a[92:95], v[72:75], v[136:139], a[92:95]\n" \
"ds_read_b128 v[84:87], v48 offset:32768\n" \
"ds_read_b128 v[88:91], v49 offset:32768\n" \
"v_mfma_f32_16x16x32_bf16 a[92:95], v[68:71], v[132:135], a[92:95]\n" \
"ds_read_b128 v[92:95], v48 offset:34816\n" \
"s_waitcnt lgkmcnt(15)\n" \
"s_nop 0\n" \
"v_smfmac_f32_16x16x64_bf16 a[124:127], v[140:143], v[76:83], v50\n" \
"v_mfma_f32_16x16x32_bf16 a[0:3], v[124:127], v[76:79], a[0:3]\n" \
"ds_read_b128 v[96:99], v49 offset:34816\n" \
"ds_read_b128 v[100:103], v48 offset:36864\n" \
"v_mfma_f32_16x16x32_bf16 a[0:3], v[128:131], v[80:83], a[0:3]\n" \
"ds_read_b128 v[104:107], v49 offset:36864\n" \
"ds_read_b128 v[108:111], v48 offset:38912\n" \
"v_mfma_f32_16x16x32_bf16 a[4:7], v[120:123], v[80:83], a[4:7]\n" \
"s_add_u32 s62, s62, 0x80\n" \
"ds_read_b128 v[112:115], v49 offset:38912\n" \
"v_mfma_f32_16x16x32_bf16 a[4:7], v[116:119], v[76:79], a[4:7]\n" \
"s_cmp_lt_u32 s62, 0x480\n" \
"s_cselect_b32 s62, s62, 0\n" \
"v_mfma_f32_16x16x32_bf16 a[32:35], v[76:79], v[132:135], a[32:35]\n" \
"s_lshl_b32 s72, s62, 6\n" \
"s_add_u32 s72, s72, s37\n" \
"v_mfma_f32_16x16x32_bf16 a[32:35], v[80:83], v[136:139], a[32:35]\n" \
"s_lshl_b32 s63, s62, 12\n" \
"v_cvt_pk_bf16_f32 v252, v252, v253\n" \
"v_mfma_f32_16x16x32_bf16 a[128:131], v[152:155], v[56:59], a[128:131]\n" \
"v_cvt_pk_bf16_f32 v253, v254, v255\n" \
"v_cvt_pk_bf16_f32 v254, v180, v181\n" \
"v_mfma_f32_16x16x32_bf16 a[128:131], v[148:151], v[52:55], a[128:131]\n" \
"v_cvt_pk_bf16_f32 v255, v182, v183\n" \
"s_waitcnt lgkmcnt(11)\n" \
"v_mfma_f32_16x16x32_bf16 a[132:135], v[156:159], v[52:55], a[132:135]\n" \
"v_mfma_f32_16x16x32_bf16 a[132:135], v[160:163], v[56:59], a[132:135]\n" \
"v_mfma_f32_16x16x32_bf16 a[136:139], v[168:171], v[56:59], a[136:139]\n" \
"v_mfma_f32_16x16x32_bf16 a[136:139], v[164:167], v[52:55], a[136:139]\n" \
"s_waitcnt lgkmcnt(7)\n" \
"v_mfma_f32_16x16x32_bf16 a[140:143], v[172:175], v[52:55], a[140:143]\n" \
"v_mfma_f32_16x16x32_bf16 a[140:143], v[176:179], v[56:59], a[140:143]\n" \
"v_mfma_f32_16x16x32_bf16 a[160:163], v[152:155], v[64:67], a[160:163]\n" \
"v_mfma_f32_16x16x32_bf16 a[160:163], v[148:151], v[60:63], a[160:163]\n" \
"v_mfma_f32_16x16x32_bf16 a[164:167], v[156:159], v[60:63], a[164:167]\n" \
"v_mfma_f32_16x16x32_bf16 a[164:167], v[160:163], v[64:67], a[164:167]\n" \
"v_mfma_f32_16x16x32_bf16 a[168:171], v[168:171], v[64:67], a[168:171]\n" \
"v_mfma_f32_16x16x32_bf16 a[168:171], v[164:167], v[60:63], a[168:171]\n" \
"v_mfma_f32_16x16x32_bf16 a[172:175], v[172:175], v[60:63], a[172:175]\n" \
"v_mfma_f32_16x16x32_bf16 a[172:175], v[176:179], v[64:67], a[172:175]\n" \
"v_mfma_f32_16x16x32_bf16 a[192:195], v[152:155], v[72:75], a[192:195]\n" \
"v_mfma_f32_16x16x32_bf16 a[192:195], v[148:151], v[68:71], a[192:195]\n" \
"s_waitcnt vmcnt(3) lgkmcnt(0)\n" \
"s_barrier\n" \
"v_mfma_f32_16x16x32_bf16 a[196:199], v[156:159], v[68:71], a[196:199]\n" \
"s_add_u32 m0, s40, 0x8000\n" \
"s_add_u32 s85, s72, 0x0\n" \
"s_nop 0\n" \
"v_mfma_f32_16x16x32_bf16 a[196:199], v[160:163], v[72:75], a[196:199]\n" \
"buffer_load_dwordx4 v20, s[80:83], s85 offen sc1 lds\n" \
"v_mfma_f32_16x16x32_bf16 a[200:203], v[168:171], v[72:75], a[200:203]\n" \
"s_add_u32 m0, s40, 0x9000\n" \
"s_add_u32 s84, s72, 0x1000\n" \
"v_mfma_f32_16x16x32_bf16 a[200:203], v[164:167], v[68:71], a[200:203]\n" \
"ds_read_b64_tr_b16 v[140:141], v39 offset:0\n" \
"ds_read_b64_tr_b16 v[142:143], v39 offset:960\n" \
"v_mfma_f32_16x16x32_bf16 a[204:207], v[172:175], v[68:71], a[204:207]\n" \
"buffer_load_dwordx4 v20, s[80:83], s84 offen sc1 lds\n" \
"v_mfma_f32_16x16x32_bf16 a[204:207], v[176:179], v[72:75], a[204:207]\n" \
"ds_read_b64_tr_b16 v[128:129], v37 offset:4096\n" \
"ds_read_b64_tr_b16 v[130:131], v37 offset:4608\n" \
"v_mfma_f32_16x16x32_bf16 a[224:227], v[152:155], v[80:83], a[224:227]\n" \
"s_add_u32 m0, s40, 0xb000\n" \
"s_add_u32 s85, s62, 0x0\n" \
"v_mfma_f32_16x16x32_bf16 a[224:227], v[148:151], v[76:79], a[224:227]\n" \
"buffer_load_dwordx4 v21, s[76:79], s85 offen lds\n" \
"v_mfma_f32_16x16x32_bf16 a[228:231], v[156:159], v[76:79], a[228:231]\n" \
"ds_read_b64_tr_b16 v[124:125], v37 offset:0\n" \
"ds_read_b64_tr_b16 v[126:127], v37 offset:512\n" \
"v_mfma_f32_16x16x32_bf16 a[228:231], v[160:163], v[80:83], a[228:231]\n" \
"s_add_u32 m0, %[mA], 0x8000\n" \
"s_add_u32 s84, s62, 0x0\n" \
"v_mfma_f32_16x16x32_bf16 a[232:235], v[168:171], v[80:83], a[232:235]\n" \
"buffer_load_dwordx4 v24, s[76:79], s84 offen lds\n" \
"v_mfma_f32_16x16x32_bf16 a[232:235], v[164:167], v[76:79], a[232:235]\n" \
"ds_read_b64_tr_b16 v[132:133], v38 offset:0\n" \
"ds_read_b64_tr_b16 v[134:135], v38 offset:512\n" \
"v_mfma_f32_16x16x32_bf16 a[236:239], v[172:175], v[76:79], a[236:239]\n" \
"ds_read_b64_tr_b16 v[136:137], v38 offset:4096\n" \
"ds_read_b64_tr_b16 v[138:139], v38 offset:4608\n" \
"v_mfma_f32_16x16x32_bf16 a[236:239], v[176:179], v[80:83], a[236:239]\n" \
"s_add_u32 m0, %[mA], 0x8400\n" \
"s_add_u32 s85, s62, 0x2400\n" \
"v_mfma_f32_16x16x32_bf16 a[144:147], v[152:155], v[88:91], a[144:147]\n" \
"buffer_load_dwordx4 v25, s[76:79], s85 offen lds\n" \
"v_mfma_f32_16x16x32_bf16 a[144:147], v[148:151], v[84:87], a[144:147]\n" \
"ds_read_b128 v[52:55], v28 offset:0\n" \
"ds_read_b128 v[56:59], v29 offset:0\n" \
"v_mfma_f32_16x16x32_bf16 a[148:151], v[156:159], v[84:87], a[148:151]\n" \
"s_add_u32 m0, %[mB], 0x8000\n" \
"s_add_u32 s84, s63, 0x0\n" \
"s_nop 0\n" \
"v_mfma_f32_16x16x32_bf16 a[148:151], v[160:163], v[88:91], a[148:151]\n" \
"buffer_load_dwordx4 v26, s[68:71], s84 offen lds\n" \
"v_mfma_f32_16x16x32_bf16 a[152:155], v[168:171], v[88:91], a[152:155]\n" \
"ds_read_b64_tr_b16 v[116:117], v36 offset:0\n" \
"ds_read_b64_tr_b16 v[118:119], v36 offset:512\n" \
"v_mfma_f32_16x16x32_bf16 a[152:155], v[164:167], v[84:87], a[152:155]\n" \
"ds_read_b64_tr_b16 v[120:121], v36 offset:4096\n" \
"ds_read_b64_tr_b16 v[122:123], v36 offset:4608\n" \
"v_mfma_f32_16x16x32_bf16 a[156:159], v[172:175], v[84:87], a[156:159]\n" \
"s_add_u32 m0, %[mA], 0x8800\n" \
"s_add_u32 s85, s62, 0x4800\n" \
"v_mfma_f32_16x16x32_bf16 a[156:159], v[176:179], v[88:91], a[156:159]\n" \
"buffer_load_dwordx4 v24, s[76:79], s85 offen lds\n" \
"v_mfma_f32_16x16x32_bf16 a[176:179], v[152:155], v[96:99], a[176:179]\n" \
"ds_read_b128 v[84:87], v22 offset:0\n" \
"ds_read_b128 v[88:91], v23 offset:0\n" \
"v_mfma_f32_16x16x32_bf16 a[176:179], v[148:151], v[92:95], a[176:179]\n" \
"s_add_u32 m0, %[mA], 0x8c00\n" \
"s_add_u32 s84, s62, 0x6c00\n" \
"v_mfma_f32_16x16x32_bf16 a[180:183], v[156:159], v[92:95], a[180:183]\n" \
"buffer_load_dwordx4 v25, s[76:79], s84 offen lds\n" \
"v_mfma_f32_16x16x32_bf16 a[180:183], v[160:163], v[96:99], a[180:183]\n" \
"ds_read_b128 v[60:63], v28 offset:2048\n" \
"ds_read_b128 v[64:67], v29 offset:2048\n" \
"v_mfma_f32_16x16x32_bf16 a[184:187], v[168:171], v[96:99], a[184:187]\n" \
"ds_read_b128 v[68:71], v28 offset:4096\n" \
"ds_read_b128 v[72:75], v29 offset:4096\n" \
"v_mfma_f32_16x16x32_bf16 a[184:187], v[164:167], v[92:95], a[184:187]\n" \
"s_add_u32 m0, %[mB], 0x8400\n" \
"s_add_u32 s85, s63, 0x8000\n" \
"v_mfma_f32_16x16x32_bf16 a[188:191], v[172:175], v[92:95], a[188:191]\n" \
"buffer_load_dwordx4 v26, s[68:71], s85 offen lds\n" \
"v_mfma_f32_16x16x32_bf16 a[188:191], v[176:179], v[96:99], a[188:191]\n" \
"ds_read_b128 v[76:79], v28 offset:6144\n" \
"ds_read_b128 v[80:83], v29 offset:6144\n" \
"v_mfma_f32_16x16x32_bf16 a[208:211], v[152:155], v[104:107], a[208:211]\n" \
"s_add_u32 m0, %[mA], 0x9000\n" \
"s_add_u32 s84, s62, 0x9000\n" \
"v_mfma_f32_16x16x32_bf16 a[208:211], v[148:151], v[100:103], a[208:211]\n" \
"buffer_load_dwordx4 v24, s[76:79], s84 offen lds\n" \
"v_mfma_f32_16x16x32_bf16 a[212:215], v[156:159], v[100:103], a[212:215]\n" \
"ds_read_b128 v[92:95], v48 offset:2048\n" \
"ds_read_b128 v[96:99], v49 offset:2048\n" \
"v_mfma_f32_16x16x32_bf16 a[212:215], v[160:163], v[104:107], a[212:215]\n" \
"v_mfma_f32_16x16x32_bf16 a[216:219], v[168:171], v[104:107], a[216:219]\n" \
"s_add_u32 m0, %[mA], 0x9400\n" \
"s_add_u32 s85, s62, 0xb400\n" \
"v_mfma_f32_16x16x32_bf16 a[216:219], v[164:167], v[100:103], a[216:219]\n" \
"buffer_load_dwordx4 v25, s[76:79], s85 offen lds\n" \
"v_mfma_f32_16x16x32_bf16 a[220:223], v[172:175], v[100:103], a[220:223]\n" \
"ds_read_b128 v[100:103], v48 offset:4096\n" \
"v_mfma_f32_16x16x32_bf16 a[220:223], v[176:179], v[104:107], a[220:223]\n" \
"s_add_u32 m0, %[mB], 0x8800\n" \
"s_add_u32 s84, s63, 0x10000\n" \
"v_mfma_f32_16x16x32_bf16 a[240:243], v[152:155], v[112:115], a[240:243]\n" \
"buffer_load_dwordx4 v27, s[68:71], s84 offen lds\n" \
"v_mfma_f32_16x16x32_bf16 a[240:243], v[148:151], v[108:111], a[240:243]\n" \
"ds_read_b64_tr_b16 v[152:153], v32 offset:8192\n" \
"ds_read_b64_tr_b16 v[154:155], v32 offset:9216\n" \
"v_mfma_f32_16x16x32_bf16 a[244:247], v[156:159], v[108:111], a[244:247]\n" \
"ds_read_b64_tr_b16 v[148:149], v32 offset:0\n" \
"ds_read_b64_tr_b16 v[150:151], v32 offset:1024\n" \
"v_mfma_f32_16x16x32_bf16 a[244:247], v[160:163], v[112:115], a[244:247]\n" \
"s_add_u32 m0, %[mA], 0x9800\n" \
"s_add_u32 s85, s62, 0xd800\n" \
"v_mfma_f32_16x16x32_bf16 a[248:251], v[168:171], v[112:115], a[248:251]\n" \
"buffer_load_dwordx4 v24, s[76:79], s85 offen lds\n" \
"v_mfma_f32_16x16x32_bf16 a[248:251], v[164:167], v[108:111], a[248:251]\n" \
"ds_read_b64_tr_b16 v[156:157], v33 offset:0\n" \
"ds_read_b64_tr_b16 v[158:159], v33 offset:1024\n" \
"v_mfma_f32_16x16x32_bf16 a[252:255], v[172:175], v[108:111], a[252:255]\n" \
"ds_read_b64_tr_b16 v[160:161], v33 offset:8192\n" \
"ds_read_b64_tr_b16 v[162:163], v33 offset:9216\n" \
"v_mfma_f32_16x16x32_bf16 a[252:255], v[176:179], v[112:115], a[252:255]\n" \
"s_add_u32 m0, %[mA], 0x9c00\n" \
"s_waitcnt lgkmcnt(11)\n" \
"s_nop 0\n" \
"v_smfmac_f32_16x16x64_bf16 a[16:19], v[140:143], v[84:91], v50\n" \
"v_mfma_f32_16x16x32_bf16 a[48:51], v[128:131], v[88:91], a[48:51]\n" \
"s_add_u32 s85, s62, 0xfc00\n" \
"ds_read_b64_tr_b16 v[168:169], v34 offset:8192\n" \
"v_mfma_f32_16x16x32_bf16 a[48:51], v[124:127], v[84:87], a[48:51]\n" \
"buffer_load_dwordx4 v25, s[76:79], s85 offen lds\n" \
"v_mfma_f32_16x16x32_bf16 a[80:83], v[84:87], v[132:135], a[80:83]\n" \
"s_add_u32 m0, %[mB], 0x8c00\n" \
"s_add_u32 s84, s63, 0x18000\n" \
"v_mfma_f32_16x16x32_bf16 a[80:83], v[88:91], v[136:139], a[80:83]\n" \
"ds_read_b64_tr_b16 v[170:171], v34 offset:9216\n" \
"ds_read_b64_tr_b16 v[164:165], v34 offset:0\n" \
"v_smfmac_f32_16x16x64_bf16 a[112:115], v[140:143], v[52:59], v50\n" \
"v_mfma_f32_16x16x32_bf16 a[20:23], v[128:131], v[56:59], a[20:23]\n" \
"buffer_load_dwordx4 v27, s[68:71], s84 offen lds\n" \
"v_mfma_f32_16x16x32_bf16 a[20:23], v[124:127], v[52:55], a[20:23]\n" \
"s_add_u32 s89, s44, 0x1000\n" \
"ds_read_b64_tr_b16 v[166:167], v34 offset:1024\n" \
"v_mfma_f32_16x16x32_bf16 a[52:55], v[116:119], v[52:55], a[52:55]\n" \
"ds_read_b64_tr_b16 v[172:173], v35 offset:0\n" \
"ds_read_b64_tr_b16 v[174:175], v35 offset:1024\n" \
"v_mfma_f32_16x16x32_bf16 a[52:55], v[120:123], v[56:59], a[52:55]\n" \
"buffer_store_dwordx4 v[212:215], v20, s[80:83], s89 offen offset:3072 sc1\n" \
"v_mfma_f32_16x16x32_bf16 a[84:87], v[56:59], v[136:139], a[84:87]\n" \
"ds_read_b64_tr_b16 v[176:177], v35 offset:8192\n" \
"ds_read_b64_tr_b16 v[178:179], v35 offset:9216\n" \
"v_mfma_f32_16x16x32_bf16 a[84:87], v[52:55], v[132:135], a[84:87]\n" \
"ds_read_b128 v[84:87], v48 offset:0\n" \
"ds_read_b128 v[88:91], v49 offset:0\n" \
"v_smfmac_f32_16x16x64_bf16 a[116:119], v[140:143], v[60:67], v50\n" \
"v_mfma_f32_16x16x32_bf16 a[24:27], v[124:127], v[60:63], a[24:27]\n" \
"ds_read_b128 v[104:107], v49 offset:4096\n" \
"ds_read_b128 v[108:111], v48 offset:6144\n" \
"v_mfma_f32_16x16x32_bf16 a[24:27], v[128:131], v[64:67], a[24:27]\n" \
"s_add_u32 s88, s42, 0x0\n" \
"ds_read_b128 v[112:115], v49 offset:6144\n" \
"v_mfma_f32_16x16x32_bf16 a[56:59], v[120:123], v[64:67], a[56:59]\n" \
"buffer_store_dwordx4 v[216:219], v20, s[80:83], s88 offen offset:0 sc1\n" \
"v_mfma_f32_16x16x32_bf16 a[56:59], v[116:119], v[60:63], a[56:59]\n" \
"s_add_u32 s62, s62, 0x80\n" \
"s_add_u32 s94, %[b], %[w]\n" \
"v_mfma_f32_16x16x32_bf16 a[88:91], v[60:63], v[132:135], a[88:91]\n" \
"s_cmp_lt_u32 s62, 0x480\n" \
"s_cselect_b32 s62, s62, 0\n" \
"v_mfma_f32_16x16x32_bf16 a[88:91], v[64:67], v[136:139], a[88:91]\n" \
"s_lshl_b32 s72, s62, 6\n" \
"s_add_u32 s72, s72, s37\n" \
"v_smfmac_f32_16x16x64_bf16 a[120:123], v[140:143], v[68:75], v50\n" \
"v_mfma_f32_16x16x32_bf16 a[28:31], v[128:131], v[72:75], a[28:31]\n" \
"s_lshl_b32 s63, s62, 12\n" \
"s_and_b32 s96, 7, %[b]\n" \
"v_mfma_f32_16x16x32_bf16 a[28:31], v[124:127], v[68:71], a[28:31]\n" \
"s_cmp_lg_u32 s94, 0\n" \
"s_cselect_b32 s95, 0x40000000, 0x5bc0000\n" \
"v_mfma_f32_16x16x32_bf16 a[60:63], v[116:119], v[68:71], a[60:63]\n" \
"s_add_u32 s89, s42, 0x1000\n" \
"s_mul_i32 s97, 0x48, s96\n" \
"v_mfma_f32_16x16x32_bf16 a[60:63], v[120:123], v[72:75], a[60:63]\n" \
"buffer_store_dwordx4 v[220:223], v20, s[80:83], s89 offen offset:0 sc1\n" \
"v_mfma_f32_16x16x32_bf16 a[92:95], v[72:75], v[136:139], a[92:95]\n" \
"s_add_u32 s98, 0x47, s97\n" \
"v_writelane_b32 v11, s95, 50\n" \
"v_mfma_f32_16x16x32_bf16 a[92:95], v[68:71], v[132:135], a[92:95]\n" \
"s_cmp_lt_u32 %[w], 3\n" \
"s_cselect_b32 s99, s98, 0xfff\n" \
"v_smfmac_f32_16x16x64_bf16 a[124:127], v[140:143], v[76:83], v50\n" \
"v_mfma_f32_16x16x32_bf16 a[0:3], v[124:127], v[76:79], a[0:3]\n" \
"s_lshr_b32 s94, %[b], 3\n" \
"s_lshl_b32 s67, s96, 2\n" \
"v_mfma_f32_16x16x32_bf16 a[0:3], v[128:131], v[80:83], a[0:3]\n" \
"s_and_b32 s95, 3, s94\n" \
"s_mul_i32 s100, 0x480, s99\n" \
"v_mfma_f32_16x16x32_bf16 a[4:7], v[120:123], v[80:83], a[4:7]\n" \
"s_lshl_b32 s64, %[w], 5\n" \
"s_add_u32 s98, s67, s95\n" \
"v_mfma_f32_16x16x32_bf16 a[4:7], v[116:119], v[76:79], a[4:7]\n" \
"s_add_u32 s101, 0x120, s100\n" \
"s_add_u32 s65, s97, s64\n" \
"v_mfma_f32_16x16x32_bf16 a[32:35], v[76:79], v[132:135], a[32:35]\n" \
"s_mul_i32 s99, 0x24000, s98\n" \
"v_writelane_b32 v11, s101, 15\n" \
"v_mfma_f32_16x16x32_bf16 a[32:35], v[80:83], v[136:139], a[32:35]\n" \
"s_mul_i32 s66, 0x480, s65\n" \
"s_waitcnt lgkmcnt(0)\n" \
"v_mfma_f32_16x16x32_bf16 a[128:131], v[152:155], v[56:59], a[128:131]\n" \
"s_add_u32 s100, 0x1b000, s99\n" \
"s_min_u32 s97, 2, %[w]\n" \
"v_mfma_f32_16x16x32_bf16 a[128:131], v[148:151], v[52:55], a[128:131]\n" \
"s_cmp_lt_u32 %[w], 3\n" \
"s_cselect_b32 s101, s66, s100\n" \
"v_mfma_f32_16x16x32_bf16 a[132:135], v[156:159], v[52:55], a[132:135]\n" \
"s_mul_i32 s64, 0x2400, s97\n" \
"v_writelane_b32 v11, s101, 16\n" \
"v_mfma_f32_16x16x32_bf16 a[132:135], v[160:163], v[56:59], a[132:135]\n" \
"s_add_u32 s65, 0x20000, s64\n" \
"s_mul_i32 s94, 0x9000, s97\n" \
"v_mfma_f32_16x16x32_bf16 a[136:139], v[168:171], v[56:59], a[136:139]\n" \
"s_mul_i32 s95, 0x2400, %[w]\n" \
"v_writelane_b32 v11, s65, 17\n" \
"v_mfma_f32_16x16x32_bf16 a[136:139], v[164:167], v[52:55], a[136:139]\n" \
"s_add_u32 s67, s99, s94\n" \
"v_writelane_b32 v11, s67, 22\n" \
"v_mfma_f32_16x16x32_bf16 a[140:143], v[172:175], v[52:55], a[140:143]\n" \
"s_cmp_lt_u32 %[w], 3\n" \
"s_cselect_b32 s98, s95, 0x12000\n" \
"v_mfma_f32_16x16x32_bf16 a[140:143], v[176:179], v[56:59], a[140:143]\n" \
"s_add_u32 s66, 0x20000, s95\n" \
"v_writelane_b32 v11, s98, 18\n" \
"v_mfma_f32_16x16x32_bf16 a[160:163], v[152:155], v[64:67], a[160:163]\n" \
"s_cmp_lt_u32 %[w], 3\n" \
"s_cselect_b32 s100, s66, 0xfc00\n" \
"v_mfma_f32_16x16x32_bf16 a[160:163], v[148:151], v[60:63], a[160:163]\n" \
"v_writelane_b32 v11, s95, 19\n" \
"s_lshl_b32 s101, %[w], 10\n" \
"v_mfma_f32_16x16x32_bf16 a[164:167], v[156:159], v[60:63], a[164:167]\n" \
"s_lshr_b32 s65, s96, 1\n" \
"v_writelane_b32 v11, s100, 20\n" \
"v_mfma_f32_16x16x32_bf16 a[164:167], v[160:163], v[64:67], a[164:167]\n" \
"s_cmp_eq_u32 %[w], s65\n" \
"s_cselect_b32 s97, 0xff0000, 0\n" \
"v_mfma_f32_16x16x32_bf16 a[168:171], v[168:171], v[64:67], a[168:171]\n" \
"s_add_u32 s64, 0x1d400, s101\n" \
"v_writelane_b32 v11, s64, 21\n" \
"v_mfma_f32_16x16x32_bf16 a[168:171], v[164:167], v[60:63], a[168:171]\n" \
"v_writelane_b32 v11, s97, 6\n" \
"v_mfma_f32_16x16x32_bf16 a[172:175], v[172:175], v[60:63], a[172:175]\n" \
"v_mfma_f32_16x16x32_bf16 a[172:175], v[176:179], v[64:67], a[172:175]\n" \
"v_mfma_f32_16x16x32_bf16 a[192:195], v[152:155], v[72:75], a[192:195]\n" \
"v_mfma_f32_16x16x32_bf16 a[192:195], v[148:151], v[68:71], a[192:195]\n" \
"s_waitcnt vmcnt(3)\n" \
"s_barrier\n" \
"v_mfma_f32_16x16x32_bf16 a[196:199], v[156:159], v[68:71], a[196:199]\n" \
"s_add_u32 m0, s40, 0x0\n" \
"s_add_u32 s85, s72, 0x0\n" \
"s_nop 0\n" \
"v_mfma_f32_16x16x32_bf16 a[196:199], v[160:163], v[72:75], a[196:199]\n" \
"buffer_load_dwordx4 v20, s[80:83], s85 offen sc1 lds\n" \
"v_mfma_f32_16x16x32_bf16 a[200:203], v[168:171], v[72:75], a[200:203]\n" \
"s_add_u32 m0, s40, 0x1000\n" \
"s_add_u32 s84, s72, 0x1000\n" \
"v_mfma_f32_16x16x32_bf16 a[200:203], v[164:167], v[68:71], a[200:203]\n" \
"ds_read_b64_tr_b16 v[140:141], v39 offset:32768\n" \
"ds_read_b64_tr_b16 v[142:143], v39 offset:33728\n" \
"v_mfma_f32_16x16x32_bf16 a[204:207], v[172:175], v[68:71], a[204:207]\n" \
"buffer_load_dwordx4 v20, s[80:83], s84 offen sc1 lds\n" \
"v_mfma_f32_16x16x32_bf16 a[204:207], v[176:179], v[72:75], a[204:207]\n" \
"ds_read_b64_tr_b16 v[128:129], v37 offset:36864\n" \
"ds_read_b64_tr_b16 v[130:131], v37 offset:37376\n" \
"v_mfma_f32_16x16x32_bf16 a[224:227], v[152:155], v[80:83], a[224:227]\n" \
"s_add_u32 m0, s40, 0x3000\n" \
"s_add_u32 s85, s62, 0x0\n" \
"v_mfma_f32_16x16x32_bf16 a[224:227], v[148:151], v[76:79], a[224:227]\n" \
"buffer_load_dwordx4 v21, s[76:79], s85 offen lds\n" \
"v_mfma_f32_16x16x32_bf16 a[228:231], v[156:159], v[76:79], a[228:231]\n" \
"ds_read_b64_tr_b16 v[124:125], v37 offset:32768\n" \
"ds_read_b64_tr_b16 v[126:127], v37 offset:33280\n" \
"v_mfma_f32_16x16x32_bf16 a[228:231], v[160:163], v[80:83], a[228:231]\n" \
"s_add_u32 m0, %[mA], 0x0\n" \
"s_add_u32 s84, s62, 0x0\n" \
"s_nop 0\n" \
"v_mfma_f32_16x16x32_bf16 a[232:235], v[168:171], v[80:83], a[232:235]\n" \
"buffer_load_dwordx4 v24, s[76:79], s84 offen lds\n" \
"L_pf6:\n" \
"s_cbranch_execz L_pf10\n" \
"s_nop 0\n" \
"v_mfma_f32_16x16x32_bf16 a[232:235], v[164:167], v[76:79], a[232:235]\n" \
"ds_read_b64_tr_b16 v[132:133], v38 offset:32768\n" \
"ds_read_b64_tr_b16 v[134:135], v38 offset:33280\n" \
"v_mfma_f32_16x16x32_bf16 a[236:239], v[172:175], v[76:79], a[236:239]\n" \
"ds_read_b64_tr_b16 v[136:137], v38 offset:36864\n" \
"ds_read_b64_tr_b16 v[138:139], v38 offset:37376\n" \
"v_mfma_f32_16x16x32_bf16 a[236:239], v[176:179], v[80:83], a[236:239]\n" \
"s_add_u32 m0, %[mA], 0x400\n" \
"s_add_u32 s85, s62, 0x2400\n" \
"v_mfma_f32_16x16x32_bf16 a[144:147], v[152:155], v[88:91], a[144:147]\n" \
"buffer_load_dwordx4 v25, s[76:79], s85 offen lds\n" \
"v_mfma_f32_16x16x32_bf16 a[144:147], v[148:151], v[84:87], a[144:147]\n" \
"ds_read_b128 v[52:55], v28 offset:32768\n" \
"ds_read_b128 v[56:59], v29 offset:32768\n" \
"v_mfma_f32_16x16x32_bf16 a[148:151], v[156:159], v[84:87], a[148:151]\n" \
"s_add_u32 m0, %[mB], 0x0\n" \
"s_add_u32 s84, s63, 0x0\n" \
"v_mfma_f32_16x16x32_bf16 a[148:151], v[160:163], v[88:91], a[148:151]\n" \
"buffer_load_dwordx4 v26, s[68:71], s84 offen lds\n" \
"v_mfma_f32_16x16x32_bf16 a[152:155], v[168:171], v[88:91], a[152:155]\n" \
"ds_read_b64_tr_b16 v[116:117], v36 offset:32768\n" \
"ds_read_b64_tr_b16 v[118:119], v36 offset:33280\n" \
"v_mfma_f32_16x16x32_bf16 a[152:155], v[164:167], v[84:87], a[152:155]\n" \
"ds_read_b64_tr_b16 v[120:121], v36 offset:36864\n" \
"ds_read_b64_tr_b16 v[122:123], v36 offset:37376\n" \
"v_mfma_f32_16x16x32_bf16 a[156:159], v[172:175], v[84:87], a[156:159]\n" \
"s_add_u32 m0, %[mA], 0x800\n" \
"s_add_u32 s85, s62, 0x4800\n" \
"v_mfma_f32_16x16x32_bf16 a[156:159], v[176:179], v[88:91], a[156:159]\n" \
"buffer_load_dwordx4 v24, s[76:79], s85 offen lds\n" \
"v_mfma_f32_16x16x32_bf16 a[176:179], v[152:155], v[96:99], a[176:179]\n" \
"ds_read_b128 v[84:87], v22 offset:32768\n" \
"ds_read_b128 v[88:91], v23 offset:32768\n" \
"v_mfma_f32_16x16x32_bf16 a[176:179], v[148:151], v[92:95], a[176:179]\n" \
"s_add_u32 m0, %[mA], 0xc00\n" \
"s_add_u32 s84, s62, 0x6c00\n" \
"v_mfma_f32_16x16x32_bf16 a[180:183], v[156:159], v[92:95], a[180:183]\n" \
"buffer_load_dwordx4 v25, s[76:79], s84 offen lds\n" \
"v_mfma_f32_16x16x32_bf16 a[180:183], v[160:163], v[96:99], a[180:183]\n" \
"ds_read_b128 v[60:63], v28 offset:34816\n" \
"ds_read_b128 v[64:67], v29 offset:34816\n" \
"v_mfma_f32_16x16x32_bf16 a[184:187], v[168:171], v[96:99], a[184:187]\n" \
"ds_read_b128 v[68:71], v28 offset:36864\n" \
"ds_read_b128 v[72:75], v29 offset:36864\n" \
"v_mfma_f32_16x16x32_bf16 a[184:187], v[164:167], v[92:95], a[184:187]\n" \
"s_add_u32 m0, %[mB], 0x400\n" \
"s_add_u32 s85, s63, 0x8000\n" \
"v_mfma_f32_16x16x32_bf16 a[188:191], v[172:175], v[92:95], a[188:191]\n" \
"buffer_load_dwordx4 v26, s[68:71], s85 offen lds\n" \
"v_mfma_f32_16x16x32_bf16 a[188:191], v[176:179], v[96:99], a[188:191]\n" \
"ds_read_b128 v[76:79], v28 offset:38912\n" \
"ds_read_b128 v[80:83], v29 offset:38912\n" \
"v_mfma_f32_16x16x32_bf16 a[208:211], v[152:155], v[104:107], a[208:211]\n" \
"s_add_u32 m0, %[mA], 0x1000\n" \
"s_add_u32 s84, s62, 0x9000\n" \
"v_mfma_f32_16x16x32_bf16 a[208:211], v[148:151], v[100:103], a[208:211]\n" \
"buffer_load_dwordx4 v24, s[76:79], s84 offen lds\n" \
"v_mfma_f32_16x16x32_bf16 a[212:215], v[156:159], v[100:103], a[212:215]\n" \
"ds_read_b128 v[92:95], v48 offset:34816\n" \
"ds_read_b128 v[96:99], v49 offset:34816\n" \
"v_mfma_f32_16x16x32_bf16 a[212:215], v[160:163], v[104:107], a[212:215]\n" \
"v_mfma_f32_16x16x32_bf16 a[216:219], v[168:171], v[104:107], a[216:219]\n" \
"s_add_u32 m0, %[mA], 0x1400\n" \
"s_add_u32 s85, s62, 0xb400\n" \
"v_mfma_f32_16x16x32_bf16 a[216:219], v[164:167], v[100:103], a[216:219]\n" \
"buffer_load_dwordx4 v25, s[76:79], s85 offen lds\n" \
"v_mfma_f32_16x16x32_bf16 a[220:223], v[172:175], v[100:103], a[220:223]\n" \
"ds_read_b128 v[100:103], v48 offset:36864\n" \
"v_mfma_f32_16x16x32_bf16 a[220:223], v[176:179], v[104:107], a[220:223]\n" \
"s_add_u32 m0, %[mB], 0x800\n" \
"s_add_u32 s84, s63, 0x10000\n" \
"v_mfma_f32_16x16x32_bf16 a[240:243], v[152:155], v[112:115], a[240:243]\n" \
"buffer_load_dwordx4 v27, s[68:71], s84 offen lds\n" \
"v_mfma_f32_16x16x32_bf16 a[240:243], v[148:151], v[108:111], a[240:243]\n" \
"ds_read_b64_tr_b16 v[152:153], v32 offset:40960\n" \
"ds_read_b64_tr_b16 v[154:155], v32 offset:41984\n" \
"v_mfma_f32_16x16x32_bf16 a[244:247], v[156:159], v[108:111], a[244:247]\n" \
"ds_read_b64_tr_b16 v[148:149], v32 offset:32768\n" \
"ds_read_b64_tr_b16 v[150:151], v32 offset:33792\n" \
"v_mfma_f32_16x16x32_bf16 a[244:247], v[160:163], v[112:115], a[244:247]\n" \
"s_add_u32 m0, %[mA], 0x1800\n" \
"s_add_u32 s85, s62, 0xd800\n" \
"v_mfma_f32_16x16x32_bf16 a[248:251], v[168:171], v[112:115], a[248:251]\n" \
"buffer_load_dwordx4 v24, s[76:79], s85 offen lds\n" \
"v_mfma_f32_16x16x32_bf16 a[248:251], v[164:167], v[108:111], a[248:251]\n" \
"ds_read_b64_tr_b16 v[156:157], v33 offset:32768\n" \
"ds_read_b64_tr_b16 v[158:159], v33 offset:33792\n" \
"v_mfma_f32_16x16x32_bf16 a[252:255], v[172:175], v[108:111], a[252:255]\n" \
"ds_read_b64_tr_b16 v[160:161], v33 offset:40960\n" \
"ds_read_b64_tr_b16 v[162:163], v33 offset:41984\n" \
"v_mfma_f32_16x16x32_bf16 a[252:255], v[176:179], v[112:115], a[252:255]\n" \
"s_add_u32 m0, %[mA], 0x1c00\n" \
"s_waitcnt lgkmcnt(11)\n" \
"s_nop 0\n" \
"v_smfmac_f32_16x16x64_bf16 a[16:19], v[140:143], v[84:91], v50\n" \
"v_mfma_f32_16x16x32_bf16 a[48:51], v[128:131], v[88:91], a[48:51]\n" \
"s_add_u32 s85, s62, 0xfc00\n" \
"ds_read_b64_tr_b16 v[168:169], v34 offset:40960\n" \
"v_mfma_f32_16x16x32_bf16 a[48:51], v[124:127], v[84:87], a[48:51]\n" \
"buffer_load_dwordx4 v25, s[76:79], s85 offen lds\n" \
"v_mfma_f32_16x16x32_bf16 a[80:83], v[84:87], v[132:135], a[80:83]\n" \
"s_add_u32 m0, %[mB], 0xc00\n" \
"s_add_u32 s84, s63, 0x18000\n" \
"v_mfma_f32_16x16x32_bf16 a[80:83], v[88:91], v[136:139], a[80:83]\n" \
"ds_read_b64_tr_b16 v[170:171], v34 offset:41984\n" \
"ds_read_b64_tr_b16 v[164:165], v34 offset:32768\n" \
"v_smfmac_f32_16x16x64_bf16 a[112:115], v[140:143], v[52:59], v50\n" \
"v_mfma_f32_16x16x32_bf16 a[20:23], v[128:131], v[56:59], a[20:23]\n" \
"buffer_load_dwordx4 v27, s[68:71], s84 offen lds\n" \
"v_mfma_f32_16x16x32_bf16 a[20:23], v[124:127], v[52:55], a[20:23]\n" \
"s_add_u32 s88, s42, 0x2000\n" \
"ds_read_b64_tr_b16 v[166:167], v34 offset:33792\n" \
"v_mfma_f32_16x16x32_bf16 a[52:55], v[116:119], v[52:55], a[52:55]\n" \
"ds_read_b64_tr_b16 v[172:173], v35 offset:32768\n" \
"ds_read_b64_tr_b16 v[174:175], v35 offset:33792\n" \
"v_mfma_f32_16x16x32_bf16 a[52:55], v[120:123], v[56:59], a[52:55]\n" \
"buffer_store_dwordx4 v[224:227], v20, s[80:83], s88 offen offset:0 sc1\n" \
"v_mfma_f32_16x16x32_bf16 a[84:87], v[56:59], v[136:139], a[84:87]\n" \
"ds_read_b64_tr_b16 v[176:177], v35 offset:40960\n" \
"ds_read_b64_tr_b16 v[178:179], v35 offset:41984\n" \
"v_mfma_f32_16x16x32_bf16 a[84:87], v[52:55], v[132:135], a[84:87]\n" \
"ds_read_b128 v[84:87], v48 offset:32768\n" \
"ds_read_b128 v[88:91], v49 offset:32768\n" \
"v_smfmac_f32_16x16x64_bf16 a[116:119], v[140:143], v[60:67], v50\n" \
"v_mfma_f32_16x16x32_bf16 a[24:27], v[124:127], v[60:63], a[24:27]\n" \
"ds_read_b128 v[104:107], v49 offset:36864\n" \
"ds_read_b128 v[108:111], v48 offset:38912\n" \
"v_mfma_f32_16x16x32_bf16 a[24:27], v[128:131], v[64:67], a[24:27]\n" \
"s_add_u32 s89, s42, 0x3000\n" \
"ds_read_b128 v[112:115], v49 offset:38912\n" \
"v_mfma_f32_16x16x32_bf16 a[56:59], v[120:123], v[64:67], a[56:59]\n" \
"buffer_store_dwordx4 v[228:231], v20, s[80:83], s89 offen offset:0 sc1\n" \
"v_mfma_f32_16x16x32_bf16 a[56:59], v[116:119], v[60:63], a[56:59]\n" \
"s_add_u32 s62, s62, 0x80\n" \
"s_lshr_b32 s96, %[b], 3\n" \
"v_mfma_f32_16x16x32_bf16 a[88:91], v[60:63], v[132:135], a[88:91]\n" \
"s_cmp_lt_u32 s62, 0x480\n" \
"s_cselect_b32 s62, s62, 0\n" \
"v_mfma_f32_16x16x32_bf16 a[88:91], v[64:67], v[136:139], a[88:91]\n" \
"s_lshl_b32 s72, s62, 6\n" \
"s_add_u32 s72, s72, s37\n" \
"v_smfmac_f32_16x16x64_bf16 a[120:123], v[140:143], v[68:75], v50\n" \
"v_mfma_f32_16x16x32_bf16 a[28:31], v[128:131], v[72:75], a[28:31]\n" \
"s_lshl_b32 s63, s62, 12\n" \
"s_and_b32 s97, 3, s96\n" \
"v_mfma_f32_16x16x32_bf16 a[28:31], v[124:127], v[68:71], a[28:31]\n" \
"s_and_b32 s94, 7, %[b]\n" \
"s_add_u32 s98, 1, s97\n" \
"v_mfma_f32_16x16x32_bf16 a[60:63], v[116:119], v[68:71], a[60:63]\n" \
"s_add_u32 s88, s42, 0x0\n" \
"s_lshl_b32 s95, s94, 2\n" \
"v_mfma_f32_16x16x32_bf16 a[60:63], v[120:123], v[72:75], a[60:63]\n" \
"buffer_store_dwordx4 v[232:235], v20, s[80:83], s88 offen offset:1024 sc1\n" \
"v_mfma_f32_16x16x32_bf16 a[92:95], v[72:75], v[136:139], a[92:95]\n" \
"s_and_b32 s99, 3, s98\n" \
"s_add_u32 s100, s95, s99\n" \
"v_mfma_f32_16x16x32_bf16 a[92:95], v[68:71], v[132:135], a[92:95]\n" \
"s_mul_i32 s101, 0x24000, s100\n" \
"s_mul_i32 s64, 0x9000, %[w]\n" \
"v_smfmac_f32_16x16x64_bf16 a[124:127], v[140:143], v[76:83], v50\n" \
"v_mfma_f32_16x16x32_bf16 a[0:3], v[124:127], v[76:79], a[0:3]\n" \
"s_and_b32 s96, 2, %[w]\n" \
"s_add_u32 s65, s101, s64\n" \
"v_mfma_f32_16x16x32_bf16 a[0:3], v[128:131], v[80:83], a[0:3]\n" \
"s_lshr_b32 s66, %[b], 5\n" \
"s_lshl_b32 s98, s96, 1\n" \
"v_mfma_f32_16x16x32_bf16 a[4:7], v[120:123], v[80:83], a[4:7]\n" \
"s_and_b32 s99, 1, %[w]\n" \
"v_writelane_b32 v11, s65, 23\n" \
"v_mfma_f32_16x16x32_bf16 a[4:7], v[116:119], v[76:79], a[4:7]\n" \
"s_lshl_b32 s67, s66, 3\n" \
"s_add_u32 s101, s98, s99\n" \
"v_mfma_f32_16x16x32_bf16 a[32:35], v[76:79], v[132:135], a[32:35]\n" \
"s_add_u32 s65, s67, s101\n" \
"s_mul_i32 s66, 0x24800, s65\n" \
"v_mfma_f32_16x16x32_bf16 a[32:35], v[80:83], v[136:139], a[32:35]\n" \
"s_lshl_b32 s98, s100, 12\n" \
"s_waitcnt lgkmcnt(0)\n" \
"v_mfma_f32_16x16x32_bf16 a[128:131], v[152:155], v[56:59], a[128:131]\n" \
"s_add_u32 s96, 0x3000000, s66\n" \
"s_add_u32 s99, 0x4800, s98\n" \
"v_mfma_f32_16x16x32_bf16 a[128:131], v[148:151], v[52:55], a[128:131]\n" \
"s_add_u32 s65, s96, s99\n" \
"s_sub_u32 s100, 0x78, s67\n" \
"v_mfma_f32_16x16x32_bf16 a[132:135], v[156:159], v[52:55], a[132:135]\n" \
"v_writelane_b32 v11, s65, 26\n" \
"s_add_u32 s98, s100, s101\n" \
"v_mfma_f32_16x16x32_bf16 a[132:135], v[160:163], v[56:59], a[132:135]\n" \
"s_mul_i32 s65, 0x24800, s98\n" \
"s_add_u32 s67, 0x3000000, s65\n" \
"v_mfma_f32_16x16x32_bf16 a[136:139], v[168:171], v[56:59], a[136:139]\n" \
"s_add_u32 s101, 2, s97\n" \
"s_add_u32 s100, s67, s99\n" \
"v_mfma_f32_16x16x32_bf16 a[136:139], v[164:167], v[52:55], a[136:139]\n" \
"s_and_b32 s98, 3, s101\n" \
"v_writelane_b32 v11, s100, 29\n" \
"v_mfma_f32_16x16x32_bf16 a[140:143], v[172:175], v[52:55], a[140:143]\n" \
"s_add_u32 s99, s95, s98\n" \
"s_mul_i32 s100, 0x24000, s99\n" \
"v_mfma_f32_16x16x32_bf16 a[140:143], v[176:179], v[56:59], a[140:143]\n" \
"s_add_u32 s101, s100, s64\n" \
"s_lshl_b32 s98, s99, 12\n" \
"v_mfma_f32_16x16x32_bf16 a[160:163], v[152:155], v[64:67], a[160:163]\n" \
"v_writelane_b32 v11, s101, 24\n" \
"s_add_u32 s100, 0x4800, s98\n" \
"v_mfma_f32_16x16x32_bf16 a[160:163], v[148:151], v[60:63], a[160:163]\n" \
"s_add_u32 s101, s96, s100\n" \
"v_writelane_b32 v11, s101, 27\n" \
"v_mfma_f32_16x16x32_bf16 a[164:167], v[156:159], v[60:63], a[164:167]\n" \
"s_add_u32 s98, 3, s97\n" \
"s_add_u32 s99, s67, s100\n" \
"v_mfma_f32_16x16x32_bf16 a[164:167], v[160:163], v[64:67], a[164:167]\n" \
"s_and_b32 s101, 3, s98\n" \
"v_writelane_b32 v11, s99, 30\n" \
"v_mfma_f32_16x16x32_bf16 a[168:171], v[168:171], v[64:67], a[168:171]\n" \
"s_add_u32 s100, s95, s101\n" \
"s_mul_i32 s99, 0x24000, s100\n" \
"v_mfma_f32_16x16x32_bf16 a[168:171], v[164:167], v[60:63], a[168:171]\n" \
"s_lshl_b32 s98, s100, 12\n" \
"s_add_u32 s97, s99, s64\n" \
"v_mfma_f32_16x16x32_bf16 a[172:175], v[172:175], v[60:63], a[172:175]\n" \
"s_add_u32 s95, 0x4800, s98\n" \
"v_writelane_b32 v11, s97, 25\n" \
"v_mfma_f32_16x16x32_bf16 a[172:175], v[176:179], v[64:67], a[172:175]\n" \
"s_add_u32 s101, s96, s95\n" \
"v_writelane_b32 v11, s101, 28\n" \
"v_mfma_f32_16x16x32_bf16 a[192:195], v[152:155], v[72:75], a[192:195]\n" \
"s_add_u32 s99, s67, s95\n" \
"s_lshl_b32 s64, s94, 11\n" \
"v_mfma_f32_16x16x32_bf16 a[192:195], v[148:151], v[68:71], a[192:195]\n" \
"v_writelane_b32 v11, s99, 31\n" \
"s_add_u32 s97, s96, s64\n" \
"s_waitcnt vmcnt(3)\n" \
"s_barrier\n" \
"v_mfma_f32_16x16x32_bf16 a[196:199], v[156:159], v[68:71], a[196:199]\n" \
"s_add_u32 m0, s40, 0x8000\n" \
"s_add_u32 s85, s72, 0x0\n" \
"v_mfma_f32_16x16x32_bf16 a[196:199], v[160:163], v[72:75], a[196:199]\n" \
"buffer_load_dwordx4 v20, s[80:83], s85 offen sc1 lds\n" \
"v_mfma_f32_16x16x32_bf16 a[200:203], v[168:171], v[72:75], a[200:203]\n" \
"s_add_u32 m0, s40, 0x9000\n" \
"s_add_u32 s84, s72, 0x1000\n" \
"v_mfma_f32_16x16x32_bf16 a[200:203], v[164:167], v[68:71], a[200:203]\n" \
"ds_read_b64_tr_b16 v[140:141], v39 offset:0\n" \
"ds_read_b64_tr_b16 v[142:143], v39 offset:960\n" \
"v_mfma_f32_16x16x32_bf16 a[204:207], v[172:175], v[68:71], a[204:207]\n" \
"buffer_load_dwordx4 v20, s[80:83], s84 offen sc1 lds\n" \
"v_mfma_f32_16x16x32_bf16 a[204:207], v[176:179], v[72:75], a[204:207]\n" \
"ds_read_b64_tr_b16 v[128:129], v37 offset:4096\n" \
"ds_read_b64_tr_b16 v[130:131], v37 offset:4608\n" \
"v_mfma_f32_16x16x32_bf16 a[224:227], v[152:155], v[80:83], a[224:227]\n" \
"s_add_u32 m0, s40, 0xb000\n" \
"s_add_u32 s85, s62, 0x0\n" \
"v_mfma_f32_16x16x32_bf16 a[224:227], v[148:151], v[76:79], a[224:227]\n" \
"buffer_load_dwordx4 v21, s[76:79], s85 offen lds\n" \
"v_mfma_f32_16x16x32_bf16 a[228:231], v[156:159], v[76:79], a[228:231]\n" \
"ds_read_b64_tr_b16 v[124:125], v37 offset:0\n" \
"ds_read_b64_tr_b16 v[126:127], v37 offset:512\n" \
"v_mfma_f32_16x16x32_bf16 a[228:231], v[160:163], v[80:83], a[228:231]\n" \
"s_add_u32 m0, %[mA], 0x8000\n" \
"s_add_u32 s84, s62, 0x0\n" \
"v_mfma_f32_16x16x32_bf16 a[232:235], v[168:171], v[80:83], a[232:235]\n" \
"buffer_load_dwordx4 v24, s[76:79], s84 offen lds\n" \
"v_mfma_f32_16x16x32_bf16 a[232:235], v[164:167], v[76:79], a[232:235]\n" \
"ds_read_b64_tr_b16 v[132:133], v38 offset:0\n" \
"ds_read_b64_tr_b16 v[134:135], v38 offset:512\n" \
"v_mfma_f32_16x16x32_bf16 a[236:239], v[172:175], v[76:79], a[236:239]\n" \
"ds_read_b64_tr_b16 v[136:137], v38 offset:4096\n" \
"ds_read_b64_tr_b16 v[138:139], v38 offset:4608\n" \
"v_mfma_f32_16x16x32_bf16 a[236:239], v[176:179], v[80:83], a[236:239]\n" \
"s_add_u32 m0, %[mA], 0x8400\n" \
"s_add_u32 s85, s62, 0x2400\n" \
"v_mfma_f32_16x16x32_bf16 a[144:147], v[152:155], v[88:91], a[144:147]\n" \
"buffer_load_dwordx4 v25, s[76:79], s85 offen lds\n" \
"v_mfma_f32_16x16x32_bf16 a[144:147], v[148:151], v[84:87], a[144:147]\n" \
"ds_read_b128 v[52:55], v28 offset:0\n" \
"ds_read_b128 v[56:59], v29 offset:0\n" \
"v_mfma_f32_16x16x32_bf16 a[148:151], v[156:159], v[84:87], a[148:151]\n" \
"s_add_u32 m0, %[mB], 0x8000\n" \
"s_add_u32 s84, s63, 0x0\n" \
"s_nop 0\n" \
"v_mfma_f32_16x16x32_bf16 a[148:151], v[160:163], v[88:91], a[148:151]\n" \
"buffer_load_dwordx4 v26, s[68:71], s84 offen lds\n" \
"v_mfma_f32_16x16x32_bf16 a[152:155], v[168:171], v[88:91], a[152:155]\n" \
"ds_read_b64_tr_b16 v[116:117], v36 offset:0\n" \
"ds_read_b64_tr_b16 v[118:119], v36 offset:512\n" \
"v_mfma_f32_16x16x32_bf16 a[152:155], v[164:167], v[84:87], a[152:155]\n" \
"ds_read_b64_tr_b16 v[120:121], v36 offset:4096\n" \
"ds_read_b64_tr_b16 v[122:123], v36 offset:4608\n" \
"v_mfma_f32_16x16x32_bf16 a[156:159], v[172:175], v[84:87], a[156:159]\n" \
"s_add_u32 m0, %[mA], 0x8800\n" \
"s_add_u32 s85, s62, 0x4800\n" \
"v_mfma_f32_16x16x32_bf16 a[156:159], v[176:179], v[88:91], a[156:159]\n" \
"buffer_load_dwordx4 v24, s[76:79], s85 offen lds\n" \
"v_mfma_f32_16x16x32_bf16 a[176:179], v[152:155], v[96:99], a[176:179]\n" \
"ds_read_b128 v[84:87], v22 offset:0\n" \
"ds_read_b128 v[88:91], v23 offset:0\n" \
"v_mfma_f32_16x16x32_bf16 a[176:179], v[148:151], v[92:95], a[176:179]\n" \
"s_add_u32 m0, %[mA], 0x8c00\n" \
"s_add_u32 s84, s62, 0x6c00\n" \
"v_mfma_f32_16x16x32_bf16 a[180:183], v[156:159], v[92:95], a[180:183]\n" \
"buffer_load_dwordx4 v25, s[76:79], s84 offen lds\n" \
"v_mfma_f32_16x16x32_bf16 a[180:183], v[160:163], v[96:99], a[180:183]\n" \
"ds_read_b128 v[60:63], v28 offset:2048\n" \
"ds_read_b128 v[64:67], v29 offset:2048\n" \
"v_mfma_f32_16x16x32_bf16 a[184:187], v[168:171], v[96:99], a[184:187]\n" \
"ds_read_b128 v[68:71], v28 offset:4096\n" \
"ds_read_b128 v[72:75], v29 offset:4096\n" \
"v_mfma_f32_16x16x32_bf16 a[184:187], v[164:167], v[92:95], a[184:187]\n" \
"s_add_u32 m0, %[mB], 0x8400\n" \
"s_add_u32 s85, s63, 0x8000\n" \
"v_mfma_f32_16x16x32_bf16 a[188:191], v[172:175], v[92:95], a[188:191]\n" \
"buffer_load_dwordx4 v26, s[68:71], s85 offen lds\n" \
"v_mfma_f32_16x16x32_bf16 a[188:191], v[176:179], v[96:99], a[188:191]\n" \
"ds_read_b128 v[76:79], v28 offset:6144\n" \
"ds_read_b128 v[80:83], v29 offset:6144\n" \
"v_mfma_f32_16x16x32_bf16 a[208:211], v[152:155], v[104:107], a[208:211]\n" \
"s_add_u32 m0, %[mA], 0x9000\n" \
"s_add_u32 s84, s62, 0x9000\n" \
"v_mfma_f32_16x16x32_bf16 a[208:211], v[148:151], v[100:103], a[208:211]\n" \
"buffer_load_dwordx4 v24, s[76:79], s84 offen lds\n" \
"v_mfma_f32_16x16x32_bf16 a[212:215], v[156:159], v[100:103], a[212:215]\n" \
"ds_read_b128 v[92:95], v48 offset:2048\n" \
"ds_read_b128 v[96:99], v49 offset:2048\n" \
"v_mfma_f32_16x16x32_bf16 a[212:215], v[160:163], v[104:107], a[212:215]\n" \
"v_writelane_b32 v11, s97, 32\n" \
"s_and_b32 s99, 1, s94\n" \
"v_mfma_f32_16x16x32_bf16 a[216:219], v[168:171], v[104:107], a[216:219]\n" \
"s_add_u32 m0, %[mA], 0x9400\n" \
"s_add_u32 s85, s62, 0xb400\n" \
"v_mfma_f32_16x16x32_bf16 a[216:219], v[164:167], v[100:103], a[216:219]\n" \
"buffer_load_dwordx4 v25, s[76:79], s85 offen lds\n" \
"v_mfma_f32_16x16x32_bf16 a[220:223], v[172:175], v[100:103], a[220:223]\n" \
"ds_read_b128 v[100:103], v48 offset:4096\n" \
"s_add_u32 s100, s67, s64\n" \
"v_mfma_f32_16x16x32_bf16 a[220:223], v[176:179], v[104:107], a[220:223]\n" \
"s_add_u32 m0, %[mB], 0x8800\n" \
"s_add_u32 s84, s63, 0x10000\n" \
"v_mfma_f32_16x16x32_bf16 a[240:243], v[152:155], v[112:115], a[240:243]\n" \
"buffer_load_dwordx4 v27, s[68:71], s84 offen lds\n" \
"v_mfma_f32_16x16x32_bf16 a[240:243], v[148:151], v[108:111], a[240:243]\n" \
"ds_read_b64_tr_b16 v[152:153], v32 offset:8192\n" \
"ds_read_b64_tr_b16 v[154:155], v32 offset:9216\n" \
"v_mfma_f32_16x16x32_bf16 a[244:247], v[156:159], v[108:111], a[244:247]\n" \
"ds_read_b64_tr_b16 v[148:149], v32 offset:0\n" \
"ds_read_b64_tr_b16 v[150:151], v32 offset:1024\n" \
"v_mfma_f32_16x16x32_bf16 a[244:247], v[160:163], v[112:115], a[244:247]\n" \
"s_add_u32 m0, %[mA], 0x9800\n" \
"s_add_u32 s85, s62, 0xd800\n" \
"v_mfma_f32_16x16x32_bf16 a[248:251], v[168:171], v[112:115], a[248:251]\n" \
"buffer_load_dwordx4 v24, s[76:79], s85 offen lds\n" \
"v_mfma_f32_16x16x32_bf16 a[248:251], v[164:167], v[108:111], a[248:251]\n" \
"ds_read_b64_tr_b16 v[156:157], v33 offset:0\n" \
"ds_read_b64_tr_b16 v[158:159], v33 offset:1024\n" \
"v_mfma_f32_16x16x32_bf16 a[252:255], v[172:175], v[108:111], a[252:255]\n" \
"ds_read_b64_tr_b16 v[160:161], v33 offset:8192\n" \
"ds_read_b64_tr_b16 v[162:163], v33 offset:9216\n" \
"v_mfma_f32_16x16x32_bf16 a[252:255], v[176:179], v[112:115], a[252:255]\n" \
"s_add_u32 m0, %[mA], 0x9c00\n" \
"s_waitcnt lgkmcnt(11)\n" \
"s_nop 0\n" \
"v_smfmac_f32_16x16x64_bf16 a[16:19], v[140:143], v[84:91], v50\n" \
"v_mfma_f32_16x16x32_bf16 a[48:51], v[128:131], v[88:91], a[48:51]\n" \
"s_add_u32 s85, s62, 0xfc00\n" \
"ds_read_b64_tr_b16 v[168:169], v34 offset:8192\n" \
"v_mfma_f32_16x16x32_bf16 a[48:51], v[124:127], v[84:87], a[48:51]\n" \
"buffer_load_dwordx4 v25, s[76:79], s85 offen lds\n" \
"v_mfma_f32_16x16x32_bf16 a[80:83], v[84:87], v[132:135], a[80:83]\n" \
"s_add_u32 m0, %[mB], 0x8c00\n" \
"s_add_u32 s84, s63, 0x18000\n" \
"v_mfma_f32_16x16x32_bf16 a[80:83], v[88:91], v[136:139], a[80:83]\n" \
"ds_read_b64_tr_b16 v[170:171], v34 offset:9216\n" \
"ds_read_b64_tr_b16 v[164:165], v34 offset:0\n" \
"v_smfmac_f32_16x16x64_bf16 a[112:115], v[140:143], v[52:59], v50\n" \
"v_mfma_f32_16x16x32_bf16 a[20:23], v[128:131], v[56:59], a[20:23]\n" \
"buffer_load_dwordx4 v27, s[68:71], s84 offen lds\n" \
"v_mfma_f32_16x16x32_bf16 a[20:23], v[124:127], v[52:55], a[20:23]\n" \
"s_add_u32 s89, s42, 0x1000\n" \
"ds_read_b64_tr_b16 v[166:167], v34 offset:1024\n" \
"v_mfma_f32_16x16x32_bf16 a[52:55], v[116:119], v[52:55], a[52:55]\n" \
"ds_read_b64_tr_b16 v[172:173], v35 offset:0\n" \
"ds_read_b64_tr_b16 v[174:175], v35 offset:1024\n" \
"v_mfma_f32_16x16x32_bf16 a[52:55], v[120:123], v[56:59], a[52:55]\n" \
"buffer_store_dwordx4 v[236:239], v20, s[80:83], s89 offen offset:1024 sc1\n" \
"v_mfma_f32_16x16x32_bf16 a[84:87], v[56:59], v[136:139], a[84:87]\n" \
"ds_read_b64_tr_b16 v[176:177], v35 offset:8192\n" \
"ds_read_b64_tr_b16 v[178:179], v35 offset:9216\n" \
"v_mfma_f32_16x16x32_bf16 a[84:87], v[52:55], v[132:135], a[84:87]\n" \
"ds_read_b128 v[84:87], v48 offset:0\n" \
"ds_read_b128 v[88:91], v49 offset:0\n" \
"v_smfmac_f32_16x16x64_bf16 a[116:119], v[140:143], v[60:67], v50\n" \
"v_mfma_f32_16x16x32_bf16 a[24:27], v[124:127], v[60:63], a[24:27]\n" \
"ds_read_b128 v[104:107], v49 offset:4096\n" \
"ds_read_b128 v[108:111], v48 offset:6144\n" \
"v_mfma_f32_16x16x32_bf16 a[24:27], v[128:131], v[64:67], a[24:27]\n" \
"s_add_u32 s88, s42, 0x2000\n" \
"ds_read_b128 v[112:115], v49 offset:6144\n" \
"v_mfma_f32_16x16x32_bf16 a[56:59], v[120:123], v[64:67], a[56:59]\n" \
"buffer_store_dwordx4 v[240:243], v20, s[80:83], s88 offen offset:1024 sc1\n" \
"v_mfma_f32_16x16x32_bf16 a[56:59], v[116:119], v[60:63], a[56:59]\n" \
"s_add_u32 s62, s62, 0x80\n" \
"v_writelane_b32 v11, s100, 33\n" \
"v_mfma_f32_16x16x32_bf16 a[88:91], v[60:63], v[132:135], a[88:91]\n" \
"s_cmp_lt_u32 s62, 0x480\n" \
"s_cselect_b32 s62, s62, 0\n" \
"v_mfma_f32_16x16x32_bf16 a[88:91], v[64:67], v[136:139], a[88:91]\n" \
"s_lshl_b32 s72, s62, 6\n" \
"s_add_u32 s72, s72, s37\n" \
"v_smfmac_f32_16x16x64_bf16 a[120:123], v[140:143], v[68:75], v50\n" \
"v_mfma_f32_16x16x32_bf16 a[28:31], v[128:131], v[72:75], a[28:31]\n" \
"s_lshl_b32 s63, s62, 12\n" \
"s_add_u32 s98, 0x3004000, s66\n" \
"v_mfma_f32_16x16x32_bf16 a[28:31], v[124:127], v[68:71], a[28:31]\n" \
"s_cmp_lg_u32 s99, 0\n" \
"s_cselect_b32 s97, s67, s96\n" \
"v_mfma_f32_16x16x32_bf16 a[60:63], v[116:119], v[68:71], a[60:63]\n" \
"s_add_u32 s89, s42, 0x3000\n" \
"s_lshr_b32 s95, s94, 1\n" \
"v_mfma_f32_16x16x32_bf16 a[60:63], v[120:123], v[72:75], a[60:63]\n" \
"buffer_store_dwordx4 v[244:247], v20, s[80:83], s89 offen offset:1024 sc1\n" \
"v_mfma_f32_16x16x32_bf16 a[92:95], v[72:75], v[136:139], a[92:95]\n" \
"s_add_u32 s64, 0x4000, s97\n" \
"v_writelane_b32 v11, s98, 34\n" \
"v_mfma_f32_16x16x32_bf16 a[92:95], v[68:71], v[132:135], a[92:95]\n" \
"s_add_u32 s101, 0x3004000, s65\n" \
"s_and_b32 s96, 2, %[w]\n" \
"v_smfmac_f32_16x16x64_bf16 a[124:127], v[140:143], v[76:83], v50\n" \
"v_mfma_f32_16x16x32_bf16 a[0:3], v[124:127], v[76:79], a[0:3]\n" \
"s_cmp_eq_u32 %[w], s95\n" \
"s_cselect_b32 s100, s64, 0x40000000\n" \
"v_mfma_f32_16x16x32_bf16 a[0:3], v[128:131], v[80:83], a[0:3]\n" \
"v_writelane_b32 v11, s101, 35\n" \
"s_lshl_b32 s97, s96, 1\n" \
"v_mfma_f32_16x16x32_bf16 a[4:7], v[120:123], v[80:83], a[4:7]\n" \
"s_and_b32 s98, 1, %[w]\n" \
"v_writelane_b32 v11, s100, 36\n" \
"v_mfma_f32_16x16x32_bf16 a[4:7], v[116:119], v[76:79], a[4:7]\n" \
"s_lshr_b32 s94, %[b], 5\n" \
"s_add_u32 s99, s97, s98\n" \
"v_mfma_f32_16x16x32_bf16 a[32:35], v[76:79], v[132:135], a[32:35]\n" \
"s_lshl_b32 s95, s94, 7\n" \
"s_lshl_b32 s100, s99, 4\n" \
"v_mfma_f32_16x16x32_bf16 a[32:35], v[80:83], v[136:139], a[32:35]\n" \
"s_add_u32 s101, s95, s100\n" \
"s_waitcnt lgkmcnt(0)\n" \
"v_mfma_f32_16x16x32_bf16 a[128:131], v[152:155], v[56:59], a[128:131]\n" \
"s_mul_i32 s64, 0x480, s101\n" \
"s_lshr_b32 s65, %[b], 3\n" \
"v_mfma_f32_16x16x32_bf16 a[128:131], v[148:151], v[52:55], a[128:131]\n" \
"s_sub_u32 s99, 0x780, s95\n" \
"s_and_b32 s66, 3, s65\n" \
"v_mfma_f32_16x16x32_bf16 a[132:135], v[156:159], v[52:55], a[132:135]\n" \
"s_add_u32 s101, s99, s100\n" \
"s_mul_i32 s67, 0x120, s66\n" \
"v_mfma_f32_16x16x32_bf16 a[132:135], v[160:163], v[56:59], a[132:135]\n" \
"s_and_b32 s96, 7, %[b]\n" \
"s_mul_i32 s65, 0x480, s101\n" \
"v_mfma_f32_16x16x32_bf16 a[136:139], v[168:171], v[56:59], a[136:139]\n" \
"s_add_u32 s94, s64, s67\n" \
"s_lshl_b32 s97, s96, 5\n" \
"v_mfma_f32_16x16x32_bf16 a[136:139], v[164:167], v[52:55], a[136:139]\n" \
"s_add_u32 s66, s65, s67\n" \
"s_and_b32 s95, 1, s96\n" \
"v_mfma_f32_16x16x32_bf16 a[140:143], v[172:175], v[52:55], a[140:143]\n" \
"s_add_u32 s98, s94, s97\n" \
"v_writelane_b32 v11, s98, 39\n" \
"v_mfma_f32_16x16x32_bf16 a[140:143], v[176:179], v[56:59], a[140:143]\n" \
"s_cmp_lg_u32 s95, 0\n" \
"s_cselect_b32 s99, s66, s94\n" \
"v_mfma_f32_16x16x32_bf16 a[160:163], v[152:155], v[64:67], a[160:163]\n" \
"s_lshr_b32 s98, s96, 1\n" \
"s_add_u32 s100, 0x100, s99\n" \
"v_mfma_f32_16x16x32_bf16 a[160:163], v[148:151], v[60:63], a[160:163]\n" \
"s_add_u32 s64, s66, s97\n" \
"s_mov_b32 s94, s67\n" \
"v_mfma_f32_16x16x32_bf16 a[164:167], v[156:159], v[60:63], a[164:167]\n" \
"s_cmp_eq_u32 %[w], s98\n" \
"s_cselect_b32 s101, s100, 0x40000000\n" \
"v_mfma_f32_16x16x32_bf16 a[164:167], v[160:163], v[64:67], a[164:167]\n" \
"v_writelane_b32 v11, s64, 40\n" \
"v_writelane_b32 v11, s101, 41\n" \
"v_mfma_f32_16x16x32_bf16 a[168:171], v[168:171], v[64:67], a[168:171]\n" \
"s_add_u32 s96, s16, s94\n" \
"s_addc_u32 s97, s17, 0\n" \
"v_mfma_f32_16x16x32_bf16 a[168:171], v[164:167], v[60:63], a[168:171]\n" \
"s_add_u32 s98, s18, s94\n" \
"s_addc_u32 s99, s19, 0\n" \
"v_mfma_f32_16x16x32_bf16 a[172:175], v[172:175], v[60:63], a[172:175]\n" \
"s_and_b32 s97, s97, 0xffff\n" \
"v_writelane_b32 v11, s96, 7\n" \
"v_mfma_f32_16x16x32_bf16 a[172:175], v[176:179], v[64:67], a[172:175]\n" \
"v_writelane_b32 v11, s97, 8\n" \
"s_and_b32 s99, s99, 0xffff\n" \
"v_mfma_f32_16x16x32_bf16 a[192:195], v[152:155], v[72:75], a[192:195]\n" \
"v_writelane_b32 v11, s98, 9\n" \
"v_writelane_b32 v11, s99, 10\n" \
"v_mfma_f32_16x16x32_bf16 a[192:195], v[148:151], v[68:71], a[192:195]\n" \
"s_cmp_lt_u32 %[w], 3\n" \
"s_cselect_b64 s[96:97], s[96:97], s[98:99]\n" \
"s_waitcnt vmcnt(3)\n" \
"s_barrier\n" \
"v_mfma_f32_16x16x32_bf16 a[196:199], v[156:159], v[68:71], a[196:199]\n" \
"s_add_u32 m0, s40, 0x0\n" \
"s_add_u32 s85, s72, 0x0\n" \
"v_mfma_f32_16x16x32_bf16 a[196:199], v[160:163], v[72:75], a[196:199]\n" \
"buffer_load_dwordx4 v20, s[80:83], s85 offen sc1 lds\n" \
"v_mfma_f32_16x16x32_bf16 a[200:203], v[168:171], v[72:75], a[200:203]\n" \
"s_add_u32 m0, s40, 0x1000\n" \
"s_add_u32 s84, s72, 0x1000\n" \
"v_mfma_f32_16x16x32_bf16 a[200:203], v[164:167], v[68:71], a[200:203]\n" \
"ds_read_b64_tr_b16 v[140:141], v39 offset:32768\n" \
"ds_read_b64_tr_b16 v[142:143], v39 offset:33728\n" \
"v_mfma_f32_16x16x32_bf16 a[204:207], v[172:175], v[68:71], a[204:207]\n" \
"buffer_load_dwordx4 v20, s[80:83], s84 offen sc1 lds\n" \
"v_mfma_f32_16x16x32_bf16 a[204:207], v[176:179], v[72:75], a[204:207]\n" \
"ds_read_b64_tr_b16 v[128:129], v37 offset:36864\n" \
"ds_read_b64_tr_b16 v[130:131], v37 offset:37376\n" \
"v_mfma_f32_16x16x32_bf16 a[224:227], v[152:155], v[80:83], a[224:227]\n" \
"s_add_u32 m0, s40, 0x3000\n" \
"s_add_u32 s85, s62, 0x0\n" \
"v_mfma_f32_16x16x32_bf16 a[224:227], v[148:151], v[76:79], a[224:227]\n" \
"buffer_load_dwordx4 v21, s[76:79], s85 offen lds\n" \
"v_mfma_f32_16x16x32_bf16 a[228:231], v[156:159], v[76:79], a[228:231]\n" \
"ds_read_b64_tr_b16 v[124:125], v37 offset:32768\n" \
"ds_read_b64_tr_b16 v[126:127], v37 offset:33280\n" \
"v_mfma_f32_16x16x32_bf16 a[228:231], v[160:163], v[80:83], a[228:231]\n" \
"s_add_u32 m0, %[mA], 0x0\n" \
"s_add_u32 s84, s62, 0x0\n" \
"s_nop 0\n" \
"v_mfma_f32_16x16x32_bf16 a[232:235], v[168:171], v[80:83], a[232:235]\n" \
"buffer_load_dwordx4 v24, s[76:79], s84 offen lds\n" \
"v_mfma_f32_16x16x32_bf16 a[232:235], v[164:167], v[76:79], a[232:235]\n" \
"ds_read_b64_tr_b16 v[132:133], v38 offset:32768\n" \
"ds_read_b64_tr_b16 v[134:135], v38 offset:33280\n" \
"v_mfma_f32_16x16x32_bf16 a[236:239], v[172:175], v[76:79], a[236:239]\n" \
"ds_read_b64_tr_b16 v[136:137], v38 offset:36864\n" \
"ds_read_b64_tr_b16 v[138:139], v38 offset:37376\n" \
"v_mfma_f32_16x16x32_bf16 a[236:239], v[176:179], v[80:83], a[236:239]\n" \
"s_add_u32 m0, %[mA], 0x400\n" \
"s_add_u32 s85, s62, 0x2400\n" \
"v_mfma_f32_16x16x32_bf16 a[144:147], v[152:155], v[88:91], a[144:147]\n" \
"buffer_load_dwordx4 v25, s[76:79], s85 offen lds\n" \
"v_mfma_f32_16x16x32_bf16 a[144:147], v[148:151], v[84:87], a[144:147]\n" \
"ds_read_b128 v[52:55], v28 offset:32768\n" \
"ds_read_b128 v[56:59], v29 offset:32768\n" \
"v_mfma_f32_16x16x32_bf16 a[148:151], v[156:159], v[84:87], a[148:151]\n" \
"s_add_u32 m0, %[mB], 0x0\n" \
"s_add_u32 s84, s63, 0x0\n" \
"v_mfma_f32_16x16x32_bf16 a[148:151], v[160:163], v[88:91], a[148:151]\n" \
"buffer_load_dwordx4 v26, s[68:71], s84 offen lds\n" \
"v_mfma_f32_16x16x32_bf16 a[152:155], v[168:171], v[88:91], a[152:155]\n" \
"ds_read_b64_tr_b16 v[116:117], v36 offset:32768\n" \
"ds_read_b64_tr_b16 v[118:119], v36 offset:33280\n" \
"v_mfma_f32_16x16x32_bf16 a[152:155], v[164:167], v[84:87], a[152:155]\n" \
"ds_read_b64_tr_b16 v[120:121], v36 offset:36864\n" \
"ds_read_b64_tr_b16 v[122:123], v36 offset:37376\n" \
"v_mfma_f32_16x16x32_bf16 a[156:159], v[172:175], v[84:87], a[156:159]\n" \
"s_add_u32 m0, %[mA], 0x800\n" \
"s_add_u32 s85, s62, 0x4800\n" \
"v_mfma_f32_16x16x32_bf16 a[156:159], v[176:179], v[88:91], a[156:159]\n" \
"buffer_load_dwordx4 v24, s[76:79], s85 offen lds\n" \
"v_mfma_f32_16x16x32_bf16 a[176:179], v[152:155], v[96:99], a[176:179]\n" \
"ds_read_b128 v[84:87], v22 offset:32768\n" \
"ds_read_b128 v[88:91], v23 offset:32768\n" \
"L_pf7:\n" \
"s_cbranch_execz L_pf11\n" \
"s_nop 0\n" \
"v_mfma_f32_16x16x32_bf16 a[176:179], v[148:151], v[92:95], a[176:179]\n" \
"s_add_u32 m0, %[mA], 0xc00\n" \
"s_add_u32 s84, s62, 0x6c00\n" \
"v_mfma_f32_16x16x32_bf16 a[180:183], v[156:159], v[92:95], a[180:183]\n" \
"buffer_load_dwordx4 v25, s[76:79], s84 offen lds\n" \
"v_mfma_f32_16x16x32_bf16 a[180:183], v[160:163], v[96:99], a[180:183]\n" \
"ds_read_b128 v[60:63], v28 offset:34816\n" \
"ds_read_b128 v[64:67], v29 offset:34816\n" \
"v_mfma_f32_16x16x32_bf16 a[184:187], v[168:171], v[96:99], a[184:187]\n" \
"ds_read_b128 v[68:71], v28 offset:36864\n" \
"ds_read_b128 v[72:75], v29 offset:36864\n" \
"v_mfma_f32_16x16x32_bf16 a[184:187], v[164:167], v[92:95], a[184:187]\n" \
"s_add_u32 m0, %[mB], 0x400\n" \
"s_add_u32 s85, s63, 0x8000\n" \
"v_mfma_f32_16x16x32_bf16 a[188:191], v[172:175], v[92:95], a[188:191]\n" \
"buffer_load_dwordx4 v26, s[68:71], s85 offen lds\n" \
"v_mfma_f32_16x16x32_bf16 a[188:191], v[176:179], v[96:99], a[188:191]\n" \
"ds_read_b128 v[76:79], v28 offset:38912\n" \
"ds_read_b128 v[80:83], v29 offset:38912\n" \
"v_mfma_f32_16x16x32_bf16 a[208:211], v[152:155], v[104:107], a[208:211]\n" \
"s_add_u32 m0, %[mA], 0x1000\n" \
"s_add_u32 s84, s62, 0x9000\n" \
"v_mfma_f32_16x16x32_bf16 a[208:211], v[148:151], v[100:103], a[208:211]\n" \
"buffer_load_dwordx4 v24, s[76:79], s84 offen lds\n" \
"v_mfma_f32_16x16x32_bf16 a[212:215], v[156:159], v[100:103], a[212:215]\n" \
"ds_read_b128 v[92:95], v48 offset:34816\n" \
"ds_read_b128 v[96:99], v49 offset:34816\n" \
"v_mfma_f32_16x16x32_bf16 a[212:215], v[160:163], v[104:107], a[212:215]\n" \
"v_writelane_b32 v11, s96, 13\n" \
"v_writelane_b32 v11, s97, 14\n" \
"v_mfma_f32_16x16x32_bf16 a[216:219], v[168:171], v[104:107], a[216:219]\n" \
"s_add_u32 m0, %[mA], 0x1400\n" \
"s_add_u32 s85, s62, 0xb400\n" \
"v_mfma_f32_16x16x32_bf16 a[216:219], v[164:167], v[100:103], a[216:219]\n" \
"buffer_load_dwordx4 v25, s[76:79], s85 offen lds\n" \
"v_mfma_f32_16x16x32_bf16 a[220:223], v[172:175], v[100:103], a[220:223]\n" \
"ds_read_b128 v[100:103], v48 offset:36864\n" \
"v_writelane_b32 v11, s20, 11\n" \
"v_mfma_f32_16x16x32_bf16 a[220:223], v[176:179], v[104:107], a[220:223]\n" \
"s_add_u32 m0, %[mB], 0x800\n" \
"s_add_u32 s84, s63, 0x10000\n" \
"v_mfma_f32_16x16x32_bf16 a[240:243], v[152:155], v[112:115], a[240:243]\n" \
"buffer_load_dwordx4 v27, s[68:71], s84 offen lds\n" \
"v_mfma_f32_16x16x32_bf16 a[240:243], v[148:151], v[108:111], a[240:243]\n" \
"ds_read_b64_tr_b16 v[152:153], v32 offset:40960\n" \
"ds_read_b64_tr_b16 v[154:155], v32 offset:41984\n" \
"v_mfma_f32_16x16x32_bf16 a[244:247], v[156:159], v[108:111], a[244:247]\n" \
"ds_read_b64_tr_b16 v[148:149], v32 offset:32768\n" \
"ds_read_b64_tr_b16 v[150:151], v32 offset:33792\n" \
"v_mfma_f32_16x16x32_bf16 a[244:247], v[160:163], v[112:115], a[244:247]\n" \
"s_add_u32 m0, %[mA], 0x1800\n" \
"s_add_u32 s85, s62, 0xd800\n" \
"v_mfma_f32_16x16x32_bf16 a[248:251], v[168:171], v[112:115], a[248:251]\n" \
"buffer_load_dwordx4 v24, s[76:79], s85 offen lds\n" \
"v_mfma_f32_16x16x32_bf16 a[248:251], v[164:167], v[108:111], a[248:251]\n" \
"ds_read_b64_tr_b16 v[156:157], v33 offset:32768\n" \
"ds_read_b64_tr_b16 v[158:159], v33 offset:33792\n" \
"v_mfma_f32_16x16x32_bf16 a[252:255], v[172:175], v[108:111], a[252:255]\n" \
"ds_read_b64_tr_b16 v[160:161], v33 offset:40960\n" \
"ds_read_b64_tr_b16 v[162:163], v33 offset:41984\n" \
"v_mfma_f32_16x16x32_bf16 a[252:255], v[176:179], v[112:115], a[252:255]\n" \
"s_add_u32 m0, %[mA], 0x1c00\n" \
"s_waitcnt lgkmcnt(11)\n" \
"s_nop 0\n" \
"v_smfmac_f32_16x16x64_bf16 a[16:19], v[140:143], v[84:91], v50\n" \
"v_mfma_f32_16x16x32_bf16 a[48:51], v[128:131], v[88:91], a[48:51]\n" \
"s_add_u32 s85, s62, 0xfc00\n" \
"ds_read_b64_tr_b16 v[168:169], v34 offset:40960\n" \
"v_mfma_f32_16x16x32_bf16 a[48:51], v[124:127], v[84:87], a[48:51]\n" \
"buffer_load_dwordx4 v25, s[76:79], s85 offen lds\n" \
"v_mfma_f32_16x16x32_bf16 a[80:83], v[84:87], v[132:135], a[80:83]\n" \
"s_add_u32 m0, %[mB], 0xc00\n" \
"s_add_u32 s84, s63, 0x18000\n" \
"v_mfma_f32_16x16x32_bf16 a[80:83], v[88:91], v[136:139], a[80:83]\n" \
"ds_read_b64_tr_b16 v[170:171], v34 offset:41984\n" \
"ds_read_b64_tr_b16 v[164:165], v34 offset:32768\n" \
"v_smfmac_f32_16x16x64_bf16 a[112:115], v[140:143], v[52:59], v50\n" \
"v_mfma_f32_16x16x32_bf16 a[20:23], v[128:131], v[56:59], a[20:23]\n" \
"buffer_load_dwordx4 v27, s[68:71], s84 offen lds\n" \
"v_mfma_f32_16x16x32_bf16 a[20:23], v[124:127], v[52:55], a[20:23]\n" \
"s_add_u32 s88, s43, 0x0\n" \
"s_nop 0\n" \
"ds_read_b64_tr_b16 v[166:167], v34 offset:33792\n" \
"v_mfma_f32_16x16x32_bf16 a[52:55], v[116:119], v[52:55], a[52:55]\n" \
"ds_read_b64_tr_b16 v[172:173], v35 offset:32768\n" \
"ds_read_b64_tr_b16 v[174:175], v35 offset:33792\n" \
"v_mfma_f32_16x16x32_bf16 a[52:55], v[120:123], v[56:59], a[52:55]\n" \
"buffer_store_dwordx4 v[248:251], v20, s[80:83], s88 offen offset:0 sc1\n" \
"v_mfma_f32_16x16x32_bf16 a[84:87], v[56:59], v[136:139], a[84:87]\n" \
"ds_read_b64_tr_b16 v[176:177], v35 offset:40960\n" \
"ds_read_b64_tr_b16 v[178:179], v35 offset:41984\n" \
"v_mfma_f32_16x16x32_bf16 a[84:87], v[52:55], v[132:135], a[84:87]\n" \
"ds_read_b128 v[84:87], v48 offset:32768\n" \
"ds_read_b128 v[88:91], v49 offset:32768\n" \
"v_smfmac_f32_16x16x64_bf16 a[116:119], v[140:143], v[60:67], v50\n" \
"v_mfma_f32_16x16x32_bf16 a[24:27], v[124:127], v[60:63], a[24:27]\n" \
"ds_read_b128 v[104:107], v49 offset:36864\n" \
"ds_read_b128 v[108:111], v48 offset:38912\n" \
"v_mfma_f32_16x16x32_bf16 a[24:27], v[128:131], v[64:67], a[24:27]\n" \
"s_add_u32 s89, s45, 0x1000\n" \
"ds_read_b128 v[112:115], v49 offset:38912\n" \
"v_mfma_f32_16x16x32_bf16 a[56:59], v[120:123], v[64:67], a[56:59]\n" \
"buffer_store_dwordx4 v[184:187], v20, s[80:83], s89 offen offset:2048 sc1\n" \
"v_mfma_f32_16x16x32_bf16 a[56:59], v[116:119], v[60:63], a[56:59]\n" \
"s_add_u32 s62, s62, 0x80\n" \
"s_and_b32 s95, s21, 0xffff\n" \
"v_mfma_f32_16x16x32_bf16 a[88:91], v[60:63], v[132:135], a[88:91]\n" \
"s_cmp_lt_u32 s62, 0x480\n" \
"s_cselect_b32 s62, s62, 0\n" \
"v_mfma_f32_16x16x32_bf16 a[88:91], v[64:67], v[136:139], a[88:91]\n" \
"s_lshl_b32 s72, s62, 6\n" \
"s_add_u32 s72, s72, s37\n" \
"v_smfmac_f32_16x16x64_bf16 a[120:123], v[140:143], v[68:75], v50\n" \
"v_mfma_f32_16x16x32_bf16 a[28:31], v[128:131], v[72:75], a[28:31]\n" \
"s_lshl_b32 s63, s62, 12\n" \
"v_writelane_b32 v11, s95, 12\n" \
"v_mfma_f32_16x16x32_bf16 a[28:31], v[124:127], v[68:71], a[28:31]\n" \
"s_and_b32 s95, 1, %[w]\n" \
"s_lshr_b32 s96, %[w], 1\n" \
"v_mfma_f32_16x16x32_bf16 a[60:63], v[116:119], v[68:71], a[60:63]\n" \
"s_add_u32 s88, s45, 0x1000\n" \
"s_lshl_b32 s94, %[w], 11\n" \
"v_mfma_f32_16x16x32_bf16 a[60:63], v[120:123], v[72:75], a[60:63]\n" \
"buffer_store_dwordx4 v[252:255], v20, s[80:83], s88 offen offset:3072 sc1\n" \
"v_mfma_f32_16x16x32_bf16 a[92:95], v[72:75], v[136:139], a[92:95]\n" \
"s_lshl_b32 s15, s95, 12\n" \
"s_lshl_b32 s16, s96, 14\n" \
"v_mfma_f32_16x16x32_bf16 a[92:95], v[68:71], v[132:135], a[92:95]\n" \
"s_add_u32 s12, 0xc000, s94\n" \
"s_add_u32 s97, s16, s15\n" \
"v_smfmac_f32_16x16x64_bf16 a[124:127], v[140:143], v[76:83], v50\n" \
"v_mfma_f32_16x16x32_bf16 a[0:3], v[124:127], v[76:79], a[0:3]\n" \
"s_and_b32 s65, 7, %[b]\n" \
"s_lshr_b32 s67, %[b], 3\n" \
"v_mfma_f32_16x16x32_bf16 a[0:3], v[128:131], v[80:83], a[0:3]\n" \
"s_xor_b32 s13, 0x8000, s97\n" \
"s_add_u32 s20, 0x14000, s97\n" \
"v_mfma_f32_16x16x32_bf16 a[4:7], v[120:123], v[80:83], a[4:7]\n" \
"s_mul_i32 s98, 0xe000, s96\n" \
"s_lshr_b32 s100, %[b], 5\n" \
"v_mfma_f32_16x16x32_bf16 a[4:7], v[116:119], v[76:79], a[4:7]\n" \
"s_lshl_b32 s66, s65, 2\n" \
"s_and_b32 s94, 3, s67\n" \
"v_mfma_f32_16x16x32_bf16 a[32:35], v[76:79], v[132:135], a[32:35]\n" \
"s_add_u32 s99, s98, s15\n" \
"s_lshl_b32 s18, s100, 1\n" \
"v_mfma_f32_16x16x32_bf16 a[32:35], v[80:83], v[136:139], a[32:35]\n" \
"s_add_u32 s97, s66, s94\n" \
"s_waitcnt lgkmcnt(0)\n" \
"v_mfma_f32_16x16x32_bf16 a[128:131], v[152:155], v[56:59], a[128:131]\n" \
"s_add_u32 s21, 0x10000, s99\n" \
"s_add_u32 s101, s18, s96\n" \
"v_mfma_f32_16x16x32_bf16 a[128:131], v[148:151], v[52:55], a[128:131]\n" \
"s_lshl_b32 s98, s97, 8\n" \
"s_lshl_b32 s14, s101, 15\n" \
"v_mfma_f32_16x16x32_bf16 a[132:135], v[156:159], v[52:55], a[132:135]\n" \
"s_mov_b32 s64, 0x4000\n" \
"s_add_u32 s99, 0x5be8020, s98\n" \
"v_mfma_f32_16x16x32_bf16 a[132:135], v[160:163], v[56:59], a[132:135]\n" \
"s_lshl_b32 s96, s100, 2\n" \
"s_add_u32 s101, s99, s96\n" \
"v_mfma_f32_16x16x32_bf16 a[136:139], v[168:171], v[56:59], a[136:139]\n" \
"s_cmp_lg_u32 s100, 0\n" \
"s_cselect_b32 s17, s64, 0x8000\n" \
"v_mfma_f32_16x16x32_bf16 a[136:139], v[164:167], v[52:55], a[136:139]\n" \
"s_cmp_eq_u32 %[w], 3\n" \
"s_cselect_b32 s22, s101, 0x40000000\n" \
"v_mfma_f32_16x16x32_bf16 a[140:143], v[172:175], v[52:55], a[140:143]\n" \
"s_and_b32 s65, 2, %[w]\n" \
"s_lshl_b32 s64, s100, 3\n" \
"v_mfma_f32_16x16x32_bf16 a[140:143], v[176:179], v[56:59], a[140:143]\n" \
"s_lshl_b32 s67, s65, 1\n" \
"s_add_u32 s66, s67, s95\n" \
"v_mfma_f32_16x16x32_bf16 a[160:163], v[152:155], v[64:67], a[160:163]\n" \
"s_sub_u32 s101, 0x78, s64\n" \
"s_add_u32 s65, s101, s66\n" \
"v_mfma_f32_16x16x32_bf16 a[160:163], v[148:151], v[60:63], a[160:163]\n" \
"s_add_u32 s94, s64, s66\n" \
"s_mul_i32 s67, 0x24800, s65\n" \
"v_mfma_f32_16x16x32_bf16 a[164:167], v[156:159], v[60:63], a[164:167]\n" \
"s_lshl_b32 s99, s97, 12\n" \
"s_mov_b32 s64, 0xaaaaaaaa\n" \
"v_mfma_f32_16x16x32_bf16 a[164:167], v[160:163], v[64:67], a[164:167]\n" \
"s_mov_b32 s65, 0xaaaaaaaa\n" \
"s_add_u32 s95, s67, s99\n" \
"v_mfma_f32_16x16x32_bf16 a[168:171], v[168:171], v[64:67], a[168:171]\n" \
"s_mov_b32 s66, 0xfff0ffff\n" \
"s_mov_b32 s67, 0xf000ff00\n" \
"v_mfma_f32_16x16x32_bf16 a[168:171], v[164:167], v[60:63], a[168:171]\n" \
"s_mul_i32 s98, 0x24800, s94\n" \
"s_add_u32 s96, s98, s99\n" \
"v_mfma_f32_16x16x32_bf16 a[172:175], v[172:175], v[60:63], a[172:175]\n" \
"s_lshl_b32 s98, s97, 20\n" \
"s_lshl_b32 s94, s100, 16\n" \
"v_mfma_f32_16x16x32_bf16 a[172:175], v[176:179], v[64:67], a[172:175]\n" \
"s_add_u32 s8, 0x1000000, s98\n" \
"s_sub_u32 s19, 14, s18\n" \
"v_mfma_f32_16x16x32_bf16 a[192:195], v[152:155], v[72:75], a[192:195]\n" \
"s_add_u32 s60, 0x3004800, s96\n" \
"s_add_u32 s61, 0x3004800, s95\n" \
"v_mfma_f32_16x16x32_bf16 a[192:195], v[148:151], v[68:71], a[192:195]\n" \
"s_sub_u32 s10, 0x100000, s94\n" \
"s_mov_b32 s11, 0x20000\n" \
"s_waitcnt vmcnt(3)\n" \
"s_barrier\n" \
"s_mov_b64 exec, 1\n" \
"buffer_store_dword v45, off, s[80:83], s38 offset:0 sc1\n" \
"s_mov_b64 exec, -1\n" \
"v_mfma_f32_16x16x32_bf16 a[196:199], v[156:159], v[68:71], a[196:199]\n" \
"s_add_u32 m0, s40, 0x8000\n" \
"s_add_u32 s85, s72, 0x0\n" \
"v_mfma_f32_16x16x32_bf16 a[196:199], v[160:163], v[72:75], a[196:199]\n" \
"ds_read_b64_tr_b16 v[140:141], v39 offset:0\n" \
"ds_read_b64_tr_b16 v[142:143], v39 offset:960\n" \
"v_mfma_f32_16x16x32_bf16 a[200:203], v[168:171], v[72:75], a[200:203]\n" \
"buffer_load_dwordx4 v20, s[80:83], s85 offen sc1 lds\n" \
"v_mfma_f32_16x16x32_bf16 a[200:203], v[164:167], v[68:71], a[200:203]\n" \
"s_add_u32 m0, s40, 0x9000\n" \
"s_add_u32 s84, s72, 0x1000\n" \
"v_mfma_f32_16x16x32_bf16 a[204:207], v[172:175], v[68:71], a[204:207]\n" \
"ds_read_b64_tr_b16 v[128:129], v37 offset:4096\n" \
"ds_read_b64_tr_b16 v[130:131], v37 offset:4608\n" \
"v_mfma_f32_16x16x32_bf16 a[204:207], v[176:179], v[72:75], a[204:207]\n" \
"buffer_load_dwordx4 v20, s[80:83], s84 offen sc1 lds\n" \
"v_mfma_f32_16x16x32_bf16 a[224:227], v[152:155], v[80:83], a[224:227]\n" \
"s_add_u32 m0, s40, 0xb000\n" \
"s_add_u32 s85, s62, 0x0\n" \
"v_mfma_f32_16x16x32_bf16 a[224:227], v[148:151], v[76:79], a[224:227]\n" \
"ds_read_b64_tr_b16 v[124:125], v37 offset:0\n" \
"ds_read_b64_tr_b16 v[126:127], v37 offset:512\n" \
"v_mfma_f32_16x16x32_bf16 a[228:231], v[156:159], v[76:79], a[228:231]\n" \
"buffer_load_dwordx4 v21, s[76:79], s85 offen lds\n" \
"v_mfma_f32_16x16x32_bf16 a[228:231], v[160:163], v[80:83], a[228:231]\n" \
"s_add_u32 m0, %[mA], 0x8000\n" \
"s_add_u32 s84, s62, 0x0\n" \
"v_mfma_f32_16x16x32_bf16 a[232:235], v[168:171], v[80:83], a[232:235]\n" \
"ds_read_b64_tr_b16 v[132:133], v38 offset:0\n" \
"ds_read_b64_tr_b16 v[134:135], v38 offset:512\n" \
"v_mfma_f32_16x16x32_bf16 a[232:235], v[164:167], v[76:79], a[232:235]\n" \
"buffer_load_dwordx4 v24, s[76:79], s84 offen lds\n" \
"v_mfma_f32_16x16x32_bf16 a[236:239], v[172:175], v[76:79], a[236:239]\n" \
"ds_read_b64_tr_b16 v[136:137], v38 offset:4096\n" \
"ds_read_b64_tr_b16 v[138:139], v38 offset:4608\n" \
"v_mfma_f32_16x16x32_bf16 a[236:239], v[176:179], v[80:83], a[236:239]\n" \
"s_add_u32 m0, %[mA], 0x8400\n" \
"s_add_u32 s85, s62, 0x2400\n" \
"v_mfma_f32_16x16x32_bf16 a[144:147], v[152:155], v[88:91], a[144:147]\n" \
"buffer_load_dwordx4 v25, s[76:79], s85 offen lds\n" \
"v_mfma_f32_16x16x32_bf16 a[144:147], v[148:151], v[84:87], a[144:147]\n" \
"ds_read_b128 v[52:55], v28 offset:0\n" \
"ds_read_b128 v[56:59], v29 offset:0\n" \
"v_mfma_f32_16x16x32_bf16 a[148:151], v[156:159], v[84:87], a[148:151]\n" \
"s_add_u32 m0, %[mB], 0x8000\n" \
"s_add_u32 s84, s63, 0x0\n" \
"s_nop 0\n" \
"v_mfma_f32_16x16x32_bf16 a[148:151], v[160:163], v[88:91], a[148:151]\n" \
"buffer_load_dwordx4 v26, s[68:71], s84 offen lds\n" \
"v_mfma_f32_16x16x32_bf16 a[152:155], v[168:171], v[88:91], a[152:155]\n" \
"ds_read_b64_tr_b16 v[116:117], v36 offset:0\n" \
"ds_read_b64_tr_b16 v[118:119], v36 offset:512\n" \
"v_mfma_f32_16x16x32_bf16 a[152:155], v[164:167], v[84:87], a[152:155]\n" \
"ds_read_b64_tr_b16 v[120:121], v36 offset:4096\n" \
"ds_read_b64_tr_b16 v[122:123], v36 offset:4608\n" \
"v_mfma_f32_16x16x32_bf16 a[156:159], v[172:175], v[84:87], a[156:159]\n" \
"s_add_u32 m0, %[mA], 0x8800\n" \
"s_add_u32 s85, s62, 0x4800\n" \
"v_mfma_f32_16x16x32_bf16 a[156:159], v[176:179], v[88:91], a[156:159]\n" \
"buffer_load_dwordx4 v24, s[76:79], s85 offen lds\n" \
"v_mfma_f32_16x16x32_bf16 a[176:179], v[152:155], v[96:99], a[176:179]\n" \
"ds_read_b128 v[84:87], v22 offset:0\n" \
"ds_read_b128 v[88:91], v23 offset:0\n" \
"v_mfma_f32_16x16x32_bf16 a[176:179], v[148:151], v[92:95], a[176:179]\n" \
"s_add_u32 m0, %[mA], 0x8c00\n" \
"s_add_u32 s84, s62, 0x6c00\n" \
"v_mfma_f32_16x16x32_bf16 a[180:183], v[156:159], v[92:95], a[180:183]\n" \
"buffer_load_dwordx4 v25, s[76:79], s84 offen lds\n" \
"v_mfma_f32_16x16x32_bf16 a[180:183], v[160:163], v[96:99], a[180:183]\n" \
"ds_read_b128 v[60:63], v28 offset:2048\n" \
"ds_read_b128 v[64:67], v29 offset:2048\n" \
"v_mfma_f32_16x16x32_bf16 a[184:187], v[168:171], v[96:99], a[184:187]\n" \
"ds_read_b128 v[68:71], v28 offset:4096\n" \
"ds_read_b128 v[72:75], v29 offset:4096\n" \
"v_mfma_f32_16x16x32_bf16 a[184:187], v[164:167], v[92:95], a[184:187]\n" \
"s_add_u32 m0, %[mB], 0x8400\n" \
"s_add_u32 s85, s63, 0x8000\n" \
"v_mfma_f32_16x16x32_bf16 a[188:191], v[172:175], v[92:95], a[188:191]\n" \
"buffer_load_dwordx4 v26, s[68:71], s85 offen lds\n" \
"v_mfma_f32_16x16x32_bf16 a[188:191], v[176:179], v[96:99], a[188:191]\n" \
"ds_read_b128 v[76:79], v28 offset:6144\n" \
"ds_read_b128 v[80:83], v29 offset:6144\n" \
"v_mfma_f32_16x16x32_bf16 a[208:211], v[152:155], v[104:107], a[208:211]\n" \
"s_add_u32 m0, %[mA], 0x9000\n" \
"s_add_u32 s84, s62, 0x9000\n" \
"v_mfma_f32_16x16x32_bf16 a[208:211], v[148:151], v[100:103], a[208:211]\n" \
"buffer_load_dwordx4 v24, s[76:79], s84 offen lds\n" \
"v_mfma_f32_16x16x32_bf16 a[212:215], v[156:159], v[100:103], a[212:215]\n" \
"ds_read_b128 v[92:95], v48 offset:2048\n" \
"ds_read_b128 v[96:99], v49 offset:2048\n" \
"v_mfma_f32_16x16x32_bf16 a[212:215], v[160:163], v[104:107], a[212:215]\n" \
"s_mov_b32 s58, 0x6000000\n" \
"s_mov_b32 s59, 0x20000\n" \
"v_mfma_f32_16x16x32_bf16 a[216:219], v[168:171], v[104:107], a[216:219]\n" \
"s_add_u32 m0, %[mA], 0x9400\n" \
"s_add_u32 s85, s62, 0xb400\n" \
"v_mfma_f32_16x16x32_bf16 a[216:219], v[164:167], v[100:103], a[216:219]\n" \
"buffer_load_dwordx4 v25, s[76:79], s85 offen lds\n" \
"v_mfma_f32_16x16x32_bf16 a[220:223], v[172:175], v[100:103], a[220:223]\n" \
"ds_read_b128 v[100:103], v48 offset:4096\n" \
"s_mov_b64 s[56:57], s[80:81]\n" \
"s_nop 0\n" \
"v_mfma_f32_16x16x32_bf16 a[220:223], v[176:179], v[104:107], a[220:223]\n" \
"s_add_u32 m0, %[mB], 0x8800\n" \
"s_add_u32 s84, s63, 0x10000\n" \
"v_mfma_f32_16x16x32_bf16 a[240:243], v[152:155], v[112:115], a[240:243]\n" \
"buffer_load_dwordx4 v27, s[68:71], s84 offen lds\n" \
"v_mfma_f32_16x16x32_bf16 a[240:243], v[148:151], v[108:111], a[240:243]\n" \
"ds_read_b64_tr_b16 v[152:153], v32 offset:8192\n" \
"ds_read_b64_tr_b16 v[154:155], v32 offset:9216\n" \
"v_mfma_f32_16x16x32_bf16 a[244:247], v[156:159], v[108:111], a[244:247]\n" \
"ds_read_b64_tr_b16 v[148:149], v32 offset:0\n" \
"ds_read_b64_tr_b16 v[150:151], v32 offset:1024\n" \
"v_mfma_f32_16x16x32_bf16 a[244:247], v[160:163], v[112:115], a[244:247]\n" \
"s_add_u32 m0, %[mA], 0x9800\n" \
"s_add_u32 s85, s62, 0xd800\n" \
"v_mfma_f32_16x16x32_bf16 a[248:251], v[168:171], v[112:115], a[248:251]\n" \
"buffer_load_dwordx4 v24, s[76:79], s85 offen lds\n" \
"v_mfma_f32_16x16x32_bf16 a[248:251], v[164:167], v[108:111], a[248:251]\n" \
"ds_read_b64_tr_b16 v[156:157], v33 offset:0\n" \
"ds_read_b64_tr_b16 v[158:159], v33 offset:1024\n" \
"v_mfma_f32_16x16x32_bf16 a[252:255], v[172:175], v[108:111], a[252:255]\n" \
"ds_read_b64_tr_b16 v[160:161], v33 offset:8192\n" \
"ds_read_b64_tr_b16 v[162:163], v33 offset:9216\n" \
"v_mfma_f32_16x16x32_bf16 a[252:255], v[176:179], v[112:115], a[252:255]\n" \
"s_add_u32 m0, %[mA], 0x9c00\n" \
"s_waitcnt lgkmcnt(11)\n" \
"s_nop 0\n" \
"v_smfmac_f32_16x16x64_bf16 a[16:19], v[140:143], v[84:91], v50\n" \
"v_mfma_f32_16x16x32_bf16 a[48:51], v[128:131], v[88:91], a[48:51]\n" \
"s_add_u32 s85, s62, 0xfc00\n" \
"ds_read_b64_tr_b16 v[168:169], v34 offset:8192\n" \
"v_mfma_f32_16x16x32_bf16 a[48:51], v[124:127], v[84:87], a[48:51]\n" \
"buffer_load_dwordx4 v25, s[76:79], s85 offen lds\n" \
"v_mfma_f32_16x16x32_bf16 a[80:83], v[84:87], v[132:135], a[80:83]\n" \
"s_add_u32 m0, %[mB], 0x8c00\n" \
"s_add_u32 s84, s63, 0x18000\n" \
"v_mfma_f32_16x16x32_bf16 a[80:83], v[88:91], v[136:139], a[80:83]\n" \
"ds_read_b64_tr_b16 v[170:171], v34 offset:9216\n" \
"ds_read_b64_tr_b16 v[164:165], v34 offset:0\n" \
"v_smfmac_f32_16x16x64_bf16 a[112:115], v[140:143], v[52:59], v50\n" \
"v_mfma_f32_16x16x32_bf16 a[20:23], v[128:131], v[56:59], a[20:23]\n" \
"buffer_load_dwordx4 v27, s[68:71], s84 offen lds\n" \
"v_mfma_f32_16x16x32_bf16 a[20:23], v[124:127], v[52:55], a[20:23]\n" \
"s_add_u32 s89, s43, 0x1000\n" \
"ds_read_b64_tr_b16 v[166:167], v34 offset:1024\n" \
"v_mfma_f32_16x16x32_bf16 a[52:55], v[116:119], v[52:55], a[52:55]\n" \
"ds_read_b64_tr_b16 v[172:173], v35 offset:0\n" \
"ds_read_b64_tr_b16 v[174:175], v35 offset:1024\n" \
"v_mfma_f32_16x16x32_bf16 a[52:55], v[120:123], v[56:59], a[52:55]\n" \
"buffer_store_dwordx4 a[36:39], v20, s[80:83], s89 offen offset:0 sc1\n" \
"v_mfma_f32_16x16x32_bf16 a[84:87], v[56:59], v[136:139], a[84:87]\n" \
"ds_read_b64_tr_b16 v[176:177], v35 offset:8192\n" \
"ds_read_b64_tr_b16 v[178:179], v35 offset:9216\n" \
"v_mfma_f32_16x16x32_bf16 a[84:87], v[52:55], v[132:135], a[84:87]\n" \
"ds_read_b128 v[84:87], v48 offset:0\n" \
"ds_read_b128 v[88:91], v49 offset:0\n" \
"v_smfmac_f32_16x16x64_bf16 a[116:119], v[140:143], v[60:67], v50\n" \
"v_mfma_f32_16x16x32_bf16 a[24:27], v[124:127], v[60:63], a[24:27]\n" \
"ds_read_b128 v[104:107], v49 offset:4096\n" \
"ds_read_b128 v[108:111], v48 offset:6144\n" \
"v_mfma_f32_16x16x32_bf16 a[24:27], v[128:131], v[64:67], a[24:27]\n" \
"s_add_u32 s88, s43, 0x2000\n" \
"ds_read_b128 v[112:115], v49 offset:6144\n" \
"v_mfma_f32_16x16x32_bf16 a[56:59], v[120:123], v[64:67], a[56:59]\n" \
"buffer_store_dwordx4 a[64:67], v20, s[80:83], s88 offen offset:0 sc1\n" \
"v_mfma_f32_16x16x32_bf16 a[56:59], v[116:119], v[60:63], a[56:59]\n" \
"s_add_u32 s8, s8, s80\n" \
"s_addc_u32 s9, s81, 0\n" \
"v_mfma_f32_16x16x32_bf16 a[88:91], v[60:63], v[132:135], a[88:91]\n" \
"s_add_u32 s62, s62, 0x80\n" \
"v_mfma_f32_16x16x32_bf16 a[88:91], v[64:67], v[136:139], a[88:91]\n" \
"s_cmp_lt_u32 s62, 0x480\n" \
"s_cselect_b32 s62, s62, 0\n" \
"v_smfmac_f32_16x16x64_bf16 a[120:123], v[140:143], v[68:75], v50\n" \
"v_mfma_f32_16x16x32_bf16 a[28:31], v[128:131], v[72:75], a[28:31]\n" \
"s_lshl_b32 s72, s62, 6\n" \
"s_add_u32 s72, s72, s37\n" \
"v_mfma_f32_16x16x32_bf16 a[28:31], v[124:127], v[68:71], a[28:31]\n" \
"s_lshl_b32 s63, s62, 12\n" \
"v_mfma_f32_16x16x32_bf16 a[60:63], v[116:119], v[68:71], a[60:63]\n" \
"s_add_u32 s89, s43, 0x3000\n" \
"v_mfma_f32_16x16x32_bf16 a[60:63], v[120:123], v[72:75], a[60:63]\n" \
"buffer_store_dwordx4 a[68:71], v20, s[80:83], s89 offen offset:0 sc1\n" \
"v_mfma_f32_16x16x32_bf16 a[92:95], v[72:75], v[136:139], a[92:95]\n" \
"v_mfma_f32_16x16x32_bf16 a[92:95], v[68:71], v[132:135], a[92:95]\n" \
"v_smfmac_f32_16x16x64_bf16 a[124:127], v[140:143], v[76:83], v50\n" \
"v_mfma_f32_16x16x32_bf16 a[0:3], v[124:127], v[76:79], a[0:3]\n" \
"v_mfma_f32_16x16x32_bf16 a[0:3], v[128:131], v[80:83], a[0:3]\n" \
"v_mfma_f32_16x16x32_bf16 a[4:7], v[120:123], v[80:83], a[4:7]\n" \
"v_mfma_f32_16x16x32_bf16 a[4:7], v[116:119], v[76:79], a[4:7]\n" \
"v_mfma_f32_16x16x32_bf16 a[32:35], v[76:79], v[132:135], a[32:35]\n" \
"v_mfma_f32_16x16x32_bf16 a[32:35], v[80:83], v[136:139], a[32:35]\n" \
"s_waitcnt lgkmcnt(0)\n" \
"s_nop 0\n" \
"v_mfma_f32_16x16x32_bf16 a[128:131], v[152:155], v[56:59], a[128:131]\n" \
"v_mfma_f32_16x16x32_bf16 a[128:131], v[148:151], v[52:55], a[128:131]\n" \
"v_mfma_f32_16x16x32_bf16 a[132:135], v[156:159], v[52:55], a[132:135]\n" \
"v_mfma_f32_16x16x32_bf16 a[132:135], v[160:163], v[56:59], a[132:135]\n" \
"v_mfma_f32_16x16x32_bf16 a[136:139], v[168:171], v[56:59], a[136:139]\n" \
"v_mfma_f32_16x16x32_bf16 a[136:139], v[164:167], v[52:55], a[136:139]\n" \
"v_mfma_f32_16x16x32_bf16 a[140:143], v[172:175], v[52:55], a[140:143]\n" \
"v_mfma_f32_16x16x32_bf16 a[140:143], v[176:179], v[56:59], a[140:143]\n" \
"v_mfma_f32_16x16x32_bf16 a[160:163], v[152:155], v[64:67], a[160:163]\n" \
"v_mfma_f32_16x16x32_bf16 a[160:163], v[148:151], v[60:63], a[160:163]\n" \
"v_mfma_f32_16x16x32_bf16 a[164:167], v[156:159], v[60:63], a[164:167]\n" \
"v_mfma_f32_16x16x32_bf16 a[164:167], v[160:163], v[64:67], a[164:167]\n" \
"v_mfma_f32_16x16x32_bf16 a[168:171], v[168:171], v[64:67], a[168:171]\n" \
"v_mfma_f32_16x16x32_bf16 a[168:171], v[164:167], v[60:63], a[168:171]\n" \
"v_mfma_f32_16x16x32_bf16 a[172:175], v[172:175], v[60:63], a[172:175]\n" \
"v_mfma_f32_16x16x32_bf16 a[172:175], v[176:179], v[64:67], a[172:175]\n" \
"v_mfma_f32_16x16x32_bf16 a[192:195], v[152:155], v[72:75], a[192:195]\n" \
"v_mfma_f32_16x16x32_bf16 a[192:195], v[148:151], v[68:71], a[192:195]\n" \
"s_waitcnt vmcnt(3)\n" \
"s_barrier\n" \
"v_mfma_f32_16x16x32_bf16 a[196:199], v[156:159], v[68:71], a[196:199]\n" \
"s_add_u32 m0, s40, 0x0\n" \
"s_add_u32 s85, s72, 0x0\n" \
"v_mfma_f32_16x16x32_bf16 a[196:199], v[160:163], v[72:75], a[196:199]\n" \
"buffer_load_dwordx4 v20, s[80:83], s85 offen sc1 lds\n" \
"v_mfma_f32_16x16x32_bf16 a[200:203], v[168:171], v[72:75], a[200:203]\n" \
"s_add_u32 m0, s40, 0x1000\n" \
"s_add_u32 s84, s72, 0x1000\n" \
"v_mfma_f32_16x16x32_bf16 a[200:203], v[164:167], v[68:71], a[200:203]\n" \
"ds_read_b64_tr_b16 v[140:141], v39 offset:32768\n" \
"ds_read_b64_tr_b16 v[142:143], v39 offset:33728\n" \
"v_mfma_f32_16x16x32_bf16 a[204:207], v[172:175], v[68:71], a[204:207]\n" \
"buffer_load_dwordx4 v20, s[80:83], s84 offen sc1 lds\n" \
"v_mfma_f32_16x16x32_bf16 a[204:207], v[176:179], v[72:75], a[204:207]\n" \
"ds_read_b64_tr_b16 v[128:129], v37 offset:36864\n" \
"ds_read_b64_tr_b16 v[130:131], v37 offset:37376\n" \
"v_mfma_f32_16x16x32_bf16 a[224:227], v[152:155], v[80:83], a[224:227]\n" \
"s_add_u32 m0, s40, 0x3000\n" \
"s_add_u32 s85, s62, 0x0\n" \
"v_mfma_f32_16x16x32_bf16 a[224:227], v[148:151], v[76:79], a[224:227]\n" \
"buffer_load_dwordx4 v21, s[76:79], s85 offen lds\n" \
"v_mfma_f32_16x16x32_bf16 a[228:231], v[156:159], v[76:79], a[228:231]\n" \
"ds_read_b64_tr_b16 v[124:125], v37 offset:32768\n" \
"ds_read_b64_tr_b16 v[126:127], v37 offset:33280\n" \
"v_mfma_f32_16x16x32_bf16 a[228:231], v[160:163], v[80:83], a[228:231]\n" \
"s_add_u32 m0, %[mA], 0x0\n" \
"s_add_u32 s84, s62, 0x0\n" \
"s_nop 0\n" \
"v_mfma_f32_16x16x32_bf16 a[232:235], v[168:171], v[80:83], a[232:235]\n" \
"buffer_load_dwordx4 v24, s[76:79], s84 offen lds\n" \
"v_mfma_f32_16x16x32_bf16 a[232:235], v[164:167], v[76:79], a[232:235]\n" \
"ds_read_b64_tr_b16 v[132:133], v38 offset:32768\n" \
"ds_read_b64_tr_b16 v[134:135], v38 offset:33280\n" \
"v_mfma_f32_16x16x32_bf16 a[236:239], v[172:175], v[76:79], a[236:239]\n" \
"ds_read_b64_tr_b16 v[136:137], v38 offset:36864\n" \
"ds_read_b64_tr_b16 v[138:139], v38 offset:37376\n" \
"v_mfma_f32_16x16x32_bf16 a[236:239], v[176:179], v[80:83], a[236:239]\n" \
"s_add_u32 m0, %[mA], 0x400\n" \
"s_add_u32 s85, s62, 0x2400\n" \
"v_mfma_f32_16x16x32_bf16 a[144:147], v[152:155], v[88:91], a[144:147]\n" \
"buffer_load_dwordx4 v25, s[76:79], s85 offen lds\n" \
"v_mfma_f32_16x16x32_bf16 a[144:147], v[148:151], v[84:87], a[144:147]\n" \
"ds_read_b128 v[52:55], v28 offset:32768\n" \
"ds_read_b128 v[56:59], v29 offset:32768\n" \
"v_mfma_f32_16x16x32_bf16 a[148:151], v[156:159], v[84:87], a[148:151]\n" \
"s_add_u32 m0, %[mB], 0x0\n" \
"s_add_u32 s84, s63, 0x0\n" \
"v_mfma_f32_16x16x32_bf16 a[148:151], v[160:163], v[88:91], a[148:151]\n" \
"buffer_load_dwordx4 v26, s[68:71], s84 offen lds\n" \
"v_mfma_f32_16x16x32_bf16 a[152:155], v[168:171], v[88:91], a[152:155]\n" \
"ds_read_b64_tr_b16 v[116:117], v36 offset:32768\n" \
"ds_read_b64_tr_b16 v[118:119], v36 offset:33280\n" \
"v_mfma_f32_16x16x32_bf16 a[152:155], v[164:167], v[84:87], a[152:155]\n" \
"ds_read_b64_tr_b16 v[120:121], v36 offset:36864\n" \
"ds_read_b64_tr_b16 v[122:123], v36 offset:37376\n" \
"v_mfma_f32_16x16x32_bf16 a[156:159], v[172:175], v[84:87], a[156:159]\n" \
"s_add_u32 m0, %[mA], 0x800\n" \
"s_add_u32 s85, s62, 0x4800\n" \
"v_mfma_f32_16x16x32_bf16 a[156:159], v[176:179], v[88:91], a[156:159]\n" \
"buffer_load_dwordx4 v24, s[76:79], s85 offen lds\n" \
"v_mfma_f32_16x16x32_bf16 a[176:179], v[152:155], v[96:99], a[176:179]\n" \
"ds_read_b128 v[84:87], v22 offset:32768\n" \
"ds_read_b128 v[88:91], v23 offset:32768\n" \
"v_mfma_f32_16x16x32_bf16 a[176:179], v[148:151], v[92:95], a[176:179]\n" \
"s_add_u32 m0, %[mA], 0xc00\n" \
"s_add_u32 s84, s62, 0x6c00\n" \
"v_mfma_f32_16x16x32_bf16 a[180:183], v[156:159], v[92:95], a[180:183]\n" \
"buffer_load_dwordx4 v25, s[76:79], s84 offen lds\n" \
"v_mfma_f32_16x16x32_bf16 a[180:183], v[160:163], v[96:99], a[180:183]\n" \
"ds_read_b128 v[60:63], v28 offset:34816\n" \
"ds_read_b128 v[64:67], v29 offset:34816\n" \
"v_mfma_f32_16x16x32_bf16 a[184:187], v[168:171], v[96:99], a[184:187]\n" \
"ds_read_b128 v[68:71], v28 offset:36864\n" \
"ds_read_b128 v[72:75], v29 offset:36864\n" \
"v_mfma_f32_16x16x32_bf16 a[184:187], v[164:167], v[92:95], a[184:187]\n" \
"s_add_u32 m0, %[mB], 0x400\n" \
"s_add_u32 s85, s63, 0x8000\n" \
"v_mfma_f32_16x16x32_bf16 a[188:191], v[172:175], v[92:95], a[188:191]\n" \
"buffer_load_dwordx4 v26, s[68:71], s85 offen lds\n" \
"v_mfma_f32_16x16x32_bf16 a[188:191], v[176:179], v[96:99], a[188:191]\n" \
"ds_read_b128 v[76:79], v28 offset:38912\n" \
"ds_read_b128 v[80:83], v29 offset:38912\n" \
"v_mfma_f32_16x16x32_bf16 a[208:211], v[152:155], v[104:107], a[208:211]\n" \
"s_add_u32 m0, %[mA], 0x1000\n" \
"s_add_u32 s84, s62, 0x9000\n" \
"v_mfma_f32_16x16x32_bf16 a[208:211], v[148:151], v[100:103], a[208:211]\n" \
"buffer_load_dwordx4 v24, s[76:79], s84 offen lds\n" \
"v_mfma_f32_16x16x32_bf16 a[212:215], v[156:159], v[100:103], a[212:215]\n" \
"ds_read_b128 v[92:95], v48 offset:34816\n" \
"ds_read_b128 v[96:99], v49 offset:34816\n" \
"v_mfma_f32_16x16x32_bf16 a[212:215], v[160:163], v[104:107], a[212:215]\n" \
"s_mov_b32 s72, 0x3eae1a93\n" \
"v_and_b32_e32 v26, 0x1ff, v20\n" \
"v_mfma_f32_16x16x32_bf16 a[216:219], v[168:171], v[104:107], a[216:219]\n" \
"s_add_u32 m0, %[mA], 0x1400\n" \
"s_add_u32 s85, s62, 0xb400\n" \
"v_mfma_f32_16x16x32_bf16 a[216:219], v[164:167], v[100:103], a[216:219]\n" \
"buffer_load_dwordx4 v25, s[76:79], s85 offen lds\n" \
"v_mfma_f32_16x16x32_bf16 a[220:223], v[172:175], v[100:103], a[220:223]\n" \
"ds_read_b128 v[100:103], v48 offset:36864\n" \
"v_add_u32_e64 v21, s48, v20\n" \
"v_mfma_f32_16x16x32_bf16 a[220:223], v[176:179], v[104:107], a[220:223]\n" \
"s_add_u32 m0, %[mB], 0x800\n" \
"s_add_u32 s84, s63, 0x10000\n" \
"v_mfma_f32_16x16x32_bf16 a[240:243], v[152:155], v[112:115], a[240:243]\n" \
"buffer_load_dwordx4 v27, s[68:71], s84 offen lds\n" \
"v_mfma_f32_16x16x32_bf16 a[240:243], v[148:151], v[108:111], a[240:243]\n" \
"ds_read_b64_tr_b16 v[152:153], v32 offset:40960\n" \
"ds_read_b64_tr_b16 v[154:155], v32 offset:41984\n" \
"v_mfma_f32_16x16x32_bf16 a[244:247], v[156:159], v[108:111], a[244:247]\n" \
"ds_read_b64_tr_b16 v[148:149], v32 offset:32768\n" \
"ds_read_b64_tr_b16 v[150:151], v32 offset:33792\n" \
"v_mfma_f32_16x16x32_bf16 a[244:247], v[160:163], v[112:115], a[244:247]\n" \
"s_add_u32 m0, %[mA], 0x1800\n" \
"s_add_u32 s85, s62, 0xd800\n" \
"v_mfma_f32_16x16x32_bf16 a[248:251], v[168:171], v[112:115], a[248:251]\n" \
"buffer_load_dwordx4 v24, s[76:79], s85 offen lds\n" \
"v_mfma_f32_16x16x32_bf16 a[248:251], v[164:167], v[108:111], a[248:251]\n" \
"ds_read_b64_tr_b16 v[156:157], v33 offset:32768\n" \
"ds_read_b64_tr_b16 v[158:159], v33 offset:33792\n" \
"v_mfma_f32_16x16x32_bf16 a[252:255], v[172:175], v[108:111], a[252:255]\n" \
"ds_read_b64_tr_b16 v[160:161], v33 offset:40960\n" \
"ds_read_b64_tr_b16 v[162:163], v33 offset:41984\n" \
"v_mfma_f32_16x16x32_bf16 a[252:255], v[176:179], v[112:115], a[252:255]\n" \
"s_add_u32 m0, %[mA], 0x1c00\n" \
"L_pf8:\n" \
"s_cbranch_execz L_pf12\n" \
"s_nop 0\n" \
"s_waitcnt lgkmcnt(11)\n" \
"v_smfmac_f32_16x16x64_bf16 a[16:19], v[140:143], v[84:91], v50\n" \
"v_mfma_f32_16x16x32_bf16 a[48:51], v[128:131], v[88:91], a[48:51]\n" \
"s_add_u32 s85, s62, 0xfc00\n" \
"ds_read_b64_tr_b16 v[168:169], v34 offset:40960\n" \
"v_mfma_f32_16x16x32_bf16 a[48:51], v[124:127], v[84:87], a[48:51]\n" \
"buffer_load_dwordx4 v25, s[76:79], s85 offen lds\n" \
"s_mov_b32 s76, s54\n" \
"v_mfma_f32_16x16x32_bf16 a[80:83], v[84:87], v[132:135], a[80:83]\n" \
"s_add_u32 m0, %[mB], 0xc00\n" \
"s_add_u32 s84, s63, 0x18000\n" \
"v_mfma_f32_16x16x32_bf16 a[80:83], v[88:91], v[136:139], a[80:83]\n" \
"ds_read_b64_tr_b16 v[170:171], v34 offset:41984\n" \
"ds_read_b64_tr_b16 v[164:165], v34 offset:32768\n" \
"v_smfmac_f32_16x16x64_bf16 a[112:115], v[140:143], v[52:59], v50\n" \
"v_mfma_f32_16x16x32_bf16 a[20:23], v[128:131], v[56:59], a[20:23]\n" \
"buffer_load_dwordx4 v27, s[68:71], s84 offen lds\n" \
"v_mfma_f32_16x16x32_bf16 a[20:23], v[124:127], v[52:55], a[20:23]\n" \
"s_add_u32 s88, s43, 0x0\n" \
"s_nop 0\n" \
"ds_read_b64_tr_b16 v[166:167], v34 offset:33792\n" \
"v_mfma_f32_16x16x32_bf16 a[52:55], v[116:119], v[52:55], a[52:55]\n" \
"ds_read_b64_tr_b16 v[172:173], v35 offset:32768\n" \
"ds_read_b64_tr_b16 v[174:175], v35 offset:33792\n" \
"v_mfma_f32_16x16x32_bf16 a[52:55], v[120:123], v[56:59], a[52:55]\n" \
"buffer_store_dwordx4 a[96:99], v20, s[80:83], s88 offen offset:1024 sc1\n" \
"v_mfma_f32_16x16x32_bf16 a[84:87], v[56:59], v[136:139], a[84:87]\n" \
"ds_read_b64_tr_b16 v[176:177], v35 offset:40960\n" \
"ds_read_b64_tr_b16 v[178:179], v35 offset:41984\n" \
"v_mfma_f32_16x16x32_bf16 a[84:87], v[52:55], v[132:135], a[84:87]\n" \
"ds_read_b128 v[84:87], v48 offset:32768\n" \
"ds_read_b128 v[88:91], v49 offset:32768\n" \
"v_smfmac_f32_16x16x64_bf16 a[116:119], v[140:143], v[60:67], v50\n" \
"v_mfma_f32_16x16x32_bf16 a[24:27], v[124:127], v[60:63], a[24:27]\n" \
"ds_read_b128 v[104:107], v49 offset:36864\n" \
"ds_read_b128 v[108:111], v48 offset:38912\n" \
"v_mfma_f32_16x16x32_bf16 a[24:27], v[128:131], v[64:67], a[24:27]\n" \
"s_add_u32 s89, s43, 0x1000\n" \
"ds_read_b128 v[112:115], v49 offset:38912\n" \
"v_mfma_f32_16x16x32_bf16 a[56:59], v[120:123], v[64:67], a[56:59]\n" \
"buffer_store_dwordx4 a[100:103], v20, s[80:83], s89 offen offset:1024 sc1\n" \
"s_add_u32 s89, s43, 0x3000\n" \
"v_mfma_f32_16x16x32_bf16 a[56:59], v[116:119], v[60:63], a[56:59]\n" \
"v_accvgpr_read_b32 v248, a16\n" \
"v_accvgpr_read_b32 v249, a17\n" \
"v_mfma_f32_16x16x32_bf16 a[88:91], v[60:63], v[132:135], a[88:91]\n" \
"v_accvgpr_read_b32 v250, a18\n" \
"v_accvgpr_read_b32 v251, a19\n" \
"v_mfma_f32_16x16x32_bf16 a[88:91], v[64:67], v[136:139], a[88:91]\n" \
"v_accvgpr_read_b32 v180, a48\n" \
"v_accvgpr_read_b32 v181, a49\n" \
"v_smfmac_f32_16x16x64_bf16 a[120:123], v[140:143], v[68:75], v50\n" \
"v_mfma_f32_16x16x32_bf16 a[28:31], v[128:131], v[72:75], a[28:31]\n" \
"v_accvgpr_read_b32 v182, a50\n" \
"v_accvgpr_read_b32 v183, a51\n" \
"v_mfma_f32_16x16x32_bf16 a[28:31], v[124:127], v[68:71], a[28:31]\n" \
"v_accvgpr_read_b32 v252, a80\n" \
"v_accvgpr_read_b32 v253, a81\n" \
"v_mfma_f32_16x16x32_bf16 a[60:63], v[116:119], v[68:71], a[60:63]\n" \
"s_add_u32 s88, s43, 0x2000\n" \
"v_accvgpr_read_b32 v254, a82\n" \
"v_mfma_f32_16x16x32_bf16 a[60:63], v[120:123], v[72:75], a[60:63]\n" \
"buffer_store_dwordx4 a[8:11], v20, s[80:83], s88 offen offset:1024 sc1\n" \
"s_add_u32 s88, s45, 0x0\n" \
"s_nop 0\n" \
"v_mfma_f32_16x16x32_bf16 a[92:95], v[72:75], v[136:139], a[92:95]\n" \
"v_accvgpr_read_b32 v255, a83\n" \
"v_accvgpr_read_b32 v196, a112\n" \
"v_mfma_f32_16x16x32_bf16 a[92:95], v[68:71], v[132:135], a[92:95]\n" \
"v_accvgpr_read_b32 v197, a113\n" \
"v_accvgpr_read_b32 v198, a114\n" \
"v_smfmac_f32_16x16x64_bf16 a[124:127], v[140:143], v[76:83], v50\n" \
"v_mfma_f32_16x16x32_bf16 a[0:3], v[124:127], v[76:79], a[0:3]\n" \
"v_accvgpr_read_b32 v199, a115\n" \
"v_accvgpr_read_b32 v188, a20\n" \
"v_mfma_f32_16x16x32_bf16 a[0:3], v[128:131], v[80:83], a[0:3]\n" \
"v_accvgpr_read_b32 v189, a21\n" \
"v_accvgpr_read_b32 v190, a22\n" \
"v_mfma_f32_16x16x32_bf16 a[4:7], v[120:123], v[80:83], a[4:7]\n" \
"v_accvgpr_read_b32 v191, a23\n" \
"v_accvgpr_read_b32 v184, a52\n" \
"v_mfma_f32_16x16x32_bf16 a[4:7], v[116:119], v[76:79], a[4:7]\n" \
"v_lshlrev_b32_e32 v45, 10, v20\n" \
"v_add_u32_e32 v45, s41, v45\n" \
"v_mfma_f32_16x16x32_bf16 a[32:35], v[76:79], v[132:135], a[32:35]\n" \
"s_mov_b32 m0, %[mS]\n" \
"v_accvgpr_read_b32 v185, a53\n" \
"v_mfma_f32_16x16x32_bf16 a[32:35], v[80:83], v[136:139], a[32:35]\n" \
"v_accvgpr_read_b32 v186, a54\n" \
"s_waitcnt lgkmcnt(0)\n" \
"v_mfma_f32_16x16x32_bf16 a[128:131], v[152:155], v[56:59], a[128:131]\n" \
"v_accvgpr_read_b32 v187, a55\n" \
"v_accvgpr_read_b32 v192, a84\n" \
"v_mfma_f32_16x16x32_bf16 a[128:131], v[148:151], v[52:55], a[128:131]\n" \
"v_accvgpr_read_b32 v193, a85\n" \
"v_accvgpr_read_b32 v194, a86\n" \
"v_mfma_f32_16x16x32_bf16 a[132:135], v[156:159], v[52:55], a[132:135]\n" \
"v_accvgpr_read_b32 v195, a87\n" \
"v_accvgpr_read_b32 v212, a116\n" \
"s_getpc_b64 s[86:87]\n" \
"L_ct15_%=:\n" \
"s_add_u32 s86, s86, L_end_%=-L_ct15_%=\n" \
"s_addc_u32 s87, s87, 0\n" \
"v_mfma_f32_16x16x32_bf16 a[132:135], v[160:163], v[56:59], a[132:135]\n" \
"v_accvgpr_read_b32 v213, a117\n" \
"v_accvgpr_read_b32 v214, a118\n" \
"v_mfma_f32_16x16x32_bf16 a[136:139], v[168:171], v[56:59], a[136:139]\n" \
"v_accvgpr_read_b32 v215, a119\n" \
"v_accvgpr_read_b32 v204, a24\n" \
"v_mfma_f32_16x16x32_bf16 a[136:139], v[164:167], v[52:55], a[136:139]\n" \
"v_accvgpr_read_b32 v205, a25\n" \
"v_accvgpr_read_b32 v206, a26\n" \
"v_mfma_f32_16x16x32_bf16 a[140:143], v[172:175], v[52:55], a[140:143]\n" \
"v_accvgpr_read_b32 v207, a27\n" \
"v_accvgpr_read_b32 v200, a56\n" \
"v_mfma_f32_16x16x32_bf16 a[140:143], v[176:179], v[56:59], a[140:143]\n" \
"v_accvgpr_read_b32 v201, a57\n" \
"v_accvgpr_read_b32 v202, a58\n" \
"v_mfma_f32_16x16x32_bf16 a[160:163], v[152:155], v[64:67], a[160:163]\n" \
"v_accvgpr_read_b32 v203, a59\n" \
"v_accvgpr_read_b32 v208, a88\n" \
"v_mfma_f32_16x16x32_bf16 a[160:163], v[148:151], v[60:63], a[160:163]\n" \
"v_accvgpr_read_b32 v209, a89\n" \
"v_accvgpr_read_b32 v210, a90\n" \
"v_mfma_f32_16x16x32_bf16 a[164:167], v[156:159], v[60:63], a[164:167]\n" \
"v_accvgpr_read_b32 v211, a91\n" \
"v_accvgpr_read_b32 v228, a120\n" \
"v_mfma_f32_16x16x32_bf16 a[164:167], v[160:163], v[64:67], a[164:167]\n" \
"v_accvgpr_read_b32 v229, a121\n" \
"v_accvgpr_read_b32 v230, a122\n" \
"v_mfma_f32_16x16x32_bf16 a[168:171], v[168:171], v[64:67], a[168:171]\n" \
"v_accvgpr_read_b32 v231, a123\n" \
"v_accvgpr_read_b32 v220, a28\n" \
"v_mfma_f32_16x16x32_bf16 a[168:171], v[164:167], v[60:63], a[168:171]\n" \
"v_accvgpr_read_b32 v221, a29\n" \
"v_accvgpr_read_b32 v222, a30\n" \
"v_mfma_f32_16x16x32_bf16 a[172:175], v[172:175], v[60:63], a[172:175]\n" \
"v_accvgpr_read_b32 v223, a31\n" \
"v_accvgpr_read_b32 v216, a60\n" \
"v_mfma_f32_16x16x32_bf16 a[172:175], v[176:179], v[64:67], a[172:175]\n" \
"v_accvgpr_read_b32 v217, a61\n" \
"v_accvgpr_read_b32 v218, a62\n" \
"v_mfma_f32_16x16x32_bf16 a[192:195], v[152:155], v[72:75], a[192:195]\n" \
"v_accvgpr_read_b32 v219, a63\n" \
"v_accvgpr_read_b32 v224, a92\n" \
"v_mfma_f32_16x16x32_bf16 a[192:195], v[148:151], v[68:71], a[192:195]\n" \
"v_accvgpr_read_b32 v225, a93\n" \
"v_accvgpr_read_b32 v226, a94\n" \
"s_waitcnt vmcnt(3)\n" \
"s_barrier\n" \
"s_mov_b64 exec, 3\n" \
"global_load_lds_dword v45, s[86:87]\n" \
"s_mov_b64 exec, -1\n" \
"s_sub_u32 m0, %[mB], 0x10000\n" \
"v_mfma_f32_16x16x32_bf16 a[196:199], v[156:159], v[68:71], a[196:199]\n" \
"ds_read_b64_tr_b16 v[140:141], v39 offset:0\n" \
"ds_read_b64_tr_b16 v[142:143], v39 offset:960\n" \
"v_mfma_f32_16x16x32_bf16 a[196:199], v[160:163], v[72:75], a[196:199]\n" \
"ds_read_b64_tr_b16 v[128:129], v37 offset:4096\n" \
"ds_read_b64_tr_b16 v[130:131], v37 offset:4608\n" \
"v_mfma_f32_16x16x32_bf16 a[200:203], v[168:171], v[72:75], a[200:203]\n" \
"ds_read_b64_tr_b16 v[124:125], v37 offset:0\n" \
"ds_read_b64_tr_b16 v[126:127], v37 offset:512\n" \
"v_mfma_f32_16x16x32_bf16 a[200:203], v[164:167], v[68:71], a[200:203]\n" \
"ds_read_b64_tr_b16 v[132:133], v38 offset:0\n" \
"ds_read_b64_tr_b16 v[134:135], v38 offset:512\n" \
"v_mfma_f32_16x16x32_bf16 a[204:207], v[172:175], v[68:71], a[204:207]\n" \
"ds_read_b64_tr_b16 v[136:137], v38 offset:4096\n" \
"ds_read_b64_tr_b16 v[138:139], v38 offset:4608\n" \
"v_mfma_f32_16x16x32_bf16 a[204:207], v[176:179], v[72:75], a[204:207]\n" \
"ds_read_b128 v[52:55], v28 offset:0\n" \
"ds_read_b128 v[56:59], v29 offset:0\n" \
"v_mfma_f32_16x16x32_bf16 a[224:227], v[152:155], v[80:83], a[224:227]\n" \
"ds_read_b64_tr_b16 v[116:117], v36 offset:0\n" \
"ds_read_b64_tr_b16 v[118:119], v36 offset:512\n" \
"v_mfma_f32_16x16x32_bf16 a[224:227], v[148:151], v[76:79], a[224:227]\n" \
"ds_read_b64_tr_b16 v[120:121], v36 offset:4096\n" \
"ds_read_b64_tr_b16 v[122:123], v36 offset:4608\n" \
"v_mfma_f32_16x16x32_bf16 a[228:231], v[156:159], v[76:79], a[228:231]\n" \
"ds_read_b128 v[60:63], v28 offset:2048\n" \
"ds_read_b128 v[64:67], v29 offset:2048\n" \
"v_mfma_f32_16x16x32_bf16 a[228:231], v[160:163], v[80:83], a[228:231]\n" \
"v_accvgpr_read_b32 v227, a95\n" \
"v_accvgpr_read_b32 v244, a124\n" \
"v_mfma_f32_16x16x32_bf16 a[232:235], v[168:171], v[80:83], a[232:235]\n" \
"ds_read_b128 v[68:71], v28 offset:4096\n" \
"ds_read_b128 v[72:75], v29 offset:4096\n" \
"v_mfma_f32_16x16x32_bf16 a[232:235], v[164:167], v[76:79], a[232:235]\n" \
"v_accvgpr_read_b32 v245, a125\n" \
"v_accvgpr_read_b32 v246, a126\n" \
"v_mfma_f32_16x16x32_bf16 a[236:239], v[172:175], v[76:79], a[236:239]\n" \
"ds_read_b128 v[76:79], v28 offset:6144\n" \
"v_accvgpr_read_b32 v247, a127\n" \
"v_mfma_f32_16x16x32_bf16 a[236:239], v[176:179], v[80:83], a[236:239]\n" \
"ds_read_b128 v[80:83], v29 offset:6144\n" \
"v_accvgpr_read_b32 v236, a0\n" \
"v_mfma_f32_16x16x32_bf16 a[144:147], v[152:155], v[88:91], a[144:147]\n" \
"v_accvgpr_read_b32 v237, a1\n" \
"v_accvgpr_read_b32 v238, a2\n" \
"v_mfma_f32_16x16x32_bf16 a[144:147], v[148:151], v[84:87], a[144:147]\n" \
"v_accvgpr_read_b32 v239, a3\n" \
"v_accvgpr_read_b32 v232, a4\n" \
"v_mfma_f32_16x16x32_bf16 a[148:151], v[156:159], v[84:87], a[148:151]\n" \
"v_accvgpr_read_b32 v233, a5\n" \
"v_accvgpr_read_b32 v234, a6\n" \
"v_mfma_f32_16x16x32_bf16 a[148:151], v[160:163], v[88:91], a[148:151]\n" \
"v_accvgpr_read_b32 v235, a7\n" \
"v_accvgpr_read_b32 v240, a32\n" \
"v_mfma_f32_16x16x32_bf16 a[152:155], v[168:171], v[88:91], a[152:155]\n" \
"v_accvgpr_read_b32 v241, a33\n" \
"v_accvgpr_read_b32 v242, a34\n" \
"v_mfma_f32_16x16x32_bf16 a[152:155], v[164:167], v[84:87], a[152:155]\n" \
"v_accvgpr_read_b32 v243, a35\n" \
"s_mov_b32 s78, -1\n" \
"v_mfma_f32_16x16x32_bf16 a[156:159], v[172:175], v[84:87], a[156:159]\n" \
"ds_read_b128 v[84:87], v22 offset:0\n" \
"s_mov_b32 s79, 0\n" \
"v_mfma_f32_16x16x32_bf16 a[156:159], v[176:179], v[88:91], a[156:159]\n" \
"ds_read_b128 v[88:91], v23 offset:0\n" \
"s_mov_b32 s68, 0xffc0fffc\n" \
"v_mfma_f32_16x16x32_bf16 a[176:179], v[152:155], v[96:99], a[176:179]\n" \
"s_mov_b32 s69, 0xc000fc00\n" \
"v_lshrrev_b32_e32 v25, 8, v20\n" \
"v_mfma_f32_16x16x32_bf16 a[176:179], v[148:151], v[92:95], a[176:179]\n" \
"v_lshrrev_b32_e32 v24, 9, v20\n" \
"v_mul_u32_u24_e32 v25, 0x24700, v25\n" \
"v_mfma_f32_16x16x32_bf16 a[180:183], v[156:159], v[92:95], a[180:183]\n" \
"v_lshl_add_u32 v24, v24, 3, v26\n" \
"v_add_u32_e64 v25, v25, v20\n" \
"v_mfma_f32_16x16x32_bf16 a[180:183], v[160:163], v[96:99], a[180:183]\n" \
"s_add_u32 s84, s50, 0x0\n" \
"s_add_u32 s85, s50, 0x24800\n" \
"v_mfma_f32_16x16x32_bf16 a[184:187], v[168:171], v[96:99], a[184:187]\n" \
"s_add_u32 s86, s50, 0x49000\n" \
"s_add_u32 s87, s50, 0x6d800\n" \
"v_mfma_f32_16x16x32_bf16 a[184:187], v[164:167], v[92:95], a[184:187]\n" \
"s_mov_b32 s70, 0x3e0293ee\n" \
"s_mov_b32 s71, 0x3e0293ee\n" \
"v_mfma_f32_16x16x32_bf16 a[188:191], v[172:175], v[92:95], a[188:191]\n" \
"ds_read_b128 v[92:95], v48 offset:2048\n" \
"v_add_u32_e32 v27, s47, v20\n" \
"v_mfma_f32_16x16x32_bf16 a[188:191], v[176:179], v[96:99], a[188:191]\n" \
"ds_read_b128 v[96:99], v49 offset:2048\n" \
"v_lshrrev_b32_e32 v26, 2, v20\n" \
"v_mfma_f32_16x16x32_bf16 a[208:211], v[152:155], v[104:107], a[208:211]\n" \
"v_add_u32_e32 v26, s46, v26\n" \
"v_mfma_f32_16x16x32_bf16 a[208:211], v[148:151], v[100:103], a[208:211]\n" \
"v_mfma_f32_16x16x32_bf16 a[212:215], v[156:159], v[100:103], a[212:215]\n" \
"v_mfma_f32_16x16x32_bf16 a[212:215], v[160:163], v[104:107], a[212:215]\n" \
"v_mfma_f32_16x16x32_bf16 a[216:219], v[168:171], v[104:107], a[216:219]\n" \
"v_mfma_f32_16x16x32_bf16 a[216:219], v[164:167], v[100:103], a[216:219]\n" \
"v_mfma_f32_16x16x32_bf16 a[220:223], v[172:175], v[100:103], a[220:223]\n" \
"ds_read_b128 v[100:103], v48 offset:4096\n" \
"v_mfma_f32_16x16x32_bf16 a[220:223], v[176:179], v[104:107], a[220:223]\n" \
"ds_read_b128 v[104:107], v49 offset:4096\n" \
"v_mfma_f32_16x16x32_bf16 a[240:243], v[152:155], v[112:115], a[240:243]\n" \
"ds_read_b64_tr_b16 v[152:153], v32 offset:8192\n" \
"ds_read_b64_tr_b16 v[154:155], v32 offset:9216\n" \
"v_mfma_f32_16x16x32_bf16 a[240:243], v[148:151], v[108:111], a[240:243]\n" \
"ds_read_b64_tr_b16 v[148:149], v32 offset:0\n" \
"ds_read_b64_tr_b16 v[150:151], v32 offset:1024\n" \
"v_mfma_f32_16x16x32_bf16 a[244:247], v[156:159], v[108:111], a[244:247]\n" \
"ds_read_b64_tr_b16 v[156:157], v33 offset:0\n" \
"ds_read_b64_tr_b16 v[158:159], v33 offset:1024\n" \
"v_mfma_f32_16x16x32_bf16 a[244:247], v[160:163], v[112:115], a[244:247]\n" \
"ds_read_b64_tr_b16 v[160:161], v33 offset:8192\n" \
"ds_read_b64_tr_b16 v[162:163], v33 offset:9216\n" \
"v_mfma_f32_16x16x32_bf16 a[248:251], v[168:171], v[112:115], a[248:251]\n" \
"ds_read_b64_tr_b16 v[168:169], v34 offset:8192\n" \
"ds_read_b64_tr_b16 v[170:171], v34 offset:9216\n" \
"v_mfma_f32_16x16x32_bf16 a[248:251], v[164:167], v[108:111], a[248:251]\n" \
"ds_read_b64_tr_b16 v[164:165], v34 offset:0\n" \
"ds_read_b64_tr_b16 v[166:167], v34 offset:1024\n" \
"v_mfma_f32_16x16x32_bf16 a[252:255], v[172:175], v[108:111], a[252:255]\n" \
"ds_read_b64_tr_b16 v[172:173], v35 offset:0\n" \
"ds_read_b64_tr_b16 v[174:175], v35 offset:1024\n" \
"v_mfma_f32_16x16x32_bf16 a[252:255], v[176:179], v[112:115], a[252:255]\n" \
"ds_read_b64_tr_b16 v[176:177], v35 offset:8192\n" \
"s_waitcnt lgkmcnt(15)\n" \
"s_nop 0\n" \
"v_smfmac_f32_16x16x64_bf16 v[248:251], v[140:143], v[84:91], v50\n" \
"v_mfma_f32_16x16x32_bf16 v[180:183], v[128:131], v[88:91], v[180:183]\n" \
"ds_read_b64_tr_b16 v[178:179], v35 offset:9216\n" \
"v_bfe_u32 v44, v20, 2, 5\n" \
"v_mfma_f32_16x16x32_bf16 v[180:183], v[124:127], v[84:87], v[180:183]\n" \
"buffer_load_dword v44, v44, s[80:83], s39 offen offset:0 sc1\n" \
"v_mfma_f32_16x16x32_bf16 v[252:255], v[84:87], v[132:135], v[252:255]\n" \
"ds_read_b128 v[84:87], v48 offset:0\n" \
"ds_read_b128 v[108:111], v48 offset:6144\n" \
"v_mfma_f32_16x16x32_bf16 v[252:255], v[88:91], v[136:139], v[252:255]\n" \
"ds_read_b128 v[88:91], v49 offset:0\n" \
"ds_read_b128 v[112:115], v49 offset:6144\n" \
"v_smfmac_f32_16x16x64_bf16 v[196:199], v[140:143], v[52:59], v50\n" \
"v_mfma_f32_16x16x32_bf16 v[188:191], v[128:131], v[56:59], v[188:191]\n" \
"v_cvt_pk_bf16_f32 v180, v180, v181\n" \
"v_cvt_pk_bf16_f32 v181, v182, v183\n" \
"v_mfma_f32_16x16x32_bf16 v[188:191], v[124:127], v[52:55], v[188:191]\n" \
"v_add_f32_e64 v249, v249, v251\n" \
"v_cvt_pk_bf16_f32 v182, v249, 0\n" \
"v_mfma_f32_16x16x32_bf16 v[184:187], v[116:119], v[52:55], v[184:187]\n" \
"v_mov_b32_e32 v183, 0x3f80\n" \
"v_add_f32_e64 v196, v196, v198\n" \
"v_mfma_f32_16x16x32_bf16 v[184:187], v[120:123], v[56:59], v[184:187]\n" \
"v_mfma_f32_16x16x16_bf16 v[248:251], v[182:183], v[46:47], 0\n" \
"v_add_f32_e64 v197, v197, v199\n" \
"v_cvt_pk_bf16_f32 v188, v188, v189\n" \
"v_mfma_f32_16x16x32_bf16 v[192:195], v[56:59], v[136:139], v[192:195]\n" \
"v_cvt_pk_bf16_f32 v189, v190, v191\n" \
"v_cvt_pk_bf16_f32 v190, v197, 0\n" \
"v_mfma_f32_16x16x32_bf16 v[192:195], v[52:55], v[132:135], v[192:195]\n" \
"v_mov_b32_e32 v191, 0x3f80\n" \
"v_cvt_pk_bf16_f32 v184, v184, v185\n" \
"v_smfmac_f32_16x16x64_bf16 v[212:215], v[140:143], v[60:67], v50\n" \
"v_mfma_f32_16x16x32_bf16 v[204:207], v[124:127], v[60:63], v[204:207]\n" \
"v_cvt_pk_bf16_f32 v185, v186, v187\n" \
"v_cndmask_b32_e64 v186, 0, v196, s[78:79]\n" \
"v_mfma_f32_16x16x32_bf16 v[204:207], v[128:131], v[64:67], v[204:207]\n" \
"v_mov_b32_e64 v187, 0\n" \
"v_cvt_pk_bf16_f32 v186, v186, 0\n" \
"v_mfma_f32_16x16x32_bf16 v[200:203], v[120:123], v[64:67], v[200:203]\n" \
"v_mfma_f32_16x16x32_bf16 v[200:203], v[116:119], v[60:63], v[200:203]\n" \
"v_add_f32_e32 v212, v212, v214\n" \
"v_add_f32_e32 v213, v213, v215\n" \
"v_mfma_f32_16x16x32_bf16 v[208:211], v[60:63], v[132:135], v[208:211]\n" \
"v_mfma_f32_16x16x32_bf16 v[208:211], v[64:67], v[136:139], v[208:211]\n" \
"v_cvt_pk_bf16_f32 v204, v204, v205\n" \
"v_cvt_pk_bf16_f32 v205, v206, v207\n" \
"v_smfmac_f32_16x16x64_bf16 v[228:231], v[140:143], v[68:75], v50\n" \
"v_mfma_f32_16x16x32_bf16 v[220:223], v[128:131], v[72:75], v[220:223]\n" \
"v_cvt_pk_bf16_f32 v200, v200, v201\n" \
"v_cvt_pk_bf16_f32 v201, v202, v203\n" \
"v_mfma_f32_16x16x32_bf16 v[220:223], v[124:127], v[68:71], v[220:223]\n" \
"v_cndmask_b32_e64 v202, 0, v212, s[78:79]\n" \
"v_cvt_pk_bf16_f32 v206, v213, 0\n" \
"v_mfma_f32_16x16x32_bf16 v[216:219], v[116:119], v[68:71], v[216:219]\n" \
"v_mov_b32_e32 v207, 0x3f80\n" \
"v_add_f32_e64 v228, v228, v230\n" \
"v_mfma_f32_16x16x32_bf16 v[216:219], v[120:123], v[72:75], v[216:219]\n" \
"v_add_f32_e32 v229, v229, v231\n" \
"v_mov_b32_e32 v203, 0\n" \
"v_mfma_f32_16x16x32_bf16 v[224:227], v[72:75], v[136:139], v[224:227]\n" \
"v_cvt_pk_bf16_f32 v220, v220, v221\n" \
"v_cvt_pk_bf16_f32 v221, v222, v223\n" \
"v_mfma_f32_16x16x32_bf16 v[224:227], v[68:71], v[132:135], v[224:227]\n" \
"v_cvt_pk_bf16_f32 v202, v202, 0\n" \
"v_cvt_pk_bf16_f32 v222, v229, 0\n" \
"v_smfmac_f32_16x16x64_bf16 v[244:247], v[140:143], v[76:83], v50\n" \
"v_mfma_f32_16x16x32_bf16 v[236:239], v[124:127], v[76:79], v[236:239]\n" \
"v_cvt_pk_bf16_f32 v216, v216, v217\n" \
"v_cvt_pk_bf16_f32 v217, v218, v219\n" \
"v_mfma_f32_16x16x32_bf16 v[236:239], v[128:131], v[80:83], v[236:239]\n" \
"v_cndmask_b32_e64 v218, 0, v228, s[78:79]\n" \
"v_cvt_pk_bf16_f32 v126, v192, v193\n" \
"v_mfma_f32_16x16x32_bf16 v[232:235], v[120:123], v[80:83], v[232:235]\n" \
"v_cvt_pk_bf16_f32 v127, v194, v195\n" \
"v_add_f32_e64 v244, v244, v246\n" \
"v_mfma_f32_16x16x32_bf16 v[232:235], v[116:119], v[76:79], v[232:235]\n" \
"v_add_f32_e64 v245, v245, v247\n" \
"v_cvt_pk_bf16_f32 v128, v208, v209\n" \
"v_mfma_f32_16x16x32_bf16 v[240:243], v[76:79], v[132:135], v[240:243]\n" \
"v_cvt_pk_bf16_f32 v236, v236, v237\n" \
"v_cvt_pk_bf16_f32 v237, v238, v239\n" \
"v_mfma_f32_16x16x32_bf16 v[240:243], v[80:83], v[136:139], v[240:243]\n" \
"v_cvt_pk_bf16_f32 v129, v210, v211\n" \
"v_mov_b32_e32 v223, 0x3f80\n" \
"s_waitcnt lgkmcnt(0)\n" \
"s_barrier\n" \
"v_mfma_f32_16x16x32_bf16 a[128:131], v[152:155], v[56:59], a[128:131]\n" \
"v_cvt_pk_bf16_f32 v232, v232, v233\n" \
"v_cvt_pk_bf16_f32 v233, v234, v235\n" \
"v_mfma_f32_16x16x32_bf16 a[128:131], v[148:151], v[52:55], a[128:131]\n" \
"v_cndmask_b32_e64 v234, 0, v244, s[78:79]\n" \
"v_mov_b32_e64 v219, 0\n" \
"v_mfma_f32_16x16x32_bf16 a[132:135], v[156:159], v[52:55], a[132:135]\n" \
"v_cvt_pk_bf16_f32 v218, v218, 0\n" \
"v_cvt_pk_bf16_f32 v130, v224, v225\n" \
"v_mfma_f32_16x16x32_bf16 a[132:135], v[160:163], v[56:59], a[132:135]\n" \
"v_cvt_pk_bf16_f32 v131, v226, v227\n" \
"v_cvt_pk_bf16_f32 v238, v245, 0\n" \
"v_mfma_f32_16x16x32_bf16 a[136:139], v[168:171], v[56:59], a[136:139]\n" \
"v_mov_b32_e32 v239, 0x3f80\n" \
"v_mov_b32_e64 v235, 0\n" \
"v_mfma_f32_16x16x32_bf16 a[136:139], v[164:167], v[52:55], a[136:139]\n" \
"v_cvt_pk_bf16_f32 v234, v234, 0\n" \
"v_cvt_pk_bf16_f32 v132, v240, v241\n" \
"v_mfma_f32_16x16x32_bf16 a[140:143], v[172:175], v[52:55], a[140:143]\n" \
"v_cvt_pk_bf16_f32 v133, v242, v243\n" \
"v_cvt_pk_bf16_f32 v134, v248, v249\n" \
"v_mfma_f32_16x16x32_bf16 a[140:143], v[176:179], v[56:59], a[140:143]\n" \
"v_mfma_f32_16x16x32_bf16 v[116:119], v[180:183], v[184:187], 0\n" \
"v_mfma_f32_16x16x32_bf16 v[192:195], v[188:191], v[184:187], 0\n" \
"v_mfma_f32_16x16x32_bf16 v[208:211], v[204:207], v[200:203], 0\n" \
"v_mfma_f32_16x16x32_bf16 v[120:123], v[188:191], v[200:203], 0\n" \
"s_waitcnt vmcnt(0)\n" \
"v_cmp_ne_u32_e32 vcc, %[tag], v44\n" \
"v_mfma_f32_16x16x32_bf16 a[160:163], v[152:155], v[64:67], a[160:163]\n" \
"v_mfma_f32_16x16x32_bf16 v[224:227], v[220:223], v[216:219], 0\n" \
"v_mfma_f32_16x16x32_bf16 v[36:39], v[204:207], v[216:219], 0\n" \
"v_mfma_f32_16x16x32_bf16 v[240:243], v[236:239], v[232:235], 0\n" \
"v_mfma_f32_16x16x32_bf16 v[48:51], v[220:223], v[232:235], 0\n" \
"s_cbranch_vccnz L_pa_%=\n" \
"L_ra_%=:\n" \
"v_cndmask_b32_e64 v116, v116, v117, s[64:65]\n" \
"buffer_load_dwordx4 v20, s[80:83], s73 offen offset:0 sc1 lds\n" \
"v_mfma_f32_16x16x32_bf16 a[160:163], v[148:151], v[60:63], a[160:163]\n" \
"v_cndmask_b32_e64 v118, v118, v119, s[64:65]\n" \
"v_cndmask_b32_e64 v192, v192, v193, s[64:65]\n" \
"v_mfma_f32_16x16x32_bf16 a[164:167], v[156:159], v[60:63], a[164:167]\n" \
"v_add_f32_e32 v116, s49, v116\n" \
"v_cndmask_b32_e64 v194, v194, v195, s[64:65]\n" \
"v_mfma_f32_16x16x32_bf16 a[164:167], v[160:163], v[64:67], a[164:167]\n" \
"v_add_f32_e64 v118, s49, v118\n" \
"v_cndmask_b32_e64 v208, v208, v209, s[64:65]\n" \
"v_mfma_f32_16x16x32_bf16 a[168:171], v[168:171], v[64:67], a[168:171]\n" \
"buffer_load_dwordx4 v20, s[80:83], s73 offen offset:1024 sc1 lds\n" \
"v_mfma_f32_16x16x32_bf16 a[168:171], v[164:167], v[60:63], a[168:171]\n" \
"v_cndmask_b32_e64 v120, v120, v121, s[64:65]\n" \
"v_cndmask_b32_e64 v210, v210, v211, s[64:65]\n" \
"v_mfma_f32_16x16x32_bf16 a[172:175], v[172:175], v[60:63], a[172:175]\n" \
"v_cndmask_b32_e64 v122, v122, v123, s[64:65]\n" \
"v_cndmask_b32_e64 v224, v224, v225, s[64:65]\n" \
"v_mfma_f32_16x16x32_bf16 a[172:175], v[176:179], v[64:67], a[172:175]\n" \
"v_cndmask_b32_e64 v36, v36, v37, s[64:65]\n" \
"v_cndmask_b32_e64 v226, v226, v227, s[64:65]\n" \
"v_mfma_f32_16x16x32_bf16 a[192:195], v[152:155], v[72:75], a[192:195]\n" \
"buffer_load_dwordx4 v20, s[80:83], s73 offen offset:2048 sc1 lds\n" \
"v_mfma_f32_16x16x32_bf16 a[192:195], v[148:151], v[68:71], a[192:195]\n" \
"v_cndmask_b32_e64 v38, v38, v39, s[64:65]\n" \
"v_cndmask_b32_e64 v240, v240, v241, s[64:65]\n" \
"v_mfma_f32_16x16x32_bf16 a[196:199], v[156:159], v[68:71], a[196:199]\n" \
"v_cndmask_b32_e64 v48, v48, v49, s[64:65]\n" \
"v_cndmask_b32_e64 v242, v242, v243, s[64:65]\n" \
"v_mfma_f32_16x16x32_bf16 a[196:199], v[160:163], v[72:75], a[196:199]\n" \
"v_cndmask_b32_e64 v50, v50, v51, s[64:65]\n" \
"v_cndmask_b32_e64 v192, v116, v192, s[66:67]\n" \
"v_mfma_f32_16x16x32_bf16 a[200:203], v[168:171], v[72:75], a[200:203]\n" \
"buffer_load_dwordx4 v20, s[80:83], s73 offen offset:3072 sc1 lds\n" \
"v_mfma_f32_16x16x32_bf16 a[200:203], v[164:167], v[68:71], a[200:203]\n" \
"s_sub_u32 m0, %[mB], s53\n" \
"s_nop 0\n" \
"v_cndmask_b32_e64 v194, v118, v194, s[68:69]\n" \
"v_mfma_f32_16x16x32_bf16 a[204:207], v[172:175], v[68:71], a[204:207]\n" \
"v_cndmask_b32_e64 v208, v120, v208, s[66:67]\n" \
"v_cndmask_b32_e64 v210, v122, v210, s[68:69]\n" \
"v_mfma_f32_16x16x32_bf16 a[204:207], v[176:179], v[72:75], a[204:207]\n" \
"v_cndmask_b32_e64 v224, v36, v224, s[66:67]\n" \
"v_cndmask_b32_e64 v226, v38, v226, s[68:69]\n" \
"v_mfma_f32_16x16x32_bf16 a[224:227], v[152:155], v[80:83], a[224:227]\n" \
"buffer_load_dwordx4 v20, s[80:83], s74 offen offset:0 sc1 lds\n" \
"v_mfma_f32_16x16x32_bf16 a[224:227], v[148:151], v[76:79], a[224:227]\n" \
"v_cndmask_b32_e64 v240, v48, v240, s[66:67]\n" \
"v_cndmask_b32_e64 v242, v50, v242, s[68:69]\n" \
"v_mfma_f32_16x16x32_bf16 a[228:231], v[156:159], v[76:79], a[228:231]\n" \
"v_max_f32_e32 v193, v192, v194\n" \
"v_max_f32_e32 v209, v208, v210\n" \
"v_mfma_f32_16x16x32_bf16 a[228:231], v[160:163], v[80:83], a[228:231]\n" \
"v_max_f32_e32 v225, v224, v226\n" \
"v_max_f32_e32 v241, v240, v242\n" \
"v_mfma_f32_16x16x32_bf16 a[232:235], v[168:171], v[80:83], a[232:235]\n" \
"buffer_load_dwordx4 v20, s[80:83], s74 offen offset:1024 sc1 lds\n" \
"v_mfma_f32_16x16x32_bf16 a[232:235], v[164:167], v[76:79], a[232:235]\n" \
"v_permlane32_swap_b32_e32 v193, v225\n" \
"v_permlane32_swap_b32_e32 v209, v241\n" \
"v_mfma_f32_16x16x32_bf16 a[236:239], v[172:175], v[76:79], a[236:239]\n" \
"v_max_f32_e32 v193, v193, v225\n" \
"v_max_f32_e32 v209, v209, v241\n" \
"v_permlane32_swap_b32_e32 v196, v228\n" \
"v_permlane32_swap_b32_e32 v212, v244\n" \
"buffer_load_dwordx4 v20, s[80:83], s74 offen offset:2048 sc1 lds\n" \
"v_permlane16_swap_b32_e32 v193, v209\n" \
"v_mfma_f32_16x16x32_bf16 a[236:239], v[176:179], v[80:83], a[236:239]\n" \
"v_permlane16_swap_b32_e32 v228, v244\n" \
"v_mfma_f32_16x16x32_bf16 a[144:147], v[152:155], v[88:91], a[144:147]\n" \
"v_max_f32_e32 v193, v193, v209\n" \
"v_mul_f32_e32 v193, s72, v193\n" \
"v_mfma_f32_16x16x32_bf16 a[144:147], v[148:151], v[84:87], a[144:147]\n" \
"v_mul_f32_e32 v45, 0xbfb8aa3b, v228\n" \
"v_mov_b32_e64 v209, v193\n" \
"buffer_load_dwordx4 v20, s[80:83], s74 offen offset:3072 sc1 lds\n" \
"v_mfma_f32_16x16x32_bf16 a[148:151], v[156:159], v[84:87], a[148:151]\n" \
"s_sub_u32 m0, %[mB], 0x4000\n" \
"v_cvt_pk_bf16_f32 v135, v250, v251\n" \
"v_mfma_f32_16x16x16_bf16 v[196:199], v[190:191], v[46:47], 0\n" \
"v_mfma_f32_16x16x16_bf16 v[212:215], v[206:207], v[46:47], 0\n" \
"v_mfma_f32_16x16x16_bf16 v[228:231], v[222:223], v[46:47], 0\n" \
"buffer_load_dwordx4 v20, s[80:83], s75 offen offset:0 sc1 lds\n" \
"v_permlane16_swap_b32_e32 v193, v209\n" \
"v_mfma_f32_16x16x16_bf16 v[244:247], v[238:239], v[46:47], 0\n" \
"v_mov_b32_e32 v225, v193\n" \
"v_mov_b32_e64 v241, v209\n" \
"v_mfma_f32_16x16x32_bf16 a[148:151], v[160:163], v[88:91], a[148:151]\n" \
"v_permlane32_swap_b32_e32 v193, v225\n" \
"v_permlane32_swap_b32_e32 v209, v241\n" \
"v_mfma_f32_16x16x32_bf16 a[152:155], v[168:171], v[88:91], a[152:155]\n" \
"buffer_load_dwordx4 v20, s[80:83], s75 offen offset:1024 sc1 lds\n" \
"v_mfma_f32_16x16x32_bf16 a[152:155], v[164:167], v[84:87], a[152:155]\n" \
"v_fma_f32 v192, v192, s72, -v193\n" \
"v_fma_f32 v194, v194, s72, -v193\n" \
"v_mfma_f32_16x16x32_bf16 a[156:159], v[172:175], v[84:87], a[156:159]\n" \
"v_fma_f32 v208, v208, s72, -v209\n" \
"v_fma_f32 v210, v210, s72, -v209\n" \
"v_exp_f32_e32 v192, v192\n" \
"v_exp_f32_e32 v194, v194\n" \
"v_exp_f32_e64 v208, v208\n" \
"buffer_load_dwordx4 v20, s[80:83], s75 offen offset:2048 sc1 lds\n" \
"v_exp_f32_e64 v210, v210\n" \
"v_mfma_f32_16x16x32_bf16 a[156:159], v[176:179], v[88:91], a[156:159]\n" \
"v_fma_f32 v224, v224, s72, -v225\n" \
"v_fma_f32 v226, v226, s72, -v225\n" \
"v_exp_f32_e32 v224, v224\n" \
"v_exp_f32_e32 v226, v226\n" \
"v_mfma_f32_16x16x32_bf16 a[176:179], v[152:155], v[96:99], a[176:179]\n" \
"v_fma_f32 v240, v240, s72, -v241\n" \
"v_fma_f32 v242, v242, s72, -v241\n" \
"buffer_load_dwordx4 v20, s[80:83], s75 offen offset:3072 sc1 lds\n" \
"v_mfma_f32_16x16x32_bf16 a[176:179], v[148:151], v[92:95], a[176:179]\n" \
"s_mov_b32 m0, s55\n" \
"v_cvt_pk_bf16_f32 v192, v192, v194\n" \
"v_mfma_f32_16x16x32_bf16 a[180:183], v[156:159], v[92:95], a[180:183]\n" \
"v_cvt_pk_bf16_f32 v208, v208, v210\n" \
"v_cvt_pk_bf16_f32 v136, v196, v197\n" \
"v_exp_f32_e32 v240, v240\n" \
"v_exp_f32_e64 v242, v242\n" \
"v_mfma_f32_16x16x32_bf16 a[180:183], v[160:163], v[96:99], a[180:183]\n" \
"buffer_load_dwordx4 v20, s[80:83], s76 offen offset:0 sc1 lds\n" \
"v_mfma_f32_16x16x32_bf16 a[184:187], v[168:171], v[96:99], a[184:187]\n" \
"v_cvt_pk_bf16_f32 v137, v198, v199\n" \
"v_cvt_pk_bf16_f32 v138, v212, v213\n" \
"v_mfma_f32_16x16x32_bf16 a[184:187], v[164:167], v[92:95], a[184:187]\n" \
"v_cvt_pk_bf16_f32 v139, v214, v215\n" \
"v_perm_b32 v184, v192, v192, v40\n" \
"v_mfma_f32_16x16x32_bf16 a[188:191], v[172:175], v[92:95], a[188:191]\n" \
"v_perm_b32 v185, v192, v192, v41\n" \
"v_perm_b32 v186, v192, v192, v42\n" \
"v_mfma_f32_16x16x32_bf16 a[188:191], v[176:179], v[96:99], a[188:191]\n" \
"buffer_load_dwordx4 v20, s[80:83], s76 offen offset:1024 sc1 lds\n" \
"v_perm_b32 v187, v192, v192, v43\n" \
"v_perm_b32 v200, v208, v208, v40\n" \
"v_perm_b32 v201, v208, v208, v41\n" \
"v_perm_b32 v202, v208, v208, v42\n" \
"v_perm_b32 v203, v208, v208, v43\n" \
"v_cvt_pk_bf16_f32 v224, v224, v226\n" \
"v_cvt_pk_bf16_f32 v140, v228, v229\n" \
"v_cvt_pk_bf16_f32 v141, v230, v231\n" \
"L_pf9:\n" \
"s_cbranch_execz L_pf13\n" \
"s_nop 0\n" \
"v_mfma_f32_16x16x32_bf16 v[116:119], v[134:137], v[184:187], 0\n" \
"v_mfma_f32_16x16x32_bf16 v[120:123], v[136:139], v[200:203], 0\n" \
"v_perm_b32 v216, v224, v224, v40\n" \
"v_perm_b32 v217, v224, v224, v41\n" \
"v_perm_b32 v218, v224, v224, v42\n" \
"v_perm_b32 v219, v224, v224, v43\n" \
"v_cvt_pk_bf16_f32 v240, v240, v242\n" \
"v_cvt_pk_bf16_f32 v142, v244, v245\n" \
"v_cvt_pk_bf16_f32 v143, v246, v247\n" \
"v_mfma_f32_16x16x32_bf16 v[36:39], v[138:141], v[216:219], 0\n" \
"v_perm_b32 v232, v240, v240, v40\n" \
"v_perm_b32 v233, v240, v240, v41\n" \
"v_perm_b32 v234, v240, v240, v42\n" \
"v_perm_b32 v235, v240, v240, v43\n" \
"v_exp_f32_e64 v45, v45\n" \
"v_mov_b32_dpp v118, v122 quad_perm:[0,1,2,3] row_mask:0x2 bank_mask:0xf\n" \
"v_mfma_f32_16x16x32_bf16 v[48:51], v[140:143], v[232:235], 0\n" \
"v_cvt_pk_bf16_f32 v124, v252, v253\n" \
"v_cvt_pk_bf16_f32 v125, v254, v255\n" \
"v_mov_b32_dpp v118, v38 quad_perm:[0,1,2,3] row_mask:0x4 bank_mask:0xf\n" \
"v_add_f32_e64 v45, 1.0, v45\n" \
"v_mfma_f32_16x16x32_bf16 v[192:195], v[124:127], v[184:187], 0\n" \
"v_mfma_f32_16x16x32_bf16 v[208:211], v[126:129], v[200:203], 0\n" \
"v_mfma_f32_16x16x32_bf16 v[224:227], v[128:131], v[216:219], 0\n" \
"v_mfma_f32_16x16x32_bf16 v[240:243], v[130:133], v[232:235], 0\n" \
"v_mov_b32_dpp v118, v50 quad_perm:[0,1,2,3] row_mask:0x8 bank_mask:0xf\n" \
"v_rcp_f32_e64 v45, v45\n" \
"v_mov_b32_dpp v116, v120 quad_perm:[0,1,2,3] row_mask:0x2 bank_mask:0xf\n" \
"v_mov_b32_dpp v117, v121 quad_perm:[0,1,2,3] row_mask:0x2 bank_mask:0xf\n" \
"v_rcp_f32_e64 v118, v118\n" \
"v_mov_b32_dpp v116, v36 quad_perm:[0,1,2,3] row_mask:0x4 bank_mask:0xf\n" \
"v_mov_b32_dpp v117, v37 quad_perm:[0,1,2,3] row_mask:0x4 bank_mask:0xf\n" \
"v_mul_f32_e64 v118, v45, v118\n" \
"v_mov_b32_dpp v116, v48 quad_perm:[0,1,2,3] row_mask:0x8 bank_mask:0xf\n" \
"buffer_store_dwordx4 a[12:15], v20, s[80:83], s89 offen offset:1024 sc1\n" \
"s_add_u32 s89, s45, 0x0\n" \
"v_mov_b32_dpp v117, v49 quad_perm:[0,1,2,3] row_mask:0x8 bank_mask:0xf\n" \
"v_mul_f32_e32 v116, v118, v116\n" \
"v_mul_f32_e32 v117, v118, v117\n" \
"v_mov_b32_e32 v122, v118\n" \
"v_cvt_pk_bf16_f32 v116, v116, v117\n" \
"v_accvgpr_read_b32 v52, a128\n" \
"v_permlane16_swap_b32_e32 v118, v122\n" \
"v_mov_b32_e32 v38, v118\n" \
"v_mov_b32_e64 v50, v122\n" \
"v_accvgpr_read_b32 v53, a129\n" \
"v_permlane32_swap_b32_e32 v118, v38\n" \
"v_permlane32_swap_b32_e32 v122, v50\n" \
"v_mul_f32_e32 v192, v118, v192\n" \
"v_mul_f32_e32 v193, v118, v193\n" \
"v_mul_f32_e32 v208, v122, v208\n" \
"v_mul_f32_e32 v209, v122, v209\n" \
"v_mul_f32_e32 v224, v38, v224\n" \
"v_mul_f32_e32 v225, v38, v225\n" \
"v_mul_f32_e32 v240, v50, v240\n" \
"v_mul_f32_e32 v241, v50, v241\n" \
"v_mul_f32_e32 v194, v118, v194\n" \
"v_mul_f32_e32 v195, v118, v195\n" \
"v_cvt_pk_bf16_f32 v192, v192, v193\n" \
"v_mul_f32_e32 v210, v122, v210\n" \
"v_mul_f32_e32 v211, v122, v211\n" \
"v_cvt_pk_bf16_f32 v208, v208, v209\n" \
"v_mul_f32_e32 v226, v38, v226\n" \
"v_mul_f32_e32 v227, v38, v227\n" \
"v_cvt_pk_bf16_f32 v224, v224, v225\n" \
"v_mul_f32_e32 v242, v50, v242\n" \
"v_mul_f32_e32 v243, v50, v243\n" \
"v_cvt_pk_bf16_f32 v240, v240, v241\n" \
"v_cvt_pk_bf16_f32 v193, v194, v195\n" \
"v_cvt_pk_bf16_f32 v209, v210, v211\n" \
"v_cvt_pk_bf16_f32 v225, v226, v227\n" \
"v_cvt_pk_bf16_f32 v241, v242, v243\n" \
"v_accvgpr_read_b32 v54, a130\n" \
"v_accvgpr_read_b32 v55, a131\n" \
"v_pk_mul_f32 v[52:53], v[52:53], s[70:71]\n" \
"v_accvgpr_read_b32 v56, a132\n" \
"v_accvgpr_read_b32 v57, a133\n" \
"v_accvgpr_read_b32 v58, a134\n" \
"v_accvgpr_read_b32 v59, a135\n" \
"v_pk_mul_f32 v[54:55], v[54:55], s[70:71]\n" \
"v_cvt_pk_bf16_f32 v52, v52, v53\n" \
"v_pk_mul_f32 v[56:57], v[56:57], s[70:71]\n" \
"v_pk_mul_f32 v[58:59], v[58:59], s[70:71]\n" \
"v_cvt_pk_bf16_f32 v53, v54, v55\n" \
"v_cvt_pk_bf16_f32 v54, v56, v57\n" \
"v_cvt_pk_bf16_f32 v55, v58, v59\n" \
"ds_write_b128 v27, v[52:55] offset:0\n" \
"buffer_store_dword v116, v25, s[80:83], s51 offen sc1\n" \
"buffer_store_dwordx2 v[192:193], v24, s[80:83], s84 offen sc1\n" \
"buffer_store_dwordx2 v[208:209], v24, s[80:83], s85 offen sc1\n" \
"buffer_store_dwordx2 v[224:225], v24, s[80:83], s86 offen sc1\n" \
"buffer_store_dwordx2 v[240:241], v24, s[80:83], s87 offen sc1\n" \
"v_accvgpr_read_b32 v76, a224\n" \
"v_accvgpr_read_b32 v77, a225\n" \
"v_accvgpr_read_b32 v78, a226\n" \
"v_accvgpr_read_b32 v79, a227\n" \
"v_pk_mul_f32 v[76:77], v[76:77], s[70:71]\n" \
"v_accvgpr_read_b32 v52, a136\n" \
"v_accvgpr_read_b32 v53, a137\n" \
"v_accvgpr_read_b32 v80, a228\n" \
"v_accvgpr_read_b32 v81, a229\n" \
"v_accvgpr_read_b32 v82, a230\n" \
"v_accvgpr_read_b32 v83, a231\n" \
"v_pk_mul_f32 v[78:79], v[78:79], s[70:71]\n" \
"v_cvt_pk_bf16_f32 v76, v76, v77\n" \
"v_accvgpr_read_b32 v54, a138\n" \
"v_accvgpr_read_b32 v55, a139\n" \
"v_pk_mul_f32 v[52:53], v[52:53], s[70:71]\n" \
"v_pk_mul_f32 v[80:81], v[80:81], s[70:71]\n" \
"v_pk_mul_f32 v[82:83], v[82:83], s[70:71]\n" \
"v_cvt_pk_bf16_f32 v77, v78, v79\n" \
"v_accvgpr_read_b32 v56, a140\n" \
"v_accvgpr_read_b32 v57, a141\n" \
"v_accvgpr_read_b32 v58, a142\n" \
"v_accvgpr_read_b32 v59, a143\n" \
"v_pk_mul_f32 v[54:55], v[54:55], s[70:71]\n" \
"v_cvt_pk_bf16_f32 v52, v52, v53\n" \
"v_cvt_pk_bf16_f32 v78, v80, v81\n" \
"v_cvt_pk_bf16_f32 v79, v82, v83\n" \
"ds_write_b128 v27, v[76:79] offset:12288\n" \
"v_pk_mul_f32 v[56:57], v[56:57], s[70:71]\n" \
"v_pk_mul_f32 v[58:59], v[58:59], s[70:71]\n" \
"v_cvt_pk_bf16_f32 v53, v54, v55\n" \
"v_cvt_pk_bf16_f32 v54, v56, v57\n" \
"v_cvt_pk_bf16_f32 v55, v58, v59\n" \
"ds_write_b128 v27, v[52:55] offset:1024\n" \
"v_accvgpr_read_b32 v76, a208\n" \
"v_accvgpr_read_b32 v77, a209\n" \
"v_accvgpr_read_b32 v78, a210\n" \
"v_accvgpr_read_b32 v79, a211\n" \
"v_accvgpr_read_b32 v52, a168\n" \
"v_accvgpr_read_b32 v53, a169\n" \
"v_mfma_f32_16x16x32_bf16 v[76:79], v[152:155], v[104:107], v[76:79]\n" \
"v_accvgpr_read_b32 v54, a170\n" \
"v_accvgpr_read_b32 v55, a171\n" \
"v_mfma_f32_16x16x32_bf16 v[76:79], v[148:151], v[100:103], v[76:79]\n" \
"v_accvgpr_read_b32 v56, a172\n" \
"v_accvgpr_read_b32 v57, a173\n" \
"v_accvgpr_read_b32 v58, a174\n" \
"v_accvgpr_read_b32 v59, a175\n" \
"v_pk_mul_f32 v[52:53], v[52:53], s[70:71]\n" \
"v_pk_mul_f32 v[54:55], v[54:55], s[70:71]\n" \
"v_cvt_pk_bf16_f32 v52, v52, v53\n" \
"v_pk_mul_f32 v[56:57], v[56:57], s[70:71]\n" \
"v_pk_mul_f32 v[58:59], v[58:59], s[70:71]\n" \
"buffer_store_dwordx4 a[40:43], v20, s[80:83], s88 offen offset:0 sc1\n" \
"s_add_u32 s88, s45, 0x0\n" \
"s_nop 0\n" \
"v_cvt_pk_bf16_f32 v53, v54, v55\n" \
"v_cvt_pk_bf16_f32 v54, v56, v57\n" \
"v_cvt_pk_bf16_f32 v55, v58, v59\n" \
"ds_write_b128 v27, v[52:55] offset:5120\n" \
"v_accvgpr_read_b32 v80, a212\n" \
"v_accvgpr_read_b32 v81, a213\n" \
"v_accvgpr_read_b32 v82, a214\n" \
"v_accvgpr_read_b32 v83, a215\n" \
"v_accvgpr_read_b32 v52, a232\n" \
"v_accvgpr_read_b32 v53, a233\n" \
"v_mfma_f32_16x16x32_bf16 v[80:83], v[156:159], v[100:103], v[80:83]\n" \
"v_accvgpr_read_b32 v54, a234\n" \
"v_accvgpr_read_b32 v55, a235\n" \
"v_mfma_f32_16x16x32_bf16 v[80:83], v[160:163], v[104:107], v[80:83]\n" \
"v_accvgpr_read_b32 v56, a236\n" \
"v_accvgpr_read_b32 v57, a237\n" \
"v_accvgpr_read_b32 v58, a238\n" \
"v_accvgpr_read_b32 v59, a239\n" \
"v_pk_mul_f32 v[52:53], v[52:53], s[70:71]\n" \
"v_pk_mul_f32 v[54:55], v[54:55], s[70:71]\n" \
"v_cvt_pk_bf16_f32 v52, v52, v53\n" \
"v_pk_mul_f32 v[56:57], v[56:57], s[70:71]\n" \
"v_pk_mul_f32 v[58:59], v[58:59], s[70:71]\n" \
"v_cvt_pk_bf16_f32 v53, v54, v55\n" \
"v_cvt_pk_bf16_f32 v54, v56, v57\n" \
"v_cvt_pk_bf16_f32 v55, v58, v59\n" \
"ds_write_b128 v27, v[52:55] offset:13312\n" \
"v_accvgpr_read_b32 v52, a216\n" \
"v_accvgpr_read_b32 v53, a217\n" \
"v_accvgpr_read_b32 v54, a218\n" \
"v_accvgpr_read_b32 v55, a219\n" \
"v_accvgpr_read_b32 v60, a160\n" \
"v_accvgpr_read_b32 v61, a161\n" \
"v_mfma_f32_16x16x32_bf16 v[52:55], v[168:171], v[104:107], v[52:55]\n" \
"v_accvgpr_read_b32 v62, a162\n" \
"v_accvgpr_read_b32 v63, a163\n" \
"v_mfma_f32_16x16x32_bf16 v[52:55], v[164:167], v[100:103], v[52:55]\n" \
"v_accvgpr_read_b32 v56, a220\n" \
"v_accvgpr_read_b32 v57, a221\n" \
"v_accvgpr_read_b32 v58, a222\n" \
"v_accvgpr_read_b32 v59, a223\n" \
"v_pk_mul_f32 v[60:61], v[60:61], s[70:71]\n" \
"v_accvgpr_read_b32 v64, a164\n" \
"v_mfma_f32_16x16x32_bf16 v[56:59], v[172:175], v[100:103], v[56:59]\n" \
"v_accvgpr_read_b32 v65, a165\n" \
"v_accvgpr_read_b32 v66, a166\n" \
"v_mfma_f32_16x16x32_bf16 v[56:59], v[176:179], v[104:107], v[56:59]\n" \
"v_accvgpr_read_b32 v67, a167\n" \
"buffer_store_dwordx4 a[44:47], v20, s[80:83], s89 offen offset:1024 sc1\n" \
"s_add_u32 s89, s45, 0x0\n" \
"s_nop 0\n" \
"v_pk_mul_f32 v[62:63], v[62:63], s[70:71]\n" \
"v_cvt_pk_bf16_f32 v60, v60, v61\n" \
"v_pk_mul_f32 v[64:65], v[64:65], s[70:71]\n" \
"v_pk_mul_f32 v[66:67], v[66:67], s[70:71]\n" \
"v_cvt_pk_bf16_f32 v61, v62, v63\n" \
"v_cvt_pk_bf16_f32 v62, v64, v65\n" \
"v_cvt_pk_bf16_f32 v63, v66, v67\n" \
"ds_write_b128 v27, v[60:63] offset:4096\n" \
"v_accvgpr_read_b32 v68, a192\n" \
"v_accvgpr_read_b32 v69, a193\n" \
"v_accvgpr_read_b32 v60, a200\n" \
"v_accvgpr_read_b32 v61, a201\n" \
"v_accvgpr_read_b32 v70, a194\n" \
"v_accvgpr_read_b32 v71, a195\n" \
"v_pk_mul_f32 v[68:69], v[68:69], s[70:71]\n" \
"v_accvgpr_read_b32 v62, a202\n" \
"v_accvgpr_read_b32 v63, a203\n" \
"v_pk_mul_f32 v[60:61], v[60:61], s[70:71]\n" \
"v_accvgpr_read_b32 v72, a196\n" \
"v_accvgpr_read_b32 v73, a197\n" \
"v_accvgpr_read_b32 v74, a198\n" \
"v_accvgpr_read_b32 v75, a199\n" \
"v_pk_mul_f32 v[70:71], v[70:71], s[70:71]\n" \
"v_cvt_pk_bf16_f32 v68, v68, v69\n" \
"v_accvgpr_read_b32 v64, a204\n" \
"v_accvgpr_read_b32 v65, a205\n" \
"v_accvgpr_read_b32 v66, a206\n" \
"v_accvgpr_read_b32 v67, a207\n" \
"v_pk_mul_f32 v[62:63], v[62:63], s[70:71]\n" \
"v_cvt_pk_bf16_f32 v60, v60, v61\n" \
"v_accvgpr_read_b32 v100, a240\n" \
"v_accvgpr_read_b32 v101, a241\n" \
"v_accvgpr_read_b32 v102, a242\n" \
"v_accvgpr_read_b32 v103, a243\n" \
"v_pk_mul_f32 v[72:73], v[72:73], s[70:71]\n" \
"v_pk_mul_f32 v[74:75], v[74:75], s[70:71]\n" \
"v_mfma_f32_16x16x32_bf16 v[100:103], v[152:155], v[112:115], v[100:103]\n" \
"v_cvt_pk_bf16_f32 v69, v70, v71\n" \
"v_cvt_pk_bf16_f32 v61, v62, v63\n" \
"v_mfma_f32_16x16x32_bf16 v[100:103], v[148:151], v[108:111], v[100:103]\n" \
"v_cvt_pk_bf16_f32 v70, v72, v73\n" \
"v_cvt_pk_bf16_f32 v71, v74, v75\n" \
"ds_write_b128 v27, v[68:71] offset:8192\n" \
"v_pk_mul_f32 v[64:65], v[64:65], s[70:71]\n" \
"v_pk_mul_f32 v[66:67], v[66:67], s[70:71]\n" \
"v_cvt_pk_bf16_f32 v62, v64, v65\n" \
"v_cvt_pk_bf16_f32 v63, v66, v67\n" \
"ds_write_b128 v27, v[60:63] offset:9216\n" \
"v_accvgpr_read_b32 v84, a144\n" \
"buffer_store_dwordx4 a[72:75], v20, s[80:83], s88 offen offset:2048 sc1\n" \
"s_add_u32 s88, s45, 0x1000\n" \
"v_accvgpr_read_b32 v85, a145\n" \
"v_accvgpr_read_b32 v68, a152\n" \
"v_accvgpr_read_b32 v69, a153\n" \
"v_accvgpr_read_b32 v92, a176\n" \
"v_accvgpr_read_b32 v93, a177\n" \
"v_accvgpr_read_b32 v60, a184\n" \
"v_accvgpr_read_b32 v61, a185\n" \
"v_accvgpr_read_b32 v86, a146\n" \
"v_accvgpr_read_b32 v87, a147\n" \
"v_pk_mul_f32 v[84:85], v[84:85], s[70:71]\n" \
"v_accvgpr_read_b32 v70, a154\n" \
"v_accvgpr_read_b32 v71, a155\n" \
"v_pk_mul_f32 v[68:69], v[68:69], s[70:71]\n" \
"v_accvgpr_read_b32 v94, a178\n" \
"v_accvgpr_read_b32 v95, a179\n" \
"v_pk_mul_f32 v[92:93], v[92:93], s[70:71]\n" \
"v_accvgpr_read_b32 v62, a186\n" \
"v_accvgpr_read_b32 v63, a187\n" \
"v_pk_mul_f32 v[60:61], v[60:61], s[70:71]\n" \
"v_pk_mul_f32 v[76:77], v[76:77], s[70:71]\n" \
"v_pk_mul_f32 v[52:53], v[52:53], s[70:71]\n" \
"v_pk_mul_f32 v[100:101], v[100:101], s[70:71]\n" \
"v_accvgpr_read_b32 v88, a148\n" \
"v_accvgpr_read_b32 v89, a149\n" \
"v_accvgpr_read_b32 v90, a150\n" \
"v_accvgpr_read_b32 v91, a151\n" \
"v_pk_mul_f32 v[86:87], v[86:87], s[70:71]\n" \
"v_cvt_pk_bf16_f32 v84, v84, v85\n" \
"v_accvgpr_read_b32 v72, a156\n" \
"v_accvgpr_read_b32 v73, a157\n" \
"v_accvgpr_read_b32 v74, a158\n" \
"v_accvgpr_read_b32 v75, a159\n" \
"v_pk_mul_f32 v[70:71], v[70:71], s[70:71]\n" \
"v_cvt_pk_bf16_f32 v68, v68, v69\n" \
"v_accvgpr_read_b32 v96, a180\n" \
"v_accvgpr_read_b32 v97, a181\n" \
"v_accvgpr_read_b32 v98, a182\n" \
"v_accvgpr_read_b32 v99, a183\n" \
"v_pk_mul_f32 v[94:95], v[94:95], s[70:71]\n" \
"v_cvt_pk_bf16_f32 v92, v92, v93\n" \
"v_accvgpr_read_b32 v64, a188\n" \
"v_accvgpr_read_b32 v65, a189\n" \
"v_accvgpr_read_b32 v66, a190\n" \
"v_accvgpr_read_b32 v67, a191\n" \
"v_pk_mul_f32 v[62:63], v[62:63], s[70:71]\n" \
"v_cvt_pk_bf16_f32 v60, v60, v61\n" \
"v_pk_mul_f32 v[78:79], v[78:79], s[70:71]\n" \
"v_cvt_pk_bf16_f32 v76, v76, v77\n" \
"v_pk_mul_f32 v[54:55], v[54:55], s[70:71]\n" \
"v_cvt_pk_bf16_f32 v52, v52, v53\n" \
"v_pk_mul_f32 v[102:103], v[102:103], s[70:71]\n" \
"v_cvt_pk_bf16_f32 v100, v100, v101\n" \
"v_pk_mul_f32 v[88:89], v[88:89], s[70:71]\n" \
"v_pk_mul_f32 v[90:91], v[90:91], s[70:71]\n" \
"v_cvt_pk_bf16_f32 v85, v86, v87\n" \
"v_pk_mul_f32 v[72:73], v[72:73], s[70:71]\n" \
"v_pk_mul_f32 v[74:75], v[74:75], s[70:71]\n" \
"v_cvt_pk_bf16_f32 v69, v70, v71\n" \
"v_pk_mul_f32 v[96:97], v[96:97], s[70:71]\n" \
"v_pk_mul_f32 v[98:99], v[98:99], s[70:71]\n" \
"buffer_store_dwordx4 a[76:79], v20, s[80:83], s89 offen offset:3072 sc1\n" \
"s_add_u32 s89, s45, 0x1000\n" \
"v_cvt_pk_bf16_f32 v93, v94, v95\n" \
"v_pk_mul_f32 v[64:65], v[64:65], s[70:71]\n" \
"v_pk_mul_f32 v[66:67], v[66:67], s[70:71]\n" \
"v_cvt_pk_bf16_f32 v61, v62, v63\n" \
"v_pk_mul_f32 v[80:81], v[80:81], s[70:71]\n" \
"v_pk_mul_f32 v[82:83], v[82:83], s[70:71]\n" \
"v_cvt_pk_bf16_f32 v77, v78, v79\n" \
"v_pk_mul_f32 v[56:57], v[56:57], s[70:71]\n" \
"v_pk_mul_f32 v[58:59], v[58:59], s[70:71]\n" \
"v_cvt_pk_bf16_f32 v53, v54, v55\n" \
"v_accvgpr_read_b32 v104, a244\n" \
"v_accvgpr_read_b32 v105, a245\n" \
"v_accvgpr_read_b32 v106, a246\n" \
"v_accvgpr_read_b32 v107, a247\n" \
"v_cvt_pk_bf16_f32 v101, v102, v103\n" \
"v_cvt_pk_bf16_f32 v86, v88, v89\n" \
"v_mfma_f32_16x16x32_bf16 v[104:107], v[156:159], v[108:111], v[104:107]\n" \
"v_cvt_pk_bf16_f32 v87, v90, v91\n" \
"v_cvt_pk_bf16_f32 v70, v72, v73\n" \
"ds_write_b128 v21, v[84:87] offset:16384\n" \
"v_mfma_f32_16x16x32_bf16 v[104:107], v[160:163], v[112:115], v[104:107]\n" \
"v_cvt_pk_bf16_f32 v71, v74, v75\n" \
"ds_write_b128 v21, v[68:71] offset:17408\n" \
"v_cvt_pk_bf16_f32 v94, v96, v97\n" \
"v_cvt_pk_bf16_f32 v95, v98, v99\n" \
"ds_write_b128 v21, v[92:95] offset:20480\n" \
"v_cvt_pk_bf16_f32 v62, v64, v65\n" \
"v_cvt_pk_bf16_f32 v63, v66, v67\n" \
"ds_write_b128 v21, v[60:63] offset:21504\n" \
"v_pk_mul_f32 v[104:105], v[104:105], s[70:71]\n" \
"v_pk_mul_f32 v[106:107], v[106:107], s[70:71]\n" \
"v_cvt_pk_bf16_f32 v78, v80, v81\n" \
"v_cvt_pk_bf16_f32 v79, v82, v83\n" \
"ds_write_b128 v21, v[76:79] offset:24576\n" \
"v_cvt_pk_bf16_f32 v54, v56, v57\n" \
"v_cvt_pk_bf16_f32 v55, v58, v59\n" \
"ds_write_b128 v21, v[52:55] offset:25600\n" \
"v_cvt_pk_bf16_f32 v102, v104, v105\n" \
"v_cvt_pk_bf16_f32 v103, v106, v107\n" \
"ds_write_b128 v21, v[100:103] offset:28672\n" \
"buffer_store_dwordx4 a[104:107], v20, s[80:83], s88 offen offset:0 sc1\n" \
"v_accvgpr_read_b32 v84, a248\n" \
"v_accvgpr_read_b32 v85, a249\n" \
"v_accvgpr_read_b32 v86, a250\n" \
"v_accvgpr_read_b32 v87, a251\n" \
"v_accvgpr_read_b32 v88, a252\n" \
"v_accvgpr_read_b32 v89, a253\n" \
"v_mfma_f32_16x16x32_bf16 v[84:87], v[168:171], v[112:115], v[84:87]\n" \
"v_accvgpr_read_b32 v90, a254\n" \
"v_accvgpr_read_b32 v91, a255\n" \
"v_mfma_f32_16x16x32_bf16 v[84:87], v[164:167], v[108:111], v[84:87]\n" \
"v_sub_f32_e32 v44, 1.0, v45\n" \
"v_mfma_f32_16x16x32_bf16 v[88:91], v[172:175], v[108:111], v[88:91]\n" \
"ds_write_b32 v26, v44\n" \
"v_mfma_f32_16x16x32_bf16 v[88:91], v[176:179], v[112:115], v[88:91]\n" \
"buffer_store_dwordx4 a[108:111], v20, s[80:83], s89 offen offset:1024 sc1\n" \
"s_nop 2\n" \
"v_pk_mul_f32 v[84:85], v[84:85], s[70:71]\n" \
"v_pk_mul_f32 v[86:87], v[86:87], s[70:71]\n" \
"v_cvt_pk_bf16_f32 v84, v84, v85\n" \
"v_cvt_pk_bf16_f32 v85, v86, v87\n" \
"v_pk_mul_f32 v[88:89], v[88:89], s[70:71]\n" \
"v_pk_mul_f32 v[90:91], v[90:91], s[70:71]\n" \
"v_cvt_pk_bf16_f32 v86, v88, v89\n" \
"v_cvt_pk_bf16_f32 v87, v90, v91\n" \
"ds_write_b128 v21, v[84:87] offset:29696\n" \
"L_end_%=:\n"
#define PROJ_VCLOB "v21","v24","v25","v33","v34","v35","v36","v37","v38","v39","v40","v41","v42","v43","v44","v45","v46","v47","v48","v49","v50","v51","v52","v53","v54","v55","v56","v57","v58","v59","v60","v61","v62","v63","v64","v65","v66","v67","v68","v69","v70","v71","v72","v73","v74","v75","v76","v77","v78","v79","v80","v81","v82","v83","v84","v85","v86","v87","v88","v89","v90","v91","v92","v93","v94","v95","v96","v97","v98","v99","v100","v101","v102","v103","v104","v105","v106","v107","v108","v109","v110","v111","v112","v113","v114","v115","v116","v117","v118","v119","v120","v121","v122","v123","v124","v125","v126","v127","v128","v129","v130","v131","v132","v133","v134","v135","v136","v137","v138","v139","v140","v141","v142","v143","v144","v145","v146","v147","v148","v149","v150","v151","v152","v153","v154","v155","v156","v157","v158","v159","v160","v161","v162","v163","v164","v165","v166","v167","v168","v169","v170","v171","v172","v173","v174","v175","v176","v177","v178","v179","v180","v181","v182","v183","v184","v185","v186","v187","v188","v189","v190","v191","v192","v193","v194","v195","v196","v197","v198","v199","v200","v201","v202","v203","v204","v205","v206","v207","v208","v209","v210","v211","v212","v213","v214","v215","v216","v217","v218","v219","v220","v221","v222","v223","v224","v225","v226","v227","v228","v229","v230","v231","v232","v233","v234","v235","v236","v237","v238","v239","v240","v241","v242","v243","v244","v245","v246","v247","v248","v249","v250","v251","v252","v253","v254","v255"
#define PROJ_SCLOB "s37","s38","s39","s40","s41","s42","s43","s44","s45","s46","s47","s48","s49","s50","s51","s52","s53","s54","s55","s62","s63","s76","s77","s78","s79","s80","s81","s82","s83","s84","s85","s86","s87","s88","s89","s94","s95","s96","s97","s98","s99","s100","s101"

#define PLO(p) ((unsigned)(size_t)(p))
#define PHI(p) ((unsigned)((size_t)(p) >> 32))
__device__ __forceinline__ unsigned proj_phase(const u16* __restrict__ x, const u16* __restrict__ ckv, const u16* __restrict__ wq, const u16* __restrict__ wuk,
                                                       const u16* __restrict__ wuv, const u16* __restrict__ wqkv, const u16* __restrict__ gw, const u16* wo, const u16* wob, const u16* out,
                                                       char* __restrict__ ws, char* lds, const unsigned wid, unsigned& tag, u32x4 (&vp)[5], u32x4 (&sa)[5], unsigned (&sd)[2]) {
    typedef unsigned u32x2 __attribute__((ext_vector_type(2)));
    unsigned* const flags = (unsigned*)(ws + WS_F);
    const int b = blockIdx.x, h = (b & 7) * 4 + ((b >> 3) & 3), mt = b >> 5;
    const unsigned L0 = (unsigned)(size_t)lds, rb = (wid < 2u ? 15u - mt : mt) * 128u;
    RTS(0);
#define PST() do {} while (0)
    const unsigned lane = threadIdx.x & 63u, wr = wid >> 1, wc = wid & 1u;
    PST();
    unsigned vAc[2], vB[2];
    const unsigned sp = b & 7, nt = (b >> 3) & 3, rot = (PROJ_ROT) & 7u;
#pragma unroll
    for (int par = 0; par < 2; ++par) {
        const unsigned lr = lane >> 3, cc = (lane & 7u) ^ (4u * par + (lr >> 1)), r0 = rb + wc * 64u + lr;
        vAc[par] = r0 * (DC * 2) + cc * 16u;
        const unsigned br = lane >> 4, p16 = lane & 15u, sc = (((p16 >> 1) ^ (br | (par << 2))) << 1) | (p16 & 1u);
        vB[par] = (16u * wid + br) * (NBIG * 2) + h * (HD * 2) + sc * 16u;
    }
#define SRD(p, n) (u32x4){PLO(p), PHI(p) & 0xffffu, n, 0x00020000u}
    u32x4 sC = SRD(ckv, S * DC * 2), sK = SRD(wuk, DC * NBIG * 2), sV = SRD(wuv, DC * NBIG * 2);
#define PRE(n) do { SBAR(); asm volatile(PROJ_PRE##n :: "{v22}"(vAc[0]), "{v23}"(vAc[1]), "{v26}"(vB[0]), "{v27}"(vB[1]), "{s[64:67]}"(sC), "{s[68:71]}"(sK), "{s[72:75]}"(sV), \
                 [mA] "s"(L0 + wid * 8192u), [mB] "s"(L0 + 65536u + wid * 4096u), [rc] "s"(rot * 128u) : "memory", "m0", "scc", "s62", "s63", "s84", "s85"); SBAR(); } while (0)
    PRE(0);
    const unsigned fr = lane & 15u, fg = lane >> 4, r4 = (lane >> 2) & 3u, sB = r4 | ((fg & 1u) << 2);
    const unsigned aA0 = L0 + (wr * 64u + fr) * 128u + ((fg ^ (fr >> 1)) << 4);
    unsigned vl = lane * 16u, r32 = L0 + 65536u + (8u * fg + r4) * 256u + (lane & 3u) * 8u + ((sB ^ (4u * wc)) << 5);
    u32x2 r28 = {aA0, aA0 ^ 64u};
    asm volatile("" : "+v"(vl), "+v"(r28), "+v"(r32));
    PST();
    PRE(1);
    PST();
    PRE(2);
    PST();
    asm volatile("s_mov_b64 exec, 0\n s_cmp_lt_u32 %0, 2\n s_cbranch_scc1 L_chA\n s_cmp_eq_u32 %0, 2\n s_cbranch_scc1 L_pf2\n s_branch L_pf3\n"
                 "L_chA: s_cmp_eq_u32 %0, 0\n s_cbranch_scc1 L_pf0\n s_branch L_pf1\n L_retA: s_mov_b64 exec, -1" :: "s"(wid) : "scc", "memory");
    PRE(3);
    PST();
    sa[0] = (u32x4){PLO(x), PHI(x), PLO(wq), PHI(wq)}; sa[1] = (u32x4){PLO(wqkv), PHI(wqkv), PLO(gw), PHI(gw)}; sa[2] = (u32x4){PLO(wo), PHI(wo), PLO(wob), PHI(wob)}; sa[3] = (u32x4){PLO(out), PHI(out), rot * 128u, 0u};
    asm volatile(PROJ_ASM
                 : [tag] "=&s"(tag), "+{s[8:11]}"(sa[0]), "+{s[12:15]}"(sa[1]), "+{s[16:19]}"(sa[2]), "+{s[20:23]}"(sa[3]), "=&{s[56:59]}"(sa[4]), "=&{s60}"(sd[0]), "=&{s61}"(sd[1]),
                   "+{s[64:67]}"(sC), "+{s[68:71]}"(sK), "+{s[72:75]}"(sV), "+{v22}"(vAc[0]), "+{v23}"(vAc[1]), "+{v26}"(vB[0]), "+{v27}"(vB[1]), "+{v[28:29]}"(r28), "+{v32}"(r32),
                   "={v[0:3]}"(vp[0]), "={v[4:7]}"(vp[1]), "={v[8:11]}"(vp[2]), "={v[12:15]}"(vp[3]), "={v[16:19]}"(vp[4])
                 : "{v20}"(vl), [wsl] "s"(PLO(ws)), [wsh] "s"(PHI(ws)), [b] "s"(b), [w] "s"(wid), [mA] "s"(L0 + wid * 8192u), [mB] "s"(L0 + 65536u + wid * 4096u), [mS] "s"(L0 + 151552u + wid * 512u)
                 : "memory", "m0", "vcc", "scc", PROJ_VCLOB, PROJ_SCLOB, ACLOB_LO, ACLOB_HI);
    RTS(1);
    return vl;
}

namespace att {
#define DI __device__ __forceinline__
constexpr int LDS_BYTES = 163840;
constexpr unsigned LDS_G = 159744u, LDS_L = 161792u;
typedef __bf16 bfx8 __attribute__((ext_vector_type(8)));
DI unsigned rfl(unsigned x) { return __builtin_amdgcn_readfirstlane(x); }
DI f32x4 mfma(u32x4 a, u32x4 b, f32x4 c) { return __builtin_amdgcn_mfma_f32_16x16x32_bf16(__builtin_bit_cast(bfx8, a), __builtin_bit_cast(bfx8, b), c, 0, 0, 0); }
typedef __bf16 bfx2 __attribute__((ext_vector_type(2)));
typedef float f32x2 __attribute__((ext_vector_type(2)));
DI unsigned cvtpk2(float lo, float hi) { f32x2 v = {lo, hi}; bfx2 b = __builtin_convertvector(v, bfx2); return __builtin_bit_cast(unsigned, b); }
DI void att_exact(const u16* __restrict__ x, const u16* __restrict__ wq, char* __restrict__ ws, unsigned h, unsigned r0, float g1) {
    const unsigned lane = lane_id(), g = lane >> 4, c = lane & 15u;
    f32x4 o[8];
#pragma unroll
    for (int n = 0; n < 8; ++n) o[n] = (f32x4){0.f, 0.f, 0.f, 0.f};
#pragma unroll 1
    for (unsigned kk = 0; kk < (unsigned)DM; kk += 32u) {
        const unsigned k = kk + 8u * g;
        const u32x4 xf = *reinterpret_cast<const u32x4*>(x + (r0 + c) * DM + k);
#pragma unroll
        for (int n = 0; n < 8; ++n) {
            const u16* w = wq + k * NBIG + h * HD + n * 16 + c; u32x4 wf;
#pragma unroll
            for (int e = 0; e < 4; ++e) wf[e] = (unsigned)w[2 * e * NBIG] | ((unsigned)w[(2 * e + 1) * NBIG] << 16);
            o[n] = mfma(wf, xf, o[n]);
        }
    }
    u32x4 q[4];
#pragma unroll
    for (int p = 0; p < 4; ++p) {
        const f32x4 a = o[2 * p] * 0.12751743082f, b = o[2 * p + 1] * 0.12751743082f;
        q[p] = (u32x4){cvtpk2(a[0], a[1]), cvtpk2(a[2], a[3]), cvtpk2(b[0], b[1]), cvtpk2(b[2], b[3])};
    }
#pragma unroll
    for (int n = 0; n < 8; ++n) o[n] = (f32x4){0.f, 0.f, 0.f, 0.f};
    const __amdgpu_buffer_rsrc_t srd = __builtin_amdgcn_make_buffer_rsrc((void*)(ws + WS_K + (size_t)h * 1048576), (short)0, 1048576, 0x00020000);
    float m = -3.0e38f, l = 0.f;
#pragma unroll 1
    for (unsigned k0 = 0; k0 <= r0 + 15u; k0 += 32u) {
        const unsigned tb = (k0 >> 6) * 32768u + (k0 & 32u) * 256u;
        f32x4 s[2]; float mx = m;
#pragma unroll
        for (int kt = 0; kt < 2; ++kt) {
            s[kt] = (f32x4){0.f, 0.f, 0.f, 0.f};
#pragma unroll
            for (int p = 0; p < 4; ++p) s[kt] = mfma(__builtin_amdgcn_raw_buffer_load_b128(srd, lane * 16u + (kt * 4 + p) * 1024u, tb, 16), q[p], s[kt]);
#pragma unroll
            for (int e = 0; e < 4; ++e) { if (k0 + 16u * kt + 4u * g + e > r0 + c) s[kt][e] = -3.0e38f; mx = fmaxf(mx, s[kt][e]); }
        }
        mx = fmaxf(mx, __shfl_xor(mx, 16)); mx = fmaxf(mx, __shfl_xor(mx, 32));
        const float al = __builtin_amdgcn_exp2f(m - mx); m = mx;
        u32x4 pf;
#pragma unroll
        for (int e = 0; e < 4; ++e) pf[e] = cvtpk2(__builtin_amdgcn_exp2f(s[e >> 1][2 * (e & 1)] - m), __builtin_amdgcn_exp2f(s[e >> 1][2 * (e & 1) + 1] - m));
        l *= al;
#pragma unroll
        for (int e = 0; e < 4; ++e) l += __uint_as_float(pf[e] << 16) + __uint_as_float(pf[e] & 0xffff0000u);
#pragma unroll
        for (int n = 0; n < 8; ++n) o[n] = mfma(__builtin_amdgcn_raw_buffer_load_b128(srd, lane * 16u + 16384u + ((n >> 2) * 8 + (n & 3)) * 1024u, tb - (k0 & 32u) * 128u, 16), pf, o[n] * al);
    }
    l += __shfl_xor(l, 16); l += __shfl_xor(l, 32);
    const float f = g1 / l;
    const __amdgpu_buffer_rsrc_t srdA = __builtin_amdgcn_make_buffer_rsrc((void*)(ws + WS_AF), (short)0, 128 * KS_TOT * 1024, 0x00020000);
#pragma unroll
    for (int p = 0; p < 4; ++p) {
        const f32x4 a = o[2 * p] * f, b = o[2 * p + 1] * f;
        __builtin_amdgcn_raw_buffer_store_b128((u32x4){cvtpk2(a[0], a[1]), cvtpk2(a[2], a[3]), cvtpk2(b[0], b[1]), cvtpk2(b[2], b[3])}, srdA, lane * 16u + p * 1024u, ((r0 >> 4) * KS_TOT + 18u + 4u * h) * 1024u, 16);
    }
}
}

#define ATT_ASM \
"L_att0:\n" \
"s_mov_b32 s101, 0\n" \
"s_waitcnt vmcnt(22) lgkmcnt(0)\n" \
"s_barrier\n" \
"v_accvgpr_write_b32 a216, 0\n" \
"v_accvgpr_write_b32 a217, 0\n" \
"v_accvgpr_write_b32 a218, 0\n" \
"v_accvgpr_write_b32 a219, 0\n" \
"v_and_b32_e32 v63, 0x300, v20\n" \
"v_add_u32_e32 v58, s20, v20\n" \
"v_mfma_f32_32x32x16_bf16 v[128:143], a[216:219], a[216:219], 0\n" \
"ds_read_b128 v[112:115], v63 offset:0\n" \
"ds_read_b128 a[0:3], v58 offset:0\n" \
"ds_read_b128 a[16:19], v58 offset:8192\n" \
"ds_read_b128 v[116:119], v63 offset:1024\n" \
"ds_read_b128 a[4:7], v58 offset:1024\n" \
"v_add_u32_e32 v59, s21, v20\n" \
"v_mfma_f32_32x32x16_bf16 v[144:159], a[216:219], a[216:219], 0\n" \
"ds_read_b128 a[32:35], v59 offset:0\n" \
"ds_read_b128 a[48:51], v59 offset:8192\n" \
"s_sub_u32 s67, s12, 0xc000\n" \
"s_mov_b32 s65, s12\n" \
"v_add_u32_e32 v55, s67, v20\n" \
"v_add_u32_e32 v52, 0xc000, v55\n" \
"v_mfma_f32_32x32x16_bf16 v[160:175], a[216:219], a[216:219], 0\n" \
"ds_read_b128 a[20:23], v58 offset:9216\n" \
"s_xor_b32 m0, s65, 0x10000\n" \
"ds_read_b128 a[36:39], v59 offset:1024\n" \
"buffer_load_dwordx4 v52, s[8:11], 0 offen offset:0 sc1 lds\n" \
"ds_read_b128 a[52:55], v59 offset:9216\n" \
"v_mfma_f32_32x32x16_bf16 v[176:191], a[216:219], a[216:219], 0\n" \
"buffer_load_dwordx4 v52, s[8:11], 0 offen offset:1024 sc1 lds\n" \
"ds_read_b128 v[120:123], v63 offset:2048\n" \
"v_add_u32_e32 v60, s15, v20\n" \
"v_add_u32_e32 v60, 0x10000, v60\n" \
"v_mov_b32_e32 v56, s62\n" \
"v_mfma_f32_32x32x16_bf16 v[192:207], a[216:219], a[216:219], 0\n" \
"ds_read_b128 a[8:11], v58 offset:2048\n" \
"ds_read_b128 a[24:27], v58 offset:10240\n" \
"v_readlane_b32 s68, v11, 0\n" \
"v_bfe_i32 v21, v20, 4, 1\n" \
"v_mov_b32_e32 v54, 0x3f803f80\n" \
"v_mov_b32_e32 v57, 0x3f003f00\n" \
"v_mfma_f32_32x32x16_bf16 v[208:223], a[216:219], a[216:219], 0\n" \
"ds_read_b128 a[40:43], v59 offset:2048\n" \
"ds_read_b128 a[56:59], v59 offset:10240\n" \
"s_waitcnt lgkmcnt(14)\n" \
"v_xor_b32_e32 v112, 0x80008000, v112\n" \
"v_xor_b32_e32 v113, 0x80008000, v113\n" \
"v_xor_b32_e32 v114, 0x80008000, v114\n" \
"v_mfma_f32_32x32x16_bf16 v[224:239], a[216:219], a[216:219], 0\n" \
"ds_read_b128 v[124:127], v63 offset:3072\n" \
"ds_read_b128 a[12:15], v58 offset:3072\n" \
"v_xor_b32_e32 v115, 0x80008000, v115\n" \
"v_and_b32_e32 v21, 0xaaaaaaaa, v21\n" \
"v_accvgpr_write_b32 a220, v112\n" \
"s_waitcnt lgkmcnt(13)\n" \
"v_xor_b32_e32 v116, 0x80008000, v116\n" \
"v_mfma_f32_32x32x16_bf16 v[240:255], a[216:219], a[216:219], 0\n" \
"ds_read_b128 a[28:31], v58 offset:11264\n" \
"ds_read_b128 a[44:47], v59 offset:3072\n" \
"v_xor_b32_e32 v117, 0x80008000, v117\n" \
"v_xor_b32_e32 v118, 0x80008000, v118\n" \
"v_xor_b32_e32 v119, 0x80008000, v119\n" \
"v_accvgpr_write_b32 a221, v113\n" \
"v_mfma_f32_32x32x16_bf16 a[192:207], a[216:219], a[216:219], 0\n" \
"ds_read_b128 a[60:63], v59 offset:11264\n" \
"ds_read_b128 a[160:163], v20 offset:0\n" \
"v_accvgpr_write_b32 a222, v114\n" \
"v_accvgpr_write_b32 a223, v115\n" \
"v_readlane_b32 s80, v11, 9\n" \
"v_readlane_b32 s81, v11, 10\n" \
"v_mfma_f32_16x16x32_bf16 v[32:35], v[112:115], a[0:3], 0\n" \
"ds_read_b128 a[164:167], v20 offset:1024\n" \
"ds_read_b128 a[168:171], v20 offset:2048\n" \
"v_mfma_f32_16x16x32_bf16 v[36:39], v[112:115], a[16:19], 0\n" \
"ds_read_b128 a[172:175], v20 offset:3072\n" \
"s_waitcnt lgkmcnt(15)\n" \
"v_mfma_f32_16x16x32_bf16 v[40:43], v[112:115], a[32:35], 0\n" \
"ds_read_b128 a[188:191], v20 offset:7168\n" \
"s_waitcnt lgkmcnt(14)\n" \
"v_xor_b32_e32 v120, 0x80008000, v120\n" \
"v_mfma_f32_16x16x32_bf16 v[44:47], v[112:115], a[48:51], 0\n" \
"ds_read_b128 a[184:187], v20 offset:6144\n" \
"v_xor_b32_e32 v121, 0x80008000, v121\n" \
"v_mfma_f32_16x16x32_bf16 v[32:35], v[116:119], a[4:7], v[32:35]\n" \
"v_xor_b32_e32 v122, 0x80008000, v122\n" \
"v_xor_b32_e32 v123, 0x80008000, v123\n" \
"v_mfma_f32_16x16x32_bf16 v[36:39], v[116:119], a[20:23], v[36:39]\n" \
"ds_read_b128 a[180:183], v20 offset:5120\n" \
"ds_read_b128 a[176:179], v20 offset:4096\n" \
"v_mfma_f32_16x16x32_bf16 v[40:43], v[116:119], a[36:39], v[40:43]\n" \
"ds_read_b128 a[128:131], v20 offset:8192\n" \
"s_mov_b32 s82, 0x47fca0\n" \
"v_mfma_f32_16x16x32_bf16 v[44:47], v[116:119], a[52:55], v[44:47]\n" \
"s_waitcnt lgkmcnt(13)\n" \
"v_xor_b32_e32 v124, 0x80008000, v124\n" \
"v_mfma_f32_16x16x32_bf16 v[32:35], v[120:123], a[8:11], v[32:35]\n" \
"v_xor_b32_e32 v125, 0x80008000, v125\n" \
"v_xor_b32_e32 v126, 0x80008000, v126\n" \
"v_mfma_f32_16x16x32_bf16 v[36:39], v[120:123], a[24:27], v[36:39]\n" \
"ds_read_b128 a[132:135], v20 offset:9216\n" \
"v_xor_b32_e32 v127, 0x80008000, v127\n" \
"v_mfma_f32_16x16x32_bf16 v[40:43], v[120:123], a[40:43], v[40:43]\n" \
"ds_read_b128 a[136:139], v20 offset:10240\n" \
"ds_read_b128 a[140:143], v20 offset:11264\n" \
"v_mfma_f32_16x16x32_bf16 v[44:47], v[120:123], a[56:59], v[44:47]\n" \
"ds_read_b128 a[144:147], v20 offset:12288\n" \
"s_waitcnt lgkmcnt(15)\n" \
"v_mfma_f32_16x16x32_bf16 v[32:35], v[124:127], a[12:15], v[32:35]\n" \
"ds_read_b128 a[148:151], v20 offset:13312\n" \
"ds_read_b128 a[152:155], v20 offset:14336\n" \
"v_mfma_f32_16x16x32_bf16 v[36:39], v[124:127], a[28:31], v[36:39]\n" \
"ds_read_b128 a[156:159], v20 offset:15360\n" \
"s_waitcnt lgkmcnt(15)\n" \
"v_mfma_f32_16x16x32_bf16 v[40:43], v[124:127], a[44:47], v[40:43]\n" \
"ds_read_b128 a[64:67], v60 offset:16384\n" \
"s_mov_b32 s83, 0x20000\n" \
"v_mfma_f32_16x16x32_bf16 v[44:47], v[124:127], a[60:63], v[44:47]\n" \
"ds_read_b128 a[68:71], v60 offset:17408\n" \
"s_mov_b32 s100, 0x71800000\n" \
"v_mfma_f32_16x16x32_bf16 v[64:67], a[160:163], a[0:3], v[32:35]\n" \
"ds_read_b128 a[72:75], v60 offset:18432\n" \
"s_waitcnt lgkmcnt(15)\n" \
"v_mfma_f32_16x16x32_bf16 v[64:67], a[164:167], a[4:7], v[64:67]\n" \
"ds_read_b128 a[76:79], v60 offset:19456\n" \
"s_mov_b32 s63, 0x30040000\n" \
"v_mfma_f32_16x16x32_bf16 v[64:67], a[168:171], a[8:11], v[64:67]\n" \
"ds_read_b128 a[80:83], v60 offset:24576\n" \
"v_mov_b32_e32 v62, 0\n" \
"v_mfma_f32_16x16x32_bf16 v[64:67], a[172:175], a[12:15], v[64:67]\n" \
"ds_read_b128 a[84:87], v60 offset:25600\n" \
"s_waitcnt lgkmcnt(15)\n" \
"v_mfma_f32_16x16x32_bf16 v[68:71], a[188:191], a[12:15], v[32:35]\n" \
"ds_read_b128 a[88:91], v60 offset:26624\n" \
"v_accvgpr_write_b32 a212, v54\n" \
"v_mfma_f32_16x16x32_bf16 v[68:71], a[184:187], a[8:11], v[68:71]\n" \
"ds_read_b128 a[92:95], v60 offset:27648\n" \
"v_accvgpr_write_b32 a208, 0\n" \
"v_mfma_f32_16x16x32_bf16 v[68:71], a[180:183], a[4:7], v[68:71]\n" \
"ds_read_b128 a[96:99], v60 offset:0\n" \
"s_waitcnt lgkmcnt(15)\n" \
"v_mfma_f32_16x16x32_bf16 v[68:71], a[176:179], a[0:3], v[68:71]\n" \
"ds_read_b128 a[100:103], v60 offset:1024\n" \
"v_accvgpr_write_b32 a213, v54\n" \
"v_mfma_f32_16x16x32_bf16 v[72:75], a[160:163], a[16:19], v[36:39]\n" \
"ds_read_b128 a[104:107], v60 offset:2048\n" \
"v_accvgpr_write_b32 a209, 0\n" \
"v_mfma_f32_16x16x32_bf16 v[72:75], a[164:167], a[20:23], v[72:75]\n" \
"ds_read_b128 a[108:111], v60 offset:3072\n" \
"v_accvgpr_write_b32 a214, v54\n" \
"v_mfma_f32_16x16x32_bf16 v[72:75], a[168:171], a[24:27], v[72:75]\n" \
"ds_read_b128 a[112:115], v60 offset:8192\n" \
"v_accvgpr_write_b32 a210, 0\n" \
"v_mfma_f32_16x16x32_bf16 v[72:75], a[172:175], a[28:31], v[72:75]\n" \
"ds_read_b128 a[116:119], v60 offset:9216\n" \
"v_accvgpr_write_b32 a215, v54\n" \
"v_mfma_f32_16x16x32_bf16 v[76:79], a[188:191], a[28:31], v[36:39]\n" \
"ds_read_b128 a[120:123], v60 offset:10240\n" \
"L_pf10:\n" \
"s_cbranch_execz L_pf14\n" \
"s_nop 0\n" \
"v_accvgpr_write_b32 a211, 0\n" \
"v_mfma_f32_16x16x32_bf16 v[76:79], a[184:187], a[24:27], v[76:79]\n" \
"ds_read_b128 a[124:127], v60 offset:11264\n" \
"v_or_b32_e32 v21, 0x44444444, v21\n" \
"v_mfma_f32_16x16x32_bf16 v[76:79], a[180:183], a[20:23], v[76:79]\n" \
"v_exp_f32_e32 v64, v64\n" \
"v_mfma_f32_16x16x32_bf16 v[76:79], a[176:179], a[16:19], v[76:79]\n" \
"v_exp_f32_e32 v65, v65\n" \
"v_mfma_f32_16x16x32_bf16 v[80:83], a[160:163], a[32:35], v[40:43]\n" \
"v_exp_f32_e32 v66, v66\n" \
"v_mfma_f32_16x16x32_bf16 v[80:83], a[164:167], a[36:39], v[80:83]\n" \
"v_exp_f32_e32 v67, v67\n" \
"v_mfma_f32_16x16x32_bf16 v[80:83], a[168:171], a[40:43], v[80:83]\n" \
"v_exp_f32_e32 v68, v68\n" \
"v_mfma_f32_16x16x32_bf16 v[80:83], a[172:175], a[44:47], v[80:83]\n" \
"v_exp_f32_e32 v69, v69\n" \
"v_mfma_f32_16x16x32_bf16 v[84:87], a[188:191], a[44:47], v[40:43]\n" \
"v_exp_f32_e32 v70, v70\n" \
"v_mfma_f32_16x16x32_bf16 v[84:87], a[184:187], a[40:43], v[84:87]\n" \
"v_exp_f32_e32 v71, v71\n" \
"v_mfma_f32_16x16x32_bf16 v[84:87], a[180:183], a[36:39], v[84:87]\n" \
"v_exp_f32_e32 v72, v72\n" \
"v_mfma_f32_16x16x32_bf16 v[84:87], a[176:179], a[32:35], v[84:87]\n" \
"v_exp_f32_e32 v73, v73\n" \
"v_mfma_f32_16x16x32_bf16 v[88:91], a[160:163], a[48:51], v[44:47]\n" \
"v_exp_f32_e32 v74, v74\n" \
"v_mfma_f32_16x16x32_bf16 v[88:91], a[164:167], a[52:55], v[88:91]\n" \
"v_exp_f32_e32 v75, v75\n" \
"v_mfma_f32_16x16x32_bf16 v[88:91], a[168:171], a[56:59], v[88:91]\n" \
"v_exp_f32_e32 v76, v76\n" \
"v_mfma_f32_16x16x32_bf16 v[88:91], a[172:175], a[60:63], v[88:91]\n" \
"v_exp_f32_e32 v77, v77\n" \
"v_mfma_f32_16x16x32_bf16 v[92:95], a[188:191], a[60:63], v[44:47]\n" \
"v_exp_f32_e32 v78, v78\n" \
"v_mfma_f32_16x16x32_bf16 v[92:95], a[184:187], a[56:59], v[92:95]\n" \
"v_exp_f32_e32 v79, v79\n" \
"v_mfma_f32_16x16x32_bf16 v[92:95], a[180:183], a[52:55], v[92:95]\n" \
"v_exp_f32_e32 v80, v80\n" \
"v_mfma_f32_16x16x32_bf16 v[92:95], a[176:179], a[48:51], v[92:95]\n" \
"v_exp_f32_e32 v81, v81\n" \
"s_waitcnt vmcnt(2) lgkmcnt(0)\n" \
"s_barrier\n" \
"s_mov_b64 exec, 1\n" \
"buffer_store_dword v56, off, s[56:59], s68 offset:0 sc1\n" \
"buffer_store_dword v56, off, s[56:59], s22 sc1\n" \
"s_mov_b64 exec, -1\n" \
"v_exp_f32_e32 v82, v82\n" \
"v_exp_f32_e32 v83, v83\n" \
"v_exp_f32_e32 v84, v84\n" \
"v_exp_f32_e32 v85, v85\n" \
"v_exp_f32_e32 v86, v86\n" \
"v_exp_f32_e32 v87, v87\n" \
"v_accvgpr_write_b32 a252, v57\n" \
"v_accvgpr_write_b32 a253, v57\n" \
"v_accvgpr_write_b32 a254, v57\n" \
"v_accvgpr_write_b32 a255, v57\n" \
"s_mov_b32 s64, 0x8000\n" \
"v_add_u32_e32 v50, 0x6000, v55\n" \
"v_add_u32_e32 v51, 0xa000, v55\n" \
"v_add_u32_e32 v53, 0x10000, v55\n" \
"v_add_u32_e32 v48, 0xc000, v20\n" \
"v_add_u32_e32 v49, s17, v20\n" \
"v_accvgpr_write_b32 a224, v116\n" \
"v_accvgpr_write_b32 a225, v117\n" \
"v_accvgpr_write_b32 a226, v118\n" \
"v_accvgpr_write_b32 a227, v119\n" \
"v_accvgpr_write_b32 a228, v120\n" \
"v_accvgpr_write_b32 a229, v121\n" \
"v_accvgpr_write_b32 a230, v122\n" \
"v_accvgpr_write_b32 a231, v123\n" \
"v_accvgpr_write_b32 a232, v124\n" \
"v_accvgpr_write_b32 a233, v125\n" \
"v_accvgpr_write_b32 a234, v126\n" \
"v_accvgpr_write_b32 a235, v127\n" \
"v_exp_f32_e32 v88, v88\n" \
"v_exp_f32_e32 v89, v89\n" \
"v_exp_f32_e32 v90, v90\n" \
"v_exp_f32_e32 v91, v91\n" \
"v_cvt_pk_bf16_f32 v96, v64, v65\n" \
"v_cvt_pk_bf16_f32 v97, v66, v67\n" \
"v_cvt_pk_bf16_f32 v98, v68, v69\n" \
"v_cvt_pk_bf16_f32 v99, v70, v71\n" \
"v_cvt_pk_bf16_f32 v104, v72, v73\n" \
"v_cvt_pk_bf16_f32 v105, v74, v75\n" \
"v_cvt_pk_bf16_f32 v106, v76, v77\n" \
"v_cvt_pk_bf16_f32 v107, v78, v79\n" \
"v_cvt_pk_bf16_f32 v112, v80, v81\n" \
"v_cvt_pk_bf16_f32 v113, v82, v83\n" \
"v_cvt_pk_bf16_f32 v114, v84, v85\n" \
"v_cvt_pk_bf16_f32 v115, v86, v87\n" \
"s_sub_u32 s66, s18, 1\n" \
"s_cbranch_scc1 L_tr_%=\n" \
"s_branch L_1_%=\n" \
"L_t1_%=:\n" \
"v_mfma_f32_16x16x32_bf16 v[252:255], a[188:191], v[124:127], v[252:255]\n" \
"v_mfma_f32_16x16x32_bf16 v[252:255], a[172:175], v[120:123], v[252:255]\n" \
"v_smfmac_f32_16x16x64_bf16 a[204:207], a[212:215], v[120:127], v21\n" \
"s_waitcnt lgkmcnt(0)\n" \
"L_1_%=:\n" \
"v_mfma_f32_16x16x32_bf16 v[64:67], a[128:131], a[0:3], v[32:35]\n" \
"ds_read_b128 a[160:163], v49 offset:0\n" \
"ds_read_b128 a[176:179], v49 offset:4096\n" \
"v_mfma_f32_16x16x32_bf16 v[64:67], a[132:135], a[4:7], v[64:67]\n" \
"v_cvt_pk_bf16_f32 v120, v88, v89\n" \
"v_cvt_pk_bf16_f32 v121, v90, v91\n" \
"v_mfma_f32_16x16x32_bf16 v[64:67], a[136:139], a[8:11], v[64:67]\n" \
"ds_read_b128 a[180:183], v49 offset:5120\n" \
"ds_read_b128 a[164:167], v49 offset:1024\n" \
"v_mfma_f32_16x16x32_bf16 v[64:67], a[140:143], a[12:15], v[64:67]\n" \
"v_exp_f32_e32 v92, v92\n" \
"v_mfma_f32_16x16x32_bf16 v[68:71], a[156:159], a[12:15], v[32:35]\n" \
"ds_read_b128 a[168:171], v49 offset:2048\n" \
"ds_read_b128 a[184:187], v49 offset:6144\n" \
"v_mfma_f32_16x16x32_bf16 v[68:71], a[152:155], a[8:11], v[68:71]\n" \
"v_exp_f32_e32 v93, v93\n" \
"v_mfma_f32_16x16x32_bf16 v[68:71], a[148:151], a[4:7], v[68:71]\n" \
"ds_read_b128 a[188:191], v49 offset:7168\n" \
"ds_read_b128 a[172:175], v49 offset:3072\n" \
"v_mfma_f32_16x16x32_bf16 v[68:71], a[144:147], a[0:3], v[68:71]\n" \
"v_exp_f32_e32 v94, v94\n" \
"v_mfma_f32_16x16x32_bf16 v[72:75], a[128:131], a[16:19], v[36:39]\n" \
"v_exp_f32_e32 v95, v95\n" \
"v_mfma_f32_16x16x32_bf16 v[72:75], a[132:135], a[20:23], v[72:75]\n" \
"v_cvt_pk_bf16_f32 v122, v92, v93\n" \
"v_cvt_pk_bf16_f32 v123, v94, v95\n" \
"v_mfma_f32_16x16x32_bf16 v[72:75], a[136:139], a[24:27], v[72:75]\n" \
"v_exp_f32_e32 v64, v64\n" \
"v_mfma_f32_16x16x32_bf16 v[72:75], a[140:143], a[28:31], v[72:75]\n" \
"v_exp_f32_e32 v65, v65\n" \
"v_mfma_f32_16x16x32_bf16 v[76:79], a[156:159], a[28:31], v[36:39]\n" \
"v_exp_f32_e32 v66, v66\n" \
"v_mfma_f32_16x16x32_bf16 v[76:79], a[152:155], a[24:27], v[76:79]\n" \
"v_exp_f32_e32 v67, v67\n" \
"v_mfma_f32_16x16x32_bf16 v[76:79], a[148:151], a[20:23], v[76:79]\n" \
"v_exp_f32_e32 v68, v68\n" \
"v_mfma_f32_16x16x32_bf16 v[76:79], a[144:147], a[16:19], v[76:79]\n" \
"v_exp_f32_e32 v69, v69\n" \
"v_mfma_f32_16x16x32_bf16 v[80:83], a[128:131], a[32:35], v[40:43]\n" \
"v_exp_f32_e32 v70, v70\n" \
"v_mfma_f32_16x16x32_bf16 v[80:83], a[132:135], a[36:39], v[80:83]\n" \
"v_exp_f32_e32 v71, v71\n" \
"v_mfma_f32_16x16x32_bf16 v[80:83], a[136:139], a[40:43], v[80:83]\n" \
"v_cvt_pk_bf16_f32 v100, v64, v65\n" \
"v_cvt_pk_bf16_f32 v101, v66, v67\n" \
"v_mfma_f32_16x16x32_bf16 v[80:83], a[140:143], a[44:47], v[80:83]\n" \
"v_cvt_pk_bf16_f32 v102, v68, v69\n" \
"v_cvt_pk_bf16_f32 v103, v70, v71\n" \
"v_mfma_f32_16x16x32_bf16 v[84:87], a[156:159], a[44:47], v[40:43]\n" \
"v_exp_f32_e32 v72, v72\n" \
"v_mfma_f32_16x16x32_bf16 v[84:87], a[152:155], a[40:43], v[84:87]\n" \
"v_exp_f32_e32 v73, v73\n" \
"v_mfma_f32_16x16x32_bf16 v[84:87], a[148:151], a[36:39], v[84:87]\n" \
"v_exp_f32_e32 v74, v74\n" \
"v_mfma_f32_16x16x32_bf16 v[84:87], a[144:147], a[32:35], v[84:87]\n" \
"v_exp_f32_e32 v75, v75\n" \
"v_mfma_f32_16x16x32_bf16 v[88:91], a[128:131], a[48:51], v[44:47]\n" \
"v_exp_f32_e32 v76, v76\n" \
"v_mfma_f32_16x16x32_bf16 v[88:91], a[132:135], a[52:55], v[88:91]\n" \
"v_exp_f32_e32 v77, v77\n" \
"v_mfma_f32_16x16x32_bf16 v[88:91], a[136:139], a[56:59], v[88:91]\n" \
"v_exp_f32_e32 v78, v78\n" \
"v_mfma_f32_16x16x32_bf16 v[88:91], a[140:143], a[60:63], v[88:91]\n" \
"v_exp_f32_e32 v79, v79\n" \
"v_mfma_f32_16x16x32_bf16 v[92:95], a[156:159], a[60:63], v[44:47]\n" \
"v_cvt_pk_bf16_f32 v108, v72, v73\n" \
"v_cvt_pk_bf16_f32 v109, v74, v75\n" \
"v_mfma_f32_16x16x32_bf16 v[92:95], a[152:155], a[56:59], v[92:95]\n" \
"v_cvt_pk_bf16_f32 v110, v76, v77\n" \
"v_cvt_pk_bf16_f32 v111, v78, v79\n" \
"v_mfma_f32_16x16x32_bf16 v[92:95], a[148:151], a[52:55], v[92:95]\n" \
"ds_read_b128 a[128:131], v48 offset:0\n" \
"ds_read_b128 a[132:135], v48 offset:1024\n" \
"v_mfma_f32_16x16x32_bf16 v[92:95], a[144:147], a[48:51], v[92:95]\n" \
"s_waitcnt lgkmcnt(2)\n" \
"v_mfma_f32_16x16x32_bf16 v[128:131], a[160:163], v[96:99], v[128:131]\n" \
"ds_read_b128 a[136:139], v48 offset:2048\n" \
"ds_read_b128 a[140:143], v48 offset:3072\n" \
"v_mfma_f32_16x16x32_bf16 v[128:131], a[176:179], v[100:103], v[128:131]\n" \
"v_exp_f32_e32 v80, v80\n" \
"v_mfma_f32_16x16x32_bf16 v[132:135], a[180:183], v[100:103], v[132:135]\n" \
"ds_read_b128 a[144:147], v48 offset:4096\n" \
"ds_read_b128 a[148:151], v48 offset:5120\n" \
"v_mfma_f32_16x16x32_bf16 v[132:135], a[164:167], v[96:99], v[132:135]\n" \
"v_exp_f32_e32 v81, v81\n" \
"v_mfma_f32_16x16x32_bf16 v[136:139], a[168:171], v[96:99], v[136:139]\n" \
"ds_read_b128 a[152:155], v48 offset:6144\n" \
"ds_read_b128 a[156:159], v48 offset:7168\n" \
"v_mfma_f32_16x16x32_bf16 v[136:139], a[184:187], v[100:103], v[136:139]\n" \
"v_exp_f32_e32 v82, v82\n" \
"v_mfma_f32_16x16x32_bf16 v[140:143], a[188:191], v[100:103], v[140:143]\n" \
"v_exp_f32_e32 v83, v83\n" \
"v_mfma_f32_16x16x32_bf16 v[140:143], a[172:175], v[96:99], v[140:143]\n" \
"v_exp_f32_e32 v84, v84\n" \
"v_smfmac_f32_16x16x64_bf16 a[192:195], a[212:215], v[96:103], v21\n" \
"v_mfma_f32_16x16x32_bf16 v[160:163], a[160:163], v[104:107], v[160:163]\n" \
"v_exp_f32_e32 v85, v85\n" \
"v_mfma_f32_16x16x32_bf16 v[160:163], a[176:179], v[108:111], v[160:163]\n" \
"v_exp_f32_e32 v86, v86\n" \
"v_mfma_f32_16x16x32_bf16 v[164:167], a[180:183], v[108:111], v[164:167]\n" \
"v_exp_f32_e32 v87, v87\n" \
"v_mfma_f32_16x16x32_bf16 v[164:167], a[164:167], v[104:107], v[164:167]\n" \
"v_cvt_pk_bf16_f32 v116, v80, v81\n" \
"v_cvt_pk_bf16_f32 v117, v82, v83\n" \
"v_mfma_f32_16x16x32_bf16 v[168:171], a[168:171], v[104:107], v[168:171]\n" \
"v_cvt_pk_bf16_f32 v118, v84, v85\n" \
"v_cvt_pk_bf16_f32 v119, v86, v87\n" \
"v_mfma_f32_16x16x32_bf16 v[168:171], a[184:187], v[108:111], v[168:171]\n" \
"v_exp_f32_e32 v88, v88\n" \
"v_mfma_f32_16x16x32_bf16 v[172:175], a[188:191], v[108:111], v[172:175]\n" \
"v_exp_f32_e32 v89, v89\n" \
"v_mfma_f32_16x16x32_bf16 v[172:175], a[172:175], v[104:107], v[172:175]\n" \
"v_exp_f32_e32 v90, v90\n" \
"v_smfmac_f32_16x16x64_bf16 a[196:199], a[212:215], v[104:111], v21\n" \
"v_mfma_f32_16x16x32_bf16 v[192:195], a[160:163], v[112:115], v[192:195]\n" \
"v_exp_f32_e32 v91, v91\n" \
"v_mfma_f32_16x16x32_bf16 v[192:195], a[176:179], v[116:119], v[192:195]\n" \
"v_exp_f32_e32 v92, v92\n" \
"v_mfma_f32_16x16x32_bf16 v[196:199], a[180:183], v[116:119], v[196:199]\n" \
"v_exp_f32_e32 v93, v93\n" \
"v_mfma_f32_16x16x32_bf16 v[196:199], a[164:167], v[112:115], v[196:199]\n" \
"v_exp_f32_e32 v94, v94\n" \
"v_mfma_f32_16x16x32_bf16 v[200:203], a[168:171], v[112:115], v[200:203]\n" \
"v_exp_f32_e32 v95, v95\n" \
"v_mfma_f32_16x16x32_bf16 v[200:203], a[184:187], v[116:119], v[200:203]\n" \
"v_cvt_pk_bf16_f32 v124, v88, v89\n" \
"v_cvt_pk_bf16_f32 v125, v90, v91\n" \
"v_mfma_f32_16x16x32_bf16 v[204:207], a[188:191], v[116:119], v[204:207]\n" \
"v_cvt_pk_bf16_f32 v126, v92, v93\n" \
"v_cvt_pk_bf16_f32 v127, v94, v95\n" \
"v_mfma_f32_16x16x32_bf16 v[204:207], a[172:175], v[112:115], v[204:207]\n" \
"v_smfmac_f32_16x16x64_bf16 a[200:203], a[212:215], v[112:119], v21\n" \
"s_waitcnt vmcnt(0) lgkmcnt(0)\n" \
"s_barrier\n" \
"v_mfma_f32_16x16x32_bf16 v[224:227], a[160:163], v[120:123], v[224:227]\n" \
"s_add_u32 s63, s63, s63\n" \
"s_cbranch_scc1 L_h1_%=\n" \
"L_hr1_%=:\n" \
"v_mfma_f32_16x16x32_bf16 v[224:227], a[176:179], v[124:127], v[224:227]\n" \
"s_xor_b32 m0, s65, 0x12000\n" \
"ds_read_b128 a[160:163], v49 offset:8192\n" \
"v_mfma_f32_16x16x32_bf16 v[228:231], a[180:183], v[124:127], v[228:231]\n" \
"buffer_load_dwordx4 v50, s[8:11], s64 offen offset:0 sc1 lds\n" \
"v_mfma_f32_16x16x32_bf16 v[228:231], a[164:167], v[120:123], v[228:231]\n" \
"ds_read_b128 a[176:179], v49 offset:12288\n" \
"ds_read_b128 a[180:183], v49 offset:13312\n" \
"v_mfma_f32_16x16x32_bf16 v[232:235], a[168:171], v[120:123], v[232:235]\n" \
"v_mfma_f32_16x16x32_bf16 v[232:235], a[184:187], v[124:127], v[232:235]\n" \
"ds_read_b128 a[164:167], v49 offset:9216\n" \
"ds_read_b128 a[168:171], v49 offset:10240\n" \
"v_mfma_f32_16x16x32_bf16 v[236:239], a[188:191], v[124:127], v[236:239]\n" \
"v_mfma_f32_16x16x32_bf16 v[236:239], a[172:175], v[120:123], v[236:239]\n" \
"ds_read_b128 a[184:187], v49 offset:14336\n" \
"ds_read_b128 a[188:191], v49 offset:15360\n" \
"v_mfma_f32_16x16x32_bf16 v[64:67], a[128:131], a[0:3], v[32:35]\n" \
"v_mfma_f32_16x16x32_bf16 v[64:67], a[132:135], a[4:7], v[64:67]\n" \
"buffer_load_dwordx4 v50, s[8:11], s64 offen offset:1024 sc1 lds\n" \
"v_mfma_f32_16x16x32_bf16 v[64:67], a[136:139], a[8:11], v[64:67]\n" \
"s_xor_b32 m0, s65, 0x6000\n" \
"ds_read_b128 a[172:175], v49 offset:11264\n" \
"v_mfma_f32_16x16x32_bf16 v[64:67], a[140:143], a[12:15], v[64:67]\n" \
"v_add_u32_e32 v49, 0x10000, v48\n" \
"v_mfma_f32_16x16x32_bf16 v[68:71], a[156:159], a[12:15], v[32:35]\n" \
"v_mfma_f32_16x16x32_bf16 v[68:71], a[152:155], a[8:11], v[68:71]\n" \
"v_mfma_f32_16x16x32_bf16 v[68:71], a[148:151], a[4:7], v[68:71]\n" \
"v_mfma_f32_16x16x32_bf16 v[68:71], a[144:147], a[0:3], v[68:71]\n" \
"v_mfma_f32_16x16x32_bf16 v[72:75], a[128:131], a[16:19], v[36:39]\n" \
"v_mfma_f32_16x16x32_bf16 v[72:75], a[132:135], a[20:23], v[72:75]\n" \
"v_mfma_f32_16x16x32_bf16 v[72:75], a[136:139], a[24:27], v[72:75]\n" \
"buffer_load_dwordx4 v51, s[8:11], s64 offen offset:0 sc1 lds\n" \
"v_mfma_f32_16x16x32_bf16 v[72:75], a[140:143], a[28:31], v[72:75]\n" \
"v_exp_f32_e32 v64, v64\n" \
"v_mfma_f32_16x16x32_bf16 v[76:79], a[156:159], a[28:31], v[36:39]\n" \
"v_exp_f32_e32 v65, v65\n" \
"v_mfma_f32_16x16x32_bf16 v[76:79], a[152:155], a[24:27], v[76:79]\n" \
"v_exp_f32_e32 v66, v66\n" \
"v_mfma_f32_16x16x32_bf16 v[76:79], a[148:151], a[20:23], v[76:79]\n" \
"v_exp_f32_e32 v67, v67\n" \
"v_mfma_f32_16x16x32_bf16 v[76:79], a[144:147], a[16:19], v[76:79]\n" \
"v_exp_f32_e32 v68, v68\n" \
"v_mfma_f32_16x16x32_bf16 v[80:83], a[128:131], a[32:35], v[40:43]\n" \
"v_exp_f32_e32 v69, v69\n" \
"v_mfma_f32_16x16x32_bf16 v[80:83], a[132:135], a[36:39], v[80:83]\n" \
"v_exp_f32_e32 v70, v70\n" \
"v_mfma_f32_16x16x32_bf16 v[80:83], a[136:139], a[40:43], v[80:83]\n" \
"v_exp_f32_e32 v71, v71\n" \
"v_mfma_f32_16x16x32_bf16 v[80:83], a[140:143], a[44:47], v[80:83]\n" \
"buffer_load_dwordx4 v51, s[8:11], s64 offen offset:1024 sc1 lds\n" \
"v_mfma_f32_16x16x32_bf16 v[84:87], a[156:159], a[44:47], v[40:43]\n" \
"v_exp_f32_e32 v72, v72\n" \
"v_mfma_f32_16x16x32_bf16 v[84:87], a[152:155], a[40:43], v[84:87]\n" \
"v_exp_f32_e32 v73, v73\n" \
"v_mfma_f32_16x16x32_bf16 v[84:87], a[148:151], a[36:39], v[84:87]\n" \
"v_exp_f32_e32 v74, v74\n" \
"v_mfma_f32_16x16x32_bf16 v[84:87], a[144:147], a[32:35], v[84:87]\n" \
"v_exp_f32_e32 v75, v75\n" \
"v_mfma_f32_16x16x32_bf16 v[88:91], a[128:131], a[48:51], v[44:47]\n" \
"s_xor_b32 m0, s65, 0x14000\n" \
"ds_read_b128 a[128:131], v48 offset:8192\n" \
"v_mfma_f32_16x16x32_bf16 v[88:91], a[132:135], a[52:55], v[88:91]\n" \
"v_exp_f32_e32 v76, v76\n" \
"v_mfma_f32_16x16x32_bf16 v[88:91], a[136:139], a[56:59], v[88:91]\n" \
"ds_read_b128 a[132:135], v48 offset:9216\n" \
"ds_read_b128 a[136:139], v48 offset:10240\n" \
"v_mfma_f32_16x16x32_bf16 v[88:91], a[140:143], a[60:63], v[88:91]\n" \
"v_exp_f32_e32 v77, v77\n" \
"v_mfma_f32_16x16x32_bf16 v[92:95], a[156:159], a[60:63], v[44:47]\n" \
"buffer_load_dwordx4 v52, s[8:11], s64 offen offset:0 sc1 lds\n" \
"v_mfma_f32_16x16x32_bf16 v[92:95], a[152:155], a[56:59], v[92:95]\n" \
"ds_read_b128 a[140:143], v48 offset:11264\n" \
"ds_read_b128 a[152:155], v48 offset:14336\n" \
"v_mfma_f32_16x16x32_bf16 v[92:95], a[148:151], a[52:55], v[92:95]\n" \
"v_exp_f32_e32 v78, v78\n" \
"v_mfma_f32_16x16x32_bf16 v[92:95], a[144:147], a[48:51], v[92:95]\n" \
"ds_read_b128 a[144:147], v48 offset:12288\n" \
"s_waitcnt lgkmcnt(6)\n" \
"v_mfma_f32_16x16x32_bf16 v[144:147], a[160:163], v[96:99], v[144:147]\n" \
"ds_read_b128 a[148:151], v48 offset:13312\n" \
"ds_read_b128 a[156:159], v48 offset:15360\n" \
"v_mfma_f32_16x16x32_bf16 v[144:147], a[176:179], v[100:103], v[144:147]\n" \
"v_exp_f32_e32 v79, v79\n" \
"v_mfma_f32_16x16x32_bf16 v[148:151], a[180:183], v[100:103], v[148:151]\n" \
"v_exp_f32_e32 v80, v80\n" \
"v_mfma_f32_16x16x32_bf16 v[148:151], a[164:167], v[96:99], v[148:151]\n" \
"v_exp_f32_e32 v81, v81\n" \
"v_mfma_f32_16x16x32_bf16 v[152:155], a[168:171], v[96:99], v[152:155]\n" \
"buffer_load_dwordx4 v52, s[8:11], s64 offen offset:1024 sc1 lds\n" \
"v_mfma_f32_16x16x32_bf16 v[152:155], a[184:187], v[100:103], v[152:155]\n" \
"s_mov_b32 m0, s65\n" \
"s_xor_b32 s65, s65, 0x4000\n" \
"v_mfma_f32_16x16x32_bf16 v[156:159], a[188:191], v[100:103], v[156:159]\n" \
"v_exp_f32_e32 v82, v82\n" \
"v_mfma_f32_16x16x32_bf16 v[156:159], a[172:175], v[96:99], v[156:159]\n" \
"v_exp_f32_e32 v83, v83\n" \
"v_mfma_f32_16x16x32_bf16 v[176:179], a[160:163], v[104:107], v[176:179]\n" \
"v_exp_f32_e32 v84, v84\n" \
"v_mfma_f32_16x16x32_bf16 v[176:179], a[176:179], v[108:111], v[176:179]\n" \
"v_exp_f32_e32 v85, v85\n" \
"v_mfma_f32_16x16x32_bf16 v[180:183], a[180:183], v[108:111], v[180:183]\n" \
"v_exp_f32_e32 v86, v86\n" \
"v_mfma_f32_16x16x32_bf16 v[180:183], a[164:167], v[104:107], v[180:183]\n" \
"v_exp_f32_e32 v87, v87\n" \
"v_mfma_f32_16x16x32_bf16 v[184:187], a[168:171], v[104:107], v[184:187]\n" \
"v_exp_f32_e32 v88, v88\n" \
"v_mfma_f32_16x16x32_bf16 v[184:187], a[184:187], v[108:111], v[184:187]\n" \
"buffer_load_dwordx4 v53, s[8:11], s64 offen offset:0 sc1 lds\n" \
"v_mfma_f32_16x16x32_bf16 v[188:191], a[188:191], v[108:111], v[188:191]\n" \
"v_exp_f32_e32 v89, v89\n" \
"v_mfma_f32_16x16x32_bf16 v[188:191], a[172:175], v[104:107], v[188:191]\n" \
"v_exp_f32_e32 v90, v90\n" \
"v_mfma_f32_16x16x32_bf16 v[208:211], a[160:163], v[112:115], v[208:211]\n" \
"v_exp_f32_e32 v91, v91\n" \
"v_mfma_f32_16x16x32_bf16 v[208:211], a[176:179], v[116:119], v[208:211]\n" \
"v_cvt_pk_bf16_f32 v96, v64, v65\n" \
"v_cvt_pk_bf16_f32 v97, v66, v67\n" \
"v_mfma_f32_16x16x32_bf16 v[212:215], a[180:183], v[116:119], v[212:215]\n" \
"v_cvt_pk_bf16_f32 v98, v68, v69\n" \
"v_cvt_pk_bf16_f32 v99, v70, v71\n" \
"v_mfma_f32_16x16x32_bf16 v[212:215], a[164:167], v[112:115], v[212:215]\n" \
"v_cvt_pk_bf16_f32 v104, v72, v73\n" \
"v_cvt_pk_bf16_f32 v105, v74, v75\n" \
"v_mfma_f32_16x16x32_bf16 v[216:219], a[168:171], v[112:115], v[216:219]\n" \
"v_cvt_pk_bf16_f32 v106, v76, v77\n" \
"v_cvt_pk_bf16_f32 v107, v78, v79\n" \
"v_mfma_f32_16x16x32_bf16 v[216:219], a[184:187], v[116:119], v[216:219]\n" \
"v_xor_b32_e32 v48, 0x4000, v48\n" \
"v_mfma_f32_16x16x32_bf16 v[220:223], a[188:191], v[116:119], v[220:223]\n" \
"buffer_load_dwordx4 v53, s[8:11], s64 offen offset:1024 sc1 lds\n" \
"v_mfma_f32_16x16x32_bf16 v[220:223], a[172:175], v[112:115], v[220:223]\n" \
"s_add_u32 s64, s64, 0x8000\n" \
"s_sub_u32 s66, s66, 1\n" \
"v_mfma_f32_16x16x32_bf16 v[240:243], a[160:163], v[120:123], v[240:243]\n" \
"v_cvt_pk_bf16_f32 v112, v80, v81\n" \
"v_cvt_pk_bf16_f32 v113, v82, v83\n" \
"v_mfma_f32_16x16x32_bf16 v[240:243], a[176:179], v[124:127], v[240:243]\n" \
"v_cvt_pk_bf16_f32 v114, v84, v85\n" \
"v_cvt_pk_bf16_f32 v115, v86, v87\n" \
"v_mfma_f32_16x16x32_bf16 v[244:247], a[180:183], v[124:127], v[244:247]\n" \
"v_mfma_f32_16x16x32_bf16 v[244:247], a[164:167], v[120:123], v[244:247]\n" \
"v_mfma_f32_16x16x32_bf16 v[248:251], a[168:171], v[120:123], v[248:251]\n" \
"v_mfma_f32_16x16x32_bf16 v[248:251], a[184:187], v[124:127], v[248:251]\n" \
"s_cbranch_scc0 L_t1_%=\n" \
"v_mfma_f32_16x16x32_bf16 v[252:255], a[188:191], v[124:127], v[252:255]\n" \
"v_mfma_f32_16x16x32_bf16 v[252:255], a[172:175], v[120:123], v[252:255]\n" \
"v_smfmac_f32_16x16x64_bf16 a[204:207], a[212:215], v[120:127], v21\n" \
"s_waitcnt lgkmcnt(0)\n" \
"s_barrier\n" \
"L_tr_%=:\n" \
"L_pf11:\n" \
"s_cbranch_execz L_pf15\n" \
"s_nop 0\n" \
"s_cmp_lg_u32 s16, 0\n" \
"s_cbranch_scc1 L_sw_%=\n" \
"v_mfma_f32_16x16x32_bf16 v[64:67], a[128:131], a[0:3], v[32:35]\n" \
"s_xor_b32 m0, s65, 0x12000\n" \
"s_mov_b32 s65, s13\n" \
"v_mfma_f32_16x16x32_bf16 v[64:67], a[132:135], a[4:7], v[64:67]\n" \
"buffer_load_dwordx4 v50, s[8:11], s64 offen offset:0 sc1 lds\n" \
"v_mfma_f32_16x16x32_bf16 v[64:67], a[136:139], a[8:11], v[64:67]\n" \
"v_add_u32_e32 v63, s15, v20\n" \
"s_add_u32 s67, s14, 40960\n" \
"v_mfma_f32_16x16x32_bf16 v[64:67], a[140:143], a[12:15], v[64:67]\n" \
"ds_read_b128 a[160:163], v49 offset:0\n" \
"ds_read_b128 a[176:179], v49 offset:4096\n" \
"v_mfma_f32_16x16x32_bf16 v[68:71], a[156:159], a[12:15], v[32:35]\n" \
"buffer_load_dwordx4 v50, s[8:11], s64 offen offset:1024 sc1 lds\n" \
"v_mfma_f32_16x16x32_bf16 v[68:71], a[152:155], a[8:11], v[68:71]\n" \
"v_add_u32_e32 v50, 0x6000, v63\n" \
"s_xor_b32 m0, s65, 0x8000\n" \
"v_mfma_f32_16x16x32_bf16 v[68:71], a[148:151], a[4:7], v[68:71]\n" \
"ds_read_b128 a[180:183], v49 offset:5120\n" \
"ds_read_b128 a[164:167], v49 offset:1024\n" \
"v_mfma_f32_16x16x32_bf16 v[68:71], a[144:147], a[0:3], v[68:71]\n" \
"ds_read_b128 a[168:171], v49 offset:2048\n" \
"ds_read_b128 a[184:187], v49 offset:6144\n" \
"v_mfma_f32_16x16x32_bf16 v[72:75], a[128:131], a[16:19], v[36:39]\n" \
"v_cvt_pk_bf16_f32 v120, v88, v89\n" \
"v_cvt_pk_bf16_f32 v121, v90, v91\n" \
"v_mfma_f32_16x16x32_bf16 v[72:75], a[132:135], a[20:23], v[72:75]\n" \
"buffer_load_dwordx4 v50, s[8:11], s67 offen offset:0 sc1 lds\n" \
"v_mfma_f32_16x16x32_bf16 v[72:75], a[136:139], a[24:27], v[72:75]\n" \
"ds_read_b128 a[188:191], v49 offset:7168\n" \
"ds_read_b128 a[172:175], v49 offset:3072\n" \
"v_mfma_f32_16x16x32_bf16 v[72:75], a[140:143], a[28:31], v[72:75]\n" \
"v_exp_f32_e32 v92, v92\n" \
"v_mfma_f32_16x16x32_bf16 v[76:79], a[156:159], a[28:31], v[36:39]\n" \
"v_exp_f32_e32 v93, v93\n" \
"v_mfma_f32_16x16x32_bf16 v[76:79], a[152:155], a[24:27], v[76:79]\n" \
"buffer_load_dwordx4 v50, s[8:11], s67 offen offset:1024 sc1 lds\n" \
"v_mfma_f32_16x16x32_bf16 v[76:79], a[148:151], a[20:23], v[76:79]\n" \
"v_exp_f32_e32 v94, v94\n" \
"v_mfma_f32_16x16x32_bf16 v[76:79], a[144:147], a[16:19], v[76:79]\n" \
"v_exp_f32_e32 v95, v95\n" \
"v_mfma_f32_16x16x32_bf16 v[80:83], a[128:131], a[32:35], v[40:43]\n" \
"v_cvt_pk_bf16_f32 v122, v92, v93\n" \
"v_cvt_pk_bf16_f32 v123, v94, v95\n" \
"v_mfma_f32_16x16x32_bf16 v[80:83], a[132:135], a[36:39], v[80:83]\n" \
"v_exp_f32_e32 v64, v64\n" \
"v_mfma_f32_16x16x32_bf16 v[80:83], a[136:139], a[40:43], v[80:83]\n" \
"buffer_load_dwordx4 v50, s[8:11], s67 offen offset:2048 sc1 lds\n" \
"v_mfma_f32_16x16x32_bf16 v[80:83], a[140:143], a[44:47], v[80:83]\n" \
"v_exp_f32_e32 v65, v65\n" \
"v_mfma_f32_16x16x32_bf16 v[84:87], a[156:159], a[44:47], v[40:43]\n" \
"v_exp_f32_e32 v66, v66\n" \
"v_mfma_f32_16x16x32_bf16 v[84:87], a[152:155], a[40:43], v[84:87]\n" \
"v_exp_f32_e32 v67, v67\n" \
"v_mfma_f32_16x16x32_bf16 v[84:87], a[148:151], a[36:39], v[84:87]\n" \
"buffer_load_dwordx4 v50, s[8:11], s67 offen offset:3072 sc1 lds\n" \
"v_mfma_f32_16x16x32_bf16 v[84:87], a[144:147], a[32:35], v[84:87]\n" \
"v_exp_f32_e32 v68, v68\n" \
"v_mfma_f32_16x16x32_bf16 v[88:91], a[128:131], a[48:51], v[44:47]\n" \
"v_exp_f32_e32 v69, v69\n" \
"v_mfma_f32_16x16x32_bf16 v[88:91], a[132:135], a[52:55], v[88:91]\n" \
"v_exp_f32_e32 v70, v70\n" \
"v_mfma_f32_16x16x32_bf16 v[88:91], a[136:139], a[56:59], v[88:91]\n" \
"v_exp_f32_e32 v71, v71\n" \
"v_mfma_f32_16x16x32_bf16 v[88:91], a[140:143], a[60:63], v[88:91]\n" \
"v_cvt_pk_bf16_f32 v100, v64, v65\n" \
"v_cvt_pk_bf16_f32 v101, v66, v67\n" \
"v_mfma_f32_16x16x32_bf16 v[92:95], a[156:159], a[60:63], v[44:47]\n" \
"v_cvt_pk_bf16_f32 v102, v68, v69\n" \
"v_cvt_pk_bf16_f32 v103, v70, v71\n" \
"v_mfma_f32_16x16x32_bf16 v[92:95], a[152:155], a[56:59], v[92:95]\n" \
"s_mov_b32 s64, s14\n" \
"v_add_u32_e32 v51, 0x12000, v63\n" \
"v_mfma_f32_16x16x32_bf16 v[92:95], a[148:151], a[52:55], v[92:95]\n" \
"s_xor_b32 m0, s65, 0xa000\n" \
"v_readlane_b32 s93, v11, 17\n" \
"v_mfma_f32_16x16x32_bf16 v[92:95], a[144:147], a[48:51], v[92:95]\n" \
"v_readlane_b32 s92, v11, 22\n" \
"s_waitcnt lgkmcnt(0)\n" \
"v_mfma_f32_16x16x32_bf16 v[128:131], a[160:163], v[96:99], v[128:131]\n" \
"buffer_load_dwordx4 v51, s[8:11], s64 offen offset:0 sc1 lds\n" \
"v_mfma_f32_16x16x32_bf16 v[128:131], a[176:179], v[100:103], v[128:131]\n" \
"buffer_load_dwordx4 v51, s[8:11], s64 offen offset:1024 sc1 lds\n" \
"v_mfma_f32_16x16x32_bf16 v[132:135], a[180:183], v[100:103], v[132:135]\n" \
"v_exp_f32_e32 v72, v72\n" \
"v_mfma_f32_16x16x32_bf16 v[132:135], a[164:167], v[96:99], v[132:135]\n" \
"v_exp_f32_e32 v73, v73\n" \
"v_mfma_f32_16x16x32_bf16 v[136:139], a[168:171], v[96:99], v[136:139]\n" \
"v_exp_f32_e32 v74, v74\n" \
"v_mfma_f32_16x16x32_bf16 v[136:139], a[184:187], v[100:103], v[136:139]\n" \
"v_exp_f32_e32 v75, v75\n" \
"v_mfma_f32_16x16x32_bf16 v[140:143], a[188:191], v[100:103], v[140:143]\n" \
"v_exp_f32_e32 v76, v76\n" \
"v_mfma_f32_16x16x32_bf16 v[140:143], a[172:175], v[96:99], v[140:143]\n" \
"v_exp_f32_e32 v77, v77\n" \
"v_smfmac_f32_16x16x64_bf16 a[192:195], a[212:215], v[96:103], v21\n" \
"v_mfma_f32_16x16x32_bf16 v[160:163], a[160:163], v[104:107], v[160:163]\n" \
"v_exp_f32_e32 v78, v78\n" \
"v_exp_f32_e32 v79, v79\n" \
"v_cvt_pk_bf16_f32 v108, v72, v73\n" \
"v_cvt_pk_bf16_f32 v109, v74, v75\n" \
"v_cvt_pk_bf16_f32 v110, v76, v77\n" \
"v_cvt_pk_bf16_f32 v111, v78, v79\n" \
"v_exp_f32_e32 v80, v80\n" \
"v_exp_f32_e32 v81, v81\n" \
"v_mfma_f32_16x16x32_bf16 v[160:163], a[176:179], v[108:111], v[160:163]\n" \
"buffer_load_dwordx4 v51, s[8:11], s64 offen offset:2048 sc1 lds\n" \
"v_mfma_f32_16x16x32_bf16 v[164:167], a[180:183], v[108:111], v[164:167]\n" \
"buffer_load_dwordx4 v51, s[8:11], s64 offen offset:3072 sc1 lds\n" \
"v_mfma_f32_16x16x32_bf16 v[164:167], a[164:167], v[104:107], v[164:167]\n" \
"v_exp_f32_e32 v82, v82\n" \
"v_mfma_f32_16x16x32_bf16 v[168:171], a[168:171], v[104:107], v[168:171]\n" \
"v_exp_f32_e32 v83, v83\n" \
"v_mfma_f32_16x16x32_bf16 v[168:171], a[184:187], v[108:111], v[168:171]\n" \
"v_exp_f32_e32 v84, v84\n" \
"v_mfma_f32_16x16x32_bf16 v[172:175], a[188:191], v[108:111], v[172:175]\n" \
"v_exp_f32_e32 v85, v85\n" \
"v_mfma_f32_16x16x32_bf16 v[172:175], a[172:175], v[104:107], v[172:175]\n" \
"v_exp_f32_e32 v86, v86\n" \
"v_smfmac_f32_16x16x64_bf16 a[196:199], a[212:215], v[104:111], v21\n" \
"v_mfma_f32_16x16x32_bf16 v[192:195], a[160:163], v[112:115], v[192:195]\n" \
"v_exp_f32_e32 v87, v87\n" \
"v_cvt_pk_bf16_f32 v116, v80, v81\n" \
"v_cvt_pk_bf16_f32 v117, v82, v83\n" \
"v_cvt_pk_bf16_f32 v118, v84, v85\n" \
"v_cvt_pk_bf16_f32 v119, v86, v87\n" \
"s_add_u32 m0, s93, 0x0\n" \
"v_exp_f32_e32 v88, v88\n" \
"v_mfma_f32_16x16x32_bf16 v[192:195], a[176:179], v[116:119], v[192:195]\n" \
"buffer_load_dwordx4 v0, s[80:83], s92 offen offset:0 lds\n" \
"v_mfma_f32_16x16x32_bf16 v[196:199], a[180:183], v[116:119], v[196:199]\n" \
"ds_read_b128 a[128:131], v48 offset:0\n" \
"ds_read_b128 a[132:135], v48 offset:1024\n" \
"v_mfma_f32_16x16x32_bf16 v[196:199], a[164:167], v[112:115], v[196:199]\n" \
"ds_read_b128 a[136:139], v48 offset:2048\n" \
"ds_read_b128 a[140:143], v48 offset:3072\n" \
"ds_read_b128 a[144:147], v48 offset:4096\n" \
"ds_read_b128 a[148:151], v48 offset:5120\n" \
"ds_read_b128 a[152:155], v48 offset:6144\n" \
"ds_read_b128 a[156:159], v48 offset:7168\n" \
"v_mfma_f32_16x16x32_bf16 v[200:203], a[168:171], v[112:115], v[200:203]\n" \
"buffer_load_dwordx4 v1, s[80:83], s92 offen offset:1024 lds\n" \
"v_mfma_f32_16x16x32_bf16 v[200:203], a[184:187], v[116:119], v[200:203]\n" \
"v_exp_f32_e32 v89, v89\n" \
"v_mfma_f32_16x16x32_bf16 v[204:207], a[188:191], v[116:119], v[204:207]\n" \
"v_exp_f32_e32 v90, v90\n" \
"v_mfma_f32_16x16x32_bf16 v[204:207], a[172:175], v[112:115], v[204:207]\n" \
"v_exp_f32_e32 v91, v91\n" \
"v_smfmac_f32_16x16x64_bf16 a[200:203], a[212:215], v[112:119], v21\n" \
"v_mfma_f32_16x16x32_bf16 v[224:227], a[160:163], v[120:123], v[224:227]\n" \
"v_exp_f32_e32 v92, v92\n" \
"v_exp_f32_e32 v93, v93\n" \
"v_exp_f32_e32 v94, v94\n" \
"v_exp_f32_e32 v95, v95\n" \
"v_cvt_pk_bf16_f32 v124, v88, v89\n" \
"v_cvt_pk_bf16_f32 v125, v90, v91\n" \
"v_cvt_pk_bf16_f32 v126, v92, v93\n" \
"v_cvt_pk_bf16_f32 v127, v94, v95\n" \
"v_add_u32_e32 v52, 0x14000, v63\n" \
"v_add_u32_e32 v53, 0x20000, v63\n" \
"v_mfma_f32_16x16x32_bf16 v[224:227], a[176:179], v[124:127], v[224:227]\n" \
"buffer_load_dwordx4 v2, s[80:83], s92 offen offset:2048 lds\n" \
"v_mfma_f32_16x16x32_bf16 v[228:231], a[180:183], v[124:127], v[228:231]\n" \
"buffer_load_dwordx4 v3, s[80:83], s92 offen offset:3072 lds\n" \
"v_mfma_f32_16x16x32_bf16 v[228:231], a[164:167], v[120:123], v[228:231]\n" \
"s_mov_b32 s66, s19\n" \
"v_mfma_f32_16x16x32_bf16 v[232:235], a[168:171], v[120:123], v[232:235]\n" \
"v_mfma_f32_16x16x32_bf16 v[232:235], a[184:187], v[124:127], v[232:235]\n" \
"v_mfma_f32_16x16x32_bf16 v[236:239], a[188:191], v[124:127], v[236:239]\n" \
"v_mfma_f32_16x16x32_bf16 v[236:239], a[172:175], v[120:123], v[236:239]\n" \
"s_waitcnt vmcnt(14) lgkmcnt(0)\n" \
"s_barrier\n" \
"v_mfma_f32_16x16x32_bf16 v[64:67], a[128:131], a[0:3], v[32:35]\n" \
"s_xor_b32 m0, s65, 0x18000\n" \
"ds_read_b128 a[160:163], v49 offset:8192\n" \
"v_mfma_f32_16x16x32_bf16 v[64:67], a[132:135], a[4:7], v[64:67]\n" \
"buffer_load_dwordx4 v52, s[8:11], s64 offen offset:0 sc1 lds\n" \
"v_mfma_f32_16x16x32_bf16 v[64:67], a[136:139], a[8:11], v[64:67]\n" \
"ds_read_b128 a[176:179], v49 offset:12288\n" \
"ds_read_b128 a[180:183], v49 offset:13312\n" \
"v_mfma_f32_16x16x32_bf16 v[64:67], a[140:143], a[12:15], v[64:67]\n" \
"ds_read_b128 a[164:167], v49 offset:9216\n" \
"ds_read_b128 a[168:171], v49 offset:10240\n" \
"v_mfma_f32_16x16x32_bf16 v[68:71], a[156:159], a[12:15], v[32:35]\n" \
"buffer_load_dwordx4 v52, s[8:11], s64 offen offset:1024 sc1 lds\n" \
"v_mfma_f32_16x16x32_bf16 v[68:71], a[152:155], a[8:11], v[68:71]\n" \
"ds_read_b128 a[184:187], v49 offset:14336\n" \
"ds_read_b128 a[188:191], v49 offset:15360\n" \
"v_mfma_f32_16x16x32_bf16 v[68:71], a[148:151], a[4:7], v[68:71]\n" \
"v_exp_f32_e32 v64, v64\n" \
"v_mfma_f32_16x16x32_bf16 v[68:71], a[144:147], a[0:3], v[68:71]\n" \
"v_exp_f32_e32 v65, v65\n" \
"v_mfma_f32_16x16x32_bf16 v[72:75], a[128:131], a[16:19], v[36:39]\n" \
"v_exp_f32_e32 v66, v66\n" \
"v_mfma_f32_16x16x32_bf16 v[72:75], a[132:135], a[20:23], v[72:75]\n" \
"buffer_load_dwordx4 v52, s[8:11], s64 offen offset:2048 sc1 lds\n" \
"v_mfma_f32_16x16x32_bf16 v[72:75], a[136:139], a[24:27], v[72:75]\n" \
"v_exp_f32_e32 v67, v67\n" \
"v_mfma_f32_16x16x32_bf16 v[72:75], a[140:143], a[28:31], v[72:75]\n" \
"v_exp_f32_e32 v68, v68\n" \
"v_mfma_f32_16x16x32_bf16 v[76:79], a[156:159], a[28:31], v[36:39]\n" \
"v_exp_f32_e32 v69, v69\n" \
"v_mfma_f32_16x16x32_bf16 v[76:79], a[152:155], a[24:27], v[76:79]\n" \
"buffer_load_dwordx4 v52, s[8:11], s64 offen offset:3072 sc1 lds\n" \
"v_mfma_f32_16x16x32_bf16 v[76:79], a[148:151], a[20:23], v[76:79]\n" \
"ds_read_b128 a[172:175], v49 offset:11264\n" \
"s_mov_b32 m0, s65\n" \
"v_mfma_f32_16x16x32_bf16 v[76:79], a[144:147], a[16:19], v[76:79]\n" \
"L_pf12:\n" \
"s_cbranch_execz L_pf16\n" \
"s_nop 0\n" \
"v_exp_f32_e32 v70, v70\n" \
"v_mfma_f32_16x16x32_bf16 v[80:83], a[128:131], a[32:35], v[40:43]\n" \
"v_exp_f32_e32 v71, v71\n" \
"v_mfma_f32_16x16x32_bf16 v[80:83], a[132:135], a[36:39], v[80:83]\n" \
"v_exp_f32_e32 v72, v72\n" \
"v_mfma_f32_16x16x32_bf16 v[80:83], a[136:139], a[40:43], v[80:83]\n" \
"buffer_load_dwordx4 v53, s[8:11], s64 offen offset:0 sc1 lds\n" \
"v_mfma_f32_16x16x32_bf16 v[80:83], a[140:143], a[44:47], v[80:83]\n" \
"v_exp_f32_e32 v73, v73\n" \
"v_mfma_f32_16x16x32_bf16 v[84:87], a[156:159], a[44:47], v[40:43]\n" \
"v_exp_f32_e32 v74, v74\n" \
"v_mfma_f32_16x16x32_bf16 v[84:87], a[152:155], a[40:43], v[84:87]\n" \
"v_exp_f32_e32 v75, v75\n" \
"v_mfma_f32_16x16x32_bf16 v[84:87], a[148:151], a[36:39], v[84:87]\n" \
"v_exp_f32_e32 v76, v76\n" \
"v_mfma_f32_16x16x32_bf16 v[84:87], a[144:147], a[32:35], v[84:87]\n" \
"buffer_load_dwordx4 v53, s[8:11], s64 offen offset:1024 sc1 lds\n" \
"v_mfma_f32_16x16x32_bf16 v[88:91], a[128:131], a[48:51], v[44:47]\n" \
"ds_read_b128 a[128:131], v48 offset:8192\n" \
"v_add_u32_e32 v49, 0x10000, v48\n" \
"v_mfma_f32_16x16x32_bf16 v[88:91], a[132:135], a[52:55], v[88:91]\n" \
"ds_read_b128 a[132:135], v48 offset:9216\n" \
"s_xor_b32 s65, s65, 0x8000\n" \
"v_mfma_f32_16x16x32_bf16 v[88:91], a[136:139], a[56:59], v[88:91]\n" \
"v_exp_f32_e32 v77, v77\n" \
"v_mfma_f32_16x16x32_bf16 v[88:91], a[140:143], a[60:63], v[88:91]\n" \
"buffer_load_dwordx4 v53, s[8:11], s64 offen offset:2048 sc1 lds\n" \
"v_mfma_f32_16x16x32_bf16 v[92:95], a[156:159], a[60:63], v[44:47]\n" \
"ds_read_b128 a[136:139], v48 offset:10240\n" \
"ds_read_b128 a[140:143], v48 offset:11264\n" \
"v_mfma_f32_16x16x32_bf16 v[92:95], a[152:155], a[56:59], v[92:95]\n" \
"ds_read_b128 a[152:155], v48 offset:14336\n" \
"ds_read_b128 a[156:159], v48 offset:15360\n" \
"v_mfma_f32_16x16x32_bf16 v[92:95], a[148:151], a[52:55], v[92:95]\n" \
"v_exp_f32_e32 v78, v78\n" \
"v_mfma_f32_16x16x32_bf16 v[92:95], a[144:147], a[48:51], v[92:95]\n" \
"ds_read_b128 a[144:147], v48 offset:12288\n" \
"s_waitcnt lgkmcnt(7)\n" \
"v_mfma_f32_16x16x32_bf16 v[144:147], a[160:163], v[96:99], v[144:147]\n" \
"buffer_load_dwordx4 v53, s[8:11], s64 offen offset:3072 sc1 lds\n" \
"v_mfma_f32_16x16x32_bf16 v[144:147], a[176:179], v[100:103], v[144:147]\n" \
"s_add_u32 m0, s93, 0x1000\n" \
"ds_read_b128 a[148:151], v48 offset:13312\n" \
"v_mfma_f32_16x16x32_bf16 v[148:151], a[180:183], v[100:103], v[148:151]\n" \
"v_exp_f32_e32 v79, v79\n" \
"v_mfma_f32_16x16x32_bf16 v[148:151], a[164:167], v[96:99], v[148:151]\n" \
"v_exp_f32_e32 v80, v80\n" \
"v_mfma_f32_16x16x32_bf16 v[152:155], a[168:171], v[96:99], v[152:155]\n" \
"buffer_load_dwordx4 v4, s[80:83], s92 offen offset:0 lds\n" \
"v_mfma_f32_16x16x32_bf16 v[152:155], a[184:187], v[100:103], v[152:155]\n" \
"v_exp_f32_e32 v81, v81\n" \
"v_mfma_f32_16x16x32_bf16 v[156:159], a[188:191], v[100:103], v[156:159]\n" \
"v_exp_f32_e32 v82, v82\n" \
"v_mfma_f32_16x16x32_bf16 v[156:159], a[172:175], v[96:99], v[156:159]\n" \
"v_exp_f32_e32 v83, v83\n" \
"v_mfma_f32_16x16x32_bf16 v[176:179], a[160:163], v[104:107], v[176:179]\n" \
"v_exp_f32_e32 v84, v84\n" \
"v_mfma_f32_16x16x32_bf16 v[176:179], a[176:179], v[108:111], v[176:179]\n" \
"buffer_load_dwordx4 v5, s[80:83], s92 offen offset:1024 lds\n" \
"v_mfma_f32_16x16x32_bf16 v[180:183], a[180:183], v[108:111], v[180:183]\n" \
"v_exp_f32_e32 v85, v85\n" \
"v_mfma_f32_16x16x32_bf16 v[180:183], a[164:167], v[104:107], v[180:183]\n" \
"v_exp_f32_e32 v86, v86\n" \
"v_mfma_f32_16x16x32_bf16 v[184:187], a[168:171], v[104:107], v[184:187]\n" \
"v_exp_f32_e32 v87, v87\n" \
"v_mfma_f32_16x16x32_bf16 v[184:187], a[184:187], v[108:111], v[184:187]\n" \
"v_exp_f32_e32 v88, v88\n" \
"v_mfma_f32_16x16x32_bf16 v[188:191], a[188:191], v[108:111], v[188:191]\n" \
"buffer_load_dwordx4 v6, s[80:83], s92 offen offset:2048 lds\n" \
"v_mfma_f32_16x16x32_bf16 v[188:191], a[172:175], v[104:107], v[188:191]\n" \
"v_exp_f32_e32 v89, v89\n" \
"v_mfma_f32_16x16x32_bf16 v[208:211], a[160:163], v[112:115], v[208:211]\n" \
"v_exp_f32_e32 v90, v90\n" \
"v_mfma_f32_16x16x32_bf16 v[208:211], a[176:179], v[116:119], v[208:211]\n" \
"v_exp_f32_e32 v91, v91\n" \
"v_mfma_f32_16x16x32_bf16 v[212:215], a[180:183], v[116:119], v[212:215]\n" \
"buffer_load_dwordx4 v7, s[80:83], s92 offen offset:3072 lds\n" \
"v_mfma_f32_16x16x32_bf16 v[212:215], a[164:167], v[112:115], v[212:215]\n" \
"s_add_u32 m0, s93, 0x2000\n" \
"v_cvt_pk_bf16_f32 v96, v64, v65\n" \
"v_mfma_f32_16x16x32_bf16 v[216:219], a[168:171], v[112:115], v[216:219]\n" \
"v_cvt_pk_bf16_f32 v97, v66, v67\n" \
"v_cvt_pk_bf16_f32 v98, v68, v69\n" \
"v_mfma_f32_16x16x32_bf16 v[216:219], a[184:187], v[116:119], v[216:219]\n" \
"v_cvt_pk_bf16_f32 v99, v70, v71\n" \
"v_cvt_pk_bf16_f32 v104, v72, v73\n" \
"v_mfma_f32_16x16x32_bf16 v[220:223], a[188:191], v[116:119], v[220:223]\n" \
"v_cvt_pk_bf16_f32 v105, v74, v75\n" \
"v_cvt_pk_bf16_f32 v106, v76, v77\n" \
"v_mfma_f32_16x16x32_bf16 v[220:223], a[172:175], v[112:115], v[220:223]\n" \
"buffer_load_dwordx4 v8, s[80:83], s92 offen offset:0 lds\n" \
"v_mfma_f32_16x16x32_bf16 v[240:243], a[160:163], v[120:123], v[240:243]\n" \
"v_cvt_pk_bf16_f32 v107, v78, v79\n" \
"v_cvt_pk_bf16_f32 v112, v80, v81\n" \
"v_mfma_f32_16x16x32_bf16 v[240:243], a[176:179], v[124:127], v[240:243]\n" \
"v_cvt_pk_bf16_f32 v113, v82, v83\n" \
"v_cvt_pk_bf16_f32 v114, v84, v85\n" \
"v_mfma_f32_16x16x32_bf16 v[244:247], a[180:183], v[124:127], v[244:247]\n" \
"v_cvt_pk_bf16_f32 v115, v86, v87\n" \
"v_add_u32_e32 v48, s16, v20\n" \
"v_mfma_f32_16x16x32_bf16 v[244:247], a[164:167], v[120:123], v[244:247]\n" \
"s_add_u32 s64, s64, 0x10000\n" \
"v_mfma_f32_16x16x32_bf16 v[248:251], a[168:171], v[120:123], v[248:251]\n" \
"v_mfma_f32_16x16x32_bf16 v[248:251], a[184:187], v[124:127], v[248:251]\n" \
"v_mfma_f32_16x16x32_bf16 v[252:255], a[188:191], v[124:127], v[252:255]\n" \
"v_mfma_f32_16x16x32_bf16 v[252:255], a[172:175], v[120:123], v[252:255]\n" \
"v_smfmac_f32_16x16x64_bf16 a[204:207], a[212:215], v[120:127], v21\n" \
"s_waitcnt vmcnt(13) lgkmcnt(0)\n" \
"s_mov_b64 exec, 0\n" \
"s_cmp_lg_u32 s15, 0\n" \
"s_cbranch_scc1 L_pf21\n" \
"s_branch L_pf20\n" \
"L_retB:\n" \
"s_mov_b64 exec, -1\n" \
"s_barrier\n" \
"s_branch L_2_%=\n" \
"L_t2_%=:\n" \
"v_mfma_f32_16x16x32_bf16 v[252:255], a[188:191], v[124:127], v[252:255]\n" \
"v_cvt_pk_bf16_f32 v114, v84, v85\n" \
"v_cvt_pk_bf16_f32 v115, v86, v87\n" \
"v_mfma_f32_16x16x32_bf16 v[252:255], a[172:175], v[120:123], v[252:255]\n" \
"v_xor_b32_e32 v48, 0x8000, v48\n" \
"v_smfmac_f32_16x16x64_bf16 a[204:207], a[212:215], v[120:127], v21\n" \
"s_waitcnt lgkmcnt(0)\n" \
"L_2_%=:\n" \
"v_mfma_f32_16x16x32_bf16 v[64:67], a[128:131], a[64:67], v[32:35]\n" \
"ds_read_b128 a[160:163], v49 offset:0\n" \
"ds_read_b128 a[176:179], v49 offset:4096\n" \
"v_mfma_f32_16x16x32_bf16 v[64:67], a[132:135], a[68:71], v[64:67]\n" \
"v_cvt_pk_bf16_f32 v120, v88, v89\n" \
"v_cvt_pk_bf16_f32 v121, v90, v91\n" \
"v_mfma_f32_16x16x32_bf16 v[64:67], a[136:139], a[72:75], v[64:67]\n" \
"ds_read_b128 a[180:183], v49 offset:5120\n" \
"ds_read_b128 a[164:167], v49 offset:1024\n" \
"v_mfma_f32_16x16x32_bf16 v[64:67], a[140:143], a[76:79], v[64:67]\n" \
"v_exp_f32_e32 v92, v92\n" \
"v_mfma_f32_16x16x32_bf16 v[68:71], a[156:159], a[76:79], v[32:35]\n" \
"ds_read_b128 a[168:171], v49 offset:2048\n" \
"ds_read_b128 a[184:187], v49 offset:6144\n" \
"v_mfma_f32_16x16x32_bf16 v[68:71], a[152:155], a[72:75], v[68:71]\n" \
"v_exp_f32_e32 v93, v93\n" \
"v_mfma_f32_16x16x32_bf16 v[68:71], a[148:151], a[68:71], v[68:71]\n" \
"ds_read_b128 a[188:191], v49 offset:7168\n" \
"ds_read_b128 a[172:175], v49 offset:3072\n" \
"v_mfma_f32_16x16x32_bf16 v[68:71], a[144:147], a[64:67], v[68:71]\n" \
"v_exp_f32_e32 v94, v94\n" \
"v_mfma_f32_16x16x32_bf16 v[72:75], a[128:131], a[80:83], v[36:39]\n" \
"v_exp_f32_e32 v95, v95\n" \
"v_mfma_f32_16x16x32_bf16 v[72:75], a[132:135], a[84:87], v[72:75]\n" \
"v_cvt_pk_bf16_f32 v122, v92, v93\n" \
"v_cvt_pk_bf16_f32 v123, v94, v95\n" \
"v_mfma_f32_16x16x32_bf16 v[72:75], a[136:139], a[88:91], v[72:75]\n" \
"v_exp_f32_e32 v64, v64\n" \
"v_mfma_f32_16x16x32_bf16 v[72:75], a[140:143], a[92:95], v[72:75]\n" \
"v_exp_f32_e32 v65, v65\n" \
"v_mfma_f32_16x16x32_bf16 v[76:79], a[156:159], a[92:95], v[36:39]\n" \
"v_exp_f32_e32 v66, v66\n" \
"v_mfma_f32_16x16x32_bf16 v[76:79], a[152:155], a[88:91], v[76:79]\n" \
"v_exp_f32_e32 v67, v67\n" \
"v_mfma_f32_16x16x32_bf16 v[76:79], a[148:151], a[84:87], v[76:79]\n" \
"v_exp_f32_e32 v68, v68\n" \
"v_mfma_f32_16x16x32_bf16 v[76:79], a[144:147], a[80:83], v[76:79]\n" \
"v_exp_f32_e32 v69, v69\n" \
"v_mfma_f32_16x16x32_bf16 v[80:83], a[128:131], a[96:99], v[40:43]\n" \
"v_exp_f32_e32 v70, v70\n" \
"v_mfma_f32_16x16x32_bf16 v[80:83], a[132:135], a[100:103], v[80:83]\n" \
"v_exp_f32_e32 v71, v71\n" \
"v_mfma_f32_16x16x32_bf16 v[80:83], a[136:139], a[104:107], v[80:83]\n" \
"v_cvt_pk_bf16_f32 v100, v64, v65\n" \
"v_cvt_pk_bf16_f32 v101, v66, v67\n" \
"v_mfma_f32_16x16x32_bf16 v[80:83], a[140:143], a[108:111], v[80:83]\n" \
"v_cvt_pk_bf16_f32 v102, v68, v69\n" \
"v_cvt_pk_bf16_f32 v103, v70, v71\n" \
"v_mfma_f32_16x16x32_bf16 v[84:87], a[156:159], a[108:111], v[40:43]\n" \
"v_exp_f32_e32 v72, v72\n" \
"v_mfma_f32_16x16x32_bf16 v[84:87], a[152:155], a[104:107], v[84:87]\n" \
"v_exp_f32_e32 v73, v73\n" \
"v_mfma_f32_16x16x32_bf16 v[84:87], a[148:151], a[100:103], v[84:87]\n" \
"v_exp_f32_e32 v74, v74\n" \
"v_mfma_f32_16x16x32_bf16 v[84:87], a[144:147], a[96:99], v[84:87]\n" \
"v_exp_f32_e32 v75, v75\n" \
"v_mfma_f32_16x16x32_bf16 v[88:91], a[128:131], a[112:115], v[44:47]\n" \
"v_exp_f32_e32 v76, v76\n" \
"v_mfma_f32_16x16x32_bf16 v[88:91], a[132:135], a[116:119], v[88:91]\n" \
"v_exp_f32_e32 v77, v77\n" \
"v_mfma_f32_16x16x32_bf16 v[88:91], a[136:139], a[120:123], v[88:91]\n" \
"v_exp_f32_e32 v78, v78\n" \
"v_mfma_f32_16x16x32_bf16 v[88:91], a[140:143], a[124:127], v[88:91]\n" \
"v_exp_f32_e32 v79, v79\n" \
"v_mfma_f32_16x16x32_bf16 v[92:95], a[156:159], a[124:127], v[44:47]\n" \
"v_cvt_pk_bf16_f32 v108, v72, v73\n" \
"v_cvt_pk_bf16_f32 v109, v74, v75\n" \
"v_mfma_f32_16x16x32_bf16 v[92:95], a[152:155], a[120:123], v[92:95]\n" \
"v_cvt_pk_bf16_f32 v110, v76, v77\n" \
"v_cvt_pk_bf16_f32 v111, v78, v79\n" \
"v_mfma_f32_16x16x32_bf16 v[92:95], a[148:151], a[116:119], v[92:95]\n" \
"ds_read_b128 a[128:131], v48 offset:0\n" \
"ds_read_b128 a[132:135], v48 offset:1024\n" \
"v_mfma_f32_16x16x32_bf16 v[92:95], a[144:147], a[112:115], v[92:95]\n" \
"s_waitcnt lgkmcnt(2)\n" \
"v_mfma_f32_16x16x32_bf16 v[128:131], a[160:163], v[96:99], v[128:131]\n" \
"ds_read_b128 a[136:139], v48 offset:2048\n" \
"ds_read_b128 a[140:143], v48 offset:3072\n" \
"v_mfma_f32_16x16x32_bf16 v[128:131], a[176:179], v[100:103], v[128:131]\n" \
"v_exp_f32_e32 v80, v80\n" \
"v_mfma_f32_16x16x32_bf16 v[132:135], a[180:183], v[100:103], v[132:135]\n" \
"ds_read_b128 a[144:147], v48 offset:4096\n" \
"ds_read_b128 a[148:151], v48 offset:5120\n" \
"v_mfma_f32_16x16x32_bf16 v[132:135], a[164:167], v[96:99], v[132:135]\n" \
"v_exp_f32_e32 v81, v81\n" \
"v_mfma_f32_16x16x32_bf16 v[136:139], a[168:171], v[96:99], v[136:139]\n" \
"ds_read_b128 a[152:155], v48 offset:6144\n" \
"ds_read_b128 a[156:159], v48 offset:7168\n" \
"v_mfma_f32_16x16x32_bf16 v[136:139], a[184:187], v[100:103], v[136:139]\n" \
"v_exp_f32_e32 v82, v82\n" \
"v_mfma_f32_16x16x32_bf16 v[140:143], a[188:191], v[100:103], v[140:143]\n" \
"v_exp_f32_e32 v83, v83\n" \
"v_mfma_f32_16x16x32_bf16 v[140:143], a[172:175], v[96:99], v[140:143]\n" \
"v_exp_f32_e32 v84, v84\n" \
"v_smfmac_f32_16x16x64_bf16 a[192:195], a[212:215], v[96:103], v21\n" \
"v_mfma_f32_16x16x32_bf16 v[160:163], a[160:163], v[104:107], v[160:163]\n" \
"v_exp_f32_e32 v85, v85\n" \
"v_mfma_f32_16x16x32_bf16 v[160:163], a[176:179], v[108:111], v[160:163]\n" \
"v_exp_f32_e32 v86, v86\n" \
"v_mfma_f32_16x16x32_bf16 v[164:167], a[180:183], v[108:111], v[164:167]\n" \
"v_exp_f32_e32 v87, v87\n" \
"v_mfma_f32_16x16x32_bf16 v[164:167], a[164:167], v[104:107], v[164:167]\n" \
"v_cvt_pk_bf16_f32 v116, v80, v81\n" \
"v_cvt_pk_bf16_f32 v117, v82, v83\n" \
"v_mfma_f32_16x16x32_bf16 v[168:171], a[168:171], v[104:107], v[168:171]\n" \
"v_cvt_pk_bf16_f32 v118, v84, v85\n" \
"v_cvt_pk_bf16_f32 v119, v86, v87\n" \
"v_mfma_f32_16x16x32_bf16 v[168:171], a[184:187], v[108:111], v[168:171]\n" \
"v_exp_f32_e32 v88, v88\n" \
"v_mfma_f32_16x16x32_bf16 v[172:175], a[188:191], v[108:111], v[172:175]\n" \
"v_exp_f32_e32 v89, v89\n" \
"v_mfma_f32_16x16x32_bf16 v[172:175], a[172:175], v[104:107], v[172:175]\n" \
"v_exp_f32_e32 v90, v90\n" \
"v_smfmac_f32_16x16x64_bf16 a[196:199], a[212:215], v[104:111], v21\n" \
"v_mfma_f32_16x16x32_bf16 v[192:195], a[160:163], v[112:115], v[192:195]\n" \
"v_exp_f32_e32 v91, v91\n" \
"v_mfma_f32_16x16x32_bf16 v[192:195], a[176:179], v[116:119], v[192:195]\n" \
"v_exp_f32_e32 v92, v92\n" \
"v_mfma_f32_16x16x32_bf16 v[196:199], a[180:183], v[116:119], v[196:199]\n" \
"v_exp_f32_e32 v93, v93\n" \
"v_mfma_f32_16x16x32_bf16 v[196:199], a[164:167], v[112:115], v[196:199]\n" \
"v_exp_f32_e32 v94, v94\n" \
"v_mfma_f32_16x16x32_bf16 v[200:203], a[168:171], v[112:115], v[200:203]\n" \
"v_exp_f32_e32 v95, v95\n" \
"v_mfma_f32_16x16x32_bf16 v[200:203], a[184:187], v[116:119], v[200:203]\n" \
"v_cvt_pk_bf16_f32 v124, v88, v89\n" \
"v_cvt_pk_bf16_f32 v125, v90, v91\n" \
"v_mfma_f32_16x16x32_bf16 v[204:207], a[188:191], v[116:119], v[204:207]\n" \
"v_cvt_pk_bf16_f32 v126, v92, v93\n" \
"v_cvt_pk_bf16_f32 v127, v94, v95\n" \
"v_mfma_f32_16x16x32_bf16 v[204:207], a[172:175], v[112:115], v[204:207]\n" \
"v_smfmac_f32_16x16x64_bf16 a[200:203], a[212:215], v[112:119], v21\n" \
"s_waitcnt vmcnt(0) lgkmcnt(0)\n" \
"s_barrier\n" \
"v_mfma_f32_16x16x32_bf16 v[224:227], a[160:163], v[120:123], v[224:227]\n" \
"s_add_u32 s63, s63, s63\n" \
"s_cbranch_scc1 L_h2_%=\n" \
"L_hr2_%=:\n" \
"v_mfma_f32_16x16x32_bf16 v[224:227], a[176:179], v[124:127], v[224:227]\n" \
"s_xor_b32 m0, s65, 0x12000\n" \
"ds_read_b128 a[160:163], v49 offset:8192\n" \
"v_mfma_f32_16x16x32_bf16 v[228:231], a[180:183], v[124:127], v[228:231]\n" \
"buffer_load_dwordx4 v50, s[8:11], s64 offen offset:0 sc1 lds\n" \
"v_mfma_f32_16x16x32_bf16 v[228:231], a[164:167], v[120:123], v[228:231]\n" \
"ds_read_b128 a[176:179], v49 offset:12288\n" \
"ds_read_b128 a[180:183], v49 offset:13312\n" \
"v_mfma_f32_16x16x32_bf16 v[232:235], a[168:171], v[120:123], v[232:235]\n" \
"v_mfma_f32_16x16x32_bf16 v[232:235], a[184:187], v[124:127], v[232:235]\n" \
"buffer_load_dwordx4 v50, s[8:11], s64 offen offset:1024 sc1 lds\n" \
"v_mfma_f32_16x16x32_bf16 v[236:239], a[188:191], v[124:127], v[236:239]\n" \
"ds_read_b128 a[164:167], v49 offset:9216\n" \
"ds_read_b128 a[168:171], v49 offset:10240\n" \
"v_mfma_f32_16x16x32_bf16 v[236:239], a[172:175], v[120:123], v[236:239]\n" \
"v_mfma_f32_16x16x32_bf16 v[64:67], a[128:131], a[64:67], v[32:35]\n" \
"ds_read_b128 a[184:187], v49 offset:14336\n" \
"ds_read_b128 a[188:191], v49 offset:15360\n" \
"v_mfma_f32_16x16x32_bf16 v[64:67], a[132:135], a[68:71], v[64:67]\n" \
"buffer_load_dwordx4 v50, s[8:11], s64 offen offset:2048 sc1 lds\n" \
"v_mfma_f32_16x16x32_bf16 v[64:67], a[136:139], a[72:75], v[64:67]\n" \
"ds_read_b128 a[172:175], v49 offset:11264\n" \
"v_add_u32_e32 v49, 0x10000, v48\n" \
"v_mfma_f32_16x16x32_bf16 v[64:67], a[140:143], a[76:79], v[64:67]\n" \
"v_mfma_f32_16x16x32_bf16 v[68:71], a[156:159], a[76:79], v[32:35]\n" \
"v_mfma_f32_16x16x32_bf16 v[68:71], a[152:155], a[72:75], v[68:71]\n" \
"v_mfma_f32_16x16x32_bf16 v[68:71], a[148:151], a[68:71], v[68:71]\n" \
"buffer_load_dwordx4 v50, s[8:11], s64 offen offset:3072 sc1 lds\n" \
"v_mfma_f32_16x16x32_bf16 v[68:71], a[144:147], a[64:67], v[68:71]\n" \
"s_xor_b32 m0, s65, 0xa000\n" \
"v_mfma_f32_16x16x32_bf16 v[72:75], a[128:131], a[80:83], v[36:39]\n" \
"v_mfma_f32_16x16x32_bf16 v[72:75], a[132:135], a[84:87], v[72:75]\n" \
"v_exp_f32_e32 v64, v64\n" \
"v_mfma_f32_16x16x32_bf16 v[72:75], a[136:139], a[88:91], v[72:75]\n" \
"buffer_load_dwordx4 v51, s[8:11], s64 offen offset:0 sc1 lds\n" \
"v_mfma_f32_16x16x32_bf16 v[72:75], a[140:143], a[92:95], v[72:75]\n" \
"v_exp_f32_e32 v65, v65\n" \
"v_mfma_f32_16x16x32_bf16 v[76:79], a[156:159], a[92:95], v[36:39]\n" \
"v_exp_f32_e32 v66, v66\n" \
"v_mfma_f32_16x16x32_bf16 v[76:79], a[152:155], a[88:91], v[76:79]\n" \
"v_exp_f32_e32 v67, v67\n" \
"v_mfma_f32_16x16x32_bf16 v[76:79], a[148:151], a[84:87], v[76:79]\n" \
"buffer_load_dwordx4 v51, s[8:11], s64 offen offset:1024 sc1 lds\n" \
"v_mfma_f32_16x16x32_bf16 v[76:79], a[144:147], a[80:83], v[76:79]\n" \
"v_exp_f32_e32 v68, v68\n" \
"v_mfma_f32_16x16x32_bf16 v[80:83], a[128:131], a[96:99], v[40:43]\n" \
"v_exp_f32_e32 v69, v69\n" \
"v_mfma_f32_16x16x32_bf16 v[80:83], a[132:135], a[100:103], v[80:83]\n" \
"v_exp_f32_e32 v70, v70\n" \
"v_mfma_f32_16x16x32_bf16 v[80:83], a[136:139], a[104:107], v[80:83]\n" \
"v_exp_f32_e32 v71, v71\n" \
"v_mfma_f32_16x16x32_bf16 v[80:83], a[140:143], a[108:111], v[80:83]\n" \
"buffer_load_dwordx4 v51, s[8:11], s64 offen offset:2048 sc1 lds\n" \
"v_mfma_f32_16x16x32_bf16 v[84:87], a[156:159], a[108:111], v[40:43]\n" \
"v_exp_f32_e32 v72, v72\n" \
"v_mfma_f32_16x16x32_bf16 v[84:87], a[152:155], a[104:107], v[84:87]\n" \
"v_exp_f32_e32 v73, v73\n" \
"v_mfma_f32_16x16x32_bf16 v[84:87], a[148:151], a[100:103], v[84:87]\n" \
"v_exp_f32_e32 v74, v74\n" \
"v_mfma_f32_16x16x32_bf16 v[84:87], a[144:147], a[96:99], v[84:87]\n" \
"buffer_load_dwordx4 v51, s[8:11], s64 offen offset:3072 sc1 lds\n" \
"v_mfma_f32_16x16x32_bf16 v[88:91], a[128:131], a[112:115], v[44:47]\n" \
"s_xor_b32 m0, s65, 0x18000\n" \
"ds_read_b128 a[128:131], v48 offset:8192\n" \
"v_mfma_f32_16x16x32_bf16 v[88:91], a[132:135], a[116:119], v[88:91]\n" \
"v_exp_f32_e32 v75, v75\n" \
"v_mfma_f32_16x16x32_bf16 v[88:91], a[136:139], a[120:123], v[88:91]\n" \
"ds_read_b128 a[132:135], v48 offset:9216\n" \
"ds_read_b128 a[136:139], v48 offset:10240\n" \
"v_mfma_f32_16x16x32_bf16 v[88:91], a[140:143], a[124:127], v[88:91]\n" \
"v_exp_f32_e32 v76, v76\n" \
"v_mfma_f32_16x16x32_bf16 v[92:95], a[156:159], a[124:127], v[44:47]\n" \
"buffer_load_dwordx4 v52, s[8:11], s64 offen offset:0 sc1 lds\n" \
"v_mfma_f32_16x16x32_bf16 v[92:95], a[152:155], a[120:123], v[92:95]\n" \
"ds_read_b128 a[140:143], v48 offset:11264\n" \
"ds_read_b128 a[152:155], v48 offset:14336\n" \
"v_mfma_f32_16x16x32_bf16 v[92:95], a[148:151], a[116:119], v[92:95]\n" \
"v_exp_f32_e32 v77, v77\n" \
"v_mfma_f32_16x16x32_bf16 v[92:95], a[144:147], a[112:115], v[92:95]\n" \
"ds_read_b128 a[144:147], v48 offset:12288\n" \
"s_waitcnt lgkmcnt(6)\n" \
"v_mfma_f32_16x16x32_bf16 v[144:147], a[160:163], v[96:99], v[144:147]\n" \
"buffer_load_dwordx4 v52, s[8:11], s64 offen offset:1024 sc1 lds\n" \
"v_mfma_f32_16x16x32_bf16 v[144:147], a[176:179], v[100:103], v[144:147]\n" \
"ds_read_b128 a[148:151], v48 offset:13312\n" \
"ds_read_b128 a[156:159], v48 offset:15360\n" \
"v_mfma_f32_16x16x32_bf16 v[148:151], a[180:183], v[100:103], v[148:151]\n" \
"v_exp_f32_e32 v78, v78\n" \
"v_mfma_f32_16x16x32_bf16 v[148:151], a[164:167], v[96:99], v[148:151]\n" \
"v_exp_f32_e32 v79, v79\n" \
"v_mfma_f32_16x16x32_bf16 v[152:155], a[168:171], v[96:99], v[152:155]\n" \
"buffer_load_dwordx4 v52, s[8:11], s64 offen offset:2048 sc1 lds\n" \
"v_mfma_f32_16x16x32_bf16 v[152:155], a[184:187], v[100:103], v[152:155]\n" \
"v_exp_f32_e32 v80, v80\n" \
"v_mfma_f32_16x16x32_bf16 v[156:159], a[188:191], v[100:103], v[156:159]\n" \
"v_exp_f32_e32 v81, v81\n" \
"v_mfma_f32_16x16x32_bf16 v[156:159], a[172:175], v[96:99], v[156:159]\n" \
"v_exp_f32_e32 v82, v82\n" \
"v_mfma_f32_16x16x32_bf16 v[176:179], a[160:163], v[104:107], v[176:179]\n" \
"v_exp_f32_e32 v83, v83\n" \
"v_mfma_f32_16x16x32_bf16 v[176:179], a[176:179], v[108:111], v[176:179]\n" \
"buffer_load_dwordx4 v52, s[8:11], s64 offen offset:3072 sc1 lds\n" \
"v_mfma_f32_16x16x32_bf16 v[180:183], a[180:183], v[108:111], v[180:183]\n" \
"s_mov_b32 m0, s65\n" \
"s_xor_b32 s65, s65, 0x8000\n" \
"v_mfma_f32_16x16x32_bf16 v[180:183], a[164:167], v[104:107], v[180:183]\n" \
"v_exp_f32_e32 v84, v84\n" \
"v_mfma_f32_16x16x32_bf16 v[184:187], a[168:171], v[104:107], v[184:187]\n" \
"v_exp_f32_e32 v85, v85\n" \
"v_mfma_f32_16x16x32_bf16 v[184:187], a[184:187], v[108:111], v[184:187]\n" \
"buffer_load_dwordx4 v53, s[8:11], s64 offen offset:0 sc1 lds\n" \
"v_mfma_f32_16x16x32_bf16 v[188:191], a[188:191], v[108:111], v[188:191]\n" \
"v_exp_f32_e32 v86, v86\n" \
"v_mfma_f32_16x16x32_bf16 v[188:191], a[172:175], v[104:107], v[188:191]\n" \
"v_exp_f32_e32 v87, v87\n" \
"v_mfma_f32_16x16x32_bf16 v[208:211], a[160:163], v[112:115], v[208:211]\n" \
"v_exp_f32_e32 v88, v88\n" \
"v_mfma_f32_16x16x32_bf16 v[208:211], a[176:179], v[116:119], v[208:211]\n" \
"buffer_load_dwordx4 v53, s[8:11], s64 offen offset:1024 sc1 lds\n" \
"v_mfma_f32_16x16x32_bf16 v[212:215], a[180:183], v[116:119], v[212:215]\n" \
"v_exp_f32_e32 v89, v89\n" \
"v_mfma_f32_16x16x32_bf16 v[212:215], a[164:167], v[112:115], v[212:215]\n" \
"v_exp_f32_e32 v90, v90\n" \
"v_mfma_f32_16x16x32_bf16 v[216:219], a[168:171], v[112:115], v[216:219]\n" \
"v_exp_f32_e32 v91, v91\n" \
"v_mfma_f32_16x16x32_bf16 v[216:219], a[184:187], v[116:119], v[216:219]\n" \
"v_cvt_pk_bf16_f32 v96, v64, v65\n" \
"v_cvt_pk_bf16_f32 v97, v66, v67\n" \
"v_mfma_f32_16x16x32_bf16 v[220:223], a[188:191], v[116:119], v[220:223]\n" \
"buffer_load_dwordx4 v53, s[8:11], s64 offen offset:2048 sc1 lds\n" \
"v_mfma_f32_16x16x32_bf16 v[220:223], a[172:175], v[112:115], v[220:223]\n" \
"v_cvt_pk_bf16_f32 v98, v68, v69\n" \
"v_cvt_pk_bf16_f32 v99, v70, v71\n" \
"v_mfma_f32_16x16x32_bf16 v[240:243], a[160:163], v[120:123], v[240:243]\n" \
"v_cvt_pk_bf16_f32 v104, v72, v73\n" \
"v_cvt_pk_bf16_f32 v105, v74, v75\n" \
"v_mfma_f32_16x16x32_bf16 v[240:243], a[176:179], v[124:127], v[240:243]\n" \
"v_cvt_pk_bf16_f32 v106, v76, v77\n" \
"v_cvt_pk_bf16_f32 v107, v78, v79\n" \
"v_mfma_f32_16x16x32_bf16 v[244:247], a[180:183], v[124:127], v[244:247]\n" \
"buffer_load_dwordx4 v53, s[8:11], s64 offen offset:3072 sc1 lds\n" \
"v_mfma_f32_16x16x32_bf16 v[244:247], a[164:167], v[120:123], v[244:247]\n" \
"s_add_u32 s64, s64, 0x10000\n" \
"s_sub_u32 s66, s66, 1\n" \
"v_mfma_f32_16x16x32_bf16 v[248:251], a[168:171], v[120:123], v[248:251]\n" \
"v_cvt_pk_bf16_f32 v112, v80, v81\n" \
"v_cvt_pk_bf16_f32 v113, v82, v83\n" \
"v_mfma_f32_16x16x32_bf16 v[248:251], a[184:187], v[124:127], v[248:251]\n" \
"s_cbranch_scc0 L_t2_%=\n" \
"v_mfma_f32_16x16x32_bf16 v[252:255], a[188:191], v[124:127], v[252:255]\n" \
"v_cvt_pk_bf16_f32 v114, v84, v85\n" \
"v_cvt_pk_bf16_f32 v115, v86, v87\n" \
"v_mfma_f32_16x16x32_bf16 v[252:255], a[172:175], v[120:123], v[252:255]\n" \
"v_xor_b32_e32 v48, 0x8000, v48\n" \
"v_smfmac_f32_16x16x64_bf16 a[204:207], a[212:215], v[120:127], v21\n" \
"s_waitcnt lgkmcnt(0)\n" \
"s_waitcnt vmcnt(12)\n" \
"s_barrier\n" \
"s_cmp_lg_u32 s16, 0\n" \
"s_cbranch_scc1 L_f1_%=\n" \
"ds_read_b128 a[160:163], v49 offset:0\n" \
"ds_read_b128 a[164:167], v49 offset:1024\n" \
"ds_read_b128 a[168:171], v49 offset:2048\n" \
"ds_read_b128 a[172:175], v49 offset:3072\n" \
"ds_read_b128 a[176:179], v49 offset:8192\n" \
"ds_read_b128 a[180:183], v49 offset:9216\n" \
"v_cvt_pk_bf16_f32 v120, v88, v89\n" \
"v_cvt_pk_bf16_f32 v121, v90, v91\n" \
"v_mfma_f32_16x16x32_bf16 v[88:91], a[128:131], a[112:115], v[44:47]\n" \
"s_add_u32 s70, s61, 0x0\n" \
"s_add_u32 s71, s61, 0x49000\n" \
"ds_read_b128 a[184:187], v49 offset:10240\n" \
"v_mfma_f32_16x16x32_bf16 v[88:91], a[132:135], a[116:119], v[88:91]\n" \
"ds_read_b128 a[188:191], v49 offset:11264\n" \
"v_add_u32_e32 v54, 0x49000, v20\n" \
"v_mfma_f32_16x16x32_bf16 v[88:91], a[136:139], a[120:123], v[88:91]\n" \
"v_exp_f32_e32 v92, v92\n" \
"v_mfma_f32_16x16x32_bf16 v[88:91], a[140:143], a[124:127], v[88:91]\n" \
"v_exp_f32_e32 v93, v93\n" \
"v_exp_f32_e32 v94, v94\n" \
"v_exp_f32_e32 v95, v95\n" \
"v_cvt_pk_bf16_f32 v122, v92, v93\n" \
"v_cvt_pk_bf16_f32 v123, v94, v95\n" \
"v_mfma_f32_16x16x32_bf16 v[92:95], a[156:159], a[124:127], v[44:47]\n" \
"buffer_load_dwordx4 a[220:223], v20, s[56:59], s60 offen offset:0 sc1\n" \
"v_mfma_f32_16x16x32_bf16 v[92:95], a[152:155], a[120:123], v[92:95]\n" \
"v_readlane_b32 s97, v11, 32\n" \
"v_readlane_b32 s98, v11, 33\n" \
"v_mfma_f32_16x16x32_bf16 v[92:95], a[148:151], a[116:119], v[92:95]\n" \
"v_and_b32_e32 v112, v12, v112\n" \
"v_and_b32_e32 v113, v13, v113\n" \
"v_mfma_f32_16x16x32_bf16 v[92:95], a[144:147], a[112:115], v[92:95]\n" \
"v_and_b32_e32 v114, v14, v114\n" \
"s_waitcnt lgkmcnt(7)\n" \
"v_mfma_f32_16x16x32_bf16 v[128:131], a[160:163], v[96:99], v[128:131]\n" \
"v_and_b32_e32 v115, v15, v115\n" \
"s_waitcnt lgkmcnt(6)\n" \
"v_mfma_f32_16x16x32_bf16 v[132:135], a[164:167], v[96:99], v[132:135]\n" \
"ds_read_b32 v60, v18 offset:0\n" \
"s_waitcnt lgkmcnt(6)\n" \
"v_mfma_f32_16x16x32_bf16 v[136:139], a[168:171], v[96:99], v[136:139]\n" \
"ds_read_b32 v61, v18 offset:128\n" \
"s_waitcnt lgkmcnt(6)\n" \
"v_mfma_f32_16x16x32_bf16 v[140:143], a[172:175], v[96:99], v[140:143]\n" \
"ds_read_b128 a[0:3], v49 offset:4096\n" \
"s_waitcnt lgkmcnt(5)\n" \
"v_mfma_f32_16x16x32_bf16 v[144:147], a[176:179], v[96:99], v[144:147]\n" \
"buffer_load_dwordx4 a[236:239], v54, s[56:59], s60 offen offset:0 sc1\n" \
"v_mfma_f32_16x16x32_bf16 v[148:151], a[180:183], v[96:99], v[148:151]\n" \
"ds_read_b128 a[4:7], v49 offset:5120\n" \
"s_waitcnt lgkmcnt(5)\n" \
"v_mfma_f32_16x16x32_bf16 v[152:155], a[184:187], v[96:99], v[152:155]\n" \
"ds_read_b128 a[8:11], v49 offset:6144\n" \
"s_waitcnt lgkmcnt(5)\n" \
"v_mfma_f32_16x16x32_bf16 v[156:159], a[188:191], v[96:99], v[156:159]\n" \
"buffer_load_dwordx4 a[224:227], v20, s[56:59], s60 offen offset:1024 sc1\n" \
"v_mfma_f32_16x16x32_bf16 a[192:195], a[252:255], v[96:99], a[192:195]\n" \
"ds_read_b128 a[12:15], v49 offset:7168\n" \
"ds_read_b128 a[16:19], v49 offset:12288\n" \
"v_mfma_f32_16x16x32_bf16 v[160:163], a[160:163], v[104:107], v[160:163]\n" \
"ds_read_b128 a[20:23], v49 offset:13312\n" \
"ds_read_b128 a[24:27], v49 offset:14336\n" \
"buffer_load_dwordx4 a[240:243], v54, s[56:59], s60 offen offset:1024 sc1\n" \
"v_mfma_f32_16x16x32_bf16 v[164:167], a[164:167], v[104:107], v[164:167]\n" \
"ds_read_b128 a[28:31], v49 offset:15360\n" \
"v_accvgpr_read_b32 v40, a192\n" \
"v_mfma_f32_16x16x32_bf16 v[168:171], a[168:171], v[104:107], v[168:171]\n" \
"v_accvgpr_read_b32 v23, a193\n" \
"v_add_f32_e32 v40, v40, v23\n" \
"v_mfma_f32_16x16x32_bf16 v[172:175], a[172:175], v[104:107], v[172:175]\n" \
"buffer_load_dwordx4 a[228:231], v20, s[56:59], s60 offen offset:2048 sc1\n" \
"v_mfma_f32_16x16x32_bf16 v[176:179], a[176:179], v[104:107], v[176:179]\n" \
"v_add_u32_e32 v50, s15, v48\n" \
"v_add_u32_e32 v51, 0x10000, v50\n" \
"v_mfma_f32_16x16x32_bf16 v[180:183], a[180:183], v[104:107], v[180:183]\n" \
"s_waitcnt vmcnt(5)\n" \
"ds_write_b128 v50, v[128:131] offset:0\n" \
"buffer_load_dwordx4 a[244:247], v54, s[56:59], s60 offen offset:2048 sc1\n" \
"v_mfma_f32_16x16x32_bf16 v[184:187], a[184:187], v[104:107], v[184:187]\n" \
"ds_write_b128 v50, v[132:135] offset:1024\n" \
"v_mfma_f32_16x16x32_bf16 v[188:191], a[188:191], v[104:107], v[188:191]\n" \
"ds_write_b128 v50, v[136:139] offset:2048\n" \
"buffer_load_dwordx4 a[232:235], v20, s[56:59], s60 offen offset:3072 sc1\n" \
"v_mfma_f32_16x16x32_bf16 a[196:199], a[252:255], v[104:107], a[196:199]\n" \
"ds_write_b128 v50, v[140:143] offset:3072\n" \
"v_mfma_f32_16x16x32_bf16 v[192:195], a[160:163], v[112:115], v[192:195]\n" \
"ds_write_b128 v50, v[144:147] offset:8192\n" \
"buffer_load_dwordx4 a[248:251], v54, s[56:59], s60 offen offset:3072 sc1\n" \
"v_mfma_f32_16x16x32_bf16 v[196:199], a[164:167], v[112:115], v[196:199]\n" \
"ds_write_b128 v50, v[148:151] offset:9216\n" \
"v_mfma_f32_16x16x32_bf16 v[200:203], a[168:171], v[112:115], v[200:203]\n" \
"ds_write_b128 v50, v[152:155] offset:10240\n" \
"buffer_load_dwordx4 a[32:35], v20, s[56:59], s97 offen offset:0 sc1\n" \
"v_mfma_f32_16x16x32_bf16 v[204:207], a[172:175], v[112:115], v[204:207]\n" \
"v_accvgpr_read_b32 v41, a196\n" \
"v_accvgpr_read_b32 v23, a197\n" \
"v_mfma_f32_16x16x32_bf16 v[208:211], a[176:179], v[112:115], v[208:211]\n" \
"v_add_f32_e32 v41, v41, v23\n" \
"v_xor_b32_e32 v52, 0x4000, v50\n" \
"v_mfma_f32_16x16x32_bf16 v[212:215], a[180:183], v[112:115], v[212:215]\n" \
"buffer_load_dwordx4 a[40:43], v54, s[56:59], s97 offen offset:0 sc1\n" \
"v_mfma_f32_16x16x32_bf16 v[216:219], a[184:187], v[112:115], v[216:219]\n" \
"ds_write_b128 v50, v[156:159] offset:11264\n" \
"v_mfma_f32_16x16x32_bf16 v[220:223], a[188:191], v[112:115], v[220:223]\n" \
"ds_write_b128 v51, v[160:163] offset:0\n" \
"buffer_load_dwordx4 a[48:51], v20, s[56:59], s98 offen offset:0 sc1\n" \
"v_mfma_f32_16x16x32_bf16 a[200:203], a[252:255], v[112:115], a[200:203]\n" \
"ds_write_b128 v51, v[164:167] offset:1024\n" \
"ds_write_b128 v51, v[168:171] offset:2048\n" \
"ds_write_b128 v51, v[172:175] offset:3072\n" \
"buffer_load_dwordx4 a[56:59], v54, s[56:59], s98 offen offset:0 sc1\n" \
"ds_write_b128 v51, v[176:179] offset:8192\n" \
"ds_write_b128 v51, v[180:183] offset:9216\n" \
"ds_write_b128 v51, v[184:187] offset:10240\n" \
"ds_write_b128 v51, v[188:191] offset:11264\n" \
"buffer_load_dwordx4 a[36:39], v20, s[56:59], s97 offen offset:1024 sc1\n" \
"ds_write_b64 v19, v[40:41]\n" \
"v_xor_b32_e32 v53, 0x4000, v51\n" \
"v_xor_b32_e32 v63, 0x400, v19\n" \
"v_readlane_b32 s93, v11, 18\n" \
"v_readlane_b32 s84, v11, 13\n" \
"v_readlane_b32 s85, v11, 14\n" \
"v_readlane_b32 s86, v11, 15\n" \
"s_mov_b32 s87, 0x20000\n" \
"v_readlane_b32 s92, v11, 16\n" \
"s_add_u32 m0, s93, 0x0\n" \
"v_exp_f32_e32 v88, v88\n" \
"v_exp_f32_e32 v89, v89\n" \
"v_exp_f32_e32 v90, v90\n" \
"v_exp_f32_e32 v91, v91\n" \
"v_exp_f32_e32 v92, v92\n" \
"v_exp_f32_e32 v93, v93\n" \
"v_exp_f32_e32 v94, v94\n" \
"v_exp_f32_e32 v95, v95\n" \
"v_cvt_pk_bf16_f32 v124, v88, v89\n" \
"v_cvt_pk_bf16_f32 v125, v90, v91\n" \
"v_cvt_pk_bf16_f32 v126, v92, v93\n" \
"v_cvt_pk_bf16_f32 v127, v94, v95\n" \
"v_and_b32_e32 v124, v12, v124\n" \
"v_and_b32_e32 v125, v13, v125\n" \
"v_and_b32_e32 v126, v14, v126\n" \
"v_and_b32_e32 v127, v15, v127\n" \
"v_accvgpr_read_b32 v56, a200\n" \
"v_accvgpr_read_b32 v23, a201\n" \
"v_add_f32_e32 v56, v56, v23\n" \
"s_waitcnt lgkmcnt(0)\n" \
"s_barrier\n" \
"buffer_load_dwordx4 a[44:47], v54, s[56:59], s97 offen offset:1024 sc1\n" \
"ds_read_b128 v[128:131], v52 offset:0\n" \
"ds_read_b128 v[132:135], v52 offset:1024\n" \
"ds_read_b128 v[136:139], v52 offset:2048\n" \
"ds_read_b128 v[140:143], v52 offset:3072\n" \
"ds_read_b128 v[144:147], v52 offset:8192\n" \
"ds_read_b128 v[148:151], v52 offset:9216\n" \
"ds_read_b128 v[152:155], v52 offset:10240\n" \
"ds_read_b128 v[156:159], v52 offset:11264\n" \
"buffer_load_dwordx4 a[52:55], v20, s[56:59], s98 offen offset:1024 sc1\n" \
"ds_read_b128 v[160:163], v53 offset:0\n" \
"ds_read_b128 v[164:167], v53 offset:1024\n" \
"ds_read_b128 v[168:171], v53 offset:2048\n" \
"ds_read_b128 v[172:175], v53 offset:3072\n" \
"ds_read_b128 v[176:179], v53 offset:8192\n" \
"ds_read_b128 v[180:183], v53 offset:9216\n" \
"ds_read_b128 v[184:187], v53 offset:10240\n" \
"ds_read_b128 v[188:191], v53 offset:11264\n" \
"buffer_load_dwordx4 a[60:63], v54, s[56:59], s98 offen offset:1024 sc1\n" \
"L_pf13:\n" \
"s_cbranch_execz L_pf17\n" \
"s_nop 0\n" \
"ds_read_b64 v[42:43], v63\n" \
"v_mfma_f32_16x16x32_bf16 v[224:227], a[160:163], v[120:123], v[224:227]\n" \
"v_mfma_f32_16x16x32_bf16 v[228:231], a[164:167], v[120:123], v[228:231]\n" \
"v_mfma_f32_16x16x32_bf16 v[232:235], a[168:171], v[120:123], v[232:235]\n" \
"buffer_load_dwordx4 v0, s[84:87], s92 offen offset:0 lds\n" \
"v_mfma_f32_16x16x32_bf16 v[236:239], a[172:175], v[120:123], v[236:239]\n" \
"v_mfma_f32_16x16x32_bf16 v[240:243], a[176:179], v[120:123], v[240:243]\n" \
"v_mfma_f32_16x16x32_bf16 v[244:247], a[180:183], v[120:123], v[244:247]\n" \
"buffer_load_dwordx4 v1, s[84:87], s92 offen offset:1024 lds\n" \
"v_mfma_f32_16x16x32_bf16 v[248:251], a[184:187], v[120:123], v[248:251]\n" \
"s_waitcnt lgkmcnt(15)\n" \
"v_add_f32_e32 v192, v192, v128\n" \
"v_mfma_f32_16x16x32_bf16 v[252:255], a[188:191], v[120:123], v[252:255]\n" \
"v_add_f32_e32 v193, v193, v129\n" \
"v_add_f32_e32 v194, v194, v130\n" \
"v_mfma_f32_16x16x32_bf16 a[204:207], a[252:255], v[120:123], a[204:207]\n" \
"buffer_load_dwordx4 v2, s[84:87], s92 offen offset:2048 lds\n" \
"v_add_f32_e32 v195, v195, v131\n" \
"s_waitcnt lgkmcnt(1)\n" \
"s_barrier\n" \
"v_mfma_f32_16x16x32_bf16 v[224:227], a[0:3], v[124:127], v[224:227]\n" \
"buffer_load_dwordx4 v3, s[84:87], s92 offen offset:3072 lds\n" \
"s_add_u32 m0, s93, 0x1000\n" \
"v_mfma_f32_16x16x32_bf16 v[228:231], a[4:7], v[124:127], v[228:231]\n" \
"s_waitcnt lgkmcnt(0)\n" \
"v_add_f32_e32 v56, v56, v42\n" \
"v_mfma_f32_16x16x32_bf16 v[232:235], a[8:11], v[124:127], v[232:235]\n" \
"v_max_f32_e32 v62, v62, v56\n" \
"v_add_f32_e32 v196, v196, v132\n" \
"v_mfma_f32_16x16x32_bf16 v[236:239], a[12:15], v[124:127], v[236:239]\n" \
"buffer_load_dwordx4 v4, s[84:87], s92 offen offset:0 lds\n" \
"v_rcp_f32_e32 v56, v56\n" \
"v_mfma_f32_16x16x32_bf16 v[240:243], a[16:19], v[124:127], v[240:243]\n" \
"v_add_f32_e32 v197, v197, v133\n" \
"v_add_f32_e32 v198, v198, v134\n" \
"v_add_f32_e32 v199, v199, v135\n" \
"v_mul_f32_e32 v56, v56, v60\n" \
"v_mul_f32_e32 v192, v56, v192\n" \
"v_mul_f32_e32 v193, v56, v193\n" \
"v_mul_f32_e32 v194, v56, v194\n" \
"v_mul_f32_e32 v195, v56, v195\n" \
"v_mul_f32_e32 v196, v56, v196\n" \
"v_mul_f32_e32 v197, v56, v197\n" \
"v_mul_f32_e32 v198, v56, v198\n" \
"v_mul_f32_e32 v199, v56, v199\n" \
"v_add_f32_e32 v200, v200, v136\n" \
"v_add_f32_e32 v201, v201, v137\n" \
"v_add_f32_e32 v202, v202, v138\n" \
"v_add_f32_e32 v203, v203, v139\n" \
"v_add_f32_e32 v204, v204, v140\n" \
"v_add_f32_e32 v205, v205, v141\n" \
"v_add_f32_e32 v206, v206, v142\n" \
"v_add_f32_e32 v207, v207, v143\n" \
"v_cvt_pk_bf16_f32 v64, v192, v193\n" \
"v_cvt_pk_bf16_f32 v65, v194, v195\n" \
"v_cvt_pk_bf16_f32 v66, v196, v197\n" \
"v_cvt_pk_bf16_f32 v67, v198, v199\n" \
"buffer_store_dwordx4 v[64:67], v20, s[56:59], s70 offen offset:0 sc1\n" \
"v_mfma_f32_16x16x32_bf16 v[244:247], a[20:23], v[124:127], v[244:247]\n" \
"v_mfma_f32_16x16x32_bf16 a[208:211], a[216:219], v[64:67], a[208:211]\n" \
"v_mul_f32_e32 v200, v56, v200\n" \
"v_mul_f32_e32 v201, v56, v201\n" \
"v_mul_f32_e32 v202, v56, v202\n" \
"v_mul_f32_e32 v203, v56, v203\n" \
"v_mul_f32_e32 v204, v56, v204\n" \
"v_mul_f32_e32 v205, v56, v205\n" \
"v_mul_f32_e32 v206, v56, v206\n" \
"v_mul_f32_e32 v207, v56, v207\n" \
"v_add_f32_e32 v208, v208, v144\n" \
"v_add_f32_e32 v209, v209, v145\n" \
"v_add_f32_e32 v210, v210, v146\n" \
"v_add_f32_e32 v211, v211, v147\n" \
"v_add_f32_e32 v212, v212, v148\n" \
"v_add_f32_e32 v213, v213, v149\n" \
"v_add_f32_e32 v214, v214, v150\n" \
"v_add_f32_e32 v215, v215, v151\n" \
"v_cvt_pk_bf16_f32 v68, v200, v201\n" \
"v_cvt_pk_bf16_f32 v69, v202, v203\n" \
"v_cvt_pk_bf16_f32 v70, v204, v205\n" \
"v_cvt_pk_bf16_f32 v71, v206, v207\n" \
"buffer_store_dwordx4 v[68:71], v20, s[56:59], s70 offen offset:1024 sc1\n" \
"v_mul_f32_e32 v208, v56, v208\n" \
"v_mfma_f32_16x16x32_bf16 a[208:211], a[216:219], v[68:71], a[208:211]\n" \
"v_mul_f32_e32 v209, v56, v209\n" \
"v_mul_f32_e32 v210, v56, v210\n" \
"v_mul_f32_e32 v211, v56, v211\n" \
"v_mul_f32_e32 v212, v56, v212\n" \
"v_mul_f32_e32 v213, v56, v213\n" \
"v_mul_f32_e32 v214, v56, v214\n" \
"v_mul_f32_e32 v215, v56, v215\n" \
"v_add_f32_e32 v216, v216, v152\n" \
"v_add_f32_e32 v217, v217, v153\n" \
"v_add_f32_e32 v218, v218, v154\n" \
"v_add_f32_e32 v219, v219, v155\n" \
"v_add_f32_e32 v220, v220, v156\n" \
"v_add_f32_e32 v221, v221, v157\n" \
"v_add_f32_e32 v222, v222, v158\n" \
"v_add_f32_e32 v223, v223, v159\n" \
"v_cvt_pk_bf16_f32 v72, v208, v209\n" \
"v_cvt_pk_bf16_f32 v73, v210, v211\n" \
"v_cvt_pk_bf16_f32 v74, v212, v213\n" \
"v_cvt_pk_bf16_f32 v75, v214, v215\n" \
"buffer_store_dwordx4 v[72:75], v20, s[56:59], s70 offen offset:2048 sc1\n" \
"v_mfma_f32_16x16x32_bf16 v[248:251], a[24:27], v[124:127], v[248:251]\n" \
"v_mfma_f32_16x16x32_bf16 a[208:211], a[216:219], v[72:75], a[208:211]\n" \
"v_mul_f32_e32 v216, v56, v216\n" \
"v_mul_f32_e32 v217, v56, v217\n" \
"v_mul_f32_e32 v218, v56, v218\n" \
"v_mul_f32_e32 v219, v56, v219\n" \
"v_mul_f32_e32 v220, v56, v220\n" \
"v_mul_f32_e32 v221, v56, v221\n" \
"v_mul_f32_e32 v222, v56, v222\n" \
"v_mul_f32_e32 v223, v56, v223\n" \
"v_cvt_pk_bf16_f32 v76, v216, v217\n" \
"v_cvt_pk_bf16_f32 v77, v218, v219\n" \
"v_cvt_pk_bf16_f32 v78, v220, v221\n" \
"v_cvt_pk_bf16_f32 v79, v222, v223\n" \
"buffer_store_dwordx4 v[76:79], v20, s[56:59], s70 offen offset:3072 sc1\n" \
"v_add_f32_e32 v224, v224, v160\n" \
"v_add_f32_e32 v225, v225, v161\n" \
"v_add_f32_e32 v226, v226, v162\n" \
"v_add_f32_e32 v227, v227, v163\n" \
"v_add_f32_e32 v228, v228, v164\n" \
"v_add_f32_e32 v229, v229, v165\n" \
"v_add_f32_e32 v230, v230, v166\n" \
"v_add_f32_e32 v231, v231, v167\n" \
"v_mfma_f32_16x16x32_bf16 a[208:211], a[216:219], v[76:79], a[208:211]\n" \
"buffer_load_dwordx4 v5, s[84:87], s92 offen offset:1024 lds\n" \
"v_add_f32_e32 v232, v232, v168\n" \
"v_add_f32_e32 v233, v233, v169\n" \
"v_add_f32_e32 v234, v234, v170\n" \
"v_add_f32_e32 v235, v235, v171\n" \
"v_add_f32_e32 v236, v236, v172\n" \
"v_add_f32_e32 v237, v237, v173\n" \
"v_add_f32_e32 v238, v238, v174\n" \
"v_add_f32_e32 v239, v239, v175\n" \
"v_mfma_f32_16x16x32_bf16 v[252:255], a[28:31], v[124:127], v[252:255]\n" \
"buffer_load_dwordx4 v6, s[84:87], s92 offen offset:2048 lds\n" \
"v_add_f32_e32 v240, v240, v176\n" \
"v_add_f32_e32 v241, v241, v177\n" \
"v_add_f32_e32 v242, v242, v178\n" \
"v_add_f32_e32 v243, v243, v179\n" \
"v_add_f32_e32 v244, v244, v180\n" \
"v_add_f32_e32 v245, v245, v181\n" \
"v_add_f32_e32 v246, v246, v182\n" \
"v_add_f32_e32 v247, v247, v183\n" \
"v_add_f32_e32 v248, v248, v184\n" \
"v_add_f32_e32 v249, v249, v185\n" \
"v_add_f32_e32 v250, v250, v186\n" \
"v_add_f32_e32 v251, v251, v187\n" \
"v_add_f32_e32 v252, v252, v188\n" \
"v_add_f32_e32 v253, v253, v189\n" \
"v_add_f32_e32 v254, v254, v190\n" \
"v_add_f32_e32 v255, v255, v191\n" \
"v_mfma_f32_16x16x32_bf16 a[204:207], a[252:255], v[124:127], a[204:207]\n" \
"buffer_load_dwordx4 v7, s[84:87], s92 offen offset:3072 lds\n" \
"s_add_u32 m0, s93, 0x2000\n" \
"s_nop 0\n" \
"buffer_load_dwordx4 v8, s[84:87], s92 offen offset:0 lds\n" \
"s_nop 3\n" \
"v_accvgpr_read_b32 v57, a204\n" \
"v_accvgpr_read_b32 v23, a205\n" \
"v_add_f32_e32 v57, v57, v23\n" \
"v_add_f32_e32 v57, v57, v43\n" \
"v_max_f32_e32 v62, v62, v57\n" \
"v_rcp_f32_e32 v57, v57\n" \
"v_cmp_lt_f32_e64 s[72:73], s100, v62\n" \
"v_mul_f32_e32 v57, v57, v61\n" \
"v_mul_f32_e32 v224, v57, v224\n" \
"v_mul_f32_e32 v225, v57, v225\n" \
"v_mul_f32_e32 v226, v57, v226\n" \
"v_mul_f32_e32 v227, v57, v227\n" \
"v_mul_f32_e32 v228, v57, v228\n" \
"v_mul_f32_e32 v229, v57, v229\n" \
"v_mul_f32_e32 v230, v57, v230\n" \
"v_mul_f32_e32 v231, v57, v231\n" \
"v_cvt_pk_bf16_f32 v80, v224, v225\n" \
"v_cvt_pk_bf16_f32 v81, v226, v227\n" \
"v_cvt_pk_bf16_f32 v82, v228, v229\n" \
"v_cvt_pk_bf16_f32 v83, v230, v231\n" \
"buffer_store_dwordx4 v[80:83], v20, s[56:59], s71 offen offset:0 sc1\n" \
"v_mul_f32_e32 v232, v57, v232\n" \
"v_mfma_f32_16x16x32_bf16 a[208:211], a[216:219], v[80:83], a[208:211]\n" \
"v_mul_f32_e32 v233, v57, v233\n" \
"v_mul_f32_e32 v234, v57, v234\n" \
"v_mul_f32_e32 v235, v57, v235\n" \
"v_mul_f32_e32 v236, v57, v236\n" \
"v_mul_f32_e32 v237, v57, v237\n" \
"v_mul_f32_e32 v238, v57, v238\n" \
"v_mul_f32_e32 v239, v57, v239\n" \
"v_cvt_pk_bf16_f32 v84, v232, v233\n" \
"v_cvt_pk_bf16_f32 v85, v234, v235\n" \
"v_cvt_pk_bf16_f32 v86, v236, v237\n" \
"v_cvt_pk_bf16_f32 v87, v238, v239\n" \
"buffer_store_dwordx4 v[84:87], v20, s[56:59], s71 offen offset:1024 sc1\n" \
"v_mul_f32_e32 v240, v57, v240\n" \
"v_mfma_f32_16x16x32_bf16 a[208:211], a[216:219], v[84:87], a[208:211]\n" \
"v_mul_f32_e32 v241, v57, v241\n" \
"v_mul_f32_e32 v242, v57, v242\n" \
"v_mul_f32_e32 v243, v57, v243\n" \
"v_mul_f32_e32 v244, v57, v244\n" \
"v_mul_f32_e32 v245, v57, v245\n" \
"v_mul_f32_e32 v246, v57, v246\n" \
"v_mul_f32_e32 v247, v57, v247\n" \
"v_cvt_pk_bf16_f32 v96, v240, v241\n" \
"v_cvt_pk_bf16_f32 v97, v242, v243\n" \
"v_cvt_pk_bf16_f32 v98, v244, v245\n" \
"v_cvt_pk_bf16_f32 v99, v246, v247\n" \
"buffer_store_dwordx4 v[96:99], v20, s[56:59], s71 offen offset:2048 sc1\n" \
"v_mul_f32_e32 v248, v57, v248\n" \
"v_mfma_f32_16x16x32_bf16 a[208:211], a[216:219], v[96:99], a[208:211]\n" \
"v_mul_f32_e32 v249, v57, v249\n" \
"v_mul_f32_e32 v250, v57, v250\n" \
"v_mul_f32_e32 v251, v57, v251\n" \
"v_mul_f32_e32 v252, v57, v252\n" \
"v_mul_f32_e32 v253, v57, v253\n" \
"v_mul_f32_e32 v254, v57, v254\n" \
"v_mul_f32_e32 v255, v57, v255\n" \
"v_cvt_pk_bf16_f32 v100, v248, v249\n" \
"v_cvt_pk_bf16_f32 v101, v250, v251\n" \
"v_cvt_pk_bf16_f32 v102, v252, v253\n" \
"v_cvt_pk_bf16_f32 v103, v254, v255\n" \
"buffer_store_dwordx4 v[100:103], v20, s[56:59], s71 offen offset:3072 sc1\n" \
"s_nop 0\n" \
"v_mfma_f32_16x16x32_bf16 a[208:211], a[216:219], v[100:103], a[208:211]\n" \
"s_nop 7\n" \
"v_accvgpr_read_b32 v63, a208\n" \
"v_cmp_u_f32_e32 vcc, v63, v63\n" \
"s_nop 0\n" \
"s_or_b64 vcc, s[72:73], vcc\n" \
"s_cbranch_scc0 L_out_%=\n" \
"s_branch L_end_%=\n" \
"L_h1_%=:\n" \
"L_pf14:\n" \
"s_cbranch_execz L_pf18\n" \
"s_nop 0\n" \
"s_cmp_eq_u32 s63, 0x400000\n" \
"s_cbranch_scc1 L_hc1_%=\n" \
"s_cmp_lg_u32 s63, 0x0\n" \
"s_cbranch_scc1 L_hn01_%=\n" \
"s_mov_b32 exec_lo, 0xffffffff\n" \
"s_mov_b32 exec_hi, 0xf\n" \
"buffer_load_dword v22, v16, s[80:83], 0 offen\n" \
"s_mov_b64 exec, -1\n" \
"s_branch L_hr1_%=\n" \
"L_hn01_%=:\n" \
"buffer_load_dword v22, v17, s[56:59], 0 offen sc1\n" \
"s_branch L_hr1_%=\n" \
"L_hs1_%=:\n" \
"s_sleep 1\n" \
"buffer_load_dword v22, v17, s[56:59], 0 offen sc1\n" \
"s_add_u32 s101, s101, 1\n" \
"s_waitcnt vmcnt(0)\n" \
"s_cmp_gt_u32 s101, 0x100000\n" \
"s_cbranch_scc1 L_hr1_%=\n" \
"L_hc1_%=:\n" \
"v_cmp_ne_u32_e32 vcc, s62, v22\n" \
"s_nop 4\n" \
"s_cbranch_vccnz L_hs1_%=\n" \
"s_branch L_hr1_%=\n" \
"L_h2_%=:\n" \
"s_cmp_eq_u32 s63, 0x400000\n" \
"s_cbranch_scc1 L_hc2_%=\n" \
"s_cmp_lg_u32 s63, 0x0\n" \
"s_cbranch_scc1 L_hn02_%=\n" \
"s_mov_b32 exec_lo, 0xffffffff\n" \
"s_mov_b32 exec_hi, 0xf\n" \
"buffer_load_dword v22, v16, s[80:83], 0 offen\n" \
"s_mov_b64 exec, -1\n" \
"s_branch L_hr2_%=\n" \
"L_hn02_%=:\n" \
"buffer_load_dword v22, v17, s[56:59], 0 offen sc1\n" \
"s_branch L_hr2_%=\n" \
"L_hs2_%=:\n" \
"s_sleep 1\n" \
"buffer_load_dword v22, v17, s[56:59], 0 offen sc1\n" \
"s_add_u32 s101, s101, 1\n" \
"s_waitcnt vmcnt(0)\n" \
"s_cmp_gt_u32 s101, 0x100000\n" \
"s_cbranch_scc1 L_hr2_%=\n" \
"L_hc2_%=:\n" \
"v_cmp_ne_u32_e32 vcc, s62, v22\n" \
"s_nop 4\n" \
"s_cbranch_vccnz L_hs2_%=\n" \
"s_branch L_hr2_%=\n" \
"L_sa_%=:\n" \
"s_sleep 1\n" \
"buffer_load_dword v37, v38, s[56:59], s64 offen sc1\n" \
"s_add_u32 s101, s101, 1\n" \
"s_waitcnt vmcnt(0)\n" \
"v_cmp_ne_u32_e32 vcc, s62, v37\n" \
"s_cmp_gt_u32 s101, 0x100000\n" \
"s_cbranch_scc1 L_ra_%=\n" \
"s_nop 2\n" \
"s_cbranch_vccnz L_sa_%=\n" \
"s_branch L_ra_%=\n" \
"L_sb_%=:\n" \
"s_sleep 1\n" \
"buffer_load_dword v47, v38, s[56:59], s71 offen sc1\n" \
"s_add_u32 s101, s101, 1\n" \
"s_waitcnt vmcnt(0)\n" \
"v_cmp_ne_u32_e32 vcc, s62, v47\n" \
"s_cmp_gt_u32 s101, 0x100000\n" \
"s_cbranch_scc1 L_rb_%=\n" \
"s_nop 2\n" \
"s_cbranch_vccnz L_sb_%=\n" \
"s_branch L_rb_%=\n" \
"L_sx0_%=:\n" \
"s_sleep 1\n" \
"buffer_load_dword v37, v40, s[56:59], s74 offen sc1\n" \
"s_add_u32 s101, s101, 1\n" \
"s_waitcnt vmcnt(0)\n" \
"v_cmp_eq_u32_e64 s[98:99], s62, v37\n" \
"s_cmp_gt_u32 s101, 0x100000\n" \
"s_cbranch_scc1 L_rx0_%=\n" \
"s_andn2_b32 s75, 0x1, s98\n" \
"s_cbranch_scc1 L_sx0_%=\n" \
"s_branch L_rx0_%=\n" \
"L_sx1_%=:\n" \
"s_sleep 1\n" \
"buffer_load_dword v37, v40, s[56:59], s74 offen sc1\n" \
"s_add_u32 s101, s101, 1\n" \
"s_waitcnt vmcnt(0)\n" \
"v_cmp_eq_u32_e64 s[98:99], s62, v37\n" \
"s_cmp_gt_u32 s101, 0x100000\n" \
"s_cbranch_scc1 L_rx1_%=\n" \
"s_andn2_b32 s75, 0x2, s98\n" \
"s_cbranch_scc1 L_sx1_%=\n" \
"s_branch L_rx1_%=\n" \
"L_sx2_%=:\n" \
"s_sleep 1\n" \
"buffer_load_dword v37, v40, s[56:59], s74 offen sc1\n" \
"s_add_u32 s101, s101, 1\n" \
"s_waitcnt vmcnt(0)\n" \
"v_cmp_eq_u32_e64 s[98:99], s62, v37\n" \
"s_cmp_gt_u32 s101, 0x100000\n" \
"s_cbranch_scc1 L_rx2_%=\n" \
"s_andn2_b32 s75, 0x4, s98\n" \
"s_cbranch_scc1 L_sx2_%=\n" \
"s_branch L_rx2_%=\n" \
"L_sx3_%=:\n" \
"s_sleep 1\n" \
"buffer_load_dword v37, v40, s[56:59], s74 offen sc1\n" \
"s_add_u32 s101, s101, 1\n" \
"s_waitcnt vmcnt(0)\n" \
"v_cmp_eq_u32_e64 s[98:99], s62, v37\n" \
"s_cmp_gt_u32 s101, 0x100000\n" \
"s_cbranch_scc1 L_rx3_%=\n" \
"s_andn2_b32 s75, s100, s98\n" \
"s_cbranch_scc1 L_sx3_%=\n" \
"s_branch L_rx3_%=\n" \
"L_sx4_%=:\n" \
"s_sleep 1\n" \
"buffer_load_dword v37, v40, s[56:59], s74 offen sc1\n" \
"s_add_u32 s101, s101, 1\n" \
"s_waitcnt vmcnt(0)\n" \
"v_cmp_eq_u32_e64 s[98:99], s62, v37\n" \
"s_cmp_gt_u32 s101, 0x100000\n" \
"s_cbranch_scc1 L_rx4_%=\n" \
"s_andn2_b32 s75, 0x10, s98\n" \
"s_cbranch_scc1 L_sx4_%=\n" \
"s_branch L_rx4_%=\n" \
"L_sx5_%=:\n" \
"s_sleep 1\n" \
"buffer_load_dword v37, v40, s[56:59], s74 offen sc1\n" \
"s_add_u32 s101, s101, 1\n" \
"s_waitcnt vmcnt(0)\n" \
"v_cmp_eq_u32_e64 s[98:99], s62, v37\n" \
"s_cmp_gt_u32 s101, 0x100000\n" \
"s_cbranch_scc1 L_rx5_%=\n" \
"s_andn2_b32 s75, 0x20, s98\n" \
"s_cbranch_scc1 L_sx5_%=\n" \
"s_branch L_rx5_%=\n" \
"L_sx6_%=:\n" \
"s_sleep 1\n" \
"buffer_load_dword v37, v40, s[56:59], s74 offen sc1\n" \
"s_add_u32 s101, s101, 1\n" \
"s_waitcnt vmcnt(0)\n" \
"v_cmp_eq_u32_e64 s[98:99], s62, v37\n" \
"s_cmp_gt_u32 s101, 0x100000\n" \
"s_cbranch_scc1 L_rx6_%=\n" \
"s_andn2_b32 s75, 0x40, s98\n" \
"s_cbranch_scc1 L_sx6_%=\n" \
"s_branch L_rx6_%=\n" \
"L_sx7_%=:\n" \
"s_sleep 1\n" \
"buffer_load_dword v37, v40, s[56:59], s74 offen sc1\n" \
"s_add_u32 s101, s101, 1\n" \
"s_waitcnt vmcnt(0)\n" \
"v_cmp_eq_u32_e64 s[98:99], s62, v37\n" \
"s_cmp_gt_u32 s101, 0x100000\n" \
"s_cbranch_scc1 L_rx7_%=\n" \
"s_andn2_b32 s75, 0x80, s98\n" \
"s_cbranch_scc1 L_sx7_%=\n" \
"s_branch L_rx7_%=\n" \
"L_sx8_%=:\n" \
"s_sleep 1\n" \
"buffer_load_dword v37, v40, s[56:59], s74 offen sc1\n" \
"s_add_u32 s101, s101, 1\n" \
"s_waitcnt vmcnt(0)\n" \
"v_cmp_eq_u32_e64 s[98:99], s62, v37\n" \
"s_cmp_gt_u32 s101, 0x100000\n" \
"s_cbranch_scc1 L_rx8_%=\n" \
"s_andn2_b32 s75, 0x100, s98\n" \
"s_cbranch_scc1 L_sx8_%=\n" \
"s_branch L_rx8_%=\n" \
"L_sx9_%=:\n" \
"s_sleep 1\n" \
"buffer_load_dword v37, v40, s[56:59], s74 offen sc1\n" \
"s_add_u32 s101, s101, 1\n" \
"s_waitcnt vmcnt(0)\n" \
"v_cmp_eq_u32_e64 s[98:99], s62, v37\n" \
"s_cmp_gt_u32 s101, 0x100000\n" \
"s_cbranch_scc1 L_rx9_%=\n" \
"s_andn2_b32 s75, 0x200, s98\n" \
"s_cbranch_scc1 L_sx9_%=\n" \
"s_branch L_rx9_%=\n" \
"L_sx10_%=:\n" \
"s_sleep 1\n" \
"buffer_load_dword v37, v40, s[56:59], s74 offen sc1\n" \
"s_add_u32 s101, s101, 1\n" \
"s_waitcnt vmcnt(0)\n" \
"v_cmp_eq_u32_e64 s[98:99], s62, v37\n" \
"s_cmp_gt_u32 s101, 0x100000\n" \
"s_cbranch_scc1 L_rx10_%=\n" \
"s_andn2_b32 s75, 0x400, s98\n" \
"s_cbranch_scc1 L_sx10_%=\n" \
"s_branch L_rx10_%=\n" \
"L_att_stub:\n" \
"s_mov_b32 s101, 0\n" \
"v_add_u32_e32 v32, 0x49000, v20\n" \
"v_readlane_b32 s96, v11, 32\n" \
"v_readlane_b32 s97, v11, 33\n" \
"buffer_load_dwordx4 a[220:223], v20, s[56:59], s60 offen offset:0 sc1\n" \
"buffer_load_dwordx4 a[236:239], v32, s[56:59], s60 offen offset:0 sc1\n" \
"buffer_load_dwordx4 a[224:227], v20, s[56:59], s60 offen offset:1024 sc1\n" \
"buffer_load_dwordx4 a[240:243], v32, s[56:59], s60 offen offset:1024 sc1\n" \
"buffer_load_dwordx4 a[228:231], v20, s[56:59], s60 offen offset:2048 sc1\n" \
"buffer_load_dwordx4 a[244:247], v32, s[56:59], s60 offen offset:2048 sc1\n" \
"buffer_load_dwordx4 a[232:235], v20, s[56:59], s60 offen offset:3072 sc1\n" \
"buffer_load_dwordx4 a[248:251], v32, s[56:59], s60 offen offset:3072 sc1\n" \
"buffer_load_dwordx4 a[32:35], v20, s[56:59], s96 offen offset:0 sc1\n" \
"buffer_load_dwordx4 a[40:43], v32, s[56:59], s96 offen offset:0 sc1\n" \
"buffer_load_dwordx4 a[48:51], v20, s[56:59], s97 offen offset:0 sc1\n" \
"buffer_load_dwordx4 a[56:59], v32, s[56:59], s97 offen offset:0 sc1\n" \
"buffer_load_dwordx4 a[36:39], v20, s[56:59], s96 offen offset:1024 sc1\n" \
"buffer_load_dwordx4 a[44:47], v32, s[56:59], s96 offen offset:1024 sc1\n" \
"buffer_load_dwordx4 a[52:55], v20, s[56:59], s97 offen offset:1024 sc1\n" \
"buffer_load_dwordx4 a[60:63], v32, s[56:59], s97 offen offset:1024 sc1\n" \
"buffer_load_dwordx4 v[64:67], v20, s[56:59], s61 offen offset:0 sc1\n" \
"buffer_load_dwordx4 v[68:71], v20, s[56:59], s61 offen offset:1024 sc1\n" \
"buffer_load_dwordx4 v[72:75], v20, s[56:59], s61 offen offset:2048 sc1\n" \
"buffer_load_dwordx4 v[76:79], v20, s[56:59], s61 offen offset:3072 sc1\n" \
"buffer_load_dwordx4 v[80:83], v32, s[56:59], s61 offen offset:0 sc1\n" \
"buffer_load_dwordx4 v[84:87], v32, s[56:59], s61 offen offset:1024 sc1\n" \
"buffer_load_dwordx4 v[96:99], v32, s[56:59], s61 offen offset:2048 sc1\n" \
"buffer_load_dwordx4 v[100:103], v32, s[56:59], s61 offen offset:3072 sc1\n" \
"s_waitcnt vmcnt(0)\n" \
"s_branch L_out_%=\n" \
"L_sw_%=:\n" \
"L_pf15:\n" \
"s_cbranch_execz L_pf19\n" \
"s_nop 0\n" \
"v_mfma_f32_16x16x32_bf16 v[72:75], a[128:131], a[16:19], v[36:39]\n" \
"s_add_u32 s68, s60, 0xfff6e000\n" \
"s_add_u32 s69, s60, 0xfffb7000\n" \
"s_add_u32 s70, s60, 0x0\n" \
"s_add_u32 s71, s60, 0x49000\n" \
"v_mfma_f32_16x16x32_bf16 v[72:75], a[132:135], a[20:23], v[72:75]\n" \
"s_xor_b32 m0, s65, 0x12000\n" \
"ds_read_b128 a[160:163], v49 offset:0\n" \
"v_mfma_f32_16x16x32_bf16 v[72:75], a[136:139], a[24:27], v[72:75]\n" \
"buffer_load_dwordx4 v50, s[8:11], s64 offen offset:0 sc1 lds\n" \
"v_mfma_f32_16x16x32_bf16 v[72:75], a[140:143], a[28:31], v[72:75]\n" \
"ds_read_b128 a[164:167], v49 offset:1024\n" \
"ds_read_b128 a[168:171], v49 offset:2048\n" \
"v_mfma_f32_16x16x32_bf16 v[76:79], a[156:159], a[28:31], v[36:39]\n" \
"ds_read_b128 a[172:175], v49 offset:3072\n" \
"v_cvt_pk_bf16_f32 v120, v88, v89\n" \
"v_mfma_f32_16x16x32_bf16 v[76:79], a[152:155], a[24:27], v[76:79]\n" \
"buffer_load_dwordx4 v50, s[8:11], s64 offen offset:1024 sc1 lds\n" \
"v_mfma_f32_16x16x32_bf16 v[76:79], a[148:151], a[20:23], v[76:79]\n" \
"ds_read_b128 a[176:179], v49 offset:4096\n" \
"v_cvt_pk_bf16_f32 v121, v90, v91\n" \
"v_mfma_f32_16x16x32_bf16 v[76:79], a[144:147], a[16:19], v[76:79]\n" \
"ds_read_b128 a[180:183], v49 offset:5120\n" \
"s_mov_b32 s65, s13\n" \
"v_mfma_f32_16x16x32_bf16 v[80:83], a[128:131], a[32:35], v[40:43]\n" \
"v_add_u32_e32 v63, s15, v20\n" \
"s_add_u32 s67, s14, 40960\n" \
"v_mfma_f32_16x16x32_bf16 v[80:83], a[132:135], a[36:39], v[80:83]\n" \
"v_add_u32_e32 v50, 0x6000, v63\n" \
"s_xor_b32 m0, s65, 0x8000\n" \
"v_mfma_f32_16x16x32_bf16 v[80:83], a[136:139], a[40:43], v[80:83]\n" \
"v_exp_f32_e32 v92, v92\n" \
"v_mfma_f32_16x16x32_bf16 v[80:83], a[140:143], a[44:47], v[80:83]\n" \
"buffer_load_dwordx4 v50, s[8:11], s67 offen offset:0 sc1 lds\n" \
"v_mfma_f32_16x16x32_bf16 v[84:87], a[156:159], a[44:47], v[40:43]\n" \
"v_exp_f32_e32 v93, v93\n" \
"v_mfma_f32_16x16x32_bf16 v[84:87], a[152:155], a[40:43], v[84:87]\n" \
"v_exp_f32_e32 v94, v94\n" \
"v_mfma_f32_16x16x32_bf16 v[84:87], a[148:151], a[36:39], v[84:87]\n" \
"v_exp_f32_e32 v95, v95\n" \
"v_mfma_f32_16x16x32_bf16 v[84:87], a[144:147], a[32:35], v[84:87]\n" \
"v_cvt_pk_bf16_f32 v122, v92, v93\n" \
"v_cvt_pk_bf16_f32 v123, v94, v95\n" \
"v_mfma_f32_16x16x32_bf16 v[88:91], a[128:131], a[48:51], v[44:47]\n" \
"ds_read_b128 a[184:187], v49 offset:6144\n" \
"ds_read_b128 a[188:191], v49 offset:7168\n" \
"v_mfma_f32_16x16x32_bf16 v[88:91], a[132:135], a[52:55], v[88:91]\n" \
"v_and_b32_e32 v96, v12, v96\n" \
"v_and_b32_e32 v97, v13, v97\n" \
"v_mfma_f32_16x16x32_bf16 v[88:91], a[136:139], a[56:59], v[88:91]\n" \
"v_and_b32_e32 v98, v14, v98\n" \
"v_and_b32_e32 v99, v15, v99\n" \
"v_mfma_f32_16x16x32_bf16 v[88:91], a[140:143], a[60:63], v[88:91]\n" \
"buffer_load_dwordx4 v50, s[8:11], s67 offen offset:1024 sc1 lds\n" \
"v_mfma_f32_16x16x32_bf16 v[92:95], a[156:159], a[60:63], v[44:47]\n" \
"v_exp_f32_e32 v72, v72\n" \
"v_mfma_f32_16x16x32_bf16 v[92:95], a[152:155], a[56:59], v[92:95]\n" \
"v_exp_f32_e32 v73, v73\n" \
"v_mfma_f32_16x16x32_bf16 v[92:95], a[148:151], a[52:55], v[92:95]\n" \
"v_exp_f32_e32 v74, v74\n" \
"v_mfma_f32_16x16x32_bf16 v[92:95], a[144:147], a[48:51], v[92:95]\n" \
"v_cvt_pk_bf16_f32 v108, v72, v73\n" \
"s_waitcnt lgkmcnt(2)\n" \
"v_mfma_f32_16x16x32_bf16 v[128:131], a[160:163], v[96:99], v[128:131]\n" \
"v_exp_f32_e32 v75, v75\n" \
"v_mfma_f32_16x16x32_bf16 v[132:135], a[164:167], v[96:99], v[132:135]\n" \
"v_exp_f32_e32 v76, v76\n" \
"v_mfma_f32_16x16x32_bf16 v[136:139], a[168:171], v[96:99], v[136:139]\n" \
"v_exp_f32_e32 v77, v77\n" \
"v_mfma_f32_16x16x32_bf16 v[140:143], a[172:175], v[96:99], v[140:143]\n" \
"v_exp_f32_e32 v78, v78\n" \
"v_mfma_f32_16x16x32_bf16 a[192:195], a[252:255], v[96:99], a[192:195]\n" \
"v_exp_f32_e32 v79, v79\n" \
"v_mfma_f32_16x16x32_bf16 v[160:163], a[160:163], v[104:107], v[160:163]\n" \
"v_cvt_pk_bf16_f32 v109, v74, v75\n" \
"v_cvt_pk_bf16_f32 v110, v76, v77\n" \
"v_cvt_pk_bf16_f32 v111, v78, v79\n" \
"v_and_b32_e32 v108, v12, v108\n" \
"v_and_b32_e32 v109, v13, v109\n" \
"v_and_b32_e32 v110, v14, v110\n" \
"v_and_b32_e32 v111, v15, v111\n" \
"v_exp_f32_e32 v80, v80\n" \
"v_exp_f32_e32 v81, v81\n" \
"v_mfma_f32_16x16x32_bf16 v[160:163], a[176:179], v[108:111], v[160:163]\n" \
"buffer_load_dwordx4 v50, s[8:11], s67 offen offset:2048 sc1 lds\n" \
"v_mfma_f32_16x16x32_bf16 v[164:167], a[180:183], v[108:111], v[164:167]\n" \
"buffer_load_dwordx4 v50, s[8:11], s67 offen offset:3072 sc1 lds\n" \
"v_mfma_f32_16x16x32_bf16 v[164:167], a[164:167], v[104:107], v[164:167]\n" \
"v_exp_f32_e32 v82, v82\n" \
"v_mfma_f32_16x16x32_bf16 v[168:171], a[168:171], v[104:107], v[168:171]\n" \
"v_cvt_pk_bf16_f32 v116, v80, v81\n" \
"s_waitcnt lgkmcnt(0)\n" \
"v_mfma_f32_16x16x32_bf16 v[168:171], a[184:187], v[108:111], v[168:171]\n" \
"v_exp_f32_e32 v83, v83\n" \
"v_mfma_f32_16x16x32_bf16 v[172:175], a[188:191], v[108:111], v[172:175]\n" \
"v_exp_f32_e32 v84, v84\n" \
"v_mfma_f32_16x16x32_bf16 v[172:175], a[172:175], v[104:107], v[172:175]\n" \
"v_exp_f32_e32 v85, v85\n" \
"v_smfmac_f32_16x16x64_bf16 a[196:199], a[212:215], v[104:111], v21\n" \
"v_mfma_f32_16x16x32_bf16 v[192:195], a[160:163], v[112:115], v[192:195]\n" \
"v_exp_f32_e32 v86, v86\n" \
"v_exp_f32_e32 v87, v87\n" \
"v_cvt_pk_bf16_f32 v117, v82, v83\n" \
"v_cvt_pk_bf16_f32 v118, v84, v85\n" \
"v_cvt_pk_bf16_f32 v119, v86, v87\n" \
"s_mov_b32 s64, s14\n" \
"v_add_u32_e32 v51, 0x12000, v63\n" \
"v_mfma_f32_16x16x32_bf16 v[192:195], a[176:179], v[116:119], v[192:195]\n" \
"s_xor_b32 m0, s65, 0xa000\n" \
"ds_read_b32 v58, v18 offset:512\n" \
"v_mfma_f32_16x16x32_bf16 v[196:199], a[180:183], v[116:119], v[196:199]\n" \
"buffer_load_dwordx4 v51, s[8:11], s64 offen offset:0 sc1 lds\n" \
"v_mfma_f32_16x16x32_bf16 v[196:199], a[164:167], v[112:115], v[196:199]\n" \
"v_exp_f32_e32 v88, v88\n" \
"v_mfma_f32_16x16x32_bf16 v[200:203], a[168:171], v[112:115], v[200:203]\n" \
"v_exp_f32_e32 v89, v89\n" \
"v_mfma_f32_16x16x32_bf16 v[200:203], a[184:187], v[116:119], v[200:203]\n" \
"v_exp_f32_e32 v90, v90\n" \
"v_mfma_f32_16x16x32_bf16 v[204:207], a[188:191], v[116:119], v[204:207]\n" \
"v_exp_f32_e32 v91, v91\n" \
"v_mfma_f32_16x16x32_bf16 v[204:207], a[172:175], v[112:115], v[204:207]\n" \
"v_exp_f32_e32 v92, v92\n" \
"v_smfmac_f32_16x16x64_bf16 a[200:203], a[212:215], v[112:119], v21\n" \
"v_mfma_f32_16x16x32_bf16 v[224:227], a[160:163], v[120:123], v[224:227]\n" \
"v_exp_f32_e32 v93, v93\n" \
"v_exp_f32_e32 v94, v94\n" \
"v_exp_f32_e32 v95, v95\n" \
"v_cvt_pk_bf16_f32 v124, v88, v89\n" \
"v_cvt_pk_bf16_f32 v125, v90, v91\n" \
"v_cvt_pk_bf16_f32 v126, v92, v93\n" \
"v_cvt_pk_bf16_f32 v127, v94, v95\n" \
"v_add_u32_e32 v52, 0x14000, v63\n" \
"v_add_u32_e32 v53, 0x20000, v63\n" \
"v_mfma_f32_16x16x32_bf16 v[224:227], a[176:179], v[124:127], v[224:227]\n" \
"buffer_load_dwordx4 v51, s[8:11], s64 offen offset:1024 sc1 lds\n" \
"v_mfma_f32_16x16x32_bf16 v[228:231], a[180:183], v[124:127], v[228:231]\n" \
"ds_read_b32 v59, v18 offset:640\n" \
"ds_read_b32 v60, v18 offset:768\n" \
"v_mfma_f32_16x16x32_bf16 v[228:231], a[164:167], v[120:123], v[228:231]\n" \
"ds_read_b32 v61, v18 offset:896\n" \
"ds_read_b128 a[128:131], v48 offset:0\n" \
"v_mfma_f32_16x16x32_bf16 v[232:235], a[168:171], v[120:123], v[232:235]\n" \
"ds_read_b128 a[132:135], v48 offset:1024\n" \
"ds_read_b128 a[136:139], v48 offset:2048\n" \
"v_mfma_f32_16x16x32_bf16 v[232:235], a[184:187], v[124:127], v[232:235]\n" \
"ds_read_b128 a[140:143], v48 offset:3072\n" \
"ds_read_b128 a[144:147], v48 offset:4096\n" \
"ds_read_b128 a[148:151], v48 offset:5120\n" \
"ds_read_b128 a[152:155], v48 offset:6144\n" \
"ds_read_b128 a[156:159], v48 offset:7168\n" \
"v_mfma_f32_16x16x32_bf16 v[236:239], a[188:191], v[124:127], v[236:239]\n" \
"buffer_load_dwordx4 v51, s[8:11], s64 offen offset:2048 sc1 lds\n" \
"v_mfma_f32_16x16x32_bf16 v[236:239], a[172:175], v[120:123], v[236:239]\n" \
"buffer_load_dwordx4 v51, s[8:11], s64 offen offset:3072 sc1 lds\n" \
"v_readlane_b32 s93, v11, 17\n" \
"v_readlane_b32 s92, v11, 22\n" \
"v_accvgpr_read_b32 v54, a192\n" \
"v_accvgpr_read_b32 v23, a193\n" \
"v_add_f32_e32 v54, v54, v23\n" \
"v_accvgpr_read_b32 v55, a196\n" \
"v_accvgpr_read_b32 v23, a197\n" \
"v_max_f32_e32 v62, v62, v54\n" \
"v_add_f32_e32 v55, v55, v23\n" \
"v_max_f32_e32 v62, v62, v55\n" \
"v_rcp_f32_e32 v54, v54\n" \
"v_rcp_f32_e32 v55, v55\n" \
"s_waitcnt lgkmcnt(11)\n" \
"v_mul_f32_e32 v54, v54, v58\n" \
"s_waitcnt lgkmcnt(10)\n" \
"v_mul_f32_e32 v55, v55, v59\n" \
"v_mul_f32_e32 v128, v54, v128\n" \
"v_mul_f32_e32 v129, v54, v129\n" \
"v_mul_f32_e32 v136, v54, v136\n" \
"v_mul_f32_e32 v137, v54, v137\n" \
"v_mul_f32_e32 v160, v55, v160\n" \
"v_mul_f32_e32 v161, v55, v161\n" \
"v_mul_f32_e32 v168, v55, v168\n" \
"v_mul_f32_e32 v169, v55, v169\n" \
"v_cvt_pk_bf16_f32 v128, v128, v129\n" \
"v_mul_f32_e32 v130, v54, v130\n" \
"v_mul_f32_e32 v131, v54, v131\n" \
"v_cvt_pk_bf16_f32 v136, v136, v137\n" \
"v_mul_f32_e32 v138, v54, v138\n" \
"v_mul_f32_e32 v139, v54, v139\n" \
"s_waitcnt vmcnt(10) lgkmcnt(0)\n" \
"s_barrier\n" \
"v_mfma_f32_16x16x32_bf16 v[80:83], a[128:131], a[32:35], v[40:43]\n" \
"ds_read_b128 a[160:163], v49 offset:8192\n" \
"ds_read_b128 a[164:167], v49 offset:9216\n" \
"v_mfma_f32_16x16x32_bf16 v[80:83], a[132:135], a[36:39], v[80:83]\n" \
"ds_read_b128 a[168:171], v49 offset:10240\n" \
"ds_read_b128 a[172:175], v49 offset:11264\n" \
"v_mfma_f32_16x16x32_bf16 v[80:83], a[136:139], a[40:43], v[80:83]\n" \
"s_xor_b32 m0, s65, 0x18000\n" \
"ds_read_b128 a[176:179], v49 offset:12288\n" \
"v_mfma_f32_16x16x32_bf16 v[80:83], a[140:143], a[44:47], v[80:83]\n" \
"buffer_load_dwordx4 v52, s[8:11], s64 offen offset:0 sc1 lds\n" \
"v_mfma_f32_16x16x32_bf16 v[84:87], a[156:159], a[44:47], v[40:43]\n" \
"ds_read_b128 a[180:183], v49 offset:13312\n" \
"ds_read_b128 a[184:187], v49 offset:14336\n" \
"v_mfma_f32_16x16x32_bf16 v[84:87], a[152:155], a[40:43], v[84:87]\n" \
"buffer_load_dwordx4 v52, s[8:11], s64 offen offset:1024 sc1 lds\n" \
"v_mfma_f32_16x16x32_bf16 v[84:87], a[148:151], a[36:39], v[84:87]\n" \
"ds_read_b128 a[188:191], v49 offset:15360\n" \
"v_add_u32_e32 v49, 0x10000, v48\n" \
"v_mfma_f32_16x16x32_bf16 v[84:87], a[144:147], a[32:35], v[84:87]\n" \
"buffer_load_dwordx4 v52, s[8:11], s64 offen offset:2048 sc1 lds\n" \
"v_mfma_f32_16x16x32_bf16 v[88:91], a[128:131], a[48:51], v[44:47]\n" \
"ds_read_b128 a[128:131], v48 offset:8192\n" \
"v_cvt_pk_bf16_f32 v160, v160, v161\n" \
"v_mfma_f32_16x16x32_bf16 v[88:91], a[132:135], a[52:55], v[88:91]\n" \
"ds_read_b128 a[132:135], v48 offset:9216\n" \
"v_mul_f32_e32 v162, v55, v162\n" \
"v_mfma_f32_16x16x32_bf16 v[88:91], a[136:139], a[56:59], v[88:91]\n" \
"buffer_load_dwordx4 v52, s[8:11], s64 offen offset:3072 sc1 lds\n" \
"v_mfma_f32_16x16x32_bf16 v[88:91], a[140:143], a[60:63], v[88:91]\n" \
"s_mov_b32 m0, s65\n" \
"ds_read_b128 a[136:139], v48 offset:10240\n" \
"v_mfma_f32_16x16x32_bf16 v[92:95], a[156:159], a[60:63], v[44:47]\n" \
"ds_read_b128 a[140:143], v48 offset:11264\n" \
"ds_read_b128 a[156:159], v48 offset:15360\n" \
"v_mfma_f32_16x16x32_bf16 v[92:95], a[152:155], a[56:59], v[92:95]\n" \
"buffer_load_dwordx4 v53, s[8:11], s64 offen offset:0 sc1 lds\n" \
"v_mfma_f32_16x16x32_bf16 v[92:95], a[148:151], a[52:55], v[92:95]\n" \
"ds_read_b128 a[148:151], v48 offset:13312\n" \
"ds_read_b128 a[152:155], v48 offset:14336\n" \
"v_mfma_f32_16x16x32_bf16 v[92:95], a[144:147], a[48:51], v[92:95]\n" \
"ds_read_b128 a[144:147], v48 offset:12288\n" \
"s_waitcnt lgkmcnt(12)\n" \
"v_mfma_f32_16x16x32_bf16 v[144:147], a[160:163], v[96:99], v[144:147]\n" \
"buffer_load_dwordx4 v53, s[8:11], s64 offen offset:1024 sc1 lds\n" \
"v_mfma_f32_16x16x32_bf16 v[148:151], a[164:167], v[96:99], v[148:151]\n" \
"v_exp_f32_e32 v88, v88\n" \
"v_mfma_f32_16x16x32_bf16 v[152:155], a[168:171], v[96:99], v[152:155]\n" \
"v_exp_f32_e32 v89, v89\n" \
"v_mfma_f32_16x16x32_bf16 v[156:159], a[172:175], v[96:99], v[156:159]\n" \
"v_exp_f32_e32 v90, v90\n" \
"v_mfma_f32_16x16x32_bf16 v[176:179], a[160:163], v[104:107], v[176:179]\n" \
"s_xor_b32 s65, s65, 0x8000\n" \
"s_waitcnt lgkmcnt(8)\n" \
"v_mfma_f32_16x16x32_bf16 v[176:179], a[176:179], v[108:111], v[176:179]\n" \
"buffer_load_dwordx4 v53, s[8:11], s64 offen offset:2048 sc1 lds\n" \
"v_mfma_f32_16x16x32_bf16 v[180:183], a[180:183], v[108:111], v[180:183]\n" \
"v_exp_f32_e32 v91, v91\n" \
"v_mfma_f32_16x16x32_bf16 v[180:183], a[164:167], v[104:107], v[180:183]\n" \
"buffer_load_dwordx4 v53, s[8:11], s64 offen offset:3072 sc1 lds\n" \
"v_mfma_f32_16x16x32_bf16 v[184:187], a[168:171], v[104:107], v[184:187]\n" \
"s_add_u32 m0, s93, 0x0\n" \
"s_add_u32 s64, s64, 0x10000\n" \
"L_pf16:\n" \
"s_cbranch_execz L_retA\n" \
"s_nop 0\n" \
"v_mfma_f32_16x16x32_bf16 v[184:187], a[184:187], v[108:111], v[184:187]\n" \
"v_exp_f32_e32 v92, v92\n" \
"v_mfma_f32_16x16x32_bf16 v[188:191], a[188:191], v[108:111], v[188:191]\n" \
"buffer_load_dwordx4 v0, s[80:83], s92 offen offset:0 lds\n" \
"v_mfma_f32_16x16x32_bf16 v[188:191], a[172:175], v[104:107], v[188:191]\n" \
"v_exp_f32_e32 v93, v93\n" \
"v_mfma_f32_16x16x32_bf16 v[208:211], a[160:163], v[112:115], v[208:211]\n" \
"v_exp_f32_e32 v94, v94\n" \
"v_mfma_f32_16x16x32_bf16 v[208:211], a[176:179], v[116:119], v[208:211]\n" \
"v_exp_f32_e32 v95, v95\n" \
"v_mfma_f32_16x16x32_bf16 v[212:215], a[180:183], v[116:119], v[212:215]\n" \
"buffer_load_dwordx4 v1, s[80:83], s92 offen offset:1024 lds\n" \
"v_mfma_f32_16x16x32_bf16 v[212:215], a[164:167], v[112:115], v[212:215]\n" \
"v_exp_f32_e32 v80, v80\n" \
"v_mfma_f32_16x16x32_bf16 v[216:219], a[168:171], v[112:115], v[216:219]\n" \
"v_exp_f32_e32 v81, v81\n" \
"v_mfma_f32_16x16x32_bf16 v[216:219], a[184:187], v[116:119], v[216:219]\n" \
"buffer_load_dwordx4 v2, s[80:83], s92 offen offset:2048 lds\n" \
"v_mfma_f32_16x16x32_bf16 v[220:223], a[188:191], v[116:119], v[220:223]\n" \
"v_exp_f32_e32 v82, v82\n" \
"v_mfma_f32_16x16x32_bf16 v[220:223], a[172:175], v[112:115], v[220:223]\n" \
"v_exp_f32_e32 v83, v83\n" \
"v_mfma_f32_16x16x32_bf16 v[240:243], a[160:163], v[120:123], v[240:243]\n" \
"buffer_load_dwordx4 v3, s[80:83], s92 offen offset:3072 lds\n" \
"v_mfma_f32_16x16x32_bf16 v[240:243], a[176:179], v[124:127], v[240:243]\n" \
"v_exp_f32_e32 v84, v84\n" \
"v_mfma_f32_16x16x32_bf16 v[244:247], a[180:183], v[124:127], v[244:247]\n" \
"v_exp_f32_e32 v85, v85\n" \
"v_mfma_f32_16x16x32_bf16 v[244:247], a[164:167], v[120:123], v[244:247]\n" \
"v_exp_f32_e32 v86, v86\n" \
"v_mfma_f32_16x16x32_bf16 v[248:251], a[168:171], v[120:123], v[248:251]\n" \
"v_exp_f32_e32 v87, v87\n" \
"v_mfma_f32_16x16x32_bf16 v[248:251], a[184:187], v[124:127], v[248:251]\n" \
"v_cvt_pk_bf16_f32 v112, v80, v81\n" \
"v_cvt_pk_bf16_f32 v113, v82, v83\n" \
"v_mfma_f32_16x16x32_bf16 v[252:255], a[188:191], v[124:127], v[252:255]\n" \
"v_cvt_pk_bf16_f32 v114, v84, v85\n" \
"v_cvt_pk_bf16_f32 v115, v86, v87\n" \
"v_mfma_f32_16x16x32_bf16 v[252:255], a[172:175], v[120:123], v[252:255]\n" \
"v_and_b32_e32 v112, v12, v112\n" \
"v_and_b32_e32 v113, v13, v113\n" \
"v_smfmac_f32_16x16x64_bf16 a[204:207], a[212:215], v[120:127], v21\n" \
"v_cvt_pk_bf16_f32 v120, v88, v89\n" \
"v_cvt_pk_bf16_f32 v121, v90, v91\n" \
"v_cvt_pk_bf16_f32 v122, v92, v93\n" \
"v_cvt_pk_bf16_f32 v123, v94, v95\n" \
"v_and_b32_e32 v114, v14, v114\n" \
"v_and_b32_e32 v115, v15, v115\n" \
"v_add_u32_e32 v48, s16, v20\n" \
"v_mul_f32_e32 v144, v54, v144\n" \
"v_mul_f32_e32 v145, v54, v145\n" \
"v_mul_f32_e32 v152, v54, v152\n" \
"v_mul_f32_e32 v153, v54, v153\n" \
"v_mul_f32_e32 v176, v55, v176\n" \
"v_mul_f32_e32 v177, v55, v177\n" \
"v_mul_f32_e32 v184, v55, v184\n" \
"v_mul_f32_e32 v185, v55, v185\n" \
"v_mul_f32_e32 v163, v55, v163\n" \
"v_cvt_pk_bf16_f32 v168, v168, v169\n" \
"v_mul_f32_e32 v170, v55, v170\n" \
"v_mul_f32_e32 v171, v55, v171\n" \
"v_cvt_pk_bf16_f32 v144, v144, v145\n" \
"v_mul_f32_e32 v146, v54, v146\n" \
"v_mul_f32_e32 v147, v54, v147\n" \
"v_cvt_pk_bf16_f32 v152, v152, v153\n" \
"v_mul_f32_e32 v154, v54, v154\n" \
"v_mul_f32_e32 v155, v54, v155\n" \
"v_cvt_pk_bf16_f32 v176, v176, v177\n" \
"v_mul_f32_e32 v178, v55, v178\n" \
"v_mul_f32_e32 v179, v55, v179\n" \
"v_cvt_pk_bf16_f32 v184, v184, v185\n" \
"v_mul_f32_e32 v186, v55, v186\n" \
"v_mul_f32_e32 v187, v55, v187\n" \
"v_cvt_pk_bf16_f32 v129, v130, v131\n" \
"v_mul_f32_e32 v132, v54, v132\n" \
"v_mul_f32_e32 v133, v54, v133\n" \
"v_mul_f32_e32 v134, v54, v134\n" \
"v_mul_f32_e32 v135, v54, v135\n" \
"v_cvt_pk_bf16_f32 v137, v138, v139\n" \
"v_mul_f32_e32 v140, v54, v140\n" \
"v_mul_f32_e32 v141, v54, v141\n" \
"v_mul_f32_e32 v142, v54, v142\n" \
"v_mul_f32_e32 v143, v54, v143\n" \
"v_cvt_pk_bf16_f32 v161, v162, v163\n" \
"v_mul_f32_e32 v164, v55, v164\n" \
"v_mul_f32_e32 v165, v55, v165\n" \
"v_mul_f32_e32 v166, v55, v166\n" \
"v_mul_f32_e32 v167, v55, v167\n" \
"v_cvt_pk_bf16_f32 v169, v170, v171\n" \
"v_mul_f32_e32 v172, v55, v172\n" \
"v_mul_f32_e32 v173, v55, v173\n" \
"v_mul_f32_e32 v174, v55, v174\n" \
"v_mul_f32_e32 v175, v55, v175\n" \
"v_cvt_pk_bf16_f32 v145, v146, v147\n" \
"v_mul_f32_e32 v148, v54, v148\n" \
"v_mul_f32_e32 v149, v54, v149\n" \
"v_mul_f32_e32 v150, v54, v150\n" \
"v_mul_f32_e32 v151, v54, v151\n" \
"v_cvt_pk_bf16_f32 v153, v154, v155\n" \
"v_mul_f32_e32 v156, v54, v156\n" \
"v_mul_f32_e32 v157, v54, v157\n" \
"v_mul_f32_e32 v158, v54, v158\n" \
"v_mul_f32_e32 v159, v54, v159\n" \
"v_cvt_pk_bf16_f32 v177, v178, v179\n" \
"v_mul_f32_e32 v180, v55, v180\n" \
"v_mul_f32_e32 v181, v55, v181\n" \
"v_mul_f32_e32 v182, v55, v182\n" \
"v_mul_f32_e32 v183, v55, v183\n" \
"v_cvt_pk_bf16_f32 v185, v186, v187\n" \
"v_mul_f32_e32 v188, v55, v188\n" \
"v_mul_f32_e32 v189, v55, v189\n" \
"v_mul_f32_e32 v190, v55, v190\n" \
"v_mul_f32_e32 v191, v55, v191\n" \
"v_cvt_pk_bf16_f32 v130, v132, v133\n" \
"v_cvt_pk_bf16_f32 v131, v134, v135\n" \
"buffer_store_dwordx4 v[128:131], v20, s[56:59], s68 offen offset:0 sc1\n" \
"v_cvt_pk_bf16_f32 v138, v140, v141\n" \
"v_cvt_pk_bf16_f32 v139, v142, v143\n" \
"buffer_store_dwordx4 v[136:139], v20, s[56:59], s68 offen offset:1024 sc1\n" \
"v_cvt_pk_bf16_f32 v162, v164, v165\n" \
"s_waitcnt vmcnt(14) lgkmcnt(0)\n" \
"s_barrier\n" \
"v_mfma_f32_16x16x32_bf16 v[88:91], a[128:131], a[48:51], v[44:47]\n" \
"s_xor_b32 m0, s65, 0x12000\n" \
"ds_read_b128 a[160:163], v49 offset:0\n" \
"v_mfma_f32_16x16x32_bf16 v[88:91], a[132:135], a[52:55], v[88:91]\n" \
"buffer_load_dwordx4 v50, s[8:11], s64 offen offset:0 sc1 lds\n" \
"v_mfma_f32_16x16x32_bf16 v[88:91], a[136:139], a[56:59], v[88:91]\n" \
"ds_read_b128 a[164:167], v49 offset:1024\n" \
"ds_read_b128 a[168:171], v49 offset:2048\n" \
"v_mfma_f32_16x16x32_bf16 v[88:91], a[140:143], a[60:63], v[88:91]\n" \
"buffer_load_dwordx4 v50, s[8:11], s64 offen offset:1024 sc1 lds\n" \
"v_mfma_f32_16x16x32_bf16 v[92:95], a[156:159], a[60:63], v[44:47]\n" \
"ds_read_b128 a[172:175], v49 offset:3072\n" \
"ds_read_b128 a[176:179], v49 offset:8192\n" \
"v_mfma_f32_16x16x32_bf16 v[92:95], a[152:155], a[56:59], v[92:95]\n" \
"ds_read_b128 a[180:183], v49 offset:9216\n" \
"ds_read_b128 a[184:187], v49 offset:10240\n" \
"v_mfma_f32_16x16x32_bf16 v[92:95], a[148:151], a[52:55], v[92:95]\n" \
"ds_read_b128 a[188:191], v49 offset:11264\n" \
"ds_read_b128 a[0:3], v49 offset:4096\n" \
"v_mfma_f32_16x16x32_bf16 v[92:95], a[144:147], a[48:51], v[92:95]\n" \
"buffer_load_dwordx4 v50, s[8:11], s64 offen offset:2048 sc1 lds\n" \
"v_mfma_f32_16x16x32_bf16 v[32:35], a[220:223], a[64:67], 0\n" \
"ds_read_b128 a[4:7], v49 offset:5120\n" \
"ds_read_b128 a[8:11], v49 offset:6144\n" \
"v_mfma_f32_16x16x32_bf16 v[36:39], a[220:223], a[80:83], 0\n" \
"ds_read_b128 a[12:15], v49 offset:7168\n" \
"ds_read_b128 a[16:19], v49 offset:12288\n" \
"v_mfma_f32_16x16x32_bf16 v[40:43], a[220:223], a[96:99], 0\n" \
"ds_read_b128 a[20:23], v49 offset:13312\n" \
"ds_read_b128 a[24:27], v49 offset:14336\n" \
"v_mfma_f32_16x16x32_bf16 v[44:47], a[220:223], a[112:115], 0\n" \
"buffer_load_dwordx4 v50, s[8:11], s64 offen offset:3072 sc1 lds\n" \
"v_mfma_f32_16x16x32_bf16 v[32:35], a[224:227], a[68:71], v[32:35]\n" \
"s_xor_b32 m0, s65, 0xa000\n" \
"ds_read_b128 a[28:31], v49 offset:15360\n" \
"v_mfma_f32_16x16x32_bf16 v[36:39], a[224:227], a[84:87], v[36:39]\n" \
"v_exp_f32_e32 v88, v88\n" \
"v_mfma_f32_16x16x32_bf16 v[40:43], a[224:227], a[100:103], v[40:43]\n" \
"buffer_load_dwordx4 v51, s[8:11], s64 offen offset:0 sc1 lds\n" \
"v_mfma_f32_16x16x32_bf16 v[44:47], a[224:227], a[116:119], v[44:47]\n" \
"v_exp_f32_e32 v89, v89\n" \
"v_mfma_f32_16x16x32_bf16 v[32:35], a[228:231], a[72:75], v[32:35]\n" \
"v_exp_f32_e32 v90, v90\n" \
"v_mfma_f32_16x16x32_bf16 v[36:39], a[228:231], a[88:91], v[36:39]\n" \
"v_exp_f32_e32 v91, v91\n" \
"v_mfma_f32_16x16x32_bf16 v[40:43], a[228:231], a[104:107], v[40:43]\n" \
"buffer_load_dwordx4 v51, s[8:11], s64 offen offset:1024 sc1 lds\n" \
"v_mfma_f32_16x16x32_bf16 v[44:47], a[228:231], a[120:123], v[44:47]\n" \
"v_exp_f32_e32 v92, v92\n" \
"v_mfma_f32_16x16x32_bf16 v[32:35], a[232:235], a[76:79], v[32:35]\n" \
"v_exp_f32_e32 v93, v93\n" \
"v_mfma_f32_16x16x32_bf16 v[36:39], a[232:235], a[92:95], v[36:39]\n" \
"v_exp_f32_e32 v94, v94\n" \
"v_mfma_f32_16x16x32_bf16 v[40:43], a[232:235], a[108:111], v[40:43]\n" \
"buffer_load_dwordx4 v51, s[8:11], s64 offen offset:2048 sc1 lds\n" \
"v_mfma_f32_16x16x32_bf16 v[44:47], a[232:235], a[124:127], v[44:47]\n" \
"v_cvt_pk_bf16_f32 v124, v88, v89\n" \
"s_waitcnt lgkmcnt(3)\n" \
"v_mfma_f32_16x16x32_bf16 v[192:195], a[160:163], v[112:115], v[192:195]\n" \
"v_exp_f32_e32 v95, v95\n" \
"v_mfma_f32_16x16x32_bf16 v[196:199], a[164:167], v[112:115], v[196:199]\n" \
"buffer_load_dwordx4 v51, s[8:11], s64 offen offset:3072 sc1 lds\n" \
"v_mfma_f32_16x16x32_bf16 v[200:203], a[168:171], v[112:115], v[200:203]\n" \
"s_add_u32 m0, s93, 0x1000\n" \
"v_cvt_pk_bf16_f32 v125, v90, v91\n" \
"v_mfma_f32_16x16x32_bf16 v[204:207], a[172:175], v[112:115], v[204:207]\n" \
"v_cvt_pk_bf16_f32 v126, v92, v93\n" \
"v_cvt_pk_bf16_f32 v127, v94, v95\n" \
"v_mfma_f32_16x16x32_bf16 v[208:211], a[176:179], v[112:115], v[208:211]\n" \
"v_and_b32_e32 v124, v12, v124\n" \
"v_and_b32_e32 v125, v13, v125\n" \
"v_mfma_f32_16x16x32_bf16 v[212:215], a[180:183], v[112:115], v[212:215]\n" \
"buffer_load_dwordx4 v4, s[80:83], s92 offen offset:0 lds\n" \
"v_mfma_f32_16x16x32_bf16 v[216:219], a[184:187], v[112:115], v[216:219]\n" \
"v_and_b32_e32 v126, v14, v126\n" \
"v_and_b32_e32 v127, v15, v127\n" \
"v_mfma_f32_16x16x32_bf16 v[220:223], a[188:191], v[112:115], v[220:223]\n" \
"ds_read_b128 a[128:131], v48 offset:0\n" \
"ds_read_b128 a[132:135], v48 offset:1024\n" \
"v_mfma_f32_16x16x32_bf16 a[200:203], a[252:255], v[112:115], a[200:203]\n" \
"ds_read_b128 a[136:139], v48 offset:2048\n" \
"ds_read_b128 a[140:143], v48 offset:3072\n" \
"v_mfma_f32_16x16x32_bf16 v[224:227], a[160:163], v[120:123], v[224:227]\n" \
"buffer_load_dwordx4 v5, s[80:83], s92 offen offset:1024 lds\n" \
"v_mfma_f32_16x16x32_bf16 v[228:231], a[164:167], v[120:123], v[228:231]\n" \
"ds_read_b128 a[144:147], v48 offset:4096\n" \
"ds_read_b128 a[148:151], v48 offset:5120\n" \
"v_mfma_f32_16x16x32_bf16 v[232:235], a[168:171], v[120:123], v[232:235]\n" \
"ds_read_b128 a[152:155], v48 offset:6144\n" \
"ds_read_b128 a[156:159], v48 offset:7168\n" \
"v_mfma_f32_16x16x32_bf16 v[236:239], a[172:175], v[120:123], v[236:239]\n" \
"buffer_load_dwordx4 v6, s[80:83], s92 offen offset:2048 lds\n" \
"v_mfma_f32_16x16x32_bf16 v[240:243], a[176:179], v[120:123], v[240:243]\n" \
"v_cvt_pk_bf16_f32 v163, v166, v167\n" \
"v_cvt_pk_bf16_f32 v170, v172, v173\n" \
"buffer_store_dwordx4 v[160:163], v20, s[56:59], s69 offen offset:0 sc1\n" \
"v_mfma_f32_16x16x32_bf16 v[244:247], a[180:183], v[120:123], v[244:247]\n" \
"v_cvt_pk_bf16_f32 v171, v174, v175\n" \
"v_cvt_pk_bf16_f32 v146, v148, v149\n" \
"buffer_store_dwordx4 v[168:171], v20, s[56:59], s69 offen offset:1024 sc1\n" \
"v_mfma_f32_16x16x32_bf16 v[248:251], a[184:187], v[120:123], v[248:251]\n" \
"v_cvt_pk_bf16_f32 v147, v150, v151\n" \
"v_cvt_pk_bf16_f32 v154, v156, v157\n" \
"buffer_store_dwordx4 v[144:147], v20, s[56:59], s68 offen offset:2048 sc1\n" \
"v_mfma_f32_16x16x32_bf16 v[252:255], a[188:191], v[120:123], v[252:255]\n" \
"buffer_load_dwordx4 v7, s[80:83], s92 offen offset:3072 lds\n" \
"v_mfma_f32_16x16x32_bf16 a[204:207], a[252:255], v[120:123], a[204:207]\n" \
"v_cvt_pk_bf16_f32 v155, v158, v159\n" \
"v_cvt_pk_bf16_f32 v178, v180, v181\n" \
"buffer_store_dwordx4 v[152:155], v20, s[56:59], s68 offen offset:3072 sc1\n" \
"v_mfma_f32_16x16x32_bf16 v[224:227], a[0:3], v[124:127], v[224:227]\n" \
"v_cvt_pk_bf16_f32 v179, v182, v183\n" \
"v_cvt_pk_bf16_f32 v186, v188, v189\n" \
"buffer_store_dwordx4 v[176:179], v20, s[56:59], s69 offen offset:2048 sc1\n" \
"v_mfma_f32_16x16x32_bf16 v[228:231], a[4:7], v[124:127], v[228:231]\n" \
"v_cvt_pk_bf16_f32 v187, v190, v191\n" \
"s_add_u32 m0, s93, 0x2000\n" \
"buffer_store_dwordx4 v[184:187], v20, s[56:59], s69 offen offset:3072 sc1\n" \
"v_mfma_f32_16x16x32_bf16 v[232:235], a[8:11], v[124:127], v[232:235]\n" \
"buffer_load_dwordx4 v8, s[80:83], s92 offen offset:0 lds\n" \
"v_mfma_f32_16x16x32_bf16 v[236:239], a[12:15], v[124:127], v[236:239]\n" \
"v_mfma_f32_16x16x32_bf16 a[208:211], a[216:219], v[128:131], a[208:211]\n" \
"v_mfma_f32_16x16x32_bf16 a[208:211], a[216:219], v[136:139], a[208:211]\n" \
"v_mfma_f32_32x32x16_bf16 v[128:143], a[216:219], a[216:219], 0\n" \
"v_mfma_f32_16x16x32_bf16 a[208:211], a[216:219], v[160:163], a[208:211]\n" \
"v_mfma_f32_16x16x32_bf16 a[208:211], a[216:219], v[168:171], a[208:211]\n" \
"v_mfma_f32_32x32x16_bf16 v[160:175], a[216:219], a[216:219], 0\n" \
"v_accvgpr_read_b32 v56, a200\n" \
"v_accvgpr_read_b32 v23, a201\n" \
"v_add_f32_e32 v56, v56, v23\n" \
"v_mfma_f32_16x16x32_bf16 a[208:211], a[216:219], v[144:147], a[208:211]\n" \
"v_max_f32_e32 v62, v62, v56\n" \
"v_readlane_b32 s95, v11, 2\n" \
"v_mfma_f32_16x16x32_bf16 v[240:243], a[16:19], v[124:127], v[240:243]\n" \
"v_add_u32_e32 v49, 0x10000, v48\n" \
"s_waitcnt lgkmcnt(0)\n" \
"v_mfma_f32_16x16x32_bf16 v[244:247], a[20:23], v[124:127], v[244:247]\n" \
"v_rcp_f32_e32 v56, v56\n" \
"v_mfma_f32_16x16x32_bf16 v[248:251], a[24:27], v[124:127], v[248:251]\n" \
"v_mfma_f32_16x16x32_bf16 a[208:211], a[216:219], v[152:155], a[208:211]\n" \
"v_mfma_f32_32x32x16_bf16 v[144:159], a[216:219], a[216:219], 0\n" \
"v_mul_f32_e32 v56, v56, v60\n" \
"v_mfma_f32_16x16x32_bf16 a[208:211], a[216:219], v[176:179], a[208:211]\n" \
"v_mul_f32_e32 v192, v56, v192\n" \
"v_mul_f32_e32 v193, v56, v193\n" \
"v_mfma_f32_16x16x32_bf16 v[252:255], a[28:31], v[124:127], v[252:255]\n" \
"v_cvt_pk_bf16_f32 v192, v192, v193\n" \
"v_mul_f32_e32 v194, v56, v194\n" \
"v_mfma_f32_16x16x32_bf16 a[204:207], a[252:255], v[124:127], a[204:207]\n" \
"v_mul_f32_e32 v195, v56, v195\n" \
"v_cvt_pk_bf16_f32 v193, v194, v195\n" \
"s_waitcnt vmcnt(19)\n" \
"s_barrier\n" \
"v_mfma_f32_16x16x32_bf16 v[64:67], a[128:131], a[64:67], v[32:35]\n" \
"s_xor_b32 m0, s65, 0x18000\n" \
"v_mul_f32_e32 v196, v56, v196\n" \
"v_mfma_f32_16x16x32_bf16 v[64:67], a[132:135], a[68:71], v[64:67]\n" \
"buffer_load_dwordx4 v52, s[8:11], s64 offen offset:0 sc1 lds\n" \
"v_mfma_f32_16x16x32_bf16 v[64:67], a[136:139], a[72:75], v[64:67]\n" \
"v_mfma_f32_16x16x32_bf16 a[208:211], a[216:219], v[184:187], a[208:211]\n" \
"v_mfma_f32_32x32x16_bf16 v[176:191], a[216:219], a[216:219], 0\n" \
"v_mul_f32_e32 v197, v56, v197\n" \
"v_mul_f32_e32 v198, v56, v198\n" \
"v_mul_f32_e32 v199, v56, v199\n" \
"v_mul_f32_e32 v200, v56, v200\n" \
"v_mul_f32_e32 v201, v56, v201\n" \
"v_accvgpr_read_b32 v57, a204\n" \
"v_mfma_f32_16x16x32_bf16 v[64:67], a[140:143], a[76:79], v[64:67]\n" \
"buffer_load_dwordx4 v52, s[8:11], s64 offen offset:1024 sc1 lds\n" \
"v_mfma_f32_16x16x32_bf16 v[68:71], a[156:159], a[76:79], v[32:35]\n" \
"v_accvgpr_read_b32 v23, a205\n" \
"v_cvt_pk_bf16_f32 v194, v196, v197\n" \
"v_mfma_f32_32x32x16_bf16 a[192:207], a[216:219], a[216:219], 0\n" \
"v_cvt_pk_bf16_f32 v195, v198, v199\n" \
"buffer_store_dwordx4 v[192:195], v20, s[56:59], s70 offen offset:0 sc1\n" \
"v_cvt_pk_bf16_f32 v200, v200, v201\n" \
"v_mfma_f32_16x16x32_bf16 v[68:71], a[152:155], a[72:75], v[68:71]\n" \
"v_mul_f32_e32 v202, v56, v202\n" \
"v_mul_f32_e32 v203, v56, v203\n" \
"v_mfma_f32_16x16x32_bf16 v[68:71], a[148:151], a[68:71], v[68:71]\n" \
"v_add_f32_e32 v57, v57, v23\n" \
"v_cvt_pk_bf16_f32 v201, v202, v203\n" \
"v_mfma_f32_16x16x32_bf16 v[68:71], a[144:147], a[64:67], v[68:71]\n" \
"buffer_load_dwordx4 v52, s[8:11], s64 offen offset:2048 sc1 lds\n" \
"v_mfma_f32_16x16x32_bf16 v[72:75], a[128:131], a[80:83], v[36:39]\n" \
"v_mfma_f32_16x16x32_bf16 a[208:211], a[216:219], v[192:195], a[208:211]\n" \
"v_mul_f32_e32 v204, v56, v204\n" \
"v_mul_f32_e32 v205, v56, v205\n" \
"v_mfma_f32_16x16x32_bf16 v[72:75], a[132:135], a[84:87], v[72:75]\n" \
"v_mul_f32_e32 v206, v56, v206\n" \
"v_mul_f32_e32 v207, v56, v207\n" \
"v_mfma_f32_16x16x32_bf16 v[72:75], a[136:139], a[88:91], v[72:75]\n" \
"v_mul_f32_e32 v208, v56, v208\n" \
"v_mul_f32_e32 v209, v56, v209\n" \
"v_mfma_f32_16x16x32_bf16 v[72:75], a[140:143], a[92:95], v[72:75]\n" \
"buffer_load_dwordx4 v52, s[8:11], s64 offen offset:3072 sc1 lds\n" \
"v_mfma_f32_16x16x32_bf16 v[76:79], a[156:159], a[92:95], v[36:39]\n" \
"s_mov_b32 m0, s65\n" \
"v_max_f32_e32 v62, v62, v57\n" \
"v_mfma_f32_16x16x32_bf16 v[76:79], a[152:155], a[88:91], v[76:79]\n" \
"v_cvt_pk_bf16_f32 v202, v204, v205\n" \
"v_cvt_pk_bf16_f32 v203, v206, v207\n" \
"buffer_store_dwordx4 v[200:203], v20, s[56:59], s70 offen offset:1024 sc1\n" \
"v_mfma_f32_16x16x32_bf16 v[76:79], a[148:151], a[84:87], v[76:79]\n" \
"v_cvt_pk_bf16_f32 v208, v208, v209\n" \
"v_mul_f32_e32 v210, v56, v210\n" \
"v_mfma_f32_16x16x32_bf16 v[76:79], a[144:147], a[80:83], v[76:79]\n" \
"buffer_load_dwordx4 v53, s[8:11], s64 offen offset:0 sc1 lds\n" \
"v_mfma_f32_16x16x32_bf16 v[80:83], a[128:131], a[96:99], v[40:43]\n" \
"v_mul_f32_e32 v211, v56, v211\n" \
"v_cvt_pk_bf16_f32 v209, v210, v211\n" \
"v_mfma_f32_16x16x32_bf16 v[80:83], a[132:135], a[100:103], v[80:83]\n" \
"v_rcp_f32_e32 v57, v57\n" \
"v_mfma_f32_16x16x32_bf16 v[80:83], a[136:139], a[104:107], v[80:83]\n" \
"buffer_load_dwordx4 v53, s[8:11], s64 offen offset:1024 sc1 lds\n" \
"v_mfma_f32_16x16x32_bf16 v[80:83], a[140:143], a[108:111], v[80:83]\n" \
"v_mfma_f32_16x16x32_bf16 a[208:211], a[216:219], v[200:203], a[208:211]\n" \
"v_mfma_f32_32x32x16_bf16 v[192:207], a[216:219], a[216:219], 0\n" \
"v_mul_f32_e32 v212, v56, v212\n" \
"v_mul_f32_e32 v213, v56, v213\n" \
"v_mul_f32_e32 v214, v56, v214\n" \
"v_mul_f32_e32 v215, v56, v215\n" \
"v_mul_f32_e32 v216, v56, v216\n" \
"v_mul_f32_e32 v217, v56, v217\n" \
"v_mfma_f32_16x16x32_bf16 v[84:87], a[156:159], a[108:111], v[40:43]\n" \
"v_cvt_pk_bf16_f32 v210, v212, v213\n" \
"v_cvt_pk_bf16_f32 v211, v214, v215\n" \
"buffer_store_dwordx4 v[208:211], v20, s[56:59], s70 offen offset:2048 sc1\n" \
"v_mfma_f32_16x16x32_bf16 v[84:87], a[152:155], a[104:107], v[84:87]\n" \
"v_cvt_pk_bf16_f32 v216, v216, v217\n" \
"v_mul_f32_e32 v218, v56, v218\n" \
"v_mfma_f32_16x16x32_bf16 v[84:87], a[148:151], a[100:103], v[84:87]\n" \
"buffer_load_dwordx4 v53, s[8:11], s64 offen offset:2048 sc1 lds\n" \
"v_mfma_f32_16x16x32_bf16 v[84:87], a[144:147], a[96:99], v[84:87]\n" \
"v_mul_f32_e32 v219, v56, v219\n" \
"v_mul_f32_e32 v57, v57, v61\n" \
"v_mfma_f32_16x16x32_bf16 v[88:91], a[128:131], a[112:115], v[44:47]\n" \
"ds_read_b128 a[128:131], v48 offset:8192\n" \
"v_cvt_pk_bf16_f32 v217, v218, v219\n" \
"v_mfma_f32_16x16x32_bf16 v[88:91], a[132:135], a[116:119], v[88:91]\n" \
"ds_read_b128 a[132:135], v48 offset:9216\n" \
"v_mul_f32_e32 v220, v56, v220\n" \
"v_mfma_f32_16x16x32_bf16 v[88:91], a[136:139], a[120:123], v[88:91]\n" \
"ds_read_b128 a[136:139], v48 offset:10240\n" \
"v_mul_f32_e32 v221, v56, v221\n" \
"v_mfma_f32_16x16x32_bf16 v[88:91], a[140:143], a[124:127], v[88:91]\n" \
"ds_read_b128 a[140:143], v48 offset:11264\n" \
"v_mul_f32_e32 v222, v56, v222\n" \
"v_mfma_f32_16x16x32_bf16 v[92:95], a[156:159], a[124:127], v[44:47]\n" \
"ds_read_b128 a[156:159], v48 offset:15360\n" \
"v_mul_f32_e32 v223, v56, v223\n" \
"v_mfma_f32_16x16x32_bf16 v[92:95], a[152:155], a[120:123], v[92:95]\n" \
"ds_read_b128 a[152:155], v48 offset:14336\n" \
"v_mul_f32_e32 v224, v57, v224\n" \
"v_mfma_f32_16x16x32_bf16 v[92:95], a[148:151], a[116:119], v[92:95]\n" \
"ds_read_b128 a[148:151], v48 offset:13312\n" \
"v_mul_f32_e32 v225, v57, v225\n" \
"v_mfma_f32_16x16x32_bf16 v[92:95], a[144:147], a[112:115], v[92:95]\n" \
"ds_read_b128 a[144:147], v48 offset:12288\n" \
"v_mfma_f32_16x16x32_bf16 a[208:211], a[216:219], v[208:211], a[208:211]\n" \
"v_cvt_pk_bf16_f32 v218, v220, v221\n" \
"v_cvt_pk_bf16_f32 v219, v222, v223\n" \
"buffer_store_dwordx4 v[216:219], v20, s[56:59], s70 offen offset:3072 sc1\n" \
"v_cvt_pk_bf16_f32 v224, v224, v225\n" \
"v_mul_f32_e32 v226, v57, v226\n" \
"v_mul_f32_e32 v227, v57, v227\n" \
"v_mfma_f32_16x16x32_bf16 a[208:211], a[216:219], v[216:219], a[208:211]\n" \
"v_mfma_f32_32x32x16_bf16 v[208:223], a[216:219], a[216:219], 0\n" \
"v_cvt_pk_bf16_f32 v225, v226, v227\n" \
"v_mul_f32_e32 v228, v57, v228\n" \
"v_mul_f32_e32 v229, v57, v229\n" \
"v_mul_f32_e32 v230, v57, v230\n" \
"v_mul_f32_e32 v231, v57, v231\n" \
"v_mul_f32_e32 v232, v57, v232\n" \
"v_mul_f32_e32 v233, v57, v233\n" \
"v_cvt_pk_bf16_f32 v226, v228, v229\n" \
"v_cvt_pk_bf16_f32 v227, v230, v231\n" \
"buffer_store_dwordx4 v[224:227], v20, s[56:59], s71 offen offset:0 sc1\n" \
"v_cvt_pk_bf16_f32 v232, v232, v233\n" \
"v_mul_f32_e32 v234, v57, v234\n" \
"v_mul_f32_e32 v235, v57, v235\n" \
"v_mfma_f32_16x16x32_bf16 a[208:211], a[216:219], v[224:227], a[208:211]\n" \
"v_cvt_pk_bf16_f32 v233, v234, v235\n" \
"v_mul_f32_e32 v236, v57, v236\n" \
"v_mul_f32_e32 v237, v57, v237\n" \
"v_mul_f32_e32 v238, v57, v238\n" \
"v_mul_f32_e32 v239, v57, v239\n" \
"v_mul_f32_e32 v240, v57, v240\n" \
"v_mul_f32_e32 v241, v57, v241\n" \
"v_cvt_pk_bf16_f32 v234, v236, v237\n" \
"v_cvt_pk_bf16_f32 v235, v238, v239\n" \
"buffer_store_dwordx4 v[232:235], v20, s[56:59], s71 offen offset:1024 sc1\n" \
"v_cvt_pk_bf16_f32 v240, v240, v241\n" \
"v_mul_f32_e32 v242, v57, v242\n" \
"v_mul_f32_e32 v243, v57, v243\n" \
"v_mfma_f32_16x16x32_bf16 a[208:211], a[216:219], v[232:235], a[208:211]\n" \
"v_mfma_f32_32x32x16_bf16 v[224:239], a[216:219], a[216:219], 0\n" \
"v_cvt_pk_bf16_f32 v241, v242, v243\n" \
"v_mul_f32_e32 v244, v57, v244\n" \
"v_mul_f32_e32 v245, v57, v245\n" \
"v_mul_f32_e32 v246, v57, v246\n" \
"v_mul_f32_e32 v247, v57, v247\n" \
"v_mul_f32_e32 v248, v57, v248\n" \
"v_mul_f32_e32 v249, v57, v249\n" \
"v_cvt_pk_bf16_f32 v242, v244, v245\n" \
"v_cvt_pk_bf16_f32 v243, v246, v247\n" \
"buffer_store_dwordx4 v[240:243], v20, s[56:59], s71 offen offset:2048 sc1\n" \
"v_cvt_pk_bf16_f32 v248, v248, v249\n" \
"v_mul_f32_e32 v250, v57, v250\n" \
"v_mul_f32_e32 v251, v57, v251\n" \
"v_mfma_f32_16x16x32_bf16 a[208:211], a[216:219], v[240:243], a[208:211]\n" \
"v_cvt_pk_bf16_f32 v249, v250, v251\n" \
"v_mul_f32_e32 v252, v57, v252\n" \
"v_mul_f32_e32 v253, v57, v253\n" \
"v_mul_f32_e32 v254, v57, v254\n" \
"v_mul_f32_e32 v255, v57, v255\n" \
"v_cvt_pk_bf16_f32 v250, v252, v253\n" \
"v_cvt_pk_bf16_f32 v251, v254, v255\n" \
"buffer_store_dwordx4 v[248:251], v20, s[56:59], s71 offen offset:3072 sc1\n" \
"buffer_load_dwordx4 v53, s[8:11], s64 offen offset:3072 sc1 lds\n" \
"v_mfma_f32_16x16x32_bf16 a[208:211], a[216:219], v[248:251], a[208:211]\n" \
"v_mfma_f32_32x32x16_bf16 v[240:255], a[216:219], a[216:219], 0\n" \
"v_cmp_lt_f32_e64 s[72:73], s100, v62\n" \
"v_exp_f32_e32 v64, v64\n" \
"v_exp_f32_e32 v65, v65\n" \
"s_xor_b32 s65, s65, 0x8000\n" \
"s_waitcnt lgkmcnt(0)\n" \
"v_exp_f32_e32 v66, v66\n" \
"v_exp_f32_e32 v67, v67\n" \
"v_accvgpr_read_b32 v63, a208\n" \
"v_cmp_u_f32_e32 vcc, v63, v63\n" \
"v_exp_f32_e32 v68, v68\n" \
"s_or_b64 vcc, s[72:73], vcc\n" \
"s_cselect_b32 s95, 0x40000000, s95\n" \
"s_cselect_b32 s94, 0x7f800000, 0\n" \
"v_exp_f32_e32 v69, v69\n" \
"v_exp_f32_e32 v70, v70\n" \
"v_exp_f32_e32 v71, v71\n" \
"v_exp_f32_e32 v72, v72\n" \
"v_exp_f32_e32 v73, v73\n" \
"v_exp_f32_e32 v74, v74\n" \
"v_exp_f32_e32 v75, v75\n" \
"v_exp_f32_e32 v76, v76\n" \
"v_exp_f32_e32 v77, v77\n" \
"v_exp_f32_e32 v78, v78\n" \
"v_exp_f32_e32 v79, v79\n" \
"v_exp_f32_e32 v80, v80\n" \
"v_exp_f32_e32 v81, v81\n" \
"v_exp_f32_e32 v82, v82\n" \
"v_exp_f32_e32 v83, v83\n" \
"v_exp_f32_e32 v84, v84\n" \
"v_exp_f32_e32 v85, v85\n" \
"v_exp_f32_e32 v86, v86\n" \
"v_exp_f32_e32 v87, v87\n" \
"s_add_u32 s64, s64, 0x10000\n" \
"v_exp_f32_e32 v88, v88\n" \
"v_exp_f32_e32 v89, v89\n" \
"v_exp_f32_e32 v90, v90\n" \
"v_exp_f32_e32 v91, v91\n" \
"v_cvt_pk_bf16_f32 v96, v64, v65\n" \
"v_cvt_pk_bf16_f32 v97, v66, v67\n" \
"v_cvt_pk_bf16_f32 v98, v68, v69\n" \
"v_cvt_pk_bf16_f32 v99, v70, v71\n" \
"v_cvt_pk_bf16_f32 v104, v72, v73\n" \
"v_cvt_pk_bf16_f32 v105, v74, v75\n" \
"v_cvt_pk_bf16_f32 v106, v76, v77\n" \
"v_cvt_pk_bf16_f32 v107, v78, v79\n" \
"v_cvt_pk_bf16_f32 v112, v80, v81\n" \
"v_cvt_pk_bf16_f32 v113, v82, v83\n" \
"v_cvt_pk_bf16_f32 v114, v84, v85\n" \
"v_cvt_pk_bf16_f32 v115, v86, v87\n" \
"v_xor_b32_e32 v48, 0x8000, v48\n" \
"s_sub_u32 s66, s19, 1\n" \
"s_cbranch_scc0 L_2_%=\n" \
"s_waitcnt vmcnt(16)\n" \
"s_barrier\n" \
"L_f1_%=:\n" \
"L_pf17:\n" \
"s_cbranch_execz L_retA\n" \
"s_nop 0\n" \
"ds_read_b128 a[160:163], v49 offset:0\n" \
"ds_read_b128 a[164:167], v49 offset:1024\n" \
"ds_read_b128 a[168:171], v49 offset:2048\n" \
"ds_read_b128 a[172:175], v49 offset:3072\n" \
"ds_read_b128 a[176:179], v49 offset:8192\n" \
"ds_read_b128 a[180:183], v49 offset:9216\n" \
"v_mfma_f32_16x16x32_bf16 v[72:75], a[128:131], a[80:83], v[36:39]\n" \
"s_add_u32 s68, s61, 0x0\n" \
"s_add_u32 s69, s61, 0x49000\n" \
"ds_read_b128 a[184:187], v49 offset:10240\n" \
"v_mfma_f32_16x16x32_bf16 v[72:75], a[132:135], a[84:87], v[72:75]\n" \
"ds_read_b128 a[188:191], v49 offset:11264\n" \
"v_add_u32_e32 v56, 0x49000, v20\n" \
"v_mfma_f32_16x16x32_bf16 v[72:75], a[136:139], a[88:91], v[72:75]\n" \
"v_readlane_b32 s97, v11, 32\n" \
"v_readlane_b32 s98, v11, 33\n" \
"v_mfma_f32_16x16x32_bf16 v[72:75], a[140:143], a[92:95], v[72:75]\n" \
"v_and_b32_e32 v96, v12, v96\n" \
"v_and_b32_e32 v97, v13, v97\n" \
"v_mfma_f32_16x16x32_bf16 v[76:79], a[156:159], a[92:95], v[36:39]\n" \
"buffer_load_dwordx4 a[32:35], v20, s[56:59], s97 offen offset:0 sc1\n" \
"v_mfma_f32_16x16x32_bf16 v[76:79], a[152:155], a[88:91], v[76:79]\n" \
"v_and_b32_e32 v98, v14, v98\n" \
"v_and_b32_e32 v99, v15, v99\n" \
"v_mfma_f32_16x16x32_bf16 v[76:79], a[148:151], a[84:87], v[76:79]\n" \
"ds_read_b128 a[0:3], v20 offset:8192\n" \
"v_mov_b32_e32 v61, s62\n" \
"v_mfma_f32_16x16x32_bf16 v[76:79], a[144:147], a[80:83], v[76:79]\n" \
"buffer_load_dwordx4 a[40:43], v56, s[56:59], s97 offen offset:0 sc1\n" \
"ds_read_b128 a[4:7], v20 offset:9216\n" \
"ds_read_b128 a[8:11], v20 offset:10240\n" \
"s_waitcnt lgkmcnt(10)\n" \
"v_mfma_f32_16x16x32_bf16 v[128:131], a[160:163], v[96:99], v[128:131]\n" \
"ds_read_b128 a[12:15], v20 offset:11264\n" \
"s_waitcnt lgkmcnt(10)\n" \
"v_mfma_f32_16x16x32_bf16 v[132:135], a[164:167], v[96:99], v[132:135]\n" \
"ds_read_b128 a[28:31], v20 offset:15360\n" \
"buffer_load_dwordx4 a[48:51], v20, s[56:59], s98 offen offset:0 sc1\n" \
"s_waitcnt lgkmcnt(10)\n" \
"v_mfma_f32_16x16x32_bf16 v[136:139], a[168:171], v[96:99], v[136:139]\n" \
"ds_read_b128 a[24:27], v20 offset:14336\n" \
"s_waitcnt lgkmcnt(10)\n" \
"v_mfma_f32_16x16x32_bf16 v[140:143], a[172:175], v[96:99], v[140:143]\n" \
"ds_read_b128 a[20:23], v20 offset:13312\n" \
"buffer_load_dwordx4 a[56:59], v56, s[56:59], s98 offen offset:0 sc1\n" \
"s_waitcnt lgkmcnt(9)\n" \
"v_mfma_f32_16x16x32_bf16 v[144:147], a[176:179], v[96:99], v[144:147]\n" \
"ds_read_b128 a[16:19], v20 offset:12288\n" \
"v_add_u32_e32 v50, s15, v48\n" \
"v_mfma_f32_16x16x32_bf16 v[148:151], a[180:183], v[96:99], v[148:151]\n" \
"v_xor_b32_e32 v48, 0x4000, v49\n" \
"s_waitcnt lgkmcnt(8)\n" \
"v_mfma_f32_16x16x32_bf16 v[152:155], a[184:187], v[96:99], v[152:155]\n" \
"buffer_load_dwordx4 a[36:39], v20, s[56:59], s97 offen offset:1024 sc1\n" \
"v_mfma_f32_16x16x32_bf16 v[156:159], a[188:191], v[96:99], v[156:159]\n" \
"ds_read_b32 v58, v18 offset:256\n" \
"ds_read_b32 v59, v18 offset:384\n" \
"v_mfma_f32_16x16x32_bf16 a[192:195], a[252:255], v[96:99], a[192:195]\n" \
"ds_read_b128 a[96:99], v48 offset:4096\n" \
"ds_read_b128 a[100:103], v48 offset:5120\n" \
"buffer_load_dwordx4 a[44:47], v56, s[56:59], s97 offen offset:1024 sc1\n" \
"v_mfma_f32_16x16x32_bf16 v[160:163], a[160:163], v[104:107], v[160:163]\n" \
"ds_read_b128 a[104:107], v48 offset:6144\n" \
"ds_read_b128 a[108:111], v48 offset:7168\n" \
"v_mfma_f32_16x16x32_bf16 v[164:167], a[164:167], v[104:107], v[164:167]\n" \
"ds_read_b128 a[112:115], v48 offset:12288\n" \
"ds_read_b128 a[116:119], v48 offset:13312\n" \
"buffer_load_dwordx4 a[52:55], v20, s[56:59], s98 offen offset:1024 sc1\n" \
"v_mfma_f32_16x16x32_bf16 v[168:171], a[168:171], v[104:107], v[168:171]\n" \
"ds_read_b128 a[120:123], v48 offset:14336\n" \
"ds_read_b128 a[124:127], v48 offset:15360\n" \
"v_mfma_f32_16x16x32_bf16 v[172:175], a[172:175], v[104:107], v[172:175]\n" \
"ds_read_b128 a[128:131], v49 offset:4096\n" \
"ds_read_b128 a[132:135], v49 offset:5120\n" \
"buffer_load_dwordx4 a[60:63], v56, s[56:59], s98 offen offset:1024 sc1\n" \
"v_mfma_f32_16x16x32_bf16 v[176:179], a[176:179], v[104:107], v[176:179]\n" \
"ds_read_b128 a[136:139], v49 offset:6144\n" \
"ds_read_b128 a[140:143], v49 offset:7168\n" \
"v_mfma_f32_16x16x32_bf16 v[180:183], a[180:183], v[104:107], v[180:183]\n" \
"ds_read_b128 a[144:147], v49 offset:12288\n" \
"ds_read_b128 a[148:151], v49 offset:13312\n" \
"s_waitcnt vmcnt(8)\n" \
"s_mov_b64 exec, 1\n" \
"buffer_store_dword v61, off, s[56:59], s95 offset:0 sc1\n" \
"s_mov_b64 exec, -1\n" \
"v_mfma_f32_16x16x32_bf16 v[184:187], a[184:187], v[104:107], v[184:187]\n" \
"ds_read_b128 a[152:155], v49 offset:14336\n" \
"ds_read_b128 a[156:159], v49 offset:15360\n" \
"v_mfma_f32_16x16x32_bf16 v[188:191], a[188:191], v[104:107], v[188:191]\n" \
"v_accvgpr_read_b32 v40, a200\n" \
"v_accvgpr_read_b32 v23, a201\n" \
"v_mfma_f32_16x16x32_bf16 a[196:199], a[252:255], v[104:107], a[196:199]\n" \
"v_add_f32_e32 v40, v40, v23\n" \
"s_waitcnt lgkmcnt(15)\n" \
"v_mfma_f32_16x16x32_bf16 v[80:83], a[0:3], a[64:67], v[32:35]\n" \
"buffer_load_dwordx4 a[220:223], v20, s[56:59], s60 offen offset:0 sc1\n" \
"v_mfma_f32_16x16x32_bf16 v[80:83], a[4:7], a[68:71], v[80:83]\n" \
"v_accvgpr_read_b32 v41, a204\n" \
"v_accvgpr_read_b32 v23, a205\n" \
"v_mfma_f32_16x16x32_bf16 v[80:83], a[8:11], a[72:75], v[80:83]\n" \
"v_add_f32_e32 v41, v41, v23\n" \
"v_add_u32_e32 v51, 0x10000, v50\n" \
"v_mfma_f32_16x16x32_bf16 v[80:83], a[12:15], a[76:79], v[80:83]\n" \
"buffer_load_dwordx4 a[236:239], v56, s[56:59], s60 offen offset:0 sc1\n" \
"v_mfma_f32_16x16x32_bf16 v[84:87], a[28:31], a[76:79], v[32:35]\n" \
"v_add_f32_e32 v40, s94, v40\n" \
"v_add_f32_e32 v41, s94, v41\n" \
"v_mfma_f32_16x16x32_bf16 v[84:87], a[24:27], a[72:75], v[84:87]\n" \
"ds_write_b128 v50, v[192:195] offset:0\n" \
"buffer_load_dwordx4 a[224:227], v20, s[56:59], s60 offen offset:1024 sc1\n" \
"v_mfma_f32_16x16x32_bf16 v[84:87], a[20:23], a[68:71], v[84:87]\n" \
"ds_write_b128 v50, v[196:199] offset:1024\n" \
"v_mfma_f32_16x16x32_bf16 v[84:87], a[16:19], a[64:67], v[84:87]\n" \
"ds_write_b128 v50, v[200:203] offset:2048\n" \
"buffer_load_dwordx4 a[240:243], v56, s[56:59], s60 offen offset:1024 sc1\n" \
"v_mfma_f32_16x16x32_bf16 v[88:91], a[0:3], a[80:83], v[36:39]\n" \
"ds_write_b128 v50, v[204:207] offset:3072\n" \
"v_mfma_f32_16x16x32_bf16 v[88:91], a[4:7], a[84:87], v[88:91]\n" \
"ds_write_b128 v50, v[208:211] offset:8192\n" \
"buffer_load_dwordx4 a[228:231], v20, s[56:59], s60 offen offset:2048 sc1\n" \
"L_pf18:\n" \
"s_cbranch_execz L_retA\n" \
"s_nop 0\n" \
"v_mfma_f32_16x16x32_bf16 v[88:91], a[8:11], a[88:91], v[88:91]\n" \
"ds_write_b128 v50, v[212:215] offset:9216\n" \
"v_mfma_f32_16x16x32_bf16 v[88:91], a[12:15], a[92:95], v[88:91]\n" \
"ds_write_b128 v50, v[216:219] offset:10240\n" \
"buffer_load_dwordx4 a[244:247], v56, s[56:59], s60 offen offset:2048 sc1\n" \
"v_mfma_f32_16x16x32_bf16 v[92:95], a[28:31], a[92:95], v[36:39]\n" \
"ds_write_b128 v50, v[220:223] offset:11264\n" \
"ds_write_b128 v51, v[224:227] offset:0\n" \
"ds_write_b128 v51, v[228:231] offset:1024\n" \
"buffer_load_dwordx4 a[232:235], v20, s[56:59], s60 offen offset:3072 sc1\n" \
"ds_write_b128 v51, v[232:235] offset:2048\n" \
"ds_write_b128 v51, v[236:239] offset:3072\n" \
"ds_write_b128 v51, v[240:243] offset:8192\n" \
"ds_write_b128 v51, v[244:247] offset:9216\n" \
"buffer_load_dwordx4 a[248:251], v56, s[56:59], s60 offen offset:3072 sc1\n" \
"ds_write_b128 v51, v[248:251] offset:10240\n" \
"ds_write_b128 v51, v[252:255] offset:11264\n" \
"ds_write_b64 v19, v[40:41]\n" \
"v_mfma_f32_16x16x32_bf16 v[92:95], a[24:27], a[88:91], v[92:95]\n" \
"v_xor_b32_e32 v52, 0x4000, v50\n" \
"v_xor_b32_e32 v53, 0x4000, v51\n" \
"v_mfma_f32_16x16x32_bf16 v[92:95], a[20:23], a[84:87], v[92:95]\n" \
"v_exp_f32_e32 v72, v72\n" \
"v_mfma_f32_16x16x32_bf16 v[92:95], a[16:19], a[80:83], v[92:95]\n" \
"v_exp_f32_e32 v73, v73\n" \
"v_exp_f32_e32 v74, v74\n" \
"v_exp_f32_e32 v75, v75\n" \
"v_exp_f32_e32 v76, v76\n" \
"v_exp_f32_e32 v77, v77\n" \
"v_exp_f32_e32 v78, v78\n" \
"v_exp_f32_e32 v79, v79\n" \
"v_cvt_pk_bf16_f32 v108, v72, v73\n" \
"v_cvt_pk_bf16_f32 v109, v74, v75\n" \
"v_cvt_pk_bf16_f32 v110, v76, v77\n" \
"v_cvt_pk_bf16_f32 v111, v78, v79\n" \
"v_and_b32_e32 v108, v12, v108\n" \
"v_and_b32_e32 v109, v13, v109\n" \
"v_and_b32_e32 v110, v14, v110\n" \
"v_and_b32_e32 v111, v15, v111\n" \
"v_readlane_b32 s93, v11, 18\n" \
"v_readlane_b32 s84, v11, 13\n" \
"v_readlane_b32 s85, v11, 14\n" \
"v_readlane_b32 s86, v11, 15\n" \
"s_mov_b32 s87, 0x20000\n" \
"v_readlane_b32 s92, v11, 16\n" \
"s_add_u32 m0, s93, 0x0\n" \
"v_xor_b32_e32 v63, 0x400, v19\n" \
"s_waitcnt lgkmcnt(0)\n" \
"s_barrier\n" \
"ds_read_b128 v[192:195], v52 offset:0\n" \
"ds_read_b128 v[196:199], v52 offset:1024\n" \
"ds_read_b128 v[200:203], v52 offset:2048\n" \
"ds_read_b128 v[204:207], v52 offset:3072\n" \
"ds_read_b128 v[208:211], v52 offset:8192\n" \
"ds_read_b128 v[212:215], v52 offset:9216\n" \
"ds_read_b128 v[216:219], v52 offset:10240\n" \
"ds_read_b128 v[220:223], v52 offset:11264\n" \
"ds_read_b128 v[224:227], v53 offset:0\n" \
"ds_read_b128 v[228:231], v53 offset:1024\n" \
"ds_read_b128 v[232:235], v53 offset:2048\n" \
"ds_read_b128 v[236:239], v53 offset:3072\n" \
"ds_read_b128 v[240:243], v53 offset:8192\n" \
"ds_read_b128 v[244:247], v53 offset:9216\n" \
"ds_read_b128 v[248:251], v53 offset:10240\n" \
"ds_read_b128 v[252:255], v53 offset:11264\n" \
"v_mfma_f32_16x16x32_bf16 v[160:163], a[128:131], v[108:111], v[160:163]\n" \
"buffer_load_dwordx4 v0, s[84:87], s92 offen offset:0 lds\n" \
"v_mfma_f32_16x16x32_bf16 v[164:167], a[132:135], v[108:111], v[164:167]\n" \
"v_exp_f32_e32 v80, v80\n" \
"v_mfma_f32_16x16x32_bf16 v[168:171], a[136:139], v[108:111], v[168:171]\n" \
"v_exp_f32_e32 v81, v81\n" \
"v_mfma_f32_16x16x32_bf16 v[172:175], a[140:143], v[108:111], v[172:175]\n" \
"buffer_load_dwordx4 v1, s[84:87], s92 offen offset:1024 lds\n" \
"v_mfma_f32_16x16x32_bf16 v[176:179], a[144:147], v[108:111], v[176:179]\n" \
"ds_read_b64 v[42:43], v63\n" \
"v_cvt_pk_bf16_f32 v116, v80, v81\n" \
"v_mfma_f32_16x16x32_bf16 v[180:183], a[148:151], v[108:111], v[180:183]\n" \
"v_exp_f32_e32 v82, v82\n" \
"v_mfma_f32_16x16x32_bf16 v[184:187], a[152:155], v[108:111], v[184:187]\n" \
"buffer_load_dwordx4 v2, s[84:87], s92 offen offset:2048 lds\n" \
"v_mfma_f32_16x16x32_bf16 v[188:191], a[156:159], v[108:111], v[188:191]\n" \
"v_exp_f32_e32 v83, v83\n" \
"v_mfma_f32_16x16x32_bf16 a[196:199], a[252:255], v[108:111], a[196:199]\n" \
"v_exp_f32_e32 v84, v84\n" \
"v_exp_f32_e32 v85, v85\n" \
"v_exp_f32_e32 v86, v86\n" \
"s_waitcnt lgkmcnt(1)\n" \
"s_barrier\n" \
"buffer_load_dwordx4 v3, s[84:87], s92 offen offset:3072 lds\n" \
"s_add_u32 m0, s93, 0x1000\n" \
"v_exp_f32_e32 v87, v87\n" \
"v_cvt_pk_bf16_f32 v117, v82, v83\n" \
"v_cvt_pk_bf16_f32 v118, v84, v85\n" \
"v_cvt_pk_bf16_f32 v119, v86, v87\n" \
"v_exp_f32_e32 v88, v88\n" \
"v_exp_f32_e32 v89, v89\n" \
"v_mfma_f32_16x16x32_bf16 v[128:131], a[96:99], v[116:119], v[128:131]\n" \
"buffer_load_dwordx4 v4, s[84:87], s92 offen offset:0 lds\n" \
"v_mfma_f32_16x16x32_bf16 v[132:135], a[100:103], v[116:119], v[132:135]\n" \
"v_exp_f32_e32 v90, v90\n" \
"v_mfma_f32_16x16x32_bf16 v[136:139], a[104:107], v[116:119], v[136:139]\n" \
"v_exp_f32_e32 v91, v91\n" \
"v_mfma_f32_16x16x32_bf16 v[140:143], a[108:111], v[116:119], v[140:143]\n" \
"buffer_load_dwordx4 v5, s[84:87], s92 offen offset:1024 lds\n" \
"v_mfma_f32_16x16x32_bf16 v[144:147], a[112:115], v[116:119], v[144:147]\n" \
"v_exp_f32_e32 v92, v92\n" \
"v_mfma_f32_16x16x32_bf16 v[148:151], a[116:119], v[116:119], v[148:151]\n" \
"v_exp_f32_e32 v93, v93\n" \
"v_mfma_f32_16x16x32_bf16 v[152:155], a[120:123], v[116:119], v[152:155]\n" \
"buffer_load_dwordx4 v6, s[84:87], s92 offen offset:2048 lds\n" \
"v_mfma_f32_16x16x32_bf16 v[156:159], a[124:127], v[116:119], v[156:159]\n" \
"v_exp_f32_e32 v94, v94\n" \
"v_mfma_f32_16x16x32_bf16 a[192:195], a[252:255], v[116:119], a[192:195]\n" \
"v_exp_f32_e32 v95, v95\n" \
"v_cvt_pk_bf16_f32 v124, v88, v89\n" \
"v_cvt_pk_bf16_f32 v125, v90, v91\n" \
"v_cvt_pk_bf16_f32 v126, v92, v93\n" \
"v_cvt_pk_bf16_f32 v127, v94, v95\n" \
"v_add_f32_e32 v160, v160, v224\n" \
"v_add_f32_e32 v161, v161, v225\n" \
"v_add_f32_e32 v162, v162, v226\n" \
"v_add_f32_e32 v163, v163, v227\n" \
"v_add_f32_e32 v164, v164, v228\n" \
"v_add_f32_e32 v165, v165, v229\n" \
"v_mfma_f32_16x16x32_bf16 v[160:163], a[96:99], v[124:127], v[160:163]\n" \
"buffer_load_dwordx4 v7, s[84:87], s92 offen offset:3072 lds\n" \
"s_add_u32 m0, s93, 0x2000\n" \
"v_add_f32_e32 v166, v166, v230\n" \
"v_add_f32_e32 v167, v167, v231\n" \
"v_add_f32_e32 v168, v168, v232\n" \
"v_add_f32_e32 v169, v169, v233\n" \
"v_mfma_f32_16x16x32_bf16 v[164:167], a[100:103], v[124:127], v[164:167]\n" \
"v_add_f32_e32 v170, v170, v234\n" \
"v_add_f32_e32 v171, v171, v235\n" \
"v_add_f32_e32 v172, v172, v236\n" \
"v_add_f32_e32 v173, v173, v237\n" \
"v_mfma_f32_16x16x32_bf16 v[168:171], a[104:107], v[124:127], v[168:171]\n" \
"buffer_load_dwordx4 v8, s[84:87], s92 offen offset:0 lds\n" \
"v_add_f32_e32 v174, v174, v238\n" \
"v_add_f32_e32 v175, v175, v239\n" \
"v_accvgpr_read_b32 v54, a192\n" \
"v_accvgpr_read_b32 v23, a193\n" \
"v_mfma_f32_16x16x32_bf16 v[172:175], a[108:111], v[124:127], v[172:175]\n" \
"v_add_f32_e32 v54, v54, v23\n" \
"s_waitcnt lgkmcnt(0)\n" \
"v_add_f32_e32 v54, v54, v42\n" \
"v_max_f32_e32 v62, v62, v54\n" \
"v_add_f32_e32 v176, v176, v240\n" \
"v_add_f32_e32 v177, v177, v241\n" \
"v_add_f32_e32 v178, v178, v242\n" \
"v_add_f32_e32 v179, v179, v243\n" \
"v_rcp_f32_e32 v54, v54\n" \
"v_add_f32_e32 v128, v128, v192\n" \
"v_mfma_f32_16x16x32_bf16 v[176:179], a[112:115], v[124:127], v[176:179]\n" \
"v_add_f32_e32 v129, v129, v193\n" \
"v_add_f32_e32 v130, v130, v194\n" \
"v_add_f32_e32 v131, v131, v195\n" \
"v_add_f32_e32 v132, v132, v196\n" \
"v_add_f32_e32 v133, v133, v197\n" \
"v_add_f32_e32 v134, v134, v198\n" \
"v_add_f32_e32 v135, v135, v199\n" \
"v_mul_f32_e32 v54, v54, v58\n" \
"v_mul_f32_e32 v128, v54, v128\n" \
"v_mul_f32_e32 v129, v54, v129\n" \
"v_mul_f32_e32 v130, v54, v130\n" \
"v_mul_f32_e32 v131, v54, v131\n" \
"v_mul_f32_e32 v132, v54, v132\n" \
"v_mul_f32_e32 v133, v54, v133\n" \
"v_mul_f32_e32 v134, v54, v134\n" \
"v_mul_f32_e32 v135, v54, v135\n" \
"v_add_f32_e32 v180, v180, v244\n" \
"v_add_f32_e32 v181, v181, v245\n" \
"v_add_f32_e32 v182, v182, v246\n" \
"v_add_f32_e32 v183, v183, v247\n" \
"v_add_f32_e32 v136, v136, v200\n" \
"v_add_f32_e32 v137, v137, v201\n" \
"v_add_f32_e32 v138, v138, v202\n" \
"v_add_f32_e32 v139, v139, v203\n" \
"v_add_f32_e32 v140, v140, v204\n" \
"v_add_f32_e32 v141, v141, v205\n" \
"v_add_f32_e32 v142, v142, v206\n" \
"v_add_f32_e32 v143, v143, v207\n" \
"v_cvt_pk_bf16_f32 v64, v128, v129\n" \
"v_cvt_pk_bf16_f32 v65, v130, v131\n" \
"v_cvt_pk_bf16_f32 v66, v132, v133\n" \
"v_cvt_pk_bf16_f32 v67, v134, v135\n" \
"buffer_store_dwordx4 v[64:67], v20, s[56:59], s68 offen offset:0 sc1\n" \
"v_mfma_f32_16x16x32_bf16 v[180:183], a[116:119], v[124:127], v[180:183]\n" \
"v_mfma_f32_16x16x32_bf16 a[208:211], a[216:219], v[64:67], a[208:211]\n" \
"v_mul_f32_e32 v136, v54, v136\n" \
"v_mul_f32_e32 v137, v54, v137\n" \
"v_mul_f32_e32 v138, v54, v138\n" \
"v_mul_f32_e32 v139, v54, v139\n" \
"v_mul_f32_e32 v140, v54, v140\n" \
"v_mul_f32_e32 v141, v54, v141\n" \
"v_mul_f32_e32 v142, v54, v142\n" \
"v_mul_f32_e32 v143, v54, v143\n" \
"v_add_f32_e32 v144, v144, v208\n" \
"v_add_f32_e32 v145, v145, v209\n" \
"v_add_f32_e32 v146, v146, v210\n" \
"v_add_f32_e32 v147, v147, v211\n" \
"v_add_f32_e32 v148, v148, v212\n" \
"v_add_f32_e32 v149, v149, v213\n" \
"v_add_f32_e32 v150, v150, v214\n" \
"v_add_f32_e32 v151, v151, v215\n" \
"v_cvt_pk_bf16_f32 v68, v136, v137\n" \
"v_cvt_pk_bf16_f32 v69, v138, v139\n" \
"v_cvt_pk_bf16_f32 v70, v140, v141\n" \
"v_cvt_pk_bf16_f32 v71, v142, v143\n" \
"buffer_store_dwordx4 v[68:71], v20, s[56:59], s68 offen offset:1024 sc1\n" \
"v_mul_f32_e32 v144, v54, v144\n" \
"v_mfma_f32_16x16x32_bf16 a[208:211], a[216:219], v[68:71], a[208:211]\n" \
"v_mul_f32_e32 v145, v54, v145\n" \
"v_mul_f32_e32 v146, v54, v146\n" \
"v_mul_f32_e32 v147, v54, v147\n" \
"v_mul_f32_e32 v148, v54, v148\n" \
"v_mul_f32_e32 v149, v54, v149\n" \
"v_mul_f32_e32 v150, v54, v150\n" \
"v_mul_f32_e32 v151, v54, v151\n" \
"v_add_f32_e32 v184, v184, v248\n" \
"v_add_f32_e32 v185, v185, v249\n" \
"v_add_f32_e32 v186, v186, v250\n" \
"v_add_f32_e32 v187, v187, v251\n" \
"v_add_f32_e32 v152, v152, v216\n" \
"v_add_f32_e32 v153, v153, v217\n" \
"v_add_f32_e32 v154, v154, v218\n" \
"v_add_f32_e32 v155, v155, v219\n" \
"v_add_f32_e32 v156, v156, v220\n" \
"v_add_f32_e32 v157, v157, v221\n" \
"v_add_f32_e32 v158, v158, v222\n" \
"v_add_f32_e32 v159, v159, v223\n" \
"v_cvt_pk_bf16_f32 v72, v144, v145\n" \
"v_cvt_pk_bf16_f32 v73, v146, v147\n" \
"v_cvt_pk_bf16_f32 v74, v148, v149\n" \
"v_cvt_pk_bf16_f32 v75, v150, v151\n" \
"buffer_store_dwordx4 v[72:75], v20, s[56:59], s68 offen offset:2048 sc1\n" \
"v_mfma_f32_16x16x32_bf16 v[184:187], a[120:123], v[124:127], v[184:187]\n" \
"v_mfma_f32_16x16x32_bf16 a[208:211], a[216:219], v[72:75], a[208:211]\n" \
"v_mul_f32_e32 v152, v54, v152\n" \
"v_mul_f32_e32 v153, v54, v153\n" \
"v_mul_f32_e32 v154, v54, v154\n" \
"v_mul_f32_e32 v155, v54, v155\n" \
"v_mul_f32_e32 v156, v54, v156\n" \
"v_mul_f32_e32 v157, v54, v157\n" \
"v_mul_f32_e32 v158, v54, v158\n" \
"v_mul_f32_e32 v159, v54, v159\n" \
"v_cvt_pk_bf16_f32 v76, v152, v153\n" \
"v_cvt_pk_bf16_f32 v77, v154, v155\n" \
"v_cvt_pk_bf16_f32 v78, v156, v157\n" \
"v_cvt_pk_bf16_f32 v79, v158, v159\n" \
"buffer_store_dwordx4 v[76:79], v20, s[56:59], s68 offen offset:3072 sc1\n" \
"v_add_f32_e32 v188, v188, v252\n" \
"v_mfma_f32_16x16x32_bf16 a[208:211], a[216:219], v[76:79], a[208:211]\n" \
"v_add_f32_e32 v189, v189, v253\n" \
"v_add_f32_e32 v190, v190, v254\n" \
"v_add_f32_e32 v191, v191, v255\n" \
"s_nop 1\n" \
"v_mfma_f32_16x16x32_bf16 v[188:191], a[124:127], v[124:127], v[188:191]\n" \
"v_mfma_f32_16x16x32_bf16 a[196:199], a[252:255], v[124:127], a[196:199]\n" \
"s_nop 7\n" \
"v_accvgpr_read_b32 v55, a196\n" \
"v_accvgpr_read_b32 v23, a197\n" \
"v_add_f32_e32 v55, v55, v23\n" \
"v_add_f32_e32 v55, v55, v43\n" \
"v_max_f32_e32 v62, v62, v55\n" \
"v_rcp_f32_e32 v55, v55\n" \
"v_cmp_lt_f32_e64 s[72:73], s100, v62\n" \
"v_mul_f32_e32 v55, v55, v59\n" \
"v_mul_f32_e32 v160, v55, v160\n" \
"v_mul_f32_e32 v161, v55, v161\n" \
"v_mul_f32_e32 v162, v55, v162\n" \
"v_mul_f32_e32 v163, v55, v163\n" \
"v_mul_f32_e32 v164, v55, v164\n" \
"v_mul_f32_e32 v165, v55, v165\n" \
"v_mul_f32_e32 v166, v55, v166\n" \
"v_mul_f32_e32 v167, v55, v167\n" \
"v_cvt_pk_bf16_f32 v80, v160, v161\n" \
"v_cvt_pk_bf16_f32 v81, v162, v163\n" \
"v_cvt_pk_bf16_f32 v82, v164, v165\n" \
"v_cvt_pk_bf16_f32 v83, v166, v167\n" \
"buffer_store_dwordx4 v[80:83], v20, s[56:59], s69 offen offset:0 sc1\n" \
"v_mul_f32_e32 v168, v55, v168\n" \
"v_mfma_f32_16x16x32_bf16 a[208:211], a[216:219], v[80:83], a[208:211]\n" \
"v_mul_f32_e32 v169, v55, v169\n" \
"v_mul_f32_e32 v170, v55, v170\n" \
"v_mul_f32_e32 v171, v55, v171\n" \
"v_mul_f32_e32 v172, v55, v172\n" \
"v_mul_f32_e32 v173, v55, v173\n" \
"v_mul_f32_e32 v174, v55, v174\n" \
"v_mul_f32_e32 v175, v55, v175\n" \
"v_cvt_pk_bf16_f32 v84, v168, v169\n" \
"v_cvt_pk_bf16_f32 v85, v170, v171\n" \
"v_cvt_pk_bf16_f32 v86, v172, v173\n" \
"v_cvt_pk_bf16_f32 v87, v174, v175\n" \
"buffer_store_dwordx4 v[84:87], v20, s[56:59], s69 offen offset:1024 sc1\n" \
"v_mul_f32_e32 v176, v55, v176\n" \
"v_mfma_f32_16x16x32_bf16 a[208:211], a[216:219], v[84:87], a[208:211]\n" \
"v_mul_f32_e32 v177, v55, v177\n" \
"v_mul_f32_e32 v178, v55, v178\n" \
"v_mul_f32_e32 v179, v55, v179\n" \
"v_mul_f32_e32 v180, v55, v180\n" \
"v_mul_f32_e32 v181, v55, v181\n" \
"v_mul_f32_e32 v182, v55, v182\n" \
"v_mul_f32_e32 v183, v55, v183\n" \
"v_cvt_pk_bf16_f32 v96, v176, v177\n" \
"v_cvt_pk_bf16_f32 v97, v178, v179\n" \
"v_cvt_pk_bf16_f32 v98, v180, v181\n" \
"v_cvt_pk_bf16_f32 v99, v182, v183\n" \
"buffer_store_dwordx4 v[96:99], v20, s[56:59], s69 offen offset:2048 sc1\n" \
"v_mul_f32_e32 v184, v55, v184\n" \
"v_mfma_f32_16x16x32_bf16 a[208:211], a[216:219], v[96:99], a[208:211]\n" \
"v_mul_f32_e32 v185, v55, v185\n" \
"v_mul_f32_e32 v186, v55, v186\n" \
"v_mul_f32_e32 v187, v55, v187\n" \
"v_mul_f32_e32 v188, v55, v188\n" \
"v_mul_f32_e32 v189, v55, v189\n" \
"v_mul_f32_e32 v190, v55, v190\n" \
"v_mul_f32_e32 v191, v55, v191\n" \
"v_cvt_pk_bf16_f32 v100, v184, v185\n" \
"v_cvt_pk_bf16_f32 v101, v186, v187\n" \
"v_cvt_pk_bf16_f32 v102, v188, v189\n" \
"v_cvt_pk_bf16_f32 v103, v190, v191\n" \
"buffer_store_dwordx4 v[100:103], v20, s[56:59], s69 offen offset:3072 sc1\n" \
"s_nop 0\n" \
"v_mfma_f32_16x16x32_bf16 a[208:211], a[216:219], v[100:103], a[208:211]\n" \
"s_nop 7\n" \
"v_accvgpr_read_b32 v63, a208\n" \
"v_cmp_u_f32_e32 vcc, v63, v63\n" \
"s_nop 0\n" \
"s_or_b64 vcc, s[72:73], vcc\n" \
"s_cbranch_scc1 L_end_%=\n" \
"L_out_%=:\n" \
"L_pf19:\n" \
"s_cbranch_execz L_retA\n" \
"s_nop 0\n" \
"v_add_u32_e32 v34, 0x20000, v10\n" \
"v_mov_b32_e32 v39, s62\n" \
"v_readlane_b32 s70, v11, 4\n" \
"v_bfe_u32 v38, v20, 2, 4\n" \
"v_readlane_b32 s64, v11, 1\n" \
"v_readlane_b32 s71, v11, 3\n" \
"v_add_u32_e32 v33, 0xfc00, v10\n" \
"v_readlane_b32 s99, v11, 19\n" \
"v_readlane_b32 s80, v11, 9\n" \
"v_readlane_b32 s81, v11, 10\n" \
"s_mov_b32 s82, 0x47fca0\n" \
"s_mov_b32 s83, 0x20000\n" \
"ds_read_b64_tr_b16 a[0:1], v34 offset:0\n" \
"ds_read_b64_tr_b16 a[2:3], v34 offset:4608\n" \
"ds_read_b64_tr_b16 a[4:5], v34 offset:9216\n" \
"ds_read_b64_tr_b16 a[6:7], v34 offset:13824\n" \
"ds_read_b64_tr_b16 a[8:9], v34 offset:18432\n" \
"ds_read_b64_tr_b16 a[10:11], v34 offset:23040\n" \
"ds_read_b64_tr_b16 a[12:13], v34 offset:32\n" \
"ds_read_b64_tr_b16 a[14:15], v34 offset:4640\n" \
"ds_read_b64_tr_b16 a[16:17], v34 offset:9248\n" \
"ds_read_b64_tr_b16 a[18:19], v34 offset:13856\n" \
"buffer_load_dword v37, v38, s[56:59], s64 offen sc1\n" \
"ds_read_b64_tr_b16 a[20:21], v34 offset:18464\n" \
"ds_read_b64_tr_b16 a[22:23], v34 offset:23072\n" \
"ds_read_b64_tr_b16 a[24:25], v34 offset:64\n" \
"ds_read_b64_tr_b16 a[26:27], v34 offset:4672\n" \
"ds_read_b64_tr_b16 a[28:29], v34 offset:9280\n" \
"ds_read_b64_tr_b16 a[30:31], v34 offset:13888\n" \
"ds_read_b64_tr_b16 a[192:193], v34 offset:18496\n" \
"ds_read_b64_tr_b16 a[194:195], v34 offset:23104\n" \
"ds_read_b64_tr_b16 a[196:197], v34 offset:96\n" \
"ds_read_b64_tr_b16 a[198:199], v34 offset:4704\n" \
"ds_read_b64_tr_b16 a[200:201], v34 offset:9312\n" \
"ds_read_b64_tr_b16 a[202:203], v34 offset:13920\n" \
"ds_read_b64_tr_b16 a[204:205], v34 offset:18528\n" \
"ds_read_b64_tr_b16 a[206:207], v34 offset:23136\n" \
"ds_read_b64_tr_b16 a[208:209], v34 offset:128\n" \
"ds_read_b64_tr_b16 a[210:211], v34 offset:4736\n" \
"ds_read_b64_tr_b16 a[212:213], v34 offset:9344\n" \
"ds_read_b64_tr_b16 a[214:215], v34 offset:13952\n" \
"ds_read_b64_tr_b16 a[216:217], v34 offset:18560\n" \
"ds_read_b64_tr_b16 a[218:219], v34 offset:23168\n" \
"v_readlane_b32 s65, v11, 23\n" \
"s_add_u32 m0, s99, 0x6c00\n" \
"v_readlane_b32 s66, v11, 26\n" \
"v_add_u32_e32 v32, 0x49000, v20\n" \
"v_lshrrev_b32_e32 v44, 9, v20\n" \
"v_mul_u32_u24_e32 v44, 0xb40, v44\n" \
"v_add_u32_e32 v35, v44, v10\n" \
"v_readlane_b32 s67, v11, 24\n" \
"v_readlane_b32 s92, v11, 20\n" \
"v_readlane_b32 s73, v11, 25\n" \
"s_waitcnt vmcnt(18) lgkmcnt(15)\n" \
"v_mfma_f32_16x16x32_bf16 v[112:115], a[0:3], a[220:223], 0\n" \
"buffer_load_dwordx4 v0, s[80:83], s65 offen offset:0 lds\n" \
"v_mfma_f32_16x16x32_bf16 v[112:115], a[4:7], a[224:227], v[112:115]\n" \
"v_readlane_b32 s94, v11, 21\n" \
"v_readlane_b32 s84, v11, 7\n" \
"v_mfma_f32_16x16x32_bf16 v[112:115], a[8:11], a[228:231], v[112:115]\n" \
"v_readlane_b32 s85, v11, 8\n" \
"s_mov_b32 s86, 0xa1ca0\n" \
"v_mfma_f32_16x16x32_bf16 v[116:119], a[8:11], a[244:247], 0\n" \
"s_mov_b32 s87, 0x20000\n" \
"v_readlane_b32 s68, v11, 27\n" \
"v_mfma_f32_16x16x32_bf16 v[116:119], a[4:7], a[240:243], v[116:119]\n" \
"v_readlane_b32 s69, v11, 28\n" \
"v_readlane_b32 s72, v11, 30\n" \
"v_mfma_f32_16x16x32_bf16 v[116:119], a[0:3], a[236:239], v[116:119]\n" \
"buffer_load_dwordx4 v1, s[80:83], s65 offen offset:1024 lds\n" \
"v_mfma_f32_16x16x32_bf16 v[184:187], a[0:3], v[64:67], 0\n" \
"v_readlane_b32 s93, v11, 31\n" \
"v_lshlrev_b32_e32 v41, 12, v20\n" \
"v_mfma_f32_16x16x32_bf16 v[184:187], a[4:7], v[68:71], v[184:187]\n" \
"v_readlane_b32 s63, v11, 37\n" \
"v_bfe_u32 v40, v20, 2, 7\n" \
"v_mfma_f32_16x16x32_bf16 v[184:187], a[8:11], v[72:75], v[184:187]\n" \
"s_add_u32 s74, s63, 0x3800\n" \
"v_readlane_b32 s95, v11, 34\n" \
"v_mfma_f32_16x16x32_bf16 v[188:191], a[8:11], v[96:99], 0\n" \
"ds_read_b64_tr_b16 a[8:9], v34 offset:18592\n" \
"ds_read_b64_tr_b16 a[10:11], v34 offset:23200\n" \
"v_mfma_f32_16x16x32_bf16 v[188:191], a[4:7], v[84:87], v[188:191]\n" \
"ds_read_b64_tr_b16 a[4:5], v34 offset:9376\n" \
"ds_read_b64_tr_b16 a[6:7], v34 offset:13984\n" \
"v_mfma_f32_16x16x32_bf16 v[188:191], a[0:3], v[80:83], v[188:191]\n" \
"ds_read_b64_tr_b16 a[0:1], v34 offset:160\n" \
"ds_read_b64_tr_b16 a[2:3], v34 offset:4768\n" \
"v_mfma_f32_16x16x32_bf16 v[120:123], a[12:15], a[220:223], 0\n" \
"buffer_load_dwordx4 v2, s[80:83], s65 offen offset:2048 lds\n" \
"v_mfma_f32_16x16x32_bf16 v[120:123], a[16:19], a[224:227], v[120:123]\n" \
"v_readlane_b32 s96, v11, 35\n" \
"v_readlane_b32 s97, v11, 36\n" \
"v_mfma_f32_16x16x32_bf16 v[120:123], a[20:23], a[228:231], v[120:123]\n" \
"v_readlane_b32 s100, v11, 6\n" \
"v_bfe_u32 v44, v20, 4, 4\n" \
"v_mfma_f32_16x16x32_bf16 v[124:127], a[20:23], a[244:247], 0\n" \
"v_bfe_u32 v45, v20, 8, 2\n" \
"v_mul_u32_u24_e32 v42, 0x480, v44\n" \
"v_mfma_f32_16x16x32_bf16 v[124:127], a[16:19], a[240:243], v[124:127]\n" \
"v_lshl_add_u32 v42, v45, 3, v42\n" \
"v_lshrrev_b32_e32 v44, 1, v44\n" \
"v_mfma_f32_16x16x32_bf16 v[124:127], a[12:15], a[236:239], v[124:127]\n" \
"buffer_load_dwordx4 v3, s[80:83], s65 offen offset:3072 lds\n" \
"v_mfma_f32_16x16x32_bf16 v[192:195], a[12:15], v[64:67], 0\n" \
"s_add_u32 m0, s99, 0x7c00\n" \
"v_lshlrev_b32_e32 v45, 1, v45\n" \
"v_mfma_f32_16x16x32_bf16 v[192:195], a[16:19], v[68:71], v[192:195]\n" \
"v_sub_u32_e32 v44, v44, v45\n" \
"v_and_b32_e32 v45, 16, v20\n" \
"v_mfma_f32_16x16x32_bf16 v[192:195], a[20:23], v[72:75], v[192:195]\n" \
"v_mov_b32_e32 v46, 0x3f80\n" \
"v_lshlrev_b32_e32 v45, v45, v46\n" \
"v_mfma_f32_16x16x32_bf16 v[196:199], a[20:23], v[96:99], 0\n" \
"ds_read_b64_tr_b16 a[20:21], v34 offset:18624\n" \
"ds_read_b64_tr_b16 a[22:23], v34 offset:23232\n" \
"v_mfma_f32_16x16x32_bf16 v[196:199], a[16:19], v[84:87], v[196:199]\n" \
"ds_read_b64_tr_b16 a[16:17], v34 offset:9408\n" \
"ds_read_b64_tr_b16 a[18:19], v34 offset:14016\n" \
"v_mfma_f32_16x16x32_bf16 v[196:199], a[12:15], v[80:83], v[196:199]\n" \
"ds_read_b64_tr_b16 a[12:13], v34 offset:192\n" \
"ds_read_b64_tr_b16 a[14:15], v34 offset:4800\n" \
"v_mfma_f32_16x16x32_bf16 v[128:131], a[24:27], a[220:223], 0\n" \
"v_mov_b32_e32 v52, 0\n" \
"s_waitcnt lgkmcnt(8)\n" \
"v_mfma_f32_16x16x32_bf16 v[128:131], a[28:31], a[224:227], v[128:131]\n" \
"buffer_load_dwordx4 v4, s[80:83], s65 offen offset:0 lds\n" \
"v_mfma_f32_16x16x32_bf16 v[128:131], a[192:195], a[228:231], v[128:131]\n" \
"v_mov_b32_e32 v53, 0\n" \
"v_mov_b32_e32 v48, 0\n" \
"v_mfma_f32_16x16x32_bf16 v[132:135], a[192:195], a[244:247], 0\n" \
"v_mov_b32_e32 v49, 0\n" \
"v_add_u32_e32 v43, 0x9000, v42\n" \
"v_mfma_f32_16x16x32_bf16 v[132:135], a[28:31], a[240:243], v[132:135]\n" \
"v_readlane_b32 s88, v11, 11\n" \
"v_readlane_b32 s89, v11, 12\n" \
"v_mfma_f32_16x16x32_bf16 v[132:135], a[24:27], a[236:239], v[132:135]\n" \
"s_mov_b32 s90, 0x240000\n" \
"s_mov_b32 s91, 0x20000\n" \
"v_mfma_f32_16x16x32_bf16 v[200:203], a[24:27], v[64:67], 0\n" \
"buffer_load_dwordx4 v5, s[80:83], s65 offen offset:1024 lds\n" \
"v_mfma_f32_16x16x32_bf16 v[200:203], a[28:31], v[68:71], v[200:203]\n" \
"v_mov_b32_e32 v46, s62\n" \
"v_mfma_f32_16x16x32_bf16 v[200:203], a[192:195], v[72:75], v[200:203]\n" \
"v_mfma_f32_16x16x32_bf16 v[204:207], a[192:195], v[96:99], 0\n" \
"ds_read_b64_tr_b16 a[192:193], v34 offset:18656\n" \
"ds_read_b64_tr_b16 a[194:195], v34 offset:23264\n" \
"v_mfma_f32_16x16x32_bf16 v[204:207], a[28:31], v[84:87], v[204:207]\n" \
"ds_read_b64_tr_b16 a[28:29], v34 offset:9440\n" \
"ds_read_b64_tr_b16 a[30:31], v34 offset:14048\n" \
"v_mfma_f32_16x16x32_bf16 v[204:207], a[24:27], v[80:83], v[204:207]\n" \
"ds_read_b64_tr_b16 a[24:25], v34 offset:224\n" \
"ds_read_b64_tr_b16 a[26:27], v34 offset:4832\n" \
"v_mfma_f32_16x16x32_bf16 v[136:139], a[196:199], a[220:223], 0\n" \
"buffer_load_dwordx4 v6, s[80:83], s65 offen offset:2048 lds\n" \
"v_mfma_f32_16x16x32_bf16 v[136:139], a[200:203], a[224:227], v[136:139]\n" \
"v_mfma_f32_16x16x32_bf16 v[136:139], a[204:207], a[228:231], v[136:139]\n" \
"v_mfma_f32_16x16x32_bf16 v[140:143], a[204:207], a[244:247], 0\n" \
"v_mfma_f32_16x16x32_bf16 v[140:143], a[200:203], a[240:243], v[140:143]\n" \
"v_mfma_f32_16x16x32_bf16 v[140:143], a[196:199], a[236:239], v[140:143]\n" \
"buffer_load_dwordx4 v7, s[80:83], s65 offen offset:3072 lds\n" \
"v_mfma_f32_16x16x32_bf16 v[208:211], a[196:199], v[64:67], 0\n" \
"s_add_u32 m0, s99, 0x8c00\n" \
"v_mfma_f32_16x16x32_bf16 v[208:211], a[200:203], v[68:71], v[208:211]\n" \
"v_mfma_f32_16x16x32_bf16 v[208:211], a[204:207], v[72:75], v[208:211]\n" \
"v_mfma_f32_16x16x32_bf16 v[212:215], a[204:207], v[96:99], 0\n" \
"ds_read_b64_tr_b16 a[204:205], v34 offset:18688\n" \
"ds_read_b64_tr_b16 a[206:207], v34 offset:23296\n" \
"v_mfma_f32_16x16x32_bf16 v[212:215], a[200:203], v[84:87], v[212:215]\n" \
"ds_read_b64_tr_b16 a[200:201], v34 offset:9472\n" \
"ds_read_b64_tr_b16 a[202:203], v34 offset:14080\n" \
"v_mfma_f32_16x16x32_bf16 v[212:215], a[196:199], v[80:83], v[212:215]\n" \
"ds_read_b64_tr_b16 a[196:197], v34 offset:256\n" \
"ds_read_b64_tr_b16 a[198:199], v34 offset:4864\n" \
"v_mfma_f32_16x16x32_bf16 v[144:147], a[208:211], a[220:223], 0\n" \
"buffer_load_dwordx4 v8, s[80:83], s65 offen offset:0 lds\n" \
"v_mfma_f32_16x16x32_bf16 v[144:147], a[212:215], a[224:227], v[144:147]\n" \
"s_waitcnt vmcnt(9)\n" \
"v_cmp_ne_u32_e32 vcc, s62, v37\n" \
"v_mfma_f32_16x16x32_bf16 v[144:147], a[216:219], a[228:231], v[144:147]\n" \
"s_add_u32 m0, s99, 0x14400\n" \
"v_readlane_b32 s65, v11, 42\n" \
"v_mfma_f32_16x16x32_bf16 v[148:151], a[216:219], a[244:247], 0\n" \
"v_mfma_f32_16x16x32_bf16 v[148:151], a[212:215], a[240:243], v[148:151]\n" \
"s_cbranch_vccnz L_sa_%=\n" \
"L_ra_%=:\n" \
"v_readlane_b32 s64, v11, 29\n" \
"v_mfma_f32_16x16x32_bf16 v[148:151], a[208:211], a[236:239], v[148:151]\n" \
"buffer_load_dwordx4 a[64:67], v20, s[56:59], s66 offen offset:0 sc1\n" \
"v_mfma_f32_16x16x32_bf16 v[216:219], a[208:211], v[64:67], 0\n" \
"v_mfma_f32_16x16x32_bf16 v[216:219], a[212:215], v[68:71], v[216:219]\n" \
"v_mfma_f32_16x16x32_bf16 v[216:219], a[216:219], v[72:75], v[216:219]\n" \
"v_mfma_f32_16x16x32_bf16 v[220:223], a[216:219], v[96:99], 0\n" \
"v_mfma_f32_16x16x32_bf16 v[220:223], a[212:215], v[84:87], v[220:223]\n" \
"buffer_load_dwordx4 a[68:71], v20, s[56:59], s66 offen offset:1024 sc1\n" \
"v_mfma_f32_16x16x32_bf16 v[220:223], a[208:211], v[80:83], v[220:223]\n" \
"s_waitcnt lgkmcnt(5)\n" \
"v_mfma_f32_16x16x32_bf16 v[152:155], a[0:3], a[220:223], 0\n" \
"v_mfma_f32_16x16x32_bf16 v[152:155], a[4:7], a[224:227], v[152:155]\n" \
"v_mfma_f32_16x16x32_bf16 v[152:155], a[8:11], a[228:231], v[152:155]\n" \
"v_mfma_f32_16x16x32_bf16 v[156:159], a[8:11], a[244:247], 0\n" \
"buffer_load_dwordx4 a[84:87], v32, s[56:59], s66 offen offset:1024 sc1\n" \
"v_mfma_f32_16x16x32_bf16 v[156:159], a[4:7], a[240:243], v[156:159]\n" \
"v_mfma_f32_16x16x32_bf16 v[156:159], a[0:3], a[236:239], v[156:159]\n" \
"v_mfma_f32_16x16x32_bf16 v[224:227], a[0:3], v[64:67], 0\n" \
"v_mfma_f32_16x16x32_bf16 v[224:227], a[4:7], v[68:71], v[224:227]\n" \
"v_mfma_f32_16x16x32_bf16 v[224:227], a[8:11], v[72:75], v[224:227]\n" \
"buffer_load_dwordx4 a[80:83], v32, s[56:59], s66 offen offset:0 sc1\n" \
"v_mfma_f32_16x16x32_bf16 v[228:231], a[8:11], v[96:99], 0\n" \
"v_mfma_f32_16x16x32_bf16 v[228:231], a[4:7], v[84:87], v[228:231]\n" \
"v_mfma_f32_16x16x32_bf16 v[228:231], a[0:3], v[80:83], v[228:231]\n" \
"v_mfma_f32_16x16x32_bf16 v[160:163], a[12:15], a[220:223], 0\n" \
"v_mfma_f32_16x16x32_bf16 v[160:163], a[16:19], a[224:227], v[160:163]\n" \
"buffer_load_dwordx4 a[72:75], v20, s[56:59], s66 offen offset:2048 sc1\n" \
"v_mfma_f32_16x16x32_bf16 v[160:163], a[20:23], a[228:231], v[160:163]\n" \
"v_mfma_f32_16x16x32_bf16 v[164:167], a[20:23], a[244:247], 0\n" \
"v_mfma_f32_16x16x32_bf16 v[164:167], a[16:19], a[240:243], v[164:167]\n" \
"v_mfma_f32_16x16x32_bf16 v[164:167], a[12:15], a[236:239], v[164:167]\n" \
"v_mfma_f32_16x16x32_bf16 v[232:235], a[12:15], v[64:67], 0\n" \
"buffer_load_dwordx4 a[76:79], v20, s[56:59], s66 offen offset:3072 sc1\n" \
"v_mfma_f32_16x16x32_bf16 v[232:235], a[16:19], v[68:71], v[232:235]\n" \
"v_mfma_f32_16x16x32_bf16 v[232:235], a[20:23], v[72:75], v[232:235]\n" \
"v_mfma_f32_16x16x32_bf16 v[236:239], a[20:23], v[96:99], 0\n" \
"v_mfma_f32_16x16x32_bf16 v[236:239], a[16:19], v[84:87], v[236:239]\n" \
"v_mfma_f32_16x16x32_bf16 v[236:239], a[12:15], v[80:83], v[236:239]\n" \
"buffer_load_dwordx4 a[92:95], v32, s[56:59], s66 offen offset:3072 sc1\n" \
"s_waitcnt lgkmcnt(0)\n" \
"s_barrier\n" \
"v_mfma_f32_16x16x32_bf16 v[168:171], a[24:27], a[220:223], 0\n" \
"ds_read_b64_tr_b16 a[208:209], v33 offset:9216\n" \
"ds_read_b64_tr_b16 a[210:211], v33 offset:13824\n" \
"v_mfma_f32_16x16x32_bf16 v[168:171], a[28:31], a[224:227], v[168:171]\n" \
"ds_read_b64_tr_b16 a[212:213], v33 offset:9248\n" \
"ds_read_b64_tr_b16 a[214:215], v33 offset:13856\n" \
"v_mfma_f32_16x16x32_bf16 v[168:171], a[192:195], a[228:231], v[168:171]\n" \
"ds_read_b64_tr_b16 a[216:217], v33 offset:9280\n" \
"ds_read_b64_tr_b16 a[218:219], v33 offset:13888\n" \
"s_mov_b64 exec, 1\n" \
"buffer_store_dword v39, off, s[56:59], s70 offset:0 sc1\n" \
"s_mov_b64 exec, -1\n" \
"v_mfma_f32_16x16x32_bf16 v[172:175], a[192:195], a[244:247], 0\n" \
"ds_read_b64_tr_b16 a[0:1], v33 offset:9312\n" \
"ds_read_b64_tr_b16 a[2:3], v33 offset:13920\n" \
"v_mfma_f32_16x16x32_bf16 v[172:175], a[28:31], a[240:243], v[172:175]\n" \
"ds_read_b64_tr_b16 a[4:5], v33 offset:9344\n" \
"ds_read_b64_tr_b16 a[6:7], v33 offset:13952\n" \
"v_mfma_f32_16x16x32_bf16 v[172:175], a[24:27], a[236:239], v[172:175]\n" \
"ds_read_b64_tr_b16 a[8:9], v33 offset:9376\n" \
"ds_read_b64_tr_b16 a[10:11], v33 offset:13984\n" \
"v_mfma_f32_16x16x32_bf16 v[240:243], a[24:27], v[64:67], 0\n" \
"ds_read_b64_tr_b16 a[12:13], v33 offset:9408\n" \
"ds_read_b64_tr_b16 a[14:15], v33 offset:14016\n" \
"v_mfma_f32_16x16x32_bf16 v[240:243], a[28:31], v[68:71], v[240:243]\n" \
"ds_read_b64_tr_b16 a[16:17], v33 offset:9440\n" \
"ds_read_b64_tr_b16 a[18:19], v33 offset:14048\n" \
"v_mfma_f32_16x16x32_bf16 v[240:243], a[192:195], v[72:75], v[240:243]\n" \
"ds_read_b64_tr_b16 a[20:21], v33 offset:9472\n" \
"ds_read_b64_tr_b16 a[22:23], v33 offset:14080\n" \
"v_mfma_f32_16x16x32_bf16 v[244:247], a[192:195], v[96:99], 0\n" \
"ds_read_b64_tr_b16 a[192:193], v35 offset:32\n" \
"ds_read_b64_tr_b16 a[194:195], v35 offset:2336\n" \
"v_mfma_f32_16x16x32_bf16 v[244:247], a[28:31], v[84:87], v[244:247]\n" \
"ds_read_b64_tr_b16 a[28:29], v35 offset:10368\n" \
"ds_read_b64_tr_b16 a[30:31], v35 offset:12672\n" \
"v_mfma_f32_16x16x32_bf16 v[244:247], a[24:27], v[80:83], v[244:247]\n" \
"ds_read_b64_tr_b16 a[24:25], v35 offset:0\n" \
"ds_read_b64_tr_b16 a[26:27], v35 offset:2304\n" \
"v_mfma_f32_16x16x32_bf16 v[176:179], a[196:199], a[220:223], 0\n" \
"buffer_load_dwordx4 a[88:91], v32, s[56:59], s66 offen offset:2048 sc1\n" \
"v_mfma_f32_16x16x32_bf16 v[176:179], a[200:203], a[224:227], v[176:179]\n" \
"v_readlane_b32 s66, v11, 43\n" \
"v_readlane_b32 s70, v11, 45\n" \
"v_mfma_f32_16x16x32_bf16 v[176:179], a[204:207], a[228:231], v[176:179]\n" \
"v_mfma_f32_16x16x32_bf16 v[180:183], a[204:207], a[244:247], 0\n" \
"v_mfma_f32_16x16x32_bf16 v[180:183], a[200:203], a[240:243], v[180:183]\n" \
"v_mfma_f32_16x16x32_bf16 v[180:183], a[196:199], a[236:239], v[180:183]\n" \
"buffer_load_dwordx4 v0, s[80:83], s67 offen offset:0 lds\n" \
"v_mfma_f32_16x16x32_bf16 v[248:251], a[196:199], v[64:67], 0\n" \
"v_mfma_f32_16x16x32_bf16 v[248:251], a[200:203], v[68:71], v[248:251]\n" \
"v_mfma_f32_16x16x32_bf16 v[248:251], a[204:207], v[72:75], v[248:251]\n" \
"v_mfma_f32_16x16x32_bf16 v[252:255], a[204:207], v[96:99], 0\n" \
"ds_read_b64_tr_b16 a[204:205], v35 offset:10432\n" \
"L_pf20:\n" \
"s_cbranch_execz L_pf22\n" \
"s_nop 0\n" \
"ds_read_b64_tr_b16 a[206:207], v35 offset:12736\n" \
"v_mfma_f32_16x16x32_bf16 v[252:255], a[200:203], v[84:87], v[252:255]\n" \
"ds_read_b64_tr_b16 a[200:201], v35 offset:64\n" \
"ds_read_b64_tr_b16 a[202:203], v35 offset:2368\n" \
"v_mfma_f32_16x16x32_bf16 v[252:255], a[196:199], v[80:83], v[252:255]\n" \
"ds_read_b64_tr_b16 a[196:197], v35 offset:10400\n" \
"s_waitcnt lgkmcnt(9)\n" \
"v_mfma_f32_16x16x32_bf16 v[112:115], a[208:211], a[232:235], v[112:115]\n" \
"buffer_load_dwordx4 v1, s[80:83], s67 offen offset:1024 lds\n" \
"v_mfma_f32_16x16x32_bf16 v[116:119], a[208:211], a[248:251], v[116:119]\n" \
"ds_read_b64_tr_b16 a[198:199], v35 offset:12704\n" \
"v_mfma_f32_16x16x32_bf16 v[184:187], a[208:211], v[76:79], v[184:187]\n" \
"v_mfma_f32_16x16x32_bf16 v[188:191], a[208:211], v[100:103], v[188:191]\n" \
"ds_read_b64_tr_b16 a[208:209], v35 offset:96\n" \
"ds_read_b64_tr_b16 a[210:211], v35 offset:2400\n" \
"v_mfma_f32_16x16x32_bf16 v[120:123], a[212:215], a[232:235], v[120:123]\n" \
"v_mfma_f32_16x16x32_bf16 v[124:127], a[212:215], a[248:251], v[124:127]\n" \
"buffer_load_dwordx4 v2, s[80:83], s67 offen offset:2048 lds\n" \
"v_mfma_f32_16x16x32_bf16 v[192:195], a[212:215], v[76:79], v[192:195]\n" \
"v_mfma_f32_16x16x32_bf16 v[196:199], a[212:215], v[100:103], v[196:199]\n" \
"ds_read_b64_tr_b16 a[212:213], v35 offset:10464\n" \
"ds_read_b64_tr_b16 a[214:215], v35 offset:12768\n" \
"v_mfma_f32_16x16x32_bf16 v[128:131], a[216:219], a[232:235], v[128:131]\n" \
"v_mfma_f32_16x16x32_bf16 v[132:135], a[216:219], a[248:251], v[132:135]\n" \
"v_mfma_f32_16x16x32_bf16 v[200:203], a[216:219], v[76:79], v[200:203]\n" \
"buffer_load_dwordx4 v3, s[80:83], s67 offen offset:3072 lds\n" \
"v_mfma_f32_16x16x32_bf16 v[204:207], a[216:219], v[100:103], v[204:207]\n" \
"ds_read_b64_tr_b16 a[216:217], v35 offset:128\n" \
"ds_read_b64_tr_b16 a[218:219], v35 offset:2432\n" \
"v_mfma_f32_16x16x32_bf16 v[136:139], a[0:3], a[232:235], v[136:139]\n" \
"s_add_u32 m0, s99, 0x15400\n" \
"v_mfma_f32_16x16x32_bf16 v[140:143], a[0:3], a[248:251], v[140:143]\n" \
"v_mfma_f32_16x16x32_bf16 v[208:211], a[0:3], v[76:79], v[208:211]\n" \
"v_mfma_f32_16x16x32_bf16 v[212:215], a[0:3], v[100:103], v[212:215]\n" \
"ds_read_b64_tr_b16 a[0:1], v35 offset:10496\n" \
"ds_read_b64_tr_b16 a[2:3], v35 offset:12800\n" \
"v_mfma_f32_16x16x32_bf16 v[144:147], a[4:7], a[232:235], v[144:147]\n" \
"buffer_load_dwordx4 v4, s[80:83], s67 offen offset:0 lds\n" \
"v_mfma_f32_16x16x32_bf16 v[148:151], a[4:7], a[248:251], v[148:151]\n" \
"v_mfma_f32_16x16x32_bf16 v[216:219], a[4:7], v[76:79], v[216:219]\n" \
"v_mfma_f32_16x16x32_bf16 v[220:223], a[4:7], v[100:103], v[220:223]\n" \
"ds_read_b64_tr_b16 a[4:5], v35 offset:160\n" \
"ds_read_b64_tr_b16 a[6:7], v35 offset:2464\n" \
"v_mfma_f32_16x16x32_bf16 v[152:155], a[8:11], a[232:235], v[152:155]\n" \
"v_mfma_f32_16x16x32_bf16 v[156:159], a[8:11], a[248:251], v[156:159]\n" \
"buffer_load_dwordx4 v5, s[80:83], s67 offen offset:1024 lds\n" \
"v_mfma_f32_16x16x32_bf16 v[224:227], a[8:11], v[76:79], v[224:227]\n" \
"v_mfma_f32_16x16x32_bf16 v[228:231], a[8:11], v[100:103], v[228:231]\n" \
"ds_read_b64_tr_b16 a[8:9], v35 offset:10528\n" \
"ds_read_b64_tr_b16 a[10:11], v35 offset:12832\n" \
"v_mfma_f32_16x16x32_bf16 v[160:163], a[12:15], a[232:235], v[160:163]\n" \
"v_mfma_f32_16x16x32_bf16 v[164:167], a[12:15], a[248:251], v[164:167]\n" \
"v_mfma_f32_16x16x32_bf16 v[232:235], a[12:15], v[76:79], v[232:235]\n" \
"buffer_load_dwordx4 v6, s[80:83], s67 offen offset:2048 lds\n" \
"v_mfma_f32_16x16x32_bf16 v[236:239], a[12:15], v[100:103], v[236:239]\n" \
"ds_read_b64_tr_b16 a[12:13], v35 offset:192\n" \
"ds_read_b64_tr_b16 a[14:15], v35 offset:2496\n" \
"v_mfma_f32_16x16x32_bf16 v[168:171], a[16:19], a[232:235], v[168:171]\n" \
"v_mfma_f32_16x16x32_bf16 v[172:175], a[16:19], a[248:251], v[172:175]\n" \
"v_mfma_f32_16x16x32_bf16 v[240:243], a[16:19], v[76:79], v[240:243]\n" \
"v_mfma_f32_16x16x32_bf16 v[244:247], a[16:19], v[100:103], v[244:247]\n" \
"buffer_load_dwordx4 v7, s[80:83], s67 offen offset:3072 lds\n" \
"v_mfma_f32_16x16x32_bf16 v[176:179], a[20:23], a[232:235], v[176:179]\n" \
"s_add_u32 m0, s99, 0x16400\n" \
"ds_read_b64_tr_b16 a[16:17], v35 offset:10560\n" \
"v_mfma_f32_16x16x32_bf16 v[180:183], a[20:23], a[248:251], v[180:183]\n" \
"ds_read_b64_tr_b16 a[18:19], v35 offset:12864\n" \
"v_mfma_f32_16x16x32_bf16 v[248:251], a[20:23], v[76:79], v[248:251]\n" \
"v_mfma_f32_16x16x32_bf16 v[252:255], a[20:23], v[100:103], v[252:255]\n" \
"ds_read_b64_tr_b16 a[20:21], v35 offset:224\n" \
"s_waitcnt lgkmcnt(7)\n" \
"v_mfma_f32_16x16x32_bf16 v[112:115], a[24:27], a[32:35], v[112:115]\n" \
"buffer_load_dwordx4 v8, s[80:83], s67 offen offset:0 lds\n" \
"v_mfma_f32_16x16x32_bf16 v[112:115], a[28:31], a[36:39], v[112:115]\n" \
"s_add_u32 m0, s92, 0x0\n" \
"ds_read_b64_tr_b16 a[22:23], v35 offset:2528\n" \
"v_mfma_f32_16x16x32_bf16 v[116:119], a[28:31], a[44:47], v[116:119]\n" \
"v_readlane_b32 s67, v11, 44\n" \
"v_mfma_f32_16x16x32_bf16 v[116:119], a[24:27], a[40:43], v[116:119]\n" \
"v_mfma_f32_16x16x32_bf16 v[184:187], a[24:27], a[48:51], v[184:187]\n" \
"v_mfma_f32_16x16x32_bf16 v[184:187], a[28:31], a[52:55], v[184:187]\n" \
"buffer_load_dword v47, v38, s[56:59], s71 offen sc1\n" \
"v_mfma_f32_16x16x32_bf16 v[188:191], a[28:31], a[60:63], v[188:191]\n" \
"ds_read_b64_tr_b16 a[28:29], v35 offset:256\n" \
"ds_read_b64_tr_b16 a[30:31], v35 offset:2560\n" \
"v_mfma_f32_16x16x32_bf16 v[188:191], a[24:27], a[56:59], v[188:191]\n" \
"ds_read_b64_tr_b16 a[24:25], v35 offset:10592\n" \
"ds_read_b64_tr_b16 a[26:27], v35 offset:12896\n" \
"v_mfma_f32_16x16x32_bf16 v[120:123], a[192:195], a[32:35], v[120:123]\n" \
"v_mfma_f32_16x16x32_bf16 v[120:123], a[196:199], a[36:39], v[120:123]\n" \
"v_mfma_f32_16x16x32_bf16 v[124:127], a[196:199], a[44:47], v[124:127]\n" \
"buffer_load_dwordx4 v0, s[80:83], s73 offen offset:0 lds\n" \
"v_mfma_f32_16x16x32_bf16 v[124:127], a[192:195], a[40:43], v[124:127]\n" \
"v_mfma_f32_16x16x32_bf16 v[192:195], a[192:195], a[48:51], v[192:195]\n" \
"v_mfma_f32_16x16x32_bf16 v[192:195], a[196:199], a[52:55], v[192:195]\n" \
"v_mfma_f32_16x16x32_bf16 v[196:199], a[196:199], a[60:63], v[196:199]\n" \
"v_mfma_f32_16x16x32_bf16 v[196:199], a[192:195], a[56:59], v[196:199]\n" \
"buffer_load_dwordx4 v1, s[80:83], s73 offen offset:1024 lds\n" \
"v_mfma_f32_16x16x32_bf16 v[128:131], a[200:203], a[32:35], v[128:131]\n" \
"ds_read_b64_tr_b16 a[192:193], v35 offset:10624\n" \
"ds_read_b64_tr_b16 a[194:195], v35 offset:12928\n" \
"v_mfma_f32_16x16x32_bf16 v[128:131], a[204:207], a[36:39], v[128:131]\n" \
"v_mfma_f32_16x16x32_bf16 v[132:135], a[204:207], a[44:47], v[132:135]\n" \
"v_mfma_f32_16x16x32_bf16 v[132:135], a[200:203], a[40:43], v[132:135]\n" \
"v_mfma_f32_16x16x32_bf16 v[200:203], a[200:203], a[48:51], v[200:203]\n" \
"buffer_load_dwordx4 v2, s[80:83], s73 offen offset:2048 lds\n" \
"v_mfma_f32_16x16x32_bf16 v[200:203], a[204:207], a[52:55], v[200:203]\n" \
"v_mfma_f32_16x16x32_bf16 v[204:207], a[204:207], a[60:63], v[204:207]\n" \
"v_mfma_f32_16x16x32_bf16 v[204:207], a[200:203], a[56:59], v[204:207]\n" \
"v_mfma_f32_16x16x32_bf16 v[136:139], a[208:211], a[32:35], v[136:139]\n" \
"v_mfma_f32_16x16x32_bf16 v[136:139], a[212:215], a[36:39], v[136:139]\n" \
"buffer_load_dwordx4 v3, s[80:83], s73 offen offset:3072 lds\n" \
"v_mfma_f32_16x16x32_bf16 v[140:143], a[212:215], a[44:47], v[140:143]\n" \
"s_add_u32 m0, s92, 0x1000\n" \
"v_mfma_f32_16x16x32_bf16 v[140:143], a[208:211], a[40:43], v[140:143]\n" \
"v_mfma_f32_16x16x32_bf16 v[208:211], a[208:211], a[48:51], v[208:211]\n" \
"v_mfma_f32_16x16x32_bf16 v[208:211], a[212:215], a[52:55], v[208:211]\n" \
"v_mfma_f32_16x16x32_bf16 v[212:215], a[212:215], a[60:63], v[212:215]\n" \
"buffer_load_dwordx4 v4, s[80:83], s73 offen offset:0 lds\n" \
"v_mfma_f32_16x16x32_bf16 v[212:215], a[208:211], a[56:59], v[212:215]\n" \
"v_mfma_f32_16x16x32_bf16 v[144:147], a[216:219], a[32:35], v[144:147]\n" \
"v_mfma_f32_16x16x32_bf16 v[144:147], a[0:3], a[36:39], v[144:147]\n" \
"v_mfma_f32_16x16x32_bf16 v[148:151], a[0:3], a[44:47], v[148:151]\n" \
"v_mfma_f32_16x16x32_bf16 v[148:151], a[216:219], a[40:43], v[148:151]\n" \
"buffer_load_dwordx4 v5, s[80:83], s73 offen offset:1024 lds\n" \
"v_mfma_f32_16x16x32_bf16 v[216:219], a[216:219], a[48:51], v[216:219]\n" \
"v_mfma_f32_16x16x32_bf16 v[216:219], a[0:3], a[52:55], v[216:219]\n" \
"v_mfma_f32_16x16x32_bf16 v[220:223], a[0:3], a[60:63], v[220:223]\n" \
"v_mfma_f32_16x16x32_bf16 v[220:223], a[216:219], a[56:59], v[220:223]\n" \
"v_mfma_f32_16x16x32_bf16 v[152:155], a[4:7], a[32:35], v[152:155]\n" \
"buffer_load_dwordx4 v6, s[80:83], s73 offen offset:2048 lds\n" \
"s_waitcnt lgkmcnt(0)\n" \
"v_mfma_f32_16x16x32_bf16 v[152:155], a[8:11], a[36:39], v[152:155]\n" \
"v_mfma_f32_16x16x32_bf16 v[156:159], a[8:11], a[44:47], v[156:159]\n" \
"v_mfma_f32_16x16x32_bf16 v[156:159], a[4:7], a[40:43], v[156:159]\n" \
"v_mfma_f32_16x16x32_bf16 v[224:227], a[4:7], a[48:51], v[224:227]\n" \
"buffer_load_dwordx4 v7, s[80:83], s73 offen offset:3072 lds\n" \
"s_add_u32 m0, s92, 0x2000\n" \
"v_mfma_f32_16x16x32_bf16 v[224:227], a[8:11], a[52:55], v[224:227]\n" \
"v_readlane_b32 s92, v11, 42\n" \
"v_mfma_f32_16x16x32_bf16 v[228:231], a[8:11], a[60:63], v[228:231]\n" \
"v_mfma_f32_16x16x32_bf16 v[228:231], a[4:7], a[56:59], v[228:231]\n" \
"s_waitcnt vmcnt(23)\n" \
"s_barrier\n" \
"buffer_load_dwordx4 v8, s[80:83], s73 offen offset:0 lds\n" \
"s_mov_b32 m0, s94\n" \
"v_mfma_f32_16x16x32_bf16 v[160:163], a[12:15], a[32:35], v[160:163]\n" \
"ds_read_b64_tr_b16 a[196:197], v10 offset:27648\n" \
"ds_read_b64_tr_b16 a[198:199], v10 offset:32256\n" \
"v_mfma_f32_16x16x32_bf16 v[160:163], a[16:19], a[36:39], v[160:163]\n" \
"ds_read_b64_tr_b16 a[200:201], v10 offset:36864\n" \
"ds_read_b64_tr_b16 a[202:203], v10 offset:41472\n" \
"v_mfma_f32_16x16x32_bf16 v[164:167], a[16:19], a[44:47], v[164:167]\n" \
"ds_read_b64_tr_b16 a[204:205], v10 offset:27680\n" \
"ds_read_b64_tr_b16 a[206:207], v10 offset:32288\n" \
"v_mfma_f32_16x16x32_bf16 v[164:167], a[12:15], a[40:43], v[164:167]\n" \
"buffer_load_dwordx4 v9, s[84:87], 0 offen lds\n" \
"v_mfma_f32_16x16x32_bf16 v[232:235], a[12:15], a[48:51], v[232:235]\n" \
"ds_read_b64_tr_b16 a[208:209], v10 offset:36896\n" \
"ds_read_b64_tr_b16 a[210:211], v10 offset:41504\n" \
"v_mfma_f32_16x16x32_bf16 v[232:235], a[16:19], a[52:55], v[232:235]\n" \
"ds_read_b64_tr_b16 a[212:213], v10 offset:27712\n" \
"ds_read_b64_tr_b16 a[214:215], v10 offset:32320\n" \
"v_mfma_f32_16x16x32_bf16 v[236:239], a[16:19], a[60:63], v[236:239]\n" \
"ds_read_b64_tr_b16 a[216:217], v10 offset:36928\n" \
"ds_read_b64_tr_b16 a[218:219], v10 offset:41536\n" \
"v_mfma_f32_16x16x32_bf16 v[236:239], a[12:15], a[56:59], v[236:239]\n" \
"ds_read_b64_tr_b16 a[0:1], v10 offset:27744\n" \
"ds_read_b64_tr_b16 a[2:3], v10 offset:32352\n" \
"buffer_load_dwordx4 a[128:131], v20, s[56:59], s68 offen offset:0 sc1\n" \
"v_mfma_f32_16x16x32_bf16 v[168:171], a[20:23], a[32:35], v[168:171]\n" \
"ds_read_b64_tr_b16 a[4:5], v10 offset:36960\n" \
"ds_read_b64_tr_b16 a[6:7], v10 offset:41568\n" \
"v_mfma_f32_16x16x32_bf16 v[168:171], a[24:27], a[36:39], v[168:171]\n" \
"ds_read_b64_tr_b16 a[8:9], v10 offset:27776\n" \
"ds_read_b64_tr_b16 a[10:11], v10 offset:32384\n" \
"v_mfma_f32_16x16x32_bf16 v[172:175], a[24:27], a[44:47], v[172:175]\n" \
"ds_read_b64_tr_b16 a[12:13], v10 offset:36992\n" \
"ds_read_b64_tr_b16 a[14:15], v10 offset:41600\n" \
"v_mfma_f32_16x16x32_bf16 v[172:175], a[20:23], a[40:43], v[172:175]\n" \
"ds_read_b64_tr_b16 a[16:17], v10 offset:27808\n" \
"ds_read_b64_tr_b16 a[18:19], v10 offset:32416\n" \
"buffer_load_dwordx4 a[132:135], v20, s[56:59], s68 offen offset:1024 sc1\n" \
"v_mfma_f32_16x16x32_bf16 v[240:243], a[20:23], a[48:51], v[240:243]\n" \
"s_waitcnt vmcnt(12)\n" \
"v_cmp_ne_u32_e32 vcc, s62, v47\n" \
"v_mfma_f32_16x16x32_bf16 v[240:243], a[24:27], a[52:55], v[240:243]\n" \
"v_readlane_b32 s73, v11, 48\n" \
"v_readlane_b32 s94, v11, 44\n" \
"v_mfma_f32_16x16x32_bf16 v[244:247], a[24:27], a[60:63], v[244:247]\n" \
"ds_read_b64_tr_b16 a[24:25], v10 offset:27840\n" \
"ds_read_b64_tr_b16 a[26:27], v10 offset:32448\n" \
"v_mfma_f32_16x16x32_bf16 v[244:247], a[20:23], a[56:59], v[244:247]\n" \
"ds_read_b64_tr_b16 a[20:21], v10 offset:37024\n" \
"ds_read_b64_tr_b16 a[22:23], v10 offset:41632\n" \
"buffer_load_dwordx4 a[136:139], v20, s[56:59], s68 offen offset:2048 sc1\n" \
"v_mfma_f32_16x16x32_bf16 v[176:179], a[28:31], a[32:35], v[176:179]\n" \
"s_cbranch_vccnz L_sb_%=\n" \
"L_rb_%=:\n" \
"v_readlane_b32 s71, v11, 46\n" \
"v_mfma_f32_16x16x32_bf16 v[176:179], a[192:195], a[36:39], v[176:179]\n" \
"v_cmp_eq_u32_e32 vcc, 0, v44\n" \
"v_mfma_f32_16x16x32_bf16 v[180:183], a[192:195], a[44:47], v[180:183]\n" \
"v_mfma_f32_16x16x32_bf16 v[180:183], a[28:31], a[40:43], v[180:183]\n" \
"v_cndmask_b32_e32 v50, 0, v45, vcc\n" \
"v_cmp_eq_u32_e32 vcc, 1, v44\n" \
"v_mfma_f32_16x16x32_bf16 v[248:251], a[28:31], a[48:51], v[248:251]\n" \
"buffer_load_dwordx4 a[140:143], v20, s[56:59], s68 offen offset:3072 sc1\n" \
"v_mfma_f32_16x16x32_bf16 v[248:251], a[192:195], a[52:55], v[248:251]\n" \
"v_cndmask_b32_e32 v51, 0, v45, vcc\n" \
"v_bfe_u32 v45, v20, 1, 5\n" \
"v_mfma_f32_16x16x32_bf16 v[252:255], a[192:195], a[60:63], v[252:255]\n" \
"ds_read_b64_tr_b16 a[192:193], v10 offset:27872\n" \
"ds_read_b64_tr_b16 a[194:195], v10 offset:32480\n" \
"v_mfma_f32_16x16x32_bf16 v[252:255], a[28:31], a[56:59], v[252:255]\n" \
"ds_read_b64_tr_b16 a[28:29], v10 offset:37056\n" \
"s_waitcnt lgkmcnt(9)\n" \
"v_mfma_f32_16x16x32_bf16 v[112:115], a[196:199], a[64:67], v[112:115]\n" \
"ds_read_b64_tr_b16 a[30:31], v10 offset:41664\n" \
"v_and_b32_e32 v45, 24, v45\n" \
"v_mfma_f32_16x16x32_bf16 v[112:115], a[200:203], a[68:71], v[112:115]\n" \
"buffer_load_dwordx4 a[156:159], v32, s[56:59], s68 offen offset:3072 sc1\n" \
"v_mfma_f32_16x16x32_bf16 v[116:119], a[200:203], a[84:87], v[116:119]\n" \
"ds_read_b64_tr_b16 a[200:201], v10 offset:27904\n" \
"ds_read_b64_tr_b16 a[202:203], v10 offset:32512\n" \
"v_mfma_f32_16x16x32_bf16 v[116:119], a[196:199], a[80:83], v[116:119]\n" \
"ds_read_b64_tr_b16 a[196:197], v10 offset:37088\n" \
"ds_read_b64_tr_b16 a[198:199], v10 offset:41696\n" \
"v_mfma_f32_16x16x32_bf16 v[120:123], a[204:207], a[64:67], v[120:123]\n" \
"v_and_b32_e32 v44, 0x3c0, v20\n" \
"v_add_u32_e32 v45, 0x1d400, v45\n" \
"v_mfma_f32_16x16x32_bf16 v[120:123], a[208:211], a[68:71], v[120:123]\n" \
"v_add_u32_e32 v36, v44, v45\n" \
"v_mfma_f32_16x16x32_bf16 v[124:127], a[208:211], a[84:87], v[124:127]\n" \
"buffer_load_dwordx4 a[152:155], v32, s[56:59], s68 offen offset:2048 sc1\n" \
"v_mfma_f32_16x16x32_bf16 v[124:127], a[204:207], a[80:83], v[124:127]\n" \
"ds_read_b64_tr_b16 a[204:205], v10 offset:37120\n" \
"ds_read_b64_tr_b16 a[206:207], v10 offset:41728\n" \
"v_mfma_f32_16x16x32_bf16 v[128:131], a[212:215], a[64:67], v[128:131]\n" \
"ds_read_b64_tr_b16 a[208:209], v10 offset:46080\n" \
"ds_read_b64_tr_b16 a[210:211], v10 offset:50688\n" \
"v_mfma_f32_16x16x32_bf16 v[128:131], a[216:219], a[68:71], v[128:131]\n" \
"v_mfma_f32_16x16x32_bf16 v[132:135], a[216:219], a[84:87], v[132:135]\n" \
"ds_read_b64_tr_b16 a[216:217], v10 offset:46112\n" \
"ds_read_b64_tr_b16 a[218:219], v10 offset:50720\n" \
"buffer_load_dwordx4 a[148:151], v32, s[56:59], s68 offen offset:1024 sc1\n" \
"v_mfma_f32_16x16x32_bf16 v[132:135], a[212:215], a[80:83], v[132:135]\n" \
"ds_read_b64_tr_b16 a[212:213], v10 offset:55296\n" \
"ds_read_b64_tr_b16 a[214:215], v10 offset:59904\n" \
"v_mfma_f32_16x16x32_bf16 v[136:139], a[0:3], a[64:67], v[136:139]\n" \
"v_mfma_f32_16x16x32_bf16 v[136:139], a[4:7], a[68:71], v[136:139]\n" \
"v_mfma_f32_16x16x32_bf16 v[140:143], a[4:7], a[84:87], v[140:143]\n" \
"ds_read_b64_tr_b16 a[4:5], v10 offset:46144\n" \
"ds_read_b64_tr_b16 a[6:7], v10 offset:50752\n" \
"buffer_load_dwordx4 a[144:147], v32, s[56:59], s68 offen offset:0 sc1\n" \
"v_mfma_f32_16x16x32_bf16 v[140:143], a[0:3], a[80:83], v[140:143]\n" \
"ds_read_b64_tr_b16 a[0:1], v10 offset:55328\n" \
"ds_read_b64_tr_b16 a[2:3], v10 offset:59936\n" \
"v_mfma_f32_16x16x32_bf16 v[144:147], a[8:11], a[64:67], v[144:147]\n" \
"v_readlane_b32 s68, v11, 49\n" \
"v_mfma_f32_16x16x32_bf16 v[144:147], a[12:15], a[68:71], v[144:147]\n" \
"v_mfma_f32_16x16x32_bf16 v[148:151], a[12:15], a[84:87], v[148:151]\n" \
"ds_read_b64_tr_b16 a[12:13], v10 offset:46176\n" \
"ds_read_b64_tr_b16 a[14:15], v10 offset:50784\n" \
"buffer_load_dwordx4 a[96:99], v20, s[56:59], s69 offen offset:0 sc1\n" \
"v_mfma_f32_16x16x32_bf16 v[148:151], a[8:11], a[80:83], v[148:151]\n" \
"ds_read_b64_tr_b16 a[8:9], v10 offset:55360\n" \
"s_waitcnt lgkmcnt(15)\n" \
"v_mfma_f32_16x16x32_bf16 v[152:155], a[16:19], a[64:67], v[152:155]\n" \
"ds_read_b64_tr_b16 a[10:11], v10 offset:59968\n" \
"v_mfma_f32_16x16x32_bf16 v[152:155], a[20:23], a[68:71], v[152:155]\n" \
"v_mfma_f32_16x16x32_bf16 v[156:159], a[20:23], a[84:87], v[156:159]\n" \
"ds_read_b64_tr_b16 a[20:21], v10 offset:46208\n" \
"ds_read_b64_tr_b16 a[22:23], v10 offset:50816\n" \
"buffer_load_dwordx4 a[100:103], v20, s[56:59], s69 offen offset:1024 sc1\n" \
"v_mfma_f32_16x16x32_bf16 v[156:159], a[16:19], a[80:83], v[156:159]\n" \
"ds_read_b64_tr_b16 a[16:17], v10 offset:55392\n" \
"ds_read_b64_tr_b16 a[18:19], v10 offset:60000\n" \
"v_mfma_f32_16x16x32_bf16 v[160:163], a[24:27], a[64:67], v[160:163]\n" \
"v_mfma_f32_16x16x32_bf16 v[160:163], a[28:31], a[68:71], v[160:163]\n" \
"v_mfma_f32_16x16x32_bf16 v[164:167], a[28:31], a[84:87], v[164:167]\n" \
"ds_read_b64_tr_b16 a[28:29], v10 offset:46240\n" \
"ds_read_b64_tr_b16 a[30:31], v10 offset:50848\n" \
"buffer_load_dwordx4 a[104:107], v20, s[56:59], s69 offen offset:2048 sc1\n" \
"v_mfma_f32_16x16x32_bf16 v[164:167], a[24:27], a[80:83], v[164:167]\n" \
"ds_read_b64_tr_b16 a[24:25], v10 offset:55424\n" \
"ds_read_b64_tr_b16 a[26:27], v10 offset:60032\n" \
"v_mfma_f32_16x16x32_bf16 v[168:171], a[192:195], a[64:67], v[168:171]\n" \
"v_mfma_f32_16x16x32_bf16 v[168:171], a[196:199], a[68:71], v[168:171]\n" \
"v_mfma_f32_16x16x32_bf16 v[172:175], a[196:199], a[84:87], v[172:175]\n" \
"ds_read_b64_tr_b16 a[196:197], v10 offset:46272\n" \
"ds_read_b64_tr_b16 a[198:199], v10 offset:50880\n" \
"buffer_load_dwordx4 a[108:111], v20, s[56:59], s69 offen offset:3072 sc1\n" \
"v_mfma_f32_16x16x32_bf16 v[172:175], a[192:195], a[80:83], v[172:175]\n" \
"ds_read_b64_tr_b16 a[192:193], v10 offset:55456\n" \
"ds_read_b64_tr_b16 a[194:195], v10 offset:60064\n" \
"v_mfma_f32_16x16x32_bf16 v[176:179], a[200:203], a[64:67], v[176:179]\n" \
"s_waitcnt lgkmcnt(13)\n" \
"v_mfma_f32_16x16x32_bf16 v[176:179], a[204:207], a[68:71], v[176:179]\n" \
"v_mfma_f32_16x16x32_bf16 v[180:183], a[204:207], a[84:87], v[180:183]\n" \
"ds_read_b64_tr_b16 a[204:205], v10 offset:46304\n" \
"ds_read_b64_tr_b16 a[206:207], v10 offset:50912\n" \
"buffer_load_dwordx4 a[124:127], v32, s[56:59], s69 offen offset:3072 sc1\n" \
"v_mfma_f32_16x16x32_bf16 v[180:183], a[200:203], a[80:83], v[180:183]\n" \
"ds_read_b64_tr_b16 a[200:201], v10 offset:55488\n" \
"ds_read_b64_tr_b16 a[202:203], v10 offset:60096\n" \
"v_mfma_f32_16x16x32_bf16 v[112:115], a[208:211], a[72:75], v[112:115]\n" \
"v_mfma_f32_16x16x32_bf16 v[112:115], a[212:215], a[76:79], v[112:115]\n" \
"v_mfma_f32_16x16x32_bf16 v[116:119], a[212:215], a[92:95], v[116:119]\n" \
"ds_read_b64_tr_b16 a[212:213], v10 offset:46336\n" \
"ds_read_b64_tr_b16 a[214:215], v10 offset:50944\n" \
"buffer_load_dwordx4 a[120:123], v32, s[56:59], s69 offen offset:2048 sc1\n" \
"v_mfma_f32_16x16x32_bf16 v[116:119], a[208:211], a[88:91], v[116:119]\n" \
"ds_read_b64_tr_b16 a[208:209], v10 offset:55520\n" \
"ds_read_b64_tr_b16 a[210:211], v10 offset:60128\n" \
"v_mfma_f32_16x16x32_bf16 v[120:123], a[216:219], a[72:75], v[120:123]\n" \
"v_mfma_f32_16x16x32_bf16 v[120:123], a[0:3], a[76:79], v[120:123]\n" \
"v_mfma_f32_16x16x32_bf16 v[124:127], a[0:3], a[92:95], v[124:127]\n" \
"v_mfma_f32_16x16x32_bf16 v[124:127], a[216:219], a[88:91], v[124:127]\n" \
"buffer_load_dwordx4 a[116:119], v32, s[56:59], s69 offen offset:1024 sc1\n" \
"v_mfma_f32_16x16x32_bf16 v[128:131], a[4:7], a[72:75], v[128:131]\n" \
"ds_read_b64_tr_b16 a[216:217], v10 offset:55552\n" \
"s_waitcnt lgkmcnt(9)\n" \
"v_mfma_f32_16x16x32_bf16 v[128:131], a[8:11], a[76:79], v[128:131]\n" \
"ds_read_b64_tr_b16 a[218:219], v10 offset:60160\n" \
"v_mfma_f32_16x16x32_bf16 v[132:135], a[8:11], a[92:95], v[132:135]\n" \
"v_mfma_f32_16x16x32_bf16 v[132:135], a[4:7], a[88:91], v[132:135]\n" \
"v_mfma_f32_16x16x32_bf16 v[136:139], a[12:15], a[72:75], v[136:139]\n" \
"buffer_load_dwordx4 a[112:115], v32, s[56:59], s69 offen offset:0 sc1\n" \
"v_mfma_f32_16x16x32_bf16 v[136:139], a[16:19], a[76:79], v[136:139]\n" \
"v_readlane_b32 s69, v11, 5\n" \
"v_mfma_f32_16x16x32_bf16 v[140:143], a[16:19], a[92:95], v[140:143]\n" \
"v_mfma_f32_16x16x32_bf16 v[140:143], a[12:15], a[88:91], v[140:143]\n" \
"s_waitcnt vmcnt(16)\n" \
"s_barrier\n" \
"buffer_load_dwordx4 a[32:35], v20, s[56:59], s64 offen offset:0 sc1\n" \
"v_mfma_f32_16x16x32_bf16 v[144:147], a[20:23], a[72:75], v[144:147]\n" \
"ds_read_b64_tr_b16 a[0:1], v33 offset:18432\n" \
"ds_read_b64_tr_b16 a[2:3], v33 offset:23040\n" \
"v_mfma_f32_16x16x32_bf16 v[144:147], a[24:27], a[76:79], v[144:147]\n" \
"ds_read_b64_tr_b16 a[4:5], v33 offset:27648\n" \
"ds_read_b64_tr_b16 a[6:7], v33 offset:32256\n" \
"v_mfma_f32_16x16x32_bf16 v[148:151], a[24:27], a[92:95], v[148:151]\n" \
"ds_read_b64_tr_b16 a[8:9], v33 offset:36864\n" \
"ds_read_b64_tr_b16 a[10:11], v33 offset:41472\n" \
"v_mfma_f32_16x16x32_bf16 v[148:151], a[20:23], a[88:91], v[148:151]\n" \
"ds_read_b64_tr_b16 a[12:13], v33 offset:46080\n" \
"ds_read_b64_tr_b16 a[14:15], v33 offset:50688\n" \
"buffer_load_dwordx4 a[36:39], v20, s[56:59], s64 offen offset:1024 sc1\n" \
"v_mfma_f32_16x16x32_bf16 v[152:155], a[28:31], a[72:75], v[152:155]\n" \
"ds_read_b64_tr_b16 a[16:17], v34 offset:0\n" \
"ds_read_b64_tr_b16 a[18:19], v34 offset:4608\n" \
"v_mfma_f32_16x16x32_bf16 v[152:155], a[192:195], a[76:79], v[152:155]\n" \
"ds_read_b64_tr_b16 a[20:21], v34 offset:9216\n" \
"ds_read_b64_tr_b16 a[22:23], v34 offset:13824\n" \
"v_mfma_f32_16x16x32_bf16 v[156:159], a[192:195], a[92:95], v[156:159]\n" \
"ds_read_b64_tr_b16 a[24:25], v34 offset:18432\n" \
"ds_read_b64_tr_b16 a[26:27], v34 offset:23040\n" \
"v_mfma_f32_16x16x32_bf16 v[156:159], a[28:31], a[88:91], v[156:159]\n" \
"ds_read_b64_tr_b16 a[28:29], v33 offset:0\n" \
"ds_read_b64_tr_b16 a[30:31], v33 offset:4608\n" \
"buffer_load_dwordx4 a[40:43], v20, s[56:59], s64 offen offset:2048 sc1\n" \
"v_mfma_f32_16x16x32_bf16 v[160:163], a[196:199], a[72:75], v[160:163]\n" \
"ds_read_b64_tr_b16 a[192:193], v33 offset:18464\n" \
"s_waitcnt lgkmcnt(15)\n" \
"v_mfma_f32_16x16x32_bf16 v[160:163], a[200:203], a[76:79], v[160:163]\n" \
"ds_read_b64_tr_b16 a[194:195], v33 offset:23072\n" \
"v_mfma_f32_16x16x32_bf16 v[164:167], a[200:203], a[92:95], v[164:167]\n" \
"buffer_load_dwordx4 a[44:47], v20, s[56:59], s64 offen offset:3072 sc1\n" \
"v_mfma_f32_16x16x32_bf16 v[164:167], a[196:199], a[88:91], v[164:167]\n" \
"ds_read_b64_tr_b16 a[196:197], v33 offset:27680\n" \
"ds_read_b64_tr_b16 a[198:199], v33 offset:32288\n" \
"v_mfma_f32_16x16x32_bf16 v[168:171], a[204:207], a[72:75], v[168:171]\n" \
"ds_read_b64_tr_b16 a[200:201], v33 offset:36896\n" \
"ds_read_b64_tr_b16 a[202:203], v33 offset:41504\n" \
"v_mfma_f32_16x16x32_bf16 v[168:171], a[208:211], a[76:79], v[168:171]\n" \
"v_mfma_f32_16x16x32_bf16 v[172:175], a[208:211], a[92:95], v[172:175]\n" \
"ds_read_b64_tr_b16 a[208:209], v34 offset:32\n" \
"ds_read_b64_tr_b16 a[210:211], v34 offset:4640\n" \
"buffer_load_dwordx4 a[60:63], v32, s[56:59], s64 offen offset:3072 sc1\n" \
"v_mfma_f32_16x16x32_bf16 v[172:175], a[204:207], a[88:91], v[172:175]\n" \
"ds_read_b64_tr_b16 a[204:205], v33 offset:46112\n" \
"ds_read_b64_tr_b16 a[206:207], v33 offset:50720\n" \
"v_mfma_f32_16x16x32_bf16 v[176:179], a[212:215], a[72:75], v[176:179]\n" \
"v_mfma_f32_16x16x32_bf16 v[176:179], a[216:219], a[76:79], v[176:179]\n" \
"v_mfma_f32_16x16x32_bf16 v[180:183], a[216:219], a[92:95], v[180:183]\n" \
"ds_read_b64_tr_b16 a[216:217], v34 offset:18464\n" \
"ds_read_b64_tr_b16 a[218:219], v34 offset:23072\n" \
"buffer_load_dwordx4 a[56:59], v32, s[56:59], s64 offen offset:2048 sc1\n" \
"v_mfma_f32_16x16x32_bf16 v[180:183], a[212:215], a[88:91], v[180:183]\n" \
"ds_read_b64_tr_b16 a[212:213], v34 offset:9248\n" \
"s_waitcnt vmcnt(21)\n" \
"v_mfma_f32_16x16x32_bf16 v[112:115], a[0:3], a[128:131], v[112:115]\n" \
"ds_read_b64_tr_b16 a[214:215], v34 offset:13856\n" \
"s_waitcnt vmcnt(20) lgkmcnt(13)\n" \
"v_mfma_f32_16x16x32_bf16 v[112:115], a[4:7], a[132:135], v[112:115]\n" \
"s_waitcnt vmcnt(19)\n" \
"v_mfma_f32_16x16x32_bf16 v[112:115], a[8:11], a[136:139], v[112:115]\n" \
"s_waitcnt vmcnt(18)\n" \
"v_mfma_f32_16x16x32_bf16 v[112:115], a[12:15], a[140:143], v[112:115]\n" \
"buffer_load_dwordx4 a[52:55], v32, s[56:59], s64 offen offset:1024 sc1\n" \
"s_waitcnt vmcnt(18)\n" \
"v_mfma_f32_16x16x32_bf16 v[116:119], a[12:15], a[156:159], v[116:119]\n" \
"ds_read_b64_tr_b16 a[12:13], v33 offset:36928\n" \
"s_waitcnt vmcnt(17)\n" \
"v_mfma_f32_16x16x32_bf16 v[116:119], a[8:11], a[152:155], v[116:119]\n" \
"ds_read_b64_tr_b16 a[8:9], v33 offset:27712\n" \
"ds_read_b64_tr_b16 a[10:11], v33 offset:32320\n" \
"ds_read_b64_tr_b16 a[14:15], v33 offset:41536\n" \
"s_waitcnt vmcnt(16)\n" \
"v_mfma_f32_16x16x32_bf16 v[116:119], a[4:7], a[148:151], v[116:119]\n" \
"buffer_load_dwordx4 a[48:51], v32, s[56:59], s64 offen offset:0 sc1\n" \
"ds_read_b64_tr_b16 a[4:5], v33 offset:18496\n" \
"ds_read_b64_tr_b16 a[6:7], v33 offset:23104\n" \
"v_readlane_b32 s64, v11, 38\n" \
"s_waitcnt vmcnt(16)\n" \
"v_mfma_f32_16x16x32_bf16 v[116:119], a[0:3], a[144:147], v[116:119]\n" \
"buffer_load_dwordx4 a[160:163], v20, s[56:59], s72 offen offset:0 sc1\n" \
"ds_read_b64_tr_b16 a[0:1], v33 offset:32\n" \
"ds_read_b64_tr_b16 a[2:3], v33 offset:4640\n" \
"s_waitcnt vmcnt(16)\n" \
"v_mfma_f32_16x16x32_bf16 v[112:115], a[16:19], a[96:99], v[112:115]\n" \
"buffer_load_dwordx4 a[164:167], v20, s[56:59], s72 offen offset:1024 sc1\n" \
"s_waitcnt vmcnt(16)\n" \
"v_mfma_f32_16x16x32_bf16 v[112:115], a[20:23], a[100:103], v[112:115]\n" \
"buffer_load_dwordx4 a[168:171], v20, s[56:59], s72 offen offset:2048 sc1\n" \
"s_waitcnt vmcnt(16)\n" \
"v_mfma_f32_16x16x32_bf16 v[112:115], a[24:27], a[104:107], v[112:115]\n" \
"buffer_load_dwordx4 a[172:175], v20, s[56:59], s72 offen offset:3072 sc1\n" \
"s_waitcnt vmcnt(16)\n" \
"v_mfma_f32_16x16x32_bf16 v[112:115], a[28:31], a[108:111], v[112:115]\n" \
"buffer_load_dwordx4 a[188:191], v32, s[56:59], s72 offen offset:3072 sc1\n" \
"s_waitcnt vmcnt(16)\n" \
"v_mfma_f32_16x16x32_bf16 v[116:119], a[28:31], a[124:127], v[116:119]\n" \
"buffer_load_dwordx4 a[184:187], v32, s[56:59], s72 offen offset:2048 sc1\n" \
"ds_read_b64_tr_b16 a[28:29], v34 offset:18496\n" \
"ds_read_b64_tr_b16 a[30:31], v34 offset:23104\n" \
"s_waitcnt vmcnt(16)\n" \
"v_mfma_f32_16x16x32_bf16 v[116:119], a[24:27], a[120:123], v[116:119]\n" \
"v_cvt_pk_bf16_f32 v112, v112, v113\n" \
"v_cvt_pk_bf16_f32 v113, v114, v115\n" \
"s_waitcnt vmcnt(15)\n" \
"v_mfma_f32_16x16x32_bf16 v[116:119], a[20:23], a[116:119], v[116:119]\n" \
"buffer_load_dwordx4 a[180:183], v32, s[56:59], s72 offen offset:1024 sc1\n" \
"ds_read_b64_tr_b16 a[20:21], v34 offset:64\n" \
"ds_read_b64_tr_b16 a[22:23], v34 offset:4672\n" \
"ds_read_b64_tr_b16 a[24:25], v34 offset:9280\n" \
"ds_read_b64_tr_b16 a[26:27], v34 offset:13888\n" \
"s_waitcnt vmcnt(15)\n" \
"v_mfma_f32_16x16x32_bf16 v[116:119], a[16:19], a[112:115], v[116:119]\n" \
"ds_read_b64_tr_b16 a[16:17], v33 offset:46144\n" \
"buffer_load_dwordx4 a[176:179], v32, s[56:59], s72 offen offset:0 sc1\n" \
"s_waitcnt lgkmcnt(5)\n" \
"v_mfma_f32_16x16x32_bf16 v[120:123], a[192:195], a[128:131], v[120:123]\n" \
"ds_read_b64_tr_b16 a[18:19], v33 offset:50752\n" \
"v_readlane_b32 s72, v11, 47\n" \
"v_mfma_f32_16x16x32_bf16 v[120:123], a[196:199], a[132:135], v[120:123]\n" \
"v_mfma_f32_16x16x32_bf16 v[120:123], a[200:203], a[136:139], v[120:123]\n" \
"v_cvt_pk_bf16_f32 v114, v116, v117\n" \
"v_cvt_pk_bf16_f32 v115, v118, v119\n" \
"v_mfma_f32_16x16x32_bf16 v[120:123], a[204:207], a[140:143], v[120:123]\n" \
"buffer_store_dwordx4 v[112:115], v20, s[56:59], s65 offen offset:0 sc1\n" \
"s_add_u32 s65, s65, 0x3800\n" \
"v_mfma_f32_16x16x32_bf16 v[124:127], a[204:207], a[156:159], v[124:127]\n" \
"ds_read_b64_tr_b16 a[204:205], v10 offset:46336\n" \
"ds_read_b64_tr_b16 a[206:207], v10 offset:50944\n" \
"v_mfma_f32_16x16x32_bf16 v[124:127], a[200:203], a[152:155], v[124:127]\n" \
"ds_read_b64_tr_b16 a[200:201], v10 offset:37120\n" \
"ds_read_b64_tr_b16 a[202:203], v10 offset:41728\n" \
"v_mfma_f32_16x16x32_bf16 v[124:127], a[196:199], a[148:151], v[124:127]\n" \
"ds_read_b64_tr_b16 a[196:197], v10 offset:27904\n" \
"ds_read_b64_tr_b16 a[198:199], v10 offset:32512\n" \
"v_mfma_f32_16x16x32_bf16 v[124:127], a[192:195], a[144:147], v[124:127]\n" \
"buffer_load_dwordx4 a[220:223], v20, s[56:59], s93 offen offset:0 sc1\n" \
"v_mfma_f32_16x16x32_bf16 v[120:123], a[208:211], a[96:99], v[120:123]\n" \
"ds_read_b64_tr_b16 a[192:193], v33 offset:64\n" \
"ds_read_b64_tr_b16 a[194:195], v33 offset:4672\n" \
"v_mfma_f32_16x16x32_bf16 v[120:123], a[212:215], a[100:103], v[120:123]\n" \
"v_mfma_f32_16x16x32_bf16 v[120:123], a[216:219], a[104:107], v[120:123]\n" \
"v_mfma_f32_16x16x32_bf16 v[120:123], a[0:3], a[108:111], v[120:123]\n" \
"v_mfma_f32_16x16x32_bf16 v[124:127], a[0:3], a[124:127], v[124:127]\n" \
"buffer_load_dwordx4 a[224:227], v20, s[56:59], s93 offen offset:1024 sc1\n" \
"v_mfma_f32_16x16x32_bf16 v[124:127], a[216:219], a[120:123], v[124:127]\n" \
"ds_read_b64_tr_b16 a[216:217], v33 offset:27904\n" \
"ds_read_b64_tr_b16 a[218:219], v33 offset:32512\n" \
"v_mfma_f32_16x16x32_bf16 v[124:127], a[212:215], a[116:119], v[124:127]\n" \
"ds_read_b64_tr_b16 a[212:213], v33 offset:18688\n" \
"ds_read_b64_tr_b16 a[214:215], v33 offset:23296\n" \
"v_cvt_pk_bf16_f32 v120, v120, v121\n" \
"v_cvt_pk_bf16_f32 v121, v122, v123\n" \
"v_mfma_f32_16x16x32_bf16 v[124:127], a[208:211], a[112:115], v[124:127]\n" \
"ds_read_b64_tr_b16 a[208:209], v10 offset:55552\n" \
"ds_read_b64_tr_b16 a[210:211], v10 offset:60160\n" \
"v_mfma_f32_16x16x32_bf16 v[128:131], a[4:7], a[128:131], v[128:131]\n" \
"buffer_load_dwordx4 a[228:231], v20, s[56:59], s93 offen offset:2048 sc1\n" \
"v_mfma_f32_16x16x32_bf16 v[128:131], a[8:11], a[132:135], v[128:131]\n" \
"ds_read_b64_tr_b16 a[0:1], v33 offset:37120\n" \
"ds_read_b64_tr_b16 a[2:3], v33 offset:41728\n" \
"v_mfma_f32_16x16x32_bf16 v[128:131], a[12:15], a[136:139], v[128:131]\n" \
"v_cvt_pk_bf16_f32 v122, v124, v125\n" \
"v_cvt_pk_bf16_f32 v123, v126, v127\n" \
"s_waitcnt lgkmcnt(13)\n" \
"v_mfma_f32_16x16x32_bf16 v[128:131], a[16:19], a[140:143], v[128:131]\n" \
"buffer_store_dwordx4 v[120:123], v20, s[56:59], s66 offen offset:0 sc1\n" \
"s_add_u32 s66, s66, 0x3000\n" \
"v_mfma_f32_16x16x32_bf16 v[132:135], a[16:19], a[156:159], v[132:135]\n" \
"ds_read_b64_tr_b16 a[16:17], v34 offset:18688\n" \
"ds_read_b64_tr_b16 a[18:19], v34 offset:23296\n" \
"v_mfma_f32_16x16x32_bf16 v[132:135], a[12:15], a[152:155], v[132:135]\n" \
"ds_read_b64_tr_b16 a[12:13], v34 offset:9472\n" \
"ds_read_b64_tr_b16 a[14:15], v34 offset:14080\n" \
"v_mfma_f32_16x16x32_bf16 v[132:135], a[8:11], a[148:151], v[132:135]\n" \
"ds_read_b64_tr_b16 a[8:9], v34 offset:256\n" \
"ds_read_b64_tr_b16 a[10:11], v34 offset:4864\n" \
"v_mfma_f32_16x16x32_bf16 v[132:135], a[4:7], a[144:147], v[132:135]\n" \
"buffer_load_dwordx4 a[232:235], v20, s[56:59], s93 offen offset:3072 sc1\n" \
"v_mfma_f32_16x16x32_bf16 v[128:131], a[20:23], a[96:99], v[128:131]\n" \
"ds_read_b64_tr_b16 a[4:5], v33 offset:46336\n" \
"ds_read_b64_tr_b16 a[6:7], v33 offset:50944\n" \
"v_mfma_f32_16x16x32_bf16 v[128:131], a[24:27], a[100:103], v[128:131]\n" \
"v_mfma_f32_16x16x32_bf16 v[128:131], a[28:31], a[104:107], v[128:131]\n" \
"s_waitcnt lgkmcnt(15)\n" \
"v_mfma_f32_16x16x32_bf16 v[128:131], a[192:195], a[108:111], v[128:131]\n" \
"v_mfma_f32_16x16x32_bf16 v[132:135], a[192:195], a[124:127], v[132:135]\n" \
"buffer_load_dwordx4 a[248:251], v32, s[56:59], s93 offen offset:3072 sc1\n" \
"v_mfma_f32_16x16x32_bf16 v[132:135], a[28:31], a[120:123], v[132:135]\n" \
"ds_read_b64_tr_b16 a[28:29], v10 offset:36960\n" \
"ds_read_b64_tr_b16 a[30:31], v10 offset:41568\n" \
"v_mfma_f32_16x16x32_bf16 v[132:135], a[24:27], a[116:119], v[132:135]\n" \
"ds_read_b64_tr_b16 a[24:25], v10 offset:27744\n" \
"ds_read_b64_tr_b16 a[26:27], v10 offset:32352\n" \
"v_cvt_pk_bf16_f32 v128, v128, v129\n" \
"v_cvt_pk_bf16_f32 v129, v130, v131\n" \
"L_pf21:\n" \
"s_cbranch_execz L_retB\n" \
"s_nop 0\n" \
"v_mfma_f32_16x16x32_bf16 v[132:135], a[20:23], a[112:115], v[132:135]\n" \
"ds_read_b64_tr_b16 a[20:21], v33 offset:256\n" \
"s_waitcnt vmcnt(22)\n" \
"v_mfma_f32_16x16x32_bf16 v[248:251], a[196:199], a[32:35], v[248:251]\n" \
"ds_read_b64_tr_b16 a[22:23], v33 offset:4864\n" \
"buffer_load_dwordx4 a[244:247], v32, s[56:59], s93 offen offset:2048 sc1\n" \
"s_waitcnt vmcnt(22)\n" \
"v_mfma_f32_16x16x32_bf16 v[248:251], a[200:203], a[36:39], v[248:251]\n" \
"ds_read_b64_tr_b16 a[192:193], v10 offset:46176\n" \
"v_cvt_pk_bf16_f32 v130, v132, v133\n" \
"v_cvt_pk_bf16_f32 v131, v134, v135\n" \
"s_waitcnt vmcnt(21)\n" \
"v_mfma_f32_16x16x32_bf16 v[248:251], a[204:207], a[40:43], v[248:251]\n" \
"ds_read_b64_tr_b16 a[194:195], v10 offset:50784\n" \
"buffer_store_dwordx4 v[128:131], v20, s[56:59], s67 offen offset:0 sc1\n" \
"s_add_u32 s67, s67, 0x2800\n" \
"s_waitcnt vmcnt(21) lgkmcnt(10)\n" \
"v_mfma_f32_16x16x32_bf16 v[248:251], a[208:211], a[44:47], v[248:251]\n" \
"s_waitcnt vmcnt(20)\n" \
"v_mfma_f32_16x16x32_bf16 v[252:255], a[208:211], a[60:63], v[252:255]\n" \
"ds_read_b64_tr_b16 a[208:209], v33 offset:36960\n" \
"ds_read_b64_tr_b16 a[210:211], v33 offset:41568\n" \
"s_waitcnt vmcnt(19)\n" \
"v_mfma_f32_16x16x32_bf16 v[252:255], a[204:207], a[56:59], v[252:255]\n" \
"buffer_load_dwordx4 a[240:243], v32, s[56:59], s93 offen offset:1024 sc1\n" \
"ds_read_b64_tr_b16 a[204:205], v33 offset:27744\n" \
"ds_read_b64_tr_b16 a[206:207], v33 offset:32352\n" \
"s_waitcnt vmcnt(19)\n" \
"v_mfma_f32_16x16x32_bf16 v[252:255], a[200:203], a[52:55], v[252:255]\n" \
"buffer_load_dwordx4 a[236:239], v32, s[56:59], s93 offen offset:0 sc1\n" \
"ds_read_b64_tr_b16 a[200:201], v33 offset:18528\n" \
"ds_read_b64_tr_b16 a[202:203], v33 offset:23136\n" \
"v_readlane_b32 s93, v11, 43\n" \
"s_waitcnt vmcnt(19)\n" \
"v_mfma_f32_16x16x32_bf16 v[252:255], a[196:199], a[48:51], v[252:255]\n" \
"ds_read_b64_tr_b16 a[196:197], v10 offset:55392\n" \
"ds_read_b64_tr_b16 a[198:199], v10 offset:60000\n" \
"v_mfma_f32_16x16x32_bf16 v[176:179], a[212:215], a[128:131], v[176:179]\n" \
"buffer_load_dwordx4 v[64:67], v20, s[56:59], s95 offen offset:0 sc1\n" \
"v_mfma_f32_16x16x32_bf16 v[176:179], a[216:219], a[132:135], v[176:179]\n" \
"v_mfma_f32_16x16x32_bf16 v[176:179], a[0:3], a[136:139], v[176:179]\n" \
"s_waitcnt lgkmcnt(6)\n" \
"v_mfma_f32_16x16x32_bf16 v[176:179], a[4:7], a[140:143], v[176:179]\n" \
"v_mfma_f32_16x16x32_bf16 v[180:183], a[4:7], a[156:159], v[180:183]\n" \
"v_mfma_f32_16x16x32_bf16 v[180:183], a[0:3], a[152:155], v[180:183]\n" \
"buffer_load_dwordx4 v[72:75], v32, s[56:59], s95 offen offset:0 sc1\n" \
"v_mfma_f32_16x16x32_bf16 v[180:183], a[216:219], a[148:151], v[180:183]\n" \
"v_mfma_f32_16x16x32_bf16 v[180:183], a[212:215], a[144:147], v[180:183]\n" \
"s_waitcnt vmcnt(20)\n" \
"v_mfma_f32_16x16x32_bf16 v[248:251], a[212:215], a[160:163], v[248:251]\n" \
"s_waitcnt vmcnt(19)\n" \
"v_mfma_f32_16x16x32_bf16 v[248:251], a[216:219], a[164:167], v[248:251]\n" \
"s_waitcnt vmcnt(18)\n" \
"v_mfma_f32_16x16x32_bf16 v[248:251], a[0:3], a[168:171], v[248:251]\n" \
"buffer_load_dwordx4 v[80:83], v20, s[56:59], s96 offen offset:0 sc1\n" \
"s_waitcnt vmcnt(18)\n" \
"v_mfma_f32_16x16x32_bf16 v[248:251], a[4:7], a[172:175], v[248:251]\n" \
"buffer_load_dwordx4 v[88:91], v32, s[56:59], s96 offen offset:0 sc1\n" \
"s_waitcnt vmcnt(18)\n" \
"v_mfma_f32_16x16x32_bf16 v[252:255], a[4:7], a[188:191], v[252:255]\n" \
"ds_read_b64_tr_b16 a[4:5], v34 offset:18528\n" \
"ds_read_b64_tr_b16 a[6:7], v34 offset:23136\n" \
"buffer_load_dwordx4 v[68:71], v20, s[56:59], s95 offen offset:1024 sc1\n" \
"s_waitcnt vmcnt(18)\n" \
"v_mfma_f32_16x16x32_bf16 v[252:255], a[0:3], a[184:187], v[252:255]\n" \
"ds_read_b64_tr_b16 a[0:1], v34 offset:9312\n" \
"ds_read_b64_tr_b16 a[2:3], v34 offset:13920\n" \
"buffer_load_dwordx4 v[76:79], v32, s[56:59], s95 offen offset:1024 sc1\n" \
"s_add_u32 s95, s63, 0x0\n" \
"s_waitcnt vmcnt(18)\n" \
"v_mfma_f32_16x16x32_bf16 v[252:255], a[216:219], a[180:183], v[252:255]\n" \
"ds_read_b64_tr_b16 a[216:217], v34 offset:96\n" \
"ds_read_b64_tr_b16 a[218:219], v34 offset:4704\n" \
"buffer_load_dwordx4 v[84:87], v20, s[56:59], s96 offen offset:1024 sc1\n" \
"s_waitcnt vmcnt(18)\n" \
"v_mfma_f32_16x16x32_bf16 v[252:255], a[212:215], a[176:179], v[252:255]\n" \
"ds_read_b64_tr_b16 a[212:213], v33 offset:46176\n" \
"ds_read_b64_tr_b16 a[214:215], v33 offset:50784\n" \
"v_mfma_f32_16x16x32_bf16 v[176:179], a[8:11], a[96:99], v[176:179]\n" \
"buffer_load_dwordx4 v[92:95], v32, s[56:59], s96 offen offset:1024 sc1\n" \
"v_mfma_f32_16x16x32_bf16 v[176:179], a[12:15], a[100:103], v[176:179]\n" \
"s_add_u32 s96, s63, 0x800\n" \
"v_mfma_f32_16x16x32_bf16 v[176:179], a[16:19], a[104:107], v[176:179]\n" \
"v_mfma_f32_16x16x32_bf16 v[176:179], a[20:23], a[108:111], v[176:179]\n" \
"s_waitcnt vmcnt(18)\n" \
"s_mov_b64 exec, 1\n" \
"buffer_store_dword v39, off, s[56:59], s65 offset:0 sc1\n" \
"s_mov_b64 exec, -1\n" \
"v_mfma_f32_16x16x32_bf16 v[180:183], a[20:23], a[124:127], v[180:183]\n" \
"s_add_u32 s65, s63, 0x1800\n" \
"v_mfma_f32_16x16x32_bf16 v[180:183], a[16:19], a[120:123], v[180:183]\n" \
"v_mfma_f32_16x16x32_bf16 v[180:183], a[12:15], a[116:119], v[180:183]\n" \
"v_cvt_pk_bf16_f32 v176, v176, v177\n" \
"v_cvt_pk_bf16_f32 v177, v178, v179\n" \
"v_mfma_f32_16x16x32_bf16 v[180:183], a[8:11], a[112:115], v[180:183]\n" \
"s_waitcnt vmcnt(18)\n" \
"v_mfma_f32_16x16x32_bf16 v[248:251], a[8:11], a[220:223], v[248:251]\n" \
"buffer_load_dwordx4 v[96:99], v20, s[56:59], s97 offen offset:0 sc1\n" \
"s_waitcnt vmcnt(18)\n" \
"v_mfma_f32_16x16x32_bf16 v[248:251], a[12:15], a[224:227], v[248:251]\n" \
"s_waitcnt vmcnt(17)\n" \
"v_mfma_f32_16x16x32_bf16 v[248:251], a[16:19], a[228:231], v[248:251]\n" \
"buffer_load_dwordx4 v[104:107], v32, s[56:59], s97 offen offset:0 sc1\n" \
"v_cvt_pk_bf16_f32 v178, v180, v181\n" \
"v_cvt_pk_bf16_f32 v179, v182, v183\n" \
"s_waitcnt vmcnt(16)\n" \
"v_mfma_f32_16x16x32_bf16 v[248:251], a[20:23], a[232:235], v[248:251]\n" \
"buffer_store_dwordx4 v[176:179], v20, s[56:59], s68 offen offset:0 sc1\n" \
"s_mov_b64 exec, 1\n" \
"buffer_store_dword v39, off, s[56:59], s66 offset:4 sc1\n" \
"s_mov_b64 exec, -1\n" \
"s_add_u32 s66, s63, 0x2000\n" \
"s_waitcnt vmcnt(17)\n" \
"v_mfma_f32_16x16x32_bf16 v[252:255], a[20:23], a[248:251], v[252:255]\n" \
"ds_read_b64_tr_b16 a[20:21], v10 offset:46208\n" \
"v_cvt_pk_bf16_f32 v248, v248, v249\n" \
"v_cvt_pk_bf16_f32 v249, v250, v251\n" \
"s_waitcnt vmcnt(16)\n" \
"v_mfma_f32_16x16x32_bf16 v[252:255], a[16:19], a[244:247], v[252:255]\n" \
"ds_read_b64_tr_b16 a[16:17], v10 offset:36992\n" \
"ds_read_b64_tr_b16 a[18:19], v10 offset:41600\n" \
"ds_read_b64_tr_b16 a[22:23], v10 offset:50816\n" \
"buffer_load_dwordx4 v[100:103], v20, s[56:59], s97 offen offset:1024 sc1\n" \
"s_waitcnt vmcnt(15)\n" \
"v_mfma_f32_16x16x32_bf16 v[252:255], a[12:15], a[240:243], v[252:255]\n" \
"s_mov_b64 exec, 1\n" \
"buffer_store_dword v39, off, s[56:59], s67 offset:8 sc1\n" \
"s_mov_b64 exec, -1\n" \
"ds_read_b64_tr_b16 a[12:13], v10 offset:27776\n" \
"ds_read_b64_tr_b16 a[14:15], v10 offset:32384\n" \
"s_add_u32 s67, s63, 0x2800\n" \
"s_waitcnt vmcnt(15)\n" \
"v_mfma_f32_16x16x32_bf16 v[252:255], a[8:11], a[236:239], v[252:255]\n" \
"ds_read_b64_tr_b16 a[8:9], v33 offset:96\n" \
"ds_read_b64_tr_b16 a[10:11], v33 offset:4704\n" \
"v_mfma_f32_16x16x32_bf16 v[208:211], a[24:27], a[32:35], v[208:211]\n" \
"buffer_load_dwordx4 v[108:111], v32, s[56:59], s97 offen offset:1024 sc1\n" \
"v_mfma_f32_16x16x32_bf16 v[208:211], a[28:31], a[36:39], v[208:211]\n" \
"s_add_u32 s97, s63, 0x1000\n" \
"v_mfma_f32_16x16x32_bf16 v[208:211], a[192:195], a[40:43], v[208:211]\n" \
"s_waitcnt lgkmcnt(4)\n" \
"v_mfma_f32_16x16x32_bf16 v[208:211], a[196:199], a[44:47], v[208:211]\n" \
"v_cvt_pk_bf16_f32 v250, v252, v253\n" \
"v_cvt_pk_bf16_f32 v251, v254, v255\n" \
"v_mfma_f32_16x16x32_bf16 v[212:215], a[196:199], a[60:63], v[212:215]\n" \
"buffer_store_dwordx4 v[248:251], v20, s[56:59], s68 offen offset:1024 sc1\n" \
"v_mfma_f32_16x16x32_bf16 v[212:215], a[192:195], a[56:59], v[212:215]\n" \
"ds_read_b64_tr_b16 a[192:193], v33 offset:27776\n" \
"ds_read_b64_tr_b16 a[194:195], v33 offset:32384\n" \
"v_mfma_f32_16x16x32_bf16 v[212:215], a[28:31], a[52:55], v[212:215]\n" \
"ds_read_b64_tr_b16 a[28:29], v33 offset:18560\n" \
"ds_read_b64_tr_b16 a[30:31], v33 offset:23168\n" \
"v_mfma_f32_16x16x32_bf16 v[212:215], a[24:27], a[48:51], v[212:215]\n" \
"ds_read_b64_tr_b16 a[24:25], v10 offset:55424\n" \
"ds_read_b64_tr_b16 a[26:27], v10 offset:60032\n" \
"v_mfma_f32_16x16x32_bf16 v[136:139], a[200:203], a[128:131], v[136:139]\n" \
"ds_read_b64_tr_b16 a[196:197], v33 offset:36992\n" \
"ds_read_b64_tr_b16 a[198:199], v33 offset:41600\n" \
"v_mfma_f32_16x16x32_bf16 v[136:139], a[204:207], a[132:135], v[136:139]\n" \
"s_add_u32 s68, s63, 0x3000\n" \
"v_mfma_f32_16x16x32_bf16 v[136:139], a[208:211], a[136:139], v[136:139]\n" \
"v_mfma_f32_16x16x32_bf16 v[136:139], a[212:215], a[140:143], v[136:139]\n" \
"v_mfma_f32_16x16x32_bf16 v[140:143], a[212:215], a[156:159], v[140:143]\n" \
"v_mfma_f32_16x16x32_bf16 v[140:143], a[208:211], a[152:155], v[140:143]\n" \
"v_mfma_f32_16x16x32_bf16 v[140:143], a[204:207], a[148:151], v[140:143]\n" \
"v_mfma_f32_16x16x32_bf16 v[140:143], a[200:203], a[144:147], v[140:143]\n" \
"v_mfma_f32_16x16x32_bf16 v[208:211], a[200:203], a[160:163], v[208:211]\n" \
"v_mfma_f32_16x16x32_bf16 v[208:211], a[204:207], a[164:167], v[208:211]\n" \
"v_mfma_f32_16x16x32_bf16 v[208:211], a[208:211], a[168:171], v[208:211]\n" \
"v_mfma_f32_16x16x32_bf16 v[208:211], a[212:215], a[172:175], v[208:211]\n" \
"v_mfma_f32_16x16x32_bf16 v[212:215], a[212:215], a[188:191], v[212:215]\n" \
"ds_read_b64_tr_b16 a[212:213], v34 offset:18560\n" \
"ds_read_b64_tr_b16 a[214:215], v34 offset:23168\n" \
"v_mfma_f32_16x16x32_bf16 v[212:215], a[208:211], a[184:187], v[212:215]\n" \
"ds_read_b64_tr_b16 a[208:209], v34 offset:9344\n" \
"ds_read_b64_tr_b16 a[210:211], v34 offset:13952\n" \
"v_mfma_f32_16x16x32_bf16 v[212:215], a[204:207], a[180:183], v[212:215]\n" \
"ds_read_b64_tr_b16 a[204:205], v34 offset:128\n" \
"ds_read_b64_tr_b16 a[206:207], v34 offset:4736\n" \
"v_mfma_f32_16x16x32_bf16 v[212:215], a[200:203], a[176:179], v[212:215]\n" \
"ds_read_b64_tr_b16 a[200:201], v33 offset:46208\n" \
"ds_read_b64_tr_b16 a[202:203], v33 offset:50816\n" \
"v_mfma_f32_16x16x32_bf16 v[136:139], a[216:219], a[96:99], v[136:139]\n" \
"v_mfma_f32_16x16x32_bf16 v[136:139], a[0:3], a[100:103], v[136:139]\n" \
"v_mfma_f32_16x16x32_bf16 v[136:139], a[4:7], a[104:107], v[136:139]\n" \
"s_waitcnt lgkmcnt(8)\n" \
"v_mfma_f32_16x16x32_bf16 v[136:139], a[8:11], a[108:111], v[136:139]\n" \
"v_mfma_f32_16x16x32_bf16 v[140:143], a[8:11], a[124:127], v[140:143]\n" \
"v_mfma_f32_16x16x32_bf16 v[140:143], a[4:7], a[120:123], v[140:143]\n" \
"v_mfma_f32_16x16x32_bf16 v[140:143], a[0:3], a[116:119], v[140:143]\n" \
"v_mfma_f32_16x16x32_bf16 v[140:143], a[216:219], a[112:115], v[140:143]\n" \
"v_mfma_f32_16x16x32_bf16 v[208:211], a[216:219], a[220:223], v[208:211]\n" \
"v_mfma_f32_16x16x32_bf16 v[208:211], a[0:3], a[224:227], v[208:211]\n" \
"v_mfma_f32_16x16x32_bf16 v[208:211], a[4:7], a[228:231], v[208:211]\n" \
"v_mfma_f32_16x16x32_bf16 v[208:211], a[8:11], a[232:235], v[208:211]\n" \
"v_cvt_pk_bf16_f32 v136, v136, v137\n" \
"v_cvt_pk_bf16_f32 v137, v138, v139\n" \
"v_mfma_f32_16x16x32_bf16 v[212:215], a[8:11], a[248:251], v[212:215]\n" \
"ds_read_b64_tr_b16 a[8:9], v10 offset:46240\n" \
"v_cvt_pk_bf16_f32 v138, v140, v141\n" \
"v_cvt_pk_bf16_f32 v139, v142, v143\n" \
"v_mfma_f32_16x16x32_bf16 v[212:215], a[4:7], a[244:247], v[212:215]\n" \
"buffer_store_dwordx4 v[136:139], v20, s[56:59], s70 offen offset:0 sc1\n" \
"v_cvt_pk_bf16_f32 v208, v208, v209\n" \
"v_cvt_pk_bf16_f32 v209, v210, v211\n" \
"v_mfma_f32_16x16x32_bf16 v[212:215], a[0:3], a[240:243], v[212:215]\n" \
"ds_read_b64_tr_b16 a[0:1], v10 offset:27808\n" \
"ds_read_b64_tr_b16 a[2:3], v10 offset:32416\n" \
"v_mfma_f32_16x16x32_bf16 v[212:215], a[216:219], a[236:239], v[212:215]\n" \
"ds_read_b64_tr_b16 a[216:217], v33 offset:128\n" \
"ds_read_b64_tr_b16 a[218:219], v33 offset:4736\n" \
"v_mfma_f32_16x16x32_bf16 v[216:219], a[12:15], a[32:35], v[216:219]\n" \
"ds_read_b64_tr_b16 a[4:5], v10 offset:37024\n" \
"ds_read_b64_tr_b16 a[6:7], v10 offset:41632\n" \
"v_mfma_f32_16x16x32_bf16 v[216:219], a[16:19], a[36:39], v[216:219]\n" \
"ds_read_b64_tr_b16 a[10:11], v10 offset:50848\n" \
"v_mfma_f32_16x16x32_bf16 v[216:219], a[20:23], a[40:43], v[216:219]\n" \
"v_cvt_pk_bf16_f32 v210, v212, v213\n" \
"v_cvt_pk_bf16_f32 v211, v214, v215\n" \
"v_mfma_f32_16x16x32_bf16 v[216:219], a[24:27], a[44:47], v[216:219]\n" \
"buffer_store_dwordx4 v[208:211], v20, s[56:59], s70 offen offset:1024 sc1\n" \
"s_add_u32 s70, s70, 0x2000\n" \
"v_mfma_f32_16x16x32_bf16 v[220:223], a[24:27], a[60:63], v[220:223]\n" \
"ds_read_b64_tr_b16 a[24:25], v33 offset:37024\n" \
"ds_read_b64_tr_b16 a[26:27], v33 offset:41632\n" \
"v_mfma_f32_16x16x32_bf16 v[220:223], a[20:23], a[56:59], v[220:223]\n" \
"ds_read_b64_tr_b16 a[20:21], v33 offset:27808\n" \
"ds_read_b64_tr_b16 a[22:23], v33 offset:32416\n" \
"v_mfma_f32_16x16x32_bf16 v[220:223], a[16:19], a[52:55], v[220:223]\n" \
"ds_read_b64_tr_b16 a[16:17], v33 offset:18592\n" \
"ds_read_b64_tr_b16 a[18:19], v33 offset:23200\n" \
"v_mfma_f32_16x16x32_bf16 v[220:223], a[12:15], a[48:51], v[220:223]\n" \
"ds_read_b64_tr_b16 a[12:13], v10 offset:55456\n" \
"ds_read_b64_tr_b16 a[14:15], v10 offset:60064\n" \
"v_mfma_f32_16x16x32_bf16 v[144:147], a[28:31], a[128:131], v[144:147]\n" \
"v_mfma_f32_16x16x32_bf16 v[144:147], a[192:195], a[132:135], v[144:147]\n" \
"v_mfma_f32_16x16x32_bf16 v[144:147], a[196:199], a[136:139], v[144:147]\n" \
"s_waitcnt lgkmcnt(15)\n" \
"v_mfma_f32_16x16x32_bf16 v[144:147], a[200:203], a[140:143], v[144:147]\n" \
"v_mfma_f32_16x16x32_bf16 v[148:151], a[200:203], a[156:159], v[148:151]\n" \
"v_mfma_f32_16x16x32_bf16 v[148:151], a[196:199], a[152:155], v[148:151]\n" \
"v_mfma_f32_16x16x32_bf16 v[148:151], a[192:195], a[148:151], v[148:151]\n" \
"v_mfma_f32_16x16x32_bf16 v[148:151], a[28:31], a[144:147], v[148:151]\n" \
"v_mfma_f32_16x16x32_bf16 v[216:219], a[28:31], a[160:163], v[216:219]\n" \
"v_mfma_f32_16x16x32_bf16 v[216:219], a[192:195], a[164:167], v[216:219]\n" \
"v_mfma_f32_16x16x32_bf16 v[216:219], a[196:199], a[168:171], v[216:219]\n" \
"v_mfma_f32_16x16x32_bf16 v[216:219], a[200:203], a[172:175], v[216:219]\n" \
"v_mfma_f32_16x16x32_bf16 v[220:223], a[200:203], a[188:191], v[220:223]\n" \
"ds_read_b64_tr_b16 a[200:201], v34 offset:18592\n" \
"ds_read_b64_tr_b16 a[202:203], v34 offset:23200\n" \
"v_mfma_f32_16x16x32_bf16 v[220:223], a[196:199], a[184:187], v[220:223]\n" \
"ds_read_b64_tr_b16 a[196:197], v34 offset:9376\n" \
"ds_read_b64_tr_b16 a[198:199], v34 offset:13984\n" \
"v_mfma_f32_16x16x32_bf16 v[220:223], a[192:195], a[180:183], v[220:223]\n" \
"ds_read_b64_tr_b16 a[192:193], v34 offset:160\n" \
"ds_read_b64_tr_b16 a[194:195], v34 offset:4768\n" \
"v_mfma_f32_16x16x32_bf16 v[220:223], a[28:31], a[176:179], v[220:223]\n" \
"ds_read_b64_tr_b16 a[28:29], v33 offset:46240\n" \
"ds_read_b64_tr_b16 a[30:31], v33 offset:50848\n" \
"v_mfma_f32_16x16x32_bf16 v[144:147], a[204:207], a[96:99], v[144:147]\n" \
"v_mfma_f32_16x16x32_bf16 v[144:147], a[208:211], a[100:103], v[144:147]\n" \
"v_mfma_f32_16x16x32_bf16 v[144:147], a[212:215], a[104:107], v[144:147]\n" \
"s_waitcnt lgkmcnt(8)\n" \
"v_mfma_f32_16x16x32_bf16 v[144:147], a[216:219], a[108:111], v[144:147]\n" \
"v_mfma_f32_16x16x32_bf16 v[148:151], a[216:219], a[124:127], v[148:151]\n" \
"v_mfma_f32_16x16x32_bf16 v[148:151], a[212:215], a[120:123], v[148:151]\n" \
"v_mfma_f32_16x16x32_bf16 v[148:151], a[208:211], a[116:119], v[148:151]\n" \
"v_mfma_f32_16x16x32_bf16 v[148:151], a[204:207], a[112:115], v[148:151]\n" \
"v_mfma_f32_16x16x32_bf16 v[216:219], a[204:207], a[220:223], v[216:219]\n" \
"v_mfma_f32_16x16x32_bf16 v[216:219], a[208:211], a[224:227], v[216:219]\n" \
"v_mfma_f32_16x16x32_bf16 v[216:219], a[212:215], a[228:231], v[216:219]\n" \
"v_mfma_f32_16x16x32_bf16 v[216:219], a[216:219], a[232:235], v[216:219]\n" \
"v_cvt_pk_bf16_f32 v144, v144, v145\n" \
"v_cvt_pk_bf16_f32 v145, v146, v147\n" \
"v_mfma_f32_16x16x32_bf16 v[220:223], a[216:219], a[248:251], v[220:223]\n" \
"ds_read_b64_tr_b16 a[216:217], v10 offset:46272\n" \
"v_cvt_pk_bf16_f32 v146, v148, v149\n" \
"v_cvt_pk_bf16_f32 v147, v150, v151\n" \
"v_mfma_f32_16x16x32_bf16 v[220:223], a[212:215], a[244:247], v[220:223]\n" \
"buffer_store_dwordx4 v[144:147], v20, s[56:59], s71 offen offset:0 sc1\n" \
"v_cvt_pk_bf16_f32 v216, v216, v217\n" \
"v_cvt_pk_bf16_f32 v217, v218, v219\n" \
"v_mfma_f32_16x16x32_bf16 v[220:223], a[208:211], a[240:243], v[220:223]\n" \
"ds_read_b64_tr_b16 a[208:209], v10 offset:27840\n" \
"ds_read_b64_tr_b16 a[210:211], v10 offset:32448\n" \
"v_mfma_f32_16x16x32_bf16 v[220:223], a[204:207], a[236:239], v[220:223]\n" \
"ds_read_b64_tr_b16 a[204:205], v33 offset:160\n" \
"ds_read_b64_tr_b16 a[206:207], v33 offset:4768\n" \
"v_mfma_f32_16x16x32_bf16 v[224:227], a[0:3], a[32:35], v[224:227]\n" \
"ds_read_b64_tr_b16 a[212:213], v10 offset:37056\n" \
"ds_read_b64_tr_b16 a[214:215], v10 offset:41664\n" \
"v_mfma_f32_16x16x32_bf16 v[224:227], a[4:7], a[36:39], v[224:227]\n" \
"ds_read_b64_tr_b16 a[218:219], v10 offset:50880\n" \
"v_mfma_f32_16x16x32_bf16 v[224:227], a[8:11], a[40:43], v[224:227]\n" \
"v_cvt_pk_bf16_f32 v218, v220, v221\n" \
"v_cvt_pk_bf16_f32 v219, v222, v223\n" \
"v_mfma_f32_16x16x32_bf16 v[224:227], a[12:15], a[44:47], v[224:227]\n" \
"buffer_store_dwordx4 v[216:219], v20, s[56:59], s71 offen offset:1024 sc1\n" \
"s_add_u32 s71, s71, 0x1800\n" \
"v_mfma_f32_16x16x32_bf16 v[228:231], a[12:15], a[60:63], v[228:231]\n" \
"ds_read_b64_tr_b16 a[12:13], v33 offset:37056\n" \
"ds_read_b64_tr_b16 a[14:15], v33 offset:41664\n" \
"v_mfma_f32_16x16x32_bf16 v[228:231], a[8:11], a[56:59], v[228:231]\n" \
"ds_read_b64_tr_b16 a[8:9], v33 offset:27840\n" \
"ds_read_b64_tr_b16 a[10:11], v33 offset:32448\n" \
"v_mfma_f32_16x16x32_bf16 v[228:231], a[4:7], a[52:55], v[228:231]\n" \
"ds_read_b64_tr_b16 a[4:5], v33 offset:18624\n" \
"ds_read_b64_tr_b16 a[6:7], v33 offset:23232\n" \
"v_mfma_f32_16x16x32_bf16 v[228:231], a[0:3], a[48:51], v[228:231]\n" \
"ds_read_b64_tr_b16 a[0:1], v10 offset:55488\n" \
"ds_read_b64_tr_b16 a[2:3], v10 offset:60096\n" \
"v_mfma_f32_16x16x32_bf16 v[152:155], a[16:19], a[128:131], v[152:155]\n" \
"v_mfma_f32_16x16x32_bf16 v[152:155], a[20:23], a[132:135], v[152:155]\n" \
"v_mfma_f32_16x16x32_bf16 v[152:155], a[24:27], a[136:139], v[152:155]\n" \
"s_waitcnt lgkmcnt(15)\n" \
"v_mfma_f32_16x16x32_bf16 v[152:155], a[28:31], a[140:143], v[152:155]\n" \
"v_mfma_f32_16x16x32_bf16 v[156:159], a[28:31], a[156:159], v[156:159]\n" \
"v_mfma_f32_16x16x32_bf16 v[156:159], a[24:27], a[152:155], v[156:159]\n" \
"v_mfma_f32_16x16x32_bf16 v[156:159], a[20:23], a[148:151], v[156:159]\n" \
"v_mfma_f32_16x16x32_bf16 v[156:159], a[16:19], a[144:147], v[156:159]\n" \
"v_mfma_f32_16x16x32_bf16 v[224:227], a[16:19], a[160:163], v[224:227]\n" \
"v_mfma_f32_16x16x32_bf16 v[224:227], a[20:23], a[164:167], v[224:227]\n" \
"s_waitcnt vmcnt(4)\n" \
"s_mov_b64 exec, 3\n" \
"buffer_store_dword v39, v41, s[56:59], s69 offen sc1\n" \
"s_mov_b64 exec, -1\n" \
"v_mfma_f32_16x16x32_bf16 v[224:227], a[24:27], a[168:171], v[224:227]\n" \
"s_add_u32 s69, s63, 0x0\n" \
"v_mfma_f32_16x16x32_bf16 v[224:227], a[28:31], a[172:175], v[224:227]\n" \
"v_mfma_f32_16x16x32_bf16 v[228:231], a[28:31], a[188:191], v[228:231]\n" \
"ds_read_b64_tr_b16 a[28:29], v34 offset:18624\n" \
"ds_read_b64_tr_b16 a[30:31], v34 offset:23232\n" \
"v_mfma_f32_16x16x32_bf16 v[228:231], a[24:27], a[184:187], v[228:231]\n" \
"ds_read_b64_tr_b16 a[24:25], v34 offset:9408\n" \
"ds_read_b64_tr_b16 a[26:27], v34 offset:14016\n" \
"v_mfma_f32_16x16x32_bf16 v[228:231], a[20:23], a[180:183], v[228:231]\n" \
"ds_read_b64_tr_b16 a[20:21], v34 offset:192\n" \
"ds_read_b64_tr_b16 a[22:23], v34 offset:4800\n" \
"v_mfma_f32_16x16x32_bf16 v[228:231], a[16:19], a[176:179], v[228:231]\n" \
"ds_read_b64_tr_b16 a[16:17], v33 offset:46272\n" \
"ds_read_b64_tr_b16 a[18:19], v33 offset:50880\n" \
"v_mfma_f32_16x16x32_bf16 v[152:155], a[192:195], a[96:99], v[152:155]\n" \
"v_mfma_f32_16x16x32_bf16 v[152:155], a[196:199], a[100:103], v[152:155]\n" \
"v_mfma_f32_16x16x32_bf16 v[152:155], a[200:203], a[104:107], v[152:155]\n" \
"s_waitcnt lgkmcnt(8)\n" \
"v_mfma_f32_16x16x32_bf16 v[152:155], a[204:207], a[108:111], v[152:155]\n" \
"v_mfma_f32_16x16x32_bf16 v[156:159], a[204:207], a[124:127], v[156:159]\n" \
"v_mfma_f32_16x16x32_bf16 v[156:159], a[200:203], a[120:123], v[156:159]\n" \
"v_mfma_f32_16x16x32_bf16 v[156:159], a[196:199], a[116:119], v[156:159]\n" \
"v_mfma_f32_16x16x32_bf16 v[156:159], a[192:195], a[112:115], v[156:159]\n" \
"v_mfma_f32_16x16x32_bf16 v[224:227], a[192:195], a[220:223], v[224:227]\n" \
"v_mfma_f32_16x16x32_bf16 v[224:227], a[196:199], a[224:227], v[224:227]\n" \
"v_mfma_f32_16x16x32_bf16 v[224:227], a[200:203], a[228:231], v[224:227]\n" \
"v_mfma_f32_16x16x32_bf16 v[224:227], a[204:207], a[232:235], v[224:227]\n" \
"v_cvt_pk_bf16_f32 v152, v152, v153\n" \
"v_cvt_pk_bf16_f32 v153, v154, v155\n" \
"v_mfma_f32_16x16x32_bf16 v[228:231], a[204:207], a[248:251], v[228:231]\n" \
"ds_read_b64_tr_b16 a[204:205], v10 offset:46080\n" \
"v_cvt_pk_bf16_f32 v154, v156, v157\n" \
"v_cvt_pk_bf16_f32 v155, v158, v159\n" \
"v_mfma_f32_16x16x32_bf16 v[228:231], a[200:203], a[244:247], v[228:231]\n" \
"buffer_store_dwordx4 v[152:155], v20, s[56:59], s72 offen offset:0 sc1\n" \
"v_cvt_pk_bf16_f32 v224, v224, v225\n" \
"v_cvt_pk_bf16_f32 v225, v226, v227\n" \
"v_mfma_f32_16x16x32_bf16 v[228:231], a[196:199], a[240:243], v[228:231]\n" \
"ds_read_b64_tr_b16 a[196:197], v10 offset:27648\n" \
"ds_read_b64_tr_b16 a[198:199], v10 offset:32256\n" \
"v_mfma_f32_16x16x32_bf16 v[228:231], a[192:195], a[236:239], v[228:231]\n" \
"ds_read_b64_tr_b16 a[192:193], v33 offset:192\n" \
"ds_read_b64_tr_b16 a[194:195], v33 offset:4800\n" \
"v_mfma_f32_16x16x32_bf16 v[232:235], a[208:211], a[32:35], v[232:235]\n" \
"ds_read_b64_tr_b16 a[200:201], v10 offset:36864\n" \
"ds_read_b64_tr_b16 a[202:203], v10 offset:41472\n" \
"v_mfma_f32_16x16x32_bf16 v[232:235], a[212:215], a[36:39], v[232:235]\n" \
"ds_read_b64_tr_b16 a[206:207], v10 offset:50688\n" \
"v_mfma_f32_16x16x32_bf16 v[232:235], a[216:219], a[40:43], v[232:235]\n" \
"v_cvt_pk_bf16_f32 v226, v228, v229\n" \
"v_cvt_pk_bf16_f32 v227, v230, v231\n" \
"v_mfma_f32_16x16x32_bf16 v[232:235], a[0:3], a[44:47], v[232:235]\n" \
"buffer_store_dwordx4 v[224:227], v20, s[56:59], s72 offen offset:1024 sc1\n" \
"s_add_u32 s72, s72, 0x1000\n" \
"v_mfma_f32_16x16x32_bf16 v[236:239], a[0:3], a[60:63], v[236:239]\n" \
"ds_read_b64_tr_b16 a[0:1], v33 offset:36864\n" \
"ds_read_b64_tr_b16 a[2:3], v33 offset:41472\n" \
"v_mfma_f32_16x16x32_bf16 v[236:239], a[216:219], a[56:59], v[236:239]\n" \
"ds_read_b64_tr_b16 a[216:217], v33 offset:27648\n" \
"ds_read_b64_tr_b16 a[218:219], v33 offset:32256\n" \
"v_mfma_f32_16x16x32_bf16 v[236:239], a[212:215], a[52:55], v[236:239]\n" \
"ds_read_b64_tr_b16 a[212:213], v33 offset:18432\n" \
"ds_read_b64_tr_b16 a[214:215], v33 offset:23040\n" \
"v_mfma_f32_16x16x32_bf16 v[236:239], a[208:211], a[48:51], v[236:239]\n" \
"ds_read_b64_tr_b16 a[208:209], v10 offset:55296\n" \
"ds_read_b64_tr_b16 a[210:211], v10 offset:59904\n" \
"v_mfma_f32_16x16x32_bf16 v[160:163], a[4:7], a[128:131], v[160:163]\n" \
"v_mfma_f32_16x16x32_bf16 v[160:163], a[8:11], a[132:135], v[160:163]\n" \
"v_mfma_f32_16x16x32_bf16 v[160:163], a[12:15], a[136:139], v[160:163]\n" \
"s_waitcnt lgkmcnt(15)\n" \
"v_mfma_f32_16x16x32_bf16 v[160:163], a[16:19], a[140:143], v[160:163]\n" \
"v_mfma_f32_16x16x32_bf16 v[164:167], a[16:19], a[156:159], v[164:167]\n" \
"v_mfma_f32_16x16x32_bf16 v[164:167], a[12:15], a[152:155], v[164:167]\n" \
"v_mfma_f32_16x16x32_bf16 v[164:167], a[8:11], a[148:151], v[164:167]\n" \
"v_mfma_f32_16x16x32_bf16 v[164:167], a[4:7], a[144:147], v[164:167]\n" \
"s_waitcnt vmcnt(5)\n" \
"s_mov_b64 exec, 1\n" \
"buffer_store_dword v39, off, s[56:59], s70 offset:16 sc1\n" \
"s_mov_b64 exec, -1\n" \
"v_mfma_f32_16x16x32_bf16 v[232:235], a[4:7], a[160:163], v[232:235]\n" \
"s_add_u32 s70, s63, 0x800\n" \
"v_mfma_f32_16x16x32_bf16 v[232:235], a[8:11], a[164:167], v[232:235]\n" \
"v_mfma_f32_16x16x32_bf16 v[232:235], a[12:15], a[168:171], v[232:235]\n" \
"v_mfma_f32_16x16x32_bf16 v[232:235], a[16:19], a[172:175], v[232:235]\n" \
"v_mfma_f32_16x16x32_bf16 v[236:239], a[16:19], a[188:191], v[236:239]\n" \
"ds_read_b64_tr_b16 a[16:17], v34 offset:18432\n" \
"ds_read_b64_tr_b16 a[18:19], v34 offset:23040\n" \
"v_mfma_f32_16x16x32_bf16 v[236:239], a[12:15], a[184:187], v[236:239]\n" \
"ds_read_b64_tr_b16 a[12:13], v34 offset:9216\n" \
"ds_read_b64_tr_b16 a[14:15], v34 offset:13824\n" \
"v_mfma_f32_16x16x32_bf16 v[236:239], a[8:11], a[180:183], v[236:239]\n" \
"ds_read_b64_tr_b16 a[8:9], v34 offset:0\n" \
"ds_read_b64_tr_b16 a[10:11], v34 offset:4608\n" \
"v_mfma_f32_16x16x32_bf16 v[236:239], a[4:7], a[176:179], v[236:239]\n" \
"ds_read_b64_tr_b16 a[4:5], v33 offset:46080\n" \
"ds_read_b64_tr_b16 a[6:7], v33 offset:50688\n" \
"v_mfma_f32_16x16x32_bf16 v[160:163], a[20:23], a[96:99], v[160:163]\n" \
"v_mfma_f32_16x16x32_bf16 v[160:163], a[24:27], a[100:103], v[160:163]\n" \
"v_mfma_f32_16x16x32_bf16 v[160:163], a[28:31], a[104:107], v[160:163]\n" \
"s_waitcnt lgkmcnt(8)\n" \
"v_mfma_f32_16x16x32_bf16 v[160:163], a[192:195], a[108:111], v[160:163]\n" \
"v_mfma_f32_16x16x32_bf16 v[164:167], a[192:195], a[124:127], v[164:167]\n" \
"v_mfma_f32_16x16x32_bf16 v[164:167], a[28:31], a[120:123], v[164:167]\n" \
"v_mfma_f32_16x16x32_bf16 v[164:167], a[24:27], a[116:119], v[164:167]\n" \
"v_mfma_f32_16x16x32_bf16 v[164:167], a[20:23], a[112:115], v[164:167]\n" \
"v_mfma_f32_16x16x32_bf16 v[232:235], a[20:23], a[220:223], v[232:235]\n" \
"v_mfma_f32_16x16x32_bf16 v[232:235], a[24:27], a[224:227], v[232:235]\n" \
"v_mfma_f32_16x16x32_bf16 v[232:235], a[28:31], a[228:231], v[232:235]\n" \
"v_mfma_f32_16x16x32_bf16 v[232:235], a[192:195], a[232:235], v[232:235]\n" \
"v_cvt_pk_bf16_f32 v160, v160, v161\n" \
"v_cvt_pk_bf16_f32 v161, v162, v163\n" \
"v_mfma_f32_16x16x32_bf16 v[236:239], a[192:195], a[248:251], v[236:239]\n" \
"ds_read_b64_tr_b16 a[192:193], v10 offset:46112\n" \
"v_cvt_pk_bf16_f32 v162, v164, v165\n" \
"v_cvt_pk_bf16_f32 v163, v166, v167\n" \
"v_mfma_f32_16x16x32_bf16 v[236:239], a[28:31], a[244:247], v[236:239]\n" \
"buffer_store_dwordx4 v[160:163], v20, s[56:59], s73 offen offset:0 sc1\n" \
"v_cvt_pk_bf16_f32 v232, v232, v233\n" \
"v_cvt_pk_bf16_f32 v233, v234, v235\n" \
"v_mfma_f32_16x16x32_bf16 v[236:239], a[24:27], a[240:243], v[236:239]\n" \
"ds_read_b64_tr_b16 a[24:25], v10 offset:27680\n" \
"ds_read_b64_tr_b16 a[26:27], v10 offset:32288\n" \
"v_mfma_f32_16x16x32_bf16 v[236:239], a[20:23], a[236:239], v[236:239]\n" \
"ds_read_b64_tr_b16 a[20:21], v33 offset:0\n" \
"ds_read_b64_tr_b16 a[22:23], v33 offset:4608\n" \
"v_mfma_f32_16x16x32_bf16 v[184:187], a[196:199], a[32:35], v[184:187]\n" \
"ds_read_b64_tr_b16 a[28:29], v10 offset:36896\n" \
"ds_read_b64_tr_b16 a[30:31], v10 offset:41504\n" \
"v_mfma_f32_16x16x32_bf16 v[184:187], a[200:203], a[36:39], v[184:187]\n" \
"ds_read_b64_tr_b16 a[194:195], v10 offset:50720\n" \
"v_mfma_f32_16x16x32_bf16 v[184:187], a[204:207], a[40:43], v[184:187]\n" \
"v_cvt_pk_bf16_f32 v234, v236, v237\n" \
"v_cvt_pk_bf16_f32 v235, v238, v239\n" \
"v_mfma_f32_16x16x32_bf16 v[184:187], a[208:211], a[44:47], v[184:187]\n" \
"buffer_store_dwordx4 v[232:235], v20, s[56:59], s73 offen offset:1024 sc1\n" \
"s_add_u32 s73, s73, 0x800\n" \
"v_mfma_f32_16x16x32_bf16 v[188:191], a[208:211], a[60:63], v[188:191]\n" \
"ds_read_b64_tr_b16 a[208:209], v33 offset:36896\n" \
"ds_read_b64_tr_b16 a[210:211], v33 offset:41504\n" \
"v_mfma_f32_16x16x32_bf16 v[188:191], a[204:207], a[56:59], v[188:191]\n" \
"ds_read_b64_tr_b16 a[204:205], v33 offset:27680\n" \
"ds_read_b64_tr_b16 a[206:207], v33 offset:32288\n" \
"v_mfma_f32_16x16x32_bf16 v[188:191], a[200:203], a[52:55], v[188:191]\n" \
"ds_read_b64_tr_b16 a[200:201], v33 offset:18464\n" \
"ds_read_b64_tr_b16 a[202:203], v33 offset:23072\n" \
"v_mfma_f32_16x16x32_bf16 v[188:191], a[196:199], a[48:51], v[188:191]\n" \
"ds_read_b64_tr_b16 a[196:197], v10 offset:55328\n" \
"ds_read_b64_tr_b16 a[198:199], v10 offset:59936\n" \
"v_mfma_f32_16x16x32_bf16 v[184:187], a[212:215], a[160:163], v[184:187]\n" \
"v_mfma_f32_16x16x32_bf16 v[184:187], a[216:219], a[164:167], v[184:187]\n" \
"v_mfma_f32_16x16x32_bf16 v[184:187], a[0:3], a[168:171], v[184:187]\n" \
"s_waitcnt lgkmcnt(15)\n" \
"v_mfma_f32_16x16x32_bf16 v[184:187], a[4:7], a[172:175], v[184:187]\n" \
"v_mfma_f32_16x16x32_bf16 v[188:191], a[4:7], a[188:191], v[188:191]\n" \
"ds_read_b64_tr_b16 a[4:5], v34 offset:18464\n" \
"ds_read_b64_tr_b16 a[6:7], v34 offset:23072\n" \
"v_mfma_f32_16x16x32_bf16 v[188:191], a[0:3], a[184:187], v[188:191]\n" \
"ds_read_b64_tr_b16 a[0:1], v34 offset:9248\n" \
"ds_read_b64_tr_b16 a[2:3], v34 offset:13856\n" \
"s_waitcnt vmcnt(6)\n" \
"s_mov_b64 exec, 1\n" \
"buffer_store_dword v39, off, s[56:59], s71 offset:20 sc1\n" \
"s_mov_b64 exec, -1\n" \
"v_mfma_f32_16x16x32_bf16 v[188:191], a[216:219], a[180:183], v[188:191]\n" \
"ds_read_b64_tr_b16 a[216:217], v34 offset:32\n" \
"ds_read_b64_tr_b16 a[218:219], v34 offset:4640\n" \
"v_mfma_f32_16x16x32_bf16 v[188:191], a[212:215], a[176:179], v[188:191]\n" \
"ds_read_b64_tr_b16 a[212:213], v33 offset:46112\n" \
"ds_read_b64_tr_b16 a[214:215], v33 offset:50720\n" \
"v_mfma_f32_16x16x32_bf16 v[184:187], a[8:11], a[220:223], v[184:187]\n" \
"s_add_u32 s71, s63, 0x1000\n" \
"v_mfma_f32_16x16x32_bf16 v[184:187], a[12:15], a[224:227], v[184:187]\n" \
"v_mfma_f32_16x16x32_bf16 v[184:187], a[16:19], a[228:231], v[184:187]\n" \
"s_waitcnt lgkmcnt(12)\n" \
"v_mfma_f32_16x16x32_bf16 v[184:187], a[20:23], a[232:235], v[184:187]\n" \
"v_mfma_f32_16x16x32_bf16 v[188:191], a[20:23], a[248:251], v[188:191]\n" \
"ds_read_b64_tr_b16 a[20:21], v10 offset:46144\n" \
"ds_read_b64_tr_b16 a[22:23], v10 offset:50752\n" \
"v_mfma_f32_16x16x32_bf16 v[188:191], a[16:19], a[244:247], v[188:191]\n" \
"ds_read_b64_tr_b16 a[16:17], v10 offset:36928\n" \
"ds_read_b64_tr_b16 a[18:19], v10 offset:41536\n" \
"v_mfma_f32_16x16x32_bf16 v[188:191], a[12:15], a[240:243], v[188:191]\n" \
"ds_read_b64_tr_b16 a[12:13], v10 offset:27712\n" \
"v_cvt_pk_bf16_f32 v184, v184, v185\n" \
"v_cvt_pk_bf16_f32 v185, v186, v187\n" \
"v_mfma_f32_16x16x32_bf16 v[188:191], a[8:11], a[236:239], v[188:191]\n" \
"ds_read_b64_tr_b16 a[8:9], v33 offset:32\n" \
"ds_read_b64_tr_b16 a[10:11], v33 offset:4640\n" \
"v_mfma_f32_16x16x32_bf16 v[192:195], a[24:27], a[32:35], v[192:195]\n" \
"ds_read_b64_tr_b16 a[14:15], v10 offset:32320\n" \
"v_mfma_f32_16x16x32_bf16 v[192:195], a[28:31], a[36:39], v[192:195]\n" \
"v_mfma_f32_16x16x32_bf16 v[192:195], a[192:195], a[40:43], v[192:195]\n" \
"s_waitcnt lgkmcnt(12)\n" \
"v_mfma_f32_16x16x32_bf16 v[192:195], a[196:199], a[44:47], v[192:195]\n" \
"v_cvt_pk_bf16_f32 v186, v188, v189\n" \
"v_cvt_pk_bf16_f32 v187, v190, v191\n" \
"v_mfma_f32_16x16x32_bf16 v[196:199], a[196:199], a[60:63], v[196:199]\n" \
"buffer_store_dwordx4 v[184:187], v20, s[56:59], s92 offen offset:1024 sc1\n" \
"s_add_u32 s92, s92, 0x3800\n" \
"v_mfma_f32_16x16x32_bf16 v[196:199], a[192:195], a[56:59], v[196:199]\n" \
"ds_read_b64_tr_b16 a[192:193], v33 offset:27712\n" \
"ds_read_b64_tr_b16 a[194:195], v33 offset:32320\n" \
"v_mfma_f32_16x16x32_bf16 v[196:199], a[28:31], a[52:55], v[196:199]\n" \
"ds_read_b64_tr_b16 a[28:29], v33 offset:18496\n" \
"ds_read_b64_tr_b16 a[30:31], v33 offset:23104\n" \
"v_mfma_f32_16x16x32_bf16 v[196:199], a[24:27], a[48:51], v[196:199]\n" \
"ds_read_b64_tr_b16 a[24:25], v10 offset:55360\n" \
"ds_read_b64_tr_b16 a[26:27], v10 offset:59968\n" \
"v_mfma_f32_16x16x32_bf16 v[192:195], a[200:203], a[160:163], v[192:195]\n" \
"ds_read_b64_tr_b16 a[196:197], v33 offset:36928\n" \
"ds_read_b64_tr_b16 a[198:199], v33 offset:41536\n" \
"L_pf22:\n" \
"s_cbranch_execz L_retB\n" \
"s_nop 0\n" \
"v_mfma_f32_16x16x32_bf16 v[192:195], a[204:207], a[164:167], v[192:195]\n" \
"v_mfma_f32_16x16x32_bf16 v[192:195], a[208:211], a[168:171], v[192:195]\n" \
"s_waitcnt lgkmcnt(14)\n" \
"v_mfma_f32_16x16x32_bf16 v[192:195], a[212:215], a[172:175], v[192:195]\n" \
"v_mfma_f32_16x16x32_bf16 v[196:199], a[212:215], a[188:191], v[196:199]\n" \
"ds_read_b64_tr_b16 a[212:213], v34 offset:18496\n" \
"ds_read_b64_tr_b16 a[214:215], v34 offset:23104\n" \
"v_mfma_f32_16x16x32_bf16 v[196:199], a[208:211], a[184:187], v[196:199]\n" \
"ds_read_b64_tr_b16 a[208:209], v34 offset:9280\n" \
"ds_read_b64_tr_b16 a[210:211], v34 offset:13888\n" \
"v_mfma_f32_16x16x32_bf16 v[196:199], a[204:207], a[180:183], v[196:199]\n" \
"ds_read_b64_tr_b16 a[204:205], v34 offset:64\n" \
"ds_read_b64_tr_b16 a[206:207], v34 offset:4672\n" \
"v_mfma_f32_16x16x32_bf16 v[196:199], a[200:203], a[176:179], v[196:199]\n" \
"ds_read_b64_tr_b16 a[200:201], v33 offset:46144\n" \
"ds_read_b64_tr_b16 a[202:203], v33 offset:50752\n" \
"v_mfma_f32_16x16x32_bf16 v[192:195], a[216:219], a[220:223], v[192:195]\n" \
"v_mfma_f32_16x16x32_bf16 v[192:195], a[0:3], a[224:227], v[192:195]\n" \
"v_mfma_f32_16x16x32_bf16 v[192:195], a[4:7], a[228:231], v[192:195]\n" \
"s_waitcnt lgkmcnt(15)\n" \
"v_mfma_f32_16x16x32_bf16 v[192:195], a[8:11], a[232:235], v[192:195]\n" \
"v_mfma_f32_16x16x32_bf16 v[196:199], a[8:11], a[248:251], v[196:199]\n" \
"ds_read_b64_tr_b16 a[8:9], v10 offset:46304\n" \
"ds_read_b64_tr_b16 a[10:11], v10 offset:50912\n" \
"v_mfma_f32_16x16x32_bf16 v[196:199], a[4:7], a[244:247], v[196:199]\n" \
"ds_read_b64_tr_b16 a[4:5], v10 offset:37088\n" \
"ds_read_b64_tr_b16 a[6:7], v10 offset:41696\n" \
"v_mfma_f32_16x16x32_bf16 v[196:199], a[0:3], a[240:243], v[196:199]\n" \
"ds_read_b64_tr_b16 a[0:1], v10 offset:27872\n" \
"v_cvt_pk_bf16_f32 v192, v192, v193\n" \
"v_cvt_pk_bf16_f32 v193, v194, v195\n" \
"v_mfma_f32_16x16x32_bf16 v[196:199], a[216:219], a[236:239], v[196:199]\n" \
"ds_read_b64_tr_b16 a[216:217], v33 offset:64\n" \
"ds_read_b64_tr_b16 a[218:219], v33 offset:4672\n" \
"v_mfma_f32_16x16x32_bf16 v[200:203], a[12:15], a[32:35], v[200:203]\n" \
"ds_read_b64_tr_b16 a[2:3], v10 offset:32480\n" \
"v_mfma_f32_16x16x32_bf16 v[200:203], a[16:19], a[36:39], v[200:203]\n" \
"v_mfma_f32_16x16x32_bf16 v[200:203], a[20:23], a[40:43], v[200:203]\n" \
"s_waitcnt lgkmcnt(14)\n" \
"v_mfma_f32_16x16x32_bf16 v[200:203], a[24:27], a[44:47], v[200:203]\n" \
"v_cvt_pk_bf16_f32 v194, v196, v197\n" \
"v_cvt_pk_bf16_f32 v195, v198, v199\n" \
"v_mfma_f32_16x16x32_bf16 v[204:207], a[24:27], a[60:63], v[204:207]\n" \
"buffer_store_dwordx4 v[192:195], v20, s[56:59], s93 offen offset:1024 sc1\n" \
"s_add_u32 s93, s93, 0x3000\n" \
"v_mfma_f32_16x16x32_bf16 v[204:207], a[20:23], a[56:59], v[204:207]\n" \
"ds_read_b64_tr_b16 a[20:21], v33 offset:27872\n" \
"ds_read_b64_tr_b16 a[22:23], v33 offset:32480\n" \
"s_waitcnt vmcnt(6)\n" \
"s_mov_b64 exec, 1\n" \
"buffer_store_dword v39, off, s[56:59], s72 offset:24 sc1\n" \
"s_mov_b64 exec, -1\n" \
"v_mfma_f32_16x16x32_bf16 v[204:207], a[16:19], a[52:55], v[204:207]\n" \
"ds_read_b64_tr_b16 a[16:17], v33 offset:18656\n" \
"ds_read_b64_tr_b16 a[18:19], v33 offset:23264\n" \
"v_mfma_f32_16x16x32_bf16 v[204:207], a[12:15], a[48:51], v[204:207]\n" \
"ds_read_b64_tr_b16 a[12:13], v10 offset:55520\n" \
"ds_read_b64_tr_b16 a[14:15], v10 offset:60128\n" \
"v_mfma_f32_16x16x32_bf16 v[200:203], a[28:31], a[160:163], v[200:203]\n" \
"ds_read_b64_tr_b16 a[24:25], v33 offset:37088\n" \
"ds_read_b64_tr_b16 a[26:27], v33 offset:41696\n" \
"v_mfma_f32_16x16x32_bf16 v[200:203], a[192:195], a[164:167], v[200:203]\n" \
"v_readlane_b32 s72, v11, 50\n" \
"v_mfma_f32_16x16x32_bf16 v[200:203], a[196:199], a[168:171], v[200:203]\n" \
"s_waitcnt lgkmcnt(12)\n" \
"v_mfma_f32_16x16x32_bf16 v[200:203], a[200:203], a[172:175], v[200:203]\n" \
"v_mfma_f32_16x16x32_bf16 v[204:207], a[200:203], a[188:191], v[204:207]\n" \
"ds_read_b64_tr_b16 a[200:201], v34 offset:18656\n" \
"ds_read_b64_tr_b16 a[202:203], v34 offset:23264\n" \
"v_mfma_f32_16x16x32_bf16 v[204:207], a[196:199], a[184:187], v[204:207]\n" \
"ds_read_b64_tr_b16 a[196:197], v34 offset:9440\n" \
"ds_read_b64_tr_b16 a[198:199], v34 offset:14048\n" \
"v_mfma_f32_16x16x32_bf16 v[204:207], a[192:195], a[180:183], v[204:207]\n" \
"ds_read_b64_tr_b16 a[192:193], v34 offset:224\n" \
"ds_read_b64_tr_b16 a[194:195], v34 offset:4832\n" \
"v_mfma_f32_16x16x32_bf16 v[204:207], a[28:31], a[176:179], v[204:207]\n" \
"ds_read_b64_tr_b16 a[28:29], v33 offset:46304\n" \
"ds_read_b64_tr_b16 a[30:31], v33 offset:50912\n" \
"v_mfma_f32_16x16x32_bf16 v[200:203], a[204:207], a[220:223], v[200:203]\n" \
"v_mfma_f32_16x16x32_bf16 v[200:203], a[208:211], a[224:227], v[200:203]\n" \
"v_mfma_f32_16x16x32_bf16 v[200:203], a[212:215], a[228:231], v[200:203]\n" \
"s_waitcnt lgkmcnt(14)\n" \
"v_mfma_f32_16x16x32_bf16 v[200:203], a[216:219], a[232:235], v[200:203]\n" \
"v_mfma_f32_16x16x32_bf16 v[204:207], a[216:219], a[248:251], v[204:207]\n" \
"ds_read_b64_tr_b16 a[216:217], v36 offset:32\n" \
"ds_read_b64_tr_b16 a[218:219], v36 offset:1056\n" \
"v_mfma_f32_16x16x32_bf16 v[204:207], a[212:215], a[244:247], v[204:207]\n" \
"ds_read_b64_tr_b16 a[212:213], v36 offset:2048\n" \
"ds_read_b64_tr_b16 a[214:215], v36 offset:3072\n" \
"v_mfma_f32_16x16x32_bf16 v[204:207], a[208:211], a[240:243], v[204:207]\n" \
"ds_read_b64_tr_b16 a[208:209], v36 offset:0\n" \
"v_cvt_pk_bf16_f32 v200, v200, v201\n" \
"v_cvt_pk_bf16_f32 v201, v202, v203\n" \
"v_mfma_f32_16x16x32_bf16 v[204:207], a[204:207], a[236:239], v[204:207]\n" \
"ds_read_b64_tr_b16 a[204:205], v33 offset:224\n" \
"ds_read_b64_tr_b16 a[206:207], v33 offset:4832\n" \
"v_mfma_f32_16x16x32_bf16 v[240:243], a[0:3], a[32:35], v[240:243]\n" \
"ds_read_b64_tr_b16 a[210:211], v36 offset:1024\n" \
"v_mfma_f32_16x16x32_bf16 v[240:243], a[4:7], a[36:39], v[240:243]\n" \
"v_mfma_f32_16x16x32_bf16 v[240:243], a[8:11], a[40:43], v[240:243]\n" \
"s_waitcnt lgkmcnt(14)\n" \
"v_mfma_f32_16x16x32_bf16 v[240:243], a[12:15], a[44:47], v[240:243]\n" \
"v_cvt_pk_bf16_f32 v202, v204, v205\n" \
"v_cvt_pk_bf16_f32 v203, v206, v207\n" \
"v_mfma_f32_16x16x32_bf16 v[244:247], a[12:15], a[60:63], v[244:247]\n" \
"buffer_store_dwordx4 v[200:203], v20, s[56:59], s94 offen offset:1024 sc1\n" \
"s_add_u32 s94, s94, 0x2800\n" \
"v_mfma_f32_16x16x32_bf16 v[244:247], a[8:11], a[56:59], v[244:247]\n" \
"v_mfma_f32_16x16x32_bf16 v[244:247], a[4:7], a[52:55], v[244:247]\n" \
"v_mfma_f32_16x16x32_bf16 v[244:247], a[0:3], a[48:51], v[244:247]\n" \
"ds_read_b64_tr_b16 a[0:1], v36 offset:2080\n" \
"ds_read_b64_tr_b16 a[2:3], v36 offset:3104\n" \
"v_mfma_f32_16x16x32_bf16 v[168:171], a[16:19], a[128:131], v[168:171]\n" \
"v_mfma_f32_16x16x32_bf16 v[168:171], a[20:23], a[132:135], v[168:171]\n" \
"v_mfma_f32_16x16x32_bf16 v[168:171], a[24:27], a[136:139], v[168:171]\n" \
"s_waitcnt lgkmcnt(8)\n" \
"v_mfma_f32_16x16x32_bf16 v[168:171], a[28:31], a[140:143], v[168:171]\n" \
"v_mfma_f32_16x16x32_bf16 v[172:175], a[28:31], a[156:159], v[172:175]\n" \
"v_mfma_f32_16x16x32_bf16 v[172:175], a[24:27], a[152:155], v[172:175]\n" \
"v_mfma_f32_16x16x32_bf16 v[172:175], a[20:23], a[148:151], v[172:175]\n" \
"v_mfma_f32_16x16x32_bf16 v[172:175], a[16:19], a[144:147], v[172:175]\n" \
"v_mfma_f32_16x16x32_bf16 v[240:243], a[16:19], a[160:163], v[240:243]\n" \
"v_mfma_f32_16x16x32_bf16 v[240:243], a[20:23], a[164:167], v[240:243]\n" \
"v_mfma_f32_16x16x32_bf16 v[240:243], a[24:27], a[168:171], v[240:243]\n" \
"v_mfma_f32_16x16x32_bf16 v[240:243], a[28:31], a[172:175], v[240:243]\n" \
"v_mfma_f32_16x16x32_bf16 v[244:247], a[28:31], a[188:191], v[244:247]\n" \
"v_mfma_f32_16x16x32_bf16 v[244:247], a[24:27], a[184:187], v[244:247]\n" \
"s_waitcnt vmcnt(5)\n" \
"s_mov_b64 exec, 1\n" \
"buffer_store_dword v39, off, s[56:59], s73 offset:28 sc1\n" \
"s_mov_b64 exec, -1\n" \
"v_mfma_f32_16x16x32_bf16 v[244:247], a[20:23], a[180:183], v[244:247]\n" \
"v_readlane_b32 s73, v11, 41\n" \
"v_mfma_f32_16x16x32_bf16 v[244:247], a[16:19], a[176:179], v[244:247]\n" \
"v_mfma_f32_16x16x32_bf16 v[168:171], a[192:195], a[96:99], v[168:171]\n" \
"v_mfma_f32_16x16x32_bf16 v[168:171], a[196:199], a[100:103], v[168:171]\n" \
"v_mfma_f32_16x16x32_bf16 v[168:171], a[200:203], a[104:107], v[168:171]\n" \
"s_waitcnt lgkmcnt(0)\n" \
"v_mfma_f32_16x16x32_bf16 v[168:171], a[204:207], a[108:111], v[168:171]\n" \
"v_mfma_f32_16x16x32_bf16 v[172:175], a[204:207], a[124:127], v[172:175]\n" \
"v_mfma_f32_16x16x32_bf16 v[172:175], a[200:203], a[120:123], v[172:175]\n" \
"v_mfma_f32_16x16x32_bf16 v[172:175], a[196:199], a[116:119], v[172:175]\n" \
"v_mfma_f32_16x16x32_bf16 v[172:175], a[192:195], a[112:115], v[172:175]\n" \
"v_mfma_f32_16x16x32_bf16 v[240:243], a[192:195], a[220:223], v[240:243]\n" \
"v_mfma_f32_16x16x32_bf16 v[240:243], a[196:199], a[224:227], v[240:243]\n" \
"v_mfma_f32_16x16x32_bf16 v[240:243], a[200:203], a[228:231], v[240:243]\n" \
"v_mfma_f32_16x16x32_bf16 v[240:243], a[204:207], a[232:235], v[240:243]\n" \
"v_mfma_f32_16x16x32_bf16 v[244:247], a[204:207], a[248:251], v[244:247]\n" \
"v_mfma_f32_16x16x32_bf16 v[244:247], a[200:203], a[244:247], v[244:247]\n" \
"v_mfma_f32_16x16x32_bf16 v[244:247], a[196:199], a[240:243], v[244:247]\n" \
"v_mfma_f32_16x16x32_bf16 v[244:247], a[192:195], a[236:239], v[244:247]\n" \
"v_mfma_f32_16x16x32_bf16 v[168:171], a[208:211], v[64:67], v[168:171]\n" \
"v_mfma_f32_16x16x32_bf16 v[172:175], a[208:211], v[72:75], v[172:175]\n" \
"v_mfma_f32_16x16x32_bf16 v[240:243], a[208:211], v[80:83], v[240:243]\n" \
"v_mfma_f32_16x16x32_bf16 v[244:247], a[208:211], v[88:91], v[244:247]\n" \
"v_mfma_f32_16x16x32_bf16 v[168:171], a[212:215], v[68:71], v[168:171]\n" \
"v_mfma_f32_16x16x32_bf16 v[172:175], a[212:215], v[76:79], v[172:175]\n" \
"v_mfma_f32_16x16x32_bf16 v[240:243], a[212:215], v[84:87], v[240:243]\n" \
"v_mfma_f32_16x16x32_bf16 v[244:247], a[212:215], v[92:95], v[244:247]\n" \
"s_waitcnt vmcnt(4)\n" \
"s_mov_b64 exec, 1\n" \
"buffer_store_dword v39, off, s[56:59], s92 offset:32 sc1\n" \
"s_mov_b64 exec, -1\n" \
"v_mfma_f32_16x16x32_bf16 v[56:59], a[216:219], v[96:99], 0\n" \
"v_readlane_b32 s92, v11, 39\n" \
"v_mfma_f32_16x16x32_bf16 v[60:63], a[216:219], v[104:107], 0\n" \
"v_mfma_f32_16x16x32_bf16 v[56:59], a[0:3], v[100:103], v[56:59]\n" \
"v_mfma_f32_16x16x32_bf16 v[60:63], a[0:3], v[108:111], v[60:63]\n" \
"s_waitcnt vmcnt(4)\n" \
"s_mov_b64 exec, 1\n" \
"buffer_store_dword v39, off, s[56:59], s93 offset:36 sc1\n" \
"s_mov_b64 exec, -1\n" \
"v_readlane_b32 s93, v11, 40\n" \
"s_waitcnt vmcnt(3)\n" \
"s_mov_b64 exec, 1\n" \
"buffer_store_dword v39, off, s[56:59], s94 offset:40 sc1\n" \
"s_mov_b64 exec, -1\n" \
"buffer_load_dword v37, v40, s[56:59], s74 offen sc1\n" \
"s_waitcnt vmcnt(0)\n" \
"v_cmp_eq_u32_e64 s[98:99], s62, v37\n" \
"s_nop 0\n" \
"s_andn2_b32 s75, 0x1, s98\n" \
"s_cbranch_scc1 L_sx0_%=\n" \
"L_rx0_%=:\n" \
"buffer_load_dwordx4 v[116:119], v20, s[56:59], s95 offen offset:0 sc1\n" \
"s_andn2_b32 s75, 0x2, s98\n" \
"s_cbranch_scc1 L_sx1_%=\n" \
"L_rx1_%=:\n" \
"buffer_load_dwordx4 v[124:127], v20, s[56:59], s96 offen offset:0 sc1\n" \
"s_andn2_b32 s75, 0x4, s98\n" \
"s_cbranch_scc1 L_sx2_%=\n" \
"L_rx2_%=:\n" \
"buffer_load_dwordx4 v[132:135], v20, s[56:59], s97 offen offset:0 sc1\n" \
"s_andn2_b32 s75, s100, s98\n" \
"s_cbranch_scc1 L_sx3_%=\n" \
"L_rx3_%=:\n" \
"buffer_load_dwordx4 v[136:139], v20, s[56:59], s64 offen offset:0 sc1\n" \
"buffer_load_dwordx4 v[208:211], v20, s[56:59], s64 offen offset:2048 sc1\n" \
"s_add_u32 s64, s64, 0x1000\n" \
"buffer_load_dwordx4 v[144:147], v20, s[56:59], s64 offen offset:0 sc1\n" \
"buffer_load_dwordx4 v[216:219], v20, s[56:59], s64 offen offset:2048 sc1\n" \
"s_add_u32 s64, s64, 0x1000\n" \
"buffer_load_dwordx4 v[152:155], v20, s[56:59], s64 offen offset:0 sc1\n" \
"buffer_load_dwordx4 v[224:227], v20, s[56:59], s64 offen offset:2048 sc1\n" \
"s_add_u32 s64, s64, 0x1000\n" \
"buffer_load_dwordx4 v[176:179], v20, s[56:59], s64 offen offset:0 sc1\n" \
"buffer_load_dwordx4 v[248:251], v20, s[56:59], s64 offen offset:2048 sc1\n" \
"s_andn2_b32 s75, 0x10, s98\n" \
"s_cbranch_scc1 L_sx4_%=\n" \
"L_rx4_%=:\n" \
"buffer_load_dwordx4 v[140:143], v20, s[56:59], s65 offen offset:0 sc1\n" \
"buffer_load_dwordx4 v[212:215], v20, s[56:59], s65 offen offset:1024 sc1\n" \
"s_andn2_b32 s75, 0x20, s98\n" \
"s_cbranch_scc1 L_sx5_%=\n" \
"L_rx5_%=:\n" \
"buffer_load_dwordx4 v[148:151], v20, s[56:59], s66 offen offset:0 sc1\n" \
"buffer_load_dwordx4 v[220:223], v20, s[56:59], s66 offen offset:1024 sc1\n" \
"s_andn2_b32 s75, 0x40, s98\n" \
"s_cbranch_scc1 L_sx6_%=\n" \
"L_rx6_%=:\n" \
"buffer_load_dwordx4 v[156:159], v20, s[56:59], s67 offen offset:0 sc1\n" \
"buffer_load_dwordx4 v[228:231], v20, s[56:59], s67 offen offset:1024 sc1\n" \
"s_andn2_b32 s75, 0x80, s98\n" \
"s_cbranch_scc1 L_sx7_%=\n" \
"L_rx7_%=:\n" \
"buffer_load_dwordx4 v[164:167], v20, s[56:59], s68 offen offset:0 sc1\n" \
"buffer_load_dwordx4 v[236:239], v20, s[56:59], s68 offen offset:1024 sc1\n" \
"s_andn2_b32 s75, 0x100, s98\n" \
"s_cbranch_scc1 L_sx8_%=\n" \
"L_rx8_%=:\n" \
"buffer_load_dwordx4 v[188:191], v20, s[56:59], s69 offen offset:1024 sc1\n" \
"s_andn2_b32 s75, 0x200, s98\n" \
"s_cbranch_scc1 L_sx9_%=\n" \
"L_rx9_%=:\n" \
"buffer_load_dwordx4 v[196:199], v20, s[56:59], s70 offen offset:1024 sc1\n" \
"s_andn2_b32 s75, 0x400, s98\n" \
"s_cbranch_scc1 L_sx10_%=\n" \
"L_rx10_%=:\n" \
"buffer_load_dwordx4 v[204:207], v20, s[56:59], s71 offen offset:1024 sc1\n" \
"s_waitcnt vmcnt(21)\n" \
"v_mfma_f32_16x16x32_bf16 v[168:171], v[50:53], v[116:119], v[168:171]\n" \
"v_mfma_f32_16x16x32_bf16 v[172:175], v[48:51], v[116:119], v[172:175]\n" \
"s_waitcnt vmcnt(20)\n" \
"v_mfma_f32_16x16x32_bf16 v[168:171], v[50:53], v[124:127], v[168:171]\n" \
"v_mfma_f32_16x16x32_bf16 v[172:175], v[48:51], v[124:127], v[172:175]\n" \
"s_waitcnt vmcnt(19)\n" \
"v_mfma_f32_16x16x32_bf16 v[168:171], v[50:53], v[132:135], v[168:171]\n" \
"v_mfma_f32_16x16x32_bf16 v[172:175], v[48:51], v[132:135], v[172:175]\n" \
"s_waitcnt vmcnt(18)\n" \
"v_mfma_f32_16x16x32_bf16 v[56:59], v[50:53], v[136:139], v[56:59]\n" \
"v_mfma_f32_16x16x32_bf16 v[60:63], v[48:51], v[136:139], v[60:63]\n" \
"s_waitcnt vmcnt(17)\n" \
"v_mfma_f32_16x16x32_bf16 v[56:59], v[50:53], v[208:211], v[56:59]\n" \
"v_mfma_f32_16x16x32_bf16 v[60:63], v[48:51], v[208:211], v[60:63]\n" \
"s_waitcnt vmcnt(16)\n" \
"v_mfma_f32_16x16x32_bf16 v[56:59], v[50:53], v[144:147], v[56:59]\n" \
"v_mfma_f32_16x16x32_bf16 v[60:63], v[48:51], v[144:147], v[60:63]\n" \
"s_waitcnt vmcnt(15)\n" \
"v_mfma_f32_16x16x32_bf16 v[56:59], v[50:53], v[216:219], v[56:59]\n" \
"v_mfma_f32_16x16x32_bf16 v[60:63], v[48:51], v[216:219], v[60:63]\n" \
"s_waitcnt vmcnt(14)\n" \
"v_mfma_f32_16x16x32_bf16 v[56:59], v[50:53], v[152:155], v[56:59]\n" \
"v_mfma_f32_16x16x32_bf16 v[60:63], v[48:51], v[152:155], v[60:63]\n" \
"s_waitcnt vmcnt(13)\n" \
"v_mfma_f32_16x16x32_bf16 v[56:59], v[50:53], v[224:227], v[56:59]\n" \
"v_mfma_f32_16x16x32_bf16 v[60:63], v[48:51], v[224:227], v[60:63]\n" \
"s_waitcnt vmcnt(12)\n" \
"v_mfma_f32_16x16x32_bf16 v[56:59], v[50:53], v[176:179], v[56:59]\n" \
"v_mfma_f32_16x16x32_bf16 v[60:63], v[48:51], v[176:179], v[60:63]\n" \
"s_waitcnt vmcnt(11)\n" \
"v_mfma_f32_16x16x32_bf16 v[56:59], v[50:53], v[248:251], v[56:59]\n" \
"v_mfma_f32_16x16x32_bf16 v[60:63], v[48:51], v[248:251], v[60:63]\n" \
"s_waitcnt vmcnt(10)\n" \
"v_mfma_f32_16x16x32_bf16 v[168:171], v[50:53], v[140:143], v[168:171]\n" \
"v_mfma_f32_16x16x32_bf16 v[172:175], v[48:51], v[140:143], v[172:175]\n" \
"s_waitcnt vmcnt(9)\n" \
"v_mfma_f32_16x16x32_bf16 v[240:243], v[50:53], v[212:215], v[240:243]\n" \
"v_mfma_f32_16x16x32_bf16 v[244:247], v[48:51], v[212:215], v[244:247]\n" \
"s_waitcnt vmcnt(8)\n" \
"v_mfma_f32_16x16x32_bf16 v[168:171], v[50:53], v[148:151], v[168:171]\n" \
"v_cvt_pk_bf16_f32 v56, v56, v57\n" \
"v_cvt_pk_bf16_f32 v60, v60, v61\n" \
"v_mfma_f32_16x16x32_bf16 v[172:175], v[48:51], v[148:151], v[172:175]\n" \
"v_cvt_pk_bf16_f32 v57, v58, v59\n" \
"s_waitcnt vmcnt(7)\n" \
"v_mfma_f32_16x16x32_bf16 v[240:243], v[50:53], v[220:223], v[240:243]\n" \
"buffer_store_dwordx2 v[56:57], v42, s[88:91], s73 offen sc0 sc1\n" \
"v_mfma_f32_16x16x32_bf16 v[244:247], v[48:51], v[220:223], v[244:247]\n" \
"v_cvt_pk_bf16_f32 v61, v62, v63\n" \
"s_waitcnt vmcnt(7)\n" \
"v_mfma_f32_16x16x32_bf16 v[168:171], v[50:53], v[156:159], v[168:171]\n" \
"buffer_store_dwordx2 v[60:61], v43, s[88:91], s73 offen sc0 sc1\n" \
"v_mfma_f32_16x16x32_bf16 v[172:175], v[48:51], v[156:159], v[172:175]\n" \
"s_waitcnt vmcnt(7)\n" \
"v_mfma_f32_16x16x32_bf16 v[240:243], v[50:53], v[228:231], v[240:243]\n" \
"v_mfma_f32_16x16x32_bf16 v[244:247], v[48:51], v[228:231], v[244:247]\n" \
"s_waitcnt vmcnt(6)\n" \
"v_mfma_f32_16x16x32_bf16 v[168:171], v[50:53], v[164:167], v[168:171]\n" \
"v_mfma_f32_16x16x32_bf16 v[172:175], v[48:51], v[164:167], v[172:175]\n" \
"s_waitcnt vmcnt(5)\n" \
"v_mfma_f32_16x16x32_bf16 v[240:243], v[50:53], v[236:239], v[240:243]\n" \
"v_mfma_f32_16x16x32_bf16 v[244:247], v[48:51], v[236:239], v[244:247]\n" \
"s_waitcnt vmcnt(4)\n" \
"v_mfma_f32_16x16x32_bf16 v[240:243], v[50:53], v[188:191], v[240:243]\n" \
"v_mfma_f32_16x16x32_bf16 v[244:247], v[48:51], v[188:191], v[244:247]\n" \
"s_waitcnt vmcnt(3)\n" \
"v_mfma_f32_16x16x32_bf16 v[240:243], v[50:53], v[196:199], v[240:243]\n" \
"v_cvt_pk_bf16_f32 v168, v168, v169\n" \
"v_cvt_pk_bf16_f32 v172, v172, v173\n" \
"v_mfma_f32_16x16x32_bf16 v[244:247], v[48:51], v[196:199], v[244:247]\n" \
"v_cvt_pk_bf16_f32 v169, v170, v171\n" \
"s_waitcnt vmcnt(2)\n" \
"v_mfma_f32_16x16x32_bf16 v[240:243], v[50:53], v[204:207], v[240:243]\n" \
"v_cvt_pk_bf16_f32 v173, v174, v175\n" \
"v_mfma_f32_16x16x32_bf16 v[244:247], v[48:51], v[204:207], v[244:247]\n" \
"s_mov_b64 exec, 1\n" \
"buffer_store_dword v46, off, s[56:59], s72 offset:0 sc1\n" \
"s_mov_b64 exec, -1\n" \
"buffer_store_dwordx2 v[168:169], v42, s[88:91], s92 offen sc0 sc1\n" \
"buffer_store_dwordx2 v[172:173], v43, s[88:91], s92 offen sc0 sc1\n" \
"s_nop 0\n" \
"v_cvt_pk_bf16_f32 v240, v240, v241\n" \
"v_cvt_pk_bf16_f32 v241, v242, v243\n" \
"v_cvt_pk_bf16_f32 v244, v244, v245\n" \
"v_cvt_pk_bf16_f32 v245, v246, v247\n" \
"buffer_store_dwordx2 v[240:241], v42, s[88:91], s93 offen sc0 sc1\n" \
"buffer_store_dwordx2 v[244:245], v43, s[88:91], s93 offen sc0 sc1\n"
#define ATT_VCLOB "v21","v22","v23","v32","v33","v34","v35","v36","v37","v38","v39","v40","v41","v42","v43","v44","v45","v46","v47","v48","v49","v50","v51","v52","v53","v54","v55","v56","v57","v58","v59","v60","v61","v62","v63","v64","v65","v66","v67","v68","v69","v70","v71","v72","v73","v74","v75","v76","v77","v78","v79","v80","v81","v82","v83","v84","v85","v86","v87","v88","v89","v90","v91","v92","v93","v94","v95","v96","v97","v98","v99","v100","v101","v102","v103","v104","v105","v106","v107","v108","v109","v110","v111","v112","v113","v114","v115","v116","v117","v118","v119","v120","v121","v122","v123","v124","v125","v126","v127","v128","v129","v130","v131","v132","v133","v134","v135","v136","v137","v138","v139","v140","v141","v142","v143","v144","v145","v146","v147","v148","v149","v150","v151","v152","v153","v154","v155","v156","v157","v158","v159","v160","v161","v162","v163","v164","v165","v166","v167","v168","v169","v170","v171","v172","v173","v174","v175","v176","v177","v178","v179","v180","v181","v182","v183","v184","v185","v186","v187","v188","v189","v190","v191","v192","v193","v194","v195","v196","v197","v198","v199","v200","v201","v202","v203","v204","v205","v206","v207","v208","v209","v210","v211","v212","v213","v214","v215","v216","v217","v218","v219","v220","v221","v222","v223","v224","v225","v226","v227","v228","v229","v230","v231","v232","v233","v234","v235","v236","v237","v238","v239","v240","v241","v242","v243","v244","v245","v246","v247","v248","v249","v250","v251","v252","v253","v254","v255"
#define ATT_SCLOB "s63","s64","s65","s66","s67","s68","s69","s70","s71","s72","s73","s74","s75","s76","s77","s78","s79","s80","s81","s82","s83","s84","s85","s86","s87","s88","s89","s90","s91","s92","s93","s94","s95","s96","s97","s98","s99","s100","s101"
enum { T_fo, T_fa, T_fas, T_fb, T_fbs, T_f8, T_mk8, T_wol, T_woh, T_wbl, T_wbh, T_outl, T_outh, T_r1l, T_r1h, T_r1n, T_r1s, T_m0, T_m1, T_mw, T_m4, T_ml, T_w0, T_w1, T_w2, T_w3, T_A1, T_A2, T_A3, T_B1, T_B2, T_B3, T_Ao, T_Bo, T_Al, T_Bl, T_l8, T_xr, T_x8r, T_oA, T_oB, T_o8, T_x0, T_x1, T_x2, T_x3, T_x4, T_x5, T_x6, T_x8, T_ep, T_xf, T_kf };

__device__ __forceinline__ void out_consts(u32x4 (&vp)[3], const u16* wo, const u16* wob, const u16* out, const unsigned b, const unsigned w, const unsigned lane) {
    const unsigned sp = b & 7u, nt = (b >> 3) & 3u, j = b >> 5, head = 4u * sp + nt, OOR = 0x40000000u;
    unsigned vw[9];
#pragma unroll
    for (unsigned i = 0; i < 9u; ++i) {
        const unsigned q = 64u * i + lane, row = __umul24(q, 3641u) >> 16, c = q - __umul24(row, 18u);
        vw[i] = __umul24(row, DM * 2) + (c < 16u ? (c * 16u + sp * 32u + 32u) & 255u : c * 16u) - (i & 3u) * 1024u;
    }
    const unsigned row = 16u * w + (lane >> 2), r9 = __umul24(16u * (row >> 5) + (row & 12u) + 2u * ((row >> 4) & 1u) + ((row >> 1) & 1u), 18u) + 16u + (row & 1u);
    unsigned vwl = __umul24(r9, DM * 2) + ((lane & 2u) ? 256u : sp * 32u) + (lane & 1u) * 16u, vrd = __umul24(lane >> 2, 288u) + (lane & 3u) * 8u;
    asm volatile("" : "+v"(vw[0]), "+v"(vw[1]), "+v"(vw[2]), "+v"(vw[3]), "+v"(vw[4]), "+v"(vw[5]), "+v"(vw[6]), "+v"(vw[7]), "+v"(vw[8]), "+v"(vwl), "+v"(vrd));
    unsigned tab = 0;
#define TB(n, v) asm("v_writelane_b32 %0, %1, %2" : "+v"(tab) : "s"((unsigned)(v)), "n"((int)T_##n))
    const unsigned FJ = (unsigned)WS_F + (F_J + 640u * j) * 4u, mt = 2u * (w & 2u) + (w & 1u), w2 = w < 2u ? w : 2u, hw = head * 147456u;
    const unsigned FR = (unsigned)WS_F + F_R * 4u + (8u * j + sp) * 1024u;
    TB(fo, FJ + 4u * head); TB(fa, FR + (w | 2u) * 256u); TB(fas, FR + w * 256u + nt * 4u);
    TB(fb, FR + 128u + w * 256u); TB(fbs, FR + 128u + w * 256u + nt * 4u);
    TB(ep, b + w ? OOR : (unsigned)WS_F + F_EPOCH * 4u);
    const u16* pwo = wo + nt * 144u; const u16* pwb = wob + nt * 144u; const u16* pr1 = w < 3u ? pwo : pwb;
    TB(wol, PLO(pwo)); TB(woh, PHI(pwo) & 0xffffu); TB(wbl, PLO(pwb)); TB(wbh, PHI(pwb) & 0xffffu); TB(outl, PLO(out)); TB(outh, PHI(out) & 0xffffu); TB(r1l, PLO(pr1)); TB(r1h, PHI(pr1) & 0xffffu);
    TB(r1n, (w < 3u ? 72u * sp + 71u : 4095u) * 1152u + 288u); TB(r1s, w < 3u ? (72u * sp + 32u * w) * 1152u : hw + 110592u);
    TB(m0, 131072u + 9216u * w2); TB(w0, hw + 36864u * w2); TB(m1, w < 3u ? 9216u * w : 73728u); TB(mw, 9216u * w); TB(m4, w < 3u ? 131072u + 9216u * w : 64512u); TB(ml, 119808u + 1024u * w);
    const unsigned tA = (unsigned)WS_AF + (8u * j + mt) * (KS_TOT * 1024u), tB = (unsigned)WS_AF + (120u - 8u * j + mt) * (KS_TOT * 1024u);
#define HK(k) (4u * sp + ((nt + k) & 3u))
    TB(w1, HK(1u) * 147456u + w * 36864u); TB(w2, HK(2u) * 147456u + w * 36864u); TB(w3, HK(3u) * 147456u + w * 36864u);
    TB(A1, tA + 18432u + HK(1u) * 4096u); TB(A2, tA + 18432u + HK(2u) * 4096u); TB(A3, tA + 18432u + HK(3u) * 4096u);
    TB(B1, tB + 18432u + HK(1u) * 4096u); TB(B2, tB + 18432u + HK(2u) * 4096u); TB(B3, tB + 18432u + HK(3u) * 4096u);
    TB(Ao, tA + sp * 2048u); TB(Bo, tB + sp * 2048u); TB(Al, tA + 16384u); TB(Bl, tB + 16384u);
    const bool red = w == (sp >> 1), blk = sp & 1u;
    const unsigned xg = (unsigned)WS_X + (4u * j + nt) * 589824u + w * 16384u, oA = (128u * j + 16u * mt) * 1152u + nt * 288u, oB = (1920u - 128u * j + 16u * mt) * 1152u + nt * 288u;
    TB(l8, red ? (blk ? tB : tA) + 16384u : OOR); TB(xr, xg + sp * 65536u); TB(x8r, red ? xg + 524288u + (sp & 1u) * 1024u : OOR);
    TB(oA, oA + sp * 32u); TB(oB, oB + sp * 32u); TB(o8, red ? (blk ? oB : oA) + 256u : OOR); TB(x8, xg + 524288u + sp * 2048u);
    TB(f8, xg + w * 131072u + 14400u + sp * 4u); TB(mk8, red ? 0xff0000u : 0u); TB(xf, xg + sp * 65536u + 14336u);
    TB(kf, (unsigned)WS_F + (F_KV + head * 64u) * 4u);
#define XI(i) TB(x##i, xg + ((sp + 1u + i) & 7u) * 65536u + i * 2048u)
    XI(0); XI(1); XI(2); XI(3); XI(4); XI(5); XI(6);
#undef XI
#undef HK
#undef TB
    vp[0] = (u32x4){vw[0], vw[1], vw[2], vw[3]}; vp[1] = (u32x4){vw[4], vw[5], vw[6], vw[7]}; vp[2] = (u32x4){vw[8], vwl, vrd, tab};
}
__global__ __launch_bounds__(256, 1) __attribute__((aligned(4096))) void fused_kernel(const u16* __restrict__ x, const u16* __restrict__ ckv, const u16* __restrict__ wq, const u16* __restrict__ wuk,
                                                       const u16* __restrict__ wuv, const u16* __restrict__ wqkv, const u16* __restrict__ gw,
                                                       const u16* __restrict__ wo, const u16* __restrict__ wob, char* __restrict__ ws, u16* __restrict__ out) {
    using namespace att;
    extern __shared__ __attribute__((aligned(16))) char lds[];
    unsigned wid = rfl(threadIdx.x >> 6);
    code_touch((blockIdx.x >> 3) * 4u + wid, (unsigned)(size_t)lds + 151552u + wid * 512u);
    SBAR();
    asm volatile("" :: "s"(x), "s"(ckv), "s"(wq), "s"(wuk), "s"(wuv), "s"(wqkv), "s"(gw), "s"(wo), "s"(wob), "s"(ws), "s"(out));
    unsigned tag;
    int b = blockIdx.x;
    u32x4 vp[5], sa[5]; unsigned sd[2];
    const unsigned vl = proj_phase(x, ckv, wq, wuk, wuv, wqkv, gw, wo, wob, out, ws, lds, wid, tag, vp, sa, sd);
    asm volatile("" : "+s"(ws));
    unsigned* const flags = (unsigned*)(ws + WS_F);
    RTS(2);
#define AST(L) do {} while (0)
#define SD(J8, H, W) ((unsigned)WS_AF + (((J8) + 2u * ((W) & 2u) + ((W) & 1u)) * KS_TOT + 18u + 4u * (H)) * 1024u)
#define SRDA (u32x4){PLO(ws), PHI(ws) & 0xffffu, (unsigned)WS_L, 0x00020000u}
#define ATT_TAIL "s_endpgm\n"
    asm volatile(ATT_ASM ATT_TAIL "L_end_%=:\n s_nop 1"
                 :
                 : "{v[0:3]}"(vp[0]), "{v[4:7]}"(vp[1]), "{v[8:11]}"(vp[2]), "{v[12:15]}"(vp[3]), "{v[16:19]}"(vp[4]), "{v20}"(vl), "{s[56:59]}"(sa[4]), "{s60}"(sd[0]), "{s61}"(sd[1]), "{s62}"(tag),
                   "{s[8:11]}"(sa[0]), "{s[12:15]}"(sa[1]), "{s[16:19]}"(sa[2]), "{s[20:23]}"(sa[3])
                 : "memory", "m0", "vcc", "scc", ATT_VCLOB, ATT_SCLOB, ACLOB_LO, ACLOB_HI);
    {
        const u16* const __attribute__((address_space(4)))* ka = (const u16* const __attribute__((address_space(4)))*)__builtin_amdgcn_kernarg_segment_ptr();
        unsigned b2 = b, w2 = wid;
        asm volatile("s_waitcnt vmcnt(0)" : "+s"(ka), "+s"(b2), "+s"(w2), "+s"(ws) :: "memory");
        const unsigned j2 = b2 >> 5, p2 = w2 & 1u, h2 = (b2 & 7u) * 4u + ((b2 >> 3) & 3u);
#pragma unroll 1
        for (unsigned i = w2 & 2u; i < 2u + 3u * (w2 & 2u); ++i)
            att_exact(ka[0], ka[2], ws, h2, (i < 4u ? 1920u - 128u * j2 : 128u * j2 - 128u) + 32u * i + 16u * p2, *reinterpret_cast<const float*>(lds + LDS_G + (32u * i + 16u * p2 + (lane_id() & 15u)) * 4u));
        asm volatile("s_waitcnt vmcnt(0)" ::: "memory");
        unsigned* fa = (unsigned*)(ws + WS_F) + F_R + (8u * j2 + (b2 & 7u)) * 256u + (2u + p2) * 64u + ((b2 >> 3) & 3u);
        if (w2 & 2u) st_flag(fa, tag);
        wait_flags(fa, ld_flag(fa), tag);
        const unsigned l2 = lane_id(); u32x4 c[3]; out_consts(c, ka[7], ka[8], ka[10], b2, w2, l2);
        asm volatile("s_waitcnt vmcnt(0) lgkmcnt(0)\n\ts_nop 7\n\ts_branch L_att_stub" :: "{v[0:3]}"(c[0]), "{v[4:7]}"(c[1]), "{v[8:11]}"(c[2]), "{v20}"(l2 * 16u), "{s[56:59]}"(SRDA), "{s60}"(SD(8u * j2, h2, w2)),
                     "{s61}"(SD(120u - 8u * j2, h2, w2)), "{s62}"(tag) : "memory");
    }
    RTS(5);
}

extern "C" void kernel_launch(void* const* d_in, const int* in_sizes, int n_in, void* d_out, int out_size, void* d_ws, size_t ws_size,
                              hipStream_t stream) {
    (void)hipFuncSetAttribute((const void*)fused_kernel, hipFuncAttributeMaxDynamicSharedMemorySize, att::LDS_BYTES);
    fused_kernel<<<dim3(256), 256, att::LDS_BYTES, stream>>>((const u16*)d_in[0], (const u16*)d_in[4], (const u16*)d_in[5], (const u16*)d_in[6], (const u16*)d_in[7], (const u16*)d_in[1],
                                                             (const u16*)d_in[9], (const u16*)d_in[2], (const u16*)d_in[8], (char*)d_ws, (u16*)d_out);
}
```

```cpp
#include <hip/hip_runtime.h>
#include <hip/hip_bf16.h>
#include <stdint.h>
#include <utility>

typedef short bf16x8 __attribute__((ext_vector_type(8)));
typedef short s16x4 __attribute__((ext_vector_type(4)));
typedef float f32x16 __attribute__((ext_vector_type(16)));
typedef float f32x4 __attribute__((ext_vector_type(4)));
typedef unsigned u32x4 __attribute__((ext_vector_type(4)));
typedef unsigned short u16;

constexpr int S = 2048, DM = 576, NH = 32, HD = 128, DC = 512, NQKV = 1728, DHA = 18;
constexpr int NBIG = NH * HD;
constexpr int KS_TOT = 146;

#define SBAR() __builtin_amdgcn_sched_barrier(0)
constexpr size_t WS_Q = 0, WS_K = WS_Q + (size_t)S * NBIG * 2, WS_V = WS_K + (size_t)S * NBIG * 2, WS_AF = WS_V + (size_t)S * NBIG * 2, WS_X = WS_AF + (size_t)128 * KS_TOT * 1024,
                 WS_P = WS_X + (size_t)64 * 16 * 9216, WS_F = WS_P + (size_t)128 * (4 * 32 * 256 + 1024) * 4;
constexpr size_t WS_L = (size_t)96 << 20;
static_assert(WS_F == 96206848, "workspace map is baked into the generated asm");
constexpr int F_EPOCH = 0, F_J = 1024, F_ST = 8192, F_KV = 40960, F_R = 65536;
constexpr unsigned TAG_STEP = 0x9E3779B1u;
__device__ __forceinline__ unsigned ld_flag(const unsigned* p) { return __hip_atomic_load(p, __ATOMIC_RELAXED, __HIP_MEMORY_SCOPE_AGENT); }
__device__ __forceinline__ void st_flag(unsigned* p, unsigned v) { __hip_atomic_store(p, v, __ATOMIC_RELAXED, __HIP_MEMORY_SCOPE_AGENT); }
__device__ __forceinline__ unsigned ld_flag_async(const unsigned* p) { unsigned v; asm volatile("global_load_dword %0, %1, off sc1" : "=v"(v) : "v"(p) : "memory"); return v; }
#define WAITV(v, N) asm volatile("s_waitcnt vmcnt(" #N ")" : "+v"(v) :: "memory")
__device__ __forceinline__ void wait_flags(const unsigned* p, unsigned v, unsigned tag) {
    if (__builtin_expect(__builtin_amdgcn_ballot_w64(v != tag) != 0ull, 0)) {
        unsigned spins = 0;
        do { __builtin_amdgcn_s_sleep(1); } while (__builtin_amdgcn_ballot_w64(ld_flag(p) != tag) != 0ull && ++spins < (1u << 22));
    }
    asm volatile("" ::: "memory");
}

__device__ __forceinline__ unsigned cvtpk(float lo, float hi) {
    unsigned r; asm volatile("v_cvt_pk_bf16_f32 %0, %1, %2" : "=v"(r) : "v"(lo), "v"(hi)); return r;
}
__device__ __forceinline__ float bf2f(u16 v) { return __uint_as_float(((unsigned)v) << 16); }
__device__ __forceinline__ int crow(int r, int hi) { return (r & 3) + 8 * (r >> 2) + 4 * hi; }

typedef __attribute__((address_space(3))) s16x4* lds_s16x4_p;
__device__ __forceinline__ s16x4 trrd(unsigned addr) { return __builtin_amdgcn_ds_read_tr16_b64_v4i16((lds_s16x4_p)addr); }

__device__ __forceinline__ unsigned lane_id() { unsigned l; asm volatile("v_mbcnt_lo_u32_b32 %0, -1, 0\n\tv_mbcnt_hi_u32_b32 %0, -1, %0" : "=v"(l)); return l; }
#define RTS(slot) do {} while (0)
#define CT_L 5u
__device__ __forceinline__ void code_touch(unsigned wi, unsigned lds_scratch) {
    unsigned long long pc; asm volatile("s_getpc_b64 %0" : "=s"(pc));
    asm volatile("s_mov_b32 m0, %0\n\ts_mov_b64 exec, %3\n\tglobal_load_lds_dword %1, %2\n\ts_mov_b64 exec, -1" :: "s"(lds_scratch), "v"(((threadIdx.x & 63u) * 128u + wi) * 128u), "s"(pc & ~127ull), "n"((1u << CT_L) - 1u) : "m0", "memory");
}
#define ACLOB_LO "a0","a1","a2","a3","a4","a5","a6","a7","a8","a9","a10","a11","a12","a13","a14","a15","a16","a17","a18","a19","a20","a21","a22","a23","a24","a25","a26","a27","a28","a29","a30","a31","a32","a33","a34","a35","a36","a37","a38","a39","a40","a41","a42","a43","a44","a45","a46","a47","a48","a49","a50","a51","a52","a53","a54","a55","a56","a57","a58","a59","a60","a61","a62","a63","a64","a65","a66","a67","a68","a69","a70","a71","a72","a73","a74","a75","a76","a77","a78","a79","a80","a81","a82","a83","a84","a85","a86","a87","a88","a89","a90","a91","a92","a93","a94","a95","a96","a97","a98","a99","a100","a101","a102","a103","a104","a105","a106","a107","a108","a109","a110","a111","a112","a113","a114","a115","a116","a117","a118","a119","a120","a121","a122","a123","a124","a125","a126","a127"
#define ACLOB_HI "a128","a129","a130","a131","a132","a133","a134","a135","a136","a137","a138","a139","a140","a141","a142","a143","a144","a145","a146","a147","a148","a149","a150","a151","a152","a153","a154","a155","a156","a157","a158","a159","a160","a161","a162","a163","a164","a165","a166","a167","a168","a169","a170","a171","a172","a173","a174","a175","a176","a177","a178","a179","a180","a181","a182","a183","a184","a185","a186","a187","a188","a189","a190","a191","a192","a193","a194","a195","a196","a197","a198","a199","a200","a201","a202","a203","a204","a205","a206","a207","a208","a209","a210","a211","a212","a213","a214","a215","a216","a217","a218","a219","a220","a221","a222","a223","a224","a225","a226","a227","a228","a229","a230","a231","a232","a233","a234","a235","a236","a237","a238","a239","a240","a241","a242","a243","a244","a245","a246","a247","a248","a249","a250","a251","a252","a253","a254","a255"
#define PROJ_PRE0 \
"s_add_u32 s62, %[rc], 0x0\n" \
"s_and_b32 s62, s62, 0x3ff\n" \
"s_lshl_b32 s63, s62, 12\n" \
"s_add_u32 m0, %[mA], 0x0\n" \
"s_add_u32 s85, s62, 0x0\n" \
"buffer_load_dwordx4 v22, s[64:67], s85 offen lds\n" \
"s_add_u32 m0, %[mA], 0x400\n" \
"s_add_u32 s84, s62, 0x2000\n" \
"buffer_load_dwordx4 v23, s[64:67], s84 offen lds\n" \
"s_add_u32 m0, %[mB], 0x0\n" \
"s_add_u32 s85, s63, 0x0\n" \
"buffer_load_dwordx4 v26, s[68:71], s85 offen lds\n" \
"s_add_u32 m0, %[mB], 0x4000\n" \
"s_add_u32 s84, s63, 0x0\n" \
"buffer_load_dwordx4 v26, s[72:75], s84 offen lds\n" \
"s_add_u32 m0, %[mA], 0x800\n" \
"s_add_u32 s85, s62, 0x4000\n" \
"buffer_load_dwordx4 v22, s[64:67], s85 offen lds\n" \
"s_add_u32 m0, %[mA], 0xc00\n" \
"s_add_u32 s84, s62, 0x6000\n" \
"buffer_load_dwordx4 v23, s[64:67], s84 offen lds\n" \
"s_add_u32 m0, %[mB], 0x400\n" \
"s_add_u32 s85, s63, 0x8000\n" \
"buffer_load_dwordx4 v26, s[68:71], s85 offen lds\n" \
"s_add_u32 m0, %[mB], 0x4400\n" \
"s_add_u32 s84, s63, 0x8000\n" \
"buffer_load_dwordx4 v26, s[72:75], s84 offen lds\n"
#define PROJ_PRE1 \
"s_add_u32 s62, %[rc], 0x0\n" \
"s_and_b32 s62, s62, 0x3ff\n" \
"s_lshl_b32 s63, s62, 12\n" \
"s_add_u32 m0, %[mA], 0x1000\n" \
"s_add_u32 s85, s62, 0x8000\n" \
"buffer_load_dwordx4 v22, s[64:67], s85 offen lds\n" \
"s_add_u32 m0, %[mA], 0x1400\n" \
"s_add_u32 s84, s62, 0xa000\n" \
"buffer_load_dwordx4 v23, s[64:67], s84 offen lds\n" \
"s_add_u32 m0, %[mB], 0x800\n" \
"s_add_u32 s85, s63, 0x10000\n" \
"buffer_load_dwordx4 v27, s[68:71], s85 offen lds\n" \
"s_add_u32 m0, %[mB], 0x4800\n" \
"s_add_u32 s84, s63, 0x10000\n" \
"buffer_load_dwordx4 v27, s[72:75], s84 offen lds\n" \
"s_add_u32 m0, %[mA], 0x1800\n" \
"s_add_u32 s85, s62, 0xc000\n" \
"buffer_load_dwordx4 v22, s[64:67], s85 offen lds\n" \
"s_add_u32 m0, %[mA], 0x1c00\n" \
"s_add_u32 s84, s62, 0xe000\n" \
"buffer_load_dwordx4 v23, s[64:67], s84 offen lds\n" \
"s_add_u32 m0, %[mB], 0xc00\n" \
"s_add_u32 s85, s63, 0x18000\n" \
"buffer_load_dwordx4 v27, s[68:71], s85 offen lds\n" \
"s_add_u32 m0, %[mB], 0x4c00\n" \
"s_add_u32 s84, s63, 0x18000\n" \
"buffer_load_dwordx4 v27, s[72:75], s84 offen lds\n"
#define PROJ_PRE2 \
"s_add_u32 s62, %[rc], 0x80\n" \
"s_and_b32 s62, s62, 0x3ff\n" \
"s_lshl_b32 s63, s62, 12\n" \
"s_add_u32 m0, %[mA], 0x8000\n" \
"s_add_u32 s85, s62, 0x0\n" \
"buffer_load_dwordx4 v22, s[64:67], s85 offen lds\n" \
"s_add_u32 m0, %[mA], 0x8400\n" \
"s_add_u32 s84, s62, 0x2000\n" \
"buffer_load_dwordx4 v23, s[64:67], s84 offen lds\n"
#define PROJ_PRE3 \
"s_add_u32 s62, %[rc], 0x80\n" \
"s_and_b32 s62, s62, 0x3ff\n" \
"s_lshl_b32 s63, s62, 12\n" \
"s_add_u32 m0, %[mB], 0x8000\n" \
"s_add_u32 s85, s63, 0x0\n" \
"buffer_load_dwordx4 v26, s[68:71], s85 offen lds\n" \
"s_add_u32 m0, %[mB], 0xc000\n" \
"s_add_u32 s84, s63, 0x0\n" \
"buffer_load_dwordx4 v26, s[72:75], s84 offen lds\n" \
"s_add_u32 m0, %[mA], 0x8800\n" \
"s_add_u32 s85, s62, 0x4000\n" \
"buffer_load_dwordx4 v22, s[64:67], s85 offen lds\n" \
"s_add_u32 m0, %[mA], 0x8c00\n" \
"s_add_u32 s84, s62, 0x6000\n" \
"buffer_load_dwordx4 v23, s[64:67], s84 offen lds\n" \
"s_add_u32 m0, %[mB], 0x8400\n" \
"s_add_u32 s85, s63, 0x8000\n" \
"buffer_load_dwordx4 v26, s[68:71], s85 offen lds\n" \
"s_add_u32 m0, %[mB], 0xc400\n" \
"s_add_u32 s84, s63, 0x8000\n" \
"buffer_load_dwordx4 v26, s[72:75], s84 offen lds\n" \
"s_add_u32 m0, %[mA], 0x9000\n" \
"s_add_u32 s85, s62, 0x8000\n" \
"buffer_load_dwordx4 v22, s[64:67], s85 offen lds\n" \
"s_add_u32 m0, %[mA], 0x9400\n" \
"s_add_u32 s84, s62, 0xa000\n" \
"buffer_load_dwordx4 v23, s[64:67], s84 offen lds\n"
#define PROJ_TKE 0
#define PROJ_T0 0
#define PROJ_ROT 2*(sp+mt)
#define PROJ_TC 0
#define PROJ_ASM \
".p2align 3\n" \
"s_branch L_go_%=\n" \
"L_pl_%=:\n" \
"s_mov_b32 s63, 0\n" \
"L_ql_%=:\n" \
"s_sleep 1\n" \
"v_bfe_u32 v44, v20, 2, 5\n" \
"buffer_load_dword v44, v44, s[80:83], s39 offen offset:64 sc1\n" \
"s_waitcnt vmcnt(0)\n" \
"v_cmp_ne_u32_e32 vcc, %[tag], v44\n" \
"s_add_u32 s63, s63, 1\n" \
"s_nop 3\n" \
"s_cbranch_vccz L_rl_%=\n" \
"s_cmp_gt_u32 s63, 0x400000\n" \
"s_cbranch_scc0 L_ql_%=\n" \
"s_branch L_rl_%=\n" \
"L_pa_%=:\n" \
"s_mov_b32 s63, 0\n" \
"L_qa_%=:\n" \
"s_sleep 1\n" \
"v_bfe_u32 v44, v20, 2, 5\n" \
"buffer_load_dword v44, v44, s[80:83], s39 offen offset:0 sc1\n" \
"s_waitcnt vmcnt(0)\n" \
"v_cmp_ne_u32_e32 vcc, %[tag], v44\n" \
"s_add_u32 s63, s63, 1\n" \
"s_nop 3\n" \
"s_cbranch_vccz L_ra_%=\n" \
"s_cmp_gt_u32 s63, 0x400000\n" \
"s_cbranch_scc0 L_qa_%=\n" \
"s_branch L_ra_%=\n" \
"L_go_%=:\n" \
"v_xor_b32_e32 v33, 0x20, v32\n" \
"s_mov_b32 s80, %[wsl]\n" \
"s_and_b32 s81, %[wsh], 0xffff\n" \
"s_mov_b32 s82, 0x10000000\n" \
"s_mov_b32 s83, 0x20000\n" \
"s_mov_b32 s84, 0x5bc0000\n" \
"s_add_u32 s62, s22, 0x80\n" \
"buffer_load_dword v180, off, s[80:83], s84 sc1\n" \
"s_and_b32 s62, s62, 0x3ff\n" \
"s_lshl_b32 s63, s62, 12\n" \
"v_xor_b32_e64 v34, 0x40, v32\n" \
"v_xor_b32_e32 v35, 0x60, v32\n" \
"s_waitcnt vmcnt(11)\n" \
"s_barrier\n" \
"ds_read_b64_tr_b16 v[120:121], v32 offset:8192\n" \
"ds_read_b64_tr_b16 v[122:123], v32 offset:9216\n" \
"ds_read_b128 v[56:59], v29 offset:16384\n" \
"ds_read_b64_tr_b16 v[116:117], v32 offset:0\n" \
"ds_read_b64_tr_b16 v[118:119], v32 offset:1024\n" \
"ds_read_b128 v[52:55], v28 offset:16384\n" \
"s_add_u32 m0, %[mB], 0x8800\n" \
"s_add_u32 s85, s63, 0x10000\n" \
"s_mov_b32 s76, s12\n" \
"ds_read_b128 v[60:63], v28 offset:18432\n" \
"s_and_b32 s77, s13, 0xffff\n" \
"s_mov_b32 s78, 0x1e6000\n" \
"s_mov_b32 s79, 0x20000\n" \
"ds_read_b128 v[64:67], v29 offset:18432\n" \
"s_waitcnt lgkmcnt(5)\n" \
"v_mfma_f32_16x16x32_bf16 a[0:3], v[120:123], v[56:59], 0\n" \
"ds_read_b128 v[72:75], v29 offset:20480\n" \
"ds_read_b128 v[68:71], v28 offset:20480\n" \
"ds_read_b128 v[76:79], v28 offset:22528\n" \
"buffer_load_dwordx4 v27, s[68:71], s85 offen lds\n" \
"ds_read_b128 v[80:83], v29 offset:22528\n" \
"s_waitcnt lgkmcnt(6)\n" \
"v_mfma_f32_16x16x32_bf16 a[0:3], v[116:119], v[52:55], a[0:3]\n" \
"ds_read_b64_tr_b16 v[128:129], v33 offset:8192\n" \
"s_waitcnt lgkmcnt(6)\n" \
"v_mfma_f32_16x16x32_bf16 a[32:35], v[116:119], v[60:63], 0\n" \
"ds_read_b64_tr_b16 v[130:131], v33 offset:9216\n" \
"ds_read_b64_tr_b16 v[124:125], v33 offset:0\n" \
"ds_read_b64_tr_b16 v[126:127], v33 offset:1024\n" \
"s_waitcnt lgkmcnt(8)\n" \
"s_nop 0\n" \
"v_mfma_f32_16x16x32_bf16 a[32:35], v[120:123], v[64:67], a[32:35]\n" \
"ds_read_b64_tr_b16 v[136:137], v34 offset:8192\n" \
"ds_read_b64_tr_b16 v[138:139], v34 offset:9216\n" \
"ds_read_b64_tr_b16 v[132:133], v34 offset:0\n" \
"s_add_u32 m0, %[mB], 0xc800\n" \
"ds_read_b64_tr_b16 v[134:135], v34 offset:1024\n" \
"s_add_u32 s84, s63, 0x10000\n" \
"buffer_load_dwordx4 v27, s[72:75], s84 offen lds\n" \
"ds_read_b64_tr_b16 v[144:145], v35 offset:8192\n" \
"ds_read_b64_tr_b16 v[146:147], v35 offset:9216\n" \
"ds_read_b64_tr_b16 v[140:141], v35 offset:0\n" \
"s_lshr_b32 s95, %[b], 3\n" \
"ds_read_b64_tr_b16 v[142:143], v35 offset:1024\n" \
"s_lshl_b32 s96, s95, 2\n" \
"ds_read_b64_tr_b16 v[152:153], v32 offset:24576\n" \
"s_add_u32 s97, s96, %[w]\n" \
"ds_read_b64_tr_b16 v[154:155], v32 offset:25600\n" \
"v_lshrrev_b32_e32 v212, 4, v20\n" \
"s_mul_i32 s98, 27, s97\n" \
"v_min_u32_e32 v213, 26, v212\n" \
"v_add_u32_e64 v214, s98, v213\n" \
"v_mul_u32_u24_e32 v215, 0xaaab, v214\n" \
"s_waitcnt lgkmcnt(15)\n" \
"v_mfma_f32_16x16x32_bf16 a[64:67], v[120:123], v[72:75], 0\n" \
"ds_read_b64_tr_b16 v[148:149], v32 offset:16384\n" \
"ds_read_b64_tr_b16 v[150:151], v32 offset:17408\n" \
"v_mfma_f32_16x16x32_bf16 a[64:67], v[116:119], v[68:71], a[64:67]\n" \
"s_add_u32 m0, %[mA], 0x9800\n" \
"s_add_u32 s85, s62, 0xc000\n" \
"v_mfma_f32_16x16x32_bf16 a[96:99], v[116:119], v[76:79], 0\n" \
"ds_read_b64_tr_b16 v[160:161], v33 offset:24576\n" \
"s_waitcnt lgkmcnt(15)\n" \
"v_mfma_f32_16x16x32_bf16 a[96:99], v[120:123], v[80:83], a[96:99]\n" \
"buffer_load_dwordx4 v22, s[64:67], s85 offen lds\n" \
"v_mfma_f32_16x16x32_bf16 a[4:7], v[128:131], v[56:59], 0\n" \
"s_add_u32 m0, %[mA], 0x9c00\n" \
"s_waitcnt lgkmcnt(13)\n" \
"s_nop 0\n" \
"v_mfma_f32_16x16x32_bf16 a[4:7], v[124:127], v[52:55], a[4:7]\n" \
"s_add_u32 s84, s62, 0xe000\n" \
"ds_read_b64_tr_b16 v[162:163], v33 offset:25600\n" \
"v_mfma_f32_16x16x32_bf16 a[36:39], v[124:127], v[60:63], 0\n" \
"buffer_load_dwordx4 v23, s[64:67], s84 offen lds\n" \
"v_mfma_f32_16x16x32_bf16 a[36:39], v[128:131], v[64:67], a[36:39]\n" \
"s_add_u32 m0, %[mB], 0x8c00\n" \
"s_add_u32 s85, s63, 0x18000\n" \
"v_mfma_f32_16x16x32_bf16 a[68:71], v[128:131], v[72:75], 0\n" \
"ds_read_b64_tr_b16 v[156:157], v33 offset:16384\n" \
"ds_read_b64_tr_b16 v[158:159], v33 offset:17408\n" \
"v_mfma_f32_16x16x32_bf16 a[68:71], v[124:127], v[68:71], a[68:71]\n" \
"buffer_load_dwordx4 v27, s[68:71], s85 offen lds\n" \
"v_mfma_f32_16x16x32_bf16 a[100:103], v[124:127], v[76:79], 0\n" \
"s_add_u32 m0, %[mB], 0xcc00\n" \
"s_add_u32 s84, s63, 0x18000\n" \
"v_mfma_f32_16x16x32_bf16 a[100:103], v[128:131], v[80:83], a[100:103]\n" \
"ds_read_b64_tr_b16 v[168:169], v34 offset:24576\n" \
"s_waitcnt lgkmcnt(7)\n" \
"v_mfma_f32_16x16x32_bf16 a[8:11], v[136:139], v[56:59], 0\n" \
"buffer_load_dwordx4 v27, s[72:75], s84 offen lds\n" \
"v_mfma_f32_16x16x32_bf16 a[8:11], v[132:135], v[52:55], a[8:11]\n" \
"ds_read_b64_tr_b16 v[170:171], v34 offset:25600\n" \
"ds_read_b64_tr_b16 v[164:165], v34 offset:16384\n" \
"v_mfma_f32_16x16x32_bf16 a[40:43], v[132:135], v[60:63], 0\n" \
"ds_read_b64_tr_b16 v[166:167], v34 offset:17408\n" \
"v_lshrrev_b32_e32 v216, 18, v215\n" \
"v_mfma_f32_16x16x32_bf16 a[40:43], v[136:139], v[64:67], a[40:43]\n" \
"v_mul_u32_u24_e32 v218, 6, v216\n" \
"v_sub_u32_e32 v219, v214, v218\n" \
"v_mfma_f32_16x16x32_bf16 a[72:75], v[136:139], v[72:75], 0\n" \
"v_lshrrev_b32_e32 v220, 1, v219\n" \
"v_mul_u32_u24_e32 v217, 27, v216\n" \
"v_mfma_f32_16x16x32_bf16 a[72:75], v[132:135], v[68:71], a[72:75]\n" \
"v_mul_u32_u24_e32 v221, 9, v220\n" \
"v_add_u32_e32 v222, v217, v221\n" \
"v_mfma_f32_16x16x32_bf16 a[104:107], v[132:135], v[76:79], 0\n" \
"v_and_b32_e32 v223, 1, v219\n" \
"s_and_b32 s94, 7, %[b]\n" \
"v_mfma_f32_16x16x32_bf16 a[104:107], v[136:139], v[80:83], a[104:107]\n" \
"ds_read_b64_tr_b16 v[176:177], v35 offset:24576\n" \
"v_add_u32_e64 v224, v222, v223\n" \
"v_mfma_f32_16x16x32_bf16 a[12:15], v[144:147], v[56:59], 0\n" \
"ds_read_b64_tr_b16 v[178:179], v35 offset:25600\n" \
"v_add_u32_e64 v225, s94, v224\n" \
"v_mfma_f32_16x16x32_bf16 a[12:15], v[140:143], v[52:55], a[12:15]\n" \
"ds_read_b64_tr_b16 v[172:173], v35 offset:16384\n" \
"v_lshlrev_b32_e32 v186, 7, v225\n" \
"v_mfma_f32_16x16x32_bf16 a[44:47], v[140:143], v[60:63], 0\n" \
"ds_read_b64_tr_b16 v[174:175], v35 offset:17408\n" \
"ds_read_b128 v[84:87], v28 offset:0\n" \
"v_mfma_f32_16x16x32_bf16 a[44:47], v[144:147], v[64:67], a[44:47]\n" \
"ds_read_b128 v[88:91], v29 offset:0\n" \
"ds_read_b128 v[92:95], v28 offset:2048\n" \
"v_mfma_f32_16x16x32_bf16 a[76:79], v[144:147], v[72:75], 0\n" \
"ds_read_b128 v[96:99], v29 offset:2048\n" \
"s_mul_i32 s89, 6, s97\n" \
"v_mfma_f32_16x16x32_bf16 a[76:79], v[140:143], v[68:71], a[76:79]\n" \
"ds_read_b128 v[100:103], v28 offset:4096\n" \
"s_add_u32 s95, 0xffffffdc, s89\n" \
"v_mfma_f32_16x16x32_bf16 a[108:111], v[140:143], v[76:79], 0\n" \
"ds_read_b128 v[104:107], v29 offset:4096\n" \
"s_mul_i32 s99, 0x1200, s94\n" \
"v_mfma_f32_16x16x32_bf16 a[108:111], v[144:147], v[80:83], a[108:111]\n" \
"ds_read_b128 v[108:111], v28 offset:6144\n" \
"s_mul_i32 s100, 36, s97\n" \
"v_mfma_f32_16x16x32_bf16 a[16:19], v[56:59], v[152:155], 0\n" \
"ds_read_b128 v[112:115], v29 offset:6144\n" \
"s_waitcnt lgkmcnt(12)\n" \
"v_mfma_f32_16x16x32_bf16 a[16:19], v[52:55], v[148:151], a[16:19]\n" \
"v_add_u32_e32 v227, s95, v212\n" \
"s_add_u32 s101, s99, s100\n" \
"v_mfma_f32_16x16x32_bf16 a[48:51], v[60:63], v[148:151], 0\n" \
"s_mul_i32 s88, 0x288, s94\n" \
"v_min_u32_e32 v228, 0x287, v227\n" \
"v_mfma_f32_16x16x32_bf16 a[48:51], v[64:67], v[152:155], a[48:51]\n" \
"s_mov_b32 m0, %[mS]\n" \
"s_mov_b32 s86, s12\n" \
"v_mfma_f32_16x16x32_bf16 a[80:83], v[72:75], v[152:155], 0\n" \
"s_mov_b32 s87, s13\n" \
"v_add_u32_e32 v226, s101, v212\n" \
"v_mfma_f32_16x16x32_bf16 a[80:83], v[68:71], v[148:151], a[80:83]\n" \
"v_add_u32_e32 v229, s88, v228\n" \
"v_cmp_gt_u32_e32 vcc, 36, v212\n" \
"v_mfma_f32_16x16x32_bf16 a[112:115], v[76:79], v[148:151], 0\n" \
"s_lshl_b32 s96, %[w], 6\n" \
"v_cndmask_b32_e32 v230, v229, v226, vcc\n" \
"v_mfma_f32_16x16x32_bf16 a[112:115], v[80:83], v[152:155], a[112:115]\n" \
"v_lshlrev_b32_e32 v16, 7, v230\n" \
"v_lshrrev_b32_e32 v212, 4, v20\n" \
"v_mfma_f32_16x16x32_bf16 a[20:23], v[56:59], v[160:163], 0\n" \
"v_add_u32_e64 v213, s96, v212\n" \
"v_mul_u32_u24_e32 v214, 0x2aab, v213\n" \
"v_mfma_f32_16x16x32_bf16 a[20:23], v[52:55], v[156:159], a[20:23]\n" \
"v_lshrrev_b32_e32 v215, 16, v214\n" \
"s_lshr_b32 s94, %[b], 5\n" \
"s_mov_b32 exec_lo, 0x7ffffff\n" \
"s_mov_b32 exec_hi, 0x0\n" \
"global_load_lds_dword v186, s[86:87]\n" \
"s_mov_b64 exec, -1\n" \
"v_mfma_f32_16x16x32_bf16 a[52:55], v[60:63], v[156:159], 0\n" \
"v_mul_u32_u24_e32 v218, 6, v215\n" \
"s_mul_i32 s95, 0x48, s94\n" \
"v_mfma_f32_16x16x32_bf16 a[52:55], v[64:67], v[160:163], a[52:55]\n" \
"v_sub_u32_e32 v219, v213, v218\n" \
"v_add_u32_e64 v216, s95, v215\n" \
"v_mfma_f32_16x16x32_bf16 a[84:87], v[72:75], v[160:163], 0\n" \
"v_lshrrev_b32_e32 v220, 1, v219\n" \
"s_and_b32 s100, 7, %[b]\n" \
"v_mfma_f32_16x16x32_bf16 a[84:87], v[68:71], v[156:159], a[84:87]\n" \
"s_lshr_b32 s88, %[b], 3\n" \
"v_mul_u32_u24_e32 v217, 0xd80, v216\n" \
"v_mfma_f32_16x16x32_bf16 a[116:119], v[76:79], v[156:159], 0\n" \
"v_mul_u32_u24_e32 v221, 0x480, v220\n" \
"v_and_b32_e32 v223, 1, v219\n" \
"v_mfma_f32_16x16x32_bf16 a[116:119], v[80:83], v[160:163], a[116:119]\n" \
"s_add_u32 s97, 0x100, s96\n" \
"s_lshl_b32 s101, s100, 2\n" \
"v_mfma_f32_16x16x32_bf16 a[24:27], v[56:59], v[168:171], 0\n" \
"s_and_b32 s89, 3, s88\n" \
"v_add_u32_e64 v222, v217, v221\n" \
"v_mfma_f32_16x16x32_bf16 a[24:27], v[52:55], v[164:167], a[24:27]\n" \
"v_lshlrev_b32_e32 v224, 4, v223\n" \
"s_add_u32 s96, s101, s89\n" \
"v_mfma_f32_16x16x32_bf16 a[56:59], v[60:63], v[164:167], 0\n" \
"v_add_u32_e32 v192, v222, v224\n" \
"s_mul_i32 s56, 36, s96\n" \
"v_mfma_f32_16x16x32_bf16 a[56:59], v[64:67], v[168:171], a[56:59]\n" \
"v_add_u32_e64 v225, s97, v212\n" \
"v_mul_u32_u24_e32 v226, 0x2aab, v225\n" \
"v_mfma_f32_16x16x32_bf16 a[88:91], v[72:75], v[168:171], 0\n" \
"v_lshrrev_b32_e32 v227, 16, v226\n" \
"v_mul_u32_u24_e32 v230, 6, v227\n" \
"v_mfma_f32_16x16x32_bf16 a[88:91], v[68:71], v[164:167], a[88:91]\n" \
"v_sub_u32_e32 v231, v225, v230\n" \
"v_add_u32_e32 v228, s95, v227\n" \
"v_mfma_f32_16x16x32_bf16 a[120:123], v[76:79], v[164:167], 0\n" \
"v_lshrrev_b32_e32 v232, 1, v231\n" \
"v_mul_u32_u24_e32 v229, 0xd80, v228\n" \
"v_mfma_f32_16x16x32_bf16 a[120:123], v[80:83], v[168:171], a[120:123]\n" \
"v_mul_u32_u24_e32 v233, 0x480, v232\n" \
"s_waitcnt lgkmcnt(0)\n" \
"v_mfma_f32_16x16x32_bf16 a[28:31], v[56:59], v[176:179], 0\n" \
"s_add_u32 s62, s22, 0x100\n" \
"s_and_b32 s62, s62, 0x3ff\n" \
"v_mfma_f32_16x16x32_bf16 a[28:31], v[52:55], v[172:175], a[28:31]\n" \
"s_lshl_b32 s63, s62, 12\n" \
"v_and_b32_e32 v235, 1, v231\n" \
"v_mfma_f32_16x16x32_bf16 a[60:63], v[60:63], v[172:175], 0\n" \
"v_add_u32_e32 v234, v229, v233\n" \
"v_lshlrev_b32_e32 v236, 4, v235\n" \
"v_mfma_f32_16x16x32_bf16 a[60:63], v[64:67], v[176:179], a[60:63]\n" \
"v_add_u32_e64 v237, v234, v236\n" \
"v_cmp_gt_u32_e32 vcc, 0x1b0, v225\n" \
"v_mfma_f32_16x16x32_bf16 a[92:95], v[72:75], v[176:179], 0\n" \
"s_mul_i32 s98, 18, %[w]\n" \
"v_cndmask_b32_e32 v193, 0x40000000, v237, vcc\n" \
"v_mfma_f32_16x16x32_bf16 a[92:95], v[68:71], v[172:175], a[92:95]\n" \
"s_add_u32 s99, s95, s98\n" \
"v_add_u32_e32 v238, s99, v212\n" \
"v_mfma_f32_16x16x32_bf16 a[124:127], v[76:79], v[172:175], 0\n" \
"v_mul_u32_u24_e32 v239, 0xd80, v238\n" \
"v_cmp_gt_u32_e32 vcc, 18, v212\n" \
"v_mfma_f32_16x16x32_bf16 a[124:127], v[80:83], v[176:179], a[124:127]\n" \
"v_lshlrev_b32_e32 v240, 1, v238\n" \
"v_cndmask_b32_e32 v194, 0x40000000, v239, vcc\n" \
"s_waitcnt vmcnt(1)\n" \
"s_barrier\n" \
"v_mfma_f32_16x16x32_bf16 a[144:147], v[88:91], v[152:155], 0\n" \
"s_add_u32 m0, %[mA], 0x0\n" \
"s_add_u32 s85, s62, 0x0\n" \
"s_nop 0\n" \
"v_mfma_f32_16x16x32_bf16 a[144:147], v[84:87], v[148:151], a[144:147]\n" \
"buffer_load_dwordx4 v22, s[64:67], s85 offen lds\n" \
"v_mfma_f32_16x16x32_bf16 a[176:179], v[92:95], v[148:151], 0\n" \
"ds_read_b128 v[56:59], v29 offset:49152\n" \
"ds_read_b128 v[52:55], v28 offset:49152\n" \
"v_mfma_f32_16x16x32_bf16 a[176:179], v[96:99], v[152:155], a[176:179]\n" \
"s_add_u32 m0, %[mA], 0x400\n" \
"s_add_u32 s84, s62, 0x2000\n" \
"v_mfma_f32_16x16x32_bf16 a[208:211], v[104:107], v[152:155], 0\n" \
"buffer_load_dwordx4 v23, s[64:67], s84 offen lds\n" \
"v_mfma_f32_16x16x32_bf16 a[208:211], v[100:103], v[148:151], a[208:211]\n" \
"ds_read_b128 v[60:63], v28 offset:51200\n" \
"ds_read_b128 v[64:67], v29 offset:51200\n" \
"v_mfma_f32_16x16x32_bf16 a[240:243], v[108:111], v[148:151], 0\n" \
"ds_read_b64_tr_b16 v[148:149], v32 offset:49152\n" \
"ds_read_b64_tr_b16 v[150:151], v32 offset:50176\n" \
"v_mfma_f32_16x16x32_bf16 a[240:243], v[112:115], v[152:155], a[240:243]\n" \
"s_add_u32 m0, %[mB], 0x0\n" \
"s_add_u32 s85, s63, 0x0\n" \
"v_mfma_f32_16x16x32_bf16 a[148:151], v[88:91], v[160:163], 0\n" \
"buffer_load_dwordx4 v26, s[68:71], s85 offen lds\n" \
"v_mfma_f32_16x16x32_bf16 a[148:151], v[84:87], v[156:159], a[148:151]\n" \
"ds_read_b64_tr_b16 v[152:153], v32 offset:57344\n" \
"ds_read_b64_tr_b16 v[154:155], v32 offset:58368\n" \
"v_mfma_f32_16x16x32_bf16 a[180:183], v[92:95], v[156:159], 0\n" \
"ds_read_b128 v[72:75], v29 offset:53248\n" \
"ds_read_b128 v[68:71], v28 offset:53248\n" \
"v_mfma_f32_16x16x32_bf16 a[180:183], v[96:99], v[160:163], a[180:183]\n" \
"s_add_u32 m0, %[mB], 0x4000\n" \
"s_add_u32 s84, s63, 0x0\n" \
"s_nop 0\n" \
"v_mfma_f32_16x16x32_bf16 a[212:215], v[104:107], v[160:163], 0\n" \
"buffer_load_dwordx4 v26, s[72:75], s84 offen lds\n" \
"v_mfma_f32_16x16x32_bf16 a[212:215], v[100:103], v[156:159], a[212:215]\n" \
"ds_read_b128 v[76:79], v28 offset:55296\n" \
"ds_read_b128 v[80:83], v29 offset:55296\n" \
"v_mfma_f32_16x16x32_bf16 a[244:247], v[108:111], v[156:159], 0\n" \
"ds_read_b64_tr_b16 v[156:157], v33 offset:49152\n" \
"ds_read_b64_tr_b16 v[158:159], v33 offset:50176\n" \
"v_mfma_f32_16x16x32_bf16 a[244:247], v[112:115], v[160:163], a[244:247]\n" \
"s_add_u32 m0, %[mA], 0x800\n" \
"s_add_u32 s85, s62, 0x4000\n" \
"v_mfma_f32_16x16x32_bf16 a[152:155], v[88:91], v[168:171], 0\n" \
"buffer_load_dwordx4 v22, s[64:67], s85 offen lds\n" \
"v_mfma_f32_16x16x32_bf16 a[152:155], v[84:87], v[164:167], a[152:155]\n" \
"ds_read_b64_tr_b16 v[160:161], v33 offset:57344\n" \
"ds_read_b64_tr_b16 v[162:163], v33 offset:58368\n" \
"v_mfma_f32_16x16x32_bf16 a[184:187], v[92:95], v[164:167], 0\n" \
"v_cmp_gt_u32_e32 vcc, 18, v212\n" \
"v_lshrrev_b32_e32 v230, 1, v238\n" \
"v_mfma_f32_16x16x32_bf16 a[184:187], v[96:99], v[168:171], a[184:187]\n" \
"s_add_u32 m0, %[mA], 0xc00\n" \
"s_add_u32 s84, s62, 0x6000\n" \
"v_mfma_f32_16x16x32_bf16 a[216:219], v[104:107], v[168:171], 0\n" \
"buffer_load_dwordx4 v23, s[64:67], s84 offen lds\n" \
"v_mfma_f32_16x16x32_bf16 a[216:219], v[100:103], v[164:167], a[216:219]\n" \
"v_cndmask_b32_e32 v195, 0x40000000, v240, vcc\n" \
"s_nop 0\n" \
"v_and_b32_e32 v227, 0xff8, v238\n" \
"v_mfma_f32_16x16x32_bf16 a[248:251], v[108:111], v[164:167], 0\n" \
"ds_read_b64_tr_b16 v[164:165], v34 offset:49152\n" \
"ds_read_b64_tr_b16 v[166:167], v34 offset:50176\n" \
"v_mfma_f32_16x16x32_bf16 a[248:251], v[112:115], v[168:171], a[248:251]\n" \
"s_add_u32 m0, %[mB], 0x400\n" \
"s_add_u32 s85, s63, 0x8000\n" \
"v_mfma_f32_16x16x32_bf16 a[156:159], v[88:91], v[176:179], 0\n" \
"buffer_load_dwordx4 v26, s[68:71], s85 offen lds\n" \
"v_mfma_f32_16x16x32_bf16 a[156:159], v[84:87], v[172:175], a[156:159]\n" \
"ds_read_b64_tr_b16 v[168:169], v34 offset:57344\n" \
"ds_read_b64_tr_b16 v[170:171], v34 offset:58368\n" \
"v_mfma_f32_16x16x32_bf16 a[188:191], v[92:95], v[172:175], 0\n" \
"v_and_b32_e32 v232, 2, v230\n" \
"v_add_u32_e32 v229, v227, v232\n" \
"v_mfma_f32_16x16x32_bf16 a[188:191], v[96:99], v[176:179], a[188:191]\n" \
"s_add_u32 m0, %[mB], 0x4400\n" \
"s_add_u32 s84, s63, 0x8000\n" \
"v_mfma_f32_16x16x32_bf16 a[220:223], v[104:107], v[176:179], 0\n" \
"buffer_load_dwordx4 v26, s[72:75], s84 offen lds\n" \
"v_mfma_f32_16x16x32_bf16 a[220:223], v[100:103], v[172:175], a[220:223]\n" \
"v_and_b32_e32 v233, 1, v238\n" \
"v_add_u32_e32 v235, v229, v233\n" \
"v_mfma_f32_16x16x32_bf16 a[252:255], v[108:111], v[172:175], 0\n" \
"ds_read_b64_tr_b16 v[172:173], v35 offset:49152\n" \
"ds_read_b64_tr_b16 v[174:175], v35 offset:50176\n" \
"v_mfma_f32_16x16x32_bf16 a[252:255], v[112:115], v[176:179], a[252:255]\n" \
"s_add_u32 m0, %[mA], 0x1000\n" \
"s_add_u32 s85, s62, 0x8000\n" \
"v_mfma_f32_16x16x32_bf16 a[128:131], v[120:123], v[88:91], 0\n" \
"buffer_load_dwordx4 v22, s[64:67], s85 offen lds\n" \
"v_mfma_f32_16x16x32_bf16 a[128:131], v[116:119], v[84:87], a[128:131]\n" \
"ds_read_b64_tr_b16 v[176:177], v35 offset:57344\n" \
"ds_read_b64_tr_b16 v[178:179], v35 offset:58368\n" \
"v_mfma_f32_16x16x32_bf16 a[160:163], v[116:119], v[92:95], 0\n" \
"v_lshrrev_b32_e32 v242, 1, v216\n" \
"v_lshrrev_b32_e32 v244, 2, v216\n" \
"v_mfma_f32_16x16x32_bf16 a[160:163], v[120:123], v[96:99], a[160:163]\n" \
"s_add_u32 m0, %[mA], 0x1400\n" \
"s_add_u32 s84, s62, 0xa000\n" \
"v_mfma_f32_16x16x32_bf16 a[192:195], v[120:123], v[104:107], 0\n" \
"buffer_load_dwordx4 v23, s[64:67], s84 offen lds\n" \
"v_mfma_f32_16x16x32_bf16 a[192:195], v[116:119], v[100:103], a[192:195]\n" \
"v_lshrrev_b32_e32 v214, 1, v228\n" \
"v_lshrrev_b32_e32 v213, 2, v228\n" \
"v_mfma_f32_16x16x32_bf16 a[224:227], v[116:119], v[108:111], 0\n" \
"ds_read_b64_tr_b16 v[116:117], v32 offset:32768\n" \
"ds_read_b64_tr_b16 v[118:119], v32 offset:33792\n" \
"v_mfma_f32_16x16x32_bf16 a[224:227], v[120:123], v[112:115], a[224:227]\n" \
"s_add_u32 m0, %[mB], 0x800\n" \
"s_add_u32 s85, s63, 0x10000\n" \
"v_mfma_f32_16x16x32_bf16 a[132:135], v[128:131], v[88:91], 0\n" \
"buffer_load_dwordx4 v27, s[68:71], s85 offen lds\n" \
"v_mfma_f32_16x16x32_bf16 a[132:135], v[124:127], v[84:87], a[132:135]\n" \
"ds_read_b64_tr_b16 v[120:121], v32 offset:40960\n" \
"ds_read_b64_tr_b16 v[122:123], v32 offset:41984\n" \
"v_mfma_f32_16x16x32_bf16 a[164:167], v[124:127], v[92:95], 0\n" \
"v_lshrrev_b32_e32 v236, 1, v235\n" \
"v_lshrrev_b32_e32 v239, 2, v235\n" \
"v_mfma_f32_16x16x32_bf16 a[164:167], v[128:131], v[96:99], a[164:167]\n" \
"s_add_u32 m0, %[mB], 0x4800\n" \
"s_add_u32 s84, s63, 0x10000\n" \
"v_mfma_f32_16x16x32_bf16 a[196:199], v[128:131], v[104:107], 0\n" \
"buffer_load_dwordx4 v27, s[72:75], s84 offen lds\n" \
"v_mfma_f32_16x16x32_bf16 a[196:199], v[124:127], v[100:103], a[196:199]\n" \
"s_lshl_b32 s98, %[w], 10\n" \
"s_mul_i32 s101, 0x12000, s96\n" \
"v_mfma_f32_16x16x32_bf16 a[228:231], v[124:127], v[108:111], 0\n" \
"ds_read_b64_tr_b16 v[124:125], v33 offset:32768\n" \
"ds_read_b64_tr_b16 v[126:127], v33 offset:33792\n" \
"L_pf0:\n" \
"s_cbranch_execz L_pf4\n" \
"s_nop 0\n" \
"v_mfma_f32_16x16x32_bf16 a[228:231], v[128:131], v[112:115], a[228:231]\n" \
"ds_read_b64_tr_b16 v[128:129], v33 offset:40960\n" \
"ds_read_b64_tr_b16 v[130:131], v33 offset:41984\n" \
"v_mfma_f32_16x16x32_bf16 a[136:139], v[136:139], v[88:91], 0\n" \
"v_and_b32_e32 v243, 1, v242\n" \
"v_and_b32_e64 v245, 2, v244\n" \
"v_mfma_f32_16x16x32_bf16 a[136:139], v[132:135], v[84:87], a[136:139]\n" \
"v_and_b32_e32 v215, 1, v214\n" \
"v_and_b32_e32 v218, 2, v213\n" \
"v_mfma_f32_16x16x32_bf16 a[168:171], v[132:135], v[92:95], 0\n" \
"v_and_b32_e32 v237, 1, v236\n" \
"v_and_b32_e32 v240, 2, v239\n" \
"v_mfma_f32_16x16x32_bf16 a[168:171], v[136:139], v[96:99], a[168:171]\n" \
"s_add_u32 s99, 0x22000, s98\n" \
"s_add_u32 s89, 0x6000000, s101\n" \
"v_mfma_f32_16x16x32_bf16 a[200:203], v[136:139], v[104:107], 0\n" \
"s_mul_i32 s97, 0x2400, s94\n" \
"v_lshlrev_b32_e32 v241, 7, v216\n" \
"v_mfma_f32_16x16x32_bf16 a[200:203], v[132:135], v[100:103], a[200:203]\n" \
"v_or_b32_e32 v246, v243, v245\n" \
"v_or_b32_e32 v220, v215, v218\n" \
"v_mfma_f32_16x16x32_bf16 a[232:235], v[132:135], v[108:111], 0\n" \
"ds_read_b64_tr_b16 v[132:133], v34 offset:32768\n" \
"ds_read_b64_tr_b16 v[134:135], v34 offset:33792\n" \
"v_mfma_f32_16x16x32_bf16 a[232:235], v[136:139], v[112:115], a[232:235]\n" \
"ds_read_b64_tr_b16 v[136:137], v34 offset:40960\n" \
"ds_read_b64_tr_b16 v[138:139], v34 offset:41984\n" \
"v_mfma_f32_16x16x32_bf16 a[140:143], v[144:147], v[88:91], 0\n" \
"ds_read_b128 v[88:91], v29 offset:32768\n" \
"v_or_b32_e32 v242, v237, v240\n" \
"v_mfma_f32_16x16x32_bf16 a[140:143], v[140:143], v[84:87], a[140:143]\n" \
"ds_read_b128 v[84:87], v28 offset:32768\n" \
"v_add_u32_e64 v199, s99, v20\n" \
"v_mfma_f32_16x16x32_bf16 a[172:175], v[140:143], v[92:95], 0\n" \
"ds_read_b128 v[92:95], v28 offset:34816\n" \
"s_add_u32 s37, s89, s98\n" \
"v_mfma_f32_16x16x32_bf16 a[172:175], v[144:147], v[96:99], a[172:175]\n" \
"ds_read_b128 v[96:99], v29 offset:34816\n" \
"v_lshlrev_b32_e32 v247, 1, v246\n" \
"v_mfma_f32_16x16x32_bf16 a[204:207], v[144:147], v[104:107], 0\n" \
"ds_read_b128 v[104:107], v29 offset:36864\n" \
"v_lshlrev_b32_e32 v217, 1, v220\n" \
"v_mfma_f32_16x16x32_bf16 a[204:207], v[140:143], v[100:103], a[204:207]\n" \
"ds_read_b128 v[100:103], v28 offset:36864\n" \
"v_xor_b32_e32 v216, 3, v242\n" \
"v_mfma_f32_16x16x32_bf16 a[236:239], v[140:143], v[108:111], 0\n" \
"ds_read_b64_tr_b16 v[140:141], v35 offset:32768\n" \
"ds_read_b64_tr_b16 v[142:143], v35 offset:33792\n" \
"v_mfma_f32_16x16x32_bf16 a[236:239], v[144:147], v[112:115], a[236:239]\n" \
"s_add_u32 m0, %[mA], 0x1800\n" \
"s_waitcnt lgkmcnt(12)\n" \
"s_nop 0\n" \
"v_mfma_f32_16x16x32_bf16 a[16:19], v[56:59], v[152:155], a[16:19]\n" \
"s_add_u32 s85, s62, 0xc000\n" \
"ds_read_b64_tr_b16 v[144:145], v35 offset:40960\n" \
"v_mfma_f32_16x16x32_bf16 a[16:19], v[52:55], v[148:151], a[16:19]\n" \
"buffer_load_dwordx4 v22, s[64:67], s85 offen lds\n" \
"v_mfma_f32_16x16x32_bf16 a[48:51], v[60:63], v[148:151], a[48:51]\n" \
"s_add_u32 m0, %[mA], 0x1c00\n" \
"s_add_u32 s84, s62, 0xe000\n" \
"v_mfma_f32_16x16x32_bf16 a[48:51], v[64:67], v[152:155], a[48:51]\n" \
"ds_read_b64_tr_b16 v[146:147], v35 offset:41984\n" \
"ds_read_b128 v[108:111], v28 offset:38912\n" \
"v_mfma_f32_16x16x32_bf16 a[80:83], v[72:75], v[152:155], a[80:83]\n" \
"buffer_load_dwordx4 v23, s[64:67], s84 offen lds\n" \
"v_mfma_f32_16x16x32_bf16 a[80:83], v[68:71], v[148:151], a[80:83]\n" \
"s_add_u32 m0, %[mB], 0xc00\n" \
"s_add_u32 s85, s63, 0x18000\n" \
"v_mfma_f32_16x16x32_bf16 a[112:115], v[76:79], v[148:151], a[112:115]\n" \
"ds_read_b128 v[112:115], v29 offset:38912\n" \
"s_add_u32 s57, s37, s97\n" \
"v_mfma_f32_16x16x32_bf16 a[112:115], v[80:83], v[152:155], a[112:115]\n" \
"buffer_load_dwordx4 v27, s[68:71], s85 offen lds\n" \
"v_mfma_f32_16x16x32_bf16 a[20:23], v[56:59], v[160:163], a[20:23]\n" \
"s_add_u32 m0, %[mB], 0x4c00\n" \
"s_add_u32 s84, s63, 0x18000\n" \
"v_mfma_f32_16x16x32_bf16 a[20:23], v[52:55], v[156:159], a[20:23]\n" \
"s_lshl_b32 s99, s88, 2\n" \
"v_xor_b32_e64 v248, v219, v247\n" \
"v_mfma_f32_16x16x32_bf16 a[52:55], v[60:63], v[156:159], a[52:55]\n" \
"buffer_load_dwordx4 v27, s[72:75], s84 offen lds\n" \
"v_mfma_f32_16x16x32_bf16 a[52:55], v[64:67], v[160:163], a[52:55]\n" \
"v_xor_b32_e32 v221, v231, v217\n" \
"v_lshlrev_b32_e32 v234, 7, v235\n" \
"v_mfma_f32_16x16x32_bf16 a[84:87], v[72:75], v[160:163], a[84:87]\n" \
"v_lshlrev_b32_e32 v244, 5, v216\n" \
"v_and_b32_e32 v245, 2, v238\n" \
"v_mfma_f32_16x16x32_bf16 a[84:87], v[68:71], v[156:159], a[84:87]\n" \
"s_add_u32 s94, 0x2000, s57\n" \
"s_add_u32 s96, s99, %[w]\n" \
"v_mfma_f32_16x16x32_bf16 a[116:119], v[76:79], v[156:159], a[116:119]\n" \
"s_lshl_b32 s89, %[b], 2\n" \
"s_sub_u32 s95, 0x22000, s97\n" \
"v_mfma_f32_16x16x32_bf16 a[116:119], v[80:83], v[160:163], a[116:119]\n" \
"s_mov_b32 m0, %[mS]\n" \
"s_mov_b32 s86, s18\n" \
"v_mfma_f32_16x16x32_bf16 a[24:27], v[56:59], v[168:171], a[24:27]\n" \
"s_mov_b32 s87, s19\n" \
"v_lshlrev_b32_e32 v249, 4, v248\n" \
"v_mfma_f32_16x16x32_bf16 a[24:27], v[52:55], v[164:167], a[24:27]\n" \
"v_lshlrev_b32_e32 v251, 7, v228\n" \
"v_lshlrev_b32_e32 v223, 4, v221\n" \
"v_mfma_f32_16x16x32_bf16 a[56:59], v[60:63], v[164:167], a[56:59]\n" \
"v_add_u32_e32 v243, v234, v244\n" \
"v_lshlrev_b32_e32 v246, 1, v245\n" \
"v_mfma_f32_16x16x32_bf16 a[56:59], v[64:67], v[168:171], a[56:59]\n" \
"s_cmp_eq_u32 %[w], 0\n" \
"s_cselect_b32 s58, s94, 0x40000000\n" \
"v_mfma_f32_16x16x32_bf16 a[88:91], v[72:75], v[168:171], a[88:91]\n" \
"s_mul_i32 s101, 36, s96\n" \
"s_add_u32 s98, s89, %[w]\n" \
"v_mfma_f32_16x16x32_bf16 a[88:91], v[68:71], v[164:167], a[88:91]\n" \
"v_add_u32_e32 v250, v241, v249\n" \
"v_add_u32_e32 v222, v251, v223\n" \
"s_mov_b32 exec_lo, 0x3ffff\n" \
"s_mov_b32 exec_hi, 0x0\n" \
"global_load_lds_dword v16, s[86:87]\n" \
"s_mov_b64 exec, -1\n" \
"v_mfma_f32_16x16x32_bf16 a[120:123], v[76:79], v[164:167], a[120:123]\n" \
"v_add_u32_e32 v219, v243, v246\n" \
"v_add_u32_e32 v248, s101, v212\n" \
"v_mfma_f32_16x16x32_bf16 a[120:123], v[80:83], v[168:171], a[120:123]\n" \
"s_mul_i32 s97, 18, s98\n" \
"v_add_u32_e32 v196, s95, v250\n" \
"v_mfma_f32_16x16x32_bf16 a[28:31], v[56:59], v[176:179], a[28:31]\n" \
"v_add_u32_e32 v224, s95, v222\n" \
"v_add_u32_e32 v247, s95, v219\n" \
"v_mfma_f32_16x16x32_bf16 a[28:31], v[52:55], v[172:175], a[28:31]\n" \
"v_lshrrev_b32_e32 v241, 3, v248\n" \
"s_add_u32 s94, 0xffffffdc, s97\n" \
"v_mfma_f32_16x16x32_bf16 a[60:63], v[60:63], v[172:175], a[60:63]\n" \
"v_min_u32_e32 v213, 53, v212\n" \
"s_lshl_b32 s95, s100, 3\n" \
"v_mfma_f32_16x16x32_bf16 a[60:63], v[64:67], v[176:179], a[60:63]\n" \
"v_lshlrev_b32_e32 v249, 6, v241\n" \
"v_add_u32_e64 v215, s94, v213\n" \
"v_mfma_f32_16x16x32_bf16 a[92:95], v[72:75], v[176:179], a[92:95]\n" \
"v_add_u32_e32 v250, s95, v249\n" \
"s_lshr_b32 s94, %[b], 5\n" \
"v_mfma_f32_16x16x32_bf16 a[92:95], v[68:71], v[172:175], a[92:95]\n" \
"s_and_b32 s97, 7, %[b]\n" \
"s_lshr_b32 s99, %[b], 3\n" \
"v_mfma_f32_16x16x32_bf16 a[124:127], v[76:79], v[172:175], a[124:127]\n" \
"s_mul_i32 s95, 0xa00, s94\n" \
"s_lshl_b32 s98, s97, 2\n" \
"v_mfma_f32_16x16x32_bf16 a[124:127], v[80:83], v[176:179], a[124:127]\n" \
"s_and_b32 s100, 3, s99\n" \
"s_add_u32 s96, 0x5bc1000, s95\n" \
"v_mfma_f32_16x16x32_bf16 a[0:3], v[120:123], v[56:59], a[0:3]\n" \
"s_add_u32 s101, s98, s100\n" \
"s_lshl_b32 s88, s101, 2\n" \
"v_mfma_f32_16x16x32_bf16 a[0:3], v[116:119], v[52:55], a[0:3]\n" \
"s_lshl_b32 s95, s94, 3\n" \
"s_add_u32 s89, s96, s88\n" \
"v_mfma_f32_16x16x32_bf16 a[32:35], v[116:119], v[60:63], a[32:35]\n" \
"s_add_u32 s99, s95, s97\n" \
"v_writelane_b32 v11, s89, 0\n" \
"v_mfma_f32_16x16x32_bf16 a[32:35], v[120:123], v[64:67], a[32:35]\n" \
"s_lshl_b32 s88, s99, 10\n" \
"s_add_u32 s89, 0x5c00000, s88\n" \
"v_mfma_f32_16x16x32_bf16 a[64:67], v[120:123], v[72:75], a[64:67]\n" \
"s_lshl_b32 s95, %[w], 8\n" \
"s_add_u32 s99, s89, s95\n" \
"v_mfma_f32_16x16x32_bf16 a[64:67], v[116:119], v[68:71], a[64:67]\n" \
"v_add_u32_e32 v226, 0x24400, v20\n" \
"v_cmp_gt_u32_e32 vcc, 0x1b0, v225\n" \
"v_mfma_f32_16x16x32_bf16 a[96:99], v[116:119], v[76:79], a[96:99]\n" \
"s_or_b32 s88, 0x200, s99\n" \
"s_lshl_b32 s89, s100, 2\n" \
"v_mfma_f32_16x16x32_bf16 a[96:99], v[120:123], v[80:83], a[96:99]\n" \
"v_cndmask_b32_e32 v197, v226, v224, vcc\n" \
"v_writelane_b32 v11, s88, 1\n" \
"v_mfma_f32_16x16x32_bf16 a[4:7], v[128:131], v[56:59], a[4:7]\n" \
"s_add_u32 s95, s99, s89\n" \
"v_cmp_gt_u32_e32 vcc, 18, v212\n" \
"v_mfma_f32_16x16x32_bf16 a[4:7], v[124:127], v[52:55], a[4:7]\n" \
"v_writelane_b32 v11, s95, 2\n" \
"s_add_u32 s88, 0x80, s99\n" \
"v_mfma_f32_16x16x32_bf16 a[36:39], v[124:127], v[60:63], a[36:39]\n" \
"v_cndmask_b32_e32 v198, v226, v247, vcc\n" \
"v_and_b32_e32 v214, 7, v248\n" \
"v_mfma_f32_16x16x32_bf16 a[36:39], v[128:131], v[64:67], a[36:39]\n" \
"v_writelane_b32 v11, s88, 3\n" \
"s_lshl_b32 s99, s101, 8\n" \
"v_mfma_f32_16x16x32_bf16 a[68:71], v[128:131], v[72:75], a[68:71]\n" \
"v_add_u32_e32 v228, v250, v214\n" \
"v_cmp_gt_u32_e32 vcc, 36, v212\n" \
"v_mfma_f32_16x16x32_bf16 a[68:71], v[124:127], v[68:71], a[68:71]\n" \
"s_add_u32 s89, 0x80, s95\n" \
"s_add_u32 s39, 0x5be8000, s99\n" \
"v_mfma_f32_16x16x32_bf16 a[100:103], v[124:127], v[76:79], a[100:103]\n" \
"s_lshl_b32 s88, s94, 2\n" \
"v_cndmask_b32_e32 v218, v215, v228, vcc\n" \
"v_mfma_f32_16x16x32_bf16 a[100:103], v[128:131], v[80:83], a[100:103]\n" \
"s_add_u32 s95, s39, s88\n" \
"s_waitcnt lgkmcnt(0)\n" \
"v_mfma_f32_16x16x32_bf16 a[8:11], v[136:139], v[56:59], a[8:11]\n" \
"v_lshrrev_b32_e32 v212, 4, v20\n" \
"v_writelane_b32 v11, s89, 4\n" \
"v_mfma_f32_16x16x32_bf16 a[8:11], v[132:135], v[52:55], a[8:11]\n" \
"s_cmp_eq_u32 %[w], 3\n" \
"s_cselect_b32 s38, s95, 0x40000000\n" \
"v_mfma_f32_16x16x32_bf16 a[40:43], v[132:135], v[60:63], a[40:43]\n" \
"v_lshlrev_b32_e32 v213, 2, v212\n" \
"v_and_b32_e32 v215, 7, v212\n" \
"v_mfma_f32_16x16x32_bf16 a[40:43], v[136:139], v[64:67], a[40:43]\n" \
"s_add_u32 s101, s88, s100\n" \
"v_add_u32_e32 v214, s96, v213\n" \
"v_mfma_f32_16x16x32_bf16 a[72:75], v[136:139], v[72:75], a[72:75]\n" \
"s_add_u32 s89, 0x5be8020, s99\n" \
"v_lshlrev_b32_e32 v216, 2, v215\n" \
"v_mfma_f32_16x16x32_bf16 a[72:75], v[132:135], v[68:71], a[72:75]\n" \
"s_mul_i32 s94, 0x90000, s101\n" \
"v_add_u32_e32 v217, s89, v216\n" \
"v_mfma_f32_16x16x32_bf16 a[104:107], v[132:135], v[76:79], a[104:107]\n" \
"s_add_u32 s95, 0x4240000, s94\n" \
"s_lshl_b32 s96, %[w], 14\n" \
"v_mfma_f32_16x16x32_bf16 a[104:107], v[136:139], v[80:83], a[104:107]\n" \
"s_add_u32 s99, s95, s96\n" \
"s_lshl_b32 s89, s97, 16\n" \
"v_mfma_f32_16x16x32_bf16 a[12:15], v[144:147], v[56:59], a[12:15]\n" \
"s_add_u32 s62, s22, 0x180\n" \
"s_and_b32 s62, s62, 0x3ff\n" \
"v_mfma_f32_16x16x32_bf16 a[12:15], v[140:143], v[52:55], a[12:15]\n" \
"s_lshl_b32 s63, s62, 12\n" \
"v_writelane_b32 v11, s39, 52\n" \
"v_mfma_f32_16x16x32_bf16 a[44:47], v[140:143], v[60:63], a[44:47]\n" \
"s_add_u32 s88, s99, s89\n" \
"s_lshl_b32 s101, s97, 11\n" \
"v_mfma_f32_16x16x32_bf16 a[44:47], v[144:147], v[64:67], a[44:47]\n" \
"v_writelane_b32 v11, s88, 37\n" \
"s_add_u32 s100, 0x3800, s88\n" \
"v_mfma_f32_16x16x32_bf16 a[76:79], v[144:147], v[72:75], a[76:79]\n" \
"s_add_u32 s94, s99, s101\n" \
"v_writelane_b32 v11, s100, 51\n" \
"v_mfma_f32_16x16x32_bf16 a[76:79], v[140:143], v[68:71], a[76:79]\n" \
"s_add_u32 s95, 0x80000, s94\n" \
"s_lshl_b32 s96, %[w], 17\n" \
"v_mfma_f32_16x16x32_bf16 a[108:111], v[140:143], v[76:79], a[108:111]\n" \
"v_writelane_b32 v11, s95, 49\n" \
"s_add_u32 s89, s99, s96\n" \
"v_mfma_f32_16x16x32_bf16 a[108:111], v[144:147], v[80:83], a[108:111]\n" \
"s_and_b32 s94, 1, s97\n" \
"s_add_u32 s88, s89, s98\n" \
"s_waitcnt vmcnt(1)\n" \
"s_barrier\n" \
"v_mfma_f32_16x16x32_bf16 a[128:131], v[120:123], v[88:91], a[128:131]\n" \
"s_add_u32 m0, %[mA], 0x8000\n" \
"s_add_u32 s85, s62, 0x0\n" \
"v_mfma_f32_16x16x32_bf16 a[128:131], v[116:119], v[84:87], a[128:131]\n" \
"buffer_load_dwordx4 v22, s[64:67], s85 offen lds\n" \
"v_mfma_f32_16x16x32_bf16 a[160:163], v[116:119], v[92:95], a[160:163]\n" \
"ds_read_b128 v[56:59], v29 offset:16384\n" \
"ds_read_b128 v[52:55], v28 offset:16384\n" \
"v_mfma_f32_16x16x32_bf16 a[160:163], v[120:123], v[96:99], a[160:163]\n" \
"s_add_u32 m0, %[mA], 0x8400\n" \
"s_add_u32 s84, s62, 0x2000\n" \
"v_mfma_f32_16x16x32_bf16 a[192:195], v[120:123], v[104:107], a[192:195]\n" \
"buffer_load_dwordx4 v23, s[64:67], s84 offen lds\n" \
"v_mfma_f32_16x16x32_bf16 a[192:195], v[116:119], v[100:103], a[192:195]\n" \
"ds_read_b128 v[60:63], v28 offset:18432\n" \
"ds_read_b128 v[64:67], v29 offset:18432\n" \
"v_mfma_f32_16x16x32_bf16 a[224:227], v[116:119], v[108:111], a[224:227]\n" \
"ds_read_b64_tr_b16 v[116:117], v32 offset:0\n" \
"ds_read_b64_tr_b16 v[118:119], v32 offset:1024\n" \
"v_mfma_f32_16x16x32_bf16 a[224:227], v[120:123], v[112:115], a[224:227]\n" \
"s_add_u32 m0, %[mB], 0x8000\n" \
"s_add_u32 s85, s63, 0x0\n" \
"v_mfma_f32_16x16x32_bf16 a[132:135], v[128:131], v[88:91], a[132:135]\n" \
"buffer_load_dwordx4 v26, s[68:71], s85 offen lds\n" \
"v_mfma_f32_16x16x32_bf16 a[132:135], v[124:127], v[84:87], a[132:135]\n" \
"ds_read_b64_tr_b16 v[120:121], v32 offset:8192\n" \
"ds_read_b64_tr_b16 v[122:123], v32 offset:9216\n" \
"v_mfma_f32_16x16x32_bf16 a[164:167], v[124:127], v[92:95], a[164:167]\n" \
"ds_read_b128 v[72:75], v29 offset:20480\n" \
"ds_read_b128 v[68:71], v28 offset:20480\n" \
"v_mfma_f32_16x16x32_bf16 a[164:167], v[128:131], v[96:99], a[164:167]\n" \
"s_add_u32 m0, %[mB], 0xc000\n" \
"s_add_u32 s84, s63, 0x0\n" \
"v_mfma_f32_16x16x32_bf16 a[196:199], v[128:131], v[104:107], a[196:199]\n" \
"buffer_load_dwordx4 v26, s[72:75], s84 offen lds\n" \
"v_mfma_f32_16x16x32_bf16 a[196:199], v[124:127], v[100:103], a[196:199]\n" \
"ds_read_b128 v[76:79], v28 offset:22528\n" \
"ds_read_b128 v[80:83], v29 offset:22528\n" \
"v_mfma_f32_16x16x32_bf16 a[228:231], v[124:127], v[108:111], a[228:231]\n" \
"ds_read_b64_tr_b16 v[124:125], v33 offset:0\n" \
"ds_read_b64_tr_b16 v[126:127], v33 offset:1024\n" \
"v_mfma_f32_16x16x32_bf16 a[228:231], v[128:131], v[112:115], a[228:231]\n" \
"s_add_u32 m0, %[mA], 0x8800\n" \
"s_add_u32 s85, s62, 0x4000\n" \
"v_mfma_f32_16x16x32_bf16 a[136:139], v[136:139], v[88:91], a[136:139]\n" \
"buffer_load_dwordx4 v22, s[64:67], s85 offen lds\n" \
"v_mfma_f32_16x16x32_bf16 a[136:139], v[132:135], v[84:87], a[136:139]\n" \
"ds_read_b64_tr_b16 v[128:129], v33 offset:8192\n" \
"ds_read_b64_tr_b16 v[130:131], v33 offset:9216\n" \
"v_mfma_f32_16x16x32_bf16 a[168:171], v[132:135], v[92:95], a[168:171]\n" \
"s_lshl_b32 s95, s94, 10\n" \
"s_add_u32 s100, 0x3840, s88\n" \
"v_mfma_f32_16x16x32_bf16 a[168:171], v[136:139], v[96:99], a[168:171]\n" \
"s_add_u32 m0, %[mA], 0x8c00\n" \
"s_add_u32 s84, s62, 0x6000\n" \
"v_mfma_f32_16x16x32_bf16 a[200:203], v[136:139], v[104:107], a[200:203]\n" \
"buffer_load_dwordx4 v23, s[64:67], s84 offen lds\n" \
"v_mfma_f32_16x16x32_bf16 a[200:203], v[132:135], v[100:103], a[200:203]\n" \
"s_add_u32 s96, s99, s95\n" \
"v_writelane_b32 v11, s100, 5\n" \
"v_mfma_f32_16x16x32_bf16 a[232:235], v[132:135], v[108:111], a[232:235]\n" \
"ds_read_b64_tr_b16 v[132:133], v34 offset:0\n" \
"ds_read_b64_tr_b16 v[134:135], v34 offset:1024\n" \
"v_mfma_f32_16x16x32_bf16 a[232:235], v[136:139], v[112:115], a[232:235]\n" \
"s_add_u32 m0, %[mB], 0x8400\n" \
"s_add_u32 s85, s63, 0x8000\n" \
"v_mfma_f32_16x16x32_bf16 a[140:143], v[144:147], v[88:91], a[140:143]\n" \
"buffer_load_dwordx4 v26, s[68:71], s85 offen lds\n" \
"v_mfma_f32_16x16x32_bf16 a[140:143], v[140:143], v[84:87], a[140:143]\n" \
"ds_read_b64_tr_b16 v[136:137], v34 offset:8192\n" \
"ds_read_b64_tr_b16 v[138:139], v34 offset:9216\n" \
"v_mfma_f32_16x16x32_bf16 a[172:175], v[140:143], v[92:95], a[172:175]\n" \
"s_lshr_b32 s101, s97, 1\n" \
"s_add_u32 s89, 0x80000, s96\n" \
"v_mfma_f32_16x16x32_bf16 a[172:175], v[144:147], v[96:99], a[172:175]\n" \
"s_add_u32 m0, %[mB], 0xc400\n" \
"s_add_u32 s84, s63, 0x8000\n" \
"v_mfma_f32_16x16x32_bf16 a[204:207], v[144:147], v[104:107], a[204:207]\n" \
"buffer_load_dwordx4 v26, s[72:75], s84 offen lds\n" \
"v_mfma_f32_16x16x32_bf16 a[204:207], v[140:143], v[100:103], a[204:207]\n" \
"s_add_u32 s88, 1, s97\n" \
"s_and_b32 s100, 7, s88\n" \
"s_nop 0\n" \
"v_mfma_f32_16x16x32_bf16 a[236:239], v[140:143], v[108:111], a[236:239]\n" \
"ds_read_b64_tr_b16 v[140:141], v35 offset:0\n" \
"ds_read_b64_tr_b16 v[142:143], v35 offset:1024\n" \
"v_mfma_f32_16x16x32_bf16 a[236:239], v[144:147], v[112:115], a[236:239]\n" \
"s_add_u32 m0, %[mA], 0x9000\n" \
"s_add_u32 s85, s62, 0x8000\n" \
"v_mfma_f32_16x16x32_bf16 a[144:147], v[88:91], v[152:155], a[144:147]\n" \
"buffer_load_dwordx4 v22, s[64:67], s85 offen lds\n" \
"v_mfma_f32_16x16x32_bf16 a[144:147], v[84:87], v[148:151], a[144:147]\n" \
"ds_read_b64_tr_b16 v[144:145], v35 offset:8192\n" \
"ds_read_b64_tr_b16 v[146:147], v35 offset:9216\n" \
"v_mfma_f32_16x16x32_bf16 a[176:179], v[92:95], v[148:151], a[176:179]\n" \
"s_cmp_eq_u32 %[w], s101\n" \
"s_cselect_b32 s98, s89, 0x40000000\n" \
"v_mfma_f32_16x16x32_bf16 a[176:179], v[96:99], v[152:155], a[176:179]\n" \
"s_add_u32 m0, %[mA], 0x9400\n" \
"s_add_u32 s84, s62, 0xa000\n" \
"v_mfma_f32_16x16x32_bf16 a[208:211], v[104:107], v[152:155], a[208:211]\n" \
"buffer_load_dwordx4 v23, s[64:67], s84 offen lds\n" \
"v_mfma_f32_16x16x32_bf16 a[208:211], v[100:103], v[148:151], a[208:211]\n" \
"s_lshl_b32 s94, s100, 16\n" \
"s_add_u32 s96, 2, s97\n" \
"v_mfma_f32_16x16x32_bf16 a[240:243], v[108:111], v[148:151], a[240:243]\n" \
"ds_read_b64_tr_b16 v[148:149], v32 offset:16384\n" \
"ds_read_b64_tr_b16 v[150:151], v32 offset:17408\n" \
"v_mfma_f32_16x16x32_bf16 a[240:243], v[112:115], v[152:155], a[240:243]\n" \
"s_add_u32 m0, %[mB], 0x8800\n" \
"s_add_u32 s85, s63, 0x10000\n" \
"v_mfma_f32_16x16x32_bf16 a[148:151], v[88:91], v[160:163], a[148:151]\n" \
"buffer_load_dwordx4 v27, s[68:71], s85 offen lds\n" \
"v_mfma_f32_16x16x32_bf16 a[148:151], v[84:87], v[156:159], a[148:151]\n" \
"ds_read_b64_tr_b16 v[152:153], v32 offset:24576\n" \
"ds_read_b64_tr_b16 v[154:155], v32 offset:25600\n" \
"v_mfma_f32_16x16x32_bf16 a[180:183], v[92:95], v[156:159], a[180:183]\n" \
"v_writelane_b32 v11, s98, 38\n" \
"s_add_u32 s95, s99, s94\n" \
"v_mfma_f32_16x16x32_bf16 a[180:183], v[96:99], v[160:163], a[180:183]\n" \
"s_add_u32 m0, %[mB], 0xc800\n" \
"s_add_u32 s84, s63, 0x10000\n" \
"v_mfma_f32_16x16x32_bf16 a[212:215], v[104:107], v[160:163], a[212:215]\n" \
"buffer_load_dwordx4 v27, s[72:75], s84 offen lds\n" \
"v_mfma_f32_16x16x32_bf16 a[212:215], v[100:103], v[156:159], a[212:215]\n" \
"s_and_b32 s101, 7, s96\n" \
"s_add_u32 s100, 3, s97\n" \
"v_mfma_f32_16x16x32_bf16 a[244:247], v[108:111], v[156:159], a[244:247]\n" \
"ds_read_b64_tr_b16 v[156:157], v33 offset:16384\n" \
"ds_read_b64_tr_b16 v[158:159], v33 offset:17408\n" \
"v_mfma_f32_16x16x32_bf16 a[244:247], v[112:115], v[160:163], a[244:247]\n" \
"ds_read_b64_tr_b16 v[160:161], v33 offset:24576\n" \
"ds_read_b64_tr_b16 v[162:163], v33 offset:25600\n" \
"v_mfma_f32_16x16x32_bf16 a[152:155], v[88:91], v[168:171], a[152:155]\n" \
"v_writelane_b32 v11, s95, 42\n" \
"s_lshl_b32 s89, s101, 16\n" \
"v_mfma_f32_16x16x32_bf16 a[152:155], v[84:87], v[164:167], a[152:155]\n" \
"s_and_b32 s94, 7, s100\n" \
"s_add_u32 s98, s99, s89\n" \
"v_mfma_f32_16x16x32_bf16 a[184:187], v[92:95], v[164:167], a[184:187]\n" \
"s_lshl_b32 s95, s94, 16\n" \
"s_add_u32 s88, 0x800, s98\n" \
"v_mfma_f32_16x16x32_bf16 a[184:187], v[96:99], v[168:171], a[184:187]\n" \
"s_add_u32 s96, s99, s95\n" \
"s_add_u32 s89, 4, s97\n" \
"v_mfma_f32_16x16x32_bf16 a[216:219], v[104:107], v[168:171], a[216:219]\n" \
"v_writelane_b32 v11, s88, 43\n" \
"s_add_u32 s101, 0x1000, s96\n" \
"v_mfma_f32_16x16x32_bf16 a[216:219], v[100:103], v[164:167], a[216:219]\n" \
"s_and_b32 s98, 7, s89\n" \
"s_add_u32 s95, 5, s97\n" \
"s_nop 0\n" \
"v_mfma_f32_16x16x32_bf16 a[248:251], v[108:111], v[164:167], a[248:251]\n" \
"ds_read_b64_tr_b16 v[164:165], v34 offset:16384\n" \
"ds_read_b64_tr_b16 v[166:167], v34 offset:17408\n" \
"v_mfma_f32_16x16x32_bf16 a[248:251], v[112:115], v[168:171], a[248:251]\n" \
"ds_read_b64_tr_b16 v[168:169], v34 offset:24576\n" \
"ds_read_b64_tr_b16 v[170:171], v34 offset:25600\n" \
"v_mfma_f32_16x16x32_bf16 a[156:159], v[88:91], v[176:179], a[156:159]\n" \
"ds_read_b128 v[88:91], v29 offset:0\n" \
"v_writelane_b32 v11, s101, 44\n" \
"v_mfma_f32_16x16x32_bf16 a[156:159], v[84:87], v[172:175], a[156:159]\n" \
"ds_read_b128 v[84:87], v28 offset:0\n" \
"s_lshl_b32 s88, s98, 16\n" \
"v_mfma_f32_16x16x32_bf16 a[188:191], v[92:95], v[172:175], a[188:191]\n" \
"ds_read_b128 v[92:95], v28 offset:2048\n" \
"s_and_b32 s96, 7, s95\n" \
"v_mfma_f32_16x16x32_bf16 a[188:191], v[96:99], v[176:179], a[188:191]\n" \
"ds_read_b128 v[96:99], v29 offset:2048\n" \
"s_add_u32 s100, s99, s88\n" \
"v_mfma_f32_16x16x32_bf16 a[220:223], v[104:107], v[176:179], a[220:223]\n" \
"ds_read_b128 v[104:107], v29 offset:4096\n" \
"s_lshl_b32 s101, s96, 16\n" \
"v_mfma_f32_16x16x32_bf16 a[220:223], v[100:103], v[172:175], a[220:223]\n" \
"ds_read_b128 v[100:103], v28 offset:4096\n" \
"s_add_u32 s94, 0x1800, s100\n" \
"v_mfma_f32_16x16x32_bf16 a[252:255], v[108:111], v[172:175], a[252:255]\n" \
"ds_read_b64_tr_b16 v[172:173], v35 offset:16384\n" \
"ds_read_b64_tr_b16 v[174:175], v35 offset:17408\n" \
"v_mfma_f32_16x16x32_bf16 a[252:255], v[112:115], v[176:179], a[252:255]\n" \
"s_add_u32 m0, %[mA], 0x9800\n" \
"s_waitcnt lgkmcnt(12)\n" \
"s_nop 0\n" \
"v_mfma_f32_16x16x32_bf16 a[0:3], v[120:123], v[56:59], a[0:3]\n" \
"s_add_u32 s85, s62, 0xc000\n" \
"ds_read_b64_tr_b16 v[176:177], v35 offset:24576\n" \
"v_mfma_f32_16x16x32_bf16 a[0:3], v[116:119], v[52:55], a[0:3]\n" \
"buffer_load_dwordx4 v22, s[64:67], s85 offen lds\n" \
"v_mfma_f32_16x16x32_bf16 a[32:35], v[116:119], v[60:63], a[32:35]\n" \
"s_add_u32 m0, %[mA], 0x9c00\n" \
"s_add_u32 s84, s62, 0xe000\n" \
"v_mfma_f32_16x16x32_bf16 a[32:35], v[120:123], v[64:67], a[32:35]\n" \
"ds_read_b64_tr_b16 v[178:179], v35 offset:25600\n" \
"ds_read_b128 v[108:111], v28 offset:6144\n" \
"v_mfma_f32_16x16x32_bf16 a[64:67], v[120:123], v[72:75], a[64:67]\n" \
"buffer_load_dwordx4 v23, s[64:67], s84 offen lds\n" \
"v_mfma_f32_16x16x32_bf16 a[64:67], v[116:119], v[68:71], a[64:67]\n" \
"s_add_u32 m0, %[mB], 0x8c00\n" \
"s_add_u32 s85, s63, 0x18000\n" \
"v_mfma_f32_16x16x32_bf16 a[96:99], v[116:119], v[76:79], a[96:99]\n" \
"ds_read_b128 v[112:115], v29 offset:6144\n" \
"s_add_u32 s89, s99, s101\n" \
"v_mfma_f32_16x16x32_bf16 a[96:99], v[120:123], v[80:83], a[96:99]\n" \
"buffer_load_dwordx4 v27, s[68:71], s85 offen lds\n" \
"v_mfma_f32_16x16x32_bf16 a[4:7], v[128:131], v[56:59], a[4:7]\n" \
"s_add_u32 m0, %[mB], 0xcc00\n" \
"s_add_u32 s84, s63, 0x18000\n" \
"v_mfma_f32_16x16x32_bf16 a[4:7], v[124:127], v[52:55], a[4:7]\n" \
"s_add_u32 s88, 6, s97\n" \
"v_writelane_b32 v11, s94, 45\n" \
"v_mfma_f32_16x16x32_bf16 a[36:39], v[124:127], v[60:63], a[36:39]\n" \
"buffer_load_dwordx4 v27, s[72:75], s84 offen lds\n" \
"v_mfma_f32_16x16x32_bf16 a[36:39], v[128:131], v[64:67], a[36:39]\n" \
"s_add_u32 s98, 0x2000, s89\n" \
"s_and_b32 s100, 7, s88\n" \
"v_mfma_f32_16x16x32_bf16 a[68:71], v[128:131], v[72:75], a[68:71]\n" \
"s_add_u32 s101, 7, s97\n" \
"v_writelane_b32 v11, s98, 46\n" \
"v_mfma_f32_16x16x32_bf16 a[68:71], v[124:127], v[68:71], a[68:71]\n" \
"buffer_load_dwordx4 v[200:203], v192, s[76:79], s56 offen offset:0\n" \
"v_mfma_f32_16x16x32_bf16 a[100:103], v[124:127], v[76:79], a[100:103]\n" \
"s_lshl_b32 s94, s100, 16\n" \
"s_and_b32 s89, 7, s101\n" \
"v_mfma_f32_16x16x32_bf16 a[100:103], v[128:131], v[80:83], a[100:103]\n" \
"s_add_u32 s95, s99, s94\n" \
"s_lshl_b32 s98, s89, 16\n" \
"v_mfma_f32_16x16x32_bf16 a[8:11], v[136:139], v[56:59], a[8:11]\n" \
"s_add_u32 s96, 0x2800, s95\n" \
"s_add_u32 s88, s99, s98\n" \
"v_mfma_f32_16x16x32_bf16 a[8:11], v[132:135], v[52:55], a[8:11]\n" \
"v_writelane_b32 v11, s96, 47\n" \
"s_add_u32 s100, 0x3000, s88\n" \
"v_mfma_f32_16x16x32_bf16 a[40:43], v[132:135], v[60:63], a[40:43]\n" \
"buffer_load_dwordx4 v[204:207], v193, s[76:79], s56 offen offset:0\n" \
"v_mfma_f32_16x16x32_bf16 a[40:43], v[136:139], v[64:67], a[40:43]\n" \
"v_writelane_b32 v11, s100, 48\n" \
"s_mov_b32 s87, 0x5040100\n" \
"v_mfma_f32_16x16x32_bf16 a[72:75], v[136:139], v[72:75], a[72:75]\n" \
"v_lshlrev_b32_e32 v188, 7, v218\n" \
"v_readfirstlane_b32 %[tag], v180\n" \
"v_mfma_f32_16x16x32_bf16 a[72:75], v[132:135], v[68:71], a[72:75]\n" \
"s_add_u32 %[tag], %[tag], 0x9E3779B1\n" \
"v_mov_b32_e32 v45, %[tag]\n" \
"v_mfma_f32_16x16x32_bf16 a[104:107], v[132:135], v[76:79], a[104:107]\n" \
"s_lshr_b32 s94, %[b], 5\n" \
"s_sub_u32 s95, 15, s94\n" \
"v_mfma_f32_16x16x32_bf16 a[104:107], v[136:139], v[80:83], a[104:107]\n" \
"s_cmp_lt_u32 %[w], 2\n" \
"s_cselect_b32 s96, s95, s94\n" \
"v_mfma_f32_16x16x32_bf16 a[12:15], v[144:147], v[56:59], a[12:15]\n" \
"buffer_load_dword v208, v194, s[76:79], s56 offen offset:32\n" \
"v_mfma_f32_16x16x32_bf16 a[12:15], v[140:143], v[52:55], a[12:15]\n" \
"s_lshl_b32 s97, s96, 7\n" \
"s_add_u32 s98, 0xfffffff0, s97\n" \
"v_mfma_f32_16x16x32_bf16 a[44:47], v[140:143], v[60:63], a[44:47]\n" \
"s_cmp_eq_u32 s97, 0\n" \
"s_cselect_b32 s99, 0, s98\n" \
"v_mfma_f32_16x16x32_bf16 a[44:47], v[144:147], v[64:67], a[44:47]\n" \
"s_and_b32 s100, 1, %[w]\n" \
"v_cmp_gt_u32_e32 vcc, 32, v212\n" \
"v_mfma_f32_16x16x32_bf16 a[76:79], v[144:147], v[72:75], a[76:79]\n" \
"s_lshl_b32 s101, s100, 3\n" \
"s_add_u32 s88, s99, s101\n" \
"v_mfma_f32_16x16x32_bf16 a[76:79], v[140:143], v[68:71], a[76:79]\n" \
"v_lshrrev_b32_e32 v212, 4, v20\n" \
"v_cndmask_b32_e32 v17, v217, v214, vcc\n" \
"v_mfma_f32_16x16x32_bf16 a[108:111], v[140:143], v[76:79], a[108:111]\n" \
"buffer_load_dword v209, v194, s[76:79], s56 offen offset:1184\n" \
"v_mfma_f32_16x16x32_bf16 a[108:111], v[144:147], v[80:83], a[108:111]\n" \
"v_lshrrev_b32_e32 v213, 3, v212\n" \
"s_lshr_b32 s99, %[b], 3\n" \
"v_mfma_f32_16x16x32_bf16 a[16:19], v[56:59], v[152:155], a[16:19]\n" \
"v_add_u32_e32 v214, s88, v213\n" \
"s_lshl_b32 s96, s100, 6\n" \
"v_mfma_f32_16x16x32_bf16 a[16:19], v[52:55], v[148:151], a[16:19]\n" \
"s_and_b32 s101, 3, s99\n" \
"s_add_u32 s98, s97, s96\n" \
"v_mfma_f32_16x16x32_bf16 a[48:51], v[60:63], v[148:151], a[48:51]\n" \
"s_add_u32 s88, s101, s94\n" \
"v_add_u32_e32 v221, s98, v213\n" \
"v_mfma_f32_16x16x32_bf16 a[48:51], v[64:67], v[152:155], a[48:51]\n" \
"s_add_u32 s96, 0xfffffff7, s88\n" \
"s_lshl_b32 s89, s100, 2\n" \
"v_mfma_f32_16x16x32_bf16 a[80:83], v[72:75], v[152:155], a[80:83]\n" \
"buffer_load_dword v211, v194, s[76:79], s56 offen offset:2336\n" \
"v_mfma_f32_16x16x32_bf16 a[80:83], v[68:71], v[148:151], a[80:83]\n" \
"s_mov_b32 s76, s14\n" \
"s_and_b32 s77, s15, 0xffff\n" \
"v_mfma_f32_16x16x32_bf16 a[112:115], v[76:79], v[148:151], a[112:115]\n" \
"s_mov_b32 s78, 0x480\n" \
"s_add_u32 s56, s57, 0x1000\n" \
"v_mfma_f32_16x16x32_bf16 a[112:115], v[80:83], v[152:155], a[112:115]\n" \
"s_cmp_lt_u32 s88, 9\n" \
"s_cselect_b32 s98, s88, s96\n" \
"v_mfma_f32_16x16x32_bf16 a[20:23], v[56:59], v[160:163], a[20:23]\n" \
"s_add_u32 s88, s97, s100\n" \
"s_lshl_b32 s23, s98, 7\n" \
"v_mfma_f32_16x16x32_bf16 a[20:23], v[52:55], v[156:159], a[20:23]\n" \
"s_cmp_eq_u32 s88, 0\n" \
"s_cselect_b32 s49, 0xff800000, 0\n" \
"v_mfma_f32_16x16x32_bf16 a[52:55], v[60:63], v[156:159], a[52:55]\n" \
"buffer_load_ushort v210, v195, s[76:79], 0 offen\n" \
"v_mfma_f32_16x16x32_bf16 a[52:55], v[64:67], v[160:163], a[52:55]\n" \
"s_lshr_b32 s96, s97, 4\n" \
"v_lshrrev_b32_e32 v217, 4, v212\n" \
"v_mfma_f32_16x16x32_bf16 a[84:87], v[72:75], v[160:163], a[84:87]\n" \
"s_add_u32 s98, s96, s89\n" \
"v_add_u32_e32 v218, s89, v217\n" \
"v_mfma_f32_16x16x32_bf16 a[84:87], v[68:71], v[156:159], a[84:87]\n" \
"s_mul_i32 s88, 0x24800, s98\n" \
"s_and_b32 s96, 7, %[b]\n" \
"v_mfma_f32_16x16x32_bf16 a[116:119], v[76:79], v[156:159], a[116:119]\n" \
"s_add_u32 s97, 0x3000000, s88\n" \
"s_lshl_b32 s89, s96, 8\n" \
"v_mfma_f32_16x16x32_bf16 a[116:119], v[80:83], v[160:163], a[116:119]\n" \
"s_add_u32 s98, s97, s89\n" \
"s_waitcnt lgkmcnt(0)\n" \
"v_mfma_f32_16x16x32_bf16 a[24:27], v[56:59], v[168:171], a[24:27]\n" \
"s_lshl_b32 s88, s101, 2\n" \
"v_and_b32_e32 v216, 7, v212\n" \
"v_mfma_f32_16x16x32_bf16 a[24:27], v[52:55], v[164:167], a[24:27]\n" \
"s_add_u32 s89, s98, s88\n" \
"v_xor_b32_e32 v219, v216, v218\n" \
"v_mfma_f32_16x16x32_bf16 a[56:59], v[60:63], v[164:167], a[56:59]\n" \
"s_lshl_b32 s98, s96, 2\n" \
"v_mul_u32_u24_e32 v215, 0x480, v214\n" \
"v_mfma_f32_16x16x32_bf16 a[56:59], v[64:67], v[168:171], a[56:59]\n" \
"v_lshlrev_b32_e32 v220, 4, v219\n" \
"v_lshrrev_b32_e32 v229, 2, v212\n" \
"v_mfma_f32_16x16x32_bf16 a[88:91], v[72:75], v[168:171], a[88:91]\n" \
"v_and_b32_e32 v234, 3, v212\n" \
"v_and_b32_e64 v249, 2, v212\n" \
"v_mfma_f32_16x16x32_bf16 a[88:91], v[68:71], v[164:167], a[88:91]\n" \
"s_add_u32 s51, 0x4000, s89\n" \
"s_add_u32 s88, s98, s101\n" \
"v_mfma_f32_16x16x32_bf16 a[120:123], v[76:79], v[164:167], a[120:123]\n" \
"v_add_u32_e32 v21, v215, v220\n" \
"v_add_u32_e64 v225, 4, v217\n" \
"v_mfma_f32_16x16x32_bf16 a[120:123], v[80:83], v[168:171], a[120:123]\n" \
"s_lshl_b32 s89, s88, 9\n" \
"s_lshl_b32 s98, s99, 2\n" \
"v_mfma_f32_16x16x32_bf16 a[28:31], v[56:59], v[176:179], a[28:31]\n" \
"s_add_u32 s62, s22, 0x200\n" \
"s_and_b32 s62, s62, 0x3ff\n" \
"v_mfma_f32_16x16x32_bf16 a[28:31], v[52:55], v[172:175], a[28:31]\n" \
"s_lshl_b32 s63, s62, 12\n" \
"v_lshrrev_b32_e32 v212, 4, v20\n" \
"v_mfma_f32_16x16x32_bf16 a[60:63], v[60:63], v[172:175], a[60:63]\n" \
"v_xor_b32_e32 v223, v216, v217\n" \
"v_xor_b32_e32 v226, v216, v225\n" \
"v_mfma_f32_16x16x32_bf16 a[60:63], v[64:67], v[176:179], a[60:63]\n" \
"v_mov_b32_e32 v250, 0xeeeeeeee\n" \
"v_mov_b32_e32 v251, 0x44444444\n" \
"v_mfma_f32_16x16x32_bf16 a[92:95], v[72:75], v[176:179], a[92:95]\n" \
"v_cmp_ne_u32_e32 vcc, 0, v249\n" \
"s_add_u32 s50, s97, s89\n" \
"v_mfma_f32_16x16x32_bf16 a[92:95], v[68:71], v[172:175], a[92:95]\n" \
"s_lshl_b32 s96, %[w], 10\n" \
"s_add_u32 s101, s98, %[w]\n" \
"v_mfma_f32_16x16x32_bf16 a[124:127], v[76:79], v[172:175], a[124:127]\n" \
"v_and_b32_e32 v215, 15, v212\n" \
"v_cndmask_b32_e32 v50, v251, v250, vcc\n" \
"v_mfma_f32_16x16x32_bf16 a[124:127], v[80:83], v[176:179], a[124:127]\n" \
"s_add_u32 s40, 0x14000, s96\n" \
"s_lshl_b32 s41, s101, 7\n" \
"s_waitcnt vmcnt(6)\n" \
"L_pf1:\n" \
"s_cbranch_execz L_pf5\n" \
"s_nop 0\n" \
"s_barrier\n" \
"v_mfma_f32_16x16x32_bf16 a[144:147], v[88:91], v[152:155], a[144:147]\n" \
"s_add_u32 m0, %[mA], 0x0\n" \
"s_add_u32 s85, s62, 0x0\n" \
"s_nop 0\n" \
"v_mfma_f32_16x16x32_bf16 a[144:147], v[84:87], v[148:151], a[144:147]\n" \
"buffer_load_dwordx4 v22, s[64:67], s85 offen lds\n" \
"v_mfma_f32_16x16x32_bf16 a[176:179], v[92:95], v[148:151], a[176:179]\n" \
"ds_read_b128 v[56:59], v29 offset:49152\n" \
"ds_read_b128 v[52:55], v28 offset:49152\n" \
"v_mfma_f32_16x16x32_bf16 a[176:179], v[96:99], v[152:155], a[176:179]\n" \
"s_add_u32 m0, %[mA], 0x400\n" \
"s_add_u32 s84, s62, 0x2000\n" \
"v_mfma_f32_16x16x32_bf16 a[208:211], v[104:107], v[152:155], a[208:211]\n" \
"buffer_load_dwordx4 v23, s[64:67], s84 offen lds\n" \
"v_mfma_f32_16x16x32_bf16 a[208:211], v[100:103], v[148:151], a[208:211]\n" \
"ds_read_b128 v[60:63], v28 offset:51200\n" \
"ds_read_b128 v[64:67], v29 offset:51200\n" \
"v_mfma_f32_16x16x32_bf16 a[240:243], v[108:111], v[148:151], a[240:243]\n" \
"ds_read_b64_tr_b16 v[148:149], v32 offset:49152\n" \
"ds_read_b64_tr_b16 v[150:151], v32 offset:50176\n" \
"v_mfma_f32_16x16x32_bf16 a[240:243], v[112:115], v[152:155], a[240:243]\n" \
"s_add_u32 m0, %[mB], 0x0\n" \
"s_add_u32 s85, s63, 0x0\n" \
"v_mfma_f32_16x16x32_bf16 a[148:151], v[88:91], v[160:163], a[148:151]\n" \
"buffer_load_dwordx4 v26, s[68:71], s85 offen lds\n" \
"v_mfma_f32_16x16x32_bf16 a[148:151], v[84:87], v[156:159], a[148:151]\n" \
"ds_read_b64_tr_b16 v[152:153], v32 offset:57344\n" \
"ds_read_b64_tr_b16 v[154:155], v32 offset:58368\n" \
"v_mfma_f32_16x16x32_bf16 a[180:183], v[92:95], v[156:159], a[180:183]\n" \
"ds_read_b128 v[72:75], v29 offset:53248\n" \
"ds_read_b128 v[68:71], v28 offset:53248\n" \
"v_mfma_f32_16x16x32_bf16 a[180:183], v[96:99], v[160:163], a[180:183]\n" \
"s_add_u32 m0, %[mB], 0x4000\n" \
"s_add_u32 s84, s63, 0x0\n" \
"s_nop 0\n" \
"v_mfma_f32_16x16x32_bf16 a[212:215], v[104:107], v[160:163], a[212:215]\n" \
"buffer_load_dwordx4 v26, s[72:75], s84 offen lds\n" \
"v_mfma_f32_16x16x32_bf16 a[212:215], v[100:103], v[156:159], a[212:215]\n" \
"ds_read_b128 v[76:79], v28 offset:55296\n" \
"ds_read_b128 v[80:83], v29 offset:55296\n" \
"v_mfma_f32_16x16x32_bf16 a[244:247], v[108:111], v[156:159], a[244:247]\n" \
"ds_read_b64_tr_b16 v[156:157], v33 offset:49152\n" \
"ds_read_b64_tr_b16 v[158:159], v33 offset:50176\n" \
"v_mfma_f32_16x16x32_bf16 a[244:247], v[112:115], v[160:163], a[244:247]\n" \
"s_add_u32 m0, %[mA], 0x800\n" \
"s_add_u32 s85, s62, 0x4000\n" \
"v_mfma_f32_16x16x32_bf16 a[152:155], v[88:91], v[168:171], a[152:155]\n" \
"buffer_load_dwordx4 v22, s[64:67], s85 offen lds\n" \
"v_mfma_f32_16x16x32_bf16 a[152:155], v[84:87], v[164:167], a[152:155]\n" \
"ds_read_b64_tr_b16 v[160:161], v33 offset:57344\n" \
"ds_read_b64_tr_b16 v[162:163], v33 offset:58368\n" \
"v_mfma_f32_16x16x32_bf16 a[184:187], v[92:95], v[164:167], a[184:187]\n" \
"s_lshl_b32 s97, s88, 20\n" \
"v_lshrrev_b32_e32 v213, 4, v212\n" \
"v_mfma_f32_16x16x32_bf16 a[184:187], v[96:99], v[168:171], a[184:187]\n" \
"s_add_u32 m0, %[mA], 0xc00\n" \
"s_add_u32 s84, s62, 0x6000\n" \
"v_mfma_f32_16x16x32_bf16 a[216:219], v[104:107], v[168:171], a[216:219]\n" \
"buffer_load_dwordx4 v23, s[64:67], s84 offen lds\n" \
"v_mfma_f32_16x16x32_bf16 a[216:219], v[100:103], v[164:167], a[216:219]\n" \
"v_and_b32_e32 v216, 1, v215\n" \
"v_lshlrev_b32_e32 v228, 3, v217\n" \
"v_mfma_f32_16x16x32_bf16 a[248:251], v[108:111], v[164:167], a[248:251]\n" \
"ds_read_b64_tr_b16 v[164:165], v34 offset:49152\n" \
"ds_read_b64_tr_b16 v[166:167], v34 offset:50176\n" \
"v_mfma_f32_16x16x32_bf16 a[248:251], v[112:115], v[168:171], a[248:251]\n" \
"s_add_u32 m0, %[mB], 0x400\n" \
"s_add_u32 s85, s63, 0x8000\n" \
"v_mfma_f32_16x16x32_bf16 a[156:159], v[88:91], v[176:179], a[156:159]\n" \
"buffer_load_dwordx4 v26, s[68:71], s85 offen lds\n" \
"v_mfma_f32_16x16x32_bf16 a[156:159], v[84:87], v[172:175], a[156:159]\n" \
"ds_read_b64_tr_b16 v[168:169], v34 offset:57344\n" \
"ds_read_b64_tr_b16 v[170:171], v34 offset:58368\n" \
"v_mfma_f32_16x16x32_bf16 a[188:191], v[92:95], v[172:175], a[188:191]\n" \
"v_and_b32_e32 v238, 1, v217\n" \
"v_lshlrev_b32_e32 v242, 4, v217\n" \
"v_mfma_f32_16x16x32_bf16 a[188:191], v[96:99], v[176:179], a[188:191]\n" \
"s_add_u32 m0, %[mB], 0x4400\n" \
"s_add_u32 s84, s63, 0x8000\n" \
"v_mfma_f32_16x16x32_bf16 a[220:223], v[104:107], v[176:179], a[220:223]\n" \
"buffer_load_dwordx4 v26, s[72:75], s84 offen lds\n" \
"v_mfma_f32_16x16x32_bf16 a[220:223], v[100:103], v[172:175], a[220:223]\n" \
"s_add_u32 s89, 0x1000000, s97\n" \
"s_lshl_b32 s96, %[w], 12\n" \
"s_nop 0\n" \
"v_mfma_f32_16x16x32_bf16 a[252:255], v[108:111], v[172:175], a[252:255]\n" \
"ds_read_b64_tr_b16 v[172:173], v35 offset:49152\n" \
"ds_read_b64_tr_b16 v[174:175], v35 offset:50176\n" \
"v_mfma_f32_16x16x32_bf16 a[252:255], v[112:115], v[176:179], a[252:255]\n" \
"s_add_u32 m0, %[mA], 0x1000\n" \
"s_add_u32 s85, s62, 0x8000\n" \
"v_mfma_f32_16x16x32_bf16 a[128:131], v[120:123], v[88:91], a[128:131]\n" \
"buffer_load_dwordx4 v22, s[64:67], s85 offen lds\n" \
"v_mfma_f32_16x16x32_bf16 a[128:131], v[116:119], v[84:87], a[128:131]\n" \
"ds_read_b64_tr_b16 v[176:177], v35 offset:57344\n" \
"ds_read_b64_tr_b16 v[178:179], v35 offset:58368\n" \
"v_mfma_f32_16x16x32_bf16 a[160:163], v[116:119], v[92:95], a[160:163]\n" \
"s_lshr_b32 s101, %[w], 1\n" \
"v_lshlrev_b32_e32 v214, 2, v213\n" \
"v_mfma_f32_16x16x32_bf16 a[160:163], v[120:123], v[96:99], a[160:163]\n" \
"s_add_u32 m0, %[mA], 0x1400\n" \
"s_add_u32 s84, s62, 0xa000\n" \
"v_mfma_f32_16x16x32_bf16 a[192:195], v[120:123], v[104:107], a[192:195]\n" \
"buffer_load_dwordx4 v23, s[64:67], s84 offen lds\n" \
"v_mfma_f32_16x16x32_bf16 a[192:195], v[116:119], v[100:103], a[192:195]\n" \
"v_mov_b32_e32 v218, 0x1000c0c\n" \
"v_mov_b32_e32 v219, 0xc0c0100\n" \
"v_mfma_f32_16x16x32_bf16 a[224:227], v[116:119], v[108:111], a[224:227]\n" \
"ds_read_b64_tr_b16 v[116:117], v32 offset:32768\n" \
"ds_read_b64_tr_b16 v[118:119], v32 offset:33792\n" \
"v_mfma_f32_16x16x32_bf16 a[224:227], v[120:123], v[112:115], a[224:227]\n" \
"s_add_u32 m0, %[mB], 0x800\n" \
"s_add_u32 s85, s63, 0x10000\n" \
"v_mfma_f32_16x16x32_bf16 a[132:135], v[128:131], v[88:91], a[132:135]\n" \
"buffer_load_dwordx4 v27, s[68:71], s85 offen lds\n" \
"v_mfma_f32_16x16x32_bf16 a[132:135], v[124:127], v[84:87], a[132:135]\n" \
"ds_read_b64_tr_b16 v[120:121], v32 offset:40960\n" \
"ds_read_b64_tr_b16 v[122:123], v32 offset:41984\n" \
"v_mfma_f32_16x16x32_bf16 a[164:167], v[124:127], v[92:95], a[164:167]\n" \
"v_cmp_ne_u32_e32 vcc, 0, v216\n" \
"v_mul_u32_u24_e32 v222, 0x480, v221\n" \
"v_mfma_f32_16x16x32_bf16 a[164:167], v[128:131], v[96:99], a[164:167]\n" \
"s_add_u32 m0, %[mB], 0x4800\n" \
"s_add_u32 s84, s63, 0x10000\n" \
"v_mfma_f32_16x16x32_bf16 a[196:199], v[128:131], v[104:107], a[196:199]\n" \
"buffer_load_dwordx4 v27, s[72:75], s84 offen lds\n" \
"v_mfma_f32_16x16x32_bf16 a[196:199], v[124:127], v[100:103], a[196:199]\n" \
"s_add_u32 s52, s89, s96\n" \
"s_lshl_b32 s99, %[w], 11\n" \
"v_mfma_f32_16x16x32_bf16 a[228:231], v[124:127], v[108:111], a[228:231]\n" \
"ds_read_b64_tr_b16 v[124:125], v33 offset:32768\n" \
"ds_read_b64_tr_b16 v[126:127], v33 offset:33792\n" \
"v_mfma_f32_16x16x32_bf16 a[228:231], v[128:131], v[112:115], a[228:231]\n" \
"ds_read_b64_tr_b16 v[128:129], v33 offset:40960\n" \
"ds_read_b64_tr_b16 v[130:131], v33 offset:41984\n" \
"v_mfma_f32_16x16x32_bf16 a[136:139], v[136:139], v[88:91], a[136:139]\n" \
"s_lshl_b32 s88, s101, 15\n" \
"v_add_u32_e64 v217, v214, v216\n" \
"v_mfma_f32_16x16x32_bf16 a[136:139], v[132:135], v[84:87], a[136:139]\n" \
"v_cndmask_b32_e32 v220, v219, v218, vcc\n" \
"v_lshlrev_b32_e32 v224, 4, v223\n" \
"v_mfma_f32_16x16x32_bf16 a[168:171], v[132:135], v[92:95], a[168:171]\n" \
"s_add_u32 s98, s89, s99\n" \
"s_add_u32 s97, s89, s88\n" \
"v_mfma_f32_16x16x32_bf16 a[168:171], v[136:139], v[96:99], a[168:171]\n" \
"s_lshl_b32 s96, s100, 11\n" \
"v_mov_b32_e32 v221, 0xc0c0c0c\n" \
"v_mfma_f32_16x16x32_bf16 a[200:203], v[136:139], v[104:107], a[200:203]\n" \
"v_cmp_le_u32_e32 vcc, v217, v215\n" \
"v_add_u32_e64 v24, v222, v224\n" \
"v_mfma_f32_16x16x32_bf16 a[200:203], v[132:135], v[100:103], a[200:203]\n" \
"v_lshlrev_b32_e32 v227, 4, v226\n" \
"s_add_u32 s99, s97, s96\n" \
"v_mfma_f32_16x16x32_bf16 a[232:235], v[132:135], v[108:111], a[232:235]\n" \
"ds_read_b64_tr_b16 v[132:133], v34 offset:32768\n" \
"ds_read_b64_tr_b16 v[134:135], v34 offset:33792\n" \
"v_mfma_f32_16x16x32_bf16 a[232:235], v[136:139], v[112:115], a[232:235]\n" \
"ds_read_b64_tr_b16 v[136:137], v34 offset:40960\n" \
"ds_read_b64_tr_b16 v[138:139], v34 offset:41984\n" \
"v_mfma_f32_16x16x32_bf16 a[140:143], v[144:147], v[88:91], a[140:143]\n" \
"ds_read_b128 v[88:91], v29 offset:32768\n" \
"s_lshl_b32 s101, s95, 16\n" \
"v_mfma_f32_16x16x32_bf16 a[140:143], v[140:143], v[84:87], a[140:143]\n" \
"ds_read_b128 v[84:87], v28 offset:32768\n" \
"v_cndmask_b32_e32 v40, v220, v221, vcc\n" \
"v_mfma_f32_16x16x32_bf16 a[172:175], v[140:143], v[92:95], a[172:175]\n" \
"ds_read_b128 v[92:95], v28 offset:34816\n" \
"v_add_u32_e64 v25, v222, v227\n" \
"v_mfma_f32_16x16x32_bf16 a[172:175], v[144:147], v[96:99], a[172:175]\n" \
"ds_read_b128 v[96:99], v29 offset:34816\n" \
"s_add_u32 s54, 0x10000, s98\n" \
"v_mfma_f32_16x16x32_bf16 a[204:207], v[144:147], v[104:107], a[204:207]\n" \
"ds_read_b128 v[104:107], v29 offset:36864\n" \
"s_add_u32 s43, s99, s101\n" \
"v_mfma_f32_16x16x32_bf16 a[204:207], v[140:143], v[100:103], a[204:207]\n" \
"ds_read_b128 v[100:103], v28 offset:36864\n" \
"s_mul_i32 s89, 0x1800, s100\n" \
"v_mfma_f32_16x16x32_bf16 a[236:239], v[140:143], v[108:111], a[236:239]\n" \
"ds_read_b64_tr_b16 v[140:141], v35 offset:32768\n" \
"ds_read_b64_tr_b16 v[142:143], v35 offset:33792\n" \
"v_mfma_f32_16x16x32_bf16 a[236:239], v[144:147], v[112:115], a[236:239]\n" \
"s_add_u32 m0, %[mA], 0x1800\n" \
"s_waitcnt lgkmcnt(12)\n" \
"v_mfma_f32_16x16x32_bf16 a[16:19], v[56:59], v[152:155], a[16:19]\n" \
"s_add_u32 s85, s62, 0xc000\n" \
"ds_read_b64_tr_b16 v[144:145], v35 offset:40960\n" \
"v_mfma_f32_16x16x32_bf16 a[16:19], v[52:55], v[148:151], a[16:19]\n" \
"buffer_load_dwordx4 v22, s[64:67], s85 offen lds\n" \
"v_mfma_f32_16x16x32_bf16 a[48:51], v[60:63], v[148:151], a[48:51]\n" \
"s_add_u32 m0, %[mA], 0x1c00\n" \
"s_add_u32 s84, s62, 0xe000\n" \
"v_mfma_f32_16x16x32_bf16 a[48:51], v[64:67], v[152:155], a[48:51]\n" \
"ds_read_b64_tr_b16 v[146:147], v35 offset:41984\n" \
"ds_read_b128 v[108:111], v28 offset:38912\n" \
"v_mfma_f32_16x16x32_bf16 a[80:83], v[72:75], v[152:155], a[80:83]\n" \
"buffer_load_dwordx4 v23, s[64:67], s84 offen lds\n" \
"v_mfma_f32_16x16x32_bf16 a[80:83], v[68:71], v[148:151], a[80:83]\n" \
"s_add_u32 m0, %[mB], 0xc00\n" \
"s_add_u32 s85, s63, 0x18000\n" \
"v_mfma_f32_16x16x32_bf16 a[112:115], v[76:79], v[148:151], a[112:115]\n" \
"ds_read_b128 v[112:115], v29 offset:38912\n" \
"v_mov_b32_e32 v223, 0x3020c0c\n" \
"v_mfma_f32_16x16x32_bf16 a[112:115], v[80:83], v[152:155], a[112:115]\n" \
"buffer_load_dwordx4 v27, s[68:71], s85 offen lds\n" \
"v_mfma_f32_16x16x32_bf16 a[20:23], v[56:59], v[160:163], a[20:23]\n" \
"s_add_u32 m0, %[mB], 0x4c00\n" \
"s_add_u32 s84, s63, 0x18000\n" \
"v_mfma_f32_16x16x32_bf16 a[20:23], v[52:55], v[156:159], a[20:23]\n" \
"v_mov_b32_e32 v224, 0xc0c0302\n" \
"v_cmp_ne_u32_e32 vcc, 0, v216\n" \
"v_mfma_f32_16x16x32_bf16 a[52:55], v[60:63], v[156:159], a[52:55]\n" \
"buffer_load_dwordx4 v27, s[72:75], s84 offen lds\n" \
"v_mfma_f32_16x16x32_bf16 a[52:55], v[64:67], v[160:163], a[52:55]\n" \
"s_mov_b32 m0, %[mS]\n" \
"s_lshl_b32 s98, s94, 16\n" \
"v_mfma_f32_16x16x32_bf16 a[84:87], v[72:75], v[160:163], a[84:87]\n" \
"s_add_u32 s97, s43, s89\n" \
"v_add_u32_e64 v222, 2, v217\n" \
"v_mfma_f32_16x16x32_bf16 a[84:87], v[68:71], v[156:159], a[84:87]\n" \
"v_cndmask_b32_e32 v225, v224, v223, vcc\n" \
"s_add_u32 s42, s99, s98\n" \
"v_mfma_f32_16x16x32_bf16 a[116:119], v[76:79], v[156:159], a[116:119]\n" \
"s_add_u32 s45, 0x4000, s97\n" \
"s_lshr_b32 s94, %[w], 1\n" \
"v_mfma_f32_16x16x32_bf16 a[116:119], v[80:83], v[160:163], a[116:119]\n" \
"s_and_b32 s95, 1, %[w]\n" \
"v_mov_b32_e32 v226, 0xc0c0c0c\n" \
"v_mfma_f32_16x16x32_bf16 a[24:27], v[56:59], v[168:171], a[24:27]\n" \
"v_cmp_le_u32_e32 vcc, v222, v215\n" \
"s_add_u32 s88, s42, s89\n" \
"v_mfma_f32_16x16x32_bf16 a[24:27], v[52:55], v[164:167], a[24:27]\n" \
"s_add_u32 s96, s94, s95\n" \
"s_add_u32 s97, 1, s94\n" \
"v_mfma_f32_16x16x32_bf16 a[56:59], v[60:63], v[164:167], a[56:59]\n" \
"v_cndmask_b32_e32 v41, v225, v226, vcc\n" \
"v_and_b32_e32 v230, 3, v229\n" \
"v_mfma_f32_16x16x32_bf16 a[56:59], v[64:67], v[168:171], a[56:59]\n" \
"s_waitcnt vmcnt(18)\n" \
"v_perm_b32 v252, v209, v208, s87\n" \
"v_mfma_f32_16x16x32_bf16 a[88:91], v[72:75], v[168:171], a[88:91]\n" \
"s_mov_b32 s87, 0x7060302\n" \
"v_perm_b32 v253, v209, v208, s87\n" \
"v_mfma_f32_16x16x32_bf16 a[88:91], v[68:71], v[164:167], a[88:91]\n" \
"s_mov_b32 s87, 0x5040100\n" \
"s_waitcnt vmcnt(16)\n" \
"v_perm_b32 v254, v211, v210, s87\n" \
"s_mov_b32 exec_lo, 0x15\n" \
"s_mov_b32 exec_hi, 0x180000\n" \
"buffer_load_dword v11, s[80:83], 0 offen sc1 lds\n" \
"s_mov_b64 exec, -1\n" \
"v_mfma_f32_16x16x32_bf16 a[120:123], v[76:79], v[164:167], a[120:123]\n" \
"s_mov_b32 s87, 0x7060100\n" \
"v_perm_b32 v255, v211, v210, s87\n" \
"v_mfma_f32_16x16x32_bf16 a[120:123], v[80:83], v[168:171], a[120:123]\n" \
"s_mov_b32 m0, %[mS]\n" \
"s_mov_b32 s86, s10\n" \
"v_mfma_f32_16x16x32_bf16 a[28:31], v[56:59], v[176:179], a[28:31]\n" \
"s_mov_b32 s87, s11\n" \
"ds_write2_b32 v198, v252, v253 offset0:0 offset1:2\n" \
"v_mfma_f32_16x16x32_bf16 a[28:31], v[52:55], v[172:175], a[28:31]\n" \
"ds_write_b128 v196, v[200:203]\n" \
"v_mfma_f32_16x16x32_bf16 a[60:63], v[60:63], v[172:175], a[60:63]\n" \
"ds_write2_b32 v198, v254, v255 offset0:4 offset1:6\n" \
"s_add_u32 s44, 0x4000, s88\n" \
"s_mov_b32 exec_lo, 0x3ffff\n" \
"s_mov_b32 exec_hi, 0x0\n" \
"global_load_lds_dword v188, s[86:87]\n" \
"s_mov_b64 exec, -1\n" \
"v_mfma_f32_16x16x32_bf16 a[60:63], v[64:67], v[176:179], a[60:63]\n" \
"ds_write_b128 v197, v[204:207]\n" \
"v_mfma_f32_16x16x32_bf16 a[92:95], v[72:75], v[176:179], a[92:95]\n" \
"s_lshl_b32 s59, s96, 13\n" \
"s_sub_u32 s98, s97, s95\n" \
"v_mfma_f32_16x16x32_bf16 a[92:95], v[68:71], v[172:175], a[92:95]\n" \
"s_lshl_b32 s99, s94, 13\n" \
"s_mul_i32 s101, 0x1800, s94\n" \
"v_mfma_f32_16x16x32_bf16 a[124:127], v[76:79], v[172:175], a[124:127]\n" \
"v_mov_b32_e32 v227, 0xc0c0c0c\n" \
"v_cmp_le_u32_e32 vcc, v217, v215\n" \
"v_mfma_f32_16x16x32_bf16 a[124:127], v[80:83], v[176:179], a[124:127]\n" \
"v_add_u32_e32 v231, v228, v230\n" \
"s_lshl_b32 s60, s98, 13\n" \
"v_mfma_f32_16x16x32_bf16 a[0:3], v[120:123], v[56:59], a[0:3]\n" \
"s_add_u32 s100, 0x1800, s99\n" \
"s_sub_u32 s88, 0x17000, s101\n" \
"v_mfma_f32_16x16x32_bf16 a[0:3], v[116:119], v[52:55], a[0:3]\n" \
"s_lshl_b32 s96, s94, 15\n" \
"v_cndmask_b32_e32 v42, v227, v220, vcc\n" \
"v_mfma_f32_16x16x32_bf16 a[32:35], v[116:119], v[60:63], a[32:35]\n" \
"v_lshlrev_b32_e32 v232, 7, v231\n" \
"s_add_u32 s97, 0x10000, s96\n" \
"v_mfma_f32_16x16x32_bf16 a[32:35], v[120:123], v[64:67], a[32:35]\n" \
"s_cmp_lg_u32 s95, 0\n" \
"s_cselect_b32 s61, s100, s88\n" \
"v_mfma_f32_16x16x32_bf16 a[64:67], v[120:123], v[72:75], a[64:67]\n" \
"s_lshl_b32 s98, s95, 11\n" \
"v_mov_b32_e32 v228, 0xc0c0c0c\n" \
"v_mfma_f32_16x16x32_bf16 a[64:67], v[116:119], v[68:71], a[64:67]\n" \
"v_cmp_le_u32_e32 vcc, v222, v215\n" \
"v_add_u32_e32 v233, 0x14000, v232\n" \
"v_mfma_f32_16x16x32_bf16 a[96:99], v[116:119], v[76:79], a[96:99]\n" \
"v_lshlrev_b32_e32 v235, 3, v234\n" \
"s_add_u32 s101, s97, s98\n" \
"v_mfma_f32_16x16x32_bf16 a[96:99], v[120:123], v[80:83], a[96:99]\n" \
"v_cndmask_b32_e32 v43, v228, v225, vcc\n" \
"v_and_b32_e32 v231, 2, v212\n" \
"v_mfma_f32_16x16x32_bf16 a[4:7], v[128:131], v[56:59], a[4:7]\n" \
"v_add_u32_e32 v236, v233, v235\n" \
"v_lshrrev_b32_e32 v237, 1, v230\n" \
"v_mfma_f32_16x16x32_bf16 a[4:7], v[124:127], v[52:55], a[4:7]\n" \
"v_add_u32_e32 v243, v242, v230\n" \
"s_add_u32 s100, s101, s99\n" \
"v_mfma_f32_16x16x32_bf16 a[36:39], v[124:127], v[60:63], a[36:39]\n" \
"v_and_b32_e64 v229, 3, v212\n" \
"v_mov_b32_e32 v232, 0x3f80\n" \
"v_mfma_f32_16x16x32_bf16 a[36:39], v[128:131], v[64:67], a[36:39]\n" \
"v_cmp_eq_u32_e32 vcc, 0, v231\n" \
"v_lshlrev_b32_e32 v239, 1, v238\n" \
"v_mfma_f32_16x16x32_bf16 a[68:71], v[128:131], v[72:75], a[68:71]\n" \
"v_lshlrev_b32_e32 v244, 7, v243\n" \
"s_add_u32 s88, 0x4000, s100\n" \
"v_mfma_f32_16x16x32_bf16 a[68:71], v[124:127], v[68:71], a[68:71]\n" \
"v_or_b32_e32 v230, 2, v229\n" \
"v_cndmask_b32_e32 v233, 0, v232, vcc\n" \
"v_mfma_f32_16x16x32_bf16 a[100:103], v[124:127], v[76:79], a[100:103]\n" \
"v_or_b32_e32 v240, v237, v239\n" \
"v_add_u32_e32 v245, 0x14000, v244\n" \
"v_mfma_f32_16x16x32_bf16 a[100:103], v[128:131], v[80:83], a[100:103]\n" \
"v_cmp_eq_u32_e32 vcc, v230, v213\n" \
"s_waitcnt lgkmcnt(0)\n" \
"v_mfma_f32_16x16x32_bf16 a[8:11], v[136:139], v[56:59], a[8:11]\n" \
"s_cmp_lg_u32 s95, 0\n" \
"s_cselect_b32 s47, s88, s101\n" \
"v_mfma_f32_16x16x32_bf16 a[8:11], v[132:135], v[52:55], a[8:11]\n" \
"v_lshlrev_b32_e32 v241, 5, v240\n" \
"v_add_u32_e32 v246, v245, v235\n" \
"v_mfma_f32_16x16x32_bf16 a[40:43], v[132:135], v[60:63], a[40:43]\n" \
"v_cndmask_b32_e32 v46, 0, v233, vcc\n" \
"v_and_b32_e32 v234, 51, v212\n" \
"v_mfma_f32_16x16x32_bf16 a[40:43], v[136:139], v[64:67], a[40:43]\n" \
"v_add_u32_e64 v190, v236, v241\n" \
"v_mov_b32_e32 v235, 0x3f80\n" \
"v_mfma_f32_16x16x32_bf16 a[72:75], v[136:139], v[72:75], a[72:75]\n" \
"v_cmp_eq_u32_e32 vcc, 2, v234\n" \
"s_lshl_b32 s88, s95, 4\n" \
"v_mfma_f32_16x16x32_bf16 a[72:75], v[132:135], v[68:71], a[72:75]\n" \
"v_xor_b32_e32 v247, 3, v237\n" \
"v_cndmask_b32_e32 v47, 0, v235, vcc\n" \
"v_mfma_f32_16x16x32_bf16 a[104:107], v[132:135], v[76:79], a[104:107]\n" \
"v_add_u32_e64 v236, s88, v215\n" \
"v_mov_b32_e32 v237, 0xffff\n" \
"v_mfma_f32_16x16x32_bf16 a[104:107], v[136:139], v[80:83], a[104:107]\n" \
"v_cmp_le_u32_e32 vcc, v214, v236\n" \
"v_mov_b32_e32 v239, 0xffff0000\n" \
"v_mfma_f32_16x16x32_bf16 a[12:15], v[144:147], v[56:59], a[12:15]\n" \
"s_add_u32 s62, s22, 0x280\n" \
"s_and_b32 s62, s62, 0x3ff\n" \
"v_mfma_f32_16x16x32_bf16 a[12:15], v[140:143], v[52:55], a[12:15]\n" \
"s_lshl_b32 s63, s62, 12\n" \
"v_cndmask_b32_e32 v238, 0, v237, vcc\n" \
"v_mfma_f32_16x16x32_bf16 a[44:47], v[140:143], v[60:63], a[44:47]\n" \
"v_cmp_lt_u32_e32 vcc, v214, v236\n" \
"v_add_u32_e64 v241, 2, v214\n" \
"v_mfma_f32_16x16x32_bf16 a[44:47], v[144:147], v[64:67], a[44:47]\n" \
"v_cndmask_b32_e32 v240, 0, v239, vcc\n" \
"v_mov_b32_e32 v242, 0xffff\n" \
"v_mfma_f32_16x16x32_bf16 a[76:79], v[144:147], v[72:75], a[76:79]\n" \
"v_cmp_le_u32_e32 vcc, v241, v236\n" \
"v_lshlrev_b32_e32 v248, 5, v247\n" \
"v_mfma_f32_16x16x32_bf16 a[76:79], v[140:143], v[68:71], a[76:79]\n" \
"v_cndmask_b32_e32 v243, 0, v242, vcc\n" \
"v_add_u32_e64 v39, v246, v248\n" \
"v_mfma_f32_16x16x32_bf16 a[108:111], v[140:143], v[76:79], a[108:111]\n" \
"v_mov_b32_e32 v244, 0xffff0000\n" \
"v_cmp_lt_u32_e32 vcc, v241, v236\n" \
"v_mfma_f32_16x16x32_bf16 a[108:111], v[144:147], v[80:83], a[108:111]\n" \
"v_add_u32_e32 v246, 16, v214\n" \
"v_cndmask_b32_e32 v245, 0, v244, vcc\n" \
"s_waitcnt vmcnt(2)\n" \
"s_barrier\n" \
"v_mfma_f32_16x16x32_bf16 a[128:131], v[120:123], v[88:91], a[128:131]\n" \
"s_add_u32 m0, %[mA], 0x8000\n" \
"s_add_u32 s85, s62, 0x0\n" \
"v_mfma_f32_16x16x32_bf16 a[128:131], v[116:119], v[84:87], a[128:131]\n" \
"buffer_load_dwordx4 v22, s[64:67], s85 offen lds\n" \
"v_mfma_f32_16x16x32_bf16 a[160:163], v[116:119], v[92:95], a[160:163]\n" \
"ds_read_b128 v[56:59], v29 offset:16384\n" \
"ds_read_b128 v[52:55], v28 offset:16384\n" \
"v_mfma_f32_16x16x32_bf16 a[160:163], v[120:123], v[96:99], a[160:163]\n" \
"s_add_u32 m0, %[mA], 0x8400\n" \
"s_add_u32 s84, s62, 0x2000\n" \
"v_mfma_f32_16x16x32_bf16 a[192:195], v[120:123], v[104:107], a[192:195]\n" \
"buffer_load_dwordx4 v23, s[64:67], s84 offen lds\n" \
"v_mfma_f32_16x16x32_bf16 a[192:195], v[116:119], v[100:103], a[192:195]\n" \
"ds_read_b128 v[60:63], v28 offset:18432\n" \
"ds_read_b128 v[64:67], v29 offset:18432\n" \
"v_mfma_f32_16x16x32_bf16 a[224:227], v[116:119], v[108:111], a[224:227]\n" \
"ds_read_b64_tr_b16 v[116:117], v32 offset:0\n" \
"ds_read_b64_tr_b16 v[118:119], v32 offset:1024\n" \
"v_mfma_f32_16x16x32_bf16 a[224:227], v[120:123], v[112:115], a[224:227]\n" \
"s_add_u32 m0, %[mB], 0x8000\n" \
"s_add_u32 s85, s63, 0x0\n" \
"v_mfma_f32_16x16x32_bf16 a[132:135], v[128:131], v[88:91], a[132:135]\n" \
"buffer_load_dwordx4 v26, s[68:71], s85 offen lds\n" \
"v_mfma_f32_16x16x32_bf16 a[132:135], v[124:127], v[84:87], a[132:135]\n" \
"ds_read_b64_tr_b16 v[120:121], v32 offset:8192\n" \
"ds_read_b64_tr_b16 v[122:123], v32 offset:9216\n" \
"v_mfma_f32_16x16x32_bf16 a[164:167], v[124:127], v[92:95], a[164:167]\n" \
"ds_read_b128 v[72:75], v29 offset:20480\n" \
"ds_read_b128 v[68:71], v28 offset:20480\n" \
"v_mfma_f32_16x16x32_bf16 a[164:167], v[128:131], v[96:99], a[164:167]\n" \
"s_add_u32 m0, %[mB], 0xc000\n" \
"s_add_u32 s84, s63, 0x0\n" \
"v_mfma_f32_16x16x32_bf16 a[196:199], v[128:131], v[104:107], a[196:199]\n" \
"buffer_load_dwordx4 v26, s[72:75], s84 offen lds\n" \
"v_mfma_f32_16x16x32_bf16 a[196:199], v[124:127], v[100:103], a[196:199]\n" \
"ds_read_b128 v[76:79], v28 offset:22528\n" \
"ds_read_b128 v[80:83], v29 offset:22528\n" \
"v_mfma_f32_16x16x32_bf16 a[228:231], v[124:127], v[108:111], a[228:231]\n" \
"ds_read_b64_tr_b16 v[124:125], v33 offset:0\n" \
"ds_read_b64_tr_b16 v[126:127], v33 offset:1024\n" \
"v_mfma_f32_16x16x32_bf16 a[228:231], v[128:131], v[112:115], a[228:231]\n" \
"s_add_u32 m0, %[mA], 0x8800\n" \
"s_add_u32 s85, s62, 0x4000\n" \
"v_mfma_f32_16x16x32_bf16 a[136:139], v[136:139], v[88:91], a[136:139]\n" \
"buffer_load_dwordx4 v22, s[64:67], s85 offen lds\n" \
"v_mfma_f32_16x16x32_bf16 a[136:139], v[132:135], v[84:87], a[136:139]\n" \
"ds_read_b64_tr_b16 v[128:129], v33 offset:8192\n" \
"ds_read_b64_tr_b16 v[130:131], v33 offset:9216\n" \
"v_mfma_f32_16x16x32_bf16 a[168:171], v[132:135], v[92:95], a[168:171]\n" \
"ds_read_b128 v[200:203], v199 offset:0\n" \
"ds_read_b128 v[204:207], v199 offset:4096\n" \
"v_mfma_f32_16x16x32_bf16 a[168:171], v[136:139], v[96:99], a[168:171]\n" \
"s_add_u32 m0, %[mA], 0x8c00\n" \
"s_add_u32 s84, s62, 0x6000\n" \
"v_mfma_f32_16x16x32_bf16 a[200:203], v[136:139], v[104:107], a[200:203]\n" \
"buffer_load_dwordx4 v23, s[64:67], s84 offen lds\n" \
"v_mfma_f32_16x16x32_bf16 a[200:203], v[132:135], v[100:103], a[200:203]\n" \
"ds_read_b128 v[208:211], v199 offset:8192\n" \
"v_mov_b32_e32 v247, 0xffff\n" \
"v_mfma_f32_16x16x32_bf16 a[232:235], v[132:135], v[108:111], a[232:235]\n" \
"ds_read_b64_tr_b16 v[132:133], v34 offset:0\n" \
"ds_read_b64_tr_b16 v[134:135], v34 offset:1024\n" \
"v_mfma_f32_16x16x32_bf16 a[232:235], v[136:139], v[112:115], a[232:235]\n" \
"s_add_u32 m0, %[mB], 0x8400\n" \
"s_add_u32 s85, s63, 0x8000\n" \
"v_mfma_f32_16x16x32_bf16 a[140:143], v[144:147], v[88:91], a[140:143]\n" \
"buffer_load_dwordx4 v26, s[68:71], s85 offen lds\n" \
"v_mfma_f32_16x16x32_bf16 a[140:143], v[140:143], v[84:87], a[140:143]\n" \
"ds_read_b64_tr_b16 v[136:137], v34 offset:8192\n" \
"ds_read_b64_tr_b16 v[138:139], v34 offset:9216\n" \
"v_mfma_f32_16x16x32_bf16 a[172:175], v[140:143], v[92:95], a[172:175]\n" \
"v_cmp_le_u32_e32 vcc, v246, v236\n" \
"v_mov_b32_e32 v249, 0xffff0000\n" \
"v_mfma_f32_16x16x32_bf16 a[172:175], v[144:147], v[96:99], a[172:175]\n" \
"s_add_u32 m0, %[mB], 0xc400\n" \
"s_add_u32 s84, s63, 0x8000\n" \
"v_mfma_f32_16x16x32_bf16 a[204:207], v[144:147], v[104:107], a[204:207]\n" \
"buffer_load_dwordx4 v26, s[72:75], s84 offen lds\n" \
"v_mfma_f32_16x16x32_bf16 a[204:207], v[140:143], v[100:103], a[204:207]\n" \
"v_cndmask_b32_e32 v248, 0, v247, vcc\n" \
"v_cmp_lt_u32_e32 vcc, v246, v236\n" \
"s_nop 0\n" \
"v_mfma_f32_16x16x32_bf16 a[236:239], v[140:143], v[108:111], a[236:239]\n" \
"ds_read_b64_tr_b16 v[140:141], v35 offset:0\n" \
"ds_read_b64_tr_b16 v[142:143], v35 offset:1024\n" \
"v_mfma_f32_16x16x32_bf16 a[236:239], v[144:147], v[112:115], a[236:239]\n" \
"s_add_u32 m0, %[mA], 0x9000\n" \
"s_add_u32 s85, s62, 0x8000\n" \
"v_mfma_f32_16x16x32_bf16 a[144:147], v[88:91], v[152:155], a[144:147]\n" \
"buffer_load_dwordx4 v22, s[64:67], s85 offen lds\n" \
"v_mfma_f32_16x16x32_bf16 a[144:147], v[84:87], v[148:151], a[144:147]\n" \
"ds_read_b64_tr_b16 v[144:145], v35 offset:8192\n" \
"ds_read_b64_tr_b16 v[146:147], v35 offset:9216\n" \
"v_mfma_f32_16x16x32_bf16 a[176:179], v[92:95], v[148:151], a[176:179]\n" \
"s_lshl_b32 s89, %[w], 8\n" \
"v_cndmask_b32_e32 v250, 0, v249, vcc\n" \
"v_mfma_f32_16x16x32_bf16 a[176:179], v[96:99], v[152:155], a[176:179]\n" \
"s_add_u32 m0, %[mA], 0x9400\n" \
"s_add_u32 s84, s62, 0xa000\n" \
"v_mfma_f32_16x16x32_bf16 a[208:211], v[104:107], v[152:155], a[208:211]\n" \
"buffer_load_dwordx4 v23, s[64:67], s84 offen lds\n" \
"v_mfma_f32_16x16x32_bf16 a[208:211], v[100:103], v[148:151], a[208:211]\n" \
"v_add_u32_e64 v251, 18, v214\n" \
"s_add_u32 s46, 0x27000, s89\n" \
"v_mfma_f32_16x16x32_bf16 a[240:243], v[108:111], v[148:151], a[240:243]\n" \
"ds_read_b64_tr_b16 v[148:149], v32 offset:16384\n" \
"ds_read_b64_tr_b16 v[150:151], v32 offset:17408\n" \
"v_mfma_f32_16x16x32_bf16 a[240:243], v[112:115], v[152:155], a[240:243]\n" \
"s_add_u32 m0, %[mB], 0x8800\n" \
"s_add_u32 s85, s63, 0x10000\n" \
"v_mfma_f32_16x16x32_bf16 a[148:151], v[88:91], v[160:163], a[148:151]\n" \
"buffer_load_dwordx4 v27, s[68:71], s85 offen lds\n" \
"v_mfma_f32_16x16x32_bf16 a[148:151], v[84:87], v[156:159], a[148:151]\n" \
"ds_read_b64_tr_b16 v[152:153], v32 offset:24576\n" \
"ds_read_b64_tr_b16 v[154:155], v32 offset:25600\n" \
"v_mfma_f32_16x16x32_bf16 a[180:183], v[92:95], v[156:159], a[180:183]\n" \
"v_mov_b32_e32 v218, 0xffff\n" \
"v_cmp_le_u32_e32 vcc, v251, v236\n" \
"v_mfma_f32_16x16x32_bf16 a[180:183], v[96:99], v[160:163], a[180:183]\n" \
"s_add_u32 m0, %[mB], 0xc800\n" \
"s_add_u32 s84, s63, 0x10000\n" \
"v_mfma_f32_16x16x32_bf16 a[212:215], v[104:107], v[160:163], a[212:215]\n" \
"buffer_load_dwordx4 v27, s[72:75], s84 offen lds\n" \
"v_mfma_f32_16x16x32_bf16 a[212:215], v[100:103], v[156:159], a[212:215]\n" \
"s_add_u32 s89, 0xffffc000, s101\n" \
"v_cndmask_b32_e32 v219, 0, v218, vcc\n" \
"v_mfma_f32_16x16x32_bf16 a[244:247], v[108:111], v[156:159], a[244:247]\n" \
"ds_read_b64_tr_b16 v[156:157], v33 offset:16384\n" \
"ds_read_b64_tr_b16 v[158:159], v33 offset:17408\n" \
"v_mfma_f32_16x16x32_bf16 a[244:247], v[112:115], v[160:163], a[244:247]\n" \
"ds_read_b64_tr_b16 v[160:161], v33 offset:24576\n" \
"ds_read_b64_tr_b16 v[162:163], v33 offset:25600\n" \
"v_mfma_f32_16x16x32_bf16 a[152:155], v[88:91], v[168:171], a[152:155]\n" \
"v_lshlrev_b32_e32 v224, 3, v212\n" \
"s_lshr_b32 s94, %[b], 5\n" \
"v_mfma_f32_16x16x32_bf16 a[152:155], v[84:87], v[164:167], a[152:155]\n" \
"s_cmp_lg_u32 s95, 0\n" \
"s_cselect_b32 s48, s89, s100\n" \
"v_mfma_f32_16x16x32_bf16 a[184:187], v[92:95], v[164:167], a[184:187]\n" \
"s_mov_b32 s96, 0xc000\n" \
"s_mov_b32 s97, 0x8000\n" \
"v_mfma_f32_16x16x32_bf16 a[184:187], v[96:99], v[168:171], a[184:187]\n" \
"v_mov_b32_e32 v221, 0xffff0000\n" \
"v_cmp_lt_u32_e32 vcc, v251, v236\n" \
"v_mfma_f32_16x16x32_bf16 a[216:219], v[104:107], v[168:171], a[216:219]\n" \
"v_lshrrev_b32_e32 v212, 4, v20\n" \
"v_cndmask_b32_e32 v216, 0, v221, vcc\n" \
"v_mfma_f32_16x16x32_bf16 a[216:219], v[100:103], v[164:167], a[216:219]\n" \
"s_cmp_lg_u32 s94, 0\n" \
"s_cselect_b32 s53, s96, 0x8000\n" \
"v_mfma_f32_16x16x32_bf16 a[248:251], v[108:111], v[164:167], a[248:251]\n" \
"ds_read_b64_tr_b16 v[164:165], v34 offset:16384\n" \
"ds_read_b64_tr_b16 v[166:167], v34 offset:17408\n" \
"v_mfma_f32_16x16x32_bf16 a[248:251], v[112:115], v[168:171], a[248:251]\n" \
"ds_read_b64_tr_b16 v[168:169], v34 offset:24576\n" \
"ds_read_b64_tr_b16 v[170:171], v34 offset:25600\n" \
"v_mfma_f32_16x16x32_bf16 a[156:159], v[88:91], v[176:179], a[156:159]\n" \
"ds_read_b128 v[88:91], v29 offset:0\n" \
"v_lshlrev_b32_e32 v223, 2, v236\n" \
"v_mfma_f32_16x16x32_bf16 a[156:159], v[84:87], v[172:175], a[156:159]\n" \
"ds_read_b128 v[84:87], v28 offset:0\n" \
"s_lshl_b32 s101, %[w], 9\n" \
"v_mfma_f32_16x16x32_bf16 a[188:191], v[92:95], v[172:175], a[188:191]\n" \
"ds_read_b128 v[92:95], v28 offset:2048\n" \
"v_mul_u32_u24_e32 v213, 0xe39, v212\n" \
"v_mfma_f32_16x16x32_bf16 a[188:191], v[96:99], v[176:179], a[188:191]\n" \
"ds_read_b128 v[96:99], v29 offset:2048\n" \
"v_or_b32_e32 v15, v219, v216\n" \
"s_nop 0\n" \
"v_mfma_f32_16x16x32_bf16 a[220:223], v[104:107], v[176:179], a[220:223]\n" \
"ds_read_b128 v[104:107], v29 offset:4096\n" \
"v_add_u32_e32 v18, 0x27000, v223\n" \
"v_mfma_f32_16x16x32_bf16 a[220:223], v[100:103], v[172:175], a[220:223]\n" \
"ds_read_b128 v[100:103], v28 offset:4096\n" \
"s_add_u32 s89, 0x27800, s101\n" \
"v_mfma_f32_16x16x32_bf16 a[252:255], v[108:111], v[172:175], a[252:255]\n" \
"ds_read_b64_tr_b16 v[172:173], v35 offset:16384\n" \
"ds_read_b64_tr_b16 v[174:175], v35 offset:17408\n" \
"v_mfma_f32_16x16x32_bf16 a[252:255], v[112:115], v[176:179], a[252:255]\n" \
"s_add_u32 m0, %[mA], 0x9800\n" \
"s_waitcnt lgkmcnt(12)\n" \
"s_nop 0\n" \
"v_mfma_f32_16x16x32_bf16 a[0:3], v[120:123], v[56:59], a[0:3]\n" \
"s_add_u32 s85, s62, 0xc000\n" \
"ds_read_b64_tr_b16 v[176:177], v35 offset:24576\n" \
"v_mfma_f32_16x16x32_bf16 a[0:3], v[116:119], v[52:55], a[0:3]\n" \
"buffer_load_dwordx4 v22, s[64:67], s85 offen lds\n" \
"v_mfma_f32_16x16x32_bf16 a[32:35], v[116:119], v[60:63], a[32:35]\n" \
"s_add_u32 m0, %[mA], 0x9c00\n" \
"s_add_u32 s84, s62, 0xe000\n" \
"v_mfma_f32_16x16x32_bf16 a[32:35], v[120:123], v[64:67], a[32:35]\n" \
"ds_read_b64_tr_b16 v[178:179], v35 offset:25600\n" \
"ds_read_b128 v[108:111], v28 offset:6144\n" \
"v_mfma_f32_16x16x32_bf16 a[64:67], v[120:123], v[72:75], a[64:67]\n" \
"buffer_load_dwordx4 v23, s[64:67], s84 offen lds\n" \
"v_mfma_f32_16x16x32_bf16 a[64:67], v[116:119], v[68:71], a[64:67]\n" \
"L_pf2:\n" \
"s_cbranch_execz L_pf6\n" \
"s_nop 0\n" \
"s_add_u32 m0, %[mB], 0x8c00\n" \
"s_add_u32 s85, s63, 0x18000\n" \
"v_mfma_f32_16x16x32_bf16 a[96:99], v[116:119], v[76:79], a[96:99]\n" \
"ds_read_b128 v[112:115], v29 offset:6144\n" \
"v_lshrrev_b32_e32 v214, 16, v213\n" \
"v_mfma_f32_16x16x32_bf16 a[96:99], v[120:123], v[80:83], a[96:99]\n" \
"buffer_load_dwordx4 v27, s[68:71], s85 offen lds\n" \
"v_mfma_f32_16x16x32_bf16 a[4:7], v[128:131], v[56:59], a[4:7]\n" \
"s_add_u32 m0, %[mB], 0xcc00\n" \
"s_add_u32 s84, s63, 0x18000\n" \
"v_mfma_f32_16x16x32_bf16 a[4:7], v[124:127], v[52:55], a[4:7]\n" \
"s_cmp_lg_u32 s94, 0\n" \
"s_cselect_b32 s98, s97, 0x4000\n" \
"v_mfma_f32_16x16x32_bf16 a[36:39], v[124:127], v[60:63], a[36:39]\n" \
"buffer_load_dwordx4 v27, s[72:75], s84 offen lds\n" \
"v_mfma_f32_16x16x32_bf16 a[36:39], v[128:131], v[64:67], a[36:39]\n" \
"v_add_u32_e32 v222, 64, v212\n" \
"v_add_u32_e32 v19, s89, v224\n" \
"v_mfma_f32_16x16x32_bf16 a[68:71], v[128:131], v[72:75], a[68:71]\n" \
"v_mul_u32_u24_e32 v216, 18, v214\n" \
"s_and_b32 s94, 7, %[b]\n" \
"v_mfma_f32_16x16x32_bf16 a[68:71], v[124:127], v[68:71], a[68:71]\n" \
"buffer_store_dwordx4 v[200:203], v20, s[80:83], s57 offen sc1\n" \
"v_mfma_f32_16x16x32_bf16 a[100:103], v[124:127], v[76:79], a[100:103]\n" \
"v_mul_u32_u24_e32 v223, 0xe39, v222\n" \
"v_sub_u32_e32 v217, v212, v216\n" \
"v_mfma_f32_16x16x32_bf16 a[100:103], v[128:131], v[80:83], a[100:103]\n" \
"s_lshl_b32 s95, s94, 5\n" \
"v_lshrrev_b32_e32 v224, 16, v223\n" \
"v_mfma_f32_16x16x32_bf16 a[8:11], v[136:139], v[56:59], a[8:11]\n" \
"v_add_u32_e32 v233, 0x80, v212\n" \
"s_add_u32 s96, 32, s95\n" \
"v_mfma_f32_16x16x32_bf16 a[8:11], v[132:135], v[52:55], a[8:11]\n" \
"v_lshlrev_b32_e32 v218, 4, v217\n" \
"v_mul_u32_u24_e32 v226, 18, v224\n" \
"v_mfma_f32_16x16x32_bf16 a[40:43], v[132:135], v[60:63], a[40:43]\n" \
"buffer_store_dwordx4 v[204:207], v20, s[80:83], s56 offen sc1\n" \
"v_mfma_f32_16x16x32_bf16 a[40:43], v[136:139], v[64:67], a[40:43]\n" \
"v_mul_u32_u24_e32 v234, 0xe39, v233\n" \
"v_or_b32_e32 v13, v243, v245\n" \
"v_mfma_f32_16x16x32_bf16 a[72:75], v[136:139], v[72:75], a[72:75]\n" \
"v_add_u32_e32 v219, s96, v218\n" \
"v_sub_u32_e32 v227, v222, v226\n" \
"v_mfma_f32_16x16x32_bf16 a[72:75], v[132:135], v[68:71], a[72:75]\n" \
"v_lshrrev_b32_e32 v235, 16, v234\n" \
"v_add_u32_e32 v244, 0xc0, v212\n" \
"v_mfma_f32_16x16x32_bf16 a[104:107], v[132:135], v[76:79], a[104:107]\n" \
"v_or_b32_e32 v12, v238, v240\n" \
"v_and_b32_e32 v220, 0xff, v219\n" \
"v_mfma_f32_16x16x32_bf16 a[104:107], v[136:139], v[80:83], a[104:107]\n" \
"v_cmp_gt_u32_e32 vcc, 16, v217\n" \
"v_lshlrev_b32_e32 v228, 4, v227\n" \
"v_mfma_f32_16x16x32_bf16 a[12:15], v[144:147], v[56:59], a[12:15]\n" \
"buffer_store_dwordx4 v[208:211], v20, s[80:83], s58 offen sc1\n" \
"v_mfma_f32_16x16x32_bf16 a[12:15], v[140:143], v[52:55], a[12:15]\n" \
"v_mul_u32_u24_e32 v237, 18, v235\n" \
"v_mul_u32_u24_e32 v245, 0xe39, v244\n" \
"v_mfma_f32_16x16x32_bf16 a[44:47], v[140:143], v[60:63], a[44:47]\n" \
"v_or_b32_e32 v14, v248, v250\n" \
"v_cndmask_b32_e32 v221, v218, v220, vcc\n" \
"v_mfma_f32_16x16x32_bf16 a[44:47], v[144:147], v[64:67], a[44:47]\n" \
"v_add_u32_e32 v229, s96, v228\n" \
"v_sub_u32_e32 v238, v233, v237\n" \
"v_mfma_f32_16x16x32_bf16 a[76:79], v[144:147], v[72:75], a[76:79]\n" \
"v_lshrrev_b32_e32 v246, 16, v245\n" \
"v_add_u32_e32 v219, 0x100, v212\n" \
"v_mfma_f32_16x16x32_bf16 a[76:79], v[140:143], v[68:71], a[76:79]\n" \
"v_mul_u32_u24_e32 v215, 0x480, v214\n" \
"v_and_b32_e32 v230, 0xff, v229\n" \
"v_mfma_f32_16x16x32_bf16 a[108:111], v[140:143], v[76:79], a[108:111]\n" \
"s_mov_b32 m0, %[mS]\n" \
"s_mov_b32 s86, s10\n" \
"v_mfma_f32_16x16x32_bf16 a[108:111], v[144:147], v[80:83], a[108:111]\n" \
"s_mov_b32 s87, s11\n" \
"v_cmp_gt_u32_e32 vcc, 16, v227\n" \
"v_mfma_f32_16x16x32_bf16 a[16:19], v[56:59], v[152:155], a[16:19]\n" \
"v_lshlrev_b32_e32 v239, 4, v238\n" \
"v_mul_u32_u24_e32 v248, 18, v246\n" \
"v_mfma_f32_16x16x32_bf16 a[16:19], v[52:55], v[148:151], a[16:19]\n" \
"v_mul_u32_u24_e32 v217, 0xe39, v219\n" \
"v_add_u32_e32 v0, v215, v221\n" \
"v_mfma_f32_16x16x32_bf16 a[48:51], v[60:63], v[148:151], a[48:51]\n" \
"v_cndmask_b32_e32 v231, v228, v230, vcc\n" \
"v_add_u32_e32 v240, s96, v239\n" \
"v_mfma_f32_16x16x32_bf16 a[48:51], v[64:67], v[152:155], a[48:51]\n" \
"v_sub_u32_e32 v249, v244, v248\n" \
"v_lshrrev_b32_e32 v220, 16, v217\n" \
"v_mfma_f32_16x16x32_bf16 a[80:83], v[72:75], v[152:155], a[80:83]\n" \
"v_add_u32_e32 v229, 0x140, v212\n" \
"v_mul_u32_u24_e32 v225, 0x480, v224\n" \
"s_mov_b32 exec_lo, 0xfffc0000\n" \
"s_mov_b32 exec_hi, 0xf\n" \
"global_load_lds_dword v188, s[86:87]\n" \
"s_mov_b64 exec, -1\n" \
"v_mfma_f32_16x16x32_bf16 a[80:83], v[68:71], v[148:151], a[80:83]\n" \
"v_and_b32_e32 v241, 0xff, v240\n" \
"v_cmp_gt_u32_e32 vcc, 16, v238\n" \
"v_mfma_f32_16x16x32_bf16 a[112:115], v[76:79], v[148:151], a[112:115]\n" \
"v_lshlrev_b32_e32 v250, 4, v249\n" \
"v_mul_u32_u24_e32 v215, 18, v220\n" \
"v_mfma_f32_16x16x32_bf16 a[112:115], v[80:83], v[152:155], a[112:115]\n" \
"v_mul_u32_u24_e32 v227, 0xe39, v229\n" \
"v_add_u32_e32 v232, v225, v231\n" \
"v_mfma_f32_16x16x32_bf16 a[20:23], v[56:59], v[160:163], a[20:23]\n" \
"v_cndmask_b32_e32 v242, v239, v241, vcc\n" \
"v_add_u32_e32 v251, s96, v250\n" \
"v_mfma_f32_16x16x32_bf16 a[20:23], v[52:55], v[156:159], a[20:23]\n" \
"v_sub_u32_e32 v221, v219, v215\n" \
"v_lshrrev_b32_e32 v230, 16, v227\n" \
"v_mfma_f32_16x16x32_bf16 a[52:55], v[60:63], v[156:159], a[52:55]\n" \
"v_add_u32_e32 v240, 0x180, v212\n" \
"v_mul_u32_u24_e32 v236, 0x480, v235\n" \
"v_mfma_f32_16x16x32_bf16 a[52:55], v[64:67], v[160:163], a[52:55]\n" \
"v_and_b32_e32 v213, 0xff, v251\n" \
"v_cmp_gt_u32_e32 vcc, 16, v249\n" \
"v_mfma_f32_16x16x32_bf16 a[84:87], v[72:75], v[160:163], a[84:87]\n" \
"v_lshlrev_b32_e32 v223, 4, v221\n" \
"v_mul_u32_u24_e32 v225, 18, v230\n" \
"v_mfma_f32_16x16x32_bf16 a[84:87], v[68:71], v[156:159], a[84:87]\n" \
"v_mul_u32_u24_e32 v238, 0xe39, v240\n" \
"v_add_u32_e32 v1, 0xfffffc00, v232\n" \
"v_mfma_f32_16x16x32_bf16 a[116:119], v[76:79], v[156:159], a[116:119]\n" \
"v_add_u32_e32 v243, v236, v242\n" \
"v_cndmask_b32_e32 v214, v250, v213, vcc\n" \
"v_mfma_f32_16x16x32_bf16 a[116:119], v[80:83], v[160:163], a[116:119]\n" \
"v_add_u32_e32 v224, s96, v223\n" \
"s_waitcnt lgkmcnt(0)\n" \
"v_mfma_f32_16x16x32_bf16 a[24:27], v[56:59], v[168:171], a[24:27]\n" \
"v_sub_u32_e32 v231, v229, v225\n" \
"v_lshrrev_b32_e32 v241, 16, v238\n" \
"v_mfma_f32_16x16x32_bf16 a[24:27], v[52:55], v[164:167], a[24:27]\n" \
"v_add_u32_e32 v251, 0x1c0, v212\n" \
"v_mul_u32_u24_e32 v247, 0x480, v246\n" \
"v_mfma_f32_16x16x32_bf16 a[56:59], v[60:63], v[164:167], a[56:59]\n" \
"v_and_b32_e32 v222, 0xff, v224\n" \
"v_cmp_gt_u32_e32 vcc, 16, v221\n" \
"v_mfma_f32_16x16x32_bf16 a[56:59], v[64:67], v[168:171], a[56:59]\n" \
"v_lshlrev_b32_e32 v232, 4, v231\n" \
"v_mul_u32_u24_e32 v236, 18, v241\n" \
"v_mfma_f32_16x16x32_bf16 a[88:91], v[72:75], v[168:171], a[88:91]\n" \
"v_mul_u32_u24_e32 v249, 0xe39, v251\n" \
"v_add_u32_e32 v2, 0xfffff800, v243\n" \
"v_mfma_f32_16x16x32_bf16 a[88:91], v[68:71], v[164:167], a[88:91]\n" \
"v_add_u32_e32 v216, v247, v214\n" \
"v_cndmask_b32_e32 v226, v223, v222, vcc\n" \
"v_mfma_f32_16x16x32_bf16 a[120:123], v[76:79], v[164:167], a[120:123]\n" \
"v_add_u32_e32 v234, s96, v232\n" \
"v_sub_u32_e32 v242, v240, v236\n" \
"v_mfma_f32_16x16x32_bf16 a[120:123], v[80:83], v[168:171], a[120:123]\n" \
"v_lshrrev_b32_e32 v213, 16, v249\n" \
"v_add_u32_e32 v224, 0x200, v212\n" \
"v_mfma_f32_16x16x32_bf16 a[28:31], v[56:59], v[176:179], a[28:31]\n" \
"s_add_u32 s62, s22, 0x300\n" \
"s_and_b32 s62, s62, 0x3ff\n" \
"v_mfma_f32_16x16x32_bf16 a[28:31], v[52:55], v[172:175], a[28:31]\n" \
"s_lshl_b32 s63, s62, 12\n" \
"v_mul_u32_u24_e32 v218, 0x480, v220\n" \
"v_mfma_f32_16x16x32_bf16 a[60:63], v[60:63], v[172:175], a[60:63]\n" \
"v_and_b32_e32 v235, 0xff, v234\n" \
"v_cmp_gt_u32_e32 vcc, 16, v231\n" \
"v_mfma_f32_16x16x32_bf16 a[60:63], v[64:67], v[176:179], a[60:63]\n" \
"v_lshlrev_b32_e32 v243, 4, v242\n" \
"v_mul_u32_u24_e32 v247, 18, v213\n" \
"v_mfma_f32_16x16x32_bf16 a[92:95], v[72:75], v[176:179], a[92:95]\n" \
"v_mul_u32_u24_e32 v221, 0xe39, v224\n" \
"v_add_u32_e32 v3, 0xfffff400, v216\n" \
"v_mfma_f32_16x16x32_bf16 a[92:95], v[68:71], v[172:175], a[92:95]\n" \
"v_add_u32_e32 v4, v218, v226\n" \
"v_cndmask_b32_e32 v233, v232, v235, vcc\n" \
"v_mfma_f32_16x16x32_bf16 a[124:127], v[76:79], v[172:175], a[124:127]\n" \
"v_add_u32_e32 v245, s96, v243\n" \
"v_sub_u32_e32 v214, v251, v247\n" \
"v_mfma_f32_16x16x32_bf16 a[124:127], v[80:83], v[176:179], a[124:127]\n" \
"v_lshrrev_b32_e32 v222, 16, v221\n" \
"v_and_b32_e32 v246, 0xff, v245\n" \
"s_waitcnt vmcnt(4)\n" \
"s_barrier\n" \
"v_mfma_f32_16x16x32_bf16 a[144:147], v[88:91], v[152:155], a[144:147]\n" \
"s_add_u32 m0, %[mA], 0x0\n" \
"s_add_u32 s85, s62, 0x0\n" \
"v_mfma_f32_16x16x32_bf16 a[144:147], v[84:87], v[148:151], a[144:147]\n" \
"buffer_load_dwordx4 v22, s[64:67], s85 offen lds\n" \
"v_mfma_f32_16x16x32_bf16 a[176:179], v[92:95], v[148:151], a[176:179]\n" \
"ds_read_b128 v[56:59], v29 offset:49152\n" \
"ds_read_b128 v[52:55], v28 offset:49152\n" \
"v_mfma_f32_16x16x32_bf16 a[176:179], v[96:99], v[152:155], a[176:179]\n" \
"s_add_u32 m0, %[mA], 0x400\n" \
"s_add_u32 s84, s62, 0x2000\n" \
"v_mfma_f32_16x16x32_bf16 a[208:211], v[104:107], v[152:155], a[208:211]\n" \
"buffer_load_dwordx4 v23, s[64:67], s84 offen lds\n" \
"v_mfma_f32_16x16x32_bf16 a[208:211], v[100:103], v[148:151], a[208:211]\n" \
"ds_read_b128 v[60:63], v28 offset:51200\n" \
"ds_read_b128 v[64:67], v29 offset:51200\n" \
"v_mfma_f32_16x16x32_bf16 a[240:243], v[108:111], v[148:151], a[240:243]\n" \
"ds_read_b64_tr_b16 v[148:149], v32 offset:49152\n" \
"ds_read_b64_tr_b16 v[150:151], v32 offset:50176\n" \
"v_mfma_f32_16x16x32_bf16 a[240:243], v[112:115], v[152:155], a[240:243]\n" \
"s_add_u32 m0, %[mB], 0x0\n" \
"s_add_u32 s85, s63, 0x0\n" \
"v_mfma_f32_16x16x32_bf16 a[148:151], v[88:91], v[160:163], a[148:151]\n" \
"buffer_load_dwordx4 v26, s[68:71], s85 offen lds\n" \
"v_mfma_f32_16x16x32_bf16 a[148:151], v[84:87], v[156:159], a[148:151]\n" \
"ds_read_b64_tr_b16 v[152:153], v32 offset:57344\n" \
"ds_read_b64_tr_b16 v[154:155], v32 offset:58368\n" \
"v_mfma_f32_16x16x32_bf16 a[180:183], v[92:95], v[156:159], a[180:183]\n" \
"ds_read_b128 v[72:75], v29 offset:53248\n" \
"ds_read_b128 v[68:71], v28 offset:53248\n" \
"v_mfma_f32_16x16x32_bf16 a[180:183], v[96:99], v[160:163], a[180:183]\n" \
"s_add_u32 m0, %[mB], 0x4000\n" \
"s_add_u32 s84, s63, 0x0\n" \
"s_nop 0\n" \
"v_mfma_f32_16x16x32_bf16 a[212:215], v[104:107], v[160:163], a[212:215]\n" \
"buffer_load_dwordx4 v26, s[72:75], s84 offen lds\n" \
"v_mfma_f32_16x16x32_bf16 a[212:215], v[100:103], v[156:159], a[212:215]\n" \
"ds_read_b128 v[76:79], v28 offset:55296\n" \
"ds_read_b128 v[80:83], v29 offset:55296\n" \
"v_mfma_f32_16x16x32_bf16 a[244:247], v[108:111], v[156:159], a[244:247]\n" \
"ds_read_b64_tr_b16 v[156:157], v33 offset:49152\n" \
"ds_read_b64_tr_b16 v[158:159], v33 offset:50176\n" \
"v_mfma_f32_16x16x32_bf16 a[244:247], v[112:115], v[160:163], a[244:247]\n" \
"s_add_u32 m0, %[mA], 0x800\n" \
"s_add_u32 s85, s62, 0x4000\n" \
"v_mfma_f32_16x16x32_bf16 a[152:155], v[88:91], v[168:171], a[152:155]\n" \
"buffer_load_dwordx4 v22, s[64:67], s85 offen lds\n" \
"v_mfma_f32_16x16x32_bf16 a[152:155], v[84:87], v[164:167], a[152:155]\n" \
"ds_read_b64_tr_b16 v[160:161], v33 offset:57344\n" \
"ds_read_b64_tr_b16 v[162:163], v33 offset:58368\n" \
"v_mfma_f32_16x16x32_bf16 a[184:187], v[92:95], v[164:167], a[184:187]\n" \
"v_cmp_gt_u32_e32 vcc, 16, v242\n" \
"v_lshlrev_b32_e32 v216, 4, v214\n" \
"v_mfma_f32_16x16x32_bf16 a[184:187], v[96:99], v[168:171], a[184:187]\n" \
"s_add_u32 m0, %[mA], 0xc00\n" \
"s_add_u32 s84, s62, 0x6000\n" \
"v_mfma_f32_16x16x32_bf16 a[216:219], v[104:107], v[168:171], a[216:219]\n" \
"buffer_load_dwordx4 v23, s[64:67], s84 offen lds\n" \
"v_mfma_f32_16x16x32_bf16 a[216:219], v[100:103], v[164:167], a[216:219]\n" \
"v_mul_u32_u24_e32 v218, 18, v222\n" \
"v_cndmask_b32_e32 v244, v243, v246, vcc\n" \
"v_mfma_f32_16x16x32_bf16 a[248:251], v[108:111], v[164:167], a[248:251]\n" \
"ds_read_b64_tr_b16 v[164:165], v34 offset:49152\n" \
"ds_read_b64_tr_b16 v[166:167], v34 offset:50176\n" \
"v_mfma_f32_16x16x32_bf16 a[248:251], v[112:115], v[168:171], a[248:251]\n" \
"s_add_u32 m0, %[mB], 0x400\n" \
"s_add_u32 s85, s63, 0x8000\n" \
"v_mfma_f32_16x16x32_bf16 a[156:159], v[88:91], v[176:179], a[156:159]\n" \
"buffer_load_dwordx4 v26, s[68:71], s85 offen lds\n" \
"v_mfma_f32_16x16x32_bf16 a[156:159], v[84:87], v[172:175], a[156:159]\n" \
"ds_read_b64_tr_b16 v[168:169], v34 offset:57344\n" \
"ds_read_b64_tr_b16 v[170:171], v34 offset:58368\n" \
"v_mfma_f32_16x16x32_bf16 a[188:191], v[92:95], v[172:175], a[188:191]\n" \
"v_add_u32_e32 v217, s96, v216\n" \
"v_sub_u32_e32 v226, v224, v218\n" \
"v_mfma_f32_16x16x32_bf16 a[188:191], v[96:99], v[176:179], a[188:191]\n" \
"s_add_u32 m0, %[mB], 0x4400\n" \
"s_add_u32 s84, s63, 0x8000\n" \
"v_mfma_f32_16x16x32_bf16 a[220:223], v[104:107], v[176:179], a[220:223]\n" \
"buffer_load_dwordx4 v26, s[72:75], s84 offen lds\n" \
"v_mfma_f32_16x16x32_bf16 a[220:223], v[100:103], v[172:175], a[220:223]\n" \
"v_mul_u32_u24_e32 v228, 0x480, v230\n" \
"v_and_b32_e32 v220, 0xff, v217\n" \
"v_mfma_f32_16x16x32_bf16 a[252:255], v[108:111], v[172:175], a[252:255]\n" \
"ds_read_b64_tr_b16 v[172:173], v35 offset:49152\n" \
"ds_read_b64_tr_b16 v[174:175], v35 offset:50176\n" \
"v_mfma_f32_16x16x32_bf16 a[252:255], v[112:115], v[176:179], a[252:255]\n" \
"s_add_u32 m0, %[mA], 0x1000\n" \
"s_add_u32 s85, s62, 0x8000\n" \
"v_mfma_f32_16x16x32_bf16 a[128:131], v[120:123], v[88:91], a[128:131]\n" \
"buffer_load_dwordx4 v22, s[64:67], s85 offen lds\n" \
"v_mfma_f32_16x16x32_bf16 a[128:131], v[116:119], v[84:87], a[128:131]\n" \
"ds_read_b64_tr_b16 v[176:177], v35 offset:57344\n" \
"ds_read_b64_tr_b16 v[178:179], v35 offset:58368\n" \
"v_mfma_f32_16x16x32_bf16 a[160:163], v[116:119], v[92:95], a[160:163]\n" \
"v_cmp_gt_u32_e32 vcc, 16, v214\n" \
"v_lshlrev_b32_e32 v227, 4, v226\n" \
"v_mfma_f32_16x16x32_bf16 a[160:163], v[120:123], v[96:99], a[160:163]\n" \
"s_add_u32 m0, %[mA], 0x1400\n" \
"s_add_u32 s84, s62, 0xa000\n" \
"v_mfma_f32_16x16x32_bf16 a[192:195], v[120:123], v[104:107], a[192:195]\n" \
"buffer_load_dwordx4 v23, s[64:67], s84 offen lds\n" \
"v_mfma_f32_16x16x32_bf16 a[192:195], v[116:119], v[100:103], a[192:195]\n" \
"v_cndmask_b32_e32 v219, v216, v220, vcc\n" \
"v_add_u32_e32 v230, s96, v227\n" \
"v_mfma_f32_16x16x32_bf16 a[224:227], v[116:119], v[108:111], a[224:227]\n" \
"ds_read_b64_tr_b16 v[116:117], v32 offset:32768\n" \
"ds_read_b64_tr_b16 v[118:119], v32 offset:33792\n" \
"v_mfma_f32_16x16x32_bf16 a[224:227], v[120:123], v[112:115], a[224:227]\n" \
"s_add_u32 m0, %[mB], 0x800\n" \
"s_add_u32 s85, s63, 0x10000\n" \
"v_mfma_f32_16x16x32_bf16 a[132:135], v[128:131], v[88:91], a[132:135]\n" \
"buffer_load_dwordx4 v27, s[68:71], s85 offen lds\n" \
"v_mfma_f32_16x16x32_bf16 a[132:135], v[124:127], v[84:87], a[132:135]\n" \
"ds_read_b64_tr_b16 v[120:121], v32 offset:40960\n" \
"ds_read_b64_tr_b16 v[122:123], v32 offset:41984\n" \
"v_mfma_f32_16x16x32_bf16 a[164:167], v[124:127], v[92:95], a[164:167]\n" \
"v_and_b32_e32 v229, 0xff, v230\n" \
"v_cmp_gt_u32_e32 vcc, 16, v226\n" \
"v_mfma_f32_16x16x32_bf16 a[164:167], v[128:131], v[96:99], a[164:167]\n" \
"s_add_u32 m0, %[mB], 0x4800\n" \
"s_add_u32 s84, s63, 0x10000\n" \
"v_mfma_f32_16x16x32_bf16 a[196:199], v[128:131], v[104:107], a[196:199]\n" \
"buffer_load_dwordx4 v27, s[72:75], s84 offen lds\n" \
"v_mfma_f32_16x16x32_bf16 a[196:199], v[124:127], v[100:103], a[196:199]\n" \
"v_mul_u32_u24_e32 v250, 0x480, v213\n" \
"v_cndmask_b32_e32 v225, v227, v229, vcc\n" \
"v_mfma_f32_16x16x32_bf16 a[228:231], v[124:127], v[108:111], a[228:231]\n" \
"ds_read_b64_tr_b16 v[124:125], v33 offset:32768\n" \
"ds_read_b64_tr_b16 v[126:127], v33 offset:33792\n" \
"v_mfma_f32_16x16x32_bf16 a[228:231], v[128:131], v[112:115], a[228:231]\n" \
"ds_read_b64_tr_b16 v[128:129], v33 offset:40960\n" \
"ds_read_b64_tr_b16 v[130:131], v33 offset:41984\n" \
"v_mfma_f32_16x16x32_bf16 a[136:139], v[136:139], v[88:91], a[136:139]\n" \
"v_and_b32_e64 v249, 2, v212\n" \
"v_mov_b32_e32 v213, 0x100\n" \
"v_mfma_f32_16x16x32_bf16 a[136:139], v[132:135], v[84:87], a[136:139]\n" \
"v_mov_b32_e32 v251, s95\n" \
"v_cmp_ne_u32_e32 vcc, 0, v249\n" \
"v_mfma_f32_16x16x32_bf16 a[168:171], v[132:135], v[92:95], a[168:171]\n" \
"s_mov_b32 s76, s8\n" \
"v_cndmask_b32_e32 v247, v251, v213, vcc\n" \
"v_mfma_f32_16x16x32_bf16 a[168:171], v[136:139], v[96:99], a[168:171]\n" \
"s_and_b32 s77, s9, 0xffff\n" \
"s_mov_b32 s78, 0x240000\n" \
"v_mfma_f32_16x16x32_bf16 a[200:203], v[136:139], v[104:107], a[200:203]\n" \
"v_xor_b32_e32 v37, 0x20, v190\n" \
"v_xor_b32_e32 v38, 0x40, v190\n" \
"v_mfma_f32_16x16x32_bf16 a[200:203], v[132:135], v[100:103], a[200:203]\n" \
"v_add_u32_e32 v48, s60, v28\n" \
"v_add_u32_e32 v49, s60, v29\n" \
"v_mfma_f32_16x16x32_bf16 a[232:235], v[132:135], v[108:111], a[232:235]\n" \
"ds_read_b64_tr_b16 v[132:133], v34 offset:32768\n" \
"ds_read_b64_tr_b16 v[134:135], v34 offset:33792\n" \
"v_mfma_f32_16x16x32_bf16 a[232:235], v[136:139], v[112:115], a[232:235]\n" \
"ds_read_b64_tr_b16 v[136:137], v34 offset:40960\n" \
"ds_read_b64_tr_b16 v[138:139], v34 offset:41984\n" \
"v_mfma_f32_16x16x32_bf16 a[140:143], v[144:147], v[88:91], a[140:143]\n" \
"ds_read_b128 v[88:91], v29 offset:32768\n" \
"v_xor_b32_e64 v36, 0x0, v190\n" \
"v_mfma_f32_16x16x32_bf16 a[140:143], v[140:143], v[84:87], a[140:143]\n" \
"ds_read_b128 v[84:87], v28 offset:32768\n" \
"v_add_u32_e64 v237, v228, v233\n" \
"v_mfma_f32_16x16x32_bf16 a[172:175], v[140:143], v[92:95], a[172:175]\n" \
"ds_read_b128 v[92:95], v28 offset:34816\n" \
"s_lshl_b32 s97, %[w], 4\n" \
"v_mfma_f32_16x16x32_bf16 a[172:175], v[144:147], v[96:99], a[172:175]\n" \
"ds_read_b128 v[96:99], v29 offset:34816\n" \
"v_lshrrev_b32_e32 v234, 2, v212\n" \
"v_mfma_f32_16x16x32_bf16 a[204:207], v[144:147], v[104:107], a[204:207]\n" \
"ds_read_b128 v[104:107], v29 offset:36864\n" \
"v_add_u32_e32 v5, 0xfffffc00, v237\n" \
"v_mfma_f32_16x16x32_bf16 a[204:207], v[140:143], v[100:103], a[204:207]\n" \
"ds_read_b128 v[100:103], v28 offset:36864\n" \
"v_add_u32_e64 v231, s97, v234\n" \
"v_mfma_f32_16x16x32_bf16 a[236:239], v[140:143], v[108:111], a[236:239]\n" \
"ds_read_b64_tr_b16 v[140:141], v35 offset:32768\n" \
"ds_read_b64_tr_b16 v[142:143], v35 offset:33792\n" \
"v_mfma_f32_16x16x32_bf16 a[236:239], v[144:147], v[112:115], a[236:239]\n" \
"s_add_u32 m0, %[mA], 0x1800\n" \
"s_waitcnt lgkmcnt(12)\n" \
"s_nop 0\n" \
"v_mfma_f32_16x16x32_bf16 a[16:19], v[56:59], v[152:155], a[16:19]\n" \
"s_add_u32 s85, s62, 0xc000\n" \
"ds_read_b64_tr_b16 v[144:145], v35 offset:40960\n" \
"v_mfma_f32_16x16x32_bf16 a[16:19], v[52:55], v[148:151], a[16:19]\n" \
"buffer_load_dwordx4 v22, s[64:67], s85 offen lds\n" \
"v_mfma_f32_16x16x32_bf16 a[48:51], v[60:63], v[148:151], a[48:51]\n" \
"s_add_u32 m0, %[mA], 0x1c00\n" \
"s_add_u32 s84, s62, 0xe000\n" \
"v_mfma_f32_16x16x32_bf16 a[48:51], v[64:67], v[152:155], a[48:51]\n" \
"ds_read_b64_tr_b16 v[146:147], v35 offset:41984\n" \
"ds_read_b128 v[108:111], v28 offset:38912\n" \
"v_mfma_f32_16x16x32_bf16 a[80:83], v[72:75], v[152:155], a[80:83]\n" \
"buffer_load_dwordx4 v23, s[64:67], s84 offen lds\n" \
"v_mfma_f32_16x16x32_bf16 a[80:83], v[68:71], v[148:151], a[80:83]\n" \
"s_add_u32 m0, %[mB], 0xc00\n" \
"s_add_u32 s85, s63, 0x18000\n" \
"v_mfma_f32_16x16x32_bf16 a[112:115], v[76:79], v[148:151], a[112:115]\n" \
"ds_read_b128 v[112:115], v29 offset:38912\n" \
"v_lshrrev_b32_e32 v235, 5, v231\n" \
"v_mfma_f32_16x16x32_bf16 a[112:115], v[80:83], v[152:155], a[112:115]\n" \
"buffer_load_dwordx4 v27, s[68:71], s85 offen lds\n" \
"v_mfma_f32_16x16x32_bf16 a[20:23], v[56:59], v[160:163], a[20:23]\n" \
"s_add_u32 m0, %[mB], 0x4c00\n" \
"s_add_u32 s84, s63, 0x18000\n" \
"v_mfma_f32_16x16x32_bf16 a[20:23], v[52:55], v[156:159], a[20:23]\n" \
"v_lshrrev_b32_e32 v237, 4, v231\n" \
"v_mul_u32_u24_e32 v239, 0x480, v241\n" \
"v_mfma_f32_16x16x32_bf16 a[52:55], v[60:63], v[156:159], a[52:55]\n" \
"buffer_load_dwordx4 v27, s[72:75], s84 offen lds\n" \
"v_mfma_f32_16x16x32_bf16 a[52:55], v[64:67], v[160:163], a[52:55]\n" \
"v_lshlrev_b32_e32 v232, 4, v235\n" \
"v_and_b32_e32 v228, 12, v231\n" \
"v_mfma_f32_16x16x32_bf16 a[84:87], v[72:75], v[160:163], a[84:87]\n" \
"v_and_b32_e32 v238, 1, v237\n" \
"v_add_u32_e32 v233, v232, v228\n" \
"v_mfma_f32_16x16x32_bf16 a[84:87], v[68:71], v[156:159], a[84:87]\n" \
"v_lshlrev_b32_e32 v241, 1, v238\n" \
"v_lshrrev_b32_e32 v236, 1, v231\n" \
"v_mfma_f32_16x16x32_bf16 a[116:119], v[76:79], v[156:159], a[116:119]\n" \
"v_add_u32_e32 v240, v233, v241\n" \
"v_and_b32_e32 v245, 1, v236\n" \
"v_mfma_f32_16x16x32_bf16 a[116:119], v[80:83], v[160:163], a[116:119]\n" \
"s_mov_b32 m0, %[mS]\n" \
"s_mov_b32 s86, s8\n" \
"v_mfma_f32_16x16x32_bf16 a[24:27], v[56:59], v[168:171], a[24:27]\n" \
"s_mov_b32 s87, s9\n" \
"v_add_u32_e32 v242, v240, v245\n" \
"v_mfma_f32_16x16x32_bf16 a[24:27], v[52:55], v[164:167], a[24:27]\n" \
"v_add_u32_e32 v248, v239, v244\n" \
"v_mul_u32_u24_e32 v246, 18, v242\n" \
"v_mfma_f32_16x16x32_bf16 a[56:59], v[60:63], v[164:167], a[56:59]\n" \
"v_add_u32_e32 v243, 16, v246\n" \
"v_and_b32_e32 v239, 1, v231\n" \
"v_mfma_f32_16x16x32_bf16 a[56:59], v[64:67], v[168:171], a[56:59]\n" \
"v_add_u32_e32 v6, 0xfffff800, v248\n" \
"v_add_u32_e64 v244, v243, v239\n" \
"v_mfma_f32_16x16x32_bf16 a[88:91], v[72:75], v[168:171], a[88:91]\n" \
"v_mul_u32_u24_e32 v248, 0x480, v244\n" \
"v_add_u32_e64 v215, v250, v219\n" \
"v_mfma_f32_16x16x32_bf16 a[88:91], v[68:71], v[164:167], a[88:91]\n" \
"v_add_u32_e32 v217, v248, v247\n" \
"v_and_b32_e32 v214, 16, v20\n" \
"s_mov_b32 exec_lo, 0x0\n" \
"s_mov_b32 exec_hi, 0x3ffff0\n" \
"global_load_lds_dword v188, s[86:87]\n" \
"s_mov_b64 exec, -1\n" \
"v_mfma_f32_16x16x32_bf16 a[120:123], v[76:79], v[164:167], a[120:123]\n" \
"v_add_u32_e32 v7, 0xfffff400, v215\n" \
"v_add_u32_e64 v9, v217, v214\n" \
"v_mfma_f32_16x16x32_bf16 a[120:123], v[80:83], v[168:171], a[120:123]\n" \
"v_and_b32_e32 v216, 3, v212\n" \
"v_lshlrev_b32_e32 v250, 3, v216\n" \
"v_mfma_f32_16x16x32_bf16 a[28:31], v[56:59], v[176:179], a[28:31]\n" \
"v_mul_u32_u24_e32 v220, 0x120, v234\n" \
"v_mul_u32_u24_e32 v223, 0x480, v222\n" \
"v_mfma_f32_16x16x32_bf16 a[28:31], v[52:55], v[172:175], a[28:31]\n" \
"v_add_u32_e32 v10, v220, v250\n" \
"v_add_u32_e32 v8, v223, v225\n" \
"v_mfma_f32_16x16x32_bf16 a[60:63], v[60:63], v[172:175], a[60:63]\n" \
"s_lshl_b32 s99, %[w], 11\n" \
"s_add_u32 s55, s98, s99\n" \
"v_mfma_f32_16x16x32_bf16 a[60:63], v[64:67], v[176:179], a[60:63]\n" \
"v_mfma_f32_16x16x32_bf16 a[92:95], v[72:75], v[176:179], a[92:95]\n" \
"v_mfma_f32_16x16x32_bf16 a[92:95], v[68:71], v[172:175], a[92:95]\n" \
"v_mfma_f32_16x16x32_bf16 a[124:127], v[76:79], v[172:175], a[124:127]\n" \
"v_mfma_f32_16x16x32_bf16 a[124:127], v[80:83], v[176:179], a[124:127]\n" \
"v_mfma_f32_16x16x32_bf16 a[0:3], v[120:123], v[56:59], a[0:3]\n" \
"v_mfma_f32_16x16x32_bf16 a[0:3], v[116:119], v[52:55], a[0:3]\n" \
"v_mfma_f32_16x16x32_bf16 a[32:35], v[116:119], v[60:63], a[32:35]\n" \
"v_mfma_f32_16x16x32_bf16 a[32:35], v[120:123], v[64:67], a[32:35]\n" \
"v_mfma_f32_16x16x32_bf16 a[64:67], v[120:123], v[72:75], a[64:67]\n" \
"v_mfma_f32_16x16x32_bf16 a[64:67], v[116:119], v[68:71], a[64:67]\n" \
"v_mfma_f32_16x16x32_bf16 a[96:99], v[116:119], v[76:79], a[96:99]\n" \
"v_mfma_f32_16x16x32_bf16 a[96:99], v[120:123], v[80:83], a[96:99]\n" \
"v_mfma_f32_16x16x32_bf16 a[4:7], v[128:131], v[56:59], a[4:7]\n" \
"v_mfma_f32_16x16x32_bf16 a[4:7], v[124:127], v[52:55], a[4:7]\n" \
"v_mfma_f32_16x16x32_bf16 a[36:39], v[124:127], v[60:63], a[36:39]\n" \
"v_mfma_f32_16x16x32_bf16 a[36:39], v[128:131], v[64:67], a[36:39]\n" \
"v_mfma_f32_16x16x32_bf16 a[68:71], v[128:131], v[72:75], a[68:71]\n" \
"v_mfma_f32_16x16x32_bf16 a[68:71], v[124:127], v[68:71], a[68:71]\n" \
"v_mfma_f32_16x16x32_bf16 a[100:103], v[124:127], v[76:79], a[100:103]\n" \
"v_mfma_f32_16x16x32_bf16 a[100:103], v[128:131], v[80:83], a[100:103]\n" \
"s_waitcnt lgkmcnt(0)\n" \
"s_nop 0\n" \
"v_mfma_f32_16x16x32_bf16 a[8:11], v[136:139], v[56:59], a[8:11]\n" \
"v_mfma_f32_16x16x32_bf16 a[8:11], v[132:135], v[52:55], a[8:11]\n" \
"v_mfma_f32_16x16x32_bf16 a[40:43], v[132:135], v[60:63], a[40:43]\n" \
"v_mfma_f32_16x16x32_bf16 a[40:43], v[136:139], v[64:67], a[40:43]\n" \
"v_mfma_f32_16x16x32_bf16 a[72:75], v[136:139], v[72:75], a[72:75]\n" \
"v_mfma_f32_16x16x32_bf16 a[72:75], v[132:135], v[68:71], a[72:75]\n" \
"v_mfma_f32_16x16x32_bf16 a[104:107], v[132:135], v[76:79], a[104:107]\n" \
"v_mfma_f32_16x16x32_bf16 a[104:107], v[136:139], v[80:83], a[104:107]\n" \
"v_mfma_f32_16x16x32_bf16 a[12:15], v[144:147], v[56:59], a[12:15]\n" \
"s_add_u32 s62, s22, 0x380\n" \
"s_and_b32 s62, s62, 0x3ff\n" \
"v_mfma_f32_16x16x32_bf16 a[12:15], v[140:143], v[52:55], a[12:15]\n" \
"s_lshl_b32 s63, s62, 12\n" \
"v_mfma_f32_16x16x32_bf16 a[44:47], v[140:143], v[60:63], a[44:47]\n" \
"v_mfma_f32_16x16x32_bf16 a[44:47], v[144:147], v[64:67], a[44:47]\n" \
"v_mfma_f32_16x16x32_bf16 a[76:79], v[144:147], v[72:75], a[76:79]\n" \
"v_mfma_f32_16x16x32_bf16 a[76:79], v[140:143], v[68:71], a[76:79]\n" \
"v_mfma_f32_16x16x32_bf16 a[108:111], v[140:143], v[76:79], a[108:111]\n" \
"v_mfma_f32_16x16x32_bf16 a[108:111], v[144:147], v[80:83], a[108:111]\n" \
"s_waitcnt vmcnt(1)\n" \
"s_barrier\n" \
"s_mov_b64 exec, 1\n" \
"buffer_store_dword v45, off, s[80:83], s38 offset:64 sc1\n" \
"s_mov_b64 exec, -1\n" \
"v_mfma_f32_16x16x32_bf16 a[128:131], v[120:123], v[88:91], a[128:131]\n" \
"s_add_u32 m0, %[mA], 0x8000\n" \
"s_add_u32 s85, s62, 0x0\n" \
"v_mfma_f32_16x16x32_bf16 a[128:131], v[116:119], v[84:87], a[128:131]\n" \
"ds_read_b128 v[56:59], v29 offset:16384\n" \
"ds_read_b128 v[52:55], v28 offset:16384\n" \
"v_mfma_f32_16x16x32_bf16 a[160:163], v[116:119], v[92:95], a[160:163]\n" \
"buffer_load_dwordx4 v22, s[64:67], s85 offen lds\n" \
"v_mfma_f32_16x16x32_bf16 a[160:163], v[120:123], v[96:99], a[160:163]\n" \
"s_add_u32 m0, %[mA], 0x8400\n" \
"s_add_u32 s84, s62, 0x2000\n" \
"v_mfma_f32_16x16x32_bf16 a[192:195], v[120:123], v[104:107], a[192:195]\n" \
"ds_read_b128 v[60:63], v28 offset:18432\n" \
"ds_read_b128 v[64:67], v29 offset:18432\n" \
"v_mfma_f32_16x16x32_bf16 a[192:195], v[116:119], v[100:103], a[192:195]\n" \
"buffer_load_dwordx4 v23, s[64:67], s84 offen lds\n" \
"v_mfma_f32_16x16x32_bf16 a[224:227], v[116:119], v[108:111], a[224:227]\n" \
"ds_read_b64_tr_b16 v[116:117], v32 offset:0\n" \
"ds_read_b64_tr_b16 v[118:119], v32 offset:1024\n" \
"v_mfma_f32_16x16x32_bf16 a[224:227], v[120:123], v[112:115], a[224:227]\n" \
"s_add_u32 m0, %[mB], 0x8000\n" \
"s_add_u32 s85, s63, 0x0\n" \
"v_mfma_f32_16x16x32_bf16 a[132:135], v[128:131], v[88:91], a[132:135]\n" \
"buffer_load_dwordx4 v26, s[68:71], s85 offen lds\n" \
"v_mfma_f32_16x16x32_bf16 a[132:135], v[124:127], v[84:87], a[132:135]\n" \
"ds_read_b64_tr_b16 v[120:121], v32 offset:8192\n" \
"ds_read_b64_tr_b16 v[122:123], v32 offset:9216\n" \
"v_mfma_f32_16x16x32_bf16 a[164:167], v[124:127], v[92:95], a[164:167]\n" \
"ds_read_b128 v[72:75], v29 offset:20480\n" \
"ds_read_b128 v[68:71], v28 offset:20480\n" \
"v_mfma_f32_16x16x32_bf16 a[164:167], v[128:131], v[96:99], a[164:167]\n" \
"s_add_u32 m0, %[mB], 0xc000\n" \
"s_add_u32 s84, s63, 0x0\n" \
"v_mfma_f32_16x16x32_bf16 a[196:199], v[128:131], v[104:107], a[196:199]\n" \
"buffer_load_dwordx4 v26, s[72:75], s84 offen lds\n" \
"v_mfma_f32_16x16x32_bf16 a[196:199], v[124:127], v[100:103], a[196:199]\n" \
"ds_read_b128 v[76:79], v28 offset:22528\n" \
"ds_read_b128 v[80:83], v29 offset:22528\n" \
"v_mfma_f32_16x16x32_bf16 a[228:231], v[124:127], v[108:111], a[228:231]\n" \
"ds_read_b64_tr_b16 v[124:125], v33 offset:0\n" \
"ds_read_b64_tr_b16 v[126:127], v33 offset:1024\n" \
"v_mfma_f32_16x16x32_bf16 a[228:231], v[128:131], v[112:115], a[228:231]\n" \
"s_add_u32 m0, %[mA], 0x8800\n" \
"s_add_u32 s85, s62, 0x4000\n" \
"v_mfma_f32_16x16x32_bf16 a[136:139], v[136:139], v[88:91], a[136:139]\n" \
"buffer_load_dwordx4 v22, s[64:67], s85 offen lds\n" \
"v_mfma_f32_16x16x32_bf16 a[136:139], v[132:135], v[84:87], a[136:139]\n" \
"ds_read_b64_tr_b16 v[128:129], v33 offset:8192\n" \
"ds_read_b64_tr_b16 v[130:131], v33 offset:9216\n" \
"v_mfma_f32_16x16x32_bf16 a[168:171], v[132:135], v[92:95], a[168:171]\n" \
"v_mov_b32_e64 v45, %[tag]\n" \
"v_mfma_f32_16x16x32_bf16 a[168:171], v[136:139], v[96:99], a[168:171]\n" \
"s_add_u32 m0, %[mA], 0x8c00\n" \
"s_add_u32 s84, s62, 0x6000\n" \
"v_mfma_f32_16x16x32_bf16 a[200:203], v[136:139], v[104:107], a[200:203]\n" \
"buffer_load_dwordx4 v23, s[64:67], s84 offen lds\n" \
"v_mfma_f32_16x16x32_bf16 a[200:203], v[132:135], v[100:103], a[200:203]\n" \
"v_mfma_f32_16x16x32_bf16 a[232:235], v[132:135], v[108:111], a[232:235]\n" \
"ds_read_b64_tr_b16 v[132:133], v34 offset:0\n" \
"ds_read_b64_tr_b16 v[134:135], v34 offset:1024\n" \
"v_mfma_f32_16x16x32_bf16 a[232:235], v[136:139], v[112:115], a[232:235]\n" \
"s_add_u32 m0, %[mB], 0x8400\n" \
"s_add_u32 s85, s63, 0x8000\n" \
"v_mfma_f32_16x16x32_bf16 a[140:143], v[144:147], v[88:91], a[140:143]\n" \
"buffer_load_dwordx4 v26, s[68:71], s85 offen lds\n" \
"v_mfma_f32_16x16x32_bf16 a[140:143], v[140:143], v[84:87], a[140:143]\n" \
"ds_read_b64_tr_b16 v[136:137], v34 offset:8192\n" \
"ds_read_b64_tr_b16 v[138:139], v34 offset:9216\n" \
"v_mfma_f32_16x16x32_bf16 a[172:175], v[140:143], v[92:95], a[172:175]\n" \
"v_mfma_f32_16x16x32_bf16 a[172:175], v[144:147], v[96:99], a[172:175]\n" \
"s_add_u32 m0, %[mB], 0xc400\n" \
"s_add_u32 s84, s63, 0x8000\n" \
"v_mfma_f32_16x16x32_bf16 a[204:207], v[144:147], v[104:107], a[204:207]\n" \
"buffer_load_dwordx4 v26, s[72:75], s84 offen lds\n" \
"v_mfma_f32_16x16x32_bf16 a[204:207], v[140:143], v[100:103], a[204:207]\n" \
"v_mfma_f32_16x16x32_bf16 a[236:239], v[140:143], v[108:111], a[236:239]\n" \
"ds_read_b64_tr_b16 v[140:141], v35 offset:0\n" \
"ds_read_b64_tr_b16 v[142:143], v35 offset:1024\n" \
"v_mfma_f32_16x16x32_bf16 a[236:239], v[144:147], v[112:115], a[236:239]\n" \
"s_add_u32 m0, %[mA], 0x9000\n" \
"s_add_u32 s85, s62, 0x8000\n" \
"v_mfma_f32_16x16x32_bf16 a[144:147], v[88:91], v[152:155], a[144:147]\n" \
"buffer_load_dwordx4 v22, s[64:67], s85 offen lds\n" \
"v_mfma_f32_16x16x32_bf16 a[144:147], v[84:87], v[148:151], a[144:147]\n" \
"ds_read_b64_tr_b16 v[144:145], v35 offset:8192\n" \
"ds_read_b64_tr_b16 v[146:147], v35 offset:9216\n" \
"v_mfma_f32_16x16x32_bf16 a[176:179], v[92:95], v[148:151], a[176:179]\n" \
"v_mfma_f32_16x16x32_bf16 a[176:179], v[96:99], v[152:155], a[176:179]\n" \
"s_add_u32 m0, %[mA], 0x9400\n" \
"s_add_u32 s84, s62, 0xa000\n" \
"v_mfma_f32_16x16x32_bf16 a[208:211], v[104:107], v[152:155], a[208:211]\n" \
"buffer_load_dwordx4 v23, s[64:67], s84 offen lds\n" \
"v_mfma_f32_16x16x32_bf16 a[208:211], v[100:103], v[148:151], a[208:211]\n" \
"v_mfma_f32_16x16x32_bf16 a[240:243], v[108:111], v[148:151], a[240:243]\n" \
"ds_read_b64_tr_b16 v[148:149], v32 offset:16384\n" \
"L_pf3:\n" \
"s_cbranch_execz L_pf7\n" \
"s_nop 0\n" \
"ds_read_b64_tr_b16 v[150:151], v32 offset:17408\n" \
"v_mfma_f32_16x16x32_bf16 a[240:243], v[112:115], v[152:155], a[240:243]\n" \
"s_add_u32 m0, %[mB], 0x8800\n" \
"s_add_u32 s85, s63, 0x10000\n" \
"v_mfma_f32_16x16x32_bf16 a[148:151], v[88:91], v[160:163], a[148:151]\n" \
"buffer_load_dwordx4 v27, s[68:71], s85 offen lds\n" \
"v_mfma_f32_16x16x32_bf16 a[148:151], v[84:87], v[156:159], a[148:151]\n" \
"ds_read_b64_tr_b16 v[152:153], v32 offset:24576\n" \
"ds_read_b64_tr_b16 v[154:155], v32 offset:25600\n" \
"v_mfma_f32_16x16x32_bf16 a[180:183], v[92:95], v[156:159], a[180:183]\n" \
"v_mfma_f32_16x16x32_bf16 a[180:183], v[96:99], v[160:163], a[180:183]\n" \
"s_add_u32 m0, %[mB], 0xc800\n" \
"s_add_u32 s84, s63, 0x10000\n" \
"v_mfma_f32_16x16x32_bf16 a[212:215], v[104:107], v[160:163], a[212:215]\n" \
"buffer_load_dwordx4 v27, s[72:75], s84 offen lds\n" \
"v_mfma_f32_16x16x32_bf16 a[212:215], v[100:103], v[156:159], a[212:215]\n" \
"v_mfma_f32_16x16x32_bf16 a[244:247], v[108:111], v[156:159], a[244:247]\n" \
"ds_read_b64_tr_b16 v[156:157], v33 offset:16384\n" \
"ds_read_b64_tr_b16 v[158:159], v33 offset:17408\n" \
"v_mfma_f32_16x16x32_bf16 a[244:247], v[112:115], v[160:163], a[244:247]\n" \
"ds_read_b64_tr_b16 v[160:161], v33 offset:24576\n" \
"ds_read_b64_tr_b16 v[162:163], v33 offset:25600\n" \
"v_mfma_f32_16x16x32_bf16 a[152:155], v[88:91], v[168:171], a[152:155]\n" \
"v_mfma_f32_16x16x32_bf16 a[152:155], v[84:87], v[164:167], a[152:155]\n" \
"v_mfma_f32_16x16x32_bf16 a[184:187], v[92:95], v[164:167], a[184:187]\n" \
"v_mfma_f32_16x16x32_bf16 a[184:187], v[96:99], v[168:171], a[184:187]\n" \
"v_mfma_f32_16x16x32_bf16 a[216:219], v[104:107], v[168:171], a[216:219]\n" \
"v_mfma_f32_16x16x32_bf16 a[216:219], v[100:103], v[164:167], a[216:219]\n" \
"v_mfma_f32_16x16x32_bf16 a[248:251], v[108:111], v[164:167], a[248:251]\n" \
"ds_read_b64_tr_b16 v[164:165], v34 offset:16384\n" \
"ds_read_b64_tr_b16 v[166:167], v34 offset:17408\n" \
"v_mfma_f32_16x16x32_bf16 a[248:251], v[112:115], v[168:171], a[248:251]\n" \
"ds_read_b64_tr_b16 v[168:169], v34 offset:24576\n" \
"ds_read_b64_tr_b16 v[170:171], v34 offset:25600\n" \
"v_mfma_f32_16x16x32_bf16 a[156:159], v[88:91], v[176:179], a[156:159]\n" \
"ds_read_b128 v[88:91], v29 offset:0\n" \
"v_mfma_f32_16x16x32_bf16 a[156:159], v[84:87], v[172:175], a[156:159]\n" \
"ds_read_b128 v[84:87], v28 offset:0\n" \
"v_mfma_f32_16x16x32_bf16 a[188:191], v[92:95], v[172:175], a[188:191]\n" \
"ds_read_b128 v[92:95], v28 offset:2048\n" \
"v_mfma_f32_16x16x32_bf16 a[188:191], v[96:99], v[176:179], a[188:191]\n" \
"ds_read_b128 v[96:99], v29 offset:2048\n" \
"v_mfma_f32_16x16x32_bf16 a[220:223], v[104:107], v[176:179], a[220:223]\n" \
"ds_read_b128 v[104:107], v29 offset:4096\n" \
"v_mfma_f32_16x16x32_bf16 a[220:223], v[100:103], v[172:175], a[220:223]\n" \
"ds_read_b128 v[100:103], v28 offset:4096\n" \
"v_mfma_f32_16x16x32_bf16 a[252:255], v[108:111], v[172:175], a[252:255]\n" \
"ds_read_b64_tr_b16 v[172:173], v35 offset:16384\n" \
"ds_read_b64_tr_b16 v[174:175], v35 offset:17408\n" \
"v_mfma_f32_16x16x32_bf16 a[252:255], v[112:115], v[176:179], a[252:255]\n" \
"s_add_u32 m0, %[mA], 0x9800\n" \
"s_waitcnt lgkmcnt(12)\n" \
"s_nop 0\n" \
"v_mfma_f32_16x16x32_bf16 a[0:3], v[120:123], v[56:59], a[0:3]\n" \
"s_add_u32 s85, s62, 0xc000\n" \
"ds_read_b64_tr_b16 v[176:177], v35 offset:24576\n" \
"v_mfma_f32_16x16x32_bf16 a[0:3], v[116:119], v[52:55], a[0:3]\n" \
"buffer_load_dwordx4 v22, s[64:67], s85 offen lds\n" \
"v_mfma_f32_16x16x32_bf16 a[32:35], v[116:119], v[60:63], a[32:35]\n" \
"s_add_u32 m0, %[mA], 0x9c00\n" \
"s_add_u32 s84, s62, 0xe000\n" \
"v_mfma_f32_16x16x32_bf16 a[32:35], v[120:123], v[64:67], a[32:35]\n" \
"ds_read_b64_tr_b16 v[178:179], v35 offset:25600\n" \
"ds_read_b128 v[108:111], v28 offset:6144\n" \
"v_mfma_f32_16x16x32_bf16 a[64:67], v[120:123], v[72:75], a[64:67]\n" \
"buffer_load_dwordx4 v23, s[64:67], s84 offen lds\n" \
"v_mfma_f32_16x16x32_bf16 a[64:67], v[116:119], v[68:71], a[64:67]\n" \
"s_add_u32 m0, %[mB], 0x8c00\n" \
"s_add_u32 s85, s63, 0x18000\n" \
"v_mfma_f32_16x16x32_bf16 a[96:99], v[116:119], v[76:79], a[96:99]\n" \
"ds_read_b128 v[112:115], v29 offset:6144\n" \
"s_mov_b32 s62, s23\n" \
"v_mfma_f32_16x16x32_bf16 a[96:99], v[120:123], v[80:83], a[96:99]\n" \
"buffer_load_dwordx4 v27, s[68:71], s85 offen lds\n" \
"v_mfma_f32_16x16x32_bf16 a[4:7], v[128:131], v[56:59], a[4:7]\n" \
"s_add_u32 m0, %[mB], 0xcc00\n" \
"s_add_u32 s84, s63, 0x18000\n" \
"v_mfma_f32_16x16x32_bf16 a[4:7], v[124:127], v[52:55], a[4:7]\n" \
"s_mov_b32 s68, s10\n" \
"s_and_b32 s69, s11, 0xffff\n" \
"v_mfma_f32_16x16x32_bf16 a[36:39], v[124:127], v[60:63], a[36:39]\n" \
"buffer_load_dwordx4 v27, s[72:75], s84 offen lds\n" \
"s_mov_b32 s73, s52\n" \
"s_add_u32 s74, s52, 0x4000\n" \
"s_add_u32 s75, s52, 0x8000\n" \
"v_mfma_f32_16x16x32_bf16 a[36:39], v[128:131], v[64:67], a[36:39]\n" \
"s_lshl_b32 s72, s62, 6\n" \
"s_add_u32 s72, s72, s37\n" \
"v_mfma_f32_16x16x32_bf16 a[68:71], v[128:131], v[72:75], a[68:71]\n" \
"v_bfe_u32 v44, v20, 2, 5\n" \
"s_mov_b32 s70, 0x480000\n" \
"v_mfma_f32_16x16x32_bf16 a[68:71], v[124:127], v[68:71], a[68:71]\n" \
"buffer_load_dword v44, v44, s[80:83], s39 offen offset:64 sc1\n" \
"v_mfma_f32_16x16x32_bf16 a[100:103], v[124:127], v[76:79], a[100:103]\n" \
"v_add_u32_e32 v22, s61, v28\n" \
"v_add_u32_e64 v23, s61, v29\n" \
"v_mfma_f32_16x16x32_bf16 a[100:103], v[128:131], v[80:83], a[100:103]\n" \
"s_mov_b32 m0, %[mS]\n" \
"s_mov_b32 s86, s18\n" \
"v_mfma_f32_16x16x32_bf16 a[8:11], v[136:139], v[56:59], a[8:11]\n" \
"s_mov_b32 s87, s19\n" \
"v_mfma_f32_16x16x32_bf16 a[8:11], v[132:135], v[52:55], a[8:11]\n" \
"v_mfma_f32_16x16x32_bf16 a[40:43], v[132:135], v[60:63], a[40:43]\n" \
"v_mfma_f32_16x16x32_bf16 a[40:43], v[136:139], v[64:67], a[40:43]\n" \
"v_mfma_f32_16x16x32_bf16 a[72:75], v[136:139], v[72:75], a[72:75]\n" \
"v_mfma_f32_16x16x32_bf16 a[72:75], v[132:135], v[68:71], a[72:75]\n" \
"s_mov_b32 exec_lo, 0xfffc0000\n" \
"s_mov_b32 exec_hi, 0xf\n" \
"global_load_lds_dword v16, s[86:87]\n" \
"s_mov_b64 exec, -1\n" \
"v_mfma_f32_16x16x32_bf16 a[104:107], v[132:135], v[76:79], a[104:107]\n" \
"s_mov_b32 m0, %[mS]\n" \
"s_mov_b32 s86, s16\n" \
"v_mfma_f32_16x16x32_bf16 a[104:107], v[136:139], v[80:83], a[104:107]\n" \
"s_mov_b32 s87, s17\n" \
"v_mfma_f32_16x16x32_bf16 a[12:15], v[144:147], v[56:59], a[12:15]\n" \
"v_mfma_f32_16x16x32_bf16 a[12:15], v[140:143], v[52:55], a[12:15]\n" \
"v_mfma_f32_16x16x32_bf16 a[44:47], v[140:143], v[60:63], a[44:47]\n" \
"s_mov_b32 exec_lo, 0x0\n" \
"s_mov_b32 exec_hi, 0x3f0\n" \
"global_load_lds_dword v16, s[86:87]\n" \
"s_mov_b64 exec, -1\n" \
"v_mfma_f32_16x16x32_bf16 a[44:47], v[144:147], v[64:67], a[44:47]\n" \
"v_mfma_f32_16x16x32_bf16 a[76:79], v[144:147], v[72:75], a[76:79]\n" \
"v_mfma_f32_16x16x32_bf16 a[76:79], v[140:143], v[68:71], a[76:79]\n" \
"v_mfma_f32_16x16x32_bf16 a[108:111], v[140:143], v[76:79], a[108:111]\n" \
"v_mfma_f32_16x16x32_bf16 a[108:111], v[144:147], v[80:83], a[108:111]\n" \
"v_mfma_f32_16x16x32_bf16 a[16:19], v[56:59], v[152:155], a[16:19]\n" \
"v_mfma_f32_16x16x32_bf16 a[16:19], v[52:55], v[148:151], a[16:19]\n" \
"v_mfma_f32_16x16x32_bf16 a[48:51], v[60:63], v[148:151], a[48:51]\n" \
"v_mfma_f32_16x16x32_bf16 a[48:51], v[64:67], v[152:155], a[48:51]\n" \
"v_mfma_f32_16x16x32_bf16 a[80:83], v[72:75], v[152:155], a[80:83]\n" \
"v_mfma_f32_16x16x32_bf16 a[80:83], v[68:71], v[148:151], a[80:83]\n" \
"v_mfma_f32_16x16x32_bf16 a[112:115], v[76:79], v[148:151], a[112:115]\n" \
"v_mfma_f32_16x16x32_bf16 a[112:115], v[80:83], v[152:155], a[112:115]\n" \
"v_mfma_f32_16x16x32_bf16 a[20:23], v[56:59], v[160:163], a[20:23]\n" \
"v_mfma_f32_16x16x32_bf16 a[20:23], v[52:55], v[156:159], a[20:23]\n" \
"v_mfma_f32_16x16x32_bf16 a[52:55], v[60:63], v[156:159], a[52:55]\n" \
"v_mfma_f32_16x16x32_bf16 a[52:55], v[64:67], v[160:163], a[52:55]\n" \
"v_mfma_f32_16x16x32_bf16 a[84:87], v[72:75], v[160:163], a[84:87]\n" \
"v_mfma_f32_16x16x32_bf16 a[84:87], v[68:71], v[156:159], a[84:87]\n" \
"v_mfma_f32_16x16x32_bf16 a[116:119], v[76:79], v[156:159], a[116:119]\n" \
"v_mfma_f32_16x16x32_bf16 a[116:119], v[80:83], v[160:163], a[116:119]\n" \
"s_waitcnt lgkmcnt(0)\n" \
"v_mfma_f32_16x16x32_bf16 a[24:27], v[56:59], v[168:171], a[24:27]\n" \
"v_mfma_f32_16x16x32_bf16 a[24:27], v[52:55], v[164:167], a[24:27]\n" \
"v_mfma_f32_16x16x32_bf16 a[56:59], v[60:63], v[164:167], a[56:59]\n" \
"v_mfma_f32_16x16x32_bf16 a[56:59], v[64:67], v[168:171], a[56:59]\n" \
"v_mfma_f32_16x16x32_bf16 a[88:91], v[72:75], v[168:171], a[88:91]\n" \
"v_mfma_f32_16x16x32_bf16 a[88:91], v[68:71], v[164:167], a[88:91]\n" \
"v_mfma_f32_16x16x32_bf16 a[120:123], v[76:79], v[164:167], a[120:123]\n" \
"v_mfma_f32_16x16x32_bf16 a[120:123], v[80:83], v[168:171], a[120:123]\n" \
"v_mfma_f32_16x16x32_bf16 a[28:31], v[56:59], v[176:179], a[28:31]\n" \
"s_waitcnt vmcnt(2)\n" \
"v_cmp_ne_u32_e32 vcc, %[tag], v44\n" \
"v_mfma_f32_16x16x32_bf16 a[28:31], v[52:55], v[172:175], a[28:31]\n" \
"v_mfma_f32_16x16x32_bf16 a[60:63], v[60:63], v[172:175], a[60:63]\n" \
"v_mfma_f32_16x16x32_bf16 a[60:63], v[64:67], v[176:179], a[60:63]\n" \
"v_mfma_f32_16x16x32_bf16 a[92:95], v[72:75], v[176:179], a[92:95]\n" \
"v_mfma_f32_16x16x32_bf16 a[92:95], v[68:71], v[172:175], a[92:95]\n" \
"s_cbranch_vccnz L_pl_%=\n" \
"L_rl_%=:\n" \
"s_lshl_b32 s63, s62, 12\n" \
"v_mfma_f32_16x16x32_bf16 a[124:127], v[76:79], v[172:175], a[124:127]\n" \
"v_mfma_f32_16x16x32_bf16 a[124:127], v[80:83], v[176:179], a[124:127]\n" \
"s_barrier\n" \
"v_mfma_f32_16x16x32_bf16 a[144:147], v[88:91], v[152:155], a[144:147]\n" \
"s_add_u32 m0, s40, 0x0\n" \
"s_add_u32 s85, s72, 0x0\n" \
"v_mfma_f32_16x16x32_bf16 a[144:147], v[84:87], v[148:151], a[144:147]\n" \
"buffer_load_dwordx4 v20, s[80:83], s85 offen sc1 lds\n" \
"v_mfma_f32_16x16x32_bf16 a[176:179], v[92:95], v[148:151], a[176:179]\n" \
"s_add_u32 m0, s40, 0x1000\n" \
"s_add_u32 s84, s72, 0x1000\n" \
"v_mfma_f32_16x16x32_bf16 a[176:179], v[96:99], v[152:155], a[176:179]\n" \
"ds_read_b128 v[56:59], v29 offset:49152\n" \
"ds_read_b128 v[52:55], v28 offset:49152\n" \
"v_mfma_f32_16x16x32_bf16 a[208:211], v[104:107], v[152:155], a[208:211]\n" \
"buffer_load_dwordx4 v20, s[80:83], s84 offen sc1 lds\n" \
"v_mfma_f32_16x16x32_bf16 a[208:211], v[100:103], v[148:151], a[208:211]\n" \
"ds_read_b128 v[60:63], v28 offset:51200\n" \
"ds_read_b128 v[64:67], v29 offset:51200\n" \
"v_mfma_f32_16x16x32_bf16 a[240:243], v[108:111], v[148:151], a[240:243]\n" \
"s_add_u32 m0, s40, 0x3000\n" \
"s_add_u32 s85, s62, 0x0\n" \
"v_mfma_f32_16x16x32_bf16 a[240:243], v[112:115], v[152:155], a[240:243]\n" \
"buffer_load_dwordx4 v21, s[76:79], s85 offen lds\n" \
"v_mfma_f32_16x16x32_bf16 a[148:151], v[88:91], v[160:163], a[148:151]\n" \
"ds_read_b64_tr_b16 v[152:153], v32 offset:57344\n" \
"ds_read_b64_tr_b16 v[154:155], v32 offset:58368\n" \
"v_mfma_f32_16x16x32_bf16 a[148:151], v[84:87], v[156:159], a[148:151]\n" \
"ds_read_b64_tr_b16 v[148:149], v32 offset:49152\n" \
"ds_read_b64_tr_b16 v[150:151], v32 offset:50176\n" \
"v_mfma_f32_16x16x32_bf16 a[180:183], v[92:95], v[156:159], a[180:183]\n" \
"s_add_u32 m0, %[mA], 0x0\n" \
"s_add_u32 s84, s62, 0x0\n" \
"s_nop 0\n" \
"v_mfma_f32_16x16x32_bf16 a[180:183], v[96:99], v[160:163], a[180:183]\n" \
"buffer_load_dwordx4 v24, s[76:79], s84 offen lds\n" \
"v_mfma_f32_16x16x32_bf16 a[212:215], v[104:107], v[160:163], a[212:215]\n" \
"ds_read_b128 v[72:75], v29 offset:53248\n" \
"ds_read_b128 v[68:71], v28 offset:53248\n" \
"v_mfma_f32_16x16x32_bf16 a[212:215], v[100:103], v[156:159], a[212:215]\n" \
"s_add_u32 m0, %[mA], 0x400\n" \
"s_add_u32 s85, s62, 0x2400\n" \
"v_mfma_f32_16x16x32_bf16 a[244:247], v[108:111], v[156:159], a[244:247]\n" \
"buffer_load_dwordx4 v25, s[76:79], s85 offen lds\n" \
"v_mfma_f32_16x16x32_bf16 a[244:247], v[112:115], v[160:163], a[244:247]\n" \
"ds_read_b128 v[76:79], v28 offset:55296\n" \
"ds_read_b128 v[80:83], v29 offset:55296\n" \
"v_mfma_f32_16x16x32_bf16 a[152:155], v[88:91], v[168:171], a[152:155]\n" \
"ds_read_b64_tr_b16 v[160:161], v33 offset:57344\n" \
"ds_read_b64_tr_b16 v[162:163], v33 offset:58368\n" \
"v_mfma_f32_16x16x32_bf16 a[152:155], v[84:87], v[164:167], a[152:155]\n" \
"s_add_u32 m0, %[mB], 0x0\n" \
"s_add_u32 s84, s63, 0x0\n" \
"v_mfma_f32_16x16x32_bf16 a[184:187], v[92:95], v[164:167], a[184:187]\n" \
"buffer_load_dwordx4 v26, s[68:71], s84 offen lds\n" \
"v_mfma_f32_16x16x32_bf16 a[184:187], v[96:99], v[168:171], a[184:187]\n" \
"ds_read_b64_tr_b16 v[156:157], v33 offset:49152\n" \
"ds_read_b64_tr_b16 v[158:159], v33 offset:50176\n" \
"v_mfma_f32_16x16x32_bf16 a[216:219], v[104:107], v[168:171], a[216:219]\n" \
"v_mfma_f32_16x16x32_bf16 a[216:219], v[100:103], v[164:167], a[216:219]\n" \
"s_add_u32 m0, %[mA], 0x800\n" \
"s_add_u32 s85, s62, 0x4800\n" \
"v_mfma_f32_16x16x32_bf16 a[248:251], v[108:111], v[164:167], a[248:251]\n" \
"buffer_load_dwordx4 v24, s[76:79], s85 offen lds\n" \
"v_mfma_f32_16x16x32_bf16 a[248:251], v[112:115], v[168:171], a[248:251]\n" \
"ds_read_b64_tr_b16 v[168:169], v34 offset:57344\n" \
"ds_read_b64_tr_b16 v[170:171], v34 offset:58368\n" \
"v_mfma_f32_16x16x32_bf16 a[156:159], v[88:91], v[176:179], a[156:159]\n" \
"s_add_u32 m0, %[mA], 0xc00\n" \
"s_add_u32 s84, s62, 0x6c00\n" \
"v_mfma_f32_16x16x32_bf16 a[156:159], v[84:87], v[172:175], a[156:159]\n" \
"buffer_load_dwordx4 v25, s[76:79], s84 offen lds\n" \
"v_mfma_f32_16x16x32_bf16 a[188:191], v[92:95], v[172:175], a[188:191]\n" \
"ds_read_b64_tr_b16 v[164:165], v34 offset:49152\n" \
"ds_read_b64_tr_b16 v[166:167], v34 offset:50176\n" \
"v_mfma_f32_16x16x32_bf16 a[188:191], v[96:99], v[176:179], a[188:191]\n" \
"v_mfma_f32_16x16x32_bf16 a[220:223], v[104:107], v[176:179], a[220:223]\n" \
"s_add_u32 m0, %[mB], 0x400\n" \
"s_add_u32 s85, s63, 0x8000\n" \
"v_mfma_f32_16x16x32_bf16 a[220:223], v[100:103], v[172:175], a[220:223]\n" \
"buffer_load_dwordx4 v26, s[68:71], s85 offen lds\n" \
"v_mfma_f32_16x16x32_bf16 a[252:255], v[108:111], v[172:175], a[252:255]\n" \
"ds_read_b64_tr_b16 v[172:173], v35 offset:49152\n" \
"ds_read_b64_tr_b16 v[174:175], v35 offset:50176\n" \
"v_mfma_f32_16x16x32_bf16 a[252:255], v[112:115], v[176:179], a[252:255]\n" \
"ds_read_b64_tr_b16 v[176:177], v35 offset:57344\n" \
"ds_read_b64_tr_b16 v[178:179], v35 offset:58368\n" \
"v_mfma_f32_16x16x32_bf16 a[128:131], v[120:123], v[88:91], a[128:131]\n" \
"s_add_u32 m0, %[mA], 0x1000\n" \
"s_add_u32 s84, s62, 0x9000\n" \
"v_mfma_f32_16x16x32_bf16 a[128:131], v[116:119], v[84:87], a[128:131]\n" \
"buffer_load_dwordx4 v24, s[76:79], s84 offen lds\n" \
"v_mfma_f32_16x16x32_bf16 a[160:163], v[116:119], v[92:95], a[160:163]\n" \
"v_mfma_f32_16x16x32_bf16 a[160:163], v[120:123], v[96:99], a[160:163]\n" \
"s_add_u32 m0, %[mA], 0x1400\n" \
"s_add_u32 s85, s62, 0xb400\n" \
"v_mfma_f32_16x16x32_bf16 a[192:195], v[120:123], v[104:107], a[192:195]\n" \
"buffer_load_dwordx4 v25, s[76:79], s85 offen lds\n" \
"v_mfma_f32_16x16x32_bf16 a[192:195], v[116:119], v[100:103], a[192:195]\n" \
"v_mfma_f32_16x16x32_bf16 a[224:227], v[116:119], v[108:111], a[224:227]\n" \
"ds_read_b64_tr_b16 v[116:117], v32 offset:32768\n" \
"ds_read_b64_tr_b16 v[118:119], v32 offset:33792\n" \
"v_mfma_f32_16x16x32_bf16 a[224:227], v[120:123], v[112:115], a[224:227]\n" \
"s_add_u32 m0, %[mB], 0x800\n" \
"s_add_u32 s84, s63, 0x10000\n" \
"v_mfma_f32_16x16x32_bf16 a[132:135], v[128:131], v[88:91], a[132:135]\n" \
"buffer_load_dwordx4 v27, s[68:71], s84 offen lds\n" \
"v_mfma_f32_16x16x32_bf16 a[132:135], v[124:127], v[84:87], a[132:135]\n" \
"ds_read_b64_tr_b16 v[120:121], v32 offset:40960\n" \
"ds_read_b64_tr_b16 v[122:123], v32 offset:41984\n" \
"v_mfma_f32_16x16x32_bf16 a[164:167], v[124:127], v[92:95], a[164:167]\n" \
"v_mfma_f32_16x16x32_bf16 a[164:167], v[128:131], v[96:99], a[164:167]\n" \
"s_add_u32 m0, %[mA], 0x1800\n" \
"s_add_u32 s85, s62, 0xd800\n" \
"v_mfma_f32_16x16x32_bf16 a[196:199], v[128:131], v[104:107], a[196:199]\n" \
"buffer_load_dwordx4 v24, s[76:79], s85 offen lds\n" \
"v_mfma_f32_16x16x32_bf16 a[196:199], v[124:127], v[100:103], a[196:199]\n" \
"v_mfma_f32_16x16x32_bf16 a[228:231], v[124:127], v[108:111], a[228:231]\n" \
"ds_read_b64_tr_b16 v[124:125], v33 offset:32768\n" \
"ds_read_b64_tr_b16 v[126:127], v33 offset:33792\n" \
"v_mfma_f32_16x16x32_bf16 a[228:231], v[128:131], v[112:115], a[228:231]\n" \
"ds_read_b64_tr_b16 v[128:129], v33 offset:40960\n" \
"ds_read_b64_tr_b16 v[130:131], v33 offset:41984\n" \
"v_mfma_f32_16x16x32_bf16 a[136:139], v[136:139], v[88:91], a[136:139]\n" \
"v_mfma_f32_16x16x32_bf16 a[136:139], v[132:135], v[84:87], a[136:139]\n" \
"v_mfma_f32_16x16x32_bf16 a[168:171], v[132:135], v[92:95], a[168:171]\n" \
"v_mfma_f32_16x16x32_bf16 a[168:171], v[136:139], v[96:99], a[168:171]\n" \
"v_mfma_f32_16x16x32_bf16 a[200:203], v[136:139], v[104:107], a[200:203]\n" \
"v_mfma_f32_16x16x32_bf16 a[200:203], v[132:135], v[100:103], a[200:203]\n" \
"v_mfma_f32_16x16x32_bf16 a[232:235], v[132:135], v[108:111], a[232:235]\n" \
"ds_read_b64_tr_b16 v[132:133], v34 offset:32768\n" \
"ds_read_b64_tr_b16 v[134:135], v34 offset:33792\n" \
"v_mfma_f32_16x16x32_bf16 a[232:235], v[136:139], v[112:115], a[232:235]\n" \
"ds_read_b64_tr_b16 v[136:137], v34 offset:40960\n" \
"ds_read_b64_tr_b16 v[138:139], v34 offset:41984\n" \
"v_mfma_f32_16x16x32_bf16 a[140:143], v[144:147], v[88:91], a[140:143]\n" \
"ds_read_b128 v[88:91], v29 offset:32768\n" \
"v_mfma_f32_16x16x32_bf16 a[140:143], v[140:143], v[84:87], a[140:143]\n" \
"ds_read_b128 v[84:87], v28 offset:32768\n" \
"v_mfma_f32_16x16x32_bf16 a[172:175], v[140:143], v[92:95], a[172:175]\n" \
"ds_read_b128 v[92:95], v28 offset:34816\n" \
"v_mfma_f32_16x16x32_bf16 a[172:175], v[144:147], v[96:99], a[172:175]\n" \
"ds_read_b128 v[96:99], v29 offset:34816\n" \
"v_mfma_f32_16x16x32_bf16 a[204:207], v[144:147], v[104:107], a[204:207]\n" \
"ds_read_b128 v[104:107], v29 offset:36864\n" \
"v_mfma_f32_16x16x32_bf16 a[204:207], v[140:143], v[100:103], a[204:207]\n" \
"ds_read_b128 v[100:103], v28 offset:36864\n" \
"v_mfma_f32_16x16x32_bf16 a[236:239], v[140:143], v[108:111], a[236:239]\n" \
"ds_read_b64_tr_b16 v[140:141], v35 offset:32768\n" \
"ds_read_b64_tr_b16 v[142:143], v35 offset:33792\n" \
"v_mfma_f32_16x16x32_bf16 a[236:239], v[144:147], v[112:115], a[236:239]\n" \
"s_add_u32 m0, %[mA], 0x1c00\n" \
"s_waitcnt lgkmcnt(12)\n" \
"s_nop 0\n" \
"v_mfma_f32_16x16x32_bf16 a[16:19], v[56:59], v[152:155], a[16:19]\n" \
"s_add_u32 s85, s62, 0xfc00\n" \
"ds_read_b64_tr_b16 v[144:145], v35 offset:40960\n" \
"v_mfma_f32_16x16x32_bf16 a[16:19], v[52:55], v[148:151], a[16:19]\n" \
"buffer_load_dwordx4 v25, s[76:79], s85 offen lds\n" \
"v_mfma_f32_16x16x32_bf16 a[48:51], v[60:63], v[148:151], a[48:51]\n" \
"s_add_u32 m0, %[mB], 0xc00\n" \
"s_add_u32 s84, s63, 0x18000\n" \
"v_mfma_f32_16x16x32_bf16 a[48:51], v[64:67], v[152:155], a[48:51]\n" \
"ds_read_b64_tr_b16 v[146:147], v35 offset:41984\n" \
"ds_read_b128 v[108:111], v28 offset:38912\n" \
"v_mfma_f32_16x16x32_bf16 a[80:83], v[72:75], v[152:155], a[80:83]\n" \
"buffer_load_dwordx4 v27, s[68:71], s84 offen lds\n" \
"v_mfma_f32_16x16x32_bf16 a[80:83], v[68:71], v[148:151], a[80:83]\n" \
"ds_read_b128 v[112:115], v29 offset:38912\n" \
"s_add_u32 s62, s62, 0x80\n" \
"v_mfma_f32_16x16x32_bf16 a[112:115], v[76:79], v[148:151], a[112:115]\n" \
"s_cmp_lt_u32 s62, 0x480\n" \
"s_cselect_b32 s62, s62, 0\n" \
"v_mfma_f32_16x16x32_bf16 a[112:115], v[80:83], v[152:155], a[112:115]\n" \
"s_lshl_b32 s72, s62, 6\n" \
"s_add_u32 s72, s72, s37\n" \
"v_mfma_f32_16x16x32_bf16 a[20:23], v[56:59], v[160:163], a[20:23]\n" \
"s_lshl_b32 s63, s62, 12\n" \
"v_add_u32_e64 v28, s59, v28\n" \
"v_mfma_f32_16x16x32_bf16 a[20:23], v[52:55], v[156:159], a[20:23]\n" \
"v_add_u32_e64 v29, s59, v29\n" \
"v_accvgpr_read_b32 v184, a16\n" \
"v_mfma_f32_16x16x32_bf16 a[52:55], v[60:63], v[156:159], a[52:55]\n" \
"v_accvgpr_read_b32 v185, a17\n" \
"v_accvgpr_read_b32 v186, a18\n" \
"v_mfma_f32_16x16x32_bf16 a[52:55], v[64:67], v[160:163], a[52:55]\n" \
"v_accvgpr_read_b32 v187, a19\n" \
"v_accvgpr_write_b32 a16, 0\n" \
"v_mfma_f32_16x16x32_bf16 a[84:87], v[72:75], v[160:163], a[84:87]\n" \
"v_accvgpr_write_b32 a17, 0\n" \
"v_accvgpr_write_b32 a18, 0\n" \
"v_mfma_f32_16x16x32_bf16 a[84:87], v[68:71], v[156:159], a[84:87]\n" \
"v_accvgpr_write_b32 a19, 0\n" \
"v_accvgpr_read_b32 v180, a48\n" \
"v_mfma_f32_16x16x32_bf16 a[116:119], v[76:79], v[156:159], a[116:119]\n" \
"v_accvgpr_read_b32 v181, a49\n" \
"v_accvgpr_read_b32 v182, a50\n" \
"v_mfma_f32_16x16x32_bf16 a[116:119], v[80:83], v[160:163], a[116:119]\n" \
"v_accvgpr_read_b32 v183, a51\n" \
"v_accvgpr_read_b32 v188, a80\n" \
"v_mfma_f32_16x16x32_bf16 a[24:27], v[56:59], v[168:171], a[24:27]\n" \
"v_accvgpr_read_b32 v189, a81\n" \
"v_accvgpr_read_b32 v190, a82\n" \
"v_mfma_f32_16x16x32_bf16 a[24:27], v[52:55], v[164:167], a[24:27]\n" \
"v_accvgpr_read_b32 v191, a83\n" \
"v_cvt_pk_bf16_f32 v184, v184, v185\n" \
"v_mfma_f32_16x16x32_bf16 a[56:59], v[60:63], v[164:167], a[56:59]\n" \
"v_cvt_pk_bf16_f32 v185, v186, v187\n" \
"v_cvt_pk_bf16_f32 v186, v180, v181\n" \
"v_mfma_f32_16x16x32_bf16 a[56:59], v[64:67], v[168:171], a[56:59]\n" \
"v_cvt_pk_bf16_f32 v187, v182, v183\n" \
"v_accvgpr_read_b32 v180, a112\n" \
"v_mfma_f32_16x16x32_bf16 a[88:91], v[72:75], v[168:171], a[88:91]\n" \
"v_accvgpr_read_b32 v181, a113\n" \
"v_accvgpr_read_b32 v182, a114\n" \
"v_mfma_f32_16x16x32_bf16 a[88:91], v[68:71], v[164:167], a[88:91]\n" \
"v_accvgpr_read_b32 v183, a115\n" \
"v_accvgpr_write_b32 a112, 0\n" \
"v_mfma_f32_16x16x32_bf16 a[120:123], v[76:79], v[164:167], a[120:123]\n" \
"v_accvgpr_write_b32 a113, 0\n" \
"v_accvgpr_write_b32 a114, 0\n" \
"v_mfma_f32_16x16x32_bf16 a[120:123], v[80:83], v[168:171], a[120:123]\n" \
"v_accvgpr_write_b32 a115, 0\n" \
"v_accvgpr_read_b32 v192, a20\n" \
"v_mfma_f32_16x16x32_bf16 a[28:31], v[56:59], v[176:179], a[28:31]\n" \
"v_accvgpr_read_b32 v193, a21\n" \
"v_accvgpr_read_b32 v194, a22\n" \
"v_mfma_f32_16x16x32_bf16 a[28:31], v[52:55], v[172:175], a[28:31]\n" \
"v_accvgpr_read_b32 v195, a23\n" \
"v_cvt_pk_bf16_f32 v188, v188, v189\n" \
"v_mfma_f32_16x16x32_bf16 a[60:63], v[60:63], v[172:175], a[60:63]\n" \
"v_cvt_pk_bf16_f32 v189, v190, v191\n" \
"v_cvt_pk_bf16_f32 v190, v180, v181\n" \
"v_mfma_f32_16x16x32_bf16 a[60:63], v[64:67], v[176:179], a[60:63]\n" \
"v_cvt_pk_bf16_f32 v191, v182, v183\n" \
"v_accvgpr_read_b32 v180, a52\n" \
"v_mfma_f32_16x16x32_bf16 a[92:95], v[72:75], v[176:179], a[92:95]\n" \
"v_accvgpr_read_b32 v181, a53\n" \
"v_accvgpr_read_b32 v182, a54\n" \
"v_mfma_f32_16x16x32_bf16 a[92:95], v[68:71], v[172:175], a[92:95]\n" \
"v_accvgpr_read_b32 v183, a55\n" \
"v_accvgpr_read_b32 v196, a84\n" \
"v_mfma_f32_16x16x32_bf16 a[124:127], v[76:79], v[172:175], a[124:127]\n" \
"v_accvgpr_read_b32 v197, a85\n" \
"v_accvgpr_read_b32 v198, a86\n" \
"v_mfma_f32_16x16x32_bf16 a[124:127], v[80:83], v[176:179], a[124:127]\n" \
"v_accvgpr_read_b32 v199, a87\n" \
"v_cvt_pk_bf16_f32 v192, v192, v193\n" \
"v_mfma_f32_16x16x32_bf16 a[0:3], v[120:123], v[56:59], a[0:3]\n" \
"v_cvt_pk_bf16_f32 v193, v194, v195\n" \
"v_cvt_pk_bf16_f32 v194, v180, v181\n" \
"v_mfma_f32_16x16x32_bf16 a[0:3], v[116:119], v[52:55], a[0:3]\n" \
"v_cvt_pk_bf16_f32 v195, v182, v183\n" \
"v_accvgpr_read_b32 v180, a116\n" \
"v_mfma_f32_16x16x32_bf16 a[32:35], v[116:119], v[60:63], a[32:35]\n" \
"v_accvgpr_read_b32 v181, a117\n" \
"v_accvgpr_read_b32 v182, a118\n" \
"v_mfma_f32_16x16x32_bf16 a[32:35], v[120:123], v[64:67], a[32:35]\n" \
"v_accvgpr_read_b32 v183, a119\n" \
"v_accvgpr_write_b32 a116, 0\n" \
"v_mfma_f32_16x16x32_bf16 a[64:67], v[120:123], v[72:75], a[64:67]\n" \
"v_accvgpr_write_b32 a117, 0\n" \
"v_accvgpr_write_b32 a118, 0\n" \
"v_mfma_f32_16x16x32_bf16 a[64:67], v[116:119], v[68:71], a[64:67]\n" \
"v_accvgpr_write_b32 a119, 0\n" \
"v_accvgpr_read_b32 v200, a24\n" \
"v_mfma_f32_16x16x32_bf16 a[96:99], v[116:119], v[76:79], a[96:99]\n" \
"v_accvgpr_read_b32 v201, a25\n" \
"v_accvgpr_read_b32 v202, a26\n" \
"v_mfma_f32_16x16x32_bf16 a[96:99], v[120:123], v[80:83], a[96:99]\n" \
"v_accvgpr_read_b32 v203, a27\n" \
"v_cvt_pk_bf16_f32 v196, v196, v197\n" \
"v_mfma_f32_16x16x32_bf16 a[4:7], v[128:131], v[56:59], a[4:7]\n" \
"v_cvt_pk_bf16_f32 v197, v198, v199\n" \
"v_cvt_pk_bf16_f32 v198, v180, v181\n" \
"v_mfma_f32_16x16x32_bf16 a[4:7], v[124:127], v[52:55], a[4:7]\n" \
"v_cvt_pk_bf16_f32 v199, v182, v183\n" \
"v_accvgpr_read_b32 v180, a56\n" \
"v_mfma_f32_16x16x32_bf16 a[36:39], v[124:127], v[60:63], a[36:39]\n" \
"v_accvgpr_read_b32 v181, a57\n" \
"v_accvgpr_read_b32 v182, a58\n" \
"v_mfma_f32_16x16x32_bf16 a[36:39], v[128:131], v[64:67], a[36:39]\n" \
"v_accvgpr_read_b32 v183, a59\n" \
"v_accvgpr_read_b32 v204, a88\n" \
"v_mfma_f32_16x16x32_bf16 a[68:71], v[128:131], v[72:75], a[68:71]\n" \
"v_accvgpr_read_b32 v205, a89\n" \
"v_accvgpr_read_b32 v206, a90\n" \
"v_mfma_f32_16x16x32_bf16 a[68:71], v[124:127], v[68:71], a[68:71]\n" \
"v_accvgpr_read_b32 v207, a91\n" \
"v_cvt_pk_bf16_f32 v200, v200, v201\n" \
"v_mfma_f32_16x16x32_bf16 a[100:103], v[124:127], v[76:79], a[100:103]\n" \
"v_cvt_pk_bf16_f32 v201, v202, v203\n" \
"v_cvt_pk_bf16_f32 v202, v180, v181\n" \
"v_mfma_f32_16x16x32_bf16 a[100:103], v[128:131], v[80:83], a[100:103]\n" \
"v_cvt_pk_bf16_f32 v203, v182, v183\n" \
"s_waitcnt lgkmcnt(0)\n" \
"s_nop 0\n" \
"v_mfma_f32_16x16x32_bf16 a[8:11], v[136:139], v[56:59], a[8:11]\n" \
"v_accvgpr_read_b32 v180, a120\n" \
"v_accvgpr_read_b32 v181, a121\n" \
"v_mfma_f32_16x16x32_bf16 a[8:11], v[132:135], v[52:55], a[8:11]\n" \
"v_accvgpr_read_b32 v182, a122\n" \
"v_accvgpr_read_b32 v183, a123\n" \
"v_mfma_f32_16x16x32_bf16 a[40:43], v[132:135], v[60:63], a[40:43]\n" \
"v_accvgpr_write_b32 a120, 0\n" \
"v_accvgpr_write_b32 a121, 0\n" \
"v_mfma_f32_16x16x32_bf16 a[40:43], v[136:139], v[64:67], a[40:43]\n" \
"v_accvgpr_write_b32 a122, 0\n" \
"v_accvgpr_write_b32 a123, 0\n" \
"v_mfma_f32_16x16x32_bf16 a[72:75], v[136:139], v[72:75], a[72:75]\n" \
"v_accvgpr_read_b32 v208, a28\n" \
"v_accvgpr_read_b32 v209, a29\n" \
"v_mfma_f32_16x16x32_bf16 a[72:75], v[132:135], v[68:71], a[72:75]\n" \
"v_accvgpr_read_b32 v210, a30\n" \
"v_accvgpr_read_b32 v211, a31\n" \
"v_mfma_f32_16x16x32_bf16 a[104:107], v[132:135], v[76:79], a[104:107]\n" \
"v_cvt_pk_bf16_f32 v204, v204, v205\n" \
"v_cvt_pk_bf16_f32 v205, v206, v207\n" \
"v_mfma_f32_16x16x32_bf16 a[104:107], v[136:139], v[80:83], a[104:107]\n" \
"v_cvt_pk_bf16_f32 v206, v180, v181\n" \
"v_cvt_pk_bf16_f32 v207, v182, v183\n" \
"v_mfma_f32_16x16x32_bf16 a[12:15], v[144:147], v[56:59], a[12:15]\n" \
"v_accvgpr_read_b32 v180, a60\n" \
"v_accvgpr_read_b32 v181, a61\n" \
"v_mfma_f32_16x16x32_bf16 a[12:15], v[140:143], v[52:55], a[12:15]\n" \
"v_accvgpr_read_b32 v182, a62\n" \
"v_accvgpr_read_b32 v183, a63\n" \
"v_mfma_f32_16x16x32_bf16 a[44:47], v[140:143], v[60:63], a[44:47]\n" \
"v_accvgpr_read_b32 v212, a92\n" \
"v_accvgpr_read_b32 v213, a93\n" \
"v_mfma_f32_16x16x32_bf16 a[44:47], v[144:147], v[64:67], a[44:47]\n" \
"v_accvgpr_read_b32 v214, a94\n" \
"v_accvgpr_read_b32 v215, a95\n" \
"v_mfma_f32_16x16x32_bf16 a[76:79], v[144:147], v[72:75], a[76:79]\n" \
"v_cvt_pk_bf16_f32 v208, v208, v209\n" \
"v_cvt_pk_bf16_f32 v209, v210, v211\n" \
"v_mfma_f32_16x16x32_bf16 a[76:79], v[140:143], v[68:71], a[76:79]\n" \
"v_cvt_pk_bf16_f32 v210, v180, v181\n" \
"v_cvt_pk_bf16_f32 v211, v182, v183\n" \
"v_mfma_f32_16x16x32_bf16 a[108:111], v[140:143], v[76:79], a[108:111]\n" \
"v_accvgpr_read_b32 v180, a124\n" \
"v_accvgpr_read_b32 v181, a125\n" \
"v_mfma_f32_16x16x32_bf16 a[108:111], v[144:147], v[80:83], a[108:111]\n" \
"v_accvgpr_read_b32 v182, a126\n" \
"v_accvgpr_read_b32 v183, a127\n" \
"s_waitcnt vmcnt(0)\n" \
"s_barrier\n" \
"v_mfma_f32_16x16x32_bf16 a[128:131], v[120:123], v[88:91], a[128:131]\n" \
"s_add_u32 m0, s40, 0x8000\n" \
"s_add_u32 s85, s72, 0x0\n" \
"s_nop 0\n" \
"v_mfma_f32_16x16x32_bf16 a[128:131], v[116:119], v[84:87], a[128:131]\n" \
"buffer_load_dwordx4 v20, s[80:83], s85 offen sc1 lds\n" \
"v_mfma_f32_16x16x32_bf16 a[160:163], v[116:119], v[92:95], a[160:163]\n" \
"s_add_u32 m0, s40, 0x9000\n" \
"s_add_u32 s84, s72, 0x1000\n" \
"v_mfma_f32_16x16x32_bf16 a[160:163], v[120:123], v[96:99], a[160:163]\n" \
"ds_read_b128 v[52:55], v28 offset:0\n" \
"ds_read_b128 v[56:59], v29 offset:0\n" \
"v_mfma_f32_16x16x32_bf16 a[192:195], v[120:123], v[104:107], a[192:195]\n" \
"buffer_load_dwordx4 v20, s[80:83], s84 offen sc1 lds\n" \
"v_mfma_f32_16x16x32_bf16 a[192:195], v[116:119], v[100:103], a[192:195]\n" \
"ds_read_b128 v[60:63], v28 offset:2048\n" \
"ds_read_b128 v[64:67], v29 offset:2048\n" \
"v_mfma_f32_16x16x32_bf16 a[224:227], v[116:119], v[108:111], a[224:227]\n" \
"s_add_u32 m0, s40, 0xb000\n" \
"s_add_u32 s85, s62, 0x0\n" \
"v_mfma_f32_16x16x32_bf16 a[224:227], v[120:123], v[112:115], a[224:227]\n" \
"buffer_load_dwordx4 v21, s[76:79], s85 offen lds\n" \
"v_mfma_f32_16x16x32_bf16 a[132:135], v[128:131], v[88:91], a[132:135]\n" \
"ds_read_b64_tr_b16 v[116:117], v36 offset:0\n" \
"ds_read_b64_tr_b16 v[118:119], v36 offset:512\n" \
"L_pf4:\n" \
"s_cbranch_execz L_pf8\n" \
"s_nop 0\n" \
"v_mfma_f32_16x16x32_bf16 a[132:135], v[124:127], v[84:87], a[132:135]\n" \
"ds_read_b64_tr_b16 v[120:121], v36 offset:4096\n" \
"ds_read_b64_tr_b16 v[122:123], v36 offset:4608\n" \
"v_mfma_f32_16x16x32_bf16 a[164:167], v[124:127], v[92:95], a[164:167]\n" \
"s_add_u32 m0, %[mA], 0x8000\n" \
"s_add_u32 s84, s62, 0x0\n" \
"v_mfma_f32_16x16x32_bf16 a[164:167], v[128:131], v[96:99], a[164:167]\n" \
"buffer_load_dwordx4 v24, s[76:79], s84 offen lds\n" \
"v_mfma_f32_16x16x32_bf16 a[196:199], v[128:131], v[104:107], a[196:199]\n" \
"ds_read_b128 v[68:71], v28 offset:4096\n" \
"ds_read_b128 v[72:75], v29 offset:4096\n" \
"v_mfma_f32_16x16x32_bf16 a[196:199], v[124:127], v[100:103], a[196:199]\n" \
"s_add_u32 m0, %[mA], 0x8400\n" \
"s_add_u32 s85, s62, 0x2400\n" \
"v_mfma_f32_16x16x32_bf16 a[228:231], v[124:127], v[108:111], a[228:231]\n" \
"buffer_load_dwordx4 v25, s[76:79], s85 offen lds\n" \
"v_mfma_f32_16x16x32_bf16 a[228:231], v[128:131], v[112:115], a[228:231]\n" \
"ds_read_b64_tr_b16 v[128:129], v37 offset:4096\n" \
"ds_read_b64_tr_b16 v[130:131], v37 offset:4608\n" \
"v_mfma_f32_16x16x32_bf16 a[136:139], v[136:139], v[88:91], a[136:139]\n" \
"ds_read_b64_tr_b16 v[124:125], v37 offset:0\n" \
"ds_read_b64_tr_b16 v[126:127], v37 offset:512\n" \
"v_mfma_f32_16x16x32_bf16 a[136:139], v[132:135], v[84:87], a[136:139]\n" \
"s_add_u32 m0, %[mB], 0x8000\n" \
"s_add_u32 s84, s63, 0x0\n" \
"s_nop 0\n" \
"v_mfma_f32_16x16x32_bf16 a[168:171], v[132:135], v[92:95], a[168:171]\n" \
"buffer_load_dwordx4 v26, s[68:71], s84 offen lds\n" \
"v_mfma_f32_16x16x32_bf16 a[168:171], v[136:139], v[96:99], a[168:171]\n" \
"ds_read_b128 v[76:79], v28 offset:6144\n" \
"ds_read_b128 v[80:83], v29 offset:6144\n" \
"v_mfma_f32_16x16x32_bf16 a[200:203], v[136:139], v[104:107], a[200:203]\n" \
"v_accvgpr_write_b32 a124, 0\n" \
"v_accvgpr_write_b32 a125, 0\n" \
"v_mfma_f32_16x16x32_bf16 a[200:203], v[132:135], v[100:103], a[200:203]\n" \
"s_add_u32 m0, %[mA], 0x8800\n" \
"s_add_u32 s85, s62, 0x4800\n" \
"v_mfma_f32_16x16x32_bf16 a[232:235], v[132:135], v[108:111], a[232:235]\n" \
"buffer_load_dwordx4 v24, s[76:79], s85 offen lds\n" \
"v_mfma_f32_16x16x32_bf16 a[232:235], v[136:139], v[112:115], a[232:235]\n" \
"ds_read_b64_tr_b16 v[132:133], v38 offset:0\n" \
"ds_read_b64_tr_b16 v[134:135], v38 offset:512\n" \
"v_mfma_f32_16x16x32_bf16 a[140:143], v[144:147], v[88:91], a[140:143]\n" \
"s_add_u32 m0, %[mA], 0x8c00\n" \
"s_add_u32 s84, s62, 0x6c00\n" \
"v_mfma_f32_16x16x32_bf16 a[140:143], v[140:143], v[84:87], a[140:143]\n" \
"buffer_load_dwordx4 v25, s[76:79], s84 offen lds\n" \
"v_mfma_f32_16x16x32_bf16 a[172:175], v[140:143], v[92:95], a[172:175]\n" \
"ds_read_b64_tr_b16 v[136:137], v38 offset:4096\n" \
"ds_read_b64_tr_b16 v[138:139], v38 offset:4608\n" \
"v_mfma_f32_16x16x32_bf16 a[172:175], v[144:147], v[96:99], a[172:175]\n" \
"v_accvgpr_write_b32 a126, 0\n" \
"v_accvgpr_write_b32 a127, 0\n" \
"v_mfma_f32_16x16x32_bf16 a[204:207], v[144:147], v[104:107], a[204:207]\n" \
"s_add_u32 m0, %[mB], 0x8400\n" \
"s_add_u32 s85, s63, 0x8000\n" \
"v_mfma_f32_16x16x32_bf16 a[204:207], v[140:143], v[100:103], a[204:207]\n" \
"buffer_load_dwordx4 v26, s[68:71], s85 offen lds\n" \
"v_mfma_f32_16x16x32_bf16 a[236:239], v[140:143], v[108:111], a[236:239]\n" \
"ds_read_b64_tr_b16 v[140:141], v39 offset:0\n" \
"ds_read_b64_tr_b16 v[142:143], v39 offset:960\n" \
"v_mfma_f32_16x16x32_bf16 a[236:239], v[144:147], v[112:115], a[236:239]\n" \
"v_accvgpr_read_b32 v216, a0\n" \
"v_accvgpr_read_b32 v217, a1\n" \
"v_mfma_f32_16x16x32_bf16 a[144:147], v[88:91], v[152:155], a[144:147]\n" \
"s_add_u32 m0, %[mA], 0x9000\n" \
"s_add_u32 s84, s62, 0x9000\n" \
"v_mfma_f32_16x16x32_bf16 a[144:147], v[84:87], v[148:151], a[144:147]\n" \
"buffer_load_dwordx4 v24, s[76:79], s84 offen lds\n" \
"v_mfma_f32_16x16x32_bf16 a[148:151], v[84:87], v[156:159], a[148:151]\n" \
"v_accvgpr_read_b32 v218, a2\n" \
"v_accvgpr_read_b32 v219, a3\n" \
"v_mfma_f32_16x16x32_bf16 a[148:151], v[88:91], v[160:163], a[148:151]\n" \
"s_add_u32 m0, %[mA], 0x9400\n" \
"s_add_u32 s85, s62, 0xb400\n" \
"v_mfma_f32_16x16x32_bf16 a[152:155], v[88:91], v[168:171], a[152:155]\n" \
"buffer_load_dwordx4 v25, s[76:79], s85 offen lds\n" \
"v_mfma_f32_16x16x32_bf16 a[152:155], v[84:87], v[164:167], a[152:155]\n" \
"v_cvt_pk_bf16_f32 v212, v212, v213\n" \
"v_cvt_pk_bf16_f32 v213, v214, v215\n" \
"v_mfma_f32_16x16x32_bf16 a[156:159], v[84:87], v[172:175], a[156:159]\n" \
"ds_read_b128 v[84:87], v22 offset:0\n" \
"v_cvt_pk_bf16_f32 v214, v180, v181\n" \
"v_mfma_f32_16x16x32_bf16 a[156:159], v[88:91], v[176:179], a[156:159]\n" \
"s_add_u32 m0, %[mB], 0x8800\n" \
"s_add_u32 s84, s63, 0x10000\n" \
"v_mfma_f32_16x16x32_bf16 a[176:179], v[96:99], v[152:155], a[176:179]\n" \
"buffer_load_dwordx4 v27, s[68:71], s84 offen lds\n" \
"v_mfma_f32_16x16x32_bf16 a[176:179], v[92:95], v[148:151], a[176:179]\n" \
"ds_read_b128 v[88:91], v23 offset:0\n" \
"v_cvt_pk_bf16_f32 v215, v182, v183\n" \
"v_mfma_f32_16x16x32_bf16 a[180:183], v[92:95], v[156:159], a[180:183]\n" \
"v_accvgpr_read_b32 v180, a4\n" \
"v_accvgpr_read_b32 v181, a5\n" \
"v_mfma_f32_16x16x32_bf16 a[180:183], v[96:99], v[160:163], a[180:183]\n" \
"s_add_u32 m0, %[mA], 0x9800\n" \
"s_add_u32 s85, s62, 0xd800\n" \
"v_mfma_f32_16x16x32_bf16 a[184:187], v[96:99], v[168:171], a[184:187]\n" \
"buffer_load_dwordx4 v24, s[76:79], s85 offen lds\n" \
"v_mfma_f32_16x16x32_bf16 a[184:187], v[92:95], v[164:167], a[184:187]\n" \
"v_accvgpr_read_b32 v182, a6\n" \
"v_accvgpr_read_b32 v183, a7\n" \
"v_mfma_f32_16x16x32_bf16 a[188:191], v[92:95], v[172:175], a[188:191]\n" \
"v_cvt_pk_bf16_f32 v216, v216, v217\n" \
"v_cvt_pk_bf16_f32 v217, v218, v219\n" \
"v_mfma_f32_16x16x32_bf16 a[188:191], v[96:99], v[176:179], a[188:191]\n" \
"v_accvgpr_read_b32 v220, a32\n" \
"v_accvgpr_read_b32 v221, a33\n" \
"v_mfma_f32_16x16x32_bf16 a[208:211], v[104:107], v[152:155], a[208:211]\n" \
"v_cvt_pk_bf16_f32 v218, v180, v181\n" \
"v_cvt_pk_bf16_f32 v219, v182, v183\n" \
"v_mfma_f32_16x16x32_bf16 a[208:211], v[100:103], v[148:151], a[208:211]\n" \
"v_accvgpr_read_b32 v222, a34\n" \
"v_accvgpr_read_b32 v223, a35\n" \
"v_mfma_f32_16x16x32_bf16 a[212:215], v[100:103], v[156:159], a[212:215]\n" \
"v_cvt_pk_bf16_f32 v220, v220, v221\n" \
"v_accvgpr_read_b32 v180, a36\n" \
"v_mfma_f32_16x16x32_bf16 a[212:215], v[104:107], v[160:163], a[212:215]\n" \
"v_accvgpr_read_b32 v181, a37\n" \
"v_accvgpr_read_b32 v182, a38\n" \
"v_mfma_f32_16x16x32_bf16 a[216:219], v[104:107], v[168:171], a[216:219]\n" \
"v_accvgpr_read_b32 v183, a39\n" \
"v_cvt_pk_bf16_f32 v221, v222, v223\n" \
"v_mfma_f32_16x16x32_bf16 a[216:219], v[100:103], v[164:167], a[216:219]\n" \
"v_accvgpr_read_b32 v224, a64\n" \
"v_accvgpr_read_b32 v225, a65\n" \
"v_mfma_f32_16x16x32_bf16 a[220:223], v[100:103], v[172:175], a[220:223]\n" \
"v_cvt_pk_bf16_f32 v222, v180, v181\n" \
"v_cvt_pk_bf16_f32 v223, v182, v183\n" \
"v_mfma_f32_16x16x32_bf16 a[220:223], v[104:107], v[176:179], a[220:223]\n" \
"v_accvgpr_read_b32 v226, a66\n" \
"v_accvgpr_read_b32 v227, a67\n" \
"v_mfma_f32_16x16x32_bf16 a[240:243], v[112:115], v[152:155], a[240:243]\n" \
"ds_read_b64_tr_b16 v[152:153], v32 offset:8192\n" \
"ds_read_b64_tr_b16 v[154:155], v32 offset:9216\n" \
"v_mfma_f32_16x16x32_bf16 a[240:243], v[108:111], v[148:151], a[240:243]\n" \
"ds_read_b64_tr_b16 v[148:149], v32 offset:0\n" \
"ds_read_b64_tr_b16 v[150:151], v32 offset:1024\n" \
"v_mfma_f32_16x16x32_bf16 a[244:247], v[108:111], v[156:159], a[244:247]\n" \
"ds_read_b64_tr_b16 v[156:157], v33 offset:0\n" \
"ds_read_b64_tr_b16 v[158:159], v33 offset:1024\n" \
"v_mfma_f32_16x16x32_bf16 a[244:247], v[112:115], v[160:163], a[244:247]\n" \
"ds_read_b64_tr_b16 v[160:161], v33 offset:8192\n" \
"ds_read_b64_tr_b16 v[162:163], v33 offset:9216\n" \
"v_mfma_f32_16x16x32_bf16 a[248:251], v[112:115], v[168:171], a[248:251]\n" \
"ds_read_b64_tr_b16 v[168:169], v34 offset:8192\n" \
"ds_read_b64_tr_b16 v[170:171], v34 offset:9216\n" \
"v_mfma_f32_16x16x32_bf16 a[248:251], v[108:111], v[164:167], a[248:251]\n" \
"ds_read_b64_tr_b16 v[164:165], v34 offset:0\n" \
"ds_read_b64_tr_b16 v[166:167], v34 offset:1024\n" \
"v_mfma_f32_16x16x32_bf16 a[252:255], v[108:111], v[172:175], a[252:255]\n" \
"ds_read_b64_tr_b16 v[172:173], v35 offset:0\n" \
"ds_read_b64_tr_b16 v[174:175], v35 offset:1024\n" \
"v_mfma_f32_16x16x32_bf16 a[252:255], v[112:115], v[176:179], a[252:255]\n" \
"s_add_u32 m0, %[mA], 0x9c00\n" \
"s_waitcnt lgkmcnt(14)\n" \
"s_nop 0\n" \
"v_smfmac_f32_16x16x64_bf16 a[16:19], v[140:143], v[84:91], v50\n" \
"v_mfma_f32_16x16x32_bf16 a[48:51], v[128:131], v[88:91], 0\n" \
"s_add_u32 s85, s62, 0xfc00\n" \
"ds_read_b64_tr_b16 v[176:177], v35 offset:8192\n" \
"v_mfma_f32_16x16x32_bf16 a[48:51], v[124:127], v[84:87], a[48:51]\n" \
"buffer_load_dwordx4 v25, s[76:79], s85 offen lds\n" \
"v_mfma_f32_16x16x32_bf16 a[80:83], v[84:87], v[132:135], 0\n" \
"s_add_u32 m0, %[mB], 0x8c00\n" \
"s_add_u32 s84, s63, 0x18000\n" \
"v_mfma_f32_16x16x32_bf16 a[80:83], v[88:91], v[136:139], a[80:83]\n" \
"ds_read_b64_tr_b16 v[178:179], v35 offset:9216\n" \
"v_cvt_pk_bf16_f32 v224, v224, v225\n" \
"v_smfmac_f32_16x16x64_bf16 a[112:115], v[140:143], v[52:59], v50\n" \
"v_mfma_f32_16x16x32_bf16 a[20:23], v[128:131], v[56:59], 0\n" \
"buffer_load_dwordx4 v27, s[68:71], s84 offen lds\n" \
"v_mfma_f32_16x16x32_bf16 a[20:23], v[124:127], v[52:55], a[20:23]\n" \
"s_add_u32 s88, s44, 0x0\n" \
"s_nop 0\n" \
"v_accvgpr_read_b32 v180, a68\n" \
"v_mfma_f32_16x16x32_bf16 a[52:55], v[116:119], v[52:55], 0\n" \
"v_accvgpr_read_b32 v181, a69\n" \
"v_accvgpr_read_b32 v182, a70\n" \
"v_mfma_f32_16x16x32_bf16 a[52:55], v[120:123], v[56:59], a[52:55]\n" \
"buffer_store_dwordx4 v[184:187], v20, s[80:83], s88 offen offset:0 sc1\n" \
"v_mfma_f32_16x16x32_bf16 a[84:87], v[56:59], v[136:139], 0\n" \
"v_accvgpr_read_b32 v183, a71\n" \
"v_cvt_pk_bf16_f32 v225, v226, v227\n" \
"v_mfma_f32_16x16x32_bf16 a[84:87], v[52:55], v[132:135], a[84:87]\n" \
"v_accvgpr_read_b32 v228, a96\n" \
"v_accvgpr_read_b32 v229, a97\n" \
"v_smfmac_f32_16x16x64_bf16 a[116:119], v[140:143], v[60:67], v50\n" \
"v_mfma_f32_16x16x32_bf16 a[24:27], v[124:127], v[60:63], 0\n" \
"v_cvt_pk_bf16_f32 v226, v180, v181\n" \
"v_cvt_pk_bf16_f32 v227, v182, v183\n" \
"v_mfma_f32_16x16x32_bf16 a[24:27], v[128:131], v[64:67], a[24:27]\n" \
"s_add_u32 s89, s44, 0x1000\n" \
"v_accvgpr_read_b32 v230, a98\n" \
"v_mfma_f32_16x16x32_bf16 a[56:59], v[120:123], v[64:67], 0\n" \
"buffer_store_dwordx4 v[188:191], v20, s[80:83], s89 offen offset:0 sc1\n" \
"v_mfma_f32_16x16x32_bf16 a[56:59], v[116:119], v[60:63], a[56:59]\n" \
"v_accvgpr_read_b32 v231, a99\n" \
"v_cvt_pk_bf16_f32 v228, v228, v229\n" \
"v_mfma_f32_16x16x32_bf16 a[88:91], v[60:63], v[132:135], 0\n" \
"v_accvgpr_read_b32 v180, a100\n" \
"v_accvgpr_read_b32 v181, a101\n" \
"v_mfma_f32_16x16x32_bf16 a[88:91], v[64:67], v[136:139], a[88:91]\n" \
"v_accvgpr_read_b32 v182, a102\n" \
"v_accvgpr_read_b32 v183, a103\n" \
"v_smfmac_f32_16x16x64_bf16 a[120:123], v[140:143], v[68:75], v50\n" \
"v_mfma_f32_16x16x32_bf16 a[28:31], v[128:131], v[72:75], 0\n" \
"v_cvt_pk_bf16_f32 v229, v230, v231\n" \
"v_accvgpr_read_b32 v232, a8\n" \
"v_mfma_f32_16x16x32_bf16 a[28:31], v[124:127], v[68:71], a[28:31]\n" \
"v_accvgpr_read_b32 v233, a9\n" \
"v_cvt_pk_bf16_f32 v230, v180, v181\n" \
"v_mfma_f32_16x16x32_bf16 a[60:63], v[116:119], v[68:71], 0\n" \
"s_add_u32 s88, s44, 0x0\n" \
"s_nop 0\n" \
"v_cvt_pk_bf16_f32 v231, v182, v183\n" \
"v_mfma_f32_16x16x32_bf16 a[60:63], v[120:123], v[72:75], a[60:63]\n" \
"buffer_store_dwordx4 v[192:195], v20, s[80:83], s88 offen offset:1024 sc1\n" \
"v_mfma_f32_16x16x32_bf16 a[92:95], v[72:75], v[136:139], 0\n" \
"v_accvgpr_read_b32 v234, a10\n" \
"v_accvgpr_read_b32 v235, a11\n" \
"v_mfma_f32_16x16x32_bf16 a[92:95], v[68:71], v[132:135], a[92:95]\n" \
"v_cvt_pk_bf16_f32 v232, v232, v233\n" \
"v_accvgpr_read_b32 v180, a12\n" \
"v_smfmac_f32_16x16x64_bf16 a[124:127], v[140:143], v[76:83], v50\n" \
"v_mfma_f32_16x16x32_bf16 a[0:3], v[124:127], v[76:79], 0\n" \
"v_accvgpr_read_b32 v181, a13\n" \
"v_accvgpr_read_b32 v182, a14\n" \
"v_mfma_f32_16x16x32_bf16 a[0:3], v[128:131], v[80:83], a[0:3]\n" \
"v_accvgpr_read_b32 v183, a15\n" \
"v_cvt_pk_bf16_f32 v233, v234, v235\n" \
"v_mfma_f32_16x16x32_bf16 a[4:7], v[120:123], v[80:83], 0\n" \
"v_accvgpr_read_b32 v236, a40\n" \
"v_accvgpr_read_b32 v237, a41\n" \
"v_mfma_f32_16x16x32_bf16 a[4:7], v[116:119], v[76:79], a[4:7]\n" \
"v_cvt_pk_bf16_f32 v234, v180, v181\n" \
"v_cvt_pk_bf16_f32 v235, v182, v183\n" \
"v_mfma_f32_16x16x32_bf16 a[32:35], v[76:79], v[132:135], 0\n" \
"v_accvgpr_read_b32 v238, a42\n" \
"v_accvgpr_read_b32 v239, a43\n" \
"v_mfma_f32_16x16x32_bf16 a[32:35], v[80:83], v[136:139], a[32:35]\n" \
"v_cvt_pk_bf16_f32 v236, v236, v237\n" \
"v_accvgpr_read_b32 v180, a44\n" \
"v_accvgpr_read_b32 v181, a45\n" \
"v_accvgpr_read_b32 v182, a46\n" \
"v_accvgpr_read_b32 v183, a47\n" \
"v_cvt_pk_bf16_f32 v237, v238, v239\n" \
"v_accvgpr_read_b32 v240, a72\n" \
"v_accvgpr_read_b32 v241, a73\n" \
"v_cvt_pk_bf16_f32 v238, v180, v181\n" \
"v_cvt_pk_bf16_f32 v239, v182, v183\n" \
"v_accvgpr_read_b32 v242, a74\n" \
"v_accvgpr_read_b32 v243, a75\n" \
"v_cvt_pk_bf16_f32 v240, v240, v241\n" \
"v_accvgpr_read_b32 v180, a76\n" \
"v_accvgpr_read_b32 v181, a77\n" \
"v_accvgpr_read_b32 v182, a78\n" \
"v_accvgpr_read_b32 v183, a79\n" \
"v_cvt_pk_bf16_f32 v241, v242, v243\n" \
"v_accvgpr_read_b32 v244, a104\n" \
"v_accvgpr_read_b32 v245, a105\n" \
"v_cvt_pk_bf16_f32 v242, v180, v181\n" \
"v_cvt_pk_bf16_f32 v243, v182, v183\n" \
"v_accvgpr_read_b32 v246, a106\n" \
"v_accvgpr_read_b32 v247, a107\n" \
"v_cvt_pk_bf16_f32 v244, v244, v245\n" \
"v_accvgpr_read_b32 v248, a128\n" \
"v_accvgpr_read_b32 v249, a129\n" \
"v_accvgpr_read_b32 v250, a130\n" \
"v_accvgpr_read_b32 v251, a131\n" \
"s_waitcnt lgkmcnt(0)\n" \
"s_nop 0\n" \
"v_mfma_f32_16x16x32_bf16 a[128:131], v[152:155], v[56:59], 0\n" \
"v_accvgpr_read_b32 v180, a108\n" \
"v_accvgpr_read_b32 v181, a109\n" \
"v_accvgpr_read_b32 v182, a110\n" \
"v_accvgpr_read_b32 v183, a111\n" \
"v_cvt_pk_bf16_f32 v245, v246, v247\n" \
"v_cvt_pk_bf16_f32 v246, v180, v181\n" \
"v_cvt_pk_bf16_f32 v247, v182, v183\n" \
"v_cvt_pk_bf16_f32 v248, v248, v249\n" \
"v_mfma_f32_16x16x32_bf16 a[128:131], v[148:151], v[52:55], a[128:131]\n" \
"v_accvgpr_read_b32 v180, a132\n" \
"v_accvgpr_read_b32 v181, a133\n" \
"v_accvgpr_read_b32 v182, a134\n" \
"v_accvgpr_read_b32 v183, a135\n" \
"v_cvt_pk_bf16_f32 v249, v250, v251\n" \
"v_accvgpr_read_b32 v252, a160\n" \
"v_accvgpr_read_b32 v253, a161\n" \
"v_cvt_pk_bf16_f32 v250, v180, v181\n" \
"v_cvt_pk_bf16_f32 v251, v182, v183\n" \
"v_accvgpr_read_b32 v254, a162\n" \
"v_accvgpr_read_b32 v255, a163\n" \
"v_cvt_pk_bf16_f32 v252, v252, v253\n" \
"v_accvgpr_read_b32 v180, a164\n" \
"v_accvgpr_read_b32 v181, a165\n" \
"v_accvgpr_read_b32 v182, a166\n" \
"v_accvgpr_read_b32 v183, a167\n" \
"v_cvt_pk_bf16_f32 v253, v254, v255\n" \
"v_cvt_pk_bf16_f32 v254, v180, v181\n" \
"v_cvt_pk_bf16_f32 v255, v182, v183\n" \
"v_accvgpr_write_b32 a36, v252\n" \
"v_accvgpr_write_b32 a37, v253\n" \
"v_mfma_f32_16x16x32_bf16 a[132:135], v[156:159], v[52:55], 0\n" \
"v_accvgpr_write_b32 a38, v254\n" \
"v_accvgpr_write_b32 a39, v255\n" \
"v_accvgpr_read_b32 v252, a192\n" \
"v_accvgpr_read_b32 v253, a193\n" \
"v_accvgpr_read_b32 v254, a194\n" \
"v_accvgpr_read_b32 v255, a195\n" \
"v_cvt_pk_bf16_f32 v252, v252, v253\n" \
"v_accvgpr_read_b32 v180, a196\n" \
"v_accvgpr_read_b32 v181, a197\n" \
"v_accvgpr_read_b32 v182, a198\n" \
"v_accvgpr_read_b32 v183, a199\n" \
"v_cvt_pk_bf16_f32 v253, v254, v255\n" \
"v_cvt_pk_bf16_f32 v254, v180, v181\n" \
"v_cvt_pk_bf16_f32 v255, v182, v183\n" \
"v_accvgpr_write_b32 a64, v252\n" \
"v_accvgpr_write_b32 a65, v253\n" \
"v_mfma_f32_16x16x32_bf16 a[132:135], v[160:163], v[56:59], a[132:135]\n" \
"v_accvgpr_write_b32 a66, v254\n" \
"v_accvgpr_write_b32 a67, v255\n" \
"v_accvgpr_read_b32 v252, a224\n" \
"v_accvgpr_read_b32 v253, a225\n" \
"v_accvgpr_read_b32 v254, a226\n" \
"v_accvgpr_read_b32 v255, a227\n" \
"v_cvt_pk_bf16_f32 v252, v252, v253\n" \
"v_accvgpr_read_b32 v180, a228\n" \
"v_accvgpr_read_b32 v181, a229\n" \
"v_accvgpr_read_b32 v182, a230\n" \
"v_accvgpr_read_b32 v183, a231\n" \
"v_cvt_pk_bf16_f32 v253, v254, v255\n" \
"v_cvt_pk_bf16_f32 v254, v180, v181\n" \
"v_cvt_pk_bf16_f32 v255, v182, v183\n" \
"v_accvgpr_write_b32 a68, v252\n" \
"v_accvgpr_write_b32 a69, v253\n" \
"v_accvgpr_write_b32 a70, v254\n" \
"v_accvgpr_write_b32 a71, v255\n" \
"v_accvgpr_read_b32 v252, a136\n" \
"v_accvgpr_read_b32 v253, a137\n" \
"v_accvgpr_read_b32 v254, a138\n" \
"v_accvgpr_read_b32 v255, a139\n" \
"v_mfma_f32_16x16x32_bf16 a[136:139], v[168:171], v[56:59], 0\n" \
"ds_read_b128 v[84:87], v48 offset:0\n" \
"ds_read_b128 v[88:91], v49 offset:0\n" \
"ds_read_b128 v[92:95], v48 offset:2048\n" \
"v_mfma_f32_16x16x32_bf16 a[136:139], v[164:167], v[52:55], a[136:139]\n" \
"ds_read_b128 v[96:99], v49 offset:2048\n" \
"v_accvgpr_read_b32 v180, a140\n" \
"v_accvgpr_read_b32 v181, a141\n" \
"v_accvgpr_read_b32 v182, a142\n" \
"v_accvgpr_read_b32 v183, a143\n" \
"v_mfma_f32_16x16x32_bf16 a[140:143], v[172:175], v[52:55], 0\n" \
"ds_read_b128 v[100:103], v48 offset:4096\n" \
"ds_read_b128 v[104:107], v49 offset:4096\n" \
"s_add_u32 s89, s44, 0x1000\n" \
"buffer_store_dwordx4 v[196:199], v20, s[80:83], s89 offen offset:1024 sc1\n" \
"ds_read_b128 v[108:111], v48 offset:6144\n" \
"v_mfma_f32_16x16x32_bf16 a[140:143], v[176:179], v[56:59], a[140:143]\n" \
"v_cvt_pk_bf16_f32 v252, v252, v253\n" \
"v_cvt_pk_bf16_f32 v253, v254, v255\n" \
"v_mfma_f32_16x16x32_bf16 a[160:163], v[152:155], v[64:67], 0\n" \
"ds_read_b128 v[112:115], v49 offset:6144\n" \
"v_cvt_pk_bf16_f32 v254, v180, v181\n" \
"v_mfma_f32_16x16x32_bf16 a[160:163], v[148:151], v[60:63], a[160:163]\n" \
"v_cvt_pk_bf16_f32 v255, v182, v183\n" \
"v_accvgpr_write_b32 a96, v252\n" \
"v_mfma_f32_16x16x32_bf16 a[164:167], v[156:159], v[60:63], 0\n" \
"v_accvgpr_write_b32 a97, v253\n" \
"v_accvgpr_write_b32 a98, v254\n" \
"v_mfma_f32_16x16x32_bf16 a[164:167], v[160:163], v[64:67], a[164:167]\n" \
"v_accvgpr_write_b32 a99, v255\n" \
"v_accvgpr_read_b32 v252, a168\n" \
"v_accvgpr_read_b32 v253, a169\n" \
"v_accvgpr_read_b32 v254, a170\n" \
"v_accvgpr_read_b32 v255, a171\n" \
"v_mfma_f32_16x16x32_bf16 a[168:171], v[168:171], v[64:67], 0\n" \
"v_accvgpr_read_b32 v180, a172\n" \
"v_accvgpr_read_b32 v181, a173\n" \
"v_mfma_f32_16x16x32_bf16 a[168:171], v[164:167], v[60:63], a[168:171]\n" \
"v_accvgpr_read_b32 v182, a174\n" \
"v_accvgpr_read_b32 v183, a175\n" \
"v_mfma_f32_16x16x32_bf16 a[172:175], v[172:175], v[60:63], 0\n" \
"v_cvt_pk_bf16_f32 v252, v252, v253\n" \
"v_cvt_pk_bf16_f32 v253, v254, v255\n" \
"v_mfma_f32_16x16x32_bf16 a[172:175], v[176:179], v[64:67], a[172:175]\n" \
"v_cvt_pk_bf16_f32 v254, v180, v181\n" \
"v_cvt_pk_bf16_f32 v255, v182, v183\n" \
"v_mfma_f32_16x16x32_bf16 a[192:195], v[152:155], v[72:75], 0\n" \
"v_accvgpr_write_b32 a100, v252\n" \
"v_accvgpr_write_b32 a101, v253\n" \
"v_mfma_f32_16x16x32_bf16 a[192:195], v[148:151], v[68:71], a[192:195]\n" \
"v_accvgpr_write_b32 a102, v254\n" \
"v_accvgpr_write_b32 a103, v255\n" \
"s_add_u32 s62, s62, 0x80\n" \
"v_accvgpr_read_b32 v252, a200\n" \
"v_accvgpr_read_b32 v253, a201\n" \
"v_accvgpr_read_b32 v254, a202\n" \
"s_waitcnt vmcnt(4) lgkmcnt(0)\n" \
"s_barrier\n" \
"v_mfma_f32_16x16x32_bf16 a[196:199], v[156:159], v[68:71], 0\n" \
"v_accvgpr_read_b32 v255, a203\n" \
"s_add_u32 m0, s40, 0x0\n" \
"v_mfma_f32_16x16x32_bf16 a[196:199], v[160:163], v[72:75], a[196:199]\n" \
"s_cmp_lt_u32 s62, 0x480\n" \
"s_cselect_b32 s62, s62, 0\n" \
"v_mfma_f32_16x16x32_bf16 a[200:203], v[168:171], v[72:75], 0\n" \
"s_lshl_b32 s72, s62, 6\n" \
"s_add_u32 s72, s72, s37\n" \
"v_mfma_f32_16x16x32_bf16 a[200:203], v[164:167], v[68:71], a[200:203]\n" \
"s_add_u32 s85, s72, 0x0\n" \
"s_nop 0\n" \
"buffer_load_dwordx4 v20, s[80:83], s85 offen sc1 lds\n" \
"v_accvgpr_read_b32 v180, a204\n" \
"v_accvgpr_read_b32 v181, a205\n" \
"v_accvgpr_read_b32 v182, a206\n" \
"v_accvgpr_read_b32 v183, a207\n" \
"v_mfma_f32_16x16x32_bf16 a[204:207], v[172:175], v[68:71], 0\n" \
"s_add_u32 m0, s40, 0x1000\n" \
"s_add_u32 s84, s72, 0x1000\n" \
"v_mfma_f32_16x16x32_bf16 a[204:207], v[176:179], v[72:75], a[204:207]\n" \
"buffer_load_dwordx4 v20, s[80:83], s84 offen sc1 lds\n" \
"v_mfma_f32_16x16x32_bf16 a[224:227], v[152:155], v[80:83], 0\n" \
"v_cvt_pk_bf16_f32 v252, v252, v253\n" \
"v_cvt_pk_bf16_f32 v253, v254, v255\n" \
"v_mfma_f32_16x16x32_bf16 a[224:227], v[148:151], v[76:79], a[224:227]\n" \
"v_cvt_pk_bf16_f32 v254, v180, v181\n" \
"v_cvt_pk_bf16_f32 v255, v182, v183\n" \
"v_mfma_f32_16x16x32_bf16 a[228:231], v[156:159], v[76:79], 0\n" \
"v_accvgpr_write_b32 a8, v252\n" \
"v_accvgpr_write_b32 a9, v253\n" \
"v_mfma_f32_16x16x32_bf16 a[228:231], v[160:163], v[80:83], a[228:231]\n" \
"v_accvgpr_write_b32 a10, v254\n" \
"v_accvgpr_write_b32 a11, v255\n" \
"v_accvgpr_read_b32 v252, a232\n" \
"v_accvgpr_read_b32 v253, a233\n" \
"v_accvgpr_read_b32 v254, a234\n" \
"v_accvgpr_read_b32 v255, a235\n" \
"v_mfma_f32_16x16x32_bf16 a[232:235], v[168:171], v[80:83], 0\n" \
"s_add_u32 m0, s40, 0x3000\n" \
"s_add_u32 s85, s62, 0x0\n" \
"buffer_load_dwordx4 v21, s[76:79], s85 offen lds\n" \
"v_mfma_f32_16x16x32_bf16 a[232:235], v[164:167], v[76:79], a[232:235]\n" \
"v_accvgpr_read_b32 v180, a236\n" \
"v_accvgpr_read_b32 v181, a237\n" \
"v_accvgpr_read_b32 v182, a238\n" \
"v_accvgpr_read_b32 v183, a239\n" \
"v_mfma_f32_16x16x32_bf16 a[236:239], v[172:175], v[76:79], 0\n" \
"v_cvt_pk_bf16_f32 v252, v252, v253\n" \
"s_add_u32 m0, %[mA], 0x0\n" \
"v_mfma_f32_16x16x32_bf16 a[236:239], v[176:179], v[80:83], a[236:239]\n" \
"s_add_u32 s84, s62, 0x0\n" \
"s_nop 0\n" \
"v_cvt_pk_bf16_f32 v253, v254, v255\n" \
"v_cvt_pk_bf16_f32 v254, v180, v181\n" \
"v_cvt_pk_bf16_f32 v255, v182, v183\n" \
"v_accvgpr_write_b32 a12, v252\n" \
"v_accvgpr_write_b32 a13, v253\n" \
"v_accvgpr_write_b32 a14, v254\n" \
"v_accvgpr_write_b32 a15, v255\n" \
"v_accvgpr_read_b32 v252, a144\n" \
"v_accvgpr_read_b32 v253, a145\n" \
"v_accvgpr_read_b32 v254, a146\n" \
"v_accvgpr_read_b32 v255, a147\n" \
"v_mfma_f32_16x16x32_bf16 a[144:147], v[152:155], v[88:91], 0\n" \
"buffer_load_dwordx4 v24, s[76:79], s84 offen lds\n" \
"v_mfma_f32_16x16x32_bf16 a[144:147], v[148:151], v[84:87], a[144:147]\n" \
"v_cvt_pk_bf16_f32 v252, v252, v253\n" \
"v_accvgpr_read_b32 v180, a176\n" \
"v_accvgpr_read_b32 v181, a177\n" \
"v_accvgpr_read_b32 v182, a178\n" \
"v_accvgpr_read_b32 v183, a179\n" \
"v_cvt_pk_bf16_f32 v253, v254, v255\n" \
"v_cvt_pk_bf16_f32 v254, v180, v181\n" \
"v_cvt_pk_bf16_f32 v255, v182, v183\n" \
"v_accvgpr_write_b32 a40, v252\n" \
"v_accvgpr_write_b32 a41, v253\n" \
"v_accvgpr_write_b32 a42, v254\n" \
"v_accvgpr_write_b32 a43, v255\n" \
"v_accvgpr_read_b32 v252, a148\n" \
"v_accvgpr_read_b32 v253, a149\n" \
"v_accvgpr_read_b32 v254, a150\n" \
"v_accvgpr_read_b32 v255, a151\n" \
"v_mfma_f32_16x16x32_bf16 a[148:151], v[156:159], v[84:87], 0\n" \
"v_cvt_pk_bf16_f32 v252, v252, v253\n" \
"s_add_u32 m0, %[mA], 0x400\n" \
"v_mfma_f32_16x16x32_bf16 a[148:151], v[160:163], v[88:91], a[148:151]\n" \
"s_add_u32 s85, s62, 0x2400\n" \
"v_accvgpr_read_b32 v180, a180\n" \
"v_accvgpr_read_b32 v181, a181\n" \
"v_accvgpr_read_b32 v182, a182\n" \
"v_accvgpr_read_b32 v183, a183\n" \
"v_cvt_pk_bf16_f32 v253, v254, v255\n" \
"v_cvt_pk_bf16_f32 v254, v180, v181\n" \
"v_cvt_pk_bf16_f32 v255, v182, v183\n" \
"v_accvgpr_write_b32 a44, v252\n" \
"v_accvgpr_write_b32 a45, v253\n" \
"v_accvgpr_write_b32 a46, v254\n" \
"v_accvgpr_write_b32 a47, v255\n" \
"v_accvgpr_read_b32 v252, a152\n" \
"v_accvgpr_read_b32 v253, a153\n" \
"v_accvgpr_read_b32 v254, a154\n" \
"v_accvgpr_read_b32 v255, a155\n" \
"v_mfma_f32_16x16x32_bf16 a[152:155], v[168:171], v[88:91], 0\n" \
"buffer_load_dwordx4 v25, s[76:79], s85 offen lds\n" \
"v_mfma_f32_16x16x32_bf16 a[152:155], v[164:167], v[84:87], a[152:155]\n" \
"v_cvt_pk_bf16_f32 v252, v252, v253\n" \
"v_accvgpr_read_b32 v180, a184\n" \
"v_accvgpr_read_b32 v181, a185\n" \
"v_accvgpr_read_b32 v182, a186\n" \
"v_accvgpr_read_b32 v183, a187\n" \
"v_cvt_pk_bf16_f32 v253, v254, v255\n" \
"v_cvt_pk_bf16_f32 v254, v180, v181\n" \
"v_cvt_pk_bf16_f32 v255, v182, v183\n" \
"s_lshl_b32 s63, s62, 12\n" \
"v_accvgpr_write_b32 a72, v252\n" \
"v_accvgpr_write_b32 a73, v253\n" \
"v_accvgpr_write_b32 a74, v254\n" \
"v_accvgpr_write_b32 a75, v255\n" \
"v_accvgpr_read_b32 v252, a156\n" \
"v_accvgpr_read_b32 v253, a157\n" \
"v_accvgpr_read_b32 v254, a158\n" \
"L_pf5:\n" \
"s_cbranch_execz L_pf9\n" \
"s_nop 0\n" \
"v_accvgpr_read_b32 v255, a159\n" \
"v_mfma_f32_16x16x32_bf16 a[156:159], v[172:175], v[84:87], 0\n" \
"s_add_u32 m0, %[mB], 0x0\n" \
"s_add_u32 s84, s63, 0x0\n" \
"s_nop 0\n" \
"buffer_load_dwordx4 v26, s[68:71], s84 offen lds\n" \
"v_mfma_f32_16x16x32_bf16 a[156:159], v[176:179], v[88:91], a[156:159]\n" \
"s_add_u32 m0, %[mA], 0x800\n" \
"s_add_u32 s85, s62, 0x4800\n" \
"v_mfma_f32_16x16x32_bf16 a[176:179], v[152:155], v[96:99], 0\n" \
"v_accvgpr_read_b32 v180, a188\n" \
"v_accvgpr_read_b32 v181, a189\n" \
"v_mfma_f32_16x16x32_bf16 a[176:179], v[148:151], v[92:95], a[176:179]\n" \
"buffer_load_dwordx4 v24, s[76:79], s85 offen lds\n" \
"v_mfma_f32_16x16x32_bf16 a[180:183], v[156:159], v[92:95], 0\n" \
"s_add_u32 m0, %[mA], 0xc00\n" \
"s_add_u32 s84, s62, 0x6c00\n" \
"v_mfma_f32_16x16x32_bf16 a[180:183], v[160:163], v[96:99], a[180:183]\n" \
"v_accvgpr_read_b32 v182, a190\n" \
"v_accvgpr_read_b32 v183, a191\n" \
"v_mfma_f32_16x16x32_bf16 a[184:187], v[168:171], v[96:99], 0\n" \
"buffer_load_dwordx4 v25, s[76:79], s84 offen lds\n" \
"v_mfma_f32_16x16x32_bf16 a[184:187], v[164:167], v[92:95], a[184:187]\n" \
"v_cvt_pk_bf16_f32 v252, v252, v253\n" \
"v_cvt_pk_bf16_f32 v253, v254, v255\n" \
"v_mfma_f32_16x16x32_bf16 a[188:191], v[172:175], v[92:95], 0\n" \
"v_cvt_pk_bf16_f32 v254, v180, v181\n" \
"v_cvt_pk_bf16_f32 v255, v182, v183\n" \
"v_mfma_f32_16x16x32_bf16 a[188:191], v[176:179], v[96:99], a[188:191]\n" \
"v_accvgpr_write_b32 a76, v252\n" \
"v_accvgpr_write_b32 a77, v253\n" \
"v_accvgpr_write_b32 a78, v254\n" \
"v_accvgpr_write_b32 a79, v255\n" \
"v_accvgpr_read_b32 v252, a208\n" \
"v_accvgpr_read_b32 v253, a209\n" \
"v_accvgpr_read_b32 v254, a210\n" \
"v_accvgpr_read_b32 v255, a211\n" \
"v_mfma_f32_16x16x32_bf16 a[208:211], v[152:155], v[104:107], 0\n" \
"v_cvt_pk_bf16_f32 v252, v252, v253\n" \
"s_add_u32 m0, %[mB], 0x400\n" \
"v_mfma_f32_16x16x32_bf16 a[208:211], v[148:151], v[100:103], a[208:211]\n" \
"s_add_u32 s85, s63, 0x8000\n" \
"v_accvgpr_read_b32 v180, a240\n" \
"v_accvgpr_read_b32 v181, a241\n" \
"v_accvgpr_read_b32 v182, a242\n" \
"v_accvgpr_read_b32 v183, a243\n" \
"v_cvt_pk_bf16_f32 v253, v254, v255\n" \
"v_cvt_pk_bf16_f32 v254, v180, v181\n" \
"v_cvt_pk_bf16_f32 v255, v182, v183\n" \
"v_accvgpr_write_b32 a104, v252\n" \
"v_accvgpr_write_b32 a105, v253\n" \
"v_accvgpr_write_b32 a106, v254\n" \
"v_accvgpr_write_b32 a107, v255\n" \
"v_accvgpr_read_b32 v252, a212\n" \
"v_accvgpr_read_b32 v253, a213\n" \
"v_accvgpr_read_b32 v254, a214\n" \
"v_accvgpr_read_b32 v255, a215\n" \
"v_mfma_f32_16x16x32_bf16 a[212:215], v[156:159], v[100:103], 0\n" \
"buffer_load_dwordx4 v26, s[68:71], s85 offen lds\n" \
"ds_read_b64_tr_b16 v[140:141], v39 offset:32768\n" \
"ds_read_b64_tr_b16 v[142:143], v39 offset:33728\n" \
"ds_read_b128 v[84:87], v22 offset:32768\n" \
"ds_read_b128 v[88:91], v23 offset:32768\n" \
"v_mfma_f32_16x16x32_bf16 a[212:215], v[160:163], v[104:107], a[212:215]\n" \
"ds_read_b64_tr_b16 v[128:129], v37 offset:36864\n" \
"ds_read_b64_tr_b16 v[130:131], v37 offset:37376\n" \
"v_accvgpr_read_b32 v184, a216\n" \
"v_accvgpr_read_b32 v185, a217\n" \
"v_accvgpr_read_b32 v186, a218\n" \
"v_accvgpr_read_b32 v187, a219\n" \
"v_mfma_f32_16x16x32_bf16 a[216:219], v[168:171], v[104:107], 0\n" \
"s_add_u32 m0, %[mA], 0x1000\n" \
"s_add_u32 s84, s62, 0x9000\n" \
"buffer_load_dwordx4 v24, s[76:79], s84 offen lds\n" \
"ds_read_b64_tr_b16 v[124:125], v37 offset:32768\n" \
"ds_read_b64_tr_b16 v[126:127], v37 offset:33280\n" \
"v_mfma_f32_16x16x32_bf16 a[216:219], v[164:167], v[100:103], a[216:219]\n" \
"v_cvt_pk_bf16_f32 v252, v252, v253\n" \
"v_accvgpr_read_b32 v180, a244\n" \
"v_accvgpr_read_b32 v181, a245\n" \
"v_accvgpr_read_b32 v182, a246\n" \
"v_accvgpr_read_b32 v183, a247\n" \
"v_cvt_pk_bf16_f32 v253, v254, v255\n" \
"v_cvt_pk_bf16_f32 v254, v180, v181\n" \
"v_cvt_pk_bf16_f32 v255, v182, v183\n" \
"v_accvgpr_write_b32 a108, v252\n" \
"v_accvgpr_write_b32 a109, v253\n" \
"v_accvgpr_write_b32 a110, v254\n" \
"v_accvgpr_write_b32 a111, v255\n" \
"v_accvgpr_read_b32 v252, a220\n" \
"v_accvgpr_read_b32 v253, a221\n" \
"v_accvgpr_read_b32 v254, a222\n" \
"v_accvgpr_read_b32 v255, a223\n" \
"v_mfma_f32_16x16x32_bf16 a[220:223], v[172:175], v[100:103], 0\n" \
"ds_read_b64_tr_b16 v[132:133], v38 offset:32768\n" \
"ds_read_b64_tr_b16 v[134:135], v38 offset:33280\n" \
"ds_read_b64_tr_b16 v[136:137], v38 offset:36864\n" \
"ds_read_b64_tr_b16 v[138:139], v38 offset:37376\n" \
"v_mfma_f32_16x16x32_bf16 a[220:223], v[176:179], v[104:107], a[220:223]\n" \
"ds_read_b128 v[52:55], v28 offset:32768\n" \
"ds_read_b128 v[56:59], v29 offset:32768\n" \
"v_mfma_f32_16x16x32_bf16 a[240:243], v[152:155], v[112:115], 0\n" \
"s_add_u32 m0, %[mA], 0x1400\n" \
"s_add_u32 s85, s62, 0xb400\n" \
"buffer_load_dwordx4 v25, s[76:79], s85 offen lds\n" \
"v_mfma_f32_16x16x32_bf16 a[240:243], v[148:151], v[108:111], a[240:243]\n" \
"ds_read_b64_tr_b16 v[116:117], v36 offset:32768\n" \
"ds_read_b64_tr_b16 v[118:119], v36 offset:33280\n" \
"v_mfma_f32_16x16x32_bf16 a[244:247], v[156:159], v[108:111], 0\n" \
"s_add_u32 m0, %[mB], 0x800\n" \
"s_add_u32 s84, s63, 0x10000\n" \
"v_mfma_f32_16x16x32_bf16 a[244:247], v[160:163], v[112:115], a[244:247]\n" \
"buffer_load_dwordx4 v27, s[68:71], s84 offen lds\n" \
"ds_read_b64_tr_b16 v[120:121], v36 offset:36864\n" \
"ds_read_b64_tr_b16 v[122:123], v36 offset:37376\n" \
"v_accvgpr_read_b32 v180, a248\n" \
"v_accvgpr_read_b32 v181, a249\n" \
"v_accvgpr_read_b32 v182, a250\n" \
"v_accvgpr_read_b32 v183, a251\n" \
"v_mfma_f32_16x16x32_bf16 a[248:251], v[168:171], v[112:115], 0\n" \
"ds_read_b128 v[60:63], v28 offset:34816\n" \
"v_cvt_pk_bf16_f32 v184, v184, v185\n" \
"v_mfma_f32_16x16x32_bf16 a[248:251], v[164:167], v[108:111], a[248:251]\n" \
"v_cvt_pk_bf16_f32 v185, v186, v187\n" \
"v_cvt_pk_bf16_f32 v186, v180, v181\n" \
"v_cvt_pk_bf16_f32 v187, v182, v183\n" \
"v_accvgpr_read_b32 v180, a252\n" \
"v_accvgpr_read_b32 v181, a253\n" \
"v_accvgpr_read_b32 v182, a254\n" \
"v_accvgpr_read_b32 v183, a255\n" \
"v_mfma_f32_16x16x32_bf16 a[252:255], v[172:175], v[108:111], 0\n" \
"ds_read_b128 v[64:67], v29 offset:34816\n" \
"s_add_u32 m0, %[mA], 0x1800\n" \
"v_mfma_f32_16x16x32_bf16 a[252:255], v[176:179], v[112:115], a[252:255]\n" \
"s_add_u32 s85, s62, 0xd800\n" \
"buffer_load_dwordx4 v24, s[76:79], s85 offen lds\n" \
"s_waitcnt lgkmcnt(12)\n" \
"v_smfmac_f32_16x16x64_bf16 a[16:19], v[140:143], v[84:91], v50\n" \
"v_mfma_f32_16x16x32_bf16 a[48:51], v[128:131], v[88:91], a[48:51]\n" \
"s_add_u32 m0, %[mA], 0x1c00\n" \
"s_add_u32 s85, s62, 0xfc00\n" \
"v_mfma_f32_16x16x32_bf16 a[48:51], v[124:127], v[84:87], a[48:51]\n" \
"ds_read_b128 v[68:71], v28 offset:36864\n" \
"s_waitcnt lgkmcnt(8)\n" \
"v_mfma_f32_16x16x32_bf16 a[80:83], v[84:87], v[132:135], a[80:83]\n" \
"buffer_load_dwordx4 v25, s[76:79], s85 offen lds\n" \
"v_mfma_f32_16x16x32_bf16 a[80:83], v[88:91], v[136:139], a[80:83]\n" \
"ds_read_b128 v[72:75], v29 offset:36864\n" \
"s_waitcnt lgkmcnt(6)\n" \
"v_smfmac_f32_16x16x64_bf16 a[112:115], v[140:143], v[52:59], v50\n" \
"v_mfma_f32_16x16x32_bf16 a[20:23], v[128:131], v[56:59], a[20:23]\n" \
"s_add_u32 m0, %[mB], 0xc00\n" \
"s_add_u32 s84, s63, 0x18000\n" \
"buffer_load_dwordx4 v27, s[68:71], s84 offen lds\n" \
"v_mfma_f32_16x16x32_bf16 a[20:23], v[124:127], v[52:55], a[20:23]\n" \
"ds_read_b128 v[76:79], v28 offset:38912\n" \
"ds_read_b128 v[80:83], v29 offset:38912\n" \
"v_mfma_f32_16x16x32_bf16 a[52:55], v[116:119], v[52:55], a[52:55]\n" \
"s_add_u32 s88, s44, 0x0\n" \
"s_waitcnt lgkmcnt(5)\n" \
"v_mfma_f32_16x16x32_bf16 a[52:55], v[120:123], v[56:59], a[52:55]\n" \
"buffer_store_dwordx4 v[200:203], v20, s[80:83], s88 offen offset:2048 sc1\n" \
"v_mfma_f32_16x16x32_bf16 a[84:87], v[56:59], v[136:139], a[84:87]\n" \
"ds_read_b64_tr_b16 v[152:153], v32 offset:40960\n" \
"ds_read_b64_tr_b16 v[154:155], v32 offset:41984\n" \
"v_mfma_f32_16x16x32_bf16 a[84:87], v[52:55], v[132:135], a[84:87]\n" \
"ds_read_b64_tr_b16 v[148:149], v32 offset:32768\n" \
"s_waitcnt lgkmcnt(7)\n" \
"v_smfmac_f32_16x16x64_bf16 a[116:119], v[140:143], v[60:67], v50\n" \
"v_mfma_f32_16x16x32_bf16 a[24:27], v[124:127], v[60:63], a[24:27]\n" \
"ds_read_b64_tr_b16 v[150:151], v32 offset:33792\n" \
"ds_read_b64_tr_b16 v[156:157], v33 offset:32768\n" \
"v_mfma_f32_16x16x32_bf16 a[24:27], v[128:131], v[64:67], a[24:27]\n" \
"ds_read_b64_tr_b16 v[158:159], v33 offset:33792\n" \
"ds_read_b64_tr_b16 v[160:161], v33 offset:40960\n" \
"v_mfma_f32_16x16x32_bf16 a[56:59], v[120:123], v[64:67], a[56:59]\n" \
"ds_read_b64_tr_b16 v[162:163], v33 offset:41984\n" \
"s_add_u32 s89, s44, 0x1000\n" \
"v_mfma_f32_16x16x32_bf16 a[56:59], v[116:119], v[60:63], a[56:59]\n" \
"buffer_store_dwordx4 v[204:207], v20, s[80:83], s89 offen offset:2048 sc1\n" \
"v_mfma_f32_16x16x32_bf16 a[88:91], v[60:63], v[132:135], a[88:91]\n" \
"ds_read_b64_tr_b16 v[168:169], v34 offset:40960\n" \
"ds_read_b64_tr_b16 v[170:171], v34 offset:41984\n" \
"v_mfma_f32_16x16x32_bf16 a[88:91], v[64:67], v[136:139], a[88:91]\n" \
"ds_read_b64_tr_b16 v[164:165], v34 offset:32768\n" \
"s_waitcnt lgkmcnt(13)\n" \
"v_smfmac_f32_16x16x64_bf16 a[120:123], v[140:143], v[68:75], v50\n" \
"v_mfma_f32_16x16x32_bf16 a[28:31], v[128:131], v[72:75], a[28:31]\n" \
"ds_read_b64_tr_b16 v[166:167], v34 offset:33792\n" \
"ds_read_b64_tr_b16 v[172:173], v35 offset:32768\n" \
"v_mfma_f32_16x16x32_bf16 a[28:31], v[124:127], v[68:71], a[28:31]\n" \
"ds_read_b64_tr_b16 v[174:175], v35 offset:33792\n" \
"ds_read_b64_tr_b16 v[176:177], v35 offset:40960\n" \
"v_mfma_f32_16x16x32_bf16 a[60:63], v[116:119], v[68:71], a[60:63]\n" \
"ds_read_b64_tr_b16 v[178:179], v35 offset:41984\n" \
"s_add_u32 s88, s44, 0x0\n" \
"v_mfma_f32_16x16x32_bf16 a[60:63], v[120:123], v[72:75], a[60:63]\n" \
"buffer_store_dwordx4 v[208:211], v20, s[80:83], s88 offen offset:3072 sc1\n" \
"v_mfma_f32_16x16x32_bf16 a[92:95], v[72:75], v[136:139], a[92:95]\n" \
"ds_read_b128 v[84:87], v48 offset:32768\n" \
"ds_read_b128 v[88:91], v49 offset:32768\n" \
"v_mfma_f32_16x16x32_bf16 a[92:95], v[68:71], v[132:135], a[92:95]\n" \
"ds_read_b128 v[92:95], v48 offset:34816\n" \
"s_waitcnt lgkmcnt(15)\n" \
"s_nop 0\n" \
"v_smfmac_f32_16x16x64_bf16 a[124:127], v[140:143], v[76:83], v50\n" \
"v_mfma_f32_16x16x32_bf16 a[0:3], v[124:127], v[76:79], a[0:3]\n" \
"ds_read_b128 v[96:99], v49 offset:34816\n" \
"ds_read_b128 v[100:103], v48 offset:36864\n" \
"v_mfma_f32_16x16x32_bf16 a[0:3], v[128:131], v[80:83], a[0:3]\n" \
"ds_read_b128 v[104:107], v49 offset:36864\n" \
"ds_read_b128 v[108:111], v48 offset:38912\n" \
"v_mfma_f32_16x16x32_bf16 a[4:7], v[120:123], v[80:83], a[4:7]\n" \
"s_add_u32 s62, s62, 0x80\n" \
"ds_read_b128 v[112:115], v49 offset:38912\n" \
"v_mfma_f32_16x16x32_bf16 a[4:7], v[116:119], v[76:79], a[4:7]\n" \
"s_cmp_lt_u32 s62, 0x480\n" \
"s_cselect_b32 s62, s62, 0\n" \
"v_mfma_f32_16x16x32_bf16 a[32:35], v[76:79], v[132:135], a[32:35]\n" \
"s_lshl_b32 s72, s62, 6\n" \
"s_add_u32 s72, s72, s37\n" \
"v_mfma_f32_16x16x32_bf16 a[32:35], v[80:83], v[136:139], a[32:35]\n" \
"s_lshl_b32 s63, s62, 12\n" \
"v_cvt_pk_bf16_f32 v252, v252, v253\n" \
"v_mfma_f32_16x16x32_bf16 a[128:131], v[152:155], v[56:59], a[128:131]\n" \
"v_cvt_pk_bf16_f32 v253, v254, v255\n" \
"v_cvt_pk_bf16_f32 v254, v180, v181\n" \
"v_mfma_f32_16x16x32_bf16 a[128:131], v[148:151], v[52:55], a[128:131]\n" \
"v_cvt_pk_bf16_f32 v255, v182, v183\n" \
"s_waitcnt lgkmcnt(11)\n" \
"v_mfma_f32_16x16x32_bf16 a[132:135], v[156:159], v[52:55], a[132:135]\n" \
"v_mfma_f32_16x16x32_bf16 a[132:135], v[160:163], v[56:59], a[132:135]\n" \
"v_mfma_f32_16x16x32_bf16 a[136:139], v[168:171], v[56:59], a[136:139]\n" \
"v_mfma_f32_16x16x32_bf16 a[136:139], v[164:167], v[52:55], a[136:139]\n" \
"s_waitcnt lgkmcnt(7)\n" \
"v_mfma_f32_16x16x32_bf16 a[140:143], v[172:175], v[52:55], a[140:143]\n" \
"v_mfma_f32_16x16x32_bf16 a[140:143], v[176:179], v[56:59], a[140:143]\n" \
"v_mfma_f32_16x16x32_bf16 a[160:163], v[152:155], v[64:67], a[160:163]\n" \
"v_mfma_f32_16x16x32_bf16 a[160:163], v[148:151], v[60:63], a[160:163]\n" \
"v_mfma_f32_16x16x32_bf16 a[164:167], v[156:159], v[60:63], a[164:167]\n" \
"v_mfma_f32_16x16x32_bf16 a[164:167], v[160:163], v[64:67], a[164:167]\n" \
"v_mfma_f32_16x16x32_bf16 a[168:171], v[168:171], v[64:67], a[168:171]\n" \
"v_mfma_f32_16x16x32_bf16 a[168:171], v[164:167], v[60:63], a[168:171]\n" \
"v_mfma_f32_16x16x32_bf16 a[172:175], v[172:175], v[60:63], a[172:175]\n" \
"v_mfma_f32_16x16x32_bf16 a[172:175], v[176:179], v[64:67], a[172:175]\n" \
"v_mfma_f32_16x16x32_bf16 a[192:195], v[152:155], v[72:75], a[192:195]\n" \
"v_mfma_f32_16x16x32_bf16 a[192:195], v[148:151], v[68:71], a[192:195]\n" \
"s_waitcnt vmcnt(3) lgkmcnt(0)\n" \
"s_barrier\n" \
"v_mfma_f32_16x16x32_bf16 a[196:199], v[156:159], v[68:71], a[196:199]\n" \
"s_add_u32 m0, s40, 0x8000\n" \
"s_add_u32 s85, s72, 0x0\n" \
"s_nop 0\n" \
"v_mfma_f32_16x16x32_bf16 a[196:199], v[160:163], v[72:75], a[196:199]\n" \
"buffer_load_dwordx4 v20, s[80:83], s85 offen sc1 lds\n" \
"v_mfma_f32_16x16x32_bf16 a[200:203], v[168:171], v[72:75], a[200:203]\n" \
"s_add_u32 m0, s40, 0x9000\n" \
"s_add_u32 s84, s72, 0x1000\n" \
"v_mfma_f32_16x16x32_bf16 a[200:203], v[164:167], v[68:71], a[200:203]\n" \
"ds_read_b64_tr_b16 v[140:141], v39 offset:0\n" \
"ds_read_b64_tr_b16 v[142:143], v39 offset:960\n" \
"v_mfma_f32_16x16x32_bf16 a[204:207], v[172:175], v[68:71], a[204:207]\n" \
"buffer_load_dwordx4 v20, s[80:83], s84 offen sc1 lds\n" \
"v_mfma_f32_16x16x32_bf16 a[204:207], v[176:179], v[72:75], a[204:207]\n" \
"ds_read_b64_tr_b16 v[128:129], v37 offset:4096\n" \
"ds_read_b64_tr_b16 v[130:131], v37 offset:4608\n" \
"v_mfma_f32_16x16x32_bf16 a[224:227], v[152:155], v[80:83], a[224:227]\n" \
"s_add_u32 m0, s40, 0xb000\n" \
"s_add_u32 s85, s62, 0x0\n" \
"v_mfma_f32_16x16x32_bf16 a[224:227], v[148:151], v[76:79], a[224:227]\n" \
"buffer_load_dwordx4 v21, s[76:79], s85 offen lds\n" \
"v_mfma_f32_16x16x32_bf16 a[228:231], v[156:159], v[76:79], a[228:231]\n" \
"ds_read_b64_tr_b16 v[124:125], v37 offset:0\n" \
"ds_read_b64_tr_b16 v[126:127], v37 offset:512\n" \
"v_mfma_f32_16x16x32_bf16 a[228:231], v[160:163], v[80:83], a[228:231]\n" \
"s_add_u32 m0, %[mA], 0x8000\n" \
"s_add_u32 s84, s62, 0x0\n" \
"v_mfma_f32_16x16x32_bf16 a[232:235], v[168:171], v[80:83], a[232:235]\n" \
"buffer_load_dwordx4 v24, s[76:79], s84 offen lds\n" \
"v_mfma_f32_16x16x32_bf16 a[232:235], v[164:167], v[76:79], a[232:235]\n" \
"ds_read_b64_tr_b16 v[132:133], v38 offset:0\n" \
"ds_read_b64_tr_b16 v[134:135], v38 offset:512\n" \
"v_mfma_f32_16x16x32_bf16 a[236:239], v[172:175], v[76:79], a[236:239]\n" \
"ds_read_b64_tr_b16 v[136:137], v38 offset:4096\n" \
"ds_read_b64_tr_b16 v[138:139], v38 offset:4608\n" \
"v_mfma_f32_16x16x32_bf16 a[236:239], v[176:179], v[80:83], a[236:239]\n" \
"s_add_u32 m0, %[mA], 0x8400\n" \
"s_add_u32 s85, s62, 0x2400\n" \
"v_mfma_f32_16x16x32_bf16 a[144:147], v[152:155], v[88:91], a[144:147]\n" \
"buffer_load_dwordx4 v25, s[76:79], s85 offen lds\n" \
"v_mfma_f32_16x16x32_bf16 a[144:147], v[148:151], v[84:87], a[144:147]\n" \
"ds_read_b128 v[52:55], v28 offset:0\n" \
"ds_read_b128 v[56:59], v29 offset:0\n" \
"v_mfma_f32_16x16x32_bf16 a[148:151], v[156:159], v[84:87], a[148:151]\n" \
"s_add_u32 m0, %[mB], 0x8000\n" \
"s_add_u32 s84, s63, 0x0\n" \
"s_nop 0\n" \
"v_mfma_f32_16x16x32_bf16 a[148:151], v[160:163], v[88:91], a[148:151]\n" \
"buffer_load_dwordx4 v26, s[68:71], s84 offen lds\n" \
"v_mfma_f32_16x16x32_bf16 a[152:155], v[168:171], v[88:91], a[152:155]\n" \
"ds_read_b64_tr_b16 v[116:117], v36 offset:0\n" \
"ds_read_b64_tr_b16 v[118:119], v36 offset:512\n" \
"v_mfma_f32_16x16x32_bf16 a[152:155], v[164:167], v[84:87], a[152:155]\n" \
"ds_read_b64_tr_b16 v[120:121], v36 offset:4096\n" \
"ds_read_b64_tr_b16 v[122:123], v36 offset:4608\n" \
"v_mfma_f32_16x16x32_bf16 a[156:159], v[172:175], v[84:87], a[156:159]\n" \
"s_add_u32 m0, %[mA], 0x8800\n" \
"s_add_u32 s85, s62, 0x4800\n" \
"v_mfma_f32_16x16x32_bf16 a[156:159], v[176:179], v[88:91], a[156:159]\n" \
"buffer_load_dwordx4 v24, s[76:79], s85 offen lds\n" \
"v_mfma_f32_16x16x32_bf16 a[176:179], v[152:155], v[96:99], a[176:179]\n" \
"ds_read_b128 v[84:87], v22 offset:0\n" \
"ds_read_b128 v[88:91], v23 offset:0\n" \
"v_mfma_f32_16x16x32_bf16 a[176:179], v[148:151], v[92:95], a[176:179]\n" \
"s_add_u32 m0, %[mA], 0x8c00\n" \
"s_add_u32 s84, s62, 0x6c00\n" \
"v_mfma_f32_16x16x32_bf16 a[180:183], v[156:159], v[92:95], a[180:183]\n" \
"buffer_load_dwordx4 v25, s[76:79], s84 offen lds\n" \
"v_mfma_f32_16x16x32_bf16 a[180:183], v[160:163], v[96:99], a[180:183]\n" \
"ds_read_b128 v[60:63], v28 offset:2048\n" \
"ds_read_b128 v[64:67], v29 offset:2048\n" \
"v_mfma_f32_16x16x32_bf16 a[184:187], v[168:171], v[96:99], a[184:187]\n" \
"ds_read_b128 v[68:71], v28 offset:4096\n" \
"ds_read_b128 v[72:75], v29 offset:4096\n" \
"v_mfma_f32_16x16x32_bf16 a[184:187], v[164:167], v[92:95], a[184:187]\n" \
"s_add_u32 m0, %[mB], 0x8400\n" \
"s_add_u32 s85, s63, 0x8000\n" \
"v_mfma_f32_16x16x32_bf16 a[188:191], v[172:175], v[92:95], a[188:191]\n" \
"buffer_load_dwordx4 v26, s[68:71], s85 offen lds\n" \
"v_mfma_f32_16x16x32_bf16 a[188:191], v[176:179], v[96:99], a[188:191]\n" \
"ds_read_b128 v[76:79], v28 offset:6144\n" \
"ds_read_b128 v[80:83], v29 offset:6144\n" \
"v_mfma_f32_16x16x32_bf16 a[208:211], v[152:155], v[104:107], a[208:211]\n" \
"s_add_u32 m0, %[mA], 0x9000\n" \
"s_add_u32 s84, s62, 0x9000\n" \
"v_mfma_f32_16x16x32_bf16 a[208:211], v[148:151], v[100:103], a[208:211]\n" \
"buffer_load_dwordx4 v24, s[76:79], s84 offen lds\n" \
"v_mfma_f32_16x16x32_bf16 a[212:215], v[156:159], v[100:103], a[212:215]\n" \
"ds_read_b128 v[92:95], v48 offset:2048\n" \
"ds_read_b128 v[96:99], v49 offset:2048\n" \
"v_mfma_f32_16x16x32_bf16 a[212:215], v[160:163], v[104:107], a[212:215]\n" \
"v_mfma_f32_16x16x32_bf16 a[216:219], v[168:171], v[104:107], a[216:219]\n" \
"s_add_u32 m0, %[mA], 0x9400\n" \
"s_add_u32 s85, s62, 0xb400\n" \
"v_mfma_f32_16x16x32_bf16 a[216:219], v[164:167], v[100:103], a[216:219]\n" \
"buffer_load_dwordx4 v25, s[76:79], s85 offen lds\n" \
"v_mfma_f32_16x16x32_bf16 a[220:223], v[172:175], v[100:103], a[220:223]\n" \
"ds_read_b128 v[100:103], v48 offset:4096\n" \
"v_mfma_f32_16x16x32_bf16 a[220:223], v[176:179], v[104:107], a[220:223]\n" \
"s_add_u32 m0, %[mB], 0x8800\n" \
"s_add_u32 s84, s63, 0x10000\n" \
"v_mfma_f32_16x16x32_bf16 a[240:243], v[152:155], v[112:115], a[240:243]\n" \
"buffer_load_dwordx4 v27, s[68:71], s84 offen lds\n" \
"v_mfma_f32_16x16x32_bf16 a[240:243], v[148:151], v[108:111], a[240:243]\n" \
"ds_read_b64_tr_b16 v[152:153], v32 offset:8192\n" \
"ds_read_b64_tr_b16 v[154:155], v32 offset:9216\n" \
"v_mfma_f32_16x16x32_bf16 a[244:247], v[156:159], v[108:111], a[244:247]\n" \
"ds_read_b64_tr_b16 v[148:149], v32 offset:0\n" \
"ds_read_b64_tr_b16 v[150:151], v32 offset:1024\n" \
"v_mfma_f32_16x16x32_bf16 a[244:247], v[160:163], v[112:115], a[244:247]\n" \
"s_add_u32 m0, %[mA], 0x9800\n" \
"s_add_u32 s85, s62, 0xd800\n" \
"v_mfma_f32_16x16x32_bf16 a[248:251], v[168:171], v[112:115], a[248:251]\n" \
"buffer_load_dwordx4 v24, s[76:79], s85 offen lds\n" \
"v_mfma_f32_16x16x32_bf16 a[248:251], v[164:167], v[108:111], a[248:251]\n" \
"ds_read_b64_tr_b16 v[156:157], v33 offset:0\n" \
"ds_read_b64_tr_b16 v[158:159], v33 offset:1024\n" \
"v_mfma_f32_16x16x32_bf16 a[252:255], v[172:175], v[108:111], a[252:255]\n" \
"ds_read_b64_tr_b16 v[160:161], v33 offset:8192\n" \
"ds_read_b64_tr_b16 v[162:163], v33 offset:9216\n" \
"v_mfma_f32_16x16x32_bf16 a[252:255], v[176:179], v[112:115], a[252:255]\n" \
"s_add_u32 m0, %[mA], 0x9c00\n" \
"s_waitcnt lgkmcnt(11)\n" \
"s_nop 0\n" \
"v_smfmac_f32_16x16x64_bf16 a[16:19], v[140:143], v[84:91], v50\n" \
"v_mfma_f32_16x16x32_bf16 a[48:51], v[128:131], v[88:91], a[48:51]\n" \
"s_add_u32 s85, s62, 0xfc00\n" \
"ds_read_b64_tr_b16 v[168:169], v34 offset:8192\n" \
"v_mfma_f32_16x16x32_bf16 a[48:51], v[124:127], v[84:87], a[48:51]\n" \
"buffer_load_dwordx4 v25, s[76:79], s85 offen lds\n" \
"v_mfma_f32_16x16x32_bf16 a[80:83], v[84:87], v[132:135], a[80:83]\n" \
"s_add_u32 m0, %[mB], 0x8c00\n" \
"s_add_u32 s84, s63, 0x18000\n" \
"v_mfma_f32_16x16x32_bf16 a[80:83], v[88:91], v[136:139], a[80:83]\n" \
"ds_read_b64_tr_b16 v[170:171], v34 offset:9216\n" \
"ds_read_b64_tr_b16 v[164:165], v34 offset:0\n" \
"v_smfmac_f32_16x16x64_bf16 a[112:115], v[140:143], v[52:59], v50\n" \
"v_mfma_f32_16x16x32_bf16 a[20:23], v[128:131], v[56:59], a[20:23]\n" \
"buffer_load_dwordx4 v27, s[68:71], s84 offen lds\n" \
"v_mfma_f32_16x16x32_bf16 a[20:23], v[124:127], v[52:55], a[20:23]\n" \
"s_add_u32 s89, s44, 0x1000\n" \
"ds_read_b64_tr_b16 v[166:167], v34 offset:1024\n" \
"v_mfma_f32_16x16x32_bf16 a[52:55], v[116:119], v[52:55], a[52:55]\n" \
"ds_read_b64_tr_b16 v[172:173], v35 offset:0\n" \
"ds_read_b64_tr_b16 v[174:175], v35 offset:1024\n" \
"v_mfma_f32_16x16x32_bf16 a[52:55], v[120:123], v[56:59], a[52:55]\n" \
"buffer_store_dwordx4 v[212:215], v20, s[80:83], s89 offen offset:3072 sc1\n" \
"v_mfma_f32_16x16x32_bf16 a[84:87], v[56:59], v[136:139], a[84:87]\n" \
"ds_read_b64_tr_b16 v[176:177], v35 offset:8192\n" \
"ds_read_b64_tr_b16 v[178:179], v35 offset:9216\n" \
"v_mfma_f32_16x16x32_bf16 a[84:87], v[52:55], v[132:135], a[84:87]\n" \
"ds_read_b128 v[84:87], v48 offset:0\n" \
"ds_read_b128 v[88:91], v49 offset:0\n" \
"v_smfmac_f32_16x16x64_bf16 a[116:119], v[140:143], v[60:67], v50\n" \
"v_mfma_f32_16x16x32_bf16 a[24:27], v[124:127], v[60:63], a[24:27]\n" \
"ds_read_b128 v[104:107], v49 offset:4096\n" \
"ds_read_b128 v[108:111], v48 offset:6144\n" \
"v_mfma_f32_16x16x32_bf16 a[24:27], v[128:131], v[64:67], a[24:27]\n" \
"s_add_u32 s88, s42, 0x0\n" \
"ds_read_b128 v[112:115], v49 offset:6144\n" \
"v_mfma_f32_16x16x32_bf16 a[56:59], v[120:123], v[64:67], a[56:59]\n" \
"buffer_store_dwordx4 v[216:219], v20, s[80:83], s88 offen offset:0 sc1\n" \
"v_mfma_f32_16x16x32_bf16 a[56:59], v[116:119], v[60:63], a[56:59]\n" \
"s_add_u32 s62, s62, 0x80\n" \
"s_add_u32 s94, %[b], %[w]\n" \
"v_mfma_f32_16x16x32_bf16 a[88:91], v[60:63], v[132:135], a[88:91]\n" \
"s_cmp_lt_u32 s62, 0x480\n" \
"s_cselect_b32 s62, s62, 0\n" \
"v_mfma_f32_16x16x32_bf16 a[88:91], v[64:67], v[136:139], a[88:91]\n" \
"s_lshl_b32 s72, s62, 6\n" \
"s_add_u32 s72, s72, s37\n" \
"v_smfmac_f32_16x16x64_bf16 a[120:123], v[140:143], v[68:75], v50\n" \
"v_mfma_f32_16x16x32_bf16 a[28:31], v[128:131], v[72:75], a[28:31]\n" \
"s_lshl_b32 s63, s62, 12\n" \
"s_and_b32 s96, 7, %[b]\n" \
"v_mfma_f32_16x16x32_bf16 a[28:31], v[124:127], v[68:71], a[28:31]\n" \
"s_cmp_lg_u32 s94, 0\n" \
"s_cselect_b32 s95, 0x40000000, 0x5bc0000\n" \
"v_mfma_f32_16x16x32_bf16 a[60:63], v[116:119], v[68:71], a[60:63]\n" \
"s_add_u32 s89, s42, 0x1000\n" \
"s_mul_i32 s97, 0x48, s96\n" \
"v_mfma_f32_16x16x32_bf16 a[60:63], v[120:123], v[72:75], a[60:63]\n" \
"buffer_store_dwordx4 v[220:223], v20, s[80:83], s89 offen offset:0 sc1\n" \
"v_mfma_f32_16x16x32_bf16 a[92:95], v[72:75], v[136:139], a[92:95]\n" \
"s_add_u32 s98, 0x47, s97\n" \
"v_writelane_b32 v11, s95, 50\n" \
"v_mfma_f32_16x16x32_bf16 a[92:95], v[68:71], v[132:135], a[92:95]\n" \
"s_cmp_lt_u32 %[w], 3\n" \
"s_cselect_b32 s99, s98, 0xfff\n" \
"v_smfmac_f32_16x16x64_bf16 a[124:127], v[140:143], v[76:83], v50\n" \
"v_mfma_f32_16x16x32_bf16 a[0:3], v[124:127], v[76:79], a[0:3]\n" \
"s_lshr_b32 s94, %[b], 3\n" \
"s_lshl_b32 s67, s96, 2\n" \
"v_mfma_f32_16x16x32_bf16 a[0:3], v[128:131], v[80:83], a[0:3]\n" \
"s_and_b32 s95, 3, s94\n" \
"s_mul_i32 s100, 0x480, s99\n" \
"v_mfma_f32_16x16x32_bf16 a[4:7], v[120:123], v[80:83], a[4:7]\n" \
"s_lshl_b32 s64, %[w], 5\n" \
"s_add_u32 s98, s67, s95\n" \
"v_mfma_f32_16x16x32_bf16 a[4:7], v[116:119], v[76:79], a[4:7]\n" \
"s_add_u32 s101, 0x120, s100\n" \
"s_add_u32 s65, s97, s64\n" \
"v_mfma_f32_16x16x32_bf16 a[32:35], v[76:79], v[132:135], a[32:35]\n" \
"s_mul_i32 s99, 0x24000, s98\n" \
"v_writelane_b32 v11, s101, 15\n" \
"v_mfma_f32_16x16x32_bf16 a[32:35], v[80:83], v[136:139], a[32:35]\n" \
"s_mul_i32 s66, 0x480, s65\n" \
"s_waitcnt lgkmcnt(0)\n" \
"v_mfma_f32_16x16x32_bf16 a[128:131], v[152:155], v[56:59], a[128:131]\n" \
"s_add_u32 s100, 0x1b000, s99\n" \
"s_min_u32 s97, 2, %[w]\n" \
"v_mfma_f32_16x16x32_bf16 a[128:131], v[148:151], v[52:55], a[128:131]\n" \
"s_cmp_lt_u32 %[w], 3\n" \
"s_cselect_b32 s101, s66, s100\n" \
"v_mfma_f32_16x16x32_bf16 a[132:135], v[156:159], v[52:55], a[132:135]\n" \
"s_mul_i32 s64, 0x2400, s97\n" \
"v_writelane_b32 v11, s101, 16\n" \
"v_mfma_f32_16x16x32_bf16 a[132:135], v[160:163], v[56:59], a[132:135]\n" \
"s_add_u32 s65, 0x20000, s64\n" \
"s_mul_i32 s94, 0x9000, s97\n" \
"v_mfma_f32_16x16x32_bf16 a[136:139], v[168:171], v[56:59], a[136:139]\n" \
"s_mul_i32 s95, 0x2400, %[w]\n" \
"v_writelane_b32 v11, s65, 17\n" \
"v_mfma_f32_16x16x32_bf16 a[136:139], v[164:167], v[52:55], a[136:139]\n" \
"s_add_u32 s67, s99, s94\n" \
"v_writelane_b32 v11, s67, 22\n" \
"v_mfma_f32_16x16x32_bf16 a[140:143], v[172:175], v[52:55], a[140:143]\n" \
"s_cmp_lt_u32 %[w], 3\n" \
"s_cselect_b32 s98, s95, 0x12000\n" \
"v_mfma_f32_16x16x32_bf16 a[140:143], v[176:179], v[56:59], a[140:143]\n" \
"s_add_u32 s66, 0x20000, s95\n" \
"v_writelane_b32 v11, s98, 18\n" \
"v_mfma_f32_16x16x32_bf16 a[160:163], v[152:155], v[64:67], a[160:163]\n" \
"s_cmp_lt_u32 %[w], 3\n" \
"s_cselect_b32 s100, s66, 0xfc00\n" \
"v_mfma_f32_16x16x32_bf16 a[160:163], v[148:151], v[60:63], a[160:163]\n" \
"v_writelane_b32 v11, s95, 19\n" \
"s_lshl_b32 s101, %[w], 10\n" \
"v_mfma_f32_16x16x32_bf16 a[164:167], v[156:159], v[60:63], a[164:167]\n" \
"s_lshr_b32 s65, s96, 1\n" \
"v_writelane_b32 v11, s100, 20\n" \
"v_mfma_f32_16x16x32_bf16 a[164:167], v[160:163], v[64:67], a[164:167]\n" \
"s_cmp_eq_u32 %[w], s65\n" \
"s_cselect_b32 s97, 0xff0000, 0\n" \
"v_mfma_f32_16x16x32_bf16 a[168:171], v[168:171], v[64:67], a[168:171]\n" \
"s_add_u32 s64, 0x1d400, s101\n" \
"v_writelane_b32 v11, s64, 21\n" \
"v_mfma_f32_16x16x32_bf16 a[168:171], v[164:167], v[60:63], a[168:171]\n" \
"v_writelane_b32 v11, s97, 6\n" \
"v_mfma_f32_16x16x32_bf16 a[172:175], v[172:175], v[60:63], a[172:175]\n" \
"v_mfma_f32_16x16x32_bf16 a[172:175], v[176:179], v[64:67], a[172:175]\n" \
"v_mfma_f32_16x16x32_bf16 a[192:195], v[152:155], v[72:75], a[192:195]\n" \
"v_mfma_f32_16x16x32_bf16 a[192:195], v[148:151], v[68:71], a[192:195]\n" \
"s_waitcnt vmcnt(3)\n" \
"s_barrier\n" \
"v_mfma_f32_16x16x32_bf16 a[196:199], v[156:159], v[68:71], a[196:199]\n" \
"s_add_u32 m0, s40, 0x0\n" \
"s_add_u32 s85, s72, 0x0\n" \
"s_nop 0\n" \
"v_mfma_f32_16x16x32_bf16 a[196:199], v[160:163], v[72:75], a[196:199]\n" \
"buffer_load_dwordx4 v20, s[80:83], s85 offen sc1 lds\n" \
"v_mfma_f32_16x16x32_bf16 a[200:203], v[168:171], v[72:75], a[200:203]\n" \
"s_add_u32 m0, s40, 0x1000\n" \
"s_add_u32 s84, s72, 0x1000\n" \
"v_mfma_f32_16x16x32_bf16 a[200:203], v[164:167], v[68:71], a[200:203]\n" \
"ds_read_b64_tr_b16 v[140:141], v39 offset:32768\n" \
"ds_read_b64_tr_b16 v[142:143], v39 offset:33728\n" \
"v_mfma_f32_16x16x32_bf16 a[204:207], v[172:175], v[68:71], a[204:207]\n" \
"buffer_load_dwordx4 v20, s[80:83], s84 offen sc1 lds\n" \
"v_mfma_f32_16x16x32_bf16 a[204:207], v[176:179], v[72:75], a[204:207]\n" \
"ds_read_b64_tr_b16 v[128:129], v37 offset:36864\n" \
"ds_read_b64_tr_b16 v[130:131], v37 offset:37376\n" \
"v_mfma_f32_16x16x32_bf16 a[224:227], v[152:155], v[80:83], a[224:227]\n" \
"s_add_u32 m0, s40, 0x3000\n" \
"s_add_u32 s85, s62, 0x0\n" \
"v_mfma_f32_16x16x32_bf16 a[224:227], v[148:151], v[76:79], a[224:227]\n" \
"buffer_load_dwordx4 v21, s[76:79], s85 offen lds\n" \
"v_mfma_f32_16x16x32_bf16 a[228:231], v[156:159], v[76:79], a[228:231]\n" \
"ds_read_b64_tr_b16 v[124:125], v37 offset:32768\n" \
"ds_read_b64_tr_b16 v[126:127], v37 offset:33280\n" \
"v_mfma_f32_16x16x32_bf16 a[228:231], v[160:163], v[80:83], a[228:231]\n" \
"s_add_u32 m0, %[mA], 0x0\n" \
"s_add_u32 s84, s62, 0x0\n" \
"s_nop 0\n" \
"v_mfma_f32_16x16x32_bf16 a[232:235], v[168:171], v[80:83], a[232:235]\n" \
"buffer_load_dwordx4 v24, s[76:79], s84 offen lds\n" \
"v_mfma_f32_16x16x32_bf16 a[232:235], v[164:167], v[76:79], a[232:235]\n" \
"ds_read_b64_tr_b16 v[132:133], v38 offset:32768\n" \
"ds_read_b64_tr_b16 v[134:135], v38 offset:33280\n" \
"v_mfma_f32_16x16x32_bf16 a[236:239], v[172:175], v[76:79], a[236:239]\n" \
"ds_read_b64_tr_b16 v[136:137], v38 offset:36864\n" \
"ds_read_b64_tr_b16 v[138:139], v38 offset:37376\n" \
"L_pf6:\n" \
"s_cbranch_execz L_pf10\n" \
"s_nop 0\n" \
"v_mfma_f32_16x16x32_bf16 a[236:239], v[176:179], v[80:83], a[236:239]\n" \
"s_add_u32 m0, %[mA], 0x400\n" \
"s_add_u32 s85, s62, 0x2400\n" \
"v_mfma_f32_16x16x32_bf16 a[144:147], v[152:155], v[88:91], a[144:147]\n" \
"buffer_load_dwordx4 v25, s[76:79], s85 offen lds\n" \
"v_mfma_f32_16x16x32_bf16 a[144:147], v[148:151], v[84:87], a[144:147]\n" \
"ds_read_b128 v[52:55], v28 offset:32768\n" \
"ds_read_b128 v[56:59], v29 offset:32768\n" \
"v_mfma_f32_16x16x32_bf16 a[148:151], v[156:159], v[84:87], a[148:151]\n" \
"s_add_u32 m0, %[mB], 0x0\n" \
"s_add_u32 s84, s63, 0x0\n" \
"v_mfma_f32_16x16x32_bf16 a[148:151], v[160:163], v[88:91], a[148:151]\n" \
"buffer_load_dwordx4 v26, s[68:71], s84 offen lds\n" \
"v_mfma_f32_16x16x32_bf16 a[152:155], v[168:171], v[88:91], a[152:155]\n" \
"ds_read_b64_tr_b16 v[116:117], v36 offset:32768\n" \
"ds_read_b64_tr_b16 v[118:119], v36 offset:33280\n" \
"v_mfma_f32_16x16x32_bf16 a[152:155], v[164:167], v[84:87], a[152:155]\n" \
"ds_read_b64_tr_b16 v[120:121], v36 offset:36864\n" \
"ds_read_b64_tr_b16 v[122:123], v36 offset:37376\n" \
"v_mfma_f32_16x16x32_bf16 a[156:159], v[172:175], v[84:87], a[156:159]\n" \
"s_add_u32 m0, %[mA], 0x800\n" \
"s_add_u32 s85, s62, 0x4800\n" \
"v_mfma_f32_16x16x32_bf16 a[156:159], v[176:179], v[88:91], a[156:159]\n" \
"buffer_load_dwordx4 v24, s[76:79], s85 offen lds\n" \
"v_mfma_f32_16x16x32_bf16 a[176:179], v[152:155], v[96:99], a[176:179]\n" \
"ds_read_b128 v[84:87], v22 offset:32768\n" \
"ds_read_b128 v[88:91], v23 offset:32768\n" \
"v_mfma_f32_16x16x32_bf16 a[176:179], v[148:151], v[92:95], a[176:179]\n" \
"s_add_u32 m0, %[mA], 0xc00\n" \
"s_add_u32 s84, s62, 0x6c00\n" \
"v_mfma_f32_16x16x32_bf16 a[180:183], v[156:159], v[92:95], a[180:183]\n" \
"buffer_load_dwordx4 v25, s[76:79], s84 offen lds\n" \
"v_mfma_f32_16x16x32_bf16 a[180:183], v[160:163], v[96:99], a[180:183]\n" \
"ds_read_b128 v[60:63], v28 offset:34816\n" \
"ds_read_b128 v[64:67], v29 offset:34816\n" \
"v_mfma_f32_16x16x32_bf16 a[184:187], v[168:171], v[96:99], a[184:187]\n" \
"ds_read_b128 v[68:71], v28 offset:36864\n" \
"ds_read_b128 v[72:75], v29 offset:36864\n" \
"v_mfma_f32_16x16x32_bf16 a[184:187], v[164:167], v[92:95], a[184:187]\n" \
"s_add_u32 m0, %[mB], 0x400\n" \
"s_add_u32 s85, s63, 0x8000\n" \
"v_mfma_f32_16x16x32_bf16 a[188:191], v[172:175], v[92:95], a[188:191]\n" \
"buffer_load_dwordx4 v26, s[68:71], s85 offen lds\n" \
"v_mfma_f32_16x16x32_bf16 a[188:191], v[176:179], v[96:99], a[188:191]\n" \
"ds_read_b128 v[76:79], v28 offset:38912\n" \
"ds_read_b128 v[80:83], v29 offset:38912\n" \
"v_mfma_f32_16x16x32_bf16 a[208:211], v[152:155], v[104:107], a[208:211]\n" \
"s_add_u32 m0, %[mA], 0x1000\n" \
"s_add_u32 s84, s62, 0x9000\n" \
"v_mfma_f32_16x16x32_bf16 a[208:211], v[148:151], v[100:103], a[208:211]\n" \
"buffer_load_dwordx4 v24, s[76:79], s84 offen lds\n" \
"v_mfma_f32_16x16x32_bf16 a[212:215], v[156:159], v[100:103], a[212:215]\n" \
"ds_read_b128 v[92:95], v48 offset:34816\n" \
"ds_read_b128 v[96:99], v49 offset:34816\n" \
"v_mfma_f32_16x16x32_bf16 a[212:215], v[160:163], v[104:107], a[212:215]\n" \
"v_mfma_f32_16x16x32_bf16 a[216:219], v[168:171], v[104:107], a[216:219]\n" \
"s_add_u32 m0, %[mA], 0x1400\n" \
"s_add_u32 s85, s62, 0xb400\n" \
"v_mfma_f32_16x16x32_bf16 a[216:219], v[164:167], v[100:103], a[216:219]\n" \
"buffer_load_dwordx4 v25, s[76:79], s85 offen lds\n" \
"v_mfma_f32_16x16x32_bf16 a[220:223], v[172:175], v[100:103], a[220:223]\n" \
"ds_read_b128 v[100:103], v48 offset:36864\n" \
"v_mfma_f32_16x16x32_bf16 a[220:223], v[176:179], v[104:107], a[220:223]\n" \
"s_add_u32 m0, %[mB], 0x800\n" \
"s_add_u32 s84, s63, 0x10000\n" \
"v_mfma_f32_16x16x32_bf16 a[240:243], v[152:155], v[112:115], a[240:243]\n" \
"buffer_load_dwordx4 v27, s[68:71], s84 offen lds\n" \
"v_mfma_f32_16x16x32_bf16 a[240:243], v[148:151], v[108:111], a[240:243]\n" \
"ds_read_b64_tr_b16 v[152:153], v32 offset:40960\n" \
"ds_read_b64_tr_b16 v[154:155], v32 offset:41984\n" \
"v_mfma_f32_16x16x32_bf16 a[244:247], v[156:159], v[108:111], a[244:247]\n" \
"ds_read_b64_tr_b16 v[148:149], v32 offset:32768\n" \
"ds_read_b64_tr_b16 v[150:151], v32 offset:33792\n" \
"v_mfma_f32_16x16x32_bf16 a[244:247], v[160:163], v[112:115], a[244:247]\n" \
"s_add_u32 m0, %[mA], 0x1800\n" \
"s_add_u32 s85, s62, 0xd800\n" \
"v_mfma_f32_16x16x32_bf16 a[248:251], v[168:171], v[112:115], a[248:251]\n" \
"buffer_load_dwordx4 v24, s[76:79], s85 offen lds\n" \
"v_mfma_f32_16x16x32_bf16 a[248:251], v[164:167], v[108:111], a[248:251]\n" \
"ds_read_b64_tr_b16 v[156:157], v33 offset:32768\n" \
"ds_read_b64_tr_b16 v[158:159], v33 offset:33792\n" \
"v_mfma_f32_16x16x32_bf16 a[252:255], v[172:175], v[108:111], a[252:255]\n" \
"ds_read_b64_tr_b16 v[160:161], v33 offset:40960\n" \
"ds_read_b64_tr_b16 v[162:163], v33 offset:41984\n" \
"v_mfma_f32_16x16x32_bf16 a[252:255], v[176:179], v[112:115], a[252:255]\n" \
"s_add_u32 m0, %[mA], 0x1c00\n" \
"s_waitcnt lgkmcnt(11)\n" \
"s_nop 0\n" \
"v_smfmac_f32_16x16x64_bf16 a[16:19], v[140:143], v[84:91], v50\n" \
"v_mfma_f32_16x16x32_bf16 a[48:51], v[128:131], v[88:91], a[48:51]\n" \
"s_add_u32 s85, s62, 0xfc00\n" \
"ds_read_b64_tr_b16 v[168:169], v34 offset:40960\n" \
"v_mfma_f32_16x16x32_bf16 a[48:51], v[124:127], v[84:87], a[48:51]\n" \
"buffer_load_dwordx4 v25, s[76:79], s85 offen lds\n" \
"v_mfma_f32_16x16x32_bf16 a[80:83], v[84:87], v[132:135], a[80:83]\n" \
"s_add_u32 m0, %[mB], 0xc00\n" \
"s_add_u32 s84, s63, 0x18000\n" \
"v_mfma_f32_16x16x32_bf16 a[80:83], v[88:91], v[136:139], a[80:83]\n" \
"ds_read_b64_tr_b16 v[170:171], v34 offset:41984\n" \
"ds_read_b64_tr_b16 v[164:165], v34 offset:32768\n" \
"v_smfmac_f32_16x16x64_bf16 a[112:115], v[140:143], v[52:59], v50\n" \
"v_mfma_f32_16x16x32_bf16 a[20:23], v[128:131], v[56:59], a[20:23]\n" \
"buffer_load_dwordx4 v27, s[68:71], s84 offen lds\n" \
"v_mfma_f32_16x16x32_bf16 a[20:23], v[124:127], v[52:55], a[20:23]\n" \
"s_add_u32 s88, s42, 0x2000\n" \
"ds_read_b64_tr_b16 v[166:167], v34 offset:33792\n" \
"v_mfma_f32_16x16x32_bf16 a[52:55], v[116:119], v[52:55], a[52:55]\n" \
"ds_read_b64_tr_b16 v[172:173], v35 offset:32768\n" \
"ds_read_b64_tr_b16 v[174:175], v35 offset:33792\n" \
"v_mfma_f32_16x16x32_bf16 a[52:55], v[120:123], v[56:59], a[52:55]\n" \
"buffer_store_dwordx4 v[224:227], v20, s[80:83], s88 offen offset:0 sc1\n" \
"v_mfma_f32_16x16x32_bf16 a[84:87], v[56:59], v[136:139], a[84:87]\n" \
"ds_read_b64_tr_b16 v[176:177], v35 offset:40960\n" \
"ds_read_b64_tr_b16 v[178:179], v35 offset:41984\n" \
"v_mfma_f32_16x16x32_bf16 a[84:87], v[52:55], v[132:135], a[84:87]\n" \
"ds_read_b128 v[84:87], v48 offset:32768\n" \
"ds_read_b128 v[88:91], v49 offset:32768\n" \
"v_smfmac_f32_16x16x64_bf16 a[116:119], v[140:143], v[60:67], v50\n" \
"v_mfma_f32_16x16x32_bf16 a[24:27], v[124:127], v[60:63], a[24:27]\n" \
"ds_read_b128 v[104:107], v49 offset:36864\n" \
"ds_read_b128 v[108:111], v48 offset:38912\n" \
"v_mfma_f32_16x16x32_bf16 a[24:27], v[128:131], v[64:67], a[24:27]\n" \
"s_add_u32 s89, s42, 0x3000\n" \
"ds_read_b128 v[112:115], v49 offset:38912\n" \
"v_mfma_f32_16x16x32_bf16 a[56:59], v[120:123], v[64:67], a[56:59]\n" \
"buffer_store_dwordx4 v[228:231], v20, s[80:83], s89 offen offset:0 sc1\n" \
"v_mfma_f32_16x16x32_bf16 a[56:59], v[116:119], v[60:63], a[56:59]\n" \
"s_add_u32 s62, s62, 0x80\n" \
"s_lshr_b32 s96, %[b], 3\n" \
"v_mfma_f32_16x16x32_bf16 a[88:91], v[60:63], v[132:135], a[88:91]\n" \
"s_cmp_lt_u32 s62, 0x480\n" \
"s_cselect_b32 s62, s62, 0\n" \
"v_mfma_f32_16x16x32_bf16 a[88:91], v[64:67], v[136:139], a[88:91]\n" \
"s_lshl_b32 s72, s62, 6\n" \
"s_add_u32 s72, s72, s37\n" \
"v_smfmac_f32_16x16x64_bf16 a[120:123], v[140:143], v[68:75], v50\n" \
"v_mfma_f32_16x16x32_bf16 a[28:31], v[128:131], v[72:75], a[28:31]\n" \
"s_lshl_b32 s63, s62, 12\n" \
"s_and_b32 s97, 3, s96\n" \
"v_mfma_f32_16x16x32_bf16 a[28:31], v[124:127], v[68:71], a[28:31]\n" \
"s_and_b32 s94, 7, %[b]\n" \
"s_add_u32 s98, 1, s97\n" \
"v_mfma_f32_16x16x32_bf16 a[60:63], v[116:119], v[68:71], a[60:63]\n" \
"s_add_u32 s88, s42, 0x0\n" \
"s_lshl_b32 s95, s94, 2\n" \
"v_mfma_f32_16x16x32_bf16 a[60:63], v[120:123], v[72:75], a[60:63]\n" \
"buffer_store_dwordx4 v[232:235], v20, s[80:83], s88 offen offset:1024 sc1\n" \
"v_mfma_f32_16x16x32_bf16 a[92:95], v[72:75], v[136:139], a[92:95]\n" \
"s_and_b32 s99, 3, s98\n" \
"s_add_u32 s100, s95, s99\n" \
"v_mfma_f32_16x16x32_bf16 a[92:95], v[68:71], v[132:135], a[92:95]\n" \
"s_mul_i32 s101, 0x24000, s100\n" \
"s_mul_i32 s64, 0x9000, %[w]\n" \
"v_smfmac_f32_16x16x64_bf16 a[124:127], v[140:143], v[76:83], v50\n" \
"v_mfma_f32_16x16x32_bf16 a[0:3], v[124:127], v[76:79], a[0:3]\n" \
"s_and_b32 s96, 2, %[w]\n" \
"s_add_u32 s65, s101, s64\n" \
"v_mfma_f32_16x16x32_bf16 a[0:3], v[128:131], v[80:83], a[0:3]\n" \
"s_lshr_b32 s66, %[b], 5\n" \
"s_lshl_b32 s98, s96, 1\n" \
"v_mfma_f32_16x16x32_bf16 a[4:7], v[120:123], v[80:83], a[4:7]\n" \
"s_and_b32 s99, 1, %[w]\n" \
"v_writelane_b32 v11, s65, 23\n" \
"v_mfma_f32_16x16x32_bf16 a[4:7], v[116:119], v[76:79], a[4:7]\n" \
"s_lshl_b32 s67, s66, 3\n" \
"s_add_u32 s101, s98, s99\n" \
"v_mfma_f32_16x16x32_bf16 a[32:35], v[76:79], v[132:135], a[32:35]\n" \
"s_add_u32 s65, s67, s101\n" \
"s_mul_i32 s66, 0x24800, s65\n" \
"v_mfma_f32_16x16x32_bf16 a[32:35], v[80:83], v[136:139], a[32:35]\n" \
"s_lshl_b32 s98, s100, 12\n" \
"s_waitcnt lgkmcnt(0)\n" \
"v_mfma_f32_16x16x32_bf16 a[128:131], v[152:155], v[56:59], a[128:131]\n" \
"s_add_u32 s96, 0x3000000, s66\n" \
"s_add_u32 s99, 0x4800, s98\n" \
"v_mfma_f32_16x16x32_bf16 a[128:131], v[148:151], v[52:55], a[128:131]\n" \
"s_add_u32 s65, s96, s99\n" \
"s_sub_u32 s100, 0x78, s67\n" \
"v_mfma_f32_16x16x32_bf16 a[132:135], v[156:159], v[52:55], a[132:135]\n" \
"v_writelane_b32 v11, s65, 26\n" \
"s_add_u32 s98, s100, s101\n" \
"v_mfma_f32_16x16x32_bf16 a[132:135], v[160:163], v[56:59], a[132:135]\n" \
"s_mul_i32 s65, 0x24800, s98\n" \
"s_add_u32 s67, 0x3000000, s65\n" \
"v_mfma_f32_16x16x32_bf16 a[136:139], v[168:171], v[56:59], a[136:139]\n" \
"s_add_u32 s101, 2, s97\n" \
"s_add_u32 s100, s67, s99\n" \
"v_mfma_f32_16x16x32_bf16 a[136:139], v[164:167], v[52:55], a[136:139]\n" \
"s_and_b32 s98, 3, s101\n" \
"v_writelane_b32 v11, s100, 29\n" \
"v_mfma_f32_16x16x32_bf16 a[140:143], v[172:175], v[52:55], a[140:143]\n" \
"s_add_u32 s99, s95, s98\n" \
"s_mul_i32 s100, 0x24000, s99\n" \
"v_mfma_f32_16x16x32_bf16 a[140:143], v[176:179], v[56:59], a[140:143]\n" \
"s_add_u32 s101, s100, s64\n" \
"s_lshl_b32 s98, s99, 12\n" \
"v_mfma_f32_16x16x32_bf16 a[160:163], v[152:155], v[64:67], a[160:163]\n" \
"v_writelane_b32 v11, s101, 24\n" \
"s_add_u32 s100, 0x4800, s98\n" \
"v_mfma_f32_16x16x32_bf16 a[160:163], v[148:151], v[60:63], a[160:163]\n" \
"s_add_u32 s101, s96, s100\n" \
"v_writelane_b32 v11, s101, 27\n" \
"v_mfma_f32_16x16x32_bf16 a[164:167], v[156:159], v[60:63], a[164:167]\n" \
"s_add_u32 s98, 3, s97\n" \
"s_add_u32 s99, s67, s100\n" \
"v_mfma_f32_16x16x32_bf16 a[164:167], v[160:163], v[64:67], a[164:167]\n" \
"s_and_b32 s101, 3, s98\n" \
"v_writelane_b32 v11, s99, 30\n" \
"v_mfma_f32_16x16x32_bf16 a[168:171], v[168:171], v[64:67], a[168:171]\n" \
"s_add_u32 s100, s95, s101\n" \
"s_mul_i32 s99, 0x24000, s100\n" \
"v_mfma_f32_16x16x32_bf16 a[168:171], v[164:167], v[60:63], a[168:171]\n" \
"s_lshl_b32 s98, s100, 12\n" \
"s_add_u32 s97, s99, s64\n" \
"v_mfma_f32_16x16x32_bf16 a[172:175], v[172:175], v[60:63], a[172:175]\n" \
"s_add_u32 s95, 0x4800, s98\n" \
"v_writelane_b32 v11, s97, 25\n" \
"v_mfma_f32_16x16x32_bf16 a[172:175], v[176:179], v[64:67], a[172:175]\n" \
"s_add_u32 s101, s96, s95\n" \
"v_writelane_b32 v11, s101, 28\n" \
"v_mfma_f32_16x16x32_bf16 a[192:195], v[152:155], v[72:75], a[192:195]\n" \
"s_add_u32 s99, s67, s95\n" \
"s_lshl_b32 s64, s94, 11\n" \
"v_mfma_f32_16x16x32_bf16 a[192:195], v[148:151], v[68:71], a[192:195]\n" \
"v_writelane_b32 v11, s99, 31\n" \
"s_add_u32 s97, s96, s64\n" \
"s_waitcnt vmcnt(3)\n" \
"s_barrier\n" \
"v_mfma_f32_16x16x32_bf16 a[196:199], v[156:159], v[68:71], a[196:199]\n" \
"s_add_u32 m0, s40, 0x8000\n" \
"s_add_u32 s85, s72, 0x0\n" \
"v_mfma_f32_16x16x32_bf16 a[196:199], v[160:163], v[72:75], a[196:199]\n" \
"buffer_load_dwordx4 v20, s[80:83], s85 offen sc1 lds\n" \
"v_mfma_f32_16x16x32_bf16 a[200:203], v[168:171], v[72:75], a[200:203]\n" \
"s_add_u32 m0, s40, 0x9000\n" \
"s_add_u32 s84, s72, 0x1000\n" \
"v_mfma_f32_16x16x32_bf16 a[200:203], v[164:167], v[68:71], a[200:203]\n" \
"ds_read_b64_tr_b16 v[140:141], v39 offset:0\n" \
"ds_read_b64_tr_b16 v[142:143], v39 offset:960\n" \
"v_mfma_f32_16x16x32_bf16 a[204:207], v[172:175], v[68:71], a[204:207]\n" \
"buffer_load_dwordx4 v20, s[80:83], s84 offen sc1 lds\n" \
"v_mfma_f32_16x16x32_bf16 a[204:207], v[176:179], v[72:75], a[204:207]\n" \
"ds_read_b64_tr_b16 v[128:129], v37 offset:4096\n" \
"ds_read_b64_tr_b16 v[130:131], v37 offset:4608\n" \
"v_mfma_f32_16x16x32_bf16 a[224:227], v[152:155], v[80:83], a[224:227]\n" \
"s_add_u32 m0, s40, 0xb000\n" \
"s_add_u32 s85, s62, 0x0\n" \
"v_mfma_f32_16x16x32_bf16 a[224:227], v[148:151], v[76:79], a[224:227]\n" \
"buffer_load_dwordx4 v21, s[76:79], s85 offen lds\n" \
"v_mfma_f32_16x16x32_bf16 a[228:231], v[156:159], v[76:79], a[228:231]\n" \
"ds_read_b64_tr_b16 v[124:125], v37 offset:0\n" \
"ds_read_b64_tr_b16 v[126:127], v37 offset:512\n" \
"v_mfma_f32_16x16x32_bf16 a[228:231], v[160:163], v[80:83], a[228:231]\n" \
"s_add_u32 m0, %[mA], 0x8000\n" \
"s_add_u32 s84, s62, 0x0\n" \
"v_mfma_f32_16x16x32_bf16 a[232:235], v[168:171], v[80:83], a[232:235]\n" \
"buffer_load_dwordx4 v24, s[76:79], s84 offen lds\n" \
"v_mfma_f32_16x16x32_bf16 a[232:235], v[164:167], v[76:79], a[232:235]\n" \
"ds_read_b64_tr_b16 v[132:133], v38 offset:0\n" \
"ds_read_b64_tr_b16 v[134:135], v38 offset:512\n" \
"v_mfma_f32_16x16x32_bf16 a[236:239], v[172:175], v[76:79], a[236:239]\n" \
"ds_read_b64_tr_b16 v[136:137], v38 offset:4096\n" \
"ds_read_b64_tr_b16 v[138:139], v38 offset:4608\n" \
"v_mfma_f32_16x16x32_bf16 a[236:239], v[176:179], v[80:83], a[236:239]\n" \
"s_add_u32 m0, %[mA], 0x8400\n" \
"s_add_u32 s85, s62, 0x2400\n" \
"v_mfma_f32_16x16x32_bf16 a[144:147], v[152:155], v[88:91], a[144:147]\n" \
"buffer_load_dwordx4 v25, s[76:79], s85 offen lds\n" \
"v_mfma_f32_16x16x32_bf16 a[144:147], v[148:151], v[84:87], a[144:147]\n" \
"ds_read_b128 v[52:55], v28 offset:0\n" \
"ds_read_b128 v[56:59], v29 offset:0\n" \
"v_mfma_f32_16x16x32_bf16 a[148:151], v[156:159], v[84:87], a[148:151]\n" \
"s_add_u32 m0, %[mB], 0x8000\n" \
"s_add_u32 s84, s63, 0x0\n" \
"s_nop 0\n" \
"v_mfma_f32_16x16x32_bf16 a[148:151], v[160:163], v[88:91], a[148:151]\n" \
"buffer_load_dwordx4 v26, s[68:71], s84 offen lds\n" \
"v_mfma_f32_16x16x32_bf16 a[152:155], v[168:171], v[88:91], a[152:155]\n" \
"ds_read_b64_tr_b16 v[116:117], v36 offset:0\n" \
"ds_read_b64_tr_b16 v[118:119], v36 offset:512\n" \
"v_mfma_f32_16x16x32_bf16 a[152:155], v[164:167], v[84:87], a[152:155]\n" \
"ds_read_b64_tr_b16 v[120:121], v36 offset:4096\n" \
"ds_read_b64_tr_b16 v[122:123], v36 offset:4608\n" \
"v_mfma_f32_16x16x32_bf16 a[156:159], v[172:175], v[84:87], a[156:159]\n" \
"s_add_u32 m0, %[mA], 0x8800\n" \
"s_add_u32 s85, s62, 0x4800\n" \
"v_mfma_f32_16x16x32_bf16 a[156:159], v[176:179], v[88:91], a[156:159]\n" \
"buffer_load_dwordx4 v24, s[76:79], s85 offen lds\n" \
"v_mfma_f32_16x16x32_bf16 a[176:179], v[152:155], v[96:99], a[176:179]\n" \
"ds_read_b128 v[84:87], v22 offset:0\n" \
"ds_read_b128 v[88:91], v23 offset:0\n" \
"v_mfma_f32_16x16x32_bf16 a[176:179], v[148:151], v[92:95], a[176:179]\n" \
"s_add_u32 m0, %[mA], 0x8c00\n" \
"s_add_u32 s84, s62, 0x6c00\n" \
"v_mfma_f32_16x16x32_bf16 a[180:183], v[156:159], v[92:95], a[180:183]\n" \
"buffer_load_dwordx4 v25, s[76:79], s84 offen lds\n" \
"v_mfma_f32_16x16x32_bf16 a[180:183], v[160:163], v[96:99], a[180:183]\n" \
"ds_read_b128 v[60:63], v28 offset:2048\n" \
"ds_read_b128 v[64:67], v29 offset:2048\n" \
"v_mfma_f32_16x16x32_bf16 a[184:187], v[168:171], v[96:99], a[184:187]\n" \
"ds_read_b128 v[68:71], v28 offset:4096\n" \
"ds_read_b128 v[72:75], v29 offset:4096\n" \
"v_mfma_f32_16x16x32_bf16 a[184:187], v[164:167], v[92:95], a[184:187]\n" \
"s_add_u32 m0, %[mB], 0x8400\n" \
"s_add_u32 s85, s63, 0x8000\n" \
"v_mfma_f32_16x16x32_bf16 a[188:191], v[172:175], v[92:95], a[188:191]\n" \
"buffer_load_dwordx4 v26, s[68:71], s85 offen lds\n" \
"v_mfma_f32_16x16x32_bf16 a[188:191], v[176:179], v[96:99], a[188:191]\n" \
"ds_read_b128 v[76:79], v28 offset:6144\n" \
"ds_read_b128 v[80:83], v29 offset:6144\n" \
"v_mfma_f32_16x16x32_bf16 a[208:211], v[152:155], v[104:107], a[208:211]\n" \
"s_add_u32 m0, %[mA], 0x9000\n" \
"s_add_u32 s84, s62, 0x9000\n" \
"v_mfma_f32_16x16x32_bf16 a[208:211], v[148:151], v[100:103], a[208:211]\n" \
"buffer_load_dwordx4 v24, s[76:79], s84 offen lds\n" \
"v_mfma_f32_16x16x32_bf16 a[212:215], v[156:159], v[100:103], a[212:215]\n" \
"ds_read_b128 v[92:95], v48 offset:2048\n" \
"ds_read_b128 v[96:99], v49 offset:2048\n" \
"v_mfma_f32_16x16x32_bf16 a[212:215], v[160:163], v[104:107], a[212:215]\n" \
"v_writelane_b32 v11, s97, 32\n" \
"s_and_b32 s99, 1, s94\n" \
"v_mfma_f32_16x16x32_bf16 a[216:219], v[168:171], v[104:107], a[216:219]\n" \
"s_add_u32 m0, %[mA], 0x9400\n" \
"s_add_u32 s85, s62, 0xb400\n" \
"v_mfma_f32_16x16x32_bf16 a[216:219], v[164:167], v[100:103], a[216:219]\n" \
"buffer_load_dwordx4 v25, s[76:79], s85 offen lds\n" \
"v_mfma_f32_16x16x32_bf16 a[220:223], v[172:175], v[100:103], a[220:223]\n" \
"ds_read_b128 v[100:103], v48 offset:4096\n" \
"s_add_u32 s100, s67, s64\n" \
"v_mfma_f32_16x16x32_bf16 a[220:223], v[176:179], v[104:107], a[220:223]\n" \
"s_add_u32 m0, %[mB], 0x8800\n" \
"s_add_u32 s84, s63, 0x10000\n" \
"v_mfma_f32_16x16x32_bf16 a[240:243], v[152:155], v[112:115], a[240:243]\n" \
"buffer_load_dwordx4 v27, s[68:71], s84 offen lds\n" \
"v_mfma_f32_16x16x32_bf16 a[240:243], v[148:151], v[108:111], a[240:243]\n" \
"ds_read_b64_tr_b16 v[152:153], v32 offset:8192\n" \
"ds_read_b64_tr_b16 v[154:155], v32 offset:9216\n" \
"v_mfma_f32_16x16x32_bf16 a[244:247], v[156:159], v[108:111], a[244:247]\n" \
"ds_read_b64_tr_b16 v[148:149], v32 offset:0\n" \
"ds_read_b64_tr_b16 v[150:151], v32 offset:1024\n" \
"v_mfma_f32_16x16x32_bf16 a[244:247], v[160:163], v[112:115], a[244:247]\n" \
"s_add_u32 m0, %[mA], 0x9800\n" \
"s_add_u32 s85, s62, 0xd800\n" \
"v_mfma_f32_16x16x32_bf16 a[248:251], v[168:171], v[112:115], a[248:251]\n" \
"buffer_load_dwordx4 v24, s[76:79], s85 offen lds\n" \
"v_mfma_f32_16x16x32_bf16 a[248:251], v[164:167], v[108:111], a[248:251]\n" \
"ds_read_b64_tr_b16 v[156:157], v33 offset:0\n" \
"ds_read_b64_tr_b16 v[158:159], v33 offset:1024\n" \
"v_mfma_f32_16x16x32_bf16 a[252:255], v[172:175], v[108:111], a[252:255]\n" \
"ds_read_b64_tr_b16 v[160:161], v33 offset:8192\n" \
"ds_read_b64_tr_b16 v[162:163], v33 offset:9216\n" \
"v_mfma_f32_16x16x32_bf16 a[252:255], v[176:179], v[112:115], a[252:255]\n" \
"s_add_u32 m0, %[mA], 0x9c00\n" \
"s_waitcnt lgkmcnt(11)\n" \
"s_nop 0\n" \
"v_smfmac_f32_16x16x64_bf16 a[16:19], v[140:143], v[84:91], v50\n" \
"v_mfma_f32_16x16x32_bf16 a[48:51], v[128:131], v[88:91], a[48:51]\n" \
"s_add_u32 s85, s62, 0xfc00\n" \
"ds_read_b64_tr_b16 v[168:169], v34 offset:8192\n" \
"v_mfma_f32_16x16x32_bf16 a[48:51], v[124:127], v[84:87], a[48:51]\n" \
"buffer_load_dwordx4 v25, s[76:79], s85 offen lds\n" \
"v_mfma_f32_16x16x32_bf16 a[80:83], v[84:87], v[132:135], a[80:83]\n" \
"s_add_u32 m0, %[mB], 0x8c00\n" \
"s_add_u32 s84, s63, 0x18000\n" \
"v_mfma_f32_16x16x32_bf16 a[80:83], v[88:91], v[136:139], a[80:83]\n" \
"ds_read_b64_tr_b16 v[170:171], v34 offset:9216\n" \
"ds_read_b64_tr_b16 v[164:165], v34 offset:0\n" \
"v_smfmac_f32_16x16x64_bf16 a[112:115], v[140:143], v[52:59], v50\n" \
"v_mfma_f32_16x16x32_bf16 a[20:23], v[128:131], v[56:59], a[20:23]\n" \
"buffer_load_dwordx4 v27, s[68:71], s84 offen lds\n" \
"v_mfma_f32_16x16x32_bf16 a[20:23], v[124:127], v[52:55], a[20:23]\n" \
"s_add_u32 s89, s42, 0x1000\n" \
"ds_read_b64_tr_b16 v[166:167], v34 offset:1024\n" \
"v_mfma_f32_16x16x32_bf16 a[52:55], v[116:119], v[52:55], a[52:55]\n" \
"ds_read_b64_tr_b16 v[172:173], v35 offset:0\n" \
"ds_read_b64_tr_b16 v[174:175], v35 offset:1024\n" \
"v_mfma_f32_16x16x32_bf16 a[52:55], v[120:123], v[56:59], a[52:55]\n" \
"buffer_store_dwordx4 v[236:239], v20, s[80:83], s89 offen offset:1024 sc1\n" \
"v_mfma_f32_16x16x32_bf16 a[84:87], v[56:59], v[136:139], a[84:87]\n" \
"ds_read_b64_tr_b16 v[176:177], v35 offset:8192\n" \
"ds_read_b64_tr_b16 v[178:179], v35 offset:9216\n" \
"v_mfma_f32_16x16x32_bf16 a[84:87], v[52:55], v[132:135], a[84:87]\n" \
"ds_read_b128 v[84:87], v48 offset:0\n" \
"ds_read_b128 v[88:91], v49 offset:0\n" \
"v_smfmac_f32_16x16x64_bf16 a[116:119], v[140:143], v[60:67], v50\n" \
"v_mfma_f32_16x16x32_bf16 a[24:27], v[124:127], v[60:63], a[24:27]\n" \
"ds_read_b128 v[104:107], v49 offset:4096\n" \
"ds_read_b128 v[108:111], v48 offset:6144\n" \
"v_mfma_f32_16x16x32_bf16 a[24:27], v[128:131], v[64:67], a[24:27]\n" \
"s_add_u32 s88, s42, 0x2000\n" \
"ds_read_b128 v[112:115], v49 offset:6144\n" \
"v_mfma_f32_16x16x32_bf16 a[56:59], v[120:123], v[64:67], a[56:59]\n" \
"buffer_store_dwordx4 v[240:243], v20, s[80:83], s88 offen offset:1024 sc1\n" \
"v_mfma_f32_16x16x32_bf16 a[56:59], v[116:119], v[60:63], a[56:59]\n" \
"s_add_u32 s62, s62, 0x80\n" \
"v_writelane_b32 v11, s100, 33\n" \
"v_mfma_f32_16x16x32_bf16 a[88:91], v[60:63], v[132:135], a[88:91]\n" \
"s_cmp_lt_u32 s62, 0x480\n" \
"s_cselect_b32 s62, s62, 0\n" \
"v_mfma_f32_16x16x32_bf16 a[88:91], v[64:67], v[136:139], a[88:91]\n" \
"s_lshl_b32 s72, s62, 6\n" \
"s_add_u32 s72, s72, s37\n" \
"v_smfmac_f32_16x16x64_bf16 a[120:123], v[140:143], v[68:75], v50\n" \
"v_mfma_f32_16x16x32_bf16 a[28:31], v[128:131], v[72:75], a[28:31]\n" \
"s_lshl_b32 s63, s62, 12\n" \
"s_add_u32 s98, 0x3004000, s66\n" \
"v_mfma_f32_16x16x32_bf16 a[28:31], v[124:127], v[68:71], a[28:31]\n" \
"s_cmp_lg_u32 s99, 0\n" \
"s_cselect_b32 s97, s67, s96\n" \
"v_mfma_f32_16x16x32_bf16 a[60:63], v[116:119], v[68:71], a[60:63]\n" \
"s_add_u32 s89, s42, 0x3000\n" \
"s_lshr_b32 s95, s94, 1\n" \
"v_mfma_f32_16x16x32_bf16 a[60:63], v[120:123], v[72:75], a[60:63]\n" \
"buffer_store_dwordx4 v[244:247], v20, s[80:83], s89 offen offset:1024 sc1\n" \
"v_mfma_f32_16x16x32_bf16 a[92:95], v[72:75], v[136:139], a[92:95]\n" \
"s_add_u32 s64, 0x4000, s97\n" \
"v_writelane_b32 v11, s98, 34\n" \
"v_mfma_f32_16x16x32_bf16 a[92:95], v[68:71], v[132:135], a[92:95]\n" \
"s_add_u32 s101, 0x3004000, s65\n" \
"s_and_b32 s96, 2, %[w]\n" \
"v_smfmac_f32_16x16x64_bf16 a[124:127], v[140:143], v[76:83], v50\n" \
"v_mfma_f32_16x16x32_bf16 a[0:3], v[124:127], v[76:79], a[0:3]\n" \
"s_cmp_eq_u32 %[w], s95\n" \
"s_cselect_b32 s100, s64, 0x40000000\n" \
"v_mfma_f32_16x16x32_bf16 a[0:3], v[128:131], v[80:83], a[0:3]\n" \
"v_writelane_b32 v11, s101, 35\n" \
"s_lshl_b32 s97, s96, 1\n" \
"v_mfma_f32_16x16x32_bf16 a[4:7], v[120:123], v[80:83], a[4:7]\n" \
"s_and_b32 s98, 1, %[w]\n" \
"v_writelane_b32 v11, s100, 36\n" \
"v_mfma_f32_16x16x32_bf16 a[4:7], v[116:119], v[76:79], a[4:7]\n" \
"s_lshr_b32 s94, %[b], 5\n" \
"s_add_u32 s99, s97, s98\n" \
"v_mfma_f32_16x16x32_bf16 a[32:35], v[76:79], v[132:135], a[32:35]\n" \
"s_lshl_b32 s95, s94, 7\n" \
"s_lshl_b32 s100, s99, 4\n" \
"v_mfma_f32_16x16x32_bf16 a[32:35], v[80:83], v[136:139], a[32:35]\n" \
"s_add_u32 s101, s95, s100\n" \
"s_waitcnt lgkmcnt(0)\n" \
"v_mfma_f32_16x16x32_bf16 a[128:131], v[152:155], v[56:59], a[128:131]\n" \
"s_mul_i32 s64, 0x480, s101\n" \
"s_lshr_b32 s65, %[b], 3\n" \
"v_mfma_f32_16x16x32_bf16 a[128:131], v[148:151], v[52:55], a[128:131]\n" \
"s_sub_u32 s99, 0x780, s95\n" \
"s_and_b32 s66, 3, s65\n" \
"v_mfma_f32_16x16x32_bf16 a[132:135], v[156:159], v[52:55], a[132:135]\n" \
"s_add_u32 s101, s99, s100\n" \
"s_mul_i32 s67, 0x120, s66\n" \
"v_mfma_f32_16x16x32_bf16 a[132:135], v[160:163], v[56:59], a[132:135]\n" \
"s_and_b32 s96, 7, %[b]\n" \
"s_mul_i32 s65, 0x480, s101\n" \
"v_mfma_f32_16x16x32_bf16 a[136:139], v[168:171], v[56:59], a[136:139]\n" \
"s_add_u32 s94, s64, s67\n" \
"s_lshl_b32 s97, s96, 5\n" \
"v_mfma_f32_16x16x32_bf16 a[136:139], v[164:167], v[52:55], a[136:139]\n" \
"s_add_u32 s66, s65, s67\n" \
"s_and_b32 s95, 1, s96\n" \
"v_mfma_f32_16x16x32_bf16 a[140:143], v[172:175], v[52:55], a[140:143]\n" \
"s_add_u32 s98, s94, s97\n" \
"v_writelane_b32 v11, s98, 39\n" \
"v_mfma_f32_16x16x32_bf16 a[140:143], v[176:179], v[56:59], a[140:143]\n" \
"s_cmp_lg_u32 s95, 0\n" \
"s_cselect_b32 s99, s66, s94\n" \
"v_mfma_f32_16x16x32_bf16 a[160:163], v[152:155], v[64:67], a[160:163]\n" \
"s_lshr_b32 s98, s96, 1\n" \
"s_add_u32 s100, 0x100, s99\n" \
"v_mfma_f32_16x16x32_bf16 a[160:163], v[148:151], v[60:63], a[160:163]\n" \
"s_add_u32 s64, s66, s97\n" \
"s_mov_b32 s94, s67\n" \
"v_mfma_f32_16x16x32_bf16 a[164:167], v[156:159], v[60:63], a[164:167]\n" \
"s_cmp_eq_u32 %[w], s98\n" \
"s_cselect_b32 s101, s100, 0x40000000\n" \
"v_mfma_f32_16x16x32_bf16 a[164:167], v[160:163], v[64:67], a[164:167]\n" \
"v_writelane_b32 v11, s64, 40\n" \
"v_writelane_b32 v11, s101, 41\n" \
"v_mfma_f32_16x16x32_bf16 a[168:171], v[168:171], v[64:67], a[168:171]\n" \
"s_add_u32 s96, s16, s94\n" \
"s_addc_u32 s97, s17, 0\n" \
"v_mfma_f32_16x16x32_bf16 a[168:171], v[164:167], v[60:63], a[168:171]\n" \
"s_add_u32 s98, s18, s94\n" \
"s_addc_u32 s99, s19, 0\n" \
"v_mfma_f32_16x16x32_bf16 a[172:175], v[172:175], v[60:63], a[172:175]\n" \
"s_and_b32 s97, s97, 0xffff\n" \
"v_writelane_b32 v11, s96, 7\n" \
"v_mfma_f32_16x16x32_bf16 a[172:175], v[176:179], v[64:67], a[172:175]\n" \
"v_writelane_b32 v11, s97, 8\n" \
"s_and_b32 s99, s99, 0xffff\n" \
"v_mfma_f32_16x16x32_bf16 a[192:195], v[152:155], v[72:75], a[192:195]\n" \
"v_writelane_b32 v11, s98, 9\n" \
"v_writelane_b32 v11, s99, 10\n" \
"v_mfma_f32_16x16x32_bf16 a[192:195], v[148:151], v[68:71], a[192:195]\n" \
"s_cmp_lt_u32 %[w], 3\n" \
"s_cselect_b64 s[96:97], s[96:97], s[98:99]\n" \
"s_waitcnt vmcnt(3)\n" \
"s_barrier\n" \
"v_mfma_f32_16x16x32_bf16 a[196:199], v[156:159], v[68:71], a[196:199]\n" \
"s_add_u32 m0, s40, 0x0\n" \
"s_add_u32 s85, s72, 0x0\n" \
"v_mfma_f32_16x16x32_bf16 a[196:199], v[160:163], v[72:75], a[196:199]\n" \
"buffer_load_dwordx4 v20, s[80:83], s85 offen sc1 lds\n" \
"v_mfma_f32_16x16x32_bf16 a[200:203], v[168:171], v[72:75], a[200:203]\n" \
"s_add_u32 m0, s40, 0x1000\n" \
"s_add_u32 s84, s72, 0x1000\n" \
"v_mfma_f32_16x16x32_bf16 a[200:203], v[164:167], v[68:71], a[200:203]\n" \
"ds_read_b64_tr_b16 v[140:141], v39 offset:32768\n" \
"ds_read_b64_tr_b16 v[142:143], v39 offset:33728\n" \
"v_mfma_f32_16x16x32_bf16 a[204:207], v[172:175], v[68:71], a[204:207]\n" \
"buffer_load_dwordx4 v20, s[80:83], s84 offen sc1 lds\n" \
"v_mfma_f32_16x16x32_bf16 a[204:207], v[176:179], v[72:75], a[204:207]\n" \
"ds_read_b64_tr_b16 v[128:129], v37 offset:36864\n" \
"ds_read_b64_tr_b16 v[130:131], v37 offset:37376\n" \
"v_mfma_f32_16x16x32_bf16 a[224:227], v[152:155], v[80:83], a[224:227]\n" \
"s_add_u32 m0, s40, 0x3000\n" \
"s_add_u32 s85, s62, 0x0\n" \
"v_mfma_f32_16x16x32_bf16 a[224:227], v[148:151], v[76:79], a[224:227]\n" \
"buffer_load_dwordx4 v21, s[76:79], s85 offen lds\n" \
"v_mfma_f32_16x16x32_bf16 a[228:231], v[156:159], v[76:79], a[228:231]\n" \
"ds_read_b64_tr_b16 v[124:125], v37 offset:32768\n" \
"ds_read_b64_tr_b16 v[126:127], v37 offset:33280\n" \
"v_mfma_f32_16x16x32_bf16 a[228:231], v[160:163], v[80:83], a[228:231]\n" \
"s_add_u32 m0, %[mA], 0x0\n" \
"s_add_u32 s84, s62, 0x0\n" \
"s_nop 0\n" \
"v_mfma_f32_16x16x32_bf16 a[232:235], v[168:171], v[80:83], a[232:235]\n" \
"buffer_load_dwordx4 v24, s[76:79], s84 offen lds\n" \
"v_mfma_f32_16x16x32_bf16 a[232:235], v[164:167], v[76:79], a[232:235]\n" \
"ds_read_b64_tr_b16 v[132:133], v38 offset:32768\n" \
"ds_read_b64_tr_b16 v[134:135], v38 offset:33280\n" \
"v_mfma_f32_16x16x32_bf16 a[236:239], v[172:175], v[76:79], a[236:239]\n" \
"ds_read_b64_tr_b16 v[136:137], v38 offset:36864\n" \
"ds_read_b64_tr_b16 v[138:139], v38 offset:37376\n" \
"v_mfma_f32_16x16x32_bf16 a[236:239], v[176:179], v[80:83], a[236:239]\n" \
"s_add_u32 m0, %[mA], 0x400\n" \
"s_add_u32 s85, s62, 0x2400\n" \
"v_mfma_f32_16x16x32_bf16 a[144:147], v[152:155], v[88:91], a[144:147]\n" \
"buffer_load_dwordx4 v25, s[76:79], s85 offen lds\n" \
"v_mfma_f32_16x16x32_bf16 a[144:147], v[148:151], v[84:87], a[144:147]\n" \
"ds_read_b128 v[52:55], v28 offset:32768\n" \
"ds_read_b128 v[56:59], v29 offset:32768\n" \
"v_mfma_f32_16x16x32_bf16 a[148:151], v[156:159], v[84:87], a[148:151]\n" \
"s_add_u32 m0, %[mB], 0x0\n" \
"s_add_u32 s84, s63, 0x0\n" \
"v_mfma_f32_16x16x32_bf16 a[148:151], v[160:163], v[88:91], a[148:151]\n" \
"buffer_load_dwordx4 v26, s[68:71], s84 offen lds\n" \
"v_mfma_f32_16x16x32_bf16 a[152:155], v[168:171], v[88:91], a[152:155]\n" \
"ds_read_b64_tr_b16 v[116:117], v36 offset:32768\n" \
"ds_read_b64_tr_b16 v[118:119], v36 offset:33280\n" \
"v_mfma_f32_16x16x32_bf16 a[152:155], v[164:167], v[84:87], a[152:155]\n" \
"ds_read_b64_tr_b16 v[120:121], v36 offset:36864\n" \
"ds_read_b64_tr_b16 v[122:123], v36 offset:37376\n" \
"v_mfma_f32_16x16x32_bf16 a[156:159], v[172:175], v[84:87], a[156:159]\n" \
"s_add_u32 m0, %[mA], 0x800\n" \
"s_add_u32 s85, s62, 0x4800\n" \
"v_mfma_f32_16x16x32_bf16 a[156:159], v[176:179], v[88:91], a[156:159]\n" \
"buffer_load_dwordx4 v24, s[76:79], s85 offen lds\n" \
"v_mfma_f32_16x16x32_bf16 a[176:179], v[152:155], v[96:99], a[176:179]\n" \
"ds_read_b128 v[84:87], v22 offset:32768\n" \
"ds_read_b128 v[88:91], v23 offset:32768\n" \
"v_mfma_f32_16x16x32_bf16 a[176:179], v[148:151], v[92:95], a[176:179]\n" \
"s_add_u32 m0, %[mA], 0xc00\n" \
"s_add_u32 s84, s62, 0x6c00\n" \
"v_mfma_f32_16x16x32_bf16 a[180:183], v[156:159], v[92:95], a[180:183]\n" \
"buffer_load_dwordx4 v25, s[76:79], s84 offen lds\n" \
"v_mfma_f32_16x16x32_bf16 a[180:183], v[160:163], v[96:99], a[180:183]\n" \
"L_pf7:\n" \
"s_cbranch_execz L_pf11\n" \
"s_nop 0\n" \
"ds_read_b128 v[60:63], v28 offset:34816\n" \
"ds_read_b128 v[64:67], v29 offset:34816\n" \
"v_mfma_f32_16x16x32_bf16 a[184:187], v[168:171], v[96:99], a[184:187]\n" \
"ds_read_b128 v[68:71], v28 offset:36864\n" \
"ds_read_b128 v[72:75], v29 offset:36864\n" \
"v_mfma_f32_16x16x32_bf16 a[184:187], v[164:167], v[92:95], a[184:187]\n" \
"s_add_u32 m0, %[mB], 0x400\n" \
"s_add_u32 s85, s63, 0x8000\n" \
"v_mfma_f32_16x16x32_bf16 a[188:191], v[172:175], v[92:95], a[188:191]\n" \
"buffer_load_dwordx4 v26, s[68:71], s85 offen lds\n" \
"v_mfma_f32_16x16x32_bf16 a[188:191], v[176:179], v[96:99], a[188:191]\n" \
"ds_read_b128 v[76:79], v28 offset:38912\n" \
"ds_read_b128 v[80:83], v29 offset:38912\n" \
"v_mfma_f32_16x16x32_bf16 a[208:211], v[152:155], v[104:107], a[208:211]\n" \
"s_add_u32 m0, %[mA], 0x1000\n" \
"s_add_u32 s84, s62, 0x9000\n" \
"v_mfma_f32_16x16x32_bf16 a[208:211], v[148:151], v[100:103], a[208:211]\n" \
"buffer_load_dwordx4 v24, s[76:79], s84 offen lds\n" \
"v_mfma_f32_16x16x32_bf16 a[212:215], v[156:159], v[100:103], a[212:215]\n" \
"ds_read_b128 v[92:95], v48 offset:34816\n" \
"ds_read_b128 v[96:99], v49 offset:34816\n" \
"v_mfma_f32_16x16x32_bf16 a[212:215], v[160:163], v[104:107], a[212:215]\n" \
"v_writelane_b32 v11, s96, 13\n" \
"v_writelane_b32 v11, s97, 14\n" \
"v_mfma_f32_16x16x32_bf16 a[216:219], v[168:171], v[104:107], a[216:219]\n" \
"s_add_u32 m0, %[mA], 0x1400\n" \
"s_add_u32 s85, s62, 0xb400\n" \
"v_mfma_f32_16x16x32_bf16 a[216:219], v[164:167], v[100:103], a[216:219]\n" \
"buffer_load_dwordx4 v25, s[76:79], s85 offen lds\n" \
"v_mfma_f32_16x16x32_bf16 a[220:223], v[172:175], v[100:103], a[220:223]\n" \
"ds_read_b128 v[100:103], v48 offset:36864\n" \
"v_writelane_b32 v11, s20, 11\n" \
"v_mfma_f32_16x16x32_bf16 a[220:223], v[176:179], v[104:107], a[220:223]\n" \
"s_add_u32 m0, %[mB], 0x800\n" \
"s_add_u32 s84, s63, 0x10000\n" \
"v_mfma_f32_16x16x32_bf16 a[240:243], v[152:155], v[112:115], a[240:243]\n" \
"buffer_load_dwordx4 v27, s[68:71], s84 offen lds\n" \
"v_mfma_f32_16x16x32_bf16 a[240:243], v[148:151], v[108:111], a[240:243]\n" \
"ds_read_b64_tr_b16 v[152:153], v32 offset:40960\n" \
"ds_read_b64_tr_b16 v[154:155], v32 offset:41984\n" \
"v_mfma_f32_16x16x32_bf16 a[244:247], v[156:159], v[108:111], a[244:247]\n" \
"ds_read_b64_tr_b16 v[148:149], v32 offset:32768\n" \
"ds_read_b64_tr_b16 v[150:151], v32 offset:33792\n" \
"v_mfma_f32_16x16x32_bf16 a[244:247], v[160:163], v[112:115], a[244:247]\n" \
"s_add_u32 m0, %[mA], 0x1800\n" \
"s_add_u32 s85, s62, 0xd800\n" \
"v_mfma_f32_16x16x32_bf16 a[248:251], v[168:171], v[112:115], a[248:251]\n" \
"buffer_load_dwordx4 v24, s[76:79], s85 offen lds\n" \
"v_mfma_f32_16x16x32_bf16 a[248:251], v[164:167], v[108:111], a[248:251]\n" \
"ds_read_b64_tr_b16 v[156:157], v33 offset:32768\n" \
"ds_read_b64_tr_b16 v[158:159], v33 offset:33792\n" \
"v_mfma_f32_16x16x32_bf16 a[252:255], v[172:175], v[108:111], a[252:255]\n" \
"ds_read_b64_tr_b16 v[160:161], v33 offset:40960\n" \
"ds_read_b64_tr_b16 v[162:163], v33 offset:41984\n" \
"v_mfma_f32_16x16x32_bf16 a[252:255], v[176:179], v[112:115], a[252:255]\n" \
"s_add_u32 m0, %[mA], 0x1c00\n" \
"s_waitcnt lgkmcnt(11)\n" \
"s_nop 0\n" \
"v_smfmac_f32_16x16x64_bf16 a[16:19], v[140:143], v[84:91], v50\n" \
"v_mfma_f32_16x16x32_bf16 a[48:51], v[128:131], v[88:91], a[48:51]\n" \
"s_add_u32 s85, s62, 0xfc00\n" \
"ds_read_b64_tr_b16 v[168:169], v34 offset:40960\n" \
"v_mfma_f32_16x16x32_bf16 a[48:51], v[124:127], v[84:87], a[48:51]\n" \
"buffer_load_dwordx4 v25, s[76:79], s85 offen lds\n" \
"v_mfma_f32_16x16x32_bf16 a[80:83], v[84:87], v[132:135], a[80:83]\n" \
"s_add_u32 m0, %[mB], 0xc00\n" \
"s_add_u32 s84, s63, 0x18000\n" \
"v_mfma_f32_16x16x32_bf16 a[80:83], v[88:91], v[136:139], a[80:83]\n" \
"ds_read_b64_tr_b16 v[170:171], v34 offset:41984\n" \
"ds_read_b64_tr_b16 v[164:165], v34 offset:32768\n" \
"v_smfmac_f32_16x16x64_bf16 a[112:115], v[140:143], v[52:59], v50\n" \
"v_mfma_f32_16x16x32_bf16 a[20:23], v[128:131], v[56:59], a[20:23]\n" \
"buffer_load_dwordx4 v27, s[68:71], s84 offen lds\n" \
"v_mfma_f32_16x16x32_bf16 a[20:23], v[124:127], v[52:55], a[20:23]\n" \
"s_add_u32 s88, s43, 0x0\n" \
"s_nop 0\n" \
"ds_read_b64_tr_b16 v[166:167], v34 offset:33792\n" \
"v_mfma_f32_16x16x32_bf16 a[52:55], v[116:119], v[52:55], a[52:55]\n" \
"ds_read_b64_tr_b16 v[172:173], v35 offset:32768\n" \
"ds_read_b64_tr_b16 v[174:175], v35 offset:33792\n" \
"v_mfma_f32_16x16x32_bf16 a[52:55], v[120:123], v[56:59], a[52:55]\n" \
"buffer_store_dwordx4 v[248:251], v20, s[80:83], s88 offen offset:0 sc1\n" \
"v_mfma_f32_16x16x32_bf16 a[84:87], v[56:59], v[136:139], a[84:87]\n" \
"ds_read_b64_tr_b16 v[176:177], v35 offset:40960\n" \
"ds_read_b64_tr_b16 v[178:179], v35 offset:41984\n" \
"v_mfma_f32_16x16x32_bf16 a[84:87], v[52:55], v[132:135], a[84:87]\n" \
"ds_read_b128 v[84:87], v48 offset:32768\n" \
"ds_read_b128 v[88:91], v49 offset:32768\n" \
"v_smfmac_f32_16x16x64_bf16 a[116:119], v[140:143], v[60:67], v50\n" \
"v_mfma_f32_16x16x32_bf16 a[24:27], v[124:127], v[60:63], a[24:27]\n" \
"ds_read_b128 v[104:107], v49 offset:36864\n" \
"ds_read_b128 v[108:111], v48 offset:38912\n" \
"v_mfma_f32_16x16x32_bf16 a[24:27], v[128:131], v[64:67], a[24:27]\n" \
"s_add_u32 s89, s45, 0x1000\n" \
"ds_read_b128 v[112:115], v49 offset:38912\n" \
"v_mfma_f32_16x16x32_bf16 a[56:59], v[120:123], v[64:67], a[56:59]\n" \
"buffer_store_dwordx4 v[184:187], v20, s[80:83], s89 offen offset:2048 sc1\n" \
"v_mfma_f32_16x16x32_bf16 a[56:59], v[116:119], v[60:63], a[56:59]\n" \
"s_add_u32 s62, s62, 0x80\n" \
"s_and_b32 s95, s21, 0xffff\n" \
"v_mfma_f32_16x16x32_bf16 a[88:91], v[60:63], v[132:135], a[88:91]\n" \
"s_cmp_lt_u32 s62, 0x480\n" \
"s_cselect_b32 s62, s62, 0\n" \
"v_mfma_f32_16x16x32_bf16 a[88:91], v[64:67], v[136:139], a[88:91]\n" \
"s_lshl_b32 s72, s62, 6\n" \
"s_add_u32 s72, s72, s37\n" \
"v_smfmac_f32_16x16x64_bf16 a[120:123], v[140:143], v[68:75], v50\n" \
"v_mfma_f32_16x16x32_bf16 a[28:31], v[128:131], v[72:75], a[28:31]\n" \
"s_lshl_b32 s63, s62, 12\n" \
"v_writelane_b32 v11, s95, 12\n" \
"v_mfma_f32_16x16x32_bf16 a[28:31], v[124:127], v[68:71], a[28:31]\n" \
"s_and_b32 s95, 1, %[w]\n" \
"s_lshr_b32 s96, %[w], 1\n" \
"v_mfma_f32_16x16x32_bf16 a[60:63], v[116:119], v[68:71], a[60:63]\n" \
"s_add_u32 s88, s45, 0x1000\n" \
"s_lshl_b32 s94, %[w], 11\n" \
"v_mfma_f32_16x16x32_bf16 a[60:63], v[120:123], v[72:75], a[60:63]\n" \
"buffer_store_dwordx4 v[252:255], v20, s[80:83], s88 offen offset:3072 sc1\n" \
"v_mfma_f32_16x16x32_bf16 a[92:95], v[72:75], v[136:139], a[92:95]\n" \
"s_lshl_b32 s15, s95, 12\n" \
"s_lshl_b32 s16, s96, 14\n" \
"v_mfma_f32_16x16x32_bf16 a[92:95], v[68:71], v[132:135], a[92:95]\n" \
"s_add_u32 s12, 0xc000, s94\n" \
"s_add_u32 s97, s16, s15\n" \
"v_smfmac_f32_16x16x64_bf16 a[124:127], v[140:143], v[76:83], v50\n" \
"v_mfma_f32_16x16x32_bf16 a[0:3], v[124:127], v[76:79], a[0:3]\n" \
"s_and_b32 s65, 7, %[b]\n" \
"s_lshr_b32 s67, %[b], 3\n" \
"v_mfma_f32_16x16x32_bf16 a[0:3], v[128:131], v[80:83], a[0:3]\n" \
"s_xor_b32 s13, 0x8000, s97\n" \
"s_add_u32 s20, 0x14000, s97\n" \
"v_mfma_f32_16x16x32_bf16 a[4:7], v[120:123], v[80:83], a[4:7]\n" \
"s_mul_i32 s98, 0xe000, s96\n" \
"s_lshr_b32 s100, %[b], 5\n" \
"v_mfma_f32_16x16x32_bf16 a[4:7], v[116:119], v[76:79], a[4:7]\n" \
"s_lshl_b32 s66, s65, 2\n" \
"s_and_b32 s94, 3, s67\n" \
"v_mfma_f32_16x16x32_bf16 a[32:35], v[76:79], v[132:135], a[32:35]\n" \
"s_add_u32 s99, s98, s15\n" \
"s_lshl_b32 s18, s100, 1\n" \
"v_mfma_f32_16x16x32_bf16 a[32:35], v[80:83], v[136:139], a[32:35]\n" \
"s_add_u32 s97, s66, s94\n" \
"s_waitcnt lgkmcnt(0)\n" \
"v_mfma_f32_16x16x32_bf16 a[128:131], v[152:155], v[56:59], a[128:131]\n" \
"s_add_u32 s21, 0x10000, s99\n" \
"s_add_u32 s101, s18, s96\n" \
"v_mfma_f32_16x16x32_bf16 a[128:131], v[148:151], v[52:55], a[128:131]\n" \
"s_lshl_b32 s98, s97, 8\n" \
"s_lshl_b32 s14, s101, 15\n" \
"v_mfma_f32_16x16x32_bf16 a[132:135], v[156:159], v[52:55], a[132:135]\n" \
"s_mov_b32 s64, 0x4000\n" \
"s_add_u32 s99, 0x5be8020, s98\n" \
"v_mfma_f32_16x16x32_bf16 a[132:135], v[160:163], v[56:59], a[132:135]\n" \
"s_lshl_b32 s96, s100, 2\n" \
"s_add_u32 s101, s99, s96\n" \
"v_mfma_f32_16x16x32_bf16 a[136:139], v[168:171], v[56:59], a[136:139]\n" \
"s_cmp_lg_u32 s100, 0\n" \
"s_cselect_b32 s17, s64, 0x8000\n" \
"v_mfma_f32_16x16x32_bf16 a[136:139], v[164:167], v[52:55], a[136:139]\n" \
"s_cmp_eq_u32 %[w], 3\n" \
"s_cselect_b32 s22, s101, 0x40000000\n" \
"v_mfma_f32_16x16x32_bf16 a[140:143], v[172:175], v[52:55], a[140:143]\n" \
"s_and_b32 s65, 2, %[w]\n" \
"s_lshl_b32 s64, s100, 3\n" \
"v_mfma_f32_16x16x32_bf16 a[140:143], v[176:179], v[56:59], a[140:143]\n" \
"s_lshl_b32 s67, s65, 1\n" \
"s_add_u32 s66, s67, s95\n" \
"v_mfma_f32_16x16x32_bf16 a[160:163], v[152:155], v[64:67], a[160:163]\n" \
"s_sub_u32 s101, 0x78, s64\n" \
"s_add_u32 s65, s101, s66\n" \
"v_mfma_f32_16x16x32_bf16 a[160:163], v[148:151], v[60:63], a[160:163]\n" \
"s_add_u32 s94, s64, s66\n" \
"s_mul_i32 s67, 0x24800, s65\n" \
"v_mfma_f32_16x16x32_bf16 a[164:167], v[156:159], v[60:63], a[164:167]\n" \
"s_lshl_b32 s99, s97, 12\n" \
"s_mov_b32 s64, 0xaaaaaaaa\n" \
"v_mfma_f32_16x16x32_bf16 a[164:167], v[160:163], v[64:67], a[164:167]\n" \
"s_mov_b32 s65, 0xaaaaaaaa\n" \
"s_add_u32 s95, s67, s99\n" \
"v_mfma_f32_16x16x32_bf16 a[168:171], v[168:171], v[64:67], a[168:171]\n" \
"s_mov_b32 s66, 0xfff0ffff\n" \
"s_mov_b32 s67, 0xf000ff00\n" \
"v_mfma_f32_16x16x32_bf16 a[168:171], v[164:167], v[60:63], a[168:171]\n" \
"s_mul_i32 s98, 0x24800, s94\n" \
"s_add_u32 s96, s98, s99\n" \
"v_mfma_f32_16x16x32_bf16 a[172:175], v[172:175], v[60:63], a[172:175]\n" \
"s_lshl_b32 s98, s97, 20\n" \
"s_lshl_b32 s94, s100, 16\n" \
"v_mfma_f32_16x16x32_bf16 a[172:175], v[176:179], v[64:67], a[172:175]\n" \
"s_add_u32 s8, 0x1000000, s98\n" \
"s_sub_u32 s19, 14, s18\n" \
"v_mfma_f32_16x16x32_bf16 a[192:195], v[152:155], v[72:75], a[192:195]\n" \
"s_add_u32 s60, 0x3004800, s96\n" \
"s_add_u32 s61, 0x3004800, s95\n" \
"v_mfma_f32_16x16x32_bf16 a[192:195], v[148:151], v[68:71], a[192:195]\n" \
"s_sub_u32 s10, 0x100000, s94\n" \
"s_mov_b32 s11, 0x20000\n" \
"s_waitcnt vmcnt(3)\n" \
"s_barrier\n" \
"s_mov_b64 exec, 1\n" \
"buffer_store_dword v45, off, s[80:83], s38 offset:0 sc1\n" \
"s_mov_b64 exec, -1\n" \
"v_mfma_f32_16x16x32_bf16 a[196:199], v[156:159], v[68:71], a[196:199]\n" \
"s_add_u32 m0, s40, 0x8000\n" \
"s_add_u32 s85, s72, 0x0\n" \
"v_mfma_f32_16x16x32_bf16 a[196:199], v[160:163], v[72:75], a[196:199]\n" \
"ds_read_b64_tr_b16 v[140:141], v39 offset:0\n" \
"ds_read_b64_tr_b16 v[142:143], v39 offset:960\n" \
"v_mfma_f32_16x16x32_bf16 a[200:203], v[168:171], v[72:75], a[200:203]\n" \
"buffer_load_dwordx4 v20, s[80:83], s85 offen sc1 lds\n" \
"v_mfma_f32_16x16x32_bf16 a[200:203], v[164:167], v[68:71], a[200:203]\n" \
"s_add_u32 m0, s40, 0x9000\n" \
"s_add_u32 s84, s72, 0x1000\n" \
"v_mfma_f32_16x16x32_bf16 a[204:207], v[172:175], v[68:71], a[204:207]\n" \
"ds_read_b64_tr_b16 v[128:129], v37 offset:4096\n" \
"ds_read_b64_tr_b16 v[130:131], v37 offset:4608\n" \
"v_mfma_f32_16x16x32_bf16 a[204:207], v[176:179], v[72:75], a[204:207]\n" \
"buffer_load_dwordx4 v20, s[80:83], s84 offen sc1 lds\n" \
"v_mfma_f32_16x16x32_bf16 a[224:227], v[152:155], v[80:83], a[224:227]\n" \
"s_add_u32 m0, s40, 0xb000\n" \
"s_add_u32 s85, s62, 0x0\n" \
"v_mfma_f32_16x16x32_bf16 a[224:227], v[148:151], v[76:79], a[224:227]\n" \
"ds_read_b64_tr_b16 v[124:125], v37 offset:0\n" \
"ds_read_b64_tr_b16 v[126:127], v37 offset:512\n" \
"v_mfma_f32_16x16x32_bf16 a[228:231], v[156:159], v[76:79], a[228:231]\n" \
"buffer_load_dwordx4 v21, s[76:79], s85 offen lds\n" \
"v_mfma_f32_16x16x32_bf16 a[228:231], v[160:163], v[80:83], a[228:231]\n" \
"s_add_u32 m0, %[mA], 0x8000\n" \
"s_add_u32 s84, s62, 0x0\n" \
"v_mfma_f32_16x16x32_bf16 a[232:235], v[168:171], v[80:83], a[232:235]\n" \
"ds_read_b64_tr_b16 v[132:133], v38 offset:0\n" \
"ds_read_b64_tr_b16 v[134:135], v38 offset:512\n" \
"v_mfma_f32_16x16x32_bf16 a[232:235], v[164:167], v[76:79], a[232:235]\n" \
"buffer_load_dwordx4 v24, s[76:79], s84 offen lds\n" \
"v_mfma_f32_16x16x32_bf16 a[236:239], v[172:175], v[76:79], a[236:239]\n" \
"ds_read_b64_tr_b16 v[136:137], v38 offset:4096\n" \
"ds_read_b64_tr_b16 v[138:139], v38 offset:4608\n" \
"v_mfma_f32_16x16x32_bf16 a[236:239], v[176:179], v[80:83], a[236:239]\n" \
"s_add_u32 m0, %[mA], 0x8400\n" \
"s_add_u32 s85, s62, 0x2400\n" \
"v_mfma_f32_16x16x32_bf16 a[144:147], v[152:155], v[88:91], a[144:147]\n" \
"buffer_load_dwordx4 v25, s[76:79], s85 offen lds\n" \
"v_mfma_f32_16x16x32_bf16 a[144:147], v[148:151], v[84:87], a[144:147]\n" \
"ds_read_b128 v[52:55], v28 offset:0\n" \
"ds_read_b128 v[56:59], v29 offset:0\n" \
"v_mfma_f32_16x16x32_bf16 a[148:151], v[156:159], v[84:87], a[148:151]\n" \
"s_add_u32 m0, %[mB], 0x8000\n" \
"s_add_u32 s84, s63, 0x0\n" \
"s_nop 0\n" \
"v_mfma_f32_16x16x32_bf16 a[148:151], v[160:163], v[88:91], a[148:151]\n" \
"buffer_load_dwordx4 v26, s[68:71], s84 offen lds\n" \
"v_mfma_f32_16x16x32_bf16 a[152:155], v[168:171], v[88:91], a[152:155]\n" \
"ds_read_b64_tr_b16 v[116:117], v36 offset:0\n" \
"ds_read_b64_tr_b16 v[118:119], v36 offset:512\n" \
"v_mfma_f32_16x16x32_bf16 a[152:155], v[164:167], v[84:87], a[152:155]\n" \
"ds_read_b64_tr_b16 v[120:121], v36 offset:4096\n" \
"ds_read_b64_tr_b16 v[122:123], v36 offset:4608\n" \
"v_mfma_f32_16x16x32_bf16 a[156:159], v[172:175], v[84:87], a[156:159]\n" \
"s_add_u32 m0, %[mA], 0x8800\n" \
"s_add_u32 s85, s62, 0x4800\n" \
"v_mfma_f32_16x16x32_bf16 a[156:159], v[176:179], v[88:91], a[156:159]\n" \
"buffer_load_dwordx4 v24, s[76:79], s85 offen lds\n" \
"v_mfma_f32_16x16x32_bf16 a[176:179], v[152:155], v[96:99], a[176:179]\n" \
"ds_read_b128 v[84:87], v22 offset:0\n" \
"ds_read_b128 v[88:91], v23 offset:0\n" \
"v_mfma_f32_16x16x32_bf16 a[176:179], v[148:151], v[92:95], a[176:179]\n" \
"s_add_u32 m0, %[mA], 0x8c00\n" \
"s_add_u32 s84, s62, 0x6c00\n" \
"v_mfma_f32_16x16x32_bf16 a[180:183], v[156:159], v[92:95], a[180:183]\n" \
"buffer_load_dwordx4 v25, s[76:79], s84 offen lds\n" \
"v_mfma_f32_16x16x32_bf16 a[180:183], v[160:163], v[96:99], a[180:183]\n" \
"ds_read_b128 v[60:63], v28 offset:2048\n" \
"ds_read_b128 v[64:67], v29 offset:2048\n" \
"v_mfma_f32_16x16x32_bf16 a[184:187], v[168:171], v[96:99], a[184:187]\n" \
"ds_read_b128 v[68:71], v28 offset:4096\n" \
"ds_read_b128 v[72:75], v29 offset:4096\n" \
"v_mfma_f32_16x16x32_bf16 a[184:187], v[164:167], v[92:95], a[184:187]\n" \
"s_add_u32 m0, %[mB], 0x8400\n" \
"s_add_u32 s85, s63, 0x8000\n" \
"v_mfma_f32_16x16x32_bf16 a[188:191], v[172:175], v[92:95], a[188:191]\n" \
"buffer_load_dwordx4 v26, s[68:71], s85 offen lds\n" \
"v_mfma_f32_16x16x32_bf16 a[188:191], v[176:179], v[96:99], a[188:191]\n" \
"ds_read_b128 v[76:79], v28 offset:6144\n" \
"ds_read_b128 v[80:83], v29 offset:6144\n" \
"v_mfma_f32_16x16x32_bf16 a[208:211], v[152:155], v[104:107], a[208:211]\n" \
"s_add_u32 m0, %[mA], 0x9000\n" \
"s_add_u32 s84, s62, 0x9000\n" \
"v_mfma_f32_16x16x32_bf16 a[208:211], v[148:151], v[100:103], a[208:211]\n" \
"buffer_load_dwordx4 v24, s[76:79], s84 offen lds\n" \
"v_mfma_f32_16x16x32_bf16 a[212:215], v[156:159], v[100:103], a[212:215]\n" \
"ds_read_b128 v[92:95], v48 offset:2048\n" \
"ds_read_b128 v[96:99], v49 offset:2048\n" \
"v_mfma_f32_16x16x32_bf16 a[212:215], v[160:163], v[104:107], a[212:215]\n" \
"s_mov_b32 s58, 0x6000000\n" \
"s_mov_b32 s59, 0x20000\n" \
"v_mfma_f32_16x16x32_bf16 a[216:219], v[168:171], v[104:107], a[216:219]\n" \
"s_add_u32 m0, %[mA], 0x9400\n" \
"s_add_u32 s85, s62, 0xb400\n" \
"v_mfma_f32_16x16x32_bf16 a[216:219], v[164:167], v[100:103], a[216:219]\n" \
"buffer_load_dwordx4 v25, s[76:79], s85 offen lds\n" \
"v_mfma_f32_16x16x32_bf16 a[220:223], v[172:175], v[100:103], a[220:223]\n" \
"ds_read_b128 v[100:103], v48 offset:4096\n" \
"s_mov_b64 s[56:57], s[80:81]\n" \
"s_nop 0\n" \
"v_mfma_f32_16x16x32_bf16 a[220:223], v[176:179], v[104:107], a[220:223]\n" \
"s_add_u32 m0, %[mB], 0x8800\n" \
"s_add_u32 s84, s63, 0x10000\n" \
"v_mfma_f32_16x16x32_bf16 a[240:243], v[152:155], v[112:115], a[240:243]\n" \
"buffer_load_dwordx4 v27, s[68:71], s84 offen lds\n" \
"v_mfma_f32_16x16x32_bf16 a[240:243], v[148:151], v[108:111], a[240:243]\n" \
"ds_read_b64_tr_b16 v[152:153], v32 offset:8192\n" \
"ds_read_b64_tr_b16 v[154:155], v32 offset:9216\n" \
"v_mfma_f32_16x16x32_bf16 a[244:247], v[156:159], v[108:111], a[244:247]\n" \
"ds_read_b64_tr_b16 v[148:149], v32 offset:0\n" \
"ds_read_b64_tr_b16 v[150:151], v32 offset:1024\n" \
"v_mfma_f32_16x16x32_bf16 a[244:247], v[160:163], v[112:115], a[244:247]\n" \
"s_add_u32 m0, %[mA], 0x9800\n" \
"s_add_u32 s85, s62, 0xd800\n" \
"v_mfma_f32_16x16x32_bf16 a[248:251], v[168:171], v[112:115], a[248:251]\n" \
"buffer_load_dwordx4 v24, s[76:79], s85 offen lds\n" \
"v_mfma_f32_16x16x32_bf16 a[248:251], v[164:167], v[108:111], a[248:251]\n" \
"ds_read_b64_tr_b16 v[156:157], v33 offset:0\n" \
"ds_read_b64_tr_b16 v[158:159], v33 offset:1024\n" \
"v_mfma_f32_16x16x32_bf16 a[252:255], v[172:175], v[108:111], a[252:255]\n" \
"ds_read_b64_tr_b16 v[160:161], v33 offset:8192\n" \
"ds_read_b64_tr_b16 v[162:163], v33 offset:9216\n" \
"v_mfma_f32_16x16x32_bf16 a[252:255], v[176:179], v[112:115], a[252:255]\n" \
"s_add_u32 m0, %[mA], 0x9c00\n" \
"s_waitcnt lgkmcnt(11)\n" \
"s_nop 0\n" \
"v_smfmac_f32_16x16x64_bf16 a[16:19], v[140:143], v[84:91], v50\n" \
"v_mfma_f32_16x16x32_bf16 a[48:51], v[128:131], v[88:91], a[48:51]\n" \
"s_add_u32 s85, s62, 0xfc00\n" \
"ds_read_b64_tr_b16 v[168:169], v34 offset:8192\n" \
"v_mfma_f32_16x16x32_bf16 a[48:51], v[124:127], v[84:87], a[48:51]\n" \
"buffer_load_dwordx4 v25, s[76:79], s85 offen lds\n" \
"v_mfma_f32_16x16x32_bf16 a[80:83], v[84:87], v[132:135], a[80:83]\n" \
"s_add_u32 m0, %[mB], 0x8c00\n" \
"s_add_u32 s84, s63, 0x18000\n" \
"v_mfma_f32_16x16x32_bf16 a[80:83], v[88:91], v[136:139], a[80:83]\n" \
"ds_read_b64_tr_b16 v[170:171], v34 offset:9216\n" \
"ds_read_b64_tr_b16 v[164:165], v34 offset:0\n" \
"v_smfmac_f32_16x16x64_bf16 a[112:115], v[140:143], v[52:59], v50\n" \
"v_mfma_f32_16x16x32_bf16 a[20:23], v[128:131], v[56:59], a[20:23]\n" \
"buffer_load_dwordx4 v27, s[68:71], s84 offen lds\n" \
"v_mfma_f32_16x16x32_bf16 a[20:23], v[124:127], v[52:55], a[20:23]\n" \
"s_add_u32 s89, s43, 0x1000\n" \
"ds_read_b64_tr_b16 v[166:167], v34 offset:1024\n" \
"v_mfma_f32_16x16x32_bf16 a[52:55], v[116:119], v[52:55], a[52:55]\n" \
"ds_read_b64_tr_b16 v[172:173], v35 offset:0\n" \
"ds_read_b64_tr_b16 v[174:175], v35 offset:1024\n" \
"v_mfma_f32_16x16x32_bf16 a[52:55], v[120:123], v[56:59], a[52:55]\n" \
"buffer_store_dwordx4 a[36:39], v20, s[80:83], s89 offen offset:0 sc1\n" \
"v_mfma_f32_16x16x32_bf16 a[84:87], v[56:59], v[136:139], a[84:87]\n" \
"ds_read_b64_tr_b16 v[176:177], v35 offset:8192\n" \
"ds_read_b64_tr_b16 v[178:179], v35 offset:9216\n" \
"v_mfma_f32_16x16x32_bf16 a[84:87], v[52:55], v[132:135], a[84:87]\n" \
"ds_read_b128 v[84:87], v48 offset:0\n" \
"ds_read_b128 v[88:91], v49 offset:0\n" \
"v_smfmac_f32_16x16x64_bf16 a[116:119], v[140:143], v[60:67], v50\n" \
"v_mfma_f32_16x16x32_bf16 a[24:27], v[124:127], v[60:63], a[24:27]\n" \
"ds_read_b128 v[104:107], v49 offset:4096\n" \
"ds_read_b128 v[108:111], v48 offset:6144\n" \
"v_mfma_f32_16x16x32_bf16 a[24:27], v[128:131], v[64:67], a[24:27]\n" \
"s_add_u32 s88, s43, 0x2000\n" \
"ds_read_b128 v[112:115], v49 offset:6144\n" \
"v_mfma_f32_16x16x32_bf16 a[56:59], v[120:123], v[64:67], a[56:59]\n" \
"buffer_store_dwordx4 a[64:67], v20, s[80:83], s88 offen offset:0 sc1\n" \
"v_mfma_f32_16x16x32_bf16 a[56:59], v[116:119], v[60:63], a[56:59]\n" \
"s_add_u32 s8, s8, s80\n" \
"s_addc_u32 s9, s81, 0\n" \
"v_mfma_f32_16x16x32_bf16 a[88:91], v[60:63], v[132:135], a[88:91]\n" \
"s_add_u32 s62, s62, 0x80\n" \
"v_mfma_f32_16x16x32_bf16 a[88:91], v[64:67], v[136:139], a[88:91]\n" \
"s_cmp_lt_u32 s62, 0x480\n" \
"s_cselect_b32 s62, s62, 0\n" \
"v_smfmac_f32_16x16x64_bf16 a[120:123], v[140:143], v[68:75], v50\n" \
"v_mfma_f32_16x16x32_bf16 a[28:31], v[128:131], v[72:75], a[28:31]\n" \
"s_lshl_b32 s72, s62, 6\n" \
"s_add_u32 s72, s72, s37\n" \
"v_mfma_f32_16x16x32_bf16 a[28:31], v[124:127], v[68:71], a[28:31]\n" \
"s_lshl_b32 s63, s62, 12\n" \
"v_mfma_f32_16x16x32_bf16 a[60:63], v[116:119], v[68:71], a[60:63]\n" \
"s_add_u32 s89, s43, 0x3000\n" \
"v_mfma_f32_16x16x32_bf16 a[60:63], v[120:123], v[72:75], a[60:63]\n" \
"buffer_store_dwordx4 a[68:71], v20, s[80:83], s89 offen offset:0 sc1\n" \
"v_mfma_f32_16x16x32_bf16 a[92:95], v[72:75], v[136:139], a[92:95]\n" \
"v_mfma_f32_16x16x32_bf16 a[92:95], v[68:71], v[132:135], a[92:95]\n" \
"v_smfmac_f32_16x16x64_bf16 a[124:127], v[140:143], v[76:83], v50\n" \
"v_mfma_f32_16x16x32_bf16 a[0:3], v[124:127], v[76:79], a[0:3]\n" \
"v_mfma_f32_16x16x32_bf16 a[0:3], v[128:131], v[80:83], a[0:3]\n" \
"v_mfma_f32_16x16x32_bf16 a[4:7], v[120:123], v[80:83], a[4:7]\n" \
"v_mfma_f32_16x16x32_bf16 a[4:7], v[116:119], v[76:79], a[4:7]\n" \
"v_mfma_f32_16x16x32_bf16 a[32:35], v[76:79], v[132:135], a[32:35]\n" \
"v_mfma_f32_16x16x32_bf16 a[32:35], v[80:83], v[136:139], a[32:35]\n" \
"s_waitcnt lgkmcnt(0)\n" \
"s_nop 0\n" \
"v_mfma_f32_16x16x32_bf16 a[128:131], v[152:155], v[56:59], a[128:131]\n" \
"v_mfma_f32_16x16x32_bf16 a[128:131], v[148:151], v[52:55], a[128:131]\n" \
"v_mfma_f32_16x16x32_bf16 a[132:135], v[156:159], v[52:55], a[132:135]\n" \
"v_mfma_f32_16x16x32_bf16 a[132:135], v[160:163], v[56:59], a[132:135]\n" \
"v_mfma_f32_16x16x32_bf16 a[136:139], v[168:171], v[56:59], a[136:139]\n" \
"v_mfma_f32_16x16x32_bf16 a[136:139], v[164:167], v[52:55], a[136:139]\n" \
"v_mfma_f32_16x16x32_bf16 a[140:143], v[172:175], v[52:55], a[140:143]\n" \
"v_mfma_f32_16x16x32_bf16 a[140:143], v[176:179], v[56:59], a[140:143]\n" \
"v_mfma_f32_16x16x32_bf16 a[160:163], v[152:155], v[64:67], a[160:163]\n" \
"v_mfma_f32_16x16x32_bf16 a[160:163], v[148:151], v[60:63], a[160:163]\n" \
"v_mfma_f32_16x16x32_bf16 a[164:167], v[156:159], v[60:63], a[164:167]\n" \
"v_mfma_f32_16x16x32_bf16 a[164:167], v[160:163], v[64:67], a[164:167]\n" \
"v_mfma_f32_16x16x32_bf16 a[168:171], v[168:171], v[64:67], a[168:171]\n" \
"v_mfma_f32_16x16x32_bf16 a[168:171], v[164:167], v[60:63], a[168:171]\n" \
"v_mfma_f32_16x16x32_bf16 a[172:175], v[172:175], v[60:63], a[172:175]\n" \
"v_mfma_f32_16x16x32_bf16 a[172:175], v[176:179], v[64:67], a[172:175]\n" \
"v_mfma_f32_16x16x32_bf16 a[192:195], v[152:155], v[72:75], a[192:195]\n" \
"v_mfma_f32_16x16x32_bf16 a[192:195], v[148:151], v[68:71], a[192:195]\n" \
"s_waitcnt vmcnt(3)\n" \
"s_barrier\n" \
"v_mfma_f32_16x16x32_bf16 a[196:199], v[156:159], v[68:71], a[196:199]\n" \
"s_add_u32 m0, s40, 0x0\n" \
"s_add_u32 s85, s72, 0x0\n" \
"v_mfma_f32_16x16x32_bf16 a[196:199], v[160:163], v[72:75], a[196:199]\n" \
"buffer_load_dwordx4 v20, s[80:83], s85 offen sc1 lds\n" \
"v_mfma_f32_16x16x32_bf16 a[200:203], v[168:171], v[72:75], a[200:203]\n" \
"s_add_u32 m0, s40, 0x1000\n" \
"s_add_u32 s84, s72, 0x1000\n" \
"v_mfma_f32_16x16x32_bf16 a[200:203], v[164:167], v[68:71], a[200:203]\n" \
"ds_read_b64_tr_b16 v[140:141], v39 offset:32768\n" \
"ds_read_b64_tr_b16 v[142:143], v39 offset:33728\n" \
"v_mfma_f32_16x16x32_bf16 a[204:207], v[172:175], v[68:71], a[204:207]\n" \
"buffer_load_dwordx4 v20, s[80:83], s84 offen sc1 lds\n" \
"v_mfma_f32_16x16x32_bf16 a[204:207], v[176:179], v[72:75], a[204:207]\n" \
"ds_read_b64_tr_b16 v[128:129], v37 offset:36864\n" \
"ds_read_b64_tr_b16 v[130:131], v37 offset:37376\n" \
"v_mfma_f32_16x16x32_bf16 a[224:227], v[152:155], v[80:83], a[224:227]\n" \
"s_add_u32 m0, s40, 0x3000\n" \
"s_add_u32 s85, s62, 0x0\n" \
"v_mfma_f32_16x16x32_bf16 a[224:227], v[148:151], v[76:79], a[224:227]\n" \
"buffer_load_dwordx4 v21, s[76:79], s85 offen lds\n" \
"v_mfma_f32_16x16x32_bf16 a[228:231], v[156:159], v[76:79], a[228:231]\n" \
"ds_read_b64_tr_b16 v[124:125], v37 offset:32768\n" \
"ds_read_b64_tr_b16 v[126:127], v37 offset:33280\n" \
"v_mfma_f32_16x16x32_bf16 a[228:231], v[160:163], v[80:83], a[228:231]\n" \
"s_add_u32 m0, %[mA], 0x0\n" \
"s_add_u32 s84, s62, 0x0\n" \
"s_nop 0\n" \
"v_mfma_f32_16x16x32_bf16 a[232:235], v[168:171], v[80:83], a[232:235]\n" \
"buffer_load_dwordx4 v24, s[76:79], s84 offen lds\n" \
"v_mfma_f32_16x16x32_bf16 a[232:235], v[164:167], v[76:79], a[232:235]\n" \
"ds_read_b64_tr_b16 v[132:133], v38 offset:32768\n" \
"ds_read_b64_tr_b16 v[134:135], v38 offset:33280\n" \
"v_mfma_f32_16x16x32_bf16 a[236:239], v[172:175], v[76:79], a[236:239]\n" \
"ds_read_b64_tr_b16 v[136:137], v38 offset:36864\n" \
"ds_read_b64_tr_b16 v[138:139], v38 offset:37376\n" \
"v_mfma_f32_16x16x32_bf16 a[236:239], v[176:179], v[80:83], a[236:239]\n" \
"s_add_u32 m0, %[mA], 0x400\n" \
"s_add_u32 s85, s62, 0x2400\n" \
"v_mfma_f32_16x16x32_bf16 a[144:147], v[152:155], v[88:91], a[144:147]\n" \
"buffer_load_dwordx4 v25, s[76:79], s85 offen lds\n" \
"v_mfma_f32_16x16x32_bf16 a[144:147], v[148:151], v[84:87], a[144:147]\n" \
"ds_read_b128 v[52:55], v28 offset:32768\n" \
"ds_read_b128 v[56:59], v29 offset:32768\n" \
"v_mfma_f32_16x16x32_bf16 a[148:151], v[156:159], v[84:87], a[148:151]\n" \
"s_add_u32 m0, %[mB], 0x0\n" \
"s_add_u32 s84, s63, 0x0\n" \
"v_mfma_f32_16x16x32_bf16 a[148:151], v[160:163], v[88:91], a[148:151]\n" \
"buffer_load_dwordx4 v26, s[68:71], s84 offen lds\n" \
"v_mfma_f32_16x16x32_bf16 a[152:155], v[168:171], v[88:91], a[152:155]\n" \
"ds_read_b64_tr_b16 v[116:117], v36 offset:32768\n" \
"ds_read_b64_tr_b16 v[118:119], v36 offset:33280\n" \
"v_mfma_f32_16x16x32_bf16 a[152:155], v[164:167], v[84:87], a[152:155]\n" \
"ds_read_b64_tr_b16 v[120:121], v36 offset:36864\n" \
"ds_read_b64_tr_b16 v[122:123], v36 offset:37376\n" \
"v_mfma_f32_16x16x32_bf16 a[156:159], v[172:175], v[84:87], a[156:159]\n" \
"s_add_u32 m0, %[mA], 0x800\n" \
"s_add_u32 s85, s62, 0x4800\n" \
"v_mfma_f32_16x16x32_bf16 a[156:159], v[176:179], v[88:91], a[156:159]\n" \
"buffer_load_dwordx4 v24, s[76:79], s85 offen lds\n" \
"v_mfma_f32_16x16x32_bf16 a[176:179], v[152:155], v[96:99], a[176:179]\n" \
"ds_read_b128 v[84:87], v22 offset:32768\n" \
"ds_read_b128 v[88:91], v23 offset:32768\n" \
"v_mfma_f32_16x16x32_bf16 a[176:179], v[148:151], v[92:95], a[176:179]\n" \
"s_add_u32 m0, %[mA], 0xc00\n" \
"s_add_u32 s84, s62, 0x6c00\n" \
"v_mfma_f32_16x16x32_bf16 a[180:183], v[156:159], v[92:95], a[180:183]\n" \
"buffer_load_dwordx4 v25, s[76:79], s84 offen lds\n" \
"v_mfma_f32_16x16x32_bf16 a[180:183], v[160:163], v[96:99], a[180:183]\n" \
"ds_read_b128 v[60:63], v28 offset:34816\n" \
"ds_read_b128 v[64:67], v29 offset:34816\n" \
"v_mfma_f32_16x16x32_bf16 a[184:187], v[168:171], v[96:99], a[184:187]\n" \
"ds_read_b128 v[68:71], v28 offset:36864\n" \
"ds_read_b128 v[72:75], v29 offset:36864\n" \
"v_mfma_f32_16x16x32_bf16 a[184:187], v[164:167], v[92:95], a[184:187]\n" \
"s_add_u32 m0, %[mB], 0x400\n" \
"s_add_u32 s85, s63, 0x8000\n" \
"v_mfma_f32_16x16x32_bf16 a[188:191], v[172:175], v[92:95], a[188:191]\n" \
"buffer_load_dwordx4 v26, s[68:71], s85 offen lds\n" \
"v_mfma_f32_16x16x32_bf16 a[188:191], v[176:179], v[96:99], a[188:191]\n" \
"ds_read_b128 v[76:79], v28 offset:38912\n" \
"ds_read_b128 v[80:83], v29 offset:38912\n" \
"v_mfma_f32_16x16x32_bf16 a[208:211], v[152:155], v[104:107], a[208:211]\n" \
"s_add_u32 m0, %[mA], 0x1000\n" \
"s_add_u32 s84, s62, 0x9000\n" \
"v_mfma_f32_16x16x32_bf16 a[208:211], v[148:151], v[100:103], a[208:211]\n" \
"buffer_load_dwordx4 v24, s[76:79], s84 offen lds\n" \
"v_mfma_f32_16x16x32_bf16 a[212:215], v[156:159], v[100:103], a[212:215]\n" \
"ds_read_b128 v[92:95], v48 offset:34816\n" \
"ds_read_b128 v[96:99], v49 offset:34816\n" \
"v_mfma_f32_16x16x32_bf16 a[212:215], v[160:163], v[104:107], a[212:215]\n" \
"s_mov_b32 s72, 0x3eae1a93\n" \
"v_and_b32_e32 v26, 0x1ff, v20\n" \
"v_mfma_f32_16x16x32_bf16 a[216:219], v[168:171], v[104:107], a[216:219]\n" \
"s_add_u32 m0, %[mA], 0x1400\n" \
"s_add_u32 s85, s62, 0xb400\n" \
"v_mfma_f32_16x16x32_bf16 a[216:219], v[164:167], v[100:103], a[216:219]\n" \
"buffer_load_dwordx4 v25, s[76:79], s85 offen lds\n" \
"v_mfma_f32_16x16x32_bf16 a[220:223], v[172:175], v[100:103], a[220:223]\n" \
"ds_read_b128 v[100:103], v48 offset:36864\n" \
"v_add_u32_e64 v21, s48, v20\n" \
"v_mfma_f32_16x16x32_bf16 a[220:223], v[176:179], v[104:107], a[220:223]\n" \
"s_add_u32 m0, %[mB], 0x800\n" \
"s_add_u32 s84, s63, 0x10000\n" \
"v_mfma_f32_16x16x32_bf16 a[240:243], v[152:155], v[112:115], a[240:243]\n" \
"buffer_load_dwordx4 v27, s[68:71], s84 offen lds\n" \
"v_mfma_f32_16x16x32_bf16 a[240:243], v[148:151], v[108:111], a[240:243]\n" \
"ds_read_b64_tr_b16 v[152:153], v32 offset:40960\n" \
"ds_read_b64_tr_b16 v[154:155], v32 offset:41984\n" \
"v_mfma_f32_16x16x32_bf16 a[244:247], v[156:159], v[108:111], a[244:247]\n" \
"ds_read_b64_tr_b16 v[148:149], v32 offset:32768\n" \
"ds_read_b64_tr_b16 v[150:151], v32 offset:33792\n" \
"v_mfma_f32_16x16x32_bf16 a[244:247], v[160:163], v[112:115], a[244:247]\n" \
"s_add_u32 m0, %[mA], 0x1800\n" \
"s_add_u32 s85, s62, 0xd800\n" \
"v_mfma_f32_16x16x32_bf16 a[248:251], v[168:171], v[112:115], a[248:251]\n" \
"buffer_load_dwordx4 v24, s[76:79], s85 offen lds\n" \
"v_mfma_f32_16x16x32_bf16 a[248:251], v[164:167], v[108:111], a[248:251]\n" \
"ds_read_b64_tr_b16 v[156:157], v33 offset:32768\n" \
"ds_read_b64_tr_b16 v[158:159], v33 offset:33792\n" \
"v_mfma_f32_16x16x32_bf16 a[252:255], v[172:175], v[108:111], a[252:255]\n" \
"ds_read_b64_tr_b16 v[160:161], v33 offset:40960\n" \
"ds_read_b64_tr_b16 v[162:163], v33 offset:41984\n" \
"v_mfma_f32_16x16x32_bf16 a[252:255], v[176:179], v[112:115], a[252:255]\n" \
"s_add_u32 m0, %[mA], 0x1c00\n" \
"s_waitcnt lgkmcnt(11)\n" \
"v_smfmac_f32_16x16x64_bf16 a[16:19], v[140:143], v[84:91], v50\n" \
"v_mfma_f32_16x16x32_bf16 a[48:51], v[128:131], v[88:91], a[48:51]\n" \
"s_add_u32 s85, s62, 0xfc00\n" \
"ds_read_b64_tr_b16 v[168:169], v34 offset:40960\n" \
"v_mfma_f32_16x16x32_bf16 a[48:51], v[124:127], v[84:87], a[48:51]\n" \
"buffer_load_dwordx4 v25, s[76:79], s85 offen lds\n" \
"L_pf8:\n" \
"s_cbranch_execz L_pf12\n" \
"s_nop 0\n" \
"s_mov_b32 s76, s54\n" \
"v_mfma_f32_16x16x32_bf16 a[80:83], v[84:87], v[132:135], a[80:83]\n" \
"s_add_u32 m0, %[mB], 0xc00\n" \
"s_add_u32 s84, s63, 0x18000\n" \
"v_mfma_f32_16x16x32_bf16 a[80:83], v[88:91], v[136:139], a[80:83]\n" \
"ds_read_b64_tr_b16 v[170:171], v34 offset:41984\n" \
"ds_read_b64_tr_b16 v[164:165], v34 offset:32768\n" \
"v_smfmac_f32_16x16x64_bf16 a[112:115], v[140:143], v[52:59], v50\n" \
"v_mfma_f32_16x16x32_bf16 a[20:23], v[128:131], v[56:59], a[20:23]\n" \
"buffer_load_dwordx4 v27, s[68:71], s84 offen lds\n" \
"v_mfma_f32_16x16x32_bf16 a[20:23], v[124:127], v[52:55], a[20:23]\n" \
"s_add_u32 s88, s43, 0x0\n" \
"s_nop 0\n" \
"ds_read_b64_tr_b16 v[166:167], v34 offset:33792\n" \
"v_mfma_f32_16x16x32_bf16 a[52:55], v[116:119], v[52:55], a[52:55]\n" \
"ds_read_b64_tr_b16 v[172:173], v35 offset:32768\n" \
"ds_read_b64_tr_b16 v[174:175], v35 offset:33792\n" \
"v_mfma_f32_16x16x32_bf16 a[52:55], v[120:123], v[56:59], a[52:55]\n" \
"buffer_store_dwordx4 a[96:99], v20, s[80:83], s88 offen offset:1024 sc1\n" \
"v_mfma_f32_16x16x32_bf16 a[84:87], v[56:59], v[136:139], a[84:87]\n" \
"ds_read_b64_tr_b16 v[176:177], v35 offset:40960\n" \
"ds_read_b64_tr_b16 v[178:179], v35 offset:41984\n" \
"v_mfma_f32_16x16x32_bf16 a[84:87], v[52:55], v[132:135], a[84:87]\n" \
"ds_read_b128 v[84:87], v48 offset:32768\n" \
"ds_read_b128 v[88:91], v49 offset:32768\n" \
"v_smfmac_f32_16x16x64_bf16 a[116:119], v[140:143], v[60:67], v50\n" \
"v_mfma_f32_16x16x32_bf16 a[24:27], v[124:127], v[60:63], a[24:27]\n" \
"ds_read_b128 v[104:107], v49 offset:36864\n" \
"ds_read_b128 v[108:111], v48 offset:38912\n" \
"v_mfma_f32_16x16x32_bf16 a[24:27], v[128:131], v[64:67], a[24:27]\n" \
"s_add_u32 s89, s43, 0x1000\n" \
"ds_read_b128 v[112:115], v49 offset:38912\n" \
"v_mfma_f32_16x16x32_bf16 a[56:59], v[120:123], v[64:67], a[56:59]\n" \
"buffer_store_dwordx4 a[100:103], v20, s[80:83], s89 offen offset:1024 sc1\n" \
"s_add_u32 s89, s43, 0x3000\n" \
"v_mfma_f32_16x16x32_bf16 a[56:59], v[116:119], v[60:63], a[56:59]\n" \
"v_accvgpr_read_b32 v248, a16\n" \
"v_accvgpr_read_b32 v249, a17\n" \
"v_mfma_f32_16x16x32_bf16 a[88:91], v[60:63], v[132:135], a[88:91]\n" \
"v_accvgpr_read_b32 v250, a18\n" \
"v_accvgpr_read_b32 v251, a19\n" \
"v_mfma_f32_16x16x32_bf16 a[88:91], v[64:67], v[136:139], a[88:91]\n" \
"v_accvgpr_read_b32 v180, a48\n" \
"v_accvgpr_read_b32 v181, a49\n" \
"v_smfmac_f32_16x16x64_bf16 a[120:123], v[140:143], v[68:75], v50\n" \
"v_mfma_f32_16x16x32_bf16 a[28:31], v[128:131], v[72:75], a[28:31]\n" \
"v_accvgpr_read_b32 v182, a50\n" \
"v_accvgpr_read_b32 v183, a51\n" \
"v_mfma_f32_16x16x32_bf16 a[28:31], v[124:127], v[68:71], a[28:31]\n" \
"v_accvgpr_read_b32 v252, a80\n" \
"v_accvgpr_read_b32 v253, a81\n" \
"v_mfma_f32_16x16x32_bf16 a[60:63], v[116:119], v[68:71], a[60:63]\n" \
"s_add_u32 s88, s43, 0x2000\n" \
"v_accvgpr_read_b32 v254, a82\n" \
"v_mfma_f32_16x16x32_bf16 a[60:63], v[120:123], v[72:75], a[60:63]\n" \
"buffer_store_dwordx4 a[8:11], v20, s[80:83], s88 offen offset:1024 sc1\n" \
"s_add_u32 s88, s45, 0x0\n" \
"s_nop 0\n" \
"v_mfma_f32_16x16x32_bf16 a[92:95], v[72:75], v[136:139], a[92:95]\n" \
"v_accvgpr_read_b32 v255, a83\n" \
"v_accvgpr_read_b32 v196, a112\n" \
"v_mfma_f32_16x16x32_bf16 a[92:95], v[68:71], v[132:135], a[92:95]\n" \
"v_accvgpr_read_b32 v197, a113\n" \
"v_accvgpr_read_b32 v198, a114\n" \
"v_smfmac_f32_16x16x64_bf16 a[124:127], v[140:143], v[76:83], v50\n" \
"v_mfma_f32_16x16x32_bf16 a[0:3], v[124:127], v[76:79], a[0:3]\n" \
"v_accvgpr_read_b32 v199, a115\n" \
"v_accvgpr_read_b32 v188, a20\n" \
"v_mfma_f32_16x16x32_bf16 a[0:3], v[128:131], v[80:83], a[0:3]\n" \
"v_accvgpr_read_b32 v189, a21\n" \
"v_accvgpr_read_b32 v190, a22\n" \
"v_mfma_f32_16x16x32_bf16 a[4:7], v[120:123], v[80:83], a[4:7]\n" \
"v_accvgpr_read_b32 v191, a23\n" \
"v_accvgpr_read_b32 v184, a52\n" \
"v_mfma_f32_16x16x32_bf16 a[4:7], v[116:119], v[76:79], a[4:7]\n" \
"v_lshlrev_b32_e32 v45, 10, v20\n" \
"v_add_u32_e32 v45, s41, v45\n" \
"v_mfma_f32_16x16x32_bf16 a[32:35], v[76:79], v[132:135], a[32:35]\n" \
"s_mov_b32 m0, %[mS]\n" \
"v_accvgpr_read_b32 v185, a53\n" \
"v_mfma_f32_16x16x32_bf16 a[32:35], v[80:83], v[136:139], a[32:35]\n" \
"v_accvgpr_read_b32 v186, a54\n" \
"s_waitcnt lgkmcnt(0)\n" \
"v_mfma_f32_16x16x32_bf16 a[128:131], v[152:155], v[56:59], a[128:131]\n" \
"v_accvgpr_read_b32 v187, a55\n" \
"v_accvgpr_read_b32 v192, a84\n" \
"v_mfma_f32_16x16x32_bf16 a[128:131], v[148:151], v[52:55], a[128:131]\n" \
"v_accvgpr_read_b32 v193, a85\n" \
"v_accvgpr_read_b32 v194, a86\n" \
"v_mfma_f32_16x16x32_bf16 a[132:135], v[156:159], v[52:55], a[132:135]\n" \
"v_accvgpr_read_b32 v195, a87\n" \
"v_accvgpr_read_b32 v212, a116\n" \
"s_getpc_b64 s[86:87]\n" \
"L_ct15_%=:\n" \
"s_add_u32 s86, s86, L_end_%=-L_ct15_%=\n" \
"s_addc_u32 s87, s87, 0\n" \
"v_mfma_f32_16x16x32_bf16 a[132:135], v[160:163], v[56:59], a[132:135]\n" \
"v_accvgpr_read_b32 v213, a117\n" \
"v_accvgpr_read_b32 v214, a118\n" \
"v_mfma_f32_16x16x32_bf16 a[136:139], v[168:171], v[56:59], a[136:139]\n" \
"v_accvgpr_read_b32 v215, a119\n" \
"v_accvgpr_read_b32 v204, a24\n" \
"v_mfma_f32_16x16x32_bf16 a[136:139], v[164:167], v[52:55], a[136:139]\n" \
"v_accvgpr_read_b32 v205, a25\n" \
"v_accvgpr_read_b32 v206, a26\n" \
"v_mfma_f32_16x16x32_bf16 a[140:143], v[172:175], v[52:55], a[140:143]\n" \
"v_accvgpr_read_b32 v207, a27\n" \
"v_accvgpr_read_b32 v200, a56\n" \
"v_mfma_f32_16x16x32_bf16 a[140:143], v[176:179], v[56:59], a[140:143]\n" \
"v_accvgpr_read_b32 v201, a57\n" \
"v_accvgpr_read_b32 v202, a58\n" \
"v_mfma_f32_16x16x32_bf16 a[160:163], v[152:155], v[64:67], a[160:163]\n" \
"v_accvgpr_read_b32 v203, a59\n" \
"v_accvgpr_read_b32 v208, a88\n" \
"v_mfma_f32_16x16x32_bf16 a[160:163], v[148:151], v[60:63], a[160:163]\n" \
"v_accvgpr_read_b32 v209, a89\n" \
"v_accvgpr_read_b32 v210, a90\n" \
"v_mfma_f32_16x16x32_bf16 a[164:167], v[156:159], v[60:63], a[164:167]\n" \
"v_accvgpr_read_b32 v211, a91\n" \
"v_accvgpr_read_b32 v228, a120\n" \
"v_mfma_f32_16x16x32_bf16 a[164:167], v[160:163], v[64:67], a[164:167]\n" \
"v_accvgpr_read_b32 v229, a121\n" \
"v_accvgpr_read_b32 v230, a122\n" \
"v_mfma_f32_16x16x32_bf16 a[168:171], v[168:171], v[64:67], a[168:171]\n" \
"v_accvgpr_read_b32 v231, a123\n" \
"v_accvgpr_read_b32 v220, a28\n" \
"v_mfma_f32_16x16x32_bf16 a[168:171], v[164:167], v[60:63], a[168:171]\n" \
"v_accvgpr_read_b32 v221, a29\n" \
"v_accvgpr_read_b32 v222, a30\n" \
"v_mfma_f32_16x16x32_bf16 a[172:175], v[172:175], v[60:63], a[172:175]\n" \
"v_accvgpr_read_b32 v223, a31\n" \
"v_accvgpr_read_b32 v216, a60\n" \
"v_mfma_f32_16x16x32_bf16 a[172:175], v[176:179], v[64:67], a[172:175]\n" \
"v_accvgpr_read_b32 v217, a61\n" \
"v_accvgpr_read_b32 v218, a62\n" \
"v_mfma_f32_16x16x32_bf16 a[192:195], v[152:155], v[72:75], a[192:195]\n" \
"v_accvgpr_read_b32 v219, a63\n" \
"v_accvgpr_read_b32 v224, a92\n" \
"v_mfma_f32_16x16x32_bf16 a[192:195], v[148:151], v[68:71], a[192:195]\n" \
"v_accvgpr_read_b32 v225, a93\n" \
"v_accvgpr_read_b32 v226, a94\n" \
"s_waitcnt vmcnt(3)\n" \
"s_barrier\n" \
"s_mov_b64 exec, 3\n" \
"global_load_lds_dword v45, s[86:87]\n" \
"s_mov_b64 exec, -1\n" \
"s_sub_u32 m0, %[mB], 0x10000\n" \
"v_mfma_f32_16x16x32_bf16 a[196:199], v[156:159], v[68:71], a[196:199]\n" \
"ds_read_b64_tr_b16 v[140:141], v39 offset:0\n" \
"ds_read_b64_tr_b16 v[142:143], v39 offset:960\n" \
"v_mfma_f32_16x16x32_bf16 a[196:199], v[160:163], v[72:75], a[196:199]\n" \
"ds_read_b64_tr_b16 v[128:129], v37 offset:4096\n" \
"ds_read_b64_tr_b16 v[130:131], v37 offset:4608\n" \
"v_mfma_f32_16x16x32_bf16 a[200:203], v[168:171], v[72:75], a[200:203]\n" \
"ds_read_b64_tr_b16 v[124:125], v37 offset:0\n" \
"ds_read_b64_tr_b16 v[126:127], v37 offset:512\n" \
"v_mfma_f32_16x16x32_bf16 a[200:203], v[164:167], v[68:71], a[200:203]\n" \
"ds_read_b64_tr_b16 v[132:133], v38 offset:0\n" \
"ds_read_b64_tr_b16 v[134:135], v38 offset:512\n" \
"v_mfma_f32_16x16x32_bf16 a[204:207], v[172:175], v[68:71], a[204:207]\n" \
"ds_read_b64_tr_b16 v[136:137], v38 offset:4096\n" \
"ds_read_b64_tr_b16 v[138:139], v38 offset:4608\n" \
"v_mfma_f32_16x16x32_bf16 a[204:207], v[176:179], v[72:75], a[204:207]\n" \
"ds_read_b128 v[52:55], v28 offset:0\n" \
"ds_read_b128 v[56:59], v29 offset:0\n" \
"v_mfma_f32_16x16x32_bf16 a[224:227], v[152:155], v[80:83], a[224:227]\n" \
"ds_read_b64_tr_b16 v[116:117], v36 offset:0\n" \
"ds_read_b64_tr_b16 v[118:119], v36 offset:512\n" \
"v_mfma_f32_16x16x32_bf16 a[224:227], v[148:151], v[76:79], a[224:227]\n" \
"ds_read_b64_tr_b16 v[120:121], v36 offset:4096\n" \
"ds_read_b64_tr_b16 v[122:123], v36 offset:4608\n" \
"v_mfma_f32_16x16x32_bf16 a[228:231], v[156:159], v[76:79], a[228:231]\n" \
"ds_read_b128 v[60:63], v28 offset:2048\n" \
"ds_read_b128 v[64:67], v29 offset:2048\n" \
"v_mfma_f32_16x16x32_bf16 a[228:231], v[160:163], v[80:83], a[228:231]\n" \
"v_accvgpr_read_b32 v227, a95\n" \
"v_accvgpr_read_b32 v244, a124\n" \
"v_mfma_f32_16x16x32_bf16 a[232:235], v[168:171], v[80:83], a[232:235]\n" \
"ds_read_b128 v[68:71], v28 offset:4096\n" \
"ds_read_b128 v[72:75], v29 offset:4096\n" \
"v_mfma_f32_16x16x32_bf16 a[232:235], v[164:167], v[76:79], a[232:235]\n" \
"v_accvgpr_read_b32 v245, a125\n" \
"v_accvgpr_read_b32 v246, a126\n" \
"v_mfma_f32_16x16x32_bf16 a[236:239], v[172:175], v[76:79], a[236:239]\n" \
"ds_read_b128 v[76:79], v28 offset:6144\n" \
"v_accvgpr_read_b32 v247, a127\n" \
"v_mfma_f32_16x16x32_bf16 a[236:239], v[176:179], v[80:83], a[236:239]\n" \
"ds_read_b128 v[80:83], v29 offset:6144\n" \
"v_accvgpr_read_b32 v236, a0\n" \
"v_mfma_f32_16x16x32_bf16 a[144:147], v[152:155], v[88:91], a[144:147]\n" \
"v_accvgpr_read_b32 v237, a1\n" \
"v_accvgpr_read_b32 v238, a2\n" \
"v_mfma_f32_16x16x32_bf16 a[144:147], v[148:151], v[84:87], a[144:147]\n" \
"v_accvgpr_read_b32 v239, a3\n" \
"v_accvgpr_read_b32 v232, a4\n" \
"v_mfma_f32_16x16x32_bf16 a[148:151], v[156:159], v[84:87], a[148:151]\n" \
"v_accvgpr_read_b32 v233, a5\n" \
"v_accvgpr_read_b32 v234, a6\n" \
"v_mfma_f32_16x16x32_bf16 a[148:151], v[160:163], v[88:91], a[148:151]\n" \
"v_accvgpr_read_b32 v235, a7\n" \
"v_accvgpr_read_b32 v240, a32\n" \
"v_mfma_f32_16x16x32_bf16 a[152:155], v[168:171], v[88:91], a[152:155]\n" \
"v_accvgpr_read_b32 v241, a33\n" \
"v_accvgpr_read_b32 v242, a34\n" \
"v_mfma_f32_16x16x32_bf16 a[152:155], v[164:167], v[84:87], a[152:155]\n" \
"v_accvgpr_read_b32 v243, a35\n" \
"s_mov_b32 s78, -1\n" \
"v_mfma_f32_16x16x32_bf16 a[156:159], v[172:175], v[84:87], a[156:159]\n" \
"ds_read_b128 v[84:87], v22 offset:0\n" \
"s_mov_b32 s79, 0\n" \
"v_mfma_f32_16x16x32_bf16 a[156:159], v[176:179], v[88:91], a[156:159]\n" \
"ds_read_b128 v[88:91], v23 offset:0\n" \
"s_mov_b32 s68, 0xffc0fffc\n" \
"v_mfma_f32_16x16x32_bf16 a[176:179], v[152:155], v[96:99], a[176:179]\n" \
"s_mov_b32 s69, 0xc000fc00\n" \
"v_lshrrev_b32_e32 v24, 9, v20\n" \
"v_mfma_f32_16x16x32_bf16 a[176:179], v[148:151], v[92:95], a[176:179]\n" \
"v_lshrrev_b32_e32 v25, 8, v20\n" \
"v_mul_u32_u24_e32 v24, 0x24800, v24\n" \
"v_mfma_f32_16x16x32_bf16 a[180:183], v[156:159], v[92:95], a[180:183]\n" \
"v_mul_u32_u24_e32 v25, 0x24700, v25\n" \
"v_add_u32_e64 v24, v24, v26\n" \
"v_mfma_f32_16x16x32_bf16 a[180:183], v[160:163], v[96:99], a[180:183]\n" \
"v_add_u32_e32 v25, v25, v20\n" \
"s_add_u32 s85, s50, 0x0\n" \
"v_mfma_f32_16x16x32_bf16 a[184:187], v[168:171], v[96:99], a[184:187]\n" \
"s_add_u32 s87, s50, 0x49000\n" \
"s_mov_b32 s70, 0x3e0293ee\n" \
"v_mfma_f32_16x16x32_bf16 a[184:187], v[164:167], v[92:95], a[184:187]\n" \
"s_mov_b32 s71, 0x3e0293ee\n" \
"v_add_u32_e64 v27, s47, v20\n" \
"v_mfma_f32_16x16x32_bf16 a[188:191], v[172:175], v[92:95], a[188:191]\n" \
"ds_read_b128 v[92:95], v48 offset:2048\n" \
"v_lshrrev_b32_e32 v26, 2, v20\n" \
"v_mfma_f32_16x16x32_bf16 a[188:191], v[176:179], v[96:99], a[188:191]\n" \
"ds_read_b128 v[96:99], v49 offset:2048\n" \
"v_add_u32_e32 v26, s46, v26\n" \
"v_mfma_f32_16x16x32_bf16 a[208:211], v[152:155], v[104:107], a[208:211]\n" \
"v_mfma_f32_16x16x32_bf16 a[208:211], v[148:151], v[100:103], a[208:211]\n" \
"v_mfma_f32_16x16x32_bf16 a[212:215], v[156:159], v[100:103], a[212:215]\n" \
"v_mfma_f32_16x16x32_bf16 a[212:215], v[160:163], v[104:107], a[212:215]\n" \
"v_mfma_f32_16x16x32_bf16 a[216:219], v[168:171], v[104:107], a[216:219]\n" \
"v_mfma_f32_16x16x32_bf16 a[216:219], v[164:167], v[100:103], a[216:219]\n" \
"v_mfma_f32_16x16x32_bf16 a[220:223], v[172:175], v[100:103], a[220:223]\n" \
"ds_read_b128 v[100:103], v48 offset:4096\n" \
"v_mfma_f32_16x16x32_bf16 a[220:223], v[176:179], v[104:107], a[220:223]\n" \
"ds_read_b128 v[104:107], v49 offset:4096\n" \
"v_mfma_f32_16x16x32_bf16 a[240:243], v[152:155], v[112:115], a[240:243]\n" \
"ds_read_b64_tr_b16 v[152:153], v32 offset:8192\n" \
"ds_read_b64_tr_b16 v[154:155], v32 offset:9216\n" \
"v_mfma_f32_16x16x32_bf16 a[240:243], v[148:151], v[108:111], a[240:243]\n" \
"ds_read_b64_tr_b16 v[148:149], v32 offset:0\n" \
"ds_read_b64_tr_b16 v[150:151], v32 offset:1024\n" \
"v_mfma_f32_16x16x32_bf16 a[244:247], v[156:159], v[108:111], a[244:247]\n" \
"ds_read_b64_tr_b16 v[156:157], v33 offset:0\n" \
"ds_read_b64_tr_b16 v[158:159], v33 offset:1024\n" \
"v_mfma_f32_16x16x32_bf16 a[244:247], v[160:163], v[112:115], a[244:247]\n" \
"ds_read_b64_tr_b16 v[160:161], v33 offset:8192\n" \
"ds_read_b64_tr_b16 v[162:163], v33 offset:9216\n" \
"v_mfma_f32_16x16x32_bf16 a[248:251], v[168:171], v[112:115], a[248:251]\n" \
"ds_read_b64_tr_b16 v[168:169], v34 offset:8192\n" \
"ds_read_b64_tr_b16 v[170:171], v34 offset:9216\n" \
"v_mfma_f32_16x16x32_bf16 a[248:251], v[164:167], v[108:111], a[248:251]\n" \
"ds_read_b64_tr_b16 v[164:165], v34 offset:0\n" \
"ds_read_b64_tr_b16 v[166:167], v34 offset:1024\n" \
"v_mfma_f32_16x16x32_bf16 a[252:255], v[172:175], v[108:111], a[252:255]\n" \
"ds_read_b64_tr_b16 v[172:173], v35 offset:0\n" \
"ds_read_b64_tr_b16 v[174:175], v35 offset:1024\n" \
"v_mfma_f32_16x16x32_bf16 a[252:255], v[176:179], v[112:115], a[252:255]\n" \
"ds_read_b64_tr_b16 v[176:177], v35 offset:8192\n" \
"s_waitcnt lgkmcnt(15)\n" \
"s_nop 0\n" \
"v_smfmac_f32_16x16x64_bf16 v[248:251], v[140:143], v[84:91], v50\n" \
"v_mfma_f32_16x16x32_bf16 v[180:183], v[128:131], v[88:91], v[180:183]\n" \
"ds_read_b64_tr_b16 v[178:179], v35 offset:9216\n" \
"v_bfe_u32 v44, v20, 2, 5\n" \
"v_mfma_f32_16x16x32_bf16 v[180:183], v[124:127], v[84:87], v[180:183]\n" \
"buffer_load_dword v44, v44, s[80:83], s39 offen offset:0 sc1\n" \
"v_mfma_f32_16x16x32_bf16 v[252:255], v[84:87], v[132:135], v[252:255]\n" \
"ds_read_b128 v[84:87], v48 offset:0\n" \
"ds_read_b128 v[108:111], v48 offset:6144\n" \
"v_mfma_f32_16x16x32_bf16 v[252:255], v[88:91], v[136:139], v[252:255]\n" \
"ds_read_b128 v[88:91], v49 offset:0\n" \
"ds_read_b128 v[112:115], v49 offset:6144\n" \
"v_smfmac_f32_16x16x64_bf16 v[196:199], v[140:143], v[52:59], v50\n" \
"v_mfma_f32_16x16x32_bf16 v[188:191], v[128:131], v[56:59], v[188:191]\n" \
"v_cvt_pk_bf16_f32 v180, v180, v181\n" \
"v_cvt_pk_bf16_f32 v181, v182, v183\n" \
"v_mfma_f32_16x16x32_bf16 v[188:191], v[124:127], v[52:55], v[188:191]\n" \
"v_add_f32_e64 v249, v249, v251\n" \
"v_cvt_pk_bf16_f32 v182, v249, 0\n" \
"v_mfma_f32_16x16x32_bf16 v[184:187], v[116:119], v[52:55], v[184:187]\n" \
"v_mov_b32_e32 v183, 0x3f80\n" \
"v_add_f32_e64 v196, v196, v198\n" \
"v_mfma_f32_16x16x32_bf16 v[184:187], v[120:123], v[56:59], v[184:187]\n" \
"v_mfma_f32_16x16x16_bf16 v[248:251], v[182:183], v[46:47], 0\n" \
"v_add_f32_e64 v197, v197, v199\n" \
"v_cvt_pk_bf16_f32 v188, v188, v189\n" \
"v_mfma_f32_16x16x32_bf16 v[192:195], v[56:59], v[136:139], v[192:195]\n" \
"v_cvt_pk_bf16_f32 v189, v190, v191\n" \
"v_cvt_pk_bf16_f32 v190, v197, 0\n" \
"v_mfma_f32_16x16x32_bf16 v[192:195], v[52:55], v[132:135], v[192:195]\n" \
"v_mov_b32_e32 v191, 0x3f80\n" \
"v_cvt_pk_bf16_f32 v184, v184, v185\n" \
"v_smfmac_f32_16x16x64_bf16 v[212:215], v[140:143], v[60:67], v50\n" \
"v_mfma_f32_16x16x32_bf16 v[204:207], v[124:127], v[60:63], v[204:207]\n" \
"v_cvt_pk_bf16_f32 v185, v186, v187\n" \
"v_cndmask_b32_e64 v186, 0, v196, s[78:79]\n" \
"v_mfma_f32_16x16x32_bf16 v[204:207], v[128:131], v[64:67], v[204:207]\n" \
"v_mov_b32_e64 v187, 0\n" \
"v_cvt_pk_bf16_f32 v186, v186, 0\n" \
"v_mfma_f32_16x16x32_bf16 v[200:203], v[120:123], v[64:67], v[200:203]\n" \
"v_mfma_f32_16x16x32_bf16 v[200:203], v[116:119], v[60:63], v[200:203]\n" \
"v_add_f32_e32 v212, v212, v214\n" \
"v_add_f32_e32 v213, v213, v215\n" \
"v_mfma_f32_16x16x32_bf16 v[208:211], v[60:63], v[132:135], v[208:211]\n" \
"v_mfma_f32_16x16x32_bf16 v[208:211], v[64:67], v[136:139], v[208:211]\n" \
"v_cvt_pk_bf16_f32 v204, v204, v205\n" \
"v_cvt_pk_bf16_f32 v205, v206, v207\n" \
"v_smfmac_f32_16x16x64_bf16 v[228:231], v[140:143], v[68:75], v50\n" \
"v_mfma_f32_16x16x32_bf16 v[220:223], v[128:131], v[72:75], v[220:223]\n" \
"v_cvt_pk_bf16_f32 v200, v200, v201\n" \
"v_cvt_pk_bf16_f32 v201, v202, v203\n" \
"v_mfma_f32_16x16x32_bf16 v[220:223], v[124:127], v[68:71], v[220:223]\n" \
"v_cndmask_b32_e64 v202, 0, v212, s[78:79]\n" \
"v_cvt_pk_bf16_f32 v206, v213, 0\n" \
"v_mfma_f32_16x16x32_bf16 v[216:219], v[116:119], v[68:71], v[216:219]\n" \
"v_mov_b32_e32 v207, 0x3f80\n" \
"v_add_f32_e64 v228, v228, v230\n" \
"v_mfma_f32_16x16x32_bf16 v[216:219], v[120:123], v[72:75], v[216:219]\n" \
"v_add_f32_e32 v229, v229, v231\n" \
"v_mov_b32_e32 v203, 0\n" \
"v_mfma_f32_16x16x32_bf16 v[224:227], v[72:75], v[136:139], v[224:227]\n" \
"v_cvt_pk_bf16_f32 v220, v220, v221\n" \
"v_cvt_pk_bf16_f32 v221, v222, v223\n" \
"v_mfma_f32_16x16x32_bf16 v[224:227], v[68:71], v[132:135], v[224:227]\n" \
"v_cvt_pk_bf16_f32 v202, v202, 0\n" \
"v_cvt_pk_bf16_f32 v222, v229, 0\n" \
"v_smfmac_f32_16x16x64_bf16 v[244:247], v[140:143], v[76:83], v50\n" \
"v_mfma_f32_16x16x32_bf16 v[236:239], v[124:127], v[76:79], v[236:239]\n" \
"v_cvt_pk_bf16_f32 v216, v216, v217\n" \
"v_cvt_pk_bf16_f32 v217, v218, v219\n" \
"v_mfma_f32_16x16x32_bf16 v[236:239], v[128:131], v[80:83], v[236:239]\n" \
"v_cndmask_b32_e64 v218, 0, v228, s[78:79]\n" \
"v_cvt_pk_bf16_f32 v126, v192, v193\n" \
"v_mfma_f32_16x16x32_bf16 v[232:235], v[120:123], v[80:83], v[232:235]\n" \
"v_cvt_pk_bf16_f32 v127, v194, v195\n" \
"v_add_f32_e64 v244, v244, v246\n" \
"v_mfma_f32_16x16x32_bf16 v[232:235], v[116:119], v[76:79], v[232:235]\n" \
"v_add_f32_e64 v245, v245, v247\n" \
"v_cvt_pk_bf16_f32 v128, v208, v209\n" \
"v_mfma_f32_16x16x32_bf16 v[240:243], v[76:79], v[132:135], v[240:243]\n" \
"v_cvt_pk_bf16_f32 v236, v236, v237\n" \
"v_cvt_pk_bf16_f32 v237, v238, v239\n" \
"v_mfma_f32_16x16x32_bf16 v[240:243], v[80:83], v[136:139], v[240:243]\n" \
"v_cvt_pk_bf16_f32 v129, v210, v211\n" \
"v_mov_b32_e32 v223, 0x3f80\n" \
"s_waitcnt lgkmcnt(0)\n" \
"s_barrier\n" \
"v_mfma_f32_16x16x32_bf16 a[128:131], v[152:155], v[56:59], a[128:131]\n" \
"v_cvt_pk_bf16_f32 v232, v232, v233\n" \
"v_cvt_pk_bf16_f32 v233, v234, v235\n" \
"v_mfma_f32_16x16x32_bf16 a[128:131], v[148:151], v[52:55], a[128:131]\n" \
"v_cndmask_b32_e64 v234, 0, v244, s[78:79]\n" \
"v_mov_b32_e64 v219, 0\n" \
"v_mfma_f32_16x16x32_bf16 a[132:135], v[156:159], v[52:55], a[132:135]\n" \
"v_cvt_pk_bf16_f32 v218, v218, 0\n" \
"v_cvt_pk_bf16_f32 v130, v224, v225\n" \
"v_mfma_f32_16x16x32_bf16 a[132:135], v[160:163], v[56:59], a[132:135]\n" \
"v_cvt_pk_bf16_f32 v131, v226, v227\n" \
"v_cvt_pk_bf16_f32 v238, v245, 0\n" \
"v_mfma_f32_16x16x32_bf16 a[136:139], v[168:171], v[56:59], a[136:139]\n" \
"v_mov_b32_e32 v239, 0x3f80\n" \
"v_mov_b32_e64 v235, 0\n" \
"v_mfma_f32_16x16x32_bf16 a[136:139], v[164:167], v[52:55], a[136:139]\n" \
"v_cvt_pk_bf16_f32 v234, v234, 0\n" \
"v_cvt_pk_bf16_f32 v132, v240, v241\n" \
"v_mfma_f32_16x16x32_bf16 a[140:143], v[172:175], v[52:55], a[140:143]\n" \
"v_cvt_pk_bf16_f32 v133, v242, v243\n" \
"v_cvt_pk_bf16_f32 v134, v248, v249\n" \
"v_mfma_f32_16x16x32_bf16 a[140:143], v[176:179], v[56:59], a[140:143]\n" \
"v_mfma_f32_16x16x32_bf16 v[116:119], v[180:183], v[184:187], 0\n" \
"v_mfma_f32_16x16x32_bf16 v[192:195], v[188:191], v[184:187], 0\n" \
"v_mfma_f32_16x16x32_bf16 v[208:211], v[204:207], v[200:203], 0\n" \
"v_mfma_f32_16x16x32_bf16 v[120:123], v[188:191], v[200:203], 0\n" \
"s_waitcnt vmcnt(0)\n" \
"v_cmp_ne_u32_e32 vcc, %[tag], v44\n" \
"v_mfma_f32_16x16x32_bf16 a[160:163], v[152:155], v[64:67], a[160:163]\n" \
"v_mfma_f32_16x16x32_bf16 v[224:227], v[220:223], v[216:219], 0\n" \
"v_mfma_f32_16x16x32_bf16 v[36:39], v[204:207], v[216:219], 0\n" \
"v_mfma_f32_16x16x32_bf16 v[240:243], v[236:239], v[232:235], 0\n" \
"v_mfma_f32_16x16x32_bf16 v[48:51], v[220:223], v[232:235], 0\n" \
"s_cbranch_vccnz L_pa_%=\n" \
"L_ra_%=:\n" \
"v_cndmask_b32_e64 v116, v116, v117, s[64:65]\n" \
"buffer_load_dwordx4 v20, s[80:83], s73 offen offset:0 sc1 lds\n" \
"v_mfma_f32_16x16x32_bf16 a[160:163], v[148:151], v[60:63], a[160:163]\n" \
"v_cndmask_b32_e64 v118, v118, v119, s[64:65]\n" \
"v_cndmask_b32_e64 v192, v192, v193, s[64:65]\n" \
"v_mfma_f32_16x16x32_bf16 a[164:167], v[156:159], v[60:63], a[164:167]\n" \
"v_add_f32_e32 v116, s49, v116\n" \
"v_cndmask_b32_e64 v194, v194, v195, s[64:65]\n" \
"v_mfma_f32_16x16x32_bf16 a[164:167], v[160:163], v[64:67], a[164:167]\n" \
"v_add_f32_e64 v118, s49, v118\n" \
"v_cndmask_b32_e64 v208, v208, v209, s[64:65]\n" \
"v_mfma_f32_16x16x32_bf16 a[168:171], v[168:171], v[64:67], a[168:171]\n" \
"buffer_load_dwordx4 v20, s[80:83], s73 offen offset:1024 sc1 lds\n" \
"v_mfma_f32_16x16x32_bf16 a[168:171], v[164:167], v[60:63], a[168:171]\n" \
"v_cndmask_b32_e64 v120, v120, v121, s[64:65]\n" \
"v_cndmask_b32_e64 v210, v210, v211, s[64:65]\n" \
"v_mfma_f32_16x16x32_bf16 a[172:175], v[172:175], v[60:63], a[172:175]\n" \
"v_cndmask_b32_e64 v122, v122, v123, s[64:65]\n" \
"v_cndmask_b32_e64 v224, v224, v225, s[64:65]\n" \
"v_mfma_f32_16x16x32_bf16 a[172:175], v[176:179], v[64:67], a[172:175]\n" \
"v_cndmask_b32_e64 v36, v36, v37, s[64:65]\n" \
"v_cndmask_b32_e64 v226, v226, v227, s[64:65]\n" \
"v_mfma_f32_16x16x32_bf16 a[192:195], v[152:155], v[72:75], a[192:195]\n" \
"buffer_load_dwordx4 v20, s[80:83], s73 offen offset:2048 sc1 lds\n" \
"v_mfma_f32_16x16x32_bf16 a[192:195], v[148:151], v[68:71], a[192:195]\n" \
"v_cndmask_b32_e64 v38, v38, v39, s[64:65]\n" \
"v_cndmask_b32_e64 v240, v240, v241, s[64:65]\n" \
"v_mfma_f32_16x16x32_bf16 a[196:199], v[156:159], v[68:71], a[196:199]\n" \
"v_cndmask_b32_e64 v48, v48, v49, s[64:65]\n" \
"v_cndmask_b32_e64 v242, v242, v243, s[64:65]\n" \
"v_mfma_f32_16x16x32_bf16 a[196:199], v[160:163], v[72:75], a[196:199]\n" \
"v_cndmask_b32_e64 v50, v50, v51, s[64:65]\n" \
"v_cndmask_b32_e64 v192, v116, v192, s[66:67]\n" \
"v_mfma_f32_16x16x32_bf16 a[200:203], v[168:171], v[72:75], a[200:203]\n" \
"buffer_load_dwordx4 v20, s[80:83], s73 offen offset:3072 sc1 lds\n" \
"v_mfma_f32_16x16x32_bf16 a[200:203], v[164:167], v[68:71], a[200:203]\n" \
"s_sub_u32 m0, %[mB], s53\n" \
"s_nop 0\n" \
"v_cndmask_b32_e64 v194, v118, v194, s[68:69]\n" \
"v_mfma_f32_16x16x32_bf16 a[204:207], v[172:175], v[68:71], a[204:207]\n" \
"v_cndmask_b32_e64 v208, v120, v208, s[66:67]\n" \
"v_cndmask_b32_e64 v210, v122, v210, s[68:69]\n" \
"v_mfma_f32_16x16x32_bf16 a[204:207], v[176:179], v[72:75], a[204:207]\n" \
"v_cndmask_b32_e64 v224, v36, v224, s[66:67]\n" \
"v_cndmask_b32_e64 v226, v38, v226, s[68:69]\n" \
"v_mfma_f32_16x16x32_bf16 a[224:227], v[152:155], v[80:83], a[224:227]\n" \
"buffer_load_dwordx4 v20, s[80:83], s74 offen offset:0 sc1 lds\n" \
"v_mfma_f32_16x16x32_bf16 a[224:227], v[148:151], v[76:79], a[224:227]\n" \
"v_cndmask_b32_e64 v240, v48, v240, s[66:67]\n" \
"v_cndmask_b32_e64 v242, v50, v242, s[68:69]\n" \
"v_mfma_f32_16x16x32_bf16 a[228:231], v[156:159], v[76:79], a[228:231]\n" \
"v_max_f32_e32 v193, v192, v194\n" \
"v_max_f32_e32 v209, v208, v210\n" \
"v_mfma_f32_16x16x32_bf16 a[228:231], v[160:163], v[80:83], a[228:231]\n" \
"v_max_f32_e32 v225, v224, v226\n" \
"v_max_f32_e32 v241, v240, v242\n" \
"v_mfma_f32_16x16x32_bf16 a[232:235], v[168:171], v[80:83], a[232:235]\n" \
"buffer_load_dwordx4 v20, s[80:83], s74 offen offset:1024 sc1 lds\n" \
"v_mfma_f32_16x16x32_bf16 a[232:235], v[164:167], v[76:79], a[232:235]\n" \
"v_permlane32_swap_b32_e32 v193, v225\n" \
"v_permlane32_swap_b32_e32 v209, v241\n" \
"v_mfma_f32_16x16x32_bf16 a[236:239], v[172:175], v[76:79], a[236:239]\n" \
"v_max_f32_e32 v193, v193, v225\n" \
"v_max_f32_e32 v209, v209, v241\n" \
"v_permlane32_swap_b32_e32 v196, v228\n" \
"v_permlane32_swap_b32_e32 v212, v244\n" \
"buffer_load_dwordx4 v20, s[80:83], s74 offen offset:2048 sc1 lds\n" \
"v_permlane16_swap_b32_e32 v193, v209\n" \
"v_permlane16_swap_b32_e32 v228, v244\n" \
"v_mfma_f32_16x16x32_bf16 a[236:239], v[176:179], v[80:83], a[236:239]\n" \
"v_max_f32_e32 v193, v193, v209\n" \
"v_mul_f32_e32 v193, s72, v193\n" \
"v_mfma_f32_16x16x32_bf16 a[144:147], v[152:155], v[88:91], a[144:147]\n" \
"v_mul_f32_e32 v45, 0xbfb8aa3b, v228\n" \
"v_mov_b32_e64 v209, v193\n" \
"v_mfma_f32_16x16x16_bf16 v[196:199], v[190:191], v[46:47], 0\n" \
"buffer_load_dwordx4 v20, s[80:83], s74 offen offset:3072 sc1 lds\n" \
"v_mfma_f32_16x16x32_bf16 a[144:147], v[148:151], v[84:87], a[144:147]\n" \
"s_sub_u32 m0, %[mB], 0x4000\n" \
"v_cvt_pk_bf16_f32 v135, v250, v251\n" \
"v_mfma_f32_16x16x16_bf16 v[212:215], v[206:207], v[46:47], 0\n" \
"v_mfma_f32_16x16x16_bf16 v[228:231], v[222:223], v[46:47], 0\n" \
"v_permlane16_swap_b32_e32 v193, v209\n" \
"v_mfma_f32_16x16x16_bf16 v[244:247], v[238:239], v[46:47], 0\n" \
"buffer_load_dwordx4 v20, s[80:83], s75 offen offset:0 sc1 lds\n" \
"v_mfma_f32_16x16x32_bf16 a[148:151], v[156:159], v[84:87], a[148:151]\n" \
"v_mov_b32_e32 v225, v193\n" \
"v_mov_b32_e64 v241, v209\n" \
"v_mfma_f32_16x16x32_bf16 a[148:151], v[160:163], v[88:91], a[148:151]\n" \
"v_permlane32_swap_b32_e32 v193, v225\n" \
"v_permlane32_swap_b32_e32 v209, v241\n" \
"v_mfma_f32_16x16x32_bf16 a[152:155], v[168:171], v[88:91], a[152:155]\n" \
"v_fma_f32 v192, v192, s72, -v193\n" \
"v_fma_f32 v194, v194, s72, -v193\n" \
"buffer_load_dwordx4 v20, s[80:83], s75 offen offset:1024 sc1 lds\n" \
"v_mfma_f32_16x16x32_bf16 a[152:155], v[164:167], v[84:87], a[152:155]\n" \
"v_fma_f32 v208, v208, s72, -v209\n" \
"v_fma_f32 v210, v210, s72, -v209\n" \
"v_exp_f32_e32 v192, v192\n" \
"v_exp_f32_e32 v194, v194\n" \
"v_exp_f32_e32 v208, v208\n" \
"v_exp_f32_e32 v210, v210\n" \
"v_mfma_f32_16x16x32_bf16 a[156:159], v[172:175], v[84:87], a[156:159]\n" \
"buffer_load_dwordx4 v20, s[80:83], s75 offen offset:2048 sc1 lds\n" \
"v_mfma_f32_16x16x32_bf16 a[156:159], v[176:179], v[88:91], a[156:159]\n" \
"v_fma_f32 v224, v224, s72, -v225\n" \
"v_fma_f32 v226, v226, s72, -v225\n" \
"v_exp_f32_e32 v224, v224\n" \
"v_exp_f32_e32 v226, v226\n" \
"v_mfma_f32_16x16x32_bf16 a[176:179], v[152:155], v[96:99], a[176:179]\n" \
"v_fma_f32 v240, v240, s72, -v241\n" \
"v_fma_f32 v242, v242, s72, -v241\n" \
"v_mfma_f32_16x16x32_bf16 a[176:179], v[148:151], v[92:95], a[176:179]\n" \
"buffer_load_dwordx4 v20, s[80:83], s75 offen offset:3072 sc1 lds\n" \
"v_mfma_f32_16x16x32_bf16 a[180:183], v[156:159], v[92:95], a[180:183]\n" \
"s_mov_b32 m0, s55\n" \
"v_cvt_pk_bf16_f32 v192, v192, v194\n" \
"v_mfma_f32_16x16x32_bf16 a[180:183], v[160:163], v[96:99], a[180:183]\n" \
"v_cvt_pk_bf16_f32 v208, v208, v210\n" \
"v_cvt_pk_bf16_f32 v136, v196, v197\n" \
"v_exp_f32_e32 v240, v240\n" \
"v_exp_f32_e64 v242, v242\n" \
"v_mfma_f32_16x16x32_bf16 a[184:187], v[168:171], v[96:99], a[184:187]\n" \
"buffer_load_dwordx4 v20, s[80:83], s76 offen offset:0 sc1 lds\n" \
"v_mfma_f32_16x16x32_bf16 a[184:187], v[164:167], v[92:95], a[184:187]\n" \
"v_cvt_pk_bf16_f32 v137, v198, v199\n" \
"v_cvt_pk_bf16_f32 v138, v212, v213\n" \
"v_mfma_f32_16x16x32_bf16 a[188:191], v[172:175], v[92:95], a[188:191]\n" \
"v_cvt_pk_bf16_f32 v139, v214, v215\n" \
"v_perm_b32 v184, v192, v192, v40\n" \
"v_mfma_f32_16x16x32_bf16 a[188:191], v[176:179], v[96:99], a[188:191]\n" \
"v_perm_b32 v185, v192, v192, v41\n" \
"v_perm_b32 v186, v192, v192, v42\n" \
"v_perm_b32 v187, v192, v192, v43\n" \
"buffer_load_dwordx4 v20, s[80:83], s76 offen offset:1024 sc1 lds\n" \
"v_perm_b32 v200, v208, v208, v40\n" \
"v_perm_b32 v201, v208, v208, v41\n" \
"v_perm_b32 v202, v208, v208, v42\n" \
"v_perm_b32 v203, v208, v208, v43\n" \
"v_cvt_pk_bf16_f32 v224, v224, v226\n" \
"v_cvt_pk_bf16_f32 v140, v228, v229\n" \
"v_cvt_pk_bf16_f32 v141, v230, v231\n" \
"v_mfma_f32_16x16x32_bf16 v[116:119], v[134:137], v[184:187], 0\n" \
"v_mfma_f32_16x16x32_bf16 v[120:123], v[136:139], v[200:203], 0\n" \
"v_perm_b32 v216, v224, v224, v40\n" \
"v_perm_b32 v217, v224, v224, v41\n" \
"v_perm_b32 v218, v224, v224, v42\n" \
"v_perm_b32 v219, v224, v224, v43\n" \
"v_cvt_pk_bf16_f32 v240, v240, v242\n" \
"v_cvt_pk_bf16_f32 v142, v244, v245\n" \
"L_pf9:\n" \
"s_cbranch_execz L_pf13\n" \
"s_nop 0\n" \
"v_cvt_pk_bf16_f32 v143, v246, v247\n" \
"v_mfma_f32_16x16x32_bf16 v[36:39], v[138:141], v[216:219], 0\n" \
"v_perm_b32 v232, v240, v240, v40\n" \
"v_perm_b32 v233, v240, v240, v41\n" \
"v_perm_b32 v234, v240, v240, v42\n" \
"v_perm_b32 v235, v240, v240, v43\n" \
"v_exp_f32_e64 v45, v45\n" \
"v_mov_b32_dpp v118, v122 quad_perm:[0,1,2,3] row_mask:0x2 bank_mask:0xf\n" \
"v_mfma_f32_16x16x32_bf16 v[48:51], v[140:143], v[232:235], 0\n" \
"v_cvt_pk_bf16_f32 v124, v252, v253\n" \
"v_cvt_pk_bf16_f32 v125, v254, v255\n" \
"v_mov_b32_dpp v118, v38 quad_perm:[0,1,2,3] row_mask:0x4 bank_mask:0xf\n" \
"v_add_f32_e64 v45, 1.0, v45\n" \
"v_mfma_f32_16x16x32_bf16 v[192:195], v[124:127], v[184:187], 0\n" \
"v_mfma_f32_16x16x32_bf16 v[224:227], v[128:131], v[216:219], 0\n" \
"v_rcp_f32_e64 v45, v45\n" \
"v_mov_b32_dpp v116, v120 quad_perm:[0,1,2,3] row_mask:0x2 bank_mask:0xf\n" \
"v_mov_b32_dpp v118, v50 quad_perm:[0,1,2,3] row_mask:0x8 bank_mask:0xf\n" \
"v_mov_b32_dpp v117, v121 quad_perm:[0,1,2,3] row_mask:0x2 bank_mask:0xf\n" \
"v_rcp_f32_e64 v118, v118\n" \
"v_mfma_f32_16x16x32_bf16 v[208:211], v[126:129], v[200:203], 0\n" \
"v_mfma_f32_16x16x32_bf16 v[240:243], v[130:133], v[232:235], 0\n" \
"v_mov_b32_dpp v116, v36 quad_perm:[0,1,2,3] row_mask:0x4 bank_mask:0xf\n" \
"v_mov_b32_dpp v117, v37 quad_perm:[0,1,2,3] row_mask:0x4 bank_mask:0xf\n" \
"v_mul_f32_e64 v118, v45, v118\n" \
"v_mov_b32_dpp v116, v48 quad_perm:[0,1,2,3] row_mask:0x8 bank_mask:0xf\n" \
"v_mov_b32_dpp v117, v49 quad_perm:[0,1,2,3] row_mask:0x8 bank_mask:0xf\n" \
"v_mul_f32_e32 v116, v118, v116\n" \
"v_mul_f32_e32 v117, v118, v117\n" \
"buffer_store_dwordx4 a[12:15], v20, s[80:83], s89 offen offset:1024 sc1\n" \
"s_add_u32 s89, s45, 0x0\n" \
"v_mov_b32_e32 v122, v118\n" \
"v_cvt_pk_bf16_f32 v116, v116, v117\n" \
"v_accvgpr_read_b32 v52, a128\n" \
"v_permlane16_swap_b32_e32 v118, v122\n" \
"v_mov_b32_e32 v38, v118\n" \
"v_mov_b32_e64 v50, v122\n" \
"v_accvgpr_read_b32 v53, a129\n" \
"v_permlane32_swap_b32_e32 v118, v38\n" \
"v_permlane32_swap_b32_e32 v122, v50\n" \
"v_mul_f32_e32 v192, v118, v192\n" \
"v_mul_f32_e32 v193, v118, v193\n" \
"v_mul_f32_e32 v224, v38, v224\n" \
"v_mul_f32_e32 v225, v38, v225\n" \
"v_mul_f32_e32 v194, v118, v194\n" \
"v_mul_f32_e32 v195, v118, v195\n" \
"v_cvt_pk_bf16_f32 v192, v192, v193\n" \
"v_mul_f32_e32 v226, v38, v226\n" \
"v_mul_f32_e32 v227, v38, v227\n" \
"v_cvt_pk_bf16_f32 v224, v224, v225\n" \
"v_cvt_pk_bf16_f32 v193, v194, v195\n" \
"v_mul_f32_e32 v208, v122, v208\n" \
"v_mul_f32_e32 v209, v122, v209\n" \
"v_mul_f32_e32 v210, v122, v210\n" \
"v_mul_f32_e32 v211, v122, v211\n" \
"v_cvt_pk_bf16_f32 v225, v226, v227\n" \
"v_mul_f32_e32 v240, v50, v240\n" \
"v_mul_f32_e32 v241, v50, v241\n" \
"v_mul_f32_e32 v242, v50, v242\n" \
"v_mul_f32_e32 v243, v50, v243\n" \
"v_cvt_pk_bf16_f32 v194, v208, v209\n" \
"v_cvt_pk_bf16_f32 v195, v210, v211\n" \
"v_cvt_pk_bf16_f32 v226, v240, v241\n" \
"v_cvt_pk_bf16_f32 v227, v242, v243\n" \
"v_permlane32_swap_b32_e32 v192, v194\n" \
"v_permlane32_swap_b32_e32 v193, v195\n" \
"v_permlane32_swap_b32_e32 v224, v226\n" \
"v_permlane32_swap_b32_e32 v225, v227\n" \
"v_accvgpr_read_b32 v54, a130\n" \
"v_accvgpr_read_b32 v55, a131\n" \
"v_pk_mul_f32 v[52:53], v[52:53], s[70:71]\n" \
"v_accvgpr_read_b32 v56, a132\n" \
"v_accvgpr_read_b32 v57, a133\n" \
"v_accvgpr_read_b32 v58, a134\n" \
"v_accvgpr_read_b32 v59, a135\n" \
"v_pk_mul_f32 v[54:55], v[54:55], s[70:71]\n" \
"v_cvt_pk_bf16_f32 v52, v52, v53\n" \
"v_pk_mul_f32 v[56:57], v[56:57], s[70:71]\n" \
"v_pk_mul_f32 v[58:59], v[58:59], s[70:71]\n" \
"v_cvt_pk_bf16_f32 v53, v54, v55\n" \
"v_cvt_pk_bf16_f32 v54, v56, v57\n" \
"v_cvt_pk_bf16_f32 v55, v58, v59\n" \
"ds_write_b128 v27, v[52:55] offset:0\n" \
"buffer_store_dwordx4 a[40:43], v20, s[80:83], s88 offen offset:0 sc1\n" \
"s_add_u32 s88, s45, 0x0\n" \
"s_nop 0\n" \
"buffer_store_dword v116, v25, s[80:83], s51 offen sc1\n" \
"buffer_store_dwordx4 v[192:195], v24, s[80:83], s85 offen sc1\n" \
"buffer_store_dwordx4 v[224:227], v24, s[80:83], s87 offen sc1\n" \
"v_accvgpr_read_b32 v76, a224\n" \
"v_accvgpr_read_b32 v77, a225\n" \
"v_accvgpr_read_b32 v78, a226\n" \
"v_accvgpr_read_b32 v79, a227\n" \
"v_pk_mul_f32 v[76:77], v[76:77], s[70:71]\n" \
"v_accvgpr_read_b32 v52, a136\n" \
"v_accvgpr_read_b32 v53, a137\n" \
"v_accvgpr_read_b32 v80, a228\n" \
"v_accvgpr_read_b32 v81, a229\n" \
"v_accvgpr_read_b32 v82, a230\n" \
"v_accvgpr_read_b32 v83, a231\n" \
"v_pk_mul_f32 v[78:79], v[78:79], s[70:71]\n" \
"v_cvt_pk_bf16_f32 v76, v76, v77\n" \
"v_accvgpr_read_b32 v54, a138\n" \
"v_accvgpr_read_b32 v55, a139\n" \
"v_pk_mul_f32 v[52:53], v[52:53], s[70:71]\n" \
"v_pk_mul_f32 v[80:81], v[80:81], s[70:71]\n" \
"v_pk_mul_f32 v[82:83], v[82:83], s[70:71]\n" \
"v_cvt_pk_bf16_f32 v77, v78, v79\n" \
"v_accvgpr_read_b32 v56, a140\n" \
"v_accvgpr_read_b32 v57, a141\n" \
"v_accvgpr_read_b32 v58, a142\n" \
"v_accvgpr_read_b32 v59, a143\n" \
"v_pk_mul_f32 v[54:55], v[54:55], s[70:71]\n" \
"v_cvt_pk_bf16_f32 v52, v52, v53\n" \
"v_cvt_pk_bf16_f32 v78, v80, v81\n" \
"v_cvt_pk_bf16_f32 v79, v82, v83\n" \
"ds_write_b128 v27, v[76:79] offset:12288\n" \
"v_pk_mul_f32 v[56:57], v[56:57], s[70:71]\n" \
"v_pk_mul_f32 v[58:59], v[58:59], s[70:71]\n" \
"v_cvt_pk_bf16_f32 v53, v54, v55\n" \
"v_cvt_pk_bf16_f32 v54, v56, v57\n" \
"v_cvt_pk_bf16_f32 v55, v58, v59\n" \
"ds_write_b128 v27, v[52:55] offset:1024\n" \
"v_accvgpr_read_b32 v76, a208\n" \
"v_accvgpr_read_b32 v77, a209\n" \
"v_accvgpr_read_b32 v78, a210\n" \
"v_accvgpr_read_b32 v79, a211\n" \
"v_accvgpr_read_b32 v52, a168\n" \
"v_accvgpr_read_b32 v53, a169\n" \
"v_mfma_f32_16x16x32_bf16 v[76:79], v[152:155], v[104:107], v[76:79]\n" \
"v_accvgpr_read_b32 v54, a170\n" \
"v_accvgpr_read_b32 v55, a171\n" \
"v_mfma_f32_16x16x32_bf16 v[76:79], v[148:151], v[100:103], v[76:79]\n" \
"v_accvgpr_read_b32 v56, a172\n" \
"v_accvgpr_read_b32 v57, a173\n" \
"v_accvgpr_read_b32 v58, a174\n" \
"v_accvgpr_read_b32 v59, a175\n" \
"v_pk_mul_f32 v[52:53], v[52:53], s[70:71]\n" \
"v_pk_mul_f32 v[54:55], v[54:55], s[70:71]\n" \
"v_cvt_pk_bf16_f32 v52, v52, v53\n" \
"v_pk_mul_f32 v[56:57], v[56:57], s[70:71]\n" \
"v_pk_mul_f32 v[58:59], v[58:59], s[70:71]\n" \
"buffer_store_dwordx4 a[44:47], v20, s[80:83], s89 offen offset:1024 sc1\n" \
"s_add_u32 s89, s45, 0x0\n" \
"s_nop 0\n" \
"v_cvt_pk_bf16_f32 v53, v54, v55\n" \
"v_cvt_pk_bf16_f32 v54, v56, v57\n" \
"v_cvt_pk_bf16_f32 v55, v58, v59\n" \
"ds_write_b128 v27, v[52:55] offset:5120\n" \
"v_accvgpr_read_b32 v80, a212\n" \
"v_accvgpr_read_b32 v81, a213\n" \
"v_accvgpr_read_b32 v82, a214\n" \
"v_accvgpr_read_b32 v83, a215\n" \
"v_accvgpr_read_b32 v52, a232\n" \
"v_accvgpr_read_b32 v53, a233\n" \
"v_mfma_f32_16x16x32_bf16 v[80:83], v[156:159], v[100:103], v[80:83]\n" \
"v_accvgpr_read_b32 v54, a234\n" \
"v_accvgpr_read_b32 v55, a235\n" \
"v_mfma_f32_16x16x32_bf16 v[80:83], v[160:163], v[104:107], v[80:83]\n" \
"v_accvgpr_read_b32 v56, a236\n" \
"v_accvgpr_read_b32 v57, a237\n" \
"v_accvgpr_read_b32 v58, a238\n" \
"v_accvgpr_read_b32 v59, a239\n" \
"v_pk_mul_f32 v[52:53], v[52:53], s[70:71]\n" \
"v_pk_mul_f32 v[54:55], v[54:55], s[70:71]\n" \
"v_cvt_pk_bf16_f32 v52, v52, v53\n" \
"v_pk_mul_f32 v[56:57], v[56:57], s[70:71]\n" \
"v_pk_mul_f32 v[58:59], v[58:59], s[70:71]\n" \
"v_cvt_pk_bf16_f32 v53, v54, v55\n" \
"v_cvt_pk_bf16_f32 v54, v56, v57\n" \
"v_cvt_pk_bf16_f32 v55, v58, v59\n" \
"ds_write_b128 v27, v[52:55] offset:13312\n" \
"v_accvgpr_read_b32 v52, a216\n" \
"v_accvgpr_read_b32 v53, a217\n" \
"v_accvgpr_read_b32 v54, a218\n" \
"v_accvgpr_read_b32 v55, a219\n" \
"v_accvgpr_read_b32 v60, a160\n" \
"v_accvgpr_read_b32 v61, a161\n" \
"v_mfma_f32_16x16x32_bf16 v[52:55], v[168:171], v[104:107], v[52:55]\n" \
"v_accvgpr_read_b32 v62, a162\n" \
"v_accvgpr_read_b32 v63, a163\n" \
"v_mfma_f32_16x16x32_bf16 v[52:55], v[164:167], v[100:103], v[52:55]\n" \
"v_accvgpr_read_b32 v56, a220\n" \
"v_accvgpr_read_b32 v57, a221\n" \
"v_accvgpr_read_b32 v58, a222\n" \
"v_accvgpr_read_b32 v59, a223\n" \
"v_pk_mul_f32 v[60:61], v[60:61], s[70:71]\n" \
"v_accvgpr_read_b32 v64, a164\n" \
"v_mfma_f32_16x16x32_bf16 v[56:59], v[172:175], v[100:103], v[56:59]\n" \
"v_accvgpr_read_b32 v65, a165\n" \
"v_accvgpr_read_b32 v66, a166\n" \
"v_mfma_f32_16x16x32_bf16 v[56:59], v[176:179], v[104:107], v[56:59]\n" \
"v_accvgpr_read_b32 v67, a167\n" \
"buffer_store_dwordx4 a[72:75], v20, s[80:83], s88 offen offset:2048 sc1\n" \
"s_add_u32 s88, s45, 0x1000\n" \
"v_pk_mul_f32 v[62:63], v[62:63], s[70:71]\n" \
"v_cvt_pk_bf16_f32 v60, v60, v61\n" \
"v_pk_mul_f32 v[64:65], v[64:65], s[70:71]\n" \
"v_pk_mul_f32 v[66:67], v[66:67], s[70:71]\n" \
"v_cvt_pk_bf16_f32 v61, v62, v63\n" \
"v_cvt_pk_bf16_f32 v62, v64, v65\n" \
"v_cvt_pk_bf16_f32 v63, v66, v67\n" \
"ds_write_b128 v27, v[60:63] offset:4096\n" \
"v_accvgpr_read_b32 v68, a192\n" \
"v_accvgpr_read_b32 v69, a193\n" \
"v_accvgpr_read_b32 v60, a200\n" \
"v_accvgpr_read_b32 v61, a201\n" \
"v_accvgpr_read_b32 v70, a194\n" \
"v_accvgpr_read_b32 v71, a195\n" \
"v_pk_mul_f32 v[68:69], v[68:69], s[70:71]\n" \
"v_accvgpr_read_b32 v62, a202\n" \
"v_accvgpr_read_b32 v63, a203\n" \
"v_pk_mul_f32 v[60:61], v[60:61], s[70:71]\n" \
"v_accvgpr_read_b32 v72, a196\n" \
"v_accvgpr_read_b32 v73, a197\n" \
"v_accvgpr_read_b32 v74, a198\n" \
"v_accvgpr_read_b32 v75, a199\n" \
"v_pk_mul_f32 v[70:71], v[70:71], s[70:71]\n" \
"v_cvt_pk_bf16_f32 v68, v68, v69\n" \
"v_accvgpr_read_b32 v64, a204\n" \
"v_accvgpr_read_b32 v65, a205\n" \
"v_accvgpr_read_b32 v66, a206\n" \
"v_accvgpr_read_b32 v67, a207\n" \
"v_pk_mul_f32 v[62:63], v[62:63], s[70:71]\n" \
"v_cvt_pk_bf16_f32 v60, v60, v61\n" \
"v_accvgpr_read_b32 v100, a240\n" \
"v_accvgpr_read_b32 v101, a241\n" \
"v_accvgpr_read_b32 v102, a242\n" \
"v_accvgpr_read_b32 v103, a243\n" \
"v_pk_mul_f32 v[72:73], v[72:73], s[70:71]\n" \
"v_pk_mul_f32 v[74:75], v[74:75], s[70:71]\n" \
"v_mfma_f32_16x16x32_bf16 v[100:103], v[152:155], v[112:115], v[100:103]\n" \
"v_cvt_pk_bf16_f32 v69, v70, v71\n" \
"v_cvt_pk_bf16_f32 v61, v62, v63\n" \
"v_mfma_f32_16x16x32_bf16 v[100:103], v[148:151], v[108:111], v[100:103]\n" \
"v_cvt_pk_bf16_f32 v70, v72, v73\n" \
"v_cvt_pk_bf16_f32 v71, v74, v75\n" \
"ds_write_b128 v27, v[68:71] offset:8192\n" \
"v_pk_mul_f32 v[64:65], v[64:65], s[70:71]\n" \
"v_pk_mul_f32 v[66:67], v[66:67], s[70:71]\n" \
"v_cvt_pk_bf16_f32 v62, v64, v65\n" \
"v_cvt_pk_bf16_f32 v63, v66, v67\n" \
"ds_write_b128 v27, v[60:63] offset:9216\n" \
"v_accvgpr_read_b32 v84, a144\n" \
"buffer_store_dwordx4 a[76:79], v20, s[80:83], s89 offen offset:3072 sc1\n" \
"s_add_u32 s89, s45, 0x1000\n" \
"v_accvgpr_read_b32 v85, a145\n" \
"v_accvgpr_read_b32 v68, a152\n" \
"v_accvgpr_read_b32 v69, a153\n" \
"v_accvgpr_read_b32 v92, a176\n" \
"v_accvgpr_read_b32 v93, a177\n" \
"v_accvgpr_read_b32 v60, a184\n" \
"v_accvgpr_read_b32 v61, a185\n" \
"v_accvgpr_read_b32 v86, a146\n" \
"v_accvgpr_read_b32 v87, a147\n" \
"v_pk_mul_f32 v[84:85], v[84:85], s[70:71]\n" \
"v_accvgpr_read_b32 v70, a154\n" \
"v_accvgpr_read_b32 v71, a155\n" \
"v_pk_mul_f32 v[68:69], v[68:69], s[70:71]\n" \
"v_accvgpr_read_b32 v94, a178\n" \
"v_accvgpr_read_b32 v95, a179\n" \
"v_pk_mul_f32 v[92:93], v[92:93], s[70:71]\n" \
"v_accvgpr_read_b32 v62, a186\n" \
"v_accvgpr_read_b32 v63, a187\n" \
"v_pk_mul_f32 v[60:61], v[60:61], s[70:71]\n" \
"v_pk_mul_f32 v[76:77], v[76:77], s[70:71]\n" \
"v_pk_mul_f32 v[52:53], v[52:53], s[70:71]\n" \
"v_pk_mul_f32 v[100:101], v[100:101], s[70:71]\n" \
"v_accvgpr_read_b32 v88, a148\n" \
"v_accvgpr_read_b32 v89, a149\n" \
"v_accvgpr_read_b32 v90, a150\n" \
"v_accvgpr_read_b32 v91, a151\n" \
"v_pk_mul_f32 v[86:87], v[86:87], s[70:71]\n" \
"v_cvt_pk_bf16_f32 v84, v84, v85\n" \
"v_accvgpr_read_b32 v72, a156\n" \
"v_accvgpr_read_b32 v73, a157\n" \
"v_accvgpr_read_b32 v74, a158\n" \
"v_accvgpr_read_b32 v75, a159\n" \
"v_pk_mul_f32 v[70:71], v[70:71], s[70:71]\n" \
"v_cvt_pk_bf16_f32 v68, v68, v69\n" \
"v_accvgpr_read_b32 v96, a180\n" \
"v_accvgpr_read_b32 v97, a181\n" \
"v_accvgpr_read_b32 v98, a182\n" \
"v_accvgpr_read_b32 v99, a183\n" \
"v_pk_mul_f32 v[94:95], v[94:95], s[70:71]\n" \
"v_cvt_pk_bf16_f32 v92, v92, v93\n" \
"v_accvgpr_read_b32 v64, a188\n" \
"v_accvgpr_read_b32 v65, a189\n" \
"v_accvgpr_read_b32 v66, a190\n" \
"v_accvgpr_read_b32 v67, a191\n" \
"v_pk_mul_f32 v[62:63], v[62:63], s[70:71]\n" \
"v_cvt_pk_bf16_f32 v60, v60, v61\n" \
"v_pk_mul_f32 v[78:79], v[78:79], s[70:71]\n" \
"v_cvt_pk_bf16_f32 v76, v76, v77\n" \
"v_pk_mul_f32 v[54:55], v[54:55], s[70:71]\n" \
"v_cvt_pk_bf16_f32 v52, v52, v53\n" \
"v_pk_mul_f32 v[102:103], v[102:103], s[70:71]\n" \
"v_cvt_pk_bf16_f32 v100, v100, v101\n" \
"v_pk_mul_f32 v[88:89], v[88:89], s[70:71]\n" \
"v_pk_mul_f32 v[90:91], v[90:91], s[70:71]\n" \
"v_cvt_pk_bf16_f32 v85, v86, v87\n" \
"v_pk_mul_f32 v[72:73], v[72:73], s[70:71]\n" \
"v_pk_mul_f32 v[74:75], v[74:75], s[70:71]\n" \
"v_cvt_pk_bf16_f32 v69, v70, v71\n" \
"v_pk_mul_f32 v[96:97], v[96:97], s[70:71]\n" \
"v_pk_mul_f32 v[98:99], v[98:99], s[70:71]\n" \
"buffer_store_dwordx4 a[104:107], v20, s[80:83], s88 offen offset:0 sc1\n" \
"v_cvt_pk_bf16_f32 v93, v94, v95\n" \
"v_pk_mul_f32 v[64:65], v[64:65], s[70:71]\n" \
"v_pk_mul_f32 v[66:67], v[66:67], s[70:71]\n" \
"v_cvt_pk_bf16_f32 v61, v62, v63\n" \
"v_pk_mul_f32 v[80:81], v[80:81], s[70:71]\n" \
"v_pk_mul_f32 v[82:83], v[82:83], s[70:71]\n" \
"v_cvt_pk_bf16_f32 v77, v78, v79\n" \
"v_pk_mul_f32 v[56:57], v[56:57], s[70:71]\n" \
"v_pk_mul_f32 v[58:59], v[58:59], s[70:71]\n" \
"v_cvt_pk_bf16_f32 v53, v54, v55\n" \
"v_accvgpr_read_b32 v104, a244\n" \
"v_accvgpr_read_b32 v105, a245\n" \
"v_accvgpr_read_b32 v106, a246\n" \
"v_accvgpr_read_b32 v107, a247\n" \
"v_cvt_pk_bf16_f32 v101, v102, v103\n" \
"v_cvt_pk_bf16_f32 v86, v88, v89\n" \
"v_mfma_f32_16x16x32_bf16 v[104:107], v[156:159], v[108:111], v[104:107]\n" \
"v_cvt_pk_bf16_f32 v87, v90, v91\n" \
"v_cvt_pk_bf16_f32 v70, v72, v73\n" \
"ds_write_b128 v21, v[84:87] offset:16384\n" \
"v_mfma_f32_16x16x32_bf16 v[104:107], v[160:163], v[112:115], v[104:107]\n" \
"v_cvt_pk_bf16_f32 v71, v74, v75\n" \
"ds_write_b128 v21, v[68:71] offset:17408\n" \
"v_cvt_pk_bf16_f32 v94, v96, v97\n" \
"v_cvt_pk_bf16_f32 v95, v98, v99\n" \
"ds_write_b128 v21, v[92:95] offset:20480\n" \
"v_cvt_pk_bf16_f32 v62, v64, v65\n" \
"v_cvt_pk_bf16_f32 v63, v66, v67\n" \
"ds_write_b128 v21, v[60:63] offset:21504\n" \
"v_pk_mul_f32 v[104:105], v[104:105], s[70:71]\n" \
"v_pk_mul_f32 v[106:107], v[106:107], s[70:71]\n" \
"v_cvt_pk_bf16_f32 v78, v80, v81\n" \
"v_cvt_pk_bf16_f32 v79, v82, v83\n" \
"ds_write_b128 v21, v[76:79] offset:24576\n" \
"v_cvt_pk_bf16_f32 v54, v56, v57\n" \
"v_cvt_pk_bf16_f32 v55, v58, v59\n" \
"ds_write_b128 v21, v[52:55] offset:25600\n" \
"v_cvt_pk_bf16_f32 v102, v104, v105\n" \
"v_cvt_pk_bf16_f32 v103, v106, v107\n" \
"ds_write_b128 v21, v[100:103] offset:28672\n" \
"buffer_store_dwordx4 a[108:111], v20, s[80:83], s89 offen offset:1024 sc1\n" \
"v_accvgpr_read_b32 v84, a248\n" \
"v_accvgpr_read_b32 v85, a249\n" \
"v_accvgpr_read_b32 v86, a250\n" \
"v_accvgpr_read_b32 v87, a251\n" \
"v_accvgpr_read_b32 v88, a252\n" \
"v_accvgpr_read_b32 v89, a253\n" \
"v_mfma_f32_16x16x32_bf16 v[84:87], v[168:171], v[112:115], v[84:87]\n" \
"v_accvgpr_read_b32 v90, a254\n" \
"v_accvgpr_read_b32 v91, a255\n" \
"v_mfma_f32_16x16x32_bf16 v[84:87], v[164:167], v[108:111], v[84:87]\n" \
"v_sub_f32_e32 v44, 1.0, v45\n" \
"v_mfma_f32_16x16x32_bf16 v[88:91], v[172:175], v[108:111], v[88:91]\n" \
"ds_write_b32 v26, v44\n" \
"v_mfma_f32_16x16x32_bf16 v[88:91], v[176:179], v[112:115], v[88:91]\n" \
"s_nop 3\n" \
"v_pk_mul_f32 v[84:85], v[84:85], s[70:71]\n" \
"v_pk_mul_f32 v[86:87], v[86:87], s[70:71]\n" \
"v_cvt_pk_bf16_f32 v84, v84, v85\n" \
"v_cvt_pk_bf16_f32 v85, v86, v87\n" \
"v_pk_mul_f32 v[88:89], v[88:89], s[70:71]\n" \
"v_pk_mul_f32 v[90:91], v[90:91], s[70:71]\n" \
"v_cvt_pk_bf16_f32 v86, v88, v89\n" \
"v_cvt_pk_bf16_f32 v87, v90, v91\n" \
"ds_write_b128 v21, v[84:87] offset:29696\n" \
"L_end_%=:\n"
#define PROJ_VCLOB "v21","v24","v25","v33","v34","v35","v36","v37","v38","v39","v40","v41","v42","v43","v44","v45","v46","v47","v48","v49","v50","v51","v52","v53","v54","v55","v56","v57","v58","v59","v60","v61","v62","v63","v64","v65","v66","v67","v68","v69","v70","v71","v72","v73","v74","v75","v76","v77","v78","v79","v80","v81","v82","v83","v84","v85","v86","v87","v88","v89","v90","v91","v92","v93","v94","v95","v96","v97","v98","v99","v100","v101","v102","v103","v104","v105","v106","v107","v108","v109","v110","v111","v112","v113","v114","v115","v116","v117","v118","v119","v120","v121","v122","v123","v124","v125","v126","v127","v128","v129","v130","v131","v132","v133","v134","v135","v136","v137","v138","v139","v140","v141","v142","v143","v144","v145","v146","v147","v148","v149","v150","v151","v152","v153","v154","v155","v156","v157","v158","v159","v160","v161","v162","v163","v164","v165","v166","v167","v168","v169","v170","v171","v172","v173","v174","v175","v176","v177","v178","v179","v180","v181","v182","v183","v184","v185","v186","v187","v188","v189","v190","v191","v192","v193","v194","v195","v196","v197","v198","v199","v200","v201","v202","v203","v204","v205","v206","v207","v208","v209","v210","v211","v212","v213","v214","v215","v216","v217","v218","v219","v220","v221","v222","v223","v224","v225","v226","v227","v228","v229","v230","v231","v232","v233","v234","v235","v236","v237","v238","v239","v240","v241","v242","v243","v244","v245","v246","v247","v248","v249","v250","v251","v252","v253","v254","v255"
#define PROJ_SCLOB "s37","s38","s39","s40","s41","s42","s43","s44","s45","s46","s47","s48","s49","s50","s51","s52","s53","s54","s55","s62","s63","s76","s77","s78","s79","s80","s81","s82","s83","s84","s85","s86","s87","s88","s89","s94","s95","s96","s97","s98","s99","s100","s101"

#define PLO(p) ((unsigned)(size_t)(p))
#define PHI(p) ((unsigned)((size_t)(p) >> 32))
__device__ __forceinline__ unsigned proj_phase(const u16* __restrict__ x, const u16* __restrict__ ckv, const u16* __restrict__ wq, const u16* __restrict__ wuk,
                                                       const u16* __restrict__ wuv, const u16* __restrict__ wqkv, const u16* __restrict__ gw, const u16* wo, const u16* wob, const u16* out,
                                                       char* __restrict__ ws, char* lds, const unsigned wid, unsigned& tag, u32x4 (&vp)[5], u32x4 (&sa)[5], unsigned (&sd)[2]) {
    typedef unsigned u32x2 __attribute__((ext_vector_type(2)));
    unsigned* const flags = (unsigned*)(ws + WS_F);
    const int b = blockIdx.x, h = (b & 7) * 4 + ((b >> 3) & 3), mt = b >> 5;
    const unsigned L0 = (unsigned)(size_t)lds, rb = (wid < 2u ? 15u - mt : mt) * 128u;
    RTS(0);
#define PST() do {} while (0)
    const unsigned lane = threadIdx.x & 63u, wr = wid >> 1, wc = wid & 1u;
    PST();
    unsigned vAc[2], vB[2];
    const unsigned sp = b & 7, nt = (b >> 3) & 3, rot = (PROJ_ROT) & 7u;
#pragma unroll
    for (int par = 0; par < 2; ++par) {
        const unsigned lr = lane >> 3, cc = (lane & 7u) ^ (4u * par + (lr >> 1)), r0 = rb + wc * 64u + lr;
        vAc[par] = r0 * (DC * 2) + cc * 16u;
        const unsigned br = lane >> 4, p16 = lane & 15u, sc = (((p16 >> 1) ^ (br | (par << 2))) << 1) | (p16 & 1u);
        vB[par] = (16u * wid + br) * (NBIG * 2) + h * (HD * 2) + sc * 16u;
    }
#define SRD(p, n) (u32x4){PLO(p), PHI(p) & 0xffffu, n, 0x00020000u}
    u32x4 sC = SRD(ckv, S * DC * 2), sK = SRD(wuk, DC * NBIG * 2), sV = SRD(wuv, DC * NBIG * 2);
#define PRE(n) do { SBAR(); asm volatile(PROJ_PRE##n :: "{v22}"(vAc[0]), "{v23}"(vAc[1]), "{v26}"(vB[0]), "{v27}"(vB[1]), "{s[64:67]}"(sC), "{s[68:71]}"(sK), "{s[72:75]}"(sV), \
                 [mA] "s"(L0 + wid * 8192u), [mB] "s"(L0 + 65536u + wid * 4096u), [rc] "s"(rot * 128u) : "memory", "m0", "scc", "s62", "s63", "s84", "s85"); SBAR(); } while (0)
    PRE(0);
    const unsigned fr = lane & 15u, fg = lane >> 4, r4 = (lane >> 2) & 3u, sB = r4 | ((fg & 1u) << 2);
    const unsigned aA0 = L0 + (wr * 64u + fr) * 128u + ((fg ^ (fr >> 1)) << 4);
    unsigned vl = lane * 16u, r32 = L0 + 65536u + (8u * fg + r4) * 256u + (lane & 3u) * 8u + ((sB ^ (4u * wc)) << 5);
    u32x2 r28 = {aA0, aA0 ^ 64u};
    asm volatile("" : "+v"(vl), "+v"(r28), "+v"(r32));
    PST();
    PRE(1);
    PST();
    PRE(2);
    PST();
    asm volatile("s_mov_b64 exec, 0\n s_cmp_lt_u32 %0, 2\n s_cbranch_scc1 L_chA\n s_cmp_eq_u32 %0, 2\n s_cbranch_scc1 L_pf2\n s_branch L_pf3\n"
                 "L_chA: s_cmp_eq_u32 %0, 0\n s_cbranch_scc1 L_pf0\n s_branch L_pf1\n L_retA: s_mov_b64 exec, -1" :: "s"(wid) : "scc", "memory");
    PRE(3);
    PST();
    sa[0] = (u32x4){PLO(x), PHI(x), PLO(wq), PHI(wq)}; sa[1] = (u32x4){PLO(wqkv), PHI(wqkv), PLO(gw), PHI(gw)}; sa[2] = (u32x4){PLO(wo), PHI(wo), PLO(wob), PHI(wob)}; sa[3] = (u32x4){PLO(out), PHI(out), rot * 128u, 0u};
    asm volatile(PROJ_ASM
                 : [tag] "=&s"(tag), "+{s[8:11]}"(sa[0]), "+{s[12:15]}"(sa[1]), "+{s[16:19]}"(sa[2]), "+{s[20:23]}"(sa[3]), "=&{s[56:59]}"(sa[4]), "=&{s60}"(sd[0]), "=&{s61}"(sd[1]),
                   "+{s[64:67]}"(sC), "+{s[68:71]}"(sK), "+{s[72:75]}"(sV), "+{v22}"(vAc[0]), "+{v23}"(vAc[1]), "+{v26}"(vB[0]), "+{v27}"(vB[1]), "+{v[28:29]}"(r28), "+{v32}"(r32),
                   "={v[0:3]}"(vp[0]), "={v[4:7]}"(vp[1]), "={v[8:11]}"(vp[2]), "={v[12:15]}"(vp[3]), "={v[16:19]}"(vp[4])
                 : "{v20}"(vl), [wsl] "s"(PLO(ws)), [wsh] "s"(PHI(ws)), [b] "s"(b), [w] "s"(wid), [mA] "s"(L0 + wid * 8192u), [mB] "s"(L0 + 65536u + wid * 4096u), [mS] "s"(L0 + 151552u + wid * 512u)
                 : "memory", "m0", "vcc", "scc", PROJ_VCLOB, PROJ_SCLOB, ACLOB_LO, ACLOB_HI);
    RTS(1);
    return vl;
}

namespace att {
#define DI __device__ __forceinline__
constexpr int LDS_BYTES = 163840;
constexpr unsigned LDS_G = 159744u, LDS_L = 161792u;
typedef __bf16 bfx8 __attribute__((ext_vector_type(8)));
DI unsigned rfl(unsigned x) { return __builtin_amdgcn_readfirstlane(x); }
DI f32x4 mfma(u32x4 a, u32x4 b, f32x4 c) { return __builtin_amdgcn_mfma_f32_16x16x32_bf16(__builtin_bit_cast(bfx8, a), __builtin_bit_cast(bfx8, b), c, 0, 0, 0); }
typedef __bf16 bfx2 __attribute__((ext_vector_type(2)));
typedef float f32x2 __attribute__((ext_vector_type(2)));
DI unsigned cvtpk2(float lo, float hi) { f32x2 v = {lo, hi}; bfx2 b = __builtin_convertvector(v, bfx2); return __builtin_bit_cast(unsigned, b); }
DI void att_exact(const u16* __restrict__ x, const u16* __restrict__ wq, char* __restrict__ ws, unsigned h, unsigned r0, float g1) {
    const unsigned lane = lane_id(), g = lane >> 4, c = lane & 15u;
    f32x4 o[8];
#pragma unroll
    for (int n = 0; n < 8; ++n) o[n] = (f32x4){0.f, 0.f, 0.f, 0.f};
#pragma unroll 1
    for (unsigned kk = 0; kk < (unsigned)DM; kk += 32u) {
        const unsigned k = kk + 8u * g;
        const u32x4 xf = *reinterpret_cast<const u32x4*>(x + (r0 + c) * DM + k);
#pragma unroll
        for (int n = 0; n < 8; ++n) {
            const u16* w = wq + k * NBIG + h * HD + n * 16 + c; u32x4 wf;
#pragma unroll
            for (int e = 0; e < 4; ++e) wf[e] = (unsigned)w[2 * e * NBIG] | ((unsigned)w[(2 * e + 1) * NBIG] << 16);
            o[n] = mfma(wf, xf, o[n]);
        }
    }
    u32x4 q[4];
#pragma unroll
    for (int p = 0; p < 4; ++p) {
        const f32x4 a = o[2 * p] * 0.12751743082f, b = o[2 * p + 1] * 0.12751743082f;
        q[p] = (u32x4){cvtpk2(a[0], a[1]), cvtpk2(a[2], a[3]), cvtpk2(b[0], b[1]), cvtpk2(b[2], b[3])};
    }
#pragma unroll
    for (int n = 0; n < 8; ++n) o[n] = (f32x4){0.f, 0.f, 0.f, 0.f};
    const __amdgpu_buffer_rsrc_t srd = __builtin_amdgcn_make_buffer_rsrc((void*)(ws + WS_K + (size_t)h * 1048576), (short)0, 1048576, 0x00020000);
    float m = -3.0e38f, l = 0.f;
#pragma unroll 1
    for (unsigned k0 = 0; k0 <= r0 + 15u; k0 += 32u) {
        const unsigned tb = (k0 >> 6) * 32768u + (k0 & 32u) * 256u;
        f32x4 s[2]; float mx = m;
#pragma unroll
        for (int kt = 0; kt < 2; ++kt) {
            s[kt] = (f32x4){0.f, 0.f, 0.f, 0.f};
#pragma unroll
            for (int p = 0; p < 4; ++p) s[kt] = mfma(__builtin_amdgcn_raw_buffer_load_b128(srd, lane * 16u + (kt * 4 + p) * 1024u, tb, 16), q[p], s[kt]);
#pragma unroll
            for (int e = 0; e < 4; ++e) { if (k0 + 16u * kt + 4u * g + e > r0 + c) s[kt][e] = -3.0e38f; mx = fmaxf(mx, s[kt][e]); }
        }
        mx = fmaxf(mx, __shfl_xor(mx, 16)); mx = fmaxf(mx, __shfl_xor(mx, 32));
        const float al = __builtin_amdgcn_exp2f(m - mx); m = mx;
        u32x4 pf;
#pragma unroll
        for (int e = 0; e < 4; ++e) pf[e] = cvtpk2(__builtin_amdgcn_exp2f(s[e >> 1][2 * (e & 1)] - m), __builtin_amdgcn_exp2f(s[e >> 1][2 * (e & 1) + 1] - m));
        l *= al;
#pragma unroll
        for (int e = 0; e < 4; ++e) l += __uint_as_float(pf[e] << 16) + __uint_as_float(pf[e] & 0xffff0000u);
#pragma unroll
        for (int n = 0; n < 8; ++n) o[n] = mfma(__builtin_amdgcn_raw_buffer_load_b128(srd, lane * 16u + 16384u + ((n >> 2) * 8 + (n & 3)) * 1024u, tb - (k0 & 32u) * 128u, 16), pf, o[n] * al);
    }
    l += __shfl_xor(l, 16); l += __shfl_xor(l, 32);
    const float f = g1 / l;
    const __amdgpu_buffer_rsrc_t srdA = __builtin_amdgcn_make_buffer_rsrc((void*)(ws + WS_AF), (short)0, 128 * KS_TOT * 1024, 0x00020000);
#pragma unroll
    for (int p = 0; p < 4; ++p) {
        const f32x4 a = o[2 * p] * f, b = o[2 * p + 1] * f;
        __builtin_amdgcn_raw_buffer_store_b128((u32x4){cvtpk2(a[0], a[1]), cvtpk2(a[2], a[3]), cvtpk2(b[0], b[1]), cvtpk2(b[2], b[3])}, srdA, lane * 16u + p * 1024u, ((r0 >> 4) * KS_TOT + 18u + 4u * h) * 1024u, 16);
    }
}
}

#define ATT_ASM \
"L_att0:\n" \
"s_mov_b32 s101, 0\n" \
"s_waitcnt vmcnt(20) lgkmcnt(0)\n" \
"s_barrier\n" \
"v_accvgpr_write_b32 a216, 0\n" \
"v_accvgpr_write_b32 a217, 0\n" \
"v_accvgpr_write_b32 a218, 0\n" \
"v_accvgpr_write_b32 a219, 0\n" \
"v_and_b32_e32 v63, 0x300, v20\n" \
"v_add_u32_e32 v58, s20, v20\n" \
"v_mfma_f32_32x32x16_bf16 v[128:143], a[216:219], a[216:219], 0\n" \
"ds_read_b128 v[112:115], v63 offset:0\n" \
"ds_read_b128 a[0:3], v58 offset:0\n" \
"ds_read_b128 a[16:19], v58 offset:8192\n" \
"ds_read_b128 v[116:119], v63 offset:1024\n" \
"ds_read_b128 a[4:7], v58 offset:1024\n" \
"v_add_u32_e32 v59, s21, v20\n" \
"v_mfma_f32_32x32x16_bf16 v[144:159], a[216:219], a[216:219], 0\n" \
"ds_read_b128 a[32:35], v59 offset:0\n" \
"ds_read_b128 a[48:51], v59 offset:8192\n" \
"s_sub_u32 s67, s12, 0xc000\n" \
"s_mov_b32 s65, s12\n" \
"v_add_u32_e32 v55, s67, v20\n" \
"v_add_u32_e32 v52, 0xc000, v55\n" \
"v_mfma_f32_32x32x16_bf16 v[160:175], a[216:219], a[216:219], 0\n" \
"ds_read_b128 a[20:23], v58 offset:9216\n" \
"s_xor_b32 m0, s65, 0x10000\n" \
"ds_read_b128 a[36:39], v59 offset:1024\n" \
"buffer_load_dwordx4 v52, s[8:11], 0 offen offset:0 sc1 lds\n" \
"ds_read_b128 a[52:55], v59 offset:9216\n" \
"v_mfma_f32_32x32x16_bf16 v[176:191], a[216:219], a[216:219], 0\n" \
"buffer_load_dwordx4 v52, s[8:11], 0 offen offset:1024 sc1 lds\n" \
"ds_read_b128 v[120:123], v63 offset:2048\n" \
"v_add_u32_e32 v60, s15, v20\n" \
"v_add_u32_e32 v60, 0x10000, v60\n" \
"v_mov_b32_e32 v56, s62\n" \
"v_mfma_f32_32x32x16_bf16 v[192:207], a[216:219], a[216:219], 0\n" \
"ds_read_b128 a[8:11], v58 offset:2048\n" \
"ds_read_b128 a[24:27], v58 offset:10240\n" \
"v_readlane_b32 s68, v11, 0\n" \
"v_bfe_i32 v21, v20, 4, 1\n" \
"v_mov_b32_e32 v54, 0x3f803f80\n" \
"v_mov_b32_e32 v57, 0x3f003f00\n" \
"v_mfma_f32_32x32x16_bf16 v[208:223], a[216:219], a[216:219], 0\n" \
"ds_read_b128 a[40:43], v59 offset:2048\n" \
"ds_read_b128 a[56:59], v59 offset:10240\n" \
"s_waitcnt lgkmcnt(14)\n" \
"v_xor_b32_e32 v112, 0x80008000, v112\n" \
"v_xor_b32_e32 v113, 0x80008000, v113\n" \
"v_xor_b32_e32 v114, 0x80008000, v114\n" \
"v_mfma_f32_32x32x16_bf16 v[224:239], a[216:219], a[216:219], 0\n" \
"ds_read_b128 v[124:127], v63 offset:3072\n" \
"ds_read_b128 a[12:15], v58 offset:3072\n" \
"v_xor_b32_e32 v115, 0x80008000, v115\n" \
"v_and_b32_e32 v21, 0xaaaaaaaa, v21\n" \
"v_accvgpr_write_b32 a220, v112\n" \
"s_waitcnt lgkmcnt(13)\n" \
"v_xor_b32_e32 v116, 0x80008000, v116\n" \
"v_mfma_f32_32x32x16_bf16 v[240:255], a[216:219], a[216:219], 0\n" \
"ds_read_b128 a[28:31], v58 offset:11264\n" \
"ds_read_b128 a[44:47], v59 offset:3072\n" \
"v_xor_b32_e32 v117, 0x80008000, v117\n" \
"v_xor_b32_e32 v118, 0x80008000, v118\n" \
"v_xor_b32_e32 v119, 0x80008000, v119\n" \
"v_accvgpr_write_b32 a221, v113\n" \
"v_mfma_f32_32x32x16_bf16 a[192:207], a[216:219], a[216:219], 0\n" \
"ds_read_b128 a[60:63], v59 offset:11264\n" \
"ds_read_b128 a[160:163], v20 offset:0\n" \
"v_accvgpr_write_b32 a222, v114\n" \
"v_accvgpr_write_b32 a223, v115\n" \
"v_readlane_b32 s80, v11, 9\n" \
"v_readlane_b32 s81, v11, 10\n" \
"v_mfma_f32_16x16x32_bf16 v[32:35], v[112:115], a[0:3], 0\n" \
"ds_read_b128 a[164:167], v20 offset:1024\n" \
"ds_read_b128 a[168:171], v20 offset:2048\n" \
"v_mfma_f32_16x16x32_bf16 v[36:39], v[112:115], a[16:19], 0\n" \
"ds_read_b128 a[172:175], v20 offset:3072\n" \
"s_waitcnt lgkmcnt(15)\n" \
"v_mfma_f32_16x16x32_bf16 v[40:43], v[112:115], a[32:35], 0\n" \
"ds_read_b128 a[188:191], v20 offset:7168\n" \
"s_waitcnt lgkmcnt(14)\n" \
"v_xor_b32_e32 v120, 0x80008000, v120\n" \
"v_mfma_f32_16x16x32_bf16 v[44:47], v[112:115], a[48:51], 0\n" \
"ds_read_b128 a[184:187], v20 offset:6144\n" \
"v_xor_b32_e32 v121, 0x80008000, v121\n" \
"v_mfma_f32_16x16x32_bf16 v[32:35], v[116:119], a[4:7], v[32:35]\n" \
"v_xor_b32_e32 v122, 0x80008000, v122\n" \
"v_xor_b32_e32 v123, 0x80008000, v123\n" \
"v_mfma_f32_16x16x32_bf16 v[36:39], v[116:119], a[20:23], v[36:39]\n" \
"ds_read_b128 a[180:183], v20 offset:5120\n" \
"ds_read_b128 a[176:179], v20 offset:4096\n" \
"v_mfma_f32_16x16x32_bf16 v[40:43], v[116:119], a[36:39], v[40:43]\n" \
"ds_read_b128 a[128:131], v20 offset:8192\n" \
"s_mov_b32 s82, 0x47fca0\n" \
"v_mfma_f32_16x16x32_bf16 v[44:47], v[116:119], a[52:55], v[44:47]\n" \
"s_waitcnt lgkmcnt(13)\n" \
"v_xor_b32_e32 v124, 0x80008000, v124\n" \
"v_mfma_f32_16x16x32_bf16 v[32:35], v[120:123], a[8:11], v[32:35]\n" \
"v_xor_b32_e32 v125, 0x80008000, v125\n" \
"v_xor_b32_e32 v126, 0x80008000, v126\n" \
"v_mfma_f32_16x16x32_bf16 v[36:39], v[120:123], a[24:27], v[36:39]\n" \
"ds_read_b128 a[132:135], v20 offset:9216\n" \
"v_xor_b32_e32 v127, 0x80008000, v127\n" \
"v_mfma_f32_16x16x32_bf16 v[40:43], v[120:123], a[40:43], v[40:43]\n" \
"ds_read_b128 a[136:139], v20 offset:10240\n" \
"ds_read_b128 a[140:143], v20 offset:11264\n" \
"v_mfma_f32_16x16x32_bf16 v[44:47], v[120:123], a[56:59], v[44:47]\n" \
"ds_read_b128 a[144:147], v20 offset:12288\n" \
"s_waitcnt lgkmcnt(15)\n" \
"v_mfma_f32_16x16x32_bf16 v[32:35], v[124:127], a[12:15], v[32:35]\n" \
"ds_read_b128 a[148:151], v20 offset:13312\n" \
"ds_read_b128 a[152:155], v20 offset:14336\n" \
"v_mfma_f32_16x16x32_bf16 v[36:39], v[124:127], a[28:31], v[36:39]\n" \
"ds_read_b128 a[156:159], v20 offset:15360\n" \
"s_waitcnt lgkmcnt(15)\n" \
"v_mfma_f32_16x16x32_bf16 v[40:43], v[124:127], a[44:47], v[40:43]\n" \
"ds_read_b128 a[64:67], v60 offset:16384\n" \
"s_mov_b32 s83, 0x20000\n" \
"v_mfma_f32_16x16x32_bf16 v[44:47], v[124:127], a[60:63], v[44:47]\n" \
"ds_read_b128 a[68:71], v60 offset:17408\n" \
"s_mov_b32 s100, 0x71800000\n" \
"v_mfma_f32_16x16x32_bf16 v[64:67], a[160:163], a[0:3], v[32:35]\n" \
"ds_read_b128 a[72:75], v60 offset:18432\n" \
"s_waitcnt lgkmcnt(15)\n" \
"v_mfma_f32_16x16x32_bf16 v[64:67], a[164:167], a[4:7], v[64:67]\n" \
"ds_read_b128 a[76:79], v60 offset:19456\n" \
"s_mov_b32 s63, 0x30040000\n" \
"v_mfma_f32_16x16x32_bf16 v[64:67], a[168:171], a[8:11], v[64:67]\n" \
"ds_read_b128 a[80:83], v60 offset:24576\n" \
"v_mov_b32_e32 v62, 0\n" \
"v_mfma_f32_16x16x32_bf16 v[64:67], a[172:175], a[12:15], v[64:67]\n" \
"ds_read_b128 a[84:87], v60 offset:25600\n" \
"s_waitcnt lgkmcnt(15)\n" \
"v_mfma_f32_16x16x32_bf16 v[68:71], a[188:191], a[12:15], v[32:35]\n" \
"ds_read_b128 a[88:91], v60 offset:26624\n" \
"v_accvgpr_write_b32 a212, v54\n" \
"v_mfma_f32_16x16x32_bf16 v[68:71], a[184:187], a[8:11], v[68:71]\n" \
"ds_read_b128 a[92:95], v60 offset:27648\n" \
"v_accvgpr_write_b32 a208, 0\n" \
"v_mfma_f32_16x16x32_bf16 v[68:71], a[180:183], a[4:7], v[68:71]\n" \
"ds_read_b128 a[96:99], v60 offset:0\n" \
"s_waitcnt lgkmcnt(15)\n" \
"v_mfma_f32_16x16x32_bf16 v[68:71], a[176:179], a[0:3], v[68:71]\n" \
"ds_read_b128 a[100:103], v60 offset:1024\n" \
"v_accvgpr_write_b32 a213, v54\n" \
"v_mfma_f32_16x16x32_bf16 v[72:75], a[160:163], a[16:19], v[36:39]\n" \
"ds_read_b128 a[104:107], v60 offset:2048\n" \
"v_accvgpr_write_b32 a209, 0\n" \
"v_mfma_f32_16x16x32_bf16 v[72:75], a[164:167], a[20:23], v[72:75]\n" \
"ds_read_b128 a[108:111], v60 offset:3072\n" \
"v_accvgpr_write_b32 a214, v54\n" \
"v_mfma_f32_16x16x32_bf16 v[72:75], a[168:171], a[24:27], v[72:75]\n" \
"ds_read_b128 a[112:115], v60 offset:8192\n" \
"v_accvgpr_write_b32 a210, 0\n" \
"v_mfma_f32_16x16x32_bf16 v[72:75], a[172:175], a[28:31], v[72:75]\n" \
"ds_read_b128 a[116:119], v60 offset:9216\n" \
"v_accvgpr_write_b32 a215, v54\n" \
"v_mfma_f32_16x16x32_bf16 v[76:79], a[188:191], a[28:31], v[36:39]\n" \
"ds_read_b128 a[120:123], v60 offset:10240\n" \
"v_accvgpr_write_b32 a211, 0\n" \
"v_mfma_f32_16x16x32_bf16 v[76:79], a[184:187], a[24:27], v[76:79]\n" \
"ds_read_b128 a[124:127], v60 offset:11264\n" \
"v_or_b32_e32 v21, 0x44444444, v21\n" \
"v_mfma_f32_16x16x32_bf16 v[76:79], a[180:183], a[20:23], v[76:79]\n" \
"v_exp_f32_e32 v64, v64\n" \
"v_mfma_f32_16x16x32_bf16 v[76:79], a[176:179], a[16:19], v[76:79]\n" \
"v_exp_f32_e32 v65, v65\n" \
"v_mfma_f32_16x16x32_bf16 v[80:83], a[160:163], a[32:35], v[40:43]\n" \
"L_pf10:\n" \
"s_cbranch_execz L_pf14\n" \
"s_nop 0\n" \
"v_exp_f32_e32 v66, v66\n" \
"v_mfma_f32_16x16x32_bf16 v[80:83], a[164:167], a[36:39], v[80:83]\n" \
"v_exp_f32_e32 v67, v67\n" \
"v_mfma_f32_16x16x32_bf16 v[80:83], a[168:171], a[40:43], v[80:83]\n" \
"v_exp_f32_e32 v68, v68\n" \
"v_mfma_f32_16x16x32_bf16 v[80:83], a[172:175], a[44:47], v[80:83]\n" \
"v_exp_f32_e32 v69, v69\n" \
"v_mfma_f32_16x16x32_bf16 v[84:87], a[188:191], a[44:47], v[40:43]\n" \
"v_exp_f32_e32 v70, v70\n" \
"v_mfma_f32_16x16x32_bf16 v[84:87], a[184:187], a[40:43], v[84:87]\n" \
"v_exp_f32_e32 v71, v71\n" \
"v_mfma_f32_16x16x32_bf16 v[84:87], a[180:183], a[36:39], v[84:87]\n" \
"v_exp_f32_e32 v72, v72\n" \
"v_mfma_f32_16x16x32_bf16 v[84:87], a[176:179], a[32:35], v[84:87]\n" \
"v_exp_f32_e32 v73, v73\n" \
"v_mfma_f32_16x16x32_bf16 v[88:91], a[160:163], a[48:51], v[44:47]\n" \
"v_exp_f32_e32 v74, v74\n" \
"v_mfma_f32_16x16x32_bf16 v[88:91], a[164:167], a[52:55], v[88:91]\n" \
"v_exp_f32_e32 v75, v75\n" \
"v_mfma_f32_16x16x32_bf16 v[88:91], a[168:171], a[56:59], v[88:91]\n" \
"v_exp_f32_e32 v76, v76\n" \
"v_mfma_f32_16x16x32_bf16 v[88:91], a[172:175], a[60:63], v[88:91]\n" \
"v_exp_f32_e32 v77, v77\n" \
"v_mfma_f32_16x16x32_bf16 v[92:95], a[188:191], a[60:63], v[44:47]\n" \
"v_exp_f32_e32 v78, v78\n" \
"v_mfma_f32_16x16x32_bf16 v[92:95], a[184:187], a[56:59], v[92:95]\n" \
"v_exp_f32_e32 v79, v79\n" \
"v_mfma_f32_16x16x32_bf16 v[92:95], a[180:183], a[52:55], v[92:95]\n" \
"v_exp_f32_e32 v80, v80\n" \
"v_mfma_f32_16x16x32_bf16 v[92:95], a[176:179], a[48:51], v[92:95]\n" \
"v_exp_f32_e32 v81, v81\n" \
"s_waitcnt vmcnt(2) lgkmcnt(0)\n" \
"s_barrier\n" \
"s_mov_b64 exec, 1\n" \
"buffer_store_dword v56, off, s[56:59], s68 offset:0 sc1\n" \
"buffer_store_dword v56, off, s[56:59], s22 sc1\n" \
"s_mov_b64 exec, -1\n" \
"v_exp_f32_e32 v82, v82\n" \
"v_exp_f32_e32 v83, v83\n" \
"v_exp_f32_e32 v84, v84\n" \
"v_exp_f32_e32 v85, v85\n" \
"v_exp_f32_e32 v86, v86\n" \
"v_exp_f32_e32 v87, v87\n" \
"v_accvgpr_write_b32 a252, v57\n" \
"v_accvgpr_write_b32 a253, v57\n" \
"v_accvgpr_write_b32 a254, v57\n" \
"v_accvgpr_write_b32 a255, v57\n" \
"s_mov_b32 s64, 0x8000\n" \
"v_add_u32_e32 v50, 0x6000, v55\n" \
"v_add_u32_e32 v51, 0xa000, v55\n" \
"v_add_u32_e32 v53, 0x10000, v55\n" \
"v_add_u32_e32 v48, 0xc000, v20\n" \
"v_add_u32_e32 v49, s17, v20\n" \
"v_accvgpr_write_b32 a224, v116\n" \
"v_accvgpr_write_b32 a225, v117\n" \
"v_accvgpr_write_b32 a226, v118\n" \
"v_accvgpr_write_b32 a227, v119\n" \
"v_accvgpr_write_b32 a228, v120\n" \
"v_accvgpr_write_b32 a229, v121\n" \
"v_accvgpr_write_b32 a230, v122\n" \
"v_accvgpr_write_b32 a231, v123\n" \
"v_accvgpr_write_b32 a232, v124\n" \
"v_accvgpr_write_b32 a233, v125\n" \
"v_accvgpr_write_b32 a234, v126\n" \
"v_accvgpr_write_b32 a235, v127\n" \
"v_exp_f32_e32 v88, v88\n" \
"v_exp_f32_e32 v89, v89\n" \
"v_exp_f32_e32 v90, v90\n" \
"v_exp_f32_e32 v91, v91\n" \
"v_cvt_pk_bf16_f32 v96, v64, v65\n" \
"v_cvt_pk_bf16_f32 v97, v66, v67\n" \
"v_cvt_pk_bf16_f32 v98, v68, v69\n" \
"v_cvt_pk_bf16_f32 v99, v70, v71\n" \
"v_cvt_pk_bf16_f32 v104, v72, v73\n" \
"v_cvt_pk_bf16_f32 v105, v74, v75\n" \
"v_cvt_pk_bf16_f32 v106, v76, v77\n" \
"v_cvt_pk_bf16_f32 v107, v78, v79\n" \
"v_cvt_pk_bf16_f32 v112, v80, v81\n" \
"v_cvt_pk_bf16_f32 v113, v82, v83\n" \
"v_cvt_pk_bf16_f32 v114, v84, v85\n" \
"v_cvt_pk_bf16_f32 v115, v86, v87\n" \
"s_sub_u32 s66, s18, 1\n" \
"s_cbranch_scc1 L_tr_%=\n" \
"s_branch L_1_%=\n" \
"L_t1_%=:\n" \
"v_mfma_f32_16x16x32_bf16 v[252:255], a[188:191], v[124:127], v[252:255]\n" \
"v_mfma_f32_16x16x32_bf16 v[252:255], a[172:175], v[120:123], v[252:255]\n" \
"v_smfmac_f32_16x16x64_bf16 a[204:207], a[212:215], v[120:127], v21\n" \
"s_waitcnt lgkmcnt(0)\n" \
"L_1_%=:\n" \
"v_mfma_f32_16x16x32_bf16 v[64:67], a[128:131], a[0:3], v[32:35]\n" \
"ds_read_b128 a[160:163], v49 offset:0\n" \
"ds_read_b128 a[176:179], v49 offset:4096\n" \
"v_mfma_f32_16x16x32_bf16 v[64:67], a[132:135], a[4:7], v[64:67]\n" \
"v_cvt_pk_bf16_f32 v120, v88, v89\n" \
"v_cvt_pk_bf16_f32 v121, v90, v91\n" \
"v_mfma_f32_16x16x32_bf16 v[64:67], a[136:139], a[8:11], v[64:67]\n" \
"ds_read_b128 a[180:183], v49 offset:5120\n" \
"ds_read_b128 a[164:167], v49 offset:1024\n" \
"v_mfma_f32_16x16x32_bf16 v[64:67], a[140:143], a[12:15], v[64:67]\n" \
"v_exp_f32_e32 v92, v92\n" \
"v_mfma_f32_16x16x32_bf16 v[68:71], a[156:159], a[12:15], v[32:35]\n" \
"ds_read_b128 a[168:171], v49 offset:2048\n" \
"ds_read_b128 a[184:187], v49 offset:6144\n" \
"v_mfma_f32_16x16x32_bf16 v[68:71], a[152:155], a[8:11], v[68:71]\n" \
"v_exp_f32_e32 v93, v93\n" \
"v_mfma_f32_16x16x32_bf16 v[68:71], a[148:151], a[4:7], v[68:71]\n" \
"ds_read_b128 a[188:191], v49 offset:7168\n" \
"ds_read_b128 a[172:175], v49 offset:3072\n" \
"v_mfma_f32_16x16x32_bf16 v[68:71], a[144:147], a[0:3], v[68:71]\n" \
"v_exp_f32_e32 v94, v94\n" \
"v_mfma_f32_16x16x32_bf16 v[72:75], a[128:131], a[16:19], v[36:39]\n" \
"v_exp_f32_e32 v95, v95\n" \
"v_mfma_f32_16x16x32_bf16 v[72:75], a[132:135], a[20:23], v[72:75]\n" \
"v_cvt_pk_bf16_f32 v122, v92, v93\n" \
"v_cvt_pk_bf16_f32 v123, v94, v95\n" \
"v_mfma_f32_16x16x32_bf16 v[72:75], a[136:139], a[24:27], v[72:75]\n" \
"v_exp_f32_e32 v64, v64\n" \
"v_mfma_f32_16x16x32_bf16 v[72:75], a[140:143], a[28:31], v[72:75]\n" \
"v_exp_f32_e32 v65, v65\n" \
"v_mfma_f32_16x16x32_bf16 v[76:79], a[156:159], a[28:31], v[36:39]\n" \
"v_exp_f32_e32 v66, v66\n" \
"v_mfma_f32_16x16x32_bf16 v[76:79], a[152:155], a[24:27], v[76:79]\n" \
"v_exp_f32_e32 v67, v67\n" \
"v_mfma_f32_16x16x32_bf16 v[76:79], a[148:151], a[20:23], v[76:79]\n" \
"v_exp_f32_e32 v68, v68\n" \
"v_mfma_f32_16x16x32_bf16 v[76:79], a[144:147], a[16:19], v[76:79]\n" \
"v_exp_f32_e32 v69, v69\n" \
"v_mfma_f32_16x16x32_bf16 v[80:83], a[128:131], a[32:35], v[40:43]\n" \
"v_exp_f32_e32 v70, v70\n" \
"v_mfma_f32_16x16x32_bf16 v[80:83], a[132:135], a[36:39], v[80:83]\n" \
"v_exp_f32_e32 v71, v71\n" \
"v_mfma_f32_16x16x32_bf16 v[80:83], a[136:139], a[40:43], v[80:83]\n" \
"v_cvt_pk_bf16_f32 v100, v64, v65\n" \
"v_cvt_pk_bf16_f32 v101, v66, v67\n" \
"v_mfma_f32_16x16x32_bf16 v[80:83], a[140:143], a[44:47], v[80:83]\n" \
"v_cvt_pk_bf16_f32 v102, v68, v69\n" \
"v_cvt_pk_bf16_f32 v103, v70, v71\n" \
"v_mfma_f32_16x16x32_bf16 v[84:87], a[156:159], a[44:47], v[40:43]\n" \
"v_exp_f32_e32 v72, v72\n" \
"v_mfma_f32_16x16x32_bf16 v[84:87], a[152:155], a[40:43], v[84:87]\n" \
"v_exp_f32_e32 v73, v73\n" \
"v_mfma_f32_16x16x32_bf16 v[84:87], a[148:151], a[36:39], v[84:87]\n" \
"v_exp_f32_e32 v74, v74\n" \
"v_mfma_f32_16x16x32_bf16 v[84:87], a[144:147], a[32:35], v[84:87]\n" \
"v_exp_f32_e32 v75, v75\n" \
"v_mfma_f32_16x16x32_bf16 v[88:91], a[128:131], a[48:51], v[44:47]\n" \
"v_exp_f32_e32 v76, v76\n" \
"v_mfma_f32_16x16x32_bf16 v[88:91], a[132:135], a[52:55], v[88:91]\n" \
"v_exp_f32_e32 v77, v77\n" \
"v_mfma_f32_16x16x32_bf16 v[88:91], a[136:139], a[56:59], v[88:91]\n" \
"v_exp_f32_e32 v78, v78\n" \
"v_mfma_f32_16x16x32_bf16 v[88:91], a[140:143], a[60:63], v[88:91]\n" \
"v_exp_f32_e32 v79, v79\n" \
"v_mfma_f32_16x16x32_bf16 v[92:95], a[156:159], a[60:63], v[44:47]\n" \
"v_cvt_pk_bf16_f32 v108, v72, v73\n" \
"v_cvt_pk_bf16_f32 v109, v74, v75\n" \
"v_mfma_f32_16x16x32_bf16 v[92:95], a[152:155], a[56:59], v[92:95]\n" \
"v_cvt_pk_bf16_f32 v110, v76, v77\n" \
"v_cvt_pk_bf16_f32 v111, v78, v79\n" \
"v_mfma_f32_16x16x32_bf16 v[92:95], a[148:151], a[52:55], v[92:95]\n" \
"ds_read_b128 a[128:131], v48 offset:0\n" \
"ds_read_b128 a[132:135], v48 offset:1024\n" \
"v_mfma_f32_16x16x32_bf16 v[92:95], a[144:147], a[48:51], v[92:95]\n" \
"s_waitcnt lgkmcnt(2)\n" \
"v_mfma_f32_16x16x32_bf16 v[128:131], a[160:163], v[96:99], v[128:131]\n" \
"ds_read_b128 a[136:139], v48 offset:2048\n" \
"ds_read_b128 a[140:143], v48 offset:3072\n" \
"v_mfma_f32_16x16x32_bf16 v[128:131], a[176:179], v[100:103], v[128:131]\n" \
"v_exp_f32_e32 v80, v80\n" \
"v_mfma_f32_16x16x32_bf16 v[132:135], a[180:183], v[100:103], v[132:135]\n" \
"ds_read_b128 a[144:147], v48 offset:4096\n" \
"ds_read_b128 a[148:151], v48 offset:5120\n" \
"v_mfma_f32_16x16x32_bf16 v[132:135], a[164:167], v[96:99], v[132:135]\n" \
"v_exp_f32_e32 v81, v81\n" \
"v_mfma_f32_16x16x32_bf16 v[136:139], a[168:171], v[96:99], v[136:139]\n" \
"ds_read_b128 a[152:155], v48 offset:6144\n" \
"ds_read_b128 a[156:159], v48 offset:7168\n" \
"v_mfma_f32_16x16x32_bf16 v[136:139], a[184:187], v[100:103], v[136:139]\n" \
"v_exp_f32_e32 v82, v82\n" \
"v_mfma_f32_16x16x32_bf16 v[140:143], a[188:191], v[100:103], v[140:143]\n" \
"v_exp_f32_e32 v83, v83\n" \
"v_mfma_f32_16x16x32_bf16 v[140:143], a[172:175], v[96:99], v[140:143]\n" \
"v_exp_f32_e32 v84, v84\n" \
"v_smfmac_f32_16x16x64_bf16 a[192:195], a[212:215], v[96:103], v21\n" \
"v_mfma_f32_16x16x32_bf16 v[160:163], a[160:163], v[104:107], v[160:163]\n" \
"v_exp_f32_e32 v85, v85\n" \
"v_mfma_f32_16x16x32_bf16 v[160:163], a[176:179], v[108:111], v[160:163]\n" \
"v_exp_f32_e32 v86, v86\n" \
"v_mfma_f32_16x16x32_bf16 v[164:167], a[180:183], v[108:111], v[164:167]\n" \
"v_exp_f32_e32 v87, v87\n" \
"v_mfma_f32_16x16x32_bf16 v[164:167], a[164:167], v[104:107], v[164:167]\n" \
"v_cvt_pk_bf16_f32 v116, v80, v81\n" \
"v_cvt_pk_bf16_f32 v117, v82, v83\n" \
"v_mfma_f32_16x16x32_bf16 v[168:171], a[168:171], v[104:107], v[168:171]\n" \
"v_cvt_pk_bf16_f32 v118, v84, v85\n" \
"v_cvt_pk_bf16_f32 v119, v86, v87\n" \
"v_mfma_f32_16x16x32_bf16 v[168:171], a[184:187], v[108:111], v[168:171]\n" \
"v_exp_f32_e32 v88, v88\n" \
"v_mfma_f32_16x16x32_bf16 v[172:175], a[188:191], v[108:111], v[172:175]\n" \
"v_exp_f32_e32 v89, v89\n" \
"v_mfma_f32_16x16x32_bf16 v[172:175], a[172:175], v[104:107], v[172:175]\n" \
"v_exp_f32_e32 v90, v90\n" \
"v_smfmac_f32_16x16x64_bf16 a[196:199], a[212:215], v[104:111], v21\n" \
"v_mfma_f32_16x16x32_bf16 v[192:195], a[160:163], v[112:115], v[192:195]\n" \
"v_exp_f32_e32 v91, v91\n" \
"v_mfma_f32_16x16x32_bf16 v[192:195], a[176:179], v[116:119], v[192:195]\n" \
"v_exp_f32_e32 v92, v92\n" \
"v_mfma_f32_16x16x32_bf16 v[196:199], a[180:183], v[116:119], v[196:199]\n" \
"v_exp_f32_e32 v93, v93\n" \
"v_mfma_f32_16x16x32_bf16 v[196:199], a[164:167], v[112:115], v[196:199]\n" \
"v_exp_f32_e32 v94, v94\n" \
"v_mfma_f32_16x16x32_bf16 v[200:203], a[168:171], v[112:115], v[200:203]\n" \
"v_exp_f32_e32 v95, v95\n" \
"v_mfma_f32_16x16x32_bf16 v[200:203], a[184:187], v[116:119], v[200:203]\n" \
"v_cvt_pk_bf16_f32 v124, v88, v89\n" \
"v_cvt_pk_bf16_f32 v125, v90, v91\n" \
"v_mfma_f32_16x16x32_bf16 v[204:207], a[188:191], v[116:119], v[204:207]\n" \
"v_cvt_pk_bf16_f32 v126, v92, v93\n" \
"v_cvt_pk_bf16_f32 v127, v94, v95\n" \
"v_mfma_f32_16x16x32_bf16 v[204:207], a[172:175], v[112:115], v[204:207]\n" \
"v_smfmac_f32_16x16x64_bf16 a[200:203], a[212:215], v[112:119], v21\n" \
"s_waitcnt vmcnt(0) lgkmcnt(0)\n" \
"s_barrier\n" \
"v_mfma_f32_16x16x32_bf16 v[224:227], a[160:163], v[120:123], v[224:227]\n" \
"s_add_u32 s63, s63, s63\n" \
"s_cbranch_scc1 L_h1_%=\n" \
"L_hr1_%=:\n" \
"v_mfma_f32_16x16x32_bf16 v[224:227], a[176:179], v[124:127], v[224:227]\n" \
"s_xor_b32 m0, s65, 0x12000\n" \
"ds_read_b128 a[160:163], v49 offset:8192\n" \
"v_mfma_f32_16x16x32_bf16 v[228:231], a[180:183], v[124:127], v[228:231]\n" \
"buffer_load_dwordx4 v50, s[8:11], s64 offen offset:0 sc1 lds\n" \
"v_mfma_f32_16x16x32_bf16 v[228:231], a[164:167], v[120:123], v[228:231]\n" \
"ds_read_b128 a[176:179], v49 offset:12288\n" \
"ds_read_b128 a[180:183], v49 offset:13312\n" \
"v_mfma_f32_16x16x32_bf16 v[232:235], a[168:171], v[120:123], v[232:235]\n" \
"v_mfma_f32_16x16x32_bf16 v[232:235], a[184:187], v[124:127], v[232:235]\n" \
"ds_read_b128 a[164:167], v49 offset:9216\n" \
"ds_read_b128 a[168:171], v49 offset:10240\n" \
"v_mfma_f32_16x16x32_bf16 v[236:239], a[188:191], v[124:127], v[236:239]\n" \
"v_mfma_f32_16x16x32_bf16 v[236:239], a[172:175], v[120:123], v[236:239]\n" \
"ds_read_b128 a[184:187], v49 offset:14336\n" \
"ds_read_b128 a[188:191], v49 offset:15360\n" \
"v_mfma_f32_16x16x32_bf16 v[64:67], a[128:131], a[0:3], v[32:35]\n" \
"v_mfma_f32_16x16x32_bf16 v[64:67], a[132:135], a[4:7], v[64:67]\n" \
"buffer_load_dwordx4 v50, s[8:11], s64 offen offset:1024 sc1 lds\n" \
"v_mfma_f32_16x16x32_bf16 v[64:67], a[136:139], a[8:11], v[64:67]\n" \
"s_xor_b32 m0, s65, 0x6000\n" \
"ds_read_b128 a[172:175], v49 offset:11264\n" \
"v_mfma_f32_16x16x32_bf16 v[64:67], a[140:143], a[12:15], v[64:67]\n" \
"v_add_u32_e32 v49, 0x10000, v48\n" \
"v_mfma_f32_16x16x32_bf16 v[68:71], a[156:159], a[12:15], v[32:35]\n" \
"v_mfma_f32_16x16x32_bf16 v[68:71], a[152:155], a[8:11], v[68:71]\n" \
"v_mfma_f32_16x16x32_bf16 v[68:71], a[148:151], a[4:7], v[68:71]\n" \
"v_mfma_f32_16x16x32_bf16 v[68:71], a[144:147], a[0:3], v[68:71]\n" \
"v_mfma_f32_16x16x32_bf16 v[72:75], a[128:131], a[16:19], v[36:39]\n" \
"v_mfma_f32_16x16x32_bf16 v[72:75], a[132:135], a[20:23], v[72:75]\n" \
"v_mfma_f32_16x16x32_bf16 v[72:75], a[136:139], a[24:27], v[72:75]\n" \
"buffer_load_dwordx4 v51, s[8:11], s64 offen offset:0 sc1 lds\n" \
"v_mfma_f32_16x16x32_bf16 v[72:75], a[140:143], a[28:31], v[72:75]\n" \
"v_exp_f32_e32 v64, v64\n" \
"v_mfma_f32_16x16x32_bf16 v[76:79], a[156:159], a[28:31], v[36:39]\n" \
"v_exp_f32_e32 v65, v65\n" \
"v_mfma_f32_16x16x32_bf16 v[76:79], a[152:155], a[24:27], v[76:79]\n" \
"v_exp_f32_e32 v66, v66\n" \
"v_mfma_f32_16x16x32_bf16 v[76:79], a[148:151], a[20:23], v[76:79]\n" \
"v_exp_f32_e32 v67, v67\n" \
"v_mfma_f32_16x16x32_bf16 v[76:79], a[144:147], a[16:19], v[76:79]\n" \
"v_exp_f32_e32 v68, v68\n" \
"v_mfma_f32_16x16x32_bf16 v[80:83], a[128:131], a[32:35], v[40:43]\n" \
"v_exp_f32_e32 v69, v69\n" \
"v_mfma_f32_16x16x32_bf16 v[80:83], a[132:135], a[36:39], v[80:83]\n" \
"v_exp_f32_e32 v70, v70\n" \
"v_mfma_f32_16x16x32_bf16 v[80:83], a[136:139], a[40:43], v[80:83]\n" \
"v_exp_f32_e32 v71, v71\n" \
"v_mfma_f32_16x16x32_bf16 v[80:83], a[140:143], a[44:47], v[80:83]\n" \
"buffer_load_dwordx4 v51, s[8:11], s64 offen offset:1024 sc1 lds\n" \
"v_mfma_f32_16x16x32_bf16 v[84:87], a[156:159], a[44:47], v[40:43]\n" \
"v_exp_f32_e32 v72, v72\n" \
"v_mfma_f32_16x16x32_bf16 v[84:87], a[152:155], a[40:43], v[84:87]\n" \
"v_exp_f32_e32 v73, v73\n" \
"v_mfma_f32_16x16x32_bf16 v[84:87], a[148:151], a[36:39], v[84:87]\n" \
"v_exp_f32_e32 v74, v74\n" \
"v_mfma_f32_16x16x32_bf16 v[84:87], a[144:147], a[32:35], v[84:87]\n" \
"v_exp_f32_e32 v75, v75\n" \
"v_mfma_f32_16x16x32_bf16 v[88:91], a[128:131], a[48:51], v[44:47]\n" \
"s_xor_b32 m0, s65, 0x14000\n" \
"ds_read_b128 a[128:131], v48 offset:8192\n" \
"v_mfma_f32_16x16x32_bf16 v[88:91], a[132:135], a[52:55], v[88:91]\n" \
"v_exp_f32_e32 v76, v76\n" \
"v_mfma_f32_16x16x32_bf16 v[88:91], a[136:139], a[56:59], v[88:91]\n" \
"ds_read_b128 a[132:135], v48 offset:9216\n" \
"ds_read_b128 a[136:139], v48 offset:10240\n" \
"v_mfma_f32_16x16x32_bf16 v[88:91], a[140:143], a[60:63], v[88:91]\n" \
"v_exp_f32_e32 v77, v77\n" \
"v_mfma_f32_16x16x32_bf16 v[92:95], a[156:159], a[60:63], v[44:47]\n" \
"buffer_load_dwordx4 v52, s[8:11], s64 offen offset:0 sc1 lds\n" \
"v_mfma_f32_16x16x32_bf16 v[92:95], a[152:155], a[56:59], v[92:95]\n" \
"ds_read_b128 a[140:143], v48 offset:11264\n" \
"ds_read_b128 a[152:155], v48 offset:14336\n" \
"v_mfma_f32_16x16x32_bf16 v[92:95], a[148:151], a[52:55], v[92:95]\n" \
"v_exp_f32_e32 v78, v78\n" \
"v_mfma_f32_16x16x32_bf16 v[92:95], a[144:147], a[48:51], v[92:95]\n" \
"ds_read_b128 a[144:147], v48 offset:12288\n" \
"s_waitcnt lgkmcnt(6)\n" \
"v_mfma_f32_16x16x32_bf16 v[144:147], a[160:163], v[96:99], v[144:147]\n" \
"ds_read_b128 a[148:151], v48 offset:13312\n" \
"ds_read_b128 a[156:159], v48 offset:15360\n" \
"v_mfma_f32_16x16x32_bf16 v[144:147], a[176:179], v[100:103], v[144:147]\n" \
"v_exp_f32_e32 v79, v79\n" \
"v_mfma_f32_16x16x32_bf16 v[148:151], a[180:183], v[100:103], v[148:151]\n" \
"v_exp_f32_e32 v80, v80\n" \
"v_mfma_f32_16x16x32_bf16 v[148:151], a[164:167], v[96:99], v[148:151]\n" \
"v_exp_f32_e32 v81, v81\n" \
"v_mfma_f32_16x16x32_bf16 v[152:155], a[168:171], v[96:99], v[152:155]\n" \
"buffer_load_dwordx4 v52, s[8:11], s64 offen offset:1024 sc1 lds\n" \
"v_mfma_f32_16x16x32_bf16 v[152:155], a[184:187], v[100:103], v[152:155]\n" \
"s_mov_b32 m0, s65\n" \
"s_xor_b32 s65, s65, 0x4000\n" \
"v_mfma_f32_16x16x32_bf16 v[156:159], a[188:191], v[100:103], v[156:159]\n" \
"v_exp_f32_e32 v82, v82\n" \
"v_mfma_f32_16x16x32_bf16 v[156:159], a[172:175], v[96:99], v[156:159]\n" \
"v_exp_f32_e32 v83, v83\n" \
"v_mfma_f32_16x16x32_bf16 v[176:179], a[160:163], v[104:107], v[176:179]\n" \
"v_exp_f32_e32 v84, v84\n" \
"v_mfma_f32_16x16x32_bf16 v[176:179], a[176:179], v[108:111], v[176:179]\n" \
"v_exp_f32_e32 v85, v85\n" \
"v_mfma_f32_16x16x32_bf16 v[180:183], a[180:183], v[108:111], v[180:183]\n" \
"v_exp_f32_e32 v86, v86\n" \
"v_mfma_f32_16x16x32_bf16 v[180:183], a[164:167], v[104:107], v[180:183]\n" \
"v_exp_f32_e32 v87, v87\n" \
"v_mfma_f32_16x16x32_bf16 v[184:187], a[168:171], v[104:107], v[184:187]\n" \
"v_exp_f32_e32 v88, v88\n" \
"v_mfma_f32_16x16x32_bf16 v[184:187], a[184:187], v[108:111], v[184:187]\n" \
"buffer_load_dwordx4 v53, s[8:11], s64 offen offset:0 sc1 lds\n" \
"v_mfma_f32_16x16x32_bf16 v[188:191], a[188:191], v[108:111], v[188:191]\n" \
"v_exp_f32_e32 v89, v89\n" \
"v_mfma_f32_16x16x32_bf16 v[188:191], a[172:175], v[104:107], v[188:191]\n" \
"v_exp_f32_e32 v90, v90\n" \
"v_mfma_f32_16x16x32_bf16 v[208:211], a[160:163], v[112:115], v[208:211]\n" \
"v_exp_f32_e32 v91, v91\n" \
"v_mfma_f32_16x16x32_bf16 v[208:211], a[176:179], v[116:119], v[208:211]\n" \
"v_cvt_pk_bf16_f32 v96, v64, v65\n" \
"v_cvt_pk_bf16_f32 v97, v66, v67\n" \
"v_mfma_f32_16x16x32_bf16 v[212:215], a[180:183], v[116:119], v[212:215]\n" \
"v_cvt_pk_bf16_f32 v98, v68, v69\n" \
"v_cvt_pk_bf16_f32 v99, v70, v71\n" \
"v_mfma_f32_16x16x32_bf16 v[212:215], a[164:167], v[112:115], v[212:215]\n" \
"v_cvt_pk_bf16_f32 v104, v72, v73\n" \
"v_cvt_pk_bf16_f32 v105, v74, v75\n" \
"v_mfma_f32_16x16x32_bf16 v[216:219], a[168:171], v[112:115], v[216:219]\n" \
"v_cvt_pk_bf16_f32 v106, v76, v77\n" \
"v_cvt_pk_bf16_f32 v107, v78, v79\n" \
"v_mfma_f32_16x16x32_bf16 v[216:219], a[184:187], v[116:119], v[216:219]\n" \
"v_xor_b32_e32 v48, 0x4000, v48\n" \
"v_mfma_f32_16x16x32_bf16 v[220:223], a[188:191], v[116:119], v[220:223]\n" \
"buffer_load_dwordx4 v53, s[8:11], s64 offen offset:1024 sc1 lds\n" \
"v_mfma_f32_16x16x32_bf16 v[220:223], a[172:175], v[112:115], v[220:223]\n" \
"s_add_u32 s64, s64, 0x8000\n" \
"s_sub_u32 s66, s66, 1\n" \
"v_mfma_f32_16x16x32_bf16 v[240:243], a[160:163], v[120:123], v[240:243]\n" \
"v_cvt_pk_bf16_f32 v112, v80, v81\n" \
"v_cvt_pk_bf16_f32 v113, v82, v83\n" \
"v_mfma_f32_16x16x32_bf16 v[240:243], a[176:179], v[124:127], v[240:243]\n" \
"v_cvt_pk_bf16_f32 v114, v84, v85\n" \
"v_cvt_pk_bf16_f32 v115, v86, v87\n" \
"v_mfma_f32_16x16x32_bf16 v[244:247], a[180:183], v[124:127], v[244:247]\n" \
"v_mfma_f32_16x16x32_bf16 v[244:247], a[164:167], v[120:123], v[244:247]\n" \
"v_mfma_f32_16x16x32_bf16 v[248:251], a[168:171], v[120:123], v[248:251]\n" \
"v_mfma_f32_16x16x32_bf16 v[248:251], a[184:187], v[124:127], v[248:251]\n" \
"s_cbranch_scc0 L_t1_%=\n" \
"v_mfma_f32_16x16x32_bf16 v[252:255], a[188:191], v[124:127], v[252:255]\n" \
"v_mfma_f32_16x16x32_bf16 v[252:255], a[172:175], v[120:123], v[252:255]\n" \
"v_smfmac_f32_16x16x64_bf16 a[204:207], a[212:215], v[120:127], v21\n" \
"s_waitcnt lgkmcnt(0)\n" \
"s_barrier\n" \
"L_tr_%=:\n" \
"L_pf11:\n" \
"s_cbranch_execz L_pf15\n" \
"s_nop 0\n" \
"s_cmp_lg_u32 s16, 0\n" \
"s_cbranch_scc1 L_sw_%=\n" \
"v_mfma_f32_16x16x32_bf16 v[64:67], a[128:131], a[0:3], v[32:35]\n" \
"s_xor_b32 m0, s65, 0x12000\n" \
"s_mov_b32 s65, s13\n" \
"v_mfma_f32_16x16x32_bf16 v[64:67], a[132:135], a[4:7], v[64:67]\n" \
"buffer_load_dwordx4 v50, s[8:11], s64 offen offset:0 sc1 lds\n" \
"v_mfma_f32_16x16x32_bf16 v[64:67], a[136:139], a[8:11], v[64:67]\n" \
"v_add_u32_e32 v63, s15, v20\n" \
"s_add_u32 s67, s14, 40960\n" \
"v_mfma_f32_16x16x32_bf16 v[64:67], a[140:143], a[12:15], v[64:67]\n" \
"ds_read_b128 a[160:163], v49 offset:0\n" \
"ds_read_b128 a[176:179], v49 offset:4096\n" \
"v_mfma_f32_16x16x32_bf16 v[68:71], a[156:159], a[12:15], v[32:35]\n" \
"buffer_load_dwordx4 v50, s[8:11], s64 offen offset:1024 sc1 lds\n" \
"v_mfma_f32_16x16x32_bf16 v[68:71], a[152:155], a[8:11], v[68:71]\n" \
"v_add_u32_e32 v50, 0x6000, v63\n" \
"s_xor_b32 m0, s65, 0x8000\n" \
"v_mfma_f32_16x16x32_bf16 v[68:71], a[148:151], a[4:7], v[68:71]\n" \
"ds_read_b128 a[180:183], v49 offset:5120\n" \
"ds_read_b128 a[164:167], v49 offset:1024\n" \
"v_mfma_f32_16x16x32_bf16 v[68:71], a[144:147], a[0:3], v[68:71]\n" \
"ds_read_b128 a[168:171], v49 offset:2048\n" \
"ds_read_b128 a[184:187], v49 offset:6144\n" \
"v_mfma_f32_16x16x32_bf16 v[72:75], a[128:131], a[16:19], v[36:39]\n" \
"v_cvt_pk_bf16_f32 v120, v88, v89\n" \
"v_cvt_pk_bf16_f32 v121, v90, v91\n" \
"v_mfma_f32_16x16x32_bf16 v[72:75], a[132:135], a[20:23], v[72:75]\n" \
"buffer_load_dwordx4 v50, s[8:11], s67 offen offset:0 sc1 lds\n" \
"v_mfma_f32_16x16x32_bf16 v[72:75], a[136:139], a[24:27], v[72:75]\n" \
"ds_read_b128 a[188:191], v49 offset:7168\n" \
"ds_read_b128 a[172:175], v49 offset:3072\n" \
"v_mfma_f32_16x16x32_bf16 v[72:75], a[140:143], a[28:31], v[72:75]\n" \
"v_exp_f32_e32 v92, v92\n" \
"v_mfma_f32_16x16x32_bf16 v[76:79], a[156:159], a[28:31], v[36:39]\n" \
"v_exp_f32_e32 v93, v93\n" \
"v_mfma_f32_16x16x32_bf16 v[76:79], a[152:155], a[24:27], v[76:79]\n" \
"buffer_load_dwordx4 v50, s[8:11], s67 offen offset:1024 sc1 lds\n" \
"v_mfma_f32_16x16x32_bf16 v[76:79], a[148:151], a[20:23], v[76:79]\n" \
"v_exp_f32_e32 v94, v94\n" \
"v_mfma_f32_16x16x32_bf16 v[76:79], a[144:147], a[16:19], v[76:79]\n" \
"v_exp_f32_e32 v95, v95\n" \
"v_mfma_f32_16x16x32_bf16 v[80:83], a[128:131], a[32:35], v[40:43]\n" \
"v_cvt_pk_bf16_f32 v122, v92, v93\n" \
"v_cvt_pk_bf16_f32 v123, v94, v95\n" \
"v_mfma_f32_16x16x32_bf16 v[80:83], a[132:135], a[36:39], v[80:83]\n" \
"v_exp_f32_e32 v64, v64\n" \
"v_mfma_f32_16x16x32_bf16 v[80:83], a[136:139], a[40:43], v[80:83]\n" \
"buffer_load_dwordx4 v50, s[8:11], s67 offen offset:2048 sc1 lds\n" \
"v_mfma_f32_16x16x32_bf16 v[80:83], a[140:143], a[44:47], v[80:83]\n" \
"v_exp_f32_e32 v65, v65\n" \
"v_mfma_f32_16x16x32_bf16 v[84:87], a[156:159], a[44:47], v[40:43]\n" \
"v_exp_f32_e32 v66, v66\n" \
"v_mfma_f32_16x16x32_bf16 v[84:87], a[152:155], a[40:43], v[84:87]\n" \
"v_exp_f32_e32 v67, v67\n" \
"v_mfma_f32_16x16x32_bf16 v[84:87], a[148:151], a[36:39], v[84:87]\n" \
"buffer_load_dwordx4 v50, s[8:11], s67 offen offset:3072 sc1 lds\n" \
"v_mfma_f32_16x16x32_bf16 v[84:87], a[144:147], a[32:35], v[84:87]\n" \
"v_exp_f32_e32 v68, v68\n" \
"v_mfma_f32_16x16x32_bf16 v[88:91], a[128:131], a[48:51], v[44:47]\n" \
"v_exp_f32_e32 v69, v69\n" \
"v_mfma_f32_16x16x32_bf16 v[88:91], a[132:135], a[52:55], v[88:91]\n" \
"v_exp_f32_e32 v70, v70\n" \
"v_mfma_f32_16x16x32_bf16 v[88:91], a[136:139], a[56:59], v[88:91]\n" \
"v_exp_f32_e32 v71, v71\n" \
"v_mfma_f32_16x16x32_bf16 v[88:91], a[140:143], a[60:63], v[88:91]\n" \
"v_cvt_pk_bf16_f32 v100, v64, v65\n" \
"v_cvt_pk_bf16_f32 v101, v66, v67\n" \
"v_mfma_f32_16x16x32_bf16 v[92:95], a[156:159], a[60:63], v[44:47]\n" \
"v_cvt_pk_bf16_f32 v102, v68, v69\n" \
"v_cvt_pk_bf16_f32 v103, v70, v71\n" \
"v_mfma_f32_16x16x32_bf16 v[92:95], a[152:155], a[56:59], v[92:95]\n" \
"s_mov_b32 s64, s14\n" \
"v_add_u32_e32 v51, 0x12000, v63\n" \
"v_mfma_f32_16x16x32_bf16 v[92:95], a[148:151], a[52:55], v[92:95]\n" \
"s_xor_b32 m0, s65, 0xa000\n" \
"v_readlane_b32 s93, v11, 17\n" \
"v_mfma_f32_16x16x32_bf16 v[92:95], a[144:147], a[48:51], v[92:95]\n" \
"v_readlane_b32 s92, v11, 22\n" \
"s_waitcnt lgkmcnt(0)\n" \
"v_mfma_f32_16x16x32_bf16 v[128:131], a[160:163], v[96:99], v[128:131]\n" \
"buffer_load_dwordx4 v51, s[8:11], s64 offen offset:0 sc1 lds\n" \
"v_mfma_f32_16x16x32_bf16 v[128:131], a[176:179], v[100:103], v[128:131]\n" \
"buffer_load_dwordx4 v51, s[8:11], s64 offen offset:1024 sc1 lds\n" \
"v_mfma_f32_16x16x32_bf16 v[132:135], a[180:183], v[100:103], v[132:135]\n" \
"v_exp_f32_e32 v72, v72\n" \
"v_mfma_f32_16x16x32_bf16 v[132:135], a[164:167], v[96:99], v[132:135]\n" \
"v_exp_f32_e32 v73, v73\n" \
"v_mfma_f32_16x16x32_bf16 v[136:139], a[168:171], v[96:99], v[136:139]\n" \
"v_exp_f32_e32 v74, v74\n" \
"v_mfma_f32_16x16x32_bf16 v[136:139], a[184:187], v[100:103], v[136:139]\n" \
"v_exp_f32_e32 v75, v75\n" \
"v_mfma_f32_16x16x32_bf16 v[140:143], a[188:191], v[100:103], v[140:143]\n" \
"v_exp_f32_e32 v76, v76\n" \
"v_mfma_f32_16x16x32_bf16 v[140:143], a[172:175], v[96:99], v[140:143]\n" \
"v_exp_f32_e32 v77, v77\n" \
"v_smfmac_f32_16x16x64_bf16 a[192:195], a[212:215], v[96:103], v21\n" \
"v_mfma_f32_16x16x32_bf16 v[160:163], a[160:163], v[104:107], v[160:163]\n" \
"v_exp_f32_e32 v78, v78\n" \
"v_exp_f32_e32 v79, v79\n" \
"v_cvt_pk_bf16_f32 v108, v72, v73\n" \
"v_cvt_pk_bf16_f32 v109, v74, v75\n" \
"v_cvt_pk_bf16_f32 v110, v76, v77\n" \
"v_cvt_pk_bf16_f32 v111, v78, v79\n" \
"v_exp_f32_e32 v80, v80\n" \
"v_exp_f32_e32 v81, v81\n" \
"v_mfma_f32_16x16x32_bf16 v[160:163], a[176:179], v[108:111], v[160:163]\n" \
"buffer_load_dwordx4 v51, s[8:11], s64 offen offset:2048 sc1 lds\n" \
"v_mfma_f32_16x16x32_bf16 v[164:167], a[180:183], v[108:111], v[164:167]\n" \
"buffer_load_dwordx4 v51, s[8:11], s64 offen offset:3072 sc1 lds\n" \
"v_mfma_f32_16x16x32_bf16 v[164:167], a[164:167], v[104:107], v[164:167]\n" \
"v_exp_f32_e32 v82, v82\n" \
"v_mfma_f32_16x16x32_bf16 v[168:171], a[168:171], v[104:107], v[168:171]\n" \
"v_exp_f32_e32 v83, v83\n" \
"v_mfma_f32_16x16x32_bf16 v[168:171], a[184:187], v[108:111], v[168:171]\n" \
"v_exp_f32_e32 v84, v84\n" \
"v_mfma_f32_16x16x32_bf16 v[172:175], a[188:191], v[108:111], v[172:175]\n" \
"v_exp_f32_e32 v85, v85\n" \
"v_mfma_f32_16x16x32_bf16 v[172:175], a[172:175], v[104:107], v[172:175]\n" \
"v_exp_f32_e32 v86, v86\n" \
"v_smfmac_f32_16x16x64_bf16 a[196:199], a[212:215], v[104:111], v21\n" \
"v_mfma_f32_16x16x32_bf16 v[192:195], a[160:163], v[112:115], v[192:195]\n" \
"v_exp_f32_e32 v87, v87\n" \
"v_cvt_pk_bf16_f32 v116, v80, v81\n" \
"v_cvt_pk_bf16_f32 v117, v82, v83\n" \
"v_cvt_pk_bf16_f32 v118, v84, v85\n" \
"v_cvt_pk_bf16_f32 v119, v86, v87\n" \
"s_add_u32 m0, s93, 0x0\n" \
"v_exp_f32_e32 v88, v88\n" \
"v_mfma_f32_16x16x32_bf16 v[192:195], a[176:179], v[116:119], v[192:195]\n" \
"buffer_load_dwordx4 v0, s[80:83], s92 offen offset:0 lds\n" \
"v_mfma_f32_16x16x32_bf16 v[196:199], a[180:183], v[116:119], v[196:199]\n" \
"ds_read_b128 a[128:131], v48 offset:0\n" \
"ds_read_b128 a[132:135], v48 offset:1024\n" \
"v_mfma_f32_16x16x32_bf16 v[196:199], a[164:167], v[112:115], v[196:199]\n" \
"ds_read_b128 a[136:139], v48 offset:2048\n" \
"ds_read_b128 a[140:143], v48 offset:3072\n" \
"ds_read_b128 a[144:147], v48 offset:4096\n" \
"ds_read_b128 a[148:151], v48 offset:5120\n" \
"ds_read_b128 a[152:155], v48 offset:6144\n" \
"ds_read_b128 a[156:159], v48 offset:7168\n" \
"v_mfma_f32_16x16x32_bf16 v[200:203], a[168:171], v[112:115], v[200:203]\n" \
"buffer_load_dwordx4 v1, s[80:83], s92 offen offset:1024 lds\n" \
"v_mfma_f32_16x16x32_bf16 v[200:203], a[184:187], v[116:119], v[200:203]\n" \
"v_exp_f32_e32 v89, v89\n" \
"v_mfma_f32_16x16x32_bf16 v[204:207], a[188:191], v[116:119], v[204:207]\n" \
"v_exp_f32_e32 v90, v90\n" \
"v_mfma_f32_16x16x32_bf16 v[204:207], a[172:175], v[112:115], v[204:207]\n" \
"v_exp_f32_e32 v91, v91\n" \
"v_smfmac_f32_16x16x64_bf16 a[200:203], a[212:215], v[112:119], v21\n" \
"v_mfma_f32_16x16x32_bf16 v[224:227], a[160:163], v[120:123], v[224:227]\n" \
"v_exp_f32_e32 v92, v92\n" \
"v_exp_f32_e32 v93, v93\n" \
"v_exp_f32_e32 v94, v94\n" \
"v_exp_f32_e32 v95, v95\n" \
"v_cvt_pk_bf16_f32 v124, v88, v89\n" \
"v_cvt_pk_bf16_f32 v125, v90, v91\n" \
"v_cvt_pk_bf16_f32 v126, v92, v93\n" \
"v_cvt_pk_bf16_f32 v127, v94, v95\n" \
"v_add_u32_e32 v52, 0x14000, v63\n" \
"v_add_u32_e32 v53, 0x20000, v63\n" \
"v_mfma_f32_16x16x32_bf16 v[224:227], a[176:179], v[124:127], v[224:227]\n" \
"buffer_load_dwordx4 v2, s[80:83], s92 offen offset:2048 lds\n" \
"v_mfma_f32_16x16x32_bf16 v[228:231], a[180:183], v[124:127], v[228:231]\n" \
"buffer_load_dwordx4 v3, s[80:83], s92 offen offset:3072 lds\n" \
"v_mfma_f32_16x16x32_bf16 v[228:231], a[164:167], v[120:123], v[228:231]\n" \
"s_mov_b32 s66, s19\n" \
"v_mfma_f32_16x16x32_bf16 v[232:235], a[168:171], v[120:123], v[232:235]\n" \
"v_mfma_f32_16x16x32_bf16 v[232:235], a[184:187], v[124:127], v[232:235]\n" \
"v_mfma_f32_16x16x32_bf16 v[236:239], a[188:191], v[124:127], v[236:239]\n" \
"v_mfma_f32_16x16x32_bf16 v[236:239], a[172:175], v[120:123], v[236:239]\n" \
"s_waitcnt vmcnt(14) lgkmcnt(0)\n" \
"s_barrier\n" \
"v_mfma_f32_16x16x32_bf16 v[64:67], a[128:131], a[0:3], v[32:35]\n" \
"s_xor_b32 m0, s65, 0x18000\n" \
"ds_read_b128 a[160:163], v49 offset:8192\n" \
"v_mfma_f32_16x16x32_bf16 v[64:67], a[132:135], a[4:7], v[64:67]\n" \
"buffer_load_dwordx4 v52, s[8:11], s64 offen offset:0 sc1 lds\n" \
"v_mfma_f32_16x16x32_bf16 v[64:67], a[136:139], a[8:11], v[64:67]\n" \
"ds_read_b128 a[176:179], v49 offset:12288\n" \
"ds_read_b128 a[180:183], v49 offset:13312\n" \
"v_mfma_f32_16x16x32_bf16 v[64:67], a[140:143], a[12:15], v[64:67]\n" \
"ds_read_b128 a[164:167], v49 offset:9216\n" \
"ds_read_b128 a[168:171], v49 offset:10240\n" \
"v_mfma_f32_16x16x32_bf16 v[68:71], a[156:159], a[12:15], v[32:35]\n" \
"buffer_load_dwordx4 v52, s[8:11], s64 offen offset:1024 sc1 lds\n" \
"v_mfma_f32_16x16x32_bf16 v[68:71], a[152:155], a[8:11], v[68:71]\n" \
"ds_read_b128 a[184:187], v49 offset:14336\n" \
"ds_read_b128 a[188:191], v49 offset:15360\n" \
"v_mfma_f32_16x16x32_bf16 v[68:71], a[148:151], a[4:7], v[68:71]\n" \
"v_exp_f32_e32 v64, v64\n" \
"v_mfma_f32_16x16x32_bf16 v[68:71], a[144:147], a[0:3], v[68:71]\n" \
"v_exp_f32_e32 v65, v65\n" \
"v_mfma_f32_16x16x32_bf16 v[72:75], a[128:131], a[16:19], v[36:39]\n" \
"v_exp_f32_e32 v66, v66\n" \
"v_mfma_f32_16x16x32_bf16 v[72:75], a[132:135], a[20:23], v[72:75]\n" \
"buffer_load_dwordx4 v52, s[8:11], s64 offen offset:2048 sc1 lds\n" \
"v_mfma_f32_16x16x32_bf16 v[72:75], a[136:139], a[24:27], v[72:75]\n" \
"v_exp_f32_e32 v67, v67\n" \
"v_mfma_f32_16x16x32_bf16 v[72:75], a[140:143], a[28:31], v[72:75]\n" \
"v_exp_f32_e32 v68, v68\n" \
"v_mfma_f32_16x16x32_bf16 v[76:79], a[156:159], a[28:31], v[36:39]\n" \
"v_exp_f32_e32 v69, v69\n" \
"v_mfma_f32_16x16x32_bf16 v[76:79], a[152:155], a[24:27], v[76:79]\n" \
"buffer_load_dwordx4 v52, s[8:11], s64 offen offset:3072 sc1 lds\n" \
"v_mfma_f32_16x16x32_bf16 v[76:79], a[148:151], a[20:23], v[76:79]\n" \
"ds_read_b128 a[172:175], v49 offset:11264\n" \
"s_mov_b32 m0, s65\n" \
"v_mfma_f32_16x16x32_bf16 v[76:79], a[144:147], a[16:19], v[76:79]\n" \
"v_exp_f32_e32 v70, v70\n" \
"v_mfma_f32_16x16x32_bf16 v[80:83], a[128:131], a[32:35], v[40:43]\n" \
"v_exp_f32_e32 v71, v71\n" \
"v_mfma_f32_16x16x32_bf16 v[80:83], a[132:135], a[36:39], v[80:83]\n" \
"v_exp_f32_e32 v72, v72\n" \
"v_mfma_f32_16x16x32_bf16 v[80:83], a[136:139], a[40:43], v[80:83]\n" \
"buffer_load_dwordx4 v53, s[8:11], s64 offen offset:0 sc1 lds\n" \
"v_mfma_f32_16x16x32_bf16 v[80:83], a[140:143], a[44:47], v[80:83]\n" \
"v_exp_f32_e32 v73, v73\n" \
"v_mfma_f32_16x16x32_bf16 v[84:87], a[156:159], a[44:47], v[40:43]\n" \
"L_pf12:\n" \
"s_cbranch_execz L_pf16\n" \
"s_nop 0\n" \
"v_exp_f32_e32 v74, v74\n" \
"v_mfma_f32_16x16x32_bf16 v[84:87], a[152:155], a[40:43], v[84:87]\n" \
"v_exp_f32_e32 v75, v75\n" \
"v_mfma_f32_16x16x32_bf16 v[84:87], a[148:151], a[36:39], v[84:87]\n" \
"v_exp_f32_e32 v76, v76\n" \
"v_mfma_f32_16x16x32_bf16 v[84:87], a[144:147], a[32:35], v[84:87]\n" \
"buffer_load_dwordx4 v53, s[8:11], s64 offen offset:1024 sc1 lds\n" \
"v_mfma_f32_16x16x32_bf16 v[88:91], a[128:131], a[48:51], v[44:47]\n" \
"ds_read_b128 a[128:131], v48 offset:8192\n" \
"v_add_u32_e32 v49, 0x10000, v48\n" \
"v_mfma_f32_16x16x32_bf16 v[88:91], a[132:135], a[52:55], v[88:91]\n" \
"ds_read_b128 a[132:135], v48 offset:9216\n" \
"s_xor_b32 s65, s65, 0x8000\n" \
"v_mfma_f32_16x16x32_bf16 v[88:91], a[136:139], a[56:59], v[88:91]\n" \
"v_exp_f32_e32 v77, v77\n" \
"v_mfma_f32_16x16x32_bf16 v[88:91], a[140:143], a[60:63], v[88:91]\n" \
"buffer_load_dwordx4 v53, s[8:11], s64 offen offset:2048 sc1 lds\n" \
"v_mfma_f32_16x16x32_bf16 v[92:95], a[156:159], a[60:63], v[44:47]\n" \
"ds_read_b128 a[136:139], v48 offset:10240\n" \
"ds_read_b128 a[140:143], v48 offset:11264\n" \
"v_mfma_f32_16x16x32_bf16 v[92:95], a[152:155], a[56:59], v[92:95]\n" \
"ds_read_b128 a[152:155], v48 offset:14336\n" \
"ds_read_b128 a[156:159], v48 offset:15360\n" \
"v_mfma_f32_16x16x32_bf16 v[92:95], a[148:151], a[52:55], v[92:95]\n" \
"v_exp_f32_e32 v78, v78\n" \
"v_mfma_f32_16x16x32_bf16 v[92:95], a[144:147], a[48:51], v[92:95]\n" \
"ds_read_b128 a[144:147], v48 offset:12288\n" \
"s_waitcnt lgkmcnt(7)\n" \
"v_mfma_f32_16x16x32_bf16 v[144:147], a[160:163], v[96:99], v[144:147]\n" \
"buffer_load_dwordx4 v53, s[8:11], s64 offen offset:3072 sc1 lds\n" \
"v_mfma_f32_16x16x32_bf16 v[144:147], a[176:179], v[100:103], v[144:147]\n" \
"s_add_u32 m0, s93, 0x1000\n" \
"ds_read_b128 a[148:151], v48 offset:13312\n" \
"v_mfma_f32_16x16x32_bf16 v[148:151], a[180:183], v[100:103], v[148:151]\n" \
"v_exp_f32_e32 v79, v79\n" \
"v_mfma_f32_16x16x32_bf16 v[148:151], a[164:167], v[96:99], v[148:151]\n" \
"v_exp_f32_e32 v80, v80\n" \
"v_mfma_f32_16x16x32_bf16 v[152:155], a[168:171], v[96:99], v[152:155]\n" \
"buffer_load_dwordx4 v4, s[80:83], s92 offen offset:0 lds\n" \
"v_mfma_f32_16x16x32_bf16 v[152:155], a[184:187], v[100:103], v[152:155]\n" \
"v_exp_f32_e32 v81, v81\n" \
"v_mfma_f32_16x16x32_bf16 v[156:159], a[188:191], v[100:103], v[156:159]\n" \
"v_exp_f32_e32 v82, v82\n" \
"v_mfma_f32_16x16x32_bf16 v[156:159], a[172:175], v[96:99], v[156:159]\n" \
"v_exp_f32_e32 v83, v83\n" \
"v_mfma_f32_16x16x32_bf16 v[176:179], a[160:163], v[104:107], v[176:179]\n" \
"v_exp_f32_e32 v84, v84\n" \
"v_mfma_f32_16x16x32_bf16 v[176:179], a[176:179], v[108:111], v[176:179]\n" \
"buffer_load_dwordx4 v5, s[80:83], s92 offen offset:1024 lds\n" \
"v_mfma_f32_16x16x32_bf16 v[180:183], a[180:183], v[108:111], v[180:183]\n" \
"v_exp_f32_e32 v85, v85\n" \
"v_mfma_f32_16x16x32_bf16 v[180:183], a[164:167], v[104:107], v[180:183]\n" \
"v_exp_f32_e32 v86, v86\n" \
"v_mfma_f32_16x16x32_bf16 v[184:187], a[168:171], v[104:107], v[184:187]\n" \
"v_exp_f32_e32 v87, v87\n" \
"v_mfma_f32_16x16x32_bf16 v[184:187], a[184:187], v[108:111], v[184:187]\n" \
"v_exp_f32_e32 v88, v88\n" \
"v_mfma_f32_16x16x32_bf16 v[188:191], a[188:191], v[108:111], v[188:191]\n" \
"buffer_load_dwordx4 v6, s[80:83], s92 offen offset:2048 lds\n" \
"v_mfma_f32_16x16x32_bf16 v[188:191], a[172:175], v[104:107], v[188:191]\n" \
"v_exp_f32_e32 v89, v89\n" \
"v_mfma_f32_16x16x32_bf16 v[208:211], a[160:163], v[112:115], v[208:211]\n" \
"v_exp_f32_e32 v90, v90\n" \
"v_mfma_f32_16x16x32_bf16 v[208:211], a[176:179], v[116:119], v[208:211]\n" \
"v_exp_f32_e32 v91, v91\n" \
"v_mfma_f32_16x16x32_bf16 v[212:215], a[180:183], v[116:119], v[212:215]\n" \
"buffer_load_dwordx4 v7, s[80:83], s92 offen offset:3072 lds\n" \
"v_mfma_f32_16x16x32_bf16 v[212:215], a[164:167], v[112:115], v[212:215]\n" \
"s_add_u32 m0, s93, 0x2000\n" \
"v_cvt_pk_bf16_f32 v96, v64, v65\n" \
"v_mfma_f32_16x16x32_bf16 v[216:219], a[168:171], v[112:115], v[216:219]\n" \
"v_cvt_pk_bf16_f32 v97, v66, v67\n" \
"v_cvt_pk_bf16_f32 v98, v68, v69\n" \
"v_mfma_f32_16x16x32_bf16 v[216:219], a[184:187], v[116:119], v[216:219]\n" \
"v_cvt_pk_bf16_f32 v99, v70, v71\n" \
"v_cvt_pk_bf16_f32 v104, v72, v73\n" \
"v_mfma_f32_16x16x32_bf16 v[220:223], a[188:191], v[116:119], v[220:223]\n" \
"v_cvt_pk_bf16_f32 v105, v74, v75\n" \
"v_cvt_pk_bf16_f32 v106, v76, v77\n" \
"v_mfma_f32_16x16x32_bf16 v[220:223], a[172:175], v[112:115], v[220:223]\n" \
"buffer_load_dwordx4 v8, s[80:83], s92 offen offset:0 lds\n" \
"v_mfma_f32_16x16x32_bf16 v[240:243], a[160:163], v[120:123], v[240:243]\n" \
"v_cvt_pk_bf16_f32 v107, v78, v79\n" \
"v_cvt_pk_bf16_f32 v112, v80, v81\n" \
"v_mfma_f32_16x16x32_bf16 v[240:243], a[176:179], v[124:127], v[240:243]\n" \
"v_cvt_pk_bf16_f32 v113, v82, v83\n" \
"v_cvt_pk_bf16_f32 v114, v84, v85\n" \
"v_mfma_f32_16x16x32_bf16 v[244:247], a[180:183], v[124:127], v[244:247]\n" \
"v_cvt_pk_bf16_f32 v115, v86, v87\n" \
"v_add_u32_e32 v48, s16, v20\n" \
"v_mfma_f32_16x16x32_bf16 v[244:247], a[164:167], v[120:123], v[244:247]\n" \
"s_add_u32 s64, s64, 0x10000\n" \
"v_mfma_f32_16x16x32_bf16 v[248:251], a[168:171], v[120:123], v[248:251]\n" \
"v_mfma_f32_16x16x32_bf16 v[248:251], a[184:187], v[124:127], v[248:251]\n" \
"v_mfma_f32_16x16x32_bf16 v[252:255], a[188:191], v[124:127], v[252:255]\n" \
"v_mfma_f32_16x16x32_bf16 v[252:255], a[172:175], v[120:123], v[252:255]\n" \
"v_smfmac_f32_16x16x64_bf16 a[204:207], a[212:215], v[120:127], v21\n" \
"s_waitcnt vmcnt(13) lgkmcnt(0)\n" \
"s_mov_b64 exec, 0\n" \
"s_cmp_lg_u32 s15, 0\n" \
"s_cbranch_scc1 L_pf21\n" \
"s_branch L_pf20\n" \
"L_retB:\n" \
"s_mov_b64 exec, -1\n" \
"s_barrier\n" \
"s_branch L_2_%=\n" \
"L_t2_%=:\n" \
"v_mfma_f32_16x16x32_bf16 v[252:255], a[188:191], v[124:127], v[252:255]\n" \
"v_cvt_pk_bf16_f32 v114, v84, v85\n" \
"v_cvt_pk_bf16_f32 v115, v86, v87\n" \
"v_mfma_f32_16x16x32_bf16 v[252:255], a[172:175], v[120:123], v[252:255]\n" \
"v_xor_b32_e32 v48, 0x8000, v48\n" \
"v_smfmac_f32_16x16x64_bf16 a[204:207], a[212:215], v[120:127], v21\n" \
"s_waitcnt lgkmcnt(0)\n" \
"L_2_%=:\n" \
"v_mfma_f32_16x16x32_bf16 v[64:67], a[128:131], a[64:67], v[32:35]\n" \
"ds_read_b128 a[160:163], v49 offset:0\n" \
"ds_read_b128 a[176:179], v49 offset:4096\n" \
"v_mfma_f32_16x16x32_bf16 v[64:67], a[132:135], a[68:71], v[64:67]\n" \
"v_cvt_pk_bf16_f32 v120, v88, v89\n" \
"v_cvt_pk_bf16_f32 v121, v90, v91\n" \
"v_mfma_f32_16x16x32_bf16 v[64:67], a[136:139], a[72:75], v[64:67]\n" \
"ds_read_b128 a[180:183], v49 offset:5120\n" \
"ds_read_b128 a[164:167], v49 offset:1024\n" \
"v_mfma_f32_16x16x32_bf16 v[64:67], a[140:143], a[76:79], v[64:67]\n" \
"v_exp_f32_e32 v92, v92\n" \
"v_mfma_f32_16x16x32_bf16 v[68:71], a[156:159], a[76:79], v[32:35]\n" \
"ds_read_b128 a[168:171], v49 offset:2048\n" \
"ds_read_b128 a[184:187], v49 offset:6144\n" \
"v_mfma_f32_16x16x32_bf16 v[68:71], a[152:155], a[72:75], v[68:71]\n" \
"v_exp_f32_e32 v93, v93\n" \
"v_mfma_f32_16x16x32_bf16 v[68:71], a[148:151], a[68:71], v[68:71]\n" \
"ds_read_b128 a[188:191], v49 offset:7168\n" \
"ds_read_b128 a[172:175], v49 offset:3072\n" \
"v_mfma_f32_16x16x32_bf16 v[68:71], a[144:147], a[64:67], v[68:71]\n" \
"v_exp_f32_e32 v94, v94\n" \
"v_mfma_f32_16x16x32_bf16 v[72:75], a[128:131], a[80:83], v[36:39]\n" \
"v_exp_f32_e32 v95, v95\n" \
"v_mfma_f32_16x16x32_bf16 v[72:75], a[132:135], a[84:87], v[72:75]\n" \
"v_cvt_pk_bf16_f32 v122, v92, v93\n" \
"v_cvt_pk_bf16_f32 v123, v94, v95\n" \
"v_mfma_f32_16x16x32_bf16 v[72:75], a[136:139], a[88:91], v[72:75]\n" \
"v_exp_f32_e32 v64, v64\n" \
"v_mfma_f32_16x16x32_bf16 v[72:75], a[140:143], a[92:95], v[72:75]\n" \
"v_exp_f32_e32 v65, v65\n" \
"v_mfma_f32_16x16x32_bf16 v[76:79], a[156:159], a[92:95], v[36:39]\n" \
"v_exp_f32_e32 v66, v66\n" \
"v_mfma_f32_16x16x32_bf16 v[76:79], a[152:155], a[88:91], v[76:79]\n" \
"v_exp_f32_e32 v67, v67\n" \
"v_mfma_f32_16x16x32_bf16 v[76:79], a[148:151], a[84:87], v[76:79]\n" \
"v_exp_f32_e32 v68, v68\n" \
"v_mfma_f32_16x16x32_bf16 v[76:79], a[144:147], a[80:83], v[76:79]\n" \
"v_exp_f32_e32 v69, v69\n" \
"v_mfma_f32_16x16x32_bf16 v[80:83], a[128:131], a[96:99], v[40:43]\n" \
"v_exp_f32_e32 v70, v70\n" \
"v_mfma_f32_16x16x32_bf16 v[80:83], a[132:135], a[100:103], v[80:83]\n" \
"v_exp_f32_e32 v71, v71\n" \
"v_mfma_f32_16x16x32_bf16 v[80:83], a[136:139], a[104:107], v[80:83]\n" \
"v_cvt_pk_bf16_f32 v100, v64, v65\n" \
"v_cvt_pk_bf16_f32 v101, v66, v67\n" \
"v_mfma_f32_16x16x32_bf16 v[80:83], a[140:143], a[108:111], v[80:83]\n" \
"v_cvt_pk_bf16_f32 v102, v68, v69\n" \
"v_cvt_pk_bf16_f32 v103, v70, v71\n" \
"v_mfma_f32_16x16x32_bf16 v[84:87], a[156:159], a[108:111], v[40:43]\n" \
"v_exp_f32_e32 v72, v72\n" \
"v_mfma_f32_16x16x32_bf16 v[84:87], a[152:155], a[104:107], v[84:87]\n" \
"v_exp_f32_e32 v73, v73\n" \
"v_mfma_f32_16x16x32_bf16 v[84:87], a[148:151], a[100:103], v[84:87]\n" \
"v_exp_f32_e32 v74, v74\n" \
"v_mfma_f32_16x16x32_bf16 v[84:87], a[144:147], a[96:99], v[84:87]\n" \
"v_exp_f32_e32 v75, v75\n" \
"v_mfma_f32_16x16x32_bf16 v[88:91], a[128:131], a[112:115], v[44:47]\n" \
"v_exp_f32_e32 v76, v76\n" \
"v_mfma_f32_16x16x32_bf16 v[88:91], a[132:135], a[116:119], v[88:91]\n" \
"v_exp_f32_e32 v77, v77\n" \
"v_mfma_f32_16x16x32_bf16 v[88:91], a[136:139], a[120:123], v[88:91]\n" \
"v_exp_f32_e32 v78, v78\n" \
"v_mfma_f32_16x16x32_bf16 v[88:91], a[140:143], a[124:127], v[88:91]\n" \
"v_exp_f32_e32 v79, v79\n" \
"v_mfma_f32_16x16x32_bf16 v[92:95], a[156:159], a[124:127], v[44:47]\n" \
"v_cvt_pk_bf16_f32 v108, v72, v73\n" \
"v_cvt_pk_bf16_f32 v109, v74, v75\n" \
"v_mfma_f32_16x16x32_bf16 v[92:95], a[152:155], a[120:123], v[92:95]\n" \
"v_cvt_pk_bf16_f32 v110, v76, v77\n" \
"v_cvt_pk_bf16_f32 v111, v78, v79\n" \
"v_mfma_f32_16x16x32_bf16 v[92:95], a[148:151], a[116:119], v[92:95]\n" \
"ds_read_b128 a[128:131], v48 offset:0\n" \
"ds_read_b128 a[132:135], v48 offset:1024\n" \
"v_mfma_f32_16x16x32_bf16 v[92:95], a[144:147], a[112:115], v[92:95]\n" \
"s_waitcnt lgkmcnt(2)\n" \
"v_mfma_f32_16x16x32_bf16 v[128:131], a[160:163], v[96:99], v[128:131]\n" \
"ds_read_b128 a[136:139], v48 offset:2048\n" \
"ds_read_b128 a[140:143], v48 offset:3072\n" \
"v_mfma_f32_16x16x32_bf16 v[128:131], a[176:179], v[100:103], v[128:131]\n" \
"v_exp_f32_e32 v80, v80\n" \
"v_mfma_f32_16x16x32_bf16 v[132:135], a[180:183], v[100:103], v[132:135]\n" \
"ds_read_b128 a[144:147], v48 offset:4096\n" \
"ds_read_b128 a[148:151], v48 offset:5120\n" \
"v_mfma_f32_16x16x32_bf16 v[132:135], a[164:167], v[96:99], v[132:135]\n" \
"v_exp_f32_e32 v81, v81\n" \
"v_mfma_f32_16x16x32_bf16 v[136:139], a[168:171], v[96:99], v[136:139]\n" \
"ds_read_b128 a[152:155], v48 offset:6144\n" \
"ds_read_b128 a[156:159], v48 offset:7168\n" \
"v_mfma_f32_16x16x32_bf16 v[136:139], a[184:187], v[100:103], v[136:139]\n" \
"v_exp_f32_e32 v82, v82\n" \
"v_mfma_f32_16x16x32_bf16 v[140:143], a[188:191], v[100:103], v[140:143]\n" \
"v_exp_f32_e32 v83, v83\n" \
"v_mfma_f32_16x16x32_bf16 v[140:143], a[172:175], v[96:99], v[140:143]\n" \
"v_exp_f32_e32 v84, v84\n" \
"v_smfmac_f32_16x16x64_bf16 a[192:195], a[212:215], v[96:103], v21\n" \
"v_mfma_f32_16x16x32_bf16 v[160:163], a[160:163], v[104:107], v[160:163]\n" \
"v_exp_f32_e32 v85, v85\n" \
"v_mfma_f32_16x16x32_bf16 v[160:163], a[176:179], v[108:111], v[160:163]\n" \
"v_exp_f32_e32 v86, v86\n" \
"v_mfma_f32_16x16x32_bf16 v[164:167], a[180:183], v[108:111], v[164:167]\n" \
"v_exp_f32_e32 v87, v87\n" \
"v_mfma_f32_16x16x32_bf16 v[164:167], a[164:167], v[104:107], v[164:167]\n" \
"v_cvt_pk_bf16_f32 v116, v80, v81\n" \
"v_cvt_pk_bf16_f32 v117, v82, v83\n" \
"v_mfma_f32_16x16x32_bf16 v[168:171], a[168:171], v[104:107], v[168:171]\n" \
"v_cvt_pk_bf16_f32 v118, v84, v85\n" \
"v_cvt_pk_bf16_f32 v119, v86, v87\n" \
"v_mfma_f32_16x16x32_bf16 v[168:171], a[184:187], v[108:111], v[168:171]\n" \
"v_exp_f32_e32 v88, v88\n" \
"v_mfma_f32_16x16x32_bf16 v[172:175], a[188:191], v[108:111], v[172:175]\n" \
"v_exp_f32_e32 v89, v89\n" \
"v_mfma_f32_16x16x32_bf16 v[172:175], a[172:175], v[104:107], v[172:175]\n" \
"v_exp_f32_e32 v90, v90\n" \
"v_smfmac_f32_16x16x64_bf16 a[196:199], a[212:215], v[104:111], v21\n" \
"v_mfma_f32_16x16x32_bf16 v[192:195], a[160:163], v[112:115], v[192:195]\n" \
"v_exp_f32_e32 v91, v91\n" \
"v_mfma_f32_16x16x32_bf16 v[192:195], a[176:179], v[116:119], v[192:195]\n" \
"v_exp_f32_e32 v92, v92\n" \
"v_mfma_f32_16x16x32_bf16 v[196:199], a[180:183], v[116:119], v[196:199]\n" \
"v_exp_f32_e32 v93, v93\n" \
"v_mfma_f32_16x16x32_bf16 v[196:199], a[164:167], v[112:115], v[196:199]\n" \
"v_exp_f32_e32 v94, v94\n" \
"v_mfma_f32_16x16x32_bf16 v[200:203], a[168:171], v[112:115], v[200:203]\n" \
"v_exp_f32_e32 v95, v95\n" \
"v_mfma_f32_16x16x32_bf16 v[200:203], a[184:187], v[116:119], v[200:203]\n" \
"v_cvt_pk_bf16_f32 v124, v88, v89\n" \
"v_cvt_pk_bf16_f32 v125, v90, v91\n" \
"v_mfma_f32_16x16x32_bf16 v[204:207], a[188:191], v[116:119], v[204:207]\n" \
"v_cvt_pk_bf16_f32 v126, v92, v93\n" \
"v_cvt_pk_bf16_f32 v127, v94, v95\n" \
"v_mfma_f32_16x16x32_bf16 v[204:207], a[172:175], v[112:115], v[204:207]\n" \
"v_smfmac_f32_16x16x64_bf16 a[200:203], a[212:215], v[112:119], v21\n" \
"s_waitcnt vmcnt(0) lgkmcnt(0)\n" \
"s_barrier\n" \
"v_mfma_f32_16x16x32_bf16 v[224:227], a[160:163], v[120:123], v[224:227]\n" \
"s_add_u32 s63, s63, s63\n" \
"s_cbranch_scc1 L_h2_%=\n" \
"L_hr2_%=:\n" \
"v_mfma_f32_16x16x32_bf16 v[224:227], a[176:179], v[124:127], v[224:227]\n" \
"s_xor_b32 m0, s65, 0x12000\n" \
"ds_read_b128 a[160:163], v49 offset:8192\n" \
"v_mfma_f32_16x16x32_bf16 v[228:231], a[180:183], v[124:127], v[228:231]\n" \
"buffer_load_dwordx4 v50, s[8:11], s64 offen offset:0 sc1 lds\n" \
"v_mfma_f32_16x16x32_bf16 v[228:231], a[164:167], v[120:123], v[228:231]\n" \
"ds_read_b128 a[176:179], v49 offset:12288\n" \
"ds_read_b128 a[180:183], v49 offset:13312\n" \
"v_mfma_f32_16x16x32_bf16 v[232:235], a[168:171], v[120:123], v[232:235]\n" \
"v_mfma_f32_16x16x32_bf16 v[232:235], a[184:187], v[124:127], v[232:235]\n" \
"buffer_load_dwordx4 v50, s[8:11], s64 offen offset:1024 sc1 lds\n" \
"v_mfma_f32_16x16x32_bf16 v[236:239], a[188:191], v[124:127], v[236:239]\n" \
"ds_read_b128 a[164:167], v49 offset:9216\n" \
"ds_read_b128 a[168:171], v49 offset:10240\n" \
"v_mfma_f32_16x16x32_bf16 v[236:239], a[172:175], v[120:123], v[236:239]\n" \
"v_mfma_f32_16x16x32_bf16 v[64:67], a[128:131], a[64:67], v[32:35]\n" \
"ds_read_b128 a[184:187], v49 offset:14336\n" \
"ds_read_b128 a[188:191], v49 offset:15360\n" \
"v_mfma_f32_16x16x32_bf16 v[64:67], a[132:135], a[68:71], v[64:67]\n" \
"buffer_load_dwordx4 v50, s[8:11], s64 offen offset:2048 sc1 lds\n" \
"v_mfma_f32_16x16x32_bf16 v[64:67], a[136:139], a[72:75], v[64:67]\n" \
"ds_read_b128 a[172:175], v49 offset:11264\n" \
"v_add_u32_e32 v49, 0x10000, v48\n" \
"v_mfma_f32_16x16x32_bf16 v[64:67], a[140:143], a[76:79], v[64:67]\n" \
"v_mfma_f32_16x16x32_bf16 v[68:71], a[156:159], a[76:79], v[32:35]\n" \
"v_mfma_f32_16x16x32_bf16 v[68:71], a[152:155], a[72:75], v[68:71]\n" \
"v_mfma_f32_16x16x32_bf16 v[68:71], a[148:151], a[68:71], v[68:71]\n" \
"buffer_load_dwordx4 v50, s[8:11], s64 offen offset:3072 sc1 lds\n" \
"v_mfma_f32_16x16x32_bf16 v[68:71], a[144:147], a[64:67], v[68:71]\n" \
"s_xor_b32 m0, s65, 0xa000\n" \
"v_mfma_f32_16x16x32_bf16 v[72:75], a[128:131], a[80:83], v[36:39]\n" \
"v_mfma_f32_16x16x32_bf16 v[72:75], a[132:135], a[84:87], v[72:75]\n" \
"v_exp_f32_e32 v64, v64\n" \
"v_mfma_f32_16x16x32_bf16 v[72:75], a[136:139], a[88:91], v[72:75]\n" \
"buffer_load_dwordx4 v51, s[8:11], s64 offen offset:0 sc1 lds\n" \
"v_mfma_f32_16x16x32_bf16 v[72:75], a[140:143], a[92:95], v[72:75]\n" \
"v_exp_f32_e32 v65, v65\n" \
"v_mfma_f32_16x16x32_bf16 v[76:79], a[156:159], a[92:95], v[36:39]\n" \
"v_exp_f32_e32 v66, v66\n" \
"v_mfma_f32_16x16x32_bf16 v[76:79], a[152:155], a[88:91], v[76:79]\n" \
"v_exp_f32_e32 v67, v67\n" \
"v_mfma_f32_16x16x32_bf16 v[76:79], a[148:151], a[84:87], v[76:79]\n" \
"buffer_load_dwordx4 v51, s[8:11], s64 offen offset:1024 sc1 lds\n" \
"v_mfma_f32_16x16x32_bf16 v[76:79], a[144:147], a[80:83], v[76:79]\n" \
"v_exp_f32_e32 v68, v68\n" \
"v_mfma_f32_16x16x32_bf16 v[80:83], a[128:131], a[96:99], v[40:43]\n" \
"v_exp_f32_e32 v69, v69\n" \
"v_mfma_f32_16x16x32_bf16 v[80:83], a[132:135], a[100:103], v[80:83]\n" \
"v_exp_f32_e32 v70, v70\n" \
"v_mfma_f32_16x16x32_bf16 v[80:83], a[136:139], a[104:107], v[80:83]\n" \
"v_exp_f32_e32 v71, v71\n" \
"v_mfma_f32_16x16x32_bf16 v[80:83], a[140:143], a[108:111], v[80:83]\n" \
"buffer_load_dwordx4 v51, s[8:11], s64 offen offset:2048 sc1 lds\n" \
"v_mfma_f32_16x16x32_bf16 v[84:87], a[156:159], a[108:111], v[40:43]\n" \
"v_exp_f32_e32 v72, v72\n" \
"v_mfma_f32_16x16x32_bf16 v[84:87], a[152:155], a[104:107], v[84:87]\n" \
"v_exp_f32_e32 v73, v73\n" \
"v_mfma_f32_16x16x32_bf16 v[84:87], a[148:151], a[100:103], v[84:87]\n" \
"v_exp_f32_e32 v74, v74\n" \
"v_mfma_f32_16x16x32_bf16 v[84:87], a[144:147], a[96:99], v[84:87]\n" \
"buffer_load_dwordx4 v51, s[8:11], s64 offen offset:3072 sc1 lds\n" \
"v_mfma_f32_16x16x32_bf16 v[88:91], a[128:131], a[112:115], v[44:47]\n" \
"s_xor_b32 m0, s65, 0x18000\n" \
"ds_read_b128 a[128:131], v48 offset:8192\n" \
"v_mfma_f32_16x16x32_bf16 v[88:91], a[132:135], a[116:119], v[88:91]\n" \
"v_exp_f32_e32 v75, v75\n" \
"v_mfma_f32_16x16x32_bf16 v[88:91], a[136:139], a[120:123], v[88:91]\n" \
"ds_read_b128 a[132:135], v48 offset:9216\n" \
"ds_read_b128 a[136:139], v48 offset:10240\n" \
"v_mfma_f32_16x16x32_bf16 v[88:91], a[140:143], a[124:127], v[88:91]\n" \
"v_exp_f32_e32 v76, v76\n" \
"v_mfma_f32_16x16x32_bf16 v[92:95], a[156:159], a[124:127], v[44:47]\n" \
"buffer_load_dwordx4 v52, s[8:11], s64 offen offset:0 sc1 lds\n" \
"v_mfma_f32_16x16x32_bf16 v[92:95], a[152:155], a[120:123], v[92:95]\n" \
"ds_read_b128 a[140:143], v48 offset:11264\n" \
"ds_read_b128 a[152:155], v48 offset:14336\n" \
"v_mfma_f32_16x16x32_bf16 v[92:95], a[148:151], a[116:119], v[92:95]\n" \
"v_exp_f32_e32 v77, v77\n" \
"v_mfma_f32_16x16x32_bf16 v[92:95], a[144:147], a[112:115], v[92:95]\n" \
"ds_read_b128 a[144:147], v48 offset:12288\n" \
"s_waitcnt lgkmcnt(6)\n" \
"v_mfma_f32_16x16x32_bf16 v[144:147], a[160:163], v[96:99], v[144:147]\n" \
"buffer_load_dwordx4 v52, s[8:11], s64 offen offset:1024 sc1 lds\n" \
"v_mfma_f32_16x16x32_bf16 v[144:147], a[176:179], v[100:103], v[144:147]\n" \
"ds_read_b128 a[148:151], v48 offset:13312\n" \
"ds_read_b128 a[156:159], v48 offset:15360\n" \
"v_mfma_f32_16x16x32_bf16 v[148:151], a[180:183], v[100:103], v[148:151]\n" \
"v_exp_f32_e32 v78, v78\n" \
"v_mfma_f32_16x16x32_bf16 v[148:151], a[164:167], v[96:99], v[148:151]\n" \
"v_exp_f32_e32 v79, v79\n" \
"v_mfma_f32_16x16x32_bf16 v[152:155], a[168:171], v[96:99], v[152:155]\n" \
"buffer_load_dwordx4 v52, s[8:11], s64 offen offset:2048 sc1 lds\n" \
"v_mfma_f32_16x16x32_bf16 v[152:155], a[184:187], v[100:103], v[152:155]\n" \
"v_exp_f32_e32 v80, v80\n" \
"v_mfma_f32_16x16x32_bf16 v[156:159], a[188:191], v[100:103], v[156:159]\n" \
"v_exp_f32_e32 v81, v81\n" \
"v_mfma_f32_16x16x32_bf16 v[156:159], a[172:175], v[96:99], v[156:159]\n" \
"v_exp_f32_e32 v82, v82\n" \
"v_mfma_f32_16x16x32_bf16 v[176:179], a[160:163], v[104:107], v[176:179]\n" \
"v_exp_f32_e32 v83, v83\n" \
"v_mfma_f32_16x16x32_bf16 v[176:179], a[176:179], v[108:111], v[176:179]\n" \
"buffer_load_dwordx4 v52, s[8:11], s64 offen offset:3072 sc1 lds\n" \
"v_mfma_f32_16x16x32_bf16 v[180:183], a[180:183], v[108:111], v[180:183]\n" \
"s_mov_b32 m0, s65\n" \
"s_xor_b32 s65, s65, 0x8000\n" \
"v_mfma_f32_16x16x32_bf16 v[180:183], a[164:167], v[104:107], v[180:183]\n" \
"v_exp_f32_e32 v84, v84\n" \
"v_mfma_f32_16x16x32_bf16 v[184:187], a[168:171], v[104:107], v[184:187]\n" \
"v_exp_f32_e32 v85, v85\n" \
"v_mfma_f32_16x16x32_bf16 v[184:187], a[184:187], v[108:111], v[184:187]\n" \
"buffer_load_dwordx4 v53, s[8:11], s64 offen offset:0 sc1 lds\n" \
"v_mfma_f32_16x16x32_bf16 v[188:191], a[188:191], v[108:111], v[188:191]\n" \
"v_exp_f32_e32 v86, v86\n" \
"v_mfma_f32_16x16x32_bf16 v[188:191], a[172:175], v[104:107], v[188:191]\n" \
"v_exp_f32_e32 v87, v87\n" \
"v_mfma_f32_16x16x32_bf16 v[208:211], a[160:163], v[112:115], v[208:211]\n" \
"v_exp_f32_e32 v88, v88\n" \
"v_mfma_f32_16x16x32_bf16 v[208:211], a[176:179], v[116:119], v[208:211]\n" \
"buffer_load_dwordx4 v53, s[8:11], s64 offen offset:1024 sc1 lds\n" \
"v_mfma_f32_16x16x32_bf16 v[212:215], a[180:183], v[116:119], v[212:215]\n" \
"v_exp_f32_e32 v89, v89\n" \
"v_mfma_f32_16x16x32_bf16 v[212:215], a[164:167], v[112:115], v[212:215]\n" \
"v_exp_f32_e32 v90, v90\n" \
"v_mfma_f32_16x16x32_bf16 v[216:219], a[168:171], v[112:115], v[216:219]\n" \
"v_exp_f32_e32 v91, v91\n" \
"v_mfma_f32_16x16x32_bf16 v[216:219], a[184:187], v[116:119], v[216:219]\n" \
"v_cvt_pk_bf16_f32 v96, v64, v65\n" \
"v_cvt_pk_bf16_f32 v97, v66, v67\n" \
"v_mfma_f32_16x16x32_bf16 v[220:223], a[188:191], v[116:119], v[220:223]\n" \
"buffer_load_dwordx4 v53, s[8:11], s64 offen offset:2048 sc1 lds\n" \
"v_mfma_f32_16x16x32_bf16 v[220:223], a[172:175], v[112:115], v[220:223]\n" \
"v_cvt_pk_bf16_f32 v98, v68, v69\n" \
"v_cvt_pk_bf16_f32 v99, v70, v71\n" \
"v_mfma_f32_16x16x32_bf16 v[240:243], a[160:163], v[120:123], v[240:243]\n" \
"v_cvt_pk_bf16_f32 v104, v72, v73\n" \
"v_cvt_pk_bf16_f32 v105, v74, v75\n" \
"v_mfma_f32_16x16x32_bf16 v[240:243], a[176:179], v[124:127], v[240:243]\n" \
"v_cvt_pk_bf16_f32 v106, v76, v77\n" \
"v_cvt_pk_bf16_f32 v107, v78, v79\n" \
"v_mfma_f32_16x16x32_bf16 v[244:247], a[180:183], v[124:127], v[244:247]\n" \
"buffer_load_dwordx4 v53, s[8:11], s64 offen offset:3072 sc1 lds\n" \
"v_mfma_f32_16x16x32_bf16 v[244:247], a[164:167], v[120:123], v[244:247]\n" \
"s_add_u32 s64, s64, 0x10000\n" \
"s_sub_u32 s66, s66, 1\n" \
"v_mfma_f32_16x16x32_bf16 v[248:251], a[168:171], v[120:123], v[248:251]\n" \
"v_cvt_pk_bf16_f32 v112, v80, v81\n" \
"v_cvt_pk_bf16_f32 v113, v82, v83\n" \
"v_mfma_f32_16x16x32_bf16 v[248:251], a[184:187], v[124:127], v[248:251]\n" \
"s_cbranch_scc0 L_t2_%=\n" \
"v_mfma_f32_16x16x32_bf16 v[252:255], a[188:191], v[124:127], v[252:255]\n" \
"v_cvt_pk_bf16_f32 v114, v84, v85\n" \
"v_cvt_pk_bf16_f32 v115, v86, v87\n" \
"v_mfma_f32_16x16x32_bf16 v[252:255], a[172:175], v[120:123], v[252:255]\n" \
"v_xor_b32_e32 v48, 0x8000, v48\n" \
"v_smfmac_f32_16x16x64_bf16 a[204:207], a[212:215], v[120:127], v21\n" \
"s_waitcnt lgkmcnt(0)\n" \
"s_waitcnt vmcnt(12)\n" \
"s_barrier\n" \
"s_cmp_lg_u32 s16, 0\n" \
"s_cbranch_scc1 L_f1_%=\n" \
"ds_read_b128 a[160:163], v49 offset:0\n" \
"ds_read_b128 a[164:167], v49 offset:1024\n" \
"ds_read_b128 a[168:171], v49 offset:2048\n" \
"ds_read_b128 a[172:175], v49 offset:3072\n" \
"ds_read_b128 a[176:179], v49 offset:8192\n" \
"ds_read_b128 a[180:183], v49 offset:9216\n" \
"v_cvt_pk_bf16_f32 v120, v88, v89\n" \
"v_cvt_pk_bf16_f32 v121, v90, v91\n" \
"v_mfma_f32_16x16x32_bf16 v[88:91], a[128:131], a[112:115], v[44:47]\n" \
"s_add_u32 s70, s61, 0x0\n" \
"s_add_u32 s71, s61, 0x49000\n" \
"ds_read_b128 a[184:187], v49 offset:10240\n" \
"v_mfma_f32_16x16x32_bf16 v[88:91], a[132:135], a[116:119], v[88:91]\n" \
"ds_read_b128 a[188:191], v49 offset:11264\n" \
"v_add_u32_e32 v54, 0x49000, v20\n" \
"v_mfma_f32_16x16x32_bf16 v[88:91], a[136:139], a[120:123], v[88:91]\n" \
"v_exp_f32_e32 v92, v92\n" \
"v_mfma_f32_16x16x32_bf16 v[88:91], a[140:143], a[124:127], v[88:91]\n" \
"v_exp_f32_e32 v93, v93\n" \
"v_exp_f32_e32 v94, v94\n" \
"v_exp_f32_e32 v95, v95\n" \
"v_cvt_pk_bf16_f32 v122, v92, v93\n" \
"v_cvt_pk_bf16_f32 v123, v94, v95\n" \
"v_mfma_f32_16x16x32_bf16 v[92:95], a[156:159], a[124:127], v[44:47]\n" \
"buffer_load_dwordx4 a[220:223], v20, s[56:59], s60 offen offset:0 sc1\n" \
"v_mfma_f32_16x16x32_bf16 v[92:95], a[152:155], a[120:123], v[92:95]\n" \
"v_readlane_b32 s97, v11, 32\n" \
"v_readlane_b32 s98, v11, 33\n" \
"v_mfma_f32_16x16x32_bf16 v[92:95], a[148:151], a[116:119], v[92:95]\n" \
"v_and_b32_e32 v112, v12, v112\n" \
"v_and_b32_e32 v113, v13, v113\n" \
"v_mfma_f32_16x16x32_bf16 v[92:95], a[144:147], a[112:115], v[92:95]\n" \
"v_and_b32_e32 v114, v14, v114\n" \
"s_waitcnt lgkmcnt(7)\n" \
"v_mfma_f32_16x16x32_bf16 v[128:131], a[160:163], v[96:99], v[128:131]\n" \
"v_and_b32_e32 v115, v15, v115\n" \
"s_waitcnt lgkmcnt(6)\n" \
"v_mfma_f32_16x16x32_bf16 v[132:135], a[164:167], v[96:99], v[132:135]\n" \
"ds_read_b32 v60, v18 offset:0\n" \
"s_waitcnt lgkmcnt(6)\n" \
"v_mfma_f32_16x16x32_bf16 v[136:139], a[168:171], v[96:99], v[136:139]\n" \
"ds_read_b32 v61, v18 offset:128\n" \
"s_waitcnt lgkmcnt(6)\n" \
"v_mfma_f32_16x16x32_bf16 v[140:143], a[172:175], v[96:99], v[140:143]\n" \
"ds_read_b128 a[0:3], v49 offset:4096\n" \
"s_waitcnt lgkmcnt(5)\n" \
"v_mfma_f32_16x16x32_bf16 v[144:147], a[176:179], v[96:99], v[144:147]\n" \
"buffer_load_dwordx4 a[236:239], v54, s[56:59], s60 offen offset:0 sc1\n" \
"v_mfma_f32_16x16x32_bf16 v[148:151], a[180:183], v[96:99], v[148:151]\n" \
"ds_read_b128 a[4:7], v49 offset:5120\n" \
"s_waitcnt lgkmcnt(5)\n" \
"v_mfma_f32_16x16x32_bf16 v[152:155], a[184:187], v[96:99], v[152:155]\n" \
"ds_read_b128 a[8:11], v49 offset:6144\n" \
"s_waitcnt lgkmcnt(5)\n" \
"v_mfma_f32_16x16x32_bf16 v[156:159], a[188:191], v[96:99], v[156:159]\n" \
"buffer_load_dwordx4 a[224:227], v20, s[56:59], s60 offen offset:1024 sc1\n" \
"v_mfma_f32_16x16x32_bf16 a[192:195], a[252:255], v[96:99], a[192:195]\n" \
"ds_read_b128 a[12:15], v49 offset:7168\n" \
"ds_read_b128 a[16:19], v49 offset:12288\n" \
"v_mfma_f32_16x16x32_bf16 v[160:163], a[160:163], v[104:107], v[160:163]\n" \
"ds_read_b128 a[20:23], v49 offset:13312\n" \
"ds_read_b128 a[24:27], v49 offset:14336\n" \
"buffer_load_dwordx4 a[240:243], v54, s[56:59], s60 offen offset:1024 sc1\n" \
"v_mfma_f32_16x16x32_bf16 v[164:167], a[164:167], v[104:107], v[164:167]\n" \
"ds_read_b128 a[28:31], v49 offset:15360\n" \
"v_accvgpr_read_b32 v40, a192\n" \
"v_mfma_f32_16x16x32_bf16 v[168:171], a[168:171], v[104:107], v[168:171]\n" \
"v_accvgpr_read_b32 v23, a193\n" \
"v_add_f32_e32 v40, v40, v23\n" \
"v_mfma_f32_16x16x32_bf16 v[172:175], a[172:175], v[104:107], v[172:175]\n" \
"buffer_load_dwordx4 a[228:231], v20, s[56:59], s60 offen offset:2048 sc1\n" \
"v_mfma_f32_16x16x32_bf16 v[176:179], a[176:179], v[104:107], v[176:179]\n" \
"v_add_u32_e32 v50, s15, v48\n" \
"v_add_u32_e32 v51, 0x10000, v50\n" \
"v_mfma_f32_16x16x32_bf16 v[180:183], a[180:183], v[104:107], v[180:183]\n" \
"s_waitcnt vmcnt(5)\n" \
"ds_write_b128 v50, v[128:131] offset:0\n" \
"buffer_load_dwordx4 a[244:247], v54, s[56:59], s60 offen offset:2048 sc1\n" \
"v_mfma_f32_16x16x32_bf16 v[184:187], a[184:187], v[104:107], v[184:187]\n" \
"ds_write_b128 v50, v[132:135] offset:1024\n" \
"v_mfma_f32_16x16x32_bf16 v[188:191], a[188:191], v[104:107], v[188:191]\n" \
"ds_write_b128 v50, v[136:139] offset:2048\n" \
"buffer_load_dwordx4 a[232:235], v20, s[56:59], s60 offen offset:3072 sc1\n" \
"v_mfma_f32_16x16x32_bf16 a[196:199], a[252:255], v[104:107], a[196:199]\n" \
"ds_write_b128 v50, v[140:143] offset:3072\n" \
"v_mfma_f32_16x16x32_bf16 v[192:195], a[160:163], v[112:115], v[192:195]\n" \
"ds_write_b128 v50, v[144:147] offset:8192\n" \
"buffer_load_dwordx4 a[248:251], v54, s[56:59], s60 offen offset:3072 sc1\n" \
"v_mfma_f32_16x16x32_bf16 v[196:199], a[164:167], v[112:115], v[196:199]\n" \
"ds_write_b128 v50, v[148:151] offset:9216\n" \
"v_mfma_f32_16x16x32_bf16 v[200:203], a[168:171], v[112:115], v[200:203]\n" \
"ds_write_b128 v50, v[152:155] offset:10240\n" \
"buffer_load_dwordx4 a[32:35], v20, s[56:59], s97 offen offset:0 sc1\n" \
"v_mfma_f32_16x16x32_bf16 v[204:207], a[172:175], v[112:115], v[204:207]\n" \
"v_accvgpr_read_b32 v41, a196\n" \
"v_accvgpr_read_b32 v23, a197\n" \
"v_mfma_f32_16x16x32_bf16 v[208:211], a[176:179], v[112:115], v[208:211]\n" \
"v_add_f32_e32 v41, v41, v23\n" \
"v_xor_b32_e32 v52, 0x4000, v50\n" \
"v_mfma_f32_16x16x32_bf16 v[212:215], a[180:183], v[112:115], v[212:215]\n" \
"buffer_load_dwordx4 a[40:43], v54, s[56:59], s97 offen offset:0 sc1\n" \
"v_mfma_f32_16x16x32_bf16 v[216:219], a[184:187], v[112:115], v[216:219]\n" \
"ds_write_b128 v50, v[156:159] offset:11264\n" \
"v_mfma_f32_16x16x32_bf16 v[220:223], a[188:191], v[112:115], v[220:223]\n" \
"ds_write_b128 v51, v[160:163] offset:0\n" \
"buffer_load_dwordx4 a[48:51], v20, s[56:59], s98 offen offset:0 sc1\n" \
"v_mfma_f32_16x16x32_bf16 a[200:203], a[252:255], v[112:115], a[200:203]\n" \
"ds_write_b128 v51, v[164:167] offset:1024\n" \
"ds_write_b128 v51, v[168:171] offset:2048\n" \
"ds_write_b128 v51, v[172:175] offset:3072\n" \
"buffer_load_dwordx4 a[56:59], v54, s[56:59], s98 offen offset:0 sc1\n" \
"ds_write_b128 v51, v[176:179] offset:8192\n" \
"ds_write_b128 v51, v[180:183] offset:9216\n" \
"ds_write_b128 v51, v[184:187] offset:10240\n" \
"ds_write_b128 v51, v[188:191] offset:11264\n" \
"buffer_load_dwordx4 a[36:39], v20, s[56:59], s97 offen offset:1024 sc1\n" \
"ds_write_b64 v19, v[40:41]\n" \
"v_xor_b32_e32 v53, 0x4000, v51\n" \
"v_xor_b32_e32 v63, 0x400, v19\n" \
"v_readlane_b32 s93, v11, 18\n" \
"v_readlane_b32 s84, v11, 13\n" \
"v_readlane_b32 s85, v11, 14\n" \
"v_readlane_b32 s86, v11, 15\n" \
"s_mov_b32 s87, 0x20000\n" \
"v_readlane_b32 s92, v11, 16\n" \
"s_add_u32 m0, s93, 0x0\n" \
"v_exp_f32_e32 v88, v88\n" \
"v_exp_f32_e32 v89, v89\n" \
"v_exp_f32_e32 v90, v90\n" \
"v_exp_f32_e32 v91, v91\n" \
"v_exp_f32_e32 v92, v92\n" \
"v_exp_f32_e32 v93, v93\n" \
"v_exp_f32_e32 v94, v94\n" \
"v_exp_f32_e32 v95, v95\n" \
"v_cvt_pk_bf16_f32 v124, v88, v89\n" \
"v_cvt_pk_bf16_f32 v125, v90, v91\n" \
"v_cvt_pk_bf16_f32 v126, v92, v93\n" \
"v_cvt_pk_bf16_f32 v127, v94, v95\n" \
"v_and_b32_e32 v124, v12, v124\n" \
"v_and_b32_e32 v125, v13, v125\n" \
"v_and_b32_e32 v126, v14, v126\n" \
"v_and_b32_e32 v127, v15, v127\n" \
"v_accvgpr_read_b32 v56, a200\n" \
"v_accvgpr_read_b32 v23, a201\n" \
"v_add_f32_e32 v56, v56, v23\n" \
"s_waitcnt lgkmcnt(0)\n" \
"s_barrier\n" \
"buffer_load_dwordx4 a[44:47], v54, s[56:59], s97 offen offset:1024 sc1\n" \
"ds_read_b128 v[128:131], v52 offset:0\n" \
"ds_read_b128 v[132:135], v52 offset:1024\n" \
"ds_read_b128 v[136:139], v52 offset:2048\n" \
"ds_read_b128 v[140:143], v52 offset:3072\n" \
"ds_read_b128 v[144:147], v52 offset:8192\n" \
"ds_read_b128 v[148:151], v52 offset:9216\n" \
"ds_read_b128 v[152:155], v52 offset:10240\n" \
"ds_read_b128 v[156:159], v52 offset:11264\n" \
"buffer_load_dwordx4 a[52:55], v20, s[56:59], s98 offen offset:1024 sc1\n" \
"ds_read_b128 v[160:163], v53 offset:0\n" \
"ds_read_b128 v[164:167], v53 offset:1024\n" \
"ds_read_b128 v[168:171], v53 offset:2048\n" \
"ds_read_b128 v[172:175], v53 offset:3072\n" \
"ds_read_b128 v[176:179], v53 offset:8192\n" \
"ds_read_b128 v[180:183], v53 offset:9216\n" \
"ds_read_b128 v[184:187], v53 offset:10240\n" \
"ds_read_b128 v[188:191], v53 offset:11264\n" \
"buffer_load_dwordx4 a[60:63], v54, s[56:59], s98 offen offset:1024 sc1\n" \
"ds_read_b64 v[42:43], v63\n" \
"v_mfma_f32_16x16x32_bf16 v[224:227], a[160:163], v[120:123], v[224:227]\n" \
"v_mfma_f32_16x16x32_bf16 v[228:231], a[164:167], v[120:123], v[228:231]\n" \
"v_mfma_f32_16x16x32_bf16 v[232:235], a[168:171], v[120:123], v[232:235]\n" \
"buffer_load_dwordx4 v0, s[84:87], s92 offen offset:0 lds\n" \
"v_mfma_f32_16x16x32_bf16 v[236:239], a[172:175], v[120:123], v[236:239]\n" \
"v_mfma_f32_16x16x32_bf16 v[240:243], a[176:179], v[120:123], v[240:243]\n" \
"v_mfma_f32_16x16x32_bf16 v[244:247], a[180:183], v[120:123], v[244:247]\n" \
"L_pf13:\n" \
"s_cbranch_execz L_pf17\n" \
"s_nop 0\n" \
"buffer_load_dwordx4 v1, s[84:87], s92 offen offset:1024 lds\n" \
"v_mfma_f32_16x16x32_bf16 v[248:251], a[184:187], v[120:123], v[248:251]\n" \
"s_waitcnt lgkmcnt(15)\n" \
"v_add_f32_e32 v192, v192, v128\n" \
"v_mfma_f32_16x16x32_bf16 v[252:255], a[188:191], v[120:123], v[252:255]\n" \
"v_add_f32_e32 v193, v193, v129\n" \
"v_add_f32_e32 v194, v194, v130\n" \
"v_mfma_f32_16x16x32_bf16 a[204:207], a[252:255], v[120:123], a[204:207]\n" \
"buffer_load_dwordx4 v2, s[84:87], s92 offen offset:2048 lds\n" \
"v_add_f32_e32 v195, v195, v131\n" \
"s_waitcnt lgkmcnt(1)\n" \
"s_barrier\n" \
"v_mfma_f32_16x16x32_bf16 v[224:227], a[0:3], v[124:127], v[224:227]\n" \
"buffer_load_dwordx4 v3, s[84:87], s92 offen offset:3072 lds\n" \
"s_add_u32 m0, s93, 0x1000\n" \
"v_mfma_f32_16x16x32_bf16 v[228:231], a[4:7], v[124:127], v[228:231]\n" \
"s_waitcnt lgkmcnt(0)\n" \
"v_add_f32_e32 v56, v56, v42\n" \
"v_mfma_f32_16x16x32_bf16 v[232:235], a[8:11], v[124:127], v[232:235]\n" \
"v_max_f32_e32 v62, v62, v56\n" \
"v_add_f32_e32 v196, v196, v132\n" \
"v_mfma_f32_16x16x32_bf16 v[236:239], a[12:15], v[124:127], v[236:239]\n" \
"buffer_load_dwordx4 v4, s[84:87], s92 offen offset:0 lds\n" \
"v_rcp_f32_e32 v56, v56\n" \
"v_mfma_f32_16x16x32_bf16 v[240:243], a[16:19], v[124:127], v[240:243]\n" \
"v_add_f32_e32 v197, v197, v133\n" \
"v_add_f32_e32 v198, v198, v134\n" \
"v_add_f32_e32 v199, v199, v135\n" \
"v_mul_f32_e32 v56, v56, v60\n" \
"v_mul_f32_e32 v192, v56, v192\n" \
"v_mul_f32_e32 v193, v56, v193\n" \
"v_mul_f32_e32 v194, v56, v194\n" \
"v_mul_f32_e32 v195, v56, v195\n" \
"v_mul_f32_e32 v196, v56, v196\n" \
"v_mul_f32_e32 v197, v56, v197\n" \
"v_mul_f32_e32 v198, v56, v198\n" \
"v_mul_f32_e32 v199, v56, v199\n" \
"v_add_f32_e32 v200, v200, v136\n" \
"v_add_f32_e32 v201, v201, v137\n" \
"v_add_f32_e32 v202, v202, v138\n" \
"v_add_f32_e32 v203, v203, v139\n" \
"v_add_f32_e32 v204, v204, v140\n" \
"v_add_f32_e32 v205, v205, v141\n" \
"v_add_f32_e32 v206, v206, v142\n" \
"v_add_f32_e32 v207, v207, v143\n" \
"v_cvt_pk_bf16_f32 v64, v192, v193\n" \
"v_cvt_pk_bf16_f32 v65, v194, v195\n" \
"v_cvt_pk_bf16_f32 v66, v196, v197\n" \
"v_cvt_pk_bf16_f32 v67, v198, v199\n" \
"buffer_store_dwordx4 v[64:67], v20, s[56:59], s70 offen offset:0 sc1\n" \
"v_mfma_f32_16x16x32_bf16 v[244:247], a[20:23], v[124:127], v[244:247]\n" \
"v_mfma_f32_16x16x32_bf16 a[208:211], a[216:219], v[64:67], a[208:211]\n" \
"v_mul_f32_e32 v200, v56, v200\n" \
"v_mul_f32_e32 v201, v56, v201\n" \
"v_mul_f32_e32 v202, v56, v202\n" \
"v_mul_f32_e32 v203, v56, v203\n" \
"v_mul_f32_e32 v204, v56, v204\n" \
"v_mul_f32_e32 v205, v56, v205\n" \
"v_mul_f32_e32 v206, v56, v206\n" \
"v_mul_f32_e32 v207, v56, v207\n" \
"v_add_f32_e32 v208, v208, v144\n" \
"v_add_f32_e32 v209, v209, v145\n" \
"v_add_f32_e32 v210, v210, v146\n" \
"v_add_f32_e32 v211, v211, v147\n" \
"v_add_f32_e32 v212, v212, v148\n" \
"v_add_f32_e32 v213, v213, v149\n" \
"v_add_f32_e32 v214, v214, v150\n" \
"v_add_f32_e32 v215, v215, v151\n" \
"v_cvt_pk_bf16_f32 v68, v200, v201\n" \
"v_cvt_pk_bf16_f32 v69, v202, v203\n" \
"v_cvt_pk_bf16_f32 v70, v204, v205\n" \
"v_cvt_pk_bf16_f32 v71, v206, v207\n" \
"buffer_store_dwordx4 v[68:71], v20, s[56:59], s70 offen offset:1024 sc1\n" \
"v_mul_f32_e32 v208, v56, v208\n" \
"v_mfma_f32_16x16x32_bf16 a[208:211], a[216:219], v[68:71], a[208:211]\n" \
"v_mul_f32_e32 v209, v56, v209\n" \
"v_mul_f32_e32 v210, v56, v210\n" \
"v_mul_f32_e32 v211, v56, v211\n" \
"v_mul_f32_e32 v212, v56, v212\n" \
"v_mul_f32_e32 v213, v56, v213\n" \
"v_mul_f32_e32 v214, v56, v214\n" \
"v_mul_f32_e32 v215, v56, v215\n" \
"v_add_f32_e32 v216, v216, v152\n" \
"v_add_f32_e32 v217, v217, v153\n" \
"v_add_f32_e32 v218, v218, v154\n" \
"v_add_f32_e32 v219, v219, v155\n" \
"v_add_f32_e32 v220, v220, v156\n" \
"v_add_f32_e32 v221, v221, v157\n" \
"v_add_f32_e32 v222, v222, v158\n" \
"v_add_f32_e32 v223, v223, v159\n" \
"v_cvt_pk_bf16_f32 v72, v208, v209\n" \
"v_cvt_pk_bf16_f32 v73, v210, v211\n" \
"v_cvt_pk_bf16_f32 v74, v212, v213\n" \
"v_cvt_pk_bf16_f32 v75, v214, v215\n" \
"buffer_store_dwordx4 v[72:75], v20, s[56:59], s70 offen offset:2048 sc1\n" \
"v_mfma_f32_16x16x32_bf16 v[248:251], a[24:27], v[124:127], v[248:251]\n" \
"v_mfma_f32_16x16x32_bf16 a[208:211], a[216:219], v[72:75], a[208:211]\n" \
"v_mul_f32_e32 v216, v56, v216\n" \
"v_mul_f32_e32 v217, v56, v217\n" \
"v_mul_f32_e32 v218, v56, v218\n" \
"v_mul_f32_e32 v219, v56, v219\n" \
"v_mul_f32_e32 v220, v56, v220\n" \
"v_mul_f32_e32 v221, v56, v221\n" \
"v_mul_f32_e32 v222, v56, v222\n" \
"v_mul_f32_e32 v223, v56, v223\n" \
"v_cvt_pk_bf16_f32 v76, v216, v217\n" \
"v_cvt_pk_bf16_f32 v77, v218, v219\n" \
"v_cvt_pk_bf16_f32 v78, v220, v221\n" \
"v_cvt_pk_bf16_f32 v79, v222, v223\n" \
"buffer_store_dwordx4 v[76:79], v20, s[56:59], s70 offen offset:3072 sc1\n" \
"v_add_f32_e32 v224, v224, v160\n" \
"v_add_f32_e32 v225, v225, v161\n" \
"v_add_f32_e32 v226, v226, v162\n" \
"v_add_f32_e32 v227, v227, v163\n" \
"v_add_f32_e32 v228, v228, v164\n" \
"v_add_f32_e32 v229, v229, v165\n" \
"v_add_f32_e32 v230, v230, v166\n" \
"v_add_f32_e32 v231, v231, v167\n" \
"v_mfma_f32_16x16x32_bf16 a[208:211], a[216:219], v[76:79], a[208:211]\n" \
"buffer_load_dwordx4 v5, s[84:87], s92 offen offset:1024 lds\n" \
"v_add_f32_e32 v232, v232, v168\n" \
"v_add_f32_e32 v233, v233, v169\n" \
"v_add_f32_e32 v234, v234, v170\n" \
"v_add_f32_e32 v235, v235, v171\n" \
"v_add_f32_e32 v236, v236, v172\n" \
"v_add_f32_e32 v237, v237, v173\n" \
"v_add_f32_e32 v238, v238, v174\n" \
"v_add_f32_e32 v239, v239, v175\n" \
"v_mfma_f32_16x16x32_bf16 v[252:255], a[28:31], v[124:127], v[252:255]\n" \
"buffer_load_dwordx4 v6, s[84:87], s92 offen offset:2048 lds\n" \
"v_add_f32_e32 v240, v240, v176\n" \
"v_add_f32_e32 v241, v241, v177\n" \
"v_add_f32_e32 v242, v242, v178\n" \
"v_add_f32_e32 v243, v243, v179\n" \
"v_add_f32_e32 v244, v244, v180\n" \
"v_add_f32_e32 v245, v245, v181\n" \
"v_add_f32_e32 v246, v246, v182\n" \
"v_add_f32_e32 v247, v247, v183\n" \
"v_add_f32_e32 v248, v248, v184\n" \
"v_add_f32_e32 v249, v249, v185\n" \
"v_add_f32_e32 v250, v250, v186\n" \
"v_add_f32_e32 v251, v251, v187\n" \
"v_add_f32_e32 v252, v252, v188\n" \
"v_add_f32_e32 v253, v253, v189\n" \
"v_add_f32_e32 v254, v254, v190\n" \
"v_add_f32_e32 v255, v255, v191\n" \
"v_mfma_f32_16x16x32_bf16 a[204:207], a[252:255], v[124:127], a[204:207]\n" \
"buffer_load_dwordx4 v7, s[84:87], s92 offen offset:3072 lds\n" \
"s_add_u32 m0, s93, 0x2000\n" \
"s_nop 0\n" \
"buffer_load_dwordx4 v8, s[84:87], s92 offen offset:0 lds\n" \
"s_nop 3\n" \
"v_accvgpr_read_b32 v57, a204\n" \
"v_accvgpr_read_b32 v23, a205\n" \
"v_add_f32_e32 v57, v57, v23\n" \
"v_add_f32_e32 v57, v57, v43\n" \
"v_max_f32_e32 v62, v62, v57\n" \
"v_rcp_f32_e32 v57, v57\n" \
"v_cmp_lt_f32_e64 s[72:73], s100, v62\n" \
"v_mul_f32_e32 v57, v57, v61\n" \
"v_mul_f32_e32 v224, v57, v224\n" \
"v_mul_f32_e32 v225, v57, v225\n" \
"v_mul_f32_e32 v226, v57, v226\n" \
"v_mul_f32_e32 v227, v57, v227\n" \
"v_mul_f32_e32 v228, v57, v228\n" \
"v_mul_f32_e32 v229, v57, v229\n" \
"v_mul_f32_e32 v230, v57, v230\n" \
"v_mul_f32_e32 v231, v57, v231\n" \
"v_cvt_pk_bf16_f32 v80, v224, v225\n" \
"v_cvt_pk_bf16_f32 v81, v226, v227\n" \
"v_cvt_pk_bf16_f32 v82, v228, v229\n" \
"v_cvt_pk_bf16_f32 v83, v230, v231\n" \
"buffer_store_dwordx4 v[80:83], v20, s[56:59], s71 offen offset:0 sc1\n" \
"v_mul_f32_e32 v232, v57, v232\n" \
"v_mfma_f32_16x16x32_bf16 a[208:211], a[216:219], v[80:83], a[208:211]\n" \
"v_mul_f32_e32 v233, v57, v233\n" \
"v_mul_f32_e32 v234, v57, v234\n" \
"v_mul_f32_e32 v235, v57, v235\n" \
"v_mul_f32_e32 v236, v57, v236\n" \
"v_mul_f32_e32 v237, v57, v237\n" \
"v_mul_f32_e32 v238, v57, v238\n" \
"v_mul_f32_e32 v239, v57, v239\n" \
"v_cvt_pk_bf16_f32 v84, v232, v233\n" \
"v_cvt_pk_bf16_f32 v85, v234, v235\n" \
"v_cvt_pk_bf16_f32 v86, v236, v237\n" \
"v_cvt_pk_bf16_f32 v87, v238, v239\n" \
"buffer_store_dwordx4 v[84:87], v20, s[56:59], s71 offen offset:1024 sc1\n" \
"v_mul_f32_e32 v240, v57, v240\n" \
"v_mfma_f32_16x16x32_bf16 a[208:211], a[216:219], v[84:87], a[208:211]\n" \
"v_mul_f32_e32 v241, v57, v241\n" \
"v_mul_f32_e32 v242, v57, v242\n" \
"v_mul_f32_e32 v243, v57, v243\n" \
"v_mul_f32_e32 v244, v57, v244\n" \
"v_mul_f32_e32 v245, v57, v245\n" \
"v_mul_f32_e32 v246, v57, v246\n" \
"v_mul_f32_e32 v247, v57, v247\n" \
"v_cvt_pk_bf16_f32 v96, v240, v241\n" \
"v_cvt_pk_bf16_f32 v97, v242, v243\n" \
"v_cvt_pk_bf16_f32 v98, v244, v245\n" \
"v_cvt_pk_bf16_f32 v99, v246, v247\n" \
"buffer_store_dwordx4 v[96:99], v20, s[56:59], s71 offen offset:2048 sc1\n" \
"v_mul_f32_e32 v248, v57, v248\n" \
"v_mfma_f32_16x16x32_bf16 a[208:211], a[216:219], v[96:99], a[208:211]\n" \
"v_mul_f32_e32 v249, v57, v249\n" \
"v_mul_f32_e32 v250, v57, v250\n" \
"v_mul_f32_e32 v251, v57, v251\n" \
"v_mul_f32_e32 v252, v57, v252\n" \
"v_mul_f32_e32 v253, v57, v253\n" \
"v_mul_f32_e32 v254, v57, v254\n" \
"v_mul_f32_e32 v255, v57, v255\n" \
"v_cvt_pk_bf16_f32 v100, v248, v249\n" \
"v_cvt_pk_bf16_f32 v101, v250, v251\n" \
"v_cvt_pk_bf16_f32 v102, v252, v253\n" \
"v_cvt_pk_bf16_f32 v103, v254, v255\n" \
"buffer_store_dwordx4 v[100:103], v20, s[56:59], s71 offen offset:3072 sc1\n" \
"s_nop 0\n" \
"v_mfma_f32_16x16x32_bf16 a[208:211], a[216:219], v[100:103], a[208:211]\n" \
"s_nop 7\n" \
"v_accvgpr_read_b32 v63, a208\n" \
"v_cmp_u_f32_e32 vcc, v63, v63\n" \
"s_nop 0\n" \
"s_or_b64 vcc, s[72:73], vcc\n" \
"s_cbranch_scc0 L_out_%=\n" \
"s_branch L_end_%=\n" \
"L_h1_%=:\n" \
"L_pf14:\n" \
"s_cbranch_execz L_pf18\n" \
"s_nop 0\n" \
"s_cmp_eq_u32 s63, 0x400000\n" \
"s_cbranch_scc1 L_hc1_%=\n" \
"s_cmp_lg_u32 s63, 0x0\n" \
"s_cbranch_scc1 L_hn01_%=\n" \
"s_mov_b32 exec_lo, 0xffffffff\n" \
"s_mov_b32 exec_hi, 0xf\n" \
"buffer_load_dword v22, v16, s[80:83], 0 offen\n" \
"s_mov_b64 exec, -1\n" \
"s_branch L_hr1_%=\n" \
"L_hn01_%=:\n" \
"buffer_load_dword v22, v17, s[56:59], 0 offen sc1\n" \
"s_branch L_hr1_%=\n" \
"L_hs1_%=:\n" \
"s_sleep 1\n" \
"buffer_load_dword v22, v17, s[56:59], 0 offen sc1\n" \
"s_add_u32 s101, s101, 1\n" \
"s_waitcnt vmcnt(0)\n" \
"s_cmp_gt_u32 s101, 0x100000\n" \
"s_cbranch_scc1 L_hr1_%=\n" \
"L_hc1_%=:\n" \
"v_cmp_ne_u32_e32 vcc, s62, v22\n" \
"s_nop 4\n" \
"s_cbranch_vccnz L_hs1_%=\n" \
"s_branch L_hr1_%=\n" \
"L_h2_%=:\n" \
"s_cmp_eq_u32 s63, 0x400000\n" \
"s_cbranch_scc1 L_hc2_%=\n" \
"s_cmp_lg_u32 s63, 0x0\n" \
"s_cbranch_scc1 L_hn02_%=\n" \
"s_mov_b32 exec_lo, 0xffffffff\n" \
"s_mov_b32 exec_hi, 0xf\n" \
"buffer_load_dword v22, v16, s[80:83], 0 offen\n" \
"s_mov_b64 exec, -1\n" \
"s_branch L_hr2_%=\n" \
"L_hn02_%=:\n" \
"buffer_load_dword v22, v17, s[56:59], 0 offen sc1\n" \
"s_branch L_hr2_%=\n" \
"L_hs2_%=:\n" \
"s_sleep 1\n" \
"buffer_load_dword v22, v17, s[56:59], 0 offen sc1\n" \
"s_add_u32 s101, s101, 1\n" \
"s_waitcnt vmcnt(0)\n" \
"s_cmp_gt_u32 s101, 0x100000\n" \
"s_cbranch_scc1 L_hr2_%=\n" \
"L_hc2_%=:\n" \
"v_cmp_ne_u32_e32 vcc, s62, v22\n" \
"s_nop 4\n" \
"s_cbranch_vccnz L_hs2_%=\n" \
"s_branch L_hr2_%=\n" \
"L_sa_%=:\n" \
"s_sleep 1\n" \
"buffer_load_dword v37, v38, s[56:59], s64 offen sc1\n" \
"s_add_u32 s101, s101, 1\n" \
"s_waitcnt vmcnt(0)\n" \
"v_cmp_ne_u32_e32 vcc, s62, v37\n" \
"s_cmp_gt_u32 s101, 0x100000\n" \
"s_cbranch_scc1 L_ra_%=\n" \
"s_nop 2\n" \
"s_cbranch_vccnz L_sa_%=\n" \
"s_branch L_ra_%=\n" \
"L_sb_%=:\n" \
"s_sleep 1\n" \
"buffer_load_dword v47, v38, s[56:59], s71 offen sc1\n" \
"s_add_u32 s101, s101, 1\n" \
"s_waitcnt vmcnt(0)\n" \
"v_cmp_ne_u32_e32 vcc, s62, v47\n" \
"s_cmp_gt_u32 s101, 0x100000\n" \
"s_cbranch_scc1 L_rb_%=\n" \
"s_nop 2\n" \
"s_cbranch_vccnz L_sb_%=\n" \
"s_branch L_rb_%=\n" \
"L_sx0_%=:\n" \
"s_sleep 1\n" \
"buffer_load_dword v37, v40, s[56:59], s74 offen sc1\n" \
"s_add_u32 s101, s101, 1\n" \
"s_waitcnt vmcnt(0)\n" \
"v_cmp_eq_u32_e64 s[98:99], s62, v37\n" \
"s_cmp_gt_u32 s101, 0x100000\n" \
"s_cbranch_scc1 L_rx0_%=\n" \
"s_andn2_b32 s75, 0x1, s98\n" \
"s_cbranch_scc1 L_sx0_%=\n" \
"s_branch L_rx0_%=\n" \
"L_sx1_%=:\n" \
"s_sleep 1\n" \
"buffer_load_dword v37, v40, s[56:59], s74 offen sc1\n" \
"s_add_u32 s101, s101, 1\n" \
"s_waitcnt vmcnt(0)\n" \
"v_cmp_eq_u32_e64 s[98:99], s62, v37\n" \
"s_cmp_gt_u32 s101, 0x100000\n" \
"s_cbranch_scc1 L_rx1_%=\n" \
"s_andn2_b32 s75, 0x2, s98\n" \
"s_cbranch_scc1 L_sx1_%=\n" \
"s_branch L_rx1_%=\n" \
"L_sx2_%=:\n" \
"s_sleep 1\n" \
"buffer_load_dword v37, v40, s[56:59], s74 offen sc1\n" \
"s_add_u32 s101, s101, 1\n" \
"s_waitcnt vmcnt(0)\n" \
"v_cmp_eq_u32_e64 s[98:99], s62, v37\n" \
"s_cmp_gt_u32 s101, 0x100000\n" \
"s_cbranch_scc1 L_rx2_%=\n" \
"s_andn2_b32 s75, 0x4, s98\n" \
"s_cbranch_scc1 L_sx2_%=\n" \
"s_branch L_rx2_%=\n" \
"L_sx3_%=:\n" \
"s_sleep 1\n" \
"buffer_load_dword v37, v40, s[56:59], s74 offen sc1\n" \
"s_add_u32 s101, s101, 1\n" \
"s_waitcnt vmcnt(0)\n" \
"v_cmp_eq_u32_e64 s[98:99], s62, v37\n" \
"s_cmp_gt_u32 s101, 0x100000\n" \
"s_cbranch_scc1 L_rx3_%=\n" \
"s_andn2_b32 s75, s100, s98\n" \
"s_cbranch_scc1 L_sx3_%=\n" \
"s_branch L_rx3_%=\n" \
"L_sx4_%=:\n" \
"s_sleep 1\n" \
"buffer_load_dword v37, v40, s[56:59], s74 offen sc1\n" \
"s_add_u32 s101, s101, 1\n" \
"s_waitcnt vmcnt(0)\n" \
"v_cmp_eq_u32_e64 s[98:99], s62, v37\n" \
"s_cmp_gt_u32 s101, 0x100000\n" \
"s_cbranch_scc1 L_rx4_%=\n" \
"s_andn2_b32 s75, 0x10, s98\n" \
"s_cbranch_scc1 L_sx4_%=\n" \
"s_branch L_rx4_%=\n" \
"L_sx5_%=:\n" \
"s_sleep 1\n" \
"buffer_load_dword v37, v40, s[56:59], s74 offen sc1\n" \
"s_add_u32 s101, s101, 1\n" \
"s_waitcnt vmcnt(0)\n" \
"v_cmp_eq_u32_e64 s[98:99], s62, v37\n" \
"s_cmp_gt_u32 s101, 0x100000\n" \
"s_cbranch_scc1 L_rx5_%=\n" \
"s_andn2_b32 s75, 0x20, s98\n" \
"s_cbranch_scc1 L_sx5_%=\n" \
"s_branch L_rx5_%=\n" \
"L_sx6_%=:\n" \
"s_sleep 1\n" \
"buffer_load_dword v37, v40, s[56:59], s74 offen sc1\n" \
"s_add_u32 s101, s101, 1\n" \
"s_waitcnt vmcnt(0)\n" \
"v_cmp_eq_u32_e64 s[98:99], s62, v37\n" \
"s_cmp_gt_u32 s101, 0x100000\n" \
"s_cbranch_scc1 L_rx6_%=\n" \
"s_andn2_b32 s75, 0x40, s98\n" \
"s_cbranch_scc1 L_sx6_%=\n" \
"s_branch L_rx6_%=\n" \
"L_sx7_%=:\n" \
"s_sleep 1\n" \
"buffer_load_dword v37, v40, s[56:59], s74 offen sc1\n" \
"s_add_u32 s101, s101, 1\n" \
"s_waitcnt vmcnt(0)\n" \
"v_cmp_eq_u32_e64 s[98:99], s62, v37\n" \
"s_cmp_gt_u32 s101, 0x100000\n" \
"s_cbranch_scc1 L_rx7_%=\n" \
"s_andn2_b32 s75, 0x80, s98\n" \
"s_cbranch_scc1 L_sx7_%=\n" \
"s_branch L_rx7_%=\n" \
"L_sx8_%=:\n" \
"s_sleep 1\n" \
"buffer_load_dword v37, v40, s[56:59], s74 offen sc1\n" \
"s_add_u32 s101, s101, 1\n" \
"s_waitcnt vmcnt(0)\n" \
"v_cmp_eq_u32_e64 s[98:99], s62, v37\n" \
"s_cmp_gt_u32 s101, 0x100000\n" \
"s_cbranch_scc1 L_rx8_%=\n" \
"s_andn2_b32 s75, 0x100, s98\n" \
"s_cbranch_scc1 L_sx8_%=\n" \
"s_branch L_rx8_%=\n" \
"L_sx9_%=:\n" \
"s_sleep 1\n" \
"buffer_load_dword v37, v40, s[56:59], s74 offen sc1\n" \
"s_add_u32 s101, s101, 1\n" \
"s_waitcnt vmcnt(0)\n" \
"v_cmp_eq_u32_e64 s[98:99], s62, v37\n" \
"s_cmp_gt_u32 s101, 0x100000\n" \
"s_cbranch_scc1 L_rx9_%=\n" \
"s_andn2_b32 s75, 0x200, s98\n" \
"s_cbranch_scc1 L_sx9_%=\n" \
"s_branch L_rx9_%=\n" \
"L_sx10_%=:\n" \
"s_sleep 1\n" \
"buffer_load_dword v37, v40, s[56:59], s74 offen sc1\n" \
"s_add_u32 s101, s101, 1\n" \
"s_waitcnt vmcnt(0)\n" \
"v_cmp_eq_u32_e64 s[98:99], s62, v37\n" \
"s_cmp_gt_u32 s101, 0x100000\n" \
"s_cbranch_scc1 L_rx10_%=\n" \
"s_andn2_b32 s75, 0x400, s98\n" \
"s_cbranch_scc1 L_sx10_%=\n" \
"s_branch L_rx10_%=\n" \
"L_att_stub:\n" \
"s_mov_b32 s101, 0\n" \
"v_add_u32_e32 v32, 0x49000, v20\n" \
"v_readlane_b32 s96, v11, 32\n" \
"v_readlane_b32 s97, v11, 33\n" \
"buffer_load_dwordx4 a[220:223], v20, s[56:59], s60 offen offset:0 sc1\n" \
"buffer_load_dwordx4 a[236:239], v32, s[56:59], s60 offen offset:0 sc1\n" \
"buffer_load_dwordx4 a[224:227], v20, s[56:59], s60 offen offset:1024 sc1\n" \
"buffer_load_dwordx4 a[240:243], v32, s[56:59], s60 offen offset:1024 sc1\n" \
"buffer_load_dwordx4 a[228:231], v20, s[56:59], s60 offen offset:2048 sc1\n" \
"buffer_load_dwordx4 a[244:247], v32, s[56:59], s60 offen offset:2048 sc1\n" \
"buffer_load_dwordx4 a[232:235], v20, s[56:59], s60 offen offset:3072 sc1\n" \
"buffer_load_dwordx4 a[248:251], v32, s[56:59], s60 offen offset:3072 sc1\n" \
"buffer_load_dwordx4 a[32:35], v20, s[56:59], s96 offen offset:0 sc1\n" \
"buffer_load_dwordx4 a[40:43], v32, s[56:59], s96 offen offset:0 sc1\n" \
"buffer_load_dwordx4 a[48:51], v20, s[56:59], s97 offen offset:0 sc1\n" \
"buffer_load_dwordx4 a[56:59], v32, s[56:59], s97 offen offset:0 sc1\n" \
"buffer_load_dwordx4 a[36:39], v20, s[56:59], s96 offen offset:1024 sc1\n" \
"buffer_load_dwordx4 a[44:47], v32, s[56:59], s96 offen offset:1024 sc1\n" \
"buffer_load_dwordx4 a[52:55], v20, s[56:59], s97 offen offset:1024 sc1\n" \
"buffer_load_dwordx4 a[60:63], v32, s[56:59], s97 offen offset:1024 sc1\n" \
"buffer_load_dwordx4 v[64:67], v20, s[56:59], s61 offen offset:0 sc1\n" \
"buffer_load_dwordx4 v[68:71], v20, s[56:59], s61 offen offset:1024 sc1\n" \
"buffer_load_dwordx4 v[72:75], v20, s[56:59], s61 offen offset:2048 sc1\n" \
"buffer_load_dwordx4 v[76:79], v20, s[56:59], s61 offen offset:3072 sc1\n" \
"buffer_load_dwordx4 v[80:83], v32, s[56:59], s61 offen offset:0 sc1\n" \
"buffer_load_dwordx4 v[84:87], v32, s[56:59], s61 offen offset:1024 sc1\n" \
"buffer_load_dwordx4 v[96:99], v32, s[56:59], s61 offen offset:2048 sc1\n" \
"buffer_load_dwordx4 v[100:103], v32, s[56:59], s61 offen offset:3072 sc1\n" \
"s_waitcnt vmcnt(0)\n" \
"s_branch L_out_%=\n" \
"L_sw_%=:\n" \
"L_pf15:\n" \
"s_cbranch_execz L_pf19\n" \
"s_nop 0\n" \
"v_mfma_f32_16x16x32_bf16 v[72:75], a[128:131], a[16:19], v[36:39]\n" \
"s_add_u32 s68, s60, 0xfff6e000\n" \
"s_add_u32 s69, s60, 0xfffb7000\n" \
"s_add_u32 s70, s60, 0x0\n" \
"s_add_u32 s71, s60, 0x49000\n" \
"v_mfma_f32_16x16x32_bf16 v[72:75], a[132:135], a[20:23], v[72:75]\n" \
"s_xor_b32 m0, s65, 0x12000\n" \
"ds_read_b128 a[160:163], v49 offset:0\n" \
"v_mfma_f32_16x16x32_bf16 v[72:75], a[136:139], a[24:27], v[72:75]\n" \
"buffer_load_dwordx4 v50, s[8:11], s64 offen offset:0 sc1 lds\n" \
"v_mfma_f32_16x16x32_bf16 v[72:75], a[140:143], a[28:31], v[72:75]\n" \
"ds_read_b128 a[164:167], v49 offset:1024\n" \
"ds_read_b128 a[168:171], v49 offset:2048\n" \
"v_mfma_f32_16x16x32_bf16 v[76:79], a[156:159], a[28:31], v[36:39]\n" \
"ds_read_b128 a[172:175], v49 offset:3072\n" \
"v_cvt_pk_bf16_f32 v120, v88, v89\n" \
"v_mfma_f32_16x16x32_bf16 v[76:79], a[152:155], a[24:27], v[76:79]\n" \
"buffer_load_dwordx4 v50, s[8:11], s64 offen offset:1024 sc1 lds\n" \
"v_mfma_f32_16x16x32_bf16 v[76:79], a[148:151], a[20:23], v[76:79]\n" \
"ds_read_b128 a[176:179], v49 offset:4096\n" \
"v_cvt_pk_bf16_f32 v121, v90, v91\n" \
"v_mfma_f32_16x16x32_bf16 v[76:79], a[144:147], a[16:19], v[76:79]\n" \
"ds_read_b128 a[180:183], v49 offset:5120\n" \
"s_mov_b32 s65, s13\n" \
"v_mfma_f32_16x16x32_bf16 v[80:83], a[128:131], a[32:35], v[40:43]\n" \
"v_add_u32_e32 v63, s15, v20\n" \
"s_add_u32 s67, s14, 40960\n" \
"v_mfma_f32_16x16x32_bf16 v[80:83], a[132:135], a[36:39], v[80:83]\n" \
"v_add_u32_e32 v50, 0x6000, v63\n" \
"s_xor_b32 m0, s65, 0x8000\n" \
"v_mfma_f32_16x16x32_bf16 v[80:83], a[136:139], a[40:43], v[80:83]\n" \
"v_exp_f32_e32 v92, v92\n" \
"v_mfma_f32_16x16x32_bf16 v[80:83], a[140:143], a[44:47], v[80:83]\n" \
"buffer_load_dwordx4 v50, s[8:11], s67 offen offset:0 sc1 lds\n" \
"v_mfma_f32_16x16x32_bf16 v[84:87], a[156:159], a[44:47], v[40:43]\n" \
"v_exp_f32_e32 v93, v93\n" \
"v_mfma_f32_16x16x32_bf16 v[84:87], a[152:155], a[40:43], v[84:87]\n" \
"v_exp_f32_e32 v94, v94\n" \
"v_mfma_f32_16x16x32_bf16 v[84:87], a[148:151], a[36:39], v[84:87]\n" \
"v_exp_f32_e32 v95, v95\n" \
"v_mfma_f32_16x16x32_bf16 v[84:87], a[144:147], a[32:35], v[84:87]\n" \
"v_cvt_pk_bf16_f32 v122, v92, v93\n" \
"v_cvt_pk_bf16_f32 v123, v94, v95\n" \
"v_mfma_f32_16x16x32_bf16 v[88:91], a[128:131], a[48:51], v[44:47]\n" \
"ds_read_b128 a[184:187], v49 offset:6144\n" \
"ds_read_b128 a[188:191], v49 offset:7168\n" \
"v_mfma_f32_16x16x32_bf16 v[88:91], a[132:135], a[52:55], v[88:91]\n" \
"v_and_b32_e32 v96, v12, v96\n" \
"v_and_b32_e32 v97, v13, v97\n" \
"v_mfma_f32_16x16x32_bf16 v[88:91], a[136:139], a[56:59], v[88:91]\n" \
"v_and_b32_e32 v98, v14, v98\n" \
"v_and_b32_e32 v99, v15, v99\n" \
"v_mfma_f32_16x16x32_bf16 v[88:91], a[140:143], a[60:63], v[88:91]\n" \
"buffer_load_dwordx4 v50, s[8:11], s67 offen offset:1024 sc1 lds\n" \
"v_mfma_f32_16x16x32_bf16 v[92:95], a[156:159], a[60:63], v[44:47]\n" \
"v_exp_f32_e32 v72, v72\n" \
"v_mfma_f32_16x16x32_bf16 v[92:95], a[152:155], a[56:59], v[92:95]\n" \
"v_exp_f32_e32 v73, v73\n" \
"v_mfma_f32_16x16x32_bf16 v[92:95], a[148:151], a[52:55], v[92:95]\n" \
"v_exp_f32_e32 v74, v74\n" \
"v_mfma_f32_16x16x32_bf16 v[92:95], a[144:147], a[48:51], v[92:95]\n" \
"v_cvt_pk_bf16_f32 v108, v72, v73\n" \
"s_waitcnt lgkmcnt(2)\n" \
"v_mfma_f32_16x16x32_bf16 v[128:131], a[160:163], v[96:99], v[128:131]\n" \
"v_exp_f32_e32 v75, v75\n" \
"v_mfma_f32_16x16x32_bf16 v[132:135], a[164:167], v[96:99], v[132:135]\n" \
"v_exp_f32_e32 v76, v76\n" \
"v_mfma_f32_16x16x32_bf16 v[136:139], a[168:171], v[96:99], v[136:139]\n" \
"v_exp_f32_e32 v77, v77\n" \
"v_mfma_f32_16x16x32_bf16 v[140:143], a[172:175], v[96:99], v[140:143]\n" \
"v_exp_f32_e32 v78, v78\n" \
"v_mfma_f32_16x16x32_bf16 a[192:195], a[252:255], v[96:99], a[192:195]\n" \
"v_exp_f32_e32 v79, v79\n" \
"v_mfma_f32_16x16x32_bf16 v[160:163], a[160:163], v[104:107], v[160:163]\n" \
"v_cvt_pk_bf16_f32 v109, v74, v75\n" \
"v_cvt_pk_bf16_f32 v110, v76, v77\n" \
"v_cvt_pk_bf16_f32 v111, v78, v79\n" \
"v_and_b32_e32 v108, v12, v108\n" \
"v_and_b32_e32 v109, v13, v109\n" \
"v_and_b32_e32 v110, v14, v110\n" \
"v_and_b32_e32 v111, v15, v111\n" \
"v_exp_f32_e32 v80, v80\n" \
"v_exp_f32_e32 v81, v81\n" \
"v_mfma_f32_16x16x32_bf16 v[160:163], a[176:179], v[108:111], v[160:163]\n" \
"buffer_load_dwordx4 v50, s[8:11], s67 offen offset:2048 sc1 lds\n" \
"v_mfma_f32_16x16x32_bf16 v[164:167], a[180:183], v[108:111], v[164:167]\n" \
"buffer_load_dwordx4 v50, s[8:11], s67 offen offset:3072 sc1 lds\n" \
"v_mfma_f32_16x16x32_bf16 v[164:167], a[164:167], v[104:107], v[164:167]\n" \
"v_exp_f32_e32 v82, v82\n" \
"v_mfma_f32_16x16x32_bf16 v[168:171], a[168:171], v[104:107], v[168:171]\n" \
"v_cvt_pk_bf16_f32 v116, v80, v81\n" \
"s_waitcnt lgkmcnt(0)\n" \
"v_mfma_f32_16x16x32_bf16 v[168:171], a[184:187], v[108:111], v[168:171]\n" \
"v_exp_f32_e32 v83, v83\n" \
"v_mfma_f32_16x16x32_bf16 v[172:175], a[188:191], v[108:111], v[172:175]\n" \
"v_exp_f32_e32 v84, v84\n" \
"v_mfma_f32_16x16x32_bf16 v[172:175], a[172:175], v[104:107], v[172:175]\n" \
"v_exp_f32_e32 v85, v85\n" \
"v_smfmac_f32_16x16x64_bf16 a[196:199], a[212:215], v[104:111], v21\n" \
"v_mfma_f32_16x16x32_bf16 v[192:195], a[160:163], v[112:115], v[192:195]\n" \
"v_exp_f32_e32 v86, v86\n" \
"v_exp_f32_e32 v87, v87\n" \
"v_cvt_pk_bf16_f32 v117, v82, v83\n" \
"v_cvt_pk_bf16_f32 v118, v84, v85\n" \
"v_cvt_pk_bf16_f32 v119, v86, v87\n" \
"s_mov_b32 s64, s14\n" \
"v_add_u32_e32 v51, 0x12000, v63\n" \
"v_mfma_f32_16x16x32_bf16 v[192:195], a[176:179], v[116:119], v[192:195]\n" \
"s_xor_b32 m0, s65, 0xa000\n" \
"ds_read_b32 v58, v18 offset:512\n" \
"v_mfma_f32_16x16x32_bf16 v[196:199], a[180:183], v[116:119], v[196:199]\n" \
"buffer_load_dwordx4 v51, s[8:11], s64 offen offset:0 sc1 lds\n" \
"v_mfma_f32_16x16x32_bf16 v[196:199], a[164:167], v[112:115], v[196:199]\n" \
"v_exp_f32_e32 v88, v88\n" \
"v_mfma_f32_16x16x32_bf16 v[200:203], a[168:171], v[112:115], v[200:203]\n" \
"v_exp_f32_e32 v89, v89\n" \
"v_mfma_f32_16x16x32_bf16 v[200:203], a[184:187], v[116:119], v[200:203]\n" \
"v_exp_f32_e32 v90, v90\n" \
"v_mfma_f32_16x16x32_bf16 v[204:207], a[188:191], v[116:119], v[204:207]\n" \
"v_exp_f32_e32 v91, v91\n" \
"v_mfma_f32_16x16x32_bf16 v[204:207], a[172:175], v[112:115], v[204:207]\n" \
"v_exp_f32_e32 v92, v92\n" \
"v_smfmac_f32_16x16x64_bf16 a[200:203], a[212:215], v[112:119], v21\n" \
"v_mfma_f32_16x16x32_bf16 v[224:227], a[160:163], v[120:123], v[224:227]\n" \
"v_exp_f32_e32 v93, v93\n" \
"v_exp_f32_e32 v94, v94\n" \
"v_exp_f32_e32 v95, v95\n" \
"v_cvt_pk_bf16_f32 v124, v88, v89\n" \
"v_cvt_pk_bf16_f32 v125, v90, v91\n" \
"v_cvt_pk_bf16_f32 v126, v92, v93\n" \
"v_cvt_pk_bf16_f32 v127, v94, v95\n" \
"v_add_u32_e32 v52, 0x14000, v63\n" \
"v_add_u32_e32 v53, 0x20000, v63\n" \
"v_mfma_f32_16x16x32_bf16 v[224:227], a[176:179], v[124:127], v[224:227]\n" \
"buffer_load_dwordx4 v51, s[8:11], s64 offen offset:1024 sc1 lds\n" \
"v_mfma_f32_16x16x32_bf16 v[228:231], a[180:183], v[124:127], v[228:231]\n" \
"ds_read_b32 v59, v18 offset:640\n" \
"ds_read_b32 v60, v18 offset:768\n" \
"v_mfma_f32_16x16x32_bf16 v[228:231], a[164:167], v[120:123], v[228:231]\n" \
"ds_read_b32 v61, v18 offset:896\n" \
"ds_read_b128 a[128:131], v48 offset:0\n" \
"v_mfma_f32_16x16x32_bf16 v[232:235], a[168:171], v[120:123], v[232:235]\n" \
"ds_read_b128 a[132:135], v48 offset:1024\n" \
"ds_read_b128 a[136:139], v48 offset:2048\n" \
"v_mfma_f32_16x16x32_bf16 v[232:235], a[184:187], v[124:127], v[232:235]\n" \
"ds_read_b128 a[140:143], v48 offset:3072\n" \
"ds_read_b128 a[144:147], v48 offset:4096\n" \
"ds_read_b128 a[148:151], v48 offset:5120\n" \
"ds_read_b128 a[152:155], v48 offset:6144\n" \
"ds_read_b128 a[156:159], v48 offset:7168\n" \
"v_mfma_f32_16x16x32_bf16 v[236:239], a[188:191], v[124:127], v[236:239]\n" \
"buffer_load_dwordx4 v51, s[8:11], s64 offen offset:2048 sc1 lds\n" \
"v_mfma_f32_16x16x32_bf16 v[236:239], a[172:175], v[120:123], v[236:239]\n" \
"buffer_load_dwordx4 v51, s[8:11], s64 offen offset:3072 sc1 lds\n" \
"v_readlane_b32 s93, v11, 17\n" \
"v_readlane_b32 s92, v11, 22\n" \
"v_accvgpr_read_b32 v54, a192\n" \
"v_accvgpr_read_b32 v23, a193\n" \
"v_add_f32_e32 v54, v54, v23\n" \
"v_accvgpr_read_b32 v55, a196\n" \
"v_accvgpr_read_b32 v23, a197\n" \
"v_max_f32_e32 v62, v62, v54\n" \
"v_add_f32_e32 v55, v55, v23\n" \
"v_max_f32_e32 v62, v62, v55\n" \
"v_rcp_f32_e32 v54, v54\n" \
"v_rcp_f32_e32 v55, v55\n" \
"s_waitcnt lgkmcnt(11)\n" \
"v_mul_f32_e32 v54, v54, v58\n" \
"s_waitcnt lgkmcnt(10)\n" \
"v_mul_f32_e32 v55, v55, v59\n" \
"v_mul_f32_e32 v128, v54, v128\n" \
"v_mul_f32_e32 v129, v54, v129\n" \
"v_mul_f32_e32 v136, v54, v136\n" \
"v_mul_f32_e32 v137, v54, v137\n" \
"v_mul_f32_e32 v160, v55, v160\n" \
"v_mul_f32_e32 v161, v55, v161\n" \
"v_mul_f32_e32 v168, v55, v168\n" \
"v_mul_f32_e32 v169, v55, v169\n" \
"v_cvt_pk_bf16_f32 v128, v128, v129\n" \
"v_mul_f32_e32 v130, v54, v130\n" \
"v_mul_f32_e32 v131, v54, v131\n" \
"v_cvt_pk_bf16_f32 v136, v136, v137\n" \
"v_mul_f32_e32 v138, v54, v138\n" \
"v_mul_f32_e32 v139, v54, v139\n" \
"s_waitcnt vmcnt(10) lgkmcnt(0)\n" \
"s_barrier\n" \
"v_mfma_f32_16x16x32_bf16 v[80:83], a[128:131], a[32:35], v[40:43]\n" \
"ds_read_b128 a[160:163], v49 offset:8192\n" \
"ds_read_b128 a[164:167], v49 offset:9216\n" \
"v_mfma_f32_16x16x32_bf16 v[80:83], a[132:135], a[36:39], v[80:83]\n" \
"ds_read_b128 a[168:171], v49 offset:10240\n" \
"ds_read_b128 a[172:175], v49 offset:11264\n" \
"v_mfma_f32_16x16x32_bf16 v[80:83], a[136:139], a[40:43], v[80:83]\n" \
"s_xor_b32 m0, s65, 0x18000\n" \
"ds_read_b128 a[176:179], v49 offset:12288\n" \
"v_mfma_f32_16x16x32_bf16 v[80:83], a[140:143], a[44:47], v[80:83]\n" \
"buffer_load_dwordx4 v52, s[8:11], s64 offen offset:0 sc1 lds\n" \
"v_mfma_f32_16x16x32_bf16 v[84:87], a[156:159], a[44:47], v[40:43]\n" \
"ds_read_b128 a[180:183], v49 offset:13312\n" \
"ds_read_b128 a[184:187], v49 offset:14336\n" \
"v_mfma_f32_16x16x32_bf16 v[84:87], a[152:155], a[40:43], v[84:87]\n" \
"buffer_load_dwordx4 v52, s[8:11], s64 offen offset:1024 sc1 lds\n" \
"v_mfma_f32_16x16x32_bf16 v[84:87], a[148:151], a[36:39], v[84:87]\n" \
"ds_read_b128 a[188:191], v49 offset:15360\n" \
"v_add_u32_e32 v49, 0x10000, v48\n" \
"v_mfma_f32_16x16x32_bf16 v[84:87], a[144:147], a[32:35], v[84:87]\n" \
"buffer_load_dwordx4 v52, s[8:11], s64 offen offset:2048 sc1 lds\n" \
"v_mfma_f32_16x16x32_bf16 v[88:91], a[128:131], a[48:51], v[44:47]\n" \
"ds_read_b128 a[128:131], v48 offset:8192\n" \
"v_cvt_pk_bf16_f32 v160, v160, v161\n" \
"v_mfma_f32_16x16x32_bf16 v[88:91], a[132:135], a[52:55], v[88:91]\n" \
"ds_read_b128 a[132:135], v48 offset:9216\n" \
"v_mul_f32_e32 v162, v55, v162\n" \
"v_mfma_f32_16x16x32_bf16 v[88:91], a[136:139], a[56:59], v[88:91]\n" \
"buffer_load_dwordx4 v52, s[8:11], s64 offen offset:3072 sc1 lds\n" \
"v_mfma_f32_16x16x32_bf16 v[88:91], a[140:143], a[60:63], v[88:91]\n" \
"s_mov_b32 m0, s65\n" \
"ds_read_b128 a[136:139], v48 offset:10240\n" \
"v_mfma_f32_16x16x32_bf16 v[92:95], a[156:159], a[60:63], v[44:47]\n" \
"ds_read_b128 a[140:143], v48 offset:11264\n" \
"ds_read_b128 a[156:159], v48 offset:15360\n" \
"v_mfma_f32_16x16x32_bf16 v[92:95], a[152:155], a[56:59], v[92:95]\n" \
"buffer_load_dwordx4 v53, s[8:11], s64 offen offset:0 sc1 lds\n" \
"v_mfma_f32_16x16x32_bf16 v[92:95], a[148:151], a[52:55], v[92:95]\n" \
"ds_read_b128 a[148:151], v48 offset:13312\n" \
"ds_read_b128 a[152:155], v48 offset:14336\n" \
"v_mfma_f32_16x16x32_bf16 v[92:95], a[144:147], a[48:51], v[92:95]\n" \
"ds_read_b128 a[144:147], v48 offset:12288\n" \
"s_waitcnt lgkmcnt(12)\n" \
"v_mfma_f32_16x16x32_bf16 v[144:147], a[160:163], v[96:99], v[144:147]\n" \
"buffer_load_dwordx4 v53, s[8:11], s64 offen offset:1024 sc1 lds\n" \
"v_mfma_f32_16x16x32_bf16 v[148:151], a[164:167], v[96:99], v[148:151]\n" \
"v_exp_f32_e32 v88, v88\n" \
"v_mfma_f32_16x16x32_bf16 v[152:155], a[168:171], v[96:99], v[152:155]\n" \
"v_exp_f32_e32 v89, v89\n" \
"v_mfma_f32_16x16x32_bf16 v[156:159], a[172:175], v[96:99], v[156:159]\n" \
"v_exp_f32_e32 v90, v90\n" \
"v_mfma_f32_16x16x32_bf16 v[176:179], a[160:163], v[104:107], v[176:179]\n" \
"s_xor_b32 s65, s65, 0x8000\n" \
"s_waitcnt lgkmcnt(8)\n" \
"v_mfma_f32_16x16x32_bf16 v[176:179], a[176:179], v[108:111], v[176:179]\n" \
"buffer_load_dwordx4 v53, s[8:11], s64 offen offset:2048 sc1 lds\n" \
"v_mfma_f32_16x16x32_bf16 v[180:183], a[180:183], v[108:111], v[180:183]\n" \
"v_exp_f32_e32 v91, v91\n" \
"v_mfma_f32_16x16x32_bf16 v[180:183], a[164:167], v[104:107], v[180:183]\n" \
"buffer_load_dwordx4 v53, s[8:11], s64 offen offset:3072 sc1 lds\n" \
"v_mfma_f32_16x16x32_bf16 v[184:187], a[168:171], v[104:107], v[184:187]\n" \
"s_add_u32 m0, s93, 0x0\n" \
"s_add_u32 s64, s64, 0x10000\n" \
"v_mfma_f32_16x16x32_bf16 v[184:187], a[184:187], v[108:111], v[184:187]\n" \
"v_exp_f32_e32 v92, v92\n" \
"v_mfma_f32_16x16x32_bf16 v[188:191], a[188:191], v[108:111], v[188:191]\n" \
"buffer_load_dwordx4 v0, s[80:83], s92 offen offset:0 lds\n" \
"v_mfma_f32_16x16x32_bf16 v[188:191], a[172:175], v[104:107], v[188:191]\n" \
"v_exp_f32_e32 v93, v93\n" \
"v_mfma_f32_16x16x32_bf16 v[208:211], a[160:163], v[112:115], v[208:211]\n" \
"v_exp_f32_e32 v94, v94\n" \
"v_mfma_f32_16x16x32_bf16 v[208:211], a[176:179], v[116:119], v[208:211]\n" \
"v_exp_f32_e32 v95, v95\n" \
"L_pf16:\n" \
"s_cbranch_execz L_retA\n" \
"s_nop 0\n" \
"v_mfma_f32_16x16x32_bf16 v[212:215], a[180:183], v[116:119], v[212:215]\n" \
"buffer_load_dwordx4 v1, s[80:83], s92 offen offset:1024 lds\n" \
"v_mfma_f32_16x16x32_bf16 v[212:215], a[164:167], v[112:115], v[212:215]\n" \
"v_exp_f32_e32 v80, v80\n" \
"v_mfma_f32_16x16x32_bf16 v[216:219], a[168:171], v[112:115], v[216:219]\n" \
"v_exp_f32_e32 v81, v81\n" \
"v_mfma_f32_16x16x32_bf16 v[216:219], a[184:187], v[116:119], v[216:219]\n" \
"buffer_load_dwordx4 v2, s[80:83], s92 offen offset:2048 lds\n" \
"v_mfma_f32_16x16x32_bf16 v[220:223], a[188:191], v[116:119], v[220:223]\n" \
"v_exp_f32_e32 v82, v82\n" \
"v_mfma_f32_16x16x32_bf16 v[220:223], a[172:175], v[112:115], v[220:223]\n" \
"v_exp_f32_e32 v83, v83\n" \
"v_mfma_f32_16x16x32_bf16 v[240:243], a[160:163], v[120:123], v[240:243]\n" \
"buffer_load_dwordx4 v3, s[80:83], s92 offen offset:3072 lds\n" \
"v_mfma_f32_16x16x32_bf16 v[240:243], a[176:179], v[124:127], v[240:243]\n" \
"v_exp_f32_e32 v84, v84\n" \
"v_mfma_f32_16x16x32_bf16 v[244:247], a[180:183], v[124:127], v[244:247]\n" \
"v_exp_f32_e32 v85, v85\n" \
"v_mfma_f32_16x16x32_bf16 v[244:247], a[164:167], v[120:123], v[244:247]\n" \
"v_exp_f32_e32 v86, v86\n" \
"v_mfma_f32_16x16x32_bf16 v[248:251], a[168:171], v[120:123], v[248:251]\n" \
"v_exp_f32_e32 v87, v87\n" \
"v_mfma_f32_16x16x32_bf16 v[248:251], a[184:187], v[124:127], v[248:251]\n" \
"v_cvt_pk_bf16_f32 v112, v80, v81\n" \
"v_cvt_pk_bf16_f32 v113, v82, v83\n" \
"v_mfma_f32_16x16x32_bf16 v[252:255], a[188:191], v[124:127], v[252:255]\n" \
"v_cvt_pk_bf16_f32 v114, v84, v85\n" \
"v_cvt_pk_bf16_f32 v115, v86, v87\n" \
"v_mfma_f32_16x16x32_bf16 v[252:255], a[172:175], v[120:123], v[252:255]\n" \
"v_and_b32_e32 v112, v12, v112\n" \
"v_and_b32_e32 v113, v13, v113\n" \
"v_smfmac_f32_16x16x64_bf16 a[204:207], a[212:215], v[120:127], v21\n" \
"v_cvt_pk_bf16_f32 v120, v88, v89\n" \
"v_cvt_pk_bf16_f32 v121, v90, v91\n" \
"v_cvt_pk_bf16_f32 v122, v92, v93\n" \
"v_cvt_pk_bf16_f32 v123, v94, v95\n" \
"v_and_b32_e32 v114, v14, v114\n" \
"v_and_b32_e32 v115, v15, v115\n" \
"v_add_u32_e32 v48, s16, v20\n" \
"v_mul_f32_e32 v144, v54, v144\n" \
"v_mul_f32_e32 v145, v54, v145\n" \
"v_mul_f32_e32 v152, v54, v152\n" \
"v_mul_f32_e32 v153, v54, v153\n" \
"v_mul_f32_e32 v176, v55, v176\n" \
"v_mul_f32_e32 v177, v55, v177\n" \
"v_mul_f32_e32 v184, v55, v184\n" \
"v_mul_f32_e32 v185, v55, v185\n" \
"v_mul_f32_e32 v163, v55, v163\n" \
"v_cvt_pk_bf16_f32 v168, v168, v169\n" \
"v_mul_f32_e32 v170, v55, v170\n" \
"v_mul_f32_e32 v171, v55, v171\n" \
"v_cvt_pk_bf16_f32 v144, v144, v145\n" \
"v_mul_f32_e32 v146, v54, v146\n" \
"v_mul_f32_e32 v147, v54, v147\n" \
"v_cvt_pk_bf16_f32 v152, v152, v153\n" \
"v_mul_f32_e32 v154, v54, v154\n" \
"v_mul_f32_e32 v155, v54, v155\n" \
"v_cvt_pk_bf16_f32 v176, v176, v177\n" \
"v_mul_f32_e32 v178, v55, v178\n" \
"v_mul_f32_e32 v179, v55, v179\n" \
"v_cvt_pk_bf16_f32 v184, v184, v185\n" \
"v_mul_f32_e32 v186, v55, v186\n" \
"v_mul_f32_e32 v187, v55, v187\n" \
"v_cvt_pk_bf16_f32 v129, v130, v131\n" \
"v_mul_f32_e32 v132, v54, v132\n" \
"v_mul_f32_e32 v133, v54, v133\n" \
"v_mul_f32_e32 v134, v54, v134\n" \
"v_mul_f32_e32 v135, v54, v135\n" \
"v_cvt_pk_bf16_f32 v137, v138, v139\n" \
"v_mul_f32_e32 v140, v54, v140\n" \
"v_mul_f32_e32 v141, v54, v141\n" \
"v_mul_f32_e32 v142, v54, v142\n" \
"v_mul_f32_e32 v143, v54, v143\n" \
"v_cvt_pk_bf16_f32 v161, v162, v163\n" \
"v_mul_f32_e32 v164, v55, v164\n" \
"v_mul_f32_e32 v165, v55, v165\n" \
"v_mul_f32_e32 v166, v55, v166\n" \
"v_mul_f32_e32 v167, v55, v167\n" \
"v_cvt_pk_bf16_f32 v169, v170, v171\n" \
"v_mul_f32_e32 v172, v55, v172\n" \
"v_mul_f32_e32 v173, v55, v173\n" \
"v_mul_f32_e32 v174, v55, v174\n" \
"v_mul_f32_e32 v175, v55, v175\n" \
"v_cvt_pk_bf16_f32 v145, v146, v147\n" \
"v_mul_f32_e32 v148, v54, v148\n" \
"v_mul_f32_e32 v149, v54, v149\n" \
"v_mul_f32_e32 v150, v54, v150\n" \
"v_mul_f32_e32 v151, v54, v151\n" \
"v_cvt_pk_bf16_f32 v153, v154, v155\n" \
"v_mul_f32_e32 v156, v54, v156\n" \
"v_mul_f32_e32 v157, v54, v157\n" \
"v_mul_f32_e32 v158, v54, v158\n" \
"v_mul_f32_e32 v159, v54, v159\n" \
"v_cvt_pk_bf16_f32 v177, v178, v179\n" \
"v_mul_f32_e32 v180, v55, v180\n" \
"v_mul_f32_e32 v181, v55, v181\n" \
"v_mul_f32_e32 v182, v55, v182\n" \
"v_mul_f32_e32 v183, v55, v183\n" \
"v_cvt_pk_bf16_f32 v185, v186, v187\n" \
"v_mul_f32_e32 v188, v55, v188\n" \
"v_mul_f32_e32 v189, v55, v189\n" \
"v_mul_f32_e32 v190, v55, v190\n" \
"v_mul_f32_e32 v191, v55, v191\n" \
"v_cvt_pk_bf16_f32 v130, v132, v133\n" \
"v_cvt_pk_bf16_f32 v131, v134, v135\n" \
"buffer_store_dwordx4 v[128:131], v20, s[56:59], s68 offen offset:0 sc1\n" \
"v_cvt_pk_bf16_f32 v138, v140, v141\n" \
"v_cvt_pk_bf16_f32 v139, v142, v143\n" \
"buffer_store_dwordx4 v[136:139], v20, s[56:59], s68 offen offset:1024 sc1\n" \
"v_cvt_pk_bf16_f32 v162, v164, v165\n" \
"s_waitcnt vmcnt(14) lgkmcnt(0)\n" \
"s_barrier\n" \
"v_mfma_f32_16x16x32_bf16 v[88:91], a[128:131], a[48:51], v[44:47]\n" \
"s_xor_b32 m0, s65, 0x12000\n" \
"ds_read_b128 a[160:163], v49 offset:0\n" \
"v_mfma_f32_16x16x32_bf16 v[88:91], a[132:135], a[52:55], v[88:91]\n" \
"buffer_load_dwordx4 v50, s[8:11], s64 offen offset:0 sc1 lds\n" \
"v_mfma_f32_16x16x32_bf16 v[88:91], a[136:139], a[56:59], v[88:91]\n" \
"ds_read_b128 a[164:167], v49 offset:1024\n" \
"ds_read_b128 a[168:171], v49 offset:2048\n" \
"v_mfma_f32_16x16x32_bf16 v[88:91], a[140:143], a[60:63], v[88:91]\n" \
"buffer_load_dwordx4 v50, s[8:11], s64 offen offset:1024 sc1 lds\n" \
"v_mfma_f32_16x16x32_bf16 v[92:95], a[156:159], a[60:63], v[44:47]\n" \
"ds_read_b128 a[172:175], v49 offset:3072\n" \
"ds_read_b128 a[176:179], v49 offset:8192\n" \
"v_mfma_f32_16x16x32_bf16 v[92:95], a[152:155], a[56:59], v[92:95]\n" \
"ds_read_b128 a[180:183], v49 offset:9216\n" \
"ds_read_b128 a[184:187], v49 offset:10240\n" \
"v_mfma_f32_16x16x32_bf16 v[92:95], a[148:151], a[52:55], v[92:95]\n" \
"ds_read_b128 a[188:191], v49 offset:11264\n" \
"ds_read_b128 a[0:3], v49 offset:4096\n" \
"v_mfma_f32_16x16x32_bf16 v[92:95], a[144:147], a[48:51], v[92:95]\n" \
"buffer_load_dwordx4 v50, s[8:11], s64 offen offset:2048 sc1 lds\n" \
"v_mfma_f32_16x16x32_bf16 v[32:35], a[220:223], a[64:67], 0\n" \
"ds_read_b128 a[4:7], v49 offset:5120\n" \
"ds_read_b128 a[8:11], v49 offset:6144\n" \
"v_mfma_f32_16x16x32_bf16 v[36:39], a[220:223], a[80:83], 0\n" \
"ds_read_b128 a[12:15], v49 offset:7168\n" \
"ds_read_b128 a[16:19], v49 offset:12288\n" \
"v_mfma_f32_16x16x32_bf16 v[40:43], a[220:223], a[96:99], 0\n" \
"ds_read_b128 a[20:23], v49 offset:13312\n" \
"ds_read_b128 a[24:27], v49 offset:14336\n" \
"v_mfma_f32_16x16x32_bf16 v[44:47], a[220:223], a[112:115], 0\n" \
"buffer_load_dwordx4 v50, s[8:11], s64 offen offset:3072 sc1 lds\n" \
"v_mfma_f32_16x16x32_bf16 v[32:35], a[224:227], a[68:71], v[32:35]\n" \
"s_xor_b32 m0, s65, 0xa000\n" \
"ds_read_b128 a[28:31], v49 offset:15360\n" \
"v_mfma_f32_16x16x32_bf16 v[36:39], a[224:227], a[84:87], v[36:39]\n" \
"v_exp_f32_e32 v88, v88\n" \
"v_mfma_f32_16x16x32_bf16 v[40:43], a[224:227], a[100:103], v[40:43]\n" \
"buffer_load_dwordx4 v51, s[8:11], s64 offen offset:0 sc1 lds\n" \
"v_mfma_f32_16x16x32_bf16 v[44:47], a[224:227], a[116:119], v[44:47]\n" \
"v_exp_f32_e32 v89, v89\n" \
"v_mfma_f32_16x16x32_bf16 v[32:35], a[228:231], a[72:75], v[32:35]\n" \
"v_exp_f32_e32 v90, v90\n" \
"v_mfma_f32_16x16x32_bf16 v[36:39], a[228:231], a[88:91], v[36:39]\n" \
"v_exp_f32_e32 v91, v91\n" \
"v_mfma_f32_16x16x32_bf16 v[40:43], a[228:231], a[104:107], v[40:43]\n" \
"buffer_load_dwordx4 v51, s[8:11], s64 offen offset:1024 sc1 lds\n" \
"v_mfma_f32_16x16x32_bf16 v[44:47], a[228:231], a[120:123], v[44:47]\n" \
"v_exp_f32_e32 v92, v92\n" \
"v_mfma_f32_16x16x32_bf16 v[32:35], a[232:235], a[76:79], v[32:35]\n" \
"v_exp_f32_e32 v93, v93\n" \
"v_mfma_f32_16x16x32_bf16 v[36:39], a[232:235], a[92:95], v[36:39]\n" \
"v_exp_f32_e32 v94, v94\n" \
"v_mfma_f32_16x16x32_bf16 v[40:43], a[232:235], a[108:111], v[40:43]\n" \
"buffer_load_dwordx4 v51, s[8:11], s64 offen offset:2048 sc1 lds\n" \
"v_mfma_f32_16x16x32_bf16 v[44:47], a[232:235], a[124:127], v[44:47]\n" \
"v_cvt_pk_bf16_f32 v124, v88, v89\n" \
"s_waitcnt lgkmcnt(3)\n" \
"v_mfma_f32_16x16x32_bf16 v[192:195], a[160:163], v[112:115], v[192:195]\n" \
"v_exp_f32_e32 v95, v95\n" \
"v_mfma_f32_16x16x32_bf16 v[196:199], a[164:167], v[112:115], v[196:199]\n" \
"buffer_load_dwordx4 v51, s[8:11], s64 offen offset:3072 sc1 lds\n" \
"v_mfma_f32_16x16x32_bf16 v[200:203], a[168:171], v[112:115], v[200:203]\n" \
"s_add_u32 m0, s93, 0x1000\n" \
"v_cvt_pk_bf16_f32 v125, v90, v91\n" \
"v_mfma_f32_16x16x32_bf16 v[204:207], a[172:175], v[112:115], v[204:207]\n" \
"v_cvt_pk_bf16_f32 v126, v92, v93\n" \
"v_cvt_pk_bf16_f32 v127, v94, v95\n" \
"v_mfma_f32_16x16x32_bf16 v[208:211], a[176:179], v[112:115], v[208:211]\n" \
"v_and_b32_e32 v124, v12, v124\n" \
"v_and_b32_e32 v125, v13, v125\n" \
"v_mfma_f32_16x16x32_bf16 v[212:215], a[180:183], v[112:115], v[212:215]\n" \
"buffer_load_dwordx4 v4, s[80:83], s92 offen offset:0 lds\n" \
"v_mfma_f32_16x16x32_bf16 v[216:219], a[184:187], v[112:115], v[216:219]\n" \
"v_and_b32_e32 v126, v14, v126\n" \
"v_and_b32_e32 v127, v15, v127\n" \
"v_mfma_f32_16x16x32_bf16 v[220:223], a[188:191], v[112:115], v[220:223]\n" \
"ds_read_b128 a[128:131], v48 offset:0\n" \
"ds_read_b128 a[132:135], v48 offset:1024\n" \
"v_mfma_f32_16x16x32_bf16 a[200:203], a[252:255], v[112:115], a[200:203]\n" \
"ds_read_b128 a[136:139], v48 offset:2048\n" \
"ds_read_b128 a[140:143], v48 offset:3072\n" \
"v_mfma_f32_16x16x32_bf16 v[224:227], a[160:163], v[120:123], v[224:227]\n" \
"buffer_load_dwordx4 v5, s[80:83], s92 offen offset:1024 lds\n" \
"v_mfma_f32_16x16x32_bf16 v[228:231], a[164:167], v[120:123], v[228:231]\n" \
"ds_read_b128 a[144:147], v48 offset:4096\n" \
"ds_read_b128 a[148:151], v48 offset:5120\n" \
"v_mfma_f32_16x16x32_bf16 v[232:235], a[168:171], v[120:123], v[232:235]\n" \
"ds_read_b128 a[152:155], v48 offset:6144\n" \
"ds_read_b128 a[156:159], v48 offset:7168\n" \
"v_mfma_f32_16x16x32_bf16 v[236:239], a[172:175], v[120:123], v[236:239]\n" \
"buffer_load_dwordx4 v6, s[80:83], s92 offen offset:2048 lds\n" \
"v_mfma_f32_16x16x32_bf16 v[240:243], a[176:179], v[120:123], v[240:243]\n" \
"v_cvt_pk_bf16_f32 v163, v166, v167\n" \
"v_cvt_pk_bf16_f32 v170, v172, v173\n" \
"buffer_store_dwordx4 v[160:163], v20, s[56:59], s69 offen offset:0 sc1\n" \
"v_mfma_f32_16x16x32_bf16 v[244:247], a[180:183], v[120:123], v[244:247]\n" \
"v_cvt_pk_bf16_f32 v171, v174, v175\n" \
"v_cvt_pk_bf16_f32 v146, v148, v149\n" \
"buffer_store_dwordx4 v[168:171], v20, s[56:59], s69 offen offset:1024 sc1\n" \
"v_mfma_f32_16x16x32_bf16 v[248:251], a[184:187], v[120:123], v[248:251]\n" \
"v_cvt_pk_bf16_f32 v147, v150, v151\n" \
"v_cvt_pk_bf16_f32 v154, v156, v157\n" \
"buffer_store_dwordx4 v[144:147], v20, s[56:59], s68 offen offset:2048 sc1\n" \
"v_mfma_f32_16x16x32_bf16 v[252:255], a[188:191], v[120:123], v[252:255]\n" \
"buffer_load_dwordx4 v7, s[80:83], s92 offen offset:3072 lds\n" \
"v_mfma_f32_16x16x32_bf16 a[204:207], a[252:255], v[120:123], a[204:207]\n" \
"v_cvt_pk_bf16_f32 v155, v158, v159\n" \
"v_cvt_pk_bf16_f32 v178, v180, v181\n" \
"buffer_store_dwordx4 v[152:155], v20, s[56:59], s68 offen offset:3072 sc1\n" \
"v_mfma_f32_16x16x32_bf16 v[224:227], a[0:3], v[124:127], v[224:227]\n" \
"v_cvt_pk_bf16_f32 v179, v182, v183\n" \
"v_cvt_pk_bf16_f32 v186, v188, v189\n" \
"buffer_store_dwordx4 v[176:179], v20, s[56:59], s69 offen offset:2048 sc1\n" \
"v_mfma_f32_16x16x32_bf16 v[228:231], a[4:7], v[124:127], v[228:231]\n" \
"v_cvt_pk_bf16_f32 v187, v190, v191\n" \
"s_add_u32 m0, s93, 0x2000\n" \
"buffer_store_dwordx4 v[184:187], v20, s[56:59], s69 offen offset:3072 sc1\n" \
"v_mfma_f32_16x16x32_bf16 v[232:235], a[8:11], v[124:127], v[232:235]\n" \
"buffer_load_dwordx4 v8, s[80:83], s92 offen offset:0 lds\n" \
"v_mfma_f32_16x16x32_bf16 v[236:239], a[12:15], v[124:127], v[236:239]\n" \
"v_mfma_f32_16x16x32_bf16 a[208:211], a[216:219], v[128:131], a[208:211]\n" \
"v_mfma_f32_16x16x32_bf16 a[208:211], a[216:219], v[136:139], a[208:211]\n" \
"v_mfma_f32_32x32x16_bf16 v[128:143], a[216:219], a[216:219], 0\n" \
"v_mfma_f32_16x16x32_bf16 a[208:211], a[216:219], v[160:163], a[208:211]\n" \
"v_mfma_f32_16x16x32_bf16 a[208:211], a[216:219], v[168:171], a[208:211]\n" \
"v_mfma_f32_32x32x16_bf16 v[160:175], a[216:219], a[216:219], 0\n" \
"v_accvgpr_read_b32 v56, a200\n" \
"v_accvgpr_read_b32 v23, a201\n" \
"v_add_f32_e32 v56, v56, v23\n" \
"v_mfma_f32_16x16x32_bf16 a[208:211], a[216:219], v[144:147], a[208:211]\n" \
"v_max_f32_e32 v62, v62, v56\n" \
"v_readlane_b32 s95, v11, 2\n" \
"v_mfma_f32_16x16x32_bf16 v[240:243], a[16:19], v[124:127], v[240:243]\n" \
"v_add_u32_e32 v49, 0x10000, v48\n" \
"s_waitcnt lgkmcnt(0)\n" \
"v_mfma_f32_16x16x32_bf16 v[244:247], a[20:23], v[124:127], v[244:247]\n" \
"v_rcp_f32_e32 v56, v56\n" \
"v_mfma_f32_16x16x32_bf16 v[248:251], a[24:27], v[124:127], v[248:251]\n" \
"v_mfma_f32_16x16x32_bf16 a[208:211], a[216:219], v[152:155], a[208:211]\n" \
"v_mfma_f32_32x32x16_bf16 v[144:159], a[216:219], a[216:219], 0\n" \
"v_mul_f32_e32 v56, v56, v60\n" \
"v_mfma_f32_16x16x32_bf16 a[208:211], a[216:219], v[176:179], a[208:211]\n" \
"v_mul_f32_e32 v192, v56, v192\n" \
"v_mul_f32_e32 v193, v56, v193\n" \
"v_mfma_f32_16x16x32_bf16 v[252:255], a[28:31], v[124:127], v[252:255]\n" \
"v_cvt_pk_bf16_f32 v192, v192, v193\n" \
"v_mul_f32_e32 v194, v56, v194\n" \
"v_mfma_f32_16x16x32_bf16 a[204:207], a[252:255], v[124:127], a[204:207]\n" \
"v_mul_f32_e32 v195, v56, v195\n" \
"v_cvt_pk_bf16_f32 v193, v194, v195\n" \
"s_waitcnt vmcnt(19)\n" \
"s_barrier\n" \
"v_mfma_f32_16x16x32_bf16 v[64:67], a[128:131], a[64:67], v[32:35]\n" \
"s_xor_b32 m0, s65, 0x18000\n" \
"v_mul_f32_e32 v196, v56, v196\n" \
"v_mfma_f32_16x16x32_bf16 v[64:67], a[132:135], a[68:71], v[64:67]\n" \
"buffer_load_dwordx4 v52, s[8:11], s64 offen offset:0 sc1 lds\n" \
"v_mfma_f32_16x16x32_bf16 v[64:67], a[136:139], a[72:75], v[64:67]\n" \
"v_mfma_f32_16x16x32_bf16 a[208:211], a[216:219], v[184:187], a[208:211]\n" \
"v_mfma_f32_32x32x16_bf16 v[176:191], a[216:219], a[216:219], 0\n" \
"v_mul_f32_e32 v197, v56, v197\n" \
"v_mul_f32_e32 v198, v56, v198\n" \
"v_mul_f32_e32 v199, v56, v199\n" \
"v_mul_f32_e32 v200, v56, v200\n" \
"v_mul_f32_e32 v201, v56, v201\n" \
"v_accvgpr_read_b32 v57, a204\n" \
"v_mfma_f32_16x16x32_bf16 v[64:67], a[140:143], a[76:79], v[64:67]\n" \
"buffer_load_dwordx4 v52, s[8:11], s64 offen offset:1024 sc1 lds\n" \
"v_mfma_f32_16x16x32_bf16 v[68:71], a[156:159], a[76:79], v[32:35]\n" \
"v_accvgpr_read_b32 v23, a205\n" \
"v_cvt_pk_bf16_f32 v194, v196, v197\n" \
"v_mfma_f32_32x32x16_bf16 a[192:207], a[216:219], a[216:219], 0\n" \
"v_cvt_pk_bf16_f32 v195, v198, v199\n" \
"buffer_store_dwordx4 v[192:195], v20, s[56:59], s70 offen offset:0 sc1\n" \
"v_cvt_pk_bf16_f32 v200, v200, v201\n" \
"v_mfma_f32_16x16x32_bf16 v[68:71], a[152:155], a[72:75], v[68:71]\n" \
"v_mul_f32_e32 v202, v56, v202\n" \
"v_mul_f32_e32 v203, v56, v203\n" \
"v_mfma_f32_16x16x32_bf16 v[68:71], a[148:151], a[68:71], v[68:71]\n" \
"v_add_f32_e32 v57, v57, v23\n" \
"v_cvt_pk_bf16_f32 v201, v202, v203\n" \
"v_mfma_f32_16x16x32_bf16 v[68:71], a[144:147], a[64:67], v[68:71]\n" \
"buffer_load_dwordx4 v52, s[8:11], s64 offen offset:2048 sc1 lds\n" \
"v_mfma_f32_16x16x32_bf16 v[72:75], a[128:131], a[80:83], v[36:39]\n" \
"v_mfma_f32_16x16x32_bf16 a[208:211], a[216:219], v[192:195], a[208:211]\n" \
"v_mul_f32_e32 v204, v56, v204\n" \
"v_mul_f32_e32 v205, v56, v205\n" \
"v_mfma_f32_16x16x32_bf16 v[72:75], a[132:135], a[84:87], v[72:75]\n" \
"v_mul_f32_e32 v206, v56, v206\n" \
"v_mul_f32_e32 v207, v56, v207\n" \
"v_mfma_f32_16x16x32_bf16 v[72:75], a[136:139], a[88:91], v[72:75]\n" \
"v_mul_f32_e32 v208, v56, v208\n" \
"v_mul_f32_e32 v209, v56, v209\n" \
"v_mfma_f32_16x16x32_bf16 v[72:75], a[140:143], a[92:95], v[72:75]\n" \
"buffer_load_dwordx4 v52, s[8:11], s64 offen offset:3072 sc1 lds\n" \
"v_mfma_f32_16x16x32_bf16 v[76:79], a[156:159], a[92:95], v[36:39]\n" \
"s_mov_b32 m0, s65\n" \
"v_max_f32_e32 v62, v62, v57\n" \
"v_mfma_f32_16x16x32_bf16 v[76:79], a[152:155], a[88:91], v[76:79]\n" \
"v_cvt_pk_bf16_f32 v202, v204, v205\n" \
"v_cvt_pk_bf16_f32 v203, v206, v207\n" \
"buffer_store_dwordx4 v[200:203], v20, s[56:59], s70 offen offset:1024 sc1\n" \
"v_mfma_f32_16x16x32_bf16 v[76:79], a[148:151], a[84:87], v[76:79]\n" \
"v_cvt_pk_bf16_f32 v208, v208, v209\n" \
"v_mul_f32_e32 v210, v56, v210\n" \
"v_mfma_f32_16x16x32_bf16 v[76:79], a[144:147], a[80:83], v[76:79]\n" \
"buffer_load_dwordx4 v53, s[8:11], s64 offen offset:0 sc1 lds\n" \
"v_mfma_f32_16x16x32_bf16 v[80:83], a[128:131], a[96:99], v[40:43]\n" \
"v_mul_f32_e32 v211, v56, v211\n" \
"v_cvt_pk_bf16_f32 v209, v210, v211\n" \
"v_mfma_f32_16x16x32_bf16 v[80:83], a[132:135], a[100:103], v[80:83]\n" \
"v_rcp_f32_e32 v57, v57\n" \
"v_mfma_f32_16x16x32_bf16 v[80:83], a[136:139], a[104:107], v[80:83]\n" \
"buffer_load_dwordx4 v53, s[8:11], s64 offen offset:1024 sc1 lds\n" \
"v_mfma_f32_16x16x32_bf16 v[80:83], a[140:143], a[108:111], v[80:83]\n" \
"v_mfma_f32_16x16x32_bf16 a[208:211], a[216:219], v[200:203], a[208:211]\n" \
"v_mfma_f32_32x32x16_bf16 v[192:207], a[216:219], a[216:219], 0\n" \
"v_mul_f32_e32 v212, v56, v212\n" \
"v_mul_f32_e32 v213, v56, v213\n" \
"v_mul_f32_e32 v214, v56, v214\n" \
"v_mul_f32_e32 v215, v56, v215\n" \
"v_mul_f32_e32 v216, v56, v216\n" \
"v_mul_f32_e32 v217, v56, v217\n" \
"v_mfma_f32_16x16x32_bf16 v[84:87], a[156:159], a[108:111], v[40:43]\n" \
"v_cvt_pk_bf16_f32 v210, v212, v213\n" \
"v_cvt_pk_bf16_f32 v211, v214, v215\n" \
"buffer_store_dwordx4 v[208:211], v20, s[56:59], s70 offen offset:2048 sc1\n" \
"v_mfma_f32_16x16x32_bf16 v[84:87], a[152:155], a[104:107], v[84:87]\n" \
"v_cvt_pk_bf16_f32 v216, v216, v217\n" \
"v_mul_f32_e32 v218, v56, v218\n" \
"v_mfma_f32_16x16x32_bf16 v[84:87], a[148:151], a[100:103], v[84:87]\n" \
"buffer_load_dwordx4 v53, s[8:11], s64 offen offset:2048 sc1 lds\n" \
"v_mfma_f32_16x16x32_bf16 v[84:87], a[144:147], a[96:99], v[84:87]\n" \
"v_mul_f32_e32 v219, v56, v219\n" \
"v_mul_f32_e32 v57, v57, v61\n" \
"v_mfma_f32_16x16x32_bf16 v[88:91], a[128:131], a[112:115], v[44:47]\n" \
"ds_read_b128 a[128:131], v48 offset:8192\n" \
"v_cvt_pk_bf16_f32 v217, v218, v219\n" \
"v_mfma_f32_16x16x32_bf16 v[88:91], a[132:135], a[116:119], v[88:91]\n" \
"ds_read_b128 a[132:135], v48 offset:9216\n" \
"v_mul_f32_e32 v220, v56, v220\n" \
"v_mfma_f32_16x16x32_bf16 v[88:91], a[136:139], a[120:123], v[88:91]\n" \
"ds_read_b128 a[136:139], v48 offset:10240\n" \
"v_mul_f32_e32 v221, v56, v221\n" \
"v_mfma_f32_16x16x32_bf16 v[88:91], a[140:143], a[124:127], v[88:91]\n" \
"ds_read_b128 a[140:143], v48 offset:11264\n" \
"v_mul_f32_e32 v222, v56, v222\n" \
"v_mfma_f32_16x16x32_bf16 v[92:95], a[156:159], a[124:127], v[44:47]\n" \
"ds_read_b128 a[156:159], v48 offset:15360\n" \
"v_mul_f32_e32 v223, v56, v223\n" \
"v_mfma_f32_16x16x32_bf16 v[92:95], a[152:155], a[120:123], v[92:95]\n" \
"ds_read_b128 a[152:155], v48 offset:14336\n" \
"v_mul_f32_e32 v224, v57, v224\n" \
"v_mfma_f32_16x16x32_bf16 v[92:95], a[148:151], a[116:119], v[92:95]\n" \
"ds_read_b128 a[148:151], v48 offset:13312\n" \
"v_mul_f32_e32 v225, v57, v225\n" \
"v_mfma_f32_16x16x32_bf16 v[92:95], a[144:147], a[112:115], v[92:95]\n" \
"ds_read_b128 a[144:147], v48 offset:12288\n" \
"v_mfma_f32_16x16x32_bf16 a[208:211], a[216:219], v[208:211], a[208:211]\n" \
"v_cvt_pk_bf16_f32 v218, v220, v221\n" \
"v_cvt_pk_bf16_f32 v219, v222, v223\n" \
"buffer_store_dwordx4 v[216:219], v20, s[56:59], s70 offen offset:3072 sc1\n" \
"v_cvt_pk_bf16_f32 v224, v224, v225\n" \
"v_mul_f32_e32 v226, v57, v226\n" \
"v_mul_f32_e32 v227, v57, v227\n" \
"v_mfma_f32_16x16x32_bf16 a[208:211], a[216:219], v[216:219], a[208:211]\n" \
"v_mfma_f32_32x32x16_bf16 v[208:223], a[216:219], a[216:219], 0\n" \
"v_cvt_pk_bf16_f32 v225, v226, v227\n" \
"v_mul_f32_e32 v228, v57, v228\n" \
"v_mul_f32_e32 v229, v57, v229\n" \
"v_mul_f32_e32 v230, v57, v230\n" \
"v_mul_f32_e32 v231, v57, v231\n" \
"v_mul_f32_e32 v232, v57, v232\n" \
"v_mul_f32_e32 v233, v57, v233\n" \
"v_cvt_pk_bf16_f32 v226, v228, v229\n" \
"v_cvt_pk_bf16_f32 v227, v230, v231\n" \
"buffer_store_dwordx4 v[224:227], v20, s[56:59], s71 offen offset:0 sc1\n" \
"v_cvt_pk_bf16_f32 v232, v232, v233\n" \
"v_mul_f32_e32 v234, v57, v234\n" \
"v_mul_f32_e32 v235, v57, v235\n" \
"v_mfma_f32_16x16x32_bf16 a[208:211], a[216:219], v[224:227], a[208:211]\n" \
"v_cvt_pk_bf16_f32 v233, v234, v235\n" \
"v_mul_f32_e32 v236, v57, v236\n" \
"v_mul_f32_e32 v237, v57, v237\n" \
"v_mul_f32_e32 v238, v57, v238\n" \
"v_mul_f32_e32 v239, v57, v239\n" \
"v_mul_f32_e32 v240, v57, v240\n" \
"v_mul_f32_e32 v241, v57, v241\n" \
"v_cvt_pk_bf16_f32 v234, v236, v237\n" \
"v_cvt_pk_bf16_f32 v235, v238, v239\n" \
"buffer_store_dwordx4 v[232:235], v20, s[56:59], s71 offen offset:1024 sc1\n" \
"v_cvt_pk_bf16_f32 v240, v240, v241\n" \
"v_mul_f32_e32 v242, v57, v242\n" \
"v_mul_f32_e32 v243, v57, v243\n" \
"v_mfma_f32_16x16x32_bf16 a[208:211], a[216:219], v[232:235], a[208:211]\n" \
"v_mfma_f32_32x32x16_bf16 v[224:239], a[216:219], a[216:219], 0\n" \
"v_cvt_pk_bf16_f32 v241, v242, v243\n" \
"v_mul_f32_e32 v244, v57, v244\n" \
"v_mul_f32_e32 v245, v57, v245\n" \
"v_mul_f32_e32 v246, v57, v246\n" \
"v_mul_f32_e32 v247, v57, v247\n" \
"v_mul_f32_e32 v248, v57, v248\n" \
"v_mul_f32_e32 v249, v57, v249\n" \
"v_cvt_pk_bf16_f32 v242, v244, v245\n" \
"v_cvt_pk_bf16_f32 v243, v246, v247\n" \
"buffer_store_dwordx4 v[240:243], v20, s[56:59], s71 offen offset:2048 sc1\n" \
"v_cvt_pk_bf16_f32 v248, v248, v249\n" \
"v_mul_f32_e32 v250, v57, v250\n" \
"v_mul_f32_e32 v251, v57, v251\n" \
"v_mfma_f32_16x16x32_bf16 a[208:211], a[216:219], v[240:243], a[208:211]\n" \
"v_cvt_pk_bf16_f32 v249, v250, v251\n" \
"v_mul_f32_e32 v252, v57, v252\n" \
"v_mul_f32_e32 v253, v57, v253\n" \
"v_mul_f32_e32 v254, v57, v254\n" \
"v_mul_f32_e32 v255, v57, v255\n" \
"v_cvt_pk_bf16_f32 v250, v252, v253\n" \
"v_cvt_pk_bf16_f32 v251, v254, v255\n" \
"buffer_store_dwordx4 v[248:251], v20, s[56:59], s71 offen offset:3072 sc1\n" \
"buffer_load_dwordx4 v53, s[8:11], s64 offen offset:3072 sc1 lds\n" \
"v_mfma_f32_16x16x32_bf16 a[208:211], a[216:219], v[248:251], a[208:211]\n" \
"v_mfma_f32_32x32x16_bf16 v[240:255], a[216:219], a[216:219], 0\n" \
"v_cmp_lt_f32_e64 s[72:73], s100, v62\n" \
"v_exp_f32_e32 v64, v64\n" \
"v_exp_f32_e32 v65, v65\n" \
"s_xor_b32 s65, s65, 0x8000\n" \
"s_waitcnt lgkmcnt(0)\n" \
"v_exp_f32_e32 v66, v66\n" \
"v_exp_f32_e32 v67, v67\n" \
"v_accvgpr_read_b32 v63, a208\n" \
"v_cmp_u_f32_e32 vcc, v63, v63\n" \
"v_exp_f32_e32 v68, v68\n" \
"s_or_b64 vcc, s[72:73], vcc\n" \
"s_cselect_b32 s95, 0x40000000, s95\n" \
"s_cselect_b32 s94, 0x7f800000, 0\n" \
"v_exp_f32_e32 v69, v69\n" \
"v_exp_f32_e32 v70, v70\n" \
"v_exp_f32_e32 v71, v71\n" \
"v_exp_f32_e32 v72, v72\n" \
"v_exp_f32_e32 v73, v73\n" \
"v_exp_f32_e32 v74, v74\n" \
"v_exp_f32_e32 v75, v75\n" \
"v_exp_f32_e32 v76, v76\n" \
"v_exp_f32_e32 v77, v77\n" \
"v_exp_f32_e32 v78, v78\n" \
"v_exp_f32_e32 v79, v79\n" \
"v_exp_f32_e32 v80, v80\n" \
"v_exp_f32_e32 v81, v81\n" \
"v_exp_f32_e32 v82, v82\n" \
"v_exp_f32_e32 v83, v83\n" \
"v_exp_f32_e32 v84, v84\n" \
"v_exp_f32_e32 v85, v85\n" \
"v_exp_f32_e32 v86, v86\n" \
"v_exp_f32_e32 v87, v87\n" \
"s_add_u32 s64, s64, 0x10000\n" \
"v_exp_f32_e32 v88, v88\n" \
"v_exp_f32_e32 v89, v89\n" \
"v_exp_f32_e32 v90, v90\n" \
"v_exp_f32_e32 v91, v91\n" \
"v_cvt_pk_bf16_f32 v96, v64, v65\n" \
"v_cvt_pk_bf16_f32 v97, v66, v67\n" \
"v_cvt_pk_bf16_f32 v98, v68, v69\n" \
"v_cvt_pk_bf16_f32 v99, v70, v71\n" \
"v_cvt_pk_bf16_f32 v104, v72, v73\n" \
"v_cvt_pk_bf16_f32 v105, v74, v75\n" \
"v_cvt_pk_bf16_f32 v106, v76, v77\n" \
"v_cvt_pk_bf16_f32 v107, v78, v79\n" \
"v_cvt_pk_bf16_f32 v112, v80, v81\n" \
"v_cvt_pk_bf16_f32 v113, v82, v83\n" \
"v_cvt_pk_bf16_f32 v114, v84, v85\n" \
"v_cvt_pk_bf16_f32 v115, v86, v87\n" \
"v_xor_b32_e32 v48, 0x8000, v48\n" \
"s_sub_u32 s66, s19, 1\n" \
"s_cbranch_scc0 L_2_%=\n" \
"s_waitcnt vmcnt(16)\n" \
"s_barrier\n" \
"L_f1_%=:\n" \
"L_pf17:\n" \
"s_cbranch_execz L_retA\n" \
"s_nop 0\n" \
"ds_read_b128 a[160:163], v49 offset:0\n" \
"ds_read_b128 a[164:167], v49 offset:1024\n" \
"ds_read_b128 a[168:171], v49 offset:2048\n" \
"ds_read_b128 a[172:175], v49 offset:3072\n" \
"ds_read_b128 a[176:179], v49 offset:8192\n" \
"ds_read_b128 a[180:183], v49 offset:9216\n" \
"v_mfma_f32_16x16x32_bf16 v[72:75], a[128:131], a[80:83], v[36:39]\n" \
"s_add_u32 s68, s61, 0x0\n" \
"s_add_u32 s69, s61, 0x49000\n" \
"ds_read_b128 a[184:187], v49 offset:10240\n" \
"v_mfma_f32_16x16x32_bf16 v[72:75], a[132:135], a[84:87], v[72:75]\n" \
"ds_read_b128 a[188:191], v49 offset:11264\n" \
"v_add_u32_e32 v56, 0x49000, v20\n" \
"v_mfma_f32_16x16x32_bf16 v[72:75], a[136:139], a[88:91], v[72:75]\n" \
"v_readlane_b32 s97, v11, 32\n" \
"v_readlane_b32 s98, v11, 33\n" \
"v_mfma_f32_16x16x32_bf16 v[72:75], a[140:143], a[92:95], v[72:75]\n" \
"v_and_b32_e32 v96, v12, v96\n" \
"v_and_b32_e32 v97, v13, v97\n" \
"v_mfma_f32_16x16x32_bf16 v[76:79], a[156:159], a[92:95], v[36:39]\n" \
"buffer_load_dwordx4 a[32:35], v20, s[56:59], s97 offen offset:0 sc1\n" \
"v_mfma_f32_16x16x32_bf16 v[76:79], a[152:155], a[88:91], v[76:79]\n" \
"v_and_b32_e32 v98, v14, v98\n" \
"v_and_b32_e32 v99, v15, v99\n" \
"v_mfma_f32_16x16x32_bf16 v[76:79], a[148:151], a[84:87], v[76:79]\n" \
"ds_read_b128 a[0:3], v20 offset:8192\n" \
"v_mov_b32_e32 v61, s62\n" \
"v_mfma_f32_16x16x32_bf16 v[76:79], a[144:147], a[80:83], v[76:79]\n" \
"buffer_load_dwordx4 a[40:43], v56, s[56:59], s97 offen offset:0 sc1\n" \
"ds_read_b128 a[4:7], v20 offset:9216\n" \
"ds_read_b128 a[8:11], v20 offset:10240\n" \
"s_waitcnt lgkmcnt(10)\n" \
"v_mfma_f32_16x16x32_bf16 v[128:131], a[160:163], v[96:99], v[128:131]\n" \
"ds_read_b128 a[12:15], v20 offset:11264\n" \
"s_waitcnt lgkmcnt(10)\n" \
"v_mfma_f32_16x16x32_bf16 v[132:135], a[164:167], v[96:99], v[132:135]\n" \
"ds_read_b128 a[28:31], v20 offset:15360\n" \
"buffer_load_dwordx4 a[48:51], v20, s[56:59], s98 offen offset:0 sc1\n" \
"s_waitcnt lgkmcnt(10)\n" \
"v_mfma_f32_16x16x32_bf16 v[136:139], a[168:171], v[96:99], v[136:139]\n" \
"ds_read_b128 a[24:27], v20 offset:14336\n" \
"s_waitcnt lgkmcnt(10)\n" \
"v_mfma_f32_16x16x32_bf16 v[140:143], a[172:175], v[96:99], v[140:143]\n" \
"ds_read_b128 a[20:23], v20 offset:13312\n" \
"buffer_load_dwordx4 a[56:59], v56, s[56:59], s98 offen offset:0 sc1\n" \
"s_waitcnt lgkmcnt(9)\n" \
"v_mfma_f32_16x16x32_bf16 v[144:147], a[176:179], v[96:99], v[144:147]\n" \
"ds_read_b128 a[16:19], v20 offset:12288\n" \
"v_add_u32_e32 v50, s15, v48\n" \
"v_mfma_f32_16x16x32_bf16 v[148:151], a[180:183], v[96:99], v[148:151]\n" \
"v_xor_b32_e32 v48, 0x4000, v49\n" \
"s_waitcnt lgkmcnt(8)\n" \
"v_mfma_f32_16x16x32_bf16 v[152:155], a[184:187], v[96:99], v[152:155]\n" \
"buffer_load_dwordx4 a[36:39], v20, s[56:59], s97 offen offset:1024 sc1\n" \
"v_mfma_f32_16x16x32_bf16 v[156:159], a[188:191], v[96:99], v[156:159]\n" \
"ds_read_b32 v58, v18 offset:256\n" \
"ds_read_b32 v59, v18 offset:384\n" \
"v_mfma_f32_16x16x32_bf16 a[192:195], a[252:255], v[96:99], a[192:195]\n" \
"ds_read_b128 a[96:99], v48 offset:4096\n" \
"ds_read_b128 a[100:103], v48 offset:5120\n" \
"buffer_load_dwordx4 a[44:47], v56, s[56:59], s97 offen offset:1024 sc1\n" \
"v_mfma_f32_16x16x32_bf16 v[160:163], a[160:163], v[104:107], v[160:163]\n" \
"ds_read_b128 a[104:107], v48 offset:6144\n" \
"ds_read_b128 a[108:111], v48 offset:7168\n" \
"v_mfma_f32_16x16x32_bf16 v[164:167], a[164:167], v[104:107], v[164:167]\n" \
"ds_read_b128 a[112:115], v48 offset:12288\n" \
"ds_read_b128 a[116:119], v48 offset:13312\n" \
"buffer_load_dwordx4 a[52:55], v20, s[56:59], s98 offen offset:1024 sc1\n" \
"v_mfma_f32_16x16x32_bf16 v[168:171], a[168:171], v[104:107], v[168:171]\n" \
"ds_read_b128 a[120:123], v48 offset:14336\n" \
"ds_read_b128 a[124:127], v48 offset:15360\n" \
"v_mfma_f32_16x16x32_bf16 v[172:175], a[172:175], v[104:107], v[172:175]\n" \
"ds_read_b128 a[128:131], v49 offset:4096\n" \
"ds_read_b128 a[132:135], v49 offset:5120\n" \
"buffer_load_dwordx4 a[60:63], v56, s[56:59], s98 offen offset:1024 sc1\n" \
"v_mfma_f32_16x16x32_bf16 v[176:179], a[176:179], v[104:107], v[176:179]\n" \
"ds_read_b128 a[136:139], v49 offset:6144\n" \
"ds_read_b128 a[140:143], v49 offset:7168\n" \
"v_mfma_f32_16x16x32_bf16 v[180:183], a[180:183], v[104:107], v[180:183]\n" \
"ds_read_b128 a[144:147], v49 offset:12288\n" \
"ds_read_b128 a[148:151], v49 offset:13312\n" \
"s_waitcnt vmcnt(8)\n" \
"s_mov_b64 exec, 1\n" \
"buffer_store_dword v61, off, s[56:59], s95 offset:0 sc1\n" \
"s_mov_b64 exec, -1\n" \
"v_mfma_f32_16x16x32_bf16 v[184:187], a[184:187], v[104:107], v[184:187]\n" \
"ds_read_b128 a[152:155], v49 offset:14336\n" \
"ds_read_b128 a[156:159], v49 offset:15360\n" \
"v_mfma_f32_16x16x32_bf16 v[188:191], a[188:191], v[104:107], v[188:191]\n" \
"v_accvgpr_read_b32 v40, a200\n" \
"v_accvgpr_read_b32 v23, a201\n" \
"v_mfma_f32_16x16x32_bf16 a[196:199], a[252:255], v[104:107], a[196:199]\n" \
"v_add_f32_e32 v40, v40, v23\n" \
"s_waitcnt lgkmcnt(15)\n" \
"v_mfma_f32_16x16x32_bf16 v[80:83], a[0:3], a[64:67], v[32:35]\n" \
"buffer_load_dwordx4 a[220:223], v20, s[56:59], s60 offen offset:0 sc1\n" \
"v_mfma_f32_16x16x32_bf16 v[80:83], a[4:7], a[68:71], v[80:83]\n" \
"v_accvgpr_read_b32 v41, a204\n" \
"v_accvgpr_read_b32 v23, a205\n" \
"v_mfma_f32_16x16x32_bf16 v[80:83], a[8:11], a[72:75], v[80:83]\n" \
"v_add_f32_e32 v41, v41, v23\n" \
"v_add_u32_e32 v51, 0x10000, v50\n" \
"v_mfma_f32_16x16x32_bf16 v[80:83], a[12:15], a[76:79], v[80:83]\n" \
"buffer_load_dwordx4 a[236:239], v56, s[56:59], s60 offen offset:0 sc1\n" \
"v_mfma_f32_16x16x32_bf16 v[84:87], a[28:31], a[76:79], v[32:35]\n" \
"v_add_f32_e32 v40, s94, v40\n" \
"v_add_f32_e32 v41, s94, v41\n" \
"v_mfma_f32_16x16x32_bf16 v[84:87], a[24:27], a[72:75], v[84:87]\n" \
"ds_write_b128 v50, v[192:195] offset:0\n" \
"buffer_load_dwordx4 a[224:227], v20, s[56:59], s60 offen offset:1024 sc1\n" \
"v_mfma_f32_16x16x32_bf16 v[84:87], a[20:23], a[68:71], v[84:87]\n" \
"ds_write_b128 v50, v[196:199] offset:1024\n" \
"v_mfma_f32_16x16x32_bf16 v[84:87], a[16:19], a[64:67], v[84:87]\n" \
"ds_write_b128 v50, v[200:203] offset:2048\n" \
"buffer_load_dwordx4 a[240:243], v56, s[56:59], s60 offen offset:1024 sc1\n" \
"v_mfma_f32_16x16x32_bf16 v[88:91], a[0:3], a[80:83], v[36:39]\n" \
"ds_write_b128 v50, v[204:207] offset:3072\n" \
"v_mfma_f32_16x16x32_bf16 v[88:91], a[4:7], a[84:87], v[88:91]\n" \
"ds_write_b128 v50, v[208:211] offset:8192\n" \
"buffer_load_dwordx4 a[228:231], v20, s[56:59], s60 offen offset:2048 sc1\n" \
"v_mfma_f32_16x16x32_bf16 v[88:91], a[8:11], a[88:91], v[88:91]\n" \
"ds_write_b128 v50, v[212:215] offset:9216\n" \
"v_mfma_f32_16x16x32_bf16 v[88:91], a[12:15], a[92:95], v[88:91]\n" \
"ds_write_b128 v50, v[216:219] offset:10240\n" \
"buffer_load_dwordx4 a[244:247], v56, s[56:59], s60 offen offset:2048 sc1\n" \
"v_mfma_f32_16x16x32_bf16 v[92:95], a[28:31], a[92:95], v[36:39]\n" \
"ds_write_b128 v50, v[220:223] offset:11264\n" \
"ds_write_b128 v51, v[224:227] offset:0\n" \
"L_pf18:\n" \
"s_cbranch_execz L_retA\n" \
"s_nop 0\n" \
"ds_write_b128 v51, v[228:231] offset:1024\n" \
"buffer_load_dwordx4 a[232:235], v20, s[56:59], s60 offen offset:3072 sc1\n" \
"ds_write_b128 v51, v[232:235] offset:2048\n" \
"ds_write_b128 v51, v[236:239] offset:3072\n" \
"ds_write_b128 v51, v[240:243] offset:8192\n" \
"ds_write_b128 v51, v[244:247] offset:9216\n" \
"buffer_load_dwordx4 a[248:251], v56, s[56:59], s60 offen offset:3072 sc1\n" \
"ds_write_b128 v51, v[248:251] offset:10240\n" \
"ds_write_b128 v51, v[252:255] offset:11264\n" \
"ds_write_b64 v19, v[40:41]\n" \
"v_mfma_f32_16x16x32_bf16 v[92:95], a[24:27], a[88:91], v[92:95]\n" \
"v_xor_b32_e32 v52, 0x4000, v50\n" \
"v_xor_b32_e32 v53, 0x4000, v51\n" \
"v_mfma_f32_16x16x32_bf16 v[92:95], a[20:23], a[84:87], v[92:95]\n" \
"v_exp_f32_e32 v72, v72\n" \
"v_mfma_f32_16x16x32_bf16 v[92:95], a[16:19], a[80:83], v[92:95]\n" \
"v_exp_f32_e32 v73, v73\n" \
"v_exp_f32_e32 v74, v74\n" \
"v_exp_f32_e32 v75, v75\n" \
"v_exp_f32_e32 v76, v76\n" \
"v_exp_f32_e32 v77, v77\n" \
"v_exp_f32_e32 v78, v78\n" \
"v_exp_f32_e32 v79, v79\n" \
"v_cvt_pk_bf16_f32 v108, v72, v73\n" \
"v_cvt_pk_bf16_f32 v109, v74, v75\n" \
"v_cvt_pk_bf16_f32 v110, v76, v77\n" \
"v_cvt_pk_bf16_f32 v111, v78, v79\n" \
"v_and_b32_e32 v108, v12, v108\n" \
"v_and_b32_e32 v109, v13, v109\n" \
"v_and_b32_e32 v110, v14, v110\n" \
"v_and_b32_e32 v111, v15, v111\n" \
"v_readlane_b32 s93, v11, 18\n" \
"v_readlane_b32 s84, v11, 13\n" \
"v_readlane_b32 s85, v11, 14\n" \
"v_readlane_b32 s86, v11, 15\n" \
"s_mov_b32 s87, 0x20000\n" \
"v_readlane_b32 s92, v11, 16\n" \
"s_add_u32 m0, s93, 0x0\n" \
"v_xor_b32_e32 v63, 0x400, v19\n" \
"s_waitcnt lgkmcnt(0)\n" \
"s_barrier\n" \
"ds_read_b128 v[192:195], v52 offset:0\n" \
"ds_read_b128 v[196:199], v52 offset:1024\n" \
"ds_read_b128 v[200:203], v52 offset:2048\n" \
"ds_read_b128 v[204:207], v52 offset:3072\n" \
"ds_read_b128 v[208:211], v52 offset:8192\n" \
"ds_read_b128 v[212:215], v52 offset:9216\n" \
"ds_read_b128 v[216:219], v52 offset:10240\n" \
"ds_read_b128 v[220:223], v52 offset:11264\n" \
"ds_read_b128 v[224:227], v53 offset:0\n" \
"ds_read_b128 v[228:231], v53 offset:1024\n" \
"ds_read_b128 v[232:235], v53 offset:2048\n" \
"ds_read_b128 v[236:239], v53 offset:3072\n" \
"ds_read_b128 v[240:243], v53 offset:8192\n" \
"ds_read_b128 v[244:247], v53 offset:9216\n" \
"ds_read_b128 v[248:251], v53 offset:10240\n" \
"ds_read_b128 v[252:255], v53 offset:11264\n" \
"v_mfma_f32_16x16x32_bf16 v[160:163], a[128:131], v[108:111], v[160:163]\n" \
"buffer_load_dwordx4 v0, s[84:87], s92 offen offset:0 lds\n" \
"v_mfma_f32_16x16x32_bf16 v[164:167], a[132:135], v[108:111], v[164:167]\n" \
"v_exp_f32_e32 v80, v80\n" \
"v_mfma_f32_16x16x32_bf16 v[168:171], a[136:139], v[108:111], v[168:171]\n" \
"v_exp_f32_e32 v81, v81\n" \
"v_mfma_f32_16x16x32_bf16 v[172:175], a[140:143], v[108:111], v[172:175]\n" \
"buffer_load_dwordx4 v1, s[84:87], s92 offen offset:1024 lds\n" \
"v_mfma_f32_16x16x32_bf16 v[176:179], a[144:147], v[108:111], v[176:179]\n" \
"ds_read_b64 v[42:43], v63\n" \
"v_cvt_pk_bf16_f32 v116, v80, v81\n" \
"v_mfma_f32_16x16x32_bf16 v[180:183], a[148:151], v[108:111], v[180:183]\n" \
"v_exp_f32_e32 v82, v82\n" \
"v_mfma_f32_16x16x32_bf16 v[184:187], a[152:155], v[108:111], v[184:187]\n" \
"buffer_load_dwordx4 v2, s[84:87], s92 offen offset:2048 lds\n" \
"v_mfma_f32_16x16x32_bf16 v[188:191], a[156:159], v[108:111], v[188:191]\n" \
"v_exp_f32_e32 v83, v83\n" \
"v_mfma_f32_16x16x32_bf16 a[196:199], a[252:255], v[108:111], a[196:199]\n" \
"v_exp_f32_e32 v84, v84\n" \
"v_exp_f32_e32 v85, v85\n" \
"v_exp_f32_e32 v86, v86\n" \
"s_waitcnt lgkmcnt(1)\n" \
"s_barrier\n" \
"buffer_load_dwordx4 v3, s[84:87], s92 offen offset:3072 lds\n" \
"s_add_u32 m0, s93, 0x1000\n" \
"v_exp_f32_e32 v87, v87\n" \
"v_cvt_pk_bf16_f32 v117, v82, v83\n" \
"v_cvt_pk_bf16_f32 v118, v84, v85\n" \
"v_cvt_pk_bf16_f32 v119, v86, v87\n" \
"v_exp_f32_e32 v88, v88\n" \
"v_exp_f32_e32 v89, v89\n" \
"v_mfma_f32_16x16x32_bf16 v[128:131], a[96:99], v[116:119], v[128:131]\n" \
"buffer_load_dwordx4 v4, s[84:87], s92 offen offset:0 lds\n" \
"v_mfma_f32_16x16x32_bf16 v[132:135], a[100:103], v[116:119], v[132:135]\n" \
"v_exp_f32_e32 v90, v90\n" \
"v_mfma_f32_16x16x32_bf16 v[136:139], a[104:107], v[116:119], v[136:139]\n" \
"v_exp_f32_e32 v91, v91\n" \
"v_mfma_f32_16x16x32_bf16 v[140:143], a[108:111], v[116:119], v[140:143]\n" \
"buffer_load_dwordx4 v5, s[84:87], s92 offen offset:1024 lds\n" \
"v_mfma_f32_16x16x32_bf16 v[144:147], a[112:115], v[116:119], v[144:147]\n" \
"v_exp_f32_e32 v92, v92\n" \
"v_mfma_f32_16x16x32_bf16 v[148:151], a[116:119], v[116:119], v[148:151]\n" \
"v_exp_f32_e32 v93, v93\n" \
"v_mfma_f32_16x16x32_bf16 v[152:155], a[120:123], v[116:119], v[152:155]\n" \
"buffer_load_dwordx4 v6, s[84:87], s92 offen offset:2048 lds\n" \
"v_mfma_f32_16x16x32_bf16 v[156:159], a[124:127], v[116:119], v[156:159]\n" \
"v_exp_f32_e32 v94, v94\n" \
"v_mfma_f32_16x16x32_bf16 a[192:195], a[252:255], v[116:119], a[192:195]\n" \
"v_exp_f32_e32 v95, v95\n" \
"v_cvt_pk_bf16_f32 v124, v88, v89\n" \
"v_cvt_pk_bf16_f32 v125, v90, v91\n" \
"v_cvt_pk_bf16_f32 v126, v92, v93\n" \
"v_cvt_pk_bf16_f32 v127, v94, v95\n" \
"v_add_f32_e32 v160, v160, v224\n" \
"v_add_f32_e32 v161, v161, v225\n" \
"v_add_f32_e32 v162, v162, v226\n" \
"v_add_f32_e32 v163, v163, v227\n" \
"v_add_f32_e32 v164, v164, v228\n" \
"v_add_f32_e32 v165, v165, v229\n" \
"v_mfma_f32_16x16x32_bf16 v[160:163], a[96:99], v[124:127], v[160:163]\n" \
"buffer_load_dwordx4 v7, s[84:87], s92 offen offset:3072 lds\n" \
"s_add_u32 m0, s93, 0x2000\n" \
"v_add_f32_e32 v166, v166, v230\n" \
"v_add_f32_e32 v167, v167, v231\n" \
"v_add_f32_e32 v168, v168, v232\n" \
"v_add_f32_e32 v169, v169, v233\n" \
"v_mfma_f32_16x16x32_bf16 v[164:167], a[100:103], v[124:127], v[164:167]\n" \
"v_add_f32_e32 v170, v170, v234\n" \
"v_add_f32_e32 v171, v171, v235\n" \
"v_add_f32_e32 v172, v172, v236\n" \
"v_add_f32_e32 v173, v173, v237\n" \
"v_mfma_f32_16x16x32_bf16 v[168:171], a[104:107], v[124:127], v[168:171]\n" \
"buffer_load_dwordx4 v8, s[84:87], s92 offen offset:0 lds\n" \
"v_add_f32_e32 v174, v174, v238\n" \
"v_add_f32_e32 v175, v175, v239\n" \
"v_accvgpr_read_b32 v54, a192\n" \
"v_accvgpr_read_b32 v23, a193\n" \
"v_mfma_f32_16x16x32_bf16 v[172:175], a[108:111], v[124:127], v[172:175]\n" \
"v_add_f32_e32 v54, v54, v23\n" \
"s_waitcnt lgkmcnt(0)\n" \
"v_add_f32_e32 v54, v54, v42\n" \
"v_max_f32_e32 v62, v62, v54\n" \
"v_add_f32_e32 v176, v176, v240\n" \
"v_add_f32_e32 v177, v177, v241\n" \
"v_add_f32_e32 v178, v178, v242\n" \
"v_add_f32_e32 v179, v179, v243\n" \
"v_rcp_f32_e32 v54, v54\n" \
"v_add_f32_e32 v128, v128, v192\n" \
"v_mfma_f32_16x16x32_bf16 v[176:179], a[112:115], v[124:127], v[176:179]\n" \
"v_add_f32_e32 v129, v129, v193\n" \
"v_add_f32_e32 v130, v130, v194\n" \
"v_add_f32_e32 v131, v131, v195\n" \
"v_add_f32_e32 v132, v132, v196\n" \
"v_add_f32_e32 v133, v133, v197\n" \
"v_add_f32_e32 v134, v134, v198\n" \
"v_add_f32_e32 v135, v135, v199\n" \
"v_mul_f32_e32 v54, v54, v58\n" \
"v_mul_f32_e32 v128, v54, v128\n" \
"v_mul_f32_e32 v129, v54, v129\n" \
"v_mul_f32_e32 v130, v54, v130\n" \
"v_mul_f32_e32 v131, v54, v131\n" \
"v_mul_f32_e32 v132, v54, v132\n" \
"v_mul_f32_e32 v133, v54, v133\n" \
"v_mul_f32_e32 v134, v54, v134\n" \
"v_mul_f32_e32 v135, v54, v135\n" \
"v_add_f32_e32 v180, v180, v244\n" \
"v_add_f32_e32 v181, v181, v245\n" \
"v_add_f32_e32 v182, v182, v246\n" \
"v_add_f32_e32 v183, v183, v247\n" \
"v_add_f32_e32 v136, v136, v200\n" \
"v_add_f32_e32 v137, v137, v201\n" \
"v_add_f32_e32 v138, v138, v202\n" \
"v_add_f32_e32 v139, v139, v203\n" \
"v_add_f32_e32 v140, v140, v204\n" \
"v_add_f32_e32 v141, v141, v205\n" \
"v_add_f32_e32 v142, v142, v206\n" \
"v_add_f32_e32 v143, v143, v207\n" \
"v_cvt_pk_bf16_f32 v64, v128, v129\n" \
"v_cvt_pk_bf16_f32 v65, v130, v131\n" \
"v_cvt_pk_bf16_f32 v66, v132, v133\n" \
"v_cvt_pk_bf16_f32 v67, v134, v135\n" \
"buffer_store_dwordx4 v[64:67], v20, s[56:59], s68 offen offset:0 sc1\n" \
"v_mfma_f32_16x16x32_bf16 v[180:183], a[116:119], v[124:127], v[180:183]\n" \
"v_mfma_f32_16x16x32_bf16 a[208:211], a[216:219], v[64:67], a[208:211]\n" \
"v_mul_f32_e32 v136, v54, v136\n" \
"v_mul_f32_e32 v137, v54, v137\n" \
"v_mul_f32_e32 v138, v54, v138\n" \
"v_mul_f32_e32 v139, v54, v139\n" \
"v_mul_f32_e32 v140, v54, v140\n" \
"v_mul_f32_e32 v141, v54, v141\n" \
"v_mul_f32_e32 v142, v54, v142\n" \
"v_mul_f32_e32 v143, v54, v143\n" \
"v_add_f32_e32 v144, v144, v208\n" \
"v_add_f32_e32 v145, v145, v209\n" \
"v_add_f32_e32 v146, v146, v210\n" \
"v_add_f32_e32 v147, v147, v211\n" \
"v_add_f32_e32 v148, v148, v212\n" \
"v_add_f32_e32 v149, v149, v213\n" \
"v_add_f32_e32 v150, v150, v214\n" \
"v_add_f32_e32 v151, v151, v215\n" \
"v_cvt_pk_bf16_f32 v68, v136, v137\n" \
"v_cvt_pk_bf16_f32 v69, v138, v139\n" \
"v_cvt_pk_bf16_f32 v70, v140, v141\n" \
"v_cvt_pk_bf16_f32 v71, v142, v143\n" \
"buffer_store_dwordx4 v[68:71], v20, s[56:59], s68 offen offset:1024 sc1\n" \
"v_mul_f32_e32 v144, v54, v144\n" \
"v_mfma_f32_16x16x32_bf16 a[208:211], a[216:219], v[68:71], a[208:211]\n" \
"v_mul_f32_e32 v145, v54, v145\n" \
"v_mul_f32_e32 v146, v54, v146\n" \
"v_mul_f32_e32 v147, v54, v147\n" \
"v_mul_f32_e32 v148, v54, v148\n" \
"v_mul_f32_e32 v149, v54, v149\n" \
"v_mul_f32_e32 v150, v54, v150\n" \
"v_mul_f32_e32 v151, v54, v151\n" \
"v_add_f32_e32 v184, v184, v248\n" \
"v_add_f32_e32 v185, v185, v249\n" \
"v_add_f32_e32 v186, v186, v250\n" \
"v_add_f32_e32 v187, v187, v251\n" \
"v_add_f32_e32 v152, v152, v216\n" \
"v_add_f32_e32 v153, v153, v217\n" \
"v_add_f32_e32 v154, v154, v218\n" \
"v_add_f32_e32 v155, v155, v219\n" \
"v_add_f32_e32 v156, v156, v220\n" \
"v_add_f32_e32 v157, v157, v221\n" \
"v_add_f32_e32 v158, v158, v222\n" \
"v_add_f32_e32 v159, v159, v223\n" \
"v_cvt_pk_bf16_f32 v72, v144, v145\n" \
"v_cvt_pk_bf16_f32 v73, v146, v147\n" \
"v_cvt_pk_bf16_f32 v74, v148, v149\n" \
"v_cvt_pk_bf16_f32 v75, v150, v151\n" \
"buffer_store_dwordx4 v[72:75], v20, s[56:59], s68 offen offset:2048 sc1\n" \
"v_mfma_f32_16x16x32_bf16 v[184:187], a[120:123], v[124:127], v[184:187]\n" \
"v_mfma_f32_16x16x32_bf16 a[208:211], a[216:219], v[72:75], a[208:211]\n" \
"v_mul_f32_e32 v152, v54, v152\n" \
"v_mul_f32_e32 v153, v54, v153\n" \
"v_mul_f32_e32 v154, v54, v154\n" \
"v_mul_f32_e32 v155, v54, v155\n" \
"v_mul_f32_e32 v156, v54, v156\n" \
"v_mul_f32_e32 v157, v54, v157\n" \
"v_mul_f32_e32 v158, v54, v158\n" \
"v_mul_f32_e32 v159, v54, v159\n" \
"v_cvt_pk_bf16_f32 v76, v152, v153\n" \
"v_cvt_pk_bf16_f32 v77, v154, v155\n" \
"v_cvt_pk_bf16_f32 v78, v156, v157\n" \
"v_cvt_pk_bf16_f32 v79, v158, v159\n" \
"buffer_store_dwordx4 v[76:79], v20, s[56:59], s68 offen offset:3072 sc1\n" \
"v_add_f32_e32 v188, v188, v252\n" \
"v_mfma_f32_16x16x32_bf16 a[208:211], a[216:219], v[76:79], a[208:211]\n" \
"v_add_f32_e32 v189, v189, v253\n" \
"v_add_f32_e32 v190, v190, v254\n" \
"v_add_f32_e32 v191, v191, v255\n" \
"s_nop 1\n" \
"v_mfma_f32_16x16x32_bf16 v[188:191], a[124:127], v[124:127], v[188:191]\n" \
"v_mfma_f32_16x16x32_bf16 a[196:199], a[252:255], v[124:127], a[196:199]\n" \
"s_nop 7\n" \
"v_accvgpr_read_b32 v55, a196\n" \
"v_accvgpr_read_b32 v23, a197\n" \
"v_add_f32_e32 v55, v55, v23\n" \
"v_add_f32_e32 v55, v55, v43\n" \
"v_max_f32_e32 v62, v62, v55\n" \
"v_rcp_f32_e32 v55, v55\n" \
"v_cmp_lt_f32_e64 s[72:73], s100, v62\n" \
"v_mul_f32_e32 v55, v55, v59\n" \
"v_mul_f32_e32 v160, v55, v160\n" \
"v_mul_f32_e32 v161, v55, v161\n" \
"v_mul_f32_e32 v162, v55, v162\n" \
"v_mul_f32_e32 v163, v55, v163\n" \
"v_mul_f32_e32 v164, v55, v164\n" \
"v_mul_f32_e32 v165, v55, v165\n" \
"v_mul_f32_e32 v166, v55, v166\n" \
"v_mul_f32_e32 v167, v55, v167\n" \
"v_cvt_pk_bf16_f32 v80, v160, v161\n" \
"v_cvt_pk_bf16_f32 v81, v162, v163\n" \
"v_cvt_pk_bf16_f32 v82, v164, v165\n" \
"v_cvt_pk_bf16_f32 v83, v166, v167\n" \
"buffer_store_dwordx4 v[80:83], v20, s[56:59], s69 offen offset:0 sc1\n" \
"v_mul_f32_e32 v168, v55, v168\n" \
"v_mfma_f32_16x16x32_bf16 a[208:211], a[216:219], v[80:83], a[208:211]\n" \
"v_mul_f32_e32 v169, v55, v169\n" \
"v_mul_f32_e32 v170, v55, v170\n" \
"v_mul_f32_e32 v171, v55, v171\n" \
"v_mul_f32_e32 v172, v55, v172\n" \
"v_mul_f32_e32 v173, v55, v173\n" \
"v_mul_f32_e32 v174, v55, v174\n" \
"v_mul_f32_e32 v175, v55, v175\n" \
"v_cvt_pk_bf16_f32 v84, v168, v169\n" \
"v_cvt_pk_bf16_f32 v85, v170, v171\n" \
"v_cvt_pk_bf16_f32 v86, v172, v173\n" \
"v_cvt_pk_bf16_f32 v87, v174, v175\n" \
"buffer_store_dwordx4 v[84:87], v20, s[56:59], s69 offen offset:1024 sc1\n" \
"v_mul_f32_e32 v176, v55, v176\n" \
"v_mfma_f32_16x16x32_bf16 a[208:211], a[216:219], v[84:87], a[208:211]\n" \
"v_mul_f32_e32 v177, v55, v177\n" \
"v_mul_f32_e32 v178, v55, v178\n" \
"v_mul_f32_e32 v179, v55, v179\n" \
"v_mul_f32_e32 v180, v55, v180\n" \
"v_mul_f32_e32 v181, v55, v181\n" \
"v_mul_f32_e32 v182, v55, v182\n" \
"v_mul_f32_e32 v183, v55, v183\n" \
"v_cvt_pk_bf16_f32 v96, v176, v177\n" \
"v_cvt_pk_bf16_f32 v97, v178, v179\n" \
"v_cvt_pk_bf16_f32 v98, v180, v181\n" \
"v_cvt_pk_bf16_f32 v99, v182, v183\n" \
"buffer_store_dwordx4 v[96:99], v20, s[56:59], s69 offen offset:2048 sc1\n" \
"v_mul_f32_e32 v184, v55, v184\n" \
"v_mfma_f32_16x16x32_bf16 a[208:211], a[216:219], v[96:99], a[208:211]\n" \
"v_mul_f32_e32 v185, v55, v185\n" \
"v_mul_f32_e32 v186, v55, v186\n" \
"v_mul_f32_e32 v187, v55, v187\n" \
"v_mul_f32_e32 v188, v55, v188\n" \
"v_mul_f32_e32 v189, v55, v189\n" \
"v_mul_f32_e32 v190, v55, v190\n" \
"v_mul_f32_e32 v191, v55, v191\n" \
"v_cvt_pk_bf16_f32 v100, v184, v185\n" \
"v_cvt_pk_bf16_f32 v101, v186, v187\n" \
"v_cvt_pk_bf16_f32 v102, v188, v189\n" \
"v_cvt_pk_bf16_f32 v103, v190, v191\n" \
"buffer_store_dwordx4 v[100:103], v20, s[56:59], s69 offen offset:3072 sc1\n" \
"s_nop 0\n" \
"v_mfma_f32_16x16x32_bf16 a[208:211], a[216:219], v[100:103], a[208:211]\n" \
"s_nop 7\n" \
"v_accvgpr_read_b32 v63, a208\n" \
"v_cmp_u_f32_e32 vcc, v63, v63\n" \
"s_nop 0\n" \
"s_or_b64 vcc, s[72:73], vcc\n" \
"s_cbranch_scc1 L_end_%=\n" \
"L_out_%=:\n" \
"L_pf19:\n" \
"s_cbranch_execz L_retA\n" \
"s_nop 0\n" \
"v_add_u32_e32 v34, 0x20000, v10\n" \
"v_mov_b32_e32 v39, s62\n" \
"v_readlane_b32 s70, v11, 4\n" \
"v_bfe_u32 v38, v20, 2, 4\n" \
"v_readlane_b32 s64, v11, 1\n" \
"v_readlane_b32 s71, v11, 3\n" \
"v_add_u32_e32 v33, 0xfc00, v10\n" \
"v_readlane_b32 s99, v11, 19\n" \
"v_readlane_b32 s80, v11, 9\n" \
"v_readlane_b32 s81, v11, 10\n" \
"s_mov_b32 s82, 0x47fca0\n" \
"s_mov_b32 s83, 0x20000\n" \
"ds_read_b64_tr_b16 a[0:1], v34 offset:0\n" \
"ds_read_b64_tr_b16 a[2:3], v34 offset:4608\n" \
"ds_read_b64_tr_b16 a[4:5], v34 offset:9216\n" \
"ds_read_b64_tr_b16 a[6:7], v34 offset:13824\n" \
"ds_read_b64_tr_b16 a[8:9], v34 offset:18432\n" \
"ds_read_b64_tr_b16 a[10:11], v34 offset:23040\n" \
"ds_read_b64_tr_b16 a[12:13], v34 offset:32\n" \
"ds_read_b64_tr_b16 a[14:15], v34 offset:4640\n" \
"ds_read_b64_tr_b16 a[16:17], v34 offset:9248\n" \
"ds_read_b64_tr_b16 a[18:19], v34 offset:13856\n" \
"buffer_load_dword v37, v38, s[56:59], s64 offen sc1\n" \
"ds_read_b64_tr_b16 a[20:21], v34 offset:18464\n" \
"ds_read_b64_tr_b16 a[22:23], v34 offset:23072\n" \
"ds_read_b64_tr_b16 a[24:25], v34 offset:64\n" \
"ds_read_b64_tr_b16 a[26:27], v34 offset:4672\n" \
"ds_read_b64_tr_b16 a[28:29], v34 offset:9280\n" \
"ds_read_b64_tr_b16 a[30:31], v34 offset:13888\n" \
"ds_read_b64_tr_b16 a[192:193], v34 offset:18496\n" \
"ds_read_b64_tr_b16 a[194:195], v34 offset:23104\n" \
"ds_read_b64_tr_b16 a[196:197], v34 offset:96\n" \
"ds_read_b64_tr_b16 a[198:199], v34 offset:4704\n" \
"ds_read_b64_tr_b16 a[200:201], v34 offset:9312\n" \
"ds_read_b64_tr_b16 a[202:203], v34 offset:13920\n" \
"ds_read_b64_tr_b16 a[204:205], v34 offset:18528\n" \
"ds_read_b64_tr_b16 a[206:207], v34 offset:23136\n" \
"ds_read_b64_tr_b16 a[208:209], v34 offset:128\n" \
"ds_read_b64_tr_b16 a[210:211], v34 offset:4736\n" \
"ds_read_b64_tr_b16 a[212:213], v34 offset:9344\n" \
"ds_read_b64_tr_b16 a[214:215], v34 offset:13952\n" \
"ds_read_b64_tr_b16 a[216:217], v34 offset:18560\n" \
"ds_read_b64_tr_b16 a[218:219], v34 offset:23168\n" \
"v_readlane_b32 s65, v11, 23\n" \
"s_add_u32 m0, s99, 0x6c00\n" \
"v_readlane_b32 s66, v11, 26\n" \
"v_add_u32_e32 v32, 0x49000, v20\n" \
"v_lshrrev_b32_e32 v44, 9, v20\n" \
"v_mul_u32_u24_e32 v44, 0xb40, v44\n" \
"v_add_u32_e32 v35, v44, v10\n" \
"v_readlane_b32 s67, v11, 24\n" \
"v_readlane_b32 s92, v11, 20\n" \
"v_readlane_b32 s73, v11, 25\n" \
"s_waitcnt vmcnt(18) lgkmcnt(15)\n" \
"v_mfma_f32_16x16x32_bf16 v[112:115], a[0:3], a[220:223], 0\n" \
"buffer_load_dwordx4 v0, s[80:83], s65 offen offset:0 lds\n" \
"v_mfma_f32_16x16x32_bf16 v[112:115], a[4:7], a[224:227], v[112:115]\n" \
"v_readlane_b32 s94, v11, 21\n" \
"v_readlane_b32 s84, v11, 7\n" \
"v_mfma_f32_16x16x32_bf16 v[112:115], a[8:11], a[228:231], v[112:115]\n" \
"v_readlane_b32 s85, v11, 8\n" \
"s_mov_b32 s86, 0xa1ca0\n" \
"v_mfma_f32_16x16x32_bf16 v[116:119], a[8:11], a[244:247], 0\n" \
"s_mov_b32 s87, 0x20000\n" \
"v_readlane_b32 s68, v11, 27\n" \
"v_mfma_f32_16x16x32_bf16 v[116:119], a[4:7], a[240:243], v[116:119]\n" \
"v_readlane_b32 s69, v11, 28\n" \
"v_readlane_b32 s72, v11, 30\n" \
"v_mfma_f32_16x16x32_bf16 v[116:119], a[0:3], a[236:239], v[116:119]\n" \
"buffer_load_dwordx4 v1, s[80:83], s65 offen offset:1024 lds\n" \
"v_mfma_f32_16x16x32_bf16 v[184:187], a[0:3], v[64:67], 0\n" \
"v_readlane_b32 s93, v11, 31\n" \
"v_lshlrev_b32_e32 v41, 12, v20\n" \
"v_mfma_f32_16x16x32_bf16 v[184:187], a[4:7], v[68:71], v[184:187]\n" \
"v_readlane_b32 s63, v11, 37\n" \
"v_bfe_u32 v40, v20, 2, 7\n" \
"v_mfma_f32_16x16x32_bf16 v[184:187], a[8:11], v[72:75], v[184:187]\n" \
"s_add_u32 s74, s63, 0x3800\n" \
"v_bfe_u32 v44, v20, 4, 4\n" \
"v_mfma_f32_16x16x32_bf16 v[188:191], a[8:11], v[96:99], 0\n" \
"ds_read_b64_tr_b16 a[8:9], v34 offset:18592\n" \
"ds_read_b64_tr_b16 a[10:11], v34 offset:23200\n" \
"v_mfma_f32_16x16x32_bf16 v[188:191], a[4:7], v[84:87], v[188:191]\n" \
"ds_read_b64_tr_b16 a[4:5], v34 offset:9376\n" \
"ds_read_b64_tr_b16 a[6:7], v34 offset:13984\n" \
"v_mfma_f32_16x16x32_bf16 v[188:191], a[0:3], v[80:83], v[188:191]\n" \
"ds_read_b64_tr_b16 a[0:1], v34 offset:160\n" \
"ds_read_b64_tr_b16 a[2:3], v34 offset:4768\n" \
"v_mfma_f32_16x16x32_bf16 v[120:123], a[12:15], a[220:223], 0\n" \
"buffer_load_dwordx4 v2, s[80:83], s65 offen offset:2048 lds\n" \
"v_mfma_f32_16x16x32_bf16 v[120:123], a[16:19], a[224:227], v[120:123]\n" \
"v_bfe_u32 v45, v20, 8, 2\n" \
"v_mul_u32_u24_e32 v42, 0x480, v44\n" \
"v_mfma_f32_16x16x32_bf16 v[120:123], a[20:23], a[228:231], v[120:123]\n" \
"v_lshl_add_u32 v42, v45, 3, v42\n" \
"v_and_b32_e32 v43, 1, v45\n" \
"v_mfma_f32_16x16x32_bf16 v[124:127], a[20:23], a[244:247], 0\n" \
"v_lshrrev_b32_e32 v44, 1, v44\n" \
"v_lshlrev_b32_e32 v45, 1, v45\n" \
"v_mfma_f32_16x16x32_bf16 v[124:127], a[16:19], a[240:243], v[124:127]\n" \
"v_sub_u32_e32 v44, v44, v45\n" \
"v_and_b32_e32 v45, 16, v20\n" \
"v_mfma_f32_16x16x32_bf16 v[124:127], a[12:15], a[236:239], v[124:127]\n" \
"buffer_load_dwordx4 v3, s[80:83], s65 offen offset:3072 lds\n" \
"v_mfma_f32_16x16x32_bf16 v[192:195], a[12:15], v[64:67], 0\n" \
"s_add_u32 m0, s99, 0x7c00\n" \
"v_mov_b32_e32 v46, 0x3f80\n" \
"v_mfma_f32_16x16x32_bf16 v[192:195], a[16:19], v[68:71], v[192:195]\n" \
"v_lshlrev_b32_e32 v45, v45, v46\n" \
"v_mov_b32_e32 v46, s62\n" \
"v_mfma_f32_16x16x32_bf16 v[192:195], a[20:23], v[72:75], v[192:195]\n" \
"v_readlane_b32 s95, v11, 34\n" \
"v_readlane_b32 s96, v11, 35\n" \
"v_mfma_f32_16x16x32_bf16 v[196:199], a[20:23], v[96:99], 0\n" \
"ds_read_b64_tr_b16 a[20:21], v34 offset:18624\n" \
"ds_read_b64_tr_b16 a[22:23], v34 offset:23232\n" \
"v_mfma_f32_16x16x32_bf16 v[196:199], a[16:19], v[84:87], v[196:199]\n" \
"ds_read_b64_tr_b16 a[16:17], v34 offset:9408\n" \
"ds_read_b64_tr_b16 a[18:19], v34 offset:14016\n" \
"v_mfma_f32_16x16x32_bf16 v[196:199], a[12:15], v[80:83], v[196:199]\n" \
"ds_read_b64_tr_b16 a[12:13], v34 offset:192\n" \
"ds_read_b64_tr_b16 a[14:15], v34 offset:4800\n" \
"v_mfma_f32_16x16x32_bf16 v[128:131], a[24:27], a[220:223], 0\n" \
"v_readlane_b32 s97, v11, 36\n" \
"s_waitcnt lgkmcnt(8)\n" \
"v_mfma_f32_16x16x32_bf16 v[128:131], a[28:31], a[224:227], v[128:131]\n" \
"buffer_load_dwordx4 v4, s[80:83], s65 offen offset:0 lds\n" \
"v_mfma_f32_16x16x32_bf16 v[128:131], a[192:195], a[228:231], v[128:131]\n" \
"v_readlane_b32 s100, v11, 6\n" \
"v_mov_b32_e32 v52, 0\n" \
"v_mfma_f32_16x16x32_bf16 v[132:135], a[192:195], a[244:247], 0\n" \
"v_mov_b32_e32 v53, 0\n" \
"v_mov_b32_e32 v48, 0\n" \
"v_mfma_f32_16x16x32_bf16 v[132:135], a[28:31], a[240:243], v[132:135]\n" \
"v_mov_b32_e32 v49, 0\n" \
"v_mul_u32_u24_e32 v43, 0x8ff8, v43\n" \
"v_mfma_f32_16x16x32_bf16 v[132:135], a[24:27], a[236:239], v[132:135]\n" \
"v_add_u32_e32 v42, v42, v43\n" \
"v_readlane_b32 s88, v11, 11\n" \
"v_mfma_f32_16x16x32_bf16 v[200:203], a[24:27], v[64:67], 0\n" \
"buffer_load_dwordx4 v5, s[80:83], s65 offen offset:1024 lds\n" \
"v_mfma_f32_16x16x32_bf16 v[200:203], a[28:31], v[68:71], v[200:203]\n" \
"v_readlane_b32 s89, v11, 12\n" \
"s_mov_b32 s90, 0x240000\n" \
"v_mfma_f32_16x16x32_bf16 v[200:203], a[192:195], v[72:75], v[200:203]\n" \
"s_mov_b32 s91, 0x20000\n" \
"v_mfma_f32_16x16x32_bf16 v[204:207], a[192:195], v[96:99], 0\n" \
"ds_read_b64_tr_b16 a[192:193], v34 offset:18656\n" \
"ds_read_b64_tr_b16 a[194:195], v34 offset:23264\n" \
"v_mfma_f32_16x16x32_bf16 v[204:207], a[28:31], v[84:87], v[204:207]\n" \
"ds_read_b64_tr_b16 a[28:29], v34 offset:9440\n" \
"ds_read_b64_tr_b16 a[30:31], v34 offset:14048\n" \
"v_mfma_f32_16x16x32_bf16 v[204:207], a[24:27], v[80:83], v[204:207]\n" \
"ds_read_b64_tr_b16 a[24:25], v34 offset:224\n" \
"ds_read_b64_tr_b16 a[26:27], v34 offset:4832\n" \
"v_mfma_f32_16x16x32_bf16 v[136:139], a[196:199], a[220:223], 0\n" \
"buffer_load_dwordx4 v6, s[80:83], s65 offen offset:2048 lds\n" \
"v_mfma_f32_16x16x32_bf16 v[136:139], a[200:203], a[224:227], v[136:139]\n" \
"v_mfma_f32_16x16x32_bf16 v[136:139], a[204:207], a[228:231], v[136:139]\n" \
"v_mfma_f32_16x16x32_bf16 v[140:143], a[204:207], a[244:247], 0\n" \
"v_mfma_f32_16x16x32_bf16 v[140:143], a[200:203], a[240:243], v[140:143]\n" \
"v_mfma_f32_16x16x32_bf16 v[140:143], a[196:199], a[236:239], v[140:143]\n" \
"buffer_load_dwordx4 v7, s[80:83], s65 offen offset:3072 lds\n" \
"v_mfma_f32_16x16x32_bf16 v[208:211], a[196:199], v[64:67], 0\n" \
"s_add_u32 m0, s99, 0x8c00\n" \
"v_mfma_f32_16x16x32_bf16 v[208:211], a[200:203], v[68:71], v[208:211]\n" \
"v_mfma_f32_16x16x32_bf16 v[208:211], a[204:207], v[72:75], v[208:211]\n" \
"v_mfma_f32_16x16x32_bf16 v[212:215], a[204:207], v[96:99], 0\n" \
"ds_read_b64_tr_b16 a[204:205], v34 offset:18688\n" \
"ds_read_b64_tr_b16 a[206:207], v34 offset:23296\n" \
"v_mfma_f32_16x16x32_bf16 v[212:215], a[200:203], v[84:87], v[212:215]\n" \
"ds_read_b64_tr_b16 a[200:201], v34 offset:9472\n" \
"ds_read_b64_tr_b16 a[202:203], v34 offset:14080\n" \
"v_mfma_f32_16x16x32_bf16 v[212:215], a[196:199], v[80:83], v[212:215]\n" \
"ds_read_b64_tr_b16 a[196:197], v34 offset:256\n" \
"ds_read_b64_tr_b16 a[198:199], v34 offset:4864\n" \
"v_mfma_f32_16x16x32_bf16 v[144:147], a[208:211], a[220:223], 0\n" \
"buffer_load_dwordx4 v8, s[80:83], s65 offen offset:0 lds\n" \
"v_mfma_f32_16x16x32_bf16 v[144:147], a[212:215], a[224:227], v[144:147]\n" \
"s_waitcnt vmcnt(9)\n" \
"v_cmp_ne_u32_e32 vcc, s62, v37\n" \
"v_mfma_f32_16x16x32_bf16 v[144:147], a[216:219], a[228:231], v[144:147]\n" \
"s_add_u32 m0, s99, 0x14400\n" \
"v_readlane_b32 s65, v11, 42\n" \
"v_mfma_f32_16x16x32_bf16 v[148:151], a[216:219], a[244:247], 0\n" \
"v_mfma_f32_16x16x32_bf16 v[148:151], a[212:215], a[240:243], v[148:151]\n" \
"s_cbranch_vccnz L_sa_%=\n" \
"L_ra_%=:\n" \
"v_readlane_b32 s64, v11, 29\n" \
"v_mfma_f32_16x16x32_bf16 v[148:151], a[208:211], a[236:239], v[148:151]\n" \
"buffer_load_dwordx4 a[64:67], v20, s[56:59], s66 offen offset:0 sc1\n" \
"v_mfma_f32_16x16x32_bf16 v[216:219], a[208:211], v[64:67], 0\n" \
"v_mfma_f32_16x16x32_bf16 v[216:219], a[212:215], v[68:71], v[216:219]\n" \
"v_mfma_f32_16x16x32_bf16 v[216:219], a[216:219], v[72:75], v[216:219]\n" \
"v_mfma_f32_16x16x32_bf16 v[220:223], a[216:219], v[96:99], 0\n" \
"v_mfma_f32_16x16x32_bf16 v[220:223], a[212:215], v[84:87], v[220:223]\n" \
"buffer_load_dwordx4 a[68:71], v20, s[56:59], s66 offen offset:1024 sc1\n" \
"v_mfma_f32_16x16x32_bf16 v[220:223], a[208:211], v[80:83], v[220:223]\n" \
"s_waitcnt lgkmcnt(5)\n" \
"v_mfma_f32_16x16x32_bf16 v[152:155], a[0:3], a[220:223], 0\n" \
"v_mfma_f32_16x16x32_bf16 v[152:155], a[4:7], a[224:227], v[152:155]\n" \
"v_mfma_f32_16x16x32_bf16 v[152:155], a[8:11], a[228:231], v[152:155]\n" \
"v_mfma_f32_16x16x32_bf16 v[156:159], a[8:11], a[244:247], 0\n" \
"buffer_load_dwordx4 a[84:87], v32, s[56:59], s66 offen offset:1024 sc1\n" \
"v_mfma_f32_16x16x32_bf16 v[156:159], a[4:7], a[240:243], v[156:159]\n" \
"v_mfma_f32_16x16x32_bf16 v[156:159], a[0:3], a[236:239], v[156:159]\n" \
"v_mfma_f32_16x16x32_bf16 v[224:227], a[0:3], v[64:67], 0\n" \
"v_mfma_f32_16x16x32_bf16 v[224:227], a[4:7], v[68:71], v[224:227]\n" \
"v_mfma_f32_16x16x32_bf16 v[224:227], a[8:11], v[72:75], v[224:227]\n" \
"buffer_load_dwordx4 a[80:83], v32, s[56:59], s66 offen offset:0 sc1\n" \
"v_mfma_f32_16x16x32_bf16 v[228:231], a[8:11], v[96:99], 0\n" \
"v_mfma_f32_16x16x32_bf16 v[228:231], a[4:7], v[84:87], v[228:231]\n" \
"v_mfma_f32_16x16x32_bf16 v[228:231], a[0:3], v[80:83], v[228:231]\n" \
"v_mfma_f32_16x16x32_bf16 v[160:163], a[12:15], a[220:223], 0\n" \
"v_mfma_f32_16x16x32_bf16 v[160:163], a[16:19], a[224:227], v[160:163]\n" \
"buffer_load_dwordx4 a[72:75], v20, s[56:59], s66 offen offset:2048 sc1\n" \
"v_mfma_f32_16x16x32_bf16 v[160:163], a[20:23], a[228:231], v[160:163]\n" \
"v_mfma_f32_16x16x32_bf16 v[164:167], a[20:23], a[244:247], 0\n" \
"v_mfma_f32_16x16x32_bf16 v[164:167], a[16:19], a[240:243], v[164:167]\n" \
"v_mfma_f32_16x16x32_bf16 v[164:167], a[12:15], a[236:239], v[164:167]\n" \
"v_mfma_f32_16x16x32_bf16 v[232:235], a[12:15], v[64:67], 0\n" \
"buffer_load_dwordx4 a[76:79], v20, s[56:59], s66 offen offset:3072 sc1\n" \
"v_mfma_f32_16x16x32_bf16 v[232:235], a[16:19], v[68:71], v[232:235]\n" \
"v_mfma_f32_16x16x32_bf16 v[232:235], a[20:23], v[72:75], v[232:235]\n" \
"v_mfma_f32_16x16x32_bf16 v[236:239], a[20:23], v[96:99], 0\n" \
"v_mfma_f32_16x16x32_bf16 v[236:239], a[16:19], v[84:87], v[236:239]\n" \
"v_mfma_f32_16x16x32_bf16 v[236:239], a[12:15], v[80:83], v[236:239]\n" \
"buffer_load_dwordx4 a[92:95], v32, s[56:59], s66 offen offset:3072 sc1\n" \
"s_waitcnt lgkmcnt(0)\n" \
"s_barrier\n" \
"v_mfma_f32_16x16x32_bf16 v[168:171], a[24:27], a[220:223], 0\n" \
"ds_read_b64_tr_b16 a[208:209], v33 offset:9216\n" \
"ds_read_b64_tr_b16 a[210:211], v33 offset:13824\n" \
"v_mfma_f32_16x16x32_bf16 v[168:171], a[28:31], a[224:227], v[168:171]\n" \
"ds_read_b64_tr_b16 a[212:213], v33 offset:9248\n" \
"ds_read_b64_tr_b16 a[214:215], v33 offset:13856\n" \
"v_mfma_f32_16x16x32_bf16 v[168:171], a[192:195], a[228:231], v[168:171]\n" \
"ds_read_b64_tr_b16 a[216:217], v33 offset:9280\n" \
"ds_read_b64_tr_b16 a[218:219], v33 offset:13888\n" \
"s_mov_b64 exec, 1\n" \
"buffer_store_dword v39, off, s[56:59], s70 offset:0 sc1\n" \
"s_mov_b64 exec, -1\n" \
"v_mfma_f32_16x16x32_bf16 v[172:175], a[192:195], a[244:247], 0\n" \
"ds_read_b64_tr_b16 a[0:1], v33 offset:9312\n" \
"ds_read_b64_tr_b16 a[2:3], v33 offset:13920\n" \
"v_mfma_f32_16x16x32_bf16 v[172:175], a[28:31], a[240:243], v[172:175]\n" \
"ds_read_b64_tr_b16 a[4:5], v33 offset:9344\n" \
"ds_read_b64_tr_b16 a[6:7], v33 offset:13952\n" \
"v_mfma_f32_16x16x32_bf16 v[172:175], a[24:27], a[236:239], v[172:175]\n" \
"ds_read_b64_tr_b16 a[8:9], v33 offset:9376\n" \
"ds_read_b64_tr_b16 a[10:11], v33 offset:13984\n" \
"v_mfma_f32_16x16x32_bf16 v[240:243], a[24:27], v[64:67], 0\n" \
"ds_read_b64_tr_b16 a[12:13], v33 offset:9408\n" \
"ds_read_b64_tr_b16 a[14:15], v33 offset:14016\n" \
"v_mfma_f32_16x16x32_bf16 v[240:243], a[28:31], v[68:71], v[240:243]\n" \
"ds_read_b64_tr_b16 a[16:17], v33 offset:9440\n" \
"ds_read_b64_tr_b16 a[18:19], v33 offset:14048\n" \
"v_mfma_f32_16x16x32_bf16 v[240:243], a[192:195], v[72:75], v[240:243]\n" \
"ds_read_b64_tr_b16 a[20:21], v33 offset:9472\n" \
"ds_read_b64_tr_b16 a[22:23], v33 offset:14080\n" \
"v_mfma_f32_16x16x32_bf16 v[244:247], a[192:195], v[96:99], 0\n" \
"ds_read_b64_tr_b16 a[192:193], v35 offset:32\n" \
"ds_read_b64_tr_b16 a[194:195], v35 offset:2336\n" \
"v_mfma_f32_16x16x32_bf16 v[244:247], a[28:31], v[84:87], v[244:247]\n" \
"ds_read_b64_tr_b16 a[28:29], v35 offset:10368\n" \
"ds_read_b64_tr_b16 a[30:31], v35 offset:12672\n" \
"v_mfma_f32_16x16x32_bf16 v[244:247], a[24:27], v[80:83], v[244:247]\n" \
"ds_read_b64_tr_b16 a[24:25], v35 offset:0\n" \
"ds_read_b64_tr_b16 a[26:27], v35 offset:2304\n" \
"v_mfma_f32_16x16x32_bf16 v[176:179], a[196:199], a[220:223], 0\n" \
"buffer_load_dwordx4 a[88:91], v32, s[56:59], s66 offen offset:2048 sc1\n" \
"v_mfma_f32_16x16x32_bf16 v[176:179], a[200:203], a[224:227], v[176:179]\n" \
"v_readlane_b32 s66, v11, 43\n" \
"v_readlane_b32 s70, v11, 45\n" \
"v_mfma_f32_16x16x32_bf16 v[176:179], a[204:207], a[228:231], v[176:179]\n" \
"v_mfma_f32_16x16x32_bf16 v[180:183], a[204:207], a[244:247], 0\n" \
"v_mfma_f32_16x16x32_bf16 v[180:183], a[200:203], a[240:243], v[180:183]\n" \
"v_mfma_f32_16x16x32_bf16 v[180:183], a[196:199], a[236:239], v[180:183]\n" \
"buffer_load_dwordx4 v0, s[80:83], s67 offen offset:0 lds\n" \
"v_mfma_f32_16x16x32_bf16 v[248:251], a[196:199], v[64:67], 0\n" \
"v_mfma_f32_16x16x32_bf16 v[248:251], a[200:203], v[68:71], v[248:251]\n" \
"v_mfma_f32_16x16x32_bf16 v[248:251], a[204:207], v[72:75], v[248:251]\n" \
"v_mfma_f32_16x16x32_bf16 v[252:255], a[204:207], v[96:99], 0\n" \
"ds_read_b64_tr_b16 a[204:205], v35 offset:10432\n" \
"ds_read_b64_tr_b16 a[206:207], v35 offset:12736\n" \
"v_mfma_f32_16x16x32_bf16 v[252:255], a[200:203], v[84:87], v[252:255]\n" \
"ds_read_b64_tr_b16 a[200:201], v35 offset:64\n" \
"ds_read_b64_tr_b16 a[202:203], v35 offset:2368\n" \
"v_mfma_f32_16x16x32_bf16 v[252:255], a[196:199], v[80:83], v[252:255]\n" \
"ds_read_b64_tr_b16 a[196:197], v35 offset:10400\n" \
"s_waitcnt lgkmcnt(9)\n" \
"v_mfma_f32_16x16x32_bf16 v[112:115], a[208:211], a[232:235], v[112:115]\n" \
"L_pf20:\n" \
"s_cbranch_execz L_pf22\n" \
"s_nop 0\n" \
"buffer_load_dwordx4 v1, s[80:83], s67 offen offset:1024 lds\n" \
"v_mfma_f32_16x16x32_bf16 v[116:119], a[208:211], a[248:251], v[116:119]\n" \
"ds_read_b64_tr_b16 a[198:199], v35 offset:12704\n" \
"v_mfma_f32_16x16x32_bf16 v[184:187], a[208:211], v[76:79], v[184:187]\n" \
"v_mfma_f32_16x16x32_bf16 v[188:191], a[208:211], v[100:103], v[188:191]\n" \
"ds_read_b64_tr_b16 a[208:209], v35 offset:96\n" \
"ds_read_b64_tr_b16 a[210:211], v35 offset:2400\n" \
"v_mfma_f32_16x16x32_bf16 v[120:123], a[212:215], a[232:235], v[120:123]\n" \
"v_mfma_f32_16x16x32_bf16 v[124:127], a[212:215], a[248:251], v[124:127]\n" \
"buffer_load_dwordx4 v2, s[80:83], s67 offen offset:2048 lds\n" \
"v_mfma_f32_16x16x32_bf16 v[192:195], a[212:215], v[76:79], v[192:195]\n" \
"v_mfma_f32_16x16x32_bf16 v[196:199], a[212:215], v[100:103], v[196:199]\n" \
"ds_read_b64_tr_b16 a[212:213], v35 offset:10464\n" \
"ds_read_b64_tr_b16 a[214:215], v35 offset:12768\n" \
"v_mfma_f32_16x16x32_bf16 v[128:131], a[216:219], a[232:235], v[128:131]\n" \
"v_mfma_f32_16x16x32_bf16 v[132:135], a[216:219], a[248:251], v[132:135]\n" \
"v_mfma_f32_16x16x32_bf16 v[200:203], a[216:219], v[76:79], v[200:203]\n" \
"buffer_load_dwordx4 v3, s[80:83], s67 offen offset:3072 lds\n" \
"v_mfma_f32_16x16x32_bf16 v[204:207], a[216:219], v[100:103], v[204:207]\n" \
"ds_read_b64_tr_b16 a[216:217], v35 offset:128\n" \
"ds_read_b64_tr_b16 a[218:219], v35 offset:2432\n" \
"v_mfma_f32_16x16x32_bf16 v[136:139], a[0:3], a[232:235], v[136:139]\n" \
"s_add_u32 m0, s99, 0x15400\n" \
"v_mfma_f32_16x16x32_bf16 v[140:143], a[0:3], a[248:251], v[140:143]\n" \
"v_mfma_f32_16x16x32_bf16 v[208:211], a[0:3], v[76:79], v[208:211]\n" \
"v_mfma_f32_16x16x32_bf16 v[212:215], a[0:3], v[100:103], v[212:215]\n" \
"ds_read_b64_tr_b16 a[0:1], v35 offset:10496\n" \
"ds_read_b64_tr_b16 a[2:3], v35 offset:12800\n" \
"v_mfma_f32_16x16x32_bf16 v[144:147], a[4:7], a[232:235], v[144:147]\n" \
"buffer_load_dwordx4 v4, s[80:83], s67 offen offset:0 lds\n" \
"v_mfma_f32_16x16x32_bf16 v[148:151], a[4:7], a[248:251], v[148:151]\n" \
"v_mfma_f32_16x16x32_bf16 v[216:219], a[4:7], v[76:79], v[216:219]\n" \
"v_mfma_f32_16x16x32_bf16 v[220:223], a[4:7], v[100:103], v[220:223]\n" \
"ds_read_b64_tr_b16 a[4:5], v35 offset:160\n" \
"ds_read_b64_tr_b16 a[6:7], v35 offset:2464\n" \
"v_mfma_f32_16x16x32_bf16 v[152:155], a[8:11], a[232:235], v[152:155]\n" \
"v_mfma_f32_16x16x32_bf16 v[156:159], a[8:11], a[248:251], v[156:159]\n" \
"buffer_load_dwordx4 v5, s[80:83], s67 offen offset:1024 lds\n" \
"v_mfma_f32_16x16x32_bf16 v[224:227], a[8:11], v[76:79], v[224:227]\n" \
"v_mfma_f32_16x16x32_bf16 v[228:231], a[8:11], v[100:103], v[228:231]\n" \
"ds_read_b64_tr_b16 a[8:9], v35 offset:10528\n" \
"ds_read_b64_tr_b16 a[10:11], v35 offset:12832\n" \
"v_mfma_f32_16x16x32_bf16 v[160:163], a[12:15], a[232:235], v[160:163]\n" \
"v_mfma_f32_16x16x32_bf16 v[164:167], a[12:15], a[248:251], v[164:167]\n" \
"v_mfma_f32_16x16x32_bf16 v[232:235], a[12:15], v[76:79], v[232:235]\n" \
"buffer_load_dwordx4 v6, s[80:83], s67 offen offset:2048 lds\n" \
"v_mfma_f32_16x16x32_bf16 v[236:239], a[12:15], v[100:103], v[236:239]\n" \
"ds_read_b64_tr_b16 a[12:13], v35 offset:192\n" \
"ds_read_b64_tr_b16 a[14:15], v35 offset:2496\n" \
"v_mfma_f32_16x16x32_bf16 v[168:171], a[16:19], a[232:235], v[168:171]\n" \
"v_mfma_f32_16x16x32_bf16 v[172:175], a[16:19], a[248:251], v[172:175]\n" \
"v_mfma_f32_16x16x32_bf16 v[240:243], a[16:19], v[76:79], v[240:243]\n" \
"v_mfma_f32_16x16x32_bf16 v[244:247], a[16:19], v[100:103], v[244:247]\n" \
"buffer_load_dwordx4 v7, s[80:83], s67 offen offset:3072 lds\n" \
"v_mfma_f32_16x16x32_bf16 v[176:179], a[20:23], a[232:235], v[176:179]\n" \
"s_add_u32 m0, s99, 0x16400\n" \
"ds_read_b64_tr_b16 a[16:17], v35 offset:10560\n" \
"v_mfma_f32_16x16x32_bf16 v[180:183], a[20:23], a[248:251], v[180:183]\n" \
"ds_read_b64_tr_b16 a[18:19], v35 offset:12864\n" \
"v_mfma_f32_16x16x32_bf16 v[248:251], a[20:23], v[76:79], v[248:251]\n" \
"v_mfma_f32_16x16x32_bf16 v[252:255], a[20:23], v[100:103], v[252:255]\n" \
"ds_read_b64_tr_b16 a[20:21], v35 offset:224\n" \
"s_waitcnt lgkmcnt(7)\n" \
"v_mfma_f32_16x16x32_bf16 v[112:115], a[24:27], a[32:35], v[112:115]\n" \
"buffer_load_dwordx4 v8, s[80:83], s67 offen offset:0 lds\n" \
"v_mfma_f32_16x16x32_bf16 v[112:115], a[28:31], a[36:39], v[112:115]\n" \
"s_add_u32 m0, s92, 0x0\n" \
"ds_read_b64_tr_b16 a[22:23], v35 offset:2528\n" \
"v_mfma_f32_16x16x32_bf16 v[116:119], a[28:31], a[44:47], v[116:119]\n" \
"v_readlane_b32 s67, v11, 44\n" \
"v_mfma_f32_16x16x32_bf16 v[116:119], a[24:27], a[40:43], v[116:119]\n" \
"v_mfma_f32_16x16x32_bf16 v[184:187], a[24:27], a[48:51], v[184:187]\n" \
"v_mfma_f32_16x16x32_bf16 v[184:187], a[28:31], a[52:55], v[184:187]\n" \
"buffer_load_dword v47, v38, s[56:59], s71 offen sc1\n" \
"v_mfma_f32_16x16x32_bf16 v[188:191], a[28:31], a[60:63], v[188:191]\n" \
"ds_read_b64_tr_b16 a[28:29], v35 offset:256\n" \
"ds_read_b64_tr_b16 a[30:31], v35 offset:2560\n" \
"v_mfma_f32_16x16x32_bf16 v[188:191], a[24:27], a[56:59], v[188:191]\n" \
"ds_read_b64_tr_b16 a[24:25], v35 offset:10592\n" \
"ds_read_b64_tr_b16 a[26:27], v35 offset:12896\n" \
"v_mfma_f32_16x16x32_bf16 v[120:123], a[192:195], a[32:35], v[120:123]\n" \
"v_mfma_f32_16x16x32_bf16 v[120:123], a[196:199], a[36:39], v[120:123]\n" \
"v_mfma_f32_16x16x32_bf16 v[124:127], a[196:199], a[44:47], v[124:127]\n" \
"buffer_load_dwordx4 v0, s[80:83], s73 offen offset:0 lds\n" \
"v_mfma_f32_16x16x32_bf16 v[124:127], a[192:195], a[40:43], v[124:127]\n" \
"v_mfma_f32_16x16x32_bf16 v[192:195], a[192:195], a[48:51], v[192:195]\n" \
"v_mfma_f32_16x16x32_bf16 v[192:195], a[196:199], a[52:55], v[192:195]\n" \
"v_mfma_f32_16x16x32_bf16 v[196:199], a[196:199], a[60:63], v[196:199]\n" \
"v_mfma_f32_16x16x32_bf16 v[196:199], a[192:195], a[56:59], v[196:199]\n" \
"buffer_load_dwordx4 v1, s[80:83], s73 offen offset:1024 lds\n" \
"v_mfma_f32_16x16x32_bf16 v[128:131], a[200:203], a[32:35], v[128:131]\n" \
"ds_read_b64_tr_b16 a[192:193], v35 offset:10624\n" \
"ds_read_b64_tr_b16 a[194:195], v35 offset:12928\n" \
"v_mfma_f32_16x16x32_bf16 v[128:131], a[204:207], a[36:39], v[128:131]\n" \
"v_mfma_f32_16x16x32_bf16 v[132:135], a[204:207], a[44:47], v[132:135]\n" \
"v_mfma_f32_16x16x32_bf16 v[132:135], a[200:203], a[40:43], v[132:135]\n" \
"v_mfma_f32_16x16x32_bf16 v[200:203], a[200:203], a[48:51], v[200:203]\n" \
"buffer_load_dwordx4 v2, s[80:83], s73 offen offset:2048 lds\n" \
"v_mfma_f32_16x16x32_bf16 v[200:203], a[204:207], a[52:55], v[200:203]\n" \
"v_mfma_f32_16x16x32_bf16 v[204:207], a[204:207], a[60:63], v[204:207]\n" \
"v_mfma_f32_16x16x32_bf16 v[204:207], a[200:203], a[56:59], v[204:207]\n" \
"v_mfma_f32_16x16x32_bf16 v[136:139], a[208:211], a[32:35], v[136:139]\n" \
"v_mfma_f32_16x16x32_bf16 v[136:139], a[212:215], a[36:39], v[136:139]\n" \
"buffer_load_dwordx4 v3, s[80:83], s73 offen offset:3072 lds\n" \
"v_mfma_f32_16x16x32_bf16 v[140:143], a[212:215], a[44:47], v[140:143]\n" \
"s_add_u32 m0, s92, 0x1000\n" \
"v_mfma_f32_16x16x32_bf16 v[140:143], a[208:211], a[40:43], v[140:143]\n" \
"v_mfma_f32_16x16x32_bf16 v[208:211], a[208:211], a[48:51], v[208:211]\n" \
"v_mfma_f32_16x16x32_bf16 v[208:211], a[212:215], a[52:55], v[208:211]\n" \
"v_mfma_f32_16x16x32_bf16 v[212:215], a[212:215], a[60:63], v[212:215]\n" \
"buffer_load_dwordx4 v4, s[80:83], s73 offen offset:0 lds\n" \
"v_mfma_f32_16x16x32_bf16 v[212:215], a[208:211], a[56:59], v[212:215]\n" \
"v_mfma_f32_16x16x32_bf16 v[144:147], a[216:219], a[32:35], v[144:147]\n" \
"v_mfma_f32_16x16x32_bf16 v[144:147], a[0:3], a[36:39], v[144:147]\n" \
"v_mfma_f32_16x16x32_bf16 v[148:151], a[0:3], a[44:47], v[148:151]\n" \
"v_mfma_f32_16x16x32_bf16 v[148:151], a[216:219], a[40:43], v[148:151]\n" \
"buffer_load_dwordx4 v5, s[80:83], s73 offen offset:1024 lds\n" \
"v_mfma_f32_16x16x32_bf16 v[216:219], a[216:219], a[48:51], v[216:219]\n" \
"v_mfma_f32_16x16x32_bf16 v[216:219], a[0:3], a[52:55], v[216:219]\n" \
"v_mfma_f32_16x16x32_bf16 v[220:223], a[0:3], a[60:63], v[220:223]\n" \
"v_mfma_f32_16x16x32_bf16 v[220:223], a[216:219], a[56:59], v[220:223]\n" \
"v_mfma_f32_16x16x32_bf16 v[152:155], a[4:7], a[32:35], v[152:155]\n" \
"buffer_load_dwordx4 v6, s[80:83], s73 offen offset:2048 lds\n" \
"s_waitcnt lgkmcnt(0)\n" \
"v_mfma_f32_16x16x32_bf16 v[152:155], a[8:11], a[36:39], v[152:155]\n" \
"v_mfma_f32_16x16x32_bf16 v[156:159], a[8:11], a[44:47], v[156:159]\n" \
"v_mfma_f32_16x16x32_bf16 v[156:159], a[4:7], a[40:43], v[156:159]\n" \
"v_mfma_f32_16x16x32_bf16 v[224:227], a[4:7], a[48:51], v[224:227]\n" \
"buffer_load_dwordx4 v7, s[80:83], s73 offen offset:3072 lds\n" \
"s_add_u32 m0, s92, 0x2000\n" \
"v_mfma_f32_16x16x32_bf16 v[224:227], a[8:11], a[52:55], v[224:227]\n" \
"v_readlane_b32 s92, v11, 42\n" \
"v_mfma_f32_16x16x32_bf16 v[228:231], a[8:11], a[60:63], v[228:231]\n" \
"v_mfma_f32_16x16x32_bf16 v[228:231], a[4:7], a[56:59], v[228:231]\n" \
"s_waitcnt vmcnt(23)\n" \
"s_barrier\n" \
"buffer_load_dwordx4 v8, s[80:83], s73 offen offset:0 lds\n" \
"s_mov_b32 m0, s94\n" \
"v_mfma_f32_16x16x32_bf16 v[160:163], a[12:15], a[32:35], v[160:163]\n" \
"ds_read_b64_tr_b16 a[196:197], v10 offset:27648\n" \
"ds_read_b64_tr_b16 a[198:199], v10 offset:32256\n" \
"v_mfma_f32_16x16x32_bf16 v[160:163], a[16:19], a[36:39], v[160:163]\n" \
"ds_read_b64_tr_b16 a[200:201], v10 offset:36864\n" \
"ds_read_b64_tr_b16 a[202:203], v10 offset:41472\n" \
"v_mfma_f32_16x16x32_bf16 v[164:167], a[16:19], a[44:47], v[164:167]\n" \
"ds_read_b64_tr_b16 a[204:205], v10 offset:27680\n" \
"ds_read_b64_tr_b16 a[206:207], v10 offset:32288\n" \
"v_mfma_f32_16x16x32_bf16 v[164:167], a[12:15], a[40:43], v[164:167]\n" \
"buffer_load_dwordx4 v9, s[84:87], 0 offen lds\n" \
"v_mfma_f32_16x16x32_bf16 v[232:235], a[12:15], a[48:51], v[232:235]\n" \
"ds_read_b64_tr_b16 a[208:209], v10 offset:36896\n" \
"ds_read_b64_tr_b16 a[210:211], v10 offset:41504\n" \
"v_mfma_f32_16x16x32_bf16 v[232:235], a[16:19], a[52:55], v[232:235]\n" \
"ds_read_b64_tr_b16 a[212:213], v10 offset:27712\n" \
"ds_read_b64_tr_b16 a[214:215], v10 offset:32320\n" \
"v_mfma_f32_16x16x32_bf16 v[236:239], a[16:19], a[60:63], v[236:239]\n" \
"ds_read_b64_tr_b16 a[216:217], v10 offset:36928\n" \
"ds_read_b64_tr_b16 a[218:219], v10 offset:41536\n" \
"v_mfma_f32_16x16x32_bf16 v[236:239], a[12:15], a[56:59], v[236:239]\n" \
"ds_read_b64_tr_b16 a[0:1], v10 offset:27744\n" \
"ds_read_b64_tr_b16 a[2:3], v10 offset:32352\n" \
"buffer_load_dwordx4 a[128:131], v20, s[56:59], s68 offen offset:0 sc1\n" \
"v_mfma_f32_16x16x32_bf16 v[168:171], a[20:23], a[32:35], v[168:171]\n" \
"ds_read_b64_tr_b16 a[4:5], v10 offset:36960\n" \
"ds_read_b64_tr_b16 a[6:7], v10 offset:41568\n" \
"v_mfma_f32_16x16x32_bf16 v[168:171], a[24:27], a[36:39], v[168:171]\n" \
"ds_read_b64_tr_b16 a[8:9], v10 offset:27776\n" \
"ds_read_b64_tr_b16 a[10:11], v10 offset:32384\n" \
"v_mfma_f32_16x16x32_bf16 v[172:175], a[24:27], a[44:47], v[172:175]\n" \
"ds_read_b64_tr_b16 a[12:13], v10 offset:36992\n" \
"ds_read_b64_tr_b16 a[14:15], v10 offset:41600\n" \
"v_mfma_f32_16x16x32_bf16 v[172:175], a[20:23], a[40:43], v[172:175]\n" \
"ds_read_b64_tr_b16 a[16:17], v10 offset:27808\n" \
"ds_read_b64_tr_b16 a[18:19], v10 offset:32416\n" \
"buffer_load_dwordx4 a[132:135], v20, s[56:59], s68 offen offset:1024 sc1\n" \
"v_mfma_f32_16x16x32_bf16 v[240:243], a[20:23], a[48:51], v[240:243]\n" \
"s_waitcnt vmcnt(12)\n" \
"v_cmp_ne_u32_e32 vcc, s62, v47\n" \
"v_mfma_f32_16x16x32_bf16 v[240:243], a[24:27], a[52:55], v[240:243]\n" \
"v_readlane_b32 s73, v11, 48\n" \
"v_readlane_b32 s94, v11, 44\n" \
"v_mfma_f32_16x16x32_bf16 v[244:247], a[24:27], a[60:63], v[244:247]\n" \
"ds_read_b64_tr_b16 a[24:25], v10 offset:27840\n" \
"ds_read_b64_tr_b16 a[26:27], v10 offset:32448\n" \
"v_mfma_f32_16x16x32_bf16 v[244:247], a[20:23], a[56:59], v[244:247]\n" \
"ds_read_b64_tr_b16 a[20:21], v10 offset:37024\n" \
"ds_read_b64_tr_b16 a[22:23], v10 offset:41632\n" \
"buffer_load_dwordx4 a[136:139], v20, s[56:59], s68 offen offset:2048 sc1\n" \
"v_mfma_f32_16x16x32_bf16 v[176:179], a[28:31], a[32:35], v[176:179]\n" \
"s_cbranch_vccnz L_sb_%=\n" \
"L_rb_%=:\n" \
"v_readlane_b32 s71, v11, 46\n" \
"v_mfma_f32_16x16x32_bf16 v[176:179], a[192:195], a[36:39], v[176:179]\n" \
"v_cmp_eq_u32_e32 vcc, 0, v44\n" \
"v_mfma_f32_16x16x32_bf16 v[180:183], a[192:195], a[44:47], v[180:183]\n" \
"v_mfma_f32_16x16x32_bf16 v[180:183], a[28:31], a[40:43], v[180:183]\n" \
"v_cndmask_b32_e32 v50, 0, v45, vcc\n" \
"v_cmp_eq_u32_e32 vcc, 1, v44\n" \
"v_mfma_f32_16x16x32_bf16 v[248:251], a[28:31], a[48:51], v[248:251]\n" \
"buffer_load_dwordx4 a[140:143], v20, s[56:59], s68 offen offset:3072 sc1\n" \
"v_mfma_f32_16x16x32_bf16 v[248:251], a[192:195], a[52:55], v[248:251]\n" \
"v_cndmask_b32_e32 v51, 0, v45, vcc\n" \
"v_bfe_u32 v45, v20, 1, 5\n" \
"v_mfma_f32_16x16x32_bf16 v[252:255], a[192:195], a[60:63], v[252:255]\n" \
"ds_read_b64_tr_b16 a[192:193], v10 offset:27872\n" \
"ds_read_b64_tr_b16 a[194:195], v10 offset:32480\n" \
"v_mfma_f32_16x16x32_bf16 v[252:255], a[28:31], a[56:59], v[252:255]\n" \
"ds_read_b64_tr_b16 a[28:29], v10 offset:37056\n" \
"s_waitcnt lgkmcnt(9)\n" \
"v_mfma_f32_16x16x32_bf16 v[112:115], a[196:199], a[64:67], v[112:115]\n" \
"ds_read_b64_tr_b16 a[30:31], v10 offset:41664\n" \
"v_and_b32_e32 v45, 24, v45\n" \
"v_mfma_f32_16x16x32_bf16 v[112:115], a[200:203], a[68:71], v[112:115]\n" \
"buffer_load_dwordx4 a[156:159], v32, s[56:59], s68 offen offset:3072 sc1\n" \
"v_mfma_f32_16x16x32_bf16 v[116:119], a[200:203], a[84:87], v[116:119]\n" \
"ds_read_b64_tr_b16 a[200:201], v10 offset:27904\n" \
"ds_read_b64_tr_b16 a[202:203], v10 offset:32512\n" \
"v_mfma_f32_16x16x32_bf16 v[116:119], a[196:199], a[80:83], v[116:119]\n" \
"ds_read_b64_tr_b16 a[196:197], v10 offset:37088\n" \
"ds_read_b64_tr_b16 a[198:199], v10 offset:41696\n" \
"v_mfma_f32_16x16x32_bf16 v[120:123], a[204:207], a[64:67], v[120:123]\n" \
"v_and_b32_e32 v44, 0x3c0, v20\n" \
"v_add_u32_e32 v45, 0x1d400, v45\n" \
"v_mfma_f32_16x16x32_bf16 v[120:123], a[208:211], a[68:71], v[120:123]\n" \
"v_add_u32_e32 v36, v44, v45\n" \
"v_mfma_f32_16x16x32_bf16 v[124:127], a[208:211], a[84:87], v[124:127]\n" \
"buffer_load_dwordx4 a[152:155], v32, s[56:59], s68 offen offset:2048 sc1\n" \
"v_mfma_f32_16x16x32_bf16 v[124:127], a[204:207], a[80:83], v[124:127]\n" \
"ds_read_b64_tr_b16 a[204:205], v10 offset:37120\n" \
"ds_read_b64_tr_b16 a[206:207], v10 offset:41728\n" \
"v_mfma_f32_16x16x32_bf16 v[128:131], a[212:215], a[64:67], v[128:131]\n" \
"ds_read_b64_tr_b16 a[208:209], v10 offset:46080\n" \
"ds_read_b64_tr_b16 a[210:211], v10 offset:50688\n" \
"v_mfma_f32_16x16x32_bf16 v[128:131], a[216:219], a[68:71], v[128:131]\n" \
"v_mfma_f32_16x16x32_bf16 v[132:135], a[216:219], a[84:87], v[132:135]\n" \
"ds_read_b64_tr_b16 a[216:217], v10 offset:46112\n" \
"ds_read_b64_tr_b16 a[218:219], v10 offset:50720\n" \
"buffer_load_dwordx4 a[148:151], v32, s[56:59], s68 offen offset:1024 sc1\n" \
"v_mfma_f32_16x16x32_bf16 v[132:135], a[212:215], a[80:83], v[132:135]\n" \
"ds_read_b64_tr_b16 a[212:213], v10 offset:55296\n" \
"ds_read_b64_tr_b16 a[214:215], v10 offset:59904\n" \
"v_mfma_f32_16x16x32_bf16 v[136:139], a[0:3], a[64:67], v[136:139]\n" \
"v_mfma_f32_16x16x32_bf16 v[136:139], a[4:7], a[68:71], v[136:139]\n" \
"v_mfma_f32_16x16x32_bf16 v[140:143], a[4:7], a[84:87], v[140:143]\n" \
"ds_read_b64_tr_b16 a[4:5], v10 offset:46144\n" \
"ds_read_b64_tr_b16 a[6:7], v10 offset:50752\n" \
"buffer_load_dwordx4 a[144:147], v32, s[56:59], s68 offen offset:0 sc1\n" \
"v_mfma_f32_16x16x32_bf16 v[140:143], a[0:3], a[80:83], v[140:143]\n" \
"ds_read_b64_tr_b16 a[0:1], v10 offset:55328\n" \
"ds_read_b64_tr_b16 a[2:3], v10 offset:59936\n" \
"v_mfma_f32_16x16x32_bf16 v[144:147], a[8:11], a[64:67], v[144:147]\n" \
"v_readlane_b32 s68, v11, 49\n" \
"v_mfma_f32_16x16x32_bf16 v[144:147], a[12:15], a[68:71], v[144:147]\n" \
"v_mfma_f32_16x16x32_bf16 v[148:151], a[12:15], a[84:87], v[148:151]\n" \
"ds_read_b64_tr_b16 a[12:13], v10 offset:46176\n" \
"ds_read_b64_tr_b16 a[14:15], v10 offset:50784\n" \
"buffer_load_dwordx4 a[96:99], v20, s[56:59], s69 offen offset:0 sc1\n" \
"v_mfma_f32_16x16x32_bf16 v[148:151], a[8:11], a[80:83], v[148:151]\n" \
"ds_read_b64_tr_b16 a[8:9], v10 offset:55360\n" \
"s_waitcnt lgkmcnt(15)\n" \
"v_mfma_f32_16x16x32_bf16 v[152:155], a[16:19], a[64:67], v[152:155]\n" \
"ds_read_b64_tr_b16 a[10:11], v10 offset:59968\n" \
"v_mfma_f32_16x16x32_bf16 v[152:155], a[20:23], a[68:71], v[152:155]\n" \
"v_mfma_f32_16x16x32_bf16 v[156:159], a[20:23], a[84:87], v[156:159]\n" \
"ds_read_b64_tr_b16 a[20:21], v10 offset:46208\n" \
"ds_read_b64_tr_b16 a[22:23], v10 offset:50816\n" \
"buffer_load_dwordx4 a[100:103], v20, s[56:59], s69 offen offset:1024 sc1\n" \
"v_mfma_f32_16x16x32_bf16 v[156:159], a[16:19], a[80:83], v[156:159]\n" \
"ds_read_b64_tr_b16 a[16:17], v10 offset:55392\n" \
"ds_read_b64_tr_b16 a[18:19], v10 offset:60000\n" \
"v_mfma_f32_16x16x32_bf16 v[160:163], a[24:27], a[64:67], v[160:163]\n" \
"v_mfma_f32_16x16x32_bf16 v[160:163], a[28:31], a[68:71], v[160:163]\n" \
"v_mfma_f32_16x16x32_bf16 v[164:167], a[28:31], a[84:87], v[164:167]\n" \
"ds_read_b64_tr_b16 a[28:29], v10 offset:46240\n" \
"ds_read_b64_tr_b16 a[30:31], v10 offset:50848\n" \
"buffer_load_dwordx4 a[104:107], v20, s[56:59], s69 offen offset:2048 sc1\n" \
"v_mfma_f32_16x16x32_bf16 v[164:167], a[24:27], a[80:83], v[164:167]\n" \
"ds_read_b64_tr_b16 a[24:25], v10 offset:55424\n" \
"ds_read_b64_tr_b16 a[26:27], v10 offset:60032\n" \
"v_mfma_f32_16x16x32_bf16 v[168:171], a[192:195], a[64:67], v[168:171]\n" \
"v_mfma_f32_16x16x32_bf16 v[168:171], a[196:199], a[68:71], v[168:171]\n" \
"v_mfma_f32_16x16x32_bf16 v[172:175], a[196:199], a[84:87], v[172:175]\n" \
"ds_read_b64_tr_b16 a[196:197], v10 offset:46272\n" \
"ds_read_b64_tr_b16 a[198:199], v10 offset:50880\n" \
"buffer_load_dwordx4 a[108:111], v20, s[56:59], s69 offen offset:3072 sc1\n" \
"v_mfma_f32_16x16x32_bf16 v[172:175], a[192:195], a[80:83], v[172:175]\n" \
"ds_read_b64_tr_b16 a[192:193], v10 offset:55456\n" \
"ds_read_b64_tr_b16 a[194:195], v10 offset:60064\n" \
"v_mfma_f32_16x16x32_bf16 v[176:179], a[200:203], a[64:67], v[176:179]\n" \
"s_waitcnt lgkmcnt(13)\n" \
"v_mfma_f32_16x16x32_bf16 v[176:179], a[204:207], a[68:71], v[176:179]\n" \
"v_mfma_f32_16x16x32_bf16 v[180:183], a[204:207], a[84:87], v[180:183]\n" \
"ds_read_b64_tr_b16 a[204:205], v10 offset:46304\n" \
"ds_read_b64_tr_b16 a[206:207], v10 offset:50912\n" \
"buffer_load_dwordx4 a[124:127], v32, s[56:59], s69 offen offset:3072 sc1\n" \
"v_mfma_f32_16x16x32_bf16 v[180:183], a[200:203], a[80:83], v[180:183]\n" \
"ds_read_b64_tr_b16 a[200:201], v10 offset:55488\n" \
"ds_read_b64_tr_b16 a[202:203], v10 offset:60096\n" \
"v_mfma_f32_16x16x32_bf16 v[112:115], a[208:211], a[72:75], v[112:115]\n" \
"v_mfma_f32_16x16x32_bf16 v[112:115], a[212:215], a[76:79], v[112:115]\n" \
"v_mfma_f32_16x16x32_bf16 v[116:119], a[212:215], a[92:95], v[116:119]\n" \
"ds_read_b64_tr_b16 a[212:213], v10 offset:46336\n" \
"ds_read_b64_tr_b16 a[214:215], v10 offset:50944\n" \
"buffer_load_dwordx4 a[120:123], v32, s[56:59], s69 offen offset:2048 sc1\n" \
"v_mfma_f32_16x16x32_bf16 v[116:119], a[208:211], a[88:91], v[116:119]\n" \
"ds_read_b64_tr_b16 a[208:209], v10 offset:55520\n" \
"ds_read_b64_tr_b16 a[210:211], v10 offset:60128\n" \
"v_mfma_f32_16x16x32_bf16 v[120:123], a[216:219], a[72:75], v[120:123]\n" \
"v_mfma_f32_16x16x32_bf16 v[120:123], a[0:3], a[76:79], v[120:123]\n" \
"v_mfma_f32_16x16x32_bf16 v[124:127], a[0:3], a[92:95], v[124:127]\n" \
"v_mfma_f32_16x16x32_bf16 v[124:127], a[216:219], a[88:91], v[124:127]\n" \
"buffer_load_dwordx4 a[116:119], v32, s[56:59], s69 offen offset:1024 sc1\n" \
"v_mfma_f32_16x16x32_bf16 v[128:131], a[4:7], a[72:75], v[128:131]\n" \
"ds_read_b64_tr_b16 a[216:217], v10 offset:55552\n" \
"s_waitcnt lgkmcnt(9)\n" \
"v_mfma_f32_16x16x32_bf16 v[128:131], a[8:11], a[76:79], v[128:131]\n" \
"ds_read_b64_tr_b16 a[218:219], v10 offset:60160\n" \
"v_mfma_f32_16x16x32_bf16 v[132:135], a[8:11], a[92:95], v[132:135]\n" \
"v_mfma_f32_16x16x32_bf16 v[132:135], a[4:7], a[88:91], v[132:135]\n" \
"v_mfma_f32_16x16x32_bf16 v[136:139], a[12:15], a[72:75], v[136:139]\n" \
"buffer_load_dwordx4 a[112:115], v32, s[56:59], s69 offen offset:0 sc1\n" \
"v_mfma_f32_16x16x32_bf16 v[136:139], a[16:19], a[76:79], v[136:139]\n" \
"v_readlane_b32 s69, v11, 5\n" \
"v_mfma_f32_16x16x32_bf16 v[140:143], a[16:19], a[92:95], v[140:143]\n" \
"v_mfma_f32_16x16x32_bf16 v[140:143], a[12:15], a[88:91], v[140:143]\n" \
"s_waitcnt vmcnt(16)\n" \
"s_barrier\n" \
"buffer_load_dwordx4 a[32:35], v20, s[56:59], s64 offen offset:0 sc1\n" \
"v_mfma_f32_16x16x32_bf16 v[144:147], a[20:23], a[72:75], v[144:147]\n" \
"ds_read_b64_tr_b16 a[0:1], v33 offset:18432\n" \
"ds_read_b64_tr_b16 a[2:3], v33 offset:23040\n" \
"v_mfma_f32_16x16x32_bf16 v[144:147], a[24:27], a[76:79], v[144:147]\n" \
"ds_read_b64_tr_b16 a[4:5], v33 offset:27648\n" \
"ds_read_b64_tr_b16 a[6:7], v33 offset:32256\n" \
"v_mfma_f32_16x16x32_bf16 v[148:151], a[24:27], a[92:95], v[148:151]\n" \
"ds_read_b64_tr_b16 a[8:9], v33 offset:36864\n" \
"ds_read_b64_tr_b16 a[10:11], v33 offset:41472\n" \
"v_mfma_f32_16x16x32_bf16 v[148:151], a[20:23], a[88:91], v[148:151]\n" \
"ds_read_b64_tr_b16 a[12:13], v33 offset:46080\n" \
"ds_read_b64_tr_b16 a[14:15], v33 offset:50688\n" \
"buffer_load_dwordx4 a[36:39], v20, s[56:59], s64 offen offset:1024 sc1\n" \
"v_mfma_f32_16x16x32_bf16 v[152:155], a[28:31], a[72:75], v[152:155]\n" \
"ds_read_b64_tr_b16 a[16:17], v34 offset:0\n" \
"ds_read_b64_tr_b16 a[18:19], v34 offset:4608\n" \
"v_mfma_f32_16x16x32_bf16 v[152:155], a[192:195], a[76:79], v[152:155]\n" \
"ds_read_b64_tr_b16 a[20:21], v34 offset:9216\n" \
"ds_read_b64_tr_b16 a[22:23], v34 offset:13824\n" \
"v_mfma_f32_16x16x32_bf16 v[156:159], a[192:195], a[92:95], v[156:159]\n" \
"ds_read_b64_tr_b16 a[24:25], v34 offset:18432\n" \
"ds_read_b64_tr_b16 a[26:27], v34 offset:23040\n" \
"v_mfma_f32_16x16x32_bf16 v[156:159], a[28:31], a[88:91], v[156:159]\n" \
"ds_read_b64_tr_b16 a[28:29], v33 offset:0\n" \
"ds_read_b64_tr_b16 a[30:31], v33 offset:4608\n" \
"buffer_load_dwordx4 a[40:43], v20, s[56:59], s64 offen offset:2048 sc1\n" \
"v_mfma_f32_16x16x32_bf16 v[160:163], a[196:199], a[72:75], v[160:163]\n" \
"ds_read_b64_tr_b16 a[192:193], v33 offset:18464\n" \
"s_waitcnt lgkmcnt(15)\n" \
"v_mfma_f32_16x16x32_bf16 v[160:163], a[200:203], a[76:79], v[160:163]\n" \
"ds_read_b64_tr_b16 a[194:195], v33 offset:23072\n" \
"v_mfma_f32_16x16x32_bf16 v[164:167], a[200:203], a[92:95], v[164:167]\n" \
"buffer_load_dwordx4 a[44:47], v20, s[56:59], s64 offen offset:3072 sc1\n" \
"v_mfma_f32_16x16x32_bf16 v[164:167], a[196:199], a[88:91], v[164:167]\n" \
"ds_read_b64_tr_b16 a[196:197], v33 offset:27680\n" \
"ds_read_b64_tr_b16 a[198:199], v33 offset:32288\n" \
"v_mfma_f32_16x16x32_bf16 v[168:171], a[204:207], a[72:75], v[168:171]\n" \
"ds_read_b64_tr_b16 a[200:201], v33 offset:36896\n" \
"ds_read_b64_tr_b16 a[202:203], v33 offset:41504\n" \
"v_mfma_f32_16x16x32_bf16 v[168:171], a[208:211], a[76:79], v[168:171]\n" \
"v_mfma_f32_16x16x32_bf16 v[172:175], a[208:211], a[92:95], v[172:175]\n" \
"ds_read_b64_tr_b16 a[208:209], v34 offset:32\n" \
"ds_read_b64_tr_b16 a[210:211], v34 offset:4640\n" \
"buffer_load_dwordx4 a[60:63], v32, s[56:59], s64 offen offset:3072 sc1\n" \
"v_mfma_f32_16x16x32_bf16 v[172:175], a[204:207], a[88:91], v[172:175]\n" \
"ds_read_b64_tr_b16 a[204:205], v33 offset:46112\n" \
"ds_read_b64_tr_b16 a[206:207], v33 offset:50720\n" \
"v_mfma_f32_16x16x32_bf16 v[176:179], a[212:215], a[72:75], v[176:179]\n" \
"v_mfma_f32_16x16x32_bf16 v[176:179], a[216:219], a[76:79], v[176:179]\n" \
"v_mfma_f32_16x16x32_bf16 v[180:183], a[216:219], a[92:95], v[180:183]\n" \
"ds_read_b64_tr_b16 a[216:217], v34 offset:18464\n" \
"ds_read_b64_tr_b16 a[218:219], v34 offset:23072\n" \
"buffer_load_dwordx4 a[56:59], v32, s[56:59], s64 offen offset:2048 sc1\n" \
"v_mfma_f32_16x16x32_bf16 v[180:183], a[212:215], a[88:91], v[180:183]\n" \
"ds_read_b64_tr_b16 a[212:213], v34 offset:9248\n" \
"s_waitcnt vmcnt(21)\n" \
"v_mfma_f32_16x16x32_bf16 v[112:115], a[0:3], a[128:131], v[112:115]\n" \
"ds_read_b64_tr_b16 a[214:215], v34 offset:13856\n" \
"s_waitcnt vmcnt(20) lgkmcnt(13)\n" \
"v_mfma_f32_16x16x32_bf16 v[112:115], a[4:7], a[132:135], v[112:115]\n" \
"s_waitcnt vmcnt(19)\n" \
"v_mfma_f32_16x16x32_bf16 v[112:115], a[8:11], a[136:139], v[112:115]\n" \
"s_waitcnt vmcnt(18)\n" \
"v_mfma_f32_16x16x32_bf16 v[112:115], a[12:15], a[140:143], v[112:115]\n" \
"buffer_load_dwordx4 a[52:55], v32, s[56:59], s64 offen offset:1024 sc1\n" \
"s_waitcnt vmcnt(18)\n" \
"v_mfma_f32_16x16x32_bf16 v[116:119], a[12:15], a[156:159], v[116:119]\n" \
"ds_read_b64_tr_b16 a[12:13], v33 offset:36928\n" \
"s_waitcnt vmcnt(17)\n" \
"v_mfma_f32_16x16x32_bf16 v[116:119], a[8:11], a[152:155], v[116:119]\n" \
"ds_read_b64_tr_b16 a[8:9], v33 offset:27712\n" \
"ds_read_b64_tr_b16 a[10:11], v33 offset:32320\n" \
"ds_read_b64_tr_b16 a[14:15], v33 offset:41536\n" \
"s_waitcnt vmcnt(16)\n" \
"v_mfma_f32_16x16x32_bf16 v[116:119], a[4:7], a[148:151], v[116:119]\n" \
"buffer_load_dwordx4 a[48:51], v32, s[56:59], s64 offen offset:0 sc1\n" \
"ds_read_b64_tr_b16 a[4:5], v33 offset:18496\n" \
"ds_read_b64_tr_b16 a[6:7], v33 offset:23104\n" \
"v_readlane_b32 s64, v11, 38\n" \
"s_waitcnt vmcnt(16)\n" \
"v_mfma_f32_16x16x32_bf16 v[116:119], a[0:3], a[144:147], v[116:119]\n" \
"buffer_load_dwordx4 a[160:163], v20, s[56:59], s72 offen offset:0 sc1\n" \
"ds_read_b64_tr_b16 a[0:1], v33 offset:32\n" \
"ds_read_b64_tr_b16 a[2:3], v33 offset:4640\n" \
"s_waitcnt vmcnt(16)\n" \
"v_mfma_f32_16x16x32_bf16 v[112:115], a[16:19], a[96:99], v[112:115]\n" \
"buffer_load_dwordx4 a[164:167], v20, s[56:59], s72 offen offset:1024 sc1\n" \
"s_waitcnt vmcnt(16)\n" \
"v_mfma_f32_16x16x32_bf16 v[112:115], a[20:23], a[100:103], v[112:115]\n" \
"buffer_load_dwordx4 a[168:171], v20, s[56:59], s72 offen offset:2048 sc1\n" \
"s_waitcnt vmcnt(16)\n" \
"v_mfma_f32_16x16x32_bf16 v[112:115], a[24:27], a[104:107], v[112:115]\n" \
"buffer_load_dwordx4 a[172:175], v20, s[56:59], s72 offen offset:3072 sc1\n" \
"s_waitcnt vmcnt(16)\n" \
"v_mfma_f32_16x16x32_bf16 v[112:115], a[28:31], a[108:111], v[112:115]\n" \
"buffer_load_dwordx4 a[188:191], v32, s[56:59], s72 offen offset:3072 sc1\n" \
"s_waitcnt vmcnt(16)\n" \
"v_mfma_f32_16x16x32_bf16 v[116:119], a[28:31], a[124:127], v[116:119]\n" \
"buffer_load_dwordx4 a[184:187], v32, s[56:59], s72 offen offset:2048 sc1\n" \
"ds_read_b64_tr_b16 a[28:29], v34 offset:18496\n" \
"ds_read_b64_tr_b16 a[30:31], v34 offset:23104\n" \
"s_waitcnt vmcnt(16)\n" \
"v_mfma_f32_16x16x32_bf16 v[116:119], a[24:27], a[120:123], v[116:119]\n" \
"v_cvt_pk_bf16_f32 v112, v112, v113\n" \
"v_cvt_pk_bf16_f32 v113, v114, v115\n" \
"s_waitcnt vmcnt(15)\n" \
"v_mfma_f32_16x16x32_bf16 v[116:119], a[20:23], a[116:119], v[116:119]\n" \
"buffer_load_dwordx4 a[180:183], v32, s[56:59], s72 offen offset:1024 sc1\n" \
"ds_read_b64_tr_b16 a[20:21], v34 offset:64\n" \
"ds_read_b64_tr_b16 a[22:23], v34 offset:4672\n" \
"ds_read_b64_tr_b16 a[24:25], v34 offset:9280\n" \
"ds_read_b64_tr_b16 a[26:27], v34 offset:13888\n" \
"s_waitcnt vmcnt(15)\n" \
"v_mfma_f32_16x16x32_bf16 v[116:119], a[16:19], a[112:115], v[116:119]\n" \
"ds_read_b64_tr_b16 a[16:17], v33 offset:46144\n" \
"buffer_load_dwordx4 a[176:179], v32, s[56:59], s72 offen offset:0 sc1\n" \
"s_waitcnt lgkmcnt(5)\n" \
"v_mfma_f32_16x16x32_bf16 v[120:123], a[192:195], a[128:131], v[120:123]\n" \
"ds_read_b64_tr_b16 a[18:19], v33 offset:50752\n" \
"v_readlane_b32 s72, v11, 47\n" \
"v_mfma_f32_16x16x32_bf16 v[120:123], a[196:199], a[132:135], v[120:123]\n" \
"v_mfma_f32_16x16x32_bf16 v[120:123], a[200:203], a[136:139], v[120:123]\n" \
"v_cvt_pk_bf16_f32 v114, v116, v117\n" \
"v_cvt_pk_bf16_f32 v115, v118, v119\n" \
"v_mfma_f32_16x16x32_bf16 v[120:123], a[204:207], a[140:143], v[120:123]\n" \
"buffer_store_dwordx4 v[112:115], v20, s[56:59], s65 offen offset:0 sc1\n" \
"s_add_u32 s65, s65, 0x3800\n" \
"v_mfma_f32_16x16x32_bf16 v[124:127], a[204:207], a[156:159], v[124:127]\n" \
"ds_read_b64_tr_b16 a[204:205], v10 offset:46336\n" \
"ds_read_b64_tr_b16 a[206:207], v10 offset:50944\n" \
"v_mfma_f32_16x16x32_bf16 v[124:127], a[200:203], a[152:155], v[124:127]\n" \
"ds_read_b64_tr_b16 a[200:201], v10 offset:37120\n" \
"ds_read_b64_tr_b16 a[202:203], v10 offset:41728\n" \
"v_mfma_f32_16x16x32_bf16 v[124:127], a[196:199], a[148:151], v[124:127]\n" \
"ds_read_b64_tr_b16 a[196:197], v10 offset:27904\n" \
"ds_read_b64_tr_b16 a[198:199], v10 offset:32512\n" \
"v_mfma_f32_16x16x32_bf16 v[124:127], a[192:195], a[144:147], v[124:127]\n" \
"buffer_load_dwordx4 a[220:223], v20, s[56:59], s93 offen offset:0 sc1\n" \
"v_mfma_f32_16x16x32_bf16 v[120:123], a[208:211], a[96:99], v[120:123]\n" \
"ds_read_b64_tr_b16 a[192:193], v33 offset:64\n" \
"ds_read_b64_tr_b16 a[194:195], v33 offset:4672\n" \
"v_mfma_f32_16x16x32_bf16 v[120:123], a[212:215], a[100:103], v[120:123]\n" \
"v_mfma_f32_16x16x32_bf16 v[120:123], a[216:219], a[104:107], v[120:123]\n" \
"v_mfma_f32_16x16x32_bf16 v[120:123], a[0:3], a[108:111], v[120:123]\n" \
"v_mfma_f32_16x16x32_bf16 v[124:127], a[0:3], a[124:127], v[124:127]\n" \
"buffer_load_dwordx4 a[224:227], v20, s[56:59], s93 offen offset:1024 sc1\n" \
"v_mfma_f32_16x16x32_bf16 v[124:127], a[216:219], a[120:123], v[124:127]\n" \
"ds_read_b64_tr_b16 a[216:217], v33 offset:27904\n" \
"ds_read_b64_tr_b16 a[218:219], v33 offset:32512\n" \
"v_mfma_f32_16x16x32_bf16 v[124:127], a[212:215], a[116:119], v[124:127]\n" \
"ds_read_b64_tr_b16 a[212:213], v33 offset:18688\n" \
"ds_read_b64_tr_b16 a[214:215], v33 offset:23296\n" \
"v_cvt_pk_bf16_f32 v120, v120, v121\n" \
"v_cvt_pk_bf16_f32 v121, v122, v123\n" \
"v_mfma_f32_16x16x32_bf16 v[124:127], a[208:211], a[112:115], v[124:127]\n" \
"ds_read_b64_tr_b16 a[208:209], v10 offset:55552\n" \
"ds_read_b64_tr_b16 a[210:211], v10 offset:60160\n" \
"v_mfma_f32_16x16x32_bf16 v[128:131], a[4:7], a[128:131], v[128:131]\n" \
"buffer_load_dwordx4 a[228:231], v20, s[56:59], s93 offen offset:2048 sc1\n" \
"v_mfma_f32_16x16x32_bf16 v[128:131], a[8:11], a[132:135], v[128:131]\n" \
"ds_read_b64_tr_b16 a[0:1], v33 offset:37120\n" \
"ds_read_b64_tr_b16 a[2:3], v33 offset:41728\n" \
"v_mfma_f32_16x16x32_bf16 v[128:131], a[12:15], a[136:139], v[128:131]\n" \
"v_cvt_pk_bf16_f32 v122, v124, v125\n" \
"v_cvt_pk_bf16_f32 v123, v126, v127\n" \
"s_waitcnt lgkmcnt(13)\n" \
"v_mfma_f32_16x16x32_bf16 v[128:131], a[16:19], a[140:143], v[128:131]\n" \
"buffer_store_dwordx4 v[120:123], v20, s[56:59], s66 offen offset:0 sc1\n" \
"s_add_u32 s66, s66, 0x3000\n" \
"v_mfma_f32_16x16x32_bf16 v[132:135], a[16:19], a[156:159], v[132:135]\n" \
"ds_read_b64_tr_b16 a[16:17], v34 offset:18688\n" \
"ds_read_b64_tr_b16 a[18:19], v34 offset:23296\n" \
"v_mfma_f32_16x16x32_bf16 v[132:135], a[12:15], a[152:155], v[132:135]\n" \
"ds_read_b64_tr_b16 a[12:13], v34 offset:9472\n" \
"ds_read_b64_tr_b16 a[14:15], v34 offset:14080\n" \
"v_mfma_f32_16x16x32_bf16 v[132:135], a[8:11], a[148:151], v[132:135]\n" \
"ds_read_b64_tr_b16 a[8:9], v34 offset:256\n" \
"ds_read_b64_tr_b16 a[10:11], v34 offset:4864\n" \
"v_mfma_f32_16x16x32_bf16 v[132:135], a[4:7], a[144:147], v[132:135]\n" \
"buffer_load_dwordx4 a[232:235], v20, s[56:59], s93 offen offset:3072 sc1\n" \
"v_mfma_f32_16x16x32_bf16 v[128:131], a[20:23], a[96:99], v[128:131]\n" \
"ds_read_b64_tr_b16 a[4:5], v33 offset:46336\n" \
"ds_read_b64_tr_b16 a[6:7], v33 offset:50944\n" \
"v_mfma_f32_16x16x32_bf16 v[128:131], a[24:27], a[100:103], v[128:131]\n" \
"v_mfma_f32_16x16x32_bf16 v[128:131], a[28:31], a[104:107], v[128:131]\n" \
"s_waitcnt lgkmcnt(15)\n" \
"v_mfma_f32_16x16x32_bf16 v[128:131], a[192:195], a[108:111], v[128:131]\n" \
"v_mfma_f32_16x16x32_bf16 v[132:135], a[192:195], a[124:127], v[132:135]\n" \
"buffer_load_dwordx4 a[248:251], v32, s[56:59], s93 offen offset:3072 sc1\n" \
"v_mfma_f32_16x16x32_bf16 v[132:135], a[28:31], a[120:123], v[132:135]\n" \
"ds_read_b64_tr_b16 a[28:29], v10 offset:36960\n" \
"ds_read_b64_tr_b16 a[30:31], v10 offset:41568\n" \
"v_mfma_f32_16x16x32_bf16 v[132:135], a[24:27], a[116:119], v[132:135]\n" \
"ds_read_b64_tr_b16 a[24:25], v10 offset:27744\n" \
"ds_read_b64_tr_b16 a[26:27], v10 offset:32352\n" \
"v_cvt_pk_bf16_f32 v128, v128, v129\n" \
"v_cvt_pk_bf16_f32 v129, v130, v131\n" \
"v_mfma_f32_16x16x32_bf16 v[132:135], a[20:23], a[112:115], v[132:135]\n" \
"ds_read_b64_tr_b16 a[20:21], v33 offset:256\n" \
"s_waitcnt vmcnt(22)\n" \
"v_mfma_f32_16x16x32_bf16 v[248:251], a[196:199], a[32:35], v[248:251]\n" \
"ds_read_b64_tr_b16 a[22:23], v33 offset:4864\n" \
"buffer_load_dwordx4 a[244:247], v32, s[56:59], s93 offen offset:2048 sc1\n" \
"s_waitcnt vmcnt(22)\n" \
"v_mfma_f32_16x16x32_bf16 v[248:251], a[200:203], a[36:39], v[248:251]\n" \
"L_pf21:\n" \
"s_cbranch_execz L_retB\n" \
"s_nop 0\n" \
"ds_read_b64_tr_b16 a[192:193], v10 offset:46176\n" \
"v_cvt_pk_bf16_f32 v130, v132, v133\n" \
"v_cvt_pk_bf16_f32 v131, v134, v135\n" \
"s_waitcnt vmcnt(21)\n" \
"v_mfma_f32_16x16x32_bf16 v[248:251], a[204:207], a[40:43], v[248:251]\n" \
"ds_read_b64_tr_b16 a[194:195], v10 offset:50784\n" \
"buffer_store_dwordx4 v[128:131], v20, s[56:59], s67 offen offset:0 sc1\n" \
"s_add_u32 s67, s67, 0x2800\n" \
"s_waitcnt vmcnt(21) lgkmcnt(10)\n" \
"v_mfma_f32_16x16x32_bf16 v[248:251], a[208:211], a[44:47], v[248:251]\n" \
"s_waitcnt vmcnt(20)\n" \
"v_mfma_f32_16x16x32_bf16 v[252:255], a[208:211], a[60:63], v[252:255]\n" \
"ds_read_b64_tr_b16 a[208:209], v33 offset:36960\n" \
"ds_read_b64_tr_b16 a[210:211], v33 offset:41568\n" \
"s_waitcnt vmcnt(19)\n" \
"v_mfma_f32_16x16x32_bf16 v[252:255], a[204:207], a[56:59], v[252:255]\n" \
"buffer_load_dwordx4 a[240:243], v32, s[56:59], s93 offen offset:1024 sc1\n" \
"ds_read_b64_tr_b16 a[204:205], v33 offset:27744\n" \
"ds_read_b64_tr_b16 a[206:207], v33 offset:32352\n" \
"s_waitcnt vmcnt(19)\n" \
"v_mfma_f32_16x16x32_bf16 v[252:255], a[200:203], a[52:55], v[252:255]\n" \
"buffer_load_dwordx4 a[236:239], v32, s[56:59], s93 offen offset:0 sc1\n" \
"ds_read_b64_tr_b16 a[200:201], v33 offset:18528\n" \
"ds_read_b64_tr_b16 a[202:203], v33 offset:23136\n" \
"v_readlane_b32 s93, v11, 43\n" \
"s_waitcnt vmcnt(19)\n" \
"v_mfma_f32_16x16x32_bf16 v[252:255], a[196:199], a[48:51], v[252:255]\n" \
"ds_read_b64_tr_b16 a[196:197], v10 offset:55392\n" \
"ds_read_b64_tr_b16 a[198:199], v10 offset:60000\n" \
"v_mfma_f32_16x16x32_bf16 v[176:179], a[212:215], a[128:131], v[176:179]\n" \
"buffer_load_dwordx4 v[64:67], v20, s[56:59], s95 offen offset:0 sc1\n" \
"v_mfma_f32_16x16x32_bf16 v[176:179], a[216:219], a[132:135], v[176:179]\n" \
"v_mfma_f32_16x16x32_bf16 v[176:179], a[0:3], a[136:139], v[176:179]\n" \
"s_waitcnt lgkmcnt(6)\n" \
"v_mfma_f32_16x16x32_bf16 v[176:179], a[4:7], a[140:143], v[176:179]\n" \
"v_mfma_f32_16x16x32_bf16 v[180:183], a[4:7], a[156:159], v[180:183]\n" \
"v_mfma_f32_16x16x32_bf16 v[180:183], a[0:3], a[152:155], v[180:183]\n" \
"buffer_load_dwordx4 v[72:75], v32, s[56:59], s95 offen offset:0 sc1\n" \
"v_mfma_f32_16x16x32_bf16 v[180:183], a[216:219], a[148:151], v[180:183]\n" \
"v_mfma_f32_16x16x32_bf16 v[180:183], a[212:215], a[144:147], v[180:183]\n" \
"s_waitcnt vmcnt(20)\n" \
"v_mfma_f32_16x16x32_bf16 v[248:251], a[212:215], a[160:163], v[248:251]\n" \
"s_waitcnt vmcnt(19)\n" \
"v_mfma_f32_16x16x32_bf16 v[248:251], a[216:219], a[164:167], v[248:251]\n" \
"s_waitcnt vmcnt(18)\n" \
"v_mfma_f32_16x16x32_bf16 v[248:251], a[0:3], a[168:171], v[248:251]\n" \
"buffer_load_dwordx4 v[80:83], v20, s[56:59], s96 offen offset:0 sc1\n" \
"s_waitcnt vmcnt(18)\n" \
"v_mfma_f32_16x16x32_bf16 v[248:251], a[4:7], a[172:175], v[248:251]\n" \
"buffer_load_dwordx4 v[88:91], v32, s[56:59], s96 offen offset:0 sc1\n" \
"s_waitcnt vmcnt(18)\n" \
"v_mfma_f32_16x16x32_bf16 v[252:255], a[4:7], a[188:191], v[252:255]\n" \
"ds_read_b64_tr_b16 a[4:5], v34 offset:18528\n" \
"ds_read_b64_tr_b16 a[6:7], v34 offset:23136\n" \
"buffer_load_dwordx4 v[68:71], v20, s[56:59], s95 offen offset:1024 sc1\n" \
"s_waitcnt vmcnt(18)\n" \
"v_mfma_f32_16x16x32_bf16 v[252:255], a[0:3], a[184:187], v[252:255]\n" \
"ds_read_b64_tr_b16 a[0:1], v34 offset:9312\n" \
"ds_read_b64_tr_b16 a[2:3], v34 offset:13920\n" \
"buffer_load_dwordx4 v[76:79], v32, s[56:59], s95 offen offset:1024 sc1\n" \
"s_add_u32 s95, s63, 0x0\n" \
"s_waitcnt vmcnt(18)\n" \
"v_mfma_f32_16x16x32_bf16 v[252:255], a[216:219], a[180:183], v[252:255]\n" \
"ds_read_b64_tr_b16 a[216:217], v34 offset:96\n" \
"ds_read_b64_tr_b16 a[218:219], v34 offset:4704\n" \
"buffer_load_dwordx4 v[84:87], v20, s[56:59], s96 offen offset:1024 sc1\n" \
"s_waitcnt vmcnt(18)\n" \
"v_mfma_f32_16x16x32_bf16 v[252:255], a[212:215], a[176:179], v[252:255]\n" \
"ds_read_b64_tr_b16 a[212:213], v33 offset:46176\n" \
"ds_read_b64_tr_b16 a[214:215], v33 offset:50784\n" \
"v_mfma_f32_16x16x32_bf16 v[176:179], a[8:11], a[96:99], v[176:179]\n" \
"buffer_load_dwordx4 v[92:95], v32, s[56:59], s96 offen offset:1024 sc1\n" \
"v_mfma_f32_16x16x32_bf16 v[176:179], a[12:15], a[100:103], v[176:179]\n" \
"s_add_u32 s96, s63, 0x800\n" \
"v_mfma_f32_16x16x32_bf16 v[176:179], a[16:19], a[104:107], v[176:179]\n" \
"v_mfma_f32_16x16x32_bf16 v[176:179], a[20:23], a[108:111], v[176:179]\n" \
"s_waitcnt vmcnt(18)\n" \
"s_mov_b64 exec, 1\n" \
"buffer_store_dword v39, off, s[56:59], s65 offset:0 sc1\n" \
"s_mov_b64 exec, -1\n" \
"v_mfma_f32_16x16x32_bf16 v[180:183], a[20:23], a[124:127], v[180:183]\n" \
"s_add_u32 s65, s63, 0x1800\n" \
"v_mfma_f32_16x16x32_bf16 v[180:183], a[16:19], a[120:123], v[180:183]\n" \
"v_mfma_f32_16x16x32_bf16 v[180:183], a[12:15], a[116:119], v[180:183]\n" \
"v_cvt_pk_bf16_f32 v176, v176, v177\n" \
"v_cvt_pk_bf16_f32 v177, v178, v179\n" \
"v_mfma_f32_16x16x32_bf16 v[180:183], a[8:11], a[112:115], v[180:183]\n" \
"s_waitcnt vmcnt(18)\n" \
"v_mfma_f32_16x16x32_bf16 v[248:251], a[8:11], a[220:223], v[248:251]\n" \
"buffer_load_dwordx4 v[96:99], v20, s[56:59], s97 offen offset:0 sc1\n" \
"s_waitcnt vmcnt(18)\n" \
"v_mfma_f32_16x16x32_bf16 v[248:251], a[12:15], a[224:227], v[248:251]\n" \
"s_waitcnt vmcnt(17)\n" \
"v_mfma_f32_16x16x32_bf16 v[248:251], a[16:19], a[228:231], v[248:251]\n" \
"buffer_load_dwordx4 v[104:107], v32, s[56:59], s97 offen offset:0 sc1\n" \
"v_cvt_pk_bf16_f32 v178, v180, v181\n" \
"v_cvt_pk_bf16_f32 v179, v182, v183\n" \
"s_waitcnt vmcnt(16)\n" \
"v_mfma_f32_16x16x32_bf16 v[248:251], a[20:23], a[232:235], v[248:251]\n" \
"buffer_store_dwordx4 v[176:179], v20, s[56:59], s68 offen offset:0 sc1\n" \
"s_mov_b64 exec, 1\n" \
"buffer_store_dword v39, off, s[56:59], s66 offset:4 sc1\n" \
"s_mov_b64 exec, -1\n" \
"s_add_u32 s66, s63, 0x2000\n" \
"s_waitcnt vmcnt(17)\n" \
"v_mfma_f32_16x16x32_bf16 v[252:255], a[20:23], a[248:251], v[252:255]\n" \
"ds_read_b64_tr_b16 a[20:21], v10 offset:46208\n" \
"v_cvt_pk_bf16_f32 v248, v248, v249\n" \
"v_cvt_pk_bf16_f32 v249, v250, v251\n" \
"s_waitcnt vmcnt(16)\n" \
"v_mfma_f32_16x16x32_bf16 v[252:255], a[16:19], a[244:247], v[252:255]\n" \
"ds_read_b64_tr_b16 a[16:17], v10 offset:36992\n" \
"ds_read_b64_tr_b16 a[18:19], v10 offset:41600\n" \
"ds_read_b64_tr_b16 a[22:23], v10 offset:50816\n" \
"buffer_load_dwordx4 v[100:103], v20, s[56:59], s97 offen offset:1024 sc1\n" \
"s_waitcnt vmcnt(15)\n" \
"v_mfma_f32_16x16x32_bf16 v[252:255], a[12:15], a[240:243], v[252:255]\n" \
"s_mov_b64 exec, 1\n" \
"buffer_store_dword v39, off, s[56:59], s67 offset:8 sc1\n" \
"s_mov_b64 exec, -1\n" \
"ds_read_b64_tr_b16 a[12:13], v10 offset:27776\n" \
"ds_read_b64_tr_b16 a[14:15], v10 offset:32384\n" \
"s_add_u32 s67, s63, 0x2800\n" \
"s_waitcnt vmcnt(15)\n" \
"v_mfma_f32_16x16x32_bf16 v[252:255], a[8:11], a[236:239], v[252:255]\n" \
"ds_read_b64_tr_b16 a[8:9], v33 offset:96\n" \
"ds_read_b64_tr_b16 a[10:11], v33 offset:4704\n" \
"v_mfma_f32_16x16x32_bf16 v[208:211], a[24:27], a[32:35], v[208:211]\n" \
"buffer_load_dwordx4 v[108:111], v32, s[56:59], s97 offen offset:1024 sc1\n" \
"v_mfma_f32_16x16x32_bf16 v[208:211], a[28:31], a[36:39], v[208:211]\n" \
"s_add_u32 s97, s63, 0x1000\n" \
"v_mfma_f32_16x16x32_bf16 v[208:211], a[192:195], a[40:43], v[208:211]\n" \
"s_waitcnt lgkmcnt(4)\n" \
"v_mfma_f32_16x16x32_bf16 v[208:211], a[196:199], a[44:47], v[208:211]\n" \
"v_cvt_pk_bf16_f32 v250, v252, v253\n" \
"v_cvt_pk_bf16_f32 v251, v254, v255\n" \
"v_mfma_f32_16x16x32_bf16 v[212:215], a[196:199], a[60:63], v[212:215]\n" \
"buffer_store_dwordx4 v[248:251], v20, s[56:59], s68 offen offset:1024 sc1\n" \
"v_mfma_f32_16x16x32_bf16 v[212:215], a[192:195], a[56:59], v[212:215]\n" \
"ds_read_b64_tr_b16 a[192:193], v33 offset:27776\n" \
"ds_read_b64_tr_b16 a[194:195], v33 offset:32384\n" \
"v_mfma_f32_16x16x32_bf16 v[212:215], a[28:31], a[52:55], v[212:215]\n" \
"ds_read_b64_tr_b16 a[28:29], v33 offset:18560\n" \
"ds_read_b64_tr_b16 a[30:31], v33 offset:23168\n" \
"v_mfma_f32_16x16x32_bf16 v[212:215], a[24:27], a[48:51], v[212:215]\n" \
"ds_read_b64_tr_b16 a[24:25], v10 offset:55424\n" \
"ds_read_b64_tr_b16 a[26:27], v10 offset:60032\n" \
"v_mfma_f32_16x16x32_bf16 v[136:139], a[200:203], a[128:131], v[136:139]\n" \
"ds_read_b64_tr_b16 a[196:197], v33 offset:36992\n" \
"ds_read_b64_tr_b16 a[198:199], v33 offset:41600\n" \
"v_mfma_f32_16x16x32_bf16 v[136:139], a[204:207], a[132:135], v[136:139]\n" \
"s_add_u32 s68, s63, 0x3000\n" \
"v_mfma_f32_16x16x32_bf16 v[136:139], a[208:211], a[136:139], v[136:139]\n" \
"v_mfma_f32_16x16x32_bf16 v[136:139], a[212:215], a[140:143], v[136:139]\n" \
"v_mfma_f32_16x16x32_bf16 v[140:143], a[212:215], a[156:159], v[140:143]\n" \
"v_mfma_f32_16x16x32_bf16 v[140:143], a[208:211], a[152:155], v[140:143]\n" \
"v_mfma_f32_16x16x32_bf16 v[140:143], a[204:207], a[148:151], v[140:143]\n" \
"v_mfma_f32_16x16x32_bf16 v[140:143], a[200:203], a[144:147], v[140:143]\n" \
"v_mfma_f32_16x16x32_bf16 v[208:211], a[200:203], a[160:163], v[208:211]\n" \
"v_mfma_f32_16x16x32_bf16 v[208:211], a[204:207], a[164:167], v[208:211]\n" \
"v_mfma_f32_16x16x32_bf16 v[208:211], a[208:211], a[168:171], v[208:211]\n" \
"v_mfma_f32_16x16x32_bf16 v[208:211], a[212:215], a[172:175], v[208:211]\n" \
"v_mfma_f32_16x16x32_bf16 v[212:215], a[212:215], a[188:191], v[212:215]\n" \
"ds_read_b64_tr_b16 a[212:213], v34 offset:18560\n" \
"ds_read_b64_tr_b16 a[214:215], v34 offset:23168\n" \
"v_mfma_f32_16x16x32_bf16 v[212:215], a[208:211], a[184:187], v[212:215]\n" \
"ds_read_b64_tr_b16 a[208:209], v34 offset:9344\n" \
"ds_read_b64_tr_b16 a[210:211], v34 offset:13952\n" \
"v_mfma_f32_16x16x32_bf16 v[212:215], a[204:207], a[180:183], v[212:215]\n" \
"ds_read_b64_tr_b16 a[204:205], v34 offset:128\n" \
"ds_read_b64_tr_b16 a[206:207], v34 offset:4736\n" \
"v_mfma_f32_16x16x32_bf16 v[212:215], a[200:203], a[176:179], v[212:215]\n" \
"ds_read_b64_tr_b16 a[200:201], v33 offset:46208\n" \
"ds_read_b64_tr_b16 a[202:203], v33 offset:50816\n" \
"v_mfma_f32_16x16x32_bf16 v[136:139], a[216:219], a[96:99], v[136:139]\n" \
"v_mfma_f32_16x16x32_bf16 v[136:139], a[0:3], a[100:103], v[136:139]\n" \
"v_mfma_f32_16x16x32_bf16 v[136:139], a[4:7], a[104:107], v[136:139]\n" \
"s_waitcnt lgkmcnt(8)\n" \
"v_mfma_f32_16x16x32_bf16 v[136:139], a[8:11], a[108:111], v[136:139]\n" \
"v_mfma_f32_16x16x32_bf16 v[140:143], a[8:11], a[124:127], v[140:143]\n" \
"v_mfma_f32_16x16x32_bf16 v[140:143], a[4:7], a[120:123], v[140:143]\n" \
"v_mfma_f32_16x16x32_bf16 v[140:143], a[0:3], a[116:119], v[140:143]\n" \
"v_mfma_f32_16x16x32_bf16 v[140:143], a[216:219], a[112:115], v[140:143]\n" \
"v_mfma_f32_16x16x32_bf16 v[208:211], a[216:219], a[220:223], v[208:211]\n" \
"v_mfma_f32_16x16x32_bf16 v[208:211], a[0:3], a[224:227], v[208:211]\n" \
"v_mfma_f32_16x16x32_bf16 v[208:211], a[4:7], a[228:231], v[208:211]\n" \
"v_mfma_f32_16x16x32_bf16 v[208:211], a[8:11], a[232:235], v[208:211]\n" \
"v_cvt_pk_bf16_f32 v136, v136, v137\n" \
"v_cvt_pk_bf16_f32 v137, v138, v139\n" \
"v_mfma_f32_16x16x32_bf16 v[212:215], a[8:11], a[248:251], v[212:215]\n" \
"ds_read_b64_tr_b16 a[8:9], v10 offset:46240\n" \
"v_cvt_pk_bf16_f32 v138, v140, v141\n" \
"v_cvt_pk_bf16_f32 v139, v142, v143\n" \
"v_mfma_f32_16x16x32_bf16 v[212:215], a[4:7], a[244:247], v[212:215]\n" \
"buffer_store_dwordx4 v[136:139], v20, s[56:59], s70 offen offset:0 sc1\n" \
"v_cvt_pk_bf16_f32 v208, v208, v209\n" \
"v_cvt_pk_bf16_f32 v209, v210, v211\n" \
"v_mfma_f32_16x16x32_bf16 v[212:215], a[0:3], a[240:243], v[212:215]\n" \
"ds_read_b64_tr_b16 a[0:1], v10 offset:27808\n" \
"ds_read_b64_tr_b16 a[2:3], v10 offset:32416\n" \
"v_mfma_f32_16x16x32_bf16 v[212:215], a[216:219], a[236:239], v[212:215]\n" \
"ds_read_b64_tr_b16 a[216:217], v33 offset:128\n" \
"ds_read_b64_tr_b16 a[218:219], v33 offset:4736\n" \
"v_mfma_f32_16x16x32_bf16 v[216:219], a[12:15], a[32:35], v[216:219]\n" \
"ds_read_b64_tr_b16 a[4:5], v10 offset:37024\n" \
"ds_read_b64_tr_b16 a[6:7], v10 offset:41632\n" \
"v_mfma_f32_16x16x32_bf16 v[216:219], a[16:19], a[36:39], v[216:219]\n" \
"ds_read_b64_tr_b16 a[10:11], v10 offset:50848\n" \
"v_mfma_f32_16x16x32_bf16 v[216:219], a[20:23], a[40:43], v[216:219]\n" \
"v_cvt_pk_bf16_f32 v210, v212, v213\n" \
"v_cvt_pk_bf16_f32 v211, v214, v215\n" \
"v_mfma_f32_16x16x32_bf16 v[216:219], a[24:27], a[44:47], v[216:219]\n" \
"buffer_store_dwordx4 v[208:211], v20, s[56:59], s70 offen offset:1024 sc1\n" \
"s_add_u32 s70, s70, 0x2000\n" \
"v_mfma_f32_16x16x32_bf16 v[220:223], a[24:27], a[60:63], v[220:223]\n" \
"ds_read_b64_tr_b16 a[24:25], v33 offset:37024\n" \
"ds_read_b64_tr_b16 a[26:27], v33 offset:41632\n" \
"v_mfma_f32_16x16x32_bf16 v[220:223], a[20:23], a[56:59], v[220:223]\n" \
"ds_read_b64_tr_b16 a[20:21], v33 offset:27808\n" \
"ds_read_b64_tr_b16 a[22:23], v33 offset:32416\n" \
"v_mfma_f32_16x16x32_bf16 v[220:223], a[16:19], a[52:55], v[220:223]\n" \
"ds_read_b64_tr_b16 a[16:17], v33 offset:18592\n" \
"ds_read_b64_tr_b16 a[18:19], v33 offset:23200\n" \
"v_mfma_f32_16x16x32_bf16 v[220:223], a[12:15], a[48:51], v[220:223]\n" \
"ds_read_b64_tr_b16 a[12:13], v10 offset:55456\n" \
"ds_read_b64_tr_b16 a[14:15], v10 offset:60064\n" \
"v_mfma_f32_16x16x32_bf16 v[144:147], a[28:31], a[128:131], v[144:147]\n" \
"v_mfma_f32_16x16x32_bf16 v[144:147], a[192:195], a[132:135], v[144:147]\n" \
"v_mfma_f32_16x16x32_bf16 v[144:147], a[196:199], a[136:139], v[144:147]\n" \
"s_waitcnt lgkmcnt(15)\n" \
"v_mfma_f32_16x16x32_bf16 v[144:147], a[200:203], a[140:143], v[144:147]\n" \
"v_mfma_f32_16x16x32_bf16 v[148:151], a[200:203], a[156:159], v[148:151]\n" \
"v_mfma_f32_16x16x32_bf16 v[148:151], a[196:199], a[152:155], v[148:151]\n" \
"v_mfma_f32_16x16x32_bf16 v[148:151], a[192:195], a[148:151], v[148:151]\n" \
"v_mfma_f32_16x16x32_bf16 v[148:151], a[28:31], a[144:147], v[148:151]\n" \
"v_mfma_f32_16x16x32_bf16 v[216:219], a[28:31], a[160:163], v[216:219]\n" \
"v_mfma_f32_16x16x32_bf16 v[216:219], a[192:195], a[164:167], v[216:219]\n" \
"v_mfma_f32_16x16x32_bf16 v[216:219], a[196:199], a[168:171], v[216:219]\n" \
"v_mfma_f32_16x16x32_bf16 v[216:219], a[200:203], a[172:175], v[216:219]\n" \
"v_mfma_f32_16x16x32_bf16 v[220:223], a[200:203], a[188:191], v[220:223]\n" \
"ds_read_b64_tr_b16 a[200:201], v34 offset:18592\n" \
"ds_read_b64_tr_b16 a[202:203], v34 offset:23200\n" \
"v_mfma_f32_16x16x32_bf16 v[220:223], a[196:199], a[184:187], v[220:223]\n" \
"ds_read_b64_tr_b16 a[196:197], v34 offset:9376\n" \
"ds_read_b64_tr_b16 a[198:199], v34 offset:13984\n" \
"v_mfma_f32_16x16x32_bf16 v[220:223], a[192:195], a[180:183], v[220:223]\n" \
"ds_read_b64_tr_b16 a[192:193], v34 offset:160\n" \
"ds_read_b64_tr_b16 a[194:195], v34 offset:4768\n" \
"v_mfma_f32_16x16x32_bf16 v[220:223], a[28:31], a[176:179], v[220:223]\n" \
"ds_read_b64_tr_b16 a[28:29], v33 offset:46240\n" \
"ds_read_b64_tr_b16 a[30:31], v33 offset:50848\n" \
"v_mfma_f32_16x16x32_bf16 v[144:147], a[204:207], a[96:99], v[144:147]\n" \
"v_mfma_f32_16x16x32_bf16 v[144:147], a[208:211], a[100:103], v[144:147]\n" \
"v_mfma_f32_16x16x32_bf16 v[144:147], a[212:215], a[104:107], v[144:147]\n" \
"s_waitcnt lgkmcnt(8)\n" \
"v_mfma_f32_16x16x32_bf16 v[144:147], a[216:219], a[108:111], v[144:147]\n" \
"v_mfma_f32_16x16x32_bf16 v[148:151], a[216:219], a[124:127], v[148:151]\n" \
"v_mfma_f32_16x16x32_bf16 v[148:151], a[212:215], a[120:123], v[148:151]\n" \
"v_mfma_f32_16x16x32_bf16 v[148:151], a[208:211], a[116:119], v[148:151]\n" \
"v_mfma_f32_16x16x32_bf16 v[148:151], a[204:207], a[112:115], v[148:151]\n" \
"v_mfma_f32_16x16x32_bf16 v[216:219], a[204:207], a[220:223], v[216:219]\n" \
"v_mfma_f32_16x16x32_bf16 v[216:219], a[208:211], a[224:227], v[216:219]\n" \
"v_mfma_f32_16x16x32_bf16 v[216:219], a[212:215], a[228:231], v[216:219]\n" \
"v_mfma_f32_16x16x32_bf16 v[216:219], a[216:219], a[232:235], v[216:219]\n" \
"v_cvt_pk_bf16_f32 v144, v144, v145\n" \
"v_cvt_pk_bf16_f32 v145, v146, v147\n" \
"v_mfma_f32_16x16x32_bf16 v[220:223], a[216:219], a[248:251], v[220:223]\n" \
"ds_read_b64_tr_b16 a[216:217], v10 offset:46272\n" \
"v_cvt_pk_bf16_f32 v146, v148, v149\n" \
"v_cvt_pk_bf16_f32 v147, v150, v151\n" \
"v_mfma_f32_16x16x32_bf16 v[220:223], a[212:215], a[244:247], v[220:223]\n" \
"buffer_store_dwordx4 v[144:147], v20, s[56:59], s71 offen offset:0 sc1\n" \
"v_cvt_pk_bf16_f32 v216, v216, v217\n" \
"v_cvt_pk_bf16_f32 v217, v218, v219\n" \
"v_mfma_f32_16x16x32_bf16 v[220:223], a[208:211], a[240:243], v[220:223]\n" \
"ds_read_b64_tr_b16 a[208:209], v10 offset:27840\n" \
"ds_read_b64_tr_b16 a[210:211], v10 offset:32448\n" \
"v_mfma_f32_16x16x32_bf16 v[220:223], a[204:207], a[236:239], v[220:223]\n" \
"ds_read_b64_tr_b16 a[204:205], v33 offset:160\n" \
"ds_read_b64_tr_b16 a[206:207], v33 offset:4768\n" \
"v_mfma_f32_16x16x32_bf16 v[224:227], a[0:3], a[32:35], v[224:227]\n" \
"ds_read_b64_tr_b16 a[212:213], v10 offset:37056\n" \
"ds_read_b64_tr_b16 a[214:215], v10 offset:41664\n" \
"v_mfma_f32_16x16x32_bf16 v[224:227], a[4:7], a[36:39], v[224:227]\n" \
"ds_read_b64_tr_b16 a[218:219], v10 offset:50880\n" \
"v_mfma_f32_16x16x32_bf16 v[224:227], a[8:11], a[40:43], v[224:227]\n" \
"v_cvt_pk_bf16_f32 v218, v220, v221\n" \
"v_cvt_pk_bf16_f32 v219, v222, v223\n" \
"v_mfma_f32_16x16x32_bf16 v[224:227], a[12:15], a[44:47], v[224:227]\n" \
"buffer_store_dwordx4 v[216:219], v20, s[56:59], s71 offen offset:1024 sc1\n" \
"s_add_u32 s71, s71, 0x1800\n" \
"v_mfma_f32_16x16x32_bf16 v[228:231], a[12:15], a[60:63], v[228:231]\n" \
"ds_read_b64_tr_b16 a[12:13], v33 offset:37056\n" \
"ds_read_b64_tr_b16 a[14:15], v33 offset:41664\n" \
"v_mfma_f32_16x16x32_bf16 v[228:231], a[8:11], a[56:59], v[228:231]\n" \
"ds_read_b64_tr_b16 a[8:9], v33 offset:27840\n" \
"ds_read_b64_tr_b16 a[10:11], v33 offset:32448\n" \
"v_mfma_f32_16x16x32_bf16 v[228:231], a[4:7], a[52:55], v[228:231]\n" \
"ds_read_b64_tr_b16 a[4:5], v33 offset:18624\n" \
"ds_read_b64_tr_b16 a[6:7], v33 offset:23232\n" \
"v_mfma_f32_16x16x32_bf16 v[228:231], a[0:3], a[48:51], v[228:231]\n" \
"ds_read_b64_tr_b16 a[0:1], v10 offset:55488\n" \
"ds_read_b64_tr_b16 a[2:3], v10 offset:60096\n" \
"v_mfma_f32_16x16x32_bf16 v[152:155], a[16:19], a[128:131], v[152:155]\n" \
"v_mfma_f32_16x16x32_bf16 v[152:155], a[20:23], a[132:135], v[152:155]\n" \
"v_mfma_f32_16x16x32_bf16 v[152:155], a[24:27], a[136:139], v[152:155]\n" \
"s_waitcnt lgkmcnt(15)\n" \
"v_mfma_f32_16x16x32_bf16 v[152:155], a[28:31], a[140:143], v[152:155]\n" \
"v_mfma_f32_16x16x32_bf16 v[156:159], a[28:31], a[156:159], v[156:159]\n" \
"v_mfma_f32_16x16x32_bf16 v[156:159], a[24:27], a[152:155], v[156:159]\n" \
"v_mfma_f32_16x16x32_bf16 v[156:159], a[20:23], a[148:151], v[156:159]\n" \
"v_mfma_f32_16x16x32_bf16 v[156:159], a[16:19], a[144:147], v[156:159]\n" \
"v_mfma_f32_16x16x32_bf16 v[224:227], a[16:19], a[160:163], v[224:227]\n" \
"v_mfma_f32_16x16x32_bf16 v[224:227], a[20:23], a[164:167], v[224:227]\n" \
"s_waitcnt vmcnt(4)\n" \
"s_mov_b64 exec, 3\n" \
"buffer_store_dword v39, v41, s[56:59], s69 offen sc1\n" \
"s_mov_b64 exec, -1\n" \
"v_mfma_f32_16x16x32_bf16 v[224:227], a[24:27], a[168:171], v[224:227]\n" \
"s_add_u32 s69, s63, 0x0\n" \
"v_mfma_f32_16x16x32_bf16 v[224:227], a[28:31], a[172:175], v[224:227]\n" \
"v_mfma_f32_16x16x32_bf16 v[228:231], a[28:31], a[188:191], v[228:231]\n" \
"ds_read_b64_tr_b16 a[28:29], v34 offset:18624\n" \
"ds_read_b64_tr_b16 a[30:31], v34 offset:23232\n" \
"v_mfma_f32_16x16x32_bf16 v[228:231], a[24:27], a[184:187], v[228:231]\n" \
"ds_read_b64_tr_b16 a[24:25], v34 offset:9408\n" \
"ds_read_b64_tr_b16 a[26:27], v34 offset:14016\n" \
"v_mfma_f32_16x16x32_bf16 v[228:231], a[20:23], a[180:183], v[228:231]\n" \
"ds_read_b64_tr_b16 a[20:21], v34 offset:192\n" \
"ds_read_b64_tr_b16 a[22:23], v34 offset:4800\n" \
"v_mfma_f32_16x16x32_bf16 v[228:231], a[16:19], a[176:179], v[228:231]\n" \
"ds_read_b64_tr_b16 a[16:17], v33 offset:46272\n" \
"ds_read_b64_tr_b16 a[18:19], v33 offset:50880\n" \
"v_mfma_f32_16x16x32_bf16 v[152:155], a[192:195], a[96:99], v[152:155]\n" \
"v_mfma_f32_16x16x32_bf16 v[152:155], a[196:199], a[100:103], v[152:155]\n" \
"v_mfma_f32_16x16x32_bf16 v[152:155], a[200:203], a[104:107], v[152:155]\n" \
"s_waitcnt lgkmcnt(8)\n" \
"v_mfma_f32_16x16x32_bf16 v[152:155], a[204:207], a[108:111], v[152:155]\n" \
"v_mfma_f32_16x16x32_bf16 v[156:159], a[204:207], a[124:127], v[156:159]\n" \
"v_mfma_f32_16x16x32_bf16 v[156:159], a[200:203], a[120:123], v[156:159]\n" \
"v_mfma_f32_16x16x32_bf16 v[156:159], a[196:199], a[116:119], v[156:159]\n" \
"v_mfma_f32_16x16x32_bf16 v[156:159], a[192:195], a[112:115], v[156:159]\n" \
"v_mfma_f32_16x16x32_bf16 v[224:227], a[192:195], a[220:223], v[224:227]\n" \
"v_mfma_f32_16x16x32_bf16 v[224:227], a[196:199], a[224:227], v[224:227]\n" \
"v_mfma_f32_16x16x32_bf16 v[224:227], a[200:203], a[228:231], v[224:227]\n" \
"v_mfma_f32_16x16x32_bf16 v[224:227], a[204:207], a[232:235], v[224:227]\n" \
"v_cvt_pk_bf16_f32 v152, v152, v153\n" \
"v_cvt_pk_bf16_f32 v153, v154, v155\n" \
"v_mfma_f32_16x16x32_bf16 v[228:231], a[204:207], a[248:251], v[228:231]\n" \
"ds_read_b64_tr_b16 a[204:205], v10 offset:46080\n" \
"v_cvt_pk_bf16_f32 v154, v156, v157\n" \
"v_cvt_pk_bf16_f32 v155, v158, v159\n" \
"v_mfma_f32_16x16x32_bf16 v[228:231], a[200:203], a[244:247], v[228:231]\n" \
"buffer_store_dwordx4 v[152:155], v20, s[56:59], s72 offen offset:0 sc1\n" \
"v_cvt_pk_bf16_f32 v224, v224, v225\n" \
"v_cvt_pk_bf16_f32 v225, v226, v227\n" \
"v_mfma_f32_16x16x32_bf16 v[228:231], a[196:199], a[240:243], v[228:231]\n" \
"ds_read_b64_tr_b16 a[196:197], v10 offset:27648\n" \
"ds_read_b64_tr_b16 a[198:199], v10 offset:32256\n" \
"v_mfma_f32_16x16x32_bf16 v[228:231], a[192:195], a[236:239], v[228:231]\n" \
"ds_read_b64_tr_b16 a[192:193], v33 offset:192\n" \
"ds_read_b64_tr_b16 a[194:195], v33 offset:4800\n" \
"v_mfma_f32_16x16x32_bf16 v[232:235], a[208:211], a[32:35], v[232:235]\n" \
"ds_read_b64_tr_b16 a[200:201], v10 offset:36864\n" \
"ds_read_b64_tr_b16 a[202:203], v10 offset:41472\n" \
"v_mfma_f32_16x16x32_bf16 v[232:235], a[212:215], a[36:39], v[232:235]\n" \
"ds_read_b64_tr_b16 a[206:207], v10 offset:50688\n" \
"v_mfma_f32_16x16x32_bf16 v[232:235], a[216:219], a[40:43], v[232:235]\n" \
"v_cvt_pk_bf16_f32 v226, v228, v229\n" \
"v_cvt_pk_bf16_f32 v227, v230, v231\n" \
"v_mfma_f32_16x16x32_bf16 v[232:235], a[0:3], a[44:47], v[232:235]\n" \
"buffer_store_dwordx4 v[224:227], v20, s[56:59], s72 offen offset:1024 sc1\n" \
"s_add_u32 s72, s72, 0x1000\n" \
"v_mfma_f32_16x16x32_bf16 v[236:239], a[0:3], a[60:63], v[236:239]\n" \
"ds_read_b64_tr_b16 a[0:1], v33 offset:36864\n" \
"ds_read_b64_tr_b16 a[2:3], v33 offset:41472\n" \
"v_mfma_f32_16x16x32_bf16 v[236:239], a[216:219], a[56:59], v[236:239]\n" \
"ds_read_b64_tr_b16 a[216:217], v33 offset:27648\n" \
"ds_read_b64_tr_b16 a[218:219], v33 offset:32256\n" \
"v_mfma_f32_16x16x32_bf16 v[236:239], a[212:215], a[52:55], v[236:239]\n" \
"ds_read_b64_tr_b16 a[212:213], v33 offset:18432\n" \
"ds_read_b64_tr_b16 a[214:215], v33 offset:23040\n" \
"v_mfma_f32_16x16x32_bf16 v[236:239], a[208:211], a[48:51], v[236:239]\n" \
"ds_read_b64_tr_b16 a[208:209], v10 offset:55296\n" \
"ds_read_b64_tr_b16 a[210:211], v10 offset:59904\n" \
"v_mfma_f32_16x16x32_bf16 v[160:163], a[4:7], a[128:131], v[160:163]\n" \
"v_mfma_f32_16x16x32_bf16 v[160:163], a[8:11], a[132:135], v[160:163]\n" \
"v_mfma_f32_16x16x32_bf16 v[160:163], a[12:15], a[136:139], v[160:163]\n" \
"s_waitcnt lgkmcnt(15)\n" \
"v_mfma_f32_16x16x32_bf16 v[160:163], a[16:19], a[140:143], v[160:163]\n" \
"v_mfma_f32_16x16x32_bf16 v[164:167], a[16:19], a[156:159], v[164:167]\n" \
"v_mfma_f32_16x16x32_bf16 v[164:167], a[12:15], a[152:155], v[164:167]\n" \
"v_mfma_f32_16x16x32_bf16 v[164:167], a[8:11], a[148:151], v[164:167]\n" \
"v_mfma_f32_16x16x32_bf16 v[164:167], a[4:7], a[144:147], v[164:167]\n" \
"s_waitcnt vmcnt(5)\n" \
"s_mov_b64 exec, 1\n" \
"buffer_store_dword v39, off, s[56:59], s70 offset:16 sc1\n" \
"s_mov_b64 exec, -1\n" \
"v_mfma_f32_16x16x32_bf16 v[232:235], a[4:7], a[160:163], v[232:235]\n" \
"s_add_u32 s70, s63, 0x800\n" \
"v_mfma_f32_16x16x32_bf16 v[232:235], a[8:11], a[164:167], v[232:235]\n" \
"v_mfma_f32_16x16x32_bf16 v[232:235], a[12:15], a[168:171], v[232:235]\n" \
"v_mfma_f32_16x16x32_bf16 v[232:235], a[16:19], a[172:175], v[232:235]\n" \
"v_mfma_f32_16x16x32_bf16 v[236:239], a[16:19], a[188:191], v[236:239]\n" \
"ds_read_b64_tr_b16 a[16:17], v34 offset:18432\n" \
"ds_read_b64_tr_b16 a[18:19], v34 offset:23040\n" \
"v_mfma_f32_16x16x32_bf16 v[236:239], a[12:15], a[184:187], v[236:239]\n" \
"ds_read_b64_tr_b16 a[12:13], v34 offset:9216\n" \
"ds_read_b64_tr_b16 a[14:15], v34 offset:13824\n" \
"v_mfma_f32_16x16x32_bf16 v[236:239], a[8:11], a[180:183], v[236:239]\n" \
"ds_read_b64_tr_b16 a[8:9], v34 offset:0\n" \
"ds_read_b64_tr_b16 a[10:11], v34 offset:4608\n" \
"v_mfma_f32_16x16x32_bf16 v[236:239], a[4:7], a[176:179], v[236:239]\n" \
"ds_read_b64_tr_b16 a[4:5], v33 offset:46080\n" \
"ds_read_b64_tr_b16 a[6:7], v33 offset:50688\n" \
"v_mfma_f32_16x16x32_bf16 v[160:163], a[20:23], a[96:99], v[160:163]\n" \
"v_mfma_f32_16x16x32_bf16 v[160:163], a[24:27], a[100:103], v[160:163]\n" \
"v_mfma_f32_16x16x32_bf16 v[160:163], a[28:31], a[104:107], v[160:163]\n" \
"s_waitcnt lgkmcnt(8)\n" \
"v_mfma_f32_16x16x32_bf16 v[160:163], a[192:195], a[108:111], v[160:163]\n" \
"v_mfma_f32_16x16x32_bf16 v[164:167], a[192:195], a[124:127], v[164:167]\n" \
"v_mfma_f32_16x16x32_bf16 v[164:167], a[28:31], a[120:123], v[164:167]\n" \
"v_mfma_f32_16x16x32_bf16 v[164:167], a[24:27], a[116:119], v[164:167]\n" \
"v_mfma_f32_16x16x32_bf16 v[164:167], a[20:23], a[112:115], v[164:167]\n" \
"v_mfma_f32_16x16x32_bf16 v[232:235], a[20:23], a[220:223], v[232:235]\n" \
"v_mfma_f32_16x16x32_bf16 v[232:235], a[24:27], a[224:227], v[232:235]\n" \
"v_mfma_f32_16x16x32_bf16 v[232:235], a[28:31], a[228:231], v[232:235]\n" \
"v_mfma_f32_16x16x32_bf16 v[232:235], a[192:195], a[232:235], v[232:235]\n" \
"v_cvt_pk_bf16_f32 v160, v160, v161\n" \
"v_cvt_pk_bf16_f32 v161, v162, v163\n" \
"v_mfma_f32_16x16x32_bf16 v[236:239], a[192:195], a[248:251], v[236:239]\n" \
"ds_read_b64_tr_b16 a[192:193], v10 offset:46112\n" \
"v_cvt_pk_bf16_f32 v162, v164, v165\n" \
"v_cvt_pk_bf16_f32 v163, v166, v167\n" \
"v_mfma_f32_16x16x32_bf16 v[236:239], a[28:31], a[244:247], v[236:239]\n" \
"buffer_store_dwordx4 v[160:163], v20, s[56:59], s73 offen offset:0 sc1\n" \
"v_cvt_pk_bf16_f32 v232, v232, v233\n" \
"v_cvt_pk_bf16_f32 v233, v234, v235\n" \
"v_mfma_f32_16x16x32_bf16 v[236:239], a[24:27], a[240:243], v[236:239]\n" \
"ds_read_b64_tr_b16 a[24:25], v10 offset:27680\n" \
"ds_read_b64_tr_b16 a[26:27], v10 offset:32288\n" \
"v_mfma_f32_16x16x32_bf16 v[236:239], a[20:23], a[236:239], v[236:239]\n" \
"ds_read_b64_tr_b16 a[20:21], v33 offset:0\n" \
"ds_read_b64_tr_b16 a[22:23], v33 offset:4608\n" \
"v_mfma_f32_16x16x32_bf16 v[184:187], a[196:199], a[32:35], v[184:187]\n" \
"ds_read_b64_tr_b16 a[28:29], v10 offset:36896\n" \
"ds_read_b64_tr_b16 a[30:31], v10 offset:41504\n" \
"v_mfma_f32_16x16x32_bf16 v[184:187], a[200:203], a[36:39], v[184:187]\n" \
"ds_read_b64_tr_b16 a[194:195], v10 offset:50720\n" \
"v_mfma_f32_16x16x32_bf16 v[184:187], a[204:207], a[40:43], v[184:187]\n" \
"v_cvt_pk_bf16_f32 v234, v236, v237\n" \
"v_cvt_pk_bf16_f32 v235, v238, v239\n" \
"v_mfma_f32_16x16x32_bf16 v[184:187], a[208:211], a[44:47], v[184:187]\n" \
"buffer_store_dwordx4 v[232:235], v20, s[56:59], s73 offen offset:1024 sc1\n" \
"s_add_u32 s73, s73, 0x800\n" \
"v_mfma_f32_16x16x32_bf16 v[188:191], a[208:211], a[60:63], v[188:191]\n" \
"ds_read_b64_tr_b16 a[208:209], v33 offset:36896\n" \
"ds_read_b64_tr_b16 a[210:211], v33 offset:41504\n" \
"v_mfma_f32_16x16x32_bf16 v[188:191], a[204:207], a[56:59], v[188:191]\n" \
"ds_read_b64_tr_b16 a[204:205], v33 offset:27680\n" \
"ds_read_b64_tr_b16 a[206:207], v33 offset:32288\n" \
"v_mfma_f32_16x16x32_bf16 v[188:191], a[200:203], a[52:55], v[188:191]\n" \
"ds_read_b64_tr_b16 a[200:201], v33 offset:18464\n" \
"ds_read_b64_tr_b16 a[202:203], v33 offset:23072\n" \
"v_mfma_f32_16x16x32_bf16 v[188:191], a[196:199], a[48:51], v[188:191]\n" \
"ds_read_b64_tr_b16 a[196:197], v10 offset:55328\n" \
"ds_read_b64_tr_b16 a[198:199], v10 offset:59936\n" \
"v_mfma_f32_16x16x32_bf16 v[184:187], a[212:215], a[160:163], v[184:187]\n" \
"v_mfma_f32_16x16x32_bf16 v[184:187], a[216:219], a[164:167], v[184:187]\n" \
"v_mfma_f32_16x16x32_bf16 v[184:187], a[0:3], a[168:171], v[184:187]\n" \
"s_waitcnt lgkmcnt(15)\n" \
"v_mfma_f32_16x16x32_bf16 v[184:187], a[4:7], a[172:175], v[184:187]\n" \
"v_mfma_f32_16x16x32_bf16 v[188:191], a[4:7], a[188:191], v[188:191]\n" \
"ds_read_b64_tr_b16 a[4:5], v34 offset:18464\n" \
"ds_read_b64_tr_b16 a[6:7], v34 offset:23072\n" \
"v_mfma_f32_16x16x32_bf16 v[188:191], a[0:3], a[184:187], v[188:191]\n" \
"ds_read_b64_tr_b16 a[0:1], v34 offset:9248\n" \
"ds_read_b64_tr_b16 a[2:3], v34 offset:13856\n" \
"s_waitcnt vmcnt(6)\n" \
"s_mov_b64 exec, 1\n" \
"buffer_store_dword v39, off, s[56:59], s71 offset:20 sc1\n" \
"s_mov_b64 exec, -1\n" \
"v_mfma_f32_16x16x32_bf16 v[188:191], a[216:219], a[180:183], v[188:191]\n" \
"ds_read_b64_tr_b16 a[216:217], v34 offset:32\n" \
"ds_read_b64_tr_b16 a[218:219], v34 offset:4640\n" \
"v_mfma_f32_16x16x32_bf16 v[188:191], a[212:215], a[176:179], v[188:191]\n" \
"ds_read_b64_tr_b16 a[212:213], v33 offset:46112\n" \
"ds_read_b64_tr_b16 a[214:215], v33 offset:50720\n" \
"v_mfma_f32_16x16x32_bf16 v[184:187], a[8:11], a[220:223], v[184:187]\n" \
"s_add_u32 s71, s63, 0x1000\n" \
"v_mfma_f32_16x16x32_bf16 v[184:187], a[12:15], a[224:227], v[184:187]\n" \
"v_mfma_f32_16x16x32_bf16 v[184:187], a[16:19], a[228:231], v[184:187]\n" \
"s_waitcnt lgkmcnt(12)\n" \
"v_mfma_f32_16x16x32_bf16 v[184:187], a[20:23], a[232:235], v[184:187]\n" \
"v_mfma_f32_16x16x32_bf16 v[188:191], a[20:23], a[248:251], v[188:191]\n" \
"ds_read_b64_tr_b16 a[20:21], v10 offset:46144\n" \
"ds_read_b64_tr_b16 a[22:23], v10 offset:50752\n" \
"v_mfma_f32_16x16x32_bf16 v[188:191], a[16:19], a[244:247], v[188:191]\n" \
"ds_read_b64_tr_b16 a[16:17], v10 offset:36928\n" \
"ds_read_b64_tr_b16 a[18:19], v10 offset:41536\n" \
"v_mfma_f32_16x16x32_bf16 v[188:191], a[12:15], a[240:243], v[188:191]\n" \
"ds_read_b64_tr_b16 a[12:13], v10 offset:27712\n" \
"v_cvt_pk_bf16_f32 v184, v184, v185\n" \
"v_cvt_pk_bf16_f32 v185, v186, v187\n" \
"v_mfma_f32_16x16x32_bf16 v[188:191], a[8:11], a[236:239], v[188:191]\n" \
"ds_read_b64_tr_b16 a[8:9], v33 offset:32\n" \
"ds_read_b64_tr_b16 a[10:11], v33 offset:4640\n" \
"v_mfma_f32_16x16x32_bf16 v[192:195], a[24:27], a[32:35], v[192:195]\n" \
"ds_read_b64_tr_b16 a[14:15], v10 offset:32320\n" \
"v_mfma_f32_16x16x32_bf16 v[192:195], a[28:31], a[36:39], v[192:195]\n" \
"v_mfma_f32_16x16x32_bf16 v[192:195], a[192:195], a[40:43], v[192:195]\n" \
"s_waitcnt lgkmcnt(12)\n" \
"v_mfma_f32_16x16x32_bf16 v[192:195], a[196:199], a[44:47], v[192:195]\n" \
"v_cvt_pk_bf16_f32 v186, v188, v189\n" \
"v_cvt_pk_bf16_f32 v187, v190, v191\n" \
"v_mfma_f32_16x16x32_bf16 v[196:199], a[196:199], a[60:63], v[196:199]\n" \
"buffer_store_dwordx4 v[184:187], v20, s[56:59], s92 offen offset:1024 sc1\n" \
"s_add_u32 s92, s92, 0x3800\n" \
"v_mfma_f32_16x16x32_bf16 v[196:199], a[192:195], a[56:59], v[196:199]\n" \
"ds_read_b64_tr_b16 a[192:193], v33 offset:27712\n" \
"ds_read_b64_tr_b16 a[194:195], v33 offset:32320\n" \
"v_mfma_f32_16x16x32_bf16 v[196:199], a[28:31], a[52:55], v[196:199]\n" \
"ds_read_b64_tr_b16 a[28:29], v33 offset:18496\n" \
"ds_read_b64_tr_b16 a[30:31], v33 offset:23104\n" \
"v_mfma_f32_16x16x32_bf16 v[196:199], a[24:27], a[48:51], v[196:199]\n" \
"ds_read_b64_tr_b16 a[24:25], v10 offset:55360\n" \
"ds_read_b64_tr_b16 a[26:27], v10 offset:59968\n" \
"v_mfma_f32_16x16x32_bf16 v[192:195], a[200:203], a[160:163], v[192:195]\n" \
"ds_read_b64_tr_b16 a[196:197], v33 offset:36928\n" \
"ds_read_b64_tr_b16 a[198:199], v33 offset:41536\n" \
"v_mfma_f32_16x16x32_bf16 v[192:195], a[204:207], a[164:167], v[192:195]\n" \
"v_mfma_f32_16x16x32_bf16 v[192:195], a[208:211], a[168:171], v[192:195]\n" \
"s_waitcnt lgkmcnt(14)\n" \
"v_mfma_f32_16x16x32_bf16 v[192:195], a[212:215], a[172:175], v[192:195]\n" \
"v_mfma_f32_16x16x32_bf16 v[196:199], a[212:215], a[188:191], v[196:199]\n" \
"ds_read_b64_tr_b16 a[212:213], v34 offset:18496\n" \
"ds_read_b64_tr_b16 a[214:215], v34 offset:23104\n" \
"v_mfma_f32_16x16x32_bf16 v[196:199], a[208:211], a[184:187], v[196:199]\n" \
"L_pf22:\n" \
"s_cbranch_execz L_retB\n" \
"s_nop 0\n" \
"ds_read_b64_tr_b16 a[208:209], v34 offset:9280\n" \
"ds_read_b64_tr_b16 a[210:211], v34 offset:13888\n" \
"v_mfma_f32_16x16x32_bf16 v[196:199], a[204:207], a[180:183], v[196:199]\n" \
"ds_read_b64_tr_b16 a[204:205], v34 offset:64\n" \
"ds_read_b64_tr_b16 a[206:207], v34 offset:4672\n" \
"v_mfma_f32_16x16x32_bf16 v[196:199], a[200:203], a[176:179], v[196:199]\n" \
"ds_read_b64_tr_b16 a[200:201], v33 offset:46144\n" \
"ds_read_b64_tr_b16 a[202:203], v33 offset:50752\n" \
"v_mfma_f32_16x16x32_bf16 v[192:195], a[216:219], a[220:223], v[192:195]\n" \
"v_mfma_f32_16x16x32_bf16 v[192:195], a[0:3], a[224:227], v[192:195]\n" \
"v_mfma_f32_16x16x32_bf16 v[192:195], a[4:7], a[228:231], v[192:195]\n" \
"s_waitcnt lgkmcnt(15)\n" \
"v_mfma_f32_16x16x32_bf16 v[192:195], a[8:11], a[232:235], v[192:195]\n" \
"v_mfma_f32_16x16x32_bf16 v[196:199], a[8:11], a[248:251], v[196:199]\n" \
"ds_read_b64_tr_b16 a[8:9], v10 offset:46304\n" \
"ds_read_b64_tr_b16 a[10:11], v10 offset:50912\n" \
"v_mfma_f32_16x16x32_bf16 v[196:199], a[4:7], a[244:247], v[196:199]\n" \
"ds_read_b64_tr_b16 a[4:5], v10 offset:37088\n" \
"ds_read_b64_tr_b16 a[6:7], v10 offset:41696\n" \
"v_mfma_f32_16x16x32_bf16 v[196:199], a[0:3], a[240:243], v[196:199]\n" \
"ds_read_b64_tr_b16 a[0:1], v10 offset:27872\n" \
"v_cvt_pk_bf16_f32 v192, v192, v193\n" \
"v_cvt_pk_bf16_f32 v193, v194, v195\n" \
"v_mfma_f32_16x16x32_bf16 v[196:199], a[216:219], a[236:239], v[196:199]\n" \
"ds_read_b64_tr_b16 a[216:217], v33 offset:64\n" \
"ds_read_b64_tr_b16 a[218:219], v33 offset:4672\n" \
"v_mfma_f32_16x16x32_bf16 v[200:203], a[12:15], a[32:35], v[200:203]\n" \
"ds_read_b64_tr_b16 a[2:3], v10 offset:32480\n" \
"v_mfma_f32_16x16x32_bf16 v[200:203], a[16:19], a[36:39], v[200:203]\n" \
"v_mfma_f32_16x16x32_bf16 v[200:203], a[20:23], a[40:43], v[200:203]\n" \
"s_waitcnt lgkmcnt(14)\n" \
"v_mfma_f32_16x16x32_bf16 v[200:203], a[24:27], a[44:47], v[200:203]\n" \
"v_cvt_pk_bf16_f32 v194, v196, v197\n" \
"v_cvt_pk_bf16_f32 v195, v198, v199\n" \
"v_mfma_f32_16x16x32_bf16 v[204:207], a[24:27], a[60:63], v[204:207]\n" \
"buffer_store_dwordx4 v[192:195], v20, s[56:59], s93 offen offset:1024 sc1\n" \
"s_add_u32 s93, s93, 0x3000\n" \
"v_mfma_f32_16x16x32_bf16 v[204:207], a[20:23], a[56:59], v[204:207]\n" \
"ds_read_b64_tr_b16 a[20:21], v33 offset:27872\n" \
"ds_read_b64_tr_b16 a[22:23], v33 offset:32480\n" \
"s_waitcnt vmcnt(6)\n" \
"s_mov_b64 exec, 1\n" \
"buffer_store_dword v39, off, s[56:59], s72 offset:24 sc1\n" \
"s_mov_b64 exec, -1\n" \
"v_readlane_b32 s72, v11, 50\n" \
"v_mfma_f32_16x16x32_bf16 v[204:207], a[16:19], a[52:55], v[204:207]\n" \
"ds_read_b64_tr_b16 a[16:17], v33 offset:18656\n" \
"ds_read_b64_tr_b16 a[18:19], v33 offset:23264\n" \
"s_mov_b64 exec, 1\n" \
"s_nop 0\n" \
"buffer_store_dword v46, off, s[56:59], s72 offset:0 sc1\n" \
"s_mov_b64 exec, -1\n" \
"v_mfma_f32_16x16x32_bf16 v[204:207], a[12:15], a[48:51], v[204:207]\n" \
"ds_read_b64_tr_b16 a[12:13], v10 offset:55520\n" \
"ds_read_b64_tr_b16 a[14:15], v10 offset:60128\n" \
"v_mfma_f32_16x16x32_bf16 v[200:203], a[28:31], a[160:163], v[200:203]\n" \
"ds_read_b64_tr_b16 a[24:25], v33 offset:37088\n" \
"ds_read_b64_tr_b16 a[26:27], v33 offset:41696\n" \
"v_mfma_f32_16x16x32_bf16 v[200:203], a[192:195], a[164:167], v[200:203]\n" \
"v_mfma_f32_16x16x32_bf16 v[200:203], a[196:199], a[168:171], v[200:203]\n" \
"s_waitcnt lgkmcnt(10)\n" \
"v_mfma_f32_16x16x32_bf16 v[200:203], a[200:203], a[172:175], v[200:203]\n" \
"v_mfma_f32_16x16x32_bf16 v[204:207], a[200:203], a[188:191], v[204:207]\n" \
"ds_read_b64_tr_b16 a[200:201], v34 offset:18656\n" \
"ds_read_b64_tr_b16 a[202:203], v34 offset:23264\n" \
"v_mfma_f32_16x16x32_bf16 v[204:207], a[196:199], a[184:187], v[204:207]\n" \
"ds_read_b64_tr_b16 a[196:197], v34 offset:9440\n" \
"ds_read_b64_tr_b16 a[198:199], v34 offset:14048\n" \
"v_mfma_f32_16x16x32_bf16 v[204:207], a[192:195], a[180:183], v[204:207]\n" \
"ds_read_b64_tr_b16 a[192:193], v34 offset:224\n" \
"ds_read_b64_tr_b16 a[194:195], v34 offset:4832\n" \
"v_mfma_f32_16x16x32_bf16 v[204:207], a[28:31], a[176:179], v[204:207]\n" \
"ds_read_b64_tr_b16 a[28:29], v33 offset:46304\n" \
"ds_read_b64_tr_b16 a[30:31], v33 offset:50912\n" \
"v_mfma_f32_16x16x32_bf16 v[200:203], a[204:207], a[220:223], v[200:203]\n" \
"v_mfma_f32_16x16x32_bf16 v[200:203], a[208:211], a[224:227], v[200:203]\n" \
"v_mfma_f32_16x16x32_bf16 v[200:203], a[212:215], a[228:231], v[200:203]\n" \
"s_waitcnt lgkmcnt(14)\n" \
"v_mfma_f32_16x16x32_bf16 v[200:203], a[216:219], a[232:235], v[200:203]\n" \
"v_mfma_f32_16x16x32_bf16 v[204:207], a[216:219], a[248:251], v[204:207]\n" \
"ds_read_b64_tr_b16 a[216:217], v36 offset:32\n" \
"ds_read_b64_tr_b16 a[218:219], v36 offset:1056\n" \
"v_mfma_f32_16x16x32_bf16 v[204:207], a[212:215], a[244:247], v[204:207]\n" \
"ds_read_b64_tr_b16 a[212:213], v36 offset:2048\n" \
"ds_read_b64_tr_b16 a[214:215], v36 offset:3072\n" \
"v_mfma_f32_16x16x32_bf16 v[204:207], a[208:211], a[240:243], v[204:207]\n" \
"ds_read_b64_tr_b16 a[208:209], v36 offset:0\n" \
"v_cvt_pk_bf16_f32 v200, v200, v201\n" \
"v_cvt_pk_bf16_f32 v201, v202, v203\n" \
"v_mfma_f32_16x16x32_bf16 v[204:207], a[204:207], a[236:239], v[204:207]\n" \
"ds_read_b64_tr_b16 a[204:205], v33 offset:224\n" \
"ds_read_b64_tr_b16 a[206:207], v33 offset:4832\n" \
"v_mfma_f32_16x16x32_bf16 v[240:243], a[0:3], a[32:35], v[240:243]\n" \
"ds_read_b64_tr_b16 a[210:211], v36 offset:1024\n" \
"v_mfma_f32_16x16x32_bf16 v[240:243], a[4:7], a[36:39], v[240:243]\n" \
"v_mfma_f32_16x16x32_bf16 v[240:243], a[8:11], a[40:43], v[240:243]\n" \
"s_waitcnt lgkmcnt(14)\n" \
"v_mfma_f32_16x16x32_bf16 v[240:243], a[12:15], a[44:47], v[240:243]\n" \
"v_cvt_pk_bf16_f32 v202, v204, v205\n" \
"v_cvt_pk_bf16_f32 v203, v206, v207\n" \
"v_mfma_f32_16x16x32_bf16 v[244:247], a[12:15], a[60:63], v[244:247]\n" \
"buffer_store_dwordx4 v[200:203], v20, s[56:59], s94 offen offset:1024 sc1\n" \
"s_add_u32 s94, s94, 0x2800\n" \
"v_mfma_f32_16x16x32_bf16 v[244:247], a[8:11], a[56:59], v[244:247]\n" \
"v_mfma_f32_16x16x32_bf16 v[244:247], a[4:7], a[52:55], v[244:247]\n" \
"v_mfma_f32_16x16x32_bf16 v[244:247], a[0:3], a[48:51], v[244:247]\n" \
"ds_read_b64_tr_b16 a[0:1], v36 offset:2080\n" \
"ds_read_b64_tr_b16 a[2:3], v36 offset:3104\n" \
"v_mfma_f32_16x16x32_bf16 v[168:171], a[16:19], a[128:131], v[168:171]\n" \
"v_mfma_f32_16x16x32_bf16 v[168:171], a[20:23], a[132:135], v[168:171]\n" \
"v_mfma_f32_16x16x32_bf16 v[168:171], a[24:27], a[136:139], v[168:171]\n" \
"s_waitcnt lgkmcnt(8)\n" \
"v_mfma_f32_16x16x32_bf16 v[168:171], a[28:31], a[140:143], v[168:171]\n" \
"v_mfma_f32_16x16x32_bf16 v[172:175], a[28:31], a[156:159], v[172:175]\n" \
"v_mfma_f32_16x16x32_bf16 v[172:175], a[24:27], a[152:155], v[172:175]\n" \
"v_mfma_f32_16x16x32_bf16 v[172:175], a[20:23], a[148:151], v[172:175]\n" \
"v_mfma_f32_16x16x32_bf16 v[172:175], a[16:19], a[144:147], v[172:175]\n" \
"v_mfma_f32_16x16x32_bf16 v[240:243], a[16:19], a[160:163], v[240:243]\n" \
"v_mfma_f32_16x16x32_bf16 v[240:243], a[20:23], a[164:167], v[240:243]\n" \
"v_mfma_f32_16x16x32_bf16 v[240:243], a[24:27], a[168:171], v[240:243]\n" \
"v_mfma_f32_16x16x32_bf16 v[240:243], a[28:31], a[172:175], v[240:243]\n" \
"v_mfma_f32_16x16x32_bf16 v[244:247], a[28:31], a[188:191], v[244:247]\n" \
"s_waitcnt vmcnt(6)\n" \
"s_mov_b64 exec, 1\n" \
"buffer_store_dword v39, off, s[56:59], s73 offset:28 sc1\n" \
"s_mov_b64 exec, -1\n" \
"v_mfma_f32_16x16x32_bf16 v[244:247], a[24:27], a[184:187], v[244:247]\n" \
"v_readlane_b32 s73, v11, 41\n" \
"v_mfma_f32_16x16x32_bf16 v[244:247], a[20:23], a[180:183], v[244:247]\n" \
"v_mfma_f32_16x16x32_bf16 v[244:247], a[16:19], a[176:179], v[244:247]\n" \
"v_mfma_f32_16x16x32_bf16 v[168:171], a[192:195], a[96:99], v[168:171]\n" \
"v_mfma_f32_16x16x32_bf16 v[168:171], a[196:199], a[100:103], v[168:171]\n" \
"v_mfma_f32_16x16x32_bf16 v[168:171], a[200:203], a[104:107], v[168:171]\n" \
"s_waitcnt lgkmcnt(0)\n" \
"v_mfma_f32_16x16x32_bf16 v[168:171], a[204:207], a[108:111], v[168:171]\n" \
"v_mfma_f32_16x16x32_bf16 v[172:175], a[204:207], a[124:127], v[172:175]\n" \
"v_mfma_f32_16x16x32_bf16 v[172:175], a[200:203], a[120:123], v[172:175]\n" \
"v_mfma_f32_16x16x32_bf16 v[172:175], a[196:199], a[116:119], v[172:175]\n" \
"v_mfma_f32_16x16x32_bf16 v[172:175], a[192:195], a[112:115], v[172:175]\n" \
"v_mfma_f32_16x16x32_bf16 v[240:243], a[192:195], a[220:223], v[240:243]\n" \
"v_mfma_f32_16x16x32_bf16 v[240:243], a[196:199], a[224:227], v[240:243]\n" \
"v_mfma_f32_16x16x32_bf16 v[240:243], a[200:203], a[228:231], v[240:243]\n" \
"v_mfma_f32_16x16x32_bf16 v[240:243], a[204:207], a[232:235], v[240:243]\n" \
"v_mfma_f32_16x16x32_bf16 v[244:247], a[204:207], a[248:251], v[244:247]\n" \
"v_mfma_f32_16x16x32_bf16 v[244:247], a[200:203], a[244:247], v[244:247]\n" \
"v_mfma_f32_16x16x32_bf16 v[244:247], a[196:199], a[240:243], v[244:247]\n" \
"v_mfma_f32_16x16x32_bf16 v[244:247], a[192:195], a[236:239], v[244:247]\n" \
"v_mfma_f32_16x16x32_bf16 v[168:171], a[208:211], v[64:67], v[168:171]\n" \
"v_mfma_f32_16x16x32_bf16 v[172:175], a[208:211], v[72:75], v[172:175]\n" \
"v_mfma_f32_16x16x32_bf16 v[240:243], a[208:211], v[80:83], v[240:243]\n" \
"v_mfma_f32_16x16x32_bf16 v[244:247], a[208:211], v[88:91], v[244:247]\n" \
"v_mfma_f32_16x16x32_bf16 v[168:171], a[212:215], v[68:71], v[168:171]\n" \
"v_mfma_f32_16x16x32_bf16 v[172:175], a[212:215], v[76:79], v[172:175]\n" \
"v_mfma_f32_16x16x32_bf16 v[240:243], a[212:215], v[84:87], v[240:243]\n" \
"s_waitcnt vmcnt(5)\n" \
"s_mov_b64 exec, 1\n" \
"buffer_store_dword v39, off, s[56:59], s92 offset:32 sc1\n" \
"s_mov_b64 exec, -1\n" \
"v_mfma_f32_16x16x32_bf16 v[244:247], a[212:215], v[92:95], v[244:247]\n" \
"v_readlane_b32 s92, v11, 39\n" \
"v_mfma_f32_16x16x32_bf16 v[56:59], a[216:219], v[96:99], 0\n" \
"v_mfma_f32_16x16x32_bf16 v[60:63], a[216:219], v[104:107], 0\n" \
"v_mfma_f32_16x16x32_bf16 v[56:59], a[0:3], v[100:103], v[56:59]\n" \
"v_mfma_f32_16x16x32_bf16 v[60:63], a[0:3], v[108:111], v[60:63]\n" \
"s_waitcnt vmcnt(5)\n" \
"s_mov_b64 exec, 1\n" \
"buffer_store_dword v39, off, s[56:59], s93 offset:36 sc1\n" \
"s_mov_b64 exec, -1\n" \
"v_readlane_b32 s93, v11, 40\n" \
"s_waitcnt vmcnt(3)\n" \
"s_mov_b64 exec, 1\n" \
"buffer_store_dword v39, off, s[56:59], s94 offset:40 sc1\n" \
"s_mov_b64 exec, -1\n" \
"buffer_load_dword v37, v40, s[56:59], s74 offen sc1\n" \
"s_waitcnt vmcnt(0)\n" \
"v_cmp_eq_u32_e64 s[98:99], s62, v37\n" \
"s_nop 0\n" \
"s_andn2_b32 s75, 0x1, s98\n" \
"s_cbranch_scc1 L_sx0_%=\n" \
"L_rx0_%=:\n" \
"buffer_load_dwordx4 v[116:119], v20, s[56:59], s95 offen offset:0 sc1\n" \
"s_andn2_b32 s75, 0x2, s98\n" \
"s_cbranch_scc1 L_sx1_%=\n" \
"L_rx1_%=:\n" \
"buffer_load_dwordx4 v[124:127], v20, s[56:59], s96 offen offset:0 sc1\n" \
"s_andn2_b32 s75, 0x4, s98\n" \
"s_cbranch_scc1 L_sx2_%=\n" \
"L_rx2_%=:\n" \
"buffer_load_dwordx4 v[132:135], v20, s[56:59], s97 offen offset:0 sc1\n" \
"s_andn2_b32 s75, s100, s98\n" \
"s_cbranch_scc1 L_sx3_%=\n" \
"L_rx3_%=:\n" \
"buffer_load_dwordx4 v[136:139], v20, s[56:59], s64 offen offset:0 sc1\n" \
"buffer_load_dwordx4 v[208:211], v20, s[56:59], s64 offen offset:2048 sc1\n" \
"s_add_u32 s64, s64, 0x1000\n" \
"buffer_load_dwordx4 v[144:147], v20, s[56:59], s64 offen offset:0 sc1\n" \
"buffer_load_dwordx4 v[216:219], v20, s[56:59], s64 offen offset:2048 sc1\n" \
"s_add_u32 s64, s64, 0x1000\n" \
"buffer_load_dwordx4 v[152:155], v20, s[56:59], s64 offen offset:0 sc1\n" \
"buffer_load_dwordx4 v[224:227], v20, s[56:59], s64 offen offset:2048 sc1\n" \
"s_add_u32 s64, s64, 0x1000\n" \
"buffer_load_dwordx4 v[176:179], v20, s[56:59], s64 offen offset:0 sc1\n" \
"buffer_load_dwordx4 v[248:251], v20, s[56:59], s64 offen offset:2048 sc1\n" \
"s_andn2_b32 s75, 0x10, s98\n" \
"s_cbranch_scc1 L_sx4_%=\n" \
"L_rx4_%=:\n" \
"buffer_load_dwordx4 v[140:143], v20, s[56:59], s65 offen offset:0 sc1\n" \
"buffer_load_dwordx4 v[212:215], v20, s[56:59], s65 offen offset:1024 sc1\n" \
"s_andn2_b32 s75, 0x20, s98\n" \
"s_cbranch_scc1 L_sx5_%=\n" \
"L_rx5_%=:\n" \
"buffer_load_dwordx4 v[148:151], v20, s[56:59], s66 offen offset:0 sc1\n" \
"buffer_load_dwordx4 v[220:223], v20, s[56:59], s66 offen offset:1024 sc1\n" \
"s_andn2_b32 s75, 0x40, s98\n" \
"s_cbranch_scc1 L_sx6_%=\n" \
"L_rx6_%=:\n" \
"buffer_load_dwordx4 v[156:159], v20, s[56:59], s67 offen offset:0 sc1\n" \
"buffer_load_dwordx4 v[228:231], v20, s[56:59], s67 offen offset:1024 sc1\n" \
"s_andn2_b32 s75, 0x80, s98\n" \
"s_cbranch_scc1 L_sx7_%=\n" \
"L_rx7_%=:\n" \
"buffer_load_dwordx4 v[164:167], v20, s[56:59], s68 offen offset:0 sc1\n" \
"buffer_load_dwordx4 v[236:239], v20, s[56:59], s68 offen offset:1024 sc1\n" \
"s_andn2_b32 s75, 0x100, s98\n" \
"s_cbranch_scc1 L_sx8_%=\n" \
"L_rx8_%=:\n" \
"buffer_load_dwordx4 v[188:191], v20, s[56:59], s69 offen offset:1024 sc1\n" \
"s_andn2_b32 s75, 0x200, s98\n" \
"s_cbranch_scc1 L_sx9_%=\n" \
"L_rx9_%=:\n" \
"buffer_load_dwordx4 v[196:199], v20, s[56:59], s70 offen offset:1024 sc1\n" \
"s_andn2_b32 s75, 0x400, s98\n" \
"s_cbranch_scc1 L_sx10_%=\n" \
"L_rx10_%=:\n" \
"buffer_load_dwordx4 v[204:207], v20, s[56:59], s71 offen offset:1024 sc1\n" \
"s_waitcnt vmcnt(21)\n" \
"v_mfma_f32_16x16x32_bf16 v[168:171], v[50:53], v[116:119], v[168:171]\n" \
"v_mfma_f32_16x16x32_bf16 v[172:175], v[48:51], v[116:119], v[172:175]\n" \
"s_waitcnt vmcnt(20)\n" \
"v_mfma_f32_16x16x32_bf16 v[168:171], v[50:53], v[124:127], v[168:171]\n" \
"v_mfma_f32_16x16x32_bf16 v[172:175], v[48:51], v[124:127], v[172:175]\n" \
"s_waitcnt vmcnt(19)\n" \
"v_mfma_f32_16x16x32_bf16 v[168:171], v[50:53], v[132:135], v[168:171]\n" \
"v_mfma_f32_16x16x32_bf16 v[172:175], v[48:51], v[132:135], v[172:175]\n" \
"s_waitcnt vmcnt(18)\n" \
"v_mfma_f32_16x16x32_bf16 v[56:59], v[50:53], v[136:139], v[56:59]\n" \
"v_mfma_f32_16x16x32_bf16 v[60:63], v[48:51], v[136:139], v[60:63]\n" \
"s_waitcnt vmcnt(17)\n" \
"v_mfma_f32_16x16x32_bf16 v[56:59], v[50:53], v[208:211], v[56:59]\n" \
"v_mfma_f32_16x16x32_bf16 v[60:63], v[48:51], v[208:211], v[60:63]\n" \
"s_waitcnt vmcnt(16)\n" \
"v_mfma_f32_16x16x32_bf16 v[56:59], v[50:53], v[144:147], v[56:59]\n" \
"v_mfma_f32_16x16x32_bf16 v[60:63], v[48:51], v[144:147], v[60:63]\n" \
"s_waitcnt vmcnt(15)\n" \
"v_mfma_f32_16x16x32_bf16 v[56:59], v[50:53], v[216:219], v[56:59]\n" \
"v_mfma_f32_16x16x32_bf16 v[60:63], v[48:51], v[216:219], v[60:63]\n" \
"s_waitcnt vmcnt(14)\n" \
"v_mfma_f32_16x16x32_bf16 v[56:59], v[50:53], v[152:155], v[56:59]\n" \
"v_mfma_f32_16x16x32_bf16 v[60:63], v[48:51], v[152:155], v[60:63]\n" \
"s_waitcnt vmcnt(13)\n" \
"v_mfma_f32_16x16x32_bf16 v[56:59], v[50:53], v[224:227], v[56:59]\n" \
"v_mfma_f32_16x16x32_bf16 v[60:63], v[48:51], v[224:227], v[60:63]\n" \
"s_waitcnt vmcnt(12)\n" \
"v_mfma_f32_16x16x32_bf16 v[56:59], v[50:53], v[176:179], v[56:59]\n" \
"v_mfma_f32_16x16x32_bf16 v[60:63], v[48:51], v[176:179], v[60:63]\n" \
"s_waitcnt vmcnt(11)\n" \
"v_mfma_f32_16x16x32_bf16 v[56:59], v[50:53], v[248:251], v[56:59]\n" \
"v_mfma_f32_16x16x32_bf16 v[60:63], v[48:51], v[248:251], v[60:63]\n" \
"s_waitcnt vmcnt(10)\n" \
"v_mfma_f32_16x16x32_bf16 v[168:171], v[50:53], v[140:143], v[168:171]\n" \
"v_mfma_f32_16x16x32_bf16 v[172:175], v[48:51], v[140:143], v[172:175]\n" \
"s_waitcnt vmcnt(9)\n" \
"v_mfma_f32_16x16x32_bf16 v[240:243], v[50:53], v[212:215], v[240:243]\n" \
"v_mfma_f32_16x16x32_bf16 v[244:247], v[48:51], v[212:215], v[244:247]\n" \
"s_waitcnt vmcnt(8)\n" \
"v_mfma_f32_16x16x32_bf16 v[168:171], v[50:53], v[148:151], v[168:171]\n" \
"v_cvt_pk_bf16_f32 v56, v56, v57\n" \
"v_cvt_pk_bf16_f32 v57, v58, v59\n" \
"v_mfma_f32_16x16x32_bf16 v[172:175], v[48:51], v[148:151], v[172:175]\n" \
"v_cvt_pk_bf16_f32 v58, v60, v61\n" \
"s_waitcnt vmcnt(7)\n" \
"v_mfma_f32_16x16x32_bf16 v[240:243], v[50:53], v[220:223], v[240:243]\n" \
"v_permlane16_swap_b32_e32 v56, v58\n" \
"v_mfma_f32_16x16x32_bf16 v[244:247], v[48:51], v[220:223], v[244:247]\n" \
"v_cvt_pk_bf16_f32 v59, v62, v63\n" \
"s_waitcnt vmcnt(6)\n" \
"v_mfma_f32_16x16x32_bf16 v[168:171], v[50:53], v[156:159], v[168:171]\n" \
"v_permlane16_swap_b32_e32 v57, v59\n" \
"buffer_store_dwordx4 v[56:59], v42, s[88:91], s73 offen sc0 sc1\n" \
"v_mfma_f32_16x16x32_bf16 v[172:175], v[48:51], v[156:159], v[172:175]\n" \
"s_waitcnt vmcnt(6)\n" \
"v_mfma_f32_16x16x32_bf16 v[240:243], v[50:53], v[228:231], v[240:243]\n" \
"v_mfma_f32_16x16x32_bf16 v[244:247], v[48:51], v[228:231], v[244:247]\n" \
"s_waitcnt vmcnt(5)\n" \
"v_mfma_f32_16x16x32_bf16 v[168:171], v[50:53], v[164:167], v[168:171]\n" \
"v_mfma_f32_16x16x32_bf16 v[172:175], v[48:51], v[164:167], v[172:175]\n" \
"s_waitcnt vmcnt(4)\n" \
"v_mfma_f32_16x16x32_bf16 v[240:243], v[50:53], v[236:239], v[240:243]\n" \
"v_mfma_f32_16x16x32_bf16 v[244:247], v[48:51], v[236:239], v[244:247]\n" \
"s_waitcnt vmcnt(3)\n" \
"v_mfma_f32_16x16x32_bf16 v[240:243], v[50:53], v[188:191], v[240:243]\n" \
"v_mfma_f32_16x16x32_bf16 v[244:247], v[48:51], v[188:191], v[244:247]\n" \
"s_waitcnt vmcnt(2)\n" \
"v_mfma_f32_16x16x32_bf16 v[240:243], v[50:53], v[196:199], v[240:243]\n" \
"v_cvt_pk_bf16_f32 v168, v168, v169\n" \
"v_cvt_pk_bf16_f32 v169, v170, v171\n" \
"v_mfma_f32_16x16x32_bf16 v[244:247], v[48:51], v[196:199], v[244:247]\n" \
"v_cvt_pk_bf16_f32 v170, v172, v173\n" \
"s_waitcnt vmcnt(1)\n" \
"v_mfma_f32_16x16x32_bf16 v[240:243], v[50:53], v[204:207], v[240:243]\n" \
"v_cvt_pk_bf16_f32 v171, v174, v175\n" \
"v_mfma_f32_16x16x32_bf16 v[244:247], v[48:51], v[204:207], v[244:247]\n" \
"v_permlane16_swap_b32_e32 v168, v170\n" \
"v_permlane16_swap_b32_e32 v169, v171\n" \
"buffer_store_dwordx4 v[168:171], v42, s[88:91], s92 offen sc0 sc1\n" \
"s_nop 2\n" \
"v_cvt_pk_bf16_f32 v240, v240, v241\n" \
"v_cvt_pk_bf16_f32 v241, v242, v243\n" \
"v_cvt_pk_bf16_f32 v242, v244, v245\n" \
"v_cvt_pk_bf16_f32 v243, v246, v247\n" \
"s_nop 0\n" \
"v_permlane16_swap_b32_e32 v240, v242\n" \
"v_permlane16_swap_b32_e32 v241, v243\n" \
"buffer_store_dwordx4 v[240:243], v42, s[88:91], s93 offen sc0 sc1\n"
#define ATT_VCLOB "v21","v22","v23","v32","v33","v34","v35","v36","v37","v38","v39","v40","v41","v42","v43","v44","v45","v46","v47","v48","v49","v50","v51","v52","v53","v54","v55","v56","v57","v58","v59","v60","v61","v62","v63","v64","v65","v66","v67","v68","v69","v70","v71","v72","v73","v74","v75","v76","v77","v78","v79","v80","v81","v82","v83","v84","v85","v86","v87","v88","v89","v90","v91","v92","v93","v94","v95","v96","v97","v98","v99","v100","v101","v102","v103","v104","v105","v106","v107","v108","v109","v110","v111","v112","v113","v114","v115","v116","v117","v118","v119","v120","v121","v122","v123","v124","v125","v126","v127","v128","v129","v130","v131","v132","v133","v134","v135","v136","v137","v138","v139","v140","v141","v142","v143","v144","v145","v146","v147","v148","v149","v150","v151","v152","v153","v154","v155","v156","v157","v158","v159","v160","v161","v162","v163","v164","v165","v166","v167","v168","v169","v170","v171","v172","v173","v174","v175","v176","v177","v178","v179","v180","v181","v182","v183","v184","v185","v186","v187","v188","v189","v190","v191","v192","v193","v194","v195","v196","v197","v198","v199","v200","v201","v202","v203","v204","v205","v206","v207","v208","v209","v210","v211","v212","v213","v214","v215","v216","v217","v218","v219","v220","v221","v222","v223","v224","v225","v226","v227","v228","v229","v230","v231","v232","v233","v234","v235","v236","v237","v238","v239","v240","v241","v242","v243","v244","v245","v246","v247","v248","v249","v250","v251","v252","v253","v254","v255"
#define ATT_SCLOB "s63","s64","s65","s66","s67","s68","s69","s70","s71","s72","s73","s74","s75","s76","s77","s78","s79","s80","s81","s82","s83","s84","s85","s86","s87","s88","s89","s90","s91","s92","s93","s95","s96","s97","s98","s99","s100","s101"
enum { T_fo, T_fa, T_fas, T_fb, T_fbs, T_f8, T_mk8, T_wol, T_woh, T_wbl, T_wbh, T_outl, T_outh, T_r1l, T_r1h, T_r1n, T_r1s, T_m0, T_m1, T_mw, T_m4, T_ml, T_w0, T_w1, T_w2, T_w3, T_A1, T_A2, T_A3, T_B1, T_B2, T_B3, T_Ao, T_Bo, T_Al, T_Bl, T_l8, T_xr, T_x8r, T_oA, T_oB, T_o8, T_x0, T_x1, T_x2, T_x3, T_x4, T_x5, T_x6, T_x8, T_ep, T_xf, T_kf };

__device__ __forceinline__ void out_consts(u32x4 (&vp)[3], const u16* wo, const u16* wob, const u16* out, const unsigned b, const unsigned w, const unsigned lane) {
    const unsigned sp = b & 7u, nt = (b >> 3) & 3u, j = b >> 5, head = 4u * sp + nt, OOR = 0x40000000u;
    unsigned vw[9];
#pragma unroll
    for (unsigned i = 0; i < 9u; ++i) {
        const unsigned q = 64u * i + lane, row = __umul24(q, 3641u) >> 16, c = q - __umul24(row, 18u);
        vw[i] = __umul24(row, DM * 2) + (c < 16u ? (c * 16u + sp * 32u + 32u) & 255u : c * 16u) - (i & 3u) * 1024u;
    }
    const unsigned row = 16u * w + (lane >> 2), r9 = __umul24(16u * (row >> 5) + (row & 12u) + 2u * ((row >> 4) & 1u) + ((row >> 1) & 1u), 18u) + 16u + (row & 1u);
    unsigned vwl = __umul24(r9, DM * 2) + ((lane & 2u) ? 256u : sp * 32u) + (lane & 1u) * 16u, vrd = __umul24(lane >> 2, 288u) + (lane & 3u) * 8u;
    asm volatile("" : "+v"(vw[0]), "+v"(vw[1]), "+v"(vw[2]), "+v"(vw[3]), "+v"(vw[4]), "+v"(vw[5]), "+v"(vw[6]), "+v"(vw[7]), "+v"(vw[8]), "+v"(vwl), "+v"(vrd));
    unsigned tab = 0;
#define TB(n, v) asm("v_writelane_b32 %0, %1, %2" : "+v"(tab) : "s"((unsigned)(v)), "n"((int)T_##n))
    const unsigned FJ = (unsigned)WS_F + (F_J + 640u * j) * 4u, mt = 2u * (w & 2u) + (w & 1u), w2 = w < 2u ? w : 2u, hw = head * 147456u;
    const unsigned FR = (unsigned)WS_F + F_R * 4u + (8u * j + sp) * 1024u;
    TB(fo, FJ + 4u * head); TB(fa, FR + (w | 2u) * 256u); TB(fas, FR + w * 256u + nt * 4u);
    TB(fb, FR + 128u + w * 256u); TB(fbs, FR + 128u + w * 256u + nt * 4u);
    TB(ep, b + w ? OOR : (unsigned)WS_F + F_EPOCH * 4u);
    const u16* pwo = wo + nt * 144u; const u16* pwb = wob + nt * 144u; const u16* pr1 = w < 3u ? pwo : pwb;
    TB(wol, PLO(pwo)); TB(woh, PHI(pwo) & 0xffffu); TB(wbl, PLO(pwb)); TB(wbh, PHI(pwb) & 0xffffu); TB(outl, PLO(out)); TB(outh, PHI(out) & 0xffffu); TB(r1l, PLO(pr1)); TB(r1h, PHI(pr1) & 0xffffu);
    TB(r1n, (w < 3u ? 72u * sp + 71u : 4095u) * 1152u + 288u); TB(r1s, w < 3u ? (72u * sp + 32u * w) * 1152u : hw + 110592u);
    TB(m0, 131072u + 9216u * w2); TB(w0, hw + 36864u * w2); TB(m1, w < 3u ? 9216u * w : 73728u); TB(mw, 9216u * w); TB(m4, w < 3u ? 131072u + 9216u * w : 64512u); TB(ml, 119808u + 1024u * w);
    const unsigned tA = (unsigned)WS_AF + (8u * j + mt) * (KS_TOT * 1024u), tB = (unsigned)WS_AF + (120u - 8u * j + mt) * (KS_TOT * 1024u);
#define HK(k) (4u * sp + ((nt + k) & 3u))
    TB(w1, HK(1u) * 147456u + w * 36864u); TB(w2, HK(2u) * 147456u + w * 36864u); TB(w3, HK(3u) * 147456u + w * 36864u);
    TB(A1, tA + 18432u + HK(1u) * 4096u); TB(A2, tA + 18432u + HK(2u) * 4096u); TB(A3, tA + 18432u + HK(3u) * 4096u);
    TB(B1, tB + 18432u + HK(1u) * 4096u); TB(B2, tB + 18432u + HK(2u) * 4096u); TB(B3, tB + 18432u + HK(3u) * 4096u);
    TB(Ao, tA + sp * 2048u); TB(Bo, tB + sp * 2048u); TB(Al, tA + 16384u); TB(Bl, tB + 16384u);
    const bool red = w == (sp >> 1), blk = sp & 1u;
    const unsigned xg = (unsigned)WS_X + (4u * j + nt) * 589824u + w * 16384u, oA = (128u * j + 16u * mt) * 1152u + nt * 288u, oB = (1920u - 128u * j + 16u * mt) * 1152u + nt * 288u;
    TB(l8, red ? (blk ? tB : tA) + 16384u : OOR); TB(xr, xg + sp * 65536u); TB(x8r, red ? xg + 524288u + (sp & 1u) * 1024u : OOR);
    TB(oA, oA + sp * 32u); TB(oB, oB + sp * 32u); TB(o8, red ? (blk ? oB : oA) + 256u : OOR); TB(x8, xg + 524288u + sp * 2048u);
    TB(f8, xg + w * 131072u + 14400u + sp * 4u); TB(mk8, red ? 0xff0000u : 0u); TB(xf, xg + sp * 65536u + 14336u);
    TB(kf, (unsigned)WS_F + (F_KV + head * 64u) * 4u);
#define XI(i) TB(x##i, xg + ((sp + 1u + i) & 7u) * 65536u + i * 2048u)
    XI(0); XI(1); XI(2); XI(3); XI(4); XI(5); XI(6);
#undef XI
#undef HK
#undef TB
    vp[0] = (u32x4){vw[0], vw[1], vw[2], vw[3]}; vp[1] = (u32x4){vw[4], vw[5], vw[6], vw[7]}; vp[2] = (u32x4){vw[8], vwl, vrd, tab};
}
__global__ __launch_bounds__(256, 1) __attribute__((aligned(4096))) void fused_kernel(const u16* __restrict__ x, const u16* __restrict__ ckv, const u16* __restrict__ wq, const u16* __restrict__ wuk,
                                                       const u16* __restrict__ wuv, const u16* __restrict__ wqkv, const u16* __restrict__ gw,
                                                       const u16* __restrict__ wo, const u16* __restrict__ wob, char* __restrict__ ws, u16* __restrict__ out) {
    using namespace att;
    extern __shared__ __attribute__((aligned(16))) char lds[];
    unsigned wid = rfl(threadIdx.x >> 6);
    code_touch((blockIdx.x >> 3) * 4u + wid, (unsigned)(size_t)lds + 151552u + wid * 512u);
    SBAR();
    asm volatile("" :: "s"(x), "s"(ckv), "s"(wq), "s"(wuk), "s"(wuv), "s"(wqkv), "s"(gw), "s"(wo), "s"(wob), "s"(ws), "s"(out));
    unsigned tag;
    int b = blockIdx.x;
    u32x4 vp[5], sa[5]; unsigned sd[2];
    const unsigned vl = proj_phase(x, ckv, wq, wuk, wuv, wqkv, gw, wo, wob, out, ws, lds, wid, tag, vp, sa, sd);
    asm volatile("" : "+s"(ws));
    unsigned* const flags = (unsigned*)(ws + WS_F);
    RTS(2);
#define AST(L) do {} while (0)
#define SD(J8, H, W) ((unsigned)WS_AF + (((J8) + 2u * ((W) & 2u) + ((W) & 1u)) * KS_TOT + 18u + 4u * (H)) * 1024u)
#define SRDA (u32x4){PLO(ws), PHI(ws) & 0xffffu, (unsigned)WS_L, 0x00020000u}
#define ATT_TAIL "s_endpgm\n"
    unsigned abad;
    asm volatile(ATT_ASM ATT_TAIL "L_end_%=:\n s_nop 1"
                 : "=&{s94}"(abad)
                 : "{v[0:3]}"(vp[0]), "{v[4:7]}"(vp[1]), "{v[8:11]}"(vp[2]), "{v[12:15]}"(vp[3]), "{v[16:19]}"(vp[4]), "{v20}"(vl), "{s[56:59]}"(sa[4]), "{s60}"(sd[0]), "{s61}"(sd[1]), "{s62}"(tag),
                   "{s[8:11]}"(sa[0]), "{s[12:15]}"(sa[1]), "{s[16:19]}"(sa[2]), "{s[20:23]}"(sa[3])
                 : "memory", "m0", "vcc", "scc", ATT_VCLOB, ATT_SCLOB, ACLOB_LO, ACLOB_HI);
    {
        const u16* const __attribute__((address_space(4)))* ka = (const u16* const __attribute__((address_space(4)))*)__builtin_amdgcn_kernarg_segment_ptr();
        unsigned b2 = b, w2 = wid;
        asm volatile("s_waitcnt vmcnt(0)" : "+s"(ka), "+s"(b2), "+s"(w2), "+s"(ws) :: "memory");
        const unsigned j2 = b2 >> 5, p2 = w2 & 1u, h2 = (b2 & 7u) * 4u + ((b2 >> 3) & 3u);
#pragma unroll 1
        for (unsigned i = w2 & 2u; i < ((w2 & 2u) ? (abad ? 8u : 4u) : 2u); ++i)
            att_exact(ka[0], ka[2], ws, h2, (i < 4u ? 1920u - 128u * j2 : 128u * j2 - 128u) + 32u * i + 16u * p2, *reinterpret_cast<const float*>(lds + LDS_G + (32u * i + 16u * p2 + (lane_id() & 15u)) * 4u));
        asm volatile("s_waitcnt vmcnt(0)" ::: "memory");
        unsigned* fa = (unsigned*)(ws + WS_F) + F_R + (8u * j2 + (b2 & 7u)) * 256u + (2u + p2) * 64u + ((b2 >> 3) & 3u);
        if (w2 & 2u) st_flag(fa, tag);
        wait_flags(fa, ld_flag(fa), tag);
        const unsigned l2 = lane_id(); u32x4 c[3]; out_consts(c, ka[7], ka[8], ka[10], b2, w2, l2);
        asm volatile("s_waitcnt vmcnt(0) lgkmcnt(0)\n\ts_nop 7\n\ts_branch L_att_stub" :: "{v[0:3]}"(c[0]), "{v[4:7]}"(c[1]), "{v[8:11]}"(c[2]), "{v20}"(l2 * 16u), "{s[56:59]}"(SRDA), "{s60}"(SD(8u * j2, h2, w2)),
                     "{s61}"(SD(120u - 8u * j2, h2, w2)), "{s62}"(tag) : "memory");
    }
    RTS(5);
}

extern "C" void kernel_launch(void* const* d_in, const int* in_sizes, int n_in, void* d_out, int out_size, void* d_ws, size_t ws_size,
                              hipStream_t stream) {
    (void)hipFuncSetAttribute((const void*)fused_kernel, hipFuncAttributeMaxDynamicSharedMemorySize, att::LDS_BYTES);
    fused_kernel<<<dim3(256), 256, att::LDS_BYTES, stream>>>((const u16*)d_in[0], (const u16*)d_in[4], (const u16*)d_in[5], (const u16*)d_in[6], (const u16*)d_in[7], (const u16*)d_in[1],
                                                             (const u16*)d_in[9], (const u16*)d_in[2], (const u16*)d_in[8], (char*)d_ws, (u16*)d_out);
}
```

```cpp
#include <hip/hip_runtime.h>
#include <hip/hip_bf16.h>
#include <stdint.h>
#include <utility>

typedef short bf16x8 __attribute__((ext_vector_type(8)));
typedef short s16x4 __attribute__((ext_vector_type(4)));
typedef float f32x16 __attribute__((ext_vector_type(16)));
typedef float f32x4 __attribute__((ext_vector_type(4)));
typedef unsigned u32x4 __attribute__((ext_vector_type(4)));
typedef unsigned short u16;

constexpr int S = 2048, DM = 576, NH = 32, HD = 128, DC = 512, NQKV = 1728, DHA = 18;
constexpr int NBIG = NH * HD;
constexpr int KS_TOT = 146;

#define SBAR() __builtin_amdgcn_sched_barrier(0)
constexpr size_t WS_Q = 0, WS_K = WS_Q + (size_t)S * NBIG * 2, WS_V = WS_K + (size_t)S * NBIG * 2, WS_AF = WS_V + (size_t)S * NBIG * 2, WS_X = WS_AF + (size_t)128 * KS_TOT * 1024,
                 WS_P = WS_X + (size_t)64 * 16 * 9216, WS_F = WS_P + (size_t)128 * (4 * 32 * 256 + 1024) * 4;
constexpr size_t WS_L = (size_t)96 << 20;
static_assert(WS_F == 96206848, "workspace map is baked into the generated asm");
constexpr int F_EPOCH = 0, F_J = 1024, F_ST = 8192, F_KV = 40960, F_R = 65536;
constexpr unsigned TAG_STEP = 0x9E3779B1u;
__device__ __forceinline__ unsigned ld_flag(const unsigned* p) { return __hip_atomic_load(p, __ATOMIC_RELAXED, __HIP_MEMORY_SCOPE_AGENT); }
__device__ __forceinline__ void st_flag(unsigned* p, unsigned v) { __hip_atomic_store(p, v, __ATOMIC_RELAXED, __HIP_MEMORY_SCOPE_AGENT); }
__device__ __forceinline__ unsigned ld_flag_async(const unsigned* p) { unsigned v; asm volatile("global_load_dword %0, %1, off sc1" : "=v"(v) : "v"(p) : "memory"); return v; }
#define WAITV(v, N) asm volatile("s_waitcnt vmcnt(" #N ")" : "+v"(v) :: "memory")
__device__ __forceinline__ void wait_flags(const unsigned* p, unsigned v, unsigned tag) {
    if (__builtin_expect(__builtin_amdgcn_ballot_w64(v != tag) != 0ull, 0)) {
        unsigned spins = 0;
        do { __builtin_amdgcn_s_sleep(1); } while (__builtin_amdgcn_ballot_w64(ld_flag(p) != tag) != 0ull && ++spins < (1u << 22));
    }
    asm volatile("" ::: "memory");
}

__device__ __forceinline__ unsigned cvtpk(float lo, float hi) {
    unsigned r; asm volatile("v_cvt_pk_bf16_f32 %0, %1, %2" : "=v"(r) : "v"(lo), "v"(hi)); return r;
}
__device__ __forceinline__ float bf2f(u16 v) { return __uint_as_float(((unsigned)v) << 16); }
__device__ __forceinline__ int crow(int r, int hi) { return (r & 3) + 8 * (r >> 2) + 4 * hi; }

typedef __attribute__((address_space(3))) s16x4* lds_s16x4_p;
__device__ __forceinline__ s16x4 trrd(unsigned addr) { return __builtin_amdgcn_ds_read_tr16_b64_v4i16((lds_s16x4_p)addr); }

__device__ __forceinline__ unsigned lane_id() { unsigned l; asm volatile("v_mbcnt_lo_u32_b32 %0, -1, 0\n\tv_mbcnt_hi_u32_b32 %0, -1, %0" : "=v"(l)); return l; }
#define RTS(slot) do {} while (0)
#define CT_L 5u
__device__ __forceinline__ void code_touch(unsigned wi, unsigned lds_scratch) {
    unsigned long long pc; asm volatile("s_getpc_b64 %0" : "=s"(pc));
    asm volatile("s_mov_b32 m0, %0\n\ts_mov_b64 exec, %3\n\tglobal_load_lds_dword %1, %2\n\ts_mov_b64 exec, -1" :: "s"(lds_scratch), "v"(((threadIdx.x & 63u) * 128u + wi) * 128u), "s"(pc & ~127ull), "n"((1u << CT_L) - 1u) : "m0", "memory");
}
#define ACLOB_LO "a0","a1","a2","a3","a4","a5","a6","a7","a8","a9","a10","a11","a12","a13","a14","a15","a16","a17","a18","a19","a20","a21","a22","a23","a24","a25","a26","a27","a28","a29","a30","a31","a32","a33","a34","a35","a36","a37","a38","a39","a40","a41","a42","a43","a44","a45","a46","a47","a48","a49","a50","a51","a52","a53","a54","a55","a56","a57","a58","a59","a60","a61","a62","a63","a64","a65","a66","a67","a68","a69","a70","a71","a72","a73","a74","a75","a76","a77","a78","a79","a80","a81","a82","a83","a84","a85","a86","a87","a88","a89","a90","a91","a92","a93","a94","a95","a96","a97","a98","a99","a100","a101","a102","a103","a104","a105","a106","a107","a108","a109","a110","a111","a112","a113","a114","a115","a116","a117","a118","a119","a120","a121","a122","a123","a124","a125","a126","a127"
#define ACLOB_HI "a128","a129","a130","a131","a132","a133","a134","a135","a136","a137","a138","a139","a140","a141","a142","a143","a144","a145","a146","a147","a148","a149","a150","a151","a152","a153","a154","a155","a156","a157","a158","a159","a160","a161","a162","a163","a164","a165","a166","a167","a168","a169","a170","a171","a172","a173","a174","a175","a176","a177","a178","a179","a180","a181","a182","a183","a184","a185","a186","a187","a188","a189","a190","a191","a192","a193","a194","a195","a196","a197","a198","a199","a200","a201","a202","a203","a204","a205","a206","a207","a208","a209","a210","a211","a212","a213","a214","a215","a216","a217","a218","a219","a220","a221","a222","a223","a224","a225","a226","a227","a228","a229","a230","a231","a232","a233","a234","a235","a236","a237","a238","a239","a240","a241","a242","a243","a244","a245","a246","a247","a248","a249","a250","a251","a252","a253","a254","a255"
#define PROJ_PRE0 \
"s_add_u32 s62, %[rc], 0x0\n" \
"s_and_b32 s62, s62, 0x3ff\n" \
"s_lshl_b32 s63, s62, 12\n" \
"s_add_u32 m0, %[mA], 0x0\n" \
"s_add_u32 s85, s62, 0x0\n" \
"buffer_load_dwordx4 v22, s[64:67], s85 offen lds\n" \
"s_add_u32 m0, %[mA], 0x400\n" \
"s_add_u32 s84, s62, 0x2000\n" \
"buffer_load_dwordx4 v23, s[64:67], s84 offen lds\n" \
"s_add_u32 m0, %[mB], 0x0\n" \
"s_add_u32 s85, s63, 0x0\n" \
"buffer_load_dwordx4 v26, s[68:71], s85 offen lds\n" \
"s_add_u32 m0, %[mB], 0x4000\n" \
"s_add_u32 s84, s63, 0x0\n" \
"buffer_load_dwordx4 v26, s[72:75], s84 offen lds\n" \
"s_add_u32 m0, %[mA], 0x800\n" \
"s_add_u32 s85, s62, 0x4000\n" \
"buffer_load_dwordx4 v22, s[64:67], s85 offen lds\n" \
"s_add_u32 m0, %[mA], 0xc00\n" \
"s_add_u32 s84, s62, 0x6000\n" \
"buffer_load_dwordx4 v23, s[64:67], s84 offen lds\n" \
"s_add_u32 m0, %[mB], 0x400\n" \
"s_add_u32 s85, s63, 0x8000\n" \
"buffer_load_dwordx4 v26, s[68:71], s85 offen lds\n" \
"s_add_u32 m0, %[mB], 0x4400\n" \
"s_add_u32 s84, s63, 0x8000\n" \
"buffer_load_dwordx4 v26, s[72:75], s84 offen lds\n"
#define PROJ_PRE1 \
"s_add_u32 s62, %[rc], 0x0\n" \
"s_and_b32 s62, s62, 0x3ff\n" \
"s_lshl_b32 s63, s62, 12\n" \
"s_add_u32 m0, %[mA], 0x1000\n" \
"s_add_u32 s85, s62, 0x8000\n" \
"buffer_load_dwordx4 v22, s[64:67], s85 offen lds\n" \
"s_add_u32 m0, %[mA], 0x1400\n" \
"s_add_u32 s84, s62, 0xa000\n" \
"buffer_load_dwordx4 v23, s[64:67], s84 offen lds\n" \
"s_add_u32 m0, %[mB], 0x800\n" \
"s_add_u32 s85, s63, 0x10000\n" \
"buffer_load_dwordx4 v27, s[68:71], s85 offen lds\n" \
"s_add_u32 m0, %[mB], 0x4800\n" \
"s_add_u32 s84, s63, 0x10000\n" \
"buffer_load_dwordx4 v27, s[72:75], s84 offen lds\n" \
"s_add_u32 m0, %[mA], 0x1800\n" \
"s_add_u32 s85, s62, 0xc000\n" \
"buffer_load_dwordx4 v22, s[64:67], s85 offen lds\n" \
"s_add_u32 m0, %[mA], 0x1c00\n" \
"s_add_u32 s84, s62, 0xe000\n" \
"buffer_load_dwordx4 v23, s[64:67], s84 offen lds\n" \
"s_add_u32 m0, %[mB], 0xc00\n" \
"s_add_u32 s85, s63, 0x18000\n" \
"buffer_load_dwordx4 v27, s[68:71], s85 offen lds\n" \
"s_add_u32 m0, %[mB], 0x4c00\n" \
"s_add_u32 s84, s63, 0x18000\n" \
"buffer_load_dwordx4 v27, s[72:75], s84 offen lds\n"
#define PROJ_PRE2 \
"s_add_u32 s62, %[rc], 0x80\n" \
"s_and_b32 s62, s62, 0x3ff\n" \
"s_lshl_b32 s63, s62, 12\n" \
"s_add_u32 m0, %[mA], 0x8000\n" \
"s_add_u32 s85, s62, 0x0\n" \
"buffer_load_dwordx4 v22, s[64:67], s85 offen lds\n" \
"s_add_u32 m0, %[mA], 0x8400\n" \
"s_add_u32 s84, s62, 0x2000\n" \
"buffer_load_dwordx4 v23, s[64:67], s84 offen lds\n"
#define PROJ_PRE3 \
"s_add_u32 s62, %[rc], 0x80\n" \
"s_and_b32 s62, s62, 0x3ff\n" \
"s_lshl_b32 s63, s62, 12\n" \
"s_add_u32 m0, %[mB], 0x8000\n" \
"s_add_u32 s85, s63, 0x0\n" \
"buffer_load_dwordx4 v26, s[68:71], s85 offen lds\n" \
"s_add_u32 m0, %[mB], 0xc000\n" \
"s_add_u32 s84, s63, 0x0\n" \
"buffer_load_dwordx4 v26, s[72:75], s84 offen lds\n" \
"s_add_u32 m0, %[mA], 0x8800\n" \
"s_add_u32 s85, s62, 0x4000\n" \
"buffer_load_dwordx4 v22, s[64:67], s85 offen lds\n" \
"s_add_u32 m0, %[mA], 0x8c00\n" \
"s_add_u32 s84, s62, 0x6000\n" \
"buffer_load_dwordx4 v23, s[64:67], s84 offen lds\n" \
"s_add_u32 m0, %[mB], 0x8400\n" \
"s_add_u32 s85, s63, 0x8000\n" \
"buffer_load_dwordx4 v26, s[68:71], s85 offen lds\n" \
"s_add_u32 m0, %[mB], 0xc400\n" \
"s_add_u32 s84, s63, 0x8000\n" \
"buffer_load_dwordx4 v26, s[72:75], s84 offen lds\n" \
"s_add_u32 m0, %[mA], 0x9000\n" \
"s_add_u32 s85, s62, 0x8000\n" \
"buffer_load_dwordx4 v22, s[64:67], s85 offen lds\n" \
"s_add_u32 m0, %[mA], 0x9400\n" \
"s_add_u32 s84, s62, 0xa000\n" \
"buffer_load_dwordx4 v23, s[64:67], s84 offen lds\n"
#define PROJ_TKE 0
#define PROJ_T0 0
#define PROJ_ROT 2*(sp+mt)
#define PROJ_TC 0
#define PROJ_ASM \
".p2align 3\n" \
"s_branch L_go_%=\n" \
"L_pl_%=:\n" \
"s_mov_b32 s63, 0\n" \
"L_ql_%=:\n" \
"s_sleep 1\n" \
"v_bfe_u32 v44, v20, 2, 5\n" \
"buffer_load_dword v44, v44, s[80:83], s39 offen offset:64 sc1\n" \
"s_waitcnt vmcnt(0)\n" \
"v_cmp_ne_u32_e32 vcc, %[tag], v44\n" \
"s_add_u32 s63, s63, 1\n" \
"s_nop 3\n" \
"s_cbranch_vccz L_rl_%=\n" \
"s_cmp_gt_u32 s63, 0x400000\n" \
"s_cbranch_scc0 L_ql_%=\n" \
"s_branch L_rl_%=\n" \
"L_pa_%=:\n" \
"s_mov_b32 s63, 0\n" \
"L_qa_%=:\n" \
"s_sleep 1\n" \
"v_bfe_u32 v44, v20, 2, 5\n" \
"buffer_load_dword v44, v44, s[80:83], s39 offen offset:0 sc1\n" \
"s_waitcnt vmcnt(0)\n" \
"v_cmp_ne_u32_e32 vcc, %[tag], v44\n" \
"s_add_u32 s63, s63, 1\n" \
"s_nop 3\n" \
"s_cbranch_vccz L_ra_%=\n" \
"s_cmp_gt_u32 s63, 0x400000\n" \
"s_cbranch_scc0 L_qa_%=\n" \
"s_branch L_ra_%=\n" \
"L_go_%=:\n" \
"v_xor_b32_e32 v33, 0x20, v32\n" \
"s_mov_b32 s80, %[wsl]\n" \
"s_and_b32 s81, %[wsh], 0xffff\n" \
"s_mov_b32 s82, 0x10000000\n" \
"s_mov_b32 s83, 0x20000\n" \
"s_mov_b32 s84, 0x5bc0000\n" \
"s_add_u32 s62, s22, 0x80\n" \
"buffer_load_dword v180, off, s[80:83], s84 sc1\n" \
"s_and_b32 s62, s62, 0x3ff\n" \
"s_lshl_b32 s63, s62, 12\n" \
"v_xor_b32_e64 v34, 0x40, v32\n" \
"v_xor_b32_e32 v35, 0x60, v32\n" \
"s_waitcnt vmcnt(11)\n" \
"s_barrier\n" \
"ds_read_b64_tr_b16 v[120:121], v32 offset:8192\n" \
"ds_read_b64_tr_b16 v[122:123], v32 offset:9216\n" \
"ds_read_b128 v[56:59], v29 offset:16384\n" \
"ds_read_b64_tr_b16 v[116:117], v32 offset:0\n" \
"ds_read_b64_tr_b16 v[118:119], v32 offset:1024\n" \
"ds_read_b128 v[52:55], v28 offset:16384\n" \
"s_add_u32 m0, %[mB], 0x8800\n" \
"s_add_u32 s85, s63, 0x10000\n" \
"s_mov_b32 s76, s12\n" \
"ds_read_b128 v[60:63], v28 offset:18432\n" \
"s_and_b32 s77, s13, 0xffff\n" \
"s_mov_b32 s78, 0x1e6000\n" \
"s_mov_b32 s79, 0x20000\n" \
"ds_read_b128 v[64:67], v29 offset:18432\n" \
"s_waitcnt lgkmcnt(5)\n" \
"v_mfma_f32_16x16x32_bf16 a[0:3], v[120:123], v[56:59], 0\n" \
"ds_read_b128 v[72:75], v29 offset:20480\n" \
"ds_read_b128 v[68:71], v28 offset:20480\n" \
"ds_read_b128 v[76:79], v28 offset:22528\n" \
"buffer_load_dwordx4 v27, s[68:71], s85 offen lds\n" \
"ds_read_b128 v[80:83], v29 offset:22528\n" \
"s_waitcnt lgkmcnt(6)\n" \
"v_mfma_f32_16x16x32_bf16 a[0:3], v[116:119], v[52:55], a[0:3]\n" \
"ds_read_b64_tr_b16 v[128:129], v33 offset:8192\n" \
"s_waitcnt lgkmcnt(6)\n" \
"v_mfma_f32_16x16x32_bf16 a[32:35], v[116:119], v[60:63], 0\n" \
"ds_read_b64_tr_b16 v[130:131], v33 offset:9216\n" \
"ds_read_b64_tr_b16 v[124:125], v33 offset:0\n" \
"ds_read_b64_tr_b16 v[126:127], v33 offset:1024\n" \
"s_waitcnt lgkmcnt(8)\n" \
"s_nop 0\n" \
"v_mfma_f32_16x16x32_bf16 a[32:35], v[120:123], v[64:67], a[32:35]\n" \
"ds_read_b64_tr_b16 v[136:137], v34 offset:8192\n" \
"ds_read_b64_tr_b16 v[138:139], v34 offset:9216\n" \
"ds_read_b64_tr_b16 v[132:133], v34 offset:0\n" \
"s_add_u32 m0, %[mB], 0xc800\n" \
"ds_read_b64_tr_b16 v[134:135], v34 offset:1024\n" \
"s_add_u32 s84, s63, 0x10000\n" \
"buffer_load_dwordx4 v27, s[72:75], s84 offen lds\n" \
"ds_read_b64_tr_b16 v[144:145], v35 offset:8192\n" \
"ds_read_b64_tr_b16 v[146:147], v35 offset:9216\n" \
"ds_read_b64_tr_b16 v[140:141], v35 offset:0\n" \
"s_lshr_b32 s95, %[b], 3\n" \
"ds_read_b64_tr_b16 v[142:143], v35 offset:1024\n" \
"s_lshl_b32 s96, s95, 2\n" \
"ds_read_b64_tr_b16 v[152:153], v32 offset:24576\n" \
"s_add_u32 s97, s96, %[w]\n" \
"ds_read_b64_tr_b16 v[154:155], v32 offset:25600\n" \
"v_lshrrev_b32_e32 v212, 4, v20\n" \
"s_mul_i32 s98, 27, s97\n" \
"v_min_u32_e32 v213, 26, v212\n" \
"v_add_u32_e64 v214, s98, v213\n" \
"v_mul_u32_u24_e32 v215, 0xaaab, v214\n" \
"s_waitcnt lgkmcnt(15)\n" \
"v_mfma_f32_16x16x32_bf16 a[64:67], v[120:123], v[72:75], 0\n" \
"ds_read_b64_tr_b16 v[148:149], v32 offset:16384\n" \
"ds_read_b64_tr_b16 v[150:151], v32 offset:17408\n" \
"v_mfma_f32_16x16x32_bf16 a[64:67], v[116:119], v[68:71], a[64:67]\n" \
"s_add_u32 m0, %[mA], 0x9800\n" \
"s_add_u32 s85, s62, 0xc000\n" \
"v_mfma_f32_16x16x32_bf16 a[96:99], v[116:119], v[76:79], 0\n" \
"ds_read_b64_tr_b16 v[160:161], v33 offset:24576\n" \
"s_waitcnt lgkmcnt(15)\n" \
"v_mfma_f32_16x16x32_bf16 a[96:99], v[120:123], v[80:83], a[96:99]\n" \
"buffer_load_dwordx4 v22, s[64:67], s85 offen lds\n" \
"v_mfma_f32_16x16x32_bf16 a[4:7], v[128:131], v[56:59], 0\n" \
"s_add_u32 m0, %[mA], 0x9c00\n" \
"s_waitcnt lgkmcnt(13)\n" \
"s_nop 0\n" \
"v_mfma_f32_16x16x32_bf16 a[4:7], v[124:127], v[52:55], a[4:7]\n" \
"s_add_u32 s84, s62, 0xe000\n" \
"ds_read_b64_tr_b16 v[162:163], v33 offset:25600\n" \
"v_mfma_f32_16x16x32_bf16 a[36:39], v[124:127], v[60:63], 0\n" \
"buffer_load_dwordx4 v23, s[64:67], s84 offen lds\n" \
"v_mfma_f32_16x16x32_bf16 a[36:39], v[128:131], v[64:67], a[36:39]\n" \
"s_add_u32 m0, %[mB], 0x8c00\n" \
"s_add_u32 s85, s63, 0x18000\n" \
"v_mfma_f32_16x16x32_bf16 a[68:71], v[128:131], v[72:75], 0\n" \
"ds_read_b64_tr_b16 v[156:157], v33 offset:16384\n" \
"ds_read_b64_tr_b16 v[158:159], v33 offset:17408\n" \
"v_mfma_f32_16x16x32_bf16 a[68:71], v[124:127], v[68:71], a[68:71]\n" \
"buffer_load_dwordx4 v27, s[68:71], s85 offen lds\n" \
"v_mfma_f32_16x16x32_bf16 a[100:103], v[124:127], v[76:79], 0\n" \
"s_add_u32 m0, %[mB], 0xcc00\n" \
"s_add_u32 s84, s63, 0x18000\n" \
"v_mfma_f32_16x16x32_bf16 a[100:103], v[128:131], v[80:83], a[100:103]\n" \
"ds_read_b64_tr_b16 v[168:169], v34 offset:24576\n" \
"s_waitcnt lgkmcnt(7)\n" \
"v_mfma_f32_16x16x32_bf16 a[8:11], v[136:139], v[56:59], 0\n" \
"buffer_load_dwordx4 v27, s[72:75], s84 offen lds\n" \
"v_mfma_f32_16x16x32_bf16 a[8:11], v[132:135], v[52:55], a[8:11]\n" \
"ds_read_b64_tr_b16 v[170:171], v34 offset:25600\n" \
"ds_read_b64_tr_b16 v[164:165], v34 offset:16384\n" \
"v_mfma_f32_16x16x32_bf16 a[40:43], v[132:135], v[60:63], 0\n" \
"ds_read_b64_tr_b16 v[166:167], v34 offset:17408\n" \
"v_lshrrev_b32_e32 v216, 18, v215\n" \
"v_mfma_f32_16x16x32_bf16 a[40:43], v[136:139], v[64:67], a[40:43]\n" \
"v_mul_u32_u24_e32 v218, 6, v216\n" \
"v_sub_u32_e32 v219, v214, v218\n" \
"v_mfma_f32_16x16x32_bf16 a[72:75], v[136:139], v[72:75], 0\n" \
"v_lshrrev_b32_e32 v220, 1, v219\n" \
"v_mul_u32_u24_e32 v217, 27, v216\n" \
"v_mfma_f32_16x16x32_bf16 a[72:75], v[132:135], v[68:71], a[72:75]\n" \
"v_mul_u32_u24_e32 v221, 9, v220\n" \
"v_add_u32_e32 v222, v217, v221\n" \
"v_mfma_f32_16x16x32_bf16 a[104:107], v[132:135], v[76:79], 0\n" \
"v_and_b32_e32 v223, 1, v219\n" \
"s_and_b32 s94, 7, %[b]\n" \
"v_mfma_f32_16x16x32_bf16 a[104:107], v[136:139], v[80:83], a[104:107]\n" \
"ds_read_b64_tr_b16 v[176:177], v35 offset:24576\n" \
"v_add_u32_e64 v224, v222, v223\n" \
"v_mfma_f32_16x16x32_bf16 a[12:15], v[144:147], v[56:59], 0\n" \
"ds_read_b64_tr_b16 v[178:179], v35 offset:25600\n" \
"v_add_u32_e64 v225, s94, v224\n" \
"v_mfma_f32_16x16x32_bf16 a[12:15], v[140:143], v[52:55], a[12:15]\n" \
"ds_read_b64_tr_b16 v[172:173], v35 offset:16384\n" \
"v_lshlrev_b32_e32 v186, 7, v225\n" \
"v_mfma_f32_16x16x32_bf16 a[44:47], v[140:143], v[60:63], 0\n" \
"ds_read_b64_tr_b16 v[174:175], v35 offset:17408\n" \
"ds_read_b128 v[84:87], v28 offset:0\n" \
"v_mfma_f32_16x16x32_bf16 a[44:47], v[144:147], v[64:67], a[44:47]\n" \
"ds_read_b128 v[88:91], v29 offset:0\n" \
"ds_read_b128 v[92:95], v28 offset:2048\n" \
"v_mfma_f32_16x16x32_bf16 a[76:79], v[144:147], v[72:75], 0\n" \
"ds_read_b128 v[96:99], v29 offset:2048\n" \
"s_mul_i32 s89, 6, s97\n" \
"v_mfma_f32_16x16x32_bf16 a[76:79], v[140:143], v[68:71], a[76:79]\n" \
"ds_read_b128 v[100:103], v28 offset:4096\n" \
"s_add_u32 s95, 0xffffffdc, s89\n" \
"v_mfma_f32_16x16x32_bf16 a[108:111], v[140:143], v[76:79], 0\n" \
"ds_read_b128 v[104:107], v29 offset:4096\n" \
"s_mul_i32 s99, 0x1200, s94\n" \
"v_mfma_f32_16x16x32_bf16 a[108:111], v[144:147], v[80:83], a[108:111]\n" \
"ds_read_b128 v[108:111], v28 offset:6144\n" \
"s_mul_i32 s100, 36, s97\n" \
"v_mfma_f32_16x16x32_bf16 a[16:19], v[56:59], v[152:155], 0\n" \
"ds_read_b128 v[112:115], v29 offset:6144\n" \
"s_waitcnt lgkmcnt(12)\n" \
"v_mfma_f32_16x16x32_bf16 a[16:19], v[52:55], v[148:151], a[16:19]\n" \
"v_add_u32_e32 v227, s95, v212\n" \
"s_add_u32 s101, s99, s100\n" \
"v_mfma_f32_16x16x32_bf16 a[48:51], v[60:63], v[148:151], 0\n" \
"s_mul_i32 s88, 0x288, s94\n" \
"v_min_u32_e32 v228, 0x287, v227\n" \
"v_mfma_f32_16x16x32_bf16 a[48:51], v[64:67], v[152:155], a[48:51]\n" \
"s_mov_b32 m0, %[mS]\n" \
"s_mov_b32 s86, s12\n" \
"v_mfma_f32_16x16x32_bf16 a[80:83], v[72:75], v[152:155], 0\n" \
"s_mov_b32 s87, s13\n" \
"v_add_u32_e32 v226, s101, v212\n" \
"v_mfma_f32_16x16x32_bf16 a[80:83], v[68:71], v[148:151], a[80:83]\n" \
"v_add_u32_e32 v229, s88, v228\n" \
"v_cmp_gt_u32_e32 vcc, 36, v212\n" \
"v_mfma_f32_16x16x32_bf16 a[112:115], v[76:79], v[148:151], 0\n" \
"s_lshl_b32 s96, %[w], 6\n" \
"v_cndmask_b32_e32 v230, v229, v226, vcc\n" \
"v_mfma_f32_16x16x32_bf16 a[112:115], v[80:83], v[152:155], a[112:115]\n" \
"v_lshlrev_b32_e32 v16, 7, v230\n" \
"v_lshrrev_b32_e32 v212, 4, v20\n" \
"v_mfma_f32_16x16x32_bf16 a[20:23], v[56:59], v[160:163], 0\n" \
"v_add_u32_e64 v213, s96, v212\n" \
"v_mul_u32_u24_e32 v214, 0x2aab, v213\n" \
"v_mfma_f32_16x16x32_bf16 a[20:23], v[52:55], v[156:159], a[20:23]\n" \
"v_lshrrev_b32_e32 v215, 16, v214\n" \
"s_lshr_b32 s94, %[b], 5\n" \
"s_mov_b32 exec_lo, 0x7ffffff\n" \
"s_mov_b32 exec_hi, 0x0\n" \
"global_load_lds_dword v186, s[86:87]\n" \
"s_mov_b64 exec, -1\n" \
"v_mfma_f32_16x16x32_bf16 a[52:55], v[60:63], v[156:159], 0\n" \
"v_mul_u32_u24_e32 v218, 6, v215\n" \
"s_mul_i32 s95, 0x48, s94\n" \
"v_mfma_f32_16x16x32_bf16 a[52:55], v[64:67], v[160:163], a[52:55]\n" \
"v_sub_u32_e32 v219, v213, v218\n" \
"v_add_u32_e64 v216, s95, v215\n" \
"v_mfma_f32_16x16x32_bf16 a[84:87], v[72:75], v[160:163], 0\n" \
"v_lshrrev_b32_e32 v220, 1, v219\n" \
"s_and_b32 s100, 7, %[b]\n" \
"v_mfma_f32_16x16x32_bf16 a[84:87], v[68:71], v[156:159], a[84:87]\n" \
"s_lshr_b32 s88, %[b], 3\n" \
"v_mul_u32_u24_e32 v217, 0xd80, v216\n" \
"v_mfma_f32_16x16x32_bf16 a[116:119], v[76:79], v[156:159], 0\n" \
"v_mul_u32_u24_e32 v221, 0x480, v220\n" \
"v_and_b32_e32 v223, 1, v219\n" \
"v_mfma_f32_16x16x32_bf16 a[116:119], v[80:83], v[160:163], a[116:119]\n" \
"s_add_u32 s97, 0x100, s96\n" \
"s_lshl_b32 s101, s100, 2\n" \
"v_mfma_f32_16x16x32_bf16 a[24:27], v[56:59], v[168:171], 0\n" \
"s_and_b32 s89, 3, s88\n" \
"v_add_u32_e64 v222, v217, v221\n" \
"v_mfma_f32_16x16x32_bf16 a[24:27], v[52:55], v[164:167], a[24:27]\n" \
"v_lshlrev_b32_e32 v224, 4, v223\n" \
"s_add_u32 s96, s101, s89\n" \
"v_mfma_f32_16x16x32_bf16 a[56:59], v[60:63], v[164:167], 0\n" \
"v_add_u32_e32 v192, v222, v224\n" \
"s_mul_i32 s56, 36, s96\n" \
"v_mfma_f32_16x16x32_bf16 a[56:59], v[64:67], v[168:171], a[56:59]\n" \
"v_add_u32_e64 v225, s97, v212\n" \
"v_mul_u32_u24_e32 v226, 0x2aab, v225\n" \
"v_mfma_f32_16x16x32_bf16 a[88:91], v[72:75], v[168:171], 0\n" \
"v_lshrrev_b32_e32 v227, 16, v226\n" \
"v_mul_u32_u24_e32 v230, 6, v227\n" \
"v_mfma_f32_16x16x32_bf16 a[88:91], v[68:71], v[164:167], a[88:91]\n" \
"v_sub_u32_e32 v231, v225, v230\n" \
"v_add_u32_e32 v228, s95, v227\n" \
"v_mfma_f32_16x16x32_bf16 a[120:123], v[76:79], v[164:167], 0\n" \
"v_lshrrev_b32_e32 v232, 1, v231\n" \
"v_mul_u32_u24_e32 v229, 0xd80, v228\n" \
"v_mfma_f32_16x16x32_bf16 a[120:123], v[80:83], v[168:171], a[120:123]\n" \
"v_mul_u32_u24_e32 v233, 0x480, v232\n" \
"s_waitcnt lgkmcnt(0)\n" \
"v_mfma_f32_16x16x32_bf16 a[28:31], v[56:59], v[176:179], 0\n" \
"s_add_u32 s62, s22, 0x100\n" \
"s_and_b32 s62, s62, 0x3ff\n" \
"v_mfma_f32_16x16x32_bf16 a[28:31], v[52:55], v[172:175], a[28:31]\n" \
"s_lshl_b32 s63, s62, 12\n" \
"v_and_b32_e32 v235, 1, v231\n" \
"v_mfma_f32_16x16x32_bf16 a[60:63], v[60:63], v[172:175], 0\n" \
"v_add_u32_e32 v234, v229, v233\n" \
"v_lshlrev_b32_e32 v236, 4, v235\n" \
"v_mfma_f32_16x16x32_bf16 a[60:63], v[64:67], v[176:179], a[60:63]\n" \
"v_add_u32_e64 v237, v234, v236\n" \
"v_cmp_gt_u32_e32 vcc, 0x1b0, v225\n" \
"v_mfma_f32_16x16x32_bf16 a[92:95], v[72:75], v[176:179], 0\n" \
"s_mul_i32 s98, 18, %[w]\n" \
"v_cndmask_b32_e32 v193, 0x40000000, v237, vcc\n" \
"v_mfma_f32_16x16x32_bf16 a[92:95], v[68:71], v[172:175], a[92:95]\n" \
"s_add_u32 s99, s95, s98\n" \
"v_add_u32_e32 v238, s99, v212\n" \
"v_mfma_f32_16x16x32_bf16 a[124:127], v[76:79], v[172:175], 0\n" \
"v_mul_u32_u24_e32 v239, 0xd80, v238\n" \
"v_cmp_gt_u32_e32 vcc, 18, v212\n" \
"v_mfma_f32_16x16x32_bf16 a[124:127], v[80:83], v[176:179], a[124:127]\n" \
"v_lshlrev_b32_e32 v240, 1, v238\n" \
"v_cndmask_b32_e32 v194, 0x40000000, v239, vcc\n" \
"s_waitcnt vmcnt(1)\n" \
"s_barrier\n" \
"v_mfma_f32_16x16x32_bf16 a[144:147], v[88:91], v[152:155], 0\n" \
"s_add_u32 m0, %[mA], 0x0\n" \
"s_add_u32 s85, s62, 0x0\n" \
"s_nop 0\n" \
"v_mfma_f32_16x16x32_bf16 a[144:147], v[84:87], v[148:151], a[144:147]\n" \
"buffer_load_dwordx4 v22, s[64:67], s85 offen lds\n" \
"v_mfma_f32_16x16x32_bf16 a[176:179], v[92:95], v[148:151], 0\n" \
"ds_read_b128 v[56:59], v29 offset:49152\n" \
"ds_read_b128 v[52:55], v28 offset:49152\n" \
"v_mfma_f32_16x16x32_bf16 a[176:179], v[96:99], v[152:155], a[176:179]\n" \
"s_add_u32 m0, %[mA], 0x400\n" \
"s_add_u32 s84, s62, 0x2000\n" \
"v_mfma_f32_16x16x32_bf16 a[208:211], v[104:107], v[152:155], 0\n" \
"buffer_load_dwordx4 v23, s[64:67], s84 offen lds\n" \
"v_mfma_f32_16x16x32_bf16 a[208:211], v[100:103], v[148:151], a[208:211]\n" \
"ds_read_b128 v[60:63], v28 offset:51200\n" \
"ds_read_b128 v[64:67], v29 offset:51200\n" \
"v_mfma_f32_16x16x32_bf16 a[240:243], v[108:111], v[148:151], 0\n" \
"ds_read_b64_tr_b16 v[148:149], v32 offset:49152\n" \
"ds_read_b64_tr_b16 v[150:151], v32 offset:50176\n" \
"v_mfma_f32_16x16x32_bf16 a[240:243], v[112:115], v[152:155], a[240:243]\n" \
"s_add_u32 m0, %[mB], 0x0\n" \
"s_add_u32 s85, s63, 0x0\n" \
"v_mfma_f32_16x16x32_bf16 a[148:151], v[88:91], v[160:163], 0\n" \
"buffer_load_dwordx4 v26, s[68:71], s85 offen lds\n" \
"v_mfma_f32_16x16x32_bf16 a[148:151], v[84:87], v[156:159], a[148:151]\n" \
"ds_read_b64_tr_b16 v[152:153], v32 offset:57344\n" \
"ds_read_b64_tr_b16 v[154:155], v32 offset:58368\n" \
"v_mfma_f32_16x16x32_bf16 a[180:183], v[92:95], v[156:159], 0\n" \
"ds_read_b128 v[72:75], v29 offset:53248\n" \
"ds_read_b128 v[68:71], v28 offset:53248\n" \
"v_mfma_f32_16x16x32_bf16 a[180:183], v[96:99], v[160:163], a[180:183]\n" \
"s_add_u32 m0, %[mB], 0x4000\n" \
"s_add_u32 s84, s63, 0x0\n" \
"s_nop 0\n" \
"v_mfma_f32_16x16x32_bf16 a[212:215], v[104:107], v[160:163], 0\n" \
"buffer_load_dwordx4 v26, s[72:75], s84 offen lds\n" \
"v_mfma_f32_16x16x32_bf16 a[212:215], v[100:103], v[156:159], a[212:215]\n" \
"ds_read_b128 v[76:79], v28 offset:55296\n" \
"ds_read_b128 v[80:83], v29 offset:55296\n" \
"v_mfma_f32_16x16x32_bf16 a[244:247], v[108:111], v[156:159], 0\n" \
"ds_read_b64_tr_b16 v[156:157], v33 offset:49152\n" \
"ds_read_b64_tr_b16 v[158:159], v33 offset:50176\n" \
"v_mfma_f32_16x16x32_bf16 a[244:247], v[112:115], v[160:163], a[244:247]\n" \
"s_add_u32 m0, %[mA], 0x800\n" \
"s_add_u32 s85, s62, 0x4000\n" \
"v_mfma_f32_16x16x32_bf16 a[152:155], v[88:91], v[168:171], 0\n" \
"buffer_load_dwordx4 v22, s[64:67], s85 offen lds\n" \
"v_mfma_f32_16x16x32_bf16 a[152:155], v[84:87], v[164:167], a[152:155]\n" \
"ds_read_b64_tr_b16 v[160:161], v33 offset:57344\n" \
"ds_read_b64_tr_b16 v[162:163], v33 offset:58368\n" \
"v_mfma_f32_16x16x32_bf16 a[184:187], v[92:95], v[164:167], 0\n" \
"v_cmp_gt_u32_e32 vcc, 18, v212\n" \
"v_lshrrev_b32_e32 v230, 1, v238\n" \
"v_mfma_f32_16x16x32_bf16 a[184:187], v[96:99], v[168:171], a[184:187]\n" \
"s_add_u32 m0, %[mA], 0xc00\n" \
"s_add_u32 s84, s62, 0x6000\n" \
"v_mfma_f32_16x16x32_bf16 a[216:219], v[104:107], v[168:171], 0\n" \
"buffer_load_dwordx4 v23, s[64:67], s84 offen lds\n" \
"v_mfma_f32_16x16x32_bf16 a[216:219], v[100:103], v[164:167], a[216:219]\n" \
"v_cndmask_b32_e32 v195, 0x40000000, v240, vcc\n" \
"s_nop 0\n" \
"v_and_b32_e32 v227, 0xff8, v238\n" \
"v_mfma_f32_16x16x32_bf16 a[248:251], v[108:111], v[164:167], 0\n" \
"ds_read_b64_tr_b16 v[164:165], v34 offset:49152\n" \
"ds_read_b64_tr_b16 v[166:167], v34 offset:50176\n" \
"v_mfma_f32_16x16x32_bf16 a[248:251], v[112:115], v[168:171], a[248:251]\n" \
"s_add_u32 m0, %[mB], 0x400\n" \
"s_add_u32 s85, s63, 0x8000\n" \
"v_mfma_f32_16x16x32_bf16 a[156:159], v[88:91], v[176:179], 0\n" \
"buffer_load_dwordx4 v26, s[68:71], s85 offen lds\n" \
"v_mfma_f32_16x16x32_bf16 a[156:159], v[84:87], v[172:175], a[156:159]\n" \
"ds_read_b64_tr_b16 v[168:169], v34 offset:57344\n" \
"ds_read_b64_tr_b16 v[170:171], v34 offset:58368\n" \
"v_mfma_f32_16x16x32_bf16 a[188:191], v[92:95], v[172:175], 0\n" \
"v_and_b32_e32 v232, 2, v230\n" \
"v_add_u32_e32 v229, v227, v232\n" \
"v_mfma_f32_16x16x32_bf16 a[188:191], v[96:99], v[176:179], a[188:191]\n" \
"s_add_u32 m0, %[mB], 0x4400\n" \
"s_add_u32 s84, s63, 0x8000\n" \
"v_mfma_f32_16x16x32_bf16 a[220:223], v[104:107], v[176:179], 0\n" \
"buffer_load_dwordx4 v26, s[72:75], s84 offen lds\n" \
"v_mfma_f32_16x16x32_bf16 a[220:223], v[100:103], v[172:175], a[220:223]\n" \
"v_and_b32_e32 v233, 1, v238\n" \
"v_add_u32_e32 v235, v229, v233\n" \
"v_mfma_f32_16x16x32_bf16 a[252:255], v[108:111], v[172:175], 0\n" \
"ds_read_b64_tr_b16 v[172:173], v35 offset:49152\n" \
"ds_read_b64_tr_b16 v[174:175], v35 offset:50176\n" \
"v_mfma_f32_16x16x32_bf16 a[252:255], v[112:115], v[176:179], a[252:255]\n" \
"s_add_u32 m0, %[mA], 0x1000\n" \
"s_add_u32 s85, s62, 0x8000\n" \
"v_mfma_f32_16x16x32_bf16 a[128:131], v[120:123], v[88:91], 0\n" \
"buffer_load_dwordx4 v22, s[64:67], s85 offen lds\n" \
"v_mfma_f32_16x16x32_bf16 a[128:131], v[116:119], v[84:87], a[128:131]\n" \
"ds_read_b64_tr_b16 v[176:177], v35 offset:57344\n" \
"ds_read_b64_tr_b16 v[178:179], v35 offset:58368\n" \
"v_mfma_f32_16x16x32_bf16 a[160:163], v[116:119], v[92:95], 0\n" \
"v_lshrrev_b32_e32 v242, 1, v216\n" \
"v_lshrrev_b32_e32 v244, 2, v216\n" \
"v_mfma_f32_16x16x32_bf16 a[160:163], v[120:123], v[96:99], a[160:163]\n" \
"s_add_u32 m0, %[mA], 0x1400\n" \
"s_add_u32 s84, s62, 0xa000\n" \
"v_mfma_f32_16x16x32_bf16 a[192:195], v[120:123], v[104:107], 0\n" \
"buffer_load_dwordx4 v23, s[64:67], s84 offen lds\n" \
"v_mfma_f32_16x16x32_bf16 a[192:195], v[116:119], v[100:103], a[192:195]\n" \
"v_lshrrev_b32_e32 v214, 1, v228\n" \
"v_lshrrev_b32_e32 v213, 2, v228\n" \
"v_mfma_f32_16x16x32_bf16 a[224:227], v[116:119], v[108:111], 0\n" \
"ds_read_b64_tr_b16 v[116:117], v32 offset:32768\n" \
"ds_read_b64_tr_b16 v[118:119], v32 offset:33792\n" \
"v_mfma_f32_16x16x32_bf16 a[224:227], v[120:123], v[112:115], a[224:227]\n" \
"s_add_u32 m0, %[mB], 0x800\n" \
"s_add_u32 s85, s63, 0x10000\n" \
"v_mfma_f32_16x16x32_bf16 a[132:135], v[128:131], v[88:91], 0\n" \
"buffer_load_dwordx4 v27, s[68:71], s85 offen lds\n" \
"v_mfma_f32_16x16x32_bf16 a[132:135], v[124:127], v[84:87], a[132:135]\n" \
"ds_read_b64_tr_b16 v[120:121], v32 offset:40960\n" \
"ds_read_b64_tr_b16 v[122:123], v32 offset:41984\n" \
"v_mfma_f32_16x16x32_bf16 a[164:167], v[124:127], v[92:95], 0\n" \
"v_lshrrev_b32_e32 v236, 1, v235\n" \
"v_lshrrev_b32_e32 v239, 2, v235\n" \
"v_mfma_f32_16x16x32_bf16 a[164:167], v[128:131], v[96:99], a[164:167]\n" \
"s_add_u32 m0, %[mB], 0x4800\n" \
"s_add_u32 s84, s63, 0x10000\n" \
"v_mfma_f32_16x16x32_bf16 a[196:199], v[128:131], v[104:107], 0\n" \
"buffer_load_dwordx4 v27, s[72:75], s84 offen lds\n" \
"v_mfma_f32_16x16x32_bf16 a[196:199], v[124:127], v[100:103], a[196:199]\n" \
"s_lshl_b32 s98, %[w], 10\n" \
"s_mul_i32 s101, 0x12000, s96\n" \
"v_mfma_f32_16x16x32_bf16 a[228:231], v[124:127], v[108:111], 0\n" \
"ds_read_b64_tr_b16 v[124:125], v33 offset:32768\n" \
"ds_read_b64_tr_b16 v[126:127], v33 offset:33792\n" \
"L_pf0:\n" \
"s_cbranch_execz L_pf4\n" \
"s_nop 0\n" \
"v_mfma_f32_16x16x32_bf16 a[228:231], v[128:131], v[112:115], a[228:231]\n" \
"ds_read_b64_tr_b16 v[128:129], v33 offset:40960\n" \
"ds_read_b64_tr_b16 v[130:131], v33 offset:41984\n" \
"v_mfma_f32_16x16x32_bf16 a[136:139], v[136:139], v[88:91], 0\n" \
"v_and_b32_e32 v243, 1, v242\n" \
"v_and_b32_e64 v245, 2, v244\n" \
"v_mfma_f32_16x16x32_bf16 a[136:139], v[132:135], v[84:87], a[136:139]\n" \
"v_and_b32_e32 v215, 1, v214\n" \
"v_and_b32_e32 v218, 2, v213\n" \
"v_mfma_f32_16x16x32_bf16 a[168:171], v[132:135], v[92:95], 0\n" \
"v_and_b32_e32 v237, 1, v236\n" \
"v_and_b32_e32 v240, 2, v239\n" \
"v_mfma_f32_16x16x32_bf16 a[168:171], v[136:139], v[96:99], a[168:171]\n" \
"s_add_u32 s99, 0x22000, s98\n" \
"s_add_u32 s89, 0x6000000, s101\n" \
"v_mfma_f32_16x16x32_bf16 a[200:203], v[136:139], v[104:107], 0\n" \
"s_mul_i32 s97, 0x2400, s94\n" \
"v_lshlrev_b32_e32 v241, 7, v216\n" \
"v_mfma_f32_16x16x32_bf16 a[200:203], v[132:135], v[100:103], a[200:203]\n" \
"v_or_b32_e32 v246, v243, v245\n" \
"v_or_b32_e32 v220, v215, v218\n" \
"v_mfma_f32_16x16x32_bf16 a[232:235], v[132:135], v[108:111], 0\n" \
"ds_read_b64_tr_b16 v[132:133], v34 offset:32768\n" \
"ds_read_b64_tr_b16 v[134:135], v34 offset:33792\n" \
"v_mfma_f32_16x16x32_bf16 a[232:235], v[136:139], v[112:115], a[232:235]\n" \
"ds_read_b64_tr_b16 v[136:137], v34 offset:40960\n" \
"ds_read_b64_tr_b16 v[138:139], v34 offset:41984\n" \
"v_mfma_f32_16x16x32_bf16 a[140:143], v[144:147], v[88:91], 0\n" \
"ds_read_b128 v[88:91], v29 offset:32768\n" \
"v_or_b32_e32 v242, v237, v240\n" \
"v_mfma_f32_16x16x32_bf16 a[140:143], v[140:143], v[84:87], a[140:143]\n" \
"ds_read_b128 v[84:87], v28 offset:32768\n" \
"v_add_u32_e64 v199, s99, v20\n" \
"v_mfma_f32_16x16x32_bf16 a[172:175], v[140:143], v[92:95], 0\n" \
"ds_read_b128 v[92:95], v28 offset:34816\n" \
"s_add_u32 s37, s89, s98\n" \
"v_mfma_f32_16x16x32_bf16 a[172:175], v[144:147], v[96:99], a[172:175]\n" \
"ds_read_b128 v[96:99], v29 offset:34816\n" \
"v_lshlrev_b32_e32 v247, 1, v246\n" \
"v_mfma_f32_16x16x32_bf16 a[204:207], v[144:147], v[104:107], 0\n" \
"ds_read_b128 v[104:107], v29 offset:36864\n" \
"v_lshlrev_b32_e32 v217, 1, v220\n" \
"v_mfma_f32_16x16x32_bf16 a[204:207], v[140:143], v[100:103], a[204:207]\n" \
"ds_read_b128 v[100:103], v28 offset:36864\n" \
"v_xor_b32_e32 v216, 3, v242\n" \
"v_mfma_f32_16x16x32_bf16 a[236:239], v[140:143], v[108:111], 0\n" \
"ds_read_b64_tr_b16 v[140:141], v35 offset:32768\n" \
"ds_read_b64_tr_b16 v[142:143], v35 offset:33792\n" \
"v_mfma_f32_16x16x32_bf16 a[236:239], v[144:147], v[112:115], a[236:239]\n" \
"s_add_u32 m0, %[mA], 0x1800\n" \
"s_waitcnt lgkmcnt(12)\n" \
"s_nop 0\n" \
"v_mfma_f32_16x16x32_bf16 a[16:19], v[56:59], v[152:155], a[16:19]\n" \
"s_add_u32 s85, s62, 0xc000\n" \
"ds_read_b64_tr_b16 v[144:145], v35 offset:40960\n" \
"v_mfma_f32_16x16x32_bf16 a[16:19], v[52:55], v[148:151], a[16:19]\n" \
"buffer_load_dwordx4 v22, s[64:67], s85 offen lds\n" \
"v_mfma_f32_16x16x32_bf16 a[48:51], v[60:63], v[148:151], a[48:51]\n" \
"s_add_u32 m0, %[mA], 0x1c00\n" \
"s_add_u32 s84, s62, 0xe000\n" \
"v_mfma_f32_16x16x32_bf16 a[48:51], v[64:67], v[152:155], a[48:51]\n" \
"ds_read_b64_tr_b16 v[146:147], v35 offset:41984\n" \
"ds_read_b128 v[108:111], v28 offset:38912\n" \
"v_mfma_f32_16x16x32_bf16 a[80:83], v[72:75], v[152:155], a[80:83]\n" \
"buffer_load_dwordx4 v23, s[64:67], s84 offen lds\n" \
"v_mfma_f32_16x16x32_bf16 a[80:83], v[68:71], v[148:151], a[80:83]\n" \
"s_add_u32 m0, %[mB], 0xc00\n" \
"s_add_u32 s85, s63, 0x18000\n" \
"v_mfma_f32_16x16x32_bf16 a[112:115], v[76:79], v[148:151], a[112:115]\n" \
"ds_read_b128 v[112:115], v29 offset:38912\n" \
"s_add_u32 s57, s37, s97\n" \
"v_mfma_f32_16x16x32_bf16 a[112:115], v[80:83], v[152:155], a[112:115]\n" \
"buffer_load_dwordx4 v27, s[68:71], s85 offen lds\n" \
"v_mfma_f32_16x16x32_bf16 a[20:23], v[56:59], v[160:163], a[20:23]\n" \
"s_add_u32 m0, %[mB], 0x4c00\n" \
"s_add_u32 s84, s63, 0x18000\n" \
"v_mfma_f32_16x16x32_bf16 a[20:23], v[52:55], v[156:159], a[20:23]\n" \
"s_lshl_b32 s99, s88, 2\n" \
"v_xor_b32_e64 v248, v219, v247\n" \
"v_mfma_f32_16x16x32_bf16 a[52:55], v[60:63], v[156:159], a[52:55]\n" \
"buffer_load_dwordx4 v27, s[72:75], s84 offen lds\n" \
"v_mfma_f32_16x16x32_bf16 a[52:55], v[64:67], v[160:163], a[52:55]\n" \
"v_xor_b32_e32 v221, v231, v217\n" \
"v_lshlrev_b32_e32 v234, 7, v235\n" \
"v_mfma_f32_16x16x32_bf16 a[84:87], v[72:75], v[160:163], a[84:87]\n" \
"v_lshlrev_b32_e32 v244, 5, v216\n" \
"v_and_b32_e32 v245, 2, v238\n" \
"v_mfma_f32_16x16x32_bf16 a[84:87], v[68:71], v[156:159], a[84:87]\n" \
"s_add_u32 s94, 0x2000, s57\n" \
"s_add_u32 s96, s99, %[w]\n" \
"v_mfma_f32_16x16x32_bf16 a[116:119], v[76:79], v[156:159], a[116:119]\n" \
"s_lshl_b32 s89, %[b], 2\n" \
"s_sub_u32 s95, 0x22000, s97\n" \
"v_mfma_f32_16x16x32_bf16 a[116:119], v[80:83], v[160:163], a[116:119]\n" \
"s_mov_b32 m0, %[mS]\n" \
"s_mov_b32 s86, s18\n" \
"v_mfma_f32_16x16x32_bf16 a[24:27], v[56:59], v[168:171], a[24:27]\n" \
"s_mov_b32 s87, s19\n" \
"v_lshlrev_b32_e32 v249, 4, v248\n" \
"v_mfma_f32_16x16x32_bf16 a[24:27], v[52:55], v[164:167], a[24:27]\n" \
"v_lshlrev_b32_e32 v251, 7, v228\n" \
"v_lshlrev_b32_e32 v223, 4, v221\n" \
"v_mfma_f32_16x16x32_bf16 a[56:59], v[60:63], v[164:167], a[56:59]\n" \
"v_add_u32_e32 v243, v234, v244\n" \
"v_lshlrev_b32_e32 v246, 1, v245\n" \
"v_mfma_f32_16x16x32_bf16 a[56:59], v[64:67], v[168:171], a[56:59]\n" \
"s_cmp_eq_u32 %[w], 0\n" \
"s_cselect_b32 s58, s94, 0x40000000\n" \
"v_mfma_f32_16x16x32_bf16 a[88:91], v[72:75], v[168:171], a[88:91]\n" \
"s_mul_i32 s101, 36, s96\n" \
"s_add_u32 s98, s89, %[w]\n" \
"v_mfma_f32_16x16x32_bf16 a[88:91], v[68:71], v[164:167], a[88:91]\n" \
"v_add_u32_e32 v250, v241, v249\n" \
"v_add_u32_e32 v222, v251, v223\n" \
"s_mov_b32 exec_lo, 0x3ffff\n" \
"s_mov_b32 exec_hi, 0x0\n" \
"global_load_lds_dword v16, s[86:87]\n" \
"s_mov_b64 exec, -1\n" \
"v_mfma_f32_16x16x32_bf16 a[120:123], v[76:79], v[164:167], a[120:123]\n" \
"v_add_u32_e32 v219, v243, v246\n" \
"v_add_u32_e32 v248, s101, v212\n" \
"v_mfma_f32_16x16x32_bf16 a[120:123], v[80:83], v[168:171], a[120:123]\n" \
"s_mul_i32 s97, 18, s98\n" \
"v_add_u32_e32 v196, s95, v250\n" \
"v_mfma_f32_16x16x32_bf16 a[28:31], v[56:59], v[176:179], a[28:31]\n" \
"v_add_u32_e32 v224, s95, v222\n" \
"v_add_u32_e32 v247, s95, v219\n" \
"v_mfma_f32_16x16x32_bf16 a[28:31], v[52:55], v[172:175], a[28:31]\n" \
"v_lshrrev_b32_e32 v241, 3, v248\n" \
"s_add_u32 s94, 0xffffffdc, s97\n" \
"v_mfma_f32_16x16x32_bf16 a[60:63], v[60:63], v[172:175], a[60:63]\n" \
"v_min_u32_e32 v213, 53, v212\n" \
"s_lshl_b32 s95, s100, 3\n" \
"v_mfma_f32_16x16x32_bf16 a[60:63], v[64:67], v[176:179], a[60:63]\n" \
"v_lshlrev_b32_e32 v249, 6, v241\n" \
"v_add_u32_e64 v215, s94, v213\n" \
"v_mfma_f32_16x16x32_bf16 a[92:95], v[72:75], v[176:179], a[92:95]\n" \
"s_and_b32 s97, 7, %[b]\n" \
"s_lshr_b32 s99, %[b], 3\n" \
"v_mfma_f32_16x16x32_bf16 a[92:95], v[68:71], v[172:175], a[92:95]\n" \
"v_add_u32_e32 v250, s95, v249\n" \
"s_lshr_b32 s94, %[b], 5\n" \
"v_mfma_f32_16x16x32_bf16 a[124:127], v[76:79], v[172:175], a[124:127]\n" \
"s_lshl_b32 s98, s97, 2\n" \
"s_and_b32 s100, 3, s99\n" \
"v_mfma_f32_16x16x32_bf16 a[124:127], v[80:83], v[176:179], a[124:127]\n" \
"s_mul_i32 s95, 0xa00, s94\n" \
"s_add_u32 s101, s98, s100\n" \
"v_mfma_f32_16x16x32_bf16 a[0:3], v[120:123], v[56:59], a[0:3]\n" \
"s_add_u32 s96, 0x5bc1000, s95\n" \
"s_lshl_b32 s88, s101, 2\n" \
"v_mfma_f32_16x16x32_bf16 a[0:3], v[116:119], v[52:55], a[0:3]\n" \
"s_add_u32 s89, s96, s88\n" \
"s_lshl_b32 s99, s94, 3\n" \
"v_mfma_f32_16x16x32_bf16 a[32:35], v[116:119], v[60:63], a[32:35]\n" \
"s_cmp_eq_u32 %[w], 0\n" \
"s_cselect_b32 s95, s89, 0x40000000\n" \
"v_mfma_f32_16x16x32_bf16 a[32:35], v[120:123], v[64:67], a[32:35]\n" \
"s_add_u32 s88, s99, s97\n" \
"v_writelane_b32 v11, s95, 0\n" \
"v_mfma_f32_16x16x32_bf16 a[64:67], v[120:123], v[72:75], a[64:67]\n" \
"s_lshl_b32 s89, s88, 10\n" \
"s_add_u32 s95, 0x5c00000, s89\n" \
"v_mfma_f32_16x16x32_bf16 a[64:67], v[116:119], v[68:71], a[64:67]\n" \
"s_lshl_b32 s99, %[w], 8\n" \
"s_add_u32 s88, s95, s99\n" \
"v_mfma_f32_16x16x32_bf16 a[96:99], v[116:119], v[76:79], a[96:99]\n" \
"v_add_u32_e32 v226, 0x24400, v20\n" \
"v_cmp_gt_u32_e32 vcc, 0x1b0, v225\n" \
"v_mfma_f32_16x16x32_bf16 a[96:99], v[120:123], v[80:83], a[96:99]\n" \
"s_or_b32 s89, 0x200, s88\n" \
"s_lshl_b32 s95, s100, 2\n" \
"v_mfma_f32_16x16x32_bf16 a[4:7], v[128:131], v[56:59], a[4:7]\n" \
"v_cndmask_b32_e32 v197, v226, v224, vcc\n" \
"v_writelane_b32 v11, s89, 1\n" \
"v_mfma_f32_16x16x32_bf16 a[4:7], v[124:127], v[52:55], a[4:7]\n" \
"s_add_u32 s99, s88, s95\n" \
"v_cmp_gt_u32_e32 vcc, 18, v212\n" \
"v_mfma_f32_16x16x32_bf16 a[36:39], v[124:127], v[60:63], a[36:39]\n" \
"v_writelane_b32 v11, s99, 2\n" \
"s_add_u32 s89, 0x80, s88\n" \
"v_mfma_f32_16x16x32_bf16 a[36:39], v[128:131], v[64:67], a[36:39]\n" \
"v_cndmask_b32_e32 v198, v226, v247, vcc\n" \
"v_and_b32_e32 v214, 7, v248\n" \
"v_mfma_f32_16x16x32_bf16 a[68:71], v[128:131], v[72:75], a[68:71]\n" \
"v_writelane_b32 v11, s89, 3\n" \
"s_lshl_b32 s88, s101, 8\n" \
"v_mfma_f32_16x16x32_bf16 a[68:71], v[124:127], v[68:71], a[68:71]\n" \
"v_add_u32_e32 v228, v250, v214\n" \
"v_cmp_gt_u32_e32 vcc, 36, v212\n" \
"v_mfma_f32_16x16x32_bf16 a[100:103], v[124:127], v[76:79], a[100:103]\n" \
"s_add_u32 s95, 0x80, s99\n" \
"s_add_u32 s39, 0x5be8000, s88\n" \
"v_mfma_f32_16x16x32_bf16 a[100:103], v[128:131], v[80:83], a[100:103]\n" \
"s_lshl_b32 s89, s94, 2\n" \
"s_waitcnt lgkmcnt(0)\n" \
"v_mfma_f32_16x16x32_bf16 a[8:11], v[136:139], v[56:59], a[8:11]\n" \
"v_cndmask_b32_e32 v218, v215, v228, vcc\n" \
"s_add_u32 s99, s39, s89\n" \
"v_mfma_f32_16x16x32_bf16 a[8:11], v[132:135], v[52:55], a[8:11]\n" \
"v_lshrrev_b32_e32 v212, 4, v20\n" \
"v_writelane_b32 v11, s95, 4\n" \
"v_mfma_f32_16x16x32_bf16 a[40:43], v[132:135], v[60:63], a[40:43]\n" \
"s_cmp_eq_u32 %[w], 3\n" \
"s_cselect_b32 s38, s99, 0x40000000\n" \
"v_mfma_f32_16x16x32_bf16 a[40:43], v[136:139], v[64:67], a[40:43]\n" \
"v_lshlrev_b32_e32 v213, 2, v212\n" \
"v_and_b32_e32 v215, 7, v212\n" \
"v_mfma_f32_16x16x32_bf16 a[72:75], v[136:139], v[72:75], a[72:75]\n" \
"s_add_u32 s101, s89, s100\n" \
"v_add_u32_e32 v214, s96, v213\n" \
"v_mfma_f32_16x16x32_bf16 a[72:75], v[132:135], v[68:71], a[72:75]\n" \
"s_add_u32 s95, 0x5be8020, s88\n" \
"v_lshlrev_b32_e32 v216, 2, v215\n" \
"v_mfma_f32_16x16x32_bf16 a[104:107], v[132:135], v[76:79], a[104:107]\n" \
"s_mul_i32 s94, 0x90000, s101\n" \
"v_add_u32_e32 v217, s95, v216\n" \
"v_mfma_f32_16x16x32_bf16 a[104:107], v[136:139], v[80:83], a[104:107]\n" \
"s_add_u32 s99, 0x4240000, s94\n" \
"s_lshl_b32 s96, %[w], 14\n" \
"v_mfma_f32_16x16x32_bf16 a[12:15], v[144:147], v[56:59], a[12:15]\n" \
"s_add_u32 s62, s22, 0x180\n" \
"s_and_b32 s62, s62, 0x3ff\n" \
"v_mfma_f32_16x16x32_bf16 a[12:15], v[140:143], v[52:55], a[12:15]\n" \
"s_lshl_b32 s63, s62, 12\n" \
"s_add_u32 s88, s99, s96\n" \
"v_mfma_f32_16x16x32_bf16 a[44:47], v[140:143], v[60:63], a[44:47]\n" \
"s_lshl_b32 s95, s97, 16\n" \
"v_writelane_b32 v11, s39, 52\n" \
"v_mfma_f32_16x16x32_bf16 a[44:47], v[144:147], v[64:67], a[44:47]\n" \
"s_add_u32 s89, s88, s95\n" \
"s_lshl_b32 s101, s97, 11\n" \
"v_mfma_f32_16x16x32_bf16 a[76:79], v[144:147], v[72:75], a[76:79]\n" \
"v_writelane_b32 v11, s89, 37\n" \
"s_add_u32 s100, 0x3800, s89\n" \
"v_mfma_f32_16x16x32_bf16 a[76:79], v[140:143], v[68:71], a[76:79]\n" \
"s_add_u32 s94, s88, s101\n" \
"v_writelane_b32 v11, s100, 51\n" \
"v_mfma_f32_16x16x32_bf16 a[108:111], v[140:143], v[76:79], a[108:111]\n" \
"s_add_u32 s99, 0x80000, s94\n" \
"s_lshl_b32 s96, %[w], 17\n" \
"v_mfma_f32_16x16x32_bf16 a[108:111], v[144:147], v[80:83], a[108:111]\n" \
"v_writelane_b32 v11, s99, 49\n" \
"s_add_u32 s95, s88, s96\n" \
"s_waitcnt vmcnt(1)\n" \
"s_barrier\n" \
"v_mfma_f32_16x16x32_bf16 a[128:131], v[120:123], v[88:91], a[128:131]\n" \
"s_add_u32 m0, %[mA], 0x8000\n" \
"s_add_u32 s85, s62, 0x0\n" \
"v_mfma_f32_16x16x32_bf16 a[128:131], v[116:119], v[84:87], a[128:131]\n" \
"buffer_load_dwordx4 v22, s[64:67], s85 offen lds\n" \
"v_mfma_f32_16x16x32_bf16 a[160:163], v[116:119], v[92:95], a[160:163]\n" \
"ds_read_b128 v[56:59], v29 offset:16384\n" \
"ds_read_b128 v[52:55], v28 offset:16384\n" \
"v_mfma_f32_16x16x32_bf16 a[160:163], v[120:123], v[96:99], a[160:163]\n" \
"s_add_u32 m0, %[mA], 0x8400\n" \
"s_add_u32 s84, s62, 0x2000\n" \
"v_mfma_f32_16x16x32_bf16 a[192:195], v[120:123], v[104:107], a[192:195]\n" \
"buffer_load_dwordx4 v23, s[64:67], s84 offen lds\n" \
"v_mfma_f32_16x16x32_bf16 a[192:195], v[116:119], v[100:103], a[192:195]\n" \
"ds_read_b128 v[60:63], v28 offset:18432\n" \
"ds_read_b128 v[64:67], v29 offset:18432\n" \
"v_mfma_f32_16x16x32_bf16 a[224:227], v[116:119], v[108:111], a[224:227]\n" \
"ds_read_b64_tr_b16 v[116:117], v32 offset:0\n" \
"ds_read_b64_tr_b16 v[118:119], v32 offset:1024\n" \
"v_mfma_f32_16x16x32_bf16 a[224:227], v[120:123], v[112:115], a[224:227]\n" \
"s_add_u32 m0, %[mB], 0x8000\n" \
"s_add_u32 s85, s63, 0x0\n" \
"v_mfma_f32_16x16x32_bf16 a[132:135], v[128:131], v[88:91], a[132:135]\n" \
"buffer_load_dwordx4 v26, s[68:71], s85 offen lds\n" \
"v_mfma_f32_16x16x32_bf16 a[132:135], v[124:127], v[84:87], a[132:135]\n" \
"ds_read_b64_tr_b16 v[120:121], v32 offset:8192\n" \
"ds_read_b64_tr_b16 v[122:123], v32 offset:9216\n" \
"v_mfma_f32_16x16x32_bf16 a[164:167], v[124:127], v[92:95], a[164:167]\n" \
"ds_read_b128 v[72:75], v29 offset:20480\n" \
"ds_read_b128 v[68:71], v28 offset:20480\n" \
"v_mfma_f32_16x16x32_bf16 a[164:167], v[128:131], v[96:99], a[164:167]\n" \
"s_add_u32 m0, %[mB], 0xc000\n" \
"s_add_u32 s84, s63, 0x0\n" \
"v_mfma_f32_16x16x32_bf16 a[196:199], v[128:131], v[104:107], a[196:199]\n" \
"buffer_load_dwordx4 v26, s[72:75], s84 offen lds\n" \
"v_mfma_f32_16x16x32_bf16 a[196:199], v[124:127], v[100:103], a[196:199]\n" \
"ds_read_b128 v[76:79], v28 offset:22528\n" \
"ds_read_b128 v[80:83], v29 offset:22528\n" \
"v_mfma_f32_16x16x32_bf16 a[228:231], v[124:127], v[108:111], a[228:231]\n" \
"ds_read_b64_tr_b16 v[124:125], v33 offset:0\n" \
"ds_read_b64_tr_b16 v[126:127], v33 offset:1024\n" \
"v_mfma_f32_16x16x32_bf16 a[228:231], v[128:131], v[112:115], a[228:231]\n" \
"s_add_u32 m0, %[mA], 0x8800\n" \
"s_add_u32 s85, s62, 0x4000\n" \
"v_mfma_f32_16x16x32_bf16 a[136:139], v[136:139], v[88:91], a[136:139]\n" \
"buffer_load_dwordx4 v22, s[64:67], s85 offen lds\n" \
"v_mfma_f32_16x16x32_bf16 a[136:139], v[132:135], v[84:87], a[136:139]\n" \
"ds_read_b64_tr_b16 v[128:129], v33 offset:8192\n" \
"ds_read_b64_tr_b16 v[130:131], v33 offset:9216\n" \
"v_mfma_f32_16x16x32_bf16 a[168:171], v[132:135], v[92:95], a[168:171]\n" \
"s_and_b32 s94, 1, s97\n" \
"s_add_u32 s89, s95, s98\n" \
"v_mfma_f32_16x16x32_bf16 a[168:171], v[136:139], v[96:99], a[168:171]\n" \
"s_add_u32 m0, %[mA], 0x8c00\n" \
"s_add_u32 s84, s62, 0x6000\n" \
"v_mfma_f32_16x16x32_bf16 a[200:203], v[136:139], v[104:107], a[200:203]\n" \
"buffer_load_dwordx4 v23, s[64:67], s84 offen lds\n" \
"v_mfma_f32_16x16x32_bf16 a[200:203], v[132:135], v[100:103], a[200:203]\n" \
"s_lshl_b32 s99, s94, 10\n" \
"s_nop 0\n" \
"s_add_u32 s100, 0x3840, s89\n" \
"v_mfma_f32_16x16x32_bf16 a[232:235], v[132:135], v[108:111], a[232:235]\n" \
"ds_read_b64_tr_b16 v[132:133], v34 offset:0\n" \
"ds_read_b64_tr_b16 v[134:135], v34 offset:1024\n" \
"v_mfma_f32_16x16x32_bf16 a[232:235], v[136:139], v[112:115], a[232:235]\n" \
"s_add_u32 m0, %[mB], 0x8400\n" \
"s_add_u32 s85, s63, 0x8000\n" \
"v_mfma_f32_16x16x32_bf16 a[140:143], v[144:147], v[88:91], a[140:143]\n" \
"buffer_load_dwordx4 v26, s[68:71], s85 offen lds\n" \
"v_mfma_f32_16x16x32_bf16 a[140:143], v[140:143], v[84:87], a[140:143]\n" \
"ds_read_b64_tr_b16 v[136:137], v34 offset:8192\n" \
"ds_read_b64_tr_b16 v[138:139], v34 offset:9216\n" \
"v_mfma_f32_16x16x32_bf16 a[172:175], v[140:143], v[92:95], a[172:175]\n" \
"s_add_u32 s96, s88, s99\n" \
"v_writelane_b32 v11, s100, 5\n" \
"v_mfma_f32_16x16x32_bf16 a[172:175], v[144:147], v[96:99], a[172:175]\n" \
"s_add_u32 m0, %[mB], 0xc400\n" \
"s_add_u32 s84, s63, 0x8000\n" \
"v_mfma_f32_16x16x32_bf16 a[204:207], v[144:147], v[104:107], a[204:207]\n" \
"buffer_load_dwordx4 v26, s[72:75], s84 offen lds\n" \
"v_mfma_f32_16x16x32_bf16 a[204:207], v[140:143], v[100:103], a[204:207]\n" \
"s_lshr_b32 s101, s97, 1\n" \
"s_add_u32 s95, 0x80000, s96\n" \
"v_mfma_f32_16x16x32_bf16 a[236:239], v[140:143], v[108:111], a[236:239]\n" \
"ds_read_b64_tr_b16 v[140:141], v35 offset:0\n" \
"ds_read_b64_tr_b16 v[142:143], v35 offset:1024\n" \
"v_mfma_f32_16x16x32_bf16 a[236:239], v[144:147], v[112:115], a[236:239]\n" \
"s_add_u32 m0, %[mA], 0x9000\n" \
"s_add_u32 s85, s62, 0x8000\n" \
"v_mfma_f32_16x16x32_bf16 a[144:147], v[88:91], v[152:155], a[144:147]\n" \
"buffer_load_dwordx4 v22, s[64:67], s85 offen lds\n" \
"v_mfma_f32_16x16x32_bf16 a[144:147], v[84:87], v[148:151], a[144:147]\n" \
"ds_read_b64_tr_b16 v[144:145], v35 offset:8192\n" \
"ds_read_b64_tr_b16 v[146:147], v35 offset:9216\n" \
"v_mfma_f32_16x16x32_bf16 a[176:179], v[92:95], v[148:151], a[176:179]\n" \
"s_add_u32 s89, 1, s97\n" \
"s_and_b32 s100, 7, s89\n" \
"v_mfma_f32_16x16x32_bf16 a[176:179], v[96:99], v[152:155], a[176:179]\n" \
"s_add_u32 m0, %[mA], 0x9400\n" \
"s_add_u32 s84, s62, 0xa000\n" \
"v_mfma_f32_16x16x32_bf16 a[208:211], v[104:107], v[152:155], a[208:211]\n" \
"buffer_load_dwordx4 v23, s[64:67], s84 offen lds\n" \
"v_mfma_f32_16x16x32_bf16 a[208:211], v[100:103], v[148:151], a[208:211]\n" \
"s_cmp_eq_u32 %[w], s101\n" \
"s_cselect_b32 s98, s95, 0x40000000\n" \
"v_mfma_f32_16x16x32_bf16 a[240:243], v[108:111], v[148:151], a[240:243]\n" \
"ds_read_b64_tr_b16 v[148:149], v32 offset:16384\n" \
"ds_read_b64_tr_b16 v[150:151], v32 offset:17408\n" \
"v_mfma_f32_16x16x32_bf16 a[240:243], v[112:115], v[152:155], a[240:243]\n" \
"s_add_u32 m0, %[mB], 0x8800\n" \
"s_add_u32 s85, s63, 0x10000\n" \
"v_mfma_f32_16x16x32_bf16 a[148:151], v[88:91], v[160:163], a[148:151]\n" \
"buffer_load_dwordx4 v27, s[68:71], s85 offen lds\n" \
"v_mfma_f32_16x16x32_bf16 a[148:151], v[84:87], v[156:159], a[148:151]\n" \
"ds_read_b64_tr_b16 v[152:153], v32 offset:24576\n" \
"ds_read_b64_tr_b16 v[154:155], v32 offset:25600\n" \
"v_mfma_f32_16x16x32_bf16 a[180:183], v[92:95], v[156:159], a[180:183]\n" \
"s_lshl_b32 s94, s100, 16\n" \
"s_add_u32 s96, 2, s97\n" \
"v_mfma_f32_16x16x32_bf16 a[180:183], v[96:99], v[160:163], a[180:183]\n" \
"s_add_u32 m0, %[mB], 0xc800\n" \
"s_add_u32 s84, s63, 0x10000\n" \
"v_mfma_f32_16x16x32_bf16 a[212:215], v[104:107], v[160:163], a[212:215]\n" \
"buffer_load_dwordx4 v27, s[72:75], s84 offen lds\n" \
"v_mfma_f32_16x16x32_bf16 a[212:215], v[100:103], v[156:159], a[212:215]\n" \
"v_writelane_b32 v11, s98, 38\n" \
"s_add_u32 s99, s88, s94\n" \
"v_mfma_f32_16x16x32_bf16 a[244:247], v[108:111], v[156:159], a[244:247]\n" \
"ds_read_b64_tr_b16 v[156:157], v33 offset:16384\n" \
"ds_read_b64_tr_b16 v[158:159], v33 offset:17408\n" \
"v_mfma_f32_16x16x32_bf16 a[244:247], v[112:115], v[160:163], a[244:247]\n" \
"ds_read_b64_tr_b16 v[160:161], v33 offset:24576\n" \
"ds_read_b64_tr_b16 v[162:163], v33 offset:25600\n" \
"v_mfma_f32_16x16x32_bf16 a[152:155], v[88:91], v[168:171], a[152:155]\n" \
"s_and_b32 s101, 7, s96\n" \
"s_add_u32 s100, 3, s97\n" \
"v_mfma_f32_16x16x32_bf16 a[152:155], v[84:87], v[164:167], a[152:155]\n" \
"v_writelane_b32 v11, s99, 42\n" \
"s_lshl_b32 s95, s101, 16\n" \
"v_mfma_f32_16x16x32_bf16 a[184:187], v[92:95], v[164:167], a[184:187]\n" \
"s_and_b32 s94, 7, s100\n" \
"s_add_u32 s98, s88, s95\n" \
"v_mfma_f32_16x16x32_bf16 a[184:187], v[96:99], v[168:171], a[184:187]\n" \
"s_lshl_b32 s99, s94, 16\n" \
"s_add_u32 s89, 0x800, s98\n" \
"v_mfma_f32_16x16x32_bf16 a[216:219], v[104:107], v[168:171], a[216:219]\n" \
"s_add_u32 s96, s88, s99\n" \
"s_add_u32 s95, 4, s97\n" \
"s_nop 0\n" \
"v_mfma_f32_16x16x32_bf16 a[216:219], v[100:103], v[164:167], a[216:219]\n" \
"v_writelane_b32 v11, s89, 43\n" \
"s_add_u32 s101, 0x1000, s96\n" \
"v_mfma_f32_16x16x32_bf16 a[248:251], v[108:111], v[164:167], a[248:251]\n" \
"ds_read_b64_tr_b16 v[164:165], v34 offset:16384\n" \
"ds_read_b64_tr_b16 v[166:167], v34 offset:17408\n" \
"v_mfma_f32_16x16x32_bf16 a[248:251], v[112:115], v[168:171], a[248:251]\n" \
"ds_read_b64_tr_b16 v[168:169], v34 offset:24576\n" \
"ds_read_b64_tr_b16 v[170:171], v34 offset:25600\n" \
"v_mfma_f32_16x16x32_bf16 a[156:159], v[88:91], v[176:179], a[156:159]\n" \
"ds_read_b128 v[88:91], v29 offset:0\n" \
"s_and_b32 s98, 7, s95\n" \
"v_mfma_f32_16x16x32_bf16 a[156:159], v[84:87], v[172:175], a[156:159]\n" \
"ds_read_b128 v[84:87], v28 offset:0\n" \
"s_add_u32 s99, 5, s97\n" \
"v_mfma_f32_16x16x32_bf16 a[188:191], v[92:95], v[172:175], a[188:191]\n" \
"ds_read_b128 v[92:95], v28 offset:2048\n" \
"v_writelane_b32 v11, s101, 44\n" \
"v_mfma_f32_16x16x32_bf16 a[188:191], v[96:99], v[176:179], a[188:191]\n" \
"ds_read_b128 v[96:99], v29 offset:2048\n" \
"s_lshl_b32 s89, s98, 16\n" \
"v_mfma_f32_16x16x32_bf16 a[220:223], v[104:107], v[176:179], a[220:223]\n" \
"ds_read_b128 v[104:107], v29 offset:4096\n" \
"s_and_b32 s96, 7, s99\n" \
"v_mfma_f32_16x16x32_bf16 a[220:223], v[100:103], v[172:175], a[220:223]\n" \
"ds_read_b128 v[100:103], v28 offset:4096\n" \
"s_add_u32 s100, s88, s89\n" \
"v_mfma_f32_16x16x32_bf16 a[252:255], v[108:111], v[172:175], a[252:255]\n" \
"ds_read_b64_tr_b16 v[172:173], v35 offset:16384\n" \
"ds_read_b64_tr_b16 v[174:175], v35 offset:17408\n" \
"v_mfma_f32_16x16x32_bf16 a[252:255], v[112:115], v[176:179], a[252:255]\n" \
"s_add_u32 m0, %[mA], 0x9800\n" \
"s_waitcnt lgkmcnt(12)\n" \
"v_mfma_f32_16x16x32_bf16 a[0:3], v[120:123], v[56:59], a[0:3]\n" \
"s_add_u32 s85, s62, 0xc000\n" \
"ds_read_b64_tr_b16 v[176:177], v35 offset:24576\n" \
"v_mfma_f32_16x16x32_bf16 a[0:3], v[116:119], v[52:55], a[0:3]\n" \
"buffer_load_dwordx4 v22, s[64:67], s85 offen lds\n" \
"v_mfma_f32_16x16x32_bf16 a[32:35], v[116:119], v[60:63], a[32:35]\n" \
"s_add_u32 m0, %[mA], 0x9c00\n" \
"s_add_u32 s84, s62, 0xe000\n" \
"v_mfma_f32_16x16x32_bf16 a[32:35], v[120:123], v[64:67], a[32:35]\n" \
"ds_read_b64_tr_b16 v[178:179], v35 offset:25600\n" \
"ds_read_b128 v[108:111], v28 offset:6144\n" \
"v_mfma_f32_16x16x32_bf16 a[64:67], v[120:123], v[72:75], a[64:67]\n" \
"buffer_load_dwordx4 v23, s[64:67], s84 offen lds\n" \
"v_mfma_f32_16x16x32_bf16 a[64:67], v[116:119], v[68:71], a[64:67]\n" \
"s_add_u32 m0, %[mB], 0x8c00\n" \
"s_add_u32 s85, s63, 0x18000\n" \
"v_mfma_f32_16x16x32_bf16 a[96:99], v[116:119], v[76:79], a[96:99]\n" \
"ds_read_b128 v[112:115], v29 offset:6144\n" \
"s_lshl_b32 s101, s96, 16\n" \
"v_mfma_f32_16x16x32_bf16 a[96:99], v[120:123], v[80:83], a[96:99]\n" \
"buffer_load_dwordx4 v27, s[68:71], s85 offen lds\n" \
"v_mfma_f32_16x16x32_bf16 a[4:7], v[128:131], v[56:59], a[4:7]\n" \
"s_add_u32 m0, %[mB], 0xcc00\n" \
"s_add_u32 s84, s63, 0x18000\n" \
"v_mfma_f32_16x16x32_bf16 a[4:7], v[124:127], v[52:55], a[4:7]\n" \
"s_add_u32 s94, 0x1800, s100\n" \
"s_add_u32 s95, s88, s101\n" \
"v_mfma_f32_16x16x32_bf16 a[36:39], v[124:127], v[60:63], a[36:39]\n" \
"buffer_load_dwordx4 v27, s[72:75], s84 offen lds\n" \
"v_mfma_f32_16x16x32_bf16 a[36:39], v[128:131], v[64:67], a[36:39]\n" \
"s_add_u32 s89, 6, s97\n" \
"v_writelane_b32 v11, s94, 45\n" \
"v_mfma_f32_16x16x32_bf16 a[68:71], v[128:131], v[72:75], a[68:71]\n" \
"s_add_u32 s98, 0x2000, s95\n" \
"s_and_b32 s100, 7, s89\n" \
"v_mfma_f32_16x16x32_bf16 a[68:71], v[124:127], v[68:71], a[68:71]\n" \
"buffer_load_dwordx4 v[200:203], v192, s[76:79], s56 offen offset:0\n" \
"v_mfma_f32_16x16x32_bf16 a[100:103], v[124:127], v[76:79], a[100:103]\n" \
"s_add_u32 s101, 7, s97\n" \
"v_writelane_b32 v11, s98, 46\n" \
"v_mfma_f32_16x16x32_bf16 a[100:103], v[128:131], v[80:83], a[100:103]\n" \
"s_lshl_b32 s94, s100, 16\n" \
"s_and_b32 s95, 7, s101\n" \
"v_mfma_f32_16x16x32_bf16 a[8:11], v[136:139], v[56:59], a[8:11]\n" \
"s_add_u32 s99, s88, s94\n" \
"s_lshl_b32 s98, s95, 16\n" \
"v_mfma_f32_16x16x32_bf16 a[8:11], v[132:135], v[52:55], a[8:11]\n" \
"s_add_u32 s96, 0x2800, s99\n" \
"s_add_u32 s89, s88, s98\n" \
"v_mfma_f32_16x16x32_bf16 a[40:43], v[132:135], v[60:63], a[40:43]\n" \
"buffer_load_dwordx4 v[204:207], v193, s[76:79], s56 offen offset:0\n" \
"v_mfma_f32_16x16x32_bf16 a[40:43], v[136:139], v[64:67], a[40:43]\n" \
"v_writelane_b32 v11, s96, 47\n" \
"s_add_u32 s100, 0x3000, s89\n" \
"v_mfma_f32_16x16x32_bf16 a[72:75], v[136:139], v[72:75], a[72:75]\n" \
"v_writelane_b32 v11, s100, 48\n" \
"s_mov_b32 s87, 0x5040100\n" \
"v_mfma_f32_16x16x32_bf16 a[72:75], v[132:135], v[68:71], a[72:75]\n" \
"v_lshlrev_b32_e32 v188, 7, v218\n" \
"v_readfirstlane_b32 %[tag], v180\n" \
"v_mfma_f32_16x16x32_bf16 a[104:107], v[132:135], v[76:79], a[104:107]\n" \
"s_add_u32 %[tag], %[tag], 0x9E3779B1\n" \
"v_mov_b32_e64 v45, %[tag]\n" \
"v_mfma_f32_16x16x32_bf16 a[104:107], v[136:139], v[80:83], a[104:107]\n" \
"s_lshr_b32 s94, %[b], 5\n" \
"s_sub_u32 s95, 15, s94\n" \
"v_mfma_f32_16x16x32_bf16 a[12:15], v[144:147], v[56:59], a[12:15]\n" \
"buffer_load_dword v208, v194, s[76:79], s56 offen offset:32\n" \
"v_mfma_f32_16x16x32_bf16 a[12:15], v[140:143], v[52:55], a[12:15]\n" \
"s_cmp_lt_u32 %[w], 2\n" \
"s_cselect_b32 s96, s95, s94\n" \
"v_mfma_f32_16x16x32_bf16 a[44:47], v[140:143], v[60:63], a[44:47]\n" \
"s_lshl_b32 s97, s96, 7\n" \
"s_add_u32 s98, 0xfffffff0, s97\n" \
"v_mfma_f32_16x16x32_bf16 a[44:47], v[144:147], v[64:67], a[44:47]\n" \
"s_cmp_eq_u32 s97, 0\n" \
"s_cselect_b32 s99, 0, s98\n" \
"v_mfma_f32_16x16x32_bf16 a[76:79], v[144:147], v[72:75], a[76:79]\n" \
"s_and_b32 s100, 1, %[w]\n" \
"v_cmp_gt_u32_e32 vcc, 32, v212\n" \
"v_mfma_f32_16x16x32_bf16 a[76:79], v[140:143], v[68:71], a[76:79]\n" \
"s_lshl_b32 s101, s100, 3\n" \
"s_add_u32 s88, s99, s101\n" \
"v_mfma_f32_16x16x32_bf16 a[108:111], v[140:143], v[76:79], a[108:111]\n" \
"buffer_load_dword v209, v194, s[76:79], s56 offen offset:1184\n" \
"v_mfma_f32_16x16x32_bf16 a[108:111], v[144:147], v[80:83], a[108:111]\n" \
"v_lshrrev_b32_e32 v212, 4, v20\n" \
"v_cndmask_b32_e32 v17, v217, v214, vcc\n" \
"v_mfma_f32_16x16x32_bf16 a[16:19], v[56:59], v[152:155], a[16:19]\n" \
"v_lshrrev_b32_e32 v213, 3, v212\n" \
"s_lshr_b32 s99, %[b], 3\n" \
"v_mfma_f32_16x16x32_bf16 a[16:19], v[52:55], v[148:151], a[16:19]\n" \
"v_add_u32_e32 v214, s88, v213\n" \
"s_lshl_b32 s96, s100, 6\n" \
"v_mfma_f32_16x16x32_bf16 a[48:51], v[60:63], v[148:151], a[48:51]\n" \
"s_and_b32 s101, 3, s99\n" \
"s_add_u32 s98, s97, s96\n" \
"v_mfma_f32_16x16x32_bf16 a[48:51], v[64:67], v[152:155], a[48:51]\n" \
"s_add_u32 s88, s101, s94\n" \
"v_add_u32_e32 v221, s98, v213\n" \
"v_mfma_f32_16x16x32_bf16 a[80:83], v[72:75], v[152:155], a[80:83]\n" \
"buffer_load_dword v211, v194, s[76:79], s56 offen offset:2336\n" \
"v_mfma_f32_16x16x32_bf16 a[80:83], v[68:71], v[148:151], a[80:83]\n" \
"s_mov_b32 s76, s14\n" \
"s_and_b32 s77, s15, 0xffff\n" \
"v_mfma_f32_16x16x32_bf16 a[112:115], v[76:79], v[148:151], a[112:115]\n" \
"s_mov_b32 s78, 0x480\n" \
"s_add_u32 s56, s57, 0x1000\n" \
"v_mfma_f32_16x16x32_bf16 a[112:115], v[80:83], v[152:155], a[112:115]\n" \
"s_add_u32 s96, 0xfffffff7, s88\n" \
"s_lshl_b32 s89, s100, 2\n" \
"v_mfma_f32_16x16x32_bf16 a[20:23], v[56:59], v[160:163], a[20:23]\n" \
"s_cmp_lt_u32 s88, 9\n" \
"s_cselect_b32 s98, s88, s96\n" \
"v_mfma_f32_16x16x32_bf16 a[20:23], v[52:55], v[156:159], a[20:23]\n" \
"s_add_u32 s88, s97, s100\n" \
"s_lshl_b32 s23, s98, 7\n" \
"v_mfma_f32_16x16x32_bf16 a[52:55], v[60:63], v[156:159], a[52:55]\n" \
"buffer_load_ushort v210, v195, s[76:79], 0 offen\n" \
"v_mfma_f32_16x16x32_bf16 a[52:55], v[64:67], v[160:163], a[52:55]\n" \
"s_cmp_eq_u32 s88, 0\n" \
"s_cselect_b32 s49, 0xff800000, 0\n" \
"v_mfma_f32_16x16x32_bf16 a[84:87], v[72:75], v[160:163], a[84:87]\n" \
"s_lshr_b32 s96, s97, 4\n" \
"v_lshrrev_b32_e32 v217, 4, v212\n" \
"v_mfma_f32_16x16x32_bf16 a[84:87], v[68:71], v[156:159], a[84:87]\n" \
"s_add_u32 s98, s96, s89\n" \
"v_add_u32_e32 v218, s89, v217\n" \
"v_mfma_f32_16x16x32_bf16 a[116:119], v[76:79], v[156:159], a[116:119]\n" \
"s_mul_i32 s88, 0x24800, s98\n" \
"s_and_b32 s96, 7, %[b]\n" \
"v_mfma_f32_16x16x32_bf16 a[116:119], v[80:83], v[160:163], a[116:119]\n" \
"s_add_u32 s97, 0x3000000, s88\n" \
"s_waitcnt lgkmcnt(0)\n" \
"v_mfma_f32_16x16x32_bf16 a[24:27], v[56:59], v[168:171], a[24:27]\n" \
"s_lshl_b32 s89, s96, 8\n" \
"s_add_u32 s98, s97, s89\n" \
"v_mfma_f32_16x16x32_bf16 a[24:27], v[52:55], v[164:167], a[24:27]\n" \
"s_lshl_b32 s88, s101, 2\n" \
"v_and_b32_e32 v216, 7, v212\n" \
"v_mfma_f32_16x16x32_bf16 a[56:59], v[60:63], v[164:167], a[56:59]\n" \
"s_add_u32 s89, s98, s88\n" \
"v_xor_b32_e32 v219, v216, v218\n" \
"v_mfma_f32_16x16x32_bf16 a[56:59], v[64:67], v[168:171], a[56:59]\n" \
"s_lshl_b32 s98, s96, 2\n" \
"v_mul_u32_u24_e32 v215, 0x480, v214\n" \
"v_mfma_f32_16x16x32_bf16 a[88:91], v[72:75], v[168:171], a[88:91]\n" \
"v_lshlrev_b32_e32 v220, 4, v219\n" \
"v_lshrrev_b32_e32 v229, 2, v212\n" \
"v_mfma_f32_16x16x32_bf16 a[88:91], v[68:71], v[164:167], a[88:91]\n" \
"v_and_b32_e32 v234, 3, v212\n" \
"v_and_b32_e64 v249, 2, v212\n" \
"v_mfma_f32_16x16x32_bf16 a[120:123], v[76:79], v[164:167], a[120:123]\n" \
"s_add_u32 s51, 0x4000, s89\n" \
"s_add_u32 s88, s98, s101\n" \
"v_mfma_f32_16x16x32_bf16 a[120:123], v[80:83], v[168:171], a[120:123]\n" \
"v_add_u32_e32 v21, v215, v220\n" \
"v_add_u32_e64 v225, 4, v217\n" \
"v_mfma_f32_16x16x32_bf16 a[28:31], v[56:59], v[176:179], a[28:31]\n" \
"s_add_u32 s62, s22, 0x200\n" \
"s_and_b32 s62, s62, 0x3ff\n" \
"v_mfma_f32_16x16x32_bf16 a[28:31], v[52:55], v[172:175], a[28:31]\n" \
"s_lshl_b32 s63, s62, 12\n" \
"s_lshl_b32 s89, s88, 9\n" \
"v_mfma_f32_16x16x32_bf16 a[60:63], v[60:63], v[172:175], a[60:63]\n" \
"s_lshl_b32 s98, s99, 2\n" \
"v_lshrrev_b32_e32 v212, 4, v20\n" \
"v_mfma_f32_16x16x32_bf16 a[60:63], v[64:67], v[176:179], a[60:63]\n" \
"v_xor_b32_e32 v223, v216, v217\n" \
"v_xor_b32_e32 v226, v216, v225\n" \
"v_mfma_f32_16x16x32_bf16 a[92:95], v[72:75], v[176:179], a[92:95]\n" \
"v_mov_b32_e32 v250, 0xeeeeeeee\n" \
"v_mov_b32_e32 v251, 0x44444444\n" \
"v_mfma_f32_16x16x32_bf16 a[92:95], v[68:71], v[172:175], a[92:95]\n" \
"v_cmp_ne_u32_e32 vcc, 0, v249\n" \
"s_add_u32 s50, s97, s89\n" \
"v_mfma_f32_16x16x32_bf16 a[124:127], v[76:79], v[172:175], a[124:127]\n" \
"s_lshl_b32 s96, %[w], 10\n" \
"s_add_u32 s101, s98, %[w]\n" \
"v_mfma_f32_16x16x32_bf16 a[124:127], v[80:83], v[176:179], a[124:127]\n" \
"v_and_b32_e32 v215, 15, v212\n" \
"v_cndmask_b32_e32 v50, v251, v250, vcc\n" \
"s_waitcnt vmcnt(6)\n" \
"s_barrier\n" \
"L_pf1:\n" \
"s_cbranch_execz L_pf5\n" \
"s_nop 0\n" \
"v_mfma_f32_16x16x32_bf16 a[144:147], v[88:91], v[152:155], a[144:147]\n" \
"s_add_u32 m0, %[mA], 0x0\n" \
"s_add_u32 s85, s62, 0x0\n" \
"v_mfma_f32_16x16x32_bf16 a[144:147], v[84:87], v[148:151], a[144:147]\n" \
"buffer_load_dwordx4 v22, s[64:67], s85 offen lds\n" \
"v_mfma_f32_16x16x32_bf16 a[176:179], v[92:95], v[148:151], a[176:179]\n" \
"ds_read_b128 v[56:59], v29 offset:49152\n" \
"ds_read_b128 v[52:55], v28 offset:49152\n" \
"v_mfma_f32_16x16x32_bf16 a[176:179], v[96:99], v[152:155], a[176:179]\n" \
"s_add_u32 m0, %[mA], 0x400\n" \
"s_add_u32 s84, s62, 0x2000\n" \
"v_mfma_f32_16x16x32_bf16 a[208:211], v[104:107], v[152:155], a[208:211]\n" \
"buffer_load_dwordx4 v23, s[64:67], s84 offen lds\n" \
"v_mfma_f32_16x16x32_bf16 a[208:211], v[100:103], v[148:151], a[208:211]\n" \
"ds_read_b128 v[60:63], v28 offset:51200\n" \
"ds_read_b128 v[64:67], v29 offset:51200\n" \
"v_mfma_f32_16x16x32_bf16 a[240:243], v[108:111], v[148:151], a[240:243]\n" \
"ds_read_b64_tr_b16 v[148:149], v32 offset:49152\n" \
"ds_read_b64_tr_b16 v[150:151], v32 offset:50176\n" \
"v_mfma_f32_16x16x32_bf16 a[240:243], v[112:115], v[152:155], a[240:243]\n" \
"s_add_u32 m0, %[mB], 0x0\n" \
"s_add_u32 s85, s63, 0x0\n" \
"v_mfma_f32_16x16x32_bf16 a[148:151], v[88:91], v[160:163], a[148:151]\n" \
"buffer_load_dwordx4 v26, s[68:71], s85 offen lds\n" \
"v_mfma_f32_16x16x32_bf16 a[148:151], v[84:87], v[156:159], a[148:151]\n" \
"ds_read_b64_tr_b16 v[152:153], v32 offset:57344\n" \
"ds_read_b64_tr_b16 v[154:155], v32 offset:58368\n" \
"v_mfma_f32_16x16x32_bf16 a[180:183], v[92:95], v[156:159], a[180:183]\n" \
"ds_read_b128 v[72:75], v29 offset:53248\n" \
"ds_read_b128 v[68:71], v28 offset:53248\n" \
"v_mfma_f32_16x16x32_bf16 a[180:183], v[96:99], v[160:163], a[180:183]\n" \
"s_add_u32 m0, %[mB], 0x4000\n" \
"s_add_u32 s84, s63, 0x0\n" \
"s_nop 0\n" \
"v_mfma_f32_16x16x32_bf16 a[212:215], v[104:107], v[160:163], a[212:215]\n" \
"buffer_load_dwordx4 v26, s[72:75], s84 offen lds\n" \
"v_mfma_f32_16x16x32_bf16 a[212:215], v[100:103], v[156:159], a[212:215]\n" \
"ds_read_b128 v[76:79], v28 offset:55296\n" \
"ds_read_b128 v[80:83], v29 offset:55296\n" \
"v_mfma_f32_16x16x32_bf16 a[244:247], v[108:111], v[156:159], a[244:247]\n" \
"ds_read_b64_tr_b16 v[156:157], v33 offset:49152\n" \
"ds_read_b64_tr_b16 v[158:159], v33 offset:50176\n" \
"v_mfma_f32_16x16x32_bf16 a[244:247], v[112:115], v[160:163], a[244:247]\n" \
"s_add_u32 m0, %[mA], 0x800\n" \
"s_add_u32 s85, s62, 0x4000\n" \
"v_mfma_f32_16x16x32_bf16 a[152:155], v[88:91], v[168:171], a[152:155]\n" \
"buffer_load_dwordx4 v22, s[64:67], s85 offen lds\n" \
"v_mfma_f32_16x16x32_bf16 a[152:155], v[84:87], v[164:167], a[152:155]\n" \
"ds_read_b64_tr_b16 v[160:161], v33 offset:57344\n" \
"ds_read_b64_tr_b16 v[162:163], v33 offset:58368\n" \
"v_mfma_f32_16x16x32_bf16 a[184:187], v[92:95], v[164:167], a[184:187]\n" \
"s_add_u32 s40, 0x14000, s96\n" \
"s_lshl_b32 s41, s101, 7\n" \
"v_mfma_f32_16x16x32_bf16 a[184:187], v[96:99], v[168:171], a[184:187]\n" \
"s_add_u32 m0, %[mA], 0xc00\n" \
"s_add_u32 s84, s62, 0x6000\n" \
"v_mfma_f32_16x16x32_bf16 a[216:219], v[104:107], v[168:171], a[216:219]\n" \
"buffer_load_dwordx4 v23, s[64:67], s84 offen lds\n" \
"v_mfma_f32_16x16x32_bf16 a[216:219], v[100:103], v[164:167], a[216:219]\n" \
"s_lshl_b32 s97, s88, 20\n" \
"v_lshrrev_b32_e32 v213, 4, v212\n" \
"s_nop 0\n" \
"v_mfma_f32_16x16x32_bf16 a[248:251], v[108:111], v[164:167], a[248:251]\n" \
"ds_read_b64_tr_b16 v[164:165], v34 offset:49152\n" \
"ds_read_b64_tr_b16 v[166:167], v34 offset:50176\n" \
"v_mfma_f32_16x16x32_bf16 a[248:251], v[112:115], v[168:171], a[248:251]\n" \
"s_add_u32 m0, %[mB], 0x400\n" \
"s_add_u32 s85, s63, 0x8000\n" \
"v_mfma_f32_16x16x32_bf16 a[156:159], v[88:91], v[176:179], a[156:159]\n" \
"buffer_load_dwordx4 v26, s[68:71], s85 offen lds\n" \
"v_mfma_f32_16x16x32_bf16 a[156:159], v[84:87], v[172:175], a[156:159]\n" \
"ds_read_b64_tr_b16 v[168:169], v34 offset:57344\n" \
"ds_read_b64_tr_b16 v[170:171], v34 offset:58368\n" \
"v_mfma_f32_16x16x32_bf16 a[188:191], v[92:95], v[172:175], a[188:191]\n" \
"v_and_b32_e32 v216, 1, v215\n" \
"v_lshlrev_b32_e32 v228, 3, v217\n" \
"v_mfma_f32_16x16x32_bf16 a[188:191], v[96:99], v[176:179], a[188:191]\n" \
"s_add_u32 m0, %[mB], 0x4400\n" \
"s_add_u32 s84, s63, 0x8000\n" \
"v_mfma_f32_16x16x32_bf16 a[220:223], v[104:107], v[176:179], a[220:223]\n" \
"buffer_load_dwordx4 v26, s[72:75], s84 offen lds\n" \
"v_mfma_f32_16x16x32_bf16 a[220:223], v[100:103], v[172:175], a[220:223]\n" \
"v_and_b32_e32 v238, 1, v217\n" \
"v_lshlrev_b32_e32 v242, 4, v217\n" \
"v_mfma_f32_16x16x32_bf16 a[252:255], v[108:111], v[172:175], a[252:255]\n" \
"ds_read_b64_tr_b16 v[172:173], v35 offset:49152\n" \
"ds_read_b64_tr_b16 v[174:175], v35 offset:50176\n" \
"v_mfma_f32_16x16x32_bf16 a[252:255], v[112:115], v[176:179], a[252:255]\n" \
"s_add_u32 m0, %[mA], 0x1000\n" \
"s_add_u32 s85, s62, 0x8000\n" \
"v_mfma_f32_16x16x32_bf16 a[128:131], v[120:123], v[88:91], a[128:131]\n" \
"buffer_load_dwordx4 v22, s[64:67], s85 offen lds\n" \
"v_mfma_f32_16x16x32_bf16 a[128:131], v[116:119], v[84:87], a[128:131]\n" \
"ds_read_b64_tr_b16 v[176:177], v35 offset:57344\n" \
"ds_read_b64_tr_b16 v[178:179], v35 offset:58368\n" \
"v_mfma_f32_16x16x32_bf16 a[160:163], v[116:119], v[92:95], a[160:163]\n" \
"s_add_u32 s89, 0x1000000, s97\n" \
"s_lshl_b32 s96, %[w], 12\n" \
"v_mfma_f32_16x16x32_bf16 a[160:163], v[120:123], v[96:99], a[160:163]\n" \
"s_add_u32 m0, %[mA], 0x1400\n" \
"s_add_u32 s84, s62, 0xa000\n" \
"v_mfma_f32_16x16x32_bf16 a[192:195], v[120:123], v[104:107], a[192:195]\n" \
"buffer_load_dwordx4 v23, s[64:67], s84 offen lds\n" \
"v_mfma_f32_16x16x32_bf16 a[192:195], v[116:119], v[100:103], a[192:195]\n" \
"s_lshr_b32 s101, %[w], 1\n" \
"v_lshlrev_b32_e32 v214, 2, v213\n" \
"s_nop 0\n" \
"v_mfma_f32_16x16x32_bf16 a[224:227], v[116:119], v[108:111], a[224:227]\n" \
"ds_read_b64_tr_b16 v[116:117], v32 offset:32768\n" \
"ds_read_b64_tr_b16 v[118:119], v32 offset:33792\n" \
"v_mfma_f32_16x16x32_bf16 a[224:227], v[120:123], v[112:115], a[224:227]\n" \
"s_add_u32 m0, %[mB], 0x800\n" \
"s_add_u32 s85, s63, 0x10000\n" \
"v_mfma_f32_16x16x32_bf16 a[132:135], v[128:131], v[88:91], a[132:135]\n" \
"buffer_load_dwordx4 v27, s[68:71], s85 offen lds\n" \
"v_mfma_f32_16x16x32_bf16 a[132:135], v[124:127], v[84:87], a[132:135]\n" \
"ds_read_b64_tr_b16 v[120:121], v32 offset:40960\n" \
"ds_read_b64_tr_b16 v[122:123], v32 offset:41984\n" \
"v_mfma_f32_16x16x32_bf16 a[164:167], v[124:127], v[92:95], a[164:167]\n" \
"v_mov_b32_e32 v218, 0x1000c0c\n" \
"v_mov_b32_e32 v219, 0xc0c0100\n" \
"v_mfma_f32_16x16x32_bf16 a[164:167], v[128:131], v[96:99], a[164:167]\n" \
"s_add_u32 m0, %[mB], 0x4800\n" \
"s_add_u32 s84, s63, 0x10000\n" \
"v_mfma_f32_16x16x32_bf16 a[196:199], v[128:131], v[104:107], a[196:199]\n" \
"buffer_load_dwordx4 v27, s[72:75], s84 offen lds\n" \
"v_mfma_f32_16x16x32_bf16 a[196:199], v[124:127], v[100:103], a[196:199]\n" \
"v_cmp_ne_u32_e32 vcc, 0, v216\n" \
"v_mul_u32_u24_e32 v222, 0x480, v221\n" \
"v_mfma_f32_16x16x32_bf16 a[228:231], v[124:127], v[108:111], a[228:231]\n" \
"ds_read_b64_tr_b16 v[124:125], v33 offset:32768\n" \
"ds_read_b64_tr_b16 v[126:127], v33 offset:33792\n" \
"v_mfma_f32_16x16x32_bf16 a[228:231], v[128:131], v[112:115], a[228:231]\n" \
"ds_read_b64_tr_b16 v[128:129], v33 offset:40960\n" \
"ds_read_b64_tr_b16 v[130:131], v33 offset:41984\n" \
"v_mfma_f32_16x16x32_bf16 a[136:139], v[136:139], v[88:91], a[136:139]\n" \
"s_add_u32 s52, s89, s96\n" \
"s_lshl_b32 s99, %[w], 11\n" \
"v_mfma_f32_16x16x32_bf16 a[136:139], v[132:135], v[84:87], a[136:139]\n" \
"s_lshl_b32 s88, s101, 15\n" \
"v_add_u32_e64 v217, v214, v216\n" \
"v_mfma_f32_16x16x32_bf16 a[168:171], v[132:135], v[92:95], a[168:171]\n" \
"v_cndmask_b32_e32 v220, v219, v218, vcc\n" \
"v_lshlrev_b32_e32 v224, 4, v223\n" \
"v_mfma_f32_16x16x32_bf16 a[168:171], v[136:139], v[96:99], a[168:171]\n" \
"s_add_u32 s98, s89, s99\n" \
"s_add_u32 s97, s89, s88\n" \
"v_mfma_f32_16x16x32_bf16 a[200:203], v[136:139], v[104:107], a[200:203]\n" \
"s_lshl_b32 s96, s100, 11\n" \
"v_mov_b32_e32 v221, 0xc0c0c0c\n" \
"v_mfma_f32_16x16x32_bf16 a[200:203], v[132:135], v[100:103], a[200:203]\n" \
"v_cmp_le_u32_e32 vcc, v217, v215\n" \
"v_add_u32_e64 v24, v222, v224\n" \
"v_mfma_f32_16x16x32_bf16 a[232:235], v[132:135], v[108:111], a[232:235]\n" \
"ds_read_b64_tr_b16 v[132:133], v34 offset:32768\n" \
"ds_read_b64_tr_b16 v[134:135], v34 offset:33792\n" \
"v_mfma_f32_16x16x32_bf16 a[232:235], v[136:139], v[112:115], a[232:235]\n" \
"ds_read_b64_tr_b16 v[136:137], v34 offset:40960\n" \
"ds_read_b64_tr_b16 v[138:139], v34 offset:41984\n" \
"v_mfma_f32_16x16x32_bf16 a[140:143], v[144:147], v[88:91], a[140:143]\n" \
"ds_read_b128 v[88:91], v29 offset:32768\n" \
"v_lshlrev_b32_e32 v227, 4, v226\n" \
"v_mfma_f32_16x16x32_bf16 a[140:143], v[140:143], v[84:87], a[140:143]\n" \
"ds_read_b128 v[84:87], v28 offset:32768\n" \
"s_add_u32 s99, s97, s96\n" \
"v_mfma_f32_16x16x32_bf16 a[172:175], v[140:143], v[92:95], a[172:175]\n" \
"ds_read_b128 v[92:95], v28 offset:34816\n" \
"s_lshl_b32 s101, s95, 16\n" \
"v_mfma_f32_16x16x32_bf16 a[172:175], v[144:147], v[96:99], a[172:175]\n" \
"ds_read_b128 v[96:99], v29 offset:34816\n" \
"v_cndmask_b32_e32 v40, v220, v221, vcc\n" \
"v_mfma_f32_16x16x32_bf16 a[204:207], v[144:147], v[104:107], a[204:207]\n" \
"ds_read_b128 v[104:107], v29 offset:36864\n" \
"v_add_u32_e64 v25, v222, v227\n" \
"v_mfma_f32_16x16x32_bf16 a[204:207], v[140:143], v[100:103], a[204:207]\n" \
"ds_read_b128 v[100:103], v28 offset:36864\n" \
"s_add_u32 s54, 0x10000, s98\n" \
"v_mfma_f32_16x16x32_bf16 a[236:239], v[140:143], v[108:111], a[236:239]\n" \
"ds_read_b64_tr_b16 v[140:141], v35 offset:32768\n" \
"ds_read_b64_tr_b16 v[142:143], v35 offset:33792\n" \
"v_mfma_f32_16x16x32_bf16 a[236:239], v[144:147], v[112:115], a[236:239]\n" \
"s_add_u32 m0, %[mA], 0x1800\n" \
"s_waitcnt lgkmcnt(12)\n" \
"s_nop 0\n" \
"v_mfma_f32_16x16x32_bf16 a[16:19], v[56:59], v[152:155], a[16:19]\n" \
"s_add_u32 s85, s62, 0xc000\n" \
"ds_read_b64_tr_b16 v[144:145], v35 offset:40960\n" \
"v_mfma_f32_16x16x32_bf16 a[16:19], v[52:55], v[148:151], a[16:19]\n" \
"buffer_load_dwordx4 v22, s[64:67], s85 offen lds\n" \
"v_mfma_f32_16x16x32_bf16 a[48:51], v[60:63], v[148:151], a[48:51]\n" \
"s_add_u32 m0, %[mA], 0x1c00\n" \
"s_add_u32 s84, s62, 0xe000\n" \
"v_mfma_f32_16x16x32_bf16 a[48:51], v[64:67], v[152:155], a[48:51]\n" \
"ds_read_b64_tr_b16 v[146:147], v35 offset:41984\n" \
"ds_read_b128 v[108:111], v28 offset:38912\n" \
"v_mfma_f32_16x16x32_bf16 a[80:83], v[72:75], v[152:155], a[80:83]\n" \
"buffer_load_dwordx4 v23, s[64:67], s84 offen lds\n" \
"v_mfma_f32_16x16x32_bf16 a[80:83], v[68:71], v[148:151], a[80:83]\n" \
"s_add_u32 m0, %[mB], 0xc00\n" \
"s_add_u32 s85, s63, 0x18000\n" \
"v_mfma_f32_16x16x32_bf16 a[112:115], v[76:79], v[148:151], a[112:115]\n" \
"ds_read_b128 v[112:115], v29 offset:38912\n" \
"s_add_u32 s43, s99, s101\n" \
"s_nop 0\n" \
"v_mfma_f32_16x16x32_bf16 a[112:115], v[80:83], v[152:155], a[112:115]\n" \
"buffer_load_dwordx4 v27, s[68:71], s85 offen lds\n" \
"v_mfma_f32_16x16x32_bf16 a[20:23], v[56:59], v[160:163], a[20:23]\n" \
"s_add_u32 m0, %[mB], 0x4c00\n" \
"s_add_u32 s84, s63, 0x18000\n" \
"v_mfma_f32_16x16x32_bf16 a[20:23], v[52:55], v[156:159], a[20:23]\n" \
"s_mul_i32 s89, 0x1800, s100\n" \
"v_mov_b32_e32 v223, 0x3020c0c\n" \
"v_mfma_f32_16x16x32_bf16 a[52:55], v[60:63], v[156:159], a[52:55]\n" \
"buffer_load_dwordx4 v27, s[72:75], s84 offen lds\n" \
"v_mfma_f32_16x16x32_bf16 a[52:55], v[64:67], v[160:163], a[52:55]\n" \
"s_mov_b32 m0, %[mS]\n" \
"v_mov_b32_e32 v224, 0xc0c0302\n" \
"v_mfma_f32_16x16x32_bf16 a[84:87], v[72:75], v[160:163], a[84:87]\n" \
"v_cmp_ne_u32_e32 vcc, 0, v216\n" \
"s_lshl_b32 s98, s94, 16\n" \
"v_mfma_f32_16x16x32_bf16 a[84:87], v[68:71], v[156:159], a[84:87]\n" \
"s_add_u32 s97, s43, s89\n" \
"v_add_u32_e64 v222, 2, v217\n" \
"v_mfma_f32_16x16x32_bf16 a[116:119], v[76:79], v[156:159], a[116:119]\n" \
"v_cndmask_b32_e32 v225, v224, v223, vcc\n" \
"s_add_u32 s42, s99, s98\n" \
"v_mfma_f32_16x16x32_bf16 a[116:119], v[80:83], v[160:163], a[116:119]\n" \
"s_add_u32 s45, 0x4000, s97\n" \
"s_lshr_b32 s94, %[w], 1\n" \
"v_mfma_f32_16x16x32_bf16 a[24:27], v[56:59], v[168:171], a[24:27]\n" \
"s_and_b32 s95, 1, %[w]\n" \
"v_mov_b32_e32 v226, 0xc0c0c0c\n" \
"v_mfma_f32_16x16x32_bf16 a[24:27], v[52:55], v[164:167], a[24:27]\n" \
"v_cmp_le_u32_e32 vcc, v222, v215\n" \
"s_add_u32 s88, s42, s89\n" \
"v_mfma_f32_16x16x32_bf16 a[56:59], v[60:63], v[164:167], a[56:59]\n" \
"s_add_u32 s96, s94, s95\n" \
"s_add_u32 s97, 1, s94\n" \
"v_mfma_f32_16x16x32_bf16 a[56:59], v[64:67], v[168:171], a[56:59]\n" \
"s_waitcnt vmcnt(18)\n" \
"v_perm_b32 v252, v209, v208, s87\n" \
"v_mfma_f32_16x16x32_bf16 a[88:91], v[72:75], v[168:171], a[88:91]\n" \
"s_mov_b32 s87, 0x7060302\n" \
"v_perm_b32 v253, v209, v208, s87\n" \
"v_mfma_f32_16x16x32_bf16 a[88:91], v[68:71], v[164:167], a[88:91]\n" \
"s_mov_b32 s87, 0x5040100\n" \
"s_waitcnt vmcnt(16)\n" \
"v_perm_b32 v254, v211, v210, s87\n" \
"s_mov_b32 exec_lo, 0x15\n" \
"s_mov_b32 exec_hi, 0x180000\n" \
"buffer_load_dword v11, s[80:83], 0 offen sc1 lds\n" \
"s_mov_b64 exec, -1\n" \
"v_mfma_f32_16x16x32_bf16 a[120:123], v[76:79], v[164:167], a[120:123]\n" \
"s_mov_b32 s87, 0x7060100\n" \
"v_perm_b32 v255, v211, v210, s87\n" \
"v_mfma_f32_16x16x32_bf16 a[120:123], v[80:83], v[168:171], a[120:123]\n" \
"s_mov_b32 m0, %[mS]\n" \
"s_mov_b32 s86, s10\n" \
"v_mfma_f32_16x16x32_bf16 a[28:31], v[56:59], v[176:179], a[28:31]\n" \
"s_mov_b32 s87, s11\n" \
"ds_write2_b32 v198, v252, v253 offset0:0 offset1:2\n" \
"v_mfma_f32_16x16x32_bf16 a[28:31], v[52:55], v[172:175], a[28:31]\n" \
"ds_write_b128 v196, v[200:203]\n" \
"v_mfma_f32_16x16x32_bf16 a[60:63], v[60:63], v[172:175], a[60:63]\n" \
"ds_write2_b32 v198, v254, v255 offset0:4 offset1:6\n" \
"v_cndmask_b32_e32 v41, v225, v226, vcc\n" \
"s_mov_b32 exec_lo, 0x3ffff\n" \
"s_mov_b32 exec_hi, 0x0\n" \
"global_load_lds_dword v188, s[86:87]\n" \
"s_mov_b64 exec, -1\n" \
"v_mfma_f32_16x16x32_bf16 a[60:63], v[64:67], v[176:179], a[60:63]\n" \
"ds_write_b128 v197, v[204:207]\n" \
"v_mfma_f32_16x16x32_bf16 a[92:95], v[72:75], v[176:179], a[92:95]\n" \
"v_and_b32_e64 v230, 3, v229\n" \
"s_add_u32 s44, 0x4000, s88\n" \
"v_mfma_f32_16x16x32_bf16 a[92:95], v[68:71], v[172:175], a[92:95]\n" \
"s_lshl_b32 s59, s96, 13\n" \
"s_sub_u32 s98, s97, s95\n" \
"v_mfma_f32_16x16x32_bf16 a[124:127], v[76:79], v[172:175], a[124:127]\n" \
"s_lshl_b32 s99, s94, 13\n" \
"s_mul_i32 s101, 0x1800, s94\n" \
"v_mfma_f32_16x16x32_bf16 a[124:127], v[80:83], v[176:179], a[124:127]\n" \
"v_mov_b32_e32 v227, 0xc0c0c0c\n" \
"v_cmp_le_u32_e32 vcc, v217, v215\n" \
"v_mfma_f32_16x16x32_bf16 a[0:3], v[120:123], v[56:59], a[0:3]\n" \
"v_add_u32_e32 v231, v228, v230\n" \
"s_lshl_b32 s60, s98, 13\n" \
"v_mfma_f32_16x16x32_bf16 a[0:3], v[116:119], v[52:55], a[0:3]\n" \
"s_add_u32 s100, 0x1800, s99\n" \
"s_sub_u32 s88, 0x17000, s101\n" \
"v_mfma_f32_16x16x32_bf16 a[32:35], v[116:119], v[60:63], a[32:35]\n" \
"s_lshl_b32 s96, s94, 15\n" \
"v_cndmask_b32_e32 v42, v227, v220, vcc\n" \
"v_mfma_f32_16x16x32_bf16 a[32:35], v[120:123], v[64:67], a[32:35]\n" \
"v_lshlrev_b32_e32 v232, 7, v231\n" \
"s_add_u32 s97, 0x10000, s96\n" \
"v_mfma_f32_16x16x32_bf16 a[64:67], v[120:123], v[72:75], a[64:67]\n" \
"s_cmp_lg_u32 s95, 0\n" \
"s_cselect_b32 s61, s100, s88\n" \
"v_mfma_f32_16x16x32_bf16 a[64:67], v[116:119], v[68:71], a[64:67]\n" \
"s_lshl_b32 s98, s95, 11\n" \
"v_mov_b32_e32 v228, 0xc0c0c0c\n" \
"v_mfma_f32_16x16x32_bf16 a[96:99], v[116:119], v[76:79], a[96:99]\n" \
"v_cmp_le_u32_e32 vcc, v222, v215\n" \
"v_add_u32_e32 v233, 0x14000, v232\n" \
"v_mfma_f32_16x16x32_bf16 a[96:99], v[120:123], v[80:83], a[96:99]\n" \
"v_lshlrev_b32_e32 v235, 3, v234\n" \
"s_add_u32 s101, s97, s98\n" \
"v_mfma_f32_16x16x32_bf16 a[4:7], v[128:131], v[56:59], a[4:7]\n" \
"v_cndmask_b32_e32 v43, v228, v225, vcc\n" \
"v_and_b32_e64 v231, 2, v212\n" \
"v_mfma_f32_16x16x32_bf16 a[4:7], v[124:127], v[52:55], a[4:7]\n" \
"v_add_u32_e32 v236, v233, v235\n" \
"v_lshrrev_b32_e32 v237, 1, v230\n" \
"v_mfma_f32_16x16x32_bf16 a[36:39], v[124:127], v[60:63], a[36:39]\n" \
"v_add_u32_e32 v243, v242, v230\n" \
"s_add_u32 s100, s101, s99\n" \
"v_mfma_f32_16x16x32_bf16 a[36:39], v[128:131], v[64:67], a[36:39]\n" \
"v_and_b32_e64 v229, 3, v212\n" \
"v_mov_b32_e32 v232, 0x3f80\n" \
"v_mfma_f32_16x16x32_bf16 a[68:71], v[128:131], v[72:75], a[68:71]\n" \
"v_cmp_eq_u32_e32 vcc, 0, v231\n" \
"v_lshlrev_b32_e32 v239, 1, v238\n" \
"v_mfma_f32_16x16x32_bf16 a[68:71], v[124:127], v[68:71], a[68:71]\n" \
"v_lshlrev_b32_e32 v244, 7, v243\n" \
"s_add_u32 s88, 0x4000, s100\n" \
"v_mfma_f32_16x16x32_bf16 a[100:103], v[124:127], v[76:79], a[100:103]\n" \
"v_or_b32_e32 v230, 2, v229\n" \
"v_cndmask_b32_e32 v233, 0, v232, vcc\n" \
"v_mfma_f32_16x16x32_bf16 a[100:103], v[128:131], v[80:83], a[100:103]\n" \
"v_or_b32_e32 v240, v237, v239\n" \
"s_waitcnt lgkmcnt(0)\n" \
"v_mfma_f32_16x16x32_bf16 a[8:11], v[136:139], v[56:59], a[8:11]\n" \
"v_add_u32_e32 v245, 0x14000, v244\n" \
"v_cmp_eq_u32_e32 vcc, v230, v213\n" \
"v_mfma_f32_16x16x32_bf16 a[8:11], v[132:135], v[52:55], a[8:11]\n" \
"s_cmp_lg_u32 s95, 0\n" \
"s_cselect_b32 s47, s88, s101\n" \
"v_mfma_f32_16x16x32_bf16 a[40:43], v[132:135], v[60:63], a[40:43]\n" \
"v_lshlrev_b32_e32 v241, 5, v240\n" \
"v_add_u32_e32 v246, v245, v235\n" \
"v_mfma_f32_16x16x32_bf16 a[40:43], v[136:139], v[64:67], a[40:43]\n" \
"v_cndmask_b32_e32 v46, 0, v233, vcc\n" \
"v_and_b32_e32 v234, 51, v212\n" \
"v_mfma_f32_16x16x32_bf16 a[72:75], v[136:139], v[72:75], a[72:75]\n" \
"v_add_u32_e64 v190, v236, v241\n" \
"v_mov_b32_e32 v235, 0x3f80\n" \
"v_mfma_f32_16x16x32_bf16 a[72:75], v[132:135], v[68:71], a[72:75]\n" \
"v_cmp_eq_u32_e32 vcc, 2, v234\n" \
"s_lshl_b32 s88, s95, 4\n" \
"v_mfma_f32_16x16x32_bf16 a[104:107], v[132:135], v[76:79], a[104:107]\n" \
"v_xor_b32_e32 v247, 3, v237\n" \
"v_cndmask_b32_e32 v47, 0, v235, vcc\n" \
"v_mfma_f32_16x16x32_bf16 a[104:107], v[136:139], v[80:83], a[104:107]\n" \
"v_add_u32_e64 v236, s88, v215\n" \
"v_mov_b32_e32 v237, 0xffff\n" \
"v_mfma_f32_16x16x32_bf16 a[12:15], v[144:147], v[56:59], a[12:15]\n" \
"s_add_u32 s62, s22, 0x280\n" \
"s_and_b32 s62, s62, 0x3ff\n" \
"v_mfma_f32_16x16x32_bf16 a[12:15], v[140:143], v[52:55], a[12:15]\n" \
"s_lshl_b32 s63, s62, 12\n" \
"v_cmp_le_u32_e32 vcc, v214, v236\n" \
"v_mfma_f32_16x16x32_bf16 a[44:47], v[140:143], v[60:63], a[44:47]\n" \
"v_mov_b32_e32 v239, 0xffff0000\n" \
"v_cndmask_b32_e32 v238, 0, v237, vcc\n" \
"v_mfma_f32_16x16x32_bf16 a[44:47], v[144:147], v[64:67], a[44:47]\n" \
"v_cmp_lt_u32_e32 vcc, v214, v236\n" \
"v_add_u32_e64 v241, 2, v214\n" \
"v_mfma_f32_16x16x32_bf16 a[76:79], v[144:147], v[72:75], a[76:79]\n" \
"v_cndmask_b32_e32 v240, 0, v239, vcc\n" \
"v_mov_b32_e32 v242, 0xffff\n" \
"v_mfma_f32_16x16x32_bf16 a[76:79], v[140:143], v[68:71], a[76:79]\n" \
"v_cmp_le_u32_e32 vcc, v241, v236\n" \
"v_lshlrev_b32_e32 v248, 5, v247\n" \
"v_mfma_f32_16x16x32_bf16 a[108:111], v[140:143], v[76:79], a[108:111]\n" \
"v_cndmask_b32_e32 v243, 0, v242, vcc\n" \
"v_add_u32_e64 v39, v246, v248\n" \
"v_mfma_f32_16x16x32_bf16 a[108:111], v[144:147], v[80:83], a[108:111]\n" \
"v_mov_b32_e32 v244, 0xffff0000\n" \
"v_cmp_lt_u32_e32 vcc, v241, v236\n" \
"s_waitcnt vmcnt(2)\n" \
"s_barrier\n" \
"v_mfma_f32_16x16x32_bf16 a[128:131], v[120:123], v[88:91], a[128:131]\n" \
"s_add_u32 m0, %[mA], 0x8000\n" \
"s_add_u32 s85, s62, 0x0\n" \
"v_mfma_f32_16x16x32_bf16 a[128:131], v[116:119], v[84:87], a[128:131]\n" \
"buffer_load_dwordx4 v22, s[64:67], s85 offen lds\n" \
"v_mfma_f32_16x16x32_bf16 a[160:163], v[116:119], v[92:95], a[160:163]\n" \
"ds_read_b128 v[56:59], v29 offset:16384\n" \
"ds_read_b128 v[52:55], v28 offset:16384\n" \
"v_mfma_f32_16x16x32_bf16 a[160:163], v[120:123], v[96:99], a[160:163]\n" \
"s_add_u32 m0, %[mA], 0x8400\n" \
"s_add_u32 s84, s62, 0x2000\n" \
"v_mfma_f32_16x16x32_bf16 a[192:195], v[120:123], v[104:107], a[192:195]\n" \
"buffer_load_dwordx4 v23, s[64:67], s84 offen lds\n" \
"v_mfma_f32_16x16x32_bf16 a[192:195], v[116:119], v[100:103], a[192:195]\n" \
"ds_read_b128 v[60:63], v28 offset:18432\n" \
"ds_read_b128 v[64:67], v29 offset:18432\n" \
"v_mfma_f32_16x16x32_bf16 a[224:227], v[116:119], v[108:111], a[224:227]\n" \
"ds_read_b64_tr_b16 v[116:117], v32 offset:0\n" \
"ds_read_b64_tr_b16 v[118:119], v32 offset:1024\n" \
"v_mfma_f32_16x16x32_bf16 a[224:227], v[120:123], v[112:115], a[224:227]\n" \
"s_add_u32 m0, %[mB], 0x8000\n" \
"s_add_u32 s85, s63, 0x0\n" \
"v_mfma_f32_16x16x32_bf16 a[132:135], v[128:131], v[88:91], a[132:135]\n" \
"buffer_load_dwordx4 v26, s[68:71], s85 offen lds\n" \
"v_mfma_f32_16x16x32_bf16 a[132:135], v[124:127], v[84:87], a[132:135]\n" \
"ds_read_b64_tr_b16 v[120:121], v32 offset:8192\n" \
"ds_read_b64_tr_b16 v[122:123], v32 offset:9216\n" \
"v_mfma_f32_16x16x32_bf16 a[164:167], v[124:127], v[92:95], a[164:167]\n" \
"ds_read_b128 v[72:75], v29 offset:20480\n" \
"ds_read_b128 v[68:71], v28 offset:20480\n" \
"v_mfma_f32_16x16x32_bf16 a[164:167], v[128:131], v[96:99], a[164:167]\n" \
"s_add_u32 m0, %[mB], 0xc000\n" \
"s_add_u32 s84, s63, 0x0\n" \
"v_mfma_f32_16x16x32_bf16 a[196:199], v[128:131], v[104:107], a[196:199]\n" \
"buffer_load_dwordx4 v26, s[72:75], s84 offen lds\n" \
"v_mfma_f32_16x16x32_bf16 a[196:199], v[124:127], v[100:103], a[196:199]\n" \
"ds_read_b128 v[76:79], v28 offset:22528\n" \
"ds_read_b128 v[80:83], v29 offset:22528\n" \
"v_mfma_f32_16x16x32_bf16 a[228:231], v[124:127], v[108:111], a[228:231]\n" \
"ds_read_b64_tr_b16 v[124:125], v33 offset:0\n" \
"ds_read_b64_tr_b16 v[126:127], v33 offset:1024\n" \
"v_mfma_f32_16x16x32_bf16 a[228:231], v[128:131], v[112:115], a[228:231]\n" \
"s_add_u32 m0, %[mA], 0x8800\n" \
"s_add_u32 s85, s62, 0x4000\n" \
"v_mfma_f32_16x16x32_bf16 a[136:139], v[136:139], v[88:91], a[136:139]\n" \
"buffer_load_dwordx4 v22, s[64:67], s85 offen lds\n" \
"v_mfma_f32_16x16x32_bf16 a[136:139], v[132:135], v[84:87], a[136:139]\n" \
"ds_read_b64_tr_b16 v[128:129], v33 offset:8192\n" \
"ds_read_b64_tr_b16 v[130:131], v33 offset:9216\n" \
"v_mfma_f32_16x16x32_bf16 a[168:171], v[132:135], v[92:95], a[168:171]\n" \
"ds_read_b128 v[200:203], v199 offset:0\n" \
"ds_read_b128 v[204:207], v199 offset:4096\n" \
"v_mfma_f32_16x16x32_bf16 a[168:171], v[136:139], v[96:99], a[168:171]\n" \
"s_add_u32 m0, %[mA], 0x8c00\n" \
"s_add_u32 s84, s62, 0x6000\n" \
"v_mfma_f32_16x16x32_bf16 a[200:203], v[136:139], v[104:107], a[200:203]\n" \
"buffer_load_dwordx4 v23, s[64:67], s84 offen lds\n" \
"v_mfma_f32_16x16x32_bf16 a[200:203], v[132:135], v[100:103], a[200:203]\n" \
"ds_read_b128 v[208:211], v199 offset:8192\n" \
"v_cndmask_b32_e32 v245, 0, v244, vcc\n" \
"s_nop 0\n" \
"v_mfma_f32_16x16x32_bf16 a[232:235], v[132:135], v[108:111], a[232:235]\n" \
"ds_read_b64_tr_b16 v[132:133], v34 offset:0\n" \
"ds_read_b64_tr_b16 v[134:135], v34 offset:1024\n" \
"v_mfma_f32_16x16x32_bf16 a[232:235], v[136:139], v[112:115], a[232:235]\n" \
"s_add_u32 m0, %[mB], 0x8400\n" \
"s_add_u32 s85, s63, 0x8000\n" \
"v_mfma_f32_16x16x32_bf16 a[140:143], v[144:147], v[88:91], a[140:143]\n" \
"buffer_load_dwordx4 v26, s[68:71], s85 offen lds\n" \
"v_mfma_f32_16x16x32_bf16 a[140:143], v[140:143], v[84:87], a[140:143]\n" \
"ds_read_b64_tr_b16 v[136:137], v34 offset:8192\n" \
"ds_read_b64_tr_b16 v[138:139], v34 offset:9216\n" \
"v_mfma_f32_16x16x32_bf16 a[172:175], v[140:143], v[92:95], a[172:175]\n" \
"v_add_u32_e64 v246, 16, v214\n" \
"v_mov_b32_e32 v247, 0xffff\n" \
"v_mfma_f32_16x16x32_bf16 a[172:175], v[144:147], v[96:99], a[172:175]\n" \
"s_add_u32 m0, %[mB], 0xc400\n" \
"s_add_u32 s84, s63, 0x8000\n" \
"v_mfma_f32_16x16x32_bf16 a[204:207], v[144:147], v[104:107], a[204:207]\n" \
"buffer_load_dwordx4 v26, s[72:75], s84 offen lds\n" \
"v_mfma_f32_16x16x32_bf16 a[204:207], v[140:143], v[100:103], a[204:207]\n" \
"v_cmp_le_u32_e32 vcc, v246, v236\n" \
"s_nop 0\n" \
"v_mov_b32_e32 v249, 0xffff0000\n" \
"v_mfma_f32_16x16x32_bf16 a[236:239], v[140:143], v[108:111], a[236:239]\n" \
"ds_read_b64_tr_b16 v[140:141], v35 offset:0\n" \
"ds_read_b64_tr_b16 v[142:143], v35 offset:1024\n" \
"v_mfma_f32_16x16x32_bf16 a[236:239], v[144:147], v[112:115], a[236:239]\n" \
"s_add_u32 m0, %[mA], 0x9000\n" \
"s_add_u32 s85, s62, 0x8000\n" \
"v_mfma_f32_16x16x32_bf16 a[144:147], v[88:91], v[152:155], a[144:147]\n" \
"buffer_load_dwordx4 v22, s[64:67], s85 offen lds\n" \
"v_mfma_f32_16x16x32_bf16 a[144:147], v[84:87], v[148:151], a[144:147]\n" \
"ds_read_b64_tr_b16 v[144:145], v35 offset:8192\n" \
"ds_read_b64_tr_b16 v[146:147], v35 offset:9216\n" \
"v_mfma_f32_16x16x32_bf16 a[176:179], v[92:95], v[148:151], a[176:179]\n" \
"v_cndmask_b32_e32 v248, 0, v247, vcc\n" \
"v_cmp_lt_u32_e32 vcc, v246, v236\n" \
"v_mfma_f32_16x16x32_bf16 a[176:179], v[96:99], v[152:155], a[176:179]\n" \
"s_add_u32 m0, %[mA], 0x9400\n" \
"s_add_u32 s84, s62, 0xa000\n" \
"v_mfma_f32_16x16x32_bf16 a[208:211], v[104:107], v[152:155], a[208:211]\n" \
"buffer_load_dwordx4 v23, s[64:67], s84 offen lds\n" \
"v_mfma_f32_16x16x32_bf16 a[208:211], v[100:103], v[148:151], a[208:211]\n" \
"s_lshl_b32 s89, %[w], 8\n" \
"v_cndmask_b32_e32 v250, 0, v249, vcc\n" \
"v_mfma_f32_16x16x32_bf16 a[240:243], v[108:111], v[148:151], a[240:243]\n" \
"ds_read_b64_tr_b16 v[148:149], v32 offset:16384\n" \
"ds_read_b64_tr_b16 v[150:151], v32 offset:17408\n" \
"v_mfma_f32_16x16x32_bf16 a[240:243], v[112:115], v[152:155], a[240:243]\n" \
"s_add_u32 m0, %[mB], 0x8800\n" \
"s_add_u32 s85, s63, 0x10000\n" \
"v_mfma_f32_16x16x32_bf16 a[148:151], v[88:91], v[160:163], a[148:151]\n" \
"buffer_load_dwordx4 v27, s[68:71], s85 offen lds\n" \
"v_mfma_f32_16x16x32_bf16 a[148:151], v[84:87], v[156:159], a[148:151]\n" \
"ds_read_b64_tr_b16 v[152:153], v32 offset:24576\n" \
"ds_read_b64_tr_b16 v[154:155], v32 offset:25600\n" \
"v_mfma_f32_16x16x32_bf16 a[180:183], v[92:95], v[156:159], a[180:183]\n" \
"v_add_u32_e64 v251, 18, v214\n" \
"s_add_u32 s46, 0x27000, s89\n" \
"v_mfma_f32_16x16x32_bf16 a[180:183], v[96:99], v[160:163], a[180:183]\n" \
"s_add_u32 m0, %[mB], 0xc800\n" \
"s_add_u32 s84, s63, 0x10000\n" \
"v_mfma_f32_16x16x32_bf16 a[212:215], v[104:107], v[160:163], a[212:215]\n" \
"buffer_load_dwordx4 v27, s[72:75], s84 offen lds\n" \
"v_mfma_f32_16x16x32_bf16 a[212:215], v[100:103], v[156:159], a[212:215]\n" \
"v_mov_b32_e32 v218, 0xffff\n" \
"v_cmp_le_u32_e32 vcc, v251, v236\n" \
"v_mfma_f32_16x16x32_bf16 a[244:247], v[108:111], v[156:159], a[244:247]\n" \
"ds_read_b64_tr_b16 v[156:157], v33 offset:16384\n" \
"ds_read_b64_tr_b16 v[158:159], v33 offset:17408\n" \
"v_mfma_f32_16x16x32_bf16 a[244:247], v[112:115], v[160:163], a[244:247]\n" \
"ds_read_b64_tr_b16 v[160:161], v33 offset:24576\n" \
"ds_read_b64_tr_b16 v[162:163], v33 offset:25600\n" \
"v_mfma_f32_16x16x32_bf16 a[152:155], v[88:91], v[168:171], a[152:155]\n" \
"s_add_u32 s89, 0xffffc000, s101\n" \
"v_cndmask_b32_e32 v219, 0, v218, vcc\n" \
"v_mfma_f32_16x16x32_bf16 a[152:155], v[84:87], v[164:167], a[152:155]\n" \
"v_lshlrev_b32_e32 v224, 3, v212\n" \
"s_lshr_b32 s94, %[b], 5\n" \
"v_mfma_f32_16x16x32_bf16 a[184:187], v[92:95], v[164:167], a[184:187]\n" \
"s_cmp_lg_u32 s95, 0\n" \
"s_cselect_b32 s48, s89, s100\n" \
"v_mfma_f32_16x16x32_bf16 a[184:187], v[96:99], v[168:171], a[184:187]\n" \
"s_mov_b32 s96, 0xc000\n" \
"s_mov_b32 s97, 0x8000\n" \
"v_mfma_f32_16x16x32_bf16 a[216:219], v[104:107], v[168:171], a[216:219]\n" \
"v_mov_b32_e32 v221, 0xffff0000\n" \
"v_cmp_lt_u32_e32 vcc, v251, v236\n" \
"v_mfma_f32_16x16x32_bf16 a[216:219], v[100:103], v[164:167], a[216:219]\n" \
"v_lshrrev_b32_e32 v212, 4, v20\n" \
"v_cndmask_b32_e32 v216, 0, v221, vcc\n" \
"v_mfma_f32_16x16x32_bf16 a[248:251], v[108:111], v[164:167], a[248:251]\n" \
"ds_read_b64_tr_b16 v[164:165], v34 offset:16384\n" \
"ds_read_b64_tr_b16 v[166:167], v34 offset:17408\n" \
"v_mfma_f32_16x16x32_bf16 a[248:251], v[112:115], v[168:171], a[248:251]\n" \
"ds_read_b64_tr_b16 v[168:169], v34 offset:24576\n" \
"ds_read_b64_tr_b16 v[170:171], v34 offset:25600\n" \
"v_mfma_f32_16x16x32_bf16 a[156:159], v[88:91], v[176:179], a[156:159]\n" \
"ds_read_b128 v[88:91], v29 offset:0\n" \
"v_lshlrev_b32_e32 v223, 2, v236\n" \
"v_mfma_f32_16x16x32_bf16 a[156:159], v[84:87], v[172:175], a[156:159]\n" \
"ds_read_b128 v[84:87], v28 offset:0\n" \
"s_lshl_b32 s101, %[w], 9\n" \
"v_mfma_f32_16x16x32_bf16 a[188:191], v[92:95], v[172:175], a[188:191]\n" \
"ds_read_b128 v[92:95], v28 offset:2048\n" \
"v_mul_u32_u24_e32 v213, 0xe39, v212\n" \
"v_mfma_f32_16x16x32_bf16 a[188:191], v[96:99], v[176:179], a[188:191]\n" \
"ds_read_b128 v[96:99], v29 offset:2048\n" \
"v_or_b32_e32 v15, v219, v216\n" \
"v_mfma_f32_16x16x32_bf16 a[220:223], v[104:107], v[176:179], a[220:223]\n" \
"ds_read_b128 v[104:107], v29 offset:4096\n" \
"v_add_u32_e32 v18, 0x27000, v223\n" \
"v_mfma_f32_16x16x32_bf16 a[220:223], v[100:103], v[172:175], a[220:223]\n" \
"ds_read_b128 v[100:103], v28 offset:4096\n" \
"s_add_u32 s89, 0x27800, s101\n" \
"v_mfma_f32_16x16x32_bf16 a[252:255], v[108:111], v[172:175], a[252:255]\n" \
"ds_read_b64_tr_b16 v[172:173], v35 offset:16384\n" \
"ds_read_b64_tr_b16 v[174:175], v35 offset:17408\n" \
"v_mfma_f32_16x16x32_bf16 a[252:255], v[112:115], v[176:179], a[252:255]\n" \
"s_add_u32 m0, %[mA], 0x9800\n" \
"s_waitcnt lgkmcnt(12)\n" \
"s_nop 0\n" \
"v_mfma_f32_16x16x32_bf16 a[0:3], v[120:123], v[56:59], a[0:3]\n" \
"s_add_u32 s85, s62, 0xc000\n" \
"ds_read_b64_tr_b16 v[176:177], v35 offset:24576\n" \
"v_mfma_f32_16x16x32_bf16 a[0:3], v[116:119], v[52:55], a[0:3]\n" \
"buffer_load_dwordx4 v22, s[64:67], s85 offen lds\n" \
"v_mfma_f32_16x16x32_bf16 a[32:35], v[116:119], v[60:63], a[32:35]\n" \
"s_add_u32 m0, %[mA], 0x9c00\n" \
"s_add_u32 s84, s62, 0xe000\n" \
"v_mfma_f32_16x16x32_bf16 a[32:35], v[120:123], v[64:67], a[32:35]\n" \
"ds_read_b64_tr_b16 v[178:179], v35 offset:25600\n" \
"ds_read_b128 v[108:111], v28 offset:6144\n" \
"v_mfma_f32_16x16x32_bf16 a[64:67], v[120:123], v[72:75], a[64:67]\n" \
"L_pf2:\n" \
"s_cbranch_execz L_pf6\n" \
"s_nop 0\n" \
"buffer_load_dwordx4 v23, s[64:67], s84 offen lds\n" \
"v_mfma_f32_16x16x32_bf16 a[64:67], v[116:119], v[68:71], a[64:67]\n" \
"s_add_u32 m0, %[mB], 0x8c00\n" \
"s_add_u32 s85, s63, 0x18000\n" \
"v_mfma_f32_16x16x32_bf16 a[96:99], v[116:119], v[76:79], a[96:99]\n" \
"ds_read_b128 v[112:115], v29 offset:6144\n" \
"v_lshrrev_b32_e32 v214, 16, v213\n" \
"v_mfma_f32_16x16x32_bf16 a[96:99], v[120:123], v[80:83], a[96:99]\n" \
"buffer_load_dwordx4 v27, s[68:71], s85 offen lds\n" \
"v_mfma_f32_16x16x32_bf16 a[4:7], v[128:131], v[56:59], a[4:7]\n" \
"s_add_u32 m0, %[mB], 0xcc00\n" \
"s_add_u32 s84, s63, 0x18000\n" \
"v_mfma_f32_16x16x32_bf16 a[4:7], v[124:127], v[52:55], a[4:7]\n" \
"s_cmp_lg_u32 s94, 0\n" \
"s_cselect_b32 s53, s96, 0x8000\n" \
"v_mfma_f32_16x16x32_bf16 a[36:39], v[124:127], v[60:63], a[36:39]\n" \
"buffer_load_dwordx4 v27, s[72:75], s84 offen lds\n" \
"v_mfma_f32_16x16x32_bf16 a[36:39], v[128:131], v[64:67], a[36:39]\n" \
"s_cmp_lg_u32 s94, 0\n" \
"s_cselect_b32 s98, s97, 0x4000\n" \
"v_mfma_f32_16x16x32_bf16 a[68:71], v[128:131], v[72:75], a[68:71]\n" \
"v_add_u32_e32 v222, 64, v212\n" \
"v_add_u32_e64 v19, s89, v224\n" \
"v_mfma_f32_16x16x32_bf16 a[68:71], v[124:127], v[68:71], a[68:71]\n" \
"buffer_store_dwordx4 v[200:203], v20, s[80:83], s57 offen sc1\n" \
"v_mfma_f32_16x16x32_bf16 a[100:103], v[124:127], v[76:79], a[100:103]\n" \
"v_mul_u32_u24_e32 v216, 18, v214\n" \
"s_and_b32 s94, 7, %[b]\n" \
"v_mfma_f32_16x16x32_bf16 a[100:103], v[128:131], v[80:83], a[100:103]\n" \
"v_mul_u32_u24_e32 v223, 0xe39, v222\n" \
"v_sub_u32_e32 v217, v212, v216\n" \
"v_mfma_f32_16x16x32_bf16 a[8:11], v[136:139], v[56:59], a[8:11]\n" \
"s_lshl_b32 s95, s94, 5\n" \
"v_lshrrev_b32_e32 v224, 16, v223\n" \
"v_mfma_f32_16x16x32_bf16 a[8:11], v[132:135], v[52:55], a[8:11]\n" \
"v_add_u32_e32 v233, 0x80, v212\n" \
"s_add_u32 s96, 32, s95\n" \
"v_mfma_f32_16x16x32_bf16 a[40:43], v[132:135], v[60:63], a[40:43]\n" \
"buffer_store_dwordx4 v[204:207], v20, s[80:83], s56 offen sc1\n" \
"v_mfma_f32_16x16x32_bf16 a[40:43], v[136:139], v[64:67], a[40:43]\n" \
"v_lshlrev_b32_e32 v218, 4, v217\n" \
"v_mul_u32_u24_e32 v226, 18, v224\n" \
"v_mfma_f32_16x16x32_bf16 a[72:75], v[136:139], v[72:75], a[72:75]\n" \
"v_mul_u32_u24_e32 v234, 0xe39, v233\n" \
"v_or_b32_e32 v13, v243, v245\n" \
"v_mfma_f32_16x16x32_bf16 a[72:75], v[132:135], v[68:71], a[72:75]\n" \
"v_add_u32_e32 v219, s96, v218\n" \
"v_sub_u32_e32 v227, v222, v226\n" \
"v_mfma_f32_16x16x32_bf16 a[104:107], v[132:135], v[76:79], a[104:107]\n" \
"v_lshrrev_b32_e32 v235, 16, v234\n" \
"v_add_u32_e32 v244, 0xc0, v212\n" \
"v_mfma_f32_16x16x32_bf16 a[104:107], v[136:139], v[80:83], a[104:107]\n" \
"v_or_b32_e32 v12, v238, v240\n" \
"v_and_b32_e32 v220, 0xff, v219\n" \
"v_mfma_f32_16x16x32_bf16 a[12:15], v[144:147], v[56:59], a[12:15]\n" \
"buffer_store_dwordx4 v[208:211], v20, s[80:83], s58 offen sc1\n" \
"v_mfma_f32_16x16x32_bf16 a[12:15], v[140:143], v[52:55], a[12:15]\n" \
"v_cmp_gt_u32_e32 vcc, 16, v217\n" \
"v_lshlrev_b32_e32 v228, 4, v227\n" \
"v_mfma_f32_16x16x32_bf16 a[44:47], v[140:143], v[60:63], a[44:47]\n" \
"v_mul_u32_u24_e32 v237, 18, v235\n" \
"v_mul_u32_u24_e32 v245, 0xe39, v244\n" \
"v_mfma_f32_16x16x32_bf16 a[44:47], v[144:147], v[64:67], a[44:47]\n" \
"v_or_b32_e32 v14, v248, v250\n" \
"v_cndmask_b32_e32 v221, v218, v220, vcc\n" \
"v_mfma_f32_16x16x32_bf16 a[76:79], v[144:147], v[72:75], a[76:79]\n" \
"v_add_u32_e32 v229, s96, v228\n" \
"v_sub_u32_e32 v238, v233, v237\n" \
"v_mfma_f32_16x16x32_bf16 a[76:79], v[140:143], v[68:71], a[76:79]\n" \
"v_lshrrev_b32_e32 v246, 16, v245\n" \
"v_add_u32_e32 v219, 0x100, v212\n" \
"v_mfma_f32_16x16x32_bf16 a[108:111], v[140:143], v[76:79], a[108:111]\n" \
"s_mov_b32 m0, %[mS]\n" \
"s_mov_b32 s86, s10\n" \
"v_mfma_f32_16x16x32_bf16 a[108:111], v[144:147], v[80:83], a[108:111]\n" \
"s_mov_b32 s87, s11\n" \
"v_mul_u32_u24_e32 v215, 0x480, v214\n" \
"v_mfma_f32_16x16x32_bf16 a[16:19], v[56:59], v[152:155], a[16:19]\n" \
"v_and_b32_e32 v230, 0xff, v229\n" \
"v_cmp_gt_u32_e32 vcc, 16, v227\n" \
"v_mfma_f32_16x16x32_bf16 a[16:19], v[52:55], v[148:151], a[16:19]\n" \
"v_lshlrev_b32_e32 v239, 4, v238\n" \
"v_mul_u32_u24_e32 v248, 18, v246\n" \
"v_mfma_f32_16x16x32_bf16 a[48:51], v[60:63], v[148:151], a[48:51]\n" \
"v_mul_u32_u24_e32 v217, 0xe39, v219\n" \
"v_add_u32_e32 v0, v215, v221\n" \
"v_mfma_f32_16x16x32_bf16 a[48:51], v[64:67], v[152:155], a[48:51]\n" \
"v_cndmask_b32_e32 v231, v228, v230, vcc\n" \
"v_add_u32_e32 v240, s96, v239\n" \
"v_mfma_f32_16x16x32_bf16 a[80:83], v[72:75], v[152:155], a[80:83]\n" \
"v_sub_u32_e32 v249, v244, v248\n" \
"v_lshrrev_b32_e32 v220, 16, v217\n" \
"s_mov_b32 exec_lo, 0xfffc0000\n" \
"s_mov_b32 exec_hi, 0xf\n" \
"global_load_lds_dword v188, s[86:87]\n" \
"s_mov_b64 exec, -1\n" \
"v_mfma_f32_16x16x32_bf16 a[80:83], v[68:71], v[148:151], a[80:83]\n" \
"v_add_u32_e32 v229, 0x140, v212\n" \
"v_mul_u32_u24_e32 v225, 0x480, v224\n" \
"v_mfma_f32_16x16x32_bf16 a[112:115], v[76:79], v[148:151], a[112:115]\n" \
"v_and_b32_e32 v241, 0xff, v240\n" \
"v_cmp_gt_u32_e32 vcc, 16, v238\n" \
"v_mfma_f32_16x16x32_bf16 a[112:115], v[80:83], v[152:155], a[112:115]\n" \
"v_lshlrev_b32_e32 v250, 4, v249\n" \
"v_mul_u32_u24_e32 v215, 18, v220\n" \
"v_mfma_f32_16x16x32_bf16 a[20:23], v[56:59], v[160:163], a[20:23]\n" \
"v_mul_u32_u24_e32 v227, 0xe39, v229\n" \
"v_add_u32_e32 v232, v225, v231\n" \
"v_mfma_f32_16x16x32_bf16 a[20:23], v[52:55], v[156:159], a[20:23]\n" \
"v_cndmask_b32_e32 v242, v239, v241, vcc\n" \
"v_add_u32_e32 v251, s96, v250\n" \
"v_mfma_f32_16x16x32_bf16 a[52:55], v[60:63], v[156:159], a[52:55]\n" \
"v_sub_u32_e32 v221, v219, v215\n" \
"v_lshrrev_b32_e32 v230, 16, v227\n" \
"v_mfma_f32_16x16x32_bf16 a[52:55], v[64:67], v[160:163], a[52:55]\n" \
"v_add_u32_e32 v240, 0x180, v212\n" \
"v_mul_u32_u24_e32 v236, 0x480, v235\n" \
"v_mfma_f32_16x16x32_bf16 a[84:87], v[72:75], v[160:163], a[84:87]\n" \
"v_and_b32_e32 v213, 0xff, v251\n" \
"v_cmp_gt_u32_e32 vcc, 16, v249\n" \
"v_mfma_f32_16x16x32_bf16 a[84:87], v[68:71], v[156:159], a[84:87]\n" \
"v_lshlrev_b32_e32 v223, 4, v221\n" \
"v_mul_u32_u24_e32 v225, 18, v230\n" \
"v_mfma_f32_16x16x32_bf16 a[116:119], v[76:79], v[156:159], a[116:119]\n" \
"v_mul_u32_u24_e32 v238, 0xe39, v240\n" \
"v_add_u32_e32 v1, 0xfffffc00, v232\n" \
"v_mfma_f32_16x16x32_bf16 a[116:119], v[80:83], v[160:163], a[116:119]\n" \
"v_add_u32_e32 v243, v236, v242\n" \
"s_waitcnt lgkmcnt(0)\n" \
"v_mfma_f32_16x16x32_bf16 a[24:27], v[56:59], v[168:171], a[24:27]\n" \
"v_cndmask_b32_e32 v214, v250, v213, vcc\n" \
"v_add_u32_e64 v224, s96, v223\n" \
"v_mfma_f32_16x16x32_bf16 a[24:27], v[52:55], v[164:167], a[24:27]\n" \
"v_sub_u32_e32 v231, v229, v225\n" \
"v_lshrrev_b32_e32 v241, 16, v238\n" \
"v_mfma_f32_16x16x32_bf16 a[56:59], v[60:63], v[164:167], a[56:59]\n" \
"v_add_u32_e32 v251, 0x1c0, v212\n" \
"v_mul_u32_u24_e32 v247, 0x480, v246\n" \
"v_mfma_f32_16x16x32_bf16 a[56:59], v[64:67], v[168:171], a[56:59]\n" \
"v_and_b32_e32 v222, 0xff, v224\n" \
"v_cmp_gt_u32_e32 vcc, 16, v221\n" \
"v_mfma_f32_16x16x32_bf16 a[88:91], v[72:75], v[168:171], a[88:91]\n" \
"v_lshlrev_b32_e32 v232, 4, v231\n" \
"v_mul_u32_u24_e32 v236, 18, v241\n" \
"v_mfma_f32_16x16x32_bf16 a[88:91], v[68:71], v[164:167], a[88:91]\n" \
"v_mul_u32_u24_e32 v249, 0xe39, v251\n" \
"v_add_u32_e32 v2, 0xfffff800, v243\n" \
"v_mfma_f32_16x16x32_bf16 a[120:123], v[76:79], v[164:167], a[120:123]\n" \
"v_add_u32_e32 v216, v247, v214\n" \
"v_cndmask_b32_e32 v226, v223, v222, vcc\n" \
"v_mfma_f32_16x16x32_bf16 a[120:123], v[80:83], v[168:171], a[120:123]\n" \
"v_add_u32_e32 v234, s96, v232\n" \
"v_sub_u32_e32 v242, v240, v236\n" \
"v_mfma_f32_16x16x32_bf16 a[28:31], v[56:59], v[176:179], a[28:31]\n" \
"s_add_u32 s62, s22, 0x300\n" \
"s_and_b32 s62, s62, 0x3ff\n" \
"v_mfma_f32_16x16x32_bf16 a[28:31], v[52:55], v[172:175], a[28:31]\n" \
"s_lshl_b32 s63, s62, 12\n" \
"v_lshrrev_b32_e32 v213, 16, v249\n" \
"v_mfma_f32_16x16x32_bf16 a[60:63], v[60:63], v[172:175], a[60:63]\n" \
"v_add_u32_e32 v224, 0x200, v212\n" \
"v_mul_u32_u24_e32 v218, 0x480, v220\n" \
"v_mfma_f32_16x16x32_bf16 a[60:63], v[64:67], v[176:179], a[60:63]\n" \
"v_and_b32_e32 v235, 0xff, v234\n" \
"v_cmp_gt_u32_e32 vcc, 16, v231\n" \
"v_mfma_f32_16x16x32_bf16 a[92:95], v[72:75], v[176:179], a[92:95]\n" \
"v_lshlrev_b32_e32 v243, 4, v242\n" \
"v_mul_u32_u24_e32 v247, 18, v213\n" \
"v_mfma_f32_16x16x32_bf16 a[92:95], v[68:71], v[172:175], a[92:95]\n" \
"v_mul_u32_u24_e32 v221, 0xe39, v224\n" \
"v_add_u32_e32 v3, 0xfffff400, v216\n" \
"v_mfma_f32_16x16x32_bf16 a[124:127], v[76:79], v[172:175], a[124:127]\n" \
"v_add_u32_e32 v4, v218, v226\n" \
"v_cndmask_b32_e32 v233, v232, v235, vcc\n" \
"v_mfma_f32_16x16x32_bf16 a[124:127], v[80:83], v[176:179], a[124:127]\n" \
"v_add_u32_e32 v245, s96, v243\n" \
"v_sub_u32_e32 v214, v251, v247\n" \
"s_waitcnt vmcnt(4)\n" \
"s_barrier\n" \
"v_mfma_f32_16x16x32_bf16 a[144:147], v[88:91], v[152:155], a[144:147]\n" \
"s_add_u32 m0, %[mA], 0x0\n" \
"s_add_u32 s85, s62, 0x0\n" \
"v_mfma_f32_16x16x32_bf16 a[144:147], v[84:87], v[148:151], a[144:147]\n" \
"buffer_load_dwordx4 v22, s[64:67], s85 offen lds\n" \
"v_mfma_f32_16x16x32_bf16 a[176:179], v[92:95], v[148:151], a[176:179]\n" \
"ds_read_b128 v[56:59], v29 offset:49152\n" \
"ds_read_b128 v[52:55], v28 offset:49152\n" \
"v_mfma_f32_16x16x32_bf16 a[176:179], v[96:99], v[152:155], a[176:179]\n" \
"s_add_u32 m0, %[mA], 0x400\n" \
"s_add_u32 s84, s62, 0x2000\n" \
"v_mfma_f32_16x16x32_bf16 a[208:211], v[104:107], v[152:155], a[208:211]\n" \
"buffer_load_dwordx4 v23, s[64:67], s84 offen lds\n" \
"v_mfma_f32_16x16x32_bf16 a[208:211], v[100:103], v[148:151], a[208:211]\n" \
"ds_read_b128 v[60:63], v28 offset:51200\n" \
"ds_read_b128 v[64:67], v29 offset:51200\n" \
"v_mfma_f32_16x16x32_bf16 a[240:243], v[108:111], v[148:151], a[240:243]\n" \
"ds_read_b64_tr_b16 v[148:149], v32 offset:49152\n" \
"ds_read_b64_tr_b16 v[150:151], v32 offset:50176\n" \
"v_mfma_f32_16x16x32_bf16 a[240:243], v[112:115], v[152:155], a[240:243]\n" \
"s_add_u32 m0, %[mB], 0x0\n" \
"s_add_u32 s85, s63, 0x0\n" \
"v_mfma_f32_16x16x32_bf16 a[148:151], v[88:91], v[160:163], a[148:151]\n" \
"buffer_load_dwordx4 v26, s[68:71], s85 offen lds\n" \
"v_mfma_f32_16x16x32_bf16 a[148:151], v[84:87], v[156:159], a[148:151]\n" \
"ds_read_b64_tr_b16 v[152:153], v32 offset:57344\n" \
"ds_read_b64_tr_b16 v[154:155], v32 offset:58368\n" \
"v_mfma_f32_16x16x32_bf16 a[180:183], v[92:95], v[156:159], a[180:183]\n" \
"ds_read_b128 v[72:75], v29 offset:53248\n" \
"ds_read_b128 v[68:71], v28 offset:53248\n" \
"v_mfma_f32_16x16x32_bf16 a[180:183], v[96:99], v[160:163], a[180:183]\n" \
"s_add_u32 m0, %[mB], 0x4000\n" \
"s_add_u32 s84, s63, 0x0\n" \
"s_nop 0\n" \
"v_mfma_f32_16x16x32_bf16 a[212:215], v[104:107], v[160:163], a[212:215]\n" \
"buffer_load_dwordx4 v26, s[72:75], s84 offen lds\n" \
"v_mfma_f32_16x16x32_bf16 a[212:215], v[100:103], v[156:159], a[212:215]\n" \
"ds_read_b128 v[76:79], v28 offset:55296\n" \
"ds_read_b128 v[80:83], v29 offset:55296\n" \
"v_mfma_f32_16x16x32_bf16 a[244:247], v[108:111], v[156:159], a[244:247]\n" \
"ds_read_b64_tr_b16 v[156:157], v33 offset:49152\n" \
"ds_read_b64_tr_b16 v[158:159], v33 offset:50176\n" \
"v_mfma_f32_16x16x32_bf16 a[244:247], v[112:115], v[160:163], a[244:247]\n" \
"s_add_u32 m0, %[mA], 0x800\n" \
"s_add_u32 s85, s62, 0x4000\n" \
"v_mfma_f32_16x16x32_bf16 a[152:155], v[88:91], v[168:171], a[152:155]\n" \
"buffer_load_dwordx4 v22, s[64:67], s85 offen lds\n" \
"v_mfma_f32_16x16x32_bf16 a[152:155], v[84:87], v[164:167], a[152:155]\n" \
"ds_read_b64_tr_b16 v[160:161], v33 offset:57344\n" \
"ds_read_b64_tr_b16 v[162:163], v33 offset:58368\n" \
"v_mfma_f32_16x16x32_bf16 a[184:187], v[92:95], v[164:167], a[184:187]\n" \
"v_lshrrev_b32_e32 v222, 16, v221\n" \
"v_and_b32_e32 v246, 0xff, v245\n" \
"v_mfma_f32_16x16x32_bf16 a[184:187], v[96:99], v[168:171], a[184:187]\n" \
"s_add_u32 m0, %[mA], 0xc00\n" \
"s_add_u32 s84, s62, 0x6000\n" \
"v_mfma_f32_16x16x32_bf16 a[216:219], v[104:107], v[168:171], a[216:219]\n" \
"buffer_load_dwordx4 v23, s[64:67], s84 offen lds\n" \
"v_mfma_f32_16x16x32_bf16 a[216:219], v[100:103], v[164:167], a[216:219]\n" \
"v_cmp_gt_u32_e32 vcc, 16, v242\n" \
"v_lshlrev_b32_e32 v216, 4, v214\n" \
"s_nop 0\n" \
"v_mfma_f32_16x16x32_bf16 a[248:251], v[108:111], v[164:167], a[248:251]\n" \
"ds_read_b64_tr_b16 v[164:165], v34 offset:49152\n" \
"ds_read_b64_tr_b16 v[166:167], v34 offset:50176\n" \
"v_mfma_f32_16x16x32_bf16 a[248:251], v[112:115], v[168:171], a[248:251]\n" \
"s_add_u32 m0, %[mB], 0x400\n" \
"s_add_u32 s85, s63, 0x8000\n" \
"v_mfma_f32_16x16x32_bf16 a[156:159], v[88:91], v[176:179], a[156:159]\n" \
"buffer_load_dwordx4 v26, s[68:71], s85 offen lds\n" \
"v_mfma_f32_16x16x32_bf16 a[156:159], v[84:87], v[172:175], a[156:159]\n" \
"ds_read_b64_tr_b16 v[168:169], v34 offset:57344\n" \
"ds_read_b64_tr_b16 v[170:171], v34 offset:58368\n" \
"v_mfma_f32_16x16x32_bf16 a[188:191], v[92:95], v[172:175], a[188:191]\n" \
"v_mul_u32_u24_e32 v218, 18, v222\n" \
"v_cndmask_b32_e32 v244, v243, v246, vcc\n" \
"v_mfma_f32_16x16x32_bf16 a[188:191], v[96:99], v[176:179], a[188:191]\n" \
"s_add_u32 m0, %[mB], 0x4400\n" \
"s_add_u32 s84, s63, 0x8000\n" \
"v_mfma_f32_16x16x32_bf16 a[220:223], v[104:107], v[176:179], a[220:223]\n" \
"buffer_load_dwordx4 v26, s[72:75], s84 offen lds\n" \
"v_mfma_f32_16x16x32_bf16 a[220:223], v[100:103], v[172:175], a[220:223]\n" \
"v_add_u32_e32 v217, s96, v216\n" \
"v_sub_u32_e32 v226, v224, v218\n" \
"v_mfma_f32_16x16x32_bf16 a[252:255], v[108:111], v[172:175], a[252:255]\n" \
"ds_read_b64_tr_b16 v[172:173], v35 offset:49152\n" \
"ds_read_b64_tr_b16 v[174:175], v35 offset:50176\n" \
"v_mfma_f32_16x16x32_bf16 a[252:255], v[112:115], v[176:179], a[252:255]\n" \
"s_add_u32 m0, %[mA], 0x1000\n" \
"s_add_u32 s85, s62, 0x8000\n" \
"v_mfma_f32_16x16x32_bf16 a[128:131], v[120:123], v[88:91], a[128:131]\n" \
"buffer_load_dwordx4 v22, s[64:67], s85 offen lds\n" \
"v_mfma_f32_16x16x32_bf16 a[128:131], v[116:119], v[84:87], a[128:131]\n" \
"ds_read_b64_tr_b16 v[176:177], v35 offset:57344\n" \
"ds_read_b64_tr_b16 v[178:179], v35 offset:58368\n" \
"v_mfma_f32_16x16x32_bf16 a[160:163], v[116:119], v[92:95], a[160:163]\n" \
"v_mul_u32_u24_e32 v228, 0x480, v230\n" \
"v_and_b32_e32 v220, 0xff, v217\n" \
"v_mfma_f32_16x16x32_bf16 a[160:163], v[120:123], v[96:99], a[160:163]\n" \
"s_add_u32 m0, %[mA], 0x1400\n" \
"s_add_u32 s84, s62, 0xa000\n" \
"v_mfma_f32_16x16x32_bf16 a[192:195], v[120:123], v[104:107], a[192:195]\n" \
"buffer_load_dwordx4 v23, s[64:67], s84 offen lds\n" \
"v_mfma_f32_16x16x32_bf16 a[192:195], v[116:119], v[100:103], a[192:195]\n" \
"v_cmp_gt_u32_e32 vcc, 16, v214\n" \
"v_lshlrev_b32_e32 v227, 4, v226\n" \
"v_mfma_f32_16x16x32_bf16 a[224:227], v[116:119], v[108:111], a[224:227]\n" \
"ds_read_b64_tr_b16 v[116:117], v32 offset:32768\n" \
"ds_read_b64_tr_b16 v[118:119], v32 offset:33792\n" \
"v_mfma_f32_16x16x32_bf16 a[224:227], v[120:123], v[112:115], a[224:227]\n" \
"s_add_u32 m0, %[mB], 0x800\n" \
"s_add_u32 s85, s63, 0x10000\n" \
"v_mfma_f32_16x16x32_bf16 a[132:135], v[128:131], v[88:91], a[132:135]\n" \
"buffer_load_dwordx4 v27, s[68:71], s85 offen lds\n" \
"v_mfma_f32_16x16x32_bf16 a[132:135], v[124:127], v[84:87], a[132:135]\n" \
"ds_read_b64_tr_b16 v[120:121], v32 offset:40960\n" \
"ds_read_b64_tr_b16 v[122:123], v32 offset:41984\n" \
"v_mfma_f32_16x16x32_bf16 a[164:167], v[124:127], v[92:95], a[164:167]\n" \
"v_cndmask_b32_e32 v219, v216, v220, vcc\n" \
"v_add_u32_e32 v230, s96, v227\n" \
"v_mfma_f32_16x16x32_bf16 a[164:167], v[128:131], v[96:99], a[164:167]\n" \
"s_add_u32 m0, %[mB], 0x4800\n" \
"s_add_u32 s84, s63, 0x10000\n" \
"v_mfma_f32_16x16x32_bf16 a[196:199], v[128:131], v[104:107], a[196:199]\n" \
"buffer_load_dwordx4 v27, s[72:75], s84 offen lds\n" \
"v_mfma_f32_16x16x32_bf16 a[196:199], v[124:127], v[100:103], a[196:199]\n" \
"v_and_b32_e32 v229, 0xff, v230\n" \
"v_cmp_gt_u32_e32 vcc, 16, v226\n" \
"v_mfma_f32_16x16x32_bf16 a[228:231], v[124:127], v[108:111], a[228:231]\n" \
"ds_read_b64_tr_b16 v[124:125], v33 offset:32768\n" \
"ds_read_b64_tr_b16 v[126:127], v33 offset:33792\n" \
"v_mfma_f32_16x16x32_bf16 a[228:231], v[128:131], v[112:115], a[228:231]\n" \
"ds_read_b64_tr_b16 v[128:129], v33 offset:40960\n" \
"ds_read_b64_tr_b16 v[130:131], v33 offset:41984\n" \
"v_mfma_f32_16x16x32_bf16 a[136:139], v[136:139], v[88:91], a[136:139]\n" \
"v_mul_u32_u24_e32 v250, 0x480, v213\n" \
"v_cndmask_b32_e32 v225, v227, v229, vcc\n" \
"v_mfma_f32_16x16x32_bf16 a[136:139], v[132:135], v[84:87], a[136:139]\n" \
"v_and_b32_e64 v249, 2, v212\n" \
"v_mov_b32_e32 v213, 0x100\n" \
"v_mfma_f32_16x16x32_bf16 a[168:171], v[132:135], v[92:95], a[168:171]\n" \
"v_mov_b32_e32 v251, s95\n" \
"v_cmp_ne_u32_e32 vcc, 0, v249\n" \
"v_mfma_f32_16x16x32_bf16 a[168:171], v[136:139], v[96:99], a[168:171]\n" \
"s_mov_b32 s76, s8\n" \
"v_cndmask_b32_e32 v247, v251, v213, vcc\n" \
"v_mfma_f32_16x16x32_bf16 a[200:203], v[136:139], v[104:107], a[200:203]\n" \
"s_and_b32 s77, s9, 0xffff\n" \
"s_mov_b32 s78, 0x240000\n" \
"v_mfma_f32_16x16x32_bf16 a[200:203], v[132:135], v[100:103], a[200:203]\n" \
"v_xor_b32_e32 v37, 0x20, v190\n" \
"v_xor_b32_e32 v38, 0x40, v190\n" \
"v_mfma_f32_16x16x32_bf16 a[232:235], v[132:135], v[108:111], a[232:235]\n" \
"ds_read_b64_tr_b16 v[132:133], v34 offset:32768\n" \
"ds_read_b64_tr_b16 v[134:135], v34 offset:33792\n" \
"v_mfma_f32_16x16x32_bf16 a[232:235], v[136:139], v[112:115], a[232:235]\n" \
"ds_read_b64_tr_b16 v[136:137], v34 offset:40960\n" \
"ds_read_b64_tr_b16 v[138:139], v34 offset:41984\n" \
"v_mfma_f32_16x16x32_bf16 a[140:143], v[144:147], v[88:91], a[140:143]\n" \
"ds_read_b128 v[88:91], v29 offset:32768\n" \
"v_add_u32_e64 v48, s60, v28\n" \
"v_mfma_f32_16x16x32_bf16 a[140:143], v[140:143], v[84:87], a[140:143]\n" \
"ds_read_b128 v[84:87], v28 offset:32768\n" \
"v_add_u32_e64 v49, s60, v29\n" \
"v_mfma_f32_16x16x32_bf16 a[172:175], v[140:143], v[92:95], a[172:175]\n" \
"ds_read_b128 v[92:95], v28 offset:34816\n" \
"v_xor_b32_e64 v36, 0x0, v190\n" \
"v_mfma_f32_16x16x32_bf16 a[172:175], v[144:147], v[96:99], a[172:175]\n" \
"ds_read_b128 v[96:99], v29 offset:34816\n" \
"v_add_u32_e64 v237, v228, v233\n" \
"v_mfma_f32_16x16x32_bf16 a[204:207], v[144:147], v[104:107], a[204:207]\n" \
"ds_read_b128 v[104:107], v29 offset:36864\n" \
"s_lshl_b32 s97, %[w], 4\n" \
"v_mfma_f32_16x16x32_bf16 a[204:207], v[140:143], v[100:103], a[204:207]\n" \
"ds_read_b128 v[100:103], v28 offset:36864\n" \
"v_lshrrev_b32_e32 v234, 2, v212\n" \
"v_mfma_f32_16x16x32_bf16 a[236:239], v[140:143], v[108:111], a[236:239]\n" \
"ds_read_b64_tr_b16 v[140:141], v35 offset:32768\n" \
"ds_read_b64_tr_b16 v[142:143], v35 offset:33792\n" \
"v_mfma_f32_16x16x32_bf16 a[236:239], v[144:147], v[112:115], a[236:239]\n" \
"s_add_u32 m0, %[mA], 0x1800\n" \
"s_waitcnt lgkmcnt(12)\n" \
"s_nop 0\n" \
"v_mfma_f32_16x16x32_bf16 a[16:19], v[56:59], v[152:155], a[16:19]\n" \
"s_add_u32 s85, s62, 0xc000\n" \
"ds_read_b64_tr_b16 v[144:145], v35 offset:40960\n" \
"v_mfma_f32_16x16x32_bf16 a[16:19], v[52:55], v[148:151], a[16:19]\n" \
"buffer_load_dwordx4 v22, s[64:67], s85 offen lds\n" \
"v_mfma_f32_16x16x32_bf16 a[48:51], v[60:63], v[148:151], a[48:51]\n" \
"s_add_u32 m0, %[mA], 0x1c00\n" \
"s_add_u32 s84, s62, 0xe000\n" \
"v_mfma_f32_16x16x32_bf16 a[48:51], v[64:67], v[152:155], a[48:51]\n" \
"ds_read_b64_tr_b16 v[146:147], v35 offset:41984\n" \
"ds_read_b128 v[108:111], v28 offset:38912\n" \
"v_mfma_f32_16x16x32_bf16 a[80:83], v[72:75], v[152:155], a[80:83]\n" \
"buffer_load_dwordx4 v23, s[64:67], s84 offen lds\n" \
"v_mfma_f32_16x16x32_bf16 a[80:83], v[68:71], v[148:151], a[80:83]\n" \
"s_add_u32 m0, %[mB], 0xc00\n" \
"s_add_u32 s85, s63, 0x18000\n" \
"v_mfma_f32_16x16x32_bf16 a[112:115], v[76:79], v[148:151], a[112:115]\n" \
"ds_read_b128 v[112:115], v29 offset:38912\n" \
"v_add_u32_e32 v5, 0xfffffc00, v237\n" \
"v_mfma_f32_16x16x32_bf16 a[112:115], v[80:83], v[152:155], a[112:115]\n" \
"buffer_load_dwordx4 v27, s[68:71], s85 offen lds\n" \
"v_mfma_f32_16x16x32_bf16 a[20:23], v[56:59], v[160:163], a[20:23]\n" \
"s_add_u32 m0, %[mB], 0x4c00\n" \
"s_add_u32 s84, s63, 0x18000\n" \
"v_mfma_f32_16x16x32_bf16 a[20:23], v[52:55], v[156:159], a[20:23]\n" \
"v_add_u32_e32 v231, s97, v234\n" \
"v_lshrrev_b32_e32 v235, 5, v231\n" \
"v_mfma_f32_16x16x32_bf16 a[52:55], v[60:63], v[156:159], a[52:55]\n" \
"buffer_load_dwordx4 v27, s[72:75], s84 offen lds\n" \
"v_mfma_f32_16x16x32_bf16 a[52:55], v[64:67], v[160:163], a[52:55]\n" \
"v_lshrrev_b32_e32 v237, 4, v231\n" \
"v_mul_u32_u24_e32 v239, 0x480, v241\n" \
"v_mfma_f32_16x16x32_bf16 a[84:87], v[72:75], v[160:163], a[84:87]\n" \
"v_lshlrev_b32_e32 v232, 4, v235\n" \
"v_and_b32_e64 v228, 12, v231\n" \
"v_mfma_f32_16x16x32_bf16 a[84:87], v[68:71], v[156:159], a[84:87]\n" \
"v_and_b32_e32 v238, 1, v237\n" \
"v_add_u32_e32 v233, v232, v228\n" \
"v_mfma_f32_16x16x32_bf16 a[116:119], v[76:79], v[156:159], a[116:119]\n" \
"v_lshlrev_b32_e32 v241, 1, v238\n" \
"v_lshrrev_b32_e32 v236, 1, v231\n" \
"v_mfma_f32_16x16x32_bf16 a[116:119], v[80:83], v[160:163], a[116:119]\n" \
"s_mov_b32 m0, %[mS]\n" \
"s_mov_b32 s86, s8\n" \
"v_mfma_f32_16x16x32_bf16 a[24:27], v[56:59], v[168:171], a[24:27]\n" \
"s_mov_b32 s87, s9\n" \
"v_add_u32_e32 v240, v233, v241\n" \
"v_mfma_f32_16x16x32_bf16 a[24:27], v[52:55], v[164:167], a[24:27]\n" \
"v_and_b32_e32 v245, 1, v236\n" \
"v_add_u32_e32 v242, v240, v245\n" \
"v_mfma_f32_16x16x32_bf16 a[56:59], v[60:63], v[164:167], a[56:59]\n" \
"v_add_u32_e32 v248, v239, v244\n" \
"v_mul_u32_u24_e32 v246, 18, v242\n" \
"v_mfma_f32_16x16x32_bf16 a[56:59], v[64:67], v[168:171], a[56:59]\n" \
"v_add_u32_e32 v243, 16, v246\n" \
"v_and_b32_e32 v239, 1, v231\n" \
"v_mfma_f32_16x16x32_bf16 a[88:91], v[72:75], v[168:171], a[88:91]\n" \
"v_add_u32_e32 v6, 0xfffff800, v248\n" \
"v_add_u32_e64 v244, v243, v239\n" \
"v_mfma_f32_16x16x32_bf16 a[88:91], v[68:71], v[164:167], a[88:91]\n" \
"v_mul_u32_u24_e32 v248, 0x480, v244\n" \
"v_add_u32_e32 v215, v250, v219\n" \
"s_mov_b32 exec_lo, 0x0\n" \
"s_mov_b32 exec_hi, 0x3ffff0\n" \
"global_load_lds_dword v188, s[86:87]\n" \
"s_mov_b64 exec, -1\n" \
"v_mfma_f32_16x16x32_bf16 a[120:123], v[76:79], v[164:167], a[120:123]\n" \
"v_add_u32_e32 v217, v248, v247\n" \
"v_and_b32_e64 v214, 16, v20\n" \
"v_mfma_f32_16x16x32_bf16 a[120:123], v[80:83], v[168:171], a[120:123]\n" \
"v_add_u32_e32 v7, 0xfffff400, v215\n" \
"v_add_u32_e64 v9, v217, v214\n" \
"v_mfma_f32_16x16x32_bf16 a[28:31], v[56:59], v[176:179], a[28:31]\n" \
"v_and_b32_e32 v216, 3, v212\n" \
"v_lshlrev_b32_e32 v250, 3, v216\n" \
"v_mfma_f32_16x16x32_bf16 a[28:31], v[52:55], v[172:175], a[28:31]\n" \
"v_mul_u32_u24_e32 v220, 0x120, v234\n" \
"v_mul_u32_u24_e32 v223, 0x480, v222\n" \
"v_mfma_f32_16x16x32_bf16 a[60:63], v[60:63], v[172:175], a[60:63]\n" \
"v_add_u32_e32 v10, v220, v250\n" \
"v_add_u32_e32 v8, v223, v225\n" \
"v_mfma_f32_16x16x32_bf16 a[60:63], v[64:67], v[176:179], a[60:63]\n" \
"s_lshl_b32 s99, %[w], 11\n" \
"s_add_u32 s55, s98, s99\n" \
"v_mfma_f32_16x16x32_bf16 a[92:95], v[72:75], v[176:179], a[92:95]\n" \
"v_mfma_f32_16x16x32_bf16 a[92:95], v[68:71], v[172:175], a[92:95]\n" \
"v_mfma_f32_16x16x32_bf16 a[124:127], v[76:79], v[172:175], a[124:127]\n" \
"v_mfma_f32_16x16x32_bf16 a[124:127], v[80:83], v[176:179], a[124:127]\n" \
"v_mfma_f32_16x16x32_bf16 a[0:3], v[120:123], v[56:59], a[0:3]\n" \
"v_mfma_f32_16x16x32_bf16 a[0:3], v[116:119], v[52:55], a[0:3]\n" \
"v_mfma_f32_16x16x32_bf16 a[32:35], v[116:119], v[60:63], a[32:35]\n" \
"v_mfma_f32_16x16x32_bf16 a[32:35], v[120:123], v[64:67], a[32:35]\n" \
"v_mfma_f32_16x16x32_bf16 a[64:67], v[120:123], v[72:75], a[64:67]\n" \
"v_mfma_f32_16x16x32_bf16 a[64:67], v[116:119], v[68:71], a[64:67]\n" \
"v_mfma_f32_16x16x32_bf16 a[96:99], v[116:119], v[76:79], a[96:99]\n" \
"v_mfma_f32_16x16x32_bf16 a[96:99], v[120:123], v[80:83], a[96:99]\n" \
"v_mfma_f32_16x16x32_bf16 a[4:7], v[128:131], v[56:59], a[4:7]\n" \
"v_mfma_f32_16x16x32_bf16 a[4:7], v[124:127], v[52:55], a[4:7]\n" \
"v_mfma_f32_16x16x32_bf16 a[36:39], v[124:127], v[60:63], a[36:39]\n" \
"v_mfma_f32_16x16x32_bf16 a[36:39], v[128:131], v[64:67], a[36:39]\n" \
"v_mfma_f32_16x16x32_bf16 a[68:71], v[128:131], v[72:75], a[68:71]\n" \
"v_mfma_f32_16x16x32_bf16 a[68:71], v[124:127], v[68:71], a[68:71]\n" \
"v_mfma_f32_16x16x32_bf16 a[100:103], v[124:127], v[76:79], a[100:103]\n" \
"v_mfma_f32_16x16x32_bf16 a[100:103], v[128:131], v[80:83], a[100:103]\n" \
"s_waitcnt lgkmcnt(0)\n" \
"s_nop 0\n" \
"v_mfma_f32_16x16x32_bf16 a[8:11], v[136:139], v[56:59], a[8:11]\n" \
"v_mfma_f32_16x16x32_bf16 a[8:11], v[132:135], v[52:55], a[8:11]\n" \
"v_mfma_f32_16x16x32_bf16 a[40:43], v[132:135], v[60:63], a[40:43]\n" \
"v_mfma_f32_16x16x32_bf16 a[40:43], v[136:139], v[64:67], a[40:43]\n" \
"v_mfma_f32_16x16x32_bf16 a[72:75], v[136:139], v[72:75], a[72:75]\n" \
"v_mfma_f32_16x16x32_bf16 a[72:75], v[132:135], v[68:71], a[72:75]\n" \
"v_mfma_f32_16x16x32_bf16 a[104:107], v[132:135], v[76:79], a[104:107]\n" \
"v_mfma_f32_16x16x32_bf16 a[104:107], v[136:139], v[80:83], a[104:107]\n" \
"v_mfma_f32_16x16x32_bf16 a[12:15], v[144:147], v[56:59], a[12:15]\n" \
"s_add_u32 s62, s22, 0x380\n" \
"s_and_b32 s62, s62, 0x3ff\n" \
"v_mfma_f32_16x16x32_bf16 a[12:15], v[140:143], v[52:55], a[12:15]\n" \
"s_lshl_b32 s63, s62, 12\n" \
"v_mfma_f32_16x16x32_bf16 a[44:47], v[140:143], v[60:63], a[44:47]\n" \
"v_mfma_f32_16x16x32_bf16 a[44:47], v[144:147], v[64:67], a[44:47]\n" \
"v_mfma_f32_16x16x32_bf16 a[76:79], v[144:147], v[72:75], a[76:79]\n" \
"v_mfma_f32_16x16x32_bf16 a[76:79], v[140:143], v[68:71], a[76:79]\n" \
"v_mfma_f32_16x16x32_bf16 a[108:111], v[140:143], v[76:79], a[108:111]\n" \
"v_mfma_f32_16x16x32_bf16 a[108:111], v[144:147], v[80:83], a[108:111]\n" \
"s_waitcnt vmcnt(1)\n" \
"s_barrier\n" \
"s_mov_b64 exec, 1\n" \
"buffer_store_dword v45, off, s[80:83], s38 offset:64 sc1\n" \
"s_mov_b64 exec, -1\n" \
"v_mfma_f32_16x16x32_bf16 a[128:131], v[120:123], v[88:91], a[128:131]\n" \
"s_add_u32 m0, %[mA], 0x8000\n" \
"s_add_u32 s85, s62, 0x0\n" \
"v_mfma_f32_16x16x32_bf16 a[128:131], v[116:119], v[84:87], a[128:131]\n" \
"ds_read_b128 v[56:59], v29 offset:16384\n" \
"ds_read_b128 v[52:55], v28 offset:16384\n" \
"v_mfma_f32_16x16x32_bf16 a[160:163], v[116:119], v[92:95], a[160:163]\n" \
"buffer_load_dwordx4 v22, s[64:67], s85 offen lds\n" \
"v_mfma_f32_16x16x32_bf16 a[160:163], v[120:123], v[96:99], a[160:163]\n" \
"s_add_u32 m0, %[mA], 0x8400\n" \
"s_add_u32 s84, s62, 0x2000\n" \
"v_mfma_f32_16x16x32_bf16 a[192:195], v[120:123], v[104:107], a[192:195]\n" \
"ds_read_b128 v[60:63], v28 offset:18432\n" \
"ds_read_b128 v[64:67], v29 offset:18432\n" \
"v_mfma_f32_16x16x32_bf16 a[192:195], v[116:119], v[100:103], a[192:195]\n" \
"buffer_load_dwordx4 v23, s[64:67], s84 offen lds\n" \
"v_mfma_f32_16x16x32_bf16 a[224:227], v[116:119], v[108:111], a[224:227]\n" \
"ds_read_b64_tr_b16 v[116:117], v32 offset:0\n" \
"ds_read_b64_tr_b16 v[118:119], v32 offset:1024\n" \
"v_mfma_f32_16x16x32_bf16 a[224:227], v[120:123], v[112:115], a[224:227]\n" \
"s_add_u32 m0, %[mB], 0x8000\n" \
"s_add_u32 s85, s63, 0x0\n" \
"v_mfma_f32_16x16x32_bf16 a[132:135], v[128:131], v[88:91], a[132:135]\n" \
"buffer_load_dwordx4 v26, s[68:71], s85 offen lds\n" \
"v_mfma_f32_16x16x32_bf16 a[132:135], v[124:127], v[84:87], a[132:135]\n" \
"ds_read_b64_tr_b16 v[120:121], v32 offset:8192\n" \
"ds_read_b64_tr_b16 v[122:123], v32 offset:9216\n" \
"v_mfma_f32_16x16x32_bf16 a[164:167], v[124:127], v[92:95], a[164:167]\n" \
"ds_read_b128 v[72:75], v29 offset:20480\n" \
"ds_read_b128 v[68:71], v28 offset:20480\n" \
"v_mfma_f32_16x16x32_bf16 a[164:167], v[128:131], v[96:99], a[164:167]\n" \
"s_add_u32 m0, %[mB], 0xc000\n" \
"s_add_u32 s84, s63, 0x0\n" \
"v_mfma_f32_16x16x32_bf16 a[196:199], v[128:131], v[104:107], a[196:199]\n" \
"buffer_load_dwordx4 v26, s[72:75], s84 offen lds\n" \
"v_mfma_f32_16x16x32_bf16 a[196:199], v[124:127], v[100:103], a[196:199]\n" \
"ds_read_b128 v[76:79], v28 offset:22528\n" \
"ds_read_b128 v[80:83], v29 offset:22528\n" \
"v_mfma_f32_16x16x32_bf16 a[228:231], v[124:127], v[108:111], a[228:231]\n" \
"ds_read_b64_tr_b16 v[124:125], v33 offset:0\n" \
"ds_read_b64_tr_b16 v[126:127], v33 offset:1024\n" \
"v_mfma_f32_16x16x32_bf16 a[228:231], v[128:131], v[112:115], a[228:231]\n" \
"s_add_u32 m0, %[mA], 0x8800\n" \
"s_add_u32 s85, s62, 0x4000\n" \
"v_mfma_f32_16x16x32_bf16 a[136:139], v[136:139], v[88:91], a[136:139]\n" \
"buffer_load_dwordx4 v22, s[64:67], s85 offen lds\n" \
"v_mfma_f32_16x16x32_bf16 a[136:139], v[132:135], v[84:87], a[136:139]\n" \
"ds_read_b64_tr_b16 v[128:129], v33 offset:8192\n" \
"ds_read_b64_tr_b16 v[130:131], v33 offset:9216\n" \
"v_mfma_f32_16x16x32_bf16 a[168:171], v[132:135], v[92:95], a[168:171]\n" \
"v_mov_b32_e64 v45, %[tag]\n" \
"v_mfma_f32_16x16x32_bf16 a[168:171], v[136:139], v[96:99], a[168:171]\n" \
"s_add_u32 m0, %[mA], 0x8c00\n" \
"s_add_u32 s84, s62, 0x6000\n" \
"v_mfma_f32_16x16x32_bf16 a[200:203], v[136:139], v[104:107], a[200:203]\n" \
"buffer_load_dwordx4 v23, s[64:67], s84 offen lds\n" \
"v_mfma_f32_16x16x32_bf16 a[200:203], v[132:135], v[100:103], a[200:203]\n" \
"v_mfma_f32_16x16x32_bf16 a[232:235], v[132:135], v[108:111], a[232:235]\n" \
"ds_read_b64_tr_b16 v[132:133], v34 offset:0\n" \
"ds_read_b64_tr_b16 v[134:135], v34 offset:1024\n" \
"v_mfma_f32_16x16x32_bf16 a[232:235], v[136:139], v[112:115], a[232:235]\n" \
"s_add_u32 m0, %[mB], 0x8400\n" \
"s_add_u32 s85, s63, 0x8000\n" \
"v_mfma_f32_16x16x32_bf16 a[140:143], v[144:147], v[88:91], a[140:143]\n" \
"buffer_load_dwordx4 v26, s[68:71], s85 offen lds\n" \
"v_mfma_f32_16x16x32_bf16 a[140:143], v[140:143], v[84:87], a[140:143]\n" \
"ds_read_b64_tr_b16 v[136:137], v34 offset:8192\n" \
"ds_read_b64_tr_b16 v[138:139], v34 offset:9216\n" \
"v_mfma_f32_16x16x32_bf16 a[172:175], v[140:143], v[92:95], a[172:175]\n" \
"v_mfma_f32_16x16x32_bf16 a[172:175], v[144:147], v[96:99], a[172:175]\n" \
"s_add_u32 m0, %[mB], 0xc400\n" \
"s_add_u32 s84, s63, 0x8000\n" \
"v_mfma_f32_16x16x32_bf16 a[204:207], v[144:147], v[104:107], a[204:207]\n" \
"buffer_load_dwordx4 v26, s[72:75], s84 offen lds\n" \
"v_mfma_f32_16x16x32_bf16 a[204:207], v[140:143], v[100:103], a[204:207]\n" \
"v_mfma_f32_16x16x32_bf16 a[236:239], v[140:143], v[108:111], a[236:239]\n" \
"ds_read_b64_tr_b16 v[140:141], v35 offset:0\n" \
"ds_read_b64_tr_b16 v[142:143], v35 offset:1024\n" \
"v_mfma_f32_16x16x32_bf16 a[236:239], v[144:147], v[112:115], a[236:239]\n" \
"s_add_u32 m0, %[mA], 0x9000\n" \
"s_add_u32 s85, s62, 0x8000\n" \
"v_mfma_f32_16x16x32_bf16 a[144:147], v[88:91], v[152:155], a[144:147]\n" \
"buffer_load_dwordx4 v22, s[64:67], s85 offen lds\n" \
"v_mfma_f32_16x16x32_bf16 a[144:147], v[84:87], v[148:151], a[144:147]\n" \
"ds_read_b64_tr_b16 v[144:145], v35 offset:8192\n" \
"ds_read_b64_tr_b16 v[146:147], v35 offset:9216\n" \
"v_mfma_f32_16x16x32_bf16 a[176:179], v[92:95], v[148:151], a[176:179]\n" \
"v_mfma_f32_16x16x32_bf16 a[176:179], v[96:99], v[152:155], a[176:179]\n" \
"s_add_u32 m0, %[mA], 0x9400\n" \
"L_pf3:\n" \
"s_cbranch_execz L_pf7\n" \
"s_nop 0\n" \
"s_add_u32 s84, s62, 0xa000\n" \
"v_mfma_f32_16x16x32_bf16 a[208:211], v[104:107], v[152:155], a[208:211]\n" \
"buffer_load_dwordx4 v23, s[64:67], s84 offen lds\n" \
"v_mfma_f32_16x16x32_bf16 a[208:211], v[100:103], v[148:151], a[208:211]\n" \
"v_mfma_f32_16x16x32_bf16 a[240:243], v[108:111], v[148:151], a[240:243]\n" \
"ds_read_b64_tr_b16 v[148:149], v32 offset:16384\n" \
"ds_read_b64_tr_b16 v[150:151], v32 offset:17408\n" \
"v_mfma_f32_16x16x32_bf16 a[240:243], v[112:115], v[152:155], a[240:243]\n" \
"s_add_u32 m0, %[mB], 0x8800\n" \
"s_add_u32 s85, s63, 0x10000\n" \
"v_mfma_f32_16x16x32_bf16 a[148:151], v[88:91], v[160:163], a[148:151]\n" \
"buffer_load_dwordx4 v27, s[68:71], s85 offen lds\n" \
"v_mfma_f32_16x16x32_bf16 a[148:151], v[84:87], v[156:159], a[148:151]\n" \
"ds_read_b64_tr_b16 v[152:153], v32 offset:24576\n" \
"ds_read_b64_tr_b16 v[154:155], v32 offset:25600\n" \
"v_mfma_f32_16x16x32_bf16 a[180:183], v[92:95], v[156:159], a[180:183]\n" \
"v_mfma_f32_16x16x32_bf16 a[180:183], v[96:99], v[160:163], a[180:183]\n" \
"s_add_u32 m0, %[mB], 0xc800\n" \
"s_add_u32 s84, s63, 0x10000\n" \
"v_mfma_f32_16x16x32_bf16 a[212:215], v[104:107], v[160:163], a[212:215]\n" \
"buffer_load_dwordx4 v27, s[72:75], s84 offen lds\n" \
"v_mfma_f32_16x16x32_bf16 a[212:215], v[100:103], v[156:159], a[212:215]\n" \
"v_mfma_f32_16x16x32_bf16 a[244:247], v[108:111], v[156:159], a[244:247]\n" \
"ds_read_b64_tr_b16 v[156:157], v33 offset:16384\n" \
"ds_read_b64_tr_b16 v[158:159], v33 offset:17408\n" \
"v_mfma_f32_16x16x32_bf16 a[244:247], v[112:115], v[160:163], a[244:247]\n" \
"ds_read_b64_tr_b16 v[160:161], v33 offset:24576\n" \
"ds_read_b64_tr_b16 v[162:163], v33 offset:25600\n" \
"v_mfma_f32_16x16x32_bf16 a[152:155], v[88:91], v[168:171], a[152:155]\n" \
"v_mfma_f32_16x16x32_bf16 a[152:155], v[84:87], v[164:167], a[152:155]\n" \
"v_mfma_f32_16x16x32_bf16 a[184:187], v[92:95], v[164:167], a[184:187]\n" \
"v_mfma_f32_16x16x32_bf16 a[184:187], v[96:99], v[168:171], a[184:187]\n" \
"v_mfma_f32_16x16x32_bf16 a[216:219], v[104:107], v[168:171], a[216:219]\n" \
"v_mfma_f32_16x16x32_bf16 a[216:219], v[100:103], v[164:167], a[216:219]\n" \
"v_mfma_f32_16x16x32_bf16 a[248:251], v[108:111], v[164:167], a[248:251]\n" \
"ds_read_b64_tr_b16 v[164:165], v34 offset:16384\n" \
"ds_read_b64_tr_b16 v[166:167], v34 offset:17408\n" \
"v_mfma_f32_16x16x32_bf16 a[248:251], v[112:115], v[168:171], a[248:251]\n" \
"ds_read_b64_tr_b16 v[168:169], v34 offset:24576\n" \
"ds_read_b64_tr_b16 v[170:171], v34 offset:25600\n" \
"v_mfma_f32_16x16x32_bf16 a[156:159], v[88:91], v[176:179], a[156:159]\n" \
"ds_read_b128 v[88:91], v29 offset:0\n" \
"v_mfma_f32_16x16x32_bf16 a[156:159], v[84:87], v[172:175], a[156:159]\n" \
"ds_read_b128 v[84:87], v28 offset:0\n" \
"v_mfma_f32_16x16x32_bf16 a[188:191], v[92:95], v[172:175], a[188:191]\n" \
"ds_read_b128 v[92:95], v28 offset:2048\n" \
"v_mfma_f32_16x16x32_bf16 a[188:191], v[96:99], v[176:179], a[188:191]\n" \
"ds_read_b128 v[96:99], v29 offset:2048\n" \
"v_mfma_f32_16x16x32_bf16 a[220:223], v[104:107], v[176:179], a[220:223]\n" \
"ds_read_b128 v[104:107], v29 offset:4096\n" \
"v_mfma_f32_16x16x32_bf16 a[220:223], v[100:103], v[172:175], a[220:223]\n" \
"ds_read_b128 v[100:103], v28 offset:4096\n" \
"v_mfma_f32_16x16x32_bf16 a[252:255], v[108:111], v[172:175], a[252:255]\n" \
"ds_read_b64_tr_b16 v[172:173], v35 offset:16384\n" \
"ds_read_b64_tr_b16 v[174:175], v35 offset:17408\n" \
"v_mfma_f32_16x16x32_bf16 a[252:255], v[112:115], v[176:179], a[252:255]\n" \
"s_add_u32 m0, %[mA], 0x9800\n" \
"s_waitcnt lgkmcnt(12)\n" \
"s_nop 0\n" \
"v_mfma_f32_16x16x32_bf16 a[0:3], v[120:123], v[56:59], a[0:3]\n" \
"s_add_u32 s85, s62, 0xc000\n" \
"ds_read_b64_tr_b16 v[176:177], v35 offset:24576\n" \
"v_mfma_f32_16x16x32_bf16 a[0:3], v[116:119], v[52:55], a[0:3]\n" \
"buffer_load_dwordx4 v22, s[64:67], s85 offen lds\n" \
"v_mfma_f32_16x16x32_bf16 a[32:35], v[116:119], v[60:63], a[32:35]\n" \
"s_add_u32 m0, %[mA], 0x9c00\n" \
"s_add_u32 s84, s62, 0xe000\n" \
"v_mfma_f32_16x16x32_bf16 a[32:35], v[120:123], v[64:67], a[32:35]\n" \
"ds_read_b64_tr_b16 v[178:179], v35 offset:25600\n" \
"ds_read_b128 v[108:111], v28 offset:6144\n" \
"v_mfma_f32_16x16x32_bf16 a[64:67], v[120:123], v[72:75], a[64:67]\n" \
"buffer_load_dwordx4 v23, s[64:67], s84 offen lds\n" \
"v_mfma_f32_16x16x32_bf16 a[64:67], v[116:119], v[68:71], a[64:67]\n" \
"s_add_u32 m0, %[mB], 0x8c00\n" \
"s_add_u32 s85, s63, 0x18000\n" \
"v_mfma_f32_16x16x32_bf16 a[96:99], v[116:119], v[76:79], a[96:99]\n" \
"ds_read_b128 v[112:115], v29 offset:6144\n" \
"s_mov_b32 s62, s23\n" \
"v_mfma_f32_16x16x32_bf16 a[96:99], v[120:123], v[80:83], a[96:99]\n" \
"buffer_load_dwordx4 v27, s[68:71], s85 offen lds\n" \
"v_mfma_f32_16x16x32_bf16 a[4:7], v[128:131], v[56:59], a[4:7]\n" \
"s_add_u32 m0, %[mB], 0xcc00\n" \
"s_add_u32 s84, s63, 0x18000\n" \
"v_mfma_f32_16x16x32_bf16 a[4:7], v[124:127], v[52:55], a[4:7]\n" \
"s_mov_b32 s68, s10\n" \
"s_and_b32 s69, s11, 0xffff\n" \
"v_mfma_f32_16x16x32_bf16 a[36:39], v[124:127], v[60:63], a[36:39]\n" \
"buffer_load_dwordx4 v27, s[72:75], s84 offen lds\n" \
"s_mov_b32 s73, s52\n" \
"s_add_u32 s74, s52, 0x4000\n" \
"s_add_u32 s75, s52, 0x8000\n" \
"v_mfma_f32_16x16x32_bf16 a[36:39], v[128:131], v[64:67], a[36:39]\n" \
"s_lshl_b32 s72, s62, 6\n" \
"s_add_u32 s72, s72, s37\n" \
"v_mfma_f32_16x16x32_bf16 a[68:71], v[128:131], v[72:75], a[68:71]\n" \
"v_bfe_u32 v44, v20, 2, 5\n" \
"s_mov_b32 s70, 0x480000\n" \
"v_mfma_f32_16x16x32_bf16 a[68:71], v[124:127], v[68:71], a[68:71]\n" \
"buffer_load_dword v44, v44, s[80:83], s39 offen offset:64 sc1\n" \
"v_mfma_f32_16x16x32_bf16 a[100:103], v[124:127], v[76:79], a[100:103]\n" \
"v_add_u32_e32 v22, s61, v28\n" \
"v_add_u32_e64 v23, s61, v29\n" \
"v_mfma_f32_16x16x32_bf16 a[100:103], v[128:131], v[80:83], a[100:103]\n" \
"s_mov_b32 m0, %[mS]\n" \
"s_mov_b32 s86, s18\n" \
"v_mfma_f32_16x16x32_bf16 a[8:11], v[136:139], v[56:59], a[8:11]\n" \
"s_mov_b32 s87, s19\n" \
"v_mfma_f32_16x16x32_bf16 a[8:11], v[132:135], v[52:55], a[8:11]\n" \
"v_mfma_f32_16x16x32_bf16 a[40:43], v[132:135], v[60:63], a[40:43]\n" \
"v_mfma_f32_16x16x32_bf16 a[40:43], v[136:139], v[64:67], a[40:43]\n" \
"v_mfma_f32_16x16x32_bf16 a[72:75], v[136:139], v[72:75], a[72:75]\n" \
"v_mfma_f32_16x16x32_bf16 a[72:75], v[132:135], v[68:71], a[72:75]\n" \
"s_mov_b32 exec_lo, 0xfffc0000\n" \
"s_mov_b32 exec_hi, 0xf\n" \
"global_load_lds_dword v16, s[86:87]\n" \
"s_mov_b64 exec, -1\n" \
"v_mfma_f32_16x16x32_bf16 a[104:107], v[132:135], v[76:79], a[104:107]\n" \
"s_mov_b32 m0, %[mS]\n" \
"s_mov_b32 s86, s16\n" \
"v_mfma_f32_16x16x32_bf16 a[104:107], v[136:139], v[80:83], a[104:107]\n" \
"s_mov_b32 s87, s17\n" \
"v_mfma_f32_16x16x32_bf16 a[12:15], v[144:147], v[56:59], a[12:15]\n" \
"v_mfma_f32_16x16x32_bf16 a[12:15], v[140:143], v[52:55], a[12:15]\n" \
"v_mfma_f32_16x16x32_bf16 a[44:47], v[140:143], v[60:63], a[44:47]\n" \
"s_mov_b32 exec_lo, 0x0\n" \
"s_mov_b32 exec_hi, 0x3f0\n" \
"global_load_lds_dword v16, s[86:87]\n" \
"s_mov_b64 exec, -1\n" \
"v_mfma_f32_16x16x32_bf16 a[44:47], v[144:147], v[64:67], a[44:47]\n" \
"v_mfma_f32_16x16x32_bf16 a[76:79], v[144:147], v[72:75], a[76:79]\n" \
"v_mfma_f32_16x16x32_bf16 a[76:79], v[140:143], v[68:71], a[76:79]\n" \
"v_mfma_f32_16x16x32_bf16 a[108:111], v[140:143], v[76:79], a[108:111]\n" \
"v_mfma_f32_16x16x32_bf16 a[108:111], v[144:147], v[80:83], a[108:111]\n" \
"v_mfma_f32_16x16x32_bf16 a[16:19], v[56:59], v[152:155], a[16:19]\n" \
"v_mfma_f32_16x16x32_bf16 a[16:19], v[52:55], v[148:151], a[16:19]\n" \
"v_mfma_f32_16x16x32_bf16 a[48:51], v[60:63], v[148:151], a[48:51]\n" \
"v_mfma_f32_16x16x32_bf16 a[48:51], v[64:67], v[152:155], a[48:51]\n" \
"v_mfma_f32_16x16x32_bf16 a[80:83], v[72:75], v[152:155], a[80:83]\n" \
"v_mfma_f32_16x16x32_bf16 a[80:83], v[68:71], v[148:151], a[80:83]\n" \
"v_mfma_f32_16x16x32_bf16 a[112:115], v[76:79], v[148:151], a[112:115]\n" \
"v_mfma_f32_16x16x32_bf16 a[112:115], v[80:83], v[152:155], a[112:115]\n" \
"v_mfma_f32_16x16x32_bf16 a[20:23], v[56:59], v[160:163], a[20:23]\n" \
"v_mfma_f32_16x16x32_bf16 a[20:23], v[52:55], v[156:159], a[20:23]\n" \
"v_mfma_f32_16x16x32_bf16 a[52:55], v[60:63], v[156:159], a[52:55]\n" \
"v_mfma_f32_16x16x32_bf16 a[52:55], v[64:67], v[160:163], a[52:55]\n" \
"v_mfma_f32_16x16x32_bf16 a[84:87], v[72:75], v[160:163], a[84:87]\n" \
"v_mfma_f32_16x16x32_bf16 a[84:87], v[68:71], v[156:159], a[84:87]\n" \
"v_mfma_f32_16x16x32_bf16 a[116:119], v[76:79], v[156:159], a[116:119]\n" \
"v_mfma_f32_16x16x32_bf16 a[116:119], v[80:83], v[160:163], a[116:119]\n" \
"s_waitcnt lgkmcnt(0)\n" \
"v_mfma_f32_16x16x32_bf16 a[24:27], v[56:59], v[168:171], a[24:27]\n" \
"v_mfma_f32_16x16x32_bf16 a[24:27], v[52:55], v[164:167], a[24:27]\n" \
"v_mfma_f32_16x16x32_bf16 a[56:59], v[60:63], v[164:167], a[56:59]\n" \
"v_mfma_f32_16x16x32_bf16 a[56:59], v[64:67], v[168:171], a[56:59]\n" \
"v_mfma_f32_16x16x32_bf16 a[88:91], v[72:75], v[168:171], a[88:91]\n" \
"v_mfma_f32_16x16x32_bf16 a[88:91], v[68:71], v[164:167], a[88:91]\n" \
"v_mfma_f32_16x16x32_bf16 a[120:123], v[76:79], v[164:167], a[120:123]\n" \
"v_mfma_f32_16x16x32_bf16 a[120:123], v[80:83], v[168:171], a[120:123]\n" \
"v_mfma_f32_16x16x32_bf16 a[28:31], v[56:59], v[176:179], a[28:31]\n" \
"s_waitcnt vmcnt(2)\n" \
"v_cmp_ne_u32_e32 vcc, %[tag], v44\n" \
"v_mfma_f32_16x16x32_bf16 a[28:31], v[52:55], v[172:175], a[28:31]\n" \
"v_mfma_f32_16x16x32_bf16 a[60:63], v[60:63], v[172:175], a[60:63]\n" \
"v_mfma_f32_16x16x32_bf16 a[60:63], v[64:67], v[176:179], a[60:63]\n" \
"v_mfma_f32_16x16x32_bf16 a[92:95], v[72:75], v[176:179], a[92:95]\n" \
"v_mfma_f32_16x16x32_bf16 a[92:95], v[68:71], v[172:175], a[92:95]\n" \
"s_cbranch_vccnz L_pl_%=\n" \
"L_rl_%=:\n" \
"s_lshl_b32 s63, s62, 12\n" \
"v_mfma_f32_16x16x32_bf16 a[124:127], v[76:79], v[172:175], a[124:127]\n" \
"v_mfma_f32_16x16x32_bf16 a[124:127], v[80:83], v[176:179], a[124:127]\n" \
"s_barrier\n" \
"v_mfma_f32_16x16x32_bf16 a[144:147], v[88:91], v[152:155], a[144:147]\n" \
"s_add_u32 m0, s40, 0x0\n" \
"s_add_u32 s85, s72, 0x0\n" \
"v_mfma_f32_16x16x32_bf16 a[144:147], v[84:87], v[148:151], a[144:147]\n" \
"buffer_load_dwordx4 v20, s[80:83], s85 offen sc1 lds\n" \
"v_mfma_f32_16x16x32_bf16 a[176:179], v[92:95], v[148:151], a[176:179]\n" \
"s_add_u32 m0, s40, 0x1000\n" \
"s_add_u32 s84, s72, 0x1000\n" \
"v_mfma_f32_16x16x32_bf16 a[176:179], v[96:99], v[152:155], a[176:179]\n" \
"ds_read_b128 v[56:59], v29 offset:49152\n" \
"ds_read_b128 v[52:55], v28 offset:49152\n" \
"v_mfma_f32_16x16x32_bf16 a[208:211], v[104:107], v[152:155], a[208:211]\n" \
"buffer_load_dwordx4 v20, s[80:83], s84 offen sc1 lds\n" \
"v_mfma_f32_16x16x32_bf16 a[208:211], v[100:103], v[148:151], a[208:211]\n" \
"ds_read_b128 v[60:63], v28 offset:51200\n" \
"ds_read_b128 v[64:67], v29 offset:51200\n" \
"v_mfma_f32_16x16x32_bf16 a[240:243], v[108:111], v[148:151], a[240:243]\n" \
"s_add_u32 m0, s40, 0x3000\n" \
"s_add_u32 s85, s62, 0x0\n" \
"v_mfma_f32_16x16x32_bf16 a[240:243], v[112:115], v[152:155], a[240:243]\n" \
"buffer_load_dwordx4 v21, s[76:79], s85 offen lds\n" \
"v_mfma_f32_16x16x32_bf16 a[148:151], v[88:91], v[160:163], a[148:151]\n" \
"ds_read_b64_tr_b16 v[152:153], v32 offset:57344\n" \
"ds_read_b64_tr_b16 v[154:155], v32 offset:58368\n" \
"v_mfma_f32_16x16x32_bf16 a[148:151], v[84:87], v[156:159], a[148:151]\n" \
"ds_read_b64_tr_b16 v[148:149], v32 offset:49152\n" \
"ds_read_b64_tr_b16 v[150:151], v32 offset:50176\n" \
"v_mfma_f32_16x16x32_bf16 a[180:183], v[92:95], v[156:159], a[180:183]\n" \
"s_add_u32 m0, %[mA], 0x0\n" \
"s_add_u32 s84, s62, 0x0\n" \
"s_nop 0\n" \
"v_mfma_f32_16x16x32_bf16 a[180:183], v[96:99], v[160:163], a[180:183]\n" \
"buffer_load_dwordx4 v24, s[76:79], s84 offen lds\n" \
"v_mfma_f32_16x16x32_bf16 a[212:215], v[104:107], v[160:163], a[212:215]\n" \
"ds_read_b128 v[72:75], v29 offset:53248\n" \
"ds_read_b128 v[68:71], v28 offset:53248\n" \
"v_mfma_f32_16x16x32_bf16 a[212:215], v[100:103], v[156:159], a[212:215]\n" \
"s_add_u32 m0, %[mA], 0x400\n" \
"s_add_u32 s85, s62, 0x2400\n" \
"v_mfma_f32_16x16x32_bf16 a[244:247], v[108:111], v[156:159], a[244:247]\n" \
"buffer_load_dwordx4 v25, s[76:79], s85 offen lds\n" \
"v_mfma_f32_16x16x32_bf16 a[244:247], v[112:115], v[160:163], a[244:247]\n" \
"ds_read_b128 v[76:79], v28 offset:55296\n" \
"ds_read_b128 v[80:83], v29 offset:55296\n" \
"v_mfma_f32_16x16x32_bf16 a[152:155], v[88:91], v[168:171], a[152:155]\n" \
"ds_read_b64_tr_b16 v[160:161], v33 offset:57344\n" \
"ds_read_b64_tr_b16 v[162:163], v33 offset:58368\n" \
"v_mfma_f32_16x16x32_bf16 a[152:155], v[84:87], v[164:167], a[152:155]\n" \
"s_add_u32 m0, %[mB], 0x0\n" \
"s_add_u32 s84, s63, 0x0\n" \
"v_mfma_f32_16x16x32_bf16 a[184:187], v[92:95], v[164:167], a[184:187]\n" \
"buffer_load_dwordx4 v26, s[68:71], s84 offen lds\n" \
"v_mfma_f32_16x16x32_bf16 a[184:187], v[96:99], v[168:171], a[184:187]\n" \
"ds_read_b64_tr_b16 v[156:157], v33 offset:49152\n" \
"ds_read_b64_tr_b16 v[158:159], v33 offset:50176\n" \
"v_mfma_f32_16x16x32_bf16 a[216:219], v[104:107], v[168:171], a[216:219]\n" \
"v_mfma_f32_16x16x32_bf16 a[216:219], v[100:103], v[164:167], a[216:219]\n" \
"s_add_u32 m0, %[mA], 0x800\n" \
"s_add_u32 s85, s62, 0x4800\n" \
"v_mfma_f32_16x16x32_bf16 a[248:251], v[108:111], v[164:167], a[248:251]\n" \
"buffer_load_dwordx4 v24, s[76:79], s85 offen lds\n" \
"v_mfma_f32_16x16x32_bf16 a[248:251], v[112:115], v[168:171], a[248:251]\n" \
"ds_read_b64_tr_b16 v[168:169], v34 offset:57344\n" \
"ds_read_b64_tr_b16 v[170:171], v34 offset:58368\n" \
"v_mfma_f32_16x16x32_bf16 a[156:159], v[88:91], v[176:179], a[156:159]\n" \
"s_add_u32 m0, %[mA], 0xc00\n" \
"s_add_u32 s84, s62, 0x6c00\n" \
"v_mfma_f32_16x16x32_bf16 a[156:159], v[84:87], v[172:175], a[156:159]\n" \
"buffer_load_dwordx4 v25, s[76:79], s84 offen lds\n" \
"v_mfma_f32_16x16x32_bf16 a[188:191], v[92:95], v[172:175], a[188:191]\n" \
"ds_read_b64_tr_b16 v[164:165], v34 offset:49152\n" \
"ds_read_b64_tr_b16 v[166:167], v34 offset:50176\n" \
"v_mfma_f32_16x16x32_bf16 a[188:191], v[96:99], v[176:179], a[188:191]\n" \
"v_mfma_f32_16x16x32_bf16 a[220:223], v[104:107], v[176:179], a[220:223]\n" \
"s_add_u32 m0, %[mB], 0x400\n" \
"s_add_u32 s85, s63, 0x8000\n" \
"v_mfma_f32_16x16x32_bf16 a[220:223], v[100:103], v[172:175], a[220:223]\n" \
"buffer_load_dwordx4 v26, s[68:71], s85 offen lds\n" \
"v_mfma_f32_16x16x32_bf16 a[252:255], v[108:111], v[172:175], a[252:255]\n" \
"ds_read_b64_tr_b16 v[172:173], v35 offset:49152\n" \
"ds_read_b64_tr_b16 v[174:175], v35 offset:50176\n" \
"v_mfma_f32_16x16x32_bf16 a[252:255], v[112:115], v[176:179], a[252:255]\n" \
"ds_read_b64_tr_b16 v[176:177], v35 offset:57344\n" \
"ds_read_b64_tr_b16 v[178:179], v35 offset:58368\n" \
"v_mfma_f32_16x16x32_bf16 a[128:131], v[120:123], v[88:91], a[128:131]\n" \
"s_add_u32 m0, %[mA], 0x1000\n" \
"s_add_u32 s84, s62, 0x9000\n" \
"v_mfma_f32_16x16x32_bf16 a[128:131], v[116:119], v[84:87], a[128:131]\n" \
"buffer_load_dwordx4 v24, s[76:79], s84 offen lds\n" \
"v_mfma_f32_16x16x32_bf16 a[160:163], v[116:119], v[92:95], a[160:163]\n" \
"v_mfma_f32_16x16x32_bf16 a[160:163], v[120:123], v[96:99], a[160:163]\n" \
"s_add_u32 m0, %[mA], 0x1400\n" \
"s_add_u32 s85, s62, 0xb400\n" \
"v_mfma_f32_16x16x32_bf16 a[192:195], v[120:123], v[104:107], a[192:195]\n" \
"buffer_load_dwordx4 v25, s[76:79], s85 offen lds\n" \
"v_mfma_f32_16x16x32_bf16 a[192:195], v[116:119], v[100:103], a[192:195]\n" \
"v_mfma_f32_16x16x32_bf16 a[224:227], v[116:119], v[108:111], a[224:227]\n" \
"ds_read_b64_tr_b16 v[116:117], v32 offset:32768\n" \
"ds_read_b64_tr_b16 v[118:119], v32 offset:33792\n" \
"v_mfma_f32_16x16x32_bf16 a[224:227], v[120:123], v[112:115], a[224:227]\n" \
"s_add_u32 m0, %[mB], 0x800\n" \
"s_add_u32 s84, s63, 0x10000\n" \
"v_mfma_f32_16x16x32_bf16 a[132:135], v[128:131], v[88:91], a[132:135]\n" \
"buffer_load_dwordx4 v27, s[68:71], s84 offen lds\n" \
"v_mfma_f32_16x16x32_bf16 a[132:135], v[124:127], v[84:87], a[132:135]\n" \
"ds_read_b64_tr_b16 v[120:121], v32 offset:40960\n" \
"ds_read_b64_tr_b16 v[122:123], v32 offset:41984\n" \
"v_mfma_f32_16x16x32_bf16 a[164:167], v[124:127], v[92:95], a[164:167]\n" \
"v_mfma_f32_16x16x32_bf16 a[164:167], v[128:131], v[96:99], a[164:167]\n" \
"s_add_u32 m0, %[mA], 0x1800\n" \
"s_add_u32 s85, s62, 0xd800\n" \
"v_mfma_f32_16x16x32_bf16 a[196:199], v[128:131], v[104:107], a[196:199]\n" \
"buffer_load_dwordx4 v24, s[76:79], s85 offen lds\n" \
"v_mfma_f32_16x16x32_bf16 a[196:199], v[124:127], v[100:103], a[196:199]\n" \
"v_mfma_f32_16x16x32_bf16 a[228:231], v[124:127], v[108:111], a[228:231]\n" \
"ds_read_b64_tr_b16 v[124:125], v33 offset:32768\n" \
"ds_read_b64_tr_b16 v[126:127], v33 offset:33792\n" \
"v_mfma_f32_16x16x32_bf16 a[228:231], v[128:131], v[112:115], a[228:231]\n" \
"ds_read_b64_tr_b16 v[128:129], v33 offset:40960\n" \
"ds_read_b64_tr_b16 v[130:131], v33 offset:41984\n" \
"v_mfma_f32_16x16x32_bf16 a[136:139], v[136:139], v[88:91], a[136:139]\n" \
"v_mfma_f32_16x16x32_bf16 a[136:139], v[132:135], v[84:87], a[136:139]\n" \
"v_mfma_f32_16x16x32_bf16 a[168:171], v[132:135], v[92:95], a[168:171]\n" \
"v_mfma_f32_16x16x32_bf16 a[168:171], v[136:139], v[96:99], a[168:171]\n" \
"v_mfma_f32_16x16x32_bf16 a[200:203], v[136:139], v[104:107], a[200:203]\n" \
"v_mfma_f32_16x16x32_bf16 a[200:203], v[132:135], v[100:103], a[200:203]\n" \
"v_mfma_f32_16x16x32_bf16 a[232:235], v[132:135], v[108:111], a[232:235]\n" \
"ds_read_b64_tr_b16 v[132:133], v34 offset:32768\n" \
"ds_read_b64_tr_b16 v[134:135], v34 offset:33792\n" \
"v_mfma_f32_16x16x32_bf16 a[232:235], v[136:139], v[112:115], a[232:235]\n" \
"ds_read_b64_tr_b16 v[136:137], v34 offset:40960\n" \
"ds_read_b64_tr_b16 v[138:139], v34 offset:41984\n" \
"v_mfma_f32_16x16x32_bf16 a[140:143], v[144:147], v[88:91], a[140:143]\n" \
"ds_read_b128 v[88:91], v29 offset:32768\n" \
"v_mfma_f32_16x16x32_bf16 a[140:143], v[140:143], v[84:87], a[140:143]\n" \
"ds_read_b128 v[84:87], v28 offset:32768\n" \
"v_mfma_f32_16x16x32_bf16 a[172:175], v[140:143], v[92:95], a[172:175]\n" \
"ds_read_b128 v[92:95], v28 offset:34816\n" \
"v_mfma_f32_16x16x32_bf16 a[172:175], v[144:147], v[96:99], a[172:175]\n" \
"ds_read_b128 v[96:99], v29 offset:34816\n" \
"v_mfma_f32_16x16x32_bf16 a[204:207], v[144:147], v[104:107], a[204:207]\n" \
"ds_read_b128 v[104:107], v29 offset:36864\n" \
"v_mfma_f32_16x16x32_bf16 a[204:207], v[140:143], v[100:103], a[204:207]\n" \
"ds_read_b128 v[100:103], v28 offset:36864\n" \
"v_mfma_f32_16x16x32_bf16 a[236:239], v[140:143], v[108:111], a[236:239]\n" \
"ds_read_b64_tr_b16 v[140:141], v35 offset:32768\n" \
"ds_read_b64_tr_b16 v[142:143], v35 offset:33792\n" \
"v_mfma_f32_16x16x32_bf16 a[236:239], v[144:147], v[112:115], a[236:239]\n" \
"s_add_u32 m0, %[mA], 0x1c00\n" \
"s_waitcnt lgkmcnt(12)\n" \
"s_nop 0\n" \
"v_mfma_f32_16x16x32_bf16 a[16:19], v[56:59], v[152:155], a[16:19]\n" \
"s_add_u32 s85, s62, 0xfc00\n" \
"ds_read_b64_tr_b16 v[144:145], v35 offset:40960\n" \
"v_mfma_f32_16x16x32_bf16 a[16:19], v[52:55], v[148:151], a[16:19]\n" \
"buffer_load_dwordx4 v25, s[76:79], s85 offen lds\n" \
"v_mfma_f32_16x16x32_bf16 a[48:51], v[60:63], v[148:151], a[48:51]\n" \
"s_add_u32 m0, %[mB], 0xc00\n" \
"s_add_u32 s84, s63, 0x18000\n" \
"v_mfma_f32_16x16x32_bf16 a[48:51], v[64:67], v[152:155], a[48:51]\n" \
"ds_read_b64_tr_b16 v[146:147], v35 offset:41984\n" \
"ds_read_b128 v[108:111], v28 offset:38912\n" \
"v_mfma_f32_16x16x32_bf16 a[80:83], v[72:75], v[152:155], a[80:83]\n" \
"buffer_load_dwordx4 v27, s[68:71], s84 offen lds\n" \
"v_mfma_f32_16x16x32_bf16 a[80:83], v[68:71], v[148:151], a[80:83]\n" \
"ds_read_b128 v[112:115], v29 offset:38912\n" \
"s_add_u32 s62, s62, 0x80\n" \
"v_mfma_f32_16x16x32_bf16 a[112:115], v[76:79], v[148:151], a[112:115]\n" \
"s_cmp_lt_u32 s62, 0x480\n" \
"s_cselect_b32 s62, s62, 0\n" \
"v_mfma_f32_16x16x32_bf16 a[112:115], v[80:83], v[152:155], a[112:115]\n" \
"s_lshl_b32 s72, s62, 6\n" \
"s_add_u32 s72, s72, s37\n" \
"v_mfma_f32_16x16x32_bf16 a[20:23], v[56:59], v[160:163], a[20:23]\n" \
"s_lshl_b32 s63, s62, 12\n" \
"v_add_u32_e64 v28, s59, v28\n" \
"v_mfma_f32_16x16x32_bf16 a[20:23], v[52:55], v[156:159], a[20:23]\n" \
"v_add_u32_e64 v29, s59, v29\n" \
"v_accvgpr_read_b32 v184, a16\n" \
"v_mfma_f32_16x16x32_bf16 a[52:55], v[60:63], v[156:159], a[52:55]\n" \
"v_accvgpr_read_b32 v185, a17\n" \
"v_accvgpr_read_b32 v186, a18\n" \
"v_mfma_f32_16x16x32_bf16 a[52:55], v[64:67], v[160:163], a[52:55]\n" \
"v_accvgpr_read_b32 v187, a19\n" \
"v_accvgpr_write_b32 a16, 0\n" \
"v_mfma_f32_16x16x32_bf16 a[84:87], v[72:75], v[160:163], a[84:87]\n" \
"v_accvgpr_write_b32 a17, 0\n" \
"v_accvgpr_write_b32 a18, 0\n" \
"v_mfma_f32_16x16x32_bf16 a[84:87], v[68:71], v[156:159], a[84:87]\n" \
"v_accvgpr_write_b32 a19, 0\n" \
"v_accvgpr_read_b32 v180, a48\n" \
"v_mfma_f32_16x16x32_bf16 a[116:119], v[76:79], v[156:159], a[116:119]\n" \
"v_accvgpr_read_b32 v181, a49\n" \
"v_accvgpr_read_b32 v182, a50\n" \
"v_mfma_f32_16x16x32_bf16 a[116:119], v[80:83], v[160:163], a[116:119]\n" \
"v_accvgpr_read_b32 v183, a51\n" \
"v_accvgpr_read_b32 v188, a80\n" \
"v_mfma_f32_16x16x32_bf16 a[24:27], v[56:59], v[168:171], a[24:27]\n" \
"v_accvgpr_read_b32 v189, a81\n" \
"v_accvgpr_read_b32 v190, a82\n" \
"v_mfma_f32_16x16x32_bf16 a[24:27], v[52:55], v[164:167], a[24:27]\n" \
"v_accvgpr_read_b32 v191, a83\n" \
"v_cvt_pk_bf16_f32 v184, v184, v185\n" \
"v_mfma_f32_16x16x32_bf16 a[56:59], v[60:63], v[164:167], a[56:59]\n" \
"v_cvt_pk_bf16_f32 v185, v186, v187\n" \
"v_cvt_pk_bf16_f32 v186, v180, v181\n" \
"v_mfma_f32_16x16x32_bf16 a[56:59], v[64:67], v[168:171], a[56:59]\n" \
"v_cvt_pk_bf16_f32 v187, v182, v183\n" \
"v_accvgpr_read_b32 v180, a112\n" \
"v_mfma_f32_16x16x32_bf16 a[88:91], v[72:75], v[168:171], a[88:91]\n" \
"v_accvgpr_read_b32 v181, a113\n" \
"v_accvgpr_read_b32 v182, a114\n" \
"v_mfma_f32_16x16x32_bf16 a[88:91], v[68:71], v[164:167], a[88:91]\n" \
"v_accvgpr_read_b32 v183, a115\n" \
"v_accvgpr_write_b32 a112, 0\n" \
"v_mfma_f32_16x16x32_bf16 a[120:123], v[76:79], v[164:167], a[120:123]\n" \
"v_accvgpr_write_b32 a113, 0\n" \
"v_accvgpr_write_b32 a114, 0\n" \
"v_mfma_f32_16x16x32_bf16 a[120:123], v[80:83], v[168:171], a[120:123]\n" \
"v_accvgpr_write_b32 a115, 0\n" \
"v_accvgpr_read_b32 v192, a20\n" \
"v_mfma_f32_16x16x32_bf16 a[28:31], v[56:59], v[176:179], a[28:31]\n" \
"v_accvgpr_read_b32 v193, a21\n" \
"v_accvgpr_read_b32 v194, a22\n" \
"v_mfma_f32_16x16x32_bf16 a[28:31], v[52:55], v[172:175], a[28:31]\n" \
"v_accvgpr_read_b32 v195, a23\n" \
"v_cvt_pk_bf16_f32 v188, v188, v189\n" \
"v_mfma_f32_16x16x32_bf16 a[60:63], v[60:63], v[172:175], a[60:63]\n" \
"v_cvt_pk_bf16_f32 v189, v190, v191\n" \
"v_cvt_pk_bf16_f32 v190, v180, v181\n" \
"v_mfma_f32_16x16x32_bf16 a[60:63], v[64:67], v[176:179], a[60:63]\n" \
"v_cvt_pk_bf16_f32 v191, v182, v183\n" \
"v_accvgpr_read_b32 v180, a52\n" \
"v_mfma_f32_16x16x32_bf16 a[92:95], v[72:75], v[176:179], a[92:95]\n" \
"v_accvgpr_read_b32 v181, a53\n" \
"v_accvgpr_read_b32 v182, a54\n" \
"v_mfma_f32_16x16x32_bf16 a[92:95], v[68:71], v[172:175], a[92:95]\n" \
"v_accvgpr_read_b32 v183, a55\n" \
"v_accvgpr_read_b32 v196, a84\n" \
"v_mfma_f32_16x16x32_bf16 a[124:127], v[76:79], v[172:175], a[124:127]\n" \
"v_accvgpr_read_b32 v197, a85\n" \
"v_accvgpr_read_b32 v198, a86\n" \
"v_mfma_f32_16x16x32_bf16 a[124:127], v[80:83], v[176:179], a[124:127]\n" \
"v_accvgpr_read_b32 v199, a87\n" \
"v_cvt_pk_bf16_f32 v192, v192, v193\n" \
"v_mfma_f32_16x16x32_bf16 a[0:3], v[120:123], v[56:59], a[0:3]\n" \
"v_cvt_pk_bf16_f32 v193, v194, v195\n" \
"v_cvt_pk_bf16_f32 v194, v180, v181\n" \
"v_mfma_f32_16x16x32_bf16 a[0:3], v[116:119], v[52:55], a[0:3]\n" \
"v_cvt_pk_bf16_f32 v195, v182, v183\n" \
"v_accvgpr_read_b32 v180, a116\n" \
"v_mfma_f32_16x16x32_bf16 a[32:35], v[116:119], v[60:63], a[32:35]\n" \
"v_accvgpr_read_b32 v181, a117\n" \
"v_accvgpr_read_b32 v182, a118\n" \
"v_mfma_f32_16x16x32_bf16 a[32:35], v[120:123], v[64:67], a[32:35]\n" \
"v_accvgpr_read_b32 v183, a119\n" \
"v_accvgpr_write_b32 a116, 0\n" \
"v_mfma_f32_16x16x32_bf16 a[64:67], v[120:123], v[72:75], a[64:67]\n" \
"v_accvgpr_write_b32 a117, 0\n" \
"v_accvgpr_write_b32 a118, 0\n" \
"v_mfma_f32_16x16x32_bf16 a[64:67], v[116:119], v[68:71], a[64:67]\n" \
"v_accvgpr_write_b32 a119, 0\n" \
"v_accvgpr_read_b32 v200, a24\n" \
"v_mfma_f32_16x16x32_bf16 a[96:99], v[116:119], v[76:79], a[96:99]\n" \
"v_accvgpr_read_b32 v201, a25\n" \
"v_accvgpr_read_b32 v202, a26\n" \
"v_mfma_f32_16x16x32_bf16 a[96:99], v[120:123], v[80:83], a[96:99]\n" \
"v_accvgpr_read_b32 v203, a27\n" \
"v_cvt_pk_bf16_f32 v196, v196, v197\n" \
"v_mfma_f32_16x16x32_bf16 a[4:7], v[128:131], v[56:59], a[4:7]\n" \
"v_cvt_pk_bf16_f32 v197, v198, v199\n" \
"v_cvt_pk_bf16_f32 v198, v180, v181\n" \
"v_mfma_f32_16x16x32_bf16 a[4:7], v[124:127], v[52:55], a[4:7]\n" \
"v_cvt_pk_bf16_f32 v199, v182, v183\n" \
"v_accvgpr_read_b32 v180, a56\n" \
"v_mfma_f32_16x16x32_bf16 a[36:39], v[124:127], v[60:63], a[36:39]\n" \
"v_accvgpr_read_b32 v181, a57\n" \
"v_accvgpr_read_b32 v182, a58\n" \
"v_mfma_f32_16x16x32_bf16 a[36:39], v[128:131], v[64:67], a[36:39]\n" \
"v_accvgpr_read_b32 v183, a59\n" \
"v_accvgpr_read_b32 v204, a88\n" \
"v_mfma_f32_16x16x32_bf16 a[68:71], v[128:131], v[72:75], a[68:71]\n" \
"v_accvgpr_read_b32 v205, a89\n" \
"v_accvgpr_read_b32 v206, a90\n" \
"v_mfma_f32_16x16x32_bf16 a[68:71], v[124:127], v[68:71], a[68:71]\n" \
"v_accvgpr_read_b32 v207, a91\n" \
"v_cvt_pk_bf16_f32 v200, v200, v201\n" \
"v_mfma_f32_16x16x32_bf16 a[100:103], v[124:127], v[76:79], a[100:103]\n" \
"v_cvt_pk_bf16_f32 v201, v202, v203\n" \
"v_cvt_pk_bf16_f32 v202, v180, v181\n" \
"v_mfma_f32_16x16x32_bf16 a[100:103], v[128:131], v[80:83], a[100:103]\n" \
"v_cvt_pk_bf16_f32 v203, v182, v183\n" \
"s_waitcnt lgkmcnt(0)\n" \
"s_nop 0\n" \
"v_mfma_f32_16x16x32_bf16 a[8:11], v[136:139], v[56:59], a[8:11]\n" \
"v_accvgpr_read_b32 v180, a120\n" \
"v_accvgpr_read_b32 v181, a121\n" \
"v_mfma_f32_16x16x32_bf16 a[8:11], v[132:135], v[52:55], a[8:11]\n" \
"v_accvgpr_read_b32 v182, a122\n" \
"v_accvgpr_read_b32 v183, a123\n" \
"v_mfma_f32_16x16x32_bf16 a[40:43], v[132:135], v[60:63], a[40:43]\n" \
"v_accvgpr_write_b32 a120, 0\n" \
"v_accvgpr_write_b32 a121, 0\n" \
"v_mfma_f32_16x16x32_bf16 a[40:43], v[136:139], v[64:67], a[40:43]\n" \
"v_accvgpr_write_b32 a122, 0\n" \
"v_accvgpr_write_b32 a123, 0\n" \
"v_mfma_f32_16x16x32_bf16 a[72:75], v[136:139], v[72:75], a[72:75]\n" \
"v_accvgpr_read_b32 v208, a28\n" \
"v_accvgpr_read_b32 v209, a29\n" \
"v_mfma_f32_16x16x32_bf16 a[72:75], v[132:135], v[68:71], a[72:75]\n" \
"v_accvgpr_read_b32 v210, a30\n" \
"v_accvgpr_read_b32 v211, a31\n" \
"v_mfma_f32_16x16x32_bf16 a[104:107], v[132:135], v[76:79], a[104:107]\n" \
"v_cvt_pk_bf16_f32 v204, v204, v205\n" \
"v_cvt_pk_bf16_f32 v205, v206, v207\n" \
"v_mfma_f32_16x16x32_bf16 a[104:107], v[136:139], v[80:83], a[104:107]\n" \
"v_cvt_pk_bf16_f32 v206, v180, v181\n" \
"v_cvt_pk_bf16_f32 v207, v182, v183\n" \
"v_mfma_f32_16x16x32_bf16 a[12:15], v[144:147], v[56:59], a[12:15]\n" \
"v_accvgpr_read_b32 v180, a60\n" \
"v_accvgpr_read_b32 v181, a61\n" \
"v_mfma_f32_16x16x32_bf16 a[12:15], v[140:143], v[52:55], a[12:15]\n" \
"v_accvgpr_read_b32 v182, a62\n" \
"v_accvgpr_read_b32 v183, a63\n" \
"v_mfma_f32_16x16x32_bf16 a[44:47], v[140:143], v[60:63], a[44:47]\n" \
"v_accvgpr_read_b32 v212, a92\n" \
"v_accvgpr_read_b32 v213, a93\n" \
"v_mfma_f32_16x16x32_bf16 a[44:47], v[144:147], v[64:67], a[44:47]\n" \
"v_accvgpr_read_b32 v214, a94\n" \
"v_accvgpr_read_b32 v215, a95\n" \
"v_mfma_f32_16x16x32_bf16 a[76:79], v[144:147], v[72:75], a[76:79]\n" \
"v_cvt_pk_bf16_f32 v208, v208, v209\n" \
"v_cvt_pk_bf16_f32 v209, v210, v211\n" \
"v_mfma_f32_16x16x32_bf16 a[76:79], v[140:143], v[68:71], a[76:79]\n" \
"v_cvt_pk_bf16_f32 v210, v180, v181\n" \
"v_cvt_pk_bf16_f32 v211, v182, v183\n" \
"v_mfma_f32_16x16x32_bf16 a[108:111], v[140:143], v[76:79], a[108:111]\n" \
"v_accvgpr_read_b32 v180, a124\n" \
"v_accvgpr_read_b32 v181, a125\n" \
"v_mfma_f32_16x16x32_bf16 a[108:111], v[144:147], v[80:83], a[108:111]\n" \
"v_accvgpr_read_b32 v182, a126\n" \
"v_accvgpr_read_b32 v183, a127\n" \
"s_waitcnt vmcnt(0)\n" \
"s_barrier\n" \
"v_mfma_f32_16x16x32_bf16 a[128:131], v[120:123], v[88:91], a[128:131]\n" \
"s_add_u32 m0, s40, 0x8000\n" \
"s_add_u32 s85, s72, 0x0\n" \
"s_nop 0\n" \
"v_mfma_f32_16x16x32_bf16 a[128:131], v[116:119], v[84:87], a[128:131]\n" \
"buffer_load_dwordx4 v20, s[80:83], s85 offen sc1 lds\n" \
"v_mfma_f32_16x16x32_bf16 a[160:163], v[116:119], v[92:95], a[160:163]\n" \
"s_add_u32 m0, s40, 0x9000\n" \
"s_add_u32 s84, s72, 0x1000\n" \
"v_mfma_f32_16x16x32_bf16 a[160:163], v[120:123], v[96:99], a[160:163]\n" \
"ds_read_b128 v[52:55], v28 offset:0\n" \
"ds_read_b128 v[56:59], v29 offset:0\n" \
"v_mfma_f32_16x16x32_bf16 a[192:195], v[120:123], v[104:107], a[192:195]\n" \
"buffer_load_dwordx4 v20, s[80:83], s84 offen sc1 lds\n" \
"v_mfma_f32_16x16x32_bf16 a[192:195], v[116:119], v[100:103], a[192:195]\n" \
"ds_read_b128 v[60:63], v28 offset:2048\n" \
"ds_read_b128 v[64:67], v29 offset:2048\n" \
"v_mfma_f32_16x16x32_bf16 a[224:227], v[116:119], v[108:111], a[224:227]\n" \
"L_pf4:\n" \
"s_cbranch_execz L_pf8\n" \
"s_nop 0\n" \
"s_add_u32 m0, s40, 0xb000\n" \
"s_add_u32 s85, s62, 0x0\n" \
"v_mfma_f32_16x16x32_bf16 a[224:227], v[120:123], v[112:115], a[224:227]\n" \
"buffer_load_dwordx4 v21, s[76:79], s85 offen lds\n" \
"v_mfma_f32_16x16x32_bf16 a[132:135], v[128:131], v[88:91], a[132:135]\n" \
"ds_read_b64_tr_b16 v[116:117], v36 offset:0\n" \
"ds_read_b64_tr_b16 v[118:119], v36 offset:512\n" \
"v_mfma_f32_16x16x32_bf16 a[132:135], v[124:127], v[84:87], a[132:135]\n" \
"ds_read_b64_tr_b16 v[120:121], v36 offset:4096\n" \
"ds_read_b64_tr_b16 v[122:123], v36 offset:4608\n" \
"v_mfma_f32_16x16x32_bf16 a[164:167], v[124:127], v[92:95], a[164:167]\n" \
"s_add_u32 m0, %[mA], 0x8000\n" \
"s_add_u32 s84, s62, 0x0\n" \
"v_mfma_f32_16x16x32_bf16 a[164:167], v[128:131], v[96:99], a[164:167]\n" \
"buffer_load_dwordx4 v24, s[76:79], s84 offen lds\n" \
"v_mfma_f32_16x16x32_bf16 a[196:199], v[128:131], v[104:107], a[196:199]\n" \
"ds_read_b128 v[68:71], v28 offset:4096\n" \
"ds_read_b128 v[72:75], v29 offset:4096\n" \
"v_mfma_f32_16x16x32_bf16 a[196:199], v[124:127], v[100:103], a[196:199]\n" \
"s_add_u32 m0, %[mA], 0x8400\n" \
"s_add_u32 s85, s62, 0x2400\n" \
"v_mfma_f32_16x16x32_bf16 a[228:231], v[124:127], v[108:111], a[228:231]\n" \
"buffer_load_dwordx4 v25, s[76:79], s85 offen lds\n" \
"v_mfma_f32_16x16x32_bf16 a[228:231], v[128:131], v[112:115], a[228:231]\n" \
"ds_read_b64_tr_b16 v[128:129], v37 offset:4096\n" \
"ds_read_b64_tr_b16 v[130:131], v37 offset:4608\n" \
"v_mfma_f32_16x16x32_bf16 a[136:139], v[136:139], v[88:91], a[136:139]\n" \
"ds_read_b64_tr_b16 v[124:125], v37 offset:0\n" \
"ds_read_b64_tr_b16 v[126:127], v37 offset:512\n" \
"v_mfma_f32_16x16x32_bf16 a[136:139], v[132:135], v[84:87], a[136:139]\n" \
"s_add_u32 m0, %[mB], 0x8000\n" \
"s_add_u32 s84, s63, 0x0\n" \
"s_nop 0\n" \
"v_mfma_f32_16x16x32_bf16 a[168:171], v[132:135], v[92:95], a[168:171]\n" \
"buffer_load_dwordx4 v26, s[68:71], s84 offen lds\n" \
"v_mfma_f32_16x16x32_bf16 a[168:171], v[136:139], v[96:99], a[168:171]\n" \
"ds_read_b128 v[76:79], v28 offset:6144\n" \
"ds_read_b128 v[80:83], v29 offset:6144\n" \
"v_mfma_f32_16x16x32_bf16 a[200:203], v[136:139], v[104:107], a[200:203]\n" \
"v_accvgpr_write_b32 a124, 0\n" \
"v_accvgpr_write_b32 a125, 0\n" \
"v_mfma_f32_16x16x32_bf16 a[200:203], v[132:135], v[100:103], a[200:203]\n" \
"s_add_u32 m0, %[mA], 0x8800\n" \
"s_add_u32 s85, s62, 0x4800\n" \
"v_mfma_f32_16x16x32_bf16 a[232:235], v[132:135], v[108:111], a[232:235]\n" \
"buffer_load_dwordx4 v24, s[76:79], s85 offen lds\n" \
"v_mfma_f32_16x16x32_bf16 a[232:235], v[136:139], v[112:115], a[232:235]\n" \
"ds_read_b64_tr_b16 v[132:133], v38 offset:0\n" \
"ds_read_b64_tr_b16 v[134:135], v38 offset:512\n" \
"v_mfma_f32_16x16x32_bf16 a[140:143], v[144:147], v[88:91], a[140:143]\n" \
"s_add_u32 m0, %[mA], 0x8c00\n" \
"s_add_u32 s84, s62, 0x6c00\n" \
"v_mfma_f32_16x16x32_bf16 a[140:143], v[140:143], v[84:87], a[140:143]\n" \
"buffer_load_dwordx4 v25, s[76:79], s84 offen lds\n" \
"v_mfma_f32_16x16x32_bf16 a[172:175], v[140:143], v[92:95], a[172:175]\n" \
"ds_read_b64_tr_b16 v[136:137], v38 offset:4096\n" \
"ds_read_b64_tr_b16 v[138:139], v38 offset:4608\n" \
"v_mfma_f32_16x16x32_bf16 a[172:175], v[144:147], v[96:99], a[172:175]\n" \
"v_accvgpr_write_b32 a126, 0\n" \
"v_accvgpr_write_b32 a127, 0\n" \
"v_mfma_f32_16x16x32_bf16 a[204:207], v[144:147], v[104:107], a[204:207]\n" \
"s_add_u32 m0, %[mB], 0x8400\n" \
"s_add_u32 s85, s63, 0x8000\n" \
"v_mfma_f32_16x16x32_bf16 a[204:207], v[140:143], v[100:103], a[204:207]\n" \
"buffer_load_dwordx4 v26, s[68:71], s85 offen lds\n" \
"v_mfma_f32_16x16x32_bf16 a[236:239], v[140:143], v[108:111], a[236:239]\n" \
"ds_read_b64_tr_b16 v[140:141], v39 offset:0\n" \
"ds_read_b64_tr_b16 v[142:143], v39 offset:960\n" \
"v_mfma_f32_16x16x32_bf16 a[236:239], v[144:147], v[112:115], a[236:239]\n" \
"v_accvgpr_read_b32 v216, a0\n" \
"v_accvgpr_read_b32 v217, a1\n" \
"v_mfma_f32_16x16x32_bf16 a[144:147], v[88:91], v[152:155], a[144:147]\n" \
"s_add_u32 m0, %[mA], 0x9000\n" \
"s_add_u32 s84, s62, 0x9000\n" \
"v_mfma_f32_16x16x32_bf16 a[144:147], v[84:87], v[148:151], a[144:147]\n" \
"buffer_load_dwordx4 v24, s[76:79], s84 offen lds\n" \
"v_mfma_f32_16x16x32_bf16 a[148:151], v[84:87], v[156:159], a[148:151]\n" \
"v_accvgpr_read_b32 v218, a2\n" \
"v_accvgpr_read_b32 v219, a3\n" \
"v_mfma_f32_16x16x32_bf16 a[148:151], v[88:91], v[160:163], a[148:151]\n" \
"s_add_u32 m0, %[mA], 0x9400\n" \
"s_add_u32 s85, s62, 0xb400\n" \
"v_mfma_f32_16x16x32_bf16 a[152:155], v[88:91], v[168:171], a[152:155]\n" \
"buffer_load_dwordx4 v25, s[76:79], s85 offen lds\n" \
"v_mfma_f32_16x16x32_bf16 a[152:155], v[84:87], v[164:167], a[152:155]\n" \
"v_cvt_pk_bf16_f32 v212, v212, v213\n" \
"v_cvt_pk_bf16_f32 v213, v214, v215\n" \
"v_mfma_f32_16x16x32_bf16 a[156:159], v[84:87], v[172:175], a[156:159]\n" \
"ds_read_b128 v[84:87], v22 offset:0\n" \
"v_cvt_pk_bf16_f32 v214, v180, v181\n" \
"v_mfma_f32_16x16x32_bf16 a[156:159], v[88:91], v[176:179], a[156:159]\n" \
"s_add_u32 m0, %[mB], 0x8800\n" \
"s_add_u32 s84, s63, 0x10000\n" \
"v_mfma_f32_16x16x32_bf16 a[176:179], v[96:99], v[152:155], a[176:179]\n" \
"buffer_load_dwordx4 v27, s[68:71], s84 offen lds\n" \
"v_mfma_f32_16x16x32_bf16 a[176:179], v[92:95], v[148:151], a[176:179]\n" \
"ds_read_b128 v[88:91], v23 offset:0\n" \
"v_cvt_pk_bf16_f32 v215, v182, v183\n" \
"v_mfma_f32_16x16x32_bf16 a[180:183], v[92:95], v[156:159], a[180:183]\n" \
"v_accvgpr_read_b32 v180, a4\n" \
"v_accvgpr_read_b32 v181, a5\n" \
"v_mfma_f32_16x16x32_bf16 a[180:183], v[96:99], v[160:163], a[180:183]\n" \
"s_add_u32 m0, %[mA], 0x9800\n" \
"s_add_u32 s85, s62, 0xd800\n" \
"v_mfma_f32_16x16x32_bf16 a[184:187], v[96:99], v[168:171], a[184:187]\n" \
"buffer_load_dwordx4 v24, s[76:79], s85 offen lds\n" \
"v_mfma_f32_16x16x32_bf16 a[184:187], v[92:95], v[164:167], a[184:187]\n" \
"v_accvgpr_read_b32 v182, a6\n" \
"v_accvgpr_read_b32 v183, a7\n" \
"v_mfma_f32_16x16x32_bf16 a[188:191], v[92:95], v[172:175], a[188:191]\n" \
"v_cvt_pk_bf16_f32 v216, v216, v217\n" \
"v_cvt_pk_bf16_f32 v217, v218, v219\n" \
"v_mfma_f32_16x16x32_bf16 a[188:191], v[96:99], v[176:179], a[188:191]\n" \
"v_accvgpr_read_b32 v220, a32\n" \
"v_accvgpr_read_b32 v221, a33\n" \
"v_mfma_f32_16x16x32_bf16 a[208:211], v[104:107], v[152:155], a[208:211]\n" \
"v_cvt_pk_bf16_f32 v218, v180, v181\n" \
"v_cvt_pk_bf16_f32 v219, v182, v183\n" \
"v_mfma_f32_16x16x32_bf16 a[208:211], v[100:103], v[148:151], a[208:211]\n" \
"v_accvgpr_read_b32 v222, a34\n" \
"v_accvgpr_read_b32 v223, a35\n" \
"v_mfma_f32_16x16x32_bf16 a[212:215], v[100:103], v[156:159], a[212:215]\n" \
"v_cvt_pk_bf16_f32 v220, v220, v221\n" \
"v_accvgpr_read_b32 v180, a36\n" \
"v_mfma_f32_16x16x32_bf16 a[212:215], v[104:107], v[160:163], a[212:215]\n" \
"v_accvgpr_read_b32 v181, a37\n" \
"v_accvgpr_read_b32 v182, a38\n" \
"v_mfma_f32_16x16x32_bf16 a[216:219], v[104:107], v[168:171], a[216:219]\n" \
"v_accvgpr_read_b32 v183, a39\n" \
"v_cvt_pk_bf16_f32 v221, v222, v223\n" \
"v_mfma_f32_16x16x32_bf16 a[216:219], v[100:103], v[164:167], a[216:219]\n" \
"v_accvgpr_read_b32 v224, a64\n" \
"v_accvgpr_read_b32 v225, a65\n" \
"v_mfma_f32_16x16x32_bf16 a[220:223], v[100:103], v[172:175], a[220:223]\n" \
"v_cvt_pk_bf16_f32 v222, v180, v181\n" \
"v_cvt_pk_bf16_f32 v223, v182, v183\n" \
"v_mfma_f32_16x16x32_bf16 a[220:223], v[104:107], v[176:179], a[220:223]\n" \
"v_accvgpr_read_b32 v226, a66\n" \
"v_accvgpr_read_b32 v227, a67\n" \
"v_mfma_f32_16x16x32_bf16 a[240:243], v[112:115], v[152:155], a[240:243]\n" \
"ds_read_b64_tr_b16 v[152:153], v32 offset:8192\n" \
"ds_read_b64_tr_b16 v[154:155], v32 offset:9216\n" \
"v_mfma_f32_16x16x32_bf16 a[240:243], v[108:111], v[148:151], a[240:243]\n" \
"ds_read_b64_tr_b16 v[148:149], v32 offset:0\n" \
"ds_read_b64_tr_b16 v[150:151], v32 offset:1024\n" \
"v_mfma_f32_16x16x32_bf16 a[244:247], v[108:111], v[156:159], a[244:247]\n" \
"ds_read_b64_tr_b16 v[156:157], v33 offset:0\n" \
"ds_read_b64_tr_b16 v[158:159], v33 offset:1024\n" \
"v_mfma_f32_16x16x32_bf16 a[244:247], v[112:115], v[160:163], a[244:247]\n" \
"ds_read_b64_tr_b16 v[160:161], v33 offset:8192\n" \
"ds_read_b64_tr_b16 v[162:163], v33 offset:9216\n" \
"v_mfma_f32_16x16x32_bf16 a[248:251], v[112:115], v[168:171], a[248:251]\n" \
"ds_read_b64_tr_b16 v[168:169], v34 offset:8192\n" \
"ds_read_b64_tr_b16 v[170:171], v34 offset:9216\n" \
"v_mfma_f32_16x16x32_bf16 a[248:251], v[108:111], v[164:167], a[248:251]\n" \
"ds_read_b64_tr_b16 v[164:165], v34 offset:0\n" \
"ds_read_b64_tr_b16 v[166:167], v34 offset:1024\n" \
"v_mfma_f32_16x16x32_bf16 a[252:255], v[108:111], v[172:175], a[252:255]\n" \
"ds_read_b64_tr_b16 v[172:173], v35 offset:0\n" \
"ds_read_b64_tr_b16 v[174:175], v35 offset:1024\n" \
"v_mfma_f32_16x16x32_bf16 a[252:255], v[112:115], v[176:179], a[252:255]\n" \
"s_add_u32 m0, %[mA], 0x9c00\n" \
"s_waitcnt lgkmcnt(14)\n" \
"s_nop 0\n" \
"v_smfmac_f32_16x16x64_bf16 a[16:19], v[140:143], v[84:91], v50\n" \
"v_mfma_f32_16x16x32_bf16 a[48:51], v[128:131], v[88:91], 0\n" \
"s_add_u32 s85, s62, 0xfc00\n" \
"ds_read_b64_tr_b16 v[176:177], v35 offset:8192\n" \
"v_mfma_f32_16x16x32_bf16 a[48:51], v[124:127], v[84:87], a[48:51]\n" \
"buffer_load_dwordx4 v25, s[76:79], s85 offen lds\n" \
"v_mfma_f32_16x16x32_bf16 a[80:83], v[84:87], v[132:135], 0\n" \
"s_add_u32 m0, %[mB], 0x8c00\n" \
"s_add_u32 s84, s63, 0x18000\n" \
"v_mfma_f32_16x16x32_bf16 a[80:83], v[88:91], v[136:139], a[80:83]\n" \
"ds_read_b64_tr_b16 v[178:179], v35 offset:9216\n" \
"v_cvt_pk_bf16_f32 v224, v224, v225\n" \
"v_smfmac_f32_16x16x64_bf16 a[112:115], v[140:143], v[52:59], v50\n" \
"v_mfma_f32_16x16x32_bf16 a[20:23], v[128:131], v[56:59], 0\n" \
"buffer_load_dwordx4 v27, s[68:71], s84 offen lds\n" \
"v_mfma_f32_16x16x32_bf16 a[20:23], v[124:127], v[52:55], a[20:23]\n" \
"s_add_u32 s88, s44, 0x0\n" \
"s_nop 0\n" \
"v_accvgpr_read_b32 v180, a68\n" \
"v_mfma_f32_16x16x32_bf16 a[52:55], v[116:119], v[52:55], 0\n" \
"v_accvgpr_read_b32 v181, a69\n" \
"v_accvgpr_read_b32 v182, a70\n" \
"v_mfma_f32_16x16x32_bf16 a[52:55], v[120:123], v[56:59], a[52:55]\n" \
"buffer_store_dwordx4 v[184:187], v20, s[80:83], s88 offen offset:0 sc1\n" \
"v_mfma_f32_16x16x32_bf16 a[84:87], v[56:59], v[136:139], 0\n" \
"v_accvgpr_read_b32 v183, a71\n" \
"v_cvt_pk_bf16_f32 v225, v226, v227\n" \
"v_mfma_f32_16x16x32_bf16 a[84:87], v[52:55], v[132:135], a[84:87]\n" \
"v_accvgpr_read_b32 v228, a96\n" \
"v_accvgpr_read_b32 v229, a97\n" \
"v_smfmac_f32_16x16x64_bf16 a[116:119], v[140:143], v[60:67], v50\n" \
"v_mfma_f32_16x16x32_bf16 a[24:27], v[124:127], v[60:63], 0\n" \
"v_cvt_pk_bf16_f32 v226, v180, v181\n" \
"v_cvt_pk_bf16_f32 v227, v182, v183\n" \
"v_mfma_f32_16x16x32_bf16 a[24:27], v[128:131], v[64:67], a[24:27]\n" \
"s_add_u32 s89, s44, 0x1000\n" \
"v_accvgpr_read_b32 v230, a98\n" \
"v_mfma_f32_16x16x32_bf16 a[56:59], v[120:123], v[64:67], 0\n" \
"buffer_store_dwordx4 v[188:191], v20, s[80:83], s89 offen offset:0 sc1\n" \
"v_mfma_f32_16x16x32_bf16 a[56:59], v[116:119], v[60:63], a[56:59]\n" \
"v_accvgpr_read_b32 v231, a99\n" \
"v_cvt_pk_bf16_f32 v228, v228, v229\n" \
"v_mfma_f32_16x16x32_bf16 a[88:91], v[60:63], v[132:135], 0\n" \
"v_accvgpr_read_b32 v180, a100\n" \
"v_accvgpr_read_b32 v181, a101\n" \
"v_mfma_f32_16x16x32_bf16 a[88:91], v[64:67], v[136:139], a[88:91]\n" \
"v_accvgpr_read_b32 v182, a102\n" \
"v_accvgpr_read_b32 v183, a103\n" \
"v_smfmac_f32_16x16x64_bf16 a[120:123], v[140:143], v[68:75], v50\n" \
"v_mfma_f32_16x16x32_bf16 a[28:31], v[128:131], v[72:75], 0\n" \
"v_cvt_pk_bf16_f32 v229, v230, v231\n" \
"v_accvgpr_read_b32 v232, a8\n" \
"v_mfma_f32_16x16x32_bf16 a[28:31], v[124:127], v[68:71], a[28:31]\n" \
"v_accvgpr_read_b32 v233, a9\n" \
"v_cvt_pk_bf16_f32 v230, v180, v181\n" \
"v_mfma_f32_16x16x32_bf16 a[60:63], v[116:119], v[68:71], 0\n" \
"s_add_u32 s88, s44, 0x0\n" \
"s_nop 0\n" \
"v_cvt_pk_bf16_f32 v231, v182, v183\n" \
"v_mfma_f32_16x16x32_bf16 a[60:63], v[120:123], v[72:75], a[60:63]\n" \
"buffer_store_dwordx4 v[192:195], v20, s[80:83], s88 offen offset:1024 sc1\n" \
"v_mfma_f32_16x16x32_bf16 a[92:95], v[72:75], v[136:139], 0\n" \
"v_accvgpr_read_b32 v234, a10\n" \
"v_accvgpr_read_b32 v235, a11\n" \
"v_mfma_f32_16x16x32_bf16 a[92:95], v[68:71], v[132:135], a[92:95]\n" \
"v_cvt_pk_bf16_f32 v232, v232, v233\n" \
"v_accvgpr_read_b32 v180, a12\n" \
"v_smfmac_f32_16x16x64_bf16 a[124:127], v[140:143], v[76:83], v50\n" \
"v_mfma_f32_16x16x32_bf16 a[0:3], v[124:127], v[76:79], 0\n" \
"v_accvgpr_read_b32 v181, a13\n" \
"v_accvgpr_read_b32 v182, a14\n" \
"v_mfma_f32_16x16x32_bf16 a[0:3], v[128:131], v[80:83], a[0:3]\n" \
"v_accvgpr_read_b32 v183, a15\n" \
"v_cvt_pk_bf16_f32 v233, v234, v235\n" \
"v_mfma_f32_16x16x32_bf16 a[4:7], v[120:123], v[80:83], 0\n" \
"v_accvgpr_read_b32 v236, a40\n" \
"v_accvgpr_read_b32 v237, a41\n" \
"v_mfma_f32_16x16x32_bf16 a[4:7], v[116:119], v[76:79], a[4:7]\n" \
"v_cvt_pk_bf16_f32 v234, v180, v181\n" \
"v_cvt_pk_bf16_f32 v235, v182, v183\n" \
"v_mfma_f32_16x16x32_bf16 a[32:35], v[76:79], v[132:135], 0\n" \
"v_accvgpr_read_b32 v238, a42\n" \
"v_accvgpr_read_b32 v239, a43\n" \
"v_mfma_f32_16x16x32_bf16 a[32:35], v[80:83], v[136:139], a[32:35]\n" \
"v_cvt_pk_bf16_f32 v236, v236, v237\n" \
"v_accvgpr_read_b32 v180, a44\n" \
"v_accvgpr_read_b32 v181, a45\n" \
"v_accvgpr_read_b32 v182, a46\n" \
"v_accvgpr_read_b32 v183, a47\n" \
"v_cvt_pk_bf16_f32 v237, v238, v239\n" \
"v_accvgpr_read_b32 v240, a72\n" \
"v_accvgpr_read_b32 v241, a73\n" \
"v_cvt_pk_bf16_f32 v238, v180, v181\n" \
"v_cvt_pk_bf16_f32 v239, v182, v183\n" \
"v_accvgpr_read_b32 v242, a74\n" \
"v_accvgpr_read_b32 v243, a75\n" \
"v_cvt_pk_bf16_f32 v240, v240, v241\n" \
"v_accvgpr_read_b32 v180, a76\n" \
"v_accvgpr_read_b32 v181, a77\n" \
"v_accvgpr_read_b32 v182, a78\n" \
"v_accvgpr_read_b32 v183, a79\n" \
"v_cvt_pk_bf16_f32 v241, v242, v243\n" \
"v_accvgpr_read_b32 v244, a104\n" \
"v_accvgpr_read_b32 v245, a105\n" \
"v_cvt_pk_bf16_f32 v242, v180, v181\n" \
"v_cvt_pk_bf16_f32 v243, v182, v183\n" \
"v_accvgpr_read_b32 v246, a106\n" \
"v_accvgpr_read_b32 v247, a107\n" \
"v_cvt_pk_bf16_f32 v244, v244, v245\n" \
"v_accvgpr_read_b32 v248, a128\n" \
"v_accvgpr_read_b32 v249, a129\n" \
"v_accvgpr_read_b32 v250, a130\n" \
"v_accvgpr_read_b32 v251, a131\n" \
"s_waitcnt lgkmcnt(0)\n" \
"s_nop 0\n" \
"v_mfma_f32_16x16x32_bf16 a[128:131], v[152:155], v[56:59], 0\n" \
"v_accvgpr_read_b32 v180, a108\n" \
"v_accvgpr_read_b32 v181, a109\n" \
"v_accvgpr_read_b32 v182, a110\n" \
"v_accvgpr_read_b32 v183, a111\n" \
"v_cvt_pk_bf16_f32 v245, v246, v247\n" \
"v_cvt_pk_bf16_f32 v246, v180, v181\n" \
"v_cvt_pk_bf16_f32 v247, v182, v183\n" \
"v_cvt_pk_bf16_f32 v248, v248, v249\n" \
"v_mfma_f32_16x16x32_bf16 a[128:131], v[148:151], v[52:55], a[128:131]\n" \
"v_accvgpr_read_b32 v180, a132\n" \
"v_accvgpr_read_b32 v181, a133\n" \
"v_accvgpr_read_b32 v182, a134\n" \
"v_accvgpr_read_b32 v183, a135\n" \
"v_cvt_pk_bf16_f32 v249, v250, v251\n" \
"v_accvgpr_read_b32 v252, a160\n" \
"v_accvgpr_read_b32 v253, a161\n" \
"v_cvt_pk_bf16_f32 v250, v180, v181\n" \
"v_cvt_pk_bf16_f32 v251, v182, v183\n" \
"v_accvgpr_read_b32 v254, a162\n" \
"v_accvgpr_read_b32 v255, a163\n" \
"v_cvt_pk_bf16_f32 v252, v252, v253\n" \
"v_accvgpr_read_b32 v180, a164\n" \
"v_accvgpr_read_b32 v181, a165\n" \
"v_accvgpr_read_b32 v182, a166\n" \
"v_accvgpr_read_b32 v183, a167\n" \
"v_cvt_pk_bf16_f32 v253, v254, v255\n" \
"v_cvt_pk_bf16_f32 v254, v180, v181\n" \
"v_cvt_pk_bf16_f32 v255, v182, v183\n" \
"v_accvgpr_write_b32 a36, v252\n" \
"v_accvgpr_write_b32 a37, v253\n" \
"v_mfma_f32_16x16x32_bf16 a[132:135], v[156:159], v[52:55], 0\n" \
"v_accvgpr_write_b32 a38, v254\n" \
"v_accvgpr_write_b32 a39, v255\n" \
"v_accvgpr_read_b32 v252, a192\n" \
"v_accvgpr_read_b32 v253, a193\n" \
"v_accvgpr_read_b32 v254, a194\n" \
"v_accvgpr_read_b32 v255, a195\n" \
"v_cvt_pk_bf16_f32 v252, v252, v253\n" \
"v_accvgpr_read_b32 v180, a196\n" \
"v_accvgpr_read_b32 v181, a197\n" \
"v_accvgpr_read_b32 v182, a198\n" \
"v_accvgpr_read_b32 v183, a199\n" \
"v_cvt_pk_bf16_f32 v253, v254, v255\n" \
"v_cvt_pk_bf16_f32 v254, v180, v181\n" \
"v_cvt_pk_bf16_f32 v255, v182, v183\n" \
"v_accvgpr_write_b32 a64, v252\n" \
"v_accvgpr_write_b32 a65, v253\n" \
"v_mfma_f32_16x16x32_bf16 a[132:135], v[160:163], v[56:59], a[132:135]\n" \
"v_accvgpr_write_b32 a66, v254\n" \
"v_accvgpr_write_b32 a67, v255\n" \
"v_accvgpr_read_b32 v252, a224\n" \
"v_accvgpr_read_b32 v253, a225\n" \
"v_accvgpr_read_b32 v254, a226\n" \
"v_accvgpr_read_b32 v255, a227\n" \
"v_cvt_pk_bf16_f32 v252, v252, v253\n" \
"v_accvgpr_read_b32 v180, a228\n" \
"v_accvgpr_read_b32 v181, a229\n" \
"v_accvgpr_read_b32 v182, a230\n" \
"v_accvgpr_read_b32 v183, a231\n" \
"v_cvt_pk_bf16_f32 v253, v254, v255\n" \
"v_cvt_pk_bf16_f32 v254, v180, v181\n" \
"v_cvt_pk_bf16_f32 v255, v182, v183\n" \
"v_accvgpr_write_b32 a68, v252\n" \
"v_accvgpr_write_b32 a69, v253\n" \
"v_accvgpr_write_b32 a70, v254\n" \
"v_accvgpr_write_b32 a71, v255\n" \
"v_accvgpr_read_b32 v252, a136\n" \
"v_accvgpr_read_b32 v253, a137\n" \
"v_accvgpr_read_b32 v254, a138\n" \
"v_accvgpr_read_b32 v255, a139\n" \
"v_mfma_f32_16x16x32_bf16 a[136:139], v[168:171], v[56:59], 0\n" \
"ds_read_b128 v[84:87], v48 offset:0\n" \
"ds_read_b128 v[88:91], v49 offset:0\n" \
"ds_read_b128 v[92:95], v48 offset:2048\n" \
"v_mfma_f32_16x16x32_bf16 a[136:139], v[164:167], v[52:55], a[136:139]\n" \
"ds_read_b128 v[96:99], v49 offset:2048\n" \
"v_accvgpr_read_b32 v180, a140\n" \
"v_accvgpr_read_b32 v181, a141\n" \
"v_accvgpr_read_b32 v182, a142\n" \
"v_accvgpr_read_b32 v183, a143\n" \
"v_mfma_f32_16x16x32_bf16 a[140:143], v[172:175], v[52:55], 0\n" \
"ds_read_b128 v[100:103], v48 offset:4096\n" \
"ds_read_b128 v[104:107], v49 offset:4096\n" \
"s_add_u32 s89, s44, 0x1000\n" \
"buffer_store_dwordx4 v[196:199], v20, s[80:83], s89 offen offset:1024 sc1\n" \
"ds_read_b128 v[108:111], v48 offset:6144\n" \
"v_mfma_f32_16x16x32_bf16 a[140:143], v[176:179], v[56:59], a[140:143]\n" \
"v_cvt_pk_bf16_f32 v252, v252, v253\n" \
"v_cvt_pk_bf16_f32 v253, v254, v255\n" \
"v_mfma_f32_16x16x32_bf16 a[160:163], v[152:155], v[64:67], 0\n" \
"ds_read_b128 v[112:115], v49 offset:6144\n" \
"v_cvt_pk_bf16_f32 v254, v180, v181\n" \
"v_mfma_f32_16x16x32_bf16 a[160:163], v[148:151], v[60:63], a[160:163]\n" \
"v_cvt_pk_bf16_f32 v255, v182, v183\n" \
"v_accvgpr_write_b32 a96, v252\n" \
"v_mfma_f32_16x16x32_bf16 a[164:167], v[156:159], v[60:63], 0\n" \
"v_accvgpr_write_b32 a97, v253\n" \
"v_accvgpr_write_b32 a98, v254\n" \
"v_mfma_f32_16x16x32_bf16 a[164:167], v[160:163], v[64:67], a[164:167]\n" \
"v_accvgpr_write_b32 a99, v255\n" \
"v_accvgpr_read_b32 v252, a168\n" \
"v_accvgpr_read_b32 v253, a169\n" \
"v_accvgpr_read_b32 v254, a170\n" \
"v_accvgpr_read_b32 v255, a171\n" \
"v_mfma_f32_16x16x32_bf16 a[168:171], v[168:171], v[64:67], 0\n" \
"v_accvgpr_read_b32 v180, a172\n" \
"v_accvgpr_read_b32 v181, a173\n" \
"v_mfma_f32_16x16x32_bf16 a[168:171], v[164:167], v[60:63], a[168:171]\n" \
"v_accvgpr_read_b32 v182, a174\n" \
"v_accvgpr_read_b32 v183, a175\n" \
"v_mfma_f32_16x16x32_bf16 a[172:175], v[172:175], v[60:63], 0\n" \
"v_cvt_pk_bf16_f32 v252, v252, v253\n" \
"v_cvt_pk_bf16_f32 v253, v254, v255\n" \
"v_mfma_f32_16x16x32_bf16 a[172:175], v[176:179], v[64:67], a[172:175]\n" \
"v_cvt_pk_bf16_f32 v254, v180, v181\n" \
"v_cvt_pk_bf16_f32 v255, v182, v183\n" \
"v_mfma_f32_16x16x32_bf16 a[192:195], v[152:155], v[72:75], 0\n" \
"v_accvgpr_write_b32 a100, v252\n" \
"v_accvgpr_write_b32 a101, v253\n" \
"v_mfma_f32_16x16x32_bf16 a[192:195], v[148:151], v[68:71], a[192:195]\n" \
"v_accvgpr_write_b32 a102, v254\n" \
"v_accvgpr_write_b32 a103, v255\n" \
"s_add_u32 s62, s62, 0x80\n" \
"v_accvgpr_read_b32 v252, a200\n" \
"v_accvgpr_read_b32 v253, a201\n" \
"v_accvgpr_read_b32 v254, a202\n" \
"s_waitcnt vmcnt(4) lgkmcnt(0)\n" \
"s_barrier\n" \
"v_mfma_f32_16x16x32_bf16 a[196:199], v[156:159], v[68:71], 0\n" \
"v_accvgpr_read_b32 v255, a203\n" \
"s_add_u32 m0, s40, 0x0\n" \
"v_mfma_f32_16x16x32_bf16 a[196:199], v[160:163], v[72:75], a[196:199]\n" \
"s_cmp_lt_u32 s62, 0x480\n" \
"s_cselect_b32 s62, s62, 0\n" \
"v_mfma_f32_16x16x32_bf16 a[200:203], v[168:171], v[72:75], 0\n" \
"s_lshl_b32 s72, s62, 6\n" \
"s_add_u32 s72, s72, s37\n" \
"v_mfma_f32_16x16x32_bf16 a[200:203], v[164:167], v[68:71], a[200:203]\n" \
"s_add_u32 s85, s72, 0x0\n" \
"s_nop 0\n" \
"buffer_load_dwordx4 v20, s[80:83], s85 offen sc1 lds\n" \
"v_accvgpr_read_b32 v180, a204\n" \
"v_accvgpr_read_b32 v181, a205\n" \
"v_accvgpr_read_b32 v182, a206\n" \
"v_accvgpr_read_b32 v183, a207\n" \
"v_mfma_f32_16x16x32_bf16 a[204:207], v[172:175], v[68:71], 0\n" \
"s_add_u32 m0, s40, 0x1000\n" \
"s_add_u32 s84, s72, 0x1000\n" \
"v_mfma_f32_16x16x32_bf16 a[204:207], v[176:179], v[72:75], a[204:207]\n" \
"buffer_load_dwordx4 v20, s[80:83], s84 offen sc1 lds\n" \
"v_mfma_f32_16x16x32_bf16 a[224:227], v[152:155], v[80:83], 0\n" \
"v_cvt_pk_bf16_f32 v252, v252, v253\n" \
"v_cvt_pk_bf16_f32 v253, v254, v255\n" \
"v_mfma_f32_16x16x32_bf16 a[224:227], v[148:151], v[76:79], a[224:227]\n" \
"v_cvt_pk_bf16_f32 v254, v180, v181\n" \
"v_cvt_pk_bf16_f32 v255, v182, v183\n" \
"v_mfma_f32_16x16x32_bf16 a[228:231], v[156:159], v[76:79], 0\n" \
"v_accvgpr_write_b32 a8, v252\n" \
"v_accvgpr_write_b32 a9, v253\n" \
"v_mfma_f32_16x16x32_bf16 a[228:231], v[160:163], v[80:83], a[228:231]\n" \
"v_accvgpr_write_b32 a10, v254\n" \
"v_accvgpr_write_b32 a11, v255\n" \
"v_accvgpr_read_b32 v252, a232\n" \
"v_accvgpr_read_b32 v253, a233\n" \
"v_accvgpr_read_b32 v254, a234\n" \
"v_accvgpr_read_b32 v255, a235\n" \
"v_mfma_f32_16x16x32_bf16 a[232:235], v[168:171], v[80:83], 0\n" \
"s_add_u32 m0, s40, 0x3000\n" \
"s_add_u32 s85, s62, 0x0\n" \
"buffer_load_dwordx4 v21, s[76:79], s85 offen lds\n" \
"v_mfma_f32_16x16x32_bf16 a[232:235], v[164:167], v[76:79], a[232:235]\n" \
"v_accvgpr_read_b32 v180, a236\n" \
"v_accvgpr_read_b32 v181, a237\n" \
"v_accvgpr_read_b32 v182, a238\n" \
"v_accvgpr_read_b32 v183, a239\n" \
"v_mfma_f32_16x16x32_bf16 a[236:239], v[172:175], v[76:79], 0\n" \
"v_cvt_pk_bf16_f32 v252, v252, v253\n" \
"s_add_u32 m0, %[mA], 0x0\n" \
"v_mfma_f32_16x16x32_bf16 a[236:239], v[176:179], v[80:83], a[236:239]\n" \
"s_add_u32 s84, s62, 0x0\n" \
"s_nop 0\n" \
"v_cvt_pk_bf16_f32 v253, v254, v255\n" \
"v_cvt_pk_bf16_f32 v254, v180, v181\n" \
"v_cvt_pk_bf16_f32 v255, v182, v183\n" \
"v_accvgpr_write_b32 a12, v252\n" \
"v_accvgpr_write_b32 a13, v253\n" \
"v_accvgpr_write_b32 a14, v254\n" \
"v_accvgpr_write_b32 a15, v255\n" \
"v_accvgpr_read_b32 v252, a144\n" \
"v_accvgpr_read_b32 v253, a145\n" \
"v_accvgpr_read_b32 v254, a146\n" \
"v_accvgpr_read_b32 v255, a147\n" \
"v_mfma_f32_16x16x32_bf16 a[144:147], v[152:155], v[88:91], 0\n" \
"buffer_load_dwordx4 v24, s[76:79], s84 offen lds\n" \
"v_mfma_f32_16x16x32_bf16 a[144:147], v[148:151], v[84:87], a[144:147]\n" \
"v_cvt_pk_bf16_f32 v252, v252, v253\n" \
"v_accvgpr_read_b32 v180, a176\n" \
"v_accvgpr_read_b32 v181, a177\n" \
"v_accvgpr_read_b32 v182, a178\n" \
"v_accvgpr_read_b32 v183, a179\n" \
"v_cvt_pk_bf16_f32 v253, v254, v255\n" \
"v_cvt_pk_bf16_f32 v254, v180, v181\n" \
"v_cvt_pk_bf16_f32 v255, v182, v183\n" \
"v_accvgpr_write_b32 a40, v252\n" \
"v_accvgpr_write_b32 a41, v253\n" \
"v_accvgpr_write_b32 a42, v254\n" \
"v_accvgpr_write_b32 a43, v255\n" \
"v_accvgpr_read_b32 v252, a148\n" \
"v_accvgpr_read_b32 v253, a149\n" \
"v_accvgpr_read_b32 v254, a150\n" \
"v_accvgpr_read_b32 v255, a151\n" \
"v_mfma_f32_16x16x32_bf16 a[148:151], v[156:159], v[84:87], 0\n" \
"v_cvt_pk_bf16_f32 v252, v252, v253\n" \
"s_add_u32 m0, %[mA], 0x400\n" \
"v_mfma_f32_16x16x32_bf16 a[148:151], v[160:163], v[88:91], a[148:151]\n" \
"s_add_u32 s85, s62, 0x2400\n" \
"v_accvgpr_read_b32 v180, a180\n" \
"v_accvgpr_read_b32 v181, a181\n" \
"v_accvgpr_read_b32 v182, a182\n" \
"v_accvgpr_read_b32 v183, a183\n" \
"v_cvt_pk_bf16_f32 v253, v254, v255\n" \
"v_cvt_pk_bf16_f32 v254, v180, v181\n" \
"v_cvt_pk_bf16_f32 v255, v182, v183\n" \
"v_accvgpr_write_b32 a44, v252\n" \
"v_accvgpr_write_b32 a45, v253\n" \
"v_accvgpr_write_b32 a46, v254\n" \
"v_accvgpr_write_b32 a47, v255\n" \
"v_accvgpr_read_b32 v252, a152\n" \
"v_accvgpr_read_b32 v253, a153\n" \
"v_accvgpr_read_b32 v254, a154\n" \
"v_accvgpr_read_b32 v255, a155\n" \
"v_mfma_f32_16x16x32_bf16 a[152:155], v[168:171], v[88:91], 0\n" \
"buffer_load_dwordx4 v25, s[76:79], s85 offen lds\n" \
"v_mfma_f32_16x16x32_bf16 a[152:155], v[164:167], v[84:87], a[152:155]\n" \
"v_cvt_pk_bf16_f32 v252, v252, v253\n" \
"v_accvgpr_read_b32 v180, a184\n" \
"v_accvgpr_read_b32 v181, a185\n" \
"v_accvgpr_read_b32 v182, a186\n" \
"v_accvgpr_read_b32 v183, a187\n" \
"v_cvt_pk_bf16_f32 v253, v254, v255\n" \
"v_cvt_pk_bf16_f32 v254, v180, v181\n" \
"v_cvt_pk_bf16_f32 v255, v182, v183\n" \
"s_lshl_b32 s63, s62, 12\n" \
"v_accvgpr_write_b32 a72, v252\n" \
"L_pf5:\n" \
"s_cbranch_execz L_pf9\n" \
"s_nop 0\n" \
"v_accvgpr_write_b32 a73, v253\n" \
"v_accvgpr_write_b32 a74, v254\n" \
"v_accvgpr_write_b32 a75, v255\n" \
"v_accvgpr_read_b32 v252, a156\n" \
"v_accvgpr_read_b32 v253, a157\n" \
"v_accvgpr_read_b32 v254, a158\n" \
"v_accvgpr_read_b32 v255, a159\n" \
"v_mfma_f32_16x16x32_bf16 a[156:159], v[172:175], v[84:87], 0\n" \
"s_add_u32 m0, %[mB], 0x0\n" \
"s_add_u32 s84, s63, 0x0\n" \
"s_nop 0\n" \
"buffer_load_dwordx4 v26, s[68:71], s84 offen lds\n" \
"v_mfma_f32_16x16x32_bf16 a[156:159], v[176:179], v[88:91], a[156:159]\n" \
"s_add_u32 m0, %[mA], 0x800\n" \
"s_add_u32 s85, s62, 0x4800\n" \
"v_mfma_f32_16x16x32_bf16 a[176:179], v[152:155], v[96:99], 0\n" \
"v_accvgpr_read_b32 v180, a188\n" \
"v_accvgpr_read_b32 v181, a189\n" \
"v_mfma_f32_16x16x32_bf16 a[176:179], v[148:151], v[92:95], a[176:179]\n" \
"buffer_load_dwordx4 v24, s[76:79], s85 offen lds\n" \
"v_mfma_f32_16x16x32_bf16 a[180:183], v[156:159], v[92:95], 0\n" \
"s_add_u32 m0, %[mA], 0xc00\n" \
"s_add_u32 s84, s62, 0x6c00\n" \
"v_mfma_f32_16x16x32_bf16 a[180:183], v[160:163], v[96:99], a[180:183]\n" \
"v_accvgpr_read_b32 v182, a190\n" \
"v_accvgpr_read_b32 v183, a191\n" \
"v_mfma_f32_16x16x32_bf16 a[184:187], v[168:171], v[96:99], 0\n" \
"buffer_load_dwordx4 v25, s[76:79], s84 offen lds\n" \
"v_mfma_f32_16x16x32_bf16 a[184:187], v[164:167], v[92:95], a[184:187]\n" \
"v_cvt_pk_bf16_f32 v252, v252, v253\n" \
"v_cvt_pk_bf16_f32 v253, v254, v255\n" \
"v_mfma_f32_16x16x32_bf16 a[188:191], v[172:175], v[92:95], 0\n" \
"v_cvt_pk_bf16_f32 v254, v180, v181\n" \
"v_cvt_pk_bf16_f32 v255, v182, v183\n" \
"v_mfma_f32_16x16x32_bf16 a[188:191], v[176:179], v[96:99], a[188:191]\n" \
"v_accvgpr_write_b32 a76, v252\n" \
"v_accvgpr_write_b32 a77, v253\n" \
"v_accvgpr_write_b32 a78, v254\n" \
"v_accvgpr_write_b32 a79, v255\n" \
"v_accvgpr_read_b32 v252, a208\n" \
"v_accvgpr_read_b32 v253, a209\n" \
"v_accvgpr_read_b32 v254, a210\n" \
"v_accvgpr_read_b32 v255, a211\n" \
"v_mfma_f32_16x16x32_bf16 a[208:211], v[152:155], v[104:107], 0\n" \
"v_cvt_pk_bf16_f32 v252, v252, v253\n" \
"s_add_u32 m0, %[mB], 0x400\n" \
"v_mfma_f32_16x16x32_bf16 a[208:211], v[148:151], v[100:103], a[208:211]\n" \
"s_add_u32 s85, s63, 0x8000\n" \
"v_accvgpr_read_b32 v180, a240\n" \
"v_accvgpr_read_b32 v181, a241\n" \
"v_accvgpr_read_b32 v182, a242\n" \
"v_accvgpr_read_b32 v183, a243\n" \
"v_cvt_pk_bf16_f32 v253, v254, v255\n" \
"v_cvt_pk_bf16_f32 v254, v180, v181\n" \
"v_cvt_pk_bf16_f32 v255, v182, v183\n" \
"v_accvgpr_write_b32 a104, v252\n" \
"v_accvgpr_write_b32 a105, v253\n" \
"v_accvgpr_write_b32 a106, v254\n" \
"v_accvgpr_write_b32 a107, v255\n" \
"v_accvgpr_read_b32 v252, a212\n" \
"v_accvgpr_read_b32 v253, a213\n" \
"v_accvgpr_read_b32 v254, a214\n" \
"v_accvgpr_read_b32 v255, a215\n" \
"v_mfma_f32_16x16x32_bf16 a[212:215], v[156:159], v[100:103], 0\n" \
"buffer_load_dwordx4 v26, s[68:71], s85 offen lds\n" \
"ds_read_b64_tr_b16 v[140:141], v39 offset:32768\n" \
"ds_read_b64_tr_b16 v[142:143], v39 offset:33728\n" \
"ds_read_b128 v[84:87], v22 offset:32768\n" \
"ds_read_b128 v[88:91], v23 offset:32768\n" \
"v_mfma_f32_16x16x32_bf16 a[212:215], v[160:163], v[104:107], a[212:215]\n" \
"ds_read_b64_tr_b16 v[128:129], v37 offset:36864\n" \
"ds_read_b64_tr_b16 v[130:131], v37 offset:37376\n" \
"v_accvgpr_read_b32 v184, a216\n" \
"v_accvgpr_read_b32 v185, a217\n" \
"v_accvgpr_read_b32 v186, a218\n" \
"v_accvgpr_read_b32 v187, a219\n" \
"v_mfma_f32_16x16x32_bf16 a[216:219], v[168:171], v[104:107], 0\n" \
"s_add_u32 m0, %[mA], 0x1000\n" \
"s_add_u32 s84, s62, 0x9000\n" \
"buffer_load_dwordx4 v24, s[76:79], s84 offen lds\n" \
"ds_read_b64_tr_b16 v[124:125], v37 offset:32768\n" \
"ds_read_b64_tr_b16 v[126:127], v37 offset:33280\n" \
"v_mfma_f32_16x16x32_bf16 a[216:219], v[164:167], v[100:103], a[216:219]\n" \
"v_cvt_pk_bf16_f32 v252, v252, v253\n" \
"v_accvgpr_read_b32 v180, a244\n" \
"v_accvgpr_read_b32 v181, a245\n" \
"v_accvgpr_read_b32 v182, a246\n" \
"v_accvgpr_read_b32 v183, a247\n" \
"v_cvt_pk_bf16_f32 v253, v254, v255\n" \
"v_cvt_pk_bf16_f32 v254, v180, v181\n" \
"v_cvt_pk_bf16_f32 v255, v182, v183\n" \
"v_accvgpr_write_b32 a108, v252\n" \
"v_accvgpr_write_b32 a109, v253\n" \
"v_accvgpr_write_b32 a110, v254\n" \
"v_accvgpr_write_b32 a111, v255\n" \
"v_accvgpr_read_b32 v252, a220\n" \
"v_accvgpr_read_b32 v253, a221\n" \
"v_accvgpr_read_b32 v254, a222\n" \
"v_accvgpr_read_b32 v255, a223\n" \
"v_mfma_f32_16x16x32_bf16 a[220:223], v[172:175], v[100:103], 0\n" \
"ds_read_b64_tr_b16 v[132:133], v38 offset:32768\n" \
"ds_read_b64_tr_b16 v[134:135], v38 offset:33280\n" \
"ds_read_b64_tr_b16 v[136:137], v38 offset:36864\n" \
"ds_read_b64_tr_b16 v[138:139], v38 offset:37376\n" \
"v_mfma_f32_16x16x32_bf16 a[220:223], v[176:179], v[104:107], a[220:223]\n" \
"ds_read_b128 v[52:55], v28 offset:32768\n" \
"ds_read_b128 v[56:59], v29 offset:32768\n" \
"v_mfma_f32_16x16x32_bf16 a[240:243], v[152:155], v[112:115], 0\n" \
"s_add_u32 m0, %[mA], 0x1400\n" \
"s_add_u32 s85, s62, 0xb400\n" \
"buffer_load_dwordx4 v25, s[76:79], s85 offen lds\n" \
"v_mfma_f32_16x16x32_bf16 a[240:243], v[148:151], v[108:111], a[240:243]\n" \
"ds_read_b64_tr_b16 v[116:117], v36 offset:32768\n" \
"ds_read_b64_tr_b16 v[118:119], v36 offset:33280\n" \
"v_mfma_f32_16x16x32_bf16 a[244:247], v[156:159], v[108:111], 0\n" \
"s_add_u32 m0, %[mB], 0x800\n" \
"s_add_u32 s84, s63, 0x10000\n" \
"v_mfma_f32_16x16x32_bf16 a[244:247], v[160:163], v[112:115], a[244:247]\n" \
"buffer_load_dwordx4 v27, s[68:71], s84 offen lds\n" \
"ds_read_b64_tr_b16 v[120:121], v36 offset:36864\n" \
"ds_read_b64_tr_b16 v[122:123], v36 offset:37376\n" \
"v_accvgpr_read_b32 v180, a248\n" \
"v_accvgpr_read_b32 v181, a249\n" \
"v_accvgpr_read_b32 v182, a250\n" \
"v_accvgpr_read_b32 v183, a251\n" \
"v_mfma_f32_16x16x32_bf16 a[248:251], v[168:171], v[112:115], 0\n" \
"ds_read_b128 v[60:63], v28 offset:34816\n" \
"v_cvt_pk_bf16_f32 v184, v184, v185\n" \
"v_mfma_f32_16x16x32_bf16 a[248:251], v[164:167], v[108:111], a[248:251]\n" \
"v_cvt_pk_bf16_f32 v185, v186, v187\n" \
"v_cvt_pk_bf16_f32 v186, v180, v181\n" \
"v_cvt_pk_bf16_f32 v187, v182, v183\n" \
"v_accvgpr_read_b32 v180, a252\n" \
"v_accvgpr_read_b32 v181, a253\n" \
"v_accvgpr_read_b32 v182, a254\n" \
"v_accvgpr_read_b32 v183, a255\n" \
"v_mfma_f32_16x16x32_bf16 a[252:255], v[172:175], v[108:111], 0\n" \
"ds_read_b128 v[64:67], v29 offset:34816\n" \
"s_add_u32 m0, %[mA], 0x1800\n" \
"v_mfma_f32_16x16x32_bf16 a[252:255], v[176:179], v[112:115], a[252:255]\n" \
"s_add_u32 s85, s62, 0xd800\n" \
"buffer_load_dwordx4 v24, s[76:79], s85 offen lds\n" \
"s_waitcnt lgkmcnt(12)\n" \
"v_smfmac_f32_16x16x64_bf16 a[16:19], v[140:143], v[84:91], v50\n" \
"v_mfma_f32_16x16x32_bf16 a[48:51], v[128:131], v[88:91], a[48:51]\n" \
"s_add_u32 m0, %[mA], 0x1c00\n" \
"s_add_u32 s85, s62, 0xfc00\n" \
"v_mfma_f32_16x16x32_bf16 a[48:51], v[124:127], v[84:87], a[48:51]\n" \
"ds_read_b128 v[68:71], v28 offset:36864\n" \
"s_waitcnt lgkmcnt(8)\n" \
"v_mfma_f32_16x16x32_bf16 a[80:83], v[84:87], v[132:135], a[80:83]\n" \
"buffer_load_dwordx4 v25, s[76:79], s85 offen lds\n" \
"v_mfma_f32_16x16x32_bf16 a[80:83], v[88:91], v[136:139], a[80:83]\n" \
"ds_read_b128 v[72:75], v29 offset:36864\n" \
"s_waitcnt lgkmcnt(6)\n" \
"v_smfmac_f32_16x16x64_bf16 a[112:115], v[140:143], v[52:59], v50\n" \
"v_mfma_f32_16x16x32_bf16 a[20:23], v[128:131], v[56:59], a[20:23]\n" \
"s_add_u32 m0, %[mB], 0xc00\n" \
"s_add_u32 s84, s63, 0x18000\n" \
"buffer_load_dwordx4 v27, s[68:71], s84 offen lds\n" \
"v_mfma_f32_16x16x32_bf16 a[20:23], v[124:127], v[52:55], a[20:23]\n" \
"ds_read_b128 v[76:79], v28 offset:38912\n" \
"ds_read_b128 v[80:83], v29 offset:38912\n" \
"v_mfma_f32_16x16x32_bf16 a[52:55], v[116:119], v[52:55], a[52:55]\n" \
"s_add_u32 s88, s44, 0x0\n" \
"s_waitcnt lgkmcnt(5)\n" \
"v_mfma_f32_16x16x32_bf16 a[52:55], v[120:123], v[56:59], a[52:55]\n" \
"buffer_store_dwordx4 v[200:203], v20, s[80:83], s88 offen offset:2048 sc1\n" \
"v_mfma_f32_16x16x32_bf16 a[84:87], v[56:59], v[136:139], a[84:87]\n" \
"ds_read_b64_tr_b16 v[152:153], v32 offset:40960\n" \
"ds_read_b64_tr_b16 v[154:155], v32 offset:41984\n" \
"v_mfma_f32_16x16x32_bf16 a[84:87], v[52:55], v[132:135], a[84:87]\n" \
"ds_read_b64_tr_b16 v[148:149], v32 offset:32768\n" \
"s_waitcnt lgkmcnt(7)\n" \
"v_smfmac_f32_16x16x64_bf16 a[116:119], v[140:143], v[60:67], v50\n" \
"v_mfma_f32_16x16x32_bf16 a[24:27], v[124:127], v[60:63], a[24:27]\n" \
"ds_read_b64_tr_b16 v[150:151], v32 offset:33792\n" \
"ds_read_b64_tr_b16 v[156:157], v33 offset:32768\n" \
"v_mfma_f32_16x16x32_bf16 a[24:27], v[128:131], v[64:67], a[24:27]\n" \
"ds_read_b64_tr_b16 v[158:159], v33 offset:33792\n" \
"ds_read_b64_tr_b16 v[160:161], v33 offset:40960\n" \
"v_mfma_f32_16x16x32_bf16 a[56:59], v[120:123], v[64:67], a[56:59]\n" \
"ds_read_b64_tr_b16 v[162:163], v33 offset:41984\n" \
"s_add_u32 s89, s44, 0x1000\n" \
"v_mfma_f32_16x16x32_bf16 a[56:59], v[116:119], v[60:63], a[56:59]\n" \
"buffer_store_dwordx4 v[204:207], v20, s[80:83], s89 offen offset:2048 sc1\n" \
"v_mfma_f32_16x16x32_bf16 a[88:91], v[60:63], v[132:135], a[88:91]\n" \
"ds_read_b64_tr_b16 v[168:169], v34 offset:40960\n" \
"ds_read_b64_tr_b16 v[170:171], v34 offset:41984\n" \
"v_mfma_f32_16x16x32_bf16 a[88:91], v[64:67], v[136:139], a[88:91]\n" \
"ds_read_b64_tr_b16 v[164:165], v34 offset:32768\n" \
"s_waitcnt lgkmcnt(13)\n" \
"v_smfmac_f32_16x16x64_bf16 a[120:123], v[140:143], v[68:75], v50\n" \
"v_mfma_f32_16x16x32_bf16 a[28:31], v[128:131], v[72:75], a[28:31]\n" \
"ds_read_b64_tr_b16 v[166:167], v34 offset:33792\n" \
"ds_read_b64_tr_b16 v[172:173], v35 offset:32768\n" \
"v_mfma_f32_16x16x32_bf16 a[28:31], v[124:127], v[68:71], a[28:31]\n" \
"ds_read_b64_tr_b16 v[174:175], v35 offset:33792\n" \
"ds_read_b64_tr_b16 v[176:177], v35 offset:40960\n" \
"v_mfma_f32_16x16x32_bf16 a[60:63], v[116:119], v[68:71], a[60:63]\n" \
"ds_read_b64_tr_b16 v[178:179], v35 offset:41984\n" \
"s_add_u32 s88, s44, 0x0\n" \
"v_mfma_f32_16x16x32_bf16 a[60:63], v[120:123], v[72:75], a[60:63]\n" \
"buffer_store_dwordx4 v[208:211], v20, s[80:83], s88 offen offset:3072 sc1\n" \
"v_mfma_f32_16x16x32_bf16 a[92:95], v[72:75], v[136:139], a[92:95]\n" \
"ds_read_b128 v[84:87], v48 offset:32768\n" \
"ds_read_b128 v[88:91], v49 offset:32768\n" \
"v_mfma_f32_16x16x32_bf16 a[92:95], v[68:71], v[132:135], a[92:95]\n" \
"ds_read_b128 v[92:95], v48 offset:34816\n" \
"s_waitcnt lgkmcnt(15)\n" \
"s_nop 0\n" \
"v_smfmac_f32_16x16x64_bf16 a[124:127], v[140:143], v[76:83], v50\n" \
"v_mfma_f32_16x16x32_bf16 a[0:3], v[124:127], v[76:79], a[0:3]\n" \
"ds_read_b128 v[96:99], v49 offset:34816\n" \
"ds_read_b128 v[100:103], v48 offset:36864\n" \
"v_mfma_f32_16x16x32_bf16 a[0:3], v[128:131], v[80:83], a[0:3]\n" \
"ds_read_b128 v[104:107], v49 offset:36864\n" \
"ds_read_b128 v[108:111], v48 offset:38912\n" \
"v_mfma_f32_16x16x32_bf16 a[4:7], v[120:123], v[80:83], a[4:7]\n" \
"s_add_u32 s62, s62, 0x80\n" \
"ds_read_b128 v[112:115], v49 offset:38912\n" \
"v_mfma_f32_16x16x32_bf16 a[4:7], v[116:119], v[76:79], a[4:7]\n" \
"s_cmp_lt_u32 s62, 0x480\n" \
"s_cselect_b32 s62, s62, 0\n" \
"v_mfma_f32_16x16x32_bf16 a[32:35], v[76:79], v[132:135], a[32:35]\n" \
"s_lshl_b32 s72, s62, 6\n" \
"s_add_u32 s72, s72, s37\n" \
"v_mfma_f32_16x16x32_bf16 a[32:35], v[80:83], v[136:139], a[32:35]\n" \
"s_lshl_b32 s63, s62, 12\n" \
"v_cvt_pk_bf16_f32 v252, v252, v253\n" \
"v_mfma_f32_16x16x32_bf16 a[128:131], v[152:155], v[56:59], a[128:131]\n" \
"v_cvt_pk_bf16_f32 v253, v254, v255\n" \
"v_cvt_pk_bf16_f32 v254, v180, v181\n" \
"v_mfma_f32_16x16x32_bf16 a[128:131], v[148:151], v[52:55], a[128:131]\n" \
"v_cvt_pk_bf16_f32 v255, v182, v183\n" \
"s_waitcnt lgkmcnt(11)\n" \
"v_mfma_f32_16x16x32_bf16 a[132:135], v[156:159], v[52:55], a[132:135]\n" \
"v_mfma_f32_16x16x32_bf16 a[132:135], v[160:163], v[56:59], a[132:135]\n" \
"v_mfma_f32_16x16x32_bf16 a[136:139], v[168:171], v[56:59], a[136:139]\n" \
"v_mfma_f32_16x16x32_bf16 a[136:139], v[164:167], v[52:55], a[136:139]\n" \
"s_waitcnt lgkmcnt(7)\n" \
"v_mfma_f32_16x16x32_bf16 a[140:143], v[172:175], v[52:55], a[140:143]\n" \
"v_mfma_f32_16x16x32_bf16 a[140:143], v[176:179], v[56:59], a[140:143]\n" \
"v_mfma_f32_16x16x32_bf16 a[160:163], v[152:155], v[64:67], a[160:163]\n" \
"v_mfma_f32_16x16x32_bf16 a[160:163], v[148:151], v[60:63], a[160:163]\n" \
"v_mfma_f32_16x16x32_bf16 a[164:167], v[156:159], v[60:63], a[164:167]\n" \
"v_mfma_f32_16x16x32_bf16 a[164:167], v[160:163], v[64:67], a[164:167]\n" \
"v_mfma_f32_16x16x32_bf16 a[168:171], v[168:171], v[64:67], a[168:171]\n" \
"v_mfma_f32_16x16x32_bf16 a[168:171], v[164:167], v[60:63], a[168:171]\n" \
"v_mfma_f32_16x16x32_bf16 a[172:175], v[172:175], v[60:63], a[172:175]\n" \
"v_mfma_f32_16x16x32_bf16 a[172:175], v[176:179], v[64:67], a[172:175]\n" \
"v_mfma_f32_16x16x32_bf16 a[192:195], v[152:155], v[72:75], a[192:195]\n" \
"v_mfma_f32_16x16x32_bf16 a[192:195], v[148:151], v[68:71], a[192:195]\n" \
"s_waitcnt vmcnt(3) lgkmcnt(0)\n" \
"s_barrier\n" \
"v_mfma_f32_16x16x32_bf16 a[196:199], v[156:159], v[68:71], a[196:199]\n" \
"s_add_u32 m0, s40, 0x8000\n" \
"s_add_u32 s85, s72, 0x0\n" \
"s_nop 0\n" \
"v_mfma_f32_16x16x32_bf16 a[196:199], v[160:163], v[72:75], a[196:199]\n" \
"buffer_load_dwordx4 v20, s[80:83], s85 offen sc1 lds\n" \
"v_mfma_f32_16x16x32_bf16 a[200:203], v[168:171], v[72:75], a[200:203]\n" \
"s_add_u32 m0, s40, 0x9000\n" \
"s_add_u32 s84, s72, 0x1000\n" \
"v_mfma_f32_16x16x32_bf16 a[200:203], v[164:167], v[68:71], a[200:203]\n" \
"ds_read_b64_tr_b16 v[140:141], v39 offset:0\n" \
"ds_read_b64_tr_b16 v[142:143], v39 offset:960\n" \
"v_mfma_f32_16x16x32_bf16 a[204:207], v[172:175], v[68:71], a[204:207]\n" \
"buffer_load_dwordx4 v20, s[80:83], s84 offen sc1 lds\n" \
"v_mfma_f32_16x16x32_bf16 a[204:207], v[176:179], v[72:75], a[204:207]\n" \
"ds_read_b64_tr_b16 v[128:129], v37 offset:4096\n" \
"ds_read_b64_tr_b16 v[130:131], v37 offset:4608\n" \
"v_mfma_f32_16x16x32_bf16 a[224:227], v[152:155], v[80:83], a[224:227]\n" \
"s_add_u32 m0, s40, 0xb000\n" \
"s_add_u32 s85, s62, 0x0\n" \
"v_mfma_f32_16x16x32_bf16 a[224:227], v[148:151], v[76:79], a[224:227]\n" \
"buffer_load_dwordx4 v21, s[76:79], s85 offen lds\n" \
"v_mfma_f32_16x16x32_bf16 a[228:231], v[156:159], v[76:79], a[228:231]\n" \
"ds_read_b64_tr_b16 v[124:125], v37 offset:0\n" \
"ds_read_b64_tr_b16 v[126:127], v37 offset:512\n" \
"v_mfma_f32_16x16x32_bf16 a[228:231], v[160:163], v[80:83], a[228:231]\n" \
"s_add_u32 m0, %[mA], 0x8000\n" \
"s_add_u32 s84, s62, 0x0\n" \
"v_mfma_f32_16x16x32_bf16 a[232:235], v[168:171], v[80:83], a[232:235]\n" \
"buffer_load_dwordx4 v24, s[76:79], s84 offen lds\n" \
"v_mfma_f32_16x16x32_bf16 a[232:235], v[164:167], v[76:79], a[232:235]\n" \
"ds_read_b64_tr_b16 v[132:133], v38 offset:0\n" \
"ds_read_b64_tr_b16 v[134:135], v38 offset:512\n" \
"v_mfma_f32_16x16x32_bf16 a[236:239], v[172:175], v[76:79], a[236:239]\n" \
"ds_read_b64_tr_b16 v[136:137], v38 offset:4096\n" \
"ds_read_b64_tr_b16 v[138:139], v38 offset:4608\n" \
"v_mfma_f32_16x16x32_bf16 a[236:239], v[176:179], v[80:83], a[236:239]\n" \
"s_add_u32 m0, %[mA], 0x8400\n" \
"s_add_u32 s85, s62, 0x2400\n" \
"v_mfma_f32_16x16x32_bf16 a[144:147], v[152:155], v[88:91], a[144:147]\n" \
"buffer_load_dwordx4 v25, s[76:79], s85 offen lds\n" \
"v_mfma_f32_16x16x32_bf16 a[144:147], v[148:151], v[84:87], a[144:147]\n" \
"ds_read_b128 v[52:55], v28 offset:0\n" \
"ds_read_b128 v[56:59], v29 offset:0\n" \
"v_mfma_f32_16x16x32_bf16 a[148:151], v[156:159], v[84:87], a[148:151]\n" \
"s_add_u32 m0, %[mB], 0x8000\n" \
"s_add_u32 s84, s63, 0x0\n" \
"s_nop 0\n" \
"v_mfma_f32_16x16x32_bf16 a[148:151], v[160:163], v[88:91], a[148:151]\n" \
"buffer_load_dwordx4 v26, s[68:71], s84 offen lds\n" \
"v_mfma_f32_16x16x32_bf16 a[152:155], v[168:171], v[88:91], a[152:155]\n" \
"ds_read_b64_tr_b16 v[116:117], v36 offset:0\n" \
"ds_read_b64_tr_b16 v[118:119], v36 offset:512\n" \
"v_mfma_f32_16x16x32_bf16 a[152:155], v[164:167], v[84:87], a[152:155]\n" \
"ds_read_b64_tr_b16 v[120:121], v36 offset:4096\n" \
"ds_read_b64_tr_b16 v[122:123], v36 offset:4608\n" \
"v_mfma_f32_16x16x32_bf16 a[156:159], v[172:175], v[84:87], a[156:159]\n" \
"s_add_u32 m0, %[mA], 0x8800\n" \
"s_add_u32 s85, s62, 0x4800\n" \
"v_mfma_f32_16x16x32_bf16 a[156:159], v[176:179], v[88:91], a[156:159]\n" \
"buffer_load_dwordx4 v24, s[76:79], s85 offen lds\n" \
"v_mfma_f32_16x16x32_bf16 a[176:179], v[152:155], v[96:99], a[176:179]\n" \
"ds_read_b128 v[84:87], v22 offset:0\n" \
"ds_read_b128 v[88:91], v23 offset:0\n" \
"v_mfma_f32_16x16x32_bf16 a[176:179], v[148:151], v[92:95], a[176:179]\n" \
"s_add_u32 m0, %[mA], 0x8c00\n" \
"s_add_u32 s84, s62, 0x6c00\n" \
"v_mfma_f32_16x16x32_bf16 a[180:183], v[156:159], v[92:95], a[180:183]\n" \
"buffer_load_dwordx4 v25, s[76:79], s84 offen lds\n" \
"v_mfma_f32_16x16x32_bf16 a[180:183], v[160:163], v[96:99], a[180:183]\n" \
"ds_read_b128 v[60:63], v28 offset:2048\n" \
"ds_read_b128 v[64:67], v29 offset:2048\n" \
"v_mfma_f32_16x16x32_bf16 a[184:187], v[168:171], v[96:99], a[184:187]\n" \
"ds_read_b128 v[68:71], v28 offset:4096\n" \
"ds_read_b128 v[72:75], v29 offset:4096\n" \
"v_mfma_f32_16x16x32_bf16 a[184:187], v[164:167], v[92:95], a[184:187]\n" \
"s_add_u32 m0, %[mB], 0x8400\n" \
"s_add_u32 s85, s63, 0x8000\n" \
"v_mfma_f32_16x16x32_bf16 a[188:191], v[172:175], v[92:95], a[188:191]\n" \
"buffer_load_dwordx4 v26, s[68:71], s85 offen lds\n" \
"v_mfma_f32_16x16x32_bf16 a[188:191], v[176:179], v[96:99], a[188:191]\n" \
"ds_read_b128 v[76:79], v28 offset:6144\n" \
"ds_read_b128 v[80:83], v29 offset:6144\n" \
"v_mfma_f32_16x16x32_bf16 a[208:211], v[152:155], v[104:107], a[208:211]\n" \
"s_add_u32 m0, %[mA], 0x9000\n" \
"s_add_u32 s84, s62, 0x9000\n" \
"v_mfma_f32_16x16x32_bf16 a[208:211], v[148:151], v[100:103], a[208:211]\n" \
"buffer_load_dwordx4 v24, s[76:79], s84 offen lds\n" \
"v_mfma_f32_16x16x32_bf16 a[212:215], v[156:159], v[100:103], a[212:215]\n" \
"ds_read_b128 v[92:95], v48 offset:2048\n" \
"ds_read_b128 v[96:99], v49 offset:2048\n" \
"v_mfma_f32_16x16x32_bf16 a[212:215], v[160:163], v[104:107], a[212:215]\n" \
"v_mfma_f32_16x16x32_bf16 a[216:219], v[168:171], v[104:107], a[216:219]\n" \
"s_add_u32 m0, %[mA], 0x9400\n" \
"s_add_u32 s85, s62, 0xb400\n" \
"v_mfma_f32_16x16x32_bf16 a[216:219], v[164:167], v[100:103], a[216:219]\n" \
"buffer_load_dwordx4 v25, s[76:79], s85 offen lds\n" \
"v_mfma_f32_16x16x32_bf16 a[220:223], v[172:175], v[100:103], a[220:223]\n" \
"ds_read_b128 v[100:103], v48 offset:4096\n" \
"v_mfma_f32_16x16x32_bf16 a[220:223], v[176:179], v[104:107], a[220:223]\n" \
"s_add_u32 m0, %[mB], 0x8800\n" \
"s_add_u32 s84, s63, 0x10000\n" \
"v_mfma_f32_16x16x32_bf16 a[240:243], v[152:155], v[112:115], a[240:243]\n" \
"buffer_load_dwordx4 v27, s[68:71], s84 offen lds\n" \
"v_mfma_f32_16x16x32_bf16 a[240:243], v[148:151], v[108:111], a[240:243]\n" \
"ds_read_b64_tr_b16 v[152:153], v32 offset:8192\n" \
"ds_read_b64_tr_b16 v[154:155], v32 offset:9216\n" \
"v_mfma_f32_16x16x32_bf16 a[244:247], v[156:159], v[108:111], a[244:247]\n" \
"ds_read_b64_tr_b16 v[148:149], v32 offset:0\n" \
"ds_read_b64_tr_b16 v[150:151], v32 offset:1024\n" \
"v_mfma_f32_16x16x32_bf16 a[244:247], v[160:163], v[112:115], a[244:247]\n" \
"s_add_u32 m0, %[mA], 0x9800\n" \
"s_add_u32 s85, s62, 0xd800\n" \
"v_mfma_f32_16x16x32_bf16 a[248:251], v[168:171], v[112:115], a[248:251]\n" \
"buffer_load_dwordx4 v24, s[76:79], s85 offen lds\n" \
"v_mfma_f32_16x16x32_bf16 a[248:251], v[164:167], v[108:111], a[248:251]\n" \
"ds_read_b64_tr_b16 v[156:157], v33 offset:0\n" \
"ds_read_b64_tr_b16 v[158:159], v33 offset:1024\n" \
"v_mfma_f32_16x16x32_bf16 a[252:255], v[172:175], v[108:111], a[252:255]\n" \
"ds_read_b64_tr_b16 v[160:161], v33 offset:8192\n" \
"ds_read_b64_tr_b16 v[162:163], v33 offset:9216\n" \
"v_mfma_f32_16x16x32_bf16 a[252:255], v[176:179], v[112:115], a[252:255]\n" \
"s_add_u32 m0, %[mA], 0x9c00\n" \
"s_waitcnt lgkmcnt(11)\n" \
"s_nop 0\n" \
"v_smfmac_f32_16x16x64_bf16 a[16:19], v[140:143], v[84:91], v50\n" \
"v_mfma_f32_16x16x32_bf16 a[48:51], v[128:131], v[88:91], a[48:51]\n" \
"s_add_u32 s85, s62, 0xfc00\n" \
"ds_read_b64_tr_b16 v[168:169], v34 offset:8192\n" \
"v_mfma_f32_16x16x32_bf16 a[48:51], v[124:127], v[84:87], a[48:51]\n" \
"buffer_load_dwordx4 v25, s[76:79], s85 offen lds\n" \
"v_mfma_f32_16x16x32_bf16 a[80:83], v[84:87], v[132:135], a[80:83]\n" \
"s_add_u32 m0, %[mB], 0x8c00\n" \
"s_add_u32 s84, s63, 0x18000\n" \
"v_mfma_f32_16x16x32_bf16 a[80:83], v[88:91], v[136:139], a[80:83]\n" \
"ds_read_b64_tr_b16 v[170:171], v34 offset:9216\n" \
"ds_read_b64_tr_b16 v[164:165], v34 offset:0\n" \
"v_smfmac_f32_16x16x64_bf16 a[112:115], v[140:143], v[52:59], v50\n" \
"v_mfma_f32_16x16x32_bf16 a[20:23], v[128:131], v[56:59], a[20:23]\n" \
"buffer_load_dwordx4 v27, s[68:71], s84 offen lds\n" \
"v_mfma_f32_16x16x32_bf16 a[20:23], v[124:127], v[52:55], a[20:23]\n" \
"s_add_u32 s89, s44, 0x1000\n" \
"ds_read_b64_tr_b16 v[166:167], v34 offset:1024\n" \
"v_mfma_f32_16x16x32_bf16 a[52:55], v[116:119], v[52:55], a[52:55]\n" \
"ds_read_b64_tr_b16 v[172:173], v35 offset:0\n" \
"ds_read_b64_tr_b16 v[174:175], v35 offset:1024\n" \
"v_mfma_f32_16x16x32_bf16 a[52:55], v[120:123], v[56:59], a[52:55]\n" \
"buffer_store_dwordx4 v[212:215], v20, s[80:83], s89 offen offset:3072 sc1\n" \
"v_mfma_f32_16x16x32_bf16 a[84:87], v[56:59], v[136:139], a[84:87]\n" \
"ds_read_b64_tr_b16 v[176:177], v35 offset:8192\n" \
"ds_read_b64_tr_b16 v[178:179], v35 offset:9216\n" \
"v_mfma_f32_16x16x32_bf16 a[84:87], v[52:55], v[132:135], a[84:87]\n" \
"ds_read_b128 v[84:87], v48 offset:0\n" \
"ds_read_b128 v[88:91], v49 offset:0\n" \
"v_smfmac_f32_16x16x64_bf16 a[116:119], v[140:143], v[60:67], v50\n" \
"v_mfma_f32_16x16x32_bf16 a[24:27], v[124:127], v[60:63], a[24:27]\n" \
"ds_read_b128 v[104:107], v49 offset:4096\n" \
"ds_read_b128 v[108:111], v48 offset:6144\n" \
"v_mfma_f32_16x16x32_bf16 a[24:27], v[128:131], v[64:67], a[24:27]\n" \
"s_add_u32 s88, s42, 0x0\n" \
"ds_read_b128 v[112:115], v49 offset:6144\n" \
"v_mfma_f32_16x16x32_bf16 a[56:59], v[120:123], v[64:67], a[56:59]\n" \
"buffer_store_dwordx4 v[216:219], v20, s[80:83], s88 offen offset:0 sc1\n" \
"v_mfma_f32_16x16x32_bf16 a[56:59], v[116:119], v[60:63], a[56:59]\n" \
"s_add_u32 s62, s62, 0x80\n" \
"s_add_u32 s94, %[b], %[w]\n" \
"v_mfma_f32_16x16x32_bf16 a[88:91], v[60:63], v[132:135], a[88:91]\n" \
"s_cmp_lt_u32 s62, 0x480\n" \
"s_cselect_b32 s62, s62, 0\n" \
"v_mfma_f32_16x16x32_bf16 a[88:91], v[64:67], v[136:139], a[88:91]\n" \
"s_lshl_b32 s72, s62, 6\n" \
"s_add_u32 s72, s72, s37\n" \
"v_smfmac_f32_16x16x64_bf16 a[120:123], v[140:143], v[68:75], v50\n" \
"v_mfma_f32_16x16x32_bf16 a[28:31], v[128:131], v[72:75], a[28:31]\n" \
"s_lshl_b32 s63, s62, 12\n" \
"s_and_b32 s96, 7, %[b]\n" \
"v_mfma_f32_16x16x32_bf16 a[28:31], v[124:127], v[68:71], a[28:31]\n" \
"s_cmp_lg_u32 s94, 0\n" \
"s_cselect_b32 s95, 0x40000000, 0x5bc0000\n" \
"v_mfma_f32_16x16x32_bf16 a[60:63], v[116:119], v[68:71], a[60:63]\n" \
"s_add_u32 s89, s42, 0x1000\n" \
"s_mul_i32 s97, 0x48, s96\n" \
"v_mfma_f32_16x16x32_bf16 a[60:63], v[120:123], v[72:75], a[60:63]\n" \
"buffer_store_dwordx4 v[220:223], v20, s[80:83], s89 offen offset:0 sc1\n" \
"v_mfma_f32_16x16x32_bf16 a[92:95], v[72:75], v[136:139], a[92:95]\n" \
"s_add_u32 s98, 0x47, s97\n" \
"v_writelane_b32 v11, s95, 50\n" \
"v_mfma_f32_16x16x32_bf16 a[92:95], v[68:71], v[132:135], a[92:95]\n" \
"s_cmp_lt_u32 %[w], 3\n" \
"s_cselect_b32 s99, s98, 0xfff\n" \
"v_smfmac_f32_16x16x64_bf16 a[124:127], v[140:143], v[76:83], v50\n" \
"v_mfma_f32_16x16x32_bf16 a[0:3], v[124:127], v[76:79], a[0:3]\n" \
"s_lshr_b32 s94, %[b], 3\n" \
"s_lshl_b32 s67, s96, 2\n" \
"v_mfma_f32_16x16x32_bf16 a[0:3], v[128:131], v[80:83], a[0:3]\n" \
"s_and_b32 s95, 3, s94\n" \
"s_mul_i32 s100, 0x480, s99\n" \
"v_mfma_f32_16x16x32_bf16 a[4:7], v[120:123], v[80:83], a[4:7]\n" \
"s_lshl_b32 s64, %[w], 5\n" \
"s_add_u32 s98, s67, s95\n" \
"v_mfma_f32_16x16x32_bf16 a[4:7], v[116:119], v[76:79], a[4:7]\n" \
"s_add_u32 s101, 0x120, s100\n" \
"s_add_u32 s65, s97, s64\n" \
"v_mfma_f32_16x16x32_bf16 a[32:35], v[76:79], v[132:135], a[32:35]\n" \
"s_mul_i32 s99, 0x24000, s98\n" \
"v_writelane_b32 v11, s101, 15\n" \
"v_mfma_f32_16x16x32_bf16 a[32:35], v[80:83], v[136:139], a[32:35]\n" \
"s_mul_i32 s66, 0x480, s65\n" \
"s_waitcnt lgkmcnt(0)\n" \
"v_mfma_f32_16x16x32_bf16 a[128:131], v[152:155], v[56:59], a[128:131]\n" \
"s_add_u32 s100, 0x1b000, s99\n" \
"s_min_u32 s97, 2, %[w]\n" \
"v_mfma_f32_16x16x32_bf16 a[128:131], v[148:151], v[52:55], a[128:131]\n" \
"s_cmp_lt_u32 %[w], 3\n" \
"s_cselect_b32 s101, s66, s100\n" \
"v_mfma_f32_16x16x32_bf16 a[132:135], v[156:159], v[52:55], a[132:135]\n" \
"s_mul_i32 s64, 0x2400, s97\n" \
"v_writelane_b32 v11, s101, 16\n" \
"v_mfma_f32_16x16x32_bf16 a[132:135], v[160:163], v[56:59], a[132:135]\n" \
"s_add_u32 s65, 0x20000, s64\n" \
"s_mul_i32 s94, 0x9000, s97\n" \
"v_mfma_f32_16x16x32_bf16 a[136:139], v[168:171], v[56:59], a[136:139]\n" \
"s_mul_i32 s95, 0x2400, %[w]\n" \
"v_writelane_b32 v11, s65, 17\n" \
"v_mfma_f32_16x16x32_bf16 a[136:139], v[164:167], v[52:55], a[136:139]\n" \
"s_add_u32 s67, s99, s94\n" \
"v_writelane_b32 v11, s67, 22\n" \
"v_mfma_f32_16x16x32_bf16 a[140:143], v[172:175], v[52:55], a[140:143]\n" \
"s_cmp_lt_u32 %[w], 3\n" \
"s_cselect_b32 s98, s95, 0x12000\n" \
"v_mfma_f32_16x16x32_bf16 a[140:143], v[176:179], v[56:59], a[140:143]\n" \
"s_add_u32 s66, 0x20000, s95\n" \
"v_writelane_b32 v11, s98, 18\n" \
"v_mfma_f32_16x16x32_bf16 a[160:163], v[152:155], v[64:67], a[160:163]\n" \
"s_cmp_lt_u32 %[w], 3\n" \
"s_cselect_b32 s100, s66, 0xfc00\n" \
"v_mfma_f32_16x16x32_bf16 a[160:163], v[148:151], v[60:63], a[160:163]\n" \
"v_writelane_b32 v11, s95, 19\n" \
"s_lshl_b32 s101, %[w], 10\n" \
"v_mfma_f32_16x16x32_bf16 a[164:167], v[156:159], v[60:63], a[164:167]\n" \
"s_lshr_b32 s65, s96, 1\n" \
"v_writelane_b32 v11, s100, 20\n" \
"v_mfma_f32_16x16x32_bf16 a[164:167], v[160:163], v[64:67], a[164:167]\n" \
"s_cmp_eq_u32 %[w], s65\n" \
"s_cselect_b32 s97, 0xff0000, 0\n" \
"v_mfma_f32_16x16x32_bf16 a[168:171], v[168:171], v[64:67], a[168:171]\n" \
"s_add_u32 s64, 0x1d400, s101\n" \
"v_writelane_b32 v11, s64, 21\n" \
"v_mfma_f32_16x16x32_bf16 a[168:171], v[164:167], v[60:63], a[168:171]\n" \
"v_writelane_b32 v11, s97, 6\n" \
"v_mfma_f32_16x16x32_bf16 a[172:175], v[172:175], v[60:63], a[172:175]\n" \
"v_mfma_f32_16x16x32_bf16 a[172:175], v[176:179], v[64:67], a[172:175]\n" \
"v_mfma_f32_16x16x32_bf16 a[192:195], v[152:155], v[72:75], a[192:195]\n" \
"v_mfma_f32_16x16x32_bf16 a[192:195], v[148:151], v[68:71], a[192:195]\n" \
"s_waitcnt vmcnt(3)\n" \
"s_barrier\n" \
"v_mfma_f32_16x16x32_bf16 a[196:199], v[156:159], v[68:71], a[196:199]\n" \
"s_add_u32 m0, s40, 0x0\n" \
"s_add_u32 s85, s72, 0x0\n" \
"s_nop 0\n" \
"v_mfma_f32_16x16x32_bf16 a[196:199], v[160:163], v[72:75], a[196:199]\n" \
"buffer_load_dwordx4 v20, s[80:83], s85 offen sc1 lds\n" \
"v_mfma_f32_16x16x32_bf16 a[200:203], v[168:171], v[72:75], a[200:203]\n" \
"s_add_u32 m0, s40, 0x1000\n" \
"s_add_u32 s84, s72, 0x1000\n" \
"v_mfma_f32_16x16x32_bf16 a[200:203], v[164:167], v[68:71], a[200:203]\n" \
"ds_read_b64_tr_b16 v[140:141], v39 offset:32768\n" \
"ds_read_b64_tr_b16 v[142:143], v39 offset:33728\n" \
"v_mfma_f32_16x16x32_bf16 a[204:207], v[172:175], v[68:71], a[204:207]\n" \
"buffer_load_dwordx4 v20, s[80:83], s84 offen sc1 lds\n" \
"v_mfma_f32_16x16x32_bf16 a[204:207], v[176:179], v[72:75], a[204:207]\n" \
"ds_read_b64_tr_b16 v[128:129], v37 offset:36864\n" \
"ds_read_b64_tr_b16 v[130:131], v37 offset:37376\n" \
"v_mfma_f32_16x16x32_bf16 a[224:227], v[152:155], v[80:83], a[224:227]\n" \
"s_add_u32 m0, s40, 0x3000\n" \
"s_add_u32 s85, s62, 0x0\n" \
"v_mfma_f32_16x16x32_bf16 a[224:227], v[148:151], v[76:79], a[224:227]\n" \
"buffer_load_dwordx4 v21, s[76:79], s85 offen lds\n" \
"v_mfma_f32_16x16x32_bf16 a[228:231], v[156:159], v[76:79], a[228:231]\n" \
"ds_read_b64_tr_b16 v[124:125], v37 offset:32768\n" \
"ds_read_b64_tr_b16 v[126:127], v37 offset:33280\n" \
"v_mfma_f32_16x16x32_bf16 a[228:231], v[160:163], v[80:83], a[228:231]\n" \
"s_add_u32 m0, %[mA], 0x0\n" \
"s_add_u32 s84, s62, 0x0\n" \
"s_nop 0\n" \
"v_mfma_f32_16x16x32_bf16 a[232:235], v[168:171], v[80:83], a[232:235]\n" \
"buffer_load_dwordx4 v24, s[76:79], s84 offen lds\n" \
"L_pf6:\n" \
"s_cbranch_execz L_pf10\n" \
"s_nop 0\n" \
"v_mfma_f32_16x16x32_bf16 a[232:235], v[164:167], v[76:79], a[232:235]\n" \
"ds_read_b64_tr_b16 v[132:133], v38 offset:32768\n" \
"ds_read_b64_tr_b16 v[134:135], v38 offset:33280\n" \
"v_mfma_f32_16x16x32_bf16 a[236:239], v[172:175], v[76:79], a[236:239]\n" \
"ds_read_b64_tr_b16 v[136:137], v38 offset:36864\n" \
"ds_read_b64_tr_b16 v[138:139], v38 offset:37376\n" \
"v_mfma_f32_16x16x32_bf16 a[236:239], v[176:179], v[80:83], a[236:239]\n" \
"s_add_u32 m0, %[mA], 0x400\n" \
"s_add_u32 s85, s62, 0x2400\n" \
"v_mfma_f32_16x16x32_bf16 a[144:147], v[152:155], v[88:91], a[144:147]\n" \
"buffer_load_dwordx4 v25, s[76:79], s85 offen lds\n" \
"v_mfma_f32_16x16x32_bf16 a[144:147], v[148:151], v[84:87], a[144:147]\n" \
"ds_read_b128 v[52:55], v28 offset:32768\n" \
"ds_read_b128 v[56:59], v29 offset:32768\n" \
"v_mfma_f32_16x16x32_bf16 a[148:151], v[156:159], v[84:87], a[148:151]\n" \
"s_add_u32 m0, %[mB], 0x0\n" \
"s_add_u32 s84, s63, 0x0\n" \
"v_mfma_f32_16x16x32_bf16 a[148:151], v[160:163], v[88:91], a[148:151]\n" \
"buffer_load_dwordx4 v26, s[68:71], s84 offen lds\n" \
"v_mfma_f32_16x16x32_bf16 a[152:155], v[168:171], v[88:91], a[152:155]\n" \
"ds_read_b64_tr_b16 v[116:117], v36 offset:32768\n" \
"ds_read_b64_tr_b16 v[118:119], v36 offset:33280\n" \
"v_mfma_f32_16x16x32_bf16 a[152:155], v[164:167], v[84:87], a[152:155]\n" \
"ds_read_b64_tr_b16 v[120:121], v36 offset:36864\n" \
"ds_read_b64_tr_b16 v[122:123], v36 offset:37376\n" \
"v_mfma_f32_16x16x32_bf16 a[156:159], v[172:175], v[84:87], a[156:159]\n" \
"s_add_u32 m0, %[mA], 0x800\n" \
"s_add_u32 s85, s62, 0x4800\n" \
"v_mfma_f32_16x16x32_bf16 a[156:159], v[176:179], v[88:91], a[156:159]\n" \
"buffer_load_dwordx4 v24, s[76:79], s85 offen lds\n" \
"v_mfma_f32_16x16x32_bf16 a[176:179], v[152:155], v[96:99], a[176:179]\n" \
"ds_read_b128 v[84:87], v22 offset:32768\n" \
"ds_read_b128 v[88:91], v23 offset:32768\n" \
"v_mfma_f32_16x16x32_bf16 a[176:179], v[148:151], v[92:95], a[176:179]\n" \
"s_add_u32 m0, %[mA], 0xc00\n" \
"s_add_u32 s84, s62, 0x6c00\n" \
"v_mfma_f32_16x16x32_bf16 a[180:183], v[156:159], v[92:95], a[180:183]\n" \
"buffer_load_dwordx4 v25, s[76:79], s84 offen lds\n" \
"v_mfma_f32_16x16x32_bf16 a[180:183], v[160:163], v[96:99], a[180:183]\n" \
"ds_read_b128 v[60:63], v28 offset:34816\n" \
"ds_read_b128 v[64:67], v29 offset:34816\n" \
"v_mfma_f32_16x16x32_bf16 a[184:187], v[168:171], v[96:99], a[184:187]\n" \
"ds_read_b128 v[68:71], v28 offset:36864\n" \
"ds_read_b128 v[72:75], v29 offset:36864\n" \
"v_mfma_f32_16x16x32_bf16 a[184:187], v[164:167], v[92:95], a[184:187]\n" \
"s_add_u32 m0, %[mB], 0x400\n" \
"s_add_u32 s85, s63, 0x8000\n" \
"v_mfma_f32_16x16x32_bf16 a[188:191], v[172:175], v[92:95], a[188:191]\n" \
"buffer_load_dwordx4 v26, s[68:71], s85 offen lds\n" \
"v_mfma_f32_16x16x32_bf16 a[188:191], v[176:179], v[96:99], a[188:191]\n" \
"ds_read_b128 v[76:79], v28 offset:38912\n" \
"ds_read_b128 v[80:83], v29 offset:38912\n" \
"v_mfma_f32_16x16x32_bf16 a[208:211], v[152:155], v[104:107], a[208:211]\n" \
"s_add_u32 m0, %[mA], 0x1000\n" \
"s_add_u32 s84, s62, 0x9000\n" \
"v_mfma_f32_16x16x32_bf16 a[208:211], v[148:151], v[100:103], a[208:211]\n" \
"buffer_load_dwordx4 v24, s[76:79], s84 offen lds\n" \
"v_mfma_f32_16x16x32_bf16 a[212:215], v[156:159], v[100:103], a[212:215]\n" \
"ds_read_b128 v[92:95], v48 offset:34816\n" \
"ds_read_b128 v[96:99], v49 offset:34816\n" \
"v_mfma_f32_16x16x32_bf16 a[212:215], v[160:163], v[104:107], a[212:215]\n" \
"v_mfma_f32_16x16x32_bf16 a[216:219], v[168:171], v[104:107], a[216:219]\n" \
"s_add_u32 m0, %[mA], 0x1400\n" \
"s_add_u32 s85, s62, 0xb400\n" \
"v_mfma_f32_16x16x32_bf16 a[216:219], v[164:167], v[100:103], a[216:219]\n" \
"buffer_load_dwordx4 v25, s[76:79], s85 offen lds\n" \
"v_mfma_f32_16x16x32_bf16 a[220:223], v[172:175], v[100:103], a[220:223]\n" \
"ds_read_b128 v[100:103], v48 offset:36864\n" \
"v_mfma_f32_16x16x32_bf16 a[220:223], v[176:179], v[104:107], a[220:223]\n" \
"s_add_u32 m0, %[mB], 0x800\n" \
"s_add_u32 s84, s63, 0x10000\n" \
"v_mfma_f32_16x16x32_bf16 a[240:243], v[152:155], v[112:115], a[240:243]\n" \
"buffer_load_dwordx4 v27, s[68:71], s84 offen lds\n" \
"v_mfma_f32_16x16x32_bf16 a[240:243], v[148:151], v[108:111], a[240:243]\n" \
"ds_read_b64_tr_b16 v[152:153], v32 offset:40960\n" \
"ds_read_b64_tr_b16 v[154:155], v32 offset:41984\n" \
"v_mfma_f32_16x16x32_bf16 a[244:247], v[156:159], v[108:111], a[244:247]\n" \
"ds_read_b64_tr_b16 v[148:149], v32 offset:32768\n" \
"ds_read_b64_tr_b16 v[150:151], v32 offset:33792\n" \
"v_mfma_f32_16x16x32_bf16 a[244:247], v[160:163], v[112:115], a[244:247]\n" \
"s_add_u32 m0, %[mA], 0x1800\n" \
"s_add_u32 s85, s62, 0xd800\n" \
"v_mfma_f32_16x16x32_bf16 a[248:251], v[168:171], v[112:115], a[248:251]\n" \
"buffer_load_dwordx4 v24, s[76:79], s85 offen lds\n" \
"v_mfma_f32_16x16x32_bf16 a[248:251], v[164:167], v[108:111], a[248:251]\n" \
"ds_read_b64_tr_b16 v[156:157], v33 offset:32768\n" \
"ds_read_b64_tr_b16 v[158:159], v33 offset:33792\n" \
"v_mfma_f32_16x16x32_bf16 a[252:255], v[172:175], v[108:111], a[252:255]\n" \
"ds_read_b64_tr_b16 v[160:161], v33 offset:40960\n" \
"ds_read_b64_tr_b16 v[162:163], v33 offset:41984\n" \
"v_mfma_f32_16x16x32_bf16 a[252:255], v[176:179], v[112:115], a[252:255]\n" \
"s_add_u32 m0, %[mA], 0x1c00\n" \
"s_waitcnt lgkmcnt(11)\n" \
"s_nop 0\n" \
"v_smfmac_f32_16x16x64_bf16 a[16:19], v[140:143], v[84:91], v50\n" \
"v_mfma_f32_16x16x32_bf16 a[48:51], v[128:131], v[88:91], a[48:51]\n" \
"s_add_u32 s85, s62, 0xfc00\n" \
"ds_read_b64_tr_b16 v[168:169], v34 offset:40960\n" \
"v_mfma_f32_16x16x32_bf16 a[48:51], v[124:127], v[84:87], a[48:51]\n" \
"buffer_load_dwordx4 v25, s[76:79], s85 offen lds\n" \
"v_mfma_f32_16x16x32_bf16 a[80:83], v[84:87], v[132:135], a[80:83]\n" \
"s_add_u32 m0, %[mB], 0xc00\n" \
"s_add_u32 s84, s63, 0x18000\n" \
"v_mfma_f32_16x16x32_bf16 a[80:83], v[88:91], v[136:139], a[80:83]\n" \
"ds_read_b64_tr_b16 v[170:171], v34 offset:41984\n" \
"ds_read_b64_tr_b16 v[164:165], v34 offset:32768\n" \
"v_smfmac_f32_16x16x64_bf16 a[112:115], v[140:143], v[52:59], v50\n" \
"v_mfma_f32_16x16x32_bf16 a[20:23], v[128:131], v[56:59], a[20:23]\n" \
"buffer_load_dwordx4 v27, s[68:71], s84 offen lds\n" \
"v_mfma_f32_16x16x32_bf16 a[20:23], v[124:127], v[52:55], a[20:23]\n" \
"s_add_u32 s88, s42, 0x2000\n" \
"ds_read_b64_tr_b16 v[166:167], v34 offset:33792\n" \
"v_mfma_f32_16x16x32_bf16 a[52:55], v[116:119], v[52:55], a[52:55]\n" \
"ds_read_b64_tr_b16 v[172:173], v35 offset:32768\n" \
"ds_read_b64_tr_b16 v[174:175], v35 offset:33792\n" \
"v_mfma_f32_16x16x32_bf16 a[52:55], v[120:123], v[56:59], a[52:55]\n" \
"buffer_store_dwordx4 v[224:227], v20, s[80:83], s88 offen offset:0 sc1\n" \
"v_mfma_f32_16x16x32_bf16 a[84:87], v[56:59], v[136:139], a[84:87]\n" \
"ds_read_b64_tr_b16 v[176:177], v35 offset:40960\n" \
"ds_read_b64_tr_b16 v[178:179], v35 offset:41984\n" \
"v_mfma_f32_16x16x32_bf16 a[84:87], v[52:55], v[132:135], a[84:87]\n" \
"ds_read_b128 v[84:87], v48 offset:32768\n" \
"ds_read_b128 v[88:91], v49 offset:32768\n" \
"v_smfmac_f32_16x16x64_bf16 a[116:119], v[140:143], v[60:67], v50\n" \
"v_mfma_f32_16x16x32_bf16 a[24:27], v[124:127], v[60:63], a[24:27]\n" \
"ds_read_b128 v[104:107], v49 offset:36864\n" \
"ds_read_b128 v[108:111], v48 offset:38912\n" \
"v_mfma_f32_16x16x32_bf16 a[24:27], v[128:131], v[64:67], a[24:27]\n" \
"s_add_u32 s89, s42, 0x3000\n" \
"ds_read_b128 v[112:115], v49 offset:38912\n" \
"v_mfma_f32_16x16x32_bf16 a[56:59], v[120:123], v[64:67], a[56:59]\n" \
"buffer_store_dwordx4 v[228:231], v20, s[80:83], s89 offen offset:0 sc1\n" \
"v_mfma_f32_16x16x32_bf16 a[56:59], v[116:119], v[60:63], a[56:59]\n" \
"s_add_u32 s62, s62, 0x80\n" \
"s_lshr_b32 s96, %[b], 3\n" \
"v_mfma_f32_16x16x32_bf16 a[88:91], v[60:63], v[132:135], a[88:91]\n" \
"s_cmp_lt_u32 s62, 0x480\n" \
"s_cselect_b32 s62, s62, 0\n" \
"v_mfma_f32_16x16x32_bf16 a[88:91], v[64:67], v[136:139], a[88:91]\n" \
"s_lshl_b32 s72, s62, 6\n" \
"s_add_u32 s72, s72, s37\n" \
"v_smfmac_f32_16x16x64_bf16 a[120:123], v[140:143], v[68:75], v50\n" \
"v_mfma_f32_16x16x32_bf16 a[28:31], v[128:131], v[72:75], a[28:31]\n" \
"s_lshl_b32 s63, s62, 12\n" \
"s_and_b32 s97, 3, s96\n" \
"v_mfma_f32_16x16x32_bf16 a[28:31], v[124:127], v[68:71], a[28:31]\n" \
"s_and_b32 s94, 7, %[b]\n" \
"s_add_u32 s98, 1, s97\n" \
"v_mfma_f32_16x16x32_bf16 a[60:63], v[116:119], v[68:71], a[60:63]\n" \
"s_add_u32 s88, s42, 0x0\n" \
"s_lshl_b32 s95, s94, 2\n" \
"v_mfma_f32_16x16x32_bf16 a[60:63], v[120:123], v[72:75], a[60:63]\n" \
"buffer_store_dwordx4 v[232:235], v20, s[80:83], s88 offen offset:1024 sc1\n" \
"v_mfma_f32_16x16x32_bf16 a[92:95], v[72:75], v[136:139], a[92:95]\n" \
"s_and_b32 s99, 3, s98\n" \
"s_add_u32 s100, s95, s99\n" \
"v_mfma_f32_16x16x32_bf16 a[92:95], v[68:71], v[132:135], a[92:95]\n" \
"s_mul_i32 s101, 0x24000, s100\n" \
"s_mul_i32 s64, 0x9000, %[w]\n" \
"v_smfmac_f32_16x16x64_bf16 a[124:127], v[140:143], v[76:83], v50\n" \
"v_mfma_f32_16x16x32_bf16 a[0:3], v[124:127], v[76:79], a[0:3]\n" \
"s_and_b32 s96, 2, %[w]\n" \
"s_add_u32 s65, s101, s64\n" \
"v_mfma_f32_16x16x32_bf16 a[0:3], v[128:131], v[80:83], a[0:3]\n" \
"s_lshr_b32 s66, %[b], 5\n" \
"s_lshl_b32 s98, s96, 1\n" \
"v_mfma_f32_16x16x32_bf16 a[4:7], v[120:123], v[80:83], a[4:7]\n" \
"s_and_b32 s99, 1, %[w]\n" \
"v_writelane_b32 v11, s65, 23\n" \
"v_mfma_f32_16x16x32_bf16 a[4:7], v[116:119], v[76:79], a[4:7]\n" \
"s_lshl_b32 s67, s66, 3\n" \
"s_add_u32 s101, s98, s99\n" \
"v_mfma_f32_16x16x32_bf16 a[32:35], v[76:79], v[132:135], a[32:35]\n" \
"s_add_u32 s65, s67, s101\n" \
"s_mul_i32 s66, 0x24800, s65\n" \
"v_mfma_f32_16x16x32_bf16 a[32:35], v[80:83], v[136:139], a[32:35]\n" \
"s_lshl_b32 s98, s100, 12\n" \
"s_waitcnt lgkmcnt(0)\n" \
"v_mfma_f32_16x16x32_bf16 a[128:131], v[152:155], v[56:59], a[128:131]\n" \
"s_add_u32 s96, 0x3000000, s66\n" \
"s_add_u32 s99, 0x4800, s98\n" \
"v_mfma_f32_16x16x32_bf16 a[128:131], v[148:151], v[52:55], a[128:131]\n" \
"s_add_u32 s65, s96, s99\n" \
"s_sub_u32 s100, 0x78, s67\n" \
"v_mfma_f32_16x16x32_bf16 a[132:135], v[156:159], v[52:55], a[132:135]\n" \
"v_writelane_b32 v11, s65, 26\n" \
"s_add_u32 s98, s100, s101\n" \
"v_mfma_f32_16x16x32_bf16 a[132:135], v[160:163], v[56:59], a[132:135]\n" \
"s_mul_i32 s65, 0x24800, s98\n" \
"s_add_u32 s67, 0x3000000, s65\n" \
"v_mfma_f32_16x16x32_bf16 a[136:139], v[168:171], v[56:59], a[136:139]\n" \
"s_add_u32 s101, 2, s97\n" \
"s_add_u32 s100, s67, s99\n" \
"v_mfma_f32_16x16x32_bf16 a[136:139], v[164:167], v[52:55], a[136:139]\n" \
"s_and_b32 s98, 3, s101\n" \
"v_writelane_b32 v11, s100, 29\n" \
"v_mfma_f32_16x16x32_bf16 a[140:143], v[172:175], v[52:55], a[140:143]\n" \
"s_add_u32 s99, s95, s98\n" \
"s_mul_i32 s100, 0x24000, s99\n" \
"v_mfma_f32_16x16x32_bf16 a[140:143], v[176:179], v[56:59], a[140:143]\n" \
"s_add_u32 s101, s100, s64\n" \
"s_lshl_b32 s98, s99, 12\n" \
"v_mfma_f32_16x16x32_bf16 a[160:163], v[152:155], v[64:67], a[160:163]\n" \
"v_writelane_b32 v11, s101, 24\n" \
"s_add_u32 s100, 0x4800, s98\n" \
"v_mfma_f32_16x16x32_bf16 a[160:163], v[148:151], v[60:63], a[160:163]\n" \
"s_add_u32 s101, s96, s100\n" \
"v_writelane_b32 v11, s101, 27\n" \
"v_mfma_f32_16x16x32_bf16 a[164:167], v[156:159], v[60:63], a[164:167]\n" \
"s_add_u32 s98, 3, s97\n" \
"s_add_u32 s99, s67, s100\n" \
"v_mfma_f32_16x16x32_bf16 a[164:167], v[160:163], v[64:67], a[164:167]\n" \
"s_and_b32 s101, 3, s98\n" \
"v_writelane_b32 v11, s99, 30\n" \
"v_mfma_f32_16x16x32_bf16 a[168:171], v[168:171], v[64:67], a[168:171]\n" \
"s_add_u32 s100, s95, s101\n" \
"s_mul_i32 s99, 0x24000, s100\n" \
"v_mfma_f32_16x16x32_bf16 a[168:171], v[164:167], v[60:63], a[168:171]\n" \
"s_lshl_b32 s98, s100, 12\n" \
"s_add_u32 s97, s99, s64\n" \
"v_mfma_f32_16x16x32_bf16 a[172:175], v[172:175], v[60:63], a[172:175]\n" \
"s_add_u32 s95, 0x4800, s98\n" \
"v_writelane_b32 v11, s97, 25\n" \
"v_mfma_f32_16x16x32_bf16 a[172:175], v[176:179], v[64:67], a[172:175]\n" \
"s_add_u32 s101, s96, s95\n" \
"v_writelane_b32 v11, s101, 28\n" \
"v_mfma_f32_16x16x32_bf16 a[192:195], v[152:155], v[72:75], a[192:195]\n" \
"s_add_u32 s99, s67, s95\n" \
"s_lshl_b32 s64, s94, 11\n" \
"v_mfma_f32_16x16x32_bf16 a[192:195], v[148:151], v[68:71], a[192:195]\n" \
"v_writelane_b32 v11, s99, 31\n" \
"s_add_u32 s97, s96, s64\n" \
"s_waitcnt vmcnt(3)\n" \
"s_barrier\n" \
"v_mfma_f32_16x16x32_bf16 a[196:199], v[156:159], v[68:71], a[196:199]\n" \
"s_add_u32 m0, s40, 0x8000\n" \
"s_add_u32 s85, s72, 0x0\n" \
"v_mfma_f32_16x16x32_bf16 a[196:199], v[160:163], v[72:75], a[196:199]\n" \
"buffer_load_dwordx4 v20, s[80:83], s85 offen sc1 lds\n" \
"v_mfma_f32_16x16x32_bf16 a[200:203], v[168:171], v[72:75], a[200:203]\n" \
"s_add_u32 m0, s40, 0x9000\n" \
"s_add_u32 s84, s72, 0x1000\n" \
"v_mfma_f32_16x16x32_bf16 a[200:203], v[164:167], v[68:71], a[200:203]\n" \
"ds_read_b64_tr_b16 v[140:141], v39 offset:0\n" \
"ds_read_b64_tr_b16 v[142:143], v39 offset:960\n" \
"v_mfma_f32_16x16x32_bf16 a[204:207], v[172:175], v[68:71], a[204:207]\n" \
"buffer_load_dwordx4 v20, s[80:83], s84 offen sc1 lds\n" \
"v_mfma_f32_16x16x32_bf16 a[204:207], v[176:179], v[72:75], a[204:207]\n" \
"ds_read_b64_tr_b16 v[128:129], v37 offset:4096\n" \
"ds_read_b64_tr_b16 v[130:131], v37 offset:4608\n" \
"v_mfma_f32_16x16x32_bf16 a[224:227], v[152:155], v[80:83], a[224:227]\n" \
"s_add_u32 m0, s40, 0xb000\n" \
"s_add_u32 s85, s62, 0x0\n" \
"v_mfma_f32_16x16x32_bf16 a[224:227], v[148:151], v[76:79], a[224:227]\n" \
"buffer_load_dwordx4 v21, s[76:79], s85 offen lds\n" \
"v_mfma_f32_16x16x32_bf16 a[228:231], v[156:159], v[76:79], a[228:231]\n" \
"ds_read_b64_tr_b16 v[124:125], v37 offset:0\n" \
"ds_read_b64_tr_b16 v[126:127], v37 offset:512\n" \
"v_mfma_f32_16x16x32_bf16 a[228:231], v[160:163], v[80:83], a[228:231]\n" \
"s_add_u32 m0, %[mA], 0x8000\n" \
"s_add_u32 s84, s62, 0x0\n" \
"v_mfma_f32_16x16x32_bf16 a[232:235], v[168:171], v[80:83], a[232:235]\n" \
"buffer_load_dwordx4 v24, s[76:79], s84 offen lds\n" \
"v_mfma_f32_16x16x32_bf16 a[232:235], v[164:167], v[76:79], a[232:235]\n" \
"ds_read_b64_tr_b16 v[132:133], v38 offset:0\n" \
"ds_read_b64_tr_b16 v[134:135], v38 offset:512\n" \
"v_mfma_f32_16x16x32_bf16 a[236:239], v[172:175], v[76:79], a[236:239]\n" \
"ds_read_b64_tr_b16 v[136:137], v38 offset:4096\n" \
"ds_read_b64_tr_b16 v[138:139], v38 offset:4608\n" \
"v_mfma_f32_16x16x32_bf16 a[236:239], v[176:179], v[80:83], a[236:239]\n" \
"s_add_u32 m0, %[mA], 0x8400\n" \
"s_add_u32 s85, s62, 0x2400\n" \
"v_mfma_f32_16x16x32_bf16 a[144:147], v[152:155], v[88:91], a[144:147]\n" \
"buffer_load_dwordx4 v25, s[76:79], s85 offen lds\n" \
"v_mfma_f32_16x16x32_bf16 a[144:147], v[148:151], v[84:87], a[144:147]\n" \
"ds_read_b128 v[52:55], v28 offset:0\n" \
"ds_read_b128 v[56:59], v29 offset:0\n" \
"v_mfma_f32_16x16x32_bf16 a[148:151], v[156:159], v[84:87], a[148:151]\n" \
"s_add_u32 m0, %[mB], 0x8000\n" \
"s_add_u32 s84, s63, 0x0\n" \
"s_nop 0\n" \
"v_mfma_f32_16x16x32_bf16 a[148:151], v[160:163], v[88:91], a[148:151]\n" \
"buffer_load_dwordx4 v26, s[68:71], s84 offen lds\n" \
"v_mfma_f32_16x16x32_bf16 a[152:155], v[168:171], v[88:91], a[152:155]\n" \
"ds_read_b64_tr_b16 v[116:117], v36 offset:0\n" \
"ds_read_b64_tr_b16 v[118:119], v36 offset:512\n" \
"v_mfma_f32_16x16x32_bf16 a[152:155], v[164:167], v[84:87], a[152:155]\n" \
"ds_read_b64_tr_b16 v[120:121], v36 offset:4096\n" \
"ds_read_b64_tr_b16 v[122:123], v36 offset:4608\n" \
"v_mfma_f32_16x16x32_bf16 a[156:159], v[172:175], v[84:87], a[156:159]\n" \
"s_add_u32 m0, %[mA], 0x8800\n" \
"s_add_u32 s85, s62, 0x4800\n" \
"v_mfma_f32_16x16x32_bf16 a[156:159], v[176:179], v[88:91], a[156:159]\n" \
"buffer_load_dwordx4 v24, s[76:79], s85 offen lds\n" \
"v_mfma_f32_16x16x32_bf16 a[176:179], v[152:155], v[96:99], a[176:179]\n" \
"ds_read_b128 v[84:87], v22 offset:0\n" \
"ds_read_b128 v[88:91], v23 offset:0\n" \
"v_mfma_f32_16x16x32_bf16 a[176:179], v[148:151], v[92:95], a[176:179]\n" \
"s_add_u32 m0, %[mA], 0x8c00\n" \
"s_add_u32 s84, s62, 0x6c00\n" \
"v_mfma_f32_16x16x32_bf16 a[180:183], v[156:159], v[92:95], a[180:183]\n" \
"buffer_load_dwordx4 v25, s[76:79], s84 offen lds\n" \
"v_mfma_f32_16x16x32_bf16 a[180:183], v[160:163], v[96:99], a[180:183]\n" \
"ds_read_b128 v[60:63], v28 offset:2048\n" \
"ds_read_b128 v[64:67], v29 offset:2048\n" \
"v_mfma_f32_16x16x32_bf16 a[184:187], v[168:171], v[96:99], a[184:187]\n" \
"ds_read_b128 v[68:71], v28 offset:4096\n" \
"ds_read_b128 v[72:75], v29 offset:4096\n" \
"v_mfma_f32_16x16x32_bf16 a[184:187], v[164:167], v[92:95], a[184:187]\n" \
"s_add_u32 m0, %[mB], 0x8400\n" \
"s_add_u32 s85, s63, 0x8000\n" \
"v_mfma_f32_16x16x32_bf16 a[188:191], v[172:175], v[92:95], a[188:191]\n" \
"buffer_load_dwordx4 v26, s[68:71], s85 offen lds\n" \
"v_mfma_f32_16x16x32_bf16 a[188:191], v[176:179], v[96:99], a[188:191]\n" \
"ds_read_b128 v[76:79], v28 offset:6144\n" \
"ds_read_b128 v[80:83], v29 offset:6144\n" \
"v_mfma_f32_16x16x32_bf16 a[208:211], v[152:155], v[104:107], a[208:211]\n" \
"s_add_u32 m0, %[mA], 0x9000\n" \
"s_add_u32 s84, s62, 0x9000\n" \
"v_mfma_f32_16x16x32_bf16 a[208:211], v[148:151], v[100:103], a[208:211]\n" \
"buffer_load_dwordx4 v24, s[76:79], s84 offen lds\n" \
"v_mfma_f32_16x16x32_bf16 a[212:215], v[156:159], v[100:103], a[212:215]\n" \
"ds_read_b128 v[92:95], v48 offset:2048\n" \
"ds_read_b128 v[96:99], v49 offset:2048\n" \
"v_mfma_f32_16x16x32_bf16 a[212:215], v[160:163], v[104:107], a[212:215]\n" \
"v_writelane_b32 v11, s97, 32\n" \
"s_and_b32 s99, 1, s94\n" \
"v_mfma_f32_16x16x32_bf16 a[216:219], v[168:171], v[104:107], a[216:219]\n" \
"s_add_u32 m0, %[mA], 0x9400\n" \
"s_add_u32 s85, s62, 0xb400\n" \
"v_mfma_f32_16x16x32_bf16 a[216:219], v[164:167], v[100:103], a[216:219]\n" \
"buffer_load_dwordx4 v25, s[76:79], s85 offen lds\n" \
"v_mfma_f32_16x16x32_bf16 a[220:223], v[172:175], v[100:103], a[220:223]\n" \
"ds_read_b128 v[100:103], v48 offset:4096\n" \
"s_add_u32 s100, s67, s64\n" \
"v_mfma_f32_16x16x32_bf16 a[220:223], v[176:179], v[104:107], a[220:223]\n" \
"s_add_u32 m0, %[mB], 0x8800\n" \
"s_add_u32 s84, s63, 0x10000\n" \
"v_mfma_f32_16x16x32_bf16 a[240:243], v[152:155], v[112:115], a[240:243]\n" \
"buffer_load_dwordx4 v27, s[68:71], s84 offen lds\n" \
"v_mfma_f32_16x16x32_bf16 a[240:243], v[148:151], v[108:111], a[240:243]\n" \
"ds_read_b64_tr_b16 v[152:153], v32 offset:8192\n" \
"ds_read_b64_tr_b16 v[154:155], v32 offset:9216\n" \
"v_mfma_f32_16x16x32_bf16 a[244:247], v[156:159], v[108:111], a[244:247]\n" \
"ds_read_b64_tr_b16 v[148:149], v32 offset:0\n" \
"ds_read_b64_tr_b16 v[150:151], v32 offset:1024\n" \
"v_mfma_f32_16x16x32_bf16 a[244:247], v[160:163], v[112:115], a[244:247]\n" \
"s_add_u32 m0, %[mA], 0x9800\n" \
"s_add_u32 s85, s62, 0xd800\n" \
"v_mfma_f32_16x16x32_bf16 a[248:251], v[168:171], v[112:115], a[248:251]\n" \
"buffer_load_dwordx4 v24, s[76:79], s85 offen lds\n" \
"v_mfma_f32_16x16x32_bf16 a[248:251], v[164:167], v[108:111], a[248:251]\n" \
"ds_read_b64_tr_b16 v[156:157], v33 offset:0\n" \
"ds_read_b64_tr_b16 v[158:159], v33 offset:1024\n" \
"v_mfma_f32_16x16x32_bf16 a[252:255], v[172:175], v[108:111], a[252:255]\n" \
"ds_read_b64_tr_b16 v[160:161], v33 offset:8192\n" \
"ds_read_b64_tr_b16 v[162:163], v33 offset:9216\n" \
"v_mfma_f32_16x16x32_bf16 a[252:255], v[176:179], v[112:115], a[252:255]\n" \
"s_add_u32 m0, %[mA], 0x9c00\n" \
"s_waitcnt lgkmcnt(11)\n" \
"s_nop 0\n" \
"v_smfmac_f32_16x16x64_bf16 a[16:19], v[140:143], v[84:91], v50\n" \
"v_mfma_f32_16x16x32_bf16 a[48:51], v[128:131], v[88:91], a[48:51]\n" \
"s_add_u32 s85, s62, 0xfc00\n" \
"ds_read_b64_tr_b16 v[168:169], v34 offset:8192\n" \
"v_mfma_f32_16x16x32_bf16 a[48:51], v[124:127], v[84:87], a[48:51]\n" \
"buffer_load_dwordx4 v25, s[76:79], s85 offen lds\n" \
"v_mfma_f32_16x16x32_bf16 a[80:83], v[84:87], v[132:135], a[80:83]\n" \
"s_add_u32 m0, %[mB], 0x8c00\n" \
"s_add_u32 s84, s63, 0x18000\n" \
"v_mfma_f32_16x16x32_bf16 a[80:83], v[88:91], v[136:139], a[80:83]\n" \
"ds_read_b64_tr_b16 v[170:171], v34 offset:9216\n" \
"ds_read_b64_tr_b16 v[164:165], v34 offset:0\n" \
"v_smfmac_f32_16x16x64_bf16 a[112:115], v[140:143], v[52:59], v50\n" \
"v_mfma_f32_16x16x32_bf16 a[20:23], v[128:131], v[56:59], a[20:23]\n" \
"buffer_load_dwordx4 v27, s[68:71], s84 offen lds\n" \
"v_mfma_f32_16x16x32_bf16 a[20:23], v[124:127], v[52:55], a[20:23]\n" \
"s_add_u32 s89, s42, 0x1000\n" \
"ds_read_b64_tr_b16 v[166:167], v34 offset:1024\n" \
"v_mfma_f32_16x16x32_bf16 a[52:55], v[116:119], v[52:55], a[52:55]\n" \
"ds_read_b64_tr_b16 v[172:173], v35 offset:0\n" \
"ds_read_b64_tr_b16 v[174:175], v35 offset:1024\n" \
"v_mfma_f32_16x16x32_bf16 a[52:55], v[120:123], v[56:59], a[52:55]\n" \
"buffer_store_dwordx4 v[236:239], v20, s[80:83], s89 offen offset:1024 sc1\n" \
"v_mfma_f32_16x16x32_bf16 a[84:87], v[56:59], v[136:139], a[84:87]\n" \
"ds_read_b64_tr_b16 v[176:177], v35 offset:8192\n" \
"ds_read_b64_tr_b16 v[178:179], v35 offset:9216\n" \
"v_mfma_f32_16x16x32_bf16 a[84:87], v[52:55], v[132:135], a[84:87]\n" \
"ds_read_b128 v[84:87], v48 offset:0\n" \
"ds_read_b128 v[88:91], v49 offset:0\n" \
"v_smfmac_f32_16x16x64_bf16 a[116:119], v[140:143], v[60:67], v50\n" \
"v_mfma_f32_16x16x32_bf16 a[24:27], v[124:127], v[60:63], a[24:27]\n" \
"ds_read_b128 v[104:107], v49 offset:4096\n" \
"ds_read_b128 v[108:111], v48 offset:6144\n" \
"v_mfma_f32_16x16x32_bf16 a[24:27], v[128:131], v[64:67], a[24:27]\n" \
"s_add_u32 s88, s42, 0x2000\n" \
"ds_read_b128 v[112:115], v49 offset:6144\n" \
"v_mfma_f32_16x16x32_bf16 a[56:59], v[120:123], v[64:67], a[56:59]\n" \
"buffer_store_dwordx4 v[240:243], v20, s[80:83], s88 offen offset:1024 sc1\n" \
"v_mfma_f32_16x16x32_bf16 a[56:59], v[116:119], v[60:63], a[56:59]\n" \
"s_add_u32 s62, s62, 0x80\n" \
"v_writelane_b32 v11, s100, 33\n" \
"v_mfma_f32_16x16x32_bf16 a[88:91], v[60:63], v[132:135], a[88:91]\n" \
"s_cmp_lt_u32 s62, 0x480\n" \
"s_cselect_b32 s62, s62, 0\n" \
"v_mfma_f32_16x16x32_bf16 a[88:91], v[64:67], v[136:139], a[88:91]\n" \
"s_lshl_b32 s72, s62, 6\n" \
"s_add_u32 s72, s72, s37\n" \
"v_smfmac_f32_16x16x64_bf16 a[120:123], v[140:143], v[68:75], v50\n" \
"v_mfma_f32_16x16x32_bf16 a[28:31], v[128:131], v[72:75], a[28:31]\n" \
"s_lshl_b32 s63, s62, 12\n" \
"s_add_u32 s98, 0x3004000, s66\n" \
"v_mfma_f32_16x16x32_bf16 a[28:31], v[124:127], v[68:71], a[28:31]\n" \
"s_cmp_lg_u32 s99, 0\n" \
"s_cselect_b32 s97, s67, s96\n" \
"v_mfma_f32_16x16x32_bf16 a[60:63], v[116:119], v[68:71], a[60:63]\n" \
"s_add_u32 s89, s42, 0x3000\n" \
"s_lshr_b32 s95, s94, 1\n" \
"v_mfma_f32_16x16x32_bf16 a[60:63], v[120:123], v[72:75], a[60:63]\n" \
"buffer_store_dwordx4 v[244:247], v20, s[80:83], s89 offen offset:1024 sc1\n" \
"v_mfma_f32_16x16x32_bf16 a[92:95], v[72:75], v[136:139], a[92:95]\n" \
"s_add_u32 s64, 0x4000, s97\n" \
"v_writelane_b32 v11, s98, 34\n" \
"v_mfma_f32_16x16x32_bf16 a[92:95], v[68:71], v[132:135], a[92:95]\n" \
"s_add_u32 s101, 0x3004000, s65\n" \
"s_and_b32 s96, 2, %[w]\n" \
"v_smfmac_f32_16x16x64_bf16 a[124:127], v[140:143], v[76:83], v50\n" \
"v_mfma_f32_16x16x32_bf16 a[0:3], v[124:127], v[76:79], a[0:3]\n" \
"s_cmp_eq_u32 %[w], s95\n" \
"s_cselect_b32 s100, s64, 0x40000000\n" \
"v_mfma_f32_16x16x32_bf16 a[0:3], v[128:131], v[80:83], a[0:3]\n" \
"v_writelane_b32 v11, s101, 35\n" \
"s_lshl_b32 s97, s96, 1\n" \
"v_mfma_f32_16x16x32_bf16 a[4:7], v[120:123], v[80:83], a[4:7]\n" \
"s_and_b32 s98, 1, %[w]\n" \
"v_writelane_b32 v11, s100, 36\n" \
"v_mfma_f32_16x16x32_bf16 a[4:7], v[116:119], v[76:79], a[4:7]\n" \
"s_lshr_b32 s94, %[b], 5\n" \
"s_add_u32 s99, s97, s98\n" \
"v_mfma_f32_16x16x32_bf16 a[32:35], v[76:79], v[132:135], a[32:35]\n" \
"s_lshl_b32 s95, s94, 7\n" \
"s_lshl_b32 s100, s99, 4\n" \
"v_mfma_f32_16x16x32_bf16 a[32:35], v[80:83], v[136:139], a[32:35]\n" \
"s_add_u32 s101, s95, s100\n" \
"s_waitcnt lgkmcnt(0)\n" \
"v_mfma_f32_16x16x32_bf16 a[128:131], v[152:155], v[56:59], a[128:131]\n" \
"s_mul_i32 s64, 0x480, s101\n" \
"s_lshr_b32 s65, %[b], 3\n" \
"v_mfma_f32_16x16x32_bf16 a[128:131], v[148:151], v[52:55], a[128:131]\n" \
"s_sub_u32 s99, 0x780, s95\n" \
"s_and_b32 s66, 3, s65\n" \
"v_mfma_f32_16x16x32_bf16 a[132:135], v[156:159], v[52:55], a[132:135]\n" \
"s_add_u32 s101, s99, s100\n" \
"s_mul_i32 s67, 0x120, s66\n" \
"v_mfma_f32_16x16x32_bf16 a[132:135], v[160:163], v[56:59], a[132:135]\n" \
"s_and_b32 s96, 7, %[b]\n" \
"s_mul_i32 s65, 0x480, s101\n" \
"v_mfma_f32_16x16x32_bf16 a[136:139], v[168:171], v[56:59], a[136:139]\n" \
"s_add_u32 s94, s64, s67\n" \
"s_lshl_b32 s97, s96, 5\n" \
"v_mfma_f32_16x16x32_bf16 a[136:139], v[164:167], v[52:55], a[136:139]\n" \
"s_add_u32 s66, s65, s67\n" \
"s_and_b32 s95, 1, s96\n" \
"v_mfma_f32_16x16x32_bf16 a[140:143], v[172:175], v[52:55], a[140:143]\n" \
"s_add_u32 s98, s94, s97\n" \
"v_writelane_b32 v11, s98, 39\n" \
"v_mfma_f32_16x16x32_bf16 a[140:143], v[176:179], v[56:59], a[140:143]\n" \
"s_cmp_lg_u32 s95, 0\n" \
"s_cselect_b32 s99, s66, s94\n" \
"v_mfma_f32_16x16x32_bf16 a[160:163], v[152:155], v[64:67], a[160:163]\n" \
"s_lshr_b32 s98, s96, 1\n" \
"s_add_u32 s100, 0x100, s99\n" \
"v_mfma_f32_16x16x32_bf16 a[160:163], v[148:151], v[60:63], a[160:163]\n" \
"s_add_u32 s64, s66, s97\n" \
"s_mov_b32 s94, s67\n" \
"v_mfma_f32_16x16x32_bf16 a[164:167], v[156:159], v[60:63], a[164:167]\n" \
"s_cmp_eq_u32 %[w], s98\n" \
"s_cselect_b32 s101, s100, 0x40000000\n" \
"v_mfma_f32_16x16x32_bf16 a[164:167], v[160:163], v[64:67], a[164:167]\n" \
"v_writelane_b32 v11, s64, 40\n" \
"v_writelane_b32 v11, s101, 41\n" \
"v_mfma_f32_16x16x32_bf16 a[168:171], v[168:171], v[64:67], a[168:171]\n" \
"s_add_u32 s96, s16, s94\n" \
"s_addc_u32 s97, s17, 0\n" \
"v_mfma_f32_16x16x32_bf16 a[168:171], v[164:167], v[60:63], a[168:171]\n" \
"s_add_u32 s98, s18, s94\n" \
"s_addc_u32 s99, s19, 0\n" \
"v_mfma_f32_16x16x32_bf16 a[172:175], v[172:175], v[60:63], a[172:175]\n" \
"s_and_b32 s97, s97, 0xffff\n" \
"v_writelane_b32 v11, s96, 7\n" \
"v_mfma_f32_16x16x32_bf16 a[172:175], v[176:179], v[64:67], a[172:175]\n" \
"v_writelane_b32 v11, s97, 8\n" \
"s_and_b32 s99, s99, 0xffff\n" \
"v_mfma_f32_16x16x32_bf16 a[192:195], v[152:155], v[72:75], a[192:195]\n" \
"v_writelane_b32 v11, s98, 9\n" \
"v_writelane_b32 v11, s99, 10\n" \
"v_mfma_f32_16x16x32_bf16 a[192:195], v[148:151], v[68:71], a[192:195]\n" \
"s_cmp_lt_u32 %[w], 3\n" \
"s_cselect_b64 s[96:97], s[96:97], s[98:99]\n" \
"s_waitcnt vmcnt(3)\n" \
"s_barrier\n" \
"v_mfma_f32_16x16x32_bf16 a[196:199], v[156:159], v[68:71], a[196:199]\n" \
"s_add_u32 m0, s40, 0x0\n" \
"s_add_u32 s85, s72, 0x0\n" \
"v_mfma_f32_16x16x32_bf16 a[196:199], v[160:163], v[72:75], a[196:199]\n" \
"buffer_load_dwordx4 v20, s[80:83], s85 offen sc1 lds\n" \
"v_mfma_f32_16x16x32_bf16 a[200:203], v[168:171], v[72:75], a[200:203]\n" \
"s_add_u32 m0, s40, 0x1000\n" \
"s_add_u32 s84, s72, 0x1000\n" \
"v_mfma_f32_16x16x32_bf16 a[200:203], v[164:167], v[68:71], a[200:203]\n" \
"ds_read_b64_tr_b16 v[140:141], v39 offset:32768\n" \
"ds_read_b64_tr_b16 v[142:143], v39 offset:33728\n" \
"v_mfma_f32_16x16x32_bf16 a[204:207], v[172:175], v[68:71], a[204:207]\n" \
"buffer_load_dwordx4 v20, s[80:83], s84 offen sc1 lds\n" \
"v_mfma_f32_16x16x32_bf16 a[204:207], v[176:179], v[72:75], a[204:207]\n" \
"ds_read_b64_tr_b16 v[128:129], v37 offset:36864\n" \
"ds_read_b64_tr_b16 v[130:131], v37 offset:37376\n" \
"v_mfma_f32_16x16x32_bf16 a[224:227], v[152:155], v[80:83], a[224:227]\n" \
"s_add_u32 m0, s40, 0x3000\n" \
"s_add_u32 s85, s62, 0x0\n" \
"v_mfma_f32_16x16x32_bf16 a[224:227], v[148:151], v[76:79], a[224:227]\n" \
"buffer_load_dwordx4 v21, s[76:79], s85 offen lds\n" \
"v_mfma_f32_16x16x32_bf16 a[228:231], v[156:159], v[76:79], a[228:231]\n" \
"ds_read_b64_tr_b16 v[124:125], v37 offset:32768\n" \
"ds_read_b64_tr_b16 v[126:127], v37 offset:33280\n" \
"v_mfma_f32_16x16x32_bf16 a[228:231], v[160:163], v[80:83], a[228:231]\n" \
"s_add_u32 m0, %[mA], 0x0\n" \
"s_add_u32 s84, s62, 0x0\n" \
"s_nop 0\n" \
"v_mfma_f32_16x16x32_bf16 a[232:235], v[168:171], v[80:83], a[232:235]\n" \
"buffer_load_dwordx4 v24, s[76:79], s84 offen lds\n" \
"v_mfma_f32_16x16x32_bf16 a[232:235], v[164:167], v[76:79], a[232:235]\n" \
"ds_read_b64_tr_b16 v[132:133], v38 offset:32768\n" \
"ds_read_b64_tr_b16 v[134:135], v38 offset:33280\n" \
"v_mfma_f32_16x16x32_bf16 a[236:239], v[172:175], v[76:79], a[236:239]\n" \
"ds_read_b64_tr_b16 v[136:137], v38 offset:36864\n" \
"ds_read_b64_tr_b16 v[138:139], v38 offset:37376\n" \
"v_mfma_f32_16x16x32_bf16 a[236:239], v[176:179], v[80:83], a[236:239]\n" \
"s_add_u32 m0, %[mA], 0x400\n" \
"s_add_u32 s85, s62, 0x2400\n" \
"v_mfma_f32_16x16x32_bf16 a[144:147], v[152:155], v[88:91], a[144:147]\n" \
"buffer_load_dwordx4 v25, s[76:79], s85 offen lds\n" \
"v_mfma_f32_16x16x32_bf16 a[144:147], v[148:151], v[84:87], a[144:147]\n" \
"ds_read_b128 v[52:55], v28 offset:32768\n" \
"ds_read_b128 v[56:59], v29 offset:32768\n" \
"v_mfma_f32_16x16x32_bf16 a[148:151], v[156:159], v[84:87], a[148:151]\n" \
"s_add_u32 m0, %[mB], 0x0\n" \
"s_add_u32 s84, s63, 0x0\n" \
"v_mfma_f32_16x16x32_bf16 a[148:151], v[160:163], v[88:91], a[148:151]\n" \
"buffer_load_dwordx4 v26, s[68:71], s84 offen lds\n" \
"v_mfma_f32_16x16x32_bf16 a[152:155], v[168:171], v[88:91], a[152:155]\n" \
"ds_read_b64_tr_b16 v[116:117], v36 offset:32768\n" \
"ds_read_b64_tr_b16 v[118:119], v36 offset:33280\n" \
"v_mfma_f32_16x16x32_bf16 a[152:155], v[164:167], v[84:87], a[152:155]\n" \
"ds_read_b64_tr_b16 v[120:121], v36 offset:36864\n" \
"ds_read_b64_tr_b16 v[122:123], v36 offset:37376\n" \
"v_mfma_f32_16x16x32_bf16 a[156:159], v[172:175], v[84:87], a[156:159]\n" \
"s_add_u32 m0, %[mA], 0x800\n" \
"s_add_u32 s85, s62, 0x4800\n" \
"v_mfma_f32_16x16x32_bf16 a[156:159], v[176:179], v[88:91], a[156:159]\n" \
"buffer_load_dwordx4 v24, s[76:79], s85 offen lds\n" \
"v_mfma_f32_16x16x32_bf16 a[176:179], v[152:155], v[96:99], a[176:179]\n" \
"ds_read_b128 v[84:87], v22 offset:32768\n" \
"ds_read_b128 v[88:91], v23 offset:32768\n" \
"L_pf7:\n" \
"s_cbranch_execz L_pf11\n" \
"s_nop 0\n" \
"v_mfma_f32_16x16x32_bf16 a[176:179], v[148:151], v[92:95], a[176:179]\n" \
"s_add_u32 m0, %[mA], 0xc00\n" \
"s_add_u32 s84, s62, 0x6c00\n" \
"v_mfma_f32_16x16x32_bf16 a[180:183], v[156:159], v[92:95], a[180:183]\n" \
"buffer_load_dwordx4 v25, s[76:79], s84 offen lds\n" \
"v_mfma_f32_16x16x32_bf16 a[180:183], v[160:163], v[96:99], a[180:183]\n" \
"ds_read_b128 v[60:63], v28 offset:34816\n" \
"ds_read_b128 v[64:67], v29 offset:34816\n" \
"v_mfma_f32_16x16x32_bf16 a[184:187], v[168:171], v[96:99], a[184:187]\n" \
"ds_read_b128 v[68:71], v28 offset:36864\n" \
"ds_read_b128 v[72:75], v29 offset:36864\n" \
"v_mfma_f32_16x16x32_bf16 a[184:187], v[164:167], v[92:95], a[184:187]\n" \
"s_add_u32 m0, %[mB], 0x400\n" \
"s_add_u32 s85, s63, 0x8000\n" \
"v_mfma_f32_16x16x32_bf16 a[188:191], v[172:175], v[92:95], a[188:191]\n" \
"buffer_load_dwordx4 v26, s[68:71], s85 offen lds\n" \
"v_mfma_f32_16x16x32_bf16 a[188:191], v[176:179], v[96:99], a[188:191]\n" \
"ds_read_b128 v[76:79], v28 offset:38912\n" \
"ds_read_b128 v[80:83], v29 offset:38912\n" \
"v_mfma_f32_16x16x32_bf16 a[208:211], v[152:155], v[104:107], a[208:211]\n" \
"s_add_u32 m0, %[mA], 0x1000\n" \
"s_add_u32 s84, s62, 0x9000\n" \
"v_mfma_f32_16x16x32_bf16 a[208:211], v[148:151], v[100:103], a[208:211]\n" \
"buffer_load_dwordx4 v24, s[76:79], s84 offen lds\n" \
"v_mfma_f32_16x16x32_bf16 a[212:215], v[156:159], v[100:103], a[212:215]\n" \
"ds_read_b128 v[92:95], v48 offset:34816\n" \
"ds_read_b128 v[96:99], v49 offset:34816\n" \
"v_mfma_f32_16x16x32_bf16 a[212:215], v[160:163], v[104:107], a[212:215]\n" \
"v_writelane_b32 v11, s96, 13\n" \
"v_writelane_b32 v11, s97, 14\n" \
"v_mfma_f32_16x16x32_bf16 a[216:219], v[168:171], v[104:107], a[216:219]\n" \
"s_add_u32 m0, %[mA], 0x1400\n" \
"s_add_u32 s85, s62, 0xb400\n" \
"v_mfma_f32_16x16x32_bf16 a[216:219], v[164:167], v[100:103], a[216:219]\n" \
"buffer_load_dwordx4 v25, s[76:79], s85 offen lds\n" \
"v_mfma_f32_16x16x32_bf16 a[220:223], v[172:175], v[100:103], a[220:223]\n" \
"ds_read_b128 v[100:103], v48 offset:36864\n" \
"v_writelane_b32 v11, s20, 11\n" \
"v_mfma_f32_16x16x32_bf16 a[220:223], v[176:179], v[104:107], a[220:223]\n" \
"s_add_u32 m0, %[mB], 0x800\n" \
"s_add_u32 s84, s63, 0x10000\n" \
"v_mfma_f32_16x16x32_bf16 a[240:243], v[152:155], v[112:115], a[240:243]\n" \
"buffer_load_dwordx4 v27, s[68:71], s84 offen lds\n" \
"v_mfma_f32_16x16x32_bf16 a[240:243], v[148:151], v[108:111], a[240:243]\n" \
"ds_read_b64_tr_b16 v[152:153], v32 offset:40960\n" \
"ds_read_b64_tr_b16 v[154:155], v32 offset:41984\n" \
"v_mfma_f32_16x16x32_bf16 a[244:247], v[156:159], v[108:111], a[244:247]\n" \
"ds_read_b64_tr_b16 v[148:149], v32 offset:32768\n" \
"ds_read_b64_tr_b16 v[150:151], v32 offset:33792\n" \
"v_mfma_f32_16x16x32_bf16 a[244:247], v[160:163], v[112:115], a[244:247]\n" \
"s_add_u32 m0, %[mA], 0x1800\n" \
"s_add_u32 s85, s62, 0xd800\n" \
"v_mfma_f32_16x16x32_bf16 a[248:251], v[168:171], v[112:115], a[248:251]\n" \
"buffer_load_dwordx4 v24, s[76:79], s85 offen lds\n" \
"v_mfma_f32_16x16x32_bf16 a[248:251], v[164:167], v[108:111], a[248:251]\n" \
"ds_read_b64_tr_b16 v[156:157], v33 offset:32768\n" \
"ds_read_b64_tr_b16 v[158:159], v33 offset:33792\n" \
"v_mfma_f32_16x16x32_bf16 a[252:255], v[172:175], v[108:111], a[252:255]\n" \
"ds_read_b64_tr_b16 v[160:161], v33 offset:40960\n" \
"ds_read_b64_tr_b16 v[162:163], v33 offset:41984\n" \
"v_mfma_f32_16x16x32_bf16 a[252:255], v[176:179], v[112:115], a[252:255]\n" \
"s_add_u32 m0, %[mA], 0x1c00\n" \
"s_waitcnt lgkmcnt(11)\n" \
"s_nop 0\n" \
"v_smfmac_f32_16x16x64_bf16 a[16:19], v[140:143], v[84:91], v50\n" \
"v_mfma_f32_16x16x32_bf16 a[48:51], v[128:131], v[88:91], a[48:51]\n" \
"s_add_u32 s85, s62, 0xfc00\n" \
"ds_read_b64_tr_b16 v[168:169], v34 offset:40960\n" \
"v_mfma_f32_16x16x32_bf16 a[48:51], v[124:127], v[84:87], a[48:51]\n" \
"buffer_load_dwordx4 v25, s[76:79], s85 offen lds\n" \
"v_mfma_f32_16x16x32_bf16 a[80:83], v[84:87], v[132:135], a[80:83]\n" \
"s_add_u32 m0, %[mB], 0xc00\n" \
"s_add_u32 s84, s63, 0x18000\n" \
"v_mfma_f32_16x16x32_bf16 a[80:83], v[88:91], v[136:139], a[80:83]\n" \
"ds_read_b64_tr_b16 v[170:171], v34 offset:41984\n" \
"ds_read_b64_tr_b16 v[164:165], v34 offset:32768\n" \
"v_smfmac_f32_16x16x64_bf16 a[112:115], v[140:143], v[52:59], v50\n" \
"v_mfma_f32_16x16x32_bf16 a[20:23], v[128:131], v[56:59], a[20:23]\n" \
"buffer_load_dwordx4 v27, s[68:71], s84 offen lds\n" \
"v_mfma_f32_16x16x32_bf16 a[20:23], v[124:127], v[52:55], a[20:23]\n" \
"s_add_u32 s88, s43, 0x0\n" \
"s_nop 0\n" \
"ds_read_b64_tr_b16 v[166:167], v34 offset:33792\n" \
"v_mfma_f32_16x16x32_bf16 a[52:55], v[116:119], v[52:55], a[52:55]\n" \
"ds_read_b64_tr_b16 v[172:173], v35 offset:32768\n" \
"ds_read_b64_tr_b16 v[174:175], v35 offset:33792\n" \
"v_mfma_f32_16x16x32_bf16 a[52:55], v[120:123], v[56:59], a[52:55]\n" \
"buffer_store_dwordx4 v[248:251], v20, s[80:83], s88 offen offset:0 sc1\n" \
"v_mfma_f32_16x16x32_bf16 a[84:87], v[56:59], v[136:139], a[84:87]\n" \
"ds_read_b64_tr_b16 v[176:177], v35 offset:40960\n" \
"ds_read_b64_tr_b16 v[178:179], v35 offset:41984\n" \
"v_mfma_f32_16x16x32_bf16 a[84:87], v[52:55], v[132:135], a[84:87]\n" \
"ds_read_b128 v[84:87], v48 offset:32768\n" \
"ds_read_b128 v[88:91], v49 offset:32768\n" \
"v_smfmac_f32_16x16x64_bf16 a[116:119], v[140:143], v[60:67], v50\n" \
"v_mfma_f32_16x16x32_bf16 a[24:27], v[124:127], v[60:63], a[24:27]\n" \
"ds_read_b128 v[104:107], v49 offset:36864\n" \
"ds_read_b128 v[108:111], v48 offset:38912\n" \
"v_mfma_f32_16x16x32_bf16 a[24:27], v[128:131], v[64:67], a[24:27]\n" \
"s_add_u32 s89, s45, 0x1000\n" \
"ds_read_b128 v[112:115], v49 offset:38912\n" \
"v_mfma_f32_16x16x32_bf16 a[56:59], v[120:123], v[64:67], a[56:59]\n" \
"buffer_store_dwordx4 v[184:187], v20, s[80:83], s89 offen offset:2048 sc1\n" \
"v_mfma_f32_16x16x32_bf16 a[56:59], v[116:119], v[60:63], a[56:59]\n" \
"s_add_u32 s62, s62, 0x80\n" \
"s_and_b32 s95, s21, 0xffff\n" \
"v_mfma_f32_16x16x32_bf16 a[88:91], v[60:63], v[132:135], a[88:91]\n" \
"s_cmp_lt_u32 s62, 0x480\n" \
"s_cselect_b32 s62, s62, 0\n" \
"v_mfma_f32_16x16x32_bf16 a[88:91], v[64:67], v[136:139], a[88:91]\n" \
"s_lshl_b32 s72, s62, 6\n" \
"s_add_u32 s72, s72, s37\n" \
"v_smfmac_f32_16x16x64_bf16 a[120:123], v[140:143], v[68:75], v50\n" \
"v_mfma_f32_16x16x32_bf16 a[28:31], v[128:131], v[72:75], a[28:31]\n" \
"s_lshl_b32 s63, s62, 12\n" \
"v_writelane_b32 v11, s95, 12\n" \
"v_mfma_f32_16x16x32_bf16 a[28:31], v[124:127], v[68:71], a[28:31]\n" \
"s_and_b32 s95, 1, %[w]\n" \
"s_lshr_b32 s96, %[w], 1\n" \
"v_mfma_f32_16x16x32_bf16 a[60:63], v[116:119], v[68:71], a[60:63]\n" \
"s_add_u32 s88, s45, 0x1000\n" \
"s_lshl_b32 s94, %[w], 11\n" \
"v_mfma_f32_16x16x32_bf16 a[60:63], v[120:123], v[72:75], a[60:63]\n" \
"buffer_store_dwordx4 v[252:255], v20, s[80:83], s88 offen offset:3072 sc1\n" \
"v_mfma_f32_16x16x32_bf16 a[92:95], v[72:75], v[136:139], a[92:95]\n" \
"s_lshl_b32 s15, s95, 12\n" \
"s_lshl_b32 s16, s96, 14\n" \
"v_mfma_f32_16x16x32_bf16 a[92:95], v[68:71], v[132:135], a[92:95]\n" \
"s_add_u32 s12, 0xc000, s94\n" \
"s_add_u32 s97, s16, s15\n" \
"v_smfmac_f32_16x16x64_bf16 a[124:127], v[140:143], v[76:83], v50\n" \
"v_mfma_f32_16x16x32_bf16 a[0:3], v[124:127], v[76:79], a[0:3]\n" \
"s_lshr_b32 s100, %[b], 5\n" \
"s_and_b32 s65, 7, %[b]\n" \
"v_mfma_f32_16x16x32_bf16 a[0:3], v[128:131], v[80:83], a[0:3]\n" \
"s_lshr_b32 s67, %[b], 3\n" \
"s_xor_b32 s13, 0x8000, s97\n" \
"v_mfma_f32_16x16x32_bf16 a[4:7], v[120:123], v[80:83], a[4:7]\n" \
"s_add_u32 s20, 0x14000, s97\n" \
"s_mul_i32 s98, 0xe000, s96\n" \
"v_mfma_f32_16x16x32_bf16 a[4:7], v[116:119], v[76:79], a[4:7]\n" \
"s_lshl_b32 s18, s100, 1\n" \
"s_lshl_b32 s66, s65, 2\n" \
"v_mfma_f32_16x16x32_bf16 a[32:35], v[76:79], v[132:135], a[32:35]\n" \
"s_and_b32 s94, 3, s67\n" \
"s_add_u32 s99, s98, s15\n" \
"v_mfma_f32_16x16x32_bf16 a[32:35], v[80:83], v[136:139], a[32:35]\n" \
"s_add_u32 s101, s18, s96\n" \
"s_waitcnt lgkmcnt(0)\n" \
"v_mfma_f32_16x16x32_bf16 a[128:131], v[152:155], v[56:59], a[128:131]\n" \
"s_mov_b32 s64, 0x4000\n" \
"s_add_u32 s97, s66, s94\n" \
"v_mfma_f32_16x16x32_bf16 a[128:131], v[148:151], v[52:55], a[128:131]\n" \
"s_add_u32 s21, 0x10000, s99\n" \
"s_lshl_b32 s14, s101, 15\n" \
"v_mfma_f32_16x16x32_bf16 a[132:135], v[156:159], v[52:55], a[132:135]\n" \
"s_cmp_lg_u32 s100, 0\n" \
"s_cselect_b32 s17, s64, 0x8000\n" \
"v_mfma_f32_16x16x32_bf16 a[132:135], v[160:163], v[56:59], a[132:135]\n" \
"s_lshl_b32 s98, s97, 8\n" \
"s_and_b32 s67, 2, %[w]\n" \
"v_mfma_f32_16x16x32_bf16 a[136:139], v[168:171], v[56:59], a[136:139]\n" \
"s_add_u32 s99, 0x5be8020, s98\n" \
"s_lshl_b32 s101, s100, 2\n" \
"v_mfma_f32_16x16x32_bf16 a[136:139], v[164:167], v[52:55], a[136:139]\n" \
"s_lshl_b32 s66, s67, 1\n" \
"s_add_u32 s64, s99, s101\n" \
"v_mfma_f32_16x16x32_bf16 a[140:143], v[172:175], v[52:55], a[140:143]\n" \
"s_lshl_b32 s65, s100, 3\n" \
"s_add_u32 s94, s66, s95\n" \
"v_mfma_f32_16x16x32_bf16 a[140:143], v[176:179], v[56:59], a[140:143]\n" \
"s_cmp_eq_u32 %[w], 1\n" \
"s_cselect_b32 s22, s64, 0x40000000\n" \
"v_mfma_f32_16x16x32_bf16 a[160:163], v[152:155], v[64:67], a[160:163]\n" \
"s_add_u32 s98, s65, s94\n" \
"s_mul_i32 s99, 0x24800, s98\n" \
"v_mfma_f32_16x16x32_bf16 a[160:163], v[148:151], v[60:63], a[160:163]\n" \
"s_lshl_b32 s101, s97, 12\n" \
"s_sub_u32 s67, 0x78, s65\n" \
"v_mfma_f32_16x16x32_bf16 a[164:167], v[156:159], v[60:63], a[164:167]\n" \
"s_add_u32 s64, s99, s101\n" \
"s_add_u32 s66, s67, s94\n" \
"v_mfma_f32_16x16x32_bf16 a[164:167], v[160:163], v[64:67], a[164:167]\n" \
"s_add_u32 s60, 0x3004800, s64\n" \
"s_mul_i32 s95, 0x24800, s66\n" \
"v_mfma_f32_16x16x32_bf16 a[168:171], v[168:171], v[64:67], a[168:171]\n" \
"s_lshl_b32 s99, s96, 1\n" \
"s_add_u32 s98, s95, s101\n" \
"v_mfma_f32_16x16x32_bf16 a[168:171], v[164:167], v[60:63], a[168:171]\n" \
"s_add_u32 s64, s100, s99\n" \
"s_mov_b32 s65, 0x18000000\n" \
"v_mfma_f32_16x16x32_bf16 a[172:175], v[172:175], v[60:63], a[172:175]\n" \
"s_mul_i32 s94, 6, s96\n" \
"s_add_u32 s66, s96, s94\n" \
"v_mfma_f32_16x16x32_bf16 a[172:175], v[176:179], v[64:67], a[172:175]\n" \
"s_cmp_lt_u32 s64, 2\n" \
"s_cselect_b32 s67, s65, 0x30000000\n" \
"v_mfma_f32_16x16x32_bf16 a[192:195], v[152:155], v[72:75], a[192:195]\n" \
"s_mov_b32 s95, 0x80000\n" \
"s_mov_b32 s64, 0xaaaaaaaa\n" \
"v_mfma_f32_16x16x32_bf16 a[192:195], v[148:151], v[68:71], a[192:195]\n" \
"s_cmp_gt_u32 s66, s100\n" \
"s_cselect_b32 s101, s95, 0x40000\n" \
"s_waitcnt vmcnt(3)\n" \
"s_barrier\n" \
"s_mov_b64 exec, 1\n" \
"buffer_store_dword v45, off, s[80:83], s38 offset:0 sc1\n" \
"s_mov_b64 exec, -1\n" \
"v_mfma_f32_16x16x32_bf16 a[196:199], v[156:159], v[68:71], a[196:199]\n" \
"s_add_u32 m0, s40, 0x8000\n" \
"s_add_u32 s85, s72, 0x0\n" \
"s_nop 0\n" \
"v_mfma_f32_16x16x32_bf16 a[196:199], v[160:163], v[72:75], a[196:199]\n" \
"ds_read_b64_tr_b16 v[140:141], v39 offset:0\n" \
"ds_read_b64_tr_b16 v[142:143], v39 offset:960\n" \
"v_mfma_f32_16x16x32_bf16 a[200:203], v[168:171], v[72:75], a[200:203]\n" \
"buffer_load_dwordx4 v20, s[80:83], s85 offen sc1 lds\n" \
"v_mfma_f32_16x16x32_bf16 a[200:203], v[164:167], v[68:71], a[200:203]\n" \
"s_add_u32 m0, s40, 0x9000\n" \
"s_add_u32 s84, s72, 0x1000\n" \
"v_mfma_f32_16x16x32_bf16 a[204:207], v[172:175], v[68:71], a[204:207]\n" \
"ds_read_b64_tr_b16 v[128:129], v37 offset:4096\n" \
"ds_read_b64_tr_b16 v[130:131], v37 offset:4608\n" \
"v_mfma_f32_16x16x32_bf16 a[204:207], v[176:179], v[72:75], a[204:207]\n" \
"buffer_load_dwordx4 v20, s[80:83], s84 offen sc1 lds\n" \
"v_mfma_f32_16x16x32_bf16 a[224:227], v[152:155], v[80:83], a[224:227]\n" \
"s_add_u32 m0, s40, 0xb000\n" \
"s_add_u32 s85, s62, 0x0\n" \
"v_mfma_f32_16x16x32_bf16 a[224:227], v[148:151], v[76:79], a[224:227]\n" \
"ds_read_b64_tr_b16 v[124:125], v37 offset:0\n" \
"ds_read_b64_tr_b16 v[126:127], v37 offset:512\n" \
"v_mfma_f32_16x16x32_bf16 a[228:231], v[156:159], v[76:79], a[228:231]\n" \
"buffer_load_dwordx4 v21, s[76:79], s85 offen lds\n" \
"v_mfma_f32_16x16x32_bf16 a[228:231], v[160:163], v[80:83], a[228:231]\n" \
"s_add_u32 m0, %[mA], 0x8000\n" \
"s_add_u32 s84, s62, 0x0\n" \
"v_mfma_f32_16x16x32_bf16 a[232:235], v[168:171], v[80:83], a[232:235]\n" \
"ds_read_b64_tr_b16 v[132:133], v38 offset:0\n" \
"ds_read_b64_tr_b16 v[134:135], v38 offset:512\n" \
"v_mfma_f32_16x16x32_bf16 a[232:235], v[164:167], v[76:79], a[232:235]\n" \
"buffer_load_dwordx4 v24, s[76:79], s84 offen lds\n" \
"v_mfma_f32_16x16x32_bf16 a[236:239], v[172:175], v[76:79], a[236:239]\n" \
"ds_read_b64_tr_b16 v[136:137], v38 offset:4096\n" \
"ds_read_b64_tr_b16 v[138:139], v38 offset:4608\n" \
"v_mfma_f32_16x16x32_bf16 a[236:239], v[176:179], v[80:83], a[236:239]\n" \
"s_add_u32 m0, %[mA], 0x8400\n" \
"s_add_u32 s85, s62, 0x2400\n" \
"v_mfma_f32_16x16x32_bf16 a[144:147], v[152:155], v[88:91], a[144:147]\n" \
"buffer_load_dwordx4 v25, s[76:79], s85 offen lds\n" \
"v_mfma_f32_16x16x32_bf16 a[144:147], v[148:151], v[84:87], a[144:147]\n" \
"ds_read_b128 v[52:55], v28 offset:0\n" \
"ds_read_b128 v[56:59], v29 offset:0\n" \
"v_mfma_f32_16x16x32_bf16 a[148:151], v[156:159], v[84:87], a[148:151]\n" \
"s_add_u32 m0, %[mB], 0x8000\n" \
"s_add_u32 s84, s63, 0x0\n" \
"s_nop 0\n" \
"v_mfma_f32_16x16x32_bf16 a[148:151], v[160:163], v[88:91], a[148:151]\n" \
"buffer_load_dwordx4 v26, s[68:71], s84 offen lds\n" \
"v_mfma_f32_16x16x32_bf16 a[152:155], v[168:171], v[88:91], a[152:155]\n" \
"ds_read_b64_tr_b16 v[116:117], v36 offset:0\n" \
"ds_read_b64_tr_b16 v[118:119], v36 offset:512\n" \
"v_mfma_f32_16x16x32_bf16 a[152:155], v[164:167], v[84:87], a[152:155]\n" \
"ds_read_b64_tr_b16 v[120:121], v36 offset:4096\n" \
"ds_read_b64_tr_b16 v[122:123], v36 offset:4608\n" \
"v_mfma_f32_16x16x32_bf16 a[156:159], v[172:175], v[84:87], a[156:159]\n" \
"s_add_u32 m0, %[mA], 0x8800\n" \
"s_add_u32 s85, s62, 0x4800\n" \
"v_mfma_f32_16x16x32_bf16 a[156:159], v[176:179], v[88:91], a[156:159]\n" \
"buffer_load_dwordx4 v24, s[76:79], s85 offen lds\n" \
"v_mfma_f32_16x16x32_bf16 a[176:179], v[152:155], v[96:99], a[176:179]\n" \
"ds_read_b128 v[84:87], v22 offset:0\n" \
"ds_read_b128 v[88:91], v23 offset:0\n" \
"v_mfma_f32_16x16x32_bf16 a[176:179], v[148:151], v[92:95], a[176:179]\n" \
"s_add_u32 m0, %[mA], 0x8c00\n" \
"s_add_u32 s84, s62, 0x6c00\n" \
"v_mfma_f32_16x16x32_bf16 a[180:183], v[156:159], v[92:95], a[180:183]\n" \
"buffer_load_dwordx4 v25, s[76:79], s84 offen lds\n" \
"v_mfma_f32_16x16x32_bf16 a[180:183], v[160:163], v[96:99], a[180:183]\n" \
"ds_read_b128 v[60:63], v28 offset:2048\n" \
"ds_read_b128 v[64:67], v29 offset:2048\n" \
"v_mfma_f32_16x16x32_bf16 a[184:187], v[168:171], v[96:99], a[184:187]\n" \
"ds_read_b128 v[68:71], v28 offset:4096\n" \
"ds_read_b128 v[72:75], v29 offset:4096\n" \
"v_mfma_f32_16x16x32_bf16 a[184:187], v[164:167], v[92:95], a[184:187]\n" \
"s_add_u32 m0, %[mB], 0x8400\n" \
"s_add_u32 s85, s63, 0x8000\n" \
"v_mfma_f32_16x16x32_bf16 a[188:191], v[172:175], v[92:95], a[188:191]\n" \
"buffer_load_dwordx4 v26, s[68:71], s85 offen lds\n" \
"v_mfma_f32_16x16x32_bf16 a[188:191], v[176:179], v[96:99], a[188:191]\n" \
"ds_read_b128 v[76:79], v28 offset:6144\n" \
"ds_read_b128 v[80:83], v29 offset:6144\n" \
"v_mfma_f32_16x16x32_bf16 a[208:211], v[152:155], v[104:107], a[208:211]\n" \
"s_add_u32 m0, %[mA], 0x9000\n" \
"s_add_u32 s84, s62, 0x9000\n" \
"v_mfma_f32_16x16x32_bf16 a[208:211], v[148:151], v[100:103], a[208:211]\n" \
"buffer_load_dwordx4 v24, s[76:79], s84 offen lds\n" \
"v_mfma_f32_16x16x32_bf16 a[212:215], v[156:159], v[100:103], a[212:215]\n" \
"ds_read_b128 v[92:95], v48 offset:2048\n" \
"ds_read_b128 v[96:99], v49 offset:2048\n" \
"v_mfma_f32_16x16x32_bf16 a[212:215], v[160:163], v[104:107], a[212:215]\n" \
"s_mov_b32 s65, 0xaaaaaaaa\n" \
"s_add_u32 s23, s67, s101\n" \
"s_nop 0\n" \
"v_mfma_f32_16x16x32_bf16 a[216:219], v[168:171], v[104:107], a[216:219]\n" \
"s_add_u32 m0, %[mA], 0x9400\n" \
"s_add_u32 s85, s62, 0xb400\n" \
"v_mfma_f32_16x16x32_bf16 a[216:219], v[164:167], v[100:103], a[216:219]\n" \
"buffer_load_dwordx4 v25, s[76:79], s85 offen lds\n" \
"v_mfma_f32_16x16x32_bf16 a[220:223], v[172:175], v[100:103], a[220:223]\n" \
"ds_read_b128 v[100:103], v48 offset:4096\n" \
"s_mov_b32 s66, 0xfff0ffff\n" \
"v_mfma_f32_16x16x32_bf16 a[220:223], v[176:179], v[104:107], a[220:223]\n" \
"s_add_u32 m0, %[mB], 0x8800\n" \
"s_add_u32 s84, s63, 0x10000\n" \
"v_mfma_f32_16x16x32_bf16 a[240:243], v[152:155], v[112:115], a[240:243]\n" \
"buffer_load_dwordx4 v27, s[68:71], s84 offen lds\n" \
"v_mfma_f32_16x16x32_bf16 a[240:243], v[148:151], v[108:111], a[240:243]\n" \
"ds_read_b64_tr_b16 v[152:153], v32 offset:8192\n" \
"ds_read_b64_tr_b16 v[154:155], v32 offset:9216\n" \
"v_mfma_f32_16x16x32_bf16 a[244:247], v[156:159], v[108:111], a[244:247]\n" \
"ds_read_b64_tr_b16 v[148:149], v32 offset:0\n" \
"ds_read_b64_tr_b16 v[150:151], v32 offset:1024\n" \
"v_mfma_f32_16x16x32_bf16 a[244:247], v[160:163], v[112:115], a[244:247]\n" \
"s_add_u32 m0, %[mA], 0x9800\n" \
"s_add_u32 s85, s62, 0xd800\n" \
"v_mfma_f32_16x16x32_bf16 a[248:251], v[168:171], v[112:115], a[248:251]\n" \
"buffer_load_dwordx4 v24, s[76:79], s85 offen lds\n" \
"v_mfma_f32_16x16x32_bf16 a[248:251], v[164:167], v[108:111], a[248:251]\n" \
"ds_read_b64_tr_b16 v[156:157], v33 offset:0\n" \
"ds_read_b64_tr_b16 v[158:159], v33 offset:1024\n" \
"v_mfma_f32_16x16x32_bf16 a[252:255], v[172:175], v[108:111], a[252:255]\n" \
"ds_read_b64_tr_b16 v[160:161], v33 offset:8192\n" \
"ds_read_b64_tr_b16 v[162:163], v33 offset:9216\n" \
"v_mfma_f32_16x16x32_bf16 a[252:255], v[176:179], v[112:115], a[252:255]\n" \
"s_add_u32 m0, %[mA], 0x9c00\n" \
"s_waitcnt lgkmcnt(11)\n" \
"s_nop 0\n" \
"v_smfmac_f32_16x16x64_bf16 a[16:19], v[140:143], v[84:91], v50\n" \
"v_mfma_f32_16x16x32_bf16 a[48:51], v[128:131], v[88:91], a[48:51]\n" \
"s_add_u32 s85, s62, 0xfc00\n" \
"ds_read_b64_tr_b16 v[168:169], v34 offset:8192\n" \
"v_mfma_f32_16x16x32_bf16 a[48:51], v[124:127], v[84:87], a[48:51]\n" \
"buffer_load_dwordx4 v25, s[76:79], s85 offen lds\n" \
"v_mfma_f32_16x16x32_bf16 a[80:83], v[84:87], v[132:135], a[80:83]\n" \
"s_add_u32 m0, %[mB], 0x8c00\n" \
"s_add_u32 s84, s63, 0x18000\n" \
"v_mfma_f32_16x16x32_bf16 a[80:83], v[88:91], v[136:139], a[80:83]\n" \
"ds_read_b64_tr_b16 v[170:171], v34 offset:9216\n" \
"ds_read_b64_tr_b16 v[164:165], v34 offset:0\n" \
"v_smfmac_f32_16x16x64_bf16 a[112:115], v[140:143], v[52:59], v50\n" \
"v_mfma_f32_16x16x32_bf16 a[20:23], v[128:131], v[56:59], a[20:23]\n" \
"buffer_load_dwordx4 v27, s[68:71], s84 offen lds\n" \
"v_mfma_f32_16x16x32_bf16 a[20:23], v[124:127], v[52:55], a[20:23]\n" \
"s_add_u32 s89, s43, 0x1000\n" \
"ds_read_b64_tr_b16 v[166:167], v34 offset:1024\n" \
"v_mfma_f32_16x16x32_bf16 a[52:55], v[116:119], v[52:55], a[52:55]\n" \
"ds_read_b64_tr_b16 v[172:173], v35 offset:0\n" \
"ds_read_b64_tr_b16 v[174:175], v35 offset:1024\n" \
"v_mfma_f32_16x16x32_bf16 a[52:55], v[120:123], v[56:59], a[52:55]\n" \
"buffer_store_dwordx4 a[36:39], v20, s[80:83], s89 offen offset:0 sc1\n" \
"v_mfma_f32_16x16x32_bf16 a[84:87], v[56:59], v[136:139], a[84:87]\n" \
"ds_read_b64_tr_b16 v[176:177], v35 offset:8192\n" \
"ds_read_b64_tr_b16 v[178:179], v35 offset:9216\n" \
"v_mfma_f32_16x16x32_bf16 a[84:87], v[52:55], v[132:135], a[84:87]\n" \
"ds_read_b128 v[84:87], v48 offset:0\n" \
"ds_read_b128 v[88:91], v49 offset:0\n" \
"v_smfmac_f32_16x16x64_bf16 a[116:119], v[140:143], v[60:67], v50\n" \
"v_mfma_f32_16x16x32_bf16 a[24:27], v[124:127], v[60:63], a[24:27]\n" \
"ds_read_b128 v[104:107], v49 offset:4096\n" \
"ds_read_b128 v[108:111], v48 offset:6144\n" \
"v_mfma_f32_16x16x32_bf16 a[24:27], v[128:131], v[64:67], a[24:27]\n" \
"s_add_u32 s88, s43, 0x2000\n" \
"ds_read_b128 v[112:115], v49 offset:6144\n" \
"v_mfma_f32_16x16x32_bf16 a[56:59], v[120:123], v[64:67], a[56:59]\n" \
"buffer_store_dwordx4 a[64:67], v20, s[80:83], s88 offen offset:0 sc1\n" \
"v_mfma_f32_16x16x32_bf16 a[56:59], v[116:119], v[60:63], a[56:59]\n" \
"s_add_u32 s62, s62, 0x80\n" \
"s_mov_b32 s67, 0xf000ff00\n" \
"v_mfma_f32_16x16x32_bf16 a[88:91], v[60:63], v[132:135], a[88:91]\n" \
"s_cmp_lt_u32 s62, 0x480\n" \
"s_cselect_b32 s62, s62, 0\n" \
"v_mfma_f32_16x16x32_bf16 a[88:91], v[64:67], v[136:139], a[88:91]\n" \
"s_lshl_b32 s72, s62, 6\n" \
"s_add_u32 s72, s72, s37\n" \
"v_smfmac_f32_16x16x64_bf16 a[120:123], v[140:143], v[68:75], v50\n" \
"v_mfma_f32_16x16x32_bf16 a[28:31], v[128:131], v[72:75], a[28:31]\n" \
"s_lshl_b32 s63, s62, 12\n" \
"s_add_u32 s61, 0x3004800, s98\n" \
"v_mfma_f32_16x16x32_bf16 a[28:31], v[124:127], v[68:71], a[28:31]\n" \
"s_lshl_b32 s99, s97, 20\n" \
"s_lshl_b32 s98, s100, 16\n" \
"v_mfma_f32_16x16x32_bf16 a[60:63], v[116:119], v[68:71], a[60:63]\n" \
"s_add_u32 s89, s43, 0x3000\n" \
"s_add_u32 s8, 0x1000000, s99\n" \
"v_mfma_f32_16x16x32_bf16 a[60:63], v[120:123], v[72:75], a[60:63]\n" \
"buffer_store_dwordx4 a[68:71], v20, s[80:83], s89 offen offset:0 sc1\n" \
"v_mfma_f32_16x16x32_bf16 a[92:95], v[72:75], v[136:139], a[92:95]\n" \
"s_sub_u32 s19, 14, s18\n" \
"s_sub_u32 s10, 0x100000, s98\n" \
"v_mfma_f32_16x16x32_bf16 a[92:95], v[68:71], v[132:135], a[92:95]\n" \
"s_mov_b32 s11, 0x20000\n" \
"s_mov_b32 s58, 0x6000000\n" \
"v_smfmac_f32_16x16x64_bf16 a[124:127], v[140:143], v[76:83], v50\n" \
"v_mfma_f32_16x16x32_bf16 a[0:3], v[124:127], v[76:79], a[0:3]\n" \
"s_mov_b32 s59, 0x20000\n" \
"s_mov_b64 s[56:57], s[80:81]\n" \
"v_mfma_f32_16x16x32_bf16 a[0:3], v[128:131], v[80:83], a[0:3]\n" \
"s_add_u32 s8, s8, s80\n" \
"s_addc_u32 s9, s81, 0\n" \
"v_mfma_f32_16x16x32_bf16 a[4:7], v[120:123], v[80:83], a[4:7]\n" \
"v_mfma_f32_16x16x32_bf16 a[4:7], v[116:119], v[76:79], a[4:7]\n" \
"v_mfma_f32_16x16x32_bf16 a[32:35], v[76:79], v[132:135], a[32:35]\n" \
"v_mfma_f32_16x16x32_bf16 a[32:35], v[80:83], v[136:139], a[32:35]\n" \
"s_waitcnt lgkmcnt(0)\n" \
"s_nop 0\n" \
"v_mfma_f32_16x16x32_bf16 a[128:131], v[152:155], v[56:59], a[128:131]\n" \
"v_mfma_f32_16x16x32_bf16 a[128:131], v[148:151], v[52:55], a[128:131]\n" \
"v_mfma_f32_16x16x32_bf16 a[132:135], v[156:159], v[52:55], a[132:135]\n" \
"v_mfma_f32_16x16x32_bf16 a[132:135], v[160:163], v[56:59], a[132:135]\n" \
"v_mfma_f32_16x16x32_bf16 a[136:139], v[168:171], v[56:59], a[136:139]\n" \
"v_mfma_f32_16x16x32_bf16 a[136:139], v[164:167], v[52:55], a[136:139]\n" \
"v_mfma_f32_16x16x32_bf16 a[140:143], v[172:175], v[52:55], a[140:143]\n" \
"v_mfma_f32_16x16x32_bf16 a[140:143], v[176:179], v[56:59], a[140:143]\n" \
"v_mfma_f32_16x16x32_bf16 a[160:163], v[152:155], v[64:67], a[160:163]\n" \
"v_mfma_f32_16x16x32_bf16 a[160:163], v[148:151], v[60:63], a[160:163]\n" \
"v_mfma_f32_16x16x32_bf16 a[164:167], v[156:159], v[60:63], a[164:167]\n" \
"v_mfma_f32_16x16x32_bf16 a[164:167], v[160:163], v[64:67], a[164:167]\n" \
"v_mfma_f32_16x16x32_bf16 a[168:171], v[168:171], v[64:67], a[168:171]\n" \
"v_mfma_f32_16x16x32_bf16 a[168:171], v[164:167], v[60:63], a[168:171]\n" \
"v_mfma_f32_16x16x32_bf16 a[172:175], v[172:175], v[60:63], a[172:175]\n" \
"v_mfma_f32_16x16x32_bf16 a[172:175], v[176:179], v[64:67], a[172:175]\n" \
"v_mfma_f32_16x16x32_bf16 a[192:195], v[152:155], v[72:75], a[192:195]\n" \
"v_mfma_f32_16x16x32_bf16 a[192:195], v[148:151], v[68:71], a[192:195]\n" \
"s_waitcnt vmcnt(3)\n" \
"s_barrier\n" \
"v_mfma_f32_16x16x32_bf16 a[196:199], v[156:159], v[68:71], a[196:199]\n" \
"s_add_u32 m0, s40, 0x0\n" \
"s_add_u32 s85, s72, 0x0\n" \
"v_mfma_f32_16x16x32_bf16 a[196:199], v[160:163], v[72:75], a[196:199]\n" \
"buffer_load_dwordx4 v20, s[80:83], s85 offen sc1 lds\n" \
"v_mfma_f32_16x16x32_bf16 a[200:203], v[168:171], v[72:75], a[200:203]\n" \
"s_add_u32 m0, s40, 0x1000\n" \
"s_add_u32 s84, s72, 0x1000\n" \
"v_mfma_f32_16x16x32_bf16 a[200:203], v[164:167], v[68:71], a[200:203]\n" \
"ds_read_b64_tr_b16 v[140:141], v39 offset:32768\n" \
"ds_read_b64_tr_b16 v[142:143], v39 offset:33728\n" \
"v_mfma_f32_16x16x32_bf16 a[204:207], v[172:175], v[68:71], a[204:207]\n" \
"buffer_load_dwordx4 v20, s[80:83], s84 offen sc1 lds\n" \
"v_mfma_f32_16x16x32_bf16 a[204:207], v[176:179], v[72:75], a[204:207]\n" \
"ds_read_b64_tr_b16 v[128:129], v37 offset:36864\n" \
"ds_read_b64_tr_b16 v[130:131], v37 offset:37376\n" \
"v_mfma_f32_16x16x32_bf16 a[224:227], v[152:155], v[80:83], a[224:227]\n" \
"s_add_u32 m0, s40, 0x3000\n" \
"s_add_u32 s85, s62, 0x0\n" \
"v_mfma_f32_16x16x32_bf16 a[224:227], v[148:151], v[76:79], a[224:227]\n" \
"buffer_load_dwordx4 v21, s[76:79], s85 offen lds\n" \
"v_mfma_f32_16x16x32_bf16 a[228:231], v[156:159], v[76:79], a[228:231]\n" \
"ds_read_b64_tr_b16 v[124:125], v37 offset:32768\n" \
"ds_read_b64_tr_b16 v[126:127], v37 offset:33280\n" \
"v_mfma_f32_16x16x32_bf16 a[228:231], v[160:163], v[80:83], a[228:231]\n" \
"s_add_u32 m0, %[mA], 0x0\n" \
"s_add_u32 s84, s62, 0x0\n" \
"s_nop 0\n" \
"v_mfma_f32_16x16x32_bf16 a[232:235], v[168:171], v[80:83], a[232:235]\n" \
"buffer_load_dwordx4 v24, s[76:79], s84 offen lds\n" \
"v_mfma_f32_16x16x32_bf16 a[232:235], v[164:167], v[76:79], a[232:235]\n" \
"ds_read_b64_tr_b16 v[132:133], v38 offset:32768\n" \
"ds_read_b64_tr_b16 v[134:135], v38 offset:33280\n" \
"v_mfma_f32_16x16x32_bf16 a[236:239], v[172:175], v[76:79], a[236:239]\n" \
"ds_read_b64_tr_b16 v[136:137], v38 offset:36864\n" \
"ds_read_b64_tr_b16 v[138:139], v38 offset:37376\n" \
"v_mfma_f32_16x16x32_bf16 a[236:239], v[176:179], v[80:83], a[236:239]\n" \
"s_add_u32 m0, %[mA], 0x400\n" \
"s_add_u32 s85, s62, 0x2400\n" \
"v_mfma_f32_16x16x32_bf16 a[144:147], v[152:155], v[88:91], a[144:147]\n" \
"buffer_load_dwordx4 v25, s[76:79], s85 offen lds\n" \
"v_mfma_f32_16x16x32_bf16 a[144:147], v[148:151], v[84:87], a[144:147]\n" \
"ds_read_b128 v[52:55], v28 offset:32768\n" \
"ds_read_b128 v[56:59], v29 offset:32768\n" \
"v_mfma_f32_16x16x32_bf16 a[148:151], v[156:159], v[84:87], a[148:151]\n" \
"s_add_u32 m0, %[mB], 0x0\n" \
"s_add_u32 s84, s63, 0x0\n" \
"v_mfma_f32_16x16x32_bf16 a[148:151], v[160:163], v[88:91], a[148:151]\n" \
"buffer_load_dwordx4 v26, s[68:71], s84 offen lds\n" \
"v_mfma_f32_16x16x32_bf16 a[152:155], v[168:171], v[88:91], a[152:155]\n" \
"ds_read_b64_tr_b16 v[116:117], v36 offset:32768\n" \
"ds_read_b64_tr_b16 v[118:119], v36 offset:33280\n" \
"v_mfma_f32_16x16x32_bf16 a[152:155], v[164:167], v[84:87], a[152:155]\n" \
"ds_read_b64_tr_b16 v[120:121], v36 offset:36864\n" \
"ds_read_b64_tr_b16 v[122:123], v36 offset:37376\n" \
"v_mfma_f32_16x16x32_bf16 a[156:159], v[172:175], v[84:87], a[156:159]\n" \
"s_add_u32 m0, %[mA], 0x800\n" \
"s_add_u32 s85, s62, 0x4800\n" \
"v_mfma_f32_16x16x32_bf16 a[156:159], v[176:179], v[88:91], a[156:159]\n" \
"buffer_load_dwordx4 v24, s[76:79], s85 offen lds\n" \
"v_mfma_f32_16x16x32_bf16 a[176:179], v[152:155], v[96:99], a[176:179]\n" \
"ds_read_b128 v[84:87], v22 offset:32768\n" \
"ds_read_b128 v[88:91], v23 offset:32768\n" \
"v_mfma_f32_16x16x32_bf16 a[176:179], v[148:151], v[92:95], a[176:179]\n" \
"s_add_u32 m0, %[mA], 0xc00\n" \
"s_add_u32 s84, s62, 0x6c00\n" \
"v_mfma_f32_16x16x32_bf16 a[180:183], v[156:159], v[92:95], a[180:183]\n" \
"buffer_load_dwordx4 v25, s[76:79], s84 offen lds\n" \
"v_mfma_f32_16x16x32_bf16 a[180:183], v[160:163], v[96:99], a[180:183]\n" \
"ds_read_b128 v[60:63], v28 offset:34816\n" \
"ds_read_b128 v[64:67], v29 offset:34816\n" \
"v_mfma_f32_16x16x32_bf16 a[184:187], v[168:171], v[96:99], a[184:187]\n" \
"ds_read_b128 v[68:71], v28 offset:36864\n" \
"ds_read_b128 v[72:75], v29 offset:36864\n" \
"v_mfma_f32_16x16x32_bf16 a[184:187], v[164:167], v[92:95], a[184:187]\n" \
"s_add_u32 m0, %[mB], 0x400\n" \
"s_add_u32 s85, s63, 0x8000\n" \
"v_mfma_f32_16x16x32_bf16 a[188:191], v[172:175], v[92:95], a[188:191]\n" \
"buffer_load_dwordx4 v26, s[68:71], s85 offen lds\n" \
"v_mfma_f32_16x16x32_bf16 a[188:191], v[176:179], v[96:99], a[188:191]\n" \
"ds_read_b128 v[76:79], v28 offset:38912\n" \
"ds_read_b128 v[80:83], v29 offset:38912\n" \
"v_mfma_f32_16x16x32_bf16 a[208:211], v[152:155], v[104:107], a[208:211]\n" \
"s_add_u32 m0, %[mA], 0x1000\n" \
"s_add_u32 s84, s62, 0x9000\n" \
"v_mfma_f32_16x16x32_bf16 a[208:211], v[148:151], v[100:103], a[208:211]\n" \
"buffer_load_dwordx4 v24, s[76:79], s84 offen lds\n" \
"v_mfma_f32_16x16x32_bf16 a[212:215], v[156:159], v[100:103], a[212:215]\n" \
"ds_read_b128 v[92:95], v48 offset:34816\n" \
"ds_read_b128 v[96:99], v49 offset:34816\n" \
"v_mfma_f32_16x16x32_bf16 a[212:215], v[160:163], v[104:107], a[212:215]\n" \
"s_mov_b32 s72, 0x3eae1a93\n" \
"v_and_b32_e32 v26, 0x1ff, v20\n" \
"v_mfma_f32_16x16x32_bf16 a[216:219], v[168:171], v[104:107], a[216:219]\n" \
"s_add_u32 m0, %[mA], 0x1400\n" \
"s_add_u32 s85, s62, 0xb400\n" \
"v_mfma_f32_16x16x32_bf16 a[216:219], v[164:167], v[100:103], a[216:219]\n" \
"buffer_load_dwordx4 v25, s[76:79], s85 offen lds\n" \
"v_mfma_f32_16x16x32_bf16 a[220:223], v[172:175], v[100:103], a[220:223]\n" \
"ds_read_b128 v[100:103], v48 offset:36864\n" \
"v_add_u32_e64 v21, s48, v20\n" \
"v_mfma_f32_16x16x32_bf16 a[220:223], v[176:179], v[104:107], a[220:223]\n" \
"s_add_u32 m0, %[mB], 0x800\n" \
"s_add_u32 s84, s63, 0x10000\n" \
"v_mfma_f32_16x16x32_bf16 a[240:243], v[152:155], v[112:115], a[240:243]\n" \
"buffer_load_dwordx4 v27, s[68:71], s84 offen lds\n" \
"v_mfma_f32_16x16x32_bf16 a[240:243], v[148:151], v[108:111], a[240:243]\n" \
"ds_read_b64_tr_b16 v[152:153], v32 offset:40960\n" \
"ds_read_b64_tr_b16 v[154:155], v32 offset:41984\n" \
"v_mfma_f32_16x16x32_bf16 a[244:247], v[156:159], v[108:111], a[244:247]\n" \
"ds_read_b64_tr_b16 v[148:149], v32 offset:32768\n" \
"ds_read_b64_tr_b16 v[150:151], v32 offset:33792\n" \
"v_mfma_f32_16x16x32_bf16 a[244:247], v[160:163], v[112:115], a[244:247]\n" \
"s_add_u32 m0, %[mA], 0x1800\n" \
"s_add_u32 s85, s62, 0xd800\n" \
"v_mfma_f32_16x16x32_bf16 a[248:251], v[168:171], v[112:115], a[248:251]\n" \
"buffer_load_dwordx4 v24, s[76:79], s85 offen lds\n" \
"L_pf8:\n" \
"s_cbranch_execz L_pf12\n" \
"s_nop 0\n" \
"v_mfma_f32_16x16x32_bf16 a[248:251], v[164:167], v[108:111], a[248:251]\n" \
"ds_read_b64_tr_b16 v[156:157], v33 offset:32768\n" \
"ds_read_b64_tr_b16 v[158:159], v33 offset:33792\n" \
"v_mfma_f32_16x16x32_bf16 a[252:255], v[172:175], v[108:111], a[252:255]\n" \
"ds_read_b64_tr_b16 v[160:161], v33 offset:40960\n" \
"ds_read_b64_tr_b16 v[162:163], v33 offset:41984\n" \
"v_mfma_f32_16x16x32_bf16 a[252:255], v[176:179], v[112:115], a[252:255]\n" \
"s_add_u32 m0, %[mA], 0x1c00\n" \
"s_waitcnt lgkmcnt(11)\n" \
"v_smfmac_f32_16x16x64_bf16 a[16:19], v[140:143], v[84:91], v50\n" \
"v_mfma_f32_16x16x32_bf16 a[48:51], v[128:131], v[88:91], a[48:51]\n" \
"s_add_u32 s85, s62, 0xfc00\n" \
"ds_read_b64_tr_b16 v[168:169], v34 offset:40960\n" \
"v_mfma_f32_16x16x32_bf16 a[48:51], v[124:127], v[84:87], a[48:51]\n" \
"buffer_load_dwordx4 v25, s[76:79], s85 offen lds\n" \
"s_mov_b32 s76, s54\n" \
"v_mfma_f32_16x16x32_bf16 a[80:83], v[84:87], v[132:135], a[80:83]\n" \
"s_add_u32 m0, %[mB], 0xc00\n" \
"s_add_u32 s84, s63, 0x18000\n" \
"v_mfma_f32_16x16x32_bf16 a[80:83], v[88:91], v[136:139], a[80:83]\n" \
"ds_read_b64_tr_b16 v[170:171], v34 offset:41984\n" \
"ds_read_b64_tr_b16 v[164:165], v34 offset:32768\n" \
"v_smfmac_f32_16x16x64_bf16 a[112:115], v[140:143], v[52:59], v50\n" \
"v_mfma_f32_16x16x32_bf16 a[20:23], v[128:131], v[56:59], a[20:23]\n" \
"buffer_load_dwordx4 v27, s[68:71], s84 offen lds\n" \
"v_mfma_f32_16x16x32_bf16 a[20:23], v[124:127], v[52:55], a[20:23]\n" \
"s_add_u32 s88, s43, 0x0\n" \
"s_nop 0\n" \
"ds_read_b64_tr_b16 v[166:167], v34 offset:33792\n" \
"v_mfma_f32_16x16x32_bf16 a[52:55], v[116:119], v[52:55], a[52:55]\n" \
"ds_read_b64_tr_b16 v[172:173], v35 offset:32768\n" \
"ds_read_b64_tr_b16 v[174:175], v35 offset:33792\n" \
"v_mfma_f32_16x16x32_bf16 a[52:55], v[120:123], v[56:59], a[52:55]\n" \
"buffer_store_dwordx4 a[96:99], v20, s[80:83], s88 offen offset:1024 sc1\n" \
"v_mfma_f32_16x16x32_bf16 a[84:87], v[56:59], v[136:139], a[84:87]\n" \
"ds_read_b64_tr_b16 v[176:177], v35 offset:40960\n" \
"ds_read_b64_tr_b16 v[178:179], v35 offset:41984\n" \
"v_mfma_f32_16x16x32_bf16 a[84:87], v[52:55], v[132:135], a[84:87]\n" \
"ds_read_b128 v[84:87], v48 offset:32768\n" \
"ds_read_b128 v[88:91], v49 offset:32768\n" \
"v_smfmac_f32_16x16x64_bf16 a[116:119], v[140:143], v[60:67], v50\n" \
"v_mfma_f32_16x16x32_bf16 a[24:27], v[124:127], v[60:63], a[24:27]\n" \
"ds_read_b128 v[104:107], v49 offset:36864\n" \
"ds_read_b128 v[108:111], v48 offset:38912\n" \
"v_mfma_f32_16x16x32_bf16 a[24:27], v[128:131], v[64:67], a[24:27]\n" \
"s_add_u32 s89, s43, 0x1000\n" \
"ds_read_b128 v[112:115], v49 offset:38912\n" \
"v_mfma_f32_16x16x32_bf16 a[56:59], v[120:123], v[64:67], a[56:59]\n" \
"buffer_store_dwordx4 a[100:103], v20, s[80:83], s89 offen offset:1024 sc1\n" \
"s_add_u32 s89, s43, 0x3000\n" \
"v_mfma_f32_16x16x32_bf16 a[56:59], v[116:119], v[60:63], a[56:59]\n" \
"v_accvgpr_read_b32 v248, a16\n" \
"v_accvgpr_read_b32 v249, a17\n" \
"v_mfma_f32_16x16x32_bf16 a[88:91], v[60:63], v[132:135], a[88:91]\n" \
"v_accvgpr_read_b32 v250, a18\n" \
"v_accvgpr_read_b32 v251, a19\n" \
"v_mfma_f32_16x16x32_bf16 a[88:91], v[64:67], v[136:139], a[88:91]\n" \
"v_accvgpr_read_b32 v180, a48\n" \
"v_accvgpr_read_b32 v181, a49\n" \
"v_smfmac_f32_16x16x64_bf16 a[120:123], v[140:143], v[68:75], v50\n" \
"v_mfma_f32_16x16x32_bf16 a[28:31], v[128:131], v[72:75], a[28:31]\n" \
"v_accvgpr_read_b32 v182, a50\n" \
"v_accvgpr_read_b32 v183, a51\n" \
"v_mfma_f32_16x16x32_bf16 a[28:31], v[124:127], v[68:71], a[28:31]\n" \
"v_accvgpr_read_b32 v252, a80\n" \
"v_accvgpr_read_b32 v253, a81\n" \
"v_mfma_f32_16x16x32_bf16 a[60:63], v[116:119], v[68:71], a[60:63]\n" \
"s_add_u32 s88, s43, 0x2000\n" \
"v_accvgpr_read_b32 v254, a82\n" \
"v_mfma_f32_16x16x32_bf16 a[60:63], v[120:123], v[72:75], a[60:63]\n" \
"buffer_store_dwordx4 a[8:11], v20, s[80:83], s88 offen offset:1024 sc1\n" \
"s_add_u32 s88, s45, 0x0\n" \
"s_nop 0\n" \
"v_mfma_f32_16x16x32_bf16 a[92:95], v[72:75], v[136:139], a[92:95]\n" \
"v_accvgpr_read_b32 v255, a83\n" \
"v_accvgpr_read_b32 v196, a112\n" \
"v_mfma_f32_16x16x32_bf16 a[92:95], v[68:71], v[132:135], a[92:95]\n" \
"v_accvgpr_read_b32 v197, a113\n" \
"v_accvgpr_read_b32 v198, a114\n" \
"v_smfmac_f32_16x16x64_bf16 a[124:127], v[140:143], v[76:83], v50\n" \
"v_mfma_f32_16x16x32_bf16 a[0:3], v[124:127], v[76:79], a[0:3]\n" \
"v_accvgpr_read_b32 v199, a115\n" \
"v_accvgpr_read_b32 v188, a20\n" \
"v_mfma_f32_16x16x32_bf16 a[0:3], v[128:131], v[80:83], a[0:3]\n" \
"v_accvgpr_read_b32 v189, a21\n" \
"v_accvgpr_read_b32 v190, a22\n" \
"v_mfma_f32_16x16x32_bf16 a[4:7], v[120:123], v[80:83], a[4:7]\n" \
"v_accvgpr_read_b32 v191, a23\n" \
"v_accvgpr_read_b32 v184, a52\n" \
"v_mfma_f32_16x16x32_bf16 a[4:7], v[116:119], v[76:79], a[4:7]\n" \
"v_lshlrev_b32_e32 v45, 10, v20\n" \
"v_add_u32_e32 v45, s41, v45\n" \
"v_mfma_f32_16x16x32_bf16 a[32:35], v[76:79], v[132:135], a[32:35]\n" \
"s_mov_b32 m0, %[mS]\n" \
"v_accvgpr_read_b32 v185, a53\n" \
"v_mfma_f32_16x16x32_bf16 a[32:35], v[80:83], v[136:139], a[32:35]\n" \
"v_accvgpr_read_b32 v186, a54\n" \
"s_waitcnt lgkmcnt(0)\n" \
"v_mfma_f32_16x16x32_bf16 a[128:131], v[152:155], v[56:59], a[128:131]\n" \
"v_accvgpr_read_b32 v187, a55\n" \
"v_accvgpr_read_b32 v192, a84\n" \
"v_mfma_f32_16x16x32_bf16 a[128:131], v[148:151], v[52:55], a[128:131]\n" \
"v_accvgpr_read_b32 v193, a85\n" \
"v_accvgpr_read_b32 v194, a86\n" \
"v_mfma_f32_16x16x32_bf16 a[132:135], v[156:159], v[52:55], a[132:135]\n" \
"v_accvgpr_read_b32 v195, a87\n" \
"v_accvgpr_read_b32 v212, a116\n" \
"s_getpc_b64 s[86:87]\n" \
"L_ct15_%=:\n" \
"s_add_u32 s86, s86, L_end_%=-L_ct15_%=\n" \
"s_addc_u32 s87, s87, 0\n" \
"v_mfma_f32_16x16x32_bf16 a[132:135], v[160:163], v[56:59], a[132:135]\n" \
"v_accvgpr_read_b32 v213, a117\n" \
"v_accvgpr_read_b32 v214, a118\n" \
"v_mfma_f32_16x16x32_bf16 a[136:139], v[168:171], v[56:59], a[136:139]\n" \
"v_accvgpr_read_b32 v215, a119\n" \
"v_accvgpr_read_b32 v204, a24\n" \
"v_mfma_f32_16x16x32_bf16 a[136:139], v[164:167], v[52:55], a[136:139]\n" \
"v_accvgpr_read_b32 v205, a25\n" \
"v_accvgpr_read_b32 v206, a26\n" \
"v_mfma_f32_16x16x32_bf16 a[140:143], v[172:175], v[52:55], a[140:143]\n" \
"v_accvgpr_read_b32 v207, a27\n" \
"v_accvgpr_read_b32 v200, a56\n" \
"v_mfma_f32_16x16x32_bf16 a[140:143], v[176:179], v[56:59], a[140:143]\n" \
"v_accvgpr_read_b32 v201, a57\n" \
"v_accvgpr_read_b32 v202, a58\n" \
"v_mfma_f32_16x16x32_bf16 a[160:163], v[152:155], v[64:67], a[160:163]\n" \
"v_accvgpr_read_b32 v203, a59\n" \
"v_accvgpr_read_b32 v208, a88\n" \
"v_mfma_f32_16x16x32_bf16 a[160:163], v[148:151], v[60:63], a[160:163]\n" \
"v_accvgpr_read_b32 v209, a89\n" \
"v_accvgpr_read_b32 v210, a90\n" \
"v_mfma_f32_16x16x32_bf16 a[164:167], v[156:159], v[60:63], a[164:167]\n" \
"v_accvgpr_read_b32 v211, a91\n" \
"v_accvgpr_read_b32 v228, a120\n" \
"v_mfma_f32_16x16x32_bf16 a[164:167], v[160:163], v[64:67], a[164:167]\n" \
"v_accvgpr_read_b32 v229, a121\n" \
"v_accvgpr_read_b32 v230, a122\n" \
"v_mfma_f32_16x16x32_bf16 a[168:171], v[168:171], v[64:67], a[168:171]\n" \
"v_accvgpr_read_b32 v231, a123\n" \
"v_accvgpr_read_b32 v220, a28\n" \
"v_mfma_f32_16x16x32_bf16 a[168:171], v[164:167], v[60:63], a[168:171]\n" \
"v_accvgpr_read_b32 v221, a29\n" \
"v_accvgpr_read_b32 v222, a30\n" \
"v_mfma_f32_16x16x32_bf16 a[172:175], v[172:175], v[60:63], a[172:175]\n" \
"v_accvgpr_read_b32 v223, a31\n" \
"v_accvgpr_read_b32 v216, a60\n" \
"v_mfma_f32_16x16x32_bf16 a[172:175], v[176:179], v[64:67], a[172:175]\n" \
"v_accvgpr_read_b32 v217, a61\n" \
"v_accvgpr_read_b32 v218, a62\n" \
"v_mfma_f32_16x16x32_bf16 a[192:195], v[152:155], v[72:75], a[192:195]\n" \
"v_accvgpr_read_b32 v219, a63\n" \
"v_accvgpr_read_b32 v224, a92\n" \
"v_mfma_f32_16x16x32_bf16 a[192:195], v[148:151], v[68:71], a[192:195]\n" \
"v_accvgpr_read_b32 v225, a93\n" \
"v_accvgpr_read_b32 v226, a94\n" \
"s_waitcnt vmcnt(3)\n" \
"s_barrier\n" \
"s_mov_b64 exec, 3\n" \
"global_load_lds_dword v45, s[86:87]\n" \
"s_mov_b64 exec, -1\n" \
"s_sub_u32 m0, %[mB], 0x10000\n" \
"v_mfma_f32_16x16x32_bf16 a[196:199], v[156:159], v[68:71], a[196:199]\n" \
"ds_read_b64_tr_b16 v[140:141], v39 offset:0\n" \
"ds_read_b64_tr_b16 v[142:143], v39 offset:960\n" \
"v_mfma_f32_16x16x32_bf16 a[196:199], v[160:163], v[72:75], a[196:199]\n" \
"ds_read_b64_tr_b16 v[128:129], v37 offset:4096\n" \
"ds_read_b64_tr_b16 v[130:131], v37 offset:4608\n" \
"v_mfma_f32_16x16x32_bf16 a[200:203], v[168:171], v[72:75], a[200:203]\n" \
"ds_read_b64_tr_b16 v[124:125], v37 offset:0\n" \
"ds_read_b64_tr_b16 v[126:127], v37 offset:512\n" \
"v_mfma_f32_16x16x32_bf16 a[200:203], v[164:167], v[68:71], a[200:203]\n" \
"ds_read_b64_tr_b16 v[132:133], v38 offset:0\n" \
"ds_read_b64_tr_b16 v[134:135], v38 offset:512\n" \
"v_mfma_f32_16x16x32_bf16 a[204:207], v[172:175], v[68:71], a[204:207]\n" \
"ds_read_b64_tr_b16 v[136:137], v38 offset:4096\n" \
"ds_read_b64_tr_b16 v[138:139], v38 offset:4608\n" \
"v_mfma_f32_16x16x32_bf16 a[204:207], v[176:179], v[72:75], a[204:207]\n" \
"ds_read_b128 v[52:55], v28 offset:0\n" \
"ds_read_b128 v[56:59], v29 offset:0\n" \
"v_mfma_f32_16x16x32_bf16 a[224:227], v[152:155], v[80:83], a[224:227]\n" \
"ds_read_b64_tr_b16 v[116:117], v36 offset:0\n" \
"ds_read_b64_tr_b16 v[118:119], v36 offset:512\n" \
"v_mfma_f32_16x16x32_bf16 a[224:227], v[148:151], v[76:79], a[224:227]\n" \
"ds_read_b64_tr_b16 v[120:121], v36 offset:4096\n" \
"ds_read_b64_tr_b16 v[122:123], v36 offset:4608\n" \
"v_mfma_f32_16x16x32_bf16 a[228:231], v[156:159], v[76:79], a[228:231]\n" \
"ds_read_b128 v[60:63], v28 offset:2048\n" \
"ds_read_b128 v[64:67], v29 offset:2048\n" \
"v_mfma_f32_16x16x32_bf16 a[228:231], v[160:163], v[80:83], a[228:231]\n" \
"v_accvgpr_read_b32 v227, a95\n" \
"v_accvgpr_read_b32 v244, a124\n" \
"v_mfma_f32_16x16x32_bf16 a[232:235], v[168:171], v[80:83], a[232:235]\n" \
"ds_read_b128 v[68:71], v28 offset:4096\n" \
"ds_read_b128 v[72:75], v29 offset:4096\n" \
"v_mfma_f32_16x16x32_bf16 a[232:235], v[164:167], v[76:79], a[232:235]\n" \
"v_accvgpr_read_b32 v245, a125\n" \
"v_accvgpr_read_b32 v246, a126\n" \
"v_mfma_f32_16x16x32_bf16 a[236:239], v[172:175], v[76:79], a[236:239]\n" \
"ds_read_b128 v[76:79], v28 offset:6144\n" \
"v_accvgpr_read_b32 v247, a127\n" \
"v_mfma_f32_16x16x32_bf16 a[236:239], v[176:179], v[80:83], a[236:239]\n" \
"ds_read_b128 v[80:83], v29 offset:6144\n" \
"v_accvgpr_read_b32 v236, a0\n" \
"v_mfma_f32_16x16x32_bf16 a[144:147], v[152:155], v[88:91], a[144:147]\n" \
"v_accvgpr_read_b32 v237, a1\n" \
"v_accvgpr_read_b32 v238, a2\n" \
"v_mfma_f32_16x16x32_bf16 a[144:147], v[148:151], v[84:87], a[144:147]\n" \
"v_accvgpr_read_b32 v239, a3\n" \
"v_accvgpr_read_b32 v232, a4\n" \
"v_mfma_f32_16x16x32_bf16 a[148:151], v[156:159], v[84:87], a[148:151]\n" \
"v_accvgpr_read_b32 v233, a5\n" \
"v_accvgpr_read_b32 v234, a6\n" \
"v_mfma_f32_16x16x32_bf16 a[148:151], v[160:163], v[88:91], a[148:151]\n" \
"v_accvgpr_read_b32 v235, a7\n" \
"v_accvgpr_read_b32 v240, a32\n" \
"v_mfma_f32_16x16x32_bf16 a[152:155], v[168:171], v[88:91], a[152:155]\n" \
"v_accvgpr_read_b32 v241, a33\n" \
"v_accvgpr_read_b32 v242, a34\n" \
"v_mfma_f32_16x16x32_bf16 a[152:155], v[164:167], v[84:87], a[152:155]\n" \
"v_accvgpr_read_b32 v243, a35\n" \
"s_mov_b32 s78, -1\n" \
"v_mfma_f32_16x16x32_bf16 a[156:159], v[172:175], v[84:87], a[156:159]\n" \
"ds_read_b128 v[84:87], v22 offset:0\n" \
"s_mov_b32 s79, 0\n" \
"v_mfma_f32_16x16x32_bf16 a[156:159], v[176:179], v[88:91], a[156:159]\n" \
"ds_read_b128 v[88:91], v23 offset:0\n" \
"s_mov_b32 s68, 0xffc0fffc\n" \
"v_mfma_f32_16x16x32_bf16 a[176:179], v[152:155], v[96:99], a[176:179]\n" \
"s_mov_b32 s69, 0xc000fc00\n" \
"v_lshrrev_b32_e32 v24, 9, v20\n" \
"v_mfma_f32_16x16x32_bf16 a[176:179], v[148:151], v[92:95], a[176:179]\n" \
"v_lshrrev_b32_e32 v25, 8, v20\n" \
"v_mul_u32_u24_e32 v24, 0x24800, v24\n" \
"v_mfma_f32_16x16x32_bf16 a[180:183], v[156:159], v[92:95], a[180:183]\n" \
"v_mul_u32_u24_e32 v25, 0x24700, v25\n" \
"v_add_u32_e64 v24, v24, v26\n" \
"v_mfma_f32_16x16x32_bf16 a[180:183], v[160:163], v[96:99], a[180:183]\n" \
"v_add_u32_e32 v25, v25, v20\n" \
"s_add_u32 s85, s50, 0x0\n" \
"v_mfma_f32_16x16x32_bf16 a[184:187], v[168:171], v[96:99], a[184:187]\n" \
"s_add_u32 s87, s50, 0x49000\n" \
"s_mov_b32 s70, 0x3e0293ee\n" \
"v_mfma_f32_16x16x32_bf16 a[184:187], v[164:167], v[92:95], a[184:187]\n" \
"s_mov_b32 s71, 0x3e0293ee\n" \
"v_add_u32_e64 v27, s47, v20\n" \
"v_mfma_f32_16x16x32_bf16 a[188:191], v[172:175], v[92:95], a[188:191]\n" \
"ds_read_b128 v[92:95], v48 offset:2048\n" \
"v_lshrrev_b32_e32 v26, 2, v20\n" \
"v_mfma_f32_16x16x32_bf16 a[188:191], v[176:179], v[96:99], a[188:191]\n" \
"ds_read_b128 v[96:99], v49 offset:2048\n" \
"v_add_u32_e32 v26, s46, v26\n" \
"v_mfma_f32_16x16x32_bf16 a[208:211], v[152:155], v[104:107], a[208:211]\n" \
"v_mfma_f32_16x16x32_bf16 a[208:211], v[148:151], v[100:103], a[208:211]\n" \
"v_mfma_f32_16x16x32_bf16 a[212:215], v[156:159], v[100:103], a[212:215]\n" \
"v_mfma_f32_16x16x32_bf16 a[212:215], v[160:163], v[104:107], a[212:215]\n" \
"v_mfma_f32_16x16x32_bf16 a[216:219], v[168:171], v[104:107], a[216:219]\n" \
"v_mfma_f32_16x16x32_bf16 a[216:219], v[164:167], v[100:103], a[216:219]\n" \
"v_mfma_f32_16x16x32_bf16 a[220:223], v[172:175], v[100:103], a[220:223]\n" \
"ds_read_b128 v[100:103], v48 offset:4096\n" \
"v_mfma_f32_16x16x32_bf16 a[220:223], v[176:179], v[104:107], a[220:223]\n" \
"ds_read_b128 v[104:107], v49 offset:4096\n" \
"v_mfma_f32_16x16x32_bf16 a[240:243], v[152:155], v[112:115], a[240:243]\n" \
"ds_read_b64_tr_b16 v[152:153], v32 offset:8192\n" \
"ds_read_b64_tr_b16 v[154:155], v32 offset:9216\n" \
"v_mfma_f32_16x16x32_bf16 a[240:243], v[148:151], v[108:111], a[240:243]\n" \
"ds_read_b64_tr_b16 v[148:149], v32 offset:0\n" \
"ds_read_b64_tr_b16 v[150:151], v32 offset:1024\n" \
"v_mfma_f32_16x16x32_bf16 a[244:247], v[156:159], v[108:111], a[244:247]\n" \
"ds_read_b64_tr_b16 v[156:157], v33 offset:0\n" \
"ds_read_b64_tr_b16 v[158:159], v33 offset:1024\n" \
"v_mfma_f32_16x16x32_bf16 a[244:247], v[160:163], v[112:115], a[244:247]\n" \
"ds_read_b64_tr_b16 v[160:161], v33 offset:8192\n" \
"ds_read_b64_tr_b16 v[162:163], v33 offset:9216\n" \
"v_mfma_f32_16x16x32_bf16 a[248:251], v[168:171], v[112:115], a[248:251]\n" \
"ds_read_b64_tr_b16 v[168:169], v34 offset:8192\n" \
"ds_read_b64_tr_b16 v[170:171], v34 offset:9216\n" \
"v_mfma_f32_16x16x32_bf16 a[248:251], v[164:167], v[108:111], a[248:251]\n" \
"ds_read_b64_tr_b16 v[164:165], v34 offset:0\n" \
"ds_read_b64_tr_b16 v[166:167], v34 offset:1024\n" \
"v_mfma_f32_16x16x32_bf16 a[252:255], v[172:175], v[108:111], a[252:255]\n" \
"ds_read_b64_tr_b16 v[172:173], v35 offset:0\n" \
"ds_read_b64_tr_b16 v[174:175], v35 offset:1024\n" \
"v_mfma_f32_16x16x32_bf16 a[252:255], v[176:179], v[112:115], a[252:255]\n" \
"ds_read_b64_tr_b16 v[176:177], v35 offset:8192\n" \
"s_waitcnt lgkmcnt(15)\n" \
"s_nop 0\n" \
"v_smfmac_f32_16x16x64_bf16 v[248:251], v[140:143], v[84:91], v50\n" \
"v_mfma_f32_16x16x32_bf16 v[180:183], v[128:131], v[88:91], v[180:183]\n" \
"ds_read_b64_tr_b16 v[178:179], v35 offset:9216\n" \
"v_bfe_u32 v44, v20, 2, 5\n" \
"v_mfma_f32_16x16x32_bf16 v[180:183], v[124:127], v[84:87], v[180:183]\n" \
"buffer_load_dword v44, v44, s[80:83], s39 offen offset:0 sc1\n" \
"v_mfma_f32_16x16x32_bf16 v[252:255], v[84:87], v[132:135], v[252:255]\n" \
"ds_read_b128 v[84:87], v48 offset:0\n" \
"ds_read_b128 v[108:111], v48 offset:6144\n" \
"v_mfma_f32_16x16x32_bf16 v[252:255], v[88:91], v[136:139], v[252:255]\n" \
"ds_read_b128 v[88:91], v49 offset:0\n" \
"ds_read_b128 v[112:115], v49 offset:6144\n" \
"v_smfmac_f32_16x16x64_bf16 v[196:199], v[140:143], v[52:59], v50\n" \
"v_mfma_f32_16x16x32_bf16 v[188:191], v[128:131], v[56:59], v[188:191]\n" \
"v_cvt_pk_bf16_f32 v180, v180, v181\n" \
"v_cvt_pk_bf16_f32 v181, v182, v183\n" \
"v_mfma_f32_16x16x32_bf16 v[188:191], v[124:127], v[52:55], v[188:191]\n" \
"v_add_f32_e64 v249, v249, v251\n" \
"v_cvt_pk_bf16_f32 v182, v249, 0\n" \
"v_mfma_f32_16x16x32_bf16 v[184:187], v[116:119], v[52:55], v[184:187]\n" \
"v_mov_b32_e32 v183, 0x3f80\n" \
"v_add_f32_e64 v196, v196, v198\n" \
"v_mfma_f32_16x16x32_bf16 v[184:187], v[120:123], v[56:59], v[184:187]\n" \
"v_mfma_f32_16x16x16_bf16 v[248:251], v[182:183], v[46:47], 0\n" \
"v_add_f32_e64 v197, v197, v199\n" \
"v_cvt_pk_bf16_f32 v188, v188, v189\n" \
"v_mfma_f32_16x16x32_bf16 v[192:195], v[56:59], v[136:139], v[192:195]\n" \
"v_cvt_pk_bf16_f32 v189, v190, v191\n" \
"v_cvt_pk_bf16_f32 v190, v197, 0\n" \
"v_mfma_f32_16x16x32_bf16 v[192:195], v[52:55], v[132:135], v[192:195]\n" \
"v_mov_b32_e32 v191, 0x3f80\n" \
"v_cvt_pk_bf16_f32 v184, v184, v185\n" \
"v_smfmac_f32_16x16x64_bf16 v[212:215], v[140:143], v[60:67], v50\n" \
"v_mfma_f32_16x16x32_bf16 v[204:207], v[124:127], v[60:63], v[204:207]\n" \
"v_cvt_pk_bf16_f32 v185, v186, v187\n" \
"v_cndmask_b32_e64 v186, 0, v196, s[78:79]\n" \
"v_mfma_f32_16x16x32_bf16 v[204:207], v[128:131], v[64:67], v[204:207]\n" \
"v_mov_b32_e64 v187, 0\n" \
"v_cvt_pk_bf16_f32 v186, v186, 0\n" \
"v_mfma_f32_16x16x32_bf16 v[200:203], v[120:123], v[64:67], v[200:203]\n" \
"v_mfma_f32_16x16x32_bf16 v[200:203], v[116:119], v[60:63], v[200:203]\n" \
"v_add_f32_e32 v212, v212, v214\n" \
"v_add_f32_e32 v213, v213, v215\n" \
"v_mfma_f32_16x16x32_bf16 v[208:211], v[60:63], v[132:135], v[208:211]\n" \
"v_mfma_f32_16x16x32_bf16 v[208:211], v[64:67], v[136:139], v[208:211]\n" \
"v_cvt_pk_bf16_f32 v204, v204, v205\n" \
"v_cvt_pk_bf16_f32 v205, v206, v207\n" \
"v_smfmac_f32_16x16x64_bf16 v[228:231], v[140:143], v[68:75], v50\n" \
"v_mfma_f32_16x16x32_bf16 v[220:223], v[128:131], v[72:75], v[220:223]\n" \
"v_cvt_pk_bf16_f32 v200, v200, v201\n" \
"v_cvt_pk_bf16_f32 v201, v202, v203\n" \
"v_mfma_f32_16x16x32_bf16 v[220:223], v[124:127], v[68:71], v[220:223]\n" \
"v_cndmask_b32_e64 v202, 0, v212, s[78:79]\n" \
"v_cvt_pk_bf16_f32 v206, v213, 0\n" \
"v_mfma_f32_16x16x32_bf16 v[216:219], v[116:119], v[68:71], v[216:219]\n" \
"v_mov_b32_e32 v207, 0x3f80\n" \
"v_add_f32_e64 v228, v228, v230\n" \
"v_mfma_f32_16x16x32_bf16 v[216:219], v[120:123], v[72:75], v[216:219]\n" \
"v_add_f32_e32 v229, v229, v231\n" \
"v_mov_b32_e32 v203, 0\n" \
"v_mfma_f32_16x16x32_bf16 v[224:227], v[72:75], v[136:139], v[224:227]\n" \
"v_cvt_pk_bf16_f32 v220, v220, v221\n" \
"v_cvt_pk_bf16_f32 v221, v222, v223\n" \
"v_mfma_f32_16x16x32_bf16 v[224:227], v[68:71], v[132:135], v[224:227]\n" \
"v_cvt_pk_bf16_f32 v202, v202, 0\n" \
"v_cvt_pk_bf16_f32 v222, v229, 0\n" \
"v_smfmac_f32_16x16x64_bf16 v[244:247], v[140:143], v[76:83], v50\n" \
"v_mfma_f32_16x16x32_bf16 v[236:239], v[124:127], v[76:79], v[236:239]\n" \
"v_cvt_pk_bf16_f32 v216, v216, v217\n" \
"v_cvt_pk_bf16_f32 v217, v218, v219\n" \
"v_mfma_f32_16x16x32_bf16 v[236:239], v[128:131], v[80:83], v[236:239]\n" \
"v_cndmask_b32_e64 v218, 0, v228, s[78:79]\n" \
"v_cvt_pk_bf16_f32 v126, v192, v193\n" \
"v_mfma_f32_16x16x32_bf16 v[232:235], v[120:123], v[80:83], v[232:235]\n" \
"v_cvt_pk_bf16_f32 v127, v194, v195\n" \
"v_add_f32_e64 v244, v244, v246\n" \
"v_mfma_f32_16x16x32_bf16 v[232:235], v[116:119], v[76:79], v[232:235]\n" \
"v_add_f32_e64 v245, v245, v247\n" \
"v_cvt_pk_bf16_f32 v128, v208, v209\n" \
"v_mfma_f32_16x16x32_bf16 v[240:243], v[76:79], v[132:135], v[240:243]\n" \
"v_cvt_pk_bf16_f32 v236, v236, v237\n" \
"v_cvt_pk_bf16_f32 v237, v238, v239\n" \
"v_mfma_f32_16x16x32_bf16 v[240:243], v[80:83], v[136:139], v[240:243]\n" \
"v_cvt_pk_bf16_f32 v129, v210, v211\n" \
"v_mov_b32_e32 v223, 0x3f80\n" \
"s_waitcnt lgkmcnt(0)\n" \
"s_barrier\n" \
"v_mfma_f32_16x16x32_bf16 a[128:131], v[152:155], v[56:59], a[128:131]\n" \
"v_cvt_pk_bf16_f32 v232, v232, v233\n" \
"v_cvt_pk_bf16_f32 v233, v234, v235\n" \
"v_mfma_f32_16x16x32_bf16 a[128:131], v[148:151], v[52:55], a[128:131]\n" \
"v_cndmask_b32_e64 v234, 0, v244, s[78:79]\n" \
"v_mov_b32_e64 v219, 0\n" \
"v_mfma_f32_16x16x32_bf16 a[132:135], v[156:159], v[52:55], a[132:135]\n" \
"v_cvt_pk_bf16_f32 v218, v218, 0\n" \
"v_cvt_pk_bf16_f32 v130, v224, v225\n" \
"v_mfma_f32_16x16x32_bf16 a[132:135], v[160:163], v[56:59], a[132:135]\n" \
"v_cvt_pk_bf16_f32 v131, v226, v227\n" \
"v_cvt_pk_bf16_f32 v238, v245, 0\n" \
"v_mfma_f32_16x16x32_bf16 a[136:139], v[168:171], v[56:59], a[136:139]\n" \
"v_mov_b32_e32 v239, 0x3f80\n" \
"v_mov_b32_e64 v235, 0\n" \
"v_mfma_f32_16x16x32_bf16 a[136:139], v[164:167], v[52:55], a[136:139]\n" \
"v_cvt_pk_bf16_f32 v234, v234, 0\n" \
"v_cvt_pk_bf16_f32 v132, v240, v241\n" \
"v_mfma_f32_16x16x32_bf16 a[140:143], v[172:175], v[52:55], a[140:143]\n" \
"v_cvt_pk_bf16_f32 v133, v242, v243\n" \
"v_cvt_pk_bf16_f32 v134, v248, v249\n" \
"v_mfma_f32_16x16x32_bf16 a[140:143], v[176:179], v[56:59], a[140:143]\n" \
"v_mfma_f32_16x16x32_bf16 v[116:119], v[180:183], v[184:187], 0\n" \
"v_mfma_f32_16x16x32_bf16 v[192:195], v[188:191], v[184:187], 0\n" \
"v_mfma_f32_16x16x32_bf16 v[208:211], v[204:207], v[200:203], 0\n" \
"v_mfma_f32_16x16x32_bf16 v[120:123], v[188:191], v[200:203], 0\n" \
"s_waitcnt vmcnt(0)\n" \
"v_cmp_ne_u32_e32 vcc, %[tag], v44\n" \
"v_mfma_f32_16x16x32_bf16 a[160:163], v[152:155], v[64:67], a[160:163]\n" \
"v_mfma_f32_16x16x32_bf16 v[224:227], v[220:223], v[216:219], 0\n" \
"v_mfma_f32_16x16x32_bf16 v[36:39], v[204:207], v[216:219], 0\n" \
"v_mfma_f32_16x16x32_bf16 v[240:243], v[236:239], v[232:235], 0\n" \
"v_mfma_f32_16x16x32_bf16 v[48:51], v[220:223], v[232:235], 0\n" \
"s_cbranch_vccnz L_pa_%=\n" \
"L_ra_%=:\n" \
"v_cndmask_b32_e64 v116, v116, v117, s[64:65]\n" \
"buffer_load_dwordx4 v20, s[80:83], s73 offen offset:0 sc1 lds\n" \
"v_mfma_f32_16x16x32_bf16 a[160:163], v[148:151], v[60:63], a[160:163]\n" \
"v_cndmask_b32_e64 v118, v118, v119, s[64:65]\n" \
"v_cndmask_b32_e64 v192, v192, v193, s[64:65]\n" \
"v_mfma_f32_16x16x32_bf16 a[164:167], v[156:159], v[60:63], a[164:167]\n" \
"v_add_f32_e32 v116, s49, v116\n" \
"v_cndmask_b32_e64 v194, v194, v195, s[64:65]\n" \
"v_mfma_f32_16x16x32_bf16 a[164:167], v[160:163], v[64:67], a[164:167]\n" \
"v_add_f32_e64 v118, s49, v118\n" \
"v_cndmask_b32_e64 v208, v208, v209, s[64:65]\n" \
"v_mfma_f32_16x16x32_bf16 a[168:171], v[168:171], v[64:67], a[168:171]\n" \
"buffer_load_dwordx4 v20, s[80:83], s73 offen offset:1024 sc1 lds\n" \
"v_mfma_f32_16x16x32_bf16 a[168:171], v[164:167], v[60:63], a[168:171]\n" \
"v_cndmask_b32_e64 v120, v120, v121, s[64:65]\n" \
"v_cndmask_b32_e64 v210, v210, v211, s[64:65]\n" \
"v_mfma_f32_16x16x32_bf16 a[172:175], v[172:175], v[60:63], a[172:175]\n" \
"v_cndmask_b32_e64 v122, v122, v123, s[64:65]\n" \
"v_cndmask_b32_e64 v224, v224, v225, s[64:65]\n" \
"v_mfma_f32_16x16x32_bf16 a[172:175], v[176:179], v[64:67], a[172:175]\n" \
"v_cndmask_b32_e64 v36, v36, v37, s[64:65]\n" \
"v_cndmask_b32_e64 v226, v226, v227, s[64:65]\n" \
"v_mfma_f32_16x16x32_bf16 a[192:195], v[152:155], v[72:75], a[192:195]\n" \
"buffer_load_dwordx4 v20, s[80:83], s73 offen offset:2048 sc1 lds\n" \
"v_mfma_f32_16x16x32_bf16 a[192:195], v[148:151], v[68:71], a[192:195]\n" \
"v_cndmask_b32_e64 v38, v38, v39, s[64:65]\n" \
"v_cndmask_b32_e64 v240, v240, v241, s[64:65]\n" \
"v_mfma_f32_16x16x32_bf16 a[196:199], v[156:159], v[68:71], a[196:199]\n" \
"v_cndmask_b32_e64 v48, v48, v49, s[64:65]\n" \
"v_cndmask_b32_e64 v242, v242, v243, s[64:65]\n" \
"v_mfma_f32_16x16x32_bf16 a[196:199], v[160:163], v[72:75], a[196:199]\n" \
"v_cndmask_b32_e64 v50, v50, v51, s[64:65]\n" \
"v_cndmask_b32_e64 v192, v116, v192, s[66:67]\n" \
"v_mfma_f32_16x16x32_bf16 a[200:203], v[168:171], v[72:75], a[200:203]\n" \
"buffer_load_dwordx4 v20, s[80:83], s73 offen offset:3072 sc1 lds\n" \
"v_mfma_f32_16x16x32_bf16 a[200:203], v[164:167], v[68:71], a[200:203]\n" \
"s_sub_u32 m0, %[mB], s53\n" \
"s_nop 0\n" \
"v_cndmask_b32_e64 v194, v118, v194, s[68:69]\n" \
"v_mfma_f32_16x16x32_bf16 a[204:207], v[172:175], v[68:71], a[204:207]\n" \
"v_cndmask_b32_e64 v208, v120, v208, s[66:67]\n" \
"v_cndmask_b32_e64 v210, v122, v210, s[68:69]\n" \
"v_mfma_f32_16x16x32_bf16 a[204:207], v[176:179], v[72:75], a[204:207]\n" \
"v_cndmask_b32_e64 v224, v36, v224, s[66:67]\n" \
"v_cndmask_b32_e64 v226, v38, v226, s[68:69]\n" \
"v_mfma_f32_16x16x32_bf16 a[224:227], v[152:155], v[80:83], a[224:227]\n" \
"buffer_load_dwordx4 v20, s[80:83], s74 offen offset:0 sc1 lds\n" \
"v_mfma_f32_16x16x32_bf16 a[224:227], v[148:151], v[76:79], a[224:227]\n" \
"v_cndmask_b32_e64 v240, v48, v240, s[66:67]\n" \
"v_cndmask_b32_e64 v242, v50, v242, s[68:69]\n" \
"v_mfma_f32_16x16x32_bf16 a[228:231], v[156:159], v[76:79], a[228:231]\n" \
"v_max_f32_e32 v193, v192, v194\n" \
"v_max_f32_e32 v209, v208, v210\n" \
"v_mfma_f32_16x16x32_bf16 a[228:231], v[160:163], v[80:83], a[228:231]\n" \
"v_max_f32_e32 v225, v224, v226\n" \
"v_max_f32_e32 v241, v240, v242\n" \
"v_mfma_f32_16x16x32_bf16 a[232:235], v[168:171], v[80:83], a[232:235]\n" \
"buffer_load_dwordx4 v20, s[80:83], s74 offen offset:1024 sc1 lds\n" \
"v_mfma_f32_16x16x32_bf16 a[232:235], v[164:167], v[76:79], a[232:235]\n" \
"v_permlane32_swap_b32_e32 v193, v225\n" \
"v_permlane32_swap_b32_e32 v209, v241\n" \
"v_mfma_f32_16x16x32_bf16 a[236:239], v[172:175], v[76:79], a[236:239]\n" \
"v_max_f32_e32 v193, v193, v225\n" \
"v_max_f32_e32 v209, v209, v241\n" \
"v_permlane32_swap_b32_e32 v196, v228\n" \
"v_permlane32_swap_b32_e32 v212, v244\n" \
"buffer_load_dwordx4 v20, s[80:83], s74 offen offset:2048 sc1 lds\n" \
"v_permlane16_swap_b32_e32 v193, v209\n" \
"v_permlane16_swap_b32_e32 v228, v244\n" \
"v_mfma_f32_16x16x32_bf16 a[236:239], v[176:179], v[80:83], a[236:239]\n" \
"v_max_f32_e32 v193, v193, v209\n" \
"v_mul_f32_e32 v193, s72, v193\n" \
"v_mfma_f32_16x16x32_bf16 a[144:147], v[152:155], v[88:91], a[144:147]\n" \
"v_mul_f32_e32 v45, 0xbfb8aa3b, v228\n" \
"v_mov_b32_e64 v209, v193\n" \
"v_mfma_f32_16x16x16_bf16 v[196:199], v[190:191], v[46:47], 0\n" \
"buffer_load_dwordx4 v20, s[80:83], s74 offen offset:3072 sc1 lds\n" \
"v_mfma_f32_16x16x32_bf16 a[144:147], v[148:151], v[84:87], a[144:147]\n" \
"s_sub_u32 m0, %[mB], 0x4000\n" \
"v_cvt_pk_bf16_f32 v135, v250, v251\n" \
"v_mfma_f32_16x16x16_bf16 v[212:215], v[206:207], v[46:47], 0\n" \
"v_mfma_f32_16x16x16_bf16 v[228:231], v[222:223], v[46:47], 0\n" \
"v_permlane16_swap_b32_e32 v193, v209\n" \
"v_mfma_f32_16x16x16_bf16 v[244:247], v[238:239], v[46:47], 0\n" \
"buffer_load_dwordx4 v20, s[80:83], s75 offen offset:0 sc1 lds\n" \
"v_mfma_f32_16x16x32_bf16 a[148:151], v[156:159], v[84:87], a[148:151]\n" \
"v_mov_b32_e32 v225, v193\n" \
"v_mov_b32_e64 v241, v209\n" \
"v_mfma_f32_16x16x32_bf16 a[148:151], v[160:163], v[88:91], a[148:151]\n" \
"v_permlane32_swap_b32_e32 v193, v225\n" \
"v_permlane32_swap_b32_e32 v209, v241\n" \
"v_mfma_f32_16x16x32_bf16 a[152:155], v[168:171], v[88:91], a[152:155]\n" \
"v_fma_f32 v192, v192, s72, -v193\n" \
"v_fma_f32 v194, v194, s72, -v193\n" \
"buffer_load_dwordx4 v20, s[80:83], s75 offen offset:1024 sc1 lds\n" \
"v_mfma_f32_16x16x32_bf16 a[152:155], v[164:167], v[84:87], a[152:155]\n" \
"v_fma_f32 v208, v208, s72, -v209\n" \
"v_fma_f32 v210, v210, s72, -v209\n" \
"v_exp_f32_e32 v192, v192\n" \
"v_exp_f32_e32 v194, v194\n" \
"v_exp_f32_e32 v208, v208\n" \
"v_exp_f32_e32 v210, v210\n" \
"v_mfma_f32_16x16x32_bf16 a[156:159], v[172:175], v[84:87], a[156:159]\n" \
"buffer_load_dwordx4 v20, s[80:83], s75 offen offset:2048 sc1 lds\n" \
"v_mfma_f32_16x16x32_bf16 a[156:159], v[176:179], v[88:91], a[156:159]\n" \
"v_fma_f32 v224, v224, s72, -v225\n" \
"v_fma_f32 v226, v226, s72, -v225\n" \
"v_exp_f32_e32 v224, v224\n" \
"v_exp_f32_e32 v226, v226\n" \
"v_mfma_f32_16x16x32_bf16 a[176:179], v[152:155], v[96:99], a[176:179]\n" \
"v_fma_f32 v240, v240, s72, -v241\n" \
"v_fma_f32 v242, v242, s72, -v241\n" \
"v_mfma_f32_16x16x32_bf16 a[176:179], v[148:151], v[92:95], a[176:179]\n" \
"buffer_load_dwordx4 v20, s[80:83], s75 offen offset:3072 sc1 lds\n" \
"v_mfma_f32_16x16x32_bf16 a[180:183], v[156:159], v[92:95], a[180:183]\n" \
"s_mov_b32 m0, s55\n" \
"v_cvt_pk_bf16_f32 v192, v192, v194\n" \
"v_mfma_f32_16x16x32_bf16 a[180:183], v[160:163], v[96:99], a[180:183]\n" \
"v_cvt_pk_bf16_f32 v208, v208, v210\n" \
"v_cvt_pk_bf16_f32 v136, v196, v197\n" \
"v_exp_f32_e32 v240, v240\n" \
"v_exp_f32_e64 v242, v242\n" \
"v_mfma_f32_16x16x32_bf16 a[184:187], v[168:171], v[96:99], a[184:187]\n" \
"buffer_load_dwordx4 v20, s[80:83], s76 offen offset:0 sc1 lds\n" \
"v_mfma_f32_16x16x32_bf16 a[184:187], v[164:167], v[92:95], a[184:187]\n" \
"v_cvt_pk_bf16_f32 v137, v198, v199\n" \
"v_cvt_pk_bf16_f32 v138, v212, v213\n" \
"v_mfma_f32_16x16x32_bf16 a[188:191], v[172:175], v[92:95], a[188:191]\n" \
"v_cvt_pk_bf16_f32 v139, v214, v215\n" \
"v_perm_b32 v184, v192, v192, v40\n" \
"v_mfma_f32_16x16x32_bf16 a[188:191], v[176:179], v[96:99], a[188:191]\n" \
"v_perm_b32 v185, v192, v192, v41\n" \
"v_perm_b32 v186, v192, v192, v42\n" \
"v_perm_b32 v187, v192, v192, v43\n" \
"buffer_load_dwordx4 v20, s[80:83], s76 offen offset:1024 sc1 lds\n" \
"v_perm_b32 v200, v208, v208, v40\n" \
"L_pf9:\n" \
"s_cbranch_execz L_pf13\n" \
"s_nop 0\n" \
"v_perm_b32 v201, v208, v208, v41\n" \
"v_perm_b32 v202, v208, v208, v42\n" \
"v_perm_b32 v203, v208, v208, v43\n" \
"v_cvt_pk_bf16_f32 v224, v224, v226\n" \
"v_cvt_pk_bf16_f32 v140, v228, v229\n" \
"v_cvt_pk_bf16_f32 v141, v230, v231\n" \
"v_mfma_f32_16x16x32_bf16 v[116:119], v[134:137], v[184:187], 0\n" \
"v_mfma_f32_16x16x32_bf16 v[120:123], v[136:139], v[200:203], 0\n" \
"v_perm_b32 v216, v224, v224, v40\n" \
"v_perm_b32 v217, v224, v224, v41\n" \
"v_perm_b32 v218, v224, v224, v42\n" \
"v_perm_b32 v219, v224, v224, v43\n" \
"v_cvt_pk_bf16_f32 v240, v240, v242\n" \
"v_cvt_pk_bf16_f32 v142, v244, v245\n" \
"v_cvt_pk_bf16_f32 v143, v246, v247\n" \
"v_mfma_f32_16x16x32_bf16 v[36:39], v[138:141], v[216:219], 0\n" \
"v_perm_b32 v232, v240, v240, v40\n" \
"v_perm_b32 v233, v240, v240, v41\n" \
"v_perm_b32 v234, v240, v240, v42\n" \
"v_perm_b32 v235, v240, v240, v43\n" \
"v_exp_f32_e64 v45, v45\n" \
"v_mov_b32_dpp v118, v122 quad_perm:[0,1,2,3] row_mask:0x2 bank_mask:0xf\n" \
"v_mfma_f32_16x16x32_bf16 v[48:51], v[140:143], v[232:235], 0\n" \
"v_cvt_pk_bf16_f32 v124, v252, v253\n" \
"v_cvt_pk_bf16_f32 v125, v254, v255\n" \
"v_mov_b32_dpp v118, v38 quad_perm:[0,1,2,3] row_mask:0x4 bank_mask:0xf\n" \
"v_add_f32_e64 v45, 1.0, v45\n" \
"v_mfma_f32_16x16x32_bf16 v[192:195], v[124:127], v[184:187], 0\n" \
"v_mfma_f32_16x16x32_bf16 v[224:227], v[128:131], v[216:219], 0\n" \
"v_rcp_f32_e64 v45, v45\n" \
"v_mov_b32_dpp v116, v120 quad_perm:[0,1,2,3] row_mask:0x2 bank_mask:0xf\n" \
"v_mov_b32_dpp v118, v50 quad_perm:[0,1,2,3] row_mask:0x8 bank_mask:0xf\n" \
"v_mov_b32_dpp v117, v121 quad_perm:[0,1,2,3] row_mask:0x2 bank_mask:0xf\n" \
"v_rcp_f32_e64 v118, v118\n" \
"v_mfma_f32_16x16x32_bf16 v[208:211], v[126:129], v[200:203], 0\n" \
"v_mfma_f32_16x16x32_bf16 v[240:243], v[130:133], v[232:235], 0\n" \
"v_mov_b32_dpp v116, v36 quad_perm:[0,1,2,3] row_mask:0x4 bank_mask:0xf\n" \
"v_mov_b32_dpp v117, v37 quad_perm:[0,1,2,3] row_mask:0x4 bank_mask:0xf\n" \
"v_mul_f32_e64 v118, v45, v118\n" \
"v_mov_b32_dpp v116, v48 quad_perm:[0,1,2,3] row_mask:0x8 bank_mask:0xf\n" \
"v_mov_b32_dpp v117, v49 quad_perm:[0,1,2,3] row_mask:0x8 bank_mask:0xf\n" \
"v_mul_f32_e32 v116, v118, v116\n" \
"v_mul_f32_e32 v117, v118, v117\n" \
"buffer_store_dwordx4 a[12:15], v20, s[80:83], s89 offen offset:1024 sc1\n" \
"s_add_u32 s89, s45, 0x0\n" \
"v_mov_b32_e32 v122, v118\n" \
"v_cvt_pk_bf16_f32 v116, v116, v117\n" \
"v_accvgpr_read_b32 v52, a128\n" \
"v_permlane16_swap_b32_e32 v118, v122\n" \
"v_mov_b32_e32 v38, v118\n" \
"v_mov_b32_e64 v50, v122\n" \
"v_accvgpr_read_b32 v53, a129\n" \
"v_permlane32_swap_b32_e32 v118, v38\n" \
"v_permlane32_swap_b32_e32 v122, v50\n" \
"v_mul_f32_e32 v192, v118, v192\n" \
"v_mul_f32_e32 v193, v118, v193\n" \
"v_mul_f32_e32 v224, v38, v224\n" \
"v_mul_f32_e32 v225, v38, v225\n" \
"v_mul_f32_e32 v194, v118, v194\n" \
"v_mul_f32_e32 v195, v118, v195\n" \
"v_cvt_pk_bf16_f32 v192, v192, v193\n" \
"v_mul_f32_e32 v226, v38, v226\n" \
"v_mul_f32_e32 v227, v38, v227\n" \
"v_cvt_pk_bf16_f32 v224, v224, v225\n" \
"v_cvt_pk_bf16_f32 v193, v194, v195\n" \
"v_mul_f32_e32 v208, v122, v208\n" \
"v_mul_f32_e32 v209, v122, v209\n" \
"v_mul_f32_e32 v210, v122, v210\n" \
"v_mul_f32_e32 v211, v122, v211\n" \
"v_cvt_pk_bf16_f32 v225, v226, v227\n" \
"v_mul_f32_e32 v240, v50, v240\n" \
"v_mul_f32_e32 v241, v50, v241\n" \
"v_mul_f32_e32 v242, v50, v242\n" \
"v_mul_f32_e32 v243, v50, v243\n" \
"v_cvt_pk_bf16_f32 v194, v208, v209\n" \
"v_cvt_pk_bf16_f32 v195, v210, v211\n" \
"v_cvt_pk_bf16_f32 v226, v240, v241\n" \
"v_cvt_pk_bf16_f32 v227, v242, v243\n" \
"v_permlane32_swap_b32_e32 v192, v194\n" \
"v_permlane32_swap_b32_e32 v193, v195\n" \
"v_permlane32_swap_b32_e32 v224, v226\n" \
"v_permlane32_swap_b32_e32 v225, v227\n" \
"v_accvgpr_read_b32 v54, a130\n" \
"v_accvgpr_read_b32 v55, a131\n" \
"v_pk_mul_f32 v[52:53], v[52:53], s[70:71]\n" \
"v_accvgpr_read_b32 v56, a132\n" \
"v_accvgpr_read_b32 v57, a133\n" \
"v_accvgpr_read_b32 v58, a134\n" \
"v_accvgpr_read_b32 v59, a135\n" \
"v_pk_mul_f32 v[54:55], v[54:55], s[70:71]\n" \
"v_cvt_pk_bf16_f32 v52, v52, v53\n" \
"v_pk_mul_f32 v[56:57], v[56:57], s[70:71]\n" \
"v_pk_mul_f32 v[58:59], v[58:59], s[70:71]\n" \
"v_cvt_pk_bf16_f32 v53, v54, v55\n" \
"v_cvt_pk_bf16_f32 v54, v56, v57\n" \
"v_cvt_pk_bf16_f32 v55, v58, v59\n" \
"ds_write_b128 v27, v[52:55] offset:0\n" \
"buffer_store_dwordx4 a[40:43], v20, s[80:83], s88 offen offset:0 sc1\n" \
"s_add_u32 s88, s45, 0x0\n" \
"s_nop 0\n" \
"buffer_store_dword v116, v25, s[80:83], s51 offen sc1\n" \
"buffer_store_dwordx4 v[192:195], v24, s[80:83], s85 offen sc1\n" \
"buffer_store_dwordx4 v[224:227], v24, s[80:83], s87 offen sc1\n" \
"v_accvgpr_read_b32 v76, a224\n" \
"v_accvgpr_read_b32 v77, a225\n" \
"v_accvgpr_read_b32 v78, a226\n" \
"v_accvgpr_read_b32 v79, a227\n" \
"v_pk_mul_f32 v[76:77], v[76:77], s[70:71]\n" \
"v_accvgpr_read_b32 v52, a136\n" \
"v_accvgpr_read_b32 v53, a137\n" \
"v_accvgpr_read_b32 v80, a228\n" \
"v_accvgpr_read_b32 v81, a229\n" \
"v_accvgpr_read_b32 v82, a230\n" \
"v_accvgpr_read_b32 v83, a231\n" \
"v_pk_mul_f32 v[78:79], v[78:79], s[70:71]\n" \
"v_cvt_pk_bf16_f32 v76, v76, v77\n" \
"v_accvgpr_read_b32 v54, a138\n" \
"v_accvgpr_read_b32 v55, a139\n" \
"v_pk_mul_f32 v[52:53], v[52:53], s[70:71]\n" \
"v_pk_mul_f32 v[80:81], v[80:81], s[70:71]\n" \
"v_pk_mul_f32 v[82:83], v[82:83], s[70:71]\n" \
"v_cvt_pk_bf16_f32 v77, v78, v79\n" \
"v_accvgpr_read_b32 v56, a140\n" \
"v_accvgpr_read_b32 v57, a141\n" \
"v_accvgpr_read_b32 v58, a142\n" \
"v_accvgpr_read_b32 v59, a143\n" \
"v_pk_mul_f32 v[54:55], v[54:55], s[70:71]\n" \
"v_cvt_pk_bf16_f32 v52, v52, v53\n" \
"v_cvt_pk_bf16_f32 v78, v80, v81\n" \
"v_cvt_pk_bf16_f32 v79, v82, v83\n" \
"ds_write_b128 v27, v[76:79] offset:12288\n" \
"v_pk_mul_f32 v[56:57], v[56:57], s[70:71]\n" \
"v_pk_mul_f32 v[58:59], v[58:59], s[70:71]\n" \
"v_cvt_pk_bf16_f32 v53, v54, v55\n" \
"v_cvt_pk_bf16_f32 v54, v56, v57\n" \
"v_cvt_pk_bf16_f32 v55, v58, v59\n" \
"ds_write_b128 v27, v[52:55] offset:1024\n" \
"v_accvgpr_read_b32 v76, a208\n" \
"v_accvgpr_read_b32 v77, a209\n" \
"v_accvgpr_read_b32 v78, a210\n" \
"v_accvgpr_read_b32 v79, a211\n" \
"v_accvgpr_read_b32 v52, a168\n" \
"v_accvgpr_read_b32 v53, a169\n" \
"v_mfma_f32_16x16x32_bf16 v[76:79], v[152:155], v[104:107], v[76:79]\n" \
"v_accvgpr_read_b32 v54, a170\n" \
"v_accvgpr_read_b32 v55, a171\n" \
"v_mfma_f32_16x16x32_bf16 v[76:79], v[148:151], v[100:103], v[76:79]\n" \
"v_accvgpr_read_b32 v56, a172\n" \
"v_accvgpr_read_b32 v57, a173\n" \
"v_accvgpr_read_b32 v58, a174\n" \
"v_accvgpr_read_b32 v59, a175\n" \
"v_pk_mul_f32 v[52:53], v[52:53], s[70:71]\n" \
"v_pk_mul_f32 v[54:55], v[54:55], s[70:71]\n" \
"v_cvt_pk_bf16_f32 v52, v52, v53\n" \
"v_pk_mul_f32 v[56:57], v[56:57], s[70:71]\n" \
"v_pk_mul_f32 v[58:59], v[58:59], s[70:71]\n" \
"buffer_store_dwordx4 a[44:47], v20, s[80:83], s89 offen offset:1024 sc1\n" \
"s_add_u32 s89, s45, 0x0\n" \
"s_nop 0\n" \
"v_cvt_pk_bf16_f32 v53, v54, v55\n" \
"v_cvt_pk_bf16_f32 v54, v56, v57\n" \
"v_cvt_pk_bf16_f32 v55, v58, v59\n" \
"ds_write_b128 v27, v[52:55] offset:5120\n" \
"v_accvgpr_read_b32 v80, a212\n" \
"v_accvgpr_read_b32 v81, a213\n" \
"v_accvgpr_read_b32 v82, a214\n" \
"v_accvgpr_read_b32 v83, a215\n" \
"v_accvgpr_read_b32 v52, a232\n" \
"v_accvgpr_read_b32 v53, a233\n" \
"v_mfma_f32_16x16x32_bf16 v[80:83], v[156:159], v[100:103], v[80:83]\n" \
"v_accvgpr_read_b32 v54, a234\n" \
"v_accvgpr_read_b32 v55, a235\n" \
"v_mfma_f32_16x16x32_bf16 v[80:83], v[160:163], v[104:107], v[80:83]\n" \
"v_accvgpr_read_b32 v56, a236\n" \
"v_accvgpr_read_b32 v57, a237\n" \
"v_accvgpr_read_b32 v58, a238\n" \
"v_accvgpr_read_b32 v59, a239\n" \
"v_pk_mul_f32 v[52:53], v[52:53], s[70:71]\n" \
"v_pk_mul_f32 v[54:55], v[54:55], s[70:71]\n" \
"v_cvt_pk_bf16_f32 v52, v52, v53\n" \
"v_pk_mul_f32 v[56:57], v[56:57], s[70:71]\n" \
"v_pk_mul_f32 v[58:59], v[58:59], s[70:71]\n" \
"v_cvt_pk_bf16_f32 v53, v54, v55\n" \
"v_cvt_pk_bf16_f32 v54, v56, v57\n" \
"v_cvt_pk_bf16_f32 v55, v58, v59\n" \
"ds_write_b128 v27, v[52:55] offset:13312\n" \
"v_accvgpr_read_b32 v52, a216\n" \
"v_accvgpr_read_b32 v53, a217\n" \
"v_accvgpr_read_b32 v54, a218\n" \
"v_accvgpr_read_b32 v55, a219\n" \
"v_accvgpr_read_b32 v60, a160\n" \
"v_accvgpr_read_b32 v61, a161\n" \
"v_mfma_f32_16x16x32_bf16 v[52:55], v[168:171], v[104:107], v[52:55]\n" \
"v_accvgpr_read_b32 v62, a162\n" \
"v_accvgpr_read_b32 v63, a163\n" \
"v_mfma_f32_16x16x32_bf16 v[52:55], v[164:167], v[100:103], v[52:55]\n" \
"v_accvgpr_read_b32 v56, a220\n" \
"v_accvgpr_read_b32 v57, a221\n" \
"v_accvgpr_read_b32 v58, a222\n" \
"v_accvgpr_read_b32 v59, a223\n" \
"v_pk_mul_f32 v[60:61], v[60:61], s[70:71]\n" \
"v_accvgpr_read_b32 v64, a164\n" \
"v_mfma_f32_16x16x32_bf16 v[56:59], v[172:175], v[100:103], v[56:59]\n" \
"v_accvgpr_read_b32 v65, a165\n" \
"v_accvgpr_read_b32 v66, a166\n" \
"v_mfma_f32_16x16x32_bf16 v[56:59], v[176:179], v[104:107], v[56:59]\n" \
"v_accvgpr_read_b32 v67, a167\n" \
"buffer_store_dwordx4 a[72:75], v20, s[80:83], s88 offen offset:2048 sc1\n" \
"s_add_u32 s88, s45, 0x1000\n" \
"v_pk_mul_f32 v[62:63], v[62:63], s[70:71]\n" \
"v_cvt_pk_bf16_f32 v60, v60, v61\n" \
"v_pk_mul_f32 v[64:65], v[64:65], s[70:71]\n" \
"v_pk_mul_f32 v[66:67], v[66:67], s[70:71]\n" \
"v_cvt_pk_bf16_f32 v61, v62, v63\n" \
"v_cvt_pk_bf16_f32 v62, v64, v65\n" \
"v_cvt_pk_bf16_f32 v63, v66, v67\n" \
"ds_write_b128 v27, v[60:63] offset:4096\n" \
"v_accvgpr_read_b32 v68, a192\n" \
"v_accvgpr_read_b32 v69, a193\n" \
"v_accvgpr_read_b32 v60, a200\n" \
"v_accvgpr_read_b32 v61, a201\n" \
"v_accvgpr_read_b32 v70, a194\n" \
"v_accvgpr_read_b32 v71, a195\n" \
"v_pk_mul_f32 v[68:69], v[68:69], s[70:71]\n" \
"v_accvgpr_read_b32 v62, a202\n" \
"v_accvgpr_read_b32 v63, a203\n" \
"v_pk_mul_f32 v[60:61], v[60:61], s[70:71]\n" \
"v_accvgpr_read_b32 v72, a196\n" \
"v_accvgpr_read_b32 v73, a197\n" \
"v_accvgpr_read_b32 v74, a198\n" \
"v_accvgpr_read_b32 v75, a199\n" \
"v_pk_mul_f32 v[70:71], v[70:71], s[70:71]\n" \
"v_cvt_pk_bf16_f32 v68, v68, v69\n" \
"v_accvgpr_read_b32 v64, a204\n" \
"v_accvgpr_read_b32 v65, a205\n" \
"v_accvgpr_read_b32 v66, a206\n" \
"v_accvgpr_read_b32 v67, a207\n" \
"v_pk_mul_f32 v[62:63], v[62:63], s[70:71]\n" \
"v_cvt_pk_bf16_f32 v60, v60, v61\n" \
"v_accvgpr_read_b32 v100, a240\n" \
"v_accvgpr_read_b32 v101, a241\n" \
"v_accvgpr_read_b32 v102, a242\n" \
"v_accvgpr_read_b32 v103, a243\n" \
"v_pk_mul_f32 v[72:73], v[72:73], s[70:71]\n" \
"v_pk_mul_f32 v[74:75], v[74:75], s[70:71]\n" \
"v_mfma_f32_16x16x32_bf16 v[100:103], v[152:155], v[112:115], v[100:103]\n" \
"v_cvt_pk_bf16_f32 v69, v70, v71\n" \
"v_cvt_pk_bf16_f32 v61, v62, v63\n" \
"v_mfma_f32_16x16x32_bf16 v[100:103], v[148:151], v[108:111], v[100:103]\n" \
"v_cvt_pk_bf16_f32 v70, v72, v73\n" \
"v_cvt_pk_bf16_f32 v71, v74, v75\n" \
"ds_write_b128 v27, v[68:71] offset:8192\n" \
"v_pk_mul_f32 v[64:65], v[64:65], s[70:71]\n" \
"v_pk_mul_f32 v[66:67], v[66:67], s[70:71]\n" \
"v_cvt_pk_bf16_f32 v62, v64, v65\n" \
"v_cvt_pk_bf16_f32 v63, v66, v67\n" \
"ds_write_b128 v27, v[60:63] offset:9216\n" \
"v_accvgpr_read_b32 v84, a144\n" \
"buffer_store_dwordx4 a[76:79], v20, s[80:83], s89 offen offset:3072 sc1\n" \
"s_add_u32 s89, s45, 0x1000\n" \
"v_accvgpr_read_b32 v85, a145\n" \
"v_accvgpr_read_b32 v68, a152\n" \
"v_accvgpr_read_b32 v69, a153\n" \
"v_accvgpr_read_b32 v92, a176\n" \
"v_accvgpr_read_b32 v93, a177\n" \
"v_accvgpr_read_b32 v60, a184\n" \
"v_accvgpr_read_b32 v61, a185\n" \
"v_accvgpr_read_b32 v86, a146\n" \
"v_accvgpr_read_b32 v87, a147\n" \
"v_pk_mul_f32 v[84:85], v[84:85], s[70:71]\n" \
"v_accvgpr_read_b32 v70, a154\n" \
"v_accvgpr_read_b32 v71, a155\n" \
"v_pk_mul_f32 v[68:69], v[68:69], s[70:71]\n" \
"v_accvgpr_read_b32 v94, a178\n" \
"v_accvgpr_read_b32 v95, a179\n" \
"v_pk_mul_f32 v[92:93], v[92:93], s[70:71]\n" \
"v_accvgpr_read_b32 v62, a186\n" \
"v_accvgpr_read_b32 v63, a187\n" \
"v_pk_mul_f32 v[60:61], v[60:61], s[70:71]\n" \
"v_pk_mul_f32 v[76:77], v[76:77], s[70:71]\n" \
"v_pk_mul_f32 v[52:53], v[52:53], s[70:71]\n" \
"v_pk_mul_f32 v[100:101], v[100:101], s[70:71]\n" \
"v_accvgpr_read_b32 v88, a148\n" \
"v_accvgpr_read_b32 v89, a149\n" \
"v_accvgpr_read_b32 v90, a150\n" \
"v_accvgpr_read_b32 v91, a151\n" \
"v_pk_mul_f32 v[86:87], v[86:87], s[70:71]\n" \
"v_cvt_pk_bf16_f32 v84, v84, v85\n" \
"v_accvgpr_read_b32 v72, a156\n" \
"v_accvgpr_read_b32 v73, a157\n" \
"v_accvgpr_read_b32 v74, a158\n" \
"v_accvgpr_read_b32 v75, a159\n" \
"v_pk_mul_f32 v[70:71], v[70:71], s[70:71]\n" \
"v_cvt_pk_bf16_f32 v68, v68, v69\n" \
"v_accvgpr_read_b32 v96, a180\n" \
"v_accvgpr_read_b32 v97, a181\n" \
"v_accvgpr_read_b32 v98, a182\n" \
"v_accvgpr_read_b32 v99, a183\n" \
"v_pk_mul_f32 v[94:95], v[94:95], s[70:71]\n" \
"v_cvt_pk_bf16_f32 v92, v92, v93\n" \
"v_accvgpr_read_b32 v64, a188\n" \
"v_accvgpr_read_b32 v65, a189\n" \
"v_accvgpr_read_b32 v66, a190\n" \
"v_accvgpr_read_b32 v67, a191\n" \
"v_pk_mul_f32 v[62:63], v[62:63], s[70:71]\n" \
"v_cvt_pk_bf16_f32 v60, v60, v61\n" \
"v_pk_mul_f32 v[78:79], v[78:79], s[70:71]\n" \
"v_cvt_pk_bf16_f32 v76, v76, v77\n" \
"v_pk_mul_f32 v[54:55], v[54:55], s[70:71]\n" \
"v_cvt_pk_bf16_f32 v52, v52, v53\n" \
"v_pk_mul_f32 v[102:103], v[102:103], s[70:71]\n" \
"v_cvt_pk_bf16_f32 v100, v100, v101\n" \
"v_pk_mul_f32 v[88:89], v[88:89], s[70:71]\n" \
"v_pk_mul_f32 v[90:91], v[90:91], s[70:71]\n" \
"v_cvt_pk_bf16_f32 v85, v86, v87\n" \
"v_pk_mul_f32 v[72:73], v[72:73], s[70:71]\n" \
"v_pk_mul_f32 v[74:75], v[74:75], s[70:71]\n" \
"v_cvt_pk_bf16_f32 v69, v70, v71\n" \
"v_pk_mul_f32 v[96:97], v[96:97], s[70:71]\n" \
"v_pk_mul_f32 v[98:99], v[98:99], s[70:71]\n" \
"buffer_store_dwordx4 a[104:107], v20, s[80:83], s88 offen offset:0 sc1\n" \
"v_cvt_pk_bf16_f32 v93, v94, v95\n" \
"v_pk_mul_f32 v[64:65], v[64:65], s[70:71]\n" \
"v_pk_mul_f32 v[66:67], v[66:67], s[70:71]\n" \
"v_cvt_pk_bf16_f32 v61, v62, v63\n" \
"v_pk_mul_f32 v[80:81], v[80:81], s[70:71]\n" \
"v_pk_mul_f32 v[82:83], v[82:83], s[70:71]\n" \
"v_cvt_pk_bf16_f32 v77, v78, v79\n" \
"v_pk_mul_f32 v[56:57], v[56:57], s[70:71]\n" \
"v_pk_mul_f32 v[58:59], v[58:59], s[70:71]\n" \
"v_cvt_pk_bf16_f32 v53, v54, v55\n" \
"v_accvgpr_read_b32 v104, a244\n" \
"v_accvgpr_read_b32 v105, a245\n" \
"v_accvgpr_read_b32 v106, a246\n" \
"v_accvgpr_read_b32 v107, a247\n" \
"v_cvt_pk_bf16_f32 v101, v102, v103\n" \
"v_cvt_pk_bf16_f32 v86, v88, v89\n" \
"v_mfma_f32_16x16x32_bf16 v[104:107], v[156:159], v[108:111], v[104:107]\n" \
"v_cvt_pk_bf16_f32 v87, v90, v91\n" \
"v_cvt_pk_bf16_f32 v70, v72, v73\n" \
"ds_write_b128 v21, v[84:87] offset:16384\n" \
"v_mfma_f32_16x16x32_bf16 v[104:107], v[160:163], v[112:115], v[104:107]\n" \
"v_cvt_pk_bf16_f32 v71, v74, v75\n" \
"ds_write_b128 v21, v[68:71] offset:17408\n" \
"v_cvt_pk_bf16_f32 v94, v96, v97\n" \
"v_cvt_pk_bf16_f32 v95, v98, v99\n" \
"ds_write_b128 v21, v[92:95] offset:20480\n" \
"v_cvt_pk_bf16_f32 v62, v64, v65\n" \
"v_cvt_pk_bf16_f32 v63, v66, v67\n" \
"ds_write_b128 v21, v[60:63] offset:21504\n" \
"v_pk_mul_f32 v[104:105], v[104:105], s[70:71]\n" \
"v_pk_mul_f32 v[106:107], v[106:107], s[70:71]\n" \
"v_cvt_pk_bf16_f32 v78, v80, v81\n" \
"v_cvt_pk_bf16_f32 v79, v82, v83\n" \
"ds_write_b128 v21, v[76:79] offset:24576\n" \
"v_cvt_pk_bf16_f32 v54, v56, v57\n" \
"v_cvt_pk_bf16_f32 v55, v58, v59\n" \
"ds_write_b128 v21, v[52:55] offset:25600\n" \
"v_cvt_pk_bf16_f32 v102, v104, v105\n" \
"v_cvt_pk_bf16_f32 v103, v106, v107\n" \
"ds_write_b128 v21, v[100:103] offset:28672\n" \
"buffer_store_dwordx4 a[108:111], v20, s[80:83], s89 offen offset:1024 sc1\n" \
"v_accvgpr_read_b32 v84, a248\n" \
"v_accvgpr_read_b32 v85, a249\n" \
"v_accvgpr_read_b32 v86, a250\n" \
"v_accvgpr_read_b32 v87, a251\n" \
"v_accvgpr_read_b32 v88, a252\n" \
"v_accvgpr_read_b32 v89, a253\n" \
"v_mfma_f32_16x16x32_bf16 v[84:87], v[168:171], v[112:115], v[84:87]\n" \
"v_accvgpr_read_b32 v90, a254\n" \
"v_accvgpr_read_b32 v91, a255\n" \
"v_mfma_f32_16x16x32_bf16 v[84:87], v[164:167], v[108:111], v[84:87]\n" \
"v_sub_f32_e32 v44, 1.0, v45\n" \
"v_mfma_f32_16x16x32_bf16 v[88:91], v[172:175], v[108:111], v[88:91]\n" \
"ds_write_b32 v26, v44\n" \
"v_mfma_f32_16x16x32_bf16 v[88:91], v[176:179], v[112:115], v[88:91]\n" \
"s_nop 3\n" \
"v_pk_mul_f32 v[84:85], v[84:85], s[70:71]\n" \
"v_pk_mul_f32 v[86:87], v[86:87], s[70:71]\n" \
"v_cvt_pk_bf16_f32 v84, v84, v85\n" \
"v_cvt_pk_bf16_f32 v85, v86, v87\n" \
"v_pk_mul_f32 v[88:89], v[88:89], s[70:71]\n" \
"v_pk_mul_f32 v[90:91], v[90:91], s[70:71]\n" \
"v_cvt_pk_bf16_f32 v86, v88, v89\n" \
"v_cvt_pk_bf16_f32 v87, v90, v91\n" \
"ds_write_b128 v21, v[84:87] offset:29696\n" \
"L_end_%=:\n"
#define PROJ_VCLOB "v21","v24","v25","v33","v34","v35","v36","v37","v38","v39","v40","v41","v42","v43","v44","v45","v46","v47","v48","v49","v50","v51","v52","v53","v54","v55","v56","v57","v58","v59","v60","v61","v62","v63","v64","v65","v66","v67","v68","v69","v70","v71","v72","v73","v74","v75","v76","v77","v78","v79","v80","v81","v82","v83","v84","v85","v86","v87","v88","v89","v90","v91","v92","v93","v94","v95","v96","v97","v98","v99","v100","v101","v102","v103","v104","v105","v106","v107","v108","v109","v110","v111","v112","v113","v114","v115","v116","v117","v118","v119","v120","v121","v122","v123","v124","v125","v126","v127","v128","v129","v130","v131","v132","v133","v134","v135","v136","v137","v138","v139","v140","v141","v142","v143","v144","v145","v146","v147","v148","v149","v150","v151","v152","v153","v154","v155","v156","v157","v158","v159","v160","v161","v162","v163","v164","v165","v166","v167","v168","v169","v170","v171","v172","v173","v174","v175","v176","v177","v178","v179","v180","v181","v182","v183","v184","v185","v186","v187","v188","v189","v190","v191","v192","v193","v194","v195","v196","v197","v198","v199","v200","v201","v202","v203","v204","v205","v206","v207","v208","v209","v210","v211","v212","v213","v214","v215","v216","v217","v218","v219","v220","v221","v222","v223","v224","v225","v226","v227","v228","v229","v230","v231","v232","v233","v234","v235","v236","v237","v238","v239","v240","v241","v242","v243","v244","v245","v246","v247","v248","v249","v250","v251","v252","v253","v254","v255"
#define PROJ_SCLOB "s37","s38","s39","s40","s41","s42","s43","s44","s45","s46","s47","s48","s49","s50","s51","s52","s53","s54","s55","s62","s63","s76","s77","s78","s79","s80","s81","s82","s83","s84","s85","s86","s87","s88","s89","s94","s95","s96","s97","s98","s99","s100","s101"

#define PLO(p) ((unsigned)(size_t)(p))
#define PHI(p) ((unsigned)((size_t)(p) >> 32))
__device__ __forceinline__ unsigned proj_phase(const u16* __restrict__ x, const u16* __restrict__ ckv, const u16* __restrict__ wq, const u16* __restrict__ wuk,
                                                       const u16* __restrict__ wuv, const u16* __restrict__ wqkv, const u16* __restrict__ gw, const u16* wo, const u16* wob, const u16* out,
                                                       char* __restrict__ ws, char* lds, const unsigned wid, unsigned& tag, u32x4 (&vp)[5], u32x4 (&sa)[5], unsigned (&sd)[2]) {
    typedef unsigned u32x2 __attribute__((ext_vector_type(2)));
    unsigned* const flags = (unsigned*)(ws + WS_F);
    const int b = blockIdx.x, h = (b & 7) * 4 + ((b >> 3) & 3), mt = b >> 5;
    const unsigned L0 = (unsigned)(size_t)lds, rb = (wid < 2u ? 15u - mt : mt) * 128u;
    RTS(0);
#define PST() do {} while (0)
    const unsigned lane = threadIdx.x & 63u, wr = wid >> 1, wc = wid & 1u;
    PST();
    unsigned vAc[2], vB[2];
    const unsigned sp = b & 7, nt = (b >> 3) & 3, rot = (PROJ_ROT) & 7u;
#pragma unroll
    for (int par = 0; par < 2; ++par) {
        const unsigned lr = lane >> 3, cc = (lane & 7u) ^ (4u * par + (lr >> 1)), r0 = rb + wc * 64u + lr;
        vAc[par] = r0 * (DC * 2) + cc * 16u;
        const unsigned br = lane >> 4, p16 = lane & 15u, sc = (((p16 >> 1) ^ (br | (par << 2))) << 1) | (p16 & 1u);
        vB[par] = (16u * wid + br) * (NBIG * 2) + h * (HD * 2) + sc * 16u;
    }
#define SRD(p, n) (u32x4){PLO(p), PHI(p) & 0xffffu, n, 0x00020000u}
    u32x4 sC = SRD(ckv, S * DC * 2), sK = SRD(wuk, DC * NBIG * 2), sV = SRD(wuv, DC * NBIG * 2);
#define PRE(n) do { SBAR(); asm volatile(PROJ_PRE##n :: "{v22}"(vAc[0]), "{v23}"(vAc[1]), "{v26}"(vB[0]), "{v27}"(vB[1]), "{s[64:67]}"(sC), "{s[68:71]}"(sK), "{s[72:75]}"(sV), \
                 [mA] "s"(L0 + wid * 8192u), [mB] "s"(L0 + 65536u + wid * 4096u), [rc] "s"(rot * 128u) : "memory", "m0", "scc", "s62", "s63", "s84", "s85"); SBAR(); } while (0)
    PRE(0);
    const unsigned fr = lane & 15u, fg = lane >> 4, r4 = (lane >> 2) & 3u, sB = r4 | ((fg & 1u) << 2);
    const unsigned aA0 = L0 + (wr * 64u + fr) * 128u + ((fg ^ (fr >> 1)) << 4);
    unsigned vl = lane * 16u, r32 = L0 + 65536u + (8u * fg + r4) * 256u + (lane & 3u) * 8u + ((sB ^ (4u * wc)) << 5);
    u32x2 r28 = {aA0, aA0 ^ 64u};
    asm volatile("" : "+v"(vl), "+v"(r28), "+v"(r32));
    PST();
    PRE(1);
    PST();
    PRE(2);
    PST();
    asm volatile("s_mov_b64 exec, 0\n s_cmp_lt_u32 %0, 2\n s_cbranch_scc1 L_chA\n s_cmp_eq_u32 %0, 2\n s_cbranch_scc1 L_pf2\n s_branch L_pf3\n"
                 "L_chA: s_cmp_eq_u32 %0, 0\n s_cbranch_scc1 L_pf0\n s_branch L_pf1\n L_retA: s_mov_b64 exec, -1" :: "s"(wid) : "scc", "memory");
    PRE(3);
    PST();
    sa[0] = (u32x4){PLO(x), PHI(x), PLO(wq), PHI(wq)}; sa[1] = (u32x4){PLO(wqkv), PHI(wqkv), PLO(gw), PHI(gw)}; sa[2] = (u32x4){PLO(wo), PHI(wo), PLO(wob), PHI(wob)}; sa[3] = (u32x4){PLO(out), PHI(out), rot * 128u, 0u};
    asm volatile(PROJ_ASM
                 : [tag] "=&s"(tag), "+{s[8:11]}"(sa[0]), "+{s[12:15]}"(sa[1]), "+{s[16:19]}"(sa[2]), "+{s[20:23]}"(sa[3]), "=&{s[56:59]}"(sa[4]), "=&{s60}"(sd[0]), "=&{s61}"(sd[1]),
                   "+{s[64:67]}"(sC), "+{s[68:71]}"(sK), "+{s[72:75]}"(sV), "+{v22}"(vAc[0]), "+{v23}"(vAc[1]), "+{v26}"(vB[0]), "+{v27}"(vB[1]), "+{v[28:29]}"(r28), "+{v32}"(r32),
                   "={v[0:3]}"(vp[0]), "={v[4:7]}"(vp[1]), "={v[8:11]}"(vp[2]), "={v[12:15]}"(vp[3]), "={v[16:19]}"(vp[4])
                 : "{v20}"(vl), [wsl] "s"(PLO(ws)), [wsh] "s"(PHI(ws)), [b] "s"(b), [w] "s"(wid), [mA] "s"(L0 + wid * 8192u), [mB] "s"(L0 + 65536u + wid * 4096u), [mS] "s"(L0 + 151552u + wid * 512u)
                 : "memory", "m0", "vcc", "scc", PROJ_VCLOB, PROJ_SCLOB, ACLOB_LO, ACLOB_HI);
    RTS(1);
    return vl;
}

namespace att {
#define DI __device__ __forceinline__
constexpr int LDS_BYTES = 163840;
constexpr unsigned LDS_G = 159744u, LDS_L = 161792u;
typedef __bf16 bfx8 __attribute__((ext_vector_type(8)));
DI unsigned rfl(unsigned x) { return __builtin_amdgcn_readfirstlane(x); }
DI f32x4 mfma(u32x4 a, u32x4 b, f32x4 c) { return __builtin_amdgcn_mfma_f32_16x16x32_bf16(__builtin_bit_cast(bfx8, a), __builtin_bit_cast(bfx8, b), c, 0, 0, 0); }
typedef __bf16 bfx2 __attribute__((ext_vector_type(2)));
typedef float f32x2 __attribute__((ext_vector_type(2)));
DI unsigned cvtpk2(float lo, float hi) { f32x2 v = {lo, hi}; bfx2 b = __builtin_convertvector(v, bfx2); return __builtin_bit_cast(unsigned, b); }
DI void att_exact(const u16* __restrict__ x, const u16* __restrict__ wq, char* __restrict__ ws, unsigned h, unsigned r0, float g1) {
    const unsigned lane = lane_id(), g = lane >> 4, c = lane & 15u;
    f32x4 o[8];
#pragma unroll
    for (int n = 0; n < 8; ++n) o[n] = (f32x4){0.f, 0.f, 0.f, 0.f};
#pragma unroll 1
    for (unsigned kk = 0; kk < (unsigned)DM; kk += 32u) {
        const unsigned k = kk + 8u * g;
        const u32x4 xf = *reinterpret_cast<const u32x4*>(x + (r0 + c) * DM + k);
#pragma unroll
        for (int n = 0; n < 8; ++n) {
            const u16* w = wq + k * NBIG + h * HD + n * 16 + c; u32x4 wf;
#pragma unroll
            for (int e = 0; e < 4; ++e) wf[e] = (unsigned)w[2 * e * NBIG] | ((unsigned)w[(2 * e + 1) * NBIG] << 16);
            o[n] = mfma(wf, xf, o[n]);
        }
    }
    u32x4 q[4];
#pragma unroll
    for (int p = 0; p < 4; ++p) {
        const f32x4 a = o[2 * p] * 0.12751743082f, b = o[2 * p + 1] * 0.12751743082f;
        q[p] = (u32x4){cvtpk2(a[0], a[1]), cvtpk2(a[2], a[3]), cvtpk2(b[0], b[1]), cvtpk2(b[2], b[3])};
    }
#pragma unroll
    for (int n = 0; n < 8; ++n) o[n] = (f32x4){0.f, 0.f, 0.f, 0.f};
    const __amdgpu_buffer_rsrc_t srd = __builtin_amdgcn_make_buffer_rsrc((void*)(ws + WS_K + (size_t)h * 1048576), (short)0, 1048576, 0x00020000);
    float m = -3.0e38f, l = 0.f;
#pragma unroll 1
    for (unsigned k0 = 0; k0 <= r0 + 15u; k0 += 32u) {
        const unsigned tb = (k0 >> 6) * 32768u + (k0 & 32u) * 256u;
        f32x4 s[2]; float mx = m;
#pragma unroll
        for (int kt = 0; kt < 2; ++kt) {
            s[kt] = (f32x4){0.f, 0.f, 0.f, 0.f};
#pragma unroll
            for (int p = 0; p < 4; ++p) s[kt] = mfma(__builtin_amdgcn_raw_buffer_load_b128(srd, lane * 16u + (kt * 4 + p) * 1024u, tb, 16), q[p], s[kt]);
#pragma unroll
            for (int e = 0; e < 4; ++e) { if (k0 + 16u * kt + 4u * g + e > r0 + c) s[kt][e] = -3.0e38f; mx = fmaxf(mx, s[kt][e]); }
        }
        mx = fmaxf(mx, __shfl_xor(mx, 16)); mx = fmaxf(mx, __shfl_xor(mx, 32));
        const float al = __builtin_amdgcn_exp2f(m - mx); m = mx;
        u32x4 pf;
#pragma unroll
        for (int e = 0; e < 4; ++e) pf[e] = cvtpk2(__builtin_amdgcn_exp2f(s[e >> 1][2 * (e & 1)] - m), __builtin_amdgcn_exp2f(s[e >> 1][2 * (e & 1) + 1] - m));
        l *= al;
#pragma unroll
        for (int e = 0; e < 4; ++e) l += __uint_as_float(pf[e] << 16) + __uint_as_float(pf[e] & 0xffff0000u);
#pragma unroll
        for (int n = 0; n < 8; ++n) o[n] = mfma(__builtin_amdgcn_raw_buffer_load_b128(srd, lane * 16u + 16384u + ((n >> 2) * 8 + (n & 3)) * 1024u, tb - (k0 & 32u) * 128u, 16), pf, o[n] * al);
    }
    l += __shfl_xor(l, 16); l += __shfl_xor(l, 32);
    const float f = g1 / l;
    const __amdgpu_buffer_rsrc_t srdA = __builtin_amdgcn_make_buffer_rsrc((void*)(ws + WS_AF), (short)0, 128 * KS_TOT * 1024, 0x00020000);
#pragma unroll
    for (int p = 0; p < 4; ++p) {
        const f32x4 a = o[2 * p] * f, b = o[2 * p + 1] * f;
        __builtin_amdgcn_raw_buffer_store_b128((u32x4){cvtpk2(a[0], a[1]), cvtpk2(a[2], a[3]), cvtpk2(b[0], b[1]), cvtpk2(b[2], b[3])}, srdA, lane * 16u + p * 1024u, ((r0 >> 4) * KS_TOT + 18u + 4u * h) * 1024u, 16);
    }
}
}

#define ATT_ASM \
"L_att0:\n" \
"s_mov_b32 s101, 0\n" \
"s_waitcnt vmcnt(20) lgkmcnt(0)\n" \
"s_barrier\n" \
"v_accvgpr_write_b32 a216, 0\n" \
"v_accvgpr_write_b32 a217, 0\n" \
"v_accvgpr_write_b32 a218, 0\n" \
"v_accvgpr_write_b32 a219, 0\n" \
"v_and_b32_e32 v63, 0x300, v20\n" \
"v_add_u32_e32 v58, s20, v20\n" \
"v_mfma_f32_32x32x16_bf16 v[128:143], a[216:219], a[216:219], 0\n" \
"ds_read_b128 v[112:115], v63 offset:0\n" \
"ds_read_b128 a[0:3], v58 offset:0\n" \
"ds_read_b128 a[16:19], v58 offset:8192\n" \
"ds_read_b128 v[116:119], v63 offset:1024\n" \
"ds_read_b128 a[4:7], v58 offset:1024\n" \
"v_add_u32_e32 v59, s21, v20\n" \
"v_mfma_f32_32x32x16_bf16 v[144:159], a[216:219], a[216:219], 0\n" \
"ds_read_b128 a[32:35], v59 offset:0\n" \
"ds_read_b128 a[48:51], v59 offset:8192\n" \
"s_sub_u32 s67, s12, 0xc000\n" \
"s_mov_b32 s65, s12\n" \
"v_add_u32_e32 v55, s67, v20\n" \
"v_add_u32_e32 v52, 0xc000, v55\n" \
"v_mfma_f32_32x32x16_bf16 v[160:175], a[216:219], a[216:219], 0\n" \
"ds_read_b128 a[20:23], v58 offset:9216\n" \
"s_xor_b32 m0, s65, 0x10000\n" \
"ds_read_b128 a[36:39], v59 offset:1024\n" \
"buffer_load_dwordx4 v52, s[8:11], 0 offen offset:0 sc1 lds\n" \
"ds_read_b128 a[52:55], v59 offset:9216\n" \
"v_mfma_f32_32x32x16_bf16 v[176:191], a[216:219], a[216:219], 0\n" \
"buffer_load_dwordx4 v52, s[8:11], 0 offen offset:1024 sc1 lds\n" \
"ds_read_b128 v[120:123], v63 offset:2048\n" \
"v_add_u32_e32 v60, s15, v20\n" \
"v_add_u32_e32 v60, 0x10000, v60\n" \
"v_mov_b32_e32 v56, s62\n" \
"v_mfma_f32_32x32x16_bf16 v[192:207], a[216:219], a[216:219], 0\n" \
"ds_read_b128 a[8:11], v58 offset:2048\n" \
"ds_read_b128 a[24:27], v58 offset:10240\n" \
"v_readlane_b32 s68, v11, 0\n" \
"v_bfe_i32 v21, v20, 4, 1\n" \
"v_mov_b32_e32 v54, 0x3f803f80\n" \
"v_mov_b32_e32 v57, 0x3f003f00\n" \
"v_mfma_f32_32x32x16_bf16 v[208:223], a[216:219], a[216:219], 0\n" \
"ds_read_b128 a[40:43], v59 offset:2048\n" \
"ds_read_b128 a[56:59], v59 offset:10240\n" \
"s_waitcnt lgkmcnt(14)\n" \
"v_xor_b32_e32 v112, 0x80008000, v112\n" \
"v_xor_b32_e32 v113, 0x80008000, v113\n" \
"v_xor_b32_e32 v114, 0x80008000, v114\n" \
"v_mfma_f32_32x32x16_bf16 v[224:239], a[216:219], a[216:219], 0\n" \
"ds_read_b128 v[124:127], v63 offset:3072\n" \
"ds_read_b128 a[12:15], v58 offset:3072\n" \
"v_xor_b32_e32 v115, 0x80008000, v115\n" \
"v_and_b32_e32 v21, 0xaaaaaaaa, v21\n" \
"v_accvgpr_write_b32 a220, v112\n" \
"s_waitcnt lgkmcnt(13)\n" \
"v_xor_b32_e32 v116, 0x80008000, v116\n" \
"v_mfma_f32_32x32x16_bf16 v[240:255], a[216:219], a[216:219], 0\n" \
"ds_read_b128 a[28:31], v58 offset:11264\n" \
"ds_read_b128 a[44:47], v59 offset:3072\n" \
"v_xor_b32_e32 v117, 0x80008000, v117\n" \
"v_xor_b32_e32 v118, 0x80008000, v118\n" \
"v_xor_b32_e32 v119, 0x80008000, v119\n" \
"v_accvgpr_write_b32 a221, v113\n" \
"v_mfma_f32_32x32x16_bf16 a[192:207], a[216:219], a[216:219], 0\n" \
"ds_read_b128 a[60:63], v59 offset:11264\n" \
"ds_read_b128 a[160:163], v20 offset:0\n" \
"v_accvgpr_write_b32 a222, v114\n" \
"v_accvgpr_write_b32 a223, v115\n" \
"v_readlane_b32 s80, v11, 9\n" \
"v_readlane_b32 s81, v11, 10\n" \
"v_mfma_f32_16x16x32_bf16 v[32:35], v[112:115], a[0:3], 0\n" \
"ds_read_b128 a[164:167], v20 offset:1024\n" \
"ds_read_b128 a[168:171], v20 offset:2048\n" \
"v_mfma_f32_16x16x32_bf16 v[36:39], v[112:115], a[16:19], 0\n" \
"ds_read_b128 a[172:175], v20 offset:3072\n" \
"s_waitcnt lgkmcnt(15)\n" \
"v_mfma_f32_16x16x32_bf16 v[40:43], v[112:115], a[32:35], 0\n" \
"ds_read_b128 a[188:191], v20 offset:7168\n" \
"s_waitcnt lgkmcnt(14)\n" \
"v_xor_b32_e32 v120, 0x80008000, v120\n" \
"v_mfma_f32_16x16x32_bf16 v[44:47], v[112:115], a[48:51], 0\n" \
"ds_read_b128 a[184:187], v20 offset:6144\n" \
"v_xor_b32_e32 v121, 0x80008000, v121\n" \
"v_mfma_f32_16x16x32_bf16 v[32:35], v[116:119], a[4:7], v[32:35]\n" \
"v_xor_b32_e32 v122, 0x80008000, v122\n" \
"v_xor_b32_e32 v123, 0x80008000, v123\n" \
"v_mfma_f32_16x16x32_bf16 v[36:39], v[116:119], a[20:23], v[36:39]\n" \
"ds_read_b128 a[180:183], v20 offset:5120\n" \
"ds_read_b128 a[176:179], v20 offset:4096\n" \
"v_mfma_f32_16x16x32_bf16 v[40:43], v[116:119], a[36:39], v[40:43]\n" \
"ds_read_b128 a[128:131], v20 offset:8192\n" \
"s_mov_b32 s82, 0x47fca0\n" \
"v_mfma_f32_16x16x32_bf16 v[44:47], v[116:119], a[52:55], v[44:47]\n" \
"s_waitcnt lgkmcnt(13)\n" \
"v_xor_b32_e32 v124, 0x80008000, v124\n" \
"v_mfma_f32_16x16x32_bf16 v[32:35], v[120:123], a[8:11], v[32:35]\n" \
"v_xor_b32_e32 v125, 0x80008000, v125\n" \
"v_xor_b32_e32 v126, 0x80008000, v126\n" \
"v_mfma_f32_16x16x32_bf16 v[36:39], v[120:123], a[24:27], v[36:39]\n" \
"ds_read_b128 a[132:135], v20 offset:9216\n" \
"v_xor_b32_e32 v127, 0x80008000, v127\n" \
"v_mfma_f32_16x16x32_bf16 v[40:43], v[120:123], a[40:43], v[40:43]\n" \
"ds_read_b128 a[136:139], v20 offset:10240\n" \
"ds_read_b128 a[140:143], v20 offset:11264\n" \
"v_mfma_f32_16x16x32_bf16 v[44:47], v[120:123], a[56:59], v[44:47]\n" \
"ds_read_b128 a[144:147], v20 offset:12288\n" \
"s_waitcnt lgkmcnt(15)\n" \
"v_mfma_f32_16x16x32_bf16 v[32:35], v[124:127], a[12:15], v[32:35]\n" \
"ds_read_b128 a[148:151], v20 offset:13312\n" \
"ds_read_b128 a[152:155], v20 offset:14336\n" \
"v_mfma_f32_16x16x32_bf16 v[36:39], v[124:127], a[28:31], v[36:39]\n" \
"ds_read_b128 a[156:159], v20 offset:15360\n" \
"s_waitcnt lgkmcnt(15)\n" \
"v_mfma_f32_16x16x32_bf16 v[40:43], v[124:127], a[44:47], v[40:43]\n" \
"ds_read_b128 a[64:67], v60 offset:16384\n" \
"s_mov_b32 s83, 0x20000\n" \
"v_mfma_f32_16x16x32_bf16 v[44:47], v[124:127], a[60:63], v[44:47]\n" \
"ds_read_b128 a[68:71], v60 offset:17408\n" \
"s_mov_b32 s100, 0x71800000\n" \
"v_mfma_f32_16x16x32_bf16 v[64:67], a[160:163], a[0:3], v[32:35]\n" \
"ds_read_b128 a[72:75], v60 offset:18432\n" \
"s_waitcnt lgkmcnt(15)\n" \
"v_mfma_f32_16x16x32_bf16 v[64:67], a[164:167], a[4:7], v[64:67]\n" \
"ds_read_b128 a[76:79], v60 offset:19456\n" \
"s_mov_b32 s63, s23\n" \
"v_mfma_f32_16x16x32_bf16 v[64:67], a[168:171], a[8:11], v[64:67]\n" \
"ds_read_b128 a[80:83], v60 offset:24576\n" \
"v_mov_b32_e32 v62, 0\n" \
"v_mfma_f32_16x16x32_bf16 v[64:67], a[172:175], a[12:15], v[64:67]\n" \
"ds_read_b128 a[84:87], v60 offset:25600\n" \
"s_waitcnt lgkmcnt(15)\n" \
"v_mfma_f32_16x16x32_bf16 v[68:71], a[188:191], a[12:15], v[32:35]\n" \
"ds_read_b128 a[88:91], v60 offset:26624\n" \
"v_accvgpr_write_b32 a212, v54\n" \
"v_mfma_f32_16x16x32_bf16 v[68:71], a[184:187], a[8:11], v[68:71]\n" \
"ds_read_b128 a[92:95], v60 offset:27648\n" \
"v_accvgpr_write_b32 a208, 0\n" \
"v_mfma_f32_16x16x32_bf16 v[68:71], a[180:183], a[4:7], v[68:71]\n" \
"ds_read_b128 a[96:99], v60 offset:0\n" \
"s_waitcnt lgkmcnt(15)\n" \
"v_mfma_f32_16x16x32_bf16 v[68:71], a[176:179], a[0:3], v[68:71]\n" \
"ds_read_b128 a[100:103], v60 offset:1024\n" \
"v_accvgpr_write_b32 a213, v54\n" \
"v_mfma_f32_16x16x32_bf16 v[72:75], a[160:163], a[16:19], v[36:39]\n" \
"ds_read_b128 a[104:107], v60 offset:2048\n" \
"v_accvgpr_write_b32 a209, 0\n" \
"v_mfma_f32_16x16x32_bf16 v[72:75], a[164:167], a[20:23], v[72:75]\n" \
"ds_read_b128 a[108:111], v60 offset:3072\n" \
"v_accvgpr_write_b32 a214, v54\n" \
"v_mfma_f32_16x16x32_bf16 v[72:75], a[168:171], a[24:27], v[72:75]\n" \
"ds_read_b128 a[112:115], v60 offset:8192\n" \
"L_pf10:\n" \
"s_cbranch_execz L_pf14\n" \
"s_nop 0\n" \
"v_accvgpr_write_b32 a210, 0\n" \
"v_mfma_f32_16x16x32_bf16 v[72:75], a[172:175], a[28:31], v[72:75]\n" \
"ds_read_b128 a[116:119], v60 offset:9216\n" \
"v_accvgpr_write_b32 a215, v54\n" \
"v_mfma_f32_16x16x32_bf16 v[76:79], a[188:191], a[28:31], v[36:39]\n" \
"ds_read_b128 a[120:123], v60 offset:10240\n" \
"v_accvgpr_write_b32 a211, 0\n" \
"v_mfma_f32_16x16x32_bf16 v[76:79], a[184:187], a[24:27], v[76:79]\n" \
"ds_read_b128 a[124:127], v60 offset:11264\n" \
"v_or_b32_e32 v21, 0x44444444, v21\n" \
"v_mfma_f32_16x16x32_bf16 v[76:79], a[180:183], a[20:23], v[76:79]\n" \
"v_exp_f32_e32 v64, v64\n" \
"v_mfma_f32_16x16x32_bf16 v[76:79], a[176:179], a[16:19], v[76:79]\n" \
"v_exp_f32_e32 v65, v65\n" \
"v_mfma_f32_16x16x32_bf16 v[80:83], a[160:163], a[32:35], v[40:43]\n" \
"v_exp_f32_e32 v66, v66\n" \
"v_mfma_f32_16x16x32_bf16 v[80:83], a[164:167], a[36:39], v[80:83]\n" \
"v_exp_f32_e32 v67, v67\n" \
"v_mfma_f32_16x16x32_bf16 v[80:83], a[168:171], a[40:43], v[80:83]\n" \
"v_exp_f32_e32 v68, v68\n" \
"v_mfma_f32_16x16x32_bf16 v[80:83], a[172:175], a[44:47], v[80:83]\n" \
"v_exp_f32_e32 v69, v69\n" \
"v_mfma_f32_16x16x32_bf16 v[84:87], a[188:191], a[44:47], v[40:43]\n" \
"v_exp_f32_e32 v70, v70\n" \
"v_mfma_f32_16x16x32_bf16 v[84:87], a[184:187], a[40:43], v[84:87]\n" \
"v_exp_f32_e32 v71, v71\n" \
"v_mfma_f32_16x16x32_bf16 v[84:87], a[180:183], a[36:39], v[84:87]\n" \
"v_exp_f32_e32 v72, v72\n" \
"v_mfma_f32_16x16x32_bf16 v[84:87], a[176:179], a[32:35], v[84:87]\n" \
"v_exp_f32_e32 v73, v73\n" \
"v_mfma_f32_16x16x32_bf16 v[88:91], a[160:163], a[48:51], v[44:47]\n" \
"v_exp_f32_e32 v74, v74\n" \
"v_mfma_f32_16x16x32_bf16 v[88:91], a[164:167], a[52:55], v[88:91]\n" \
"v_exp_f32_e32 v75, v75\n" \
"v_mfma_f32_16x16x32_bf16 v[88:91], a[168:171], a[56:59], v[88:91]\n" \
"v_exp_f32_e32 v76, v76\n" \
"v_mfma_f32_16x16x32_bf16 v[88:91], a[172:175], a[60:63], v[88:91]\n" \
"v_exp_f32_e32 v77, v77\n" \
"v_mfma_f32_16x16x32_bf16 v[92:95], a[188:191], a[60:63], v[44:47]\n" \
"v_exp_f32_e32 v78, v78\n" \
"v_mfma_f32_16x16x32_bf16 v[92:95], a[184:187], a[56:59], v[92:95]\n" \
"v_exp_f32_e32 v79, v79\n" \
"v_mfma_f32_16x16x32_bf16 v[92:95], a[180:183], a[52:55], v[92:95]\n" \
"v_exp_f32_e32 v80, v80\n" \
"v_mfma_f32_16x16x32_bf16 v[92:95], a[176:179], a[48:51], v[92:95]\n" \
"v_exp_f32_e32 v81, v81\n" \
"s_waitcnt vmcnt(2) lgkmcnt(0)\n" \
"s_barrier\n" \
"s_mov_b64 exec, 1\n" \
"buffer_store_dword v56, off, s[56:59], s68 offset:0 sc1\n" \
"buffer_store_dword v56, off, s[56:59], s22 sc1\n" \
"s_mov_b64 exec, -1\n" \
"v_exp_f32_e32 v82, v82\n" \
"v_exp_f32_e32 v83, v83\n" \
"v_exp_f32_e32 v84, v84\n" \
"v_exp_f32_e32 v85, v85\n" \
"v_exp_f32_e32 v86, v86\n" \
"v_exp_f32_e32 v87, v87\n" \
"v_accvgpr_write_b32 a252, v57\n" \
"v_accvgpr_write_b32 a253, v57\n" \
"v_accvgpr_write_b32 a254, v57\n" \
"v_accvgpr_write_b32 a255, v57\n" \
"s_mov_b32 s64, 0x8000\n" \
"v_add_u32_e32 v50, 0x6000, v55\n" \
"v_add_u32_e32 v51, 0xa000, v55\n" \
"v_add_u32_e32 v53, 0x10000, v55\n" \
"v_add_u32_e32 v48, 0xc000, v20\n" \
"v_add_u32_e32 v49, s17, v20\n" \
"v_accvgpr_write_b32 a224, v116\n" \
"v_accvgpr_write_b32 a225, v117\n" \
"v_accvgpr_write_b32 a226, v118\n" \
"v_accvgpr_write_b32 a227, v119\n" \
"v_accvgpr_write_b32 a228, v120\n" \
"v_accvgpr_write_b32 a229, v121\n" \
"v_accvgpr_write_b32 a230, v122\n" \
"v_accvgpr_write_b32 a231, v123\n" \
"v_accvgpr_write_b32 a232, v124\n" \
"v_accvgpr_write_b32 a233, v125\n" \
"v_accvgpr_write_b32 a234, v126\n" \
"v_accvgpr_write_b32 a235, v127\n" \
"v_exp_f32_e32 v88, v88\n" \
"v_exp_f32_e32 v89, v89\n" \
"v_exp_f32_e32 v90, v90\n" \
"v_exp_f32_e32 v91, v91\n" \
"v_cvt_pk_bf16_f32 v96, v64, v65\n" \
"v_cvt_pk_bf16_f32 v97, v66, v67\n" \
"v_cvt_pk_bf16_f32 v98, v68, v69\n" \
"v_cvt_pk_bf16_f32 v99, v70, v71\n" \
"v_cvt_pk_bf16_f32 v104, v72, v73\n" \
"v_cvt_pk_bf16_f32 v105, v74, v75\n" \
"v_cvt_pk_bf16_f32 v106, v76, v77\n" \
"v_cvt_pk_bf16_f32 v107, v78, v79\n" \
"v_cvt_pk_bf16_f32 v112, v80, v81\n" \
"v_cvt_pk_bf16_f32 v113, v82, v83\n" \
"v_cvt_pk_bf16_f32 v114, v84, v85\n" \
"v_cvt_pk_bf16_f32 v115, v86, v87\n" \
"s_sub_u32 s66, s18, 1\n" \
"s_cbranch_scc1 L_tr_%=\n" \
"s_branch L_1_%=\n" \
"L_t1_%=:\n" \
"v_mfma_f32_16x16x32_bf16 v[252:255], a[188:191], v[124:127], v[252:255]\n" \
"v_mfma_f32_16x16x32_bf16 v[252:255], a[172:175], v[120:123], v[252:255]\n" \
"v_smfmac_f32_16x16x64_bf16 a[204:207], a[212:215], v[120:127], v21\n" \
"s_waitcnt lgkmcnt(0)\n" \
"L_1_%=:\n" \
"v_mfma_f32_16x16x32_bf16 v[64:67], a[128:131], a[0:3], v[32:35]\n" \
"ds_read_b128 a[160:163], v49 offset:0\n" \
"ds_read_b128 a[176:179], v49 offset:4096\n" \
"v_mfma_f32_16x16x32_bf16 v[64:67], a[132:135], a[4:7], v[64:67]\n" \
"v_cvt_pk_bf16_f32 v120, v88, v89\n" \
"v_cvt_pk_bf16_f32 v121, v90, v91\n" \
"v_mfma_f32_16x16x32_bf16 v[64:67], a[136:139], a[8:11], v[64:67]\n" \
"ds_read_b128 a[180:183], v49 offset:5120\n" \
"ds_read_b128 a[164:167], v49 offset:1024\n" \
"v_mfma_f32_16x16x32_bf16 v[64:67], a[140:143], a[12:15], v[64:67]\n" \
"v_exp_f32_e32 v92, v92\n" \
"v_mfma_f32_16x16x32_bf16 v[68:71], a[156:159], a[12:15], v[32:35]\n" \
"ds_read_b128 a[168:171], v49 offset:2048\n" \
"ds_read_b128 a[184:187], v49 offset:6144\n" \
"v_mfma_f32_16x16x32_bf16 v[68:71], a[152:155], a[8:11], v[68:71]\n" \
"v_exp_f32_e32 v93, v93\n" \
"v_mfma_f32_16x16x32_bf16 v[68:71], a[148:151], a[4:7], v[68:71]\n" \
"ds_read_b128 a[188:191], v49 offset:7168\n" \
"ds_read_b128 a[172:175], v49 offset:3072\n" \
"v_mfma_f32_16x16x32_bf16 v[68:71], a[144:147], a[0:3], v[68:71]\n" \
"v_exp_f32_e32 v94, v94\n" \
"v_mfma_f32_16x16x32_bf16 v[72:75], a[128:131], a[16:19], v[36:39]\n" \
"v_exp_f32_e32 v95, v95\n" \
"v_mfma_f32_16x16x32_bf16 v[72:75], a[132:135], a[20:23], v[72:75]\n" \
"v_cvt_pk_bf16_f32 v122, v92, v93\n" \
"v_cvt_pk_bf16_f32 v123, v94, v95\n" \
"v_mfma_f32_16x16x32_bf16 v[72:75], a[136:139], a[24:27], v[72:75]\n" \
"v_exp_f32_e32 v64, v64\n" \
"v_mfma_f32_16x16x32_bf16 v[72:75], a[140:143], a[28:31], v[72:75]\n" \
"v_exp_f32_e32 v65, v65\n" \
"v_mfma_f32_16x16x32_bf16 v[76:79], a[156:159], a[28:31], v[36:39]\n" \
"v_exp_f32_e32 v66, v66\n" \
"v_mfma_f32_16x16x32_bf16 v[76:79], a[152:155], a[24:27], v[76:79]\n" \
"v_exp_f32_e32 v67, v67\n" \
"v_mfma_f32_16x16x32_bf16 v[76:79], a[148:151], a[20:23], v[76:79]\n" \
"v_exp_f32_e32 v68, v68\n" \
"v_mfma_f32_16x16x32_bf16 v[76:79], a[144:147], a[16:19], v[76:79]\n" \
"v_exp_f32_e32 v69, v69\n" \
"v_mfma_f32_16x16x32_bf16 v[80:83], a[128:131], a[32:35], v[40:43]\n" \
"v_exp_f32_e32 v70, v70\n" \
"v_mfma_f32_16x16x32_bf16 v[80:83], a[132:135], a[36:39], v[80:83]\n" \
"v_exp_f32_e32 v71, v71\n" \
"v_mfma_f32_16x16x32_bf16 v[80:83], a[136:139], a[40:43], v[80:83]\n" \
"v_cvt_pk_bf16_f32 v100, v64, v65\n" \
"v_cvt_pk_bf16_f32 v101, v66, v67\n" \
"v_mfma_f32_16x16x32_bf16 v[80:83], a[140:143], a[44:47], v[80:83]\n" \
"v_cvt_pk_bf16_f32 v102, v68, v69\n" \
"v_cvt_pk_bf16_f32 v103, v70, v71\n" \
"v_mfma_f32_16x16x32_bf16 v[84:87], a[156:159], a[44:47], v[40:43]\n" \
"v_exp_f32_e32 v72, v72\n" \
"v_mfma_f32_16x16x32_bf16 v[84:87], a[152:155], a[40:43], v[84:87]\n" \
"v_exp_f32_e32 v73, v73\n" \
"v_mfma_f32_16x16x32_bf16 v[84:87], a[148:151], a[36:39], v[84:87]\n" \
"v_exp_f32_e32 v74, v74\n" \
"v_mfma_f32_16x16x32_bf16 v[84:87], a[144:147], a[32:35], v[84:87]\n" \
"v_exp_f32_e32 v75, v75\n" \
"v_mfma_f32_16x16x32_bf16 v[88:91], a[128:131], a[48:51], v[44:47]\n" \
"v_exp_f32_e32 v76, v76\n" \
"v_mfma_f32_16x16x32_bf16 v[88:91], a[132:135], a[52:55], v[88:91]\n" \
"v_exp_f32_e32 v77, v77\n" \
"v_mfma_f32_16x16x32_bf16 v[88:91], a[136:139], a[56:59], v[88:91]\n" \
"v_exp_f32_e32 v78, v78\n" \
"v_mfma_f32_16x16x32_bf16 v[88:91], a[140:143], a[60:63], v[88:91]\n" \
"v_exp_f32_e32 v79, v79\n" \
"v_mfma_f32_16x16x32_bf16 v[92:95], a[156:159], a[60:63], v[44:47]\n" \
"v_cvt_pk_bf16_f32 v108, v72, v73\n" \
"v_cvt_pk_bf16_f32 v109, v74, v75\n" \
"v_mfma_f32_16x16x32_bf16 v[92:95], a[152:155], a[56:59], v[92:95]\n" \
"v_cvt_pk_bf16_f32 v110, v76, v77\n" \
"v_cvt_pk_bf16_f32 v111, v78, v79\n" \
"v_mfma_f32_16x16x32_bf16 v[92:95], a[148:151], a[52:55], v[92:95]\n" \
"ds_read_b128 a[128:131], v48 offset:0\n" \
"ds_read_b128 a[132:135], v48 offset:1024\n" \
"v_mfma_f32_16x16x32_bf16 v[92:95], a[144:147], a[48:51], v[92:95]\n" \
"s_waitcnt lgkmcnt(2)\n" \
"v_mfma_f32_16x16x32_bf16 v[128:131], a[160:163], v[96:99], v[128:131]\n" \
"ds_read_b128 a[136:139], v48 offset:2048\n" \
"ds_read_b128 a[140:143], v48 offset:3072\n" \
"v_mfma_f32_16x16x32_bf16 v[128:131], a[176:179], v[100:103], v[128:131]\n" \
"v_exp_f32_e32 v80, v80\n" \
"v_mfma_f32_16x16x32_bf16 v[132:135], a[180:183], v[100:103], v[132:135]\n" \
"ds_read_b128 a[144:147], v48 offset:4096\n" \
"ds_read_b128 a[148:151], v48 offset:5120\n" \
"v_mfma_f32_16x16x32_bf16 v[132:135], a[164:167], v[96:99], v[132:135]\n" \
"v_exp_f32_e32 v81, v81\n" \
"v_mfma_f32_16x16x32_bf16 v[136:139], a[168:171], v[96:99], v[136:139]\n" \
"ds_read_b128 a[152:155], v48 offset:6144\n" \
"ds_read_b128 a[156:159], v48 offset:7168\n" \
"v_mfma_f32_16x16x32_bf16 v[136:139], a[184:187], v[100:103], v[136:139]\n" \
"v_exp_f32_e32 v82, v82\n" \
"v_mfma_f32_16x16x32_bf16 v[140:143], a[188:191], v[100:103], v[140:143]\n" \
"v_exp_f32_e32 v83, v83\n" \
"v_mfma_f32_16x16x32_bf16 v[140:143], a[172:175], v[96:99], v[140:143]\n" \
"v_exp_f32_e32 v84, v84\n" \
"v_smfmac_f32_16x16x64_bf16 a[192:195], a[212:215], v[96:103], v21\n" \
"v_mfma_f32_16x16x32_bf16 v[160:163], a[160:163], v[104:107], v[160:163]\n" \
"v_exp_f32_e32 v85, v85\n" \
"v_mfma_f32_16x16x32_bf16 v[160:163], a[176:179], v[108:111], v[160:163]\n" \
"v_exp_f32_e32 v86, v86\n" \
"v_mfma_f32_16x16x32_bf16 v[164:167], a[180:183], v[108:111], v[164:167]\n" \
"v_exp_f32_e32 v87, v87\n" \
"v_mfma_f32_16x16x32_bf16 v[164:167], a[164:167], v[104:107], v[164:167]\n" \
"v_cvt_pk_bf16_f32 v116, v80, v81\n" \
"v_cvt_pk_bf16_f32 v117, v82, v83\n" \
"v_mfma_f32_16x16x32_bf16 v[168:171], a[168:171], v[104:107], v[168:171]\n" \
"v_cvt_pk_bf16_f32 v118, v84, v85\n" \
"v_cvt_pk_bf16_f32 v119, v86, v87\n" \
"v_mfma_f32_16x16x32_bf16 v[168:171], a[184:187], v[108:111], v[168:171]\n" \
"v_exp_f32_e32 v88, v88\n" \
"v_mfma_f32_16x16x32_bf16 v[172:175], a[188:191], v[108:111], v[172:175]\n" \
"v_exp_f32_e32 v89, v89\n" \
"v_mfma_f32_16x16x32_bf16 v[172:175], a[172:175], v[104:107], v[172:175]\n" \
"v_exp_f32_e32 v90, v90\n" \
"v_smfmac_f32_16x16x64_bf16 a[196:199], a[212:215], v[104:111], v21\n" \
"v_mfma_f32_16x16x32_bf16 v[192:195], a[160:163], v[112:115], v[192:195]\n" \
"v_exp_f32_e32 v91, v91\n" \
"v_mfma_f32_16x16x32_bf16 v[192:195], a[176:179], v[116:119], v[192:195]\n" \
"v_exp_f32_e32 v92, v92\n" \
"v_mfma_f32_16x16x32_bf16 v[196:199], a[180:183], v[116:119], v[196:199]\n" \
"v_exp_f32_e32 v93, v93\n" \
"v_mfma_f32_16x16x32_bf16 v[196:199], a[164:167], v[112:115], v[196:199]\n" \
"v_exp_f32_e32 v94, v94\n" \
"v_mfma_f32_16x16x32_bf16 v[200:203], a[168:171], v[112:115], v[200:203]\n" \
"v_exp_f32_e32 v95, v95\n" \
"v_mfma_f32_16x16x32_bf16 v[200:203], a[184:187], v[116:119], v[200:203]\n" \
"v_cvt_pk_bf16_f32 v124, v88, v89\n" \
"v_cvt_pk_bf16_f32 v125, v90, v91\n" \
"v_mfma_f32_16x16x32_bf16 v[204:207], a[188:191], v[116:119], v[204:207]\n" \
"v_cvt_pk_bf16_f32 v126, v92, v93\n" \
"v_cvt_pk_bf16_f32 v127, v94, v95\n" \
"v_mfma_f32_16x16x32_bf16 v[204:207], a[172:175], v[112:115], v[204:207]\n" \
"v_smfmac_f32_16x16x64_bf16 a[200:203], a[212:215], v[112:119], v21\n" \
"s_waitcnt vmcnt(0) lgkmcnt(0)\n" \
"s_barrier\n" \
"v_mfma_f32_16x16x32_bf16 v[224:227], a[160:163], v[120:123], v[224:227]\n" \
"s_add_u32 s63, s63, s63\n" \
"s_cbranch_scc1 L_h1_%=\n" \
"L_hr1_%=:\n" \
"v_mfma_f32_16x16x32_bf16 v[224:227], a[176:179], v[124:127], v[224:227]\n" \
"s_xor_b32 m0, s65, 0x12000\n" \
"ds_read_b128 a[160:163], v49 offset:8192\n" \
"v_mfma_f32_16x16x32_bf16 v[228:231], a[180:183], v[124:127], v[228:231]\n" \
"buffer_load_dwordx4 v50, s[8:11], s64 offen offset:0 sc1 lds\n" \
"v_mfma_f32_16x16x32_bf16 v[228:231], a[164:167], v[120:123], v[228:231]\n" \
"ds_read_b128 a[176:179], v49 offset:12288\n" \
"ds_read_b128 a[180:183], v49 offset:13312\n" \
"v_mfma_f32_16x16x32_bf16 v[232:235], a[168:171], v[120:123], v[232:235]\n" \
"v_mfma_f32_16x16x32_bf16 v[232:235], a[184:187], v[124:127], v[232:235]\n" \
"ds_read_b128 a[164:167], v49 offset:9216\n" \
"ds_read_b128 a[168:171], v49 offset:10240\n" \
"v_mfma_f32_16x16x32_bf16 v[236:239], a[188:191], v[124:127], v[236:239]\n" \
"v_mfma_f32_16x16x32_bf16 v[236:239], a[172:175], v[120:123], v[236:239]\n" \
"ds_read_b128 a[184:187], v49 offset:14336\n" \
"ds_read_b128 a[188:191], v49 offset:15360\n" \
"v_mfma_f32_16x16x32_bf16 v[64:67], a[128:131], a[0:3], v[32:35]\n" \
"v_mfma_f32_16x16x32_bf16 v[64:67], a[132:135], a[4:7], v[64:67]\n" \
"buffer_load_dwordx4 v50, s[8:11], s64 offen offset:1024 sc1 lds\n" \
"v_mfma_f32_16x16x32_bf16 v[64:67], a[136:139], a[8:11], v[64:67]\n" \
"s_xor_b32 m0, s65, 0x6000\n" \
"ds_read_b128 a[172:175], v49 offset:11264\n" \
"v_mfma_f32_16x16x32_bf16 v[64:67], a[140:143], a[12:15], v[64:67]\n" \
"v_add_u32_e32 v49, 0x10000, v48\n" \
"v_mfma_f32_16x16x32_bf16 v[68:71], a[156:159], a[12:15], v[32:35]\n" \
"v_mfma_f32_16x16x32_bf16 v[68:71], a[152:155], a[8:11], v[68:71]\n" \
"v_mfma_f32_16x16x32_bf16 v[68:71], a[148:151], a[4:7], v[68:71]\n" \
"v_mfma_f32_16x16x32_bf16 v[68:71], a[144:147], a[0:3], v[68:71]\n" \
"v_mfma_f32_16x16x32_bf16 v[72:75], a[128:131], a[16:19], v[36:39]\n" \
"v_mfma_f32_16x16x32_bf16 v[72:75], a[132:135], a[20:23], v[72:75]\n" \
"v_mfma_f32_16x16x32_bf16 v[72:75], a[136:139], a[24:27], v[72:75]\n" \
"buffer_load_dwordx4 v51, s[8:11], s64 offen offset:0 sc1 lds\n" \
"v_mfma_f32_16x16x32_bf16 v[72:75], a[140:143], a[28:31], v[72:75]\n" \
"v_exp_f32_e32 v64, v64\n" \
"v_mfma_f32_16x16x32_bf16 v[76:79], a[156:159], a[28:31], v[36:39]\n" \
"v_exp_f32_e32 v65, v65\n" \
"v_mfma_f32_16x16x32_bf16 v[76:79], a[152:155], a[24:27], v[76:79]\n" \
"v_exp_f32_e32 v66, v66\n" \
"v_mfma_f32_16x16x32_bf16 v[76:79], a[148:151], a[20:23], v[76:79]\n" \
"v_exp_f32_e32 v67, v67\n" \
"v_mfma_f32_16x16x32_bf16 v[76:79], a[144:147], a[16:19], v[76:79]\n" \
"v_exp_f32_e32 v68, v68\n" \
"v_mfma_f32_16x16x32_bf16 v[80:83], a[128:131], a[32:35], v[40:43]\n" \
"v_exp_f32_e32 v69, v69\n" \
"v_mfma_f32_16x16x32_bf16 v[80:83], a[132:135], a[36:39], v[80:83]\n" \
"v_exp_f32_e32 v70, v70\n" \
"v_mfma_f32_16x16x32_bf16 v[80:83], a[136:139], a[40:43], v[80:83]\n" \
"v_exp_f32_e32 v71, v71\n" \
"v_mfma_f32_16x16x32_bf16 v[80:83], a[140:143], a[44:47], v[80:83]\n" \
"buffer_load_dwordx4 v51, s[8:11], s64 offen offset:1024 sc1 lds\n" \
"v_mfma_f32_16x16x32_bf16 v[84:87], a[156:159], a[44:47], v[40:43]\n" \
"v_exp_f32_e32 v72, v72\n" \
"v_mfma_f32_16x16x32_bf16 v[84:87], a[152:155], a[40:43], v[84:87]\n" \
"v_exp_f32_e32 v73, v73\n" \
"v_mfma_f32_16x16x32_bf16 v[84:87], a[148:151], a[36:39], v[84:87]\n" \
"v_exp_f32_e32 v74, v74\n" \
"v_mfma_f32_16x16x32_bf16 v[84:87], a[144:147], a[32:35], v[84:87]\n" \
"v_exp_f32_e32 v75, v75\n" \
"v_mfma_f32_16x16x32_bf16 v[88:91], a[128:131], a[48:51], v[44:47]\n" \
"s_xor_b32 m0, s65, 0x14000\n" \
"ds_read_b128 a[128:131], v48 offset:8192\n" \
"v_mfma_f32_16x16x32_bf16 v[88:91], a[132:135], a[52:55], v[88:91]\n" \
"v_exp_f32_e32 v76, v76\n" \
"v_mfma_f32_16x16x32_bf16 v[88:91], a[136:139], a[56:59], v[88:91]\n" \
"ds_read_b128 a[132:135], v48 offset:9216\n" \
"ds_read_b128 a[136:139], v48 offset:10240\n" \
"v_mfma_f32_16x16x32_bf16 v[88:91], a[140:143], a[60:63], v[88:91]\n" \
"v_exp_f32_e32 v77, v77\n" \
"v_mfma_f32_16x16x32_bf16 v[92:95], a[156:159], a[60:63], v[44:47]\n" \
"buffer_load_dwordx4 v52, s[8:11], s64 offen offset:0 sc1 lds\n" \
"v_mfma_f32_16x16x32_bf16 v[92:95], a[152:155], a[56:59], v[92:95]\n" \
"ds_read_b128 a[140:143], v48 offset:11264\n" \
"ds_read_b128 a[152:155], v48 offset:14336\n" \
"v_mfma_f32_16x16x32_bf16 v[92:95], a[148:151], a[52:55], v[92:95]\n" \
"v_exp_f32_e32 v78, v78\n" \
"v_mfma_f32_16x16x32_bf16 v[92:95], a[144:147], a[48:51], v[92:95]\n" \
"ds_read_b128 a[144:147], v48 offset:12288\n" \
"s_waitcnt lgkmcnt(6)\n" \
"v_mfma_f32_16x16x32_bf16 v[144:147], a[160:163], v[96:99], v[144:147]\n" \
"ds_read_b128 a[148:151], v48 offset:13312\n" \
"ds_read_b128 a[156:159], v48 offset:15360\n" \
"v_mfma_f32_16x16x32_bf16 v[144:147], a[176:179], v[100:103], v[144:147]\n" \
"v_exp_f32_e32 v79, v79\n" \
"v_mfma_f32_16x16x32_bf16 v[148:151], a[180:183], v[100:103], v[148:151]\n" \
"v_exp_f32_e32 v80, v80\n" \
"v_mfma_f32_16x16x32_bf16 v[148:151], a[164:167], v[96:99], v[148:151]\n" \
"v_exp_f32_e32 v81, v81\n" \
"v_mfma_f32_16x16x32_bf16 v[152:155], a[168:171], v[96:99], v[152:155]\n" \
"buffer_load_dwordx4 v52, s[8:11], s64 offen offset:1024 sc1 lds\n" \
"v_mfma_f32_16x16x32_bf16 v[152:155], a[184:187], v[100:103], v[152:155]\n" \
"s_mov_b32 m0, s65\n" \
"s_xor_b32 s65, s65, 0x4000\n" \
"v_mfma_f32_16x16x32_bf16 v[156:159], a[188:191], v[100:103], v[156:159]\n" \
"v_exp_f32_e32 v82, v82\n" \
"v_mfma_f32_16x16x32_bf16 v[156:159], a[172:175], v[96:99], v[156:159]\n" \
"v_exp_f32_e32 v83, v83\n" \
"v_mfma_f32_16x16x32_bf16 v[176:179], a[160:163], v[104:107], v[176:179]\n" \
"v_exp_f32_e32 v84, v84\n" \
"v_mfma_f32_16x16x32_bf16 v[176:179], a[176:179], v[108:111], v[176:179]\n" \
"v_exp_f32_e32 v85, v85\n" \
"v_mfma_f32_16x16x32_bf16 v[180:183], a[180:183], v[108:111], v[180:183]\n" \
"v_exp_f32_e32 v86, v86\n" \
"v_mfma_f32_16x16x32_bf16 v[180:183], a[164:167], v[104:107], v[180:183]\n" \
"v_exp_f32_e32 v87, v87\n" \
"v_mfma_f32_16x16x32_bf16 v[184:187], a[168:171], v[104:107], v[184:187]\n" \
"v_exp_f32_e32 v88, v88\n" \
"v_mfma_f32_16x16x32_bf16 v[184:187], a[184:187], v[108:111], v[184:187]\n" \
"buffer_load_dwordx4 v53, s[8:11], s64 offen offset:0 sc1 lds\n" \
"v_mfma_f32_16x16x32_bf16 v[188:191], a[188:191], v[108:111], v[188:191]\n" \
"v_exp_f32_e32 v89, v89\n" \
"v_mfma_f32_16x16x32_bf16 v[188:191], a[172:175], v[104:107], v[188:191]\n" \
"v_exp_f32_e32 v90, v90\n" \
"v_mfma_f32_16x16x32_bf16 v[208:211], a[160:163], v[112:115], v[208:211]\n" \
"v_exp_f32_e32 v91, v91\n" \
"v_mfma_f32_16x16x32_bf16 v[208:211], a[176:179], v[116:119], v[208:211]\n" \
"v_cvt_pk_bf16_f32 v96, v64, v65\n" \
"v_cvt_pk_bf16_f32 v97, v66, v67\n" \
"v_mfma_f32_16x16x32_bf16 v[212:215], a[180:183], v[116:119], v[212:215]\n" \
"v_cvt_pk_bf16_f32 v98, v68, v69\n" \
"v_cvt_pk_bf16_f32 v99, v70, v71\n" \
"v_mfma_f32_16x16x32_bf16 v[212:215], a[164:167], v[112:115], v[212:215]\n" \
"v_cvt_pk_bf16_f32 v104, v72, v73\n" \
"v_cvt_pk_bf16_f32 v105, v74, v75\n" \
"v_mfma_f32_16x16x32_bf16 v[216:219], a[168:171], v[112:115], v[216:219]\n" \
"v_cvt_pk_bf16_f32 v106, v76, v77\n" \
"v_cvt_pk_bf16_f32 v107, v78, v79\n" \
"v_mfma_f32_16x16x32_bf16 v[216:219], a[184:187], v[116:119], v[216:219]\n" \
"v_xor_b32_e32 v48, 0x4000, v48\n" \
"v_mfma_f32_16x16x32_bf16 v[220:223], a[188:191], v[116:119], v[220:223]\n" \
"buffer_load_dwordx4 v53, s[8:11], s64 offen offset:1024 sc1 lds\n" \
"v_mfma_f32_16x16x32_bf16 v[220:223], a[172:175], v[112:115], v[220:223]\n" \
"s_add_u32 s64, s64, 0x8000\n" \
"s_sub_u32 s66, s66, 1\n" \
"v_mfma_f32_16x16x32_bf16 v[240:243], a[160:163], v[120:123], v[240:243]\n" \
"v_cvt_pk_bf16_f32 v112, v80, v81\n" \
"v_cvt_pk_bf16_f32 v113, v82, v83\n" \
"v_mfma_f32_16x16x32_bf16 v[240:243], a[176:179], v[124:127], v[240:243]\n" \
"v_cvt_pk_bf16_f32 v114, v84, v85\n" \
"v_cvt_pk_bf16_f32 v115, v86, v87\n" \
"v_mfma_f32_16x16x32_bf16 v[244:247], a[180:183], v[124:127], v[244:247]\n" \
"v_mfma_f32_16x16x32_bf16 v[244:247], a[164:167], v[120:123], v[244:247]\n" \
"v_mfma_f32_16x16x32_bf16 v[248:251], a[168:171], v[120:123], v[248:251]\n" \
"v_mfma_f32_16x16x32_bf16 v[248:251], a[184:187], v[124:127], v[248:251]\n" \
"s_cbranch_scc0 L_t1_%=\n" \
"v_mfma_f32_16x16x32_bf16 v[252:255], a[188:191], v[124:127], v[252:255]\n" \
"v_mfma_f32_16x16x32_bf16 v[252:255], a[172:175], v[120:123], v[252:255]\n" \
"v_smfmac_f32_16x16x64_bf16 a[204:207], a[212:215], v[120:127], v21\n" \
"s_waitcnt lgkmcnt(0)\n" \
"s_barrier\n" \
"L_tr_%=:\n" \
"L_pf11:\n" \
"s_cbranch_execz L_pf15\n" \
"s_nop 0\n" \
"s_cmp_lg_u32 s16, 0\n" \
"s_cbranch_scc1 L_sw_%=\n" \
"v_mfma_f32_16x16x32_bf16 v[64:67], a[128:131], a[0:3], v[32:35]\n" \
"s_xor_b32 m0, s65, 0x12000\n" \
"s_mov_b32 s65, s13\n" \
"v_mfma_f32_16x16x32_bf16 v[64:67], a[132:135], a[4:7], v[64:67]\n" \
"buffer_load_dwordx4 v50, s[8:11], s64 offen offset:0 sc1 lds\n" \
"v_mfma_f32_16x16x32_bf16 v[64:67], a[136:139], a[8:11], v[64:67]\n" \
"v_add_u32_e32 v63, s15, v20\n" \
"s_add_u32 s67, s14, 40960\n" \
"v_mfma_f32_16x16x32_bf16 v[64:67], a[140:143], a[12:15], v[64:67]\n" \
"ds_read_b128 a[160:163], v49 offset:0\n" \
"ds_read_b128 a[176:179], v49 offset:4096\n" \
"v_mfma_f32_16x16x32_bf16 v[68:71], a[156:159], a[12:15], v[32:35]\n" \
"buffer_load_dwordx4 v50, s[8:11], s64 offen offset:1024 sc1 lds\n" \
"v_mfma_f32_16x16x32_bf16 v[68:71], a[152:155], a[8:11], v[68:71]\n" \
"v_add_u32_e32 v50, 0x6000, v63\n" \
"s_xor_b32 m0, s65, 0x8000\n" \
"v_mfma_f32_16x16x32_bf16 v[68:71], a[148:151], a[4:7], v[68:71]\n" \
"ds_read_b128 a[180:183], v49 offset:5120\n" \
"ds_read_b128 a[164:167], v49 offset:1024\n" \
"v_mfma_f32_16x16x32_bf16 v[68:71], a[144:147], a[0:3], v[68:71]\n" \
"ds_read_b128 a[168:171], v49 offset:2048\n" \
"ds_read_b128 a[184:187], v49 offset:6144\n" \
"v_mfma_f32_16x16x32_bf16 v[72:75], a[128:131], a[16:19], v[36:39]\n" \
"v_cvt_pk_bf16_f32 v120, v88, v89\n" \
"v_cvt_pk_bf16_f32 v121, v90, v91\n" \
"v_mfma_f32_16x16x32_bf16 v[72:75], a[132:135], a[20:23], v[72:75]\n" \
"buffer_load_dwordx4 v50, s[8:11], s67 offen offset:0 sc1 lds\n" \
"v_mfma_f32_16x16x32_bf16 v[72:75], a[136:139], a[24:27], v[72:75]\n" \
"ds_read_b128 a[188:191], v49 offset:7168\n" \
"ds_read_b128 a[172:175], v49 offset:3072\n" \
"v_mfma_f32_16x16x32_bf16 v[72:75], a[140:143], a[28:31], v[72:75]\n" \
"v_exp_f32_e32 v92, v92\n" \
"v_mfma_f32_16x16x32_bf16 v[76:79], a[156:159], a[28:31], v[36:39]\n" \
"v_exp_f32_e32 v93, v93\n" \
"v_mfma_f32_16x16x32_bf16 v[76:79], a[152:155], a[24:27], v[76:79]\n" \
"buffer_load_dwordx4 v50, s[8:11], s67 offen offset:1024 sc1 lds\n" \
"v_mfma_f32_16x16x32_bf16 v[76:79], a[148:151], a[20:23], v[76:79]\n" \
"v_exp_f32_e32 v94, v94\n" \
"v_mfma_f32_16x16x32_bf16 v[76:79], a[144:147], a[16:19], v[76:79]\n" \
"v_exp_f32_e32 v95, v95\n" \
"v_mfma_f32_16x16x32_bf16 v[80:83], a[128:131], a[32:35], v[40:43]\n" \
"v_cvt_pk_bf16_f32 v122, v92, v93\n" \
"v_cvt_pk_bf16_f32 v123, v94, v95\n" \
"v_mfma_f32_16x16x32_bf16 v[80:83], a[132:135], a[36:39], v[80:83]\n" \
"v_exp_f32_e32 v64, v64\n" \
"v_mfma_f32_16x16x32_bf16 v[80:83], a[136:139], a[40:43], v[80:83]\n" \
"buffer_load_dwordx4 v50, s[8:11], s67 offen offset:2048 sc1 lds\n" \
"v_mfma_f32_16x16x32_bf16 v[80:83], a[140:143], a[44:47], v[80:83]\n" \
"v_exp_f32_e32 v65, v65\n" \
"v_mfma_f32_16x16x32_bf16 v[84:87], a[156:159], a[44:47], v[40:43]\n" \
"v_exp_f32_e32 v66, v66\n" \
"v_mfma_f32_16x16x32_bf16 v[84:87], a[152:155], a[40:43], v[84:87]\n" \
"v_exp_f32_e32 v67, v67\n" \
"v_mfma_f32_16x16x32_bf16 v[84:87], a[148:151], a[36:39], v[84:87]\n" \
"buffer_load_dwordx4 v50, s[8:11], s67 offen offset:3072 sc1 lds\n" \
"v_mfma_f32_16x16x32_bf16 v[84:87], a[144:147], a[32:35], v[84:87]\n" \
"v_exp_f32_e32 v68, v68\n" \
"v_mfma_f32_16x16x32_bf16 v[88:91], a[128:131], a[48:51], v[44:47]\n" \
"v_exp_f32_e32 v69, v69\n" \
"v_mfma_f32_16x16x32_bf16 v[88:91], a[132:135], a[52:55], v[88:91]\n" \
"v_exp_f32_e32 v70, v70\n" \
"v_mfma_f32_16x16x32_bf16 v[88:91], a[136:139], a[56:59], v[88:91]\n" \
"v_exp_f32_e32 v71, v71\n" \
"v_mfma_f32_16x16x32_bf16 v[88:91], a[140:143], a[60:63], v[88:91]\n" \
"v_cvt_pk_bf16_f32 v100, v64, v65\n" \
"v_cvt_pk_bf16_f32 v101, v66, v67\n" \
"v_mfma_f32_16x16x32_bf16 v[92:95], a[156:159], a[60:63], v[44:47]\n" \
"v_cvt_pk_bf16_f32 v102, v68, v69\n" \
"v_cvt_pk_bf16_f32 v103, v70, v71\n" \
"v_mfma_f32_16x16x32_bf16 v[92:95], a[152:155], a[56:59], v[92:95]\n" \
"s_mov_b32 s64, s14\n" \
"v_add_u32_e32 v51, 0x12000, v63\n" \
"v_mfma_f32_16x16x32_bf16 v[92:95], a[148:151], a[52:55], v[92:95]\n" \
"s_xor_b32 m0, s65, 0xa000\n" \
"v_readlane_b32 s93, v11, 17\n" \
"v_mfma_f32_16x16x32_bf16 v[92:95], a[144:147], a[48:51], v[92:95]\n" \
"v_readlane_b32 s92, v11, 22\n" \
"s_waitcnt lgkmcnt(0)\n" \
"v_mfma_f32_16x16x32_bf16 v[128:131], a[160:163], v[96:99], v[128:131]\n" \
"buffer_load_dwordx4 v51, s[8:11], s64 offen offset:0 sc1 lds\n" \
"v_mfma_f32_16x16x32_bf16 v[128:131], a[176:179], v[100:103], v[128:131]\n" \
"buffer_load_dwordx4 v51, s[8:11], s64 offen offset:1024 sc1 lds\n" \
"v_mfma_f32_16x16x32_bf16 v[132:135], a[180:183], v[100:103], v[132:135]\n" \
"v_exp_f32_e32 v72, v72\n" \
"v_mfma_f32_16x16x32_bf16 v[132:135], a[164:167], v[96:99], v[132:135]\n" \
"v_exp_f32_e32 v73, v73\n" \
"v_mfma_f32_16x16x32_bf16 v[136:139], a[168:171], v[96:99], v[136:139]\n" \
"v_exp_f32_e32 v74, v74\n" \
"v_mfma_f32_16x16x32_bf16 v[136:139], a[184:187], v[100:103], v[136:139]\n" \
"v_exp_f32_e32 v75, v75\n" \
"v_mfma_f32_16x16x32_bf16 v[140:143], a[188:191], v[100:103], v[140:143]\n" \
"v_exp_f32_e32 v76, v76\n" \
"v_mfma_f32_16x16x32_bf16 v[140:143], a[172:175], v[96:99], v[140:143]\n" \
"v_exp_f32_e32 v77, v77\n" \
"v_smfmac_f32_16x16x64_bf16 a[192:195], a[212:215], v[96:103], v21\n" \
"v_mfma_f32_16x16x32_bf16 v[160:163], a[160:163], v[104:107], v[160:163]\n" \
"v_exp_f32_e32 v78, v78\n" \
"v_exp_f32_e32 v79, v79\n" \
"v_cvt_pk_bf16_f32 v108, v72, v73\n" \
"v_cvt_pk_bf16_f32 v109, v74, v75\n" \
"v_cvt_pk_bf16_f32 v110, v76, v77\n" \
"v_cvt_pk_bf16_f32 v111, v78, v79\n" \
"v_exp_f32_e32 v80, v80\n" \
"v_exp_f32_e32 v81, v81\n" \
"v_mfma_f32_16x16x32_bf16 v[160:163], a[176:179], v[108:111], v[160:163]\n" \
"buffer_load_dwordx4 v51, s[8:11], s64 offen offset:2048 sc1 lds\n" \
"v_mfma_f32_16x16x32_bf16 v[164:167], a[180:183], v[108:111], v[164:167]\n" \
"buffer_load_dwordx4 v51, s[8:11], s64 offen offset:3072 sc1 lds\n" \
"v_mfma_f32_16x16x32_bf16 v[164:167], a[164:167], v[104:107], v[164:167]\n" \
"v_exp_f32_e32 v82, v82\n" \
"v_mfma_f32_16x16x32_bf16 v[168:171], a[168:171], v[104:107], v[168:171]\n" \
"v_exp_f32_e32 v83, v83\n" \
"v_mfma_f32_16x16x32_bf16 v[168:171], a[184:187], v[108:111], v[168:171]\n" \
"v_exp_f32_e32 v84, v84\n" \
"v_mfma_f32_16x16x32_bf16 v[172:175], a[188:191], v[108:111], v[172:175]\n" \
"v_exp_f32_e32 v85, v85\n" \
"v_mfma_f32_16x16x32_bf16 v[172:175], a[172:175], v[104:107], v[172:175]\n" \
"v_exp_f32_e32 v86, v86\n" \
"v_smfmac_f32_16x16x64_bf16 a[196:199], a[212:215], v[104:111], v21\n" \
"v_mfma_f32_16x16x32_bf16 v[192:195], a[160:163], v[112:115], v[192:195]\n" \
"v_exp_f32_e32 v87, v87\n" \
"v_cvt_pk_bf16_f32 v116, v80, v81\n" \
"v_cvt_pk_bf16_f32 v117, v82, v83\n" \
"v_cvt_pk_bf16_f32 v118, v84, v85\n" \
"v_cvt_pk_bf16_f32 v119, v86, v87\n" \
"s_add_u32 m0, s93, 0x0\n" \
"v_exp_f32_e32 v88, v88\n" \
"v_mfma_f32_16x16x32_bf16 v[192:195], a[176:179], v[116:119], v[192:195]\n" \
"buffer_load_dwordx4 v0, s[80:83], s92 offen offset:0 lds\n" \
"v_mfma_f32_16x16x32_bf16 v[196:199], a[180:183], v[116:119], v[196:199]\n" \
"ds_read_b128 a[128:131], v48 offset:0\n" \
"ds_read_b128 a[132:135], v48 offset:1024\n" \
"v_mfma_f32_16x16x32_bf16 v[196:199], a[164:167], v[112:115], v[196:199]\n" \
"ds_read_b128 a[136:139], v48 offset:2048\n" \
"ds_read_b128 a[140:143], v48 offset:3072\n" \
"ds_read_b128 a[144:147], v48 offset:4096\n" \
"ds_read_b128 a[148:151], v48 offset:5120\n" \
"ds_read_b128 a[152:155], v48 offset:6144\n" \
"ds_read_b128 a[156:159], v48 offset:7168\n" \
"v_mfma_f32_16x16x32_bf16 v[200:203], a[168:171], v[112:115], v[200:203]\n" \
"buffer_load_dwordx4 v1, s[80:83], s92 offen offset:1024 lds\n" \
"v_mfma_f32_16x16x32_bf16 v[200:203], a[184:187], v[116:119], v[200:203]\n" \
"v_exp_f32_e32 v89, v89\n" \
"v_mfma_f32_16x16x32_bf16 v[204:207], a[188:191], v[116:119], v[204:207]\n" \
"v_exp_f32_e32 v90, v90\n" \
"v_mfma_f32_16x16x32_bf16 v[204:207], a[172:175], v[112:115], v[204:207]\n" \
"v_exp_f32_e32 v91, v91\n" \
"v_smfmac_f32_16x16x64_bf16 a[200:203], a[212:215], v[112:119], v21\n" \
"v_mfma_f32_16x16x32_bf16 v[224:227], a[160:163], v[120:123], v[224:227]\n" \
"v_exp_f32_e32 v92, v92\n" \
"v_exp_f32_e32 v93, v93\n" \
"v_exp_f32_e32 v94, v94\n" \
"v_exp_f32_e32 v95, v95\n" \
"v_cvt_pk_bf16_f32 v124, v88, v89\n" \
"v_cvt_pk_bf16_f32 v125, v90, v91\n" \
"v_cvt_pk_bf16_f32 v126, v92, v93\n" \
"v_cvt_pk_bf16_f32 v127, v94, v95\n" \
"v_add_u32_e32 v52, 0x14000, v63\n" \
"v_add_u32_e32 v53, 0x20000, v63\n" \
"v_mfma_f32_16x16x32_bf16 v[224:227], a[176:179], v[124:127], v[224:227]\n" \
"buffer_load_dwordx4 v2, s[80:83], s92 offen offset:2048 lds\n" \
"v_mfma_f32_16x16x32_bf16 v[228:231], a[180:183], v[124:127], v[228:231]\n" \
"buffer_load_dwordx4 v3, s[80:83], s92 offen offset:3072 lds\n" \
"v_mfma_f32_16x16x32_bf16 v[228:231], a[164:167], v[120:123], v[228:231]\n" \
"s_mov_b32 s66, s19\n" \
"v_mfma_f32_16x16x32_bf16 v[232:235], a[168:171], v[120:123], v[232:235]\n" \
"v_mfma_f32_16x16x32_bf16 v[232:235], a[184:187], v[124:127], v[232:235]\n" \
"v_mfma_f32_16x16x32_bf16 v[236:239], a[188:191], v[124:127], v[236:239]\n" \
"v_mfma_f32_16x16x32_bf16 v[236:239], a[172:175], v[120:123], v[236:239]\n" \
"s_waitcnt vmcnt(14) lgkmcnt(0)\n" \
"s_barrier\n" \
"v_mfma_f32_16x16x32_bf16 v[64:67], a[128:131], a[0:3], v[32:35]\n" \
"s_xor_b32 m0, s65, 0x18000\n" \
"ds_read_b128 a[160:163], v49 offset:8192\n" \
"v_mfma_f32_16x16x32_bf16 v[64:67], a[132:135], a[4:7], v[64:67]\n" \
"buffer_load_dwordx4 v52, s[8:11], s64 offen offset:0 sc1 lds\n" \
"v_mfma_f32_16x16x32_bf16 v[64:67], a[136:139], a[8:11], v[64:67]\n" \
"ds_read_b128 a[176:179], v49 offset:12288\n" \
"ds_read_b128 a[180:183], v49 offset:13312\n" \
"v_mfma_f32_16x16x32_bf16 v[64:67], a[140:143], a[12:15], v[64:67]\n" \
"ds_read_b128 a[164:167], v49 offset:9216\n" \
"ds_read_b128 a[168:171], v49 offset:10240\n" \
"v_mfma_f32_16x16x32_bf16 v[68:71], a[156:159], a[12:15], v[32:35]\n" \
"buffer_load_dwordx4 v52, s[8:11], s64 offen offset:1024 sc1 lds\n" \
"v_mfma_f32_16x16x32_bf16 v[68:71], a[152:155], a[8:11], v[68:71]\n" \
"ds_read_b128 a[184:187], v49 offset:14336\n" \
"ds_read_b128 a[188:191], v49 offset:15360\n" \
"v_mfma_f32_16x16x32_bf16 v[68:71], a[148:151], a[4:7], v[68:71]\n" \
"v_exp_f32_e32 v64, v64\n" \
"v_mfma_f32_16x16x32_bf16 v[68:71], a[144:147], a[0:3], v[68:71]\n" \
"v_exp_f32_e32 v65, v65\n" \
"v_mfma_f32_16x16x32_bf16 v[72:75], a[128:131], a[16:19], v[36:39]\n" \
"v_exp_f32_e32 v66, v66\n" \
"v_mfma_f32_16x16x32_bf16 v[72:75], a[132:135], a[20:23], v[72:75]\n" \
"buffer_load_dwordx4 v52, s[8:11], s64 offen offset:2048 sc1 lds\n" \
"v_mfma_f32_16x16x32_bf16 v[72:75], a[136:139], a[24:27], v[72:75]\n" \
"v_exp_f32_e32 v67, v67\n" \
"v_mfma_f32_16x16x32_bf16 v[72:75], a[140:143], a[28:31], v[72:75]\n" \
"v_exp_f32_e32 v68, v68\n" \
"v_mfma_f32_16x16x32_bf16 v[76:79], a[156:159], a[28:31], v[36:39]\n" \
"v_exp_f32_e32 v69, v69\n" \
"L_pf12:\n" \
"s_cbranch_execz L_pf16\n" \
"s_nop 0\n" \
"v_mfma_f32_16x16x32_bf16 v[76:79], a[152:155], a[24:27], v[76:79]\n" \
"buffer_load_dwordx4 v52, s[8:11], s64 offen offset:3072 sc1 lds\n" \
"v_mfma_f32_16x16x32_bf16 v[76:79], a[148:151], a[20:23], v[76:79]\n" \
"ds_read_b128 a[172:175], v49 offset:11264\n" \
"s_mov_b32 m0, s65\n" \
"v_mfma_f32_16x16x32_bf16 v[76:79], a[144:147], a[16:19], v[76:79]\n" \
"v_exp_f32_e32 v70, v70\n" \
"v_mfma_f32_16x16x32_bf16 v[80:83], a[128:131], a[32:35], v[40:43]\n" \
"v_exp_f32_e32 v71, v71\n" \
"v_mfma_f32_16x16x32_bf16 v[80:83], a[132:135], a[36:39], v[80:83]\n" \
"v_exp_f32_e32 v72, v72\n" \
"v_mfma_f32_16x16x32_bf16 v[80:83], a[136:139], a[40:43], v[80:83]\n" \
"buffer_load_dwordx4 v53, s[8:11], s64 offen offset:0 sc1 lds\n" \
"v_mfma_f32_16x16x32_bf16 v[80:83], a[140:143], a[44:47], v[80:83]\n" \
"v_exp_f32_e32 v73, v73\n" \
"v_mfma_f32_16x16x32_bf16 v[84:87], a[156:159], a[44:47], v[40:43]\n" \
"v_exp_f32_e32 v74, v74\n" \
"v_mfma_f32_16x16x32_bf16 v[84:87], a[152:155], a[40:43], v[84:87]\n" \
"v_exp_f32_e32 v75, v75\n" \
"v_mfma_f32_16x16x32_bf16 v[84:87], a[148:151], a[36:39], v[84:87]\n" \
"v_exp_f32_e32 v76, v76\n" \
"v_mfma_f32_16x16x32_bf16 v[84:87], a[144:147], a[32:35], v[84:87]\n" \
"buffer_load_dwordx4 v53, s[8:11], s64 offen offset:1024 sc1 lds\n" \
"v_mfma_f32_16x16x32_bf16 v[88:91], a[128:131], a[48:51], v[44:47]\n" \
"ds_read_b128 a[128:131], v48 offset:8192\n" \
"v_add_u32_e32 v49, 0x10000, v48\n" \
"v_mfma_f32_16x16x32_bf16 v[88:91], a[132:135], a[52:55], v[88:91]\n" \
"ds_read_b128 a[132:135], v48 offset:9216\n" \
"s_xor_b32 s65, s65, 0x8000\n" \
"v_mfma_f32_16x16x32_bf16 v[88:91], a[136:139], a[56:59], v[88:91]\n" \
"v_exp_f32_e32 v77, v77\n" \
"v_mfma_f32_16x16x32_bf16 v[88:91], a[140:143], a[60:63], v[88:91]\n" \
"buffer_load_dwordx4 v53, s[8:11], s64 offen offset:2048 sc1 lds\n" \
"v_mfma_f32_16x16x32_bf16 v[92:95], a[156:159], a[60:63], v[44:47]\n" \
"ds_read_b128 a[136:139], v48 offset:10240\n" \
"ds_read_b128 a[140:143], v48 offset:11264\n" \
"v_mfma_f32_16x16x32_bf16 v[92:95], a[152:155], a[56:59], v[92:95]\n" \
"ds_read_b128 a[152:155], v48 offset:14336\n" \
"ds_read_b128 a[156:159], v48 offset:15360\n" \
"v_mfma_f32_16x16x32_bf16 v[92:95], a[148:151], a[52:55], v[92:95]\n" \
"v_exp_f32_e32 v78, v78\n" \
"v_mfma_f32_16x16x32_bf16 v[92:95], a[144:147], a[48:51], v[92:95]\n" \
"ds_read_b128 a[144:147], v48 offset:12288\n" \
"s_waitcnt lgkmcnt(7)\n" \
"v_mfma_f32_16x16x32_bf16 v[144:147], a[160:163], v[96:99], v[144:147]\n" \
"buffer_load_dwordx4 v53, s[8:11], s64 offen offset:3072 sc1 lds\n" \
"v_mfma_f32_16x16x32_bf16 v[144:147], a[176:179], v[100:103], v[144:147]\n" \
"s_add_u32 m0, s93, 0x1000\n" \
"ds_read_b128 a[148:151], v48 offset:13312\n" \
"v_mfma_f32_16x16x32_bf16 v[148:151], a[180:183], v[100:103], v[148:151]\n" \
"v_exp_f32_e32 v79, v79\n" \
"v_mfma_f32_16x16x32_bf16 v[148:151], a[164:167], v[96:99], v[148:151]\n" \
"v_exp_f32_e32 v80, v80\n" \
"v_mfma_f32_16x16x32_bf16 v[152:155], a[168:171], v[96:99], v[152:155]\n" \
"buffer_load_dwordx4 v4, s[80:83], s92 offen offset:0 lds\n" \
"v_mfma_f32_16x16x32_bf16 v[152:155], a[184:187], v[100:103], v[152:155]\n" \
"v_exp_f32_e32 v81, v81\n" \
"v_mfma_f32_16x16x32_bf16 v[156:159], a[188:191], v[100:103], v[156:159]\n" \
"v_exp_f32_e32 v82, v82\n" \
"v_mfma_f32_16x16x32_bf16 v[156:159], a[172:175], v[96:99], v[156:159]\n" \
"v_exp_f32_e32 v83, v83\n" \
"v_mfma_f32_16x16x32_bf16 v[176:179], a[160:163], v[104:107], v[176:179]\n" \
"v_exp_f32_e32 v84, v84\n" \
"v_mfma_f32_16x16x32_bf16 v[176:179], a[176:179], v[108:111], v[176:179]\n" \
"buffer_load_dwordx4 v5, s[80:83], s92 offen offset:1024 lds\n" \
"v_mfma_f32_16x16x32_bf16 v[180:183], a[180:183], v[108:111], v[180:183]\n" \
"v_exp_f32_e32 v85, v85\n" \
"v_mfma_f32_16x16x32_bf16 v[180:183], a[164:167], v[104:107], v[180:183]\n" \
"v_exp_f32_e32 v86, v86\n" \
"v_mfma_f32_16x16x32_bf16 v[184:187], a[168:171], v[104:107], v[184:187]\n" \
"v_exp_f32_e32 v87, v87\n" \
"v_mfma_f32_16x16x32_bf16 v[184:187], a[184:187], v[108:111], v[184:187]\n" \
"v_exp_f32_e32 v88, v88\n" \
"v_mfma_f32_16x16x32_bf16 v[188:191], a[188:191], v[108:111], v[188:191]\n" \
"buffer_load_dwordx4 v6, s[80:83], s92 offen offset:2048 lds\n" \
"v_mfma_f32_16x16x32_bf16 v[188:191], a[172:175], v[104:107], v[188:191]\n" \
"v_exp_f32_e32 v89, v89\n" \
"v_mfma_f32_16x16x32_bf16 v[208:211], a[160:163], v[112:115], v[208:211]\n" \
"v_exp_f32_e32 v90, v90\n" \
"v_mfma_f32_16x16x32_bf16 v[208:211], a[176:179], v[116:119], v[208:211]\n" \
"v_exp_f32_e32 v91, v91\n" \
"v_mfma_f32_16x16x32_bf16 v[212:215], a[180:183], v[116:119], v[212:215]\n" \
"buffer_load_dwordx4 v7, s[80:83], s92 offen offset:3072 lds\n" \
"v_mfma_f32_16x16x32_bf16 v[212:215], a[164:167], v[112:115], v[212:215]\n" \
"s_add_u32 m0, s93, 0x2000\n" \
"v_cvt_pk_bf16_f32 v96, v64, v65\n" \
"v_mfma_f32_16x16x32_bf16 v[216:219], a[168:171], v[112:115], v[216:219]\n" \
"v_cvt_pk_bf16_f32 v97, v66, v67\n" \
"v_cvt_pk_bf16_f32 v98, v68, v69\n" \
"v_mfma_f32_16x16x32_bf16 v[216:219], a[184:187], v[116:119], v[216:219]\n" \
"v_cvt_pk_bf16_f32 v99, v70, v71\n" \
"v_cvt_pk_bf16_f32 v104, v72, v73\n" \
"v_mfma_f32_16x16x32_bf16 v[220:223], a[188:191], v[116:119], v[220:223]\n" \
"v_cvt_pk_bf16_f32 v105, v74, v75\n" \
"v_cvt_pk_bf16_f32 v106, v76, v77\n" \
"v_mfma_f32_16x16x32_bf16 v[220:223], a[172:175], v[112:115], v[220:223]\n" \
"buffer_load_dwordx4 v8, s[80:83], s92 offen offset:0 lds\n" \
"v_mfma_f32_16x16x32_bf16 v[240:243], a[160:163], v[120:123], v[240:243]\n" \
"v_cvt_pk_bf16_f32 v107, v78, v79\n" \
"v_cvt_pk_bf16_f32 v112, v80, v81\n" \
"v_mfma_f32_16x16x32_bf16 v[240:243], a[176:179], v[124:127], v[240:243]\n" \
"v_cvt_pk_bf16_f32 v113, v82, v83\n" \
"v_cvt_pk_bf16_f32 v114, v84, v85\n" \
"v_mfma_f32_16x16x32_bf16 v[244:247], a[180:183], v[124:127], v[244:247]\n" \
"v_cvt_pk_bf16_f32 v115, v86, v87\n" \
"v_add_u32_e32 v48, s16, v20\n" \
"v_mfma_f32_16x16x32_bf16 v[244:247], a[164:167], v[120:123], v[244:247]\n" \
"s_add_u32 s64, s64, 0x10000\n" \
"v_mfma_f32_16x16x32_bf16 v[248:251], a[168:171], v[120:123], v[248:251]\n" \
"v_mfma_f32_16x16x32_bf16 v[248:251], a[184:187], v[124:127], v[248:251]\n" \
"v_mfma_f32_16x16x32_bf16 v[252:255], a[188:191], v[124:127], v[252:255]\n" \
"v_mfma_f32_16x16x32_bf16 v[252:255], a[172:175], v[120:123], v[252:255]\n" \
"v_smfmac_f32_16x16x64_bf16 a[204:207], a[212:215], v[120:127], v21\n" \
"s_waitcnt vmcnt(13) lgkmcnt(0)\n" \
"s_mov_b64 exec, 0\n" \
"s_cmp_lg_u32 s15, 0\n" \
"s_cbranch_scc1 L_pf21\n" \
"s_branch L_pf20\n" \
"L_retB:\n" \
"s_mov_b64 exec, -1\n" \
"s_barrier\n" \
"s_branch L_2_%=\n" \
"L_t2_%=:\n" \
"v_mfma_f32_16x16x32_bf16 v[252:255], a[188:191], v[124:127], v[252:255]\n" \
"v_cvt_pk_bf16_f32 v114, v84, v85\n" \
"v_cvt_pk_bf16_f32 v115, v86, v87\n" \
"v_mfma_f32_16x16x32_bf16 v[252:255], a[172:175], v[120:123], v[252:255]\n" \
"v_xor_b32_e32 v48, 0x8000, v48\n" \
"v_smfmac_f32_16x16x64_bf16 a[204:207], a[212:215], v[120:127], v21\n" \
"s_waitcnt lgkmcnt(0)\n" \
"L_2_%=:\n" \
"v_mfma_f32_16x16x32_bf16 v[64:67], a[128:131], a[64:67], v[32:35]\n" \
"ds_read_b128 a[160:163], v49 offset:0\n" \
"ds_read_b128 a[176:179], v49 offset:4096\n" \
"v_mfma_f32_16x16x32_bf16 v[64:67], a[132:135], a[68:71], v[64:67]\n" \
"v_cvt_pk_bf16_f32 v120, v88, v89\n" \
"v_cvt_pk_bf16_f32 v121, v90, v91\n" \
"v_mfma_f32_16x16x32_bf16 v[64:67], a[136:139], a[72:75], v[64:67]\n" \
"ds_read_b128 a[180:183], v49 offset:5120\n" \
"ds_read_b128 a[164:167], v49 offset:1024\n" \
"v_mfma_f32_16x16x32_bf16 v[64:67], a[140:143], a[76:79], v[64:67]\n" \
"v_exp_f32_e32 v92, v92\n" \
"v_mfma_f32_16x16x32_bf16 v[68:71], a[156:159], a[76:79], v[32:35]\n" \
"ds_read_b128 a[168:171], v49 offset:2048\n" \
"ds_read_b128 a[184:187], v49 offset:6144\n" \
"v_mfma_f32_16x16x32_bf16 v[68:71], a[152:155], a[72:75], v[68:71]\n" \
"v_exp_f32_e32 v93, v93\n" \
"v_mfma_f32_16x16x32_bf16 v[68:71], a[148:151], a[68:71], v[68:71]\n" \
"ds_read_b128 a[188:191], v49 offset:7168\n" \
"ds_read_b128 a[172:175], v49 offset:3072\n" \
"v_mfma_f32_16x16x32_bf16 v[68:71], a[144:147], a[64:67], v[68:71]\n" \
"v_exp_f32_e32 v94, v94\n" \
"v_mfma_f32_16x16x32_bf16 v[72:75], a[128:131], a[80:83], v[36:39]\n" \
"v_exp_f32_e32 v95, v95\n" \
"v_mfma_f32_16x16x32_bf16 v[72:75], a[132:135], a[84:87], v[72:75]\n" \
"v_cvt_pk_bf16_f32 v122, v92, v93\n" \
"v_cvt_pk_bf16_f32 v123, v94, v95\n" \
"v_mfma_f32_16x16x32_bf16 v[72:75], a[136:139], a[88:91], v[72:75]\n" \
"v_exp_f32_e32 v64, v64\n" \
"v_mfma_f32_16x16x32_bf16 v[72:75], a[140:143], a[92:95], v[72:75]\n" \
"v_exp_f32_e32 v65, v65\n" \
"v_mfma_f32_16x16x32_bf16 v[76:79], a[156:159], a[92:95], v[36:39]\n" \
"v_exp_f32_e32 v66, v66\n" \
"v_mfma_f32_16x16x32_bf16 v[76:79], a[152:155], a[88:91], v[76:79]\n" \
"v_exp_f32_e32 v67, v67\n" \
"v_mfma_f32_16x16x32_bf16 v[76:79], a[148:151], a[84:87], v[76:79]\n" \
"v_exp_f32_e32 v68, v68\n" \
"v_mfma_f32_16x16x32_bf16 v[76:79], a[144:147], a[80:83], v[76:79]\n" \
"v_exp_f32_e32 v69, v69\n" \
"v_mfma_f32_16x16x32_bf16 v[80:83], a[128:131], a[96:99], v[40:43]\n" \
"v_exp_f32_e32 v70, v70\n" \
"v_mfma_f32_16x16x32_bf16 v[80:83], a[132:135], a[100:103], v[80:83]\n" \
"v_exp_f32_e32 v71, v71\n" \
"v_mfma_f32_16x16x32_bf16 v[80:83], a[136:139], a[104:107], v[80:83]\n" \
"v_cvt_pk_bf16_f32 v100, v64, v65\n" \
"v_cvt_pk_bf16_f32 v101, v66, v67\n" \
"v_mfma_f32_16x16x32_bf16 v[80:83], a[140:143], a[108:111], v[80:83]\n" \
"v_cvt_pk_bf16_f32 v102, v68, v69\n" \
"v_cvt_pk_bf16_f32 v103, v70, v71\n" \
"v_mfma_f32_16x16x32_bf16 v[84:87], a[156:159], a[108:111], v[40:43]\n" \
"v_exp_f32_e32 v72, v72\n" \
"v_mfma_f32_16x16x32_bf16 v[84:87], a[152:155], a[104:107], v[84:87]\n" \
"v_exp_f32_e32 v73, v73\n" \
"v_mfma_f32_16x16x32_bf16 v[84:87], a[148:151], a[100:103], v[84:87]\n" \
"v_exp_f32_e32 v74, v74\n" \
"v_mfma_f32_16x16x32_bf16 v[84:87], a[144:147], a[96:99], v[84:87]\n" \
"v_exp_f32_e32 v75, v75\n" \
"v_mfma_f32_16x16x32_bf16 v[88:91], a[128:131], a[112:115], v[44:47]\n" \
"v_exp_f32_e32 v76, v76\n" \
"v_mfma_f32_16x16x32_bf16 v[88:91], a[132:135], a[116:119], v[88:91]\n" \
"v_exp_f32_e32 v77, v77\n" \
"v_mfma_f32_16x16x32_bf16 v[88:91], a[136:139], a[120:123], v[88:91]\n" \
"v_exp_f32_e32 v78, v78\n" \
"v_mfma_f32_16x16x32_bf16 v[88:91], a[140:143], a[124:127], v[88:91]\n" \
"v_exp_f32_e32 v79, v79\n" \
"v_mfma_f32_16x16x32_bf16 v[92:95], a[156:159], a[124:127], v[44:47]\n" \
"v_cvt_pk_bf16_f32 v108, v72, v73\n" \
"v_cvt_pk_bf16_f32 v109, v74, v75\n" \
"v_mfma_f32_16x16x32_bf16 v[92:95], a[152:155], a[120:123], v[92:95]\n" \
"v_cvt_pk_bf16_f32 v110, v76, v77\n" \
"v_cvt_pk_bf16_f32 v111, v78, v79\n" \
"v_mfma_f32_16x16x32_bf16 v[92:95], a[148:151], a[116:119], v[92:95]\n" \
"ds_read_b128 a[128:131], v48 offset:0\n" \
"ds_read_b128 a[132:135], v48 offset:1024\n" \
"v_mfma_f32_16x16x32_bf16 v[92:95], a[144:147], a[112:115], v[92:95]\n" \
"s_waitcnt lgkmcnt(2)\n" \
"v_mfma_f32_16x16x32_bf16 v[128:131], a[160:163], v[96:99], v[128:131]\n" \
"ds_read_b128 a[136:139], v48 offset:2048\n" \
"ds_read_b128 a[140:143], v48 offset:3072\n" \
"v_mfma_f32_16x16x32_bf16 v[128:131], a[176:179], v[100:103], v[128:131]\n" \
"v_exp_f32_e32 v80, v80\n" \
"v_mfma_f32_16x16x32_bf16 v[132:135], a[180:183], v[100:103], v[132:135]\n" \
"ds_read_b128 a[144:147], v48 offset:4096\n" \
"ds_read_b128 a[148:151], v48 offset:5120\n" \
"v_mfma_f32_16x16x32_bf16 v[132:135], a[164:167], v[96:99], v[132:135]\n" \
"v_exp_f32_e32 v81, v81\n" \
"v_mfma_f32_16x16x32_bf16 v[136:139], a[168:171], v[96:99], v[136:139]\n" \
"ds_read_b128 a[152:155], v48 offset:6144\n" \
"ds_read_b128 a[156:159], v48 offset:7168\n" \
"v_mfma_f32_16x16x32_bf16 v[136:139], a[184:187], v[100:103], v[136:139]\n" \
"v_exp_f32_e32 v82, v82\n" \
"v_mfma_f32_16x16x32_bf16 v[140:143], a[188:191], v[100:103], v[140:143]\n" \
"v_exp_f32_e32 v83, v83\n" \
"v_mfma_f32_16x16x32_bf16 v[140:143], a[172:175], v[96:99], v[140:143]\n" \
"v_exp_f32_e32 v84, v84\n" \
"v_smfmac_f32_16x16x64_bf16 a[192:195], a[212:215], v[96:103], v21\n" \
"v_mfma_f32_16x16x32_bf16 v[160:163], a[160:163], v[104:107], v[160:163]\n" \
"v_exp_f32_e32 v85, v85\n" \
"v_mfma_f32_16x16x32_bf16 v[160:163], a[176:179], v[108:111], v[160:163]\n" \
"v_exp_f32_e32 v86, v86\n" \
"v_mfma_f32_16x16x32_bf16 v[164:167], a[180:183], v[108:111], v[164:167]\n" \
"v_exp_f32_e32 v87, v87\n" \
"v_mfma_f32_16x16x32_bf16 v[164:167], a[164:167], v[104:107], v[164:167]\n" \
"v_cvt_pk_bf16_f32 v116, v80, v81\n" \
"v_cvt_pk_bf16_f32 v117, v82, v83\n" \
"v_mfma_f32_16x16x32_bf16 v[168:171], a[168:171], v[104:107], v[168:171]\n" \
"v_cvt_pk_bf16_f32 v118, v84, v85\n" \
"v_cvt_pk_bf16_f32 v119, v86, v87\n" \
"v_mfma_f32_16x16x32_bf16 v[168:171], a[184:187], v[108:111], v[168:171]\n" \
"v_exp_f32_e32 v88, v88\n" \
"v_mfma_f32_16x16x32_bf16 v[172:175], a[188:191], v[108:111], v[172:175]\n" \
"v_exp_f32_e32 v89, v89\n" \
"v_mfma_f32_16x16x32_bf16 v[172:175], a[172:175], v[104:107], v[172:175]\n" \
"v_exp_f32_e32 v90, v90\n" \
"v_smfmac_f32_16x16x64_bf16 a[196:199], a[212:215], v[104:111], v21\n" \
"v_mfma_f32_16x16x32_bf16 v[192:195], a[160:163], v[112:115], v[192:195]\n" \
"v_exp_f32_e32 v91, v91\n" \
"v_mfma_f32_16x16x32_bf16 v[192:195], a[176:179], v[116:119], v[192:195]\n" \
"v_exp_f32_e32 v92, v92\n" \
"v_mfma_f32_16x16x32_bf16 v[196:199], a[180:183], v[116:119], v[196:199]\n" \
"v_exp_f32_e32 v93, v93\n" \
"v_mfma_f32_16x16x32_bf16 v[196:199], a[164:167], v[112:115], v[196:199]\n" \
"v_exp_f32_e32 v94, v94\n" \
"v_mfma_f32_16x16x32_bf16 v[200:203], a[168:171], v[112:115], v[200:203]\n" \
"v_exp_f32_e32 v95, v95\n" \
"v_mfma_f32_16x16x32_bf16 v[200:203], a[184:187], v[116:119], v[200:203]\n" \
"v_cvt_pk_bf16_f32 v124, v88, v89\n" \
"v_cvt_pk_bf16_f32 v125, v90, v91\n" \
"v_mfma_f32_16x16x32_bf16 v[204:207], a[188:191], v[116:119], v[204:207]\n" \
"v_cvt_pk_bf16_f32 v126, v92, v93\n" \
"v_cvt_pk_bf16_f32 v127, v94, v95\n" \
"v_mfma_f32_16x16x32_bf16 v[204:207], a[172:175], v[112:115], v[204:207]\n" \
"v_smfmac_f32_16x16x64_bf16 a[200:203], a[212:215], v[112:119], v21\n" \
"s_waitcnt vmcnt(0) lgkmcnt(0)\n" \
"s_barrier\n" \
"v_mfma_f32_16x16x32_bf16 v[224:227], a[160:163], v[120:123], v[224:227]\n" \
"s_add_u32 s63, s63, s63\n" \
"s_cbranch_scc1 L_h2_%=\n" \
"L_hr2_%=:\n" \
"v_mfma_f32_16x16x32_bf16 v[224:227], a[176:179], v[124:127], v[224:227]\n" \
"s_xor_b32 m0, s65, 0x12000\n" \
"ds_read_b128 a[160:163], v49 offset:8192\n" \
"v_mfma_f32_16x16x32_bf16 v[228:231], a[180:183], v[124:127], v[228:231]\n" \
"buffer_load_dwordx4 v50, s[8:11], s64 offen offset:0 sc1 lds\n" \
"v_mfma_f32_16x16x32_bf16 v[228:231], a[164:167], v[120:123], v[228:231]\n" \
"ds_read_b128 a[176:179], v49 offset:12288\n" \
"ds_read_b128 a[180:183], v49 offset:13312\n" \
"v_mfma_f32_16x16x32_bf16 v[232:235], a[168:171], v[120:123], v[232:235]\n" \
"v_mfma_f32_16x16x32_bf16 v[232:235], a[184:187], v[124:127], v[232:235]\n" \
"buffer_load_dwordx4 v50, s[8:11], s64 offen offset:1024 sc1 lds\n" \
"v_mfma_f32_16x16x32_bf16 v[236:239], a[188:191], v[124:127], v[236:239]\n" \
"ds_read_b128 a[164:167], v49 offset:9216\n" \
"ds_read_b128 a[168:171], v49 offset:10240\n" \
"v_mfma_f32_16x16x32_bf16 v[236:239], a[172:175], v[120:123], v[236:239]\n" \
"v_mfma_f32_16x16x32_bf16 v[64:67], a[128:131], a[64:67], v[32:35]\n" \
"ds_read_b128 a[184:187], v49 offset:14336\n" \
"ds_read_b128 a[188:191], v49 offset:15360\n" \
"v_mfma_f32_16x16x32_bf16 v[64:67], a[132:135], a[68:71], v[64:67]\n" \
"buffer_load_dwordx4 v50, s[8:11], s64 offen offset:2048 sc1 lds\n" \
"v_mfma_f32_16x16x32_bf16 v[64:67], a[136:139], a[72:75], v[64:67]\n" \
"ds_read_b128 a[172:175], v49 offset:11264\n" \
"v_add_u32_e32 v49, 0x10000, v48\n" \
"v_mfma_f32_16x16x32_bf16 v[64:67], a[140:143], a[76:79], v[64:67]\n" \
"v_mfma_f32_16x16x32_bf16 v[68:71], a[156:159], a[76:79], v[32:35]\n" \
"v_mfma_f32_16x16x32_bf16 v[68:71], a[152:155], a[72:75], v[68:71]\n" \
"v_mfma_f32_16x16x32_bf16 v[68:71], a[148:151], a[68:71], v[68:71]\n" \
"buffer_load_dwordx4 v50, s[8:11], s64 offen offset:3072 sc1 lds\n" \
"v_mfma_f32_16x16x32_bf16 v[68:71], a[144:147], a[64:67], v[68:71]\n" \
"s_xor_b32 m0, s65, 0xa000\n" \
"v_mfma_f32_16x16x32_bf16 v[72:75], a[128:131], a[80:83], v[36:39]\n" \
"v_mfma_f32_16x16x32_bf16 v[72:75], a[132:135], a[84:87], v[72:75]\n" \
"v_exp_f32_e32 v64, v64\n" \
"v_mfma_f32_16x16x32_bf16 v[72:75], a[136:139], a[88:91], v[72:75]\n" \
"buffer_load_dwordx4 v51, s[8:11], s64 offen offset:0 sc1 lds\n" \
"v_mfma_f32_16x16x32_bf16 v[72:75], a[140:143], a[92:95], v[72:75]\n" \
"v_exp_f32_e32 v65, v65\n" \
"v_mfma_f32_16x16x32_bf16 v[76:79], a[156:159], a[92:95], v[36:39]\n" \
"v_exp_f32_e32 v66, v66\n" \
"v_mfma_f32_16x16x32_bf16 v[76:79], a[152:155], a[88:91], v[76:79]\n" \
"v_exp_f32_e32 v67, v67\n" \
"v_mfma_f32_16x16x32_bf16 v[76:79], a[148:151], a[84:87], v[76:79]\n" \
"buffer_load_dwordx4 v51, s[8:11], s64 offen offset:1024 sc1 lds\n" \
"v_mfma_f32_16x16x32_bf16 v[76:79], a[144:147], a[80:83], v[76:79]\n" \
"v_exp_f32_e32 v68, v68\n" \
"v_mfma_f32_16x16x32_bf16 v[80:83], a[128:131], a[96:99], v[40:43]\n" \
"v_exp_f32_e32 v69, v69\n" \
"v_mfma_f32_16x16x32_bf16 v[80:83], a[132:135], a[100:103], v[80:83]\n" \
"v_exp_f32_e32 v70, v70\n" \
"v_mfma_f32_16x16x32_bf16 v[80:83], a[136:139], a[104:107], v[80:83]\n" \
"v_exp_f32_e32 v71, v71\n" \
"v_mfma_f32_16x16x32_bf16 v[80:83], a[140:143], a[108:111], v[80:83]\n" \
"buffer_load_dwordx4 v51, s[8:11], s64 offen offset:2048 sc1 lds\n" \
"v_mfma_f32_16x16x32_bf16 v[84:87], a[156:159], a[108:111], v[40:43]\n" \
"v_exp_f32_e32 v72, v72\n" \
"v_mfma_f32_16x16x32_bf16 v[84:87], a[152:155], a[104:107], v[84:87]\n" \
"v_exp_f32_e32 v73, v73\n" \
"v_mfma_f32_16x16x32_bf16 v[84:87], a[148:151], a[100:103], v[84:87]\n" \
"v_exp_f32_e32 v74, v74\n" \
"v_mfma_f32_16x16x32_bf16 v[84:87], a[144:147], a[96:99], v[84:87]\n" \
"buffer_load_dwordx4 v51, s[8:11], s64 offen offset:3072 sc1 lds\n" \
"v_mfma_f32_16x16x32_bf16 v[88:91], a[128:131], a[112:115], v[44:47]\n" \
"s_xor_b32 m0, s65, 0x18000\n" \
"ds_read_b128 a[128:131], v48 offset:8192\n" \
"v_mfma_f32_16x16x32_bf16 v[88:91], a[132:135], a[116:119], v[88:91]\n" \
"v_exp_f32_e32 v75, v75\n" \
"v_mfma_f32_16x16x32_bf16 v[88:91], a[136:139], a[120:123], v[88:91]\n" \
"ds_read_b128 a[132:135], v48 offset:9216\n" \
"ds_read_b128 a[136:139], v48 offset:10240\n" \
"v_mfma_f32_16x16x32_bf16 v[88:91], a[140:143], a[124:127], v[88:91]\n" \
"v_exp_f32_e32 v76, v76\n" \
"v_mfma_f32_16x16x32_bf16 v[92:95], a[156:159], a[124:127], v[44:47]\n" \
"buffer_load_dwordx4 v52, s[8:11], s64 offen offset:0 sc1 lds\n" \
"v_mfma_f32_16x16x32_bf16 v[92:95], a[152:155], a[120:123], v[92:95]\n" \
"ds_read_b128 a[140:143], v48 offset:11264\n" \
"ds_read_b128 a[152:155], v48 offset:14336\n" \
"v_mfma_f32_16x16x32_bf16 v[92:95], a[148:151], a[116:119], v[92:95]\n" \
"v_exp_f32_e32 v77, v77\n" \
"v_mfma_f32_16x16x32_bf16 v[92:95], a[144:147], a[112:115], v[92:95]\n" \
"ds_read_b128 a[144:147], v48 offset:12288\n" \
"s_waitcnt lgkmcnt(6)\n" \
"v_mfma_f32_16x16x32_bf16 v[144:147], a[160:163], v[96:99], v[144:147]\n" \
"buffer_load_dwordx4 v52, s[8:11], s64 offen offset:1024 sc1 lds\n" \
"v_mfma_f32_16x16x32_bf16 v[144:147], a[176:179], v[100:103], v[144:147]\n" \
"ds_read_b128 a[148:151], v48 offset:13312\n" \
"ds_read_b128 a[156:159], v48 offset:15360\n" \
"v_mfma_f32_16x16x32_bf16 v[148:151], a[180:183], v[100:103], v[148:151]\n" \
"v_exp_f32_e32 v78, v78\n" \
"v_mfma_f32_16x16x32_bf16 v[148:151], a[164:167], v[96:99], v[148:151]\n" \
"v_exp_f32_e32 v79, v79\n" \
"v_mfma_f32_16x16x32_bf16 v[152:155], a[168:171], v[96:99], v[152:155]\n" \
"buffer_load_dwordx4 v52, s[8:11], s64 offen offset:2048 sc1 lds\n" \
"v_mfma_f32_16x16x32_bf16 v[152:155], a[184:187], v[100:103], v[152:155]\n" \
"v_exp_f32_e32 v80, v80\n" \
"v_mfma_f32_16x16x32_bf16 v[156:159], a[188:191], v[100:103], v[156:159]\n" \
"v_exp_f32_e32 v81, v81\n" \
"v_mfma_f32_16x16x32_bf16 v[156:159], a[172:175], v[96:99], v[156:159]\n" \
"v_exp_f32_e32 v82, v82\n" \
"v_mfma_f32_16x16x32_bf16 v[176:179], a[160:163], v[104:107], v[176:179]\n" \
"v_exp_f32_e32 v83, v83\n" \
"v_mfma_f32_16x16x32_bf16 v[176:179], a[176:179], v[108:111], v[176:179]\n" \
"buffer_load_dwordx4 v52, s[8:11], s64 offen offset:3072 sc1 lds\n" \
"v_mfma_f32_16x16x32_bf16 v[180:183], a[180:183], v[108:111], v[180:183]\n" \
"s_mov_b32 m0, s65\n" \
"s_xor_b32 s65, s65, 0x8000\n" \
"v_mfma_f32_16x16x32_bf16 v[180:183], a[164:167], v[104:107], v[180:183]\n" \
"v_exp_f32_e32 v84, v84\n" \
"v_mfma_f32_16x16x32_bf16 v[184:187], a[168:171], v[104:107], v[184:187]\n" \
"v_exp_f32_e32 v85, v85\n" \
"v_mfma_f32_16x16x32_bf16 v[184:187], a[184:187], v[108:111], v[184:187]\n" \
"buffer_load_dwordx4 v53, s[8:11], s64 offen offset:0 sc1 lds\n" \
"v_mfma_f32_16x16x32_bf16 v[188:191], a[188:191], v[108:111], v[188:191]\n" \
"v_exp_f32_e32 v86, v86\n" \
"v_mfma_f32_16x16x32_bf16 v[188:191], a[172:175], v[104:107], v[188:191]\n" \
"v_exp_f32_e32 v87, v87\n" \
"v_mfma_f32_16x16x32_bf16 v[208:211], a[160:163], v[112:115], v[208:211]\n" \
"v_exp_f32_e32 v88, v88\n" \
"v_mfma_f32_16x16x32_bf16 v[208:211], a[176:179], v[116:119], v[208:211]\n" \
"buffer_load_dwordx4 v53, s[8:11], s64 offen offset:1024 sc1 lds\n" \
"v_mfma_f32_16x16x32_bf16 v[212:215], a[180:183], v[116:119], v[212:215]\n" \
"v_exp_f32_e32 v89, v89\n" \
"v_mfma_f32_16x16x32_bf16 v[212:215], a[164:167], v[112:115], v[212:215]\n" \
"v_exp_f32_e32 v90, v90\n" \
"v_mfma_f32_16x16x32_bf16 v[216:219], a[168:171], v[112:115], v[216:219]\n" \
"v_exp_f32_e32 v91, v91\n" \
"v_mfma_f32_16x16x32_bf16 v[216:219], a[184:187], v[116:119], v[216:219]\n" \
"v_cvt_pk_bf16_f32 v96, v64, v65\n" \
"v_cvt_pk_bf16_f32 v97, v66, v67\n" \
"v_mfma_f32_16x16x32_bf16 v[220:223], a[188:191], v[116:119], v[220:223]\n" \
"buffer_load_dwordx4 v53, s[8:11], s64 offen offset:2048 sc1 lds\n" \
"v_mfma_f32_16x16x32_bf16 v[220:223], a[172:175], v[112:115], v[220:223]\n" \
"v_cvt_pk_bf16_f32 v98, v68, v69\n" \
"v_cvt_pk_bf16_f32 v99, v70, v71\n" \
"v_mfma_f32_16x16x32_bf16 v[240:243], a[160:163], v[120:123], v[240:243]\n" \
"v_cvt_pk_bf16_f32 v104, v72, v73\n" \
"v_cvt_pk_bf16_f32 v105, v74, v75\n" \
"v_mfma_f32_16x16x32_bf16 v[240:243], a[176:179], v[124:127], v[240:243]\n" \
"v_cvt_pk_bf16_f32 v106, v76, v77\n" \
"v_cvt_pk_bf16_f32 v107, v78, v79\n" \
"v_mfma_f32_16x16x32_bf16 v[244:247], a[180:183], v[124:127], v[244:247]\n" \
"buffer_load_dwordx4 v53, s[8:11], s64 offen offset:3072 sc1 lds\n" \
"v_mfma_f32_16x16x32_bf16 v[244:247], a[164:167], v[120:123], v[244:247]\n" \
"s_add_u32 s64, s64, 0x10000\n" \
"s_sub_u32 s66, s66, 1\n" \
"v_mfma_f32_16x16x32_bf16 v[248:251], a[168:171], v[120:123], v[248:251]\n" \
"v_cvt_pk_bf16_f32 v112, v80, v81\n" \
"v_cvt_pk_bf16_f32 v113, v82, v83\n" \
"v_mfma_f32_16x16x32_bf16 v[248:251], a[184:187], v[124:127], v[248:251]\n" \
"s_cbranch_scc0 L_t2_%=\n" \
"v_mfma_f32_16x16x32_bf16 v[252:255], a[188:191], v[124:127], v[252:255]\n" \
"v_cvt_pk_bf16_f32 v114, v84, v85\n" \
"v_cvt_pk_bf16_f32 v115, v86, v87\n" \
"v_mfma_f32_16x16x32_bf16 v[252:255], a[172:175], v[120:123], v[252:255]\n" \
"v_xor_b32_e32 v48, 0x8000, v48\n" \
"v_smfmac_f32_16x16x64_bf16 a[204:207], a[212:215], v[120:127], v21\n" \
"s_waitcnt lgkmcnt(0)\n" \
"s_waitcnt vmcnt(12)\n" \
"s_barrier\n" \
"s_cmp_lg_u32 s16, 0\n" \
"s_cbranch_scc1 L_f1_%=\n" \
"ds_read_b128 a[160:163], v49 offset:0\n" \
"ds_read_b128 a[164:167], v49 offset:1024\n" \
"ds_read_b128 a[168:171], v49 offset:2048\n" \
"ds_read_b128 a[172:175], v49 offset:3072\n" \
"ds_read_b128 a[176:179], v49 offset:8192\n" \
"ds_read_b128 a[180:183], v49 offset:9216\n" \
"v_cvt_pk_bf16_f32 v120, v88, v89\n" \
"v_cvt_pk_bf16_f32 v121, v90, v91\n" \
"v_mfma_f32_16x16x32_bf16 v[88:91], a[128:131], a[112:115], v[44:47]\n" \
"s_add_u32 s70, s61, 0x0\n" \
"s_add_u32 s71, s61, 0x49000\n" \
"ds_read_b128 a[184:187], v49 offset:10240\n" \
"v_mfma_f32_16x16x32_bf16 v[88:91], a[132:135], a[116:119], v[88:91]\n" \
"ds_read_b128 a[188:191], v49 offset:11264\n" \
"v_add_u32_e32 v54, 0x49000, v20\n" \
"v_mfma_f32_16x16x32_bf16 v[88:91], a[136:139], a[120:123], v[88:91]\n" \
"v_exp_f32_e32 v92, v92\n" \
"v_mfma_f32_16x16x32_bf16 v[88:91], a[140:143], a[124:127], v[88:91]\n" \
"v_exp_f32_e32 v93, v93\n" \
"v_exp_f32_e32 v94, v94\n" \
"v_exp_f32_e32 v95, v95\n" \
"v_cvt_pk_bf16_f32 v122, v92, v93\n" \
"v_cvt_pk_bf16_f32 v123, v94, v95\n" \
"v_mfma_f32_16x16x32_bf16 v[92:95], a[156:159], a[124:127], v[44:47]\n" \
"buffer_load_dwordx4 a[220:223], v20, s[56:59], s60 offen offset:0 sc1\n" \
"v_mfma_f32_16x16x32_bf16 v[92:95], a[152:155], a[120:123], v[92:95]\n" \
"v_readlane_b32 s97, v11, 32\n" \
"v_readlane_b32 s98, v11, 33\n" \
"v_mfma_f32_16x16x32_bf16 v[92:95], a[148:151], a[116:119], v[92:95]\n" \
"v_and_b32_e32 v112, v12, v112\n" \
"v_and_b32_e32 v113, v13, v113\n" \
"v_mfma_f32_16x16x32_bf16 v[92:95], a[144:147], a[112:115], v[92:95]\n" \
"v_and_b32_e32 v114, v14, v114\n" \
"s_waitcnt lgkmcnt(7)\n" \
"v_mfma_f32_16x16x32_bf16 v[128:131], a[160:163], v[96:99], v[128:131]\n" \
"v_and_b32_e32 v115, v15, v115\n" \
"s_waitcnt lgkmcnt(6)\n" \
"v_mfma_f32_16x16x32_bf16 v[132:135], a[164:167], v[96:99], v[132:135]\n" \
"ds_read_b32 v60, v18 offset:0\n" \
"s_waitcnt lgkmcnt(6)\n" \
"v_mfma_f32_16x16x32_bf16 v[136:139], a[168:171], v[96:99], v[136:139]\n" \
"ds_read_b32 v61, v18 offset:128\n" \
"s_waitcnt lgkmcnt(6)\n" \
"v_mfma_f32_16x16x32_bf16 v[140:143], a[172:175], v[96:99], v[140:143]\n" \
"ds_read_b128 a[0:3], v49 offset:4096\n" \
"s_waitcnt lgkmcnt(5)\n" \
"v_mfma_f32_16x16x32_bf16 v[144:147], a[176:179], v[96:99], v[144:147]\n" \
"buffer_load_dwordx4 a[236:239], v54, s[56:59], s60 offen offset:0 sc1\n" \
"v_mfma_f32_16x16x32_bf16 v[148:151], a[180:183], v[96:99], v[148:151]\n" \
"ds_read_b128 a[4:7], v49 offset:5120\n" \
"s_waitcnt lgkmcnt(5)\n" \
"v_mfma_f32_16x16x32_bf16 v[152:155], a[184:187], v[96:99], v[152:155]\n" \
"ds_read_b128 a[8:11], v49 offset:6144\n" \
"s_waitcnt lgkmcnt(5)\n" \
"v_mfma_f32_16x16x32_bf16 v[156:159], a[188:191], v[96:99], v[156:159]\n" \
"buffer_load_dwordx4 a[224:227], v20, s[56:59], s60 offen offset:1024 sc1\n" \
"v_mfma_f32_16x16x32_bf16 a[192:195], a[252:255], v[96:99], a[192:195]\n" \
"ds_read_b128 a[12:15], v49 offset:7168\n" \
"ds_read_b128 a[16:19], v49 offset:12288\n" \
"v_mfma_f32_16x16x32_bf16 v[160:163], a[160:163], v[104:107], v[160:163]\n" \
"ds_read_b128 a[20:23], v49 offset:13312\n" \
"ds_read_b128 a[24:27], v49 offset:14336\n" \
"buffer_load_dwordx4 a[240:243], v54, s[56:59], s60 offen offset:1024 sc1\n" \
"v_mfma_f32_16x16x32_bf16 v[164:167], a[164:167], v[104:107], v[164:167]\n" \
"ds_read_b128 a[28:31], v49 offset:15360\n" \
"v_accvgpr_read_b32 v40, a192\n" \
"v_mfma_f32_16x16x32_bf16 v[168:171], a[168:171], v[104:107], v[168:171]\n" \
"v_accvgpr_read_b32 v23, a193\n" \
"v_add_f32_e32 v40, v40, v23\n" \
"v_mfma_f32_16x16x32_bf16 v[172:175], a[172:175], v[104:107], v[172:175]\n" \
"buffer_load_dwordx4 a[228:231], v20, s[56:59], s60 offen offset:2048 sc1\n" \
"v_mfma_f32_16x16x32_bf16 v[176:179], a[176:179], v[104:107], v[176:179]\n" \
"v_add_u32_e32 v50, s15, v48\n" \
"v_add_u32_e32 v51, 0x10000, v50\n" \
"v_mfma_f32_16x16x32_bf16 v[180:183], a[180:183], v[104:107], v[180:183]\n" \
"s_waitcnt vmcnt(5)\n" \
"ds_write_b128 v50, v[128:131] offset:0\n" \
"buffer_load_dwordx4 a[244:247], v54, s[56:59], s60 offen offset:2048 sc1\n" \
"v_mfma_f32_16x16x32_bf16 v[184:187], a[184:187], v[104:107], v[184:187]\n" \
"ds_write_b128 v50, v[132:135] offset:1024\n" \
"v_mfma_f32_16x16x32_bf16 v[188:191], a[188:191], v[104:107], v[188:191]\n" \
"ds_write_b128 v50, v[136:139] offset:2048\n" \
"buffer_load_dwordx4 a[232:235], v20, s[56:59], s60 offen offset:3072 sc1\n" \
"v_mfma_f32_16x16x32_bf16 a[196:199], a[252:255], v[104:107], a[196:199]\n" \
"ds_write_b128 v50, v[140:143] offset:3072\n" \
"v_mfma_f32_16x16x32_bf16 v[192:195], a[160:163], v[112:115], v[192:195]\n" \
"ds_write_b128 v50, v[144:147] offset:8192\n" \
"buffer_load_dwordx4 a[248:251], v54, s[56:59], s60 offen offset:3072 sc1\n" \
"v_mfma_f32_16x16x32_bf16 v[196:199], a[164:167], v[112:115], v[196:199]\n" \
"ds_write_b128 v50, v[148:151] offset:9216\n" \
"v_mfma_f32_16x16x32_bf16 v[200:203], a[168:171], v[112:115], v[200:203]\n" \
"ds_write_b128 v50, v[152:155] offset:10240\n" \
"buffer_load_dwordx4 a[32:35], v20, s[56:59], s97 offen offset:0 sc1\n" \
"v_mfma_f32_16x16x32_bf16 v[204:207], a[172:175], v[112:115], v[204:207]\n" \
"v_accvgpr_read_b32 v41, a196\n" \
"v_accvgpr_read_b32 v23, a197\n" \
"v_mfma_f32_16x16x32_bf16 v[208:211], a[176:179], v[112:115], v[208:211]\n" \
"v_add_f32_e32 v41, v41, v23\n" \
"v_xor_b32_e32 v52, 0x4000, v50\n" \
"v_mfma_f32_16x16x32_bf16 v[212:215], a[180:183], v[112:115], v[212:215]\n" \
"buffer_load_dwordx4 a[40:43], v54, s[56:59], s97 offen offset:0 sc1\n" \
"v_mfma_f32_16x16x32_bf16 v[216:219], a[184:187], v[112:115], v[216:219]\n" \
"ds_write_b128 v50, v[156:159] offset:11264\n" \
"v_mfma_f32_16x16x32_bf16 v[220:223], a[188:191], v[112:115], v[220:223]\n" \
"ds_write_b128 v51, v[160:163] offset:0\n" \
"buffer_load_dwordx4 a[48:51], v20, s[56:59], s98 offen offset:0 sc1\n" \
"v_mfma_f32_16x16x32_bf16 a[200:203], a[252:255], v[112:115], a[200:203]\n" \
"ds_write_b128 v51, v[164:167] offset:1024\n" \
"ds_write_b128 v51, v[168:171] offset:2048\n" \
"ds_write_b128 v51, v[172:175] offset:3072\n" \
"buffer_load_dwordx4 a[56:59], v54, s[56:59], s98 offen offset:0 sc1\n" \
"ds_write_b128 v51, v[176:179] offset:8192\n" \
"ds_write_b128 v51, v[180:183] offset:9216\n" \
"ds_write_b128 v51, v[184:187] offset:10240\n" \
"ds_write_b128 v51, v[188:191] offset:11264\n" \
"buffer_load_dwordx4 a[36:39], v20, s[56:59], s97 offen offset:1024 sc1\n" \
"ds_write_b64 v19, v[40:41]\n" \
"v_xor_b32_e32 v53, 0x4000, v51\n" \
"v_xor_b32_e32 v63, 0x400, v19\n" \
"v_readlane_b32 s93, v11, 18\n" \
"v_readlane_b32 s84, v11, 13\n" \
"v_readlane_b32 s85, v11, 14\n" \
"v_readlane_b32 s86, v11, 15\n" \
"s_mov_b32 s87, 0x20000\n" \
"v_readlane_b32 s92, v11, 16\n" \
"s_add_u32 m0, s93, 0x0\n" \
"v_exp_f32_e32 v88, v88\n" \
"v_exp_f32_e32 v89, v89\n" \
"v_exp_f32_e32 v90, v90\n" \
"v_exp_f32_e32 v91, v91\n" \
"v_exp_f32_e32 v92, v92\n" \
"v_exp_f32_e32 v93, v93\n" \
"v_exp_f32_e32 v94, v94\n" \
"v_exp_f32_e32 v95, v95\n" \
"v_cvt_pk_bf16_f32 v124, v88, v89\n" \
"v_cvt_pk_bf16_f32 v125, v90, v91\n" \
"v_cvt_pk_bf16_f32 v126, v92, v93\n" \
"v_cvt_pk_bf16_f32 v127, v94, v95\n" \
"v_and_b32_e32 v124, v12, v124\n" \
"v_and_b32_e32 v125, v13, v125\n" \
"v_and_b32_e32 v126, v14, v126\n" \
"v_and_b32_e32 v127, v15, v127\n" \
"v_accvgpr_read_b32 v56, a200\n" \
"v_accvgpr_read_b32 v23, a201\n" \
"v_add_f32_e32 v56, v56, v23\n" \
"s_waitcnt lgkmcnt(0)\n" \
"s_barrier\n" \
"buffer_load_dwordx4 a[44:47], v54, s[56:59], s97 offen offset:1024 sc1\n" \
"ds_read_b128 v[128:131], v52 offset:0\n" \
"ds_read_b128 v[132:135], v52 offset:1024\n" \
"ds_read_b128 v[136:139], v52 offset:2048\n" \
"ds_read_b128 v[140:143], v52 offset:3072\n" \
"ds_read_b128 v[144:147], v52 offset:8192\n" \
"ds_read_b128 v[148:151], v52 offset:9216\n" \
"ds_read_b128 v[152:155], v52 offset:10240\n" \
"ds_read_b128 v[156:159], v52 offset:11264\n" \
"buffer_load_dwordx4 a[52:55], v20, s[56:59], s98 offen offset:1024 sc1\n" \
"ds_read_b128 v[160:163], v53 offset:0\n" \
"ds_read_b128 v[164:167], v53 offset:1024\n" \
"ds_read_b128 v[168:171], v53 offset:2048\n" \
"L_pf13:\n" \
"s_cbranch_execz L_pf17\n" \
"s_nop 0\n" \
"ds_read_b128 v[172:175], v53 offset:3072\n" \
"ds_read_b128 v[176:179], v53 offset:8192\n" \
"ds_read_b128 v[180:183], v53 offset:9216\n" \
"ds_read_b128 v[184:187], v53 offset:10240\n" \
"ds_read_b128 v[188:191], v53 offset:11264\n" \
"buffer_load_dwordx4 a[60:63], v54, s[56:59], s98 offen offset:1024 sc1\n" \
"ds_read_b64 v[42:43], v63\n" \
"v_mfma_f32_16x16x32_bf16 v[224:227], a[160:163], v[120:123], v[224:227]\n" \
"v_mfma_f32_16x16x32_bf16 v[228:231], a[164:167], v[120:123], v[228:231]\n" \
"v_mfma_f32_16x16x32_bf16 v[232:235], a[168:171], v[120:123], v[232:235]\n" \
"buffer_load_dwordx4 v0, s[84:87], s92 offen offset:0 lds\n" \
"v_mfma_f32_16x16x32_bf16 v[236:239], a[172:175], v[120:123], v[236:239]\n" \
"v_mfma_f32_16x16x32_bf16 v[240:243], a[176:179], v[120:123], v[240:243]\n" \
"v_mfma_f32_16x16x32_bf16 v[244:247], a[180:183], v[120:123], v[244:247]\n" \
"buffer_load_dwordx4 v1, s[84:87], s92 offen offset:1024 lds\n" \
"v_mfma_f32_16x16x32_bf16 v[248:251], a[184:187], v[120:123], v[248:251]\n" \
"s_waitcnt lgkmcnt(15)\n" \
"v_add_f32_e32 v192, v192, v128\n" \
"v_mfma_f32_16x16x32_bf16 v[252:255], a[188:191], v[120:123], v[252:255]\n" \
"v_add_f32_e32 v193, v193, v129\n" \
"v_add_f32_e32 v194, v194, v130\n" \
"v_mfma_f32_16x16x32_bf16 a[204:207], a[252:255], v[120:123], a[204:207]\n" \
"buffer_load_dwordx4 v2, s[84:87], s92 offen offset:2048 lds\n" \
"v_add_f32_e32 v195, v195, v131\n" \
"s_waitcnt lgkmcnt(1)\n" \
"s_barrier\n" \
"v_mfma_f32_16x16x32_bf16 v[224:227], a[0:3], v[124:127], v[224:227]\n" \
"buffer_load_dwordx4 v3, s[84:87], s92 offen offset:3072 lds\n" \
"s_add_u32 m0, s93, 0x1000\n" \
"v_mfma_f32_16x16x32_bf16 v[228:231], a[4:7], v[124:127], v[228:231]\n" \
"s_waitcnt lgkmcnt(0)\n" \
"v_add_f32_e32 v56, v56, v42\n" \
"v_mfma_f32_16x16x32_bf16 v[232:235], a[8:11], v[124:127], v[232:235]\n" \
"v_max_f32_e32 v62, v62, v56\n" \
"v_add_f32_e32 v196, v196, v132\n" \
"v_mfma_f32_16x16x32_bf16 v[236:239], a[12:15], v[124:127], v[236:239]\n" \
"buffer_load_dwordx4 v4, s[84:87], s92 offen offset:0 lds\n" \
"v_rcp_f32_e32 v56, v56\n" \
"v_mfma_f32_16x16x32_bf16 v[240:243], a[16:19], v[124:127], v[240:243]\n" \
"v_add_f32_e32 v197, v197, v133\n" \
"v_add_f32_e32 v198, v198, v134\n" \
"v_add_f32_e32 v199, v199, v135\n" \
"v_mul_f32_e32 v56, v56, v60\n" \
"v_mul_f32_e32 v192, v56, v192\n" \
"v_mul_f32_e32 v193, v56, v193\n" \
"v_mul_f32_e32 v194, v56, v194\n" \
"v_mul_f32_e32 v195, v56, v195\n" \
"v_mul_f32_e32 v196, v56, v196\n" \
"v_mul_f32_e32 v197, v56, v197\n" \
"v_mul_f32_e32 v198, v56, v198\n" \
"v_mul_f32_e32 v199, v56, v199\n" \
"v_add_f32_e32 v200, v200, v136\n" \
"v_add_f32_e32 v201, v201, v137\n" \
"v_add_f32_e32 v202, v202, v138\n" \
"v_add_f32_e32 v203, v203, v139\n" \
"v_add_f32_e32 v204, v204, v140\n" \
"v_add_f32_e32 v205, v205, v141\n" \
"v_add_f32_e32 v206, v206, v142\n" \
"v_add_f32_e32 v207, v207, v143\n" \
"v_cvt_pk_bf16_f32 v64, v192, v193\n" \
"v_cvt_pk_bf16_f32 v65, v194, v195\n" \
"v_cvt_pk_bf16_f32 v66, v196, v197\n" \
"v_cvt_pk_bf16_f32 v67, v198, v199\n" \
"buffer_store_dwordx4 v[64:67], v20, s[56:59], s70 offen offset:0 sc1\n" \
"v_mfma_f32_16x16x32_bf16 v[244:247], a[20:23], v[124:127], v[244:247]\n" \
"v_mfma_f32_16x16x32_bf16 a[208:211], a[216:219], v[64:67], a[208:211]\n" \
"v_mul_f32_e32 v200, v56, v200\n" \
"v_mul_f32_e32 v201, v56, v201\n" \
"v_mul_f32_e32 v202, v56, v202\n" \
"v_mul_f32_e32 v203, v56, v203\n" \
"v_mul_f32_e32 v204, v56, v204\n" \
"v_mul_f32_e32 v205, v56, v205\n" \
"v_mul_f32_e32 v206, v56, v206\n" \
"v_mul_f32_e32 v207, v56, v207\n" \
"v_add_f32_e32 v208, v208, v144\n" \
"v_add_f32_e32 v209, v209, v145\n" \
"v_add_f32_e32 v210, v210, v146\n" \
"v_add_f32_e32 v211, v211, v147\n" \
"v_add_f32_e32 v212, v212, v148\n" \
"v_add_f32_e32 v213, v213, v149\n" \
"v_add_f32_e32 v214, v214, v150\n" \
"v_add_f32_e32 v215, v215, v151\n" \
"v_cvt_pk_bf16_f32 v68, v200, v201\n" \
"v_cvt_pk_bf16_f32 v69, v202, v203\n" \
"v_cvt_pk_bf16_f32 v70, v204, v205\n" \
"v_cvt_pk_bf16_f32 v71, v206, v207\n" \
"buffer_store_dwordx4 v[68:71], v20, s[56:59], s70 offen offset:1024 sc1\n" \
"v_mul_f32_e32 v208, v56, v208\n" \
"v_mfma_f32_16x16x32_bf16 a[208:211], a[216:219], v[68:71], a[208:211]\n" \
"v_mul_f32_e32 v209, v56, v209\n" \
"v_mul_f32_e32 v210, v56, v210\n" \
"v_mul_f32_e32 v211, v56, v211\n" \
"v_mul_f32_e32 v212, v56, v212\n" \
"v_mul_f32_e32 v213, v56, v213\n" \
"v_mul_f32_e32 v214, v56, v214\n" \
"v_mul_f32_e32 v215, v56, v215\n" \
"v_add_f32_e32 v216, v216, v152\n" \
"v_add_f32_e32 v217, v217, v153\n" \
"v_add_f32_e32 v218, v218, v154\n" \
"v_add_f32_e32 v219, v219, v155\n" \
"v_add_f32_e32 v220, v220, v156\n" \
"v_add_f32_e32 v221, v221, v157\n" \
"v_add_f32_e32 v222, v222, v158\n" \
"v_add_f32_e32 v223, v223, v159\n" \
"v_cvt_pk_bf16_f32 v72, v208, v209\n" \
"v_cvt_pk_bf16_f32 v73, v210, v211\n" \
"v_cvt_pk_bf16_f32 v74, v212, v213\n" \
"v_cvt_pk_bf16_f32 v75, v214, v215\n" \
"buffer_store_dwordx4 v[72:75], v20, s[56:59], s70 offen offset:2048 sc1\n" \
"v_mfma_f32_16x16x32_bf16 v[248:251], a[24:27], v[124:127], v[248:251]\n" \
"v_mfma_f32_16x16x32_bf16 a[208:211], a[216:219], v[72:75], a[208:211]\n" \
"v_mul_f32_e32 v216, v56, v216\n" \
"v_mul_f32_e32 v217, v56, v217\n" \
"v_mul_f32_e32 v218, v56, v218\n" \
"v_mul_f32_e32 v219, v56, v219\n" \
"v_mul_f32_e32 v220, v56, v220\n" \
"v_mul_f32_e32 v221, v56, v221\n" \
"v_mul_f32_e32 v222, v56, v222\n" \
"v_mul_f32_e32 v223, v56, v223\n" \
"v_cvt_pk_bf16_f32 v76, v216, v217\n" \
"v_cvt_pk_bf16_f32 v77, v218, v219\n" \
"v_cvt_pk_bf16_f32 v78, v220, v221\n" \
"v_cvt_pk_bf16_f32 v79, v222, v223\n" \
"buffer_store_dwordx4 v[76:79], v20, s[56:59], s70 offen offset:3072 sc1\n" \
"v_add_f32_e32 v224, v224, v160\n" \
"v_add_f32_e32 v225, v225, v161\n" \
"v_add_f32_e32 v226, v226, v162\n" \
"v_add_f32_e32 v227, v227, v163\n" \
"v_add_f32_e32 v228, v228, v164\n" \
"v_add_f32_e32 v229, v229, v165\n" \
"v_add_f32_e32 v230, v230, v166\n" \
"v_add_f32_e32 v231, v231, v167\n" \
"v_mfma_f32_16x16x32_bf16 a[208:211], a[216:219], v[76:79], a[208:211]\n" \
"buffer_load_dwordx4 v5, s[84:87], s92 offen offset:1024 lds\n" \
"v_add_f32_e32 v232, v232, v168\n" \
"v_add_f32_e32 v233, v233, v169\n" \
"v_add_f32_e32 v234, v234, v170\n" \
"v_add_f32_e32 v235, v235, v171\n" \
"v_add_f32_e32 v236, v236, v172\n" \
"v_add_f32_e32 v237, v237, v173\n" \
"v_add_f32_e32 v238, v238, v174\n" \
"v_add_f32_e32 v239, v239, v175\n" \
"v_mfma_f32_16x16x32_bf16 v[252:255], a[28:31], v[124:127], v[252:255]\n" \
"buffer_load_dwordx4 v6, s[84:87], s92 offen offset:2048 lds\n" \
"v_add_f32_e32 v240, v240, v176\n" \
"v_add_f32_e32 v241, v241, v177\n" \
"v_add_f32_e32 v242, v242, v178\n" \
"v_add_f32_e32 v243, v243, v179\n" \
"v_add_f32_e32 v244, v244, v180\n" \
"v_add_f32_e32 v245, v245, v181\n" \
"v_add_f32_e32 v246, v246, v182\n" \
"v_add_f32_e32 v247, v247, v183\n" \
"v_add_f32_e32 v248, v248, v184\n" \
"v_add_f32_e32 v249, v249, v185\n" \
"v_add_f32_e32 v250, v250, v186\n" \
"v_add_f32_e32 v251, v251, v187\n" \
"v_add_f32_e32 v252, v252, v188\n" \
"v_add_f32_e32 v253, v253, v189\n" \
"v_add_f32_e32 v254, v254, v190\n" \
"v_add_f32_e32 v255, v255, v191\n" \
"v_mfma_f32_16x16x32_bf16 a[204:207], a[252:255], v[124:127], a[204:207]\n" \
"buffer_load_dwordx4 v7, s[84:87], s92 offen offset:3072 lds\n" \
"s_add_u32 m0, s93, 0x2000\n" \
"s_nop 0\n" \
"buffer_load_dwordx4 v8, s[84:87], s92 offen offset:0 lds\n" \
"s_nop 3\n" \
"v_accvgpr_read_b32 v57, a204\n" \
"v_accvgpr_read_b32 v23, a205\n" \
"v_add_f32_e32 v57, v57, v23\n" \
"v_add_f32_e32 v57, v57, v43\n" \
"v_max_f32_e32 v62, v62, v57\n" \
"v_rcp_f32_e32 v57, v57\n" \
"v_cmp_lt_f32_e64 s[72:73], s100, v62\n" \
"v_mul_f32_e32 v57, v57, v61\n" \
"v_mul_f32_e32 v224, v57, v224\n" \
"v_mul_f32_e32 v225, v57, v225\n" \
"v_mul_f32_e32 v226, v57, v226\n" \
"v_mul_f32_e32 v227, v57, v227\n" \
"v_mul_f32_e32 v228, v57, v228\n" \
"v_mul_f32_e32 v229, v57, v229\n" \
"v_mul_f32_e32 v230, v57, v230\n" \
"v_mul_f32_e32 v231, v57, v231\n" \
"v_cvt_pk_bf16_f32 v80, v224, v225\n" \
"v_cvt_pk_bf16_f32 v81, v226, v227\n" \
"v_cvt_pk_bf16_f32 v82, v228, v229\n" \
"v_cvt_pk_bf16_f32 v83, v230, v231\n" \
"buffer_store_dwordx4 v[80:83], v20, s[56:59], s71 offen offset:0 sc1\n" \
"v_mul_f32_e32 v232, v57, v232\n" \
"v_mfma_f32_16x16x32_bf16 a[208:211], a[216:219], v[80:83], a[208:211]\n" \
"v_mul_f32_e32 v233, v57, v233\n" \
"v_mul_f32_e32 v234, v57, v234\n" \
"v_mul_f32_e32 v235, v57, v235\n" \
"v_mul_f32_e32 v236, v57, v236\n" \
"v_mul_f32_e32 v237, v57, v237\n" \
"v_mul_f32_e32 v238, v57, v238\n" \
"v_mul_f32_e32 v239, v57, v239\n" \
"v_cvt_pk_bf16_f32 v84, v232, v233\n" \
"v_cvt_pk_bf16_f32 v85, v234, v235\n" \
"v_cvt_pk_bf16_f32 v86, v236, v237\n" \
"v_cvt_pk_bf16_f32 v87, v238, v239\n" \
"buffer_store_dwordx4 v[84:87], v20, s[56:59], s71 offen offset:1024 sc1\n" \
"v_mul_f32_e32 v240, v57, v240\n" \
"v_mfma_f32_16x16x32_bf16 a[208:211], a[216:219], v[84:87], a[208:211]\n" \
"v_mul_f32_e32 v241, v57, v241\n" \
"v_mul_f32_e32 v242, v57, v242\n" \
"v_mul_f32_e32 v243, v57, v243\n" \
"v_mul_f32_e32 v244, v57, v244\n" \
"v_mul_f32_e32 v245, v57, v245\n" \
"v_mul_f32_e32 v246, v57, v246\n" \
"v_mul_f32_e32 v247, v57, v247\n" \
"v_cvt_pk_bf16_f32 v96, v240, v241\n" \
"v_cvt_pk_bf16_f32 v97, v242, v243\n" \
"v_cvt_pk_bf16_f32 v98, v244, v245\n" \
"v_cvt_pk_bf16_f32 v99, v246, v247\n" \
"buffer_store_dwordx4 v[96:99], v20, s[56:59], s71 offen offset:2048 sc1\n" \
"v_mul_f32_e32 v248, v57, v248\n" \
"v_mfma_f32_16x16x32_bf16 a[208:211], a[216:219], v[96:99], a[208:211]\n" \
"v_mul_f32_e32 v249, v57, v249\n" \
"v_mul_f32_e32 v250, v57, v250\n" \
"v_mul_f32_e32 v251, v57, v251\n" \
"v_mul_f32_e32 v252, v57, v252\n" \
"v_mul_f32_e32 v253, v57, v253\n" \
"v_mul_f32_e32 v254, v57, v254\n" \
"v_mul_f32_e32 v255, v57, v255\n" \
"v_cvt_pk_bf16_f32 v100, v248, v249\n" \
"v_cvt_pk_bf16_f32 v101, v250, v251\n" \
"v_cvt_pk_bf16_f32 v102, v252, v253\n" \
"v_cvt_pk_bf16_f32 v103, v254, v255\n" \
"buffer_store_dwordx4 v[100:103], v20, s[56:59], s71 offen offset:3072 sc1\n" \
"s_nop 0\n" \
"v_mfma_f32_16x16x32_bf16 a[208:211], a[216:219], v[100:103], a[208:211]\n" \
"s_nop 7\n" \
"v_accvgpr_read_b32 v63, a208\n" \
"v_cmp_u_f32_e32 vcc, v63, v63\n" \
"s_nop 0\n" \
"s_or_b64 vcc, s[72:73], vcc\n" \
"s_cbranch_scc0 L_out_%=\n" \
"s_branch L_end_%=\n" \
"L_h1_%=:\n" \
"L_pf14:\n" \
"s_cbranch_execz L_pf18\n" \
"s_nop 0\n" \
"s_cmp_gt_i32 s63, 0\n" \
"s_cbranch_scc1 L_hc1_%=\n" \
"s_cmp_lt_i32 s63, 0\n" \
"s_cbranch_scc1 L_hn1_%=\n" \
"s_mov_b32 exec_hi, 0xf\n" \
"buffer_load_dword v22, v16, s[80:83], 0 offen\n" \
"s_mov_b32 exec_hi, -1\n" \
"s_branch L_hr1_%=\n" \
"L_hn1_%=:\n" \
"buffer_load_dword v22, v17, s[56:59], 0 offen sc1\n" \
"s_branch L_hr1_%=\n" \
"L_hs1_%=:\n" \
"s_sleep 1\n" \
"buffer_load_dword v22, v17, s[56:59], 0 offen sc1\n" \
"s_add_u32 s101, s101, 1\n" \
"s_waitcnt vmcnt(0)\n" \
"s_cmp_gt_u32 s101, 0x100000\n" \
"s_cbranch_scc1 L_hr1_%=\n" \
"L_hc1_%=:\n" \
"v_cmp_ne_u32_e32 vcc, s62, v22\n" \
"s_nop 4\n" \
"s_cbranch_vccnz L_hs1_%=\n" \
"s_branch L_hr1_%=\n" \
"L_h2_%=:\n" \
"s_cmp_gt_i32 s63, 0\n" \
"s_cbranch_scc1 L_hc2_%=\n" \
"s_cmp_lt_i32 s63, 0\n" \
"s_cbranch_scc1 L_hn2_%=\n" \
"s_mov_b32 exec_hi, 0xf\n" \
"buffer_load_dword v22, v16, s[80:83], 0 offen\n" \
"s_mov_b32 exec_hi, -1\n" \
"s_branch L_hr2_%=\n" \
"L_hn2_%=:\n" \
"buffer_load_dword v22, v17, s[56:59], 0 offen sc1\n" \
"s_branch L_hr2_%=\n" \
"L_hs2_%=:\n" \
"s_sleep 1\n" \
"buffer_load_dword v22, v17, s[56:59], 0 offen sc1\n" \
"s_add_u32 s101, s101, 1\n" \
"s_waitcnt vmcnt(0)\n" \
"s_cmp_gt_u32 s101, 0x100000\n" \
"s_cbranch_scc1 L_hr2_%=\n" \
"L_hc2_%=:\n" \
"v_cmp_ne_u32_e32 vcc, s62, v22\n" \
"s_nop 4\n" \
"s_cbranch_vccnz L_hs2_%=\n" \
"s_branch L_hr2_%=\n" \
"L_sa_%=:\n" \
"s_sleep 1\n" \
"buffer_load_dword v37, v38, s[56:59], s64 offen sc1\n" \
"s_add_u32 s101, s101, 1\n" \
"s_waitcnt vmcnt(0)\n" \
"v_cmp_ne_u32_e32 vcc, s62, v37\n" \
"s_cmp_gt_u32 s101, 0x100000\n" \
"s_cbranch_scc1 L_ra_%=\n" \
"s_nop 2\n" \
"s_cbranch_vccnz L_sa_%=\n" \
"s_branch L_ra_%=\n" \
"L_sb_%=:\n" \
"s_sleep 1\n" \
"buffer_load_dword v47, v38, s[56:59], s71 offen sc1\n" \
"s_add_u32 s101, s101, 1\n" \
"s_waitcnt vmcnt(0)\n" \
"v_cmp_ne_u32_e32 vcc, s62, v47\n" \
"s_cmp_gt_u32 s101, 0x100000\n" \
"s_cbranch_scc1 L_rb_%=\n" \
"s_nop 2\n" \
"s_cbranch_vccnz L_sb_%=\n" \
"s_branch L_rb_%=\n" \
"L_sx0_%=:\n" \
"s_sleep 1\n" \
"buffer_load_dword v37, v40, s[56:59], s74 offen sc1\n" \
"s_add_u32 s101, s101, 1\n" \
"s_waitcnt vmcnt(0)\n" \
"v_cmp_eq_u32_e64 s[98:99], s62, v37\n" \
"s_cmp_gt_u32 s101, 0x100000\n" \
"s_cbranch_scc1 L_rx0_%=\n" \
"s_andn2_b32 s75, 0x1, s98\n" \
"s_cbranch_scc1 L_sx0_%=\n" \
"s_branch L_rx0_%=\n" \
"L_sx1_%=:\n" \
"s_sleep 1\n" \
"buffer_load_dword v37, v40, s[56:59], s74 offen sc1\n" \
"s_add_u32 s101, s101, 1\n" \
"s_waitcnt vmcnt(0)\n" \
"v_cmp_eq_u32_e64 s[98:99], s62, v37\n" \
"s_cmp_gt_u32 s101, 0x100000\n" \
"s_cbranch_scc1 L_rx1_%=\n" \
"s_andn2_b32 s75, 0x2, s98\n" \
"s_cbranch_scc1 L_sx1_%=\n" \
"s_branch L_rx1_%=\n" \
"L_sx2_%=:\n" \
"s_sleep 1\n" \
"buffer_load_dword v37, v40, s[56:59], s74 offen sc1\n" \
"s_add_u32 s101, s101, 1\n" \
"s_waitcnt vmcnt(0)\n" \
"v_cmp_eq_u32_e64 s[98:99], s62, v37\n" \
"s_cmp_gt_u32 s101, 0x100000\n" \
"s_cbranch_scc1 L_rx2_%=\n" \
"s_andn2_b32 s75, 0x4, s98\n" \
"s_cbranch_scc1 L_sx2_%=\n" \
"s_branch L_rx2_%=\n" \
"L_sx3_%=:\n" \
"s_sleep 1\n" \
"buffer_load_dword v37, v40, s[56:59], s74 offen sc1\n" \
"s_add_u32 s101, s101, 1\n" \
"s_waitcnt vmcnt(0)\n" \
"v_cmp_eq_u32_e64 s[98:99], s62, v37\n" \
"s_cmp_gt_u32 s101, 0x100000\n" \
"s_cbranch_scc1 L_rx3_%=\n" \
"s_andn2_b32 s75, s100, s98\n" \
"s_cbranch_scc1 L_sx3_%=\n" \
"s_branch L_rx3_%=\n" \
"L_sx4_%=:\n" \
"s_sleep 1\n" \
"buffer_load_dword v37, v40, s[56:59], s74 offen sc1\n" \
"s_add_u32 s101, s101, 1\n" \
"s_waitcnt vmcnt(0)\n" \
"v_cmp_eq_u32_e64 s[98:99], s62, v37\n" \
"s_cmp_gt_u32 s101, 0x100000\n" \
"s_cbranch_scc1 L_rx4_%=\n" \
"s_andn2_b32 s75, 0x10, s98\n" \
"s_cbranch_scc1 L_sx4_%=\n" \
"s_branch L_rx4_%=\n" \
"L_sx5_%=:\n" \
"s_sleep 1\n" \
"buffer_load_dword v37, v40, s[56:59], s74 offen sc1\n" \
"s_add_u32 s101, s101, 1\n" \
"s_waitcnt vmcnt(0)\n" \
"v_cmp_eq_u32_e64 s[98:99], s62, v37\n" \
"s_cmp_gt_u32 s101, 0x100000\n" \
"s_cbranch_scc1 L_rx5_%=\n" \
"s_andn2_b32 s75, 0x20, s98\n" \
"s_cbranch_scc1 L_sx5_%=\n" \
"s_branch L_rx5_%=\n" \
"L_sx6_%=:\n" \
"s_sleep 1\n" \
"buffer_load_dword v37, v40, s[56:59], s74 offen sc1\n" \
"s_add_u32 s101, s101, 1\n" \
"s_waitcnt vmcnt(0)\n" \
"v_cmp_eq_u32_e64 s[98:99], s62, v37\n" \
"s_cmp_gt_u32 s101, 0x100000\n" \
"s_cbranch_scc1 L_rx6_%=\n" \
"s_andn2_b32 s75, 0x40, s98\n" \
"s_cbranch_scc1 L_sx6_%=\n" \
"s_branch L_rx6_%=\n" \
"L_sx7_%=:\n" \
"s_sleep 1\n" \
"buffer_load_dword v37, v40, s[56:59], s74 offen sc1\n" \
"s_add_u32 s101, s101, 1\n" \
"s_waitcnt vmcnt(0)\n" \
"v_cmp_eq_u32_e64 s[98:99], s62, v37\n" \
"s_cmp_gt_u32 s101, 0x100000\n" \
"s_cbranch_scc1 L_rx7_%=\n" \
"s_andn2_b32 s75, 0x80, s98\n" \
"s_cbranch_scc1 L_sx7_%=\n" \
"s_branch L_rx7_%=\n" \
"L_sx8_%=:\n" \
"s_sleep 1\n" \
"buffer_load_dword v37, v40, s[56:59], s74 offen sc1\n" \
"s_add_u32 s101, s101, 1\n" \
"s_waitcnt vmcnt(0)\n" \
"v_cmp_eq_u32_e64 s[98:99], s62, v37\n" \
"s_cmp_gt_u32 s101, 0x100000\n" \
"s_cbranch_scc1 L_rx8_%=\n" \
"s_andn2_b32 s75, 0x100, s98\n" \
"s_cbranch_scc1 L_sx8_%=\n" \
"s_branch L_rx8_%=\n" \
"L_sx9_%=:\n" \
"s_sleep 1\n" \
"buffer_load_dword v37, v40, s[56:59], s74 offen sc1\n" \
"s_add_u32 s101, s101, 1\n" \
"s_waitcnt vmcnt(0)\n" \
"v_cmp_eq_u32_e64 s[98:99], s62, v37\n" \
"s_cmp_gt_u32 s101, 0x100000\n" \
"s_cbranch_scc1 L_rx9_%=\n" \
"s_andn2_b32 s75, 0x200, s98\n" \
"s_cbranch_scc1 L_sx9_%=\n" \
"s_branch L_rx9_%=\n" \
"L_sx10_%=:\n" \
"s_sleep 1\n" \
"buffer_load_dword v37, v40, s[56:59], s74 offen sc1\n" \
"s_add_u32 s101, s101, 1\n" \
"s_waitcnt vmcnt(0)\n" \
"v_cmp_eq_u32_e64 s[98:99], s62, v37\n" \
"s_cmp_gt_u32 s101, 0x100000\n" \
"s_cbranch_scc1 L_rx10_%=\n" \
"s_andn2_b32 s75, 0x400, s98\n" \
"s_cbranch_scc1 L_sx10_%=\n" \
"s_branch L_rx10_%=\n" \
"L_att_stub:\n" \
"s_mov_b32 s101, 0\n" \
"v_add_u32_e32 v32, 0x49000, v20\n" \
"v_readlane_b32 s96, v11, 32\n" \
"v_readlane_b32 s97, v11, 33\n" \
"buffer_load_dwordx4 a[220:223], v20, s[56:59], s60 offen offset:0 sc1\n" \
"buffer_load_dwordx4 a[236:239], v32, s[56:59], s60 offen offset:0 sc1\n" \
"buffer_load_dwordx4 a[224:227], v20, s[56:59], s60 offen offset:1024 sc1\n" \
"buffer_load_dwordx4 a[240:243], v32, s[56:59], s60 offen offset:1024 sc1\n" \
"buffer_load_dwordx4 a[228:231], v20, s[56:59], s60 offen offset:2048 sc1\n" \
"buffer_load_dwordx4 a[244:247], v32, s[56:59], s60 offen offset:2048 sc1\n" \
"buffer_load_dwordx4 a[232:235], v20, s[56:59], s60 offen offset:3072 sc1\n" \
"buffer_load_dwordx4 a[248:251], v32, s[56:59], s60 offen offset:3072 sc1\n" \
"buffer_load_dwordx4 a[32:35], v20, s[56:59], s96 offen offset:0 sc1\n" \
"buffer_load_dwordx4 a[40:43], v32, s[56:59], s96 offen offset:0 sc1\n" \
"buffer_load_dwordx4 a[48:51], v20, s[56:59], s97 offen offset:0 sc1\n" \
"buffer_load_dwordx4 a[56:59], v32, s[56:59], s97 offen offset:0 sc1\n" \
"buffer_load_dwordx4 a[36:39], v20, s[56:59], s96 offen offset:1024 sc1\n" \
"buffer_load_dwordx4 a[44:47], v32, s[56:59], s96 offen offset:1024 sc1\n" \
"buffer_load_dwordx4 a[52:55], v20, s[56:59], s97 offen offset:1024 sc1\n" \
"buffer_load_dwordx4 a[60:63], v32, s[56:59], s97 offen offset:1024 sc1\n" \
"buffer_load_dwordx4 v[64:67], v20, s[56:59], s61 offen offset:0 sc1\n" \
"buffer_load_dwordx4 v[68:71], v20, s[56:59], s61 offen offset:1024 sc1\n" \
"buffer_load_dwordx4 v[72:75], v20, s[56:59], s61 offen offset:2048 sc1\n" \
"buffer_load_dwordx4 v[76:79], v20, s[56:59], s61 offen offset:3072 sc1\n" \
"buffer_load_dwordx4 v[80:83], v32, s[56:59], s61 offen offset:0 sc1\n" \
"buffer_load_dwordx4 v[84:87], v32, s[56:59], s61 offen offset:1024 sc1\n" \
"buffer_load_dwordx4 v[96:99], v32, s[56:59], s61 offen offset:2048 sc1\n" \
"buffer_load_dwordx4 v[100:103], v32, s[56:59], s61 offen offset:3072 sc1\n" \
"s_waitcnt vmcnt(0)\n" \
"s_branch L_out_%=\n" \
"L_sw_%=:\n" \
"L_pf15:\n" \
"s_cbranch_execz L_pf19\n" \
"s_nop 0\n" \
"v_mfma_f32_16x16x32_bf16 v[72:75], a[128:131], a[16:19], v[36:39]\n" \
"s_add_u32 s68, s60, 0xfff6e000\n" \
"s_add_u32 s69, s60, 0xfffb7000\n" \
"s_add_u32 s70, s60, 0x0\n" \
"s_add_u32 s71, s60, 0x49000\n" \
"v_mfma_f32_16x16x32_bf16 v[72:75], a[132:135], a[20:23], v[72:75]\n" \
"s_xor_b32 m0, s65, 0x12000\n" \
"ds_read_b128 a[160:163], v49 offset:0\n" \
"v_mfma_f32_16x16x32_bf16 v[72:75], a[136:139], a[24:27], v[72:75]\n" \
"buffer_load_dwordx4 v50, s[8:11], s64 offen offset:0 sc1 lds\n" \
"v_mfma_f32_16x16x32_bf16 v[72:75], a[140:143], a[28:31], v[72:75]\n" \
"ds_read_b128 a[164:167], v49 offset:1024\n" \
"ds_read_b128 a[168:171], v49 offset:2048\n" \
"v_mfma_f32_16x16x32_bf16 v[76:79], a[156:159], a[28:31], v[36:39]\n" \
"ds_read_b128 a[172:175], v49 offset:3072\n" \
"v_cvt_pk_bf16_f32 v120, v88, v89\n" \
"v_mfma_f32_16x16x32_bf16 v[76:79], a[152:155], a[24:27], v[76:79]\n" \
"buffer_load_dwordx4 v50, s[8:11], s64 offen offset:1024 sc1 lds\n" \
"v_mfma_f32_16x16x32_bf16 v[76:79], a[148:151], a[20:23], v[76:79]\n" \
"ds_read_b128 a[176:179], v49 offset:4096\n" \
"v_cvt_pk_bf16_f32 v121, v90, v91\n" \
"v_mfma_f32_16x16x32_bf16 v[76:79], a[144:147], a[16:19], v[76:79]\n" \
"ds_read_b128 a[180:183], v49 offset:5120\n" \
"s_mov_b32 s65, s13\n" \
"v_mfma_f32_16x16x32_bf16 v[80:83], a[128:131], a[32:35], v[40:43]\n" \
"v_add_u32_e32 v63, s15, v20\n" \
"s_add_u32 s67, s14, 40960\n" \
"v_mfma_f32_16x16x32_bf16 v[80:83], a[132:135], a[36:39], v[80:83]\n" \
"v_add_u32_e32 v50, 0x6000, v63\n" \
"s_xor_b32 m0, s65, 0x8000\n" \
"v_mfma_f32_16x16x32_bf16 v[80:83], a[136:139], a[40:43], v[80:83]\n" \
"v_exp_f32_e32 v92, v92\n" \
"v_mfma_f32_16x16x32_bf16 v[80:83], a[140:143], a[44:47], v[80:83]\n" \
"buffer_load_dwordx4 v50, s[8:11], s67 offen offset:0 sc1 lds\n" \
"v_mfma_f32_16x16x32_bf16 v[84:87], a[156:159], a[44:47], v[40:43]\n" \
"v_exp_f32_e32 v93, v93\n" \
"v_mfma_f32_16x16x32_bf16 v[84:87], a[152:155], a[40:43], v[84:87]\n" \
"v_exp_f32_e32 v94, v94\n" \
"v_mfma_f32_16x16x32_bf16 v[84:87], a[148:151], a[36:39], v[84:87]\n" \
"v_exp_f32_e32 v95, v95\n" \
"v_mfma_f32_16x16x32_bf16 v[84:87], a[144:147], a[32:35], v[84:87]\n" \
"v_cvt_pk_bf16_f32 v122, v92, v93\n" \
"v_cvt_pk_bf16_f32 v123, v94, v95\n" \
"v_mfma_f32_16x16x32_bf16 v[88:91], a[128:131], a[48:51], v[44:47]\n" \
"ds_read_b128 a[184:187], v49 offset:6144\n" \
"ds_read_b128 a[188:191], v49 offset:7168\n" \
"v_mfma_f32_16x16x32_bf16 v[88:91], a[132:135], a[52:55], v[88:91]\n" \
"v_and_b32_e32 v96, v12, v96\n" \
"v_and_b32_e32 v97, v13, v97\n" \
"v_mfma_f32_16x16x32_bf16 v[88:91], a[136:139], a[56:59], v[88:91]\n" \
"v_and_b32_e32 v98, v14, v98\n" \
"v_and_b32_e32 v99, v15, v99\n" \
"v_mfma_f32_16x16x32_bf16 v[88:91], a[140:143], a[60:63], v[88:91]\n" \
"buffer_load_dwordx4 v50, s[8:11], s67 offen offset:1024 sc1 lds\n" \
"v_mfma_f32_16x16x32_bf16 v[92:95], a[156:159], a[60:63], v[44:47]\n" \
"v_exp_f32_e32 v72, v72\n" \
"v_mfma_f32_16x16x32_bf16 v[92:95], a[152:155], a[56:59], v[92:95]\n" \
"v_exp_f32_e32 v73, v73\n" \
"v_mfma_f32_16x16x32_bf16 v[92:95], a[148:151], a[52:55], v[92:95]\n" \
"v_exp_f32_e32 v74, v74\n" \
"v_mfma_f32_16x16x32_bf16 v[92:95], a[144:147], a[48:51], v[92:95]\n" \
"v_cvt_pk_bf16_f32 v108, v72, v73\n" \
"s_waitcnt lgkmcnt(2)\n" \
"v_mfma_f32_16x16x32_bf16 v[128:131], a[160:163], v[96:99], v[128:131]\n" \
"v_exp_f32_e32 v75, v75\n" \
"v_mfma_f32_16x16x32_bf16 v[132:135], a[164:167], v[96:99], v[132:135]\n" \
"v_exp_f32_e32 v76, v76\n" \
"v_mfma_f32_16x16x32_bf16 v[136:139], a[168:171], v[96:99], v[136:139]\n" \
"v_exp_f32_e32 v77, v77\n" \
"v_mfma_f32_16x16x32_bf16 v[140:143], a[172:175], v[96:99], v[140:143]\n" \
"v_exp_f32_e32 v78, v78\n" \
"v_mfma_f32_16x16x32_bf16 a[192:195], a[252:255], v[96:99], a[192:195]\n" \
"v_exp_f32_e32 v79, v79\n" \
"v_mfma_f32_16x16x32_bf16 v[160:163], a[160:163], v[104:107], v[160:163]\n" \
"v_cvt_pk_bf16_f32 v109, v74, v75\n" \
"v_cvt_pk_bf16_f32 v110, v76, v77\n" \
"v_cvt_pk_bf16_f32 v111, v78, v79\n" \
"v_and_b32_e32 v108, v12, v108\n" \
"v_and_b32_e32 v109, v13, v109\n" \
"v_and_b32_e32 v110, v14, v110\n" \
"v_and_b32_e32 v111, v15, v111\n" \
"v_exp_f32_e32 v80, v80\n" \
"v_exp_f32_e32 v81, v81\n" \
"v_mfma_f32_16x16x32_bf16 v[160:163], a[176:179], v[108:111], v[160:163]\n" \
"buffer_load_dwordx4 v50, s[8:11], s67 offen offset:2048 sc1 lds\n" \
"v_mfma_f32_16x16x32_bf16 v[164:167], a[180:183], v[108:111], v[164:167]\n" \
"buffer_load_dwordx4 v50, s[8:11], s67 offen offset:3072 sc1 lds\n" \
"v_mfma_f32_16x16x32_bf16 v[164:167], a[164:167], v[104:107], v[164:167]\n" \
"v_exp_f32_e32 v82, v82\n" \
"v_mfma_f32_16x16x32_bf16 v[168:171], a[168:171], v[104:107], v[168:171]\n" \
"v_cvt_pk_bf16_f32 v116, v80, v81\n" \
"s_waitcnt lgkmcnt(0)\n" \
"v_mfma_f32_16x16x32_bf16 v[168:171], a[184:187], v[108:111], v[168:171]\n" \
"v_exp_f32_e32 v83, v83\n" \
"v_mfma_f32_16x16x32_bf16 v[172:175], a[188:191], v[108:111], v[172:175]\n" \
"v_exp_f32_e32 v84, v84\n" \
"v_mfma_f32_16x16x32_bf16 v[172:175], a[172:175], v[104:107], v[172:175]\n" \
"v_exp_f32_e32 v85, v85\n" \
"v_smfmac_f32_16x16x64_bf16 a[196:199], a[212:215], v[104:111], v21\n" \
"v_mfma_f32_16x16x32_bf16 v[192:195], a[160:163], v[112:115], v[192:195]\n" \
"v_exp_f32_e32 v86, v86\n" \
"v_exp_f32_e32 v87, v87\n" \
"v_cvt_pk_bf16_f32 v117, v82, v83\n" \
"v_cvt_pk_bf16_f32 v118, v84, v85\n" \
"v_cvt_pk_bf16_f32 v119, v86, v87\n" \
"s_mov_b32 s64, s14\n" \
"v_add_u32_e32 v51, 0x12000, v63\n" \
"v_mfma_f32_16x16x32_bf16 v[192:195], a[176:179], v[116:119], v[192:195]\n" \
"s_xor_b32 m0, s65, 0xa000\n" \
"ds_read_b32 v58, v18 offset:512\n" \
"v_mfma_f32_16x16x32_bf16 v[196:199], a[180:183], v[116:119], v[196:199]\n" \
"buffer_load_dwordx4 v51, s[8:11], s64 offen offset:0 sc1 lds\n" \
"v_mfma_f32_16x16x32_bf16 v[196:199], a[164:167], v[112:115], v[196:199]\n" \
"v_exp_f32_e32 v88, v88\n" \
"v_mfma_f32_16x16x32_bf16 v[200:203], a[168:171], v[112:115], v[200:203]\n" \
"v_exp_f32_e32 v89, v89\n" \
"v_mfma_f32_16x16x32_bf16 v[200:203], a[184:187], v[116:119], v[200:203]\n" \
"v_exp_f32_e32 v90, v90\n" \
"v_mfma_f32_16x16x32_bf16 v[204:207], a[188:191], v[116:119], v[204:207]\n" \
"v_exp_f32_e32 v91, v91\n" \
"v_mfma_f32_16x16x32_bf16 v[204:207], a[172:175], v[112:115], v[204:207]\n" \
"v_exp_f32_e32 v92, v92\n" \
"v_smfmac_f32_16x16x64_bf16 a[200:203], a[212:215], v[112:119], v21\n" \
"v_mfma_f32_16x16x32_bf16 v[224:227], a[160:163], v[120:123], v[224:227]\n" \
"v_exp_f32_e32 v93, v93\n" \
"v_exp_f32_e32 v94, v94\n" \
"v_exp_f32_e32 v95, v95\n" \
"v_cvt_pk_bf16_f32 v124, v88, v89\n" \
"v_cvt_pk_bf16_f32 v125, v90, v91\n" \
"v_cvt_pk_bf16_f32 v126, v92, v93\n" \
"v_cvt_pk_bf16_f32 v127, v94, v95\n" \
"v_add_u32_e32 v52, 0x14000, v63\n" \
"v_add_u32_e32 v53, 0x20000, v63\n" \
"v_mfma_f32_16x16x32_bf16 v[224:227], a[176:179], v[124:127], v[224:227]\n" \
"buffer_load_dwordx4 v51, s[8:11], s64 offen offset:1024 sc1 lds\n" \
"v_mfma_f32_16x16x32_bf16 v[228:231], a[180:183], v[124:127], v[228:231]\n" \
"ds_read_b32 v59, v18 offset:640\n" \
"ds_read_b32 v60, v18 offset:768\n" \
"v_mfma_f32_16x16x32_bf16 v[228:231], a[164:167], v[120:123], v[228:231]\n" \
"ds_read_b32 v61, v18 offset:896\n" \
"ds_read_b128 a[128:131], v48 offset:0\n" \
"v_mfma_f32_16x16x32_bf16 v[232:235], a[168:171], v[120:123], v[232:235]\n" \
"ds_read_b128 a[132:135], v48 offset:1024\n" \
"ds_read_b128 a[136:139], v48 offset:2048\n" \
"v_mfma_f32_16x16x32_bf16 v[232:235], a[184:187], v[124:127], v[232:235]\n" \
"ds_read_b128 a[140:143], v48 offset:3072\n" \
"ds_read_b128 a[144:147], v48 offset:4096\n" \
"ds_read_b128 a[148:151], v48 offset:5120\n" \
"ds_read_b128 a[152:155], v48 offset:6144\n" \
"ds_read_b128 a[156:159], v48 offset:7168\n" \
"v_mfma_f32_16x16x32_bf16 v[236:239], a[188:191], v[124:127], v[236:239]\n" \
"buffer_load_dwordx4 v51, s[8:11], s64 offen offset:2048 sc1 lds\n" \
"v_mfma_f32_16x16x32_bf16 v[236:239], a[172:175], v[120:123], v[236:239]\n" \
"buffer_load_dwordx4 v51, s[8:11], s64 offen offset:3072 sc1 lds\n" \
"v_readlane_b32 s93, v11, 17\n" \
"v_readlane_b32 s92, v11, 22\n" \
"v_accvgpr_read_b32 v54, a192\n" \
"v_accvgpr_read_b32 v23, a193\n" \
"v_add_f32_e32 v54, v54, v23\n" \
"v_accvgpr_read_b32 v55, a196\n" \
"v_accvgpr_read_b32 v23, a197\n" \
"v_max_f32_e32 v62, v62, v54\n" \
"v_add_f32_e32 v55, v55, v23\n" \
"v_max_f32_e32 v62, v62, v55\n" \
"v_rcp_f32_e32 v54, v54\n" \
"v_rcp_f32_e32 v55, v55\n" \
"s_waitcnt lgkmcnt(11)\n" \
"v_mul_f32_e32 v54, v54, v58\n" \
"s_waitcnt lgkmcnt(10)\n" \
"v_mul_f32_e32 v55, v55, v59\n" \
"v_mul_f32_e32 v128, v54, v128\n" \
"v_mul_f32_e32 v129, v54, v129\n" \
"v_mul_f32_e32 v136, v54, v136\n" \
"v_mul_f32_e32 v137, v54, v137\n" \
"v_mul_f32_e32 v160, v55, v160\n" \
"v_mul_f32_e32 v161, v55, v161\n" \
"v_mul_f32_e32 v168, v55, v168\n" \
"v_mul_f32_e32 v169, v55, v169\n" \
"v_cvt_pk_bf16_f32 v128, v128, v129\n" \
"v_mul_f32_e32 v130, v54, v130\n" \
"v_mul_f32_e32 v131, v54, v131\n" \
"v_cvt_pk_bf16_f32 v136, v136, v137\n" \
"v_mul_f32_e32 v138, v54, v138\n" \
"v_mul_f32_e32 v139, v54, v139\n" \
"s_waitcnt vmcnt(10) lgkmcnt(0)\n" \
"s_barrier\n" \
"v_mfma_f32_16x16x32_bf16 v[80:83], a[128:131], a[32:35], v[40:43]\n" \
"ds_read_b128 a[160:163], v49 offset:8192\n" \
"ds_read_b128 a[164:167], v49 offset:9216\n" \
"v_mfma_f32_16x16x32_bf16 v[80:83], a[132:135], a[36:39], v[80:83]\n" \
"ds_read_b128 a[168:171], v49 offset:10240\n" \
"ds_read_b128 a[172:175], v49 offset:11264\n" \
"v_mfma_f32_16x16x32_bf16 v[80:83], a[136:139], a[40:43], v[80:83]\n" \
"s_xor_b32 m0, s65, 0x18000\n" \
"ds_read_b128 a[176:179], v49 offset:12288\n" \
"v_mfma_f32_16x16x32_bf16 v[80:83], a[140:143], a[44:47], v[80:83]\n" \
"buffer_load_dwordx4 v52, s[8:11], s64 offen offset:0 sc1 lds\n" \
"v_mfma_f32_16x16x32_bf16 v[84:87], a[156:159], a[44:47], v[40:43]\n" \
"ds_read_b128 a[180:183], v49 offset:13312\n" \
"ds_read_b128 a[184:187], v49 offset:14336\n" \
"v_mfma_f32_16x16x32_bf16 v[84:87], a[152:155], a[40:43], v[84:87]\n" \
"buffer_load_dwordx4 v52, s[8:11], s64 offen offset:1024 sc1 lds\n" \
"v_mfma_f32_16x16x32_bf16 v[84:87], a[148:151], a[36:39], v[84:87]\n" \
"ds_read_b128 a[188:191], v49 offset:15360\n" \
"v_add_u32_e32 v49, 0x10000, v48\n" \
"v_mfma_f32_16x16x32_bf16 v[84:87], a[144:147], a[32:35], v[84:87]\n" \
"buffer_load_dwordx4 v52, s[8:11], s64 offen offset:2048 sc1 lds\n" \
"v_mfma_f32_16x16x32_bf16 v[88:91], a[128:131], a[48:51], v[44:47]\n" \
"ds_read_b128 a[128:131], v48 offset:8192\n" \
"v_cvt_pk_bf16_f32 v160, v160, v161\n" \
"v_mfma_f32_16x16x32_bf16 v[88:91], a[132:135], a[52:55], v[88:91]\n" \
"ds_read_b128 a[132:135], v48 offset:9216\n" \
"v_mul_f32_e32 v162, v55, v162\n" \
"v_mfma_f32_16x16x32_bf16 v[88:91], a[136:139], a[56:59], v[88:91]\n" \
"buffer_load_dwordx4 v52, s[8:11], s64 offen offset:3072 sc1 lds\n" \
"v_mfma_f32_16x16x32_bf16 v[88:91], a[140:143], a[60:63], v[88:91]\n" \
"s_mov_b32 m0, s65\n" \
"ds_read_b128 a[136:139], v48 offset:10240\n" \
"v_mfma_f32_16x16x32_bf16 v[92:95], a[156:159], a[60:63], v[44:47]\n" \
"ds_read_b128 a[140:143], v48 offset:11264\n" \
"ds_read_b128 a[156:159], v48 offset:15360\n" \
"v_mfma_f32_16x16x32_bf16 v[92:95], a[152:155], a[56:59], v[92:95]\n" \
"buffer_load_dwordx4 v53, s[8:11], s64 offen offset:0 sc1 lds\n" \
"v_mfma_f32_16x16x32_bf16 v[92:95], a[148:151], a[52:55], v[92:95]\n" \
"ds_read_b128 a[148:151], v48 offset:13312\n" \
"ds_read_b128 a[152:155], v48 offset:14336\n" \
"v_mfma_f32_16x16x32_bf16 v[92:95], a[144:147], a[48:51], v[92:95]\n" \
"ds_read_b128 a[144:147], v48 offset:12288\n" \
"s_waitcnt lgkmcnt(12)\n" \
"v_mfma_f32_16x16x32_bf16 v[144:147], a[160:163], v[96:99], v[144:147]\n" \
"buffer_load_dwordx4 v53, s[8:11], s64 offen offset:1024 sc1 lds\n" \
"v_mfma_f32_16x16x32_bf16 v[148:151], a[164:167], v[96:99], v[148:151]\n" \
"v_exp_f32_e32 v88, v88\n" \
"v_mfma_f32_16x16x32_bf16 v[152:155], a[168:171], v[96:99], v[152:155]\n" \
"v_exp_f32_e32 v89, v89\n" \
"v_mfma_f32_16x16x32_bf16 v[156:159], a[172:175], v[96:99], v[156:159]\n" \
"v_exp_f32_e32 v90, v90\n" \
"v_mfma_f32_16x16x32_bf16 v[176:179], a[160:163], v[104:107], v[176:179]\n" \
"s_xor_b32 s65, s65, 0x8000\n" \
"s_waitcnt lgkmcnt(8)\n" \
"v_mfma_f32_16x16x32_bf16 v[176:179], a[176:179], v[108:111], v[176:179]\n" \
"buffer_load_dwordx4 v53, s[8:11], s64 offen offset:2048 sc1 lds\n" \
"v_mfma_f32_16x16x32_bf16 v[180:183], a[180:183], v[108:111], v[180:183]\n" \
"v_exp_f32_e32 v91, v91\n" \
"v_mfma_f32_16x16x32_bf16 v[180:183], a[164:167], v[104:107], v[180:183]\n" \
"L_pf16:\n" \
"s_cbranch_execz L_retA\n" \
"s_nop 0\n" \
"buffer_load_dwordx4 v53, s[8:11], s64 offen offset:3072 sc1 lds\n" \
"v_mfma_f32_16x16x32_bf16 v[184:187], a[168:171], v[104:107], v[184:187]\n" \
"s_add_u32 m0, s93, 0x0\n" \
"s_add_u32 s64, s64, 0x10000\n" \
"v_mfma_f32_16x16x32_bf16 v[184:187], a[184:187], v[108:111], v[184:187]\n" \
"v_exp_f32_e32 v92, v92\n" \
"v_mfma_f32_16x16x32_bf16 v[188:191], a[188:191], v[108:111], v[188:191]\n" \
"buffer_load_dwordx4 v0, s[80:83], s92 offen offset:0 lds\n" \
"v_mfma_f32_16x16x32_bf16 v[188:191], a[172:175], v[104:107], v[188:191]\n" \
"v_exp_f32_e32 v93, v93\n" \
"v_mfma_f32_16x16x32_bf16 v[208:211], a[160:163], v[112:115], v[208:211]\n" \
"v_exp_f32_e32 v94, v94\n" \
"v_mfma_f32_16x16x32_bf16 v[208:211], a[176:179], v[116:119], v[208:211]\n" \
"v_exp_f32_e32 v95, v95\n" \
"v_mfma_f32_16x16x32_bf16 v[212:215], a[180:183], v[116:119], v[212:215]\n" \
"buffer_load_dwordx4 v1, s[80:83], s92 offen offset:1024 lds\n" \
"v_mfma_f32_16x16x32_bf16 v[212:215], a[164:167], v[112:115], v[212:215]\n" \
"v_exp_f32_e32 v80, v80\n" \
"v_mfma_f32_16x16x32_bf16 v[216:219], a[168:171], v[112:115], v[216:219]\n" \
"v_exp_f32_e32 v81, v81\n" \
"v_mfma_f32_16x16x32_bf16 v[216:219], a[184:187], v[116:119], v[216:219]\n" \
"buffer_load_dwordx4 v2, s[80:83], s92 offen offset:2048 lds\n" \
"v_mfma_f32_16x16x32_bf16 v[220:223], a[188:191], v[116:119], v[220:223]\n" \
"v_exp_f32_e32 v82, v82\n" \
"v_mfma_f32_16x16x32_bf16 v[220:223], a[172:175], v[112:115], v[220:223]\n" \
"v_exp_f32_e32 v83, v83\n" \
"v_mfma_f32_16x16x32_bf16 v[240:243], a[160:163], v[120:123], v[240:243]\n" \
"buffer_load_dwordx4 v3, s[80:83], s92 offen offset:3072 lds\n" \
"v_mfma_f32_16x16x32_bf16 v[240:243], a[176:179], v[124:127], v[240:243]\n" \
"v_exp_f32_e32 v84, v84\n" \
"v_mfma_f32_16x16x32_bf16 v[244:247], a[180:183], v[124:127], v[244:247]\n" \
"v_exp_f32_e32 v85, v85\n" \
"v_mfma_f32_16x16x32_bf16 v[244:247], a[164:167], v[120:123], v[244:247]\n" \
"v_exp_f32_e32 v86, v86\n" \
"v_mfma_f32_16x16x32_bf16 v[248:251], a[168:171], v[120:123], v[248:251]\n" \
"v_exp_f32_e32 v87, v87\n" \
"v_mfma_f32_16x16x32_bf16 v[248:251], a[184:187], v[124:127], v[248:251]\n" \
"v_cvt_pk_bf16_f32 v112, v80, v81\n" \
"v_cvt_pk_bf16_f32 v113, v82, v83\n" \
"v_mfma_f32_16x16x32_bf16 v[252:255], a[188:191], v[124:127], v[252:255]\n" \
"v_cvt_pk_bf16_f32 v114, v84, v85\n" \
"v_cvt_pk_bf16_f32 v115, v86, v87\n" \
"v_mfma_f32_16x16x32_bf16 v[252:255], a[172:175], v[120:123], v[252:255]\n" \
"v_and_b32_e32 v112, v12, v112\n" \
"v_and_b32_e32 v113, v13, v113\n" \
"v_smfmac_f32_16x16x64_bf16 a[204:207], a[212:215], v[120:127], v21\n" \
"v_cvt_pk_bf16_f32 v120, v88, v89\n" \
"v_cvt_pk_bf16_f32 v121, v90, v91\n" \
"v_cvt_pk_bf16_f32 v122, v92, v93\n" \
"v_cvt_pk_bf16_f32 v123, v94, v95\n" \
"v_and_b32_e32 v114, v14, v114\n" \
"v_and_b32_e32 v115, v15, v115\n" \
"v_add_u32_e32 v48, s16, v20\n" \
"v_mul_f32_e32 v144, v54, v144\n" \
"v_mul_f32_e32 v145, v54, v145\n" \
"v_mul_f32_e32 v152, v54, v152\n" \
"v_mul_f32_e32 v153, v54, v153\n" \
"v_mul_f32_e32 v176, v55, v176\n" \
"v_mul_f32_e32 v177, v55, v177\n" \
"v_mul_f32_e32 v184, v55, v184\n" \
"v_mul_f32_e32 v185, v55, v185\n" \
"v_mul_f32_e32 v163, v55, v163\n" \
"v_cvt_pk_bf16_f32 v168, v168, v169\n" \
"v_mul_f32_e32 v170, v55, v170\n" \
"v_mul_f32_e32 v171, v55, v171\n" \
"v_cvt_pk_bf16_f32 v144, v144, v145\n" \
"v_mul_f32_e32 v146, v54, v146\n" \
"v_mul_f32_e32 v147, v54, v147\n" \
"v_cvt_pk_bf16_f32 v152, v152, v153\n" \
"v_mul_f32_e32 v154, v54, v154\n" \
"v_mul_f32_e32 v155, v54, v155\n" \
"v_cvt_pk_bf16_f32 v176, v176, v177\n" \
"v_mul_f32_e32 v178, v55, v178\n" \
"v_mul_f32_e32 v179, v55, v179\n" \
"v_cvt_pk_bf16_f32 v184, v184, v185\n" \
"v_mul_f32_e32 v186, v55, v186\n" \
"v_mul_f32_e32 v187, v55, v187\n" \
"v_cvt_pk_bf16_f32 v129, v130, v131\n" \
"v_mul_f32_e32 v132, v54, v132\n" \
"v_mul_f32_e32 v133, v54, v133\n" \
"v_mul_f32_e32 v134, v54, v134\n" \
"v_mul_f32_e32 v135, v54, v135\n" \
"v_cvt_pk_bf16_f32 v137, v138, v139\n" \
"v_mul_f32_e32 v140, v54, v140\n" \
"v_mul_f32_e32 v141, v54, v141\n" \
"v_mul_f32_e32 v142, v54, v142\n" \
"v_mul_f32_e32 v143, v54, v143\n" \
"v_cvt_pk_bf16_f32 v161, v162, v163\n" \
"v_mul_f32_e32 v164, v55, v164\n" \
"v_mul_f32_e32 v165, v55, v165\n" \
"v_mul_f32_e32 v166, v55, v166\n" \
"v_mul_f32_e32 v167, v55, v167\n" \
"v_cvt_pk_bf16_f32 v169, v170, v171\n" \
"v_mul_f32_e32 v172, v55, v172\n" \
"v_mul_f32_e32 v173, v55, v173\n" \
"v_mul_f32_e32 v174, v55, v174\n" \
"v_mul_f32_e32 v175, v55, v175\n" \
"v_cvt_pk_bf16_f32 v145, v146, v147\n" \
"v_mul_f32_e32 v148, v54, v148\n" \
"v_mul_f32_e32 v149, v54, v149\n" \
"v_mul_f32_e32 v150, v54, v150\n" \
"v_mul_f32_e32 v151, v54, v151\n" \
"v_cvt_pk_bf16_f32 v153, v154, v155\n" \
"v_mul_f32_e32 v156, v54, v156\n" \
"v_mul_f32_e32 v157, v54, v157\n" \
"v_mul_f32_e32 v158, v54, v158\n" \
"v_mul_f32_e32 v159, v54, v159\n" \
"v_cvt_pk_bf16_f32 v177, v178, v179\n" \
"v_mul_f32_e32 v180, v55, v180\n" \
"v_mul_f32_e32 v181, v55, v181\n" \
"v_mul_f32_e32 v182, v55, v182\n" \
"v_mul_f32_e32 v183, v55, v183\n" \
"v_cvt_pk_bf16_f32 v185, v186, v187\n" \
"v_mul_f32_e32 v188, v55, v188\n" \
"v_mul_f32_e32 v189, v55, v189\n" \
"v_mul_f32_e32 v190, v55, v190\n" \
"v_mul_f32_e32 v191, v55, v191\n" \
"v_cvt_pk_bf16_f32 v130, v132, v133\n" \
"v_cvt_pk_bf16_f32 v131, v134, v135\n" \
"buffer_store_dwordx4 v[128:131], v20, s[56:59], s68 offen offset:0 sc1\n" \
"v_cvt_pk_bf16_f32 v138, v140, v141\n" \
"v_cvt_pk_bf16_f32 v139, v142, v143\n" \
"buffer_store_dwordx4 v[136:139], v20, s[56:59], s68 offen offset:1024 sc1\n" \
"v_cvt_pk_bf16_f32 v162, v164, v165\n" \
"s_waitcnt vmcnt(14) lgkmcnt(0)\n" \
"s_barrier\n" \
"v_mfma_f32_16x16x32_bf16 v[88:91], a[128:131], a[48:51], v[44:47]\n" \
"s_xor_b32 m0, s65, 0x12000\n" \
"ds_read_b128 a[160:163], v49 offset:0\n" \
"v_mfma_f32_16x16x32_bf16 v[88:91], a[132:135], a[52:55], v[88:91]\n" \
"buffer_load_dwordx4 v50, s[8:11], s64 offen offset:0 sc1 lds\n" \
"v_mfma_f32_16x16x32_bf16 v[88:91], a[136:139], a[56:59], v[88:91]\n" \
"ds_read_b128 a[164:167], v49 offset:1024\n" \
"ds_read_b128 a[168:171], v49 offset:2048\n" \
"v_mfma_f32_16x16x32_bf16 v[88:91], a[140:143], a[60:63], v[88:91]\n" \
"buffer_load_dwordx4 v50, s[8:11], s64 offen offset:1024 sc1 lds\n" \
"v_mfma_f32_16x16x32_bf16 v[92:95], a[156:159], a[60:63], v[44:47]\n" \
"ds_read_b128 a[172:175], v49 offset:3072\n" \
"ds_read_b128 a[176:179], v49 offset:8192\n" \
"v_mfma_f32_16x16x32_bf16 v[92:95], a[152:155], a[56:59], v[92:95]\n" \
"ds_read_b128 a[180:183], v49 offset:9216\n" \
"ds_read_b128 a[184:187], v49 offset:10240\n" \
"v_mfma_f32_16x16x32_bf16 v[92:95], a[148:151], a[52:55], v[92:95]\n" \
"ds_read_b128 a[188:191], v49 offset:11264\n" \
"ds_read_b128 a[0:3], v49 offset:4096\n" \
"v_mfma_f32_16x16x32_bf16 v[92:95], a[144:147], a[48:51], v[92:95]\n" \
"buffer_load_dwordx4 v50, s[8:11], s64 offen offset:2048 sc1 lds\n" \
"v_mfma_f32_16x16x32_bf16 v[32:35], a[220:223], a[64:67], 0\n" \
"ds_read_b128 a[4:7], v49 offset:5120\n" \
"ds_read_b128 a[8:11], v49 offset:6144\n" \
"v_mfma_f32_16x16x32_bf16 v[36:39], a[220:223], a[80:83], 0\n" \
"ds_read_b128 a[12:15], v49 offset:7168\n" \
"ds_read_b128 a[16:19], v49 offset:12288\n" \
"v_mfma_f32_16x16x32_bf16 v[40:43], a[220:223], a[96:99], 0\n" \
"ds_read_b128 a[20:23], v49 offset:13312\n" \
"ds_read_b128 a[24:27], v49 offset:14336\n" \
"v_mfma_f32_16x16x32_bf16 v[44:47], a[220:223], a[112:115], 0\n" \
"buffer_load_dwordx4 v50, s[8:11], s64 offen offset:3072 sc1 lds\n" \
"v_mfma_f32_16x16x32_bf16 v[32:35], a[224:227], a[68:71], v[32:35]\n" \
"s_xor_b32 m0, s65, 0xa000\n" \
"ds_read_b128 a[28:31], v49 offset:15360\n" \
"v_mfma_f32_16x16x32_bf16 v[36:39], a[224:227], a[84:87], v[36:39]\n" \
"v_exp_f32_e32 v88, v88\n" \
"v_mfma_f32_16x16x32_bf16 v[40:43], a[224:227], a[100:103], v[40:43]\n" \
"buffer_load_dwordx4 v51, s[8:11], s64 offen offset:0 sc1 lds\n" \
"v_mfma_f32_16x16x32_bf16 v[44:47], a[224:227], a[116:119], v[44:47]\n" \
"v_exp_f32_e32 v89, v89\n" \
"v_mfma_f32_16x16x32_bf16 v[32:35], a[228:231], a[72:75], v[32:35]\n" \
"v_exp_f32_e32 v90, v90\n" \
"v_mfma_f32_16x16x32_bf16 v[36:39], a[228:231], a[88:91], v[36:39]\n" \
"v_exp_f32_e32 v91, v91\n" \
"v_mfma_f32_16x16x32_bf16 v[40:43], a[228:231], a[104:107], v[40:43]\n" \
"buffer_load_dwordx4 v51, s[8:11], s64 offen offset:1024 sc1 lds\n" \
"v_mfma_f32_16x16x32_bf16 v[44:47], a[228:231], a[120:123], v[44:47]\n" \
"v_exp_f32_e32 v92, v92\n" \
"v_mfma_f32_16x16x32_bf16 v[32:35], a[232:235], a[76:79], v[32:35]\n" \
"v_exp_f32_e32 v93, v93\n" \
"v_mfma_f32_16x16x32_bf16 v[36:39], a[232:235], a[92:95], v[36:39]\n" \
"v_exp_f32_e32 v94, v94\n" \
"v_mfma_f32_16x16x32_bf16 v[40:43], a[232:235], a[108:111], v[40:43]\n" \
"buffer_load_dwordx4 v51, s[8:11], s64 offen offset:2048 sc1 lds\n" \
"v_mfma_f32_16x16x32_bf16 v[44:47], a[232:235], a[124:127], v[44:47]\n" \
"v_cvt_pk_bf16_f32 v124, v88, v89\n" \
"s_waitcnt lgkmcnt(3)\n" \
"v_mfma_f32_16x16x32_bf16 v[192:195], a[160:163], v[112:115], v[192:195]\n" \
"v_exp_f32_e32 v95, v95\n" \
"v_mfma_f32_16x16x32_bf16 v[196:199], a[164:167], v[112:115], v[196:199]\n" \
"buffer_load_dwordx4 v51, s[8:11], s64 offen offset:3072 sc1 lds\n" \
"v_mfma_f32_16x16x32_bf16 v[200:203], a[168:171], v[112:115], v[200:203]\n" \
"s_add_u32 m0, s93, 0x1000\n" \
"v_cvt_pk_bf16_f32 v125, v90, v91\n" \
"v_mfma_f32_16x16x32_bf16 v[204:207], a[172:175], v[112:115], v[204:207]\n" \
"v_cvt_pk_bf16_f32 v126, v92, v93\n" \
"v_cvt_pk_bf16_f32 v127, v94, v95\n" \
"v_mfma_f32_16x16x32_bf16 v[208:211], a[176:179], v[112:115], v[208:211]\n" \
"v_and_b32_e32 v124, v12, v124\n" \
"v_and_b32_e32 v125, v13, v125\n" \
"v_mfma_f32_16x16x32_bf16 v[212:215], a[180:183], v[112:115], v[212:215]\n" \
"buffer_load_dwordx4 v4, s[80:83], s92 offen offset:0 lds\n" \
"v_mfma_f32_16x16x32_bf16 v[216:219], a[184:187], v[112:115], v[216:219]\n" \
"v_and_b32_e32 v126, v14, v126\n" \
"v_and_b32_e32 v127, v15, v127\n" \
"v_mfma_f32_16x16x32_bf16 v[220:223], a[188:191], v[112:115], v[220:223]\n" \
"ds_read_b128 a[128:131], v48 offset:0\n" \
"ds_read_b128 a[132:135], v48 offset:1024\n" \
"v_mfma_f32_16x16x32_bf16 a[200:203], a[252:255], v[112:115], a[200:203]\n" \
"ds_read_b128 a[136:139], v48 offset:2048\n" \
"ds_read_b128 a[140:143], v48 offset:3072\n" \
"v_mfma_f32_16x16x32_bf16 v[224:227], a[160:163], v[120:123], v[224:227]\n" \
"buffer_load_dwordx4 v5, s[80:83], s92 offen offset:1024 lds\n" \
"v_mfma_f32_16x16x32_bf16 v[228:231], a[164:167], v[120:123], v[228:231]\n" \
"ds_read_b128 a[144:147], v48 offset:4096\n" \
"ds_read_b128 a[148:151], v48 offset:5120\n" \
"v_mfma_f32_16x16x32_bf16 v[232:235], a[168:171], v[120:123], v[232:235]\n" \
"ds_read_b128 a[152:155], v48 offset:6144\n" \
"ds_read_b128 a[156:159], v48 offset:7168\n" \
"v_mfma_f32_16x16x32_bf16 v[236:239], a[172:175], v[120:123], v[236:239]\n" \
"buffer_load_dwordx4 v6, s[80:83], s92 offen offset:2048 lds\n" \
"v_mfma_f32_16x16x32_bf16 v[240:243], a[176:179], v[120:123], v[240:243]\n" \
"v_cvt_pk_bf16_f32 v163, v166, v167\n" \
"v_cvt_pk_bf16_f32 v170, v172, v173\n" \
"buffer_store_dwordx4 v[160:163], v20, s[56:59], s69 offen offset:0 sc1\n" \
"v_mfma_f32_16x16x32_bf16 v[244:247], a[180:183], v[120:123], v[244:247]\n" \
"v_cvt_pk_bf16_f32 v171, v174, v175\n" \
"v_cvt_pk_bf16_f32 v146, v148, v149\n" \
"buffer_store_dwordx4 v[168:171], v20, s[56:59], s69 offen offset:1024 sc1\n" \
"v_mfma_f32_16x16x32_bf16 v[248:251], a[184:187], v[120:123], v[248:251]\n" \
"v_cvt_pk_bf16_f32 v147, v150, v151\n" \
"v_cvt_pk_bf16_f32 v154, v156, v157\n" \
"buffer_store_dwordx4 v[144:147], v20, s[56:59], s68 offen offset:2048 sc1\n" \
"v_mfma_f32_16x16x32_bf16 v[252:255], a[188:191], v[120:123], v[252:255]\n" \
"buffer_load_dwordx4 v7, s[80:83], s92 offen offset:3072 lds\n" \
"v_mfma_f32_16x16x32_bf16 a[204:207], a[252:255], v[120:123], a[204:207]\n" \
"v_cvt_pk_bf16_f32 v155, v158, v159\n" \
"v_cvt_pk_bf16_f32 v178, v180, v181\n" \
"buffer_store_dwordx4 v[152:155], v20, s[56:59], s68 offen offset:3072 sc1\n" \
"v_mfma_f32_16x16x32_bf16 v[224:227], a[0:3], v[124:127], v[224:227]\n" \
"v_cvt_pk_bf16_f32 v179, v182, v183\n" \
"v_cvt_pk_bf16_f32 v186, v188, v189\n" \
"buffer_store_dwordx4 v[176:179], v20, s[56:59], s69 offen offset:2048 sc1\n" \
"v_mfma_f32_16x16x32_bf16 v[228:231], a[4:7], v[124:127], v[228:231]\n" \
"v_cvt_pk_bf16_f32 v187, v190, v191\n" \
"s_add_u32 m0, s93, 0x2000\n" \
"buffer_store_dwordx4 v[184:187], v20, s[56:59], s69 offen offset:3072 sc1\n" \
"v_mfma_f32_16x16x32_bf16 v[232:235], a[8:11], v[124:127], v[232:235]\n" \
"buffer_load_dwordx4 v8, s[80:83], s92 offen offset:0 lds\n" \
"v_mfma_f32_16x16x32_bf16 v[236:239], a[12:15], v[124:127], v[236:239]\n" \
"v_mfma_f32_16x16x32_bf16 a[208:211], a[216:219], v[128:131], a[208:211]\n" \
"v_mfma_f32_16x16x32_bf16 a[208:211], a[216:219], v[136:139], a[208:211]\n" \
"v_mfma_f32_32x32x16_bf16 v[128:143], a[216:219], a[216:219], 0\n" \
"v_mfma_f32_16x16x32_bf16 a[208:211], a[216:219], v[160:163], a[208:211]\n" \
"v_mfma_f32_16x16x32_bf16 a[208:211], a[216:219], v[168:171], a[208:211]\n" \
"v_mfma_f32_32x32x16_bf16 v[160:175], a[216:219], a[216:219], 0\n" \
"v_accvgpr_read_b32 v56, a200\n" \
"v_accvgpr_read_b32 v23, a201\n" \
"v_add_f32_e32 v56, v56, v23\n" \
"v_mfma_f32_16x16x32_bf16 a[208:211], a[216:219], v[144:147], a[208:211]\n" \
"v_max_f32_e32 v62, v62, v56\n" \
"v_readlane_b32 s95, v11, 2\n" \
"v_mfma_f32_16x16x32_bf16 v[240:243], a[16:19], v[124:127], v[240:243]\n" \
"v_add_u32_e32 v49, 0x10000, v48\n" \
"s_waitcnt lgkmcnt(0)\n" \
"v_mfma_f32_16x16x32_bf16 v[244:247], a[20:23], v[124:127], v[244:247]\n" \
"v_rcp_f32_e32 v56, v56\n" \
"v_mfma_f32_16x16x32_bf16 v[248:251], a[24:27], v[124:127], v[248:251]\n" \
"v_mfma_f32_16x16x32_bf16 a[208:211], a[216:219], v[152:155], a[208:211]\n" \
"v_mfma_f32_32x32x16_bf16 v[144:159], a[216:219], a[216:219], 0\n" \
"v_mul_f32_e32 v56, v56, v60\n" \
"v_mfma_f32_16x16x32_bf16 a[208:211], a[216:219], v[176:179], a[208:211]\n" \
"v_mul_f32_e32 v192, v56, v192\n" \
"v_mul_f32_e32 v193, v56, v193\n" \
"v_mfma_f32_16x16x32_bf16 v[252:255], a[28:31], v[124:127], v[252:255]\n" \
"v_cvt_pk_bf16_f32 v192, v192, v193\n" \
"v_mul_f32_e32 v194, v56, v194\n" \
"v_mfma_f32_16x16x32_bf16 a[204:207], a[252:255], v[124:127], a[204:207]\n" \
"v_mul_f32_e32 v195, v56, v195\n" \
"v_cvt_pk_bf16_f32 v193, v194, v195\n" \
"s_waitcnt vmcnt(19)\n" \
"s_barrier\n" \
"v_mfma_f32_16x16x32_bf16 v[64:67], a[128:131], a[64:67], v[32:35]\n" \
"s_xor_b32 m0, s65, 0x18000\n" \
"v_mul_f32_e32 v196, v56, v196\n" \
"v_mfma_f32_16x16x32_bf16 v[64:67], a[132:135], a[68:71], v[64:67]\n" \
"buffer_load_dwordx4 v52, s[8:11], s64 offen offset:0 sc1 lds\n" \
"v_mfma_f32_16x16x32_bf16 v[64:67], a[136:139], a[72:75], v[64:67]\n" \
"v_mfma_f32_16x16x32_bf16 a[208:211], a[216:219], v[184:187], a[208:211]\n" \
"v_mfma_f32_32x32x16_bf16 v[176:191], a[216:219], a[216:219], 0\n" \
"v_mul_f32_e32 v197, v56, v197\n" \
"v_mul_f32_e32 v198, v56, v198\n" \
"v_mul_f32_e32 v199, v56, v199\n" \
"v_mul_f32_e32 v200, v56, v200\n" \
"v_mul_f32_e32 v201, v56, v201\n" \
"v_accvgpr_read_b32 v57, a204\n" \
"v_mfma_f32_16x16x32_bf16 v[64:67], a[140:143], a[76:79], v[64:67]\n" \
"buffer_load_dwordx4 v52, s[8:11], s64 offen offset:1024 sc1 lds\n" \
"v_mfma_f32_16x16x32_bf16 v[68:71], a[156:159], a[76:79], v[32:35]\n" \
"v_accvgpr_read_b32 v23, a205\n" \
"v_cvt_pk_bf16_f32 v194, v196, v197\n" \
"v_mfma_f32_32x32x16_bf16 a[192:207], a[216:219], a[216:219], 0\n" \
"v_cvt_pk_bf16_f32 v195, v198, v199\n" \
"buffer_store_dwordx4 v[192:195], v20, s[56:59], s70 offen offset:0 sc1\n" \
"v_cvt_pk_bf16_f32 v200, v200, v201\n" \
"v_mfma_f32_16x16x32_bf16 v[68:71], a[152:155], a[72:75], v[68:71]\n" \
"v_mul_f32_e32 v202, v56, v202\n" \
"v_mul_f32_e32 v203, v56, v203\n" \
"v_mfma_f32_16x16x32_bf16 v[68:71], a[148:151], a[68:71], v[68:71]\n" \
"v_add_f32_e32 v57, v57, v23\n" \
"v_cvt_pk_bf16_f32 v201, v202, v203\n" \
"v_mfma_f32_16x16x32_bf16 v[68:71], a[144:147], a[64:67], v[68:71]\n" \
"buffer_load_dwordx4 v52, s[8:11], s64 offen offset:2048 sc1 lds\n" \
"v_mfma_f32_16x16x32_bf16 v[72:75], a[128:131], a[80:83], v[36:39]\n" \
"v_mfma_f32_16x16x32_bf16 a[208:211], a[216:219], v[192:195], a[208:211]\n" \
"v_mul_f32_e32 v204, v56, v204\n" \
"v_mul_f32_e32 v205, v56, v205\n" \
"v_mfma_f32_16x16x32_bf16 v[72:75], a[132:135], a[84:87], v[72:75]\n" \
"v_mul_f32_e32 v206, v56, v206\n" \
"v_mul_f32_e32 v207, v56, v207\n" \
"v_mfma_f32_16x16x32_bf16 v[72:75], a[136:139], a[88:91], v[72:75]\n" \
"v_mul_f32_e32 v208, v56, v208\n" \
"v_mul_f32_e32 v209, v56, v209\n" \
"v_mfma_f32_16x16x32_bf16 v[72:75], a[140:143], a[92:95], v[72:75]\n" \
"buffer_load_dwordx4 v52, s[8:11], s64 offen offset:3072 sc1 lds\n" \
"v_mfma_f32_16x16x32_bf16 v[76:79], a[156:159], a[92:95], v[36:39]\n" \
"s_mov_b32 m0, s65\n" \
"v_max_f32_e32 v62, v62, v57\n" \
"v_mfma_f32_16x16x32_bf16 v[76:79], a[152:155], a[88:91], v[76:79]\n" \
"v_cvt_pk_bf16_f32 v202, v204, v205\n" \
"v_cvt_pk_bf16_f32 v203, v206, v207\n" \
"buffer_store_dwordx4 v[200:203], v20, s[56:59], s70 offen offset:1024 sc1\n" \
"v_mfma_f32_16x16x32_bf16 v[76:79], a[148:151], a[84:87], v[76:79]\n" \
"v_cvt_pk_bf16_f32 v208, v208, v209\n" \
"v_mul_f32_e32 v210, v56, v210\n" \
"v_mfma_f32_16x16x32_bf16 v[76:79], a[144:147], a[80:83], v[76:79]\n" \
"buffer_load_dwordx4 v53, s[8:11], s64 offen offset:0 sc1 lds\n" \
"v_mfma_f32_16x16x32_bf16 v[80:83], a[128:131], a[96:99], v[40:43]\n" \
"v_mul_f32_e32 v211, v56, v211\n" \
"v_cvt_pk_bf16_f32 v209, v210, v211\n" \
"v_mfma_f32_16x16x32_bf16 v[80:83], a[132:135], a[100:103], v[80:83]\n" \
"v_rcp_f32_e32 v57, v57\n" \
"v_mfma_f32_16x16x32_bf16 v[80:83], a[136:139], a[104:107], v[80:83]\n" \
"buffer_load_dwordx4 v53, s[8:11], s64 offen offset:1024 sc1 lds\n" \
"v_mfma_f32_16x16x32_bf16 v[80:83], a[140:143], a[108:111], v[80:83]\n" \
"v_mfma_f32_16x16x32_bf16 a[208:211], a[216:219], v[200:203], a[208:211]\n" \
"v_mfma_f32_32x32x16_bf16 v[192:207], a[216:219], a[216:219], 0\n" \
"v_mul_f32_e32 v212, v56, v212\n" \
"v_mul_f32_e32 v213, v56, v213\n" \
"v_mul_f32_e32 v214, v56, v214\n" \
"v_mul_f32_e32 v215, v56, v215\n" \
"v_mul_f32_e32 v216, v56, v216\n" \
"v_mul_f32_e32 v217, v56, v217\n" \
"v_mfma_f32_16x16x32_bf16 v[84:87], a[156:159], a[108:111], v[40:43]\n" \
"v_cvt_pk_bf16_f32 v210, v212, v213\n" \
"v_cvt_pk_bf16_f32 v211, v214, v215\n" \
"buffer_store_dwordx4 v[208:211], v20, s[56:59], s70 offen offset:2048 sc1\n" \
"v_mfma_f32_16x16x32_bf16 v[84:87], a[152:155], a[104:107], v[84:87]\n" \
"v_cvt_pk_bf16_f32 v216, v216, v217\n" \
"v_mul_f32_e32 v218, v56, v218\n" \
"v_mfma_f32_16x16x32_bf16 v[84:87], a[148:151], a[100:103], v[84:87]\n" \
"buffer_load_dwordx4 v53, s[8:11], s64 offen offset:2048 sc1 lds\n" \
"v_mfma_f32_16x16x32_bf16 v[84:87], a[144:147], a[96:99], v[84:87]\n" \
"v_mul_f32_e32 v219, v56, v219\n" \
"v_mul_f32_e32 v57, v57, v61\n" \
"v_mfma_f32_16x16x32_bf16 v[88:91], a[128:131], a[112:115], v[44:47]\n" \
"ds_read_b128 a[128:131], v48 offset:8192\n" \
"v_cvt_pk_bf16_f32 v217, v218, v219\n" \
"v_mfma_f32_16x16x32_bf16 v[88:91], a[132:135], a[116:119], v[88:91]\n" \
"ds_read_b128 a[132:135], v48 offset:9216\n" \
"v_mul_f32_e32 v220, v56, v220\n" \
"v_mfma_f32_16x16x32_bf16 v[88:91], a[136:139], a[120:123], v[88:91]\n" \
"ds_read_b128 a[136:139], v48 offset:10240\n" \
"v_mul_f32_e32 v221, v56, v221\n" \
"v_mfma_f32_16x16x32_bf16 v[88:91], a[140:143], a[124:127], v[88:91]\n" \
"ds_read_b128 a[140:143], v48 offset:11264\n" \
"v_mul_f32_e32 v222, v56, v222\n" \
"v_mfma_f32_16x16x32_bf16 v[92:95], a[156:159], a[124:127], v[44:47]\n" \
"ds_read_b128 a[156:159], v48 offset:15360\n" \
"v_mul_f32_e32 v223, v56, v223\n" \
"v_mfma_f32_16x16x32_bf16 v[92:95], a[152:155], a[120:123], v[92:95]\n" \
"ds_read_b128 a[152:155], v48 offset:14336\n" \
"v_mul_f32_e32 v224, v57, v224\n" \
"v_mfma_f32_16x16x32_bf16 v[92:95], a[148:151], a[116:119], v[92:95]\n" \
"ds_read_b128 a[148:151], v48 offset:13312\n" \
"v_mul_f32_e32 v225, v57, v225\n" \
"v_mfma_f32_16x16x32_bf16 v[92:95], a[144:147], a[112:115], v[92:95]\n" \
"ds_read_b128 a[144:147], v48 offset:12288\n" \
"v_mfma_f32_16x16x32_bf16 a[208:211], a[216:219], v[208:211], a[208:211]\n" \
"v_cvt_pk_bf16_f32 v218, v220, v221\n" \
"v_cvt_pk_bf16_f32 v219, v222, v223\n" \
"buffer_store_dwordx4 v[216:219], v20, s[56:59], s70 offen offset:3072 sc1\n" \
"v_cvt_pk_bf16_f32 v224, v224, v225\n" \
"v_mul_f32_e32 v226, v57, v226\n" \
"v_mul_f32_e32 v227, v57, v227\n" \
"v_mfma_f32_16x16x32_bf16 a[208:211], a[216:219], v[216:219], a[208:211]\n" \
"v_mfma_f32_32x32x16_bf16 v[208:223], a[216:219], a[216:219], 0\n" \
"v_cvt_pk_bf16_f32 v225, v226, v227\n" \
"v_mul_f32_e32 v228, v57, v228\n" \
"v_mul_f32_e32 v229, v57, v229\n" \
"v_mul_f32_e32 v230, v57, v230\n" \
"v_mul_f32_e32 v231, v57, v231\n" \
"v_mul_f32_e32 v232, v57, v232\n" \
"v_mul_f32_e32 v233, v57, v233\n" \
"v_cvt_pk_bf16_f32 v226, v228, v229\n" \
"v_cvt_pk_bf16_f32 v227, v230, v231\n" \
"buffer_store_dwordx4 v[224:227], v20, s[56:59], s71 offen offset:0 sc1\n" \
"v_cvt_pk_bf16_f32 v232, v232, v233\n" \
"v_mul_f32_e32 v234, v57, v234\n" \
"v_mul_f32_e32 v235, v57, v235\n" \
"v_mfma_f32_16x16x32_bf16 a[208:211], a[216:219], v[224:227], a[208:211]\n" \
"v_cvt_pk_bf16_f32 v233, v234, v235\n" \
"v_mul_f32_e32 v236, v57, v236\n" \
"v_mul_f32_e32 v237, v57, v237\n" \
"v_mul_f32_e32 v238, v57, v238\n" \
"v_mul_f32_e32 v239, v57, v239\n" \
"v_mul_f32_e32 v240, v57, v240\n" \
"v_mul_f32_e32 v241, v57, v241\n" \
"v_cvt_pk_bf16_f32 v234, v236, v237\n" \
"v_cvt_pk_bf16_f32 v235, v238, v239\n" \
"buffer_store_dwordx4 v[232:235], v20, s[56:59], s71 offen offset:1024 sc1\n" \
"v_cvt_pk_bf16_f32 v240, v240, v241\n" \
"v_mul_f32_e32 v242, v57, v242\n" \
"v_mul_f32_e32 v243, v57, v243\n" \
"v_mfma_f32_16x16x32_bf16 a[208:211], a[216:219], v[232:235], a[208:211]\n" \
"v_mfma_f32_32x32x16_bf16 v[224:239], a[216:219], a[216:219], 0\n" \
"v_cvt_pk_bf16_f32 v241, v242, v243\n" \
"v_mul_f32_e32 v244, v57, v244\n" \
"v_mul_f32_e32 v245, v57, v245\n" \
"v_mul_f32_e32 v246, v57, v246\n" \
"v_mul_f32_e32 v247, v57, v247\n" \
"v_mul_f32_e32 v248, v57, v248\n" \
"v_mul_f32_e32 v249, v57, v249\n" \
"v_cvt_pk_bf16_f32 v242, v244, v245\n" \
"v_cvt_pk_bf16_f32 v243, v246, v247\n" \
"buffer_store_dwordx4 v[240:243], v20, s[56:59], s71 offen offset:2048 sc1\n" \
"v_cvt_pk_bf16_f32 v248, v248, v249\n" \
"v_mul_f32_e32 v250, v57, v250\n" \
"v_mul_f32_e32 v251, v57, v251\n" \
"v_mfma_f32_16x16x32_bf16 a[208:211], a[216:219], v[240:243], a[208:211]\n" \
"v_cvt_pk_bf16_f32 v249, v250, v251\n" \
"v_mul_f32_e32 v252, v57, v252\n" \
"v_mul_f32_e32 v253, v57, v253\n" \
"v_mul_f32_e32 v254, v57, v254\n" \
"v_mul_f32_e32 v255, v57, v255\n" \
"v_cvt_pk_bf16_f32 v250, v252, v253\n" \
"v_cvt_pk_bf16_f32 v251, v254, v255\n" \
"buffer_store_dwordx4 v[248:251], v20, s[56:59], s71 offen offset:3072 sc1\n" \
"buffer_load_dwordx4 v53, s[8:11], s64 offen offset:3072 sc1 lds\n" \
"v_mfma_f32_16x16x32_bf16 a[208:211], a[216:219], v[248:251], a[208:211]\n" \
"v_mfma_f32_32x32x16_bf16 v[240:255], a[216:219], a[216:219], 0\n" \
"v_cmp_lt_f32_e64 s[72:73], s100, v62\n" \
"v_exp_f32_e32 v64, v64\n" \
"v_exp_f32_e32 v65, v65\n" \
"s_xor_b32 s65, s65, 0x8000\n" \
"s_waitcnt lgkmcnt(0)\n" \
"v_exp_f32_e32 v66, v66\n" \
"v_exp_f32_e32 v67, v67\n" \
"v_accvgpr_read_b32 v63, a208\n" \
"v_cmp_u_f32_e32 vcc, v63, v63\n" \
"v_exp_f32_e32 v68, v68\n" \
"s_or_b64 vcc, s[72:73], vcc\n" \
"s_cselect_b32 s95, 0x40000000, s95\n" \
"s_cselect_b32 s94, 0x7f800000, 0\n" \
"v_exp_f32_e32 v69, v69\n" \
"v_exp_f32_e32 v70, v70\n" \
"v_exp_f32_e32 v71, v71\n" \
"v_exp_f32_e32 v72, v72\n" \
"v_exp_f32_e32 v73, v73\n" \
"v_exp_f32_e32 v74, v74\n" \
"v_exp_f32_e32 v75, v75\n" \
"v_exp_f32_e32 v76, v76\n" \
"v_exp_f32_e32 v77, v77\n" \
"v_exp_f32_e32 v78, v78\n" \
"v_exp_f32_e32 v79, v79\n" \
"v_exp_f32_e32 v80, v80\n" \
"v_exp_f32_e32 v81, v81\n" \
"v_exp_f32_e32 v82, v82\n" \
"v_exp_f32_e32 v83, v83\n" \
"v_exp_f32_e32 v84, v84\n" \
"v_exp_f32_e32 v85, v85\n" \
"v_exp_f32_e32 v86, v86\n" \
"v_exp_f32_e32 v87, v87\n" \
"s_add_u32 s64, s64, 0x10000\n" \
"v_exp_f32_e32 v88, v88\n" \
"v_exp_f32_e32 v89, v89\n" \
"v_exp_f32_e32 v90, v90\n" \
"v_exp_f32_e32 v91, v91\n" \
"v_cvt_pk_bf16_f32 v96, v64, v65\n" \
"v_cvt_pk_bf16_f32 v97, v66, v67\n" \
"v_cvt_pk_bf16_f32 v98, v68, v69\n" \
"v_cvt_pk_bf16_f32 v99, v70, v71\n" \
"v_cvt_pk_bf16_f32 v104, v72, v73\n" \
"v_cvt_pk_bf16_f32 v105, v74, v75\n" \
"v_cvt_pk_bf16_f32 v106, v76, v77\n" \
"v_cvt_pk_bf16_f32 v107, v78, v79\n" \
"v_cvt_pk_bf16_f32 v112, v80, v81\n" \
"v_cvt_pk_bf16_f32 v113, v82, v83\n" \
"v_cvt_pk_bf16_f32 v114, v84, v85\n" \
"v_cvt_pk_bf16_f32 v115, v86, v87\n" \
"v_xor_b32_e32 v48, 0x8000, v48\n" \
"s_sub_u32 s66, s19, 1\n" \
"s_cbranch_scc0 L_2_%=\n" \
"s_waitcnt vmcnt(16)\n" \
"s_barrier\n" \
"L_f1_%=:\n" \
"L_pf17:\n" \
"s_cbranch_execz L_retA\n" \
"s_nop 0\n" \
"ds_read_b128 a[160:163], v49 offset:0\n" \
"ds_read_b128 a[164:167], v49 offset:1024\n" \
"ds_read_b128 a[168:171], v49 offset:2048\n" \
"ds_read_b128 a[172:175], v49 offset:3072\n" \
"ds_read_b128 a[176:179], v49 offset:8192\n" \
"ds_read_b128 a[180:183], v49 offset:9216\n" \
"v_mfma_f32_16x16x32_bf16 v[72:75], a[128:131], a[80:83], v[36:39]\n" \
"s_add_u32 s68, s61, 0x0\n" \
"s_add_u32 s69, s61, 0x49000\n" \
"ds_read_b128 a[184:187], v49 offset:10240\n" \
"v_mfma_f32_16x16x32_bf16 v[72:75], a[132:135], a[84:87], v[72:75]\n" \
"ds_read_b128 a[188:191], v49 offset:11264\n" \
"v_add_u32_e32 v56, 0x49000, v20\n" \
"v_mfma_f32_16x16x32_bf16 v[72:75], a[136:139], a[88:91], v[72:75]\n" \
"v_readlane_b32 s97, v11, 32\n" \
"v_readlane_b32 s98, v11, 33\n" \
"v_mfma_f32_16x16x32_bf16 v[72:75], a[140:143], a[92:95], v[72:75]\n" \
"v_and_b32_e32 v96, v12, v96\n" \
"v_and_b32_e32 v97, v13, v97\n" \
"v_mfma_f32_16x16x32_bf16 v[76:79], a[156:159], a[92:95], v[36:39]\n" \
"buffer_load_dwordx4 a[32:35], v20, s[56:59], s97 offen offset:0 sc1\n" \
"v_mfma_f32_16x16x32_bf16 v[76:79], a[152:155], a[88:91], v[76:79]\n" \
"v_and_b32_e32 v98, v14, v98\n" \
"v_and_b32_e32 v99, v15, v99\n" \
"v_mfma_f32_16x16x32_bf16 v[76:79], a[148:151], a[84:87], v[76:79]\n" \
"ds_read_b128 a[0:3], v20 offset:8192\n" \
"v_mov_b32_e32 v61, s62\n" \
"v_mfma_f32_16x16x32_bf16 v[76:79], a[144:147], a[80:83], v[76:79]\n" \
"buffer_load_dwordx4 a[40:43], v56, s[56:59], s97 offen offset:0 sc1\n" \
"ds_read_b128 a[4:7], v20 offset:9216\n" \
"ds_read_b128 a[8:11], v20 offset:10240\n" \
"s_waitcnt lgkmcnt(10)\n" \
"v_mfma_f32_16x16x32_bf16 v[128:131], a[160:163], v[96:99], v[128:131]\n" \
"ds_read_b128 a[12:15], v20 offset:11264\n" \
"s_waitcnt lgkmcnt(10)\n" \
"v_mfma_f32_16x16x32_bf16 v[132:135], a[164:167], v[96:99], v[132:135]\n" \
"ds_read_b128 a[28:31], v20 offset:15360\n" \
"buffer_load_dwordx4 a[48:51], v20, s[56:59], s98 offen offset:0 sc1\n" \
"s_waitcnt lgkmcnt(10)\n" \
"v_mfma_f32_16x16x32_bf16 v[136:139], a[168:171], v[96:99], v[136:139]\n" \
"ds_read_b128 a[24:27], v20 offset:14336\n" \
"s_waitcnt lgkmcnt(10)\n" \
"v_mfma_f32_16x16x32_bf16 v[140:143], a[172:175], v[96:99], v[140:143]\n" \
"ds_read_b128 a[20:23], v20 offset:13312\n" \
"buffer_load_dwordx4 a[56:59], v56, s[56:59], s98 offen offset:0 sc1\n" \
"s_waitcnt lgkmcnt(9)\n" \
"v_mfma_f32_16x16x32_bf16 v[144:147], a[176:179], v[96:99], v[144:147]\n" \
"ds_read_b128 a[16:19], v20 offset:12288\n" \
"v_add_u32_e32 v50, s15, v48\n" \
"v_mfma_f32_16x16x32_bf16 v[148:151], a[180:183], v[96:99], v[148:151]\n" \
"v_xor_b32_e32 v48, 0x4000, v49\n" \
"s_waitcnt lgkmcnt(8)\n" \
"v_mfma_f32_16x16x32_bf16 v[152:155], a[184:187], v[96:99], v[152:155]\n" \
"buffer_load_dwordx4 a[36:39], v20, s[56:59], s97 offen offset:1024 sc1\n" \
"v_mfma_f32_16x16x32_bf16 v[156:159], a[188:191], v[96:99], v[156:159]\n" \
"ds_read_b32 v58, v18 offset:256\n" \
"ds_read_b32 v59, v18 offset:384\n" \
"v_mfma_f32_16x16x32_bf16 a[192:195], a[252:255], v[96:99], a[192:195]\n" \
"ds_read_b128 a[96:99], v48 offset:4096\n" \
"ds_read_b128 a[100:103], v48 offset:5120\n" \
"buffer_load_dwordx4 a[44:47], v56, s[56:59], s97 offen offset:1024 sc1\n" \
"v_mfma_f32_16x16x32_bf16 v[160:163], a[160:163], v[104:107], v[160:163]\n" \
"ds_read_b128 a[104:107], v48 offset:6144\n" \
"ds_read_b128 a[108:111], v48 offset:7168\n" \
"v_mfma_f32_16x16x32_bf16 v[164:167], a[164:167], v[104:107], v[164:167]\n" \
"ds_read_b128 a[112:115], v48 offset:12288\n" \
"ds_read_b128 a[116:119], v48 offset:13312\n" \
"buffer_load_dwordx4 a[52:55], v20, s[56:59], s98 offen offset:1024 sc1\n" \
"v_mfma_f32_16x16x32_bf16 v[168:171], a[168:171], v[104:107], v[168:171]\n" \
"ds_read_b128 a[120:123], v48 offset:14336\n" \
"ds_read_b128 a[124:127], v48 offset:15360\n" \
"v_mfma_f32_16x16x32_bf16 v[172:175], a[172:175], v[104:107], v[172:175]\n" \
"ds_read_b128 a[128:131], v49 offset:4096\n" \
"ds_read_b128 a[132:135], v49 offset:5120\n" \
"buffer_load_dwordx4 a[60:63], v56, s[56:59], s98 offen offset:1024 sc1\n" \
"v_mfma_f32_16x16x32_bf16 v[176:179], a[176:179], v[104:107], v[176:179]\n" \
"ds_read_b128 a[136:139], v49 offset:6144\n" \
"ds_read_b128 a[140:143], v49 offset:7168\n" \
"v_mfma_f32_16x16x32_bf16 v[180:183], a[180:183], v[104:107], v[180:183]\n" \
"ds_read_b128 a[144:147], v49 offset:12288\n" \
"ds_read_b128 a[148:151], v49 offset:13312\n" \
"s_waitcnt vmcnt(8)\n" \
"s_mov_b64 exec, 1\n" \
"buffer_store_dword v61, off, s[56:59], s95 offset:0 sc1\n" \
"s_mov_b64 exec, -1\n" \
"v_mfma_f32_16x16x32_bf16 v[184:187], a[184:187], v[104:107], v[184:187]\n" \
"ds_read_b128 a[152:155], v49 offset:14336\n" \
"ds_read_b128 a[156:159], v49 offset:15360\n" \
"v_mfma_f32_16x16x32_bf16 v[188:191], a[188:191], v[104:107], v[188:191]\n" \
"v_accvgpr_read_b32 v40, a200\n" \
"v_accvgpr_read_b32 v23, a201\n" \
"v_mfma_f32_16x16x32_bf16 a[196:199], a[252:255], v[104:107], a[196:199]\n" \
"v_add_f32_e32 v40, v40, v23\n" \
"s_waitcnt lgkmcnt(15)\n" \
"v_mfma_f32_16x16x32_bf16 v[80:83], a[0:3], a[64:67], v[32:35]\n" \
"buffer_load_dwordx4 a[220:223], v20, s[56:59], s60 offen offset:0 sc1\n" \
"v_mfma_f32_16x16x32_bf16 v[80:83], a[4:7], a[68:71], v[80:83]\n" \
"v_accvgpr_read_b32 v41, a204\n" \
"v_accvgpr_read_b32 v23, a205\n" \
"v_mfma_f32_16x16x32_bf16 v[80:83], a[8:11], a[72:75], v[80:83]\n" \
"v_add_f32_e32 v41, v41, v23\n" \
"v_add_u32_e32 v51, 0x10000, v50\n" \
"v_mfma_f32_16x16x32_bf16 v[80:83], a[12:15], a[76:79], v[80:83]\n" \
"buffer_load_dwordx4 a[236:239], v56, s[56:59], s60 offen offset:0 sc1\n" \
"v_mfma_f32_16x16x32_bf16 v[84:87], a[28:31], a[76:79], v[32:35]\n" \
"v_add_f32_e32 v40, s94, v40\n" \
"v_add_f32_e32 v41, s94, v41\n" \
"v_mfma_f32_16x16x32_bf16 v[84:87], a[24:27], a[72:75], v[84:87]\n" \
"ds_write_b128 v50, v[192:195] offset:0\n" \
"buffer_load_dwordx4 a[224:227], v20, s[56:59], s60 offen offset:1024 sc1\n" \
"v_mfma_f32_16x16x32_bf16 v[84:87], a[20:23], a[68:71], v[84:87]\n" \
"ds_write_b128 v50, v[196:199] offset:1024\n" \
"v_mfma_f32_16x16x32_bf16 v[84:87], a[16:19], a[64:67], v[84:87]\n" \
"ds_write_b128 v50, v[200:203] offset:2048\n" \
"buffer_load_dwordx4 a[240:243], v56, s[56:59], s60 offen offset:1024 sc1\n" \
"v_mfma_f32_16x16x32_bf16 v[88:91], a[0:3], a[80:83], v[36:39]\n" \
"ds_write_b128 v50, v[204:207] offset:3072\n" \
"L_pf18:\n" \
"s_cbranch_execz L_retA\n" \
"s_nop 0\n" \
"v_mfma_f32_16x16x32_bf16 v[88:91], a[4:7], a[84:87], v[88:91]\n" \
"ds_write_b128 v50, v[208:211] offset:8192\n" \
"buffer_load_dwordx4 a[228:231], v20, s[56:59], s60 offen offset:2048 sc1\n" \
"v_mfma_f32_16x16x32_bf16 v[88:91], a[8:11], a[88:91], v[88:91]\n" \
"ds_write_b128 v50, v[212:215] offset:9216\n" \
"v_mfma_f32_16x16x32_bf16 v[88:91], a[12:15], a[92:95], v[88:91]\n" \
"ds_write_b128 v50, v[216:219] offset:10240\n" \
"buffer_load_dwordx4 a[244:247], v56, s[56:59], s60 offen offset:2048 sc1\n" \
"v_mfma_f32_16x16x32_bf16 v[92:95], a[28:31], a[92:95], v[36:39]\n" \
"ds_write_b128 v50, v[220:223] offset:11264\n" \
"ds_write_b128 v51, v[224:227] offset:0\n" \
"ds_write_b128 v51, v[228:231] offset:1024\n" \
"buffer_load_dwordx4 a[232:235], v20, s[56:59], s60 offen offset:3072 sc1\n" \
"ds_write_b128 v51, v[232:235] offset:2048\n" \
"ds_write_b128 v51, v[236:239] offset:3072\n" \
"ds_write_b128 v51, v[240:243] offset:8192\n" \
"ds_write_b128 v51, v[244:247] offset:9216\n" \
"buffer_load_dwordx4 a[248:251], v56, s[56:59], s60 offen offset:3072 sc1\n" \
"ds_write_b128 v51, v[248:251] offset:10240\n" \
"ds_write_b128 v51, v[252:255] offset:11264\n" \
"ds_write_b64 v19, v[40:41]\n" \
"v_mfma_f32_16x16x32_bf16 v[92:95], a[24:27], a[88:91], v[92:95]\n" \
"v_xor_b32_e32 v52, 0x4000, v50\n" \
"v_xor_b32_e32 v53, 0x4000, v51\n" \
"v_mfma_f32_16x16x32_bf16 v[92:95], a[20:23], a[84:87], v[92:95]\n" \
"v_exp_f32_e32 v72, v72\n" \
"v_mfma_f32_16x16x32_bf16 v[92:95], a[16:19], a[80:83], v[92:95]\n" \
"v_exp_f32_e32 v73, v73\n" \
"v_exp_f32_e32 v74, v74\n" \
"v_exp_f32_e32 v75, v75\n" \
"v_exp_f32_e32 v76, v76\n" \
"v_exp_f32_e32 v77, v77\n" \
"v_exp_f32_e32 v78, v78\n" \
"v_exp_f32_e32 v79, v79\n" \
"v_cvt_pk_bf16_f32 v108, v72, v73\n" \
"v_cvt_pk_bf16_f32 v109, v74, v75\n" \
"v_cvt_pk_bf16_f32 v110, v76, v77\n" \
"v_cvt_pk_bf16_f32 v111, v78, v79\n" \
"v_and_b32_e32 v108, v12, v108\n" \
"v_and_b32_e32 v109, v13, v109\n" \
"v_and_b32_e32 v110, v14, v110\n" \
"v_and_b32_e32 v111, v15, v111\n" \
"v_readlane_b32 s93, v11, 18\n" \
"v_readlane_b32 s84, v11, 13\n" \
"v_readlane_b32 s85, v11, 14\n" \
"v_readlane_b32 s86, v11, 15\n" \
"s_mov_b32 s87, 0x20000\n" \
"v_readlane_b32 s92, v11, 16\n" \
"s_add_u32 m0, s93, 0x0\n" \
"v_xor_b32_e32 v63, 0x400, v19\n" \
"s_waitcnt lgkmcnt(0)\n" \
"s_barrier\n" \
"ds_read_b128 v[192:195], v52 offset:0\n" \
"ds_read_b128 v[196:199], v52 offset:1024\n" \
"ds_read_b128 v[200:203], v52 offset:2048\n" \
"ds_read_b128 v[204:207], v52 offset:3072\n" \
"ds_read_b128 v[208:211], v52 offset:8192\n" \
"ds_read_b128 v[212:215], v52 offset:9216\n" \
"ds_read_b128 v[216:219], v52 offset:10240\n" \
"ds_read_b128 v[220:223], v52 offset:11264\n" \
"ds_read_b128 v[224:227], v53 offset:0\n" \
"ds_read_b128 v[228:231], v53 offset:1024\n" \
"ds_read_b128 v[232:235], v53 offset:2048\n" \
"ds_read_b128 v[236:239], v53 offset:3072\n" \
"ds_read_b128 v[240:243], v53 offset:8192\n" \
"ds_read_b128 v[244:247], v53 offset:9216\n" \
"ds_read_b128 v[248:251], v53 offset:10240\n" \
"ds_read_b128 v[252:255], v53 offset:11264\n" \
"v_mfma_f32_16x16x32_bf16 v[160:163], a[128:131], v[108:111], v[160:163]\n" \
"buffer_load_dwordx4 v0, s[84:87], s92 offen offset:0 lds\n" \
"v_mfma_f32_16x16x32_bf16 v[164:167], a[132:135], v[108:111], v[164:167]\n" \
"v_exp_f32_e32 v80, v80\n" \
"v_mfma_f32_16x16x32_bf16 v[168:171], a[136:139], v[108:111], v[168:171]\n" \
"v_exp_f32_e32 v81, v81\n" \
"v_mfma_f32_16x16x32_bf16 v[172:175], a[140:143], v[108:111], v[172:175]\n" \
"buffer_load_dwordx4 v1, s[84:87], s92 offen offset:1024 lds\n" \
"v_mfma_f32_16x16x32_bf16 v[176:179], a[144:147], v[108:111], v[176:179]\n" \
"ds_read_b64 v[42:43], v63\n" \
"v_cvt_pk_bf16_f32 v116, v80, v81\n" \
"v_mfma_f32_16x16x32_bf16 v[180:183], a[148:151], v[108:111], v[180:183]\n" \
"v_exp_f32_e32 v82, v82\n" \
"v_mfma_f32_16x16x32_bf16 v[184:187], a[152:155], v[108:111], v[184:187]\n" \
"buffer_load_dwordx4 v2, s[84:87], s92 offen offset:2048 lds\n" \
"v_mfma_f32_16x16x32_bf16 v[188:191], a[156:159], v[108:111], v[188:191]\n" \
"v_exp_f32_e32 v83, v83\n" \
"v_mfma_f32_16x16x32_bf16 a[196:199], a[252:255], v[108:111], a[196:199]\n" \
"v_exp_f32_e32 v84, v84\n" \
"v_exp_f32_e32 v85, v85\n" \
"v_exp_f32_e32 v86, v86\n" \
"s_waitcnt lgkmcnt(1)\n" \
"s_barrier\n" \
"buffer_load_dwordx4 v3, s[84:87], s92 offen offset:3072 lds\n" \
"s_add_u32 m0, s93, 0x1000\n" \
"v_exp_f32_e32 v87, v87\n" \
"v_cvt_pk_bf16_f32 v117, v82, v83\n" \
"v_cvt_pk_bf16_f32 v118, v84, v85\n" \
"v_cvt_pk_bf16_f32 v119, v86, v87\n" \
"v_exp_f32_e32 v88, v88\n" \
"v_exp_f32_e32 v89, v89\n" \
"v_mfma_f32_16x16x32_bf16 v[128:131], a[96:99], v[116:119], v[128:131]\n" \
"buffer_load_dwordx4 v4, s[84:87], s92 offen offset:0 lds\n" \
"v_mfma_f32_16x16x32_bf16 v[132:135], a[100:103], v[116:119], v[132:135]\n" \
"v_exp_f32_e32 v90, v90\n" \
"v_mfma_f32_16x16x32_bf16 v[136:139], a[104:107], v[116:119], v[136:139]\n" \
"v_exp_f32_e32 v91, v91\n" \
"v_mfma_f32_16x16x32_bf16 v[140:143], a[108:111], v[116:119], v[140:143]\n" \
"buffer_load_dwordx4 v5, s[84:87], s92 offen offset:1024 lds\n" \
"v_mfma_f32_16x16x32_bf16 v[144:147], a[112:115], v[116:119], v[144:147]\n" \
"v_exp_f32_e32 v92, v92\n" \
"v_mfma_f32_16x16x32_bf16 v[148:151], a[116:119], v[116:119], v[148:151]\n" \
"v_exp_f32_e32 v93, v93\n" \
"v_mfma_f32_16x16x32_bf16 v[152:155], a[120:123], v[116:119], v[152:155]\n" \
"buffer_load_dwordx4 v6, s[84:87], s92 offen offset:2048 lds\n" \
"v_mfma_f32_16x16x32_bf16 v[156:159], a[124:127], v[116:119], v[156:159]\n" \
"v_exp_f32_e32 v94, v94\n" \
"v_mfma_f32_16x16x32_bf16 a[192:195], a[252:255], v[116:119], a[192:195]\n" \
"v_exp_f32_e32 v95, v95\n" \
"v_cvt_pk_bf16_f32 v124, v88, v89\n" \
"v_cvt_pk_bf16_f32 v125, v90, v91\n" \
"v_cvt_pk_bf16_f32 v126, v92, v93\n" \
"v_cvt_pk_bf16_f32 v127, v94, v95\n" \
"v_add_f32_e32 v160, v160, v224\n" \
"v_add_f32_e32 v161, v161, v225\n" \
"v_add_f32_e32 v162, v162, v226\n" \
"v_add_f32_e32 v163, v163, v227\n" \
"v_add_f32_e32 v164, v164, v228\n" \
"v_add_f32_e32 v165, v165, v229\n" \
"v_mfma_f32_16x16x32_bf16 v[160:163], a[96:99], v[124:127], v[160:163]\n" \
"buffer_load_dwordx4 v7, s[84:87], s92 offen offset:3072 lds\n" \
"s_add_u32 m0, s93, 0x2000\n" \
"v_add_f32_e32 v166, v166, v230\n" \
"v_add_f32_e32 v167, v167, v231\n" \
"v_add_f32_e32 v168, v168, v232\n" \
"v_add_f32_e32 v169, v169, v233\n" \
"v_mfma_f32_16x16x32_bf16 v[164:167], a[100:103], v[124:127], v[164:167]\n" \
"v_add_f32_e32 v170, v170, v234\n" \
"v_add_f32_e32 v171, v171, v235\n" \
"v_add_f32_e32 v172, v172, v236\n" \
"v_add_f32_e32 v173, v173, v237\n" \
"v_mfma_f32_16x16x32_bf16 v[168:171], a[104:107], v[124:127], v[168:171]\n" \
"buffer_load_dwordx4 v8, s[84:87], s92 offen offset:0 lds\n" \
"v_add_f32_e32 v174, v174, v238\n" \
"v_add_f32_e32 v175, v175, v239\n" \
"v_accvgpr_read_b32 v54, a192\n" \
"v_accvgpr_read_b32 v23, a193\n" \
"v_mfma_f32_16x16x32_bf16 v[172:175], a[108:111], v[124:127], v[172:175]\n" \
"v_add_f32_e32 v54, v54, v23\n" \
"s_waitcnt lgkmcnt(0)\n" \
"v_add_f32_e32 v54, v54, v42\n" \
"v_max_f32_e32 v62, v62, v54\n" \
"v_add_f32_e32 v176, v176, v240\n" \
"v_add_f32_e32 v177, v177, v241\n" \
"v_add_f32_e32 v178, v178, v242\n" \
"v_add_f32_e32 v179, v179, v243\n" \
"v_rcp_f32_e32 v54, v54\n" \
"v_add_f32_e32 v128, v128, v192\n" \
"v_mfma_f32_16x16x32_bf16 v[176:179], a[112:115], v[124:127], v[176:179]\n" \
"v_add_f32_e32 v129, v129, v193\n" \
"v_add_f32_e32 v130, v130, v194\n" \
"v_add_f32_e32 v131, v131, v195\n" \
"v_add_f32_e32 v132, v132, v196\n" \
"v_add_f32_e32 v133, v133, v197\n" \
"v_add_f32_e32 v134, v134, v198\n" \
"v_add_f32_e32 v135, v135, v199\n" \
"v_mul_f32_e32 v54, v54, v58\n" \
"v_mul_f32_e32 v128, v54, v128\n" \
"v_mul_f32_e32 v129, v54, v129\n" \
"v_mul_f32_e32 v130, v54, v130\n" \
"v_mul_f32_e32 v131, v54, v131\n" \
"v_mul_f32_e32 v132, v54, v132\n" \
"v_mul_f32_e32 v133, v54, v133\n" \
"v_mul_f32_e32 v134, v54, v134\n" \
"v_mul_f32_e32 v135, v54, v135\n" \
"v_add_f32_e32 v180, v180, v244\n" \
"v_add_f32_e32 v181, v181, v245\n" \
"v_add_f32_e32 v182, v182, v246\n" \
"v_add_f32_e32 v183, v183, v247\n" \
"v_add_f32_e32 v136, v136, v200\n" \
"v_add_f32_e32 v137, v137, v201\n" \
"v_add_f32_e32 v138, v138, v202\n" \
"v_add_f32_e32 v139, v139, v203\n" \
"v_add_f32_e32 v140, v140, v204\n" \
"v_add_f32_e32 v141, v141, v205\n" \
"v_add_f32_e32 v142, v142, v206\n" \
"v_add_f32_e32 v143, v143, v207\n" \
"v_cvt_pk_bf16_f32 v64, v128, v129\n" \
"v_cvt_pk_bf16_f32 v65, v130, v131\n" \
"v_cvt_pk_bf16_f32 v66, v132, v133\n" \
"v_cvt_pk_bf16_f32 v67, v134, v135\n" \
"buffer_store_dwordx4 v[64:67], v20, s[56:59], s68 offen offset:0 sc1\n" \
"v_mfma_f32_16x16x32_bf16 v[180:183], a[116:119], v[124:127], v[180:183]\n" \
"v_mfma_f32_16x16x32_bf16 a[208:211], a[216:219], v[64:67], a[208:211]\n" \
"v_mul_f32_e32 v136, v54, v136\n" \
"v_mul_f32_e32 v137, v54, v137\n" \
"v_mul_f32_e32 v138, v54, v138\n" \
"v_mul_f32_e32 v139, v54, v139\n" \
"v_mul_f32_e32 v140, v54, v140\n" \
"v_mul_f32_e32 v141, v54, v141\n" \
"v_mul_f32_e32 v142, v54, v142\n" \
"v_mul_f32_e32 v143, v54, v143\n" \
"v_add_f32_e32 v144, v144, v208\n" \
"v_add_f32_e32 v145, v145, v209\n" \
"v_add_f32_e32 v146, v146, v210\n" \
"v_add_f32_e32 v147, v147, v211\n" \
"v_add_f32_e32 v148, v148, v212\n" \
"v_add_f32_e32 v149, v149, v213\n" \
"v_add_f32_e32 v150, v150, v214\n" \
"v_add_f32_e32 v151, v151, v215\n" \
"v_cvt_pk_bf16_f32 v68, v136, v137\n" \
"v_cvt_pk_bf16_f32 v69, v138, v139\n" \
"v_cvt_pk_bf16_f32 v70, v140, v141\n" \
"v_cvt_pk_bf16_f32 v71, v142, v143\n" \
"buffer_store_dwordx4 v[68:71], v20, s[56:59], s68 offen offset:1024 sc1\n" \
"v_mul_f32_e32 v144, v54, v144\n" \
"v_mfma_f32_16x16x32_bf16 a[208:211], a[216:219], v[68:71], a[208:211]\n" \
"v_mul_f32_e32 v145, v54, v145\n" \
"v_mul_f32_e32 v146, v54, v146\n" \
"v_mul_f32_e32 v147, v54, v147\n" \
"v_mul_f32_e32 v148, v54, v148\n" \
"v_mul_f32_e32 v149, v54, v149\n" \
"v_mul_f32_e32 v150, v54, v150\n" \
"v_mul_f32_e32 v151, v54, v151\n" \
"v_add_f32_e32 v184, v184, v248\n" \
"v_add_f32_e32 v185, v185, v249\n" \
"v_add_f32_e32 v186, v186, v250\n" \
"v_add_f32_e32 v187, v187, v251\n" \
"v_add_f32_e32 v152, v152, v216\n" \
"v_add_f32_e32 v153, v153, v217\n" \
"v_add_f32_e32 v154, v154, v218\n" \
"v_add_f32_e32 v155, v155, v219\n" \
"v_add_f32_e32 v156, v156, v220\n" \
"v_add_f32_e32 v157, v157, v221\n" \
"v_add_f32_e32 v158, v158, v222\n" \
"v_add_f32_e32 v159, v159, v223\n" \
"v_cvt_pk_bf16_f32 v72, v144, v145\n" \
"v_cvt_pk_bf16_f32 v73, v146, v147\n" \
"v_cvt_pk_bf16_f32 v74, v148, v149\n" \
"v_cvt_pk_bf16_f32 v75, v150, v151\n" \
"buffer_store_dwordx4 v[72:75], v20, s[56:59], s68 offen offset:2048 sc1\n" \
"v_mfma_f32_16x16x32_bf16 v[184:187], a[120:123], v[124:127], v[184:187]\n" \
"v_mfma_f32_16x16x32_bf16 a[208:211], a[216:219], v[72:75], a[208:211]\n" \
"v_mul_f32_e32 v152, v54, v152\n" \
"v_mul_f32_e32 v153, v54, v153\n" \
"v_mul_f32_e32 v154, v54, v154\n" \
"v_mul_f32_e32 v155, v54, v155\n" \
"v_mul_f32_e32 v156, v54, v156\n" \
"v_mul_f32_e32 v157, v54, v157\n" \
"v_mul_f32_e32 v158, v54, v158\n" \
"v_mul_f32_e32 v159, v54, v159\n" \
"v_cvt_pk_bf16_f32 v76, v152, v153\n" \
"v_cvt_pk_bf16_f32 v77, v154, v155\n" \
"v_cvt_pk_bf16_f32 v78, v156, v157\n" \
"v_cvt_pk_bf16_f32 v79, v158, v159\n" \
"buffer_store_dwordx4 v[76:79], v20, s[56:59], s68 offen offset:3072 sc1\n" \
"v_add_f32_e32 v188, v188, v252\n" \
"v_mfma_f32_16x16x32_bf16 a[208:211], a[216:219], v[76:79], a[208:211]\n" \
"v_add_f32_e32 v189, v189, v253\n" \
"v_add_f32_e32 v190, v190, v254\n" \
"v_add_f32_e32 v191, v191, v255\n" \
"s_nop 1\n" \
"v_mfma_f32_16x16x32_bf16 v[188:191], a[124:127], v[124:127], v[188:191]\n" \
"v_mfma_f32_16x16x32_bf16 a[196:199], a[252:255], v[124:127], a[196:199]\n" \
"s_nop 7\n" \
"v_accvgpr_read_b32 v55, a196\n" \
"v_accvgpr_read_b32 v23, a197\n" \
"v_add_f32_e32 v55, v55, v23\n" \
"v_add_f32_e32 v55, v55, v43\n" \
"v_max_f32_e32 v62, v62, v55\n" \
"v_rcp_f32_e32 v55, v55\n" \
"v_cmp_lt_f32_e64 s[72:73], s100, v62\n" \
"v_mul_f32_e32 v55, v55, v59\n" \
"v_mul_f32_e32 v160, v55, v160\n" \
"v_mul_f32_e32 v161, v55, v161\n" \
"v_mul_f32_e32 v162, v55, v162\n" \
"v_mul_f32_e32 v163, v55, v163\n" \
"v_mul_f32_e32 v164, v55, v164\n" \
"v_mul_f32_e32 v165, v55, v165\n" \
"v_mul_f32_e32 v166, v55, v166\n" \
"v_mul_f32_e32 v167, v55, v167\n" \
"v_cvt_pk_bf16_f32 v80, v160, v161\n" \
"v_cvt_pk_bf16_f32 v81, v162, v163\n" \
"v_cvt_pk_bf16_f32 v82, v164, v165\n" \
"v_cvt_pk_bf16_f32 v83, v166, v167\n" \
"buffer_store_dwordx4 v[80:83], v20, s[56:59], s69 offen offset:0 sc1\n" \
"v_mul_f32_e32 v168, v55, v168\n" \
"v_mfma_f32_16x16x32_bf16 a[208:211], a[216:219], v[80:83], a[208:211]\n" \
"v_mul_f32_e32 v169, v55, v169\n" \
"v_mul_f32_e32 v170, v55, v170\n" \
"v_mul_f32_e32 v171, v55, v171\n" \
"v_mul_f32_e32 v172, v55, v172\n" \
"v_mul_f32_e32 v173, v55, v173\n" \
"v_mul_f32_e32 v174, v55, v174\n" \
"v_mul_f32_e32 v175, v55, v175\n" \
"v_cvt_pk_bf16_f32 v84, v168, v169\n" \
"v_cvt_pk_bf16_f32 v85, v170, v171\n" \
"v_cvt_pk_bf16_f32 v86, v172, v173\n" \
"v_cvt_pk_bf16_f32 v87, v174, v175\n" \
"buffer_store_dwordx4 v[84:87], v20, s[56:59], s69 offen offset:1024 sc1\n" \
"v_mul_f32_e32 v176, v55, v176\n" \
"v_mfma_f32_16x16x32_bf16 a[208:211], a[216:219], v[84:87], a[208:211]\n" \
"v_mul_f32_e32 v177, v55, v177\n" \
"v_mul_f32_e32 v178, v55, v178\n" \
"v_mul_f32_e32 v179, v55, v179\n" \
"v_mul_f32_e32 v180, v55, v180\n" \
"v_mul_f32_e32 v181, v55, v181\n" \
"v_mul_f32_e32 v182, v55, v182\n" \
"v_mul_f32_e32 v183, v55, v183\n" \
"v_cvt_pk_bf16_f32 v96, v176, v177\n" \
"v_cvt_pk_bf16_f32 v97, v178, v179\n" \
"v_cvt_pk_bf16_f32 v98, v180, v181\n" \
"v_cvt_pk_bf16_f32 v99, v182, v183\n" \
"buffer_store_dwordx4 v[96:99], v20, s[56:59], s69 offen offset:2048 sc1\n" \
"v_mul_f32_e32 v184, v55, v184\n" \
"v_mfma_f32_16x16x32_bf16 a[208:211], a[216:219], v[96:99], a[208:211]\n" \
"v_mul_f32_e32 v185, v55, v185\n" \
"v_mul_f32_e32 v186, v55, v186\n" \
"v_mul_f32_e32 v187, v55, v187\n" \
"v_mul_f32_e32 v188, v55, v188\n" \
"v_mul_f32_e32 v189, v55, v189\n" \
"v_mul_f32_e32 v190, v55, v190\n" \
"v_mul_f32_e32 v191, v55, v191\n" \
"v_cvt_pk_bf16_f32 v100, v184, v185\n" \
"v_cvt_pk_bf16_f32 v101, v186, v187\n" \
"v_cvt_pk_bf16_f32 v102, v188, v189\n" \
"v_cvt_pk_bf16_f32 v103, v190, v191\n" \
"buffer_store_dwordx4 v[100:103], v20, s[56:59], s69 offen offset:3072 sc1\n" \
"s_nop 0\n" \
"v_mfma_f32_16x16x32_bf16 a[208:211], a[216:219], v[100:103], a[208:211]\n" \
"s_nop 7\n" \
"v_accvgpr_read_b32 v63, a208\n" \
"v_cmp_u_f32_e32 vcc, v63, v63\n" \
"s_nop 0\n" \
"s_or_b64 vcc, s[72:73], vcc\n" \
"s_cbranch_scc1 L_end_%=\n" \
"L_out_%=:\n" \
"L_pf19:\n" \
"s_cbranch_execz L_retA\n" \
"s_nop 0\n" \
"v_add_u32_e32 v34, 0x20000, v10\n" \
"v_mov_b32_e32 v39, s62\n" \
"v_readlane_b32 s70, v11, 4\n" \
"v_bfe_u32 v38, v20, 2, 4\n" \
"v_readlane_b32 s64, v11, 1\n" \
"v_readlane_b32 s71, v11, 3\n" \
"v_add_u32_e32 v33, 0xfc00, v10\n" \
"v_readlane_b32 s99, v11, 19\n" \
"v_readlane_b32 s80, v11, 9\n" \
"v_readlane_b32 s81, v11, 10\n" \
"s_mov_b32 s82, 0x47fca0\n" \
"s_mov_b32 s83, 0x20000\n" \
"ds_read_b64_tr_b16 a[0:1], v34 offset:0\n" \
"ds_read_b64_tr_b16 a[2:3], v34 offset:4608\n" \
"ds_read_b64_tr_b16 a[4:5], v34 offset:9216\n" \
"ds_read_b64_tr_b16 a[6:7], v34 offset:13824\n" \
"ds_read_b64_tr_b16 a[8:9], v34 offset:18432\n" \
"ds_read_b64_tr_b16 a[10:11], v34 offset:23040\n" \
"ds_read_b64_tr_b16 a[12:13], v34 offset:32\n" \
"ds_read_b64_tr_b16 a[14:15], v34 offset:4640\n" \
"ds_read_b64_tr_b16 a[16:17], v34 offset:9248\n" \
"ds_read_b64_tr_b16 a[18:19], v34 offset:13856\n" \
"buffer_load_dword v37, v38, s[56:59], s64 offen sc1\n" \
"ds_read_b64_tr_b16 a[20:21], v34 offset:18464\n" \
"ds_read_b64_tr_b16 a[22:23], v34 offset:23072\n" \
"ds_read_b64_tr_b16 a[24:25], v34 offset:64\n" \
"ds_read_b64_tr_b16 a[26:27], v34 offset:4672\n" \
"ds_read_b64_tr_b16 a[28:29], v34 offset:9280\n" \
"ds_read_b64_tr_b16 a[30:31], v34 offset:13888\n" \
"ds_read_b64_tr_b16 a[192:193], v34 offset:18496\n" \
"ds_read_b64_tr_b16 a[194:195], v34 offset:23104\n" \
"ds_read_b64_tr_b16 a[196:197], v34 offset:96\n" \
"ds_read_b64_tr_b16 a[198:199], v34 offset:4704\n" \
"ds_read_b64_tr_b16 a[200:201], v34 offset:9312\n" \
"ds_read_b64_tr_b16 a[202:203], v34 offset:13920\n" \
"ds_read_b64_tr_b16 a[204:205], v34 offset:18528\n" \
"ds_read_b64_tr_b16 a[206:207], v34 offset:23136\n" \
"ds_read_b64_tr_b16 a[208:209], v34 offset:128\n" \
"ds_read_b64_tr_b16 a[210:211], v34 offset:4736\n" \
"ds_read_b64_tr_b16 a[212:213], v34 offset:9344\n" \
"ds_read_b64_tr_b16 a[214:215], v34 offset:13952\n" \
"ds_read_b64_tr_b16 a[216:217], v34 offset:18560\n" \
"ds_read_b64_tr_b16 a[218:219], v34 offset:23168\n" \
"v_readlane_b32 s65, v11, 23\n" \
"s_add_u32 m0, s99, 0x6c00\n" \
"v_readlane_b32 s66, v11, 26\n" \
"v_add_u32_e32 v32, 0x49000, v20\n" \
"v_lshrrev_b32_e32 v44, 9, v20\n" \
"v_mul_u32_u24_e32 v44, 0xb40, v44\n" \
"v_add_u32_e32 v35, v44, v10\n" \
"v_readlane_b32 s67, v11, 24\n" \
"v_readlane_b32 s92, v11, 20\n" \
"v_readlane_b32 s73, v11, 25\n" \
"s_waitcnt vmcnt(18) lgkmcnt(15)\n" \
"v_mfma_f32_16x16x32_bf16 v[112:115], a[0:3], a[220:223], 0\n" \
"buffer_load_dwordx4 v0, s[80:83], s65 offen offset:0 lds\n" \
"v_mfma_f32_16x16x32_bf16 v[112:115], a[4:7], a[224:227], v[112:115]\n" \
"v_readlane_b32 s94, v11, 21\n" \
"v_readlane_b32 s84, v11, 7\n" \
"v_mfma_f32_16x16x32_bf16 v[112:115], a[8:11], a[228:231], v[112:115]\n" \
"v_readlane_b32 s85, v11, 8\n" \
"s_mov_b32 s86, 0xa1ca0\n" \
"v_mfma_f32_16x16x32_bf16 v[116:119], a[8:11], a[244:247], 0\n" \
"s_mov_b32 s87, 0x20000\n" \
"v_readlane_b32 s68, v11, 27\n" \
"v_mfma_f32_16x16x32_bf16 v[116:119], a[4:7], a[240:243], v[116:119]\n" \
"v_readlane_b32 s69, v11, 28\n" \
"v_readlane_b32 s72, v11, 30\n" \
"v_mfma_f32_16x16x32_bf16 v[116:119], a[0:3], a[236:239], v[116:119]\n" \
"buffer_load_dwordx4 v1, s[80:83], s65 offen offset:1024 lds\n" \
"v_mfma_f32_16x16x32_bf16 v[184:187], a[0:3], v[64:67], 0\n" \
"v_readlane_b32 s93, v11, 31\n" \
"v_lshlrev_b32_e32 v41, 12, v20\n" \
"v_mfma_f32_16x16x32_bf16 v[184:187], a[4:7], v[68:71], v[184:187]\n" \
"v_readlane_b32 s63, v11, 37\n" \
"v_bfe_u32 v40, v20, 2, 7\n" \
"v_mfma_f32_16x16x32_bf16 v[184:187], a[8:11], v[72:75], v[184:187]\n" \
"s_add_u32 s74, s63, 0x3800\n" \
"v_bfe_u32 v44, v20, 4, 4\n" \
"v_mfma_f32_16x16x32_bf16 v[188:191], a[8:11], v[96:99], 0\n" \
"ds_read_b64_tr_b16 a[8:9], v34 offset:18592\n" \
"ds_read_b64_tr_b16 a[10:11], v34 offset:23200\n" \
"v_mfma_f32_16x16x32_bf16 v[188:191], a[4:7], v[84:87], v[188:191]\n" \
"ds_read_b64_tr_b16 a[4:5], v34 offset:9376\n" \
"ds_read_b64_tr_b16 a[6:7], v34 offset:13984\n" \
"v_mfma_f32_16x16x32_bf16 v[188:191], a[0:3], v[80:83], v[188:191]\n" \
"ds_read_b64_tr_b16 a[0:1], v34 offset:160\n" \
"ds_read_b64_tr_b16 a[2:3], v34 offset:4768\n" \
"v_mfma_f32_16x16x32_bf16 v[120:123], a[12:15], a[220:223], 0\n" \
"buffer_load_dwordx4 v2, s[80:83], s65 offen offset:2048 lds\n" \
"v_mfma_f32_16x16x32_bf16 v[120:123], a[16:19], a[224:227], v[120:123]\n" \
"v_bfe_u32 v45, v20, 8, 2\n" \
"v_mul_u32_u24_e32 v42, 0x480, v44\n" \
"v_mfma_f32_16x16x32_bf16 v[120:123], a[20:23], a[228:231], v[120:123]\n" \
"v_lshl_add_u32 v42, v45, 3, v42\n" \
"v_and_b32_e32 v43, 1, v45\n" \
"v_mfma_f32_16x16x32_bf16 v[124:127], a[20:23], a[244:247], 0\n" \
"v_lshrrev_b32_e32 v44, 1, v44\n" \
"v_lshlrev_b32_e32 v45, 1, v45\n" \
"v_mfma_f32_16x16x32_bf16 v[124:127], a[16:19], a[240:243], v[124:127]\n" \
"v_sub_u32_e32 v44, v44, v45\n" \
"v_and_b32_e32 v45, 16, v20\n" \
"v_mfma_f32_16x16x32_bf16 v[124:127], a[12:15], a[236:239], v[124:127]\n" \
"buffer_load_dwordx4 v3, s[80:83], s65 offen offset:3072 lds\n" \
"v_mfma_f32_16x16x32_bf16 v[192:195], a[12:15], v[64:67], 0\n" \
"s_add_u32 m0, s99, 0x7c00\n" \
"v_mov_b32_e32 v46, 0x3f80\n" \
"v_mfma_f32_16x16x32_bf16 v[192:195], a[16:19], v[68:71], v[192:195]\n" \
"v_lshlrev_b32_e32 v45, v45, v46\n" \
"v_mov_b32_e32 v46, s62\n" \
"v_mfma_f32_16x16x32_bf16 v[192:195], a[20:23], v[72:75], v[192:195]\n" \
"v_readlane_b32 s95, v11, 34\n" \
"v_readlane_b32 s96, v11, 35\n" \
"v_mfma_f32_16x16x32_bf16 v[196:199], a[20:23], v[96:99], 0\n" \
"ds_read_b64_tr_b16 a[20:21], v34 offset:18624\n" \
"ds_read_b64_tr_b16 a[22:23], v34 offset:23232\n" \
"v_mfma_f32_16x16x32_bf16 v[196:199], a[16:19], v[84:87], v[196:199]\n" \
"ds_read_b64_tr_b16 a[16:17], v34 offset:9408\n" \
"ds_read_b64_tr_b16 a[18:19], v34 offset:14016\n" \
"v_mfma_f32_16x16x32_bf16 v[196:199], a[12:15], v[80:83], v[196:199]\n" \
"ds_read_b64_tr_b16 a[12:13], v34 offset:192\n" \
"ds_read_b64_tr_b16 a[14:15], v34 offset:4800\n" \
"v_mfma_f32_16x16x32_bf16 v[128:131], a[24:27], a[220:223], 0\n" \
"v_readlane_b32 s97, v11, 36\n" \
"s_waitcnt lgkmcnt(8)\n" \
"v_mfma_f32_16x16x32_bf16 v[128:131], a[28:31], a[224:227], v[128:131]\n" \
"buffer_load_dwordx4 v4, s[80:83], s65 offen offset:0 lds\n" \
"v_mfma_f32_16x16x32_bf16 v[128:131], a[192:195], a[228:231], v[128:131]\n" \
"v_readlane_b32 s100, v11, 6\n" \
"v_mov_b32_e32 v52, 0\n" \
"v_mfma_f32_16x16x32_bf16 v[132:135], a[192:195], a[244:247], 0\n" \
"v_mov_b32_e32 v53, 0\n" \
"v_mov_b32_e32 v48, 0\n" \
"v_mfma_f32_16x16x32_bf16 v[132:135], a[28:31], a[240:243], v[132:135]\n" \
"v_mov_b32_e32 v49, 0\n" \
"v_mul_u32_u24_e32 v43, 0x8ff8, v43\n" \
"v_mfma_f32_16x16x32_bf16 v[132:135], a[24:27], a[236:239], v[132:135]\n" \
"v_add_u32_e32 v42, v42, v43\n" \
"v_readlane_b32 s88, v11, 11\n" \
"v_mfma_f32_16x16x32_bf16 v[200:203], a[24:27], v[64:67], 0\n" \
"buffer_load_dwordx4 v5, s[80:83], s65 offen offset:1024 lds\n" \
"v_mfma_f32_16x16x32_bf16 v[200:203], a[28:31], v[68:71], v[200:203]\n" \
"v_readlane_b32 s89, v11, 12\n" \
"s_mov_b32 s90, 0x240000\n" \
"v_mfma_f32_16x16x32_bf16 v[200:203], a[192:195], v[72:75], v[200:203]\n" \
"s_mov_b32 s91, 0x20000\n" \
"v_mfma_f32_16x16x32_bf16 v[204:207], a[192:195], v[96:99], 0\n" \
"ds_read_b64_tr_b16 a[192:193], v34 offset:18656\n" \
"ds_read_b64_tr_b16 a[194:195], v34 offset:23264\n" \
"v_mfma_f32_16x16x32_bf16 v[204:207], a[28:31], v[84:87], v[204:207]\n" \
"ds_read_b64_tr_b16 a[28:29], v34 offset:9440\n" \
"ds_read_b64_tr_b16 a[30:31], v34 offset:14048\n" \
"v_mfma_f32_16x16x32_bf16 v[204:207], a[24:27], v[80:83], v[204:207]\n" \
"ds_read_b64_tr_b16 a[24:25], v34 offset:224\n" \
"ds_read_b64_tr_b16 a[26:27], v34 offset:4832\n" \
"v_mfma_f32_16x16x32_bf16 v[136:139], a[196:199], a[220:223], 0\n" \
"buffer_load_dwordx4 v6, s[80:83], s65 offen offset:2048 lds\n" \
"v_mfma_f32_16x16x32_bf16 v[136:139], a[200:203], a[224:227], v[136:139]\n" \
"v_mfma_f32_16x16x32_bf16 v[136:139], a[204:207], a[228:231], v[136:139]\n" \
"v_mfma_f32_16x16x32_bf16 v[140:143], a[204:207], a[244:247], 0\n" \
"v_mfma_f32_16x16x32_bf16 v[140:143], a[200:203], a[240:243], v[140:143]\n" \
"v_mfma_f32_16x16x32_bf16 v[140:143], a[196:199], a[236:239], v[140:143]\n" \
"buffer_load_dwordx4 v7, s[80:83], s65 offen offset:3072 lds\n" \
"v_mfma_f32_16x16x32_bf16 v[208:211], a[196:199], v[64:67], 0\n" \
"s_add_u32 m0, s99, 0x8c00\n" \
"v_mfma_f32_16x16x32_bf16 v[208:211], a[200:203], v[68:71], v[208:211]\n" \
"v_mfma_f32_16x16x32_bf16 v[208:211], a[204:207], v[72:75], v[208:211]\n" \
"v_mfma_f32_16x16x32_bf16 v[212:215], a[204:207], v[96:99], 0\n" \
"ds_read_b64_tr_b16 a[204:205], v34 offset:18688\n" \
"ds_read_b64_tr_b16 a[206:207], v34 offset:23296\n" \
"v_mfma_f32_16x16x32_bf16 v[212:215], a[200:203], v[84:87], v[212:215]\n" \
"ds_read_b64_tr_b16 a[200:201], v34 offset:9472\n" \
"ds_read_b64_tr_b16 a[202:203], v34 offset:14080\n" \
"v_mfma_f32_16x16x32_bf16 v[212:215], a[196:199], v[80:83], v[212:215]\n" \
"ds_read_b64_tr_b16 a[196:197], v34 offset:256\n" \
"ds_read_b64_tr_b16 a[198:199], v34 offset:4864\n" \
"v_mfma_f32_16x16x32_bf16 v[144:147], a[208:211], a[220:223], 0\n" \
"buffer_load_dwordx4 v8, s[80:83], s65 offen offset:0 lds\n" \
"v_mfma_f32_16x16x32_bf16 v[144:147], a[212:215], a[224:227], v[144:147]\n" \
"s_waitcnt vmcnt(9)\n" \
"v_cmp_ne_u32_e32 vcc, s62, v37\n" \
"v_mfma_f32_16x16x32_bf16 v[144:147], a[216:219], a[228:231], v[144:147]\n" \
"s_add_u32 m0, s99, 0x14400\n" \
"v_readlane_b32 s65, v11, 42\n" \
"v_mfma_f32_16x16x32_bf16 v[148:151], a[216:219], a[244:247], 0\n" \
"v_mfma_f32_16x16x32_bf16 v[148:151], a[212:215], a[240:243], v[148:151]\n" \
"s_cbranch_vccnz L_sa_%=\n" \
"L_ra_%=:\n" \
"v_readlane_b32 s64, v11, 29\n" \
"v_mfma_f32_16x16x32_bf16 v[148:151], a[208:211], a[236:239], v[148:151]\n" \
"buffer_load_dwordx4 a[64:67], v20, s[56:59], s66 offen offset:0 sc1\n" \
"v_mfma_f32_16x16x32_bf16 v[216:219], a[208:211], v[64:67], 0\n" \
"v_mfma_f32_16x16x32_bf16 v[216:219], a[212:215], v[68:71], v[216:219]\n" \
"v_mfma_f32_16x16x32_bf16 v[216:219], a[216:219], v[72:75], v[216:219]\n" \
"v_mfma_f32_16x16x32_bf16 v[220:223], a[216:219], v[96:99], 0\n" \
"v_mfma_f32_16x16x32_bf16 v[220:223], a[212:215], v[84:87], v[220:223]\n" \
"buffer_load_dwordx4 a[68:71], v20, s[56:59], s66 offen offset:1024 sc1\n" \
"v_mfma_f32_16x16x32_bf16 v[220:223], a[208:211], v[80:83], v[220:223]\n" \
"s_waitcnt lgkmcnt(5)\n" \
"v_mfma_f32_16x16x32_bf16 v[152:155], a[0:3], a[220:223], 0\n" \
"v_mfma_f32_16x16x32_bf16 v[152:155], a[4:7], a[224:227], v[152:155]\n" \
"v_mfma_f32_16x16x32_bf16 v[152:155], a[8:11], a[228:231], v[152:155]\n" \
"v_mfma_f32_16x16x32_bf16 v[156:159], a[8:11], a[244:247], 0\n" \
"buffer_load_dwordx4 a[84:87], v32, s[56:59], s66 offen offset:1024 sc1\n" \
"v_mfma_f32_16x16x32_bf16 v[156:159], a[4:7], a[240:243], v[156:159]\n" \
"v_mfma_f32_16x16x32_bf16 v[156:159], a[0:3], a[236:239], v[156:159]\n" \
"v_mfma_f32_16x16x32_bf16 v[224:227], a[0:3], v[64:67], 0\n" \
"v_mfma_f32_16x16x32_bf16 v[224:227], a[4:7], v[68:71], v[224:227]\n" \
"v_mfma_f32_16x16x32_bf16 v[224:227], a[8:11], v[72:75], v[224:227]\n" \
"buffer_load_dwordx4 a[80:83], v32, s[56:59], s66 offen offset:0 sc1\n" \
"v_mfma_f32_16x16x32_bf16 v[228:231], a[8:11], v[96:99], 0\n" \
"v_mfma_f32_16x16x32_bf16 v[228:231], a[4:7], v[84:87], v[228:231]\n" \
"v_mfma_f32_16x16x32_bf16 v[228:231], a[0:3], v[80:83], v[228:231]\n" \
"v_mfma_f32_16x16x32_bf16 v[160:163], a[12:15], a[220:223], 0\n" \
"v_mfma_f32_16x16x32_bf16 v[160:163], a[16:19], a[224:227], v[160:163]\n" \
"buffer_load_dwordx4 a[72:75], v20, s[56:59], s66 offen offset:2048 sc1\n" \
"v_mfma_f32_16x16x32_bf16 v[160:163], a[20:23], a[228:231], v[160:163]\n" \
"v_mfma_f32_16x16x32_bf16 v[164:167], a[20:23], a[244:247], 0\n" \
"v_mfma_f32_16x16x32_bf16 v[164:167], a[16:19], a[240:243], v[164:167]\n" \
"v_mfma_f32_16x16x32_bf16 v[164:167], a[12:15], a[236:239], v[164:167]\n" \
"v_mfma_f32_16x16x32_bf16 v[232:235], a[12:15], v[64:67], 0\n" \
"buffer_load_dwordx4 a[76:79], v20, s[56:59], s66 offen offset:3072 sc1\n" \
"v_mfma_f32_16x16x32_bf16 v[232:235], a[16:19], v[68:71], v[232:235]\n" \
"v_mfma_f32_16x16x32_bf16 v[232:235], a[20:23], v[72:75], v[232:235]\n" \
"v_mfma_f32_16x16x32_bf16 v[236:239], a[20:23], v[96:99], 0\n" \
"v_mfma_f32_16x16x32_bf16 v[236:239], a[16:19], v[84:87], v[236:239]\n" \
"v_mfma_f32_16x16x32_bf16 v[236:239], a[12:15], v[80:83], v[236:239]\n" \
"buffer_load_dwordx4 a[92:95], v32, s[56:59], s66 offen offset:3072 sc1\n" \
"s_waitcnt lgkmcnt(0)\n" \
"s_barrier\n" \
"v_mfma_f32_16x16x32_bf16 v[168:171], a[24:27], a[220:223], 0\n" \
"ds_read_b64_tr_b16 a[208:209], v33 offset:9216\n" \
"ds_read_b64_tr_b16 a[210:211], v33 offset:13824\n" \
"v_mfma_f32_16x16x32_bf16 v[168:171], a[28:31], a[224:227], v[168:171]\n" \
"ds_read_b64_tr_b16 a[212:213], v33 offset:9248\n" \
"ds_read_b64_tr_b16 a[214:215], v33 offset:13856\n" \
"v_mfma_f32_16x16x32_bf16 v[168:171], a[192:195], a[228:231], v[168:171]\n" \
"ds_read_b64_tr_b16 a[216:217], v33 offset:9280\n" \
"ds_read_b64_tr_b16 a[218:219], v33 offset:13888\n" \
"s_mov_b64 exec, 1\n" \
"buffer_store_dword v39, off, s[56:59], s70 offset:0 sc1\n" \
"s_mov_b64 exec, -1\n" \
"v_mfma_f32_16x16x32_bf16 v[172:175], a[192:195], a[244:247], 0\n" \
"ds_read_b64_tr_b16 a[0:1], v33 offset:9312\n" \
"ds_read_b64_tr_b16 a[2:3], v33 offset:13920\n" \
"v_mfma_f32_16x16x32_bf16 v[172:175], a[28:31], a[240:243], v[172:175]\n" \
"ds_read_b64_tr_b16 a[4:5], v33 offset:9344\n" \
"ds_read_b64_tr_b16 a[6:7], v33 offset:13952\n" \
"v_mfma_f32_16x16x32_bf16 v[172:175], a[24:27], a[236:239], v[172:175]\n" \
"ds_read_b64_tr_b16 a[8:9], v33 offset:9376\n" \
"ds_read_b64_tr_b16 a[10:11], v33 offset:13984\n" \
"v_mfma_f32_16x16x32_bf16 v[240:243], a[24:27], v[64:67], 0\n" \
"ds_read_b64_tr_b16 a[12:13], v33 offset:9408\n" \
"ds_read_b64_tr_b16 a[14:15], v33 offset:14016\n" \
"v_mfma_f32_16x16x32_bf16 v[240:243], a[28:31], v[68:71], v[240:243]\n" \
"ds_read_b64_tr_b16 a[16:17], v33 offset:9440\n" \
"ds_read_b64_tr_b16 a[18:19], v33 offset:14048\n" \
"v_mfma_f32_16x16x32_bf16 v[240:243], a[192:195], v[72:75], v[240:243]\n" \
"ds_read_b64_tr_b16 a[20:21], v33 offset:9472\n" \
"ds_read_b64_tr_b16 a[22:23], v33 offset:14080\n" \
"v_mfma_f32_16x16x32_bf16 v[244:247], a[192:195], v[96:99], 0\n" \
"ds_read_b64_tr_b16 a[192:193], v35 offset:32\n" \
"ds_read_b64_tr_b16 a[194:195], v35 offset:2336\n" \
"v_mfma_f32_16x16x32_bf16 v[244:247], a[28:31], v[84:87], v[244:247]\n" \
"ds_read_b64_tr_b16 a[28:29], v35 offset:10368\n" \
"ds_read_b64_tr_b16 a[30:31], v35 offset:12672\n" \
"v_mfma_f32_16x16x32_bf16 v[244:247], a[24:27], v[80:83], v[244:247]\n" \
"ds_read_b64_tr_b16 a[24:25], v35 offset:0\n" \
"ds_read_b64_tr_b16 a[26:27], v35 offset:2304\n" \
"v_mfma_f32_16x16x32_bf16 v[176:179], a[196:199], a[220:223], 0\n" \
"buffer_load_dwordx4 a[88:91], v32, s[56:59], s66 offen offset:2048 sc1\n" \
"v_mfma_f32_16x16x32_bf16 v[176:179], a[200:203], a[224:227], v[176:179]\n" \
"v_readlane_b32 s66, v11, 43\n" \
"v_readlane_b32 s70, v11, 45\n" \
"v_mfma_f32_16x16x32_bf16 v[176:179], a[204:207], a[228:231], v[176:179]\n" \
"v_mfma_f32_16x16x32_bf16 v[180:183], a[204:207], a[244:247], 0\n" \
"v_mfma_f32_16x16x32_bf16 v[180:183], a[200:203], a[240:243], v[180:183]\n" \
"v_mfma_f32_16x16x32_bf16 v[180:183], a[196:199], a[236:239], v[180:183]\n" \
"buffer_load_dwordx4 v0, s[80:83], s67 offen offset:0 lds\n" \
"v_mfma_f32_16x16x32_bf16 v[248:251], a[196:199], v[64:67], 0\n" \
"L_pf20:\n" \
"s_cbranch_execz L_pf22\n" \
"s_nop 0\n" \
"v_mfma_f32_16x16x32_bf16 v[248:251], a[200:203], v[68:71], v[248:251]\n" \
"v_mfma_f32_16x16x32_bf16 v[248:251], a[204:207], v[72:75], v[248:251]\n" \
"v_mfma_f32_16x16x32_bf16 v[252:255], a[204:207], v[96:99], 0\n" \
"ds_read_b64_tr_b16 a[204:205], v35 offset:10432\n" \
"ds_read_b64_tr_b16 a[206:207], v35 offset:12736\n" \
"v_mfma_f32_16x16x32_bf16 v[252:255], a[200:203], v[84:87], v[252:255]\n" \
"ds_read_b64_tr_b16 a[200:201], v35 offset:64\n" \
"ds_read_b64_tr_b16 a[202:203], v35 offset:2368\n" \
"v_mfma_f32_16x16x32_bf16 v[252:255], a[196:199], v[80:83], v[252:255]\n" \
"ds_read_b64_tr_b16 a[196:197], v35 offset:10400\n" \
"s_waitcnt lgkmcnt(9)\n" \
"v_mfma_f32_16x16x32_bf16 v[112:115], a[208:211], a[232:235], v[112:115]\n" \
"buffer_load_dwordx4 v1, s[80:83], s67 offen offset:1024 lds\n" \
"v_mfma_f32_16x16x32_bf16 v[116:119], a[208:211], a[248:251], v[116:119]\n" \
"ds_read_b64_tr_b16 a[198:199], v35 offset:12704\n" \
"v_mfma_f32_16x16x32_bf16 v[184:187], a[208:211], v[76:79], v[184:187]\n" \
"v_mfma_f32_16x16x32_bf16 v[188:191], a[208:211], v[100:103], v[188:191]\n" \
"ds_read_b64_tr_b16 a[208:209], v35 offset:96\n" \
"ds_read_b64_tr_b16 a[210:211], v35 offset:2400\n" \
"v_mfma_f32_16x16x32_bf16 v[120:123], a[212:215], a[232:235], v[120:123]\n" \
"v_mfma_f32_16x16x32_bf16 v[124:127], a[212:215], a[248:251], v[124:127]\n" \
"buffer_load_dwordx4 v2, s[80:83], s67 offen offset:2048 lds\n" \
"v_mfma_f32_16x16x32_bf16 v[192:195], a[212:215], v[76:79], v[192:195]\n" \
"v_mfma_f32_16x16x32_bf16 v[196:199], a[212:215], v[100:103], v[196:199]\n" \
"ds_read_b64_tr_b16 a[212:213], v35 offset:10464\n" \
"ds_read_b64_tr_b16 a[214:215], v35 offset:12768\n" \
"v_mfma_f32_16x16x32_bf16 v[128:131], a[216:219], a[232:235], v[128:131]\n" \
"v_mfma_f32_16x16x32_bf16 v[132:135], a[216:219], a[248:251], v[132:135]\n" \
"v_mfma_f32_16x16x32_bf16 v[200:203], a[216:219], v[76:79], v[200:203]\n" \
"buffer_load_dwordx4 v3, s[80:83], s67 offen offset:3072 lds\n" \
"v_mfma_f32_16x16x32_bf16 v[204:207], a[216:219], v[100:103], v[204:207]\n" \
"ds_read_b64_tr_b16 a[216:217], v35 offset:128\n" \
"ds_read_b64_tr_b16 a[218:219], v35 offset:2432\n" \
"v_mfma_f32_16x16x32_bf16 v[136:139], a[0:3], a[232:235], v[136:139]\n" \
"s_add_u32 m0, s99, 0x15400\n" \
"v_mfma_f32_16x16x32_bf16 v[140:143], a[0:3], a[248:251], v[140:143]\n" \
"v_mfma_f32_16x16x32_bf16 v[208:211], a[0:3], v[76:79], v[208:211]\n" \
"v_mfma_f32_16x16x32_bf16 v[212:215], a[0:3], v[100:103], v[212:215]\n" \
"ds_read_b64_tr_b16 a[0:1], v35 offset:10496\n" \
"ds_read_b64_tr_b16 a[2:3], v35 offset:12800\n" \
"v_mfma_f32_16x16x32_bf16 v[144:147], a[4:7], a[232:235], v[144:147]\n" \
"buffer_load_dwordx4 v4, s[80:83], s67 offen offset:0 lds\n" \
"v_mfma_f32_16x16x32_bf16 v[148:151], a[4:7], a[248:251], v[148:151]\n" \
"v_mfma_f32_16x16x32_bf16 v[216:219], a[4:7], v[76:79], v[216:219]\n" \
"v_mfma_f32_16x16x32_bf16 v[220:223], a[4:7], v[100:103], v[220:223]\n" \
"ds_read_b64_tr_b16 a[4:5], v35 offset:160\n" \
"ds_read_b64_tr_b16 a[6:7], v35 offset:2464\n" \
"v_mfma_f32_16x16x32_bf16 v[152:155], a[8:11], a[232:235], v[152:155]\n" \
"v_mfma_f32_16x16x32_bf16 v[156:159], a[8:11], a[248:251], v[156:159]\n" \
"buffer_load_dwordx4 v5, s[80:83], s67 offen offset:1024 lds\n" \
"v_mfma_f32_16x16x32_bf16 v[224:227], a[8:11], v[76:79], v[224:227]\n" \
"v_mfma_f32_16x16x32_bf16 v[228:231], a[8:11], v[100:103], v[228:231]\n" \
"ds_read_b64_tr_b16 a[8:9], v35 offset:10528\n" \
"ds_read_b64_tr_b16 a[10:11], v35 offset:12832\n" \
"v_mfma_f32_16x16x32_bf16 v[160:163], a[12:15], a[232:235], v[160:163]\n" \
"v_mfma_f32_16x16x32_bf16 v[164:167], a[12:15], a[248:251], v[164:167]\n" \
"v_mfma_f32_16x16x32_bf16 v[232:235], a[12:15], v[76:79], v[232:235]\n" \
"buffer_load_dwordx4 v6, s[80:83], s67 offen offset:2048 lds\n" \
"v_mfma_f32_16x16x32_bf16 v[236:239], a[12:15], v[100:103], v[236:239]\n" \
"ds_read_b64_tr_b16 a[12:13], v35 offset:192\n" \
"ds_read_b64_tr_b16 a[14:15], v35 offset:2496\n" \
"v_mfma_f32_16x16x32_bf16 v[168:171], a[16:19], a[232:235], v[168:171]\n" \
"v_mfma_f32_16x16x32_bf16 v[172:175], a[16:19], a[248:251], v[172:175]\n" \
"v_mfma_f32_16x16x32_bf16 v[240:243], a[16:19], v[76:79], v[240:243]\n" \
"v_mfma_f32_16x16x32_bf16 v[244:247], a[16:19], v[100:103], v[244:247]\n" \
"buffer_load_dwordx4 v7, s[80:83], s67 offen offset:3072 lds\n" \
"v_mfma_f32_16x16x32_bf16 v[176:179], a[20:23], a[232:235], v[176:179]\n" \
"s_add_u32 m0, s99, 0x16400\n" \
"ds_read_b64_tr_b16 a[16:17], v35 offset:10560\n" \
"v_mfma_f32_16x16x32_bf16 v[180:183], a[20:23], a[248:251], v[180:183]\n" \
"ds_read_b64_tr_b16 a[18:19], v35 offset:12864\n" \
"v_mfma_f32_16x16x32_bf16 v[248:251], a[20:23], v[76:79], v[248:251]\n" \
"v_mfma_f32_16x16x32_bf16 v[252:255], a[20:23], v[100:103], v[252:255]\n" \
"ds_read_b64_tr_b16 a[20:21], v35 offset:224\n" \
"s_waitcnt lgkmcnt(7)\n" \
"v_mfma_f32_16x16x32_bf16 v[112:115], a[24:27], a[32:35], v[112:115]\n" \
"buffer_load_dwordx4 v8, s[80:83], s67 offen offset:0 lds\n" \
"v_mfma_f32_16x16x32_bf16 v[112:115], a[28:31], a[36:39], v[112:115]\n" \
"s_add_u32 m0, s92, 0x0\n" \
"ds_read_b64_tr_b16 a[22:23], v35 offset:2528\n" \
"v_mfma_f32_16x16x32_bf16 v[116:119], a[28:31], a[44:47], v[116:119]\n" \
"v_readlane_b32 s67, v11, 44\n" \
"v_mfma_f32_16x16x32_bf16 v[116:119], a[24:27], a[40:43], v[116:119]\n" \
"v_mfma_f32_16x16x32_bf16 v[184:187], a[24:27], a[48:51], v[184:187]\n" \
"v_mfma_f32_16x16x32_bf16 v[184:187], a[28:31], a[52:55], v[184:187]\n" \
"buffer_load_dword v47, v38, s[56:59], s71 offen sc1\n" \
"v_mfma_f32_16x16x32_bf16 v[188:191], a[28:31], a[60:63], v[188:191]\n" \
"ds_read_b64_tr_b16 a[28:29], v35 offset:256\n" \
"ds_read_b64_tr_b16 a[30:31], v35 offset:2560\n" \
"v_mfma_f32_16x16x32_bf16 v[188:191], a[24:27], a[56:59], v[188:191]\n" \
"ds_read_b64_tr_b16 a[24:25], v35 offset:10592\n" \
"ds_read_b64_tr_b16 a[26:27], v35 offset:12896\n" \
"v_mfma_f32_16x16x32_bf16 v[120:123], a[192:195], a[32:35], v[120:123]\n" \
"v_mfma_f32_16x16x32_bf16 v[120:123], a[196:199], a[36:39], v[120:123]\n" \
"v_mfma_f32_16x16x32_bf16 v[124:127], a[196:199], a[44:47], v[124:127]\n" \
"buffer_load_dwordx4 v0, s[80:83], s73 offen offset:0 lds\n" \
"v_mfma_f32_16x16x32_bf16 v[124:127], a[192:195], a[40:43], v[124:127]\n" \
"v_mfma_f32_16x16x32_bf16 v[192:195], a[192:195], a[48:51], v[192:195]\n" \
"v_mfma_f32_16x16x32_bf16 v[192:195], a[196:199], a[52:55], v[192:195]\n" \
"v_mfma_f32_16x16x32_bf16 v[196:199], a[196:199], a[60:63], v[196:199]\n" \
"v_mfma_f32_16x16x32_bf16 v[196:199], a[192:195], a[56:59], v[196:199]\n" \
"buffer_load_dwordx4 v1, s[80:83], s73 offen offset:1024 lds\n" \
"v_mfma_f32_16x16x32_bf16 v[128:131], a[200:203], a[32:35], v[128:131]\n" \
"ds_read_b64_tr_b16 a[192:193], v35 offset:10624\n" \
"ds_read_b64_tr_b16 a[194:195], v35 offset:12928\n" \
"v_mfma_f32_16x16x32_bf16 v[128:131], a[204:207], a[36:39], v[128:131]\n" \
"v_mfma_f32_16x16x32_bf16 v[132:135], a[204:207], a[44:47], v[132:135]\n" \
"v_mfma_f32_16x16x32_bf16 v[132:135], a[200:203], a[40:43], v[132:135]\n" \
"v_mfma_f32_16x16x32_bf16 v[200:203], a[200:203], a[48:51], v[200:203]\n" \
"buffer_load_dwordx4 v2, s[80:83], s73 offen offset:2048 lds\n" \
"v_mfma_f32_16x16x32_bf16 v[200:203], a[204:207], a[52:55], v[200:203]\n" \
"v_mfma_f32_16x16x32_bf16 v[204:207], a[204:207], a[60:63], v[204:207]\n" \
"v_mfma_f32_16x16x32_bf16 v[204:207], a[200:203], a[56:59], v[204:207]\n" \
"v_mfma_f32_16x16x32_bf16 v[136:139], a[208:211], a[32:35], v[136:139]\n" \
"v_mfma_f32_16x16x32_bf16 v[136:139], a[212:215], a[36:39], v[136:139]\n" \
"buffer_load_dwordx4 v3, s[80:83], s73 offen offset:3072 lds\n" \
"v_mfma_f32_16x16x32_bf16 v[140:143], a[212:215], a[44:47], v[140:143]\n" \
"s_add_u32 m0, s92, 0x1000\n" \
"v_mfma_f32_16x16x32_bf16 v[140:143], a[208:211], a[40:43], v[140:143]\n" \
"v_mfma_f32_16x16x32_bf16 v[208:211], a[208:211], a[48:51], v[208:211]\n" \
"v_mfma_f32_16x16x32_bf16 v[208:211], a[212:215], a[52:55], v[208:211]\n" \
"v_mfma_f32_16x16x32_bf16 v[212:215], a[212:215], a[60:63], v[212:215]\n" \
"buffer_load_dwordx4 v4, s[80:83], s73 offen offset:0 lds\n" \
"v_mfma_f32_16x16x32_bf16 v[212:215], a[208:211], a[56:59], v[212:215]\n" \
"v_mfma_f32_16x16x32_bf16 v[144:147], a[216:219], a[32:35], v[144:147]\n" \
"v_mfma_f32_16x16x32_bf16 v[144:147], a[0:3], a[36:39], v[144:147]\n" \
"v_mfma_f32_16x16x32_bf16 v[148:151], a[0:3], a[44:47], v[148:151]\n" \
"v_mfma_f32_16x16x32_bf16 v[148:151], a[216:219], a[40:43], v[148:151]\n" \
"buffer_load_dwordx4 v5, s[80:83], s73 offen offset:1024 lds\n" \
"v_mfma_f32_16x16x32_bf16 v[216:219], a[216:219], a[48:51], v[216:219]\n" \
"v_mfma_f32_16x16x32_bf16 v[216:219], a[0:3], a[52:55], v[216:219]\n" \
"v_mfma_f32_16x16x32_bf16 v[220:223], a[0:3], a[60:63], v[220:223]\n" \
"v_mfma_f32_16x16x32_bf16 v[220:223], a[216:219], a[56:59], v[220:223]\n" \
"v_mfma_f32_16x16x32_bf16 v[152:155], a[4:7], a[32:35], v[152:155]\n" \
"buffer_load_dwordx4 v6, s[80:83], s73 offen offset:2048 lds\n" \
"s_waitcnt lgkmcnt(0)\n" \
"v_mfma_f32_16x16x32_bf16 v[152:155], a[8:11], a[36:39], v[152:155]\n" \
"v_mfma_f32_16x16x32_bf16 v[156:159], a[8:11], a[44:47], v[156:159]\n" \
"v_mfma_f32_16x16x32_bf16 v[156:159], a[4:7], a[40:43], v[156:159]\n" \
"v_mfma_f32_16x16x32_bf16 v[224:227], a[4:7], a[48:51], v[224:227]\n" \
"buffer_load_dwordx4 v7, s[80:83], s73 offen offset:3072 lds\n" \
"s_add_u32 m0, s92, 0x2000\n" \
"v_mfma_f32_16x16x32_bf16 v[224:227], a[8:11], a[52:55], v[224:227]\n" \
"v_readlane_b32 s92, v11, 42\n" \
"v_mfma_f32_16x16x32_bf16 v[228:231], a[8:11], a[60:63], v[228:231]\n" \
"v_mfma_f32_16x16x32_bf16 v[228:231], a[4:7], a[56:59], v[228:231]\n" \
"s_waitcnt vmcnt(23)\n" \
"s_barrier\n" \
"buffer_load_dwordx4 v8, s[80:83], s73 offen offset:0 lds\n" \
"s_mov_b32 m0, s94\n" \
"v_mfma_f32_16x16x32_bf16 v[160:163], a[12:15], a[32:35], v[160:163]\n" \
"ds_read_b64_tr_b16 a[196:197], v10 offset:27648\n" \
"ds_read_b64_tr_b16 a[198:199], v10 offset:32256\n" \
"v_mfma_f32_16x16x32_bf16 v[160:163], a[16:19], a[36:39], v[160:163]\n" \
"ds_read_b64_tr_b16 a[200:201], v10 offset:36864\n" \
"ds_read_b64_tr_b16 a[202:203], v10 offset:41472\n" \
"v_mfma_f32_16x16x32_bf16 v[164:167], a[16:19], a[44:47], v[164:167]\n" \
"ds_read_b64_tr_b16 a[204:205], v10 offset:27680\n" \
"ds_read_b64_tr_b16 a[206:207], v10 offset:32288\n" \
"v_mfma_f32_16x16x32_bf16 v[164:167], a[12:15], a[40:43], v[164:167]\n" \
"buffer_load_dwordx4 v9, s[84:87], 0 offen lds\n" \
"v_mfma_f32_16x16x32_bf16 v[232:235], a[12:15], a[48:51], v[232:235]\n" \
"ds_read_b64_tr_b16 a[208:209], v10 offset:36896\n" \
"ds_read_b64_tr_b16 a[210:211], v10 offset:41504\n" \
"v_mfma_f32_16x16x32_bf16 v[232:235], a[16:19], a[52:55], v[232:235]\n" \
"ds_read_b64_tr_b16 a[212:213], v10 offset:27712\n" \
"ds_read_b64_tr_b16 a[214:215], v10 offset:32320\n" \
"v_mfma_f32_16x16x32_bf16 v[236:239], a[16:19], a[60:63], v[236:239]\n" \
"ds_read_b64_tr_b16 a[216:217], v10 offset:36928\n" \
"ds_read_b64_tr_b16 a[218:219], v10 offset:41536\n" \
"v_mfma_f32_16x16x32_bf16 v[236:239], a[12:15], a[56:59], v[236:239]\n" \
"ds_read_b64_tr_b16 a[0:1], v10 offset:27744\n" \
"ds_read_b64_tr_b16 a[2:3], v10 offset:32352\n" \
"buffer_load_dwordx4 a[128:131], v20, s[56:59], s68 offen offset:0 sc1\n" \
"v_mfma_f32_16x16x32_bf16 v[168:171], a[20:23], a[32:35], v[168:171]\n" \
"ds_read_b64_tr_b16 a[4:5], v10 offset:36960\n" \
"ds_read_b64_tr_b16 a[6:7], v10 offset:41568\n" \
"v_mfma_f32_16x16x32_bf16 v[168:171], a[24:27], a[36:39], v[168:171]\n" \
"ds_read_b64_tr_b16 a[8:9], v10 offset:27776\n" \
"ds_read_b64_tr_b16 a[10:11], v10 offset:32384\n" \
"v_mfma_f32_16x16x32_bf16 v[172:175], a[24:27], a[44:47], v[172:175]\n" \
"ds_read_b64_tr_b16 a[12:13], v10 offset:36992\n" \
"ds_read_b64_tr_b16 a[14:15], v10 offset:41600\n" \
"v_mfma_f32_16x16x32_bf16 v[172:175], a[20:23], a[40:43], v[172:175]\n" \
"ds_read_b64_tr_b16 a[16:17], v10 offset:27808\n" \
"ds_read_b64_tr_b16 a[18:19], v10 offset:32416\n" \
"buffer_load_dwordx4 a[132:135], v20, s[56:59], s68 offen offset:1024 sc1\n" \
"v_mfma_f32_16x16x32_bf16 v[240:243], a[20:23], a[48:51], v[240:243]\n" \
"s_waitcnt vmcnt(12)\n" \
"v_cmp_ne_u32_e32 vcc, s62, v47\n" \
"v_mfma_f32_16x16x32_bf16 v[240:243], a[24:27], a[52:55], v[240:243]\n" \
"v_readlane_b32 s73, v11, 48\n" \
"v_readlane_b32 s94, v11, 44\n" \
"v_mfma_f32_16x16x32_bf16 v[244:247], a[24:27], a[60:63], v[244:247]\n" \
"ds_read_b64_tr_b16 a[24:25], v10 offset:27840\n" \
"ds_read_b64_tr_b16 a[26:27], v10 offset:32448\n" \
"v_mfma_f32_16x16x32_bf16 v[244:247], a[20:23], a[56:59], v[244:247]\n" \
"ds_read_b64_tr_b16 a[20:21], v10 offset:37024\n" \
"ds_read_b64_tr_b16 a[22:23], v10 offset:41632\n" \
"buffer_load_dwordx4 a[136:139], v20, s[56:59], s68 offen offset:2048 sc1\n" \
"v_mfma_f32_16x16x32_bf16 v[176:179], a[28:31], a[32:35], v[176:179]\n" \
"s_cbranch_vccnz L_sb_%=\n" \
"L_rb_%=:\n" \
"v_readlane_b32 s71, v11, 46\n" \
"v_mfma_f32_16x16x32_bf16 v[176:179], a[192:195], a[36:39], v[176:179]\n" \
"v_cmp_eq_u32_e32 vcc, 0, v44\n" \
"v_mfma_f32_16x16x32_bf16 v[180:183], a[192:195], a[44:47], v[180:183]\n" \
"v_mfma_f32_16x16x32_bf16 v[180:183], a[28:31], a[40:43], v[180:183]\n" \
"v_cndmask_b32_e32 v50, 0, v45, vcc\n" \
"v_cmp_eq_u32_e32 vcc, 1, v44\n" \
"v_mfma_f32_16x16x32_bf16 v[248:251], a[28:31], a[48:51], v[248:251]\n" \
"buffer_load_dwordx4 a[140:143], v20, s[56:59], s68 offen offset:3072 sc1\n" \
"v_mfma_f32_16x16x32_bf16 v[248:251], a[192:195], a[52:55], v[248:251]\n" \
"v_cndmask_b32_e32 v51, 0, v45, vcc\n" \
"v_bfe_u32 v45, v20, 1, 5\n" \
"v_mfma_f32_16x16x32_bf16 v[252:255], a[192:195], a[60:63], v[252:255]\n" \
"ds_read_b64_tr_b16 a[192:193], v10 offset:27872\n" \
"ds_read_b64_tr_b16 a[194:195], v10 offset:32480\n" \
"v_mfma_f32_16x16x32_bf16 v[252:255], a[28:31], a[56:59], v[252:255]\n" \
"ds_read_b64_tr_b16 a[28:29], v10 offset:37056\n" \
"s_waitcnt lgkmcnt(9)\n" \
"v_mfma_f32_16x16x32_bf16 v[112:115], a[196:199], a[64:67], v[112:115]\n" \
"ds_read_b64_tr_b16 a[30:31], v10 offset:41664\n" \
"v_and_b32_e32 v45, 24, v45\n" \
"v_mfma_f32_16x16x32_bf16 v[112:115], a[200:203], a[68:71], v[112:115]\n" \
"buffer_load_dwordx4 a[156:159], v32, s[56:59], s68 offen offset:3072 sc1\n" \
"v_mfma_f32_16x16x32_bf16 v[116:119], a[200:203], a[84:87], v[116:119]\n" \
"ds_read_b64_tr_b16 a[200:201], v10 offset:27904\n" \
"ds_read_b64_tr_b16 a[202:203], v10 offset:32512\n" \
"v_mfma_f32_16x16x32_bf16 v[116:119], a[196:199], a[80:83], v[116:119]\n" \
"ds_read_b64_tr_b16 a[196:197], v10 offset:37088\n" \
"ds_read_b64_tr_b16 a[198:199], v10 offset:41696\n" \
"v_mfma_f32_16x16x32_bf16 v[120:123], a[204:207], a[64:67], v[120:123]\n" \
"v_and_b32_e32 v44, 0x3c0, v20\n" \
"v_add_u32_e32 v45, 0x1d400, v45\n" \
"v_mfma_f32_16x16x32_bf16 v[120:123], a[208:211], a[68:71], v[120:123]\n" \
"v_add_u32_e32 v36, v44, v45\n" \
"v_mfma_f32_16x16x32_bf16 v[124:127], a[208:211], a[84:87], v[124:127]\n" \
"buffer_load_dwordx4 a[152:155], v32, s[56:59], s68 offen offset:2048 sc1\n" \
"v_mfma_f32_16x16x32_bf16 v[124:127], a[204:207], a[80:83], v[124:127]\n" \
"ds_read_b64_tr_b16 a[204:205], v10 offset:37120\n" \
"ds_read_b64_tr_b16 a[206:207], v10 offset:41728\n" \
"v_mfma_f32_16x16x32_bf16 v[128:131], a[212:215], a[64:67], v[128:131]\n" \
"ds_read_b64_tr_b16 a[208:209], v10 offset:46080\n" \
"ds_read_b64_tr_b16 a[210:211], v10 offset:50688\n" \
"v_mfma_f32_16x16x32_bf16 v[128:131], a[216:219], a[68:71], v[128:131]\n" \
"v_mfma_f32_16x16x32_bf16 v[132:135], a[216:219], a[84:87], v[132:135]\n" \
"ds_read_b64_tr_b16 a[216:217], v10 offset:46112\n" \
"ds_read_b64_tr_b16 a[218:219], v10 offset:50720\n" \
"buffer_load_dwordx4 a[148:151], v32, s[56:59], s68 offen offset:1024 sc1\n" \
"v_mfma_f32_16x16x32_bf16 v[132:135], a[212:215], a[80:83], v[132:135]\n" \
"ds_read_b64_tr_b16 a[212:213], v10 offset:55296\n" \
"ds_read_b64_tr_b16 a[214:215], v10 offset:59904\n" \
"v_mfma_f32_16x16x32_bf16 v[136:139], a[0:3], a[64:67], v[136:139]\n" \
"v_mfma_f32_16x16x32_bf16 v[136:139], a[4:7], a[68:71], v[136:139]\n" \
"v_mfma_f32_16x16x32_bf16 v[140:143], a[4:7], a[84:87], v[140:143]\n" \
"ds_read_b64_tr_b16 a[4:5], v10 offset:46144\n" \
"ds_read_b64_tr_b16 a[6:7], v10 offset:50752\n" \
"buffer_load_dwordx4 a[144:147], v32, s[56:59], s68 offen offset:0 sc1\n" \
"v_mfma_f32_16x16x32_bf16 v[140:143], a[0:3], a[80:83], v[140:143]\n" \
"ds_read_b64_tr_b16 a[0:1], v10 offset:55328\n" \
"ds_read_b64_tr_b16 a[2:3], v10 offset:59936\n" \
"v_mfma_f32_16x16x32_bf16 v[144:147], a[8:11], a[64:67], v[144:147]\n" \
"v_readlane_b32 s68, v11, 49\n" \
"v_mfma_f32_16x16x32_bf16 v[144:147], a[12:15], a[68:71], v[144:147]\n" \
"v_mfma_f32_16x16x32_bf16 v[148:151], a[12:15], a[84:87], v[148:151]\n" \
"ds_read_b64_tr_b16 a[12:13], v10 offset:46176\n" \
"ds_read_b64_tr_b16 a[14:15], v10 offset:50784\n" \
"buffer_load_dwordx4 a[96:99], v20, s[56:59], s69 offen offset:0 sc1\n" \
"v_mfma_f32_16x16x32_bf16 v[148:151], a[8:11], a[80:83], v[148:151]\n" \
"ds_read_b64_tr_b16 a[8:9], v10 offset:55360\n" \
"s_waitcnt lgkmcnt(15)\n" \
"v_mfma_f32_16x16x32_bf16 v[152:155], a[16:19], a[64:67], v[152:155]\n" \
"ds_read_b64_tr_b16 a[10:11], v10 offset:59968\n" \
"v_mfma_f32_16x16x32_bf16 v[152:155], a[20:23], a[68:71], v[152:155]\n" \
"v_mfma_f32_16x16x32_bf16 v[156:159], a[20:23], a[84:87], v[156:159]\n" \
"ds_read_b64_tr_b16 a[20:21], v10 offset:46208\n" \
"ds_read_b64_tr_b16 a[22:23], v10 offset:50816\n" \
"buffer_load_dwordx4 a[100:103], v20, s[56:59], s69 offen offset:1024 sc1\n" \
"v_mfma_f32_16x16x32_bf16 v[156:159], a[16:19], a[80:83], v[156:159]\n" \
"ds_read_b64_tr_b16 a[16:17], v10 offset:55392\n" \
"ds_read_b64_tr_b16 a[18:19], v10 offset:60000\n" \
"v_mfma_f32_16x16x32_bf16 v[160:163], a[24:27], a[64:67], v[160:163]\n" \
"v_mfma_f32_16x16x32_bf16 v[160:163], a[28:31], a[68:71], v[160:163]\n" \
"v_mfma_f32_16x16x32_bf16 v[164:167], a[28:31], a[84:87], v[164:167]\n" \
"ds_read_b64_tr_b16 a[28:29], v10 offset:46240\n" \
"ds_read_b64_tr_b16 a[30:31], v10 offset:50848\n" \
"buffer_load_dwordx4 a[104:107], v20, s[56:59], s69 offen offset:2048 sc1\n" \
"v_mfma_f32_16x16x32_bf16 v[164:167], a[24:27], a[80:83], v[164:167]\n" \
"ds_read_b64_tr_b16 a[24:25], v10 offset:55424\n" \
"ds_read_b64_tr_b16 a[26:27], v10 offset:60032\n" \
"v_mfma_f32_16x16x32_bf16 v[168:171], a[192:195], a[64:67], v[168:171]\n" \
"v_mfma_f32_16x16x32_bf16 v[168:171], a[196:199], a[68:71], v[168:171]\n" \
"v_mfma_f32_16x16x32_bf16 v[172:175], a[196:199], a[84:87], v[172:175]\n" \
"ds_read_b64_tr_b16 a[196:197], v10 offset:46272\n" \
"ds_read_b64_tr_b16 a[198:199], v10 offset:50880\n" \
"buffer_load_dwordx4 a[108:111], v20, s[56:59], s69 offen offset:3072 sc1\n" \
"v_mfma_f32_16x16x32_bf16 v[172:175], a[192:195], a[80:83], v[172:175]\n" \
"ds_read_b64_tr_b16 a[192:193], v10 offset:55456\n" \
"ds_read_b64_tr_b16 a[194:195], v10 offset:60064\n" \
"v_mfma_f32_16x16x32_bf16 v[176:179], a[200:203], a[64:67], v[176:179]\n" \
"s_waitcnt lgkmcnt(13)\n" \
"v_mfma_f32_16x16x32_bf16 v[176:179], a[204:207], a[68:71], v[176:179]\n" \
"v_mfma_f32_16x16x32_bf16 v[180:183], a[204:207], a[84:87], v[180:183]\n" \
"ds_read_b64_tr_b16 a[204:205], v10 offset:46304\n" \
"ds_read_b64_tr_b16 a[206:207], v10 offset:50912\n" \
"buffer_load_dwordx4 a[124:127], v32, s[56:59], s69 offen offset:3072 sc1\n" \
"v_mfma_f32_16x16x32_bf16 v[180:183], a[200:203], a[80:83], v[180:183]\n" \
"ds_read_b64_tr_b16 a[200:201], v10 offset:55488\n" \
"ds_read_b64_tr_b16 a[202:203], v10 offset:60096\n" \
"v_mfma_f32_16x16x32_bf16 v[112:115], a[208:211], a[72:75], v[112:115]\n" \
"v_mfma_f32_16x16x32_bf16 v[112:115], a[212:215], a[76:79], v[112:115]\n" \
"v_mfma_f32_16x16x32_bf16 v[116:119], a[212:215], a[92:95], v[116:119]\n" \
"ds_read_b64_tr_b16 a[212:213], v10 offset:46336\n" \
"ds_read_b64_tr_b16 a[214:215], v10 offset:50944\n" \
"buffer_load_dwordx4 a[120:123], v32, s[56:59], s69 offen offset:2048 sc1\n" \
"v_mfma_f32_16x16x32_bf16 v[116:119], a[208:211], a[88:91], v[116:119]\n" \
"ds_read_b64_tr_b16 a[208:209], v10 offset:55520\n" \
"ds_read_b64_tr_b16 a[210:211], v10 offset:60128\n" \
"v_mfma_f32_16x16x32_bf16 v[120:123], a[216:219], a[72:75], v[120:123]\n" \
"v_mfma_f32_16x16x32_bf16 v[120:123], a[0:3], a[76:79], v[120:123]\n" \
"v_mfma_f32_16x16x32_bf16 v[124:127], a[0:3], a[92:95], v[124:127]\n" \
"v_mfma_f32_16x16x32_bf16 v[124:127], a[216:219], a[88:91], v[124:127]\n" \
"buffer_load_dwordx4 a[116:119], v32, s[56:59], s69 offen offset:1024 sc1\n" \
"v_mfma_f32_16x16x32_bf16 v[128:131], a[4:7], a[72:75], v[128:131]\n" \
"ds_read_b64_tr_b16 a[216:217], v10 offset:55552\n" \
"s_waitcnt lgkmcnt(9)\n" \
"v_mfma_f32_16x16x32_bf16 v[128:131], a[8:11], a[76:79], v[128:131]\n" \
"ds_read_b64_tr_b16 a[218:219], v10 offset:60160\n" \
"v_mfma_f32_16x16x32_bf16 v[132:135], a[8:11], a[92:95], v[132:135]\n" \
"v_mfma_f32_16x16x32_bf16 v[132:135], a[4:7], a[88:91], v[132:135]\n" \
"v_mfma_f32_16x16x32_bf16 v[136:139], a[12:15], a[72:75], v[136:139]\n" \
"buffer_load_dwordx4 a[112:115], v32, s[56:59], s69 offen offset:0 sc1\n" \
"v_mfma_f32_16x16x32_bf16 v[136:139], a[16:19], a[76:79], v[136:139]\n" \
"v_readlane_b32 s69, v11, 5\n" \
"v_mfma_f32_16x16x32_bf16 v[140:143], a[16:19], a[92:95], v[140:143]\n" \
"v_mfma_f32_16x16x32_bf16 v[140:143], a[12:15], a[88:91], v[140:143]\n" \
"s_waitcnt vmcnt(16)\n" \
"s_barrier\n" \
"buffer_load_dwordx4 a[32:35], v20, s[56:59], s64 offen offset:0 sc1\n" \
"v_mfma_f32_16x16x32_bf16 v[144:147], a[20:23], a[72:75], v[144:147]\n" \
"ds_read_b64_tr_b16 a[0:1], v33 offset:18432\n" \
"ds_read_b64_tr_b16 a[2:3], v33 offset:23040\n" \
"v_mfma_f32_16x16x32_bf16 v[144:147], a[24:27], a[76:79], v[144:147]\n" \
"ds_read_b64_tr_b16 a[4:5], v33 offset:27648\n" \
"ds_read_b64_tr_b16 a[6:7], v33 offset:32256\n" \
"v_mfma_f32_16x16x32_bf16 v[148:151], a[24:27], a[92:95], v[148:151]\n" \
"ds_read_b64_tr_b16 a[8:9], v33 offset:36864\n" \
"ds_read_b64_tr_b16 a[10:11], v33 offset:41472\n" \
"v_mfma_f32_16x16x32_bf16 v[148:151], a[20:23], a[88:91], v[148:151]\n" \
"ds_read_b64_tr_b16 a[12:13], v33 offset:46080\n" \
"ds_read_b64_tr_b16 a[14:15], v33 offset:50688\n" \
"buffer_load_dwordx4 a[36:39], v20, s[56:59], s64 offen offset:1024 sc1\n" \
"v_mfma_f32_16x16x32_bf16 v[152:155], a[28:31], a[72:75], v[152:155]\n" \
"ds_read_b64_tr_b16 a[16:17], v34 offset:0\n" \
"ds_read_b64_tr_b16 a[18:19], v34 offset:4608\n" \
"v_mfma_f32_16x16x32_bf16 v[152:155], a[192:195], a[76:79], v[152:155]\n" \
"ds_read_b64_tr_b16 a[20:21], v34 offset:9216\n" \
"ds_read_b64_tr_b16 a[22:23], v34 offset:13824\n" \
"v_mfma_f32_16x16x32_bf16 v[156:159], a[192:195], a[92:95], v[156:159]\n" \
"ds_read_b64_tr_b16 a[24:25], v34 offset:18432\n" \
"ds_read_b64_tr_b16 a[26:27], v34 offset:23040\n" \
"v_mfma_f32_16x16x32_bf16 v[156:159], a[28:31], a[88:91], v[156:159]\n" \
"ds_read_b64_tr_b16 a[28:29], v33 offset:0\n" \
"ds_read_b64_tr_b16 a[30:31], v33 offset:4608\n" \
"buffer_load_dwordx4 a[40:43], v20, s[56:59], s64 offen offset:2048 sc1\n" \
"v_mfma_f32_16x16x32_bf16 v[160:163], a[196:199], a[72:75], v[160:163]\n" \
"ds_read_b64_tr_b16 a[192:193], v33 offset:18464\n" \
"s_waitcnt lgkmcnt(15)\n" \
"v_mfma_f32_16x16x32_bf16 v[160:163], a[200:203], a[76:79], v[160:163]\n" \
"ds_read_b64_tr_b16 a[194:195], v33 offset:23072\n" \
"v_mfma_f32_16x16x32_bf16 v[164:167], a[200:203], a[92:95], v[164:167]\n" \
"buffer_load_dwordx4 a[44:47], v20, s[56:59], s64 offen offset:3072 sc1\n" \
"v_mfma_f32_16x16x32_bf16 v[164:167], a[196:199], a[88:91], v[164:167]\n" \
"ds_read_b64_tr_b16 a[196:197], v33 offset:27680\n" \
"ds_read_b64_tr_b16 a[198:199], v33 offset:32288\n" \
"v_mfma_f32_16x16x32_bf16 v[168:171], a[204:207], a[72:75], v[168:171]\n" \
"ds_read_b64_tr_b16 a[200:201], v33 offset:36896\n" \
"ds_read_b64_tr_b16 a[202:203], v33 offset:41504\n" \
"v_mfma_f32_16x16x32_bf16 v[168:171], a[208:211], a[76:79], v[168:171]\n" \
"v_mfma_f32_16x16x32_bf16 v[172:175], a[208:211], a[92:95], v[172:175]\n" \
"ds_read_b64_tr_b16 a[208:209], v34 offset:32\n" \
"ds_read_b64_tr_b16 a[210:211], v34 offset:4640\n" \
"buffer_load_dwordx4 a[60:63], v32, s[56:59], s64 offen offset:3072 sc1\n" \
"v_mfma_f32_16x16x32_bf16 v[172:175], a[204:207], a[88:91], v[172:175]\n" \
"ds_read_b64_tr_b16 a[204:205], v33 offset:46112\n" \
"ds_read_b64_tr_b16 a[206:207], v33 offset:50720\n" \
"v_mfma_f32_16x16x32_bf16 v[176:179], a[212:215], a[72:75], v[176:179]\n" \
"v_mfma_f32_16x16x32_bf16 v[176:179], a[216:219], a[76:79], v[176:179]\n" \
"v_mfma_f32_16x16x32_bf16 v[180:183], a[216:219], a[92:95], v[180:183]\n" \
"ds_read_b64_tr_b16 a[216:217], v34 offset:18464\n" \
"ds_read_b64_tr_b16 a[218:219], v34 offset:23072\n" \
"buffer_load_dwordx4 a[56:59], v32, s[56:59], s64 offen offset:2048 sc1\n" \
"v_mfma_f32_16x16x32_bf16 v[180:183], a[212:215], a[88:91], v[180:183]\n" \
"ds_read_b64_tr_b16 a[212:213], v34 offset:9248\n" \
"s_waitcnt vmcnt(21)\n" \
"v_mfma_f32_16x16x32_bf16 v[112:115], a[0:3], a[128:131], v[112:115]\n" \
"ds_read_b64_tr_b16 a[214:215], v34 offset:13856\n" \
"s_waitcnt vmcnt(20) lgkmcnt(13)\n" \
"v_mfma_f32_16x16x32_bf16 v[112:115], a[4:7], a[132:135], v[112:115]\n" \
"s_waitcnt vmcnt(19)\n" \
"v_mfma_f32_16x16x32_bf16 v[112:115], a[8:11], a[136:139], v[112:115]\n" \
"s_waitcnt vmcnt(18)\n" \
"v_mfma_f32_16x16x32_bf16 v[112:115], a[12:15], a[140:143], v[112:115]\n" \
"buffer_load_dwordx4 a[52:55], v32, s[56:59], s64 offen offset:1024 sc1\n" \
"s_waitcnt vmcnt(18)\n" \
"v_mfma_f32_16x16x32_bf16 v[116:119], a[12:15], a[156:159], v[116:119]\n" \
"ds_read_b64_tr_b16 a[12:13], v33 offset:36928\n" \
"s_waitcnt vmcnt(17)\n" \
"v_mfma_f32_16x16x32_bf16 v[116:119], a[8:11], a[152:155], v[116:119]\n" \
"ds_read_b64_tr_b16 a[8:9], v33 offset:27712\n" \
"ds_read_b64_tr_b16 a[10:11], v33 offset:32320\n" \
"ds_read_b64_tr_b16 a[14:15], v33 offset:41536\n" \
"s_waitcnt vmcnt(16)\n" \
"v_mfma_f32_16x16x32_bf16 v[116:119], a[4:7], a[148:151], v[116:119]\n" \
"buffer_load_dwordx4 a[48:51], v32, s[56:59], s64 offen offset:0 sc1\n" \
"ds_read_b64_tr_b16 a[4:5], v33 offset:18496\n" \
"ds_read_b64_tr_b16 a[6:7], v33 offset:23104\n" \
"v_readlane_b32 s64, v11, 38\n" \
"s_waitcnt vmcnt(16)\n" \
"v_mfma_f32_16x16x32_bf16 v[116:119], a[0:3], a[144:147], v[116:119]\n" \
"buffer_load_dwordx4 a[160:163], v20, s[56:59], s72 offen offset:0 sc1\n" \
"ds_read_b64_tr_b16 a[0:1], v33 offset:32\n" \
"ds_read_b64_tr_b16 a[2:3], v33 offset:4640\n" \
"s_waitcnt vmcnt(16)\n" \
"v_mfma_f32_16x16x32_bf16 v[112:115], a[16:19], a[96:99], v[112:115]\n" \
"buffer_load_dwordx4 a[164:167], v20, s[56:59], s72 offen offset:1024 sc1\n" \
"s_waitcnt vmcnt(16)\n" \
"v_mfma_f32_16x16x32_bf16 v[112:115], a[20:23], a[100:103], v[112:115]\n" \
"buffer_load_dwordx4 a[168:171], v20, s[56:59], s72 offen offset:2048 sc1\n" \
"s_waitcnt vmcnt(16)\n" \
"v_mfma_f32_16x16x32_bf16 v[112:115], a[24:27], a[104:107], v[112:115]\n" \
"buffer_load_dwordx4 a[172:175], v20, s[56:59], s72 offen offset:3072 sc1\n" \
"s_waitcnt vmcnt(16)\n" \
"v_mfma_f32_16x16x32_bf16 v[112:115], a[28:31], a[108:111], v[112:115]\n" \
"buffer_load_dwordx4 a[188:191], v32, s[56:59], s72 offen offset:3072 sc1\n" \
"s_waitcnt vmcnt(16)\n" \
"v_mfma_f32_16x16x32_bf16 v[116:119], a[28:31], a[124:127], v[116:119]\n" \
"buffer_load_dwordx4 a[184:187], v32, s[56:59], s72 offen offset:2048 sc1\n" \
"ds_read_b64_tr_b16 a[28:29], v34 offset:18496\n" \
"ds_read_b64_tr_b16 a[30:31], v34 offset:23104\n" \
"s_waitcnt vmcnt(16)\n" \
"v_mfma_f32_16x16x32_bf16 v[116:119], a[24:27], a[120:123], v[116:119]\n" \
"v_cvt_pk_bf16_f32 v112, v112, v113\n" \
"v_cvt_pk_bf16_f32 v113, v114, v115\n" \
"s_waitcnt vmcnt(15)\n" \
"v_mfma_f32_16x16x32_bf16 v[116:119], a[20:23], a[116:119], v[116:119]\n" \
"buffer_load_dwordx4 a[180:183], v32, s[56:59], s72 offen offset:1024 sc1\n" \
"ds_read_b64_tr_b16 a[20:21], v34 offset:64\n" \
"ds_read_b64_tr_b16 a[22:23], v34 offset:4672\n" \
"ds_read_b64_tr_b16 a[24:25], v34 offset:9280\n" \
"ds_read_b64_tr_b16 a[26:27], v34 offset:13888\n" \
"s_waitcnt vmcnt(15)\n" \
"v_mfma_f32_16x16x32_bf16 v[116:119], a[16:19], a[112:115], v[116:119]\n" \
"ds_read_b64_tr_b16 a[16:17], v33 offset:46144\n" \
"buffer_load_dwordx4 a[176:179], v32, s[56:59], s72 offen offset:0 sc1\n" \
"s_waitcnt lgkmcnt(5)\n" \
"v_mfma_f32_16x16x32_bf16 v[120:123], a[192:195], a[128:131], v[120:123]\n" \
"ds_read_b64_tr_b16 a[18:19], v33 offset:50752\n" \
"v_readlane_b32 s72, v11, 47\n" \
"v_mfma_f32_16x16x32_bf16 v[120:123], a[196:199], a[132:135], v[120:123]\n" \
"v_mfma_f32_16x16x32_bf16 v[120:123], a[200:203], a[136:139], v[120:123]\n" \
"v_cvt_pk_bf16_f32 v114, v116, v117\n" \
"v_cvt_pk_bf16_f32 v115, v118, v119\n" \
"v_mfma_f32_16x16x32_bf16 v[120:123], a[204:207], a[140:143], v[120:123]\n" \
"buffer_store_dwordx4 v[112:115], v20, s[56:59], s65 offen offset:0 sc1\n" \
"s_add_u32 s65, s65, 0x3800\n" \
"v_mfma_f32_16x16x32_bf16 v[124:127], a[204:207], a[156:159], v[124:127]\n" \
"ds_read_b64_tr_b16 a[204:205], v10 offset:46336\n" \
"ds_read_b64_tr_b16 a[206:207], v10 offset:50944\n" \
"v_mfma_f32_16x16x32_bf16 v[124:127], a[200:203], a[152:155], v[124:127]\n" \
"ds_read_b64_tr_b16 a[200:201], v10 offset:37120\n" \
"ds_read_b64_tr_b16 a[202:203], v10 offset:41728\n" \
"v_mfma_f32_16x16x32_bf16 v[124:127], a[196:199], a[148:151], v[124:127]\n" \
"ds_read_b64_tr_b16 a[196:197], v10 offset:27904\n" \
"ds_read_b64_tr_b16 a[198:199], v10 offset:32512\n" \
"v_mfma_f32_16x16x32_bf16 v[124:127], a[192:195], a[144:147], v[124:127]\n" \
"buffer_load_dwordx4 a[220:223], v20, s[56:59], s93 offen offset:0 sc1\n" \
"v_mfma_f32_16x16x32_bf16 v[120:123], a[208:211], a[96:99], v[120:123]\n" \
"ds_read_b64_tr_b16 a[192:193], v33 offset:64\n" \
"ds_read_b64_tr_b16 a[194:195], v33 offset:4672\n" \
"v_mfma_f32_16x16x32_bf16 v[120:123], a[212:215], a[100:103], v[120:123]\n" \
"v_mfma_f32_16x16x32_bf16 v[120:123], a[216:219], a[104:107], v[120:123]\n" \
"v_mfma_f32_16x16x32_bf16 v[120:123], a[0:3], a[108:111], v[120:123]\n" \
"v_mfma_f32_16x16x32_bf16 v[124:127], a[0:3], a[124:127], v[124:127]\n" \
"buffer_load_dwordx4 a[224:227], v20, s[56:59], s93 offen offset:1024 sc1\n" \
"v_mfma_f32_16x16x32_bf16 v[124:127], a[216:219], a[120:123], v[124:127]\n" \
"ds_read_b64_tr_b16 a[216:217], v33 offset:27904\n" \
"ds_read_b64_tr_b16 a[218:219], v33 offset:32512\n" \
"v_mfma_f32_16x16x32_bf16 v[124:127], a[212:215], a[116:119], v[124:127]\n" \
"ds_read_b64_tr_b16 a[212:213], v33 offset:18688\n" \
"ds_read_b64_tr_b16 a[214:215], v33 offset:23296\n" \
"v_cvt_pk_bf16_f32 v120, v120, v121\n" \
"v_cvt_pk_bf16_f32 v121, v122, v123\n" \
"v_mfma_f32_16x16x32_bf16 v[124:127], a[208:211], a[112:115], v[124:127]\n" \
"ds_read_b64_tr_b16 a[208:209], v10 offset:55552\n" \
"ds_read_b64_tr_b16 a[210:211], v10 offset:60160\n" \
"v_mfma_f32_16x16x32_bf16 v[128:131], a[4:7], a[128:131], v[128:131]\n" \
"buffer_load_dwordx4 a[228:231], v20, s[56:59], s93 offen offset:2048 sc1\n" \
"v_mfma_f32_16x16x32_bf16 v[128:131], a[8:11], a[132:135], v[128:131]\n" \
"ds_read_b64_tr_b16 a[0:1], v33 offset:37120\n" \
"ds_read_b64_tr_b16 a[2:3], v33 offset:41728\n" \
"v_mfma_f32_16x16x32_bf16 v[128:131], a[12:15], a[136:139], v[128:131]\n" \
"v_cvt_pk_bf16_f32 v122, v124, v125\n" \
"v_cvt_pk_bf16_f32 v123, v126, v127\n" \
"s_waitcnt lgkmcnt(13)\n" \
"v_mfma_f32_16x16x32_bf16 v[128:131], a[16:19], a[140:143], v[128:131]\n" \
"buffer_store_dwordx4 v[120:123], v20, s[56:59], s66 offen offset:0 sc1\n" \
"s_add_u32 s66, s66, 0x3000\n" \
"v_mfma_f32_16x16x32_bf16 v[132:135], a[16:19], a[156:159], v[132:135]\n" \
"ds_read_b64_tr_b16 a[16:17], v34 offset:18688\n" \
"ds_read_b64_tr_b16 a[18:19], v34 offset:23296\n" \
"v_mfma_f32_16x16x32_bf16 v[132:135], a[12:15], a[152:155], v[132:135]\n" \
"ds_read_b64_tr_b16 a[12:13], v34 offset:9472\n" \
"ds_read_b64_tr_b16 a[14:15], v34 offset:14080\n" \
"v_mfma_f32_16x16x32_bf16 v[132:135], a[8:11], a[148:151], v[132:135]\n" \
"ds_read_b64_tr_b16 a[8:9], v34 offset:256\n" \
"ds_read_b64_tr_b16 a[10:11], v34 offset:4864\n" \
"v_mfma_f32_16x16x32_bf16 v[132:135], a[4:7], a[144:147], v[132:135]\n" \
"buffer_load_dwordx4 a[232:235], v20, s[56:59], s93 offen offset:3072 sc1\n" \
"v_mfma_f32_16x16x32_bf16 v[128:131], a[20:23], a[96:99], v[128:131]\n" \
"ds_read_b64_tr_b16 a[4:5], v33 offset:46336\n" \
"ds_read_b64_tr_b16 a[6:7], v33 offset:50944\n" \
"v_mfma_f32_16x16x32_bf16 v[128:131], a[24:27], a[100:103], v[128:131]\n" \
"v_mfma_f32_16x16x32_bf16 v[128:131], a[28:31], a[104:107], v[128:131]\n" \
"s_waitcnt lgkmcnt(15)\n" \
"v_mfma_f32_16x16x32_bf16 v[128:131], a[192:195], a[108:111], v[128:131]\n" \
"v_mfma_f32_16x16x32_bf16 v[132:135], a[192:195], a[124:127], v[132:135]\n" \
"buffer_load_dwordx4 a[248:251], v32, s[56:59], s93 offen offset:3072 sc1\n" \
"v_mfma_f32_16x16x32_bf16 v[132:135], a[28:31], a[120:123], v[132:135]\n" \
"ds_read_b64_tr_b16 a[28:29], v10 offset:36960\n" \
"ds_read_b64_tr_b16 a[30:31], v10 offset:41568\n" \
"v_mfma_f32_16x16x32_bf16 v[132:135], a[24:27], a[116:119], v[132:135]\n" \
"L_pf21:\n" \
"s_cbranch_execz L_retB\n" \
"s_nop 0\n" \
"ds_read_b64_tr_b16 a[24:25], v10 offset:27744\n" \
"ds_read_b64_tr_b16 a[26:27], v10 offset:32352\n" \
"v_cvt_pk_bf16_f32 v128, v128, v129\n" \
"v_cvt_pk_bf16_f32 v129, v130, v131\n" \
"v_mfma_f32_16x16x32_bf16 v[132:135], a[20:23], a[112:115], v[132:135]\n" \
"ds_read_b64_tr_b16 a[20:21], v33 offset:256\n" \
"s_waitcnt vmcnt(22)\n" \
"v_mfma_f32_16x16x32_bf16 v[248:251], a[196:199], a[32:35], v[248:251]\n" \
"ds_read_b64_tr_b16 a[22:23], v33 offset:4864\n" \
"buffer_load_dwordx4 a[244:247], v32, s[56:59], s93 offen offset:2048 sc1\n" \
"s_waitcnt vmcnt(22)\n" \
"v_mfma_f32_16x16x32_bf16 v[248:251], a[200:203], a[36:39], v[248:251]\n" \
"ds_read_b64_tr_b16 a[192:193], v10 offset:46176\n" \
"v_cvt_pk_bf16_f32 v130, v132, v133\n" \
"v_cvt_pk_bf16_f32 v131, v134, v135\n" \
"s_waitcnt vmcnt(21)\n" \
"v_mfma_f32_16x16x32_bf16 v[248:251], a[204:207], a[40:43], v[248:251]\n" \
"ds_read_b64_tr_b16 a[194:195], v10 offset:50784\n" \
"buffer_store_dwordx4 v[128:131], v20, s[56:59], s67 offen offset:0 sc1\n" \
"s_add_u32 s67, s67, 0x2800\n" \
"s_waitcnt vmcnt(21) lgkmcnt(10)\n" \
"v_mfma_f32_16x16x32_bf16 v[248:251], a[208:211], a[44:47], v[248:251]\n" \
"s_waitcnt vmcnt(20)\n" \
"v_mfma_f32_16x16x32_bf16 v[252:255], a[208:211], a[60:63], v[252:255]\n" \
"ds_read_b64_tr_b16 a[208:209], v33 offset:36960\n" \
"ds_read_b64_tr_b16 a[210:211], v33 offset:41568\n" \
"s_waitcnt vmcnt(19)\n" \
"v_mfma_f32_16x16x32_bf16 v[252:255], a[204:207], a[56:59], v[252:255]\n" \
"buffer_load_dwordx4 a[240:243], v32, s[56:59], s93 offen offset:1024 sc1\n" \
"ds_read_b64_tr_b16 a[204:205], v33 offset:27744\n" \
"ds_read_b64_tr_b16 a[206:207], v33 offset:32352\n" \
"s_waitcnt vmcnt(19)\n" \
"v_mfma_f32_16x16x32_bf16 v[252:255], a[200:203], a[52:55], v[252:255]\n" \
"buffer_load_dwordx4 a[236:239], v32, s[56:59], s93 offen offset:0 sc1\n" \
"ds_read_b64_tr_b16 a[200:201], v33 offset:18528\n" \
"ds_read_b64_tr_b16 a[202:203], v33 offset:23136\n" \
"v_readlane_b32 s93, v11, 43\n" \
"s_waitcnt vmcnt(19)\n" \
"v_mfma_f32_16x16x32_bf16 v[252:255], a[196:199], a[48:51], v[252:255]\n" \
"ds_read_b64_tr_b16 a[196:197], v10 offset:55392\n" \
"ds_read_b64_tr_b16 a[198:199], v10 offset:60000\n" \
"v_mfma_f32_16x16x32_bf16 v[176:179], a[212:215], a[128:131], v[176:179]\n" \
"buffer_load_dwordx4 v[64:67], v20, s[56:59], s95 offen offset:0 sc1\n" \
"v_mfma_f32_16x16x32_bf16 v[176:179], a[216:219], a[132:135], v[176:179]\n" \
"v_mfma_f32_16x16x32_bf16 v[176:179], a[0:3], a[136:139], v[176:179]\n" \
"s_waitcnt lgkmcnt(6)\n" \
"v_mfma_f32_16x16x32_bf16 v[176:179], a[4:7], a[140:143], v[176:179]\n" \
"v_mfma_f32_16x16x32_bf16 v[180:183], a[4:7], a[156:159], v[180:183]\n" \
"v_mfma_f32_16x16x32_bf16 v[180:183], a[0:3], a[152:155], v[180:183]\n" \
"buffer_load_dwordx4 v[72:75], v32, s[56:59], s95 offen offset:0 sc1\n" \
"v_mfma_f32_16x16x32_bf16 v[180:183], a[216:219], a[148:151], v[180:183]\n" \
"v_mfma_f32_16x16x32_bf16 v[180:183], a[212:215], a[144:147], v[180:183]\n" \
"s_waitcnt vmcnt(20)\n" \
"v_mfma_f32_16x16x32_bf16 v[248:251], a[212:215], a[160:163], v[248:251]\n" \
"s_waitcnt vmcnt(19)\n" \
"v_mfma_f32_16x16x32_bf16 v[248:251], a[216:219], a[164:167], v[248:251]\n" \
"s_waitcnt vmcnt(18)\n" \
"v_mfma_f32_16x16x32_bf16 v[248:251], a[0:3], a[168:171], v[248:251]\n" \
"buffer_load_dwordx4 v[80:83], v20, s[56:59], s96 offen offset:0 sc1\n" \
"s_waitcnt vmcnt(18)\n" \
"v_mfma_f32_16x16x32_bf16 v[248:251], a[4:7], a[172:175], v[248:251]\n" \
"buffer_load_dwordx4 v[88:91], v32, s[56:59], s96 offen offset:0 sc1\n" \
"s_waitcnt vmcnt(18)\n" \
"v_mfma_f32_16x16x32_bf16 v[252:255], a[4:7], a[188:191], v[252:255]\n" \
"ds_read_b64_tr_b16 a[4:5], v34 offset:18528\n" \
"ds_read_b64_tr_b16 a[6:7], v34 offset:23136\n" \
"buffer_load_dwordx4 v[68:71], v20, s[56:59], s95 offen offset:1024 sc1\n" \
"s_waitcnt vmcnt(18)\n" \
"v_mfma_f32_16x16x32_bf16 v[252:255], a[0:3], a[184:187], v[252:255]\n" \
"ds_read_b64_tr_b16 a[0:1], v34 offset:9312\n" \
"ds_read_b64_tr_b16 a[2:3], v34 offset:13920\n" \
"buffer_load_dwordx4 v[76:79], v32, s[56:59], s95 offen offset:1024 sc1\n" \
"s_add_u32 s95, s63, 0x0\n" \
"s_waitcnt vmcnt(18)\n" \
"v_mfma_f32_16x16x32_bf16 v[252:255], a[216:219], a[180:183], v[252:255]\n" \
"ds_read_b64_tr_b16 a[216:217], v34 offset:96\n" \
"ds_read_b64_tr_b16 a[218:219], v34 offset:4704\n" \
"buffer_load_dwordx4 v[84:87], v20, s[56:59], s96 offen offset:1024 sc1\n" \
"s_waitcnt vmcnt(18)\n" \
"v_mfma_f32_16x16x32_bf16 v[252:255], a[212:215], a[176:179], v[252:255]\n" \
"ds_read_b64_tr_b16 a[212:213], v33 offset:46176\n" \
"ds_read_b64_tr_b16 a[214:215], v33 offset:50784\n" \
"v_mfma_f32_16x16x32_bf16 v[176:179], a[8:11], a[96:99], v[176:179]\n" \
"buffer_load_dwordx4 v[92:95], v32, s[56:59], s96 offen offset:1024 sc1\n" \
"v_mfma_f32_16x16x32_bf16 v[176:179], a[12:15], a[100:103], v[176:179]\n" \
"s_add_u32 s96, s63, 0x800\n" \
"v_mfma_f32_16x16x32_bf16 v[176:179], a[16:19], a[104:107], v[176:179]\n" \
"v_mfma_f32_16x16x32_bf16 v[176:179], a[20:23], a[108:111], v[176:179]\n" \
"s_waitcnt vmcnt(18)\n" \
"s_mov_b64 exec, 1\n" \
"buffer_store_dword v39, off, s[56:59], s65 offset:0 sc1\n" \
"s_mov_b64 exec, -1\n" \
"v_mfma_f32_16x16x32_bf16 v[180:183], a[20:23], a[124:127], v[180:183]\n" \
"s_add_u32 s65, s63, 0x1800\n" \
"v_mfma_f32_16x16x32_bf16 v[180:183], a[16:19], a[120:123], v[180:183]\n" \
"v_mfma_f32_16x16x32_bf16 v[180:183], a[12:15], a[116:119], v[180:183]\n" \
"v_cvt_pk_bf16_f32 v176, v176, v177\n" \
"v_cvt_pk_bf16_f32 v177, v178, v179\n" \
"v_mfma_f32_16x16x32_bf16 v[180:183], a[8:11], a[112:115], v[180:183]\n" \
"s_waitcnt vmcnt(18)\n" \
"v_mfma_f32_16x16x32_bf16 v[248:251], a[8:11], a[220:223], v[248:251]\n" \
"buffer_load_dwordx4 v[96:99], v20, s[56:59], s97 offen offset:0 sc1\n" \
"s_waitcnt vmcnt(18)\n" \
"v_mfma_f32_16x16x32_bf16 v[248:251], a[12:15], a[224:227], v[248:251]\n" \
"s_waitcnt vmcnt(17)\n" \
"v_mfma_f32_16x16x32_bf16 v[248:251], a[16:19], a[228:231], v[248:251]\n" \
"buffer_load_dwordx4 v[104:107], v32, s[56:59], s97 offen offset:0 sc1\n" \
"v_cvt_pk_bf16_f32 v178, v180, v181\n" \
"v_cvt_pk_bf16_f32 v179, v182, v183\n" \
"s_waitcnt vmcnt(16)\n" \
"v_mfma_f32_16x16x32_bf16 v[248:251], a[20:23], a[232:235], v[248:251]\n" \
"buffer_store_dwordx4 v[176:179], v20, s[56:59], s68 offen offset:0 sc1\n" \
"s_mov_b64 exec, 1\n" \
"buffer_store_dword v39, off, s[56:59], s66 offset:4 sc1\n" \
"s_mov_b64 exec, -1\n" \
"s_add_u32 s66, s63, 0x2000\n" \
"s_waitcnt vmcnt(17)\n" \
"v_mfma_f32_16x16x32_bf16 v[252:255], a[20:23], a[248:251], v[252:255]\n" \
"ds_read_b64_tr_b16 a[20:21], v10 offset:46208\n" \
"v_cvt_pk_bf16_f32 v248, v248, v249\n" \
"v_cvt_pk_bf16_f32 v249, v250, v251\n" \
"s_waitcnt vmcnt(16)\n" \
"v_mfma_f32_16x16x32_bf16 v[252:255], a[16:19], a[244:247], v[252:255]\n" \
"ds_read_b64_tr_b16 a[16:17], v10 offset:36992\n" \
"ds_read_b64_tr_b16 a[18:19], v10 offset:41600\n" \
"ds_read_b64_tr_b16 a[22:23], v10 offset:50816\n" \
"buffer_load_dwordx4 v[100:103], v20, s[56:59], s97 offen offset:1024 sc1\n" \
"s_waitcnt vmcnt(15)\n" \
"v_mfma_f32_16x16x32_bf16 v[252:255], a[12:15], a[240:243], v[252:255]\n" \
"s_mov_b64 exec, 1\n" \
"buffer_store_dword v39, off, s[56:59], s67 offset:8 sc1\n" \
"s_mov_b64 exec, -1\n" \
"ds_read_b64_tr_b16 a[12:13], v10 offset:27776\n" \
"ds_read_b64_tr_b16 a[14:15], v10 offset:32384\n" \
"s_add_u32 s67, s63, 0x2800\n" \
"s_waitcnt vmcnt(15)\n" \
"v_mfma_f32_16x16x32_bf16 v[252:255], a[8:11], a[236:239], v[252:255]\n" \
"ds_read_b64_tr_b16 a[8:9], v33 offset:96\n" \
"ds_read_b64_tr_b16 a[10:11], v33 offset:4704\n" \
"v_mfma_f32_16x16x32_bf16 v[208:211], a[24:27], a[32:35], v[208:211]\n" \
"buffer_load_dwordx4 v[108:111], v32, s[56:59], s97 offen offset:1024 sc1\n" \
"v_mfma_f32_16x16x32_bf16 v[208:211], a[28:31], a[36:39], v[208:211]\n" \
"s_add_u32 s97, s63, 0x1000\n" \
"v_mfma_f32_16x16x32_bf16 v[208:211], a[192:195], a[40:43], v[208:211]\n" \
"s_waitcnt lgkmcnt(4)\n" \
"v_mfma_f32_16x16x32_bf16 v[208:211], a[196:199], a[44:47], v[208:211]\n" \
"v_cvt_pk_bf16_f32 v250, v252, v253\n" \
"v_cvt_pk_bf16_f32 v251, v254, v255\n" \
"v_mfma_f32_16x16x32_bf16 v[212:215], a[196:199], a[60:63], v[212:215]\n" \
"buffer_store_dwordx4 v[248:251], v20, s[56:59], s68 offen offset:1024 sc1\n" \
"v_mfma_f32_16x16x32_bf16 v[212:215], a[192:195], a[56:59], v[212:215]\n" \
"ds_read_b64_tr_b16 a[192:193], v33 offset:27776\n" \
"ds_read_b64_tr_b16 a[194:195], v33 offset:32384\n" \
"v_mfma_f32_16x16x32_bf16 v[212:215], a[28:31], a[52:55], v[212:215]\n" \
"ds_read_b64_tr_b16 a[28:29], v33 offset:18560\n" \
"ds_read_b64_tr_b16 a[30:31], v33 offset:23168\n" \
"v_mfma_f32_16x16x32_bf16 v[212:215], a[24:27], a[48:51], v[212:215]\n" \
"ds_read_b64_tr_b16 a[24:25], v10 offset:55424\n" \
"ds_read_b64_tr_b16 a[26:27], v10 offset:60032\n" \
"v_mfma_f32_16x16x32_bf16 v[136:139], a[200:203], a[128:131], v[136:139]\n" \
"ds_read_b64_tr_b16 a[196:197], v33 offset:36992\n" \
"ds_read_b64_tr_b16 a[198:199], v33 offset:41600\n" \
"v_mfma_f32_16x16x32_bf16 v[136:139], a[204:207], a[132:135], v[136:139]\n" \
"s_add_u32 s68, s63, 0x3000\n" \
"v_mfma_f32_16x16x32_bf16 v[136:139], a[208:211], a[136:139], v[136:139]\n" \
"v_mfma_f32_16x16x32_bf16 v[136:139], a[212:215], a[140:143], v[136:139]\n" \
"v_mfma_f32_16x16x32_bf16 v[140:143], a[212:215], a[156:159], v[140:143]\n" \
"v_mfma_f32_16x16x32_bf16 v[140:143], a[208:211], a[152:155], v[140:143]\n" \
"v_mfma_f32_16x16x32_bf16 v[140:143], a[204:207], a[148:151], v[140:143]\n" \
"v_mfma_f32_16x16x32_bf16 v[140:143], a[200:203], a[144:147], v[140:143]\n" \
"v_mfma_f32_16x16x32_bf16 v[208:211], a[200:203], a[160:163], v[208:211]\n" \
"v_mfma_f32_16x16x32_bf16 v[208:211], a[204:207], a[164:167], v[208:211]\n" \
"v_mfma_f32_16x16x32_bf16 v[208:211], a[208:211], a[168:171], v[208:211]\n" \
"v_mfma_f32_16x16x32_bf16 v[208:211], a[212:215], a[172:175], v[208:211]\n" \
"v_mfma_f32_16x16x32_bf16 v[212:215], a[212:215], a[188:191], v[212:215]\n" \
"ds_read_b64_tr_b16 a[212:213], v34 offset:18560\n" \
"ds_read_b64_tr_b16 a[214:215], v34 offset:23168\n" \
"v_mfma_f32_16x16x32_bf16 v[212:215], a[208:211], a[184:187], v[212:215]\n" \
"ds_read_b64_tr_b16 a[208:209], v34 offset:9344\n" \
"ds_read_b64_tr_b16 a[210:211], v34 offset:13952\n" \
"v_mfma_f32_16x16x32_bf16 v[212:215], a[204:207], a[180:183], v[212:215]\n" \
"ds_read_b64_tr_b16 a[204:205], v34 offset:128\n" \
"ds_read_b64_tr_b16 a[206:207], v34 offset:4736\n" \
"v_mfma_f32_16x16x32_bf16 v[212:215], a[200:203], a[176:179], v[212:215]\n" \
"ds_read_b64_tr_b16 a[200:201], v33 offset:46208\n" \
"ds_read_b64_tr_b16 a[202:203], v33 offset:50816\n" \
"v_mfma_f32_16x16x32_bf16 v[136:139], a[216:219], a[96:99], v[136:139]\n" \
"v_mfma_f32_16x16x32_bf16 v[136:139], a[0:3], a[100:103], v[136:139]\n" \
"v_mfma_f32_16x16x32_bf16 v[136:139], a[4:7], a[104:107], v[136:139]\n" \
"s_waitcnt lgkmcnt(8)\n" \
"v_mfma_f32_16x16x32_bf16 v[136:139], a[8:11], a[108:111], v[136:139]\n" \
"v_mfma_f32_16x16x32_bf16 v[140:143], a[8:11], a[124:127], v[140:143]\n" \
"v_mfma_f32_16x16x32_bf16 v[140:143], a[4:7], a[120:123], v[140:143]\n" \
"v_mfma_f32_16x16x32_bf16 v[140:143], a[0:3], a[116:119], v[140:143]\n" \
"v_mfma_f32_16x16x32_bf16 v[140:143], a[216:219], a[112:115], v[140:143]\n" \
"v_mfma_f32_16x16x32_bf16 v[208:211], a[216:219], a[220:223], v[208:211]\n" \
"v_mfma_f32_16x16x32_bf16 v[208:211], a[0:3], a[224:227], v[208:211]\n" \
"v_mfma_f32_16x16x32_bf16 v[208:211], a[4:7], a[228:231], v[208:211]\n" \
"v_mfma_f32_16x16x32_bf16 v[208:211], a[8:11], a[232:235], v[208:211]\n" \
"v_cvt_pk_bf16_f32 v136, v136, v137\n" \
"v_cvt_pk_bf16_f32 v137, v138, v139\n" \
"v_mfma_f32_16x16x32_bf16 v[212:215], a[8:11], a[248:251], v[212:215]\n" \
"ds_read_b64_tr_b16 a[8:9], v10 offset:46240\n" \
"v_cvt_pk_bf16_f32 v138, v140, v141\n" \
"v_cvt_pk_bf16_f32 v139, v142, v143\n" \
"v_mfma_f32_16x16x32_bf16 v[212:215], a[4:7], a[244:247], v[212:215]\n" \
"buffer_store_dwordx4 v[136:139], v20, s[56:59], s70 offen offset:0 sc1\n" \
"v_cvt_pk_bf16_f32 v208, v208, v209\n" \
"v_cvt_pk_bf16_f32 v209, v210, v211\n" \
"v_mfma_f32_16x16x32_bf16 v[212:215], a[0:3], a[240:243], v[212:215]\n" \
"ds_read_b64_tr_b16 a[0:1], v10 offset:27808\n" \
"ds_read_b64_tr_b16 a[2:3], v10 offset:32416\n" \
"v_mfma_f32_16x16x32_bf16 v[212:215], a[216:219], a[236:239], v[212:215]\n" \
"ds_read_b64_tr_b16 a[216:217], v33 offset:128\n" \
"ds_read_b64_tr_b16 a[218:219], v33 offset:4736\n" \
"v_mfma_f32_16x16x32_bf16 v[216:219], a[12:15], a[32:35], v[216:219]\n" \
"ds_read_b64_tr_b16 a[4:5], v10 offset:37024\n" \
"ds_read_b64_tr_b16 a[6:7], v10 offset:41632\n" \
"v_mfma_f32_16x16x32_bf16 v[216:219], a[16:19], a[36:39], v[216:219]\n" \
"ds_read_b64_tr_b16 a[10:11], v10 offset:50848\n" \
"v_mfma_f32_16x16x32_bf16 v[216:219], a[20:23], a[40:43], v[216:219]\n" \
"v_cvt_pk_bf16_f32 v210, v212, v213\n" \
"v_cvt_pk_bf16_f32 v211, v214, v215\n" \
"v_mfma_f32_16x16x32_bf16 v[216:219], a[24:27], a[44:47], v[216:219]\n" \
"buffer_store_dwordx4 v[208:211], v20, s[56:59], s70 offen offset:1024 sc1\n" \
"s_add_u32 s70, s70, 0x2000\n" \
"v_mfma_f32_16x16x32_bf16 v[220:223], a[24:27], a[60:63], v[220:223]\n" \
"ds_read_b64_tr_b16 a[24:25], v33 offset:37024\n" \
"ds_read_b64_tr_b16 a[26:27], v33 offset:41632\n" \
"v_mfma_f32_16x16x32_bf16 v[220:223], a[20:23], a[56:59], v[220:223]\n" \
"ds_read_b64_tr_b16 a[20:21], v33 offset:27808\n" \
"ds_read_b64_tr_b16 a[22:23], v33 offset:32416\n" \
"v_mfma_f32_16x16x32_bf16 v[220:223], a[16:19], a[52:55], v[220:223]\n" \
"ds_read_b64_tr_b16 a[16:17], v33 offset:18592\n" \
"ds_read_b64_tr_b16 a[18:19], v33 offset:23200\n" \
"v_mfma_f32_16x16x32_bf16 v[220:223], a[12:15], a[48:51], v[220:223]\n" \
"ds_read_b64_tr_b16 a[12:13], v10 offset:55456\n" \
"ds_read_b64_tr_b16 a[14:15], v10 offset:60064\n" \
"v_mfma_f32_16x16x32_bf16 v[144:147], a[28:31], a[128:131], v[144:147]\n" \
"v_mfma_f32_16x16x32_bf16 v[144:147], a[192:195], a[132:135], v[144:147]\n" \
"v_mfma_f32_16x16x32_bf16 v[144:147], a[196:199], a[136:139], v[144:147]\n" \
"s_waitcnt lgkmcnt(15)\n" \
"v_mfma_f32_16x16x32_bf16 v[144:147], a[200:203], a[140:143], v[144:147]\n" \
"v_mfma_f32_16x16x32_bf16 v[148:151], a[200:203], a[156:159], v[148:151]\n" \
"v_mfma_f32_16x16x32_bf16 v[148:151], a[196:199], a[152:155], v[148:151]\n" \
"v_mfma_f32_16x16x32_bf16 v[148:151], a[192:195], a[148:151], v[148:151]\n" \
"v_mfma_f32_16x16x32_bf16 v[148:151], a[28:31], a[144:147], v[148:151]\n" \
"v_mfma_f32_16x16x32_bf16 v[216:219], a[28:31], a[160:163], v[216:219]\n" \
"v_mfma_f32_16x16x32_bf16 v[216:219], a[192:195], a[164:167], v[216:219]\n" \
"v_mfma_f32_16x16x32_bf16 v[216:219], a[196:199], a[168:171], v[216:219]\n" \
"v_mfma_f32_16x16x32_bf16 v[216:219], a[200:203], a[172:175], v[216:219]\n" \
"v_mfma_f32_16x16x32_bf16 v[220:223], a[200:203], a[188:191], v[220:223]\n" \
"ds_read_b64_tr_b16 a[200:201], v34 offset:18592\n" \
"ds_read_b64_tr_b16 a[202:203], v34 offset:23200\n" \
"v_mfma_f32_16x16x32_bf16 v[220:223], a[196:199], a[184:187], v[220:223]\n" \
"ds_read_b64_tr_b16 a[196:197], v34 offset:9376\n" \
"ds_read_b64_tr_b16 a[198:199], v34 offset:13984\n" \
"v_mfma_f32_16x16x32_bf16 v[220:223], a[192:195], a[180:183], v[220:223]\n" \
"ds_read_b64_tr_b16 a[192:193], v34 offset:160\n" \
"ds_read_b64_tr_b16 a[194:195], v34 offset:4768\n" \
"v_mfma_f32_16x16x32_bf16 v[220:223], a[28:31], a[176:179], v[220:223]\n" \
"ds_read_b64_tr_b16 a[28:29], v33 offset:46240\n" \
"ds_read_b64_tr_b16 a[30:31], v33 offset:50848\n" \
"v_mfma_f32_16x16x32_bf16 v[144:147], a[204:207], a[96:99], v[144:147]\n" \
"v_mfma_f32_16x16x32_bf16 v[144:147], a[208:211], a[100:103], v[144:147]\n" \
"v_mfma_f32_16x16x32_bf16 v[144:147], a[212:215], a[104:107], v[144:147]\n" \
"s_waitcnt lgkmcnt(8)\n" \
"v_mfma_f32_16x16x32_bf16 v[144:147], a[216:219], a[108:111], v[144:147]\n" \
"v_mfma_f32_16x16x32_bf16 v[148:151], a[216:219], a[124:127], v[148:151]\n" \
"v_mfma_f32_16x16x32_bf16 v[148:151], a[212:215], a[120:123], v[148:151]\n" \
"v_mfma_f32_16x16x32_bf16 v[148:151], a[208:211], a[116:119], v[148:151]\n" \
"v_mfma_f32_16x16x32_bf16 v[148:151], a[204:207], a[112:115], v[148:151]\n" \
"v_mfma_f32_16x16x32_bf16 v[216:219], a[204:207], a[220:223], v[216:219]\n" \
"v_mfma_f32_16x16x32_bf16 v[216:219], a[208:211], a[224:227], v[216:219]\n" \
"v_mfma_f32_16x16x32_bf16 v[216:219], a[212:215], a[228:231], v[216:219]\n" \
"v_mfma_f32_16x16x32_bf16 v[216:219], a[216:219], a[232:235], v[216:219]\n" \
"v_cvt_pk_bf16_f32 v144, v144, v145\n" \
"v_cvt_pk_bf16_f32 v145, v146, v147\n" \
"v_mfma_f32_16x16x32_bf16 v[220:223], a[216:219], a[248:251], v[220:223]\n" \
"ds_read_b64_tr_b16 a[216:217], v10 offset:46272\n" \
"v_cvt_pk_bf16_f32 v146, v148, v149\n" \
"v_cvt_pk_bf16_f32 v147, v150, v151\n" \
"v_mfma_f32_16x16x32_bf16 v[220:223], a[212:215], a[244:247], v[220:223]\n" \
"buffer_store_dwordx4 v[144:147], v20, s[56:59], s71 offen offset:0 sc1\n" \
"v_cvt_pk_bf16_f32 v216, v216, v217\n" \
"v_cvt_pk_bf16_f32 v217, v218, v219\n" \
"v_mfma_f32_16x16x32_bf16 v[220:223], a[208:211], a[240:243], v[220:223]\n" \
"ds_read_b64_tr_b16 a[208:209], v10 offset:27840\n" \
"ds_read_b64_tr_b16 a[210:211], v10 offset:32448\n" \
"v_mfma_f32_16x16x32_bf16 v[220:223], a[204:207], a[236:239], v[220:223]\n" \
"ds_read_b64_tr_b16 a[204:205], v33 offset:160\n" \
"ds_read_b64_tr_b16 a[206:207], v33 offset:4768\n" \
"v_mfma_f32_16x16x32_bf16 v[224:227], a[0:3], a[32:35], v[224:227]\n" \
"ds_read_b64_tr_b16 a[212:213], v10 offset:37056\n" \
"ds_read_b64_tr_b16 a[214:215], v10 offset:41664\n" \
"v_mfma_f32_16x16x32_bf16 v[224:227], a[4:7], a[36:39], v[224:227]\n" \
"ds_read_b64_tr_b16 a[218:219], v10 offset:50880\n" \
"v_mfma_f32_16x16x32_bf16 v[224:227], a[8:11], a[40:43], v[224:227]\n" \
"v_cvt_pk_bf16_f32 v218, v220, v221\n" \
"v_cvt_pk_bf16_f32 v219, v222, v223\n" \
"v_mfma_f32_16x16x32_bf16 v[224:227], a[12:15], a[44:47], v[224:227]\n" \
"buffer_store_dwordx4 v[216:219], v20, s[56:59], s71 offen offset:1024 sc1\n" \
"s_add_u32 s71, s71, 0x1800\n" \
"v_mfma_f32_16x16x32_bf16 v[228:231], a[12:15], a[60:63], v[228:231]\n" \
"ds_read_b64_tr_b16 a[12:13], v33 offset:37056\n" \
"ds_read_b64_tr_b16 a[14:15], v33 offset:41664\n" \
"v_mfma_f32_16x16x32_bf16 v[228:231], a[8:11], a[56:59], v[228:231]\n" \
"ds_read_b64_tr_b16 a[8:9], v33 offset:27840\n" \
"ds_read_b64_tr_b16 a[10:11], v33 offset:32448\n" \
"v_mfma_f32_16x16x32_bf16 v[228:231], a[4:7], a[52:55], v[228:231]\n" \
"ds_read_b64_tr_b16 a[4:5], v33 offset:18624\n" \
"ds_read_b64_tr_b16 a[6:7], v33 offset:23232\n" \
"v_mfma_f32_16x16x32_bf16 v[228:231], a[0:3], a[48:51], v[228:231]\n" \
"ds_read_b64_tr_b16 a[0:1], v10 offset:55488\n" \
"ds_read_b64_tr_b16 a[2:3], v10 offset:60096\n" \
"v_mfma_f32_16x16x32_bf16 v[152:155], a[16:19], a[128:131], v[152:155]\n" \
"v_mfma_f32_16x16x32_bf16 v[152:155], a[20:23], a[132:135], v[152:155]\n" \
"v_mfma_f32_16x16x32_bf16 v[152:155], a[24:27], a[136:139], v[152:155]\n" \
"s_waitcnt lgkmcnt(15)\n" \
"v_mfma_f32_16x16x32_bf16 v[152:155], a[28:31], a[140:143], v[152:155]\n" \
"v_mfma_f32_16x16x32_bf16 v[156:159], a[28:31], a[156:159], v[156:159]\n" \
"v_mfma_f32_16x16x32_bf16 v[156:159], a[24:27], a[152:155], v[156:159]\n" \
"v_mfma_f32_16x16x32_bf16 v[156:159], a[20:23], a[148:151], v[156:159]\n" \
"v_mfma_f32_16x16x32_bf16 v[156:159], a[16:19], a[144:147], v[156:159]\n" \
"v_mfma_f32_16x16x32_bf16 v[224:227], a[16:19], a[160:163], v[224:227]\n" \
"v_mfma_f32_16x16x32_bf16 v[224:227], a[20:23], a[164:167], v[224:227]\n" \
"s_waitcnt vmcnt(4)\n" \
"s_mov_b64 exec, 3\n" \
"buffer_store_dword v39, v41, s[56:59], s69 offen sc1\n" \
"s_mov_b64 exec, -1\n" \
"v_mfma_f32_16x16x32_bf16 v[224:227], a[24:27], a[168:171], v[224:227]\n" \
"s_add_u32 s69, s63, 0x0\n" \
"v_mfma_f32_16x16x32_bf16 v[224:227], a[28:31], a[172:175], v[224:227]\n" \
"v_mfma_f32_16x16x32_bf16 v[228:231], a[28:31], a[188:191], v[228:231]\n" \
"ds_read_b64_tr_b16 a[28:29], v34 offset:18624\n" \
"ds_read_b64_tr_b16 a[30:31], v34 offset:23232\n" \
"v_mfma_f32_16x16x32_bf16 v[228:231], a[24:27], a[184:187], v[228:231]\n" \
"ds_read_b64_tr_b16 a[24:25], v34 offset:9408\n" \
"ds_read_b64_tr_b16 a[26:27], v34 offset:14016\n" \
"v_mfma_f32_16x16x32_bf16 v[228:231], a[20:23], a[180:183], v[228:231]\n" \
"ds_read_b64_tr_b16 a[20:21], v34 offset:192\n" \
"ds_read_b64_tr_b16 a[22:23], v34 offset:4800\n" \
"v_mfma_f32_16x16x32_bf16 v[228:231], a[16:19], a[176:179], v[228:231]\n" \
"ds_read_b64_tr_b16 a[16:17], v33 offset:46272\n" \
"ds_read_b64_tr_b16 a[18:19], v33 offset:50880\n" \
"v_mfma_f32_16x16x32_bf16 v[152:155], a[192:195], a[96:99], v[152:155]\n" \
"v_mfma_f32_16x16x32_bf16 v[152:155], a[196:199], a[100:103], v[152:155]\n" \
"v_mfma_f32_16x16x32_bf16 v[152:155], a[200:203], a[104:107], v[152:155]\n" \
"s_waitcnt lgkmcnt(8)\n" \
"v_mfma_f32_16x16x32_bf16 v[152:155], a[204:207], a[108:111], v[152:155]\n" \
"v_mfma_f32_16x16x32_bf16 v[156:159], a[204:207], a[124:127], v[156:159]\n" \
"v_mfma_f32_16x16x32_bf16 v[156:159], a[200:203], a[120:123], v[156:159]\n" \
"v_mfma_f32_16x16x32_bf16 v[156:159], a[196:199], a[116:119], v[156:159]\n" \
"v_mfma_f32_16x16x32_bf16 v[156:159], a[192:195], a[112:115], v[156:159]\n" \
"v_mfma_f32_16x16x32_bf16 v[224:227], a[192:195], a[220:223], v[224:227]\n" \
"v_mfma_f32_16x16x32_bf16 v[224:227], a[196:199], a[224:227], v[224:227]\n" \
"v_mfma_f32_16x16x32_bf16 v[224:227], a[200:203], a[228:231], v[224:227]\n" \
"v_mfma_f32_16x16x32_bf16 v[224:227], a[204:207], a[232:235], v[224:227]\n" \
"v_cvt_pk_bf16_f32 v152, v152, v153\n" \
"v_cvt_pk_bf16_f32 v153, v154, v155\n" \
"v_mfma_f32_16x16x32_bf16 v[228:231], a[204:207], a[248:251], v[228:231]\n" \
"ds_read_b64_tr_b16 a[204:205], v10 offset:46080\n" \
"v_cvt_pk_bf16_f32 v154, v156, v157\n" \
"v_cvt_pk_bf16_f32 v155, v158, v159\n" \
"v_mfma_f32_16x16x32_bf16 v[228:231], a[200:203], a[244:247], v[228:231]\n" \
"buffer_store_dwordx4 v[152:155], v20, s[56:59], s72 offen offset:0 sc1\n" \
"v_cvt_pk_bf16_f32 v224, v224, v225\n" \
"v_cvt_pk_bf16_f32 v225, v226, v227\n" \
"v_mfma_f32_16x16x32_bf16 v[228:231], a[196:199], a[240:243], v[228:231]\n" \
"ds_read_b64_tr_b16 a[196:197], v10 offset:27648\n" \
"ds_read_b64_tr_b16 a[198:199], v10 offset:32256\n" \
"v_mfma_f32_16x16x32_bf16 v[228:231], a[192:195], a[236:239], v[228:231]\n" \
"ds_read_b64_tr_b16 a[192:193], v33 offset:192\n" \
"ds_read_b64_tr_b16 a[194:195], v33 offset:4800\n" \
"v_mfma_f32_16x16x32_bf16 v[232:235], a[208:211], a[32:35], v[232:235]\n" \
"ds_read_b64_tr_b16 a[200:201], v10 offset:36864\n" \
"ds_read_b64_tr_b16 a[202:203], v10 offset:41472\n" \
"v_mfma_f32_16x16x32_bf16 v[232:235], a[212:215], a[36:39], v[232:235]\n" \
"ds_read_b64_tr_b16 a[206:207], v10 offset:50688\n" \
"v_mfma_f32_16x16x32_bf16 v[232:235], a[216:219], a[40:43], v[232:235]\n" \
"v_cvt_pk_bf16_f32 v226, v228, v229\n" \
"v_cvt_pk_bf16_f32 v227, v230, v231\n" \
"v_mfma_f32_16x16x32_bf16 v[232:235], a[0:3], a[44:47], v[232:235]\n" \
"buffer_store_dwordx4 v[224:227], v20, s[56:59], s72 offen offset:1024 sc1\n" \
"s_add_u32 s72, s72, 0x1000\n" \
"v_mfma_f32_16x16x32_bf16 v[236:239], a[0:3], a[60:63], v[236:239]\n" \
"ds_read_b64_tr_b16 a[0:1], v33 offset:36864\n" \
"ds_read_b64_tr_b16 a[2:3], v33 offset:41472\n" \
"v_mfma_f32_16x16x32_bf16 v[236:239], a[216:219], a[56:59], v[236:239]\n" \
"ds_read_b64_tr_b16 a[216:217], v33 offset:27648\n" \
"ds_read_b64_tr_b16 a[218:219], v33 offset:32256\n" \
"v_mfma_f32_16x16x32_bf16 v[236:239], a[212:215], a[52:55], v[236:239]\n" \
"ds_read_b64_tr_b16 a[212:213], v33 offset:18432\n" \
"ds_read_b64_tr_b16 a[214:215], v33 offset:23040\n" \
"v_mfma_f32_16x16x32_bf16 v[236:239], a[208:211], a[48:51], v[236:239]\n" \
"ds_read_b64_tr_b16 a[208:209], v10 offset:55296\n" \
"ds_read_b64_tr_b16 a[210:211], v10 offset:59904\n" \
"v_mfma_f32_16x16x32_bf16 v[160:163], a[4:7], a[128:131], v[160:163]\n" \
"v_mfma_f32_16x16x32_bf16 v[160:163], a[8:11], a[132:135], v[160:163]\n" \
"v_mfma_f32_16x16x32_bf16 v[160:163], a[12:15], a[136:139], v[160:163]\n" \
"s_waitcnt lgkmcnt(15)\n" \
"v_mfma_f32_16x16x32_bf16 v[160:163], a[16:19], a[140:143], v[160:163]\n" \
"v_mfma_f32_16x16x32_bf16 v[164:167], a[16:19], a[156:159], v[164:167]\n" \
"v_mfma_f32_16x16x32_bf16 v[164:167], a[12:15], a[152:155], v[164:167]\n" \
"v_mfma_f32_16x16x32_bf16 v[164:167], a[8:11], a[148:151], v[164:167]\n" \
"v_mfma_f32_16x16x32_bf16 v[164:167], a[4:7], a[144:147], v[164:167]\n" \
"s_waitcnt vmcnt(5)\n" \
"s_mov_b64 exec, 1\n" \
"buffer_store_dword v39, off, s[56:59], s70 offset:16 sc1\n" \
"s_mov_b64 exec, -1\n" \
"v_mfma_f32_16x16x32_bf16 v[232:235], a[4:7], a[160:163], v[232:235]\n" \
"s_add_u32 s70, s63, 0x800\n" \
"v_mfma_f32_16x16x32_bf16 v[232:235], a[8:11], a[164:167], v[232:235]\n" \
"v_mfma_f32_16x16x32_bf16 v[232:235], a[12:15], a[168:171], v[232:235]\n" \
"v_mfma_f32_16x16x32_bf16 v[232:235], a[16:19], a[172:175], v[232:235]\n" \
"v_mfma_f32_16x16x32_bf16 v[236:239], a[16:19], a[188:191], v[236:239]\n" \
"ds_read_b64_tr_b16 a[16:17], v34 offset:18432\n" \
"ds_read_b64_tr_b16 a[18:19], v34 offset:23040\n" \
"v_mfma_f32_16x16x32_bf16 v[236:239], a[12:15], a[184:187], v[236:239]\n" \
"ds_read_b64_tr_b16 a[12:13], v34 offset:9216\n" \
"ds_read_b64_tr_b16 a[14:15], v34 offset:13824\n" \
"v_mfma_f32_16x16x32_bf16 v[236:239], a[8:11], a[180:183], v[236:239]\n" \
"ds_read_b64_tr_b16 a[8:9], v34 offset:0\n" \
"ds_read_b64_tr_b16 a[10:11], v34 offset:4608\n" \
"v_mfma_f32_16x16x32_bf16 v[236:239], a[4:7], a[176:179], v[236:239]\n" \
"ds_read_b64_tr_b16 a[4:5], v33 offset:46080\n" \
"ds_read_b64_tr_b16 a[6:7], v33 offset:50688\n" \
"v_mfma_f32_16x16x32_bf16 v[160:163], a[20:23], a[96:99], v[160:163]\n" \
"v_mfma_f32_16x16x32_bf16 v[160:163], a[24:27], a[100:103], v[160:163]\n" \
"v_mfma_f32_16x16x32_bf16 v[160:163], a[28:31], a[104:107], v[160:163]\n" \
"s_waitcnt lgkmcnt(8)\n" \
"v_mfma_f32_16x16x32_bf16 v[160:163], a[192:195], a[108:111], v[160:163]\n" \
"v_mfma_f32_16x16x32_bf16 v[164:167], a[192:195], a[124:127], v[164:167]\n" \
"v_mfma_f32_16x16x32_bf16 v[164:167], a[28:31], a[120:123], v[164:167]\n" \
"v_mfma_f32_16x16x32_bf16 v[164:167], a[24:27], a[116:119], v[164:167]\n" \
"v_mfma_f32_16x16x32_bf16 v[164:167], a[20:23], a[112:115], v[164:167]\n" \
"v_mfma_f32_16x16x32_bf16 v[232:235], a[20:23], a[220:223], v[232:235]\n" \
"v_mfma_f32_16x16x32_bf16 v[232:235], a[24:27], a[224:227], v[232:235]\n" \
"v_mfma_f32_16x16x32_bf16 v[232:235], a[28:31], a[228:231], v[232:235]\n" \
"v_mfma_f32_16x16x32_bf16 v[232:235], a[192:195], a[232:235], v[232:235]\n" \
"v_cvt_pk_bf16_f32 v160, v160, v161\n" \
"v_cvt_pk_bf16_f32 v161, v162, v163\n" \
"v_mfma_f32_16x16x32_bf16 v[236:239], a[192:195], a[248:251], v[236:239]\n" \
"ds_read_b64_tr_b16 a[192:193], v10 offset:46112\n" \
"v_cvt_pk_bf16_f32 v162, v164, v165\n" \
"v_cvt_pk_bf16_f32 v163, v166, v167\n" \
"v_mfma_f32_16x16x32_bf16 v[236:239], a[28:31], a[244:247], v[236:239]\n" \
"buffer_store_dwordx4 v[160:163], v20, s[56:59], s73 offen offset:0 sc1\n" \
"v_cvt_pk_bf16_f32 v232, v232, v233\n" \
"v_cvt_pk_bf16_f32 v233, v234, v235\n" \
"v_mfma_f32_16x16x32_bf16 v[236:239], a[24:27], a[240:243], v[236:239]\n" \
"ds_read_b64_tr_b16 a[24:25], v10 offset:27680\n" \
"ds_read_b64_tr_b16 a[26:27], v10 offset:32288\n" \
"v_mfma_f32_16x16x32_bf16 v[236:239], a[20:23], a[236:239], v[236:239]\n" \
"ds_read_b64_tr_b16 a[20:21], v33 offset:0\n" \
"ds_read_b64_tr_b16 a[22:23], v33 offset:4608\n" \
"v_mfma_f32_16x16x32_bf16 v[184:187], a[196:199], a[32:35], v[184:187]\n" \
"ds_read_b64_tr_b16 a[28:29], v10 offset:36896\n" \
"ds_read_b64_tr_b16 a[30:31], v10 offset:41504\n" \
"v_mfma_f32_16x16x32_bf16 v[184:187], a[200:203], a[36:39], v[184:187]\n" \
"ds_read_b64_tr_b16 a[194:195], v10 offset:50720\n" \
"v_mfma_f32_16x16x32_bf16 v[184:187], a[204:207], a[40:43], v[184:187]\n" \
"v_cvt_pk_bf16_f32 v234, v236, v237\n" \
"v_cvt_pk_bf16_f32 v235, v238, v239\n" \
"v_mfma_f32_16x16x32_bf16 v[184:187], a[208:211], a[44:47], v[184:187]\n" \
"buffer_store_dwordx4 v[232:235], v20, s[56:59], s73 offen offset:1024 sc1\n" \
"s_add_u32 s73, s73, 0x800\n" \
"v_mfma_f32_16x16x32_bf16 v[188:191], a[208:211], a[60:63], v[188:191]\n" \
"ds_read_b64_tr_b16 a[208:209], v33 offset:36896\n" \
"ds_read_b64_tr_b16 a[210:211], v33 offset:41504\n" \
"v_mfma_f32_16x16x32_bf16 v[188:191], a[204:207], a[56:59], v[188:191]\n" \
"ds_read_b64_tr_b16 a[204:205], v33 offset:27680\n" \
"ds_read_b64_tr_b16 a[206:207], v33 offset:32288\n" \
"v_mfma_f32_16x16x32_bf16 v[188:191], a[200:203], a[52:55], v[188:191]\n" \
"ds_read_b64_tr_b16 a[200:201], v33 offset:18464\n" \
"ds_read_b64_tr_b16 a[202:203], v33 offset:23072\n" \
"v_mfma_f32_16x16x32_bf16 v[188:191], a[196:199], a[48:51], v[188:191]\n" \
"ds_read_b64_tr_b16 a[196:197], v10 offset:55328\n" \
"ds_read_b64_tr_b16 a[198:199], v10 offset:59936\n" \
"v_mfma_f32_16x16x32_bf16 v[184:187], a[212:215], a[160:163], v[184:187]\n" \
"v_mfma_f32_16x16x32_bf16 v[184:187], a[216:219], a[164:167], v[184:187]\n" \
"v_mfma_f32_16x16x32_bf16 v[184:187], a[0:3], a[168:171], v[184:187]\n" \
"s_waitcnt lgkmcnt(15)\n" \
"v_mfma_f32_16x16x32_bf16 v[184:187], a[4:7], a[172:175], v[184:187]\n" \
"v_mfma_f32_16x16x32_bf16 v[188:191], a[4:7], a[188:191], v[188:191]\n" \
"ds_read_b64_tr_b16 a[4:5], v34 offset:18464\n" \
"ds_read_b64_tr_b16 a[6:7], v34 offset:23072\n" \
"v_mfma_f32_16x16x32_bf16 v[188:191], a[0:3], a[184:187], v[188:191]\n" \
"ds_read_b64_tr_b16 a[0:1], v34 offset:9248\n" \
"ds_read_b64_tr_b16 a[2:3], v34 offset:13856\n" \
"s_waitcnt vmcnt(6)\n" \
"s_mov_b64 exec, 1\n" \
"buffer_store_dword v39, off, s[56:59], s71 offset:20 sc1\n" \
"s_mov_b64 exec, -1\n" \
"v_mfma_f32_16x16x32_bf16 v[188:191], a[216:219], a[180:183], v[188:191]\n" \
"ds_read_b64_tr_b16 a[216:217], v34 offset:32\n" \
"ds_read_b64_tr_b16 a[218:219], v34 offset:4640\n" \
"v_mfma_f32_16x16x32_bf16 v[188:191], a[212:215], a[176:179], v[188:191]\n" \
"ds_read_b64_tr_b16 a[212:213], v33 offset:46112\n" \
"ds_read_b64_tr_b16 a[214:215], v33 offset:50720\n" \
"v_mfma_f32_16x16x32_bf16 v[184:187], a[8:11], a[220:223], v[184:187]\n" \
"s_add_u32 s71, s63, 0x1000\n" \
"v_mfma_f32_16x16x32_bf16 v[184:187], a[12:15], a[224:227], v[184:187]\n" \
"v_mfma_f32_16x16x32_bf16 v[184:187], a[16:19], a[228:231], v[184:187]\n" \
"s_waitcnt lgkmcnt(12)\n" \
"v_mfma_f32_16x16x32_bf16 v[184:187], a[20:23], a[232:235], v[184:187]\n" \
"v_mfma_f32_16x16x32_bf16 v[188:191], a[20:23], a[248:251], v[188:191]\n" \
"ds_read_b64_tr_b16 a[20:21], v10 offset:46144\n" \
"ds_read_b64_tr_b16 a[22:23], v10 offset:50752\n" \
"v_mfma_f32_16x16x32_bf16 v[188:191], a[16:19], a[244:247], v[188:191]\n" \
"ds_read_b64_tr_b16 a[16:17], v10 offset:36928\n" \
"ds_read_b64_tr_b16 a[18:19], v10 offset:41536\n" \
"v_mfma_f32_16x16x32_bf16 v[188:191], a[12:15], a[240:243], v[188:191]\n" \
"ds_read_b64_tr_b16 a[12:13], v10 offset:27712\n" \
"v_cvt_pk_bf16_f32 v184, v184, v185\n" \
"v_cvt_pk_bf16_f32 v185, v186, v187\n" \
"v_mfma_f32_16x16x32_bf16 v[188:191], a[8:11], a[236:239], v[188:191]\n" \
"ds_read_b64_tr_b16 a[8:9], v33 offset:32\n" \
"ds_read_b64_tr_b16 a[10:11], v33 offset:4640\n" \
"v_mfma_f32_16x16x32_bf16 v[192:195], a[24:27], a[32:35], v[192:195]\n" \
"ds_read_b64_tr_b16 a[14:15], v10 offset:32320\n" \
"v_mfma_f32_16x16x32_bf16 v[192:195], a[28:31], a[36:39], v[192:195]\n" \
"v_mfma_f32_16x16x32_bf16 v[192:195], a[192:195], a[40:43], v[192:195]\n" \
"s_waitcnt lgkmcnt(12)\n" \
"v_mfma_f32_16x16x32_bf16 v[192:195], a[196:199], a[44:47], v[192:195]\n" \
"v_cvt_pk_bf16_f32 v186, v188, v189\n" \
"v_cvt_pk_bf16_f32 v187, v190, v191\n" \
"v_mfma_f32_16x16x32_bf16 v[196:199], a[196:199], a[60:63], v[196:199]\n" \
"buffer_store_dwordx4 v[184:187], v20, s[56:59], s92 offen offset:1024 sc1\n" \
"s_add_u32 s92, s92, 0x3800\n" \
"v_mfma_f32_16x16x32_bf16 v[196:199], a[192:195], a[56:59], v[196:199]\n" \
"ds_read_b64_tr_b16 a[192:193], v33 offset:27712\n" \
"ds_read_b64_tr_b16 a[194:195], v33 offset:32320\n" \
"v_mfma_f32_16x16x32_bf16 v[196:199], a[28:31], a[52:55], v[196:199]\n" \
"ds_read_b64_tr_b16 a[28:29], v33 offset:18496\n" \
"ds_read_b64_tr_b16 a[30:31], v33 offset:23104\n" \
"v_mfma_f32_16x16x32_bf16 v[196:199], a[24:27], a[48:51], v[196:199]\n" \
"ds_read_b64_tr_b16 a[24:25], v10 offset:55360\n" \
"L_pf22:\n" \
"s_cbranch_execz L_retB\n" \
"s_nop 0\n" \
"ds_read_b64_tr_b16 a[26:27], v10 offset:59968\n" \
"v_mfma_f32_16x16x32_bf16 v[192:195], a[200:203], a[160:163], v[192:195]\n" \
"ds_read_b64_tr_b16 a[196:197], v33 offset:36928\n" \
"ds_read_b64_tr_b16 a[198:199], v33 offset:41536\n" \
"v_mfma_f32_16x16x32_bf16 v[192:195], a[204:207], a[164:167], v[192:195]\n" \
"v_mfma_f32_16x16x32_bf16 v[192:195], a[208:211], a[168:171], v[192:195]\n" \
"s_waitcnt lgkmcnt(14)\n" \
"v_mfma_f32_16x16x32_bf16 v[192:195], a[212:215], a[172:175], v[192:195]\n" \
"v_mfma_f32_16x16x32_bf16 v[196:199], a[212:215], a[188:191], v[196:199]\n" \
"ds_read_b64_tr_b16 a[212:213], v34 offset:18496\n" \
"ds_read_b64_tr_b16 a[214:215], v34 offset:23104\n" \
"v_mfma_f32_16x16x32_bf16 v[196:199], a[208:211], a[184:187], v[196:199]\n" \
"ds_read_b64_tr_b16 a[208:209], v34 offset:9280\n" \
"ds_read_b64_tr_b16 a[210:211], v34 offset:13888\n" \
"v_mfma_f32_16x16x32_bf16 v[196:199], a[204:207], a[180:183], v[196:199]\n" \
"ds_read_b64_tr_b16 a[204:205], v34 offset:64\n" \
"ds_read_b64_tr_b16 a[206:207], v34 offset:4672\n" \
"v_mfma_f32_16x16x32_bf16 v[196:199], a[200:203], a[176:179], v[196:199]\n" \
"ds_read_b64_tr_b16 a[200:201], v33 offset:46144\n" \
"ds_read_b64_tr_b16 a[202:203], v33 offset:50752\n" \
"v_mfma_f32_16x16x32_bf16 v[192:195], a[216:219], a[220:223], v[192:195]\n" \
"v_mfma_f32_16x16x32_bf16 v[192:195], a[0:3], a[224:227], v[192:195]\n" \
"v_mfma_f32_16x16x32_bf16 v[192:195], a[4:7], a[228:231], v[192:195]\n" \
"s_waitcnt lgkmcnt(15)\n" \
"v_mfma_f32_16x16x32_bf16 v[192:195], a[8:11], a[232:235], v[192:195]\n" \
"v_mfma_f32_16x16x32_bf16 v[196:199], a[8:11], a[248:251], v[196:199]\n" \
"ds_read_b64_tr_b16 a[8:9], v10 offset:46304\n" \
"ds_read_b64_tr_b16 a[10:11], v10 offset:50912\n" \
"v_mfma_f32_16x16x32_bf16 v[196:199], a[4:7], a[244:247], v[196:199]\n" \
"ds_read_b64_tr_b16 a[4:5], v10 offset:37088\n" \
"ds_read_b64_tr_b16 a[6:7], v10 offset:41696\n" \
"v_mfma_f32_16x16x32_bf16 v[196:199], a[0:3], a[240:243], v[196:199]\n" \
"ds_read_b64_tr_b16 a[0:1], v10 offset:27872\n" \
"v_cvt_pk_bf16_f32 v192, v192, v193\n" \
"v_cvt_pk_bf16_f32 v193, v194, v195\n" \
"v_mfma_f32_16x16x32_bf16 v[196:199], a[216:219], a[236:239], v[196:199]\n" \
"ds_read_b64_tr_b16 a[216:217], v33 offset:64\n" \
"ds_read_b64_tr_b16 a[218:219], v33 offset:4672\n" \
"v_mfma_f32_16x16x32_bf16 v[200:203], a[12:15], a[32:35], v[200:203]\n" \
"ds_read_b64_tr_b16 a[2:3], v10 offset:32480\n" \
"v_mfma_f32_16x16x32_bf16 v[200:203], a[16:19], a[36:39], v[200:203]\n" \
"v_mfma_f32_16x16x32_bf16 v[200:203], a[20:23], a[40:43], v[200:203]\n" \
"s_waitcnt lgkmcnt(14)\n" \
"v_mfma_f32_16x16x32_bf16 v[200:203], a[24:27], a[44:47], v[200:203]\n" \
"v_cvt_pk_bf16_f32 v194, v196, v197\n" \
"v_cvt_pk_bf16_f32 v195, v198, v199\n" \
"v_mfma_f32_16x16x32_bf16 v[204:207], a[24:27], a[60:63], v[204:207]\n" \
"buffer_store_dwordx4 v[192:195], v20, s[56:59], s93 offen offset:1024 sc1\n" \
"s_add_u32 s93, s93, 0x3000\n" \
"v_mfma_f32_16x16x32_bf16 v[204:207], a[20:23], a[56:59], v[204:207]\n" \
"ds_read_b64_tr_b16 a[20:21], v33 offset:27872\n" \
"ds_read_b64_tr_b16 a[22:23], v33 offset:32480\n" \
"s_waitcnt vmcnt(6)\n" \
"s_mov_b64 exec, 1\n" \
"buffer_store_dword v39, off, s[56:59], s72 offset:24 sc1\n" \
"s_mov_b64 exec, -1\n" \
"v_readlane_b32 s72, v11, 50\n" \
"v_mfma_f32_16x16x32_bf16 v[204:207], a[16:19], a[52:55], v[204:207]\n" \
"ds_read_b64_tr_b16 a[16:17], v33 offset:18656\n" \
"ds_read_b64_tr_b16 a[18:19], v33 offset:23264\n" \
"s_mov_b64 exec, 1\n" \
"s_nop 0\n" \
"buffer_store_dword v46, off, s[56:59], s72 offset:0 sc1\n" \
"s_mov_b64 exec, -1\n" \
"v_mfma_f32_16x16x32_bf16 v[204:207], a[12:15], a[48:51], v[204:207]\n" \
"ds_read_b64_tr_b16 a[12:13], v10 offset:55520\n" \
"ds_read_b64_tr_b16 a[14:15], v10 offset:60128\n" \
"v_mfma_f32_16x16x32_bf16 v[200:203], a[28:31], a[160:163], v[200:203]\n" \
"ds_read_b64_tr_b16 a[24:25], v33 offset:37088\n" \
"ds_read_b64_tr_b16 a[26:27], v33 offset:41696\n" \
"v_mfma_f32_16x16x32_bf16 v[200:203], a[192:195], a[164:167], v[200:203]\n" \
"v_mfma_f32_16x16x32_bf16 v[200:203], a[196:199], a[168:171], v[200:203]\n" \
"s_waitcnt lgkmcnt(10)\n" \
"v_mfma_f32_16x16x32_bf16 v[200:203], a[200:203], a[172:175], v[200:203]\n" \
"v_mfma_f32_16x16x32_bf16 v[204:207], a[200:203], a[188:191], v[204:207]\n" \
"ds_read_b64_tr_b16 a[200:201], v34 offset:18656\n" \
"ds_read_b64_tr_b16 a[202:203], v34 offset:23264\n" \
"v_mfma_f32_16x16x32_bf16 v[204:207], a[196:199], a[184:187], v[204:207]\n" \
"ds_read_b64_tr_b16 a[196:197], v34 offset:9440\n" \
"ds_read_b64_tr_b16 a[198:199], v34 offset:14048\n" \
"v_mfma_f32_16x16x32_bf16 v[204:207], a[192:195], a[180:183], v[204:207]\n" \
"ds_read_b64_tr_b16 a[192:193], v34 offset:224\n" \
"ds_read_b64_tr_b16 a[194:195], v34 offset:4832\n" \
"v_mfma_f32_16x16x32_bf16 v[204:207], a[28:31], a[176:179], v[204:207]\n" \
"ds_read_b64_tr_b16 a[28:29], v33 offset:46304\n" \
"ds_read_b64_tr_b16 a[30:31], v33 offset:50912\n" \
"v_mfma_f32_16x16x32_bf16 v[200:203], a[204:207], a[220:223], v[200:203]\n" \
"v_mfma_f32_16x16x32_bf16 v[200:203], a[208:211], a[224:227], v[200:203]\n" \
"v_mfma_f32_16x16x32_bf16 v[200:203], a[212:215], a[228:231], v[200:203]\n" \
"s_waitcnt lgkmcnt(14)\n" \
"v_mfma_f32_16x16x32_bf16 v[200:203], a[216:219], a[232:235], v[200:203]\n" \
"v_mfma_f32_16x16x32_bf16 v[204:207], a[216:219], a[248:251], v[204:207]\n" \
"ds_read_b64_tr_b16 a[216:217], v36 offset:32\n" \
"ds_read_b64_tr_b16 a[218:219], v36 offset:1056\n" \
"v_mfma_f32_16x16x32_bf16 v[204:207], a[212:215], a[244:247], v[204:207]\n" \
"ds_read_b64_tr_b16 a[212:213], v36 offset:2048\n" \
"ds_read_b64_tr_b16 a[214:215], v36 offset:3072\n" \
"v_mfma_f32_16x16x32_bf16 v[204:207], a[208:211], a[240:243], v[204:207]\n" \
"ds_read_b64_tr_b16 a[208:209], v36 offset:0\n" \
"v_cvt_pk_bf16_f32 v200, v200, v201\n" \
"v_cvt_pk_bf16_f32 v201, v202, v203\n" \
"v_mfma_f32_16x16x32_bf16 v[204:207], a[204:207], a[236:239], v[204:207]\n" \
"ds_read_b64_tr_b16 a[204:205], v33 offset:224\n" \
"ds_read_b64_tr_b16 a[206:207], v33 offset:4832\n" \
"v_mfma_f32_16x16x32_bf16 v[240:243], a[0:3], a[32:35], v[240:243]\n" \
"ds_read_b64_tr_b16 a[210:211], v36 offset:1024\n" \
"v_mfma_f32_16x16x32_bf16 v[240:243], a[4:7], a[36:39], v[240:243]\n" \
"v_mfma_f32_16x16x32_bf16 v[240:243], a[8:11], a[40:43], v[240:243]\n" \
"s_waitcnt lgkmcnt(14)\n" \
"v_mfma_f32_16x16x32_bf16 v[240:243], a[12:15], a[44:47], v[240:243]\n" \
"v_cvt_pk_bf16_f32 v202, v204, v205\n" \
"v_cvt_pk_bf16_f32 v203, v206, v207\n" \
"v_mfma_f32_16x16x32_bf16 v[244:247], a[12:15], a[60:63], v[244:247]\n" \
"buffer_store_dwordx4 v[200:203], v20, s[56:59], s94 offen offset:1024 sc1\n" \
"s_add_u32 s94, s94, 0x2800\n" \
"v_mfma_f32_16x16x32_bf16 v[244:247], a[8:11], a[56:59], v[244:247]\n" \
"v_mfma_f32_16x16x32_bf16 v[244:247], a[4:7], a[52:55], v[244:247]\n" \
"v_mfma_f32_16x16x32_bf16 v[244:247], a[0:3], a[48:51], v[244:247]\n" \
"ds_read_b64_tr_b16 a[0:1], v36 offset:2080\n" \
"ds_read_b64_tr_b16 a[2:3], v36 offset:3104\n" \
"v_mfma_f32_16x16x32_bf16 v[168:171], a[16:19], a[128:131], v[168:171]\n" \
"v_mfma_f32_16x16x32_bf16 v[168:171], a[20:23], a[132:135], v[168:171]\n" \
"v_mfma_f32_16x16x32_bf16 v[168:171], a[24:27], a[136:139], v[168:171]\n" \
"s_waitcnt lgkmcnt(8)\n" \
"v_mfma_f32_16x16x32_bf16 v[168:171], a[28:31], a[140:143], v[168:171]\n" \
"v_mfma_f32_16x16x32_bf16 v[172:175], a[28:31], a[156:159], v[172:175]\n" \
"v_mfma_f32_16x16x32_bf16 v[172:175], a[24:27], a[152:155], v[172:175]\n" \
"v_mfma_f32_16x16x32_bf16 v[172:175], a[20:23], a[148:151], v[172:175]\n" \
"v_mfma_f32_16x16x32_bf16 v[172:175], a[16:19], a[144:147], v[172:175]\n" \
"v_mfma_f32_16x16x32_bf16 v[240:243], a[16:19], a[160:163], v[240:243]\n" \
"v_mfma_f32_16x16x32_bf16 v[240:243], a[20:23], a[164:167], v[240:243]\n" \
"v_mfma_f32_16x16x32_bf16 v[240:243], a[24:27], a[168:171], v[240:243]\n" \
"v_mfma_f32_16x16x32_bf16 v[240:243], a[28:31], a[172:175], v[240:243]\n" \
"v_mfma_f32_16x16x32_bf16 v[244:247], a[28:31], a[188:191], v[244:247]\n" \
"s_waitcnt vmcnt(6)\n" \
"s_mov_b64 exec, 1\n" \
"buffer_store_dword v39, off, s[56:59], s73 offset:28 sc1\n" \
"s_mov_b64 exec, -1\n" \
"v_mfma_f32_16x16x32_bf16 v[244:247], a[24:27], a[184:187], v[244:247]\n" \
"v_readlane_b32 s73, v11, 41\n" \
"v_mfma_f32_16x16x32_bf16 v[244:247], a[20:23], a[180:183], v[244:247]\n" \
"v_mfma_f32_16x16x32_bf16 v[244:247], a[16:19], a[176:179], v[244:247]\n" \
"v_mfma_f32_16x16x32_bf16 v[168:171], a[192:195], a[96:99], v[168:171]\n" \
"v_mfma_f32_16x16x32_bf16 v[168:171], a[196:199], a[100:103], v[168:171]\n" \
"v_mfma_f32_16x16x32_bf16 v[168:171], a[200:203], a[104:107], v[168:171]\n" \
"s_waitcnt lgkmcnt(0)\n" \
"v_mfma_f32_16x16x32_bf16 v[168:171], a[204:207], a[108:111], v[168:171]\n" \
"v_mfma_f32_16x16x32_bf16 v[172:175], a[204:207], a[124:127], v[172:175]\n" \
"v_mfma_f32_16x16x32_bf16 v[172:175], a[200:203], a[120:123], v[172:175]\n" \
"v_mfma_f32_16x16x32_bf16 v[172:175], a[196:199], a[116:119], v[172:175]\n" \
"v_mfma_f32_16x16x32_bf16 v[172:175], a[192:195], a[112:115], v[172:175]\n" \
"v_mfma_f32_16x16x32_bf16 v[240:243], a[192:195], a[220:223], v[240:243]\n" \
"v_mfma_f32_16x16x32_bf16 v[240:243], a[196:199], a[224:227], v[240:243]\n" \
"v_mfma_f32_16x16x32_bf16 v[240:243], a[200:203], a[228:231], v[240:243]\n" \
"v_mfma_f32_16x16x32_bf16 v[240:243], a[204:207], a[232:235], v[240:243]\n" \
"v_mfma_f32_16x16x32_bf16 v[244:247], a[204:207], a[248:251], v[244:247]\n" \
"v_mfma_f32_16x16x32_bf16 v[244:247], a[200:203], a[244:247], v[244:247]\n" \
"v_mfma_f32_16x16x32_bf16 v[244:247], a[196:199], a[240:243], v[244:247]\n" \
"v_mfma_f32_16x16x32_bf16 v[244:247], a[192:195], a[236:239], v[244:247]\n" \
"v_mfma_f32_16x16x32_bf16 v[168:171], a[208:211], v[64:67], v[168:171]\n" \
"v_mfma_f32_16x16x32_bf16 v[172:175], a[208:211], v[72:75], v[172:175]\n" \
"v_mfma_f32_16x16x32_bf16 v[240:243], a[208:211], v[80:83], v[240:243]\n" \
"v_mfma_f32_16x16x32_bf16 v[244:247], a[208:211], v[88:91], v[244:247]\n" \
"v_mfma_f32_16x16x32_bf16 v[168:171], a[212:215], v[68:71], v[168:171]\n" \
"v_mfma_f32_16x16x32_bf16 v[172:175], a[212:215], v[76:79], v[172:175]\n" \
"v_mfma_f32_16x16x32_bf16 v[240:243], a[212:215], v[84:87], v[240:243]\n" \
"s_waitcnt vmcnt(5)\n" \
"s_mov_b64 exec, 1\n" \
"buffer_store_dword v39, off, s[56:59], s92 offset:32 sc1\n" \
"s_mov_b64 exec, -1\n" \
"v_mfma_f32_16x16x32_bf16 v[244:247], a[212:215], v[92:95], v[244:247]\n" \
"v_readlane_b32 s92, v11, 39\n" \
"v_mfma_f32_16x16x32_bf16 v[56:59], a[216:219], v[96:99], 0\n" \
"v_mfma_f32_16x16x32_bf16 v[60:63], a[216:219], v[104:107], 0\n" \
"v_mfma_f32_16x16x32_bf16 v[56:59], a[0:3], v[100:103], v[56:59]\n" \
"v_mfma_f32_16x16x32_bf16 v[60:63], a[0:3], v[108:111], v[60:63]\n" \
"s_waitcnt vmcnt(5)\n" \
"s_mov_b64 exec, 1\n" \
"buffer_store_dword v39, off, s[56:59], s93 offset:36 sc1\n" \
"s_mov_b64 exec, -1\n" \
"v_readlane_b32 s93, v11, 40\n" \
"s_waitcnt vmcnt(3)\n" \
"s_mov_b64 exec, 1\n" \
"buffer_store_dword v39, off, s[56:59], s94 offset:40 sc1\n" \
"s_mov_b64 exec, -1\n" \
"buffer_load_dword v37, v40, s[56:59], s74 offen sc1\n" \
"s_waitcnt vmcnt(0)\n" \
"v_cmp_eq_u32_e64 s[98:99], s62, v37\n" \
"s_nop 0\n" \
"s_andn2_b32 s75, 0x1, s98\n" \
"s_cbranch_scc1 L_sx0_%=\n" \
"L_rx0_%=:\n" \
"buffer_load_dwordx4 v[116:119], v20, s[56:59], s95 offen offset:0 sc1\n" \
"s_andn2_b32 s75, 0x2, s98\n" \
"s_cbranch_scc1 L_sx1_%=\n" \
"L_rx1_%=:\n" \
"buffer_load_dwordx4 v[124:127], v20, s[56:59], s96 offen offset:0 sc1\n" \
"s_andn2_b32 s75, 0x4, s98\n" \
"s_cbranch_scc1 L_sx2_%=\n" \
"L_rx2_%=:\n" \
"buffer_load_dwordx4 v[132:135], v20, s[56:59], s97 offen offset:0 sc1\n" \
"s_andn2_b32 s75, s100, s98\n" \
"s_cbranch_scc1 L_sx3_%=\n" \
"L_rx3_%=:\n" \
"buffer_load_dwordx4 v[136:139], v20, s[56:59], s64 offen offset:0 sc1\n" \
"buffer_load_dwordx4 v[208:211], v20, s[56:59], s64 offen offset:2048 sc1\n" \
"s_add_u32 s64, s64, 0x1000\n" \
"buffer_load_dwordx4 v[144:147], v20, s[56:59], s64 offen offset:0 sc1\n" \
"buffer_load_dwordx4 v[216:219], v20, s[56:59], s64 offen offset:2048 sc1\n" \
"s_add_u32 s64, s64, 0x1000\n" \
"buffer_load_dwordx4 v[152:155], v20, s[56:59], s64 offen offset:0 sc1\n" \
"buffer_load_dwordx4 v[224:227], v20, s[56:59], s64 offen offset:2048 sc1\n" \
"s_add_u32 s64, s64, 0x1000\n" \
"buffer_load_dwordx4 v[176:179], v20, s[56:59], s64 offen offset:0 sc1\n" \
"buffer_load_dwordx4 v[248:251], v20, s[56:59], s64 offen offset:2048 sc1\n" \
"s_andn2_b32 s75, 0x10, s98\n" \
"s_cbranch_scc1 L_sx4_%=\n" \
"L_rx4_%=:\n" \
"buffer_load_dwordx4 v[140:143], v20, s[56:59], s65 offen offset:0 sc1\n" \
"buffer_load_dwordx4 v[212:215], v20, s[56:59], s65 offen offset:1024 sc1\n" \
"s_andn2_b32 s75, 0x20, s98\n" \
"s_cbranch_scc1 L_sx5_%=\n" \
"L_rx5_%=:\n" \
"buffer_load_dwordx4 v[148:151], v20, s[56:59], s66 offen offset:0 sc1\n" \
"buffer_load_dwordx4 v[220:223], v20, s[56:59], s66 offen offset:1024 sc1\n" \
"s_andn2_b32 s75, 0x40, s98\n" \
"s_cbranch_scc1 L_sx6_%=\n" \
"L_rx6_%=:\n" \
"buffer_load_dwordx4 v[156:159], v20, s[56:59], s67 offen offset:0 sc1\n" \
"buffer_load_dwordx4 v[228:231], v20, s[56:59], s67 offen offset:1024 sc1\n" \
"s_andn2_b32 s75, 0x80, s98\n" \
"s_cbranch_scc1 L_sx7_%=\n" \
"L_rx7_%=:\n" \
"buffer_load_dwordx4 v[164:167], v20, s[56:59], s68 offen offset:0 sc1\n" \
"buffer_load_dwordx4 v[236:239], v20, s[56:59], s68 offen offset:1024 sc1\n" \
"s_andn2_b32 s75, 0x100, s98\n" \
"s_cbranch_scc1 L_sx8_%=\n" \
"L_rx8_%=:\n" \
"buffer_load_dwordx4 v[188:191], v20, s[56:59], s69 offen offset:1024 sc1\n" \
"s_andn2_b32 s75, 0x200, s98\n" \
"s_cbranch_scc1 L_sx9_%=\n" \
"L_rx9_%=:\n" \
"buffer_load_dwordx4 v[196:199], v20, s[56:59], s70 offen offset:1024 sc1\n" \
"s_andn2_b32 s75, 0x400, s98\n" \
"s_cbranch_scc1 L_sx10_%=\n" \
"L_rx10_%=:\n" \
"buffer_load_dwordx4 v[204:207], v20, s[56:59], s71 offen offset:1024 sc1\n" \
"s_waitcnt vmcnt(21)\n" \
"v_mfma_f32_16x16x32_bf16 v[168:171], v[50:53], v[116:119], v[168:171]\n" \
"v_mfma_f32_16x16x32_bf16 v[172:175], v[48:51], v[116:119], v[172:175]\n" \
"s_waitcnt vmcnt(20)\n" \
"v_mfma_f32_16x16x32_bf16 v[168:171], v[50:53], v[124:127], v[168:171]\n" \
"v_mfma_f32_16x16x32_bf16 v[172:175], v[48:51], v[124:127], v[172:175]\n" \
"s_waitcnt vmcnt(19)\n" \
"v_mfma_f32_16x16x32_bf16 v[168:171], v[50:53], v[132:135], v[168:171]\n" \
"v_mfma_f32_16x16x32_bf16 v[172:175], v[48:51], v[132:135], v[172:175]\n" \
"s_waitcnt vmcnt(18)\n" \
"v_mfma_f32_16x16x32_bf16 v[56:59], v[50:53], v[136:139], v[56:59]\n" \
"v_mfma_f32_16x16x32_bf16 v[60:63], v[48:51], v[136:139], v[60:63]\n" \
"s_waitcnt vmcnt(17)\n" \
"v_mfma_f32_16x16x32_bf16 v[56:59], v[50:53], v[208:211], v[56:59]\n" \
"v_mfma_f32_16x16x32_bf16 v[60:63], v[48:51], v[208:211], v[60:63]\n" \
"s_waitcnt vmcnt(16)\n" \
"v_mfma_f32_16x16x32_bf16 v[56:59], v[50:53], v[144:147], v[56:59]\n" \
"v_mfma_f32_16x16x32_bf16 v[60:63], v[48:51], v[144:147], v[60:63]\n" \
"s_waitcnt vmcnt(15)\n" \
"v_mfma_f32_16x16x32_bf16 v[56:59], v[50:53], v[216:219], v[56:59]\n" \
"v_mfma_f32_16x16x32_bf16 v[60:63], v[48:51], v[216:219], v[60:63]\n" \
"s_waitcnt vmcnt(14)\n" \
"v_mfma_f32_16x16x32_bf16 v[56:59], v[50:53], v[152:155], v[56:59]\n" \
"v_mfma_f32_16x16x32_bf16 v[60:63], v[48:51], v[152:155], v[60:63]\n" \
"s_waitcnt vmcnt(13)\n" \
"v_mfma_f32_16x16x32_bf16 v[56:59], v[50:53], v[224:227], v[56:59]\n" \
"v_mfma_f32_16x16x32_bf16 v[60:63], v[48:51], v[224:227], v[60:63]\n" \
"s_waitcnt vmcnt(12)\n" \
"v_mfma_f32_16x16x32_bf16 v[56:59], v[50:53], v[176:179], v[56:59]\n" \
"v_mfma_f32_16x16x32_bf16 v[60:63], v[48:51], v[176:179], v[60:63]\n" \
"s_waitcnt vmcnt(11)\n" \
"v_mfma_f32_16x16x32_bf16 v[56:59], v[50:53], v[248:251], v[56:59]\n" \
"v_mfma_f32_16x16x32_bf16 v[60:63], v[48:51], v[248:251], v[60:63]\n" \
"s_waitcnt vmcnt(10)\n" \
"v_mfma_f32_16x16x32_bf16 v[168:171], v[50:53], v[140:143], v[168:171]\n" \
"v_mfma_f32_16x16x32_bf16 v[172:175], v[48:51], v[140:143], v[172:175]\n" \
"s_waitcnt vmcnt(9)\n" \
"v_mfma_f32_16x16x32_bf16 v[240:243], v[50:53], v[212:215], v[240:243]\n" \
"v_mfma_f32_16x16x32_bf16 v[244:247], v[48:51], v[212:215], v[244:247]\n" \
"s_waitcnt vmcnt(8)\n" \
"v_mfma_f32_16x16x32_bf16 v[168:171], v[50:53], v[148:151], v[168:171]\n" \
"v_cvt_pk_bf16_f32 v56, v56, v57\n" \
"v_cvt_pk_bf16_f32 v57, v58, v59\n" \
"v_mfma_f32_16x16x32_bf16 v[172:175], v[48:51], v[148:151], v[172:175]\n" \
"v_cvt_pk_bf16_f32 v58, v60, v61\n" \
"s_waitcnt vmcnt(7)\n" \
"v_mfma_f32_16x16x32_bf16 v[240:243], v[50:53], v[220:223], v[240:243]\n" \
"v_permlane16_swap_b32_e32 v56, v58\n" \
"v_mfma_f32_16x16x32_bf16 v[244:247], v[48:51], v[220:223], v[244:247]\n" \
"v_cvt_pk_bf16_f32 v59, v62, v63\n" \
"s_waitcnt vmcnt(6)\n" \
"v_mfma_f32_16x16x32_bf16 v[168:171], v[50:53], v[156:159], v[168:171]\n" \
"v_permlane16_swap_b32_e32 v57, v59\n" \
"buffer_store_dwordx4 v[56:59], v42, s[88:91], s73 offen sc0 sc1\n" \
"v_mfma_f32_16x16x32_bf16 v[172:175], v[48:51], v[156:159], v[172:175]\n" \
"s_waitcnt vmcnt(6)\n" \
"v_mfma_f32_16x16x32_bf16 v[240:243], v[50:53], v[228:231], v[240:243]\n" \
"v_mfma_f32_16x16x32_bf16 v[244:247], v[48:51], v[228:231], v[244:247]\n" \
"s_waitcnt vmcnt(5)\n" \
"v_mfma_f32_16x16x32_bf16 v[168:171], v[50:53], v[164:167], v[168:171]\n" \
"v_mfma_f32_16x16x32_bf16 v[172:175], v[48:51], v[164:167], v[172:175]\n" \
"s_waitcnt vmcnt(4)\n" \
"v_mfma_f32_16x16x32_bf16 v[240:243], v[50:53], v[236:239], v[240:243]\n" \
"v_mfma_f32_16x16x32_bf16 v[244:247], v[48:51], v[236:239], v[244:247]\n" \
"s_waitcnt vmcnt(3)\n" \
"v_mfma_f32_16x16x32_bf16 v[240:243], v[50:53], v[188:191], v[240:243]\n" \
"v_mfma_f32_16x16x32_bf16 v[244:247], v[48:51], v[188:191], v[244:247]\n" \
"s_waitcnt vmcnt(2)\n" \
"v_mfma_f32_16x16x32_bf16 v[240:243], v[50:53], v[196:199], v[240:243]\n" \
"v_cvt_pk_bf16_f32 v168, v168, v169\n" \
"v_cvt_pk_bf16_f32 v169, v170, v171\n" \
"v_mfma_f32_16x16x32_bf16 v[244:247], v[48:51], v[196:199], v[244:247]\n" \
"v_cvt_pk_bf16_f32 v170, v172, v173\n" \
"s_waitcnt vmcnt(1)\n" \
"v_mfma_f32_16x16x32_bf16 v[240:243], v[50:53], v[204:207], v[240:243]\n" \
"v_cvt_pk_bf16_f32 v171, v174, v175\n" \
"v_mfma_f32_16x16x32_bf16 v[244:247], v[48:51], v[204:207], v[244:247]\n" \
"v_permlane16_swap_b32_e32 v168, v170\n" \
"v_permlane16_swap_b32_e32 v169, v171\n" \
"buffer_store_dwordx4 v[168:171], v42, s[88:91], s92 offen sc0 sc1\n" \
"s_nop 2\n" \
"v_cvt_pk_bf16_f32 v240, v240, v241\n" \
"v_cvt_pk_bf16_f32 v241, v242, v243\n" \
"v_cvt_pk_bf16_f32 v242, v244, v245\n" \
"v_cvt_pk_bf16_f32 v243, v246, v247\n" \
"s_nop 0\n" \
"v_permlane16_swap_b32_e32 v240, v242\n" \
"v_permlane16_swap_b32_e32 v241, v243\n" \
"buffer_store_dwordx4 v[240:243], v42, s[88:91], s93 offen sc0 sc1\n"
#define ATT_VCLOB "v21","v22","v23","v32","v33","v34","v35","v36","v37","v38","v39","v40","v41","v42","v43","v44","v45","v46","v47","v48","v49","v50","v51","v52","v53","v54","v55","v56","v57","v58","v59","v60","v61","v62","v63","v64","v65","v66","v67","v68","v69","v70","v71","v72","v73","v74","v75","v76","v77","v78","v79","v80","v81","v82","v83","v84","v85","v86","v87","v88","v89","v90","v91","v92","v93","v94","v95","v96","v97","v98","v99","v100","v101","v102","v103","v104","v105","v106","v107","v108","v109","v110","v111","v112","v113","v114","v115","v116","v117","v118","v119","v120","v121","v122","v123","v124","v125","v126","v127","v128","v129","v130","v131","v132","v133","v134","v135","v136","v137","v138","v139","v140","v141","v142","v143","v144","v145","v146","v147","v148","v149","v150","v151","v152","v153","v154","v155","v156","v157","v158","v159","v160","v161","v162","v163","v164","v165","v166","v167","v168","v169","v170","v171","v172","v173","v174","v175","v176","v177","v178","v179","v180","v181","v182","v183","v184","v185","v186","v187","v188","v189","v190","v191","v192","v193","v194","v195","v196","v197","v198","v199","v200","v201","v202","v203","v204","v205","v206","v207","v208","v209","v210","v211","v212","v213","v214","v215","v216","v217","v218","v219","v220","v221","v222","v223","v224","v225","v226","v227","v228","v229","v230","v231","v232","v233","v234","v235","v236","v237","v238","v239","v240","v241","v242","v243","v244","v245","v246","v247","v248","v249","v250","v251","v252","v253","v254","v255"
#define ATT_SCLOB "s63","s64","s65","s66","s67","s68","s69","s70","s71","s72","s73","s74","s75","s76","s77","s78","s79","s80","s81","s82","s83","s84","s85","s86","s87","s88","s89","s90","s91","s92","s93","s95","s96","s97","s98","s99","s100","s101"
enum { T_fo, T_fa, T_fas, T_fb, T_fbs, T_f8, T_mk8, T_wol, T_woh, T_wbl, T_wbh, T_outl, T_outh, T_r1l, T_r1h, T_r1n, T_r1s, T_m0, T_m1, T_mw, T_m4, T_ml, T_w0, T_w1, T_w2, T_w3, T_A1, T_A2, T_A3, T_B1, T_B2, T_B3, T_Ao, T_Bo, T_Al, T_Bl, T_l8, T_xr, T_x8r, T_oA, T_oB, T_o8, T_x0, T_x1, T_x2, T_x3, T_x4, T_x5, T_x6, T_x8, T_ep, T_xf, T_kf };

__device__ __forceinline__ void out_consts(u32x4 (&vp)[3], const u16* wo, const u16* wob, const u16* out, const unsigned b, const unsigned w, const unsigned lane) {
    const unsigned sp = b & 7u, nt = (b >> 3) & 3u, j = b >> 5, head = 4u * sp + nt, OOR = 0x40000000u;
    unsigned vw[9];
#pragma unroll
    for (unsigned i = 0; i < 9u; ++i) {
        const unsigned q = 64u * i + lane, row = __umul24(q, 3641u) >> 16, c = q - __umul24(row, 18u);
        vw[i] = __umul24(row, DM * 2) + (c < 16u ? (c * 16u + sp * 32u + 32u) & 255u : c * 16u) - (i & 3u) * 1024u;
    }
    const unsigned row = 16u * w + (lane >> 2), r9 = __umul24(16u * (row >> 5) + (row & 12u) + 2u * ((row >> 4) & 1u) + ((row >> 1) & 1u), 18u) + 16u + (row & 1u);
    unsigned vwl = __umul24(r9, DM * 2) + ((lane & 2u) ? 256u : sp * 32u) + (lane & 1u) * 16u, vrd = __umul24(lane >> 2, 288u) + (lane & 3u) * 8u;
    asm volatile("" : "+v"(vw[0]), "+v"(vw[1]), "+v"(vw[2]), "+v"(vw[3]), "+v"(vw[4]), "+v"(vw[5]), "+v"(vw[6]), "+v"(vw[7]), "+v"(vw[8]), "+v"(vwl), "+v"(vrd));
    unsigned tab = 0;
#define TB(n, v) asm("v_writelane_b32 %0, %1, %2" : "+v"(tab) : "s"((unsigned)(v)), "n"((int)T_##n))
    const unsigned FJ = (unsigned)WS_F + (F_J + 640u * j) * 4u, mt = 2u * (w & 2u) + (w & 1u), w2 = w < 2u ? w : 2u, hw = head * 147456u;
    const unsigned FR = (unsigned)WS_F + F_R * 4u + (8u * j + sp) * 1024u;
    TB(fo, FJ + 4u * head); TB(fa, FR + (w | 2u) * 256u); TB(fas, FR + w * 256u + nt * 4u);
    TB(fb, FR + 128u + w * 256u); TB(fbs, FR + 128u + w * 256u + nt * 4u);
    TB(ep, b + w ? OOR : (unsigned)WS_F + F_EPOCH * 4u);
    const u16* pwo = wo + nt * 144u; const u16* pwb = wob + nt * 144u; const u16* pr1 = w < 3u ? pwo : pwb;
    TB(wol, PLO(pwo)); TB(woh, PHI(pwo) & 0xffffu); TB(wbl, PLO(pwb)); TB(wbh, PHI(pwb) & 0xffffu); TB(outl, PLO(out)); TB(outh, PHI(out) & 0xffffu); TB(r1l, PLO(pr1)); TB(r1h, PHI(pr1) & 0xffffu);
    TB(r1n, (w < 3u ? 72u * sp + 71u : 4095u) * 1152u + 288u); TB(r1s, w < 3u ? (72u * sp + 32u * w) * 1152u : hw + 110592u);
    TB(m0, 131072u + 9216u * w2); TB(w0, hw + 36864u * w2); TB(m1, w < 3u ? 9216u * w : 73728u); TB(mw, 9216u * w); TB(m4, w < 3u ? 131072u + 9216u * w : 64512u); TB(ml, 119808u + 1024u * w);
    const unsigned tA = (unsigned)WS_AF + (8u * j + mt) * (KS_TOT * 1024u), tB = (unsigned)WS_AF + (120u - 8u * j + mt) * (KS_TOT * 1024u);
#define HK(k) (4u * sp + ((nt + k) & 3u))
    TB(w1, HK(1u) * 147456u + w * 36864u); TB(w2, HK(2u) * 147456u + w * 36864u); TB(w3, HK(3u) * 147456u + w * 36864u);
    TB(A1, tA + 18432u + HK(1u) * 4096u); TB(A2, tA + 18432u + HK(2u) * 4096u); TB(A3, tA + 18432u + HK(3u) * 4096u);
    TB(B1, tB + 18432u + HK(1u) * 4096u); TB(B2, tB + 18432u + HK(2u) * 4096u); TB(B3, tB + 18432u + HK(3u) * 4096u);
    TB(Ao, tA + sp * 2048u); TB(Bo, tB + sp * 2048u); TB(Al, tA + 16384u); TB(Bl, tB + 16384u);
    const bool red = w == (sp >> 1), blk = sp & 1u;
    const unsigned xg = (unsigned)WS_X + (4u * j + nt) * 589824u + w * 16384u, oA = (128u * j + 16u * mt) * 1152u + nt * 288u, oB = (1920u - 128u * j + 16u * mt) * 1152u + nt * 288u;
    TB(l8, red ? (blk ? tB : tA) + 16384u : OOR); TB(xr, xg + sp * 65536u); TB(x8r, red ? xg + 524288u + (sp & 1u) * 1024u : OOR);
    TB(oA, oA + sp * 32u); TB(oB, oB + sp * 32u); TB(o8, red ? (blk ? oB : oA) + 256u : OOR); TB(x8, xg + 524288u + sp * 2048u);
    TB(f8, xg + w * 131072u + 14400u + sp * 4u); TB(mk8, red ? 0xff0000u : 0u); TB(xf, xg + sp * 65536u + 14336u);
    TB(kf, (unsigned)WS_F + (F_KV + head * 64u) * 4u);
#define XI(i) TB(x##i, xg + ((sp + 1u + i) & 7u) * 65536u + i * 2048u)
    XI(0); XI(1); XI(2); XI(3); XI(4); XI(5); XI(6);
#undef XI
#undef HK
#undef TB
    vp[0] = (u32x4){vw[0], vw[1], vw[2], vw[3]}; vp[1] = (u32x4){vw[4], vw[5], vw[6], vw[7]}; vp[2] = (u32x4){vw[8], vwl, vrd, tab};
}
__global__ __launch_bounds__(256, 1) __attribute__((aligned(4096))) void fused_kernel(const u16* __restrict__ x, const u16* __restrict__ ckv, const u16* __restrict__ wq, const u16* __restrict__ wuk,
                                                       const u16* __restrict__ wuv, const u16* __restrict__ wqkv, const u16* __restrict__ gw,
                                                       const u16* __restrict__ wo, const u16* __restrict__ wob, char* __restrict__ ws, u16* __restrict__ out) {
    using namespace att;
    extern __shared__ __attribute__((aligned(16))) char lds[];
    unsigned wid = rfl(threadIdx.x >> 6);
    code_touch((blockIdx.x >> 3) * 4u + wid, (unsigned)(size_t)lds + 151552u + wid * 512u);
    SBAR();
    asm volatile("" :: "s"(x), "s"(ckv), "s"(wq), "s"(wuk), "s"(wuv), "s"(wqkv), "s"(gw), "s"(wo), "s"(wob), "s"(ws), "s"(out));
    unsigned tag;
    int b = blockIdx.x;
    u32x4 vp[5], sa[5]; unsigned sd[2];
    const unsigned vl = proj_phase(x, ckv, wq, wuk, wuv, wqkv, gw, wo, wob, out, ws, lds, wid, tag, vp, sa, sd);
    asm volatile("" : "+s"(ws));
    unsigned* const flags = (unsigned*)(ws + WS_F);
    RTS(2);
#define AST(L) do {} while (0)
#define SD(J8, H, W) ((unsigned)WS_AF + (((J8) + 2u * ((W) & 2u) + ((W) & 1u)) * KS_TOT + 18u + 4u * (H)) * 1024u)
#define SRDA (u32x4){PLO(ws), PHI(ws) & 0xffffu, (unsigned)WS_L, 0x00020000u}
#define ATT_TAIL "s_endpgm\n"
    unsigned abad;
    asm volatile(ATT_ASM ATT_TAIL "L_end_%=:\n s_nop 1"
                 : "=&{s94}"(abad)
                 : "{v[0:3]}"(vp[0]), "{v[4:7]}"(vp[1]), "{v[8:11]}"(vp[2]), "{v[12:15]}"(vp[3]), "{v[16:19]}"(vp[4]), "{v20}"(vl), "{s[56:59]}"(sa[4]), "{s60}"(sd[0]), "{s61}"(sd[1]), "{s62}"(tag),
                   "{s[8:11]}"(sa[0]), "{s[12:15]}"(sa[1]), "{s[16:19]}"(sa[2]), "{s[20:23]}"(sa[3])
                 : "memory", "m0", "vcc", "scc", ATT_VCLOB, ATT_SCLOB, ACLOB_LO, ACLOB_HI);
    {
        const u16* const __attribute__((address_space(4)))* ka = (const u16* const __attribute__((address_space(4)))*)__builtin_amdgcn_kernarg_segment_ptr();
        unsigned b2 = b, w2 = wid;
        asm volatile("s_waitcnt vmcnt(0)" : "+s"(ka), "+s"(b2), "+s"(w2), "+s"(ws) :: "memory");
        const unsigned j2 = b2 >> 5, p2 = w2 & 1u, h2 = (b2 & 7u) * 4u + ((b2 >> 3) & 3u);
#pragma unroll 1
        for (unsigned i = w2 & 2u; i < ((w2 & 2u) ? (abad ? 8u : 4u) : 2u); ++i)
            att_exact(ka[0], ka[2], ws, h2, (i < 4u ? 1920u - 128u * j2 : 128u * j2 - 128u) + 32u * i + 16u * p2, *reinterpret_cast<const float*>(lds + LDS_G + (32u * i + 16u * p2 + (lane_id() & 15u)) * 4u));
        asm volatile("s_waitcnt vmcnt(0)" ::: "memory");
        unsigned* fa = (unsigned*)(ws + WS_F) + F_R + (8u * j2 + (b2 & 7u)) * 256u + (2u + p2) * 64u + ((b2 >> 3) & 3u);
        if (w2 & 2u) st_flag(fa, tag);
        wait_flags(fa, ld_flag(fa), tag);
        const unsigned l2 = lane_id(); u32x4 c[3]; out_consts(c, ka[7], ka[8], ka[10], b2, w2, l2);
        asm volatile("s_waitcnt vmcnt(0) lgkmcnt(0)\n\ts_nop 7\n\ts_branch L_att_stub" :: "{v[0:3]}"(c[0]), "{v[4:7]}"(c[1]), "{v[8:11]}"(c[2]), "{v20}"(l2 * 16u), "{s[56:59]}"(SRDA), "{s60}"(SD(8u * j2, h2, w2)),
                     "{s61}"(SD(120u - 8u * j2, h2, w2)), "{s62}"(tag) : "memory");
    }
    RTS(5);
}

extern "C" void kernel_launch(void* const* d_in, const int* in_sizes, int n_in, void* d_out, int out_size, void* d_ws, size_t ws_size,
                              hipStream_t stream) {
    (void)hipFuncSetAttribute((const void*)fused_kernel, hipFuncAttributeMaxDynamicSharedMemorySize, att::LDS_BYTES);
    fused_kernel<<<dim3(256), 256, att::LDS_BYTES, stream>>>((const u16*)d_in[0], (const u16*)d_in[4], (const u16*)d_in[5], (const u16*)d_in[6], (const u16*)d_in[7], (const u16*)d_in[1],
                                                             (const u16*)d_in[9], (const u16*)d_in[2], (const u16*)d_in[8], (char*)d_ws, (u16*)d_out);
}
```

```cpp
#include <hip/hip_runtime.h>
#include <hip/hip_bf16.h>
#include <stdint.h>
#include <utility>

typedef short bf16x8 __attribute__((ext_vector_type(8)));
typedef short s16x4 __attribute__((ext_vector_type(4)));
typedef float f32x16 __attribute__((ext_vector_type(16)));
typedef float f32x4 __attribute__((ext_vector_type(4)));
typedef unsigned u32x4 __attribute__((ext_vector_type(4)));
typedef unsigned short u16;

constexpr int S = 2048, DM = 576, NH = 32, HD = 128, DC = 512, NQKV = 1728, DHA = 18;
constexpr int NBIG = NH * HD;
constexpr int KS_TOT = 146;

#define SBAR() __builtin_amdgcn_sched_barrier(0)
constexpr size_t WS_Q = 0, WS_K = WS_Q + (size_t)S * NBIG * 2, WS_V = WS_K + (size_t)S * NBIG * 2, WS_AF = WS_V + (size_t)S * NBIG * 2, WS_X = WS_AF + (size_t)128 * KS_TOT * 1024,
                 WS_P = WS_X + (size_t)64 * 16 * 9216, WS_F = WS_P + (size_t)128 * (4 * 32 * 256 + 1024) * 4;
constexpr size_t WS_L = (size_t)96 << 20;
static_assert(WS_F == 96206848, "workspace map is baked into the generated asm");
constexpr int F_EPOCH = 0, F_J = 1024, F_ST = 8192, F_KV = 40960, F_R = 65536;
constexpr unsigned TAG_STEP = 0x9E3779B1u;
__device__ __forceinline__ unsigned ld_flag(const unsigned* p) { return __hip_atomic_load(p, __ATOMIC_RELAXED, __HIP_MEMORY_SCOPE_AGENT); }
__device__ __forceinline__ void st_flag(unsigned* p, unsigned v) { __hip_atomic_store(p, v, __ATOMIC_RELAXED, __HIP_MEMORY_SCOPE_AGENT); }
__device__ __forceinline__ unsigned ld_flag_async(const unsigned* p) { unsigned v; asm volatile("global_load_dword %0, %1, off sc1" : "=v"(v) : "v"(p) : "memory"); return v; }
#define WAITV(v, N) asm volatile("s_waitcnt vmcnt(" #N ")" : "+v"(v) :: "memory")
__device__ __forceinline__ void wait_flags(const unsigned* p, unsigned v, unsigned tag) {
    if (__builtin_expect(__builtin_amdgcn_ballot_w64(v != tag) != 0ull, 0)) {
        unsigned spins = 0;
        do { __builtin_amdgcn_s_sleep(1); } while (__builtin_amdgcn_ballot_w64(ld_flag(p) != tag) != 0ull && ++spins < (1u << 22));
    }
    asm volatile("" ::: "memory");
}

__device__ __forceinline__ unsigned cvtpk(float lo, float hi) {
    unsigned r; asm volatile("v_cvt_pk_bf16_f32 %0, %1, %2" : "=v"(r) : "v"(lo), "v"(hi)); return r;
}
__device__ __forceinline__ float bf2f(u16 v) { return __uint_as_float(((unsigned)v) << 16); }
__device__ __forceinline__ int crow(int r, int hi) { return (r & 3) + 8 * (r >> 2) + 4 * hi; }

typedef __attribute__((address_space(3))) s16x4* lds_s16x4_p;
__device__ __forceinline__ s16x4 trrd(unsigned addr) { return __builtin_amdgcn_ds_read_tr16_b64_v4i16((lds_s16x4_p)addr); }

__device__ __forceinline__ unsigned lane_id() { unsigned l; asm volatile("v_mbcnt_lo_u32_b32 %0, -1, 0\n\tv_mbcnt_hi_u32_b32 %0, -1, %0" : "=v"(l)); return l; }
#define RTS(slot) do {} while (0)
#define CT_L 5u
__device__ __forceinline__ void code_touch(unsigned wi, unsigned lds_scratch) {
    unsigned long long pc; asm volatile("s_getpc_b64 %0" : "=s"(pc));
    asm volatile("s_mov_b32 m0, %0\n\ts_mov_b64 exec, %3\n\tglobal_load_lds_dword %1, %2\n\ts_mov_b64 exec, -1" :: "s"(lds_scratch), "v"(((threadIdx.x & 63u) * 128u + wi) * 128u), "s"(pc & ~127ull), "n"((1u << CT_L) - 1u) : "m0", "memory");
}
#define ACLOB_LO "a0","a1","a2","a3","a4","a5","a6","a7","a8","a9","a10","a11","a12","a13","a14","a15","a16","a17","a18","a19","a20","a21","a22","a23","a24","a25","a26","a27","a28","a29","a30","a31","a32","a33","a34","a35","a36","a37","a38","a39","a40","a41","a42","a43","a44","a45","a46","a47","a48","a49","a50","a51","a52","a53","a54","a55","a56","a57","a58","a59","a60","a61","a62","a63","a64","a65","a66","a67","a68","a69","a70","a71","a72","a73","a74","a75","a76","a77","a78","a79","a80","a81","a82","a83","a84","a85","a86","a87","a88","a89","a90","a91","a92","a93","a94","a95","a96","a97","a98","a99","a100","a101","a102","a103","a104","a105","a106","a107","a108","a109","a110","a111","a112","a113","a114","a115","a116","a117","a118","a119","a120","a121","a122","a123","a124","a125","a126","a127"
#define ACLOB_HI "a128","a129","a130","a131","a132","a133","a134","a135","a136","a137","a138","a139","a140","a141","a142","a143","a144","a145","a146","a147","a148","a149","a150","a151","a152","a153","a154","a155","a156","a157","a158","a159","a160","a161","a162","a163","a164","a165","a166","a167","a168","a169","a170","a171","a172","a173","a174","a175","a176","a177","a178","a179","a180","a181","a182","a183","a184","a185","a186","a187","a188","a189","a190","a191","a192","a193","a194","a195","a196","a197","a198","a199","a200","a201","a202","a203","a204","a205","a206","a207","a208","a209","a210","a211","a212","a213","a214","a215","a216","a217","a218","a219","a220","a221","a222","a223","a224","a225","a226","a227","a228","a229","a230","a231","a232","a233","a234","a235","a236","a237","a238","a239","a240","a241","a242","a243","a244","a245","a246","a247","a248","a249","a250","a251","a252","a253","a254","a255"
#define PROJ_PRE0 \
"s_add_u32 s62, %[rc], 0x0\n" \
"s_and_b32 s62, s62, 0x3ff\n" \
"s_lshl_b32 s63, s62, 12\n" \
"s_add_u32 m0, %[mA], 0x0\n" \
"s_add_u32 s85, s62, 0x0\n" \
"buffer_load_dwordx4 v22, s[64:67], s85 offen lds\n" \
"s_add_u32 m0, %[mA], 0x400\n" \
"s_add_u32 s84, s62, 0x2000\n" \
"buffer_load_dwordx4 v23, s[64:67], s84 offen lds\n" \
"s_add_u32 m0, %[mB], 0x0\n" \
"s_add_u32 s85, s63, 0x0\n" \
"buffer_load_dwordx4 v26, s[68:71], s85 offen lds\n" \
"s_add_u32 m0, %[mB], 0x4000\n" \
"s_add_u32 s84, s63, 0x0\n" \
"buffer_load_dwordx4 v26, s[72:75], s84 offen lds\n" \
"s_add_u32 m0, %[mA], 0x800\n" \
"s_add_u32 s85, s62, 0x4000\n" \
"buffer_load_dwordx4 v22, s[64:67], s85 offen lds\n" \
"s_add_u32 m0, %[mA], 0xc00\n" \
"s_add_u32 s84, s62, 0x6000\n" \
"buffer_load_dwordx4 v23, s[64:67], s84 offen lds\n" \
"s_add_u32 m0, %[mB], 0x400\n" \
"s_add_u32 s85, s63, 0x8000\n" \
"buffer_load_dwordx4 v26, s[68:71], s85 offen lds\n" \
"s_add_u32 m0, %[mB], 0x4400\n" \
"s_add_u32 s84, s63, 0x8000\n" \
"buffer_load_dwordx4 v26, s[72:75], s84 offen lds\n"
#define PROJ_PRE1 \
"s_add_u32 s62, %[rc], 0x0\n" \
"s_and_b32 s62, s62, 0x3ff\n" \
"s_lshl_b32 s63, s62, 12\n" \
"s_add_u32 m0, %[mA], 0x1000\n" \
"s_add_u32 s85, s62, 0x8000\n" \
"buffer_load_dwordx4 v22, s[64:67], s85 offen lds\n" \
"s_add_u32 m0, %[mA], 0x1400\n" \
"s_add_u32 s84, s62, 0xa000\n" \
"buffer_load_dwordx4 v23, s[64:67], s84 offen lds\n" \
"s_add_u32 m0, %[mB], 0x800\n" \
"s_add_u32 s85, s63, 0x10000\n" \
"buffer_load_dwordx4 v27, s[68:71], s85 offen lds\n" \
"s_add_u32 m0, %[mB], 0x4800\n" \
"s_add_u32 s84, s63, 0x10000\n" \
"buffer_load_dwordx4 v27, s[72:75], s84 offen lds\n" \
"s_add_u32 m0, %[mA], 0x1800\n" \
"s_add_u32 s85, s62, 0xc000\n" \
"buffer_load_dwordx4 v22, s[64:67], s85 offen lds\n" \
"s_add_u32 m0, %[mA], 0x1c00\n" \
"s_add_u32 s84, s62, 0xe000\n" \
"buffer_load_dwordx4 v23, s[64:67], s84 offen lds\n" \
"s_add_u32 m0, %[mB], 0xc00\n" \
"s_add_u32 s85, s63, 0x18000\n" \
"buffer_load_dwordx4 v27, s[68:71], s85 offen lds\n" \
"s_add_u32 m0, %[mB], 0x4c00\n" \
"s_add_u32 s84, s63, 0x18000\n" \
"buffer_load_dwordx4 v27, s[72:75], s84 offen lds\n"
#define PROJ_PRE2 \
"s_add_u32 s62, %[rc], 0x80\n" \
"s_and_b32 s62, s62, 0x3ff\n" \
"s_lshl_b32 s63, s62, 12\n" \
"s_add_u32 m0, %[mA], 0x8000\n" \
"s_add_u32 s85, s62, 0x0\n" \
"buffer_load_dwordx4 v22, s[64:67], s85 offen lds\n" \
"s_add_u32 m0, %[mA], 0x8400\n" \
"s_add_u32 s84, s62, 0x2000\n" \
"buffer_load_dwordx4 v23, s[64:67], s84 offen lds\n"
#define PROJ_PRE3 \
"s_add_u32 s62, %[rc], 0x80\n" \
"s_and_b32 s62, s62, 0x3ff\n" \
"s_lshl_b32 s63, s62, 12\n" \
"s_add_u32 m0, %[mB], 0x8000\n" \
"s_add_u32 s85, s63, 0x0\n" \
"buffer_load_dwordx4 v26, s[68:71], s85 offen lds\n" \
"s_add_u32 m0, %[mB], 0xc000\n" \
"s_add_u32 s84, s63, 0x0\n" \
"buffer_load_dwordx4 v26, s[72:75], s84 offen lds\n" \
"s_add_u32 m0, %[mA], 0x8800\n" \
"s_add_u32 s85, s62, 0x4000\n" \
"buffer_load_dwordx4 v22, s[64:67], s85 offen lds\n" \
"s_add_u32 m0, %[mA], 0x8c00\n" \
"s_add_u32 s84, s62, 0x6000\n" \
"buffer_load_dwordx4 v23, s[64:67], s84 offen lds\n" \
"s_add_u32 m0, %[mB], 0x8400\n" \
"s_add_u32 s85, s63, 0x8000\n" \
"buffer_load_dwordx4 v26, s[68:71], s85 offen lds\n" \
"s_add_u32 m0, %[mB], 0xc400\n" \
"s_add_u32 s84, s63, 0x8000\n" \
"buffer_load_dwordx4 v26, s[72:75], s84 offen lds\n" \
"s_add_u32 m0, %[mA], 0x9000\n" \
"s_add_u32 s85, s62, 0x8000\n" \
"buffer_load_dwordx4 v22, s[64:67], s85 offen lds\n" \
"s_add_u32 m0, %[mA], 0x9400\n" \
"s_add_u32 s84, s62, 0xa000\n" \
"buffer_load_dwordx4 v23, s[64:67], s84 offen lds\n"
#define PROJ_TKE 0
#define PROJ_T0 0
#define PROJ_ROT 2*(sp+mt)
#define PROJ_TC 0
#define PROJ_ASM \
".p2align 3\n" \
"s_branch L_go_%=\n" \
"L_pl_%=:\n" \
"s_mov_b32 s63, 0\n" \
"L_ql_%=:\n" \
"s_sleep 1\n" \
"v_bfe_u32 v44, v20, 2, 5\n" \
"buffer_load_dword v44, v44, s[80:83], s39 offen offset:64 sc1\n" \
"s_waitcnt vmcnt(0)\n" \
"v_cmp_ne_u32_e32 vcc, %[tag], v44\n" \
"s_add_u32 s63, s63, 1\n" \
"s_nop 3\n" \
"s_cbranch_vccz L_rl_%=\n" \
"s_cmp_gt_u32 s63, 0x400000\n" \
"s_cbranch_scc0 L_ql_%=\n" \
"s_branch L_rl_%=\n" \
"L_pa_%=:\n" \
"s_mov_b32 s63, 0\n" \
"L_qa_%=:\n" \
"s_sleep 1\n" \
"v_bfe_u32 v44, v20, 2, 5\n" \
"buffer_load_dword v44, v44, s[80:83], s39 offen offset:0 sc1\n" \
"s_waitcnt vmcnt(0)\n" \
"v_cmp_ne_u32_e32 vcc, %[tag], v44\n" \
"s_add_u32 s63, s63, 1\n" \
"s_nop 3\n" \
"s_cbranch_vccz L_ra_%=\n" \
"s_cmp_gt_u32 s63, 0x400000\n" \
"s_cbranch_scc0 L_qa_%=\n" \
"s_branch L_ra_%=\n" \
"L_go_%=:\n" \
"v_xor_b32_e32 v33, 0x20, v32\n" \
"s_mov_b32 s80, %[wsl]\n" \
"s_and_b32 s81, %[wsh], 0xffff\n" \
"s_mov_b32 s82, 0x10000000\n" \
"s_mov_b32 s83, 0x20000\n" \
"s_mov_b32 s84, 0x5bc0000\n" \
"s_add_u32 s62, s22, 0x80\n" \
"buffer_load_dword v180, off, s[80:83], s84 sc1\n" \
"s_and_b32 s62, s62, 0x3ff\n" \
"s_lshl_b32 s63, s62, 12\n" \
"v_xor_b32_e64 v34, 0x40, v32\n" \
"v_xor_b32_e32 v35, 0x60, v32\n" \
"s_waitcnt vmcnt(11)\n" \
"s_barrier\n" \
"ds_read_b64_tr_b16 v[120:121], v32 offset:8192\n" \
"ds_read_b64_tr_b16 v[122:123], v32 offset:9216\n" \
"ds_read_b128 v[56:59], v29 offset:16384\n" \
"ds_read_b64_tr_b16 v[116:117], v32 offset:0\n" \
"ds_read_b64_tr_b16 v[118:119], v32 offset:1024\n" \
"ds_read_b128 v[52:55], v28 offset:16384\n" \
"s_add_u32 m0, %[mB], 0x8800\n" \
"s_add_u32 s85, s63, 0x10000\n" \
"s_mov_b32 s76, s12\n" \
"ds_read_b128 v[60:63], v28 offset:18432\n" \
"s_and_b32 s77, s13, 0xffff\n" \
"s_mov_b32 s78, 0x1e6000\n" \
"s_mov_b32 s79, 0x20000\n" \
"ds_read_b128 v[64:67], v29 offset:18432\n" \
"s_waitcnt lgkmcnt(5)\n" \
"v_mfma_f32_16x16x32_bf16 a[0:3], v[120:123], v[56:59], 0\n" \
"ds_read_b128 v[72:75], v29 offset:20480\n" \
"ds_read_b128 v[68:71], v28 offset:20480\n" \
"ds_read_b128 v[76:79], v28 offset:22528\n" \
"buffer_load_dwordx4 v27, s[68:71], s85 offen lds\n" \
"ds_read_b128 v[80:83], v29 offset:22528\n" \
"s_waitcnt lgkmcnt(6)\n" \
"v_mfma_f32_16x16x32_bf16 a[0:3], v[116:119], v[52:55], a[0:3]\n" \
"ds_read_b64_tr_b16 v[128:129], v33 offset:8192\n" \
"s_waitcnt lgkmcnt(6)\n" \
"v_mfma_f32_16x16x32_bf16 a[32:35], v[116:119], v[60:63], 0\n" \
"ds_read_b64_tr_b16 v[130:131], v33 offset:9216\n" \
"ds_read_b64_tr_b16 v[124:125], v33 offset:0\n" \
"ds_read_b64_tr_b16 v[126:127], v33 offset:1024\n" \
"s_waitcnt lgkmcnt(8)\n" \
"s_nop 0\n" \
"v_mfma_f32_16x16x32_bf16 a[32:35], v[120:123], v[64:67], a[32:35]\n" \
"ds_read_b64_tr_b16 v[136:137], v34 offset:8192\n" \
"ds_read_b64_tr_b16 v[138:139], v34 offset:9216\n" \
"ds_read_b64_tr_b16 v[132:133], v34 offset:0\n" \
"s_add_u32 m0, %[mB], 0xc800\n" \
"ds_read_b64_tr_b16 v[134:135], v34 offset:1024\n" \
"s_add_u32 s84, s63, 0x10000\n" \
"buffer_load_dwordx4 v27, s[72:75], s84 offen lds\n" \
"ds_read_b64_tr_b16 v[144:145], v35 offset:8192\n" \
"ds_read_b64_tr_b16 v[146:147], v35 offset:9216\n" \
"ds_read_b64_tr_b16 v[140:141], v35 offset:0\n" \
"s_lshr_b32 s95, %[b], 3\n" \
"ds_read_b64_tr_b16 v[142:143], v35 offset:1024\n" \
"s_lshl_b32 s96, s95, 2\n" \
"ds_read_b64_tr_b16 v[152:153], v32 offset:24576\n" \
"s_add_u32 s97, s96, %[w]\n" \
"ds_read_b64_tr_b16 v[154:155], v32 offset:25600\n" \
"v_lshrrev_b32_e32 v212, 4, v20\n" \
"s_mul_i32 s98, 27, s97\n" \
"v_min_u32_e32 v213, 26, v212\n" \
"v_add_u32_e64 v214, s98, v213\n" \
"v_mul_u32_u24_e32 v215, 0xaaab, v214\n" \
"s_waitcnt lgkmcnt(15)\n" \
"v_mfma_f32_16x16x32_bf16 a[64:67], v[120:123], v[72:75], 0\n" \
"ds_read_b64_tr_b16 v[148:149], v32 offset:16384\n" \
"ds_read_b64_tr_b16 v[150:151], v32 offset:17408\n" \
"v_mfma_f32_16x16x32_bf16 a[64:67], v[116:119], v[68:71], a[64:67]\n" \
"s_add_u32 m0, %[mA], 0x9800\n" \
"s_add_u32 s85, s62, 0xc000\n" \
"v_mfma_f32_16x16x32_bf16 a[96:99], v[116:119], v[76:79], 0\n" \
"ds_read_b64_tr_b16 v[160:161], v33 offset:24576\n" \
"s_waitcnt lgkmcnt(15)\n" \
"v_mfma_f32_16x16x32_bf16 a[96:99], v[120:123], v[80:83], a[96:99]\n" \
"buffer_load_dwordx4 v22, s[64:67], s85 offen lds\n" \
"v_mfma_f32_16x16x32_bf16 a[4:7], v[128:131], v[56:59], 0\n" \
"s_add_u32 m0, %[mA], 0x9c00\n" \
"s_waitcnt lgkmcnt(13)\n" \
"s_nop 0\n" \
"v_mfma_f32_16x16x32_bf16 a[4:7], v[124:127], v[52:55], a[4:7]\n" \
"s_add_u32 s84, s62, 0xe000\n" \
"ds_read_b64_tr_b16 v[162:163], v33 offset:25600\n" \
"v_mfma_f32_16x16x32_bf16 a[36:39], v[124:127], v[60:63], 0\n" \
"buffer_load_dwordx4 v23, s[64:67], s84 offen lds\n" \
"v_mfma_f32_16x16x32_bf16 a[36:39], v[128:131], v[64:67], a[36:39]\n" \
"s_add_u32 m0, %[mB], 0x8c00\n" \
"s_add_u32 s85, s63, 0x18000\n" \
"v_mfma_f32_16x16x32_bf16 a[68:71], v[128:131], v[72:75], 0\n" \
"ds_read_b64_tr_b16 v[156:157], v33 offset:16384\n" \
"ds_read_b64_tr_b16 v[158:159], v33 offset:17408\n" \
"v_mfma_f32_16x16x32_bf16 a[68:71], v[124:127], v[68:71], a[68:71]\n" \
"buffer_load_dwordx4 v27, s[68:71], s85 offen lds\n" \
"v_mfma_f32_16x16x32_bf16 a[100:103], v[124:127], v[76:79], 0\n" \
"s_add_u32 m0, %[mB], 0xcc00\n" \
"s_add_u32 s84, s63, 0x18000\n" \
"v_mfma_f32_16x16x32_bf16 a[100:103], v[128:131], v[80:83], a[100:103]\n" \
"ds_read_b64_tr_b16 v[168:169], v34 offset:24576\n" \
"s_waitcnt lgkmcnt(7)\n" \
"v_mfma_f32_16x16x32_bf16 a[8:11], v[136:139], v[56:59], 0\n" \
"buffer_load_dwordx4 v27, s[72:75], s84 offen lds\n" \
"v_mfma_f32_16x16x32_bf16 a[8:11], v[132:135], v[52:55], a[8:11]\n" \
"ds_read_b64_tr_b16 v[170:171], v34 offset:25600\n" \
"ds_read_b64_tr_b16 v[164:165], v34 offset:16384\n" \
"v_mfma_f32_16x16x32_bf16 a[40:43], v[132:135], v[60:63], 0\n" \
"ds_read_b64_tr_b16 v[166:167], v34 offset:17408\n" \
"v_lshrrev_b32_e32 v216, 18, v215\n" \
"v_mfma_f32_16x16x32_bf16 a[40:43], v[136:139], v[64:67], a[40:43]\n" \
"v_mul_u32_u24_e32 v218, 6, v216\n" \
"v_sub_u32_e32 v219, v214, v218\n" \
"v_mfma_f32_16x16x32_bf16 a[72:75], v[136:139], v[72:75], 0\n" \
"v_lshrrev_b32_e32 v220, 1, v219\n" \
"v_mul_u32_u24_e32 v217, 27, v216\n" \
"v_mfma_f32_16x16x32_bf16 a[72:75], v[132:135], v[68:71], a[72:75]\n" \
"v_mul_u32_u24_e32 v221, 9, v220\n" \
"v_add_u32_e32 v222, v217, v221\n" \
"v_mfma_f32_16x16x32_bf16 a[104:107], v[132:135], v[76:79], 0\n" \
"v_and_b32_e32 v223, 1, v219\n" \
"s_and_b32 s94, 7, %[b]\n" \
"v_mfma_f32_16x16x32_bf16 a[104:107], v[136:139], v[80:83], a[104:107]\n" \
"ds_read_b64_tr_b16 v[176:177], v35 offset:24576\n" \
"v_add_u32_e64 v224, v222, v223\n" \
"v_mfma_f32_16x16x32_bf16 a[12:15], v[144:147], v[56:59], 0\n" \
"ds_read_b64_tr_b16 v[178:179], v35 offset:25600\n" \
"v_add_u32_e64 v225, s94, v224\n" \
"v_mfma_f32_16x16x32_bf16 a[12:15], v[140:143], v[52:55], a[12:15]\n" \
"ds_read_b64_tr_b16 v[172:173], v35 offset:16384\n" \
"v_lshlrev_b32_e32 v186, 7, v225\n" \
"v_mfma_f32_16x16x32_bf16 a[44:47], v[140:143], v[60:63], 0\n" \
"ds_read_b64_tr_b16 v[174:175], v35 offset:17408\n" \
"ds_read_b128 v[88:91], v29 offset:0\n" \
"v_mfma_f32_16x16x32_bf16 a[44:47], v[144:147], v[64:67], a[44:47]\n" \
"ds_read_b128 v[84:87], v28 offset:0\n" \
"ds_read_b128 v[92:95], v28 offset:2048\n" \
"v_mfma_f32_16x16x32_bf16 a[76:79], v[144:147], v[72:75], 0\n" \
"ds_read_b128 v[96:99], v29 offset:2048\n" \
"s_mul_i32 s89, 6, s97\n" \
"v_mfma_f32_16x16x32_bf16 a[76:79], v[140:143], v[68:71], a[76:79]\n" \
"ds_read_b128 v[104:107], v29 offset:4096\n" \
"s_add_u32 s95, 0xffffffdc, s89\n" \
"v_mfma_f32_16x16x32_bf16 a[108:111], v[140:143], v[76:79], 0\n" \
"ds_read_b128 v[100:103], v28 offset:4096\n" \
"s_mul_i32 s99, 0x1200, s94\n" \
"v_mfma_f32_16x16x32_bf16 a[108:111], v[144:147], v[80:83], a[108:111]\n" \
"ds_read_b128 v[108:111], v28 offset:6144\n" \
"s_mul_i32 s100, 36, s97\n" \
"v_mfma_f32_16x16x32_bf16 a[16:19], v[56:59], v[152:155], 0\n" \
"ds_read_b128 v[112:115], v29 offset:6144\n" \
"s_waitcnt lgkmcnt(12)\n" \
"v_mfma_f32_16x16x32_bf16 a[16:19], v[52:55], v[148:151], a[16:19]\n" \
"v_add_u32_e32 v227, s95, v212\n" \
"s_add_u32 s101, s99, s100\n" \
"v_mfma_f32_16x16x32_bf16 a[48:51], v[60:63], v[148:151], 0\n" \
"s_mul_i32 s88, 0x288, s94\n" \
"v_min_u32_e32 v228, 0x287, v227\n" \
"v_mfma_f32_16x16x32_bf16 a[48:51], v[64:67], v[152:155], a[48:51]\n" \
"s_mov_b32 m0, %[mS]\n" \
"s_mov_b32 s86, s12\n" \
"v_mfma_f32_16x16x32_bf16 a[80:83], v[72:75], v[152:155], 0\n" \
"s_mov_b32 s87, s13\n" \
"v_add_u32_e32 v226, s101, v212\n" \
"v_mfma_f32_16x16x32_bf16 a[80:83], v[68:71], v[148:151], a[80:83]\n" \
"v_add_u32_e32 v229, s88, v228\n" \
"v_cmp_gt_u32_e32 vcc, 36, v212\n" \
"v_mfma_f32_16x16x32_bf16 a[112:115], v[76:79], v[148:151], 0\n" \
"s_lshl_b32 s96, %[w], 6\n" \
"v_cndmask_b32_e32 v230, v229, v226, vcc\n" \
"v_mfma_f32_16x16x32_bf16 a[112:115], v[80:83], v[152:155], a[112:115]\n" \
"v_lshlrev_b32_e32 v16, 7, v230\n" \
"v_lshrrev_b32_e32 v212, 4, v20\n" \
"v_mfma_f32_16x16x32_bf16 a[20:23], v[56:59], v[160:163], 0\n" \
"v_add_u32_e64 v213, s96, v212\n" \
"v_mul_u32_u24_e32 v214, 0x2aab, v213\n" \
"v_mfma_f32_16x16x32_bf16 a[20:23], v[52:55], v[156:159], a[20:23]\n" \
"v_lshrrev_b32_e32 v215, 16, v214\n" \
"s_lshr_b32 s94, %[b], 5\n" \
"s_mov_b32 exec_lo, 0x7ffffff\n" \
"s_mov_b32 exec_hi, 0x0\n" \
"global_load_lds_dword v186, s[86:87]\n" \
"s_mov_b64 exec, -1\n" \
"v_mfma_f32_16x16x32_bf16 a[52:55], v[60:63], v[156:159], 0\n" \
"v_mul_u32_u24_e32 v218, 6, v215\n" \
"s_mul_i32 s95, 0x48, s94\n" \
"v_mfma_f32_16x16x32_bf16 a[52:55], v[64:67], v[160:163], a[52:55]\n" \
"v_sub_u32_e32 v219, v213, v218\n" \
"v_add_u32_e64 v216, s95, v215\n" \
"v_mfma_f32_16x16x32_bf16 a[84:87], v[72:75], v[160:163], 0\n" \
"v_lshrrev_b32_e32 v220, 1, v219\n" \
"s_and_b32 s100, 7, %[b]\n" \
"v_mfma_f32_16x16x32_bf16 a[84:87], v[68:71], v[156:159], a[84:87]\n" \
"s_lshr_b32 s88, %[b], 3\n" \
"v_mul_u32_u24_e32 v217, 0xd80, v216\n" \
"v_mfma_f32_16x16x32_bf16 a[116:119], v[76:79], v[156:159], 0\n" \
"v_mul_u32_u24_e32 v221, 0x480, v220\n" \
"v_and_b32_e32 v223, 1, v219\n" \
"v_mfma_f32_16x16x32_bf16 a[116:119], v[80:83], v[160:163], a[116:119]\n" \
"s_add_u32 s97, 0x100, s96\n" \
"s_lshl_b32 s101, s100, 2\n" \
"v_mfma_f32_16x16x32_bf16 a[24:27], v[56:59], v[168:171], 0\n" \
"s_and_b32 s89, 3, s88\n" \
"v_add_u32_e64 v222, v217, v221\n" \
"v_mfma_f32_16x16x32_bf16 a[24:27], v[52:55], v[164:167], a[24:27]\n" \
"v_lshlrev_b32_e32 v224, 4, v223\n" \
"s_add_u32 s96, s101, s89\n" \
"v_mfma_f32_16x16x32_bf16 a[56:59], v[60:63], v[164:167], 0\n" \
"v_add_u32_e32 v192, v222, v224\n" \
"s_mul_i32 s56, 36, s96\n" \
"v_mfma_f32_16x16x32_bf16 a[56:59], v[64:67], v[168:171], a[56:59]\n" \
"v_add_u32_e64 v225, s97, v212\n" \
"v_mul_u32_u24_e32 v226, 0x2aab, v225\n" \
"v_mfma_f32_16x16x32_bf16 a[88:91], v[72:75], v[168:171], 0\n" \
"v_lshrrev_b32_e32 v227, 16, v226\n" \
"v_mul_u32_u24_e32 v230, 6, v227\n" \
"v_mfma_f32_16x16x32_bf16 a[88:91], v[68:71], v[164:167], a[88:91]\n" \
"v_sub_u32_e32 v231, v225, v230\n" \
"v_add_u32_e32 v228, s95, v227\n" \
"v_mfma_f32_16x16x32_bf16 a[120:123], v[76:79], v[164:167], 0\n" \
"v_lshrrev_b32_e32 v232, 1, v231\n" \
"v_mul_u32_u24_e32 v229, 0xd80, v228\n" \
"v_mfma_f32_16x16x32_bf16 a[120:123], v[80:83], v[168:171], a[120:123]\n" \
"v_mul_u32_u24_e32 v233, 0x480, v232\n" \
"s_waitcnt lgkmcnt(0)\n" \
"v_mfma_f32_16x16x32_bf16 a[28:31], v[56:59], v[176:179], 0\n" \
"v_and_b32_e32 v235, 1, v231\n" \
"v_add_u32_e32 v234, v229, v233\n" \
"v_mfma_f32_16x16x32_bf16 a[28:31], v[52:55], v[172:175], a[28:31]\n" \
"v_lshlrev_b32_e32 v236, 4, v235\n" \
"v_add_u32_e32 v237, v234, v236\n" \
"v_mfma_f32_16x16x32_bf16 a[60:63], v[60:63], v[172:175], 0\n" \
"v_cmp_gt_u32_e32 vcc, 0x1b0, v225\n" \
"s_mul_i32 s98, 18, %[w]\n" \
"v_mfma_f32_16x16x32_bf16 a[60:63], v[64:67], v[176:179], a[60:63]\n" \
"v_cndmask_b32_e32 v193, 0x40000000, v237, vcc\n" \
"s_add_u32 s99, s95, s98\n" \
"v_mfma_f32_16x16x32_bf16 a[92:95], v[72:75], v[176:179], 0\n" \
"v_add_u32_e32 v238, s99, v212\n" \
"v_mul_u32_u24_e32 v239, 0xd80, v238\n" \
"v_mfma_f32_16x16x32_bf16 a[92:95], v[68:71], v[172:175], a[92:95]\n" \
"v_cmp_gt_u32_e32 vcc, 18, v212\n" \
"v_lshlrev_b32_e32 v240, 1, v238\n" \
"v_mfma_f32_16x16x32_bf16 a[124:127], v[76:79], v[172:175], 0\n" \
"v_cndmask_b32_e32 v194, 0x40000000, v239, vcc\n" \
"v_cmp_gt_u32_e32 vcc, 18, v212\n" \
"v_mfma_f32_16x16x32_bf16 a[124:127], v[80:83], v[176:179], a[124:127]\n" \
"v_lshrrev_b32_e32 v230, 1, v238\n" \
"v_cndmask_b32_e32 v195, 0x40000000, v240, vcc\n" \
"v_mfma_f32_16x16x32_bf16 a[144:147], v[88:91], v[152:155], 0\n" \
"v_and_b32_e32 v227, 0xff8, v238\n" \
"v_and_b32_e64 v232, 2, v230\n" \
"v_mfma_f32_16x16x32_bf16 a[144:147], v[84:87], v[148:151], a[144:147]\n" \
"v_add_u32_e32 v229, v227, v232\n" \
"v_and_b32_e32 v233, 1, v238\n" \
"v_mfma_f32_16x16x32_bf16 a[176:179], v[92:95], v[148:151], 0\n" \
"v_add_u32_e32 v235, v229, v233\n" \
"v_lshrrev_b32_e32 v242, 1, v216\n" \
"v_mfma_f32_16x16x32_bf16 a[176:179], v[96:99], v[152:155], a[176:179]\n" \
"v_lshrrev_b32_e32 v244, 2, v216\n" \
"v_lshrrev_b32_e32 v214, 1, v228\n" \
"v_mfma_f32_16x16x32_bf16 a[208:211], v[104:107], v[152:155], 0\n" \
"v_lshrrev_b32_e32 v213, 2, v228\n" \
"v_lshrrev_b32_e32 v236, 1, v235\n" \
"v_mfma_f32_16x16x32_bf16 a[208:211], v[100:103], v[148:151], a[208:211]\n" \
"v_lshrrev_b32_e32 v239, 2, v235\n" \
"s_lshl_b32 s98, %[w], 10\n" \
"v_mfma_f32_16x16x32_bf16 a[240:243], v[108:111], v[148:151], 0\n" \
"s_mul_i32 s101, 0x12000, s96\n" \
"v_and_b32_e64 v243, 1, v242\n" \
"v_mfma_f32_16x16x32_bf16 a[240:243], v[112:115], v[152:155], a[240:243]\n" \
"v_and_b32_e32 v245, 2, v244\n" \
"v_and_b32_e32 v215, 1, v214\n" \
"v_mfma_f32_16x16x32_bf16 a[148:151], v[88:91], v[160:163], 0\n" \
"v_and_b32_e32 v218, 2, v213\n" \
"v_and_b32_e32 v237, 1, v236\n" \
"v_mfma_f32_16x16x32_bf16 a[148:151], v[84:87], v[156:159], a[148:151]\n" \
"v_and_b32_e64 v240, 2, v239\n" \
"s_add_u32 s99, 0x22000, s98\n" \
"v_mfma_f32_16x16x32_bf16 a[180:183], v[92:95], v[156:159], 0\n" \
"s_add_u32 s89, 0x6000000, s101\n" \
"s_mul_i32 s97, 0x2400, s94\n" \
"v_mfma_f32_16x16x32_bf16 a[180:183], v[96:99], v[160:163], a[180:183]\n" \
"v_lshlrev_b32_e32 v241, 7, v216\n" \
"v_or_b32_e32 v246, v243, v245\n" \
"v_mfma_f32_16x16x32_bf16 a[212:215], v[104:107], v[160:163], 0\n" \
"v_or_b32_e32 v220, v215, v218\n" \
"v_or_b32_e32 v242, v237, v240\n" \
"v_mfma_f32_16x16x32_bf16 a[212:215], v[100:103], v[156:159], a[212:215]\n" \
"v_add_u32_e32 v199, s99, v20\n" \
"s_add_u32 s37, s89, s98\n" \
"v_mfma_f32_16x16x32_bf16 a[244:247], v[108:111], v[156:159], 0\n" \
"v_lshlrev_b32_e32 v247, 1, v246\n" \
"v_lshlrev_b32_e32 v217, 1, v220\n" \
"v_mfma_f32_16x16x32_bf16 a[244:247], v[112:115], v[160:163], a[244:247]\n" \
"v_xor_b32_e32 v216, 3, v242\n" \
"s_add_u32 s57, s37, s97\n" \
"v_mfma_f32_16x16x32_bf16 a[152:155], v[88:91], v[168:171], 0\n" \
"s_lshl_b32 s99, s88, 2\n" \
"v_xor_b32_e32 v248, v219, v247\n" \
"v_mfma_f32_16x16x32_bf16 a[152:155], v[84:87], v[164:167], a[152:155]\n" \
"v_xor_b32_e32 v221, v231, v217\n" \
"v_lshlrev_b32_e32 v234, 7, v235\n" \
"v_mfma_f32_16x16x32_bf16 a[184:187], v[92:95], v[164:167], 0\n" \
"v_lshlrev_b32_e32 v244, 5, v216\n" \
"v_and_b32_e32 v245, 2, v238\n" \
"v_mfma_f32_16x16x32_bf16 a[184:187], v[96:99], v[168:171], a[184:187]\n" \
"s_add_u32 s94, 0x2000, s57\n" \
"s_add_u32 s96, s99, %[w]\n" \
"v_mfma_f32_16x16x32_bf16 a[216:219], v[104:107], v[168:171], 0\n" \
"s_lshl_b32 s89, %[b], 2\n" \
"s_sub_u32 s95, 0x22000, s97\n" \
"v_mfma_f32_16x16x32_bf16 a[216:219], v[100:103], v[164:167], a[216:219]\n" \
"v_lshlrev_b32_e32 v249, 4, v248\n" \
"v_lshlrev_b32_e32 v251, 7, v228\n" \
"v_mfma_f32_16x16x32_bf16 a[248:251], v[108:111], v[164:167], 0\n" \
"v_lshlrev_b32_e32 v223, 4, v221\n" \
"v_add_u32_e32 v243, v234, v244\n" \
"v_mfma_f32_16x16x32_bf16 a[248:251], v[112:115], v[168:171], a[248:251]\n" \
"v_lshlrev_b32_e32 v246, 1, v245\n" \
"s_mul_i32 s101, 36, s96\n" \
"v_mfma_f32_16x16x32_bf16 a[156:159], v[88:91], v[176:179], 0\n" \
"s_cmp_eq_u32 %[w], 0\n" \
"s_cselect_b32 s58, s94, 0x40000000\n" \
"v_mfma_f32_16x16x32_bf16 a[156:159], v[84:87], v[172:175], a[156:159]\n" \
"s_add_u32 s98, s89, %[w]\n" \
"v_add_u32_e32 v250, v241, v249\n" \
"v_mfma_f32_16x16x32_bf16 a[188:191], v[92:95], v[172:175], 0\n" \
"v_add_u32_e32 v222, v251, v223\n" \
"v_add_u32_e32 v219, v243, v246\n" \
"v_mfma_f32_16x16x32_bf16 a[188:191], v[96:99], v[176:179], a[188:191]\n" \
"v_add_u32_e32 v248, s101, v212\n" \
"s_mul_i32 s97, 18, s98\n" \
"v_mfma_f32_16x16x32_bf16 a[220:223], v[104:107], v[176:179], 0\n" \
"v_add_u32_e32 v196, s95, v250\n" \
"v_add_u32_e32 v224, s95, v222\n" \
"v_mfma_f32_16x16x32_bf16 a[220:223], v[100:103], v[172:175], a[220:223]\n" \
"v_add_u32_e32 v247, s95, v219\n" \
"v_lshrrev_b32_e32 v241, 3, v248\n" \
"v_mfma_f32_16x16x32_bf16 a[252:255], v[108:111], v[172:175], 0\n" \
"s_add_u32 s94, 0xffffffdc, s97\n" \
"v_min_u32_e32 v213, 53, v212\n" \
"v_mfma_f32_16x16x32_bf16 a[252:255], v[112:115], v[176:179], a[252:255]\n" \
"s_lshl_b32 s95, s100, 3\n" \
"v_lshlrev_b32_e32 v249, 6, v241\n" \
"v_mfma_f32_16x16x32_bf16 a[128:131], v[120:123], v[88:91], 0\n" \
"v_add_u32_e32 v215, s94, v213\n" \
"s_and_b32 s97, 7, %[b]\n" \
"v_mfma_f32_16x16x32_bf16 a[128:131], v[116:119], v[84:87], a[128:131]\n" \
"s_lshr_b32 s99, %[b], 3\n" \
"v_add_u32_e64 v250, s95, v249\n" \
"v_mfma_f32_16x16x32_bf16 a[160:163], v[116:119], v[92:95], 0\n" \
"s_lshr_b32 s94, %[b], 5\n" \
"s_lshl_b32 s98, s97, 2\n" \
"v_mfma_f32_16x16x32_bf16 a[160:163], v[120:123], v[96:99], a[160:163]\n" \
"s_and_b32 s100, 3, s99\n" \
"s_mul_i32 s95, 0xa00, s94\n" \
"v_mfma_f32_16x16x32_bf16 a[192:195], v[120:123], v[104:107], 0\n" \
"s_add_u32 s101, s98, s100\n" \
"s_add_u32 s96, 0x5bc1000, s95\n" \
"v_mfma_f32_16x16x32_bf16 a[192:195], v[116:119], v[100:103], a[192:195]\n" \
"s_lshl_b32 s88, s101, 2\n" \
"s_add_u32 s89, s96, s88\n" \
"v_mfma_f32_16x16x32_bf16 a[224:227], v[116:119], v[108:111], 0\n" \
"s_cmp_eq_u32 %[w], 0\n" \
"s_cselect_b32 s95, s89, 0x40000000\n" \
"v_mfma_f32_16x16x32_bf16 a[224:227], v[120:123], v[112:115], a[224:227]\n" \
"s_lshl_b32 s99, s94, 3\n" \
"s_add_u32 s88, s99, s97\n" \
"v_mfma_f32_16x16x32_bf16 a[132:135], v[128:131], v[88:91], 0\n" \
"s_add_u32 s62, s22, 0x100\n" \
"s_and_b32 s62, s62, 0x3ff\n" \
"v_mfma_f32_16x16x32_bf16 a[132:135], v[124:127], v[84:87], a[132:135]\n" \
"s_lshl_b32 s63, s62, 12\n" \
"v_writelane_b32 v11, s95, 0\n" \
"v_mfma_f32_16x16x32_bf16 a[164:167], v[124:127], v[92:95], 0\n" \
"s_lshl_b32 s89, s88, 10\n" \
"s_add_u32 s95, 0x5c00000, s89\n" \
"v_mfma_f32_16x16x32_bf16 a[164:167], v[128:131], v[96:99], a[164:167]\n" \
"s_lshl_b32 s99, %[w], 8\n" \
"s_add_u32 s88, s95, s99\n" \
"v_mfma_f32_16x16x32_bf16 a[196:199], v[128:131], v[104:107], 0\n" \
"v_add_u32_e32 v226, 0x24400, v20\n" \
"v_cmp_gt_u32_e32 vcc, 0x1b0, v225\n" \
"v_mfma_f32_16x16x32_bf16 a[196:199], v[124:127], v[100:103], a[196:199]\n" \
"s_or_b32 s89, 0x200, s88\n" \
"s_lshl_b32 s95, s100, 2\n" \
"v_mfma_f32_16x16x32_bf16 a[228:231], v[124:127], v[108:111], 0\n" \
"v_cndmask_b32_e32 v197, v226, v224, vcc\n" \
"v_writelane_b32 v11, s89, 1\n" \
"v_mfma_f32_16x16x32_bf16 a[228:231], v[128:131], v[112:115], a[228:231]\n" \
"s_add_u32 s99, s88, s95\n" \
"v_cmp_gt_u32_e32 vcc, 18, v212\n" \
"s_waitcnt vmcnt(1)\n" \
"s_barrier\n" \
"v_mfma_f32_16x16x32_bf16 a[136:139], v[136:139], v[88:91], 0\n" \
"ds_read_b64_tr_b16 v[152:153], v32 offset:57344\n" \
"ds_read_b64_tr_b16 v[154:155], v32 offset:58368\n" \
"v_mfma_f32_16x16x32_bf16 a[136:139], v[132:135], v[84:87], a[136:139]\n" \
"ds_read_b128 v[56:59], v29 offset:49152\n" \
"s_add_u32 m0, %[mA], 0x0\n" \
"v_mfma_f32_16x16x32_bf16 a[168:171], v[132:135], v[92:95], 0\n" \
"s_add_u32 s85, s62, 0x0\n" \
"ds_read_b64_tr_b16 v[148:149], v32 offset:49152\n" \
"v_mfma_f32_16x16x32_bf16 a[168:171], v[136:139], v[96:99], a[168:171]\n" \
"buffer_load_dwordx4 v22, s[64:67], s85 offen lds\n" \
"ds_read_b64_tr_b16 v[150:151], v32 offset:50176\n" \
"ds_read_b128 v[52:55], v28 offset:49152\n" \
"v_mfma_f32_16x16x32_bf16 a[200:203], v[136:139], v[104:107], 0\n" \
"ds_read_b128 v[60:63], v28 offset:51200\n" \
"ds_read_b128 v[64:67], v29 offset:51200\n" \
"L_pf0:\n" \
"s_cbranch_execz L_pf4\n" \
"s_nop 0\n" \
"v_mfma_f32_16x16x32_bf16 a[200:203], v[132:135], v[100:103], a[200:203]\n" \
"s_add_u32 m0, %[mA], 0x400\n" \
"s_add_u32 s84, s62, 0x2000\n" \
"v_mfma_f32_16x16x32_bf16 a[232:235], v[132:135], v[108:111], 0\n" \
"buffer_load_dwordx4 v23, s[64:67], s84 offen lds\n" \
"ds_read_b128 v[72:75], v29 offset:53248\n" \
"v_mfma_f32_16x16x32_bf16 a[232:235], v[136:139], v[112:115], a[232:235]\n" \
"ds_read_b128 v[68:71], v28 offset:53248\n" \
"ds_read_b128 v[76:79], v28 offset:55296\n" \
"v_mfma_f32_16x16x32_bf16 a[140:143], v[144:147], v[88:91], 0\n" \
"ds_read_b128 v[80:83], v29 offset:55296\n" \
"s_add_u32 m0, %[mB], 0x0\n" \
"v_mfma_f32_16x16x32_bf16 a[140:143], v[140:143], v[84:87], a[140:143]\n" \
"s_add_u32 s85, s63, 0x0\n" \
"ds_read_b64_tr_b16 v[160:161], v33 offset:57344\n" \
"v_mfma_f32_16x16x32_bf16 a[172:175], v[140:143], v[92:95], 0\n" \
"buffer_load_dwordx4 v26, s[68:71], s85 offen lds\n" \
"ds_read_b64_tr_b16 v[162:163], v33 offset:58368\n" \
"v_mfma_f32_16x16x32_bf16 a[172:175], v[144:147], v[96:99], a[172:175]\n" \
"ds_read_b64_tr_b16 v[156:157], v33 offset:49152\n" \
"ds_read_b64_tr_b16 v[158:159], v33 offset:50176\n" \
"v_mfma_f32_16x16x32_bf16 a[204:207], v[144:147], v[104:107], 0\n" \
"s_add_u32 m0, %[mB], 0x4000\n" \
"s_add_u32 s84, s63, 0x0\n" \
"v_mfma_f32_16x16x32_bf16 a[204:207], v[140:143], v[100:103], a[204:207]\n" \
"buffer_load_dwordx4 v26, s[72:75], s84 offen lds\n" \
"v_mfma_f32_16x16x32_bf16 a[236:239], v[140:143], v[108:111], 0\n" \
"s_add_u32 m0, %[mA], 0x800\n" \
"s_add_u32 s85, s62, 0x4000\n" \
"v_mfma_f32_16x16x32_bf16 a[236:239], v[144:147], v[112:115], a[236:239]\n" \
"ds_read_b64_tr_b16 v[168:169], v34 offset:57344\n" \
"s_waitcnt lgkmcnt(13)\n" \
"v_mfma_f32_16x16x32_bf16 a[16:19], v[56:59], v[152:155], a[16:19]\n" \
"ds_read_b64_tr_b16 v[170:171], v34 offset:58368\n" \
"s_waitcnt lgkmcnt(12)\n" \
"v_mfma_f32_16x16x32_bf16 a[16:19], v[52:55], v[148:151], a[16:19]\n" \
"ds_read_b64_tr_b16 v[164:165], v34 offset:49152\n" \
"s_waitcnt lgkmcnt(12)\n" \
"v_mfma_f32_16x16x32_bf16 a[48:51], v[60:63], v[148:151], a[48:51]\n" \
"ds_read_b64_tr_b16 v[166:167], v34 offset:50176\n" \
"buffer_load_dwordx4 v22, s[64:67], s85 offen lds\n" \
"s_waitcnt lgkmcnt(12)\n" \
"v_mfma_f32_16x16x32_bf16 a[48:51], v[64:67], v[152:155], a[48:51]\n" \
"s_add_u32 m0, %[mA], 0xc00\n" \
"s_waitcnt lgkmcnt(11)\n" \
"v_mfma_f32_16x16x32_bf16 a[80:83], v[72:75], v[152:155], a[80:83]\n" \
"s_add_u32 s84, s62, 0x6000\n" \
"s_waitcnt lgkmcnt(10)\n" \
"v_mfma_f32_16x16x32_bf16 a[80:83], v[68:71], v[148:151], a[80:83]\n" \
"s_add_u32 s85, s63, 0x8000\n" \
"buffer_load_dwordx4 v23, s[64:67], s84 offen lds\n" \
"s_waitcnt lgkmcnt(9)\n" \
"v_mfma_f32_16x16x32_bf16 a[112:115], v[76:79], v[148:151], a[112:115]\n" \
"s_add_u32 m0, %[mB], 0x400\n" \
"s_waitcnt lgkmcnt(8)\n" \
"v_mfma_f32_16x16x32_bf16 a[112:115], v[80:83], v[152:155], a[112:115]\n" \
"ds_read_b64_tr_b16 v[176:177], v35 offset:57344\n" \
"s_waitcnt lgkmcnt(7)\n" \
"v_mfma_f32_16x16x32_bf16 a[20:23], v[56:59], v[160:163], a[20:23]\n" \
"ds_read_b64_tr_b16 v[178:179], v35 offset:58368\n" \
"s_waitcnt lgkmcnt(6)\n" \
"s_nop 0\n" \
"v_mfma_f32_16x16x32_bf16 a[20:23], v[52:55], v[156:159], a[20:23]\n" \
"buffer_load_dwordx4 v26, s[68:71], s85 offen lds\n" \
"v_mfma_f32_16x16x32_bf16 a[52:55], v[60:63], v[156:159], a[52:55]\n" \
"ds_read_b64_tr_b16 v[172:173], v35 offset:49152\n" \
"ds_read_b64_tr_b16 v[174:175], v35 offset:50176\n" \
"v_mfma_f32_16x16x32_bf16 a[52:55], v[64:67], v[160:163], a[52:55]\n" \
"s_add_u32 m0, %[mB], 0x4400\n" \
"s_add_u32 s84, s63, 0x8000\n" \
"v_mfma_f32_16x16x32_bf16 a[84:87], v[72:75], v[160:163], a[84:87]\n" \
"buffer_load_dwordx4 v26, s[72:75], s84 offen lds\n" \
"v_mfma_f32_16x16x32_bf16 a[84:87], v[68:71], v[156:159], a[84:87]\n" \
"s_add_u32 m0, %[mA], 0x1000\n" \
"s_add_u32 s85, s62, 0x8000\n" \
"v_mfma_f32_16x16x32_bf16 a[116:119], v[76:79], v[156:159], a[116:119]\n" \
"ds_read_b64_tr_b16 v[120:121], v32 offset:40960\n" \
"ds_read_b64_tr_b16 v[122:123], v32 offset:41984\n" \
"v_mfma_f32_16x16x32_bf16 a[116:119], v[80:83], v[160:163], a[116:119]\n" \
"ds_read_b64_tr_b16 v[116:117], v32 offset:32768\n" \
"s_waitcnt lgkmcnt(7)\n" \
"s_nop 0\n" \
"v_mfma_f32_16x16x32_bf16 a[24:27], v[56:59], v[168:171], a[24:27]\n" \
"buffer_load_dwordx4 v22, s[64:67], s85 offen lds\n" \
"v_mfma_f32_16x16x32_bf16 a[24:27], v[52:55], v[164:167], a[24:27]\n" \
"ds_read_b64_tr_b16 v[118:119], v32 offset:33792\n" \
"s_add_u32 m0, %[mA], 0x1400\n" \
"v_mfma_f32_16x16x32_bf16 a[56:59], v[60:63], v[164:167], a[56:59]\n" \
"s_add_u32 s84, s62, 0xa000\n" \
"ds_read_b64_tr_b16 v[128:129], v33 offset:40960\n" \
"v_mfma_f32_16x16x32_bf16 a[56:59], v[64:67], v[168:171], a[56:59]\n" \
"buffer_load_dwordx4 v23, s[64:67], s84 offen lds\n" \
"v_mfma_f32_16x16x32_bf16 a[88:91], v[72:75], v[168:171], a[88:91]\n" \
"s_add_u32 m0, %[mB], 0x800\n" \
"s_add_u32 s85, s63, 0x10000\n" \
"v_mfma_f32_16x16x32_bf16 a[88:91], v[68:71], v[164:167], a[88:91]\n" \
"ds_read_b64_tr_b16 v[130:131], v33 offset:41984\n" \
"ds_read_b64_tr_b16 v[124:125], v33 offset:32768\n" \
"v_mfma_f32_16x16x32_bf16 a[120:123], v[76:79], v[164:167], a[120:123]\n" \
"buffer_load_dwordx4 v27, s[68:71], s85 offen lds\n" \
"v_mfma_f32_16x16x32_bf16 a[120:123], v[80:83], v[168:171], a[120:123]\n" \
"ds_read_b64_tr_b16 v[126:127], v33 offset:33792\n" \
"s_waitcnt lgkmcnt(10)\n" \
"v_mfma_f32_16x16x32_bf16 a[28:31], v[56:59], v[176:179], a[28:31]\n" \
"s_add_u32 m0, %[mB], 0x4800\n" \
"s_waitcnt lgkmcnt(8)\n" \
"v_mfma_f32_16x16x32_bf16 a[28:31], v[52:55], v[172:175], a[28:31]\n" \
"s_add_u32 s84, s63, 0x10000\n" \
"ds_read_b64_tr_b16 v[136:137], v34 offset:40960\n" \
"v_mfma_f32_16x16x32_bf16 a[60:63], v[60:63], v[172:175], a[60:63]\n" \
"buffer_load_dwordx4 v27, s[72:75], s84 offen lds\n" \
"v_mfma_f32_16x16x32_bf16 a[60:63], v[64:67], v[176:179], a[60:63]\n" \
"ds_read_b64_tr_b16 v[138:139], v34 offset:41984\n" \
"ds_read_b64_tr_b16 v[132:133], v34 offset:32768\n" \
"v_mfma_f32_16x16x32_bf16 a[92:95], v[72:75], v[176:179], a[92:95]\n" \
"s_add_u32 m0, %[mA], 0x1800\n" \
"s_add_u32 s85, s62, 0xc000\n" \
"v_mfma_f32_16x16x32_bf16 a[92:95], v[68:71], v[172:175], a[92:95]\n" \
"buffer_load_dwordx4 v22, s[64:67], s85 offen lds\n" \
"v_mfma_f32_16x16x32_bf16 a[124:127], v[76:79], v[172:175], a[124:127]\n" \
"ds_read_b64_tr_b16 v[134:135], v34 offset:33792\n" \
"ds_read_b64_tr_b16 v[144:145], v35 offset:40960\n" \
"v_mfma_f32_16x16x32_bf16 a[124:127], v[80:83], v[176:179], a[124:127]\n" \
"s_add_u32 m0, %[mA], 0x1c00\n" \
"s_waitcnt lgkmcnt(9)\n" \
"s_nop 0\n" \
"v_mfma_f32_16x16x32_bf16 a[0:3], v[120:123], v[56:59], a[0:3]\n" \
"s_add_u32 s84, s62, 0xe000\n" \
"ds_read_b64_tr_b16 v[146:147], v35 offset:41984\n" \
"v_mfma_f32_16x16x32_bf16 a[0:3], v[116:119], v[52:55], a[0:3]\n" \
"buffer_load_dwordx4 v23, s[64:67], s84 offen lds\n" \
"v_mfma_f32_16x16x32_bf16 a[32:35], v[116:119], v[60:63], a[32:35]\n" \
"ds_read_b64_tr_b16 v[140:141], v35 offset:32768\n" \
"ds_read_b64_tr_b16 v[142:143], v35 offset:33792\n" \
"v_mfma_f32_16x16x32_bf16 a[32:35], v[120:123], v[64:67], a[32:35]\n" \
"s_add_u32 m0, %[mB], 0xc00\n" \
"s_add_u32 s85, s63, 0x18000\n" \
"v_mfma_f32_16x16x32_bf16 a[64:67], v[120:123], v[72:75], a[64:67]\n" \
"buffer_load_dwordx4 v27, s[68:71], s85 offen lds\n" \
"v_mfma_f32_16x16x32_bf16 a[64:67], v[116:119], v[68:71], a[64:67]\n" \
"ds_read_b128 v[88:91], v29 offset:32768\n" \
"ds_read_b128 v[84:87], v28 offset:32768\n" \
"v_mfma_f32_16x16x32_bf16 a[96:99], v[116:119], v[76:79], a[96:99]\n" \
"ds_read_b128 v[92:95], v28 offset:34816\n" \
"ds_read_b128 v[96:99], v29 offset:34816\n" \
"v_mfma_f32_16x16x32_bf16 a[96:99], v[120:123], v[80:83], a[96:99]\n" \
"s_add_u32 m0, %[mB], 0x4c00\n" \
"s_waitcnt lgkmcnt(11)\n" \
"v_mfma_f32_16x16x32_bf16 a[4:7], v[128:131], v[56:59], a[4:7]\n" \
"s_add_u32 s84, s63, 0x18000\n" \
"ds_read_b128 v[104:107], v29 offset:36864\n" \
"v_mfma_f32_16x16x32_bf16 a[4:7], v[124:127], v[52:55], a[4:7]\n" \
"buffer_load_dwordx4 v27, s[72:75], s84 offen lds\n" \
"v_mfma_f32_16x16x32_bf16 a[36:39], v[124:127], v[60:63], a[36:39]\n" \
"ds_read_b128 v[100:103], v28 offset:36864\n" \
"ds_read_b128 v[108:111], v28 offset:38912\n" \
"v_mfma_f32_16x16x32_bf16 a[36:39], v[128:131], v[64:67], a[36:39]\n" \
"s_mov_b32 m0, %[mS]\n" \
"s_mov_b32 s86, s18\n" \
"v_mfma_f32_16x16x32_bf16 a[68:71], v[128:131], v[72:75], a[68:71]\n" \
"s_mov_b32 s87, s19\n" \
"ds_read_b128 v[112:115], v29 offset:38912\n" \
"v_mfma_f32_16x16x32_bf16 a[68:71], v[124:127], v[68:71], a[68:71]\n" \
"v_writelane_b32 v11, s99, 2\n" \
"s_add_u32 s89, 0x80, s88\n" \
"v_mfma_f32_16x16x32_bf16 a[100:103], v[124:127], v[76:79], a[100:103]\n" \
"v_cndmask_b32_e32 v198, v226, v247, vcc\n" \
"v_and_b32_e32 v214, 7, v248\n" \
"v_mfma_f32_16x16x32_bf16 a[100:103], v[128:131], v[80:83], a[100:103]\n" \
"v_writelane_b32 v11, s89, 3\n" \
"s_waitcnt lgkmcnt(10)\n" \
"v_mfma_f32_16x16x32_bf16 a[8:11], v[136:139], v[56:59], a[8:11]\n" \
"s_lshl_b32 s88, s101, 8\n" \
"v_add_u32_e64 v228, v250, v214\n" \
"v_mfma_f32_16x16x32_bf16 a[8:11], v[132:135], v[52:55], a[8:11]\n" \
"v_cmp_gt_u32_e32 vcc, 36, v212\n" \
"s_add_u32 s95, 0x80, s99\n" \
"s_mov_b32 exec_lo, 0x3ffff\n" \
"s_mov_b32 exec_hi, 0x0\n" \
"global_load_lds_dword v16, s[86:87]\n" \
"s_mov_b64 exec, -1\n" \
"v_mfma_f32_16x16x32_bf16 a[40:43], v[132:135], v[60:63], a[40:43]\n" \
"s_add_u32 s39, 0x5be8000, s88\n" \
"s_lshl_b32 s89, s94, 2\n" \
"v_mfma_f32_16x16x32_bf16 a[40:43], v[136:139], v[64:67], a[40:43]\n" \
"v_cndmask_b32_e32 v218, v215, v228, vcc\n" \
"s_add_u32 s99, s39, s89\n" \
"v_mfma_f32_16x16x32_bf16 a[72:75], v[136:139], v[72:75], a[72:75]\n" \
"v_lshrrev_b32_e32 v212, 4, v20\n" \
"v_writelane_b32 v11, s95, 4\n" \
"v_mfma_f32_16x16x32_bf16 a[72:75], v[132:135], v[68:71], a[72:75]\n" \
"s_cmp_eq_u32 %[w], 3\n" \
"s_cselect_b32 s38, s99, 0x40000000\n" \
"v_mfma_f32_16x16x32_bf16 a[104:107], v[132:135], v[76:79], a[104:107]\n" \
"v_lshlrev_b32_e32 v213, 2, v212\n" \
"v_and_b32_e64 v215, 7, v212\n" \
"v_mfma_f32_16x16x32_bf16 a[104:107], v[136:139], v[80:83], a[104:107]\n" \
"s_add_u32 s101, s89, s100\n" \
"v_add_u32_e32 v214, s96, v213\n" \
"v_mfma_f32_16x16x32_bf16 a[12:15], v[144:147], v[56:59], a[12:15]\n" \
"s_add_u32 s95, 0x5be8020, s88\n" \
"s_waitcnt lgkmcnt(1)\n" \
"v_mfma_f32_16x16x32_bf16 a[12:15], v[140:143], v[52:55], a[12:15]\n" \
"v_lshlrev_b32_e32 v216, 2, v215\n" \
"s_mul_i32 s94, 0x90000, s101\n" \
"v_mfma_f32_16x16x32_bf16 a[44:47], v[140:143], v[60:63], a[44:47]\n" \
"v_add_u32_e64 v217, s95, v216\n" \
"s_add_u32 s99, 0x4240000, s94\n" \
"v_mfma_f32_16x16x32_bf16 a[44:47], v[144:147], v[64:67], a[44:47]\n" \
"s_lshl_b32 s96, %[w], 14\n" \
"s_add_u32 s88, s99, s96\n" \
"v_mfma_f32_16x16x32_bf16 a[76:79], v[144:147], v[72:75], a[76:79]\n" \
"s_lshl_b32 s95, s97, 16\n" \
"v_writelane_b32 v11, s39, 52\n" \
"v_mfma_f32_16x16x32_bf16 a[76:79], v[140:143], v[68:71], a[76:79]\n" \
"s_add_u32 s89, s88, s95\n" \
"s_lshl_b32 s101, s97, 11\n" \
"v_mfma_f32_16x16x32_bf16 a[108:111], v[140:143], v[76:79], a[108:111]\n" \
"v_writelane_b32 v11, s89, 37\n" \
"s_add_u32 s100, 0x3800, s89\n" \
"v_mfma_f32_16x16x32_bf16 a[108:111], v[144:147], v[80:83], a[108:111]\n" \
"s_add_u32 s94, s88, s101\n" \
"v_writelane_b32 v11, s100, 51\n" \
"v_mfma_f32_16x16x32_bf16 a[128:131], v[120:123], v[88:91], a[128:131]\n" \
"s_add_u32 s99, 0x80000, s94\n" \
"s_lshl_b32 s96, %[w], 17\n" \
"v_mfma_f32_16x16x32_bf16 a[128:131], v[116:119], v[84:87], a[128:131]\n" \
"v_writelane_b32 v11, s99, 49\n" \
"s_add_u32 s95, s88, s96\n" \
"v_mfma_f32_16x16x32_bf16 a[160:163], v[116:119], v[92:95], a[160:163]\n" \
"s_and_b32 s94, 1, s97\n" \
"s_add_u32 s89, s95, s98\n" \
"v_mfma_f32_16x16x32_bf16 a[160:163], v[120:123], v[96:99], a[160:163]\n" \
"s_lshl_b32 s99, s94, 10\n" \
"s_add_u32 s100, 0x3840, s89\n" \
"v_mfma_f32_16x16x32_bf16 a[192:195], v[120:123], v[104:107], a[192:195]\n" \
"s_add_u32 s96, s88, s99\n" \
"v_writelane_b32 v11, s100, 5\n" \
"v_mfma_f32_16x16x32_bf16 a[192:195], v[116:119], v[100:103], a[192:195]\n" \
"s_lshr_b32 s101, s97, 1\n" \
"s_add_u32 s95, 0x80000, s96\n" \
"v_mfma_f32_16x16x32_bf16 a[224:227], v[116:119], v[108:111], a[224:227]\n" \
"s_add_u32 s89, 1, s97\n" \
"s_waitcnt lgkmcnt(0)\n" \
"v_mfma_f32_16x16x32_bf16 a[224:227], v[120:123], v[112:115], a[224:227]\n" \
"s_cmp_eq_u32 %[w], s101\n" \
"s_cselect_b32 s98, s95, 0x40000000\n" \
"v_mfma_f32_16x16x32_bf16 a[132:135], v[128:131], v[88:91], a[132:135]\n" \
"s_and_b32 s100, 7, s89\n" \
"s_lshl_b32 s94, s100, 16\n" \
"v_mfma_f32_16x16x32_bf16 a[132:135], v[124:127], v[84:87], a[132:135]\n" \
"s_add_u32 s96, 2, s97\n" \
"v_writelane_b32 v11, s98, 38\n" \
"v_mfma_f32_16x16x32_bf16 a[164:167], v[124:127], v[92:95], a[164:167]\n" \
"s_add_u32 s99, s88, s94\n" \
"s_and_b32 s101, 7, s96\n" \
"v_mfma_f32_16x16x32_bf16 a[164:167], v[128:131], v[96:99], a[164:167]\n" \
"s_add_u32 s100, 3, s97\n" \
"v_writelane_b32 v11, s99, 42\n" \
"v_mfma_f32_16x16x32_bf16 a[196:199], v[128:131], v[104:107], a[196:199]\n" \
"s_lshl_b32 s95, s101, 16\n" \
"s_and_b32 s94, 7, s100\n" \
"v_mfma_f32_16x16x32_bf16 a[196:199], v[124:127], v[100:103], a[196:199]\n" \
"s_add_u32 s98, s88, s95\n" \
"s_lshl_b32 s99, s94, 16\n" \
"v_mfma_f32_16x16x32_bf16 a[228:231], v[124:127], v[108:111], a[228:231]\n" \
"s_add_u32 s89, 0x800, s98\n" \
"s_add_u32 s96, s88, s99\n" \
"v_mfma_f32_16x16x32_bf16 a[228:231], v[128:131], v[112:115], a[228:231]\n" \
"s_add_u32 s95, 4, s97\n" \
"v_writelane_b32 v11, s89, 43\n" \
"v_mfma_f32_16x16x32_bf16 a[136:139], v[136:139], v[88:91], a[136:139]\n" \
"s_add_u32 s101, 0x1000, s96\n" \
"s_and_b32 s98, 7, s95\n" \
"v_mfma_f32_16x16x32_bf16 a[136:139], v[132:135], v[84:87], a[136:139]\n" \
"s_add_u32 s99, 5, s97\n" \
"v_writelane_b32 v11, s101, 44\n" \
"v_mfma_f32_16x16x32_bf16 a[168:171], v[132:135], v[92:95], a[168:171]\n" \
"s_lshl_b32 s89, s98, 16\n" \
"s_and_b32 s96, 7, s99\n" \
"v_mfma_f32_16x16x32_bf16 a[168:171], v[136:139], v[96:99], a[168:171]\n" \
"s_add_u32 s100, s88, s89\n" \
"s_lshl_b32 s101, s96, 16\n" \
"v_mfma_f32_16x16x32_bf16 a[200:203], v[136:139], v[104:107], a[200:203]\n" \
"s_add_u32 s94, 0x1800, s100\n" \
"s_add_u32 s95, s88, s101\n" \
"v_mfma_f32_16x16x32_bf16 a[200:203], v[132:135], v[100:103], a[200:203]\n" \
"s_add_u32 s89, 6, s97\n" \
"v_writelane_b32 v11, s94, 45\n" \
"v_mfma_f32_16x16x32_bf16 a[232:235], v[132:135], v[108:111], a[232:235]\n" \
"s_add_u32 s98, 0x2000, s95\n" \
"s_and_b32 s100, 7, s89\n" \
"v_mfma_f32_16x16x32_bf16 a[232:235], v[136:139], v[112:115], a[232:235]\n" \
"s_add_u32 s101, 7, s97\n" \
"v_writelane_b32 v11, s98, 46\n" \
"v_mfma_f32_16x16x32_bf16 a[140:143], v[144:147], v[88:91], a[140:143]\n" \
"s_add_u32 s62, s22, 0x180\n" \
"s_and_b32 s62, s62, 0x3ff\n" \
"v_mfma_f32_16x16x32_bf16 a[140:143], v[140:143], v[84:87], a[140:143]\n" \
"s_lshl_b32 s63, s62, 12\n" \
"s_lshl_b32 s94, s100, 16\n" \
"v_mfma_f32_16x16x32_bf16 a[172:175], v[140:143], v[92:95], a[172:175]\n" \
"s_and_b32 s95, 7, s101\n" \
"s_add_u32 s99, s88, s94\n" \
"v_mfma_f32_16x16x32_bf16 a[172:175], v[144:147], v[96:99], a[172:175]\n" \
"s_lshl_b32 s98, s95, 16\n" \
"s_add_u32 s96, 0x2800, s99\n" \
"v_mfma_f32_16x16x32_bf16 a[204:207], v[144:147], v[104:107], a[204:207]\n" \
"s_add_u32 s89, s88, s98\n" \
"v_writelane_b32 v11, s96, 47\n" \
"v_mfma_f32_16x16x32_bf16 a[204:207], v[140:143], v[100:103], a[204:207]\n" \
"s_add_u32 s100, 0x3000, s89\n" \
"v_writelane_b32 v11, s100, 48\n" \
"v_mfma_f32_16x16x32_bf16 a[236:239], v[140:143], v[108:111], a[236:239]\n" \
"s_mov_b32 s87, 0x5040100\n" \
"v_lshlrev_b32_e32 v188, 7, v218\n" \
"v_mfma_f32_16x16x32_bf16 a[236:239], v[144:147], v[112:115], a[236:239]\n" \
"v_readfirstlane_b32 %[tag], v180\n" \
"s_lshr_b32 s94, %[b], 5\n" \
"s_waitcnt vmcnt(1)\n" \
"s_barrier\n" \
"v_mfma_f32_16x16x32_bf16 a[144:147], v[88:91], v[152:155], a[144:147]\n" \
"s_add_u32 m0, %[mA], 0x8000\n" \
"s_add_u32 s85, s62, 0x0\n" \
"s_nop 0\n" \
"v_mfma_f32_16x16x32_bf16 a[144:147], v[84:87], v[148:151], a[144:147]\n" \
"buffer_load_dwordx4 v22, s[64:67], s85 offen lds\n" \
"v_mfma_f32_16x16x32_bf16 a[176:179], v[92:95], v[148:151], a[176:179]\n" \
"s_add_u32 m0, %[mA], 0x8400\n" \
"s_add_u32 s84, s62, 0x2000\n" \
"v_mfma_f32_16x16x32_bf16 a[176:179], v[96:99], v[152:155], a[176:179]\n" \
"ds_read_b64_tr_b16 v[120:121], v32 offset:8192\n" \
"ds_read_b64_tr_b16 v[122:123], v32 offset:9216\n" \
"v_mfma_f32_16x16x32_bf16 a[208:211], v[104:107], v[152:155], a[208:211]\n" \
"buffer_load_dwordx4 v23, s[64:67], s84 offen lds\n" \
"v_mfma_f32_16x16x32_bf16 a[208:211], v[100:103], v[148:151], a[208:211]\n" \
"ds_read_b128 v[56:59], v29 offset:16384\n" \
"ds_read_b64_tr_b16 v[116:117], v32 offset:0\n" \
"v_mfma_f32_16x16x32_bf16 a[240:243], v[108:111], v[148:151], a[240:243]\n" \
"s_add_u32 m0, %[mB], 0x8000\n" \
"s_add_u32 s85, s63, 0x0\n" \
"v_mfma_f32_16x16x32_bf16 a[240:243], v[112:115], v[152:155], a[240:243]\n" \
"buffer_load_dwordx4 v26, s[68:71], s85 offen lds\n" \
"v_mfma_f32_16x16x32_bf16 a[148:151], v[88:91], v[160:163], a[148:151]\n" \
"ds_read_b64_tr_b16 v[118:119], v32 offset:1024\n" \
"ds_read_b128 v[52:55], v28 offset:16384\n" \
"v_mfma_f32_16x16x32_bf16 a[148:151], v[84:87], v[156:159], a[148:151]\n" \
"ds_read_b128 v[60:63], v28 offset:18432\n" \
"ds_read_b128 v[64:67], v29 offset:18432\n" \
"v_mfma_f32_16x16x32_bf16 a[180:183], v[92:95], v[156:159], a[180:183]\n" \
"s_add_u32 m0, %[mB], 0xc000\n" \
"s_add_u32 s84, s63, 0x0\n" \
"v_mfma_f32_16x16x32_bf16 a[180:183], v[96:99], v[160:163], a[180:183]\n" \
"buffer_load_dwordx4 v26, s[72:75], s84 offen lds\n" \
"v_mfma_f32_16x16x32_bf16 a[212:215], v[104:107], v[160:163], a[212:215]\n" \
"ds_read_b128 v[72:75], v29 offset:20480\n" \
"ds_read_b128 v[68:71], v28 offset:20480\n" \
"v_mfma_f32_16x16x32_bf16 a[212:215], v[100:103], v[156:159], a[212:215]\n" \
"ds_read_b128 v[76:79], v28 offset:22528\n" \
"ds_read_b128 v[80:83], v29 offset:22528\n" \
"v_mfma_f32_16x16x32_bf16 a[244:247], v[108:111], v[156:159], a[244:247]\n" \
"s_add_u32 m0, %[mA], 0x8800\n" \
"s_add_u32 s85, s62, 0x4000\n" \
"v_mfma_f32_16x16x32_bf16 a[244:247], v[112:115], v[160:163], a[244:247]\n" \
"buffer_load_dwordx4 v22, s[64:67], s85 offen lds\n" \
"v_mfma_f32_16x16x32_bf16 a[152:155], v[88:91], v[168:171], a[152:155]\n" \
"ds_read_b64_tr_b16 v[128:129], v33 offset:8192\n" \
"ds_read_b64_tr_b16 v[130:131], v33 offset:9216\n" \
"v_mfma_f32_16x16x32_bf16 a[152:155], v[84:87], v[164:167], a[152:155]\n" \
"s_add_u32 m0, %[mA], 0x8c00\n" \
"s_add_u32 s84, s62, 0x6000\n" \
"v_mfma_f32_16x16x32_bf16 a[184:187], v[92:95], v[164:167], a[184:187]\n" \
"buffer_load_dwordx4 v23, s[64:67], s84 offen lds\n" \
"v_mfma_f32_16x16x32_bf16 a[184:187], v[96:99], v[168:171], a[184:187]\n" \
"ds_read_b64_tr_b16 v[124:125], v33 offset:0\n" \
"ds_read_b64_tr_b16 v[126:127], v33 offset:1024\n" \
"v_mfma_f32_16x16x32_bf16 a[216:219], v[104:107], v[168:171], a[216:219]\n" \
"ds_read_b64_tr_b16 v[136:137], v34 offset:8192\n" \
"ds_read_b64_tr_b16 v[138:139], v34 offset:9216\n" \
"v_mfma_f32_16x16x32_bf16 a[216:219], v[100:103], v[164:167], a[216:219]\n" \
"s_add_u32 m0, %[mB], 0x8400\n" \
"s_add_u32 s85, s63, 0x8000\n" \
"v_mfma_f32_16x16x32_bf16 a[248:251], v[108:111], v[164:167], a[248:251]\n" \
"buffer_load_dwordx4 v26, s[68:71], s85 offen lds\n" \
"v_mfma_f32_16x16x32_bf16 a[248:251], v[112:115], v[168:171], a[248:251]\n" \
"ds_read_b64_tr_b16 v[132:133], v34 offset:0\n" \
"ds_read_b64_tr_b16 v[134:135], v34 offset:1024\n" \
"v_mfma_f32_16x16x32_bf16 a[156:159], v[88:91], v[176:179], a[156:159]\n" \
"ds_read_b64_tr_b16 v[144:145], v35 offset:8192\n" \
"ds_read_b64_tr_b16 v[146:147], v35 offset:9216\n" \
"v_mfma_f32_16x16x32_bf16 a[156:159], v[84:87], v[172:175], a[156:159]\n" \
"s_add_u32 m0, %[mB], 0xc400\n" \
"s_add_u32 s84, s63, 0x8000\n" \
"v_mfma_f32_16x16x32_bf16 a[188:191], v[92:95], v[172:175], a[188:191]\n" \
"buffer_load_dwordx4 v26, s[72:75], s84 offen lds\n" \
"v_mfma_f32_16x16x32_bf16 a[188:191], v[96:99], v[176:179], a[188:191]\n" \
"ds_read_b64_tr_b16 v[140:141], v35 offset:0\n" \
"ds_read_b64_tr_b16 v[142:143], v35 offset:1024\n" \
"v_mfma_f32_16x16x32_bf16 a[220:223], v[104:107], v[176:179], a[220:223]\n" \
"ds_read_b64_tr_b16 v[152:153], v32 offset:24576\n" \
"ds_read_b64_tr_b16 v[154:155], v32 offset:25600\n" \
"v_mfma_f32_16x16x32_bf16 a[220:223], v[100:103], v[172:175], a[220:223]\n" \
"s_add_u32 m0, %[mA], 0x9000\n" \
"s_add_u32 s85, s62, 0x8000\n" \
"v_mfma_f32_16x16x32_bf16 a[252:255], v[108:111], v[172:175], a[252:255]\n" \
"buffer_load_dwordx4 v22, s[64:67], s85 offen lds\n" \
"v_mfma_f32_16x16x32_bf16 a[252:255], v[112:115], v[176:179], a[252:255]\n" \
"ds_read_b64_tr_b16 v[148:149], v32 offset:16384\n" \
"s_waitcnt lgkmcnt(13)\n" \
"s_nop 0\n" \
"v_mfma_f32_16x16x32_bf16 a[0:3], v[120:123], v[56:59], a[0:3]\n" \
"s_add_u32 m0, %[mA], 0x9400\n" \
"s_add_u32 s84, s62, 0xa000\n" \
"v_mfma_f32_16x16x32_bf16 a[0:3], v[116:119], v[52:55], a[0:3]\n" \
"buffer_load_dwordx4 v23, s[64:67], s84 offen lds\n" \
"v_mfma_f32_16x16x32_bf16 a[32:35], v[116:119], v[60:63], a[32:35]\n" \
"ds_read_b64_tr_b16 v[150:151], v32 offset:17408\n" \
"ds_read_b64_tr_b16 v[160:161], v33 offset:24576\n" \
"v_mfma_f32_16x16x32_bf16 a[32:35], v[120:123], v[64:67], a[32:35]\n" \
"ds_read_b64_tr_b16 v[162:163], v33 offset:25600\n" \
"ds_read_b64_tr_b16 v[156:157], v33 offset:16384\n" \
"v_mfma_f32_16x16x32_bf16 a[64:67], v[120:123], v[72:75], a[64:67]\n" \
"s_add_u32 m0, %[mB], 0x8800\n" \
"s_add_u32 s85, s63, 0x10000\n" \
"v_mfma_f32_16x16x32_bf16 a[64:67], v[116:119], v[68:71], a[64:67]\n" \
"buffer_load_dwordx4 v27, s[68:71], s85 offen lds\n" \
"v_mfma_f32_16x16x32_bf16 a[96:99], v[116:119], v[76:79], a[96:99]\n" \
"ds_read_b64_tr_b16 v[158:159], v33 offset:17408\n" \
"ds_read_b64_tr_b16 v[168:169], v34 offset:24576\n" \
"v_mfma_f32_16x16x32_bf16 a[96:99], v[120:123], v[80:83], a[96:99]\n" \
"ds_read_b64_tr_b16 v[170:171], v34 offset:25600\n" \
"ds_read_b64_tr_b16 v[164:165], v34 offset:16384\n" \
"v_mfma_f32_16x16x32_bf16 a[4:7], v[128:131], v[56:59], a[4:7]\n" \
"s_add_u32 m0, %[mB], 0xc800\n" \
"s_waitcnt lgkmcnt(13)\n" \
"s_nop 0\n" \
"v_mfma_f32_16x16x32_bf16 a[4:7], v[124:127], v[52:55], a[4:7]\n" \
"s_add_u32 s84, s63, 0x10000\n" \
"ds_read_b64_tr_b16 v[166:167], v34 offset:17408\n" \
"v_mfma_f32_16x16x32_bf16 a[36:39], v[124:127], v[60:63], a[36:39]\n" \
"buffer_load_dwordx4 v27, s[72:75], s84 offen lds\n" \
"v_mfma_f32_16x16x32_bf16 a[36:39], v[128:131], v[64:67], a[36:39]\n" \
"ds_read_b64_tr_b16 v[176:177], v35 offset:24576\n" \
"ds_read_b64_tr_b16 v[178:179], v35 offset:25600\n" \
"v_mfma_f32_16x16x32_bf16 a[68:71], v[128:131], v[72:75], a[68:71]\n" \
"s_add_u32 m0, %[mA], 0x9800\n" \
"s_add_u32 s85, s62, 0xc000\n" \
"v_mfma_f32_16x16x32_bf16 a[68:71], v[124:127], v[68:71], a[68:71]\n" \
"buffer_load_dwordx4 v22, s[64:67], s85 offen lds\n" \
"v_mfma_f32_16x16x32_bf16 a[100:103], v[124:127], v[76:79], a[100:103]\n" \
"ds_read_b64_tr_b16 v[172:173], v35 offset:16384\n" \
"ds_read_b64_tr_b16 v[174:175], v35 offset:17408\n" \
"v_mfma_f32_16x16x32_bf16 a[100:103], v[128:131], v[80:83], a[100:103]\n" \
"s_add_u32 m0, %[mA], 0x9c00\n" \
"s_add_u32 s84, s62, 0xe000\n" \
"v_mfma_f32_16x16x32_bf16 a[8:11], v[136:139], v[56:59], a[8:11]\n" \
"buffer_load_dwordx4 v23, s[64:67], s84 offen lds\n" \
"v_mfma_f32_16x16x32_bf16 a[8:11], v[132:135], v[52:55], a[8:11]\n" \
"ds_read_b128 v[88:91], v29 offset:0\n" \
"ds_read_b128 v[84:87], v28 offset:0\n" \
"v_mfma_f32_16x16x32_bf16 a[40:43], v[132:135], v[60:63], a[40:43]\n" \
"ds_read_b128 v[92:95], v28 offset:2048\n" \
"ds_read_b128 v[96:99], v29 offset:2048\n" \
"v_mfma_f32_16x16x32_bf16 a[40:43], v[136:139], v[64:67], a[40:43]\n" \
"s_add_u32 m0, %[mB], 0x8c00\n" \
"s_add_u32 s85, s63, 0x18000\n" \
"v_mfma_f32_16x16x32_bf16 a[72:75], v[136:139], v[72:75], a[72:75]\n" \
"buffer_load_dwordx4 v27, s[68:71], s85 offen lds\n" \
"v_mfma_f32_16x16x32_bf16 a[72:75], v[132:135], v[68:71], a[72:75]\n" \
"ds_read_b128 v[104:107], v29 offset:4096\n" \
"ds_read_b128 v[100:103], v28 offset:4096\n" \
"v_mfma_f32_16x16x32_bf16 a[104:107], v[132:135], v[76:79], a[104:107]\n" \
"ds_read_b128 v[108:111], v28 offset:6144\n" \
"ds_read_b128 v[112:115], v29 offset:6144\n" \
"v_mfma_f32_16x16x32_bf16 a[104:107], v[136:139], v[80:83], a[104:107]\n" \
"s_add_u32 m0, %[mB], 0xcc00\n" \
"s_add_u32 s84, s63, 0x18000\n" \
"v_mfma_f32_16x16x32_bf16 a[12:15], v[144:147], v[56:59], a[12:15]\n" \
"s_add_u32 %[tag], %[tag], 0x9E3779B1\n" \
"s_waitcnt lgkmcnt(12)\n" \
"v_mfma_f32_16x16x32_bf16 a[12:15], v[140:143], v[52:55], a[12:15]\n" \
"buffer_load_dwordx4 v27, s[72:75], s84 offen lds\n" \
"v_mfma_f32_16x16x32_bf16 a[44:47], v[140:143], v[60:63], a[44:47]\n" \
"v_mov_b32_e32 v45, %[tag]\n" \
"s_sub_u32 s95, 15, s94\n" \
"v_mfma_f32_16x16x32_bf16 a[44:47], v[144:147], v[64:67], a[44:47]\n" \
"s_cmp_lt_u32 %[w], 2\n" \
"s_cselect_b32 s96, s95, s94\n" \
"v_mfma_f32_16x16x32_bf16 a[76:79], v[144:147], v[72:75], a[76:79]\n" \
"buffer_load_dwordx4 v[200:203], v192, s[76:79], s56 offen offset:0\n" \
"v_mfma_f32_16x16x32_bf16 a[76:79], v[140:143], v[68:71], a[76:79]\n" \
"s_lshl_b32 s97, s96, 7\n" \
"s_add_u32 s98, 0xfffffff0, s97\n" \
"v_mfma_f32_16x16x32_bf16 a[108:111], v[140:143], v[76:79], a[108:111]\n" \
"s_cmp_eq_u32 s97, 0\n" \
"s_cselect_b32 s99, 0, s98\n" \
"v_mfma_f32_16x16x32_bf16 a[108:111], v[144:147], v[80:83], a[108:111]\n" \
"s_and_b32 s100, 1, %[w]\n" \
"v_cmp_gt_u32_e32 vcc, 32, v212\n" \
"v_mfma_f32_16x16x32_bf16 a[16:19], v[56:59], v[152:155], a[16:19]\n" \
"s_lshl_b32 s101, s100, 3\n" \
"s_add_u32 s88, s99, s101\n" \
"v_mfma_f32_16x16x32_bf16 a[16:19], v[52:55], v[148:151], a[16:19]\n" \
"buffer_load_dwordx4 v[204:207], v193, s[76:79], s56 offen offset:0\n" \
"v_mfma_f32_16x16x32_bf16 a[48:51], v[60:63], v[148:151], a[48:51]\n" \
"v_lshrrev_b32_e32 v212, 4, v20\n" \
"v_cndmask_b32_e32 v17, v217, v214, vcc\n" \
"v_mfma_f32_16x16x32_bf16 a[48:51], v[64:67], v[152:155], a[48:51]\n" \
"v_lshrrev_b32_e32 v213, 3, v212\n" \
"s_lshr_b32 s99, %[b], 3\n" \
"v_mfma_f32_16x16x32_bf16 a[80:83], v[72:75], v[152:155], a[80:83]\n" \
"v_add_u32_e32 v214, s88, v213\n" \
"s_lshl_b32 s96, s100, 6\n" \
"v_mfma_f32_16x16x32_bf16 a[80:83], v[68:71], v[148:151], a[80:83]\n" \
"s_and_b32 s101, 3, s99\n" \
"s_add_u32 s98, s97, s96\n" \
"v_mfma_f32_16x16x32_bf16 a[112:115], v[76:79], v[148:151], a[112:115]\n" \
"s_add_u32 s88, s101, s94\n" \
"v_add_u32_e64 v221, s98, v213\n" \
"v_mfma_f32_16x16x32_bf16 a[112:115], v[80:83], v[152:155], a[112:115]\n" \
"buffer_load_dword v208, v194, s[76:79], s56 offen offset:32\n" \
"v_mfma_f32_16x16x32_bf16 a[20:23], v[56:59], v[160:163], a[20:23]\n" \
"s_add_u32 s96, 0xfffffff7, s88\n" \
"s_lshl_b32 s89, s100, 2\n" \
"v_mfma_f32_16x16x32_bf16 a[20:23], v[52:55], v[156:159], a[20:23]\n" \
"s_cmp_lt_u32 s88, 9\n" \
"s_cselect_b32 s98, s88, s96\n" \
"v_mfma_f32_16x16x32_bf16 a[52:55], v[60:63], v[156:159], a[52:55]\n" \
"s_add_u32 s88, s97, s100\n" \
"s_lshl_b32 s23, s98, 7\n" \
"v_mfma_f32_16x16x32_bf16 a[52:55], v[64:67], v[160:163], a[52:55]\n" \
"s_cmp_eq_u32 s88, 0\n" \
"s_cselect_b32 s49, 0xff800000, 0\n" \
"v_mfma_f32_16x16x32_bf16 a[84:87], v[72:75], v[160:163], a[84:87]\n" \
"s_lshr_b32 s96, s97, 4\n" \
"v_lshrrev_b32_e32 v217, 4, v212\n" \
"v_mfma_f32_16x16x32_bf16 a[84:87], v[68:71], v[156:159], a[84:87]\n" \
"buffer_load_dword v209, v194, s[76:79], s56 offen offset:1184\n" \
"v_mfma_f32_16x16x32_bf16 a[116:119], v[76:79], v[156:159], a[116:119]\n" \
"s_add_u32 s98, s96, s89\n" \
"v_add_u32_e64 v218, s89, v217\n" \
"v_mfma_f32_16x16x32_bf16 a[116:119], v[80:83], v[160:163], a[116:119]\n" \
"s_mul_i32 s88, 0x24800, s98\n" \
"s_and_b32 s96, 7, %[b]\n" \
"v_mfma_f32_16x16x32_bf16 a[24:27], v[56:59], v[168:171], a[24:27]\n" \
"s_add_u32 s97, 0x3000000, s88\n" \
"s_lshl_b32 s89, s96, 8\n" \
"v_mfma_f32_16x16x32_bf16 a[24:27], v[52:55], v[164:167], a[24:27]\n" \
"s_add_u32 s98, s97, s89\n" \
"s_lshl_b32 s88, s101, 2\n" \
"v_mfma_f32_16x16x32_bf16 a[56:59], v[60:63], v[164:167], a[56:59]\n" \
"v_and_b32_e32 v216, 7, v212\n" \
"s_add_u32 s89, s98, s88\n" \
"v_mfma_f32_16x16x32_bf16 a[56:59], v[64:67], v[168:171], a[56:59]\n" \
"buffer_load_dword v211, v194, s[76:79], s56 offen offset:2336\n" \
"v_mfma_f32_16x16x32_bf16 a[88:91], v[72:75], v[168:171], a[88:91]\n" \
"s_mov_b32 s76, s14\n" \
"s_and_b32 s77, s15, 0xffff\n" \
"v_mfma_f32_16x16x32_bf16 a[88:91], v[68:71], v[164:167], a[88:91]\n" \
"s_mov_b32 s78, 0x480\n" \
"s_add_u32 s56, s57, 0x1000\n" \
"v_mfma_f32_16x16x32_bf16 a[120:123], v[76:79], v[164:167], a[120:123]\n" \
"v_xor_b32_e32 v219, v216, v218\n" \
"s_lshl_b32 s98, s96, 2\n" \
"v_mfma_f32_16x16x32_bf16 a[120:123], v[80:83], v[168:171], a[120:123]\n" \
"v_mul_u32_u24_e32 v215, 0x480, v214\n" \
"s_waitcnt lgkmcnt(0)\n" \
"v_mfma_f32_16x16x32_bf16 a[28:31], v[56:59], v[176:179], a[28:31]\n" \
"v_lshlrev_b32_e32 v220, 4, v219\n" \
"v_lshrrev_b32_e32 v229, 2, v212\n" \
"v_mfma_f32_16x16x32_bf16 a[28:31], v[52:55], v[172:175], a[28:31]\n" \
"buffer_load_ushort v210, v195, s[76:79], 0 offen\n" \
"v_mfma_f32_16x16x32_bf16 a[60:63], v[60:63], v[172:175], a[60:63]\n" \
"v_and_b32_e32 v234, 3, v212\n" \
"v_and_b32_e32 v249, 2, v212\n" \
"v_mfma_f32_16x16x32_bf16 a[60:63], v[64:67], v[176:179], a[60:63]\n" \
"s_add_u32 s51, 0x4000, s89\n" \
"s_add_u32 s88, s98, s101\n" \
"v_mfma_f32_16x16x32_bf16 a[92:95], v[72:75], v[176:179], a[92:95]\n" \
"v_add_u32_e32 v21, v215, v220\n" \
"v_add_u32_e64 v225, 4, v217\n" \
"v_mfma_f32_16x16x32_bf16 a[92:95], v[68:71], v[172:175], a[92:95]\n" \
"s_lshl_b32 s89, s88, 9\n" \
"s_lshl_b32 s98, s99, 2\n" \
"v_mfma_f32_16x16x32_bf16 a[124:127], v[76:79], v[172:175], a[124:127]\n" \
"v_lshrrev_b32_e32 v212, 4, v20\n" \
"v_xor_b32_e32 v223, v216, v217\n" \
"L_pf1:\n" \
"s_cbranch_execz L_pf5\n" \
"s_nop 0\n" \
"v_mfma_f32_16x16x32_bf16 a[124:127], v[80:83], v[176:179], a[124:127]\n" \
"v_xor_b32_e64 v226, v216, v225\n" \
"v_mov_b32_e32 v250, 0xeeeeeeee\n" \
"v_mfma_f32_16x16x32_bf16 a[144:147], v[88:91], v[152:155], a[144:147]\n" \
"v_mov_b32_e32 v251, 0x44444444\n" \
"v_cmp_ne_u32_e32 vcc, 0, v249\n" \
"v_mfma_f32_16x16x32_bf16 a[144:147], v[84:87], v[148:151], a[144:147]\n" \
"s_add_u32 s50, s97, s89\n" \
"s_lshl_b32 s96, %[w], 10\n" \
"v_mfma_f32_16x16x32_bf16 a[176:179], v[92:95], v[148:151], a[176:179]\n" \
"s_add_u32 s101, s98, %[w]\n" \
"v_and_b32_e64 v215, 15, v212\n" \
"v_mfma_f32_16x16x32_bf16 a[176:179], v[96:99], v[152:155], a[176:179]\n" \
"v_cndmask_b32_e32 v50, v251, v250, vcc\n" \
"s_add_u32 s40, 0x14000, s96\n" \
"v_mfma_f32_16x16x32_bf16 a[208:211], v[104:107], v[152:155], a[208:211]\n" \
"s_lshl_b32 s41, s101, 7\n" \
"s_lshl_b32 s97, s88, 20\n" \
"v_mfma_f32_16x16x32_bf16 a[208:211], v[100:103], v[148:151], a[208:211]\n" \
"v_lshrrev_b32_e32 v213, 4, v212\n" \
"v_and_b32_e64 v216, 1, v215\n" \
"v_mfma_f32_16x16x32_bf16 a[240:243], v[108:111], v[148:151], a[240:243]\n" \
"v_lshlrev_b32_e32 v228, 3, v217\n" \
"v_and_b32_e32 v238, 1, v217\n" \
"v_mfma_f32_16x16x32_bf16 a[240:243], v[112:115], v[152:155], a[240:243]\n" \
"v_lshlrev_b32_e32 v242, 4, v217\n" \
"s_add_u32 s89, 0x1000000, s97\n" \
"v_mfma_f32_16x16x32_bf16 a[148:151], v[88:91], v[160:163], a[148:151]\n" \
"s_add_u32 s62, s22, 0x200\n" \
"s_and_b32 s62, s62, 0x3ff\n" \
"v_mfma_f32_16x16x32_bf16 a[148:151], v[84:87], v[156:159], a[148:151]\n" \
"s_lshl_b32 s63, s62, 12\n" \
"s_lshl_b32 s96, %[w], 12\n" \
"v_mfma_f32_16x16x32_bf16 a[180:183], v[92:95], v[156:159], a[180:183]\n" \
"s_lshr_b32 s101, %[w], 1\n" \
"v_lshlrev_b32_e32 v214, 2, v213\n" \
"v_mfma_f32_16x16x32_bf16 a[180:183], v[96:99], v[160:163], a[180:183]\n" \
"v_mov_b32_e32 v218, 0x1000c0c\n" \
"v_mov_b32_e32 v219, 0xc0c0100\n" \
"v_mfma_f32_16x16x32_bf16 a[212:215], v[104:107], v[160:163], a[212:215]\n" \
"v_cmp_ne_u32_e32 vcc, 0, v216\n" \
"v_mul_u32_u24_e32 v222, 0x480, v221\n" \
"v_mfma_f32_16x16x32_bf16 a[212:215], v[100:103], v[156:159], a[212:215]\n" \
"s_add_u32 s52, s89, s96\n" \
"s_lshl_b32 s99, %[w], 11\n" \
"v_mfma_f32_16x16x32_bf16 a[244:247], v[108:111], v[156:159], a[244:247]\n" \
"s_lshl_b32 s88, s101, 15\n" \
"v_add_u32_e32 v217, v214, v216\n" \
"v_mfma_f32_16x16x32_bf16 a[244:247], v[112:115], v[160:163], a[244:247]\n" \
"v_cndmask_b32_e32 v220, v219, v218, vcc\n" \
"v_lshlrev_b32_e32 v224, 4, v223\n" \
"s_waitcnt vmcnt(6)\n" \
"s_barrier\n" \
"v_mfma_f32_16x16x32_bf16 a[152:155], v[88:91], v[168:171], a[152:155]\n" \
"s_add_u32 m0, %[mA], 0x0\n" \
"s_add_u32 s85, s62, 0x0\n" \
"v_mfma_f32_16x16x32_bf16 a[152:155], v[84:87], v[164:167], a[152:155]\n" \
"buffer_load_dwordx4 v22, s[64:67], s85 offen lds\n" \
"v_mfma_f32_16x16x32_bf16 a[184:187], v[92:95], v[164:167], a[184:187]\n" \
"s_add_u32 m0, %[mA], 0x400\n" \
"s_add_u32 s84, s62, 0x2000\n" \
"v_mfma_f32_16x16x32_bf16 a[184:187], v[96:99], v[168:171], a[184:187]\n" \
"ds_read_b64_tr_b16 v[152:153], v32 offset:57344\n" \
"ds_read_b64_tr_b16 v[154:155], v32 offset:58368\n" \
"v_mfma_f32_16x16x32_bf16 a[216:219], v[104:107], v[168:171], a[216:219]\n" \
"buffer_load_dwordx4 v23, s[64:67], s84 offen lds\n" \
"v_mfma_f32_16x16x32_bf16 a[216:219], v[100:103], v[164:167], a[216:219]\n" \
"ds_read_b128 v[56:59], v29 offset:49152\n" \
"ds_read_b64_tr_b16 v[148:149], v32 offset:49152\n" \
"v_mfma_f32_16x16x32_bf16 a[248:251], v[108:111], v[164:167], a[248:251]\n" \
"s_add_u32 m0, %[mB], 0x0\n" \
"s_add_u32 s85, s63, 0x0\n" \
"v_mfma_f32_16x16x32_bf16 a[248:251], v[112:115], v[168:171], a[248:251]\n" \
"buffer_load_dwordx4 v26, s[68:71], s85 offen lds\n" \
"v_mfma_f32_16x16x32_bf16 a[156:159], v[88:91], v[176:179], a[156:159]\n" \
"ds_read_b64_tr_b16 v[150:151], v32 offset:50176\n" \
"ds_read_b128 v[52:55], v28 offset:49152\n" \
"v_mfma_f32_16x16x32_bf16 a[156:159], v[84:87], v[172:175], a[156:159]\n" \
"ds_read_b128 v[60:63], v28 offset:51200\n" \
"ds_read_b128 v[64:67], v29 offset:51200\n" \
"v_mfma_f32_16x16x32_bf16 a[188:191], v[92:95], v[172:175], a[188:191]\n" \
"s_add_u32 m0, %[mB], 0x4000\n" \
"s_add_u32 s84, s63, 0x0\n" \
"s_nop 0\n" \
"v_mfma_f32_16x16x32_bf16 a[188:191], v[96:99], v[176:179], a[188:191]\n" \
"buffer_load_dwordx4 v26, s[72:75], s84 offen lds\n" \
"v_mfma_f32_16x16x32_bf16 a[220:223], v[104:107], v[176:179], a[220:223]\n" \
"ds_read_b128 v[72:75], v29 offset:53248\n" \
"ds_read_b128 v[68:71], v28 offset:53248\n" \
"v_mfma_f32_16x16x32_bf16 a[220:223], v[100:103], v[172:175], a[220:223]\n" \
"ds_read_b128 v[76:79], v28 offset:55296\n" \
"ds_read_b128 v[80:83], v29 offset:55296\n" \
"v_mfma_f32_16x16x32_bf16 a[252:255], v[108:111], v[172:175], a[252:255]\n" \
"s_add_u32 m0, %[mA], 0x800\n" \
"s_add_u32 s85, s62, 0x4000\n" \
"v_mfma_f32_16x16x32_bf16 a[252:255], v[112:115], v[176:179], a[252:255]\n" \
"buffer_load_dwordx4 v22, s[64:67], s85 offen lds\n" \
"v_mfma_f32_16x16x32_bf16 a[128:131], v[120:123], v[88:91], a[128:131]\n" \
"ds_read_b64_tr_b16 v[160:161], v33 offset:57344\n" \
"ds_read_b64_tr_b16 v[162:163], v33 offset:58368\n" \
"v_mfma_f32_16x16x32_bf16 a[128:131], v[116:119], v[84:87], a[128:131]\n" \
"s_add_u32 m0, %[mA], 0xc00\n" \
"s_add_u32 s84, s62, 0x6000\n" \
"v_mfma_f32_16x16x32_bf16 a[160:163], v[116:119], v[92:95], a[160:163]\n" \
"buffer_load_dwordx4 v23, s[64:67], s84 offen lds\n" \
"v_mfma_f32_16x16x32_bf16 a[160:163], v[120:123], v[96:99], a[160:163]\n" \
"ds_read_b64_tr_b16 v[156:157], v33 offset:49152\n" \
"ds_read_b64_tr_b16 v[158:159], v33 offset:50176\n" \
"v_mfma_f32_16x16x32_bf16 a[192:195], v[120:123], v[104:107], a[192:195]\n" \
"ds_read_b64_tr_b16 v[168:169], v34 offset:57344\n" \
"ds_read_b64_tr_b16 v[170:171], v34 offset:58368\n" \
"v_mfma_f32_16x16x32_bf16 a[192:195], v[116:119], v[100:103], a[192:195]\n" \
"s_add_u32 m0, %[mB], 0x400\n" \
"s_add_u32 s85, s63, 0x8000\n" \
"v_mfma_f32_16x16x32_bf16 a[224:227], v[116:119], v[108:111], a[224:227]\n" \
"buffer_load_dwordx4 v26, s[68:71], s85 offen lds\n" \
"v_mfma_f32_16x16x32_bf16 a[224:227], v[120:123], v[112:115], a[224:227]\n" \
"ds_read_b64_tr_b16 v[164:165], v34 offset:49152\n" \
"ds_read_b64_tr_b16 v[166:167], v34 offset:50176\n" \
"v_mfma_f32_16x16x32_bf16 a[132:135], v[128:131], v[88:91], a[132:135]\n" \
"ds_read_b64_tr_b16 v[176:177], v35 offset:57344\n" \
"ds_read_b64_tr_b16 v[178:179], v35 offset:58368\n" \
"v_mfma_f32_16x16x32_bf16 a[132:135], v[124:127], v[84:87], a[132:135]\n" \
"s_add_u32 m0, %[mB], 0x4400\n" \
"s_add_u32 s84, s63, 0x8000\n" \
"v_mfma_f32_16x16x32_bf16 a[164:167], v[124:127], v[92:95], a[164:167]\n" \
"buffer_load_dwordx4 v26, s[72:75], s84 offen lds\n" \
"v_mfma_f32_16x16x32_bf16 a[164:167], v[128:131], v[96:99], a[164:167]\n" \
"ds_read_b64_tr_b16 v[172:173], v35 offset:49152\n" \
"ds_read_b64_tr_b16 v[174:175], v35 offset:50176\n" \
"v_mfma_f32_16x16x32_bf16 a[196:199], v[128:131], v[104:107], a[196:199]\n" \
"ds_read_b64_tr_b16 v[120:121], v32 offset:40960\n" \
"ds_read_b64_tr_b16 v[122:123], v32 offset:41984\n" \
"v_mfma_f32_16x16x32_bf16 a[196:199], v[124:127], v[100:103], a[196:199]\n" \
"s_add_u32 m0, %[mA], 0x1000\n" \
"s_add_u32 s85, s62, 0x8000\n" \
"v_mfma_f32_16x16x32_bf16 a[228:231], v[124:127], v[108:111], a[228:231]\n" \
"buffer_load_dwordx4 v22, s[64:67], s85 offen lds\n" \
"v_mfma_f32_16x16x32_bf16 a[228:231], v[128:131], v[112:115], a[228:231]\n" \
"ds_read_b64_tr_b16 v[116:117], v32 offset:32768\n" \
"ds_read_b64_tr_b16 v[118:119], v32 offset:33792\n" \
"v_mfma_f32_16x16x32_bf16 a[136:139], v[136:139], v[88:91], a[136:139]\n" \
"s_add_u32 m0, %[mA], 0x1400\n" \
"s_add_u32 s84, s62, 0xa000\n" \
"v_mfma_f32_16x16x32_bf16 a[136:139], v[132:135], v[84:87], a[136:139]\n" \
"buffer_load_dwordx4 v23, s[64:67], s84 offen lds\n" \
"v_mfma_f32_16x16x32_bf16 a[168:171], v[132:135], v[92:95], a[168:171]\n" \
"ds_read_b64_tr_b16 v[128:129], v33 offset:40960\n" \
"ds_read_b64_tr_b16 v[130:131], v33 offset:41984\n" \
"v_mfma_f32_16x16x32_bf16 a[168:171], v[136:139], v[96:99], a[168:171]\n" \
"ds_read_b64_tr_b16 v[124:125], v33 offset:32768\n" \
"ds_read_b64_tr_b16 v[126:127], v33 offset:33792\n" \
"v_mfma_f32_16x16x32_bf16 a[200:203], v[136:139], v[104:107], a[200:203]\n" \
"s_add_u32 m0, %[mB], 0x800\n" \
"s_add_u32 s85, s63, 0x10000\n" \
"v_mfma_f32_16x16x32_bf16 a[200:203], v[132:135], v[100:103], a[200:203]\n" \
"buffer_load_dwordx4 v27, s[68:71], s85 offen lds\n" \
"v_mfma_f32_16x16x32_bf16 a[232:235], v[132:135], v[108:111], a[232:235]\n" \
"ds_read_b64_tr_b16 v[132:133], v34 offset:32768\n" \
"ds_read_b64_tr_b16 v[134:135], v34 offset:33792\n" \
"v_mfma_f32_16x16x32_bf16 a[232:235], v[136:139], v[112:115], a[232:235]\n" \
"ds_read_b64_tr_b16 v[136:137], v34 offset:40960\n" \
"ds_read_b64_tr_b16 v[138:139], v34 offset:41984\n" \
"v_mfma_f32_16x16x32_bf16 a[140:143], v[144:147], v[88:91], a[140:143]\n" \
"s_add_u32 m0, %[mB], 0x4800\n" \
"s_add_u32 s84, s63, 0x10000\n" \
"v_mfma_f32_16x16x32_bf16 a[140:143], v[140:143], v[84:87], a[140:143]\n" \
"buffer_load_dwordx4 v27, s[72:75], s84 offen lds\n" \
"v_mfma_f32_16x16x32_bf16 a[172:175], v[140:143], v[92:95], a[172:175]\n" \
"ds_read_b128 v[84:87], v28 offset:32768\n" \
"ds_read_b128 v[88:91], v29 offset:32768\n" \
"v_mfma_f32_16x16x32_bf16 a[172:175], v[144:147], v[96:99], a[172:175]\n" \
"ds_read_b128 v[92:95], v28 offset:34816\n" \
"ds_read_b128 v[96:99], v29 offset:34816\n" \
"v_mfma_f32_16x16x32_bf16 a[204:207], v[144:147], v[104:107], a[204:207]\n" \
"s_add_u32 m0, %[mA], 0x1800\n" \
"s_add_u32 s85, s62, 0xc000\n" \
"v_mfma_f32_16x16x32_bf16 a[204:207], v[140:143], v[100:103], a[204:207]\n" \
"buffer_load_dwordx4 v22, s[64:67], s85 offen lds\n" \
"v_mfma_f32_16x16x32_bf16 a[236:239], v[140:143], v[108:111], a[236:239]\n" \
"ds_read_b64_tr_b16 v[140:141], v35 offset:32768\n" \
"ds_read_b64_tr_b16 v[142:143], v35 offset:33792\n" \
"v_mfma_f32_16x16x32_bf16 a[236:239], v[144:147], v[112:115], a[236:239]\n" \
"s_add_u32 m0, %[mA], 0x1c00\n" \
"s_waitcnt lgkmcnt(14)\n" \
"s_nop 0\n" \
"v_mfma_f32_16x16x32_bf16 a[16:19], v[56:59], v[152:155], a[16:19]\n" \
"s_add_u32 s84, s62, 0xe000\n" \
"ds_read_b64_tr_b16 v[144:145], v35 offset:40960\n" \
"v_mfma_f32_16x16x32_bf16 a[16:19], v[52:55], v[148:151], a[16:19]\n" \
"buffer_load_dwordx4 v23, s[64:67], s84 offen lds\n" \
"v_mfma_f32_16x16x32_bf16 a[48:51], v[60:63], v[148:151], a[48:51]\n" \
"ds_read_b64_tr_b16 v[146:147], v35 offset:41984\n" \
"ds_read_b128 v[100:103], v28 offset:36864\n" \
"v_mfma_f32_16x16x32_bf16 a[48:51], v[64:67], v[152:155], a[48:51]\n" \
"s_add_u32 m0, %[mB], 0xc00\n" \
"s_add_u32 s85, s63, 0x18000\n" \
"v_mfma_f32_16x16x32_bf16 a[80:83], v[72:75], v[152:155], a[80:83]\n" \
"buffer_load_dwordx4 v27, s[68:71], s85 offen lds\n" \
"v_mfma_f32_16x16x32_bf16 a[80:83], v[68:71], v[148:151], a[80:83]\n" \
"ds_read_b128 v[104:107], v29 offset:36864\n" \
"ds_read_b128 v[108:111], v28 offset:38912\n" \
"v_mfma_f32_16x16x32_bf16 a[112:115], v[76:79], v[148:151], a[112:115]\n" \
"ds_read_b128 v[112:115], v29 offset:38912\n" \
"s_add_u32 s98, s89, s99\n" \
"v_mfma_f32_16x16x32_bf16 a[112:115], v[80:83], v[152:155], a[112:115]\n" \
"s_add_u32 m0, %[mB], 0x4c00\n" \
"s_add_u32 s84, s63, 0x18000\n" \
"v_mfma_f32_16x16x32_bf16 a[20:23], v[56:59], v[160:163], a[20:23]\n" \
"buffer_load_dwordx4 v27, s[72:75], s84 offen lds\n" \
"v_mfma_f32_16x16x32_bf16 a[20:23], v[52:55], v[156:159], a[20:23]\n" \
"s_mov_b32 m0, %[mS]\n" \
"s_add_u32 s97, s89, s88\n" \
"v_mfma_f32_16x16x32_bf16 a[52:55], v[60:63], v[156:159], a[52:55]\n" \
"s_lshl_b32 s96, s100, 11\n" \
"v_mov_b32_e32 v221, 0xc0c0c0c\n" \
"v_mfma_f32_16x16x32_bf16 a[52:55], v[64:67], v[160:163], a[52:55]\n" \
"v_cmp_le_u32_e32 vcc, v217, v215\n" \
"v_add_u32_e32 v24, v222, v224\n" \
"v_mfma_f32_16x16x32_bf16 a[84:87], v[72:75], v[160:163], a[84:87]\n" \
"v_lshlrev_b32_e32 v227, 4, v226\n" \
"s_add_u32 s99, s97, s96\n" \
"v_mfma_f32_16x16x32_bf16 a[84:87], v[68:71], v[156:159], a[84:87]\n" \
"s_lshl_b32 s101, s95, 16\n" \
"v_cndmask_b32_e32 v40, v220, v221, vcc\n" \
"v_mfma_f32_16x16x32_bf16 a[116:119], v[76:79], v[156:159], a[116:119]\n" \
"v_add_u32_e64 v25, v222, v227\n" \
"s_add_u32 s54, 0x10000, s98\n" \
"v_mfma_f32_16x16x32_bf16 a[116:119], v[80:83], v[160:163], a[116:119]\n" \
"s_add_u32 s43, s99, s101\n" \
"s_mul_i32 s89, 0x1800, s100\n" \
"v_mfma_f32_16x16x32_bf16 a[24:27], v[56:59], v[168:171], a[24:27]\n" \
"v_mov_b32_e32 v223, 0x3020c0c\n" \
"v_mov_b32_e32 v224, 0xc0c0302\n" \
"v_mfma_f32_16x16x32_bf16 a[24:27], v[52:55], v[164:167], a[24:27]\n" \
"v_cmp_ne_u32_e32 vcc, 0, v216\n" \
"s_lshl_b32 s98, s94, 16\n" \
"v_mfma_f32_16x16x32_bf16 a[56:59], v[60:63], v[164:167], a[56:59]\n" \
"s_add_u32 s97, s43, s89\n" \
"v_add_u32_e64 v222, 2, v217\n" \
"v_mfma_f32_16x16x32_bf16 a[56:59], v[64:67], v[168:171], a[56:59]\n" \
"v_cndmask_b32_e32 v225, v224, v223, vcc\n" \
"s_add_u32 s42, s99, s98\n" \
"v_mfma_f32_16x16x32_bf16 a[88:91], v[72:75], v[168:171], a[88:91]\n" \
"s_mov_b32 s86, s10\n" \
"s_add_u32 s45, 0x4000, s97\n" \
"v_mfma_f32_16x16x32_bf16 a[88:91], v[68:71], v[164:167], a[88:91]\n" \
"s_lshr_b32 s94, %[w], 1\n" \
"s_and_b32 s95, 1, %[w]\n" \
"s_mov_b32 exec_lo, 0x15\n" \
"s_mov_b32 exec_hi, 0x180000\n" \
"buffer_load_dword v11, s[80:83], 0 offen sc1 lds\n" \
"s_mov_b64 exec, -1\n" \
"v_mfma_f32_16x16x32_bf16 a[120:123], v[76:79], v[164:167], a[120:123]\n" \
"s_mov_b32 m0, %[mS]\n" \
"v_mov_b32_e32 v226, 0xc0c0c0c\n" \
"v_mfma_f32_16x16x32_bf16 a[120:123], v[80:83], v[168:171], a[120:123]\n" \
"v_cmp_le_u32_e32 vcc, v222, v215\n" \
"s_add_u32 s88, s42, s89\n" \
"v_mfma_f32_16x16x32_bf16 a[28:31], v[56:59], v[176:179], a[28:31]\n" \
"s_add_u32 s96, s94, s95\n" \
"s_add_u32 s97, 1, s94\n" \
"v_mfma_f32_16x16x32_bf16 a[28:31], v[52:55], v[172:175], a[28:31]\n" \
"v_cndmask_b32_e32 v41, v225, v226, vcc\n" \
"v_and_b32_e32 v230, 3, v229\n" \
"v_mfma_f32_16x16x32_bf16 a[60:63], v[60:63], v[172:175], a[60:63]\n" \
"s_add_u32 s44, 0x4000, s88\n" \
"s_lshl_b32 s59, s96, 13\n" \
"v_mfma_f32_16x16x32_bf16 a[60:63], v[64:67], v[176:179], a[60:63]\n" \
"s_sub_u32 s98, s97, s95\n" \
"s_lshl_b32 s99, s94, 13\n" \
"v_mfma_f32_16x16x32_bf16 a[92:95], v[72:75], v[176:179], a[92:95]\n" \
"s_mul_i32 s101, 0x1800, s94\n" \
"v_mov_b32_e32 v227, 0xc0c0c0c\n" \
"v_mfma_f32_16x16x32_bf16 a[92:95], v[68:71], v[172:175], a[92:95]\n" \
"v_cmp_le_u32_e32 vcc, v217, v215\n" \
"v_add_u32_e64 v231, v228, v230\n" \
"v_mfma_f32_16x16x32_bf16 a[124:127], v[76:79], v[172:175], a[124:127]\n" \
"s_lshl_b32 s60, s98, 13\n" \
"s_add_u32 s100, 0x1800, s99\n" \
"v_mfma_f32_16x16x32_bf16 a[124:127], v[80:83], v[176:179], a[124:127]\n" \
"s_sub_u32 s88, 0x17000, s101\n" \
"s_lshl_b32 s96, s94, 15\n" \
"v_mfma_f32_16x16x32_bf16 a[0:3], v[120:123], v[56:59], a[0:3]\n" \
"v_cndmask_b32_e32 v42, v227, v220, vcc\n" \
"v_lshlrev_b32_e32 v232, 7, v231\n" \
"v_mfma_f32_16x16x32_bf16 a[0:3], v[116:119], v[52:55], a[0:3]\n" \
"s_cmp_lg_u32 s95, 0\n" \
"s_cselect_b32 s61, s100, s88\n" \
"v_mfma_f32_16x16x32_bf16 a[32:35], v[116:119], v[60:63], a[32:35]\n" \
"s_add_u32 s97, 0x10000, s96\n" \
"s_lshl_b32 s98, s95, 11\n" \
"v_mfma_f32_16x16x32_bf16 a[32:35], v[120:123], v[64:67], a[32:35]\n" \
"v_mov_b32_e32 v228, 0xc0c0c0c\n" \
"v_cmp_le_u32_e32 vcc, v222, v215\n" \
"v_mfma_f32_16x16x32_bf16 a[64:67], v[120:123], v[72:75], a[64:67]\n" \
"v_add_u32_e32 v233, 0x14000, v232\n" \
"v_lshlrev_b32_e32 v235, 3, v234\n" \
"v_mfma_f32_16x16x32_bf16 a[64:67], v[116:119], v[68:71], a[64:67]\n" \
"s_waitcnt vmcnt(19)\n" \
"v_perm_b32 v252, v209, v208, s87\n" \
"s_mov_b32 s87, 0x7060302\n" \
"v_perm_b32 v253, v209, v208, s87\n" \
"s_mov_b32 s87, 0x5040100\n" \
"s_waitcnt vmcnt(17)\n" \
"v_perm_b32 v254, v211, v210, s87\n" \
"s_mov_b32 s87, 0x7060100\n" \
"v_perm_b32 v255, v211, v210, s87\n" \
"s_mov_b32 s87, s11\n" \
"s_mov_b32 exec_lo, 0x3ffff\n" \
"s_mov_b32 exec_hi, 0x0\n" \
"global_load_lds_dword v188, s[86:87]\n" \
"s_mov_b64 exec, -1\n" \
"v_mfma_f32_16x16x32_bf16 a[96:99], v[116:119], v[76:79], a[96:99]\n" \
"ds_write_b128 v196, v[200:203]\n" \
"v_mfma_f32_16x16x32_bf16 a[96:99], v[120:123], v[80:83], a[96:99]\n" \
"ds_write2_b32 v198, v252, v253 offset0:0 offset1:2\n" \
"s_waitcnt lgkmcnt(2)\n" \
"v_mfma_f32_16x16x32_bf16 a[4:7], v[128:131], v[56:59], a[4:7]\n" \
"ds_write2_b32 v198, v254, v255 offset0:4 offset1:6\n" \
"s_add_u32 s101, s97, s98\n" \
"v_mfma_f32_16x16x32_bf16 a[4:7], v[124:127], v[52:55], a[4:7]\n" \
"v_cndmask_b32_e32 v43, v228, v225, vcc\n" \
"v_and_b32_e32 v231, 2, v212\n" \
"v_mfma_f32_16x16x32_bf16 a[36:39], v[124:127], v[60:63], a[36:39]\n" \
"v_add_u32_e32 v236, v233, v235\n" \
"v_lshrrev_b32_e32 v237, 1, v230\n" \
"v_mfma_f32_16x16x32_bf16 a[36:39], v[128:131], v[64:67], a[36:39]\n" \
"ds_write_b128 v197, v[204:207]\n" \
"v_mfma_f32_16x16x32_bf16 a[68:71], v[128:131], v[72:75], a[68:71]\n" \
"v_add_u32_e32 v243, v242, v230\n" \
"s_add_u32 s100, s101, s99\n" \
"v_mfma_f32_16x16x32_bf16 a[68:71], v[124:127], v[68:71], a[68:71]\n" \
"v_and_b32_e64 v229, 3, v212\n" \
"v_mov_b32_e32 v232, 0x3f80\n" \
"v_mfma_f32_16x16x32_bf16 a[100:103], v[124:127], v[76:79], a[100:103]\n" \
"v_cmp_eq_u32_e32 vcc, 0, v231\n" \
"v_lshlrev_b32_e32 v239, 1, v238\n" \
"v_mfma_f32_16x16x32_bf16 a[100:103], v[128:131], v[80:83], a[100:103]\n" \
"v_lshlrev_b32_e32 v244, 7, v243\n" \
"s_add_u32 s88, 0x4000, s100\n" \
"v_mfma_f32_16x16x32_bf16 a[8:11], v[136:139], v[56:59], a[8:11]\n" \
"v_or_b32_e32 v230, 2, v229\n" \
"v_cndmask_b32_e32 v233, 0, v232, vcc\n" \
"v_mfma_f32_16x16x32_bf16 a[8:11], v[132:135], v[52:55], a[8:11]\n" \
"v_or_b32_e32 v240, v237, v239\n" \
"v_add_u32_e32 v245, 0x14000, v244\n" \
"v_mfma_f32_16x16x32_bf16 a[40:43], v[132:135], v[60:63], a[40:43]\n" \
"s_cmp_lg_u32 s95, 0\n" \
"s_cselect_b32 s47, s88, s101\n" \
"v_mfma_f32_16x16x32_bf16 a[40:43], v[136:139], v[64:67], a[40:43]\n" \
"v_cmp_eq_u32_e32 vcc, v230, v213\n" \
"v_lshlrev_b32_e32 v241, 5, v240\n" \
"v_mfma_f32_16x16x32_bf16 a[72:75], v[136:139], v[72:75], a[72:75]\n" \
"v_add_u32_e32 v246, v245, v235\n" \
"v_cndmask_b32_e32 v46, 0, v233, vcc\n" \
"v_mfma_f32_16x16x32_bf16 a[72:75], v[132:135], v[68:71], a[72:75]\n" \
"v_and_b32_e32 v234, 51, v212\n" \
"v_add_u32_e32 v190, v236, v241\n" \
"v_mfma_f32_16x16x32_bf16 a[104:107], v[132:135], v[76:79], a[104:107]\n" \
"v_mov_b32_e32 v235, 0x3f80\n" \
"v_cmp_eq_u32_e32 vcc, 2, v234\n" \
"v_mfma_f32_16x16x32_bf16 a[104:107], v[136:139], v[80:83], a[104:107]\n" \
"s_lshl_b32 s88, s95, 4\n" \
"v_xor_b32_e64 v247, 3, v237\n" \
"v_mfma_f32_16x16x32_bf16 a[12:15], v[144:147], v[56:59], a[12:15]\n" \
"s_add_u32 s62, s22, 0x280\n" \
"s_and_b32 s62, s62, 0x3ff\n" \
"v_mfma_f32_16x16x32_bf16 a[12:15], v[140:143], v[52:55], a[12:15]\n" \
"s_lshl_b32 s63, s62, 12\n" \
"v_cndmask_b32_e32 v47, 0, v235, vcc\n" \
"v_mfma_f32_16x16x32_bf16 a[44:47], v[140:143], v[60:63], a[44:47]\n" \
"v_add_u32_e64 v236, s88, v215\n" \
"v_mov_b32_e32 v237, 0xffff\n" \
"v_mfma_f32_16x16x32_bf16 a[44:47], v[144:147], v[64:67], a[44:47]\n" \
"v_cmp_le_u32_e32 vcc, v214, v236\n" \
"v_mov_b32_e32 v239, 0xffff0000\n" \
"v_mfma_f32_16x16x32_bf16 a[76:79], v[144:147], v[72:75], a[76:79]\n" \
"v_cndmask_b32_e32 v238, 0, v237, vcc\n" \
"v_cmp_lt_u32_e32 vcc, v214, v236\n" \
"v_mfma_f32_16x16x32_bf16 a[76:79], v[140:143], v[68:71], a[76:79]\n" \
"v_add_u32_e32 v241, 2, v214\n" \
"v_cndmask_b32_e32 v240, 0, v239, vcc\n" \
"v_mfma_f32_16x16x32_bf16 a[108:111], v[140:143], v[76:79], a[108:111]\n" \
"v_mov_b32_e32 v242, 0xffff\n" \
"v_cmp_le_u32_e32 vcc, v241, v236\n" \
"v_mfma_f32_16x16x32_bf16 a[108:111], v[144:147], v[80:83], a[108:111]\n" \
"v_lshlrev_b32_e32 v248, 5, v247\n" \
"v_cndmask_b32_e32 v243, 0, v242, vcc\n" \
"s_waitcnt vmcnt(2) lgkmcnt(0)\n" \
"s_barrier\n" \
"v_mfma_f32_16x16x32_bf16 a[128:131], v[120:123], v[88:91], a[128:131]\n" \
"s_add_u32 m0, %[mA], 0x8000\n" \
"s_add_u32 s85, s62, 0x0\n" \
"s_nop 0\n" \
"v_mfma_f32_16x16x32_bf16 a[128:131], v[116:119], v[84:87], a[128:131]\n" \
"buffer_load_dwordx4 v22, s[64:67], s85 offen lds\n" \
"v_mfma_f32_16x16x32_bf16 a[160:163], v[116:119], v[92:95], a[160:163]\n" \
"ds_read_b128 v[56:59], v29 offset:16384\n" \
"ds_read_b128 v[52:55], v28 offset:16384\n" \
"v_mfma_f32_16x16x32_bf16 a[160:163], v[120:123], v[96:99], a[160:163]\n" \
"s_add_u32 m0, %[mA], 0x8400\n" \
"s_add_u32 s84, s62, 0x2000\n" \
"v_mfma_f32_16x16x32_bf16 a[192:195], v[120:123], v[104:107], a[192:195]\n" \
"buffer_load_dwordx4 v23, s[64:67], s84 offen lds\n" \
"v_mfma_f32_16x16x32_bf16 a[192:195], v[116:119], v[100:103], a[192:195]\n" \
"ds_read_b128 v[60:63], v28 offset:18432\n" \
"ds_read_b128 v[64:67], v29 offset:18432\n" \
"v_mfma_f32_16x16x32_bf16 a[224:227], v[116:119], v[108:111], a[224:227]\n" \
"ds_read_b64_tr_b16 v[116:117], v32 offset:0\n" \
"ds_read_b64_tr_b16 v[118:119], v32 offset:1024\n" \
"v_mfma_f32_16x16x32_bf16 a[224:227], v[120:123], v[112:115], a[224:227]\n" \
"s_add_u32 m0, %[mB], 0x8000\n" \
"s_add_u32 s85, s63, 0x0\n" \
"v_mfma_f32_16x16x32_bf16 a[132:135], v[128:131], v[88:91], a[132:135]\n" \
"buffer_load_dwordx4 v26, s[68:71], s85 offen lds\n" \
"v_mfma_f32_16x16x32_bf16 a[132:135], v[124:127], v[84:87], a[132:135]\n" \
"ds_read_b64_tr_b16 v[120:121], v32 offset:8192\n" \
"ds_read_b64_tr_b16 v[122:123], v32 offset:9216\n" \
"v_mfma_f32_16x16x32_bf16 a[164:167], v[124:127], v[92:95], a[164:167]\n" \
"ds_read_b128 v[72:75], v29 offset:20480\n" \
"ds_read_b128 v[68:71], v28 offset:20480\n" \
"v_mfma_f32_16x16x32_bf16 a[164:167], v[128:131], v[96:99], a[164:167]\n" \
"s_add_u32 m0, %[mB], 0xc000\n" \
"s_add_u32 s84, s63, 0x0\n" \
"v_mfma_f32_16x16x32_bf16 a[196:199], v[128:131], v[104:107], a[196:199]\n" \
"buffer_load_dwordx4 v26, s[72:75], s84 offen lds\n" \
"v_mfma_f32_16x16x32_bf16 a[196:199], v[124:127], v[100:103], a[196:199]\n" \
"ds_read_b128 v[76:79], v28 offset:22528\n" \
"ds_read_b128 v[80:83], v29 offset:22528\n" \
"v_mfma_f32_16x16x32_bf16 a[228:231], v[124:127], v[108:111], a[228:231]\n" \
"ds_read_b64_tr_b16 v[124:125], v33 offset:0\n" \
"ds_read_b64_tr_b16 v[126:127], v33 offset:1024\n" \
"v_mfma_f32_16x16x32_bf16 a[228:231], v[128:131], v[112:115], a[228:231]\n" \
"s_add_u32 m0, %[mA], 0x8800\n" \
"s_add_u32 s85, s62, 0x4000\n" \
"v_mfma_f32_16x16x32_bf16 a[136:139], v[136:139], v[88:91], a[136:139]\n" \
"buffer_load_dwordx4 v22, s[64:67], s85 offen lds\n" \
"v_mfma_f32_16x16x32_bf16 a[136:139], v[132:135], v[84:87], a[136:139]\n" \
"ds_read_b64_tr_b16 v[128:129], v33 offset:8192\n" \
"ds_read_b64_tr_b16 v[130:131], v33 offset:9216\n" \
"v_mfma_f32_16x16x32_bf16 a[168:171], v[132:135], v[92:95], a[168:171]\n" \
"ds_read_b128 v[200:203], v199 offset:0\n" \
"ds_read_b128 v[204:207], v199 offset:4096\n" \
"v_mfma_f32_16x16x32_bf16 a[168:171], v[136:139], v[96:99], a[168:171]\n" \
"s_add_u32 m0, %[mA], 0x8c00\n" \
"s_add_u32 s84, s62, 0x6000\n" \
"v_mfma_f32_16x16x32_bf16 a[200:203], v[136:139], v[104:107], a[200:203]\n" \
"buffer_load_dwordx4 v23, s[64:67], s84 offen lds\n" \
"v_mfma_f32_16x16x32_bf16 a[200:203], v[132:135], v[100:103], a[200:203]\n" \
"ds_read_b128 v[208:211], v199 offset:8192\n" \
"v_add_u32_e64 v39, v246, v248\n" \
"v_mfma_f32_16x16x32_bf16 a[232:235], v[132:135], v[108:111], a[232:235]\n" \
"ds_read_b64_tr_b16 v[132:133], v34 offset:0\n" \
"ds_read_b64_tr_b16 v[134:135], v34 offset:1024\n" \
"v_mfma_f32_16x16x32_bf16 a[232:235], v[136:139], v[112:115], a[232:235]\n" \
"s_add_u32 m0, %[mB], 0x8400\n" \
"s_add_u32 s85, s63, 0x8000\n" \
"v_mfma_f32_16x16x32_bf16 a[140:143], v[144:147], v[88:91], a[140:143]\n" \
"buffer_load_dwordx4 v26, s[68:71], s85 offen lds\n" \
"v_mfma_f32_16x16x32_bf16 a[140:143], v[140:143], v[84:87], a[140:143]\n" \
"ds_read_b64_tr_b16 v[136:137], v34 offset:8192\n" \
"ds_read_b64_tr_b16 v[138:139], v34 offset:9216\n" \
"v_mfma_f32_16x16x32_bf16 a[172:175], v[140:143], v[92:95], a[172:175]\n" \
"v_mov_b32_e32 v244, 0xffff0000\n" \
"v_cmp_lt_u32_e32 vcc, v241, v236\n" \
"v_mfma_f32_16x16x32_bf16 a[172:175], v[144:147], v[96:99], a[172:175]\n" \
"s_add_u32 m0, %[mB], 0xc400\n" \
"s_add_u32 s84, s63, 0x8000\n" \
"v_mfma_f32_16x16x32_bf16 a[204:207], v[144:147], v[104:107], a[204:207]\n" \
"buffer_load_dwordx4 v26, s[72:75], s84 offen lds\n" \
"v_mfma_f32_16x16x32_bf16 a[204:207], v[140:143], v[100:103], a[204:207]\n" \
"v_cndmask_b32_e32 v245, 0, v244, vcc\n" \
"v_add_u32_e64 v246, 16, v214\n" \
"v_mfma_f32_16x16x32_bf16 a[236:239], v[140:143], v[108:111], a[236:239]\n" \
"ds_read_b64_tr_b16 v[140:141], v35 offset:0\n" \
"ds_read_b64_tr_b16 v[142:143], v35 offset:1024\n" \
"v_mfma_f32_16x16x32_bf16 a[236:239], v[144:147], v[112:115], a[236:239]\n" \
"s_add_u32 m0, %[mA], 0x9000\n" \
"s_add_u32 s85, s62, 0x8000\n" \
"v_mfma_f32_16x16x32_bf16 a[144:147], v[88:91], v[152:155], a[144:147]\n" \
"buffer_load_dwordx4 v22, s[64:67], s85 offen lds\n" \
"v_mfma_f32_16x16x32_bf16 a[144:147], v[84:87], v[148:151], a[144:147]\n" \
"ds_read_b64_tr_b16 v[144:145], v35 offset:8192\n" \
"ds_read_b64_tr_b16 v[146:147], v35 offset:9216\n" \
"v_mfma_f32_16x16x32_bf16 a[176:179], v[92:95], v[148:151], a[176:179]\n" \
"v_mov_b32_e32 v247, 0xffff\n" \
"v_cmp_le_u32_e32 vcc, v246, v236\n" \
"v_mfma_f32_16x16x32_bf16 a[176:179], v[96:99], v[152:155], a[176:179]\n" \
"s_add_u32 m0, %[mA], 0x9400\n" \
"s_add_u32 s84, s62, 0xa000\n" \
"v_mfma_f32_16x16x32_bf16 a[208:211], v[104:107], v[152:155], a[208:211]\n" \
"buffer_load_dwordx4 v23, s[64:67], s84 offen lds\n" \
"v_mfma_f32_16x16x32_bf16 a[208:211], v[100:103], v[148:151], a[208:211]\n" \
"v_cndmask_b32_e32 v248, 0, v247, vcc\n" \
"v_mov_b32_e32 v249, 0xffff0000\n" \
"v_mfma_f32_16x16x32_bf16 a[240:243], v[108:111], v[148:151], a[240:243]\n" \
"ds_read_b64_tr_b16 v[148:149], v32 offset:16384\n" \
"ds_read_b64_tr_b16 v[150:151], v32 offset:17408\n" \
"v_mfma_f32_16x16x32_bf16 a[240:243], v[112:115], v[152:155], a[240:243]\n" \
"s_add_u32 m0, %[mB], 0x8800\n" \
"s_add_u32 s85, s63, 0x10000\n" \
"v_mfma_f32_16x16x32_bf16 a[148:151], v[88:91], v[160:163], a[148:151]\n" \
"buffer_load_dwordx4 v27, s[68:71], s85 offen lds\n" \
"v_mfma_f32_16x16x32_bf16 a[148:151], v[84:87], v[156:159], a[148:151]\n" \
"ds_read_b64_tr_b16 v[152:153], v32 offset:24576\n" \
"ds_read_b64_tr_b16 v[154:155], v32 offset:25600\n" \
"v_mfma_f32_16x16x32_bf16 a[180:183], v[92:95], v[156:159], a[180:183]\n" \
"v_cmp_lt_u32_e32 vcc, v246, v236\n" \
"s_lshl_b32 s89, %[w], 8\n" \
"v_mfma_f32_16x16x32_bf16 a[180:183], v[96:99], v[160:163], a[180:183]\n" \
"s_add_u32 m0, %[mB], 0xc800\n" \
"s_add_u32 s84, s63, 0x10000\n" \
"v_mfma_f32_16x16x32_bf16 a[212:215], v[104:107], v[160:163], a[212:215]\n" \
"buffer_load_dwordx4 v27, s[72:75], s84 offen lds\n" \
"v_mfma_f32_16x16x32_bf16 a[212:215], v[100:103], v[156:159], a[212:215]\n" \
"v_cndmask_b32_e32 v250, 0, v249, vcc\n" \
"v_add_u32_e32 v251, 18, v214\n" \
"v_mfma_f32_16x16x32_bf16 a[244:247], v[108:111], v[156:159], a[244:247]\n" \
"ds_read_b64_tr_b16 v[156:157], v33 offset:16384\n" \
"ds_read_b64_tr_b16 v[158:159], v33 offset:17408\n" \
"v_mfma_f32_16x16x32_bf16 a[244:247], v[112:115], v[160:163], a[244:247]\n" \
"ds_read_b64_tr_b16 v[160:161], v33 offset:24576\n" \
"ds_read_b64_tr_b16 v[162:163], v33 offset:25600\n" \
"v_mfma_f32_16x16x32_bf16 a[152:155], v[88:91], v[168:171], a[152:155]\n" \
"s_add_u32 s46, 0x27000, s89\n" \
"v_mov_b32_e32 v218, 0xffff\n" \
"v_mfma_f32_16x16x32_bf16 a[152:155], v[84:87], v[164:167], a[152:155]\n" \
"v_cmp_le_u32_e32 vcc, v251, v236\n" \
"s_add_u32 s89, 0xffffc000, s101\n" \
"v_mfma_f32_16x16x32_bf16 a[184:187], v[92:95], v[164:167], a[184:187]\n" \
"v_cndmask_b32_e32 v219, 0, v218, vcc\n" \
"v_lshlrev_b32_e32 v224, 3, v212\n" \
"v_mfma_f32_16x16x32_bf16 a[184:187], v[96:99], v[168:171], a[184:187]\n" \
"s_cmp_lg_u32 s95, 0\n" \
"s_cselect_b32 s48, s89, s100\n" \
"v_mfma_f32_16x16x32_bf16 a[216:219], v[104:107], v[168:171], a[216:219]\n" \
"s_lshr_b32 s94, %[b], 5\n" \
"s_mov_b32 s96, 0xc000\n" \
"v_mfma_f32_16x16x32_bf16 a[216:219], v[100:103], v[164:167], a[216:219]\n" \
"s_mov_b32 s97, 0x8000\n" \
"v_mov_b32_e32 v221, 0xffff0000\n" \
"v_mfma_f32_16x16x32_bf16 a[248:251], v[108:111], v[164:167], a[248:251]\n" \
"ds_read_b64_tr_b16 v[164:165], v34 offset:16384\n" \
"ds_read_b64_tr_b16 v[166:167], v34 offset:17408\n" \
"v_mfma_f32_16x16x32_bf16 a[248:251], v[112:115], v[168:171], a[248:251]\n" \
"ds_read_b64_tr_b16 v[168:169], v34 offset:24576\n" \
"ds_read_b64_tr_b16 v[170:171], v34 offset:25600\n" \
"v_mfma_f32_16x16x32_bf16 a[156:159], v[88:91], v[176:179], a[156:159]\n" \
"ds_read_b128 v[88:91], v29 offset:0\n" \
"v_cmp_lt_u32_e32 vcc, v251, v236\n" \
"v_mfma_f32_16x16x32_bf16 a[156:159], v[84:87], v[172:175], a[156:159]\n" \
"ds_read_b128 v[84:87], v28 offset:0\n" \
"v_lshrrev_b32_e32 v212, 4, v20\n" \
"v_mfma_f32_16x16x32_bf16 a[188:191], v[92:95], v[172:175], a[188:191]\n" \
"ds_read_b128 v[92:95], v28 offset:2048\n" \
"v_cndmask_b32_e32 v216, 0, v221, vcc\n" \
"v_mfma_f32_16x16x32_bf16 a[188:191], v[96:99], v[176:179], a[188:191]\n" \
"ds_read_b128 v[96:99], v29 offset:2048\n" \
"v_lshlrev_b32_e32 v223, 2, v236\n" \
"v_mfma_f32_16x16x32_bf16 a[220:223], v[104:107], v[176:179], a[220:223]\n" \
"ds_read_b128 v[104:107], v29 offset:4096\n" \
"s_lshl_b32 s101, %[w], 9\n" \
"v_mfma_f32_16x16x32_bf16 a[220:223], v[100:103], v[172:175], a[220:223]\n" \
"ds_read_b128 v[100:103], v28 offset:4096\n" \
"v_mul_u32_u24_e32 v213, 0xe39, v212\n" \
"v_mfma_f32_16x16x32_bf16 a[252:255], v[108:111], v[172:175], a[252:255]\n" \
"ds_read_b64_tr_b16 v[172:173], v35 offset:16384\n" \
"ds_read_b64_tr_b16 v[174:175], v35 offset:17408\n" \
"v_mfma_f32_16x16x32_bf16 a[252:255], v[112:115], v[176:179], a[252:255]\n" \
"s_add_u32 m0, %[mA], 0x9800\n" \
"s_waitcnt lgkmcnt(12)\n" \
"v_mfma_f32_16x16x32_bf16 a[0:3], v[120:123], v[56:59], a[0:3]\n" \
"s_add_u32 s85, s62, 0xc000\n" \
"ds_read_b64_tr_b16 v[176:177], v35 offset:24576\n" \
"v_mfma_f32_16x16x32_bf16 a[0:3], v[116:119], v[52:55], a[0:3]\n" \
"buffer_load_dwordx4 v22, s[64:67], s85 offen lds\n" \
"v_mfma_f32_16x16x32_bf16 a[32:35], v[116:119], v[60:63], a[32:35]\n" \
"s_add_u32 m0, %[mA], 0x9c00\n" \
"s_add_u32 s84, s62, 0xe000\n" \
"L_pf2:\n" \
"s_cbranch_execz L_pf6\n" \
"s_nop 0\n" \
"v_mfma_f32_16x16x32_bf16 a[32:35], v[120:123], v[64:67], a[32:35]\n" \
"ds_read_b64_tr_b16 v[178:179], v35 offset:25600\n" \
"ds_read_b128 v[108:111], v28 offset:6144\n" \
"v_mfma_f32_16x16x32_bf16 a[64:67], v[120:123], v[72:75], a[64:67]\n" \
"buffer_load_dwordx4 v23, s[64:67], s84 offen lds\n" \
"v_mfma_f32_16x16x32_bf16 a[64:67], v[116:119], v[68:71], a[64:67]\n" \
"s_add_u32 m0, %[mB], 0x8c00\n" \
"s_add_u32 s85, s63, 0x18000\n" \
"v_mfma_f32_16x16x32_bf16 a[96:99], v[116:119], v[76:79], a[96:99]\n" \
"ds_read_b128 v[112:115], v29 offset:6144\n" \
"v_or_b32_e32 v15, v219, v216\n" \
"v_mfma_f32_16x16x32_bf16 a[96:99], v[120:123], v[80:83], a[96:99]\n" \
"buffer_load_dwordx4 v27, s[68:71], s85 offen lds\n" \
"v_mfma_f32_16x16x32_bf16 a[4:7], v[128:131], v[56:59], a[4:7]\n" \
"s_add_u32 m0, %[mB], 0xcc00\n" \
"s_add_u32 s84, s63, 0x18000\n" \
"v_mfma_f32_16x16x32_bf16 a[4:7], v[124:127], v[52:55], a[4:7]\n" \
"s_cmp_lg_u32 s94, 0\n" \
"s_cselect_b32 s53, s96, 0x8000\n" \
"v_mfma_f32_16x16x32_bf16 a[36:39], v[124:127], v[60:63], a[36:39]\n" \
"buffer_load_dwordx4 v27, s[72:75], s84 offen lds\n" \
"v_mfma_f32_16x16x32_bf16 a[36:39], v[128:131], v[64:67], a[36:39]\n" \
"s_cmp_lg_u32 s94, 0\n" \
"s_cselect_b32 s98, s97, 0x4000\n" \
"v_mfma_f32_16x16x32_bf16 a[68:71], v[128:131], v[72:75], a[68:71]\n" \
"v_add_u32_e32 v18, 0x27000, v223\n" \
"s_add_u32 s89, 0x27800, s101\n" \
"v_mfma_f32_16x16x32_bf16 a[68:71], v[124:127], v[68:71], a[68:71]\n" \
"buffer_store_dwordx4 v[200:203], v20, s[80:83], s57 offen sc1\n" \
"v_mfma_f32_16x16x32_bf16 a[100:103], v[124:127], v[76:79], a[100:103]\n" \
"v_lshrrev_b32_e32 v214, 16, v213\n" \
"v_add_u32_e64 v222, 64, v212\n" \
"v_mfma_f32_16x16x32_bf16 a[100:103], v[128:131], v[80:83], a[100:103]\n" \
"v_add_u32_e32 v19, s89, v224\n" \
"v_mul_u32_u24_e32 v216, 18, v214\n" \
"v_mfma_f32_16x16x32_bf16 a[8:11], v[136:139], v[56:59], a[8:11]\n" \
"s_and_b32 s94, 7, %[b]\n" \
"v_mul_u32_u24_e32 v223, 0xe39, v222\n" \
"v_mfma_f32_16x16x32_bf16 a[8:11], v[132:135], v[52:55], a[8:11]\n" \
"v_sub_u32_e32 v217, v212, v216\n" \
"s_lshl_b32 s95, s94, 5\n" \
"v_mfma_f32_16x16x32_bf16 a[40:43], v[132:135], v[60:63], a[40:43]\n" \
"buffer_store_dwordx4 v[204:207], v20, s[80:83], s56 offen sc1\n" \
"v_mfma_f32_16x16x32_bf16 a[40:43], v[136:139], v[64:67], a[40:43]\n" \
"v_lshrrev_b32_e32 v224, 16, v223\n" \
"v_add_u32_e32 v233, 0x80, v212\n" \
"v_mfma_f32_16x16x32_bf16 a[72:75], v[136:139], v[72:75], a[72:75]\n" \
"s_add_u32 s96, 32, s95\n" \
"v_lshlrev_b32_e32 v218, 4, v217\n" \
"v_mfma_f32_16x16x32_bf16 a[72:75], v[132:135], v[68:71], a[72:75]\n" \
"v_mul_u32_u24_e32 v226, 18, v224\n" \
"v_mul_u32_u24_e32 v234, 0xe39, v233\n" \
"v_mfma_f32_16x16x32_bf16 a[104:107], v[132:135], v[76:79], a[104:107]\n" \
"v_or_b32_e32 v13, v243, v245\n" \
"v_add_u32_e64 v219, s96, v218\n" \
"v_mfma_f32_16x16x32_bf16 a[104:107], v[136:139], v[80:83], a[104:107]\n" \
"v_sub_u32_e32 v227, v222, v226\n" \
"v_lshrrev_b32_e32 v235, 16, v234\n" \
"v_mfma_f32_16x16x32_bf16 a[12:15], v[144:147], v[56:59], a[12:15]\n" \
"buffer_store_dwordx4 v[208:211], v20, s[80:83], s58 offen sc1\n" \
"v_mfma_f32_16x16x32_bf16 a[12:15], v[140:143], v[52:55], a[12:15]\n" \
"v_add_u32_e32 v244, 0xc0, v212\n" \
"v_or_b32_e32 v12, v238, v240\n" \
"v_mfma_f32_16x16x32_bf16 a[44:47], v[140:143], v[60:63], a[44:47]\n" \
"v_and_b32_e32 v220, 0xff, v219\n" \
"v_cmp_gt_u32_e32 vcc, 16, v217\n" \
"v_mfma_f32_16x16x32_bf16 a[44:47], v[144:147], v[64:67], a[44:47]\n" \
"v_lshlrev_b32_e32 v228, 4, v227\n" \
"v_mul_u32_u24_e32 v237, 18, v235\n" \
"v_mfma_f32_16x16x32_bf16 a[76:79], v[144:147], v[72:75], a[76:79]\n" \
"v_mul_u32_u24_e32 v245, 0xe39, v244\n" \
"v_or_b32_e32 v14, v248, v250\n" \
"v_mfma_f32_16x16x32_bf16 a[76:79], v[140:143], v[68:71], a[76:79]\n" \
"v_cndmask_b32_e32 v221, v218, v220, vcc\n" \
"v_add_u32_e64 v229, s96, v228\n" \
"v_mfma_f32_16x16x32_bf16 a[108:111], v[140:143], v[76:79], a[108:111]\n" \
"s_mov_b32 m0, %[mS]\n" \
"s_mov_b32 s86, s10\n" \
"v_mfma_f32_16x16x32_bf16 a[108:111], v[144:147], v[80:83], a[108:111]\n" \
"s_mov_b32 s87, s11\n" \
"v_sub_u32_e32 v238, v233, v237\n" \
"v_mfma_f32_16x16x32_bf16 a[16:19], v[56:59], v[152:155], a[16:19]\n" \
"v_lshrrev_b32_e32 v246, 16, v245\n" \
"v_add_u32_e32 v219, 0x100, v212\n" \
"v_mfma_f32_16x16x32_bf16 a[16:19], v[52:55], v[148:151], a[16:19]\n" \
"v_mul_u32_u24_e32 v215, 0x480, v214\n" \
"v_and_b32_e32 v230, 0xff, v229\n" \
"v_mfma_f32_16x16x32_bf16 a[48:51], v[60:63], v[148:151], a[48:51]\n" \
"v_cmp_gt_u32_e32 vcc, 16, v227\n" \
"v_lshlrev_b32_e32 v239, 4, v238\n" \
"v_mfma_f32_16x16x32_bf16 a[48:51], v[64:67], v[152:155], a[48:51]\n" \
"v_mul_u32_u24_e32 v248, 18, v246\n" \
"v_mul_u32_u24_e32 v217, 0xe39, v219\n" \
"v_mfma_f32_16x16x32_bf16 a[80:83], v[72:75], v[152:155], a[80:83]\n" \
"v_add_u32_e32 v0, v215, v221\n" \
"v_cndmask_b32_e32 v231, v228, v230, vcc\n" \
"s_mov_b32 exec_lo, 0xfffc0000\n" \
"s_mov_b32 exec_hi, 0xf\n" \
"global_load_lds_dword v188, s[86:87]\n" \
"s_mov_b64 exec, -1\n" \
"v_mfma_f32_16x16x32_bf16 a[80:83], v[68:71], v[148:151], a[80:83]\n" \
"v_add_u32_e32 v240, s96, v239\n" \
"v_sub_u32_e32 v249, v244, v248\n" \
"v_mfma_f32_16x16x32_bf16 a[112:115], v[76:79], v[148:151], a[112:115]\n" \
"v_lshrrev_b32_e32 v220, 16, v217\n" \
"v_add_u32_e32 v229, 0x140, v212\n" \
"v_mfma_f32_16x16x32_bf16 a[112:115], v[80:83], v[152:155], a[112:115]\n" \
"v_mul_u32_u24_e32 v225, 0x480, v224\n" \
"v_and_b32_e32 v241, 0xff, v240\n" \
"v_mfma_f32_16x16x32_bf16 a[20:23], v[56:59], v[160:163], a[20:23]\n" \
"v_cmp_gt_u32_e32 vcc, 16, v238\n" \
"v_lshlrev_b32_e32 v250, 4, v249\n" \
"v_mfma_f32_16x16x32_bf16 a[20:23], v[52:55], v[156:159], a[20:23]\n" \
"v_mul_u32_u24_e32 v215, 18, v220\n" \
"v_mul_u32_u24_e32 v227, 0xe39, v229\n" \
"v_mfma_f32_16x16x32_bf16 a[52:55], v[60:63], v[156:159], a[52:55]\n" \
"v_add_u32_e32 v232, v225, v231\n" \
"v_cndmask_b32_e32 v242, v239, v241, vcc\n" \
"v_mfma_f32_16x16x32_bf16 a[52:55], v[64:67], v[160:163], a[52:55]\n" \
"v_add_u32_e32 v251, s96, v250\n" \
"v_sub_u32_e32 v221, v219, v215\n" \
"v_mfma_f32_16x16x32_bf16 a[84:87], v[72:75], v[160:163], a[84:87]\n" \
"v_lshrrev_b32_e32 v230, 16, v227\n" \
"v_add_u32_e32 v240, 0x180, v212\n" \
"v_mfma_f32_16x16x32_bf16 a[84:87], v[68:71], v[156:159], a[84:87]\n" \
"v_mul_u32_u24_e32 v236, 0x480, v235\n" \
"v_and_b32_e32 v213, 0xff, v251\n" \
"v_mfma_f32_16x16x32_bf16 a[116:119], v[76:79], v[156:159], a[116:119]\n" \
"v_cmp_gt_u32_e32 vcc, 16, v249\n" \
"v_lshlrev_b32_e32 v223, 4, v221\n" \
"v_mfma_f32_16x16x32_bf16 a[116:119], v[80:83], v[160:163], a[116:119]\n" \
"v_mul_u32_u24_e32 v225, 18, v230\n" \
"s_waitcnt lgkmcnt(0)\n" \
"v_mfma_f32_16x16x32_bf16 a[24:27], v[56:59], v[168:171], a[24:27]\n" \
"v_mul_u32_u24_e32 v238, 0xe39, v240\n" \
"v_add_u32_e32 v1, 0xfffffc00, v232\n" \
"v_mfma_f32_16x16x32_bf16 a[24:27], v[52:55], v[164:167], a[24:27]\n" \
"v_add_u32_e32 v243, v236, v242\n" \
"v_cndmask_b32_e32 v214, v250, v213, vcc\n" \
"v_mfma_f32_16x16x32_bf16 a[56:59], v[60:63], v[164:167], a[56:59]\n" \
"v_add_u32_e32 v224, s96, v223\n" \
"v_sub_u32_e32 v231, v229, v225\n" \
"v_mfma_f32_16x16x32_bf16 a[56:59], v[64:67], v[168:171], a[56:59]\n" \
"v_lshrrev_b32_e32 v241, 16, v238\n" \
"v_add_u32_e32 v251, 0x1c0, v212\n" \
"v_mfma_f32_16x16x32_bf16 a[88:91], v[72:75], v[168:171], a[88:91]\n" \
"v_mul_u32_u24_e32 v247, 0x480, v246\n" \
"v_and_b32_e32 v222, 0xff, v224\n" \
"v_mfma_f32_16x16x32_bf16 a[88:91], v[68:71], v[164:167], a[88:91]\n" \
"v_cmp_gt_u32_e32 vcc, 16, v221\n" \
"v_lshlrev_b32_e32 v232, 4, v231\n" \
"v_mfma_f32_16x16x32_bf16 a[120:123], v[76:79], v[164:167], a[120:123]\n" \
"v_mul_u32_u24_e32 v236, 18, v241\n" \
"v_mul_u32_u24_e32 v249, 0xe39, v251\n" \
"v_mfma_f32_16x16x32_bf16 a[120:123], v[80:83], v[168:171], a[120:123]\n" \
"v_add_u32_e32 v2, 0xfffff800, v243\n" \
"v_add_u32_e32 v216, v247, v214\n" \
"v_mfma_f32_16x16x32_bf16 a[28:31], v[56:59], v[176:179], a[28:31]\n" \
"s_add_u32 s62, s22, 0x300\n" \
"s_and_b32 s62, s62, 0x3ff\n" \
"v_mfma_f32_16x16x32_bf16 a[28:31], v[52:55], v[172:175], a[28:31]\n" \
"s_lshl_b32 s63, s62, 12\n" \
"v_cndmask_b32_e32 v226, v223, v222, vcc\n" \
"v_mfma_f32_16x16x32_bf16 a[60:63], v[60:63], v[172:175], a[60:63]\n" \
"v_add_u32_e32 v234, s96, v232\n" \
"v_sub_u32_e32 v242, v240, v236\n" \
"v_mfma_f32_16x16x32_bf16 a[60:63], v[64:67], v[176:179], a[60:63]\n" \
"v_lshrrev_b32_e32 v213, 16, v249\n" \
"v_add_u32_e32 v224, 0x200, v212\n" \
"v_mfma_f32_16x16x32_bf16 a[92:95], v[72:75], v[176:179], a[92:95]\n" \
"v_mul_u32_u24_e32 v218, 0x480, v220\n" \
"v_and_b32_e32 v235, 0xff, v234\n" \
"v_mfma_f32_16x16x32_bf16 a[92:95], v[68:71], v[172:175], a[92:95]\n" \
"v_cmp_gt_u32_e32 vcc, 16, v231\n" \
"v_lshlrev_b32_e32 v243, 4, v242\n" \
"v_mfma_f32_16x16x32_bf16 a[124:127], v[76:79], v[172:175], a[124:127]\n" \
"v_mul_u32_u24_e32 v247, 18, v213\n" \
"v_mul_u32_u24_e32 v221, 0xe39, v224\n" \
"v_mfma_f32_16x16x32_bf16 a[124:127], v[80:83], v[176:179], a[124:127]\n" \
"v_add_u32_e32 v3, 0xfffff400, v216\n" \
"v_add_u32_e32 v4, v218, v226\n" \
"s_waitcnt vmcnt(4)\n" \
"s_barrier\n" \
"v_mfma_f32_16x16x32_bf16 a[144:147], v[88:91], v[152:155], a[144:147]\n" \
"s_add_u32 m0, %[mA], 0x0\n" \
"s_add_u32 s85, s62, 0x0\n" \
"s_nop 0\n" \
"v_mfma_f32_16x16x32_bf16 a[144:147], v[84:87], v[148:151], a[144:147]\n" \
"buffer_load_dwordx4 v22, s[64:67], s85 offen lds\n" \
"v_mfma_f32_16x16x32_bf16 a[176:179], v[92:95], v[148:151], a[176:179]\n" \
"ds_read_b128 v[56:59], v29 offset:49152\n" \
"ds_read_b128 v[52:55], v28 offset:49152\n" \
"v_mfma_f32_16x16x32_bf16 a[176:179], v[96:99], v[152:155], a[176:179]\n" \
"s_add_u32 m0, %[mA], 0x400\n" \
"s_add_u32 s84, s62, 0x2000\n" \
"v_mfma_f32_16x16x32_bf16 a[208:211], v[104:107], v[152:155], a[208:211]\n" \
"buffer_load_dwordx4 v23, s[64:67], s84 offen lds\n" \
"v_mfma_f32_16x16x32_bf16 a[208:211], v[100:103], v[148:151], a[208:211]\n" \
"ds_read_b128 v[60:63], v28 offset:51200\n" \
"ds_read_b128 v[64:67], v29 offset:51200\n" \
"v_mfma_f32_16x16x32_bf16 a[240:243], v[108:111], v[148:151], a[240:243]\n" \
"ds_read_b64_tr_b16 v[148:149], v32 offset:49152\n" \
"ds_read_b64_tr_b16 v[150:151], v32 offset:50176\n" \
"v_mfma_f32_16x16x32_bf16 a[240:243], v[112:115], v[152:155], a[240:243]\n" \
"s_add_u32 m0, %[mB], 0x0\n" \
"s_add_u32 s85, s63, 0x0\n" \
"v_mfma_f32_16x16x32_bf16 a[148:151], v[88:91], v[160:163], a[148:151]\n" \
"buffer_load_dwordx4 v26, s[68:71], s85 offen lds\n" \
"v_mfma_f32_16x16x32_bf16 a[148:151], v[84:87], v[156:159], a[148:151]\n" \
"ds_read_b64_tr_b16 v[152:153], v32 offset:57344\n" \
"ds_read_b64_tr_b16 v[154:155], v32 offset:58368\n" \
"v_mfma_f32_16x16x32_bf16 a[180:183], v[92:95], v[156:159], a[180:183]\n" \
"ds_read_b128 v[72:75], v29 offset:53248\n" \
"ds_read_b128 v[68:71], v28 offset:53248\n" \
"v_mfma_f32_16x16x32_bf16 a[180:183], v[96:99], v[160:163], a[180:183]\n" \
"s_add_u32 m0, %[mB], 0x4000\n" \
"s_add_u32 s84, s63, 0x0\n" \
"s_nop 0\n" \
"v_mfma_f32_16x16x32_bf16 a[212:215], v[104:107], v[160:163], a[212:215]\n" \
"buffer_load_dwordx4 v26, s[72:75], s84 offen lds\n" \
"v_mfma_f32_16x16x32_bf16 a[212:215], v[100:103], v[156:159], a[212:215]\n" \
"ds_read_b128 v[76:79], v28 offset:55296\n" \
"ds_read_b128 v[80:83], v29 offset:55296\n" \
"v_mfma_f32_16x16x32_bf16 a[244:247], v[108:111], v[156:159], a[244:247]\n" \
"ds_read_b64_tr_b16 v[156:157], v33 offset:49152\n" \
"ds_read_b64_tr_b16 v[158:159], v33 offset:50176\n" \
"v_mfma_f32_16x16x32_bf16 a[244:247], v[112:115], v[160:163], a[244:247]\n" \
"s_add_u32 m0, %[mA], 0x800\n" \
"s_add_u32 s85, s62, 0x4000\n" \
"v_mfma_f32_16x16x32_bf16 a[152:155], v[88:91], v[168:171], a[152:155]\n" \
"buffer_load_dwordx4 v22, s[64:67], s85 offen lds\n" \
"v_mfma_f32_16x16x32_bf16 a[152:155], v[84:87], v[164:167], a[152:155]\n" \
"ds_read_b64_tr_b16 v[160:161], v33 offset:57344\n" \
"ds_read_b64_tr_b16 v[162:163], v33 offset:58368\n" \
"v_mfma_f32_16x16x32_bf16 a[184:187], v[92:95], v[164:167], a[184:187]\n" \
"v_cndmask_b32_e32 v233, v232, v235, vcc\n" \
"v_add_u32_e32 v245, s96, v243\n" \
"v_mfma_f32_16x16x32_bf16 a[184:187], v[96:99], v[168:171], a[184:187]\n" \
"s_add_u32 m0, %[mA], 0xc00\n" \
"s_add_u32 s84, s62, 0x6000\n" \
"v_mfma_f32_16x16x32_bf16 a[216:219], v[104:107], v[168:171], a[216:219]\n" \
"buffer_load_dwordx4 v23, s[64:67], s84 offen lds\n" \
"v_mfma_f32_16x16x32_bf16 a[216:219], v[100:103], v[164:167], a[216:219]\n" \
"v_sub_u32_e32 v214, v251, v247\n" \
"v_lshrrev_b32_e32 v222, 16, v221\n" \
"v_mfma_f32_16x16x32_bf16 a[248:251], v[108:111], v[164:167], a[248:251]\n" \
"ds_read_b64_tr_b16 v[164:165], v34 offset:49152\n" \
"ds_read_b64_tr_b16 v[166:167], v34 offset:50176\n" \
"v_mfma_f32_16x16x32_bf16 a[248:251], v[112:115], v[168:171], a[248:251]\n" \
"s_add_u32 m0, %[mB], 0x400\n" \
"s_add_u32 s85, s63, 0x8000\n" \
"v_mfma_f32_16x16x32_bf16 a[156:159], v[88:91], v[176:179], a[156:159]\n" \
"buffer_load_dwordx4 v26, s[68:71], s85 offen lds\n" \
"v_mfma_f32_16x16x32_bf16 a[156:159], v[84:87], v[172:175], a[156:159]\n" \
"ds_read_b64_tr_b16 v[168:169], v34 offset:57344\n" \
"ds_read_b64_tr_b16 v[170:171], v34 offset:58368\n" \
"v_mfma_f32_16x16x32_bf16 a[188:191], v[92:95], v[172:175], a[188:191]\n" \
"v_and_b32_e32 v246, 0xff, v245\n" \
"v_cmp_gt_u32_e32 vcc, 16, v242\n" \
"v_mfma_f32_16x16x32_bf16 a[188:191], v[96:99], v[176:179], a[188:191]\n" \
"s_add_u32 m0, %[mB], 0x4400\n" \
"s_add_u32 s84, s63, 0x8000\n" \
"v_mfma_f32_16x16x32_bf16 a[220:223], v[104:107], v[176:179], a[220:223]\n" \
"buffer_load_dwordx4 v26, s[72:75], s84 offen lds\n" \
"v_mfma_f32_16x16x32_bf16 a[220:223], v[100:103], v[172:175], a[220:223]\n" \
"v_lshlrev_b32_e32 v216, 4, v214\n" \
"v_mul_u32_u24_e32 v218, 18, v222\n" \
"s_nop 0\n" \
"v_mfma_f32_16x16x32_bf16 a[252:255], v[108:111], v[172:175], a[252:255]\n" \
"ds_read_b64_tr_b16 v[172:173], v35 offset:49152\n" \
"ds_read_b64_tr_b16 v[174:175], v35 offset:50176\n" \
"v_mfma_f32_16x16x32_bf16 a[252:255], v[112:115], v[176:179], a[252:255]\n" \
"s_add_u32 m0, %[mA], 0x1000\n" \
"s_add_u32 s85, s62, 0x8000\n" \
"v_mfma_f32_16x16x32_bf16 a[128:131], v[120:123], v[88:91], a[128:131]\n" \
"buffer_load_dwordx4 v22, s[64:67], s85 offen lds\n" \
"v_mfma_f32_16x16x32_bf16 a[128:131], v[116:119], v[84:87], a[128:131]\n" \
"ds_read_b64_tr_b16 v[176:177], v35 offset:57344\n" \
"ds_read_b64_tr_b16 v[178:179], v35 offset:58368\n" \
"v_mfma_f32_16x16x32_bf16 a[160:163], v[116:119], v[92:95], a[160:163]\n" \
"v_cndmask_b32_e32 v244, v243, v246, vcc\n" \
"v_add_u32_e32 v217, s96, v216\n" \
"v_mfma_f32_16x16x32_bf16 a[160:163], v[120:123], v[96:99], a[160:163]\n" \
"s_add_u32 m0, %[mA], 0x1400\n" \
"s_add_u32 s84, s62, 0xa000\n" \
"v_mfma_f32_16x16x32_bf16 a[192:195], v[120:123], v[104:107], a[192:195]\n" \
"buffer_load_dwordx4 v23, s[64:67], s84 offen lds\n" \
"v_mfma_f32_16x16x32_bf16 a[192:195], v[116:119], v[100:103], a[192:195]\n" \
"v_sub_u32_e32 v226, v224, v218\n" \
"s_nop 0\n" \
"v_mul_u32_u24_e32 v228, 0x480, v230\n" \
"v_mfma_f32_16x16x32_bf16 a[224:227], v[116:119], v[108:111], a[224:227]\n" \
"ds_read_b64_tr_b16 v[116:117], v32 offset:32768\n" \
"ds_read_b64_tr_b16 v[118:119], v32 offset:33792\n" \
"v_mfma_f32_16x16x32_bf16 a[224:227], v[120:123], v[112:115], a[224:227]\n" \
"s_add_u32 m0, %[mB], 0x800\n" \
"s_add_u32 s85, s63, 0x10000\n" \
"v_mfma_f32_16x16x32_bf16 a[132:135], v[128:131], v[88:91], a[132:135]\n" \
"buffer_load_dwordx4 v27, s[68:71], s85 offen lds\n" \
"v_mfma_f32_16x16x32_bf16 a[132:135], v[124:127], v[84:87], a[132:135]\n" \
"ds_read_b64_tr_b16 v[120:121], v32 offset:40960\n" \
"ds_read_b64_tr_b16 v[122:123], v32 offset:41984\n" \
"v_mfma_f32_16x16x32_bf16 a[164:167], v[124:127], v[92:95], a[164:167]\n" \
"v_and_b32_e32 v220, 0xff, v217\n" \
"v_cmp_gt_u32_e32 vcc, 16, v214\n" \
"v_mfma_f32_16x16x32_bf16 a[164:167], v[128:131], v[96:99], a[164:167]\n" \
"s_add_u32 m0, %[mB], 0x4800\n" \
"s_add_u32 s84, s63, 0x10000\n" \
"v_mfma_f32_16x16x32_bf16 a[196:199], v[128:131], v[104:107], a[196:199]\n" \
"buffer_load_dwordx4 v27, s[72:75], s84 offen lds\n" \
"v_mfma_f32_16x16x32_bf16 a[196:199], v[124:127], v[100:103], a[196:199]\n" \
"v_lshlrev_b32_e32 v227, 4, v226\n" \
"v_cndmask_b32_e32 v219, v216, v220, vcc\n" \
"v_mfma_f32_16x16x32_bf16 a[228:231], v[124:127], v[108:111], a[228:231]\n" \
"ds_read_b64_tr_b16 v[124:125], v33 offset:32768\n" \
"ds_read_b64_tr_b16 v[126:127], v33 offset:33792\n" \
"v_mfma_f32_16x16x32_bf16 a[228:231], v[128:131], v[112:115], a[228:231]\n" \
"ds_read_b64_tr_b16 v[128:129], v33 offset:40960\n" \
"ds_read_b64_tr_b16 v[130:131], v33 offset:41984\n" \
"v_mfma_f32_16x16x32_bf16 a[136:139], v[136:139], v[88:91], a[136:139]\n" \
"v_add_u32_e32 v230, s96, v227\n" \
"v_and_b32_e32 v229, 0xff, v230\n" \
"v_mfma_f32_16x16x32_bf16 a[136:139], v[132:135], v[84:87], a[136:139]\n" \
"v_cmp_gt_u32_e32 vcc, 16, v226\n" \
"v_mul_u32_u24_e32 v250, 0x480, v213\n" \
"v_mfma_f32_16x16x32_bf16 a[168:171], v[132:135], v[92:95], a[168:171]\n" \
"v_cndmask_b32_e32 v225, v227, v229, vcc\n" \
"v_and_b32_e64 v249, 2, v212\n" \
"v_mfma_f32_16x16x32_bf16 a[168:171], v[136:139], v[96:99], a[168:171]\n" \
"v_mov_b32_e32 v213, 0x100\n" \
"v_mov_b32_e64 v251, s95\n" \
"v_mfma_f32_16x16x32_bf16 a[200:203], v[136:139], v[104:107], a[200:203]\n" \
"v_cmp_ne_u32_e32 vcc, 0, v249\n" \
"s_mov_b32 s76, s8\n" \
"v_mfma_f32_16x16x32_bf16 a[200:203], v[132:135], v[100:103], a[200:203]\n" \
"v_cndmask_b32_e32 v247, v251, v213, vcc\n" \
"s_nop 0\n" \
"s_and_b32 s77, s9, 0xffff\n" \
"v_mfma_f32_16x16x32_bf16 a[232:235], v[132:135], v[108:111], a[232:235]\n" \
"ds_read_b64_tr_b16 v[132:133], v34 offset:32768\n" \
"ds_read_b64_tr_b16 v[134:135], v34 offset:33792\n" \
"v_mfma_f32_16x16x32_bf16 a[232:235], v[136:139], v[112:115], a[232:235]\n" \
"ds_read_b64_tr_b16 v[136:137], v34 offset:40960\n" \
"ds_read_b64_tr_b16 v[138:139], v34 offset:41984\n" \
"v_mfma_f32_16x16x32_bf16 a[140:143], v[144:147], v[88:91], a[140:143]\n" \
"ds_read_b128 v[88:91], v29 offset:32768\n" \
"s_mov_b32 s78, 0x240000\n" \
"v_mfma_f32_16x16x32_bf16 a[140:143], v[140:143], v[84:87], a[140:143]\n" \
"ds_read_b128 v[84:87], v28 offset:32768\n" \
"v_xor_b32_e64 v37, 0x20, v190\n" \
"v_mfma_f32_16x16x32_bf16 a[172:175], v[140:143], v[92:95], a[172:175]\n" \
"ds_read_b128 v[92:95], v28 offset:34816\n" \
"v_xor_b32_e64 v38, 0x40, v190\n" \
"v_mfma_f32_16x16x32_bf16 a[172:175], v[144:147], v[96:99], a[172:175]\n" \
"ds_read_b128 v[96:99], v29 offset:34816\n" \
"v_add_u32_e64 v48, s60, v28\n" \
"v_mfma_f32_16x16x32_bf16 a[204:207], v[144:147], v[104:107], a[204:207]\n" \
"ds_read_b128 v[104:107], v29 offset:36864\n" \
"v_add_u32_e64 v49, s60, v29\n" \
"v_mfma_f32_16x16x32_bf16 a[204:207], v[140:143], v[100:103], a[204:207]\n" \
"ds_read_b128 v[100:103], v28 offset:36864\n" \
"v_xor_b32_e64 v36, 0x0, v190\n" \
"v_mfma_f32_16x16x32_bf16 a[236:239], v[140:143], v[108:111], a[236:239]\n" \
"ds_read_b64_tr_b16 v[140:141], v35 offset:32768\n" \
"ds_read_b64_tr_b16 v[142:143], v35 offset:33792\n" \
"v_mfma_f32_16x16x32_bf16 a[236:239], v[144:147], v[112:115], a[236:239]\n" \
"s_add_u32 m0, %[mA], 0x1800\n" \
"s_waitcnt lgkmcnt(12)\n" \
"s_nop 0\n" \
"v_mfma_f32_16x16x32_bf16 a[16:19], v[56:59], v[152:155], a[16:19]\n" \
"s_add_u32 s85, s62, 0xc000\n" \
"ds_read_b64_tr_b16 v[144:145], v35 offset:40960\n" \
"v_mfma_f32_16x16x32_bf16 a[16:19], v[52:55], v[148:151], a[16:19]\n" \
"buffer_load_dwordx4 v22, s[64:67], s85 offen lds\n" \
"v_mfma_f32_16x16x32_bf16 a[48:51], v[60:63], v[148:151], a[48:51]\n" \
"s_add_u32 m0, %[mA], 0x1c00\n" \
"s_add_u32 s84, s62, 0xe000\n" \
"v_mfma_f32_16x16x32_bf16 a[48:51], v[64:67], v[152:155], a[48:51]\n" \
"ds_read_b64_tr_b16 v[146:147], v35 offset:41984\n" \
"ds_read_b128 v[108:111], v28 offset:38912\n" \
"v_mfma_f32_16x16x32_bf16 a[80:83], v[72:75], v[152:155], a[80:83]\n" \
"buffer_load_dwordx4 v23, s[64:67], s84 offen lds\n" \
"v_mfma_f32_16x16x32_bf16 a[80:83], v[68:71], v[148:151], a[80:83]\n" \
"s_add_u32 m0, %[mB], 0xc00\n" \
"s_add_u32 s85, s63, 0x18000\n" \
"v_mfma_f32_16x16x32_bf16 a[112:115], v[76:79], v[148:151], a[112:115]\n" \
"ds_read_b128 v[112:115], v29 offset:38912\n" \
"v_add_u32_e64 v237, v228, v233\n" \
"v_mfma_f32_16x16x32_bf16 a[112:115], v[80:83], v[152:155], a[112:115]\n" \
"buffer_load_dwordx4 v27, s[68:71], s85 offen lds\n" \
"v_mfma_f32_16x16x32_bf16 a[20:23], v[56:59], v[160:163], a[20:23]\n" \
"s_add_u32 m0, %[mB], 0x4c00\n" \
"s_add_u32 s84, s63, 0x18000\n" \
"v_mfma_f32_16x16x32_bf16 a[20:23], v[52:55], v[156:159], a[20:23]\n" \
"s_lshl_b32 s97, %[w], 4\n" \
"v_lshrrev_b32_e32 v234, 2, v212\n" \
"v_mfma_f32_16x16x32_bf16 a[52:55], v[60:63], v[156:159], a[52:55]\n" \
"buffer_load_dwordx4 v27, s[72:75], s84 offen lds\n" \
"v_mfma_f32_16x16x32_bf16 a[52:55], v[64:67], v[160:163], a[52:55]\n" \
"v_add_u32_e32 v5, 0xfffffc00, v237\n" \
"v_add_u32_e64 v231, s97, v234\n" \
"v_mfma_f32_16x16x32_bf16 a[84:87], v[72:75], v[160:163], a[84:87]\n" \
"v_lshrrev_b32_e32 v235, 5, v231\n" \
"v_lshrrev_b32_e32 v237, 4, v231\n" \
"v_mfma_f32_16x16x32_bf16 a[84:87], v[68:71], v[156:159], a[84:87]\n" \
"v_mul_u32_u24_e32 v239, 0x480, v241\n" \
"v_lshlrev_b32_e32 v232, 4, v235\n" \
"v_mfma_f32_16x16x32_bf16 a[116:119], v[76:79], v[156:159], a[116:119]\n" \
"v_and_b32_e32 v228, 12, v231\n" \
"v_and_b32_e64 v238, 1, v237\n" \
"v_mfma_f32_16x16x32_bf16 a[116:119], v[80:83], v[160:163], a[116:119]\n" \
"s_mov_b32 m0, %[mS]\n" \
"s_mov_b32 s86, s8\n" \
"v_mfma_f32_16x16x32_bf16 a[24:27], v[56:59], v[168:171], a[24:27]\n" \
"s_mov_b32 s87, s9\n" \
"v_add_u32_e32 v233, v232, v228\n" \
"v_mfma_f32_16x16x32_bf16 a[24:27], v[52:55], v[164:167], a[24:27]\n" \
"v_lshlrev_b32_e32 v241, 1, v238\n" \
"v_lshrrev_b32_e32 v236, 1, v231\n" \
"v_mfma_f32_16x16x32_bf16 a[56:59], v[60:63], v[164:167], a[56:59]\n" \
"v_add_u32_e32 v240, v233, v241\n" \
"v_and_b32_e32 v245, 1, v236\n" \
"v_mfma_f32_16x16x32_bf16 a[56:59], v[64:67], v[168:171], a[56:59]\n" \
"v_add_u32_e32 v242, v240, v245\n" \
"v_add_u32_e32 v248, v239, v244\n" \
"v_mfma_f32_16x16x32_bf16 a[88:91], v[72:75], v[168:171], a[88:91]\n" \
"v_mul_u32_u24_e32 v246, 18, v242\n" \
"v_add_u32_e32 v243, 16, v246\n" \
"v_mfma_f32_16x16x32_bf16 a[88:91], v[68:71], v[164:167], a[88:91]\n" \
"v_and_b32_e64 v239, 1, v231\n" \
"v_add_u32_e32 v6, 0xfffff800, v248\n" \
"s_mov_b32 exec_lo, 0x0\n" \
"s_mov_b32 exec_hi, 0x3ffff0\n" \
"global_load_lds_dword v188, s[86:87]\n" \
"s_mov_b64 exec, -1\n" \
"v_mfma_f32_16x16x32_bf16 a[120:123], v[76:79], v[164:167], a[120:123]\n" \
"v_add_u32_e64 v244, v243, v239\n" \
"v_mul_u32_u24_e32 v248, 0x480, v244\n" \
"v_mfma_f32_16x16x32_bf16 a[120:123], v[80:83], v[168:171], a[120:123]\n" \
"v_add_u32_e32 v215, v250, v219\n" \
"v_add_u32_e32 v217, v248, v247\n" \
"v_mfma_f32_16x16x32_bf16 a[28:31], v[56:59], v[176:179], a[28:31]\n" \
"v_and_b32_e64 v214, 16, v20\n" \
"v_add_u32_e32 v7, 0xfffff400, v215\n" \
"v_mfma_f32_16x16x32_bf16 a[28:31], v[52:55], v[172:175], a[28:31]\n" \
"v_add_u32_e32 v9, v217, v214\n" \
"v_and_b32_e32 v216, 3, v212\n" \
"v_mfma_f32_16x16x32_bf16 a[60:63], v[60:63], v[172:175], a[60:63]\n" \
"v_lshlrev_b32_e32 v250, 3, v216\n" \
"v_mul_u32_u24_e32 v220, 0x120, v234\n" \
"v_mfma_f32_16x16x32_bf16 a[60:63], v[64:67], v[176:179], a[60:63]\n" \
"v_mul_u32_u24_e32 v223, 0x480, v222\n" \
"v_add_u32_e32 v10, v220, v250\n" \
"v_mfma_f32_16x16x32_bf16 a[92:95], v[72:75], v[176:179], a[92:95]\n" \
"v_add_u32_e32 v8, v223, v225\n" \
"s_lshl_b32 s99, %[w], 11\n" \
"v_mfma_f32_16x16x32_bf16 a[92:95], v[68:71], v[172:175], a[92:95]\n" \
"s_add_u32 s55, s98, s99\n" \
"s_nop 0\n" \
"v_mfma_f32_16x16x32_bf16 a[124:127], v[76:79], v[172:175], a[124:127]\n" \
"v_mfma_f32_16x16x32_bf16 a[124:127], v[80:83], v[176:179], a[124:127]\n" \
"v_mfma_f32_16x16x32_bf16 a[0:3], v[120:123], v[56:59], a[0:3]\n" \
"v_mfma_f32_16x16x32_bf16 a[0:3], v[116:119], v[52:55], a[0:3]\n" \
"v_mfma_f32_16x16x32_bf16 a[32:35], v[116:119], v[60:63], a[32:35]\n" \
"v_mfma_f32_16x16x32_bf16 a[32:35], v[120:123], v[64:67], a[32:35]\n" \
"v_mfma_f32_16x16x32_bf16 a[64:67], v[120:123], v[72:75], a[64:67]\n" \
"v_mfma_f32_16x16x32_bf16 a[64:67], v[116:119], v[68:71], a[64:67]\n" \
"v_mfma_f32_16x16x32_bf16 a[96:99], v[116:119], v[76:79], a[96:99]\n" \
"v_mfma_f32_16x16x32_bf16 a[96:99], v[120:123], v[80:83], a[96:99]\n" \
"v_mfma_f32_16x16x32_bf16 a[4:7], v[128:131], v[56:59], a[4:7]\n" \
"v_mfma_f32_16x16x32_bf16 a[4:7], v[124:127], v[52:55], a[4:7]\n" \
"v_mfma_f32_16x16x32_bf16 a[36:39], v[124:127], v[60:63], a[36:39]\n" \
"v_mfma_f32_16x16x32_bf16 a[36:39], v[128:131], v[64:67], a[36:39]\n" \
"v_mfma_f32_16x16x32_bf16 a[68:71], v[128:131], v[72:75], a[68:71]\n" \
"v_mfma_f32_16x16x32_bf16 a[68:71], v[124:127], v[68:71], a[68:71]\n" \
"v_mfma_f32_16x16x32_bf16 a[100:103], v[124:127], v[76:79], a[100:103]\n" \
"v_mfma_f32_16x16x32_bf16 a[100:103], v[128:131], v[80:83], a[100:103]\n" \
"s_waitcnt lgkmcnt(0)\n" \
"s_nop 0\n" \
"v_mfma_f32_16x16x32_bf16 a[8:11], v[136:139], v[56:59], a[8:11]\n" \
"v_mfma_f32_16x16x32_bf16 a[8:11], v[132:135], v[52:55], a[8:11]\n" \
"v_mfma_f32_16x16x32_bf16 a[40:43], v[132:135], v[60:63], a[40:43]\n" \
"v_mfma_f32_16x16x32_bf16 a[40:43], v[136:139], v[64:67], a[40:43]\n" \
"v_mfma_f32_16x16x32_bf16 a[72:75], v[136:139], v[72:75], a[72:75]\n" \
"v_mfma_f32_16x16x32_bf16 a[72:75], v[132:135], v[68:71], a[72:75]\n" \
"v_mfma_f32_16x16x32_bf16 a[104:107], v[132:135], v[76:79], a[104:107]\n" \
"v_mfma_f32_16x16x32_bf16 a[104:107], v[136:139], v[80:83], a[104:107]\n" \
"v_mfma_f32_16x16x32_bf16 a[12:15], v[144:147], v[56:59], a[12:15]\n" \
"s_add_u32 s62, s22, 0x380\n" \
"s_and_b32 s62, s62, 0x3ff\n" \
"v_mfma_f32_16x16x32_bf16 a[12:15], v[140:143], v[52:55], a[12:15]\n" \
"s_lshl_b32 s63, s62, 12\n" \
"v_mfma_f32_16x16x32_bf16 a[44:47], v[140:143], v[60:63], a[44:47]\n" \
"v_mfma_f32_16x16x32_bf16 a[44:47], v[144:147], v[64:67], a[44:47]\n" \
"v_mfma_f32_16x16x32_bf16 a[76:79], v[144:147], v[72:75], a[76:79]\n" \
"v_mfma_f32_16x16x32_bf16 a[76:79], v[140:143], v[68:71], a[76:79]\n" \
"v_mfma_f32_16x16x32_bf16 a[108:111], v[140:143], v[76:79], a[108:111]\n" \
"v_mfma_f32_16x16x32_bf16 a[108:111], v[144:147], v[80:83], a[108:111]\n" \
"s_waitcnt vmcnt(1)\n" \
"s_barrier\n" \
"s_mov_b64 exec, 1\n" \
"buffer_store_dword v45, off, s[80:83], s38 offset:64 sc1\n" \
"s_mov_b64 exec, -1\n" \
"v_mfma_f32_16x16x32_bf16 a[128:131], v[120:123], v[88:91], a[128:131]\n" \
"s_add_u32 m0, %[mA], 0x8000\n" \
"s_add_u32 s85, s62, 0x0\n" \
"v_mfma_f32_16x16x32_bf16 a[128:131], v[116:119], v[84:87], a[128:131]\n" \
"ds_read_b128 v[56:59], v29 offset:16384\n" \
"ds_read_b128 v[52:55], v28 offset:16384\n" \
"v_mfma_f32_16x16x32_bf16 a[160:163], v[116:119], v[92:95], a[160:163]\n" \
"buffer_load_dwordx4 v22, s[64:67], s85 offen lds\n" \
"v_mfma_f32_16x16x32_bf16 a[160:163], v[120:123], v[96:99], a[160:163]\n" \
"s_add_u32 m0, %[mA], 0x8400\n" \
"s_add_u32 s84, s62, 0x2000\n" \
"v_mfma_f32_16x16x32_bf16 a[192:195], v[120:123], v[104:107], a[192:195]\n" \
"ds_read_b128 v[60:63], v28 offset:18432\n" \
"ds_read_b128 v[64:67], v29 offset:18432\n" \
"v_mfma_f32_16x16x32_bf16 a[192:195], v[116:119], v[100:103], a[192:195]\n" \
"buffer_load_dwordx4 v23, s[64:67], s84 offen lds\n" \
"v_mfma_f32_16x16x32_bf16 a[224:227], v[116:119], v[108:111], a[224:227]\n" \
"ds_read_b64_tr_b16 v[116:117], v32 offset:0\n" \
"ds_read_b64_tr_b16 v[118:119], v32 offset:1024\n" \
"v_mfma_f32_16x16x32_bf16 a[224:227], v[120:123], v[112:115], a[224:227]\n" \
"s_add_u32 m0, %[mB], 0x8000\n" \
"s_add_u32 s85, s63, 0x0\n" \
"v_mfma_f32_16x16x32_bf16 a[132:135], v[128:131], v[88:91], a[132:135]\n" \
"buffer_load_dwordx4 v26, s[68:71], s85 offen lds\n" \
"v_mfma_f32_16x16x32_bf16 a[132:135], v[124:127], v[84:87], a[132:135]\n" \
"ds_read_b64_tr_b16 v[120:121], v32 offset:8192\n" \
"ds_read_b64_tr_b16 v[122:123], v32 offset:9216\n" \
"v_mfma_f32_16x16x32_bf16 a[164:167], v[124:127], v[92:95], a[164:167]\n" \
"ds_read_b128 v[72:75], v29 offset:20480\n" \
"ds_read_b128 v[68:71], v28 offset:20480\n" \
"v_mfma_f32_16x16x32_bf16 a[164:167], v[128:131], v[96:99], a[164:167]\n" \
"s_add_u32 m0, %[mB], 0xc000\n" \
"s_add_u32 s84, s63, 0x0\n" \
"v_mfma_f32_16x16x32_bf16 a[196:199], v[128:131], v[104:107], a[196:199]\n" \
"buffer_load_dwordx4 v26, s[72:75], s84 offen lds\n" \
"v_mfma_f32_16x16x32_bf16 a[196:199], v[124:127], v[100:103], a[196:199]\n" \
"ds_read_b128 v[76:79], v28 offset:22528\n" \
"ds_read_b128 v[80:83], v29 offset:22528\n" \
"v_mfma_f32_16x16x32_bf16 a[228:231], v[124:127], v[108:111], a[228:231]\n" \
"ds_read_b64_tr_b16 v[124:125], v33 offset:0\n" \
"ds_read_b64_tr_b16 v[126:127], v33 offset:1024\n" \
"v_mfma_f32_16x16x32_bf16 a[228:231], v[128:131], v[112:115], a[228:231]\n" \
"s_add_u32 m0, %[mA], 0x8800\n" \
"s_add_u32 s85, s62, 0x4000\n" \
"v_mfma_f32_16x16x32_bf16 a[136:139], v[136:139], v[88:91], a[136:139]\n" \
"buffer_load_dwordx4 v22, s[64:67], s85 offen lds\n" \
"v_mfma_f32_16x16x32_bf16 a[136:139], v[132:135], v[84:87], a[136:139]\n" \
"ds_read_b64_tr_b16 v[128:129], v33 offset:8192\n" \
"ds_read_b64_tr_b16 v[130:131], v33 offset:9216\n" \
"v_mfma_f32_16x16x32_bf16 a[168:171], v[132:135], v[92:95], a[168:171]\n" \
"v_mov_b32_e64 v45, %[tag]\n" \
"v_mfma_f32_16x16x32_bf16 a[168:171], v[136:139], v[96:99], a[168:171]\n" \
"s_add_u32 m0, %[mA], 0x8c00\n" \
"s_add_u32 s84, s62, 0x6000\n" \
"v_mfma_f32_16x16x32_bf16 a[200:203], v[136:139], v[104:107], a[200:203]\n" \
"buffer_load_dwordx4 v23, s[64:67], s84 offen lds\n" \
"v_mfma_f32_16x16x32_bf16 a[200:203], v[132:135], v[100:103], a[200:203]\n" \
"v_mfma_f32_16x16x32_bf16 a[232:235], v[132:135], v[108:111], a[232:235]\n" \
"ds_read_b64_tr_b16 v[132:133], v34 offset:0\n" \
"ds_read_b64_tr_b16 v[134:135], v34 offset:1024\n" \
"v_mfma_f32_16x16x32_bf16 a[232:235], v[136:139], v[112:115], a[232:235]\n" \
"s_add_u32 m0, %[mB], 0x8400\n" \
"s_add_u32 s85, s63, 0x8000\n" \
"v_mfma_f32_16x16x32_bf16 a[140:143], v[144:147], v[88:91], a[140:143]\n" \
"buffer_load_dwordx4 v26, s[68:71], s85 offen lds\n" \
"v_mfma_f32_16x16x32_bf16 a[140:143], v[140:143], v[84:87], a[140:143]\n" \
"ds_read_b64_tr_b16 v[136:137], v34 offset:8192\n" \
"ds_read_b64_tr_b16 v[138:139], v34 offset:9216\n" \
"v_mfma_f32_16x16x32_bf16 a[172:175], v[140:143], v[92:95], a[172:175]\n" \
"v_mfma_f32_16x16x32_bf16 a[172:175], v[144:147], v[96:99], a[172:175]\n" \
"s_add_u32 m0, %[mB], 0xc400\n" \
"s_add_u32 s84, s63, 0x8000\n" \
"v_mfma_f32_16x16x32_bf16 a[204:207], v[144:147], v[104:107], a[204:207]\n" \
"buffer_load_dwordx4 v26, s[72:75], s84 offen lds\n" \
"v_mfma_f32_16x16x32_bf16 a[204:207], v[140:143], v[100:103], a[204:207]\n" \
"v_mfma_f32_16x16x32_bf16 a[236:239], v[140:143], v[108:111], a[236:239]\n" \
"ds_read_b64_tr_b16 v[140:141], v35 offset:0\n" \
"ds_read_b64_tr_b16 v[142:143], v35 offset:1024\n" \
"L_pf3:\n" \
"s_cbranch_execz L_pf7\n" \
"s_nop 0\n" \
"v_mfma_f32_16x16x32_bf16 a[236:239], v[144:147], v[112:115], a[236:239]\n" \
"s_add_u32 m0, %[mA], 0x9000\n" \
"s_add_u32 s85, s62, 0x8000\n" \
"v_mfma_f32_16x16x32_bf16 a[144:147], v[88:91], v[152:155], a[144:147]\n" \
"buffer_load_dwordx4 v22, s[64:67], s85 offen lds\n" \
"v_mfma_f32_16x16x32_bf16 a[144:147], v[84:87], v[148:151], a[144:147]\n" \
"ds_read_b64_tr_b16 v[144:145], v35 offset:8192\n" \
"ds_read_b64_tr_b16 v[146:147], v35 offset:9216\n" \
"v_mfma_f32_16x16x32_bf16 a[176:179], v[92:95], v[148:151], a[176:179]\n" \
"v_mfma_f32_16x16x32_bf16 a[176:179], v[96:99], v[152:155], a[176:179]\n" \
"s_add_u32 m0, %[mA], 0x9400\n" \
"s_add_u32 s84, s62, 0xa000\n" \
"v_mfma_f32_16x16x32_bf16 a[208:211], v[104:107], v[152:155], a[208:211]\n" \
"buffer_load_dwordx4 v23, s[64:67], s84 offen lds\n" \
"v_mfma_f32_16x16x32_bf16 a[208:211], v[100:103], v[148:151], a[208:211]\n" \
"v_mfma_f32_16x16x32_bf16 a[240:243], v[108:111], v[148:151], a[240:243]\n" \
"ds_read_b64_tr_b16 v[148:149], v32 offset:16384\n" \
"ds_read_b64_tr_b16 v[150:151], v32 offset:17408\n" \
"v_mfma_f32_16x16x32_bf16 a[240:243], v[112:115], v[152:155], a[240:243]\n" \
"s_add_u32 m0, %[mB], 0x8800\n" \
"s_add_u32 s85, s63, 0x10000\n" \
"v_mfma_f32_16x16x32_bf16 a[148:151], v[88:91], v[160:163], a[148:151]\n" \
"buffer_load_dwordx4 v27, s[68:71], s85 offen lds\n" \
"v_mfma_f32_16x16x32_bf16 a[148:151], v[84:87], v[156:159], a[148:151]\n" \
"ds_read_b64_tr_b16 v[152:153], v32 offset:24576\n" \
"ds_read_b64_tr_b16 v[154:155], v32 offset:25600\n" \
"v_mfma_f32_16x16x32_bf16 a[180:183], v[92:95], v[156:159], a[180:183]\n" \
"v_mfma_f32_16x16x32_bf16 a[180:183], v[96:99], v[160:163], a[180:183]\n" \
"s_add_u32 m0, %[mB], 0xc800\n" \
"s_add_u32 s84, s63, 0x10000\n" \
"v_mfma_f32_16x16x32_bf16 a[212:215], v[104:107], v[160:163], a[212:215]\n" \
"buffer_load_dwordx4 v27, s[72:75], s84 offen lds\n" \
"v_mfma_f32_16x16x32_bf16 a[212:215], v[100:103], v[156:159], a[212:215]\n" \
"v_mfma_f32_16x16x32_bf16 a[244:247], v[108:111], v[156:159], a[244:247]\n" \
"ds_read_b64_tr_b16 v[156:157], v33 offset:16384\n" \
"ds_read_b64_tr_b16 v[158:159], v33 offset:17408\n" \
"v_mfma_f32_16x16x32_bf16 a[244:247], v[112:115], v[160:163], a[244:247]\n" \
"ds_read_b64_tr_b16 v[160:161], v33 offset:24576\n" \
"ds_read_b64_tr_b16 v[162:163], v33 offset:25600\n" \
"v_mfma_f32_16x16x32_bf16 a[152:155], v[88:91], v[168:171], a[152:155]\n" \
"v_mfma_f32_16x16x32_bf16 a[152:155], v[84:87], v[164:167], a[152:155]\n" \
"v_mfma_f32_16x16x32_bf16 a[184:187], v[92:95], v[164:167], a[184:187]\n" \
"v_mfma_f32_16x16x32_bf16 a[184:187], v[96:99], v[168:171], a[184:187]\n" \
"v_mfma_f32_16x16x32_bf16 a[216:219], v[104:107], v[168:171], a[216:219]\n" \
"v_mfma_f32_16x16x32_bf16 a[216:219], v[100:103], v[164:167], a[216:219]\n" \
"v_mfma_f32_16x16x32_bf16 a[248:251], v[108:111], v[164:167], a[248:251]\n" \
"ds_read_b64_tr_b16 v[164:165], v34 offset:16384\n" \
"ds_read_b64_tr_b16 v[166:167], v34 offset:17408\n" \
"v_mfma_f32_16x16x32_bf16 a[248:251], v[112:115], v[168:171], a[248:251]\n" \
"ds_read_b64_tr_b16 v[168:169], v34 offset:24576\n" \
"ds_read_b64_tr_b16 v[170:171], v34 offset:25600\n" \
"v_mfma_f32_16x16x32_bf16 a[156:159], v[88:91], v[176:179], a[156:159]\n" \
"ds_read_b128 v[88:91], v29 offset:0\n" \
"v_mfma_f32_16x16x32_bf16 a[156:159], v[84:87], v[172:175], a[156:159]\n" \
"ds_read_b128 v[84:87], v28 offset:0\n" \
"v_mfma_f32_16x16x32_bf16 a[188:191], v[92:95], v[172:175], a[188:191]\n" \
"ds_read_b128 v[92:95], v28 offset:2048\n" \
"v_mfma_f32_16x16x32_bf16 a[188:191], v[96:99], v[176:179], a[188:191]\n" \
"ds_read_b128 v[96:99], v29 offset:2048\n" \
"v_mfma_f32_16x16x32_bf16 a[220:223], v[104:107], v[176:179], a[220:223]\n" \
"ds_read_b128 v[104:107], v29 offset:4096\n" \
"v_mfma_f32_16x16x32_bf16 a[220:223], v[100:103], v[172:175], a[220:223]\n" \
"ds_read_b128 v[100:103], v28 offset:4096\n" \
"v_mfma_f32_16x16x32_bf16 a[252:255], v[108:111], v[172:175], a[252:255]\n" \
"ds_read_b64_tr_b16 v[172:173], v35 offset:16384\n" \
"ds_read_b64_tr_b16 v[174:175], v35 offset:17408\n" \
"v_mfma_f32_16x16x32_bf16 a[252:255], v[112:115], v[176:179], a[252:255]\n" \
"s_add_u32 m0, %[mA], 0x9800\n" \
"s_waitcnt lgkmcnt(12)\n" \
"s_nop 0\n" \
"v_mfma_f32_16x16x32_bf16 a[0:3], v[120:123], v[56:59], a[0:3]\n" \
"s_add_u32 s85, s62, 0xc000\n" \
"ds_read_b64_tr_b16 v[176:177], v35 offset:24576\n" \
"v_mfma_f32_16x16x32_bf16 a[0:3], v[116:119], v[52:55], a[0:3]\n" \
"buffer_load_dwordx4 v22, s[64:67], s85 offen lds\n" \
"v_mfma_f32_16x16x32_bf16 a[32:35], v[116:119], v[60:63], a[32:35]\n" \
"s_add_u32 m0, %[mA], 0x9c00\n" \
"s_add_u32 s84, s62, 0xe000\n" \
"v_mfma_f32_16x16x32_bf16 a[32:35], v[120:123], v[64:67], a[32:35]\n" \
"ds_read_b64_tr_b16 v[178:179], v35 offset:25600\n" \
"ds_read_b128 v[108:111], v28 offset:6144\n" \
"v_mfma_f32_16x16x32_bf16 a[64:67], v[120:123], v[72:75], a[64:67]\n" \
"buffer_load_dwordx4 v23, s[64:67], s84 offen lds\n" \
"v_mfma_f32_16x16x32_bf16 a[64:67], v[116:119], v[68:71], a[64:67]\n" \
"s_add_u32 m0, %[mB], 0x8c00\n" \
"s_add_u32 s85, s63, 0x18000\n" \
"v_mfma_f32_16x16x32_bf16 a[96:99], v[116:119], v[76:79], a[96:99]\n" \
"ds_read_b128 v[112:115], v29 offset:6144\n" \
"s_mov_b32 s62, s23\n" \
"v_mfma_f32_16x16x32_bf16 a[96:99], v[120:123], v[80:83], a[96:99]\n" \
"buffer_load_dwordx4 v27, s[68:71], s85 offen lds\n" \
"v_mfma_f32_16x16x32_bf16 a[4:7], v[128:131], v[56:59], a[4:7]\n" \
"s_add_u32 m0, %[mB], 0xcc00\n" \
"s_add_u32 s84, s63, 0x18000\n" \
"v_mfma_f32_16x16x32_bf16 a[4:7], v[124:127], v[52:55], a[4:7]\n" \
"s_mov_b32 s68, s10\n" \
"s_and_b32 s69, s11, 0xffff\n" \
"v_mfma_f32_16x16x32_bf16 a[36:39], v[124:127], v[60:63], a[36:39]\n" \
"buffer_load_dwordx4 v27, s[72:75], s84 offen lds\n" \
"s_mov_b32 s73, s52\n" \
"s_add_u32 s74, s52, 0x4000\n" \
"s_add_u32 s75, s52, 0x8000\n" \
"v_mfma_f32_16x16x32_bf16 a[36:39], v[128:131], v[64:67], a[36:39]\n" \
"s_lshl_b32 s72, s62, 6\n" \
"s_add_u32 s72, s72, s37\n" \
"v_mfma_f32_16x16x32_bf16 a[68:71], v[128:131], v[72:75], a[68:71]\n" \
"v_bfe_u32 v44, v20, 2, 5\n" \
"s_mov_b32 s70, 0x480000\n" \
"v_mfma_f32_16x16x32_bf16 a[68:71], v[124:127], v[68:71], a[68:71]\n" \
"buffer_load_dword v44, v44, s[80:83], s39 offen offset:64 sc1\n" \
"v_mfma_f32_16x16x32_bf16 a[100:103], v[124:127], v[76:79], a[100:103]\n" \
"v_add_u32_e32 v22, s61, v28\n" \
"v_add_u32_e64 v23, s61, v29\n" \
"v_mfma_f32_16x16x32_bf16 a[100:103], v[128:131], v[80:83], a[100:103]\n" \
"s_mov_b32 m0, %[mS]\n" \
"s_mov_b32 s86, s18\n" \
"v_mfma_f32_16x16x32_bf16 a[8:11], v[136:139], v[56:59], a[8:11]\n" \
"s_mov_b32 s87, s19\n" \
"v_mfma_f32_16x16x32_bf16 a[8:11], v[132:135], v[52:55], a[8:11]\n" \
"v_mfma_f32_16x16x32_bf16 a[40:43], v[132:135], v[60:63], a[40:43]\n" \
"v_mfma_f32_16x16x32_bf16 a[40:43], v[136:139], v[64:67], a[40:43]\n" \
"v_mfma_f32_16x16x32_bf16 a[72:75], v[136:139], v[72:75], a[72:75]\n" \
"v_mfma_f32_16x16x32_bf16 a[72:75], v[132:135], v[68:71], a[72:75]\n" \
"s_mov_b32 exec_lo, 0xfffc0000\n" \
"s_mov_b32 exec_hi, 0xf\n" \
"global_load_lds_dword v16, s[86:87]\n" \
"s_mov_b64 exec, -1\n" \
"v_mfma_f32_16x16x32_bf16 a[104:107], v[132:135], v[76:79], a[104:107]\n" \
"s_mov_b32 m0, %[mS]\n" \
"s_mov_b32 s86, s16\n" \
"v_mfma_f32_16x16x32_bf16 a[104:107], v[136:139], v[80:83], a[104:107]\n" \
"s_mov_b32 s87, s17\n" \
"v_mfma_f32_16x16x32_bf16 a[12:15], v[144:147], v[56:59], a[12:15]\n" \
"v_mfma_f32_16x16x32_bf16 a[12:15], v[140:143], v[52:55], a[12:15]\n" \
"v_mfma_f32_16x16x32_bf16 a[44:47], v[140:143], v[60:63], a[44:47]\n" \
"s_mov_b32 exec_lo, 0x0\n" \
"s_mov_b32 exec_hi, 0x3f0\n" \
"global_load_lds_dword v16, s[86:87]\n" \
"s_mov_b64 exec, -1\n" \
"v_mfma_f32_16x16x32_bf16 a[44:47], v[144:147], v[64:67], a[44:47]\n" \
"v_mfma_f32_16x16x32_bf16 a[76:79], v[144:147], v[72:75], a[76:79]\n" \
"v_mfma_f32_16x16x32_bf16 a[76:79], v[140:143], v[68:71], a[76:79]\n" \
"v_mfma_f32_16x16x32_bf16 a[108:111], v[140:143], v[76:79], a[108:111]\n" \
"v_mfma_f32_16x16x32_bf16 a[108:111], v[144:147], v[80:83], a[108:111]\n" \
"v_mfma_f32_16x16x32_bf16 a[16:19], v[56:59], v[152:155], a[16:19]\n" \
"v_mfma_f32_16x16x32_bf16 a[16:19], v[52:55], v[148:151], a[16:19]\n" \
"v_mfma_f32_16x16x32_bf16 a[48:51], v[60:63], v[148:151], a[48:51]\n" \
"v_mfma_f32_16x16x32_bf16 a[48:51], v[64:67], v[152:155], a[48:51]\n" \
"v_mfma_f32_16x16x32_bf16 a[80:83], v[72:75], v[152:155], a[80:83]\n" \
"v_mfma_f32_16x16x32_bf16 a[80:83], v[68:71], v[148:151], a[80:83]\n" \
"v_mfma_f32_16x16x32_bf16 a[112:115], v[76:79], v[148:151], a[112:115]\n" \
"v_mfma_f32_16x16x32_bf16 a[112:115], v[80:83], v[152:155], a[112:115]\n" \
"v_mfma_f32_16x16x32_bf16 a[20:23], v[56:59], v[160:163], a[20:23]\n" \
"v_mfma_f32_16x16x32_bf16 a[20:23], v[52:55], v[156:159], a[20:23]\n" \
"v_mfma_f32_16x16x32_bf16 a[52:55], v[60:63], v[156:159], a[52:55]\n" \
"v_mfma_f32_16x16x32_bf16 a[52:55], v[64:67], v[160:163], a[52:55]\n" \
"v_mfma_f32_16x16x32_bf16 a[84:87], v[72:75], v[160:163], a[84:87]\n" \
"v_mfma_f32_16x16x32_bf16 a[84:87], v[68:71], v[156:159], a[84:87]\n" \
"v_mfma_f32_16x16x32_bf16 a[116:119], v[76:79], v[156:159], a[116:119]\n" \
"v_mfma_f32_16x16x32_bf16 a[116:119], v[80:83], v[160:163], a[116:119]\n" \
"s_waitcnt lgkmcnt(0)\n" \
"v_mfma_f32_16x16x32_bf16 a[24:27], v[56:59], v[168:171], a[24:27]\n" \
"v_mfma_f32_16x16x32_bf16 a[24:27], v[52:55], v[164:167], a[24:27]\n" \
"v_mfma_f32_16x16x32_bf16 a[56:59], v[60:63], v[164:167], a[56:59]\n" \
"v_mfma_f32_16x16x32_bf16 a[56:59], v[64:67], v[168:171], a[56:59]\n" \
"v_mfma_f32_16x16x32_bf16 a[88:91], v[72:75], v[168:171], a[88:91]\n" \
"v_mfma_f32_16x16x32_bf16 a[88:91], v[68:71], v[164:167], a[88:91]\n" \
"v_mfma_f32_16x16x32_bf16 a[120:123], v[76:79], v[164:167], a[120:123]\n" \
"v_mfma_f32_16x16x32_bf16 a[120:123], v[80:83], v[168:171], a[120:123]\n" \
"v_mfma_f32_16x16x32_bf16 a[28:31], v[56:59], v[176:179], a[28:31]\n" \
"s_waitcnt vmcnt(2)\n" \
"v_cmp_ne_u32_e32 vcc, %[tag], v44\n" \
"v_mfma_f32_16x16x32_bf16 a[28:31], v[52:55], v[172:175], a[28:31]\n" \
"v_mfma_f32_16x16x32_bf16 a[60:63], v[60:63], v[172:175], a[60:63]\n" \
"v_mfma_f32_16x16x32_bf16 a[60:63], v[64:67], v[176:179], a[60:63]\n" \
"v_mfma_f32_16x16x32_bf16 a[92:95], v[72:75], v[176:179], a[92:95]\n" \
"v_mfma_f32_16x16x32_bf16 a[92:95], v[68:71], v[172:175], a[92:95]\n" \
"s_cbranch_vccnz L_pl_%=\n" \
"L_rl_%=:\n" \
"s_lshl_b32 s63, s62, 12\n" \
"v_mfma_f32_16x16x32_bf16 a[124:127], v[76:79], v[172:175], a[124:127]\n" \
"v_mfma_f32_16x16x32_bf16 a[124:127], v[80:83], v[176:179], a[124:127]\n" \
"s_barrier\n" \
"v_mfma_f32_16x16x32_bf16 a[144:147], v[88:91], v[152:155], a[144:147]\n" \
"s_add_u32 m0, s40, 0x0\n" \
"s_add_u32 s85, s72, 0x0\n" \
"v_mfma_f32_16x16x32_bf16 a[144:147], v[84:87], v[148:151], a[144:147]\n" \
"buffer_load_dwordx4 v20, s[80:83], s85 offen sc1 lds\n" \
"v_mfma_f32_16x16x32_bf16 a[176:179], v[92:95], v[148:151], a[176:179]\n" \
"s_add_u32 m0, s40, 0x1000\n" \
"s_add_u32 s84, s72, 0x1000\n" \
"v_mfma_f32_16x16x32_bf16 a[176:179], v[96:99], v[152:155], a[176:179]\n" \
"ds_read_b128 v[56:59], v29 offset:49152\n" \
"ds_read_b128 v[52:55], v28 offset:49152\n" \
"v_mfma_f32_16x16x32_bf16 a[208:211], v[104:107], v[152:155], a[208:211]\n" \
"buffer_load_dwordx4 v20, s[80:83], s84 offen sc1 lds\n" \
"v_mfma_f32_16x16x32_bf16 a[208:211], v[100:103], v[148:151], a[208:211]\n" \
"ds_read_b128 v[60:63], v28 offset:51200\n" \
"ds_read_b128 v[64:67], v29 offset:51200\n" \
"v_mfma_f32_16x16x32_bf16 a[240:243], v[108:111], v[148:151], a[240:243]\n" \
"s_add_u32 m0, s40, 0x3000\n" \
"s_add_u32 s85, s62, 0x0\n" \
"v_mfma_f32_16x16x32_bf16 a[240:243], v[112:115], v[152:155], a[240:243]\n" \
"buffer_load_dwordx4 v21, s[76:79], s85 offen lds\n" \
"v_mfma_f32_16x16x32_bf16 a[148:151], v[88:91], v[160:163], a[148:151]\n" \
"ds_read_b64_tr_b16 v[152:153], v32 offset:57344\n" \
"ds_read_b64_tr_b16 v[154:155], v32 offset:58368\n" \
"v_mfma_f32_16x16x32_bf16 a[148:151], v[84:87], v[156:159], a[148:151]\n" \
"ds_read_b64_tr_b16 v[148:149], v32 offset:49152\n" \
"ds_read_b64_tr_b16 v[150:151], v32 offset:50176\n" \
"v_mfma_f32_16x16x32_bf16 a[180:183], v[92:95], v[156:159], a[180:183]\n" \
"s_add_u32 m0, %[mA], 0x0\n" \
"s_add_u32 s84, s62, 0x0\n" \
"s_nop 0\n" \
"v_mfma_f32_16x16x32_bf16 a[180:183], v[96:99], v[160:163], a[180:183]\n" \
"buffer_load_dwordx4 v24, s[76:79], s84 offen lds\n" \
"v_mfma_f32_16x16x32_bf16 a[212:215], v[104:107], v[160:163], a[212:215]\n" \
"ds_read_b128 v[72:75], v29 offset:53248\n" \
"ds_read_b128 v[68:71], v28 offset:53248\n" \
"v_mfma_f32_16x16x32_bf16 a[212:215], v[100:103], v[156:159], a[212:215]\n" \
"s_add_u32 m0, %[mA], 0x400\n" \
"s_add_u32 s85, s62, 0x2400\n" \
"v_mfma_f32_16x16x32_bf16 a[244:247], v[108:111], v[156:159], a[244:247]\n" \
"buffer_load_dwordx4 v25, s[76:79], s85 offen lds\n" \
"v_mfma_f32_16x16x32_bf16 a[244:247], v[112:115], v[160:163], a[244:247]\n" \
"ds_read_b128 v[76:79], v28 offset:55296\n" \
"ds_read_b128 v[80:83], v29 offset:55296\n" \
"v_mfma_f32_16x16x32_bf16 a[152:155], v[88:91], v[168:171], a[152:155]\n" \
"ds_read_b64_tr_b16 v[160:161], v33 offset:57344\n" \
"ds_read_b64_tr_b16 v[162:163], v33 offset:58368\n" \
"v_mfma_f32_16x16x32_bf16 a[152:155], v[84:87], v[164:167], a[152:155]\n" \
"s_add_u32 m0, %[mB], 0x0\n" \
"s_add_u32 s84, s63, 0x0\n" \
"v_mfma_f32_16x16x32_bf16 a[184:187], v[92:95], v[164:167], a[184:187]\n" \
"buffer_load_dwordx4 v26, s[68:71], s84 offen lds\n" \
"v_mfma_f32_16x16x32_bf16 a[184:187], v[96:99], v[168:171], a[184:187]\n" \
"ds_read_b64_tr_b16 v[156:157], v33 offset:49152\n" \
"ds_read_b64_tr_b16 v[158:159], v33 offset:50176\n" \
"v_mfma_f32_16x16x32_bf16 a[216:219], v[104:107], v[168:171], a[216:219]\n" \
"v_mfma_f32_16x16x32_bf16 a[216:219], v[100:103], v[164:167], a[216:219]\n" \
"s_add_u32 m0, %[mA], 0x800\n" \
"s_add_u32 s85, s62, 0x4800\n" \
"v_mfma_f32_16x16x32_bf16 a[248:251], v[108:111], v[164:167], a[248:251]\n" \
"buffer_load_dwordx4 v24, s[76:79], s85 offen lds\n" \
"v_mfma_f32_16x16x32_bf16 a[248:251], v[112:115], v[168:171], a[248:251]\n" \
"ds_read_b64_tr_b16 v[168:169], v34 offset:57344\n" \
"ds_read_b64_tr_b16 v[170:171], v34 offset:58368\n" \
"v_mfma_f32_16x16x32_bf16 a[156:159], v[88:91], v[176:179], a[156:159]\n" \
"s_add_u32 m0, %[mA], 0xc00\n" \
"s_add_u32 s84, s62, 0x6c00\n" \
"v_mfma_f32_16x16x32_bf16 a[156:159], v[84:87], v[172:175], a[156:159]\n" \
"buffer_load_dwordx4 v25, s[76:79], s84 offen lds\n" \
"v_mfma_f32_16x16x32_bf16 a[188:191], v[92:95], v[172:175], a[188:191]\n" \
"ds_read_b64_tr_b16 v[164:165], v34 offset:49152\n" \
"ds_read_b64_tr_b16 v[166:167], v34 offset:50176\n" \
"v_mfma_f32_16x16x32_bf16 a[188:191], v[96:99], v[176:179], a[188:191]\n" \
"v_mfma_f32_16x16x32_bf16 a[220:223], v[104:107], v[176:179], a[220:223]\n" \
"s_add_u32 m0, %[mB], 0x400\n" \
"s_add_u32 s85, s63, 0x8000\n" \
"v_mfma_f32_16x16x32_bf16 a[220:223], v[100:103], v[172:175], a[220:223]\n" \
"buffer_load_dwordx4 v26, s[68:71], s85 offen lds\n" \
"v_mfma_f32_16x16x32_bf16 a[252:255], v[108:111], v[172:175], a[252:255]\n" \
"ds_read_b64_tr_b16 v[172:173], v35 offset:49152\n" \
"ds_read_b64_tr_b16 v[174:175], v35 offset:50176\n" \
"v_mfma_f32_16x16x32_bf16 a[252:255], v[112:115], v[176:179], a[252:255]\n" \
"ds_read_b64_tr_b16 v[176:177], v35 offset:57344\n" \
"ds_read_b64_tr_b16 v[178:179], v35 offset:58368\n" \
"v_mfma_f32_16x16x32_bf16 a[128:131], v[120:123], v[88:91], a[128:131]\n" \
"s_add_u32 m0, %[mA], 0x1000\n" \
"s_add_u32 s84, s62, 0x9000\n" \
"v_mfma_f32_16x16x32_bf16 a[128:131], v[116:119], v[84:87], a[128:131]\n" \
"buffer_load_dwordx4 v24, s[76:79], s84 offen lds\n" \
"v_mfma_f32_16x16x32_bf16 a[160:163], v[116:119], v[92:95], a[160:163]\n" \
"v_mfma_f32_16x16x32_bf16 a[160:163], v[120:123], v[96:99], a[160:163]\n" \
"s_add_u32 m0, %[mA], 0x1400\n" \
"s_add_u32 s85, s62, 0xb400\n" \
"v_mfma_f32_16x16x32_bf16 a[192:195], v[120:123], v[104:107], a[192:195]\n" \
"buffer_load_dwordx4 v25, s[76:79], s85 offen lds\n" \
"v_mfma_f32_16x16x32_bf16 a[192:195], v[116:119], v[100:103], a[192:195]\n" \
"v_mfma_f32_16x16x32_bf16 a[224:227], v[116:119], v[108:111], a[224:227]\n" \
"ds_read_b64_tr_b16 v[116:117], v32 offset:32768\n" \
"ds_read_b64_tr_b16 v[118:119], v32 offset:33792\n" \
"v_mfma_f32_16x16x32_bf16 a[224:227], v[120:123], v[112:115], a[224:227]\n" \
"s_add_u32 m0, %[mB], 0x800\n" \
"s_add_u32 s84, s63, 0x10000\n" \
"v_mfma_f32_16x16x32_bf16 a[132:135], v[128:131], v[88:91], a[132:135]\n" \
"buffer_load_dwordx4 v27, s[68:71], s84 offen lds\n" \
"v_mfma_f32_16x16x32_bf16 a[132:135], v[124:127], v[84:87], a[132:135]\n" \
"ds_read_b64_tr_b16 v[120:121], v32 offset:40960\n" \
"ds_read_b64_tr_b16 v[122:123], v32 offset:41984\n" \
"v_mfma_f32_16x16x32_bf16 a[164:167], v[124:127], v[92:95], a[164:167]\n" \
"v_mfma_f32_16x16x32_bf16 a[164:167], v[128:131], v[96:99], a[164:167]\n" \
"s_add_u32 m0, %[mA], 0x1800\n" \
"s_add_u32 s85, s62, 0xd800\n" \
"v_mfma_f32_16x16x32_bf16 a[196:199], v[128:131], v[104:107], a[196:199]\n" \
"buffer_load_dwordx4 v24, s[76:79], s85 offen lds\n" \
"v_mfma_f32_16x16x32_bf16 a[196:199], v[124:127], v[100:103], a[196:199]\n" \
"v_mfma_f32_16x16x32_bf16 a[228:231], v[124:127], v[108:111], a[228:231]\n" \
"ds_read_b64_tr_b16 v[124:125], v33 offset:32768\n" \
"ds_read_b64_tr_b16 v[126:127], v33 offset:33792\n" \
"v_mfma_f32_16x16x32_bf16 a[228:231], v[128:131], v[112:115], a[228:231]\n" \
"ds_read_b64_tr_b16 v[128:129], v33 offset:40960\n" \
"ds_read_b64_tr_b16 v[130:131], v33 offset:41984\n" \
"v_mfma_f32_16x16x32_bf16 a[136:139], v[136:139], v[88:91], a[136:139]\n" \
"v_mfma_f32_16x16x32_bf16 a[136:139], v[132:135], v[84:87], a[136:139]\n" \
"v_mfma_f32_16x16x32_bf16 a[168:171], v[132:135], v[92:95], a[168:171]\n" \
"v_mfma_f32_16x16x32_bf16 a[168:171], v[136:139], v[96:99], a[168:171]\n" \
"v_mfma_f32_16x16x32_bf16 a[200:203], v[136:139], v[104:107], a[200:203]\n" \
"v_mfma_f32_16x16x32_bf16 a[200:203], v[132:135], v[100:103], a[200:203]\n" \
"v_mfma_f32_16x16x32_bf16 a[232:235], v[132:135], v[108:111], a[232:235]\n" \
"ds_read_b64_tr_b16 v[132:133], v34 offset:32768\n" \
"ds_read_b64_tr_b16 v[134:135], v34 offset:33792\n" \
"v_mfma_f32_16x16x32_bf16 a[232:235], v[136:139], v[112:115], a[232:235]\n" \
"ds_read_b64_tr_b16 v[136:137], v34 offset:40960\n" \
"ds_read_b64_tr_b16 v[138:139], v34 offset:41984\n" \
"v_mfma_f32_16x16x32_bf16 a[140:143], v[144:147], v[88:91], a[140:143]\n" \
"ds_read_b128 v[88:91], v29 offset:32768\n" \
"v_mfma_f32_16x16x32_bf16 a[140:143], v[140:143], v[84:87], a[140:143]\n" \
"ds_read_b128 v[84:87], v28 offset:32768\n" \
"v_mfma_f32_16x16x32_bf16 a[172:175], v[140:143], v[92:95], a[172:175]\n" \
"ds_read_b128 v[92:95], v28 offset:34816\n" \
"v_mfma_f32_16x16x32_bf16 a[172:175], v[144:147], v[96:99], a[172:175]\n" \
"ds_read_b128 v[96:99], v29 offset:34816\n" \
"v_mfma_f32_16x16x32_bf16 a[204:207], v[144:147], v[104:107], a[204:207]\n" \
"ds_read_b128 v[104:107], v29 offset:36864\n" \
"v_mfma_f32_16x16x32_bf16 a[204:207], v[140:143], v[100:103], a[204:207]\n" \
"ds_read_b128 v[100:103], v28 offset:36864\n" \
"v_mfma_f32_16x16x32_bf16 a[236:239], v[140:143], v[108:111], a[236:239]\n" \
"ds_read_b64_tr_b16 v[140:141], v35 offset:32768\n" \
"ds_read_b64_tr_b16 v[142:143], v35 offset:33792\n" \
"v_mfma_f32_16x16x32_bf16 a[236:239], v[144:147], v[112:115], a[236:239]\n" \
"s_add_u32 m0, %[mA], 0x1c00\n" \
"s_waitcnt lgkmcnt(12)\n" \
"s_nop 0\n" \
"v_mfma_f32_16x16x32_bf16 a[16:19], v[56:59], v[152:155], a[16:19]\n" \
"s_add_u32 s85, s62, 0xfc00\n" \
"ds_read_b64_tr_b16 v[144:145], v35 offset:40960\n" \
"v_mfma_f32_16x16x32_bf16 a[16:19], v[52:55], v[148:151], a[16:19]\n" \
"buffer_load_dwordx4 v25, s[76:79], s85 offen lds\n" \
"v_mfma_f32_16x16x32_bf16 a[48:51], v[60:63], v[148:151], a[48:51]\n" \
"s_add_u32 m0, %[mB], 0xc00\n" \
"s_add_u32 s84, s63, 0x18000\n" \
"v_mfma_f32_16x16x32_bf16 a[48:51], v[64:67], v[152:155], a[48:51]\n" \
"ds_read_b64_tr_b16 v[146:147], v35 offset:41984\n" \
"ds_read_b128 v[108:111], v28 offset:38912\n" \
"v_mfma_f32_16x16x32_bf16 a[80:83], v[72:75], v[152:155], a[80:83]\n" \
"buffer_load_dwordx4 v27, s[68:71], s84 offen lds\n" \
"v_mfma_f32_16x16x32_bf16 a[80:83], v[68:71], v[148:151], a[80:83]\n" \
"ds_read_b128 v[112:115], v29 offset:38912\n" \
"s_add_u32 s62, s62, 0x80\n" \
"v_mfma_f32_16x16x32_bf16 a[112:115], v[76:79], v[148:151], a[112:115]\n" \
"s_cmp_lt_u32 s62, 0x480\n" \
"s_cselect_b32 s62, s62, 0\n" \
"v_mfma_f32_16x16x32_bf16 a[112:115], v[80:83], v[152:155], a[112:115]\n" \
"s_lshl_b32 s72, s62, 6\n" \
"s_add_u32 s72, s72, s37\n" \
"v_mfma_f32_16x16x32_bf16 a[20:23], v[56:59], v[160:163], a[20:23]\n" \
"s_lshl_b32 s63, s62, 12\n" \
"v_add_u32_e64 v28, s59, v28\n" \
"v_mfma_f32_16x16x32_bf16 a[20:23], v[52:55], v[156:159], a[20:23]\n" \
"v_add_u32_e64 v29, s59, v29\n" \
"v_accvgpr_read_b32 v184, a16\n" \
"v_mfma_f32_16x16x32_bf16 a[52:55], v[60:63], v[156:159], a[52:55]\n" \
"v_accvgpr_read_b32 v185, a17\n" \
"v_accvgpr_read_b32 v186, a18\n" \
"v_mfma_f32_16x16x32_bf16 a[52:55], v[64:67], v[160:163], a[52:55]\n" \
"v_accvgpr_read_b32 v187, a19\n" \
"v_accvgpr_write_b32 a16, 0\n" \
"v_mfma_f32_16x16x32_bf16 a[84:87], v[72:75], v[160:163], a[84:87]\n" \
"v_accvgpr_write_b32 a17, 0\n" \
"v_accvgpr_write_b32 a18, 0\n" \
"v_mfma_f32_16x16x32_bf16 a[84:87], v[68:71], v[156:159], a[84:87]\n" \
"v_accvgpr_write_b32 a19, 0\n" \
"v_accvgpr_read_b32 v180, a48\n" \
"v_mfma_f32_16x16x32_bf16 a[116:119], v[76:79], v[156:159], a[116:119]\n" \
"v_accvgpr_read_b32 v181, a49\n" \
"v_accvgpr_read_b32 v182, a50\n" \
"v_mfma_f32_16x16x32_bf16 a[116:119], v[80:83], v[160:163], a[116:119]\n" \
"v_accvgpr_read_b32 v183, a51\n" \
"v_accvgpr_read_b32 v188, a80\n" \
"v_mfma_f32_16x16x32_bf16 a[24:27], v[56:59], v[168:171], a[24:27]\n" \
"v_accvgpr_read_b32 v189, a81\n" \
"v_accvgpr_read_b32 v190, a82\n" \
"v_mfma_f32_16x16x32_bf16 a[24:27], v[52:55], v[164:167], a[24:27]\n" \
"v_accvgpr_read_b32 v191, a83\n" \
"v_cvt_pk_bf16_f32 v184, v184, v185\n" \
"v_mfma_f32_16x16x32_bf16 a[56:59], v[60:63], v[164:167], a[56:59]\n" \
"v_cvt_pk_bf16_f32 v185, v186, v187\n" \
"v_cvt_pk_bf16_f32 v186, v180, v181\n" \
"v_mfma_f32_16x16x32_bf16 a[56:59], v[64:67], v[168:171], a[56:59]\n" \
"v_cvt_pk_bf16_f32 v187, v182, v183\n" \
"v_accvgpr_read_b32 v180, a112\n" \
"v_mfma_f32_16x16x32_bf16 a[88:91], v[72:75], v[168:171], a[88:91]\n" \
"v_accvgpr_read_b32 v181, a113\n" \
"v_accvgpr_read_b32 v182, a114\n" \
"v_mfma_f32_16x16x32_bf16 a[88:91], v[68:71], v[164:167], a[88:91]\n" \
"v_accvgpr_read_b32 v183, a115\n" \
"v_accvgpr_write_b32 a112, 0\n" \
"v_mfma_f32_16x16x32_bf16 a[120:123], v[76:79], v[164:167], a[120:123]\n" \
"v_accvgpr_write_b32 a113, 0\n" \
"v_accvgpr_write_b32 a114, 0\n" \
"v_mfma_f32_16x16x32_bf16 a[120:123], v[80:83], v[168:171], a[120:123]\n" \
"v_accvgpr_write_b32 a115, 0\n" \
"v_accvgpr_read_b32 v192, a20\n" \
"v_mfma_f32_16x16x32_bf16 a[28:31], v[56:59], v[176:179], a[28:31]\n" \
"v_accvgpr_read_b32 v193, a21\n" \
"v_accvgpr_read_b32 v194, a22\n" \
"v_mfma_f32_16x16x32_bf16 a[28:31], v[52:55], v[172:175], a[28:31]\n" \
"v_accvgpr_read_b32 v195, a23\n" \
"v_cvt_pk_bf16_f32 v188, v188, v189\n" \
"v_mfma_f32_16x16x32_bf16 a[60:63], v[60:63], v[172:175], a[60:63]\n" \
"v_cvt_pk_bf16_f32 v189, v190, v191\n" \
"v_cvt_pk_bf16_f32 v190, v180, v181\n" \
"v_mfma_f32_16x16x32_bf16 a[60:63], v[64:67], v[176:179], a[60:63]\n" \
"v_cvt_pk_bf16_f32 v191, v182, v183\n" \
"v_accvgpr_read_b32 v180, a52\n" \
"v_mfma_f32_16x16x32_bf16 a[92:95], v[72:75], v[176:179], a[92:95]\n" \
"v_accvgpr_read_b32 v181, a53\n" \
"v_accvgpr_read_b32 v182, a54\n" \
"v_mfma_f32_16x16x32_bf16 a[92:95], v[68:71], v[172:175], a[92:95]\n" \
"v_accvgpr_read_b32 v183, a55\n" \
"v_accvgpr_read_b32 v196, a84\n" \
"v_mfma_f32_16x16x32_bf16 a[124:127], v[76:79], v[172:175], a[124:127]\n" \
"v_accvgpr_read_b32 v197, a85\n" \
"v_accvgpr_read_b32 v198, a86\n" \
"v_mfma_f32_16x16x32_bf16 a[124:127], v[80:83], v[176:179], a[124:127]\n" \
"v_accvgpr_read_b32 v199, a87\n" \
"v_cvt_pk_bf16_f32 v192, v192, v193\n" \
"v_mfma_f32_16x16x32_bf16 a[0:3], v[120:123], v[56:59], a[0:3]\n" \
"v_cvt_pk_bf16_f32 v193, v194, v195\n" \
"v_cvt_pk_bf16_f32 v194, v180, v181\n" \
"v_mfma_f32_16x16x32_bf16 a[0:3], v[116:119], v[52:55], a[0:3]\n" \
"v_cvt_pk_bf16_f32 v195, v182, v183\n" \
"v_accvgpr_read_b32 v180, a116\n" \
"v_mfma_f32_16x16x32_bf16 a[32:35], v[116:119], v[60:63], a[32:35]\n" \
"v_accvgpr_read_b32 v181, a117\n" \
"v_accvgpr_read_b32 v182, a118\n" \
"v_mfma_f32_16x16x32_bf16 a[32:35], v[120:123], v[64:67], a[32:35]\n" \
"v_accvgpr_read_b32 v183, a119\n" \
"v_accvgpr_write_b32 a116, 0\n" \
"v_mfma_f32_16x16x32_bf16 a[64:67], v[120:123], v[72:75], a[64:67]\n" \
"v_accvgpr_write_b32 a117, 0\n" \
"v_accvgpr_write_b32 a118, 0\n" \
"v_mfma_f32_16x16x32_bf16 a[64:67], v[116:119], v[68:71], a[64:67]\n" \
"v_accvgpr_write_b32 a119, 0\n" \
"v_accvgpr_read_b32 v200, a24\n" \
"v_mfma_f32_16x16x32_bf16 a[96:99], v[116:119], v[76:79], a[96:99]\n" \
"v_accvgpr_read_b32 v201, a25\n" \
"v_accvgpr_read_b32 v202, a26\n" \
"v_mfma_f32_16x16x32_bf16 a[96:99], v[120:123], v[80:83], a[96:99]\n" \
"v_accvgpr_read_b32 v203, a27\n" \
"v_cvt_pk_bf16_f32 v196, v196, v197\n" \
"v_mfma_f32_16x16x32_bf16 a[4:7], v[128:131], v[56:59], a[4:7]\n" \
"v_cvt_pk_bf16_f32 v197, v198, v199\n" \
"v_cvt_pk_bf16_f32 v198, v180, v181\n" \
"v_mfma_f32_16x16x32_bf16 a[4:7], v[124:127], v[52:55], a[4:7]\n" \
"v_cvt_pk_bf16_f32 v199, v182, v183\n" \
"v_accvgpr_read_b32 v180, a56\n" \
"v_mfma_f32_16x16x32_bf16 a[36:39], v[124:127], v[60:63], a[36:39]\n" \
"v_accvgpr_read_b32 v181, a57\n" \
"v_accvgpr_read_b32 v182, a58\n" \
"v_mfma_f32_16x16x32_bf16 a[36:39], v[128:131], v[64:67], a[36:39]\n" \
"v_accvgpr_read_b32 v183, a59\n" \
"v_accvgpr_read_b32 v204, a88\n" \
"v_mfma_f32_16x16x32_bf16 a[68:71], v[128:131], v[72:75], a[68:71]\n" \
"v_accvgpr_read_b32 v205, a89\n" \
"v_accvgpr_read_b32 v206, a90\n" \
"v_mfma_f32_16x16x32_bf16 a[68:71], v[124:127], v[68:71], a[68:71]\n" \
"v_accvgpr_read_b32 v207, a91\n" \
"v_cvt_pk_bf16_f32 v200, v200, v201\n" \
"v_mfma_f32_16x16x32_bf16 a[100:103], v[124:127], v[76:79], a[100:103]\n" \
"v_cvt_pk_bf16_f32 v201, v202, v203\n" \
"v_cvt_pk_bf16_f32 v202, v180, v181\n" \
"v_mfma_f32_16x16x32_bf16 a[100:103], v[128:131], v[80:83], a[100:103]\n" \
"v_cvt_pk_bf16_f32 v203, v182, v183\n" \
"s_waitcnt lgkmcnt(0)\n" \
"s_nop 0\n" \
"v_mfma_f32_16x16x32_bf16 a[8:11], v[136:139], v[56:59], a[8:11]\n" \
"v_accvgpr_read_b32 v180, a120\n" \
"v_accvgpr_read_b32 v181, a121\n" \
"v_mfma_f32_16x16x32_bf16 a[8:11], v[132:135], v[52:55], a[8:11]\n" \
"v_accvgpr_read_b32 v182, a122\n" \
"v_accvgpr_read_b32 v183, a123\n" \
"v_mfma_f32_16x16x32_bf16 a[40:43], v[132:135], v[60:63], a[40:43]\n" \
"v_accvgpr_write_b32 a120, 0\n" \
"v_accvgpr_write_b32 a121, 0\n" \
"v_mfma_f32_16x16x32_bf16 a[40:43], v[136:139], v[64:67], a[40:43]\n" \
"v_accvgpr_write_b32 a122, 0\n" \
"v_accvgpr_write_b32 a123, 0\n" \
"v_mfma_f32_16x16x32_bf16 a[72:75], v[136:139], v[72:75], a[72:75]\n" \
"v_accvgpr_read_b32 v208, a28\n" \
"v_accvgpr_read_b32 v209, a29\n" \
"v_mfma_f32_16x16x32_bf16 a[72:75], v[132:135], v[68:71], a[72:75]\n" \
"v_accvgpr_read_b32 v210, a30\n" \
"v_accvgpr_read_b32 v211, a31\n" \
"v_mfma_f32_16x16x32_bf16 a[104:107], v[132:135], v[76:79], a[104:107]\n" \
"v_cvt_pk_bf16_f32 v204, v204, v205\n" \
"v_cvt_pk_bf16_f32 v205, v206, v207\n" \
"v_mfma_f32_16x16x32_bf16 a[104:107], v[136:139], v[80:83], a[104:107]\n" \
"v_cvt_pk_bf16_f32 v206, v180, v181\n" \
"v_cvt_pk_bf16_f32 v207, v182, v183\n" \
"v_mfma_f32_16x16x32_bf16 a[12:15], v[144:147], v[56:59], a[12:15]\n" \
"v_accvgpr_read_b32 v180, a60\n" \
"v_accvgpr_read_b32 v181, a61\n" \
"v_mfma_f32_16x16x32_bf16 a[12:15], v[140:143], v[52:55], a[12:15]\n" \
"v_accvgpr_read_b32 v182, a62\n" \
"v_accvgpr_read_b32 v183, a63\n" \
"v_mfma_f32_16x16x32_bf16 a[44:47], v[140:143], v[60:63], a[44:47]\n" \
"v_accvgpr_read_b32 v212, a92\n" \
"v_accvgpr_read_b32 v213, a93\n" \
"v_mfma_f32_16x16x32_bf16 a[44:47], v[144:147], v[64:67], a[44:47]\n" \
"v_accvgpr_read_b32 v214, a94\n" \
"v_accvgpr_read_b32 v215, a95\n" \
"v_mfma_f32_16x16x32_bf16 a[76:79], v[144:147], v[72:75], a[76:79]\n" \
"v_cvt_pk_bf16_f32 v208, v208, v209\n" \
"v_cvt_pk_bf16_f32 v209, v210, v211\n" \
"v_mfma_f32_16x16x32_bf16 a[76:79], v[140:143], v[68:71], a[76:79]\n" \
"v_cvt_pk_bf16_f32 v210, v180, v181\n" \
"v_cvt_pk_bf16_f32 v211, v182, v183\n" \
"v_mfma_f32_16x16x32_bf16 a[108:111], v[140:143], v[76:79], a[108:111]\n" \
"v_accvgpr_read_b32 v180, a124\n" \
"v_accvgpr_read_b32 v181, a125\n" \
"v_mfma_f32_16x16x32_bf16 a[108:111], v[144:147], v[80:83], a[108:111]\n" \
"v_accvgpr_read_b32 v182, a126\n" \
"v_accvgpr_read_b32 v183, a127\n" \
"s_waitcnt vmcnt(0)\n" \
"s_barrier\n" \
"v_mfma_f32_16x16x32_bf16 a[128:131], v[120:123], v[88:91], a[128:131]\n" \
"s_add_u32 m0, s40, 0x8000\n" \
"s_add_u32 s85, s72, 0x0\n" \
"s_nop 0\n" \
"v_mfma_f32_16x16x32_bf16 a[128:131], v[116:119], v[84:87], a[128:131]\n" \
"buffer_load_dwordx4 v20, s[80:83], s85 offen sc1 lds\n" \
"v_mfma_f32_16x16x32_bf16 a[160:163], v[116:119], v[92:95], a[160:163]\n" \
"L_pf4:\n" \
"s_cbranch_execz L_pf8\n" \
"s_nop 0\n" \
"s_add_u32 m0, s40, 0x9000\n" \
"s_add_u32 s84, s72, 0x1000\n" \
"v_mfma_f32_16x16x32_bf16 a[160:163], v[120:123], v[96:99], a[160:163]\n" \
"ds_read_b128 v[52:55], v28 offset:0\n" \
"ds_read_b128 v[56:59], v29 offset:0\n" \
"v_mfma_f32_16x16x32_bf16 a[192:195], v[120:123], v[104:107], a[192:195]\n" \
"buffer_load_dwordx4 v20, s[80:83], s84 offen sc1 lds\n" \
"v_mfma_f32_16x16x32_bf16 a[192:195], v[116:119], v[100:103], a[192:195]\n" \
"ds_read_b128 v[60:63], v28 offset:2048\n" \
"ds_read_b128 v[64:67], v29 offset:2048\n" \
"v_mfma_f32_16x16x32_bf16 a[224:227], v[116:119], v[108:111], a[224:227]\n" \
"s_add_u32 m0, s40, 0xb000\n" \
"s_add_u32 s85, s62, 0x0\n" \
"v_mfma_f32_16x16x32_bf16 a[224:227], v[120:123], v[112:115], a[224:227]\n" \
"buffer_load_dwordx4 v21, s[76:79], s85 offen lds\n" \
"v_mfma_f32_16x16x32_bf16 a[132:135], v[128:131], v[88:91], a[132:135]\n" \
"ds_read_b64_tr_b16 v[116:117], v36 offset:0\n" \
"ds_read_b64_tr_b16 v[118:119], v36 offset:512\n" \
"v_mfma_f32_16x16x32_bf16 a[132:135], v[124:127], v[84:87], a[132:135]\n" \
"ds_read_b64_tr_b16 v[120:121], v36 offset:4096\n" \
"ds_read_b64_tr_b16 v[122:123], v36 offset:4608\n" \
"v_mfma_f32_16x16x32_bf16 a[164:167], v[124:127], v[92:95], a[164:167]\n" \
"s_add_u32 m0, %[mA], 0x8000\n" \
"s_add_u32 s84, s62, 0x0\n" \
"v_mfma_f32_16x16x32_bf16 a[164:167], v[128:131], v[96:99], a[164:167]\n" \
"buffer_load_dwordx4 v24, s[76:79], s84 offen lds\n" \
"v_mfma_f32_16x16x32_bf16 a[196:199], v[128:131], v[104:107], a[196:199]\n" \
"ds_read_b128 v[68:71], v28 offset:4096\n" \
"ds_read_b128 v[72:75], v29 offset:4096\n" \
"v_mfma_f32_16x16x32_bf16 a[196:199], v[124:127], v[100:103], a[196:199]\n" \
"s_add_u32 m0, %[mA], 0x8400\n" \
"s_add_u32 s85, s62, 0x2400\n" \
"v_mfma_f32_16x16x32_bf16 a[228:231], v[124:127], v[108:111], a[228:231]\n" \
"buffer_load_dwordx4 v25, s[76:79], s85 offen lds\n" \
"v_mfma_f32_16x16x32_bf16 a[228:231], v[128:131], v[112:115], a[228:231]\n" \
"ds_read_b64_tr_b16 v[128:129], v37 offset:4096\n" \
"ds_read_b64_tr_b16 v[130:131], v37 offset:4608\n" \
"v_mfma_f32_16x16x32_bf16 a[136:139], v[136:139], v[88:91], a[136:139]\n" \
"ds_read_b64_tr_b16 v[124:125], v37 offset:0\n" \
"ds_read_b64_tr_b16 v[126:127], v37 offset:512\n" \
"v_mfma_f32_16x16x32_bf16 a[136:139], v[132:135], v[84:87], a[136:139]\n" \
"s_add_u32 m0, %[mB], 0x8000\n" \
"s_add_u32 s84, s63, 0x0\n" \
"s_nop 0\n" \
"v_mfma_f32_16x16x32_bf16 a[168:171], v[132:135], v[92:95], a[168:171]\n" \
"buffer_load_dwordx4 v26, s[68:71], s84 offen lds\n" \
"v_mfma_f32_16x16x32_bf16 a[168:171], v[136:139], v[96:99], a[168:171]\n" \
"ds_read_b128 v[76:79], v28 offset:6144\n" \
"ds_read_b128 v[80:83], v29 offset:6144\n" \
"v_mfma_f32_16x16x32_bf16 a[200:203], v[136:139], v[104:107], a[200:203]\n" \
"v_accvgpr_write_b32 a124, 0\n" \
"v_accvgpr_write_b32 a125, 0\n" \
"v_mfma_f32_16x16x32_bf16 a[200:203], v[132:135], v[100:103], a[200:203]\n" \
"s_add_u32 m0, %[mA], 0x8800\n" \
"s_add_u32 s85, s62, 0x4800\n" \
"v_mfma_f32_16x16x32_bf16 a[232:235], v[132:135], v[108:111], a[232:235]\n" \
"buffer_load_dwordx4 v24, s[76:79], s85 offen lds\n" \
"v_mfma_f32_16x16x32_bf16 a[232:235], v[136:139], v[112:115], a[232:235]\n" \
"ds_read_b64_tr_b16 v[132:133], v38 offset:0\n" \
"ds_read_b64_tr_b16 v[134:135], v38 offset:512\n" \
"v_mfma_f32_16x16x32_bf16 a[140:143], v[144:147], v[88:91], a[140:143]\n" \
"s_add_u32 m0, %[mA], 0x8c00\n" \
"s_add_u32 s84, s62, 0x6c00\n" \
"v_mfma_f32_16x16x32_bf16 a[140:143], v[140:143], v[84:87], a[140:143]\n" \
"buffer_load_dwordx4 v25, s[76:79], s84 offen lds\n" \
"v_mfma_f32_16x16x32_bf16 a[172:175], v[140:143], v[92:95], a[172:175]\n" \
"ds_read_b64_tr_b16 v[136:137], v38 offset:4096\n" \
"ds_read_b64_tr_b16 v[138:139], v38 offset:4608\n" \
"v_mfma_f32_16x16x32_bf16 a[172:175], v[144:147], v[96:99], a[172:175]\n" \
"v_accvgpr_write_b32 a126, 0\n" \
"v_accvgpr_write_b32 a127, 0\n" \
"v_mfma_f32_16x16x32_bf16 a[204:207], v[144:147], v[104:107], a[204:207]\n" \
"s_add_u32 m0, %[mB], 0x8400\n" \
"s_add_u32 s85, s63, 0x8000\n" \
"v_mfma_f32_16x16x32_bf16 a[204:207], v[140:143], v[100:103], a[204:207]\n" \
"buffer_load_dwordx4 v26, s[68:71], s85 offen lds\n" \
"v_mfma_f32_16x16x32_bf16 a[236:239], v[140:143], v[108:111], a[236:239]\n" \
"ds_read_b64_tr_b16 v[140:141], v39 offset:0\n" \
"ds_read_b64_tr_b16 v[142:143], v39 offset:960\n" \
"v_mfma_f32_16x16x32_bf16 a[236:239], v[144:147], v[112:115], a[236:239]\n" \
"v_accvgpr_read_b32 v216, a0\n" \
"v_accvgpr_read_b32 v217, a1\n" \
"v_mfma_f32_16x16x32_bf16 a[144:147], v[88:91], v[152:155], a[144:147]\n" \
"s_add_u32 m0, %[mA], 0x9000\n" \
"s_add_u32 s84, s62, 0x9000\n" \
"v_mfma_f32_16x16x32_bf16 a[144:147], v[84:87], v[148:151], a[144:147]\n" \
"buffer_load_dwordx4 v24, s[76:79], s84 offen lds\n" \
"v_mfma_f32_16x16x32_bf16 a[148:151], v[84:87], v[156:159], a[148:151]\n" \
"v_accvgpr_read_b32 v218, a2\n" \
"v_accvgpr_read_b32 v219, a3\n" \
"v_mfma_f32_16x16x32_bf16 a[148:151], v[88:91], v[160:163], a[148:151]\n" \
"s_add_u32 m0, %[mA], 0x9400\n" \
"s_add_u32 s85, s62, 0xb400\n" \
"v_mfma_f32_16x16x32_bf16 a[152:155], v[88:91], v[168:171], a[152:155]\n" \
"buffer_load_dwordx4 v25, s[76:79], s85 offen lds\n" \
"v_mfma_f32_16x16x32_bf16 a[152:155], v[84:87], v[164:167], a[152:155]\n" \
"v_cvt_pk_bf16_f32 v212, v212, v213\n" \
"v_cvt_pk_bf16_f32 v213, v214, v215\n" \
"v_mfma_f32_16x16x32_bf16 a[156:159], v[84:87], v[172:175], a[156:159]\n" \
"ds_read_b128 v[84:87], v22 offset:0\n" \
"v_cvt_pk_bf16_f32 v214, v180, v181\n" \
"v_mfma_f32_16x16x32_bf16 a[156:159], v[88:91], v[176:179], a[156:159]\n" \
"s_add_u32 m0, %[mB], 0x8800\n" \
"s_add_u32 s84, s63, 0x10000\n" \
"v_mfma_f32_16x16x32_bf16 a[176:179], v[96:99], v[152:155], a[176:179]\n" \
"buffer_load_dwordx4 v27, s[68:71], s84 offen lds\n" \
"v_mfma_f32_16x16x32_bf16 a[176:179], v[92:95], v[148:151], a[176:179]\n" \
"ds_read_b128 v[88:91], v23 offset:0\n" \
"v_cvt_pk_bf16_f32 v215, v182, v183\n" \
"v_mfma_f32_16x16x32_bf16 a[180:183], v[92:95], v[156:159], a[180:183]\n" \
"v_accvgpr_read_b32 v180, a4\n" \
"v_accvgpr_read_b32 v181, a5\n" \
"v_mfma_f32_16x16x32_bf16 a[180:183], v[96:99], v[160:163], a[180:183]\n" \
"s_add_u32 m0, %[mA], 0x9800\n" \
"s_add_u32 s85, s62, 0xd800\n" \
"v_mfma_f32_16x16x32_bf16 a[184:187], v[96:99], v[168:171], a[184:187]\n" \
"buffer_load_dwordx4 v24, s[76:79], s85 offen lds\n" \
"v_mfma_f32_16x16x32_bf16 a[184:187], v[92:95], v[164:167], a[184:187]\n" \
"v_accvgpr_read_b32 v182, a6\n" \
"v_accvgpr_read_b32 v183, a7\n" \
"v_mfma_f32_16x16x32_bf16 a[188:191], v[92:95], v[172:175], a[188:191]\n" \
"v_cvt_pk_bf16_f32 v216, v216, v217\n" \
"v_cvt_pk_bf16_f32 v217, v218, v219\n" \
"v_mfma_f32_16x16x32_bf16 a[188:191], v[96:99], v[176:179], a[188:191]\n" \
"v_accvgpr_read_b32 v220, a32\n" \
"v_accvgpr_read_b32 v221, a33\n" \
"v_mfma_f32_16x16x32_bf16 a[208:211], v[104:107], v[152:155], a[208:211]\n" \
"v_cvt_pk_bf16_f32 v218, v180, v181\n" \
"v_cvt_pk_bf16_f32 v219, v182, v183\n" \
"v_mfma_f32_16x16x32_bf16 a[208:211], v[100:103], v[148:151], a[208:211]\n" \
"v_accvgpr_read_b32 v222, a34\n" \
"v_accvgpr_read_b32 v223, a35\n" \
"v_mfma_f32_16x16x32_bf16 a[212:215], v[100:103], v[156:159], a[212:215]\n" \
"v_cvt_pk_bf16_f32 v220, v220, v221\n" \
"v_accvgpr_read_b32 v180, a36\n" \
"v_mfma_f32_16x16x32_bf16 a[212:215], v[104:107], v[160:163], a[212:215]\n" \
"v_accvgpr_read_b32 v181, a37\n" \
"v_accvgpr_read_b32 v182, a38\n" \
"v_mfma_f32_16x16x32_bf16 a[216:219], v[104:107], v[168:171], a[216:219]\n" \
"v_accvgpr_read_b32 v183, a39\n" \
"v_cvt_pk_bf16_f32 v221, v222, v223\n" \
"v_mfma_f32_16x16x32_bf16 a[216:219], v[100:103], v[164:167], a[216:219]\n" \
"v_accvgpr_read_b32 v224, a64\n" \
"v_accvgpr_read_b32 v225, a65\n" \
"v_mfma_f32_16x16x32_bf16 a[220:223], v[100:103], v[172:175], a[220:223]\n" \
"v_cvt_pk_bf16_f32 v222, v180, v181\n" \
"v_cvt_pk_bf16_f32 v223, v182, v183\n" \
"v_mfma_f32_16x16x32_bf16 a[220:223], v[104:107], v[176:179], a[220:223]\n" \
"v_accvgpr_read_b32 v226, a66\n" \
"v_accvgpr_read_b32 v227, a67\n" \
"v_mfma_f32_16x16x32_bf16 a[240:243], v[112:115], v[152:155], a[240:243]\n" \
"ds_read_b64_tr_b16 v[152:153], v32 offset:8192\n" \
"ds_read_b64_tr_b16 v[154:155], v32 offset:9216\n" \
"v_mfma_f32_16x16x32_bf16 a[240:243], v[108:111], v[148:151], a[240:243]\n" \
"ds_read_b64_tr_b16 v[148:149], v32 offset:0\n" \
"ds_read_b64_tr_b16 v[150:151], v32 offset:1024\n" \
"v_mfma_f32_16x16x32_bf16 a[244:247], v[108:111], v[156:159], a[244:247]\n" \
"ds_read_b64_tr_b16 v[156:157], v33 offset:0\n" \
"ds_read_b64_tr_b16 v[158:159], v33 offset:1024\n" \
"v_mfma_f32_16x16x32_bf16 a[244:247], v[112:115], v[160:163], a[244:247]\n" \
"ds_read_b64_tr_b16 v[160:161], v33 offset:8192\n" \
"ds_read_b64_tr_b16 v[162:163], v33 offset:9216\n" \
"v_mfma_f32_16x16x32_bf16 a[248:251], v[112:115], v[168:171], a[248:251]\n" \
"ds_read_b64_tr_b16 v[168:169], v34 offset:8192\n" \
"ds_read_b64_tr_b16 v[170:171], v34 offset:9216\n" \
"v_mfma_f32_16x16x32_bf16 a[248:251], v[108:111], v[164:167], a[248:251]\n" \
"ds_read_b64_tr_b16 v[164:165], v34 offset:0\n" \
"ds_read_b64_tr_b16 v[166:167], v34 offset:1024\n" \
"v_mfma_f32_16x16x32_bf16 a[252:255], v[108:111], v[172:175], a[252:255]\n" \
"ds_read_b64_tr_b16 v[172:173], v35 offset:0\n" \
"ds_read_b64_tr_b16 v[174:175], v35 offset:1024\n" \
"v_mfma_f32_16x16x32_bf16 a[252:255], v[112:115], v[176:179], a[252:255]\n" \
"s_add_u32 m0, %[mA], 0x9c00\n" \
"s_waitcnt lgkmcnt(14)\n" \
"s_nop 0\n" \
"v_smfmac_f32_16x16x64_bf16 a[16:19], v[140:143], v[84:91], v50\n" \
"v_mfma_f32_16x16x32_bf16 a[48:51], v[128:131], v[88:91], 0\n" \
"s_add_u32 s85, s62, 0xfc00\n" \
"ds_read_b64_tr_b16 v[176:177], v35 offset:8192\n" \
"v_mfma_f32_16x16x32_bf16 a[48:51], v[124:127], v[84:87], a[48:51]\n" \
"buffer_load_dwordx4 v25, s[76:79], s85 offen lds\n" \
"v_mfma_f32_16x16x32_bf16 a[80:83], v[84:87], v[132:135], 0\n" \
"s_add_u32 m0, %[mB], 0x8c00\n" \
"s_add_u32 s84, s63, 0x18000\n" \
"v_mfma_f32_16x16x32_bf16 a[80:83], v[88:91], v[136:139], a[80:83]\n" \
"ds_read_b64_tr_b16 v[178:179], v35 offset:9216\n" \
"v_cvt_pk_bf16_f32 v224, v224, v225\n" \
"v_smfmac_f32_16x16x64_bf16 a[112:115], v[140:143], v[52:59], v50\n" \
"v_mfma_f32_16x16x32_bf16 a[20:23], v[128:131], v[56:59], 0\n" \
"buffer_load_dwordx4 v27, s[68:71], s84 offen lds\n" \
"v_mfma_f32_16x16x32_bf16 a[20:23], v[124:127], v[52:55], a[20:23]\n" \
"s_add_u32 s88, s44, 0x0\n" \
"s_nop 0\n" \
"v_accvgpr_read_b32 v180, a68\n" \
"v_mfma_f32_16x16x32_bf16 a[52:55], v[116:119], v[52:55], 0\n" \
"v_accvgpr_read_b32 v181, a69\n" \
"v_accvgpr_read_b32 v182, a70\n" \
"v_mfma_f32_16x16x32_bf16 a[52:55], v[120:123], v[56:59], a[52:55]\n" \
"buffer_store_dwordx4 v[184:187], v20, s[80:83], s88 offen offset:0 sc1\n" \
"v_mfma_f32_16x16x32_bf16 a[84:87], v[56:59], v[136:139], 0\n" \
"v_accvgpr_read_b32 v183, a71\n" \
"v_cvt_pk_bf16_f32 v225, v226, v227\n" \
"v_mfma_f32_16x16x32_bf16 a[84:87], v[52:55], v[132:135], a[84:87]\n" \
"v_accvgpr_read_b32 v228, a96\n" \
"v_accvgpr_read_b32 v229, a97\n" \
"v_smfmac_f32_16x16x64_bf16 a[116:119], v[140:143], v[60:67], v50\n" \
"v_mfma_f32_16x16x32_bf16 a[24:27], v[124:127], v[60:63], 0\n" \
"v_cvt_pk_bf16_f32 v226, v180, v181\n" \
"v_cvt_pk_bf16_f32 v227, v182, v183\n" \
"v_mfma_f32_16x16x32_bf16 a[24:27], v[128:131], v[64:67], a[24:27]\n" \
"s_add_u32 s89, s44, 0x1000\n" \
"v_accvgpr_read_b32 v230, a98\n" \
"v_mfma_f32_16x16x32_bf16 a[56:59], v[120:123], v[64:67], 0\n" \
"buffer_store_dwordx4 v[188:191], v20, s[80:83], s89 offen offset:0 sc1\n" \
"v_mfma_f32_16x16x32_bf16 a[56:59], v[116:119], v[60:63], a[56:59]\n" \
"v_accvgpr_read_b32 v231, a99\n" \
"v_cvt_pk_bf16_f32 v228, v228, v229\n" \
"v_mfma_f32_16x16x32_bf16 a[88:91], v[60:63], v[132:135], 0\n" \
"v_accvgpr_read_b32 v180, a100\n" \
"v_accvgpr_read_b32 v181, a101\n" \
"v_mfma_f32_16x16x32_bf16 a[88:91], v[64:67], v[136:139], a[88:91]\n" \
"v_accvgpr_read_b32 v182, a102\n" \
"v_accvgpr_read_b32 v183, a103\n" \
"v_smfmac_f32_16x16x64_bf16 a[120:123], v[140:143], v[68:75], v50\n" \
"v_mfma_f32_16x16x32_bf16 a[28:31], v[128:131], v[72:75], 0\n" \
"v_cvt_pk_bf16_f32 v229, v230, v231\n" \
"v_accvgpr_read_b32 v232, a8\n" \
"v_mfma_f32_16x16x32_bf16 a[28:31], v[124:127], v[68:71], a[28:31]\n" \
"v_accvgpr_read_b32 v233, a9\n" \
"v_cvt_pk_bf16_f32 v230, v180, v181\n" \
"v_mfma_f32_16x16x32_bf16 a[60:63], v[116:119], v[68:71], 0\n" \
"s_add_u32 s88, s44, 0x0\n" \
"s_nop 0\n" \
"v_cvt_pk_bf16_f32 v231, v182, v183\n" \
"v_mfma_f32_16x16x32_bf16 a[60:63], v[120:123], v[72:75], a[60:63]\n" \
"buffer_store_dwordx4 v[192:195], v20, s[80:83], s88 offen offset:1024 sc1\n" \
"v_mfma_f32_16x16x32_bf16 a[92:95], v[72:75], v[136:139], 0\n" \
"v_accvgpr_read_b32 v234, a10\n" \
"v_accvgpr_read_b32 v235, a11\n" \
"v_mfma_f32_16x16x32_bf16 a[92:95], v[68:71], v[132:135], a[92:95]\n" \
"v_cvt_pk_bf16_f32 v232, v232, v233\n" \
"v_accvgpr_read_b32 v180, a12\n" \
"v_smfmac_f32_16x16x64_bf16 a[124:127], v[140:143], v[76:83], v50\n" \
"v_mfma_f32_16x16x32_bf16 a[0:3], v[124:127], v[76:79], 0\n" \
"v_accvgpr_read_b32 v181, a13\n" \
"v_accvgpr_read_b32 v182, a14\n" \
"v_mfma_f32_16x16x32_bf16 a[0:3], v[128:131], v[80:83], a[0:3]\n" \
"v_accvgpr_read_b32 v183, a15\n" \
"v_cvt_pk_bf16_f32 v233, v234, v235\n" \
"v_mfma_f32_16x16x32_bf16 a[4:7], v[120:123], v[80:83], 0\n" \
"v_accvgpr_read_b32 v236, a40\n" \
"v_accvgpr_read_b32 v237, a41\n" \
"v_mfma_f32_16x16x32_bf16 a[4:7], v[116:119], v[76:79], a[4:7]\n" \
"v_cvt_pk_bf16_f32 v234, v180, v181\n" \
"v_cvt_pk_bf16_f32 v235, v182, v183\n" \
"v_mfma_f32_16x16x32_bf16 a[32:35], v[76:79], v[132:135], 0\n" \
"v_accvgpr_read_b32 v238, a42\n" \
"v_accvgpr_read_b32 v239, a43\n" \
"v_mfma_f32_16x16x32_bf16 a[32:35], v[80:83], v[136:139], a[32:35]\n" \
"v_cvt_pk_bf16_f32 v236, v236, v237\n" \
"v_accvgpr_read_b32 v180, a44\n" \
"v_accvgpr_read_b32 v181, a45\n" \
"v_accvgpr_read_b32 v182, a46\n" \
"v_accvgpr_read_b32 v183, a47\n" \
"v_cvt_pk_bf16_f32 v237, v238, v239\n" \
"v_accvgpr_read_b32 v240, a72\n" \
"v_accvgpr_read_b32 v241, a73\n" \
"v_cvt_pk_bf16_f32 v238, v180, v181\n" \
"v_cvt_pk_bf16_f32 v239, v182, v183\n" \
"v_accvgpr_read_b32 v242, a74\n" \
"v_accvgpr_read_b32 v243, a75\n" \
"v_cvt_pk_bf16_f32 v240, v240, v241\n" \
"v_accvgpr_read_b32 v180, a76\n" \
"v_accvgpr_read_b32 v181, a77\n" \
"v_accvgpr_read_b32 v182, a78\n" \
"v_accvgpr_read_b32 v183, a79\n" \
"v_cvt_pk_bf16_f32 v241, v242, v243\n" \
"v_accvgpr_read_b32 v244, a104\n" \
"v_accvgpr_read_b32 v245, a105\n" \
"v_cvt_pk_bf16_f32 v242, v180, v181\n" \
"v_cvt_pk_bf16_f32 v243, v182, v183\n" \
"v_accvgpr_read_b32 v246, a106\n" \
"v_accvgpr_read_b32 v247, a107\n" \
"v_cvt_pk_bf16_f32 v244, v244, v245\n" \
"v_accvgpr_read_b32 v248, a128\n" \
"v_accvgpr_read_b32 v249, a129\n" \
"v_accvgpr_read_b32 v250, a130\n" \
"v_accvgpr_read_b32 v251, a131\n" \
"s_waitcnt lgkmcnt(0)\n" \
"s_nop 0\n" \
"v_mfma_f32_16x16x32_bf16 a[128:131], v[152:155], v[56:59], 0\n" \
"v_accvgpr_read_b32 v180, a108\n" \
"v_accvgpr_read_b32 v181, a109\n" \
"v_accvgpr_read_b32 v182, a110\n" \
"v_accvgpr_read_b32 v183, a111\n" \
"v_cvt_pk_bf16_f32 v245, v246, v247\n" \
"v_cvt_pk_bf16_f32 v246, v180, v181\n" \
"v_cvt_pk_bf16_f32 v247, v182, v183\n" \
"v_cvt_pk_bf16_f32 v248, v248, v249\n" \
"v_mfma_f32_16x16x32_bf16 a[128:131], v[148:151], v[52:55], a[128:131]\n" \
"v_accvgpr_read_b32 v180, a132\n" \
"v_accvgpr_read_b32 v181, a133\n" \
"v_accvgpr_read_b32 v182, a134\n" \
"v_accvgpr_read_b32 v183, a135\n" \
"v_cvt_pk_bf16_f32 v249, v250, v251\n" \
"v_accvgpr_read_b32 v252, a160\n" \
"v_accvgpr_read_b32 v253, a161\n" \
"v_cvt_pk_bf16_f32 v250, v180, v181\n" \
"v_cvt_pk_bf16_f32 v251, v182, v183\n" \
"v_accvgpr_read_b32 v254, a162\n" \
"v_accvgpr_read_b32 v255, a163\n" \
"v_cvt_pk_bf16_f32 v252, v252, v253\n" \
"v_accvgpr_read_b32 v180, a164\n" \
"v_accvgpr_read_b32 v181, a165\n" \
"v_accvgpr_read_b32 v182, a166\n" \
"v_accvgpr_read_b32 v183, a167\n" \
"v_cvt_pk_bf16_f32 v253, v254, v255\n" \
"v_cvt_pk_bf16_f32 v254, v180, v181\n" \
"v_cvt_pk_bf16_f32 v255, v182, v183\n" \
"v_accvgpr_write_b32 a36, v252\n" \
"v_accvgpr_write_b32 a37, v253\n" \
"v_mfma_f32_16x16x32_bf16 a[132:135], v[156:159], v[52:55], 0\n" \
"v_accvgpr_write_b32 a38, v254\n" \
"v_accvgpr_write_b32 a39, v255\n" \
"v_accvgpr_read_b32 v252, a192\n" \
"v_accvgpr_read_b32 v253, a193\n" \
"v_accvgpr_read_b32 v254, a194\n" \
"v_accvgpr_read_b32 v255, a195\n" \
"v_cvt_pk_bf16_f32 v252, v252, v253\n" \
"v_accvgpr_read_b32 v180, a196\n" \
"v_accvgpr_read_b32 v181, a197\n" \
"v_accvgpr_read_b32 v182, a198\n" \
"v_accvgpr_read_b32 v183, a199\n" \
"v_cvt_pk_bf16_f32 v253, v254, v255\n" \
"v_cvt_pk_bf16_f32 v254, v180, v181\n" \
"v_cvt_pk_bf16_f32 v255, v182, v183\n" \
"v_accvgpr_write_b32 a64, v252\n" \
"v_accvgpr_write_b32 a65, v253\n" \
"v_mfma_f32_16x16x32_bf16 a[132:135], v[160:163], v[56:59], a[132:135]\n" \
"v_accvgpr_write_b32 a66, v254\n" \
"v_accvgpr_write_b32 a67, v255\n" \
"v_accvgpr_read_b32 v252, a224\n" \
"v_accvgpr_read_b32 v253, a225\n" \
"v_accvgpr_read_b32 v254, a226\n" \
"v_accvgpr_read_b32 v255, a227\n" \
"v_cvt_pk_bf16_f32 v252, v252, v253\n" \
"v_accvgpr_read_b32 v180, a228\n" \
"v_accvgpr_read_b32 v181, a229\n" \
"v_accvgpr_read_b32 v182, a230\n" \
"v_accvgpr_read_b32 v183, a231\n" \
"v_cvt_pk_bf16_f32 v253, v254, v255\n" \
"v_cvt_pk_bf16_f32 v254, v180, v181\n" \
"v_cvt_pk_bf16_f32 v255, v182, v183\n" \
"v_accvgpr_write_b32 a68, v252\n" \
"v_accvgpr_write_b32 a69, v253\n" \
"v_accvgpr_write_b32 a70, v254\n" \
"v_accvgpr_write_b32 a71, v255\n" \
"v_accvgpr_read_b32 v252, a136\n" \
"v_accvgpr_read_b32 v253, a137\n" \
"v_accvgpr_read_b32 v254, a138\n" \
"v_accvgpr_read_b32 v255, a139\n" \
"v_mfma_f32_16x16x32_bf16 a[136:139], v[168:171], v[56:59], 0\n" \
"ds_read_b128 v[84:87], v48 offset:0\n" \
"ds_read_b128 v[88:91], v49 offset:0\n" \
"ds_read_b128 v[92:95], v48 offset:2048\n" \
"v_mfma_f32_16x16x32_bf16 a[136:139], v[164:167], v[52:55], a[136:139]\n" \
"ds_read_b128 v[96:99], v49 offset:2048\n" \
"v_accvgpr_read_b32 v180, a140\n" \
"v_accvgpr_read_b32 v181, a141\n" \
"v_accvgpr_read_b32 v182, a142\n" \
"v_accvgpr_read_b32 v183, a143\n" \
"v_mfma_f32_16x16x32_bf16 a[140:143], v[172:175], v[52:55], 0\n" \
"ds_read_b128 v[100:103], v48 offset:4096\n" \
"ds_read_b128 v[104:107], v49 offset:4096\n" \
"s_add_u32 s89, s44, 0x1000\n" \
"buffer_store_dwordx4 v[196:199], v20, s[80:83], s89 offen offset:1024 sc1\n" \
"ds_read_b128 v[108:111], v48 offset:6144\n" \
"v_mfma_f32_16x16x32_bf16 a[140:143], v[176:179], v[56:59], a[140:143]\n" \
"v_cvt_pk_bf16_f32 v252, v252, v253\n" \
"v_cvt_pk_bf16_f32 v253, v254, v255\n" \
"v_mfma_f32_16x16x32_bf16 a[160:163], v[152:155], v[64:67], 0\n" \
"ds_read_b128 v[112:115], v49 offset:6144\n" \
"v_cvt_pk_bf16_f32 v254, v180, v181\n" \
"v_mfma_f32_16x16x32_bf16 a[160:163], v[148:151], v[60:63], a[160:163]\n" \
"v_cvt_pk_bf16_f32 v255, v182, v183\n" \
"v_accvgpr_write_b32 a96, v252\n" \
"v_mfma_f32_16x16x32_bf16 a[164:167], v[156:159], v[60:63], 0\n" \
"v_accvgpr_write_b32 a97, v253\n" \
"v_accvgpr_write_b32 a98, v254\n" \
"v_mfma_f32_16x16x32_bf16 a[164:167], v[160:163], v[64:67], a[164:167]\n" \
"v_accvgpr_write_b32 a99, v255\n" \
"v_accvgpr_read_b32 v252, a168\n" \
"v_accvgpr_read_b32 v253, a169\n" \
"v_accvgpr_read_b32 v254, a170\n" \
"v_accvgpr_read_b32 v255, a171\n" \
"v_mfma_f32_16x16x32_bf16 a[168:171], v[168:171], v[64:67], 0\n" \
"v_accvgpr_read_b32 v180, a172\n" \
"v_accvgpr_read_b32 v181, a173\n" \
"v_mfma_f32_16x16x32_bf16 a[168:171], v[164:167], v[60:63], a[168:171]\n" \
"v_accvgpr_read_b32 v182, a174\n" \
"v_accvgpr_read_b32 v183, a175\n" \
"v_mfma_f32_16x16x32_bf16 a[172:175], v[172:175], v[60:63], 0\n" \
"v_cvt_pk_bf16_f32 v252, v252, v253\n" \
"v_cvt_pk_bf16_f32 v253, v254, v255\n" \
"v_mfma_f32_16x16x32_bf16 a[172:175], v[176:179], v[64:67], a[172:175]\n" \
"v_cvt_pk_bf16_f32 v254, v180, v181\n" \
"v_cvt_pk_bf16_f32 v255, v182, v183\n" \
"v_mfma_f32_16x16x32_bf16 a[192:195], v[152:155], v[72:75], 0\n" \
"v_accvgpr_write_b32 a100, v252\n" \
"v_accvgpr_write_b32 a101, v253\n" \
"v_mfma_f32_16x16x32_bf16 a[192:195], v[148:151], v[68:71], a[192:195]\n" \
"v_accvgpr_write_b32 a102, v254\n" \
"v_accvgpr_write_b32 a103, v255\n" \
"s_add_u32 s62, s62, 0x80\n" \
"v_accvgpr_read_b32 v252, a200\n" \
"v_accvgpr_read_b32 v253, a201\n" \
"v_accvgpr_read_b32 v254, a202\n" \
"s_waitcnt vmcnt(4) lgkmcnt(0)\n" \
"s_barrier\n" \
"v_mfma_f32_16x16x32_bf16 a[196:199], v[156:159], v[68:71], 0\n" \
"v_accvgpr_read_b32 v255, a203\n" \
"s_add_u32 m0, s40, 0x0\n" \
"v_mfma_f32_16x16x32_bf16 a[196:199], v[160:163], v[72:75], a[196:199]\n" \
"s_cmp_lt_u32 s62, 0x480\n" \
"s_cselect_b32 s62, s62, 0\n" \
"v_mfma_f32_16x16x32_bf16 a[200:203], v[168:171], v[72:75], 0\n" \
"s_lshl_b32 s72, s62, 6\n" \
"s_add_u32 s72, s72, s37\n" \
"v_mfma_f32_16x16x32_bf16 a[200:203], v[164:167], v[68:71], a[200:203]\n" \
"s_add_u32 s85, s72, 0x0\n" \
"s_nop 0\n" \
"buffer_load_dwordx4 v20, s[80:83], s85 offen sc1 lds\n" \
"v_accvgpr_read_b32 v180, a204\n" \
"v_accvgpr_read_b32 v181, a205\n" \
"v_accvgpr_read_b32 v182, a206\n" \
"v_accvgpr_read_b32 v183, a207\n" \
"v_mfma_f32_16x16x32_bf16 a[204:207], v[172:175], v[68:71], 0\n" \
"s_add_u32 m0, s40, 0x1000\n" \
"s_add_u32 s84, s72, 0x1000\n" \
"v_mfma_f32_16x16x32_bf16 a[204:207], v[176:179], v[72:75], a[204:207]\n" \
"buffer_load_dwordx4 v20, s[80:83], s84 offen sc1 lds\n" \
"v_mfma_f32_16x16x32_bf16 a[224:227], v[152:155], v[80:83], 0\n" \
"v_cvt_pk_bf16_f32 v252, v252, v253\n" \
"v_cvt_pk_bf16_f32 v253, v254, v255\n" \
"v_mfma_f32_16x16x32_bf16 a[224:227], v[148:151], v[76:79], a[224:227]\n" \
"v_cvt_pk_bf16_f32 v254, v180, v181\n" \
"v_cvt_pk_bf16_f32 v255, v182, v183\n" \
"v_mfma_f32_16x16x32_bf16 a[228:231], v[156:159], v[76:79], 0\n" \
"v_accvgpr_write_b32 a8, v252\n" \
"v_accvgpr_write_b32 a9, v253\n" \
"v_mfma_f32_16x16x32_bf16 a[228:231], v[160:163], v[80:83], a[228:231]\n" \
"v_accvgpr_write_b32 a10, v254\n" \
"v_accvgpr_write_b32 a11, v255\n" \
"v_accvgpr_read_b32 v252, a232\n" \
"v_accvgpr_read_b32 v253, a233\n" \
"v_accvgpr_read_b32 v254, a234\n" \
"v_accvgpr_read_b32 v255, a235\n" \
"v_mfma_f32_16x16x32_bf16 a[232:235], v[168:171], v[80:83], 0\n" \
"s_add_u32 m0, s40, 0x3000\n" \
"s_add_u32 s85, s62, 0x0\n" \
"buffer_load_dwordx4 v21, s[76:79], s85 offen lds\n" \
"v_mfma_f32_16x16x32_bf16 a[232:235], v[164:167], v[76:79], a[232:235]\n" \
"v_accvgpr_read_b32 v180, a236\n" \
"v_accvgpr_read_b32 v181, a237\n" \
"v_accvgpr_read_b32 v182, a238\n" \
"v_accvgpr_read_b32 v183, a239\n" \
"v_mfma_f32_16x16x32_bf16 a[236:239], v[172:175], v[76:79], 0\n" \
"v_cvt_pk_bf16_f32 v252, v252, v253\n" \
"s_add_u32 m0, %[mA], 0x0\n" \
"v_mfma_f32_16x16x32_bf16 a[236:239], v[176:179], v[80:83], a[236:239]\n" \
"s_add_u32 s84, s62, 0x0\n" \
"s_nop 0\n" \
"v_cvt_pk_bf16_f32 v253, v254, v255\n" \
"v_cvt_pk_bf16_f32 v254, v180, v181\n" \
"v_cvt_pk_bf16_f32 v255, v182, v183\n" \
"v_accvgpr_write_b32 a12, v252\n" \
"v_accvgpr_write_b32 a13, v253\n" \
"v_accvgpr_write_b32 a14, v254\n" \
"v_accvgpr_write_b32 a15, v255\n" \
"v_accvgpr_read_b32 v252, a144\n" \
"v_accvgpr_read_b32 v253, a145\n" \
"v_accvgpr_read_b32 v254, a146\n" \
"v_accvgpr_read_b32 v255, a147\n" \
"v_mfma_f32_16x16x32_bf16 a[144:147], v[152:155], v[88:91], 0\n" \
"buffer_load_dwordx4 v24, s[76:79], s84 offen lds\n" \
"v_mfma_f32_16x16x32_bf16 a[144:147], v[148:151], v[84:87], a[144:147]\n" \
"v_cvt_pk_bf16_f32 v252, v252, v253\n" \
"v_accvgpr_read_b32 v180, a176\n" \
"v_accvgpr_read_b32 v181, a177\n" \
"v_accvgpr_read_b32 v182, a178\n" \
"v_accvgpr_read_b32 v183, a179\n" \
"v_cvt_pk_bf16_f32 v253, v254, v255\n" \
"v_cvt_pk_bf16_f32 v254, v180, v181\n" \
"v_cvt_pk_bf16_f32 v255, v182, v183\n" \
"v_accvgpr_write_b32 a40, v252\n" \
"v_accvgpr_write_b32 a41, v253\n" \
"v_accvgpr_write_b32 a42, v254\n" \
"v_accvgpr_write_b32 a43, v255\n" \
"v_accvgpr_read_b32 v252, a148\n" \
"v_accvgpr_read_b32 v253, a149\n" \
"v_accvgpr_read_b32 v254, a150\n" \
"v_accvgpr_read_b32 v255, a151\n" \
"v_mfma_f32_16x16x32_bf16 a[148:151], v[156:159], v[84:87], 0\n" \
"v_cvt_pk_bf16_f32 v252, v252, v253\n" \
"s_add_u32 m0, %[mA], 0x400\n" \
"v_mfma_f32_16x16x32_bf16 a[148:151], v[160:163], v[88:91], a[148:151]\n" \
"s_add_u32 s85, s62, 0x2400\n" \
"v_accvgpr_read_b32 v180, a180\n" \
"v_accvgpr_read_b32 v181, a181\n" \
"v_accvgpr_read_b32 v182, a182\n" \
"v_accvgpr_read_b32 v183, a183\n" \
"v_cvt_pk_bf16_f32 v253, v254, v255\n" \
"v_cvt_pk_bf16_f32 v254, v180, v181\n" \
"v_cvt_pk_bf16_f32 v255, v182, v183\n" \
"v_accvgpr_write_b32 a44, v252\n" \
"v_accvgpr_write_b32 a45, v253\n" \
"v_accvgpr_write_b32 a46, v254\n" \
"v_accvgpr_write_b32 a47, v255\n" \
"v_accvgpr_read_b32 v252, a152\n" \
"v_accvgpr_read_b32 v253, a153\n" \
"v_accvgpr_read_b32 v254, a154\n" \
"v_accvgpr_read_b32 v255, a155\n" \
"v_mfma_f32_16x16x32_bf16 a[152:155], v[168:171], v[88:91], 0\n" \
"L_pf5:\n" \
"s_cbranch_execz L_pf9\n" \
"s_nop 0\n" \
"buffer_load_dwordx4 v25, s[76:79], s85 offen lds\n" \
"v_mfma_f32_16x16x32_bf16 a[152:155], v[164:167], v[84:87], a[152:155]\n" \
"v_cvt_pk_bf16_f32 v252, v252, v253\n" \
"v_accvgpr_read_b32 v180, a184\n" \
"v_accvgpr_read_b32 v181, a185\n" \
"v_accvgpr_read_b32 v182, a186\n" \
"v_accvgpr_read_b32 v183, a187\n" \
"v_cvt_pk_bf16_f32 v253, v254, v255\n" \
"v_cvt_pk_bf16_f32 v254, v180, v181\n" \
"v_cvt_pk_bf16_f32 v255, v182, v183\n" \
"s_lshl_b32 s63, s62, 12\n" \
"v_accvgpr_write_b32 a72, v252\n" \
"v_accvgpr_write_b32 a73, v253\n" \
"v_accvgpr_write_b32 a74, v254\n" \
"v_accvgpr_write_b32 a75, v255\n" \
"v_accvgpr_read_b32 v252, a156\n" \
"v_accvgpr_read_b32 v253, a157\n" \
"v_accvgpr_read_b32 v254, a158\n" \
"v_accvgpr_read_b32 v255, a159\n" \
"v_mfma_f32_16x16x32_bf16 a[156:159], v[172:175], v[84:87], 0\n" \
"s_add_u32 m0, %[mB], 0x0\n" \
"s_add_u32 s84, s63, 0x0\n" \
"s_nop 0\n" \
"buffer_load_dwordx4 v26, s[68:71], s84 offen lds\n" \
"v_mfma_f32_16x16x32_bf16 a[156:159], v[176:179], v[88:91], a[156:159]\n" \
"s_add_u32 m0, %[mA], 0x800\n" \
"s_add_u32 s85, s62, 0x4800\n" \
"v_mfma_f32_16x16x32_bf16 a[176:179], v[152:155], v[96:99], 0\n" \
"v_accvgpr_read_b32 v180, a188\n" \
"v_accvgpr_read_b32 v181, a189\n" \
"v_mfma_f32_16x16x32_bf16 a[176:179], v[148:151], v[92:95], a[176:179]\n" \
"buffer_load_dwordx4 v24, s[76:79], s85 offen lds\n" \
"v_mfma_f32_16x16x32_bf16 a[180:183], v[156:159], v[92:95], 0\n" \
"s_add_u32 m0, %[mA], 0xc00\n" \
"s_add_u32 s84, s62, 0x6c00\n" \
"v_mfma_f32_16x16x32_bf16 a[180:183], v[160:163], v[96:99], a[180:183]\n" \
"v_accvgpr_read_b32 v182, a190\n" \
"v_accvgpr_read_b32 v183, a191\n" \
"v_mfma_f32_16x16x32_bf16 a[184:187], v[168:171], v[96:99], 0\n" \
"buffer_load_dwordx4 v25, s[76:79], s84 offen lds\n" \
"v_mfma_f32_16x16x32_bf16 a[184:187], v[164:167], v[92:95], a[184:187]\n" \
"v_cvt_pk_bf16_f32 v252, v252, v253\n" \
"v_cvt_pk_bf16_f32 v253, v254, v255\n" \
"v_mfma_f32_16x16x32_bf16 a[188:191], v[172:175], v[92:95], 0\n" \
"v_cvt_pk_bf16_f32 v254, v180, v181\n" \
"v_cvt_pk_bf16_f32 v255, v182, v183\n" \
"v_mfma_f32_16x16x32_bf16 a[188:191], v[176:179], v[96:99], a[188:191]\n" \
"v_accvgpr_write_b32 a76, v252\n" \
"v_accvgpr_write_b32 a77, v253\n" \
"v_accvgpr_write_b32 a78, v254\n" \
"v_accvgpr_write_b32 a79, v255\n" \
"v_accvgpr_read_b32 v252, a208\n" \
"v_accvgpr_read_b32 v253, a209\n" \
"v_accvgpr_read_b32 v254, a210\n" \
"v_accvgpr_read_b32 v255, a211\n" \
"v_mfma_f32_16x16x32_bf16 a[208:211], v[152:155], v[104:107], 0\n" \
"v_cvt_pk_bf16_f32 v252, v252, v253\n" \
"s_add_u32 m0, %[mB], 0x400\n" \
"v_mfma_f32_16x16x32_bf16 a[208:211], v[148:151], v[100:103], a[208:211]\n" \
"s_add_u32 s85, s63, 0x8000\n" \
"v_accvgpr_read_b32 v180, a240\n" \
"v_accvgpr_read_b32 v181, a241\n" \
"v_accvgpr_read_b32 v182, a242\n" \
"v_accvgpr_read_b32 v183, a243\n" \
"v_cvt_pk_bf16_f32 v253, v254, v255\n" \
"v_cvt_pk_bf16_f32 v254, v180, v181\n" \
"v_cvt_pk_bf16_f32 v255, v182, v183\n" \
"v_accvgpr_write_b32 a104, v252\n" \
"v_accvgpr_write_b32 a105, v253\n" \
"v_accvgpr_write_b32 a106, v254\n" \
"v_accvgpr_write_b32 a107, v255\n" \
"v_accvgpr_read_b32 v252, a212\n" \
"v_accvgpr_read_b32 v253, a213\n" \
"v_accvgpr_read_b32 v254, a214\n" \
"v_accvgpr_read_b32 v255, a215\n" \
"v_mfma_f32_16x16x32_bf16 a[212:215], v[156:159], v[100:103], 0\n" \
"buffer_load_dwordx4 v26, s[68:71], s85 offen lds\n" \
"ds_read_b64_tr_b16 v[140:141], v39 offset:32768\n" \
"ds_read_b64_tr_b16 v[142:143], v39 offset:33728\n" \
"ds_read_b128 v[84:87], v22 offset:32768\n" \
"ds_read_b128 v[88:91], v23 offset:32768\n" \
"v_mfma_f32_16x16x32_bf16 a[212:215], v[160:163], v[104:107], a[212:215]\n" \
"ds_read_b64_tr_b16 v[128:129], v37 offset:36864\n" \
"ds_read_b64_tr_b16 v[130:131], v37 offset:37376\n" \
"v_accvgpr_read_b32 v184, a216\n" \
"v_accvgpr_read_b32 v185, a217\n" \
"v_accvgpr_read_b32 v186, a218\n" \
"v_accvgpr_read_b32 v187, a219\n" \
"v_mfma_f32_16x16x32_bf16 a[216:219], v[168:171], v[104:107], 0\n" \
"s_add_u32 m0, %[mA], 0x1000\n" \
"s_add_u32 s84, s62, 0x9000\n" \
"buffer_load_dwordx4 v24, s[76:79], s84 offen lds\n" \
"ds_read_b64_tr_b16 v[124:125], v37 offset:32768\n" \
"ds_read_b64_tr_b16 v[126:127], v37 offset:33280\n" \
"v_mfma_f32_16x16x32_bf16 a[216:219], v[164:167], v[100:103], a[216:219]\n" \
"v_cvt_pk_bf16_f32 v252, v252, v253\n" \
"v_accvgpr_read_b32 v180, a244\n" \
"v_accvgpr_read_b32 v181, a245\n" \
"v_accvgpr_read_b32 v182, a246\n" \
"v_accvgpr_read_b32 v183, a247\n" \
"v_cvt_pk_bf16_f32 v253, v254, v255\n" \
"v_cvt_pk_bf16_f32 v254, v180, v181\n" \
"v_cvt_pk_bf16_f32 v255, v182, v183\n" \
"v_accvgpr_write_b32 a108, v252\n" \
"v_accvgpr_write_b32 a109, v253\n" \
"v_accvgpr_write_b32 a110, v254\n" \
"v_accvgpr_write_b32 a111, v255\n" \
"v_accvgpr_read_b32 v252, a220\n" \
"v_accvgpr_read_b32 v253, a221\n" \
"v_accvgpr_read_b32 v254, a222\n" \
"v_accvgpr_read_b32 v255, a223\n" \
"v_mfma_f32_16x16x32_bf16 a[220:223], v[172:175], v[100:103], 0\n" \
"ds_read_b64_tr_b16 v[132:133], v38 offset:32768\n" \
"ds_read_b64_tr_b16 v[134:135], v38 offset:33280\n" \
"ds_read_b64_tr_b16 v[136:137], v38 offset:36864\n" \
"ds_read_b64_tr_b16 v[138:139], v38 offset:37376\n" \
"v_mfma_f32_16x16x32_bf16 a[220:223], v[176:179], v[104:107], a[220:223]\n" \
"ds_read_b128 v[52:55], v28 offset:32768\n" \
"ds_read_b128 v[56:59], v29 offset:32768\n" \
"v_mfma_f32_16x16x32_bf16 a[240:243], v[152:155], v[112:115], 0\n" \
"s_add_u32 m0, %[mA], 0x1400\n" \
"s_add_u32 s85, s62, 0xb400\n" \
"buffer_load_dwordx4 v25, s[76:79], s85 offen lds\n" \
"v_mfma_f32_16x16x32_bf16 a[240:243], v[148:151], v[108:111], a[240:243]\n" \
"ds_read_b64_tr_b16 v[116:117], v36 offset:32768\n" \
"ds_read_b64_tr_b16 v[118:119], v36 offset:33280\n" \
"v_mfma_f32_16x16x32_bf16 a[244:247], v[156:159], v[108:111], 0\n" \
"s_add_u32 m0, %[mB], 0x800\n" \
"s_add_u32 s84, s63, 0x10000\n" \
"v_mfma_f32_16x16x32_bf16 a[244:247], v[160:163], v[112:115], a[244:247]\n" \
"buffer_load_dwordx4 v27, s[68:71], s84 offen lds\n" \
"ds_read_b64_tr_b16 v[120:121], v36 offset:36864\n" \
"ds_read_b64_tr_b16 v[122:123], v36 offset:37376\n" \
"v_accvgpr_read_b32 v180, a248\n" \
"v_accvgpr_read_b32 v181, a249\n" \
"v_accvgpr_read_b32 v182, a250\n" \
"v_accvgpr_read_b32 v183, a251\n" \
"v_mfma_f32_16x16x32_bf16 a[248:251], v[168:171], v[112:115], 0\n" \
"ds_read_b128 v[60:63], v28 offset:34816\n" \
"v_cvt_pk_bf16_f32 v184, v184, v185\n" \
"v_mfma_f32_16x16x32_bf16 a[248:251], v[164:167], v[108:111], a[248:251]\n" \
"v_cvt_pk_bf16_f32 v185, v186, v187\n" \
"v_cvt_pk_bf16_f32 v186, v180, v181\n" \
"v_cvt_pk_bf16_f32 v187, v182, v183\n" \
"v_accvgpr_read_b32 v180, a252\n" \
"v_accvgpr_read_b32 v181, a253\n" \
"v_accvgpr_read_b32 v182, a254\n" \
"v_accvgpr_read_b32 v183, a255\n" \
"v_mfma_f32_16x16x32_bf16 a[252:255], v[172:175], v[108:111], 0\n" \
"ds_read_b128 v[64:67], v29 offset:34816\n" \
"s_add_u32 m0, %[mA], 0x1800\n" \
"v_mfma_f32_16x16x32_bf16 a[252:255], v[176:179], v[112:115], a[252:255]\n" \
"s_add_u32 s85, s62, 0xd800\n" \
"buffer_load_dwordx4 v24, s[76:79], s85 offen lds\n" \
"s_waitcnt lgkmcnt(12)\n" \
"v_smfmac_f32_16x16x64_bf16 a[16:19], v[140:143], v[84:91], v50\n" \
"v_mfma_f32_16x16x32_bf16 a[48:51], v[128:131], v[88:91], a[48:51]\n" \
"s_add_u32 m0, %[mA], 0x1c00\n" \
"s_add_u32 s85, s62, 0xfc00\n" \
"v_mfma_f32_16x16x32_bf16 a[48:51], v[124:127], v[84:87], a[48:51]\n" \
"ds_read_b128 v[68:71], v28 offset:36864\n" \
"s_waitcnt lgkmcnt(8)\n" \
"v_mfma_f32_16x16x32_bf16 a[80:83], v[84:87], v[132:135], a[80:83]\n" \
"buffer_load_dwordx4 v25, s[76:79], s85 offen lds\n" \
"v_mfma_f32_16x16x32_bf16 a[80:83], v[88:91], v[136:139], a[80:83]\n" \
"ds_read_b128 v[72:75], v29 offset:36864\n" \
"s_waitcnt lgkmcnt(6)\n" \
"v_smfmac_f32_16x16x64_bf16 a[112:115], v[140:143], v[52:59], v50\n" \
"v_mfma_f32_16x16x32_bf16 a[20:23], v[128:131], v[56:59], a[20:23]\n" \
"s_add_u32 m0, %[mB], 0xc00\n" \
"s_add_u32 s84, s63, 0x18000\n" \
"buffer_load_dwordx4 v27, s[68:71], s84 offen lds\n" \
"v_mfma_f32_16x16x32_bf16 a[20:23], v[124:127], v[52:55], a[20:23]\n" \
"ds_read_b128 v[76:79], v28 offset:38912\n" \
"ds_read_b128 v[80:83], v29 offset:38912\n" \
"v_mfma_f32_16x16x32_bf16 a[52:55], v[116:119], v[52:55], a[52:55]\n" \
"s_add_u32 s88, s44, 0x0\n" \
"s_waitcnt lgkmcnt(5)\n" \
"v_mfma_f32_16x16x32_bf16 a[52:55], v[120:123], v[56:59], a[52:55]\n" \
"buffer_store_dwordx4 v[200:203], v20, s[80:83], s88 offen offset:2048 sc1\n" \
"v_mfma_f32_16x16x32_bf16 a[84:87], v[56:59], v[136:139], a[84:87]\n" \
"ds_read_b64_tr_b16 v[152:153], v32 offset:40960\n" \
"ds_read_b64_tr_b16 v[154:155], v32 offset:41984\n" \
"v_mfma_f32_16x16x32_bf16 a[84:87], v[52:55], v[132:135], a[84:87]\n" \
"ds_read_b64_tr_b16 v[148:149], v32 offset:32768\n" \
"s_waitcnt lgkmcnt(7)\n" \
"v_smfmac_f32_16x16x64_bf16 a[116:119], v[140:143], v[60:67], v50\n" \
"v_mfma_f32_16x16x32_bf16 a[24:27], v[124:127], v[60:63], a[24:27]\n" \
"ds_read_b64_tr_b16 v[150:151], v32 offset:33792\n" \
"ds_read_b64_tr_b16 v[156:157], v33 offset:32768\n" \
"v_mfma_f32_16x16x32_bf16 a[24:27], v[128:131], v[64:67], a[24:27]\n" \
"ds_read_b64_tr_b16 v[158:159], v33 offset:33792\n" \
"ds_read_b64_tr_b16 v[160:161], v33 offset:40960\n" \
"v_mfma_f32_16x16x32_bf16 a[56:59], v[120:123], v[64:67], a[56:59]\n" \
"ds_read_b64_tr_b16 v[162:163], v33 offset:41984\n" \
"s_add_u32 s89, s44, 0x1000\n" \
"v_mfma_f32_16x16x32_bf16 a[56:59], v[116:119], v[60:63], a[56:59]\n" \
"buffer_store_dwordx4 v[204:207], v20, s[80:83], s89 offen offset:2048 sc1\n" \
"v_mfma_f32_16x16x32_bf16 a[88:91], v[60:63], v[132:135], a[88:91]\n" \
"ds_read_b64_tr_b16 v[168:169], v34 offset:40960\n" \
"ds_read_b64_tr_b16 v[170:171], v34 offset:41984\n" \
"v_mfma_f32_16x16x32_bf16 a[88:91], v[64:67], v[136:139], a[88:91]\n" \
"ds_read_b64_tr_b16 v[164:165], v34 offset:32768\n" \
"s_waitcnt lgkmcnt(13)\n" \
"v_smfmac_f32_16x16x64_bf16 a[120:123], v[140:143], v[68:75], v50\n" \
"v_mfma_f32_16x16x32_bf16 a[28:31], v[128:131], v[72:75], a[28:31]\n" \
"ds_read_b64_tr_b16 v[166:167], v34 offset:33792\n" \
"ds_read_b64_tr_b16 v[172:173], v35 offset:32768\n" \
"v_mfma_f32_16x16x32_bf16 a[28:31], v[124:127], v[68:71], a[28:31]\n" \
"ds_read_b64_tr_b16 v[174:175], v35 offset:33792\n" \
"ds_read_b64_tr_b16 v[176:177], v35 offset:40960\n" \
"v_mfma_f32_16x16x32_bf16 a[60:63], v[116:119], v[68:71], a[60:63]\n" \
"ds_read_b64_tr_b16 v[178:179], v35 offset:41984\n" \
"s_add_u32 s88, s44, 0x0\n" \
"v_mfma_f32_16x16x32_bf16 a[60:63], v[120:123], v[72:75], a[60:63]\n" \
"buffer_store_dwordx4 v[208:211], v20, s[80:83], s88 offen offset:3072 sc1\n" \
"v_mfma_f32_16x16x32_bf16 a[92:95], v[72:75], v[136:139], a[92:95]\n" \
"ds_read_b128 v[84:87], v48 offset:32768\n" \
"ds_read_b128 v[88:91], v49 offset:32768\n" \
"v_mfma_f32_16x16x32_bf16 a[92:95], v[68:71], v[132:135], a[92:95]\n" \
"ds_read_b128 v[92:95], v48 offset:34816\n" \
"s_waitcnt lgkmcnt(15)\n" \
"s_nop 0\n" \
"v_smfmac_f32_16x16x64_bf16 a[124:127], v[140:143], v[76:83], v50\n" \
"v_mfma_f32_16x16x32_bf16 a[0:3], v[124:127], v[76:79], a[0:3]\n" \
"ds_read_b128 v[96:99], v49 offset:34816\n" \
"ds_read_b128 v[100:103], v48 offset:36864\n" \
"v_mfma_f32_16x16x32_bf16 a[0:3], v[128:131], v[80:83], a[0:3]\n" \
"ds_read_b128 v[104:107], v49 offset:36864\n" \
"ds_read_b128 v[108:111], v48 offset:38912\n" \
"v_mfma_f32_16x16x32_bf16 a[4:7], v[120:123], v[80:83], a[4:7]\n" \
"s_add_u32 s62, s62, 0x80\n" \
"ds_read_b128 v[112:115], v49 offset:38912\n" \
"v_mfma_f32_16x16x32_bf16 a[4:7], v[116:119], v[76:79], a[4:7]\n" \
"s_cmp_lt_u32 s62, 0x480\n" \
"s_cselect_b32 s62, s62, 0\n" \
"v_mfma_f32_16x16x32_bf16 a[32:35], v[76:79], v[132:135], a[32:35]\n" \
"s_lshl_b32 s72, s62, 6\n" \
"s_add_u32 s72, s72, s37\n" \
"v_mfma_f32_16x16x32_bf16 a[32:35], v[80:83], v[136:139], a[32:35]\n" \
"s_lshl_b32 s63, s62, 12\n" \
"v_cvt_pk_bf16_f32 v252, v252, v253\n" \
"v_mfma_f32_16x16x32_bf16 a[128:131], v[152:155], v[56:59], a[128:131]\n" \
"v_cvt_pk_bf16_f32 v253, v254, v255\n" \
"v_cvt_pk_bf16_f32 v254, v180, v181\n" \
"v_mfma_f32_16x16x32_bf16 a[128:131], v[148:151], v[52:55], a[128:131]\n" \
"v_cvt_pk_bf16_f32 v255, v182, v183\n" \
"s_waitcnt lgkmcnt(11)\n" \
"v_mfma_f32_16x16x32_bf16 a[132:135], v[156:159], v[52:55], a[132:135]\n" \
"v_mfma_f32_16x16x32_bf16 a[132:135], v[160:163], v[56:59], a[132:135]\n" \
"v_mfma_f32_16x16x32_bf16 a[136:139], v[168:171], v[56:59], a[136:139]\n" \
"v_mfma_f32_16x16x32_bf16 a[136:139], v[164:167], v[52:55], a[136:139]\n" \
"s_waitcnt lgkmcnt(7)\n" \
"v_mfma_f32_16x16x32_bf16 a[140:143], v[172:175], v[52:55], a[140:143]\n" \
"v_mfma_f32_16x16x32_bf16 a[140:143], v[176:179], v[56:59], a[140:143]\n" \
"v_mfma_f32_16x16x32_bf16 a[160:163], v[152:155], v[64:67], a[160:163]\n" \
"v_mfma_f32_16x16x32_bf16 a[160:163], v[148:151], v[60:63], a[160:163]\n" \
"v_mfma_f32_16x16x32_bf16 a[164:167], v[156:159], v[60:63], a[164:167]\n" \
"v_mfma_f32_16x16x32_bf16 a[164:167], v[160:163], v[64:67], a[164:167]\n" \
"v_mfma_f32_16x16x32_bf16 a[168:171], v[168:171], v[64:67], a[168:171]\n" \
"v_mfma_f32_16x16x32_bf16 a[168:171], v[164:167], v[60:63], a[168:171]\n" \
"v_mfma_f32_16x16x32_bf16 a[172:175], v[172:175], v[60:63], a[172:175]\n" \
"v_mfma_f32_16x16x32_bf16 a[172:175], v[176:179], v[64:67], a[172:175]\n" \
"v_mfma_f32_16x16x32_bf16 a[192:195], v[152:155], v[72:75], a[192:195]\n" \
"v_mfma_f32_16x16x32_bf16 a[192:195], v[148:151], v[68:71], a[192:195]\n" \
"s_waitcnt vmcnt(3) lgkmcnt(0)\n" \
"s_barrier\n" \
"v_mfma_f32_16x16x32_bf16 a[196:199], v[156:159], v[68:71], a[196:199]\n" \
"s_add_u32 m0, s40, 0x8000\n" \
"s_add_u32 s85, s72, 0x0\n" \
"s_nop 0\n" \
"v_mfma_f32_16x16x32_bf16 a[196:199], v[160:163], v[72:75], a[196:199]\n" \
"buffer_load_dwordx4 v20, s[80:83], s85 offen sc1 lds\n" \
"v_mfma_f32_16x16x32_bf16 a[200:203], v[168:171], v[72:75], a[200:203]\n" \
"s_add_u32 m0, s40, 0x9000\n" \
"s_add_u32 s84, s72, 0x1000\n" \
"v_mfma_f32_16x16x32_bf16 a[200:203], v[164:167], v[68:71], a[200:203]\n" \
"ds_read_b64_tr_b16 v[140:141], v39 offset:0\n" \
"ds_read_b64_tr_b16 v[142:143], v39 offset:960\n" \
"v_mfma_f32_16x16x32_bf16 a[204:207], v[172:175], v[68:71], a[204:207]\n" \
"buffer_load_dwordx4 v20, s[80:83], s84 offen sc1 lds\n" \
"v_mfma_f32_16x16x32_bf16 a[204:207], v[176:179], v[72:75], a[204:207]\n" \
"ds_read_b64_tr_b16 v[128:129], v37 offset:4096\n" \
"ds_read_b64_tr_b16 v[130:131], v37 offset:4608\n" \
"v_mfma_f32_16x16x32_bf16 a[224:227], v[152:155], v[80:83], a[224:227]\n" \
"s_add_u32 m0, s40, 0xb000\n" \
"s_add_u32 s85, s62, 0x0\n" \
"v_mfma_f32_16x16x32_bf16 a[224:227], v[148:151], v[76:79], a[224:227]\n" \
"buffer_load_dwordx4 v21, s[76:79], s85 offen lds\n" \
"v_mfma_f32_16x16x32_bf16 a[228:231], v[156:159], v[76:79], a[228:231]\n" \
"ds_read_b64_tr_b16 v[124:125], v37 offset:0\n" \
"ds_read_b64_tr_b16 v[126:127], v37 offset:512\n" \
"v_mfma_f32_16x16x32_bf16 a[228:231], v[160:163], v[80:83], a[228:231]\n" \
"s_add_u32 m0, %[mA], 0x8000\n" \
"s_add_u32 s84, s62, 0x0\n" \
"v_mfma_f32_16x16x32_bf16 a[232:235], v[168:171], v[80:83], a[232:235]\n" \
"buffer_load_dwordx4 v24, s[76:79], s84 offen lds\n" \
"v_mfma_f32_16x16x32_bf16 a[232:235], v[164:167], v[76:79], a[232:235]\n" \
"ds_read_b64_tr_b16 v[132:133], v38 offset:0\n" \
"ds_read_b64_tr_b16 v[134:135], v38 offset:512\n" \
"v_mfma_f32_16x16x32_bf16 a[236:239], v[172:175], v[76:79], a[236:239]\n" \
"ds_read_b64_tr_b16 v[136:137], v38 offset:4096\n" \
"ds_read_b64_tr_b16 v[138:139], v38 offset:4608\n" \
"v_mfma_f32_16x16x32_bf16 a[236:239], v[176:179], v[80:83], a[236:239]\n" \
"s_add_u32 m0, %[mA], 0x8400\n" \
"s_add_u32 s85, s62, 0x2400\n" \
"v_mfma_f32_16x16x32_bf16 a[144:147], v[152:155], v[88:91], a[144:147]\n" \
"buffer_load_dwordx4 v25, s[76:79], s85 offen lds\n" \
"v_mfma_f32_16x16x32_bf16 a[144:147], v[148:151], v[84:87], a[144:147]\n" \
"ds_read_b128 v[52:55], v28 offset:0\n" \
"ds_read_b128 v[56:59], v29 offset:0\n" \
"v_mfma_f32_16x16x32_bf16 a[148:151], v[156:159], v[84:87], a[148:151]\n" \
"s_add_u32 m0, %[mB], 0x8000\n" \
"s_add_u32 s84, s63, 0x0\n" \
"s_nop 0\n" \
"v_mfma_f32_16x16x32_bf16 a[148:151], v[160:163], v[88:91], a[148:151]\n" \
"buffer_load_dwordx4 v26, s[68:71], s84 offen lds\n" \
"v_mfma_f32_16x16x32_bf16 a[152:155], v[168:171], v[88:91], a[152:155]\n" \
"ds_read_b64_tr_b16 v[116:117], v36 offset:0\n" \
"ds_read_b64_tr_b16 v[118:119], v36 offset:512\n" \
"v_mfma_f32_16x16x32_bf16 a[152:155], v[164:167], v[84:87], a[152:155]\n" \
"ds_read_b64_tr_b16 v[120:121], v36 offset:4096\n" \
"ds_read_b64_tr_b16 v[122:123], v36 offset:4608\n" \
"v_mfma_f32_16x16x32_bf16 a[156:159], v[172:175], v[84:87], a[156:159]\n" \
"s_add_u32 m0, %[mA], 0x8800\n" \
"s_add_u32 s85, s62, 0x4800\n" \
"v_mfma_f32_16x16x32_bf16 a[156:159], v[176:179], v[88:91], a[156:159]\n" \
"buffer_load_dwordx4 v24, s[76:79], s85 offen lds\n" \
"v_mfma_f32_16x16x32_bf16 a[176:179], v[152:155], v[96:99], a[176:179]\n" \
"ds_read_b128 v[84:87], v22 offset:0\n" \
"ds_read_b128 v[88:91], v23 offset:0\n" \
"v_mfma_f32_16x16x32_bf16 a[176:179], v[148:151], v[92:95], a[176:179]\n" \
"s_add_u32 m0, %[mA], 0x8c00\n" \
"s_add_u32 s84, s62, 0x6c00\n" \
"v_mfma_f32_16x16x32_bf16 a[180:183], v[156:159], v[92:95], a[180:183]\n" \
"buffer_load_dwordx4 v25, s[76:79], s84 offen lds\n" \
"v_mfma_f32_16x16x32_bf16 a[180:183], v[160:163], v[96:99], a[180:183]\n" \
"ds_read_b128 v[60:63], v28 offset:2048\n" \
"ds_read_b128 v[64:67], v29 offset:2048\n" \
"v_mfma_f32_16x16x32_bf16 a[184:187], v[168:171], v[96:99], a[184:187]\n" \
"ds_read_b128 v[68:71], v28 offset:4096\n" \
"ds_read_b128 v[72:75], v29 offset:4096\n" \
"v_mfma_f32_16x16x32_bf16 a[184:187], v[164:167], v[92:95], a[184:187]\n" \
"s_add_u32 m0, %[mB], 0x8400\n" \
"s_add_u32 s85, s63, 0x8000\n" \
"v_mfma_f32_16x16x32_bf16 a[188:191], v[172:175], v[92:95], a[188:191]\n" \
"buffer_load_dwordx4 v26, s[68:71], s85 offen lds\n" \
"v_mfma_f32_16x16x32_bf16 a[188:191], v[176:179], v[96:99], a[188:191]\n" \
"ds_read_b128 v[76:79], v28 offset:6144\n" \
"ds_read_b128 v[80:83], v29 offset:6144\n" \
"v_mfma_f32_16x16x32_bf16 a[208:211], v[152:155], v[104:107], a[208:211]\n" \
"s_add_u32 m0, %[mA], 0x9000\n" \
"s_add_u32 s84, s62, 0x9000\n" \
"v_mfma_f32_16x16x32_bf16 a[208:211], v[148:151], v[100:103], a[208:211]\n" \
"buffer_load_dwordx4 v24, s[76:79], s84 offen lds\n" \
"v_mfma_f32_16x16x32_bf16 a[212:215], v[156:159], v[100:103], a[212:215]\n" \
"ds_read_b128 v[92:95], v48 offset:2048\n" \
"ds_read_b128 v[96:99], v49 offset:2048\n" \
"v_mfma_f32_16x16x32_bf16 a[212:215], v[160:163], v[104:107], a[212:215]\n" \
"v_mfma_f32_16x16x32_bf16 a[216:219], v[168:171], v[104:107], a[216:219]\n" \
"s_add_u32 m0, %[mA], 0x9400\n" \
"s_add_u32 s85, s62, 0xb400\n" \
"v_mfma_f32_16x16x32_bf16 a[216:219], v[164:167], v[100:103], a[216:219]\n" \
"buffer_load_dwordx4 v25, s[76:79], s85 offen lds\n" \
"v_mfma_f32_16x16x32_bf16 a[220:223], v[172:175], v[100:103], a[220:223]\n" \
"ds_read_b128 v[100:103], v48 offset:4096\n" \
"v_mfma_f32_16x16x32_bf16 a[220:223], v[176:179], v[104:107], a[220:223]\n" \
"s_add_u32 m0, %[mB], 0x8800\n" \
"s_add_u32 s84, s63, 0x10000\n" \
"v_mfma_f32_16x16x32_bf16 a[240:243], v[152:155], v[112:115], a[240:243]\n" \
"buffer_load_dwordx4 v27, s[68:71], s84 offen lds\n" \
"v_mfma_f32_16x16x32_bf16 a[240:243], v[148:151], v[108:111], a[240:243]\n" \
"ds_read_b64_tr_b16 v[152:153], v32 offset:8192\n" \
"ds_read_b64_tr_b16 v[154:155], v32 offset:9216\n" \
"v_mfma_f32_16x16x32_bf16 a[244:247], v[156:159], v[108:111], a[244:247]\n" \
"ds_read_b64_tr_b16 v[148:149], v32 offset:0\n" \
"ds_read_b64_tr_b16 v[150:151], v32 offset:1024\n" \
"v_mfma_f32_16x16x32_bf16 a[244:247], v[160:163], v[112:115], a[244:247]\n" \
"s_add_u32 m0, %[mA], 0x9800\n" \
"s_add_u32 s85, s62, 0xd800\n" \
"v_mfma_f32_16x16x32_bf16 a[248:251], v[168:171], v[112:115], a[248:251]\n" \
"buffer_load_dwordx4 v24, s[76:79], s85 offen lds\n" \
"v_mfma_f32_16x16x32_bf16 a[248:251], v[164:167], v[108:111], a[248:251]\n" \
"ds_read_b64_tr_b16 v[156:157], v33 offset:0\n" \
"ds_read_b64_tr_b16 v[158:159], v33 offset:1024\n" \
"v_mfma_f32_16x16x32_bf16 a[252:255], v[172:175], v[108:111], a[252:255]\n" \
"ds_read_b64_tr_b16 v[160:161], v33 offset:8192\n" \
"ds_read_b64_tr_b16 v[162:163], v33 offset:9216\n" \
"v_mfma_f32_16x16x32_bf16 a[252:255], v[176:179], v[112:115], a[252:255]\n" \
"s_add_u32 m0, %[mA], 0x9c00\n" \
"s_waitcnt lgkmcnt(11)\n" \
"s_nop 0\n" \
"v_smfmac_f32_16x16x64_bf16 a[16:19], v[140:143], v[84:91], v50\n" \
"v_mfma_f32_16x16x32_bf16 a[48:51], v[128:131], v[88:91], a[48:51]\n" \
"s_add_u32 s85, s62, 0xfc00\n" \
"ds_read_b64_tr_b16 v[168:169], v34 offset:8192\n" \
"v_mfma_f32_16x16x32_bf16 a[48:51], v[124:127], v[84:87], a[48:51]\n" \
"buffer_load_dwordx4 v25, s[76:79], s85 offen lds\n" \
"v_mfma_f32_16x16x32_bf16 a[80:83], v[84:87], v[132:135], a[80:83]\n" \
"s_add_u32 m0, %[mB], 0x8c00\n" \
"s_add_u32 s84, s63, 0x18000\n" \
"v_mfma_f32_16x16x32_bf16 a[80:83], v[88:91], v[136:139], a[80:83]\n" \
"ds_read_b64_tr_b16 v[170:171], v34 offset:9216\n" \
"ds_read_b64_tr_b16 v[164:165], v34 offset:0\n" \
"v_smfmac_f32_16x16x64_bf16 a[112:115], v[140:143], v[52:59], v50\n" \
"v_mfma_f32_16x16x32_bf16 a[20:23], v[128:131], v[56:59], a[20:23]\n" \
"buffer_load_dwordx4 v27, s[68:71], s84 offen lds\n" \
"v_mfma_f32_16x16x32_bf16 a[20:23], v[124:127], v[52:55], a[20:23]\n" \
"s_add_u32 s89, s44, 0x1000\n" \
"ds_read_b64_tr_b16 v[166:167], v34 offset:1024\n" \
"v_mfma_f32_16x16x32_bf16 a[52:55], v[116:119], v[52:55], a[52:55]\n" \
"ds_read_b64_tr_b16 v[172:173], v35 offset:0\n" \
"ds_read_b64_tr_b16 v[174:175], v35 offset:1024\n" \
"v_mfma_f32_16x16x32_bf16 a[52:55], v[120:123], v[56:59], a[52:55]\n" \
"buffer_store_dwordx4 v[212:215], v20, s[80:83], s89 offen offset:3072 sc1\n" \
"v_mfma_f32_16x16x32_bf16 a[84:87], v[56:59], v[136:139], a[84:87]\n" \
"ds_read_b64_tr_b16 v[176:177], v35 offset:8192\n" \
"ds_read_b64_tr_b16 v[178:179], v35 offset:9216\n" \
"v_mfma_f32_16x16x32_bf16 a[84:87], v[52:55], v[132:135], a[84:87]\n" \
"ds_read_b128 v[84:87], v48 offset:0\n" \
"ds_read_b128 v[88:91], v49 offset:0\n" \
"v_smfmac_f32_16x16x64_bf16 a[116:119], v[140:143], v[60:67], v50\n" \
"v_mfma_f32_16x16x32_bf16 a[24:27], v[124:127], v[60:63], a[24:27]\n" \
"ds_read_b128 v[104:107], v49 offset:4096\n" \
"ds_read_b128 v[108:111], v48 offset:6144\n" \
"v_mfma_f32_16x16x32_bf16 a[24:27], v[128:131], v[64:67], a[24:27]\n" \
"s_add_u32 s88, s42, 0x0\n" \
"ds_read_b128 v[112:115], v49 offset:6144\n" \
"v_mfma_f32_16x16x32_bf16 a[56:59], v[120:123], v[64:67], a[56:59]\n" \
"buffer_store_dwordx4 v[216:219], v20, s[80:83], s88 offen offset:0 sc1\n" \
"v_mfma_f32_16x16x32_bf16 a[56:59], v[116:119], v[60:63], a[56:59]\n" \
"s_add_u32 s62, s62, 0x80\n" \
"s_add_u32 s94, %[b], %[w]\n" \
"v_mfma_f32_16x16x32_bf16 a[88:91], v[60:63], v[132:135], a[88:91]\n" \
"s_cmp_lt_u32 s62, 0x480\n" \
"s_cselect_b32 s62, s62, 0\n" \
"v_mfma_f32_16x16x32_bf16 a[88:91], v[64:67], v[136:139], a[88:91]\n" \
"s_lshl_b32 s72, s62, 6\n" \
"s_add_u32 s72, s72, s37\n" \
"v_smfmac_f32_16x16x64_bf16 a[120:123], v[140:143], v[68:75], v50\n" \
"v_mfma_f32_16x16x32_bf16 a[28:31], v[128:131], v[72:75], a[28:31]\n" \
"s_lshl_b32 s63, s62, 12\n" \
"s_and_b32 s96, 7, %[b]\n" \
"v_mfma_f32_16x16x32_bf16 a[28:31], v[124:127], v[68:71], a[28:31]\n" \
"s_cmp_lg_u32 s94, 0\n" \
"s_cselect_b32 s95, 0x40000000, 0x5bc0000\n" \
"v_mfma_f32_16x16x32_bf16 a[60:63], v[116:119], v[68:71], a[60:63]\n" \
"s_add_u32 s89, s42, 0x1000\n" \
"s_mul_i32 s97, 0x48, s96\n" \
"v_mfma_f32_16x16x32_bf16 a[60:63], v[120:123], v[72:75], a[60:63]\n" \
"buffer_store_dwordx4 v[220:223], v20, s[80:83], s89 offen offset:0 sc1\n" \
"v_mfma_f32_16x16x32_bf16 a[92:95], v[72:75], v[136:139], a[92:95]\n" \
"s_add_u32 s98, 0x47, s97\n" \
"v_writelane_b32 v11, s95, 50\n" \
"v_mfma_f32_16x16x32_bf16 a[92:95], v[68:71], v[132:135], a[92:95]\n" \
"s_cmp_lt_u32 %[w], 3\n" \
"s_cselect_b32 s99, s98, 0xfff\n" \
"v_smfmac_f32_16x16x64_bf16 a[124:127], v[140:143], v[76:83], v50\n" \
"v_mfma_f32_16x16x32_bf16 a[0:3], v[124:127], v[76:79], a[0:3]\n" \
"s_lshr_b32 s94, %[b], 3\n" \
"s_lshl_b32 s67, s96, 2\n" \
"v_mfma_f32_16x16x32_bf16 a[0:3], v[128:131], v[80:83], a[0:3]\n" \
"s_and_b32 s95, 3, s94\n" \
"s_mul_i32 s100, 0x480, s99\n" \
"v_mfma_f32_16x16x32_bf16 a[4:7], v[120:123], v[80:83], a[4:7]\n" \
"s_lshl_b32 s64, %[w], 5\n" \
"s_add_u32 s98, s67, s95\n" \
"v_mfma_f32_16x16x32_bf16 a[4:7], v[116:119], v[76:79], a[4:7]\n" \
"s_add_u32 s101, 0x120, s100\n" \
"s_add_u32 s65, s97, s64\n" \
"v_mfma_f32_16x16x32_bf16 a[32:35], v[76:79], v[132:135], a[32:35]\n" \
"s_mul_i32 s99, 0x24000, s98\n" \
"v_writelane_b32 v11, s101, 15\n" \
"v_mfma_f32_16x16x32_bf16 a[32:35], v[80:83], v[136:139], a[32:35]\n" \
"s_mul_i32 s66, 0x480, s65\n" \
"s_waitcnt lgkmcnt(0)\n" \
"v_mfma_f32_16x16x32_bf16 a[128:131], v[152:155], v[56:59], a[128:131]\n" \
"s_add_u32 s100, 0x1b000, s99\n" \
"s_min_u32 s97, 2, %[w]\n" \
"v_mfma_f32_16x16x32_bf16 a[128:131], v[148:151], v[52:55], a[128:131]\n" \
"s_cmp_lt_u32 %[w], 3\n" \
"s_cselect_b32 s101, s66, s100\n" \
"v_mfma_f32_16x16x32_bf16 a[132:135], v[156:159], v[52:55], a[132:135]\n" \
"s_mul_i32 s64, 0x2400, s97\n" \
"v_writelane_b32 v11, s101, 16\n" \
"v_mfma_f32_16x16x32_bf16 a[132:135], v[160:163], v[56:59], a[132:135]\n" \
"s_add_u32 s65, 0x20000, s64\n" \
"s_mul_i32 s94, 0x9000, s97\n" \
"v_mfma_f32_16x16x32_bf16 a[136:139], v[168:171], v[56:59], a[136:139]\n" \
"s_mul_i32 s95, 0x2400, %[w]\n" \
"v_writelane_b32 v11, s65, 17\n" \
"v_mfma_f32_16x16x32_bf16 a[136:139], v[164:167], v[52:55], a[136:139]\n" \
"s_add_u32 s67, s99, s94\n" \
"v_writelane_b32 v11, s67, 22\n" \
"v_mfma_f32_16x16x32_bf16 a[140:143], v[172:175], v[52:55], a[140:143]\n" \
"s_cmp_lt_u32 %[w], 3\n" \
"s_cselect_b32 s98, s95, 0x12000\n" \
"v_mfma_f32_16x16x32_bf16 a[140:143], v[176:179], v[56:59], a[140:143]\n" \
"s_add_u32 s66, 0x20000, s95\n" \
"v_writelane_b32 v11, s98, 18\n" \
"v_mfma_f32_16x16x32_bf16 a[160:163], v[152:155], v[64:67], a[160:163]\n" \
"s_cmp_lt_u32 %[w], 3\n" \
"s_cselect_b32 s100, s66, 0xfc00\n" \
"v_mfma_f32_16x16x32_bf16 a[160:163], v[148:151], v[60:63], a[160:163]\n" \
"v_writelane_b32 v11, s95, 19\n" \
"s_lshl_b32 s101, %[w], 10\n" \
"v_mfma_f32_16x16x32_bf16 a[164:167], v[156:159], v[60:63], a[164:167]\n" \
"s_lshr_b32 s65, s96, 1\n" \
"v_writelane_b32 v11, s100, 20\n" \
"v_mfma_f32_16x16x32_bf16 a[164:167], v[160:163], v[64:67], a[164:167]\n" \
"s_cmp_eq_u32 %[w], s65\n" \
"s_cselect_b32 s97, 0xff0000, 0\n" \
"v_mfma_f32_16x16x32_bf16 a[168:171], v[168:171], v[64:67], a[168:171]\n" \
"s_add_u32 s64, 0x1d400, s101\n" \
"v_writelane_b32 v11, s64, 21\n" \
"v_mfma_f32_16x16x32_bf16 a[168:171], v[164:167], v[60:63], a[168:171]\n" \
"v_writelane_b32 v11, s97, 6\n" \
"v_mfma_f32_16x16x32_bf16 a[172:175], v[172:175], v[60:63], a[172:175]\n" \
"v_mfma_f32_16x16x32_bf16 a[172:175], v[176:179], v[64:67], a[172:175]\n" \
"v_mfma_f32_16x16x32_bf16 a[192:195], v[152:155], v[72:75], a[192:195]\n" \
"v_mfma_f32_16x16x32_bf16 a[192:195], v[148:151], v[68:71], a[192:195]\n" \
"s_waitcnt vmcnt(3)\n" \
"s_barrier\n" \
"v_mfma_f32_16x16x32_bf16 a[196:199], v[156:159], v[68:71], a[196:199]\n" \
"s_add_u32 m0, s40, 0x0\n" \
"s_add_u32 s85, s72, 0x0\n" \
"s_nop 0\n" \
"v_mfma_f32_16x16x32_bf16 a[196:199], v[160:163], v[72:75], a[196:199]\n" \
"buffer_load_dwordx4 v20, s[80:83], s85 offen sc1 lds\n" \
"v_mfma_f32_16x16x32_bf16 a[200:203], v[168:171], v[72:75], a[200:203]\n" \
"s_add_u32 m0, s40, 0x1000\n" \
"s_add_u32 s84, s72, 0x1000\n" \
"v_mfma_f32_16x16x32_bf16 a[200:203], v[164:167], v[68:71], a[200:203]\n" \
"ds_read_b64_tr_b16 v[140:141], v39 offset:32768\n" \
"ds_read_b64_tr_b16 v[142:143], v39 offset:33728\n" \
"v_mfma_f32_16x16x32_bf16 a[204:207], v[172:175], v[68:71], a[204:207]\n" \
"buffer_load_dwordx4 v20, s[80:83], s84 offen sc1 lds\n" \
"v_mfma_f32_16x16x32_bf16 a[204:207], v[176:179], v[72:75], a[204:207]\n" \
"ds_read_b64_tr_b16 v[128:129], v37 offset:36864\n" \
"ds_read_b64_tr_b16 v[130:131], v37 offset:37376\n" \
"v_mfma_f32_16x16x32_bf16 a[224:227], v[152:155], v[80:83], a[224:227]\n" \
"L_pf6:\n" \
"s_cbranch_execz L_pf10\n" \
"s_nop 0\n" \
"s_add_u32 m0, s40, 0x3000\n" \
"s_add_u32 s85, s62, 0x0\n" \
"v_mfma_f32_16x16x32_bf16 a[224:227], v[148:151], v[76:79], a[224:227]\n" \
"buffer_load_dwordx4 v21, s[76:79], s85 offen lds\n" \
"v_mfma_f32_16x16x32_bf16 a[228:231], v[156:159], v[76:79], a[228:231]\n" \
"ds_read_b64_tr_b16 v[124:125], v37 offset:32768\n" \
"ds_read_b64_tr_b16 v[126:127], v37 offset:33280\n" \
"v_mfma_f32_16x16x32_bf16 a[228:231], v[160:163], v[80:83], a[228:231]\n" \
"s_add_u32 m0, %[mA], 0x0\n" \
"s_add_u32 s84, s62, 0x0\n" \
"s_nop 0\n" \
"v_mfma_f32_16x16x32_bf16 a[232:235], v[168:171], v[80:83], a[232:235]\n" \
"buffer_load_dwordx4 v24, s[76:79], s84 offen lds\n" \
"v_mfma_f32_16x16x32_bf16 a[232:235], v[164:167], v[76:79], a[232:235]\n" \
"ds_read_b64_tr_b16 v[132:133], v38 offset:32768\n" \
"ds_read_b64_tr_b16 v[134:135], v38 offset:33280\n" \
"v_mfma_f32_16x16x32_bf16 a[236:239], v[172:175], v[76:79], a[236:239]\n" \
"ds_read_b64_tr_b16 v[136:137], v38 offset:36864\n" \
"ds_read_b64_tr_b16 v[138:139], v38 offset:37376\n" \
"v_mfma_f32_16x16x32_bf16 a[236:239], v[176:179], v[80:83], a[236:239]\n" \
"s_add_u32 m0, %[mA], 0x400\n" \
"s_add_u32 s85, s62, 0x2400\n" \
"v_mfma_f32_16x16x32_bf16 a[144:147], v[152:155], v[88:91], a[144:147]\n" \
"buffer_load_dwordx4 v25, s[76:79], s85 offen lds\n" \
"v_mfma_f32_16x16x32_bf16 a[144:147], v[148:151], v[84:87], a[144:147]\n" \
"ds_read_b128 v[52:55], v28 offset:32768\n" \
"ds_read_b128 v[56:59], v29 offset:32768\n" \
"v_mfma_f32_16x16x32_bf16 a[148:151], v[156:159], v[84:87], a[148:151]\n" \
"s_add_u32 m0, %[mB], 0x0\n" \
"s_add_u32 s84, s63, 0x0\n" \
"v_mfma_f32_16x16x32_bf16 a[148:151], v[160:163], v[88:91], a[148:151]\n" \
"buffer_load_dwordx4 v26, s[68:71], s84 offen lds\n" \
"v_mfma_f32_16x16x32_bf16 a[152:155], v[168:171], v[88:91], a[152:155]\n" \
"ds_read_b64_tr_b16 v[116:117], v36 offset:32768\n" \
"ds_read_b64_tr_b16 v[118:119], v36 offset:33280\n" \
"v_mfma_f32_16x16x32_bf16 a[152:155], v[164:167], v[84:87], a[152:155]\n" \
"ds_read_b64_tr_b16 v[120:121], v36 offset:36864\n" \
"ds_read_b64_tr_b16 v[122:123], v36 offset:37376\n" \
"v_mfma_f32_16x16x32_bf16 a[156:159], v[172:175], v[84:87], a[156:159]\n" \
"s_add_u32 m0, %[mA], 0x800\n" \
"s_add_u32 s85, s62, 0x4800\n" \
"v_mfma_f32_16x16x32_bf16 a[156:159], v[176:179], v[88:91], a[156:159]\n" \
"buffer_load_dwordx4 v24, s[76:79], s85 offen lds\n" \
"v_mfma_f32_16x16x32_bf16 a[176:179], v[152:155], v[96:99], a[176:179]\n" \
"ds_read_b128 v[84:87], v22 offset:32768\n" \
"ds_read_b128 v[88:91], v23 offset:32768\n" \
"v_mfma_f32_16x16x32_bf16 a[176:179], v[148:151], v[92:95], a[176:179]\n" \
"s_add_u32 m0, %[mA], 0xc00\n" \
"s_add_u32 s84, s62, 0x6c00\n" \
"v_mfma_f32_16x16x32_bf16 a[180:183], v[156:159], v[92:95], a[180:183]\n" \
"buffer_load_dwordx4 v25, s[76:79], s84 offen lds\n" \
"v_mfma_f32_16x16x32_bf16 a[180:183], v[160:163], v[96:99], a[180:183]\n" \
"ds_read_b128 v[60:63], v28 offset:34816\n" \
"ds_read_b128 v[64:67], v29 offset:34816\n" \
"v_mfma_f32_16x16x32_bf16 a[184:187], v[168:171], v[96:99], a[184:187]\n" \
"ds_read_b128 v[68:71], v28 offset:36864\n" \
"ds_read_b128 v[72:75], v29 offset:36864\n" \
"v_mfma_f32_16x16x32_bf16 a[184:187], v[164:167], v[92:95], a[184:187]\n" \
"s_add_u32 m0, %[mB], 0x400\n" \
"s_add_u32 s85, s63, 0x8000\n" \
"v_mfma_f32_16x16x32_bf16 a[188:191], v[172:175], v[92:95], a[188:191]\n" \
"buffer_load_dwordx4 v26, s[68:71], s85 offen lds\n" \
"v_mfma_f32_16x16x32_bf16 a[188:191], v[176:179], v[96:99], a[188:191]\n" \
"ds_read_b128 v[76:79], v28 offset:38912\n" \
"ds_read_b128 v[80:83], v29 offset:38912\n" \
"v_mfma_f32_16x16x32_bf16 a[208:211], v[152:155], v[104:107], a[208:211]\n" \
"s_add_u32 m0, %[mA], 0x1000\n" \
"s_add_u32 s84, s62, 0x9000\n" \
"v_mfma_f32_16x16x32_bf16 a[208:211], v[148:151], v[100:103], a[208:211]\n" \
"buffer_load_dwordx4 v24, s[76:79], s84 offen lds\n" \
"v_mfma_f32_16x16x32_bf16 a[212:215], v[156:159], v[100:103], a[212:215]\n" \
"ds_read_b128 v[92:95], v48 offset:34816\n" \
"ds_read_b128 v[96:99], v49 offset:34816\n" \
"v_mfma_f32_16x16x32_bf16 a[212:215], v[160:163], v[104:107], a[212:215]\n" \
"v_mfma_f32_16x16x32_bf16 a[216:219], v[168:171], v[104:107], a[216:219]\n" \
"s_add_u32 m0, %[mA], 0x1400\n" \
"s_add_u32 s85, s62, 0xb400\n" \
"v_mfma_f32_16x16x32_bf16 a[216:219], v[164:167], v[100:103], a[216:219]\n" \
"buffer_load_dwordx4 v25, s[76:79], s85 offen lds\n" \
"v_mfma_f32_16x16x32_bf16 a[220:223], v[172:175], v[100:103], a[220:223]\n" \
"ds_read_b128 v[100:103], v48 offset:36864\n" \
"v_mfma_f32_16x16x32_bf16 a[220:223], v[176:179], v[104:107], a[220:223]\n" \
"s_add_u32 m0, %[mB], 0x800\n" \
"s_add_u32 s84, s63, 0x10000\n" \
"v_mfma_f32_16x16x32_bf16 a[240:243], v[152:155], v[112:115], a[240:243]\n" \
"buffer_load_dwordx4 v27, s[68:71], s84 offen lds\n" \
"v_mfma_f32_16x16x32_bf16 a[240:243], v[148:151], v[108:111], a[240:243]\n" \
"ds_read_b64_tr_b16 v[152:153], v32 offset:40960\n" \
"ds_read_b64_tr_b16 v[154:155], v32 offset:41984\n" \
"v_mfma_f32_16x16x32_bf16 a[244:247], v[156:159], v[108:111], a[244:247]\n" \
"ds_read_b64_tr_b16 v[148:149], v32 offset:32768\n" \
"ds_read_b64_tr_b16 v[150:151], v32 offset:33792\n" \
"v_mfma_f32_16x16x32_bf16 a[244:247], v[160:163], v[112:115], a[244:247]\n" \
"s_add_u32 m0, %[mA], 0x1800\n" \
"s_add_u32 s85, s62, 0xd800\n" \
"v_mfma_f32_16x16x32_bf16 a[248:251], v[168:171], v[112:115], a[248:251]\n" \
"buffer_load_dwordx4 v24, s[76:79], s85 offen lds\n" \
"v_mfma_f32_16x16x32_bf16 a[248:251], v[164:167], v[108:111], a[248:251]\n" \
"ds_read_b64_tr_b16 v[156:157], v33 offset:32768\n" \
"ds_read_b64_tr_b16 v[158:159], v33 offset:33792\n" \
"v_mfma_f32_16x16x32_bf16 a[252:255], v[172:175], v[108:111], a[252:255]\n" \
"ds_read_b64_tr_b16 v[160:161], v33 offset:40960\n" \
"ds_read_b64_tr_b16 v[162:163], v33 offset:41984\n" \
"v_mfma_f32_16x16x32_bf16 a[252:255], v[176:179], v[112:115], a[252:255]\n" \
"s_add_u32 m0, %[mA], 0x1c00\n" \
"s_waitcnt lgkmcnt(11)\n" \
"s_nop 0\n" \
"v_smfmac_f32_16x16x64_bf16 a[16:19], v[140:143], v[84:91], v50\n" \
"v_mfma_f32_16x16x32_bf16 a[48:51], v[128:131], v[88:91], a[48:51]\n" \
"s_add_u32 s85, s62, 0xfc00\n" \
"ds_read_b64_tr_b16 v[168:169], v34 offset:40960\n" \
"v_mfma_f32_16x16x32_bf16 a[48:51], v[124:127], v[84:87], a[48:51]\n" \
"buffer_load_dwordx4 v25, s[76:79], s85 offen lds\n" \
"v_mfma_f32_16x16x32_bf16 a[80:83], v[84:87], v[132:135], a[80:83]\n" \
"s_add_u32 m0, %[mB], 0xc00\n" \
"s_add_u32 s84, s63, 0x18000\n" \
"v_mfma_f32_16x16x32_bf16 a[80:83], v[88:91], v[136:139], a[80:83]\n" \
"ds_read_b64_tr_b16 v[170:171], v34 offset:41984\n" \
"ds_read_b64_tr_b16 v[164:165], v34 offset:32768\n" \
"v_smfmac_f32_16x16x64_bf16 a[112:115], v[140:143], v[52:59], v50\n" \
"v_mfma_f32_16x16x32_bf16 a[20:23], v[128:131], v[56:59], a[20:23]\n" \
"buffer_load_dwordx4 v27, s[68:71], s84 offen lds\n" \
"v_mfma_f32_16x16x32_bf16 a[20:23], v[124:127], v[52:55], a[20:23]\n" \
"s_add_u32 s88, s42, 0x2000\n" \
"ds_read_b64_tr_b16 v[166:167], v34 offset:33792\n" \
"v_mfma_f32_16x16x32_bf16 a[52:55], v[116:119], v[52:55], a[52:55]\n" \
"ds_read_b64_tr_b16 v[172:173], v35 offset:32768\n" \
"ds_read_b64_tr_b16 v[174:175], v35 offset:33792\n" \
"v_mfma_f32_16x16x32_bf16 a[52:55], v[120:123], v[56:59], a[52:55]\n" \
"buffer_store_dwordx4 v[224:227], v20, s[80:83], s88 offen offset:0 sc1\n" \
"v_mfma_f32_16x16x32_bf16 a[84:87], v[56:59], v[136:139], a[84:87]\n" \
"ds_read_b64_tr_b16 v[176:177], v35 offset:40960\n" \
"ds_read_b64_tr_b16 v[178:179], v35 offset:41984\n" \
"v_mfma_f32_16x16x32_bf16 a[84:87], v[52:55], v[132:135], a[84:87]\n" \
"ds_read_b128 v[84:87], v48 offset:32768\n" \
"ds_read_b128 v[88:91], v49 offset:32768\n" \
"v_smfmac_f32_16x16x64_bf16 a[116:119], v[140:143], v[60:67], v50\n" \
"v_mfma_f32_16x16x32_bf16 a[24:27], v[124:127], v[60:63], a[24:27]\n" \
"ds_read_b128 v[104:107], v49 offset:36864\n" \
"ds_read_b128 v[108:111], v48 offset:38912\n" \
"v_mfma_f32_16x16x32_bf16 a[24:27], v[128:131], v[64:67], a[24:27]\n" \
"s_add_u32 s89, s42, 0x3000\n" \
"ds_read_b128 v[112:115], v49 offset:38912\n" \
"v_mfma_f32_16x16x32_bf16 a[56:59], v[120:123], v[64:67], a[56:59]\n" \
"buffer_store_dwordx4 v[228:231], v20, s[80:83], s89 offen offset:0 sc1\n" \
"v_mfma_f32_16x16x32_bf16 a[56:59], v[116:119], v[60:63], a[56:59]\n" \
"s_add_u32 s62, s62, 0x80\n" \
"s_lshr_b32 s96, %[b], 3\n" \
"v_mfma_f32_16x16x32_bf16 a[88:91], v[60:63], v[132:135], a[88:91]\n" \
"s_cmp_lt_u32 s62, 0x480\n" \
"s_cselect_b32 s62, s62, 0\n" \
"v_mfma_f32_16x16x32_bf16 a[88:91], v[64:67], v[136:139], a[88:91]\n" \
"s_lshl_b32 s72, s62, 6\n" \
"s_add_u32 s72, s72, s37\n" \
"v_smfmac_f32_16x16x64_bf16 a[120:123], v[140:143], v[68:75], v50\n" \
"v_mfma_f32_16x16x32_bf16 a[28:31], v[128:131], v[72:75], a[28:31]\n" \
"s_lshl_b32 s63, s62, 12\n" \
"s_and_b32 s97, 3, s96\n" \
"v_mfma_f32_16x16x32_bf16 a[28:31], v[124:127], v[68:71], a[28:31]\n" \
"s_and_b32 s94, 7, %[b]\n" \
"s_add_u32 s98, 1, s97\n" \
"v_mfma_f32_16x16x32_bf16 a[60:63], v[116:119], v[68:71], a[60:63]\n" \
"s_add_u32 s88, s42, 0x0\n" \
"s_lshl_b32 s95, s94, 2\n" \
"v_mfma_f32_16x16x32_bf16 a[60:63], v[120:123], v[72:75], a[60:63]\n" \
"buffer_store_dwordx4 v[232:235], v20, s[80:83], s88 offen offset:1024 sc1\n" \
"v_mfma_f32_16x16x32_bf16 a[92:95], v[72:75], v[136:139], a[92:95]\n" \
"s_and_b32 s99, 3, s98\n" \
"s_add_u32 s100, s95, s99\n" \
"v_mfma_f32_16x16x32_bf16 a[92:95], v[68:71], v[132:135], a[92:95]\n" \
"s_mul_i32 s101, 0x24000, s100\n" \
"s_mul_i32 s64, 0x9000, %[w]\n" \
"v_smfmac_f32_16x16x64_bf16 a[124:127], v[140:143], v[76:83], v50\n" \
"v_mfma_f32_16x16x32_bf16 a[0:3], v[124:127], v[76:79], a[0:3]\n" \
"s_and_b32 s96, 2, %[w]\n" \
"s_add_u32 s65, s101, s64\n" \
"v_mfma_f32_16x16x32_bf16 a[0:3], v[128:131], v[80:83], a[0:3]\n" \
"s_lshr_b32 s66, %[b], 5\n" \
"s_lshl_b32 s98, s96, 1\n" \
"v_mfma_f32_16x16x32_bf16 a[4:7], v[120:123], v[80:83], a[4:7]\n" \
"s_and_b32 s99, 1, %[w]\n" \
"v_writelane_b32 v11, s65, 23\n" \
"v_mfma_f32_16x16x32_bf16 a[4:7], v[116:119], v[76:79], a[4:7]\n" \
"s_lshl_b32 s67, s66, 3\n" \
"s_add_u32 s101, s98, s99\n" \
"v_mfma_f32_16x16x32_bf16 a[32:35], v[76:79], v[132:135], a[32:35]\n" \
"s_add_u32 s65, s67, s101\n" \
"s_mul_i32 s66, 0x24800, s65\n" \
"v_mfma_f32_16x16x32_bf16 a[32:35], v[80:83], v[136:139], a[32:35]\n" \
"s_lshl_b32 s98, s100, 12\n" \
"s_waitcnt lgkmcnt(0)\n" \
"v_mfma_f32_16x16x32_bf16 a[128:131], v[152:155], v[56:59], a[128:131]\n" \
"s_add_u32 s96, 0x3000000, s66\n" \
"s_add_u32 s99, 0x4800, s98\n" \
"v_mfma_f32_16x16x32_bf16 a[128:131], v[148:151], v[52:55], a[128:131]\n" \
"s_add_u32 s65, s96, s99\n" \
"s_sub_u32 s100, 0x78, s67\n" \
"v_mfma_f32_16x16x32_bf16 a[132:135], v[156:159], v[52:55], a[132:135]\n" \
"v_writelane_b32 v11, s65, 26\n" \
"s_add_u32 s98, s100, s101\n" \
"v_mfma_f32_16x16x32_bf16 a[132:135], v[160:163], v[56:59], a[132:135]\n" \
"s_mul_i32 s65, 0x24800, s98\n" \
"s_add_u32 s67, 0x3000000, s65\n" \
"v_mfma_f32_16x16x32_bf16 a[136:139], v[168:171], v[56:59], a[136:139]\n" \
"s_add_u32 s101, 2, s97\n" \
"s_add_u32 s100, s67, s99\n" \
"v_mfma_f32_16x16x32_bf16 a[136:139], v[164:167], v[52:55], a[136:139]\n" \
"s_and_b32 s98, 3, s101\n" \
"v_writelane_b32 v11, s100, 29\n" \
"v_mfma_f32_16x16x32_bf16 a[140:143], v[172:175], v[52:55], a[140:143]\n" \
"s_add_u32 s99, s95, s98\n" \
"s_mul_i32 s100, 0x24000, s99\n" \
"v_mfma_f32_16x16x32_bf16 a[140:143], v[176:179], v[56:59], a[140:143]\n" \
"s_add_u32 s101, s100, s64\n" \
"s_lshl_b32 s98, s99, 12\n" \
"v_mfma_f32_16x16x32_bf16 a[160:163], v[152:155], v[64:67], a[160:163]\n" \
"v_writelane_b32 v11, s101, 24\n" \
"s_add_u32 s100, 0x4800, s98\n" \
"v_mfma_f32_16x16x32_bf16 a[160:163], v[148:151], v[60:63], a[160:163]\n" \
"s_add_u32 s101, s96, s100\n" \
"v_writelane_b32 v11, s101, 27\n" \
"v_mfma_f32_16x16x32_bf16 a[164:167], v[156:159], v[60:63], a[164:167]\n" \
"s_add_u32 s98, 3, s97\n" \
"s_add_u32 s99, s67, s100\n" \
"v_mfma_f32_16x16x32_bf16 a[164:167], v[160:163], v[64:67], a[164:167]\n" \
"s_and_b32 s101, 3, s98\n" \
"v_writelane_b32 v11, s99, 30\n" \
"v_mfma_f32_16x16x32_bf16 a[168:171], v[168:171], v[64:67], a[168:171]\n" \
"s_add_u32 s100, s95, s101\n" \
"s_mul_i32 s99, 0x24000, s100\n" \
"v_mfma_f32_16x16x32_bf16 a[168:171], v[164:167], v[60:63], a[168:171]\n" \
"s_lshl_b32 s98, s100, 12\n" \
"s_add_u32 s97, s99, s64\n" \
"v_mfma_f32_16x16x32_bf16 a[172:175], v[172:175], v[60:63], a[172:175]\n" \
"s_add_u32 s95, 0x4800, s98\n" \
"v_writelane_b32 v11, s97, 25\n" \
"v_mfma_f32_16x16x32_bf16 a[172:175], v[176:179], v[64:67], a[172:175]\n" \
"s_add_u32 s101, s96, s95\n" \
"v_writelane_b32 v11, s101, 28\n" \
"v_mfma_f32_16x16x32_bf16 a[192:195], v[152:155], v[72:75], a[192:195]\n" \
"s_add_u32 s99, s67, s95\n" \
"s_lshl_b32 s64, s94, 11\n" \
"v_mfma_f32_16x16x32_bf16 a[192:195], v[148:151], v[68:71], a[192:195]\n" \
"v_writelane_b32 v11, s99, 31\n" \
"s_add_u32 s97, s96, s64\n" \
"s_waitcnt vmcnt(3)\n" \
"s_barrier\n" \
"v_mfma_f32_16x16x32_bf16 a[196:199], v[156:159], v[68:71], a[196:199]\n" \
"s_add_u32 m0, s40, 0x8000\n" \
"s_add_u32 s85, s72, 0x0\n" \
"v_mfma_f32_16x16x32_bf16 a[196:199], v[160:163], v[72:75], a[196:199]\n" \
"buffer_load_dwordx4 v20, s[80:83], s85 offen sc1 lds\n" \
"v_mfma_f32_16x16x32_bf16 a[200:203], v[168:171], v[72:75], a[200:203]\n" \
"s_add_u32 m0, s40, 0x9000\n" \
"s_add_u32 s84, s72, 0x1000\n" \
"v_mfma_f32_16x16x32_bf16 a[200:203], v[164:167], v[68:71], a[200:203]\n" \
"ds_read_b64_tr_b16 v[140:141], v39 offset:0\n" \
"ds_read_b64_tr_b16 v[142:143], v39 offset:960\n" \
"v_mfma_f32_16x16x32_bf16 a[204:207], v[172:175], v[68:71], a[204:207]\n" \
"buffer_load_dwordx4 v20, s[80:83], s84 offen sc1 lds\n" \
"v_mfma_f32_16x16x32_bf16 a[204:207], v[176:179], v[72:75], a[204:207]\n" \
"ds_read_b64_tr_b16 v[128:129], v37 offset:4096\n" \
"ds_read_b64_tr_b16 v[130:131], v37 offset:4608\n" \
"v_mfma_f32_16x16x32_bf16 a[224:227], v[152:155], v[80:83], a[224:227]\n" \
"s_add_u32 m0, s40, 0xb000\n" \
"s_add_u32 s85, s62, 0x0\n" \
"v_mfma_f32_16x16x32_bf16 a[224:227], v[148:151], v[76:79], a[224:227]\n" \
"buffer_load_dwordx4 v21, s[76:79], s85 offen lds\n" \
"v_mfma_f32_16x16x32_bf16 a[228:231], v[156:159], v[76:79], a[228:231]\n" \
"ds_read_b64_tr_b16 v[124:125], v37 offset:0\n" \
"ds_read_b64_tr_b16 v[126:127], v37 offset:512\n" \
"v_mfma_f32_16x16x32_bf16 a[228:231], v[160:163], v[80:83], a[228:231]\n" \
"s_add_u32 m0, %[mA], 0x8000\n" \
"s_add_u32 s84, s62, 0x0\n" \
"v_mfma_f32_16x16x32_bf16 a[232:235], v[168:171], v[80:83], a[232:235]\n" \
"buffer_load_dwordx4 v24, s[76:79], s84 offen lds\n" \
"v_mfma_f32_16x16x32_bf16 a[232:235], v[164:167], v[76:79], a[232:235]\n" \
"ds_read_b64_tr_b16 v[132:133], v38 offset:0\n" \
"ds_read_b64_tr_b16 v[134:135], v38 offset:512\n" \
"v_mfma_f32_16x16x32_bf16 a[236:239], v[172:175], v[76:79], a[236:239]\n" \
"ds_read_b64_tr_b16 v[136:137], v38 offset:4096\n" \
"ds_read_b64_tr_b16 v[138:139], v38 offset:4608\n" \
"v_mfma_f32_16x16x32_bf16 a[236:239], v[176:179], v[80:83], a[236:239]\n" \
"s_add_u32 m0, %[mA], 0x8400\n" \
"s_add_u32 s85, s62, 0x2400\n" \
"v_mfma_f32_16x16x32_bf16 a[144:147], v[152:155], v[88:91], a[144:147]\n" \
"buffer_load_dwordx4 v25, s[76:79], s85 offen lds\n" \
"v_mfma_f32_16x16x32_bf16 a[144:147], v[148:151], v[84:87], a[144:147]\n" \
"ds_read_b128 v[52:55], v28 offset:0\n" \
"ds_read_b128 v[56:59], v29 offset:0\n" \
"v_mfma_f32_16x16x32_bf16 a[148:151], v[156:159], v[84:87], a[148:151]\n" \
"s_add_u32 m0, %[mB], 0x8000\n" \
"s_add_u32 s84, s63, 0x0\n" \
"s_nop 0\n" \
"v_mfma_f32_16x16x32_bf16 a[148:151], v[160:163], v[88:91], a[148:151]\n" \
"buffer_load_dwordx4 v26, s[68:71], s84 offen lds\n" \
"v_mfma_f32_16x16x32_bf16 a[152:155], v[168:171], v[88:91], a[152:155]\n" \
"ds_read_b64_tr_b16 v[116:117], v36 offset:0\n" \
"ds_read_b64_tr_b16 v[118:119], v36 offset:512\n" \
"v_mfma_f32_16x16x32_bf16 a[152:155], v[164:167], v[84:87], a[152:155]\n" \
"ds_read_b64_tr_b16 v[120:121], v36 offset:4096\n" \
"ds_read_b64_tr_b16 v[122:123], v36 offset:4608\n" \
"v_mfma_f32_16x16x32_bf16 a[156:159], v[172:175], v[84:87], a[156:159]\n" \
"s_add_u32 m0, %[mA], 0x8800\n" \
"s_add_u32 s85, s62, 0x4800\n" \
"v_mfma_f32_16x16x32_bf16 a[156:159], v[176:179], v[88:91], a[156:159]\n" \
"buffer_load_dwordx4 v24, s[76:79], s85 offen lds\n" \
"v_mfma_f32_16x16x32_bf16 a[176:179], v[152:155], v[96:99], a[176:179]\n" \
"ds_read_b128 v[84:87], v22 offset:0\n" \
"ds_read_b128 v[88:91], v23 offset:0\n" \
"v_mfma_f32_16x16x32_bf16 a[176:179], v[148:151], v[92:95], a[176:179]\n" \
"s_add_u32 m0, %[mA], 0x8c00\n" \
"s_add_u32 s84, s62, 0x6c00\n" \
"v_mfma_f32_16x16x32_bf16 a[180:183], v[156:159], v[92:95], a[180:183]\n" \
"buffer_load_dwordx4 v25, s[76:79], s84 offen lds\n" \
"v_mfma_f32_16x16x32_bf16 a[180:183], v[160:163], v[96:99], a[180:183]\n" \
"ds_read_b128 v[60:63], v28 offset:2048\n" \
"ds_read_b128 v[64:67], v29 offset:2048\n" \
"v_mfma_f32_16x16x32_bf16 a[184:187], v[168:171], v[96:99], a[184:187]\n" \
"ds_read_b128 v[68:71], v28 offset:4096\n" \
"ds_read_b128 v[72:75], v29 offset:4096\n" \
"v_mfma_f32_16x16x32_bf16 a[184:187], v[164:167], v[92:95], a[184:187]\n" \
"s_add_u32 m0, %[mB], 0x8400\n" \
"s_add_u32 s85, s63, 0x8000\n" \
"v_mfma_f32_16x16x32_bf16 a[188:191], v[172:175], v[92:95], a[188:191]\n" \
"buffer_load_dwordx4 v26, s[68:71], s85 offen lds\n" \
"v_mfma_f32_16x16x32_bf16 a[188:191], v[176:179], v[96:99], a[188:191]\n" \
"ds_read_b128 v[76:79], v28 offset:6144\n" \
"ds_read_b128 v[80:83], v29 offset:6144\n" \
"v_mfma_f32_16x16x32_bf16 a[208:211], v[152:155], v[104:107], a[208:211]\n" \
"s_add_u32 m0, %[mA], 0x9000\n" \
"s_add_u32 s84, s62, 0x9000\n" \
"v_mfma_f32_16x16x32_bf16 a[208:211], v[148:151], v[100:103], a[208:211]\n" \
"buffer_load_dwordx4 v24, s[76:79], s84 offen lds\n" \
"v_mfma_f32_16x16x32_bf16 a[212:215], v[156:159], v[100:103], a[212:215]\n" \
"ds_read_b128 v[92:95], v48 offset:2048\n" \
"ds_read_b128 v[96:99], v49 offset:2048\n" \
"v_mfma_f32_16x16x32_bf16 a[212:215], v[160:163], v[104:107], a[212:215]\n" \
"v_writelane_b32 v11, s97, 32\n" \
"s_and_b32 s99, 1, s94\n" \
"v_mfma_f32_16x16x32_bf16 a[216:219], v[168:171], v[104:107], a[216:219]\n" \
"s_add_u32 m0, %[mA], 0x9400\n" \
"s_add_u32 s85, s62, 0xb400\n" \
"v_mfma_f32_16x16x32_bf16 a[216:219], v[164:167], v[100:103], a[216:219]\n" \
"buffer_load_dwordx4 v25, s[76:79], s85 offen lds\n" \
"v_mfma_f32_16x16x32_bf16 a[220:223], v[172:175], v[100:103], a[220:223]\n" \
"ds_read_b128 v[100:103], v48 offset:4096\n" \
"s_add_u32 s100, s67, s64\n" \
"v_mfma_f32_16x16x32_bf16 a[220:223], v[176:179], v[104:107], a[220:223]\n" \
"s_add_u32 m0, %[mB], 0x8800\n" \
"s_add_u32 s84, s63, 0x10000\n" \
"v_mfma_f32_16x16x32_bf16 a[240:243], v[152:155], v[112:115], a[240:243]\n" \
"buffer_load_dwordx4 v27, s[68:71], s84 offen lds\n" \
"v_mfma_f32_16x16x32_bf16 a[240:243], v[148:151], v[108:111], a[240:243]\n" \
"ds_read_b64_tr_b16 v[152:153], v32 offset:8192\n" \
"ds_read_b64_tr_b16 v[154:155], v32 offset:9216\n" \
"v_mfma_f32_16x16x32_bf16 a[244:247], v[156:159], v[108:111], a[244:247]\n" \
"ds_read_b64_tr_b16 v[148:149], v32 offset:0\n" \
"ds_read_b64_tr_b16 v[150:151], v32 offset:1024\n" \
"v_mfma_f32_16x16x32_bf16 a[244:247], v[160:163], v[112:115], a[244:247]\n" \
"s_add_u32 m0, %[mA], 0x9800\n" \
"s_add_u32 s85, s62, 0xd800\n" \
"v_mfma_f32_16x16x32_bf16 a[248:251], v[168:171], v[112:115], a[248:251]\n" \
"buffer_load_dwordx4 v24, s[76:79], s85 offen lds\n" \
"v_mfma_f32_16x16x32_bf16 a[248:251], v[164:167], v[108:111], a[248:251]\n" \
"ds_read_b64_tr_b16 v[156:157], v33 offset:0\n" \
"ds_read_b64_tr_b16 v[158:159], v33 offset:1024\n" \
"v_mfma_f32_16x16x32_bf16 a[252:255], v[172:175], v[108:111], a[252:255]\n" \
"ds_read_b64_tr_b16 v[160:161], v33 offset:8192\n" \
"ds_read_b64_tr_b16 v[162:163], v33 offset:9216\n" \
"v_mfma_f32_16x16x32_bf16 a[252:255], v[176:179], v[112:115], a[252:255]\n" \
"s_add_u32 m0, %[mA], 0x9c00\n" \
"s_waitcnt lgkmcnt(11)\n" \
"s_nop 0\n" \
"v_smfmac_f32_16x16x64_bf16 a[16:19], v[140:143], v[84:91], v50\n" \
"v_mfma_f32_16x16x32_bf16 a[48:51], v[128:131], v[88:91], a[48:51]\n" \
"s_add_u32 s85, s62, 0xfc00\n" \
"ds_read_b64_tr_b16 v[168:169], v34 offset:8192\n" \
"v_mfma_f32_16x16x32_bf16 a[48:51], v[124:127], v[84:87], a[48:51]\n" \
"buffer_load_dwordx4 v25, s[76:79], s85 offen lds\n" \
"v_mfma_f32_16x16x32_bf16 a[80:83], v[84:87], v[132:135], a[80:83]\n" \
"s_add_u32 m0, %[mB], 0x8c00\n" \
"s_add_u32 s84, s63, 0x18000\n" \
"v_mfma_f32_16x16x32_bf16 a[80:83], v[88:91], v[136:139], a[80:83]\n" \
"ds_read_b64_tr_b16 v[170:171], v34 offset:9216\n" \
"ds_read_b64_tr_b16 v[164:165], v34 offset:0\n" \
"v_smfmac_f32_16x16x64_bf16 a[112:115], v[140:143], v[52:59], v50\n" \
"v_mfma_f32_16x16x32_bf16 a[20:23], v[128:131], v[56:59], a[20:23]\n" \
"buffer_load_dwordx4 v27, s[68:71], s84 offen lds\n" \
"v_mfma_f32_16x16x32_bf16 a[20:23], v[124:127], v[52:55], a[20:23]\n" \
"s_add_u32 s89, s42, 0x1000\n" \
"ds_read_b64_tr_b16 v[166:167], v34 offset:1024\n" \
"v_mfma_f32_16x16x32_bf16 a[52:55], v[116:119], v[52:55], a[52:55]\n" \
"ds_read_b64_tr_b16 v[172:173], v35 offset:0\n" \
"ds_read_b64_tr_b16 v[174:175], v35 offset:1024\n" \
"v_mfma_f32_16x16x32_bf16 a[52:55], v[120:123], v[56:59], a[52:55]\n" \
"buffer_store_dwordx4 v[236:239], v20, s[80:83], s89 offen offset:1024 sc1\n" \
"v_mfma_f32_16x16x32_bf16 a[84:87], v[56:59], v[136:139], a[84:87]\n" \
"ds_read_b64_tr_b16 v[176:177], v35 offset:8192\n" \
"ds_read_b64_tr_b16 v[178:179], v35 offset:9216\n" \
"v_mfma_f32_16x16x32_bf16 a[84:87], v[52:55], v[132:135], a[84:87]\n" \
"ds_read_b128 v[84:87], v48 offset:0\n" \
"ds_read_b128 v[88:91], v49 offset:0\n" \
"v_smfmac_f32_16x16x64_bf16 a[116:119], v[140:143], v[60:67], v50\n" \
"v_mfma_f32_16x16x32_bf16 a[24:27], v[124:127], v[60:63], a[24:27]\n" \
"ds_read_b128 v[104:107], v49 offset:4096\n" \
"ds_read_b128 v[108:111], v48 offset:6144\n" \
"v_mfma_f32_16x16x32_bf16 a[24:27], v[128:131], v[64:67], a[24:27]\n" \
"s_add_u32 s88, s42, 0x2000\n" \
"ds_read_b128 v[112:115], v49 offset:6144\n" \
"v_mfma_f32_16x16x32_bf16 a[56:59], v[120:123], v[64:67], a[56:59]\n" \
"buffer_store_dwordx4 v[240:243], v20, s[80:83], s88 offen offset:1024 sc1\n" \
"v_mfma_f32_16x16x32_bf16 a[56:59], v[116:119], v[60:63], a[56:59]\n" \
"s_add_u32 s62, s62, 0x80\n" \
"v_writelane_b32 v11, s100, 33\n" \
"v_mfma_f32_16x16x32_bf16 a[88:91], v[60:63], v[132:135], a[88:91]\n" \
"s_cmp_lt_u32 s62, 0x480\n" \
"s_cselect_b32 s62, s62, 0\n" \
"v_mfma_f32_16x16x32_bf16 a[88:91], v[64:67], v[136:139], a[88:91]\n" \
"s_lshl_b32 s72, s62, 6\n" \
"s_add_u32 s72, s72, s37\n" \
"v_smfmac_f32_16x16x64_bf16 a[120:123], v[140:143], v[68:75], v50\n" \
"v_mfma_f32_16x16x32_bf16 a[28:31], v[128:131], v[72:75], a[28:31]\n" \
"s_lshl_b32 s63, s62, 12\n" \
"s_add_u32 s98, 0x3004000, s66\n" \
"v_mfma_f32_16x16x32_bf16 a[28:31], v[124:127], v[68:71], a[28:31]\n" \
"s_cmp_lg_u32 s99, 0\n" \
"s_cselect_b32 s97, s67, s96\n" \
"v_mfma_f32_16x16x32_bf16 a[60:63], v[116:119], v[68:71], a[60:63]\n" \
"s_add_u32 s89, s42, 0x3000\n" \
"s_lshr_b32 s95, s94, 1\n" \
"v_mfma_f32_16x16x32_bf16 a[60:63], v[120:123], v[72:75], a[60:63]\n" \
"buffer_store_dwordx4 v[244:247], v20, s[80:83], s89 offen offset:1024 sc1\n" \
"v_mfma_f32_16x16x32_bf16 a[92:95], v[72:75], v[136:139], a[92:95]\n" \
"s_add_u32 s64, 0x4000, s97\n" \
"v_writelane_b32 v11, s98, 34\n" \
"v_mfma_f32_16x16x32_bf16 a[92:95], v[68:71], v[132:135], a[92:95]\n" \
"s_add_u32 s101, 0x3004000, s65\n" \
"s_and_b32 s96, 2, %[w]\n" \
"v_smfmac_f32_16x16x64_bf16 a[124:127], v[140:143], v[76:83], v50\n" \
"v_mfma_f32_16x16x32_bf16 a[0:3], v[124:127], v[76:79], a[0:3]\n" \
"s_cmp_eq_u32 %[w], s95\n" \
"s_cselect_b32 s100, s64, 0x40000000\n" \
"v_mfma_f32_16x16x32_bf16 a[0:3], v[128:131], v[80:83], a[0:3]\n" \
"v_writelane_b32 v11, s101, 35\n" \
"s_lshl_b32 s97, s96, 1\n" \
"v_mfma_f32_16x16x32_bf16 a[4:7], v[120:123], v[80:83], a[4:7]\n" \
"s_and_b32 s98, 1, %[w]\n" \
"v_writelane_b32 v11, s100, 36\n" \
"v_mfma_f32_16x16x32_bf16 a[4:7], v[116:119], v[76:79], a[4:7]\n" \
"s_lshr_b32 s94, %[b], 5\n" \
"s_add_u32 s99, s97, s98\n" \
"v_mfma_f32_16x16x32_bf16 a[32:35], v[76:79], v[132:135], a[32:35]\n" \
"s_lshl_b32 s95, s94, 7\n" \
"s_lshl_b32 s100, s99, 4\n" \
"v_mfma_f32_16x16x32_bf16 a[32:35], v[80:83], v[136:139], a[32:35]\n" \
"s_add_u32 s101, s95, s100\n" \
"s_waitcnt lgkmcnt(0)\n" \
"v_mfma_f32_16x16x32_bf16 a[128:131], v[152:155], v[56:59], a[128:131]\n" \
"s_mul_i32 s64, 0x480, s101\n" \
"s_lshr_b32 s65, %[b], 3\n" \
"v_mfma_f32_16x16x32_bf16 a[128:131], v[148:151], v[52:55], a[128:131]\n" \
"s_sub_u32 s99, 0x780, s95\n" \
"s_and_b32 s66, 3, s65\n" \
"v_mfma_f32_16x16x32_bf16 a[132:135], v[156:159], v[52:55], a[132:135]\n" \
"s_add_u32 s101, s99, s100\n" \
"s_mul_i32 s67, 0x120, s66\n" \
"v_mfma_f32_16x16x32_bf16 a[132:135], v[160:163], v[56:59], a[132:135]\n" \
"s_and_b32 s96, 7, %[b]\n" \
"s_mul_i32 s65, 0x480, s101\n" \
"v_mfma_f32_16x16x32_bf16 a[136:139], v[168:171], v[56:59], a[136:139]\n" \
"s_add_u32 s94, s64, s67\n" \
"s_lshl_b32 s97, s96, 5\n" \
"v_mfma_f32_16x16x32_bf16 a[136:139], v[164:167], v[52:55], a[136:139]\n" \
"s_add_u32 s66, s65, s67\n" \
"s_and_b32 s95, 1, s96\n" \
"v_mfma_f32_16x16x32_bf16 a[140:143], v[172:175], v[52:55], a[140:143]\n" \
"s_add_u32 s98, s94, s97\n" \
"v_writelane_b32 v11, s98, 39\n" \
"v_mfma_f32_16x16x32_bf16 a[140:143], v[176:179], v[56:59], a[140:143]\n" \
"s_cmp_lg_u32 s95, 0\n" \
"s_cselect_b32 s99, s66, s94\n" \
"v_mfma_f32_16x16x32_bf16 a[160:163], v[152:155], v[64:67], a[160:163]\n" \
"s_lshr_b32 s98, s96, 1\n" \
"s_add_u32 s100, 0x100, s99\n" \
"v_mfma_f32_16x16x32_bf16 a[160:163], v[148:151], v[60:63], a[160:163]\n" \
"s_add_u32 s64, s66, s97\n" \
"s_mov_b32 s94, s67\n" \
"v_mfma_f32_16x16x32_bf16 a[164:167], v[156:159], v[60:63], a[164:167]\n" \
"s_cmp_eq_u32 %[w], s98\n" \
"s_cselect_b32 s101, s100, 0x40000000\n" \
"v_mfma_f32_16x16x32_bf16 a[164:167], v[160:163], v[64:67], a[164:167]\n" \
"v_writelane_b32 v11, s64, 40\n" \
"v_writelane_b32 v11, s101, 41\n" \
"v_mfma_f32_16x16x32_bf16 a[168:171], v[168:171], v[64:67], a[168:171]\n" \
"s_add_u32 s96, s16, s94\n" \
"s_addc_u32 s97, s17, 0\n" \
"v_mfma_f32_16x16x32_bf16 a[168:171], v[164:167], v[60:63], a[168:171]\n" \
"s_add_u32 s98, s18, s94\n" \
"s_addc_u32 s99, s19, 0\n" \
"v_mfma_f32_16x16x32_bf16 a[172:175], v[172:175], v[60:63], a[172:175]\n" \
"s_and_b32 s97, s97, 0xffff\n" \
"v_writelane_b32 v11, s96, 7\n" \
"v_mfma_f32_16x16x32_bf16 a[172:175], v[176:179], v[64:67], a[172:175]\n" \
"v_writelane_b32 v11, s97, 8\n" \
"s_and_b32 s99, s99, 0xffff\n" \
"v_mfma_f32_16x16x32_bf16 a[192:195], v[152:155], v[72:75], a[192:195]\n" \
"v_writelane_b32 v11, s98, 9\n" \
"v_writelane_b32 v11, s99, 10\n" \
"v_mfma_f32_16x16x32_bf16 a[192:195], v[148:151], v[68:71], a[192:195]\n" \
"s_cmp_lt_u32 %[w], 3\n" \
"s_cselect_b64 s[96:97], s[96:97], s[98:99]\n" \
"s_waitcnt vmcnt(3)\n" \
"s_barrier\n" \
"v_mfma_f32_16x16x32_bf16 a[196:199], v[156:159], v[68:71], a[196:199]\n" \
"s_add_u32 m0, s40, 0x0\n" \
"s_add_u32 s85, s72, 0x0\n" \
"v_mfma_f32_16x16x32_bf16 a[196:199], v[160:163], v[72:75], a[196:199]\n" \
"buffer_load_dwordx4 v20, s[80:83], s85 offen sc1 lds\n" \
"v_mfma_f32_16x16x32_bf16 a[200:203], v[168:171], v[72:75], a[200:203]\n" \
"s_add_u32 m0, s40, 0x1000\n" \
"s_add_u32 s84, s72, 0x1000\n" \
"v_mfma_f32_16x16x32_bf16 a[200:203], v[164:167], v[68:71], a[200:203]\n" \
"ds_read_b64_tr_b16 v[140:141], v39 offset:32768\n" \
"ds_read_b64_tr_b16 v[142:143], v39 offset:33728\n" \
"v_mfma_f32_16x16x32_bf16 a[204:207], v[172:175], v[68:71], a[204:207]\n" \
"buffer_load_dwordx4 v20, s[80:83], s84 offen sc1 lds\n" \
"v_mfma_f32_16x16x32_bf16 a[204:207], v[176:179], v[72:75], a[204:207]\n" \
"ds_read_b64_tr_b16 v[128:129], v37 offset:36864\n" \
"ds_read_b64_tr_b16 v[130:131], v37 offset:37376\n" \
"v_mfma_f32_16x16x32_bf16 a[224:227], v[152:155], v[80:83], a[224:227]\n" \
"s_add_u32 m0, s40, 0x3000\n" \
"s_add_u32 s85, s62, 0x0\n" \
"v_mfma_f32_16x16x32_bf16 a[224:227], v[148:151], v[76:79], a[224:227]\n" \
"buffer_load_dwordx4 v21, s[76:79], s85 offen lds\n" \
"v_mfma_f32_16x16x32_bf16 a[228:231], v[156:159], v[76:79], a[228:231]\n" \
"ds_read_b64_tr_b16 v[124:125], v37 offset:32768\n" \
"ds_read_b64_tr_b16 v[126:127], v37 offset:33280\n" \
"v_mfma_f32_16x16x32_bf16 a[228:231], v[160:163], v[80:83], a[228:231]\n" \
"s_add_u32 m0, %[mA], 0x0\n" \
"s_add_u32 s84, s62, 0x0\n" \
"s_nop 0\n" \
"v_mfma_f32_16x16x32_bf16 a[232:235], v[168:171], v[80:83], a[232:235]\n" \
"buffer_load_dwordx4 v24, s[76:79], s84 offen lds\n" \
"v_mfma_f32_16x16x32_bf16 a[232:235], v[164:167], v[76:79], a[232:235]\n" \
"ds_read_b64_tr_b16 v[132:133], v38 offset:32768\n" \
"ds_read_b64_tr_b16 v[134:135], v38 offset:33280\n" \
"v_mfma_f32_16x16x32_bf16 a[236:239], v[172:175], v[76:79], a[236:239]\n" \
"ds_read_b64_tr_b16 v[136:137], v38 offset:36864\n" \
"ds_read_b64_tr_b16 v[138:139], v38 offset:37376\n" \
"v_mfma_f32_16x16x32_bf16 a[236:239], v[176:179], v[80:83], a[236:239]\n" \
"s_add_u32 m0, %[mA], 0x400\n" \
"s_add_u32 s85, s62, 0x2400\n" \
"v_mfma_f32_16x16x32_bf16 a[144:147], v[152:155], v[88:91], a[144:147]\n" \
"buffer_load_dwordx4 v25, s[76:79], s85 offen lds\n" \
"v_mfma_f32_16x16x32_bf16 a[144:147], v[148:151], v[84:87], a[144:147]\n" \
"ds_read_b128 v[52:55], v28 offset:32768\n" \
"ds_read_b128 v[56:59], v29 offset:32768\n" \
"v_mfma_f32_16x16x32_bf16 a[148:151], v[156:159], v[84:87], a[148:151]\n" \
"s_add_u32 m0, %[mB], 0x0\n" \
"s_add_u32 s84, s63, 0x0\n" \
"v_mfma_f32_16x16x32_bf16 a[148:151], v[160:163], v[88:91], a[148:151]\n" \
"buffer_load_dwordx4 v26, s[68:71], s84 offen lds\n" \
"v_mfma_f32_16x16x32_bf16 a[152:155], v[168:171], v[88:91], a[152:155]\n" \
"ds_read_b64_tr_b16 v[116:117], v36 offset:32768\n" \
"ds_read_b64_tr_b16 v[118:119], v36 offset:33280\n" \
"L_pf7:\n" \
"s_cbranch_execz L_pf11\n" \
"s_nop 0\n" \
"v_mfma_f32_16x16x32_bf16 a[152:155], v[164:167], v[84:87], a[152:155]\n" \
"ds_read_b64_tr_b16 v[120:121], v36 offset:36864\n" \
"ds_read_b64_tr_b16 v[122:123], v36 offset:37376\n" \
"v_mfma_f32_16x16x32_bf16 a[156:159], v[172:175], v[84:87], a[156:159]\n" \
"s_add_u32 m0, %[mA], 0x800\n" \
"s_add_u32 s85, s62, 0x4800\n" \
"v_mfma_f32_16x16x32_bf16 a[156:159], v[176:179], v[88:91], a[156:159]\n" \
"buffer_load_dwordx4 v24, s[76:79], s85 offen lds\n" \
"v_mfma_f32_16x16x32_bf16 a[176:179], v[152:155], v[96:99], a[176:179]\n" \
"ds_read_b128 v[84:87], v22 offset:32768\n" \
"ds_read_b128 v[88:91], v23 offset:32768\n" \
"v_mfma_f32_16x16x32_bf16 a[176:179], v[148:151], v[92:95], a[176:179]\n" \
"s_add_u32 m0, %[mA], 0xc00\n" \
"s_add_u32 s84, s62, 0x6c00\n" \
"v_mfma_f32_16x16x32_bf16 a[180:183], v[156:159], v[92:95], a[180:183]\n" \
"buffer_load_dwordx4 v25, s[76:79], s84 offen lds\n" \
"v_mfma_f32_16x16x32_bf16 a[180:183], v[160:163], v[96:99], a[180:183]\n" \
"ds_read_b128 v[60:63], v28 offset:34816\n" \
"ds_read_b128 v[64:67], v29 offset:34816\n" \
"v_mfma_f32_16x16x32_bf16 a[184:187], v[168:171], v[96:99], a[184:187]\n" \
"ds_read_b128 v[68:71], v28 offset:36864\n" \
"ds_read_b128 v[72:75], v29 offset:36864\n" \
"v_mfma_f32_16x16x32_bf16 a[184:187], v[164:167], v[92:95], a[184:187]\n" \
"s_add_u32 m0, %[mB], 0x400\n" \
"s_add_u32 s85, s63, 0x8000\n" \
"v_mfma_f32_16x16x32_bf16 a[188:191], v[172:175], v[92:95], a[188:191]\n" \
"buffer_load_dwordx4 v26, s[68:71], s85 offen lds\n" \
"v_mfma_f32_16x16x32_bf16 a[188:191], v[176:179], v[96:99], a[188:191]\n" \
"ds_read_b128 v[76:79], v28 offset:38912\n" \
"ds_read_b128 v[80:83], v29 offset:38912\n" \
"v_mfma_f32_16x16x32_bf16 a[208:211], v[152:155], v[104:107], a[208:211]\n" \
"s_add_u32 m0, %[mA], 0x1000\n" \
"s_add_u32 s84, s62, 0x9000\n" \
"v_mfma_f32_16x16x32_bf16 a[208:211], v[148:151], v[100:103], a[208:211]\n" \
"buffer_load_dwordx4 v24, s[76:79], s84 offen lds\n" \
"v_mfma_f32_16x16x32_bf16 a[212:215], v[156:159], v[100:103], a[212:215]\n" \
"ds_read_b128 v[92:95], v48 offset:34816\n" \
"ds_read_b128 v[96:99], v49 offset:34816\n" \
"v_mfma_f32_16x16x32_bf16 a[212:215], v[160:163], v[104:107], a[212:215]\n" \
"v_writelane_b32 v11, s96, 13\n" \
"v_writelane_b32 v11, s97, 14\n" \
"v_mfma_f32_16x16x32_bf16 a[216:219], v[168:171], v[104:107], a[216:219]\n" \
"s_add_u32 m0, %[mA], 0x1400\n" \
"s_add_u32 s85, s62, 0xb400\n" \
"v_mfma_f32_16x16x32_bf16 a[216:219], v[164:167], v[100:103], a[216:219]\n" \
"buffer_load_dwordx4 v25, s[76:79], s85 offen lds\n" \
"v_mfma_f32_16x16x32_bf16 a[220:223], v[172:175], v[100:103], a[220:223]\n" \
"ds_read_b128 v[100:103], v48 offset:36864\n" \
"v_writelane_b32 v11, s20, 11\n" \
"v_mfma_f32_16x16x32_bf16 a[220:223], v[176:179], v[104:107], a[220:223]\n" \
"s_add_u32 m0, %[mB], 0x800\n" \
"s_add_u32 s84, s63, 0x10000\n" \
"v_mfma_f32_16x16x32_bf16 a[240:243], v[152:155], v[112:115], a[240:243]\n" \
"buffer_load_dwordx4 v27, s[68:71], s84 offen lds\n" \
"v_mfma_f32_16x16x32_bf16 a[240:243], v[148:151], v[108:111], a[240:243]\n" \
"ds_read_b64_tr_b16 v[152:153], v32 offset:40960\n" \
"ds_read_b64_tr_b16 v[154:155], v32 offset:41984\n" \
"v_mfma_f32_16x16x32_bf16 a[244:247], v[156:159], v[108:111], a[244:247]\n" \
"ds_read_b64_tr_b16 v[148:149], v32 offset:32768\n" \
"ds_read_b64_tr_b16 v[150:151], v32 offset:33792\n" \
"v_mfma_f32_16x16x32_bf16 a[244:247], v[160:163], v[112:115], a[244:247]\n" \
"s_add_u32 m0, %[mA], 0x1800\n" \
"s_add_u32 s85, s62, 0xd800\n" \
"v_mfma_f32_16x16x32_bf16 a[248:251], v[168:171], v[112:115], a[248:251]\n" \
"buffer_load_dwordx4 v24, s[76:79], s85 offen lds\n" \
"v_mfma_f32_16x16x32_bf16 a[248:251], v[164:167], v[108:111], a[248:251]\n" \
"ds_read_b64_tr_b16 v[156:157], v33 offset:32768\n" \
"ds_read_b64_tr_b16 v[158:159], v33 offset:33792\n" \
"v_mfma_f32_16x16x32_bf16 a[252:255], v[172:175], v[108:111], a[252:255]\n" \
"ds_read_b64_tr_b16 v[160:161], v33 offset:40960\n" \
"ds_read_b64_tr_b16 v[162:163], v33 offset:41984\n" \
"v_mfma_f32_16x16x32_bf16 a[252:255], v[176:179], v[112:115], a[252:255]\n" \
"s_add_u32 m0, %[mA], 0x1c00\n" \
"s_waitcnt lgkmcnt(11)\n" \
"s_nop 0\n" \
"v_smfmac_f32_16x16x64_bf16 a[16:19], v[140:143], v[84:91], v50\n" \
"v_mfma_f32_16x16x32_bf16 a[48:51], v[128:131], v[88:91], a[48:51]\n" \
"s_add_u32 s85, s62, 0xfc00\n" \
"ds_read_b64_tr_b16 v[168:169], v34 offset:40960\n" \
"v_mfma_f32_16x16x32_bf16 a[48:51], v[124:127], v[84:87], a[48:51]\n" \
"buffer_load_dwordx4 v25, s[76:79], s85 offen lds\n" \
"v_mfma_f32_16x16x32_bf16 a[80:83], v[84:87], v[132:135], a[80:83]\n" \
"s_add_u32 m0, %[mB], 0xc00\n" \
"s_add_u32 s84, s63, 0x18000\n" \
"v_mfma_f32_16x16x32_bf16 a[80:83], v[88:91], v[136:139], a[80:83]\n" \
"ds_read_b64_tr_b16 v[170:171], v34 offset:41984\n" \
"ds_read_b64_tr_b16 v[164:165], v34 offset:32768\n" \
"v_smfmac_f32_16x16x64_bf16 a[112:115], v[140:143], v[52:59], v50\n" \
"v_mfma_f32_16x16x32_bf16 a[20:23], v[128:131], v[56:59], a[20:23]\n" \
"buffer_load_dwordx4 v27, s[68:71], s84 offen lds\n" \
"v_mfma_f32_16x16x32_bf16 a[20:23], v[124:127], v[52:55], a[20:23]\n" \
"s_add_u32 s88, s43, 0x0\n" \
"s_nop 0\n" \
"ds_read_b64_tr_b16 v[166:167], v34 offset:33792\n" \
"v_mfma_f32_16x16x32_bf16 a[52:55], v[116:119], v[52:55], a[52:55]\n" \
"ds_read_b64_tr_b16 v[172:173], v35 offset:32768\n" \
"ds_read_b64_tr_b16 v[174:175], v35 offset:33792\n" \
"v_mfma_f32_16x16x32_bf16 a[52:55], v[120:123], v[56:59], a[52:55]\n" \
"buffer_store_dwordx4 v[248:251], v20, s[80:83], s88 offen offset:0 sc1\n" \
"v_mfma_f32_16x16x32_bf16 a[84:87], v[56:59], v[136:139], a[84:87]\n" \
"ds_read_b64_tr_b16 v[176:177], v35 offset:40960\n" \
"ds_read_b64_tr_b16 v[178:179], v35 offset:41984\n" \
"v_mfma_f32_16x16x32_bf16 a[84:87], v[52:55], v[132:135], a[84:87]\n" \
"ds_read_b128 v[84:87], v48 offset:32768\n" \
"ds_read_b128 v[88:91], v49 offset:32768\n" \
"v_smfmac_f32_16x16x64_bf16 a[116:119], v[140:143], v[60:67], v50\n" \
"v_mfma_f32_16x16x32_bf16 a[24:27], v[124:127], v[60:63], a[24:27]\n" \
"ds_read_b128 v[104:107], v49 offset:36864\n" \
"ds_read_b128 v[108:111], v48 offset:38912\n" \
"v_mfma_f32_16x16x32_bf16 a[24:27], v[128:131], v[64:67], a[24:27]\n" \
"s_add_u32 s89, s45, 0x1000\n" \
"ds_read_b128 v[112:115], v49 offset:38912\n" \
"v_mfma_f32_16x16x32_bf16 a[56:59], v[120:123], v[64:67], a[56:59]\n" \
"buffer_store_dwordx4 v[184:187], v20, s[80:83], s89 offen offset:2048 sc1\n" \
"v_mfma_f32_16x16x32_bf16 a[56:59], v[116:119], v[60:63], a[56:59]\n" \
"s_add_u32 s62, s62, 0x80\n" \
"s_and_b32 s95, s21, 0xffff\n" \
"v_mfma_f32_16x16x32_bf16 a[88:91], v[60:63], v[132:135], a[88:91]\n" \
"s_cmp_lt_u32 s62, 0x480\n" \
"s_cselect_b32 s62, s62, 0\n" \
"v_mfma_f32_16x16x32_bf16 a[88:91], v[64:67], v[136:139], a[88:91]\n" \
"s_lshl_b32 s72, s62, 6\n" \
"s_add_u32 s72, s72, s37\n" \
"v_smfmac_f32_16x16x64_bf16 a[120:123], v[140:143], v[68:75], v50\n" \
"v_mfma_f32_16x16x32_bf16 a[28:31], v[128:131], v[72:75], a[28:31]\n" \
"s_lshl_b32 s63, s62, 12\n" \
"v_writelane_b32 v11, s95, 12\n" \
"v_mfma_f32_16x16x32_bf16 a[28:31], v[124:127], v[68:71], a[28:31]\n" \
"s_and_b32 s95, 1, %[w]\n" \
"s_lshr_b32 s96, %[w], 1\n" \
"v_mfma_f32_16x16x32_bf16 a[60:63], v[116:119], v[68:71], a[60:63]\n" \
"s_add_u32 s88, s45, 0x1000\n" \
"s_lshl_b32 s94, %[w], 11\n" \
"v_mfma_f32_16x16x32_bf16 a[60:63], v[120:123], v[72:75], a[60:63]\n" \
"buffer_store_dwordx4 v[252:255], v20, s[80:83], s88 offen offset:3072 sc1\n" \
"v_mfma_f32_16x16x32_bf16 a[92:95], v[72:75], v[136:139], a[92:95]\n" \
"s_lshl_b32 s15, s95, 12\n" \
"s_lshl_b32 s16, s96, 14\n" \
"v_mfma_f32_16x16x32_bf16 a[92:95], v[68:71], v[132:135], a[92:95]\n" \
"s_add_u32 s12, 0xc000, s94\n" \
"s_add_u32 s97, s16, s15\n" \
"v_smfmac_f32_16x16x64_bf16 a[124:127], v[140:143], v[76:83], v50\n" \
"v_mfma_f32_16x16x32_bf16 a[0:3], v[124:127], v[76:79], a[0:3]\n" \
"s_lshr_b32 s100, %[b], 5\n" \
"s_and_b32 s65, 7, %[b]\n" \
"v_mfma_f32_16x16x32_bf16 a[0:3], v[128:131], v[80:83], a[0:3]\n" \
"s_lshr_b32 s67, %[b], 3\n" \
"s_xor_b32 s13, 0x8000, s97\n" \
"v_mfma_f32_16x16x32_bf16 a[4:7], v[120:123], v[80:83], a[4:7]\n" \
"s_add_u32 s20, 0x14000, s97\n" \
"s_mul_i32 s98, 0xe000, s96\n" \
"v_mfma_f32_16x16x32_bf16 a[4:7], v[116:119], v[76:79], a[4:7]\n" \
"s_lshl_b32 s18, s100, 1\n" \
"s_lshl_b32 s66, s65, 2\n" \
"v_mfma_f32_16x16x32_bf16 a[32:35], v[76:79], v[132:135], a[32:35]\n" \
"s_and_b32 s94, 3, s67\n" \
"s_add_u32 s99, s98, s15\n" \
"v_mfma_f32_16x16x32_bf16 a[32:35], v[80:83], v[136:139], a[32:35]\n" \
"s_add_u32 s101, s18, s96\n" \
"s_waitcnt lgkmcnt(0)\n" \
"v_mfma_f32_16x16x32_bf16 a[128:131], v[152:155], v[56:59], a[128:131]\n" \
"s_mov_b32 s64, 0x4000\n" \
"s_add_u32 s97, s66, s94\n" \
"v_mfma_f32_16x16x32_bf16 a[128:131], v[148:151], v[52:55], a[128:131]\n" \
"s_add_u32 s21, 0x10000, s99\n" \
"s_lshl_b32 s14, s101, 15\n" \
"v_mfma_f32_16x16x32_bf16 a[132:135], v[156:159], v[52:55], a[132:135]\n" \
"s_cmp_lg_u32 s100, 0\n" \
"s_cselect_b32 s17, s64, 0x8000\n" \
"v_mfma_f32_16x16x32_bf16 a[132:135], v[160:163], v[56:59], a[132:135]\n" \
"s_lshl_b32 s98, s97, 8\n" \
"s_and_b32 s67, 2, %[w]\n" \
"v_mfma_f32_16x16x32_bf16 a[136:139], v[168:171], v[56:59], a[136:139]\n" \
"s_add_u32 s99, 0x5be8020, s98\n" \
"s_lshl_b32 s101, s100, 2\n" \
"v_mfma_f32_16x16x32_bf16 a[136:139], v[164:167], v[52:55], a[136:139]\n" \
"s_lshl_b32 s66, s67, 1\n" \
"s_add_u32 s64, s99, s101\n" \
"v_mfma_f32_16x16x32_bf16 a[140:143], v[172:175], v[52:55], a[140:143]\n" \
"s_lshl_b32 s65, s100, 3\n" \
"s_add_u32 s94, s66, s95\n" \
"v_mfma_f32_16x16x32_bf16 a[140:143], v[176:179], v[56:59], a[140:143]\n" \
"s_cmp_eq_u32 %[w], 1\n" \
"s_cselect_b32 s22, s64, 0x40000000\n" \
"v_mfma_f32_16x16x32_bf16 a[160:163], v[152:155], v[64:67], a[160:163]\n" \
"s_add_u32 s98, s65, s94\n" \
"s_mul_i32 s99, 0x24800, s98\n" \
"v_mfma_f32_16x16x32_bf16 a[160:163], v[148:151], v[60:63], a[160:163]\n" \
"s_lshl_b32 s101, s97, 12\n" \
"s_sub_u32 s67, 0x78, s65\n" \
"v_mfma_f32_16x16x32_bf16 a[164:167], v[156:159], v[60:63], a[164:167]\n" \
"s_add_u32 s64, s99, s101\n" \
"s_add_u32 s66, s67, s94\n" \
"v_mfma_f32_16x16x32_bf16 a[164:167], v[160:163], v[64:67], a[164:167]\n" \
"s_add_u32 s60, 0x3004800, s64\n" \
"s_mul_i32 s95, 0x24800, s66\n" \
"v_mfma_f32_16x16x32_bf16 a[168:171], v[168:171], v[64:67], a[168:171]\n" \
"s_lshl_b32 s99, s96, 1\n" \
"s_add_u32 s98, s95, s101\n" \
"v_mfma_f32_16x16x32_bf16 a[168:171], v[164:167], v[60:63], a[168:171]\n" \
"s_add_u32 s64, s100, s99\n" \
"s_mov_b32 s65, 0x18000000\n" \
"v_mfma_f32_16x16x32_bf16 a[172:175], v[172:175], v[60:63], a[172:175]\n" \
"s_mul_i32 s94, 6, s96\n" \
"s_add_u32 s66, s96, s94\n" \
"v_mfma_f32_16x16x32_bf16 a[172:175], v[176:179], v[64:67], a[172:175]\n" \
"s_cmp_lt_u32 s64, 2\n" \
"s_cselect_b32 s67, s65, 0x30000000\n" \
"v_mfma_f32_16x16x32_bf16 a[192:195], v[152:155], v[72:75], a[192:195]\n" \
"s_mov_b32 s95, 0x80000\n" \
"s_mov_b32 s64, 0xaaaaaaaa\n" \
"v_mfma_f32_16x16x32_bf16 a[192:195], v[148:151], v[68:71], a[192:195]\n" \
"s_cmp_gt_u32 s66, s100\n" \
"s_cselect_b32 s101, s95, 0x40000\n" \
"s_waitcnt vmcnt(3)\n" \
"s_barrier\n" \
"s_mov_b64 exec, 1\n" \
"buffer_store_dword v45, off, s[80:83], s38 offset:0 sc1\n" \
"s_mov_b64 exec, -1\n" \
"v_mfma_f32_16x16x32_bf16 a[196:199], v[156:159], v[68:71], a[196:199]\n" \
"s_add_u32 m0, s40, 0x8000\n" \
"s_add_u32 s85, s72, 0x0\n" \
"s_nop 0\n" \
"v_mfma_f32_16x16x32_bf16 a[196:199], v[160:163], v[72:75], a[196:199]\n" \
"ds_read_b64_tr_b16 v[140:141], v39 offset:0\n" \
"ds_read_b64_tr_b16 v[142:143], v39 offset:960\n" \
"v_mfma_f32_16x16x32_bf16 a[200:203], v[168:171], v[72:75], a[200:203]\n" \
"buffer_load_dwordx4 v20, s[80:83], s85 offen sc1 lds\n" \
"v_mfma_f32_16x16x32_bf16 a[200:203], v[164:167], v[68:71], a[200:203]\n" \
"s_add_u32 m0, s40, 0x9000\n" \
"s_add_u32 s84, s72, 0x1000\n" \
"v_mfma_f32_16x16x32_bf16 a[204:207], v[172:175], v[68:71], a[204:207]\n" \
"ds_read_b64_tr_b16 v[128:129], v37 offset:4096\n" \
"ds_read_b64_tr_b16 v[130:131], v37 offset:4608\n" \
"v_mfma_f32_16x16x32_bf16 a[204:207], v[176:179], v[72:75], a[204:207]\n" \
"buffer_load_dwordx4 v20, s[80:83], s84 offen sc1 lds\n" \
"v_mfma_f32_16x16x32_bf16 a[224:227], v[152:155], v[80:83], a[224:227]\n" \
"s_add_u32 m0, s40, 0xb000\n" \
"s_add_u32 s85, s62, 0x0\n" \
"v_mfma_f32_16x16x32_bf16 a[224:227], v[148:151], v[76:79], a[224:227]\n" \
"ds_read_b64_tr_b16 v[124:125], v37 offset:0\n" \
"ds_read_b64_tr_b16 v[126:127], v37 offset:512\n" \
"v_mfma_f32_16x16x32_bf16 a[228:231], v[156:159], v[76:79], a[228:231]\n" \
"buffer_load_dwordx4 v21, s[76:79], s85 offen lds\n" \
"v_mfma_f32_16x16x32_bf16 a[228:231], v[160:163], v[80:83], a[228:231]\n" \
"s_add_u32 m0, %[mA], 0x8000\n" \
"s_add_u32 s84, s62, 0x0\n" \
"v_mfma_f32_16x16x32_bf16 a[232:235], v[168:171], v[80:83], a[232:235]\n" \
"ds_read_b64_tr_b16 v[132:133], v38 offset:0\n" \
"ds_read_b64_tr_b16 v[134:135], v38 offset:512\n" \
"v_mfma_f32_16x16x32_bf16 a[232:235], v[164:167], v[76:79], a[232:235]\n" \
"buffer_load_dwordx4 v24, s[76:79], s84 offen lds\n" \
"v_mfma_f32_16x16x32_bf16 a[236:239], v[172:175], v[76:79], a[236:239]\n" \
"ds_read_b64_tr_b16 v[136:137], v38 offset:4096\n" \
"ds_read_b64_tr_b16 v[138:139], v38 offset:4608\n" \
"v_mfma_f32_16x16x32_bf16 a[236:239], v[176:179], v[80:83], a[236:239]\n" \
"s_add_u32 m0, %[mA], 0x8400\n" \
"s_add_u32 s85, s62, 0x2400\n" \
"v_mfma_f32_16x16x32_bf16 a[144:147], v[152:155], v[88:91], a[144:147]\n" \
"buffer_load_dwordx4 v25, s[76:79], s85 offen lds\n" \
"v_mfma_f32_16x16x32_bf16 a[144:147], v[148:151], v[84:87], a[144:147]\n" \
"ds_read_b128 v[52:55], v28 offset:0\n" \
"ds_read_b128 v[56:59], v29 offset:0\n" \
"v_mfma_f32_16x16x32_bf16 a[148:151], v[156:159], v[84:87], a[148:151]\n" \
"s_add_u32 m0, %[mB], 0x8000\n" \
"s_add_u32 s84, s63, 0x0\n" \
"s_nop 0\n" \
"v_mfma_f32_16x16x32_bf16 a[148:151], v[160:163], v[88:91], a[148:151]\n" \
"buffer_load_dwordx4 v26, s[68:71], s84 offen lds\n" \
"v_mfma_f32_16x16x32_bf16 a[152:155], v[168:171], v[88:91], a[152:155]\n" \
"ds_read_b64_tr_b16 v[116:117], v36 offset:0\n" \
"ds_read_b64_tr_b16 v[118:119], v36 offset:512\n" \
"v_mfma_f32_16x16x32_bf16 a[152:155], v[164:167], v[84:87], a[152:155]\n" \
"ds_read_b64_tr_b16 v[120:121], v36 offset:4096\n" \
"ds_read_b64_tr_b16 v[122:123], v36 offset:4608\n" \
"v_mfma_f32_16x16x32_bf16 a[156:159], v[172:175], v[84:87], a[156:159]\n" \
"s_add_u32 m0, %[mA], 0x8800\n" \
"s_add_u32 s85, s62, 0x4800\n" \
"v_mfma_f32_16x16x32_bf16 a[156:159], v[176:179], v[88:91], a[156:159]\n" \
"buffer_load_dwordx4 v24, s[76:79], s85 offen lds\n" \
"v_mfma_f32_16x16x32_bf16 a[176:179], v[152:155], v[96:99], a[176:179]\n" \
"ds_read_b128 v[84:87], v22 offset:0\n" \
"ds_read_b128 v[88:91], v23 offset:0\n" \
"v_mfma_f32_16x16x32_bf16 a[176:179], v[148:151], v[92:95], a[176:179]\n" \
"s_add_u32 m0, %[mA], 0x8c00\n" \
"s_add_u32 s84, s62, 0x6c00\n" \
"v_mfma_f32_16x16x32_bf16 a[180:183], v[156:159], v[92:95], a[180:183]\n" \
"buffer_load_dwordx4 v25, s[76:79], s84 offen lds\n" \
"v_mfma_f32_16x16x32_bf16 a[180:183], v[160:163], v[96:99], a[180:183]\n" \
"ds_read_b128 v[60:63], v28 offset:2048\n" \
"ds_read_b128 v[64:67], v29 offset:2048\n" \
"v_mfma_f32_16x16x32_bf16 a[184:187], v[168:171], v[96:99], a[184:187]\n" \
"ds_read_b128 v[68:71], v28 offset:4096\n" \
"ds_read_b128 v[72:75], v29 offset:4096\n" \
"v_mfma_f32_16x16x32_bf16 a[184:187], v[164:167], v[92:95], a[184:187]\n" \
"s_add_u32 m0, %[mB], 0x8400\n" \
"s_add_u32 s85, s63, 0x8000\n" \
"v_mfma_f32_16x16x32_bf16 a[188:191], v[172:175], v[92:95], a[188:191]\n" \
"buffer_load_dwordx4 v26, s[68:71], s85 offen lds\n" \
"v_mfma_f32_16x16x32_bf16 a[188:191], v[176:179], v[96:99], a[188:191]\n" \
"ds_read_b128 v[76:79], v28 offset:6144\n" \
"ds_read_b128 v[80:83], v29 offset:6144\n" \
"v_mfma_f32_16x16x32_bf16 a[208:211], v[152:155], v[104:107], a[208:211]\n" \
"s_add_u32 m0, %[mA], 0x9000\n" \
"s_add_u32 s84, s62, 0x9000\n" \
"v_mfma_f32_16x16x32_bf16 a[208:211], v[148:151], v[100:103], a[208:211]\n" \
"buffer_load_dwordx4 v24, s[76:79], s84 offen lds\n" \
"v_mfma_f32_16x16x32_bf16 a[212:215], v[156:159], v[100:103], a[212:215]\n" \
"ds_read_b128 v[92:95], v48 offset:2048\n" \
"ds_read_b128 v[96:99], v49 offset:2048\n" \
"v_mfma_f32_16x16x32_bf16 a[212:215], v[160:163], v[104:107], a[212:215]\n" \
"s_mov_b32 s65, 0xaaaaaaaa\n" \
"s_add_u32 s23, s67, s101\n" \
"s_nop 0\n" \
"v_mfma_f32_16x16x32_bf16 a[216:219], v[168:171], v[104:107], a[216:219]\n" \
"s_add_u32 m0, %[mA], 0x9400\n" \
"s_add_u32 s85, s62, 0xb400\n" \
"v_mfma_f32_16x16x32_bf16 a[216:219], v[164:167], v[100:103], a[216:219]\n" \
"buffer_load_dwordx4 v25, s[76:79], s85 offen lds\n" \
"v_mfma_f32_16x16x32_bf16 a[220:223], v[172:175], v[100:103], a[220:223]\n" \
"ds_read_b128 v[100:103], v48 offset:4096\n" \
"s_mov_b32 s66, 0xfff0ffff\n" \
"v_mfma_f32_16x16x32_bf16 a[220:223], v[176:179], v[104:107], a[220:223]\n" \
"s_add_u32 m0, %[mB], 0x8800\n" \
"s_add_u32 s84, s63, 0x10000\n" \
"v_mfma_f32_16x16x32_bf16 a[240:243], v[152:155], v[112:115], a[240:243]\n" \
"buffer_load_dwordx4 v27, s[68:71], s84 offen lds\n" \
"v_mfma_f32_16x16x32_bf16 a[240:243], v[148:151], v[108:111], a[240:243]\n" \
"ds_read_b64_tr_b16 v[152:153], v32 offset:8192\n" \
"ds_read_b64_tr_b16 v[154:155], v32 offset:9216\n" \
"v_mfma_f32_16x16x32_bf16 a[244:247], v[156:159], v[108:111], a[244:247]\n" \
"ds_read_b64_tr_b16 v[148:149], v32 offset:0\n" \
"ds_read_b64_tr_b16 v[150:151], v32 offset:1024\n" \
"v_mfma_f32_16x16x32_bf16 a[244:247], v[160:163], v[112:115], a[244:247]\n" \
"s_add_u32 m0, %[mA], 0x9800\n" \
"s_add_u32 s85, s62, 0xd800\n" \
"v_mfma_f32_16x16x32_bf16 a[248:251], v[168:171], v[112:115], a[248:251]\n" \
"buffer_load_dwordx4 v24, s[76:79], s85 offen lds\n" \
"v_mfma_f32_16x16x32_bf16 a[248:251], v[164:167], v[108:111], a[248:251]\n" \
"ds_read_b64_tr_b16 v[156:157], v33 offset:0\n" \
"ds_read_b64_tr_b16 v[158:159], v33 offset:1024\n" \
"v_mfma_f32_16x16x32_bf16 a[252:255], v[172:175], v[108:111], a[252:255]\n" \
"ds_read_b64_tr_b16 v[160:161], v33 offset:8192\n" \
"ds_read_b64_tr_b16 v[162:163], v33 offset:9216\n" \
"v_mfma_f32_16x16x32_bf16 a[252:255], v[176:179], v[112:115], a[252:255]\n" \
"s_add_u32 m0, %[mA], 0x9c00\n" \
"s_waitcnt lgkmcnt(11)\n" \
"s_nop 0\n" \
"v_smfmac_f32_16x16x64_bf16 a[16:19], v[140:143], v[84:91], v50\n" \
"v_mfma_f32_16x16x32_bf16 a[48:51], v[128:131], v[88:91], a[48:51]\n" \
"s_add_u32 s85, s62, 0xfc00\n" \
"ds_read_b64_tr_b16 v[168:169], v34 offset:8192\n" \
"v_mfma_f32_16x16x32_bf16 a[48:51], v[124:127], v[84:87], a[48:51]\n" \
"buffer_load_dwordx4 v25, s[76:79], s85 offen lds\n" \
"v_mfma_f32_16x16x32_bf16 a[80:83], v[84:87], v[132:135], a[80:83]\n" \
"s_add_u32 m0, %[mB], 0x8c00\n" \
"s_add_u32 s84, s63, 0x18000\n" \
"v_mfma_f32_16x16x32_bf16 a[80:83], v[88:91], v[136:139], a[80:83]\n" \
"ds_read_b64_tr_b16 v[170:171], v34 offset:9216\n" \
"ds_read_b64_tr_b16 v[164:165], v34 offset:0\n" \
"v_smfmac_f32_16x16x64_bf16 a[112:115], v[140:143], v[52:59], v50\n" \
"v_mfma_f32_16x16x32_bf16 a[20:23], v[128:131], v[56:59], a[20:23]\n" \
"buffer_load_dwordx4 v27, s[68:71], s84 offen lds\n" \
"v_mfma_f32_16x16x32_bf16 a[20:23], v[124:127], v[52:55], a[20:23]\n" \
"s_add_u32 s89, s43, 0x1000\n" \
"ds_read_b64_tr_b16 v[166:167], v34 offset:1024\n" \
"v_mfma_f32_16x16x32_bf16 a[52:55], v[116:119], v[52:55], a[52:55]\n" \
"ds_read_b64_tr_b16 v[172:173], v35 offset:0\n" \
"ds_read_b64_tr_b16 v[174:175], v35 offset:1024\n" \
"v_mfma_f32_16x16x32_bf16 a[52:55], v[120:123], v[56:59], a[52:55]\n" \
"buffer_store_dwordx4 a[36:39], v20, s[80:83], s89 offen offset:0 sc1\n" \
"v_mfma_f32_16x16x32_bf16 a[84:87], v[56:59], v[136:139], a[84:87]\n" \
"ds_read_b64_tr_b16 v[176:177], v35 offset:8192\n" \
"ds_read_b64_tr_b16 v[178:179], v35 offset:9216\n" \
"v_mfma_f32_16x16x32_bf16 a[84:87], v[52:55], v[132:135], a[84:87]\n" \
"ds_read_b128 v[84:87], v48 offset:0\n" \
"ds_read_b128 v[88:91], v49 offset:0\n" \
"v_smfmac_f32_16x16x64_bf16 a[116:119], v[140:143], v[60:67], v50\n" \
"v_mfma_f32_16x16x32_bf16 a[24:27], v[124:127], v[60:63], a[24:27]\n" \
"ds_read_b128 v[104:107], v49 offset:4096\n" \
"ds_read_b128 v[108:111], v48 offset:6144\n" \
"v_mfma_f32_16x16x32_bf16 a[24:27], v[128:131], v[64:67], a[24:27]\n" \
"s_add_u32 s88, s43, 0x2000\n" \
"ds_read_b128 v[112:115], v49 offset:6144\n" \
"v_mfma_f32_16x16x32_bf16 a[56:59], v[120:123], v[64:67], a[56:59]\n" \
"buffer_store_dwordx4 a[64:67], v20, s[80:83], s88 offen offset:0 sc1\n" \
"v_mfma_f32_16x16x32_bf16 a[56:59], v[116:119], v[60:63], a[56:59]\n" \
"s_add_u32 s62, s62, 0x80\n" \
"s_mov_b32 s67, 0xf000ff00\n" \
"v_mfma_f32_16x16x32_bf16 a[88:91], v[60:63], v[132:135], a[88:91]\n" \
"s_cmp_lt_u32 s62, 0x480\n" \
"s_cselect_b32 s62, s62, 0\n" \
"v_mfma_f32_16x16x32_bf16 a[88:91], v[64:67], v[136:139], a[88:91]\n" \
"s_lshl_b32 s72, s62, 6\n" \
"s_add_u32 s72, s72, s37\n" \
"v_smfmac_f32_16x16x64_bf16 a[120:123], v[140:143], v[68:75], v50\n" \
"v_mfma_f32_16x16x32_bf16 a[28:31], v[128:131], v[72:75], a[28:31]\n" \
"s_lshl_b32 s63, s62, 12\n" \
"s_add_u32 s61, 0x3004800, s98\n" \
"v_mfma_f32_16x16x32_bf16 a[28:31], v[124:127], v[68:71], a[28:31]\n" \
"s_lshl_b32 s99, s97, 20\n" \
"s_lshl_b32 s98, s100, 16\n" \
"v_mfma_f32_16x16x32_bf16 a[60:63], v[116:119], v[68:71], a[60:63]\n" \
"s_add_u32 s89, s43, 0x3000\n" \
"s_add_u32 s8, 0x1000000, s99\n" \
"v_mfma_f32_16x16x32_bf16 a[60:63], v[120:123], v[72:75], a[60:63]\n" \
"buffer_store_dwordx4 a[68:71], v20, s[80:83], s89 offen offset:0 sc1\n" \
"v_mfma_f32_16x16x32_bf16 a[92:95], v[72:75], v[136:139], a[92:95]\n" \
"s_sub_u32 s19, 14, s18\n" \
"s_sub_u32 s10, 0x100000, s98\n" \
"v_mfma_f32_16x16x32_bf16 a[92:95], v[68:71], v[132:135], a[92:95]\n" \
"s_mov_b32 s11, 0x20000\n" \
"s_mov_b32 s58, 0x6000000\n" \
"v_smfmac_f32_16x16x64_bf16 a[124:127], v[140:143], v[76:83], v50\n" \
"v_mfma_f32_16x16x32_bf16 a[0:3], v[124:127], v[76:79], a[0:3]\n" \
"s_mov_b32 s59, 0x20000\n" \
"s_mov_b64 s[56:57], s[80:81]\n" \
"v_mfma_f32_16x16x32_bf16 a[0:3], v[128:131], v[80:83], a[0:3]\n" \
"s_add_u32 s8, s8, s80\n" \
"s_addc_u32 s9, s81, 0\n" \
"v_mfma_f32_16x16x32_bf16 a[4:7], v[120:123], v[80:83], a[4:7]\n" \
"v_mfma_f32_16x16x32_bf16 a[4:7], v[116:119], v[76:79], a[4:7]\n" \
"v_mfma_f32_16x16x32_bf16 a[32:35], v[76:79], v[132:135], a[32:35]\n" \
"v_mfma_f32_16x16x32_bf16 a[32:35], v[80:83], v[136:139], a[32:35]\n" \
"s_waitcnt lgkmcnt(0)\n" \
"s_nop 0\n" \
"v_mfma_f32_16x16x32_bf16 a[128:131], v[152:155], v[56:59], a[128:131]\n" \
"v_mfma_f32_16x16x32_bf16 a[128:131], v[148:151], v[52:55], a[128:131]\n" \
"v_mfma_f32_16x16x32_bf16 a[132:135], v[156:159], v[52:55], a[132:135]\n" \
"v_mfma_f32_16x16x32_bf16 a[132:135], v[160:163], v[56:59], a[132:135]\n" \
"v_mfma_f32_16x16x32_bf16 a[136:139], v[168:171], v[56:59], a[136:139]\n" \
"v_mfma_f32_16x16x32_bf16 a[136:139], v[164:167], v[52:55], a[136:139]\n" \
"v_mfma_f32_16x16x32_bf16 a[140:143], v[172:175], v[52:55], a[140:143]\n" \
"v_mfma_f32_16x16x32_bf16 a[140:143], v[176:179], v[56:59], a[140:143]\n" \
"v_mfma_f32_16x16x32_bf16 a[160:163], v[152:155], v[64:67], a[160:163]\n" \
"v_mfma_f32_16x16x32_bf16 a[160:163], v[148:151], v[60:63], a[160:163]\n" \
"v_mfma_f32_16x16x32_bf16 a[164:167], v[156:159], v[60:63], a[164:167]\n" \
"v_mfma_f32_16x16x32_bf16 a[164:167], v[160:163], v[64:67], a[164:167]\n" \
"v_mfma_f32_16x16x32_bf16 a[168:171], v[168:171], v[64:67], a[168:171]\n" \
"v_mfma_f32_16x16x32_bf16 a[168:171], v[164:167], v[60:63], a[168:171]\n" \
"v_mfma_f32_16x16x32_bf16 a[172:175], v[172:175], v[60:63], a[172:175]\n" \
"v_mfma_f32_16x16x32_bf16 a[172:175], v[176:179], v[64:67], a[172:175]\n" \
"v_mfma_f32_16x16x32_bf16 a[192:195], v[152:155], v[72:75], a[192:195]\n" \
"v_mfma_f32_16x16x32_bf16 a[192:195], v[148:151], v[68:71], a[192:195]\n" \
"s_waitcnt vmcnt(3)\n" \
"s_barrier\n" \
"v_mfma_f32_16x16x32_bf16 a[196:199], v[156:159], v[68:71], a[196:199]\n" \
"s_add_u32 m0, s40, 0x0\n" \
"s_add_u32 s85, s72, 0x0\n" \
"v_mfma_f32_16x16x32_bf16 a[196:199], v[160:163], v[72:75], a[196:199]\n" \
"buffer_load_dwordx4 v20, s[80:83], s85 offen sc1 lds\n" \
"v_mfma_f32_16x16x32_bf16 a[200:203], v[168:171], v[72:75], a[200:203]\n" \
"s_add_u32 m0, s40, 0x1000\n" \
"s_add_u32 s84, s72, 0x1000\n" \
"v_mfma_f32_16x16x32_bf16 a[200:203], v[164:167], v[68:71], a[200:203]\n" \
"ds_read_b64_tr_b16 v[140:141], v39 offset:32768\n" \
"ds_read_b64_tr_b16 v[142:143], v39 offset:33728\n" \
"v_mfma_f32_16x16x32_bf16 a[204:207], v[172:175], v[68:71], a[204:207]\n" \
"buffer_load_dwordx4 v20, s[80:83], s84 offen sc1 lds\n" \
"v_mfma_f32_16x16x32_bf16 a[204:207], v[176:179], v[72:75], a[204:207]\n" \
"ds_read_b64_tr_b16 v[128:129], v37 offset:36864\n" \
"ds_read_b64_tr_b16 v[130:131], v37 offset:37376\n" \
"v_mfma_f32_16x16x32_bf16 a[224:227], v[152:155], v[80:83], a[224:227]\n" \
"s_add_u32 m0, s40, 0x3000\n" \
"s_add_u32 s85, s62, 0x0\n" \
"v_mfma_f32_16x16x32_bf16 a[224:227], v[148:151], v[76:79], a[224:227]\n" \
"buffer_load_dwordx4 v21, s[76:79], s85 offen lds\n" \
"v_mfma_f32_16x16x32_bf16 a[228:231], v[156:159], v[76:79], a[228:231]\n" \
"ds_read_b64_tr_b16 v[124:125], v37 offset:32768\n" \
"ds_read_b64_tr_b16 v[126:127], v37 offset:33280\n" \
"v_mfma_f32_16x16x32_bf16 a[228:231], v[160:163], v[80:83], a[228:231]\n" \
"s_add_u32 m0, %[mA], 0x0\n" \
"s_add_u32 s84, s62, 0x0\n" \
"s_nop 0\n" \
"v_mfma_f32_16x16x32_bf16 a[232:235], v[168:171], v[80:83], a[232:235]\n" \
"buffer_load_dwordx4 v24, s[76:79], s84 offen lds\n" \
"v_mfma_f32_16x16x32_bf16 a[232:235], v[164:167], v[76:79], a[232:235]\n" \
"ds_read_b64_tr_b16 v[132:133], v38 offset:32768\n" \
"ds_read_b64_tr_b16 v[134:135], v38 offset:33280\n" \
"v_mfma_f32_16x16x32_bf16 a[236:239], v[172:175], v[76:79], a[236:239]\n" \
"ds_read_b64_tr_b16 v[136:137], v38 offset:36864\n" \
"ds_read_b64_tr_b16 v[138:139], v38 offset:37376\n" \
"v_mfma_f32_16x16x32_bf16 a[236:239], v[176:179], v[80:83], a[236:239]\n" \
"s_add_u32 m0, %[mA], 0x400\n" \
"s_add_u32 s85, s62, 0x2400\n" \
"v_mfma_f32_16x16x32_bf16 a[144:147], v[152:155], v[88:91], a[144:147]\n" \
"buffer_load_dwordx4 v25, s[76:79], s85 offen lds\n" \
"v_mfma_f32_16x16x32_bf16 a[144:147], v[148:151], v[84:87], a[144:147]\n" \
"ds_read_b128 v[52:55], v28 offset:32768\n" \
"ds_read_b128 v[56:59], v29 offset:32768\n" \
"v_mfma_f32_16x16x32_bf16 a[148:151], v[156:159], v[84:87], a[148:151]\n" \
"s_add_u32 m0, %[mB], 0x0\n" \
"s_add_u32 s84, s63, 0x0\n" \
"v_mfma_f32_16x16x32_bf16 a[148:151], v[160:163], v[88:91], a[148:151]\n" \
"buffer_load_dwordx4 v26, s[68:71], s84 offen lds\n" \
"v_mfma_f32_16x16x32_bf16 a[152:155], v[168:171], v[88:91], a[152:155]\n" \
"ds_read_b64_tr_b16 v[116:117], v36 offset:32768\n" \
"ds_read_b64_tr_b16 v[118:119], v36 offset:33280\n" \
"v_mfma_f32_16x16x32_bf16 a[152:155], v[164:167], v[84:87], a[152:155]\n" \
"ds_read_b64_tr_b16 v[120:121], v36 offset:36864\n" \
"ds_read_b64_tr_b16 v[122:123], v36 offset:37376\n" \
"v_mfma_f32_16x16x32_bf16 a[156:159], v[172:175], v[84:87], a[156:159]\n" \
"s_add_u32 m0, %[mA], 0x800\n" \
"s_add_u32 s85, s62, 0x4800\n" \
"v_mfma_f32_16x16x32_bf16 a[156:159], v[176:179], v[88:91], a[156:159]\n" \
"buffer_load_dwordx4 v24, s[76:79], s85 offen lds\n" \
"v_mfma_f32_16x16x32_bf16 a[176:179], v[152:155], v[96:99], a[176:179]\n" \
"ds_read_b128 v[84:87], v22 offset:32768\n" \
"ds_read_b128 v[88:91], v23 offset:32768\n" \
"v_mfma_f32_16x16x32_bf16 a[176:179], v[148:151], v[92:95], a[176:179]\n" \
"s_add_u32 m0, %[mA], 0xc00\n" \
"s_add_u32 s84, s62, 0x6c00\n" \
"v_mfma_f32_16x16x32_bf16 a[180:183], v[156:159], v[92:95], a[180:183]\n" \
"buffer_load_dwordx4 v25, s[76:79], s84 offen lds\n" \
"v_mfma_f32_16x16x32_bf16 a[180:183], v[160:163], v[96:99], a[180:183]\n" \
"ds_read_b128 v[60:63], v28 offset:34816\n" \
"ds_read_b128 v[64:67], v29 offset:34816\n" \
"v_mfma_f32_16x16x32_bf16 a[184:187], v[168:171], v[96:99], a[184:187]\n" \
"ds_read_b128 v[68:71], v28 offset:36864\n" \
"ds_read_b128 v[72:75], v29 offset:36864\n" \
"v_mfma_f32_16x16x32_bf16 a[184:187], v[164:167], v[92:95], a[184:187]\n" \
"s_add_u32 m0, %[mB], 0x400\n" \
"s_add_u32 s85, s63, 0x8000\n" \
"v_mfma_f32_16x16x32_bf16 a[188:191], v[172:175], v[92:95], a[188:191]\n" \
"buffer_load_dwordx4 v26, s[68:71], s85 offen lds\n" \
"v_mfma_f32_16x16x32_bf16 a[188:191], v[176:179], v[96:99], a[188:191]\n" \
"ds_read_b128 v[76:79], v28 offset:38912\n" \
"ds_read_b128 v[80:83], v29 offset:38912\n" \
"v_mfma_f32_16x16x32_bf16 a[208:211], v[152:155], v[104:107], a[208:211]\n" \
"s_add_u32 m0, %[mA], 0x1000\n" \
"s_add_u32 s84, s62, 0x9000\n" \
"v_mfma_f32_16x16x32_bf16 a[208:211], v[148:151], v[100:103], a[208:211]\n" \
"buffer_load_dwordx4 v24, s[76:79], s84 offen lds\n" \
"v_mfma_f32_16x16x32_bf16 a[212:215], v[156:159], v[100:103], a[212:215]\n" \
"ds_read_b128 v[92:95], v48 offset:34816\n" \
"ds_read_b128 v[96:99], v49 offset:34816\n" \
"v_mfma_f32_16x16x32_bf16 a[212:215], v[160:163], v[104:107], a[212:215]\n" \
"s_mov_b32 s72, 0x3eae1a93\n" \
"v_and_b32_e32 v26, 0x1ff, v20\n" \
"v_mfma_f32_16x16x32_bf16 a[216:219], v[168:171], v[104:107], a[216:219]\n" \
"s_add_u32 m0, %[mA], 0x1400\n" \
"s_add_u32 s85, s62, 0xb400\n" \
"v_mfma_f32_16x16x32_bf16 a[216:219], v[164:167], v[100:103], a[216:219]\n" \
"buffer_load_dwordx4 v25, s[76:79], s85 offen lds\n" \
"v_mfma_f32_16x16x32_bf16 a[220:223], v[172:175], v[100:103], a[220:223]\n" \
"ds_read_b128 v[100:103], v48 offset:36864\n" \
"v_add_u32_e64 v21, s48, v20\n" \
"v_mfma_f32_16x16x32_bf16 a[220:223], v[176:179], v[104:107], a[220:223]\n" \
"s_add_u32 m0, %[mB], 0x800\n" \
"s_add_u32 s84, s63, 0x10000\n" \
"v_mfma_f32_16x16x32_bf16 a[240:243], v[152:155], v[112:115], a[240:243]\n" \
"buffer_load_dwordx4 v27, s[68:71], s84 offen lds\n" \
"L_pf8:\n" \
"s_cbranch_execz L_pf12\n" \
"s_nop 0\n" \
"v_mfma_f32_16x16x32_bf16 a[240:243], v[148:151], v[108:111], a[240:243]\n" \
"ds_read_b64_tr_b16 v[152:153], v32 offset:40960\n" \
"ds_read_b64_tr_b16 v[154:155], v32 offset:41984\n" \
"v_mfma_f32_16x16x32_bf16 a[244:247], v[156:159], v[108:111], a[244:247]\n" \
"ds_read_b64_tr_b16 v[148:149], v32 offset:32768\n" \
"ds_read_b64_tr_b16 v[150:151], v32 offset:33792\n" \
"v_mfma_f32_16x16x32_bf16 a[244:247], v[160:163], v[112:115], a[244:247]\n" \
"s_add_u32 m0, %[mA], 0x1800\n" \
"s_add_u32 s85, s62, 0xd800\n" \
"v_mfma_f32_16x16x32_bf16 a[248:251], v[168:171], v[112:115], a[248:251]\n" \
"buffer_load_dwordx4 v24, s[76:79], s85 offen lds\n" \
"v_mfma_f32_16x16x32_bf16 a[248:251], v[164:167], v[108:111], a[248:251]\n" \
"ds_read_b64_tr_b16 v[156:157], v33 offset:32768\n" \
"ds_read_b64_tr_b16 v[158:159], v33 offset:33792\n" \
"v_mfma_f32_16x16x32_bf16 a[252:255], v[172:175], v[108:111], a[252:255]\n" \
"ds_read_b64_tr_b16 v[160:161], v33 offset:40960\n" \
"ds_read_b64_tr_b16 v[162:163], v33 offset:41984\n" \
"v_mfma_f32_16x16x32_bf16 a[252:255], v[176:179], v[112:115], a[252:255]\n" \
"s_add_u32 m0, %[mA], 0x1c00\n" \
"s_waitcnt lgkmcnt(11)\n" \
"v_smfmac_f32_16x16x64_bf16 a[16:19], v[140:143], v[84:91], v50\n" \
"v_mfma_f32_16x16x32_bf16 a[48:51], v[128:131], v[88:91], a[48:51]\n" \
"s_add_u32 s85, s62, 0xfc00\n" \
"ds_read_b64_tr_b16 v[168:169], v34 offset:40960\n" \
"v_mfma_f32_16x16x32_bf16 a[48:51], v[124:127], v[84:87], a[48:51]\n" \
"buffer_load_dwordx4 v25, s[76:79], s85 offen lds\n" \
"s_mov_b32 s76, s54\n" \
"v_mfma_f32_16x16x32_bf16 a[80:83], v[84:87], v[132:135], a[80:83]\n" \
"s_add_u32 m0, %[mB], 0xc00\n" \
"s_add_u32 s84, s63, 0x18000\n" \
"v_mfma_f32_16x16x32_bf16 a[80:83], v[88:91], v[136:139], a[80:83]\n" \
"ds_read_b64_tr_b16 v[170:171], v34 offset:41984\n" \
"ds_read_b64_tr_b16 v[164:165], v34 offset:32768\n" \
"v_smfmac_f32_16x16x64_bf16 a[112:115], v[140:143], v[52:59], v50\n" \
"v_mfma_f32_16x16x32_bf16 a[20:23], v[128:131], v[56:59], a[20:23]\n" \
"buffer_load_dwordx4 v27, s[68:71], s84 offen lds\n" \
"v_mfma_f32_16x16x32_bf16 a[20:23], v[124:127], v[52:55], a[20:23]\n" \
"s_add_u32 s88, s43, 0x0\n" \
"s_nop 0\n" \
"ds_read_b64_tr_b16 v[166:167], v34 offset:33792\n" \
"v_mfma_f32_16x16x32_bf16 a[52:55], v[116:119], v[52:55], a[52:55]\n" \
"ds_read_b64_tr_b16 v[172:173], v35 offset:32768\n" \
"ds_read_b64_tr_b16 v[174:175], v35 offset:33792\n" \
"v_mfma_f32_16x16x32_bf16 a[52:55], v[120:123], v[56:59], a[52:55]\n" \
"buffer_store_dwordx4 a[96:99], v20, s[80:83], s88 offen offset:1024 sc1\n" \
"v_mfma_f32_16x16x32_bf16 a[84:87], v[56:59], v[136:139], a[84:87]\n" \
"ds_read_b64_tr_b16 v[176:177], v35 offset:40960\n" \
"ds_read_b64_tr_b16 v[178:179], v35 offset:41984\n" \
"v_mfma_f32_16x16x32_bf16 a[84:87], v[52:55], v[132:135], a[84:87]\n" \
"ds_read_b128 v[84:87], v48 offset:32768\n" \
"ds_read_b128 v[88:91], v49 offset:32768\n" \
"v_smfmac_f32_16x16x64_bf16 a[116:119], v[140:143], v[60:67], v50\n" \
"v_mfma_f32_16x16x32_bf16 a[24:27], v[124:127], v[60:63], a[24:27]\n" \
"ds_read_b128 v[104:107], v49 offset:36864\n" \
"ds_read_b128 v[108:111], v48 offset:38912\n" \
"v_mfma_f32_16x16x32_bf16 a[24:27], v[128:131], v[64:67], a[24:27]\n" \
"s_add_u32 s89, s43, 0x1000\n" \
"ds_read_b128 v[112:115], v49 offset:38912\n" \
"v_mfma_f32_16x16x32_bf16 a[56:59], v[120:123], v[64:67], a[56:59]\n" \
"buffer_store_dwordx4 a[100:103], v20, s[80:83], s89 offen offset:1024 sc1\n" \
"s_add_u32 s89, s43, 0x3000\n" \
"v_mfma_f32_16x16x32_bf16 a[56:59], v[116:119], v[60:63], a[56:59]\n" \
"v_accvgpr_read_b32 v248, a16\n" \
"v_accvgpr_read_b32 v249, a17\n" \
"v_mfma_f32_16x16x32_bf16 a[88:91], v[60:63], v[132:135], a[88:91]\n" \
"v_accvgpr_read_b32 v250, a18\n" \
"v_accvgpr_read_b32 v251, a19\n" \
"v_mfma_f32_16x16x32_bf16 a[88:91], v[64:67], v[136:139], a[88:91]\n" \
"v_accvgpr_read_b32 v180, a48\n" \
"v_accvgpr_read_b32 v181, a49\n" \
"v_smfmac_f32_16x16x64_bf16 a[120:123], v[140:143], v[68:75], v50\n" \
"v_mfma_f32_16x16x32_bf16 a[28:31], v[128:131], v[72:75], a[28:31]\n" \
"v_accvgpr_read_b32 v182, a50\n" \
"v_accvgpr_read_b32 v183, a51\n" \
"v_mfma_f32_16x16x32_bf16 a[28:31], v[124:127], v[68:71], a[28:31]\n" \
"v_accvgpr_read_b32 v252, a80\n" \
"v_accvgpr_read_b32 v253, a81\n" \
"v_mfma_f32_16x16x32_bf16 a[60:63], v[116:119], v[68:71], a[60:63]\n" \
"s_add_u32 s88, s43, 0x2000\n" \
"v_accvgpr_read_b32 v254, a82\n" \
"v_mfma_f32_16x16x32_bf16 a[60:63], v[120:123], v[72:75], a[60:63]\n" \
"buffer_store_dwordx4 a[8:11], v20, s[80:83], s88 offen offset:1024 sc1\n" \
"s_add_u32 s88, s45, 0x0\n" \
"s_nop 0\n" \
"v_mfma_f32_16x16x32_bf16 a[92:95], v[72:75], v[136:139], a[92:95]\n" \
"v_accvgpr_read_b32 v255, a83\n" \
"v_accvgpr_read_b32 v196, a112\n" \
"v_mfma_f32_16x16x32_bf16 a[92:95], v[68:71], v[132:135], a[92:95]\n" \
"v_accvgpr_read_b32 v197, a113\n" \
"v_accvgpr_read_b32 v198, a114\n" \
"v_smfmac_f32_16x16x64_bf16 a[124:127], v[140:143], v[76:83], v50\n" \
"v_mfma_f32_16x16x32_bf16 a[0:3], v[124:127], v[76:79], a[0:3]\n" \
"v_accvgpr_read_b32 v199, a115\n" \
"v_accvgpr_read_b32 v188, a20\n" \
"v_mfma_f32_16x16x32_bf16 a[0:3], v[128:131], v[80:83], a[0:3]\n" \
"v_accvgpr_read_b32 v189, a21\n" \
"v_accvgpr_read_b32 v190, a22\n" \
"v_mfma_f32_16x16x32_bf16 a[4:7], v[120:123], v[80:83], a[4:7]\n" \
"v_accvgpr_read_b32 v191, a23\n" \
"v_accvgpr_read_b32 v184, a52\n" \
"v_mfma_f32_16x16x32_bf16 a[4:7], v[116:119], v[76:79], a[4:7]\n" \
"v_lshlrev_b32_e32 v45, 10, v20\n" \
"v_add_u32_e32 v45, s41, v45\n" \
"v_mfma_f32_16x16x32_bf16 a[32:35], v[76:79], v[132:135], a[32:35]\n" \
"s_mov_b32 m0, %[mS]\n" \
"v_accvgpr_read_b32 v185, a53\n" \
"v_mfma_f32_16x16x32_bf16 a[32:35], v[80:83], v[136:139], a[32:35]\n" \
"v_accvgpr_read_b32 v186, a54\n" \
"s_waitcnt lgkmcnt(0)\n" \
"v_mfma_f32_16x16x32_bf16 a[128:131], v[152:155], v[56:59], a[128:131]\n" \
"v_accvgpr_read_b32 v187, a55\n" \
"v_accvgpr_read_b32 v192, a84\n" \
"v_mfma_f32_16x16x32_bf16 a[128:131], v[148:151], v[52:55], a[128:131]\n" \
"v_accvgpr_read_b32 v193, a85\n" \
"v_accvgpr_read_b32 v194, a86\n" \
"v_mfma_f32_16x16x32_bf16 a[132:135], v[156:159], v[52:55], a[132:135]\n" \
"v_accvgpr_read_b32 v195, a87\n" \
"v_accvgpr_read_b32 v212, a116\n" \
"s_getpc_b64 s[86:87]\n" \
"L_ct15_%=:\n" \
"s_add_u32 s86, s86, L_end_%=-L_ct15_%=\n" \
"s_addc_u32 s87, s87, 0\n" \
"v_mfma_f32_16x16x32_bf16 a[132:135], v[160:163], v[56:59], a[132:135]\n" \
"v_accvgpr_read_b32 v213, a117\n" \
"v_accvgpr_read_b32 v214, a118\n" \
"v_mfma_f32_16x16x32_bf16 a[136:139], v[168:171], v[56:59], a[136:139]\n" \
"v_accvgpr_read_b32 v215, a119\n" \
"v_accvgpr_read_b32 v204, a24\n" \
"v_mfma_f32_16x16x32_bf16 a[136:139], v[164:167], v[52:55], a[136:139]\n" \
"v_accvgpr_read_b32 v205, a25\n" \
"v_accvgpr_read_b32 v206, a26\n" \
"v_mfma_f32_16x16x32_bf16 a[140:143], v[172:175], v[52:55], a[140:143]\n" \
"v_accvgpr_read_b32 v207, a27\n" \
"v_accvgpr_read_b32 v200, a56\n" \
"v_mfma_f32_16x16x32_bf16 a[140:143], v[176:179], v[56:59], a[140:143]\n" \
"v_accvgpr_read_b32 v201, a57\n" \
"v_accvgpr_read_b32 v202, a58\n" \
"v_mfma_f32_16x16x32_bf16 a[160:163], v[152:155], v[64:67], a[160:163]\n" \
"v_accvgpr_read_b32 v203, a59\n" \
"v_accvgpr_read_b32 v208, a88\n" \
"v_mfma_f32_16x16x32_bf16 a[160:163], v[148:151], v[60:63], a[160:163]\n" \
"v_accvgpr_read_b32 v209, a89\n" \
"v_accvgpr_read_b32 v210, a90\n" \
"v_mfma_f32_16x16x32_bf16 a[164:167], v[156:159], v[60:63], a[164:167]\n" \
"v_accvgpr_read_b32 v211, a91\n" \
"v_accvgpr_read_b32 v228, a120\n" \
"v_mfma_f32_16x16x32_bf16 a[164:167], v[160:163], v[64:67], a[164:167]\n" \
"v_accvgpr_read_b32 v229, a121\n" \
"v_accvgpr_read_b32 v230, a122\n" \
"v_mfma_f32_16x16x32_bf16 a[168:171], v[168:171], v[64:67], a[168:171]\n" \
"v_accvgpr_read_b32 v231, a123\n" \
"v_accvgpr_read_b32 v220, a28\n" \
"v_mfma_f32_16x16x32_bf16 a[168:171], v[164:167], v[60:63], a[168:171]\n" \
"v_accvgpr_read_b32 v221, a29\n" \
"v_accvgpr_read_b32 v222, a30\n" \
"v_mfma_f32_16x16x32_bf16 a[172:175], v[172:175], v[60:63], a[172:175]\n" \
"v_accvgpr_read_b32 v223, a31\n" \
"v_accvgpr_read_b32 v216, a60\n" \
"v_mfma_f32_16x16x32_bf16 a[172:175], v[176:179], v[64:67], a[172:175]\n" \
"v_accvgpr_read_b32 v217, a61\n" \
"v_accvgpr_read_b32 v218, a62\n" \
"v_mfma_f32_16x16x32_bf16 a[192:195], v[152:155], v[72:75], a[192:195]\n" \
"v_accvgpr_read_b32 v219, a63\n" \
"v_accvgpr_read_b32 v224, a92\n" \
"v_mfma_f32_16x16x32_bf16 a[192:195], v[148:151], v[68:71], a[192:195]\n" \
"v_accvgpr_read_b32 v225, a93\n" \
"v_accvgpr_read_b32 v226, a94\n" \
"s_waitcnt vmcnt(3)\n" \
"s_barrier\n" \
"s_mov_b64 exec, 3\n" \
"global_load_lds_dword v45, s[86:87]\n" \
"s_mov_b64 exec, -1\n" \
"s_sub_u32 m0, %[mB], 0x10000\n" \
"v_mfma_f32_16x16x32_bf16 a[196:199], v[156:159], v[68:71], a[196:199]\n" \
"ds_read_b64_tr_b16 v[140:141], v39 offset:0\n" \
"ds_read_b64_tr_b16 v[142:143], v39 offset:960\n" \
"v_mfma_f32_16x16x32_bf16 a[196:199], v[160:163], v[72:75], a[196:199]\n" \
"ds_read_b64_tr_b16 v[128:129], v37 offset:4096\n" \
"ds_read_b64_tr_b16 v[130:131], v37 offset:4608\n" \
"v_mfma_f32_16x16x32_bf16 a[200:203], v[168:171], v[72:75], a[200:203]\n" \
"ds_read_b64_tr_b16 v[124:125], v37 offset:0\n" \
"ds_read_b64_tr_b16 v[126:127], v37 offset:512\n" \
"v_mfma_f32_16x16x32_bf16 a[200:203], v[164:167], v[68:71], a[200:203]\n" \
"ds_read_b64_tr_b16 v[132:133], v38 offset:0\n" \
"ds_read_b64_tr_b16 v[134:135], v38 offset:512\n" \
"v_mfma_f32_16x16x32_bf16 a[204:207], v[172:175], v[68:71], a[204:207]\n" \
"ds_read_b64_tr_b16 v[136:137], v38 offset:4096\n" \
"ds_read_b64_tr_b16 v[138:139], v38 offset:4608\n" \
"v_mfma_f32_16x16x32_bf16 a[204:207], v[176:179], v[72:75], a[204:207]\n" \
"ds_read_b128 v[52:55], v28 offset:0\n" \
"ds_read_b128 v[56:59], v29 offset:0\n" \
"v_mfma_f32_16x16x32_bf16 a[224:227], v[152:155], v[80:83], a[224:227]\n" \
"ds_read_b64_tr_b16 v[116:117], v36 offset:0\n" \
"ds_read_b64_tr_b16 v[118:119], v36 offset:512\n" \
"v_mfma_f32_16x16x32_bf16 a[224:227], v[148:151], v[76:79], a[224:227]\n" \
"ds_read_b64_tr_b16 v[120:121], v36 offset:4096\n" \
"ds_read_b64_tr_b16 v[122:123], v36 offset:4608\n" \
"v_mfma_f32_16x16x32_bf16 a[228:231], v[156:159], v[76:79], a[228:231]\n" \
"ds_read_b128 v[60:63], v28 offset:2048\n" \
"ds_read_b128 v[64:67], v29 offset:2048\n" \
"v_mfma_f32_16x16x32_bf16 a[228:231], v[160:163], v[80:83], a[228:231]\n" \
"v_accvgpr_read_b32 v227, a95\n" \
"v_accvgpr_read_b32 v244, a124\n" \
"v_mfma_f32_16x16x32_bf16 a[232:235], v[168:171], v[80:83], a[232:235]\n" \
"ds_read_b128 v[68:71], v28 offset:4096\n" \
"ds_read_b128 v[72:75], v29 offset:4096\n" \
"v_mfma_f32_16x16x32_bf16 a[232:235], v[164:167], v[76:79], a[232:235]\n" \
"v_accvgpr_read_b32 v245, a125\n" \
"v_accvgpr_read_b32 v246, a126\n" \
"v_mfma_f32_16x16x32_bf16 a[236:239], v[172:175], v[76:79], a[236:239]\n" \
"ds_read_b128 v[76:79], v28 offset:6144\n" \
"v_accvgpr_read_b32 v247, a127\n" \
"v_mfma_f32_16x16x32_bf16 a[236:239], v[176:179], v[80:83], a[236:239]\n" \
"ds_read_b128 v[80:83], v29 offset:6144\n" \
"v_accvgpr_read_b32 v236, a0\n" \
"v_mfma_f32_16x16x32_bf16 a[144:147], v[152:155], v[88:91], a[144:147]\n" \
"v_accvgpr_read_b32 v237, a1\n" \
"v_accvgpr_read_b32 v238, a2\n" \
"v_mfma_f32_16x16x32_bf16 a[144:147], v[148:151], v[84:87], a[144:147]\n" \
"v_accvgpr_read_b32 v239, a3\n" \
"v_accvgpr_read_b32 v232, a4\n" \
"v_mfma_f32_16x16x32_bf16 a[148:151], v[156:159], v[84:87], a[148:151]\n" \
"v_accvgpr_read_b32 v233, a5\n" \
"v_accvgpr_read_b32 v234, a6\n" \
"v_mfma_f32_16x16x32_bf16 a[148:151], v[160:163], v[88:91], a[148:151]\n" \
"v_accvgpr_read_b32 v235, a7\n" \
"v_accvgpr_read_b32 v240, a32\n" \
"v_mfma_f32_16x16x32_bf16 a[152:155], v[168:171], v[88:91], a[152:155]\n" \
"v_accvgpr_read_b32 v241, a33\n" \
"v_accvgpr_read_b32 v242, a34\n" \
"v_mfma_f32_16x16x32_bf16 a[152:155], v[164:167], v[84:87], a[152:155]\n" \
"v_accvgpr_read_b32 v243, a35\n" \
"s_mov_b32 s78, -1\n" \
"v_mfma_f32_16x16x32_bf16 a[156:159], v[172:175], v[84:87], a[156:159]\n" \
"ds_read_b128 v[84:87], v22 offset:0\n" \
"s_mov_b32 s79, 0\n" \
"v_mfma_f32_16x16x32_bf16 a[156:159], v[176:179], v[88:91], a[156:159]\n" \
"ds_read_b128 v[88:91], v23 offset:0\n" \
"s_mov_b32 s68, 0xffc0fffc\n" \
"v_mfma_f32_16x16x32_bf16 a[176:179], v[152:155], v[96:99], a[176:179]\n" \
"s_mov_b32 s69, 0xc000fc00\n" \
"v_lshrrev_b32_e32 v24, 9, v20\n" \
"v_mfma_f32_16x16x32_bf16 a[176:179], v[148:151], v[92:95], a[176:179]\n" \
"v_lshrrev_b32_e32 v25, 8, v20\n" \
"v_mul_u32_u24_e32 v24, 0x24800, v24\n" \
"v_mfma_f32_16x16x32_bf16 a[180:183], v[156:159], v[92:95], a[180:183]\n" \
"v_mul_u32_u24_e32 v25, 0x24700, v25\n" \
"v_add_u32_e64 v24, v24, v26\n" \
"v_mfma_f32_16x16x32_bf16 a[180:183], v[160:163], v[96:99], a[180:183]\n" \
"v_add_u32_e32 v25, v25, v20\n" \
"s_add_u32 s85, s50, 0x0\n" \
"v_mfma_f32_16x16x32_bf16 a[184:187], v[168:171], v[96:99], a[184:187]\n" \
"s_add_u32 s87, s50, 0x49000\n" \
"s_mov_b32 s70, 0x3e0293ee\n" \
"v_mfma_f32_16x16x32_bf16 a[184:187], v[164:167], v[92:95], a[184:187]\n" \
"s_mov_b32 s71, 0x3e0293ee\n" \
"v_add_u32_e64 v27, s47, v20\n" \
"v_mfma_f32_16x16x32_bf16 a[188:191], v[172:175], v[92:95], a[188:191]\n" \
"ds_read_b128 v[92:95], v48 offset:2048\n" \
"v_lshrrev_b32_e32 v26, 2, v20\n" \
"v_mfma_f32_16x16x32_bf16 a[188:191], v[176:179], v[96:99], a[188:191]\n" \
"ds_read_b128 v[96:99], v49 offset:2048\n" \
"v_add_u32_e32 v26, s46, v26\n" \
"v_mfma_f32_16x16x32_bf16 a[208:211], v[152:155], v[104:107], a[208:211]\n" \
"v_mfma_f32_16x16x32_bf16 a[208:211], v[148:151], v[100:103], a[208:211]\n" \
"v_mfma_f32_16x16x32_bf16 a[212:215], v[156:159], v[100:103], a[212:215]\n" \
"v_mfma_f32_16x16x32_bf16 a[212:215], v[160:163], v[104:107], a[212:215]\n" \
"v_mfma_f32_16x16x32_bf16 a[216:219], v[168:171], v[104:107], a[216:219]\n" \
"v_mfma_f32_16x16x32_bf16 a[216:219], v[164:167], v[100:103], a[216:219]\n" \
"v_mfma_f32_16x16x32_bf16 a[220:223], v[172:175], v[100:103], a[220:223]\n" \
"ds_read_b128 v[100:103], v48 offset:4096\n" \
"v_mfma_f32_16x16x32_bf16 a[220:223], v[176:179], v[104:107], a[220:223]\n" \
"ds_read_b128 v[104:107], v49 offset:4096\n" \
"v_mfma_f32_16x16x32_bf16 a[240:243], v[152:155], v[112:115], a[240:243]\n" \
"ds_read_b64_tr_b16 v[152:153], v32 offset:8192\n" \
"ds_read_b64_tr_b16 v[154:155], v32 offset:9216\n" \
"v_mfma_f32_16x16x32_bf16 a[240:243], v[148:151], v[108:111], a[240:243]\n" \
"ds_read_b64_tr_b16 v[148:149], v32 offset:0\n" \
"ds_read_b64_tr_b16 v[150:151], v32 offset:1024\n" \
"v_mfma_f32_16x16x32_bf16 a[244:247], v[156:159], v[108:111], a[244:247]\n" \
"ds_read_b64_tr_b16 v[156:157], v33 offset:0\n" \
"ds_read_b64_tr_b16 v[158:159], v33 offset:1024\n" \
"v_mfma_f32_16x16x32_bf16 a[244:247], v[160:163], v[112:115], a[244:247]\n" \
"ds_read_b64_tr_b16 v[160:161], v33 offset:8192\n" \
"ds_read_b64_tr_b16 v[162:163], v33 offset:9216\n" \
"v_mfma_f32_16x16x32_bf16 a[248:251], v[168:171], v[112:115], a[248:251]\n" \
"ds_read_b64_tr_b16 v[168:169], v34 offset:8192\n" \
"ds_read_b64_tr_b16 v[170:171], v34 offset:9216\n" \
"v_mfma_f32_16x16x32_bf16 a[248:251], v[164:167], v[108:111], a[248:251]\n" \
"ds_read_b64_tr_b16 v[164:165], v34 offset:0\n" \
"ds_read_b64_tr_b16 v[166:167], v34 offset:1024\n" \
"v_mfma_f32_16x16x32_bf16 a[252:255], v[172:175], v[108:111], a[252:255]\n" \
"ds_read_b64_tr_b16 v[172:173], v35 offset:0\n" \
"ds_read_b64_tr_b16 v[174:175], v35 offset:1024\n" \
"v_mfma_f32_16x16x32_bf16 a[252:255], v[176:179], v[112:115], a[252:255]\n" \
"ds_read_b64_tr_b16 v[176:177], v35 offset:8192\n" \
"s_waitcnt lgkmcnt(15)\n" \
"s_nop 0\n" \
"v_smfmac_f32_16x16x64_bf16 v[248:251], v[140:143], v[84:91], v50\n" \
"v_mfma_f32_16x16x32_bf16 v[180:183], v[128:131], v[88:91], v[180:183]\n" \
"ds_read_b64_tr_b16 v[178:179], v35 offset:9216\n" \
"v_bfe_u32 v44, v20, 2, 5\n" \
"v_mfma_f32_16x16x32_bf16 v[180:183], v[124:127], v[84:87], v[180:183]\n" \
"buffer_load_dword v44, v44, s[80:83], s39 offen offset:0 sc1\n" \
"v_mfma_f32_16x16x32_bf16 v[252:255], v[84:87], v[132:135], v[252:255]\n" \
"ds_read_b128 v[84:87], v48 offset:0\n" \
"ds_read_b128 v[108:111], v48 offset:6144\n" \
"v_mfma_f32_16x16x32_bf16 v[252:255], v[88:91], v[136:139], v[252:255]\n" \
"ds_read_b128 v[88:91], v49 offset:0\n" \
"ds_read_b128 v[112:115], v49 offset:6144\n" \
"v_smfmac_f32_16x16x64_bf16 v[196:199], v[140:143], v[52:59], v50\n" \
"v_mfma_f32_16x16x32_bf16 v[188:191], v[128:131], v[56:59], v[188:191]\n" \
"v_cvt_pk_bf16_f32 v180, v180, v181\n" \
"v_cvt_pk_bf16_f32 v181, v182, v183\n" \
"v_mfma_f32_16x16x32_bf16 v[188:191], v[124:127], v[52:55], v[188:191]\n" \
"v_add_f32_e64 v249, v249, v251\n" \
"v_cvt_pk_bf16_f32 v182, v249, 0\n" \
"v_mfma_f32_16x16x32_bf16 v[184:187], v[116:119], v[52:55], v[184:187]\n" \
"v_mov_b32_e32 v183, 0x3f80\n" \
"v_add_f32_e64 v196, v196, v198\n" \
"v_mfma_f32_16x16x32_bf16 v[184:187], v[120:123], v[56:59], v[184:187]\n" \
"v_mfma_f32_16x16x16_bf16 v[248:251], v[182:183], v[46:47], 0\n" \
"v_add_f32_e64 v197, v197, v199\n" \
"v_cvt_pk_bf16_f32 v188, v188, v189\n" \
"v_mfma_f32_16x16x32_bf16 v[192:195], v[56:59], v[136:139], v[192:195]\n" \
"v_cvt_pk_bf16_f32 v189, v190, v191\n" \
"v_cvt_pk_bf16_f32 v190, v197, 0\n" \
"v_mfma_f32_16x16x32_bf16 v[192:195], v[52:55], v[132:135], v[192:195]\n" \
"v_mov_b32_e32 v191, 0x3f80\n" \
"v_cvt_pk_bf16_f32 v184, v184, v185\n" \
"v_smfmac_f32_16x16x64_bf16 v[212:215], v[140:143], v[60:67], v50\n" \
"v_mfma_f32_16x16x32_bf16 v[204:207], v[124:127], v[60:63], v[204:207]\n" \
"v_cvt_pk_bf16_f32 v185, v186, v187\n" \
"v_cndmask_b32_e64 v186, 0, v196, s[78:79]\n" \
"v_mfma_f32_16x16x32_bf16 v[204:207], v[128:131], v[64:67], v[204:207]\n" \
"v_mov_b32_e64 v187, 0\n" \
"v_cvt_pk_bf16_f32 v186, v186, 0\n" \
"v_mfma_f32_16x16x32_bf16 v[200:203], v[120:123], v[64:67], v[200:203]\n" \
"v_mfma_f32_16x16x32_bf16 v[200:203], v[116:119], v[60:63], v[200:203]\n" \
"v_add_f32_e32 v212, v212, v214\n" \
"v_add_f32_e32 v213, v213, v215\n" \
"v_mfma_f32_16x16x32_bf16 v[208:211], v[60:63], v[132:135], v[208:211]\n" \
"v_mfma_f32_16x16x32_bf16 v[208:211], v[64:67], v[136:139], v[208:211]\n" \
"v_cvt_pk_bf16_f32 v204, v204, v205\n" \
"v_cvt_pk_bf16_f32 v205, v206, v207\n" \
"v_smfmac_f32_16x16x64_bf16 v[228:231], v[140:143], v[68:75], v50\n" \
"v_mfma_f32_16x16x32_bf16 v[220:223], v[128:131], v[72:75], v[220:223]\n" \
"v_cvt_pk_bf16_f32 v200, v200, v201\n" \
"v_cvt_pk_bf16_f32 v201, v202, v203\n" \
"v_mfma_f32_16x16x32_bf16 v[220:223], v[124:127], v[68:71], v[220:223]\n" \
"v_cndmask_b32_e64 v202, 0, v212, s[78:79]\n" \
"v_cvt_pk_bf16_f32 v206, v213, 0\n" \
"v_mfma_f32_16x16x32_bf16 v[216:219], v[116:119], v[68:71], v[216:219]\n" \
"v_mov_b32_e32 v207, 0x3f80\n" \
"v_add_f32_e64 v228, v228, v230\n" \
"v_mfma_f32_16x16x32_bf16 v[216:219], v[120:123], v[72:75], v[216:219]\n" \
"v_add_f32_e32 v229, v229, v231\n" \
"v_mov_b32_e32 v203, 0\n" \
"v_mfma_f32_16x16x32_bf16 v[224:227], v[72:75], v[136:139], v[224:227]\n" \
"v_cvt_pk_bf16_f32 v220, v220, v221\n" \
"v_cvt_pk_bf16_f32 v221, v222, v223\n" \
"v_mfma_f32_16x16x32_bf16 v[224:227], v[68:71], v[132:135], v[224:227]\n" \
"v_cvt_pk_bf16_f32 v202, v202, 0\n" \
"v_cvt_pk_bf16_f32 v222, v229, 0\n" \
"v_smfmac_f32_16x16x64_bf16 v[244:247], v[140:143], v[76:83], v50\n" \
"v_mfma_f32_16x16x32_bf16 v[236:239], v[124:127], v[76:79], v[236:239]\n" \
"v_cvt_pk_bf16_f32 v216, v216, v217\n" \
"v_cvt_pk_bf16_f32 v217, v218, v219\n" \
"v_mfma_f32_16x16x32_bf16 v[236:239], v[128:131], v[80:83], v[236:239]\n" \
"v_cndmask_b32_e64 v218, 0, v228, s[78:79]\n" \
"v_cvt_pk_bf16_f32 v126, v192, v193\n" \
"v_mfma_f32_16x16x32_bf16 v[232:235], v[120:123], v[80:83], v[232:235]\n" \
"v_cvt_pk_bf16_f32 v127, v194, v195\n" \
"v_add_f32_e64 v244, v244, v246\n" \
"v_mfma_f32_16x16x32_bf16 v[232:235], v[116:119], v[76:79], v[232:235]\n" \
"v_add_f32_e64 v245, v245, v247\n" \
"v_cvt_pk_bf16_f32 v128, v208, v209\n" \
"v_mfma_f32_16x16x32_bf16 v[240:243], v[76:79], v[132:135], v[240:243]\n" \
"v_cvt_pk_bf16_f32 v236, v236, v237\n" \
"v_cvt_pk_bf16_f32 v237, v238, v239\n" \
"v_mfma_f32_16x16x32_bf16 v[240:243], v[80:83], v[136:139], v[240:243]\n" \
"v_cvt_pk_bf16_f32 v129, v210, v211\n" \
"v_mov_b32_e32 v223, 0x3f80\n" \
"s_waitcnt lgkmcnt(0)\n" \
"s_barrier\n" \
"v_mfma_f32_16x16x32_bf16 a[128:131], v[152:155], v[56:59], a[128:131]\n" \
"v_cvt_pk_bf16_f32 v232, v232, v233\n" \
"v_cvt_pk_bf16_f32 v233, v234, v235\n" \
"v_mfma_f32_16x16x32_bf16 a[128:131], v[148:151], v[52:55], a[128:131]\n" \
"v_cndmask_b32_e64 v234, 0, v244, s[78:79]\n" \
"v_mov_b32_e64 v219, 0\n" \
"v_mfma_f32_16x16x32_bf16 a[132:135], v[156:159], v[52:55], a[132:135]\n" \
"v_cvt_pk_bf16_f32 v218, v218, 0\n" \
"v_cvt_pk_bf16_f32 v130, v224, v225\n" \
"v_mfma_f32_16x16x32_bf16 a[132:135], v[160:163], v[56:59], a[132:135]\n" \
"v_cvt_pk_bf16_f32 v131, v226, v227\n" \
"v_cvt_pk_bf16_f32 v238, v245, 0\n" \
"v_mfma_f32_16x16x32_bf16 a[136:139], v[168:171], v[56:59], a[136:139]\n" \
"v_mov_b32_e32 v239, 0x3f80\n" \
"v_mov_b32_e64 v235, 0\n" \
"v_mfma_f32_16x16x32_bf16 a[136:139], v[164:167], v[52:55], a[136:139]\n" \
"v_cvt_pk_bf16_f32 v234, v234, 0\n" \
"v_cvt_pk_bf16_f32 v132, v240, v241\n" \
"v_mfma_f32_16x16x32_bf16 a[140:143], v[172:175], v[52:55], a[140:143]\n" \
"v_cvt_pk_bf16_f32 v133, v242, v243\n" \
"v_cvt_pk_bf16_f32 v134, v248, v249\n" \
"v_mfma_f32_16x16x32_bf16 a[140:143], v[176:179], v[56:59], a[140:143]\n" \
"v_mfma_f32_16x16x32_bf16 v[116:119], v[180:183], v[184:187], 0\n" \
"v_mfma_f32_16x16x32_bf16 v[192:195], v[188:191], v[184:187], 0\n" \
"v_mfma_f32_16x16x32_bf16 v[208:211], v[204:207], v[200:203], 0\n" \
"v_mfma_f32_16x16x32_bf16 v[120:123], v[188:191], v[200:203], 0\n" \
"s_waitcnt vmcnt(0)\n" \
"v_cmp_ne_u32_e32 vcc, %[tag], v44\n" \
"v_mfma_f32_16x16x32_bf16 a[160:163], v[152:155], v[64:67], a[160:163]\n" \
"v_mfma_f32_16x16x32_bf16 v[224:227], v[220:223], v[216:219], 0\n" \
"v_mfma_f32_16x16x32_bf16 v[36:39], v[204:207], v[216:219], 0\n" \
"v_mfma_f32_16x16x32_bf16 v[240:243], v[236:239], v[232:235], 0\n" \
"v_mfma_f32_16x16x32_bf16 v[48:51], v[220:223], v[232:235], 0\n" \
"s_cbranch_vccnz L_pa_%=\n" \
"L_ra_%=:\n" \
"v_cndmask_b32_e64 v116, v116, v117, s[64:65]\n" \
"buffer_load_dwordx4 v20, s[80:83], s73 offen offset:0 sc1 lds\n" \
"v_mfma_f32_16x16x32_bf16 a[160:163], v[148:151], v[60:63], a[160:163]\n" \
"v_cndmask_b32_e64 v118, v118, v119, s[64:65]\n" \
"v_cndmask_b32_e64 v192, v192, v193, s[64:65]\n" \
"v_mfma_f32_16x16x32_bf16 a[164:167], v[156:159], v[60:63], a[164:167]\n" \
"v_add_f32_e32 v116, s49, v116\n" \
"v_cndmask_b32_e64 v194, v194, v195, s[64:65]\n" \
"v_mfma_f32_16x16x32_bf16 a[164:167], v[160:163], v[64:67], a[164:167]\n" \
"v_add_f32_e64 v118, s49, v118\n" \
"v_cndmask_b32_e64 v208, v208, v209, s[64:65]\n" \
"v_mfma_f32_16x16x32_bf16 a[168:171], v[168:171], v[64:67], a[168:171]\n" \
"buffer_load_dwordx4 v20, s[80:83], s73 offen offset:1024 sc1 lds\n" \
"v_mfma_f32_16x16x32_bf16 a[168:171], v[164:167], v[60:63], a[168:171]\n" \
"v_cndmask_b32_e64 v120, v120, v121, s[64:65]\n" \
"v_cndmask_b32_e64 v210, v210, v211, s[64:65]\n" \
"v_mfma_f32_16x16x32_bf16 a[172:175], v[172:175], v[60:63], a[172:175]\n" \
"v_cndmask_b32_e64 v122, v122, v123, s[64:65]\n" \
"v_cndmask_b32_e64 v224, v224, v225, s[64:65]\n" \
"v_mfma_f32_16x16x32_bf16 a[172:175], v[176:179], v[64:67], a[172:175]\n" \
"v_cndmask_b32_e64 v36, v36, v37, s[64:65]\n" \
"v_cndmask_b32_e64 v226, v226, v227, s[64:65]\n" \
"v_mfma_f32_16x16x32_bf16 a[192:195], v[152:155], v[72:75], a[192:195]\n" \
"buffer_load_dwordx4 v20, s[80:83], s73 offen offset:2048 sc1 lds\n" \
"v_mfma_f32_16x16x32_bf16 a[192:195], v[148:151], v[68:71], a[192:195]\n" \
"v_cndmask_b32_e64 v38, v38, v39, s[64:65]\n" \
"v_cndmask_b32_e64 v240, v240, v241, s[64:65]\n" \
"v_mfma_f32_16x16x32_bf16 a[196:199], v[156:159], v[68:71], a[196:199]\n" \
"v_cndmask_b32_e64 v48, v48, v49, s[64:65]\n" \
"v_cndmask_b32_e64 v242, v242, v243, s[64:65]\n" \
"v_mfma_f32_16x16x32_bf16 a[196:199], v[160:163], v[72:75], a[196:199]\n" \
"v_cndmask_b32_e64 v50, v50, v51, s[64:65]\n" \
"v_cndmask_b32_e64 v192, v116, v192, s[66:67]\n" \
"v_mfma_f32_16x16x32_bf16 a[200:203], v[168:171], v[72:75], a[200:203]\n" \
"buffer_load_dwordx4 v20, s[80:83], s73 offen offset:3072 sc1 lds\n" \
"v_mfma_f32_16x16x32_bf16 a[200:203], v[164:167], v[68:71], a[200:203]\n" \
"s_sub_u32 m0, %[mB], s53\n" \
"s_nop 0\n" \
"v_cndmask_b32_e64 v194, v118, v194, s[68:69]\n" \
"v_mfma_f32_16x16x32_bf16 a[204:207], v[172:175], v[68:71], a[204:207]\n" \
"v_cndmask_b32_e64 v208, v120, v208, s[66:67]\n" \
"v_cndmask_b32_e64 v210, v122, v210, s[68:69]\n" \
"v_mfma_f32_16x16x32_bf16 a[204:207], v[176:179], v[72:75], a[204:207]\n" \
"v_cndmask_b32_e64 v224, v36, v224, s[66:67]\n" \
"v_cndmask_b32_e64 v226, v38, v226, s[68:69]\n" \
"v_mfma_f32_16x16x32_bf16 a[224:227], v[152:155], v[80:83], a[224:227]\n" \
"buffer_load_dwordx4 v20, s[80:83], s74 offen offset:0 sc1 lds\n" \
"v_mfma_f32_16x16x32_bf16 a[224:227], v[148:151], v[76:79], a[224:227]\n" \
"v_cndmask_b32_e64 v240, v48, v240, s[66:67]\n" \
"v_cndmask_b32_e64 v242, v50, v242, s[68:69]\n" \
"v_mfma_f32_16x16x32_bf16 a[228:231], v[156:159], v[76:79], a[228:231]\n" \
"v_max_f32_e32 v193, v192, v194\n" \
"v_max_f32_e32 v209, v208, v210\n" \
"v_mfma_f32_16x16x32_bf16 a[228:231], v[160:163], v[80:83], a[228:231]\n" \
"v_max_f32_e32 v225, v224, v226\n" \
"v_max_f32_e32 v241, v240, v242\n" \
"v_mfma_f32_16x16x32_bf16 a[232:235], v[168:171], v[80:83], a[232:235]\n" \
"buffer_load_dwordx4 v20, s[80:83], s74 offen offset:1024 sc1 lds\n" \
"v_mfma_f32_16x16x32_bf16 a[232:235], v[164:167], v[76:79], a[232:235]\n" \
"v_permlane32_swap_b32_e32 v193, v225\n" \
"v_permlane32_swap_b32_e32 v209, v241\n" \
"v_mfma_f32_16x16x32_bf16 a[236:239], v[172:175], v[76:79], a[236:239]\n" \
"v_max_f32_e32 v193, v193, v225\n" \
"v_max_f32_e32 v209, v209, v241\n" \
"v_permlane32_swap_b32_e32 v196, v228\n" \
"v_permlane32_swap_b32_e32 v212, v244\n" \
"buffer_load_dwordx4 v20, s[80:83], s74 offen offset:2048 sc1 lds\n" \
"v_permlane16_swap_b32_e32 v193, v209\n" \
"v_permlane16_swap_b32_e32 v228, v244\n" \
"v_mfma_f32_16x16x32_bf16 a[236:239], v[176:179], v[80:83], a[236:239]\n" \
"v_max_f32_e32 v193, v193, v209\n" \
"v_mul_f32_e32 v193, s72, v193\n" \
"v_mfma_f32_16x16x32_bf16 a[144:147], v[152:155], v[88:91], a[144:147]\n" \
"v_mul_f32_e32 v45, 0xbfb8aa3b, v228\n" \
"v_mov_b32_e64 v209, v193\n" \
"v_mfma_f32_16x16x16_bf16 v[196:199], v[190:191], v[46:47], 0\n" \
"buffer_load_dwordx4 v20, s[80:83], s74 offen offset:3072 sc1 lds\n" \
"v_mfma_f32_16x16x32_bf16 a[144:147], v[148:151], v[84:87], a[144:147]\n" \
"s_sub_u32 m0, %[mB], 0x4000\n" \
"v_cvt_pk_bf16_f32 v135, v250, v251\n" \
"v_mfma_f32_16x16x16_bf16 v[212:215], v[206:207], v[46:47], 0\n" \
"v_mfma_f32_16x16x16_bf16 v[228:231], v[222:223], v[46:47], 0\n" \
"v_permlane16_swap_b32_e32 v193, v209\n" \
"v_mfma_f32_16x16x16_bf16 v[244:247], v[238:239], v[46:47], 0\n" \
"buffer_load_dwordx4 v20, s[80:83], s75 offen offset:0 sc1 lds\n" \
"v_mfma_f32_16x16x32_bf16 a[148:151], v[156:159], v[84:87], a[148:151]\n" \
"v_mov_b32_e32 v225, v193\n" \
"v_mov_b32_e64 v241, v209\n" \
"v_mfma_f32_16x16x32_bf16 a[148:151], v[160:163], v[88:91], a[148:151]\n" \
"v_permlane32_swap_b32_e32 v193, v225\n" \
"v_permlane32_swap_b32_e32 v209, v241\n" \
"v_mfma_f32_16x16x32_bf16 a[152:155], v[168:171], v[88:91], a[152:155]\n" \
"v_fma_f32 v192, v192, s72, -v193\n" \
"v_fma_f32 v194, v194, s72, -v193\n" \
"buffer_load_dwordx4 v20, s[80:83], s75 offen offset:1024 sc1 lds\n" \
"v_mfma_f32_16x16x32_bf16 a[152:155], v[164:167], v[84:87], a[152:155]\n" \
"v_fma_f32 v208, v208, s72, -v209\n" \
"v_fma_f32 v210, v210, s72, -v209\n" \
"v_exp_f32_e32 v192, v192\n" \
"v_exp_f32_e32 v194, v194\n" \
"v_exp_f32_e32 v208, v208\n" \
"v_exp_f32_e32 v210, v210\n" \
"v_mfma_f32_16x16x32_bf16 a[156:159], v[172:175], v[84:87], a[156:159]\n" \
"buffer_load_dwordx4 v20, s[80:83], s75 offen offset:2048 sc1 lds\n" \
"v_mfma_f32_16x16x32_bf16 a[156:159], v[176:179], v[88:91], a[156:159]\n" \
"v_fma_f32 v224, v224, s72, -v225\n" \
"v_fma_f32 v226, v226, s72, -v225\n" \
"v_exp_f32_e32 v224, v224\n" \
"v_exp_f32_e32 v226, v226\n" \
"v_mfma_f32_16x16x32_bf16 a[176:179], v[152:155], v[96:99], a[176:179]\n" \
"v_fma_f32 v240, v240, s72, -v241\n" \
"v_fma_f32 v242, v242, s72, -v241\n" \
"v_mfma_f32_16x16x32_bf16 a[176:179], v[148:151], v[92:95], a[176:179]\n" \
"buffer_load_dwordx4 v20, s[80:83], s75 offen offset:3072 sc1 lds\n" \
"v_mfma_f32_16x16x32_bf16 a[180:183], v[156:159], v[92:95], a[180:183]\n" \
"s_mov_b32 m0, s55\n" \
"v_cvt_pk_bf16_f32 v192, v192, v194\n" \
"v_mfma_f32_16x16x32_bf16 a[180:183], v[160:163], v[96:99], a[180:183]\n" \
"v_cvt_pk_bf16_f32 v208, v208, v210\n" \
"v_cvt_pk_bf16_f32 v136, v196, v197\n" \
"v_exp_f32_e32 v240, v240\n" \
"v_exp_f32_e64 v242, v242\n" \
"v_mfma_f32_16x16x32_bf16 a[184:187], v[168:171], v[96:99], a[184:187]\n" \
"buffer_load_dwordx4 v20, s[80:83], s76 offen offset:0 sc1 lds\n" \
"v_mfma_f32_16x16x32_bf16 a[184:187], v[164:167], v[92:95], a[184:187]\n" \
"L_pf9:\n" \
"s_cbranch_execz L_pf13\n" \
"s_nop 0\n" \
"v_cvt_pk_bf16_f32 v137, v198, v199\n" \
"v_cvt_pk_bf16_f32 v138, v212, v213\n" \
"v_mfma_f32_16x16x32_bf16 a[188:191], v[172:175], v[92:95], a[188:191]\n" \
"v_cvt_pk_bf16_f32 v139, v214, v215\n" \
"v_perm_b32 v184, v192, v192, v40\n" \
"v_mfma_f32_16x16x32_bf16 a[188:191], v[176:179], v[96:99], a[188:191]\n" \
"v_perm_b32 v185, v192, v192, v41\n" \
"v_perm_b32 v186, v192, v192, v42\n" \
"v_perm_b32 v187, v192, v192, v43\n" \
"buffer_load_dwordx4 v20, s[80:83], s76 offen offset:1024 sc1 lds\n" \
"v_perm_b32 v200, v208, v208, v40\n" \
"v_perm_b32 v201, v208, v208, v41\n" \
"v_perm_b32 v202, v208, v208, v42\n" \
"v_perm_b32 v203, v208, v208, v43\n" \
"v_cvt_pk_bf16_f32 v224, v224, v226\n" \
"v_cvt_pk_bf16_f32 v140, v228, v229\n" \
"v_cvt_pk_bf16_f32 v141, v230, v231\n" \
"v_mfma_f32_16x16x32_bf16 v[116:119], v[134:137], v[184:187], 0\n" \
"v_mfma_f32_16x16x32_bf16 v[120:123], v[136:139], v[200:203], 0\n" \
"v_perm_b32 v216, v224, v224, v40\n" \
"v_perm_b32 v217, v224, v224, v41\n" \
"v_perm_b32 v218, v224, v224, v42\n" \
"v_perm_b32 v219, v224, v224, v43\n" \
"v_cvt_pk_bf16_f32 v240, v240, v242\n" \
"v_cvt_pk_bf16_f32 v142, v244, v245\n" \
"v_cvt_pk_bf16_f32 v143, v246, v247\n" \
"v_mfma_f32_16x16x32_bf16 v[36:39], v[138:141], v[216:219], 0\n" \
"v_perm_b32 v232, v240, v240, v40\n" \
"v_perm_b32 v233, v240, v240, v41\n" \
"v_perm_b32 v234, v240, v240, v42\n" \
"v_perm_b32 v235, v240, v240, v43\n" \
"v_exp_f32_e64 v45, v45\n" \
"v_mov_b32_dpp v118, v122 quad_perm:[0,1,2,3] row_mask:0x2 bank_mask:0xf\n" \
"v_mfma_f32_16x16x32_bf16 v[48:51], v[140:143], v[232:235], 0\n" \
"v_cvt_pk_bf16_f32 v124, v252, v253\n" \
"v_cvt_pk_bf16_f32 v125, v254, v255\n" \
"v_mov_b32_dpp v118, v38 quad_perm:[0,1,2,3] row_mask:0x4 bank_mask:0xf\n" \
"v_add_f32_e64 v45, 1.0, v45\n" \
"v_mfma_f32_16x16x32_bf16 v[192:195], v[124:127], v[184:187], 0\n" \
"v_mfma_f32_16x16x32_bf16 v[224:227], v[128:131], v[216:219], 0\n" \
"v_rcp_f32_e64 v45, v45\n" \
"v_mov_b32_dpp v116, v120 quad_perm:[0,1,2,3] row_mask:0x2 bank_mask:0xf\n" \
"v_mov_b32_dpp v118, v50 quad_perm:[0,1,2,3] row_mask:0x8 bank_mask:0xf\n" \
"v_mov_b32_dpp v117, v121 quad_perm:[0,1,2,3] row_mask:0x2 bank_mask:0xf\n" \
"v_rcp_f32_e64 v118, v118\n" \
"v_mfma_f32_16x16x32_bf16 v[208:211], v[126:129], v[200:203], 0\n" \
"v_mfma_f32_16x16x32_bf16 v[240:243], v[130:133], v[232:235], 0\n" \
"v_mov_b32_dpp v116, v36 quad_perm:[0,1,2,3] row_mask:0x4 bank_mask:0xf\n" \
"v_mov_b32_dpp v117, v37 quad_perm:[0,1,2,3] row_mask:0x4 bank_mask:0xf\n" \
"v_mul_f32_e64 v118, v45, v118\n" \
"v_mov_b32_dpp v116, v48 quad_perm:[0,1,2,3] row_mask:0x8 bank_mask:0xf\n" \
"v_mov_b32_dpp v117, v49 quad_perm:[0,1,2,3] row_mask:0x8 bank_mask:0xf\n" \
"v_mul_f32_e32 v116, v118, v116\n" \
"v_mul_f32_e32 v117, v118, v117\n" \
"buffer_store_dwordx4 a[12:15], v20, s[80:83], s89 offen offset:1024 sc1\n" \
"s_add_u32 s89, s45, 0x0\n" \
"v_mov_b32_e32 v122, v118\n" \
"v_cvt_pk_bf16_f32 v116, v116, v117\n" \
"v_accvgpr_read_b32 v52, a128\n" \
"v_permlane16_swap_b32_e32 v118, v122\n" \
"v_mov_b32_e32 v38, v118\n" \
"v_mov_b32_e64 v50, v122\n" \
"v_accvgpr_read_b32 v53, a129\n" \
"v_permlane32_swap_b32_e32 v118, v38\n" \
"v_permlane32_swap_b32_e32 v122, v50\n" \
"v_mul_f32_e32 v192, v118, v192\n" \
"v_mul_f32_e32 v193, v118, v193\n" \
"v_mul_f32_e32 v224, v38, v224\n" \
"v_mul_f32_e32 v225, v38, v225\n" \
"v_mul_f32_e32 v194, v118, v194\n" \
"v_mul_f32_e32 v195, v118, v195\n" \
"v_cvt_pk_bf16_f32 v192, v192, v193\n" \
"v_mul_f32_e32 v226, v38, v226\n" \
"v_mul_f32_e32 v227, v38, v227\n" \
"v_cvt_pk_bf16_f32 v224, v224, v225\n" \
"v_cvt_pk_bf16_f32 v193, v194, v195\n" \
"v_mul_f32_e32 v208, v122, v208\n" \
"v_mul_f32_e32 v209, v122, v209\n" \
"v_mul_f32_e32 v210, v122, v210\n" \
"v_mul_f32_e32 v211, v122, v211\n" \
"v_cvt_pk_bf16_f32 v225, v226, v227\n" \
"v_mul_f32_e32 v240, v50, v240\n" \
"v_mul_f32_e32 v241, v50, v241\n" \
"v_mul_f32_e32 v242, v50, v242\n" \
"v_mul_f32_e32 v243, v50, v243\n" \
"v_cvt_pk_bf16_f32 v194, v208, v209\n" \
"v_cvt_pk_bf16_f32 v195, v210, v211\n" \
"v_cvt_pk_bf16_f32 v226, v240, v241\n" \
"v_cvt_pk_bf16_f32 v227, v242, v243\n" \
"v_permlane32_swap_b32_e32 v192, v194\n" \
"v_permlane32_swap_b32_e32 v193, v195\n" \
"v_permlane32_swap_b32_e32 v224, v226\n" \
"v_permlane32_swap_b32_e32 v225, v227\n" \
"v_accvgpr_read_b32 v54, a130\n" \
"v_accvgpr_read_b32 v55, a131\n" \
"v_pk_mul_f32 v[52:53], v[52:53], s[70:71]\n" \
"v_accvgpr_read_b32 v56, a132\n" \
"v_accvgpr_read_b32 v57, a133\n" \
"v_accvgpr_read_b32 v58, a134\n" \
"v_accvgpr_read_b32 v59, a135\n" \
"v_pk_mul_f32 v[54:55], v[54:55], s[70:71]\n" \
"v_cvt_pk_bf16_f32 v52, v52, v53\n" \
"v_pk_mul_f32 v[56:57], v[56:57], s[70:71]\n" \
"v_pk_mul_f32 v[58:59], v[58:59], s[70:71]\n" \
"v_cvt_pk_bf16_f32 v53, v54, v55\n" \
"v_cvt_pk_bf16_f32 v54, v56, v57\n" \
"v_cvt_pk_bf16_f32 v55, v58, v59\n" \
"ds_write_b128 v27, v[52:55] offset:0\n" \
"buffer_store_dwordx4 a[40:43], v20, s[80:83], s88 offen offset:0 sc1\n" \
"s_add_u32 s88, s45, 0x0\n" \
"s_nop 0\n" \
"buffer_store_dword v116, v25, s[80:83], s51 offen sc1\n" \
"buffer_store_dwordx4 v[192:195], v24, s[80:83], s85 offen sc1\n" \
"buffer_store_dwordx4 v[224:227], v24, s[80:83], s87 offen sc1\n" \
"v_accvgpr_read_b32 v76, a224\n" \
"v_accvgpr_read_b32 v77, a225\n" \
"v_accvgpr_read_b32 v78, a226\n" \
"v_accvgpr_read_b32 v79, a227\n" \
"v_pk_mul_f32 v[76:77], v[76:77], s[70:71]\n" \
"v_accvgpr_read_b32 v52, a136\n" \
"v_accvgpr_read_b32 v53, a137\n" \
"v_accvgpr_read_b32 v80, a228\n" \
"v_accvgpr_read_b32 v81, a229\n" \
"v_accvgpr_read_b32 v82, a230\n" \
"v_accvgpr_read_b32 v83, a231\n" \
"v_pk_mul_f32 v[78:79], v[78:79], s[70:71]\n" \
"v_cvt_pk_bf16_f32 v76, v76, v77\n" \
"v_accvgpr_read_b32 v54, a138\n" \
"v_accvgpr_read_b32 v55, a139\n" \
"v_pk_mul_f32 v[52:53], v[52:53], s[70:71]\n" \
"v_pk_mul_f32 v[80:81], v[80:81], s[70:71]\n" \
"v_pk_mul_f32 v[82:83], v[82:83], s[70:71]\n" \
"v_cvt_pk_bf16_f32 v77, v78, v79\n" \
"v_accvgpr_read_b32 v56, a140\n" \
"v_accvgpr_read_b32 v57, a141\n" \
"v_accvgpr_read_b32 v58, a142\n" \
"v_accvgpr_read_b32 v59, a143\n" \
"v_pk_mul_f32 v[54:55], v[54:55], s[70:71]\n" \
"v_cvt_pk_bf16_f32 v52, v52, v53\n" \
"v_cvt_pk_bf16_f32 v78, v80, v81\n" \
"v_cvt_pk_bf16_f32 v79, v82, v83\n" \
"ds_write_b128 v27, v[76:79] offset:12288\n" \
"v_pk_mul_f32 v[56:57], v[56:57], s[70:71]\n" \
"v_pk_mul_f32 v[58:59], v[58:59], s[70:71]\n" \
"v_cvt_pk_bf16_f32 v53, v54, v55\n" \
"v_cvt_pk_bf16_f32 v54, v56, v57\n" \
"v_cvt_pk_bf16_f32 v55, v58, v59\n" \
"ds_write_b128 v27, v[52:55] offset:1024\n" \
"v_accvgpr_read_b32 v76, a208\n" \
"v_accvgpr_read_b32 v77, a209\n" \
"v_accvgpr_read_b32 v78, a210\n" \
"v_accvgpr_read_b32 v79, a211\n" \
"v_accvgpr_read_b32 v52, a168\n" \
"v_accvgpr_read_b32 v53, a169\n" \
"v_mfma_f32_16x16x32_bf16 v[76:79], v[152:155], v[104:107], v[76:79]\n" \
"v_accvgpr_read_b32 v54, a170\n" \
"v_accvgpr_read_b32 v55, a171\n" \
"v_mfma_f32_16x16x32_bf16 v[76:79], v[148:151], v[100:103], v[76:79]\n" \
"v_accvgpr_read_b32 v56, a172\n" \
"v_accvgpr_read_b32 v57, a173\n" \
"v_accvgpr_read_b32 v58, a174\n" \
"v_accvgpr_read_b32 v59, a175\n" \
"v_pk_mul_f32 v[52:53], v[52:53], s[70:71]\n" \
"v_pk_mul_f32 v[54:55], v[54:55], s[70:71]\n" \
"v_cvt_pk_bf16_f32 v52, v52, v53\n" \
"v_pk_mul_f32 v[56:57], v[56:57], s[70:71]\n" \
"v_pk_mul_f32 v[58:59], v[58:59], s[70:71]\n" \
"buffer_store_dwordx4 a[44:47], v20, s[80:83], s89 offen offset:1024 sc1\n" \
"s_add_u32 s89, s45, 0x0\n" \
"s_nop 0\n" \
"v_cvt_pk_bf16_f32 v53, v54, v55\n" \
"v_cvt_pk_bf16_f32 v54, v56, v57\n" \
"v_cvt_pk_bf16_f32 v55, v58, v59\n" \
"ds_write_b128 v27, v[52:55] offset:5120\n" \
"v_accvgpr_read_b32 v80, a212\n" \
"v_accvgpr_read_b32 v81, a213\n" \
"v_accvgpr_read_b32 v82, a214\n" \
"v_accvgpr_read_b32 v83, a215\n" \
"v_accvgpr_read_b32 v52, a232\n" \
"v_accvgpr_read_b32 v53, a233\n" \
"v_mfma_f32_16x16x32_bf16 v[80:83], v[156:159], v[100:103], v[80:83]\n" \
"v_accvgpr_read_b32 v54, a234\n" \
"v_accvgpr_read_b32 v55, a235\n" \
"v_mfma_f32_16x16x32_bf16 v[80:83], v[160:163], v[104:107], v[80:83]\n" \
"v_accvgpr_read_b32 v56, a236\n" \
"v_accvgpr_read_b32 v57, a237\n" \
"v_accvgpr_read_b32 v58, a238\n" \
"v_accvgpr_read_b32 v59, a239\n" \
"v_pk_mul_f32 v[52:53], v[52:53], s[70:71]\n" \
"v_pk_mul_f32 v[54:55], v[54:55], s[70:71]\n" \
"v_cvt_pk_bf16_f32 v52, v52, v53\n" \
"v_pk_mul_f32 v[56:57], v[56:57], s[70:71]\n" \
"v_pk_mul_f32 v[58:59], v[58:59], s[70:71]\n" \
"v_cvt_pk_bf16_f32 v53, v54, v55\n" \
"v_cvt_pk_bf16_f32 v54, v56, v57\n" \
"v_cvt_pk_bf16_f32 v55, v58, v59\n" \
"ds_write_b128 v27, v[52:55] offset:13312\n" \
"v_accvgpr_read_b32 v52, a216\n" \
"v_accvgpr_read_b32 v53, a217\n" \
"v_accvgpr_read_b32 v54, a218\n" \
"v_accvgpr_read_b32 v55, a219\n" \
"v_accvgpr_read_b32 v60, a160\n" \
"v_accvgpr_read_b32 v61, a161\n" \
"v_mfma_f32_16x16x32_bf16 v[52:55], v[168:171], v[104:107], v[52:55]\n" \
"v_accvgpr_read_b32 v62, a162\n" \
"v_accvgpr_read_b32 v63, a163\n" \
"v_mfma_f32_16x16x32_bf16 v[52:55], v[164:167], v[100:103], v[52:55]\n" \
"v_accvgpr_read_b32 v56, a220\n" \
"v_accvgpr_read_b32 v57, a221\n" \
"v_accvgpr_read_b32 v58, a222\n" \
"v_accvgpr_read_b32 v59, a223\n" \
"v_pk_mul_f32 v[60:61], v[60:61], s[70:71]\n" \
"v_accvgpr_read_b32 v64, a164\n" \
"v_mfma_f32_16x16x32_bf16 v[56:59], v[172:175], v[100:103], v[56:59]\n" \
"v_accvgpr_read_b32 v65, a165\n" \
"v_accvgpr_read_b32 v66, a166\n" \
"v_mfma_f32_16x16x32_bf16 v[56:59], v[176:179], v[104:107], v[56:59]\n" \
"v_accvgpr_read_b32 v67, a167\n" \
"buffer_store_dwordx4 a[72:75], v20, s[80:83], s88 offen offset:2048 sc1\n" \
"s_add_u32 s88, s45, 0x1000\n" \
"v_pk_mul_f32 v[62:63], v[62:63], s[70:71]\n" \
"v_cvt_pk_bf16_f32 v60, v60, v61\n" \
"v_pk_mul_f32 v[64:65], v[64:65], s[70:71]\n" \
"v_pk_mul_f32 v[66:67], v[66:67], s[70:71]\n" \
"v_cvt_pk_bf16_f32 v61, v62, v63\n" \
"v_cvt_pk_bf16_f32 v62, v64, v65\n" \
"v_cvt_pk_bf16_f32 v63, v66, v67\n" \
"ds_write_b128 v27, v[60:63] offset:4096\n" \
"v_accvgpr_read_b32 v68, a192\n" \
"v_accvgpr_read_b32 v69, a193\n" \
"v_accvgpr_read_b32 v60, a200\n" \
"v_accvgpr_read_b32 v61, a201\n" \
"v_accvgpr_read_b32 v70, a194\n" \
"v_accvgpr_read_b32 v71, a195\n" \
"v_pk_mul_f32 v[68:69], v[68:69], s[70:71]\n" \
"v_accvgpr_read_b32 v62, a202\n" \
"v_accvgpr_read_b32 v63, a203\n" \
"v_pk_mul_f32 v[60:61], v[60:61], s[70:71]\n" \
"v_accvgpr_read_b32 v72, a196\n" \
"v_accvgpr_read_b32 v73, a197\n" \
"v_accvgpr_read_b32 v74, a198\n" \
"v_accvgpr_read_b32 v75, a199\n" \
"v_pk_mul_f32 v[70:71], v[70:71], s[70:71]\n" \
"v_cvt_pk_bf16_f32 v68, v68, v69\n" \
"v_accvgpr_read_b32 v64, a204\n" \
"v_accvgpr_read_b32 v65, a205\n" \
"v_accvgpr_read_b32 v66, a206\n" \
"v_accvgpr_read_b32 v67, a207\n" \
"v_pk_mul_f32 v[62:63], v[62:63], s[70:71]\n" \
"v_cvt_pk_bf16_f32 v60, v60, v61\n" \
"v_accvgpr_read_b32 v100, a240\n" \
"v_accvgpr_read_b32 v101, a241\n" \
"v_accvgpr_read_b32 v102, a242\n" \
"v_accvgpr_read_b32 v103, a243\n" \
"v_pk_mul_f32 v[72:73], v[72:73], s[70:71]\n" \
"v_pk_mul_f32 v[74:75], v[74:75], s[70:71]\n" \
"v_mfma_f32_16x16x32_bf16 v[100:103], v[152:155], v[112:115], v[100:103]\n" \
"v_cvt_pk_bf16_f32 v69, v70, v71\n" \
"v_cvt_pk_bf16_f32 v61, v62, v63\n" \
"v_mfma_f32_16x16x32_bf16 v[100:103], v[148:151], v[108:111], v[100:103]\n" \
"v_cvt_pk_bf16_f32 v70, v72, v73\n" \
"v_cvt_pk_bf16_f32 v71, v74, v75\n" \
"ds_write_b128 v27, v[68:71] offset:8192\n" \
"v_pk_mul_f32 v[64:65], v[64:65], s[70:71]\n" \
"v_pk_mul_f32 v[66:67], v[66:67], s[70:71]\n" \
"v_cvt_pk_bf16_f32 v62, v64, v65\n" \
"v_cvt_pk_bf16_f32 v63, v66, v67\n" \
"ds_write_b128 v27, v[60:63] offset:9216\n" \
"v_accvgpr_read_b32 v84, a144\n" \
"buffer_store_dwordx4 a[76:79], v20, s[80:83], s89 offen offset:3072 sc1\n" \
"s_add_u32 s89, s45, 0x1000\n" \
"v_accvgpr_read_b32 v85, a145\n" \
"v_accvgpr_read_b32 v68, a152\n" \
"v_accvgpr_read_b32 v69, a153\n" \
"v_accvgpr_read_b32 v92, a176\n" \
"v_accvgpr_read_b32 v93, a177\n" \
"v_accvgpr_read_b32 v60, a184\n" \
"v_accvgpr_read_b32 v61, a185\n" \
"v_accvgpr_read_b32 v86, a146\n" \
"v_accvgpr_read_b32 v87, a147\n" \
"v_pk_mul_f32 v[84:85], v[84:85], s[70:71]\n" \
"v_accvgpr_read_b32 v70, a154\n" \
"v_accvgpr_read_b32 v71, a155\n" \
"v_pk_mul_f32 v[68:69], v[68:69], s[70:71]\n" \
"v_accvgpr_read_b32 v94, a178\n" \
"v_accvgpr_read_b32 v95, a179\n" \
"v_pk_mul_f32 v[92:93], v[92:93], s[70:71]\n" \
"v_accvgpr_read_b32 v62, a186\n" \
"v_accvgpr_read_b32 v63, a187\n" \
"v_pk_mul_f32 v[60:61], v[60:61], s[70:71]\n" \
"v_pk_mul_f32 v[76:77], v[76:77], s[70:71]\n" \
"v_pk_mul_f32 v[52:53], v[52:53], s[70:71]\n" \
"v_pk_mul_f32 v[100:101], v[100:101], s[70:71]\n" \
"v_accvgpr_read_b32 v88, a148\n" \
"v_accvgpr_read_b32 v89, a149\n" \
"v_accvgpr_read_b32 v90, a150\n" \
"v_accvgpr_read_b32 v91, a151\n" \
"v_pk_mul_f32 v[86:87], v[86:87], s[70:71]\n" \
"v_cvt_pk_bf16_f32 v84, v84, v85\n" \
"v_accvgpr_read_b32 v72, a156\n" \
"v_accvgpr_read_b32 v73, a157\n" \
"v_accvgpr_read_b32 v74, a158\n" \
"v_accvgpr_read_b32 v75, a159\n" \
"v_pk_mul_f32 v[70:71], v[70:71], s[70:71]\n" \
"v_cvt_pk_bf16_f32 v68, v68, v69\n" \
"v_accvgpr_read_b32 v96, a180\n" \
"v_accvgpr_read_b32 v97, a181\n" \
"v_accvgpr_read_b32 v98, a182\n" \
"v_accvgpr_read_b32 v99, a183\n" \
"v_pk_mul_f32 v[94:95], v[94:95], s[70:71]\n" \
"v_cvt_pk_bf16_f32 v92, v92, v93\n" \
"v_accvgpr_read_b32 v64, a188\n" \
"v_accvgpr_read_b32 v65, a189\n" \
"v_accvgpr_read_b32 v66, a190\n" \
"v_accvgpr_read_b32 v67, a191\n" \
"v_pk_mul_f32 v[62:63], v[62:63], s[70:71]\n" \
"v_cvt_pk_bf16_f32 v60, v60, v61\n" \
"v_pk_mul_f32 v[78:79], v[78:79], s[70:71]\n" \
"v_cvt_pk_bf16_f32 v76, v76, v77\n" \
"v_pk_mul_f32 v[54:55], v[54:55], s[70:71]\n" \
"v_cvt_pk_bf16_f32 v52, v52, v53\n" \
"v_pk_mul_f32 v[102:103], v[102:103], s[70:71]\n" \
"v_cvt_pk_bf16_f32 v100, v100, v101\n" \
"v_pk_mul_f32 v[88:89], v[88:89], s[70:71]\n" \
"v_pk_mul_f32 v[90:91], v[90:91], s[70:71]\n" \
"v_cvt_pk_bf16_f32 v85, v86, v87\n" \
"v_pk_mul_f32 v[72:73], v[72:73], s[70:71]\n" \
"v_pk_mul_f32 v[74:75], v[74:75], s[70:71]\n" \
"v_cvt_pk_bf16_f32 v69, v70, v71\n" \
"v_pk_mul_f32 v[96:97], v[96:97], s[70:71]\n" \
"v_pk_mul_f32 v[98:99], v[98:99], s[70:71]\n" \
"buffer_store_dwordx4 a[104:107], v20, s[80:83], s88 offen offset:0 sc1\n" \
"v_cvt_pk_bf16_f32 v93, v94, v95\n" \
"v_pk_mul_f32 v[64:65], v[64:65], s[70:71]\n" \
"v_pk_mul_f32 v[66:67], v[66:67], s[70:71]\n" \
"v_cvt_pk_bf16_f32 v61, v62, v63\n" \
"v_pk_mul_f32 v[80:81], v[80:81], s[70:71]\n" \
"v_pk_mul_f32 v[82:83], v[82:83], s[70:71]\n" \
"v_cvt_pk_bf16_f32 v77, v78, v79\n" \
"v_pk_mul_f32 v[56:57], v[56:57], s[70:71]\n" \
"v_pk_mul_f32 v[58:59], v[58:59], s[70:71]\n" \
"v_cvt_pk_bf16_f32 v53, v54, v55\n" \
"v_accvgpr_read_b32 v104, a244\n" \
"v_accvgpr_read_b32 v105, a245\n" \
"v_accvgpr_read_b32 v106, a246\n" \
"v_accvgpr_read_b32 v107, a247\n" \
"v_cvt_pk_bf16_f32 v101, v102, v103\n" \
"v_cvt_pk_bf16_f32 v86, v88, v89\n" \
"v_mfma_f32_16x16x32_bf16 v[104:107], v[156:159], v[108:111], v[104:107]\n" \
"v_cvt_pk_bf16_f32 v87, v90, v91\n" \
"v_cvt_pk_bf16_f32 v70, v72, v73\n" \
"ds_write_b128 v21, v[84:87] offset:16384\n" \
"v_mfma_f32_16x16x32_bf16 v[104:107], v[160:163], v[112:115], v[104:107]\n" \
"v_cvt_pk_bf16_f32 v71, v74, v75\n" \
"ds_write_b128 v21, v[68:71] offset:17408\n" \
"v_cvt_pk_bf16_f32 v94, v96, v97\n" \
"v_cvt_pk_bf16_f32 v95, v98, v99\n" \
"ds_write_b128 v21, v[92:95] offset:20480\n" \
"v_cvt_pk_bf16_f32 v62, v64, v65\n" \
"v_cvt_pk_bf16_f32 v63, v66, v67\n" \
"ds_write_b128 v21, v[60:63] offset:21504\n" \
"v_pk_mul_f32 v[104:105], v[104:105], s[70:71]\n" \
"v_pk_mul_f32 v[106:107], v[106:107], s[70:71]\n" \
"v_cvt_pk_bf16_f32 v78, v80, v81\n" \
"v_cvt_pk_bf16_f32 v79, v82, v83\n" \
"ds_write_b128 v21, v[76:79] offset:24576\n" \
"v_cvt_pk_bf16_f32 v54, v56, v57\n" \
"v_cvt_pk_bf16_f32 v55, v58, v59\n" \
"ds_write_b128 v21, v[52:55] offset:25600\n" \
"v_cvt_pk_bf16_f32 v102, v104, v105\n" \
"v_cvt_pk_bf16_f32 v103, v106, v107\n" \
"ds_write_b128 v21, v[100:103] offset:28672\n" \
"buffer_store_dwordx4 a[108:111], v20, s[80:83], s89 offen offset:1024 sc1\n" \
"v_accvgpr_read_b32 v84, a248\n" \
"v_accvgpr_read_b32 v85, a249\n" \
"v_accvgpr_read_b32 v86, a250\n" \
"v_accvgpr_read_b32 v87, a251\n" \
"v_accvgpr_read_b32 v88, a252\n" \
"v_accvgpr_read_b32 v89, a253\n" \
"v_mfma_f32_16x16x32_bf16 v[84:87], v[168:171], v[112:115], v[84:87]\n" \
"v_accvgpr_read_b32 v90, a254\n" \
"v_accvgpr_read_b32 v91, a255\n" \
"v_mfma_f32_16x16x32_bf16 v[84:87], v[164:167], v[108:111], v[84:87]\n" \
"v_sub_f32_e32 v44, 1.0, v45\n" \
"v_mfma_f32_16x16x32_bf16 v[88:91], v[172:175], v[108:111], v[88:91]\n" \
"ds_write_b32 v26, v44\n" \
"v_mfma_f32_16x16x32_bf16 v[88:91], v[176:179], v[112:115], v[88:91]\n" \
"s_nop 3\n" \
"v_pk_mul_f32 v[84:85], v[84:85], s[70:71]\n" \
"v_pk_mul_f32 v[86:87], v[86:87], s[70:71]\n" \
"v_cvt_pk_bf16_f32 v84, v84, v85\n" \
"v_cvt_pk_bf16_f32 v85, v86, v87\n" \
"v_pk_mul_f32 v[88:89], v[88:89], s[70:71]\n" \
"v_pk_mul_f32 v[90:91], v[90:91], s[70:71]\n" \
"v_cvt_pk_bf16_f32 v86, v88, v89\n" \
"v_cvt_pk_bf16_f32 v87, v90, v91\n" \
"ds_write_b128 v21, v[84:87] offset:29696\n" \
"L_end_%=:\n"
#define PROJ_VCLOB "v21","v24","v25","v33","v34","v35","v36","v37","v38","v39","v40","v41","v42","v43","v44","v45","v46","v47","v48","v49","v50","v51","v52","v53","v54","v55","v56","v57","v58","v59","v60","v61","v62","v63","v64","v65","v66","v67","v68","v69","v70","v71","v72","v73","v74","v75","v76","v77","v78","v79","v80","v81","v82","v83","v84","v85","v86","v87","v88","v89","v90","v91","v92","v93","v94","v95","v96","v97","v98","v99","v100","v101","v102","v103","v104","v105","v106","v107","v108","v109","v110","v111","v112","v113","v114","v115","v116","v117","v118","v119","v120","v121","v122","v123","v124","v125","v126","v127","v128","v129","v130","v131","v132","v133","v134","v135","v136","v137","v138","v139","v140","v141","v142","v143","v144","v145","v146","v147","v148","v149","v150","v151","v152","v153","v154","v155","v156","v157","v158","v159","v160","v161","v162","v163","v164","v165","v166","v167","v168","v169","v170","v171","v172","v173","v174","v175","v176","v177","v178","v179","v180","v181","v182","v183","v184","v185","v186","v187","v188","v189","v190","v191","v192","v193","v194","v195","v196","v197","v198","v199","v200","v201","v202","v203","v204","v205","v206","v207","v208","v209","v210","v211","v212","v213","v214","v215","v216","v217","v218","v219","v220","v221","v222","v223","v224","v225","v226","v227","v228","v229","v230","v231","v232","v233","v234","v235","v236","v237","v238","v239","v240","v241","v242","v243","v244","v245","v246","v247","v248","v249","v250","v251","v252","v253","v254","v255"
#define PROJ_SCLOB "s37","s38","s39","s40","s41","s42","s43","s44","s45","s46","s47","s48","s49","s50","s51","s52","s53","s54","s55","s62","s63","s76","s77","s78","s79","s80","s81","s82","s83","s84","s85","s86","s87","s88","s89","s94","s95","s96","s97","s98","s99","s100","s101"

#define PLO(p) ((unsigned)(size_t)(p))
#define PHI(p) ((unsigned)((size_t)(p) >> 32))
__device__ __forceinline__ unsigned proj_phase(const u16* __restrict__ x, const u16* __restrict__ ckv, const u16* __restrict__ wq, const u16* __restrict__ wuk,
                                                       const u16* __restrict__ wuv, const u16* __restrict__ wqkv, const u16* __restrict__ gw, const u16* wo, const u16* wob, const u16* out,
                                                       char* __restrict__ ws, char* lds, const unsigned wid, unsigned& tag, u32x4 (&vp)[5], u32x4 (&sa)[5], unsigned (&sd)[2]) {
    typedef unsigned u32x2 __attribute__((ext_vector_type(2)));
    unsigned* const flags = (unsigned*)(ws + WS_F);
    const int b = blockIdx.x, h = (b & 7) * 4 + ((b >> 3) & 3), mt = b >> 5;
    const unsigned L0 = (unsigned)(size_t)lds, rb = (wid < 2u ? 15u - mt : mt) * 128u;
    RTS(0);
#define PST() do {} while (0)
    const unsigned lane = threadIdx.x & 63u, wr = wid >> 1, wc = wid & 1u;
    PST();
    unsigned vAc[2], vB[2];
    const unsigned sp = b & 7, nt = (b >> 3) & 3, rot = (PROJ_ROT) & 7u;
#pragma unroll
    for (int par = 0; par < 2; ++par) {
        const unsigned lr = lane >> 3, cc = (lane & 7u) ^ (4u * par + (lr >> 1)), r0 = rb + wc * 64u + lr;
        vAc[par] = r0 * (DC * 2) + cc * 16u;
        const unsigned br = lane >> 4, p16 = lane & 15u, sc = (((p16 >> 1) ^ (br | (par << 2))) << 1) | (p16 & 1u);
        vB[par] = (16u * wid + br) * (NBIG * 2) + h * (HD * 2) + sc * 16u;
    }
#define SRD(p, n) (u32x4){PLO(p), PHI(p) & 0xffffu, n, 0x00020000u}
    u32x4 sC = SRD(ckv, S * DC * 2), sK = SRD(wuk, DC * NBIG * 2), sV = SRD(wuv, DC * NBIG * 2);
#define PRE(n) do { SBAR(); asm volatile(PROJ_PRE##n :: "{v22}"(vAc[0]), "{v23}"(vAc[1]), "{v26}"(vB[0]), "{v27}"(vB[1]), "{s[64:67]}"(sC), "{s[68:71]}"(sK), "{s[72:75]}"(sV), \
                 [mA] "s"(L0 + wid * 8192u), [mB] "s"(L0 + 65536u + wid * 4096u), [rc] "s"(rot * 128u) : "memory", "m0", "scc", "s62", "s63", "s84", "s85"); SBAR(); } while (0)
    PRE(0);
    const unsigned fr = lane & 15u, fg = lane >> 4, r4 = (lane >> 2) & 3u, sB = r4 | ((fg & 1u) << 2);
    const unsigned aA0 = L0 + (wr * 64u + fr) * 128u + ((fg ^ (fr >> 1)) << 4);
    unsigned vl = lane * 16u, r32 = L0 + 65536u + (8u * fg + r4) * 256u + (lane & 3u) * 8u + ((sB ^ (4u * wc)) << 5);
    u32x2 r28 = {aA0, aA0 ^ 64u};
    asm volatile("" : "+v"(vl), "+v"(r28), "+v"(r32));
    PST();
    PRE(1);
    PST();
    PRE(2);
    PST();
    asm volatile("s_mov_b64 exec, 0\n s_cmp_lt_u32 %0, 2\n s_cbranch_scc1 L_chA\n s_cmp_eq_u32 %0, 2\n s_cbranch_scc1 L_pf2\n s_branch L_pf3\n"
                 "L_chA: s_cmp_eq_u32 %0, 0\n s_cbranch_scc1 L_pf0\n s_branch L_pf1\n L_retA: s_mov_b64 exec, -1" :: "s"(wid) : "scc", "memory");
    PRE(3);
    PST();
    sa[0] = (u32x4){PLO(x), PHI(x), PLO(wq), PHI(wq)}; sa[1] = (u32x4){PLO(wqkv), PHI(wqkv), PLO(gw), PHI(gw)}; sa[2] = (u32x4){PLO(wo), PHI(wo), PLO(wob), PHI(wob)}; sa[3] = (u32x4){PLO(out), PHI(out), rot * 128u, 0u};
    asm volatile(PROJ_ASM
                 : [tag] "=&s"(tag), "+{s[8:11]}"(sa[0]), "+{s[12:15]}"(sa[1]), "+{s[16:19]}"(sa[2]), "+{s[20:23]}"(sa[3]), "=&{s[56:59]}"(sa[4]), "=&{s60}"(sd[0]), "=&{s61}"(sd[1]),
                   "+{s[64:67]}"(sC), "+{s[68:71]}"(sK), "+{s[72:75]}"(sV), "+{v22}"(vAc[0]), "+{v23}"(vAc[1]), "+{v26}"(vB[0]), "+{v27}"(vB[1]), "+{v[28:29]}"(r28), "+{v32}"(r32),
                   "={v[0:3]}"(vp[0]), "={v[4:7]}"(vp[1]), "={v[8:11]}"(vp[2]), "={v[12:15]}"(vp[3]), "={v[16:19]}"(vp[4])
                 : "{v20}"(vl), [wsl] "s"(PLO(ws)), [wsh] "s"(PHI(ws)), [b] "s"(b), [w] "s"(wid), [mA] "s"(L0 + wid * 8192u), [mB] "s"(L0 + 65536u + wid * 4096u), [mS] "s"(L0 + 151552u + wid * 512u)
                 : "memory", "m0", "vcc", "scc", PROJ_VCLOB, PROJ_SCLOB, ACLOB_LO, ACLOB_HI);
    RTS(1);
    return vl;
}

namespace att {
#define DI __device__ __forceinline__
constexpr int LDS_BYTES = 163840;
constexpr unsigned LDS_G = 159744u, LDS_L = 161792u;
typedef __bf16 bfx8 __attribute__((ext_vector_type(8)));
DI unsigned rfl(unsigned x) { return __builtin_amdgcn_readfirstlane(x); }
DI f32x4 mfma(u32x4 a, u32x4 b, f32x4 c) { return __builtin_amdgcn_mfma_f32_16x16x32_bf16(__builtin_bit_cast(bfx8, a), __builtin_bit_cast(bfx8, b), c, 0, 0, 0); }
typedef __bf16 bfx2 __attribute__((ext_vector_type(2)));
typedef float f32x2 __attribute__((ext_vector_type(2)));
DI unsigned cvtpk2(float lo, float hi) { f32x2 v = {lo, hi}; bfx2 b = __builtin_convertvector(v, bfx2); return __builtin_bit_cast(unsigned, b); }
DI void att_exact(const u16* __restrict__ x, const u16* __restrict__ wq, char* __restrict__ ws, unsigned h, unsigned r0, float g1) {
    const unsigned lane = lane_id(), g = lane >> 4, c = lane & 15u;
    f32x4 o[8];
#pragma unroll
    for (int n = 0; n < 8; ++n) o[n] = (f32x4){0.f, 0.f, 0.f, 0.f};
#pragma unroll 1
    for (unsigned kk = 0; kk < (unsigned)DM; kk += 32u) {
        const unsigned k = kk + 8u * g;
        const u32x4 xf = *reinterpret_cast<const u32x4*>(x + (r0 + c) * DM + k);
#pragma unroll
        for (int n = 0; n < 8; ++n) {
            const u16* w = wq + k * NBIG + h * HD + n * 16 + c; u32x4 wf;
#pragma unroll
            for (int e = 0; e < 4; ++e) wf[e] = (unsigned)w[2 * e * NBIG] | ((unsigned)w[(2 * e + 1) * NBIG] << 16);
            o[n] = mfma(wf, xf, o[n]);
        }
    }
    u32x4 q[4];
#pragma unroll
    for (int p = 0; p < 4; ++p) {
        const f32x4 a = o[2 * p] * 0.12751743082f, b = o[2 * p + 1] * 0.12751743082f;
        q[p] = (u32x4){cvtpk2(a[0], a[1]), cvtpk2(a[2], a[3]), cvtpk2(b[0], b[1]), cvtpk2(b[2], b[3])};
    }
#pragma unroll
    for (int n = 0; n < 8; ++n) o[n] = (f32x4){0.f, 0.f, 0.f, 0.f};
    const __amdgpu_buffer_rsrc_t srd = __builtin_amdgcn_make_buffer_rsrc((void*)(ws + WS_K + (size_t)h * 1048576), (short)0, 1048576, 0x00020000);
    float m = -3.0e38f, l = 0.f;
#pragma unroll 1
    for (unsigned k0 = 0; k0 <= r0 + 15u; k0 += 32u) {
        const unsigned tb = (k0 >> 6) * 32768u + (k0 & 32u) * 256u;
        f32x4 s[2]; float mx = m;
#pragma unroll
        for (int kt = 0; kt < 2; ++kt) {
            s[kt] = (f32x4){0.f, 0.f, 0.f, 0.f};
#pragma unroll
            for (int p = 0; p < 4; ++p) s[kt] = mfma(__builtin_amdgcn_raw_buffer_load_b128(srd, lane * 16u + (kt * 4 + p) * 1024u, tb, 16), q[p], s[kt]);
#pragma unroll
            for (int e = 0; e < 4; ++e) { if (k0 + 16u * kt + 4u * g + e > r0 + c) s[kt][e] = -3.0e38f; mx = fmaxf(mx, s[kt][e]); }
        }
        mx = fmaxf(mx, __shfl_xor(mx, 16)); mx = fmaxf(mx, __shfl_xor(mx, 32));
        const float al = __builtin_amdgcn_exp2f(m - mx); m = mx;
        u32x4 pf;
#pragma unroll
        for (int e = 0; e < 4; ++e) pf[e] = cvtpk2(__builtin_amdgcn_exp2f(s[e >> 1][2 * (e & 1)] - m), __builtin_amdgcn_exp2f(s[e >> 1][2 * (e & 1) + 1] - m));
        l *= al;
#pragma unroll
        for (int e = 0; e < 4; ++e) l += __uint_as_float(pf[e] << 16) + __uint_as_float(pf[e] & 0xffff0000u);
#pragma unroll
        for (int n = 0; n < 8; ++n) o[n] = mfma(__builtin_amdgcn_raw_buffer_load_b128(srd, lane * 16u + 16384u + ((n >> 2) * 8 + (n & 3)) * 1024u, tb - (k0 & 32u) * 128u, 16), pf, o[n] * al);
    }
    l += __shfl_xor(l, 16); l += __shfl_xor(l, 32);
    const float f = g1 / l;
    const __amdgpu_buffer_rsrc_t srdA = __builtin_amdgcn_make_buffer_rsrc((void*)(ws + WS_AF), (short)0, 128 * KS_TOT * 1024, 0x00020000);
#pragma unroll
    for (int p = 0; p < 4; ++p) {
        const f32x4 a = o[2 * p] * f, b = o[2 * p + 1] * f;
        __builtin_amdgcn_raw_buffer_store_b128((u32x4){cvtpk2(a[0], a[1]), cvtpk2(a[2], a[3]), cvtpk2(b[0], b[1]), cvtpk2(b[2], b[3])}, srdA, lane * 16u + p * 1024u, ((r0 >> 4) * KS_TOT + 18u + 4u * h) * 1024u, 16);
    }
}
}

#define ATT_ASM \
"L_att0:\n" \
"s_mov_b32 s101, 0\n" \
"s_waitcnt vmcnt(20) lgkmcnt(0)\n" \
"s_barrier\n" \
"v_accvgpr_write_b32 a216, 0\n" \
"v_accvgpr_write_b32 a217, 0\n" \
"v_accvgpr_write_b32 a218, 0\n" \
"v_accvgpr_write_b32 a219, 0\n" \
"v_and_b32_e32 v63, 0x300, v20\n" \
"v_add_u32_e32 v58, s20, v20\n" \
"v_mfma_f32_32x32x16_bf16 v[128:143], a[216:219], a[216:219], 0\n" \
"ds_read_b128 v[112:115], v63 offset:0\n" \
"ds_read_b128 a[0:3], v58 offset:0\n" \
"ds_read_b128 a[16:19], v58 offset:8192\n" \
"ds_read_b128 v[116:119], v63 offset:1024\n" \
"ds_read_b128 a[4:7], v58 offset:1024\n" \
"v_add_u32_e32 v59, s21, v20\n" \
"v_mfma_f32_32x32x16_bf16 v[144:159], a[216:219], a[216:219], 0\n" \
"ds_read_b128 a[32:35], v59 offset:0\n" \
"ds_read_b128 a[48:51], v59 offset:8192\n" \
"s_sub_u32 s67, s12, 0xc000\n" \
"s_mov_b32 s65, s12\n" \
"v_add_u32_e32 v55, s67, v20\n" \
"v_add_u32_e32 v52, 0xc000, v55\n" \
"v_mfma_f32_32x32x16_bf16 v[160:175], a[216:219], a[216:219], 0\n" \
"ds_read_b128 a[20:23], v58 offset:9216\n" \
"s_xor_b32 m0, s65, 0x10000\n" \
"ds_read_b128 a[36:39], v59 offset:1024\n" \
"buffer_load_dwordx4 v52, s[8:11], 0 offen offset:0 sc1 lds\n" \
"ds_read_b128 a[52:55], v59 offset:9216\n" \
"v_mfma_f32_32x32x16_bf16 v[176:191], a[216:219], a[216:219], 0\n" \
"buffer_load_dwordx4 v52, s[8:11], 0 offen offset:1024 sc1 lds\n" \
"ds_read_b128 v[120:123], v63 offset:2048\n" \
"v_add_u32_e32 v60, s15, v20\n" \
"v_add_u32_e32 v60, 0x10000, v60\n" \
"v_mov_b32_e32 v56, s62\n" \
"v_mfma_f32_32x32x16_bf16 v[192:207], a[216:219], a[216:219], 0\n" \
"ds_read_b128 a[8:11], v58 offset:2048\n" \
"ds_read_b128 a[24:27], v58 offset:10240\n" \
"v_readlane_b32 s68, v11, 0\n" \
"v_bfe_i32 v21, v20, 4, 1\n" \
"v_mov_b32_e32 v54, 0x3f803f80\n" \
"v_mov_b32_e32 v57, 0x3f003f00\n" \
"v_mfma_f32_32x32x16_bf16 v[208:223], a[216:219], a[216:219], 0\n" \
"ds_read_b128 a[40:43], v59 offset:2048\n" \
"ds_read_b128 a[56:59], v59 offset:10240\n" \
"s_waitcnt lgkmcnt(14)\n" \
"v_xor_b32_e32 v112, 0x80008000, v112\n" \
"v_xor_b32_e32 v113, 0x80008000, v113\n" \
"v_xor_b32_e32 v114, 0x80008000, v114\n" \
"v_mfma_f32_32x32x16_bf16 v[224:239], a[216:219], a[216:219], 0\n" \
"ds_read_b128 v[124:127], v63 offset:3072\n" \
"ds_read_b128 a[12:15], v58 offset:3072\n" \
"v_xor_b32_e32 v115, 0x80008000, v115\n" \
"v_and_b32_e32 v21, 0xaaaaaaaa, v21\n" \
"v_accvgpr_write_b32 a220, v112\n" \
"s_waitcnt lgkmcnt(13)\n" \
"v_xor_b32_e32 v116, 0x80008000, v116\n" \
"v_mfma_f32_32x32x16_bf16 v[240:255], a[216:219], a[216:219], 0\n" \
"ds_read_b128 a[28:31], v58 offset:11264\n" \
"ds_read_b128 a[44:47], v59 offset:3072\n" \
"v_xor_b32_e32 v117, 0x80008000, v117\n" \
"v_xor_b32_e32 v118, 0x80008000, v118\n" \
"v_xor_b32_e32 v119, 0x80008000, v119\n" \
"v_accvgpr_write_b32 a221, v113\n" \
"v_mfma_f32_32x32x16_bf16 a[192:207], a[216:219], a[216:219], 0\n" \
"ds_read_b128 a[60:63], v59 offset:11264\n" \
"ds_read_b128 a[160:163], v20 offset:0\n" \
"v_accvgpr_write_b32 a222, v114\n" \
"v_accvgpr_write_b32 a223, v115\n" \
"v_readlane_b32 s80, v11, 9\n" \
"v_readlane_b32 s81, v11, 10\n" \
"v_mfma_f32_16x16x32_bf16 v[32:35], v[112:115], a[0:3], 0\n" \
"ds_read_b128 a[164:167], v20 offset:1024\n" \
"ds_read_b128 a[168:171], v20 offset:2048\n" \
"v_mfma_f32_16x16x32_bf16 v[36:39], v[112:115], a[16:19], 0\n" \
"ds_read_b128 a[172:175], v20 offset:3072\n" \
"s_waitcnt lgkmcnt(15)\n" \
"v_mfma_f32_16x16x32_bf16 v[40:43], v[112:115], a[32:35], 0\n" \
"ds_read_b128 a[188:191], v20 offset:7168\n" \
"s_waitcnt lgkmcnt(14)\n" \
"v_xor_b32_e32 v120, 0x80008000, v120\n" \
"v_mfma_f32_16x16x32_bf16 v[44:47], v[112:115], a[48:51], 0\n" \
"ds_read_b128 a[184:187], v20 offset:6144\n" \
"v_xor_b32_e32 v121, 0x80008000, v121\n" \
"v_mfma_f32_16x16x32_bf16 v[32:35], v[116:119], a[4:7], v[32:35]\n" \
"v_xor_b32_e32 v122, 0x80008000, v122\n" \
"v_xor_b32_e32 v123, 0x80008000, v123\n" \
"v_mfma_f32_16x16x32_bf16 v[36:39], v[116:119], a[20:23], v[36:39]\n" \
"ds_read_b128 a[180:183], v20 offset:5120\n" \
"ds_read_b128 a[176:179], v20 offset:4096\n" \
"v_mfma_f32_16x16x32_bf16 v[40:43], v[116:119], a[36:39], v[40:43]\n" \
"ds_read_b128 a[128:131], v20 offset:8192\n" \
"s_mov_b32 s82, 0x47fca0\n" \
"v_mfma_f32_16x16x32_bf16 v[44:47], v[116:119], a[52:55], v[44:47]\n" \
"s_waitcnt lgkmcnt(13)\n" \
"v_xor_b32_e32 v124, 0x80008000, v124\n" \
"v_mfma_f32_16x16x32_bf16 v[32:35], v[120:123], a[8:11], v[32:35]\n" \
"v_xor_b32_e32 v125, 0x80008000, v125\n" \
"v_xor_b32_e32 v126, 0x80008000, v126\n" \
"v_mfma_f32_16x16x32_bf16 v[36:39], v[120:123], a[24:27], v[36:39]\n" \
"ds_read_b128 a[132:135], v20 offset:9216\n" \
"v_xor_b32_e32 v127, 0x80008000, v127\n" \
"v_mfma_f32_16x16x32_bf16 v[40:43], v[120:123], a[40:43], v[40:43]\n" \
"ds_read_b128 a[136:139], v20 offset:10240\n" \
"ds_read_b128 a[140:143], v20 offset:11264\n" \
"v_mfma_f32_16x16x32_bf16 v[44:47], v[120:123], a[56:59], v[44:47]\n" \
"ds_read_b128 a[144:147], v20 offset:12288\n" \
"s_waitcnt lgkmcnt(15)\n" \
"v_mfma_f32_16x16x32_bf16 v[32:35], v[124:127], a[12:15], v[32:35]\n" \
"ds_read_b128 a[148:151], v20 offset:13312\n" \
"ds_read_b128 a[152:155], v20 offset:14336\n" \
"v_mfma_f32_16x16x32_bf16 v[36:39], v[124:127], a[28:31], v[36:39]\n" \
"ds_read_b128 a[156:159], v20 offset:15360\n" \
"s_waitcnt lgkmcnt(15)\n" \
"v_mfma_f32_16x16x32_bf16 v[40:43], v[124:127], a[44:47], v[40:43]\n" \
"ds_read_b128 a[64:67], v60 offset:16384\n" \
"s_mov_b32 s83, 0x20000\n" \
"v_mfma_f32_16x16x32_bf16 v[44:47], v[124:127], a[60:63], v[44:47]\n" \
"ds_read_b128 a[68:71], v60 offset:17408\n" \
"s_mov_b32 s100, 0x71800000\n" \
"v_mfma_f32_16x16x32_bf16 v[64:67], a[160:163], a[0:3], v[32:35]\n" \
"ds_read_b128 a[72:75], v60 offset:18432\n" \
"s_waitcnt lgkmcnt(15)\n" \
"v_mfma_f32_16x16x32_bf16 v[64:67], a[164:167], a[4:7], v[64:67]\n" \
"ds_read_b128 a[76:79], v60 offset:19456\n" \
"s_mov_b32 s63, s23\n" \
"v_mfma_f32_16x16x32_bf16 v[64:67], a[168:171], a[8:11], v[64:67]\n" \
"ds_read_b128 a[80:83], v60 offset:24576\n" \
"v_mov_b32_e32 v62, 0\n" \
"v_mfma_f32_16x16x32_bf16 v[64:67], a[172:175], a[12:15], v[64:67]\n" \
"ds_read_b128 a[84:87], v60 offset:25600\n" \
"s_waitcnt lgkmcnt(15)\n" \
"v_mfma_f32_16x16x32_bf16 v[68:71], a[188:191], a[12:15], v[32:35]\n" \
"ds_read_b128 a[88:91], v60 offset:26624\n" \
"v_accvgpr_write_b32 a212, v54\n" \
"v_mfma_f32_16x16x32_bf16 v[68:71], a[184:187], a[8:11], v[68:71]\n" \
"ds_read_b128 a[92:95], v60 offset:27648\n" \
"v_accvgpr_write_b32 a208, 0\n" \
"v_mfma_f32_16x16x32_bf16 v[68:71], a[180:183], a[4:7], v[68:71]\n" \
"ds_read_b128 a[96:99], v60 offset:0\n" \
"s_waitcnt lgkmcnt(15)\n" \
"L_pf10:\n" \
"s_cbranch_execz L_pf14\n" \
"s_nop 0\n" \
"v_mfma_f32_16x16x32_bf16 v[68:71], a[176:179], a[0:3], v[68:71]\n" \
"ds_read_b128 a[100:103], v60 offset:1024\n" \
"v_accvgpr_write_b32 a213, v54\n" \
"v_mfma_f32_16x16x32_bf16 v[72:75], a[160:163], a[16:19], v[36:39]\n" \
"ds_read_b128 a[104:107], v60 offset:2048\n" \
"v_accvgpr_write_b32 a209, 0\n" \
"v_mfma_f32_16x16x32_bf16 v[72:75], a[164:167], a[20:23], v[72:75]\n" \
"ds_read_b128 a[108:111], v60 offset:3072\n" \
"v_accvgpr_write_b32 a214, v54\n" \
"v_mfma_f32_16x16x32_bf16 v[72:75], a[168:171], a[24:27], v[72:75]\n" \
"ds_read_b128 a[112:115], v60 offset:8192\n" \
"v_accvgpr_write_b32 a210, 0\n" \
"v_mfma_f32_16x16x32_bf16 v[72:75], a[172:175], a[28:31], v[72:75]\n" \
"ds_read_b128 a[116:119], v60 offset:9216\n" \
"v_accvgpr_write_b32 a215, v54\n" \
"v_mfma_f32_16x16x32_bf16 v[76:79], a[188:191], a[28:31], v[36:39]\n" \
"ds_read_b128 a[120:123], v60 offset:10240\n" \
"v_accvgpr_write_b32 a211, 0\n" \
"v_mfma_f32_16x16x32_bf16 v[76:79], a[184:187], a[24:27], v[76:79]\n" \
"ds_read_b128 a[124:127], v60 offset:11264\n" \
"v_or_b32_e32 v21, 0x44444444, v21\n" \
"v_mfma_f32_16x16x32_bf16 v[76:79], a[180:183], a[20:23], v[76:79]\n" \
"v_exp_f32_e32 v64, v64\n" \
"v_mfma_f32_16x16x32_bf16 v[76:79], a[176:179], a[16:19], v[76:79]\n" \
"v_exp_f32_e32 v65, v65\n" \
"v_mfma_f32_16x16x32_bf16 v[80:83], a[160:163], a[32:35], v[40:43]\n" \
"v_exp_f32_e32 v66, v66\n" \
"v_mfma_f32_16x16x32_bf16 v[80:83], a[164:167], a[36:39], v[80:83]\n" \
"v_exp_f32_e32 v67, v67\n" \
"v_mfma_f32_16x16x32_bf16 v[80:83], a[168:171], a[40:43], v[80:83]\n" \
"v_exp_f32_e32 v68, v68\n" \
"v_mfma_f32_16x16x32_bf16 v[80:83], a[172:175], a[44:47], v[80:83]\n" \
"v_exp_f32_e32 v69, v69\n" \
"v_mfma_f32_16x16x32_bf16 v[84:87], a[188:191], a[44:47], v[40:43]\n" \
"v_exp_f32_e32 v70, v70\n" \
"v_mfma_f32_16x16x32_bf16 v[84:87], a[184:187], a[40:43], v[84:87]\n" \
"v_exp_f32_e32 v71, v71\n" \
"v_mfma_f32_16x16x32_bf16 v[84:87], a[180:183], a[36:39], v[84:87]\n" \
"v_exp_f32_e32 v72, v72\n" \
"v_mfma_f32_16x16x32_bf16 v[84:87], a[176:179], a[32:35], v[84:87]\n" \
"v_exp_f32_e32 v73, v73\n" \
"v_mfma_f32_16x16x32_bf16 v[88:91], a[160:163], a[48:51], v[44:47]\n" \
"v_exp_f32_e32 v74, v74\n" \
"v_mfma_f32_16x16x32_bf16 v[88:91], a[164:167], a[52:55], v[88:91]\n" \
"v_exp_f32_e32 v75, v75\n" \
"v_mfma_f32_16x16x32_bf16 v[88:91], a[168:171], a[56:59], v[88:91]\n" \
"v_exp_f32_e32 v76, v76\n" \
"v_mfma_f32_16x16x32_bf16 v[88:91], a[172:175], a[60:63], v[88:91]\n" \
"v_exp_f32_e32 v77, v77\n" \
"v_mfma_f32_16x16x32_bf16 v[92:95], a[188:191], a[60:63], v[44:47]\n" \
"v_exp_f32_e32 v78, v78\n" \
"v_mfma_f32_16x16x32_bf16 v[92:95], a[184:187], a[56:59], v[92:95]\n" \
"v_exp_f32_e32 v79, v79\n" \
"v_mfma_f32_16x16x32_bf16 v[92:95], a[180:183], a[52:55], v[92:95]\n" \
"v_exp_f32_e32 v80, v80\n" \
"v_mfma_f32_16x16x32_bf16 v[92:95], a[176:179], a[48:51], v[92:95]\n" \
"v_exp_f32_e32 v81, v81\n" \
"v_exp_f32_e32 v82, v82\n" \
"v_exp_f32_e32 v83, v83\n" \
"v_exp_f32_e32 v84, v84\n" \
"v_exp_f32_e32 v85, v85\n" \
"v_exp_f32_e32 v86, v86\n" \
"v_exp_f32_e32 v87, v87\n" \
"v_accvgpr_write_b32 a252, v57\n" \
"v_accvgpr_write_b32 a253, v57\n" \
"v_accvgpr_write_b32 a254, v57\n" \
"v_accvgpr_write_b32 a255, v57\n" \
"s_mov_b32 s64, 0x8000\n" \
"v_add_u32_e32 v50, 0x6000, v55\n" \
"v_add_u32_e32 v51, 0xa000, v55\n" \
"v_add_u32_e32 v53, 0x10000, v55\n" \
"v_add_u32_e32 v48, 0xc000, v20\n" \
"v_add_u32_e32 v49, s17, v20\n" \
"v_accvgpr_write_b32 a224, v116\n" \
"v_accvgpr_write_b32 a225, v117\n" \
"v_accvgpr_write_b32 a226, v118\n" \
"v_accvgpr_write_b32 a227, v119\n" \
"v_accvgpr_write_b32 a228, v120\n" \
"v_accvgpr_write_b32 a229, v121\n" \
"v_accvgpr_write_b32 a230, v122\n" \
"v_accvgpr_write_b32 a231, v123\n" \
"v_accvgpr_write_b32 a232, v124\n" \
"v_accvgpr_write_b32 a233, v125\n" \
"v_accvgpr_write_b32 a234, v126\n" \
"v_accvgpr_write_b32 a235, v127\n" \
"v_exp_f32_e32 v88, v88\n" \
"v_exp_f32_e32 v89, v89\n" \
"v_exp_f32_e32 v90, v90\n" \
"v_exp_f32_e32 v91, v91\n" \
"v_cvt_pk_bf16_f32 v96, v64, v65\n" \
"v_cvt_pk_bf16_f32 v97, v66, v67\n" \
"v_cvt_pk_bf16_f32 v98, v68, v69\n" \
"v_cvt_pk_bf16_f32 v99, v70, v71\n" \
"v_cvt_pk_bf16_f32 v104, v72, v73\n" \
"v_cvt_pk_bf16_f32 v105, v74, v75\n" \
"v_cvt_pk_bf16_f32 v106, v76, v77\n" \
"v_cvt_pk_bf16_f32 v107, v78, v79\n" \
"v_cvt_pk_bf16_f32 v112, v80, v81\n" \
"v_cvt_pk_bf16_f32 v113, v82, v83\n" \
"v_cvt_pk_bf16_f32 v114, v84, v85\n" \
"v_cvt_pk_bf16_f32 v115, v86, v87\n" \
"s_sub_u32 s66, s18, 1\n" \
"s_waitcnt vmcnt(2) lgkmcnt(0)\n" \
"s_barrier\n" \
"s_mov_b64 exec, 1\n" \
"buffer_store_dword v56, off, s[56:59], s68 offset:0 sc1\n" \
"buffer_store_dword v56, off, s[56:59], s22 sc1\n" \
"s_mov_b64 exec, -1\n" \
"s_cbranch_scc1 L_tr_%=\n" \
"s_branch L_1_%=\n" \
"L_t1_%=:\n" \
"v_mfma_f32_16x16x32_bf16 v[252:255], a[188:191], v[124:127], v[252:255]\n" \
"v_mfma_f32_16x16x32_bf16 v[252:255], a[172:175], v[120:123], v[252:255]\n" \
"v_smfmac_f32_16x16x64_bf16 a[204:207], a[212:215], v[120:127], v21\n" \
"s_waitcnt lgkmcnt(0)\n" \
"L_1_%=:\n" \
"v_mfma_f32_16x16x32_bf16 v[64:67], a[128:131], a[0:3], v[32:35]\n" \
"ds_read_b128 a[160:163], v49 offset:0\n" \
"ds_read_b128 a[176:179], v49 offset:4096\n" \
"v_mfma_f32_16x16x32_bf16 v[64:67], a[132:135], a[4:7], v[64:67]\n" \
"v_cvt_pk_bf16_f32 v120, v88, v89\n" \
"v_cvt_pk_bf16_f32 v121, v90, v91\n" \
"v_mfma_f32_16x16x32_bf16 v[64:67], a[136:139], a[8:11], v[64:67]\n" \
"ds_read_b128 a[180:183], v49 offset:5120\n" \
"ds_read_b128 a[164:167], v49 offset:1024\n" \
"v_mfma_f32_16x16x32_bf16 v[64:67], a[140:143], a[12:15], v[64:67]\n" \
"v_exp_f32_e32 v92, v92\n" \
"v_mfma_f32_16x16x32_bf16 v[68:71], a[156:159], a[12:15], v[32:35]\n" \
"ds_read_b128 a[168:171], v49 offset:2048\n" \
"ds_read_b128 a[184:187], v49 offset:6144\n" \
"v_mfma_f32_16x16x32_bf16 v[68:71], a[152:155], a[8:11], v[68:71]\n" \
"v_exp_f32_e32 v93, v93\n" \
"v_mfma_f32_16x16x32_bf16 v[68:71], a[148:151], a[4:7], v[68:71]\n" \
"ds_read_b128 a[188:191], v49 offset:7168\n" \
"ds_read_b128 a[172:175], v49 offset:3072\n" \
"v_mfma_f32_16x16x32_bf16 v[68:71], a[144:147], a[0:3], v[68:71]\n" \
"v_exp_f32_e32 v94, v94\n" \
"v_mfma_f32_16x16x32_bf16 v[72:75], a[128:131], a[16:19], v[36:39]\n" \
"v_exp_f32_e32 v95, v95\n" \
"v_mfma_f32_16x16x32_bf16 v[72:75], a[132:135], a[20:23], v[72:75]\n" \
"v_cvt_pk_bf16_f32 v122, v92, v93\n" \
"v_cvt_pk_bf16_f32 v123, v94, v95\n" \
"v_mfma_f32_16x16x32_bf16 v[72:75], a[136:139], a[24:27], v[72:75]\n" \
"v_exp_f32_e32 v64, v64\n" \
"v_mfma_f32_16x16x32_bf16 v[72:75], a[140:143], a[28:31], v[72:75]\n" \
"v_exp_f32_e32 v65, v65\n" \
"v_mfma_f32_16x16x32_bf16 v[76:79], a[156:159], a[28:31], v[36:39]\n" \
"v_exp_f32_e32 v66, v66\n" \
"v_mfma_f32_16x16x32_bf16 v[76:79], a[152:155], a[24:27], v[76:79]\n" \
"v_exp_f32_e32 v67, v67\n" \
"v_mfma_f32_16x16x32_bf16 v[76:79], a[148:151], a[20:23], v[76:79]\n" \
"v_exp_f32_e32 v68, v68\n" \
"v_mfma_f32_16x16x32_bf16 v[76:79], a[144:147], a[16:19], v[76:79]\n" \
"v_exp_f32_e32 v69, v69\n" \
"v_mfma_f32_16x16x32_bf16 v[80:83], a[128:131], a[32:35], v[40:43]\n" \
"v_exp_f32_e32 v70, v70\n" \
"v_mfma_f32_16x16x32_bf16 v[80:83], a[132:135], a[36:39], v[80:83]\n" \
"v_exp_f32_e32 v71, v71\n" \
"v_mfma_f32_16x16x32_bf16 v[80:83], a[136:139], a[40:43], v[80:83]\n" \
"v_cvt_pk_bf16_f32 v100, v64, v65\n" \
"v_cvt_pk_bf16_f32 v101, v66, v67\n" \
"v_mfma_f32_16x16x32_bf16 v[80:83], a[140:143], a[44:47], v[80:83]\n" \
"v_cvt_pk_bf16_f32 v102, v68, v69\n" \
"v_cvt_pk_bf16_f32 v103, v70, v71\n" \
"v_mfma_f32_16x16x32_bf16 v[84:87], a[156:159], a[44:47], v[40:43]\n" \
"v_exp_f32_e32 v72, v72\n" \
"v_mfma_f32_16x16x32_bf16 v[84:87], a[152:155], a[40:43], v[84:87]\n" \
"v_exp_f32_e32 v73, v73\n" \
"v_mfma_f32_16x16x32_bf16 v[84:87], a[148:151], a[36:39], v[84:87]\n" \
"v_exp_f32_e32 v74, v74\n" \
"v_mfma_f32_16x16x32_bf16 v[84:87], a[144:147], a[32:35], v[84:87]\n" \
"v_exp_f32_e32 v75, v75\n" \
"v_mfma_f32_16x16x32_bf16 v[88:91], a[128:131], a[48:51], v[44:47]\n" \
"v_exp_f32_e32 v76, v76\n" \
"v_mfma_f32_16x16x32_bf16 v[88:91], a[132:135], a[52:55], v[88:91]\n" \
"v_exp_f32_e32 v77, v77\n" \
"v_mfma_f32_16x16x32_bf16 v[88:91], a[136:139], a[56:59], v[88:91]\n" \
"v_exp_f32_e32 v78, v78\n" \
"v_mfma_f32_16x16x32_bf16 v[88:91], a[140:143], a[60:63], v[88:91]\n" \
"v_exp_f32_e32 v79, v79\n" \
"v_mfma_f32_16x16x32_bf16 v[92:95], a[156:159], a[60:63], v[44:47]\n" \
"v_cvt_pk_bf16_f32 v108, v72, v73\n" \
"v_cvt_pk_bf16_f32 v109, v74, v75\n" \
"v_mfma_f32_16x16x32_bf16 v[92:95], a[152:155], a[56:59], v[92:95]\n" \
"v_cvt_pk_bf16_f32 v110, v76, v77\n" \
"v_cvt_pk_bf16_f32 v111, v78, v79\n" \
"v_mfma_f32_16x16x32_bf16 v[92:95], a[148:151], a[52:55], v[92:95]\n" \
"ds_read_b128 a[128:131], v48 offset:0\n" \
"ds_read_b128 a[132:135], v48 offset:1024\n" \
"v_mfma_f32_16x16x32_bf16 v[92:95], a[144:147], a[48:51], v[92:95]\n" \
"s_add_u32 s63, s63, s63\n" \
"s_waitcnt lgkmcnt(2)\n" \
"v_mfma_f32_16x16x32_bf16 v[128:131], a[160:163], v[96:99], v[128:131]\n" \
"ds_read_b128 a[136:139], v48 offset:2048\n" \
"ds_read_b128 a[140:143], v48 offset:3072\n" \
"v_mfma_f32_16x16x32_bf16 v[128:131], a[176:179], v[100:103], v[128:131]\n" \
"v_exp_f32_e32 v80, v80\n" \
"v_mfma_f32_16x16x32_bf16 v[132:135], a[180:183], v[100:103], v[132:135]\n" \
"ds_read_b128 a[144:147], v48 offset:4096\n" \
"ds_read_b128 a[148:151], v48 offset:5120\n" \
"v_mfma_f32_16x16x32_bf16 v[132:135], a[164:167], v[96:99], v[132:135]\n" \
"v_exp_f32_e32 v81, v81\n" \
"v_mfma_f32_16x16x32_bf16 v[136:139], a[168:171], v[96:99], v[136:139]\n" \
"ds_read_b128 a[152:155], v48 offset:6144\n" \
"ds_read_b128 a[156:159], v48 offset:7168\n" \
"v_mfma_f32_16x16x32_bf16 v[136:139], a[184:187], v[100:103], v[136:139]\n" \
"v_exp_f32_e32 v82, v82\n" \
"v_mfma_f32_16x16x32_bf16 v[140:143], a[188:191], v[100:103], v[140:143]\n" \
"v_exp_f32_e32 v83, v83\n" \
"v_mfma_f32_16x16x32_bf16 v[140:143], a[172:175], v[96:99], v[140:143]\n" \
"v_exp_f32_e32 v84, v84\n" \
"v_smfmac_f32_16x16x64_bf16 a[192:195], a[212:215], v[96:103], v21\n" \
"v_mfma_f32_16x16x32_bf16 v[160:163], a[160:163], v[104:107], v[160:163]\n" \
"v_exp_f32_e32 v85, v85\n" \
"v_mfma_f32_16x16x32_bf16 v[160:163], a[176:179], v[108:111], v[160:163]\n" \
"v_exp_f32_e32 v86, v86\n" \
"v_mfma_f32_16x16x32_bf16 v[164:167], a[180:183], v[108:111], v[164:167]\n" \
"v_exp_f32_e32 v87, v87\n" \
"v_mfma_f32_16x16x32_bf16 v[164:167], a[164:167], v[104:107], v[164:167]\n" \
"v_cvt_pk_bf16_f32 v116, v80, v81\n" \
"v_cvt_pk_bf16_f32 v117, v82, v83\n" \
"v_mfma_f32_16x16x32_bf16 v[168:171], a[168:171], v[104:107], v[168:171]\n" \
"v_cvt_pk_bf16_f32 v118, v84, v85\n" \
"v_cvt_pk_bf16_f32 v119, v86, v87\n" \
"v_mfma_f32_16x16x32_bf16 v[168:171], a[184:187], v[108:111], v[168:171]\n" \
"v_exp_f32_e32 v88, v88\n" \
"v_mfma_f32_16x16x32_bf16 v[172:175], a[188:191], v[108:111], v[172:175]\n" \
"v_exp_f32_e32 v89, v89\n" \
"v_mfma_f32_16x16x32_bf16 v[172:175], a[172:175], v[104:107], v[172:175]\n" \
"v_exp_f32_e32 v90, v90\n" \
"v_smfmac_f32_16x16x64_bf16 a[196:199], a[212:215], v[104:111], v21\n" \
"v_mfma_f32_16x16x32_bf16 v[192:195], a[160:163], v[112:115], v[192:195]\n" \
"v_exp_f32_e32 v91, v91\n" \
"v_mfma_f32_16x16x32_bf16 v[192:195], a[176:179], v[116:119], v[192:195]\n" \
"v_exp_f32_e32 v92, v92\n" \
"v_mfma_f32_16x16x32_bf16 v[196:199], a[180:183], v[116:119], v[196:199]\n" \
"v_exp_f32_e32 v93, v93\n" \
"v_mfma_f32_16x16x32_bf16 v[196:199], a[164:167], v[112:115], v[196:199]\n" \
"v_exp_f32_e32 v94, v94\n" \
"v_mfma_f32_16x16x32_bf16 v[200:203], a[168:171], v[112:115], v[200:203]\n" \
"v_exp_f32_e32 v95, v95\n" \
"v_mfma_f32_16x16x32_bf16 v[200:203], a[184:187], v[116:119], v[200:203]\n" \
"v_cvt_pk_bf16_f32 v124, v88, v89\n" \
"v_cvt_pk_bf16_f32 v125, v90, v91\n" \
"v_mfma_f32_16x16x32_bf16 v[204:207], a[188:191], v[116:119], v[204:207]\n" \
"v_cvt_pk_bf16_f32 v126, v92, v93\n" \
"v_cvt_pk_bf16_f32 v127, v94, v95\n" \
"v_mfma_f32_16x16x32_bf16 v[204:207], a[172:175], v[112:115], v[204:207]\n" \
"v_smfmac_f32_16x16x64_bf16 a[200:203], a[212:215], v[112:119], v21\n" \
"s_waitcnt vmcnt(0) lgkmcnt(0)\n" \
"s_barrier\n" \
"v_mfma_f32_16x16x32_bf16 v[224:227], a[160:163], v[120:123], v[224:227]\n" \
"s_cbranch_scc1 L_h1_%=\n" \
"L_hr1_%=:\n" \
"v_mfma_f32_16x16x32_bf16 v[224:227], a[176:179], v[124:127], v[224:227]\n" \
"s_xor_b32 m0, s65, 0x12000\n" \
"ds_read_b128 a[160:163], v49 offset:8192\n" \
"v_mfma_f32_16x16x32_bf16 v[228:231], a[180:183], v[124:127], v[228:231]\n" \
"buffer_load_dwordx4 v50, s[8:11], s64 offen offset:0 sc1 lds\n" \
"v_mfma_f32_16x16x32_bf16 v[228:231], a[164:167], v[120:123], v[228:231]\n" \
"ds_read_b128 a[176:179], v49 offset:12288\n" \
"ds_read_b128 a[180:183], v49 offset:13312\n" \
"v_mfma_f32_16x16x32_bf16 v[232:235], a[168:171], v[120:123], v[232:235]\n" \
"v_mfma_f32_16x16x32_bf16 v[232:235], a[184:187], v[124:127], v[232:235]\n" \
"ds_read_b128 a[164:167], v49 offset:9216\n" \
"ds_read_b128 a[168:171], v49 offset:10240\n" \
"v_mfma_f32_16x16x32_bf16 v[236:239], a[188:191], v[124:127], v[236:239]\n" \
"v_mfma_f32_16x16x32_bf16 v[236:239], a[172:175], v[120:123], v[236:239]\n" \
"ds_read_b128 a[184:187], v49 offset:14336\n" \
"ds_read_b128 a[188:191], v49 offset:15360\n" \
"v_mfma_f32_16x16x32_bf16 v[64:67], a[128:131], a[0:3], v[32:35]\n" \
"v_mfma_f32_16x16x32_bf16 v[64:67], a[132:135], a[4:7], v[64:67]\n" \
"buffer_load_dwordx4 v50, s[8:11], s64 offen offset:1024 sc1 lds\n" \
"v_mfma_f32_16x16x32_bf16 v[64:67], a[136:139], a[8:11], v[64:67]\n" \
"s_xor_b32 m0, s65, 0x6000\n" \
"ds_read_b128 a[172:175], v49 offset:11264\n" \
"v_mfma_f32_16x16x32_bf16 v[64:67], a[140:143], a[12:15], v[64:67]\n" \
"v_add_u32_e32 v49, 0x10000, v48\n" \
"v_mfma_f32_16x16x32_bf16 v[68:71], a[156:159], a[12:15], v[32:35]\n" \
"v_mfma_f32_16x16x32_bf16 v[68:71], a[152:155], a[8:11], v[68:71]\n" \
"v_mfma_f32_16x16x32_bf16 v[68:71], a[148:151], a[4:7], v[68:71]\n" \
"v_mfma_f32_16x16x32_bf16 v[68:71], a[144:147], a[0:3], v[68:71]\n" \
"v_mfma_f32_16x16x32_bf16 v[72:75], a[128:131], a[16:19], v[36:39]\n" \
"v_mfma_f32_16x16x32_bf16 v[72:75], a[132:135], a[20:23], v[72:75]\n" \
"v_mfma_f32_16x16x32_bf16 v[72:75], a[136:139], a[24:27], v[72:75]\n" \
"buffer_load_dwordx4 v51, s[8:11], s64 offen offset:0 sc1 lds\n" \
"v_mfma_f32_16x16x32_bf16 v[72:75], a[140:143], a[28:31], v[72:75]\n" \
"v_exp_f32_e32 v64, v64\n" \
"v_mfma_f32_16x16x32_bf16 v[76:79], a[156:159], a[28:31], v[36:39]\n" \
"v_exp_f32_e32 v65, v65\n" \
"v_mfma_f32_16x16x32_bf16 v[76:79], a[152:155], a[24:27], v[76:79]\n" \
"v_exp_f32_e32 v66, v66\n" \
"v_mfma_f32_16x16x32_bf16 v[76:79], a[148:151], a[20:23], v[76:79]\n" \
"v_exp_f32_e32 v67, v67\n" \
"v_mfma_f32_16x16x32_bf16 v[76:79], a[144:147], a[16:19], v[76:79]\n" \
"v_exp_f32_e32 v68, v68\n" \
"v_mfma_f32_16x16x32_bf16 v[80:83], a[128:131], a[32:35], v[40:43]\n" \
"v_exp_f32_e32 v69, v69\n" \
"v_mfma_f32_16x16x32_bf16 v[80:83], a[132:135], a[36:39], v[80:83]\n" \
"v_exp_f32_e32 v70, v70\n" \
"v_mfma_f32_16x16x32_bf16 v[80:83], a[136:139], a[40:43], v[80:83]\n" \
"v_exp_f32_e32 v71, v71\n" \
"v_mfma_f32_16x16x32_bf16 v[80:83], a[140:143], a[44:47], v[80:83]\n" \
"buffer_load_dwordx4 v51, s[8:11], s64 offen offset:1024 sc1 lds\n" \
"v_mfma_f32_16x16x32_bf16 v[84:87], a[156:159], a[44:47], v[40:43]\n" \
"v_exp_f32_e32 v72, v72\n" \
"v_mfma_f32_16x16x32_bf16 v[84:87], a[152:155], a[40:43], v[84:87]\n" \
"v_exp_f32_e32 v73, v73\n" \
"v_mfma_f32_16x16x32_bf16 v[84:87], a[148:151], a[36:39], v[84:87]\n" \
"v_exp_f32_e32 v74, v74\n" \
"v_mfma_f32_16x16x32_bf16 v[84:87], a[144:147], a[32:35], v[84:87]\n" \
"v_exp_f32_e32 v75, v75\n" \
"v_mfma_f32_16x16x32_bf16 v[88:91], a[128:131], a[48:51], v[44:47]\n" \
"s_xor_b32 m0, s65, 0x14000\n" \
"ds_read_b128 a[128:131], v48 offset:8192\n" \
"v_mfma_f32_16x16x32_bf16 v[88:91], a[132:135], a[52:55], v[88:91]\n" \
"v_exp_f32_e32 v76, v76\n" \
"v_mfma_f32_16x16x32_bf16 v[88:91], a[136:139], a[56:59], v[88:91]\n" \
"ds_read_b128 a[132:135], v48 offset:9216\n" \
"ds_read_b128 a[136:139], v48 offset:10240\n" \
"v_mfma_f32_16x16x32_bf16 v[88:91], a[140:143], a[60:63], v[88:91]\n" \
"v_exp_f32_e32 v77, v77\n" \
"v_mfma_f32_16x16x32_bf16 v[92:95], a[156:159], a[60:63], v[44:47]\n" \
"buffer_load_dwordx4 v52, s[8:11], s64 offen offset:0 sc1 lds\n" \
"v_mfma_f32_16x16x32_bf16 v[92:95], a[152:155], a[56:59], v[92:95]\n" \
"ds_read_b128 a[140:143], v48 offset:11264\n" \
"ds_read_b128 a[152:155], v48 offset:14336\n" \
"v_mfma_f32_16x16x32_bf16 v[92:95], a[148:151], a[52:55], v[92:95]\n" \
"v_exp_f32_e32 v78, v78\n" \
"v_mfma_f32_16x16x32_bf16 v[92:95], a[144:147], a[48:51], v[92:95]\n" \
"ds_read_b128 a[144:147], v48 offset:12288\n" \
"s_waitcnt lgkmcnt(6)\n" \
"v_mfma_f32_16x16x32_bf16 v[144:147], a[160:163], v[96:99], v[144:147]\n" \
"ds_read_b128 a[148:151], v48 offset:13312\n" \
"ds_read_b128 a[156:159], v48 offset:15360\n" \
"v_mfma_f32_16x16x32_bf16 v[144:147], a[176:179], v[100:103], v[144:147]\n" \
"v_exp_f32_e32 v79, v79\n" \
"v_mfma_f32_16x16x32_bf16 v[148:151], a[180:183], v[100:103], v[148:151]\n" \
"v_exp_f32_e32 v80, v80\n" \
"v_mfma_f32_16x16x32_bf16 v[148:151], a[164:167], v[96:99], v[148:151]\n" \
"v_exp_f32_e32 v81, v81\n" \
"v_mfma_f32_16x16x32_bf16 v[152:155], a[168:171], v[96:99], v[152:155]\n" \
"buffer_load_dwordx4 v52, s[8:11], s64 offen offset:1024 sc1 lds\n" \
"v_mfma_f32_16x16x32_bf16 v[152:155], a[184:187], v[100:103], v[152:155]\n" \
"s_mov_b32 m0, s65\n" \
"s_xor_b32 s65, s65, 0x4000\n" \
"v_mfma_f32_16x16x32_bf16 v[156:159], a[188:191], v[100:103], v[156:159]\n" \
"v_exp_f32_e32 v82, v82\n" \
"v_mfma_f32_16x16x32_bf16 v[156:159], a[172:175], v[96:99], v[156:159]\n" \
"v_exp_f32_e32 v83, v83\n" \
"v_mfma_f32_16x16x32_bf16 v[176:179], a[160:163], v[104:107], v[176:179]\n" \
"v_exp_f32_e32 v84, v84\n" \
"v_mfma_f32_16x16x32_bf16 v[176:179], a[176:179], v[108:111], v[176:179]\n" \
"v_exp_f32_e32 v85, v85\n" \
"v_mfma_f32_16x16x32_bf16 v[180:183], a[180:183], v[108:111], v[180:183]\n" \
"v_exp_f32_e32 v86, v86\n" \
"v_mfma_f32_16x16x32_bf16 v[180:183], a[164:167], v[104:107], v[180:183]\n" \
"v_exp_f32_e32 v87, v87\n" \
"v_mfma_f32_16x16x32_bf16 v[184:187], a[168:171], v[104:107], v[184:187]\n" \
"v_exp_f32_e32 v88, v88\n" \
"v_mfma_f32_16x16x32_bf16 v[184:187], a[184:187], v[108:111], v[184:187]\n" \
"buffer_load_dwordx4 v53, s[8:11], s64 offen offset:0 sc1 lds\n" \
"v_mfma_f32_16x16x32_bf16 v[188:191], a[188:191], v[108:111], v[188:191]\n" \
"v_exp_f32_e32 v89, v89\n" \
"v_mfma_f32_16x16x32_bf16 v[188:191], a[172:175], v[104:107], v[188:191]\n" \
"v_exp_f32_e32 v90, v90\n" \
"v_mfma_f32_16x16x32_bf16 v[208:211], a[160:163], v[112:115], v[208:211]\n" \
"v_exp_f32_e32 v91, v91\n" \
"v_mfma_f32_16x16x32_bf16 v[208:211], a[176:179], v[116:119], v[208:211]\n" \
"v_cvt_pk_bf16_f32 v96, v64, v65\n" \
"v_cvt_pk_bf16_f32 v97, v66, v67\n" \
"v_mfma_f32_16x16x32_bf16 v[212:215], a[180:183], v[116:119], v[212:215]\n" \
"v_cvt_pk_bf16_f32 v98, v68, v69\n" \
"v_cvt_pk_bf16_f32 v99, v70, v71\n" \
"v_mfma_f32_16x16x32_bf16 v[212:215], a[164:167], v[112:115], v[212:215]\n" \
"v_cvt_pk_bf16_f32 v104, v72, v73\n" \
"v_cvt_pk_bf16_f32 v105, v74, v75\n" \
"v_mfma_f32_16x16x32_bf16 v[216:219], a[168:171], v[112:115], v[216:219]\n" \
"v_cvt_pk_bf16_f32 v106, v76, v77\n" \
"v_cvt_pk_bf16_f32 v107, v78, v79\n" \
"v_mfma_f32_16x16x32_bf16 v[216:219], a[184:187], v[116:119], v[216:219]\n" \
"v_xor_b32_e32 v48, 0x4000, v48\n" \
"v_mfma_f32_16x16x32_bf16 v[220:223], a[188:191], v[116:119], v[220:223]\n" \
"buffer_load_dwordx4 v53, s[8:11], s64 offen offset:1024 sc1 lds\n" \
"v_mfma_f32_16x16x32_bf16 v[220:223], a[172:175], v[112:115], v[220:223]\n" \
"s_add_u32 s64, s64, 0x8000\n" \
"s_sub_u32 s66, s66, 1\n" \
"v_mfma_f32_16x16x32_bf16 v[240:243], a[160:163], v[120:123], v[240:243]\n" \
"v_cvt_pk_bf16_f32 v112, v80, v81\n" \
"v_cvt_pk_bf16_f32 v113, v82, v83\n" \
"v_mfma_f32_16x16x32_bf16 v[240:243], a[176:179], v[124:127], v[240:243]\n" \
"v_cvt_pk_bf16_f32 v114, v84, v85\n" \
"v_cvt_pk_bf16_f32 v115, v86, v87\n" \
"v_mfma_f32_16x16x32_bf16 v[244:247], a[180:183], v[124:127], v[244:247]\n" \
"v_mfma_f32_16x16x32_bf16 v[244:247], a[164:167], v[120:123], v[244:247]\n" \
"v_mfma_f32_16x16x32_bf16 v[248:251], a[168:171], v[120:123], v[248:251]\n" \
"v_mfma_f32_16x16x32_bf16 v[248:251], a[184:187], v[124:127], v[248:251]\n" \
"s_cbranch_scc0 L_t1_%=\n" \
"v_mfma_f32_16x16x32_bf16 v[252:255], a[188:191], v[124:127], v[252:255]\n" \
"v_mfma_f32_16x16x32_bf16 v[252:255], a[172:175], v[120:123], v[252:255]\n" \
"v_smfmac_f32_16x16x64_bf16 a[204:207], a[212:215], v[120:127], v21\n" \
"s_waitcnt lgkmcnt(0)\n" \
"s_barrier\n" \
"L_tr_%=:\n" \
"L_pf11:\n" \
"s_cbranch_execz L_pf15\n" \
"s_nop 0\n" \
"s_cmp_lg_u32 s16, 0\n" \
"s_cbranch_scc1 L_sw_%=\n" \
"v_mfma_f32_16x16x32_bf16 v[64:67], a[128:131], a[0:3], v[32:35]\n" \
"s_xor_b32 m0, s65, 0x12000\n" \
"ds_read_b128 a[160:163], v49 offset:0\n" \
"v_mfma_f32_16x16x32_bf16 v[64:67], a[132:135], a[4:7], v[64:67]\n" \
"buffer_load_dwordx4 v50, s[8:11], s64 offen offset:0 sc1 lds\n" \
"v_mfma_f32_16x16x32_bf16 v[64:67], a[136:139], a[8:11], v[64:67]\n" \
"ds_read_b128 a[176:179], v49 offset:4096\n" \
"ds_read_b128 a[180:183], v49 offset:5120\n" \
"v_mfma_f32_16x16x32_bf16 v[64:67], a[140:143], a[12:15], v[64:67]\n" \
"s_mov_b32 s65, s13\n" \
"v_add_u32_e32 v63, s15, v20\n" \
"v_mfma_f32_16x16x32_bf16 v[68:71], a[156:159], a[12:15], v[32:35]\n" \
"s_add_u32 s67, s14, 40960\n" \
"ds_read_b128 a[164:167], v49 offset:1024\n" \
"v_mfma_f32_16x16x32_bf16 v[68:71], a[152:155], a[8:11], v[68:71]\n" \
"ds_read_b128 a[168:171], v49 offset:2048\n" \
"ds_read_b128 a[184:187], v49 offset:6144\n" \
"v_mfma_f32_16x16x32_bf16 v[68:71], a[148:151], a[4:7], v[68:71]\n" \
"buffer_load_dwordx4 v50, s[8:11], s64 offen offset:1024 sc1 lds\n" \
"v_mfma_f32_16x16x32_bf16 v[68:71], a[144:147], a[0:3], v[68:71]\n" \
"v_add_u32_e32 v50, 0x6000, v63\n" \
"s_xor_b32 m0, s65, 0x8000\n" \
"v_mfma_f32_16x16x32_bf16 v[72:75], a[128:131], a[16:19], v[36:39]\n" \
"v_cvt_pk_bf16_f32 v120, v88, v89\n" \
"v_cvt_pk_bf16_f32 v121, v90, v91\n" \
"v_mfma_f32_16x16x32_bf16 v[72:75], a[132:135], a[20:23], v[72:75]\n" \
"ds_read_b128 a[188:191], v49 offset:7168\n" \
"ds_read_b128 a[172:175], v49 offset:3072\n" \
"v_mfma_f32_16x16x32_bf16 v[72:75], a[136:139], a[24:27], v[72:75]\n" \
"v_exp_f32_e32 v92, v92\n" \
"v_mfma_f32_16x16x32_bf16 v[72:75], a[140:143], a[28:31], v[72:75]\n" \
"v_exp_f32_e32 v93, v93\n" \
"v_mfma_f32_16x16x32_bf16 v[76:79], a[156:159], a[28:31], v[36:39]\n" \
"buffer_load_dwordx4 v50, s[8:11], s67 offen offset:0 sc1 lds\n" \
"v_mfma_f32_16x16x32_bf16 v[76:79], a[152:155], a[24:27], v[76:79]\n" \
"v_exp_f32_e32 v94, v94\n" \
"v_mfma_f32_16x16x32_bf16 v[76:79], a[148:151], a[20:23], v[76:79]\n" \
"v_exp_f32_e32 v95, v95\n" \
"v_mfma_f32_16x16x32_bf16 v[76:79], a[144:147], a[16:19], v[76:79]\n" \
"v_cvt_pk_bf16_f32 v122, v92, v93\n" \
"v_cvt_pk_bf16_f32 v123, v94, v95\n" \
"v_mfma_f32_16x16x32_bf16 v[80:83], a[128:131], a[32:35], v[40:43]\n" \
"v_exp_f32_e32 v64, v64\n" \
"v_mfma_f32_16x16x32_bf16 v[80:83], a[132:135], a[36:39], v[80:83]\n" \
"v_exp_f32_e32 v65, v65\n" \
"v_mfma_f32_16x16x32_bf16 v[80:83], a[136:139], a[40:43], v[80:83]\n" \
"buffer_load_dwordx4 v50, s[8:11], s67 offen offset:1024 sc1 lds\n" \
"v_mfma_f32_16x16x32_bf16 v[80:83], a[140:143], a[44:47], v[80:83]\n" \
"v_exp_f32_e32 v66, v66\n" \
"v_mfma_f32_16x16x32_bf16 v[84:87], a[156:159], a[44:47], v[40:43]\n" \
"v_exp_f32_e32 v67, v67\n" \
"v_mfma_f32_16x16x32_bf16 v[84:87], a[152:155], a[40:43], v[84:87]\n" \
"v_exp_f32_e32 v68, v68\n" \
"v_mfma_f32_16x16x32_bf16 v[84:87], a[148:151], a[36:39], v[84:87]\n" \
"v_exp_f32_e32 v69, v69\n" \
"v_mfma_f32_16x16x32_bf16 v[84:87], a[144:147], a[32:35], v[84:87]\n" \
"v_exp_f32_e32 v70, v70\n" \
"v_mfma_f32_16x16x32_bf16 v[88:91], a[128:131], a[48:51], v[44:47]\n" \
"v_exp_f32_e32 v71, v71\n" \
"v_mfma_f32_16x16x32_bf16 v[88:91], a[132:135], a[52:55], v[88:91]\n" \
"v_cvt_pk_bf16_f32 v100, v64, v65\n" \
"v_cvt_pk_bf16_f32 v101, v66, v67\n" \
"v_mfma_f32_16x16x32_bf16 v[88:91], a[136:139], a[56:59], v[88:91]\n" \
"v_cvt_pk_bf16_f32 v102, v68, v69\n" \
"v_cvt_pk_bf16_f32 v103, v70, v71\n" \
"v_mfma_f32_16x16x32_bf16 v[88:91], a[140:143], a[60:63], v[88:91]\n" \
"buffer_load_dwordx4 v50, s[8:11], s67 offen offset:2048 sc1 lds\n" \
"v_mfma_f32_16x16x32_bf16 v[92:95], a[156:159], a[60:63], v[44:47]\n" \
"v_exp_f32_e32 v72, v72\n" \
"v_mfma_f32_16x16x32_bf16 v[92:95], a[152:155], a[56:59], v[92:95]\n" \
"v_exp_f32_e32 v73, v73\n" \
"v_mfma_f32_16x16x32_bf16 v[92:95], a[148:151], a[52:55], v[92:95]\n" \
"v_exp_f32_e32 v74, v74\n" \
"v_mfma_f32_16x16x32_bf16 v[92:95], a[144:147], a[48:51], v[92:95]\n" \
"v_cvt_pk_bf16_f32 v108, v72, v73\n" \
"s_waitcnt lgkmcnt(0)\n" \
"v_mfma_f32_16x16x32_bf16 v[128:131], a[160:163], v[96:99], v[128:131]\n" \
"buffer_load_dwordx4 v50, s[8:11], s67 offen offset:3072 sc1 lds\n" \
"v_mfma_f32_16x16x32_bf16 v[128:131], a[176:179], v[100:103], v[128:131]\n" \
"v_exp_f32_e32 v75, v75\n" \
"v_mfma_f32_16x16x32_bf16 v[132:135], a[180:183], v[100:103], v[132:135]\n" \
"v_exp_f32_e32 v76, v76\n" \
"v_mfma_f32_16x16x32_bf16 v[132:135], a[164:167], v[96:99], v[132:135]\n" \
"v_exp_f32_e32 v77, v77\n" \
"v_mfma_f32_16x16x32_bf16 v[136:139], a[168:171], v[96:99], v[136:139]\n" \
"v_exp_f32_e32 v78, v78\n" \
"v_mfma_f32_16x16x32_bf16 v[136:139], a[184:187], v[100:103], v[136:139]\n" \
"v_exp_f32_e32 v79, v79\n" \
"v_mfma_f32_16x16x32_bf16 v[140:143], a[188:191], v[100:103], v[140:143]\n" \
"v_cvt_pk_bf16_f32 v109, v74, v75\n" \
"v_cvt_pk_bf16_f32 v110, v76, v77\n" \
"v_mfma_f32_16x16x32_bf16 v[140:143], a[172:175], v[96:99], v[140:143]\n" \
"v_cvt_pk_bf16_f32 v111, v78, v79\n" \
"s_mov_b32 s64, s14\n" \
"v_smfmac_f32_16x16x64_bf16 a[192:195], a[212:215], v[96:103], v21\n" \
"v_mfma_f32_16x16x32_bf16 v[160:163], a[160:163], v[104:107], v[160:163]\n" \
"v_add_u32_e32 v51, 0x12000, v63\n" \
"s_xor_b32 m0, s65, 0xa000\n" \
"v_mfma_f32_16x16x32_bf16 v[160:163], a[176:179], v[108:111], v[160:163]\n" \
"buffer_load_dwordx4 v51, s[8:11], s64 offen offset:0 sc1 lds\n" \
"v_mfma_f32_16x16x32_bf16 v[164:167], a[180:183], v[108:111], v[164:167]\n" \
"v_exp_f32_e32 v80, v80\n" \
"v_mfma_f32_16x16x32_bf16 v[164:167], a[164:167], v[104:107], v[164:167]\n" \
"v_exp_f32_e32 v81, v81\n" \
"v_mfma_f32_16x16x32_bf16 v[168:171], a[168:171], v[104:107], v[168:171]\n" \
"v_exp_f32_e32 v82, v82\n" \
"v_mfma_f32_16x16x32_bf16 v[168:171], a[184:187], v[108:111], v[168:171]\n" \
"v_exp_f32_e32 v83, v83\n" \
"v_mfma_f32_16x16x32_bf16 v[172:175], a[188:191], v[108:111], v[172:175]\n" \
"v_exp_f32_e32 v84, v84\n" \
"v_mfma_f32_16x16x32_bf16 v[172:175], a[172:175], v[104:107], v[172:175]\n" \
"v_exp_f32_e32 v85, v85\n" \
"v_smfmac_f32_16x16x64_bf16 a[196:199], a[212:215], v[104:111], v21\n" \
"v_mfma_f32_16x16x32_bf16 v[192:195], a[160:163], v[112:115], v[192:195]\n" \
"v_exp_f32_e32 v86, v86\n" \
"v_exp_f32_e32 v87, v87\n" \
"v_cvt_pk_bf16_f32 v116, v80, v81\n" \
"v_cvt_pk_bf16_f32 v117, v82, v83\n" \
"v_cvt_pk_bf16_f32 v118, v84, v85\n" \
"v_cvt_pk_bf16_f32 v119, v86, v87\n" \
"v_exp_f32_e32 v88, v88\n" \
"v_exp_f32_e32 v89, v89\n" \
"v_mfma_f32_16x16x32_bf16 v[192:195], a[176:179], v[116:119], v[192:195]\n" \
"buffer_load_dwordx4 v51, s[8:11], s64 offen offset:1024 sc1 lds\n" \
"v_mfma_f32_16x16x32_bf16 v[196:199], a[180:183], v[116:119], v[196:199]\n" \
"ds_read_b128 a[128:131], v48 offset:0\n" \
"ds_read_b128 a[132:135], v48 offset:1024\n" \
"v_mfma_f32_16x16x32_bf16 v[196:199], a[164:167], v[112:115], v[196:199]\n" \
"ds_read_b128 a[136:139], v48 offset:2048\n" \
"ds_read_b128 a[140:143], v48 offset:3072\n" \
"ds_read_b128 a[144:147], v48 offset:4096\n" \
"ds_read_b128 a[148:151], v48 offset:5120\n" \
"ds_read_b128 a[152:155], v48 offset:6144\n" \
"ds_read_b128 a[156:159], v48 offset:7168\n" \
"v_mfma_f32_16x16x32_bf16 v[200:203], a[168:171], v[112:115], v[200:203]\n" \
"v_exp_f32_e32 v90, v90\n" \
"v_mfma_f32_16x16x32_bf16 v[200:203], a[184:187], v[116:119], v[200:203]\n" \
"v_exp_f32_e32 v91, v91\n" \
"v_mfma_f32_16x16x32_bf16 v[204:207], a[188:191], v[116:119], v[204:207]\n" \
"v_exp_f32_e32 v92, v92\n" \
"v_mfma_f32_16x16x32_bf16 v[204:207], a[172:175], v[112:115], v[204:207]\n" \
"v_exp_f32_e32 v93, v93\n" \
"v_smfmac_f32_16x16x64_bf16 a[200:203], a[212:215], v[112:119], v21\n" \
"v_mfma_f32_16x16x32_bf16 v[224:227], a[160:163], v[120:123], v[224:227]\n" \
"v_exp_f32_e32 v94, v94\n" \
"v_exp_f32_e32 v95, v95\n" \
"v_cvt_pk_bf16_f32 v124, v88, v89\n" \
"v_cvt_pk_bf16_f32 v125, v90, v91\n" \
"v_cvt_pk_bf16_f32 v126, v92, v93\n" \
"v_cvt_pk_bf16_f32 v127, v94, v95\n" \
"v_add_u32_e32 v52, 0x14000, v63\n" \
"v_add_u32_e32 v53, 0x20000, v63\n" \
"v_mfma_f32_16x16x32_bf16 v[224:227], a[176:179], v[124:127], v[224:227]\n" \
"buffer_load_dwordx4 v51, s[8:11], s64 offen offset:2048 sc1 lds\n" \
"v_mfma_f32_16x16x32_bf16 v[228:231], a[180:183], v[124:127], v[228:231]\n" \
"buffer_load_dwordx4 v51, s[8:11], s64 offen offset:3072 sc1 lds\n" \
"v_mfma_f32_16x16x32_bf16 v[228:231], a[164:167], v[120:123], v[228:231]\n" \
"v_readlane_b32 s93, v11, 17\n" \
"v_readlane_b32 s92, v11, 22\n" \
"v_mfma_f32_16x16x32_bf16 v[232:235], a[168:171], v[120:123], v[232:235]\n" \
"s_mov_b32 s66, s19\n" \
"v_mfma_f32_16x16x32_bf16 v[232:235], a[184:187], v[124:127], v[232:235]\n" \
"v_mfma_f32_16x16x32_bf16 v[236:239], a[188:191], v[124:127], v[236:239]\n" \
"v_mfma_f32_16x16x32_bf16 v[236:239], a[172:175], v[120:123], v[236:239]\n" \
"s_waitcnt vmcnt(10) lgkmcnt(0)\n" \
"s_barrier\n" \
"v_mfma_f32_16x16x32_bf16 v[64:67], a[128:131], a[0:3], v[32:35]\n" \
"s_xor_b32 m0, s65, 0x18000\n" \
"ds_read_b128 a[160:163], v49 offset:8192\n" \
"v_mfma_f32_16x16x32_bf16 v[64:67], a[132:135], a[4:7], v[64:67]\n" \
"buffer_load_dwordx4 v52, s[8:11], s64 offen offset:0 sc1 lds\n" \
"v_mfma_f32_16x16x32_bf16 v[64:67], a[136:139], a[8:11], v[64:67]\n" \
"ds_read_b128 a[176:179], v49 offset:12288\n" \
"ds_read_b128 a[180:183], v49 offset:13312\n" \
"v_mfma_f32_16x16x32_bf16 v[64:67], a[140:143], a[12:15], v[64:67]\n" \
"buffer_load_dwordx4 v52, s[8:11], s64 offen offset:1024 sc1 lds\n" \
"v_mfma_f32_16x16x32_bf16 v[68:71], a[156:159], a[12:15], v[32:35]\n" \
"ds_read_b128 a[164:167], v49 offset:9216\n" \
"ds_read_b128 a[168:171], v49 offset:10240\n" \
"v_mfma_f32_16x16x32_bf16 v[68:71], a[152:155], a[8:11], v[68:71]\n" \
"ds_read_b128 a[184:187], v49 offset:14336\n" \
"ds_read_b128 a[188:191], v49 offset:15360\n" \
"v_mfma_f32_16x16x32_bf16 v[68:71], a[148:151], a[4:7], v[68:71]\n" \
"v_exp_f32_e32 v64, v64\n" \
"v_mfma_f32_16x16x32_bf16 v[68:71], a[144:147], a[0:3], v[68:71]\n" \
"buffer_load_dwordx4 v52, s[8:11], s64 offen offset:2048 sc1 lds\n" \
"v_mfma_f32_16x16x32_bf16 v[72:75], a[128:131], a[16:19], v[36:39]\n" \
"L_pf12:\n" \
"s_cbranch_execz L_pf16\n" \
"s_nop 0\n" \
"v_exp_f32_e32 v65, v65\n" \
"v_mfma_f32_16x16x32_bf16 v[72:75], a[132:135], a[20:23], v[72:75]\n" \
"v_exp_f32_e32 v66, v66\n" \
"v_mfma_f32_16x16x32_bf16 v[72:75], a[136:139], a[24:27], v[72:75]\n" \
"buffer_load_dwordx4 v52, s[8:11], s64 offen offset:3072 sc1 lds\n" \
"v_mfma_f32_16x16x32_bf16 v[72:75], a[140:143], a[28:31], v[72:75]\n" \
"s_mov_b32 m0, s65\n" \
"ds_read_b128 a[172:175], v49 offset:11264\n" \
"v_mfma_f32_16x16x32_bf16 v[76:79], a[156:159], a[28:31], v[36:39]\n" \
"v_exp_f32_e32 v67, v67\n" \
"v_mfma_f32_16x16x32_bf16 v[76:79], a[152:155], a[24:27], v[76:79]\n" \
"v_exp_f32_e32 v68, v68\n" \
"v_mfma_f32_16x16x32_bf16 v[76:79], a[148:151], a[20:23], v[76:79]\n" \
"buffer_load_dwordx4 v53, s[8:11], s64 offen offset:0 sc1 lds\n" \
"v_mfma_f32_16x16x32_bf16 v[76:79], a[144:147], a[16:19], v[76:79]\n" \
"v_exp_f32_e32 v69, v69\n" \
"v_mfma_f32_16x16x32_bf16 v[80:83], a[128:131], a[32:35], v[40:43]\n" \
"v_exp_f32_e32 v70, v70\n" \
"v_mfma_f32_16x16x32_bf16 v[80:83], a[132:135], a[36:39], v[80:83]\n" \
"buffer_load_dwordx4 v53, s[8:11], s64 offen offset:1024 sc1 lds\n" \
"v_mfma_f32_16x16x32_bf16 v[80:83], a[136:139], a[40:43], v[80:83]\n" \
"v_exp_f32_e32 v71, v71\n" \
"v_mfma_f32_16x16x32_bf16 v[80:83], a[140:143], a[44:47], v[80:83]\n" \
"v_exp_f32_e32 v72, v72\n" \
"v_mfma_f32_16x16x32_bf16 v[84:87], a[156:159], a[44:47], v[40:43]\n" \
"v_exp_f32_e32 v73, v73\n" \
"v_mfma_f32_16x16x32_bf16 v[84:87], a[152:155], a[40:43], v[84:87]\n" \
"buffer_load_dwordx4 v53, s[8:11], s64 offen offset:2048 sc1 lds\n" \
"v_mfma_f32_16x16x32_bf16 v[84:87], a[148:151], a[36:39], v[84:87]\n" \
"v_exp_f32_e32 v74, v74\n" \
"v_mfma_f32_16x16x32_bf16 v[84:87], a[144:147], a[32:35], v[84:87]\n" \
"v_exp_f32_e32 v75, v75\n" \
"v_mfma_f32_16x16x32_bf16 v[88:91], a[128:131], a[48:51], v[44:47]\n" \
"buffer_load_dwordx4 v53, s[8:11], s64 offen offset:3072 sc1 lds\n" \
"v_mfma_f32_16x16x32_bf16 v[88:91], a[132:135], a[52:55], v[88:91]\n" \
"s_add_u32 m0, s93, 0x0\n" \
"ds_read_b128 a[128:131], v48 offset:8192\n" \
"v_mfma_f32_16x16x32_bf16 v[88:91], a[136:139], a[56:59], v[88:91]\n" \
"ds_read_b128 a[132:135], v48 offset:9216\n" \
"ds_read_b128 a[136:139], v48 offset:10240\n" \
"v_mfma_f32_16x16x32_bf16 v[88:91], a[140:143], a[60:63], v[88:91]\n" \
"ds_read_b128 a[140:143], v48 offset:11264\n" \
"v_add_u32_e32 v49, 0x10000, v48\n" \
"v_mfma_f32_16x16x32_bf16 v[92:95], a[156:159], a[60:63], v[44:47]\n" \
"buffer_load_dwordx4 v0, s[80:83], s92 offen offset:0 lds\n" \
"v_mfma_f32_16x16x32_bf16 v[92:95], a[152:155], a[56:59], v[92:95]\n" \
"ds_read_b128 a[152:155], v48 offset:14336\n" \
"ds_read_b128 a[156:159], v48 offset:15360\n" \
"v_mfma_f32_16x16x32_bf16 v[92:95], a[148:151], a[52:55], v[92:95]\n" \
"ds_read_b128 a[148:151], v48 offset:13312\n" \
"s_xor_b32 s65, s65, 0x8000\n" \
"v_mfma_f32_16x16x32_bf16 v[92:95], a[144:147], a[48:51], v[92:95]\n" \
"ds_read_b128 a[144:147], v48 offset:12288\n" \
"s_waitcnt lgkmcnt(8)\n" \
"v_mfma_f32_16x16x32_bf16 v[144:147], a[160:163], v[96:99], v[144:147]\n" \
"buffer_load_dwordx4 v1, s[80:83], s92 offen offset:1024 lds\n" \
"v_mfma_f32_16x16x32_bf16 v[144:147], a[176:179], v[100:103], v[144:147]\n" \
"v_exp_f32_e32 v76, v76\n" \
"v_mfma_f32_16x16x32_bf16 v[148:151], a[180:183], v[100:103], v[148:151]\n" \
"v_exp_f32_e32 v77, v77\n" \
"v_mfma_f32_16x16x32_bf16 v[148:151], a[164:167], v[96:99], v[148:151]\n" \
"buffer_load_dwordx4 v2, s[80:83], s92 offen offset:2048 lds\n" \
"v_mfma_f32_16x16x32_bf16 v[152:155], a[168:171], v[96:99], v[152:155]\n" \
"v_exp_f32_e32 v78, v78\n" \
"v_mfma_f32_16x16x32_bf16 v[152:155], a[184:187], v[100:103], v[152:155]\n" \
"v_exp_f32_e32 v79, v79\n" \
"v_mfma_f32_16x16x32_bf16 v[156:159], a[188:191], v[100:103], v[156:159]\n" \
"buffer_load_dwordx4 v3, s[80:83], s92 offen offset:3072 lds\n" \
"v_mfma_f32_16x16x32_bf16 v[156:159], a[172:175], v[96:99], v[156:159]\n" \
"s_add_u32 m0, s93, 0x1000\n" \
"v_cvt_pk_bf16_f32 v96, v64, v65\n" \
"v_mfma_f32_16x16x32_bf16 v[176:179], a[160:163], v[104:107], v[176:179]\n" \
"v_exp_f32_e32 v80, v80\n" \
"v_mfma_f32_16x16x32_bf16 v[176:179], a[176:179], v[108:111], v[176:179]\n" \
"v_exp_f32_e32 v81, v81\n" \
"v_mfma_f32_16x16x32_bf16 v[180:183], a[180:183], v[108:111], v[180:183]\n" \
"buffer_load_dwordx4 v4, s[80:83], s92 offen offset:0 lds\n" \
"v_mfma_f32_16x16x32_bf16 v[180:183], a[164:167], v[104:107], v[180:183]\n" \
"v_exp_f32_e32 v82, v82\n" \
"v_mfma_f32_16x16x32_bf16 v[184:187], a[168:171], v[104:107], v[184:187]\n" \
"v_exp_f32_e32 v83, v83\n" \
"v_mfma_f32_16x16x32_bf16 v[184:187], a[184:187], v[108:111], v[184:187]\n" \
"buffer_load_dwordx4 v5, s[80:83], s92 offen offset:1024 lds\n" \
"v_mfma_f32_16x16x32_bf16 v[188:191], a[188:191], v[108:111], v[188:191]\n" \
"v_exp_f32_e32 v84, v84\n" \
"v_mfma_f32_16x16x32_bf16 v[188:191], a[172:175], v[104:107], v[188:191]\n" \
"v_exp_f32_e32 v85, v85\n" \
"v_mfma_f32_16x16x32_bf16 v[208:211], a[160:163], v[112:115], v[208:211]\n" \
"v_exp_f32_e32 v86, v86\n" \
"v_mfma_f32_16x16x32_bf16 v[208:211], a[176:179], v[116:119], v[208:211]\n" \
"buffer_load_dwordx4 v6, s[80:83], s92 offen offset:2048 lds\n" \
"v_mfma_f32_16x16x32_bf16 v[212:215], a[180:183], v[116:119], v[212:215]\n" \
"v_exp_f32_e32 v87, v87\n" \
"v_mfma_f32_16x16x32_bf16 v[212:215], a[164:167], v[112:115], v[212:215]\n" \
"v_exp_f32_e32 v88, v88\n" \
"v_mfma_f32_16x16x32_bf16 v[216:219], a[168:171], v[112:115], v[216:219]\n" \
"buffer_load_dwordx4 v7, s[80:83], s92 offen offset:3072 lds\n" \
"v_mfma_f32_16x16x32_bf16 v[216:219], a[184:187], v[116:119], v[216:219]\n" \
"s_add_u32 m0, s93, 0x2000\n" \
"v_cvt_pk_bf16_f32 v97, v66, v67\n" \
"v_mfma_f32_16x16x32_bf16 v[220:223], a[188:191], v[116:119], v[220:223]\n" \
"v_exp_f32_e32 v89, v89\n" \
"v_mfma_f32_16x16x32_bf16 v[220:223], a[172:175], v[112:115], v[220:223]\n" \
"v_exp_f32_e32 v90, v90\n" \
"v_mfma_f32_16x16x32_bf16 v[240:243], a[160:163], v[120:123], v[240:243]\n" \
"buffer_load_dwordx4 v8, s[80:83], s92 offen offset:0 lds\n" \
"v_mfma_f32_16x16x32_bf16 v[240:243], a[176:179], v[124:127], v[240:243]\n" \
"v_exp_f32_e32 v91, v91\n" \
"v_mfma_f32_16x16x32_bf16 v[244:247], a[180:183], v[124:127], v[244:247]\n" \
"v_cvt_pk_bf16_f32 v98, v68, v69\n" \
"v_cvt_pk_bf16_f32 v99, v70, v71\n" \
"v_mfma_f32_16x16x32_bf16 v[244:247], a[164:167], v[120:123], v[244:247]\n" \
"v_cvt_pk_bf16_f32 v104, v72, v73\n" \
"v_cvt_pk_bf16_f32 v105, v74, v75\n" \
"v_mfma_f32_16x16x32_bf16 v[248:251], a[168:171], v[120:123], v[248:251]\n" \
"v_cvt_pk_bf16_f32 v106, v76, v77\n" \
"v_cvt_pk_bf16_f32 v107, v78, v79\n" \
"v_mfma_f32_16x16x32_bf16 v[248:251], a[184:187], v[124:127], v[248:251]\n" \
"v_cvt_pk_bf16_f32 v112, v80, v81\n" \
"v_cvt_pk_bf16_f32 v113, v82, v83\n" \
"v_mfma_f32_16x16x32_bf16 v[252:255], a[188:191], v[124:127], v[252:255]\n" \
"v_cvt_pk_bf16_f32 v114, v84, v85\n" \
"v_cvt_pk_bf16_f32 v115, v86, v87\n" \
"v_mfma_f32_16x16x32_bf16 v[252:255], a[172:175], v[120:123], v[252:255]\n" \
"v_add_u32_e32 v48, s16, v20\n" \
"s_add_u32 s64, s64, 0x10000\n" \
"v_smfmac_f32_16x16x64_bf16 a[204:207], a[212:215], v[120:127], v21\n" \
"s_waitcnt vmcnt(17) lgkmcnt(0)\n" \
"s_barrier\n" \
"s_mov_b64 exec, 0\n" \
"s_cmp_lg_u32 s15, 0\n" \
"s_cbranch_scc1 L_pf21\n" \
"s_branch L_pf20\n" \
"L_retB:\n" \
"s_mov_b64 exec, -1\n" \
"s_branch L_2_%=\n" \
"L_t2_%=:\n" \
"v_mfma_f32_16x16x32_bf16 v[252:255], a[188:191], v[124:127], v[252:255]\n" \
"v_cvt_pk_bf16_f32 v114, v84, v85\n" \
"v_cvt_pk_bf16_f32 v115, v86, v87\n" \
"v_mfma_f32_16x16x32_bf16 v[252:255], a[172:175], v[120:123], v[252:255]\n" \
"v_xor_b32_e32 v48, 0x8000, v48\n" \
"v_smfmac_f32_16x16x64_bf16 a[204:207], a[212:215], v[120:127], v21\n" \
"s_waitcnt lgkmcnt(0)\n" \
"L_2_%=:\n" \
"v_mfma_f32_16x16x32_bf16 v[64:67], a[128:131], a[64:67], v[32:35]\n" \
"ds_read_b128 a[160:163], v49 offset:0\n" \
"ds_read_b128 a[176:179], v49 offset:4096\n" \
"v_mfma_f32_16x16x32_bf16 v[64:67], a[132:135], a[68:71], v[64:67]\n" \
"v_cvt_pk_bf16_f32 v120, v88, v89\n" \
"v_cvt_pk_bf16_f32 v121, v90, v91\n" \
"v_mfma_f32_16x16x32_bf16 v[64:67], a[136:139], a[72:75], v[64:67]\n" \
"ds_read_b128 a[180:183], v49 offset:5120\n" \
"ds_read_b128 a[164:167], v49 offset:1024\n" \
"v_mfma_f32_16x16x32_bf16 v[64:67], a[140:143], a[76:79], v[64:67]\n" \
"v_exp_f32_e32 v92, v92\n" \
"v_mfma_f32_16x16x32_bf16 v[68:71], a[156:159], a[76:79], v[32:35]\n" \
"ds_read_b128 a[168:171], v49 offset:2048\n" \
"ds_read_b128 a[184:187], v49 offset:6144\n" \
"v_mfma_f32_16x16x32_bf16 v[68:71], a[152:155], a[72:75], v[68:71]\n" \
"v_exp_f32_e32 v93, v93\n" \
"v_mfma_f32_16x16x32_bf16 v[68:71], a[148:151], a[68:71], v[68:71]\n" \
"ds_read_b128 a[188:191], v49 offset:7168\n" \
"ds_read_b128 a[172:175], v49 offset:3072\n" \
"v_mfma_f32_16x16x32_bf16 v[68:71], a[144:147], a[64:67], v[68:71]\n" \
"v_exp_f32_e32 v94, v94\n" \
"v_mfma_f32_16x16x32_bf16 v[72:75], a[128:131], a[80:83], v[36:39]\n" \
"v_exp_f32_e32 v95, v95\n" \
"v_mfma_f32_16x16x32_bf16 v[72:75], a[132:135], a[84:87], v[72:75]\n" \
"v_cvt_pk_bf16_f32 v122, v92, v93\n" \
"v_cvt_pk_bf16_f32 v123, v94, v95\n" \
"v_mfma_f32_16x16x32_bf16 v[72:75], a[136:139], a[88:91], v[72:75]\n" \
"v_exp_f32_e32 v64, v64\n" \
"v_mfma_f32_16x16x32_bf16 v[72:75], a[140:143], a[92:95], v[72:75]\n" \
"v_exp_f32_e32 v65, v65\n" \
"v_mfma_f32_16x16x32_bf16 v[76:79], a[156:159], a[92:95], v[36:39]\n" \
"v_exp_f32_e32 v66, v66\n" \
"v_mfma_f32_16x16x32_bf16 v[76:79], a[152:155], a[88:91], v[76:79]\n" \
"v_exp_f32_e32 v67, v67\n" \
"v_mfma_f32_16x16x32_bf16 v[76:79], a[148:151], a[84:87], v[76:79]\n" \
"v_exp_f32_e32 v68, v68\n" \
"v_mfma_f32_16x16x32_bf16 v[76:79], a[144:147], a[80:83], v[76:79]\n" \
"v_exp_f32_e32 v69, v69\n" \
"v_mfma_f32_16x16x32_bf16 v[80:83], a[128:131], a[96:99], v[40:43]\n" \
"v_exp_f32_e32 v70, v70\n" \
"v_mfma_f32_16x16x32_bf16 v[80:83], a[132:135], a[100:103], v[80:83]\n" \
"v_exp_f32_e32 v71, v71\n" \
"v_mfma_f32_16x16x32_bf16 v[80:83], a[136:139], a[104:107], v[80:83]\n" \
"v_cvt_pk_bf16_f32 v100, v64, v65\n" \
"v_cvt_pk_bf16_f32 v101, v66, v67\n" \
"v_mfma_f32_16x16x32_bf16 v[80:83], a[140:143], a[108:111], v[80:83]\n" \
"v_cvt_pk_bf16_f32 v102, v68, v69\n" \
"v_cvt_pk_bf16_f32 v103, v70, v71\n" \
"v_mfma_f32_16x16x32_bf16 v[84:87], a[156:159], a[108:111], v[40:43]\n" \
"v_exp_f32_e32 v72, v72\n" \
"v_mfma_f32_16x16x32_bf16 v[84:87], a[152:155], a[104:107], v[84:87]\n" \
"v_exp_f32_e32 v73, v73\n" \
"v_mfma_f32_16x16x32_bf16 v[84:87], a[148:151], a[100:103], v[84:87]\n" \
"v_exp_f32_e32 v74, v74\n" \
"v_mfma_f32_16x16x32_bf16 v[84:87], a[144:147], a[96:99], v[84:87]\n" \
"v_exp_f32_e32 v75, v75\n" \
"v_mfma_f32_16x16x32_bf16 v[88:91], a[128:131], a[112:115], v[44:47]\n" \
"v_exp_f32_e32 v76, v76\n" \
"v_mfma_f32_16x16x32_bf16 v[88:91], a[132:135], a[116:119], v[88:91]\n" \
"v_exp_f32_e32 v77, v77\n" \
"v_mfma_f32_16x16x32_bf16 v[88:91], a[136:139], a[120:123], v[88:91]\n" \
"v_exp_f32_e32 v78, v78\n" \
"v_mfma_f32_16x16x32_bf16 v[88:91], a[140:143], a[124:127], v[88:91]\n" \
"v_exp_f32_e32 v79, v79\n" \
"v_mfma_f32_16x16x32_bf16 v[92:95], a[156:159], a[124:127], v[44:47]\n" \
"v_cvt_pk_bf16_f32 v108, v72, v73\n" \
"v_cvt_pk_bf16_f32 v109, v74, v75\n" \
"v_mfma_f32_16x16x32_bf16 v[92:95], a[152:155], a[120:123], v[92:95]\n" \
"v_cvt_pk_bf16_f32 v110, v76, v77\n" \
"v_cvt_pk_bf16_f32 v111, v78, v79\n" \
"v_mfma_f32_16x16x32_bf16 v[92:95], a[148:151], a[116:119], v[92:95]\n" \
"ds_read_b128 a[128:131], v48 offset:0\n" \
"ds_read_b128 a[132:135], v48 offset:1024\n" \
"v_mfma_f32_16x16x32_bf16 v[92:95], a[144:147], a[112:115], v[92:95]\n" \
"s_add_u32 s63, s63, s63\n" \
"s_waitcnt lgkmcnt(2)\n" \
"v_mfma_f32_16x16x32_bf16 v[128:131], a[160:163], v[96:99], v[128:131]\n" \
"ds_read_b128 a[136:139], v48 offset:2048\n" \
"ds_read_b128 a[140:143], v48 offset:3072\n" \
"v_mfma_f32_16x16x32_bf16 v[128:131], a[176:179], v[100:103], v[128:131]\n" \
"v_exp_f32_e32 v80, v80\n" \
"v_mfma_f32_16x16x32_bf16 v[132:135], a[180:183], v[100:103], v[132:135]\n" \
"ds_read_b128 a[144:147], v48 offset:4096\n" \
"ds_read_b128 a[148:151], v48 offset:5120\n" \
"v_mfma_f32_16x16x32_bf16 v[132:135], a[164:167], v[96:99], v[132:135]\n" \
"v_exp_f32_e32 v81, v81\n" \
"v_mfma_f32_16x16x32_bf16 v[136:139], a[168:171], v[96:99], v[136:139]\n" \
"ds_read_b128 a[152:155], v48 offset:6144\n" \
"ds_read_b128 a[156:159], v48 offset:7168\n" \
"v_mfma_f32_16x16x32_bf16 v[136:139], a[184:187], v[100:103], v[136:139]\n" \
"v_exp_f32_e32 v82, v82\n" \
"v_mfma_f32_16x16x32_bf16 v[140:143], a[188:191], v[100:103], v[140:143]\n" \
"v_exp_f32_e32 v83, v83\n" \
"v_mfma_f32_16x16x32_bf16 v[140:143], a[172:175], v[96:99], v[140:143]\n" \
"v_exp_f32_e32 v84, v84\n" \
"v_smfmac_f32_16x16x64_bf16 a[192:195], a[212:215], v[96:103], v21\n" \
"v_mfma_f32_16x16x32_bf16 v[160:163], a[160:163], v[104:107], v[160:163]\n" \
"v_exp_f32_e32 v85, v85\n" \
"v_mfma_f32_16x16x32_bf16 v[160:163], a[176:179], v[108:111], v[160:163]\n" \
"v_exp_f32_e32 v86, v86\n" \
"v_mfma_f32_16x16x32_bf16 v[164:167], a[180:183], v[108:111], v[164:167]\n" \
"v_exp_f32_e32 v87, v87\n" \
"v_mfma_f32_16x16x32_bf16 v[164:167], a[164:167], v[104:107], v[164:167]\n" \
"v_cvt_pk_bf16_f32 v116, v80, v81\n" \
"v_cvt_pk_bf16_f32 v117, v82, v83\n" \
"v_mfma_f32_16x16x32_bf16 v[168:171], a[168:171], v[104:107], v[168:171]\n" \
"v_cvt_pk_bf16_f32 v118, v84, v85\n" \
"v_cvt_pk_bf16_f32 v119, v86, v87\n" \
"v_mfma_f32_16x16x32_bf16 v[168:171], a[184:187], v[108:111], v[168:171]\n" \
"v_exp_f32_e32 v88, v88\n" \
"v_mfma_f32_16x16x32_bf16 v[172:175], a[188:191], v[108:111], v[172:175]\n" \
"v_exp_f32_e32 v89, v89\n" \
"v_mfma_f32_16x16x32_bf16 v[172:175], a[172:175], v[104:107], v[172:175]\n" \
"v_exp_f32_e32 v90, v90\n" \
"v_smfmac_f32_16x16x64_bf16 a[196:199], a[212:215], v[104:111], v21\n" \
"v_mfma_f32_16x16x32_bf16 v[192:195], a[160:163], v[112:115], v[192:195]\n" \
"v_exp_f32_e32 v91, v91\n" \
"v_mfma_f32_16x16x32_bf16 v[192:195], a[176:179], v[116:119], v[192:195]\n" \
"v_exp_f32_e32 v92, v92\n" \
"v_mfma_f32_16x16x32_bf16 v[196:199], a[180:183], v[116:119], v[196:199]\n" \
"v_exp_f32_e32 v93, v93\n" \
"v_mfma_f32_16x16x32_bf16 v[196:199], a[164:167], v[112:115], v[196:199]\n" \
"v_exp_f32_e32 v94, v94\n" \
"v_mfma_f32_16x16x32_bf16 v[200:203], a[168:171], v[112:115], v[200:203]\n" \
"v_exp_f32_e32 v95, v95\n" \
"v_mfma_f32_16x16x32_bf16 v[200:203], a[184:187], v[116:119], v[200:203]\n" \
"v_cvt_pk_bf16_f32 v124, v88, v89\n" \
"v_cvt_pk_bf16_f32 v125, v90, v91\n" \
"v_mfma_f32_16x16x32_bf16 v[204:207], a[188:191], v[116:119], v[204:207]\n" \
"v_cvt_pk_bf16_f32 v126, v92, v93\n" \
"v_cvt_pk_bf16_f32 v127, v94, v95\n" \
"v_mfma_f32_16x16x32_bf16 v[204:207], a[172:175], v[112:115], v[204:207]\n" \
"v_smfmac_f32_16x16x64_bf16 a[200:203], a[212:215], v[112:119], v21\n" \
"s_waitcnt vmcnt(0) lgkmcnt(0)\n" \
"s_barrier\n" \
"v_mfma_f32_16x16x32_bf16 v[224:227], a[160:163], v[120:123], v[224:227]\n" \
"s_cbranch_scc1 L_h2_%=\n" \
"L_hr2_%=:\n" \
"v_mfma_f32_16x16x32_bf16 v[224:227], a[176:179], v[124:127], v[224:227]\n" \
"s_xor_b32 m0, s65, 0x12000\n" \
"ds_read_b128 a[160:163], v49 offset:8192\n" \
"v_mfma_f32_16x16x32_bf16 v[228:231], a[180:183], v[124:127], v[228:231]\n" \
"buffer_load_dwordx4 v50, s[8:11], s64 offen offset:0 sc1 lds\n" \
"v_mfma_f32_16x16x32_bf16 v[228:231], a[164:167], v[120:123], v[228:231]\n" \
"ds_read_b128 a[176:179], v49 offset:12288\n" \
"ds_read_b128 a[180:183], v49 offset:13312\n" \
"v_mfma_f32_16x16x32_bf16 v[232:235], a[168:171], v[120:123], v[232:235]\n" \
"v_mfma_f32_16x16x32_bf16 v[232:235], a[184:187], v[124:127], v[232:235]\n" \
"buffer_load_dwordx4 v50, s[8:11], s64 offen offset:1024 sc1 lds\n" \
"v_mfma_f32_16x16x32_bf16 v[236:239], a[188:191], v[124:127], v[236:239]\n" \
"ds_read_b128 a[164:167], v49 offset:9216\n" \
"ds_read_b128 a[168:171], v49 offset:10240\n" \
"v_mfma_f32_16x16x32_bf16 v[236:239], a[172:175], v[120:123], v[236:239]\n" \
"v_mfma_f32_16x16x32_bf16 v[64:67], a[128:131], a[64:67], v[32:35]\n" \
"ds_read_b128 a[184:187], v49 offset:14336\n" \
"ds_read_b128 a[188:191], v49 offset:15360\n" \
"v_mfma_f32_16x16x32_bf16 v[64:67], a[132:135], a[68:71], v[64:67]\n" \
"buffer_load_dwordx4 v50, s[8:11], s64 offen offset:2048 sc1 lds\n" \
"v_mfma_f32_16x16x32_bf16 v[64:67], a[136:139], a[72:75], v[64:67]\n" \
"ds_read_b128 a[172:175], v49 offset:11264\n" \
"v_add_u32_e32 v49, 0x10000, v48\n" \
"v_mfma_f32_16x16x32_bf16 v[64:67], a[140:143], a[76:79], v[64:67]\n" \
"v_mfma_f32_16x16x32_bf16 v[68:71], a[156:159], a[76:79], v[32:35]\n" \
"v_mfma_f32_16x16x32_bf16 v[68:71], a[152:155], a[72:75], v[68:71]\n" \
"v_mfma_f32_16x16x32_bf16 v[68:71], a[148:151], a[68:71], v[68:71]\n" \
"buffer_load_dwordx4 v50, s[8:11], s64 offen offset:3072 sc1 lds\n" \
"v_mfma_f32_16x16x32_bf16 v[68:71], a[144:147], a[64:67], v[68:71]\n" \
"s_xor_b32 m0, s65, 0xa000\n" \
"v_mfma_f32_16x16x32_bf16 v[72:75], a[128:131], a[80:83], v[36:39]\n" \
"v_mfma_f32_16x16x32_bf16 v[72:75], a[132:135], a[84:87], v[72:75]\n" \
"v_exp_f32_e32 v64, v64\n" \
"v_mfma_f32_16x16x32_bf16 v[72:75], a[136:139], a[88:91], v[72:75]\n" \
"buffer_load_dwordx4 v51, s[8:11], s64 offen offset:0 sc1 lds\n" \
"v_mfma_f32_16x16x32_bf16 v[72:75], a[140:143], a[92:95], v[72:75]\n" \
"v_exp_f32_e32 v65, v65\n" \
"v_mfma_f32_16x16x32_bf16 v[76:79], a[156:159], a[92:95], v[36:39]\n" \
"v_exp_f32_e32 v66, v66\n" \
"v_mfma_f32_16x16x32_bf16 v[76:79], a[152:155], a[88:91], v[76:79]\n" \
"v_exp_f32_e32 v67, v67\n" \
"v_mfma_f32_16x16x32_bf16 v[76:79], a[148:151], a[84:87], v[76:79]\n" \
"buffer_load_dwordx4 v51, s[8:11], s64 offen offset:1024 sc1 lds\n" \
"v_mfma_f32_16x16x32_bf16 v[76:79], a[144:147], a[80:83], v[76:79]\n" \
"v_exp_f32_e32 v68, v68\n" \
"v_mfma_f32_16x16x32_bf16 v[80:83], a[128:131], a[96:99], v[40:43]\n" \
"v_exp_f32_e32 v69, v69\n" \
"v_mfma_f32_16x16x32_bf16 v[80:83], a[132:135], a[100:103], v[80:83]\n" \
"v_exp_f32_e32 v70, v70\n" \
"v_mfma_f32_16x16x32_bf16 v[80:83], a[136:139], a[104:107], v[80:83]\n" \
"v_exp_f32_e32 v71, v71\n" \
"v_mfma_f32_16x16x32_bf16 v[80:83], a[140:143], a[108:111], v[80:83]\n" \
"buffer_load_dwordx4 v51, s[8:11], s64 offen offset:2048 sc1 lds\n" \
"v_mfma_f32_16x16x32_bf16 v[84:87], a[156:159], a[108:111], v[40:43]\n" \
"v_exp_f32_e32 v72, v72\n" \
"v_mfma_f32_16x16x32_bf16 v[84:87], a[152:155], a[104:107], v[84:87]\n" \
"v_exp_f32_e32 v73, v73\n" \
"v_mfma_f32_16x16x32_bf16 v[84:87], a[148:151], a[100:103], v[84:87]\n" \
"v_exp_f32_e32 v74, v74\n" \
"v_mfma_f32_16x16x32_bf16 v[84:87], a[144:147], a[96:99], v[84:87]\n" \
"buffer_load_dwordx4 v51, s[8:11], s64 offen offset:3072 sc1 lds\n" \
"v_mfma_f32_16x16x32_bf16 v[88:91], a[128:131], a[112:115], v[44:47]\n" \
"s_xor_b32 m0, s65, 0x18000\n" \
"ds_read_b128 a[128:131], v48 offset:8192\n" \
"v_mfma_f32_16x16x32_bf16 v[88:91], a[132:135], a[116:119], v[88:91]\n" \
"v_exp_f32_e32 v75, v75\n" \
"v_mfma_f32_16x16x32_bf16 v[88:91], a[136:139], a[120:123], v[88:91]\n" \
"ds_read_b128 a[132:135], v48 offset:9216\n" \
"ds_read_b128 a[136:139], v48 offset:10240\n" \
"v_mfma_f32_16x16x32_bf16 v[88:91], a[140:143], a[124:127], v[88:91]\n" \
"v_exp_f32_e32 v76, v76\n" \
"v_mfma_f32_16x16x32_bf16 v[92:95], a[156:159], a[124:127], v[44:47]\n" \
"buffer_load_dwordx4 v52, s[8:11], s64 offen offset:0 sc1 lds\n" \
"v_mfma_f32_16x16x32_bf16 v[92:95], a[152:155], a[120:123], v[92:95]\n" \
"ds_read_b128 a[140:143], v48 offset:11264\n" \
"ds_read_b128 a[152:155], v48 offset:14336\n" \
"v_mfma_f32_16x16x32_bf16 v[92:95], a[148:151], a[116:119], v[92:95]\n" \
"v_exp_f32_e32 v77, v77\n" \
"v_mfma_f32_16x16x32_bf16 v[92:95], a[144:147], a[112:115], v[92:95]\n" \
"ds_read_b128 a[144:147], v48 offset:12288\n" \
"s_waitcnt lgkmcnt(6)\n" \
"v_mfma_f32_16x16x32_bf16 v[144:147], a[160:163], v[96:99], v[144:147]\n" \
"buffer_load_dwordx4 v52, s[8:11], s64 offen offset:1024 sc1 lds\n" \
"v_mfma_f32_16x16x32_bf16 v[144:147], a[176:179], v[100:103], v[144:147]\n" \
"ds_read_b128 a[148:151], v48 offset:13312\n" \
"ds_read_b128 a[156:159], v48 offset:15360\n" \
"v_mfma_f32_16x16x32_bf16 v[148:151], a[180:183], v[100:103], v[148:151]\n" \
"v_exp_f32_e32 v78, v78\n" \
"v_mfma_f32_16x16x32_bf16 v[148:151], a[164:167], v[96:99], v[148:151]\n" \
"v_exp_f32_e32 v79, v79\n" \
"v_mfma_f32_16x16x32_bf16 v[152:155], a[168:171], v[96:99], v[152:155]\n" \
"buffer_load_dwordx4 v52, s[8:11], s64 offen offset:2048 sc1 lds\n" \
"v_mfma_f32_16x16x32_bf16 v[152:155], a[184:187], v[100:103], v[152:155]\n" \
"v_exp_f32_e32 v80, v80\n" \
"v_mfma_f32_16x16x32_bf16 v[156:159], a[188:191], v[100:103], v[156:159]\n" \
"v_exp_f32_e32 v81, v81\n" \
"v_mfma_f32_16x16x32_bf16 v[156:159], a[172:175], v[96:99], v[156:159]\n" \
"v_exp_f32_e32 v82, v82\n" \
"v_mfma_f32_16x16x32_bf16 v[176:179], a[160:163], v[104:107], v[176:179]\n" \
"v_exp_f32_e32 v83, v83\n" \
"v_mfma_f32_16x16x32_bf16 v[176:179], a[176:179], v[108:111], v[176:179]\n" \
"buffer_load_dwordx4 v52, s[8:11], s64 offen offset:3072 sc1 lds\n" \
"v_mfma_f32_16x16x32_bf16 v[180:183], a[180:183], v[108:111], v[180:183]\n" \
"s_mov_b32 m0, s65\n" \
"s_xor_b32 s65, s65, 0x8000\n" \
"v_mfma_f32_16x16x32_bf16 v[180:183], a[164:167], v[104:107], v[180:183]\n" \
"v_exp_f32_e32 v84, v84\n" \
"v_mfma_f32_16x16x32_bf16 v[184:187], a[168:171], v[104:107], v[184:187]\n" \
"v_exp_f32_e32 v85, v85\n" \
"v_mfma_f32_16x16x32_bf16 v[184:187], a[184:187], v[108:111], v[184:187]\n" \
"buffer_load_dwordx4 v53, s[8:11], s64 offen offset:0 sc1 lds\n" \
"v_mfma_f32_16x16x32_bf16 v[188:191], a[188:191], v[108:111], v[188:191]\n" \
"v_exp_f32_e32 v86, v86\n" \
"v_mfma_f32_16x16x32_bf16 v[188:191], a[172:175], v[104:107], v[188:191]\n" \
"v_exp_f32_e32 v87, v87\n" \
"v_mfma_f32_16x16x32_bf16 v[208:211], a[160:163], v[112:115], v[208:211]\n" \
"v_exp_f32_e32 v88, v88\n" \
"v_mfma_f32_16x16x32_bf16 v[208:211], a[176:179], v[116:119], v[208:211]\n" \
"buffer_load_dwordx4 v53, s[8:11], s64 offen offset:1024 sc1 lds\n" \
"v_mfma_f32_16x16x32_bf16 v[212:215], a[180:183], v[116:119], v[212:215]\n" \
"v_exp_f32_e32 v89, v89\n" \
"v_mfma_f32_16x16x32_bf16 v[212:215], a[164:167], v[112:115], v[212:215]\n" \
"v_exp_f32_e32 v90, v90\n" \
"v_mfma_f32_16x16x32_bf16 v[216:219], a[168:171], v[112:115], v[216:219]\n" \
"v_exp_f32_e32 v91, v91\n" \
"v_mfma_f32_16x16x32_bf16 v[216:219], a[184:187], v[116:119], v[216:219]\n" \
"v_cvt_pk_bf16_f32 v96, v64, v65\n" \
"v_cvt_pk_bf16_f32 v97, v66, v67\n" \
"v_mfma_f32_16x16x32_bf16 v[220:223], a[188:191], v[116:119], v[220:223]\n" \
"buffer_load_dwordx4 v53, s[8:11], s64 offen offset:2048 sc1 lds\n" \
"v_mfma_f32_16x16x32_bf16 v[220:223], a[172:175], v[112:115], v[220:223]\n" \
"v_cvt_pk_bf16_f32 v98, v68, v69\n" \
"v_cvt_pk_bf16_f32 v99, v70, v71\n" \
"v_mfma_f32_16x16x32_bf16 v[240:243], a[160:163], v[120:123], v[240:243]\n" \
"v_cvt_pk_bf16_f32 v104, v72, v73\n" \
"v_cvt_pk_bf16_f32 v105, v74, v75\n" \
"v_mfma_f32_16x16x32_bf16 v[240:243], a[176:179], v[124:127], v[240:243]\n" \
"v_cvt_pk_bf16_f32 v106, v76, v77\n" \
"v_cvt_pk_bf16_f32 v107, v78, v79\n" \
"v_mfma_f32_16x16x32_bf16 v[244:247], a[180:183], v[124:127], v[244:247]\n" \
"buffer_load_dwordx4 v53, s[8:11], s64 offen offset:3072 sc1 lds\n" \
"v_mfma_f32_16x16x32_bf16 v[244:247], a[164:167], v[120:123], v[244:247]\n" \
"s_add_u32 s64, s64, 0x10000\n" \
"s_sub_u32 s66, s66, 1\n" \
"v_mfma_f32_16x16x32_bf16 v[248:251], a[168:171], v[120:123], v[248:251]\n" \
"v_cvt_pk_bf16_f32 v112, v80, v81\n" \
"v_cvt_pk_bf16_f32 v113, v82, v83\n" \
"v_mfma_f32_16x16x32_bf16 v[248:251], a[184:187], v[124:127], v[248:251]\n" \
"s_cbranch_scc0 L_t2_%=\n" \
"v_mfma_f32_16x16x32_bf16 v[252:255], a[188:191], v[124:127], v[252:255]\n" \
"v_cvt_pk_bf16_f32 v114, v84, v85\n" \
"v_cvt_pk_bf16_f32 v115, v86, v87\n" \
"v_mfma_f32_16x16x32_bf16 v[252:255], a[172:175], v[120:123], v[252:255]\n" \
"v_xor_b32_e32 v48, 0x8000, v48\n" \
"v_smfmac_f32_16x16x64_bf16 a[204:207], a[212:215], v[120:127], v21\n" \
"s_waitcnt lgkmcnt(0)\n" \
"s_waitcnt vmcnt(12)\n" \
"s_barrier\n" \
"s_cmp_lg_u32 s16, 0\n" \
"s_cbranch_scc1 L_f1_%=\n" \
"ds_read_b128 a[160:163], v49 offset:0\n" \
"ds_read_b128 a[164:167], v49 offset:1024\n" \
"ds_read_b128 a[168:171], v49 offset:2048\n" \
"ds_read_b128 a[172:175], v49 offset:3072\n" \
"ds_read_b128 a[176:179], v49 offset:8192\n" \
"ds_read_b128 a[180:183], v49 offset:9216\n" \
"v_cvt_pk_bf16_f32 v120, v88, v89\n" \
"v_cvt_pk_bf16_f32 v121, v90, v91\n" \
"v_mfma_f32_16x16x32_bf16 v[88:91], a[128:131], a[112:115], v[44:47]\n" \
"s_add_u32 s70, s61, 0x0\n" \
"s_add_u32 s71, s61, 0x49000\n" \
"ds_read_b128 a[184:187], v49 offset:10240\n" \
"v_mfma_f32_16x16x32_bf16 v[88:91], a[132:135], a[116:119], v[88:91]\n" \
"ds_read_b128 a[188:191], v49 offset:11264\n" \
"v_add_u32_e32 v54, 0x49000, v20\n" \
"v_mfma_f32_16x16x32_bf16 v[88:91], a[136:139], a[120:123], v[88:91]\n" \
"v_exp_f32_e32 v92, v92\n" \
"v_mfma_f32_16x16x32_bf16 v[88:91], a[140:143], a[124:127], v[88:91]\n" \
"v_exp_f32_e32 v93, v93\n" \
"v_exp_f32_e32 v94, v94\n" \
"v_exp_f32_e32 v95, v95\n" \
"v_cvt_pk_bf16_f32 v122, v92, v93\n" \
"v_cvt_pk_bf16_f32 v123, v94, v95\n" \
"v_mfma_f32_16x16x32_bf16 v[92:95], a[156:159], a[124:127], v[44:47]\n" \
"buffer_load_dwordx4 a[220:223], v20, s[56:59], s60 offen offset:0 sc1\n" \
"v_mfma_f32_16x16x32_bf16 v[92:95], a[152:155], a[120:123], v[92:95]\n" \
"v_readlane_b32 s97, v11, 32\n" \
"v_readlane_b32 s98, v11, 33\n" \
"v_mfma_f32_16x16x32_bf16 v[92:95], a[148:151], a[116:119], v[92:95]\n" \
"v_and_b32_e32 v112, v12, v112\n" \
"v_and_b32_e32 v113, v13, v113\n" \
"v_mfma_f32_16x16x32_bf16 v[92:95], a[144:147], a[112:115], v[92:95]\n" \
"v_and_b32_e32 v114, v14, v114\n" \
"s_waitcnt lgkmcnt(7)\n" \
"v_mfma_f32_16x16x32_bf16 v[128:131], a[160:163], v[96:99], v[128:131]\n" \
"v_and_b32_e32 v115, v15, v115\n" \
"s_waitcnt lgkmcnt(6)\n" \
"v_mfma_f32_16x16x32_bf16 v[132:135], a[164:167], v[96:99], v[132:135]\n" \
"ds_read_b32 v60, v18 offset:0\n" \
"s_waitcnt lgkmcnt(6)\n" \
"v_mfma_f32_16x16x32_bf16 v[136:139], a[168:171], v[96:99], v[136:139]\n" \
"ds_read_b32 v61, v18 offset:128\n" \
"s_waitcnt lgkmcnt(6)\n" \
"v_mfma_f32_16x16x32_bf16 v[140:143], a[172:175], v[96:99], v[140:143]\n" \
"ds_read_b128 a[0:3], v49 offset:4096\n" \
"s_waitcnt lgkmcnt(5)\n" \
"v_mfma_f32_16x16x32_bf16 v[144:147], a[176:179], v[96:99], v[144:147]\n" \
"buffer_load_dwordx4 a[236:239], v54, s[56:59], s60 offen offset:0 sc1\n" \
"v_mfma_f32_16x16x32_bf16 v[148:151], a[180:183], v[96:99], v[148:151]\n" \
"ds_read_b128 a[4:7], v49 offset:5120\n" \
"s_waitcnt lgkmcnt(5)\n" \
"v_mfma_f32_16x16x32_bf16 v[152:155], a[184:187], v[96:99], v[152:155]\n" \
"ds_read_b128 a[8:11], v49 offset:6144\n" \
"s_waitcnt lgkmcnt(5)\n" \
"v_mfma_f32_16x16x32_bf16 v[156:159], a[188:191], v[96:99], v[156:159]\n" \
"buffer_load_dwordx4 a[224:227], v20, s[56:59], s60 offen offset:1024 sc1\n" \
"v_mfma_f32_16x16x32_bf16 a[192:195], a[252:255], v[96:99], a[192:195]\n" \
"ds_read_b128 a[12:15], v49 offset:7168\n" \
"ds_read_b128 a[16:19], v49 offset:12288\n" \
"v_mfma_f32_16x16x32_bf16 v[160:163], a[160:163], v[104:107], v[160:163]\n" \
"ds_read_b128 a[20:23], v49 offset:13312\n" \
"ds_read_b128 a[24:27], v49 offset:14336\n" \
"buffer_load_dwordx4 a[240:243], v54, s[56:59], s60 offen offset:1024 sc1\n" \
"v_mfma_f32_16x16x32_bf16 v[164:167], a[164:167], v[104:107], v[164:167]\n" \
"ds_read_b128 a[28:31], v49 offset:15360\n" \
"v_accvgpr_read_b32 v40, a192\n" \
"v_mfma_f32_16x16x32_bf16 v[168:171], a[168:171], v[104:107], v[168:171]\n" \
"v_accvgpr_read_b32 v23, a193\n" \
"v_add_f32_e32 v40, v40, v23\n" \
"v_mfma_f32_16x16x32_bf16 v[172:175], a[172:175], v[104:107], v[172:175]\n" \
"buffer_load_dwordx4 a[228:231], v20, s[56:59], s60 offen offset:2048 sc1\n" \
"v_mfma_f32_16x16x32_bf16 v[176:179], a[176:179], v[104:107], v[176:179]\n" \
"v_add_u32_e32 v50, s15, v48\n" \
"v_add_u32_e32 v51, 0x10000, v50\n" \
"v_mfma_f32_16x16x32_bf16 v[180:183], a[180:183], v[104:107], v[180:183]\n" \
"s_waitcnt vmcnt(5)\n" \
"ds_write_b128 v50, v[128:131] offset:0\n" \
"buffer_load_dwordx4 a[244:247], v54, s[56:59], s60 offen offset:2048 sc1\n" \
"v_mfma_f32_16x16x32_bf16 v[184:187], a[184:187], v[104:107], v[184:187]\n" \
"ds_write_b128 v50, v[132:135] offset:1024\n" \
"v_mfma_f32_16x16x32_bf16 v[188:191], a[188:191], v[104:107], v[188:191]\n" \
"ds_write_b128 v50, v[136:139] offset:2048\n" \
"buffer_load_dwordx4 a[232:235], v20, s[56:59], s60 offen offset:3072 sc1\n" \
"v_mfma_f32_16x16x32_bf16 a[196:199], a[252:255], v[104:107], a[196:199]\n" \
"ds_write_b128 v50, v[140:143] offset:3072\n" \
"v_mfma_f32_16x16x32_bf16 v[192:195], a[160:163], v[112:115], v[192:195]\n" \
"ds_write_b128 v50, v[144:147] offset:8192\n" \
"buffer_load_dwordx4 a[248:251], v54, s[56:59], s60 offen offset:3072 sc1\n" \
"v_mfma_f32_16x16x32_bf16 v[196:199], a[164:167], v[112:115], v[196:199]\n" \
"ds_write_b128 v50, v[148:151] offset:9216\n" \
"v_mfma_f32_16x16x32_bf16 v[200:203], a[168:171], v[112:115], v[200:203]\n" \
"ds_write_b128 v50, v[152:155] offset:10240\n" \
"buffer_load_dwordx4 a[32:35], v20, s[56:59], s97 offen offset:0 sc1\n" \
"v_mfma_f32_16x16x32_bf16 v[204:207], a[172:175], v[112:115], v[204:207]\n" \
"v_accvgpr_read_b32 v41, a196\n" \
"v_accvgpr_read_b32 v23, a197\n" \
"v_mfma_f32_16x16x32_bf16 v[208:211], a[176:179], v[112:115], v[208:211]\n" \
"v_add_f32_e32 v41, v41, v23\n" \
"v_xor_b32_e32 v52, 0x4000, v50\n" \
"v_mfma_f32_16x16x32_bf16 v[212:215], a[180:183], v[112:115], v[212:215]\n" \
"buffer_load_dwordx4 a[40:43], v54, s[56:59], s97 offen offset:0 sc1\n" \
"v_mfma_f32_16x16x32_bf16 v[216:219], a[184:187], v[112:115], v[216:219]\n" \
"ds_write_b128 v50, v[156:159] offset:11264\n" \
"v_mfma_f32_16x16x32_bf16 v[220:223], a[188:191], v[112:115], v[220:223]\n" \
"ds_write_b128 v51, v[160:163] offset:0\n" \
"buffer_load_dwordx4 a[48:51], v20, s[56:59], s98 offen offset:0 sc1\n" \
"v_mfma_f32_16x16x32_bf16 a[200:203], a[252:255], v[112:115], a[200:203]\n" \
"ds_write_b128 v51, v[164:167] offset:1024\n" \
"ds_write_b128 v51, v[168:171] offset:2048\n" \
"ds_write_b128 v51, v[172:175] offset:3072\n" \
"buffer_load_dwordx4 a[56:59], v54, s[56:59], s98 offen offset:0 sc1\n" \
"ds_write_b128 v51, v[176:179] offset:8192\n" \
"ds_write_b128 v51, v[180:183] offset:9216\n" \
"ds_write_b128 v51, v[184:187] offset:10240\n" \
"ds_write_b128 v51, v[188:191] offset:11264\n" \
"buffer_load_dwordx4 a[36:39], v20, s[56:59], s97 offen offset:1024 sc1\n" \
"ds_write_b64 v19, v[40:41]\n" \
"v_xor_b32_e32 v53, 0x4000, v51\n" \
"v_xor_b32_e32 v63, 0x400, v19\n" \
"v_readlane_b32 s93, v11, 18\n" \
"v_readlane_b32 s84, v11, 13\n" \
"v_readlane_b32 s85, v11, 14\n" \
"v_readlane_b32 s86, v11, 15\n" \
"s_mov_b32 s87, 0x20000\n" \
"v_readlane_b32 s92, v11, 16\n" \
"s_add_u32 m0, s93, 0x0\n" \
"v_exp_f32_e32 v88, v88\n" \
"v_exp_f32_e32 v89, v89\n" \
"v_exp_f32_e32 v90, v90\n" \
"v_exp_f32_e32 v91, v91\n" \
"v_exp_f32_e32 v92, v92\n" \
"v_exp_f32_e32 v93, v93\n" \
"v_exp_f32_e32 v94, v94\n" \
"v_exp_f32_e32 v95, v95\n" \
"v_cvt_pk_bf16_f32 v124, v88, v89\n" \
"v_cvt_pk_bf16_f32 v125, v90, v91\n" \
"v_cvt_pk_bf16_f32 v126, v92, v93\n" \
"v_cvt_pk_bf16_f32 v127, v94, v95\n" \
"v_and_b32_e32 v124, v12, v124\n" \
"v_and_b32_e32 v125, v13, v125\n" \
"v_and_b32_e32 v126, v14, v126\n" \
"v_and_b32_e32 v127, v15, v127\n" \
"v_accvgpr_read_b32 v56, a200\n" \
"v_accvgpr_read_b32 v23, a201\n" \
"v_add_f32_e32 v56, v56, v23\n" \
"s_waitcnt lgkmcnt(0)\n" \
"s_barrier\n" \
"buffer_load_dwordx4 a[44:47], v54, s[56:59], s97 offen offset:1024 sc1\n" \
"ds_read_b128 v[128:131], v52 offset:0\n" \
"L_pf13:\n" \
"s_cbranch_execz L_pf17\n" \
"s_nop 0\n" \
"ds_read_b128 v[132:135], v52 offset:1024\n" \
"ds_read_b128 v[136:139], v52 offset:2048\n" \
"ds_read_b128 v[140:143], v52 offset:3072\n" \
"ds_read_b128 v[144:147], v52 offset:8192\n" \
"ds_read_b128 v[148:151], v52 offset:9216\n" \
"ds_read_b128 v[152:155], v52 offset:10240\n" \
"ds_read_b128 v[156:159], v52 offset:11264\n" \
"buffer_load_dwordx4 a[52:55], v20, s[56:59], s98 offen offset:1024 sc1\n" \
"ds_read_b128 v[160:163], v53 offset:0\n" \
"ds_read_b128 v[164:167], v53 offset:1024\n" \
"ds_read_b128 v[168:171], v53 offset:2048\n" \
"ds_read_b128 v[172:175], v53 offset:3072\n" \
"ds_read_b128 v[176:179], v53 offset:8192\n" \
"ds_read_b128 v[180:183], v53 offset:9216\n" \
"ds_read_b128 v[184:187], v53 offset:10240\n" \
"ds_read_b128 v[188:191], v53 offset:11264\n" \
"buffer_load_dwordx4 a[60:63], v54, s[56:59], s98 offen offset:1024 sc1\n" \
"ds_read_b64 v[42:43], v63\n" \
"v_mfma_f32_16x16x32_bf16 v[224:227], a[160:163], v[120:123], v[224:227]\n" \
"v_mfma_f32_16x16x32_bf16 v[228:231], a[164:167], v[120:123], v[228:231]\n" \
"v_mfma_f32_16x16x32_bf16 v[232:235], a[168:171], v[120:123], v[232:235]\n" \
"buffer_load_dwordx4 v0, s[84:87], s92 offen offset:0 lds\n" \
"v_mfma_f32_16x16x32_bf16 v[236:239], a[172:175], v[120:123], v[236:239]\n" \
"v_mfma_f32_16x16x32_bf16 v[240:243], a[176:179], v[120:123], v[240:243]\n" \
"v_mfma_f32_16x16x32_bf16 v[244:247], a[180:183], v[120:123], v[244:247]\n" \
"buffer_load_dwordx4 v1, s[84:87], s92 offen offset:1024 lds\n" \
"v_mfma_f32_16x16x32_bf16 v[248:251], a[184:187], v[120:123], v[248:251]\n" \
"s_waitcnt lgkmcnt(15)\n" \
"v_add_f32_e32 v192, v192, v128\n" \
"v_mfma_f32_16x16x32_bf16 v[252:255], a[188:191], v[120:123], v[252:255]\n" \
"v_add_f32_e32 v193, v193, v129\n" \
"v_add_f32_e32 v194, v194, v130\n" \
"v_mfma_f32_16x16x32_bf16 a[204:207], a[252:255], v[120:123], a[204:207]\n" \
"buffer_load_dwordx4 v2, s[84:87], s92 offen offset:2048 lds\n" \
"v_add_f32_e32 v195, v195, v131\n" \
"s_waitcnt lgkmcnt(1)\n" \
"s_barrier\n" \
"v_mfma_f32_16x16x32_bf16 v[224:227], a[0:3], v[124:127], v[224:227]\n" \
"buffer_load_dwordx4 v3, s[84:87], s92 offen offset:3072 lds\n" \
"s_add_u32 m0, s93, 0x1000\n" \
"v_mfma_f32_16x16x32_bf16 v[228:231], a[4:7], v[124:127], v[228:231]\n" \
"s_waitcnt lgkmcnt(0)\n" \
"v_add_f32_e32 v56, v56, v42\n" \
"v_mfma_f32_16x16x32_bf16 v[232:235], a[8:11], v[124:127], v[232:235]\n" \
"v_max_f32_e32 v62, v62, v56\n" \
"v_add_f32_e32 v196, v196, v132\n" \
"v_mfma_f32_16x16x32_bf16 v[236:239], a[12:15], v[124:127], v[236:239]\n" \
"buffer_load_dwordx4 v4, s[84:87], s92 offen offset:0 lds\n" \
"v_rcp_f32_e32 v56, v56\n" \
"v_mfma_f32_16x16x32_bf16 v[240:243], a[16:19], v[124:127], v[240:243]\n" \
"v_add_f32_e32 v197, v197, v133\n" \
"v_add_f32_e32 v198, v198, v134\n" \
"v_add_f32_e32 v199, v199, v135\n" \
"v_mul_f32_e32 v56, v56, v60\n" \
"v_mul_f32_e32 v192, v56, v192\n" \
"v_mul_f32_e32 v193, v56, v193\n" \
"v_mul_f32_e32 v194, v56, v194\n" \
"v_mul_f32_e32 v195, v56, v195\n" \
"v_mul_f32_e32 v196, v56, v196\n" \
"v_mul_f32_e32 v197, v56, v197\n" \
"v_mul_f32_e32 v198, v56, v198\n" \
"v_mul_f32_e32 v199, v56, v199\n" \
"v_add_f32_e32 v200, v200, v136\n" \
"v_add_f32_e32 v201, v201, v137\n" \
"v_add_f32_e32 v202, v202, v138\n" \
"v_add_f32_e32 v203, v203, v139\n" \
"v_add_f32_e32 v204, v204, v140\n" \
"v_add_f32_e32 v205, v205, v141\n" \
"v_add_f32_e32 v206, v206, v142\n" \
"v_add_f32_e32 v207, v207, v143\n" \
"v_cvt_pk_bf16_f32 v64, v192, v193\n" \
"v_cvt_pk_bf16_f32 v65, v194, v195\n" \
"v_cvt_pk_bf16_f32 v66, v196, v197\n" \
"v_cvt_pk_bf16_f32 v67, v198, v199\n" \
"buffer_store_dwordx4 v[64:67], v20, s[56:59], s70 offen offset:0 sc1\n" \
"v_mfma_f32_16x16x32_bf16 v[244:247], a[20:23], v[124:127], v[244:247]\n" \
"v_mul_f32_e32 v200, v56, v200\n" \
"v_mul_f32_e32 v201, v56, v201\n" \
"v_mul_f32_e32 v202, v56, v202\n" \
"v_mul_f32_e32 v203, v56, v203\n" \
"v_mul_f32_e32 v204, v56, v204\n" \
"v_mul_f32_e32 v205, v56, v205\n" \
"v_mul_f32_e32 v206, v56, v206\n" \
"v_mul_f32_e32 v207, v56, v207\n" \
"v_add_f32_e32 v208, v208, v144\n" \
"v_add_f32_e32 v209, v209, v145\n" \
"v_add_f32_e32 v210, v210, v146\n" \
"v_add_f32_e32 v211, v211, v147\n" \
"v_add_f32_e32 v212, v212, v148\n" \
"v_add_f32_e32 v213, v213, v149\n" \
"v_add_f32_e32 v214, v214, v150\n" \
"v_add_f32_e32 v215, v215, v151\n" \
"v_cvt_pk_bf16_f32 v68, v200, v201\n" \
"v_cvt_pk_bf16_f32 v69, v202, v203\n" \
"v_cvt_pk_bf16_f32 v70, v204, v205\n" \
"v_cvt_pk_bf16_f32 v71, v206, v207\n" \
"buffer_store_dwordx4 v[68:71], v20, s[56:59], s70 offen offset:1024 sc1\n" \
"v_mul_f32_e32 v208, v56, v208\n" \
"v_mul_f32_e32 v209, v56, v209\n" \
"v_mul_f32_e32 v210, v56, v210\n" \
"v_mul_f32_e32 v211, v56, v211\n" \
"v_mul_f32_e32 v212, v56, v212\n" \
"v_mul_f32_e32 v213, v56, v213\n" \
"v_mul_f32_e32 v214, v56, v214\n" \
"v_mul_f32_e32 v215, v56, v215\n" \
"v_add_f32_e32 v216, v216, v152\n" \
"v_add_f32_e32 v217, v217, v153\n" \
"v_add_f32_e32 v218, v218, v154\n" \
"v_add_f32_e32 v219, v219, v155\n" \
"v_add_f32_e32 v220, v220, v156\n" \
"v_add_f32_e32 v221, v221, v157\n" \
"v_add_f32_e32 v222, v222, v158\n" \
"v_add_f32_e32 v223, v223, v159\n" \
"v_cvt_pk_bf16_f32 v72, v208, v209\n" \
"v_cvt_pk_bf16_f32 v73, v210, v211\n" \
"v_cvt_pk_bf16_f32 v74, v212, v213\n" \
"v_cvt_pk_bf16_f32 v75, v214, v215\n" \
"buffer_store_dwordx4 v[72:75], v20, s[56:59], s70 offen offset:2048 sc1\n" \
"v_mfma_f32_16x16x32_bf16 v[248:251], a[24:27], v[124:127], v[248:251]\n" \
"v_mul_f32_e32 v216, v56, v216\n" \
"v_mul_f32_e32 v217, v56, v217\n" \
"v_mul_f32_e32 v218, v56, v218\n" \
"v_mul_f32_e32 v219, v56, v219\n" \
"v_mul_f32_e32 v220, v56, v220\n" \
"v_mul_f32_e32 v221, v56, v221\n" \
"v_mul_f32_e32 v222, v56, v222\n" \
"v_mul_f32_e32 v223, v56, v223\n" \
"v_cvt_pk_bf16_f32 v76, v216, v217\n" \
"v_cvt_pk_bf16_f32 v77, v218, v219\n" \
"v_cvt_pk_bf16_f32 v78, v220, v221\n" \
"v_cvt_pk_bf16_f32 v79, v222, v223\n" \
"buffer_store_dwordx4 v[76:79], v20, s[56:59], s70 offen offset:3072 sc1\n" \
"v_add_f32_e32 v224, v224, v160\n" \
"v_add_f32_e32 v225, v225, v161\n" \
"v_add_f32_e32 v226, v226, v162\n" \
"v_add_f32_e32 v227, v227, v163\n" \
"v_add_f32_e32 v228, v228, v164\n" \
"v_add_f32_e32 v229, v229, v165\n" \
"v_add_f32_e32 v230, v230, v166\n" \
"v_add_f32_e32 v231, v231, v167\n" \
"v_add_f32_e32 v232, v232, v168\n" \
"v_add_f32_e32 v233, v233, v169\n" \
"v_add_f32_e32 v234, v234, v170\n" \
"v_add_f32_e32 v235, v235, v171\n" \
"v_add_f32_e32 v236, v236, v172\n" \
"v_add_f32_e32 v237, v237, v173\n" \
"v_add_f32_e32 v238, v238, v174\n" \
"v_add_f32_e32 v239, v239, v175\n" \
"v_mfma_f32_16x16x32_bf16 v[252:255], a[28:31], v[124:127], v[252:255]\n" \
"buffer_load_dwordx4 v5, s[84:87], s92 offen offset:1024 lds\n" \
"v_add_f32_e32 v240, v240, v176\n" \
"v_add_f32_e32 v241, v241, v177\n" \
"v_add_f32_e32 v242, v242, v178\n" \
"v_add_f32_e32 v243, v243, v179\n" \
"v_add_f32_e32 v244, v244, v180\n" \
"v_add_f32_e32 v245, v245, v181\n" \
"v_add_f32_e32 v246, v246, v182\n" \
"v_add_f32_e32 v247, v247, v183\n" \
"v_add_f32_e32 v248, v248, v184\n" \
"v_add_f32_e32 v249, v249, v185\n" \
"v_add_f32_e32 v250, v250, v186\n" \
"v_add_f32_e32 v251, v251, v187\n" \
"v_add_f32_e32 v252, v252, v188\n" \
"v_add_f32_e32 v253, v253, v189\n" \
"v_add_f32_e32 v254, v254, v190\n" \
"v_add_f32_e32 v255, v255, v191\n" \
"v_mfma_f32_16x16x32_bf16 a[204:207], a[252:255], v[124:127], a[204:207]\n" \
"buffer_load_dwordx4 v6, s[84:87], s92 offen offset:2048 lds\n" \
"v_mfma_f32_16x16x32_bf16 a[208:211], a[216:219], v[64:67], a[208:211]\n" \
"v_mfma_f32_16x16x32_bf16 a[208:211], a[216:219], v[68:71], a[208:211]\n" \
"v_mfma_f32_16x16x32_bf16 a[208:211], a[216:219], v[72:75], a[208:211]\n" \
"buffer_load_dwordx4 v7, s[84:87], s92 offen offset:3072 lds\n" \
"v_mfma_f32_16x16x32_bf16 a[208:211], a[216:219], v[76:79], a[208:211]\n" \
"s_add_u32 m0, s93, 0x2000\n" \
"s_nop 0\n" \
"buffer_load_dwordx4 v8, s[84:87], s92 offen offset:0 lds\n" \
"v_accvgpr_read_b32 v57, a204\n" \
"v_accvgpr_read_b32 v23, a205\n" \
"v_add_f32_e32 v57, v57, v23\n" \
"v_add_f32_e32 v57, v57, v43\n" \
"v_max_f32_e32 v62, v62, v57\n" \
"v_rcp_f32_e32 v57, v57\n" \
"v_cmp_lt_f32_e64 s[72:73], s100, v62\n" \
"v_mul_f32_e32 v57, v57, v61\n" \
"v_mul_f32_e32 v224, v57, v224\n" \
"v_mul_f32_e32 v225, v57, v225\n" \
"v_mul_f32_e32 v226, v57, v226\n" \
"v_mul_f32_e32 v227, v57, v227\n" \
"v_mul_f32_e32 v228, v57, v228\n" \
"v_mul_f32_e32 v229, v57, v229\n" \
"v_mul_f32_e32 v230, v57, v230\n" \
"v_mul_f32_e32 v231, v57, v231\n" \
"v_cvt_pk_bf16_f32 v80, v224, v225\n" \
"v_cvt_pk_bf16_f32 v81, v226, v227\n" \
"v_cvt_pk_bf16_f32 v82, v228, v229\n" \
"v_cvt_pk_bf16_f32 v83, v230, v231\n" \
"buffer_store_dwordx4 v[80:83], v20, s[56:59], s71 offen offset:0 sc1\n" \
"v_mul_f32_e32 v232, v57, v232\n" \
"v_mfma_f32_16x16x32_bf16 a[208:211], a[216:219], v[80:83], a[208:211]\n" \
"v_mul_f32_e32 v233, v57, v233\n" \
"v_mul_f32_e32 v234, v57, v234\n" \
"v_mul_f32_e32 v235, v57, v235\n" \
"v_mul_f32_e32 v236, v57, v236\n" \
"v_mul_f32_e32 v237, v57, v237\n" \
"v_mul_f32_e32 v238, v57, v238\n" \
"v_mul_f32_e32 v239, v57, v239\n" \
"v_cvt_pk_bf16_f32 v84, v232, v233\n" \
"v_cvt_pk_bf16_f32 v85, v234, v235\n" \
"v_cvt_pk_bf16_f32 v86, v236, v237\n" \
"v_cvt_pk_bf16_f32 v87, v238, v239\n" \
"buffer_store_dwordx4 v[84:87], v20, s[56:59], s71 offen offset:1024 sc1\n" \
"v_mul_f32_e32 v240, v57, v240\n" \
"v_mfma_f32_16x16x32_bf16 a[208:211], a[216:219], v[84:87], a[208:211]\n" \
"v_mul_f32_e32 v241, v57, v241\n" \
"v_mul_f32_e32 v242, v57, v242\n" \
"v_mul_f32_e32 v243, v57, v243\n" \
"v_mul_f32_e32 v244, v57, v244\n" \
"v_mul_f32_e32 v245, v57, v245\n" \
"v_mul_f32_e32 v246, v57, v246\n" \
"v_mul_f32_e32 v247, v57, v247\n" \
"v_cvt_pk_bf16_f32 v96, v240, v241\n" \
"v_cvt_pk_bf16_f32 v97, v242, v243\n" \
"v_cvt_pk_bf16_f32 v98, v244, v245\n" \
"v_cvt_pk_bf16_f32 v99, v246, v247\n" \
"buffer_store_dwordx4 v[96:99], v20, s[56:59], s71 offen offset:2048 sc1\n" \
"v_mul_f32_e32 v248, v57, v248\n" \
"v_mfma_f32_16x16x32_bf16 a[208:211], a[216:219], v[96:99], a[208:211]\n" \
"v_mul_f32_e32 v249, v57, v249\n" \
"v_mul_f32_e32 v250, v57, v250\n" \
"v_mul_f32_e32 v251, v57, v251\n" \
"v_mul_f32_e32 v252, v57, v252\n" \
"v_mul_f32_e32 v253, v57, v253\n" \
"v_mul_f32_e32 v254, v57, v254\n" \
"v_mul_f32_e32 v255, v57, v255\n" \
"v_cvt_pk_bf16_f32 v100, v248, v249\n" \
"v_cvt_pk_bf16_f32 v101, v250, v251\n" \
"v_cvt_pk_bf16_f32 v102, v252, v253\n" \
"v_cvt_pk_bf16_f32 v103, v254, v255\n" \
"buffer_store_dwordx4 v[100:103], v20, s[56:59], s71 offen offset:3072 sc1\n" \
"s_nop 0\n" \
"v_mfma_f32_16x16x32_bf16 a[208:211], a[216:219], v[100:103], a[208:211]\n" \
"s_nop 7\n" \
"v_accvgpr_read_b32 v63, a208\n" \
"v_cmp_u_f32_e32 vcc, v63, v63\n" \
"s_nop 0\n" \
"s_or_b64 vcc, s[72:73], vcc\n" \
"s_cbranch_scc0 L_out_%=\n" \
"s_branch L_end_%=\n" \
"L_h1_%=:\n" \
"L_pf14:\n" \
"s_cbranch_execz L_pf18\n" \
"s_nop 0\n" \
"s_cmp_gt_i32 s63, 0\n" \
"s_cbranch_scc1 L_hc1_%=\n" \
"s_cmp_lt_i32 s63, 0\n" \
"s_cbranch_scc1 L_hn1_%=\n" \
"s_mov_b32 exec_hi, 0xf\n" \
"buffer_load_dword v22, v16, s[80:83], 0 offen\n" \
"s_mov_b32 exec_hi, -1\n" \
"s_branch L_hr1_%=\n" \
"L_hn1_%=:\n" \
"buffer_load_dword v22, v17, s[56:59], 0 offen sc1\n" \
"s_branch L_hr1_%=\n" \
"L_hs1_%=:\n" \
"s_sleep 1\n" \
"buffer_load_dword v22, v17, s[56:59], 0 offen sc1\n" \
"s_add_u32 s101, s101, 1\n" \
"s_waitcnt vmcnt(0)\n" \
"s_cmp_gt_u32 s101, 0x100000\n" \
"s_cbranch_scc1 L_hr1_%=\n" \
"L_hc1_%=:\n" \
"v_cmp_ne_u32_e32 vcc, s62, v22\n" \
"s_nop 4\n" \
"s_cbranch_vccnz L_hs1_%=\n" \
"s_branch L_hr1_%=\n" \
"L_h2_%=:\n" \
"s_cmp_gt_i32 s63, 0\n" \
"s_cbranch_scc1 L_hc2_%=\n" \
"s_cmp_lt_i32 s63, 0\n" \
"s_cbranch_scc1 L_hn2_%=\n" \
"s_mov_b32 exec_hi, 0xf\n" \
"buffer_load_dword v22, v16, s[80:83], 0 offen\n" \
"s_mov_b32 exec_hi, -1\n" \
"s_branch L_hr2_%=\n" \
"L_hn2_%=:\n" \
"buffer_load_dword v22, v17, s[56:59], 0 offen sc1\n" \
"s_branch L_hr2_%=\n" \
"L_hs2_%=:\n" \
"s_sleep 1\n" \
"buffer_load_dword v22, v17, s[56:59], 0 offen sc1\n" \
"s_add_u32 s101, s101, 1\n" \
"s_waitcnt vmcnt(0)\n" \
"s_cmp_gt_u32 s101, 0x100000\n" \
"s_cbranch_scc1 L_hr2_%=\n" \
"L_hc2_%=:\n" \
"v_cmp_ne_u32_e32 vcc, s62, v22\n" \
"s_nop 4\n" \
"s_cbranch_vccnz L_hs2_%=\n" \
"s_branch L_hr2_%=\n" \
"L_sa_%=:\n" \
"s_sleep 1\n" \
"buffer_load_dword v37, v38, s[56:59], s64 offen sc1\n" \
"s_add_u32 s101, s101, 1\n" \
"s_waitcnt vmcnt(0)\n" \
"v_cmp_ne_u32_e32 vcc, s62, v37\n" \
"s_cmp_gt_u32 s101, 0x100000\n" \
"s_cbranch_scc1 L_ra_%=\n" \
"s_nop 2\n" \
"s_cbranch_vccnz L_sa_%=\n" \
"s_branch L_ra_%=\n" \
"L_sb_%=:\n" \
"s_sleep 1\n" \
"buffer_load_dword v47, v38, s[56:59], s71 offen sc1\n" \
"s_add_u32 s101, s101, 1\n" \
"s_waitcnt vmcnt(0)\n" \
"v_cmp_ne_u32_e32 vcc, s62, v47\n" \
"s_cmp_gt_u32 s101, 0x100000\n" \
"s_cbranch_scc1 L_rb_%=\n" \
"s_nop 2\n" \
"s_cbranch_vccnz L_sb_%=\n" \
"s_branch L_rb_%=\n" \
"L_sx0_%=:\n" \
"s_sleep 1\n" \
"buffer_load_dword v37, v40, s[56:59], s74 offen sc1\n" \
"s_add_u32 s101, s101, 1\n" \
"s_waitcnt vmcnt(0)\n" \
"v_cmp_eq_u32_e64 s[98:99], s62, v37\n" \
"s_cmp_gt_u32 s101, 0x100000\n" \
"s_cbranch_scc1 L_rx0_%=\n" \
"s_andn2_b32 s75, 0x1, s98\n" \
"s_cbranch_scc1 L_sx0_%=\n" \
"s_branch L_rx0_%=\n" \
"L_sx1_%=:\n" \
"s_sleep 1\n" \
"buffer_load_dword v37, v40, s[56:59], s74 offen sc1\n" \
"s_add_u32 s101, s101, 1\n" \
"s_waitcnt vmcnt(0)\n" \
"v_cmp_eq_u32_e64 s[98:99], s62, v37\n" \
"s_cmp_gt_u32 s101, 0x100000\n" \
"s_cbranch_scc1 L_rx1_%=\n" \
"s_andn2_b32 s75, 0x2, s98\n" \
"s_cbranch_scc1 L_sx1_%=\n" \
"s_branch L_rx1_%=\n" \
"L_sx2_%=:\n" \
"s_sleep 1\n" \
"buffer_load_dword v37, v40, s[56:59], s74 offen sc1\n" \
"s_add_u32 s101, s101, 1\n" \
"s_waitcnt vmcnt(0)\n" \
"v_cmp_eq_u32_e64 s[98:99], s62, v37\n" \
"s_cmp_gt_u32 s101, 0x100000\n" \
"s_cbranch_scc1 L_rx2_%=\n" \
"s_andn2_b32 s75, 0x4, s98\n" \
"s_cbranch_scc1 L_sx2_%=\n" \
"s_branch L_rx2_%=\n" \
"L_sx3_%=:\n" \
"s_sleep 1\n" \
"buffer_load_dword v37, v40, s[56:59], s74 offen sc1\n" \
"s_add_u32 s101, s101, 1\n" \
"s_waitcnt vmcnt(0)\n" \
"v_cmp_eq_u32_e64 s[98:99], s62, v37\n" \
"s_cmp_gt_u32 s101, 0x100000\n" \
"s_cbranch_scc1 L_rx3_%=\n" \
"s_andn2_b32 s75, s100, s98\n" \
"s_cbranch_scc1 L_sx3_%=\n" \
"s_branch L_rx3_%=\n" \
"L_sx4_%=:\n" \
"s_sleep 1\n" \
"buffer_load_dword v37, v40, s[56:59], s74 offen sc1\n" \
"s_add_u32 s101, s101, 1\n" \
"s_waitcnt vmcnt(0)\n" \
"v_cmp_eq_u32_e64 s[98:99], s62, v37\n" \
"s_cmp_gt_u32 s101, 0x100000\n" \
"s_cbranch_scc1 L_rx4_%=\n" \
"s_andn2_b32 s75, 0x10, s98\n" \
"s_cbranch_scc1 L_sx4_%=\n" \
"s_branch L_rx4_%=\n" \
"L_sx5_%=:\n" \
"s_sleep 1\n" \
"buffer_load_dword v37, v40, s[56:59], s74 offen sc1\n" \
"s_add_u32 s101, s101, 1\n" \
"s_waitcnt vmcnt(0)\n" \
"v_cmp_eq_u32_e64 s[98:99], s62, v37\n" \
"s_cmp_gt_u32 s101, 0x100000\n" \
"s_cbranch_scc1 L_rx5_%=\n" \
"s_andn2_b32 s75, 0x20, s98\n" \
"s_cbranch_scc1 L_sx5_%=\n" \
"s_branch L_rx5_%=\n" \
"L_sx6_%=:\n" \
"s_sleep 1\n" \
"buffer_load_dword v37, v40, s[56:59], s74 offen sc1\n" \
"s_add_u32 s101, s101, 1\n" \
"s_waitcnt vmcnt(0)\n" \
"v_cmp_eq_u32_e64 s[98:99], s62, v37\n" \
"s_cmp_gt_u32 s101, 0x100000\n" \
"s_cbranch_scc1 L_rx6_%=\n" \
"s_andn2_b32 s75, 0x40, s98\n" \
"s_cbranch_scc1 L_sx6_%=\n" \
"s_branch L_rx6_%=\n" \
"L_sx7_%=:\n" \
"s_sleep 1\n" \
"buffer_load_dword v37, v40, s[56:59], s74 offen sc1\n" \
"s_add_u32 s101, s101, 1\n" \
"s_waitcnt vmcnt(0)\n" \
"v_cmp_eq_u32_e64 s[98:99], s62, v37\n" \
"s_cmp_gt_u32 s101, 0x100000\n" \
"s_cbranch_scc1 L_rx7_%=\n" \
"s_andn2_b32 s75, 0x80, s98\n" \
"s_cbranch_scc1 L_sx7_%=\n" \
"s_branch L_rx7_%=\n" \
"L_sx8_%=:\n" \
"s_sleep 1\n" \
"buffer_load_dword v37, v40, s[56:59], s74 offen sc1\n" \
"s_add_u32 s101, s101, 1\n" \
"s_waitcnt vmcnt(0)\n" \
"v_cmp_eq_u32_e64 s[98:99], s62, v37\n" \
"s_cmp_gt_u32 s101, 0x100000\n" \
"s_cbranch_scc1 L_rx8_%=\n" \
"s_andn2_b32 s75, 0x100, s98\n" \
"s_cbranch_scc1 L_sx8_%=\n" \
"s_branch L_rx8_%=\n" \
"L_sx9_%=:\n" \
"s_sleep 1\n" \
"buffer_load_dword v37, v40, s[56:59], s74 offen sc1\n" \
"s_add_u32 s101, s101, 1\n" \
"s_waitcnt vmcnt(0)\n" \
"v_cmp_eq_u32_e64 s[98:99], s62, v37\n" \
"s_cmp_gt_u32 s101, 0x100000\n" \
"s_cbranch_scc1 L_rx9_%=\n" \
"s_andn2_b32 s75, 0x200, s98\n" \
"s_cbranch_scc1 L_sx9_%=\n" \
"s_branch L_rx9_%=\n" \
"L_sx10_%=:\n" \
"s_sleep 1\n" \
"buffer_load_dword v37, v40, s[56:59], s74 offen sc1\n" \
"s_add_u32 s101, s101, 1\n" \
"s_waitcnt vmcnt(0)\n" \
"v_cmp_eq_u32_e64 s[98:99], s62, v37\n" \
"s_cmp_gt_u32 s101, 0x100000\n" \
"s_cbranch_scc1 L_rx10_%=\n" \
"s_andn2_b32 s75, 0x400, s98\n" \
"s_cbranch_scc1 L_sx10_%=\n" \
"s_branch L_rx10_%=\n" \
"L_att_stub:\n" \
"s_mov_b32 s101, 0\n" \
"v_add_u32_e32 v32, 0x49000, v20\n" \
"v_readlane_b32 s96, v11, 32\n" \
"v_readlane_b32 s97, v11, 33\n" \
"buffer_load_dwordx4 a[220:223], v20, s[56:59], s60 offen offset:0 sc1\n" \
"buffer_load_dwordx4 a[236:239], v32, s[56:59], s60 offen offset:0 sc1\n" \
"buffer_load_dwordx4 a[224:227], v20, s[56:59], s60 offen offset:1024 sc1\n" \
"buffer_load_dwordx4 a[240:243], v32, s[56:59], s60 offen offset:1024 sc1\n" \
"buffer_load_dwordx4 a[228:231], v20, s[56:59], s60 offen offset:2048 sc1\n" \
"buffer_load_dwordx4 a[244:247], v32, s[56:59], s60 offen offset:2048 sc1\n" \
"buffer_load_dwordx4 a[232:235], v20, s[56:59], s60 offen offset:3072 sc1\n" \
"buffer_load_dwordx4 a[248:251], v32, s[56:59], s60 offen offset:3072 sc1\n" \
"buffer_load_dwordx4 a[32:35], v20, s[56:59], s96 offen offset:0 sc1\n" \
"buffer_load_dwordx4 a[40:43], v32, s[56:59], s96 offen offset:0 sc1\n" \
"buffer_load_dwordx4 a[48:51], v20, s[56:59], s97 offen offset:0 sc1\n" \
"buffer_load_dwordx4 a[56:59], v32, s[56:59], s97 offen offset:0 sc1\n" \
"buffer_load_dwordx4 a[36:39], v20, s[56:59], s96 offen offset:1024 sc1\n" \
"buffer_load_dwordx4 a[44:47], v32, s[56:59], s96 offen offset:1024 sc1\n" \
"buffer_load_dwordx4 a[52:55], v20, s[56:59], s97 offen offset:1024 sc1\n" \
"buffer_load_dwordx4 a[60:63], v32, s[56:59], s97 offen offset:1024 sc1\n" \
"buffer_load_dwordx4 v[64:67], v20, s[56:59], s61 offen offset:0 sc1\n" \
"buffer_load_dwordx4 v[68:71], v20, s[56:59], s61 offen offset:1024 sc1\n" \
"buffer_load_dwordx4 v[72:75], v20, s[56:59], s61 offen offset:2048 sc1\n" \
"buffer_load_dwordx4 v[76:79], v20, s[56:59], s61 offen offset:3072 sc1\n" \
"buffer_load_dwordx4 v[80:83], v32, s[56:59], s61 offen offset:0 sc1\n" \
"buffer_load_dwordx4 v[84:87], v32, s[56:59], s61 offen offset:1024 sc1\n" \
"buffer_load_dwordx4 v[96:99], v32, s[56:59], s61 offen offset:2048 sc1\n" \
"buffer_load_dwordx4 v[100:103], v32, s[56:59], s61 offen offset:3072 sc1\n" \
"s_waitcnt vmcnt(0)\n" \
"s_branch L_out_%=\n" \
"L_sw_%=:\n" \
"L_pf15:\n" \
"s_cbranch_execz L_pf19\n" \
"s_nop 0\n" \
"v_mfma_f32_16x16x32_bf16 v[72:75], a[128:131], a[16:19], v[36:39]\n" \
"s_add_u32 s68, s60, 0xfff6e000\n" \
"s_add_u32 s69, s60, 0xfffb7000\n" \
"s_add_u32 s70, s60, 0x0\n" \
"s_add_u32 s71, s60, 0x49000\n" \
"v_mfma_f32_16x16x32_bf16 v[72:75], a[132:135], a[20:23], v[72:75]\n" \
"s_xor_b32 m0, s65, 0x12000\n" \
"ds_read_b128 a[160:163], v49 offset:0\n" \
"v_mfma_f32_16x16x32_bf16 v[72:75], a[136:139], a[24:27], v[72:75]\n" \
"buffer_load_dwordx4 v50, s[8:11], s64 offen offset:0 sc1 lds\n" \
"v_mfma_f32_16x16x32_bf16 v[72:75], a[140:143], a[28:31], v[72:75]\n" \
"ds_read_b128 a[164:167], v49 offset:1024\n" \
"ds_read_b128 a[168:171], v49 offset:2048\n" \
"v_mfma_f32_16x16x32_bf16 v[76:79], a[156:159], a[28:31], v[36:39]\n" \
"ds_read_b128 a[172:175], v49 offset:3072\n" \
"v_cvt_pk_bf16_f32 v120, v88, v89\n" \
"v_mfma_f32_16x16x32_bf16 v[76:79], a[152:155], a[24:27], v[76:79]\n" \
"buffer_load_dwordx4 v50, s[8:11], s64 offen offset:1024 sc1 lds\n" \
"v_mfma_f32_16x16x32_bf16 v[76:79], a[148:151], a[20:23], v[76:79]\n" \
"ds_read_b128 a[176:179], v49 offset:4096\n" \
"v_cvt_pk_bf16_f32 v121, v90, v91\n" \
"v_mfma_f32_16x16x32_bf16 v[76:79], a[144:147], a[16:19], v[76:79]\n" \
"ds_read_b128 a[180:183], v49 offset:5120\n" \
"s_mov_b32 s65, s13\n" \
"v_mfma_f32_16x16x32_bf16 v[80:83], a[128:131], a[32:35], v[40:43]\n" \
"v_add_u32_e32 v63, s15, v20\n" \
"s_add_u32 s67, s14, 40960\n" \
"v_mfma_f32_16x16x32_bf16 v[80:83], a[132:135], a[36:39], v[80:83]\n" \
"v_add_u32_e32 v50, 0x6000, v63\n" \
"s_xor_b32 m0, s65, 0x8000\n" \
"v_mfma_f32_16x16x32_bf16 v[80:83], a[136:139], a[40:43], v[80:83]\n" \
"v_exp_f32_e32 v92, v92\n" \
"v_mfma_f32_16x16x32_bf16 v[80:83], a[140:143], a[44:47], v[80:83]\n" \
"buffer_load_dwordx4 v50, s[8:11], s67 offen offset:0 sc1 lds\n" \
"v_mfma_f32_16x16x32_bf16 v[84:87], a[156:159], a[44:47], v[40:43]\n" \
"v_exp_f32_e32 v93, v93\n" \
"v_mfma_f32_16x16x32_bf16 v[84:87], a[152:155], a[40:43], v[84:87]\n" \
"v_exp_f32_e32 v94, v94\n" \
"v_mfma_f32_16x16x32_bf16 v[84:87], a[148:151], a[36:39], v[84:87]\n" \
"v_exp_f32_e32 v95, v95\n" \
"v_mfma_f32_16x16x32_bf16 v[84:87], a[144:147], a[32:35], v[84:87]\n" \
"v_cvt_pk_bf16_f32 v122, v92, v93\n" \
"v_cvt_pk_bf16_f32 v123, v94, v95\n" \
"v_mfma_f32_16x16x32_bf16 v[88:91], a[128:131], a[48:51], v[44:47]\n" \
"ds_read_b128 a[184:187], v49 offset:6144\n" \
"ds_read_b128 a[188:191], v49 offset:7168\n" \
"v_mfma_f32_16x16x32_bf16 v[88:91], a[132:135], a[52:55], v[88:91]\n" \
"v_and_b32_e32 v96, v12, v96\n" \
"v_and_b32_e32 v97, v13, v97\n" \
"v_mfma_f32_16x16x32_bf16 v[88:91], a[136:139], a[56:59], v[88:91]\n" \
"v_and_b32_e32 v98, v14, v98\n" \
"v_and_b32_e32 v99, v15, v99\n" \
"v_mfma_f32_16x16x32_bf16 v[88:91], a[140:143], a[60:63], v[88:91]\n" \
"buffer_load_dwordx4 v50, s[8:11], s67 offen offset:1024 sc1 lds\n" \
"v_mfma_f32_16x16x32_bf16 v[92:95], a[156:159], a[60:63], v[44:47]\n" \
"v_exp_f32_e32 v72, v72\n" \
"v_mfma_f32_16x16x32_bf16 v[92:95], a[152:155], a[56:59], v[92:95]\n" \
"v_exp_f32_e32 v73, v73\n" \
"v_mfma_f32_16x16x32_bf16 v[92:95], a[148:151], a[52:55], v[92:95]\n" \
"v_exp_f32_e32 v74, v74\n" \
"v_mfma_f32_16x16x32_bf16 v[92:95], a[144:147], a[48:51], v[92:95]\n" \
"v_cvt_pk_bf16_f32 v108, v72, v73\n" \
"s_waitcnt lgkmcnt(2)\n" \
"v_mfma_f32_16x16x32_bf16 v[128:131], a[160:163], v[96:99], v[128:131]\n" \
"v_exp_f32_e32 v75, v75\n" \
"v_mfma_f32_16x16x32_bf16 v[132:135], a[164:167], v[96:99], v[132:135]\n" \
"v_exp_f32_e32 v76, v76\n" \
"v_mfma_f32_16x16x32_bf16 v[136:139], a[168:171], v[96:99], v[136:139]\n" \
"v_exp_f32_e32 v77, v77\n" \
"v_mfma_f32_16x16x32_bf16 v[140:143], a[172:175], v[96:99], v[140:143]\n" \
"v_exp_f32_e32 v78, v78\n" \
"v_mfma_f32_16x16x32_bf16 a[192:195], a[252:255], v[96:99], a[192:195]\n" \
"v_exp_f32_e32 v79, v79\n" \
"v_mfma_f32_16x16x32_bf16 v[160:163], a[160:163], v[104:107], v[160:163]\n" \
"v_cvt_pk_bf16_f32 v109, v74, v75\n" \
"v_cvt_pk_bf16_f32 v110, v76, v77\n" \
"v_cvt_pk_bf16_f32 v111, v78, v79\n" \
"v_and_b32_e32 v108, v12, v108\n" \
"v_and_b32_e32 v109, v13, v109\n" \
"v_and_b32_e32 v110, v14, v110\n" \
"v_and_b32_e32 v111, v15, v111\n" \
"v_exp_f32_e32 v80, v80\n" \
"v_exp_f32_e32 v81, v81\n" \
"v_mfma_f32_16x16x32_bf16 v[160:163], a[176:179], v[108:111], v[160:163]\n" \
"buffer_load_dwordx4 v50, s[8:11], s67 offen offset:2048 sc1 lds\n" \
"v_mfma_f32_16x16x32_bf16 v[164:167], a[180:183], v[108:111], v[164:167]\n" \
"buffer_load_dwordx4 v50, s[8:11], s67 offen offset:3072 sc1 lds\n" \
"v_mfma_f32_16x16x32_bf16 v[164:167], a[164:167], v[104:107], v[164:167]\n" \
"v_exp_f32_e32 v82, v82\n" \
"v_mfma_f32_16x16x32_bf16 v[168:171], a[168:171], v[104:107], v[168:171]\n" \
"v_cvt_pk_bf16_f32 v116, v80, v81\n" \
"s_waitcnt lgkmcnt(0)\n" \
"v_mfma_f32_16x16x32_bf16 v[168:171], a[184:187], v[108:111], v[168:171]\n" \
"v_exp_f32_e32 v83, v83\n" \
"v_mfma_f32_16x16x32_bf16 v[172:175], a[188:191], v[108:111], v[172:175]\n" \
"v_exp_f32_e32 v84, v84\n" \
"v_mfma_f32_16x16x32_bf16 v[172:175], a[172:175], v[104:107], v[172:175]\n" \
"v_exp_f32_e32 v85, v85\n" \
"v_smfmac_f32_16x16x64_bf16 a[196:199], a[212:215], v[104:111], v21\n" \
"v_mfma_f32_16x16x32_bf16 v[192:195], a[160:163], v[112:115], v[192:195]\n" \
"v_exp_f32_e32 v86, v86\n" \
"v_exp_f32_e32 v87, v87\n" \
"v_cvt_pk_bf16_f32 v117, v82, v83\n" \
"v_cvt_pk_bf16_f32 v118, v84, v85\n" \
"v_cvt_pk_bf16_f32 v119, v86, v87\n" \
"s_mov_b32 s64, s14\n" \
"v_add_u32_e32 v51, 0x12000, v63\n" \
"v_mfma_f32_16x16x32_bf16 v[192:195], a[176:179], v[116:119], v[192:195]\n" \
"s_xor_b32 m0, s65, 0xa000\n" \
"ds_read_b32 v58, v18 offset:512\n" \
"v_mfma_f32_16x16x32_bf16 v[196:199], a[180:183], v[116:119], v[196:199]\n" \
"buffer_load_dwordx4 v51, s[8:11], s64 offen offset:0 sc1 lds\n" \
"v_mfma_f32_16x16x32_bf16 v[196:199], a[164:167], v[112:115], v[196:199]\n" \
"v_exp_f32_e32 v88, v88\n" \
"v_mfma_f32_16x16x32_bf16 v[200:203], a[168:171], v[112:115], v[200:203]\n" \
"v_exp_f32_e32 v89, v89\n" \
"v_mfma_f32_16x16x32_bf16 v[200:203], a[184:187], v[116:119], v[200:203]\n" \
"v_exp_f32_e32 v90, v90\n" \
"v_mfma_f32_16x16x32_bf16 v[204:207], a[188:191], v[116:119], v[204:207]\n" \
"v_exp_f32_e32 v91, v91\n" \
"v_mfma_f32_16x16x32_bf16 v[204:207], a[172:175], v[112:115], v[204:207]\n" \
"v_exp_f32_e32 v92, v92\n" \
"v_smfmac_f32_16x16x64_bf16 a[200:203], a[212:215], v[112:119], v21\n" \
"v_mfma_f32_16x16x32_bf16 v[224:227], a[160:163], v[120:123], v[224:227]\n" \
"v_exp_f32_e32 v93, v93\n" \
"v_exp_f32_e32 v94, v94\n" \
"v_exp_f32_e32 v95, v95\n" \
"v_cvt_pk_bf16_f32 v124, v88, v89\n" \
"v_cvt_pk_bf16_f32 v125, v90, v91\n" \
"v_cvt_pk_bf16_f32 v126, v92, v93\n" \
"v_cvt_pk_bf16_f32 v127, v94, v95\n" \
"v_add_u32_e32 v52, 0x14000, v63\n" \
"v_add_u32_e32 v53, 0x20000, v63\n" \
"v_mfma_f32_16x16x32_bf16 v[224:227], a[176:179], v[124:127], v[224:227]\n" \
"buffer_load_dwordx4 v51, s[8:11], s64 offen offset:1024 sc1 lds\n" \
"v_mfma_f32_16x16x32_bf16 v[228:231], a[180:183], v[124:127], v[228:231]\n" \
"ds_read_b32 v59, v18 offset:640\n" \
"ds_read_b32 v60, v18 offset:768\n" \
"v_mfma_f32_16x16x32_bf16 v[228:231], a[164:167], v[120:123], v[228:231]\n" \
"ds_read_b32 v61, v18 offset:896\n" \
"ds_read_b128 a[128:131], v48 offset:0\n" \
"v_mfma_f32_16x16x32_bf16 v[232:235], a[168:171], v[120:123], v[232:235]\n" \
"ds_read_b128 a[132:135], v48 offset:1024\n" \
"ds_read_b128 a[136:139], v48 offset:2048\n" \
"v_mfma_f32_16x16x32_bf16 v[232:235], a[184:187], v[124:127], v[232:235]\n" \
"ds_read_b128 a[140:143], v48 offset:3072\n" \
"ds_read_b128 a[144:147], v48 offset:4096\n" \
"ds_read_b128 a[148:151], v48 offset:5120\n" \
"ds_read_b128 a[152:155], v48 offset:6144\n" \
"ds_read_b128 a[156:159], v48 offset:7168\n" \
"v_mfma_f32_16x16x32_bf16 v[236:239], a[188:191], v[124:127], v[236:239]\n" \
"buffer_load_dwordx4 v51, s[8:11], s64 offen offset:2048 sc1 lds\n" \
"v_mfma_f32_16x16x32_bf16 v[236:239], a[172:175], v[120:123], v[236:239]\n" \
"buffer_load_dwordx4 v51, s[8:11], s64 offen offset:3072 sc1 lds\n" \
"v_readlane_b32 s93, v11, 17\n" \
"v_readlane_b32 s92, v11, 22\n" \
"v_accvgpr_read_b32 v54, a192\n" \
"v_accvgpr_read_b32 v23, a193\n" \
"v_add_f32_e32 v54, v54, v23\n" \
"v_accvgpr_read_b32 v55, a196\n" \
"v_accvgpr_read_b32 v23, a197\n" \
"v_max_f32_e32 v62, v62, v54\n" \
"v_add_f32_e32 v55, v55, v23\n" \
"v_max_f32_e32 v62, v62, v55\n" \
"v_rcp_f32_e32 v54, v54\n" \
"v_rcp_f32_e32 v55, v55\n" \
"s_waitcnt lgkmcnt(11)\n" \
"v_mul_f32_e32 v54, v54, v58\n" \
"s_waitcnt lgkmcnt(10)\n" \
"v_mul_f32_e32 v55, v55, v59\n" \
"v_mul_f32_e32 v128, v54, v128\n" \
"v_mul_f32_e32 v129, v54, v129\n" \
"v_mul_f32_e32 v136, v54, v136\n" \
"v_mul_f32_e32 v137, v54, v137\n" \
"v_mul_f32_e32 v160, v55, v160\n" \
"v_mul_f32_e32 v161, v55, v161\n" \
"v_mul_f32_e32 v168, v55, v168\n" \
"v_mul_f32_e32 v169, v55, v169\n" \
"v_cvt_pk_bf16_f32 v128, v128, v129\n" \
"v_mul_f32_e32 v130, v54, v130\n" \
"v_mul_f32_e32 v131, v54, v131\n" \
"v_cvt_pk_bf16_f32 v136, v136, v137\n" \
"v_mul_f32_e32 v138, v54, v138\n" \
"v_mul_f32_e32 v139, v54, v139\n" \
"s_waitcnt vmcnt(10) lgkmcnt(0)\n" \
"s_barrier\n" \
"v_mfma_f32_16x16x32_bf16 v[80:83], a[128:131], a[32:35], v[40:43]\n" \
"ds_read_b128 a[160:163], v49 offset:8192\n" \
"ds_read_b128 a[164:167], v49 offset:9216\n" \
"v_mfma_f32_16x16x32_bf16 v[80:83], a[132:135], a[36:39], v[80:83]\n" \
"ds_read_b128 a[168:171], v49 offset:10240\n" \
"ds_read_b128 a[172:175], v49 offset:11264\n" \
"v_mfma_f32_16x16x32_bf16 v[80:83], a[136:139], a[40:43], v[80:83]\n" \
"s_xor_b32 m0, s65, 0x18000\n" \
"ds_read_b128 a[176:179], v49 offset:12288\n" \
"v_mfma_f32_16x16x32_bf16 v[80:83], a[140:143], a[44:47], v[80:83]\n" \
"buffer_load_dwordx4 v52, s[8:11], s64 offen offset:0 sc1 lds\n" \
"v_mfma_f32_16x16x32_bf16 v[84:87], a[156:159], a[44:47], v[40:43]\n" \
"ds_read_b128 a[180:183], v49 offset:13312\n" \
"ds_read_b128 a[184:187], v49 offset:14336\n" \
"v_mfma_f32_16x16x32_bf16 v[84:87], a[152:155], a[40:43], v[84:87]\n" \
"buffer_load_dwordx4 v52, s[8:11], s64 offen offset:1024 sc1 lds\n" \
"v_mfma_f32_16x16x32_bf16 v[84:87], a[148:151], a[36:39], v[84:87]\n" \
"ds_read_b128 a[188:191], v49 offset:15360\n" \
"v_add_u32_e32 v49, 0x10000, v48\n" \
"v_mfma_f32_16x16x32_bf16 v[84:87], a[144:147], a[32:35], v[84:87]\n" \
"buffer_load_dwordx4 v52, s[8:11], s64 offen offset:2048 sc1 lds\n" \
"v_mfma_f32_16x16x32_bf16 v[88:91], a[128:131], a[48:51], v[44:47]\n" \
"ds_read_b128 a[128:131], v48 offset:8192\n" \
"v_cvt_pk_bf16_f32 v160, v160, v161\n" \
"v_mfma_f32_16x16x32_bf16 v[88:91], a[132:135], a[52:55], v[88:91]\n" \
"ds_read_b128 a[132:135], v48 offset:9216\n" \
"v_mul_f32_e32 v162, v55, v162\n" \
"v_mfma_f32_16x16x32_bf16 v[88:91], a[136:139], a[56:59], v[88:91]\n" \
"buffer_load_dwordx4 v52, s[8:11], s64 offen offset:3072 sc1 lds\n" \
"v_mfma_f32_16x16x32_bf16 v[88:91], a[140:143], a[60:63], v[88:91]\n" \
"s_mov_b32 m0, s65\n" \
"ds_read_b128 a[136:139], v48 offset:10240\n" \
"v_mfma_f32_16x16x32_bf16 v[92:95], a[156:159], a[60:63], v[44:47]\n" \
"ds_read_b128 a[140:143], v48 offset:11264\n" \
"ds_read_b128 a[156:159], v48 offset:15360\n" \
"v_mfma_f32_16x16x32_bf16 v[92:95], a[152:155], a[56:59], v[92:95]\n" \
"buffer_load_dwordx4 v53, s[8:11], s64 offen offset:0 sc1 lds\n" \
"v_mfma_f32_16x16x32_bf16 v[92:95], a[148:151], a[52:55], v[92:95]\n" \
"ds_read_b128 a[148:151], v48 offset:13312\n" \
"ds_read_b128 a[152:155], v48 offset:14336\n" \
"v_mfma_f32_16x16x32_bf16 v[92:95], a[144:147], a[48:51], v[92:95]\n" \
"ds_read_b128 a[144:147], v48 offset:12288\n" \
"s_waitcnt lgkmcnt(12)\n" \
"v_mfma_f32_16x16x32_bf16 v[144:147], a[160:163], v[96:99], v[144:147]\n" \
"buffer_load_dwordx4 v53, s[8:11], s64 offen offset:1024 sc1 lds\n" \
"v_mfma_f32_16x16x32_bf16 v[148:151], a[164:167], v[96:99], v[148:151]\n" \
"L_pf16:\n" \
"s_cbranch_execz L_retA\n" \
"s_nop 0\n" \
"v_exp_f32_e32 v88, v88\n" \
"v_mfma_f32_16x16x32_bf16 v[152:155], a[168:171], v[96:99], v[152:155]\n" \
"v_exp_f32_e32 v89, v89\n" \
"v_mfma_f32_16x16x32_bf16 v[156:159], a[172:175], v[96:99], v[156:159]\n" \
"v_exp_f32_e32 v90, v90\n" \
"v_mfma_f32_16x16x32_bf16 v[176:179], a[160:163], v[104:107], v[176:179]\n" \
"s_xor_b32 s65, s65, 0x8000\n" \
"s_waitcnt lgkmcnt(8)\n" \
"v_mfma_f32_16x16x32_bf16 v[176:179], a[176:179], v[108:111], v[176:179]\n" \
"buffer_load_dwordx4 v53, s[8:11], s64 offen offset:2048 sc1 lds\n" \
"v_mfma_f32_16x16x32_bf16 v[180:183], a[180:183], v[108:111], v[180:183]\n" \
"v_exp_f32_e32 v91, v91\n" \
"v_mfma_f32_16x16x32_bf16 v[180:183], a[164:167], v[104:107], v[180:183]\n" \
"buffer_load_dwordx4 v53, s[8:11], s64 offen offset:3072 sc1 lds\n" \
"v_mfma_f32_16x16x32_bf16 v[184:187], a[168:171], v[104:107], v[184:187]\n" \
"s_add_u32 m0, s93, 0x0\n" \
"s_add_u32 s64, s64, 0x10000\n" \
"v_mfma_f32_16x16x32_bf16 v[184:187], a[184:187], v[108:111], v[184:187]\n" \
"v_exp_f32_e32 v92, v92\n" \
"v_mfma_f32_16x16x32_bf16 v[188:191], a[188:191], v[108:111], v[188:191]\n" \
"buffer_load_dwordx4 v0, s[80:83], s92 offen offset:0 lds\n" \
"v_mfma_f32_16x16x32_bf16 v[188:191], a[172:175], v[104:107], v[188:191]\n" \
"v_exp_f32_e32 v93, v93\n" \
"v_mfma_f32_16x16x32_bf16 v[208:211], a[160:163], v[112:115], v[208:211]\n" \
"v_exp_f32_e32 v94, v94\n" \
"v_mfma_f32_16x16x32_bf16 v[208:211], a[176:179], v[116:119], v[208:211]\n" \
"v_exp_f32_e32 v95, v95\n" \
"v_mfma_f32_16x16x32_bf16 v[212:215], a[180:183], v[116:119], v[212:215]\n" \
"buffer_load_dwordx4 v1, s[80:83], s92 offen offset:1024 lds\n" \
"v_mfma_f32_16x16x32_bf16 v[212:215], a[164:167], v[112:115], v[212:215]\n" \
"v_exp_f32_e32 v80, v80\n" \
"v_mfma_f32_16x16x32_bf16 v[216:219], a[168:171], v[112:115], v[216:219]\n" \
"v_exp_f32_e32 v81, v81\n" \
"v_mfma_f32_16x16x32_bf16 v[216:219], a[184:187], v[116:119], v[216:219]\n" \
"buffer_load_dwordx4 v2, s[80:83], s92 offen offset:2048 lds\n" \
"v_mfma_f32_16x16x32_bf16 v[220:223], a[188:191], v[116:119], v[220:223]\n" \
"v_exp_f32_e32 v82, v82\n" \
"v_mfma_f32_16x16x32_bf16 v[220:223], a[172:175], v[112:115], v[220:223]\n" \
"v_exp_f32_e32 v83, v83\n" \
"v_mfma_f32_16x16x32_bf16 v[240:243], a[160:163], v[120:123], v[240:243]\n" \
"buffer_load_dwordx4 v3, s[80:83], s92 offen offset:3072 lds\n" \
"v_mfma_f32_16x16x32_bf16 v[240:243], a[176:179], v[124:127], v[240:243]\n" \
"v_exp_f32_e32 v84, v84\n" \
"v_mfma_f32_16x16x32_bf16 v[244:247], a[180:183], v[124:127], v[244:247]\n" \
"v_exp_f32_e32 v85, v85\n" \
"v_mfma_f32_16x16x32_bf16 v[244:247], a[164:167], v[120:123], v[244:247]\n" \
"v_exp_f32_e32 v86, v86\n" \
"v_mfma_f32_16x16x32_bf16 v[248:251], a[168:171], v[120:123], v[248:251]\n" \
"v_exp_f32_e32 v87, v87\n" \
"v_mfma_f32_16x16x32_bf16 v[248:251], a[184:187], v[124:127], v[248:251]\n" \
"v_cvt_pk_bf16_f32 v112, v80, v81\n" \
"v_cvt_pk_bf16_f32 v113, v82, v83\n" \
"v_mfma_f32_16x16x32_bf16 v[252:255], a[188:191], v[124:127], v[252:255]\n" \
"v_cvt_pk_bf16_f32 v114, v84, v85\n" \
"v_cvt_pk_bf16_f32 v115, v86, v87\n" \
"v_mfma_f32_16x16x32_bf16 v[252:255], a[172:175], v[120:123], v[252:255]\n" \
"v_and_b32_e32 v112, v12, v112\n" \
"v_and_b32_e32 v113, v13, v113\n" \
"v_smfmac_f32_16x16x64_bf16 a[204:207], a[212:215], v[120:127], v21\n" \
"v_cvt_pk_bf16_f32 v120, v88, v89\n" \
"v_cvt_pk_bf16_f32 v121, v90, v91\n" \
"v_cvt_pk_bf16_f32 v122, v92, v93\n" \
"v_cvt_pk_bf16_f32 v123, v94, v95\n" \
"v_and_b32_e32 v114, v14, v114\n" \
"v_and_b32_e32 v115, v15, v115\n" \
"v_add_u32_e32 v48, s16, v20\n" \
"v_mul_f32_e32 v144, v54, v144\n" \
"v_mul_f32_e32 v145, v54, v145\n" \
"v_mul_f32_e32 v152, v54, v152\n" \
"v_mul_f32_e32 v153, v54, v153\n" \
"v_mul_f32_e32 v176, v55, v176\n" \
"v_mul_f32_e32 v177, v55, v177\n" \
"v_mul_f32_e32 v184, v55, v184\n" \
"v_mul_f32_e32 v185, v55, v185\n" \
"v_mul_f32_e32 v163, v55, v163\n" \
"v_cvt_pk_bf16_f32 v168, v168, v169\n" \
"v_mul_f32_e32 v170, v55, v170\n" \
"v_mul_f32_e32 v171, v55, v171\n" \
"v_cvt_pk_bf16_f32 v144, v144, v145\n" \
"v_mul_f32_e32 v146, v54, v146\n" \
"v_mul_f32_e32 v147, v54, v147\n" \
"v_cvt_pk_bf16_f32 v152, v152, v153\n" \
"v_mul_f32_e32 v154, v54, v154\n" \
"v_mul_f32_e32 v155, v54, v155\n" \
"v_cvt_pk_bf16_f32 v176, v176, v177\n" \
"v_mul_f32_e32 v178, v55, v178\n" \
"v_mul_f32_e32 v179, v55, v179\n" \
"v_cvt_pk_bf16_f32 v184, v184, v185\n" \
"v_mul_f32_e32 v186, v55, v186\n" \
"v_mul_f32_e32 v187, v55, v187\n" \
"v_cvt_pk_bf16_f32 v129, v130, v131\n" \
"v_mul_f32_e32 v132, v54, v132\n" \
"v_mul_f32_e32 v133, v54, v133\n" \
"v_mul_f32_e32 v134, v54, v134\n" \
"v_mul_f32_e32 v135, v54, v135\n" \
"v_cvt_pk_bf16_f32 v137, v138, v139\n" \
"v_mul_f32_e32 v140, v54, v140\n" \
"v_mul_f32_e32 v141, v54, v141\n" \
"v_mul_f32_e32 v142, v54, v142\n" \
"v_mul_f32_e32 v143, v54, v143\n" \
"v_cvt_pk_bf16_f32 v161, v162, v163\n" \
"v_mul_f32_e32 v164, v55, v164\n" \
"v_mul_f32_e32 v165, v55, v165\n" \
"v_mul_f32_e32 v166, v55, v166\n" \
"v_mul_f32_e32 v167, v55, v167\n" \
"v_cvt_pk_bf16_f32 v169, v170, v171\n" \
"v_mul_f32_e32 v172, v55, v172\n" \
"v_mul_f32_e32 v173, v55, v173\n" \
"v_mul_f32_e32 v174, v55, v174\n" \
"v_mul_f32_e32 v175, v55, v175\n" \
"v_cvt_pk_bf16_f32 v145, v146, v147\n" \
"v_mul_f32_e32 v148, v54, v148\n" \
"v_mul_f32_e32 v149, v54, v149\n" \
"v_mul_f32_e32 v150, v54, v150\n" \
"v_mul_f32_e32 v151, v54, v151\n" \
"v_cvt_pk_bf16_f32 v153, v154, v155\n" \
"v_mul_f32_e32 v156, v54, v156\n" \
"v_mul_f32_e32 v157, v54, v157\n" \
"v_mul_f32_e32 v158, v54, v158\n" \
"v_mul_f32_e32 v159, v54, v159\n" \
"v_cvt_pk_bf16_f32 v177, v178, v179\n" \
"v_mul_f32_e32 v180, v55, v180\n" \
"v_mul_f32_e32 v181, v55, v181\n" \
"v_mul_f32_e32 v182, v55, v182\n" \
"v_mul_f32_e32 v183, v55, v183\n" \
"v_cvt_pk_bf16_f32 v185, v186, v187\n" \
"v_mul_f32_e32 v188, v55, v188\n" \
"v_mul_f32_e32 v189, v55, v189\n" \
"v_mul_f32_e32 v190, v55, v190\n" \
"v_mul_f32_e32 v191, v55, v191\n" \
"v_cvt_pk_bf16_f32 v130, v132, v133\n" \
"v_cvt_pk_bf16_f32 v131, v134, v135\n" \
"buffer_store_dwordx4 v[128:131], v20, s[56:59], s68 offen offset:0 sc1\n" \
"v_cvt_pk_bf16_f32 v138, v140, v141\n" \
"v_mfma_f32_16x16x32_bf16 a[208:211], a[216:219], v[128:131], a[208:211]\n" \
"v_cvt_pk_bf16_f32 v139, v142, v143\n" \
"buffer_store_dwordx4 v[136:139], v20, s[56:59], s68 offen offset:1024 sc1\n" \
"s_waitcnt vmcnt(14) lgkmcnt(0)\n" \
"s_barrier\n" \
"v_mfma_f32_16x16x32_bf16 v[88:91], a[128:131], a[48:51], v[44:47]\n" \
"s_xor_b32 m0, s65, 0x12000\n" \
"ds_read_b128 a[160:163], v49 offset:0\n" \
"v_mfma_f32_16x16x32_bf16 v[88:91], a[132:135], a[52:55], v[88:91]\n" \
"buffer_load_dwordx4 v50, s[8:11], s64 offen offset:0 sc1 lds\n" \
"v_mfma_f32_16x16x32_bf16 v[88:91], a[136:139], a[56:59], v[88:91]\n" \
"ds_read_b128 a[164:167], v49 offset:1024\n" \
"ds_read_b128 a[168:171], v49 offset:2048\n" \
"v_mfma_f32_16x16x32_bf16 v[88:91], a[140:143], a[60:63], v[88:91]\n" \
"buffer_load_dwordx4 v50, s[8:11], s64 offen offset:1024 sc1 lds\n" \
"v_mfma_f32_16x16x32_bf16 v[92:95], a[156:159], a[60:63], v[44:47]\n" \
"ds_read_b128 a[172:175], v49 offset:3072\n" \
"ds_read_b128 a[176:179], v49 offset:8192\n" \
"v_mfma_f32_16x16x32_bf16 v[92:95], a[152:155], a[56:59], v[92:95]\n" \
"ds_read_b128 a[180:183], v49 offset:9216\n" \
"ds_read_b128 a[184:187], v49 offset:10240\n" \
"v_mfma_f32_16x16x32_bf16 v[92:95], a[148:151], a[52:55], v[92:95]\n" \
"ds_read_b128 a[188:191], v49 offset:11264\n" \
"ds_read_b128 a[0:3], v49 offset:4096\n" \
"v_mfma_f32_16x16x32_bf16 v[92:95], a[144:147], a[48:51], v[92:95]\n" \
"buffer_load_dwordx4 v50, s[8:11], s64 offen offset:2048 sc1 lds\n" \
"v_mfma_f32_16x16x32_bf16 v[32:35], a[220:223], a[64:67], 0\n" \
"ds_read_b128 a[4:7], v49 offset:5120\n" \
"ds_read_b128 a[8:11], v49 offset:6144\n" \
"v_mfma_f32_16x16x32_bf16 v[36:39], a[220:223], a[80:83], 0\n" \
"ds_read_b128 a[12:15], v49 offset:7168\n" \
"ds_read_b128 a[16:19], v49 offset:12288\n" \
"v_mfma_f32_16x16x32_bf16 v[40:43], a[220:223], a[96:99], 0\n" \
"ds_read_b128 a[20:23], v49 offset:13312\n" \
"ds_read_b128 a[24:27], v49 offset:14336\n" \
"v_mfma_f32_16x16x32_bf16 v[44:47], a[220:223], a[112:115], 0\n" \
"buffer_load_dwordx4 v50, s[8:11], s64 offen offset:3072 sc1 lds\n" \
"v_mfma_f32_16x16x32_bf16 v[32:35], a[224:227], a[68:71], v[32:35]\n" \
"s_xor_b32 m0, s65, 0xa000\n" \
"ds_read_b128 a[28:31], v49 offset:15360\n" \
"v_mfma_f32_16x16x32_bf16 v[36:39], a[224:227], a[84:87], v[36:39]\n" \
"v_exp_f32_e32 v88, v88\n" \
"v_mfma_f32_16x16x32_bf16 v[40:43], a[224:227], a[100:103], v[40:43]\n" \
"buffer_load_dwordx4 v51, s[8:11], s64 offen offset:0 sc1 lds\n" \
"v_mfma_f32_16x16x32_bf16 v[44:47], a[224:227], a[116:119], v[44:47]\n" \
"v_exp_f32_e32 v89, v89\n" \
"v_mfma_f32_16x16x32_bf16 v[32:35], a[228:231], a[72:75], v[32:35]\n" \
"v_exp_f32_e32 v90, v90\n" \
"v_mfma_f32_16x16x32_bf16 v[36:39], a[228:231], a[88:91], v[36:39]\n" \
"v_exp_f32_e32 v91, v91\n" \
"v_mfma_f32_16x16x32_bf16 v[40:43], a[228:231], a[104:107], v[40:43]\n" \
"buffer_load_dwordx4 v51, s[8:11], s64 offen offset:1024 sc1 lds\n" \
"v_mfma_f32_16x16x32_bf16 v[44:47], a[228:231], a[120:123], v[44:47]\n" \
"v_exp_f32_e32 v92, v92\n" \
"v_mfma_f32_16x16x32_bf16 v[32:35], a[232:235], a[76:79], v[32:35]\n" \
"v_exp_f32_e32 v93, v93\n" \
"v_mfma_f32_16x16x32_bf16 v[36:39], a[232:235], a[92:95], v[36:39]\n" \
"v_exp_f32_e32 v94, v94\n" \
"v_mfma_f32_16x16x32_bf16 v[40:43], a[232:235], a[108:111], v[40:43]\n" \
"buffer_load_dwordx4 v51, s[8:11], s64 offen offset:2048 sc1 lds\n" \
"v_mfma_f32_16x16x32_bf16 v[44:47], a[232:235], a[124:127], v[44:47]\n" \
"v_cvt_pk_bf16_f32 v124, v88, v89\n" \
"s_waitcnt lgkmcnt(3)\n" \
"v_mfma_f32_16x16x32_bf16 v[192:195], a[160:163], v[112:115], v[192:195]\n" \
"v_exp_f32_e32 v95, v95\n" \
"v_mfma_f32_16x16x32_bf16 v[196:199], a[164:167], v[112:115], v[196:199]\n" \
"buffer_load_dwordx4 v51, s[8:11], s64 offen offset:3072 sc1 lds\n" \
"v_mfma_f32_16x16x32_bf16 v[200:203], a[168:171], v[112:115], v[200:203]\n" \
"s_add_u32 m0, s93, 0x1000\n" \
"v_cvt_pk_bf16_f32 v125, v90, v91\n" \
"v_mfma_f32_16x16x32_bf16 v[204:207], a[172:175], v[112:115], v[204:207]\n" \
"v_cvt_pk_bf16_f32 v126, v92, v93\n" \
"v_cvt_pk_bf16_f32 v127, v94, v95\n" \
"v_mfma_f32_16x16x32_bf16 v[208:211], a[176:179], v[112:115], v[208:211]\n" \
"v_and_b32_e32 v124, v12, v124\n" \
"v_and_b32_e32 v125, v13, v125\n" \
"v_mfma_f32_16x16x32_bf16 v[212:215], a[180:183], v[112:115], v[212:215]\n" \
"buffer_load_dwordx4 v4, s[80:83], s92 offen offset:0 lds\n" \
"v_mfma_f32_16x16x32_bf16 v[216:219], a[184:187], v[112:115], v[216:219]\n" \
"v_and_b32_e32 v126, v14, v126\n" \
"v_and_b32_e32 v127, v15, v127\n" \
"v_mfma_f32_16x16x32_bf16 v[220:223], a[188:191], v[112:115], v[220:223]\n" \
"ds_read_b128 a[128:131], v48 offset:0\n" \
"ds_read_b128 a[132:135], v48 offset:1024\n" \
"v_mfma_f32_16x16x32_bf16 a[200:203], a[252:255], v[112:115], a[200:203]\n" \
"ds_read_b128 a[136:139], v48 offset:2048\n" \
"ds_read_b128 a[140:143], v48 offset:3072\n" \
"v_mfma_f32_16x16x32_bf16 v[224:227], a[160:163], v[120:123], v[224:227]\n" \
"buffer_load_dwordx4 v5, s[80:83], s92 offen offset:1024 lds\n" \
"v_mfma_f32_16x16x32_bf16 v[228:231], a[164:167], v[120:123], v[228:231]\n" \
"ds_read_b128 a[144:147], v48 offset:4096\n" \
"ds_read_b128 a[148:151], v48 offset:5120\n" \
"v_mfma_f32_16x16x32_bf16 v[232:235], a[168:171], v[120:123], v[232:235]\n" \
"ds_read_b128 a[152:155], v48 offset:6144\n" \
"ds_read_b128 a[156:159], v48 offset:7168\n" \
"v_mfma_f32_16x16x32_bf16 v[236:239], a[172:175], v[120:123], v[236:239]\n" \
"buffer_load_dwordx4 v6, s[80:83], s92 offen offset:2048 lds\n" \
"v_mfma_f32_16x16x32_bf16 v[240:243], a[176:179], v[120:123], v[240:243]\n" \
"v_cvt_pk_bf16_f32 v162, v164, v165\n" \
"v_cvt_pk_bf16_f32 v163, v166, v167\n" \
"buffer_store_dwordx4 v[160:163], v20, s[56:59], s69 offen offset:0 sc1\n" \
"v_mfma_f32_16x16x32_bf16 v[244:247], a[180:183], v[120:123], v[244:247]\n" \
"v_cvt_pk_bf16_f32 v170, v172, v173\n" \
"v_cvt_pk_bf16_f32 v171, v174, v175\n" \
"buffer_store_dwordx4 v[168:171], v20, s[56:59], s69 offen offset:1024 sc1\n" \
"v_mfma_f32_16x16x32_bf16 v[248:251], a[184:187], v[120:123], v[248:251]\n" \
"v_cvt_pk_bf16_f32 v146, v148, v149\n" \
"v_cvt_pk_bf16_f32 v147, v150, v151\n" \
"buffer_store_dwordx4 v[144:147], v20, s[56:59], s68 offen offset:2048 sc1\n" \
"v_mfma_f32_16x16x32_bf16 v[252:255], a[188:191], v[120:123], v[252:255]\n" \
"buffer_load_dwordx4 v7, s[80:83], s92 offen offset:3072 lds\n" \
"v_mfma_f32_16x16x32_bf16 a[204:207], a[252:255], v[120:123], a[204:207]\n" \
"v_cvt_pk_bf16_f32 v154, v156, v157\n" \
"v_cvt_pk_bf16_f32 v155, v158, v159\n" \
"buffer_store_dwordx4 v[152:155], v20, s[56:59], s68 offen offset:3072 sc1\n" \
"v_mfma_f32_16x16x32_bf16 v[224:227], a[0:3], v[124:127], v[224:227]\n" \
"v_cvt_pk_bf16_f32 v178, v180, v181\n" \
"v_cvt_pk_bf16_f32 v179, v182, v183\n" \
"buffer_store_dwordx4 v[176:179], v20, s[56:59], s69 offen offset:2048 sc1\n" \
"v_mfma_f32_16x16x32_bf16 v[228:231], a[4:7], v[124:127], v[228:231]\n" \
"v_cvt_pk_bf16_f32 v186, v188, v189\n" \
"v_cvt_pk_bf16_f32 v187, v190, v191\n" \
"buffer_store_dwordx4 v[184:187], v20, s[56:59], s69 offen offset:3072 sc1\n" \
"v_mfma_f32_16x16x32_bf16 v[232:235], a[8:11], v[124:127], v[232:235]\n" \
"s_add_u32 m0, s93, 0x2000\n" \
"v_accvgpr_read_b32 v56, a200\n" \
"v_mfma_f32_16x16x32_bf16 v[236:239], a[12:15], v[124:127], v[236:239]\n" \
"buffer_load_dwordx4 v8, s[80:83], s92 offen offset:0 lds\n" \
"v_mfma_f32_16x16x32_bf16 v[240:243], a[16:19], v[124:127], v[240:243]\n" \
"v_accvgpr_read_b32 v23, a201\n" \
"s_waitcnt lgkmcnt(0)\n" \
"v_mfma_f32_16x16x32_bf16 v[244:247], a[20:23], v[124:127], v[244:247]\n" \
"v_add_f32_e32 v56, v56, v23\n" \
"v_max_f32_e32 v62, v62, v56\n" \
"v_mfma_f32_16x16x32_bf16 v[248:251], a[24:27], v[124:127], v[248:251]\n" \
"v_rcp_f32_e32 v56, v56\n" \
"v_mfma_f32_16x16x32_bf16 v[252:255], a[28:31], v[124:127], v[252:255]\n" \
"v_mul_f32_e32 v56, v56, v60\n" \
"v_mul_f32_e32 v192, v56, v192\n" \
"v_mfma_f32_16x16x32_bf16 a[204:207], a[252:255], v[124:127], a[204:207]\n" \
"v_mul_f32_e32 v193, v56, v193\n" \
"v_cvt_pk_bf16_f32 v192, v192, v193\n" \
"v_mfma_f32_16x16x32_bf16 a[208:211], a[216:219], v[136:139], a[208:211]\n" \
"v_mul_f32_e32 v194, v56, v194\n" \
"v_mul_f32_e32 v195, v56, v195\n" \
"v_mfma_f32_16x16x32_bf16 a[208:211], a[216:219], v[160:163], a[208:211]\n" \
"v_cvt_pk_bf16_f32 v193, v194, v195\n" \
"v_mul_f32_e32 v196, v56, v196\n" \
"v_mfma_f32_16x16x32_bf16 a[208:211], a[216:219], v[168:171], a[208:211]\n" \
"v_mul_f32_e32 v197, v56, v197\n" \
"v_mul_f32_e32 v198, v56, v198\n" \
"v_mfma_f32_16x16x32_bf16 a[208:211], a[216:219], v[144:147], a[208:211]\n" \
"v_mul_f32_e32 v199, v56, v199\n" \
"v_mul_f32_e32 v200, v56, v200\n" \
"v_mfma_f32_16x16x32_bf16 a[208:211], a[216:219], v[152:155], a[208:211]\n" \
"v_mul_f32_e32 v201, v56, v201\n" \
"v_accvgpr_read_b32 v57, a204\n" \
"v_mfma_f32_16x16x32_bf16 a[208:211], a[216:219], v[176:179], a[208:211]\n" \
"v_accvgpr_read_b32 v23, a205\n" \
"v_cvt_pk_bf16_f32 v194, v196, v197\n" \
"v_mfma_f32_16x16x32_bf16 a[208:211], a[216:219], v[184:187], a[208:211]\n" \
"v_cvt_pk_bf16_f32 v195, v198, v199\n" \
"buffer_store_dwordx4 v[192:195], v20, s[56:59], s70 offen offset:0 sc1\n" \
"v_mfma_f32_32x32x16_bf16 v[128:143], a[216:219], a[216:219], 0\n" \
"v_cvt_pk_bf16_f32 v200, v200, v201\n" \
"v_mul_f32_e32 v202, v56, v202\n" \
"v_mul_f32_e32 v203, v56, v203\n" \
"v_add_f32_e32 v57, v57, v23\n" \
"v_cvt_pk_bf16_f32 v201, v202, v203\n" \
"v_mul_f32_e32 v204, v56, v204\n" \
"v_mfma_f32_16x16x32_bf16 a[208:211], a[216:219], v[192:195], a[208:211]\n" \
"v_mul_f32_e32 v205, v56, v205\n" \
"v_mul_f32_e32 v206, v56, v206\n" \
"v_mfma_f32_32x32x16_bf16 v[144:159], a[216:219], a[216:219], 0\n" \
"v_mul_f32_e32 v207, v56, v207\n" \
"v_mul_f32_e32 v208, v56, v208\n" \
"v_mul_f32_e32 v209, v56, v209\n" \
"v_max_f32_e32 v62, v62, v57\n" \
"v_cvt_pk_bf16_f32 v202, v204, v205\n" \
"v_cvt_pk_bf16_f32 v203, v206, v207\n" \
"buffer_store_dwordx4 v[200:203], v20, s[56:59], s70 offen offset:1024 sc1\n" \
"v_mfma_f32_32x32x16_bf16 v[160:175], a[216:219], a[216:219], 0\n" \
"v_cvt_pk_bf16_f32 v208, v208, v209\n" \
"v_mul_f32_e32 v210, v56, v210\n" \
"v_mul_f32_e32 v211, v56, v211\n" \
"v_rcp_f32_e32 v57, v57\n" \
"v_cvt_pk_bf16_f32 v209, v210, v211\n" \
"v_mfma_f32_16x16x32_bf16 a[208:211], a[216:219], v[200:203], a[208:211]\n" \
"v_mul_f32_e32 v212, v56, v212\n" \
"v_mul_f32_e32 v213, v56, v213\n" \
"v_mfma_f32_32x32x16_bf16 v[176:191], a[216:219], a[216:219], 0\n" \
"v_mul_f32_e32 v214, v56, v214\n" \
"v_mul_f32_e32 v215, v56, v215\n" \
"v_mul_f32_e32 v216, v56, v216\n" \
"v_mul_f32_e32 v217, v56, v217\n" \
"v_cvt_pk_bf16_f32 v210, v212, v213\n" \
"v_cvt_pk_bf16_f32 v211, v214, v215\n" \
"s_waitcnt vmcnt(21)\n" \
"s_barrier\n" \
"buffer_store_dwordx4 v[208:211], v20, s[56:59], s70 offen offset:2048 sc1\n" \
"v_mfma_f32_16x16x32_bf16 v[64:67], a[128:131], a[64:67], v[32:35]\n" \
"s_xor_b32 m0, s65, 0x18000\n" \
"v_cvt_pk_bf16_f32 v216, v216, v217\n" \
"v_mfma_f32_16x16x32_bf16 v[64:67], a[132:135], a[68:71], v[64:67]\n" \
"buffer_load_dwordx4 v52, s[8:11], s64 offen offset:0 sc1 lds\n" \
"v_mfma_f32_16x16x32_bf16 v[64:67], a[136:139], a[72:75], v[64:67]\n" \
"v_mul_f32_e32 v218, v56, v218\n" \
"v_mul_f32_e32 v219, v56, v219\n" \
"v_mfma_f32_16x16x32_bf16 v[64:67], a[140:143], a[76:79], v[64:67]\n" \
"buffer_load_dwordx4 v52, s[8:11], s64 offen offset:1024 sc1 lds\n" \
"v_mfma_f32_16x16x32_bf16 v[68:71], a[156:159], a[76:79], v[32:35]\n" \
"v_mul_f32_e32 v57, v57, v61\n" \
"v_cvt_pk_bf16_f32 v217, v218, v219\n" \
"v_mfma_f32_16x16x32_bf16 v[68:71], a[152:155], a[72:75], v[68:71]\n" \
"v_mul_f32_e32 v220, v56, v220\n" \
"v_mul_f32_e32 v221, v56, v221\n" \
"v_mfma_f32_16x16x32_bf16 v[68:71], a[148:151], a[68:71], v[68:71]\n" \
"v_mul_f32_e32 v222, v56, v222\n" \
"v_mul_f32_e32 v223, v56, v223\n" \
"v_mfma_f32_16x16x32_bf16 v[68:71], a[144:147], a[64:67], v[68:71]\n" \
"buffer_load_dwordx4 v52, s[8:11], s64 offen offset:2048 sc1 lds\n" \
"v_mfma_f32_16x16x32_bf16 v[72:75], a[128:131], a[80:83], v[36:39]\n" \
"v_mul_f32_e32 v224, v57, v224\n" \
"v_mul_f32_e32 v225, v57, v225\n" \
"v_mfma_f32_16x16x32_bf16 v[72:75], a[132:135], a[84:87], v[72:75]\n" \
"v_cvt_pk_bf16_f32 v218, v220, v221\n" \
"v_cvt_pk_bf16_f32 v219, v222, v223\n" \
"buffer_store_dwordx4 v[216:219], v20, s[56:59], s70 offen offset:3072 sc1\n" \
"v_mfma_f32_16x16x32_bf16 v[72:75], a[136:139], a[88:91], v[72:75]\n" \
"v_cvt_pk_bf16_f32 v224, v224, v225\n" \
"v_mul_f32_e32 v226, v57, v226\n" \
"v_mfma_f32_16x16x32_bf16 v[72:75], a[140:143], a[92:95], v[72:75]\n" \
"buffer_load_dwordx4 v52, s[8:11], s64 offen offset:3072 sc1 lds\n" \
"v_mfma_f32_16x16x32_bf16 v[76:79], a[156:159], a[92:95], v[36:39]\n" \
"s_mov_b32 m0, s65\n" \
"v_mul_f32_e32 v227, v57, v227\n" \
"v_mfma_f32_16x16x32_bf16 v[76:79], a[152:155], a[88:91], v[76:79]\n" \
"v_cvt_pk_bf16_f32 v225, v226, v227\n" \
"v_mul_f32_e32 v228, v57, v228\n" \
"v_mfma_f32_16x16x32_bf16 v[76:79], a[148:151], a[84:87], v[76:79]\n" \
"v_mul_f32_e32 v229, v57, v229\n" \
"v_mul_f32_e32 v230, v57, v230\n" \
"v_mfma_f32_16x16x32_bf16 v[76:79], a[144:147], a[80:83], v[76:79]\n" \
"buffer_load_dwordx4 v53, s[8:11], s64 offen offset:0 sc1 lds\n" \
"v_mfma_f32_16x16x32_bf16 v[80:83], a[128:131], a[96:99], v[40:43]\n" \
"v_mul_f32_e32 v231, v57, v231\n" \
"v_mul_f32_e32 v232, v57, v232\n" \
"v_mfma_f32_16x16x32_bf16 v[80:83], a[132:135], a[100:103], v[80:83]\n" \
"v_mul_f32_e32 v233, v57, v233\n" \
"v_cvt_pk_bf16_f32 v226, v228, v229\n" \
"v_mfma_f32_16x16x32_bf16 v[80:83], a[136:139], a[104:107], v[80:83]\n" \
"buffer_load_dwordx4 v53, s[8:11], s64 offen offset:1024 sc1 lds\n" \
"v_mfma_f32_16x16x32_bf16 v[80:83], a[140:143], a[108:111], v[80:83]\n" \
"v_cvt_pk_bf16_f32 v227, v230, v231\n" \
"v_cvt_pk_bf16_f32 v232, v232, v233\n" \
"buffer_store_dwordx4 v[224:227], v20, s[56:59], s71 offen offset:0 sc1\n" \
"v_mfma_f32_16x16x32_bf16 v[84:87], a[156:159], a[108:111], v[40:43]\n" \
"v_mul_f32_e32 v234, v57, v234\n" \
"v_mul_f32_e32 v235, v57, v235\n" \
"v_mfma_f32_16x16x32_bf16 v[84:87], a[152:155], a[104:107], v[84:87]\n" \
"v_cvt_pk_bf16_f32 v233, v234, v235\n" \
"v_mul_f32_e32 v236, v57, v236\n" \
"v_mfma_f32_16x16x32_bf16 v[84:87], a[148:151], a[100:103], v[84:87]\n" \
"buffer_load_dwordx4 v53, s[8:11], s64 offen offset:2048 sc1 lds\n" \
"v_mfma_f32_16x16x32_bf16 v[84:87], a[144:147], a[96:99], v[84:87]\n" \
"v_mul_f32_e32 v237, v57, v237\n" \
"v_mul_f32_e32 v238, v57, v238\n" \
"v_mfma_f32_16x16x32_bf16 v[88:91], a[128:131], a[112:115], v[44:47]\n" \
"ds_read_b128 a[128:131], v48 offset:8192\n" \
"v_mul_f32_e32 v239, v57, v239\n" \
"v_mfma_f32_16x16x32_bf16 v[88:91], a[132:135], a[116:119], v[88:91]\n" \
"ds_read_b128 a[132:135], v48 offset:9216\n" \
"v_mul_f32_e32 v240, v57, v240\n" \
"v_mfma_f32_16x16x32_bf16 v[88:91], a[136:139], a[120:123], v[88:91]\n" \
"ds_read_b128 a[136:139], v48 offset:10240\n" \
"v_mul_f32_e32 v241, v57, v241\n" \
"v_mfma_f32_16x16x32_bf16 v[88:91], a[140:143], a[124:127], v[88:91]\n" \
"ds_read_b128 a[140:143], v48 offset:11264\n" \
"v_cvt_pk_bf16_f32 v234, v236, v237\n" \
"v_mfma_f32_16x16x32_bf16 v[92:95], a[156:159], a[124:127], v[44:47]\n" \
"ds_read_b128 a[156:159], v48 offset:15360\n" \
"v_cvt_pk_bf16_f32 v235, v238, v239\n" \
"buffer_store_dwordx4 v[232:235], v20, s[56:59], s71 offen offset:1024 sc1\n" \
"v_mfma_f32_16x16x32_bf16 v[92:95], a[152:155], a[120:123], v[92:95]\n" \
"ds_read_b128 a[152:155], v48 offset:14336\n" \
"v_cvt_pk_bf16_f32 v240, v240, v241\n" \
"v_mfma_f32_16x16x32_bf16 v[92:95], a[148:151], a[116:119], v[92:95]\n" \
"ds_read_b128 a[148:151], v48 offset:13312\n" \
"v_mul_f32_e32 v242, v57, v242\n" \
"v_mfma_f32_16x16x32_bf16 v[92:95], a[144:147], a[112:115], v[92:95]\n" \
"ds_read_b128 a[144:147], v48 offset:12288\n" \
"v_mul_f32_e32 v243, v57, v243\n" \
"v_mfma_f32_16x16x32_bf16 a[208:211], a[216:219], v[208:211], a[208:211]\n" \
"v_cvt_pk_bf16_f32 v241, v242, v243\n" \
"v_mul_f32_e32 v244, v57, v244\n" \
"v_mfma_f32_16x16x32_bf16 a[208:211], a[216:219], v[216:219], a[208:211]\n" \
"v_mul_f32_e32 v245, v57, v245\n" \
"v_mul_f32_e32 v246, v57, v246\n" \
"v_mfma_f32_16x16x32_bf16 a[208:211], a[216:219], v[224:227], a[208:211]\n" \
"v_mul_f32_e32 v247, v57, v247\n" \
"v_mul_f32_e32 v248, v57, v248\n" \
"v_mfma_f32_16x16x32_bf16 a[208:211], a[216:219], v[232:235], a[208:211]\n" \
"v_mul_f32_e32 v249, v57, v249\n" \
"v_cvt_pk_bf16_f32 v242, v244, v245\n" \
"v_mfma_f32_32x32x16_bf16 v[192:207], a[216:219], a[216:219], 0\n" \
"v_cvt_pk_bf16_f32 v243, v246, v247\n" \
"buffer_store_dwordx4 v[240:243], v20, s[56:59], s71 offen offset:2048 sc1\n" \
"v_cvt_pk_bf16_f32 v248, v248, v249\n" \
"v_mfma_f32_16x16x32_bf16 a[208:211], a[216:219], v[240:243], a[208:211]\n" \
"v_mul_f32_e32 v250, v57, v250\n" \
"v_mul_f32_e32 v251, v57, v251\n" \
"v_mfma_f32_32x32x16_bf16 v[208:223], a[216:219], a[216:219], 0\n" \
"v_cvt_pk_bf16_f32 v249, v250, v251\n" \
"v_mul_f32_e32 v252, v57, v252\n" \
"v_mul_f32_e32 v253, v57, v253\n" \
"v_mul_f32_e32 v254, v57, v254\n" \
"v_mul_f32_e32 v255, v57, v255\n" \
"v_cvt_pk_bf16_f32 v250, v252, v253\n" \
"v_mfma_f32_32x32x16_bf16 v[224:239], a[216:219], a[216:219], 0\n" \
"v_cvt_pk_bf16_f32 v251, v254, v255\n" \
"buffer_store_dwordx4 v[248:251], v20, s[56:59], s71 offen offset:3072 sc1\n" \
"buffer_load_dwordx4 v53, s[8:11], s64 offen offset:3072 sc1 lds\n" \
"v_mfma_f32_16x16x32_bf16 a[208:211], a[216:219], v[248:251], a[208:211]\n" \
"v_cmp_lt_f32_e64 s[72:73], s100, v62\n" \
"v_readlane_b32 s95, v11, 2\n" \
"v_mfma_f32_32x32x16_bf16 v[240:255], a[216:219], a[216:219], 0\n" \
"v_exp_f32_e32 v64, v64\n" \
"v_exp_f32_e32 v65, v65\n" \
"v_exp_f32_e32 v66, v66\n" \
"v_mfma_f32_32x32x16_bf16 a[192:207], a[216:219], a[216:219], 0\n" \
"v_exp_f32_e32 v67, v67\n" \
"v_accvgpr_read_b32 v63, a208\n" \
"v_cmp_u_f32_e32 vcc, v63, v63\n" \
"v_exp_f32_e32 v68, v68\n" \
"s_or_b64 vcc, s[72:73], vcc\n" \
"s_cselect_b32 s95, 0x40000000, s95\n" \
"s_cselect_b32 s94, 0x7f800000, 0\n" \
"v_exp_f32_e32 v69, v69\n" \
"v_exp_f32_e32 v70, v70\n" \
"v_exp_f32_e32 v71, v71\n" \
"v_exp_f32_e32 v72, v72\n" \
"v_exp_f32_e32 v73, v73\n" \
"v_exp_f32_e32 v74, v74\n" \
"v_exp_f32_e32 v75, v75\n" \
"v_exp_f32_e32 v76, v76\n" \
"v_exp_f32_e32 v77, v77\n" \
"v_exp_f32_e32 v78, v78\n" \
"v_exp_f32_e32 v79, v79\n" \
"v_exp_f32_e32 v80, v80\n" \
"v_exp_f32_e32 v81, v81\n" \
"v_exp_f32_e32 v82, v82\n" \
"v_exp_f32_e32 v83, v83\n" \
"v_exp_f32_e32 v84, v84\n" \
"v_exp_f32_e32 v85, v85\n" \
"v_exp_f32_e32 v86, v86\n" \
"v_exp_f32_e32 v87, v87\n" \
"v_add_u32_e32 v49, 0x10000, v48\n" \
"s_xor_b32 s65, s65, 0x8000\n" \
"s_add_u32 s64, s64, 0x10000\n" \
"v_exp_f32_e32 v88, v88\n" \
"v_exp_f32_e32 v89, v89\n" \
"v_exp_f32_e32 v90, v90\n" \
"v_exp_f32_e32 v91, v91\n" \
"v_cvt_pk_bf16_f32 v96, v64, v65\n" \
"v_cvt_pk_bf16_f32 v97, v66, v67\n" \
"v_cvt_pk_bf16_f32 v98, v68, v69\n" \
"v_cvt_pk_bf16_f32 v99, v70, v71\n" \
"v_cvt_pk_bf16_f32 v104, v72, v73\n" \
"s_waitcnt lgkmcnt(0)\n" \
"v_cvt_pk_bf16_f32 v105, v74, v75\n" \
"v_cvt_pk_bf16_f32 v106, v76, v77\n" \
"v_cvt_pk_bf16_f32 v107, v78, v79\n" \
"v_cvt_pk_bf16_f32 v112, v80, v81\n" \
"v_cvt_pk_bf16_f32 v113, v82, v83\n" \
"v_cvt_pk_bf16_f32 v114, v84, v85\n" \
"v_cvt_pk_bf16_f32 v115, v86, v87\n" \
"v_xor_b32_e32 v48, 0x8000, v48\n" \
"s_sub_u32 s66, s19, 1\n" \
"s_cbranch_scc0 L_2_%=\n" \
"s_waitcnt vmcnt(14)\n" \
"s_barrier\n" \
"L_f1_%=:\n" \
"L_pf17:\n" \
"s_cbranch_execz L_retA\n" \
"s_nop 0\n" \
"ds_read_b128 a[160:163], v49 offset:0\n" \
"ds_read_b128 a[164:167], v49 offset:1024\n" \
"ds_read_b128 a[168:171], v49 offset:2048\n" \
"ds_read_b128 a[172:175], v49 offset:3072\n" \
"ds_read_b128 a[176:179], v49 offset:8192\n" \
"ds_read_b128 a[180:183], v49 offset:9216\n" \
"v_mfma_f32_16x16x32_bf16 v[72:75], a[128:131], a[80:83], v[36:39]\n" \
"s_add_u32 s68, s61, 0x0\n" \
"s_add_u32 s69, s61, 0x49000\n" \
"ds_read_b128 a[184:187], v49 offset:10240\n" \
"v_mfma_f32_16x16x32_bf16 v[72:75], a[132:135], a[84:87], v[72:75]\n" \
"ds_read_b128 a[188:191], v49 offset:11264\n" \
"v_add_u32_e32 v56, 0x49000, v20\n" \
"v_mfma_f32_16x16x32_bf16 v[72:75], a[136:139], a[88:91], v[72:75]\n" \
"v_readlane_b32 s97, v11, 32\n" \
"v_readlane_b32 s98, v11, 33\n" \
"v_mfma_f32_16x16x32_bf16 v[72:75], a[140:143], a[92:95], v[72:75]\n" \
"v_and_b32_e32 v96, v12, v96\n" \
"v_and_b32_e32 v97, v13, v97\n" \
"v_mfma_f32_16x16x32_bf16 v[76:79], a[156:159], a[92:95], v[36:39]\n" \
"buffer_load_dwordx4 a[32:35], v20, s[56:59], s97 offen offset:0 sc1\n" \
"v_mfma_f32_16x16x32_bf16 v[76:79], a[152:155], a[88:91], v[76:79]\n" \
"v_and_b32_e32 v98, v14, v98\n" \
"v_and_b32_e32 v99, v15, v99\n" \
"v_mfma_f32_16x16x32_bf16 v[76:79], a[148:151], a[84:87], v[76:79]\n" \
"ds_read_b128 a[0:3], v20 offset:8192\n" \
"v_mov_b32_e32 v61, s62\n" \
"v_mfma_f32_16x16x32_bf16 v[76:79], a[144:147], a[80:83], v[76:79]\n" \
"buffer_load_dwordx4 a[40:43], v56, s[56:59], s97 offen offset:0 sc1\n" \
"ds_read_b128 a[4:7], v20 offset:9216\n" \
"ds_read_b128 a[8:11], v20 offset:10240\n" \
"s_waitcnt lgkmcnt(10)\n" \
"v_mfma_f32_16x16x32_bf16 v[128:131], a[160:163], v[96:99], v[128:131]\n" \
"ds_read_b128 a[12:15], v20 offset:11264\n" \
"s_waitcnt lgkmcnt(10)\n" \
"v_mfma_f32_16x16x32_bf16 v[132:135], a[164:167], v[96:99], v[132:135]\n" \
"ds_read_b128 a[28:31], v20 offset:15360\n" \
"buffer_load_dwordx4 a[48:51], v20, s[56:59], s98 offen offset:0 sc1\n" \
"s_waitcnt lgkmcnt(10)\n" \
"v_mfma_f32_16x16x32_bf16 v[136:139], a[168:171], v[96:99], v[136:139]\n" \
"ds_read_b128 a[24:27], v20 offset:14336\n" \
"s_waitcnt lgkmcnt(10)\n" \
"v_mfma_f32_16x16x32_bf16 v[140:143], a[172:175], v[96:99], v[140:143]\n" \
"ds_read_b128 a[20:23], v20 offset:13312\n" \
"buffer_load_dwordx4 a[56:59], v56, s[56:59], s98 offen offset:0 sc1\n" \
"s_waitcnt lgkmcnt(9)\n" \
"v_mfma_f32_16x16x32_bf16 v[144:147], a[176:179], v[96:99], v[144:147]\n" \
"ds_read_b128 a[16:19], v20 offset:12288\n" \
"v_add_u32_e32 v50, s15, v48\n" \
"v_mfma_f32_16x16x32_bf16 v[148:151], a[180:183], v[96:99], v[148:151]\n" \
"v_xor_b32_e32 v48, 0x4000, v49\n" \
"s_waitcnt lgkmcnt(8)\n" \
"v_mfma_f32_16x16x32_bf16 v[152:155], a[184:187], v[96:99], v[152:155]\n" \
"buffer_load_dwordx4 a[36:39], v20, s[56:59], s97 offen offset:1024 sc1\n" \
"v_mfma_f32_16x16x32_bf16 v[156:159], a[188:191], v[96:99], v[156:159]\n" \
"ds_read_b32 v58, v18 offset:256\n" \
"ds_read_b32 v59, v18 offset:384\n" \
"v_mfma_f32_16x16x32_bf16 a[192:195], a[252:255], v[96:99], a[192:195]\n" \
"ds_read_b128 a[96:99], v48 offset:4096\n" \
"ds_read_b128 a[100:103], v48 offset:5120\n" \
"buffer_load_dwordx4 a[44:47], v56, s[56:59], s97 offen offset:1024 sc1\n" \
"v_mfma_f32_16x16x32_bf16 v[160:163], a[160:163], v[104:107], v[160:163]\n" \
"ds_read_b128 a[104:107], v48 offset:6144\n" \
"ds_read_b128 a[108:111], v48 offset:7168\n" \
"v_mfma_f32_16x16x32_bf16 v[164:167], a[164:167], v[104:107], v[164:167]\n" \
"ds_read_b128 a[112:115], v48 offset:12288\n" \
"ds_read_b128 a[116:119], v48 offset:13312\n" \
"buffer_load_dwordx4 a[52:55], v20, s[56:59], s98 offen offset:1024 sc1\n" \
"v_mfma_f32_16x16x32_bf16 v[168:171], a[168:171], v[104:107], v[168:171]\n" \
"ds_read_b128 a[120:123], v48 offset:14336\n" \
"ds_read_b128 a[124:127], v48 offset:15360\n" \
"v_mfma_f32_16x16x32_bf16 v[172:175], a[172:175], v[104:107], v[172:175]\n" \
"ds_read_b128 a[128:131], v49 offset:4096\n" \
"ds_read_b128 a[132:135], v49 offset:5120\n" \
"buffer_load_dwordx4 a[60:63], v56, s[56:59], s98 offen offset:1024 sc1\n" \
"v_mfma_f32_16x16x32_bf16 v[176:179], a[176:179], v[104:107], v[176:179]\n" \
"ds_read_b128 a[136:139], v49 offset:6144\n" \
"ds_read_b128 a[140:143], v49 offset:7168\n" \
"v_mfma_f32_16x16x32_bf16 v[180:183], a[180:183], v[104:107], v[180:183]\n" \
"ds_read_b128 a[144:147], v49 offset:12288\n" \
"ds_read_b128 a[148:151], v49 offset:13312\n" \
"s_waitcnt vmcnt(8)\n" \
"s_mov_b64 exec, 1\n" \
"buffer_store_dword v61, off, s[56:59], s95 offset:0 sc1\n" \
"s_mov_b64 exec, -1\n" \
"v_mfma_f32_16x16x32_bf16 v[184:187], a[184:187], v[104:107], v[184:187]\n" \
"ds_read_b128 a[152:155], v49 offset:14336\n" \
"ds_read_b128 a[156:159], v49 offset:15360\n" \
"v_mfma_f32_16x16x32_bf16 v[188:191], a[188:191], v[104:107], v[188:191]\n" \
"v_accvgpr_read_b32 v40, a200\n" \
"v_accvgpr_read_b32 v23, a201\n" \
"v_mfma_f32_16x16x32_bf16 a[196:199], a[252:255], v[104:107], a[196:199]\n" \
"v_add_f32_e32 v40, v40, v23\n" \
"s_waitcnt lgkmcnt(15)\n" \
"v_mfma_f32_16x16x32_bf16 v[80:83], a[0:3], a[64:67], v[32:35]\n" \
"buffer_load_dwordx4 a[220:223], v20, s[56:59], s60 offen offset:0 sc1\n" \
"v_mfma_f32_16x16x32_bf16 v[80:83], a[4:7], a[68:71], v[80:83]\n" \
"v_accvgpr_read_b32 v41, a204\n" \
"v_accvgpr_read_b32 v23, a205\n" \
"v_mfma_f32_16x16x32_bf16 v[80:83], a[8:11], a[72:75], v[80:83]\n" \
"v_add_f32_e32 v41, v41, v23\n" \
"v_add_u32_e32 v51, 0x10000, v50\n" \
"v_mfma_f32_16x16x32_bf16 v[80:83], a[12:15], a[76:79], v[80:83]\n" \
"buffer_load_dwordx4 a[236:239], v56, s[56:59], s60 offen offset:0 sc1\n" \
"v_mfma_f32_16x16x32_bf16 v[84:87], a[28:31], a[76:79], v[32:35]\n" \
"L_pf18:\n" \
"s_cbranch_execz L_retA\n" \
"s_nop 0\n" \
"v_add_f32_e32 v40, s94, v40\n" \
"v_add_f32_e32 v41, s94, v41\n" \
"v_mfma_f32_16x16x32_bf16 v[84:87], a[24:27], a[72:75], v[84:87]\n" \
"ds_write_b128 v50, v[192:195] offset:0\n" \
"buffer_load_dwordx4 a[224:227], v20, s[56:59], s60 offen offset:1024 sc1\n" \
"v_mfma_f32_16x16x32_bf16 v[84:87], a[20:23], a[68:71], v[84:87]\n" \
"ds_write_b128 v50, v[196:199] offset:1024\n" \
"v_mfma_f32_16x16x32_bf16 v[84:87], a[16:19], a[64:67], v[84:87]\n" \
"ds_write_b128 v50, v[200:203] offset:2048\n" \
"buffer_load_dwordx4 a[240:243], v56, s[56:59], s60 offen offset:1024 sc1\n" \
"v_mfma_f32_16x16x32_bf16 v[88:91], a[0:3], a[80:83], v[36:39]\n" \
"ds_write_b128 v50, v[204:207] offset:3072\n" \
"v_mfma_f32_16x16x32_bf16 v[88:91], a[4:7], a[84:87], v[88:91]\n" \
"ds_write_b128 v50, v[208:211] offset:8192\n" \
"buffer_load_dwordx4 a[228:231], v20, s[56:59], s60 offen offset:2048 sc1\n" \
"v_mfma_f32_16x16x32_bf16 v[88:91], a[8:11], a[88:91], v[88:91]\n" \
"ds_write_b128 v50, v[212:215] offset:9216\n" \
"v_mfma_f32_16x16x32_bf16 v[88:91], a[12:15], a[92:95], v[88:91]\n" \
"ds_write_b128 v50, v[216:219] offset:10240\n" \
"buffer_load_dwordx4 a[244:247], v56, s[56:59], s60 offen offset:2048 sc1\n" \
"v_mfma_f32_16x16x32_bf16 v[92:95], a[28:31], a[92:95], v[36:39]\n" \
"ds_write_b128 v50, v[220:223] offset:11264\n" \
"ds_write_b128 v51, v[224:227] offset:0\n" \
"ds_write_b128 v51, v[228:231] offset:1024\n" \
"buffer_load_dwordx4 a[232:235], v20, s[56:59], s60 offen offset:3072 sc1\n" \
"ds_write_b128 v51, v[232:235] offset:2048\n" \
"ds_write_b128 v51, v[236:239] offset:3072\n" \
"ds_write_b128 v51, v[240:243] offset:8192\n" \
"ds_write_b128 v51, v[244:247] offset:9216\n" \
"buffer_load_dwordx4 a[248:251], v56, s[56:59], s60 offen offset:3072 sc1\n" \
"ds_write_b128 v51, v[248:251] offset:10240\n" \
"ds_write_b128 v51, v[252:255] offset:11264\n" \
"ds_write_b64 v19, v[40:41]\n" \
"v_mfma_f32_16x16x32_bf16 v[92:95], a[24:27], a[88:91], v[92:95]\n" \
"v_xor_b32_e32 v52, 0x4000, v50\n" \
"v_xor_b32_e32 v53, 0x4000, v51\n" \
"v_mfma_f32_16x16x32_bf16 v[92:95], a[20:23], a[84:87], v[92:95]\n" \
"v_exp_f32_e32 v72, v72\n" \
"v_mfma_f32_16x16x32_bf16 v[92:95], a[16:19], a[80:83], v[92:95]\n" \
"v_exp_f32_e32 v73, v73\n" \
"v_exp_f32_e32 v74, v74\n" \
"v_exp_f32_e32 v75, v75\n" \
"v_exp_f32_e32 v76, v76\n" \
"v_exp_f32_e32 v77, v77\n" \
"v_exp_f32_e32 v78, v78\n" \
"v_exp_f32_e32 v79, v79\n" \
"v_cvt_pk_bf16_f32 v108, v72, v73\n" \
"v_cvt_pk_bf16_f32 v109, v74, v75\n" \
"v_cvt_pk_bf16_f32 v110, v76, v77\n" \
"v_cvt_pk_bf16_f32 v111, v78, v79\n" \
"v_and_b32_e32 v108, v12, v108\n" \
"v_and_b32_e32 v109, v13, v109\n" \
"v_and_b32_e32 v110, v14, v110\n" \
"v_and_b32_e32 v111, v15, v111\n" \
"v_readlane_b32 s93, v11, 18\n" \
"v_readlane_b32 s84, v11, 13\n" \
"v_readlane_b32 s85, v11, 14\n" \
"v_readlane_b32 s86, v11, 15\n" \
"s_mov_b32 s87, 0x20000\n" \
"v_readlane_b32 s92, v11, 16\n" \
"s_add_u32 m0, s93, 0x0\n" \
"v_xor_b32_e32 v63, 0x400, v19\n" \
"s_waitcnt lgkmcnt(0)\n" \
"s_barrier\n" \
"ds_read_b128 v[192:195], v52 offset:0\n" \
"ds_read_b128 v[196:199], v52 offset:1024\n" \
"ds_read_b128 v[200:203], v52 offset:2048\n" \
"ds_read_b128 v[204:207], v52 offset:3072\n" \
"ds_read_b128 v[208:211], v52 offset:8192\n" \
"ds_read_b128 v[212:215], v52 offset:9216\n" \
"ds_read_b128 v[216:219], v52 offset:10240\n" \
"ds_read_b128 v[220:223], v52 offset:11264\n" \
"ds_read_b128 v[224:227], v53 offset:0\n" \
"ds_read_b128 v[228:231], v53 offset:1024\n" \
"ds_read_b128 v[232:235], v53 offset:2048\n" \
"ds_read_b128 v[236:239], v53 offset:3072\n" \
"ds_read_b128 v[240:243], v53 offset:8192\n" \
"ds_read_b128 v[244:247], v53 offset:9216\n" \
"ds_read_b128 v[248:251], v53 offset:10240\n" \
"ds_read_b128 v[252:255], v53 offset:11264\n" \
"v_mfma_f32_16x16x32_bf16 v[160:163], a[128:131], v[108:111], v[160:163]\n" \
"buffer_load_dwordx4 v0, s[84:87], s92 offen offset:0 lds\n" \
"v_mfma_f32_16x16x32_bf16 v[164:167], a[132:135], v[108:111], v[164:167]\n" \
"v_exp_f32_e32 v80, v80\n" \
"v_mfma_f32_16x16x32_bf16 v[168:171], a[136:139], v[108:111], v[168:171]\n" \
"v_exp_f32_e32 v81, v81\n" \
"v_mfma_f32_16x16x32_bf16 v[172:175], a[140:143], v[108:111], v[172:175]\n" \
"buffer_load_dwordx4 v1, s[84:87], s92 offen offset:1024 lds\n" \
"v_mfma_f32_16x16x32_bf16 v[176:179], a[144:147], v[108:111], v[176:179]\n" \
"ds_read_b64 v[42:43], v63\n" \
"v_cvt_pk_bf16_f32 v116, v80, v81\n" \
"v_mfma_f32_16x16x32_bf16 v[180:183], a[148:151], v[108:111], v[180:183]\n" \
"v_exp_f32_e32 v82, v82\n" \
"v_mfma_f32_16x16x32_bf16 v[184:187], a[152:155], v[108:111], v[184:187]\n" \
"buffer_load_dwordx4 v2, s[84:87], s92 offen offset:2048 lds\n" \
"v_mfma_f32_16x16x32_bf16 v[188:191], a[156:159], v[108:111], v[188:191]\n" \
"v_exp_f32_e32 v83, v83\n" \
"v_mfma_f32_16x16x32_bf16 a[196:199], a[252:255], v[108:111], a[196:199]\n" \
"v_exp_f32_e32 v84, v84\n" \
"v_exp_f32_e32 v85, v85\n" \
"v_exp_f32_e32 v86, v86\n" \
"s_waitcnt lgkmcnt(1)\n" \
"s_barrier\n" \
"buffer_load_dwordx4 v3, s[84:87], s92 offen offset:3072 lds\n" \
"s_add_u32 m0, s93, 0x1000\n" \
"v_exp_f32_e32 v87, v87\n" \
"v_cvt_pk_bf16_f32 v117, v82, v83\n" \
"v_cvt_pk_bf16_f32 v118, v84, v85\n" \
"v_cvt_pk_bf16_f32 v119, v86, v87\n" \
"v_exp_f32_e32 v88, v88\n" \
"v_exp_f32_e32 v89, v89\n" \
"v_mfma_f32_16x16x32_bf16 v[128:131], a[96:99], v[116:119], v[128:131]\n" \
"buffer_load_dwordx4 v4, s[84:87], s92 offen offset:0 lds\n" \
"v_mfma_f32_16x16x32_bf16 v[132:135], a[100:103], v[116:119], v[132:135]\n" \
"v_exp_f32_e32 v90, v90\n" \
"v_mfma_f32_16x16x32_bf16 v[136:139], a[104:107], v[116:119], v[136:139]\n" \
"v_exp_f32_e32 v91, v91\n" \
"v_mfma_f32_16x16x32_bf16 v[140:143], a[108:111], v[116:119], v[140:143]\n" \
"buffer_load_dwordx4 v5, s[84:87], s92 offen offset:1024 lds\n" \
"v_mfma_f32_16x16x32_bf16 v[144:147], a[112:115], v[116:119], v[144:147]\n" \
"v_exp_f32_e32 v92, v92\n" \
"v_mfma_f32_16x16x32_bf16 v[148:151], a[116:119], v[116:119], v[148:151]\n" \
"v_exp_f32_e32 v93, v93\n" \
"v_mfma_f32_16x16x32_bf16 v[152:155], a[120:123], v[116:119], v[152:155]\n" \
"buffer_load_dwordx4 v6, s[84:87], s92 offen offset:2048 lds\n" \
"v_mfma_f32_16x16x32_bf16 v[156:159], a[124:127], v[116:119], v[156:159]\n" \
"v_exp_f32_e32 v94, v94\n" \
"v_mfma_f32_16x16x32_bf16 a[192:195], a[252:255], v[116:119], a[192:195]\n" \
"v_exp_f32_e32 v95, v95\n" \
"v_cvt_pk_bf16_f32 v124, v88, v89\n" \
"v_cvt_pk_bf16_f32 v125, v90, v91\n" \
"v_cvt_pk_bf16_f32 v126, v92, v93\n" \
"v_cvt_pk_bf16_f32 v127, v94, v95\n" \
"v_add_f32_e32 v160, v160, v224\n" \
"v_add_f32_e32 v161, v161, v225\n" \
"v_add_f32_e32 v162, v162, v226\n" \
"v_add_f32_e32 v163, v163, v227\n" \
"v_add_f32_e32 v164, v164, v228\n" \
"v_add_f32_e32 v165, v165, v229\n" \
"v_mfma_f32_16x16x32_bf16 v[160:163], a[96:99], v[124:127], v[160:163]\n" \
"buffer_load_dwordx4 v7, s[84:87], s92 offen offset:3072 lds\n" \
"s_add_u32 m0, s93, 0x2000\n" \
"v_add_f32_e32 v166, v166, v230\n" \
"v_add_f32_e32 v167, v167, v231\n" \
"v_add_f32_e32 v168, v168, v232\n" \
"v_add_f32_e32 v169, v169, v233\n" \
"v_mfma_f32_16x16x32_bf16 v[164:167], a[100:103], v[124:127], v[164:167]\n" \
"v_add_f32_e32 v170, v170, v234\n" \
"v_add_f32_e32 v171, v171, v235\n" \
"v_add_f32_e32 v172, v172, v236\n" \
"v_add_f32_e32 v173, v173, v237\n" \
"v_mfma_f32_16x16x32_bf16 v[168:171], a[104:107], v[124:127], v[168:171]\n" \
"buffer_load_dwordx4 v8, s[84:87], s92 offen offset:0 lds\n" \
"v_add_f32_e32 v174, v174, v238\n" \
"v_add_f32_e32 v175, v175, v239\n" \
"v_accvgpr_read_b32 v54, a192\n" \
"v_accvgpr_read_b32 v23, a193\n" \
"v_mfma_f32_16x16x32_bf16 v[172:175], a[108:111], v[124:127], v[172:175]\n" \
"v_add_f32_e32 v54, v54, v23\n" \
"s_waitcnt lgkmcnt(0)\n" \
"v_add_f32_e32 v54, v54, v42\n" \
"v_max_f32_e32 v62, v62, v54\n" \
"v_add_f32_e32 v176, v176, v240\n" \
"v_add_f32_e32 v177, v177, v241\n" \
"v_add_f32_e32 v178, v178, v242\n" \
"v_add_f32_e32 v179, v179, v243\n" \
"v_rcp_f32_e32 v54, v54\n" \
"v_add_f32_e32 v128, v128, v192\n" \
"v_mfma_f32_16x16x32_bf16 v[176:179], a[112:115], v[124:127], v[176:179]\n" \
"v_add_f32_e32 v129, v129, v193\n" \
"v_add_f32_e32 v130, v130, v194\n" \
"v_add_f32_e32 v131, v131, v195\n" \
"v_add_f32_e32 v132, v132, v196\n" \
"v_add_f32_e32 v133, v133, v197\n" \
"v_add_f32_e32 v134, v134, v198\n" \
"v_add_f32_e32 v135, v135, v199\n" \
"v_mul_f32_e32 v54, v54, v58\n" \
"v_mul_f32_e32 v128, v54, v128\n" \
"v_mul_f32_e32 v129, v54, v129\n" \
"v_mul_f32_e32 v130, v54, v130\n" \
"v_mul_f32_e32 v131, v54, v131\n" \
"v_mul_f32_e32 v132, v54, v132\n" \
"v_mul_f32_e32 v133, v54, v133\n" \
"v_mul_f32_e32 v134, v54, v134\n" \
"v_mul_f32_e32 v135, v54, v135\n" \
"v_add_f32_e32 v180, v180, v244\n" \
"v_add_f32_e32 v181, v181, v245\n" \
"v_add_f32_e32 v182, v182, v246\n" \
"v_add_f32_e32 v183, v183, v247\n" \
"v_add_f32_e32 v136, v136, v200\n" \
"v_add_f32_e32 v137, v137, v201\n" \
"v_add_f32_e32 v138, v138, v202\n" \
"v_add_f32_e32 v139, v139, v203\n" \
"v_add_f32_e32 v140, v140, v204\n" \
"v_add_f32_e32 v141, v141, v205\n" \
"v_add_f32_e32 v142, v142, v206\n" \
"v_add_f32_e32 v143, v143, v207\n" \
"v_cvt_pk_bf16_f32 v64, v128, v129\n" \
"v_cvt_pk_bf16_f32 v65, v130, v131\n" \
"v_cvt_pk_bf16_f32 v66, v132, v133\n" \
"v_cvt_pk_bf16_f32 v67, v134, v135\n" \
"buffer_store_dwordx4 v[64:67], v20, s[56:59], s68 offen offset:0 sc1\n" \
"v_mfma_f32_16x16x32_bf16 v[180:183], a[116:119], v[124:127], v[180:183]\n" \
"v_mul_f32_e32 v136, v54, v136\n" \
"v_mul_f32_e32 v137, v54, v137\n" \
"v_mfma_f32_16x16x32_bf16 a[208:211], a[216:219], v[64:67], a[208:211]\n" \
"v_mul_f32_e32 v138, v54, v138\n" \
"v_mul_f32_e32 v139, v54, v139\n" \
"v_mul_f32_e32 v140, v54, v140\n" \
"v_mul_f32_e32 v141, v54, v141\n" \
"v_mul_f32_e32 v142, v54, v142\n" \
"v_mul_f32_e32 v143, v54, v143\n" \
"v_add_f32_e32 v144, v144, v208\n" \
"v_add_f32_e32 v145, v145, v209\n" \
"v_add_f32_e32 v146, v146, v210\n" \
"v_add_f32_e32 v147, v147, v211\n" \
"v_add_f32_e32 v148, v148, v212\n" \
"v_add_f32_e32 v149, v149, v213\n" \
"v_add_f32_e32 v150, v150, v214\n" \
"v_add_f32_e32 v151, v151, v215\n" \
"v_cvt_pk_bf16_f32 v68, v136, v137\n" \
"v_cvt_pk_bf16_f32 v69, v138, v139\n" \
"v_cvt_pk_bf16_f32 v70, v140, v141\n" \
"v_cvt_pk_bf16_f32 v71, v142, v143\n" \
"buffer_store_dwordx4 v[68:71], v20, s[56:59], s68 offen offset:1024 sc1\n" \
"v_mul_f32_e32 v144, v54, v144\n" \
"v_mfma_f32_16x16x32_bf16 a[208:211], a[216:219], v[68:71], a[208:211]\n" \
"v_mul_f32_e32 v145, v54, v145\n" \
"v_mul_f32_e32 v146, v54, v146\n" \
"v_mul_f32_e32 v147, v54, v147\n" \
"v_mul_f32_e32 v148, v54, v148\n" \
"v_mul_f32_e32 v149, v54, v149\n" \
"v_mul_f32_e32 v150, v54, v150\n" \
"v_mul_f32_e32 v151, v54, v151\n" \
"v_add_f32_e32 v184, v184, v248\n" \
"v_add_f32_e32 v185, v185, v249\n" \
"v_add_f32_e32 v186, v186, v250\n" \
"v_add_f32_e32 v187, v187, v251\n" \
"v_add_f32_e32 v152, v152, v216\n" \
"v_add_f32_e32 v153, v153, v217\n" \
"v_add_f32_e32 v154, v154, v218\n" \
"v_add_f32_e32 v155, v155, v219\n" \
"v_add_f32_e32 v156, v156, v220\n" \
"v_add_f32_e32 v157, v157, v221\n" \
"v_add_f32_e32 v158, v158, v222\n" \
"v_add_f32_e32 v159, v159, v223\n" \
"v_cvt_pk_bf16_f32 v72, v144, v145\n" \
"v_cvt_pk_bf16_f32 v73, v146, v147\n" \
"v_cvt_pk_bf16_f32 v74, v148, v149\n" \
"v_cvt_pk_bf16_f32 v75, v150, v151\n" \
"buffer_store_dwordx4 v[72:75], v20, s[56:59], s68 offen offset:2048 sc1\n" \
"v_mfma_f32_16x16x32_bf16 v[184:187], a[120:123], v[124:127], v[184:187]\n" \
"v_mul_f32_e32 v152, v54, v152\n" \
"v_mul_f32_e32 v153, v54, v153\n" \
"v_mfma_f32_16x16x32_bf16 a[208:211], a[216:219], v[72:75], a[208:211]\n" \
"v_mul_f32_e32 v154, v54, v154\n" \
"v_mul_f32_e32 v155, v54, v155\n" \
"v_mul_f32_e32 v156, v54, v156\n" \
"v_mul_f32_e32 v157, v54, v157\n" \
"v_mul_f32_e32 v158, v54, v158\n" \
"v_mul_f32_e32 v159, v54, v159\n" \
"v_cvt_pk_bf16_f32 v76, v152, v153\n" \
"v_cvt_pk_bf16_f32 v77, v154, v155\n" \
"v_cvt_pk_bf16_f32 v78, v156, v157\n" \
"v_cvt_pk_bf16_f32 v79, v158, v159\n" \
"buffer_store_dwordx4 v[76:79], v20, s[56:59], s68 offen offset:3072 sc1\n" \
"v_add_f32_e32 v188, v188, v252\n" \
"v_mfma_f32_16x16x32_bf16 a[208:211], a[216:219], v[76:79], a[208:211]\n" \
"v_add_f32_e32 v189, v189, v253\n" \
"v_add_f32_e32 v190, v190, v254\n" \
"v_add_f32_e32 v191, v191, v255\n" \
"s_nop 1\n" \
"v_mfma_f32_16x16x32_bf16 v[188:191], a[124:127], v[124:127], v[188:191]\n" \
"v_mfma_f32_16x16x32_bf16 a[196:199], a[252:255], v[124:127], a[196:199]\n" \
"s_nop 7\n" \
"v_accvgpr_read_b32 v55, a196\n" \
"v_accvgpr_read_b32 v23, a197\n" \
"v_add_f32_e32 v55, v55, v23\n" \
"v_add_f32_e32 v55, v55, v43\n" \
"v_max_f32_e32 v62, v62, v55\n" \
"v_rcp_f32_e32 v55, v55\n" \
"v_cmp_lt_f32_e64 s[72:73], s100, v62\n" \
"v_mul_f32_e32 v55, v55, v59\n" \
"v_mul_f32_e32 v160, v55, v160\n" \
"v_mul_f32_e32 v161, v55, v161\n" \
"v_mul_f32_e32 v162, v55, v162\n" \
"v_mul_f32_e32 v163, v55, v163\n" \
"v_mul_f32_e32 v164, v55, v164\n" \
"v_mul_f32_e32 v165, v55, v165\n" \
"v_mul_f32_e32 v166, v55, v166\n" \
"v_mul_f32_e32 v167, v55, v167\n" \
"v_cvt_pk_bf16_f32 v80, v160, v161\n" \
"v_cvt_pk_bf16_f32 v81, v162, v163\n" \
"v_cvt_pk_bf16_f32 v82, v164, v165\n" \
"v_cvt_pk_bf16_f32 v83, v166, v167\n" \
"buffer_store_dwordx4 v[80:83], v20, s[56:59], s69 offen offset:0 sc1\n" \
"v_mul_f32_e32 v168, v55, v168\n" \
"v_mfma_f32_16x16x32_bf16 a[208:211], a[216:219], v[80:83], a[208:211]\n" \
"v_mul_f32_e32 v169, v55, v169\n" \
"v_mul_f32_e32 v170, v55, v170\n" \
"v_mul_f32_e32 v171, v55, v171\n" \
"v_mul_f32_e32 v172, v55, v172\n" \
"v_mul_f32_e32 v173, v55, v173\n" \
"v_mul_f32_e32 v174, v55, v174\n" \
"v_mul_f32_e32 v175, v55, v175\n" \
"v_cvt_pk_bf16_f32 v84, v168, v169\n" \
"v_cvt_pk_bf16_f32 v85, v170, v171\n" \
"v_cvt_pk_bf16_f32 v86, v172, v173\n" \
"v_cvt_pk_bf16_f32 v87, v174, v175\n" \
"buffer_store_dwordx4 v[84:87], v20, s[56:59], s69 offen offset:1024 sc1\n" \
"v_mul_f32_e32 v176, v55, v176\n" \
"v_mfma_f32_16x16x32_bf16 a[208:211], a[216:219], v[84:87], a[208:211]\n" \
"v_mul_f32_e32 v177, v55, v177\n" \
"v_mul_f32_e32 v178, v55, v178\n" \
"v_mul_f32_e32 v179, v55, v179\n" \
"v_mul_f32_e32 v180, v55, v180\n" \
"v_mul_f32_e32 v181, v55, v181\n" \
"v_mul_f32_e32 v182, v55, v182\n" \
"v_mul_f32_e32 v183, v55, v183\n" \
"v_cvt_pk_bf16_f32 v96, v176, v177\n" \
"v_cvt_pk_bf16_f32 v97, v178, v179\n" \
"v_cvt_pk_bf16_f32 v98, v180, v181\n" \
"v_cvt_pk_bf16_f32 v99, v182, v183\n" \
"buffer_store_dwordx4 v[96:99], v20, s[56:59], s69 offen offset:2048 sc1\n" \
"v_mul_f32_e32 v184, v55, v184\n" \
"v_mfma_f32_16x16x32_bf16 a[208:211], a[216:219], v[96:99], a[208:211]\n" \
"v_mul_f32_e32 v185, v55, v185\n" \
"v_mul_f32_e32 v186, v55, v186\n" \
"v_mul_f32_e32 v187, v55, v187\n" \
"v_mul_f32_e32 v188, v55, v188\n" \
"v_mul_f32_e32 v189, v55, v189\n" \
"v_mul_f32_e32 v190, v55, v190\n" \
"v_mul_f32_e32 v191, v55, v191\n" \
"v_cvt_pk_bf16_f32 v100, v184, v185\n" \
"v_cvt_pk_bf16_f32 v101, v186, v187\n" \
"v_cvt_pk_bf16_f32 v102, v188, v189\n" \
"v_cvt_pk_bf16_f32 v103, v190, v191\n" \
"buffer_store_dwordx4 v[100:103], v20, s[56:59], s69 offen offset:3072 sc1\n" \
"s_nop 0\n" \
"v_mfma_f32_16x16x32_bf16 a[208:211], a[216:219], v[100:103], a[208:211]\n" \
"s_nop 7\n" \
"v_accvgpr_read_b32 v63, a208\n" \
"v_cmp_u_f32_e32 vcc, v63, v63\n" \
"s_nop 0\n" \
"s_or_b64 vcc, s[72:73], vcc\n" \
"s_cbranch_scc1 L_end_%=\n" \
"L_out_%=:\n" \
"L_pf19:\n" \
"s_cbranch_execz L_retA\n" \
"s_nop 0\n" \
"v_add_u32_e32 v34, 0x20000, v10\n" \
"v_mov_b32_e32 v39, s62\n" \
"v_readlane_b32 s70, v11, 4\n" \
"v_bfe_u32 v38, v20, 2, 4\n" \
"v_readlane_b32 s64, v11, 1\n" \
"v_readlane_b32 s71, v11, 3\n" \
"v_add_u32_e32 v33, 0xfc00, v10\n" \
"v_readlane_b32 s99, v11, 19\n" \
"v_readlane_b32 s80, v11, 9\n" \
"v_readlane_b32 s81, v11, 10\n" \
"s_mov_b32 s82, 0x47fca0\n" \
"s_mov_b32 s83, 0x20000\n" \
"ds_read_b64_tr_b16 a[0:1], v34 offset:0\n" \
"ds_read_b64_tr_b16 a[2:3], v34 offset:4608\n" \
"ds_read_b64_tr_b16 a[4:5], v34 offset:9216\n" \
"ds_read_b64_tr_b16 a[6:7], v34 offset:13824\n" \
"ds_read_b64_tr_b16 a[8:9], v34 offset:18432\n" \
"ds_read_b64_tr_b16 a[10:11], v34 offset:23040\n" \
"ds_read_b64_tr_b16 a[12:13], v34 offset:32\n" \
"ds_read_b64_tr_b16 a[14:15], v34 offset:4640\n" \
"ds_read_b64_tr_b16 a[16:17], v34 offset:9248\n" \
"ds_read_b64_tr_b16 a[18:19], v34 offset:13856\n" \
"buffer_load_dword v37, v38, s[56:59], s64 offen sc1\n" \
"ds_read_b64_tr_b16 a[20:21], v34 offset:18464\n" \
"ds_read_b64_tr_b16 a[22:23], v34 offset:23072\n" \
"ds_read_b64_tr_b16 a[24:25], v34 offset:64\n" \
"ds_read_b64_tr_b16 a[26:27], v34 offset:4672\n" \
"ds_read_b64_tr_b16 a[28:29], v34 offset:9280\n" \
"ds_read_b64_tr_b16 a[30:31], v34 offset:13888\n" \
"ds_read_b64_tr_b16 a[192:193], v34 offset:18496\n" \
"ds_read_b64_tr_b16 a[194:195], v34 offset:23104\n" \
"ds_read_b64_tr_b16 a[196:197], v34 offset:96\n" \
"ds_read_b64_tr_b16 a[198:199], v34 offset:4704\n" \
"ds_read_b64_tr_b16 a[200:201], v34 offset:9312\n" \
"ds_read_b64_tr_b16 a[202:203], v34 offset:13920\n" \
"ds_read_b64_tr_b16 a[204:205], v34 offset:18528\n" \
"ds_read_b64_tr_b16 a[206:207], v34 offset:23136\n" \
"ds_read_b64_tr_b16 a[208:209], v34 offset:128\n" \
"ds_read_b64_tr_b16 a[210:211], v34 offset:4736\n" \
"ds_read_b64_tr_b16 a[212:213], v34 offset:9344\n" \
"ds_read_b64_tr_b16 a[214:215], v34 offset:13952\n" \
"ds_read_b64_tr_b16 a[216:217], v34 offset:18560\n" \
"ds_read_b64_tr_b16 a[218:219], v34 offset:23168\n" \
"v_readlane_b32 s65, v11, 23\n" \
"s_add_u32 m0, s99, 0x6c00\n" \
"v_readlane_b32 s66, v11, 26\n" \
"v_add_u32_e32 v32, 0x49000, v20\n" \
"v_lshrrev_b32_e32 v44, 9, v20\n" \
"v_mul_u32_u24_e32 v44, 0xb40, v44\n" \
"v_add_u32_e32 v35, v44, v10\n" \
"v_readlane_b32 s67, v11, 24\n" \
"v_readlane_b32 s92, v11, 20\n" \
"v_readlane_b32 s73, v11, 25\n" \
"s_waitcnt vmcnt(18) lgkmcnt(15)\n" \
"v_mfma_f32_16x16x32_bf16 v[112:115], a[0:3], a[220:223], 0\n" \
"buffer_load_dwordx4 v0, s[80:83], s65 offen offset:0 lds\n" \
"v_mfma_f32_16x16x32_bf16 v[112:115], a[4:7], a[224:227], v[112:115]\n" \
"v_readlane_b32 s94, v11, 21\n" \
"v_readlane_b32 s84, v11, 7\n" \
"v_mfma_f32_16x16x32_bf16 v[112:115], a[8:11], a[228:231], v[112:115]\n" \
"v_readlane_b32 s85, v11, 8\n" \
"s_mov_b32 s86, 0xa1ca0\n" \
"v_mfma_f32_16x16x32_bf16 v[116:119], a[8:11], a[244:247], 0\n" \
"s_mov_b32 s87, 0x20000\n" \
"v_readlane_b32 s68, v11, 27\n" \
"v_mfma_f32_16x16x32_bf16 v[116:119], a[4:7], a[240:243], v[116:119]\n" \
"v_readlane_b32 s69, v11, 28\n" \
"v_readlane_b32 s72, v11, 30\n" \
"v_mfma_f32_16x16x32_bf16 v[116:119], a[0:3], a[236:239], v[116:119]\n" \
"buffer_load_dwordx4 v1, s[80:83], s65 offen offset:1024 lds\n" \
"v_mfma_f32_16x16x32_bf16 v[184:187], a[0:3], v[64:67], 0\n" \
"v_readlane_b32 s93, v11, 31\n" \
"v_lshlrev_b32_e32 v41, 12, v20\n" \
"v_mfma_f32_16x16x32_bf16 v[184:187], a[4:7], v[68:71], v[184:187]\n" \
"v_readlane_b32 s63, v11, 37\n" \
"v_bfe_u32 v40, v20, 2, 7\n" \
"v_mfma_f32_16x16x32_bf16 v[184:187], a[8:11], v[72:75], v[184:187]\n" \
"s_add_u32 s74, s63, 0x3800\n" \
"v_bfe_u32 v44, v20, 4, 4\n" \
"v_mfma_f32_16x16x32_bf16 v[188:191], a[8:11], v[96:99], 0\n" \
"ds_read_b64_tr_b16 a[8:9], v34 offset:18592\n" \
"ds_read_b64_tr_b16 a[10:11], v34 offset:23200\n" \
"v_mfma_f32_16x16x32_bf16 v[188:191], a[4:7], v[84:87], v[188:191]\n" \
"ds_read_b64_tr_b16 a[4:5], v34 offset:9376\n" \
"ds_read_b64_tr_b16 a[6:7], v34 offset:13984\n" \
"v_mfma_f32_16x16x32_bf16 v[188:191], a[0:3], v[80:83], v[188:191]\n" \
"ds_read_b64_tr_b16 a[0:1], v34 offset:160\n" \
"ds_read_b64_tr_b16 a[2:3], v34 offset:4768\n" \
"v_mfma_f32_16x16x32_bf16 v[120:123], a[12:15], a[220:223], 0\n" \
"buffer_load_dwordx4 v2, s[80:83], s65 offen offset:2048 lds\n" \
"v_mfma_f32_16x16x32_bf16 v[120:123], a[16:19], a[224:227], v[120:123]\n" \
"v_bfe_u32 v45, v20, 8, 2\n" \
"v_mul_u32_u24_e32 v42, 0x480, v44\n" \
"v_mfma_f32_16x16x32_bf16 v[120:123], a[20:23], a[228:231], v[120:123]\n" \
"v_lshl_add_u32 v42, v45, 3, v42\n" \
"v_and_b32_e32 v43, 1, v45\n" \
"v_mfma_f32_16x16x32_bf16 v[124:127], a[20:23], a[244:247], 0\n" \
"v_lshrrev_b32_e32 v44, 1, v44\n" \
"v_lshlrev_b32_e32 v45, 1, v45\n" \
"v_mfma_f32_16x16x32_bf16 v[124:127], a[16:19], a[240:243], v[124:127]\n" \
"v_sub_u32_e32 v44, v44, v45\n" \
"v_and_b32_e32 v45, 16, v20\n" \
"v_mfma_f32_16x16x32_bf16 v[124:127], a[12:15], a[236:239], v[124:127]\n" \
"buffer_load_dwordx4 v3, s[80:83], s65 offen offset:3072 lds\n" \
"v_mfma_f32_16x16x32_bf16 v[192:195], a[12:15], v[64:67], 0\n" \
"s_add_u32 m0, s99, 0x7c00\n" \
"v_mov_b32_e32 v46, 0x3f80\n" \
"v_mfma_f32_16x16x32_bf16 v[192:195], a[16:19], v[68:71], v[192:195]\n" \
"v_lshlrev_b32_e32 v45, v45, v46\n" \
"v_mov_b32_e32 v46, s62\n" \
"v_mfma_f32_16x16x32_bf16 v[192:195], a[20:23], v[72:75], v[192:195]\n" \
"v_readlane_b32 s95, v11, 34\n" \
"v_readlane_b32 s96, v11, 35\n" \
"v_mfma_f32_16x16x32_bf16 v[196:199], a[20:23], v[96:99], 0\n" \
"ds_read_b64_tr_b16 a[20:21], v34 offset:18624\n" \
"ds_read_b64_tr_b16 a[22:23], v34 offset:23232\n" \
"v_mfma_f32_16x16x32_bf16 v[196:199], a[16:19], v[84:87], v[196:199]\n" \
"ds_read_b64_tr_b16 a[16:17], v34 offset:9408\n" \
"ds_read_b64_tr_b16 a[18:19], v34 offset:14016\n" \
"v_mfma_f32_16x16x32_bf16 v[196:199], a[12:15], v[80:83], v[196:199]\n" \
"ds_read_b64_tr_b16 a[12:13], v34 offset:192\n" \
"ds_read_b64_tr_b16 a[14:15], v34 offset:4800\n" \
"v_mfma_f32_16x16x32_bf16 v[128:131], a[24:27], a[220:223], 0\n" \
"v_readlane_b32 s97, v11, 36\n" \
"s_waitcnt lgkmcnt(8)\n" \
"v_mfma_f32_16x16x32_bf16 v[128:131], a[28:31], a[224:227], v[128:131]\n" \
"buffer_load_dwordx4 v4, s[80:83], s65 offen offset:0 lds\n" \
"v_mfma_f32_16x16x32_bf16 v[128:131], a[192:195], a[228:231], v[128:131]\n" \
"v_readlane_b32 s100, v11, 6\n" \
"v_mov_b32_e32 v52, 0\n" \
"v_mfma_f32_16x16x32_bf16 v[132:135], a[192:195], a[244:247], 0\n" \
"v_mov_b32_e32 v53, 0\n" \
"v_mov_b32_e32 v48, 0\n" \
"v_mfma_f32_16x16x32_bf16 v[132:135], a[28:31], a[240:243], v[132:135]\n" \
"v_mov_b32_e32 v49, 0\n" \
"v_mul_u32_u24_e32 v43, 0x8ff8, v43\n" \
"v_mfma_f32_16x16x32_bf16 v[132:135], a[24:27], a[236:239], v[132:135]\n" \
"v_add_u32_e32 v42, v42, v43\n" \
"v_readlane_b32 s88, v11, 11\n" \
"v_mfma_f32_16x16x32_bf16 v[200:203], a[24:27], v[64:67], 0\n" \
"buffer_load_dwordx4 v5, s[80:83], s65 offen offset:1024 lds\n" \
"v_mfma_f32_16x16x32_bf16 v[200:203], a[28:31], v[68:71], v[200:203]\n" \
"v_readlane_b32 s89, v11, 12\n" \
"s_mov_b32 s90, 0x240000\n" \
"v_mfma_f32_16x16x32_bf16 v[200:203], a[192:195], v[72:75], v[200:203]\n" \
"s_mov_b32 s91, 0x20000\n" \
"v_mfma_f32_16x16x32_bf16 v[204:207], a[192:195], v[96:99], 0\n" \
"ds_read_b64_tr_b16 a[192:193], v34 offset:18656\n" \
"ds_read_b64_tr_b16 a[194:195], v34 offset:23264\n" \
"v_mfma_f32_16x16x32_bf16 v[204:207], a[28:31], v[84:87], v[204:207]\n" \
"ds_read_b64_tr_b16 a[28:29], v34 offset:9440\n" \
"ds_read_b64_tr_b16 a[30:31], v34 offset:14048\n" \
"v_mfma_f32_16x16x32_bf16 v[204:207], a[24:27], v[80:83], v[204:207]\n" \
"ds_read_b64_tr_b16 a[24:25], v34 offset:224\n" \
"ds_read_b64_tr_b16 a[26:27], v34 offset:4832\n" \
"v_mfma_f32_16x16x32_bf16 v[136:139], a[196:199], a[220:223], 0\n" \
"buffer_load_dwordx4 v6, s[80:83], s65 offen offset:2048 lds\n" \
"v_mfma_f32_16x16x32_bf16 v[136:139], a[200:203], a[224:227], v[136:139]\n" \
"v_mfma_f32_16x16x32_bf16 v[136:139], a[204:207], a[228:231], v[136:139]\n" \
"v_mfma_f32_16x16x32_bf16 v[140:143], a[204:207], a[244:247], 0\n" \
"v_mfma_f32_16x16x32_bf16 v[140:143], a[200:203], a[240:243], v[140:143]\n" \
"v_mfma_f32_16x16x32_bf16 v[140:143], a[196:199], a[236:239], v[140:143]\n" \
"buffer_load_dwordx4 v7, s[80:83], s65 offen offset:3072 lds\n" \
"v_mfma_f32_16x16x32_bf16 v[208:211], a[196:199], v[64:67], 0\n" \
"s_add_u32 m0, s99, 0x8c00\n" \
"v_mfma_f32_16x16x32_bf16 v[208:211], a[200:203], v[68:71], v[208:211]\n" \
"v_mfma_f32_16x16x32_bf16 v[208:211], a[204:207], v[72:75], v[208:211]\n" \
"v_mfma_f32_16x16x32_bf16 v[212:215], a[204:207], v[96:99], 0\n" \
"ds_read_b64_tr_b16 a[204:205], v34 offset:18688\n" \
"ds_read_b64_tr_b16 a[206:207], v34 offset:23296\n" \
"v_mfma_f32_16x16x32_bf16 v[212:215], a[200:203], v[84:87], v[212:215]\n" \
"ds_read_b64_tr_b16 a[200:201], v34 offset:9472\n" \
"ds_read_b64_tr_b16 a[202:203], v34 offset:14080\n" \
"v_mfma_f32_16x16x32_bf16 v[212:215], a[196:199], v[80:83], v[212:215]\n" \
"ds_read_b64_tr_b16 a[196:197], v34 offset:256\n" \
"ds_read_b64_tr_b16 a[198:199], v34 offset:4864\n" \
"v_mfma_f32_16x16x32_bf16 v[144:147], a[208:211], a[220:223], 0\n" \
"buffer_load_dwordx4 v8, s[80:83], s65 offen offset:0 lds\n" \
"v_mfma_f32_16x16x32_bf16 v[144:147], a[212:215], a[224:227], v[144:147]\n" \
"s_waitcnt vmcnt(9)\n" \
"v_cmp_ne_u32_e32 vcc, s62, v37\n" \
"v_mfma_f32_16x16x32_bf16 v[144:147], a[216:219], a[228:231], v[144:147]\n" \
"s_add_u32 m0, s99, 0x14400\n" \
"v_readlane_b32 s65, v11, 42\n" \
"v_mfma_f32_16x16x32_bf16 v[148:151], a[216:219], a[244:247], 0\n" \
"v_mfma_f32_16x16x32_bf16 v[148:151], a[212:215], a[240:243], v[148:151]\n" \
"s_cbranch_vccnz L_sa_%=\n" \
"L_ra_%=:\n" \
"v_readlane_b32 s64, v11, 29\n" \
"v_mfma_f32_16x16x32_bf16 v[148:151], a[208:211], a[236:239], v[148:151]\n" \
"buffer_load_dwordx4 a[64:67], v20, s[56:59], s66 offen offset:0 sc1\n" \
"v_mfma_f32_16x16x32_bf16 v[216:219], a[208:211], v[64:67], 0\n" \
"v_mfma_f32_16x16x32_bf16 v[216:219], a[212:215], v[68:71], v[216:219]\n" \
"v_mfma_f32_16x16x32_bf16 v[216:219], a[216:219], v[72:75], v[216:219]\n" \
"v_mfma_f32_16x16x32_bf16 v[220:223], a[216:219], v[96:99], 0\n" \
"v_mfma_f32_16x16x32_bf16 v[220:223], a[212:215], v[84:87], v[220:223]\n" \
"buffer_load_dwordx4 a[68:71], v20, s[56:59], s66 offen offset:1024 sc1\n" \
"v_mfma_f32_16x16x32_bf16 v[220:223], a[208:211], v[80:83], v[220:223]\n" \
"s_waitcnt lgkmcnt(5)\n" \
"v_mfma_f32_16x16x32_bf16 v[152:155], a[0:3], a[220:223], 0\n" \
"v_mfma_f32_16x16x32_bf16 v[152:155], a[4:7], a[224:227], v[152:155]\n" \
"v_mfma_f32_16x16x32_bf16 v[152:155], a[8:11], a[228:231], v[152:155]\n" \
"v_mfma_f32_16x16x32_bf16 v[156:159], a[8:11], a[244:247], 0\n" \
"buffer_load_dwordx4 a[84:87], v32, s[56:59], s66 offen offset:1024 sc1\n" \
"v_mfma_f32_16x16x32_bf16 v[156:159], a[4:7], a[240:243], v[156:159]\n" \
"v_mfma_f32_16x16x32_bf16 v[156:159], a[0:3], a[236:239], v[156:159]\n" \
"v_mfma_f32_16x16x32_bf16 v[224:227], a[0:3], v[64:67], 0\n" \
"v_mfma_f32_16x16x32_bf16 v[224:227], a[4:7], v[68:71], v[224:227]\n" \
"v_mfma_f32_16x16x32_bf16 v[224:227], a[8:11], v[72:75], v[224:227]\n" \
"buffer_load_dwordx4 a[80:83], v32, s[56:59], s66 offen offset:0 sc1\n" \
"v_mfma_f32_16x16x32_bf16 v[228:231], a[8:11], v[96:99], 0\n" \
"v_mfma_f32_16x16x32_bf16 v[228:231], a[4:7], v[84:87], v[228:231]\n" \
"v_mfma_f32_16x16x32_bf16 v[228:231], a[0:3], v[80:83], v[228:231]\n" \
"v_mfma_f32_16x16x32_bf16 v[160:163], a[12:15], a[220:223], 0\n" \
"v_mfma_f32_16x16x32_bf16 v[160:163], a[16:19], a[224:227], v[160:163]\n" \
"buffer_load_dwordx4 a[72:75], v20, s[56:59], s66 offen offset:2048 sc1\n" \
"v_mfma_f32_16x16x32_bf16 v[160:163], a[20:23], a[228:231], v[160:163]\n" \
"v_mfma_f32_16x16x32_bf16 v[164:167], a[20:23], a[244:247], 0\n" \
"v_mfma_f32_16x16x32_bf16 v[164:167], a[16:19], a[240:243], v[164:167]\n" \
"v_mfma_f32_16x16x32_bf16 v[164:167], a[12:15], a[236:239], v[164:167]\n" \
"v_mfma_f32_16x16x32_bf16 v[232:235], a[12:15], v[64:67], 0\n" \
"buffer_load_dwordx4 a[76:79], v20, s[56:59], s66 offen offset:3072 sc1\n" \
"v_mfma_f32_16x16x32_bf16 v[232:235], a[16:19], v[68:71], v[232:235]\n" \
"v_mfma_f32_16x16x32_bf16 v[232:235], a[20:23], v[72:75], v[232:235]\n" \
"v_mfma_f32_16x16x32_bf16 v[236:239], a[20:23], v[96:99], 0\n" \
"v_mfma_f32_16x16x32_bf16 v[236:239], a[16:19], v[84:87], v[236:239]\n" \
"v_mfma_f32_16x16x32_bf16 v[236:239], a[12:15], v[80:83], v[236:239]\n" \
"buffer_load_dwordx4 a[92:95], v32, s[56:59], s66 offen offset:3072 sc1\n" \
"s_waitcnt lgkmcnt(0)\n" \
"s_barrier\n" \
"v_mfma_f32_16x16x32_bf16 v[168:171], a[24:27], a[220:223], 0\n" \
"ds_read_b64_tr_b16 a[208:209], v33 offset:9216\n" \
"ds_read_b64_tr_b16 a[210:211], v33 offset:13824\n" \
"v_mfma_f32_16x16x32_bf16 v[168:171], a[28:31], a[224:227], v[168:171]\n" \
"ds_read_b64_tr_b16 a[212:213], v33 offset:9248\n" \
"ds_read_b64_tr_b16 a[214:215], v33 offset:13856\n" \
"v_mfma_f32_16x16x32_bf16 v[168:171], a[192:195], a[228:231], v[168:171]\n" \
"ds_read_b64_tr_b16 a[216:217], v33 offset:9280\n" \
"ds_read_b64_tr_b16 a[218:219], v33 offset:13888\n" \
"s_mov_b64 exec, 1\n" \
"buffer_store_dword v39, off, s[56:59], s70 offset:0 sc1\n" \
"s_mov_b64 exec, -1\n" \
"v_mfma_f32_16x16x32_bf16 v[172:175], a[192:195], a[244:247], 0\n" \
"ds_read_b64_tr_b16 a[0:1], v33 offset:9312\n" \
"ds_read_b64_tr_b16 a[2:3], v33 offset:13920\n" \
"v_mfma_f32_16x16x32_bf16 v[172:175], a[28:31], a[240:243], v[172:175]\n" \
"ds_read_b64_tr_b16 a[4:5], v33 offset:9344\n" \
"ds_read_b64_tr_b16 a[6:7], v33 offset:13952\n" \
"v_mfma_f32_16x16x32_bf16 v[172:175], a[24:27], a[236:239], v[172:175]\n" \
"ds_read_b64_tr_b16 a[8:9], v33 offset:9376\n" \
"ds_read_b64_tr_b16 a[10:11], v33 offset:13984\n" \
"v_mfma_f32_16x16x32_bf16 v[240:243], a[24:27], v[64:67], 0\n" \
"ds_read_b64_tr_b16 a[12:13], v33 offset:9408\n" \
"ds_read_b64_tr_b16 a[14:15], v33 offset:14016\n" \
"v_mfma_f32_16x16x32_bf16 v[240:243], a[28:31], v[68:71], v[240:243]\n" \
"ds_read_b64_tr_b16 a[16:17], v33 offset:9440\n" \
"ds_read_b64_tr_b16 a[18:19], v33 offset:14048\n" \
"v_mfma_f32_16x16x32_bf16 v[240:243], a[192:195], v[72:75], v[240:243]\n" \
"ds_read_b64_tr_b16 a[20:21], v33 offset:9472\n" \
"ds_read_b64_tr_b16 a[22:23], v33 offset:14080\n" \
"v_mfma_f32_16x16x32_bf16 v[244:247], a[192:195], v[96:99], 0\n" \
"ds_read_b64_tr_b16 a[192:193], v35 offset:32\n" \
"ds_read_b64_tr_b16 a[194:195], v35 offset:2336\n" \
"v_mfma_f32_16x16x32_bf16 v[244:247], a[28:31], v[84:87], v[244:247]\n" \
"ds_read_b64_tr_b16 a[28:29], v35 offset:10368\n" \
"ds_read_b64_tr_b16 a[30:31], v35 offset:12672\n" \
"v_mfma_f32_16x16x32_bf16 v[244:247], a[24:27], v[80:83], v[244:247]\n" \
"ds_read_b64_tr_b16 a[24:25], v35 offset:0\n" \
"ds_read_b64_tr_b16 a[26:27], v35 offset:2304\n" \
"L_pf20:\n" \
"s_cbranch_execz L_pf22\n" \
"s_nop 0\n" \
"v_mfma_f32_16x16x32_bf16 v[176:179], a[196:199], a[220:223], 0\n" \
"buffer_load_dwordx4 a[88:91], v32, s[56:59], s66 offen offset:2048 sc1\n" \
"v_mfma_f32_16x16x32_bf16 v[176:179], a[200:203], a[224:227], v[176:179]\n" \
"v_readlane_b32 s66, v11, 43\n" \
"v_readlane_b32 s70, v11, 45\n" \
"v_mfma_f32_16x16x32_bf16 v[176:179], a[204:207], a[228:231], v[176:179]\n" \
"v_mfma_f32_16x16x32_bf16 v[180:183], a[204:207], a[244:247], 0\n" \
"v_mfma_f32_16x16x32_bf16 v[180:183], a[200:203], a[240:243], v[180:183]\n" \
"v_mfma_f32_16x16x32_bf16 v[180:183], a[196:199], a[236:239], v[180:183]\n" \
"buffer_load_dwordx4 v0, s[80:83], s67 offen offset:0 lds\n" \
"v_mfma_f32_16x16x32_bf16 v[248:251], a[196:199], v[64:67], 0\n" \
"v_mfma_f32_16x16x32_bf16 v[248:251], a[200:203], v[68:71], v[248:251]\n" \
"v_mfma_f32_16x16x32_bf16 v[248:251], a[204:207], v[72:75], v[248:251]\n" \
"v_mfma_f32_16x16x32_bf16 v[252:255], a[204:207], v[96:99], 0\n" \
"ds_read_b64_tr_b16 a[204:205], v35 offset:10432\n" \
"ds_read_b64_tr_b16 a[206:207], v35 offset:12736\n" \
"v_mfma_f32_16x16x32_bf16 v[252:255], a[200:203], v[84:87], v[252:255]\n" \
"ds_read_b64_tr_b16 a[200:201], v35 offset:64\n" \
"ds_read_b64_tr_b16 a[202:203], v35 offset:2368\n" \
"v_mfma_f32_16x16x32_bf16 v[252:255], a[196:199], v[80:83], v[252:255]\n" \
"ds_read_b64_tr_b16 a[196:197], v35 offset:10400\n" \
"s_waitcnt lgkmcnt(9)\n" \
"v_mfma_f32_16x16x32_bf16 v[112:115], a[208:211], a[232:235], v[112:115]\n" \
"buffer_load_dwordx4 v1, s[80:83], s67 offen offset:1024 lds\n" \
"v_mfma_f32_16x16x32_bf16 v[116:119], a[208:211], a[248:251], v[116:119]\n" \
"ds_read_b64_tr_b16 a[198:199], v35 offset:12704\n" \
"v_mfma_f32_16x16x32_bf16 v[184:187], a[208:211], v[76:79], v[184:187]\n" \
"v_mfma_f32_16x16x32_bf16 v[188:191], a[208:211], v[100:103], v[188:191]\n" \
"ds_read_b64_tr_b16 a[208:209], v35 offset:96\n" \
"ds_read_b64_tr_b16 a[210:211], v35 offset:2400\n" \
"v_mfma_f32_16x16x32_bf16 v[120:123], a[212:215], a[232:235], v[120:123]\n" \
"v_mfma_f32_16x16x32_bf16 v[124:127], a[212:215], a[248:251], v[124:127]\n" \
"buffer_load_dwordx4 v2, s[80:83], s67 offen offset:2048 lds\n" \
"v_mfma_f32_16x16x32_bf16 v[192:195], a[212:215], v[76:79], v[192:195]\n" \
"v_mfma_f32_16x16x32_bf16 v[196:199], a[212:215], v[100:103], v[196:199]\n" \
"ds_read_b64_tr_b16 a[212:213], v35 offset:10464\n" \
"ds_read_b64_tr_b16 a[214:215], v35 offset:12768\n" \
"v_mfma_f32_16x16x32_bf16 v[128:131], a[216:219], a[232:235], v[128:131]\n" \
"v_mfma_f32_16x16x32_bf16 v[132:135], a[216:219], a[248:251], v[132:135]\n" \
"v_mfma_f32_16x16x32_bf16 v[200:203], a[216:219], v[76:79], v[200:203]\n" \
"buffer_load_dwordx4 v3, s[80:83], s67 offen offset:3072 lds\n" \
"v_mfma_f32_16x16x32_bf16 v[204:207], a[216:219], v[100:103], v[204:207]\n" \
"ds_read_b64_tr_b16 a[216:217], v35 offset:128\n" \
"ds_read_b64_tr_b16 a[218:219], v35 offset:2432\n" \
"v_mfma_f32_16x16x32_bf16 v[136:139], a[0:3], a[232:235], v[136:139]\n" \
"s_add_u32 m0, s99, 0x15400\n" \
"v_mfma_f32_16x16x32_bf16 v[140:143], a[0:3], a[248:251], v[140:143]\n" \
"v_mfma_f32_16x16x32_bf16 v[208:211], a[0:3], v[76:79], v[208:211]\n" \
"v_mfma_f32_16x16x32_bf16 v[212:215], a[0:3], v[100:103], v[212:215]\n" \
"ds_read_b64_tr_b16 a[0:1], v35 offset:10496\n" \
"ds_read_b64_tr_b16 a[2:3], v35 offset:12800\n" \
"v_mfma_f32_16x16x32_bf16 v[144:147], a[4:7], a[232:235], v[144:147]\n" \
"buffer_load_dwordx4 v4, s[80:83], s67 offen offset:0 lds\n" \
"v_mfma_f32_16x16x32_bf16 v[148:151], a[4:7], a[248:251], v[148:151]\n" \
"v_mfma_f32_16x16x32_bf16 v[216:219], a[4:7], v[76:79], v[216:219]\n" \
"v_mfma_f32_16x16x32_bf16 v[220:223], a[4:7], v[100:103], v[220:223]\n" \
"ds_read_b64_tr_b16 a[4:5], v35 offset:160\n" \
"ds_read_b64_tr_b16 a[6:7], v35 offset:2464\n" \
"v_mfma_f32_16x16x32_bf16 v[152:155], a[8:11], a[232:235], v[152:155]\n" \
"v_mfma_f32_16x16x32_bf16 v[156:159], a[8:11], a[248:251], v[156:159]\n" \
"buffer_load_dwordx4 v5, s[80:83], s67 offen offset:1024 lds\n" \
"v_mfma_f32_16x16x32_bf16 v[224:227], a[8:11], v[76:79], v[224:227]\n" \
"v_mfma_f32_16x16x32_bf16 v[228:231], a[8:11], v[100:103], v[228:231]\n" \
"ds_read_b64_tr_b16 a[8:9], v35 offset:10528\n" \
"ds_read_b64_tr_b16 a[10:11], v35 offset:12832\n" \
"v_mfma_f32_16x16x32_bf16 v[160:163], a[12:15], a[232:235], v[160:163]\n" \
"v_mfma_f32_16x16x32_bf16 v[164:167], a[12:15], a[248:251], v[164:167]\n" \
"v_mfma_f32_16x16x32_bf16 v[232:235], a[12:15], v[76:79], v[232:235]\n" \
"buffer_load_dwordx4 v6, s[80:83], s67 offen offset:2048 lds\n" \
"v_mfma_f32_16x16x32_bf16 v[236:239], a[12:15], v[100:103], v[236:239]\n" \
"ds_read_b64_tr_b16 a[12:13], v35 offset:192\n" \
"ds_read_b64_tr_b16 a[14:15], v35 offset:2496\n" \
"v_mfma_f32_16x16x32_bf16 v[168:171], a[16:19], a[232:235], v[168:171]\n" \
"v_mfma_f32_16x16x32_bf16 v[172:175], a[16:19], a[248:251], v[172:175]\n" \
"v_mfma_f32_16x16x32_bf16 v[240:243], a[16:19], v[76:79], v[240:243]\n" \
"v_mfma_f32_16x16x32_bf16 v[244:247], a[16:19], v[100:103], v[244:247]\n" \
"buffer_load_dwordx4 v7, s[80:83], s67 offen offset:3072 lds\n" \
"v_mfma_f32_16x16x32_bf16 v[176:179], a[20:23], a[232:235], v[176:179]\n" \
"s_add_u32 m0, s99, 0x16400\n" \
"ds_read_b64_tr_b16 a[16:17], v35 offset:10560\n" \
"v_mfma_f32_16x16x32_bf16 v[180:183], a[20:23], a[248:251], v[180:183]\n" \
"ds_read_b64_tr_b16 a[18:19], v35 offset:12864\n" \
"v_mfma_f32_16x16x32_bf16 v[248:251], a[20:23], v[76:79], v[248:251]\n" \
"v_mfma_f32_16x16x32_bf16 v[252:255], a[20:23], v[100:103], v[252:255]\n" \
"ds_read_b64_tr_b16 a[20:21], v35 offset:224\n" \
"s_waitcnt lgkmcnt(7)\n" \
"v_mfma_f32_16x16x32_bf16 v[112:115], a[24:27], a[32:35], v[112:115]\n" \
"buffer_load_dwordx4 v8, s[80:83], s67 offen offset:0 lds\n" \
"v_mfma_f32_16x16x32_bf16 v[112:115], a[28:31], a[36:39], v[112:115]\n" \
"s_add_u32 m0, s92, 0x0\n" \
"ds_read_b64_tr_b16 a[22:23], v35 offset:2528\n" \
"v_mfma_f32_16x16x32_bf16 v[116:119], a[28:31], a[44:47], v[116:119]\n" \
"v_readlane_b32 s67, v11, 44\n" \
"v_mfma_f32_16x16x32_bf16 v[116:119], a[24:27], a[40:43], v[116:119]\n" \
"v_mfma_f32_16x16x32_bf16 v[184:187], a[24:27], a[48:51], v[184:187]\n" \
"v_mfma_f32_16x16x32_bf16 v[184:187], a[28:31], a[52:55], v[184:187]\n" \
"buffer_load_dword v47, v38, s[56:59], s71 offen sc1\n" \
"v_mfma_f32_16x16x32_bf16 v[188:191], a[28:31], a[60:63], v[188:191]\n" \
"ds_read_b64_tr_b16 a[28:29], v35 offset:256\n" \
"ds_read_b64_tr_b16 a[30:31], v35 offset:2560\n" \
"v_mfma_f32_16x16x32_bf16 v[188:191], a[24:27], a[56:59], v[188:191]\n" \
"ds_read_b64_tr_b16 a[24:25], v35 offset:10592\n" \
"ds_read_b64_tr_b16 a[26:27], v35 offset:12896\n" \
"v_mfma_f32_16x16x32_bf16 v[120:123], a[192:195], a[32:35], v[120:123]\n" \
"v_mfma_f32_16x16x32_bf16 v[120:123], a[196:199], a[36:39], v[120:123]\n" \
"v_mfma_f32_16x16x32_bf16 v[124:127], a[196:199], a[44:47], v[124:127]\n" \
"buffer_load_dwordx4 v0, s[80:83], s73 offen offset:0 lds\n" \
"v_mfma_f32_16x16x32_bf16 v[124:127], a[192:195], a[40:43], v[124:127]\n" \
"v_mfma_f32_16x16x32_bf16 v[192:195], a[192:195], a[48:51], v[192:195]\n" \
"v_mfma_f32_16x16x32_bf16 v[192:195], a[196:199], a[52:55], v[192:195]\n" \
"v_mfma_f32_16x16x32_bf16 v[196:199], a[196:199], a[60:63], v[196:199]\n" \
"v_mfma_f32_16x16x32_bf16 v[196:199], a[192:195], a[56:59], v[196:199]\n" \
"buffer_load_dwordx4 v1, s[80:83], s73 offen offset:1024 lds\n" \
"v_mfma_f32_16x16x32_bf16 v[128:131], a[200:203], a[32:35], v[128:131]\n" \
"ds_read_b64_tr_b16 a[192:193], v35 offset:10624\n" \
"ds_read_b64_tr_b16 a[194:195], v35 offset:12928\n" \
"v_mfma_f32_16x16x32_bf16 v[128:131], a[204:207], a[36:39], v[128:131]\n" \
"v_mfma_f32_16x16x32_bf16 v[132:135], a[204:207], a[44:47], v[132:135]\n" \
"v_mfma_f32_16x16x32_bf16 v[132:135], a[200:203], a[40:43], v[132:135]\n" \
"v_mfma_f32_16x16x32_bf16 v[200:203], a[200:203], a[48:51], v[200:203]\n" \
"buffer_load_dwordx4 v2, s[80:83], s73 offen offset:2048 lds\n" \
"v_mfma_f32_16x16x32_bf16 v[200:203], a[204:207], a[52:55], v[200:203]\n" \
"v_mfma_f32_16x16x32_bf16 v[204:207], a[204:207], a[60:63], v[204:207]\n" \
"v_mfma_f32_16x16x32_bf16 v[204:207], a[200:203], a[56:59], v[204:207]\n" \
"v_mfma_f32_16x16x32_bf16 v[136:139], a[208:211], a[32:35], v[136:139]\n" \
"v_mfma_f32_16x16x32_bf16 v[136:139], a[212:215], a[36:39], v[136:139]\n" \
"buffer_load_dwordx4 v3, s[80:83], s73 offen offset:3072 lds\n" \
"v_mfma_f32_16x16x32_bf16 v[140:143], a[212:215], a[44:47], v[140:143]\n" \
"s_add_u32 m0, s92, 0x1000\n" \
"v_mfma_f32_16x16x32_bf16 v[140:143], a[208:211], a[40:43], v[140:143]\n" \
"v_mfma_f32_16x16x32_bf16 v[208:211], a[208:211], a[48:51], v[208:211]\n" \
"v_mfma_f32_16x16x32_bf16 v[208:211], a[212:215], a[52:55], v[208:211]\n" \
"v_mfma_f32_16x16x32_bf16 v[212:215], a[212:215], a[60:63], v[212:215]\n" \
"buffer_load_dwordx4 v4, s[80:83], s73 offen offset:0 lds\n" \
"v_mfma_f32_16x16x32_bf16 v[212:215], a[208:211], a[56:59], v[212:215]\n" \
"v_mfma_f32_16x16x32_bf16 v[144:147], a[216:219], a[32:35], v[144:147]\n" \
"v_mfma_f32_16x16x32_bf16 v[144:147], a[0:3], a[36:39], v[144:147]\n" \
"v_mfma_f32_16x16x32_bf16 v[148:151], a[0:3], a[44:47], v[148:151]\n" \
"v_mfma_f32_16x16x32_bf16 v[148:151], a[216:219], a[40:43], v[148:151]\n" \
"buffer_load_dwordx4 v5, s[80:83], s73 offen offset:1024 lds\n" \
"v_mfma_f32_16x16x32_bf16 v[216:219], a[216:219], a[48:51], v[216:219]\n" \
"v_mfma_f32_16x16x32_bf16 v[216:219], a[0:3], a[52:55], v[216:219]\n" \
"v_mfma_f32_16x16x32_bf16 v[220:223], a[0:3], a[60:63], v[220:223]\n" \
"v_mfma_f32_16x16x32_bf16 v[220:223], a[216:219], a[56:59], v[220:223]\n" \
"v_mfma_f32_16x16x32_bf16 v[152:155], a[4:7], a[32:35], v[152:155]\n" \
"buffer_load_dwordx4 v6, s[80:83], s73 offen offset:2048 lds\n" \
"s_waitcnt lgkmcnt(0)\n" \
"v_mfma_f32_16x16x32_bf16 v[152:155], a[8:11], a[36:39], v[152:155]\n" \
"v_mfma_f32_16x16x32_bf16 v[156:159], a[8:11], a[44:47], v[156:159]\n" \
"v_mfma_f32_16x16x32_bf16 v[156:159], a[4:7], a[40:43], v[156:159]\n" \
"v_mfma_f32_16x16x32_bf16 v[224:227], a[4:7], a[48:51], v[224:227]\n" \
"buffer_load_dwordx4 v7, s[80:83], s73 offen offset:3072 lds\n" \
"s_add_u32 m0, s92, 0x2000\n" \
"v_mfma_f32_16x16x32_bf16 v[224:227], a[8:11], a[52:55], v[224:227]\n" \
"v_readlane_b32 s92, v11, 42\n" \
"v_mfma_f32_16x16x32_bf16 v[228:231], a[8:11], a[60:63], v[228:231]\n" \
"v_mfma_f32_16x16x32_bf16 v[228:231], a[4:7], a[56:59], v[228:231]\n" \
"s_waitcnt vmcnt(23)\n" \
"s_barrier\n" \
"buffer_load_dwordx4 v8, s[80:83], s73 offen offset:0 lds\n" \
"s_mov_b32 m0, s94\n" \
"v_mfma_f32_16x16x32_bf16 v[160:163], a[12:15], a[32:35], v[160:163]\n" \
"ds_read_b64_tr_b16 a[196:197], v10 offset:27648\n" \
"ds_read_b64_tr_b16 a[198:199], v10 offset:32256\n" \
"v_mfma_f32_16x16x32_bf16 v[160:163], a[16:19], a[36:39], v[160:163]\n" \
"ds_read_b64_tr_b16 a[200:201], v10 offset:36864\n" \
"ds_read_b64_tr_b16 a[202:203], v10 offset:41472\n" \
"v_mfma_f32_16x16x32_bf16 v[164:167], a[16:19], a[44:47], v[164:167]\n" \
"ds_read_b64_tr_b16 a[204:205], v10 offset:27680\n" \
"ds_read_b64_tr_b16 a[206:207], v10 offset:32288\n" \
"v_mfma_f32_16x16x32_bf16 v[164:167], a[12:15], a[40:43], v[164:167]\n" \
"buffer_load_dwordx4 v9, s[84:87], 0 offen lds\n" \
"v_mfma_f32_16x16x32_bf16 v[232:235], a[12:15], a[48:51], v[232:235]\n" \
"ds_read_b64_tr_b16 a[208:209], v10 offset:36896\n" \
"ds_read_b64_tr_b16 a[210:211], v10 offset:41504\n" \
"v_mfma_f32_16x16x32_bf16 v[232:235], a[16:19], a[52:55], v[232:235]\n" \
"ds_read_b64_tr_b16 a[212:213], v10 offset:27712\n" \
"ds_read_b64_tr_b16 a[214:215], v10 offset:32320\n" \
"v_mfma_f32_16x16x32_bf16 v[236:239], a[16:19], a[60:63], v[236:239]\n" \
"ds_read_b64_tr_b16 a[216:217], v10 offset:36928\n" \
"ds_read_b64_tr_b16 a[218:219], v10 offset:41536\n" \
"v_mfma_f32_16x16x32_bf16 v[236:239], a[12:15], a[56:59], v[236:239]\n" \
"ds_read_b64_tr_b16 a[0:1], v10 offset:27744\n" \
"ds_read_b64_tr_b16 a[2:3], v10 offset:32352\n" \
"buffer_load_dwordx4 a[128:131], v20, s[56:59], s68 offen offset:0 sc1\n" \
"v_mfma_f32_16x16x32_bf16 v[168:171], a[20:23], a[32:35], v[168:171]\n" \
"ds_read_b64_tr_b16 a[4:5], v10 offset:36960\n" \
"ds_read_b64_tr_b16 a[6:7], v10 offset:41568\n" \
"v_mfma_f32_16x16x32_bf16 v[168:171], a[24:27], a[36:39], v[168:171]\n" \
"ds_read_b64_tr_b16 a[8:9], v10 offset:27776\n" \
"ds_read_b64_tr_b16 a[10:11], v10 offset:32384\n" \
"v_mfma_f32_16x16x32_bf16 v[172:175], a[24:27], a[44:47], v[172:175]\n" \
"ds_read_b64_tr_b16 a[12:13], v10 offset:36992\n" \
"ds_read_b64_tr_b16 a[14:15], v10 offset:41600\n" \
"v_mfma_f32_16x16x32_bf16 v[172:175], a[20:23], a[40:43], v[172:175]\n" \
"ds_read_b64_tr_b16 a[16:17], v10 offset:27808\n" \
"ds_read_b64_tr_b16 a[18:19], v10 offset:32416\n" \
"buffer_load_dwordx4 a[132:135], v20, s[56:59], s68 offen offset:1024 sc1\n" \
"v_mfma_f32_16x16x32_bf16 v[240:243], a[20:23], a[48:51], v[240:243]\n" \
"s_waitcnt vmcnt(12)\n" \
"v_cmp_ne_u32_e32 vcc, s62, v47\n" \
"v_mfma_f32_16x16x32_bf16 v[240:243], a[24:27], a[52:55], v[240:243]\n" \
"v_readlane_b32 s73, v11, 48\n" \
"v_readlane_b32 s94, v11, 44\n" \
"v_mfma_f32_16x16x32_bf16 v[244:247], a[24:27], a[60:63], v[244:247]\n" \
"ds_read_b64_tr_b16 a[24:25], v10 offset:27840\n" \
"ds_read_b64_tr_b16 a[26:27], v10 offset:32448\n" \
"v_mfma_f32_16x16x32_bf16 v[244:247], a[20:23], a[56:59], v[244:247]\n" \
"ds_read_b64_tr_b16 a[20:21], v10 offset:37024\n" \
"ds_read_b64_tr_b16 a[22:23], v10 offset:41632\n" \
"buffer_load_dwordx4 a[136:139], v20, s[56:59], s68 offen offset:2048 sc1\n" \
"v_mfma_f32_16x16x32_bf16 v[176:179], a[28:31], a[32:35], v[176:179]\n" \
"s_cbranch_vccnz L_sb_%=\n" \
"L_rb_%=:\n" \
"v_readlane_b32 s71, v11, 46\n" \
"v_mfma_f32_16x16x32_bf16 v[176:179], a[192:195], a[36:39], v[176:179]\n" \
"v_cmp_eq_u32_e32 vcc, 0, v44\n" \
"v_mfma_f32_16x16x32_bf16 v[180:183], a[192:195], a[44:47], v[180:183]\n" \
"v_mfma_f32_16x16x32_bf16 v[180:183], a[28:31], a[40:43], v[180:183]\n" \
"v_cndmask_b32_e32 v50, 0, v45, vcc\n" \
"v_cmp_eq_u32_e32 vcc, 1, v44\n" \
"v_mfma_f32_16x16x32_bf16 v[248:251], a[28:31], a[48:51], v[248:251]\n" \
"buffer_load_dwordx4 a[140:143], v20, s[56:59], s68 offen offset:3072 sc1\n" \
"v_mfma_f32_16x16x32_bf16 v[248:251], a[192:195], a[52:55], v[248:251]\n" \
"v_cndmask_b32_e32 v51, 0, v45, vcc\n" \
"v_bfe_u32 v45, v20, 1, 5\n" \
"v_mfma_f32_16x16x32_bf16 v[252:255], a[192:195], a[60:63], v[252:255]\n" \
"ds_read_b64_tr_b16 a[192:193], v10 offset:27872\n" \
"ds_read_b64_tr_b16 a[194:195], v10 offset:32480\n" \
"v_mfma_f32_16x16x32_bf16 v[252:255], a[28:31], a[56:59], v[252:255]\n" \
"ds_read_b64_tr_b16 a[28:29], v10 offset:37056\n" \
"s_waitcnt lgkmcnt(9)\n" \
"v_mfma_f32_16x16x32_bf16 v[112:115], a[196:199], a[64:67], v[112:115]\n" \
"ds_read_b64_tr_b16 a[30:31], v10 offset:41664\n" \
"v_and_b32_e32 v45, 24, v45\n" \
"v_mfma_f32_16x16x32_bf16 v[112:115], a[200:203], a[68:71], v[112:115]\n" \
"buffer_load_dwordx4 a[156:159], v32, s[56:59], s68 offen offset:3072 sc1\n" \
"v_mfma_f32_16x16x32_bf16 v[116:119], a[200:203], a[84:87], v[116:119]\n" \
"ds_read_b64_tr_b16 a[200:201], v10 offset:27904\n" \
"ds_read_b64_tr_b16 a[202:203], v10 offset:32512\n" \
"v_mfma_f32_16x16x32_bf16 v[116:119], a[196:199], a[80:83], v[116:119]\n" \
"ds_read_b64_tr_b16 a[196:197], v10 offset:37088\n" \
"ds_read_b64_tr_b16 a[198:199], v10 offset:41696\n" \
"v_mfma_f32_16x16x32_bf16 v[120:123], a[204:207], a[64:67], v[120:123]\n" \
"v_and_b32_e32 v44, 0x3c0, v20\n" \
"v_add_u32_e32 v45, 0x1d400, v45\n" \
"v_mfma_f32_16x16x32_bf16 v[120:123], a[208:211], a[68:71], v[120:123]\n" \
"v_add_u32_e32 v36, v44, v45\n" \
"v_mfma_f32_16x16x32_bf16 v[124:127], a[208:211], a[84:87], v[124:127]\n" \
"buffer_load_dwordx4 a[152:155], v32, s[56:59], s68 offen offset:2048 sc1\n" \
"v_mfma_f32_16x16x32_bf16 v[124:127], a[204:207], a[80:83], v[124:127]\n" \
"ds_read_b64_tr_b16 a[204:205], v10 offset:37120\n" \
"ds_read_b64_tr_b16 a[206:207], v10 offset:41728\n" \
"v_mfma_f32_16x16x32_bf16 v[128:131], a[212:215], a[64:67], v[128:131]\n" \
"ds_read_b64_tr_b16 a[208:209], v10 offset:46080\n" \
"ds_read_b64_tr_b16 a[210:211], v10 offset:50688\n" \
"v_mfma_f32_16x16x32_bf16 v[128:131], a[216:219], a[68:71], v[128:131]\n" \
"v_mfma_f32_16x16x32_bf16 v[132:135], a[216:219], a[84:87], v[132:135]\n" \
"ds_read_b64_tr_b16 a[216:217], v10 offset:46112\n" \
"ds_read_b64_tr_b16 a[218:219], v10 offset:50720\n" \
"buffer_load_dwordx4 a[148:151], v32, s[56:59], s68 offen offset:1024 sc1\n" \
"v_mfma_f32_16x16x32_bf16 v[132:135], a[212:215], a[80:83], v[132:135]\n" \
"ds_read_b64_tr_b16 a[212:213], v10 offset:55296\n" \
"ds_read_b64_tr_b16 a[214:215], v10 offset:59904\n" \
"v_mfma_f32_16x16x32_bf16 v[136:139], a[0:3], a[64:67], v[136:139]\n" \
"v_mfma_f32_16x16x32_bf16 v[136:139], a[4:7], a[68:71], v[136:139]\n" \
"v_mfma_f32_16x16x32_bf16 v[140:143], a[4:7], a[84:87], v[140:143]\n" \
"ds_read_b64_tr_b16 a[4:5], v10 offset:46144\n" \
"ds_read_b64_tr_b16 a[6:7], v10 offset:50752\n" \
"buffer_load_dwordx4 a[144:147], v32, s[56:59], s68 offen offset:0 sc1\n" \
"v_mfma_f32_16x16x32_bf16 v[140:143], a[0:3], a[80:83], v[140:143]\n" \
"ds_read_b64_tr_b16 a[0:1], v10 offset:55328\n" \
"ds_read_b64_tr_b16 a[2:3], v10 offset:59936\n" \
"v_mfma_f32_16x16x32_bf16 v[144:147], a[8:11], a[64:67], v[144:147]\n" \
"v_readlane_b32 s68, v11, 49\n" \
"v_mfma_f32_16x16x32_bf16 v[144:147], a[12:15], a[68:71], v[144:147]\n" \
"v_mfma_f32_16x16x32_bf16 v[148:151], a[12:15], a[84:87], v[148:151]\n" \
"ds_read_b64_tr_b16 a[12:13], v10 offset:46176\n" \
"ds_read_b64_tr_b16 a[14:15], v10 offset:50784\n" \
"buffer_load_dwordx4 a[96:99], v20, s[56:59], s69 offen offset:0 sc1\n" \
"v_mfma_f32_16x16x32_bf16 v[148:151], a[8:11], a[80:83], v[148:151]\n" \
"ds_read_b64_tr_b16 a[8:9], v10 offset:55360\n" \
"s_waitcnt lgkmcnt(15)\n" \
"v_mfma_f32_16x16x32_bf16 v[152:155], a[16:19], a[64:67], v[152:155]\n" \
"ds_read_b64_tr_b16 a[10:11], v10 offset:59968\n" \
"v_mfma_f32_16x16x32_bf16 v[152:155], a[20:23], a[68:71], v[152:155]\n" \
"v_mfma_f32_16x16x32_bf16 v[156:159], a[20:23], a[84:87], v[156:159]\n" \
"ds_read_b64_tr_b16 a[20:21], v10 offset:46208\n" \
"ds_read_b64_tr_b16 a[22:23], v10 offset:50816\n" \
"buffer_load_dwordx4 a[100:103], v20, s[56:59], s69 offen offset:1024 sc1\n" \
"v_mfma_f32_16x16x32_bf16 v[156:159], a[16:19], a[80:83], v[156:159]\n" \
"ds_read_b64_tr_b16 a[16:17], v10 offset:55392\n" \
"ds_read_b64_tr_b16 a[18:19], v10 offset:60000\n" \
"v_mfma_f32_16x16x32_bf16 v[160:163], a[24:27], a[64:67], v[160:163]\n" \
"v_mfma_f32_16x16x32_bf16 v[160:163], a[28:31], a[68:71], v[160:163]\n" \
"v_mfma_f32_16x16x32_bf16 v[164:167], a[28:31], a[84:87], v[164:167]\n" \
"ds_read_b64_tr_b16 a[28:29], v10 offset:46240\n" \
"ds_read_b64_tr_b16 a[30:31], v10 offset:50848\n" \
"buffer_load_dwordx4 a[104:107], v20, s[56:59], s69 offen offset:2048 sc1\n" \
"v_mfma_f32_16x16x32_bf16 v[164:167], a[24:27], a[80:83], v[164:167]\n" \
"ds_read_b64_tr_b16 a[24:25], v10 offset:55424\n" \
"ds_read_b64_tr_b16 a[26:27], v10 offset:60032\n" \
"v_mfma_f32_16x16x32_bf16 v[168:171], a[192:195], a[64:67], v[168:171]\n" \
"v_mfma_f32_16x16x32_bf16 v[168:171], a[196:199], a[68:71], v[168:171]\n" \
"v_mfma_f32_16x16x32_bf16 v[172:175], a[196:199], a[84:87], v[172:175]\n" \
"ds_read_b64_tr_b16 a[196:197], v10 offset:46272\n" \
"ds_read_b64_tr_b16 a[198:199], v10 offset:50880\n" \
"buffer_load_dwordx4 a[108:111], v20, s[56:59], s69 offen offset:3072 sc1\n" \
"v_mfma_f32_16x16x32_bf16 v[172:175], a[192:195], a[80:83], v[172:175]\n" \
"ds_read_b64_tr_b16 a[192:193], v10 offset:55456\n" \
"ds_read_b64_tr_b16 a[194:195], v10 offset:60064\n" \
"v_mfma_f32_16x16x32_bf16 v[176:179], a[200:203], a[64:67], v[176:179]\n" \
"s_waitcnt lgkmcnt(13)\n" \
"v_mfma_f32_16x16x32_bf16 v[176:179], a[204:207], a[68:71], v[176:179]\n" \
"v_mfma_f32_16x16x32_bf16 v[180:183], a[204:207], a[84:87], v[180:183]\n" \
"ds_read_b64_tr_b16 a[204:205], v10 offset:46304\n" \
"ds_read_b64_tr_b16 a[206:207], v10 offset:50912\n" \
"buffer_load_dwordx4 a[124:127], v32, s[56:59], s69 offen offset:3072 sc1\n" \
"v_mfma_f32_16x16x32_bf16 v[180:183], a[200:203], a[80:83], v[180:183]\n" \
"ds_read_b64_tr_b16 a[200:201], v10 offset:55488\n" \
"ds_read_b64_tr_b16 a[202:203], v10 offset:60096\n" \
"v_mfma_f32_16x16x32_bf16 v[112:115], a[208:211], a[72:75], v[112:115]\n" \
"v_mfma_f32_16x16x32_bf16 v[112:115], a[212:215], a[76:79], v[112:115]\n" \
"v_mfma_f32_16x16x32_bf16 v[116:119], a[212:215], a[92:95], v[116:119]\n" \
"ds_read_b64_tr_b16 a[212:213], v10 offset:46336\n" \
"ds_read_b64_tr_b16 a[214:215], v10 offset:50944\n" \
"buffer_load_dwordx4 a[120:123], v32, s[56:59], s69 offen offset:2048 sc1\n" \
"v_mfma_f32_16x16x32_bf16 v[116:119], a[208:211], a[88:91], v[116:119]\n" \
"ds_read_b64_tr_b16 a[208:209], v10 offset:55520\n" \
"ds_read_b64_tr_b16 a[210:211], v10 offset:60128\n" \
"v_mfma_f32_16x16x32_bf16 v[120:123], a[216:219], a[72:75], v[120:123]\n" \
"v_mfma_f32_16x16x32_bf16 v[120:123], a[0:3], a[76:79], v[120:123]\n" \
"v_mfma_f32_16x16x32_bf16 v[124:127], a[0:3], a[92:95], v[124:127]\n" \
"v_mfma_f32_16x16x32_bf16 v[124:127], a[216:219], a[88:91], v[124:127]\n" \
"buffer_load_dwordx4 a[116:119], v32, s[56:59], s69 offen offset:1024 sc1\n" \
"v_mfma_f32_16x16x32_bf16 v[128:131], a[4:7], a[72:75], v[128:131]\n" \
"ds_read_b64_tr_b16 a[216:217], v10 offset:55552\n" \
"s_waitcnt lgkmcnt(9)\n" \
"v_mfma_f32_16x16x32_bf16 v[128:131], a[8:11], a[76:79], v[128:131]\n" \
"ds_read_b64_tr_b16 a[218:219], v10 offset:60160\n" \
"v_mfma_f32_16x16x32_bf16 v[132:135], a[8:11], a[92:95], v[132:135]\n" \
"v_mfma_f32_16x16x32_bf16 v[132:135], a[4:7], a[88:91], v[132:135]\n" \
"v_mfma_f32_16x16x32_bf16 v[136:139], a[12:15], a[72:75], v[136:139]\n" \
"buffer_load_dwordx4 a[112:115], v32, s[56:59], s69 offen offset:0 sc1\n" \
"v_mfma_f32_16x16x32_bf16 v[136:139], a[16:19], a[76:79], v[136:139]\n" \
"v_readlane_b32 s69, v11, 5\n" \
"v_mfma_f32_16x16x32_bf16 v[140:143], a[16:19], a[92:95], v[140:143]\n" \
"v_mfma_f32_16x16x32_bf16 v[140:143], a[12:15], a[88:91], v[140:143]\n" \
"s_waitcnt vmcnt(16)\n" \
"s_barrier\n" \
"buffer_load_dwordx4 a[32:35], v20, s[56:59], s64 offen offset:0 sc1\n" \
"v_mfma_f32_16x16x32_bf16 v[144:147], a[20:23], a[72:75], v[144:147]\n" \
"ds_read_b64_tr_b16 a[0:1], v33 offset:18432\n" \
"ds_read_b64_tr_b16 a[2:3], v33 offset:23040\n" \
"v_mfma_f32_16x16x32_bf16 v[144:147], a[24:27], a[76:79], v[144:147]\n" \
"ds_read_b64_tr_b16 a[4:5], v33 offset:27648\n" \
"ds_read_b64_tr_b16 a[6:7], v33 offset:32256\n" \
"v_mfma_f32_16x16x32_bf16 v[148:151], a[24:27], a[92:95], v[148:151]\n" \
"ds_read_b64_tr_b16 a[8:9], v33 offset:36864\n" \
"ds_read_b64_tr_b16 a[10:11], v33 offset:41472\n" \
"v_mfma_f32_16x16x32_bf16 v[148:151], a[20:23], a[88:91], v[148:151]\n" \
"ds_read_b64_tr_b16 a[12:13], v33 offset:46080\n" \
"ds_read_b64_tr_b16 a[14:15], v33 offset:50688\n" \
"buffer_load_dwordx4 a[36:39], v20, s[56:59], s64 offen offset:1024 sc1\n" \
"v_mfma_f32_16x16x32_bf16 v[152:155], a[28:31], a[72:75], v[152:155]\n" \
"ds_read_b64_tr_b16 a[16:17], v34 offset:0\n" \
"ds_read_b64_tr_b16 a[18:19], v34 offset:4608\n" \
"v_mfma_f32_16x16x32_bf16 v[152:155], a[192:195], a[76:79], v[152:155]\n" \
"ds_read_b64_tr_b16 a[20:21], v34 offset:9216\n" \
"ds_read_b64_tr_b16 a[22:23], v34 offset:13824\n" \
"v_mfma_f32_16x16x32_bf16 v[156:159], a[192:195], a[92:95], v[156:159]\n" \
"ds_read_b64_tr_b16 a[24:25], v34 offset:18432\n" \
"ds_read_b64_tr_b16 a[26:27], v34 offset:23040\n" \
"v_mfma_f32_16x16x32_bf16 v[156:159], a[28:31], a[88:91], v[156:159]\n" \
"ds_read_b64_tr_b16 a[28:29], v33 offset:0\n" \
"ds_read_b64_tr_b16 a[30:31], v33 offset:4608\n" \
"buffer_load_dwordx4 a[40:43], v20, s[56:59], s64 offen offset:2048 sc1\n" \
"v_mfma_f32_16x16x32_bf16 v[160:163], a[196:199], a[72:75], v[160:163]\n" \
"ds_read_b64_tr_b16 a[192:193], v33 offset:18464\n" \
"s_waitcnt lgkmcnt(15)\n" \
"v_mfma_f32_16x16x32_bf16 v[160:163], a[200:203], a[76:79], v[160:163]\n" \
"ds_read_b64_tr_b16 a[194:195], v33 offset:23072\n" \
"v_mfma_f32_16x16x32_bf16 v[164:167], a[200:203], a[92:95], v[164:167]\n" \
"buffer_load_dwordx4 a[44:47], v20, s[56:59], s64 offen offset:3072 sc1\n" \
"v_mfma_f32_16x16x32_bf16 v[164:167], a[196:199], a[88:91], v[164:167]\n" \
"ds_read_b64_tr_b16 a[196:197], v33 offset:27680\n" \
"ds_read_b64_tr_b16 a[198:199], v33 offset:32288\n" \
"v_mfma_f32_16x16x32_bf16 v[168:171], a[204:207], a[72:75], v[168:171]\n" \
"ds_read_b64_tr_b16 a[200:201], v33 offset:36896\n" \
"ds_read_b64_tr_b16 a[202:203], v33 offset:41504\n" \
"v_mfma_f32_16x16x32_bf16 v[168:171], a[208:211], a[76:79], v[168:171]\n" \
"v_mfma_f32_16x16x32_bf16 v[172:175], a[208:211], a[92:95], v[172:175]\n" \
"ds_read_b64_tr_b16 a[208:209], v34 offset:32\n" \
"ds_read_b64_tr_b16 a[210:211], v34 offset:4640\n" \
"buffer_load_dwordx4 a[60:63], v32, s[56:59], s64 offen offset:3072 sc1\n" \
"v_mfma_f32_16x16x32_bf16 v[172:175], a[204:207], a[88:91], v[172:175]\n" \
"ds_read_b64_tr_b16 a[204:205], v33 offset:46112\n" \
"ds_read_b64_tr_b16 a[206:207], v33 offset:50720\n" \
"v_mfma_f32_16x16x32_bf16 v[176:179], a[212:215], a[72:75], v[176:179]\n" \
"v_mfma_f32_16x16x32_bf16 v[176:179], a[216:219], a[76:79], v[176:179]\n" \
"v_mfma_f32_16x16x32_bf16 v[180:183], a[216:219], a[92:95], v[180:183]\n" \
"ds_read_b64_tr_b16 a[216:217], v34 offset:18464\n" \
"ds_read_b64_tr_b16 a[218:219], v34 offset:23072\n" \
"buffer_load_dwordx4 a[56:59], v32, s[56:59], s64 offen offset:2048 sc1\n" \
"v_mfma_f32_16x16x32_bf16 v[180:183], a[212:215], a[88:91], v[180:183]\n" \
"ds_read_b64_tr_b16 a[212:213], v34 offset:9248\n" \
"s_waitcnt vmcnt(21)\n" \
"v_mfma_f32_16x16x32_bf16 v[112:115], a[0:3], a[128:131], v[112:115]\n" \
"ds_read_b64_tr_b16 a[214:215], v34 offset:13856\n" \
"s_waitcnt vmcnt(20) lgkmcnt(13)\n" \
"v_mfma_f32_16x16x32_bf16 v[112:115], a[4:7], a[132:135], v[112:115]\n" \
"s_waitcnt vmcnt(19)\n" \
"v_mfma_f32_16x16x32_bf16 v[112:115], a[8:11], a[136:139], v[112:115]\n" \
"s_waitcnt vmcnt(18)\n" \
"v_mfma_f32_16x16x32_bf16 v[112:115], a[12:15], a[140:143], v[112:115]\n" \
"buffer_load_dwordx4 a[52:55], v32, s[56:59], s64 offen offset:1024 sc1\n" \
"s_waitcnt vmcnt(18)\n" \
"v_mfma_f32_16x16x32_bf16 v[116:119], a[12:15], a[156:159], v[116:119]\n" \
"ds_read_b64_tr_b16 a[12:13], v33 offset:36928\n" \
"s_waitcnt vmcnt(17)\n" \
"v_mfma_f32_16x16x32_bf16 v[116:119], a[8:11], a[152:155], v[116:119]\n" \
"ds_read_b64_tr_b16 a[8:9], v33 offset:27712\n" \
"ds_read_b64_tr_b16 a[10:11], v33 offset:32320\n" \
"ds_read_b64_tr_b16 a[14:15], v33 offset:41536\n" \
"s_waitcnt vmcnt(16)\n" \
"v_mfma_f32_16x16x32_bf16 v[116:119], a[4:7], a[148:151], v[116:119]\n" \
"buffer_load_dwordx4 a[48:51], v32, s[56:59], s64 offen offset:0 sc1\n" \
"ds_read_b64_tr_b16 a[4:5], v33 offset:18496\n" \
"ds_read_b64_tr_b16 a[6:7], v33 offset:23104\n" \
"v_readlane_b32 s64, v11, 38\n" \
"s_waitcnt vmcnt(16)\n" \
"v_mfma_f32_16x16x32_bf16 v[116:119], a[0:3], a[144:147], v[116:119]\n" \
"buffer_load_dwordx4 a[160:163], v20, s[56:59], s72 offen offset:0 sc1\n" \
"ds_read_b64_tr_b16 a[0:1], v33 offset:32\n" \
"ds_read_b64_tr_b16 a[2:3], v33 offset:4640\n" \
"s_waitcnt vmcnt(16)\n" \
"v_mfma_f32_16x16x32_bf16 v[112:115], a[16:19], a[96:99], v[112:115]\n" \
"buffer_load_dwordx4 a[164:167], v20, s[56:59], s72 offen offset:1024 sc1\n" \
"s_waitcnt vmcnt(16)\n" \
"v_mfma_f32_16x16x32_bf16 v[112:115], a[20:23], a[100:103], v[112:115]\n" \
"buffer_load_dwordx4 a[168:171], v20, s[56:59], s72 offen offset:2048 sc1\n" \
"s_waitcnt vmcnt(16)\n" \
"v_mfma_f32_16x16x32_bf16 v[112:115], a[24:27], a[104:107], v[112:115]\n" \
"buffer_load_dwordx4 a[172:175], v20, s[56:59], s72 offen offset:3072 sc1\n" \
"s_waitcnt vmcnt(16)\n" \
"v_mfma_f32_16x16x32_bf16 v[112:115], a[28:31], a[108:111], v[112:115]\n" \
"buffer_load_dwordx4 a[188:191], v32, s[56:59], s72 offen offset:3072 sc1\n" \
"s_waitcnt vmcnt(16)\n" \
"v_mfma_f32_16x16x32_bf16 v[116:119], a[28:31], a[124:127], v[116:119]\n" \
"buffer_load_dwordx4 a[184:187], v32, s[56:59], s72 offen offset:2048 sc1\n" \
"ds_read_b64_tr_b16 a[28:29], v34 offset:18496\n" \
"ds_read_b64_tr_b16 a[30:31], v34 offset:23104\n" \
"s_waitcnt vmcnt(16)\n" \
"v_mfma_f32_16x16x32_bf16 v[116:119], a[24:27], a[120:123], v[116:119]\n" \
"v_cvt_pk_bf16_f32 v112, v112, v113\n" \
"v_cvt_pk_bf16_f32 v113, v114, v115\n" \
"s_waitcnt vmcnt(15)\n" \
"v_mfma_f32_16x16x32_bf16 v[116:119], a[20:23], a[116:119], v[116:119]\n" \
"buffer_load_dwordx4 a[180:183], v32, s[56:59], s72 offen offset:1024 sc1\n" \
"ds_read_b64_tr_b16 a[20:21], v34 offset:64\n" \
"ds_read_b64_tr_b16 a[22:23], v34 offset:4672\n" \
"ds_read_b64_tr_b16 a[24:25], v34 offset:9280\n" \
"ds_read_b64_tr_b16 a[26:27], v34 offset:13888\n" \
"s_waitcnt vmcnt(15)\n" \
"v_mfma_f32_16x16x32_bf16 v[116:119], a[16:19], a[112:115], v[116:119]\n" \
"ds_read_b64_tr_b16 a[16:17], v33 offset:46144\n" \
"buffer_load_dwordx4 a[176:179], v32, s[56:59], s72 offen offset:0 sc1\n" \
"s_waitcnt lgkmcnt(5)\n" \
"v_mfma_f32_16x16x32_bf16 v[120:123], a[192:195], a[128:131], v[120:123]\n" \
"ds_read_b64_tr_b16 a[18:19], v33 offset:50752\n" \
"v_readlane_b32 s72, v11, 47\n" \
"v_mfma_f32_16x16x32_bf16 v[120:123], a[196:199], a[132:135], v[120:123]\n" \
"v_mfma_f32_16x16x32_bf16 v[120:123], a[200:203], a[136:139], v[120:123]\n" \
"v_cvt_pk_bf16_f32 v114, v116, v117\n" \
"v_cvt_pk_bf16_f32 v115, v118, v119\n" \
"v_mfma_f32_16x16x32_bf16 v[120:123], a[204:207], a[140:143], v[120:123]\n" \
"buffer_store_dwordx4 v[112:115], v20, s[56:59], s65 offen offset:0 sc1\n" \
"s_add_u32 s65, s65, 0x3800\n" \
"v_mfma_f32_16x16x32_bf16 v[124:127], a[204:207], a[156:159], v[124:127]\n" \
"ds_read_b64_tr_b16 a[204:205], v10 offset:46336\n" \
"ds_read_b64_tr_b16 a[206:207], v10 offset:50944\n" \
"v_mfma_f32_16x16x32_bf16 v[124:127], a[200:203], a[152:155], v[124:127]\n" \
"ds_read_b64_tr_b16 a[200:201], v10 offset:37120\n" \
"ds_read_b64_tr_b16 a[202:203], v10 offset:41728\n" \
"v_mfma_f32_16x16x32_bf16 v[124:127], a[196:199], a[148:151], v[124:127]\n" \
"ds_read_b64_tr_b16 a[196:197], v10 offset:27904\n" \
"ds_read_b64_tr_b16 a[198:199], v10 offset:32512\n" \
"v_mfma_f32_16x16x32_bf16 v[124:127], a[192:195], a[144:147], v[124:127]\n" \
"buffer_load_dwordx4 a[220:223], v20, s[56:59], s93 offen offset:0 sc1\n" \
"v_mfma_f32_16x16x32_bf16 v[120:123], a[208:211], a[96:99], v[120:123]\n" \
"ds_read_b64_tr_b16 a[192:193], v33 offset:64\n" \
"ds_read_b64_tr_b16 a[194:195], v33 offset:4672\n" \
"v_mfma_f32_16x16x32_bf16 v[120:123], a[212:215], a[100:103], v[120:123]\n" \
"v_mfma_f32_16x16x32_bf16 v[120:123], a[216:219], a[104:107], v[120:123]\n" \
"v_mfma_f32_16x16x32_bf16 v[120:123], a[0:3], a[108:111], v[120:123]\n" \
"v_mfma_f32_16x16x32_bf16 v[124:127], a[0:3], a[124:127], v[124:127]\n" \
"buffer_load_dwordx4 a[224:227], v20, s[56:59], s93 offen offset:1024 sc1\n" \
"v_mfma_f32_16x16x32_bf16 v[124:127], a[216:219], a[120:123], v[124:127]\n" \
"ds_read_b64_tr_b16 a[216:217], v33 offset:27904\n" \
"ds_read_b64_tr_b16 a[218:219], v33 offset:32512\n" \
"v_mfma_f32_16x16x32_bf16 v[124:127], a[212:215], a[116:119], v[124:127]\n" \
"ds_read_b64_tr_b16 a[212:213], v33 offset:18688\n" \
"ds_read_b64_tr_b16 a[214:215], v33 offset:23296\n" \
"v_cvt_pk_bf16_f32 v120, v120, v121\n" \
"v_cvt_pk_bf16_f32 v121, v122, v123\n" \
"v_mfma_f32_16x16x32_bf16 v[124:127], a[208:211], a[112:115], v[124:127]\n" \
"ds_read_b64_tr_b16 a[208:209], v10 offset:55552\n" \
"ds_read_b64_tr_b16 a[210:211], v10 offset:60160\n" \
"v_mfma_f32_16x16x32_bf16 v[128:131], a[4:7], a[128:131], v[128:131]\n" \
"buffer_load_dwordx4 a[228:231], v20, s[56:59], s93 offen offset:2048 sc1\n" \
"v_mfma_f32_16x16x32_bf16 v[128:131], a[8:11], a[132:135], v[128:131]\n" \
"ds_read_b64_tr_b16 a[0:1], v33 offset:37120\n" \
"ds_read_b64_tr_b16 a[2:3], v33 offset:41728\n" \
"v_mfma_f32_16x16x32_bf16 v[128:131], a[12:15], a[136:139], v[128:131]\n" \
"v_cvt_pk_bf16_f32 v122, v124, v125\n" \
"v_cvt_pk_bf16_f32 v123, v126, v127\n" \
"s_waitcnt lgkmcnt(13)\n" \
"v_mfma_f32_16x16x32_bf16 v[128:131], a[16:19], a[140:143], v[128:131]\n" \
"buffer_store_dwordx4 v[120:123], v20, s[56:59], s66 offen offset:0 sc1\n" \
"s_add_u32 s66, s66, 0x3000\n" \
"v_mfma_f32_16x16x32_bf16 v[132:135], a[16:19], a[156:159], v[132:135]\n" \
"ds_read_b64_tr_b16 a[16:17], v34 offset:18688\n" \
"ds_read_b64_tr_b16 a[18:19], v34 offset:23296\n" \
"v_mfma_f32_16x16x32_bf16 v[132:135], a[12:15], a[152:155], v[132:135]\n" \
"ds_read_b64_tr_b16 a[12:13], v34 offset:9472\n" \
"ds_read_b64_tr_b16 a[14:15], v34 offset:14080\n" \
"v_mfma_f32_16x16x32_bf16 v[132:135], a[8:11], a[148:151], v[132:135]\n" \
"ds_read_b64_tr_b16 a[8:9], v34 offset:256\n" \
"ds_read_b64_tr_b16 a[10:11], v34 offset:4864\n" \
"v_mfma_f32_16x16x32_bf16 v[132:135], a[4:7], a[144:147], v[132:135]\n" \
"buffer_load_dwordx4 a[232:235], v20, s[56:59], s93 offen offset:3072 sc1\n" \
"v_mfma_f32_16x16x32_bf16 v[128:131], a[20:23], a[96:99], v[128:131]\n" \
"ds_read_b64_tr_b16 a[4:5], v33 offset:46336\n" \
"L_pf21:\n" \
"s_cbranch_execz L_retB\n" \
"s_nop 0\n" \
"ds_read_b64_tr_b16 a[6:7], v33 offset:50944\n" \
"v_mfma_f32_16x16x32_bf16 v[128:131], a[24:27], a[100:103], v[128:131]\n" \
"v_mfma_f32_16x16x32_bf16 v[128:131], a[28:31], a[104:107], v[128:131]\n" \
"s_waitcnt lgkmcnt(15)\n" \
"v_mfma_f32_16x16x32_bf16 v[128:131], a[192:195], a[108:111], v[128:131]\n" \
"v_mfma_f32_16x16x32_bf16 v[132:135], a[192:195], a[124:127], v[132:135]\n" \
"buffer_load_dwordx4 a[248:251], v32, s[56:59], s93 offen offset:3072 sc1\n" \
"v_mfma_f32_16x16x32_bf16 v[132:135], a[28:31], a[120:123], v[132:135]\n" \
"ds_read_b64_tr_b16 a[28:29], v10 offset:36960\n" \
"ds_read_b64_tr_b16 a[30:31], v10 offset:41568\n" \
"v_mfma_f32_16x16x32_bf16 v[132:135], a[24:27], a[116:119], v[132:135]\n" \
"ds_read_b64_tr_b16 a[24:25], v10 offset:27744\n" \
"ds_read_b64_tr_b16 a[26:27], v10 offset:32352\n" \
"v_cvt_pk_bf16_f32 v128, v128, v129\n" \
"v_cvt_pk_bf16_f32 v129, v130, v131\n" \
"v_mfma_f32_16x16x32_bf16 v[132:135], a[20:23], a[112:115], v[132:135]\n" \
"ds_read_b64_tr_b16 a[20:21], v33 offset:256\n" \
"s_waitcnt vmcnt(22)\n" \
"v_mfma_f32_16x16x32_bf16 v[248:251], a[196:199], a[32:35], v[248:251]\n" \
"ds_read_b64_tr_b16 a[22:23], v33 offset:4864\n" \
"buffer_load_dwordx4 a[244:247], v32, s[56:59], s93 offen offset:2048 sc1\n" \
"s_waitcnt vmcnt(22)\n" \
"v_mfma_f32_16x16x32_bf16 v[248:251], a[200:203], a[36:39], v[248:251]\n" \
"ds_read_b64_tr_b16 a[192:193], v10 offset:46176\n" \
"v_cvt_pk_bf16_f32 v130, v132, v133\n" \
"v_cvt_pk_bf16_f32 v131, v134, v135\n" \
"s_waitcnt vmcnt(21)\n" \
"v_mfma_f32_16x16x32_bf16 v[248:251], a[204:207], a[40:43], v[248:251]\n" \
"ds_read_b64_tr_b16 a[194:195], v10 offset:50784\n" \
"buffer_store_dwordx4 v[128:131], v20, s[56:59], s67 offen offset:0 sc1\n" \
"s_add_u32 s67, s67, 0x2800\n" \
"s_waitcnt vmcnt(21) lgkmcnt(10)\n" \
"v_mfma_f32_16x16x32_bf16 v[248:251], a[208:211], a[44:47], v[248:251]\n" \
"s_waitcnt vmcnt(20)\n" \
"v_mfma_f32_16x16x32_bf16 v[252:255], a[208:211], a[60:63], v[252:255]\n" \
"ds_read_b64_tr_b16 a[208:209], v33 offset:36960\n" \
"ds_read_b64_tr_b16 a[210:211], v33 offset:41568\n" \
"s_waitcnt vmcnt(19)\n" \
"v_mfma_f32_16x16x32_bf16 v[252:255], a[204:207], a[56:59], v[252:255]\n" \
"buffer_load_dwordx4 a[240:243], v32, s[56:59], s93 offen offset:1024 sc1\n" \
"ds_read_b64_tr_b16 a[204:205], v33 offset:27744\n" \
"ds_read_b64_tr_b16 a[206:207], v33 offset:32352\n" \
"s_waitcnt vmcnt(19)\n" \
"v_mfma_f32_16x16x32_bf16 v[252:255], a[200:203], a[52:55], v[252:255]\n" \
"buffer_load_dwordx4 a[236:239], v32, s[56:59], s93 offen offset:0 sc1\n" \
"ds_read_b64_tr_b16 a[200:201], v33 offset:18528\n" \
"ds_read_b64_tr_b16 a[202:203], v33 offset:23136\n" \
"v_readlane_b32 s93, v11, 43\n" \
"s_waitcnt vmcnt(19)\n" \
"v_mfma_f32_16x16x32_bf16 v[252:255], a[196:199], a[48:51], v[252:255]\n" \
"ds_read_b64_tr_b16 a[196:197], v10 offset:55392\n" \
"ds_read_b64_tr_b16 a[198:199], v10 offset:60000\n" \
"v_mfma_f32_16x16x32_bf16 v[176:179], a[212:215], a[128:131], v[176:179]\n" \
"buffer_load_dwordx4 v[64:67], v20, s[56:59], s95 offen offset:0 sc1\n" \
"v_mfma_f32_16x16x32_bf16 v[176:179], a[216:219], a[132:135], v[176:179]\n" \
"v_mfma_f32_16x16x32_bf16 v[176:179], a[0:3], a[136:139], v[176:179]\n" \
"s_waitcnt lgkmcnt(6)\n" \
"v_mfma_f32_16x16x32_bf16 v[176:179], a[4:7], a[140:143], v[176:179]\n" \
"v_mfma_f32_16x16x32_bf16 v[180:183], a[4:7], a[156:159], v[180:183]\n" \
"v_mfma_f32_16x16x32_bf16 v[180:183], a[0:3], a[152:155], v[180:183]\n" \
"buffer_load_dwordx4 v[72:75], v32, s[56:59], s95 offen offset:0 sc1\n" \
"v_mfma_f32_16x16x32_bf16 v[180:183], a[216:219], a[148:151], v[180:183]\n" \
"v_mfma_f32_16x16x32_bf16 v[180:183], a[212:215], a[144:147], v[180:183]\n" \
"s_waitcnt vmcnt(20)\n" \
"v_mfma_f32_16x16x32_bf16 v[248:251], a[212:215], a[160:163], v[248:251]\n" \
"s_waitcnt vmcnt(19)\n" \
"v_mfma_f32_16x16x32_bf16 v[248:251], a[216:219], a[164:167], v[248:251]\n" \
"s_waitcnt vmcnt(18)\n" \
"v_mfma_f32_16x16x32_bf16 v[248:251], a[0:3], a[168:171], v[248:251]\n" \
"buffer_load_dwordx4 v[80:83], v20, s[56:59], s96 offen offset:0 sc1\n" \
"s_waitcnt vmcnt(18)\n" \
"v_mfma_f32_16x16x32_bf16 v[248:251], a[4:7], a[172:175], v[248:251]\n" \
"buffer_load_dwordx4 v[88:91], v32, s[56:59], s96 offen offset:0 sc1\n" \
"s_waitcnt vmcnt(18)\n" \
"v_mfma_f32_16x16x32_bf16 v[252:255], a[4:7], a[188:191], v[252:255]\n" \
"ds_read_b64_tr_b16 a[4:5], v34 offset:18528\n" \
"ds_read_b64_tr_b16 a[6:7], v34 offset:23136\n" \
"buffer_load_dwordx4 v[68:71], v20, s[56:59], s95 offen offset:1024 sc1\n" \
"s_waitcnt vmcnt(18)\n" \
"v_mfma_f32_16x16x32_bf16 v[252:255], a[0:3], a[184:187], v[252:255]\n" \
"ds_read_b64_tr_b16 a[0:1], v34 offset:9312\n" \
"ds_read_b64_tr_b16 a[2:3], v34 offset:13920\n" \
"buffer_load_dwordx4 v[76:79], v32, s[56:59], s95 offen offset:1024 sc1\n" \
"s_add_u32 s95, s63, 0x0\n" \
"s_waitcnt vmcnt(18)\n" \
"v_mfma_f32_16x16x32_bf16 v[252:255], a[216:219], a[180:183], v[252:255]\n" \
"ds_read_b64_tr_b16 a[216:217], v34 offset:96\n" \
"ds_read_b64_tr_b16 a[218:219], v34 offset:4704\n" \
"buffer_load_dwordx4 v[84:87], v20, s[56:59], s96 offen offset:1024 sc1\n" \
"s_waitcnt vmcnt(18)\n" \
"v_mfma_f32_16x16x32_bf16 v[252:255], a[212:215], a[176:179], v[252:255]\n" \
"ds_read_b64_tr_b16 a[212:213], v33 offset:46176\n" \
"ds_read_b64_tr_b16 a[214:215], v33 offset:50784\n" \
"v_mfma_f32_16x16x32_bf16 v[176:179], a[8:11], a[96:99], v[176:179]\n" \
"buffer_load_dwordx4 v[92:95], v32, s[56:59], s96 offen offset:1024 sc1\n" \
"v_mfma_f32_16x16x32_bf16 v[176:179], a[12:15], a[100:103], v[176:179]\n" \
"s_add_u32 s96, s63, 0x800\n" \
"v_mfma_f32_16x16x32_bf16 v[176:179], a[16:19], a[104:107], v[176:179]\n" \
"v_mfma_f32_16x16x32_bf16 v[176:179], a[20:23], a[108:111], v[176:179]\n" \
"s_waitcnt vmcnt(18)\n" \
"s_mov_b64 exec, 1\n" \
"buffer_store_dword v39, off, s[56:59], s65 offset:0 sc1\n" \
"s_mov_b64 exec, -1\n" \
"v_mfma_f32_16x16x32_bf16 v[180:183], a[20:23], a[124:127], v[180:183]\n" \
"s_add_u32 s65, s63, 0x1800\n" \
"v_mfma_f32_16x16x32_bf16 v[180:183], a[16:19], a[120:123], v[180:183]\n" \
"v_mfma_f32_16x16x32_bf16 v[180:183], a[12:15], a[116:119], v[180:183]\n" \
"v_cvt_pk_bf16_f32 v176, v176, v177\n" \
"v_cvt_pk_bf16_f32 v177, v178, v179\n" \
"v_mfma_f32_16x16x32_bf16 v[180:183], a[8:11], a[112:115], v[180:183]\n" \
"s_waitcnt vmcnt(18)\n" \
"v_mfma_f32_16x16x32_bf16 v[248:251], a[8:11], a[220:223], v[248:251]\n" \
"buffer_load_dwordx4 v[96:99], v20, s[56:59], s97 offen offset:0 sc1\n" \
"s_waitcnt vmcnt(18)\n" \
"v_mfma_f32_16x16x32_bf16 v[248:251], a[12:15], a[224:227], v[248:251]\n" \
"s_waitcnt vmcnt(17)\n" \
"v_mfma_f32_16x16x32_bf16 v[248:251], a[16:19], a[228:231], v[248:251]\n" \
"buffer_load_dwordx4 v[104:107], v32, s[56:59], s97 offen offset:0 sc1\n" \
"v_cvt_pk_bf16_f32 v178, v180, v181\n" \
"v_cvt_pk_bf16_f32 v179, v182, v183\n" \
"s_waitcnt vmcnt(16)\n" \
"v_mfma_f32_16x16x32_bf16 v[248:251], a[20:23], a[232:235], v[248:251]\n" \
"buffer_store_dwordx4 v[176:179], v20, s[56:59], s68 offen offset:0 sc1\n" \
"s_mov_b64 exec, 1\n" \
"buffer_store_dword v39, off, s[56:59], s66 offset:4 sc1\n" \
"s_mov_b64 exec, -1\n" \
"s_add_u32 s66, s63, 0x2000\n" \
"s_waitcnt vmcnt(17)\n" \
"v_mfma_f32_16x16x32_bf16 v[252:255], a[20:23], a[248:251], v[252:255]\n" \
"ds_read_b64_tr_b16 a[20:21], v10 offset:46208\n" \
"v_cvt_pk_bf16_f32 v248, v248, v249\n" \
"v_cvt_pk_bf16_f32 v249, v250, v251\n" \
"s_waitcnt vmcnt(16)\n" \
"v_mfma_f32_16x16x32_bf16 v[252:255], a[16:19], a[244:247], v[252:255]\n" \
"ds_read_b64_tr_b16 a[16:17], v10 offset:36992\n" \
"ds_read_b64_tr_b16 a[18:19], v10 offset:41600\n" \
"ds_read_b64_tr_b16 a[22:23], v10 offset:50816\n" \
"buffer_load_dwordx4 v[100:103], v20, s[56:59], s97 offen offset:1024 sc1\n" \
"s_waitcnt vmcnt(15)\n" \
"v_mfma_f32_16x16x32_bf16 v[252:255], a[12:15], a[240:243], v[252:255]\n" \
"s_mov_b64 exec, 1\n" \
"buffer_store_dword v39, off, s[56:59], s67 offset:8 sc1\n" \
"s_mov_b64 exec, -1\n" \
"ds_read_b64_tr_b16 a[12:13], v10 offset:27776\n" \
"ds_read_b64_tr_b16 a[14:15], v10 offset:32384\n" \
"s_add_u32 s67, s63, 0x2800\n" \
"s_waitcnt vmcnt(15)\n" \
"v_mfma_f32_16x16x32_bf16 v[252:255], a[8:11], a[236:239], v[252:255]\n" \
"ds_read_b64_tr_b16 a[8:9], v33 offset:96\n" \
"ds_read_b64_tr_b16 a[10:11], v33 offset:4704\n" \
"v_mfma_f32_16x16x32_bf16 v[208:211], a[24:27], a[32:35], v[208:211]\n" \
"buffer_load_dwordx4 v[108:111], v32, s[56:59], s97 offen offset:1024 sc1\n" \
"v_mfma_f32_16x16x32_bf16 v[208:211], a[28:31], a[36:39], v[208:211]\n" \
"s_add_u32 s97, s63, 0x1000\n" \
"v_mfma_f32_16x16x32_bf16 v[208:211], a[192:195], a[40:43], v[208:211]\n" \
"s_waitcnt lgkmcnt(4)\n" \
"v_mfma_f32_16x16x32_bf16 v[208:211], a[196:199], a[44:47], v[208:211]\n" \
"v_cvt_pk_bf16_f32 v250, v252, v253\n" \
"v_cvt_pk_bf16_f32 v251, v254, v255\n" \
"v_mfma_f32_16x16x32_bf16 v[212:215], a[196:199], a[60:63], v[212:215]\n" \
"buffer_store_dwordx4 v[248:251], v20, s[56:59], s68 offen offset:1024 sc1\n" \
"v_mfma_f32_16x16x32_bf16 v[212:215], a[192:195], a[56:59], v[212:215]\n" \
"ds_read_b64_tr_b16 a[192:193], v33 offset:27776\n" \
"ds_read_b64_tr_b16 a[194:195], v33 offset:32384\n" \
"v_mfma_f32_16x16x32_bf16 v[212:215], a[28:31], a[52:55], v[212:215]\n" \
"ds_read_b64_tr_b16 a[28:29], v33 offset:18560\n" \
"ds_read_b64_tr_b16 a[30:31], v33 offset:23168\n" \
"v_mfma_f32_16x16x32_bf16 v[212:215], a[24:27], a[48:51], v[212:215]\n" \
"ds_read_b64_tr_b16 a[24:25], v10 offset:55424\n" \
"ds_read_b64_tr_b16 a[26:27], v10 offset:60032\n" \
"v_mfma_f32_16x16x32_bf16 v[136:139], a[200:203], a[128:131], v[136:139]\n" \
"ds_read_b64_tr_b16 a[196:197], v33 offset:36992\n" \
"ds_read_b64_tr_b16 a[198:199], v33 offset:41600\n" \
"v_mfma_f32_16x16x32_bf16 v[136:139], a[204:207], a[132:135], v[136:139]\n" \
"s_add_u32 s68, s63, 0x3000\n" \
"v_mfma_f32_16x16x32_bf16 v[136:139], a[208:211], a[136:139], v[136:139]\n" \
"v_mfma_f32_16x16x32_bf16 v[136:139], a[212:215], a[140:143], v[136:139]\n" \
"v_mfma_f32_16x16x32_bf16 v[140:143], a[212:215], a[156:159], v[140:143]\n" \
"v_mfma_f32_16x16x32_bf16 v[140:143], a[208:211], a[152:155], v[140:143]\n" \
"v_mfma_f32_16x16x32_bf16 v[140:143], a[204:207], a[148:151], v[140:143]\n" \
"v_mfma_f32_16x16x32_bf16 v[140:143], a[200:203], a[144:147], v[140:143]\n" \
"v_mfma_f32_16x16x32_bf16 v[208:211], a[200:203], a[160:163], v[208:211]\n" \
"v_mfma_f32_16x16x32_bf16 v[208:211], a[204:207], a[164:167], v[208:211]\n" \
"v_mfma_f32_16x16x32_bf16 v[208:211], a[208:211], a[168:171], v[208:211]\n" \
"v_mfma_f32_16x16x32_bf16 v[208:211], a[212:215], a[172:175], v[208:211]\n" \
"v_mfma_f32_16x16x32_bf16 v[212:215], a[212:215], a[188:191], v[212:215]\n" \
"ds_read_b64_tr_b16 a[212:213], v34 offset:18560\n" \
"ds_read_b64_tr_b16 a[214:215], v34 offset:23168\n" \
"v_mfma_f32_16x16x32_bf16 v[212:215], a[208:211], a[184:187], v[212:215]\n" \
"ds_read_b64_tr_b16 a[208:209], v34 offset:9344\n" \
"ds_read_b64_tr_b16 a[210:211], v34 offset:13952\n" \
"v_mfma_f32_16x16x32_bf16 v[212:215], a[204:207], a[180:183], v[212:215]\n" \
"ds_read_b64_tr_b16 a[204:205], v34 offset:128\n" \
"ds_read_b64_tr_b16 a[206:207], v34 offset:4736\n" \
"v_mfma_f32_16x16x32_bf16 v[212:215], a[200:203], a[176:179], v[212:215]\n" \
"ds_read_b64_tr_b16 a[200:201], v33 offset:46208\n" \
"ds_read_b64_tr_b16 a[202:203], v33 offset:50816\n" \
"v_mfma_f32_16x16x32_bf16 v[136:139], a[216:219], a[96:99], v[136:139]\n" \
"v_mfma_f32_16x16x32_bf16 v[136:139], a[0:3], a[100:103], v[136:139]\n" \
"v_mfma_f32_16x16x32_bf16 v[136:139], a[4:7], a[104:107], v[136:139]\n" \
"s_waitcnt lgkmcnt(8)\n" \
"v_mfma_f32_16x16x32_bf16 v[136:139], a[8:11], a[108:111], v[136:139]\n" \
"v_mfma_f32_16x16x32_bf16 v[140:143], a[8:11], a[124:127], v[140:143]\n" \
"v_mfma_f32_16x16x32_bf16 v[140:143], a[4:7], a[120:123], v[140:143]\n" \
"v_mfma_f32_16x16x32_bf16 v[140:143], a[0:3], a[116:119], v[140:143]\n" \
"v_mfma_f32_16x16x32_bf16 v[140:143], a[216:219], a[112:115], v[140:143]\n" \
"v_mfma_f32_16x16x32_bf16 v[208:211], a[216:219], a[220:223], v[208:211]\n" \
"v_mfma_f32_16x16x32_bf16 v[208:211], a[0:3], a[224:227], v[208:211]\n" \
"v_mfma_f32_16x16x32_bf16 v[208:211], a[4:7], a[228:231], v[208:211]\n" \
"v_mfma_f32_16x16x32_bf16 v[208:211], a[8:11], a[232:235], v[208:211]\n" \
"v_cvt_pk_bf16_f32 v136, v136, v137\n" \
"v_cvt_pk_bf16_f32 v137, v138, v139\n" \
"v_mfma_f32_16x16x32_bf16 v[212:215], a[8:11], a[248:251], v[212:215]\n" \
"ds_read_b64_tr_b16 a[8:9], v10 offset:46240\n" \
"v_cvt_pk_bf16_f32 v138, v140, v141\n" \
"v_cvt_pk_bf16_f32 v139, v142, v143\n" \
"v_mfma_f32_16x16x32_bf16 v[212:215], a[4:7], a[244:247], v[212:215]\n" \
"buffer_store_dwordx4 v[136:139], v20, s[56:59], s70 offen offset:0 sc1\n" \
"v_cvt_pk_bf16_f32 v208, v208, v209\n" \
"v_cvt_pk_bf16_f32 v209, v210, v211\n" \
"v_mfma_f32_16x16x32_bf16 v[212:215], a[0:3], a[240:243], v[212:215]\n" \
"ds_read_b64_tr_b16 a[0:1], v10 offset:27808\n" \
"ds_read_b64_tr_b16 a[2:3], v10 offset:32416\n" \
"v_mfma_f32_16x16x32_bf16 v[212:215], a[216:219], a[236:239], v[212:215]\n" \
"ds_read_b64_tr_b16 a[216:217], v33 offset:128\n" \
"ds_read_b64_tr_b16 a[218:219], v33 offset:4736\n" \
"v_mfma_f32_16x16x32_bf16 v[216:219], a[12:15], a[32:35], v[216:219]\n" \
"ds_read_b64_tr_b16 a[4:5], v10 offset:37024\n" \
"ds_read_b64_tr_b16 a[6:7], v10 offset:41632\n" \
"v_mfma_f32_16x16x32_bf16 v[216:219], a[16:19], a[36:39], v[216:219]\n" \
"ds_read_b64_tr_b16 a[10:11], v10 offset:50848\n" \
"v_mfma_f32_16x16x32_bf16 v[216:219], a[20:23], a[40:43], v[216:219]\n" \
"v_cvt_pk_bf16_f32 v210, v212, v213\n" \
"v_cvt_pk_bf16_f32 v211, v214, v215\n" \
"v_mfma_f32_16x16x32_bf16 v[216:219], a[24:27], a[44:47], v[216:219]\n" \
"buffer_store_dwordx4 v[208:211], v20, s[56:59], s70 offen offset:1024 sc1\n" \
"s_add_u32 s70, s70, 0x2000\n" \
"v_mfma_f32_16x16x32_bf16 v[220:223], a[24:27], a[60:63], v[220:223]\n" \
"ds_read_b64_tr_b16 a[24:25], v33 offset:37024\n" \
"ds_read_b64_tr_b16 a[26:27], v33 offset:41632\n" \
"v_mfma_f32_16x16x32_bf16 v[220:223], a[20:23], a[56:59], v[220:223]\n" \
"ds_read_b64_tr_b16 a[20:21], v33 offset:27808\n" \
"ds_read_b64_tr_b16 a[22:23], v33 offset:32416\n" \
"v_mfma_f32_16x16x32_bf16 v[220:223], a[16:19], a[52:55], v[220:223]\n" \
"ds_read_b64_tr_b16 a[16:17], v33 offset:18592\n" \
"ds_read_b64_tr_b16 a[18:19], v33 offset:23200\n" \
"v_mfma_f32_16x16x32_bf16 v[220:223], a[12:15], a[48:51], v[220:223]\n" \
"ds_read_b64_tr_b16 a[12:13], v10 offset:55456\n" \
"ds_read_b64_tr_b16 a[14:15], v10 offset:60064\n" \
"v_mfma_f32_16x16x32_bf16 v[144:147], a[28:31], a[128:131], v[144:147]\n" \
"v_mfma_f32_16x16x32_bf16 v[144:147], a[192:195], a[132:135], v[144:147]\n" \
"v_mfma_f32_16x16x32_bf16 v[144:147], a[196:199], a[136:139], v[144:147]\n" \
"s_waitcnt lgkmcnt(15)\n" \
"v_mfma_f32_16x16x32_bf16 v[144:147], a[200:203], a[140:143], v[144:147]\n" \
"v_mfma_f32_16x16x32_bf16 v[148:151], a[200:203], a[156:159], v[148:151]\n" \
"v_mfma_f32_16x16x32_bf16 v[148:151], a[196:199], a[152:155], v[148:151]\n" \
"v_mfma_f32_16x16x32_bf16 v[148:151], a[192:195], a[148:151], v[148:151]\n" \
"v_mfma_f32_16x16x32_bf16 v[148:151], a[28:31], a[144:147], v[148:151]\n" \
"v_mfma_f32_16x16x32_bf16 v[216:219], a[28:31], a[160:163], v[216:219]\n" \
"v_mfma_f32_16x16x32_bf16 v[216:219], a[192:195], a[164:167], v[216:219]\n" \
"v_mfma_f32_16x16x32_bf16 v[216:219], a[196:199], a[168:171], v[216:219]\n" \
"v_mfma_f32_16x16x32_bf16 v[216:219], a[200:203], a[172:175], v[216:219]\n" \
"v_mfma_f32_16x16x32_bf16 v[220:223], a[200:203], a[188:191], v[220:223]\n" \
"ds_read_b64_tr_b16 a[200:201], v34 offset:18592\n" \
"ds_read_b64_tr_b16 a[202:203], v34 offset:23200\n" \
"v_mfma_f32_16x16x32_bf16 v[220:223], a[196:199], a[184:187], v[220:223]\n" \
"ds_read_b64_tr_b16 a[196:197], v34 offset:9376\n" \
"ds_read_b64_tr_b16 a[198:199], v34 offset:13984\n" \
"v_mfma_f32_16x16x32_bf16 v[220:223], a[192:195], a[180:183], v[220:223]\n" \
"ds_read_b64_tr_b16 a[192:193], v34 offset:160\n" \
"ds_read_b64_tr_b16 a[194:195], v34 offset:4768\n" \
"v_mfma_f32_16x16x32_bf16 v[220:223], a[28:31], a[176:179], v[220:223]\n" \
"ds_read_b64_tr_b16 a[28:29], v33 offset:46240\n" \
"ds_read_b64_tr_b16 a[30:31], v33 offset:50848\n" \
"v_mfma_f32_16x16x32_bf16 v[144:147], a[204:207], a[96:99], v[144:147]\n" \
"v_mfma_f32_16x16x32_bf16 v[144:147], a[208:211], a[100:103], v[144:147]\n" \
"v_mfma_f32_16x16x32_bf16 v[144:147], a[212:215], a[104:107], v[144:147]\n" \
"s_waitcnt lgkmcnt(8)\n" \
"v_mfma_f32_16x16x32_bf16 v[144:147], a[216:219], a[108:111], v[144:147]\n" \
"v_mfma_f32_16x16x32_bf16 v[148:151], a[216:219], a[124:127], v[148:151]\n" \
"v_mfma_f32_16x16x32_bf16 v[148:151], a[212:215], a[120:123], v[148:151]\n" \
"v_mfma_f32_16x16x32_bf16 v[148:151], a[208:211], a[116:119], v[148:151]\n" \
"v_mfma_f32_16x16x32_bf16 v[148:151], a[204:207], a[112:115], v[148:151]\n" \
"v_mfma_f32_16x16x32_bf16 v[216:219], a[204:207], a[220:223], v[216:219]\n" \
"v_mfma_f32_16x16x32_bf16 v[216:219], a[208:211], a[224:227], v[216:219]\n" \
"v_mfma_f32_16x16x32_bf16 v[216:219], a[212:215], a[228:231], v[216:219]\n" \
"v_mfma_f32_16x16x32_bf16 v[216:219], a[216:219], a[232:235], v[216:219]\n" \
"v_cvt_pk_bf16_f32 v144, v144, v145\n" \
"v_cvt_pk_bf16_f32 v145, v146, v147\n" \
"v_mfma_f32_16x16x32_bf16 v[220:223], a[216:219], a[248:251], v[220:223]\n" \
"ds_read_b64_tr_b16 a[216:217], v10 offset:46272\n" \
"v_cvt_pk_bf16_f32 v146, v148, v149\n" \
"v_cvt_pk_bf16_f32 v147, v150, v151\n" \
"v_mfma_f32_16x16x32_bf16 v[220:223], a[212:215], a[244:247], v[220:223]\n" \
"buffer_store_dwordx4 v[144:147], v20, s[56:59], s71 offen offset:0 sc1\n" \
"v_cvt_pk_bf16_f32 v216, v216, v217\n" \
"v_cvt_pk_bf16_f32 v217, v218, v219\n" \
"v_mfma_f32_16x16x32_bf16 v[220:223], a[208:211], a[240:243], v[220:223]\n" \
"ds_read_b64_tr_b16 a[208:209], v10 offset:27840\n" \
"ds_read_b64_tr_b16 a[210:211], v10 offset:32448\n" \
"v_mfma_f32_16x16x32_bf16 v[220:223], a[204:207], a[236:239], v[220:223]\n" \
"ds_read_b64_tr_b16 a[204:205], v33 offset:160\n" \
"ds_read_b64_tr_b16 a[206:207], v33 offset:4768\n" \
"v_mfma_f32_16x16x32_bf16 v[224:227], a[0:3], a[32:35], v[224:227]\n" \
"ds_read_b64_tr_b16 a[212:213], v10 offset:37056\n" \
"ds_read_b64_tr_b16 a[214:215], v10 offset:41664\n" \
"v_mfma_f32_16x16x32_bf16 v[224:227], a[4:7], a[36:39], v[224:227]\n" \
"ds_read_b64_tr_b16 a[218:219], v10 offset:50880\n" \
"v_mfma_f32_16x16x32_bf16 v[224:227], a[8:11], a[40:43], v[224:227]\n" \
"v_cvt_pk_bf16_f32 v218, v220, v221\n" \
"v_cvt_pk_bf16_f32 v219, v222, v223\n" \
"v_mfma_f32_16x16x32_bf16 v[224:227], a[12:15], a[44:47], v[224:227]\n" \
"buffer_store_dwordx4 v[216:219], v20, s[56:59], s71 offen offset:1024 sc1\n" \
"s_add_u32 s71, s71, 0x1800\n" \
"v_mfma_f32_16x16x32_bf16 v[228:231], a[12:15], a[60:63], v[228:231]\n" \
"ds_read_b64_tr_b16 a[12:13], v33 offset:37056\n" \
"ds_read_b64_tr_b16 a[14:15], v33 offset:41664\n" \
"v_mfma_f32_16x16x32_bf16 v[228:231], a[8:11], a[56:59], v[228:231]\n" \
"ds_read_b64_tr_b16 a[8:9], v33 offset:27840\n" \
"ds_read_b64_tr_b16 a[10:11], v33 offset:32448\n" \
"v_mfma_f32_16x16x32_bf16 v[228:231], a[4:7], a[52:55], v[228:231]\n" \
"ds_read_b64_tr_b16 a[4:5], v33 offset:18624\n" \
"ds_read_b64_tr_b16 a[6:7], v33 offset:23232\n" \
"v_mfma_f32_16x16x32_bf16 v[228:231], a[0:3], a[48:51], v[228:231]\n" \
"ds_read_b64_tr_b16 a[0:1], v10 offset:55488\n" \
"ds_read_b64_tr_b16 a[2:3], v10 offset:60096\n" \
"v_mfma_f32_16x16x32_bf16 v[152:155], a[16:19], a[128:131], v[152:155]\n" \
"v_mfma_f32_16x16x32_bf16 v[152:155], a[20:23], a[132:135], v[152:155]\n" \
"v_mfma_f32_16x16x32_bf16 v[152:155], a[24:27], a[136:139], v[152:155]\n" \
"s_waitcnt lgkmcnt(15)\n" \
"v_mfma_f32_16x16x32_bf16 v[152:155], a[28:31], a[140:143], v[152:155]\n" \
"v_mfma_f32_16x16x32_bf16 v[156:159], a[28:31], a[156:159], v[156:159]\n" \
"v_mfma_f32_16x16x32_bf16 v[156:159], a[24:27], a[152:155], v[156:159]\n" \
"v_mfma_f32_16x16x32_bf16 v[156:159], a[20:23], a[148:151], v[156:159]\n" \
"v_mfma_f32_16x16x32_bf16 v[156:159], a[16:19], a[144:147], v[156:159]\n" \
"v_mfma_f32_16x16x32_bf16 v[224:227], a[16:19], a[160:163], v[224:227]\n" \
"v_mfma_f32_16x16x32_bf16 v[224:227], a[20:23], a[164:167], v[224:227]\n" \
"s_waitcnt vmcnt(4)\n" \
"s_mov_b64 exec, 3\n" \
"buffer_store_dword v39, v41, s[56:59], s69 offen sc1\n" \
"s_mov_b64 exec, -1\n" \
"v_mfma_f32_16x16x32_bf16 v[224:227], a[24:27], a[168:171], v[224:227]\n" \
"s_add_u32 s69, s63, 0x0\n" \
"v_mfma_f32_16x16x32_bf16 v[224:227], a[28:31], a[172:175], v[224:227]\n" \
"v_mfma_f32_16x16x32_bf16 v[228:231], a[28:31], a[188:191], v[228:231]\n" \
"ds_read_b64_tr_b16 a[28:29], v34 offset:18624\n" \
"ds_read_b64_tr_b16 a[30:31], v34 offset:23232\n" \
"v_mfma_f32_16x16x32_bf16 v[228:231], a[24:27], a[184:187], v[228:231]\n" \
"ds_read_b64_tr_b16 a[24:25], v34 offset:9408\n" \
"ds_read_b64_tr_b16 a[26:27], v34 offset:14016\n" \
"v_mfma_f32_16x16x32_bf16 v[228:231], a[20:23], a[180:183], v[228:231]\n" \
"ds_read_b64_tr_b16 a[20:21], v34 offset:192\n" \
"ds_read_b64_tr_b16 a[22:23], v34 offset:4800\n" \
"v_mfma_f32_16x16x32_bf16 v[228:231], a[16:19], a[176:179], v[228:231]\n" \
"ds_read_b64_tr_b16 a[16:17], v33 offset:46272\n" \
"ds_read_b64_tr_b16 a[18:19], v33 offset:50880\n" \
"v_mfma_f32_16x16x32_bf16 v[152:155], a[192:195], a[96:99], v[152:155]\n" \
"v_mfma_f32_16x16x32_bf16 v[152:155], a[196:199], a[100:103], v[152:155]\n" \
"v_mfma_f32_16x16x32_bf16 v[152:155], a[200:203], a[104:107], v[152:155]\n" \
"s_waitcnt lgkmcnt(8)\n" \
"v_mfma_f32_16x16x32_bf16 v[152:155], a[204:207], a[108:111], v[152:155]\n" \
"v_mfma_f32_16x16x32_bf16 v[156:159], a[204:207], a[124:127], v[156:159]\n" \
"v_mfma_f32_16x16x32_bf16 v[156:159], a[200:203], a[120:123], v[156:159]\n" \
"v_mfma_f32_16x16x32_bf16 v[156:159], a[196:199], a[116:119], v[156:159]\n" \
"v_mfma_f32_16x16x32_bf16 v[156:159], a[192:195], a[112:115], v[156:159]\n" \
"v_mfma_f32_16x16x32_bf16 v[224:227], a[192:195], a[220:223], v[224:227]\n" \
"v_mfma_f32_16x16x32_bf16 v[224:227], a[196:199], a[224:227], v[224:227]\n" \
"v_mfma_f32_16x16x32_bf16 v[224:227], a[200:203], a[228:231], v[224:227]\n" \
"v_mfma_f32_16x16x32_bf16 v[224:227], a[204:207], a[232:235], v[224:227]\n" \
"v_cvt_pk_bf16_f32 v152, v152, v153\n" \
"v_cvt_pk_bf16_f32 v153, v154, v155\n" \
"v_mfma_f32_16x16x32_bf16 v[228:231], a[204:207], a[248:251], v[228:231]\n" \
"ds_read_b64_tr_b16 a[204:205], v10 offset:46080\n" \
"v_cvt_pk_bf16_f32 v154, v156, v157\n" \
"v_cvt_pk_bf16_f32 v155, v158, v159\n" \
"v_mfma_f32_16x16x32_bf16 v[228:231], a[200:203], a[244:247], v[228:231]\n" \
"buffer_store_dwordx4 v[152:155], v20, s[56:59], s72 offen offset:0 sc1\n" \
"v_cvt_pk_bf16_f32 v224, v224, v225\n" \
"v_cvt_pk_bf16_f32 v225, v226, v227\n" \
"v_mfma_f32_16x16x32_bf16 v[228:231], a[196:199], a[240:243], v[228:231]\n" \
"ds_read_b64_tr_b16 a[196:197], v10 offset:27648\n" \
"ds_read_b64_tr_b16 a[198:199], v10 offset:32256\n" \
"v_mfma_f32_16x16x32_bf16 v[228:231], a[192:195], a[236:239], v[228:231]\n" \
"ds_read_b64_tr_b16 a[192:193], v33 offset:192\n" \
"ds_read_b64_tr_b16 a[194:195], v33 offset:4800\n" \
"v_mfma_f32_16x16x32_bf16 v[232:235], a[208:211], a[32:35], v[232:235]\n" \
"ds_read_b64_tr_b16 a[200:201], v10 offset:36864\n" \
"ds_read_b64_tr_b16 a[202:203], v10 offset:41472\n" \
"v_mfma_f32_16x16x32_bf16 v[232:235], a[212:215], a[36:39], v[232:235]\n" \
"ds_read_b64_tr_b16 a[206:207], v10 offset:50688\n" \
"v_mfma_f32_16x16x32_bf16 v[232:235], a[216:219], a[40:43], v[232:235]\n" \
"v_cvt_pk_bf16_f32 v226, v228, v229\n" \
"v_cvt_pk_bf16_f32 v227, v230, v231\n" \
"v_mfma_f32_16x16x32_bf16 v[232:235], a[0:3], a[44:47], v[232:235]\n" \
"buffer_store_dwordx4 v[224:227], v20, s[56:59], s72 offen offset:1024 sc1\n" \
"s_add_u32 s72, s72, 0x1000\n" \
"v_mfma_f32_16x16x32_bf16 v[236:239], a[0:3], a[60:63], v[236:239]\n" \
"ds_read_b64_tr_b16 a[0:1], v33 offset:36864\n" \
"ds_read_b64_tr_b16 a[2:3], v33 offset:41472\n" \
"v_mfma_f32_16x16x32_bf16 v[236:239], a[216:219], a[56:59], v[236:239]\n" \
"ds_read_b64_tr_b16 a[216:217], v33 offset:27648\n" \
"ds_read_b64_tr_b16 a[218:219], v33 offset:32256\n" \
"v_mfma_f32_16x16x32_bf16 v[236:239], a[212:215], a[52:55], v[236:239]\n" \
"ds_read_b64_tr_b16 a[212:213], v33 offset:18432\n" \
"ds_read_b64_tr_b16 a[214:215], v33 offset:23040\n" \
"v_mfma_f32_16x16x32_bf16 v[236:239], a[208:211], a[48:51], v[236:239]\n" \
"ds_read_b64_tr_b16 a[208:209], v10 offset:55296\n" \
"ds_read_b64_tr_b16 a[210:211], v10 offset:59904\n" \
"v_mfma_f32_16x16x32_bf16 v[160:163], a[4:7], a[128:131], v[160:163]\n" \
"v_mfma_f32_16x16x32_bf16 v[160:163], a[8:11], a[132:135], v[160:163]\n" \
"v_mfma_f32_16x16x32_bf16 v[160:163], a[12:15], a[136:139], v[160:163]\n" \
"s_waitcnt lgkmcnt(15)\n" \
"v_mfma_f32_16x16x32_bf16 v[160:163], a[16:19], a[140:143], v[160:163]\n" \
"v_mfma_f32_16x16x32_bf16 v[164:167], a[16:19], a[156:159], v[164:167]\n" \
"v_mfma_f32_16x16x32_bf16 v[164:167], a[12:15], a[152:155], v[164:167]\n" \
"v_mfma_f32_16x16x32_bf16 v[164:167], a[8:11], a[148:151], v[164:167]\n" \
"v_mfma_f32_16x16x32_bf16 v[164:167], a[4:7], a[144:147], v[164:167]\n" \
"s_waitcnt vmcnt(5)\n" \
"s_mov_b64 exec, 1\n" \
"buffer_store_dword v39, off, s[56:59], s70 offset:16 sc1\n" \
"s_mov_b64 exec, -1\n" \
"v_mfma_f32_16x16x32_bf16 v[232:235], a[4:7], a[160:163], v[232:235]\n" \
"s_add_u32 s70, s63, 0x800\n" \
"v_mfma_f32_16x16x32_bf16 v[232:235], a[8:11], a[164:167], v[232:235]\n" \
"v_mfma_f32_16x16x32_bf16 v[232:235], a[12:15], a[168:171], v[232:235]\n" \
"v_mfma_f32_16x16x32_bf16 v[232:235], a[16:19], a[172:175], v[232:235]\n" \
"v_mfma_f32_16x16x32_bf16 v[236:239], a[16:19], a[188:191], v[236:239]\n" \
"ds_read_b64_tr_b16 a[16:17], v34 offset:18432\n" \
"ds_read_b64_tr_b16 a[18:19], v34 offset:23040\n" \
"v_mfma_f32_16x16x32_bf16 v[236:239], a[12:15], a[184:187], v[236:239]\n" \
"ds_read_b64_tr_b16 a[12:13], v34 offset:9216\n" \
"ds_read_b64_tr_b16 a[14:15], v34 offset:13824\n" \
"v_mfma_f32_16x16x32_bf16 v[236:239], a[8:11], a[180:183], v[236:239]\n" \
"ds_read_b64_tr_b16 a[8:9], v34 offset:0\n" \
"ds_read_b64_tr_b16 a[10:11], v34 offset:4608\n" \
"v_mfma_f32_16x16x32_bf16 v[236:239], a[4:7], a[176:179], v[236:239]\n" \
"ds_read_b64_tr_b16 a[4:5], v33 offset:46080\n" \
"ds_read_b64_tr_b16 a[6:7], v33 offset:50688\n" \
"v_mfma_f32_16x16x32_bf16 v[160:163], a[20:23], a[96:99], v[160:163]\n" \
"v_mfma_f32_16x16x32_bf16 v[160:163], a[24:27], a[100:103], v[160:163]\n" \
"v_mfma_f32_16x16x32_bf16 v[160:163], a[28:31], a[104:107], v[160:163]\n" \
"s_waitcnt lgkmcnt(8)\n" \
"v_mfma_f32_16x16x32_bf16 v[160:163], a[192:195], a[108:111], v[160:163]\n" \
"v_mfma_f32_16x16x32_bf16 v[164:167], a[192:195], a[124:127], v[164:167]\n" \
"v_mfma_f32_16x16x32_bf16 v[164:167], a[28:31], a[120:123], v[164:167]\n" \
"v_mfma_f32_16x16x32_bf16 v[164:167], a[24:27], a[116:119], v[164:167]\n" \
"v_mfma_f32_16x16x32_bf16 v[164:167], a[20:23], a[112:115], v[164:167]\n" \
"v_mfma_f32_16x16x32_bf16 v[232:235], a[20:23], a[220:223], v[232:235]\n" \
"v_mfma_f32_16x16x32_bf16 v[232:235], a[24:27], a[224:227], v[232:235]\n" \
"v_mfma_f32_16x16x32_bf16 v[232:235], a[28:31], a[228:231], v[232:235]\n" \
"v_mfma_f32_16x16x32_bf16 v[232:235], a[192:195], a[232:235], v[232:235]\n" \
"v_cvt_pk_bf16_f32 v160, v160, v161\n" \
"v_cvt_pk_bf16_f32 v161, v162, v163\n" \
"v_mfma_f32_16x16x32_bf16 v[236:239], a[192:195], a[248:251], v[236:239]\n" \
"ds_read_b64_tr_b16 a[192:193], v10 offset:46112\n" \
"v_cvt_pk_bf16_f32 v162, v164, v165\n" \
"v_cvt_pk_bf16_f32 v163, v166, v167\n" \
"v_mfma_f32_16x16x32_bf16 v[236:239], a[28:31], a[244:247], v[236:239]\n" \
"buffer_store_dwordx4 v[160:163], v20, s[56:59], s73 offen offset:0 sc1\n" \
"v_cvt_pk_bf16_f32 v232, v232, v233\n" \
"v_cvt_pk_bf16_f32 v233, v234, v235\n" \
"v_mfma_f32_16x16x32_bf16 v[236:239], a[24:27], a[240:243], v[236:239]\n" \
"ds_read_b64_tr_b16 a[24:25], v10 offset:27680\n" \
"ds_read_b64_tr_b16 a[26:27], v10 offset:32288\n" \
"v_mfma_f32_16x16x32_bf16 v[236:239], a[20:23], a[236:239], v[236:239]\n" \
"ds_read_b64_tr_b16 a[20:21], v33 offset:0\n" \
"ds_read_b64_tr_b16 a[22:23], v33 offset:4608\n" \
"v_mfma_f32_16x16x32_bf16 v[184:187], a[196:199], a[32:35], v[184:187]\n" \
"ds_read_b64_tr_b16 a[28:29], v10 offset:36896\n" \
"ds_read_b64_tr_b16 a[30:31], v10 offset:41504\n" \
"v_mfma_f32_16x16x32_bf16 v[184:187], a[200:203], a[36:39], v[184:187]\n" \
"ds_read_b64_tr_b16 a[194:195], v10 offset:50720\n" \
"v_mfma_f32_16x16x32_bf16 v[184:187], a[204:207], a[40:43], v[184:187]\n" \
"v_cvt_pk_bf16_f32 v234, v236, v237\n" \
"v_cvt_pk_bf16_f32 v235, v238, v239\n" \
"v_mfma_f32_16x16x32_bf16 v[184:187], a[208:211], a[44:47], v[184:187]\n" \
"buffer_store_dwordx4 v[232:235], v20, s[56:59], s73 offen offset:1024 sc1\n" \
"s_add_u32 s73, s73, 0x800\n" \
"v_mfma_f32_16x16x32_bf16 v[188:191], a[208:211], a[60:63], v[188:191]\n" \
"ds_read_b64_tr_b16 a[208:209], v33 offset:36896\n" \
"ds_read_b64_tr_b16 a[210:211], v33 offset:41504\n" \
"v_mfma_f32_16x16x32_bf16 v[188:191], a[204:207], a[56:59], v[188:191]\n" \
"ds_read_b64_tr_b16 a[204:205], v33 offset:27680\n" \
"ds_read_b64_tr_b16 a[206:207], v33 offset:32288\n" \
"v_mfma_f32_16x16x32_bf16 v[188:191], a[200:203], a[52:55], v[188:191]\n" \
"ds_read_b64_tr_b16 a[200:201], v33 offset:18464\n" \
"ds_read_b64_tr_b16 a[202:203], v33 offset:23072\n" \
"v_mfma_f32_16x16x32_bf16 v[188:191], a[196:199], a[48:51], v[188:191]\n" \
"ds_read_b64_tr_b16 a[196:197], v10 offset:55328\n" \
"ds_read_b64_tr_b16 a[198:199], v10 offset:59936\n" \
"v_mfma_f32_16x16x32_bf16 v[184:187], a[212:215], a[160:163], v[184:187]\n" \
"v_mfma_f32_16x16x32_bf16 v[184:187], a[216:219], a[164:167], v[184:187]\n" \
"v_mfma_f32_16x16x32_bf16 v[184:187], a[0:3], a[168:171], v[184:187]\n" \
"s_waitcnt lgkmcnt(15)\n" \
"v_mfma_f32_16x16x32_bf16 v[184:187], a[4:7], a[172:175], v[184:187]\n" \
"v_mfma_f32_16x16x32_bf16 v[188:191], a[4:7], a[188:191], v[188:191]\n" \
"ds_read_b64_tr_b16 a[4:5], v34 offset:18464\n" \
"ds_read_b64_tr_b16 a[6:7], v34 offset:23072\n" \
"v_mfma_f32_16x16x32_bf16 v[188:191], a[0:3], a[184:187], v[188:191]\n" \
"ds_read_b64_tr_b16 a[0:1], v34 offset:9248\n" \
"ds_read_b64_tr_b16 a[2:3], v34 offset:13856\n" \
"s_waitcnt vmcnt(6)\n" \
"s_mov_b64 exec, 1\n" \
"buffer_store_dword v39, off, s[56:59], s71 offset:20 sc1\n" \
"s_mov_b64 exec, -1\n" \
"v_mfma_f32_16x16x32_bf16 v[188:191], a[216:219], a[180:183], v[188:191]\n" \
"ds_read_b64_tr_b16 a[216:217], v34 offset:32\n" \
"ds_read_b64_tr_b16 a[218:219], v34 offset:4640\n" \
"v_mfma_f32_16x16x32_bf16 v[188:191], a[212:215], a[176:179], v[188:191]\n" \
"ds_read_b64_tr_b16 a[212:213], v33 offset:46112\n" \
"ds_read_b64_tr_b16 a[214:215], v33 offset:50720\n" \
"v_mfma_f32_16x16x32_bf16 v[184:187], a[8:11], a[220:223], v[184:187]\n" \
"s_add_u32 s71, s63, 0x1000\n" \
"v_mfma_f32_16x16x32_bf16 v[184:187], a[12:15], a[224:227], v[184:187]\n" \
"v_mfma_f32_16x16x32_bf16 v[184:187], a[16:19], a[228:231], v[184:187]\n" \
"s_waitcnt lgkmcnt(12)\n" \
"v_mfma_f32_16x16x32_bf16 v[184:187], a[20:23], a[232:235], v[184:187]\n" \
"v_mfma_f32_16x16x32_bf16 v[188:191], a[20:23], a[248:251], v[188:191]\n" \
"ds_read_b64_tr_b16 a[20:21], v10 offset:46144\n" \
"ds_read_b64_tr_b16 a[22:23], v10 offset:50752\n" \
"v_mfma_f32_16x16x32_bf16 v[188:191], a[16:19], a[244:247], v[188:191]\n" \
"ds_read_b64_tr_b16 a[16:17], v10 offset:36928\n" \
"ds_read_b64_tr_b16 a[18:19], v10 offset:41536\n" \
"v_mfma_f32_16x16x32_bf16 v[188:191], a[12:15], a[240:243], v[188:191]\n" \
"ds_read_b64_tr_b16 a[12:13], v10 offset:27712\n" \
"v_cvt_pk_bf16_f32 v184, v184, v185\n" \
"v_cvt_pk_bf16_f32 v185, v186, v187\n" \
"v_mfma_f32_16x16x32_bf16 v[188:191], a[8:11], a[236:239], v[188:191]\n" \
"ds_read_b64_tr_b16 a[8:9], v33 offset:32\n" \
"ds_read_b64_tr_b16 a[10:11], v33 offset:4640\n" \
"v_mfma_f32_16x16x32_bf16 v[192:195], a[24:27], a[32:35], v[192:195]\n" \
"ds_read_b64_tr_b16 a[14:15], v10 offset:32320\n" \
"v_mfma_f32_16x16x32_bf16 v[192:195], a[28:31], a[36:39], v[192:195]\n" \
"v_mfma_f32_16x16x32_bf16 v[192:195], a[192:195], a[40:43], v[192:195]\n" \
"s_waitcnt lgkmcnt(12)\n" \
"v_mfma_f32_16x16x32_bf16 v[192:195], a[196:199], a[44:47], v[192:195]\n" \
"v_cvt_pk_bf16_f32 v186, v188, v189\n" \
"v_cvt_pk_bf16_f32 v187, v190, v191\n" \
"L_pf22:\n" \
"s_cbranch_execz L_retB\n" \
"s_nop 0\n" \
"v_mfma_f32_16x16x32_bf16 v[196:199], a[196:199], a[60:63], v[196:199]\n" \
"buffer_store_dwordx4 v[184:187], v20, s[56:59], s92 offen offset:1024 sc1\n" \
"s_add_u32 s92, s92, 0x3800\n" \
"v_mfma_f32_16x16x32_bf16 v[196:199], a[192:195], a[56:59], v[196:199]\n" \
"ds_read_b64_tr_b16 a[192:193], v33 offset:27712\n" \
"ds_read_b64_tr_b16 a[194:195], v33 offset:32320\n" \
"v_mfma_f32_16x16x32_bf16 v[196:199], a[28:31], a[52:55], v[196:199]\n" \
"ds_read_b64_tr_b16 a[28:29], v33 offset:18496\n" \
"ds_read_b64_tr_b16 a[30:31], v33 offset:23104\n" \
"v_mfma_f32_16x16x32_bf16 v[196:199], a[24:27], a[48:51], v[196:199]\n" \
"ds_read_b64_tr_b16 a[24:25], v10 offset:55360\n" \
"ds_read_b64_tr_b16 a[26:27], v10 offset:59968\n" \
"v_mfma_f32_16x16x32_bf16 v[192:195], a[200:203], a[160:163], v[192:195]\n" \
"ds_read_b64_tr_b16 a[196:197], v33 offset:36928\n" \
"ds_read_b64_tr_b16 a[198:199], v33 offset:41536\n" \
"v_mfma_f32_16x16x32_bf16 v[192:195], a[204:207], a[164:167], v[192:195]\n" \
"v_mfma_f32_16x16x32_bf16 v[192:195], a[208:211], a[168:171], v[192:195]\n" \
"s_waitcnt lgkmcnt(14)\n" \
"v_mfma_f32_16x16x32_bf16 v[192:195], a[212:215], a[172:175], v[192:195]\n" \
"v_mfma_f32_16x16x32_bf16 v[196:199], a[212:215], a[188:191], v[196:199]\n" \
"ds_read_b64_tr_b16 a[212:213], v34 offset:18496\n" \
"ds_read_b64_tr_b16 a[214:215], v34 offset:23104\n" \
"v_mfma_f32_16x16x32_bf16 v[196:199], a[208:211], a[184:187], v[196:199]\n" \
"ds_read_b64_tr_b16 a[208:209], v34 offset:9280\n" \
"ds_read_b64_tr_b16 a[210:211], v34 offset:13888\n" \
"v_mfma_f32_16x16x32_bf16 v[196:199], a[204:207], a[180:183], v[196:199]\n" \
"ds_read_b64_tr_b16 a[204:205], v34 offset:64\n" \
"ds_read_b64_tr_b16 a[206:207], v34 offset:4672\n" \
"v_mfma_f32_16x16x32_bf16 v[196:199], a[200:203], a[176:179], v[196:199]\n" \
"ds_read_b64_tr_b16 a[200:201], v33 offset:46144\n" \
"ds_read_b64_tr_b16 a[202:203], v33 offset:50752\n" \
"v_mfma_f32_16x16x32_bf16 v[192:195], a[216:219], a[220:223], v[192:195]\n" \
"v_mfma_f32_16x16x32_bf16 v[192:195], a[0:3], a[224:227], v[192:195]\n" \
"v_mfma_f32_16x16x32_bf16 v[192:195], a[4:7], a[228:231], v[192:195]\n" \
"s_waitcnt lgkmcnt(15)\n" \
"v_mfma_f32_16x16x32_bf16 v[192:195], a[8:11], a[232:235], v[192:195]\n" \
"v_mfma_f32_16x16x32_bf16 v[196:199], a[8:11], a[248:251], v[196:199]\n" \
"ds_read_b64_tr_b16 a[8:9], v10 offset:46304\n" \
"ds_read_b64_tr_b16 a[10:11], v10 offset:50912\n" \
"v_mfma_f32_16x16x32_bf16 v[196:199], a[4:7], a[244:247], v[196:199]\n" \
"ds_read_b64_tr_b16 a[4:5], v10 offset:37088\n" \
"ds_read_b64_tr_b16 a[6:7], v10 offset:41696\n" \
"v_mfma_f32_16x16x32_bf16 v[196:199], a[0:3], a[240:243], v[196:199]\n" \
"ds_read_b64_tr_b16 a[0:1], v10 offset:27872\n" \
"v_cvt_pk_bf16_f32 v192, v192, v193\n" \
"v_cvt_pk_bf16_f32 v193, v194, v195\n" \
"v_mfma_f32_16x16x32_bf16 v[196:199], a[216:219], a[236:239], v[196:199]\n" \
"ds_read_b64_tr_b16 a[216:217], v33 offset:64\n" \
"ds_read_b64_tr_b16 a[218:219], v33 offset:4672\n" \
"v_mfma_f32_16x16x32_bf16 v[200:203], a[12:15], a[32:35], v[200:203]\n" \
"ds_read_b64_tr_b16 a[2:3], v10 offset:32480\n" \
"v_mfma_f32_16x16x32_bf16 v[200:203], a[16:19], a[36:39], v[200:203]\n" \
"v_mfma_f32_16x16x32_bf16 v[200:203], a[20:23], a[40:43], v[200:203]\n" \
"s_waitcnt lgkmcnt(14)\n" \
"v_mfma_f32_16x16x32_bf16 v[200:203], a[24:27], a[44:47], v[200:203]\n" \
"v_cvt_pk_bf16_f32 v194, v196, v197\n" \
"v_cvt_pk_bf16_f32 v195, v198, v199\n" \
"v_mfma_f32_16x16x32_bf16 v[204:207], a[24:27], a[60:63], v[204:207]\n" \
"buffer_store_dwordx4 v[192:195], v20, s[56:59], s93 offen offset:1024 sc1\n" \
"s_add_u32 s93, s93, 0x3000\n" \
"v_mfma_f32_16x16x32_bf16 v[204:207], a[20:23], a[56:59], v[204:207]\n" \
"ds_read_b64_tr_b16 a[20:21], v33 offset:27872\n" \
"ds_read_b64_tr_b16 a[22:23], v33 offset:32480\n" \
"s_waitcnt vmcnt(6)\n" \
"s_mov_b64 exec, 1\n" \
"buffer_store_dword v39, off, s[56:59], s72 offset:24 sc1\n" \
"s_mov_b64 exec, -1\n" \
"v_readlane_b32 s72, v11, 50\n" \
"v_mfma_f32_16x16x32_bf16 v[204:207], a[16:19], a[52:55], v[204:207]\n" \
"ds_read_b64_tr_b16 a[16:17], v33 offset:18656\n" \
"ds_read_b64_tr_b16 a[18:19], v33 offset:23264\n" \
"s_mov_b64 exec, 1\n" \
"s_nop 0\n" \
"buffer_store_dword v46, off, s[56:59], s72 offset:0 sc1\n" \
"s_mov_b64 exec, -1\n" \
"v_mfma_f32_16x16x32_bf16 v[204:207], a[12:15], a[48:51], v[204:207]\n" \
"ds_read_b64_tr_b16 a[12:13], v10 offset:55520\n" \
"ds_read_b64_tr_b16 a[14:15], v10 offset:60128\n" \
"v_mfma_f32_16x16x32_bf16 v[200:203], a[28:31], a[160:163], v[200:203]\n" \
"ds_read_b64_tr_b16 a[24:25], v33 offset:37088\n" \
"ds_read_b64_tr_b16 a[26:27], v33 offset:41696\n" \
"v_mfma_f32_16x16x32_bf16 v[200:203], a[192:195], a[164:167], v[200:203]\n" \
"v_mfma_f32_16x16x32_bf16 v[200:203], a[196:199], a[168:171], v[200:203]\n" \
"s_waitcnt lgkmcnt(10)\n" \
"v_mfma_f32_16x16x32_bf16 v[200:203], a[200:203], a[172:175], v[200:203]\n" \
"v_mfma_f32_16x16x32_bf16 v[204:207], a[200:203], a[188:191], v[204:207]\n" \
"ds_read_b64_tr_b16 a[200:201], v34 offset:18656\n" \
"ds_read_b64_tr_b16 a[202:203], v34 offset:23264\n" \
"v_mfma_f32_16x16x32_bf16 v[204:207], a[196:199], a[184:187], v[204:207]\n" \
"ds_read_b64_tr_b16 a[196:197], v34 offset:9440\n" \
"ds_read_b64_tr_b16 a[198:199], v34 offset:14048\n" \
"v_mfma_f32_16x16x32_bf16 v[204:207], a[192:195], a[180:183], v[204:207]\n" \
"ds_read_b64_tr_b16 a[192:193], v34 offset:224\n" \
"ds_read_b64_tr_b16 a[194:195], v34 offset:4832\n" \
"v_mfma_f32_16x16x32_bf16 v[204:207], a[28:31], a[176:179], v[204:207]\n" \
"ds_read_b64_tr_b16 a[28:29], v33 offset:46304\n" \
"ds_read_b64_tr_b16 a[30:31], v33 offset:50912\n" \
"v_mfma_f32_16x16x32_bf16 v[200:203], a[204:207], a[220:223], v[200:203]\n" \
"v_mfma_f32_16x16x32_bf16 v[200:203], a[208:211], a[224:227], v[200:203]\n" \
"v_mfma_f32_16x16x32_bf16 v[200:203], a[212:215], a[228:231], v[200:203]\n" \
"s_waitcnt lgkmcnt(14)\n" \
"v_mfma_f32_16x16x32_bf16 v[200:203], a[216:219], a[232:235], v[200:203]\n" \
"v_mfma_f32_16x16x32_bf16 v[204:207], a[216:219], a[248:251], v[204:207]\n" \
"ds_read_b64_tr_b16 a[216:217], v36 offset:32\n" \
"ds_read_b64_tr_b16 a[218:219], v36 offset:1056\n" \
"v_mfma_f32_16x16x32_bf16 v[204:207], a[212:215], a[244:247], v[204:207]\n" \
"ds_read_b64_tr_b16 a[212:213], v36 offset:2048\n" \
"ds_read_b64_tr_b16 a[214:215], v36 offset:3072\n" \
"v_mfma_f32_16x16x32_bf16 v[204:207], a[208:211], a[240:243], v[204:207]\n" \
"ds_read_b64_tr_b16 a[208:209], v36 offset:0\n" \
"v_cvt_pk_bf16_f32 v200, v200, v201\n" \
"v_cvt_pk_bf16_f32 v201, v202, v203\n" \
"v_mfma_f32_16x16x32_bf16 v[204:207], a[204:207], a[236:239], v[204:207]\n" \
"ds_read_b64_tr_b16 a[204:205], v33 offset:224\n" \
"ds_read_b64_tr_b16 a[206:207], v33 offset:4832\n" \
"v_mfma_f32_16x16x32_bf16 v[240:243], a[0:3], a[32:35], v[240:243]\n" \
"ds_read_b64_tr_b16 a[210:211], v36 offset:1024\n" \
"v_mfma_f32_16x16x32_bf16 v[240:243], a[4:7], a[36:39], v[240:243]\n" \
"v_mfma_f32_16x16x32_bf16 v[240:243], a[8:11], a[40:43], v[240:243]\n" \
"s_waitcnt lgkmcnt(14)\n" \
"v_mfma_f32_16x16x32_bf16 v[240:243], a[12:15], a[44:47], v[240:243]\n" \
"v_cvt_pk_bf16_f32 v202, v204, v205\n" \
"v_cvt_pk_bf16_f32 v203, v206, v207\n" \
"v_mfma_f32_16x16x32_bf16 v[244:247], a[12:15], a[60:63], v[244:247]\n" \
"buffer_store_dwordx4 v[200:203], v20, s[56:59], s94 offen offset:1024 sc1\n" \
"s_add_u32 s94, s94, 0x2800\n" \
"v_mfma_f32_16x16x32_bf16 v[244:247], a[8:11], a[56:59], v[244:247]\n" \
"v_mfma_f32_16x16x32_bf16 v[244:247], a[4:7], a[52:55], v[244:247]\n" \
"v_mfma_f32_16x16x32_bf16 v[244:247], a[0:3], a[48:51], v[244:247]\n" \
"ds_read_b64_tr_b16 a[0:1], v36 offset:2080\n" \
"ds_read_b64_tr_b16 a[2:3], v36 offset:3104\n" \
"v_mfma_f32_16x16x32_bf16 v[168:171], a[16:19], a[128:131], v[168:171]\n" \
"v_mfma_f32_16x16x32_bf16 v[168:171], a[20:23], a[132:135], v[168:171]\n" \
"v_mfma_f32_16x16x32_bf16 v[168:171], a[24:27], a[136:139], v[168:171]\n" \
"s_waitcnt lgkmcnt(8)\n" \
"v_mfma_f32_16x16x32_bf16 v[168:171], a[28:31], a[140:143], v[168:171]\n" \
"v_mfma_f32_16x16x32_bf16 v[172:175], a[28:31], a[156:159], v[172:175]\n" \
"v_mfma_f32_16x16x32_bf16 v[172:175], a[24:27], a[152:155], v[172:175]\n" \
"v_mfma_f32_16x16x32_bf16 v[172:175], a[20:23], a[148:151], v[172:175]\n" \
"v_mfma_f32_16x16x32_bf16 v[172:175], a[16:19], a[144:147], v[172:175]\n" \
"v_mfma_f32_16x16x32_bf16 v[240:243], a[16:19], a[160:163], v[240:243]\n" \
"v_mfma_f32_16x16x32_bf16 v[240:243], a[20:23], a[164:167], v[240:243]\n" \
"v_mfma_f32_16x16x32_bf16 v[240:243], a[24:27], a[168:171], v[240:243]\n" \
"v_mfma_f32_16x16x32_bf16 v[240:243], a[28:31], a[172:175], v[240:243]\n" \
"v_mfma_f32_16x16x32_bf16 v[244:247], a[28:31], a[188:191], v[244:247]\n" \
"s_waitcnt vmcnt(6)\n" \
"s_mov_b64 exec, 1\n" \
"buffer_store_dword v39, off, s[56:59], s73 offset:28 sc1\n" \
"s_mov_b64 exec, -1\n" \
"v_mfma_f32_16x16x32_bf16 v[244:247], a[24:27], a[184:187], v[244:247]\n" \
"v_readlane_b32 s73, v11, 41\n" \
"v_mfma_f32_16x16x32_bf16 v[244:247], a[20:23], a[180:183], v[244:247]\n" \
"v_mfma_f32_16x16x32_bf16 v[244:247], a[16:19], a[176:179], v[244:247]\n" \
"v_mfma_f32_16x16x32_bf16 v[168:171], a[192:195], a[96:99], v[168:171]\n" \
"v_mfma_f32_16x16x32_bf16 v[168:171], a[196:199], a[100:103], v[168:171]\n" \
"v_mfma_f32_16x16x32_bf16 v[168:171], a[200:203], a[104:107], v[168:171]\n" \
"s_waitcnt lgkmcnt(0)\n" \
"v_mfma_f32_16x16x32_bf16 v[168:171], a[204:207], a[108:111], v[168:171]\n" \
"v_mfma_f32_16x16x32_bf16 v[172:175], a[204:207], a[124:127], v[172:175]\n" \
"v_mfma_f32_16x16x32_bf16 v[172:175], a[200:203], a[120:123], v[172:175]\n" \
"v_mfma_f32_16x16x32_bf16 v[172:175], a[196:199], a[116:119], v[172:175]\n" \
"v_mfma_f32_16x16x32_bf16 v[172:175], a[192:195], a[112:115], v[172:175]\n" \
"v_mfma_f32_16x16x32_bf16 v[240:243], a[192:195], a[220:223], v[240:243]\n" \
"v_mfma_f32_16x16x32_bf16 v[240:243], a[196:199], a[224:227], v[240:243]\n" \
"v_mfma_f32_16x16x32_bf16 v[240:243], a[200:203], a[228:231], v[240:243]\n" \
"v_mfma_f32_16x16x32_bf16 v[240:243], a[204:207], a[232:235], v[240:243]\n" \
"v_mfma_f32_16x16x32_bf16 v[244:247], a[204:207], a[248:251], v[244:247]\n" \
"v_mfma_f32_16x16x32_bf16 v[244:247], a[200:203], a[244:247], v[244:247]\n" \
"v_mfma_f32_16x16x32_bf16 v[244:247], a[196:199], a[240:243], v[244:247]\n" \
"v_mfma_f32_16x16x32_bf16 v[244:247], a[192:195], a[236:239], v[244:247]\n" \
"v_mfma_f32_16x16x32_bf16 v[168:171], a[208:211], v[64:67], v[168:171]\n" \
"v_mfma_f32_16x16x32_bf16 v[172:175], a[208:211], v[72:75], v[172:175]\n" \
"v_mfma_f32_16x16x32_bf16 v[240:243], a[208:211], v[80:83], v[240:243]\n" \
"v_mfma_f32_16x16x32_bf16 v[244:247], a[208:211], v[88:91], v[244:247]\n" \
"v_mfma_f32_16x16x32_bf16 v[168:171], a[212:215], v[68:71], v[168:171]\n" \
"v_mfma_f32_16x16x32_bf16 v[172:175], a[212:215], v[76:79], v[172:175]\n" \
"v_mfma_f32_16x16x32_bf16 v[240:243], a[212:215], v[84:87], v[240:243]\n" \
"s_waitcnt vmcnt(5)\n" \
"s_mov_b64 exec, 1\n" \
"buffer_store_dword v39, off, s[56:59], s92 offset:32 sc1\n" \
"s_mov_b64 exec, -1\n" \
"v_mfma_f32_16x16x32_bf16 v[244:247], a[212:215], v[92:95], v[244:247]\n" \
"v_readlane_b32 s92, v11, 39\n" \
"v_mfma_f32_16x16x32_bf16 v[56:59], a[216:219], v[96:99], 0\n" \
"v_mfma_f32_16x16x32_bf16 v[60:63], a[216:219], v[104:107], 0\n" \
"v_mfma_f32_16x16x32_bf16 v[56:59], a[0:3], v[100:103], v[56:59]\n" \
"v_mfma_f32_16x16x32_bf16 v[60:63], a[0:3], v[108:111], v[60:63]\n" \
"s_waitcnt vmcnt(5)\n" \
"s_mov_b64 exec, 1\n" \
"buffer_store_dword v39, off, s[56:59], s93 offset:36 sc1\n" \
"s_mov_b64 exec, -1\n" \
"v_readlane_b32 s93, v11, 40\n" \
"s_waitcnt vmcnt(3)\n" \
"s_mov_b64 exec, 1\n" \
"buffer_store_dword v39, off, s[56:59], s94 offset:40 sc1\n" \
"s_mov_b64 exec, -1\n" \
"buffer_load_dword v37, v40, s[56:59], s74 offen sc1\n" \
"s_waitcnt vmcnt(0)\n" \
"v_cmp_eq_u32_e64 s[98:99], s62, v37\n" \
"s_nop 0\n" \
"s_andn2_b32 s75, 0x1, s98\n" \
"s_cbranch_scc1 L_sx0_%=\n" \
"L_rx0_%=:\n" \
"buffer_load_dwordx4 v[116:119], v20, s[56:59], s95 offen offset:0 sc1\n" \
"s_andn2_b32 s75, 0x2, s98\n" \
"s_cbranch_scc1 L_sx1_%=\n" \
"L_rx1_%=:\n" \
"buffer_load_dwordx4 v[124:127], v20, s[56:59], s96 offen offset:0 sc1\n" \
"s_andn2_b32 s75, 0x4, s98\n" \
"s_cbranch_scc1 L_sx2_%=\n" \
"L_rx2_%=:\n" \
"buffer_load_dwordx4 v[132:135], v20, s[56:59], s97 offen offset:0 sc1\n" \
"s_andn2_b32 s75, s100, s98\n" \
"s_cbranch_scc1 L_sx3_%=\n" \
"L_rx3_%=:\n" \
"buffer_load_dwordx4 v[136:139], v20, s[56:59], s64 offen offset:0 sc1\n" \
"buffer_load_dwordx4 v[208:211], v20, s[56:59], s64 offen offset:2048 sc1\n" \
"s_add_u32 s64, s64, 0x1000\n" \
"buffer_load_dwordx4 v[144:147], v20, s[56:59], s64 offen offset:0 sc1\n" \
"buffer_load_dwordx4 v[216:219], v20, s[56:59], s64 offen offset:2048 sc1\n" \
"s_add_u32 s64, s64, 0x1000\n" \
"buffer_load_dwordx4 v[152:155], v20, s[56:59], s64 offen offset:0 sc1\n" \
"buffer_load_dwordx4 v[224:227], v20, s[56:59], s64 offen offset:2048 sc1\n" \
"s_add_u32 s64, s64, 0x1000\n" \
"buffer_load_dwordx4 v[176:179], v20, s[56:59], s64 offen offset:0 sc1\n" \
"buffer_load_dwordx4 v[248:251], v20, s[56:59], s64 offen offset:2048 sc1\n" \
"s_andn2_b32 s75, 0x10, s98\n" \
"s_cbranch_scc1 L_sx4_%=\n" \
"L_rx4_%=:\n" \
"buffer_load_dwordx4 v[140:143], v20, s[56:59], s65 offen offset:0 sc1\n" \
"buffer_load_dwordx4 v[212:215], v20, s[56:59], s65 offen offset:1024 sc1\n" \
"s_andn2_b32 s75, 0x20, s98\n" \
"s_cbranch_scc1 L_sx5_%=\n" \
"L_rx5_%=:\n" \
"buffer_load_dwordx4 v[148:151], v20, s[56:59], s66 offen offset:0 sc1\n" \
"buffer_load_dwordx4 v[220:223], v20, s[56:59], s66 offen offset:1024 sc1\n" \
"s_andn2_b32 s75, 0x40, s98\n" \
"s_cbranch_scc1 L_sx6_%=\n" \
"L_rx6_%=:\n" \
"buffer_load_dwordx4 v[156:159], v20, s[56:59], s67 offen offset:0 sc1\n" \
"buffer_load_dwordx4 v[228:231], v20, s[56:59], s67 offen offset:1024 sc1\n" \
"s_andn2_b32 s75, 0x80, s98\n" \
"s_cbranch_scc1 L_sx7_%=\n" \
"L_rx7_%=:\n" \
"buffer_load_dwordx4 v[164:167], v20, s[56:59], s68 offen offset:0 sc1\n" \
"buffer_load_dwordx4 v[236:239], v20, s[56:59], s68 offen offset:1024 sc1\n" \
"s_andn2_b32 s75, 0x100, s98\n" \
"s_cbranch_scc1 L_sx8_%=\n" \
"L_rx8_%=:\n" \
"buffer_load_dwordx4 v[188:191], v20, s[56:59], s69 offen offset:1024 sc1\n" \
"s_andn2_b32 s75, 0x200, s98\n" \
"s_cbranch_scc1 L_sx9_%=\n" \
"L_rx9_%=:\n" \
"buffer_load_dwordx4 v[196:199], v20, s[56:59], s70 offen offset:1024 sc1\n" \
"s_andn2_b32 s75, 0x400, s98\n" \
"s_cbranch_scc1 L_sx10_%=\n" \
"L_rx10_%=:\n" \
"buffer_load_dwordx4 v[204:207], v20, s[56:59], s71 offen offset:1024 sc1\n" \
"s_waitcnt vmcnt(21)\n" \
"v_mfma_f32_16x16x32_bf16 v[168:171], v[50:53], v[116:119], v[168:171]\n" \
"v_mfma_f32_16x16x32_bf16 v[172:175], v[48:51], v[116:119], v[172:175]\n" \
"s_waitcnt vmcnt(20)\n" \
"v_mfma_f32_16x16x32_bf16 v[168:171], v[50:53], v[124:127], v[168:171]\n" \
"v_mfma_f32_16x16x32_bf16 v[172:175], v[48:51], v[124:127], v[172:175]\n" \
"s_waitcnt vmcnt(19)\n" \
"v_mfma_f32_16x16x32_bf16 v[168:171], v[50:53], v[132:135], v[168:171]\n" \
"v_mfma_f32_16x16x32_bf16 v[172:175], v[48:51], v[132:135], v[172:175]\n" \
"s_waitcnt vmcnt(18)\n" \
"v_mfma_f32_16x16x32_bf16 v[56:59], v[50:53], v[136:139], v[56:59]\n" \
"v_mfma_f32_16x16x32_bf16 v[60:63], v[48:51], v[136:139], v[60:63]\n" \
"s_waitcnt vmcnt(17)\n" \
"v_mfma_f32_16x16x32_bf16 v[56:59], v[50:53], v[208:211], v[56:59]\n" \
"v_mfma_f32_16x16x32_bf16 v[60:63], v[48:51], v[208:211], v[60:63]\n" \
"s_waitcnt vmcnt(16)\n" \
"v_mfma_f32_16x16x32_bf16 v[56:59], v[50:53], v[144:147], v[56:59]\n" \
"v_mfma_f32_16x16x32_bf16 v[60:63], v[48:51], v[144:147], v[60:63]\n" \
"s_waitcnt vmcnt(15)\n" \
"v_mfma_f32_16x16x32_bf16 v[56:59], v[50:53], v[216:219], v[56:59]\n" \
"v_mfma_f32_16x16x32_bf16 v[60:63], v[48:51], v[216:219], v[60:63]\n" \
"s_waitcnt vmcnt(14)\n" \
"v_mfma_f32_16x16x32_bf16 v[56:59], v[50:53], v[152:155], v[56:59]\n" \
"v_mfma_f32_16x16x32_bf16 v[60:63], v[48:51], v[152:155], v[60:63]\n" \
"s_waitcnt vmcnt(13)\n" \
"v_mfma_f32_16x16x32_bf16 v[56:59], v[50:53], v[224:227], v[56:59]\n" \
"v_mfma_f32_16x16x32_bf16 v[60:63], v[48:51], v[224:227], v[60:63]\n" \
"s_waitcnt vmcnt(12)\n" \
"v_mfma_f32_16x16x32_bf16 v[56:59], v[50:53], v[176:179], v[56:59]\n" \
"v_mfma_f32_16x16x32_bf16 v[60:63], v[48:51], v[176:179], v[60:63]\n" \
"s_waitcnt vmcnt(11)\n" \
"v_mfma_f32_16x16x32_bf16 v[56:59], v[50:53], v[248:251], v[56:59]\n" \
"v_mfma_f32_16x16x32_bf16 v[60:63], v[48:51], v[248:251], v[60:63]\n" \
"s_waitcnt vmcnt(10)\n" \
"v_mfma_f32_16x16x32_bf16 v[168:171], v[50:53], v[140:143], v[168:171]\n" \
"v_mfma_f32_16x16x32_bf16 v[172:175], v[48:51], v[140:143], v[172:175]\n" \
"s_waitcnt vmcnt(9)\n" \
"v_mfma_f32_16x16x32_bf16 v[240:243], v[50:53], v[212:215], v[240:243]\n" \
"v_mfma_f32_16x16x32_bf16 v[244:247], v[48:51], v[212:215], v[244:247]\n" \
"s_waitcnt vmcnt(8)\n" \
"v_mfma_f32_16x16x32_bf16 v[168:171], v[50:53], v[148:151], v[168:171]\n" \
"v_cvt_pk_bf16_f32 v56, v56, v57\n" \
"v_cvt_pk_bf16_f32 v57, v58, v59\n" \
"v_mfma_f32_16x16x32_bf16 v[172:175], v[48:51], v[148:151], v[172:175]\n" \
"v_cvt_pk_bf16_f32 v58, v60, v61\n" \
"s_waitcnt vmcnt(7)\n" \
"v_mfma_f32_16x16x32_bf16 v[240:243], v[50:53], v[220:223], v[240:243]\n" \
"v_permlane16_swap_b32_e32 v56, v58\n" \
"v_mfma_f32_16x16x32_bf16 v[244:247], v[48:51], v[220:223], v[244:247]\n" \
"v_cvt_pk_bf16_f32 v59, v62, v63\n" \
"s_waitcnt vmcnt(6)\n" \
"v_mfma_f32_16x16x32_bf16 v[168:171], v[50:53], v[156:159], v[168:171]\n" \
"v_permlane16_swap_b32_e32 v57, v59\n" \
"buffer_store_dwordx4 v[56:59], v42, s[88:91], s73 offen sc0 sc1\n" \
"v_mfma_f32_16x16x32_bf16 v[172:175], v[48:51], v[156:159], v[172:175]\n" \
"s_waitcnt vmcnt(6)\n" \
"v_mfma_f32_16x16x32_bf16 v[240:243], v[50:53], v[228:231], v[240:243]\n" \
"v_mfma_f32_16x16x32_bf16 v[244:247], v[48:51], v[228:231], v[244:247]\n" \
"s_waitcnt vmcnt(5)\n" \
"v_mfma_f32_16x16x32_bf16 v[168:171], v[50:53], v[164:167], v[168:171]\n" \
"v_mfma_f32_16x16x32_bf16 v[172:175], v[48:51], v[164:167], v[172:175]\n" \
"s_waitcnt vmcnt(4)\n" \
"v_mfma_f32_16x16x32_bf16 v[240:243], v[50:53], v[236:239], v[240:243]\n" \
"v_mfma_f32_16x16x32_bf16 v[244:247], v[48:51], v[236:239], v[244:247]\n" \
"s_waitcnt vmcnt(3)\n" \
"v_mfma_f32_16x16x32_bf16 v[240:243], v[50:53], v[188:191], v[240:243]\n" \
"v_mfma_f32_16x16x32_bf16 v[244:247], v[48:51], v[188:191], v[244:247]\n" \
"s_waitcnt vmcnt(2)\n" \
"v_mfma_f32_16x16x32_bf16 v[240:243], v[50:53], v[196:199], v[240:243]\n" \
"v_cvt_pk_bf16_f32 v168, v168, v169\n" \
"v_cvt_pk_bf16_f32 v169, v170, v171\n" \
"v_mfma_f32_16x16x32_bf16 v[244:247], v[48:51], v[196:199], v[244:247]\n" \
"v_cvt_pk_bf16_f32 v170, v172, v173\n" \
"s_waitcnt vmcnt(1)\n" \
"v_mfma_f32_16x16x32_bf16 v[240:243], v[50:53], v[204:207], v[240:243]\n" \
"v_cvt_pk_bf16_f32 v171, v174, v175\n" \
"v_mfma_f32_16x16x32_bf16 v[244:247], v[48:51], v[204:207], v[244:247]\n" \
"v_permlane16_swap_b32_e32 v168, v170\n" \
"v_permlane16_swap_b32_e32 v169, v171\n" \
"buffer_store_dwordx4 v[168:171], v42, s[88:91], s92 offen sc0 sc1\n" \
"s_nop 2\n" \
"v_cvt_pk_bf16_f32 v240, v240, v241\n" \
"v_cvt_pk_bf16_f32 v241, v242, v243\n" \
"v_cvt_pk_bf16_f32 v242, v244, v245\n" \
"v_cvt_pk_bf16_f32 v243, v246, v247\n" \
"s_nop 0\n" \
"v_permlane16_swap_b32_e32 v240, v242\n" \
"v_permlane16_swap_b32_e32 v241, v243\n" \
"buffer_store_dwordx4 v[240:243], v42, s[88:91], s93 offen sc0 sc1\n"
#define ATT_VCLOB "v21","v22","v23","v32","v33","v34","v35","v36","v37","v38","v39","v40","v41","v42","v43","v44","v45","v46","v47","v48","v49","v50","v51","v52","v53","v54","v55","v56","v57","v58","v59","v60","v61","v62","v63","v64","v65","v66","v67","v68","v69","v70","v71","v72","v73","v74","v75","v76","v77","v78","v79","v80","v81","v82","v83","v84","v85","v86","v87","v88","v89","v90","v91","v92","v93","v94","v95","v96","v97","v98","v99","v100","v101","v102","v103","v104","v105","v106","v107","v108","v109","v110","v111","v112","v113","v114","v115","v116","v117","v118","v119","v120","v121","v122","v123","v124","v125","v126","v127","v128","v129","v130","v131","v132","v133","v134","v135","v136","v137","v138","v139","v140","v141","v142","v143","v144","v145","v146","v147","v148","v149","v150","v151","v152","v153","v154","v155","v156","v157","v158","v159","v160","v161","v162","v163","v164","v165","v166","v167","v168","v169","v170","v171","v172","v173","v174","v175","v176","v177","v178","v179","v180","v181","v182","v183","v184","v185","v186","v187","v188","v189","v190","v191","v192","v193","v194","v195","v196","v197","v198","v199","v200","v201","v202","v203","v204","v205","v206","v207","v208","v209","v210","v211","v212","v213","v214","v215","v216","v217","v218","v219","v220","v221","v222","v223","v224","v225","v226","v227","v228","v229","v230","v231","v232","v233","v234","v235","v236","v237","v238","v239","v240","v241","v242","v243","v244","v245","v246","v247","v248","v249","v250","v251","v252","v253","v254","v255"
#define ATT_SCLOB "s63","s64","s65","s66","s67","s68","s69","s70","s71","s72","s73","s74","s75","s76","s77","s78","s79","s80","s81","s82","s83","s84","s85","s86","s87","s88","s89","s90","s91","s92","s93","s95","s96","s97","s98","s99","s100","s101"
enum { T_fo, T_fa, T_fas, T_fb, T_fbs, T_f8, T_mk8, T_wol, T_woh, T_wbl, T_wbh, T_outl, T_outh, T_r1l, T_r1h, T_r1n, T_r1s, T_m0, T_m1, T_mw, T_m4, T_ml, T_w0, T_w1, T_w2, T_w3, T_A1, T_A2, T_A3, T_B1, T_B2, T_B3, T_Ao, T_Bo, T_Al, T_Bl, T_l8, T_xr, T_x8r, T_oA, T_oB, T_o8, T_x0, T_x1, T_x2, T_x3, T_x4, T_x5, T_x6, T_x8, T_ep, T_xf, T_kf };

__device__ __forceinline__ void out_consts(u32x4 (&vp)[3], const u16* wo, const u16* wob, const u16* out, const unsigned b, const unsigned w, const unsigned lane) {
    const unsigned sp = b & 7u, nt = (b >> 3) & 3u, j = b >> 5, head = 4u * sp + nt, OOR = 0x40000000u;
    unsigned vw[9];
#pragma unroll
    for (unsigned i = 0; i < 9u; ++i) {
        const unsigned q = 64u * i + lane, row = __umul24(q, 3641u) >> 16, c = q - __umul24(row, 18u);
        vw[i] = __umul24(row, DM * 2) + (c < 16u ? (c * 16u + sp * 32u + 32u) & 255u : c * 16u) - (i & 3u) * 1024u;
    }
    const unsigned row = 16u * w + (lane >> 2), r9 = __umul24(16u * (row >> 5) + (row & 12u) + 2u * ((row >> 4) & 1u) + ((row >> 1) & 1u), 18u) + 16u + (row & 1u);
    unsigned vwl = __umul24(r9, DM * 2) + ((lane & 2u) ? 256u : sp * 32u) + (lane & 1u) * 16u, vrd = __umul24(lane >> 2, 288u) + (lane & 3u) * 8u;
    asm volatile("" : "+v"(vw[0]), "+v"(vw[1]), "+v"(vw[2]), "+v"(vw[3]), "+v"(vw[4]), "+v"(vw[5]), "+v"(vw[6]), "+v"(vw[7]), "+v"(vw[8]), "+v"(vwl), "+v"(vrd));
    unsigned tab = 0;
#define TB(n, v) asm("v_writelane_b32 %0, %1, %2" : "+v"(tab) : "s"((unsigned)(v)), "n"((int)T_##n))
    const unsigned FJ = (unsigned)WS_F + (F_J + 640u * j) * 4u, mt = 2u * (w & 2u) + (w & 1u), w2 = w < 2u ? w : 2u, hw = head * 147456u;
    const unsigned FR = (unsigned)WS_F + F_R * 4u + (8u * j + sp) * 1024u;
    TB(fo, FJ + 4u * head); TB(fa, FR + (w | 2u) * 256u); TB(fas, FR + w * 256u + nt * 4u);
    TB(fb, FR + 128u + w * 256u); TB(fbs, FR + 128u + w * 256u + nt * 4u);
    TB(ep, b + w ? OOR : (unsigned)WS_F + F_EPOCH * 4u);
    const u16* pwo = wo + nt * 144u; const u16* pwb = wob + nt * 144u; const u16* pr1 = w < 3u ? pwo : pwb;
    TB(wol, PLO(pwo)); TB(woh, PHI(pwo) & 0xffffu); TB(wbl, PLO(pwb)); TB(wbh, PHI(pwb) & 0xffffu); TB(outl, PLO(out)); TB(outh, PHI(out) & 0xffffu); TB(r1l, PLO(pr1)); TB(r1h, PHI(pr1) & 0xffffu);
    TB(r1n, (w < 3u ? 72u * sp + 71u : 4095u) * 1152u + 288u); TB(r1s, w < 3u ? (72u * sp + 32u * w) * 1152u : hw + 110592u);
    TB(m0, 131072u + 9216u * w2); TB(w0, hw + 36864u * w2); TB(m1, w < 3u ? 9216u * w : 73728u); TB(mw, 9216u * w); TB(m4, w < 3u ? 131072u + 9216u * w : 64512u); TB(ml, 119808u + 1024u * w);
    const unsigned tA = (unsigned)WS_AF + (8u * j + mt) * (KS_TOT * 1024u), tB = (unsigned)WS_AF + (120u - 8u * j + mt) * (KS_TOT * 1024u);
#define HK(k) (4u * sp + ((nt + k) & 3u))
    TB(w1, HK(1u) * 147456u + w * 36864u); TB(w2, HK(2u) * 147456u + w * 36864u); TB(w3, HK(3u) * 147456u + w * 36864u);
    TB(A1, tA + 18432u + HK(1u) * 4096u); TB(A2, tA + 18432u + HK(2u) * 4096u); TB(A3, tA + 18432u + HK(3u) * 4096u);
    TB(B1, tB + 18432u + HK(1u) * 4096u); TB(B2, tB + 18432u + HK(2u) * 4096u); TB(B3, tB + 18432u + HK(3u) * 4096u);
    TB(Ao, tA + sp * 2048u); TB(Bo, tB + sp * 2048u); TB(Al, tA + 16384u); TB(Bl, tB + 16384u);
    const bool red = w == (sp >> 1), blk = sp & 1u;
    const unsigned xg = (unsigned)WS_X + (4u * j + nt) * 589824u + w * 16384u, oA = (128u * j + 16u * mt) * 1152u + nt * 288u, oB = (1920u - 128u * j + 16u * mt) * 1152u + nt * 288u;
    TB(l8, red ? (blk ? tB : tA) + 16384u : OOR); TB(xr, xg + sp * 65536u); TB(x8r, red ? xg + 524288u + (sp & 1u) * 1024u : OOR);
    TB(oA, oA + sp * 32u); TB(oB, oB + sp * 32u); TB(o8, red ? (blk ? oB : oA) + 256u : OOR); TB(x8, xg + 524288u + sp * 2048u);
    TB(f8, xg + w * 131072u + 14400u + sp * 4u); TB(mk8, red ? 0xff0000u : 0u); TB(xf, xg + sp * 65536u + 14336u);
    TB(kf, (unsigned)WS_F + (F_KV + head * 64u) * 4u);
#define XI(i) TB(x##i, xg + ((sp + 1u + i) & 7u) * 65536u + i * 2048u)
    XI(0); XI(1); XI(2); XI(3); XI(4); XI(5); XI(6);
#undef XI
#undef HK
#undef TB
    vp[0] = (u32x4){vw[0], vw[1], vw[2], vw[3]}; vp[1] = (u32x4){vw[4], vw[5], vw[6], vw[7]}; vp[2] = (u32x4){vw[8], vwl, vrd, tab};
}
__global__ __launch_bounds__(256, 1) __attribute__((aligned(4096))) void fused_kernel(const u16* __restrict__ x, const u16* __restrict__ ckv, const u16* __restrict__ wq, const u16* __restrict__ wuk,
                                                       const u16* __restrict__ wuv, const u16* __restrict__ wqkv, const u16* __restrict__ gw,
                                                       const u16* __restrict__ wo, const u16* __restrict__ wob, char* __restrict__ ws, u16* __restrict__ out) {
    using namespace att;
    extern __shared__ __attribute__((aligned(16))) char lds[];
    unsigned wid = rfl(threadIdx.x >> 6);
    code_touch((blockIdx.x >> 3) * 4u + wid, (unsigned)(size_t)lds + 151552u + wid * 512u);
    SBAR();
    asm volatile("" :: "s"(x), "s"(ckv), "s"(wq), "s"(wuk), "s"(wuv), "s"(wqkv), "s"(gw), "s"(wo), "s"(wob), "s"(ws), "s"(out));
    unsigned tag;
    int b = blockIdx.x;
    u32x4 vp[5], sa[5]; unsigned sd[2];
    const unsigned vl = proj_phase(x, ckv, wq, wuk, wuv, wqkv, gw, wo, wob, out, ws, lds, wid, tag, vp, sa, sd);
    asm volatile("" : "+s"(ws));
    unsigned* const flags = (unsigned*)(ws + WS_F);
    RTS(2);
#define AST(L) do {} while (0)
#define SD(J8, H, W) ((unsigned)WS_AF + (((J8) + 2u * ((W) & 2u) + ((W) & 1u)) * KS_TOT + 18u + 4u * (H)) * 1024u)
#define SRDA (u32x4){PLO(ws), PHI(ws) & 0xffffu, (unsigned)WS_L, 0x00020000u}
#define ATT_TAIL "s_endpgm\n"
    unsigned abad;
    asm volatile(ATT_ASM ATT_TAIL "L_end_%=:\n s_nop 1"
                 : "=&{s94}"(abad)
                 : "{v[0:3]}"(vp[0]), "{v[4:7]}"(vp[1]), "{v[8:11]}"(vp[2]), "{v[12:15]}"(vp[3]), "{v[16:19]}"(vp[4]), "{v20}"(vl), "{s[56:59]}"(sa[4]), "{s60}"(sd[0]), "{s61}"(sd[1]), "{s62}"(tag),
                   "{s[8:11]}"(sa[0]), "{s[12:15]}"(sa[1]), "{s[16:19]}"(sa[2]), "{s[20:23]}"(sa[3])
                 : "memory", "m0", "vcc", "scc", ATT_VCLOB, ATT_SCLOB, ACLOB_LO, ACLOB_HI);
    {
        const u16* const __attribute__((address_space(4)))* ka = (const u16* const __attribute__((address_space(4)))*)__builtin_amdgcn_kernarg_segment_ptr();
        unsigned b2 = b, w2 = wid;
        asm volatile("s_waitcnt vmcnt(0)" : "+s"(ka), "+s"(b2), "+s"(w2), "+s"(ws) :: "memory");
        const unsigned j2 = b2 >> 5, p2 = w2 & 1u, h2 = (b2 & 7u) * 4u + ((b2 >> 3) & 3u);
#pragma unroll 1
        for (unsigned i = w2 & 2u; i < ((w2 & 2u) ? (abad ? 8u : 4u) : 2u); ++i)
            att_exact(ka[0], ka[2], ws, h2, (i < 4u ? 1920u - 128u * j2 : 128u * j2 - 128u) + 32u * i + 16u * p2, *reinterpret_cast<const float*>(lds + LDS_G + (32u * i + 16u * p2 + (lane_id() & 15u)) * 4u));
        asm volatile("s_waitcnt vmcnt(0)" ::: "memory");
        unsigned* fa = (unsigned*)(ws + WS_F) + F_R + (8u * j2 + (b2 & 7u)) * 256u + (2u + p2) * 64u + ((b2 >> 3) & 3u);
        if (w2 & 2u) st_flag(fa, tag);
        wait_flags(fa, ld_flag(fa), tag);
        const unsigned l2 = lane_id(); u32x4 c[3]; out_consts(c, ka[7], ka[8], ka[10], b2, w2, l2);
        asm volatile("s_waitcnt vmcnt(0) lgkmcnt(0)\n\ts_nop 7\n\ts_branch L_att_stub" :: "{v[0:3]}"(c[0]), "{v[4:7]}"(c[1]), "{v[8:11]}"(c[2]), "{v20}"(l2 * 16u), "{s[56:59]}"(SRDA), "{s60}"(SD(8u * j2, h2, w2)),
                     "{s61}"(SD(120u - 8u * j2, h2, w2)), "{s62}"(tag) : "memory");
    }
    RTS(5);
}

extern "C" void kernel_launch(void* const* d_in, const int* in_sizes, int n_in, void* d_out, int out_size, void* d_ws, size_t ws_size,
                              hipStream_t stream) {
    (void)hipFuncSetAttribute((const void*)fused_kernel, hipFuncAttributeMaxDynamicSharedMemorySize, att::LDS_BYTES);
    fused_kernel<<<dim3(256), 256, att::LDS_BYTES, stream>>>((const u16*)d_in[0], (const u16*)d_in[4], (const u16*)d_in[5], (const u16*)d_in[6], (const u16*)d_in[7], (const u16*)d_in[1],
                                                             (const u16*)d_in[9], (const u16*)d_in[2], (const u16*)d_in[8], (char*)d_ws, (u16*)d_out);
}
```

```cpp
#include <hip/hip_runtime.h>
#include <hip/hip_bf16.h>
#include <stdint.h>
#include <utility>

typedef short bf16x8 __attribute__((ext_vector_type(8)));
typedef short s16x4 __attribute__((ext_vector_type(4)));
typedef float f32x16 __attribute__((ext_vector_type(16)));
typedef float f32x4 __attribute__((ext_vector_type(4)));
typedef unsigned u32x4 __attribute__((ext_vector_type(4)));
typedef unsigned short u16;

constexpr int S = 2048, DM = 576, NH = 32, HD = 128, DC = 512, NQKV = 1728, DHA = 18;
constexpr int NBIG = NH * HD;
constexpr int KS_TOT = 146;

#define SBAR() __builtin_amdgcn_sched_barrier(0)
constexpr size_t WS_Q = 0, WS_K = WS_Q + (size_t)S * NBIG * 2, WS_V = WS_K + (size_t)S * NBIG * 2, WS_AF = WS_V + (size_t)S * NBIG * 2, WS_X = WS_AF + (size_t)128 * KS_TOT * 1024,
                 WS_P = WS_X + (size_t)64 * 16 * 9216, WS_F = WS_P + (size_t)128 * (4 * 32 * 256 + 1024) * 4;
constexpr size_t WS_L = (size_t)96 << 20;
static_assert(WS_F == 96206848, "workspace map is baked into the generated asm");
constexpr int F_EPOCH = 0, F_J = 1024, F_ST = 8192, F_KV = 40960, F_R = 65536;
constexpr unsigned TAG_STEP = 0x9E3779B1u;
__device__ __forceinline__ unsigned ld_flag(const unsigned* p) { return __hip_atomic_load(p, __ATOMIC_RELAXED, __HIP_MEMORY_SCOPE_AGENT); }
__device__ __forceinline__ void st_flag(unsigned* p, unsigned v) { __hip_atomic_store(p, v, __ATOMIC_RELAXED, __HIP_MEMORY_SCOPE_AGENT); }
__device__ __forceinline__ unsigned ld_flag_async(const unsigned* p) { unsigned v; asm volatile("global_load_dword %0, %1, off sc1" : "=v"(v) : "v"(p) : "memory"); return v; }
#define WAITV(v, N) asm volatile("s_waitcnt vmcnt(" #N ")" : "+v"(v) :: "memory")
__device__ __forceinline__ void wait_flags(const unsigned* p, unsigned v, unsigned tag) {
    if (__builtin_expect(__builtin_amdgcn_ballot_w64(v != tag) != 0ull, 0)) {
        unsigned spins = 0;
        do { __builtin_amdgcn_s_sleep(1); } while (__builtin_amdgcn_ballot_w64(ld_flag(p) != tag) != 0ull && ++spins < (1u << 22));
    }
    asm volatile("" ::: "memory");
}

__device__ __forceinline__ unsigned cvtpk(float lo, float hi) {
    unsigned r; asm volatile("v_cvt_pk_bf16_f32 %0, %1, %2" : "=v"(r) : "v"(lo), "v"(hi)); return r;
}
__device__ __forceinline__ float bf2f(u16 v) { return __uint_as_float(((unsigned)v) << 16); }
__device__ __forceinline__ int crow(int r, int hi) { return (r & 3) + 8 * (r >> 2) + 4 * hi; }

typedef __attribute__((address_space(3))) s16x4* lds_s16x4_p;
__device__ __forceinline__ s16x4 trrd(unsigned addr) { return __builtin_amdgcn_ds_read_tr16_b64_v4i16((lds_s16x4_p)addr); }

__device__ __forceinline__ unsigned lane_id() { unsigned l; asm volatile("v_mbcnt_lo_u32_b32 %0, -1, 0\n\tv_mbcnt_hi_u32_b32 %0, -1, %0" : "=v"(l)); return l; }
#define RTS(slot) do {} while (0)
#define CT_L 5u
__device__ __forceinline__ void code_touch(unsigned wi, unsigned lds_scratch) {
    unsigned long long pc; asm volatile("s_getpc_b64 %0" : "=s"(pc));
    asm volatile("s_mov_b32 m0, %0\n\ts_mov_b64 exec, %3\n\tglobal_load_lds_dword %1, %2\n\ts_mov_b64 exec, -1" :: "s"(lds_scratch), "v"(((threadIdx.x & 63u) * 128u + wi) * 128u), "s"(pc & ~127ull), "n"((1u << CT_L) - 1u) : "m0", "memory");
}
#define ACLOB_LO "a0","a1","a2","a3","a4","a5","a6","a7","a8","a9","a10","a11","a12","a13","a14","a15","a16","a17","a18","a19","a20","a21","a22","a23","a24","a25","a26","a27","a28","a29","a30","a31","a32","a33","a34","a35","a36","a37","a38","a39","a40","a41","a42","a43","a44","a45","a46","a47","a48","a49","a50","a51","a52","a53","a54","a55","a56","a57","a58","a59","a60","a61","a62","a63","a64","a65","a66","a67","a68","a69","a70","a71","a72","a73","a74","a75","a76","a77","a78","a79","a80","a81","a82","a83","a84","a85","a86","a87","a88","a89","a90","a91","a92","a93","a94","a95","a96","a97","a98","a99","a100","a101","a102","a103","a104","a105","a106","a107","a108","a109","a110","a111","a112","a113","a114","a115","a116","a117","a118","a119","a120","a121","a122","a123","a124","a125","a126","a127"
#define ACLOB_HI "a128","a129","a130","a131","a132","a133","a134","a135","a136","a137","a138","a139","a140","a141","a142","a143","a144","a145","a146","a147","a148","a149","a150","a151","a152","a153","a154","a155","a156","a157","a158","a159","a160","a161","a162","a163","a164","a165","a166","a167","a168","a169","a170","a171","a172","a173","a174","a175","a176","a177","a178","a179","a180","a181","a182","a183","a184","a185","a186","a187","a188","a189","a190","a191","a192","a193","a194","a195","a196","a197","a198","a199","a200","a201","a202","a203","a204","a205","a206","a207","a208","a209","a210","a211","a212","a213","a214","a215","a216","a217","a218","a219","a220","a221","a222","a223","a224","a225","a226","a227","a228","a229","a230","a231","a232","a233","a234","a235","a236","a237","a238","a239","a240","a241","a242","a243","a244","a245","a246","a247","a248","a249","a250","a251","a252","a253","a254","a255"
#define PROJ_PRE0 \
"s_add_u32 s62, %[rc], 0x0\n" \
"s_and_b32 s62, s62, 0x3ff\n" \
"s_lshl_b32 s63, s62, 12\n" \
"s_add_u32 m0, %[mA], 0x0\n" \
"s_add_u32 s85, s62, 0x0\n" \
"buffer_load_dwordx4 v22, s[64:67], s85 offen lds\n" \
"s_add_u32 m0, %[mA], 0x400\n" \
"s_add_u32 s84, s62, 0x2000\n" \
"buffer_load_dwordx4 v23, s[64:67], s84 offen lds\n" \
"s_add_u32 m0, %[mB], 0x0\n" \
"s_add_u32 s85, s63, 0x0\n" \
"buffer_load_dwordx4 v26, s[68:71], s85 offen lds\n" \
"s_add_u32 m0, %[mB], 0x4000\n" \
"s_add_u32 s84, s63, 0x0\n" \
"buffer_load_dwordx4 v26, s[72:75], s84 offen lds\n" \
"s_add_u32 m0, %[mA], 0x800\n" \
"s_add_u32 s85, s62, 0x4000\n" \
"buffer_load_dwordx4 v22, s[64:67], s85 offen lds\n" \
"s_add_u32 m0, %[mA], 0xc00\n" \
"s_add_u32 s84, s62, 0x6000\n" \
"buffer_load_dwordx4 v23, s[64:67], s84 offen lds\n" \
"s_add_u32 m0, %[mB], 0x400\n" \
"s_add_u32 s85, s63, 0x8000\n" \
"buffer_load_dwordx4 v26, s[68:71], s85 offen lds\n" \
"s_add_u32 m0, %[mB], 0x4400\n" \
"s_add_u32 s84, s63, 0x8000\n" \
"buffer_load_dwordx4 v26, s[72:75], s84 offen lds\n"
#define PROJ_PRE1 \
"s_add_u32 s62, %[rc], 0x0\n" \
"s_and_b32 s62, s62, 0x3ff\n" \
"s_lshl_b32 s63, s62, 12\n" \
"s_add_u32 m0, %[mA], 0x1000\n" \
"s_add_u32 s85, s62, 0x8000\n" \
"buffer_load_dwordx4 v22, s[64:67], s85 offen lds\n" \
"s_add_u32 m0, %[mA], 0x1400\n" \
"s_add_u32 s84, s62, 0xa000\n" \
"buffer_load_dwordx4 v23, s[64:67], s84 offen lds\n" \
"s_add_u32 m0, %[mB], 0x800\n" \
"s_add_u32 s85, s63, 0x10000\n" \
"buffer_load_dwordx4 v27, s[68:71], s85 offen lds\n" \
"s_add_u32 m0, %[mB], 0x4800\n" \
"s_add_u32 s84, s63, 0x10000\n" \
"buffer_load_dwordx4 v27, s[72:75], s84 offen lds\n" \
"s_add_u32 m0, %[mA], 0x1800\n" \
"s_add_u32 s85, s62, 0xc000\n" \
"buffer_load_dwordx4 v22, s[64:67], s85 offen lds\n" \
"s_add_u32 m0, %[mA], 0x1c00\n" \
"s_add_u32 s84, s62, 0xe000\n" \
"buffer_load_dwordx4 v23, s[64:67], s84 offen lds\n" \
"s_add_u32 m0, %[mB], 0xc00\n" \
"s_add_u32 s85, s63, 0x18000\n" \
"buffer_load_dwordx4 v27, s[68:71], s85 offen lds\n" \
"s_add_u32 m0, %[mB], 0x4c00\n" \
"s_add_u32 s84, s63, 0x18000\n" \
"buffer_load_dwordx4 v27, s[72:75], s84 offen lds\n"
#define PROJ_PRE2 \
"s_add_u32 s62, %[rc], 0x80\n" \
"s_and_b32 s62, s62, 0x3ff\n" \
"s_lshl_b32 s63, s62, 12\n" \
"s_add_u32 m0, %[mA], 0x8000\n" \
"s_add_u32 s85, s62, 0x0\n" \
"buffer_load_dwordx4 v22, s[64:67], s85 offen lds\n" \
"s_add_u32 m0, %[mA], 0x8400\n" \
"s_add_u32 s84, s62, 0x2000\n" \
"buffer_load_dwordx4 v23, s[64:67], s84 offen lds\n"
#define PROJ_PRE3 \
"s_add_u32 s62, %[rc], 0x80\n" \
"s_and_b32 s62, s62, 0x3ff\n" \
"s_lshl_b32 s63, s62, 12\n" \
"s_add_u32 m0, %[mB], 0x8000\n" \
"s_add_u32 s85, s63, 0x0\n" \
"buffer_load_dwordx4 v26, s[68:71], s85 offen lds\n" \
"s_add_u32 m0, %[mB], 0xc000\n" \
"s_add_u32 s84, s63, 0x0\n" \
"buffer_load_dwordx4 v26, s[72:75], s84 offen lds\n" \
"s_add_u32 m0, %[mA], 0x8800\n" \
"s_add_u32 s85, s62, 0x4000\n" \
"buffer_load_dwordx4 v22, s[64:67], s85 offen lds\n" \
"s_add_u32 m0, %[mA], 0x8c00\n" \
"s_add_u32 s84, s62, 0x6000\n" \
"buffer_load_dwordx4 v23, s[64:67], s84 offen lds\n" \
"s_add_u32 m0, %[mB], 0x8400\n" \
"s_add_u32 s85, s63, 0x8000\n" \
"buffer_load_dwordx4 v26, s[68:71], s85 offen lds\n" \
"s_add_u32 m0, %[mB], 0xc400\n" \
"s_add_u32 s84, s63, 0x8000\n" \
"buffer_load_dwordx4 v26, s[72:75], s84 offen lds\n" \
"s_add_u32 m0, %[mA], 0x9000\n" \
"s_add_u32 s85, s62, 0x8000\n" \
"buffer_load_dwordx4 v22, s[64:67], s85 offen lds\n" \
"s_add_u32 m0, %[mA], 0x9400\n" \
"s_add_u32 s84, s62, 0xa000\n" \
"buffer_load_dwordx4 v23, s[64:67], s84 offen lds\n"
#define PROJ_TKE 0
#define PROJ_T0 0
#define PROJ_ROT 2*(sp+mt)
#define PROJ_TC 0
#define PROJ_ASM \
".p2align 3\n" \
"s_branch L_go_%=\n" \
"L_pl_%=:\n" \
"s_mov_b32 s63, 0\n" \
"L_ql_%=:\n" \
"s_sleep 1\n" \
"v_bfe_u32 v44, v20, 2, 5\n" \
"buffer_load_dword v44, v44, s[80:83], s39 offen offset:64 sc1\n" \
"s_waitcnt vmcnt(0)\n" \
"v_cmp_ne_u32_e32 vcc, %[tag], v44\n" \
"s_add_u32 s63, s63, 1\n" \
"s_nop 3\n" \
"s_cbranch_vccz L_rl_%=\n" \
"s_cmp_gt_u32 s63, 0x400000\n" \
"s_cbranch_scc0 L_ql_%=\n" \
"s_branch L_rl_%=\n" \
"L_pa_%=:\n" \
"s_mov_b32 s63, 0\n" \
"L_qa_%=:\n" \
"s_sleep 1\n" \
"v_bfe_u32 v44, v20, 2, 5\n" \
"buffer_load_dword v44, v44, s[80:83], s39 offen offset:0 sc1\n" \
"s_waitcnt vmcnt(0)\n" \
"v_cmp_ne_u32_e32 vcc, %[tag], v44\n" \
"s_add_u32 s63, s63, 1\n" \
"s_nop 3\n" \
"s_cbranch_vccz L_ra_%=\n" \
"s_cmp_gt_u32 s63, 0x400000\n" \
"s_cbranch_scc0 L_qa_%=\n" \
"s_branch L_ra_%=\n" \
"L_go_%=:\n" \
"v_xor_b32_e32 v33, 0x20, v32\n" \
"s_mov_b32 s80, %[wsl]\n" \
"s_and_b32 s81, %[wsh], 0xffff\n" \
"s_mov_b32 s82, 0x10000000\n" \
"s_mov_b32 s83, 0x20000\n" \
"s_mov_b32 s84, 0x5bc0000\n" \
"s_add_u32 s62, s22, 0x80\n" \
"buffer_load_dword v180, off, s[80:83], s84 sc1\n" \
"s_and_b32 s62, s62, 0x3ff\n" \
"s_lshl_b32 s63, s62, 12\n" \
"v_xor_b32_e64 v34, 0x40, v32\n" \
"v_xor_b32_e32 v35, 0x60, v32\n" \
"s_waitcnt vmcnt(11)\n" \
"s_barrier\n" \
"ds_read_b64_tr_b16 v[120:121], v32 offset:8192\n" \
"ds_read_b64_tr_b16 v[122:123], v32 offset:9216\n" \
"ds_read_b128 v[56:59], v29 offset:16384\n" \
"ds_read_b64_tr_b16 v[116:117], v32 offset:0\n" \
"ds_read_b64_tr_b16 v[118:119], v32 offset:1024\n" \
"ds_read_b128 v[52:55], v28 offset:16384\n" \
"s_add_u32 m0, %[mB], 0x8800\n" \
"s_add_u32 s85, s63, 0x10000\n" \
"s_mov_b32 s76, s12\n" \
"ds_read_b128 v[60:63], v28 offset:18432\n" \
"s_and_b32 s77, s13, 0xffff\n" \
"s_mov_b32 s78, 0x1e6000\n" \
"s_mov_b32 s79, 0x20000\n" \
"ds_read_b128 v[64:67], v29 offset:18432\n" \
"s_waitcnt lgkmcnt(5)\n" \
"v_mfma_f32_16x16x32_bf16 a[0:3], v[120:123], v[56:59], 0\n" \
"ds_read_b128 v[72:75], v29 offset:20480\n" \
"ds_read_b128 v[68:71], v28 offset:20480\n" \
"ds_read_b128 v[76:79], v28 offset:22528\n" \
"buffer_load_dwordx4 v27, s[68:71], s85 offen lds\n" \
"ds_read_b128 v[80:83], v29 offset:22528\n" \
"s_waitcnt lgkmcnt(6)\n" \
"v_mfma_f32_16x16x32_bf16 a[0:3], v[116:119], v[52:55], a[0:3]\n" \
"ds_read_b64_tr_b16 v[128:129], v33 offset:8192\n" \
"s_waitcnt lgkmcnt(6)\n" \
"v_mfma_f32_16x16x32_bf16 a[32:35], v[116:119], v[60:63], 0\n" \
"ds_read_b64_tr_b16 v[130:131], v33 offset:9216\n" \
"ds_read_b64_tr_b16 v[124:125], v33 offset:0\n" \
"ds_read_b64_tr_b16 v[126:127], v33 offset:1024\n" \
"s_waitcnt lgkmcnt(8)\n" \
"s_nop 0\n" \
"v_mfma_f32_16x16x32_bf16 a[32:35], v[120:123], v[64:67], a[32:35]\n" \
"ds_read_b64_tr_b16 v[136:137], v34 offset:8192\n" \
"ds_read_b64_tr_b16 v[138:139], v34 offset:9216\n" \
"ds_read_b64_tr_b16 v[132:133], v34 offset:0\n" \
"s_add_u32 m0, %[mB], 0xc800\n" \
"ds_read_b64_tr_b16 v[134:135], v34 offset:1024\n" \
"s_add_u32 s84, s63, 0x10000\n" \
"buffer_load_dwordx4 v27, s[72:75], s84 offen lds\n" \
"ds_read_b64_tr_b16 v[144:145], v35 offset:8192\n" \
"ds_read_b64_tr_b16 v[146:147], v35 offset:9216\n" \
"ds_read_b64_tr_b16 v[140:141], v35 offset:0\n" \
"s_lshr_b32 s95, %[b], 3\n" \
"ds_read_b64_tr_b16 v[142:143], v35 offset:1024\n" \
"s_lshl_b32 s96, s95, 2\n" \
"ds_read_b64_tr_b16 v[152:153], v32 offset:24576\n" \
"s_add_u32 s97, s96, %[w]\n" \
"ds_read_b64_tr_b16 v[154:155], v32 offset:25600\n" \
"v_lshrrev_b32_e32 v212, 4, v20\n" \
"s_mul_i32 s98, 27, s97\n" \
"v_min_u32_e32 v213, 26, v212\n" \
"v_add_u32_e64 v214, s98, v213\n" \
"v_mul_u32_u24_e32 v215, 0xaaab, v214\n" \
"s_waitcnt lgkmcnt(15)\n" \
"v_mfma_f32_16x16x32_bf16 a[64:67], v[120:123], v[72:75], 0\n" \
"ds_read_b64_tr_b16 v[148:149], v32 offset:16384\n" \
"ds_read_b64_tr_b16 v[150:151], v32 offset:17408\n" \
"v_mfma_f32_16x16x32_bf16 a[64:67], v[116:119], v[68:71], a[64:67]\n" \
"s_add_u32 m0, %[mA], 0x9800\n" \
"s_add_u32 s85, s62, 0xc000\n" \
"v_mfma_f32_16x16x32_bf16 a[96:99], v[116:119], v[76:79], 0\n" \
"ds_read_b64_tr_b16 v[160:161], v33 offset:24576\n" \
"s_waitcnt lgkmcnt(15)\n" \
"v_mfma_f32_16x16x32_bf16 a[96:99], v[120:123], v[80:83], a[96:99]\n" \
"buffer_load_dwordx4 v22, s[64:67], s85 offen lds\n" \
"v_mfma_f32_16x16x32_bf16 a[4:7], v[128:131], v[56:59], 0\n" \
"s_add_u32 m0, %[mA], 0x9c00\n" \
"s_waitcnt lgkmcnt(13)\n" \
"s_nop 0\n" \
"v_mfma_f32_16x16x32_bf16 a[4:7], v[124:127], v[52:55], a[4:7]\n" \
"s_add_u32 s84, s62, 0xe000\n" \
"ds_read_b64_tr_b16 v[162:163], v33 offset:25600\n" \
"v_mfma_f32_16x16x32_bf16 a[36:39], v[124:127], v[60:63], 0\n" \
"buffer_load_dwordx4 v23, s[64:67], s84 offen lds\n" \
"v_mfma_f32_16x16x32_bf16 a[36:39], v[128:131], v[64:67], a[36:39]\n" \
"s_add_u32 m0, %[mB], 0x8c00\n" \
"s_add_u32 s85, s63, 0x18000\n" \
"v_mfma_f32_16x16x32_bf16 a[68:71], v[128:131], v[72:75], 0\n" \
"ds_read_b64_tr_b16 v[156:157], v33 offset:16384\n" \
"ds_read_b64_tr_b16 v[158:159], v33 offset:17408\n" \
"v_mfma_f32_16x16x32_bf16 a[68:71], v[124:127], v[68:71], a[68:71]\n" \
"buffer_load_dwordx4 v27, s[68:71], s85 offen lds\n" \
"v_mfma_f32_16x16x32_bf16 a[100:103], v[124:127], v[76:79], 0\n" \
"s_add_u32 m0, %[mB], 0xcc00\n" \
"s_add_u32 s84, s63, 0x18000\n" \
"v_mfma_f32_16x16x32_bf16 a[100:103], v[128:131], v[80:83], a[100:103]\n" \
"ds_read_b64_tr_b16 v[168:169], v34 offset:24576\n" \
"s_waitcnt lgkmcnt(7)\n" \
"v_mfma_f32_16x16x32_bf16 a[8:11], v[136:139], v[56:59], 0\n" \
"buffer_load_dwordx4 v27, s[72:75], s84 offen lds\n" \
"v_mfma_f32_16x16x32_bf16 a[8:11], v[132:135], v[52:55], a[8:11]\n" \
"ds_read_b64_tr_b16 v[170:171], v34 offset:25600\n" \
"ds_read_b64_tr_b16 v[164:165], v34 offset:16384\n" \
"v_mfma_f32_16x16x32_bf16 a[40:43], v[132:135], v[60:63], 0\n" \
"ds_read_b64_tr_b16 v[166:167], v34 offset:17408\n" \
"v_lshrrev_b32_e32 v216, 18, v215\n" \
"v_mfma_f32_16x16x32_bf16 a[40:43], v[136:139], v[64:67], a[40:43]\n" \
"v_mul_u32_u24_e32 v218, 6, v216\n" \
"v_sub_u32_e32 v219, v214, v218\n" \
"v_mfma_f32_16x16x32_bf16 a[72:75], v[136:139], v[72:75], 0\n" \
"v_lshrrev_b32_e32 v220, 1, v219\n" \
"v_mul_u32_u24_e32 v217, 27, v216\n" \
"v_mfma_f32_16x16x32_bf16 a[72:75], v[132:135], v[68:71], a[72:75]\n" \
"v_mul_u32_u24_e32 v221, 9, v220\n" \
"v_add_u32_e32 v222, v217, v221\n" \
"v_mfma_f32_16x16x32_bf16 a[104:107], v[132:135], v[76:79], 0\n" \
"v_and_b32_e32 v223, 1, v219\n" \
"s_and_b32 s94, 7, %[b]\n" \
"v_mfma_f32_16x16x32_bf16 a[104:107], v[136:139], v[80:83], a[104:107]\n" \
"ds_read_b64_tr_b16 v[176:177], v35 offset:24576\n" \
"v_add_u32_e64 v224, v222, v223\n" \
"v_mfma_f32_16x16x32_bf16 a[12:15], v[144:147], v[56:59], 0\n" \
"ds_read_b64_tr_b16 v[178:179], v35 offset:25600\n" \
"v_add_u32_e64 v225, s94, v224\n" \
"v_mfma_f32_16x16x32_bf16 a[12:15], v[140:143], v[52:55], a[12:15]\n" \
"ds_read_b64_tr_b16 v[172:173], v35 offset:16384\n" \
"v_lshlrev_b32_e32 v186, 7, v225\n" \
"v_mfma_f32_16x16x32_bf16 a[44:47], v[140:143], v[60:63], 0\n" \
"ds_read_b64_tr_b16 v[174:175], v35 offset:17408\n" \
"ds_read_b128 v[88:91], v29 offset:0\n" \
"v_mfma_f32_16x16x32_bf16 a[44:47], v[144:147], v[64:67], a[44:47]\n" \
"ds_read_b128 v[84:87], v28 offset:0\n" \
"ds_read_b128 v[92:95], v28 offset:2048\n" \
"v_mfma_f32_16x16x32_bf16 a[76:79], v[144:147], v[72:75], 0\n" \
"ds_read_b128 v[96:99], v29 offset:2048\n" \
"s_mul_i32 s89, 6, s97\n" \
"v_mfma_f32_16x16x32_bf16 a[76:79], v[140:143], v[68:71], a[76:79]\n" \
"ds_read_b128 v[104:107], v29 offset:4096\n" \
"s_add_u32 s95, 0xffffffdc, s89\n" \
"v_mfma_f32_16x16x32_bf16 a[108:111], v[140:143], v[76:79], 0\n" \
"ds_read_b128 v[100:103], v28 offset:4096\n" \
"s_mul_i32 s99, 0x1200, s94\n" \
"v_mfma_f32_16x16x32_bf16 a[108:111], v[144:147], v[80:83], a[108:111]\n" \
"ds_read_b128 v[108:111], v28 offset:6144\n" \
"s_mul_i32 s100, 36, s97\n" \
"v_mfma_f32_16x16x32_bf16 a[16:19], v[56:59], v[152:155], 0\n" \
"ds_read_b128 v[112:115], v29 offset:6144\n" \
"s_waitcnt lgkmcnt(12)\n" \
"v_mfma_f32_16x16x32_bf16 a[16:19], v[52:55], v[148:151], a[16:19]\n" \
"v_add_u32_e32 v227, s95, v212\n" \
"s_add_u32 s101, s99, s100\n" \
"v_mfma_f32_16x16x32_bf16 a[48:51], v[60:63], v[148:151], 0\n" \
"s_mul_i32 s88, 0x288, s94\n" \
"v_min_u32_e32 v228, 0x287, v227\n" \
"v_mfma_f32_16x16x32_bf16 a[48:51], v[64:67], v[152:155], a[48:51]\n" \
"s_mov_b32 m0, %[mS]\n" \
"s_mov_b32 s86, s12\n" \
"v_mfma_f32_16x16x32_bf16 a[80:83], v[72:75], v[152:155], 0\n" \
"s_mov_b32 s87, s13\n" \
"v_add_u32_e32 v226, s101, v212\n" \
"v_mfma_f32_16x16x32_bf16 a[80:83], v[68:71], v[148:151], a[80:83]\n" \
"v_add_u32_e32 v229, s88, v228\n" \
"v_cmp_gt_u32_e32 vcc, 36, v212\n" \
"v_mfma_f32_16x16x32_bf16 a[112:115], v[76:79], v[148:151], 0\n" \
"s_lshl_b32 s96, %[w], 6\n" \
"v_cndmask_b32_e32 v230, v229, v226, vcc\n" \
"v_mfma_f32_16x16x32_bf16 a[112:115], v[80:83], v[152:155], a[112:115]\n" \
"v_lshlrev_b32_e32 v16, 7, v230\n" \
"v_lshrrev_b32_e32 v212, 4, v20\n" \
"v_mfma_f32_16x16x32_bf16 a[20:23], v[56:59], v[160:163], 0\n" \
"v_add_u32_e64 v213, s96, v212\n" \
"v_mul_u32_u24_e32 v214, 0x2aab, v213\n" \
"v_mfma_f32_16x16x32_bf16 a[20:23], v[52:55], v[156:159], a[20:23]\n" \
"v_lshrrev_b32_e32 v215, 16, v214\n" \
"s_lshr_b32 s94, %[b], 5\n" \
"s_mov_b32 exec_lo, 0x7ffffff\n" \
"s_mov_b32 exec_hi, 0x0\n" \
"global_load_lds_dword v186, s[86:87]\n" \
"s_mov_b64 exec, -1\n" \
"v_mfma_f32_16x16x32_bf16 a[52:55], v[60:63], v[156:159], 0\n" \
"v_mul_u32_u24_e32 v218, 6, v215\n" \
"s_mul_i32 s95, 0x48, s94\n" \
"v_mfma_f32_16x16x32_bf16 a[52:55], v[64:67], v[160:163], a[52:55]\n" \
"v_sub_u32_e32 v219, v213, v218\n" \
"v_add_u32_e64 v216, s95, v215\n" \
"v_mfma_f32_16x16x32_bf16 a[84:87], v[72:75], v[160:163], 0\n" \
"v_lshrrev_b32_e32 v220, 1, v219\n" \
"s_and_b32 s100, 7, %[b]\n" \
"v_mfma_f32_16x16x32_bf16 a[84:87], v[68:71], v[156:159], a[84:87]\n" \
"s_lshr_b32 s88, %[b], 3\n" \
"v_mul_u32_u24_e32 v217, 0xd80, v216\n" \
"v_mfma_f32_16x16x32_bf16 a[116:119], v[76:79], v[156:159], 0\n" \
"v_mul_u32_u24_e32 v221, 0x480, v220\n" \
"v_and_b32_e32 v223, 1, v219\n" \
"v_mfma_f32_16x16x32_bf16 a[116:119], v[80:83], v[160:163], a[116:119]\n" \
"s_add_u32 s97, 0x100, s96\n" \
"s_lshl_b32 s101, s100, 2\n" \
"v_mfma_f32_16x16x32_bf16 a[24:27], v[56:59], v[168:171], 0\n" \
"s_and_b32 s89, 3, s88\n" \
"v_add_u32_e64 v222, v217, v221\n" \
"v_mfma_f32_16x16x32_bf16 a[24:27], v[52:55], v[164:167], a[24:27]\n" \
"v_lshlrev_b32_e32 v224, 4, v223\n" \
"s_add_u32 s96, s101, s89\n" \
"v_mfma_f32_16x16x32_bf16 a[56:59], v[60:63], v[164:167], 0\n" \
"v_add_u32_e32 v192, v222, v224\n" \
"s_mul_i32 s56, 36, s96\n" \
"v_mfma_f32_16x16x32_bf16 a[56:59], v[64:67], v[168:171], a[56:59]\n" \
"v_add_u32_e64 v225, s97, v212\n" \
"v_mul_u32_u24_e32 v226, 0x2aab, v225\n" \
"v_mfma_f32_16x16x32_bf16 a[88:91], v[72:75], v[168:171], 0\n" \
"v_lshrrev_b32_e32 v227, 16, v226\n" \
"v_mul_u32_u24_e32 v230, 6, v227\n" \
"v_mfma_f32_16x16x32_bf16 a[88:91], v[68:71], v[164:167], a[88:91]\n" \
"v_sub_u32_e32 v231, v225, v230\n" \
"v_add_u32_e32 v228, s95, v227\n" \
"v_mfma_f32_16x16x32_bf16 a[120:123], v[76:79], v[164:167], 0\n" \
"v_lshrrev_b32_e32 v232, 1, v231\n" \
"v_mul_u32_u24_e32 v229, 0xd80, v228\n" \
"v_mfma_f32_16x16x32_bf16 a[120:123], v[80:83], v[168:171], a[120:123]\n" \
"v_mul_u32_u24_e32 v233, 0x480, v232\n" \
"s_waitcnt lgkmcnt(0)\n" \
"v_mfma_f32_16x16x32_bf16 a[28:31], v[56:59], v[176:179], 0\n" \
"v_and_b32_e32 v235, 1, v231\n" \
"v_add_u32_e32 v234, v229, v233\n" \
"v_mfma_f32_16x16x32_bf16 a[28:31], v[52:55], v[172:175], a[28:31]\n" \
"v_lshlrev_b32_e32 v236, 4, v235\n" \
"v_add_u32_e32 v237, v234, v236\n" \
"v_mfma_f32_16x16x32_bf16 a[60:63], v[60:63], v[172:175], 0\n" \
"v_cmp_gt_u32_e32 vcc, 0x1b0, v225\n" \
"s_mul_i32 s98, 18, %[w]\n" \
"v_mfma_f32_16x16x32_bf16 a[60:63], v[64:67], v[176:179], a[60:63]\n" \
"v_cndmask_b32_e32 v193, 0x40000000, v237, vcc\n" \
"s_add_u32 s99, s95, s98\n" \
"v_mfma_f32_16x16x32_bf16 a[92:95], v[72:75], v[176:179], 0\n" \
"v_add_u32_e32 v238, s99, v212\n" \
"v_mul_u32_u24_e32 v239, 0xd80, v238\n" \
"v_mfma_f32_16x16x32_bf16 a[92:95], v[68:71], v[172:175], a[92:95]\n" \
"v_cmp_gt_u32_e32 vcc, 18, v212\n" \
"v_lshlrev_b32_e32 v240, 1, v238\n" \
"v_mfma_f32_16x16x32_bf16 a[124:127], v[76:79], v[172:175], 0\n" \
"v_cndmask_b32_e32 v194, 0x40000000, v239, vcc\n" \
"v_cmp_gt_u32_e32 vcc, 18, v212\n" \
"v_mfma_f32_16x16x32_bf16 a[124:127], v[80:83], v[176:179], a[124:127]\n" \
"v_lshrrev_b32_e32 v230, 1, v238\n" \
"v_cndmask_b32_e32 v195, 0x40000000, v240, vcc\n" \
"v_mfma_f32_16x16x32_bf16 a[144:147], v[88:91], v[152:155], 0\n" \
"v_and_b32_e32 v227, 0xff8, v238\n" \
"v_and_b32_e64 v232, 2, v230\n" \
"v_mfma_f32_16x16x32_bf16 a[144:147], v[84:87], v[148:151], a[144:147]\n" \
"v_add_u32_e32 v229, v227, v232\n" \
"v_and_b32_e32 v233, 1, v238\n" \
"v_mfma_f32_16x16x32_bf16 a[176:179], v[92:95], v[148:151], 0\n" \
"v_add_u32_e32 v235, v229, v233\n" \
"v_lshrrev_b32_e32 v242, 1, v216\n" \
"v_mfma_f32_16x16x32_bf16 a[176:179], v[96:99], v[152:155], a[176:179]\n" \
"v_lshrrev_b32_e32 v244, 2, v216\n" \
"v_lshrrev_b32_e32 v214, 1, v228\n" \
"v_mfma_f32_16x16x32_bf16 a[208:211], v[104:107], v[152:155], 0\n" \
"v_lshrrev_b32_e32 v213, 2, v228\n" \
"v_lshrrev_b32_e32 v236, 1, v235\n" \
"v_mfma_f32_16x16x32_bf16 a[208:211], v[100:103], v[148:151], a[208:211]\n" \
"v_lshrrev_b32_e32 v239, 2, v235\n" \
"s_lshl_b32 s98, %[w], 10\n" \
"v_mfma_f32_16x16x32_bf16 a[240:243], v[108:111], v[148:151], 0\n" \
"s_mul_i32 s101, 0x12000, s96\n" \
"v_and_b32_e64 v243, 1, v242\n" \
"v_mfma_f32_16x16x32_bf16 a[240:243], v[112:115], v[152:155], a[240:243]\n" \
"v_and_b32_e32 v245, 2, v244\n" \
"v_and_b32_e32 v215, 1, v214\n" \
"v_mfma_f32_16x16x32_bf16 a[148:151], v[88:91], v[160:163], 0\n" \
"v_and_b32_e32 v218, 2, v213\n" \
"v_and_b32_e32 v237, 1, v236\n" \
"v_mfma_f32_16x16x32_bf16 a[148:151], v[84:87], v[156:159], a[148:151]\n" \
"v_and_b32_e64 v240, 2, v239\n" \
"s_add_u32 s99, 0x22000, s98\n" \
"v_mfma_f32_16x16x32_bf16 a[180:183], v[92:95], v[156:159], 0\n" \
"s_add_u32 s89, 0x6000000, s101\n" \
"s_mul_i32 s97, 0x2400, s94\n" \
"v_mfma_f32_16x16x32_bf16 a[180:183], v[96:99], v[160:163], a[180:183]\n" \
"v_lshlrev_b32_e32 v241, 7, v216\n" \
"v_or_b32_e32 v246, v243, v245\n" \
"v_mfma_f32_16x16x32_bf16 a[212:215], v[104:107], v[160:163], 0\n" \
"v_or_b32_e32 v220, v215, v218\n" \
"v_or_b32_e32 v242, v237, v240\n" \
"v_mfma_f32_16x16x32_bf16 a[212:215], v[100:103], v[156:159], a[212:215]\n" \
"v_add_u32_e32 v199, s99, v20\n" \
"s_add_u32 s37, s89, s98\n" \
"v_mfma_f32_16x16x32_bf16 a[244:247], v[108:111], v[156:159], 0\n" \
"v_lshlrev_b32_e32 v247, 1, v246\n" \
"v_lshlrev_b32_e32 v217, 1, v220\n" \
"v_mfma_f32_16x16x32_bf16 a[244:247], v[112:115], v[160:163], a[244:247]\n" \
"v_xor_b32_e32 v216, 3, v242\n" \
"s_add_u32 s57, s37, s97\n" \
"v_mfma_f32_16x16x32_bf16 a[152:155], v[88:91], v[168:171], 0\n" \
"s_lshl_b32 s99, s88, 2\n" \
"v_xor_b32_e32 v248, v219, v247\n" \
"v_mfma_f32_16x16x32_bf16 a[152:155], v[84:87], v[164:167], a[152:155]\n" \
"v_xor_b32_e32 v221, v231, v217\n" \
"v_lshlrev_b32_e32 v234, 7, v235\n" \
"v_mfma_f32_16x16x32_bf16 a[184:187], v[92:95], v[164:167], 0\n" \
"v_lshlrev_b32_e32 v244, 5, v216\n" \
"v_and_b32_e32 v245, 2, v238\n" \
"v_mfma_f32_16x16x32_bf16 a[184:187], v[96:99], v[168:171], a[184:187]\n" \
"s_add_u32 s94, 0x2000, s57\n" \
"s_add_u32 s96, s99, %[w]\n" \
"v_mfma_f32_16x16x32_bf16 a[216:219], v[104:107], v[168:171], 0\n" \
"s_lshl_b32 s89, %[b], 2\n" \
"s_sub_u32 s95, 0x22000, s97\n" \
"v_mfma_f32_16x16x32_bf16 a[216:219], v[100:103], v[164:167], a[216:219]\n" \
"v_lshlrev_b32_e32 v249, 4, v248\n" \
"v_lshlrev_b32_e32 v251, 7, v228\n" \
"v_mfma_f32_16x16x32_bf16 a[248:251], v[108:111], v[164:167], 0\n" \
"v_lshlrev_b32_e32 v223, 4, v221\n" \
"v_add_u32_e32 v243, v234, v244\n" \
"v_mfma_f32_16x16x32_bf16 a[248:251], v[112:115], v[168:171], a[248:251]\n" \
"v_lshlrev_b32_e32 v246, 1, v245\n" \
"s_mul_i32 s101, 36, s96\n" \
"v_mfma_f32_16x16x32_bf16 a[156:159], v[88:91], v[176:179], 0\n" \
"s_cmp_eq_u32 %[w], 0\n" \
"s_cselect_b32 s58, s94, 0x40000000\n" \
"v_mfma_f32_16x16x32_bf16 a[156:159], v[84:87], v[172:175], a[156:159]\n" \
"s_add_u32 s98, s89, %[w]\n" \
"v_add_u32_e32 v250, v241, v249\n" \
"v_mfma_f32_16x16x32_bf16 a[188:191], v[92:95], v[172:175], 0\n" \
"v_add_u32_e32 v222, v251, v223\n" \
"v_add_u32_e32 v219, v243, v246\n" \
"v_mfma_f32_16x16x32_bf16 a[188:191], v[96:99], v[176:179], a[188:191]\n" \
"v_add_u32_e32 v248, s101, v212\n" \
"s_mul_i32 s97, 18, s98\n" \
"v_mfma_f32_16x16x32_bf16 a[220:223], v[104:107], v[176:179], 0\n" \
"v_add_u32_e32 v196, s95, v250\n" \
"v_add_u32_e32 v224, s95, v222\n" \
"v_mfma_f32_16x16x32_bf16 a[220:223], v[100:103], v[172:175], a[220:223]\n" \
"v_add_u32_e32 v247, s95, v219\n" \
"v_lshrrev_b32_e32 v241, 3, v248\n" \
"v_mfma_f32_16x16x32_bf16 a[252:255], v[108:111], v[172:175], 0\n" \
"s_add_u32 s94, 0xffffffdc, s97\n" \
"v_min_u32_e32 v213, 53, v212\n" \
"v_mfma_f32_16x16x32_bf16 a[252:255], v[112:115], v[176:179], a[252:255]\n" \
"s_lshl_b32 s95, s100, 3\n" \
"v_lshlrev_b32_e32 v249, 6, v241\n" \
"v_mfma_f32_16x16x32_bf16 a[128:131], v[120:123], v[88:91], 0\n" \
"v_add_u32_e32 v215, s94, v213\n" \
"s_and_b32 s97, 7, %[b]\n" \
"v_mfma_f32_16x16x32_bf16 a[128:131], v[116:119], v[84:87], a[128:131]\n" \
"s_lshr_b32 s99, %[b], 3\n" \
"v_add_u32_e64 v250, s95, v249\n" \
"v_mfma_f32_16x16x32_bf16 a[160:163], v[116:119], v[92:95], 0\n" \
"s_lshr_b32 s94, %[b], 5\n" \
"s_lshl_b32 s98, s97, 2\n" \
"v_mfma_f32_16x16x32_bf16 a[160:163], v[120:123], v[96:99], a[160:163]\n" \
"s_and_b32 s100, 3, s99\n" \
"s_mul_i32 s95, 0xa00, s94\n" \
"v_mfma_f32_16x16x32_bf16 a[192:195], v[120:123], v[104:107], 0\n" \
"s_add_u32 s101, s98, s100\n" \
"s_add_u32 s96, 0x5bc1000, s95\n" \
"v_mfma_f32_16x16x32_bf16 a[192:195], v[116:119], v[100:103], a[192:195]\n" \
"s_lshl_b32 s88, s101, 2\n" \
"s_add_u32 s89, s96, s88\n" \
"v_mfma_f32_16x16x32_bf16 a[224:227], v[116:119], v[108:111], 0\n" \
"s_cmp_eq_u32 %[w], 0\n" \
"s_cselect_b32 s95, s89, 0x40000000\n" \
"v_mfma_f32_16x16x32_bf16 a[224:227], v[120:123], v[112:115], a[224:227]\n" \
"s_lshl_b32 s99, s94, 3\n" \
"s_add_u32 s88, s99, s97\n" \
"v_mfma_f32_16x16x32_bf16 a[132:135], v[128:131], v[88:91], 0\n" \
"s_add_u32 s62, s22, 0x100\n" \
"s_and_b32 s62, s62, 0x3ff\n" \
"v_mfma_f32_16x16x32_bf16 a[132:135], v[124:127], v[84:87], a[132:135]\n" \
"s_lshl_b32 s63, s62, 12\n" \
"v_writelane_b32 v11, s95, 0\n" \
"v_mfma_f32_16x16x32_bf16 a[164:167], v[124:127], v[92:95], 0\n" \
"s_lshl_b32 s89, s88, 10\n" \
"s_add_u32 s95, 0x5c00000, s89\n" \
"v_mfma_f32_16x16x32_bf16 a[164:167], v[128:131], v[96:99], a[164:167]\n" \
"s_lshl_b32 s99, %[w], 8\n" \
"s_add_u32 s88, s95, s99\n" \
"v_mfma_f32_16x16x32_bf16 a[196:199], v[128:131], v[104:107], 0\n" \
"v_add_u32_e32 v226, 0x24400, v20\n" \
"v_cmp_gt_u32_e32 vcc, 0x1b0, v225\n" \
"v_mfma_f32_16x16x32_bf16 a[196:199], v[124:127], v[100:103], a[196:199]\n" \
"s_or_b32 s89, 0x200, s88\n" \
"s_lshl_b32 s95, s100, 2\n" \
"v_mfma_f32_16x16x32_bf16 a[228:231], v[124:127], v[108:111], 0\n" \
"v_cndmask_b32_e32 v197, v226, v224, vcc\n" \
"v_writelane_b32 v11, s89, 1\n" \
"v_mfma_f32_16x16x32_bf16 a[228:231], v[128:131], v[112:115], a[228:231]\n" \
"s_add_u32 s99, s88, s95\n" \
"v_cmp_gt_u32_e32 vcc, 18, v212\n" \
"s_waitcnt vmcnt(1)\n" \
"s_barrier\n" \
"v_mfma_f32_16x16x32_bf16 a[136:139], v[136:139], v[88:91], 0\n" \
"ds_read_b64_tr_b16 v[152:153], v32 offset:57344\n" \
"ds_read_b64_tr_b16 v[154:155], v32 offset:58368\n" \
"v_mfma_f32_16x16x32_bf16 a[136:139], v[132:135], v[84:87], a[136:139]\n" \
"ds_read_b128 v[56:59], v29 offset:49152\n" \
"s_add_u32 m0, %[mA], 0x0\n" \
"v_mfma_f32_16x16x32_bf16 a[168:171], v[132:135], v[92:95], 0\n" \
"s_add_u32 s85, s62, 0x0\n" \
"ds_read_b64_tr_b16 v[148:149], v32 offset:49152\n" \
"v_mfma_f32_16x16x32_bf16 a[168:171], v[136:139], v[96:99], a[168:171]\n" \
"buffer_load_dwordx4 v22, s[64:67], s85 offen lds\n" \
"ds_read_b64_tr_b16 v[150:151], v32 offset:50176\n" \
"ds_read_b128 v[52:55], v28 offset:49152\n" \
"v_mfma_f32_16x16x32_bf16 a[200:203], v[136:139], v[104:107], 0\n" \
"ds_read_b128 v[60:63], v28 offset:51200\n" \
"ds_read_b128 v[64:67], v29 offset:51200\n" \
"L_pf0:\n" \
"s_cbranch_execz L_pf4\n" \
"s_nop 0\n" \
"v_mfma_f32_16x16x32_bf16 a[200:203], v[132:135], v[100:103], a[200:203]\n" \
"s_add_u32 m0, %[mA], 0x400\n" \
"s_add_u32 s84, s62, 0x2000\n" \
"v_mfma_f32_16x16x32_bf16 a[232:235], v[132:135], v[108:111], 0\n" \
"buffer_load_dwordx4 v23, s[64:67], s84 offen lds\n" \
"ds_read_b128 v[72:75], v29 offset:53248\n" \
"v_mfma_f32_16x16x32_bf16 a[232:235], v[136:139], v[112:115], a[232:235]\n" \
"ds_read_b128 v[68:71], v28 offset:53248\n" \
"ds_read_b128 v[76:79], v28 offset:55296\n" \
"v_mfma_f32_16x16x32_bf16 a[140:143], v[144:147], v[88:91], 0\n" \
"ds_read_b128 v[80:83], v29 offset:55296\n" \
"s_add_u32 m0, %[mB], 0x0\n" \
"v_mfma_f32_16x16x32_bf16 a[140:143], v[140:143], v[84:87], a[140:143]\n" \
"s_add_u32 s85, s63, 0x0\n" \
"ds_read_b64_tr_b16 v[160:161], v33 offset:57344\n" \
"v_mfma_f32_16x16x32_bf16 a[172:175], v[140:143], v[92:95], 0\n" \
"buffer_load_dwordx4 v26, s[68:71], s85 offen lds\n" \
"ds_read_b64_tr_b16 v[162:163], v33 offset:58368\n" \
"v_mfma_f32_16x16x32_bf16 a[172:175], v[144:147], v[96:99], a[172:175]\n" \
"ds_read_b64_tr_b16 v[156:157], v33 offset:49152\n" \
"ds_read_b64_tr_b16 v[158:159], v33 offset:50176\n" \
"v_mfma_f32_16x16x32_bf16 a[204:207], v[144:147], v[104:107], 0\n" \
"s_add_u32 m0, %[mB], 0x4000\n" \
"s_add_u32 s84, s63, 0x0\n" \
"v_mfma_f32_16x16x32_bf16 a[204:207], v[140:143], v[100:103], a[204:207]\n" \
"buffer_load_dwordx4 v26, s[72:75], s84 offen lds\n" \
"v_mfma_f32_16x16x32_bf16 a[236:239], v[140:143], v[108:111], 0\n" \
"s_add_u32 m0, %[mA], 0x800\n" \
"s_add_u32 s85, s62, 0x4000\n" \
"v_mfma_f32_16x16x32_bf16 a[236:239], v[144:147], v[112:115], a[236:239]\n" \
"ds_read_b64_tr_b16 v[168:169], v34 offset:57344\n" \
"s_waitcnt lgkmcnt(13)\n" \
"v_mfma_f32_16x16x32_bf16 a[16:19], v[56:59], v[152:155], a[16:19]\n" \
"ds_read_b64_tr_b16 v[170:171], v34 offset:58368\n" \
"s_waitcnt lgkmcnt(12)\n" \
"v_mfma_f32_16x16x32_bf16 a[16:19], v[52:55], v[148:151], a[16:19]\n" \
"ds_read_b64_tr_b16 v[164:165], v34 offset:49152\n" \
"s_waitcnt lgkmcnt(12)\n" \
"v_mfma_f32_16x16x32_bf16 a[48:51], v[60:63], v[148:151], a[48:51]\n" \
"ds_read_b64_tr_b16 v[166:167], v34 offset:50176\n" \
"buffer_load_dwordx4 v22, s[64:67], s85 offen lds\n" \
"s_waitcnt lgkmcnt(12)\n" \
"v_mfma_f32_16x16x32_bf16 a[48:51], v[64:67], v[152:155], a[48:51]\n" \
"s_add_u32 m0, %[mA], 0xc00\n" \
"s_waitcnt lgkmcnt(11)\n" \
"v_mfma_f32_16x16x32_bf16 a[80:83], v[72:75], v[152:155], a[80:83]\n" \
"s_add_u32 s84, s62, 0x6000\n" \
"s_waitcnt lgkmcnt(10)\n" \
"v_mfma_f32_16x16x32_bf16 a[80:83], v[68:71], v[148:151], a[80:83]\n" \
"s_add_u32 s85, s63, 0x8000\n" \
"buffer_load_dwordx4 v23, s[64:67], s84 offen lds\n" \
"s_waitcnt lgkmcnt(9)\n" \
"v_mfma_f32_16x16x32_bf16 a[112:115], v[76:79], v[148:151], a[112:115]\n" \
"s_add_u32 m0, %[mB], 0x400\n" \
"s_waitcnt lgkmcnt(8)\n" \
"v_mfma_f32_16x16x32_bf16 a[112:115], v[80:83], v[152:155], a[112:115]\n" \
"ds_read_b64_tr_b16 v[176:177], v35 offset:57344\n" \
"s_waitcnt lgkmcnt(7)\n" \
"v_mfma_f32_16x16x32_bf16 a[20:23], v[56:59], v[160:163], a[20:23]\n" \
"ds_read_b64_tr_b16 v[178:179], v35 offset:58368\n" \
"s_waitcnt lgkmcnt(6)\n" \
"s_nop 0\n" \
"v_mfma_f32_16x16x32_bf16 a[20:23], v[52:55], v[156:159], a[20:23]\n" \
"buffer_load_dwordx4 v26, s[68:71], s85 offen lds\n" \
"v_mfma_f32_16x16x32_bf16 a[52:55], v[60:63], v[156:159], a[52:55]\n" \
"ds_read_b64_tr_b16 v[172:173], v35 offset:49152\n" \
"ds_read_b64_tr_b16 v[174:175], v35 offset:50176\n" \
"v_mfma_f32_16x16x32_bf16 a[52:55], v[64:67], v[160:163], a[52:55]\n" \
"s_add_u32 m0, %[mB], 0x4400\n" \
"s_add_u32 s84, s63, 0x8000\n" \
"v_mfma_f32_16x16x32_bf16 a[84:87], v[72:75], v[160:163], a[84:87]\n" \
"buffer_load_dwordx4 v26, s[72:75], s84 offen lds\n" \
"v_mfma_f32_16x16x32_bf16 a[84:87], v[68:71], v[156:159], a[84:87]\n" \
"s_add_u32 m0, %[mA], 0x1000\n" \
"s_add_u32 s85, s62, 0x8000\n" \
"v_mfma_f32_16x16x32_bf16 a[116:119], v[76:79], v[156:159], a[116:119]\n" \
"ds_read_b64_tr_b16 v[120:121], v32 offset:40960\n" \
"ds_read_b64_tr_b16 v[122:123], v32 offset:41984\n" \
"v_mfma_f32_16x16x32_bf16 a[116:119], v[80:83], v[160:163], a[116:119]\n" \
"ds_read_b64_tr_b16 v[116:117], v32 offset:32768\n" \
"s_waitcnt lgkmcnt(7)\n" \
"s_nop 0\n" \
"v_mfma_f32_16x16x32_bf16 a[24:27], v[56:59], v[168:171], a[24:27]\n" \
"buffer_load_dwordx4 v22, s[64:67], s85 offen lds\n" \
"v_mfma_f32_16x16x32_bf16 a[24:27], v[52:55], v[164:167], a[24:27]\n" \
"ds_read_b64_tr_b16 v[118:119], v32 offset:33792\n" \
"s_add_u32 m0, %[mA], 0x1400\n" \
"v_mfma_f32_16x16x32_bf16 a[56:59], v[60:63], v[164:167], a[56:59]\n" \
"s_add_u32 s84, s62, 0xa000\n" \
"ds_read_b64_tr_b16 v[128:129], v33 offset:40960\n" \
"v_mfma_f32_16x16x32_bf16 a[56:59], v[64:67], v[168:171], a[56:59]\n" \
"buffer_load_dwordx4 v23, s[64:67], s84 offen lds\n" \
"v_mfma_f32_16x16x32_bf16 a[88:91], v[72:75], v[168:171], a[88:91]\n" \
"s_add_u32 m0, %[mB], 0x800\n" \
"s_add_u32 s85, s63, 0x10000\n" \
"v_mfma_f32_16x16x32_bf16 a[88:91], v[68:71], v[164:167], a[88:91]\n" \
"ds_read_b64_tr_b16 v[130:131], v33 offset:41984\n" \
"ds_read_b64_tr_b16 v[124:125], v33 offset:32768\n" \
"v_mfma_f32_16x16x32_bf16 a[120:123], v[76:79], v[164:167], a[120:123]\n" \
"buffer_load_dwordx4 v27, s[68:71], s85 offen lds\n" \
"v_mfma_f32_16x16x32_bf16 a[120:123], v[80:83], v[168:171], a[120:123]\n" \
"ds_read_b64_tr_b16 v[126:127], v33 offset:33792\n" \
"s_waitcnt lgkmcnt(10)\n" \
"v_mfma_f32_16x16x32_bf16 a[28:31], v[56:59], v[176:179], a[28:31]\n" \
"s_add_u32 m0, %[mB], 0x4800\n" \
"s_waitcnt lgkmcnt(8)\n" \
"v_mfma_f32_16x16x32_bf16 a[28:31], v[52:55], v[172:175], a[28:31]\n" \
"s_add_u32 s84, s63, 0x10000\n" \
"ds_read_b64_tr_b16 v[136:137], v34 offset:40960\n" \
"v_mfma_f32_16x16x32_bf16 a[60:63], v[60:63], v[172:175], a[60:63]\n" \
"buffer_load_dwordx4 v27, s[72:75], s84 offen lds\n" \
"v_mfma_f32_16x16x32_bf16 a[60:63], v[64:67], v[176:179], a[60:63]\n" \
"ds_read_b64_tr_b16 v[138:139], v34 offset:41984\n" \
"ds_read_b64_tr_b16 v[132:133], v34 offset:32768\n" \
"v_mfma_f32_16x16x32_bf16 a[92:95], v[72:75], v[176:179], a[92:95]\n" \
"s_add_u32 m0, %[mA], 0x1800\n" \
"s_add_u32 s85, s62, 0xc000\n" \
"v_mfma_f32_16x16x32_bf16 a[92:95], v[68:71], v[172:175], a[92:95]\n" \
"buffer_load_dwordx4 v22, s[64:67], s85 offen lds\n" \
"v_mfma_f32_16x16x32_bf16 a[124:127], v[76:79], v[172:175], a[124:127]\n" \
"ds_read_b64_tr_b16 v[134:135], v34 offset:33792\n" \
"ds_read_b64_tr_b16 v[144:145], v35 offset:40960\n" \
"v_mfma_f32_16x16x32_bf16 a[124:127], v[80:83], v[176:179], a[124:127]\n" \
"s_add_u32 m0, %[mA], 0x1c00\n" \
"s_waitcnt lgkmcnt(9)\n" \
"s_nop 0\n" \
"v_mfma_f32_16x16x32_bf16 a[0:3], v[120:123], v[56:59], a[0:3]\n" \
"s_add_u32 s84, s62, 0xe000\n" \
"ds_read_b64_tr_b16 v[146:147], v35 offset:41984\n" \
"v_mfma_f32_16x16x32_bf16 a[0:3], v[116:119], v[52:55], a[0:3]\n" \
"buffer_load_dwordx4 v23, s[64:67], s84 offen lds\n" \
"v_mfma_f32_16x16x32_bf16 a[32:35], v[116:119], v[60:63], a[32:35]\n" \
"ds_read_b64_tr_b16 v[140:141], v35 offset:32768\n" \
"ds_read_b64_tr_b16 v[142:143], v35 offset:33792\n" \
"v_mfma_f32_16x16x32_bf16 a[32:35], v[120:123], v[64:67], a[32:35]\n" \
"s_add_u32 m0, %[mB], 0xc00\n" \
"s_add_u32 s85, s63, 0x18000\n" \
"v_mfma_f32_16x16x32_bf16 a[64:67], v[120:123], v[72:75], a[64:67]\n" \
"buffer_load_dwordx4 v27, s[68:71], s85 offen lds\n" \
"v_mfma_f32_16x16x32_bf16 a[64:67], v[116:119], v[68:71], a[64:67]\n" \
"ds_read_b128 v[88:91], v29 offset:32768\n" \
"ds_read_b128 v[84:87], v28 offset:32768\n" \
"v_mfma_f32_16x16x32_bf16 a[96:99], v[116:119], v[76:79], a[96:99]\n" \
"ds_read_b128 v[92:95], v28 offset:34816\n" \
"ds_read_b128 v[96:99], v29 offset:34816\n" \
"v_mfma_f32_16x16x32_bf16 a[96:99], v[120:123], v[80:83], a[96:99]\n" \
"s_add_u32 m0, %[mB], 0x4c00\n" \
"s_waitcnt lgkmcnt(11)\n" \
"v_mfma_f32_16x16x32_bf16 a[4:7], v[128:131], v[56:59], a[4:7]\n" \
"s_add_u32 s84, s63, 0x18000\n" \
"ds_read_b128 v[104:107], v29 offset:36864\n" \
"v_mfma_f32_16x16x32_bf16 a[4:7], v[124:127], v[52:55], a[4:7]\n" \
"buffer_load_dwordx4 v27, s[72:75], s84 offen lds\n" \
"v_mfma_f32_16x16x32_bf16 a[36:39], v[124:127], v[60:63], a[36:39]\n" \
"ds_read_b128 v[100:103], v28 offset:36864\n" \
"ds_read_b128 v[108:111], v28 offset:38912\n" \
"v_mfma_f32_16x16x32_bf16 a[36:39], v[128:131], v[64:67], a[36:39]\n" \
"s_mov_b32 m0, %[mS]\n" \
"s_mov_b32 s86, s18\n" \
"v_mfma_f32_16x16x32_bf16 a[68:71], v[128:131], v[72:75], a[68:71]\n" \
"s_mov_b32 s87, s19\n" \
"ds_read_b128 v[112:115], v29 offset:38912\n" \
"v_mfma_f32_16x16x32_bf16 a[68:71], v[124:127], v[68:71], a[68:71]\n" \
"v_writelane_b32 v11, s99, 2\n" \
"s_add_u32 s89, 0x80, s88\n" \
"v_mfma_f32_16x16x32_bf16 a[100:103], v[124:127], v[76:79], a[100:103]\n" \
"v_cndmask_b32_e32 v198, v226, v247, vcc\n" \
"v_and_b32_e32 v214, 7, v248\n" \
"v_mfma_f32_16x16x32_bf16 a[100:103], v[128:131], v[80:83], a[100:103]\n" \
"v_writelane_b32 v11, s89, 3\n" \
"s_waitcnt lgkmcnt(10)\n" \
"v_mfma_f32_16x16x32_bf16 a[8:11], v[136:139], v[56:59], a[8:11]\n" \
"s_lshl_b32 s88, s101, 8\n" \
"v_add_u32_e64 v228, v250, v214\n" \
"v_mfma_f32_16x16x32_bf16 a[8:11], v[132:135], v[52:55], a[8:11]\n" \
"v_cmp_gt_u32_e32 vcc, 36, v212\n" \
"s_add_u32 s95, 0x80, s99\n" \
"s_mov_b32 exec_lo, 0x3ffff\n" \
"s_mov_b32 exec_hi, 0x0\n" \
"global_load_lds_dword v16, s[86:87]\n" \
"s_mov_b64 exec, -1\n" \
"v_mfma_f32_16x16x32_bf16 a[40:43], v[132:135], v[60:63], a[40:43]\n" \
"s_add_u32 s39, 0x5be8000, s88\n" \
"s_lshl_b32 s89, s94, 2\n" \
"v_mfma_f32_16x16x32_bf16 a[40:43], v[136:139], v[64:67], a[40:43]\n" \
"v_cndmask_b32_e32 v218, v215, v228, vcc\n" \
"s_add_u32 s99, s39, s89\n" \
"v_mfma_f32_16x16x32_bf16 a[72:75], v[136:139], v[72:75], a[72:75]\n" \
"v_lshrrev_b32_e32 v212, 4, v20\n" \
"v_writelane_b32 v11, s95, 4\n" \
"v_mfma_f32_16x16x32_bf16 a[72:75], v[132:135], v[68:71], a[72:75]\n" \
"s_cmp_eq_u32 %[w], 3\n" \
"s_cselect_b32 s38, s99, 0x40000000\n" \
"v_mfma_f32_16x16x32_bf16 a[104:107], v[132:135], v[76:79], a[104:107]\n" \
"v_lshlrev_b32_e32 v213, 2, v212\n" \
"v_and_b32_e64 v215, 7, v212\n" \
"v_mfma_f32_16x16x32_bf16 a[104:107], v[136:139], v[80:83], a[104:107]\n" \
"s_add_u32 s101, s89, s100\n" \
"v_add_u32_e32 v214, s96, v213\n" \
"v_mfma_f32_16x16x32_bf16 a[12:15], v[144:147], v[56:59], a[12:15]\n" \
"s_add_u32 s95, 0x5be8020, s88\n" \
"s_waitcnt lgkmcnt(1)\n" \
"v_mfma_f32_16x16x32_bf16 a[12:15], v[140:143], v[52:55], a[12:15]\n" \
"v_lshlrev_b32_e32 v216, 2, v215\n" \
"s_mul_i32 s94, 0x90000, s101\n" \
"v_mfma_f32_16x16x32_bf16 a[44:47], v[140:143], v[60:63], a[44:47]\n" \
"v_add_u32_e64 v217, s95, v216\n" \
"s_add_u32 s99, 0x4240000, s94\n" \
"v_mfma_f32_16x16x32_bf16 a[44:47], v[144:147], v[64:67], a[44:47]\n" \
"s_lshl_b32 s96, %[w], 14\n" \
"s_add_u32 s88, s99, s96\n" \
"v_mfma_f32_16x16x32_bf16 a[76:79], v[144:147], v[72:75], a[76:79]\n" \
"s_lshl_b32 s95, s97, 16\n" \
"v_writelane_b32 v11, s39, 52\n" \
"v_mfma_f32_16x16x32_bf16 a[76:79], v[140:143], v[68:71], a[76:79]\n" \
"s_add_u32 s89, s88, s95\n" \
"s_lshl_b32 s101, s97, 11\n" \
"v_mfma_f32_16x16x32_bf16 a[108:111], v[140:143], v[76:79], a[108:111]\n" \
"v_writelane_b32 v11, s89, 37\n" \
"s_add_u32 s100, 0x3800, s89\n" \
"v_mfma_f32_16x16x32_bf16 a[108:111], v[144:147], v[80:83], a[108:111]\n" \
"s_add_u32 s94, s88, s101\n" \
"v_writelane_b32 v11, s100, 51\n" \
"v_mfma_f32_16x16x32_bf16 a[128:131], v[120:123], v[88:91], a[128:131]\n" \
"s_add_u32 s99, 0x80000, s94\n" \
"s_lshl_b32 s96, %[w], 17\n" \
"v_mfma_f32_16x16x32_bf16 a[128:131], v[116:119], v[84:87], a[128:131]\n" \
"v_writelane_b32 v11, s99, 49\n" \
"s_add_u32 s95, s88, s96\n" \
"v_mfma_f32_16x16x32_bf16 a[160:163], v[116:119], v[92:95], a[160:163]\n" \
"s_and_b32 s94, 1, s97\n" \
"s_add_u32 s89, s95, s98\n" \
"v_mfma_f32_16x16x32_bf16 a[160:163], v[120:123], v[96:99], a[160:163]\n" \
"s_lshl_b32 s99, s94, 10\n" \
"s_add_u32 s100, 0x3840, s89\n" \
"v_mfma_f32_16x16x32_bf16 a[192:195], v[120:123], v[104:107], a[192:195]\n" \
"s_add_u32 s96, s88, s99\n" \
"v_writelane_b32 v11, s100, 5\n" \
"v_mfma_f32_16x16x32_bf16 a[192:195], v[116:119], v[100:103], a[192:195]\n" \
"s_lshr_b32 s101, s97, 1\n" \
"s_add_u32 s95, 0x80000, s96\n" \
"v_mfma_f32_16x16x32_bf16 a[224:227], v[116:119], v[108:111], a[224:227]\n" \
"s_add_u32 s89, 1, s97\n" \
"s_waitcnt lgkmcnt(0)\n" \
"v_mfma_f32_16x16x32_bf16 a[224:227], v[120:123], v[112:115], a[224:227]\n" \
"s_cmp_eq_u32 %[w], s101\n" \
"s_cselect_b32 s98, s95, 0x40000000\n" \
"v_mfma_f32_16x16x32_bf16 a[132:135], v[128:131], v[88:91], a[132:135]\n" \
"s_and_b32 s100, 7, s89\n" \
"s_lshl_b32 s94, s100, 16\n" \
"v_mfma_f32_16x16x32_bf16 a[132:135], v[124:127], v[84:87], a[132:135]\n" \
"s_add_u32 s96, 2, s97\n" \
"v_writelane_b32 v11, s98, 38\n" \
"v_mfma_f32_16x16x32_bf16 a[164:167], v[124:127], v[92:95], a[164:167]\n" \
"s_add_u32 s99, s88, s94\n" \
"s_and_b32 s101, 7, s96\n" \
"v_mfma_f32_16x16x32_bf16 a[164:167], v[128:131], v[96:99], a[164:167]\n" \
"s_add_u32 s100, 3, s97\n" \
"v_writelane_b32 v11, s99, 42\n" \
"v_mfma_f32_16x16x32_bf16 a[196:199], v[128:131], v[104:107], a[196:199]\n" \
"s_lshl_b32 s95, s101, 16\n" \
"s_and_b32 s94, 7, s100\n" \
"v_mfma_f32_16x16x32_bf16 a[196:199], v[124:127], v[100:103], a[196:199]\n" \
"s_add_u32 s98, s88, s95\n" \
"s_lshl_b32 s99, s94, 16\n" \
"v_mfma_f32_16x16x32_bf16 a[228:231], v[124:127], v[108:111], a[228:231]\n" \
"s_add_u32 s89, 0x800, s98\n" \
"s_add_u32 s96, s88, s99\n" \
"v_mfma_f32_16x16x32_bf16 a[228:231], v[128:131], v[112:115], a[228:231]\n" \
"s_add_u32 s95, 4, s97\n" \
"v_writelane_b32 v11, s89, 43\n" \
"v_mfma_f32_16x16x32_bf16 a[136:139], v[136:139], v[88:91], a[136:139]\n" \
"s_add_u32 s101, 0x1000, s96\n" \
"s_and_b32 s98, 7, s95\n" \
"v_mfma_f32_16x16x32_bf16 a[136:139], v[132:135], v[84:87], a[136:139]\n" \
"s_add_u32 s99, 5, s97\n" \
"v_writelane_b32 v11, s101, 44\n" \
"v_mfma_f32_16x16x32_bf16 a[168:171], v[132:135], v[92:95], a[168:171]\n" \
"s_lshl_b32 s89, s98, 16\n" \
"s_and_b32 s96, 7, s99\n" \
"v_mfma_f32_16x16x32_bf16 a[168:171], v[136:139], v[96:99], a[168:171]\n" \
"s_add_u32 s100, s88, s89\n" \
"s_lshl_b32 s101, s96, 16\n" \
"v_mfma_f32_16x16x32_bf16 a[200:203], v[136:139], v[104:107], a[200:203]\n" \
"s_add_u32 s94, 0x1800, s100\n" \
"s_add_u32 s95, s88, s101\n" \
"v_mfma_f32_16x16x32_bf16 a[200:203], v[132:135], v[100:103], a[200:203]\n" \
"s_add_u32 s89, 6, s97\n" \
"v_writelane_b32 v11, s94, 45\n" \
"v_mfma_f32_16x16x32_bf16 a[232:235], v[132:135], v[108:111], a[232:235]\n" \
"s_add_u32 s98, 0x2000, s95\n" \
"s_and_b32 s100, 7, s89\n" \
"v_mfma_f32_16x16x32_bf16 a[232:235], v[136:139], v[112:115], a[232:235]\n" \
"s_add_u32 s101, 7, s97\n" \
"v_writelane_b32 v11, s98, 46\n" \
"v_mfma_f32_16x16x32_bf16 a[140:143], v[144:147], v[88:91], a[140:143]\n" \
"s_add_u32 s62, s22, 0x180\n" \
"s_and_b32 s62, s62, 0x3ff\n" \
"v_mfma_f32_16x16x32_bf16 a[140:143], v[140:143], v[84:87], a[140:143]\n" \
"s_lshl_b32 s63, s62, 12\n" \
"s_lshl_b32 s94, s100, 16\n" \
"v_mfma_f32_16x16x32_bf16 a[172:175], v[140:143], v[92:95], a[172:175]\n" \
"s_and_b32 s95, 7, s101\n" \
"s_add_u32 s99, s88, s94\n" \
"v_mfma_f32_16x16x32_bf16 a[172:175], v[144:147], v[96:99], a[172:175]\n" \
"s_lshl_b32 s98, s95, 16\n" \
"s_add_u32 s96, 0x2800, s99\n" \
"v_mfma_f32_16x16x32_bf16 a[204:207], v[144:147], v[104:107], a[204:207]\n" \
"s_add_u32 s89, s88, s98\n" \
"v_writelane_b32 v11, s96, 47\n" \
"v_mfma_f32_16x16x32_bf16 a[204:207], v[140:143], v[100:103], a[204:207]\n" \
"s_add_u32 s100, 0x3000, s89\n" \
"v_writelane_b32 v11, s100, 48\n" \
"v_mfma_f32_16x16x32_bf16 a[236:239], v[140:143], v[108:111], a[236:239]\n" \
"s_mov_b32 s87, 0x5040100\n" \
"v_lshlrev_b32_e32 v188, 7, v218\n" \
"v_mfma_f32_16x16x32_bf16 a[236:239], v[144:147], v[112:115], a[236:239]\n" \
"v_readfirstlane_b32 %[tag], v180\n" \
"s_lshr_b32 s94, %[b], 5\n" \
"s_waitcnt vmcnt(1)\n" \
"s_barrier\n" \
"v_mfma_f32_16x16x32_bf16 a[144:147], v[88:91], v[152:155], a[144:147]\n" \
"s_add_u32 m0, %[mA], 0x8000\n" \
"s_add_u32 s85, s62, 0x0\n" \
"s_nop 0\n" \
"v_mfma_f32_16x16x32_bf16 a[144:147], v[84:87], v[148:151], a[144:147]\n" \
"buffer_load_dwordx4 v22, s[64:67], s85 offen lds\n" \
"v_mfma_f32_16x16x32_bf16 a[176:179], v[92:95], v[148:151], a[176:179]\n" \
"s_add_u32 m0, %[mA], 0x8400\n" \
"s_add_u32 s84, s62, 0x2000\n" \
"v_mfma_f32_16x16x32_bf16 a[176:179], v[96:99], v[152:155], a[176:179]\n" \
"ds_read_b64_tr_b16 v[120:121], v32 offset:8192\n" \
"ds_read_b64_tr_b16 v[122:123], v32 offset:9216\n" \
"v_mfma_f32_16x16x32_bf16 a[208:211], v[104:107], v[152:155], a[208:211]\n" \
"buffer_load_dwordx4 v23, s[64:67], s84 offen lds\n" \
"v_mfma_f32_16x16x32_bf16 a[208:211], v[100:103], v[148:151], a[208:211]\n" \
"ds_read_b128 v[56:59], v29 offset:16384\n" \
"ds_read_b64_tr_b16 v[116:117], v32 offset:0\n" \
"v_mfma_f32_16x16x32_bf16 a[240:243], v[108:111], v[148:151], a[240:243]\n" \
"s_add_u32 m0, %[mB], 0x8000\n" \
"s_add_u32 s85, s63, 0x0\n" \
"v_mfma_f32_16x16x32_bf16 a[240:243], v[112:115], v[152:155], a[240:243]\n" \
"buffer_load_dwordx4 v26, s[68:71], s85 offen lds\n" \
"v_mfma_f32_16x16x32_bf16 a[148:151], v[88:91], v[160:163], a[148:151]\n" \
"ds_read_b64_tr_b16 v[118:119], v32 offset:1024\n" \
"ds_read_b128 v[52:55], v28 offset:16384\n" \
"v_mfma_f32_16x16x32_bf16 a[148:151], v[84:87], v[156:159], a[148:151]\n" \
"ds_read_b128 v[60:63], v28 offset:18432\n" \
"ds_read_b128 v[64:67], v29 offset:18432\n" \
"v_mfma_f32_16x16x32_bf16 a[180:183], v[92:95], v[156:159], a[180:183]\n" \
"s_add_u32 m0, %[mB], 0xc000\n" \
"s_add_u32 s84, s63, 0x0\n" \
"v_mfma_f32_16x16x32_bf16 a[180:183], v[96:99], v[160:163], a[180:183]\n" \
"buffer_load_dwordx4 v26, s[72:75], s84 offen lds\n" \
"v_mfma_f32_16x16x32_bf16 a[212:215], v[104:107], v[160:163], a[212:215]\n" \
"ds_read_b128 v[72:75], v29 offset:20480\n" \
"ds_read_b128 v[68:71], v28 offset:20480\n" \
"v_mfma_f32_16x16x32_bf16 a[212:215], v[100:103], v[156:159], a[212:215]\n" \
"ds_read_b128 v[76:79], v28 offset:22528\n" \
"ds_read_b128 v[80:83], v29 offset:22528\n" \
"v_mfma_f32_16x16x32_bf16 a[244:247], v[108:111], v[156:159], a[244:247]\n" \
"s_add_u32 m0, %[mA], 0x8800\n" \
"s_add_u32 s85, s62, 0x4000\n" \
"v_mfma_f32_16x16x32_bf16 a[244:247], v[112:115], v[160:163], a[244:247]\n" \
"buffer_load_dwordx4 v22, s[64:67], s85 offen lds\n" \
"v_mfma_f32_16x16x32_bf16 a[152:155], v[88:91], v[168:171], a[152:155]\n" \
"ds_read_b64_tr_b16 v[128:129], v33 offset:8192\n" \
"ds_read_b64_tr_b16 v[130:131], v33 offset:9216\n" \
"v_mfma_f32_16x16x32_bf16 a[152:155], v[84:87], v[164:167], a[152:155]\n" \
"s_add_u32 m0, %[mA], 0x8c00\n" \
"s_add_u32 s84, s62, 0x6000\n" \
"v_mfma_f32_16x16x32_bf16 a[184:187], v[92:95], v[164:167], a[184:187]\n" \
"buffer_load_dwordx4 v23, s[64:67], s84 offen lds\n" \
"v_mfma_f32_16x16x32_bf16 a[184:187], v[96:99], v[168:171], a[184:187]\n" \
"ds_read_b64_tr_b16 v[124:125], v33 offset:0\n" \
"ds_read_b64_tr_b16 v[126:127], v33 offset:1024\n" \
"v_mfma_f32_16x16x32_bf16 a[216:219], v[104:107], v[168:171], a[216:219]\n" \
"ds_read_b64_tr_b16 v[136:137], v34 offset:8192\n" \
"ds_read_b64_tr_b16 v[138:139], v34 offset:9216\n" \
"v_mfma_f32_16x16x32_bf16 a[216:219], v[100:103], v[164:167], a[216:219]\n" \
"s_add_u32 m0, %[mB], 0x8400\n" \
"s_add_u32 s85, s63, 0x8000\n" \
"v_mfma_f32_16x16x32_bf16 a[248:251], v[108:111], v[164:167], a[248:251]\n" \
"buffer_load_dwordx4 v26, s[68:71], s85 offen lds\n" \
"v_mfma_f32_16x16x32_bf16 a[248:251], v[112:115], v[168:171], a[248:251]\n" \
"ds_read_b64_tr_b16 v[132:133], v34 offset:0\n" \
"ds_read_b64_tr_b16 v[134:135], v34 offset:1024\n" \
"v_mfma_f32_16x16x32_bf16 a[156:159], v[88:91], v[176:179], a[156:159]\n" \
"ds_read_b64_tr_b16 v[144:145], v35 offset:8192\n" \
"ds_read_b64_tr_b16 v[146:147], v35 offset:9216\n" \
"v_mfma_f32_16x16x32_bf16 a[156:159], v[84:87], v[172:175], a[156:159]\n" \
"s_add_u32 m0, %[mB], 0xc400\n" \
"s_add_u32 s84, s63, 0x8000\n" \
"v_mfma_f32_16x16x32_bf16 a[188:191], v[92:95], v[172:175], a[188:191]\n" \
"buffer_load_dwordx4 v26, s[72:75], s84 offen lds\n" \
"v_mfma_f32_16x16x32_bf16 a[188:191], v[96:99], v[176:179], a[188:191]\n" \
"ds_read_b64_tr_b16 v[140:141], v35 offset:0\n" \
"ds_read_b64_tr_b16 v[142:143], v35 offset:1024\n" \
"v_mfma_f32_16x16x32_bf16 a[220:223], v[104:107], v[176:179], a[220:223]\n" \
"ds_read_b64_tr_b16 v[152:153], v32 offset:24576\n" \
"ds_read_b64_tr_b16 v[154:155], v32 offset:25600\n" \
"v_mfma_f32_16x16x32_bf16 a[220:223], v[100:103], v[172:175], a[220:223]\n" \
"s_add_u32 m0, %[mA], 0x9000\n" \
"s_add_u32 s85, s62, 0x8000\n" \
"v_mfma_f32_16x16x32_bf16 a[252:255], v[108:111], v[172:175], a[252:255]\n" \
"buffer_load_dwordx4 v22, s[64:67], s85 offen lds\n" \
"v_mfma_f32_16x16x32_bf16 a[252:255], v[112:115], v[176:179], a[252:255]\n" \
"ds_read_b64_tr_b16 v[148:149], v32 offset:16384\n" \
"s_waitcnt lgkmcnt(13)\n" \
"s_nop 0\n" \
"v_mfma_f32_16x16x32_bf16 a[0:3], v[120:123], v[56:59], a[0:3]\n" \
"s_add_u32 m0, %[mA], 0x9400\n" \
"s_add_u32 s84, s62, 0xa000\n" \
"v_mfma_f32_16x16x32_bf16 a[0:3], v[116:119], v[52:55], a[0:3]\n" \
"buffer_load_dwordx4 v23, s[64:67], s84 offen lds\n" \
"v_mfma_f32_16x16x32_bf16 a[32:35], v[116:119], v[60:63], a[32:35]\n" \
"ds_read_b64_tr_b16 v[150:151], v32 offset:17408\n" \
"ds_read_b64_tr_b16 v[160:161], v33 offset:24576\n" \
"v_mfma_f32_16x16x32_bf16 a[32:35], v[120:123], v[64:67], a[32:35]\n" \
"ds_read_b64_tr_b16 v[162:163], v33 offset:25600\n" \
"ds_read_b64_tr_b16 v[156:157], v33 offset:16384\n" \
"v_mfma_f32_16x16x32_bf16 a[64:67], v[120:123], v[72:75], a[64:67]\n" \
"s_add_u32 m0, %[mB], 0x8800\n" \
"s_add_u32 s85, s63, 0x10000\n" \
"v_mfma_f32_16x16x32_bf16 a[64:67], v[116:119], v[68:71], a[64:67]\n" \
"buffer_load_dwordx4 v27, s[68:71], s85 offen lds\n" \
"v_mfma_f32_16x16x32_bf16 a[96:99], v[116:119], v[76:79], a[96:99]\n" \
"ds_read_b64_tr_b16 v[158:159], v33 offset:17408\n" \
"ds_read_b64_tr_b16 v[168:169], v34 offset:24576\n" \
"v_mfma_f32_16x16x32_bf16 a[96:99], v[120:123], v[80:83], a[96:99]\n" \
"ds_read_b64_tr_b16 v[170:171], v34 offset:25600\n" \
"ds_read_b64_tr_b16 v[164:165], v34 offset:16384\n" \
"v_mfma_f32_16x16x32_bf16 a[4:7], v[128:131], v[56:59], a[4:7]\n" \
"s_add_u32 m0, %[mB], 0xc800\n" \
"s_waitcnt lgkmcnt(13)\n" \
"s_nop 0\n" \
"v_mfma_f32_16x16x32_bf16 a[4:7], v[124:127], v[52:55], a[4:7]\n" \
"s_add_u32 s84, s63, 0x10000\n" \
"ds_read_b64_tr_b16 v[166:167], v34 offset:17408\n" \
"v_mfma_f32_16x16x32_bf16 a[36:39], v[124:127], v[60:63], a[36:39]\n" \
"buffer_load_dwordx4 v27, s[72:75], s84 offen lds\n" \
"v_mfma_f32_16x16x32_bf16 a[36:39], v[128:131], v[64:67], a[36:39]\n" \
"ds_read_b64_tr_b16 v[176:177], v35 offset:24576\n" \
"ds_read_b64_tr_b16 v[178:179], v35 offset:25600\n" \
"v_mfma_f32_16x16x32_bf16 a[68:71], v[128:131], v[72:75], a[68:71]\n" \
"s_add_u32 m0, %[mA], 0x9800\n" \
"s_add_u32 s85, s62, 0xc000\n" \
"v_mfma_f32_16x16x32_bf16 a[68:71], v[124:127], v[68:71], a[68:71]\n" \
"buffer_load_dwordx4 v22, s[64:67], s85 offen lds\n" \
"v_mfma_f32_16x16x32_bf16 a[100:103], v[124:127], v[76:79], a[100:103]\n" \
"ds_read_b64_tr_b16 v[172:173], v35 offset:16384\n" \
"ds_read_b64_tr_b16 v[174:175], v35 offset:17408\n" \
"v_mfma_f32_16x16x32_bf16 a[100:103], v[128:131], v[80:83], a[100:103]\n" \
"s_add_u32 m0, %[mA], 0x9c00\n" \
"s_add_u32 s84, s62, 0xe000\n" \
"v_mfma_f32_16x16x32_bf16 a[8:11], v[136:139], v[56:59], a[8:11]\n" \
"buffer_load_dwordx4 v23, s[64:67], s84 offen lds\n" \
"v_mfma_f32_16x16x32_bf16 a[8:11], v[132:135], v[52:55], a[8:11]\n" \
"ds_read_b128 v[88:91], v29 offset:0\n" \
"ds_read_b128 v[84:87], v28 offset:0\n" \
"v_mfma_f32_16x16x32_bf16 a[40:43], v[132:135], v[60:63], a[40:43]\n" \
"ds_read_b128 v[92:95], v28 offset:2048\n" \
"ds_read_b128 v[96:99], v29 offset:2048\n" \
"v_mfma_f32_16x16x32_bf16 a[40:43], v[136:139], v[64:67], a[40:43]\n" \
"s_add_u32 m0, %[mB], 0x8c00\n" \
"s_add_u32 s85, s63, 0x18000\n" \
"v_mfma_f32_16x16x32_bf16 a[72:75], v[136:139], v[72:75], a[72:75]\n" \
"buffer_load_dwordx4 v27, s[68:71], s85 offen lds\n" \
"v_mfma_f32_16x16x32_bf16 a[72:75], v[132:135], v[68:71], a[72:75]\n" \
"ds_read_b128 v[104:107], v29 offset:4096\n" \
"ds_read_b128 v[100:103], v28 offset:4096\n" \
"v_mfma_f32_16x16x32_bf16 a[104:107], v[132:135], v[76:79], a[104:107]\n" \
"ds_read_b128 v[108:111], v28 offset:6144\n" \
"ds_read_b128 v[112:115], v29 offset:6144\n" \
"v_mfma_f32_16x16x32_bf16 a[104:107], v[136:139], v[80:83], a[104:107]\n" \
"s_add_u32 m0, %[mB], 0xcc00\n" \
"s_add_u32 s84, s63, 0x18000\n" \
"v_mfma_f32_16x16x32_bf16 a[12:15], v[144:147], v[56:59], a[12:15]\n" \
"s_add_u32 %[tag], %[tag], 0x9E3779B1\n" \
"s_waitcnt lgkmcnt(12)\n" \
"v_mfma_f32_16x16x32_bf16 a[12:15], v[140:143], v[52:55], a[12:15]\n" \
"buffer_load_dwordx4 v27, s[72:75], s84 offen lds\n" \
"v_mfma_f32_16x16x32_bf16 a[44:47], v[140:143], v[60:63], a[44:47]\n" \
"v_mov_b32_e32 v45, %[tag]\n" \
"s_sub_u32 s95, 15, s94\n" \
"v_mfma_f32_16x16x32_bf16 a[44:47], v[144:147], v[64:67], a[44:47]\n" \
"s_cmp_lt_u32 %[w], 2\n" \
"s_cselect_b32 s96, s95, s94\n" \
"v_mfma_f32_16x16x32_bf16 a[76:79], v[144:147], v[72:75], a[76:79]\n" \
"buffer_load_dwordx4 v[200:203], v192, s[76:79], s56 offen offset:0\n" \
"v_mfma_f32_16x16x32_bf16 a[76:79], v[140:143], v[68:71], a[76:79]\n" \
"s_lshl_b32 s97, s96, 7\n" \
"s_add_u32 s98, 0xfffffff0, s97\n" \
"v_mfma_f32_16x16x32_bf16 a[108:111], v[140:143], v[76:79], a[108:111]\n" \
"s_cmp_eq_u32 s97, 0\n" \
"s_cselect_b32 s99, 0, s98\n" \
"v_mfma_f32_16x16x32_bf16 a[108:111], v[144:147], v[80:83], a[108:111]\n" \
"s_and_b32 s100, 1, %[w]\n" \
"v_cmp_gt_u32_e32 vcc, 32, v212\n" \
"v_mfma_f32_16x16x32_bf16 a[16:19], v[56:59], v[152:155], a[16:19]\n" \
"s_lshl_b32 s101, s100, 3\n" \
"s_add_u32 s88, s99, s101\n" \
"v_mfma_f32_16x16x32_bf16 a[16:19], v[52:55], v[148:151], a[16:19]\n" \
"buffer_load_dwordx4 v[204:207], v193, s[76:79], s56 offen offset:0\n" \
"v_mfma_f32_16x16x32_bf16 a[48:51], v[60:63], v[148:151], a[48:51]\n" \
"v_lshrrev_b32_e32 v212, 4, v20\n" \
"v_cndmask_b32_e32 v17, v217, v214, vcc\n" \
"v_mfma_f32_16x16x32_bf16 a[48:51], v[64:67], v[152:155], a[48:51]\n" \
"v_lshrrev_b32_e32 v213, 3, v212\n" \
"s_lshr_b32 s99, %[b], 3\n" \
"v_mfma_f32_16x16x32_bf16 a[80:83], v[72:75], v[152:155], a[80:83]\n" \
"v_add_u32_e32 v214, s88, v213\n" \
"s_lshl_b32 s96, s100, 6\n" \
"v_mfma_f32_16x16x32_bf16 a[80:83], v[68:71], v[148:151], a[80:83]\n" \
"s_and_b32 s101, 3, s99\n" \
"s_add_u32 s98, s97, s96\n" \
"v_mfma_f32_16x16x32_bf16 a[112:115], v[76:79], v[148:151], a[112:115]\n" \
"s_add_u32 s88, s101, s94\n" \
"v_add_u32_e64 v221, s98, v213\n" \
"v_mfma_f32_16x16x32_bf16 a[112:115], v[80:83], v[152:155], a[112:115]\n" \
"buffer_load_dword v208, v194, s[76:79], s56 offen offset:32\n" \
"v_mfma_f32_16x16x32_bf16 a[20:23], v[56:59], v[160:163], a[20:23]\n" \
"s_add_u32 s96, 0xfffffff7, s88\n" \
"s_lshl_b32 s89, s100, 2\n" \
"v_mfma_f32_16x16x32_bf16 a[20:23], v[52:55], v[156:159], a[20:23]\n" \
"s_cmp_lt_u32 s88, 9\n" \
"s_cselect_b32 s98, s88, s96\n" \
"v_mfma_f32_16x16x32_bf16 a[52:55], v[60:63], v[156:159], a[52:55]\n" \
"s_add_u32 s88, s97, s100\n" \
"s_lshl_b32 s23, s98, 7\n" \
"v_mfma_f32_16x16x32_bf16 a[52:55], v[64:67], v[160:163], a[52:55]\n" \
"s_cmp_eq_u32 s88, 0\n" \
"s_cselect_b32 s49, 0xff800000, 0\n" \
"v_mfma_f32_16x16x32_bf16 a[84:87], v[72:75], v[160:163], a[84:87]\n" \
"s_lshr_b32 s96, s97, 4\n" \
"v_lshrrev_b32_e32 v217, 4, v212\n" \
"v_mfma_f32_16x16x32_bf16 a[84:87], v[68:71], v[156:159], a[84:87]\n" \
"buffer_load_dword v209, v194, s[76:79], s56 offen offset:1184\n" \
"v_mfma_f32_16x16x32_bf16 a[116:119], v[76:79], v[156:159], a[116:119]\n" \
"s_add_u32 s98, s96, s89\n" \
"v_add_u32_e64 v218, s89, v217\n" \
"v_mfma_f32_16x16x32_bf16 a[116:119], v[80:83], v[160:163], a[116:119]\n" \
"s_mul_i32 s88, 0x24800, s98\n" \
"s_and_b32 s96, 7, %[b]\n" \
"v_mfma_f32_16x16x32_bf16 a[24:27], v[56:59], v[168:171], a[24:27]\n" \
"s_add_u32 s97, 0x3000000, s88\n" \
"s_lshl_b32 s89, s96, 8\n" \
"v_mfma_f32_16x16x32_bf16 a[24:27], v[52:55], v[164:167], a[24:27]\n" \
"s_add_u32 s98, s97, s89\n" \
"s_lshl_b32 s88, s101, 2\n" \
"v_mfma_f32_16x16x32_bf16 a[56:59], v[60:63], v[164:167], a[56:59]\n" \
"v_and_b32_e32 v216, 7, v212\n" \
"s_add_u32 s89, s98, s88\n" \
"v_mfma_f32_16x16x32_bf16 a[56:59], v[64:67], v[168:171], a[56:59]\n" \
"buffer_load_dword v211, v194, s[76:79], s56 offen offset:2336\n" \
"v_mfma_f32_16x16x32_bf16 a[88:91], v[72:75], v[168:171], a[88:91]\n" \
"s_mov_b32 s76, s14\n" \
"s_and_b32 s77, s15, 0xffff\n" \
"v_mfma_f32_16x16x32_bf16 a[88:91], v[68:71], v[164:167], a[88:91]\n" \
"s_mov_b32 s78, 0x480\n" \
"s_add_u32 s56, s57, 0x1000\n" \
"v_mfma_f32_16x16x32_bf16 a[120:123], v[76:79], v[164:167], a[120:123]\n" \
"v_xor_b32_e32 v219, v216, v218\n" \
"s_lshl_b32 s98, s96, 2\n" \
"v_mfma_f32_16x16x32_bf16 a[120:123], v[80:83], v[168:171], a[120:123]\n" \
"v_mul_u32_u24_e32 v215, 0x480, v214\n" \
"s_waitcnt lgkmcnt(0)\n" \
"v_mfma_f32_16x16x32_bf16 a[28:31], v[56:59], v[176:179], a[28:31]\n" \
"v_lshlrev_b32_e32 v220, 4, v219\n" \
"v_lshrrev_b32_e32 v229, 2, v212\n" \
"v_mfma_f32_16x16x32_bf16 a[28:31], v[52:55], v[172:175], a[28:31]\n" \
"buffer_load_ushort v210, v195, s[76:79], 0 offen\n" \
"v_mfma_f32_16x16x32_bf16 a[60:63], v[60:63], v[172:175], a[60:63]\n" \
"v_and_b32_e32 v234, 3, v212\n" \
"v_and_b32_e32 v249, 2, v212\n" \
"v_mfma_f32_16x16x32_bf16 a[60:63], v[64:67], v[176:179], a[60:63]\n" \
"s_add_u32 s51, 0x4000, s89\n" \
"s_add_u32 s88, s98, s101\n" \
"v_mfma_f32_16x16x32_bf16 a[92:95], v[72:75], v[176:179], a[92:95]\n" \
"v_add_u32_e32 v21, v215, v220\n" \
"v_add_u32_e64 v225, 4, v217\n" \
"v_mfma_f32_16x16x32_bf16 a[92:95], v[68:71], v[172:175], a[92:95]\n" \
"s_lshl_b32 s89, s88, 9\n" \
"s_lshl_b32 s98, s99, 2\n" \
"v_mfma_f32_16x16x32_bf16 a[124:127], v[76:79], v[172:175], a[124:127]\n" \
"v_lshrrev_b32_e32 v212, 4, v20\n" \
"v_xor_b32_e32 v223, v216, v217\n" \
"L_pf1:\n" \
"s_cbranch_execz L_pf5\n" \
"s_nop 0\n" \
"v_mfma_f32_16x16x32_bf16 a[124:127], v[80:83], v[176:179], a[124:127]\n" \
"v_xor_b32_e64 v226, v216, v225\n" \
"v_mov_b32_e32 v250, 0xeeeeeeee\n" \
"v_mfma_f32_16x16x32_bf16 a[144:147], v[88:91], v[152:155], a[144:147]\n" \
"v_mov_b32_e32 v251, 0x44444444\n" \
"v_cmp_ne_u32_e32 vcc, 0, v249\n" \
"v_mfma_f32_16x16x32_bf16 a[144:147], v[84:87], v[148:151], a[144:147]\n" \
"s_add_u32 s50, s97, s89\n" \
"s_lshl_b32 s96, %[w], 10\n" \
"v_mfma_f32_16x16x32_bf16 a[176:179], v[92:95], v[148:151], a[176:179]\n" \
"s_add_u32 s101, s98, %[w]\n" \
"v_and_b32_e64 v215, 15, v212\n" \
"v_mfma_f32_16x16x32_bf16 a[176:179], v[96:99], v[152:155], a[176:179]\n" \
"v_cndmask_b32_e32 v50, v251, v250, vcc\n" \
"s_add_u32 s40, 0x14000, s96\n" \
"v_mfma_f32_16x16x32_bf16 a[208:211], v[104:107], v[152:155], a[208:211]\n" \
"s_lshl_b32 s41, s101, 7\n" \
"s_lshl_b32 s97, s88, 20\n" \
"v_mfma_f32_16x16x32_bf16 a[208:211], v[100:103], v[148:151], a[208:211]\n" \
"v_lshrrev_b32_e32 v213, 4, v212\n" \
"v_and_b32_e64 v216, 1, v215\n" \
"v_mfma_f32_16x16x32_bf16 a[240:243], v[108:111], v[148:151], a[240:243]\n" \
"v_lshlrev_b32_e32 v228, 3, v217\n" \
"v_and_b32_e32 v238, 1, v217\n" \
"v_mfma_f32_16x16x32_bf16 a[240:243], v[112:115], v[152:155], a[240:243]\n" \
"v_lshlrev_b32_e32 v242, 4, v217\n" \
"s_add_u32 s89, 0x1000000, s97\n" \
"v_mfma_f32_16x16x32_bf16 a[148:151], v[88:91], v[160:163], a[148:151]\n" \
"s_add_u32 s62, s22, 0x200\n" \
"s_and_b32 s62, s62, 0x3ff\n" \
"v_mfma_f32_16x16x32_bf16 a[148:151], v[84:87], v[156:159], a[148:151]\n" \
"s_lshl_b32 s63, s62, 12\n" \
"s_lshl_b32 s96, %[w], 12\n" \
"v_mfma_f32_16x16x32_bf16 a[180:183], v[92:95], v[156:159], a[180:183]\n" \
"s_lshr_b32 s101, %[w], 1\n" \
"v_lshlrev_b32_e32 v214, 2, v213\n" \
"v_mfma_f32_16x16x32_bf16 a[180:183], v[96:99], v[160:163], a[180:183]\n" \
"v_mov_b32_e32 v218, 0x1000c0c\n" \
"v_mov_b32_e32 v219, 0xc0c0100\n" \
"v_mfma_f32_16x16x32_bf16 a[212:215], v[104:107], v[160:163], a[212:215]\n" \
"v_cmp_ne_u32_e32 vcc, 0, v216\n" \
"v_mul_u32_u24_e32 v222, 0x480, v221\n" \
"v_mfma_f32_16x16x32_bf16 a[212:215], v[100:103], v[156:159], a[212:215]\n" \
"s_add_u32 s52, s89, s96\n" \
"s_lshl_b32 s99, %[w], 11\n" \
"v_mfma_f32_16x16x32_bf16 a[244:247], v[108:111], v[156:159], a[244:247]\n" \
"s_lshl_b32 s88, s101, 15\n" \
"v_add_u32_e32 v217, v214, v216\n" \
"v_mfma_f32_16x16x32_bf16 a[244:247], v[112:115], v[160:163], a[244:247]\n" \
"v_cndmask_b32_e32 v220, v219, v218, vcc\n" \
"v_lshlrev_b32_e32 v224, 4, v223\n" \
"s_waitcnt vmcnt(6)\n" \
"s_barrier\n" \
"v_mfma_f32_16x16x32_bf16 a[152:155], v[88:91], v[168:171], a[152:155]\n" \
"s_add_u32 m0, %[mA], 0x0\n" \
"s_add_u32 s85, s62, 0x0\n" \
"v_mfma_f32_16x16x32_bf16 a[152:155], v[84:87], v[164:167], a[152:155]\n" \
"buffer_load_dwordx4 v22, s[64:67], s85 offen lds\n" \
"v_mfma_f32_16x16x32_bf16 a[184:187], v[92:95], v[164:167], a[184:187]\n" \
"s_add_u32 m0, %[mA], 0x400\n" \
"s_add_u32 s84, s62, 0x2000\n" \
"v_mfma_f32_16x16x32_bf16 a[184:187], v[96:99], v[168:171], a[184:187]\n" \
"ds_read_b64_tr_b16 v[152:153], v32 offset:57344\n" \
"ds_read_b64_tr_b16 v[154:155], v32 offset:58368\n" \
"v_mfma_f32_16x16x32_bf16 a[216:219], v[104:107], v[168:171], a[216:219]\n" \
"buffer_load_dwordx4 v23, s[64:67], s84 offen lds\n" \
"v_mfma_f32_16x16x32_bf16 a[216:219], v[100:103], v[164:167], a[216:219]\n" \
"ds_read_b128 v[56:59], v29 offset:49152\n" \
"ds_read_b64_tr_b16 v[148:149], v32 offset:49152\n" \
"v_mfma_f32_16x16x32_bf16 a[248:251], v[108:111], v[164:167], a[248:251]\n" \
"s_add_u32 m0, %[mB], 0x0\n" \
"s_add_u32 s85, s63, 0x0\n" \
"v_mfma_f32_16x16x32_bf16 a[248:251], v[112:115], v[168:171], a[248:251]\n" \
"buffer_load_dwordx4 v26, s[68:71], s85 offen lds\n" \
"v_mfma_f32_16x16x32_bf16 a[156:159], v[88:91], v[176:179], a[156:159]\n" \
"ds_read_b64_tr_b16 v[150:151], v32 offset:50176\n" \
"ds_read_b128 v[52:55], v28 offset:49152\n" \
"v_mfma_f32_16x16x32_bf16 a[156:159], v[84:87], v[172:175], a[156:159]\n" \
"ds_read_b128 v[60:63], v28 offset:51200\n" \
"ds_read_b128 v[64:67], v29 offset:51200\n" \
"v_mfma_f32_16x16x32_bf16 a[188:191], v[92:95], v[172:175], a[188:191]\n" \
"s_add_u32 m0, %[mB], 0x4000\n" \
"s_add_u32 s84, s63, 0x0\n" \
"s_nop 0\n" \
"v_mfma_f32_16x16x32_bf16 a[188:191], v[96:99], v[176:179], a[188:191]\n" \
"buffer_load_dwordx4 v26, s[72:75], s84 offen lds\n" \
"v_mfma_f32_16x16x32_bf16 a[220:223], v[104:107], v[176:179], a[220:223]\n" \
"ds_read_b128 v[72:75], v29 offset:53248\n" \
"ds_read_b128 v[68:71], v28 offset:53248\n" \
"v_mfma_f32_16x16x32_bf16 a[220:223], v[100:103], v[172:175], a[220:223]\n" \
"ds_read_b128 v[76:79], v28 offset:55296\n" \
"ds_read_b128 v[80:83], v29 offset:55296\n" \
"v_mfma_f32_16x16x32_bf16 a[252:255], v[108:111], v[172:175], a[252:255]\n" \
"s_add_u32 m0, %[mA], 0x800\n" \
"s_add_u32 s85, s62, 0x4000\n" \
"v_mfma_f32_16x16x32_bf16 a[252:255], v[112:115], v[176:179], a[252:255]\n" \
"buffer_load_dwordx4 v22, s[64:67], s85 offen lds\n" \
"v_mfma_f32_16x16x32_bf16 a[128:131], v[120:123], v[88:91], a[128:131]\n" \
"ds_read_b64_tr_b16 v[160:161], v33 offset:57344\n" \
"ds_read_b64_tr_b16 v[162:163], v33 offset:58368\n" \
"v_mfma_f32_16x16x32_bf16 a[128:131], v[116:119], v[84:87], a[128:131]\n" \
"s_add_u32 m0, %[mA], 0xc00\n" \
"s_add_u32 s84, s62, 0x6000\n" \
"v_mfma_f32_16x16x32_bf16 a[160:163], v[116:119], v[92:95], a[160:163]\n" \
"buffer_load_dwordx4 v23, s[64:67], s84 offen lds\n" \
"v_mfma_f32_16x16x32_bf16 a[160:163], v[120:123], v[96:99], a[160:163]\n" \
"ds_read_b64_tr_b16 v[156:157], v33 offset:49152\n" \
"ds_read_b64_tr_b16 v[158:159], v33 offset:50176\n" \
"v_mfma_f32_16x16x32_bf16 a[192:195], v[120:123], v[104:107], a[192:195]\n" \
"ds_read_b64_tr_b16 v[168:169], v34 offset:57344\n" \
"ds_read_b64_tr_b16 v[170:171], v34 offset:58368\n" \
"v_mfma_f32_16x16x32_bf16 a[192:195], v[116:119], v[100:103], a[192:195]\n" \
"s_add_u32 m0, %[mB], 0x400\n" \
"s_add_u32 s85, s63, 0x8000\n" \
"v_mfma_f32_16x16x32_bf16 a[224:227], v[116:119], v[108:111], a[224:227]\n" \
"buffer_load_dwordx4 v26, s[68:71], s85 offen lds\n" \
"v_mfma_f32_16x16x32_bf16 a[224:227], v[120:123], v[112:115], a[224:227]\n" \
"ds_read_b64_tr_b16 v[164:165], v34 offset:49152\n" \
"ds_read_b64_tr_b16 v[166:167], v34 offset:50176\n" \
"v_mfma_f32_16x16x32_bf16 a[132:135], v[128:131], v[88:91], a[132:135]\n" \
"ds_read_b64_tr_b16 v[176:177], v35 offset:57344\n" \
"ds_read_b64_tr_b16 v[178:179], v35 offset:58368\n" \
"v_mfma_f32_16x16x32_bf16 a[132:135], v[124:127], v[84:87], a[132:135]\n" \
"s_add_u32 m0, %[mB], 0x4400\n" \
"s_add_u32 s84, s63, 0x8000\n" \
"v_mfma_f32_16x16x32_bf16 a[164:167], v[124:127], v[92:95], a[164:167]\n" \
"buffer_load_dwordx4 v26, s[72:75], s84 offen lds\n" \
"v_mfma_f32_16x16x32_bf16 a[164:167], v[128:131], v[96:99], a[164:167]\n" \
"ds_read_b64_tr_b16 v[172:173], v35 offset:49152\n" \
"ds_read_b64_tr_b16 v[174:175], v35 offset:50176\n" \
"v_mfma_f32_16x16x32_bf16 a[196:199], v[128:131], v[104:107], a[196:199]\n" \
"ds_read_b64_tr_b16 v[120:121], v32 offset:40960\n" \
"ds_read_b64_tr_b16 v[122:123], v32 offset:41984\n" \
"v_mfma_f32_16x16x32_bf16 a[196:199], v[124:127], v[100:103], a[196:199]\n" \
"s_add_u32 m0, %[mA], 0x1000\n" \
"s_add_u32 s85, s62, 0x8000\n" \
"v_mfma_f32_16x16x32_bf16 a[228:231], v[124:127], v[108:111], a[228:231]\n" \
"buffer_load_dwordx4 v22, s[64:67], s85 offen lds\n" \
"v_mfma_f32_16x16x32_bf16 a[228:231], v[128:131], v[112:115], a[228:231]\n" \
"ds_read_b64_tr_b16 v[116:117], v32 offset:32768\n" \
"ds_read_b64_tr_b16 v[118:119], v32 offset:33792\n" \
"v_mfma_f32_16x16x32_bf16 a[136:139], v[136:139], v[88:91], a[136:139]\n" \
"s_add_u32 m0, %[mA], 0x1400\n" \
"s_add_u32 s84, s62, 0xa000\n" \
"v_mfma_f32_16x16x32_bf16 a[136:139], v[132:135], v[84:87], a[136:139]\n" \
"buffer_load_dwordx4 v23, s[64:67], s84 offen lds\n" \
"v_mfma_f32_16x16x32_bf16 a[168:171], v[132:135], v[92:95], a[168:171]\n" \
"ds_read_b64_tr_b16 v[128:129], v33 offset:40960\n" \
"ds_read_b64_tr_b16 v[130:131], v33 offset:41984\n" \
"v_mfma_f32_16x16x32_bf16 a[168:171], v[136:139], v[96:99], a[168:171]\n" \
"ds_read_b64_tr_b16 v[124:125], v33 offset:32768\n" \
"ds_read_b64_tr_b16 v[126:127], v33 offset:33792\n" \
"v_mfma_f32_16x16x32_bf16 a[200:203], v[136:139], v[104:107], a[200:203]\n" \
"s_add_u32 m0, %[mB], 0x800\n" \
"s_add_u32 s85, s63, 0x10000\n" \
"v_mfma_f32_16x16x32_bf16 a[200:203], v[132:135], v[100:103], a[200:203]\n" \
"buffer_load_dwordx4 v27, s[68:71], s85 offen lds\n" \
"v_mfma_f32_16x16x32_bf16 a[232:235], v[132:135], v[108:111], a[232:235]\n" \
"ds_read_b64_tr_b16 v[132:133], v34 offset:32768\n" \
"ds_read_b64_tr_b16 v[134:135], v34 offset:33792\n" \
"v_mfma_f32_16x16x32_bf16 a[232:235], v[136:139], v[112:115], a[232:235]\n" \
"ds_read_b64_tr_b16 v[136:137], v34 offset:40960\n" \
"ds_read_b64_tr_b16 v[138:139], v34 offset:41984\n" \
"v_mfma_f32_16x16x32_bf16 a[140:143], v[144:147], v[88:91], a[140:143]\n" \
"s_add_u32 m0, %[mB], 0x4800\n" \
"s_add_u32 s84, s63, 0x10000\n" \
"v_mfma_f32_16x16x32_bf16 a[140:143], v[140:143], v[84:87], a[140:143]\n" \
"buffer_load_dwordx4 v27, s[72:75], s84 offen lds\n" \
"v_mfma_f32_16x16x32_bf16 a[172:175], v[140:143], v[92:95], a[172:175]\n" \
"ds_read_b128 v[84:87], v28 offset:32768\n" \
"ds_read_b128 v[88:91], v29 offset:32768\n" \
"v_mfma_f32_16x16x32_bf16 a[172:175], v[144:147], v[96:99], a[172:175]\n" \
"ds_read_b128 v[92:95], v28 offset:34816\n" \
"ds_read_b128 v[96:99], v29 offset:34816\n" \
"v_mfma_f32_16x16x32_bf16 a[204:207], v[144:147], v[104:107], a[204:207]\n" \
"s_add_u32 m0, %[mA], 0x1800\n" \
"s_add_u32 s85, s62, 0xc000\n" \
"v_mfma_f32_16x16x32_bf16 a[204:207], v[140:143], v[100:103], a[204:207]\n" \
"buffer_load_dwordx4 v22, s[64:67], s85 offen lds\n" \
"v_mfma_f32_16x16x32_bf16 a[236:239], v[140:143], v[108:111], a[236:239]\n" \
"ds_read_b64_tr_b16 v[140:141], v35 offset:32768\n" \
"ds_read_b64_tr_b16 v[142:143], v35 offset:33792\n" \
"v_mfma_f32_16x16x32_bf16 a[236:239], v[144:147], v[112:115], a[236:239]\n" \
"s_add_u32 m0, %[mA], 0x1c00\n" \
"s_waitcnt lgkmcnt(14)\n" \
"s_nop 0\n" \
"v_mfma_f32_16x16x32_bf16 a[16:19], v[56:59], v[152:155], a[16:19]\n" \
"s_add_u32 s84, s62, 0xe000\n" \
"ds_read_b64_tr_b16 v[144:145], v35 offset:40960\n" \
"v_mfma_f32_16x16x32_bf16 a[16:19], v[52:55], v[148:151], a[16:19]\n" \
"buffer_load_dwordx4 v23, s[64:67], s84 offen lds\n" \
"v_mfma_f32_16x16x32_bf16 a[48:51], v[60:63], v[148:151], a[48:51]\n" \
"ds_read_b64_tr_b16 v[146:147], v35 offset:41984\n" \
"ds_read_b128 v[100:103], v28 offset:36864\n" \
"v_mfma_f32_16x16x32_bf16 a[48:51], v[64:67], v[152:155], a[48:51]\n" \
"s_add_u32 m0, %[mB], 0xc00\n" \
"s_add_u32 s85, s63, 0x18000\n" \
"v_mfma_f32_16x16x32_bf16 a[80:83], v[72:75], v[152:155], a[80:83]\n" \
"buffer_load_dwordx4 v27, s[68:71], s85 offen lds\n" \
"v_mfma_f32_16x16x32_bf16 a[80:83], v[68:71], v[148:151], a[80:83]\n" \
"ds_read_b128 v[104:107], v29 offset:36864\n" \
"ds_read_b128 v[108:111], v28 offset:38912\n" \
"v_mfma_f32_16x16x32_bf16 a[112:115], v[76:79], v[148:151], a[112:115]\n" \
"ds_read_b128 v[112:115], v29 offset:38912\n" \
"s_add_u32 s98, s89, s99\n" \
"v_mfma_f32_16x16x32_bf16 a[112:115], v[80:83], v[152:155], a[112:115]\n" \
"s_add_u32 m0, %[mB], 0x4c00\n" \
"s_add_u32 s84, s63, 0x18000\n" \
"v_mfma_f32_16x16x32_bf16 a[20:23], v[56:59], v[160:163], a[20:23]\n" \
"buffer_load_dwordx4 v27, s[72:75], s84 offen lds\n" \
"v_mfma_f32_16x16x32_bf16 a[20:23], v[52:55], v[156:159], a[20:23]\n" \
"s_mov_b32 m0, %[mS]\n" \
"s_add_u32 s97, s89, s88\n" \
"v_mfma_f32_16x16x32_bf16 a[52:55], v[60:63], v[156:159], a[52:55]\n" \
"s_lshl_b32 s96, s100, 11\n" \
"v_mov_b32_e32 v221, 0xc0c0c0c\n" \
"v_mfma_f32_16x16x32_bf16 a[52:55], v[64:67], v[160:163], a[52:55]\n" \
"v_cmp_le_u32_e32 vcc, v217, v215\n" \
"v_add_u32_e32 v24, v222, v224\n" \
"v_mfma_f32_16x16x32_bf16 a[84:87], v[72:75], v[160:163], a[84:87]\n" \
"v_lshlrev_b32_e32 v227, 4, v226\n" \
"s_add_u32 s99, s97, s96\n" \
"v_mfma_f32_16x16x32_bf16 a[84:87], v[68:71], v[156:159], a[84:87]\n" \
"s_lshl_b32 s101, s95, 16\n" \
"v_cndmask_b32_e32 v40, v220, v221, vcc\n" \
"v_mfma_f32_16x16x32_bf16 a[116:119], v[76:79], v[156:159], a[116:119]\n" \
"v_add_u32_e64 v25, v222, v227\n" \
"s_add_u32 s54, 0x10000, s98\n" \
"v_mfma_f32_16x16x32_bf16 a[116:119], v[80:83], v[160:163], a[116:119]\n" \
"s_add_u32 s43, s99, s101\n" \
"s_mul_i32 s89, 0x1800, s100\n" \
"v_mfma_f32_16x16x32_bf16 a[24:27], v[56:59], v[168:171], a[24:27]\n" \
"v_mov_b32_e32 v223, 0x3020c0c\n" \
"v_mov_b32_e32 v224, 0xc0c0302\n" \
"v_mfma_f32_16x16x32_bf16 a[24:27], v[52:55], v[164:167], a[24:27]\n" \
"v_cmp_ne_u32_e32 vcc, 0, v216\n" \
"s_lshl_b32 s98, s94, 16\n" \
"v_mfma_f32_16x16x32_bf16 a[56:59], v[60:63], v[164:167], a[56:59]\n" \
"s_add_u32 s97, s43, s89\n" \
"v_add_u32_e64 v222, 2, v217\n" \
"v_mfma_f32_16x16x32_bf16 a[56:59], v[64:67], v[168:171], a[56:59]\n" \
"v_cndmask_b32_e32 v225, v224, v223, vcc\n" \
"s_add_u32 s42, s99, s98\n" \
"v_mfma_f32_16x16x32_bf16 a[88:91], v[72:75], v[168:171], a[88:91]\n" \
"s_mov_b32 s86, s10\n" \
"s_add_u32 s45, 0x4000, s97\n" \
"v_mfma_f32_16x16x32_bf16 a[88:91], v[68:71], v[164:167], a[88:91]\n" \
"s_lshr_b32 s94, %[w], 1\n" \
"s_and_b32 s95, 1, %[w]\n" \
"s_mov_b32 exec_lo, 0x15\n" \
"s_mov_b32 exec_hi, 0x180000\n" \
"buffer_load_dword v11, s[80:83], 0 offen sc1 lds\n" \
"s_mov_b64 exec, -1\n" \
"v_mfma_f32_16x16x32_bf16 a[120:123], v[76:79], v[164:167], a[120:123]\n" \
"s_mov_b32 m0, %[mS]\n" \
"v_mov_b32_e32 v226, 0xc0c0c0c\n" \
"v_mfma_f32_16x16x32_bf16 a[120:123], v[80:83], v[168:171], a[120:123]\n" \
"v_cmp_le_u32_e32 vcc, v222, v215\n" \
"s_add_u32 s88, s42, s89\n" \
"v_mfma_f32_16x16x32_bf16 a[28:31], v[56:59], v[176:179], a[28:31]\n" \
"s_add_u32 s96, s94, s95\n" \
"s_add_u32 s97, 1, s94\n" \
"v_mfma_f32_16x16x32_bf16 a[28:31], v[52:55], v[172:175], a[28:31]\n" \
"v_cndmask_b32_e32 v41, v225, v226, vcc\n" \
"v_and_b32_e32 v230, 3, v229\n" \
"v_mfma_f32_16x16x32_bf16 a[60:63], v[60:63], v[172:175], a[60:63]\n" \
"s_add_u32 s44, 0x4000, s88\n" \
"s_lshl_b32 s59, s96, 13\n" \
"v_mfma_f32_16x16x32_bf16 a[60:63], v[64:67], v[176:179], a[60:63]\n" \
"s_sub_u32 s98, s97, s95\n" \
"s_lshl_b32 s99, s94, 13\n" \
"v_mfma_f32_16x16x32_bf16 a[92:95], v[72:75], v[176:179], a[92:95]\n" \
"s_mul_i32 s101, 0x1800, s94\n" \
"v_mov_b32_e32 v227, 0xc0c0c0c\n" \
"v_mfma_f32_16x16x32_bf16 a[92:95], v[68:71], v[172:175], a[92:95]\n" \
"v_cmp_le_u32_e32 vcc, v217, v215\n" \
"v_add_u32_e64 v231, v228, v230\n" \
"v_mfma_f32_16x16x32_bf16 a[124:127], v[76:79], v[172:175], a[124:127]\n" \
"s_lshl_b32 s60, s98, 13\n" \
"s_add_u32 s100, 0x1800, s99\n" \
"v_mfma_f32_16x16x32_bf16 a[124:127], v[80:83], v[176:179], a[124:127]\n" \
"s_sub_u32 s88, 0x17000, s101\n" \
"s_lshl_b32 s96, s94, 15\n" \
"v_mfma_f32_16x16x32_bf16 a[0:3], v[120:123], v[56:59], a[0:3]\n" \
"v_cndmask_b32_e32 v42, v227, v220, vcc\n" \
"v_lshlrev_b32_e32 v232, 7, v231\n" \
"v_mfma_f32_16x16x32_bf16 a[0:3], v[116:119], v[52:55], a[0:3]\n" \
"s_cmp_lg_u32 s95, 0\n" \
"s_cselect_b32 s61, s100, s88\n" \
"v_mfma_f32_16x16x32_bf16 a[32:35], v[116:119], v[60:63], a[32:35]\n" \
"s_add_u32 s97, 0x10000, s96\n" \
"s_lshl_b32 s98, s95, 11\n" \
"v_mfma_f32_16x16x32_bf16 a[32:35], v[120:123], v[64:67], a[32:35]\n" \
"v_mov_b32_e32 v228, 0xc0c0c0c\n" \
"v_cmp_le_u32_e32 vcc, v222, v215\n" \
"v_mfma_f32_16x16x32_bf16 a[64:67], v[120:123], v[72:75], a[64:67]\n" \
"v_add_u32_e32 v233, 0x14000, v232\n" \
"v_lshlrev_b32_e32 v235, 3, v234\n" \
"v_mfma_f32_16x16x32_bf16 a[64:67], v[116:119], v[68:71], a[64:67]\n" \
"s_waitcnt vmcnt(19)\n" \
"v_perm_b32 v252, v209, v208, s87\n" \
"s_mov_b32 s87, 0x7060302\n" \
"v_perm_b32 v253, v209, v208, s87\n" \
"s_mov_b32 s87, 0x5040100\n" \
"s_waitcnt vmcnt(17)\n" \
"v_perm_b32 v254, v211, v210, s87\n" \
"s_mov_b32 s87, 0x7060100\n" \
"v_perm_b32 v255, v211, v210, s87\n" \
"s_mov_b32 s87, s11\n" \
"s_mov_b32 exec_lo, 0x3ffff\n" \
"s_mov_b32 exec_hi, 0x0\n" \
"global_load_lds_dword v188, s[86:87]\n" \
"s_mov_b64 exec, -1\n" \
"v_mfma_f32_16x16x32_bf16 a[96:99], v[116:119], v[76:79], a[96:99]\n" \
"ds_write_b128 v196, v[200:203]\n" \
"v_mfma_f32_16x16x32_bf16 a[96:99], v[120:123], v[80:83], a[96:99]\n" \
"ds_write2_b32 v198, v252, v253 offset0:0 offset1:2\n" \
"s_waitcnt lgkmcnt(2)\n" \
"v_mfma_f32_16x16x32_bf16 a[4:7], v[128:131], v[56:59], a[4:7]\n" \
"ds_write2_b32 v198, v254, v255 offset0:4 offset1:6\n" \
"s_add_u32 s101, s97, s98\n" \
"v_mfma_f32_16x16x32_bf16 a[4:7], v[124:127], v[52:55], a[4:7]\n" \
"v_cndmask_b32_e32 v43, v228, v225, vcc\n" \
"v_and_b32_e32 v231, 2, v212\n" \
"v_mfma_f32_16x16x32_bf16 a[36:39], v[124:127], v[60:63], a[36:39]\n" \
"v_add_u32_e32 v236, v233, v235\n" \
"v_lshrrev_b32_e32 v237, 1, v230\n" \
"v_mfma_f32_16x16x32_bf16 a[36:39], v[128:131], v[64:67], a[36:39]\n" \
"ds_write_b128 v197, v[204:207]\n" \
"v_mfma_f32_16x16x32_bf16 a[68:71], v[128:131], v[72:75], a[68:71]\n" \
"v_add_u32_e32 v243, v242, v230\n" \
"s_add_u32 s100, s101, s99\n" \
"v_mfma_f32_16x16x32_bf16 a[68:71], v[124:127], v[68:71], a[68:71]\n" \
"v_and_b32_e64 v229, 3, v212\n" \
"v_mov_b32_e32 v232, 0x3f80\n" \
"v_mfma_f32_16x16x32_bf16 a[100:103], v[124:127], v[76:79], a[100:103]\n" \
"v_cmp_eq_u32_e32 vcc, 0, v231\n" \
"v_lshlrev_b32_e32 v239, 1, v238\n" \
"v_mfma_f32_16x16x32_bf16 a[100:103], v[128:131], v[80:83], a[100:103]\n" \
"v_lshlrev_b32_e32 v244, 7, v243\n" \
"s_add_u32 s88, 0x4000, s100\n" \
"v_mfma_f32_16x16x32_bf16 a[8:11], v[136:139], v[56:59], a[8:11]\n" \
"v_or_b32_e32 v230, 2, v229\n" \
"v_cndmask_b32_e32 v233, 0, v232, vcc\n" \
"v_mfma_f32_16x16x32_bf16 a[8:11], v[132:135], v[52:55], a[8:11]\n" \
"v_or_b32_e32 v240, v237, v239\n" \
"v_add_u32_e32 v245, 0x14000, v244\n" \
"v_mfma_f32_16x16x32_bf16 a[40:43], v[132:135], v[60:63], a[40:43]\n" \
"s_cmp_lg_u32 s95, 0\n" \
"s_cselect_b32 s47, s88, s101\n" \
"v_mfma_f32_16x16x32_bf16 a[40:43], v[136:139], v[64:67], a[40:43]\n" \
"v_cmp_eq_u32_e32 vcc, v230, v213\n" \
"v_lshlrev_b32_e32 v241, 5, v240\n" \
"v_mfma_f32_16x16x32_bf16 a[72:75], v[136:139], v[72:75], a[72:75]\n" \
"v_add_u32_e32 v246, v245, v235\n" \
"v_cndmask_b32_e32 v46, 0, v233, vcc\n" \
"v_mfma_f32_16x16x32_bf16 a[72:75], v[132:135], v[68:71], a[72:75]\n" \
"v_and_b32_e32 v234, 51, v212\n" \
"v_add_u32_e32 v190, v236, v241\n" \
"v_mfma_f32_16x16x32_bf16 a[104:107], v[132:135], v[76:79], a[104:107]\n" \
"v_mov_b32_e32 v235, 0x3f80\n" \
"v_cmp_eq_u32_e32 vcc, 2, v234\n" \
"v_mfma_f32_16x16x32_bf16 a[104:107], v[136:139], v[80:83], a[104:107]\n" \
"s_lshl_b32 s88, s95, 4\n" \
"v_xor_b32_e64 v247, 3, v237\n" \
"v_mfma_f32_16x16x32_bf16 a[12:15], v[144:147], v[56:59], a[12:15]\n" \
"s_add_u32 s62, s22, 0x280\n" \
"s_and_b32 s62, s62, 0x3ff\n" \
"v_mfma_f32_16x16x32_bf16 a[12:15], v[140:143], v[52:55], a[12:15]\n" \
"s_lshl_b32 s63, s62, 12\n" \
"v_cndmask_b32_e32 v47, 0, v235, vcc\n" \
"v_mfma_f32_16x16x32_bf16 a[44:47], v[140:143], v[60:63], a[44:47]\n" \
"v_add_u32_e64 v236, s88, v215\n" \
"v_mov_b32_e32 v237, 0xffff\n" \
"v_mfma_f32_16x16x32_bf16 a[44:47], v[144:147], v[64:67], a[44:47]\n" \
"v_cmp_le_u32_e32 vcc, v214, v236\n" \
"v_mov_b32_e32 v239, 0xffff0000\n" \
"v_mfma_f32_16x16x32_bf16 a[76:79], v[144:147], v[72:75], a[76:79]\n" \
"v_cndmask_b32_e32 v238, 0, v237, vcc\n" \
"v_cmp_lt_u32_e32 vcc, v214, v236\n" \
"v_mfma_f32_16x16x32_bf16 a[76:79], v[140:143], v[68:71], a[76:79]\n" \
"v_add_u32_e32 v241, 2, v214\n" \
"v_cndmask_b32_e32 v240, 0, v239, vcc\n" \
"v_mfma_f32_16x16x32_bf16 a[108:111], v[140:143], v[76:79], a[108:111]\n" \
"v_mov_b32_e32 v242, 0xffff\n" \
"v_cmp_le_u32_e32 vcc, v241, v236\n" \
"v_mfma_f32_16x16x32_bf16 a[108:111], v[144:147], v[80:83], a[108:111]\n" \
"v_lshlrev_b32_e32 v248, 5, v247\n" \
"v_cndmask_b32_e32 v243, 0, v242, vcc\n" \
"s_waitcnt vmcnt(2) lgkmcnt(0)\n" \
"s_barrier\n" \
"v_mfma_f32_16x16x32_bf16 a[128:131], v[120:123], v[88:91], a[128:131]\n" \
"s_add_u32 m0, %[mA], 0x8000\n" \
"s_add_u32 s85, s62, 0x0\n" \
"s_nop 0\n" \
"v_mfma_f32_16x16x32_bf16 a[128:131], v[116:119], v[84:87], a[128:131]\n" \
"buffer_load_dwordx4 v22, s[64:67], s85 offen lds\n" \
"v_mfma_f32_16x16x32_bf16 a[160:163], v[116:119], v[92:95], a[160:163]\n" \
"ds_read_b128 v[56:59], v29 offset:16384\n" \
"ds_read_b128 v[52:55], v28 offset:16384\n" \
"v_mfma_f32_16x16x32_bf16 a[160:163], v[120:123], v[96:99], a[160:163]\n" \
"s_add_u32 m0, %[mA], 0x8400\n" \
"s_add_u32 s84, s62, 0x2000\n" \
"v_mfma_f32_16x16x32_bf16 a[192:195], v[120:123], v[104:107], a[192:195]\n" \
"buffer_load_dwordx4 v23, s[64:67], s84 offen lds\n" \
"v_mfma_f32_16x16x32_bf16 a[192:195], v[116:119], v[100:103], a[192:195]\n" \
"ds_read_b128 v[60:63], v28 offset:18432\n" \
"ds_read_b128 v[64:67], v29 offset:18432\n" \
"v_mfma_f32_16x16x32_bf16 a[224:227], v[116:119], v[108:111], a[224:227]\n" \
"ds_read_b64_tr_b16 v[116:117], v32 offset:0\n" \
"ds_read_b64_tr_b16 v[118:119], v32 offset:1024\n" \
"v_mfma_f32_16x16x32_bf16 a[224:227], v[120:123], v[112:115], a[224:227]\n" \
"s_add_u32 m0, %[mB], 0x8000\n" \
"s_add_u32 s85, s63, 0x0\n" \
"v_mfma_f32_16x16x32_bf16 a[132:135], v[128:131], v[88:91], a[132:135]\n" \
"buffer_load_dwordx4 v26, s[68:71], s85 offen lds\n" \
"v_mfma_f32_16x16x32_bf16 a[132:135], v[124:127], v[84:87], a[132:135]\n" \
"ds_read_b64_tr_b16 v[120:121], v32 offset:8192\n" \
"ds_read_b64_tr_b16 v[122:123], v32 offset:9216\n" \
"v_mfma_f32_16x16x32_bf16 a[164:167], v[124:127], v[92:95], a[164:167]\n" \
"ds_read_b128 v[72:75], v29 offset:20480\n" \
"ds_read_b128 v[68:71], v28 offset:20480\n" \
"v_mfma_f32_16x16x32_bf16 a[164:167], v[128:131], v[96:99], a[164:167]\n" \
"s_add_u32 m0, %[mB], 0xc000\n" \
"s_add_u32 s84, s63, 0x0\n" \
"v_mfma_f32_16x16x32_bf16 a[196:199], v[128:131], v[104:107], a[196:199]\n" \
"buffer_load_dwordx4 v26, s[72:75], s84 offen lds\n" \
"v_mfma_f32_16x16x32_bf16 a[196:199], v[124:127], v[100:103], a[196:199]\n" \
"ds_read_b128 v[76:79], v28 offset:22528\n" \
"ds_read_b128 v[80:83], v29 offset:22528\n" \
"v_mfma_f32_16x16x32_bf16 a[228:231], v[124:127], v[108:111], a[228:231]\n" \
"ds_read_b64_tr_b16 v[124:125], v33 offset:0\n" \
"ds_read_b64_tr_b16 v[126:127], v33 offset:1024\n" \
"v_mfma_f32_16x16x32_bf16 a[228:231], v[128:131], v[112:115], a[228:231]\n" \
"s_add_u32 m0, %[mA], 0x8800\n" \
"s_add_u32 s85, s62, 0x4000\n" \
"v_mfma_f32_16x16x32_bf16 a[136:139], v[136:139], v[88:91], a[136:139]\n" \
"buffer_load_dwordx4 v22, s[64:67], s85 offen lds\n" \
"v_mfma_f32_16x16x32_bf16 a[136:139], v[132:135], v[84:87], a[136:139]\n" \
"ds_read_b64_tr_b16 v[128:129], v33 offset:8192\n" \
"ds_read_b64_tr_b16 v[130:131], v33 offset:9216\n" \
"v_mfma_f32_16x16x32_bf16 a[168:171], v[132:135], v[92:95], a[168:171]\n" \
"ds_read_b128 v[200:203], v199 offset:0\n" \
"ds_read_b128 v[204:207], v199 offset:4096\n" \
"v_mfma_f32_16x16x32_bf16 a[168:171], v[136:139], v[96:99], a[168:171]\n" \
"s_add_u32 m0, %[mA], 0x8c00\n" \
"s_add_u32 s84, s62, 0x6000\n" \
"v_mfma_f32_16x16x32_bf16 a[200:203], v[136:139], v[104:107], a[200:203]\n" \
"buffer_load_dwordx4 v23, s[64:67], s84 offen lds\n" \
"v_mfma_f32_16x16x32_bf16 a[200:203], v[132:135], v[100:103], a[200:203]\n" \
"ds_read_b128 v[208:211], v199 offset:8192\n" \
"v_add_u32_e64 v39, v246, v248\n" \
"v_mfma_f32_16x16x32_bf16 a[232:235], v[132:135], v[108:111], a[232:235]\n" \
"ds_read_b64_tr_b16 v[132:133], v34 offset:0\n" \
"ds_read_b64_tr_b16 v[134:135], v34 offset:1024\n" \
"v_mfma_f32_16x16x32_bf16 a[232:235], v[136:139], v[112:115], a[232:235]\n" \
"s_add_u32 m0, %[mB], 0x8400\n" \
"s_add_u32 s85, s63, 0x8000\n" \
"v_mfma_f32_16x16x32_bf16 a[140:143], v[144:147], v[88:91], a[140:143]\n" \
"buffer_load_dwordx4 v26, s[68:71], s85 offen lds\n" \
"v_mfma_f32_16x16x32_bf16 a[140:143], v[140:143], v[84:87], a[140:143]\n" \
"ds_read_b64_tr_b16 v[136:137], v34 offset:8192\n" \
"ds_read_b64_tr_b16 v[138:139], v34 offset:9216\n" \
"v_mfma_f32_16x16x32_bf16 a[172:175], v[140:143], v[92:95], a[172:175]\n" \
"v_mov_b32_e32 v244, 0xffff0000\n" \
"v_cmp_lt_u32_e32 vcc, v241, v236\n" \
"v_mfma_f32_16x16x32_bf16 a[172:175], v[144:147], v[96:99], a[172:175]\n" \
"s_add_u32 m0, %[mB], 0xc400\n" \
"s_add_u32 s84, s63, 0x8000\n" \
"v_mfma_f32_16x16x32_bf16 a[204:207], v[144:147], v[104:107], a[204:207]\n" \
"buffer_load_dwordx4 v26, s[72:75], s84 offen lds\n" \
"v_mfma_f32_16x16x32_bf16 a[204:207], v[140:143], v[100:103], a[204:207]\n" \
"v_cndmask_b32_e32 v245, 0, v244, vcc\n" \
"v_add_u32_e64 v246, 16, v214\n" \
"v_mfma_f32_16x16x32_bf16 a[236:239], v[140:143], v[108:111], a[236:239]\n" \
"ds_read_b64_tr_b16 v[140:141], v35 offset:0\n" \
"ds_read_b64_tr_b16 v[142:143], v35 offset:1024\n" \
"v_mfma_f32_16x16x32_bf16 a[236:239], v[144:147], v[112:115], a[236:239]\n" \
"s_add_u32 m0, %[mA], 0x9000\n" \
"s_add_u32 s85, s62, 0x8000\n" \
"v_mfma_f32_16x16x32_bf16 a[144:147], v[88:91], v[152:155], a[144:147]\n" \
"buffer_load_dwordx4 v22, s[64:67], s85 offen lds\n" \
"v_mfma_f32_16x16x32_bf16 a[144:147], v[84:87], v[148:151], a[144:147]\n" \
"ds_read_b64_tr_b16 v[144:145], v35 offset:8192\n" \
"ds_read_b64_tr_b16 v[146:147], v35 offset:9216\n" \
"v_mfma_f32_16x16x32_bf16 a[176:179], v[92:95], v[148:151], a[176:179]\n" \
"v_mov_b32_e32 v247, 0xffff\n" \
"v_cmp_le_u32_e32 vcc, v246, v236\n" \
"v_mfma_f32_16x16x32_bf16 a[176:179], v[96:99], v[152:155], a[176:179]\n" \
"s_add_u32 m0, %[mA], 0x9400\n" \
"s_add_u32 s84, s62, 0xa000\n" \
"v_mfma_f32_16x16x32_bf16 a[208:211], v[104:107], v[152:155], a[208:211]\n" \
"buffer_load_dwordx4 v23, s[64:67], s84 offen lds\n" \
"v_mfma_f32_16x16x32_bf16 a[208:211], v[100:103], v[148:151], a[208:211]\n" \
"v_cndmask_b32_e32 v248, 0, v247, vcc\n" \
"v_mov_b32_e32 v249, 0xffff0000\n" \
"v_mfma_f32_16x16x32_bf16 a[240:243], v[108:111], v[148:151], a[240:243]\n" \
"ds_read_b64_tr_b16 v[148:149], v32 offset:16384\n" \
"ds_read_b64_tr_b16 v[150:151], v32 offset:17408\n" \
"v_mfma_f32_16x16x32_bf16 a[240:243], v[112:115], v[152:155], a[240:243]\n" \
"s_add_u32 m0, %[mB], 0x8800\n" \
"s_add_u32 s85, s63, 0x10000\n" \
"v_mfma_f32_16x16x32_bf16 a[148:151], v[88:91], v[160:163], a[148:151]\n" \
"buffer_load_dwordx4 v27, s[68:71], s85 offen lds\n" \
"v_mfma_f32_16x16x32_bf16 a[148:151], v[84:87], v[156:159], a[148:151]\n" \
"ds_read_b64_tr_b16 v[152:153], v32 offset:24576\n" \
"ds_read_b64_tr_b16 v[154:155], v32 offset:25600\n" \
"v_mfma_f32_16x16x32_bf16 a[180:183], v[92:95], v[156:159], a[180:183]\n" \
"v_cmp_lt_u32_e32 vcc, v246, v236\n" \
"s_lshl_b32 s89, %[w], 8\n" \
"v_mfma_f32_16x16x32_bf16 a[180:183], v[96:99], v[160:163], a[180:183]\n" \
"s_add_u32 m0, %[mB], 0xc800\n" \
"s_add_u32 s84, s63, 0x10000\n" \
"v_mfma_f32_16x16x32_bf16 a[212:215], v[104:107], v[160:163], a[212:215]\n" \
"buffer_load_dwordx4 v27, s[72:75], s84 offen lds\n" \
"v_mfma_f32_16x16x32_bf16 a[212:215], v[100:103], v[156:159], a[212:215]\n" \
"v_cndmask_b32_e32 v250, 0, v249, vcc\n" \
"v_add_u32_e32 v251, 18, v214\n" \
"v_mfma_f32_16x16x32_bf16 a[244:247], v[108:111], v[156:159], a[244:247]\n" \
"ds_read_b64_tr_b16 v[156:157], v33 offset:16384\n" \
"ds_read_b64_tr_b16 v[158:159], v33 offset:17408\n" \
"v_mfma_f32_16x16x32_bf16 a[244:247], v[112:115], v[160:163], a[244:247]\n" \
"ds_read_b64_tr_b16 v[160:161], v33 offset:24576\n" \
"ds_read_b64_tr_b16 v[162:163], v33 offset:25600\n" \
"v_mfma_f32_16x16x32_bf16 a[152:155], v[88:91], v[168:171], a[152:155]\n" \
"s_add_u32 s46, 0x27000, s89\n" \
"v_mov_b32_e32 v218, 0xffff\n" \
"v_mfma_f32_16x16x32_bf16 a[152:155], v[84:87], v[164:167], a[152:155]\n" \
"v_cmp_le_u32_e32 vcc, v251, v236\n" \
"s_add_u32 s89, 0xffffc000, s101\n" \
"v_mfma_f32_16x16x32_bf16 a[184:187], v[92:95], v[164:167], a[184:187]\n" \
"v_cndmask_b32_e32 v219, 0, v218, vcc\n" \
"v_lshlrev_b32_e32 v224, 3, v212\n" \
"v_mfma_f32_16x16x32_bf16 a[184:187], v[96:99], v[168:171], a[184:187]\n" \
"s_cmp_lg_u32 s95, 0\n" \
"s_cselect_b32 s48, s89, s100\n" \
"v_mfma_f32_16x16x32_bf16 a[216:219], v[104:107], v[168:171], a[216:219]\n" \
"s_lshr_b32 s94, %[b], 5\n" \
"s_mov_b32 s96, 0xc000\n" \
"v_mfma_f32_16x16x32_bf16 a[216:219], v[100:103], v[164:167], a[216:219]\n" \
"s_mov_b32 s97, 0x8000\n" \
"v_mov_b32_e32 v221, 0xffff0000\n" \
"v_mfma_f32_16x16x32_bf16 a[248:251], v[108:111], v[164:167], a[248:251]\n" \
"ds_read_b64_tr_b16 v[164:165], v34 offset:16384\n" \
"ds_read_b64_tr_b16 v[166:167], v34 offset:17408\n" \
"v_mfma_f32_16x16x32_bf16 a[248:251], v[112:115], v[168:171], a[248:251]\n" \
"ds_read_b64_tr_b16 v[168:169], v34 offset:24576\n" \
"ds_read_b64_tr_b16 v[170:171], v34 offset:25600\n" \
"v_mfma_f32_16x16x32_bf16 a[156:159], v[88:91], v[176:179], a[156:159]\n" \
"ds_read_b128 v[88:91], v29 offset:0\n" \
"v_cmp_lt_u32_e32 vcc, v251, v236\n" \
"v_mfma_f32_16x16x32_bf16 a[156:159], v[84:87], v[172:175], a[156:159]\n" \
"ds_read_b128 v[84:87], v28 offset:0\n" \
"v_lshrrev_b32_e32 v212, 4, v20\n" \
"v_mfma_f32_16x16x32_bf16 a[188:191], v[92:95], v[172:175], a[188:191]\n" \
"ds_read_b128 v[92:95], v28 offset:2048\n" \
"v_cndmask_b32_e32 v216, 0, v221, vcc\n" \
"v_mfma_f32_16x16x32_bf16 a[188:191], v[96:99], v[176:179], a[188:191]\n" \
"ds_read_b128 v[96:99], v29 offset:2048\n" \
"v_lshlrev_b32_e32 v223, 2, v236\n" \
"v_mfma_f32_16x16x32_bf16 a[220:223], v[104:107], v[176:179], a[220:223]\n" \
"ds_read_b128 v[104:107], v29 offset:4096\n" \
"s_lshl_b32 s101, %[w], 9\n" \
"v_mfma_f32_16x16x32_bf16 a[220:223], v[100:103], v[172:175], a[220:223]\n" \
"ds_read_b128 v[100:103], v28 offset:4096\n" \
"v_mul_u32_u24_e32 v213, 0xe39, v212\n" \
"v_mfma_f32_16x16x32_bf16 a[252:255], v[108:111], v[172:175], a[252:255]\n" \
"ds_read_b64_tr_b16 v[172:173], v35 offset:16384\n" \
"ds_read_b64_tr_b16 v[174:175], v35 offset:17408\n" \
"v_mfma_f32_16x16x32_bf16 a[252:255], v[112:115], v[176:179], a[252:255]\n" \
"s_add_u32 m0, %[mA], 0x9800\n" \
"s_waitcnt lgkmcnt(12)\n" \
"v_mfma_f32_16x16x32_bf16 a[0:3], v[120:123], v[56:59], a[0:3]\n" \
"s_add_u32 s85, s62, 0xc000\n" \
"ds_read_b64_tr_b16 v[176:177], v35 offset:24576\n" \
"v_mfma_f32_16x16x32_bf16 a[0:3], v[116:119], v[52:55], a[0:3]\n" \
"buffer_load_dwordx4 v22, s[64:67], s85 offen lds\n" \
"v_mfma_f32_16x16x32_bf16 a[32:35], v[116:119], v[60:63], a[32:35]\n" \
"s_add_u32 m0, %[mA], 0x9c00\n" \
"s_add_u32 s84, s62, 0xe000\n" \
"L_pf2:\n" \
"s_cbranch_execz L_pf6\n" \
"s_nop 0\n" \
"v_mfma_f32_16x16x32_bf16 a[32:35], v[120:123], v[64:67], a[32:35]\n" \
"ds_read_b64_tr_b16 v[178:179], v35 offset:25600\n" \
"ds_read_b128 v[108:111], v28 offset:6144\n" \
"v_mfma_f32_16x16x32_bf16 a[64:67], v[120:123], v[72:75], a[64:67]\n" \
"buffer_load_dwordx4 v23, s[64:67], s84 offen lds\n" \
"v_mfma_f32_16x16x32_bf16 a[64:67], v[116:119], v[68:71], a[64:67]\n" \
"s_add_u32 m0, %[mB], 0x8c00\n" \
"s_add_u32 s85, s63, 0x18000\n" \
"v_mfma_f32_16x16x32_bf16 a[96:99], v[116:119], v[76:79], a[96:99]\n" \
"ds_read_b128 v[112:115], v29 offset:6144\n" \
"v_or_b32_e32 v15, v219, v216\n" \
"v_mfma_f32_16x16x32_bf16 a[96:99], v[120:123], v[80:83], a[96:99]\n" \
"buffer_load_dwordx4 v27, s[68:71], s85 offen lds\n" \
"v_mfma_f32_16x16x32_bf16 a[4:7], v[128:131], v[56:59], a[4:7]\n" \
"s_add_u32 m0, %[mB], 0xcc00\n" \
"s_add_u32 s84, s63, 0x18000\n" \
"v_mfma_f32_16x16x32_bf16 a[4:7], v[124:127], v[52:55], a[4:7]\n" \
"s_cmp_lg_u32 s94, 0\n" \
"s_cselect_b32 s53, s96, 0x8000\n" \
"v_mfma_f32_16x16x32_bf16 a[36:39], v[124:127], v[60:63], a[36:39]\n" \
"buffer_load_dwordx4 v27, s[72:75], s84 offen lds\n" \
"v_mfma_f32_16x16x32_bf16 a[36:39], v[128:131], v[64:67], a[36:39]\n" \
"s_cmp_lg_u32 s94, 0\n" \
"s_cselect_b32 s98, s97, 0x4000\n" \
"v_mfma_f32_16x16x32_bf16 a[68:71], v[128:131], v[72:75], a[68:71]\n" \
"v_add_u32_e32 v18, 0x27000, v223\n" \
"s_add_u32 s89, 0x27800, s101\n" \
"v_mfma_f32_16x16x32_bf16 a[68:71], v[124:127], v[68:71], a[68:71]\n" \
"buffer_store_dwordx4 v[200:203], v20, s[80:83], s57 offen sc1\n" \
"v_mfma_f32_16x16x32_bf16 a[100:103], v[124:127], v[76:79], a[100:103]\n" \
"v_lshrrev_b32_e32 v214, 16, v213\n" \
"v_add_u32_e64 v222, 64, v212\n" \
"v_mfma_f32_16x16x32_bf16 a[100:103], v[128:131], v[80:83], a[100:103]\n" \
"v_add_u32_e32 v19, s89, v224\n" \
"v_mul_u32_u24_e32 v216, 18, v214\n" \
"v_mfma_f32_16x16x32_bf16 a[8:11], v[136:139], v[56:59], a[8:11]\n" \
"s_and_b32 s94, 7, %[b]\n" \
"v_mul_u32_u24_e32 v223, 0xe39, v222\n" \
"v_mfma_f32_16x16x32_bf16 a[8:11], v[132:135], v[52:55], a[8:11]\n" \
"v_sub_u32_e32 v217, v212, v216\n" \
"s_lshl_b32 s95, s94, 5\n" \
"v_mfma_f32_16x16x32_bf16 a[40:43], v[132:135], v[60:63], a[40:43]\n" \
"buffer_store_dwordx4 v[204:207], v20, s[80:83], s56 offen sc1\n" \
"v_mfma_f32_16x16x32_bf16 a[40:43], v[136:139], v[64:67], a[40:43]\n" \
"v_lshrrev_b32_e32 v224, 16, v223\n" \
"v_add_u32_e32 v233, 0x80, v212\n" \
"v_mfma_f32_16x16x32_bf16 a[72:75], v[136:139], v[72:75], a[72:75]\n" \
"s_add_u32 s96, 32, s95\n" \
"v_lshlrev_b32_e32 v218, 4, v217\n" \
"v_mfma_f32_16x16x32_bf16 a[72:75], v[132:135], v[68:71], a[72:75]\n" \
"v_mul_u32_u24_e32 v226, 18, v224\n" \
"v_mul_u32_u24_e32 v234, 0xe39, v233\n" \
"v_mfma_f32_16x16x32_bf16 a[104:107], v[132:135], v[76:79], a[104:107]\n" \
"v_or_b32_e32 v13, v243, v245\n" \
"v_add_u32_e64 v219, s96, v218\n" \
"v_mfma_f32_16x16x32_bf16 a[104:107], v[136:139], v[80:83], a[104:107]\n" \
"v_sub_u32_e32 v227, v222, v226\n" \
"v_lshrrev_b32_e32 v235, 16, v234\n" \
"v_mfma_f32_16x16x32_bf16 a[12:15], v[144:147], v[56:59], a[12:15]\n" \
"buffer_store_dwordx4 v[208:211], v20, s[80:83], s58 offen sc1\n" \
"v_mfma_f32_16x16x32_bf16 a[12:15], v[140:143], v[52:55], a[12:15]\n" \
"v_add_u32_e32 v244, 0xc0, v212\n" \
"v_or_b32_e32 v12, v238, v240\n" \
"v_mfma_f32_16x16x32_bf16 a[44:47], v[140:143], v[60:63], a[44:47]\n" \
"v_and_b32_e32 v220, 0xff, v219\n" \
"v_cmp_gt_u32_e32 vcc, 16, v217\n" \
"v_mfma_f32_16x16x32_bf16 a[44:47], v[144:147], v[64:67], a[44:47]\n" \
"v_lshlrev_b32_e32 v228, 4, v227\n" \
"v_mul_u32_u24_e32 v237, 18, v235\n" \
"v_mfma_f32_16x16x32_bf16 a[76:79], v[144:147], v[72:75], a[76:79]\n" \
"v_mul_u32_u24_e32 v245, 0xe39, v244\n" \
"v_or_b32_e32 v14, v248, v250\n" \
"v_mfma_f32_16x16x32_bf16 a[76:79], v[140:143], v[68:71], a[76:79]\n" \
"v_cndmask_b32_e32 v221, v218, v220, vcc\n" \
"v_add_u32_e64 v229, s96, v228\n" \
"v_mfma_f32_16x16x32_bf16 a[108:111], v[140:143], v[76:79], a[108:111]\n" \
"s_mov_b32 m0, %[mS]\n" \
"s_mov_b32 s86, s10\n" \
"v_mfma_f32_16x16x32_bf16 a[108:111], v[144:147], v[80:83], a[108:111]\n" \
"s_mov_b32 s87, s11\n" \
"v_sub_u32_e32 v238, v233, v237\n" \
"v_mfma_f32_16x16x32_bf16 a[16:19], v[56:59], v[152:155], a[16:19]\n" \
"v_lshrrev_b32_e32 v246, 16, v245\n" \
"v_add_u32_e32 v219, 0x100, v212\n" \
"v_mfma_f32_16x16x32_bf16 a[16:19], v[52:55], v[148:151], a[16:19]\n" \
"v_mul_u32_u24_e32 v215, 0x480, v214\n" \
"v_and_b32_e32 v230, 0xff, v229\n" \
"v_mfma_f32_16x16x32_bf16 a[48:51], v[60:63], v[148:151], a[48:51]\n" \
"v_cmp_gt_u32_e32 vcc, 16, v227\n" \
"v_lshlrev_b32_e32 v239, 4, v238\n" \
"v_mfma_f32_16x16x32_bf16 a[48:51], v[64:67], v[152:155], a[48:51]\n" \
"v_mul_u32_u24_e32 v248, 18, v246\n" \
"v_mul_u32_u24_e32 v217, 0xe39, v219\n" \
"v_mfma_f32_16x16x32_bf16 a[80:83], v[72:75], v[152:155], a[80:83]\n" \
"v_add_u32_e32 v0, v215, v221\n" \
"v_cndmask_b32_e32 v231, v228, v230, vcc\n" \
"s_mov_b32 exec_lo, 0xfffc0000\n" \
"s_mov_b32 exec_hi, 0xf\n" \
"global_load_lds_dword v188, s[86:87]\n" \
"s_mov_b64 exec, -1\n" \
"v_mfma_f32_16x16x32_bf16 a[80:83], v[68:71], v[148:151], a[80:83]\n" \
"v_add_u32_e32 v240, s96, v239\n" \
"v_sub_u32_e32 v249, v244, v248\n" \
"v_mfma_f32_16x16x32_bf16 a[112:115], v[76:79], v[148:151], a[112:115]\n" \
"v_lshrrev_b32_e32 v220, 16, v217\n" \
"v_add_u32_e32 v229, 0x140, v212\n" \
"v_mfma_f32_16x16x32_bf16 a[112:115], v[80:83], v[152:155], a[112:115]\n" \
"v_mul_u32_u24_e32 v225, 0x480, v224\n" \
"v_and_b32_e32 v241, 0xff, v240\n" \
"v_mfma_f32_16x16x32_bf16 a[20:23], v[56:59], v[160:163], a[20:23]\n" \
"v_cmp_gt_u32_e32 vcc, 16, v238\n" \
"v_lshlrev_b32_e32 v250, 4, v249\n" \
"v_mfma_f32_16x16x32_bf16 a[20:23], v[52:55], v[156:159], a[20:23]\n" \
"v_mul_u32_u24_e32 v215, 18, v220\n" \
"v_mul_u32_u24_e32 v227, 0xe39, v229\n" \
"v_mfma_f32_16x16x32_bf16 a[52:55], v[60:63], v[156:159], a[52:55]\n" \
"v_add_u32_e32 v232, v225, v231\n" \
"v_cndmask_b32_e32 v242, v239, v241, vcc\n" \
"v_mfma_f32_16x16x32_bf16 a[52:55], v[64:67], v[160:163], a[52:55]\n" \
"v_add_u32_e32 v251, s96, v250\n" \
"v_sub_u32_e32 v221, v219, v215\n" \
"v_mfma_f32_16x16x32_bf16 a[84:87], v[72:75], v[160:163], a[84:87]\n" \
"v_lshrrev_b32_e32 v230, 16, v227\n" \
"v_add_u32_e32 v240, 0x180, v212\n" \
"v_mfma_f32_16x16x32_bf16 a[84:87], v[68:71], v[156:159], a[84:87]\n" \
"v_mul_u32_u24_e32 v236, 0x480, v235\n" \
"v_and_b32_e32 v213, 0xff, v251\n" \
"v_mfma_f32_16x16x32_bf16 a[116:119], v[76:79], v[156:159], a[116:119]\n" \
"v_cmp_gt_u32_e32 vcc, 16, v249\n" \
"v_lshlrev_b32_e32 v223, 4, v221\n" \
"v_mfma_f32_16x16x32_bf16 a[116:119], v[80:83], v[160:163], a[116:119]\n" \
"v_mul_u32_u24_e32 v225, 18, v230\n" \
"s_waitcnt lgkmcnt(0)\n" \
"v_mfma_f32_16x16x32_bf16 a[24:27], v[56:59], v[168:171], a[24:27]\n" \
"v_mul_u32_u24_e32 v238, 0xe39, v240\n" \
"v_add_u32_e32 v1, 0xfffffc00, v232\n" \
"v_mfma_f32_16x16x32_bf16 a[24:27], v[52:55], v[164:167], a[24:27]\n" \
"v_add_u32_e32 v243, v236, v242\n" \
"v_cndmask_b32_e32 v214, v250, v213, vcc\n" \
"v_mfma_f32_16x16x32_bf16 a[56:59], v[60:63], v[164:167], a[56:59]\n" \
"v_add_u32_e32 v224, s96, v223\n" \
"v_sub_u32_e32 v231, v229, v225\n" \
"v_mfma_f32_16x16x32_bf16 a[56:59], v[64:67], v[168:171], a[56:59]\n" \
"v_lshrrev_b32_e32 v241, 16, v238\n" \
"v_add_u32_e32 v251, 0x1c0, v212\n" \
"v_mfma_f32_16x16x32_bf16 a[88:91], v[72:75], v[168:171], a[88:91]\n" \
"v_mul_u32_u24_e32 v247, 0x480, v246\n" \
"v_and_b32_e32 v222, 0xff, v224\n" \
"v_mfma_f32_16x16x32_bf16 a[88:91], v[68:71], v[164:167], a[88:91]\n" \
"v_cmp_gt_u32_e32 vcc, 16, v221\n" \
"v_lshlrev_b32_e32 v232, 4, v231\n" \
"v_mfma_f32_16x16x32_bf16 a[120:123], v[76:79], v[164:167], a[120:123]\n" \
"v_mul_u32_u24_e32 v236, 18, v241\n" \
"v_mul_u32_u24_e32 v249, 0xe39, v251\n" \
"v_mfma_f32_16x16x32_bf16 a[120:123], v[80:83], v[168:171], a[120:123]\n" \
"v_add_u32_e32 v2, 0xfffff800, v243\n" \
"v_add_u32_e32 v216, v247, v214\n" \
"v_mfma_f32_16x16x32_bf16 a[28:31], v[56:59], v[176:179], a[28:31]\n" \
"s_add_u32 s62, s22, 0x300\n" \
"s_and_b32 s62, s62, 0x3ff\n" \
"v_mfma_f32_16x16x32_bf16 a[28:31], v[52:55], v[172:175], a[28:31]\n" \
"s_lshl_b32 s63, s62, 12\n" \
"v_cndmask_b32_e32 v226, v223, v222, vcc\n" \
"v_mfma_f32_16x16x32_bf16 a[60:63], v[60:63], v[172:175], a[60:63]\n" \
"v_add_u32_e32 v234, s96, v232\n" \
"v_sub_u32_e32 v242, v240, v236\n" \
"v_mfma_f32_16x16x32_bf16 a[60:63], v[64:67], v[176:179], a[60:63]\n" \
"v_lshrrev_b32_e32 v213, 16, v249\n" \
"v_add_u32_e32 v224, 0x200, v212\n" \
"v_mfma_f32_16x16x32_bf16 a[92:95], v[72:75], v[176:179], a[92:95]\n" \
"v_mul_u32_u24_e32 v218, 0x480, v220\n" \
"v_and_b32_e32 v235, 0xff, v234\n" \
"v_mfma_f32_16x16x32_bf16 a[92:95], v[68:71], v[172:175], a[92:95]\n" \
"v_cmp_gt_u32_e32 vcc, 16, v231\n" \
"v_lshlrev_b32_e32 v243, 4, v242\n" \
"v_mfma_f32_16x16x32_bf16 a[124:127], v[76:79], v[172:175], a[124:127]\n" \
"v_mul_u32_u24_e32 v247, 18, v213\n" \
"v_mul_u32_u24_e32 v221, 0xe39, v224\n" \
"v_mfma_f32_16x16x32_bf16 a[124:127], v[80:83], v[176:179], a[124:127]\n" \
"v_add_u32_e32 v3, 0xfffff400, v216\n" \
"v_add_u32_e32 v4, v218, v226\n" \
"s_waitcnt vmcnt(4)\n" \
"s_barrier\n" \
"v_mfma_f32_16x16x32_bf16 a[144:147], v[88:91], v[152:155], a[144:147]\n" \
"s_add_u32 m0, %[mA], 0x0\n" \
"s_add_u32 s85, s62, 0x0\n" \
"s_nop 0\n" \
"v_mfma_f32_16x16x32_bf16 a[144:147], v[84:87], v[148:151], a[144:147]\n" \
"buffer_load_dwordx4 v22, s[64:67], s85 offen lds\n" \
"v_mfma_f32_16x16x32_bf16 a[176:179], v[92:95], v[148:151], a[176:179]\n" \
"ds_read_b128 v[56:59], v29 offset:49152\n" \
"ds_read_b128 v[52:55], v28 offset:49152\n" \
"v_mfma_f32_16x16x32_bf16 a[176:179], v[96:99], v[152:155], a[176:179]\n" \
"s_add_u32 m0, %[mA], 0x400\n" \
"s_add_u32 s84, s62, 0x2000\n" \
"v_mfma_f32_16x16x32_bf16 a[208:211], v[104:107], v[152:155], a[208:211]\n" \
"buffer_load_dwordx4 v23, s[64:67], s84 offen lds\n" \
"v_mfma_f32_16x16x32_bf16 a[208:211], v[100:103], v[148:151], a[208:211]\n" \
"ds_read_b128 v[60:63], v28 offset:51200\n" \
"ds_read_b128 v[64:67], v29 offset:51200\n" \
"v_mfma_f32_16x16x32_bf16 a[240:243], v[108:111], v[148:151], a[240:243]\n" \
"ds_read_b64_tr_b16 v[148:149], v32 offset:49152\n" \
"ds_read_b64_tr_b16 v[150:151], v32 offset:50176\n" \
"v_mfma_f32_16x16x32_bf16 a[240:243], v[112:115], v[152:155], a[240:243]\n" \
"s_add_u32 m0, %[mB], 0x0\n" \
"s_add_u32 s85, s63, 0x0\n" \
"v_mfma_f32_16x16x32_bf16 a[148:151], v[88:91], v[160:163], a[148:151]\n" \
"buffer_load_dwordx4 v26, s[68:71], s85 offen lds\n" \
"v_mfma_f32_16x16x32_bf16 a[148:151], v[84:87], v[156:159], a[148:151]\n" \
"ds_read_b64_tr_b16 v[152:153], v32 offset:57344\n" \
"ds_read_b64_tr_b16 v[154:155], v32 offset:58368\n" \
"v_mfma_f32_16x16x32_bf16 a[180:183], v[92:95], v[156:159], a[180:183]\n" \
"ds_read_b128 v[72:75], v29 offset:53248\n" \
"ds_read_b128 v[68:71], v28 offset:53248\n" \
"v_mfma_f32_16x16x32_bf16 a[180:183], v[96:99], v[160:163], a[180:183]\n" \
"s_add_u32 m0, %[mB], 0x4000\n" \
"s_add_u32 s84, s63, 0x0\n" \
"s_nop 0\n" \
"v_mfma_f32_16x16x32_bf16 a[212:215], v[104:107], v[160:163], a[212:215]\n" \
"buffer_load_dwordx4 v26, s[72:75], s84 offen lds\n" \
"v_mfma_f32_16x16x32_bf16 a[212:215], v[100:103], v[156:159], a[212:215]\n" \
"ds_read_b128 v[76:79], v28 offset:55296\n" \
"ds_read_b128 v[80:83], v29 offset:55296\n" \
"v_mfma_f32_16x16x32_bf16 a[244:247], v[108:111], v[156:159], a[244:247]\n" \
"ds_read_b64_tr_b16 v[156:157], v33 offset:49152\n" \
"ds_read_b64_tr_b16 v[158:159], v33 offset:50176\n" \
"v_mfma_f32_16x16x32_bf16 a[244:247], v[112:115], v[160:163], a[244:247]\n" \
"s_add_u32 m0, %[mA], 0x800\n" \
"s_add_u32 s85, s62, 0x4000\n" \
"v_mfma_f32_16x16x32_bf16 a[152:155], v[88:91], v[168:171], a[152:155]\n" \
"buffer_load_dwordx4 v22, s[64:67], s85 offen lds\n" \
"v_mfma_f32_16x16x32_bf16 a[152:155], v[84:87], v[164:167], a[152:155]\n" \
"ds_read_b64_tr_b16 v[160:161], v33 offset:57344\n" \
"ds_read_b64_tr_b16 v[162:163], v33 offset:58368\n" \
"v_mfma_f32_16x16x32_bf16 a[184:187], v[92:95], v[164:167], a[184:187]\n" \
"v_cndmask_b32_e32 v233, v232, v235, vcc\n" \
"v_add_u32_e32 v245, s96, v243\n" \
"v_mfma_f32_16x16x32_bf16 a[184:187], v[96:99], v[168:171], a[184:187]\n" \
"s_add_u32 m0, %[mA], 0xc00\n" \
"s_add_u32 s84, s62, 0x6000\n" \
"v_mfma_f32_16x16x32_bf16 a[216:219], v[104:107], v[168:171], a[216:219]\n" \
"buffer_load_dwordx4 v23, s[64:67], s84 offen lds\n" \
"v_mfma_f32_16x16x32_bf16 a[216:219], v[100:103], v[164:167], a[216:219]\n" \
"v_sub_u32_e32 v214, v251, v247\n" \
"v_lshrrev_b32_e32 v222, 16, v221\n" \
"v_mfma_f32_16x16x32_bf16 a[248:251], v[108:111], v[164:167], a[248:251]\n" \
"ds_read_b64_tr_b16 v[164:165], v34 offset:49152\n" \
"ds_read_b64_tr_b16 v[166:167], v34 offset:50176\n" \
"v_mfma_f32_16x16x32_bf16 a[248:251], v[112:115], v[168:171], a[248:251]\n" \
"s_add_u32 m0, %[mB], 0x400\n" \
"s_add_u32 s85, s63, 0x8000\n" \
"v_mfma_f32_16x16x32_bf16 a[156:159], v[88:91], v[176:179], a[156:159]\n" \
"buffer_load_dwordx4 v26, s[68:71], s85 offen lds\n" \
"v_mfma_f32_16x16x32_bf16 a[156:159], v[84:87], v[172:175], a[156:159]\n" \
"ds_read_b64_tr_b16 v[168:169], v34 offset:57344\n" \
"ds_read_b64_tr_b16 v[170:171], v34 offset:58368\n" \
"v_mfma_f32_16x16x32_bf16 a[188:191], v[92:95], v[172:175], a[188:191]\n" \
"v_and_b32_e32 v246, 0xff, v245\n" \
"v_cmp_gt_u32_e32 vcc, 16, v242\n" \
"v_mfma_f32_16x16x32_bf16 a[188:191], v[96:99], v[176:179], a[188:191]\n" \
"s_add_u32 m0, %[mB], 0x4400\n" \
"s_add_u32 s84, s63, 0x8000\n" \
"v_mfma_f32_16x16x32_bf16 a[220:223], v[104:107], v[176:179], a[220:223]\n" \
"buffer_load_dwordx4 v26, s[72:75], s84 offen lds\n" \
"v_mfma_f32_16x16x32_bf16 a[220:223], v[100:103], v[172:175], a[220:223]\n" \
"v_lshlrev_b32_e32 v216, 4, v214\n" \
"v_mul_u32_u24_e32 v218, 18, v222\n" \
"s_nop 0\n" \
"v_mfma_f32_16x16x32_bf16 a[252:255], v[108:111], v[172:175], a[252:255]\n" \
"ds_read_b64_tr_b16 v[172:173], v35 offset:49152\n" \
"ds_read_b64_tr_b16 v[174:175], v35 offset:50176\n" \
"v_mfma_f32_16x16x32_bf16 a[252:255], v[112:115], v[176:179], a[252:255]\n" \
"s_add_u32 m0, %[mA], 0x1000\n" \
"s_add_u32 s85, s62, 0x8000\n" \
"v_mfma_f32_16x16x32_bf16 a[128:131], v[120:123], v[88:91], a[128:131]\n" \
"buffer_load_dwordx4 v22, s[64:67], s85 offen lds\n" \
"v_mfma_f32_16x16x32_bf16 a[128:131], v[116:119], v[84:87], a[128:131]\n" \
"ds_read_b64_tr_b16 v[176:177], v35 offset:57344\n" \
"ds_read_b64_tr_b16 v[178:179], v35 offset:58368\n" \
"v_mfma_f32_16x16x32_bf16 a[160:163], v[116:119], v[92:95], a[160:163]\n" \
"v_cndmask_b32_e32 v244, v243, v246, vcc\n" \
"v_add_u32_e32 v217, s96, v216\n" \
"v_mfma_f32_16x16x32_bf16 a[160:163], v[120:123], v[96:99], a[160:163]\n" \
"s_add_u32 m0, %[mA], 0x1400\n" \
"s_add_u32 s84, s62, 0xa000\n" \
"v_mfma_f32_16x16x32_bf16 a[192:195], v[120:123], v[104:107], a[192:195]\n" \
"buffer_load_dwordx4 v23, s[64:67], s84 offen lds\n" \
"v_mfma_f32_16x16x32_bf16 a[192:195], v[116:119], v[100:103], a[192:195]\n" \
"v_sub_u32_e32 v226, v224, v218\n" \
"s_nop 0\n" \
"v_mul_u32_u24_e32 v228, 0x480, v230\n" \
"v_mfma_f32_16x16x32_bf16 a[224:227], v[116:119], v[108:111], a[224:227]\n" \
"ds_read_b64_tr_b16 v[116:117], v32 offset:32768\n" \
"ds_read_b64_tr_b16 v[118:119], v32 offset:33792\n" \
"v_mfma_f32_16x16x32_bf16 a[224:227], v[120:123], v[112:115], a[224:227]\n" \
"s_add_u32 m0, %[mB], 0x800\n" \
"s_add_u32 s85, s63, 0x10000\n" \
"v_mfma_f32_16x16x32_bf16 a[132:135], v[128:131], v[88:91], a[132:135]\n" \
"buffer_load_dwordx4 v27, s[68:71], s85 offen lds\n" \
"v_mfma_f32_16x16x32_bf16 a[132:135], v[124:127], v[84:87], a[132:135]\n" \
"ds_read_b64_tr_b16 v[120:121], v32 offset:40960\n" \
"ds_read_b64_tr_b16 v[122:123], v32 offset:41984\n" \
"v_mfma_f32_16x16x32_bf16 a[164:167], v[124:127], v[92:95], a[164:167]\n" \
"v_and_b32_e32 v220, 0xff, v217\n" \
"v_cmp_gt_u32_e32 vcc, 16, v214\n" \
"v_mfma_f32_16x16x32_bf16 a[164:167], v[128:131], v[96:99], a[164:167]\n" \
"s_add_u32 m0, %[mB], 0x4800\n" \
"s_add_u32 s84, s63, 0x10000\n" \
"v_mfma_f32_16x16x32_bf16 a[196:199], v[128:131], v[104:107], a[196:199]\n" \
"buffer_load_dwordx4 v27, s[72:75], s84 offen lds\n" \
"v_mfma_f32_16x16x32_bf16 a[196:199], v[124:127], v[100:103], a[196:199]\n" \
"v_lshlrev_b32_e32 v227, 4, v226\n" \
"v_cndmask_b32_e32 v219, v216, v220, vcc\n" \
"v_mfma_f32_16x16x32_bf16 a[228:231], v[124:127], v[108:111], a[228:231]\n" \
"ds_read_b64_tr_b16 v[124:125], v33 offset:32768\n" \
"ds_read_b64_tr_b16 v[126:127], v33 offset:33792\n" \
"v_mfma_f32_16x16x32_bf16 a[228:231], v[128:131], v[112:115], a[228:231]\n" \
"ds_read_b64_tr_b16 v[128:129], v33 offset:40960\n" \
"ds_read_b64_tr_b16 v[130:131], v33 offset:41984\n" \
"v_mfma_f32_16x16x32_bf16 a[136:139], v[136:139], v[88:91], a[136:139]\n" \
"v_add_u32_e32 v230, s96, v227\n" \
"v_and_b32_e32 v229, 0xff, v230\n" \
"v_mfma_f32_16x16x32_bf16 a[136:139], v[132:135], v[84:87], a[136:139]\n" \
"v_cmp_gt_u32_e32 vcc, 16, v226\n" \
"v_mul_u32_u24_e32 v250, 0x480, v213\n" \
"v_mfma_f32_16x16x32_bf16 a[168:171], v[132:135], v[92:95], a[168:171]\n" \
"v_cndmask_b32_e32 v225, v227, v229, vcc\n" \
"v_and_b32_e64 v249, 2, v212\n" \
"v_mfma_f32_16x16x32_bf16 a[168:171], v[136:139], v[96:99], a[168:171]\n" \
"v_mov_b32_e32 v213, 0x100\n" \
"v_mov_b32_e64 v251, s95\n" \
"v_mfma_f32_16x16x32_bf16 a[200:203], v[136:139], v[104:107], a[200:203]\n" \
"v_cmp_ne_u32_e32 vcc, 0, v249\n" \
"s_mov_b32 s76, s8\n" \
"v_mfma_f32_16x16x32_bf16 a[200:203], v[132:135], v[100:103], a[200:203]\n" \
"v_cndmask_b32_e32 v247, v251, v213, vcc\n" \
"s_nop 0\n" \
"s_and_b32 s77, s9, 0xffff\n" \
"v_mfma_f32_16x16x32_bf16 a[232:235], v[132:135], v[108:111], a[232:235]\n" \
"ds_read_b64_tr_b16 v[132:133], v34 offset:32768\n" \
"ds_read_b64_tr_b16 v[134:135], v34 offset:33792\n" \
"v_mfma_f32_16x16x32_bf16 a[232:235], v[136:139], v[112:115], a[232:235]\n" \
"ds_read_b64_tr_b16 v[136:137], v34 offset:40960\n" \
"ds_read_b64_tr_b16 v[138:139], v34 offset:41984\n" \
"v_mfma_f32_16x16x32_bf16 a[140:143], v[144:147], v[88:91], a[140:143]\n" \
"ds_read_b128 v[88:91], v29 offset:32768\n" \
"s_mov_b32 s78, 0x240000\n" \
"v_mfma_f32_16x16x32_bf16 a[140:143], v[140:143], v[84:87], a[140:143]\n" \
"ds_read_b128 v[84:87], v28 offset:32768\n" \
"v_xor_b32_e64 v37, 0x20, v190\n" \
"v_mfma_f32_16x16x32_bf16 a[172:175], v[140:143], v[92:95], a[172:175]\n" \
"ds_read_b128 v[92:95], v28 offset:34816\n" \
"v_xor_b32_e64 v38, 0x40, v190\n" \
"v_mfma_f32_16x16x32_bf16 a[172:175], v[144:147], v[96:99], a[172:175]\n" \
"ds_read_b128 v[96:99], v29 offset:34816\n" \
"v_add_u32_e64 v48, s60, v28\n" \
"v_mfma_f32_16x16x32_bf16 a[204:207], v[144:147], v[104:107], a[204:207]\n" \
"ds_read_b128 v[104:107], v29 offset:36864\n" \
"v_add_u32_e64 v49, s60, v29\n" \
"v_mfma_f32_16x16x32_bf16 a[204:207], v[140:143], v[100:103], a[204:207]\n" \
"ds_read_b128 v[100:103], v28 offset:36864\n" \
"v_xor_b32_e64 v36, 0x0, v190\n" \
"v_mfma_f32_16x16x32_bf16 a[236:239], v[140:143], v[108:111], a[236:239]\n" \
"ds_read_b64_tr_b16 v[140:141], v35 offset:32768\n" \
"ds_read_b64_tr_b16 v[142:143], v35 offset:33792\n" \
"v_mfma_f32_16x16x32_bf16 a[236:239], v[144:147], v[112:115], a[236:239]\n" \
"s_add_u32 m0, %[mA], 0x1800\n" \
"s_waitcnt lgkmcnt(12)\n" \
"s_nop 0\n" \
"v_mfma_f32_16x16x32_bf16 a[16:19], v[56:59], v[152:155], a[16:19]\n" \
"s_add_u32 s85, s62, 0xc000\n" \
"ds_read_b64_tr_b16 v[144:145], v35 offset:40960\n" \
"v_mfma_f32_16x16x32_bf16 a[16:19], v[52:55], v[148:151], a[16:19]\n" \
"buffer_load_dwordx4 v22, s[64:67], s85 offen lds\n" \
"v_mfma_f32_16x16x32_bf16 a[48:51], v[60:63], v[148:151], a[48:51]\n" \
"s_add_u32 m0, %[mA], 0x1c00\n" \
"s_add_u32 s84, s62, 0xe000\n" \
"v_mfma_f32_16x16x32_bf16 a[48:51], v[64:67], v[152:155], a[48:51]\n" \
"ds_read_b64_tr_b16 v[146:147], v35 offset:41984\n" \
"ds_read_b128 v[108:111], v28 offset:38912\n" \
"v_mfma_f32_16x16x32_bf16 a[80:83], v[72:75], v[152:155], a[80:83]\n" \
"buffer_load_dwordx4 v23, s[64:67], s84 offen lds\n" \
"v_mfma_f32_16x16x32_bf16 a[80:83], v[68:71], v[148:151], a[80:83]\n" \
"s_add_u32 m0, %[mB], 0xc00\n" \
"s_add_u32 s85, s63, 0x18000\n" \
"v_mfma_f32_16x16x32_bf16 a[112:115], v[76:79], v[148:151], a[112:115]\n" \
"ds_read_b128 v[112:115], v29 offset:38912\n" \
"v_add_u32_e64 v237, v228, v233\n" \
"v_mfma_f32_16x16x32_bf16 a[112:115], v[80:83], v[152:155], a[112:115]\n" \
"buffer_load_dwordx4 v27, s[68:71], s85 offen lds\n" \
"v_mfma_f32_16x16x32_bf16 a[20:23], v[56:59], v[160:163], a[20:23]\n" \
"s_add_u32 m0, %[mB], 0x4c00\n" \
"s_add_u32 s84, s63, 0x18000\n" \
"v_mfma_f32_16x16x32_bf16 a[20:23], v[52:55], v[156:159], a[20:23]\n" \
"s_lshl_b32 s97, %[w], 4\n" \
"v_lshrrev_b32_e32 v234, 2, v212\n" \
"v_mfma_f32_16x16x32_bf16 a[52:55], v[60:63], v[156:159], a[52:55]\n" \
"buffer_load_dwordx4 v27, s[72:75], s84 offen lds\n" \
"v_mfma_f32_16x16x32_bf16 a[52:55], v[64:67], v[160:163], a[52:55]\n" \
"v_add_u32_e32 v5, 0xfffffc00, v237\n" \
"v_add_u32_e64 v231, s97, v234\n" \
"v_mfma_f32_16x16x32_bf16 a[84:87], v[72:75], v[160:163], a[84:87]\n" \
"v_lshrrev_b32_e32 v235, 5, v231\n" \
"v_lshrrev_b32_e32 v237, 4, v231\n" \
"v_mfma_f32_16x16x32_bf16 a[84:87], v[68:71], v[156:159], a[84:87]\n" \
"v_mul_u32_u24_e32 v239, 0x480, v241\n" \
"v_lshlrev_b32_e32 v232, 4, v235\n" \
"v_mfma_f32_16x16x32_bf16 a[116:119], v[76:79], v[156:159], a[116:119]\n" \
"v_and_b32_e32 v228, 12, v231\n" \
"v_and_b32_e64 v238, 1, v237\n" \
"v_mfma_f32_16x16x32_bf16 a[116:119], v[80:83], v[160:163], a[116:119]\n" \
"s_mov_b32 m0, %[mS]\n" \
"s_mov_b32 s86, s8\n" \
"v_mfma_f32_16x16x32_bf16 a[24:27], v[56:59], v[168:171], a[24:27]\n" \
"s_mov_b32 s87, s9\n" \
"v_add_u32_e32 v233, v232, v228\n" \
"v_mfma_f32_16x16x32_bf16 a[24:27], v[52:55], v[164:167], a[24:27]\n" \
"v_lshlrev_b32_e32 v241, 1, v238\n" \
"v_lshrrev_b32_e32 v236, 1, v231\n" \
"v_mfma_f32_16x16x32_bf16 a[56:59], v[60:63], v[164:167], a[56:59]\n" \
"v_add_u32_e32 v240, v233, v241\n" \
"v_and_b32_e32 v245, 1, v236\n" \
"v_mfma_f32_16x16x32_bf16 a[56:59], v[64:67], v[168:171], a[56:59]\n" \
"v_add_u32_e32 v242, v240, v245\n" \
"v_add_u32_e32 v248, v239, v244\n" \
"v_mfma_f32_16x16x32_bf16 a[88:91], v[72:75], v[168:171], a[88:91]\n" \
"v_mul_u32_u24_e32 v246, 18, v242\n" \
"v_add_u32_e32 v243, 16, v246\n" \
"v_mfma_f32_16x16x32_bf16 a[88:91], v[68:71], v[164:167], a[88:91]\n" \
"v_and_b32_e64 v239, 1, v231\n" \
"v_add_u32_e32 v6, 0xfffff800, v248\n" \
"s_mov_b32 exec_lo, 0x0\n" \
"s_mov_b32 exec_hi, 0x3ffff0\n" \
"global_load_lds_dword v188, s[86:87]\n" \
"s_mov_b64 exec, -1\n" \
"v_mfma_f32_16x16x32_bf16 a[120:123], v[76:79], v[164:167], a[120:123]\n" \
"v_add_u32_e64 v244, v243, v239\n" \
"v_mul_u32_u24_e32 v248, 0x480, v244\n" \
"v_mfma_f32_16x16x32_bf16 a[120:123], v[80:83], v[168:171], a[120:123]\n" \
"v_add_u32_e32 v215, v250, v219\n" \
"v_add_u32_e32 v217, v248, v247\n" \
"v_mfma_f32_16x16x32_bf16 a[28:31], v[56:59], v[176:179], a[28:31]\n" \
"v_and_b32_e64 v214, 16, v20\n" \
"v_add_u32_e32 v7, 0xfffff400, v215\n" \
"v_mfma_f32_16x16x32_bf16 a[28:31], v[52:55], v[172:175], a[28:31]\n" \
"v_add_u32_e32 v9, v217, v214\n" \
"v_and_b32_e32 v216, 3, v212\n" \
"v_mfma_f32_16x16x32_bf16 a[60:63], v[60:63], v[172:175], a[60:63]\n" \
"v_lshlrev_b32_e32 v250, 3, v216\n" \
"v_mul_u32_u24_e32 v220, 0x120, v234\n" \
"v_mfma_f32_16x16x32_bf16 a[60:63], v[64:67], v[176:179], a[60:63]\n" \
"v_mul_u32_u24_e32 v223, 0x480, v222\n" \
"v_add_u32_e32 v10, v220, v250\n" \
"v_mfma_f32_16x16x32_bf16 a[92:95], v[72:75], v[176:179], a[92:95]\n" \
"v_add_u32_e32 v8, v223, v225\n" \
"s_lshl_b32 s99, %[w], 11\n" \
"v_mfma_f32_16x16x32_bf16 a[92:95], v[68:71], v[172:175], a[92:95]\n" \
"s_add_u32 s55, s98, s99\n" \
"s_nop 0\n" \
"v_mfma_f32_16x16x32_bf16 a[124:127], v[76:79], v[172:175], a[124:127]\n" \
"v_mfma_f32_16x16x32_bf16 a[124:127], v[80:83], v[176:179], a[124:127]\n" \
"v_mfma_f32_16x16x32_bf16 a[0:3], v[120:123], v[56:59], a[0:3]\n" \
"v_mfma_f32_16x16x32_bf16 a[0:3], v[116:119], v[52:55], a[0:3]\n" \
"v_mfma_f32_16x16x32_bf16 a[32:35], v[116:119], v[60:63], a[32:35]\n" \
"v_mfma_f32_16x16x32_bf16 a[32:35], v[120:123], v[64:67], a[32:35]\n" \
"v_mfma_f32_16x16x32_bf16 a[64:67], v[120:123], v[72:75], a[64:67]\n" \
"v_mfma_f32_16x16x32_bf16 a[64:67], v[116:119], v[68:71], a[64:67]\n" \
"v_mfma_f32_16x16x32_bf16 a[96:99], v[116:119], v[76:79], a[96:99]\n" \
"v_mfma_f32_16x16x32_bf16 a[96:99], v[120:123], v[80:83], a[96:99]\n" \
"v_mfma_f32_16x16x32_bf16 a[4:7], v[128:131], v[56:59], a[4:7]\n" \
"v_mfma_f32_16x16x32_bf16 a[4:7], v[124:127], v[52:55], a[4:7]\n" \
"v_mfma_f32_16x16x32_bf16 a[36:39], v[124:127], v[60:63], a[36:39]\n" \
"v_mfma_f32_16x16x32_bf16 a[36:39], v[128:131], v[64:67], a[36:39]\n" \
"v_mfma_f32_16x16x32_bf16 a[68:71], v[128:131], v[72:75], a[68:71]\n" \
"v_mfma_f32_16x16x32_bf16 a[68:71], v[124:127], v[68:71], a[68:71]\n" \
"v_mfma_f32_16x16x32_bf16 a[100:103], v[124:127], v[76:79], a[100:103]\n" \
"v_mfma_f32_16x16x32_bf16 a[100:103], v[128:131], v[80:83], a[100:103]\n" \
"s_waitcnt lgkmcnt(0)\n" \
"s_nop 0\n" \
"v_mfma_f32_16x16x32_bf16 a[8:11], v[136:139], v[56:59], a[8:11]\n" \
"v_mfma_f32_16x16x32_bf16 a[8:11], v[132:135], v[52:55], a[8:11]\n" \
"v_mfma_f32_16x16x32_bf16 a[40:43], v[132:135], v[60:63], a[40:43]\n" \
"v_mfma_f32_16x16x32_bf16 a[40:43], v[136:139], v[64:67], a[40:43]\n" \
"v_mfma_f32_16x16x32_bf16 a[72:75], v[136:139], v[72:75], a[72:75]\n" \
"v_mfma_f32_16x16x32_bf16 a[72:75], v[132:135], v[68:71], a[72:75]\n" \
"v_mfma_f32_16x16x32_bf16 a[104:107], v[132:135], v[76:79], a[104:107]\n" \
"v_mfma_f32_16x16x32_bf16 a[104:107], v[136:139], v[80:83], a[104:107]\n" \
"v_mfma_f32_16x16x32_bf16 a[12:15], v[144:147], v[56:59], a[12:15]\n" \
"s_add_u32 s62, s22, 0x380\n" \
"s_and_b32 s62, s62, 0x3ff\n" \
"v_mfma_f32_16x16x32_bf16 a[12:15], v[140:143], v[52:55], a[12:15]\n" \
"s_lshl_b32 s63, s62, 12\n" \
"v_mfma_f32_16x16x32_bf16 a[44:47], v[140:143], v[60:63], a[44:47]\n" \
"v_mfma_f32_16x16x32_bf16 a[44:47], v[144:147], v[64:67], a[44:47]\n" \
"v_mfma_f32_16x16x32_bf16 a[76:79], v[144:147], v[72:75], a[76:79]\n" \
"v_mfma_f32_16x16x32_bf16 a[76:79], v[140:143], v[68:71], a[76:79]\n" \
"v_mfma_f32_16x16x32_bf16 a[108:111], v[140:143], v[76:79], a[108:111]\n" \
"v_mfma_f32_16x16x32_bf16 a[108:111], v[144:147], v[80:83], a[108:111]\n" \
"s_waitcnt vmcnt(1)\n" \
"s_barrier\n" \
"s_mov_b64 exec, 1\n" \
"buffer_store_dword v45, off, s[80:83], s38 offset:64 sc1\n" \
"s_mov_b64 exec, -1\n" \
"v_mfma_f32_16x16x32_bf16 a[128:131], v[120:123], v[88:91], a[128:131]\n" \
"s_add_u32 m0, %[mA], 0x8000\n" \
"s_add_u32 s85, s62, 0x0\n" \
"v_mfma_f32_16x16x32_bf16 a[128:131], v[116:119], v[84:87], a[128:131]\n" \
"ds_read_b128 v[56:59], v29 offset:16384\n" \
"ds_read_b128 v[52:55], v28 offset:16384\n" \
"v_mfma_f32_16x16x32_bf16 a[160:163], v[116:119], v[92:95], a[160:163]\n" \
"buffer_load_dwordx4 v22, s[64:67], s85 offen lds\n" \
"v_mfma_f32_16x16x32_bf16 a[160:163], v[120:123], v[96:99], a[160:163]\n" \
"s_add_u32 m0, %[mA], 0x8400\n" \
"s_add_u32 s84, s62, 0x2000\n" \
"v_mfma_f32_16x16x32_bf16 a[192:195], v[120:123], v[104:107], a[192:195]\n" \
"ds_read_b128 v[60:63], v28 offset:18432\n" \
"ds_read_b128 v[64:67], v29 offset:18432\n" \
"v_mfma_f32_16x16x32_bf16 a[192:195], v[116:119], v[100:103], a[192:195]\n" \
"buffer_load_dwordx4 v23, s[64:67], s84 offen lds\n" \
"v_mfma_f32_16x16x32_bf16 a[224:227], v[116:119], v[108:111], a[224:227]\n" \
"ds_read_b64_tr_b16 v[116:117], v32 offset:0\n" \
"ds_read_b64_tr_b16 v[118:119], v32 offset:1024\n" \
"v_mfma_f32_16x16x32_bf16 a[224:227], v[120:123], v[112:115], a[224:227]\n" \
"s_add_u32 m0, %[mB], 0x8000\n" \
"s_add_u32 s85, s63, 0x0\n" \
"v_mfma_f32_16x16x32_bf16 a[132:135], v[128:131], v[88:91], a[132:135]\n" \
"buffer_load_dwordx4 v26, s[68:71], s85 offen lds\n" \
"v_mfma_f32_16x16x32_bf16 a[132:135], v[124:127], v[84:87], a[132:135]\n" \
"ds_read_b64_tr_b16 v[120:121], v32 offset:8192\n" \
"ds_read_b64_tr_b16 v[122:123], v32 offset:9216\n" \
"v_mfma_f32_16x16x32_bf16 a[164:167], v[124:127], v[92:95], a[164:167]\n" \
"ds_read_b128 v[72:75], v29 offset:20480\n" \
"ds_read_b128 v[68:71], v28 offset:20480\n" \
"v_mfma_f32_16x16x32_bf16 a[164:167], v[128:131], v[96:99], a[164:167]\n" \
"s_add_u32 m0, %[mB], 0xc000\n" \
"s_add_u32 s84, s63, 0x0\n" \
"v_mfma_f32_16x16x32_bf16 a[196:199], v[128:131], v[104:107], a[196:199]\n" \
"buffer_load_dwordx4 v26, s[72:75], s84 offen lds\n" \
"v_mfma_f32_16x16x32_bf16 a[196:199], v[124:127], v[100:103], a[196:199]\n" \
"ds_read_b128 v[76:79], v28 offset:22528\n" \
"ds_read_b128 v[80:83], v29 offset:22528\n" \
"v_mfma_f32_16x16x32_bf16 a[228:231], v[124:127], v[108:111], a[228:231]\n" \
"ds_read_b64_tr_b16 v[124:125], v33 offset:0\n" \
"ds_read_b64_tr_b16 v[126:127], v33 offset:1024\n" \
"v_mfma_f32_16x16x32_bf16 a[228:231], v[128:131], v[112:115], a[228:231]\n" \
"s_add_u32 m0, %[mA], 0x8800\n" \
"s_add_u32 s85, s62, 0x4000\n" \
"v_mfma_f32_16x16x32_bf16 a[136:139], v[136:139], v[88:91], a[136:139]\n" \
"buffer_load_dwordx4 v22, s[64:67], s85 offen lds\n" \
"v_mfma_f32_16x16x32_bf16 a[136:139], v[132:135], v[84:87], a[136:139]\n" \
"ds_read_b64_tr_b16 v[128:129], v33 offset:8192\n" \
"ds_read_b64_tr_b16 v[130:131], v33 offset:9216\n" \
"v_mfma_f32_16x16x32_bf16 a[168:171], v[132:135], v[92:95], a[168:171]\n" \
"v_mov_b32_e64 v45, %[tag]\n" \
"v_mfma_f32_16x16x32_bf16 a[168:171], v[136:139], v[96:99], a[168:171]\n" \
"s_add_u32 m0, %[mA], 0x8c00\n" \
"s_add_u32 s84, s62, 0x6000\n" \
"v_mfma_f32_16x16x32_bf16 a[200:203], v[136:139], v[104:107], a[200:203]\n" \
"buffer_load_dwordx4 v23, s[64:67], s84 offen lds\n" \
"v_mfma_f32_16x16x32_bf16 a[200:203], v[132:135], v[100:103], a[200:203]\n" \
"v_mfma_f32_16x16x32_bf16 a[232:235], v[132:135], v[108:111], a[232:235]\n" \
"ds_read_b64_tr_b16 v[132:133], v34 offset:0\n" \
"ds_read_b64_tr_b16 v[134:135], v34 offset:1024\n" \
"v_mfma_f32_16x16x32_bf16 a[232:235], v[136:139], v[112:115], a[232:235]\n" \
"s_add_u32 m0, %[mB], 0x8400\n" \
"s_add_u32 s85, s63, 0x8000\n" \
"v_mfma_f32_16x16x32_bf16 a[140:143], v[144:147], v[88:91], a[140:143]\n" \
"buffer_load_dwordx4 v26, s[68:71], s85 offen lds\n" \
"v_mfma_f32_16x16x32_bf16 a[140:143], v[140:143], v[84:87], a[140:143]\n" \
"ds_read_b64_tr_b16 v[136:137], v34 offset:8192\n" \
"ds_read_b64_tr_b16 v[138:139], v34 offset:9216\n" \
"v_mfma_f32_16x16x32_bf16 a[172:175], v[140:143], v[92:95], a[172:175]\n" \
"v_mfma_f32_16x16x32_bf16 a[172:175], v[144:147], v[96:99], a[172:175]\n" \
"s_add_u32 m0, %[mB], 0xc400\n" \
"s_add_u32 s84, s63, 0x8000\n" \
"v_mfma_f32_16x16x32_bf16 a[204:207], v[144:147], v[104:107], a[204:207]\n" \
"buffer_load_dwordx4 v26, s[72:75], s84 offen lds\n" \
"v_mfma_f32_16x16x32_bf16 a[204:207], v[140:143], v[100:103], a[204:207]\n" \
"v_mfma_f32_16x16x32_bf16 a[236:239], v[140:143], v[108:111], a[236:239]\n" \
"ds_read_b64_tr_b16 v[140:141], v35 offset:0\n" \
"ds_read_b64_tr_b16 v[142:143], v35 offset:1024\n" \
"L_pf3:\n" \
"s_cbranch_execz L_pf7\n" \
"s_nop 0\n" \
"v_mfma_f32_16x16x32_bf16 a[236:239], v[144:147], v[112:115], a[236:239]\n" \
"s_add_u32 m0, %[mA], 0x9000\n" \
"s_add_u32 s85, s62, 0x8000\n" \
"v_mfma_f32_16x16x32_bf16 a[144:147], v[88:91], v[152:155], a[144:147]\n" \
"buffer_load_dwordx4 v22, s[64:67], s85 offen lds\n" \
"v_mfma_f32_16x16x32_bf16 a[144:147], v[84:87], v[148:151], a[144:147]\n" \
"ds_read_b64_tr_b16 v[144:145], v35 offset:8192\n" \
"ds_read_b64_tr_b16 v[146:147], v35 offset:9216\n" \
"v_mfma_f32_16x16x32_bf16 a[176:179], v[92:95], v[148:151], a[176:179]\n" \
"v_mfma_f32_16x16x32_bf16 a[176:179], v[96:99], v[152:155], a[176:179]\n" \
"s_add_u32 m0, %[mA], 0x9400\n" \
"s_add_u32 s84, s62, 0xa000\n" \
"v_mfma_f32_16x16x32_bf16 a[208:211], v[104:107], v[152:155], a[208:211]\n" \
"buffer_load_dwordx4 v23, s[64:67], s84 offen lds\n" \
"v_mfma_f32_16x16x32_bf16 a[208:211], v[100:103], v[148:151], a[208:211]\n" \
"v_mfma_f32_16x16x32_bf16 a[240:243], v[108:111], v[148:151], a[240:243]\n" \
"ds_read_b64_tr_b16 v[148:149], v32 offset:16384\n" \
"ds_read_b64_tr_b16 v[150:151], v32 offset:17408\n" \
"v_mfma_f32_16x16x32_bf16 a[240:243], v[112:115], v[152:155], a[240:243]\n" \
"s_add_u32 m0, %[mB], 0x8800\n" \
"s_add_u32 s85, s63, 0x10000\n" \
"v_mfma_f32_16x16x32_bf16 a[148:151], v[88:91], v[160:163], a[148:151]\n" \
"buffer_load_dwordx4 v27, s[68:71], s85 offen lds\n" \
"v_mfma_f32_16x16x32_bf16 a[148:151], v[84:87], v[156:159], a[148:151]\n" \
"ds_read_b64_tr_b16 v[152:153], v32 offset:24576\n" \
"ds_read_b64_tr_b16 v[154:155], v32 offset:25600\n" \
"v_mfma_f32_16x16x32_bf16 a[180:183], v[92:95], v[156:159], a[180:183]\n" \
"v_mfma_f32_16x16x32_bf16 a[180:183], v[96:99], v[160:163], a[180:183]\n" \
"s_add_u32 m0, %[mB], 0xc800\n" \
"s_add_u32 s84, s63, 0x10000\n" \
"v_mfma_f32_16x16x32_bf16 a[212:215], v[104:107], v[160:163], a[212:215]\n" \
"buffer_load_dwordx4 v27, s[72:75], s84 offen lds\n" \
"v_mfma_f32_16x16x32_bf16 a[212:215], v[100:103], v[156:159], a[212:215]\n" \
"v_mfma_f32_16x16x32_bf16 a[244:247], v[108:111], v[156:159], a[244:247]\n" \
"ds_read_b64_tr_b16 v[156:157], v33 offset:16384\n" \
"ds_read_b64_tr_b16 v[158:159], v33 offset:17408\n" \
"v_mfma_f32_16x16x32_bf16 a[244:247], v[112:115], v[160:163], a[244:247]\n" \
"ds_read_b64_tr_b16 v[160:161], v33 offset:24576\n" \
"ds_read_b64_tr_b16 v[162:163], v33 offset:25600\n" \
"v_mfma_f32_16x16x32_bf16 a[152:155], v[88:91], v[168:171], a[152:155]\n" \
"v_mfma_f32_16x16x32_bf16 a[152:155], v[84:87], v[164:167], a[152:155]\n" \
"v_mfma_f32_16x16x32_bf16 a[184:187], v[92:95], v[164:167], a[184:187]\n" \
"v_mfma_f32_16x16x32_bf16 a[184:187], v[96:99], v[168:171], a[184:187]\n" \
"v_mfma_f32_16x16x32_bf16 a[216:219], v[104:107], v[168:171], a[216:219]\n" \
"v_mfma_f32_16x16x32_bf16 a[216:219], v[100:103], v[164:167], a[216:219]\n" \
"v_mfma_f32_16x16x32_bf16 a[248:251], v[108:111], v[164:167], a[248:251]\n" \
"ds_read_b64_tr_b16 v[164:165], v34 offset:16384\n" \
"ds_read_b64_tr_b16 v[166:167], v34 offset:17408\n" \
"v_mfma_f32_16x16x32_bf16 a[248:251], v[112:115], v[168:171], a[248:251]\n" \
"ds_read_b64_tr_b16 v[168:169], v34 offset:24576\n" \
"ds_read_b64_tr_b16 v[170:171], v34 offset:25600\n" \
"v_mfma_f32_16x16x32_bf16 a[156:159], v[88:91], v[176:179], a[156:159]\n" \
"ds_read_b128 v[88:91], v29 offset:0\n" \
"v_mfma_f32_16x16x32_bf16 a[156:159], v[84:87], v[172:175], a[156:159]\n" \
"ds_read_b128 v[84:87], v28 offset:0\n" \
"v_mfma_f32_16x16x32_bf16 a[188:191], v[92:95], v[172:175], a[188:191]\n" \
"ds_read_b128 v[92:95], v28 offset:2048\n" \
"v_mfma_f32_16x16x32_bf16 a[188:191], v[96:99], v[176:179], a[188:191]\n" \
"ds_read_b128 v[96:99], v29 offset:2048\n" \
"v_mfma_f32_16x16x32_bf16 a[220:223], v[104:107], v[176:179], a[220:223]\n" \
"ds_read_b128 v[104:107], v29 offset:4096\n" \
"v_mfma_f32_16x16x32_bf16 a[220:223], v[100:103], v[172:175], a[220:223]\n" \
"ds_read_b128 v[100:103], v28 offset:4096\n" \
"v_mfma_f32_16x16x32_bf16 a[252:255], v[108:111], v[172:175], a[252:255]\n" \
"ds_read_b64_tr_b16 v[172:173], v35 offset:16384\n" \
"ds_read_b64_tr_b16 v[174:175], v35 offset:17408\n" \
"v_mfma_f32_16x16x32_bf16 a[252:255], v[112:115], v[176:179], a[252:255]\n" \
"s_add_u32 m0, %[mA], 0x9800\n" \
"s_waitcnt lgkmcnt(12)\n" \
"s_nop 0\n" \
"v_mfma_f32_16x16x32_bf16 a[0:3], v[120:123], v[56:59], a[0:3]\n" \
"s_add_u32 s85, s62, 0xc000\n" \
"ds_read_b64_tr_b16 v[176:177], v35 offset:24576\n" \
"v_mfma_f32_16x16x32_bf16 a[0:3], v[116:119], v[52:55], a[0:3]\n" \
"buffer_load_dwordx4 v22, s[64:67], s85 offen lds\n" \
"v_mfma_f32_16x16x32_bf16 a[32:35], v[116:119], v[60:63], a[32:35]\n" \
"s_add_u32 m0, %[mA], 0x9c00\n" \
"s_add_u32 s84, s62, 0xe000\n" \
"v_mfma_f32_16x16x32_bf16 a[32:35], v[120:123], v[64:67], a[32:35]\n" \
"ds_read_b64_tr_b16 v[178:179], v35 offset:25600\n" \
"ds_read_b128 v[108:111], v28 offset:6144\n" \
"v_mfma_f32_16x16x32_bf16 a[64:67], v[120:123], v[72:75], a[64:67]\n" \
"buffer_load_dwordx4 v23, s[64:67], s84 offen lds\n" \
"v_mfma_f32_16x16x32_bf16 a[64:67], v[116:119], v[68:71], a[64:67]\n" \
"s_add_u32 m0, %[mB], 0x8c00\n" \
"s_add_u32 s85, s63, 0x18000\n" \
"v_mfma_f32_16x16x32_bf16 a[96:99], v[116:119], v[76:79], a[96:99]\n" \
"ds_read_b128 v[112:115], v29 offset:6144\n" \
"s_mov_b32 s62, s23\n" \
"v_mfma_f32_16x16x32_bf16 a[96:99], v[120:123], v[80:83], a[96:99]\n" \
"buffer_load_dwordx4 v27, s[68:71], s85 offen lds\n" \
"v_mfma_f32_16x16x32_bf16 a[4:7], v[128:131], v[56:59], a[4:7]\n" \
"s_add_u32 m0, %[mB], 0xcc00\n" \
"s_add_u32 s84, s63, 0x18000\n" \
"v_mfma_f32_16x16x32_bf16 a[4:7], v[124:127], v[52:55], a[4:7]\n" \
"s_mov_b32 s68, s10\n" \
"s_and_b32 s69, s11, 0xffff\n" \
"v_mfma_f32_16x16x32_bf16 a[36:39], v[124:127], v[60:63], a[36:39]\n" \
"buffer_load_dwordx4 v27, s[72:75], s84 offen lds\n" \
"s_mov_b32 s73, s52\n" \
"s_add_u32 s74, s52, 0x4000\n" \
"s_add_u32 s75, s52, 0x8000\n" \
"v_mfma_f32_16x16x32_bf16 a[36:39], v[128:131], v[64:67], a[36:39]\n" \
"s_lshl_b32 s72, s62, 6\n" \
"s_add_u32 s72, s72, s37\n" \
"v_mfma_f32_16x16x32_bf16 a[68:71], v[128:131], v[72:75], a[68:71]\n" \
"v_bfe_u32 v44, v20, 2, 5\n" \
"s_mov_b32 s70, 0x480000\n" \
"v_mfma_f32_16x16x32_bf16 a[68:71], v[124:127], v[68:71], a[68:71]\n" \
"buffer_load_dword v44, v44, s[80:83], s39 offen offset:64 sc1\n" \
"v_mfma_f32_16x16x32_bf16 a[100:103], v[124:127], v[76:79], a[100:103]\n" \
"v_add_u32_e32 v22, s61, v28\n" \
"v_add_u32_e64 v23, s61, v29\n" \
"v_mfma_f32_16x16x32_bf16 a[100:103], v[128:131], v[80:83], a[100:103]\n" \
"s_mov_b32 m0, %[mS]\n" \
"s_mov_b32 s86, s18\n" \
"v_mfma_f32_16x16x32_bf16 a[8:11], v[136:139], v[56:59], a[8:11]\n" \
"s_mov_b32 s87, s19\n" \
"v_mfma_f32_16x16x32_bf16 a[8:11], v[132:135], v[52:55], a[8:11]\n" \
"v_mfma_f32_16x16x32_bf16 a[40:43], v[132:135], v[60:63], a[40:43]\n" \
"v_mfma_f32_16x16x32_bf16 a[40:43], v[136:139], v[64:67], a[40:43]\n" \
"v_mfma_f32_16x16x32_bf16 a[72:75], v[136:139], v[72:75], a[72:75]\n" \
"v_mfma_f32_16x16x32_bf16 a[72:75], v[132:135], v[68:71], a[72:75]\n" \
"s_mov_b32 exec_lo, 0xfffc0000\n" \
"s_mov_b32 exec_hi, 0xf\n" \
"global_load_lds_dword v16, s[86:87]\n" \
"s_mov_b64 exec, -1\n" \
"v_mfma_f32_16x16x32_bf16 a[104:107], v[132:135], v[76:79], a[104:107]\n" \
"s_mov_b32 m0, %[mS]\n" \
"s_mov_b32 s86, s16\n" \
"v_mfma_f32_16x16x32_bf16 a[104:107], v[136:139], v[80:83], a[104:107]\n" \
"s_mov_b32 s87, s17\n" \
"v_mfma_f32_16x16x32_bf16 a[12:15], v[144:147], v[56:59], a[12:15]\n" \
"v_mfma_f32_16x16x32_bf16 a[12:15], v[140:143], v[52:55], a[12:15]\n" \
"v_mfma_f32_16x16x32_bf16 a[44:47], v[140:143], v[60:63], a[44:47]\n" \
"s_mov_b32 exec_lo, 0x0\n" \
"s_mov_b32 exec_hi, 0x3f0\n" \
"global_load_lds_dword v16, s[86:87]\n" \
"s_mov_b64 exec, -1\n" \
"v_mfma_f32_16x16x32_bf16 a[44:47], v[144:147], v[64:67], a[44:47]\n" \
"v_mfma_f32_16x16x32_bf16 a[76:79], v[144:147], v[72:75], a[76:79]\n" \
"v_mfma_f32_16x16x32_bf16 a[76:79], v[140:143], v[68:71], a[76:79]\n" \
"v_mfma_f32_16x16x32_bf16 a[108:111], v[140:143], v[76:79], a[108:111]\n" \
"v_mfma_f32_16x16x32_bf16 a[108:111], v[144:147], v[80:83], a[108:111]\n" \
"v_mfma_f32_16x16x32_bf16 a[16:19], v[56:59], v[152:155], a[16:19]\n" \
"v_mfma_f32_16x16x32_bf16 a[16:19], v[52:55], v[148:151], a[16:19]\n" \
"v_mfma_f32_16x16x32_bf16 a[48:51], v[60:63], v[148:151], a[48:51]\n" \
"v_mfma_f32_16x16x32_bf16 a[48:51], v[64:67], v[152:155], a[48:51]\n" \
"v_mfma_f32_16x16x32_bf16 a[80:83], v[72:75], v[152:155], a[80:83]\n" \
"v_mfma_f32_16x16x32_bf16 a[80:83], v[68:71], v[148:151], a[80:83]\n" \
"v_mfma_f32_16x16x32_bf16 a[112:115], v[76:79], v[148:151], a[112:115]\n" \
"v_mfma_f32_16x16x32_bf16 a[112:115], v[80:83], v[152:155], a[112:115]\n" \
"v_mfma_f32_16x16x32_bf16 a[20:23], v[56:59], v[160:163], a[20:23]\n" \
"v_mfma_f32_16x16x32_bf16 a[20:23], v[52:55], v[156:159], a[20:23]\n" \
"v_mfma_f32_16x16x32_bf16 a[52:55], v[60:63], v[156:159], a[52:55]\n" \
"v_mfma_f32_16x16x32_bf16 a[52:55], v[64:67], v[160:163], a[52:55]\n" \
"v_mfma_f32_16x16x32_bf16 a[84:87], v[72:75], v[160:163], a[84:87]\n" \
"v_mfma_f32_16x16x32_bf16 a[84:87], v[68:71], v[156:159], a[84:87]\n" \
"v_mfma_f32_16x16x32_bf16 a[116:119], v[76:79], v[156:159], a[116:119]\n" \
"v_mfma_f32_16x16x32_bf16 a[116:119], v[80:83], v[160:163], a[116:119]\n" \
"s_waitcnt lgkmcnt(0)\n" \
"v_mfma_f32_16x16x32_bf16 a[24:27], v[56:59], v[168:171], a[24:27]\n" \
"v_mfma_f32_16x16x32_bf16 a[24:27], v[52:55], v[164:167], a[24:27]\n" \
"v_mfma_f32_16x16x32_bf16 a[56:59], v[60:63], v[164:167], a[56:59]\n" \
"v_mfma_f32_16x16x32_bf16 a[56:59], v[64:67], v[168:171], a[56:59]\n" \
"v_mfma_f32_16x16x32_bf16 a[88:91], v[72:75], v[168:171], a[88:91]\n" \
"v_mfma_f32_16x16x32_bf16 a[88:91], v[68:71], v[164:167], a[88:91]\n" \
"v_mfma_f32_16x16x32_bf16 a[120:123], v[76:79], v[164:167], a[120:123]\n" \
"v_mfma_f32_16x16x32_bf16 a[120:123], v[80:83], v[168:171], a[120:123]\n" \
"v_mfma_f32_16x16x32_bf16 a[28:31], v[56:59], v[176:179], a[28:31]\n" \
"s_waitcnt vmcnt(2)\n" \
"v_cmp_ne_u32_e32 vcc, %[tag], v44\n" \
"v_mfma_f32_16x16x32_bf16 a[28:31], v[52:55], v[172:175], a[28:31]\n" \
"v_mfma_f32_16x16x32_bf16 a[60:63], v[60:63], v[172:175], a[60:63]\n" \
"v_mfma_f32_16x16x32_bf16 a[60:63], v[64:67], v[176:179], a[60:63]\n" \
"v_mfma_f32_16x16x32_bf16 a[92:95], v[72:75], v[176:179], a[92:95]\n" \
"v_mfma_f32_16x16x32_bf16 a[92:95], v[68:71], v[172:175], a[92:95]\n" \
"s_cbranch_vccnz L_pl_%=\n" \
"L_rl_%=:\n" \
"s_lshl_b32 s63, s62, 12\n" \
"v_mfma_f32_16x16x32_bf16 a[124:127], v[76:79], v[172:175], a[124:127]\n" \
"v_mfma_f32_16x16x32_bf16 a[124:127], v[80:83], v[176:179], a[124:127]\n" \
"s_barrier\n" \
"v_mfma_f32_16x16x32_bf16 a[144:147], v[88:91], v[152:155], a[144:147]\n" \
"s_add_u32 m0, s40, 0x0\n" \
"s_add_u32 s85, s72, 0x0\n" \
"v_mfma_f32_16x16x32_bf16 a[144:147], v[84:87], v[148:151], a[144:147]\n" \
"buffer_load_dwordx4 v20, s[80:83], s85 offen sc1 lds\n" \
"v_mfma_f32_16x16x32_bf16 a[176:179], v[92:95], v[148:151], a[176:179]\n" \
"s_add_u32 m0, s40, 0x1000\n" \
"s_add_u32 s84, s72, 0x1000\n" \
"v_mfma_f32_16x16x32_bf16 a[176:179], v[96:99], v[152:155], a[176:179]\n" \
"ds_read_b128 v[56:59], v29 offset:49152\n" \
"ds_read_b128 v[52:55], v28 offset:49152\n" \
"v_mfma_f32_16x16x32_bf16 a[208:211], v[104:107], v[152:155], a[208:211]\n" \
"buffer_load_dwordx4 v20, s[80:83], s84 offen sc1 lds\n" \
"v_mfma_f32_16x16x32_bf16 a[208:211], v[100:103], v[148:151], a[208:211]\n" \
"ds_read_b128 v[60:63], v28 offset:51200\n" \
"ds_read_b128 v[64:67], v29 offset:51200\n" \
"v_mfma_f32_16x16x32_bf16 a[240:243], v[108:111], v[148:151], a[240:243]\n" \
"s_add_u32 m0, s40, 0x3000\n" \
"s_add_u32 s85, s62, 0x0\n" \
"v_mfma_f32_16x16x32_bf16 a[240:243], v[112:115], v[152:155], a[240:243]\n" \
"buffer_load_dwordx4 v21, s[76:79], s85 offen lds\n" \
"v_mfma_f32_16x16x32_bf16 a[148:151], v[88:91], v[160:163], a[148:151]\n" \
"ds_read_b64_tr_b16 v[152:153], v32 offset:57344\n" \
"ds_read_b64_tr_b16 v[154:155], v32 offset:58368\n" \
"v_mfma_f32_16x16x32_bf16 a[148:151], v[84:87], v[156:159], a[148:151]\n" \
"ds_read_b64_tr_b16 v[148:149], v32 offset:49152\n" \
"ds_read_b64_tr_b16 v[150:151], v32 offset:50176\n" \
"v_mfma_f32_16x16x32_bf16 a[180:183], v[92:95], v[156:159], a[180:183]\n" \
"s_add_u32 m0, %[mA], 0x0\n" \
"s_add_u32 s84, s62, 0x0\n" \
"s_nop 0\n" \
"v_mfma_f32_16x16x32_bf16 a[180:183], v[96:99], v[160:163], a[180:183]\n" \
"buffer_load_dwordx4 v24, s[76:79], s84 offen lds\n" \
"v_mfma_f32_16x16x32_bf16 a[212:215], v[104:107], v[160:163], a[212:215]\n" \
"ds_read_b128 v[72:75], v29 offset:53248\n" \
"ds_read_b128 v[68:71], v28 offset:53248\n" \
"v_mfma_f32_16x16x32_bf16 a[212:215], v[100:103], v[156:159], a[212:215]\n" \
"s_add_u32 m0, %[mA], 0x400\n" \
"s_add_u32 s85, s62, 0x2400\n" \
"v_mfma_f32_16x16x32_bf16 a[244:247], v[108:111], v[156:159], a[244:247]\n" \
"buffer_load_dwordx4 v25, s[76:79], s85 offen lds\n" \
"v_mfma_f32_16x16x32_bf16 a[244:247], v[112:115], v[160:163], a[244:247]\n" \
"ds_read_b128 v[76:79], v28 offset:55296\n" \
"ds_read_b128 v[80:83], v29 offset:55296\n" \
"v_mfma_f32_16x16x32_bf16 a[152:155], v[88:91], v[168:171], a[152:155]\n" \
"ds_read_b64_tr_b16 v[160:161], v33 offset:57344\n" \
"ds_read_b64_tr_b16 v[162:163], v33 offset:58368\n" \
"v_mfma_f32_16x16x32_bf16 a[152:155], v[84:87], v[164:167], a[152:155]\n" \
"s_add_u32 m0, %[mB], 0x0\n" \
"s_add_u32 s84, s63, 0x0\n" \
"v_mfma_f32_16x16x32_bf16 a[184:187], v[92:95], v[164:167], a[184:187]\n" \
"buffer_load_dwordx4 v26, s[68:71], s84 offen lds\n" \
"v_mfma_f32_16x16x32_bf16 a[184:187], v[96:99], v[168:171], a[184:187]\n" \
"ds_read_b64_tr_b16 v[156:157], v33 offset:49152\n" \
"ds_read_b64_tr_b16 v[158:159], v33 offset:50176\n" \
"v_mfma_f32_16x16x32_bf16 a[216:219], v[104:107], v[168:171], a[216:219]\n" \
"v_mfma_f32_16x16x32_bf16 a[216:219], v[100:103], v[164:167], a[216:219]\n" \
"s_add_u32 m0, %[mA], 0x800\n" \
"s_add_u32 s85, s62, 0x4800\n" \
"v_mfma_f32_16x16x32_bf16 a[248:251], v[108:111], v[164:167], a[248:251]\n" \
"buffer_load_dwordx4 v24, s[76:79], s85 offen lds\n" \
"v_mfma_f32_16x16x32_bf16 a[248:251], v[112:115], v[168:171], a[248:251]\n" \
"ds_read_b64_tr_b16 v[168:169], v34 offset:57344\n" \
"ds_read_b64_tr_b16 v[170:171], v34 offset:58368\n" \
"v_mfma_f32_16x16x32_bf16 a[156:159], v[88:91], v[176:179], a[156:159]\n" \
"s_add_u32 m0, %[mA], 0xc00\n" \
"s_add_u32 s84, s62, 0x6c00\n" \
"v_mfma_f32_16x16x32_bf16 a[156:159], v[84:87], v[172:175], a[156:159]\n" \
"buffer_load_dwordx4 v25, s[76:79], s84 offen lds\n" \
"v_mfma_f32_16x16x32_bf16 a[188:191], v[92:95], v[172:175], a[188:191]\n" \
"ds_read_b64_tr_b16 v[164:165], v34 offset:49152\n" \
"ds_read_b64_tr_b16 v[166:167], v34 offset:50176\n" \
"v_mfma_f32_16x16x32_bf16 a[188:191], v[96:99], v[176:179], a[188:191]\n" \
"v_mfma_f32_16x16x32_bf16 a[220:223], v[104:107], v[176:179], a[220:223]\n" \
"s_add_u32 m0, %[mB], 0x400\n" \
"s_add_u32 s85, s63, 0x8000\n" \
"v_mfma_f32_16x16x32_bf16 a[220:223], v[100:103], v[172:175], a[220:223]\n" \
"buffer_load_dwordx4 v26, s[68:71], s85 offen lds\n" \
"v_mfma_f32_16x16x32_bf16 a[252:255], v[108:111], v[172:175], a[252:255]\n" \
"ds_read_b64_tr_b16 v[172:173], v35 offset:49152\n" \
"ds_read_b64_tr_b16 v[174:175], v35 offset:50176\n" \
"v_mfma_f32_16x16x32_bf16 a[252:255], v[112:115], v[176:179], a[252:255]\n" \
"ds_read_b64_tr_b16 v[176:177], v35 offset:57344\n" \
"ds_read_b64_tr_b16 v[178:179], v35 offset:58368\n" \
"v_mfma_f32_16x16x32_bf16 a[128:131], v[120:123], v[88:91], a[128:131]\n" \
"s_add_u32 m0, %[mA], 0x1000\n" \
"s_add_u32 s84, s62, 0x9000\n" \
"v_mfma_f32_16x16x32_bf16 a[128:131], v[116:119], v[84:87], a[128:131]\n" \
"buffer_load_dwordx4 v24, s[76:79], s84 offen lds\n" \
"v_mfma_f32_16x16x32_bf16 a[160:163], v[116:119], v[92:95], a[160:163]\n" \
"v_mfma_f32_16x16x32_bf16 a[160:163], v[120:123], v[96:99], a[160:163]\n" \
"s_add_u32 m0, %[mA], 0x1400\n" \
"s_add_u32 s85, s62, 0xb400\n" \
"v_mfma_f32_16x16x32_bf16 a[192:195], v[120:123], v[104:107], a[192:195]\n" \
"buffer_load_dwordx4 v25, s[76:79], s85 offen lds\n" \
"v_mfma_f32_16x16x32_bf16 a[192:195], v[116:119], v[100:103], a[192:195]\n" \
"v_mfma_f32_16x16x32_bf16 a[224:227], v[116:119], v[108:111], a[224:227]\n" \
"ds_read_b64_tr_b16 v[116:117], v32 offset:32768\n" \
"ds_read_b64_tr_b16 v[118:119], v32 offset:33792\n" \
"v_mfma_f32_16x16x32_bf16 a[224:227], v[120:123], v[112:115], a[224:227]\n" \
"s_add_u32 m0, %[mB], 0x800\n" \
"s_add_u32 s84, s63, 0x10000\n" \
"v_mfma_f32_16x16x32_bf16 a[132:135], v[128:131], v[88:91], a[132:135]\n" \
"buffer_load_dwordx4 v27, s[68:71], s84 offen lds\n" \
"v_mfma_f32_16x16x32_bf16 a[132:135], v[124:127], v[84:87], a[132:135]\n" \
"ds_read_b64_tr_b16 v[120:121], v32 offset:40960\n" \
"ds_read_b64_tr_b16 v[122:123], v32 offset:41984\n" \
"v_mfma_f32_16x16x32_bf16 a[164:167], v[124:127], v[92:95], a[164:167]\n" \
"v_mfma_f32_16x16x32_bf16 a[164:167], v[128:131], v[96:99], a[164:167]\n" \
"s_add_u32 m0, %[mA], 0x1800\n" \
"s_add_u32 s85, s62, 0xd800\n" \
"v_mfma_f32_16x16x32_bf16 a[196:199], v[128:131], v[104:107], a[196:199]\n" \
"buffer_load_dwordx4 v24, s[76:79], s85 offen lds\n" \
"v_mfma_f32_16x16x32_bf16 a[196:199], v[124:127], v[100:103], a[196:199]\n" \
"v_mfma_f32_16x16x32_bf16 a[228:231], v[124:127], v[108:111], a[228:231]\n" \
"ds_read_b64_tr_b16 v[124:125], v33 offset:32768\n" \
"ds_read_b64_tr_b16 v[126:127], v33 offset:33792\n" \
"v_mfma_f32_16x16x32_bf16 a[228:231], v[128:131], v[112:115], a[228:231]\n" \
"ds_read_b64_tr_b16 v[128:129], v33 offset:40960\n" \
"ds_read_b64_tr_b16 v[130:131], v33 offset:41984\n" \
"v_mfma_f32_16x16x32_bf16 a[136:139], v[136:139], v[88:91], a[136:139]\n" \
"v_mfma_f32_16x16x32_bf16 a[136:139], v[132:135], v[84:87], a[136:139]\n" \
"v_mfma_f32_16x16x32_bf16 a[168:171], v[132:135], v[92:95], a[168:171]\n" \
"v_mfma_f32_16x16x32_bf16 a[168:171], v[136:139], v[96:99], a[168:171]\n" \
"v_mfma_f32_16x16x32_bf16 a[200:203], v[136:139], v[104:107], a[200:203]\n" \
"v_mfma_f32_16x16x32_bf16 a[200:203], v[132:135], v[100:103], a[200:203]\n" \
"v_mfma_f32_16x16x32_bf16 a[232:235], v[132:135], v[108:111], a[232:235]\n" \
"ds_read_b64_tr_b16 v[132:133], v34 offset:32768\n" \
"ds_read_b64_tr_b16 v[134:135], v34 offset:33792\n" \
"v_mfma_f32_16x16x32_bf16 a[232:235], v[136:139], v[112:115], a[232:235]\n" \
"ds_read_b64_tr_b16 v[136:137], v34 offset:40960\n" \
"ds_read_b64_tr_b16 v[138:139], v34 offset:41984\n" \
"v_mfma_f32_16x16x32_bf16 a[140:143], v[144:147], v[88:91], a[140:143]\n" \
"ds_read_b128 v[88:91], v29 offset:32768\n" \
"v_mfma_f32_16x16x32_bf16 a[140:143], v[140:143], v[84:87], a[140:143]\n" \
"ds_read_b128 v[84:87], v28 offset:32768\n" \
"v_mfma_f32_16x16x32_bf16 a[172:175], v[140:143], v[92:95], a[172:175]\n" \
"ds_read_b128 v[92:95], v28 offset:34816\n" \
"v_mfma_f32_16x16x32_bf16 a[172:175], v[144:147], v[96:99], a[172:175]\n" \
"ds_read_b128 v[96:99], v29 offset:34816\n" \
"v_mfma_f32_16x16x32_bf16 a[204:207], v[144:147], v[104:107], a[204:207]\n" \
"ds_read_b128 v[104:107], v29 offset:36864\n" \
"v_mfma_f32_16x16x32_bf16 a[204:207], v[140:143], v[100:103], a[204:207]\n" \
"ds_read_b128 v[100:103], v28 offset:36864\n" \
"v_mfma_f32_16x16x32_bf16 a[236:239], v[140:143], v[108:111], a[236:239]\n" \
"ds_read_b64_tr_b16 v[140:141], v35 offset:32768\n" \
"ds_read_b64_tr_b16 v[142:143], v35 offset:33792\n" \
"v_mfma_f32_16x16x32_bf16 a[236:239], v[144:147], v[112:115], a[236:239]\n" \
"s_add_u32 m0, %[mA], 0x1c00\n" \
"s_waitcnt lgkmcnt(12)\n" \
"s_nop 0\n" \
"v_mfma_f32_16x16x32_bf16 a[16:19], v[56:59], v[152:155], a[16:19]\n" \
"s_add_u32 s85, s62, 0xfc00\n" \
"ds_read_b64_tr_b16 v[144:145], v35 offset:40960\n" \
"v_mfma_f32_16x16x32_bf16 a[16:19], v[52:55], v[148:151], a[16:19]\n" \
"buffer_load_dwordx4 v25, s[76:79], s85 offen lds\n" \
"v_mfma_f32_16x16x32_bf16 a[48:51], v[60:63], v[148:151], a[48:51]\n" \
"s_add_u32 m0, %[mB], 0xc00\n" \
"s_add_u32 s84, s63, 0x18000\n" \
"v_mfma_f32_16x16x32_bf16 a[48:51], v[64:67], v[152:155], a[48:51]\n" \
"ds_read_b64_tr_b16 v[146:147], v35 offset:41984\n" \
"ds_read_b128 v[108:111], v28 offset:38912\n" \
"v_mfma_f32_16x16x32_bf16 a[80:83], v[72:75], v[152:155], a[80:83]\n" \
"buffer_load_dwordx4 v27, s[68:71], s84 offen lds\n" \
"v_mfma_f32_16x16x32_bf16 a[80:83], v[68:71], v[148:151], a[80:83]\n" \
"ds_read_b128 v[112:115], v29 offset:38912\n" \
"s_add_u32 s62, s62, 0x80\n" \
"v_mfma_f32_16x16x32_bf16 a[112:115], v[76:79], v[148:151], a[112:115]\n" \
"s_cmp_lt_u32 s62, 0x480\n" \
"s_cselect_b32 s62, s62, 0\n" \
"v_mfma_f32_16x16x32_bf16 a[112:115], v[80:83], v[152:155], a[112:115]\n" \
"s_lshl_b32 s72, s62, 6\n" \
"s_add_u32 s72, s72, s37\n" \
"v_mfma_f32_16x16x32_bf16 a[20:23], v[56:59], v[160:163], a[20:23]\n" \
"s_lshl_b32 s63, s62, 12\n" \
"v_add_u32_e64 v28, s59, v28\n" \
"v_mfma_f32_16x16x32_bf16 a[20:23], v[52:55], v[156:159], a[20:23]\n" \
"v_add_u32_e64 v29, s59, v29\n" \
"v_accvgpr_read_b32 v184, a16\n" \
"v_mfma_f32_16x16x32_bf16 a[52:55], v[60:63], v[156:159], a[52:55]\n" \
"v_accvgpr_read_b32 v185, a17\n" \
"v_accvgpr_read_b32 v186, a18\n" \
"v_mfma_f32_16x16x32_bf16 a[52:55], v[64:67], v[160:163], a[52:55]\n" \
"v_accvgpr_read_b32 v187, a19\n" \
"v_accvgpr_write_b32 a16, 0\n" \
"v_mfma_f32_16x16x32_bf16 a[84:87], v[72:75], v[160:163], a[84:87]\n" \
"v_accvgpr_write_b32 a17, 0\n" \
"v_accvgpr_write_b32 a18, 0\n" \
"v_mfma_f32_16x16x32_bf16 a[84:87], v[68:71], v[156:159], a[84:87]\n" \
"v_accvgpr_write_b32 a19, 0\n" \
"v_accvgpr_read_b32 v180, a48\n" \
"v_mfma_f32_16x16x32_bf16 a[116:119], v[76:79], v[156:159], a[116:119]\n" \
"v_accvgpr_read_b32 v181, a49\n" \
"v_accvgpr_read_b32 v182, a50\n" \
"v_mfma_f32_16x16x32_bf16 a[116:119], v[80:83], v[160:163], a[116:119]\n" \
"v_accvgpr_read_b32 v183, a51\n" \
"v_accvgpr_read_b32 v188, a80\n" \
"v_mfma_f32_16x16x32_bf16 a[24:27], v[56:59], v[168:171], a[24:27]\n" \
"v_accvgpr_read_b32 v189, a81\n" \
"v_accvgpr_read_b32 v190, a82\n" \
"v_mfma_f32_16x16x32_bf16 a[24:27], v[52:55], v[164:167], a[24:27]\n" \
"v_accvgpr_read_b32 v191, a83\n" \
"v_cvt_pk_bf16_f32 v184, v184, v185\n" \
"v_mfma_f32_16x16x32_bf16 a[56:59], v[60:63], v[164:167], a[56:59]\n" \
"v_cvt_pk_bf16_f32 v185, v186, v187\n" \
"v_cvt_pk_bf16_f32 v186, v180, v181\n" \
"v_mfma_f32_16x16x32_bf16 a[56:59], v[64:67], v[168:171], a[56:59]\n" \
"v_cvt_pk_bf16_f32 v187, v182, v183\n" \
"v_accvgpr_read_b32 v180, a112\n" \
"v_mfma_f32_16x16x32_bf16 a[88:91], v[72:75], v[168:171], a[88:91]\n" \
"v_accvgpr_read_b32 v181, a113\n" \
"v_accvgpr_read_b32 v182, a114\n" \
"v_mfma_f32_16x16x32_bf16 a[88:91], v[68:71], v[164:167], a[88:91]\n" \
"v_accvgpr_read_b32 v183, a115\n" \
"v_accvgpr_write_b32 a112, 0\n" \
"v_mfma_f32_16x16x32_bf16 a[120:123], v[76:79], v[164:167], a[120:123]\n" \
"v_accvgpr_write_b32 a113, 0\n" \
"v_accvgpr_write_b32 a114, 0\n" \
"v_mfma_f32_16x16x32_bf16 a[120:123], v[80:83], v[168:171], a[120:123]\n" \
"v_accvgpr_write_b32 a115, 0\n" \
"v_accvgpr_read_b32 v192, a20\n" \
"v_mfma_f32_16x16x32_bf16 a[28:31], v[56:59], v[176:179], a[28:31]\n" \
"v_accvgpr_read_b32 v193, a21\n" \
"v_accvgpr_read_b32 v194, a22\n" \
"v_mfma_f32_16x16x32_bf16 a[28:31], v[52:55], v[172:175], a[28:31]\n" \
"v_accvgpr_read_b32 v195, a23\n" \
"v_cvt_pk_bf16_f32 v188, v188, v189\n" \
"v_mfma_f32_16x16x32_bf16 a[60:63], v[60:63], v[172:175], a[60:63]\n" \
"v_cvt_pk_bf16_f32 v189, v190, v191\n" \
"v_cvt_pk_bf16_f32 v190, v180, v181\n" \
"v_mfma_f32_16x16x32_bf16 a[60:63], v[64:67], v[176:179], a[60:63]\n" \
"v_cvt_pk_bf16_f32 v191, v182, v183\n" \
"v_accvgpr_read_b32 v180, a52\n" \
"v_mfma_f32_16x16x32_bf16 a[92:95], v[72:75], v[176:179], a[92:95]\n" \
"v_accvgpr_read_b32 v181, a53\n" \
"v_accvgpr_read_b32 v182, a54\n" \
"v_mfma_f32_16x16x32_bf16 a[92:95], v[68:71], v[172:175], a[92:95]\n" \
"v_accvgpr_read_b32 v183, a55\n" \
"v_accvgpr_read_b32 v196, a84\n" \
"v_mfma_f32_16x16x32_bf16 a[124:127], v[76:79], v[172:175], a[124:127]\n" \
"v_accvgpr_read_b32 v197, a85\n" \
"v_accvgpr_read_b32 v198, a86\n" \
"v_mfma_f32_16x16x32_bf16 a[124:127], v[80:83], v[176:179], a[124:127]\n" \
"v_accvgpr_read_b32 v199, a87\n" \
"v_cvt_pk_bf16_f32 v192, v192, v193\n" \
"v_mfma_f32_16x16x32_bf16 a[0:3], v[120:123], v[56:59], a[0:3]\n" \
"v_cvt_pk_bf16_f32 v193, v194, v195\n" \
"v_cvt_pk_bf16_f32 v194, v180, v181\n" \
"v_mfma_f32_16x16x32_bf16 a[0:3], v[116:119], v[52:55], a[0:3]\n" \
"v_cvt_pk_bf16_f32 v195, v182, v183\n" \
"v_accvgpr_read_b32 v180, a116\n" \
"v_mfma_f32_16x16x32_bf16 a[32:35], v[116:119], v[60:63], a[32:35]\n" \
"v_accvgpr_read_b32 v181, a117\n" \
"v_accvgpr_read_b32 v182, a118\n" \
"v_mfma_f32_16x16x32_bf16 a[32:35], v[120:123], v[64:67], a[32:35]\n" \
"v_accvgpr_read_b32 v183, a119\n" \
"v_accvgpr_write_b32 a116, 0\n" \
"v_mfma_f32_16x16x32_bf16 a[64:67], v[120:123], v[72:75], a[64:67]\n" \
"v_accvgpr_write_b32 a117, 0\n" \
"v_accvgpr_write_b32 a118, 0\n" \
"v_mfma_f32_16x16x32_bf16 a[64:67], v[116:119], v[68:71], a[64:67]\n" \
"v_accvgpr_write_b32 a119, 0\n" \
"v_accvgpr_read_b32 v200, a24\n" \
"v_mfma_f32_16x16x32_bf16 a[96:99], v[116:119], v[76:79], a[96:99]\n" \
"v_accvgpr_read_b32 v201, a25\n" \
"v_accvgpr_read_b32 v202, a26\n" \
"v_mfma_f32_16x16x32_bf16 a[96:99], v[120:123], v[80:83], a[96:99]\n" \
"v_accvgpr_read_b32 v203, a27\n" \
"v_cvt_pk_bf16_f32 v196, v196, v197\n" \
"v_mfma_f32_16x16x32_bf16 a[4:7], v[128:131], v[56:59], a[4:7]\n" \
"v_cvt_pk_bf16_f32 v197, v198, v199\n" \
"v_cvt_pk_bf16_f32 v198, v180, v181\n" \
"v_mfma_f32_16x16x32_bf16 a[4:7], v[124:127], v[52:55], a[4:7]\n" \
"v_cvt_pk_bf16_f32 v199, v182, v183\n" \
"v_accvgpr_read_b32 v180, a56\n" \
"v_mfma_f32_16x16x32_bf16 a[36:39], v[124:127], v[60:63], a[36:39]\n" \
"v_accvgpr_read_b32 v181, a57\n" \
"v_accvgpr_read_b32 v182, a58\n" \
"v_mfma_f32_16x16x32_bf16 a[36:39], v[128:131], v[64:67], a[36:39]\n" \
"v_accvgpr_read_b32 v183, a59\n" \
"v_accvgpr_read_b32 v204, a88\n" \
"v_mfma_f32_16x16x32_bf16 a[68:71], v[128:131], v[72:75], a[68:71]\n" \
"v_accvgpr_read_b32 v205, a89\n" \
"v_accvgpr_read_b32 v206, a90\n" \
"v_mfma_f32_16x16x32_bf16 a[68:71], v[124:127], v[68:71], a[68:71]\n" \
"v_accvgpr_read_b32 v207, a91\n" \
"v_cvt_pk_bf16_f32 v200, v200, v201\n" \
"v_mfma_f32_16x16x32_bf16 a[100:103], v[124:127], v[76:79], a[100:103]\n" \
"v_cvt_pk_bf16_f32 v201, v202, v203\n" \
"v_cvt_pk_bf16_f32 v202, v180, v181\n" \
"v_mfma_f32_16x16x32_bf16 a[100:103], v[128:131], v[80:83], a[100:103]\n" \
"v_cvt_pk_bf16_f32 v203, v182, v183\n" \
"s_waitcnt lgkmcnt(0)\n" \
"s_nop 0\n" \
"v_mfma_f32_16x16x32_bf16 a[8:11], v[136:139], v[56:59], a[8:11]\n" \
"v_accvgpr_read_b32 v180, a120\n" \
"v_accvgpr_read_b32 v181, a121\n" \
"v_mfma_f32_16x16x32_bf16 a[8:11], v[132:135], v[52:55], a[8:11]\n" \
"v_accvgpr_read_b32 v182, a122\n" \
"v_accvgpr_read_b32 v183, a123\n" \
"v_mfma_f32_16x16x32_bf16 a[40:43], v[132:135], v[60:63], a[40:43]\n" \
"v_accvgpr_write_b32 a120, 0\n" \
"v_accvgpr_write_b32 a121, 0\n" \
"v_mfma_f32_16x16x32_bf16 a[40:43], v[136:139], v[64:67], a[40:43]\n" \
"v_accvgpr_write_b32 a122, 0\n" \
"v_accvgpr_write_b32 a123, 0\n" \
"v_mfma_f32_16x16x32_bf16 a[72:75], v[136:139], v[72:75], a[72:75]\n" \
"v_accvgpr_read_b32 v208, a28\n" \
"v_accvgpr_read_b32 v209, a29\n" \
"v_mfma_f32_16x16x32_bf16 a[72:75], v[132:135], v[68:71], a[72:75]\n" \
"v_accvgpr_read_b32 v210, a30\n" \
"v_accvgpr_read_b32 v211, a31\n" \
"v_mfma_f32_16x16x32_bf16 a[104:107], v[132:135], v[76:79], a[104:107]\n" \
"v_cvt_pk_bf16_f32 v204, v204, v205\n" \
"v_cvt_pk_bf16_f32 v205, v206, v207\n" \
"v_mfma_f32_16x16x32_bf16 a[104:107], v[136:139], v[80:83], a[104:107]\n" \
"v_cvt_pk_bf16_f32 v206, v180, v181\n" \
"v_cvt_pk_bf16_f32 v207, v182, v183\n" \
"v_mfma_f32_16x16x32_bf16 a[12:15], v[144:147], v[56:59], a[12:15]\n" \
"v_accvgpr_read_b32 v180, a60\n" \
"v_accvgpr_read_b32 v181, a61\n" \
"v_mfma_f32_16x16x32_bf16 a[12:15], v[140:143], v[52:55], a[12:15]\n" \
"v_accvgpr_read_b32 v182, a62\n" \
"v_accvgpr_read_b32 v183, a63\n" \
"v_mfma_f32_16x16x32_bf16 a[44:47], v[140:143], v[60:63], a[44:47]\n" \
"v_accvgpr_read_b32 v212, a92\n" \
"v_accvgpr_read_b32 v213, a93\n" \
"v_mfma_f32_16x16x32_bf16 a[44:47], v[144:147], v[64:67], a[44:47]\n" \
"v_accvgpr_read_b32 v214, a94\n" \
"v_accvgpr_read_b32 v215, a95\n" \
"v_mfma_f32_16x16x32_bf16 a[76:79], v[144:147], v[72:75], a[76:79]\n" \
"v_cvt_pk_bf16_f32 v208, v208, v209\n" \
"v_cvt_pk_bf16_f32 v209, v210, v211\n" \
"v_mfma_f32_16x16x32_bf16 a[76:79], v[140:143], v[68:71], a[76:79]\n" \
"v_cvt_pk_bf16_f32 v210, v180, v181\n" \
"v_cvt_pk_bf16_f32 v211, v182, v183\n" \
"v_mfma_f32_16x16x32_bf16 a[108:111], v[140:143], v[76:79], a[108:111]\n" \
"v_accvgpr_read_b32 v180, a124\n" \
"v_accvgpr_read_b32 v181, a125\n" \
"v_mfma_f32_16x16x32_bf16 a[108:111], v[144:147], v[80:83], a[108:111]\n" \
"v_accvgpr_read_b32 v182, a126\n" \
"v_accvgpr_read_b32 v183, a127\n" \
"s_waitcnt vmcnt(0)\n" \
"s_barrier\n" \
"v_mfma_f32_16x16x32_bf16 a[128:131], v[120:123], v[88:91], a[128:131]\n" \
"s_add_u32 m0, s40, 0x8000\n" \
"s_add_u32 s85, s72, 0x0\n" \
"s_nop 0\n" \
"v_mfma_f32_16x16x32_bf16 a[128:131], v[116:119], v[84:87], a[128:131]\n" \
"buffer_load_dwordx4 v20, s[80:83], s85 offen sc1 lds\n" \
"v_mfma_f32_16x16x32_bf16 a[160:163], v[116:119], v[92:95], a[160:163]\n" \
"L_pf4:\n" \
"s_cbranch_execz L_pf8\n" \
"s_nop 0\n" \
"s_add_u32 m0, s40, 0x9000\n" \
"s_add_u32 s84, s72, 0x1000\n" \
"v_mfma_f32_16x16x32_bf16 a[160:163], v[120:123], v[96:99], a[160:163]\n" \
"ds_read_b128 v[52:55], v28 offset:0\n" \
"ds_read_b128 v[56:59], v29 offset:0\n" \
"v_mfma_f32_16x16x32_bf16 a[192:195], v[120:123], v[104:107], a[192:195]\n" \
"buffer_load_dwordx4 v20, s[80:83], s84 offen sc1 lds\n" \
"v_mfma_f32_16x16x32_bf16 a[192:195], v[116:119], v[100:103], a[192:195]\n" \
"ds_read_b128 v[60:63], v28 offset:2048\n" \
"ds_read_b128 v[64:67], v29 offset:2048\n" \
"v_mfma_f32_16x16x32_bf16 a[224:227], v[116:119], v[108:111], a[224:227]\n" \
"s_add_u32 m0, s40, 0xb000\n" \
"s_add_u32 s85, s62, 0x0\n" \
"v_mfma_f32_16x16x32_bf16 a[224:227], v[120:123], v[112:115], a[224:227]\n" \
"buffer_load_dwordx4 v21, s[76:79], s85 offen lds\n" \
"v_mfma_f32_16x16x32_bf16 a[132:135], v[128:131], v[88:91], a[132:135]\n" \
"ds_read_b64_tr_b16 v[116:117], v36 offset:0\n" \
"ds_read_b64_tr_b16 v[118:119], v36 offset:512\n" \
"v_mfma_f32_16x16x32_bf16 a[132:135], v[124:127], v[84:87], a[132:135]\n" \
"ds_read_b64_tr_b16 v[120:121], v36 offset:4096\n" \
"ds_read_b64_tr_b16 v[122:123], v36 offset:4608\n" \
"v_mfma_f32_16x16x32_bf16 a[164:167], v[124:127], v[92:95], a[164:167]\n" \
"s_add_u32 m0, %[mA], 0x8000\n" \
"s_add_u32 s84, s62, 0x0\n" \
"v_mfma_f32_16x16x32_bf16 a[164:167], v[128:131], v[96:99], a[164:167]\n" \
"buffer_load_dwordx4 v24, s[76:79], s84 offen lds\n" \
"v_mfma_f32_16x16x32_bf16 a[196:199], v[128:131], v[104:107], a[196:199]\n" \
"ds_read_b128 v[68:71], v28 offset:4096\n" \
"ds_read_b128 v[72:75], v29 offset:4096\n" \
"v_mfma_f32_16x16x32_bf16 a[196:199], v[124:127], v[100:103], a[196:199]\n" \
"s_add_u32 m0, %[mA], 0x8400\n" \
"s_add_u32 s85, s62, 0x2400\n" \
"v_mfma_f32_16x16x32_bf16 a[228:231], v[124:127], v[108:111], a[228:231]\n" \
"buffer_load_dwordx4 v25, s[76:79], s85 offen lds\n" \
"v_mfma_f32_16x16x32_bf16 a[228:231], v[128:131], v[112:115], a[228:231]\n" \
"ds_read_b64_tr_b16 v[128:129], v37 offset:4096\n" \
"ds_read_b64_tr_b16 v[130:131], v37 offset:4608\n" \
"v_mfma_f32_16x16x32_bf16 a[136:139], v[136:139], v[88:91], a[136:139]\n" \
"ds_read_b64_tr_b16 v[124:125], v37 offset:0\n" \
"ds_read_b64_tr_b16 v[126:127], v37 offset:512\n" \
"v_mfma_f32_16x16x32_bf16 a[136:139], v[132:135], v[84:87], a[136:139]\n" \
"s_add_u32 m0, %[mB], 0x8000\n" \
"s_add_u32 s84, s63, 0x0\n" \
"s_nop 0\n" \
"v_mfma_f32_16x16x32_bf16 a[168:171], v[132:135], v[92:95], a[168:171]\n" \
"buffer_load_dwordx4 v26, s[68:71], s84 offen lds\n" \
"v_mfma_f32_16x16x32_bf16 a[168:171], v[136:139], v[96:99], a[168:171]\n" \
"ds_read_b128 v[76:79], v28 offset:6144\n" \
"ds_read_b128 v[80:83], v29 offset:6144\n" \
"v_mfma_f32_16x16x32_bf16 a[200:203], v[136:139], v[104:107], a[200:203]\n" \
"v_accvgpr_write_b32 a124, 0\n" \
"v_accvgpr_write_b32 a125, 0\n" \
"v_mfma_f32_16x16x32_bf16 a[200:203], v[132:135], v[100:103], a[200:203]\n" \
"s_add_u32 m0, %[mA], 0x8800\n" \
"s_add_u32 s85, s62, 0x4800\n" \
"v_mfma_f32_16x16x32_bf16 a[232:235], v[132:135], v[108:111], a[232:235]\n" \
"buffer_load_dwordx4 v24, s[76:79], s85 offen lds\n" \
"v_mfma_f32_16x16x32_bf16 a[232:235], v[136:139], v[112:115], a[232:235]\n" \
"ds_read_b64_tr_b16 v[132:133], v38 offset:0\n" \
"ds_read_b64_tr_b16 v[134:135], v38 offset:512\n" \
"v_mfma_f32_16x16x32_bf16 a[140:143], v[144:147], v[88:91], a[140:143]\n" \
"s_add_u32 m0, %[mA], 0x8c00\n" \
"s_add_u32 s84, s62, 0x6c00\n" \
"v_mfma_f32_16x16x32_bf16 a[140:143], v[140:143], v[84:87], a[140:143]\n" \
"buffer_load_dwordx4 v25, s[76:79], s84 offen lds\n" \
"v_mfma_f32_16x16x32_bf16 a[172:175], v[140:143], v[92:95], a[172:175]\n" \
"ds_read_b64_tr_b16 v[136:137], v38 offset:4096\n" \
"ds_read_b64_tr_b16 v[138:139], v38 offset:4608\n" \
"v_mfma_f32_16x16x32_bf16 a[172:175], v[144:147], v[96:99], a[172:175]\n" \
"v_accvgpr_write_b32 a126, 0\n" \
"v_accvgpr_write_b32 a127, 0\n" \
"v_mfma_f32_16x16x32_bf16 a[204:207], v[144:147], v[104:107], a[204:207]\n" \
"s_add_u32 m0, %[mB], 0x8400\n" \
"s_add_u32 s85, s63, 0x8000\n" \
"v_mfma_f32_16x16x32_bf16 a[204:207], v[140:143], v[100:103], a[204:207]\n" \
"buffer_load_dwordx4 v26, s[68:71], s85 offen lds\n" \
"v_mfma_f32_16x16x32_bf16 a[236:239], v[140:143], v[108:111], a[236:239]\n" \
"ds_read_b64_tr_b16 v[140:141], v39 offset:0\n" \
"ds_read_b64_tr_b16 v[142:143], v39 offset:960\n" \
"v_mfma_f32_16x16x32_bf16 a[236:239], v[144:147], v[112:115], a[236:239]\n" \
"v_accvgpr_read_b32 v216, a0\n" \
"v_accvgpr_read_b32 v217, a1\n" \
"v_mfma_f32_16x16x32_bf16 a[144:147], v[88:91], v[152:155], a[144:147]\n" \
"s_add_u32 m0, %[mA], 0x9000\n" \
"s_add_u32 s84, s62, 0x9000\n" \
"v_mfma_f32_16x16x32_bf16 a[144:147], v[84:87], v[148:151], a[144:147]\n" \
"buffer_load_dwordx4 v24, s[76:79], s84 offen lds\n" \
"v_mfma_f32_16x16x32_bf16 a[148:151], v[84:87], v[156:159], a[148:151]\n" \
"v_accvgpr_read_b32 v218, a2\n" \
"v_accvgpr_read_b32 v219, a3\n" \
"v_mfma_f32_16x16x32_bf16 a[148:151], v[88:91], v[160:163], a[148:151]\n" \
"s_add_u32 m0, %[mA], 0x9400\n" \
"s_add_u32 s85, s62, 0xb400\n" \
"v_mfma_f32_16x16x32_bf16 a[152:155], v[88:91], v[168:171], a[152:155]\n" \
"buffer_load_dwordx4 v25, s[76:79], s85 offen lds\n" \
"v_mfma_f32_16x16x32_bf16 a[152:155], v[84:87], v[164:167], a[152:155]\n" \
"v_cvt_pk_bf16_f32 v212, v212, v213\n" \
"v_cvt_pk_bf16_f32 v213, v214, v215\n" \
"v_mfma_f32_16x16x32_bf16 a[156:159], v[84:87], v[172:175], a[156:159]\n" \
"ds_read_b128 v[84:87], v22 offset:0\n" \
"v_cvt_pk_bf16_f32 v214, v180, v181\n" \
"v_mfma_f32_16x16x32_bf16 a[156:159], v[88:91], v[176:179], a[156:159]\n" \
"s_add_u32 m0, %[mB], 0x8800\n" \
"s_add_u32 s84, s63, 0x10000\n" \
"v_mfma_f32_16x16x32_bf16 a[176:179], v[96:99], v[152:155], a[176:179]\n" \
"buffer_load_dwordx4 v27, s[68:71], s84 offen lds\n" \
"v_mfma_f32_16x16x32_bf16 a[176:179], v[92:95], v[148:151], a[176:179]\n" \
"ds_read_b128 v[88:91], v23 offset:0\n" \
"v_cvt_pk_bf16_f32 v215, v182, v183\n" \
"v_mfma_f32_16x16x32_bf16 a[180:183], v[92:95], v[156:159], a[180:183]\n" \
"v_accvgpr_read_b32 v180, a4\n" \
"v_accvgpr_read_b32 v181, a5\n" \
"v_mfma_f32_16x16x32_bf16 a[180:183], v[96:99], v[160:163], a[180:183]\n" \
"s_add_u32 m0, %[mA], 0x9800\n" \
"s_add_u32 s85, s62, 0xd800\n" \
"v_mfma_f32_16x16x32_bf16 a[184:187], v[96:99], v[168:171], a[184:187]\n" \
"buffer_load_dwordx4 v24, s[76:79], s85 offen lds\n" \
"v_mfma_f32_16x16x32_bf16 a[184:187], v[92:95], v[164:167], a[184:187]\n" \
"v_accvgpr_read_b32 v182, a6\n" \
"v_accvgpr_read_b32 v183, a7\n" \
"v_mfma_f32_16x16x32_bf16 a[188:191], v[92:95], v[172:175], a[188:191]\n" \
"v_cvt_pk_bf16_f32 v216, v216, v217\n" \
"v_cvt_pk_bf16_f32 v217, v218, v219\n" \
"v_mfma_f32_16x16x32_bf16 a[188:191], v[96:99], v[176:179], a[188:191]\n" \
"v_accvgpr_read_b32 v220, a32\n" \
"v_accvgpr_read_b32 v221, a33\n" \
"v_mfma_f32_16x16x32_bf16 a[208:211], v[104:107], v[152:155], a[208:211]\n" \
"v_cvt_pk_bf16_f32 v218, v180, v181\n" \
"v_cvt_pk_bf16_f32 v219, v182, v183\n" \
"v_mfma_f32_16x16x32_bf16 a[208:211], v[100:103], v[148:151], a[208:211]\n" \
"v_accvgpr_read_b32 v222, a34\n" \
"v_accvgpr_read_b32 v223, a35\n" \
"v_mfma_f32_16x16x32_bf16 a[212:215], v[100:103], v[156:159], a[212:215]\n" \
"v_cvt_pk_bf16_f32 v220, v220, v221\n" \
"v_accvgpr_read_b32 v180, a36\n" \
"v_mfma_f32_16x16x32_bf16 a[212:215], v[104:107], v[160:163], a[212:215]\n" \
"v_accvgpr_read_b32 v181, a37\n" \
"v_accvgpr_read_b32 v182, a38\n" \
"v_mfma_f32_16x16x32_bf16 a[216:219], v[104:107], v[168:171], a[216:219]\n" \
"v_accvgpr_read_b32 v183, a39\n" \
"v_cvt_pk_bf16_f32 v221, v222, v223\n" \
"v_mfma_f32_16x16x32_bf16 a[216:219], v[100:103], v[164:167], a[216:219]\n" \
"v_accvgpr_read_b32 v224, a64\n" \
"v_accvgpr_read_b32 v225, a65\n" \
"v_mfma_f32_16x16x32_bf16 a[220:223], v[100:103], v[172:175], a[220:223]\n" \
"v_cvt_pk_bf16_f32 v222, v180, v181\n" \
"v_cvt_pk_bf16_f32 v223, v182, v183\n" \
"v_mfma_f32_16x16x32_bf16 a[220:223], v[104:107], v[176:179], a[220:223]\n" \
"v_accvgpr_read_b32 v226, a66\n" \
"v_accvgpr_read_b32 v227, a67\n" \
"v_mfma_f32_16x16x32_bf16 a[240:243], v[112:115], v[152:155], a[240:243]\n" \
"ds_read_b64_tr_b16 v[152:153], v32 offset:8192\n" \
"ds_read_b64_tr_b16 v[154:155], v32 offset:9216\n" \
"v_mfma_f32_16x16x32_bf16 a[240:243], v[108:111], v[148:151], a[240:243]\n" \
"ds_read_b64_tr_b16 v[148:149], v32 offset:0\n" \
"ds_read_b64_tr_b16 v[150:151], v32 offset:1024\n" \
"v_mfma_f32_16x16x32_bf16 a[244:247], v[108:111], v[156:159], a[244:247]\n" \
"ds_read_b64_tr_b16 v[156:157], v33 offset:0\n" \
"ds_read_b64_tr_b16 v[158:159], v33 offset:1024\n" \
"v_mfma_f32_16x16x32_bf16 a[244:247], v[112:115], v[160:163], a[244:247]\n" \
"ds_read_b64_tr_b16 v[160:161], v33 offset:8192\n" \
"ds_read_b64_tr_b16 v[162:163], v33 offset:9216\n" \
"v_mfma_f32_16x16x32_bf16 a[248:251], v[112:115], v[168:171], a[248:251]\n" \
"ds_read_b64_tr_b16 v[168:169], v34 offset:8192\n" \
"ds_read_b64_tr_b16 v[170:171], v34 offset:9216\n" \
"v_mfma_f32_16x16x32_bf16 a[248:251], v[108:111], v[164:167], a[248:251]\n" \
"ds_read_b64_tr_b16 v[164:165], v34 offset:0\n" \
"ds_read_b64_tr_b16 v[166:167], v34 offset:1024\n" \
"v_mfma_f32_16x16x32_bf16 a[252:255], v[108:111], v[172:175], a[252:255]\n" \
"ds_read_b64_tr_b16 v[172:173], v35 offset:0\n" \
"ds_read_b64_tr_b16 v[174:175], v35 offset:1024\n" \
"v_mfma_f32_16x16x32_bf16 a[252:255], v[112:115], v[176:179], a[252:255]\n" \
"s_add_u32 m0, %[mA], 0x9c00\n" \
"s_waitcnt lgkmcnt(14)\n" \
"s_nop 0\n" \
"v_smfmac_f32_16x16x64_bf16 a[16:19], v[140:143], v[84:91], v50\n" \
"v_mfma_f32_16x16x32_bf16 a[48:51], v[128:131], v[88:91], 0\n" \
"s_add_u32 s85, s62, 0xfc00\n" \
"ds_read_b64_tr_b16 v[176:177], v35 offset:8192\n" \
"v_mfma_f32_16x16x32_bf16 a[48:51], v[124:127], v[84:87], a[48:51]\n" \
"buffer_load_dwordx4 v25, s[76:79], s85 offen lds\n" \
"v_mfma_f32_16x16x32_bf16 a[80:83], v[84:87], v[132:135], 0\n" \
"s_add_u32 m0, %[mB], 0x8c00\n" \
"s_add_u32 s84, s63, 0x18000\n" \
"v_mfma_f32_16x16x32_bf16 a[80:83], v[88:91], v[136:139], a[80:83]\n" \
"ds_read_b64_tr_b16 v[178:179], v35 offset:9216\n" \
"v_cvt_pk_bf16_f32 v224, v224, v225\n" \
"v_smfmac_f32_16x16x64_bf16 a[112:115], v[140:143], v[52:59], v50\n" \
"v_mfma_f32_16x16x32_bf16 a[20:23], v[128:131], v[56:59], 0\n" \
"buffer_load_dwordx4 v27, s[68:71], s84 offen lds\n" \
"v_mfma_f32_16x16x32_bf16 a[20:23], v[124:127], v[52:55], a[20:23]\n" \
"s_add_u32 s88, s44, 0x0\n" \
"s_nop 0\n" \
"v_accvgpr_read_b32 v180, a68\n" \
"v_mfma_f32_16x16x32_bf16 a[52:55], v[116:119], v[52:55], 0\n" \
"v_accvgpr_read_b32 v181, a69\n" \
"v_accvgpr_read_b32 v182, a70\n" \
"v_mfma_f32_16x16x32_bf16 a[52:55], v[120:123], v[56:59], a[52:55]\n" \
"buffer_store_dwordx4 v[184:187], v20, s[80:83], s88 offen offset:0 sc1\n" \
"v_mfma_f32_16x16x32_bf16 a[84:87], v[56:59], v[136:139], 0\n" \
"v_accvgpr_read_b32 v183, a71\n" \
"v_cvt_pk_bf16_f32 v225, v226, v227\n" \
"v_mfma_f32_16x16x32_bf16 a[84:87], v[52:55], v[132:135], a[84:87]\n" \
"v_accvgpr_read_b32 v228, a96\n" \
"v_accvgpr_read_b32 v229, a97\n" \
"v_smfmac_f32_16x16x64_bf16 a[116:119], v[140:143], v[60:67], v50\n" \
"v_mfma_f32_16x16x32_bf16 a[24:27], v[124:127], v[60:63], 0\n" \
"v_cvt_pk_bf16_f32 v226, v180, v181\n" \
"v_cvt_pk_bf16_f32 v227, v182, v183\n" \
"v_mfma_f32_16x16x32_bf16 a[24:27], v[128:131], v[64:67], a[24:27]\n" \
"s_add_u32 s89, s44, 0x1000\n" \
"v_accvgpr_read_b32 v230, a98\n" \
"v_mfma_f32_16x16x32_bf16 a[56:59], v[120:123], v[64:67], 0\n" \
"buffer_store_dwordx4 v[188:191], v20, s[80:83], s89 offen offset:0 sc1\n" \
"v_mfma_f32_16x16x32_bf16 a[56:59], v[116:119], v[60:63], a[56:59]\n" \
"v_accvgpr_read_b32 v231, a99\n" \
"v_cvt_pk_bf16_f32 v228, v228, v229\n" \
"v_mfma_f32_16x16x32_bf16 a[88:91], v[60:63], v[132:135], 0\n" \
"v_accvgpr_read_b32 v180, a100\n" \
"v_accvgpr_read_b32 v181, a101\n" \
"v_mfma_f32_16x16x32_bf16 a[88:91], v[64:67], v[136:139], a[88:91]\n" \
"v_accvgpr_read_b32 v182, a102\n" \
"v_accvgpr_read_b32 v183, a103\n" \
"v_smfmac_f32_16x16x64_bf16 a[120:123], v[140:143], v[68:75], v50\n" \
"v_mfma_f32_16x16x32_bf16 a[28:31], v[128:131], v[72:75], 0\n" \
"v_cvt_pk_bf16_f32 v229, v230, v231\n" \
"v_accvgpr_read_b32 v232, a8\n" \
"v_mfma_f32_16x16x32_bf16 a[28:31], v[124:127], v[68:71], a[28:31]\n" \
"v_accvgpr_read_b32 v233, a9\n" \
"v_cvt_pk_bf16_f32 v230, v180, v181\n" \
"v_mfma_f32_16x16x32_bf16 a[60:63], v[116:119], v[68:71], 0\n" \
"s_add_u32 s88, s44, 0x0\n" \
"s_nop 0\n" \
"v_cvt_pk_bf16_f32 v231, v182, v183\n" \
"v_mfma_f32_16x16x32_bf16 a[60:63], v[120:123], v[72:75], a[60:63]\n" \
"buffer_store_dwordx4 v[192:195], v20, s[80:83], s88 offen offset:1024 sc1\n" \
"v_mfma_f32_16x16x32_bf16 a[92:95], v[72:75], v[136:139], 0\n" \
"v_accvgpr_read_b32 v234, a10\n" \
"v_accvgpr_read_b32 v235, a11\n" \
"v_mfma_f32_16x16x32_bf16 a[92:95], v[68:71], v[132:135], a[92:95]\n" \
"v_cvt_pk_bf16_f32 v232, v232, v233\n" \
"v_accvgpr_read_b32 v180, a12\n" \
"v_smfmac_f32_16x16x64_bf16 a[124:127], v[140:143], v[76:83], v50\n" \
"v_mfma_f32_16x16x32_bf16 a[0:3], v[124:127], v[76:79], 0\n" \
"v_accvgpr_read_b32 v181, a13\n" \
"v_accvgpr_read_b32 v182, a14\n" \
"v_mfma_f32_16x16x32_bf16 a[0:3], v[128:131], v[80:83], a[0:3]\n" \
"v_accvgpr_read_b32 v183, a15\n" \
"v_cvt_pk_bf16_f32 v233, v234, v235\n" \
"v_mfma_f32_16x16x32_bf16 a[4:7], v[120:123], v[80:83], 0\n" \
"v_accvgpr_read_b32 v236, a40\n" \
"v_accvgpr_read_b32 v237, a41\n" \
"v_mfma_f32_16x16x32_bf16 a[4:7], v[116:119], v[76:79], a[4:7]\n" \
"v_cvt_pk_bf16_f32 v234, v180, v181\n" \
"v_cvt_pk_bf16_f32 v235, v182, v183\n" \
"v_mfma_f32_16x16x32_bf16 a[32:35], v[76:79], v[132:135], 0\n" \
"v_accvgpr_read_b32 v238, a42\n" \
"v_accvgpr_read_b32 v239, a43\n" \
"v_mfma_f32_16x16x32_bf16 a[32:35], v[80:83], v[136:139], a[32:35]\n" \
"v_cvt_pk_bf16_f32 v236, v236, v237\n" \
"v_accvgpr_read_b32 v180, a44\n" \
"v_accvgpr_read_b32 v181, a45\n" \
"v_accvgpr_read_b32 v182, a46\n" \
"v_accvgpr_read_b32 v183, a47\n" \
"v_cvt_pk_bf16_f32 v237, v238, v239\n" \
"v_accvgpr_read_b32 v240, a72\n" \
"v_accvgpr_read_b32 v241, a73\n" \
"v_cvt_pk_bf16_f32 v238, v180, v181\n" \
"v_cvt_pk_bf16_f32 v239, v182, v183\n" \
"v_accvgpr_read_b32 v242, a74\n" \
"v_accvgpr_read_b32 v243, a75\n" \
"v_cvt_pk_bf16_f32 v240, v240, v241\n" \
"v_accvgpr_read_b32 v180, a76\n" \
"v_accvgpr_read_b32 v181, a77\n" \
"v_accvgpr_read_b32 v182, a78\n" \
"v_accvgpr_read_b32 v183, a79\n" \
"v_cvt_pk_bf16_f32 v241, v242, v243\n" \
"v_accvgpr_read_b32 v244, a104\n" \
"v_accvgpr_read_b32 v245, a105\n" \
"v_cvt_pk_bf16_f32 v242, v180, v181\n" \
"v_cvt_pk_bf16_f32 v243, v182, v183\n" \
"v_accvgpr_read_b32 v246, a106\n" \
"v_accvgpr_read_b32 v247, a107\n" \
"v_cvt_pk_bf16_f32 v244, v244, v245\n" \
"v_accvgpr_read_b32 v248, a128\n" \
"v_accvgpr_read_b32 v249, a129\n" \
"v_accvgpr_read_b32 v250, a130\n" \
"v_accvgpr_read_b32 v251, a131\n" \
"s_waitcnt lgkmcnt(0)\n" \
"s_nop 0\n" \
"v_mfma_f32_16x16x32_bf16 a[128:131], v[152:155], v[56:59], 0\n" \
"v_accvgpr_read_b32 v180, a108\n" \
"v_accvgpr_read_b32 v181, a109\n" \
"v_accvgpr_read_b32 v182, a110\n" \
"v_accvgpr_read_b32 v183, a111\n" \
"v_cvt_pk_bf16_f32 v245, v246, v247\n" \
"v_cvt_pk_bf16_f32 v246, v180, v181\n" \
"v_cvt_pk_bf16_f32 v247, v182, v183\n" \
"v_cvt_pk_bf16_f32 v248, v248, v249\n" \
"v_mfma_f32_16x16x32_bf16 a[128:131], v[148:151], v[52:55], a[128:131]\n" \
"v_accvgpr_read_b32 v180, a132\n" \
"v_accvgpr_read_b32 v181, a133\n" \
"v_accvgpr_read_b32 v182, a134\n" \
"v_accvgpr_read_b32 v183, a135\n" \
"v_cvt_pk_bf16_f32 v249, v250, v251\n" \
"v_accvgpr_read_b32 v252, a160\n" \
"v_accvgpr_read_b32 v253, a161\n" \
"v_cvt_pk_bf16_f32 v250, v180, v181\n" \
"v_cvt_pk_bf16_f32 v251, v182, v183\n" \
"v_accvgpr_read_b32 v254, a162\n" \
"v_accvgpr_read_b32 v255, a163\n" \
"v_cvt_pk_bf16_f32 v252, v252, v253\n" \
"v_accvgpr_read_b32 v180, a164\n" \
"v_accvgpr_read_b32 v181, a165\n" \
"v_accvgpr_read_b32 v182, a166\n" \
"v_accvgpr_read_b32 v183, a167\n" \
"v_cvt_pk_bf16_f32 v253, v254, v255\n" \
"v_cvt_pk_bf16_f32 v254, v180, v181\n" \
"v_cvt_pk_bf16_f32 v255, v182, v183\n" \
"v_accvgpr_write_b32 a36, v252\n" \
"v_accvgpr_write_b32 a37, v253\n" \
"v_mfma_f32_16x16x32_bf16 a[132:135], v[156:159], v[52:55], 0\n" \
"v_accvgpr_write_b32 a38, v254\n" \
"v_accvgpr_write_b32 a39, v255\n" \
"v_accvgpr_read_b32 v252, a192\n" \
"v_accvgpr_read_b32 v253, a193\n" \
"v_accvgpr_read_b32 v254, a194\n" \
"v_accvgpr_read_b32 v255, a195\n" \
"v_cvt_pk_bf16_f32 v252, v252, v253\n" \
"v_accvgpr_read_b32 v180, a196\n" \
"v_accvgpr_read_b32 v181, a197\n" \
"v_accvgpr_read_b32 v182, a198\n" \
"v_accvgpr_read_b32 v183, a199\n" \
"v_cvt_pk_bf16_f32 v253, v254, v255\n" \
"v_cvt_pk_bf16_f32 v254, v180, v181\n" \
"v_cvt_pk_bf16_f32 v255, v182, v183\n" \
"v_accvgpr_write_b32 a64, v252\n" \
"v_accvgpr_write_b32 a65, v253\n" \
"v_mfma_f32_16x16x32_bf16 a[132:135], v[160:163], v[56:59], a[132:135]\n" \
"v_accvgpr_write_b32 a66, v254\n" \
"v_accvgpr_write_b32 a67, v255\n" \
"v_accvgpr_read_b32 v252, a224\n" \
"v_accvgpr_read_b32 v253, a225\n" \
"v_accvgpr_read_b32 v254, a226\n" \
"v_accvgpr_read_b32 v255, a227\n" \
"v_cvt_pk_bf16_f32 v252, v252, v253\n" \
"v_accvgpr_read_b32 v180, a228\n" \
"v_accvgpr_read_b32 v181, a229\n" \
"v_accvgpr_read_b32 v182, a230\n" \
"v_accvgpr_read_b32 v183, a231\n" \
"v_cvt_pk_bf16_f32 v253, v254, v255\n" \
"v_cvt_pk_bf16_f32 v254, v180, v181\n" \
"v_cvt_pk_bf16_f32 v255, v182, v183\n" \
"v_accvgpr_write_b32 a68, v252\n" \
"v_accvgpr_write_b32 a69, v253\n" \
"v_accvgpr_write_b32 a70, v254\n" \
"v_accvgpr_write_b32 a71, v255\n" \
"v_accvgpr_read_b32 v252, a136\n" \
"v_accvgpr_read_b32 v253, a137\n" \
"v_accvgpr_read_b32 v254, a138\n" \
"v_accvgpr_read_b32 v255, a139\n" \
"v_mfma_f32_16x16x32_bf16 a[136:139], v[168:171], v[56:59], 0\n" \
"ds_read_b128 v[84:87], v48 offset:0\n" \
"ds_read_b128 v[88:91], v49 offset:0\n" \
"ds_read_b128 v[92:95], v48 offset:2048\n" \
"v_mfma_f32_16x16x32_bf16 a[136:139], v[164:167], v[52:55], a[136:139]\n" \
"ds_read_b128 v[96:99], v49 offset:2048\n" \
"v_accvgpr_read_b32 v180, a140\n" \
"v_accvgpr_read_b32 v181, a141\n" \
"v_accvgpr_read_b32 v182, a142\n" \
"v_accvgpr_read_b32 v183, a143\n" \
"v_mfma_f32_16x16x32_bf16 a[140:143], v[172:175], v[52:55], 0\n" \
"ds_read_b128 v[100:103], v48 offset:4096\n" \
"ds_read_b128 v[104:107], v49 offset:4096\n" \
"s_add_u32 s89, s44, 0x1000\n" \
"buffer_store_dwordx4 v[196:199], v20, s[80:83], s89 offen offset:1024 sc1\n" \
"ds_read_b128 v[108:111], v48 offset:6144\n" \
"v_mfma_f32_16x16x32_bf16 a[140:143], v[176:179], v[56:59], a[140:143]\n" \
"v_cvt_pk_bf16_f32 v252, v252, v253\n" \
"v_cvt_pk_bf16_f32 v253, v254, v255\n" \
"v_mfma_f32_16x16x32_bf16 a[160:163], v[152:155], v[64:67], 0\n" \
"ds_read_b128 v[112:115], v49 offset:6144\n" \
"v_cvt_pk_bf16_f32 v254, v180, v181\n" \
"v_mfma_f32_16x16x32_bf16 a[160:163], v[148:151], v[60:63], a[160:163]\n" \
"v_cvt_pk_bf16_f32 v255, v182, v183\n" \
"v_accvgpr_write_b32 a96, v252\n" \
"v_mfma_f32_16x16x32_bf16 a[164:167], v[156:159], v[60:63], 0\n" \
"v_accvgpr_write_b32 a97, v253\n" \
"v_accvgpr_write_b32 a98, v254\n" \
"v_mfma_f32_16x16x32_bf16 a[164:167], v[160:163], v[64:67], a[164:167]\n" \
"v_accvgpr_write_b32 a99, v255\n" \
"v_accvgpr_read_b32 v252, a168\n" \
"v_accvgpr_read_b32 v253, a169\n" \
"v_accvgpr_read_b32 v254, a170\n" \
"v_accvgpr_read_b32 v255, a171\n" \
"v_mfma_f32_16x16x32_bf16 a[168:171], v[168:171], v[64:67], 0\n" \
"v_accvgpr_read_b32 v180, a172\n" \
"v_accvgpr_read_b32 v181, a173\n" \
"v_mfma_f32_16x16x32_bf16 a[168:171], v[164:167], v[60:63], a[168:171]\n" \
"v_accvgpr_read_b32 v182, a174\n" \
"v_accvgpr_read_b32 v183, a175\n" \
"v_mfma_f32_16x16x32_bf16 a[172:175], v[172:175], v[60:63], 0\n" \
"v_cvt_pk_bf16_f32 v252, v252, v253\n" \
"v_cvt_pk_bf16_f32 v253, v254, v255\n" \
"v_mfma_f32_16x16x32_bf16 a[172:175], v[176:179], v[64:67], a[172:175]\n" \
"v_cvt_pk_bf16_f32 v254, v180, v181\n" \
"v_cvt_pk_bf16_f32 v255, v182, v183\n" \
"v_mfma_f32_16x16x32_bf16 a[192:195], v[152:155], v[72:75], 0\n" \
"v_accvgpr_write_b32 a100, v252\n" \
"v_accvgpr_write_b32 a101, v253\n" \
"v_mfma_f32_16x16x32_bf16 a[192:195], v[148:151], v[68:71], a[192:195]\n" \
"v_accvgpr_write_b32 a102, v254\n" \
"v_accvgpr_write_b32 a103, v255\n" \
"s_add_u32 s62, s62, 0x80\n" \
"v_accvgpr_read_b32 v252, a200\n" \
"v_accvgpr_read_b32 v253, a201\n" \
"v_accvgpr_read_b32 v254, a202\n" \
"s_waitcnt vmcnt(4) lgkmcnt(0)\n" \
"s_barrier\n" \
"v_mfma_f32_16x16x32_bf16 a[196:199], v[156:159], v[68:71], 0\n" \
"v_accvgpr_read_b32 v255, a203\n" \
"s_add_u32 m0, s40, 0x0\n" \
"v_mfma_f32_16x16x32_bf16 a[196:199], v[160:163], v[72:75], a[196:199]\n" \
"s_cmp_lt_u32 s62, 0x480\n" \
"s_cselect_b32 s62, s62, 0\n" \
"v_mfma_f32_16x16x32_bf16 a[200:203], v[168:171], v[72:75], 0\n" \
"s_lshl_b32 s72, s62, 6\n" \
"s_add_u32 s72, s72, s37\n" \
"v_mfma_f32_16x16x32_bf16 a[200:203], v[164:167], v[68:71], a[200:203]\n" \
"s_add_u32 s85, s72, 0x0\n" \
"s_nop 0\n" \
"buffer_load_dwordx4 v20, s[80:83], s85 offen sc1 lds\n" \
"v_accvgpr_read_b32 v180, a204\n" \
"v_accvgpr_read_b32 v181, a205\n" \
"v_accvgpr_read_b32 v182, a206\n" \
"v_accvgpr_read_b32 v183, a207\n" \
"v_mfma_f32_16x16x32_bf16 a[204:207], v[172:175], v[68:71], 0\n" \
"s_add_u32 m0, s40, 0x1000\n" \
"s_add_u32 s84, s72, 0x1000\n" \
"v_mfma_f32_16x16x32_bf16 a[204:207], v[176:179], v[72:75], a[204:207]\n" \
"buffer_load_dwordx4 v20, s[80:83], s84 offen sc1 lds\n" \
"v_mfma_f32_16x16x32_bf16 a[224:227], v[152:155], v[80:83], 0\n" \
"v_cvt_pk_bf16_f32 v252, v252, v253\n" \
"v_cvt_pk_bf16_f32 v253, v254, v255\n" \
"v_mfma_f32_16x16x32_bf16 a[224:227], v[148:151], v[76:79], a[224:227]\n" \
"v_cvt_pk_bf16_f32 v254, v180, v181\n" \
"v_cvt_pk_bf16_f32 v255, v182, v183\n" \
"v_mfma_f32_16x16x32_bf16 a[228:231], v[156:159], v[76:79], 0\n" \
"v_accvgpr_write_b32 a8, v252\n" \
"v_accvgpr_write_b32 a9, v253\n" \
"v_mfma_f32_16x16x32_bf16 a[228:231], v[160:163], v[80:83], a[228:231]\n" \
"v_accvgpr_write_b32 a10, v254\n" \
"v_accvgpr_write_b32 a11, v255\n" \
"v_accvgpr_read_b32 v252, a232\n" \
"v_accvgpr_read_b32 v253, a233\n" \
"v_accvgpr_read_b32 v254, a234\n" \
"v_accvgpr_read_b32 v255, a235\n" \
"v_mfma_f32_16x16x32_bf16 a[232:235], v[168:171], v[80:83], 0\n" \
"s_add_u32 m0, s40, 0x3000\n" \
"s_add_u32 s85, s62, 0x0\n" \
"buffer_load_dwordx4 v21, s[76:79], s85 offen lds\n" \
"v_mfma_f32_16x16x32_bf16 a[232:235], v[164:167], v[76:79], a[232:235]\n" \
"v_accvgpr_read_b32 v180, a236\n" \
"v_accvgpr_read_b32 v181, a237\n" \
"v_accvgpr_read_b32 v182, a238\n" \
"v_accvgpr_read_b32 v183, a239\n" \
"v_mfma_f32_16x16x32_bf16 a[236:239], v[172:175], v[76:79], 0\n" \
"v_cvt_pk_bf16_f32 v252, v252, v253\n" \
"s_add_u32 m0, %[mA], 0x0\n" \
"v_mfma_f32_16x16x32_bf16 a[236:239], v[176:179], v[80:83], a[236:239]\n" \
"s_add_u32 s84, s62, 0x0\n" \
"s_nop 0\n" \
"v_cvt_pk_bf16_f32 v253, v254, v255\n" \
"v_cvt_pk_bf16_f32 v254, v180, v181\n" \
"v_cvt_pk_bf16_f32 v255, v182, v183\n" \
"v_accvgpr_write_b32 a12, v252\n" \
"v_accvgpr_write_b32 a13, v253\n" \
"v_accvgpr_write_b32 a14, v254\n" \
"v_accvgpr_write_b32 a15, v255\n" \
"v_accvgpr_read_b32 v252, a144\n" \
"v_accvgpr_read_b32 v253, a145\n" \
"v_accvgpr_read_b32 v254, a146\n" \
"v_accvgpr_read_b32 v255, a147\n" \
"v_mfma_f32_16x16x32_bf16 a[144:147], v[152:155], v[88:91], 0\n" \
"buffer_load_dwordx4 v24, s[76:79], s84 offen lds\n" \
"v_mfma_f32_16x16x32_bf16 a[144:147], v[148:151], v[84:87], a[144:147]\n" \
"v_cvt_pk_bf16_f32 v252, v252, v253\n" \
"v_accvgpr_read_b32 v180, a176\n" \
"v_accvgpr_read_b32 v181, a177\n" \
"v_accvgpr_read_b32 v182, a178\n" \
"v_accvgpr_read_b32 v183, a179\n" \
"v_cvt_pk_bf16_f32 v253, v254, v255\n" \
"v_cvt_pk_bf16_f32 v254, v180, v181\n" \
"v_cvt_pk_bf16_f32 v255, v182, v183\n" \
"v_accvgpr_write_b32 a40, v252\n" \
"v_accvgpr_write_b32 a41, v253\n" \
"v_accvgpr_write_b32 a42, v254\n" \
"v_accvgpr_write_b32 a43, v255\n" \
"v_accvgpr_read_b32 v252, a148\n" \
"v_accvgpr_read_b32 v253, a149\n" \
"v_accvgpr_read_b32 v254, a150\n" \
"v_accvgpr_read_b32 v255, a151\n" \
"v_mfma_f32_16x16x32_bf16 a[148:151], v[156:159], v[84:87], 0\n" \
"v_cvt_pk_bf16_f32 v252, v252, v253\n" \
"s_add_u32 m0, %[mA], 0x400\n" \
"v_mfma_f32_16x16x32_bf16 a[148:151], v[160:163], v[88:91], a[148:151]\n" \
"s_add_u32 s85, s62, 0x2400\n" \
"v_accvgpr_read_b32 v180, a180\n" \
"v_accvgpr_read_b32 v181, a181\n" \
"v_accvgpr_read_b32 v182, a182\n" \
"v_accvgpr_read_b32 v183, a183\n" \
"v_cvt_pk_bf16_f32 v253, v254, v255\n" \
"v_cvt_pk_bf16_f32 v254, v180, v181\n" \
"v_cvt_pk_bf16_f32 v255, v182, v183\n" \
"v_accvgpr_write_b32 a44, v252\n" \
"v_accvgpr_write_b32 a45, v253\n" \
"v_accvgpr_write_b32 a46, v254\n" \
"v_accvgpr_write_b32 a47, v255\n" \
"v_accvgpr_read_b32 v252, a152\n" \
"v_accvgpr_read_b32 v253, a153\n" \
"v_accvgpr_read_b32 v254, a154\n" \
"v_accvgpr_read_b32 v255, a155\n" \
"v_mfma_f32_16x16x32_bf16 a[152:155], v[168:171], v[88:91], 0\n" \
"L_pf5:\n" \
"s_cbranch_execz L_pf9\n" \
"s_nop 0\n" \
"buffer_load_dwordx4 v25, s[76:79], s85 offen lds\n" \
"v_mfma_f32_16x16x32_bf16 a[152:155], v[164:167], v[84:87], a[152:155]\n" \
"v_cvt_pk_bf16_f32 v252, v252, v253\n" \
"v_accvgpr_read_b32 v180, a184\n" \
"v_accvgpr_read_b32 v181, a185\n" \
"v_accvgpr_read_b32 v182, a186\n" \
"v_accvgpr_read_b32 v183, a187\n" \
"v_cvt_pk_bf16_f32 v253, v254, v255\n" \
"v_cvt_pk_bf16_f32 v254, v180, v181\n" \
"v_cvt_pk_bf16_f32 v255, v182, v183\n" \
"s_lshl_b32 s63, s62, 12\n" \
"v_accvgpr_write_b32 a72, v252\n" \
"v_accvgpr_write_b32 a73, v253\n" \
"v_accvgpr_write_b32 a74, v254\n" \
"v_accvgpr_write_b32 a75, v255\n" \
"v_accvgpr_read_b32 v252, a156\n" \
"v_accvgpr_read_b32 v253, a157\n" \
"v_accvgpr_read_b32 v254, a158\n" \
"v_accvgpr_read_b32 v255, a159\n" \
"v_mfma_f32_16x16x32_bf16 a[156:159], v[172:175], v[84:87], 0\n" \
"s_add_u32 m0, %[mB], 0x0\n" \
"s_add_u32 s84, s63, 0x0\n" \
"s_nop 0\n" \
"buffer_load_dwordx4 v26, s[68:71], s84 offen lds\n" \
"v_mfma_f32_16x16x32_bf16 a[156:159], v[176:179], v[88:91], a[156:159]\n" \
"s_add_u32 m0, %[mA], 0x800\n" \
"s_add_u32 s85, s62, 0x4800\n" \
"v_mfma_f32_16x16x32_bf16 a[176:179], v[152:155], v[96:99], 0\n" \
"v_accvgpr_read_b32 v180, a188\n" \
"v_accvgpr_read_b32 v181, a189\n" \
"v_mfma_f32_16x16x32_bf16 a[176:179], v[148:151], v[92:95], a[176:179]\n" \
"buffer_load_dwordx4 v24, s[76:79], s85 offen lds\n" \
"v_mfma_f32_16x16x32_bf16 a[180:183], v[156:159], v[92:95], 0\n" \
"s_add_u32 m0, %[mA], 0xc00\n" \
"s_add_u32 s84, s62, 0x6c00\n" \
"v_mfma_f32_16x16x32_bf16 a[180:183], v[160:163], v[96:99], a[180:183]\n" \
"v_accvgpr_read_b32 v182, a190\n" \
"v_accvgpr_read_b32 v183, a191\n" \
"v_mfma_f32_16x16x32_bf16 a[184:187], v[168:171], v[96:99], 0\n" \
"buffer_load_dwordx4 v25, s[76:79], s84 offen lds\n" \
"v_mfma_f32_16x16x32_bf16 a[184:187], v[164:167], v[92:95], a[184:187]\n" \
"v_cvt_pk_bf16_f32 v252, v252, v253\n" \
"v_cvt_pk_bf16_f32 v253, v254, v255\n" \
"v_mfma_f32_16x16x32_bf16 a[188:191], v[172:175], v[92:95], 0\n" \
"v_cvt_pk_bf16_f32 v254, v180, v181\n" \
"v_cvt_pk_bf16_f32 v255, v182, v183\n" \
"v_mfma_f32_16x16x32_bf16 a[188:191], v[176:179], v[96:99], a[188:191]\n" \
"v_accvgpr_write_b32 a76, v252\n" \
"v_accvgpr_write_b32 a77, v253\n" \
"v_accvgpr_write_b32 a78, v254\n" \
"v_accvgpr_write_b32 a79, v255\n" \
"v_accvgpr_read_b32 v252, a208\n" \
"v_accvgpr_read_b32 v253, a209\n" \
"v_accvgpr_read_b32 v254, a210\n" \
"v_accvgpr_read_b32 v255, a211\n" \
"v_mfma_f32_16x16x32_bf16 a[208:211], v[152:155], v[104:107], 0\n" \
"v_cvt_pk_bf16_f32 v252, v252, v253\n" \
"s_add_u32 m0, %[mB], 0x400\n" \
"v_mfma_f32_16x16x32_bf16 a[208:211], v[148:151], v[100:103], a[208:211]\n" \
"s_add_u32 s85, s63, 0x8000\n" \
"v_accvgpr_read_b32 v180, a240\n" \
"v_accvgpr_read_b32 v181, a241\n" \
"v_accvgpr_read_b32 v182, a242\n" \
"v_accvgpr_read_b32 v183, a243\n" \
"v_cvt_pk_bf16_f32 v253, v254, v255\n" \
"v_cvt_pk_bf16_f32 v254, v180, v181\n" \
"v_cvt_pk_bf16_f32 v255, v182, v183\n" \
"v_accvgpr_write_b32 a104, v252\n" \
"v_accvgpr_write_b32 a105, v253\n" \
"v_accvgpr_write_b32 a106, v254\n" \
"v_accvgpr_write_b32 a107, v255\n" \
"v_accvgpr_read_b32 v252, a212\n" \
"v_accvgpr_read_b32 v253, a213\n" \
"v_accvgpr_read_b32 v254, a214\n" \
"v_accvgpr_read_b32 v255, a215\n" \
"v_mfma_f32_16x16x32_bf16 a[212:215], v[156:159], v[100:103], 0\n" \
"buffer_load_dwordx4 v26, s[68:71], s85 offen lds\n" \
"ds_read_b64_tr_b16 v[140:141], v39 offset:32768\n" \
"ds_read_b64_tr_b16 v[142:143], v39 offset:33728\n" \
"ds_read_b128 v[84:87], v22 offset:32768\n" \
"ds_read_b128 v[88:91], v23 offset:32768\n" \
"v_mfma_f32_16x16x32_bf16 a[212:215], v[160:163], v[104:107], a[212:215]\n" \
"ds_read_b64_tr_b16 v[128:129], v37 offset:36864\n" \
"ds_read_b64_tr_b16 v[130:131], v37 offset:37376\n" \
"v_accvgpr_read_b32 v184, a216\n" \
"v_accvgpr_read_b32 v185, a217\n" \
"v_accvgpr_read_b32 v186, a218\n" \
"v_accvgpr_read_b32 v187, a219\n" \
"v_mfma_f32_16x16x32_bf16 a[216:219], v[168:171], v[104:107], 0\n" \
"s_add_u32 m0, %[mA], 0x1000\n" \
"s_add_u32 s84, s62, 0x9000\n" \
"buffer_load_dwordx4 v24, s[76:79], s84 offen lds\n" \
"ds_read_b64_tr_b16 v[124:125], v37 offset:32768\n" \
"ds_read_b64_tr_b16 v[126:127], v37 offset:33280\n" \
"v_mfma_f32_16x16x32_bf16 a[216:219], v[164:167], v[100:103], a[216:219]\n" \
"v_cvt_pk_bf16_f32 v252, v252, v253\n" \
"v_accvgpr_read_b32 v180, a244\n" \
"v_accvgpr_read_b32 v181, a245\n" \
"v_accvgpr_read_b32 v182, a246\n" \
"v_accvgpr_read_b32 v183, a247\n" \
"v_cvt_pk_bf16_f32 v253, v254, v255\n" \
"v_cvt_pk_bf16_f32 v254, v180, v181\n" \
"v_cvt_pk_bf16_f32 v255, v182, v183\n" \
"v_accvgpr_write_b32 a108, v252\n" \
"v_accvgpr_write_b32 a109, v253\n" \
"v_accvgpr_write_b32 a110, v254\n" \
"v_accvgpr_write_b32 a111, v255\n" \
"v_accvgpr_read_b32 v252, a220\n" \
"v_accvgpr_read_b32 v253, a221\n" \
"v_accvgpr_read_b32 v254, a222\n" \
"v_accvgpr_read_b32 v255, a223\n" \
"v_mfma_f32_16x16x32_bf16 a[220:223], v[172:175], v[100:103], 0\n" \
"ds_read_b64_tr_b16 v[132:133], v38 offset:32768\n" \
"ds_read_b64_tr_b16 v[134:135], v38 offset:33280\n" \
"ds_read_b64_tr_b16 v[136:137], v38 offset:36864\n" \
"ds_read_b64_tr_b16 v[138:139], v38 offset:37376\n" \
"v_mfma_f32_16x16x32_bf16 a[220:223], v[176:179], v[104:107], a[220:223]\n" \
"ds_read_b128 v[52:55], v28 offset:32768\n" \
"ds_read_b128 v[56:59], v29 offset:32768\n" \
"v_mfma_f32_16x16x32_bf16 a[240:243], v[152:155], v[112:115], 0\n" \
"s_add_u32 m0, %[mA], 0x1400\n" \
"s_add_u32 s85, s62, 0xb400\n" \
"buffer_load_dwordx4 v25, s[76:79], s85 offen lds\n" \
"v_mfma_f32_16x16x32_bf16 a[240:243], v[148:151], v[108:111], a[240:243]\n" \
"ds_read_b64_tr_b16 v[116:117], v36 offset:32768\n" \
"ds_read_b64_tr_b16 v[118:119], v36 offset:33280\n" \
"v_mfma_f32_16x16x32_bf16 a[244:247], v[156:159], v[108:111], 0\n" \
"s_add_u32 m0, %[mB], 0x800\n" \
"s_add_u32 s84, s63, 0x10000\n" \
"v_mfma_f32_16x16x32_bf16 a[244:247], v[160:163], v[112:115], a[244:247]\n" \
"buffer_load_dwordx4 v27, s[68:71], s84 offen lds\n" \
"ds_read_b64_tr_b16 v[120:121], v36 offset:36864\n" \
"ds_read_b64_tr_b16 v[122:123], v36 offset:37376\n" \
"v_accvgpr_read_b32 v180, a248\n" \
"v_accvgpr_read_b32 v181, a249\n" \
"v_accvgpr_read_b32 v182, a250\n" \
"v_accvgpr_read_b32 v183, a251\n" \
"v_mfma_f32_16x16x32_bf16 a[248:251], v[168:171], v[112:115], 0\n" \
"ds_read_b128 v[60:63], v28 offset:34816\n" \
"v_cvt_pk_bf16_f32 v184, v184, v185\n" \
"v_mfma_f32_16x16x32_bf16 a[248:251], v[164:167], v[108:111], a[248:251]\n" \
"v_cvt_pk_bf16_f32 v185, v186, v187\n" \
"v_cvt_pk_bf16_f32 v186, v180, v181\n" \
"v_cvt_pk_bf16_f32 v187, v182, v183\n" \
"v_accvgpr_read_b32 v180, a252\n" \
"v_accvgpr_read_b32 v181, a253\n" \
"v_accvgpr_read_b32 v182, a254\n" \
"v_accvgpr_read_b32 v183, a255\n" \
"v_mfma_f32_16x16x32_bf16 a[252:255], v[172:175], v[108:111], 0\n" \
"ds_read_b128 v[64:67], v29 offset:34816\n" \
"s_add_u32 m0, %[mA], 0x1800\n" \
"v_mfma_f32_16x16x32_bf16 a[252:255], v[176:179], v[112:115], a[252:255]\n" \
"s_add_u32 s85, s62, 0xd800\n" \
"buffer_load_dwordx4 v24, s[76:79], s85 offen lds\n" \
"s_waitcnt lgkmcnt(12)\n" \
"v_smfmac_f32_16x16x64_bf16 a[16:19], v[140:143], v[84:91], v50\n" \
"v_mfma_f32_16x16x32_bf16 a[48:51], v[128:131], v[88:91], a[48:51]\n" \
"s_add_u32 m0, %[mA], 0x1c00\n" \
"s_add_u32 s85, s62, 0xfc00\n" \
"v_mfma_f32_16x16x32_bf16 a[48:51], v[124:127], v[84:87], a[48:51]\n" \
"ds_read_b128 v[68:71], v28 offset:36864\n" \
"s_waitcnt lgkmcnt(8)\n" \
"v_mfma_f32_16x16x32_bf16 a[80:83], v[84:87], v[132:135], a[80:83]\n" \
"buffer_load_dwordx4 v25, s[76:79], s85 offen lds\n" \
"v_mfma_f32_16x16x32_bf16 a[80:83], v[88:91], v[136:139], a[80:83]\n" \
"ds_read_b128 v[72:75], v29 offset:36864\n" \
"s_waitcnt lgkmcnt(6)\n" \
"v_smfmac_f32_16x16x64_bf16 a[112:115], v[140:143], v[52:59], v50\n" \
"v_mfma_f32_16x16x32_bf16 a[20:23], v[128:131], v[56:59], a[20:23]\n" \
"s_add_u32 m0, %[mB], 0xc00\n" \
"s_add_u32 s84, s63, 0x18000\n" \
"buffer_load_dwordx4 v27, s[68:71], s84 offen lds\n" \
"v_mfma_f32_16x16x32_bf16 a[20:23], v[124:127], v[52:55], a[20:23]\n" \
"ds_read_b128 v[76:79], v28 offset:38912\n" \
"ds_read_b128 v[80:83], v29 offset:38912\n" \
"v_mfma_f32_16x16x32_bf16 a[52:55], v[116:119], v[52:55], a[52:55]\n" \
"s_add_u32 s88, s44, 0x0\n" \
"s_waitcnt lgkmcnt(5)\n" \
"v_mfma_f32_16x16x32_bf16 a[52:55], v[120:123], v[56:59], a[52:55]\n" \
"buffer_store_dwordx4 v[200:203], v20, s[80:83], s88 offen offset:2048 sc1\n" \
"v_mfma_f32_16x16x32_bf16 a[84:87], v[56:59], v[136:139], a[84:87]\n" \
"ds_read_b64_tr_b16 v[152:153], v32 offset:40960\n" \
"ds_read_b64_tr_b16 v[154:155], v32 offset:41984\n" \
"v_mfma_f32_16x16x32_bf16 a[84:87], v[52:55], v[132:135], a[84:87]\n" \
"ds_read_b64_tr_b16 v[148:149], v32 offset:32768\n" \
"s_waitcnt lgkmcnt(7)\n" \
"v_smfmac_f32_16x16x64_bf16 a[116:119], v[140:143], v[60:67], v50\n" \
"v_mfma_f32_16x16x32_bf16 a[24:27], v[124:127], v[60:63], a[24:27]\n" \
"ds_read_b64_tr_b16 v[150:151], v32 offset:33792\n" \
"ds_read_b64_tr_b16 v[156:157], v33 offset:32768\n" \
"v_mfma_f32_16x16x32_bf16 a[24:27], v[128:131], v[64:67], a[24:27]\n" \
"ds_read_b64_tr_b16 v[158:159], v33 offset:33792\n" \
"ds_read_b64_tr_b16 v[160:161], v33 offset:40960\n" \
"v_mfma_f32_16x16x32_bf16 a[56:59], v[120:123], v[64:67], a[56:59]\n" \
"ds_read_b64_tr_b16 v[162:163], v33 offset:41984\n" \
"s_add_u32 s89, s44, 0x1000\n" \
"v_mfma_f32_16x16x32_bf16 a[56:59], v[116:119], v[60:63], a[56:59]\n" \
"buffer_store_dwordx4 v[204:207], v20, s[80:83], s89 offen offset:2048 sc1\n" \
"v_mfma_f32_16x16x32_bf16 a[88:91], v[60:63], v[132:135], a[88:91]\n" \
"ds_read_b64_tr_b16 v[168:169], v34 offset:40960\n" \
"ds_read_b64_tr_b16 v[170:171], v34 offset:41984\n" \
"v_mfma_f32_16x16x32_bf16 a[88:91], v[64:67], v[136:139], a[88:91]\n" \
"ds_read_b64_tr_b16 v[164:165], v34 offset:32768\n" \
"s_waitcnt lgkmcnt(13)\n" \
"v_smfmac_f32_16x16x64_bf16 a[120:123], v[140:143], v[68:75], v50\n" \
"v_mfma_f32_16x16x32_bf16 a[28:31], v[128:131], v[72:75], a[28:31]\n" \
"ds_read_b64_tr_b16 v[166:167], v34 offset:33792\n" \
"ds_read_b64_tr_b16 v[172:173], v35 offset:32768\n" \
"v_mfma_f32_16x16x32_bf16 a[28:31], v[124:127], v[68:71], a[28:31]\n" \
"ds_read_b64_tr_b16 v[174:175], v35 offset:33792\n" \
"ds_read_b64_tr_b16 v[176:177], v35 offset:40960\n" \
"v_mfma_f32_16x16x32_bf16 a[60:63], v[116:119], v[68:71], a[60:63]\n" \
"ds_read_b64_tr_b16 v[178:179], v35 offset:41984\n" \
"s_add_u32 s88, s44, 0x0\n" \
"v_mfma_f32_16x16x32_bf16 a[60:63], v[120:123], v[72:75], a[60:63]\n" \
"buffer_store_dwordx4 v[208:211], v20, s[80:83], s88 offen offset:3072 sc1\n" \
"v_mfma_f32_16x16x32_bf16 a[92:95], v[72:75], v[136:139], a[92:95]\n" \
"ds_read_b128 v[84:87], v48 offset:32768\n" \
"ds_read_b128 v[88:91], v49 offset:32768\n" \
"v_mfma_f32_16x16x32_bf16 a[92:95], v[68:71], v[132:135], a[92:95]\n" \
"ds_read_b128 v[92:95], v48 offset:34816\n" \
"s_waitcnt lgkmcnt(15)\n" \
"s_nop 0\n" \
"v_smfmac_f32_16x16x64_bf16 a[124:127], v[140:143], v[76:83], v50\n" \
"v_mfma_f32_16x16x32_bf16 a[0:3], v[124:127], v[76:79], a[0:3]\n" \
"ds_read_b128 v[96:99], v49 offset:34816\n" \
"ds_read_b128 v[100:103], v48 offset:36864\n" \
"v_mfma_f32_16x16x32_bf16 a[0:3], v[128:131], v[80:83], a[0:3]\n" \
"ds_read_b128 v[104:107], v49 offset:36864\n" \
"ds_read_b128 v[108:111], v48 offset:38912\n" \
"v_mfma_f32_16x16x32_bf16 a[4:7], v[120:123], v[80:83], a[4:7]\n" \
"s_add_u32 s62, s62, 0x80\n" \
"ds_read_b128 v[112:115], v49 offset:38912\n" \
"v_mfma_f32_16x16x32_bf16 a[4:7], v[116:119], v[76:79], a[4:7]\n" \
"s_cmp_lt_u32 s62, 0x480\n" \
"s_cselect_b32 s62, s62, 0\n" \
"v_mfma_f32_16x16x32_bf16 a[32:35], v[76:79], v[132:135], a[32:35]\n" \
"s_lshl_b32 s72, s62, 6\n" \
"s_add_u32 s72, s72, s37\n" \
"v_mfma_f32_16x16x32_bf16 a[32:35], v[80:83], v[136:139], a[32:35]\n" \
"s_lshl_b32 s63, s62, 12\n" \
"v_cvt_pk_bf16_f32 v252, v252, v253\n" \
"v_mfma_f32_16x16x32_bf16 a[128:131], v[152:155], v[56:59], a[128:131]\n" \
"v_cvt_pk_bf16_f32 v253, v254, v255\n" \
"v_cvt_pk_bf16_f32 v254, v180, v181\n" \
"v_mfma_f32_16x16x32_bf16 a[128:131], v[148:151], v[52:55], a[128:131]\n" \
"v_cvt_pk_bf16_f32 v255, v182, v183\n" \
"s_waitcnt lgkmcnt(11)\n" \
"v_mfma_f32_16x16x32_bf16 a[132:135], v[156:159], v[52:55], a[132:135]\n" \
"v_mfma_f32_16x16x32_bf16 a[132:135], v[160:163], v[56:59], a[132:135]\n" \
"v_mfma_f32_16x16x32_bf16 a[136:139], v[168:171], v[56:59], a[136:139]\n" \
"v_mfma_f32_16x16x32_bf16 a[136:139], v[164:167], v[52:55], a[136:139]\n" \
"s_waitcnt lgkmcnt(7)\n" \
"v_mfma_f32_16x16x32_bf16 a[140:143], v[172:175], v[52:55], a[140:143]\n" \
"v_mfma_f32_16x16x32_bf16 a[140:143], v[176:179], v[56:59], a[140:143]\n" \
"v_mfma_f32_16x16x32_bf16 a[160:163], v[152:155], v[64:67], a[160:163]\n" \
"v_mfma_f32_16x16x32_bf16 a[160:163], v[148:151], v[60:63], a[160:163]\n" \
"v_mfma_f32_16x16x32_bf16 a[164:167], v[156:159], v[60:63], a[164:167]\n" \
"v_mfma_f32_16x16x32_bf16 a[164:167], v[160:163], v[64:67], a[164:167]\n" \
"v_mfma_f32_16x16x32_bf16 a[168:171], v[168:171], v[64:67], a[168:171]\n" \
"v_mfma_f32_16x16x32_bf16 a[168:171], v[164:167], v[60:63], a[168:171]\n" \
"v_mfma_f32_16x16x32_bf16 a[172:175], v[172:175], v[60:63], a[172:175]\n" \
"v_mfma_f32_16x16x32_bf16 a[172:175], v[176:179], v[64:67], a[172:175]\n" \
"v_mfma_f32_16x16x32_bf16 a[192:195], v[152:155], v[72:75], a[192:195]\n" \
"v_mfma_f32_16x16x32_bf16 a[192:195], v[148:151], v[68:71], a[192:195]\n" \
"s_waitcnt vmcnt(3) lgkmcnt(0)\n" \
"s_barrier\n" \
"v_mfma_f32_16x16x32_bf16 a[196:199], v[156:159], v[68:71], a[196:199]\n" \
"s_add_u32 m0, s40, 0x8000\n" \
"s_add_u32 s85, s72, 0x0\n" \
"s_nop 0\n" \
"v_mfma_f32_16x16x32_bf16 a[196:199], v[160:163], v[72:75], a[196:199]\n" \
"buffer_load_dwordx4 v20, s[80:83], s85 offen sc1 lds\n" \
"v_mfma_f32_16x16x32_bf16 a[200:203], v[168:171], v[72:75], a[200:203]\n" \
"s_add_u32 m0, s40, 0x9000\n" \
"s_add_u32 s84, s72, 0x1000\n" \
"v_mfma_f32_16x16x32_bf16 a[200:203], v[164:167], v[68:71], a[200:203]\n" \
"ds_read_b64_tr_b16 v[140:141], v39 offset:0\n" \
"ds_read_b64_tr_b16 v[142:143], v39 offset:960\n" \
"v_mfma_f32_16x16x32_bf16 a[204:207], v[172:175], v[68:71], a[204:207]\n" \
"buffer_load_dwordx4 v20, s[80:83], s84 offen sc1 lds\n" \
"v_mfma_f32_16x16x32_bf16 a[204:207], v[176:179], v[72:75], a[204:207]\n" \
"ds_read_b64_tr_b16 v[128:129], v37 offset:4096\n" \
"ds_read_b64_tr_b16 v[130:131], v37 offset:4608\n" \
"v_mfma_f32_16x16x32_bf16 a[224:227], v[152:155], v[80:83], a[224:227]\n" \
"s_add_u32 m0, s40, 0xb000\n" \
"s_add_u32 s85, s62, 0x0\n" \
"v_mfma_f32_16x16x32_bf16 a[224:227], v[148:151], v[76:79], a[224:227]\n" \
"buffer_load_dwordx4 v21, s[76:79], s85 offen lds\n" \
"v_mfma_f32_16x16x32_bf16 a[228:231], v[156:159], v[76:79], a[228:231]\n" \
"ds_read_b64_tr_b16 v[124:125], v37 offset:0\n" \
"ds_read_b64_tr_b16 v[126:127], v37 offset:512\n" \
"v_mfma_f32_16x16x32_bf16 a[228:231], v[160:163], v[80:83], a[228:231]\n" \
"s_add_u32 m0, %[mA], 0x8000\n" \
"s_add_u32 s84, s62, 0x0\n" \
"v_mfma_f32_16x16x32_bf16 a[232:235], v[168:171], v[80:83], a[232:235]\n" \
"buffer_load_dwordx4 v24, s[76:79], s84 offen lds\n" \
"v_mfma_f32_16x16x32_bf16 a[232:235], v[164:167], v[76:79], a[232:235]\n" \
"ds_read_b64_tr_b16 v[132:133], v38 offset:0\n" \
"ds_read_b64_tr_b16 v[134:135], v38 offset:512\n" \
"v_mfma_f32_16x16x32_bf16 a[236:239], v[172:175], v[76:79], a[236:239]\n" \
"ds_read_b64_tr_b16 v[136:137], v38 offset:4096\n" \
"ds_read_b64_tr_b16 v[138:139], v38 offset:4608\n" \
"v_mfma_f32_16x16x32_bf16 a[236:239], v[176:179], v[80:83], a[236:239]\n" \
"s_add_u32 m0, %[mA], 0x8400\n" \
"s_add_u32 s85, s62, 0x2400\n" \
"v_mfma_f32_16x16x32_bf16 a[144:147], v[152:155], v[88:91], a[144:147]\n" \
"buffer_load_dwordx4 v25, s[76:79], s85 offen lds\n" \
"v_mfma_f32_16x16x32_bf16 a[144:147], v[148:151], v[84:87], a[144:147]\n" \
"ds_read_b128 v[52:55], v28 offset:0\n" \
"ds_read_b128 v[56:59], v29 offset:0\n" \
"v_mfma_f32_16x16x32_bf16 a[148:151], v[156:159], v[84:87], a[148:151]\n" \
"s_add_u32 m0, %[mB], 0x8000\n" \
"s_add_u32 s84, s63, 0x0\n" \
"s_nop 0\n" \
"v_mfma_f32_16x16x32_bf16 a[148:151], v[160:163], v[88:91], a[148:151]\n" \
"buffer_load_dwordx4 v26, s[68:71], s84 offen lds\n" \
"v_mfma_f32_16x16x32_bf16 a[152:155], v[168:171], v[88:91], a[152:155]\n" \
"ds_read_b64_tr_b16 v[116:117], v36 offset:0\n" \
"ds_read_b64_tr_b16 v[118:119], v36 offset:512\n" \
"v_mfma_f32_16x16x32_bf16 a[152:155], v[164:167], v[84:87], a[152:155]\n" \
"ds_read_b64_tr_b16 v[120:121], v36 offset:4096\n" \
"ds_read_b64_tr_b16 v[122:123], v36 offset:4608\n" \
"v_mfma_f32_16x16x32_bf16 a[156:159], v[172:175], v[84:87], a[156:159]\n" \
"s_add_u32 m0, %[mA], 0x8800\n" \
"s_add_u32 s85, s62, 0x4800\n" \
"v_mfma_f32_16x16x32_bf16 a[156:159], v[176:179], v[88:91], a[156:159]\n" \
"buffer_load_dwordx4 v24, s[76:79], s85 offen lds\n" \
"v_mfma_f32_16x16x32_bf16 a[176:179], v[152:155], v[96:99], a[176:179]\n" \
"ds_read_b128 v[84:87], v22 offset:0\n" \
"ds_read_b128 v[88:91], v23 offset:0\n" \
"v_mfma_f32_16x16x32_bf16 a[176:179], v[148:151], v[92:95], a[176:179]\n" \
"s_add_u32 m0, %[mA], 0x8c00\n" \
"s_add_u32 s84, s62, 0x6c00\n" \
"v_mfma_f32_16x16x32_bf16 a[180:183], v[156:159], v[92:95], a[180:183]\n" \
"buffer_load_dwordx4 v25, s[76:79], s84 offen lds\n" \
"v_mfma_f32_16x16x32_bf16 a[180:183], v[160:163], v[96:99], a[180:183]\n" \
"ds_read_b128 v[60:63], v28 offset:2048\n" \
"ds_read_b128 v[64:67], v29 offset:2048\n" \
"v_mfma_f32_16x16x32_bf16 a[184:187], v[168:171], v[96:99], a[184:187]\n" \
"ds_read_b128 v[68:71], v28 offset:4096\n" \
"ds_read_b128 v[72:75], v29 offset:4096\n" \
"v_mfma_f32_16x16x32_bf16 a[184:187], v[164:167], v[92:95], a[184:187]\n" \
"s_add_u32 m0, %[mB], 0x8400\n" \
"s_add_u32 s85, s63, 0x8000\n" \
"v_mfma_f32_16x16x32_bf16 a[188:191], v[172:175], v[92:95], a[188:191]\n" \
"buffer_load_dwordx4 v26, s[68:71], s85 offen lds\n" \
"v_mfma_f32_16x16x32_bf16 a[188:191], v[176:179], v[96:99], a[188:191]\n" \
"ds_read_b128 v[76:79], v28 offset:6144\n" \
"ds_read_b128 v[80:83], v29 offset:6144\n" \
"v_mfma_f32_16x16x32_bf16 a[208:211], v[152:155], v[104:107], a[208:211]\n" \
"s_add_u32 m0, %[mA], 0x9000\n" \
"s_add_u32 s84, s62, 0x9000\n" \
"v_mfma_f32_16x16x32_bf16 a[208:211], v[148:151], v[100:103], a[208:211]\n" \
"buffer_load_dwordx4 v24, s[76:79], s84 offen lds\n" \
"v_mfma_f32_16x16x32_bf16 a[212:215], v[156:159], v[100:103], a[212:215]\n" \
"ds_read_b128 v[92:95], v48 offset:2048\n" \
"ds_read_b128 v[96:99], v49 offset:2048\n" \
"v_mfma_f32_16x16x32_bf16 a[212:215], v[160:163], v[104:107], a[212:215]\n" \
"v_mfma_f32_16x16x32_bf16 a[216:219], v[168:171], v[104:107], a[216:219]\n" \
"s_add_u32 m0, %[mA], 0x9400\n" \
"s_add_u32 s85, s62, 0xb400\n" \
"v_mfma_f32_16x16x32_bf16 a[216:219], v[164:167], v[100:103], a[216:219]\n" \
"buffer_load_dwordx4 v25, s[76:79], s85 offen lds\n" \
"v_mfma_f32_16x16x32_bf16 a[220:223], v[172:175], v[100:103], a[220:223]\n" \
"ds_read_b128 v[100:103], v48 offset:4096\n" \
"v_mfma_f32_16x16x32_bf16 a[220:223], v[176:179], v[104:107], a[220:223]\n" \
"s_add_u32 m0, %[mB], 0x8800\n" \
"s_add_u32 s84, s63, 0x10000\n" \
"v_mfma_f32_16x16x32_bf16 a[240:243], v[152:155], v[112:115], a[240:243]\n" \
"buffer_load_dwordx4 v27, s[68:71], s84 offen lds\n" \
"v_mfma_f32_16x16x32_bf16 a[240:243], v[148:151], v[108:111], a[240:243]\n" \
"ds_read_b64_tr_b16 v[152:153], v32 offset:8192\n" \
"ds_read_b64_tr_b16 v[154:155], v32 offset:9216\n" \
"v_mfma_f32_16x16x32_bf16 a[244:247], v[156:159], v[108:111], a[244:247]\n" \
"ds_read_b64_tr_b16 v[148:149], v32 offset:0\n" \
"ds_read_b64_tr_b16 v[150:151], v32 offset:1024\n" \
"v_mfma_f32_16x16x32_bf16 a[244:247], v[160:163], v[112:115], a[244:247]\n" \
"s_add_u32 m0, %[mA], 0x9800\n" \
"s_add_u32 s85, s62, 0xd800\n" \
"v_mfma_f32_16x16x32_bf16 a[248:251], v[168:171], v[112:115], a[248:251]\n" \
"buffer_load_dwordx4 v24, s[76:79], s85 offen lds\n" \
"v_mfma_f32_16x16x32_bf16 a[248:251], v[164:167], v[108:111], a[248:251]\n" \
"ds_read_b64_tr_b16 v[156:157], v33 offset:0\n" \
"ds_read_b64_tr_b16 v[158:159], v33 offset:1024\n" \
"v_mfma_f32_16x16x32_bf16 a[252:255], v[172:175], v[108:111], a[252:255]\n" \
"ds_read_b64_tr_b16 v[160:161], v33 offset:8192\n" \
"ds_read_b64_tr_b16 v[162:163], v33 offset:9216\n" \
"v_mfma_f32_16x16x32_bf16 a[252:255], v[176:179], v[112:115], a[252:255]\n" \
"s_add_u32 m0, %[mA], 0x9c00\n" \
"s_waitcnt lgkmcnt(11)\n" \
"s_nop 0\n" \
"v_smfmac_f32_16x16x64_bf16 a[16:19], v[140:143], v[84:91], v50\n" \
"v_mfma_f32_16x16x32_bf16 a[48:51], v[128:131], v[88:91], a[48:51]\n" \
"s_add_u32 s85, s62, 0xfc00\n" \
"ds_read_b64_tr_b16 v[168:169], v34 offset:8192\n" \
"v_mfma_f32_16x16x32_bf16 a[48:51], v[124:127], v[84:87], a[48:51]\n" \
"buffer_load_dwordx4 v25, s[76:79], s85 offen lds\n" \
"v_mfma_f32_16x16x32_bf16 a[80:83], v[84:87], v[132:135], a[80:83]\n" \
"s_add_u32 m0, %[mB], 0x8c00\n" \
"s_add_u32 s84, s63, 0x18000\n" \
"v_mfma_f32_16x16x32_bf16 a[80:83], v[88:91], v[136:139], a[80:83]\n" \
"ds_read_b64_tr_b16 v[170:171], v34 offset:9216\n" \
"ds_read_b64_tr_b16 v[164:165], v34 offset:0\n" \
"v_smfmac_f32_16x16x64_bf16 a[112:115], v[140:143], v[52:59], v50\n" \
"v_mfma_f32_16x16x32_bf16 a[20:23], v[128:131], v[56:59], a[20:23]\n" \
"buffer_load_dwordx4 v27, s[68:71], s84 offen lds\n" \
"v_mfma_f32_16x16x32_bf16 a[20:23], v[124:127], v[52:55], a[20:23]\n" \
"s_add_u32 s89, s44, 0x1000\n" \
"ds_read_b64_tr_b16 v[166:167], v34 offset:1024\n" \
"v_mfma_f32_16x16x32_bf16 a[52:55], v[116:119], v[52:55], a[52:55]\n" \
"ds_read_b64_tr_b16 v[172:173], v35 offset:0\n" \
"ds_read_b64_tr_b16 v[174:175], v35 offset:1024\n" \
"v_mfma_f32_16x16x32_bf16 a[52:55], v[120:123], v[56:59], a[52:55]\n" \
"buffer_store_dwordx4 v[212:215], v20, s[80:83], s89 offen offset:3072 sc1\n" \
"v_mfma_f32_16x16x32_bf16 a[84:87], v[56:59], v[136:139], a[84:87]\n" \
"ds_read_b64_tr_b16 v[176:177], v35 offset:8192\n" \
"ds_read_b64_tr_b16 v[178:179], v35 offset:9216\n" \
"v_mfma_f32_16x16x32_bf16 a[84:87], v[52:55], v[132:135], a[84:87]\n" \
"ds_read_b128 v[84:87], v48 offset:0\n" \
"ds_read_b128 v[88:91], v49 offset:0\n" \
"v_smfmac_f32_16x16x64_bf16 a[116:119], v[140:143], v[60:67], v50\n" \
"v_mfma_f32_16x16x32_bf16 a[24:27], v[124:127], v[60:63], a[24:27]\n" \
"ds_read_b128 v[104:107], v49 offset:4096\n" \
"ds_read_b128 v[108:111], v48 offset:6144\n" \
"v_mfma_f32_16x16x32_bf16 a[24:27], v[128:131], v[64:67], a[24:27]\n" \
"s_add_u32 s88, s42, 0x0\n" \
"ds_read_b128 v[112:115], v49 offset:6144\n" \
"v_mfma_f32_16x16x32_bf16 a[56:59], v[120:123], v[64:67], a[56:59]\n" \
"buffer_store_dwordx4 v[216:219], v20, s[80:83], s88 offen offset:0 sc1\n" \
"v_mfma_f32_16x16x32_bf16 a[56:59], v[116:119], v[60:63], a[56:59]\n" \
"s_add_u32 s62, s62, 0x80\n" \
"s_add_u32 s94, %[b], %[w]\n" \
"v_mfma_f32_16x16x32_bf16 a[88:91], v[60:63], v[132:135], a[88:91]\n" \
"s_cmp_lt_u32 s62, 0x480\n" \
"s_cselect_b32 s62, s62, 0\n" \
"v_mfma_f32_16x16x32_bf16 a[88:91], v[64:67], v[136:139], a[88:91]\n" \
"s_lshl_b32 s72, s62, 6\n" \
"s_add_u32 s72, s72, s37\n" \
"v_smfmac_f32_16x16x64_bf16 a[120:123], v[140:143], v[68:75], v50\n" \
"v_mfma_f32_16x16x32_bf16 a[28:31], v[128:131], v[72:75], a[28:31]\n" \
"s_lshl_b32 s63, s62, 12\n" \
"s_and_b32 s96, 7, %[b]\n" \
"v_mfma_f32_16x16x32_bf16 a[28:31], v[124:127], v[68:71], a[28:31]\n" \
"s_cmp_lg_u32 s94, 0\n" \
"s_cselect_b32 s95, 0x40000000, 0x5bc0000\n" \
"v_mfma_f32_16x16x32_bf16 a[60:63], v[116:119], v[68:71], a[60:63]\n" \
"s_add_u32 s89, s42, 0x1000\n" \
"s_mul_i32 s97, 0x48, s96\n" \
"v_mfma_f32_16x16x32_bf16 a[60:63], v[120:123], v[72:75], a[60:63]\n" \
"buffer_store_dwordx4 v[220:223], v20, s[80:83], s89 offen offset:0 sc1\n" \
"v_mfma_f32_16x16x32_bf16 a[92:95], v[72:75], v[136:139], a[92:95]\n" \
"s_add_u32 s98, 0x47, s97\n" \
"v_writelane_b32 v11, s95, 50\n" \
"v_mfma_f32_16x16x32_bf16 a[92:95], v[68:71], v[132:135], a[92:95]\n" \
"s_cmp_lt_u32 %[w], 3\n" \
"s_cselect_b32 s99, s98, 0xfff\n" \
"v_smfmac_f32_16x16x64_bf16 a[124:127], v[140:143], v[76:83], v50\n" \
"v_mfma_f32_16x16x32_bf16 a[0:3], v[124:127], v[76:79], a[0:3]\n" \
"s_lshr_b32 s94, %[b], 3\n" \
"s_lshl_b32 s67, s96, 2\n" \
"v_mfma_f32_16x16x32_bf16 a[0:3], v[128:131], v[80:83], a[0:3]\n" \
"s_and_b32 s95, 3, s94\n" \
"s_mul_i32 s100, 0x480, s99\n" \
"v_mfma_f32_16x16x32_bf16 a[4:7], v[120:123], v[80:83], a[4:7]\n" \
"s_lshl_b32 s64, %[w], 5\n" \
"s_add_u32 s98, s67, s95\n" \
"v_mfma_f32_16x16x32_bf16 a[4:7], v[116:119], v[76:79], a[4:7]\n" \
"s_add_u32 s101, 0x120, s100\n" \
"s_add_u32 s65, s97, s64\n" \
"v_mfma_f32_16x16x32_bf16 a[32:35], v[76:79], v[132:135], a[32:35]\n" \
"s_mul_i32 s99, 0x24000, s98\n" \
"v_writelane_b32 v11, s101, 15\n" \
"v_mfma_f32_16x16x32_bf16 a[32:35], v[80:83], v[136:139], a[32:35]\n" \
"s_mul_i32 s66, 0x480, s65\n" \
"s_waitcnt lgkmcnt(0)\n" \
"v_mfma_f32_16x16x32_bf16 a[128:131], v[152:155], v[56:59], a[128:131]\n" \
"s_add_u32 s100, 0x1b000, s99\n" \
"s_min_u32 s97, 2, %[w]\n" \
"v_mfma_f32_16x16x32_bf16 a[128:131], v[148:151], v[52:55], a[128:131]\n" \
"s_cmp_lt_u32 %[w], 3\n" \
"s_cselect_b32 s101, s66, s100\n" \
"v_mfma_f32_16x16x32_bf16 a[132:135], v[156:159], v[52:55], a[132:135]\n" \
"s_mul_i32 s64, 0x2400, s97\n" \
"v_writelane_b32 v11, s101, 16\n" \
"v_mfma_f32_16x16x32_bf16 a[132:135], v[160:163], v[56:59], a[132:135]\n" \
"s_add_u32 s65, 0x20000, s64\n" \
"s_mul_i32 s94, 0x9000, s97\n" \
"v_mfma_f32_16x16x32_bf16 a[136:139], v[168:171], v[56:59], a[136:139]\n" \
"s_mul_i32 s95, 0x2400, %[w]\n" \
"v_writelane_b32 v11, s65, 17\n" \
"v_mfma_f32_16x16x32_bf16 a[136:139], v[164:167], v[52:55], a[136:139]\n" \
"s_add_u32 s67, s99, s94\n" \
"v_writelane_b32 v11, s67, 22\n" \
"v_mfma_f32_16x16x32_bf16 a[140:143], v[172:175], v[52:55], a[140:143]\n" \
"s_cmp_lt_u32 %[w], 3\n" \
"s_cselect_b32 s98, s95, 0x12000\n" \
"v_mfma_f32_16x16x32_bf16 a[140:143], v[176:179], v[56:59], a[140:143]\n" \
"s_add_u32 s66, 0x20000, s95\n" \
"v_writelane_b32 v11, s98, 18\n" \
"v_mfma_f32_16x16x32_bf16 a[160:163], v[152:155], v[64:67], a[160:163]\n" \
"s_cmp_lt_u32 %[w], 3\n" \
"s_cselect_b32 s100, s66, 0xfc00\n" \
"v_mfma_f32_16x16x32_bf16 a[160:163], v[148:151], v[60:63], a[160:163]\n" \
"v_writelane_b32 v11, s95, 19\n" \
"s_lshl_b32 s101, %[w], 10\n" \
"v_mfma_f32_16x16x32_bf16 a[164:167], v[156:159], v[60:63], a[164:167]\n" \
"s_lshr_b32 s65, s96, 1\n" \
"v_writelane_b32 v11, s100, 20\n" \
"v_mfma_f32_16x16x32_bf16 a[164:167], v[160:163], v[64:67], a[164:167]\n" \
"s_cmp_eq_u32 %[w], s65\n" \
"s_cselect_b32 s97, 0xff0000, 0\n" \
"v_mfma_f32_16x16x32_bf16 a[168:171], v[168:171], v[64:67], a[168:171]\n" \
"s_add_u32 s64, 0x1d400, s101\n" \
"v_writelane_b32 v11, s64, 21\n" \
"v_mfma_f32_16x16x32_bf16 a[168:171], v[164:167], v[60:63], a[168:171]\n" \
"v_writelane_b32 v11, s97, 6\n" \
"v_mfma_f32_16x16x32_bf16 a[172:175], v[172:175], v[60:63], a[172:175]\n" \
"v_mfma_f32_16x16x32_bf16 a[172:175], v[176:179], v[64:67], a[172:175]\n" \
"v_mfma_f32_16x16x32_bf16 a[192:195], v[152:155], v[72:75], a[192:195]\n" \
"v_mfma_f32_16x16x32_bf16 a[192:195], v[148:151], v[68:71], a[192:195]\n" \
"s_waitcnt vmcnt(3)\n" \
"s_barrier\n" \
"v_mfma_f32_16x16x32_bf16 a[196:199], v[156:159], v[68:71], a[196:199]\n" \
"s_add_u32 m0, s40, 0x0\n" \
"s_add_u32 s85, s72, 0x0\n" \
"s_nop 0\n" \
"v_mfma_f32_16x16x32_bf16 a[196:199], v[160:163], v[72:75], a[196:199]\n" \
"buffer_load_dwordx4 v20, s[80:83], s85 offen sc1 lds\n" \
"v_mfma_f32_16x16x32_bf16 a[200:203], v[168:171], v[72:75], a[200:203]\n" \
"s_add_u32 m0, s40, 0x1000\n" \
"s_add_u32 s84, s72, 0x1000\n" \
"v_mfma_f32_16x16x32_bf16 a[200:203], v[164:167], v[68:71], a[200:203]\n" \
"ds_read_b64_tr_b16 v[140:141], v39 offset:32768\n" \
"ds_read_b64_tr_b16 v[142:143], v39 offset:33728\n" \
"v_mfma_f32_16x16x32_bf16 a[204:207], v[172:175], v[68:71], a[204:207]\n" \
"buffer_load_dwordx4 v20, s[80:83], s84 offen sc1 lds\n" \
"v_mfma_f32_16x16x32_bf16 a[204:207], v[176:179], v[72:75], a[204:207]\n" \
"ds_read_b64_tr_b16 v[128:129], v37 offset:36864\n" \
"ds_read_b64_tr_b16 v[130:131], v37 offset:37376\n" \
"v_mfma_f32_16x16x32_bf16 a[224:227], v[152:155], v[80:83], a[224:227]\n" \
"L_pf6:\n" \
"s_cbranch_execz L_pf10\n" \
"s_nop 0\n" \
"s_add_u32 m0, s40, 0x3000\n" \
"s_add_u32 s85, s62, 0x0\n" \
"v_mfma_f32_16x16x32_bf16 a[224:227], v[148:151], v[76:79], a[224:227]\n" \
"buffer_load_dwordx4 v21, s[76:79], s85 offen lds\n" \
"v_mfma_f32_16x16x32_bf16 a[228:231], v[156:159], v[76:79], a[228:231]\n" \
"ds_read_b64_tr_b16 v[124:125], v37 offset:32768\n" \
"ds_read_b64_tr_b16 v[126:127], v37 offset:33280\n" \
"v_mfma_f32_16x16x32_bf16 a[228:231], v[160:163], v[80:83], a[228:231]\n" \
"s_add_u32 m0, %[mA], 0x0\n" \
"s_add_u32 s84, s62, 0x0\n" \
"s_nop 0\n" \
"v_mfma_f32_16x16x32_bf16 a[232:235], v[168:171], v[80:83], a[232:235]\n" \
"buffer_load_dwordx4 v24, s[76:79], s84 offen lds\n" \
"v_mfma_f32_16x16x32_bf16 a[232:235], v[164:167], v[76:79], a[232:235]\n" \
"ds_read_b64_tr_b16 v[132:133], v38 offset:32768\n" \
"ds_read_b64_tr_b16 v[134:135], v38 offset:33280\n" \
"v_mfma_f32_16x16x32_bf16 a[236:239], v[172:175], v[76:79], a[236:239]\n" \
"ds_read_b64_tr_b16 v[136:137], v38 offset:36864\n" \
"ds_read_b64_tr_b16 v[138:139], v38 offset:37376\n" \
"v_mfma_f32_16x16x32_bf16 a[236:239], v[176:179], v[80:83], a[236:239]\n" \
"s_add_u32 m0, %[mA], 0x400\n" \
"s_add_u32 s85, s62, 0x2400\n" \
"v_mfma_f32_16x16x32_bf16 a[144:147], v[152:155], v[88:91], a[144:147]\n" \
"buffer_load_dwordx4 v25, s[76:79], s85 offen lds\n" \
"v_mfma_f32_16x16x32_bf16 a[144:147], v[148:151], v[84:87], a[144:147]\n" \
"ds_read_b128 v[52:55], v28 offset:32768\n" \
"ds_read_b128 v[56:59], v29 offset:32768\n" \
"v_mfma_f32_16x16x32_bf16 a[148:151], v[156:159], v[84:87], a[148:151]\n" \
"s_add_u32 m0, %[mB], 0x0\n" \
"s_add_u32 s84, s63, 0x0\n" \
"v_mfma_f32_16x16x32_bf16 a[148:151], v[160:163], v[88:91], a[148:151]\n" \
"buffer_load_dwordx4 v26, s[68:71], s84 offen lds\n" \
"v_mfma_f32_16x16x32_bf16 a[152:155], v[168:171], v[88:91], a[152:155]\n" \
"ds_read_b64_tr_b16 v[116:117], v36 offset:32768\n" \
"ds_read_b64_tr_b16 v[118:119], v36 offset:33280\n" \
"v_mfma_f32_16x16x32_bf16 a[152:155], v[164:167], v[84:87], a[152:155]\n" \
"ds_read_b64_tr_b16 v[120:121], v36 offset:36864\n" \
"ds_read_b64_tr_b16 v[122:123], v36 offset:37376\n" \
"v_mfma_f32_16x16x32_bf16 a[156:159], v[172:175], v[84:87], a[156:159]\n" \
"s_add_u32 m0, %[mA], 0x800\n" \
"s_add_u32 s85, s62, 0x4800\n" \
"v_mfma_f32_16x16x32_bf16 a[156:159], v[176:179], v[88:91], a[156:159]\n" \
"buffer_load_dwordx4 v24, s[76:79], s85 offen lds\n" \
"v_mfma_f32_16x16x32_bf16 a[176:179], v[152:155], v[96:99], a[176:179]\n" \
"ds_read_b128 v[84:87], v22 offset:32768\n" \
"ds_read_b128 v[88:91], v23 offset:32768\n" \
"v_mfma_f32_16x16x32_bf16 a[176:179], v[148:151], v[92:95], a[176:179]\n" \
"s_add_u32 m0, %[mA], 0xc00\n" \
"s_add_u32 s84, s62, 0x6c00\n" \
"v_mfma_f32_16x16x32_bf16 a[180:183], v[156:159], v[92:95], a[180:183]\n" \
"buffer_load_dwordx4 v25, s[76:79], s84 offen lds\n" \
"v_mfma_f32_16x16x32_bf16 a[180:183], v[160:163], v[96:99], a[180:183]\n" \
"ds_read_b128 v[60:63], v28 offset:34816\n" \
"ds_read_b128 v[64:67], v29 offset:34816\n" \
"v_mfma_f32_16x16x32_bf16 a[184:187], v[168:171], v[96:99], a[184:187]\n" \
"ds_read_b128 v[68:71], v28 offset:36864\n" \
"ds_read_b128 v[72:75], v29 offset:36864\n" \
"v_mfma_f32_16x16x32_bf16 a[184:187], v[164:167], v[92:95], a[184:187]\n" \
"s_add_u32 m0, %[mB], 0x400\n" \
"s_add_u32 s85, s63, 0x8000\n" \
"v_mfma_f32_16x16x32_bf16 a[188:191], v[172:175], v[92:95], a[188:191]\n" \
"buffer_load_dwordx4 v26, s[68:71], s85 offen lds\n" \
"v_mfma_f32_16x16x32_bf16 a[188:191], v[176:179], v[96:99], a[188:191]\n" \
"ds_read_b128 v[76:79], v28 offset:38912\n" \
"ds_read_b128 v[80:83], v29 offset:38912\n" \
"v_mfma_f32_16x16x32_bf16 a[208:211], v[152:155], v[104:107], a[208:211]\n" \
"s_add_u32 m0, %[mA], 0x1000\n" \
"s_add_u32 s84, s62, 0x9000\n" \
"v_mfma_f32_16x16x32_bf16 a[208:211], v[148:151], v[100:103], a[208:211]\n" \
"buffer_load_dwordx4 v24, s[76:79], s84 offen lds\n" \
"v_mfma_f32_16x16x32_bf16 a[212:215], v[156:159], v[100:103], a[212:215]\n" \
"ds_read_b128 v[92:95], v48 offset:34816\n" \
"ds_read_b128 v[96:99], v49 offset:34816\n" \
"v_mfma_f32_16x16x32_bf16 a[212:215], v[160:163], v[104:107], a[212:215]\n" \
"v_mfma_f32_16x16x32_bf16 a[216:219], v[168:171], v[104:107], a[216:219]\n" \
"s_add_u32 m0, %[mA], 0x1400\n" \
"s_add_u32 s85, s62, 0xb400\n" \
"v_mfma_f32_16x16x32_bf16 a[216:219], v[164:167], v[100:103], a[216:219]\n" \
"buffer_load_dwordx4 v25, s[76:79], s85 offen lds\n" \
"v_mfma_f32_16x16x32_bf16 a[220:223], v[172:175], v[100:103], a[220:223]\n" \
"ds_read_b128 v[100:103], v48 offset:36864\n" \
"v_mfma_f32_16x16x32_bf16 a[220:223], v[176:179], v[104:107], a[220:223]\n" \
"s_add_u32 m0, %[mB], 0x800\n" \
"s_add_u32 s84, s63, 0x10000\n" \
"v_mfma_f32_16x16x32_bf16 a[240:243], v[152:155], v[112:115], a[240:243]\n" \
"buffer_load_dwordx4 v27, s[68:71], s84 offen lds\n" \
"v_mfma_f32_16x16x32_bf16 a[240:243], v[148:151], v[108:111], a[240:243]\n" \
"ds_read_b64_tr_b16 v[152:153], v32 offset:40960\n" \
"ds_read_b64_tr_b16 v[154:155], v32 offset:41984\n" \
"v_mfma_f32_16x16x32_bf16 a[244:247], v[156:159], v[108:111], a[244:247]\n" \
"ds_read_b64_tr_b16 v[148:149], v32 offset:32768\n" \
"ds_read_b64_tr_b16 v[150:151], v32 offset:33792\n" \
"v_mfma_f32_16x16x32_bf16 a[244:247], v[160:163], v[112:115], a[244:247]\n" \
"s_add_u32 m0, %[mA], 0x1800\n" \
"s_add_u32 s85, s62, 0xd800\n" \
"v_mfma_f32_16x16x32_bf16 a[248:251], v[168:171], v[112:115], a[248:251]\n" \
"buffer_load_dwordx4 v24, s[76:79], s85 offen lds\n" \
"v_mfma_f32_16x16x32_bf16 a[248:251], v[164:167], v[108:111], a[248:251]\n" \
"ds_read_b64_tr_b16 v[156:157], v33 offset:32768\n" \
"ds_read_b64_tr_b16 v[158:159], v33 offset:33792\n" \
"v_mfma_f32_16x16x32_bf16 a[252:255], v[172:175], v[108:111], a[252:255]\n" \
"ds_read_b64_tr_b16 v[160:161], v33 offset:40960\n" \
"ds_read_b64_tr_b16 v[162:163], v33 offset:41984\n" \
"v_mfma_f32_16x16x32_bf16 a[252:255], v[176:179], v[112:115], a[252:255]\n" \
"s_add_u32 m0, %[mA], 0x1c00\n" \
"s_waitcnt lgkmcnt(11)\n" \
"s_nop 0\n" \
"v_smfmac_f32_16x16x64_bf16 a[16:19], v[140:143], v[84:91], v50\n" \
"v_mfma_f32_16x16x32_bf16 a[48:51], v[128:131], v[88:91], a[48:51]\n" \
"s_add_u32 s85, s62, 0xfc00\n" \
"ds_read_b64_tr_b16 v[168:169], v34 offset:40960\n" \
"v_mfma_f32_16x16x32_bf16 a[48:51], v[124:127], v[84:87], a[48:51]\n" \
"buffer_load_dwordx4 v25, s[76:79], s85 offen lds\n" \
"v_mfma_f32_16x16x32_bf16 a[80:83], v[84:87], v[132:135], a[80:83]\n" \
"s_add_u32 m0, %[mB], 0xc00\n" \
"s_add_u32 s84, s63, 0x18000\n" \
"v_mfma_f32_16x16x32_bf16 a[80:83], v[88:91], v[136:139], a[80:83]\n" \
"ds_read_b64_tr_b16 v[170:171], v34 offset:41984\n" \
"ds_read_b64_tr_b16 v[164:165], v34 offset:32768\n" \
"v_smfmac_f32_16x16x64_bf16 a[112:115], v[140:143], v[52:59], v50\n" \
"v_mfma_f32_16x16x32_bf16 a[20:23], v[128:131], v[56:59], a[20:23]\n" \
"buffer_load_dwordx4 v27, s[68:71], s84 offen lds\n" \
"v_mfma_f32_16x16x32_bf16 a[20:23], v[124:127], v[52:55], a[20:23]\n" \
"s_add_u32 s88, s42, 0x2000\n" \
"ds_read_b64_tr_b16 v[166:167], v34 offset:33792\n" \
"v_mfma_f32_16x16x32_bf16 a[52:55], v[116:119], v[52:55], a[52:55]\n" \
"ds_read_b64_tr_b16 v[172:173], v35 offset:32768\n" \
"ds_read_b64_tr_b16 v[174:175], v35 offset:33792\n" \
"v_mfma_f32_16x16x32_bf16 a[52:55], v[120:123], v[56:59], a[52:55]\n" \
"buffer_store_dwordx4 v[224:227], v20, s[80:83], s88 offen offset:0 sc1\n" \
"v_mfma_f32_16x16x32_bf16 a[84:87], v[56:59], v[136:139], a[84:87]\n" \
"ds_read_b64_tr_b16 v[176:177], v35 offset:40960\n" \
"ds_read_b64_tr_b16 v[178:179], v35 offset:41984\n" \
"v_mfma_f32_16x16x32_bf16 a[84:87], v[52:55], v[132:135], a[84:87]\n" \
"ds_read_b128 v[84:87], v48 offset:32768\n" \
"ds_read_b128 v[88:91], v49 offset:32768\n" \
"v_smfmac_f32_16x16x64_bf16 a[116:119], v[140:143], v[60:67], v50\n" \
"v_mfma_f32_16x16x32_bf16 a[24:27], v[124:127], v[60:63], a[24:27]\n" \
"ds_read_b128 v[104:107], v49 offset:36864\n" \
"ds_read_b128 v[108:111], v48 offset:38912\n" \
"v_mfma_f32_16x16x32_bf16 a[24:27], v[128:131], v[64:67], a[24:27]\n" \
"s_add_u32 s89, s42, 0x3000\n" \
"ds_read_b128 v[112:115], v49 offset:38912\n" \
"v_mfma_f32_16x16x32_bf16 a[56:59], v[120:123], v[64:67], a[56:59]\n" \
"buffer_store_dwordx4 v[228:231], v20, s[80:83], s89 offen offset:0 sc1\n" \
"v_mfma_f32_16x16x32_bf16 a[56:59], v[116:119], v[60:63], a[56:59]\n" \
"s_add_u32 s62, s62, 0x80\n" \
"s_lshr_b32 s96, %[b], 3\n" \
"v_mfma_f32_16x16x32_bf16 a[88:91], v[60:63], v[132:135], a[88:91]\n" \
"s_cmp_lt_u32 s62, 0x480\n" \
"s_cselect_b32 s62, s62, 0\n" \
"v_mfma_f32_16x16x32_bf16 a[88:91], v[64:67], v[136:139], a[88:91]\n" \
"s_lshl_b32 s72, s62, 6\n" \
"s_add_u32 s72, s72, s37\n" \
"v_smfmac_f32_16x16x64_bf16 a[120:123], v[140:143], v[68:75], v50\n" \
"v_mfma_f32_16x16x32_bf16 a[28:31], v[128:131], v[72:75], a[28:31]\n" \
"s_lshl_b32 s63, s62, 12\n" \
"s_and_b32 s97, 3, s96\n" \
"v_mfma_f32_16x16x32_bf16 a[28:31], v[124:127], v[68:71], a[28:31]\n" \
"s_and_b32 s94, 7, %[b]\n" \
"s_add_u32 s98, 1, s97\n" \
"v_mfma_f32_16x16x32_bf16 a[60:63], v[116:119], v[68:71], a[60:63]\n" \
"s_add_u32 s88, s42, 0x0\n" \
"s_lshl_b32 s95, s94, 2\n" \
"v_mfma_f32_16x16x32_bf16 a[60:63], v[120:123], v[72:75], a[60:63]\n" \
"buffer_store_dwordx4 v[232:235], v20, s[80:83], s88 offen offset:1024 sc1\n" \
"v_mfma_f32_16x16x32_bf16 a[92:95], v[72:75], v[136:139], a[92:95]\n" \
"s_and_b32 s99, 3, s98\n" \
"s_add_u32 s100, s95, s99\n" \
"v_mfma_f32_16x16x32_bf16 a[92:95], v[68:71], v[132:135], a[92:95]\n" \
"s_mul_i32 s101, 0x24000, s100\n" \
"s_mul_i32 s64, 0x9000, %[w]\n" \
"v_smfmac_f32_16x16x64_bf16 a[124:127], v[140:143], v[76:83], v50\n" \
"v_mfma_f32_16x16x32_bf16 a[0:3], v[124:127], v[76:79], a[0:3]\n" \
"s_and_b32 s96, 2, %[w]\n" \
"s_add_u32 s65, s101, s64\n" \
"v_mfma_f32_16x16x32_bf16 a[0:3], v[128:131], v[80:83], a[0:3]\n" \
"s_lshr_b32 s66, %[b], 5\n" \
"s_lshl_b32 s98, s96, 1\n" \
"v_mfma_f32_16x16x32_bf16 a[4:7], v[120:123], v[80:83], a[4:7]\n" \
"s_and_b32 s99, 1, %[w]\n" \
"v_writelane_b32 v11, s65, 23\n" \
"v_mfma_f32_16x16x32_bf16 a[4:7], v[116:119], v[76:79], a[4:7]\n" \
"s_lshl_b32 s67, s66, 3\n" \
"s_add_u32 s101, s98, s99\n" \
"v_mfma_f32_16x16x32_bf16 a[32:35], v[76:79], v[132:135], a[32:35]\n" \
"s_add_u32 s65, s67, s101\n" \
"s_mul_i32 s66, 0x24800, s65\n" \
"v_mfma_f32_16x16x32_bf16 a[32:35], v[80:83], v[136:139], a[32:35]\n" \
"s_lshl_b32 s98, s100, 12\n" \
"s_waitcnt lgkmcnt(0)\n" \
"v_mfma_f32_16x16x32_bf16 a[128:131], v[152:155], v[56:59], a[128:131]\n" \
"s_add_u32 s96, 0x3000000, s66\n" \
"s_add_u32 s99, 0x4800, s98\n" \
"v_mfma_f32_16x16x32_bf16 a[128:131], v[148:151], v[52:55], a[128:131]\n" \
"s_add_u32 s65, s96, s99\n" \
"s_sub_u32 s100, 0x78, s67\n" \
"v_mfma_f32_16x16x32_bf16 a[132:135], v[156:159], v[52:55], a[132:135]\n" \
"v_writelane_b32 v11, s65, 26\n" \
"s_add_u32 s98, s100, s101\n" \
"v_mfma_f32_16x16x32_bf16 a[132:135], v[160:163], v[56:59], a[132:135]\n" \
"s_mul_i32 s65, 0x24800, s98\n" \
"s_add_u32 s67, 0x3000000, s65\n" \
"v_mfma_f32_16x16x32_bf16 a[136:139], v[168:171], v[56:59], a[136:139]\n" \
"s_add_u32 s101, 2, s97\n" \
"s_add_u32 s100, s67, s99\n" \
"v_mfma_f32_16x16x32_bf16 a[136:139], v[164:167], v[52:55], a[136:139]\n" \
"s_and_b32 s98, 3, s101\n" \
"v_writelane_b32 v11, s100, 29\n" \
"v_mfma_f32_16x16x32_bf16 a[140:143], v[172:175], v[52:55], a[140:143]\n" \
"s_add_u32 s99, s95, s98\n" \
"s_mul_i32 s100, 0x24000, s99\n" \
"v_mfma_f32_16x16x32_bf16 a[140:143], v[176:179], v[56:59], a[140:143]\n" \
"s_add_u32 s101, s100, s64\n" \
"s_lshl_b32 s98, s99, 12\n" \
"v_mfma_f32_16x16x32_bf16 a[160:163], v[152:155], v[64:67], a[160:163]\n" \
"v_writelane_b32 v11, s101, 24\n" \
"s_add_u32 s100, 0x4800, s98\n" \
"v_mfma_f32_16x16x32_bf16 a[160:163], v[148:151], v[60:63], a[160:163]\n" \
"s_add_u32 s101, s96, s100\n" \
"v_writelane_b32 v11, s101, 27\n" \
"v_mfma_f32_16x16x32_bf16 a[164:167], v[156:159], v[60:63], a[164:167]\n" \
"s_add_u32 s98, 3, s97\n" \
"s_add_u32 s99, s67, s100\n" \
"v_mfma_f32_16x16x32_bf16 a[164:167], v[160:163], v[64:67], a[164:167]\n" \
"s_and_b32 s101, 3, s98\n" \
"v_writelane_b32 v11, s99, 30\n" \
"v_mfma_f32_16x16x32_bf16 a[168:171], v[168:171], v[64:67], a[168:171]\n" \
"s_add_u32 s100, s95, s101\n" \
"s_mul_i32 s99, 0x24000, s100\n" \
"v_mfma_f32_16x16x32_bf16 a[168:171], v[164:167], v[60:63], a[168:171]\n" \
"s_lshl_b32 s98, s100, 12\n" \
"s_add_u32 s97, s99, s64\n" \
"v_mfma_f32_16x16x32_bf16 a[172:175], v[172:175], v[60:63], a[172:175]\n" \
"s_add_u32 s95, 0x4800, s98\n" \
"v_writelane_b32 v11, s97, 25\n" \
"v_mfma_f32_16x16x32_bf16 a[172:175], v[176:179], v[64:67], a[172:175]\n" \
"s_add_u32 s101, s96, s95\n" \
"v_writelane_b32 v11, s101, 28\n" \
"v_mfma_f32_16x16x32_bf16 a[192:195], v[152:155], v[72:75], a[192:195]\n" \
"s_add_u32 s99, s67, s95\n" \
"s_lshl_b32 s64, s94, 11\n" \
"v_mfma_f32_16x16x32_bf16 a[192:195], v[148:151], v[68:71], a[192:195]\n" \
"v_writelane_b32 v11, s99, 31\n" \
"s_add_u32 s97, s96, s64\n" \
"s_waitcnt vmcnt(3)\n" \
"s_barrier\n" \
"v_mfma_f32_16x16x32_bf16 a[196:199], v[156:159], v[68:71], a[196:199]\n" \
"s_add_u32 m0, s40, 0x8000\n" \
"s_add_u32 s85, s72, 0x0\n" \
"v_mfma_f32_16x16x32_bf16 a[196:199], v[160:163], v[72:75], a[196:199]\n" \
"buffer_load_dwordx4 v20, s[80:83], s85 offen sc1 lds\n" \
"v_mfma_f32_16x16x32_bf16 a[200:203], v[168:171], v[72:75], a[200:203]\n" \
"s_add_u32 m0, s40, 0x9000\n" \
"s_add_u32 s84, s72, 0x1000\n" \
"v_mfma_f32_16x16x32_bf16 a[200:203], v[164:167], v[68:71], a[200:203]\n" \
"ds_read_b64_tr_b16 v[140:141], v39 offset:0\n" \
"ds_read_b64_tr_b16 v[142:143], v39 offset:960\n" \
"v_mfma_f32_16x16x32_bf16 a[204:207], v[172:175], v[68:71], a[204:207]\n" \
"buffer_load_dwordx4 v20, s[80:83], s84 offen sc1 lds\n" \
"v_mfma_f32_16x16x32_bf16 a[204:207], v[176:179], v[72:75], a[204:207]\n" \
"ds_read_b64_tr_b16 v[128:129], v37 offset:4096\n" \
"ds_read_b64_tr_b16 v[130:131], v37 offset:4608\n" \
"v_mfma_f32_16x16x32_bf16 a[224:227], v[152:155], v[80:83], a[224:227]\n" \
"s_add_u32 m0, s40, 0xb000\n" \
"s_add_u32 s85, s62, 0x0\n" \
"v_mfma_f32_16x16x32_bf16 a[224:227], v[148:151], v[76:79], a[224:227]\n" \
"buffer_load_dwordx4 v21, s[76:79], s85 offen lds\n" \
"v_mfma_f32_16x16x32_bf16 a[228:231], v[156:159], v[76:79], a[228:231]\n" \
"ds_read_b64_tr_b16 v[124:125], v37 offset:0\n" \
"ds_read_b64_tr_b16 v[126:127], v37 offset:512\n" \
"v_mfma_f32_16x16x32_bf16 a[228:231], v[160:163], v[80:83], a[228:231]\n" \
"s_add_u32 m0, %[mA], 0x8000\n" \
"s_add_u32 s84, s62, 0x0\n" \
"v_mfma_f32_16x16x32_bf16 a[232:235], v[168:171], v[80:83], a[232:235]\n" \
"buffer_load_dwordx4 v24, s[76:79], s84 offen lds\n" \
"v_mfma_f32_16x16x32_bf16 a[232:235], v[164:167], v[76:79], a[232:235]\n" \
"ds_read_b64_tr_b16 v[132:133], v38 offset:0\n" \
"ds_read_b64_tr_b16 v[134:135], v38 offset:512\n" \
"v_mfma_f32_16x16x32_bf16 a[236:239], v[172:175], v[76:79], a[236:239]\n" \
"ds_read_b64_tr_b16 v[136:137], v38 offset:4096\n" \
"ds_read_b64_tr_b16 v[138:139], v38 offset:4608\n" \
"v_mfma_f32_16x16x32_bf16 a[236:239], v[176:179], v[80:83], a[236:239]\n" \
"s_add_u32 m0, %[mA], 0x8400\n" \
"s_add_u32 s85, s62, 0x2400\n" \
"v_mfma_f32_16x16x32_bf16 a[144:147], v[152:155], v[88:91], a[144:147]\n" \
"buffer_load_dwordx4 v25, s[76:79], s85 offen lds\n" \
"v_mfma_f32_16x16x32_bf16 a[144:147], v[148:151], v[84:87], a[144:147]\n" \
"ds_read_b128 v[52:55], v28 offset:0\n" \
"ds_read_b128 v[56:59], v29 offset:0\n" \
"v_mfma_f32_16x16x32_bf16 a[148:151], v[156:159], v[84:87], a[148:151]\n" \
"s_add_u32 m0, %[mB], 0x8000\n" \
"s_add_u32 s84, s63, 0x0\n" \
"s_nop 0\n" \
"v_mfma_f32_16x16x32_bf16 a[148:151], v[160:163], v[88:91], a[148:151]\n" \
"buffer_load_dwordx4 v26, s[68:71], s84 offen lds\n" \
"v_mfma_f32_16x16x32_bf16 a[152:155], v[168:171], v[88:91], a[152:155]\n" \
"ds_read_b64_tr_b16 v[116:117], v36 offset:0\n" \
"ds_read_b64_tr_b16 v[118:119], v36 offset:512\n" \
"v_mfma_f32_16x16x32_bf16 a[152:155], v[164:167], v[84:87], a[152:155]\n" \
"ds_read_b64_tr_b16 v[120:121], v36 offset:4096\n" \
"ds_read_b64_tr_b16 v[122:123], v36 offset:4608\n" \
"v_mfma_f32_16x16x32_bf16 a[156:159], v[172:175], v[84:87], a[156:159]\n" \
"s_add_u32 m0, %[mA], 0x8800\n" \
"s_add_u32 s85, s62, 0x4800\n" \
"v_mfma_f32_16x16x32_bf16 a[156:159], v[176:179], v[88:91], a[156:159]\n" \
"buffer_load_dwordx4 v24, s[76:79], s85 offen lds\n" \
"v_mfma_f32_16x16x32_bf16 a[176:179], v[152:155], v[96:99], a[176:179]\n" \
"ds_read_b128 v[84:87], v22 offset:0\n" \
"ds_read_b128 v[88:91], v23 offset:0\n" \
"v_mfma_f32_16x16x32_bf16 a[176:179], v[148:151], v[92:95], a[176:179]\n" \
"s_add_u32 m0, %[mA], 0x8c00\n" \
"s_add_u32 s84, s62, 0x6c00\n" \
"v_mfma_f32_16x16x32_bf16 a[180:183], v[156:159], v[92:95], a[180:183]\n" \
"buffer_load_dwordx4 v25, s[76:79], s84 offen lds\n" \
"v_mfma_f32_16x16x32_bf16 a[180:183], v[160:163], v[96:99], a[180:183]\n" \
"ds_read_b128 v[60:63], v28 offset:2048\n" \
"ds_read_b128 v[64:67], v29 offset:2048\n" \
"v_mfma_f32_16x16x32_bf16 a[184:187], v[168:171], v[96:99], a[184:187]\n" \
"ds_read_b128 v[68:71], v28 offset:4096\n" \
"ds_read_b128 v[72:75], v29 offset:4096\n" \
"v_mfma_f32_16x16x32_bf16 a[184:187], v[164:167], v[92:95], a[184:187]\n" \
"s_add_u32 m0, %[mB], 0x8400\n" \
"s_add_u32 s85, s63, 0x8000\n" \
"v_mfma_f32_16x16x32_bf16 a[188:191], v[172:175], v[92:95], a[188:191]\n" \
"buffer_load_dwordx4 v26, s[68:71], s85 offen lds\n" \
"v_mfma_f32_16x16x32_bf16 a[188:191], v[176:179], v[96:99], a[188:191]\n" \
"ds_read_b128 v[76:79], v28 offset:6144\n" \
"ds_read_b128 v[80:83], v29 offset:6144\n" \
"v_mfma_f32_16x16x32_bf16 a[208:211], v[152:155], v[104:107], a[208:211]\n" \
"s_add_u32 m0, %[mA], 0x9000\n" \
"s_add_u32 s84, s62, 0x9000\n" \
"v_mfma_f32_16x16x32_bf16 a[208:211], v[148:151], v[100:103], a[208:211]\n" \
"buffer_load_dwordx4 v24, s[76:79], s84 offen lds\n" \
"v_mfma_f32_16x16x32_bf16 a[212:215], v[156:159], v[100:103], a[212:215]\n" \
"ds_read_b128 v[92:95], v48 offset:2048\n" \
"ds_read_b128 v[96:99], v49 offset:2048\n" \
"v_mfma_f32_16x16x32_bf16 a[212:215], v[160:163], v[104:107], a[212:215]\n" \
"v_writelane_b32 v11, s97, 32\n" \
"s_and_b32 s99, 1, s94\n" \
"v_mfma_f32_16x16x32_bf16 a[216:219], v[168:171], v[104:107], a[216:219]\n" \
"s_add_u32 m0, %[mA], 0x9400\n" \
"s_add_u32 s85, s62, 0xb400\n" \
"v_mfma_f32_16x16x32_bf16 a[216:219], v[164:167], v[100:103], a[216:219]\n" \
"buffer_load_dwordx4 v25, s[76:79], s85 offen lds\n" \
"v_mfma_f32_16x16x32_bf16 a[220:223], v[172:175], v[100:103], a[220:223]\n" \
"ds_read_b128 v[100:103], v48 offset:4096\n" \
"s_add_u32 s100, s67, s64\n" \
"v_mfma_f32_16x16x32_bf16 a[220:223], v[176:179], v[104:107], a[220:223]\n" \
"s_add_u32 m0, %[mB], 0x8800\n" \
"s_add_u32 s84, s63, 0x10000\n" \
"v_mfma_f32_16x16x32_bf16 a[240:243], v[152:155], v[112:115], a[240:243]\n" \
"buffer_load_dwordx4 v27, s[68:71], s84 offen lds\n" \
"v_mfma_f32_16x16x32_bf16 a[240:243], v[148:151], v[108:111], a[240:243]\n" \
"ds_read_b64_tr_b16 v[152:153], v32 offset:8192\n" \
"ds_read_b64_tr_b16 v[154:155], v32 offset:9216\n" \
"v_mfma_f32_16x16x32_bf16 a[244:247], v[156:159], v[108:111], a[244:247]\n" \
"ds_read_b64_tr_b16 v[148:149], v32 offset:0\n" \
"ds_read_b64_tr_b16 v[150:151], v32 offset:1024\n" \
"v_mfma_f32_16x16x32_bf16 a[244:247], v[160:163], v[112:115], a[244:247]\n" \
"s_add_u32 m0, %[mA], 0x9800\n" \
"s_add_u32 s85, s62, 0xd800\n" \
"v_mfma_f32_16x16x32_bf16 a[248:251], v[168:171], v[112:115], a[248:251]\n" \
"buffer_load_dwordx4 v24, s[76:79], s85 offen lds\n" \
"v_mfma_f32_16x16x32_bf16 a[248:251], v[164:167], v[108:111], a[248:251]\n" \
"ds_read_b64_tr_b16 v[156:157], v33 offset:0\n" \
"ds_read_b64_tr_b16 v[158:159], v33 offset:1024\n" \
"v_mfma_f32_16x16x32_bf16 a[252:255], v[172:175], v[108:111], a[252:255]\n" \
"ds_read_b64_tr_b16 v[160:161], v33 offset:8192\n" \
"ds_read_b64_tr_b16 v[162:163], v33 offset:9216\n" \
"v_mfma_f32_16x16x32_bf16 a[252:255], v[176:179], v[112:115], a[252:255]\n" \
"s_add_u32 m0, %[mA], 0x9c00\n" \
"s_waitcnt lgkmcnt(11)\n" \
"s_nop 0\n" \
"v_smfmac_f32_16x16x64_bf16 a[16:19], v[140:143], v[84:91], v50\n" \
"v_mfma_f32_16x16x32_bf16 a[48:51], v[128:131], v[88:91], a[48:51]\n" \
"s_add_u32 s85, s62, 0xfc00\n" \
"ds_read_b64_tr_b16 v[168:169], v34 offset:8192\n" \
"v_mfma_f32_16x16x32_bf16 a[48:51], v[124:127], v[84:87], a[48:51]\n" \
"buffer_load_dwordx4 v25, s[76:79], s85 offen lds\n" \
"v_mfma_f32_16x16x32_bf16 a[80:83], v[84:87], v[132:135], a[80:83]\n" \
"s_add_u32 m0, %[mB], 0x8c00\n" \
"s_add_u32 s84, s63, 0x18000\n" \
"v_mfma_f32_16x16x32_bf16 a[80:83], v[88:91], v[136:139], a[80:83]\n" \
"ds_read_b64_tr_b16 v[170:171], v34 offset:9216\n" \
"ds_read_b64_tr_b16 v[164:165], v34 offset:0\n" \
"v_smfmac_f32_16x16x64_bf16 a[112:115], v[140:143], v[52:59], v50\n" \
"v_mfma_f32_16x16x32_bf16 a[20:23], v[128:131], v[56:59], a[20:23]\n" \
"buffer_load_dwordx4 v27, s[68:71], s84 offen lds\n" \
"v_mfma_f32_16x16x32_bf16 a[20:23], v[124:127], v[52:55], a[20:23]\n" \
"s_add_u32 s89, s42, 0x1000\n" \
"ds_read_b64_tr_b16 v[166:167], v34 offset:1024\n" \
"v_mfma_f32_16x16x32_bf16 a[52:55], v[116:119], v[52:55], a[52:55]\n" \
"ds_read_b64_tr_b16 v[172:173], v35 offset:0\n" \
"ds_read_b64_tr_b16 v[174:175], v35 offset:1024\n" \
"v_mfma_f32_16x16x32_bf16 a[52:55], v[120:123], v[56:59], a[52:55]\n" \
"buffer_store_dwordx4 v[236:239], v20, s[80:83], s89 offen offset:1024 sc1\n" \
"v_mfma_f32_16x16x32_bf16 a[84:87], v[56:59], v[136:139], a[84:87]\n" \
"ds_read_b64_tr_b16 v[176:177], v35 offset:8192\n" \
"ds_read_b64_tr_b16 v[178:179], v35 offset:9216\n" \
"v_mfma_f32_16x16x32_bf16 a[84:87], v[52:55], v[132:135], a[84:87]\n" \
"ds_read_b128 v[84:87], v48 offset:0\n" \
"ds_read_b128 v[88:91], v49 offset:0\n" \
"v_smfmac_f32_16x16x64_bf16 a[116:119], v[140:143], v[60:67], v50\n" \
"v_mfma_f32_16x16x32_bf16 a[24:27], v[124:127], v[60:63], a[24:27]\n" \
"ds_read_b128 v[104:107], v49 offset:4096\n" \
"ds_read_b128 v[108:111], v48 offset:6144\n" \
"v_mfma_f32_16x16x32_bf16 a[24:27], v[128:131], v[64:67], a[24:27]\n" \
"s_add_u32 s88, s42, 0x2000\n" \
"ds_read_b128 v[112:115], v49 offset:6144\n" \
"v_mfma_f32_16x16x32_bf16 a[56:59], v[120:123], v[64:67], a[56:59]\n" \
"buffer_store_dwordx4 v[240:243], v20, s[80:83], s88 offen offset:1024 sc1\n" \
"v_mfma_f32_16x16x32_bf16 a[56:59], v[116:119], v[60:63], a[56:59]\n" \
"s_add_u32 s62, s62, 0x80\n" \
"v_writelane_b32 v11, s100, 33\n" \
"v_mfma_f32_16x16x32_bf16 a[88:91], v[60:63], v[132:135], a[88:91]\n" \
"s_cmp_lt_u32 s62, 0x480\n" \
"s_cselect_b32 s62, s62, 0\n" \
"v_mfma_f32_16x16x32_bf16 a[88:91], v[64:67], v[136:139], a[88:91]\n" \
"s_lshl_b32 s72, s62, 6\n" \
"s_add_u32 s72, s72, s37\n" \
"v_smfmac_f32_16x16x64_bf16 a[120:123], v[140:143], v[68:75], v50\n" \
"v_mfma_f32_16x16x32_bf16 a[28:31], v[128:131], v[72:75], a[28:31]\n" \
"s_lshl_b32 s63, s62, 12\n" \
"s_add_u32 s98, 0x3004000, s66\n" \
"v_mfma_f32_16x16x32_bf16 a[28:31], v[124:127], v[68:71], a[28:31]\n" \
"s_cmp_lg_u32 s99, 0\n" \
"s_cselect_b32 s97, s67, s96\n" \
"v_mfma_f32_16x16x32_bf16 a[60:63], v[116:119], v[68:71], a[60:63]\n" \
"s_add_u32 s89, s42, 0x3000\n" \
"s_lshr_b32 s95, s94, 1\n" \
"v_mfma_f32_16x16x32_bf16 a[60:63], v[120:123], v[72:75], a[60:63]\n" \
"buffer_store_dwordx4 v[244:247], v20, s[80:83], s89 offen offset:1024 sc1\n" \
"v_mfma_f32_16x16x32_bf16 a[92:95], v[72:75], v[136:139], a[92:95]\n" \
"s_add_u32 s64, 0x4000, s97\n" \
"v_writelane_b32 v11, s98, 34\n" \
"v_mfma_f32_16x16x32_bf16 a[92:95], v[68:71], v[132:135], a[92:95]\n" \
"s_add_u32 s101, 0x3004000, s65\n" \
"s_and_b32 s96, 2, %[w]\n" \
"v_smfmac_f32_16x16x64_bf16 a[124:127], v[140:143], v[76:83], v50\n" \
"v_mfma_f32_16x16x32_bf16 a[0:3], v[124:127], v[76:79], a[0:3]\n" \
"s_cmp_eq_u32 %[w], s95\n" \
"s_cselect_b32 s100, s64, 0x40000000\n" \
"v_mfma_f32_16x16x32_bf16 a[0:3], v[128:131], v[80:83], a[0:3]\n" \
"v_writelane_b32 v11, s101, 35\n" \
"s_lshl_b32 s97, s96, 1\n" \
"v_mfma_f32_16x16x32_bf16 a[4:7], v[120:123], v[80:83], a[4:7]\n" \
"s_and_b32 s98, 1, %[w]\n" \
"v_writelane_b32 v11, s100, 36\n" \
"v_mfma_f32_16x16x32_bf16 a[4:7], v[116:119], v[76:79], a[4:7]\n" \
"s_lshr_b32 s94, %[b], 5\n" \
"s_add_u32 s99, s97, s98\n" \
"v_mfma_f32_16x16x32_bf16 a[32:35], v[76:79], v[132:135], a[32:35]\n" \
"s_lshl_b32 s95, s94, 7\n" \
"s_lshl_b32 s100, s99, 4\n" \
"v_mfma_f32_16x16x32_bf16 a[32:35], v[80:83], v[136:139], a[32:35]\n" \
"s_add_u32 s101, s95, s100\n" \
"s_waitcnt lgkmcnt(0)\n" \
"v_mfma_f32_16x16x32_bf16 a[128:131], v[152:155], v[56:59], a[128:131]\n" \
"s_mul_i32 s64, 0x480, s101\n" \
"s_lshr_b32 s65, %[b], 3\n" \
"v_mfma_f32_16x16x32_bf16 a[128:131], v[148:151], v[52:55], a[128:131]\n" \
"s_sub_u32 s99, 0x780, s95\n" \
"s_and_b32 s66, 3, s65\n" \
"v_mfma_f32_16x16x32_bf16 a[132:135], v[156:159], v[52:55], a[132:135]\n" \
"s_add_u32 s101, s99, s100\n" \
"s_mul_i32 s67, 0x120, s66\n" \
"v_mfma_f32_16x16x32_bf16 a[132:135], v[160:163], v[56:59], a[132:135]\n" \
"s_and_b32 s96, 7, %[b]\n" \
"s_mul_i32 s65, 0x480, s101\n" \
"v_mfma_f32_16x16x32_bf16 a[136:139], v[168:171], v[56:59], a[136:139]\n" \
"s_add_u32 s94, s64, s67\n" \
"s_lshl_b32 s97, s96, 5\n" \
"v_mfma_f32_16x16x32_bf16 a[136:139], v[164:167], v[52:55], a[136:139]\n" \
"s_add_u32 s66, s65, s67\n" \
"s_and_b32 s95, 1, s96\n" \
"v_mfma_f32_16x16x32_bf16 a[140:143], v[172:175], v[52:55], a[140:143]\n" \
"s_add_u32 s98, s94, s97\n" \
"v_writelane_b32 v11, s98, 39\n" \
"v_mfma_f32_16x16x32_bf16 a[140:143], v[176:179], v[56:59], a[140:143]\n" \
"s_cmp_lg_u32 s95, 0\n" \
"s_cselect_b32 s99, s66, s94\n" \
"v_mfma_f32_16x16x32_bf16 a[160:163], v[152:155], v[64:67], a[160:163]\n" \
"s_lshr_b32 s98, s96, 1\n" \
"s_add_u32 s100, 0x100, s99\n" \
"v_mfma_f32_16x16x32_bf16 a[160:163], v[148:151], v[60:63], a[160:163]\n" \
"s_add_u32 s64, s66, s97\n" \
"s_mov_b32 s94, s67\n" \
"v_mfma_f32_16x16x32_bf16 a[164:167], v[156:159], v[60:63], a[164:167]\n" \
"s_cmp_eq_u32 %[w], s98\n" \
"s_cselect_b32 s101, s100, 0x40000000\n" \
"v_mfma_f32_16x16x32_bf16 a[164:167], v[160:163], v[64:67], a[164:167]\n" \
"v_writelane_b32 v11, s64, 40\n" \
"v_writelane_b32 v11, s101, 41\n" \
"v_mfma_f32_16x16x32_bf16 a[168:171], v[168:171], v[64:67], a[168:171]\n" \
"s_add_u32 s96, s16, s94\n" \
"s_addc_u32 s97, s17, 0\n" \
"v_mfma_f32_16x16x32_bf16 a[168:171], v[164:167], v[60:63], a[168:171]\n" \
"s_add_u32 s98, s18, s94\n" \
"s_addc_u32 s99, s19, 0\n" \
"v_mfma_f32_16x16x32_bf16 a[172:175], v[172:175], v[60:63], a[172:175]\n" \
"s_and_b32 s97, s97, 0xffff\n" \
"v_writelane_b32 v11, s96, 7\n" \
"v_mfma_f32_16x16x32_bf16 a[172:175], v[176:179], v[64:67], a[172:175]\n" \
"v_writelane_b32 v11, s97, 8\n" \
"s_and_b32 s99, s99, 0xffff\n" \
"v_mfma_f32_16x16x32_bf16 a[192:195], v[152:155], v[72:75], a[192:195]\n" \
"v_writelane_b32 v11, s98, 9\n" \
"v_writelane_b32 v11, s99, 10\n" \
"v_mfma_f32_16x16x32_bf16 a[192:195], v[148:151], v[68:71], a[192:195]\n" \
"s_cmp_lt_u32 %[w], 3\n" \
"s_cselect_b64 s[96:97], s[96:97], s[98:99]\n" \
"s_waitcnt vmcnt(3)\n" \
"s_barrier\n" \
"v_mfma_f32_16x16x32_bf16 a[196:199], v[156:159], v[68:71], a[196:199]\n" \
"s_add_u32 m0, s40, 0x0\n" \
"s_add_u32 s85, s72, 0x0\n" \
"v_mfma_f32_16x16x32_bf16 a[196:199], v[160:163], v[72:75], a[196:199]\n" \
"buffer_load_dwordx4 v20, s[80:83], s85 offen sc1 lds\n" \
"v_mfma_f32_16x16x32_bf16 a[200:203], v[168:171], v[72:75], a[200:203]\n" \
"s_add_u32 m0, s40, 0x1000\n" \
"s_add_u32 s84, s72, 0x1000\n" \
"v_mfma_f32_16x16x32_bf16 a[200:203], v[164:167], v[68:71], a[200:203]\n" \
"ds_read_b64_tr_b16 v[140:141], v39 offset:32768\n" \
"ds_read_b64_tr_b16 v[142:143], v39 offset:33728\n" \
"v_mfma_f32_16x16x32_bf16 a[204:207], v[172:175], v[68:71], a[204:207]\n" \
"buffer_load_dwordx4 v20, s[80:83], s84 offen sc1 lds\n" \
"v_mfma_f32_16x16x32_bf16 a[204:207], v[176:179], v[72:75], a[204:207]\n" \
"ds_read_b64_tr_b16 v[128:129], v37 offset:36864\n" \
"ds_read_b64_tr_b16 v[130:131], v37 offset:37376\n" \
"v_mfma_f32_16x16x32_bf16 a[224:227], v[152:155], v[80:83], a[224:227]\n" \
"s_add_u32 m0, s40, 0x3000\n" \
"s_add_u32 s85, s62, 0x0\n" \
"v_mfma_f32_16x16x32_bf16 a[224:227], v[148:151], v[76:79], a[224:227]\n" \
"buffer_load_dwordx4 v21, s[76:79], s85 offen lds\n" \
"v_mfma_f32_16x16x32_bf16 a[228:231], v[156:159], v[76:79], a[228:231]\n" \
"ds_read_b64_tr_b16 v[124:125], v37 offset:32768\n" \
"ds_read_b64_tr_b16 v[126:127], v37 offset:33280\n" \
"v_mfma_f32_16x16x32_bf16 a[228:231], v[160:163], v[80:83], a[228:231]\n" \
"s_add_u32 m0, %[mA], 0x0\n" \
"s_add_u32 s84, s62, 0x0\n" \
"s_nop 0\n" \
"v_mfma_f32_16x16x32_bf16 a[232:235], v[168:171], v[80:83], a[232:235]\n" \
"buffer_load_dwordx4 v24, s[76:79], s84 offen lds\n" \
"v_mfma_f32_16x16x32_bf16 a[232:235], v[164:167], v[76:79], a[232:235]\n" \
"ds_read_b64_tr_b16 v[132:133], v38 offset:32768\n" \
"ds_read_b64_tr_b16 v[134:135], v38 offset:33280\n" \
"v_mfma_f32_16x16x32_bf16 a[236:239], v[172:175], v[76:79], a[236:239]\n" \
"ds_read_b64_tr_b16 v[136:137], v38 offset:36864\n" \
"ds_read_b64_tr_b16 v[138:139], v38 offset:37376\n" \
"v_mfma_f32_16x16x32_bf16 a[236:239], v[176:179], v[80:83], a[236:239]\n" \
"s_add_u32 m0, %[mA], 0x400\n" \
"s_add_u32 s85, s62, 0x2400\n" \
"v_mfma_f32_16x16x32_bf16 a[144:147], v[152:155], v[88:91], a[144:147]\n" \
"buffer_load_dwordx4 v25, s[76:79], s85 offen lds\n" \
"v_mfma_f32_16x16x32_bf16 a[144:147], v[148:151], v[84:87], a[144:147]\n" \
"ds_read_b128 v[52:55], v28 offset:32768\n" \
"ds_read_b128 v[56:59], v29 offset:32768\n" \
"v_mfma_f32_16x16x32_bf16 a[148:151], v[156:159], v[84:87], a[148:151]\n" \
"s_add_u32 m0, %[mB], 0x0\n" \
"s_add_u32 s84, s63, 0x0\n" \
"v_mfma_f32_16x16x32_bf16 a[148:151], v[160:163], v[88:91], a[148:151]\n" \
"buffer_load_dwordx4 v26, s[68:71], s84 offen lds\n" \
"v_mfma_f32_16x16x32_bf16 a[152:155], v[168:171], v[88:91], a[152:155]\n" \
"ds_read_b64_tr_b16 v[116:117], v36 offset:32768\n" \
"ds_read_b64_tr_b16 v[118:119], v36 offset:33280\n" \
"L_pf7:\n" \
"s_cbranch_execz L_pf11\n" \
"s_nop 0\n" \
"v_mfma_f32_16x16x32_bf16 a[152:155], v[164:167], v[84:87], a[152:155]\n" \
"ds_read_b64_tr_b16 v[120:121], v36 offset:36864\n" \
"ds_read_b64_tr_b16 v[122:123], v36 offset:37376\n" \
"v_mfma_f32_16x16x32_bf16 a[156:159], v[172:175], v[84:87], a[156:159]\n" \
"s_add_u32 m0, %[mA], 0x800\n" \
"s_add_u32 s85, s62, 0x4800\n" \
"v_mfma_f32_16x16x32_bf16 a[156:159], v[176:179], v[88:91], a[156:159]\n" \
"buffer_load_dwordx4 v24, s[76:79], s85 offen lds\n" \
"v_mfma_f32_16x16x32_bf16 a[176:179], v[152:155], v[96:99], a[176:179]\n" \
"ds_read_b128 v[84:87], v22 offset:32768\n" \
"ds_read_b128 v[88:91], v23 offset:32768\n" \
"v_mfma_f32_16x16x32_bf16 a[176:179], v[148:151], v[92:95], a[176:179]\n" \
"s_add_u32 m0, %[mA], 0xc00\n" \
"s_add_u32 s84, s62, 0x6c00\n" \
"v_mfma_f32_16x16x32_bf16 a[180:183], v[156:159], v[92:95], a[180:183]\n" \
"buffer_load_dwordx4 v25, s[76:79], s84 offen lds\n" \
"v_mfma_f32_16x16x32_bf16 a[180:183], v[160:163], v[96:99], a[180:183]\n" \
"ds_read_b128 v[60:63], v28 offset:34816\n" \
"ds_read_b128 v[64:67], v29 offset:34816\n" \
"v_mfma_f32_16x16x32_bf16 a[184:187], v[168:171], v[96:99], a[184:187]\n" \
"ds_read_b128 v[68:71], v28 offset:36864\n" \
"ds_read_b128 v[72:75], v29 offset:36864\n" \
"v_mfma_f32_16x16x32_bf16 a[184:187], v[164:167], v[92:95], a[184:187]\n" \
"s_add_u32 m0, %[mB], 0x400\n" \
"s_add_u32 s85, s63, 0x8000\n" \
"v_mfma_f32_16x16x32_bf16 a[188:191], v[172:175], v[92:95], a[188:191]\n" \
"buffer_load_dwordx4 v26, s[68:71], s85 offen lds\n" \
"v_mfma_f32_16x16x32_bf16 a[188:191], v[176:179], v[96:99], a[188:191]\n" \
"ds_read_b128 v[76:79], v28 offset:38912\n" \
"ds_read_b128 v[80:83], v29 offset:38912\n" \
"v_mfma_f32_16x16x32_bf16 a[208:211], v[152:155], v[104:107], a[208:211]\n" \
"s_add_u32 m0, %[mA], 0x1000\n" \
"s_add_u32 s84, s62, 0x9000\n" \
"v_mfma_f32_16x16x32_bf16 a[208:211], v[148:151], v[100:103], a[208:211]\n" \
"buffer_load_dwordx4 v24, s[76:79], s84 offen lds\n" \
"v_mfma_f32_16x16x32_bf16 a[212:215], v[156:159], v[100:103], a[212:215]\n" \
"ds_read_b128 v[92:95], v48 offset:34816\n" \
"ds_read_b128 v[96:99], v49 offset:34816\n" \
"v_mfma_f32_16x16x32_bf16 a[212:215], v[160:163], v[104:107], a[212:215]\n" \
"v_writelane_b32 v11, s96, 13\n" \
"v_writelane_b32 v11, s97, 14\n" \
"v_mfma_f32_16x16x32_bf16 a[216:219], v[168:171], v[104:107], a[216:219]\n" \
"s_add_u32 m0, %[mA], 0x1400\n" \
"s_add_u32 s85, s62, 0xb400\n" \
"v_mfma_f32_16x16x32_bf16 a[216:219], v[164:167], v[100:103], a[216:219]\n" \
"buffer_load_dwordx4 v25, s[76:79], s85 offen lds\n" \
"v_mfma_f32_16x16x32_bf16 a[220:223], v[172:175], v[100:103], a[220:223]\n" \
"ds_read_b128 v[100:103], v48 offset:36864\n" \
"v_writelane_b32 v11, s20, 11\n" \
"v_mfma_f32_16x16x32_bf16 a[220:223], v[176:179], v[104:107], a[220:223]\n" \
"s_add_u32 m0, %[mB], 0x800\n" \
"s_add_u32 s84, s63, 0x10000\n" \
"v_mfma_f32_16x16x32_bf16 a[240:243], v[152:155], v[112:115], a[240:243]\n" \
"buffer_load_dwordx4 v27, s[68:71], s84 offen lds\n" \
"v_mfma_f32_16x16x32_bf16 a[240:243], v[148:151], v[108:111], a[240:243]\n" \
"ds_read_b64_tr_b16 v[152:153], v32 offset:40960\n" \
"ds_read_b64_tr_b16 v[154:155], v32 offset:41984\n" \
"v_mfma_f32_16x16x32_bf16 a[244:247], v[156:159], v[108:111], a[244:247]\n" \
"ds_read_b64_tr_b16 v[148:149], v32 offset:32768\n" \
"ds_read_b64_tr_b16 v[150:151], v32 offset:33792\n" \
"v_mfma_f32_16x16x32_bf16 a[244:247], v[160:163], v[112:115], a[244:247]\n" \
"s_add_u32 m0, %[mA], 0x1800\n" \
"s_add_u32 s85, s62, 0xd800\n" \
"v_mfma_f32_16x16x32_bf16 a[248:251], v[168:171], v[112:115], a[248:251]\n" \
"buffer_load_dwordx4 v24, s[76:79], s85 offen lds\n" \
"v_mfma_f32_16x16x32_bf16 a[248:251], v[164:167], v[108:111], a[248:251]\n" \
"ds_read_b64_tr_b16 v[156:157], v33 offset:32768\n" \
"ds_read_b64_tr_b16 v[158:159], v33 offset:33792\n" \
"v_mfma_f32_16x16x32_bf16 a[252:255], v[172:175], v[108:111], a[252:255]\n" \
"ds_read_b64_tr_b16 v[160:161], v33 offset:40960\n" \
"ds_read_b64_tr_b16 v[162:163], v33 offset:41984\n" \
"v_mfma_f32_16x16x32_bf16 a[252:255], v[176:179], v[112:115], a[252:255]\n" \
"s_add_u32 m0, %[mA], 0x1c00\n" \
"s_waitcnt lgkmcnt(11)\n" \
"s_nop 0\n" \
"v_smfmac_f32_16x16x64_bf16 a[16:19], v[140:143], v[84:91], v50\n" \
"v_mfma_f32_16x16x32_bf16 a[48:51], v[128:131], v[88:91], a[48:51]\n" \
"s_add_u32 s85, s62, 0xfc00\n" \
"ds_read_b64_tr_b16 v[168:169], v34 offset:40960\n" \
"v_mfma_f32_16x16x32_bf16 a[48:51], v[124:127], v[84:87], a[48:51]\n" \
"buffer_load_dwordx4 v25, s[76:79], s85 offen lds\n" \
"v_mfma_f32_16x16x32_bf16 a[80:83], v[84:87], v[132:135], a[80:83]\n" \
"s_add_u32 m0, %[mB], 0xc00\n" \
"s_add_u32 s84, s63, 0x18000\n" \
"v_mfma_f32_16x16x32_bf16 a[80:83], v[88:91], v[136:139], a[80:83]\n" \
"ds_read_b64_tr_b16 v[170:171], v34 offset:41984\n" \
"ds_read_b64_tr_b16 v[164:165], v34 offset:32768\n" \
"v_smfmac_f32_16x16x64_bf16 a[112:115], v[140:143], v[52:59], v50\n" \
"v_mfma_f32_16x16x32_bf16 a[20:23], v[128:131], v[56:59], a[20:23]\n" \
"buffer_load_dwordx4 v27, s[68:71], s84 offen lds\n" \
"v_mfma_f32_16x16x32_bf16 a[20:23], v[124:127], v[52:55], a[20:23]\n" \
"s_add_u32 s88, s43, 0x0\n" \
"s_nop 0\n" \
"ds_read_b64_tr_b16 v[166:167], v34 offset:33792\n" \
"v_mfma_f32_16x16x32_bf16 a[52:55], v[116:119], v[52:55], a[52:55]\n" \
"ds_read_b64_tr_b16 v[172:173], v35 offset:32768\n" \
"ds_read_b64_tr_b16 v[174:175], v35 offset:33792\n" \
"v_mfma_f32_16x16x32_bf16 a[52:55], v[120:123], v[56:59], a[52:55]\n" \
"buffer_store_dwordx4 v[248:251], v20, s[80:83], s88 offen offset:0 sc1\n" \
"v_mfma_f32_16x16x32_bf16 a[84:87], v[56:59], v[136:139], a[84:87]\n" \
"ds_read_b64_tr_b16 v[176:177], v35 offset:40960\n" \
"ds_read_b64_tr_b16 v[178:179], v35 offset:41984\n" \
"v_mfma_f32_16x16x32_bf16 a[84:87], v[52:55], v[132:135], a[84:87]\n" \
"ds_read_b128 v[84:87], v48 offset:32768\n" \
"ds_read_b128 v[88:91], v49 offset:32768\n" \
"v_smfmac_f32_16x16x64_bf16 a[116:119], v[140:143], v[60:67], v50\n" \
"v_mfma_f32_16x16x32_bf16 a[24:27], v[124:127], v[60:63], a[24:27]\n" \
"ds_read_b128 v[104:107], v49 offset:36864\n" \
"ds_read_b128 v[108:111], v48 offset:38912\n" \
"v_mfma_f32_16x16x32_bf16 a[24:27], v[128:131], v[64:67], a[24:27]\n" \
"s_add_u32 s89, s45, 0x1000\n" \
"ds_read_b128 v[112:115], v49 offset:38912\n" \
"v_mfma_f32_16x16x32_bf16 a[56:59], v[120:123], v[64:67], a[56:59]\n" \
"buffer_store_dwordx4 v[184:187], v20, s[80:83], s89 offen offset:2048 sc1\n" \
"v_mfma_f32_16x16x32_bf16 a[56:59], v[116:119], v[60:63], a[56:59]\n" \
"s_add_u32 s62, s62, 0x80\n" \
"s_and_b32 s95, s21, 0xffff\n" \
"v_mfma_f32_16x16x32_bf16 a[88:91], v[60:63], v[132:135], a[88:91]\n" \
"s_cmp_lt_u32 s62, 0x480\n" \
"s_cselect_b32 s62, s62, 0\n" \
"v_mfma_f32_16x16x32_bf16 a[88:91], v[64:67], v[136:139], a[88:91]\n" \
"s_lshl_b32 s72, s62, 6\n" \
"s_add_u32 s72, s72, s37\n" \
"v_smfmac_f32_16x16x64_bf16 a[120:123], v[140:143], v[68:75], v50\n" \
"v_mfma_f32_16x16x32_bf16 a[28:31], v[128:131], v[72:75], a[28:31]\n" \
"s_lshl_b32 s63, s62, 12\n" \
"v_writelane_b32 v11, s95, 12\n" \
"v_mfma_f32_16x16x32_bf16 a[28:31], v[124:127], v[68:71], a[28:31]\n" \
"s_and_b32 s95, 1, %[w]\n" \
"s_lshr_b32 s96, %[w], 1\n" \
"v_mfma_f32_16x16x32_bf16 a[60:63], v[116:119], v[68:71], a[60:63]\n" \
"s_add_u32 s88, s45, 0x1000\n" \
"s_lshl_b32 s94, %[w], 11\n" \
"v_mfma_f32_16x16x32_bf16 a[60:63], v[120:123], v[72:75], a[60:63]\n" \
"buffer_store_dwordx4 v[252:255], v20, s[80:83], s88 offen offset:3072 sc1\n" \
"v_mfma_f32_16x16x32_bf16 a[92:95], v[72:75], v[136:139], a[92:95]\n" \
"s_lshl_b32 s15, s95, 12\n" \
"s_lshl_b32 s16, s96, 14\n" \
"v_mfma_f32_16x16x32_bf16 a[92:95], v[68:71], v[132:135], a[92:95]\n" \
"s_add_u32 s12, 0xc000, s94\n" \
"s_add_u32 s97, s16, s15\n" \
"v_smfmac_f32_16x16x64_bf16 a[124:127], v[140:143], v[76:83], v50\n" \
"v_mfma_f32_16x16x32_bf16 a[0:3], v[124:127], v[76:79], a[0:3]\n" \
"s_lshr_b32 s100, %[b], 5\n" \
"s_and_b32 s65, 7, %[b]\n" \
"v_mfma_f32_16x16x32_bf16 a[0:3], v[128:131], v[80:83], a[0:3]\n" \
"s_lshr_b32 s67, %[b], 3\n" \
"s_xor_b32 s13, 0x8000, s97\n" \
"v_mfma_f32_16x16x32_bf16 a[4:7], v[120:123], v[80:83], a[4:7]\n" \
"s_add_u32 s20, 0x14000, s97\n" \
"s_mul_i32 s98, 0xe000, s96\n" \
"v_mfma_f32_16x16x32_bf16 a[4:7], v[116:119], v[76:79], a[4:7]\n" \
"s_lshl_b32 s18, s100, 1\n" \
"s_lshl_b32 s66, s65, 2\n" \
"v_mfma_f32_16x16x32_bf16 a[32:35], v[76:79], v[132:135], a[32:35]\n" \
"s_and_b32 s94, 3, s67\n" \
"s_add_u32 s99, s98, s15\n" \
"v_mfma_f32_16x16x32_bf16 a[32:35], v[80:83], v[136:139], a[32:35]\n" \
"s_add_u32 s101, s18, s96\n" \
"s_waitcnt lgkmcnt(0)\n" \
"v_mfma_f32_16x16x32_bf16 a[128:131], v[152:155], v[56:59], a[128:131]\n" \
"s_mov_b32 s64, 0x4000\n" \
"s_add_u32 s97, s66, s94\n" \
"v_mfma_f32_16x16x32_bf16 a[128:131], v[148:151], v[52:55], a[128:131]\n" \
"s_add_u32 s21, 0x10000, s99\n" \
"s_lshl_b32 s14, s101, 15\n" \
"v_mfma_f32_16x16x32_bf16 a[132:135], v[156:159], v[52:55], a[132:135]\n" \
"s_cmp_lg_u32 s100, 0\n" \
"s_cselect_b32 s17, s64, 0x8000\n" \
"v_mfma_f32_16x16x32_bf16 a[132:135], v[160:163], v[56:59], a[132:135]\n" \
"s_lshl_b32 s98, s97, 8\n" \
"s_and_b32 s67, 2, %[w]\n" \
"v_mfma_f32_16x16x32_bf16 a[136:139], v[168:171], v[56:59], a[136:139]\n" \
"s_add_u32 s99, 0x5be8020, s98\n" \
"s_lshl_b32 s101, s100, 2\n" \
"v_mfma_f32_16x16x32_bf16 a[136:139], v[164:167], v[52:55], a[136:139]\n" \
"s_lshl_b32 s66, s67, 1\n" \
"s_add_u32 s64, s99, s101\n" \
"v_mfma_f32_16x16x32_bf16 a[140:143], v[172:175], v[52:55], a[140:143]\n" \
"s_lshl_b32 s65, s100, 3\n" \
"s_add_u32 s94, s66, s95\n" \
"v_mfma_f32_16x16x32_bf16 a[140:143], v[176:179], v[56:59], a[140:143]\n" \
"s_cmp_eq_u32 %[w], 1\n" \
"s_cselect_b32 s22, s64, 0x40000000\n" \
"v_mfma_f32_16x16x32_bf16 a[160:163], v[152:155], v[64:67], a[160:163]\n" \
"s_add_u32 s98, s65, s94\n" \
"s_mul_i32 s99, 0x24800, s98\n" \
"v_mfma_f32_16x16x32_bf16 a[160:163], v[148:151], v[60:63], a[160:163]\n" \
"s_lshl_b32 s101, s97, 12\n" \
"s_sub_u32 s67, 0x78, s65\n" \
"v_mfma_f32_16x16x32_bf16 a[164:167], v[156:159], v[60:63], a[164:167]\n" \
"s_add_u32 s64, s99, s101\n" \
"s_add_u32 s66, s67, s94\n" \
"v_mfma_f32_16x16x32_bf16 a[164:167], v[160:163], v[64:67], a[164:167]\n" \
"s_add_u32 s60, 0x3004800, s64\n" \
"s_mul_i32 s95, 0x24800, s66\n" \
"v_mfma_f32_16x16x32_bf16 a[168:171], v[168:171], v[64:67], a[168:171]\n" \
"s_lshl_b32 s99, s96, 1\n" \
"s_add_u32 s98, s95, s101\n" \
"v_mfma_f32_16x16x32_bf16 a[168:171], v[164:167], v[60:63], a[168:171]\n" \
"s_add_u32 s64, s100, s99\n" \
"s_mov_b32 s65, 0x18000000\n" \
"v_mfma_f32_16x16x32_bf16 a[172:175], v[172:175], v[60:63], a[172:175]\n" \
"s_mul_i32 s94, 6, s96\n" \
"s_add_u32 s66, s96, s94\n" \
"v_mfma_f32_16x16x32_bf16 a[172:175], v[176:179], v[64:67], a[172:175]\n" \
"s_cmp_lt_u32 s64, 2\n" \
"s_cselect_b32 s67, s65, 0x30000000\n" \
"v_mfma_f32_16x16x32_bf16 a[192:195], v[152:155], v[72:75], a[192:195]\n" \
"s_mov_b32 s95, 0x80000\n" \
"s_mov_b32 s64, 0xaaaaaaaa\n" \
"v_mfma_f32_16x16x32_bf16 a[192:195], v[148:151], v[68:71], a[192:195]\n" \
"s_cmp_gt_u32 s66, s100\n" \
"s_cselect_b32 s101, s95, 0x40000\n" \
"s_waitcnt vmcnt(3)\n" \
"s_barrier\n" \
"s_mov_b64 exec, 1\n" \
"buffer_store_dword v45, off, s[80:83], s38 offset:0 sc1\n" \
"s_mov_b64 exec, -1\n" \
"v_mfma_f32_16x16x32_bf16 a[196:199], v[156:159], v[68:71], a[196:199]\n" \
"s_add_u32 m0, s40, 0x8000\n" \
"s_add_u32 s85, s72, 0x0\n" \
"s_nop 0\n" \
"v_mfma_f32_16x16x32_bf16 a[196:199], v[160:163], v[72:75], a[196:199]\n" \
"ds_read_b64_tr_b16 v[140:141], v39 offset:0\n" \
"ds_read_b64_tr_b16 v[142:143], v39 offset:960\n" \
"v_mfma_f32_16x16x32_bf16 a[200:203], v[168:171], v[72:75], a[200:203]\n" \
"buffer_load_dwordx4 v20, s[80:83], s85 offen sc1 lds\n" \
"v_mfma_f32_16x16x32_bf16 a[200:203], v[164:167], v[68:71], a[200:203]\n" \
"s_add_u32 m0, s40, 0x9000\n" \
"s_add_u32 s84, s72, 0x1000\n" \
"v_mfma_f32_16x16x32_bf16 a[204:207], v[172:175], v[68:71], a[204:207]\n" \
"ds_read_b64_tr_b16 v[128:129], v37 offset:4096\n" \
"ds_read_b64_tr_b16 v[130:131], v37 offset:4608\n" \
"v_mfma_f32_16x16x32_bf16 a[204:207], v[176:179], v[72:75], a[204:207]\n" \
"buffer_load_dwordx4 v20, s[80:83], s84 offen sc1 lds\n" \
"v_mfma_f32_16x16x32_bf16 a[224:227], v[152:155], v[80:83], a[224:227]\n" \
"s_add_u32 m0, s40, 0xb000\n" \
"s_add_u32 s85, s62, 0x0\n" \
"v_mfma_f32_16x16x32_bf16 a[224:227], v[148:151], v[76:79], a[224:227]\n" \
"ds_read_b64_tr_b16 v[124:125], v37 offset:0\n" \
"ds_read_b64_tr_b16 v[126:127], v37 offset:512\n" \
"v_mfma_f32_16x16x32_bf16 a[228:231], v[156:159], v[76:79], a[228:231]\n" \
"buffer_load_dwordx4 v21, s[76:79], s85 offen lds\n" \
"v_mfma_f32_16x16x32_bf16 a[228:231], v[160:163], v[80:83], a[228:231]\n" \
"s_add_u32 m0, %[mA], 0x8000\n" \
"s_add_u32 s84, s62, 0x0\n" \
"v_mfma_f32_16x16x32_bf16 a[232:235], v[168:171], v[80:83], a[232:235]\n" \
"ds_read_b64_tr_b16 v[132:133], v38 offset:0\n" \
"ds_read_b64_tr_b16 v[134:135], v38 offset:512\n" \
"v_mfma_f32_16x16x32_bf16 a[232:235], v[164:167], v[76:79], a[232:235]\n" \
"buffer_load_dwordx4 v24, s[76:79], s84 offen lds\n" \
"v_mfma_f32_16x16x32_bf16 a[236:239], v[172:175], v[76:79], a[236:239]\n" \
"ds_read_b64_tr_b16 v[136:137], v38 offset:4096\n" \
"ds_read_b64_tr_b16 v[138:139], v38 offset:4608\n" \
"v_mfma_f32_16x16x32_bf16 a[236:239], v[176:179], v[80:83], a[236:239]\n" \
"s_add_u32 m0, %[mA], 0x8400\n" \
"s_add_u32 s85, s62, 0x2400\n" \
"v_mfma_f32_16x16x32_bf16 a[144:147], v[152:155], v[88:91], a[144:147]\n" \
"buffer_load_dwordx4 v25, s[76:79], s85 offen lds\n" \
"v_mfma_f32_16x16x32_bf16 a[144:147], v[148:151], v[84:87], a[144:147]\n" \
"ds_read_b128 v[52:55], v28 offset:0\n" \
"ds_read_b128 v[56:59], v29 offset:0\n" \
"v_mfma_f32_16x16x32_bf16 a[148:151], v[156:159], v[84:87], a[148:151]\n" \
"s_add_u32 m0, %[mB], 0x8000\n" \
"s_add_u32 s84, s63, 0x0\n" \
"s_nop 0\n" \
"v_mfma_f32_16x16x32_bf16 a[148:151], v[160:163], v[88:91], a[148:151]\n" \
"buffer_load_dwordx4 v26, s[68:71], s84 offen lds\n" \
"v_mfma_f32_16x16x32_bf16 a[152:155], v[168:171], v[88:91], a[152:155]\n" \
"ds_read_b64_tr_b16 v[116:117], v36 offset:0\n" \
"ds_read_b64_tr_b16 v[118:119], v36 offset:512\n" \
"v_mfma_f32_16x16x32_bf16 a[152:155], v[164:167], v[84:87], a[152:155]\n" \
"ds_read_b64_tr_b16 v[120:121], v36 offset:4096\n" \
"ds_read_b64_tr_b16 v[122:123], v36 offset:4608\n" \
"v_mfma_f32_16x16x32_bf16 a[156:159], v[172:175], v[84:87], a[156:159]\n" \
"s_add_u32 m0, %[mA], 0x8800\n" \
"s_add_u32 s85, s62, 0x4800\n" \
"v_mfma_f32_16x16x32_bf16 a[156:159], v[176:179], v[88:91], a[156:159]\n" \
"buffer_load_dwordx4 v24, s[76:79], s85 offen lds\n" \
"v_mfma_f32_16x16x32_bf16 a[176:179], v[152:155], v[96:99], a[176:179]\n" \
"ds_read_b128 v[84:87], v22 offset:0\n" \
"ds_read_b128 v[88:91], v23 offset:0\n" \
"v_mfma_f32_16x16x32_bf16 a[176:179], v[148:151], v[92:95], a[176:179]\n" \
"s_add_u32 m0, %[mA], 0x8c00\n" \
"s_add_u32 s84, s62, 0x6c00\n" \
"v_mfma_f32_16x16x32_bf16 a[180:183], v[156:159], v[92:95], a[180:183]\n" \
"buffer_load_dwordx4 v25, s[76:79], s84 offen lds\n" \
"v_mfma_f32_16x16x32_bf16 a[180:183], v[160:163], v[96:99], a[180:183]\n" \
"ds_read_b128 v[60:63], v28 offset:2048\n" \
"ds_read_b128 v[64:67], v29 offset:2048\n" \
"v_mfma_f32_16x16x32_bf16 a[184:187], v[168:171], v[96:99], a[184:187]\n" \
"ds_read_b128 v[68:71], v28 offset:4096\n" \
"ds_read_b128 v[72:75], v29 offset:4096\n" \
"v_mfma_f32_16x16x32_bf16 a[184:187], v[164:167], v[92:95], a[184:187]\n" \
"s_add_u32 m0, %[mB], 0x8400\n" \
"s_add_u32 s85, s63, 0x8000\n" \
"v_mfma_f32_16x16x32_bf16 a[188:191], v[172:175], v[92:95], a[188:191]\n" \
"buffer_load_dwordx4 v26, s[68:71], s85 offen lds\n" \
"v_mfma_f32_16x16x32_bf16 a[188:191], v[176:179], v[96:99], a[188:191]\n" \
"ds_read_b128 v[76:79], v28 offset:6144\n" \
"ds_read_b128 v[80:83], v29 offset:6144\n" \
"v_mfma_f32_16x16x32_bf16 a[208:211], v[152:155], v[104:107], a[208:211]\n" \
"s_add_u32 m0, %[mA], 0x9000\n" \
"s_add_u32 s84, s62, 0x9000\n" \
"v_mfma_f32_16x16x32_bf16 a[208:211], v[148:151], v[100:103], a[208:211]\n" \
"buffer_load_dwordx4 v24, s[76:79], s84 offen lds\n" \
"v_mfma_f32_16x16x32_bf16 a[212:215], v[156:159], v[100:103], a[212:215]\n" \
"ds_read_b128 v[92:95], v48 offset:2048\n" \
"ds_read_b128 v[96:99], v49 offset:2048\n" \
"v_mfma_f32_16x16x32_bf16 a[212:215], v[160:163], v[104:107], a[212:215]\n" \
"s_mov_b32 s65, 0xaaaaaaaa\n" \
"s_add_u32 s23, s67, s101\n" \
"s_nop 0\n" \
"v_mfma_f32_16x16x32_bf16 a[216:219], v[168:171], v[104:107], a[216:219]\n" \
"s_add_u32 m0, %[mA], 0x9400\n" \
"s_add_u32 s85, s62, 0xb400\n" \
"v_mfma_f32_16x16x32_bf16 a[216:219], v[164:167], v[100:103], a[216:219]\n" \
"buffer_load_dwordx4 v25, s[76:79], s85 offen lds\n" \
"v_mfma_f32_16x16x32_bf16 a[220:223], v[172:175], v[100:103], a[220:223]\n" \
"ds_read_b128 v[100:103], v48 offset:4096\n" \
"s_mov_b32 s66, 0xfff0ffff\n" \
"v_mfma_f32_16x16x32_bf16 a[220:223], v[176:179], v[104:107], a[220:223]\n" \
"s_add_u32 m0, %[mB], 0x8800\n" \
"s_add_u32 s84, s63, 0x10000\n" \
"v_mfma_f32_16x16x32_bf16 a[240:243], v[152:155], v[112:115], a[240:243]\n" \
"buffer_load_dwordx4 v27, s[68:71], s84 offen lds\n" \
"v_mfma_f32_16x16x32_bf16 a[240:243], v[148:151], v[108:111], a[240:243]\n" \
"ds_read_b64_tr_b16 v[152:153], v32 offset:8192\n" \
"ds_read_b64_tr_b16 v[154:155], v32 offset:9216\n" \
"v_mfma_f32_16x16x32_bf16 a[244:247], v[156:159], v[108:111], a[244:247]\n" \
"ds_read_b64_tr_b16 v[148:149], v32 offset:0\n" \
"ds_read_b64_tr_b16 v[150:151], v32 offset:1024\n" \
"v_mfma_f32_16x16x32_bf16 a[244:247], v[160:163], v[112:115], a[244:247]\n" \
"s_add_u32 m0, %[mA], 0x9800\n" \
"s_add_u32 s85, s62, 0xd800\n" \
"v_mfma_f32_16x16x32_bf16 a[248:251], v[168:171], v[112:115], a[248:251]\n" \
"buffer_load_dwordx4 v24, s[76:79], s85 offen lds\n" \
"v_mfma_f32_16x16x32_bf16 a[248:251], v[164:167], v[108:111], a[248:251]\n" \
"ds_read_b64_tr_b16 v[156:157], v33 offset:0\n" \
"ds_read_b64_tr_b16 v[158:159], v33 offset:1024\n" \
"v_mfma_f32_16x16x32_bf16 a[252:255], v[172:175], v[108:111], a[252:255]\n" \
"ds_read_b64_tr_b16 v[160:161], v33 offset:8192\n" \
"ds_read_b64_tr_b16 v[162:163], v33 offset:9216\n" \
"v_mfma_f32_16x16x32_bf16 a[252:255], v[176:179], v[112:115], a[252:255]\n" \
"s_add_u32 m0, %[mA], 0x9c00\n" \
"s_waitcnt lgkmcnt(11)\n" \
"s_nop 0\n" \
"v_smfmac_f32_16x16x64_bf16 a[16:19], v[140:143], v[84:91], v50\n" \
"v_mfma_f32_16x16x32_bf16 a[48:51], v[128:131], v[88:91], a[48:51]\n" \
"s_add_u32 s85, s62, 0xfc00\n" \
"ds_read_b64_tr_b16 v[168:169], v34 offset:8192\n" \
"v_mfma_f32_16x16x32_bf16 a[48:51], v[124:127], v[84:87], a[48:51]\n" \
"buffer_load_dwordx4 v25, s[76:79], s85 offen lds\n" \
"v_mfma_f32_16x16x32_bf16 a[80:83], v[84:87], v[132:135], a[80:83]\n" \
"s_add_u32 m0, %[mB], 0x8c00\n" \
"s_add_u32 s84, s63, 0x18000\n" \
"v_mfma_f32_16x16x32_bf16 a[80:83], v[88:91], v[136:139], a[80:83]\n" \
"ds_read_b64_tr_b16 v[170:171], v34 offset:9216\n" \
"ds_read_b64_tr_b16 v[164:165], v34 offset:0\n" \
"v_smfmac_f32_16x16x64_bf16 a[112:115], v[140:143], v[52:59], v50\n" \
"v_mfma_f32_16x16x32_bf16 a[20:23], v[128:131], v[56:59], a[20:23]\n" \
"buffer_load_dwordx4 v27, s[68:71], s84 offen lds\n" \
"v_mfma_f32_16x16x32_bf16 a[20:23], v[124:127], v[52:55], a[20:23]\n" \
"s_add_u32 s89, s43, 0x1000\n" \
"ds_read_b64_tr_b16 v[166:167], v34 offset:1024\n" \
"v_mfma_f32_16x16x32_bf16 a[52:55], v[116:119], v[52:55], a[52:55]\n" \
"ds_read_b64_tr_b16 v[172:173], v35 offset:0\n" \
"ds_read_b64_tr_b16 v[174:175], v35 offset:1024\n" \
"v_mfma_f32_16x16x32_bf16 a[52:55], v[120:123], v[56:59], a[52:55]\n" \
"buffer_store_dwordx4 a[36:39], v20, s[80:83], s89 offen offset:0 sc1\n" \
"v_mfma_f32_16x16x32_bf16 a[84:87], v[56:59], v[136:139], a[84:87]\n" \
"ds_read_b64_tr_b16 v[176:177], v35 offset:8192\n" \
"ds_read_b64_tr_b16 v[178:179], v35 offset:9216\n" \
"v_mfma_f32_16x16x32_bf16 a[84:87], v[52:55], v[132:135], a[84:87]\n" \
"ds_read_b128 v[84:87], v48 offset:0\n" \
"ds_read_b128 v[88:91], v49 offset:0\n" \
"v_smfmac_f32_16x16x64_bf16 a[116:119], v[140:143], v[60:67], v50\n" \
"v_mfma_f32_16x16x32_bf16 a[24:27], v[124:127], v[60:63], a[24:27]\n" \
"ds_read_b128 v[104:107], v49 offset:4096\n" \
"ds_read_b128 v[108:111], v48 offset:6144\n" \
"v_mfma_f32_16x16x32_bf16 a[24:27], v[128:131], v[64:67], a[24:27]\n" \
"s_add_u32 s88, s43, 0x2000\n" \
"ds_read_b128 v[112:115], v49 offset:6144\n" \
"v_mfma_f32_16x16x32_bf16 a[56:59], v[120:123], v[64:67], a[56:59]\n" \
"buffer_store_dwordx4 a[64:67], v20, s[80:83], s88 offen offset:0 sc1\n" \
"v_mfma_f32_16x16x32_bf16 a[56:59], v[116:119], v[60:63], a[56:59]\n" \
"s_add_u32 s62, s62, 0x80\n" \
"s_mov_b32 s67, 0xf000ff00\n" \
"v_mfma_f32_16x16x32_bf16 a[88:91], v[60:63], v[132:135], a[88:91]\n" \
"s_cmp_lt_u32 s62, 0x480\n" \
"s_cselect_b32 s62, s62, 0\n" \
"v_mfma_f32_16x16x32_bf16 a[88:91], v[64:67], v[136:139], a[88:91]\n" \
"s_lshl_b32 s72, s62, 6\n" \
"s_add_u32 s72, s72, s37\n" \
"v_smfmac_f32_16x16x64_bf16 a[120:123], v[140:143], v[68:75], v50\n" \
"v_mfma_f32_16x16x32_bf16 a[28:31], v[128:131], v[72:75], a[28:31]\n" \
"s_lshl_b32 s63, s62, 12\n" \
"s_add_u32 s61, 0x3004800, s98\n" \
"v_mfma_f32_16x16x32_bf16 a[28:31], v[124:127], v[68:71], a[28:31]\n" \
"s_lshl_b32 s99, s97, 20\n" \
"s_lshl_b32 s98, s100, 16\n" \
"v_mfma_f32_16x16x32_bf16 a[60:63], v[116:119], v[68:71], a[60:63]\n" \
"s_add_u32 s89, s43, 0x3000\n" \
"s_add_u32 s8, 0x1000000, s99\n" \
"v_mfma_f32_16x16x32_bf16 a[60:63], v[120:123], v[72:75], a[60:63]\n" \
"buffer_store_dwordx4 a[68:71], v20, s[80:83], s89 offen offset:0 sc1\n" \
"v_mfma_f32_16x16x32_bf16 a[92:95], v[72:75], v[136:139], a[92:95]\n" \
"s_sub_u32 s19, 14, s18\n" \
"s_sub_u32 s10, 0x100000, s98\n" \
"v_mfma_f32_16x16x32_bf16 a[92:95], v[68:71], v[132:135], a[92:95]\n" \
"s_mov_b32 s11, 0x20000\n" \
"s_mov_b32 s58, 0x6000000\n" \
"v_smfmac_f32_16x16x64_bf16 a[124:127], v[140:143], v[76:83], v50\n" \
"v_mfma_f32_16x16x32_bf16 a[0:3], v[124:127], v[76:79], a[0:3]\n" \
"s_mov_b32 s59, 0x20000\n" \
"s_mov_b64 s[56:57], s[80:81]\n" \
"v_mfma_f32_16x16x32_bf16 a[0:3], v[128:131], v[80:83], a[0:3]\n" \
"s_add_u32 s8, s8, s80\n" \
"s_addc_u32 s9, s81, 0\n" \
"v_mfma_f32_16x16x32_bf16 a[4:7], v[120:123], v[80:83], a[4:7]\n" \
"v_mfma_f32_16x16x32_bf16 a[4:7], v[116:119], v[76:79], a[4:7]\n" \
"v_mfma_f32_16x16x32_bf16 a[32:35], v[76:79], v[132:135], a[32:35]\n" \
"v_mfma_f32_16x16x32_bf16 a[32:35], v[80:83], v[136:139], a[32:35]\n" \
"s_waitcnt lgkmcnt(0)\n" \
"s_nop 0\n" \
"v_mfma_f32_16x16x32_bf16 a[128:131], v[152:155], v[56:59], a[128:131]\n" \
"v_mfma_f32_16x16x32_bf16 a[128:131], v[148:151], v[52:55], a[128:131]\n" \
"v_mfma_f32_16x16x32_bf16 a[132:135], v[156:159], v[52:55], a[132:135]\n" \
"v_mfma_f32_16x16x32_bf16 a[132:135], v[160:163], v[56:59], a[132:135]\n" \
"v_mfma_f32_16x16x32_bf16 a[136:139], v[168:171], v[56:59], a[136:139]\n" \
"v_mfma_f32_16x16x32_bf16 a[136:139], v[164:167], v[52:55], a[136:139]\n" \
"v_mfma_f32_16x16x32_bf16 a[140:143], v[172:175], v[52:55], a[140:143]\n" \
"v_mfma_f32_16x16x32_bf16 a[140:143], v[176:179], v[56:59], a[140:143]\n" \
"v_mfma_f32_16x16x32_bf16 a[160:163], v[152:155], v[64:67], a[160:163]\n" \
"v_mfma_f32_16x16x32_bf16 a[160:163], v[148:151], v[60:63], a[160:163]\n" \
"v_mfma_f32_16x16x32_bf16 a[164:167], v[156:159], v[60:63], a[164:167]\n" \
"v_mfma_f32_16x16x32_bf16 a[164:167], v[160:163], v[64:67], a[164:167]\n" \
"v_mfma_f32_16x16x32_bf16 a[168:171], v[168:171], v[64:67], a[168:171]\n" \
"v_mfma_f32_16x16x32_bf16 a[168:171], v[164:167], v[60:63], a[168:171]\n" \
"v_mfma_f32_16x16x32_bf16 a[172:175], v[172:175], v[60:63], a[172:175]\n" \
"v_mfma_f32_16x16x32_bf16 a[172:175], v[176:179], v[64:67], a[172:175]\n" \
"v_mfma_f32_16x16x32_bf16 a[192:195], v[152:155], v[72:75], a[192:195]\n" \
"v_mfma_f32_16x16x32_bf16 a[192:195], v[148:151], v[68:71], a[192:195]\n" \
"s_waitcnt vmcnt(3)\n" \
"s_barrier\n" \
"v_mfma_f32_16x16x32_bf16 a[196:199], v[156:159], v[68:71], a[196:199]\n" \
"s_add_u32 m0, s40, 0x0\n" \
"s_add_u32 s85, s72, 0x0\n" \
"v_mfma_f32_16x16x32_bf16 a[196:199], v[160:163], v[72:75], a[196:199]\n" \
"buffer_load_dwordx4 v20, s[80:83], s85 offen sc1 lds\n" \
"v_mfma_f32_16x16x32_bf16 a[200:203], v[168:171], v[72:75], a[200:203]\n" \
"s_add_u32 m0, s40, 0x1000\n" \
"s_add_u32 s84, s72, 0x1000\n" \
"v_mfma_f32_16x16x32_bf16 a[200:203], v[164:167], v[68:71], a[200:203]\n" \
"ds_read_b64_tr_b16 v[140:141], v39 offset:32768\n" \
"ds_read_b64_tr_b16 v[142:143], v39 offset:33728\n" \
"v_mfma_f32_16x16x32_bf16 a[204:207], v[172:175], v[68:71], a[204:207]\n" \
"buffer_load_dwordx4 v20, s[80:83], s84 offen sc1 lds\n" \
"v_mfma_f32_16x16x32_bf16 a[204:207], v[176:179], v[72:75], a[204:207]\n" \
"ds_read_b64_tr_b16 v[128:129], v37 offset:36864\n" \
"ds_read_b64_tr_b16 v[130:131], v37 offset:37376\n" \
"v_mfma_f32_16x16x32_bf16 a[224:227], v[152:155], v[80:83], a[224:227]\n" \
"s_add_u32 m0, s40, 0x3000\n" \
"s_add_u32 s85, s62, 0x0\n" \
"v_mfma_f32_16x16x32_bf16 a[224:227], v[148:151], v[76:79], a[224:227]\n" \
"buffer_load_dwordx4 v21, s[76:79], s85 offen lds\n" \
"v_mfma_f32_16x16x32_bf16 a[228:231], v[156:159], v[76:79], a[228:231]\n" \
"ds_read_b64_tr_b16 v[124:125], v37 offset:32768\n" \
"ds_read_b64_tr_b16 v[126:127], v37 offset:33280\n" \
"v_mfma_f32_16x16x32_bf16 a[228:231], v[160:163], v[80:83], a[228:231]\n" \
"s_add_u32 m0, %[mA], 0x0\n" \
"s_add_u32 s84, s62, 0x0\n" \
"s_nop 0\n" \
"v_mfma_f32_16x16x32_bf16 a[232:235], v[168:171], v[80:83], a[232:235]\n" \
"buffer_load_dwordx4 v24, s[76:79], s84 offen lds\n" \
"v_mfma_f32_16x16x32_bf16 a[232:235], v[164:167], v[76:79], a[232:235]\n" \
"ds_read_b64_tr_b16 v[132:133], v38 offset:32768\n" \
"ds_read_b64_tr_b16 v[134:135], v38 offset:33280\n" \
"v_mfma_f32_16x16x32_bf16 a[236:239], v[172:175], v[76:79], a[236:239]\n" \
"ds_read_b64_tr_b16 v[136:137], v38 offset:36864\n" \
"ds_read_b64_tr_b16 v[138:139], v38 offset:37376\n" \
"v_mfma_f32_16x16x32_bf16 a[236:239], v[176:179], v[80:83], a[236:239]\n" \
"s_add_u32 m0, %[mA], 0x400\n" \
"s_add_u32 s85, s62, 0x2400\n" \
"v_mfma_f32_16x16x32_bf16 a[144:147], v[152:155], v[88:91], a[144:147]\n" \
"buffer_load_dwordx4 v25, s[76:79], s85 offen lds\n" \
"v_mfma_f32_16x16x32_bf16 a[144:147], v[148:151], v[84:87], a[144:147]\n" \
"ds_read_b128 v[52:55], v28 offset:32768\n" \
"ds_read_b128 v[56:59], v29 offset:32768\n" \
"v_mfma_f32_16x16x32_bf16 a[148:151], v[156:159], v[84:87], a[148:151]\n" \
"s_add_u32 m0, %[mB], 0x0\n" \
"s_add_u32 s84, s63, 0x0\n" \
"v_mfma_f32_16x16x32_bf16 a[148:151], v[160:163], v[88:91], a[148:151]\n" \
"buffer_load_dwordx4 v26, s[68:71], s84 offen lds\n" \
"v_mfma_f32_16x16x32_bf16 a[152:155], v[168:171], v[88:91], a[152:155]\n" \
"ds_read_b64_tr_b16 v[116:117], v36 offset:32768\n" \
"ds_read_b64_tr_b16 v[118:119], v36 offset:33280\n" \
"v_mfma_f32_16x16x32_bf16 a[152:155], v[164:167], v[84:87], a[152:155]\n" \
"ds_read_b64_tr_b16 v[120:121], v36 offset:36864\n" \
"ds_read_b64_tr_b16 v[122:123], v36 offset:37376\n" \
"v_mfma_f32_16x16x32_bf16 a[156:159], v[172:175], v[84:87], a[156:159]\n" \
"s_add_u32 m0, %[mA], 0x800\n" \
"s_add_u32 s85, s62, 0x4800\n" \
"v_mfma_f32_16x16x32_bf16 a[156:159], v[176:179], v[88:91], a[156:159]\n" \
"buffer_load_dwordx4 v24, s[76:79], s85 offen lds\n" \
"v_mfma_f32_16x16x32_bf16 a[176:179], v[152:155], v[96:99], a[176:179]\n" \
"ds_read_b128 v[84:87], v22 offset:32768\n" \
"ds_read_b128 v[88:91], v23 offset:32768\n" \
"v_mfma_f32_16x16x32_bf16 a[176:179], v[148:151], v[92:95], a[176:179]\n" \
"s_add_u32 m0, %[mA], 0xc00\n" \
"s_add_u32 s84, s62, 0x6c00\n" \
"v_mfma_f32_16x16x32_bf16 a[180:183], v[156:159], v[92:95], a[180:183]\n" \
"buffer_load_dwordx4 v25, s[76:79], s84 offen lds\n" \
"v_mfma_f32_16x16x32_bf16 a[180:183], v[160:163], v[96:99], a[180:183]\n" \
"ds_read_b128 v[60:63], v28 offset:34816\n" \
"ds_read_b128 v[64:67], v29 offset:34816\n" \
"v_mfma_f32_16x16x32_bf16 a[184:187], v[168:171], v[96:99], a[184:187]\n" \
"ds_read_b128 v[68:71], v28 offset:36864\n" \
"ds_read_b128 v[72:75], v29 offset:36864\n" \
"v_mfma_f32_16x16x32_bf16 a[184:187], v[164:167], v[92:95], a[184:187]\n" \
"s_add_u32 m0, %[mB], 0x400\n" \
"s_add_u32 s85, s63, 0x8000\n" \
"v_mfma_f32_16x16x32_bf16 a[188:191], v[172:175], v[92:95], a[188:191]\n" \
"buffer_load_dwordx4 v26, s[68:71], s85 offen lds\n" \
"v_mfma_f32_16x16x32_bf16 a[188:191], v[176:179], v[96:99], a[188:191]\n" \
"ds_read_b128 v[76:79], v28 offset:38912\n" \
"ds_read_b128 v[80:83], v29 offset:38912\n" \
"v_mfma_f32_16x16x32_bf16 a[208:211], v[152:155], v[104:107], a[208:211]\n" \
"s_add_u32 m0, %[mA], 0x1000\n" \
"s_add_u32 s84, s62, 0x9000\n" \
"v_mfma_f32_16x16x32_bf16 a[208:211], v[148:151], v[100:103], a[208:211]\n" \
"buffer_load_dwordx4 v24, s[76:79], s84 offen lds\n" \
"v_mfma_f32_16x16x32_bf16 a[212:215], v[156:159], v[100:103], a[212:215]\n" \
"ds_read_b128 v[92:95], v48 offset:34816\n" \
"ds_read_b128 v[96:99], v49 offset:34816\n" \
"v_mfma_f32_16x16x32_bf16 a[212:215], v[160:163], v[104:107], a[212:215]\n" \
"s_mov_b32 s72, 0x3eae1a93\n" \
"v_and_b32_e32 v26, 0x1ff, v20\n" \
"v_mfma_f32_16x16x32_bf16 a[216:219], v[168:171], v[104:107], a[216:219]\n" \
"s_add_u32 m0, %[mA], 0x1400\n" \
"s_add_u32 s85, s62, 0xb400\n" \
"v_mfma_f32_16x16x32_bf16 a[216:219], v[164:167], v[100:103], a[216:219]\n" \
"buffer_load_dwordx4 v25, s[76:79], s85 offen lds\n" \
"v_mfma_f32_16x16x32_bf16 a[220:223], v[172:175], v[100:103], a[220:223]\n" \
"ds_read_b128 v[100:103], v48 offset:36864\n" \
"v_add_u32_e64 v21, s48, v20\n" \
"v_mfma_f32_16x16x32_bf16 a[220:223], v[176:179], v[104:107], a[220:223]\n" \
"s_add_u32 m0, %[mB], 0x800\n" \
"s_add_u32 s84, s63, 0x10000\n" \
"v_mfma_f32_16x16x32_bf16 a[240:243], v[152:155], v[112:115], a[240:243]\n" \
"buffer_load_dwordx4 v27, s[68:71], s84 offen lds\n" \
"L_pf8:\n" \
"s_cbranch_execz L_pf12\n" \
"s_nop 0\n" \
"v_mfma_f32_16x16x32_bf16 a[240:243], v[148:151], v[108:111], a[240:243]\n" \
"ds_read_b64_tr_b16 v[152:153], v32 offset:40960\n" \
"ds_read_b64_tr_b16 v[154:155], v32 offset:41984\n" \
"v_mfma_f32_16x16x32_bf16 a[244:247], v[156:159], v[108:111], a[244:247]\n" \
"ds_read_b64_tr_b16 v[148:149], v32 offset:32768\n" \
"ds_read_b64_tr_b16 v[150:151], v32 offset:33792\n" \
"v_mfma_f32_16x16x32_bf16 a[244:247], v[160:163], v[112:115], a[244:247]\n" \
"s_add_u32 m0, %[mA], 0x1800\n" \
"s_add_u32 s85, s62, 0xd800\n" \
"v_mfma_f32_16x16x32_bf16 a[248:251], v[168:171], v[112:115], a[248:251]\n" \
"buffer_load_dwordx4 v24, s[76:79], s85 offen lds\n" \
"v_mfma_f32_16x16x32_bf16 a[248:251], v[164:167], v[108:111], a[248:251]\n" \
"ds_read_b64_tr_b16 v[156:157], v33 offset:32768\n" \
"ds_read_b64_tr_b16 v[158:159], v33 offset:33792\n" \
"v_mfma_f32_16x16x32_bf16 a[252:255], v[172:175], v[108:111], a[252:255]\n" \
"ds_read_b64_tr_b16 v[160:161], v33 offset:40960\n" \
"ds_read_b64_tr_b16 v[162:163], v33 offset:41984\n" \
"v_mfma_f32_16x16x32_bf16 a[252:255], v[176:179], v[112:115], a[252:255]\n" \
"s_add_u32 m0, %[mA], 0x1c00\n" \
"s_waitcnt lgkmcnt(11)\n" \
"v_smfmac_f32_16x16x64_bf16 a[16:19], v[140:143], v[84:91], v50\n" \
"v_mfma_f32_16x16x32_bf16 a[48:51], v[128:131], v[88:91], a[48:51]\n" \
"s_add_u32 s85, s62, 0xfc00\n" \
"ds_read_b64_tr_b16 v[168:169], v34 offset:40960\n" \
"v_mfma_f32_16x16x32_bf16 a[48:51], v[124:127], v[84:87], a[48:51]\n" \
"buffer_load_dwordx4 v25, s[76:79], s85 offen lds\n" \
"s_mov_b32 s76, s54\n" \
"v_mfma_f32_16x16x32_bf16 a[80:83], v[84:87], v[132:135], a[80:83]\n" \
"s_add_u32 m0, %[mB], 0xc00\n" \
"s_add_u32 s84, s63, 0x18000\n" \
"v_mfma_f32_16x16x32_bf16 a[80:83], v[88:91], v[136:139], a[80:83]\n" \
"ds_read_b64_tr_b16 v[170:171], v34 offset:41984\n" \
"ds_read_b64_tr_b16 v[164:165], v34 offset:32768\n" \
"v_smfmac_f32_16x16x64_bf16 a[112:115], v[140:143], v[52:59], v50\n" \
"v_mfma_f32_16x16x32_bf16 a[20:23], v[128:131], v[56:59], a[20:23]\n" \
"buffer_load_dwordx4 v27, s[68:71], s84 offen lds\n" \
"v_mfma_f32_16x16x32_bf16 a[20:23], v[124:127], v[52:55], a[20:23]\n" \
"s_add_u32 s88, s43, 0x0\n" \
"s_nop 0\n" \
"ds_read_b64_tr_b16 v[166:167], v34 offset:33792\n" \
"v_mfma_f32_16x16x32_bf16 a[52:55], v[116:119], v[52:55], a[52:55]\n" \
"ds_read_b64_tr_b16 v[172:173], v35 offset:32768\n" \
"ds_read_b64_tr_b16 v[174:175], v35 offset:33792\n" \
"v_mfma_f32_16x16x32_bf16 a[52:55], v[120:123], v[56:59], a[52:55]\n" \
"buffer_store_dwordx4 a[96:99], v20, s[80:83], s88 offen offset:1024 sc1\n" \
"v_mfma_f32_16x16x32_bf16 a[84:87], v[56:59], v[136:139], a[84:87]\n" \
"ds_read_b64_tr_b16 v[176:177], v35 offset:40960\n" \
"ds_read_b64_tr_b16 v[178:179], v35 offset:41984\n" \
"v_mfma_f32_16x16x32_bf16 a[84:87], v[52:55], v[132:135], a[84:87]\n" \
"ds_read_b128 v[84:87], v48 offset:32768\n" \
"ds_read_b128 v[88:91], v49 offset:32768\n" \
"v_smfmac_f32_16x16x64_bf16 a[116:119], v[140:143], v[60:67], v50\n" \
"v_mfma_f32_16x16x32_bf16 a[24:27], v[124:127], v[60:63], a[24:27]\n" \
"ds_read_b128 v[104:107], v49 offset:36864\n" \
"ds_read_b128 v[108:111], v48 offset:38912\n" \
"v_mfma_f32_16x16x32_bf16 a[24:27], v[128:131], v[64:67], a[24:27]\n" \
"s_add_u32 s89, s43, 0x1000\n" \
"ds_read_b128 v[112:115], v49 offset:38912\n" \
"v_mfma_f32_16x16x32_bf16 a[56:59], v[120:123], v[64:67], a[56:59]\n" \
"buffer_store_dwordx4 a[100:103], v20, s[80:83], s89 offen offset:1024 sc1\n" \
"s_add_u32 s89, s43, 0x3000\n" \
"v_mfma_f32_16x16x32_bf16 a[56:59], v[116:119], v[60:63], a[56:59]\n" \
"v_accvgpr_read_b32 v248, a16\n" \
"v_accvgpr_read_b32 v249, a17\n" \
"v_mfma_f32_16x16x32_bf16 a[88:91], v[60:63], v[132:135], a[88:91]\n" \
"v_accvgpr_read_b32 v250, a18\n" \
"v_accvgpr_read_b32 v251, a19\n" \
"v_mfma_f32_16x16x32_bf16 a[88:91], v[64:67], v[136:139], a[88:91]\n" \
"v_accvgpr_read_b32 v180, a48\n" \
"v_accvgpr_read_b32 v181, a49\n" \
"v_smfmac_f32_16x16x64_bf16 a[120:123], v[140:143], v[68:75], v50\n" \
"v_mfma_f32_16x16x32_bf16 a[28:31], v[128:131], v[72:75], a[28:31]\n" \
"v_accvgpr_read_b32 v182, a50\n" \
"v_accvgpr_read_b32 v183, a51\n" \
"v_mfma_f32_16x16x32_bf16 a[28:31], v[124:127], v[68:71], a[28:31]\n" \
"v_accvgpr_read_b32 v252, a80\n" \
"v_accvgpr_read_b32 v253, a81\n" \
"v_mfma_f32_16x16x32_bf16 a[60:63], v[116:119], v[68:71], a[60:63]\n" \
"s_add_u32 s88, s43, 0x2000\n" \
"v_accvgpr_read_b32 v254, a82\n" \
"v_mfma_f32_16x16x32_bf16 a[60:63], v[120:123], v[72:75], a[60:63]\n" \
"buffer_store_dwordx4 a[8:11], v20, s[80:83], s88 offen offset:1024 sc1\n" \
"s_add_u32 s88, s45, 0x0\n" \
"s_nop 0\n" \
"v_mfma_f32_16x16x32_bf16 a[92:95], v[72:75], v[136:139], a[92:95]\n" \
"v_accvgpr_read_b32 v255, a83\n" \
"v_accvgpr_read_b32 v196, a112\n" \
"v_mfma_f32_16x16x32_bf16 a[92:95], v[68:71], v[132:135], a[92:95]\n" \
"v_accvgpr_read_b32 v197, a113\n" \
"v_accvgpr_read_b32 v198, a114\n" \
"v_smfmac_f32_16x16x64_bf16 a[124:127], v[140:143], v[76:83], v50\n" \
"v_mfma_f32_16x16x32_bf16 a[0:3], v[124:127], v[76:79], a[0:3]\n" \
"v_accvgpr_read_b32 v199, a115\n" \
"v_accvgpr_read_b32 v188, a20\n" \
"v_mfma_f32_16x16x32_bf16 a[0:3], v[128:131], v[80:83], a[0:3]\n" \
"v_accvgpr_read_b32 v189, a21\n" \
"v_accvgpr_read_b32 v190, a22\n" \
"v_mfma_f32_16x16x32_bf16 a[4:7], v[120:123], v[80:83], a[4:7]\n" \
"v_accvgpr_read_b32 v191, a23\n" \
"v_accvgpr_read_b32 v184, a52\n" \
"v_mfma_f32_16x16x32_bf16 a[4:7], v[116:119], v[76:79], a[4:7]\n" \
"v_lshlrev_b32_e32 v45, 10, v20\n" \
"v_add_u32_e32 v45, s41, v45\n" \
"v_mfma_f32_16x16x32_bf16 a[32:35], v[76:79], v[132:135], a[32:35]\n" \
"s_mov_b32 m0, %[mS]\n" \
"v_accvgpr_read_b32 v185, a53\n" \
"v_mfma_f32_16x16x32_bf16 a[32:35], v[80:83], v[136:139], a[32:35]\n" \
"v_accvgpr_read_b32 v186, a54\n" \
"s_waitcnt lgkmcnt(0)\n" \
"v_mfma_f32_16x16x32_bf16 a[128:131], v[152:155], v[56:59], a[128:131]\n" \
"v_accvgpr_read_b32 v187, a55\n" \
"v_accvgpr_read_b32 v192, a84\n" \
"v_mfma_f32_16x16x32_bf16 a[128:131], v[148:151], v[52:55], a[128:131]\n" \
"v_accvgpr_read_b32 v193, a85\n" \
"v_accvgpr_read_b32 v194, a86\n" \
"v_mfma_f32_16x16x32_bf16 a[132:135], v[156:159], v[52:55], a[132:135]\n" \
"v_accvgpr_read_b32 v195, a87\n" \
"v_accvgpr_read_b32 v212, a116\n" \
"s_getpc_b64 s[86:87]\n" \
"L_ct15_%=:\n" \
"s_add_u32 s86, s86, L_end_%=-L_ct15_%=\n" \
"s_addc_u32 s87, s87, 0\n" \
"v_mfma_f32_16x16x32_bf16 a[132:135], v[160:163], v[56:59], a[132:135]\n" \
"v_accvgpr_read_b32 v213, a117\n" \
"v_accvgpr_read_b32 v214, a118\n" \
"v_mfma_f32_16x16x32_bf16 a[136:139], v[168:171], v[56:59], a[136:139]\n" \
"v_accvgpr_read_b32 v215, a119\n" \
"v_accvgpr_read_b32 v204, a24\n" \
"v_mfma_f32_16x16x32_bf16 a[136:139], v[164:167], v[52:55], a[136:139]\n" \
"v_accvgpr_read_b32 v205, a25\n" \
"v_accvgpr_read_b32 v206, a26\n" \
"v_mfma_f32_16x16x32_bf16 a[140:143], v[172:175], v[52:55], a[140:143]\n" \
"v_accvgpr_read_b32 v207, a27\n" \
"v_accvgpr_read_b32 v200, a56\n" \
"v_mfma_f32_16x16x32_bf16 a[140:143], v[176:179], v[56:59], a[140:143]\n" \
"v_accvgpr_read_b32 v201, a57\n" \
"v_accvgpr_read_b32 v202, a58\n" \
"v_mfma_f32_16x16x32_bf16 a[160:163], v[152:155], v[64:67], a[160:163]\n" \
"v_accvgpr_read_b32 v203, a59\n" \
"v_accvgpr_read_b32 v208, a88\n" \
"v_mfma_f32_16x16x32_bf16 a[160:163], v[148:151], v[60:63], a[160:163]\n" \
"v_accvgpr_read_b32 v209, a89\n" \
"v_accvgpr_read_b32 v210, a90\n" \
"v_mfma_f32_16x16x32_bf16 a[164:167], v[156:159], v[60:63], a[164:167]\n" \
"v_accvgpr_read_b32 v211, a91\n" \
"v_accvgpr_read_b32 v228, a120\n" \
"v_mfma_f32_16x16x32_bf16 a[164:167], v[160:163], v[64:67], a[164:167]\n" \
"v_accvgpr_read_b32 v229, a121\n" \
"v_accvgpr_read_b32 v230, a122\n" \
"v_mfma_f32_16x16x32_bf16 a[168:171], v[168:171], v[64:67], a[168:171]\n" \
"v_accvgpr_read_b32 v231, a123\n" \
"v_accvgpr_read_b32 v220, a28\n" \
"v_mfma_f32_16x16x32_bf16 a[168:171], v[164:167], v[60:63], a[168:171]\n" \
"v_accvgpr_read_b32 v221, a29\n" \
"v_accvgpr_read_b32 v222, a30\n" \
"v_mfma_f32_16x16x32_bf16 a[172:175], v[172:175], v[60:63], a[172:175]\n" \
"v_accvgpr_read_b32 v223, a31\n" \
"v_accvgpr_read_b32 v216, a60\n" \
"v_mfma_f32_16x16x32_bf16 a[172:175], v[176:179], v[64:67], a[172:175]\n" \
"v_accvgpr_read_b32 v217, a61\n" \
"v_accvgpr_read_b32 v218, a62\n" \
"v_mfma_f32_16x16x32_bf16 a[192:195], v[152:155], v[72:75], a[192:195]\n" \
"v_accvgpr_read_b32 v219, a63\n" \
"v_accvgpr_read_b32 v224, a92\n" \
"v_mfma_f32_16x16x32_bf16 a[192:195], v[148:151], v[68:71], a[192:195]\n" \
"v_accvgpr_read_b32 v225, a93\n" \
"v_accvgpr_read_b32 v226, a94\n" \
"s_waitcnt vmcnt(3)\n" \
"s_barrier\n" \
"s_mov_b64 exec, 3\n" \
"global_load_lds_dword v45, s[86:87]\n" \
"s_mov_b64 exec, -1\n" \
"s_sub_u32 m0, %[mB], 0x10000\n" \
"v_mfma_f32_16x16x32_bf16 a[196:199], v[156:159], v[68:71], a[196:199]\n" \
"ds_read_b64_tr_b16 v[140:141], v39 offset:0\n" \
"ds_read_b64_tr_b16 v[142:143], v39 offset:960\n" \
"v_mfma_f32_16x16x32_bf16 a[196:199], v[160:163], v[72:75], a[196:199]\n" \
"ds_read_b64_tr_b16 v[128:129], v37 offset:4096\n" \
"ds_read_b64_tr_b16 v[130:131], v37 offset:4608\n" \
"v_mfma_f32_16x16x32_bf16 a[200:203], v[168:171], v[72:75], a[200:203]\n" \
"ds_read_b64_tr_b16 v[124:125], v37 offset:0\n" \
"ds_read_b64_tr_b16 v[126:127], v37 offset:512\n" \
"v_mfma_f32_16x16x32_bf16 a[200:203], v[164:167], v[68:71], a[200:203]\n" \
"ds_read_b64_tr_b16 v[132:133], v38 offset:0\n" \
"ds_read_b64_tr_b16 v[134:135], v38 offset:512\n" \
"v_mfma_f32_16x16x32_bf16 a[204:207], v[172:175], v[68:71], a[204:207]\n" \
"ds_read_b64_tr_b16 v[136:137], v38 offset:4096\n" \
"ds_read_b64_tr_b16 v[138:139], v38 offset:4608\n" \
"v_mfma_f32_16x16x32_bf16 a[204:207], v[176:179], v[72:75], a[204:207]\n" \
"ds_read_b128 v[52:55], v28 offset:0\n" \
"ds_read_b128 v[56:59], v29 offset:0\n" \
"v_mfma_f32_16x16x32_bf16 a[224:227], v[152:155], v[80:83], a[224:227]\n" \
"ds_read_b64_tr_b16 v[116:117], v36 offset:0\n" \
"ds_read_b64_tr_b16 v[118:119], v36 offset:512\n" \
"v_mfma_f32_16x16x32_bf16 a[224:227], v[148:151], v[76:79], a[224:227]\n" \
"ds_read_b64_tr_b16 v[120:121], v36 offset:4096\n" \
"ds_read_b64_tr_b16 v[122:123], v36 offset:4608\n" \
"v_mfma_f32_16x16x32_bf16 a[228:231], v[156:159], v[76:79], a[228:231]\n" \
"ds_read_b128 v[60:63], v28 offset:2048\n" \
"ds_read_b128 v[64:67], v29 offset:2048\n" \
"v_mfma_f32_16x16x32_bf16 a[228:231], v[160:163], v[80:83], a[228:231]\n" \
"v_accvgpr_read_b32 v227, a95\n" \
"v_accvgpr_read_b32 v244, a124\n" \
"v_mfma_f32_16x16x32_bf16 a[232:235], v[168:171], v[80:83], a[232:235]\n" \
"ds_read_b128 v[68:71], v28 offset:4096\n" \
"ds_read_b128 v[72:75], v29 offset:4096\n" \
"v_mfma_f32_16x16x32_bf16 a[232:235], v[164:167], v[76:79], a[232:235]\n" \
"v_accvgpr_read_b32 v245, a125\n" \
"v_accvgpr_read_b32 v246, a126\n" \
"v_mfma_f32_16x16x32_bf16 a[236:239], v[172:175], v[76:79], a[236:239]\n" \
"ds_read_b128 v[76:79], v28 offset:6144\n" \
"v_accvgpr_read_b32 v247, a127\n" \
"v_mfma_f32_16x16x32_bf16 a[236:239], v[176:179], v[80:83], a[236:239]\n" \
"ds_read_b128 v[80:83], v29 offset:6144\n" \
"v_accvgpr_read_b32 v236, a0\n" \
"v_mfma_f32_16x16x32_bf16 a[144:147], v[152:155], v[88:91], a[144:147]\n" \
"v_accvgpr_read_b32 v237, a1\n" \
"v_accvgpr_read_b32 v238, a2\n" \
"v_mfma_f32_16x16x32_bf16 a[144:147], v[148:151], v[84:87], a[144:147]\n" \
"v_accvgpr_read_b32 v239, a3\n" \
"v_accvgpr_read_b32 v232, a4\n" \
"v_mfma_f32_16x16x32_bf16 a[148:151], v[156:159], v[84:87], a[148:151]\n" \
"v_accvgpr_read_b32 v233, a5\n" \
"v_accvgpr_read_b32 v234, a6\n" \
"v_mfma_f32_16x16x32_bf16 a[148:151], v[160:163], v[88:91], a[148:151]\n" \
"v_accvgpr_read_b32 v235, a7\n" \
"v_accvgpr_read_b32 v240, a32\n" \
"v_mfma_f32_16x16x32_bf16 a[152:155], v[168:171], v[88:91], a[152:155]\n" \
"v_accvgpr_read_b32 v241, a33\n" \
"v_accvgpr_read_b32 v242, a34\n" \
"v_mfma_f32_16x16x32_bf16 a[152:155], v[164:167], v[84:87], a[152:155]\n" \
"v_accvgpr_read_b32 v243, a35\n" \
"s_mov_b32 s78, -1\n" \
"v_mfma_f32_16x16x32_bf16 a[156:159], v[172:175], v[84:87], a[156:159]\n" \
"ds_read_b128 v[84:87], v22 offset:0\n" \
"s_mov_b32 s79, 0\n" \
"v_mfma_f32_16x16x32_bf16 a[156:159], v[176:179], v[88:91], a[156:159]\n" \
"ds_read_b128 v[88:91], v23 offset:0\n" \
"s_mov_b32 s68, 0xffc0fffc\n" \
"v_mfma_f32_16x16x32_bf16 a[176:179], v[152:155], v[96:99], a[176:179]\n" \
"s_mov_b32 s69, 0xc000fc00\n" \
"v_lshrrev_b32_e32 v24, 9, v20\n" \
"v_mfma_f32_16x16x32_bf16 a[176:179], v[148:151], v[92:95], a[176:179]\n" \
"v_lshrrev_b32_e32 v25, 8, v20\n" \
"v_mul_u32_u24_e32 v24, 0x24800, v24\n" \
"v_mfma_f32_16x16x32_bf16 a[180:183], v[156:159], v[92:95], a[180:183]\n" \
"v_mul_u32_u24_e32 v25, 0x24700, v25\n" \
"v_add_u32_e64 v24, v24, v26\n" \
"v_mfma_f32_16x16x32_bf16 a[180:183], v[160:163], v[96:99], a[180:183]\n" \
"v_add_u32_e32 v25, v25, v20\n" \
"s_add_u32 s85, s50, 0x0\n" \
"v_mfma_f32_16x16x32_bf16 a[184:187], v[168:171], v[96:99], a[184:187]\n" \
"s_add_u32 s87, s50, 0x49000\n" \
"s_mov_b32 s70, 0x3e0293ee\n" \
"v_mfma_f32_16x16x32_bf16 a[184:187], v[164:167], v[92:95], a[184:187]\n" \
"s_mov_b32 s71, 0x3e0293ee\n" \
"v_add_u32_e64 v27, s47, v20\n" \
"v_mfma_f32_16x16x32_bf16 a[188:191], v[172:175], v[92:95], a[188:191]\n" \
"ds_read_b128 v[92:95], v48 offset:2048\n" \
"v_lshrrev_b32_e32 v26, 2, v20\n" \
"v_mfma_f32_16x16x32_bf16 a[188:191], v[176:179], v[96:99], a[188:191]\n" \
"ds_read_b128 v[96:99], v49 offset:2048\n" \
"v_add_u32_e32 v26, s46, v26\n" \
"v_mfma_f32_16x16x32_bf16 a[208:211], v[152:155], v[104:107], a[208:211]\n" \
"v_mfma_f32_16x16x32_bf16 a[208:211], v[148:151], v[100:103], a[208:211]\n" \
"v_mfma_f32_16x16x32_bf16 a[212:215], v[156:159], v[100:103], a[212:215]\n" \
"v_mfma_f32_16x16x32_bf16 a[212:215], v[160:163], v[104:107], a[212:215]\n" \
"v_mfma_f32_16x16x32_bf16 a[216:219], v[168:171], v[104:107], a[216:219]\n" \
"v_mfma_f32_16x16x32_bf16 a[216:219], v[164:167], v[100:103], a[216:219]\n" \
"v_mfma_f32_16x16x32_bf16 a[220:223], v[172:175], v[100:103], a[220:223]\n" \
"ds_read_b128 v[100:103], v48 offset:4096\n" \
"v_mfma_f32_16x16x32_bf16 a[220:223], v[176:179], v[104:107], a[220:223]\n" \
"ds_read_b128 v[104:107], v49 offset:4096\n" \
"v_mfma_f32_16x16x32_bf16 a[240:243], v[152:155], v[112:115], a[240:243]\n" \
"ds_read_b64_tr_b16 v[152:153], v32 offset:8192\n" \
"ds_read_b64_tr_b16 v[154:155], v32 offset:9216\n" \
"v_mfma_f32_16x16x32_bf16 a[240:243], v[148:151], v[108:111], a[240:243]\n" \
"ds_read_b64_tr_b16 v[148:149], v32 offset:0\n" \
"ds_read_b64_tr_b16 v[150:151], v32 offset:1024\n" \
"v_mfma_f32_16x16x32_bf16 a[244:247], v[156:159], v[108:111], a[244:247]\n" \
"ds_read_b64_tr_b16 v[156:157], v33 offset:0\n" \
"ds_read_b64_tr_b16 v[158:159], v33 offset:1024\n" \
"v_mfma_f32_16x16x32_bf16 a[244:247], v[160:163], v[112:115], a[244:247]\n" \
"ds_read_b64_tr_b16 v[160:161], v33 offset:8192\n" \
"ds_read_b64_tr_b16 v[162:163], v33 offset:9216\n" \
"v_mfma_f32_16x16x32_bf16 a[248:251], v[168:171], v[112:115], a[248:251]\n" \
"ds_read_b64_tr_b16 v[168:169], v34 offset:8192\n" \
"ds_read_b64_tr_b16 v[170:171], v34 offset:9216\n" \
"v_mfma_f32_16x16x32_bf16 a[248:251], v[164:167], v[108:111], a[248:251]\n" \
"ds_read_b64_tr_b16 v[164:165], v34 offset:0\n" \
"ds_read_b64_tr_b16 v[166:167], v34 offset:1024\n" \
"v_mfma_f32_16x16x32_bf16 a[252:255], v[172:175], v[108:111], a[252:255]\n" \
"ds_read_b64_tr_b16 v[172:173], v35 offset:0\n" \
"ds_read_b64_tr_b16 v[174:175], v35 offset:1024\n" \
"v_mfma_f32_16x16x32_bf16 a[252:255], v[176:179], v[112:115], a[252:255]\n" \
"ds_read_b64_tr_b16 v[176:177], v35 offset:8192\n" \
"s_waitcnt lgkmcnt(15)\n" \
"s_nop 0\n" \
"v_smfmac_f32_16x16x64_bf16 v[248:251], v[140:143], v[84:91], v50\n" \
"v_mfma_f32_16x16x32_bf16 v[180:183], v[128:131], v[88:91], v[180:183]\n" \
"ds_read_b64_tr_b16 v[178:179], v35 offset:9216\n" \
"v_bfe_u32 v44, v20, 2, 5\n" \
"v_mfma_f32_16x16x32_bf16 v[180:183], v[124:127], v[84:87], v[180:183]\n" \
"buffer_load_dword v44, v44, s[80:83], s39 offen offset:0 sc1\n" \
"v_mfma_f32_16x16x32_bf16 v[252:255], v[84:87], v[132:135], v[252:255]\n" \
"ds_read_b128 v[84:87], v48 offset:0\n" \
"ds_read_b128 v[108:111], v48 offset:6144\n" \
"v_mfma_f32_16x16x32_bf16 v[252:255], v[88:91], v[136:139], v[252:255]\n" \
"ds_read_b128 v[88:91], v49 offset:0\n" \
"ds_read_b128 v[112:115], v49 offset:6144\n" \
"v_smfmac_f32_16x16x64_bf16 v[196:199], v[140:143], v[52:59], v50\n" \
"v_mfma_f32_16x16x32_bf16 v[188:191], v[128:131], v[56:59], v[188:191]\n" \
"v_cvt_pk_bf16_f32 v180, v180, v181\n" \
"v_cvt_pk_bf16_f32 v181, v182, v183\n" \
"v_mfma_f32_16x16x32_bf16 v[188:191], v[124:127], v[52:55], v[188:191]\n" \
"v_add_f32_e64 v249, v249, v251\n" \
"v_cvt_pk_bf16_f32 v182, v249, 0\n" \
"v_mfma_f32_16x16x32_bf16 v[184:187], v[116:119], v[52:55], v[184:187]\n" \
"v_mov_b32_e32 v183, 0x3f80\n" \
"v_add_f32_e64 v196, v196, v198\n" \
"v_mfma_f32_16x16x32_bf16 v[184:187], v[120:123], v[56:59], v[184:187]\n" \
"v_mfma_f32_16x16x16_bf16 v[248:251], v[182:183], v[46:47], 0\n" \
"v_add_f32_e64 v197, v197, v199\n" \
"v_cvt_pk_bf16_f32 v188, v188, v189\n" \
"v_mfma_f32_16x16x32_bf16 v[192:195], v[56:59], v[136:139], v[192:195]\n" \
"v_cvt_pk_bf16_f32 v189, v190, v191\n" \
"v_cvt_pk_bf16_f32 v190, v197, 0\n" \
"v_mfma_f32_16x16x32_bf16 v[192:195], v[52:55], v[132:135], v[192:195]\n" \
"v_mov_b32_e32 v191, 0x3f80\n" \
"v_cvt_pk_bf16_f32 v184, v184, v185\n" \
"v_smfmac_f32_16x16x64_bf16 v[212:215], v[140:143], v[60:67], v50\n" \
"v_mfma_f32_16x16x32_bf16 v[204:207], v[124:127], v[60:63], v[204:207]\n" \
"v_cvt_pk_bf16_f32 v185, v186, v187\n" \
"v_cndmask_b32_e64 v186, 0, v196, s[78:79]\n" \
"v_mfma_f32_16x16x32_bf16 v[204:207], v[128:131], v[64:67], v[204:207]\n" \
"v_mov_b32_e64 v187, 0\n" \
"v_cvt_pk_bf16_f32 v186, v186, 0\n" \
"v_mfma_f32_16x16x32_bf16 v[200:203], v[120:123], v[64:67], v[200:203]\n" \
"v_mfma_f32_16x16x32_bf16 v[200:203], v[116:119], v[60:63], v[200:203]\n" \
"v_add_f32_e32 v212, v212, v214\n" \
"v_add_f32_e32 v213, v213, v215\n" \
"v_mfma_f32_16x16x32_bf16 v[208:211], v[60:63], v[132:135], v[208:211]\n" \
"v_mfma_f32_16x16x32_bf16 v[208:211], v[64:67], v[136:139], v[208:211]\n" \
"v_cvt_pk_bf16_f32 v204, v204, v205\n" \
"v_cvt_pk_bf16_f32 v205, v206, v207\n" \
"v_smfmac_f32_16x16x64_bf16 v[228:231], v[140:143], v[68:75], v50\n" \
"v_mfma_f32_16x16x32_bf16 v[220:223], v[128:131], v[72:75], v[220:223]\n" \
"v_cvt_pk_bf16_f32 v200, v200, v201\n" \
"v_cvt_pk_bf16_f32 v201, v202, v203\n" \
"v_mfma_f32_16x16x32_bf16 v[220:223], v[124:127], v[68:71], v[220:223]\n" \
"v_cndmask_b32_e64 v202, 0, v212, s[78:79]\n" \
"v_cvt_pk_bf16_f32 v206, v213, 0\n" \
"v_mfma_f32_16x16x32_bf16 v[216:219], v[116:119], v[68:71], v[216:219]\n" \
"v_mov_b32_e32 v207, 0x3f80\n" \
"v_add_f32_e64 v228, v228, v230\n" \
"v_mfma_f32_16x16x32_bf16 v[216:219], v[120:123], v[72:75], v[216:219]\n" \
"v_add_f32_e32 v229, v229, v231\n" \
"v_mov_b32_e32 v203, 0\n" \
"v_mfma_f32_16x16x32_bf16 v[224:227], v[72:75], v[136:139], v[224:227]\n" \
"v_cvt_pk_bf16_f32 v220, v220, v221\n" \
"v_cvt_pk_bf16_f32 v221, v222, v223\n" \
"v_mfma_f32_16x16x32_bf16 v[224:227], v[68:71], v[132:135], v[224:227]\n" \
"v_cvt_pk_bf16_f32 v202, v202, 0\n" \
"v_cvt_pk_bf16_f32 v222, v229, 0\n" \
"v_smfmac_f32_16x16x64_bf16 v[244:247], v[140:143], v[76:83], v50\n" \
"v_mfma_f32_16x16x32_bf16 v[236:239], v[124:127], v[76:79], v[236:239]\n" \
"v_cvt_pk_bf16_f32 v216, v216, v217\n" \
"v_cvt_pk_bf16_f32 v217, v218, v219\n" \
"v_mfma_f32_16x16x32_bf16 v[236:239], v[128:131], v[80:83], v[236:239]\n" \
"v_cndmask_b32_e64 v218, 0, v228, s[78:79]\n" \
"v_cvt_pk_bf16_f32 v126, v192, v193\n" \
"v_mfma_f32_16x16x32_bf16 v[232:235], v[120:123], v[80:83], v[232:235]\n" \
"v_cvt_pk_bf16_f32 v127, v194, v195\n" \
"v_add_f32_e64 v244, v244, v246\n" \
"v_mfma_f32_16x16x32_bf16 v[232:235], v[116:119], v[76:79], v[232:235]\n" \
"v_add_f32_e64 v245, v245, v247\n" \
"v_cvt_pk_bf16_f32 v128, v208, v209\n" \
"v_mfma_f32_16x16x32_bf16 v[240:243], v[76:79], v[132:135], v[240:243]\n" \
"v_cvt_pk_bf16_f32 v236, v236, v237\n" \
"v_cvt_pk_bf16_f32 v237, v238, v239\n" \
"v_mfma_f32_16x16x32_bf16 v[240:243], v[80:83], v[136:139], v[240:243]\n" \
"v_cvt_pk_bf16_f32 v129, v210, v211\n" \
"v_mov_b32_e32 v223, 0x3f80\n" \
"s_waitcnt lgkmcnt(0)\n" \
"s_barrier\n" \
"v_mfma_f32_16x16x32_bf16 a[128:131], v[152:155], v[56:59], a[128:131]\n" \
"v_cvt_pk_bf16_f32 v232, v232, v233\n" \
"v_cvt_pk_bf16_f32 v233, v234, v235\n" \
"v_mfma_f32_16x16x32_bf16 a[128:131], v[148:151], v[52:55], a[128:131]\n" \
"v_cndmask_b32_e64 v234, 0, v244, s[78:79]\n" \
"v_mov_b32_e64 v219, 0\n" \
"v_mfma_f32_16x16x32_bf16 a[132:135], v[156:159], v[52:55], a[132:135]\n" \
"v_cvt_pk_bf16_f32 v218, v218, 0\n" \
"v_cvt_pk_bf16_f32 v130, v224, v225\n" \
"v_mfma_f32_16x16x32_bf16 a[132:135], v[160:163], v[56:59], a[132:135]\n" \
"v_cvt_pk_bf16_f32 v131, v226, v227\n" \
"v_cvt_pk_bf16_f32 v238, v245, 0\n" \
"v_mfma_f32_16x16x32_bf16 a[136:139], v[168:171], v[56:59], a[136:139]\n" \
"v_mov_b32_e32 v239, 0x3f80\n" \
"v_mov_b32_e64 v235, 0\n" \
"v_mfma_f32_16x16x32_bf16 a[136:139], v[164:167], v[52:55], a[136:139]\n" \
"v_cvt_pk_bf16_f32 v234, v234, 0\n" \
"v_cvt_pk_bf16_f32 v132, v240, v241\n" \
"v_mfma_f32_16x16x32_bf16 a[140:143], v[172:175], v[52:55], a[140:143]\n" \
"v_cvt_pk_bf16_f32 v133, v242, v243\n" \
"v_cvt_pk_bf16_f32 v134, v248, v249\n" \
"v_mfma_f32_16x16x32_bf16 a[140:143], v[176:179], v[56:59], a[140:143]\n" \
"v_mfma_f32_16x16x32_bf16 v[116:119], v[180:183], v[184:187], 0\n" \
"v_mfma_f32_16x16x32_bf16 v[192:195], v[188:191], v[184:187], 0\n" \
"v_mfma_f32_16x16x32_bf16 v[208:211], v[204:207], v[200:203], 0\n" \
"v_mfma_f32_16x16x32_bf16 v[120:123], v[188:191], v[200:203], 0\n" \
"s_waitcnt vmcnt(0)\n" \
"v_cmp_ne_u32_e32 vcc, %[tag], v44\n" \
"v_mfma_f32_16x16x32_bf16 a[160:163], v[152:155], v[64:67], a[160:163]\n" \
"v_mfma_f32_16x16x32_bf16 v[224:227], v[220:223], v[216:219], 0\n" \
"v_mfma_f32_16x16x32_bf16 v[36:39], v[204:207], v[216:219], 0\n" \
"v_mfma_f32_16x16x32_bf16 v[240:243], v[236:239], v[232:235], 0\n" \
"v_mfma_f32_16x16x32_bf16 v[48:51], v[220:223], v[232:235], 0\n" \
"s_cbranch_vccnz L_pa_%=\n" \
"L_ra_%=:\n" \
"v_cndmask_b32_e64 v116, v116, v117, s[64:65]\n" \
"buffer_load_dwordx4 v20, s[80:83], s73 offen offset:0 sc1 lds\n" \
"v_mfma_f32_16x16x32_bf16 a[160:163], v[148:151], v[60:63], a[160:163]\n" \
"v_cndmask_b32_e64 v118, v118, v119, s[64:65]\n" \
"v_cndmask_b32_e64 v192, v192, v193, s[64:65]\n" \
"v_mfma_f32_16x16x32_bf16 a[164:167], v[156:159], v[60:63], a[164:167]\n" \
"v_add_f32_e32 v116, s49, v116\n" \
"v_cndmask_b32_e64 v194, v194, v195, s[64:65]\n" \
"v_mfma_f32_16x16x32_bf16 a[164:167], v[160:163], v[64:67], a[164:167]\n" \
"v_add_f32_e64 v118, s49, v118\n" \
"v_cndmask_b32_e64 v208, v208, v209, s[64:65]\n" \
"v_mfma_f32_16x16x32_bf16 a[168:171], v[168:171], v[64:67], a[168:171]\n" \
"buffer_load_dwordx4 v20, s[80:83], s73 offen offset:1024 sc1 lds\n" \
"v_mfma_f32_16x16x32_bf16 a[168:171], v[164:167], v[60:63], a[168:171]\n" \
"v_cndmask_b32_e64 v120, v120, v121, s[64:65]\n" \
"v_cndmask_b32_e64 v210, v210, v211, s[64:65]\n" \
"v_mfma_f32_16x16x32_bf16 a[172:175], v[172:175], v[60:63], a[172:175]\n" \
"v_cndmask_b32_e64 v122, v122, v123, s[64:65]\n" \
"v_cndmask_b32_e64 v224, v224, v225, s[64:65]\n" \
"v_mfma_f32_16x16x32_bf16 a[172:175], v[176:179], v[64:67], a[172:175]\n" \
"v_cndmask_b32_e64 v36, v36, v37, s[64:65]\n" \
"v_cndmask_b32_e64 v226, v226, v227, s[64:65]\n" \
"v_mfma_f32_16x16x32_bf16 a[192:195], v[152:155], v[72:75], a[192:195]\n" \
"buffer_load_dwordx4 v20, s[80:83], s73 offen offset:2048 sc1 lds\n" \
"v_mfma_f32_16x16x32_bf16 a[192:195], v[148:151], v[68:71], a[192:195]\n" \
"v_cndmask_b32_e64 v38, v38, v39, s[64:65]\n" \
"v_cndmask_b32_e64 v240, v240, v241, s[64:65]\n" \
"v_mfma_f32_16x16x32_bf16 a[196:199], v[156:159], v[68:71], a[196:199]\n" \
"v_cndmask_b32_e64 v48, v48, v49, s[64:65]\n" \
"v_cndmask_b32_e64 v242, v242, v243, s[64:65]\n" \
"v_mfma_f32_16x16x32_bf16 a[196:199], v[160:163], v[72:75], a[196:199]\n" \
"v_cndmask_b32_e64 v50, v50, v51, s[64:65]\n" \
"v_cndmask_b32_e64 v192, v116, v192, s[66:67]\n" \
"v_mfma_f32_16x16x32_bf16 a[200:203], v[168:171], v[72:75], a[200:203]\n" \
"buffer_load_dwordx4 v20, s[80:83], s73 offen offset:3072 sc1 lds\n" \
"v_mfma_f32_16x16x32_bf16 a[200:203], v[164:167], v[68:71], a[200:203]\n" \
"s_sub_u32 m0, %[mB], s53\n" \
"s_nop 0\n" \
"v_cndmask_b32_e64 v194, v118, v194, s[68:69]\n" \
"v_mfma_f32_16x16x32_bf16 a[204:207], v[172:175], v[68:71], a[204:207]\n" \
"v_cndmask_b32_e64 v208, v120, v208, s[66:67]\n" \
"v_cndmask_b32_e64 v210, v122, v210, s[68:69]\n" \
"v_mfma_f32_16x16x32_bf16 a[204:207], v[176:179], v[72:75], a[204:207]\n" \
"v_cndmask_b32_e64 v224, v36, v224, s[66:67]\n" \
"v_cndmask_b32_e64 v226, v38, v226, s[68:69]\n" \
"v_mfma_f32_16x16x32_bf16 a[224:227], v[152:155], v[80:83], a[224:227]\n" \
"buffer_load_dwordx4 v20, s[80:83], s74 offen offset:0 sc1 lds\n" \
"v_mfma_f32_16x16x32_bf16 a[224:227], v[148:151], v[76:79], a[224:227]\n" \
"v_cndmask_b32_e64 v240, v48, v240, s[66:67]\n" \
"v_cndmask_b32_e64 v242, v50, v242, s[68:69]\n" \
"v_mfma_f32_16x16x32_bf16 a[228:231], v[156:159], v[76:79], a[228:231]\n" \
"v_max_f32_e32 v193, v192, v194\n" \
"v_max_f32_e32 v209, v208, v210\n" \
"v_mfma_f32_16x16x32_bf16 a[228:231], v[160:163], v[80:83], a[228:231]\n" \
"v_max_f32_e32 v225, v224, v226\n" \
"v_max_f32_e32 v241, v240, v242\n" \
"v_mfma_f32_16x16x32_bf16 a[232:235], v[168:171], v[80:83], a[232:235]\n" \
"buffer_load_dwordx4 v20, s[80:83], s74 offen offset:1024 sc1 lds\n" \
"v_mfma_f32_16x16x32_bf16 a[232:235], v[164:167], v[76:79], a[232:235]\n" \
"v_permlane32_swap_b32_e32 v193, v225\n" \
"v_permlane32_swap_b32_e32 v209, v241\n" \
"v_mfma_f32_16x16x32_bf16 a[236:239], v[172:175], v[76:79], a[236:239]\n" \
"v_max_f32_e32 v193, v193, v225\n" \
"v_max_f32_e32 v209, v209, v241\n" \
"v_permlane32_swap_b32_e32 v196, v228\n" \
"v_permlane32_swap_b32_e32 v212, v244\n" \
"buffer_load_dwordx4 v20, s[80:83], s74 offen offset:2048 sc1 lds\n" \
"v_permlane16_swap_b32_e32 v193, v209\n" \
"v_permlane16_swap_b32_e32 v228, v244\n" \
"v_mfma_f32_16x16x32_bf16 a[236:239], v[176:179], v[80:83], a[236:239]\n" \
"v_max_f32_e32 v193, v193, v209\n" \
"v_mul_f32_e32 v193, s72, v193\n" \
"v_mfma_f32_16x16x32_bf16 a[144:147], v[152:155], v[88:91], a[144:147]\n" \
"v_mul_f32_e32 v45, 0xbfb8aa3b, v228\n" \
"v_mov_b32_e64 v209, v193\n" \
"v_mfma_f32_16x16x16_bf16 v[196:199], v[190:191], v[46:47], 0\n" \
"buffer_load_dwordx4 v20, s[80:83], s74 offen offset:3072 sc1 lds\n" \
"v_mfma_f32_16x16x32_bf16 a[144:147], v[148:151], v[84:87], a[144:147]\n" \
"s_sub_u32 m0, %[mB], 0x4000\n" \
"v_cvt_pk_bf16_f32 v135, v250, v251\n" \
"v_mfma_f32_16x16x16_bf16 v[212:215], v[206:207], v[46:47], 0\n" \
"v_mfma_f32_16x16x16_bf16 v[228:231], v[222:223], v[46:47], 0\n" \
"v_permlane16_swap_b32_e32 v193, v209\n" \
"v_mfma_f32_16x16x16_bf16 v[244:247], v[238:239], v[46:47], 0\n" \
"buffer_load_dwordx4 v20, s[80:83], s75 offen offset:0 sc1 lds\n" \
"v_mfma_f32_16x16x32_bf16 a[148:151], v[156:159], v[84:87], a[148:151]\n" \
"v_mov_b32_e32 v225, v193\n" \
"v_mov_b32_e64 v241, v209\n" \
"v_mfma_f32_16x16x32_bf16 a[148:151], v[160:163], v[88:91], a[148:151]\n" \
"v_permlane32_swap_b32_e32 v193, v225\n" \
"v_permlane32_swap_b32_e32 v209, v241\n" \
"v_mfma_f32_16x16x32_bf16 a[152:155], v[168:171], v[88:91], a[152:155]\n" \
"v_fma_f32 v192, v192, s72, -v193\n" \
"v_fma_f32 v194, v194, s72, -v193\n" \
"buffer_load_dwordx4 v20, s[80:83], s75 offen offset:1024 sc1 lds\n" \
"v_mfma_f32_16x16x32_bf16 a[152:155], v[164:167], v[84:87], a[152:155]\n" \
"v_fma_f32 v208, v208, s72, -v209\n" \
"v_fma_f32 v210, v210, s72, -v209\n" \
"v_exp_f32_e32 v192, v192\n" \
"v_exp_f32_e32 v194, v194\n" \
"v_exp_f32_e32 v208, v208\n" \
"v_exp_f32_e32 v210, v210\n" \
"v_mfma_f32_16x16x32_bf16 a[156:159], v[172:175], v[84:87], a[156:159]\n" \
"buffer_load_dwordx4 v20, s[80:83], s75 offen offset:2048 sc1 lds\n" \
"v_mfma_f32_16x16x32_bf16 a[156:159], v[176:179], v[88:91], a[156:159]\n" \
"v_fma_f32 v224, v224, s72, -v225\n" \
"v_fma_f32 v226, v226, s72, -v225\n" \
"v_exp_f32_e32 v224, v224\n" \
"v_exp_f32_e32 v226, v226\n" \
"v_mfma_f32_16x16x32_bf16 a[176:179], v[152:155], v[96:99], a[176:179]\n" \
"v_fma_f32 v240, v240, s72, -v241\n" \
"v_fma_f32 v242, v242, s72, -v241\n" \
"v_mfma_f32_16x16x32_bf16 a[176:179], v[148:151], v[92:95], a[176:179]\n" \
"buffer_load_dwordx4 v20, s[80:83], s75 offen offset:3072 sc1 lds\n" \
"v_mfma_f32_16x16x32_bf16 a[180:183], v[156:159], v[92:95], a[180:183]\n" \
"s_mov_b32 m0, s55\n" \
"v_cvt_pk_bf16_f32 v192, v192, v194\n" \
"v_mfma_f32_16x16x32_bf16 a[180:183], v[160:163], v[96:99], a[180:183]\n" \
"v_cvt_pk_bf16_f32 v208, v208, v210\n" \
"v_cvt_pk_bf16_f32 v136, v196, v197\n" \
"v_exp_f32_e32 v240, v240\n" \
"v_exp_f32_e64 v242, v242\n" \
"v_mfma_f32_16x16x32_bf16 a[184:187], v[168:171], v[96:99], a[184:187]\n" \
"buffer_load_dwordx4 v20, s[80:83], s76 offen offset:0 sc1 lds\n" \
"v_mfma_f32_16x16x32_bf16 a[184:187], v[164:167], v[92:95], a[184:187]\n" \
"L_pf9:\n" \
"s_cbranch_execz L_pf13\n" \
"s_nop 0\n" \
"v_cvt_pk_bf16_f32 v137, v198, v199\n" \
"v_cvt_pk_bf16_f32 v138, v212, v213\n" \
"v_mfma_f32_16x16x32_bf16 a[188:191], v[172:175], v[92:95], a[188:191]\n" \
"v_cvt_pk_bf16_f32 v139, v214, v215\n" \
"v_perm_b32 v184, v192, v192, v40\n" \
"v_mfma_f32_16x16x32_bf16 a[188:191], v[176:179], v[96:99], a[188:191]\n" \
"v_perm_b32 v185, v192, v192, v41\n" \
"v_perm_b32 v186, v192, v192, v42\n" \
"v_perm_b32 v187, v192, v192, v43\n" \
"buffer_load_dwordx4 v20, s[80:83], s76 offen offset:1024 sc1 lds\n" \
"v_perm_b32 v200, v208, v208, v40\n" \
"v_perm_b32 v201, v208, v208, v41\n" \
"v_perm_b32 v202, v208, v208, v42\n" \
"v_perm_b32 v203, v208, v208, v43\n" \
"v_cvt_pk_bf16_f32 v224, v224, v226\n" \
"v_cvt_pk_bf16_f32 v140, v228, v229\n" \
"v_cvt_pk_bf16_f32 v141, v230, v231\n" \
"v_mfma_f32_16x16x32_bf16 v[116:119], v[134:137], v[184:187], 0\n" \
"v_mfma_f32_16x16x32_bf16 v[120:123], v[136:139], v[200:203], 0\n" \
"v_perm_b32 v216, v224, v224, v40\n" \
"v_perm_b32 v217, v224, v224, v41\n" \
"v_perm_b32 v218, v224, v224, v42\n" \
"v_perm_b32 v219, v224, v224, v43\n" \
"v_cvt_pk_bf16_f32 v240, v240, v242\n" \
"v_cvt_pk_bf16_f32 v142, v244, v245\n" \
"v_cvt_pk_bf16_f32 v143, v246, v247\n" \
"v_mfma_f32_16x16x32_bf16 v[36:39], v[138:141], v[216:219], 0\n" \
"v_perm_b32 v232, v240, v240, v40\n" \
"v_perm_b32 v233, v240, v240, v41\n" \
"v_perm_b32 v234, v240, v240, v42\n" \
"v_perm_b32 v235, v240, v240, v43\n" \
"v_exp_f32_e64 v45, v45\n" \
"v_mov_b32_dpp v118, v122 quad_perm:[0,1,2,3] row_mask:0x2 bank_mask:0xf\n" \
"v_mfma_f32_16x16x32_bf16 v[48:51], v[140:143], v[232:235], 0\n" \
"v_cvt_pk_bf16_f32 v124, v252, v253\n" \
"v_cvt_pk_bf16_f32 v125, v254, v255\n" \
"v_mov_b32_dpp v118, v38 quad_perm:[0,1,2,3] row_mask:0x4 bank_mask:0xf\n" \
"v_add_f32_e64 v45, 1.0, v45\n" \
"v_mfma_f32_16x16x32_bf16 v[192:195], v[124:127], v[184:187], 0\n" \
"v_mfma_f32_16x16x32_bf16 v[224:227], v[128:131], v[216:219], 0\n" \
"v_rcp_f32_e64 v45, v45\n" \
"v_mov_b32_dpp v116, v120 quad_perm:[0,1,2,3] row_mask:0x2 bank_mask:0xf\n" \
"v_mov_b32_dpp v118, v50 quad_perm:[0,1,2,3] row_mask:0x8 bank_mask:0xf\n" \
"v_mov_b32_dpp v117, v121 quad_perm:[0,1,2,3] row_mask:0x2 bank_mask:0xf\n" \
"v_rcp_f32_e64 v118, v118\n" \
"v_mfma_f32_16x16x32_bf16 v[208:211], v[126:129], v[200:203], 0\n" \
"v_mfma_f32_16x16x32_bf16 v[240:243], v[130:133], v[232:235], 0\n" \
"v_mov_b32_dpp v116, v36 quad_perm:[0,1,2,3] row_mask:0x4 bank_mask:0xf\n" \
"v_mov_b32_dpp v117, v37 quad_perm:[0,1,2,3] row_mask:0x4 bank_mask:0xf\n" \
"v_mul_f32_e64 v118, v45, v118\n" \
"v_mov_b32_dpp v116, v48 quad_perm:[0,1,2,3] row_mask:0x8 bank_mask:0xf\n" \
"v_mov_b32_dpp v117, v49 quad_perm:[0,1,2,3] row_mask:0x8 bank_mask:0xf\n" \
"v_mul_f32_e32 v116, v118, v116\n" \
"v_mul_f32_e32 v117, v118, v117\n" \
"buffer_store_dwordx4 a[12:15], v20, s[80:83], s89 offen offset:1024 sc1\n" \
"s_add_u32 s89, s45, 0x0\n" \
"v_mov_b32_e32 v122, v118\n" \
"v_cvt_pk_bf16_f32 v116, v116, v117\n" \
"v_accvgpr_read_b32 v52, a128\n" \
"v_permlane16_swap_b32_e32 v118, v122\n" \
"v_mov_b32_e32 v38, v118\n" \
"v_mov_b32_e64 v50, v122\n" \
"v_accvgpr_read_b32 v53, a129\n" \
"v_permlane32_swap_b32_e32 v118, v38\n" \
"v_permlane32_swap_b32_e32 v122, v50\n" \
"v_mul_f32_e32 v192, v118, v192\n" \
"v_mul_f32_e32 v193, v118, v193\n" \
"v_mul_f32_e32 v224, v38, v224\n" \
"v_mul_f32_e32 v225, v38, v225\n" \
"v_mul_f32_e32 v194, v118, v194\n" \
"v_mul_f32_e32 v195, v118, v195\n" \
"v_cvt_pk_bf16_f32 v192, v192, v193\n" \
"v_mul_f32_e32 v226, v38, v226\n" \
"v_mul_f32_e32 v227, v38, v227\n" \
"v_cvt_pk_bf16_f32 v224, v224, v225\n" \
"v_cvt_pk_bf16_f32 v193, v194, v195\n" \
"v_mul_f32_e32 v208, v122, v208\n" \
"v_mul_f32_e32 v209, v122, v209\n" \
"v_mul_f32_e32 v210, v122, v210\n" \
"v_mul_f32_e32 v211, v122, v211\n" \
"v_cvt_pk_bf16_f32 v225, v226, v227\n" \
"v_mul_f32_e32 v240, v50, v240\n" \
"v_mul_f32_e32 v241, v50, v241\n" \
"v_mul_f32_e32 v242, v50, v242\n" \
"v_mul_f32_e32 v243, v50, v243\n" \
"v_cvt_pk_bf16_f32 v194, v208, v209\n" \
"v_cvt_pk_bf16_f32 v195, v210, v211\n" \
"v_cvt_pk_bf16_f32 v226, v240, v241\n" \
"v_cvt_pk_bf16_f32 v227, v242, v243\n" \
"v_permlane32_swap_b32_e32 v192, v194\n" \
"v_permlane32_swap_b32_e32 v193, v195\n" \
"v_permlane32_swap_b32_e32 v224, v226\n" \
"v_permlane32_swap_b32_e32 v225, v227\n" \
"v_accvgpr_read_b32 v54, a130\n" \
"v_accvgpr_read_b32 v55, a131\n" \
"v_pk_mul_f32 v[52:53], v[52:53], s[70:71]\n" \
"v_accvgpr_read_b32 v56, a132\n" \
"v_accvgpr_read_b32 v57, a133\n" \
"v_accvgpr_read_b32 v58, a134\n" \
"v_accvgpr_read_b32 v59, a135\n" \
"v_pk_mul_f32 v[54:55], v[54:55], s[70:71]\n" \
"v_cvt_pk_bf16_f32 v52, v52, v53\n" \
"v_pk_mul_f32 v[56:57], v[56:57], s[70:71]\n" \
"v_pk_mul_f32 v[58:59], v[58:59], s[70:71]\n" \
"v_cvt_pk_bf16_f32 v53, v54, v55\n" \
"v_cvt_pk_bf16_f32 v54, v56, v57\n" \
"v_cvt_pk_bf16_f32 v55, v58, v59\n" \
"ds_write_b128 v27, v[52:55] offset:0\n" \
"buffer_store_dwordx4 a[40:43], v20, s[80:83], s88 offen offset:0 sc1\n" \
"s_add_u32 s88, s45, 0x0\n" \
"s_nop 0\n" \
"buffer_store_dword v116, v25, s[80:83], s51 offen sc1\n" \
"buffer_store_dwordx4 v[192:195], v24, s[80:83], s85 offen sc1\n" \
"buffer_store_dwordx4 v[224:227], v24, s[80:83], s87 offen sc1\n" \
"v_accvgpr_read_b32 v76, a224\n" \
"v_accvgpr_read_b32 v77, a225\n" \
"v_accvgpr_read_b32 v78, a226\n" \
"v_accvgpr_read_b32 v79, a227\n" \
"v_pk_mul_f32 v[76:77], v[76:77], s[70:71]\n" \
"v_accvgpr_read_b32 v52, a136\n" \
"v_accvgpr_read_b32 v53, a137\n" \
"v_accvgpr_read_b32 v80, a228\n" \
"v_accvgpr_read_b32 v81, a229\n" \
"v_accvgpr_read_b32 v82, a230\n" \
"v_accvgpr_read_b32 v83, a231\n" \
"v_pk_mul_f32 v[78:79], v[78:79], s[70:71]\n" \
"v_cvt_pk_bf16_f32 v76, v76, v77\n" \
"v_accvgpr_read_b32 v54, a138\n" \
"v_accvgpr_read_b32 v55, a139\n" \
"v_pk_mul_f32 v[52:53], v[52:53], s[70:71]\n" \
"v_pk_mul_f32 v[80:81], v[80:81], s[70:71]\n" \
"v_pk_mul_f32 v[82:83], v[82:83], s[70:71]\n" \
"v_cvt_pk_bf16_f32 v77, v78, v79\n" \
"v_accvgpr_read_b32 v56, a140\n" \
"v_accvgpr_read_b32 v57, a141\n" \
"v_accvgpr_read_b32 v58, a142\n" \
"v_accvgpr_read_b32 v59, a143\n" \
"v_pk_mul_f32 v[54:55], v[54:55], s[70:71]\n" \
"v_cvt_pk_bf16_f32 v52, v52, v53\n" \
"v_cvt_pk_bf16_f32 v78, v80, v81\n" \
"v_cvt_pk_bf16_f32 v79, v82, v83\n" \
"ds_write_b128 v27, v[76:79] offset:12288\n" \
"v_pk_mul_f32 v[56:57], v[56:57], s[70:71]\n" \
"v_pk_mul_f32 v[58:59], v[58:59], s[70:71]\n" \
"v_cvt_pk_bf16_f32 v53, v54, v55\n" \
"v_cvt_pk_bf16_f32 v54, v56, v57\n" \
"v_cvt_pk_bf16_f32 v55, v58, v59\n" \
"ds_write_b128 v27, v[52:55] offset:1024\n" \
"v_accvgpr_read_b32 v76, a208\n" \
"v_accvgpr_read_b32 v77, a209\n" \
"v_accvgpr_read_b32 v78, a210\n" \
"v_accvgpr_read_b32 v79, a211\n" \
"v_accvgpr_read_b32 v52, a168\n" \
"v_accvgpr_read_b32 v53, a169\n" \
"v_mfma_f32_16x16x32_bf16 v[76:79], v[152:155], v[104:107], v[76:79]\n" \
"v_accvgpr_read_b32 v54, a170\n" \
"v_accvgpr_read_b32 v55, a171\n" \
"v_mfma_f32_16x16x32_bf16 v[76:79], v[148:151], v[100:103], v[76:79]\n" \
"v_accvgpr_read_b32 v56, a172\n" \
"v_accvgpr_read_b32 v57, a173\n" \
"v_accvgpr_read_b32 v58, a174\n" \
"v_accvgpr_read_b32 v59, a175\n" \
"v_pk_mul_f32 v[52:53], v[52:53], s[70:71]\n" \
"v_pk_mul_f32 v[54:55], v[54:55], s[70:71]\n" \
"v_cvt_pk_bf16_f32 v52, v52, v53\n" \
"v_pk_mul_f32 v[56:57], v[56:57], s[70:71]\n" \
"v_pk_mul_f32 v[58:59], v[58:59], s[70:71]\n" \
"buffer_store_dwordx4 a[44:47], v20, s[80:83], s89 offen offset:1024 sc1\n" \
"s_add_u32 s89, s45, 0x0\n" \
"s_nop 0\n" \
"v_cvt_pk_bf16_f32 v53, v54, v55\n" \
"v_cvt_pk_bf16_f32 v54, v56, v57\n" \
"v_cvt_pk_bf16_f32 v55, v58, v59\n" \
"ds_write_b128 v27, v[52:55] offset:5120\n" \
"v_accvgpr_read_b32 v80, a212\n" \
"v_accvgpr_read_b32 v81, a213\n" \
"v_accvgpr_read_b32 v82, a214\n" \
"v_accvgpr_read_b32 v83, a215\n" \
"v_accvgpr_read_b32 v52, a232\n" \
"v_accvgpr_read_b32 v53, a233\n" \
"v_mfma_f32_16x16x32_bf16 v[80:83], v[156:159], v[100:103], v[80:83]\n" \
"v_accvgpr_read_b32 v54, a234\n" \
"v_accvgpr_read_b32 v55, a235\n" \
"v_mfma_f32_16x16x32_bf16 v[80:83], v[160:163], v[104:107], v[80:83]\n" \
"v_accvgpr_read_b32 v56, a236\n" \
"v_accvgpr_read_b32 v57, a237\n" \
"v_accvgpr_read_b32 v58, a238\n" \
"v_accvgpr_read_b32 v59, a239\n" \
"v_pk_mul_f32 v[52:53], v[52:53], s[70:71]\n" \
"v_pk_mul_f32 v[54:55], v[54:55], s[70:71]\n" \
"v_cvt_pk_bf16_f32 v52, v52, v53\n" \
"v_pk_mul_f32 v[56:57], v[56:57], s[70:71]\n" \
"v_pk_mul_f32 v[58:59], v[58:59], s[70:71]\n" \
"v_cvt_pk_bf16_f32 v53, v54, v55\n" \
"v_cvt_pk_bf16_f32 v54, v56, v57\n" \
"v_cvt_pk_bf16_f32 v55, v58, v59\n" \
"ds_write_b128 v27, v[52:55] offset:13312\n" \
"v_accvgpr_read_b32 v52, a216\n" \
"v_accvgpr_read_b32 v53, a217\n" \
"v_accvgpr_read_b32 v54, a218\n" \
"v_accvgpr_read_b32 v55, a219\n" \
"v_accvgpr_read_b32 v60, a160\n" \
"v_accvgpr_read_b32 v61, a161\n" \
"v_mfma_f32_16x16x32_bf16 v[52:55], v[168:171], v[104:107], v[52:55]\n" \
"v_accvgpr_read_b32 v62, a162\n" \
"v_accvgpr_read_b32 v63, a163\n" \
"v_mfma_f32_16x16x32_bf16 v[52:55], v[164:167], v[100:103], v[52:55]\n" \
"v_accvgpr_read_b32 v56, a220\n" \
"v_accvgpr_read_b32 v57, a221\n" \
"v_accvgpr_read_b32 v58, a222\n" \
"v_accvgpr_read_b32 v59, a223\n" \
"v_pk_mul_f32 v[60:61], v[60:61], s[70:71]\n" \
"v_accvgpr_read_b32 v64, a164\n" \
"v_mfma_f32_16x16x32_bf16 v[56:59], v[172:175], v[100:103], v[56:59]\n" \
"v_accvgpr_read_b32 v65, a165\n" \
"v_accvgpr_read_b32 v66, a166\n" \
"v_mfma_f32_16x16x32_bf16 v[56:59], v[176:179], v[104:107], v[56:59]\n" \
"v_accvgpr_read_b32 v67, a167\n" \
"buffer_store_dwordx4 a[72:75], v20, s[80:83], s88 offen offset:2048 sc1\n" \
"s_add_u32 s88, s45, 0x1000\n" \
"v_pk_mul_f32 v[62:63], v[62:63], s[70:71]\n" \
"v_cvt_pk_bf16_f32 v60, v60, v61\n" \
"v_pk_mul_f32 v[64:65], v[64:65], s[70:71]\n" \
"v_pk_mul_f32 v[66:67], v[66:67], s[70:71]\n" \
"v_cvt_pk_bf16_f32 v61, v62, v63\n" \
"v_cvt_pk_bf16_f32 v62, v64, v65\n" \
"v_cvt_pk_bf16_f32 v63, v66, v67\n" \
"ds_write_b128 v27, v[60:63] offset:4096\n" \
"v_accvgpr_read_b32 v68, a192\n" \
"v_accvgpr_read_b32 v69, a193\n" \
"v_accvgpr_read_b32 v60, a200\n" \
"v_accvgpr_read_b32 v61, a201\n" \
"v_accvgpr_read_b32 v70, a194\n" \
"v_accvgpr_read_b32 v71, a195\n" \
"v_pk_mul_f32 v[68:69], v[68:69], s[70:71]\n" \
"v_accvgpr_read_b32 v62, a202\n" \
"v_accvgpr_read_b32 v63, a203\n" \
"v_pk_mul_f32 v[60:61], v[60:61], s[70:71]\n" \
"v_accvgpr_read_b32 v72, a196\n" \
"v_accvgpr_read_b32 v73, a197\n" \
"v_accvgpr_read_b32 v74, a198\n" \
"v_accvgpr_read_b32 v75, a199\n" \
"v_pk_mul_f32 v[70:71], v[70:71], s[70:71]\n" \
"v_cvt_pk_bf16_f32 v68, v68, v69\n" \
"v_accvgpr_read_b32 v64, a204\n" \
"v_accvgpr_read_b32 v65, a205\n" \
"v_accvgpr_read_b32 v66, a206\n" \
"v_accvgpr_read_b32 v67, a207\n" \
"v_pk_mul_f32 v[62:63], v[62:63], s[70:71]\n" \
"v_cvt_pk_bf16_f32 v60, v60, v61\n" \
"v_accvgpr_read_b32 v100, a240\n" \
"v_accvgpr_read_b32 v101, a241\n" \
"v_accvgpr_read_b32 v102, a242\n" \
"v_accvgpr_read_b32 v103, a243\n" \
"v_pk_mul_f32 v[72:73], v[72:73], s[70:71]\n" \
"v_pk_mul_f32 v[74:75], v[74:75], s[70:71]\n" \
"v_mfma_f32_16x16x32_bf16 v[100:103], v[152:155], v[112:115], v[100:103]\n" \
"v_cvt_pk_bf16_f32 v69, v70, v71\n" \
"v_cvt_pk_bf16_f32 v61, v62, v63\n" \
"v_mfma_f32_16x16x32_bf16 v[100:103], v[148:151], v[108:111], v[100:103]\n" \
"v_cvt_pk_bf16_f32 v70, v72, v73\n" \
"v_cvt_pk_bf16_f32 v71, v74, v75\n" \
"ds_write_b128 v27, v[68:71] offset:8192\n" \
"v_pk_mul_f32 v[64:65], v[64:65], s[70:71]\n" \
"v_pk_mul_f32 v[66:67], v[66:67], s[70:71]\n" \
"v_cvt_pk_bf16_f32 v62, v64, v65\n" \
"v_cvt_pk_bf16_f32 v63, v66, v67\n" \
"ds_write_b128 v27, v[60:63] offset:9216\n" \
"v_accvgpr_read_b32 v84, a144\n" \
"buffer_store_dwordx4 a[76:79], v20, s[80:83], s89 offen offset:3072 sc1\n" \
"s_add_u32 s89, s45, 0x1000\n" \
"v_accvgpr_read_b32 v85, a145\n" \
"v_accvgpr_read_b32 v68, a152\n" \
"v_accvgpr_read_b32 v69, a153\n" \
"v_accvgpr_read_b32 v92, a176\n" \
"v_accvgpr_read_b32 v93, a177\n" \
"v_accvgpr_read_b32 v60, a184\n" \
"v_accvgpr_read_b32 v61, a185\n" \
"v_accvgpr_read_b32 v86, a146\n" \
"v_accvgpr_read_b32 v87, a147\n" \
"v_pk_mul_f32 v[84:85], v[84:85], s[70:71]\n" \
"v_accvgpr_read_b32 v70, a154\n" \
"v_accvgpr_read_b32 v71, a155\n" \
"v_pk_mul_f32 v[68:69], v[68:69], s[70:71]\n" \
"v_accvgpr_read_b32 v94, a178\n" \
"v_accvgpr_read_b32 v95, a179\n" \
"v_pk_mul_f32 v[92:93], v[92:93], s[70:71]\n" \
"v_accvgpr_read_b32 v62, a186\n" \
"v_accvgpr_read_b32 v63, a187\n" \
"v_pk_mul_f32 v[60:61], v[60:61], s[70:71]\n" \
"v_pk_mul_f32 v[76:77], v[76:77], s[70:71]\n" \
"v_pk_mul_f32 v[52:53], v[52:53], s[70:71]\n" \
"v_pk_mul_f32 v[100:101], v[100:101], s[70:71]\n" \
"v_accvgpr_read_b32 v88, a148\n" \
"v_accvgpr_read_b32 v89, a149\n" \
"v_accvgpr_read_b32 v90, a150\n" \
"v_accvgpr_read_b32 v91, a151\n" \
"v_pk_mul_f32 v[86:87], v[86:87], s[70:71]\n" \
"v_cvt_pk_bf16_f32 v84, v84, v85\n" \
"v_accvgpr_read_b32 v72, a156\n" \
"v_accvgpr_read_b32 v73, a157\n" \
"v_accvgpr_read_b32 v74, a158\n" \
"v_accvgpr_read_b32 v75, a159\n" \
"v_pk_mul_f32 v[70:71], v[70:71], s[70:71]\n" \
"v_cvt_pk_bf16_f32 v68, v68, v69\n" \
"v_accvgpr_read_b32 v96, a180\n" \
"v_accvgpr_read_b32 v97, a181\n" \
"v_accvgpr_read_b32 v98, a182\n" \
"v_accvgpr_read_b32 v99, a183\n" \
"v_pk_mul_f32 v[94:95], v[94:95], s[70:71]\n" \
"v_cvt_pk_bf16_f32 v92, v92, v93\n" \
"v_accvgpr_read_b32 v64, a188\n" \
"v_accvgpr_read_b32 v65, a189\n" \
"v_accvgpr_read_b32 v66, a190\n" \
"v_accvgpr_read_b32 v67, a191\n" \
"v_pk_mul_f32 v[62:63], v[62:63], s[70:71]\n" \
"v_cvt_pk_bf16_f32 v60, v60, v61\n" \
"v_pk_mul_f32 v[78:79], v[78:79], s[70:71]\n" \
"v_cvt_pk_bf16_f32 v76, v76, v77\n" \
"v_pk_mul_f32 v[54:55], v[54:55], s[70:71]\n" \
"v_cvt_pk_bf16_f32 v52, v52, v53\n" \
"v_pk_mul_f32 v[102:103], v[102:103], s[70:71]\n" \
"v_cvt_pk_bf16_f32 v100, v100, v101\n" \
"v_pk_mul_f32 v[88:89], v[88:89], s[70:71]\n" \
"v_pk_mul_f32 v[90:91], v[90:91], s[70:71]\n" \
"v_cvt_pk_bf16_f32 v85, v86, v87\n" \
"v_pk_mul_f32 v[72:73], v[72:73], s[70:71]\n" \
"v_pk_mul_f32 v[74:75], v[74:75], s[70:71]\n" \
"v_cvt_pk_bf16_f32 v69, v70, v71\n" \
"v_pk_mul_f32 v[96:97], v[96:97], s[70:71]\n" \
"v_pk_mul_f32 v[98:99], v[98:99], s[70:71]\n" \
"buffer_store_dwordx4 a[104:107], v20, s[80:83], s88 offen offset:0 sc1\n" \
"v_cvt_pk_bf16_f32 v93, v94, v95\n" \
"v_pk_mul_f32 v[64:65], v[64:65], s[70:71]\n" \
"v_pk_mul_f32 v[66:67], v[66:67], s[70:71]\n" \
"v_cvt_pk_bf16_f32 v61, v62, v63\n" \
"v_pk_mul_f32 v[80:81], v[80:81], s[70:71]\n" \
"v_pk_mul_f32 v[82:83], v[82:83], s[70:71]\n" \
"v_cvt_pk_bf16_f32 v77, v78, v79\n" \
"v_pk_mul_f32 v[56:57], v[56:57], s[70:71]\n" \
"v_pk_mul_f32 v[58:59], v[58:59], s[70:71]\n" \
"v_cvt_pk_bf16_f32 v53, v54, v55\n" \
"v_accvgpr_read_b32 v104, a244\n" \
"v_accvgpr_read_b32 v105, a245\n" \
"v_accvgpr_read_b32 v106, a246\n" \
"v_accvgpr_read_b32 v107, a247\n" \
"v_cvt_pk_bf16_f32 v101, v102, v103\n" \
"v_cvt_pk_bf16_f32 v86, v88, v89\n" \
"v_mfma_f32_16x16x32_bf16 v[104:107], v[156:159], v[108:111], v[104:107]\n" \
"v_cvt_pk_bf16_f32 v87, v90, v91\n" \
"v_cvt_pk_bf16_f32 v70, v72, v73\n" \
"ds_write_b128 v21, v[84:87] offset:16384\n" \
"v_mfma_f32_16x16x32_bf16 v[104:107], v[160:163], v[112:115], v[104:107]\n" \
"v_cvt_pk_bf16_f32 v71, v74, v75\n" \
"ds_write_b128 v21, v[68:71] offset:17408\n" \
"v_cvt_pk_bf16_f32 v94, v96, v97\n" \
"v_cvt_pk_bf16_f32 v95, v98, v99\n" \
"ds_write_b128 v21, v[92:95] offset:20480\n" \
"v_cvt_pk_bf16_f32 v62, v64, v65\n" \
"v_cvt_pk_bf16_f32 v63, v66, v67\n" \
"ds_write_b128 v21, v[60:63] offset:21504\n" \
"v_pk_mul_f32 v[104:105], v[104:105], s[70:71]\n" \
"v_pk_mul_f32 v[106:107], v[106:107], s[70:71]\n" \
"v_cvt_pk_bf16_f32 v78, v80, v81\n" \
"v_cvt_pk_bf16_f32 v79, v82, v83\n" \
"ds_write_b128 v21, v[76:79] offset:24576\n" \
"v_cvt_pk_bf16_f32 v54, v56, v57\n" \
"v_cvt_pk_bf16_f32 v55, v58, v59\n" \
"ds_write_b128 v21, v[52:55] offset:25600\n" \
"v_cvt_pk_bf16_f32 v102, v104, v105\n" \
"v_cvt_pk_bf16_f32 v103, v106, v107\n" \
"ds_write_b128 v21, v[100:103] offset:28672\n" \
"buffer_store_dwordx4 a[108:111], v20, s[80:83], s89 offen offset:1024 sc1\n" \
"v_accvgpr_read_b32 v84, a248\n" \
"v_accvgpr_read_b32 v85, a249\n" \
"v_accvgpr_read_b32 v86, a250\n" \
"v_accvgpr_read_b32 v87, a251\n" \
"v_accvgpr_read_b32 v88, a252\n" \
"v_accvgpr_read_b32 v89, a253\n" \
"v_mfma_f32_16x16x32_bf16 v[84:87], v[168:171], v[112:115], v[84:87]\n" \
"v_accvgpr_read_b32 v90, a254\n" \
"v_accvgpr_read_b32 v91, a255\n" \
"v_mfma_f32_16x16x32_bf16 v[84:87], v[164:167], v[108:111], v[84:87]\n" \
"v_sub_f32_e32 v44, 1.0, v45\n" \
"v_mfma_f32_16x16x32_bf16 v[88:91], v[172:175], v[108:111], v[88:91]\n" \
"ds_write_b32 v26, v44\n" \
"v_mfma_f32_16x16x32_bf16 v[88:91], v[176:179], v[112:115], v[88:91]\n" \
"s_nop 3\n" \
"v_pk_mul_f32 v[84:85], v[84:85], s[70:71]\n" \
"v_pk_mul_f32 v[86:87], v[86:87], s[70:71]\n" \
"v_cvt_pk_bf16_f32 v84, v84, v85\n" \
"v_cvt_pk_bf16_f32 v85, v86, v87\n" \
"v_pk_mul_f32 v[88:89], v[88:89], s[70:71]\n" \
"v_pk_mul_f32 v[90:91], v[90:91], s[70:71]\n" \
"v_cvt_pk_bf16_f32 v86, v88, v89\n" \
"v_cvt_pk_bf16_f32 v87, v90, v91\n" \
"ds_write_b128 v21, v[84:87] offset:29696\n" \
"L_end_%=:\n"
#define PROJ_VCLOB "v21","v24","v25","v33","v34","v35","v36","v37","v38","v39","v40","v41","v42","v43","v44","v45","v46","v47","v48","v49","v50","v51","v52","v53","v54","v55","v56","v57","v58","v59","v60","v61","v62","v63","v64","v65","v66","v67","v68","v69","v70","v71","v72","v73","v74","v75","v76","v77","v78","v79","v80","v81","v82","v83","v84","v85","v86","v87","v88","v89","v90","v91","v92","v93","v94","v95","v96","v97","v98","v99","v100","v101","v102","v103","v104","v105","v106","v107","v108","v109","v110","v111","v112","v113","v114","v115","v116","v117","v118","v119","v120","v121","v122","v123","v124","v125","v126","v127","v128","v129","v130","v131","v132","v133","v134","v135","v136","v137","v138","v139","v140","v141","v142","v143","v144","v145","v146","v147","v148","v149","v150","v151","v152","v153","v154","v155","v156","v157","v158","v159","v160","v161","v162","v163","v164","v165","v166","v167","v168","v169","v170","v171","v172","v173","v174","v175","v176","v177","v178","v179","v180","v181","v182","v183","v184","v185","v186","v187","v188","v189","v190","v191","v192","v193","v194","v195","v196","v197","v198","v199","v200","v201","v202","v203","v204","v205","v206","v207","v208","v209","v210","v211","v212","v213","v214","v215","v216","v217","v218","v219","v220","v221","v222","v223","v224","v225","v226","v227","v228","v229","v230","v231","v232","v233","v234","v235","v236","v237","v238","v239","v240","v241","v242","v243","v244","v245","v246","v247","v248","v249","v250","v251","v252","v253","v254","v255"
#define PROJ_SCLOB "s37","s38","s39","s40","s41","s42","s43","s44","s45","s46","s47","s48","s49","s50","s51","s52","s53","s54","s55","s62","s63","s76","s77","s78","s79","s80","s81","s82","s83","s84","s85","s86","s87","s88","s89","s94","s95","s96","s97","s98","s99","s100","s101"

#define PLO(p) ((unsigned)(size_t)(p))
#define PHI(p) ((unsigned)((size_t)(p) >> 32))
__device__ __forceinline__ unsigned proj_phase(const u16* __restrict__ x, const u16* __restrict__ ckv, const u16* __restrict__ wq, const u16* __restrict__ wuk,
                                                       const u16* __restrict__ wuv, const u16* __restrict__ wqkv, const u16* __restrict__ gw, const u16* wo, const u16* wob, const u16* out,
                                                       char* __restrict__ ws, char* lds, const unsigned wid, unsigned& tag, u32x4 (&vp)[5], u32x4 (&sa)[5], unsigned (&sd)[2]) {
    typedef unsigned u32x2 __attribute__((ext_vector_type(2)));
    unsigned* const flags = (unsigned*)(ws + WS_F);
    const int b = blockIdx.x, h = (b & 7) * 4 + ((b >> 3) & 3), mt = b >> 5;
    const unsigned L0 = (unsigned)(size_t)lds, rb = (wid < 2u ? 15u - mt : mt) * 128u;
    RTS(0);
#define PST() do {} while (0)
    const unsigned lane = threadIdx.x & 63u, wr = wid >> 1, wc = wid & 1u;
    PST();
    unsigned vAc[2], vB[2];
    const unsigned sp = b & 7, nt = (b >> 3) & 3, rot = (PROJ_ROT) & 7u;
#pragma unroll
    for (int par = 0; par < 2; ++par) {
        const unsigned lr = lane >> 3, cc = (lane & 7u) ^ (4u * par + (lr >> 1)), r0 = rb + wc * 64u + lr;
        vAc[par] = r0 * (DC * 2) + cc * 16u;
        const unsigned br = lane >> 4, p16 = lane & 15u, sc = (((p16 >> 1) ^ (br | (par << 2))) << 1) | (p16 & 1u);
        vB[par] = (16u * wid + br) * (NBIG * 2) + h * (HD * 2) + sc * 16u;
    }
#define SRD(p, n) (u32x4){PLO(p), PHI(p) & 0xffffu, n, 0x00020000u}
    u32x4 sC = SRD(ckv, S * DC * 2), sK = SRD(wuk, DC * NBIG * 2), sV = SRD(wuv, DC * NBIG * 2);
#define PRE(n) do { SBAR(); asm volatile(PROJ_PRE##n :: "{v22}"(vAc[0]), "{v23}"(vAc[1]), "{v26}"(vB[0]), "{v27}"(vB[1]), "{s[64:67]}"(sC), "{s[68:71]}"(sK), "{s[72:75]}"(sV), \
                 [mA] "s"(L0 + wid * 8192u), [mB] "s"(L0 + 65536u + wid * 4096u), [rc] "s"(rot * 128u) : "memory", "m0", "scc", "s62", "s63", "s84", "s85"); SBAR(); } while (0)
    PRE(0);
    const unsigned fr = lane & 15u, fg = lane >> 4, r4 = (lane >> 2) & 3u, sB = r4 | ((fg & 1u) << 2);
    const unsigned aA0 = L0 + (wr * 64u + fr) * 128u + ((fg ^ (fr >> 1)) << 4);
    unsigned vl = lane * 16u, r32 = L0 + 65536u + (8u * fg + r4) * 256u + (lane & 3u) * 8u + ((sB ^ (4u * wc)) << 5);
    u32x2 r28 = {aA0, aA0 ^ 64u};
    asm volatile("" : "+v"(vl), "+v"(r28), "+v"(r32));
    PST();
    PRE(1);
    PST();
    PRE(2);
    PST();
    asm volatile("s_mov_b64 exec, 0\n s_cmp_lt_u32 %0, 2\n s_cbranch_scc1 L_chA\n s_cmp_eq_u32 %0, 2\n s_cbranch_scc1 L_pf2\n s_branch L_pf3\n"
                 "L_chA: s_cmp_eq_u32 %0, 0\n s_cbranch_scc1 L_pf0\n s_branch L_pf1\n L_retA: s_mov_b64 exec, -1" :: "s"(wid) : "scc", "memory");
    PRE(3);
    PST();
    sa[0] = (u32x4){PLO(x), PHI(x), PLO(wq), PHI(wq)}; sa[1] = (u32x4){PLO(wqkv), PHI(wqkv), PLO(gw), PHI(gw)}; sa[2] = (u32x4){PLO(wo), PHI(wo), PLO(wob), PHI(wob)}; sa[3] = (u32x4){PLO(out), PHI(out), rot * 128u, 0u};
    asm volatile(PROJ_ASM
                 : [tag] "=&s"(tag), "+{s[8:11]}"(sa[0]), "+{s[12:15]}"(sa[1]), "+{s[16:19]}"(sa[2]), "+{s[20:23]}"(sa[3]), "=&{s[56:59]}"(sa[4]), "=&{s60}"(sd[0]), "=&{s61}"(sd[1]),
                   "+{s[64:67]}"(sC), "+{s[68:71]}"(sK), "+{s[72:75]}"(sV), "+{v22}"(vAc[0]), "+{v23}"(vAc[1]), "+{v26}"(vB[0]), "+{v27}"(vB[1]), "+{v[28:29]}"(r28), "+{v32}"(r32),
                   "={v[0:3]}"(vp[0]), "={v[4:7]}"(vp[1]), "={v[8:11]}"(vp[2]), "={v[12:15]}"(vp[3]), "={v[16:19]}"(vp[4])
                 : "{v20}"(vl), [wsl] "s"(PLO(ws)), [wsh] "s"(PHI(ws)), [b] "s"(b), [w] "s"(wid), [mA] "s"(L0 + wid * 8192u), [mB] "s"(L0 + 65536u + wid * 4096u), [mS] "s"(L0 + 151552u + wid * 512u)
                 : "memory", "m0", "vcc", "scc", PROJ_VCLOB, PROJ_SCLOB, ACLOB_LO, ACLOB_HI);
    RTS(1);
    return vl;
}

namespace att {
#define DI __device__ __forceinline__
constexpr int LDS_BYTES = 163840;
constexpr unsigned LDS_G = 159744u, LDS_L = 161792u;
typedef __bf16 bfx8 __attribute__((ext_vector_type(8)));
DI unsigned rfl(unsigned x) { return __builtin_amdgcn_readfirstlane(x); }
DI f32x4 mfma(u32x4 a, u32x4 b, f32x4 c) { return __builtin_amdgcn_mfma_f32_16x16x32_bf16(__builtin_bit_cast(bfx8, a), __builtin_bit_cast(bfx8, b), c, 0, 0, 0); }
typedef __bf16 bfx2 __attribute__((ext_vector_type(2)));
typedef float f32x2 __attribute__((ext_vector_type(2)));
DI unsigned cvtpk2(float lo, float hi) { f32x2 v = {lo, hi}; bfx2 b = __builtin_convertvector(v, bfx2); return __builtin_bit_cast(unsigned, b); }
DI void att_exact(const u16* __restrict__ x, const u16* __restrict__ wq, char* __restrict__ ws, unsigned h, unsigned r0, float g1) {
    const unsigned lane = lane_id(), g = lane >> 4, c = lane & 15u;
    f32x4 o[8];
#pragma unroll
    for (int n = 0; n < 8; ++n) o[n] = (f32x4){0.f, 0.f, 0.f, 0.f};
#pragma unroll 1
    for (unsigned kk = 0; kk < (unsigned)DM; kk += 32u) {
        const unsigned k = kk + 8u * g;
        const u32x4 xf = *reinterpret_cast<const u32x4*>(x + (r0 + c) * DM + k);
#pragma unroll
        for (int n = 0; n < 8; ++n) {
            const u16* w = wq + k * NBIG + h * HD + n * 16 + c; u32x4 wf;
#pragma unroll
            for (int e = 0; e < 4; ++e) wf[e] = (unsigned)w[2 * e * NBIG] | ((unsigned)w[(2 * e + 1) * NBIG] << 16);
            o[n] = mfma(wf, xf, o[n]);
        }
    }
    u32x4 q[4];
#pragma unroll
    for (int p = 0; p < 4; ++p) {
        const f32x4 a = o[2 * p] * 0.12751743082f, b = o[2 * p + 1] * 0.12751743082f;
        q[p] = (u32x4){cvtpk2(a[0], a[1]), cvtpk2(a[2], a[3]), cvtpk2(b[0], b[1]), cvtpk2(b[2], b[3])};
    }
#pragma unroll
    for (int n = 0; n < 8; ++n) o[n] = (f32x4){0.f, 0.f, 0.f, 0.f};
    const __amdgpu_buffer_rsrc_t srd = __builtin_amdgcn_make_buffer_rsrc((void*)(ws + WS_K + (size_t)h * 1048576), (short)0, 1048576, 0x00020000);
    float m = -3.0e38f, l = 0.f;
#pragma unroll 1
    for (unsigned k0 = 0; k0 <= r0 + 15u; k0 += 32u) {
        const unsigned tb = (k0 >> 6) * 32768u + (k0 & 32u) * 256u;
        f32x4 s[2]; float mx = m;
#pragma unroll
        for (int kt = 0; kt < 2; ++kt) {
            s[kt] = (f32x4){0.f, 0.f, 0.f, 0.f};
#pragma unroll
            for (int p = 0; p < 4; ++p) s[kt] = mfma(__builtin_amdgcn_raw_buffer_load_b128(srd, lane * 16u + (kt * 4 + p) * 1024u, tb, 16), q[p], s[kt]);
#pragma unroll
            for (int e = 0; e < 4; ++e) { if (k0 + 16u * kt + 4u * g + e > r0 + c) s[kt][e] = -3.0e38f; mx = fmaxf(mx, s[kt][e]); }
        }
        mx = fmaxf(mx, __shfl_xor(mx, 16)); mx = fmaxf(mx, __shfl_xor(mx, 32));
        const float al = __builtin_amdgcn_exp2f(m - mx); m = mx;
        u32x4 pf;
#pragma unroll
        for (int e = 0; e < 4; ++e) pf[e] = cvtpk2(__builtin_amdgcn_exp2f(s[e >> 1][2 * (e & 1)] - m), __builtin_amdgcn_exp2f(s[e >> 1][2 * (e & 1) + 1] - m));
        l *= al;
#pragma unroll
        for (int e = 0; e < 4; ++e) l += __uint_as_float(pf[e] << 16) + __uint_as_float(pf[e] & 0xffff0000u);
#pragma unroll
        for (int n = 0; n < 8; ++n) o[n] = mfma(__builtin_amdgcn_raw_buffer_load_b128(srd, lane * 16u + 16384u + ((n >> 2) * 8 + (n & 3)) * 1024u, tb - (k0 & 32u) * 128u, 16), pf, o[n] * al);
    }
    l += __shfl_xor(l, 16); l += __shfl_xor(l, 32);
    const float f = g1 / l;
    const __amdgpu_buffer_rsrc_t srdA = __builtin_amdgcn_make_buffer_rsrc((void*)(ws + WS_AF), (short)0, 128 * KS_TOT * 1024, 0x00020000);
#pragma unroll
    for (int p = 0; p < 4; ++p) {
        const f32x4 a = o[2 * p] * f, b = o[2 * p + 1] * f;
        __builtin_amdgcn_raw_buffer_store_b128((u32x4){cvtpk2(a[0], a[1]), cvtpk2(a[2], a[3]), cvtpk2(b[0], b[1]), cvtpk2(b[2], b[3])}, srdA, lane * 16u + p * 1024u, ((r0 >> 4) * KS_TOT + 18u + 4u * h) * 1024u, 16);
    }
}
}

#define ATT_ASM \
"L_att0:\n" \
"s_mov_b32 s101, 0\n" \
"s_waitcnt vmcnt(20) lgkmcnt(0)\n" \
"s_barrier\n" \
"v_accvgpr_write_b32 a216, 0\n" \
"v_accvgpr_write_b32 a217, 0\n" \
"v_accvgpr_write_b32 a218, 0\n" \
"v_accvgpr_write_b32 a219, 0\n" \
"v_and_b32_e32 v63, 0x300, v20\n" \
"v_add_u32_e32 v58, s20, v20\n" \
"v_mfma_f32_32x32x16_bf16 v[128:143], a[216:219], a[216:219], 0\n" \
"ds_read_b128 v[112:115], v63 offset:0\n" \
"ds_read_b128 a[0:3], v58 offset:0\n" \
"ds_read_b128 a[16:19], v58 offset:8192\n" \
"ds_read_b128 v[116:119], v63 offset:1024\n" \
"ds_read_b128 a[4:7], v58 offset:1024\n" \
"v_add_u32_e32 v59, s21, v20\n" \
"v_mfma_f32_32x32x16_bf16 v[144:159], a[216:219], a[216:219], 0\n" \
"ds_read_b128 a[32:35], v59 offset:0\n" \
"ds_read_b128 a[48:51], v59 offset:8192\n" \
"s_sub_u32 s67, s12, 0xc000\n" \
"s_mov_b32 s65, s12\n" \
"v_add_u32_e32 v55, s67, v20\n" \
"v_add_u32_e32 v52, 0xc000, v55\n" \
"v_mfma_f32_32x32x16_bf16 v[160:175], a[216:219], a[216:219], 0\n" \
"ds_read_b128 a[20:23], v58 offset:9216\n" \
"s_xor_b32 m0, s65, 0x10000\n" \
"ds_read_b128 a[36:39], v59 offset:1024\n" \
"buffer_load_dwordx4 v52, s[8:11], 0 offen offset:0 sc1 lds\n" \
"ds_read_b128 a[52:55], v59 offset:9216\n" \
"v_mfma_f32_32x32x16_bf16 v[176:191], a[216:219], a[216:219], 0\n" \
"buffer_load_dwordx4 v52, s[8:11], 0 offen offset:1024 sc1 lds\n" \
"ds_read_b128 v[120:123], v63 offset:2048\n" \
"v_add_u32_e32 v60, s15, v20\n" \
"v_add_u32_e32 v60, 0x10000, v60\n" \
"v_mov_b32_e32 v56, s62\n" \
"v_mfma_f32_32x32x16_bf16 v[192:207], a[216:219], a[216:219], 0\n" \
"ds_read_b128 a[8:11], v58 offset:2048\n" \
"ds_read_b128 a[24:27], v58 offset:10240\n" \
"v_readlane_b32 s68, v11, 0\n" \
"v_bfe_i32 v21, v20, 4, 1\n" \
"v_mov_b32_e32 v54, 0x3f803f80\n" \
"v_mov_b32_e32 v57, 0x3f003f00\n" \
"v_mfma_f32_32x32x16_bf16 v[208:223], a[216:219], a[216:219], 0\n" \
"ds_read_b128 a[40:43], v59 offset:2048\n" \
"ds_read_b128 a[56:59], v59 offset:10240\n" \
"s_waitcnt lgkmcnt(14)\n" \
"v_xor_b32_e32 v112, 0x80008000, v112\n" \
"v_xor_b32_e32 v113, 0x80008000, v113\n" \
"v_xor_b32_e32 v114, 0x80008000, v114\n" \
"v_mfma_f32_32x32x16_bf16 v[224:239], a[216:219], a[216:219], 0\n" \
"ds_read_b128 v[124:127], v63 offset:3072\n" \
"ds_read_b128 a[12:15], v58 offset:3072\n" \
"v_xor_b32_e32 v115, 0x80008000, v115\n" \
"v_and_b32_e32 v21, 0xaaaaaaaa, v21\n" \
"v_accvgpr_write_b32 a220, v112\n" \
"s_waitcnt lgkmcnt(13)\n" \
"v_xor_b32_e32 v116, 0x80008000, v116\n" \
"v_mfma_f32_32x32x16_bf16 v[240:255], a[216:219], a[216:219], 0\n" \
"ds_read_b128 a[28:31], v58 offset:11264\n" \
"ds_read_b128 a[44:47], v59 offset:3072\n" \
"v_xor_b32_e32 v117, 0x80008000, v117\n" \
"v_xor_b32_e32 v118, 0x80008000, v118\n" \
"v_xor_b32_e32 v119, 0x80008000, v119\n" \
"v_accvgpr_write_b32 a221, v113\n" \
"v_mfma_f32_32x32x16_bf16 a[192:207], a[216:219], a[216:219], 0\n" \
"ds_read_b128 a[60:63], v59 offset:11264\n" \
"ds_read_b128 a[160:163], v20 offset:0\n" \
"v_accvgpr_write_b32 a222, v114\n" \
"v_accvgpr_write_b32 a223, v115\n" \
"v_readlane_b32 s80, v11, 9\n" \
"v_readlane_b32 s81, v11, 10\n" \
"v_mfma_f32_16x16x32_bf16 v[32:35], v[112:115], a[0:3], 0\n" \
"ds_read_b128 a[164:167], v20 offset:1024\n" \
"ds_read_b128 a[168:171], v20 offset:2048\n" \
"v_mfma_f32_16x16x32_bf16 v[36:39], v[112:115], a[16:19], 0\n" \
"ds_read_b128 a[172:175], v20 offset:3072\n" \
"s_waitcnt lgkmcnt(15)\n" \
"v_mfma_f32_16x16x32_bf16 v[40:43], v[112:115], a[32:35], 0\n" \
"ds_read_b128 a[188:191], v20 offset:7168\n" \
"s_waitcnt lgkmcnt(14)\n" \
"v_xor_b32_e32 v120, 0x80008000, v120\n" \
"v_mfma_f32_16x16x32_bf16 v[44:47], v[112:115], a[48:51], 0\n" \
"ds_read_b128 a[184:187], v20 offset:6144\n" \
"v_xor_b32_e32 v121, 0x80008000, v121\n" \
"v_mfma_f32_16x16x32_bf16 v[32:35], v[116:119], a[4:7], v[32:35]\n" \
"v_xor_b32_e32 v122, 0x80008000, v122\n" \
"v_xor_b32_e32 v123, 0x80008000, v123\n" \
"v_mfma_f32_16x16x32_bf16 v[36:39], v[116:119], a[20:23], v[36:39]\n" \
"ds_read_b128 a[180:183], v20 offset:5120\n" \
"ds_read_b128 a[176:179], v20 offset:4096\n" \
"v_mfma_f32_16x16x32_bf16 v[40:43], v[116:119], a[36:39], v[40:43]\n" \
"ds_read_b128 a[128:131], v20 offset:8192\n" \
"s_mov_b32 s82, 0x47fca0\n" \
"v_mfma_f32_16x16x32_bf16 v[44:47], v[116:119], a[52:55], v[44:47]\n" \
"s_waitcnt lgkmcnt(13)\n" \
"v_xor_b32_e32 v124, 0x80008000, v124\n" \
"v_mfma_f32_16x16x32_bf16 v[32:35], v[120:123], a[8:11], v[32:35]\n" \
"v_xor_b32_e32 v125, 0x80008000, v125\n" \
"v_xor_b32_e32 v126, 0x80008000, v126\n" \
"v_mfma_f32_16x16x32_bf16 v[36:39], v[120:123], a[24:27], v[36:39]\n" \
"ds_read_b128 a[132:135], v20 offset:9216\n" \
"v_xor_b32_e32 v127, 0x80008000, v127\n" \
"v_mfma_f32_16x16x32_bf16 v[40:43], v[120:123], a[40:43], v[40:43]\n" \
"ds_read_b128 a[136:139], v20 offset:10240\n" \
"ds_read_b128 a[140:143], v20 offset:11264\n" \
"v_mfma_f32_16x16x32_bf16 v[44:47], v[120:123], a[56:59], v[44:47]\n" \
"ds_read_b128 a[144:147], v20 offset:12288\n" \
"s_waitcnt lgkmcnt(15)\n" \
"v_mfma_f32_16x16x32_bf16 v[32:35], v[124:127], a[12:15], v[32:35]\n" \
"ds_read_b128 a[148:151], v20 offset:13312\n" \
"ds_read_b128 a[152:155], v20 offset:14336\n" \
"v_mfma_f32_16x16x32_bf16 v[36:39], v[124:127], a[28:31], v[36:39]\n" \
"ds_read_b128 a[156:159], v20 offset:15360\n" \
"s_waitcnt lgkmcnt(15)\n" \
"v_mfma_f32_16x16x32_bf16 v[40:43], v[124:127], a[44:47], v[40:43]\n" \
"ds_read_b128 a[64:67], v60 offset:16384\n" \
"s_mov_b32 s83, 0x20000\n" \
"v_mfma_f32_16x16x32_bf16 v[44:47], v[124:127], a[60:63], v[44:47]\n" \
"ds_read_b128 a[68:71], v60 offset:17408\n" \
"s_mov_b32 s100, 0x71800000\n" \
"v_mfma_f32_16x16x32_bf16 v[64:67], a[160:163], a[0:3], v[32:35]\n" \
"ds_read_b128 a[72:75], v60 offset:18432\n" \
"s_waitcnt lgkmcnt(15)\n" \
"v_mfma_f32_16x16x32_bf16 v[64:67], a[164:167], a[4:7], v[64:67]\n" \
"ds_read_b128 a[76:79], v60 offset:19456\n" \
"s_mov_b32 s63, s23\n" \
"v_mfma_f32_16x16x32_bf16 v[64:67], a[168:171], a[8:11], v[64:67]\n" \
"ds_read_b128 a[80:83], v60 offset:24576\n" \
"v_mov_b32_e32 v62, 0\n" \
"v_mfma_f32_16x16x32_bf16 v[64:67], a[172:175], a[12:15], v[64:67]\n" \
"ds_read_b128 a[84:87], v60 offset:25600\n" \
"s_waitcnt lgkmcnt(15)\n" \
"v_mfma_f32_16x16x32_bf16 v[68:71], a[188:191], a[12:15], v[32:35]\n" \
"ds_read_b128 a[88:91], v60 offset:26624\n" \
"v_accvgpr_write_b32 a212, v54\n" \
"v_mfma_f32_16x16x32_bf16 v[68:71], a[184:187], a[8:11], v[68:71]\n" \
"ds_read_b128 a[92:95], v60 offset:27648\n" \
"v_accvgpr_write_b32 a208, 0\n" \
"v_mfma_f32_16x16x32_bf16 v[68:71], a[180:183], a[4:7], v[68:71]\n" \
"ds_read_b128 a[96:99], v60 offset:0\n" \
"s_waitcnt lgkmcnt(15)\n" \
"L_pf10:\n" \
"s_cbranch_execz L_pf14\n" \
"s_nop 0\n" \
"v_mfma_f32_16x16x32_bf16 v[68:71], a[176:179], a[0:3], v[68:71]\n" \
"ds_read_b128 a[100:103], v60 offset:1024\n" \
"v_accvgpr_write_b32 a213, v54\n" \
"v_mfma_f32_16x16x32_bf16 v[72:75], a[160:163], a[16:19], v[36:39]\n" \
"ds_read_b128 a[104:107], v60 offset:2048\n" \
"v_accvgpr_write_b32 a209, 0\n" \
"v_mfma_f32_16x16x32_bf16 v[72:75], a[164:167], a[20:23], v[72:75]\n" \
"ds_read_b128 a[108:111], v60 offset:3072\n" \
"v_accvgpr_write_b32 a214, v54\n" \
"v_mfma_f32_16x16x32_bf16 v[72:75], a[168:171], a[24:27], v[72:75]\n" \
"ds_read_b128 a[112:115], v60 offset:8192\n" \
"v_accvgpr_write_b32 a210, 0\n" \
"v_mfma_f32_16x16x32_bf16 v[72:75], a[172:175], a[28:31], v[72:75]\n" \
"ds_read_b128 a[116:119], v60 offset:9216\n" \
"v_accvgpr_write_b32 a215, v54\n" \
"v_mfma_f32_16x16x32_bf16 v[76:79], a[188:191], a[28:31], v[36:39]\n" \
"ds_read_b128 a[120:123], v60 offset:10240\n" \
"v_accvgpr_write_b32 a211, 0\n" \
"v_mfma_f32_16x16x32_bf16 v[76:79], a[184:187], a[24:27], v[76:79]\n" \
"ds_read_b128 a[124:127], v60 offset:11264\n" \
"v_or_b32_e32 v21, 0x44444444, v21\n" \
"v_mfma_f32_16x16x32_bf16 v[76:79], a[180:183], a[20:23], v[76:79]\n" \
"v_exp_f32_e32 v64, v64\n" \
"v_mfma_f32_16x16x32_bf16 v[76:79], a[176:179], a[16:19], v[76:79]\n" \
"v_exp_f32_e32 v65, v65\n" \
"v_mfma_f32_16x16x32_bf16 v[80:83], a[160:163], a[32:35], v[40:43]\n" \
"v_exp_f32_e32 v66, v66\n" \
"v_mfma_f32_16x16x32_bf16 v[80:83], a[164:167], a[36:39], v[80:83]\n" \
"v_exp_f32_e32 v67, v67\n" \
"v_mfma_f32_16x16x32_bf16 v[80:83], a[168:171], a[40:43], v[80:83]\n" \
"v_exp_f32_e32 v68, v68\n" \
"v_mfma_f32_16x16x32_bf16 v[80:83], a[172:175], a[44:47], v[80:83]\n" \
"v_exp_f32_e32 v69, v69\n" \
"v_mfma_f32_16x16x32_bf16 v[84:87], a[188:191], a[44:47], v[40:43]\n" \
"v_exp_f32_e32 v70, v70\n" \
"v_mfma_f32_16x16x32_bf16 v[84:87], a[184:187], a[40:43], v[84:87]\n" \
"v_exp_f32_e32 v71, v71\n" \
"v_mfma_f32_16x16x32_bf16 v[84:87], a[180:183], a[36:39], v[84:87]\n" \
"v_exp_f32_e32 v72, v72\n" \
"v_mfma_f32_16x16x32_bf16 v[84:87], a[176:179], a[32:35], v[84:87]\n" \
"v_exp_f32_e32 v73, v73\n" \
"v_mfma_f32_16x16x32_bf16 v[88:91], a[160:163], a[48:51], v[44:47]\n" \
"v_exp_f32_e32 v74, v74\n" \
"v_mfma_f32_16x16x32_bf16 v[88:91], a[164:167], a[52:55], v[88:91]\n" \
"v_exp_f32_e32 v75, v75\n" \
"v_mfma_f32_16x16x32_bf16 v[88:91], a[168:171], a[56:59], v[88:91]\n" \
"v_exp_f32_e32 v76, v76\n" \
"v_mfma_f32_16x16x32_bf16 v[88:91], a[172:175], a[60:63], v[88:91]\n" \
"v_exp_f32_e32 v77, v77\n" \
"v_mfma_f32_16x16x32_bf16 v[92:95], a[188:191], a[60:63], v[44:47]\n" \
"v_exp_f32_e32 v78, v78\n" \
"v_mfma_f32_16x16x32_bf16 v[92:95], a[184:187], a[56:59], v[92:95]\n" \
"v_exp_f32_e32 v79, v79\n" \
"v_mfma_f32_16x16x32_bf16 v[92:95], a[180:183], a[52:55], v[92:95]\n" \
"v_exp_f32_e32 v80, v80\n" \
"v_mfma_f32_16x16x32_bf16 v[92:95], a[176:179], a[48:51], v[92:95]\n" \
"v_exp_f32_e32 v81, v81\n" \
"v_exp_f32_e32 v82, v82\n" \
"v_exp_f32_e32 v83, v83\n" \
"v_exp_f32_e32 v84, v84\n" \
"v_exp_f32_e32 v85, v85\n" \
"v_exp_f32_e32 v86, v86\n" \
"v_exp_f32_e32 v87, v87\n" \
"v_accvgpr_write_b32 a252, v57\n" \
"v_accvgpr_write_b32 a253, v57\n" \
"v_accvgpr_write_b32 a254, v57\n" \
"v_accvgpr_write_b32 a255, v57\n" \
"s_mov_b32 s64, 0x8000\n" \
"v_add_u32_e32 v50, 0x6000, v55\n" \
"v_add_u32_e32 v51, 0xa000, v55\n" \
"v_add_u32_e32 v53, 0x10000, v55\n" \
"v_add_u32_e32 v48, 0xc000, v20\n" \
"v_add_u32_e32 v49, s17, v20\n" \
"v_accvgpr_write_b32 a224, v116\n" \
"v_accvgpr_write_b32 a225, v117\n" \
"v_accvgpr_write_b32 a226, v118\n" \
"v_accvgpr_write_b32 a227, v119\n" \
"v_accvgpr_write_b32 a228, v120\n" \
"v_accvgpr_write_b32 a229, v121\n" \
"v_accvgpr_write_b32 a230, v122\n" \
"v_accvgpr_write_b32 a231, v123\n" \
"v_accvgpr_write_b32 a232, v124\n" \
"v_accvgpr_write_b32 a233, v125\n" \
"v_accvgpr_write_b32 a234, v126\n" \
"v_accvgpr_write_b32 a235, v127\n" \
"v_exp_f32_e32 v88, v88\n" \
"v_exp_f32_e32 v89, v89\n" \
"v_exp_f32_e32 v90, v90\n" \
"v_exp_f32_e32 v91, v91\n" \
"v_cvt_pk_bf16_f32 v96, v64, v65\n" \
"v_cvt_pk_bf16_f32 v97, v66, v67\n" \
"v_cvt_pk_bf16_f32 v98, v68, v69\n" \
"v_cvt_pk_bf16_f32 v99, v70, v71\n" \
"v_cvt_pk_bf16_f32 v104, v72, v73\n" \
"v_cvt_pk_bf16_f32 v105, v74, v75\n" \
"v_cvt_pk_bf16_f32 v106, v76, v77\n" \
"v_cvt_pk_bf16_f32 v107, v78, v79\n" \
"v_cvt_pk_bf16_f32 v112, v80, v81\n" \
"v_cvt_pk_bf16_f32 v113, v82, v83\n" \
"v_cvt_pk_bf16_f32 v114, v84, v85\n" \
"v_cvt_pk_bf16_f32 v115, v86, v87\n" \
"s_sub_u32 s66, s18, 1\n" \
"s_waitcnt vmcnt(2) lgkmcnt(0)\n" \
"s_barrier\n" \
"s_mov_b64 exec, 1\n" \
"buffer_store_dword v56, off, s[56:59], s68 offset:0 sc1\n" \
"buffer_store_dword v56, off, s[56:59], s22 sc1\n" \
"s_mov_b64 exec, -1\n" \
"s_cbranch_scc1 L_tr_%=\n" \
"s_branch L_1_%=\n" \
"L_t1_%=:\n" \
"v_mfma_f32_16x16x32_bf16 v[252:255], a[188:191], v[124:127], v[252:255]\n" \
"v_mfma_f32_16x16x32_bf16 v[252:255], a[172:175], v[120:123], v[252:255]\n" \
"v_smfmac_f32_16x16x64_bf16 a[204:207], a[212:215], v[120:127], v21\n" \
"s_waitcnt lgkmcnt(0)\n" \
"L_1_%=:\n" \
"v_mfma_f32_16x16x32_bf16 v[64:67], a[128:131], a[0:3], v[32:35]\n" \
"ds_read_b128 a[160:163], v49 offset:0\n" \
"ds_read_b128 a[176:179], v49 offset:4096\n" \
"v_mfma_f32_16x16x32_bf16 v[64:67], a[132:135], a[4:7], v[64:67]\n" \
"v_cvt_pk_bf16_f32 v120, v88, v89\n" \
"v_cvt_pk_bf16_f32 v121, v90, v91\n" \
"v_mfma_f32_16x16x32_bf16 v[64:67], a[136:139], a[8:11], v[64:67]\n" \
"ds_read_b128 a[180:183], v49 offset:5120\n" \
"ds_read_b128 a[164:167], v49 offset:1024\n" \
"v_mfma_f32_16x16x32_bf16 v[64:67], a[140:143], a[12:15], v[64:67]\n" \
"v_exp_f32_e32 v92, v92\n" \
"v_mfma_f32_16x16x32_bf16 v[68:71], a[156:159], a[12:15], v[32:35]\n" \
"ds_read_b128 a[168:171], v49 offset:2048\n" \
"ds_read_b128 a[184:187], v49 offset:6144\n" \
"v_mfma_f32_16x16x32_bf16 v[68:71], a[152:155], a[8:11], v[68:71]\n" \
"v_exp_f32_e32 v93, v93\n" \
"v_mfma_f32_16x16x32_bf16 v[68:71], a[148:151], a[4:7], v[68:71]\n" \
"ds_read_b128 a[188:191], v49 offset:7168\n" \
"ds_read_b128 a[172:175], v49 offset:3072\n" \
"v_mfma_f32_16x16x32_bf16 v[68:71], a[144:147], a[0:3], v[68:71]\n" \
"v_exp_f32_e32 v94, v94\n" \
"v_mfma_f32_16x16x32_bf16 v[72:75], a[128:131], a[16:19], v[36:39]\n" \
"v_exp_f32_e32 v95, v95\n" \
"v_mfma_f32_16x16x32_bf16 v[72:75], a[132:135], a[20:23], v[72:75]\n" \
"v_cvt_pk_bf16_f32 v122, v92, v93\n" \
"v_cvt_pk_bf16_f32 v123, v94, v95\n" \
"v_mfma_f32_16x16x32_bf16 v[72:75], a[136:139], a[24:27], v[72:75]\n" \
"v_exp_f32_e32 v64, v64\n" \
"v_mfma_f32_16x16x32_bf16 v[72:75], a[140:143], a[28:31], v[72:75]\n" \
"v_exp_f32_e32 v65, v65\n" \
"v_mfma_f32_16x16x32_bf16 v[76:79], a[156:159], a[28:31], v[36:39]\n" \
"v_exp_f32_e32 v66, v66\n" \
"v_mfma_f32_16x16x32_bf16 v[76:79], a[152:155], a[24:27], v[76:79]\n" \
"v_exp_f32_e32 v67, v67\n" \
"v_mfma_f32_16x16x32_bf16 v[76:79], a[148:151], a[20:23], v[76:79]\n" \
"v_exp_f32_e32 v68, v68\n" \
"v_mfma_f32_16x16x32_bf16 v[76:79], a[144:147], a[16:19], v[76:79]\n" \
"v_exp_f32_e32 v69, v69\n" \
"v_mfma_f32_16x16x32_bf16 v[80:83], a[128:131], a[32:35], v[40:43]\n" \
"v_exp_f32_e32 v70, v70\n" \
"v_mfma_f32_16x16x32_bf16 v[80:83], a[132:135], a[36:39], v[80:83]\n" \
"v_exp_f32_e32 v71, v71\n" \
"v_mfma_f32_16x16x32_bf16 v[80:83], a[136:139], a[40:43], v[80:83]\n" \
"v_cvt_pk_bf16_f32 v100, v64, v65\n" \
"v_cvt_pk_bf16_f32 v101, v66, v67\n" \
"v_mfma_f32_16x16x32_bf16 v[80:83], a[140:143], a[44:47], v[80:83]\n" \
"v_cvt_pk_bf16_f32 v102, v68, v69\n" \
"v_cvt_pk_bf16_f32 v103, v70, v71\n" \
"v_mfma_f32_16x16x32_bf16 v[84:87], a[156:159], a[44:47], v[40:43]\n" \
"v_exp_f32_e32 v72, v72\n" \
"v_mfma_f32_16x16x32_bf16 v[84:87], a[152:155], a[40:43], v[84:87]\n" \
"v_exp_f32_e32 v73, v73\n" \
"v_mfma_f32_16x16x32_bf16 v[84:87], a[148:151], a[36:39], v[84:87]\n" \
"v_exp_f32_e32 v74, v74\n" \
"v_mfma_f32_16x16x32_bf16 v[84:87], a[144:147], a[32:35], v[84:87]\n" \
"v_exp_f32_e32 v75, v75\n" \
"v_mfma_f32_16x16x32_bf16 v[88:91], a[128:131], a[48:51], v[44:47]\n" \
"v_exp_f32_e32 v76, v76\n" \
"v_mfma_f32_16x16x32_bf16 v[88:91], a[132:135], a[52:55], v[88:91]\n" \
"v_exp_f32_e32 v77, v77\n" \
"v_mfma_f32_16x16x32_bf16 v[88:91], a[136:139], a[56:59], v[88:91]\n" \
"v_exp_f32_e32 v78, v78\n" \
"v_mfma_f32_16x16x32_bf16 v[88:91], a[140:143], a[60:63], v[88:91]\n" \
"v_exp_f32_e32 v79, v79\n" \
"v_mfma_f32_16x16x32_bf16 v[92:95], a[156:159], a[60:63], v[44:47]\n" \
"v_cvt_pk_bf16_f32 v108, v72, v73\n" \
"v_cvt_pk_bf16_f32 v109, v74, v75\n" \
"v_mfma_f32_16x16x32_bf16 v[92:95], a[152:155], a[56:59], v[92:95]\n" \
"v_cvt_pk_bf16_f32 v110, v76, v77\n" \
"v_cvt_pk_bf16_f32 v111, v78, v79\n" \
"v_mfma_f32_16x16x32_bf16 v[92:95], a[148:151], a[52:55], v[92:95]\n" \
"ds_read_b128 a[128:131], v48 offset:0\n" \
"ds_read_b128 a[132:135], v48 offset:1024\n" \
"v_mfma_f32_16x16x32_bf16 v[92:95], a[144:147], a[48:51], v[92:95]\n" \
"s_add_u32 s63, s63, s63\n" \
"s_waitcnt lgkmcnt(2)\n" \
"v_mfma_f32_16x16x32_bf16 v[128:131], a[160:163], v[96:99], v[128:131]\n" \
"ds_read_b128 a[136:139], v48 offset:2048\n" \
"ds_read_b128 a[140:143], v48 offset:3072\n" \
"v_mfma_f32_16x16x32_bf16 v[128:131], a[176:179], v[100:103], v[128:131]\n" \
"v_exp_f32_e32 v80, v80\n" \
"v_mfma_f32_16x16x32_bf16 v[132:135], a[180:183], v[100:103], v[132:135]\n" \
"ds_read_b128 a[144:147], v48 offset:4096\n" \
"ds_read_b128 a[148:151], v48 offset:5120\n" \
"v_mfma_f32_16x16x32_bf16 v[132:135], a[164:167], v[96:99], v[132:135]\n" \
"v_exp_f32_e32 v81, v81\n" \
"v_mfma_f32_16x16x32_bf16 v[136:139], a[168:171], v[96:99], v[136:139]\n" \
"ds_read_b128 a[152:155], v48 offset:6144\n" \
"ds_read_b128 a[156:159], v48 offset:7168\n" \
"v_mfma_f32_16x16x32_bf16 v[136:139], a[184:187], v[100:103], v[136:139]\n" \
"v_exp_f32_e32 v82, v82\n" \
"v_mfma_f32_16x16x32_bf16 v[140:143], a[188:191], v[100:103], v[140:143]\n" \
"v_exp_f32_e32 v83, v83\n" \
"v_mfma_f32_16x16x32_bf16 v[140:143], a[172:175], v[96:99], v[140:143]\n" \
"v_exp_f32_e32 v84, v84\n" \
"v_smfmac_f32_16x16x64_bf16 a[192:195], a[212:215], v[96:103], v21\n" \
"v_mfma_f32_16x16x32_bf16 v[160:163], a[160:163], v[104:107], v[160:163]\n" \
"v_exp_f32_e32 v85, v85\n" \
"v_mfma_f32_16x16x32_bf16 v[160:163], a[176:179], v[108:111], v[160:163]\n" \
"v_exp_f32_e32 v86, v86\n" \
"v_mfma_f32_16x16x32_bf16 v[164:167], a[180:183], v[108:111], v[164:167]\n" \
"v_exp_f32_e32 v87, v87\n" \
"v_mfma_f32_16x16x32_bf16 v[164:167], a[164:167], v[104:107], v[164:167]\n" \
"v_cvt_pk_bf16_f32 v116, v80, v81\n" \
"v_cvt_pk_bf16_f32 v117, v82, v83\n" \
"v_mfma_f32_16x16x32_bf16 v[168:171], a[168:171], v[104:107], v[168:171]\n" \
"v_cvt_pk_bf16_f32 v118, v84, v85\n" \
"v_cvt_pk_bf16_f32 v119, v86, v87\n" \
"v_mfma_f32_16x16x32_bf16 v[168:171], a[184:187], v[108:111], v[168:171]\n" \
"v_exp_f32_e32 v88, v88\n" \
"v_mfma_f32_16x16x32_bf16 v[172:175], a[188:191], v[108:111], v[172:175]\n" \
"v_exp_f32_e32 v89, v89\n" \
"v_mfma_f32_16x16x32_bf16 v[172:175], a[172:175], v[104:107], v[172:175]\n" \
"v_exp_f32_e32 v90, v90\n" \
"v_smfmac_f32_16x16x64_bf16 a[196:199], a[212:215], v[104:111], v21\n" \
"v_mfma_f32_16x16x32_bf16 v[192:195], a[160:163], v[112:115], v[192:195]\n" \
"v_exp_f32_e32 v91, v91\n" \
"v_mfma_f32_16x16x32_bf16 v[192:195], a[176:179], v[116:119], v[192:195]\n" \
"v_exp_f32_e32 v92, v92\n" \
"v_mfma_f32_16x16x32_bf16 v[196:199], a[180:183], v[116:119], v[196:199]\n" \
"v_exp_f32_e32 v93, v93\n" \
"v_mfma_f32_16x16x32_bf16 v[196:199], a[164:167], v[112:115], v[196:199]\n" \
"v_exp_f32_e32 v94, v94\n" \
"v_mfma_f32_16x16x32_bf16 v[200:203], a[168:171], v[112:115], v[200:203]\n" \
"v_exp_f32_e32 v95, v95\n" \
"v_mfma_f32_16x16x32_bf16 v[200:203], a[184:187], v[116:119], v[200:203]\n" \
"v_cvt_pk_bf16_f32 v124, v88, v89\n" \
"v_cvt_pk_bf16_f32 v125, v90, v91\n" \
"v_mfma_f32_16x16x32_bf16 v[204:207], a[188:191], v[116:119], v[204:207]\n" \
"v_cvt_pk_bf16_f32 v126, v92, v93\n" \
"v_cvt_pk_bf16_f32 v127, v94, v95\n" \
"v_mfma_f32_16x16x32_bf16 v[204:207], a[172:175], v[112:115], v[204:207]\n" \
"v_smfmac_f32_16x16x64_bf16 a[200:203], a[212:215], v[112:119], v21\n" \
"s_waitcnt vmcnt(0) lgkmcnt(0)\n" \
"s_barrier\n" \
"v_mfma_f32_16x16x32_bf16 v[224:227], a[160:163], v[120:123], v[224:227]\n" \
"s_cbranch_scc1 L_h1_%=\n" \
"L_hr1_%=:\n" \
"v_mfma_f32_16x16x32_bf16 v[224:227], a[176:179], v[124:127], v[224:227]\n" \
"s_xor_b32 m0, s65, 0x12000\n" \
"ds_read_b128 a[160:163], v49 offset:8192\n" \
"v_mfma_f32_16x16x32_bf16 v[228:231], a[180:183], v[124:127], v[228:231]\n" \
"buffer_load_dwordx4 v50, s[8:11], s64 offen offset:0 sc1 lds\n" \
"v_mfma_f32_16x16x32_bf16 v[228:231], a[164:167], v[120:123], v[228:231]\n" \
"ds_read_b128 a[176:179], v49 offset:12288\n" \
"ds_read_b128 a[180:183], v49 offset:13312\n" \
"v_mfma_f32_16x16x32_bf16 v[232:235], a[168:171], v[120:123], v[232:235]\n" \
"v_mfma_f32_16x16x32_bf16 v[232:235], a[184:187], v[124:127], v[232:235]\n" \
"ds_read_b128 a[164:167], v49 offset:9216\n" \
"ds_read_b128 a[168:171], v49 offset:10240\n" \
"v_mfma_f32_16x16x32_bf16 v[236:239], a[188:191], v[124:127], v[236:239]\n" \
"v_mfma_f32_16x16x32_bf16 v[236:239], a[172:175], v[120:123], v[236:239]\n" \
"ds_read_b128 a[184:187], v49 offset:14336\n" \
"ds_read_b128 a[188:191], v49 offset:15360\n" \
"v_mfma_f32_16x16x32_bf16 v[64:67], a[128:131], a[0:3], v[32:35]\n" \
"v_mfma_f32_16x16x32_bf16 v[64:67], a[132:135], a[4:7], v[64:67]\n" \
"buffer_load_dwordx4 v50, s[8:11], s64 offen offset:1024 sc1 lds\n" \
"v_mfma_f32_16x16x32_bf16 v[64:67], a[136:139], a[8:11], v[64:67]\n" \
"s_xor_b32 m0, s65, 0x6000\n" \
"ds_read_b128 a[172:175], v49 offset:11264\n" \
"v_mfma_f32_16x16x32_bf16 v[64:67], a[140:143], a[12:15], v[64:67]\n" \
"v_add_u32_e32 v49, 0x10000, v48\n" \
"v_mfma_f32_16x16x32_bf16 v[68:71], a[156:159], a[12:15], v[32:35]\n" \
"v_mfma_f32_16x16x32_bf16 v[68:71], a[152:155], a[8:11], v[68:71]\n" \
"v_mfma_f32_16x16x32_bf16 v[68:71], a[148:151], a[4:7], v[68:71]\n" \
"v_mfma_f32_16x16x32_bf16 v[68:71], a[144:147], a[0:3], v[68:71]\n" \
"v_mfma_f32_16x16x32_bf16 v[72:75], a[128:131], a[16:19], v[36:39]\n" \
"v_mfma_f32_16x16x32_bf16 v[72:75], a[132:135], a[20:23], v[72:75]\n" \
"v_mfma_f32_16x16x32_bf16 v[72:75], a[136:139], a[24:27], v[72:75]\n" \
"buffer_load_dwordx4 v51, s[8:11], s64 offen offset:0 sc1 lds\n" \
"v_mfma_f32_16x16x32_bf16 v[72:75], a[140:143], a[28:31], v[72:75]\n" \
"v_exp_f32_e32 v64, v64\n" \
"v_mfma_f32_16x16x32_bf16 v[76:79], a[156:159], a[28:31], v[36:39]\n" \
"v_exp_f32_e32 v65, v65\n" \
"v_mfma_f32_16x16x32_bf16 v[76:79], a[152:155], a[24:27], v[76:79]\n" \
"v_exp_f32_e32 v66, v66\n" \
"v_mfma_f32_16x16x32_bf16 v[76:79], a[148:151], a[20:23], v[76:79]\n" \
"v_exp_f32_e32 v67, v67\n" \
"v_mfma_f32_16x16x32_bf16 v[76:79], a[144:147], a[16:19], v[76:79]\n" \
"v_exp_f32_e32 v68, v68\n" \
"v_mfma_f32_16x16x32_bf16 v[80:83], a[128:131], a[32:35], v[40:43]\n" \
"v_exp_f32_e32 v69, v69\n" \
"v_mfma_f32_16x16x32_bf16 v[80:83], a[132:135], a[36:39], v[80:83]\n" \
"v_exp_f32_e32 v70, v70\n" \
"v_mfma_f32_16x16x32_bf16 v[80:83], a[136:139], a[40:43], v[80:83]\n" \
"v_exp_f32_e32 v71, v71\n" \
"v_mfma_f32_16x16x32_bf16 v[80:83], a[140:143], a[44:47], v[80:83]\n" \
"buffer_load_dwordx4 v51, s[8:11], s64 offen offset:1024 sc1 lds\n" \
"v_mfma_f32_16x16x32_bf16 v[84:87], a[156:159], a[44:47], v[40:43]\n" \
"v_exp_f32_e32 v72, v72\n" \
"v_mfma_f32_16x16x32_bf16 v[84:87], a[152:155], a[40:43], v[84:87]\n" \
"v_exp_f32_e32 v73, v73\n" \
"v_mfma_f32_16x16x32_bf16 v[84:87], a[148:151], a[36:39], v[84:87]\n" \
"v_exp_f32_e32 v74, v74\n" \
"v_mfma_f32_16x16x32_bf16 v[84:87], a[144:147], a[32:35], v[84:87]\n" \
"v_exp_f32_e32 v75, v75\n" \
"v_mfma_f32_16x16x32_bf16 v[88:91], a[128:131], a[48:51], v[44:47]\n" \
"s_xor_b32 m0, s65, 0x14000\n" \
"ds_read_b128 a[128:131], v48 offset:8192\n" \
"v_mfma_f32_16x16x32_bf16 v[88:91], a[132:135], a[52:55], v[88:91]\n" \
"v_exp_f32_e32 v76, v76\n" \
"v_mfma_f32_16x16x32_bf16 v[88:91], a[136:139], a[56:59], v[88:91]\n" \
"ds_read_b128 a[132:135], v48 offset:9216\n" \
"ds_read_b128 a[136:139], v48 offset:10240\n" \
"v_mfma_f32_16x16x32_bf16 v[88:91], a[140:143], a[60:63], v[88:91]\n" \
"v_exp_f32_e32 v77, v77\n" \
"v_mfma_f32_16x16x32_bf16 v[92:95], a[156:159], a[60:63], v[44:47]\n" \
"buffer_load_dwordx4 v52, s[8:11], s64 offen offset:0 sc1 lds\n" \
"v_mfma_f32_16x16x32_bf16 v[92:95], a[152:155], a[56:59], v[92:95]\n" \
"ds_read_b128 a[140:143], v48 offset:11264\n" \
"ds_read_b128 a[152:155], v48 offset:14336\n" \
"v_mfma_f32_16x16x32_bf16 v[92:95], a[148:151], a[52:55], v[92:95]\n" \
"v_exp_f32_e32 v78, v78\n" \
"v_mfma_f32_16x16x32_bf16 v[92:95], a[144:147], a[48:51], v[92:95]\n" \
"ds_read_b128 a[144:147], v48 offset:12288\n" \
"s_waitcnt lgkmcnt(6)\n" \
"v_mfma_f32_16x16x32_bf16 v[144:147], a[160:163], v[96:99], v[144:147]\n" \
"ds_read_b128 a[148:151], v48 offset:13312\n" \
"ds_read_b128 a[156:159], v48 offset:15360\n" \
"v_mfma_f32_16x16x32_bf16 v[144:147], a[176:179], v[100:103], v[144:147]\n" \
"v_exp_f32_e32 v79, v79\n" \
"v_mfma_f32_16x16x32_bf16 v[148:151], a[180:183], v[100:103], v[148:151]\n" \
"v_exp_f32_e32 v80, v80\n" \
"v_mfma_f32_16x16x32_bf16 v[148:151], a[164:167], v[96:99], v[148:151]\n" \
"v_exp_f32_e32 v81, v81\n" \
"v_mfma_f32_16x16x32_bf16 v[152:155], a[168:171], v[96:99], v[152:155]\n" \
"buffer_load_dwordx4 v52, s[8:11], s64 offen offset:1024 sc1 lds\n" \
"v_mfma_f32_16x16x32_bf16 v[152:155], a[184:187], v[100:103], v[152:155]\n" \
"s_mov_b32 m0, s65\n" \
"s_xor_b32 s65, s65, 0x4000\n" \
"v_mfma_f32_16x16x32_bf16 v[156:159], a[188:191], v[100:103], v[156:159]\n" \
"v_exp_f32_e32 v82, v82\n" \
"v_mfma_f32_16x16x32_bf16 v[156:159], a[172:175], v[96:99], v[156:159]\n" \
"v_exp_f32_e32 v83, v83\n" \
"v_mfma_f32_16x16x32_bf16 v[176:179], a[160:163], v[104:107], v[176:179]\n" \
"v_exp_f32_e32 v84, v84\n" \
"v_mfma_f32_16x16x32_bf16 v[176:179], a[176:179], v[108:111], v[176:179]\n" \
"v_exp_f32_e32 v85, v85\n" \
"v_mfma_f32_16x16x32_bf16 v[180:183], a[180:183], v[108:111], v[180:183]\n" \
"v_exp_f32_e32 v86, v86\n" \
"v_mfma_f32_16x16x32_bf16 v[180:183], a[164:167], v[104:107], v[180:183]\n" \
"v_exp_f32_e32 v87, v87\n" \
"v_mfma_f32_16x16x32_bf16 v[184:187], a[168:171], v[104:107], v[184:187]\n" \
"v_exp_f32_e32 v88, v88\n" \
"v_mfma_f32_16x16x32_bf16 v[184:187], a[184:187], v[108:111], v[184:187]\n" \
"buffer_load_dwordx4 v53, s[8:11], s64 offen offset:0 sc1 lds\n" \
"v_mfma_f32_16x16x32_bf16 v[188:191], a[188:191], v[108:111], v[188:191]\n" \
"v_exp_f32_e32 v89, v89\n" \
"v_mfma_f32_16x16x32_bf16 v[188:191], a[172:175], v[104:107], v[188:191]\n" \
"v_exp_f32_e32 v90, v90\n" \
"v_mfma_f32_16x16x32_bf16 v[208:211], a[160:163], v[112:115], v[208:211]\n" \
"v_exp_f32_e32 v91, v91\n" \
"v_mfma_f32_16x16x32_bf16 v[208:211], a[176:179], v[116:119], v[208:211]\n" \
"v_cvt_pk_bf16_f32 v96, v64, v65\n" \
"v_cvt_pk_bf16_f32 v97, v66, v67\n" \
"v_mfma_f32_16x16x32_bf16 v[212:215], a[180:183], v[116:119], v[212:215]\n" \
"v_cvt_pk_bf16_f32 v98, v68, v69\n" \
"v_cvt_pk_bf16_f32 v99, v70, v71\n" \
"v_mfma_f32_16x16x32_bf16 v[212:215], a[164:167], v[112:115], v[212:215]\n" \
"v_cvt_pk_bf16_f32 v104, v72, v73\n" \
"v_cvt_pk_bf16_f32 v105, v74, v75\n" \
"v_mfma_f32_16x16x32_bf16 v[216:219], a[168:171], v[112:115], v[216:219]\n" \
"v_cvt_pk_bf16_f32 v106, v76, v77\n" \
"v_cvt_pk_bf16_f32 v107, v78, v79\n" \
"v_mfma_f32_16x16x32_bf16 v[216:219], a[184:187], v[116:119], v[216:219]\n" \
"v_xor_b32_e32 v48, 0x4000, v48\n" \
"v_mfma_f32_16x16x32_bf16 v[220:223], a[188:191], v[116:119], v[220:223]\n" \
"buffer_load_dwordx4 v53, s[8:11], s64 offen offset:1024 sc1 lds\n" \
"v_mfma_f32_16x16x32_bf16 v[220:223], a[172:175], v[112:115], v[220:223]\n" \
"s_add_u32 s64, s64, 0x8000\n" \
"s_sub_u32 s66, s66, 1\n" \
"v_mfma_f32_16x16x32_bf16 v[240:243], a[160:163], v[120:123], v[240:243]\n" \
"v_cvt_pk_bf16_f32 v112, v80, v81\n" \
"v_cvt_pk_bf16_f32 v113, v82, v83\n" \
"v_mfma_f32_16x16x32_bf16 v[240:243], a[176:179], v[124:127], v[240:243]\n" \
"v_cvt_pk_bf16_f32 v114, v84, v85\n" \
"v_cvt_pk_bf16_f32 v115, v86, v87\n" \
"v_mfma_f32_16x16x32_bf16 v[244:247], a[180:183], v[124:127], v[244:247]\n" \
"v_mfma_f32_16x16x32_bf16 v[244:247], a[164:167], v[120:123], v[244:247]\n" \
"v_mfma_f32_16x16x32_bf16 v[248:251], a[168:171], v[120:123], v[248:251]\n" \
"v_mfma_f32_16x16x32_bf16 v[248:251], a[184:187], v[124:127], v[248:251]\n" \
"s_cbranch_scc0 L_t1_%=\n" \
"v_mfma_f32_16x16x32_bf16 v[252:255], a[188:191], v[124:127], v[252:255]\n" \
"v_mfma_f32_16x16x32_bf16 v[252:255], a[172:175], v[120:123], v[252:255]\n" \
"v_smfmac_f32_16x16x64_bf16 a[204:207], a[212:215], v[120:127], v21\n" \
"s_waitcnt lgkmcnt(0)\n" \
"s_barrier\n" \
"L_tr_%=:\n" \
"L_pf11:\n" \
"s_cbranch_execz L_pf15\n" \
"s_nop 0\n" \
"s_cmp_lg_u32 s16, 0\n" \
"s_cbranch_scc1 L_sw_%=\n" \
"v_mfma_f32_16x16x32_bf16 v[64:67], a[128:131], a[0:3], v[32:35]\n" \
"s_xor_b32 m0, s65, 0x12000\n" \
"ds_read_b128 a[160:163], v49 offset:0\n" \
"v_mfma_f32_16x16x32_bf16 v[64:67], a[132:135], a[4:7], v[64:67]\n" \
"buffer_load_dwordx4 v50, s[8:11], s64 offen offset:0 sc1 lds\n" \
"v_mfma_f32_16x16x32_bf16 v[64:67], a[136:139], a[8:11], v[64:67]\n" \
"ds_read_b128 a[176:179], v49 offset:4096\n" \
"ds_read_b128 a[180:183], v49 offset:5120\n" \
"v_mfma_f32_16x16x32_bf16 v[64:67], a[140:143], a[12:15], v[64:67]\n" \
"s_mov_b32 s65, s13\n" \
"v_add_u32_e32 v63, s15, v20\n" \
"v_mfma_f32_16x16x32_bf16 v[68:71], a[156:159], a[12:15], v[32:35]\n" \
"s_add_u32 s67, s14, 40960\n" \
"ds_read_b128 a[164:167], v49 offset:1024\n" \
"v_mfma_f32_16x16x32_bf16 v[68:71], a[152:155], a[8:11], v[68:71]\n" \
"ds_read_b128 a[168:171], v49 offset:2048\n" \
"ds_read_b128 a[184:187], v49 offset:6144\n" \
"v_mfma_f32_16x16x32_bf16 v[68:71], a[148:151], a[4:7], v[68:71]\n" \
"buffer_load_dwordx4 v50, s[8:11], s64 offen offset:1024 sc1 lds\n" \
"v_mfma_f32_16x16x32_bf16 v[68:71], a[144:147], a[0:3], v[68:71]\n" \
"v_add_u32_e32 v50, 0x6000, v63\n" \
"s_xor_b32 m0, s65, 0x8000\n" \
"v_mfma_f32_16x16x32_bf16 v[72:75], a[128:131], a[16:19], v[36:39]\n" \
"v_cvt_pk_bf16_f32 v120, v88, v89\n" \
"v_cvt_pk_bf16_f32 v121, v90, v91\n" \
"v_mfma_f32_16x16x32_bf16 v[72:75], a[132:135], a[20:23], v[72:75]\n" \
"ds_read_b128 a[188:191], v49 offset:7168\n" \
"ds_read_b128 a[172:175], v49 offset:3072\n" \
"v_mfma_f32_16x16x32_bf16 v[72:75], a[136:139], a[24:27], v[72:75]\n" \
"v_exp_f32_e32 v92, v92\n" \
"v_mfma_f32_16x16x32_bf16 v[72:75], a[140:143], a[28:31], v[72:75]\n" \
"v_exp_f32_e32 v93, v93\n" \
"v_mfma_f32_16x16x32_bf16 v[76:79], a[156:159], a[28:31], v[36:39]\n" \
"buffer_load_dwordx4 v50, s[8:11], s67 offen offset:0 sc1 lds\n" \
"v_mfma_f32_16x16x32_bf16 v[76:79], a[152:155], a[24:27], v[76:79]\n" \
"v_exp_f32_e32 v94, v94\n" \
"v_mfma_f32_16x16x32_bf16 v[76:79], a[148:151], a[20:23], v[76:79]\n" \
"v_exp_f32_e32 v95, v95\n" \
"v_mfma_f32_16x16x32_bf16 v[76:79], a[144:147], a[16:19], v[76:79]\n" \
"v_cvt_pk_bf16_f32 v122, v92, v93\n" \
"v_cvt_pk_bf16_f32 v123, v94, v95\n" \
"v_mfma_f32_16x16x32_bf16 v[80:83], a[128:131], a[32:35], v[40:43]\n" \
"v_exp_f32_e32 v64, v64\n" \
"v_mfma_f32_16x16x32_bf16 v[80:83], a[132:135], a[36:39], v[80:83]\n" \
"v_exp_f32_e32 v65, v65\n" \
"v_mfma_f32_16x16x32_bf16 v[80:83], a[136:139], a[40:43], v[80:83]\n" \
"buffer_load_dwordx4 v50, s[8:11], s67 offen offset:1024 sc1 lds\n" \
"v_mfma_f32_16x16x32_bf16 v[80:83], a[140:143], a[44:47], v[80:83]\n" \
"v_exp_f32_e32 v66, v66\n" \
"v_mfma_f32_16x16x32_bf16 v[84:87], a[156:159], a[44:47], v[40:43]\n" \
"v_exp_f32_e32 v67, v67\n" \
"v_mfma_f32_16x16x32_bf16 v[84:87], a[152:155], a[40:43], v[84:87]\n" \
"v_exp_f32_e32 v68, v68\n" \
"v_mfma_f32_16x16x32_bf16 v[84:87], a[148:151], a[36:39], v[84:87]\n" \
"v_exp_f32_e32 v69, v69\n" \
"v_mfma_f32_16x16x32_bf16 v[84:87], a[144:147], a[32:35], v[84:87]\n" \
"v_exp_f32_e32 v70, v70\n" \
"v_mfma_f32_16x16x32_bf16 v[88:91], a[128:131], a[48:51], v[44:47]\n" \
"v_exp_f32_e32 v71, v71\n" \
"v_mfma_f32_16x16x32_bf16 v[88:91], a[132:135], a[52:55], v[88:91]\n" \
"v_cvt_pk_bf16_f32 v100, v64, v65\n" \
"v_cvt_pk_bf16_f32 v101, v66, v67\n" \
"v_mfma_f32_16x16x32_bf16 v[88:91], a[136:139], a[56:59], v[88:91]\n" \
"v_cvt_pk_bf16_f32 v102, v68, v69\n" \
"v_cvt_pk_bf16_f32 v103, v70, v71\n" \
"v_mfma_f32_16x16x32_bf16 v[88:91], a[140:143], a[60:63], v[88:91]\n" \
"buffer_load_dwordx4 v50, s[8:11], s67 offen offset:2048 sc1 lds\n" \
"v_mfma_f32_16x16x32_bf16 v[92:95], a[156:159], a[60:63], v[44:47]\n" \
"v_exp_f32_e32 v72, v72\n" \
"v_mfma_f32_16x16x32_bf16 v[92:95], a[152:155], a[56:59], v[92:95]\n" \
"v_exp_f32_e32 v73, v73\n" \
"v_mfma_f32_16x16x32_bf16 v[92:95], a[148:151], a[52:55], v[92:95]\n" \
"v_exp_f32_e32 v74, v74\n" \
"v_mfma_f32_16x16x32_bf16 v[92:95], a[144:147], a[48:51], v[92:95]\n" \
"v_cvt_pk_bf16_f32 v108, v72, v73\n" \
"s_waitcnt lgkmcnt(0)\n" \
"v_mfma_f32_16x16x32_bf16 v[128:131], a[160:163], v[96:99], v[128:131]\n" \
"buffer_load_dwordx4 v50, s[8:11], s67 offen offset:3072 sc1 lds\n" \
"v_mfma_f32_16x16x32_bf16 v[128:131], a[176:179], v[100:103], v[128:131]\n" \
"v_exp_f32_e32 v75, v75\n" \
"v_mfma_f32_16x16x32_bf16 v[132:135], a[180:183], v[100:103], v[132:135]\n" \
"v_exp_f32_e32 v76, v76\n" \
"v_mfma_f32_16x16x32_bf16 v[132:135], a[164:167], v[96:99], v[132:135]\n" \
"v_exp_f32_e32 v77, v77\n" \
"v_mfma_f32_16x16x32_bf16 v[136:139], a[168:171], v[96:99], v[136:139]\n" \
"v_exp_f32_e32 v78, v78\n" \
"v_mfma_f32_16x16x32_bf16 v[136:139], a[184:187], v[100:103], v[136:139]\n" \
"v_exp_f32_e32 v79, v79\n" \
"v_mfma_f32_16x16x32_bf16 v[140:143], a[188:191], v[100:103], v[140:143]\n" \
"v_cvt_pk_bf16_f32 v109, v74, v75\n" \
"v_cvt_pk_bf16_f32 v110, v76, v77\n" \
"v_mfma_f32_16x16x32_bf16 v[140:143], a[172:175], v[96:99], v[140:143]\n" \
"v_cvt_pk_bf16_f32 v111, v78, v79\n" \
"s_mov_b32 s64, s14\n" \
"v_smfmac_f32_16x16x64_bf16 a[192:195], a[212:215], v[96:103], v21\n" \
"v_mfma_f32_16x16x32_bf16 v[160:163], a[160:163], v[104:107], v[160:163]\n" \
"v_add_u32_e32 v51, 0x12000, v63\n" \
"s_xor_b32 m0, s65, 0xa000\n" \
"v_mfma_f32_16x16x32_bf16 v[160:163], a[176:179], v[108:111], v[160:163]\n" \
"buffer_load_dwordx4 v51, s[8:11], s64 offen offset:0 sc1 lds\n" \
"v_mfma_f32_16x16x32_bf16 v[164:167], a[180:183], v[108:111], v[164:167]\n" \
"v_exp_f32_e32 v80, v80\n" \
"v_mfma_f32_16x16x32_bf16 v[164:167], a[164:167], v[104:107], v[164:167]\n" \
"v_exp_f32_e32 v81, v81\n" \
"v_mfma_f32_16x16x32_bf16 v[168:171], a[168:171], v[104:107], v[168:171]\n" \
"v_exp_f32_e32 v82, v82\n" \
"v_mfma_f32_16x16x32_bf16 v[168:171], a[184:187], v[108:111], v[168:171]\n" \
"v_exp_f32_e32 v83, v83\n" \
"v_mfma_f32_16x16x32_bf16 v[172:175], a[188:191], v[108:111], v[172:175]\n" \
"v_exp_f32_e32 v84, v84\n" \
"v_mfma_f32_16x16x32_bf16 v[172:175], a[172:175], v[104:107], v[172:175]\n" \
"v_exp_f32_e32 v85, v85\n" \
"v_smfmac_f32_16x16x64_bf16 a[196:199], a[212:215], v[104:111], v21\n" \
"v_mfma_f32_16x16x32_bf16 v[192:195], a[160:163], v[112:115], v[192:195]\n" \
"v_exp_f32_e32 v86, v86\n" \
"v_exp_f32_e32 v87, v87\n" \
"v_cvt_pk_bf16_f32 v116, v80, v81\n" \
"v_cvt_pk_bf16_f32 v117, v82, v83\n" \
"v_cvt_pk_bf16_f32 v118, v84, v85\n" \
"v_cvt_pk_bf16_f32 v119, v86, v87\n" \
"v_exp_f32_e32 v88, v88\n" \
"v_exp_f32_e32 v89, v89\n" \
"v_mfma_f32_16x16x32_bf16 v[192:195], a[176:179], v[116:119], v[192:195]\n" \
"buffer_load_dwordx4 v51, s[8:11], s64 offen offset:1024 sc1 lds\n" \
"v_mfma_f32_16x16x32_bf16 v[196:199], a[180:183], v[116:119], v[196:199]\n" \
"ds_read_b128 a[128:131], v48 offset:0\n" \
"ds_read_b128 a[132:135], v48 offset:1024\n" \
"v_mfma_f32_16x16x32_bf16 v[196:199], a[164:167], v[112:115], v[196:199]\n" \
"ds_read_b128 a[136:139], v48 offset:2048\n" \
"ds_read_b128 a[140:143], v48 offset:3072\n" \
"ds_read_b128 a[144:147], v48 offset:4096\n" \
"ds_read_b128 a[148:151], v48 offset:5120\n" \
"ds_read_b128 a[152:155], v48 offset:6144\n" \
"ds_read_b128 a[156:159], v48 offset:7168\n" \
"v_mfma_f32_16x16x32_bf16 v[200:203], a[168:171], v[112:115], v[200:203]\n" \
"v_exp_f32_e32 v90, v90\n" \
"v_mfma_f32_16x16x32_bf16 v[200:203], a[184:187], v[116:119], v[200:203]\n" \
"v_exp_f32_e32 v91, v91\n" \
"v_mfma_f32_16x16x32_bf16 v[204:207], a[188:191], v[116:119], v[204:207]\n" \
"v_exp_f32_e32 v92, v92\n" \
"v_mfma_f32_16x16x32_bf16 v[204:207], a[172:175], v[112:115], v[204:207]\n" \
"v_exp_f32_e32 v93, v93\n" \
"v_smfmac_f32_16x16x64_bf16 a[200:203], a[212:215], v[112:119], v21\n" \
"v_mfma_f32_16x16x32_bf16 v[224:227], a[160:163], v[120:123], v[224:227]\n" \
"v_exp_f32_e32 v94, v94\n" \
"v_exp_f32_e32 v95, v95\n" \
"v_cvt_pk_bf16_f32 v124, v88, v89\n" \
"v_cvt_pk_bf16_f32 v125, v90, v91\n" \
"v_cvt_pk_bf16_f32 v126, v92, v93\n" \
"v_cvt_pk_bf16_f32 v127, v94, v95\n" \
"v_add_u32_e32 v52, 0x14000, v63\n" \
"v_add_u32_e32 v53, 0x20000, v63\n" \
"v_mfma_f32_16x16x32_bf16 v[224:227], a[176:179], v[124:127], v[224:227]\n" \
"buffer_load_dwordx4 v51, s[8:11], s64 offen offset:2048 sc1 lds\n" \
"v_mfma_f32_16x16x32_bf16 v[228:231], a[180:183], v[124:127], v[228:231]\n" \
"buffer_load_dwordx4 v51, s[8:11], s64 offen offset:3072 sc1 lds\n" \
"v_mfma_f32_16x16x32_bf16 v[228:231], a[164:167], v[120:123], v[228:231]\n" \
"v_readlane_b32 s93, v11, 17\n" \
"v_readlane_b32 s92, v11, 22\n" \
"v_mfma_f32_16x16x32_bf16 v[232:235], a[168:171], v[120:123], v[232:235]\n" \
"s_mov_b32 s66, s19\n" \
"v_mfma_f32_16x16x32_bf16 v[232:235], a[184:187], v[124:127], v[232:235]\n" \
"v_mfma_f32_16x16x32_bf16 v[236:239], a[188:191], v[124:127], v[236:239]\n" \
"v_mfma_f32_16x16x32_bf16 v[236:239], a[172:175], v[120:123], v[236:239]\n" \
"s_waitcnt vmcnt(10) lgkmcnt(0)\n" \
"s_barrier\n" \
"v_mfma_f32_16x16x32_bf16 v[64:67], a[128:131], a[0:3], v[32:35]\n" \
"s_xor_b32 m0, s65, 0x18000\n" \
"ds_read_b128 a[160:163], v49 offset:8192\n" \
"v_mfma_f32_16x16x32_bf16 v[64:67], a[132:135], a[4:7], v[64:67]\n" \
"buffer_load_dwordx4 v52, s[8:11], s64 offen offset:0 sc1 lds\n" \
"v_mfma_f32_16x16x32_bf16 v[64:67], a[136:139], a[8:11], v[64:67]\n" \
"ds_read_b128 a[176:179], v49 offset:12288\n" \
"ds_read_b128 a[180:183], v49 offset:13312\n" \
"v_mfma_f32_16x16x32_bf16 v[64:67], a[140:143], a[12:15], v[64:67]\n" \
"buffer_load_dwordx4 v52, s[8:11], s64 offen offset:1024 sc1 lds\n" \
"v_mfma_f32_16x16x32_bf16 v[68:71], a[156:159], a[12:15], v[32:35]\n" \
"ds_read_b128 a[164:167], v49 offset:9216\n" \
"ds_read_b128 a[168:171], v49 offset:10240\n" \
"v_mfma_f32_16x16x32_bf16 v[68:71], a[152:155], a[8:11], v[68:71]\n" \
"ds_read_b128 a[184:187], v49 offset:14336\n" \
"ds_read_b128 a[188:191], v49 offset:15360\n" \
"v_mfma_f32_16x16x32_bf16 v[68:71], a[148:151], a[4:7], v[68:71]\n" \
"v_exp_f32_e32 v64, v64\n" \
"v_mfma_f32_16x16x32_bf16 v[68:71], a[144:147], a[0:3], v[68:71]\n" \
"buffer_load_dwordx4 v52, s[8:11], s64 offen offset:2048 sc1 lds\n" \
"v_mfma_f32_16x16x32_bf16 v[72:75], a[128:131], a[16:19], v[36:39]\n" \
"L_pf12:\n" \
"s_cbranch_execz L_pf16\n" \
"s_nop 0\n" \
"v_exp_f32_e32 v65, v65\n" \
"v_mfma_f32_16x16x32_bf16 v[72:75], a[132:135], a[20:23], v[72:75]\n" \
"v_exp_f32_e32 v66, v66\n" \
"v_mfma_f32_16x16x32_bf16 v[72:75], a[136:139], a[24:27], v[72:75]\n" \
"buffer_load_dwordx4 v52, s[8:11], s64 offen offset:3072 sc1 lds\n" \
"v_mfma_f32_16x16x32_bf16 v[72:75], a[140:143], a[28:31], v[72:75]\n" \
"s_mov_b32 m0, s65\n" \
"ds_read_b128 a[172:175], v49 offset:11264\n" \
"v_mfma_f32_16x16x32_bf16 v[76:79], a[156:159], a[28:31], v[36:39]\n" \
"v_exp_f32_e32 v67, v67\n" \
"v_mfma_f32_16x16x32_bf16 v[76:79], a[152:155], a[24:27], v[76:79]\n" \
"v_exp_f32_e32 v68, v68\n" \
"v_mfma_f32_16x16x32_bf16 v[76:79], a[148:151], a[20:23], v[76:79]\n" \
"buffer_load_dwordx4 v53, s[8:11], s64 offen offset:0 sc1 lds\n" \
"v_mfma_f32_16x16x32_bf16 v[76:79], a[144:147], a[16:19], v[76:79]\n" \
"v_exp_f32_e32 v69, v69\n" \
"v_mfma_f32_16x16x32_bf16 v[80:83], a[128:131], a[32:35], v[40:43]\n" \
"v_exp_f32_e32 v70, v70\n" \
"v_mfma_f32_16x16x32_bf16 v[80:83], a[132:135], a[36:39], v[80:83]\n" \
"buffer_load_dwordx4 v53, s[8:11], s64 offen offset:1024 sc1 lds\n" \
"v_mfma_f32_16x16x32_bf16 v[80:83], a[136:139], a[40:43], v[80:83]\n" \
"v_exp_f32_e32 v71, v71\n" \
"v_mfma_f32_16x16x32_bf16 v[80:83], a[140:143], a[44:47], v[80:83]\n" \
"v_exp_f32_e32 v72, v72\n" \
"v_mfma_f32_16x16x32_bf16 v[84:87], a[156:159], a[44:47], v[40:43]\n" \
"v_exp_f32_e32 v73, v73\n" \
"v_mfma_f32_16x16x32_bf16 v[84:87], a[152:155], a[40:43], v[84:87]\n" \
"buffer_load_dwordx4 v53, s[8:11], s64 offen offset:2048 sc1 lds\n" \
"v_mfma_f32_16x16x32_bf16 v[84:87], a[148:151], a[36:39], v[84:87]\n" \
"v_exp_f32_e32 v74, v74\n" \
"v_mfma_f32_16x16x32_bf16 v[84:87], a[144:147], a[32:35], v[84:87]\n" \
"v_exp_f32_e32 v75, v75\n" \
"v_mfma_f32_16x16x32_bf16 v[88:91], a[128:131], a[48:51], v[44:47]\n" \
"buffer_load_dwordx4 v53, s[8:11], s64 offen offset:3072 sc1 lds\n" \
"v_mfma_f32_16x16x32_bf16 v[88:91], a[132:135], a[52:55], v[88:91]\n" \
"s_add_u32 m0, s93, 0x0\n" \
"ds_read_b128 a[128:131], v48 offset:8192\n" \
"v_mfma_f32_16x16x32_bf16 v[88:91], a[136:139], a[56:59], v[88:91]\n" \
"ds_read_b128 a[132:135], v48 offset:9216\n" \
"ds_read_b128 a[136:139], v48 offset:10240\n" \
"v_mfma_f32_16x16x32_bf16 v[88:91], a[140:143], a[60:63], v[88:91]\n" \
"ds_read_b128 a[140:143], v48 offset:11264\n" \
"v_add_u32_e32 v49, 0x10000, v48\n" \
"v_mfma_f32_16x16x32_bf16 v[92:95], a[156:159], a[60:63], v[44:47]\n" \
"buffer_load_dwordx4 v0, s[80:83], s92 offen offset:0 lds\n" \
"v_mfma_f32_16x16x32_bf16 v[92:95], a[152:155], a[56:59], v[92:95]\n" \
"ds_read_b128 a[152:155], v48 offset:14336\n" \
"ds_read_b128 a[156:159], v48 offset:15360\n" \
"v_mfma_f32_16x16x32_bf16 v[92:95], a[148:151], a[52:55], v[92:95]\n" \
"ds_read_b128 a[148:151], v48 offset:13312\n" \
"s_xor_b32 s65, s65, 0x8000\n" \
"v_mfma_f32_16x16x32_bf16 v[92:95], a[144:147], a[48:51], v[92:95]\n" \
"ds_read_b128 a[144:147], v48 offset:12288\n" \
"s_waitcnt lgkmcnt(8)\n" \
"v_mfma_f32_16x16x32_bf16 v[144:147], a[160:163], v[96:99], v[144:147]\n" \
"buffer_load_dwordx4 v1, s[80:83], s92 offen offset:1024 lds\n" \
"v_mfma_f32_16x16x32_bf16 v[144:147], a[176:179], v[100:103], v[144:147]\n" \
"v_exp_f32_e32 v76, v76\n" \
"v_mfma_f32_16x16x32_bf16 v[148:151], a[180:183], v[100:103], v[148:151]\n" \
"v_exp_f32_e32 v77, v77\n" \
"v_mfma_f32_16x16x32_bf16 v[148:151], a[164:167], v[96:99], v[148:151]\n" \
"buffer_load_dwordx4 v2, s[80:83], s92 offen offset:2048 lds\n" \
"v_mfma_f32_16x16x32_bf16 v[152:155], a[168:171], v[96:99], v[152:155]\n" \
"v_exp_f32_e32 v78, v78\n" \
"v_mfma_f32_16x16x32_bf16 v[152:155], a[184:187], v[100:103], v[152:155]\n" \
"v_exp_f32_e32 v79, v79\n" \
"v_mfma_f32_16x16x32_bf16 v[156:159], a[188:191], v[100:103], v[156:159]\n" \
"buffer_load_dwordx4 v3, s[80:83], s92 offen offset:3072 lds\n" \
"v_mfma_f32_16x16x32_bf16 v[156:159], a[172:175], v[96:99], v[156:159]\n" \
"s_add_u32 m0, s93, 0x1000\n" \
"v_cvt_pk_bf16_f32 v96, v64, v65\n" \
"v_mfma_f32_16x16x32_bf16 v[176:179], a[160:163], v[104:107], v[176:179]\n" \
"v_exp_f32_e32 v80, v80\n" \
"v_mfma_f32_16x16x32_bf16 v[176:179], a[176:179], v[108:111], v[176:179]\n" \
"v_exp_f32_e32 v81, v81\n" \
"v_mfma_f32_16x16x32_bf16 v[180:183], a[180:183], v[108:111], v[180:183]\n" \
"buffer_load_dwordx4 v4, s[80:83], s92 offen offset:0 lds\n" \
"v_mfma_f32_16x16x32_bf16 v[180:183], a[164:167], v[104:107], v[180:183]\n" \
"v_exp_f32_e32 v82, v82\n" \
"v_mfma_f32_16x16x32_bf16 v[184:187], a[168:171], v[104:107], v[184:187]\n" \
"v_exp_f32_e32 v83, v83\n" \
"v_mfma_f32_16x16x32_bf16 v[184:187], a[184:187], v[108:111], v[184:187]\n" \
"buffer_load_dwordx4 v5, s[80:83], s92 offen offset:1024 lds\n" \
"v_mfma_f32_16x16x32_bf16 v[188:191], a[188:191], v[108:111], v[188:191]\n" \
"v_exp_f32_e32 v84, v84\n" \
"v_mfma_f32_16x16x32_bf16 v[188:191], a[172:175], v[104:107], v[188:191]\n" \
"v_exp_f32_e32 v85, v85\n" \
"v_mfma_f32_16x16x32_bf16 v[208:211], a[160:163], v[112:115], v[208:211]\n" \
"v_exp_f32_e32 v86, v86\n" \
"v_mfma_f32_16x16x32_bf16 v[208:211], a[176:179], v[116:119], v[208:211]\n" \
"buffer_load_dwordx4 v6, s[80:83], s92 offen offset:2048 lds\n" \
"v_mfma_f32_16x16x32_bf16 v[212:215], a[180:183], v[116:119], v[212:215]\n" \
"v_exp_f32_e32 v87, v87\n" \
"v_mfma_f32_16x16x32_bf16 v[212:215], a[164:167], v[112:115], v[212:215]\n" \
"v_exp_f32_e32 v88, v88\n" \
"v_mfma_f32_16x16x32_bf16 v[216:219], a[168:171], v[112:115], v[216:219]\n" \
"buffer_load_dwordx4 v7, s[80:83], s92 offen offset:3072 lds\n" \
"v_mfma_f32_16x16x32_bf16 v[216:219], a[184:187], v[116:119], v[216:219]\n" \
"s_add_u32 m0, s93, 0x2000\n" \
"v_cvt_pk_bf16_f32 v97, v66, v67\n" \
"v_mfma_f32_16x16x32_bf16 v[220:223], a[188:191], v[116:119], v[220:223]\n" \
"v_exp_f32_e32 v89, v89\n" \
"v_mfma_f32_16x16x32_bf16 v[220:223], a[172:175], v[112:115], v[220:223]\n" \
"v_exp_f32_e32 v90, v90\n" \
"v_mfma_f32_16x16x32_bf16 v[240:243], a[160:163], v[120:123], v[240:243]\n" \
"buffer_load_dwordx4 v8, s[80:83], s92 offen offset:0 lds\n" \
"v_mfma_f32_16x16x32_bf16 v[240:243], a[176:179], v[124:127], v[240:243]\n" \
"v_exp_f32_e32 v91, v91\n" \
"v_mfma_f32_16x16x32_bf16 v[244:247], a[180:183], v[124:127], v[244:247]\n" \
"v_cvt_pk_bf16_f32 v98, v68, v69\n" \
"v_cvt_pk_bf16_f32 v99, v70, v71\n" \
"v_mfma_f32_16x16x32_bf16 v[244:247], a[164:167], v[120:123], v[244:247]\n" \
"v_cvt_pk_bf16_f32 v104, v72, v73\n" \
"v_cvt_pk_bf16_f32 v105, v74, v75\n" \
"v_mfma_f32_16x16x32_bf16 v[248:251], a[168:171], v[120:123], v[248:251]\n" \
"v_cvt_pk_bf16_f32 v106, v76, v77\n" \
"v_cvt_pk_bf16_f32 v107, v78, v79\n" \
"v_mfma_f32_16x16x32_bf16 v[248:251], a[184:187], v[124:127], v[248:251]\n" \
"v_cvt_pk_bf16_f32 v112, v80, v81\n" \
"v_cvt_pk_bf16_f32 v113, v82, v83\n" \
"v_mfma_f32_16x16x32_bf16 v[252:255], a[188:191], v[124:127], v[252:255]\n" \
"v_cvt_pk_bf16_f32 v114, v84, v85\n" \
"v_cvt_pk_bf16_f32 v115, v86, v87\n" \
"v_mfma_f32_16x16x32_bf16 v[252:255], a[172:175], v[120:123], v[252:255]\n" \
"v_add_u32_e32 v48, s16, v20\n" \
"s_add_u32 s64, s64, 0x10000\n" \
"v_smfmac_f32_16x16x64_bf16 a[204:207], a[212:215], v[120:127], v21\n" \
"s_waitcnt vmcnt(17) lgkmcnt(0)\n" \
"s_barrier\n" \
"s_mov_b64 exec, 0\n" \
"s_cmp_lg_u32 s15, 0\n" \
"s_cbranch_scc1 L_pf22\n" \
"s_branch L_pf21\n" \
"L_retB:\n" \
"s_mov_b64 exec, -1\n" \
"s_branch L_2_%=\n" \
"L_t2_%=:\n" \
"v_mfma_f32_16x16x32_bf16 v[252:255], a[188:191], v[124:127], v[252:255]\n" \
"v_cvt_pk_bf16_f32 v114, v84, v85\n" \
"v_cvt_pk_bf16_f32 v115, v86, v87\n" \
"v_mfma_f32_16x16x32_bf16 v[252:255], a[172:175], v[120:123], v[252:255]\n" \
"v_xor_b32_e32 v48, 0x8000, v48\n" \
"v_smfmac_f32_16x16x64_bf16 a[204:207], a[212:215], v[120:127], v21\n" \
"s_waitcnt lgkmcnt(0)\n" \
"L_2_%=:\n" \
"v_mfma_f32_16x16x32_bf16 v[64:67], a[128:131], a[64:67], v[32:35]\n" \
"ds_read_b128 a[160:163], v49 offset:0\n" \
"ds_read_b128 a[176:179], v49 offset:4096\n" \
"v_mfma_f32_16x16x32_bf16 v[64:67], a[132:135], a[68:71], v[64:67]\n" \
"v_cvt_pk_bf16_f32 v120, v88, v89\n" \
"v_cvt_pk_bf16_f32 v121, v90, v91\n" \
"v_mfma_f32_16x16x32_bf16 v[64:67], a[136:139], a[72:75], v[64:67]\n" \
"ds_read_b128 a[180:183], v49 offset:5120\n" \
"ds_read_b128 a[164:167], v49 offset:1024\n" \
"v_mfma_f32_16x16x32_bf16 v[64:67], a[140:143], a[76:79], v[64:67]\n" \
"v_exp_f32_e32 v92, v92\n" \
"v_mfma_f32_16x16x32_bf16 v[68:71], a[156:159], a[76:79], v[32:35]\n" \
"ds_read_b128 a[168:171], v49 offset:2048\n" \
"ds_read_b128 a[184:187], v49 offset:6144\n" \
"v_mfma_f32_16x16x32_bf16 v[68:71], a[152:155], a[72:75], v[68:71]\n" \
"v_exp_f32_e32 v93, v93\n" \
"v_mfma_f32_16x16x32_bf16 v[68:71], a[148:151], a[68:71], v[68:71]\n" \
"ds_read_b128 a[188:191], v49 offset:7168\n" \
"ds_read_b128 a[172:175], v49 offset:3072\n" \
"v_mfma_f32_16x16x32_bf16 v[68:71], a[144:147], a[64:67], v[68:71]\n" \
"v_exp_f32_e32 v94, v94\n" \
"v_mfma_f32_16x16x32_bf16 v[72:75], a[128:131], a[80:83], v[36:39]\n" \
"v_exp_f32_e32 v95, v95\n" \
"v_mfma_f32_16x16x32_bf16 v[72:75], a[132:135], a[84:87], v[72:75]\n" \
"v_cvt_pk_bf16_f32 v122, v92, v93\n" \
"v_cvt_pk_bf16_f32 v123, v94, v95\n" \
"v_mfma_f32_16x16x32_bf16 v[72:75], a[136:139], a[88:91], v[72:75]\n" \
"v_exp_f32_e32 v64, v64\n" \
"v_mfma_f32_16x16x32_bf16 v[72:75], a[140:143], a[92:95], v[72:75]\n" \
"v_exp_f32_e32 v65, v65\n" \
"v_mfma_f32_16x16x32_bf16 v[76:79], a[156:159], a[92:95], v[36:39]\n" \
"v_exp_f32_e32 v66, v66\n" \
"v_mfma_f32_16x16x32_bf16 v[76:79], a[152:155], a[88:91], v[76:79]\n" \
"v_exp_f32_e32 v67, v67\n" \
"v_mfma_f32_16x16x32_bf16 v[76:79], a[148:151], a[84:87], v[76:79]\n" \
"v_exp_f32_e32 v68, v68\n" \
"v_mfma_f32_16x16x32_bf16 v[76:79], a[144:147], a[80:83], v[76:79]\n" \
"v_exp_f32_e32 v69, v69\n" \
"v_mfma_f32_16x16x32_bf16 v[80:83], a[128:131], a[96:99], v[40:43]\n" \
"v_exp_f32_e32 v70, v70\n" \
"v_mfma_f32_16x16x32_bf16 v[80:83], a[132:135], a[100:103], v[80:83]\n" \
"v_exp_f32_e32 v71, v71\n" \
"v_mfma_f32_16x16x32_bf16 v[80:83], a[136:139], a[104:107], v[80:83]\n" \
"v_cvt_pk_bf16_f32 v100, v64, v65\n" \
"v_cvt_pk_bf16_f32 v101, v66, v67\n" \
"v_mfma_f32_16x16x32_bf16 v[80:83], a[140:143], a[108:111], v[80:83]\n" \
"v_cvt_pk_bf16_f32 v102, v68, v69\n" \
"v_cvt_pk_bf16_f32 v103, v70, v71\n" \
"v_mfma_f32_16x16x32_bf16 v[84:87], a[156:159], a[108:111], v[40:43]\n" \
"v_exp_f32_e32 v72, v72\n" \
"v_mfma_f32_16x16x32_bf16 v[84:87], a[152:155], a[104:107], v[84:87]\n" \
"v_exp_f32_e32 v73, v73\n" \
"v_mfma_f32_16x16x32_bf16 v[84:87], a[148:151], a[100:103], v[84:87]\n" \
"v_exp_f32_e32 v74, v74\n" \
"v_mfma_f32_16x16x32_bf16 v[84:87], a[144:147], a[96:99], v[84:87]\n" \
"v_exp_f32_e32 v75, v75\n" \
"v_mfma_f32_16x16x32_bf16 v[88:91], a[128:131], a[112:115], v[44:47]\n" \
"v_exp_f32_e32 v76, v76\n" \
"v_mfma_f32_16x16x32_bf16 v[88:91], a[132:135], a[116:119], v[88:91]\n" \
"v_exp_f32_e32 v77, v77\n" \
"v_mfma_f32_16x16x32_bf16 v[88:91], a[136:139], a[120:123], v[88:91]\n" \
"v_exp_f32_e32 v78, v78\n" \
"v_mfma_f32_16x16x32_bf16 v[88:91], a[140:143], a[124:127], v[88:91]\n" \
"v_exp_f32_e32 v79, v79\n" \
"v_mfma_f32_16x16x32_bf16 v[92:95], a[156:159], a[124:127], v[44:47]\n" \
"v_cvt_pk_bf16_f32 v108, v72, v73\n" \
"v_cvt_pk_bf16_f32 v109, v74, v75\n" \
"v_mfma_f32_16x16x32_bf16 v[92:95], a[152:155], a[120:123], v[92:95]\n" \
"v_cvt_pk_bf16_f32 v110, v76, v77\n" \
"v_cvt_pk_bf16_f32 v111, v78, v79\n" \
"v_mfma_f32_16x16x32_bf16 v[92:95], a[148:151], a[116:119], v[92:95]\n" \
"ds_read_b128 a[128:131], v48 offset:0\n" \
"ds_read_b128 a[132:135], v48 offset:1024\n" \
"v_mfma_f32_16x16x32_bf16 v[92:95], a[144:147], a[112:115], v[92:95]\n" \
"s_add_u32 s63, s63, s63\n" \
"s_waitcnt lgkmcnt(2)\n" \
"v_mfma_f32_16x16x32_bf16 v[128:131], a[160:163], v[96:99], v[128:131]\n" \
"ds_read_b128 a[136:139], v48 offset:2048\n" \
"ds_read_b128 a[140:143], v48 offset:3072\n" \
"v_mfma_f32_16x16x32_bf16 v[128:131], a[176:179], v[100:103], v[128:131]\n" \
"v_exp_f32_e32 v80, v80\n" \
"v_mfma_f32_16x16x32_bf16 v[132:135], a[180:183], v[100:103], v[132:135]\n" \
"ds_read_b128 a[144:147], v48 offset:4096\n" \
"ds_read_b128 a[148:151], v48 offset:5120\n" \
"v_mfma_f32_16x16x32_bf16 v[132:135], a[164:167], v[96:99], v[132:135]\n" \
"v_exp_f32_e32 v81, v81\n" \
"v_mfma_f32_16x16x32_bf16 v[136:139], a[168:171], v[96:99], v[136:139]\n" \
"ds_read_b128 a[152:155], v48 offset:6144\n" \
"ds_read_b128 a[156:159], v48 offset:7168\n" \
"v_mfma_f32_16x16x32_bf16 v[136:139], a[184:187], v[100:103], v[136:139]\n" \
"v_exp_f32_e32 v82, v82\n" \
"v_mfma_f32_16x16x32_bf16 v[140:143], a[188:191], v[100:103], v[140:143]\n" \
"v_exp_f32_e32 v83, v83\n" \
"v_mfma_f32_16x16x32_bf16 v[140:143], a[172:175], v[96:99], v[140:143]\n" \
"v_exp_f32_e32 v84, v84\n" \
"v_smfmac_f32_16x16x64_bf16 a[192:195], a[212:215], v[96:103], v21\n" \
"v_mfma_f32_16x16x32_bf16 v[160:163], a[160:163], v[104:107], v[160:163]\n" \
"v_exp_f32_e32 v85, v85\n" \
"v_mfma_f32_16x16x32_bf16 v[160:163], a[176:179], v[108:111], v[160:163]\n" \
"v_exp_f32_e32 v86, v86\n" \
"v_mfma_f32_16x16x32_bf16 v[164:167], a[180:183], v[108:111], v[164:167]\n" \
"v_exp_f32_e32 v87, v87\n" \
"v_mfma_f32_16x16x32_bf16 v[164:167], a[164:167], v[104:107], v[164:167]\n" \
"v_cvt_pk_bf16_f32 v116, v80, v81\n" \
"v_cvt_pk_bf16_f32 v117, v82, v83\n" \
"v_mfma_f32_16x16x32_bf16 v[168:171], a[168:171], v[104:107], v[168:171]\n" \
"v_cvt_pk_bf16_f32 v118, v84, v85\n" \
"v_cvt_pk_bf16_f32 v119, v86, v87\n" \
"v_mfma_f32_16x16x32_bf16 v[168:171], a[184:187], v[108:111], v[168:171]\n" \
"v_exp_f32_e32 v88, v88\n" \
"v_mfma_f32_16x16x32_bf16 v[172:175], a[188:191], v[108:111], v[172:175]\n" \
"v_exp_f32_e32 v89, v89\n" \
"v_mfma_f32_16x16x32_bf16 v[172:175], a[172:175], v[104:107], v[172:175]\n" \
"v_exp_f32_e32 v90, v90\n" \
"v_smfmac_f32_16x16x64_bf16 a[196:199], a[212:215], v[104:111], v21\n" \
"v_mfma_f32_16x16x32_bf16 v[192:195], a[160:163], v[112:115], v[192:195]\n" \
"v_exp_f32_e32 v91, v91\n" \
"v_mfma_f32_16x16x32_bf16 v[192:195], a[176:179], v[116:119], v[192:195]\n" \
"v_exp_f32_e32 v92, v92\n" \
"v_mfma_f32_16x16x32_bf16 v[196:199], a[180:183], v[116:119], v[196:199]\n" \
"v_exp_f32_e32 v93, v93\n" \
"v_mfma_f32_16x16x32_bf16 v[196:199], a[164:167], v[112:115], v[196:199]\n" \
"v_exp_f32_e32 v94, v94\n" \
"v_mfma_f32_16x16x32_bf16 v[200:203], a[168:171], v[112:115], v[200:203]\n" \
"v_exp_f32_e32 v95, v95\n" \
"v_mfma_f32_16x16x32_bf16 v[200:203], a[184:187], v[116:119], v[200:203]\n" \
"v_cvt_pk_bf16_f32 v124, v88, v89\n" \
"v_cvt_pk_bf16_f32 v125, v90, v91\n" \
"v_mfma_f32_16x16x32_bf16 v[204:207], a[188:191], v[116:119], v[204:207]\n" \
"v_cvt_pk_bf16_f32 v126, v92, v93\n" \
"v_cvt_pk_bf16_f32 v127, v94, v95\n" \
"v_mfma_f32_16x16x32_bf16 v[204:207], a[172:175], v[112:115], v[204:207]\n" \
"v_smfmac_f32_16x16x64_bf16 a[200:203], a[212:215], v[112:119], v21\n" \
"s_waitcnt vmcnt(0) lgkmcnt(0)\n" \
"s_barrier\n" \
"v_mfma_f32_16x16x32_bf16 v[224:227], a[160:163], v[120:123], v[224:227]\n" \
"s_cbranch_scc1 L_h2_%=\n" \
"L_hr2_%=:\n" \
"v_mfma_f32_16x16x32_bf16 v[224:227], a[176:179], v[124:127], v[224:227]\n" \
"s_xor_b32 m0, s65, 0x12000\n" \
"ds_read_b128 a[160:163], v49 offset:8192\n" \
"v_mfma_f32_16x16x32_bf16 v[228:231], a[180:183], v[124:127], v[228:231]\n" \
"buffer_load_dwordx4 v50, s[8:11], s64 offen offset:0 sc1 lds\n" \
"v_mfma_f32_16x16x32_bf16 v[228:231], a[164:167], v[120:123], v[228:231]\n" \
"ds_read_b128 a[176:179], v49 offset:12288\n" \
"ds_read_b128 a[180:183], v49 offset:13312\n" \
"v_mfma_f32_16x16x32_bf16 v[232:235], a[168:171], v[120:123], v[232:235]\n" \
"v_mfma_f32_16x16x32_bf16 v[232:235], a[184:187], v[124:127], v[232:235]\n" \
"buffer_load_dwordx4 v50, s[8:11], s64 offen offset:1024 sc1 lds\n" \
"v_mfma_f32_16x16x32_bf16 v[236:239], a[188:191], v[124:127], v[236:239]\n" \
"ds_read_b128 a[164:167], v49 offset:9216\n" \
"ds_read_b128 a[168:171], v49 offset:10240\n" \
"v_mfma_f32_16x16x32_bf16 v[236:239], a[172:175], v[120:123], v[236:239]\n" \
"v_mfma_f32_16x16x32_bf16 v[64:67], a[128:131], a[64:67], v[32:35]\n" \
"ds_read_b128 a[184:187], v49 offset:14336\n" \
"ds_read_b128 a[188:191], v49 offset:15360\n" \
"v_mfma_f32_16x16x32_bf16 v[64:67], a[132:135], a[68:71], v[64:67]\n" \
"buffer_load_dwordx4 v50, s[8:11], s64 offen offset:2048 sc1 lds\n" \
"v_mfma_f32_16x16x32_bf16 v[64:67], a[136:139], a[72:75], v[64:67]\n" \
"ds_read_b128 a[172:175], v49 offset:11264\n" \
"v_add_u32_e32 v49, 0x10000, v48\n" \
"v_mfma_f32_16x16x32_bf16 v[64:67], a[140:143], a[76:79], v[64:67]\n" \
"v_mfma_f32_16x16x32_bf16 v[68:71], a[156:159], a[76:79], v[32:35]\n" \
"v_mfma_f32_16x16x32_bf16 v[68:71], a[152:155], a[72:75], v[68:71]\n" \
"v_mfma_f32_16x16x32_bf16 v[68:71], a[148:151], a[68:71], v[68:71]\n" \
"buffer_load_dwordx4 v50, s[8:11], s64 offen offset:3072 sc1 lds\n" \
"v_mfma_f32_16x16x32_bf16 v[68:71], a[144:147], a[64:67], v[68:71]\n" \
"s_xor_b32 m0, s65, 0xa000\n" \
"v_mfma_f32_16x16x32_bf16 v[72:75], a[128:131], a[80:83], v[36:39]\n" \
"v_mfma_f32_16x16x32_bf16 v[72:75], a[132:135], a[84:87], v[72:75]\n" \
"v_exp_f32_e32 v64, v64\n" \
"v_mfma_f32_16x16x32_bf16 v[72:75], a[136:139], a[88:91], v[72:75]\n" \
"buffer_load_dwordx4 v51, s[8:11], s64 offen offset:0 sc1 lds\n" \
"v_mfma_f32_16x16x32_bf16 v[72:75], a[140:143], a[92:95], v[72:75]\n" \
"v_exp_f32_e32 v65, v65\n" \
"v_mfma_f32_16x16x32_bf16 v[76:79], a[156:159], a[92:95], v[36:39]\n" \
"v_exp_f32_e32 v66, v66\n" \
"v_mfma_f32_16x16x32_bf16 v[76:79], a[152:155], a[88:91], v[76:79]\n" \
"v_exp_f32_e32 v67, v67\n" \
"v_mfma_f32_16x16x32_bf16 v[76:79], a[148:151], a[84:87], v[76:79]\n" \
"buffer_load_dwordx4 v51, s[8:11], s64 offen offset:1024 sc1 lds\n" \
"v_mfma_f32_16x16x32_bf16 v[76:79], a[144:147], a[80:83], v[76:79]\n" \
"v_exp_f32_e32 v68, v68\n" \
"v_mfma_f32_16x16x32_bf16 v[80:83], a[128:131], a[96:99], v[40:43]\n" \
"v_exp_f32_e32 v69, v69\n" \
"v_mfma_f32_16x16x32_bf16 v[80:83], a[132:135], a[100:103], v[80:83]\n" \
"v_exp_f32_e32 v70, v70\n" \
"v_mfma_f32_16x16x32_bf16 v[80:83], a[136:139], a[104:107], v[80:83]\n" \
"v_exp_f32_e32 v71, v71\n" \
"v_mfma_f32_16x16x32_bf16 v[80:83], a[140:143], a[108:111], v[80:83]\n" \
"buffer_load_dwordx4 v51, s[8:11], s64 offen offset:2048 sc1 lds\n" \
"v_mfma_f32_16x16x32_bf16 v[84:87], a[156:159], a[108:111], v[40:43]\n" \
"v_exp_f32_e32 v72, v72\n" \
"v_mfma_f32_16x16x32_bf16 v[84:87], a[152:155], a[104:107], v[84:87]\n" \
"v_exp_f32_e32 v73, v73\n" \
"v_mfma_f32_16x16x32_bf16 v[84:87], a[148:151], a[100:103], v[84:87]\n" \
"v_exp_f32_e32 v74, v74\n" \
"v_mfma_f32_16x16x32_bf16 v[84:87], a[144:147], a[96:99], v[84:87]\n" \
"buffer_load_dwordx4 v51, s[8:11], s64 offen offset:3072 sc1 lds\n" \
"v_mfma_f32_16x16x32_bf16 v[88:91], a[128:131], a[112:115], v[44:47]\n" \
"s_xor_b32 m0, s65, 0x18000\n" \
"ds_read_b128 a[128:131], v48 offset:8192\n" \
"v_mfma_f32_16x16x32_bf16 v[88:91], a[132:135], a[116:119], v[88:91]\n" \
"v_exp_f32_e32 v75, v75\n" \
"v_mfma_f32_16x16x32_bf16 v[88:91], a[136:139], a[120:123], v[88:91]\n" \
"ds_read_b128 a[132:135], v48 offset:9216\n" \
"ds_read_b128 a[136:139], v48 offset:10240\n" \
"v_mfma_f32_16x16x32_bf16 v[88:91], a[140:143], a[124:127], v[88:91]\n" \
"v_exp_f32_e32 v76, v76\n" \
"v_mfma_f32_16x16x32_bf16 v[92:95], a[156:159], a[124:127], v[44:47]\n" \
"buffer_load_dwordx4 v52, s[8:11], s64 offen offset:0 sc1 lds\n" \
"v_mfma_f32_16x16x32_bf16 v[92:95], a[152:155], a[120:123], v[92:95]\n" \
"ds_read_b128 a[140:143], v48 offset:11264\n" \
"ds_read_b128 a[152:155], v48 offset:14336\n" \
"v_mfma_f32_16x16x32_bf16 v[92:95], a[148:151], a[116:119], v[92:95]\n" \
"v_exp_f32_e32 v77, v77\n" \
"v_mfma_f32_16x16x32_bf16 v[92:95], a[144:147], a[112:115], v[92:95]\n" \
"ds_read_b128 a[144:147], v48 offset:12288\n" \
"s_waitcnt lgkmcnt(6)\n" \
"v_mfma_f32_16x16x32_bf16 v[144:147], a[160:163], v[96:99], v[144:147]\n" \
"buffer_load_dwordx4 v52, s[8:11], s64 offen offset:1024 sc1 lds\n" \
"v_mfma_f32_16x16x32_bf16 v[144:147], a[176:179], v[100:103], v[144:147]\n" \
"ds_read_b128 a[148:151], v48 offset:13312\n" \
"ds_read_b128 a[156:159], v48 offset:15360\n" \
"v_mfma_f32_16x16x32_bf16 v[148:151], a[180:183], v[100:103], v[148:151]\n" \
"v_exp_f32_e32 v78, v78\n" \
"v_mfma_f32_16x16x32_bf16 v[148:151], a[164:167], v[96:99], v[148:151]\n" \
"v_exp_f32_e32 v79, v79\n" \
"v_mfma_f32_16x16x32_bf16 v[152:155], a[168:171], v[96:99], v[152:155]\n" \
"buffer_load_dwordx4 v52, s[8:11], s64 offen offset:2048 sc1 lds\n" \
"v_mfma_f32_16x16x32_bf16 v[152:155], a[184:187], v[100:103], v[152:155]\n" \
"v_exp_f32_e32 v80, v80\n" \
"v_mfma_f32_16x16x32_bf16 v[156:159], a[188:191], v[100:103], v[156:159]\n" \
"v_exp_f32_e32 v81, v81\n" \
"v_mfma_f32_16x16x32_bf16 v[156:159], a[172:175], v[96:99], v[156:159]\n" \
"v_exp_f32_e32 v82, v82\n" \
"v_mfma_f32_16x16x32_bf16 v[176:179], a[160:163], v[104:107], v[176:179]\n" \
"v_exp_f32_e32 v83, v83\n" \
"v_mfma_f32_16x16x32_bf16 v[176:179], a[176:179], v[108:111], v[176:179]\n" \
"buffer_load_dwordx4 v52, s[8:11], s64 offen offset:3072 sc1 lds\n" \
"v_mfma_f32_16x16x32_bf16 v[180:183], a[180:183], v[108:111], v[180:183]\n" \
"s_mov_b32 m0, s65\n" \
"s_xor_b32 s65, s65, 0x8000\n" \
"v_mfma_f32_16x16x32_bf16 v[180:183], a[164:167], v[104:107], v[180:183]\n" \
"v_exp_f32_e32 v84, v84\n" \
"v_mfma_f32_16x16x32_bf16 v[184:187], a[168:171], v[104:107], v[184:187]\n" \
"v_exp_f32_e32 v85, v85\n" \
"v_mfma_f32_16x16x32_bf16 v[184:187], a[184:187], v[108:111], v[184:187]\n" \
"buffer_load_dwordx4 v53, s[8:11], s64 offen offset:0 sc1 lds\n" \
"v_mfma_f32_16x16x32_bf16 v[188:191], a[188:191], v[108:111], v[188:191]\n" \
"v_exp_f32_e32 v86, v86\n" \
"v_mfma_f32_16x16x32_bf16 v[188:191], a[172:175], v[104:107], v[188:191]\n" \
"v_exp_f32_e32 v87, v87\n" \
"v_mfma_f32_16x16x32_bf16 v[208:211], a[160:163], v[112:115], v[208:211]\n" \
"v_exp_f32_e32 v88, v88\n" \
"v_mfma_f32_16x16x32_bf16 v[208:211], a[176:179], v[116:119], v[208:211]\n" \
"buffer_load_dwordx4 v53, s[8:11], s64 offen offset:1024 sc1 lds\n" \
"v_mfma_f32_16x16x32_bf16 v[212:215], a[180:183], v[116:119], v[212:215]\n" \
"v_exp_f32_e32 v89, v89\n" \
"v_mfma_f32_16x16x32_bf16 v[212:215], a[164:167], v[112:115], v[212:215]\n" \
"v_exp_f32_e32 v90, v90\n" \
"v_mfma_f32_16x16x32_bf16 v[216:219], a[168:171], v[112:115], v[216:219]\n" \
"v_exp_f32_e32 v91, v91\n" \
"v_mfma_f32_16x16x32_bf16 v[216:219], a[184:187], v[116:119], v[216:219]\n" \
"v_cvt_pk_bf16_f32 v96, v64, v65\n" \
"v_cvt_pk_bf16_f32 v97, v66, v67\n" \
"v_mfma_f32_16x16x32_bf16 v[220:223], a[188:191], v[116:119], v[220:223]\n" \
"buffer_load_dwordx4 v53, s[8:11], s64 offen offset:2048 sc1 lds\n" \
"v_mfma_f32_16x16x32_bf16 v[220:223], a[172:175], v[112:115], v[220:223]\n" \
"v_cvt_pk_bf16_f32 v98, v68, v69\n" \
"v_cvt_pk_bf16_f32 v99, v70, v71\n" \
"v_mfma_f32_16x16x32_bf16 v[240:243], a[160:163], v[120:123], v[240:243]\n" \
"v_cvt_pk_bf16_f32 v104, v72, v73\n" \
"v_cvt_pk_bf16_f32 v105, v74, v75\n" \
"v_mfma_f32_16x16x32_bf16 v[240:243], a[176:179], v[124:127], v[240:243]\n" \
"v_cvt_pk_bf16_f32 v106, v76, v77\n" \
"v_cvt_pk_bf16_f32 v107, v78, v79\n" \
"v_mfma_f32_16x16x32_bf16 v[244:247], a[180:183], v[124:127], v[244:247]\n" \
"buffer_load_dwordx4 v53, s[8:11], s64 offen offset:3072 sc1 lds\n" \
"v_mfma_f32_16x16x32_bf16 v[244:247], a[164:167], v[120:123], v[244:247]\n" \
"s_add_u32 s64, s64, 0x10000\n" \
"s_sub_u32 s66, s66, 1\n" \
"v_mfma_f32_16x16x32_bf16 v[248:251], a[168:171], v[120:123], v[248:251]\n" \
"v_cvt_pk_bf16_f32 v112, v80, v81\n" \
"v_cvt_pk_bf16_f32 v113, v82, v83\n" \
"v_mfma_f32_16x16x32_bf16 v[248:251], a[184:187], v[124:127], v[248:251]\n" \
"s_cbranch_scc0 L_t2_%=\n" \
"v_mfma_f32_16x16x32_bf16 v[252:255], a[188:191], v[124:127], v[252:255]\n" \
"v_cvt_pk_bf16_f32 v114, v84, v85\n" \
"v_cvt_pk_bf16_f32 v115, v86, v87\n" \
"v_mfma_f32_16x16x32_bf16 v[252:255], a[172:175], v[120:123], v[252:255]\n" \
"v_xor_b32_e32 v48, 0x8000, v48\n" \
"v_smfmac_f32_16x16x64_bf16 a[204:207], a[212:215], v[120:127], v21\n" \
"s_waitcnt lgkmcnt(0)\n" \
"s_waitcnt vmcnt(12)\n" \
"s_barrier\n" \
"s_cmp_lg_u32 s16, 0\n" \
"s_cbranch_scc1 L_f1_%=\n" \
"ds_read_b128 a[160:163], v49 offset:0\n" \
"ds_read_b128 a[164:167], v49 offset:1024\n" \
"ds_read_b128 a[168:171], v49 offset:2048\n" \
"ds_read_b128 a[172:175], v49 offset:3072\n" \
"ds_read_b128 a[176:179], v49 offset:8192\n" \
"ds_read_b128 a[180:183], v49 offset:9216\n" \
"v_cvt_pk_bf16_f32 v120, v88, v89\n" \
"v_cvt_pk_bf16_f32 v121, v90, v91\n" \
"v_mfma_f32_16x16x32_bf16 v[88:91], a[128:131], a[112:115], v[44:47]\n" \
"s_add_u32 s70, s61, 0x0\n" \
"s_add_u32 s71, s61, 0x49000\n" \
"ds_read_b128 a[184:187], v49 offset:10240\n" \
"v_mfma_f32_16x16x32_bf16 v[88:91], a[132:135], a[116:119], v[88:91]\n" \
"ds_read_b128 a[188:191], v49 offset:11264\n" \
"v_add_u32_e32 v54, 0x49000, v20\n" \
"v_mfma_f32_16x16x32_bf16 v[88:91], a[136:139], a[120:123], v[88:91]\n" \
"v_exp_f32_e32 v92, v92\n" \
"v_mfma_f32_16x16x32_bf16 v[88:91], a[140:143], a[124:127], v[88:91]\n" \
"v_exp_f32_e32 v93, v93\n" \
"v_exp_f32_e32 v94, v94\n" \
"v_exp_f32_e32 v95, v95\n" \
"v_cvt_pk_bf16_f32 v122, v92, v93\n" \
"v_cvt_pk_bf16_f32 v123, v94, v95\n" \
"v_mfma_f32_16x16x32_bf16 v[92:95], a[156:159], a[124:127], v[44:47]\n" \
"buffer_load_dwordx4 a[220:223], v20, s[56:59], s60 offen offset:0 sc1\n" \
"v_mfma_f32_16x16x32_bf16 v[92:95], a[152:155], a[120:123], v[92:95]\n" \
"v_readlane_b32 s97, v11, 32\n" \
"v_readlane_b32 s98, v11, 33\n" \
"v_mfma_f32_16x16x32_bf16 v[92:95], a[148:151], a[116:119], v[92:95]\n" \
"v_and_b32_e32 v112, v12, v112\n" \
"v_and_b32_e32 v113, v13, v113\n" \
"v_mfma_f32_16x16x32_bf16 v[92:95], a[144:147], a[112:115], v[92:95]\n" \
"v_and_b32_e32 v114, v14, v114\n" \
"s_waitcnt lgkmcnt(7)\n" \
"v_mfma_f32_16x16x32_bf16 v[128:131], a[160:163], v[96:99], v[128:131]\n" \
"v_and_b32_e32 v115, v15, v115\n" \
"s_waitcnt lgkmcnt(6)\n" \
"v_mfma_f32_16x16x32_bf16 v[132:135], a[164:167], v[96:99], v[132:135]\n" \
"ds_read_b32 v60, v18 offset:0\n" \
"s_waitcnt lgkmcnt(6)\n" \
"v_mfma_f32_16x16x32_bf16 v[136:139], a[168:171], v[96:99], v[136:139]\n" \
"ds_read_b32 v61, v18 offset:128\n" \
"s_waitcnt lgkmcnt(6)\n" \
"v_mfma_f32_16x16x32_bf16 v[140:143], a[172:175], v[96:99], v[140:143]\n" \
"ds_read_b128 a[0:3], v49 offset:4096\n" \
"s_waitcnt lgkmcnt(5)\n" \
"v_mfma_f32_16x16x32_bf16 v[144:147], a[176:179], v[96:99], v[144:147]\n" \
"buffer_load_dwordx4 a[236:239], v54, s[56:59], s60 offen offset:0 sc1\n" \
"v_mfma_f32_16x16x32_bf16 v[148:151], a[180:183], v[96:99], v[148:151]\n" \
"ds_read_b128 a[4:7], v49 offset:5120\n" \
"s_waitcnt lgkmcnt(5)\n" \
"v_mfma_f32_16x16x32_bf16 v[152:155], a[184:187], v[96:99], v[152:155]\n" \
"ds_read_b128 a[8:11], v49 offset:6144\n" \
"s_waitcnt lgkmcnt(5)\n" \
"v_mfma_f32_16x16x32_bf16 v[156:159], a[188:191], v[96:99], v[156:159]\n" \
"buffer_load_dwordx4 a[224:227], v20, s[56:59], s60 offen offset:1024 sc1\n" \
"v_mfma_f32_16x16x32_bf16 a[192:195], a[252:255], v[96:99], a[192:195]\n" \
"ds_read_b128 a[12:15], v49 offset:7168\n" \
"ds_read_b128 a[16:19], v49 offset:12288\n" \
"v_mfma_f32_16x16x32_bf16 v[160:163], a[160:163], v[104:107], v[160:163]\n" \
"ds_read_b128 a[20:23], v49 offset:13312\n" \
"ds_read_b128 a[24:27], v49 offset:14336\n" \
"buffer_load_dwordx4 a[240:243], v54, s[56:59], s60 offen offset:1024 sc1\n" \
"v_mfma_f32_16x16x32_bf16 v[164:167], a[164:167], v[104:107], v[164:167]\n" \
"ds_read_b128 a[28:31], v49 offset:15360\n" \
"v_accvgpr_read_b32 v40, a192\n" \
"v_mfma_f32_16x16x32_bf16 v[168:171], a[168:171], v[104:107], v[168:171]\n" \
"v_accvgpr_read_b32 v23, a193\n" \
"v_add_f32_e32 v40, v40, v23\n" \
"v_mfma_f32_16x16x32_bf16 v[172:175], a[172:175], v[104:107], v[172:175]\n" \
"buffer_load_dwordx4 a[228:231], v20, s[56:59], s60 offen offset:2048 sc1\n" \
"v_mfma_f32_16x16x32_bf16 v[176:179], a[176:179], v[104:107], v[176:179]\n" \
"v_add_u32_e32 v50, s15, v48\n" \
"v_add_u32_e32 v51, 0x10000, v50\n" \
"v_mfma_f32_16x16x32_bf16 v[180:183], a[180:183], v[104:107], v[180:183]\n" \
"s_waitcnt vmcnt(5)\n" \
"ds_write_b128 v50, v[128:131] offset:0\n" \
"buffer_load_dwordx4 a[244:247], v54, s[56:59], s60 offen offset:2048 sc1\n" \
"v_mfma_f32_16x16x32_bf16 v[184:187], a[184:187], v[104:107], v[184:187]\n" \
"ds_write_b128 v50, v[132:135] offset:1024\n" \
"v_mfma_f32_16x16x32_bf16 v[188:191], a[188:191], v[104:107], v[188:191]\n" \
"ds_write_b128 v50, v[136:139] offset:2048\n" \
"buffer_load_dwordx4 a[232:235], v20, s[56:59], s60 offen offset:3072 sc1\n" \
"v_mfma_f32_16x16x32_bf16 a[196:199], a[252:255], v[104:107], a[196:199]\n" \
"ds_write_b128 v50, v[140:143] offset:3072\n" \
"v_mfma_f32_16x16x32_bf16 v[192:195], a[160:163], v[112:115], v[192:195]\n" \
"ds_write_b128 v50, v[144:147] offset:8192\n" \
"buffer_load_dwordx4 a[248:251], v54, s[56:59], s60 offen offset:3072 sc1\n" \
"v_mfma_f32_16x16x32_bf16 v[196:199], a[164:167], v[112:115], v[196:199]\n" \
"ds_write_b128 v50, v[148:151] offset:9216\n" \
"v_mfma_f32_16x16x32_bf16 v[200:203], a[168:171], v[112:115], v[200:203]\n" \
"ds_write_b128 v50, v[152:155] offset:10240\n" \
"buffer_load_dwordx4 a[32:35], v20, s[56:59], s97 offen offset:0 sc1\n" \
"v_mfma_f32_16x16x32_bf16 v[204:207], a[172:175], v[112:115], v[204:207]\n" \
"v_accvgpr_read_b32 v41, a196\n" \
"v_accvgpr_read_b32 v23, a197\n" \
"v_mfma_f32_16x16x32_bf16 v[208:211], a[176:179], v[112:115], v[208:211]\n" \
"v_add_f32_e32 v41, v41, v23\n" \
"v_xor_b32_e32 v52, 0x4000, v50\n" \
"v_mfma_f32_16x16x32_bf16 v[212:215], a[180:183], v[112:115], v[212:215]\n" \
"buffer_load_dwordx4 a[40:43], v54, s[56:59], s97 offen offset:0 sc1\n" \
"v_mfma_f32_16x16x32_bf16 v[216:219], a[184:187], v[112:115], v[216:219]\n" \
"ds_write_b128 v50, v[156:159] offset:11264\n" \
"v_mfma_f32_16x16x32_bf16 v[220:223], a[188:191], v[112:115], v[220:223]\n" \
"ds_write_b128 v51, v[160:163] offset:0\n" \
"buffer_load_dwordx4 a[48:51], v20, s[56:59], s98 offen offset:0 sc1\n" \
"v_mfma_f32_16x16x32_bf16 a[200:203], a[252:255], v[112:115], a[200:203]\n" \
"ds_write_b128 v51, v[164:167] offset:1024\n" \
"ds_write_b128 v51, v[168:171] offset:2048\n" \
"ds_write_b128 v51, v[172:175] offset:3072\n" \
"buffer_load_dwordx4 a[56:59], v54, s[56:59], s98 offen offset:0 sc1\n" \
"ds_write_b128 v51, v[176:179] offset:8192\n" \
"ds_write_b128 v51, v[180:183] offset:9216\n" \
"ds_write_b128 v51, v[184:187] offset:10240\n" \
"ds_write_b128 v51, v[188:191] offset:11264\n" \
"buffer_load_dwordx4 a[36:39], v20, s[56:59], s97 offen offset:1024 sc1\n" \
"ds_write_b64 v19, v[40:41]\n" \
"v_xor_b32_e32 v53, 0x4000, v51\n" \
"v_xor_b32_e32 v63, 0x400, v19\n" \
"v_readlane_b32 s93, v11, 18\n" \
"v_readlane_b32 s84, v11, 13\n" \
"v_readlane_b32 s85, v11, 14\n" \
"v_readlane_b32 s86, v11, 15\n" \
"s_mov_b32 s87, 0x20000\n" \
"v_readlane_b32 s92, v11, 16\n" \
"s_add_u32 m0, s93, 0x0\n" \
"v_exp_f32_e32 v88, v88\n" \
"v_exp_f32_e32 v89, v89\n" \
"v_exp_f32_e32 v90, v90\n" \
"v_exp_f32_e32 v91, v91\n" \
"v_exp_f32_e32 v92, v92\n" \
"v_exp_f32_e32 v93, v93\n" \
"v_exp_f32_e32 v94, v94\n" \
"v_exp_f32_e32 v95, v95\n" \
"v_cvt_pk_bf16_f32 v124, v88, v89\n" \
"v_cvt_pk_bf16_f32 v125, v90, v91\n" \
"v_cvt_pk_bf16_f32 v126, v92, v93\n" \
"v_cvt_pk_bf16_f32 v127, v94, v95\n" \
"v_and_b32_e32 v124, v12, v124\n" \
"v_and_b32_e32 v125, v13, v125\n" \
"v_and_b32_e32 v126, v14, v126\n" \
"v_and_b32_e32 v127, v15, v127\n" \
"v_accvgpr_read_b32 v56, a200\n" \
"v_accvgpr_read_b32 v23, a201\n" \
"v_add_f32_e32 v56, v56, v23\n" \
"s_waitcnt lgkmcnt(0)\n" \
"s_barrier\n" \
"buffer_load_dwordx4 a[44:47], v54, s[56:59], s97 offen offset:1024 sc1\n" \
"ds_read_b128 v[128:131], v52 offset:0\n" \
"L_pf13:\n" \
"s_cbranch_execz L_pf17\n" \
"s_nop 0\n" \
"ds_read_b128 v[132:135], v52 offset:1024\n" \
"ds_read_b128 v[136:139], v52 offset:2048\n" \
"ds_read_b128 v[140:143], v52 offset:3072\n" \
"ds_read_b128 v[144:147], v52 offset:8192\n" \
"ds_read_b128 v[148:151], v52 offset:9216\n" \
"ds_read_b128 v[152:155], v52 offset:10240\n" \
"ds_read_b128 v[156:159], v52 offset:11264\n" \
"buffer_load_dwordx4 a[52:55], v20, s[56:59], s98 offen offset:1024 sc1\n" \
"ds_read_b128 v[160:163], v53 offset:0\n" \
"ds_read_b128 v[164:167], v53 offset:1024\n" \
"ds_read_b128 v[168:171], v53 offset:2048\n" \
"ds_read_b128 v[172:175], v53 offset:3072\n" \
"ds_read_b128 v[176:179], v53 offset:8192\n" \
"ds_read_b128 v[180:183], v53 offset:9216\n" \
"ds_read_b128 v[184:187], v53 offset:10240\n" \
"ds_read_b128 v[188:191], v53 offset:11264\n" \
"buffer_load_dwordx4 a[60:63], v54, s[56:59], s98 offen offset:1024 sc1\n" \
"ds_read_b64 v[42:43], v63\n" \
"v_mfma_f32_16x16x32_bf16 v[224:227], a[160:163], v[120:123], v[224:227]\n" \
"v_mfma_f32_16x16x32_bf16 v[228:231], a[164:167], v[120:123], v[228:231]\n" \
"v_mfma_f32_16x16x32_bf16 v[232:235], a[168:171], v[120:123], v[232:235]\n" \
"buffer_load_dwordx4 v0, s[84:87], s92 offen offset:0 lds\n" \
"v_mfma_f32_16x16x32_bf16 v[236:239], a[172:175], v[120:123], v[236:239]\n" \
"v_mfma_f32_16x16x32_bf16 v[240:243], a[176:179], v[120:123], v[240:243]\n" \
"v_mfma_f32_16x16x32_bf16 v[244:247], a[180:183], v[120:123], v[244:247]\n" \
"buffer_load_dwordx4 v1, s[84:87], s92 offen offset:1024 lds\n" \
"v_mfma_f32_16x16x32_bf16 v[248:251], a[184:187], v[120:123], v[248:251]\n" \
"s_waitcnt lgkmcnt(15)\n" \
"v_add_f32_e32 v192, v192, v128\n" \
"v_mfma_f32_16x16x32_bf16 v[252:255], a[188:191], v[120:123], v[252:255]\n" \
"v_add_f32_e32 v193, v193, v129\n" \
"v_add_f32_e32 v194, v194, v130\n" \
"v_mfma_f32_16x16x32_bf16 a[204:207], a[252:255], v[120:123], a[204:207]\n" \
"buffer_load_dwordx4 v2, s[84:87], s92 offen offset:2048 lds\n" \
"v_add_f32_e32 v195, v195, v131\n" \
"s_waitcnt lgkmcnt(1)\n" \
"s_barrier\n" \
"v_mfma_f32_16x16x32_bf16 v[224:227], a[0:3], v[124:127], v[224:227]\n" \
"buffer_load_dwordx4 v3, s[84:87], s92 offen offset:3072 lds\n" \
"s_add_u32 m0, s93, 0x1000\n" \
"v_mfma_f32_16x16x32_bf16 v[228:231], a[4:7], v[124:127], v[228:231]\n" \
"s_waitcnt lgkmcnt(0)\n" \
"v_add_f32_e32 v56, v56, v42\n" \
"v_mfma_f32_16x16x32_bf16 v[232:235], a[8:11], v[124:127], v[232:235]\n" \
"v_max_f32_e32 v62, v62, v56\n" \
"v_add_f32_e32 v196, v196, v132\n" \
"v_mfma_f32_16x16x32_bf16 v[236:239], a[12:15], v[124:127], v[236:239]\n" \
"buffer_load_dwordx4 v4, s[84:87], s92 offen offset:0 lds\n" \
"v_rcp_f32_e32 v56, v56\n" \
"v_mfma_f32_16x16x32_bf16 v[240:243], a[16:19], v[124:127], v[240:243]\n" \
"v_add_f32_e32 v197, v197, v133\n" \
"v_add_f32_e32 v198, v198, v134\n" \
"v_add_f32_e32 v199, v199, v135\n" \
"v_mul_f32_e32 v56, v56, v60\n" \
"v_mul_f32_e32 v192, v56, v192\n" \
"v_mul_f32_e32 v193, v56, v193\n" \
"v_mul_f32_e32 v194, v56, v194\n" \
"v_mul_f32_e32 v195, v56, v195\n" \
"v_mul_f32_e32 v196, v56, v196\n" \
"v_mul_f32_e32 v197, v56, v197\n" \
"v_mul_f32_e32 v198, v56, v198\n" \
"v_mul_f32_e32 v199, v56, v199\n" \
"v_add_f32_e32 v200, v200, v136\n" \
"v_add_f32_e32 v201, v201, v137\n" \
"v_add_f32_e32 v202, v202, v138\n" \
"v_add_f32_e32 v203, v203, v139\n" \
"v_add_f32_e32 v204, v204, v140\n" \
"v_add_f32_e32 v205, v205, v141\n" \
"v_add_f32_e32 v206, v206, v142\n" \
"v_add_f32_e32 v207, v207, v143\n" \
"v_cvt_pk_bf16_f32 v64, v192, v193\n" \
"v_cvt_pk_bf16_f32 v65, v194, v195\n" \
"v_cvt_pk_bf16_f32 v66, v196, v197\n" \
"v_cvt_pk_bf16_f32 v67, v198, v199\n" \
"buffer_store_dwordx4 v[64:67], v20, s[56:59], s70 offen offset:0 sc1\n" \
"v_mfma_f32_16x16x32_bf16 v[244:247], a[20:23], v[124:127], v[244:247]\n" \
"v_mul_f32_e32 v200, v56, v200\n" \
"v_mul_f32_e32 v201, v56, v201\n" \
"v_mul_f32_e32 v202, v56, v202\n" \
"v_mul_f32_e32 v203, v56, v203\n" \
"v_mul_f32_e32 v204, v56, v204\n" \
"v_mul_f32_e32 v205, v56, v205\n" \
"v_mul_f32_e32 v206, v56, v206\n" \
"v_mul_f32_e32 v207, v56, v207\n" \
"v_add_f32_e32 v208, v208, v144\n" \
"v_add_f32_e32 v209, v209, v145\n" \
"v_add_f32_e32 v210, v210, v146\n" \
"v_add_f32_e32 v211, v211, v147\n" \
"v_add_f32_e32 v212, v212, v148\n" \
"v_add_f32_e32 v213, v213, v149\n" \
"v_add_f32_e32 v214, v214, v150\n" \
"v_add_f32_e32 v215, v215, v151\n" \
"v_cvt_pk_bf16_f32 v68, v200, v201\n" \
"v_cvt_pk_bf16_f32 v69, v202, v203\n" \
"v_cvt_pk_bf16_f32 v70, v204, v205\n" \
"v_cvt_pk_bf16_f32 v71, v206, v207\n" \
"buffer_store_dwordx4 v[68:71], v20, s[56:59], s70 offen offset:1024 sc1\n" \
"v_mul_f32_e32 v208, v56, v208\n" \
"v_mul_f32_e32 v209, v56, v209\n" \
"v_mul_f32_e32 v210, v56, v210\n" \
"v_mul_f32_e32 v211, v56, v211\n" \
"v_mul_f32_e32 v212, v56, v212\n" \
"v_mul_f32_e32 v213, v56, v213\n" \
"v_mul_f32_e32 v214, v56, v214\n" \
"v_mul_f32_e32 v215, v56, v215\n" \
"v_add_f32_e32 v216, v216, v152\n" \
"v_add_f32_e32 v217, v217, v153\n" \
"v_add_f32_e32 v218, v218, v154\n" \
"v_add_f32_e32 v219, v219, v155\n" \
"v_add_f32_e32 v220, v220, v156\n" \
"v_add_f32_e32 v221, v221, v157\n" \
"v_add_f32_e32 v222, v222, v158\n" \
"v_add_f32_e32 v223, v223, v159\n" \
"v_cvt_pk_bf16_f32 v72, v208, v209\n" \
"v_cvt_pk_bf16_f32 v73, v210, v211\n" \
"v_cvt_pk_bf16_f32 v74, v212, v213\n" \
"v_cvt_pk_bf16_f32 v75, v214, v215\n" \
"buffer_store_dwordx4 v[72:75], v20, s[56:59], s70 offen offset:2048 sc1\n" \
"v_mfma_f32_16x16x32_bf16 v[248:251], a[24:27], v[124:127], v[248:251]\n" \
"v_mul_f32_e32 v216, v56, v216\n" \
"v_mul_f32_e32 v217, v56, v217\n" \
"v_mul_f32_e32 v218, v56, v218\n" \
"v_mul_f32_e32 v219, v56, v219\n" \
"v_mul_f32_e32 v220, v56, v220\n" \
"v_mul_f32_e32 v221, v56, v221\n" \
"v_mul_f32_e32 v222, v56, v222\n" \
"v_mul_f32_e32 v223, v56, v223\n" \
"v_cvt_pk_bf16_f32 v76, v216, v217\n" \
"v_cvt_pk_bf16_f32 v77, v218, v219\n" \
"v_cvt_pk_bf16_f32 v78, v220, v221\n" \
"v_cvt_pk_bf16_f32 v79, v222, v223\n" \
"buffer_store_dwordx4 v[76:79], v20, s[56:59], s70 offen offset:3072 sc1\n" \
"v_add_f32_e32 v224, v224, v160\n" \
"v_add_f32_e32 v225, v225, v161\n" \
"v_add_f32_e32 v226, v226, v162\n" \
"v_add_f32_e32 v227, v227, v163\n" \
"v_add_f32_e32 v228, v228, v164\n" \
"v_add_f32_e32 v229, v229, v165\n" \
"v_add_f32_e32 v230, v230, v166\n" \
"v_add_f32_e32 v231, v231, v167\n" \
"v_add_f32_e32 v232, v232, v168\n" \
"v_add_f32_e32 v233, v233, v169\n" \
"v_add_f32_e32 v234, v234, v170\n" \
"v_add_f32_e32 v235, v235, v171\n" \
"v_add_f32_e32 v236, v236, v172\n" \
"v_add_f32_e32 v237, v237, v173\n" \
"v_add_f32_e32 v238, v238, v174\n" \
"v_add_f32_e32 v239, v239, v175\n" \
"v_mfma_f32_16x16x32_bf16 v[252:255], a[28:31], v[124:127], v[252:255]\n" \
"buffer_load_dwordx4 v5, s[84:87], s92 offen offset:1024 lds\n" \
"v_add_f32_e32 v240, v240, v176\n" \
"v_add_f32_e32 v241, v241, v177\n" \
"v_add_f32_e32 v242, v242, v178\n" \
"v_add_f32_e32 v243, v243, v179\n" \
"v_add_f32_e32 v244, v244, v180\n" \
"v_add_f32_e32 v245, v245, v181\n" \
"v_add_f32_e32 v246, v246, v182\n" \
"v_add_f32_e32 v247, v247, v183\n" \
"v_add_f32_e32 v248, v248, v184\n" \
"v_add_f32_e32 v249, v249, v185\n" \
"v_add_f32_e32 v250, v250, v186\n" \
"v_add_f32_e32 v251, v251, v187\n" \
"v_add_f32_e32 v252, v252, v188\n" \
"v_add_f32_e32 v253, v253, v189\n" \
"v_add_f32_e32 v254, v254, v190\n" \
"v_add_f32_e32 v255, v255, v191\n" \
"v_mfma_f32_16x16x32_bf16 a[204:207], a[252:255], v[124:127], a[204:207]\n" \
"buffer_load_dwordx4 v6, s[84:87], s92 offen offset:2048 lds\n" \
"v_mfma_f32_16x16x32_bf16 a[208:211], a[216:219], v[64:67], a[208:211]\n" \
"v_mfma_f32_16x16x32_bf16 a[208:211], a[216:219], v[68:71], a[208:211]\n" \
"v_mfma_f32_16x16x32_bf16 a[208:211], a[216:219], v[72:75], a[208:211]\n" \
"buffer_load_dwordx4 v7, s[84:87], s92 offen offset:3072 lds\n" \
"v_mfma_f32_16x16x32_bf16 a[208:211], a[216:219], v[76:79], a[208:211]\n" \
"s_add_u32 m0, s93, 0x2000\n" \
"s_nop 0\n" \
"buffer_load_dwordx4 v8, s[84:87], s92 offen offset:0 lds\n" \
"v_accvgpr_read_b32 v57, a204\n" \
"v_accvgpr_read_b32 v23, a205\n" \
"v_add_f32_e32 v57, v57, v23\n" \
"v_add_f32_e32 v57, v57, v43\n" \
"v_max_f32_e32 v62, v62, v57\n" \
"v_rcp_f32_e32 v57, v57\n" \
"v_cmp_lt_f32_e64 s[72:73], s100, v62\n" \
"v_mul_f32_e32 v57, v57, v61\n" \
"v_mul_f32_e32 v224, v57, v224\n" \
"v_mul_f32_e32 v225, v57, v225\n" \
"v_mul_f32_e32 v226, v57, v226\n" \
"v_mul_f32_e32 v227, v57, v227\n" \
"v_mul_f32_e32 v228, v57, v228\n" \
"v_mul_f32_e32 v229, v57, v229\n" \
"v_mul_f32_e32 v230, v57, v230\n" \
"v_mul_f32_e32 v231, v57, v231\n" \
"v_cvt_pk_bf16_f32 v80, v224, v225\n" \
"v_cvt_pk_bf16_f32 v81, v226, v227\n" \
"v_cvt_pk_bf16_f32 v82, v228, v229\n" \
"v_cvt_pk_bf16_f32 v83, v230, v231\n" \
"buffer_store_dwordx4 v[80:83], v20, s[56:59], s71 offen offset:0 sc1\n" \
"v_mul_f32_e32 v232, v57, v232\n" \
"v_mfma_f32_16x16x32_bf16 a[208:211], a[216:219], v[80:83], a[208:211]\n" \
"v_mul_f32_e32 v233, v57, v233\n" \
"v_mul_f32_e32 v234, v57, v234\n" \
"v_mul_f32_e32 v235, v57, v235\n" \
"v_mul_f32_e32 v236, v57, v236\n" \
"v_mul_f32_e32 v237, v57, v237\n" \
"v_mul_f32_e32 v238, v57, v238\n" \
"v_mul_f32_e32 v239, v57, v239\n" \
"v_cvt_pk_bf16_f32 v84, v232, v233\n" \
"v_cvt_pk_bf16_f32 v85, v234, v235\n" \
"v_cvt_pk_bf16_f32 v86, v236, v237\n" \
"v_cvt_pk_bf16_f32 v87, v238, v239\n" \
"buffer_store_dwordx4 v[84:87], v20, s[56:59], s71 offen offset:1024 sc1\n" \
"v_mul_f32_e32 v240, v57, v240\n" \
"v_mfma_f32_16x16x32_bf16 a[208:211], a[216:219], v[84:87], a[208:211]\n" \
"v_mul_f32_e32 v241, v57, v241\n" \
"v_mul_f32_e32 v242, v57, v242\n" \
"v_mul_f32_e32 v243, v57, v243\n" \
"v_mul_f32_e32 v244, v57, v244\n" \
"v_mul_f32_e32 v245, v57, v245\n" \
"v_mul_f32_e32 v246, v57, v246\n" \
"v_mul_f32_e32 v247, v57, v247\n" \
"v_cvt_pk_bf16_f32 v96, v240, v241\n" \
"v_cvt_pk_bf16_f32 v97, v242, v243\n" \
"v_cvt_pk_bf16_f32 v98, v244, v245\n" \
"v_cvt_pk_bf16_f32 v99, v246, v247\n" \
"buffer_store_dwordx4 v[96:99], v20, s[56:59], s71 offen offset:2048 sc1\n" \
"v_mul_f32_e32 v248, v57, v248\n" \
"v_mfma_f32_16x16x32_bf16 a[208:211], a[216:219], v[96:99], a[208:211]\n" \
"v_mul_f32_e32 v249, v57, v249\n" \
"v_mul_f32_e32 v250, v57, v250\n" \
"v_mul_f32_e32 v251, v57, v251\n" \
"v_mul_f32_e32 v252, v57, v252\n" \
"v_mul_f32_e32 v253, v57, v253\n" \
"v_mul_f32_e32 v254, v57, v254\n" \
"v_mul_f32_e32 v255, v57, v255\n" \
"v_cvt_pk_bf16_f32 v100, v248, v249\n" \
"v_cvt_pk_bf16_f32 v101, v250, v251\n" \
"v_cvt_pk_bf16_f32 v102, v252, v253\n" \
"v_cvt_pk_bf16_f32 v103, v254, v255\n" \
"buffer_store_dwordx4 v[100:103], v20, s[56:59], s71 offen offset:3072 sc1\n" \
"s_nop 0\n" \
"v_mfma_f32_16x16x32_bf16 a[208:211], a[216:219], v[100:103], a[208:211]\n" \
"s_nop 7\n" \
"v_accvgpr_read_b32 v63, a208\n" \
"v_cmp_u_f32_e32 vcc, v63, v63\n" \
"s_nop 0\n" \
"s_or_b64 vcc, s[72:73], vcc\n" \
"s_cbranch_scc0 L_out_%=\n" \
"s_branch L_end_%=\n" \
"L_h1_%=:\n" \
"L_pf14:\n" \
"s_cbranch_execz L_pf18\n" \
"s_nop 0\n" \
"s_cmp_gt_i32 s63, 0\n" \
"s_cbranch_scc1 L_hc1_%=\n" \
"s_cmp_lt_i32 s63, 0\n" \
"s_cbranch_scc1 L_hn1_%=\n" \
"s_mov_b32 exec_hi, 0xf\n" \
"buffer_load_dword v22, v16, s[80:83], 0 offen\n" \
"s_mov_b32 exec_hi, -1\n" \
"s_branch L_hr1_%=\n" \
"L_hn1_%=:\n" \
"buffer_load_dword v22, v17, s[56:59], 0 offen sc1\n" \
"s_branch L_hr1_%=\n" \
"L_hs1_%=:\n" \
"s_sleep 1\n" \
"buffer_load_dword v22, v17, s[56:59], 0 offen sc1\n" \
"s_add_u32 s101, s101, 1\n" \
"s_waitcnt vmcnt(0)\n" \
"s_cmp_gt_u32 s101, 0x100000\n" \
"s_cbranch_scc1 L_hr1_%=\n" \
"L_hc1_%=:\n" \
"v_cmp_ne_u32_e32 vcc, s62, v22\n" \
"s_nop 4\n" \
"s_cbranch_vccnz L_hs1_%=\n" \
"s_branch L_hr1_%=\n" \
"L_h2_%=:\n" \
"s_cmp_gt_i32 s63, 0\n" \
"s_cbranch_scc1 L_hc2_%=\n" \
"s_cmp_lt_i32 s63, 0\n" \
"s_cbranch_scc1 L_hn2_%=\n" \
"s_mov_b32 exec_hi, 0xf\n" \
"buffer_load_dword v22, v16, s[80:83], 0 offen\n" \
"s_mov_b32 exec_hi, -1\n" \
"s_branch L_hr2_%=\n" \
"L_hn2_%=:\n" \
"buffer_load_dword v22, v17, s[56:59], 0 offen sc1\n" \
"s_branch L_hr2_%=\n" \
"L_hs2_%=:\n" \
"s_sleep 1\n" \
"buffer_load_dword v22, v17, s[56:59], 0 offen sc1\n" \
"s_add_u32 s101, s101, 1\n" \
"s_waitcnt vmcnt(0)\n" \
"s_cmp_gt_u32 s101, 0x100000\n" \
"s_cbranch_scc1 L_hr2_%=\n" \
"L_hc2_%=:\n" \
"v_cmp_ne_u32_e32 vcc, s62, v22\n" \
"s_nop 4\n" \
"s_cbranch_vccnz L_hs2_%=\n" \
"s_branch L_hr2_%=\n" \
"L_sa_%=:\n" \
"s_sleep 1\n" \
"buffer_load_dword v37, v38, s[56:59], s64 offen sc1\n" \
"s_add_u32 s101, s101, 1\n" \
"s_waitcnt vmcnt(0)\n" \
"v_cmp_ne_u32_e32 vcc, s62, v37\n" \
"s_cmp_gt_u32 s101, 0x100000\n" \
"s_cbranch_scc1 L_ra_%=\n" \
"s_nop 2\n" \
"s_cbranch_vccnz L_sa_%=\n" \
"s_branch L_ra_%=\n" \
"L_sb_%=:\n" \
"s_sleep 1\n" \
"buffer_load_dword v47, v38, s[56:59], s71 offen sc1\n" \
"s_add_u32 s101, s101, 1\n" \
"s_waitcnt vmcnt(0)\n" \
"v_cmp_ne_u32_e32 vcc, s62, v47\n" \
"s_cmp_gt_u32 s101, 0x100000\n" \
"s_cbranch_scc1 L_rb_%=\n" \
"s_nop 2\n" \
"s_cbranch_vccnz L_sb_%=\n" \
"s_branch L_rb_%=\n" \
"L_sp0_%=:\n" \
"s_sleep 1\n" \
"L_sx0_%=:\n" \
"L_pf15:\n" \
"s_cbranch_execz L_pf19\n" \
"s_nop 0\n" \
"buffer_load_dword v37, v40, s[56:59], s74 offen sc1\n" \
"s_add_u32 s101, s101, 1\n" \
"s_waitcnt vmcnt(0)\n" \
"v_cmp_eq_u32_e32 vcc, s62, v37\n" \
"s_nop 1\n" \
"s_and_b32 s98, vcc_lo, 0xfef\n" \
"s_andn2_b32 s99, s100, vcc_lo\n" \
"s_cselect_b32 s99, 0, 16\n" \
"s_or_b32 s98, s98, s99\n" \
"s_andn2_b32 s98, s98, s84\n" \
"s_cbranch_scc0 L_sn0_%=\n" \
"s_or_b32 s84, s84, s98\n" \
"s_bitcmp1_b32 s98, 0\n" \
"s_cbranch_scc0 L_xs00_%=\n" \
"buffer_load_dwordx4 v[116:119], v20, s[56:59], s63 offen offset:0 sc1\n" \
"L_xs00_%=:\n" \
"s_bitcmp1_b32 s98, 1\n" \
"s_cbranch_scc0 L_xs01_%=\n" \
"buffer_load_dwordx4 v[124:127], v20, s[56:59], s63 offen offset:2048 sc1\n" \
"L_xs01_%=:\n" \
"s_bitcmp1_b32 s98, 2\n" \
"s_cbranch_scc0 L_xs02_%=\n" \
"buffer_load_dwordx4 v[132:135], v38, s[56:59], s63 offen offset:0 sc1\n" \
"L_xs02_%=:\n" \
"s_bitcmp1_b32 s98, 3\n" \
"s_cbranch_scc0 L_xs03_%=\n" \
"buffer_load_dwordx4 v[140:143], v38, s[56:59], s63 offen offset:2048 sc1\n" \
"L_xs03_%=:\n" \
"s_bitcmp1_b32 s98, 4\n" \
"s_cbranch_scc0 L_xs04_%=\n" \
"buffer_load_dwordx4 v[176:179], v20, s[56:59], s75 offen offset:0 sc1\n" \
"buffer_load_dwordx4 v[248:251], v20, s[56:59], s75 offen offset:2048 sc1\n" \
"buffer_load_dwordx4 v[180:183], v38, s[56:59], s75 offen offset:0 sc1\n" \
"buffer_load_dwordx4 v[252:255], v38, s[56:59], s75 offen offset:2048 sc1\n" \
"buffer_load_dwordx4 v[112:115], v47, s[56:59], s75 offen offset:0 sc1\n" \
"buffer_load_dwordx4 v[120:123], v47, s[56:59], s75 offen offset:2048 sc1\n" \
"buffer_load_dwordx4 v[128:131], v54, s[56:59], s75 offen offset:0 sc1\n" \
"buffer_load_dwordx4 v[136:139], v54, s[56:59], s75 offen offset:2048 sc1\n" \
"L_xs04_%=:\n" \
"s_bitcmp1_b32 s98, 5\n" \
"s_cbranch_scc0 L_xs05_%=\n" \
"buffer_load_dwordx4 v[148:151], v47, s[56:59], s63 offen offset:0 sc1\n" \
"buffer_load_dwordx4 v[220:223], v47, s[56:59], s63 offen offset:1024 sc1\n" \
"L_xs05_%=:\n" \
"s_bitcmp1_b32 s98, 6\n" \
"s_cbranch_scc0 L_xs06_%=\n" \
"buffer_load_dwordx4 v[156:159], v47, s[56:59], s63 offen offset:2048 sc1\n" \
"buffer_load_dwordx4 v[228:231], v47, s[56:59], s63 offen offset:3072 sc1\n" \
"L_xs06_%=:\n" \
"s_bitcmp1_b32 s98, 7\n" \
"s_cbranch_scc0 L_xs07_%=\n" \
"buffer_load_dwordx4 v[164:167], v54, s[56:59], s63 offen offset:0 sc1\n" \
"buffer_load_dwordx4 v[236:239], v54, s[56:59], s63 offen offset:1024 sc1\n" \
"L_xs07_%=:\n" \
"s_bitcmp1_b32 s98, 8\n" \
"s_cbranch_scc0 L_xs08_%=\n" \
"buffer_load_dwordx4 v[188:191], v20, s[56:59], s63 offen offset:1024 sc1\n" \
"L_xs08_%=:\n" \
"s_bitcmp1_b32 s98, 9\n" \
"s_cbranch_scc0 L_xs09_%=\n" \
"buffer_load_dwordx4 v[196:199], v20, s[56:59], s63 offen offset:3072 sc1\n" \
"L_xs09_%=:\n" \
"s_bitcmp1_b32 s98, 10\n" \
"s_cbranch_scc0 L_xs010_%=\n" \
"buffer_load_dwordx4 v[204:207], v38, s[56:59], s63 offen offset:1024 sc1\n" \
"L_xs010_%=:\n" \
"s_bitcmp1_b32 s98, 11\n" \
"s_cbranch_scc0 L_xs011_%=\n" \
"buffer_load_dwordx4 v[212:215], v38, s[56:59], s63 offen offset:3072 sc1\n" \
"L_xs011_%=:\n" \
"L_sn0_%=:\n" \
"s_cmp_gt_u32 s101, 0x100000\n" \
"s_cbranch_scc1 L_se0_%=\n" \
"s_andn2_b32 s99, 0xff, s84\n" \
"s_cbranch_scc1 L_sp0_%=\n" \
"L_se0_%=:\n" \
"s_waitcnt vmcnt(0)\n" \
"s_branch L_rx0_%=\n" \
"L_sp1_%=:\n" \
"s_sleep 1\n" \
"L_sx1_%=:\n" \
"buffer_load_dword v37, v40, s[56:59], s74 offen sc1\n" \
"s_add_u32 s101, s101, 1\n" \
"s_waitcnt vmcnt(0)\n" \
"v_cmp_eq_u32_e32 vcc, s62, v37\n" \
"s_nop 1\n" \
"s_and_b32 s98, vcc_lo, 0xf00\n" \
"s_andn2_b32 s98, s98, s84\n" \
"s_cbranch_scc0 L_sn1_%=\n" \
"s_or_b32 s84, s84, s98\n" \
"s_bitcmp1_b32 s98, 8\n" \
"s_cbranch_scc0 L_xs18_%=\n" \
"buffer_load_dwordx4 v[188:191], v20, s[56:59], s63 offen offset:1024 sc1\n" \
"L_xs18_%=:\n" \
"s_bitcmp1_b32 s98, 9\n" \
"s_cbranch_scc0 L_xs19_%=\n" \
"buffer_load_dwordx4 v[196:199], v20, s[56:59], s63 offen offset:3072 sc1\n" \
"L_xs19_%=:\n" \
"s_bitcmp1_b32 s98, 10\n" \
"s_cbranch_scc0 L_xs110_%=\n" \
"buffer_load_dwordx4 v[204:207], v38, s[56:59], s63 offen offset:1024 sc1\n" \
"L_xs110_%=:\n" \
"s_bitcmp1_b32 s98, 11\n" \
"s_cbranch_scc0 L_xs111_%=\n" \
"buffer_load_dwordx4 v[212:215], v38, s[56:59], s63 offen offset:3072 sc1\n" \
"L_xs111_%=:\n" \
"L_sn1_%=:\n" \
"s_cmp_gt_u32 s101, 0x100000\n" \
"s_cbranch_scc1 L_se1_%=\n" \
"s_andn2_b32 s99, 0xf00, s84\n" \
"s_cbranch_scc1 L_sp1_%=\n" \
"L_se1_%=:\n" \
"s_waitcnt vmcnt(0)\n" \
"s_branch L_rx1_%=\n" \
"L_att_stub:\n" \
"s_mov_b32 s101, 0\n" \
"v_add_u32_e32 v32, 0x49000, v20\n" \
"v_readlane_b32 s96, v11, 32\n" \
"v_readlane_b32 s97, v11, 33\n" \
"buffer_load_dwordx4 a[220:223], v20, s[56:59], s60 offen offset:0 sc1\n" \
"buffer_load_dwordx4 a[236:239], v32, s[56:59], s60 offen offset:0 sc1\n" \
"buffer_load_dwordx4 a[224:227], v20, s[56:59], s60 offen offset:1024 sc1\n" \
"buffer_load_dwordx4 a[240:243], v32, s[56:59], s60 offen offset:1024 sc1\n" \
"buffer_load_dwordx4 a[228:231], v20, s[56:59], s60 offen offset:2048 sc1\n" \
"buffer_load_dwordx4 a[244:247], v32, s[56:59], s60 offen offset:2048 sc1\n" \
"buffer_load_dwordx4 a[232:235], v20, s[56:59], s60 offen offset:3072 sc1\n" \
"buffer_load_dwordx4 a[248:251], v32, s[56:59], s60 offen offset:3072 sc1\n" \
"buffer_load_dwordx4 a[32:35], v20, s[56:59], s96 offen offset:0 sc1\n" \
"buffer_load_dwordx4 a[40:43], v32, s[56:59], s96 offen offset:0 sc1\n" \
"buffer_load_dwordx4 a[48:51], v20, s[56:59], s97 offen offset:0 sc1\n" \
"buffer_load_dwordx4 a[56:59], v32, s[56:59], s97 offen offset:0 sc1\n" \
"buffer_load_dwordx4 a[36:39], v20, s[56:59], s96 offen offset:1024 sc1\n" \
"buffer_load_dwordx4 a[44:47], v32, s[56:59], s96 offen offset:1024 sc1\n" \
"buffer_load_dwordx4 a[52:55], v20, s[56:59], s97 offen offset:1024 sc1\n" \
"buffer_load_dwordx4 a[60:63], v32, s[56:59], s97 offen offset:1024 sc1\n" \
"buffer_load_dwordx4 v[64:67], v20, s[56:59], s61 offen offset:0 sc1\n" \
"buffer_load_dwordx4 v[68:71], v20, s[56:59], s61 offen offset:1024 sc1\n" \
"buffer_load_dwordx4 v[72:75], v20, s[56:59], s61 offen offset:2048 sc1\n" \
"buffer_load_dwordx4 v[76:79], v20, s[56:59], s61 offen offset:3072 sc1\n" \
"buffer_load_dwordx4 v[80:83], v32, s[56:59], s61 offen offset:0 sc1\n" \
"buffer_load_dwordx4 v[84:87], v32, s[56:59], s61 offen offset:1024 sc1\n" \
"buffer_load_dwordx4 v[96:99], v32, s[56:59], s61 offen offset:2048 sc1\n" \
"buffer_load_dwordx4 v[100:103], v32, s[56:59], s61 offen offset:3072 sc1\n" \
"s_waitcnt vmcnt(0)\n" \
"s_branch L_out_%=\n" \
"L_sw_%=:\n" \
"L_pf16:\n" \
"s_cbranch_execz L_pf20\n" \
"s_nop 0\n" \
"v_mfma_f32_16x16x32_bf16 v[72:75], a[128:131], a[16:19], v[36:39]\n" \
"s_add_u32 s68, s60, 0xfff6e000\n" \
"s_add_u32 s69, s60, 0xfffb7000\n" \
"s_add_u32 s70, s60, 0x0\n" \
"s_add_u32 s71, s60, 0x49000\n" \
"v_mfma_f32_16x16x32_bf16 v[72:75], a[132:135], a[20:23], v[72:75]\n" \
"s_xor_b32 m0, s65, 0x12000\n" \
"ds_read_b128 a[160:163], v49 offset:0\n" \
"v_mfma_f32_16x16x32_bf16 v[72:75], a[136:139], a[24:27], v[72:75]\n" \
"buffer_load_dwordx4 v50, s[8:11], s64 offen offset:0 sc1 lds\n" \
"v_mfma_f32_16x16x32_bf16 v[72:75], a[140:143], a[28:31], v[72:75]\n" \
"ds_read_b128 a[164:167], v49 offset:1024\n" \
"ds_read_b128 a[168:171], v49 offset:2048\n" \
"v_mfma_f32_16x16x32_bf16 v[76:79], a[156:159], a[28:31], v[36:39]\n" \
"ds_read_b128 a[172:175], v49 offset:3072\n" \
"v_cvt_pk_bf16_f32 v120, v88, v89\n" \
"v_mfma_f32_16x16x32_bf16 v[76:79], a[152:155], a[24:27], v[76:79]\n" \
"buffer_load_dwordx4 v50, s[8:11], s64 offen offset:1024 sc1 lds\n" \
"v_mfma_f32_16x16x32_bf16 v[76:79], a[148:151], a[20:23], v[76:79]\n" \
"ds_read_b128 a[176:179], v49 offset:4096\n" \
"v_cvt_pk_bf16_f32 v121, v90, v91\n" \
"v_mfma_f32_16x16x32_bf16 v[76:79], a[144:147], a[16:19], v[76:79]\n" \
"ds_read_b128 a[180:183], v49 offset:5120\n" \
"s_mov_b32 s65, s13\n" \
"v_mfma_f32_16x16x32_bf16 v[80:83], a[128:131], a[32:35], v[40:43]\n" \
"v_add_u32_e32 v63, s15, v20\n" \
"s_add_u32 s67, s14, 40960\n" \
"v_mfma_f32_16x16x32_bf16 v[80:83], a[132:135], a[36:39], v[80:83]\n" \
"v_add_u32_e32 v50, 0x6000, v63\n" \
"s_xor_b32 m0, s65, 0x8000\n" \
"v_mfma_f32_16x16x32_bf16 v[80:83], a[136:139], a[40:43], v[80:83]\n" \
"v_exp_f32_e32 v92, v92\n" \
"v_mfma_f32_16x16x32_bf16 v[80:83], a[140:143], a[44:47], v[80:83]\n" \
"buffer_load_dwordx4 v50, s[8:11], s67 offen offset:0 sc1 lds\n" \
"v_mfma_f32_16x16x32_bf16 v[84:87], a[156:159], a[44:47], v[40:43]\n" \
"v_exp_f32_e32 v93, v93\n" \
"v_mfma_f32_16x16x32_bf16 v[84:87], a[152:155], a[40:43], v[84:87]\n" \
"v_exp_f32_e32 v94, v94\n" \
"v_mfma_f32_16x16x32_bf16 v[84:87], a[148:151], a[36:39], v[84:87]\n" \
"v_exp_f32_e32 v95, v95\n" \
"v_mfma_f32_16x16x32_bf16 v[84:87], a[144:147], a[32:35], v[84:87]\n" \
"v_cvt_pk_bf16_f32 v122, v92, v93\n" \
"v_cvt_pk_bf16_f32 v123, v94, v95\n" \
"v_mfma_f32_16x16x32_bf16 v[88:91], a[128:131], a[48:51], v[44:47]\n" \
"ds_read_b128 a[184:187], v49 offset:6144\n" \
"ds_read_b128 a[188:191], v49 offset:7168\n" \
"v_mfma_f32_16x16x32_bf16 v[88:91], a[132:135], a[52:55], v[88:91]\n" \
"v_and_b32_e32 v96, v12, v96\n" \
"v_and_b32_e32 v97, v13, v97\n" \
"v_mfma_f32_16x16x32_bf16 v[88:91], a[136:139], a[56:59], v[88:91]\n" \
"v_and_b32_e32 v98, v14, v98\n" \
"v_and_b32_e32 v99, v15, v99\n" \
"v_mfma_f32_16x16x32_bf16 v[88:91], a[140:143], a[60:63], v[88:91]\n" \
"buffer_load_dwordx4 v50, s[8:11], s67 offen offset:1024 sc1 lds\n" \
"v_mfma_f32_16x16x32_bf16 v[92:95], a[156:159], a[60:63], v[44:47]\n" \
"v_exp_f32_e32 v72, v72\n" \
"v_mfma_f32_16x16x32_bf16 v[92:95], a[152:155], a[56:59], v[92:95]\n" \
"v_exp_f32_e32 v73, v73\n" \
"v_mfma_f32_16x16x32_bf16 v[92:95], a[148:151], a[52:55], v[92:95]\n" \
"v_exp_f32_e32 v74, v74\n" \
"v_mfma_f32_16x16x32_bf16 v[92:95], a[144:147], a[48:51], v[92:95]\n" \
"v_cvt_pk_bf16_f32 v108, v72, v73\n" \
"s_waitcnt lgkmcnt(2)\n" \
"v_mfma_f32_16x16x32_bf16 v[128:131], a[160:163], v[96:99], v[128:131]\n" \
"v_exp_f32_e32 v75, v75\n" \
"v_mfma_f32_16x16x32_bf16 v[132:135], a[164:167], v[96:99], v[132:135]\n" \
"v_exp_f32_e32 v76, v76\n" \
"v_mfma_f32_16x16x32_bf16 v[136:139], a[168:171], v[96:99], v[136:139]\n" \
"v_exp_f32_e32 v77, v77\n" \
"v_mfma_f32_16x16x32_bf16 v[140:143], a[172:175], v[96:99], v[140:143]\n" \
"v_exp_f32_e32 v78, v78\n" \
"v_mfma_f32_16x16x32_bf16 a[192:195], a[252:255], v[96:99], a[192:195]\n" \
"v_exp_f32_e32 v79, v79\n" \
"v_mfma_f32_16x16x32_bf16 v[160:163], a[160:163], v[104:107], v[160:163]\n" \
"v_cvt_pk_bf16_f32 v109, v74, v75\n" \
"v_cvt_pk_bf16_f32 v110, v76, v77\n" \
"v_cvt_pk_bf16_f32 v111, v78, v79\n" \
"v_and_b32_e32 v108, v12, v108\n" \
"v_and_b32_e32 v109, v13, v109\n" \
"v_and_b32_e32 v110, v14, v110\n" \
"v_and_b32_e32 v111, v15, v111\n" \
"v_exp_f32_e32 v80, v80\n" \
"v_exp_f32_e32 v81, v81\n" \
"v_mfma_f32_16x16x32_bf16 v[160:163], a[176:179], v[108:111], v[160:163]\n" \
"buffer_load_dwordx4 v50, s[8:11], s67 offen offset:2048 sc1 lds\n" \
"v_mfma_f32_16x16x32_bf16 v[164:167], a[180:183], v[108:111], v[164:167]\n" \
"buffer_load_dwordx4 v50, s[8:11], s67 offen offset:3072 sc1 lds\n" \
"v_mfma_f32_16x16x32_bf16 v[164:167], a[164:167], v[104:107], v[164:167]\n" \
"v_exp_f32_e32 v82, v82\n" \
"v_mfma_f32_16x16x32_bf16 v[168:171], a[168:171], v[104:107], v[168:171]\n" \
"v_cvt_pk_bf16_f32 v116, v80, v81\n" \
"s_waitcnt lgkmcnt(0)\n" \
"v_mfma_f32_16x16x32_bf16 v[168:171], a[184:187], v[108:111], v[168:171]\n" \
"v_exp_f32_e32 v83, v83\n" \
"v_mfma_f32_16x16x32_bf16 v[172:175], a[188:191], v[108:111], v[172:175]\n" \
"v_exp_f32_e32 v84, v84\n" \
"v_mfma_f32_16x16x32_bf16 v[172:175], a[172:175], v[104:107], v[172:175]\n" \
"v_exp_f32_e32 v85, v85\n" \
"v_smfmac_f32_16x16x64_bf16 a[196:199], a[212:215], v[104:111], v21\n" \
"v_mfma_f32_16x16x32_bf16 v[192:195], a[160:163], v[112:115], v[192:195]\n" \
"v_exp_f32_e32 v86, v86\n" \
"v_exp_f32_e32 v87, v87\n" \
"v_cvt_pk_bf16_f32 v117, v82, v83\n" \
"v_cvt_pk_bf16_f32 v118, v84, v85\n" \
"v_cvt_pk_bf16_f32 v119, v86, v87\n" \
"s_mov_b32 s64, s14\n" \
"v_add_u32_e32 v51, 0x12000, v63\n" \
"v_mfma_f32_16x16x32_bf16 v[192:195], a[176:179], v[116:119], v[192:195]\n" \
"s_xor_b32 m0, s65, 0xa000\n" \
"ds_read_b32 v58, v18 offset:512\n" \
"v_mfma_f32_16x16x32_bf16 v[196:199], a[180:183], v[116:119], v[196:199]\n" \
"buffer_load_dwordx4 v51, s[8:11], s64 offen offset:0 sc1 lds\n" \
"v_mfma_f32_16x16x32_bf16 v[196:199], a[164:167], v[112:115], v[196:199]\n" \
"v_exp_f32_e32 v88, v88\n" \
"v_mfma_f32_16x16x32_bf16 v[200:203], a[168:171], v[112:115], v[200:203]\n" \
"v_exp_f32_e32 v89, v89\n" \
"v_mfma_f32_16x16x32_bf16 v[200:203], a[184:187], v[116:119], v[200:203]\n" \
"v_exp_f32_e32 v90, v90\n" \
"v_mfma_f32_16x16x32_bf16 v[204:207], a[188:191], v[116:119], v[204:207]\n" \
"v_exp_f32_e32 v91, v91\n" \
"v_mfma_f32_16x16x32_bf16 v[204:207], a[172:175], v[112:115], v[204:207]\n" \
"v_exp_f32_e32 v92, v92\n" \
"v_smfmac_f32_16x16x64_bf16 a[200:203], a[212:215], v[112:119], v21\n" \
"v_mfma_f32_16x16x32_bf16 v[224:227], a[160:163], v[120:123], v[224:227]\n" \
"v_exp_f32_e32 v93, v93\n" \
"v_exp_f32_e32 v94, v94\n" \
"v_exp_f32_e32 v95, v95\n" \
"v_cvt_pk_bf16_f32 v124, v88, v89\n" \
"v_cvt_pk_bf16_f32 v125, v90, v91\n" \
"v_cvt_pk_bf16_f32 v126, v92, v93\n" \
"v_cvt_pk_bf16_f32 v127, v94, v95\n" \
"v_add_u32_e32 v52, 0x14000, v63\n" \
"v_add_u32_e32 v53, 0x20000, v63\n" \
"v_mfma_f32_16x16x32_bf16 v[224:227], a[176:179], v[124:127], v[224:227]\n" \
"buffer_load_dwordx4 v51, s[8:11], s64 offen offset:1024 sc1 lds\n" \
"v_mfma_f32_16x16x32_bf16 v[228:231], a[180:183], v[124:127], v[228:231]\n" \
"ds_read_b32 v59, v18 offset:640\n" \
"ds_read_b32 v60, v18 offset:768\n" \
"v_mfma_f32_16x16x32_bf16 v[228:231], a[164:167], v[120:123], v[228:231]\n" \
"ds_read_b32 v61, v18 offset:896\n" \
"ds_read_b128 a[128:131], v48 offset:0\n" \
"v_mfma_f32_16x16x32_bf16 v[232:235], a[168:171], v[120:123], v[232:235]\n" \
"ds_read_b128 a[132:135], v48 offset:1024\n" \
"ds_read_b128 a[136:139], v48 offset:2048\n" \
"v_mfma_f32_16x16x32_bf16 v[232:235], a[184:187], v[124:127], v[232:235]\n" \
"ds_read_b128 a[140:143], v48 offset:3072\n" \
"ds_read_b128 a[144:147], v48 offset:4096\n" \
"ds_read_b128 a[148:151], v48 offset:5120\n" \
"ds_read_b128 a[152:155], v48 offset:6144\n" \
"ds_read_b128 a[156:159], v48 offset:7168\n" \
"v_mfma_f32_16x16x32_bf16 v[236:239], a[188:191], v[124:127], v[236:239]\n" \
"buffer_load_dwordx4 v51, s[8:11], s64 offen offset:2048 sc1 lds\n" \
"v_mfma_f32_16x16x32_bf16 v[236:239], a[172:175], v[120:123], v[236:239]\n" \
"buffer_load_dwordx4 v51, s[8:11], s64 offen offset:3072 sc1 lds\n" \
"v_readlane_b32 s93, v11, 17\n" \
"v_readlane_b32 s92, v11, 22\n" \
"v_accvgpr_read_b32 v54, a192\n" \
"v_accvgpr_read_b32 v23, a193\n" \
"v_add_f32_e32 v54, v54, v23\n" \
"v_accvgpr_read_b32 v55, a196\n" \
"v_accvgpr_read_b32 v23, a197\n" \
"v_max_f32_e32 v62, v62, v54\n" \
"v_add_f32_e32 v55, v55, v23\n" \
"v_max_f32_e32 v62, v62, v55\n" \
"v_rcp_f32_e32 v54, v54\n" \
"v_rcp_f32_e32 v55, v55\n" \
"s_waitcnt lgkmcnt(11)\n" \
"v_mul_f32_e32 v54, v54, v58\n" \
"s_waitcnt lgkmcnt(10)\n" \
"v_mul_f32_e32 v55, v55, v59\n" \
"v_mul_f32_e32 v128, v54, v128\n" \
"v_mul_f32_e32 v129, v54, v129\n" \
"v_mul_f32_e32 v136, v54, v136\n" \
"v_mul_f32_e32 v137, v54, v137\n" \
"v_mul_f32_e32 v160, v55, v160\n" \
"v_mul_f32_e32 v161, v55, v161\n" \
"v_mul_f32_e32 v168, v55, v168\n" \
"v_mul_f32_e32 v169, v55, v169\n" \
"v_cvt_pk_bf16_f32 v128, v128, v129\n" \
"v_mul_f32_e32 v130, v54, v130\n" \
"v_mul_f32_e32 v131, v54, v131\n" \
"v_cvt_pk_bf16_f32 v136, v136, v137\n" \
"v_mul_f32_e32 v138, v54, v138\n" \
"v_mul_f32_e32 v139, v54, v139\n" \
"s_waitcnt vmcnt(10) lgkmcnt(0)\n" \
"s_barrier\n" \
"v_mfma_f32_16x16x32_bf16 v[80:83], a[128:131], a[32:35], v[40:43]\n" \
"ds_read_b128 a[160:163], v49 offset:8192\n" \
"ds_read_b128 a[164:167], v49 offset:9216\n" \
"v_mfma_f32_16x16x32_bf16 v[80:83], a[132:135], a[36:39], v[80:83]\n" \
"ds_read_b128 a[168:171], v49 offset:10240\n" \
"ds_read_b128 a[172:175], v49 offset:11264\n" \
"v_mfma_f32_16x16x32_bf16 v[80:83], a[136:139], a[40:43], v[80:83]\n" \
"s_xor_b32 m0, s65, 0x18000\n" \
"ds_read_b128 a[176:179], v49 offset:12288\n" \
"v_mfma_f32_16x16x32_bf16 v[80:83], a[140:143], a[44:47], v[80:83]\n" \
"buffer_load_dwordx4 v52, s[8:11], s64 offen offset:0 sc1 lds\n" \
"v_mfma_f32_16x16x32_bf16 v[84:87], a[156:159], a[44:47], v[40:43]\n" \
"ds_read_b128 a[180:183], v49 offset:13312\n" \
"ds_read_b128 a[184:187], v49 offset:14336\n" \
"v_mfma_f32_16x16x32_bf16 v[84:87], a[152:155], a[40:43], v[84:87]\n" \
"buffer_load_dwordx4 v52, s[8:11], s64 offen offset:1024 sc1 lds\n" \
"v_mfma_f32_16x16x32_bf16 v[84:87], a[148:151], a[36:39], v[84:87]\n" \
"ds_read_b128 a[188:191], v49 offset:15360\n" \
"v_add_u32_e32 v49, 0x10000, v48\n" \
"v_mfma_f32_16x16x32_bf16 v[84:87], a[144:147], a[32:35], v[84:87]\n" \
"buffer_load_dwordx4 v52, s[8:11], s64 offen offset:2048 sc1 lds\n" \
"v_mfma_f32_16x16x32_bf16 v[88:91], a[128:131], a[48:51], v[44:47]\n" \
"ds_read_b128 a[128:131], v48 offset:8192\n" \
"v_cvt_pk_bf16_f32 v160, v160, v161\n" \
"v_mfma_f32_16x16x32_bf16 v[88:91], a[132:135], a[52:55], v[88:91]\n" \
"ds_read_b128 a[132:135], v48 offset:9216\n" \
"v_mul_f32_e32 v162, v55, v162\n" \
"v_mfma_f32_16x16x32_bf16 v[88:91], a[136:139], a[56:59], v[88:91]\n" \
"buffer_load_dwordx4 v52, s[8:11], s64 offen offset:3072 sc1 lds\n" \
"v_mfma_f32_16x16x32_bf16 v[88:91], a[140:143], a[60:63], v[88:91]\n" \
"s_mov_b32 m0, s65\n" \
"ds_read_b128 a[136:139], v48 offset:10240\n" \
"v_mfma_f32_16x16x32_bf16 v[92:95], a[156:159], a[60:63], v[44:47]\n" \
"ds_read_b128 a[140:143], v48 offset:11264\n" \
"ds_read_b128 a[156:159], v48 offset:15360\n" \
"v_mfma_f32_16x16x32_bf16 v[92:95], a[152:155], a[56:59], v[92:95]\n" \
"buffer_load_dwordx4 v53, s[8:11], s64 offen offset:0 sc1 lds\n" \
"v_mfma_f32_16x16x32_bf16 v[92:95], a[148:151], a[52:55], v[92:95]\n" \
"ds_read_b128 a[148:151], v48 offset:13312\n" \
"ds_read_b128 a[152:155], v48 offset:14336\n" \
"v_mfma_f32_16x16x32_bf16 v[92:95], a[144:147], a[48:51], v[92:95]\n" \
"ds_read_b128 a[144:147], v48 offset:12288\n" \
"s_waitcnt lgkmcnt(12)\n" \
"v_mfma_f32_16x16x32_bf16 v[144:147], a[160:163], v[96:99], v[144:147]\n" \
"buffer_load_dwordx4 v53, s[8:11], s64 offen offset:1024 sc1 lds\n" \
"v_mfma_f32_16x16x32_bf16 v[148:151], a[164:167], v[96:99], v[148:151]\n" \
"v_exp_f32_e32 v88, v88\n" \
"v_mfma_f32_16x16x32_bf16 v[152:155], a[168:171], v[96:99], v[152:155]\n" \
"v_exp_f32_e32 v89, v89\n" \
"v_mfma_f32_16x16x32_bf16 v[156:159], a[172:175], v[96:99], v[156:159]\n" \
"v_exp_f32_e32 v90, v90\n" \
"v_mfma_f32_16x16x32_bf16 v[176:179], a[160:163], v[104:107], v[176:179]\n" \
"s_xor_b32 s65, s65, 0x8000\n" \
"s_waitcnt lgkmcnt(8)\n" \
"v_mfma_f32_16x16x32_bf16 v[176:179], a[176:179], v[108:111], v[176:179]\n" \
"buffer_load_dwordx4 v53, s[8:11], s64 offen offset:2048 sc1 lds\n" \
"v_mfma_f32_16x16x32_bf16 v[180:183], a[180:183], v[108:111], v[180:183]\n" \
"L_pf17:\n" \
"s_cbranch_execz L_retA\n" \
"s_nop 0\n" \
"v_exp_f32_e32 v91, v91\n" \
"v_mfma_f32_16x16x32_bf16 v[180:183], a[164:167], v[104:107], v[180:183]\n" \
"buffer_load_dwordx4 v53, s[8:11], s64 offen offset:3072 sc1 lds\n" \
"v_mfma_f32_16x16x32_bf16 v[184:187], a[168:171], v[104:107], v[184:187]\n" \
"s_add_u32 m0, s93, 0x0\n" \
"s_add_u32 s64, s64, 0x10000\n" \
"v_mfma_f32_16x16x32_bf16 v[184:187], a[184:187], v[108:111], v[184:187]\n" \
"v_exp_f32_e32 v92, v92\n" \
"v_mfma_f32_16x16x32_bf16 v[188:191], a[188:191], v[108:111], v[188:191]\n" \
"buffer_load_dwordx4 v0, s[80:83], s92 offen offset:0 lds\n" \
"v_mfma_f32_16x16x32_bf16 v[188:191], a[172:175], v[104:107], v[188:191]\n" \
"v_exp_f32_e32 v93, v93\n" \
"v_mfma_f32_16x16x32_bf16 v[208:211], a[160:163], v[112:115], v[208:211]\n" \
"v_exp_f32_e32 v94, v94\n" \
"v_mfma_f32_16x16x32_bf16 v[208:211], a[176:179], v[116:119], v[208:211]\n" \
"v_exp_f32_e32 v95, v95\n" \
"v_mfma_f32_16x16x32_bf16 v[212:215], a[180:183], v[116:119], v[212:215]\n" \
"buffer_load_dwordx4 v1, s[80:83], s92 offen offset:1024 lds\n" \
"v_mfma_f32_16x16x32_bf16 v[212:215], a[164:167], v[112:115], v[212:215]\n" \
"v_exp_f32_e32 v80, v80\n" \
"v_mfma_f32_16x16x32_bf16 v[216:219], a[168:171], v[112:115], v[216:219]\n" \
"v_exp_f32_e32 v81, v81\n" \
"v_mfma_f32_16x16x32_bf16 v[216:219], a[184:187], v[116:119], v[216:219]\n" \
"buffer_load_dwordx4 v2, s[80:83], s92 offen offset:2048 lds\n" \
"v_mfma_f32_16x16x32_bf16 v[220:223], a[188:191], v[116:119], v[220:223]\n" \
"v_exp_f32_e32 v82, v82\n" \
"v_mfma_f32_16x16x32_bf16 v[220:223], a[172:175], v[112:115], v[220:223]\n" \
"v_exp_f32_e32 v83, v83\n" \
"v_mfma_f32_16x16x32_bf16 v[240:243], a[160:163], v[120:123], v[240:243]\n" \
"buffer_load_dwordx4 v3, s[80:83], s92 offen offset:3072 lds\n" \
"v_mfma_f32_16x16x32_bf16 v[240:243], a[176:179], v[124:127], v[240:243]\n" \
"v_exp_f32_e32 v84, v84\n" \
"v_mfma_f32_16x16x32_bf16 v[244:247], a[180:183], v[124:127], v[244:247]\n" \
"v_exp_f32_e32 v85, v85\n" \
"v_mfma_f32_16x16x32_bf16 v[244:247], a[164:167], v[120:123], v[244:247]\n" \
"v_exp_f32_e32 v86, v86\n" \
"v_mfma_f32_16x16x32_bf16 v[248:251], a[168:171], v[120:123], v[248:251]\n" \
"v_exp_f32_e32 v87, v87\n" \
"v_mfma_f32_16x16x32_bf16 v[248:251], a[184:187], v[124:127], v[248:251]\n" \
"v_cvt_pk_bf16_f32 v112, v80, v81\n" \
"v_cvt_pk_bf16_f32 v113, v82, v83\n" \
"v_mfma_f32_16x16x32_bf16 v[252:255], a[188:191], v[124:127], v[252:255]\n" \
"v_cvt_pk_bf16_f32 v114, v84, v85\n" \
"v_cvt_pk_bf16_f32 v115, v86, v87\n" \
"v_mfma_f32_16x16x32_bf16 v[252:255], a[172:175], v[120:123], v[252:255]\n" \
"v_and_b32_e32 v112, v12, v112\n" \
"v_and_b32_e32 v113, v13, v113\n" \
"v_smfmac_f32_16x16x64_bf16 a[204:207], a[212:215], v[120:127], v21\n" \
"v_cvt_pk_bf16_f32 v120, v88, v89\n" \
"v_cvt_pk_bf16_f32 v121, v90, v91\n" \
"v_cvt_pk_bf16_f32 v122, v92, v93\n" \
"v_cvt_pk_bf16_f32 v123, v94, v95\n" \
"v_and_b32_e32 v114, v14, v114\n" \
"v_and_b32_e32 v115, v15, v115\n" \
"v_add_u32_e32 v48, s16, v20\n" \
"v_mul_f32_e32 v144, v54, v144\n" \
"v_mul_f32_e32 v145, v54, v145\n" \
"v_mul_f32_e32 v152, v54, v152\n" \
"v_mul_f32_e32 v153, v54, v153\n" \
"v_mul_f32_e32 v176, v55, v176\n" \
"v_mul_f32_e32 v177, v55, v177\n" \
"v_mul_f32_e32 v184, v55, v184\n" \
"v_mul_f32_e32 v185, v55, v185\n" \
"v_mul_f32_e32 v163, v55, v163\n" \
"v_cvt_pk_bf16_f32 v168, v168, v169\n" \
"v_mul_f32_e32 v170, v55, v170\n" \
"v_mul_f32_e32 v171, v55, v171\n" \
"v_cvt_pk_bf16_f32 v144, v144, v145\n" \
"v_mul_f32_e32 v146, v54, v146\n" \
"v_mul_f32_e32 v147, v54, v147\n" \
"v_cvt_pk_bf16_f32 v152, v152, v153\n" \
"v_mul_f32_e32 v154, v54, v154\n" \
"v_mul_f32_e32 v155, v54, v155\n" \
"v_cvt_pk_bf16_f32 v176, v176, v177\n" \
"v_mul_f32_e32 v178, v55, v178\n" \
"v_mul_f32_e32 v179, v55, v179\n" \
"v_cvt_pk_bf16_f32 v184, v184, v185\n" \
"v_mul_f32_e32 v186, v55, v186\n" \
"v_mul_f32_e32 v187, v55, v187\n" \
"v_cvt_pk_bf16_f32 v129, v130, v131\n" \
"v_mul_f32_e32 v132, v54, v132\n" \
"v_mul_f32_e32 v133, v54, v133\n" \
"v_mul_f32_e32 v134, v54, v134\n" \
"v_mul_f32_e32 v135, v54, v135\n" \
"v_cvt_pk_bf16_f32 v137, v138, v139\n" \
"v_mul_f32_e32 v140, v54, v140\n" \
"v_mul_f32_e32 v141, v54, v141\n" \
"v_mul_f32_e32 v142, v54, v142\n" \
"v_mul_f32_e32 v143, v54, v143\n" \
"v_cvt_pk_bf16_f32 v161, v162, v163\n" \
"v_mul_f32_e32 v164, v55, v164\n" \
"v_mul_f32_e32 v165, v55, v165\n" \
"v_mul_f32_e32 v166, v55, v166\n" \
"v_mul_f32_e32 v167, v55, v167\n" \
"v_cvt_pk_bf16_f32 v169, v170, v171\n" \
"v_mul_f32_e32 v172, v55, v172\n" \
"v_mul_f32_e32 v173, v55, v173\n" \
"v_mul_f32_e32 v174, v55, v174\n" \
"v_mul_f32_e32 v175, v55, v175\n" \
"v_cvt_pk_bf16_f32 v145, v146, v147\n" \
"v_mul_f32_e32 v148, v54, v148\n" \
"v_mul_f32_e32 v149, v54, v149\n" \
"v_mul_f32_e32 v150, v54, v150\n" \
"v_mul_f32_e32 v151, v54, v151\n" \
"v_cvt_pk_bf16_f32 v153, v154, v155\n" \
"v_mul_f32_e32 v156, v54, v156\n" \
"v_mul_f32_e32 v157, v54, v157\n" \
"v_mul_f32_e32 v158, v54, v158\n" \
"v_mul_f32_e32 v159, v54, v159\n" \
"v_cvt_pk_bf16_f32 v177, v178, v179\n" \
"v_mul_f32_e32 v180, v55, v180\n" \
"v_mul_f32_e32 v181, v55, v181\n" \
"v_mul_f32_e32 v182, v55, v182\n" \
"v_mul_f32_e32 v183, v55, v183\n" \
"v_cvt_pk_bf16_f32 v185, v186, v187\n" \
"v_mul_f32_e32 v188, v55, v188\n" \
"v_mul_f32_e32 v189, v55, v189\n" \
"v_mul_f32_e32 v190, v55, v190\n" \
"v_mul_f32_e32 v191, v55, v191\n" \
"v_cvt_pk_bf16_f32 v130, v132, v133\n" \
"v_cvt_pk_bf16_f32 v131, v134, v135\n" \
"buffer_store_dwordx4 v[128:131], v20, s[56:59], s68 offen offset:0 sc1\n" \
"v_cvt_pk_bf16_f32 v138, v140, v141\n" \
"v_mfma_f32_16x16x32_bf16 a[208:211], a[216:219], v[128:131], a[208:211]\n" \
"v_cvt_pk_bf16_f32 v139, v142, v143\n" \
"buffer_store_dwordx4 v[136:139], v20, s[56:59], s68 offen offset:1024 sc1\n" \
"s_waitcnt vmcnt(14) lgkmcnt(0)\n" \
"s_barrier\n" \
"v_mfma_f32_16x16x32_bf16 v[88:91], a[128:131], a[48:51], v[44:47]\n" \
"s_xor_b32 m0, s65, 0x12000\n" \
"ds_read_b128 a[160:163], v49 offset:0\n" \
"v_mfma_f32_16x16x32_bf16 v[88:91], a[132:135], a[52:55], v[88:91]\n" \
"buffer_load_dwordx4 v50, s[8:11], s64 offen offset:0 sc1 lds\n" \
"v_mfma_f32_16x16x32_bf16 v[88:91], a[136:139], a[56:59], v[88:91]\n" \
"ds_read_b128 a[164:167], v49 offset:1024\n" \
"ds_read_b128 a[168:171], v49 offset:2048\n" \
"v_mfma_f32_16x16x32_bf16 v[88:91], a[140:143], a[60:63], v[88:91]\n" \
"buffer_load_dwordx4 v50, s[8:11], s64 offen offset:1024 sc1 lds\n" \
"v_mfma_f32_16x16x32_bf16 v[92:95], a[156:159], a[60:63], v[44:47]\n" \
"ds_read_b128 a[172:175], v49 offset:3072\n" \
"ds_read_b128 a[176:179], v49 offset:8192\n" \
"v_mfma_f32_16x16x32_bf16 v[92:95], a[152:155], a[56:59], v[92:95]\n" \
"ds_read_b128 a[180:183], v49 offset:9216\n" \
"ds_read_b128 a[184:187], v49 offset:10240\n" \
"v_mfma_f32_16x16x32_bf16 v[92:95], a[148:151], a[52:55], v[92:95]\n" \
"ds_read_b128 a[188:191], v49 offset:11264\n" \
"ds_read_b128 a[0:3], v49 offset:4096\n" \
"v_mfma_f32_16x16x32_bf16 v[92:95], a[144:147], a[48:51], v[92:95]\n" \
"buffer_load_dwordx4 v50, s[8:11], s64 offen offset:2048 sc1 lds\n" \
"v_mfma_f32_16x16x32_bf16 v[32:35], a[220:223], a[64:67], 0\n" \
"ds_read_b128 a[4:7], v49 offset:5120\n" \
"ds_read_b128 a[8:11], v49 offset:6144\n" \
"v_mfma_f32_16x16x32_bf16 v[36:39], a[220:223], a[80:83], 0\n" \
"ds_read_b128 a[12:15], v49 offset:7168\n" \
"ds_read_b128 a[16:19], v49 offset:12288\n" \
"v_mfma_f32_16x16x32_bf16 v[40:43], a[220:223], a[96:99], 0\n" \
"ds_read_b128 a[20:23], v49 offset:13312\n" \
"ds_read_b128 a[24:27], v49 offset:14336\n" \
"v_mfma_f32_16x16x32_bf16 v[44:47], a[220:223], a[112:115], 0\n" \
"buffer_load_dwordx4 v50, s[8:11], s64 offen offset:3072 sc1 lds\n" \
"v_mfma_f32_16x16x32_bf16 v[32:35], a[224:227], a[68:71], v[32:35]\n" \
"s_xor_b32 m0, s65, 0xa000\n" \
"ds_read_b128 a[28:31], v49 offset:15360\n" \
"v_mfma_f32_16x16x32_bf16 v[36:39], a[224:227], a[84:87], v[36:39]\n" \
"v_exp_f32_e32 v88, v88\n" \
"v_mfma_f32_16x16x32_bf16 v[40:43], a[224:227], a[100:103], v[40:43]\n" \
"buffer_load_dwordx4 v51, s[8:11], s64 offen offset:0 sc1 lds\n" \
"v_mfma_f32_16x16x32_bf16 v[44:47], a[224:227], a[116:119], v[44:47]\n" \
"v_exp_f32_e32 v89, v89\n" \
"v_mfma_f32_16x16x32_bf16 v[32:35], a[228:231], a[72:75], v[32:35]\n" \
"v_exp_f32_e32 v90, v90\n" \
"v_mfma_f32_16x16x32_bf16 v[36:39], a[228:231], a[88:91], v[36:39]\n" \
"v_exp_f32_e32 v91, v91\n" \
"v_mfma_f32_16x16x32_bf16 v[40:43], a[228:231], a[104:107], v[40:43]\n" \
"buffer_load_dwordx4 v51, s[8:11], s64 offen offset:1024 sc1 lds\n" \
"v_mfma_f32_16x16x32_bf16 v[44:47], a[228:231], a[120:123], v[44:47]\n" \
"v_exp_f32_e32 v92, v92\n" \
"v_mfma_f32_16x16x32_bf16 v[32:35], a[232:235], a[76:79], v[32:35]\n" \
"v_exp_f32_e32 v93, v93\n" \
"v_mfma_f32_16x16x32_bf16 v[36:39], a[232:235], a[92:95], v[36:39]\n" \
"v_exp_f32_e32 v94, v94\n" \
"v_mfma_f32_16x16x32_bf16 v[40:43], a[232:235], a[108:111], v[40:43]\n" \
"buffer_load_dwordx4 v51, s[8:11], s64 offen offset:2048 sc1 lds\n" \
"v_mfma_f32_16x16x32_bf16 v[44:47], a[232:235], a[124:127], v[44:47]\n" \
"v_cvt_pk_bf16_f32 v124, v88, v89\n" \
"s_waitcnt lgkmcnt(3)\n" \
"v_mfma_f32_16x16x32_bf16 v[192:195], a[160:163], v[112:115], v[192:195]\n" \
"v_exp_f32_e32 v95, v95\n" \
"v_mfma_f32_16x16x32_bf16 v[196:199], a[164:167], v[112:115], v[196:199]\n" \
"buffer_load_dwordx4 v51, s[8:11], s64 offen offset:3072 sc1 lds\n" \
"v_mfma_f32_16x16x32_bf16 v[200:203], a[168:171], v[112:115], v[200:203]\n" \
"s_add_u32 m0, s93, 0x1000\n" \
"v_cvt_pk_bf16_f32 v125, v90, v91\n" \
"v_mfma_f32_16x16x32_bf16 v[204:207], a[172:175], v[112:115], v[204:207]\n" \
"v_cvt_pk_bf16_f32 v126, v92, v93\n" \
"v_cvt_pk_bf16_f32 v127, v94, v95\n" \
"v_mfma_f32_16x16x32_bf16 v[208:211], a[176:179], v[112:115], v[208:211]\n" \
"v_and_b32_e32 v124, v12, v124\n" \
"v_and_b32_e32 v125, v13, v125\n" \
"v_mfma_f32_16x16x32_bf16 v[212:215], a[180:183], v[112:115], v[212:215]\n" \
"buffer_load_dwordx4 v4, s[80:83], s92 offen offset:0 lds\n" \
"v_mfma_f32_16x16x32_bf16 v[216:219], a[184:187], v[112:115], v[216:219]\n" \
"v_and_b32_e32 v126, v14, v126\n" \
"v_and_b32_e32 v127, v15, v127\n" \
"v_mfma_f32_16x16x32_bf16 v[220:223], a[188:191], v[112:115], v[220:223]\n" \
"ds_read_b128 a[128:131], v48 offset:0\n" \
"ds_read_b128 a[132:135], v48 offset:1024\n" \
"v_mfma_f32_16x16x32_bf16 a[200:203], a[252:255], v[112:115], a[200:203]\n" \
"ds_read_b128 a[136:139], v48 offset:2048\n" \
"ds_read_b128 a[140:143], v48 offset:3072\n" \
"v_mfma_f32_16x16x32_bf16 v[224:227], a[160:163], v[120:123], v[224:227]\n" \
"buffer_load_dwordx4 v5, s[80:83], s92 offen offset:1024 lds\n" \
"v_mfma_f32_16x16x32_bf16 v[228:231], a[164:167], v[120:123], v[228:231]\n" \
"ds_read_b128 a[144:147], v48 offset:4096\n" \
"ds_read_b128 a[148:151], v48 offset:5120\n" \
"v_mfma_f32_16x16x32_bf16 v[232:235], a[168:171], v[120:123], v[232:235]\n" \
"ds_read_b128 a[152:155], v48 offset:6144\n" \
"ds_read_b128 a[156:159], v48 offset:7168\n" \
"v_mfma_f32_16x16x32_bf16 v[236:239], a[172:175], v[120:123], v[236:239]\n" \
"buffer_load_dwordx4 v6, s[80:83], s92 offen offset:2048 lds\n" \
"v_mfma_f32_16x16x32_bf16 v[240:243], a[176:179], v[120:123], v[240:243]\n" \
"v_cvt_pk_bf16_f32 v162, v164, v165\n" \
"v_cvt_pk_bf16_f32 v163, v166, v167\n" \
"buffer_store_dwordx4 v[160:163], v20, s[56:59], s69 offen offset:0 sc1\n" \
"v_mfma_f32_16x16x32_bf16 v[244:247], a[180:183], v[120:123], v[244:247]\n" \
"v_cvt_pk_bf16_f32 v170, v172, v173\n" \
"v_cvt_pk_bf16_f32 v171, v174, v175\n" \
"buffer_store_dwordx4 v[168:171], v20, s[56:59], s69 offen offset:1024 sc1\n" \
"v_mfma_f32_16x16x32_bf16 v[248:251], a[184:187], v[120:123], v[248:251]\n" \
"v_cvt_pk_bf16_f32 v146, v148, v149\n" \
"v_cvt_pk_bf16_f32 v147, v150, v151\n" \
"buffer_store_dwordx4 v[144:147], v20, s[56:59], s68 offen offset:2048 sc1\n" \
"v_mfma_f32_16x16x32_bf16 v[252:255], a[188:191], v[120:123], v[252:255]\n" \
"buffer_load_dwordx4 v7, s[80:83], s92 offen offset:3072 lds\n" \
"v_mfma_f32_16x16x32_bf16 a[204:207], a[252:255], v[120:123], a[204:207]\n" \
"v_cvt_pk_bf16_f32 v154, v156, v157\n" \
"v_cvt_pk_bf16_f32 v155, v158, v159\n" \
"buffer_store_dwordx4 v[152:155], v20, s[56:59], s68 offen offset:3072 sc1\n" \
"v_mfma_f32_16x16x32_bf16 v[224:227], a[0:3], v[124:127], v[224:227]\n" \
"v_cvt_pk_bf16_f32 v178, v180, v181\n" \
"v_cvt_pk_bf16_f32 v179, v182, v183\n" \
"buffer_store_dwordx4 v[176:179], v20, s[56:59], s69 offen offset:2048 sc1\n" \
"v_mfma_f32_16x16x32_bf16 v[228:231], a[4:7], v[124:127], v[228:231]\n" \
"v_cvt_pk_bf16_f32 v186, v188, v189\n" \
"v_cvt_pk_bf16_f32 v187, v190, v191\n" \
"buffer_store_dwordx4 v[184:187], v20, s[56:59], s69 offen offset:3072 sc1\n" \
"v_mfma_f32_16x16x32_bf16 v[232:235], a[8:11], v[124:127], v[232:235]\n" \
"s_add_u32 m0, s93, 0x2000\n" \
"v_accvgpr_read_b32 v56, a200\n" \
"v_mfma_f32_16x16x32_bf16 v[236:239], a[12:15], v[124:127], v[236:239]\n" \
"buffer_load_dwordx4 v8, s[80:83], s92 offen offset:0 lds\n" \
"v_mfma_f32_16x16x32_bf16 v[240:243], a[16:19], v[124:127], v[240:243]\n" \
"v_accvgpr_read_b32 v23, a201\n" \
"s_waitcnt lgkmcnt(0)\n" \
"v_mfma_f32_16x16x32_bf16 v[244:247], a[20:23], v[124:127], v[244:247]\n" \
"v_add_f32_e32 v56, v56, v23\n" \
"v_max_f32_e32 v62, v62, v56\n" \
"v_mfma_f32_16x16x32_bf16 v[248:251], a[24:27], v[124:127], v[248:251]\n" \
"v_rcp_f32_e32 v56, v56\n" \
"v_mfma_f32_16x16x32_bf16 v[252:255], a[28:31], v[124:127], v[252:255]\n" \
"v_mul_f32_e32 v56, v56, v60\n" \
"v_mul_f32_e32 v192, v56, v192\n" \
"v_mfma_f32_16x16x32_bf16 a[204:207], a[252:255], v[124:127], a[204:207]\n" \
"v_mul_f32_e32 v193, v56, v193\n" \
"v_cvt_pk_bf16_f32 v192, v192, v193\n" \
"v_mfma_f32_16x16x32_bf16 a[208:211], a[216:219], v[136:139], a[208:211]\n" \
"v_mul_f32_e32 v194, v56, v194\n" \
"v_mul_f32_e32 v195, v56, v195\n" \
"v_mfma_f32_16x16x32_bf16 a[208:211], a[216:219], v[160:163], a[208:211]\n" \
"v_cvt_pk_bf16_f32 v193, v194, v195\n" \
"v_mul_f32_e32 v196, v56, v196\n" \
"v_mfma_f32_16x16x32_bf16 a[208:211], a[216:219], v[168:171], a[208:211]\n" \
"v_mul_f32_e32 v197, v56, v197\n" \
"v_mul_f32_e32 v198, v56, v198\n" \
"v_mfma_f32_16x16x32_bf16 a[208:211], a[216:219], v[144:147], a[208:211]\n" \
"v_mul_f32_e32 v199, v56, v199\n" \
"v_mul_f32_e32 v200, v56, v200\n" \
"v_mfma_f32_16x16x32_bf16 a[208:211], a[216:219], v[152:155], a[208:211]\n" \
"v_mul_f32_e32 v201, v56, v201\n" \
"v_accvgpr_read_b32 v57, a204\n" \
"v_mfma_f32_16x16x32_bf16 a[208:211], a[216:219], v[176:179], a[208:211]\n" \
"v_accvgpr_read_b32 v23, a205\n" \
"v_cvt_pk_bf16_f32 v194, v196, v197\n" \
"v_mfma_f32_16x16x32_bf16 a[208:211], a[216:219], v[184:187], a[208:211]\n" \
"v_cvt_pk_bf16_f32 v195, v198, v199\n" \
"buffer_store_dwordx4 v[192:195], v20, s[56:59], s70 offen offset:0 sc1\n" \
"v_mfma_f32_32x32x16_bf16 v[128:143], a[216:219], a[216:219], 0\n" \
"v_cvt_pk_bf16_f32 v200, v200, v201\n" \
"v_mul_f32_e32 v202, v56, v202\n" \
"v_mul_f32_e32 v203, v56, v203\n" \
"v_add_f32_e32 v57, v57, v23\n" \
"v_cvt_pk_bf16_f32 v201, v202, v203\n" \
"v_mul_f32_e32 v204, v56, v204\n" \
"v_mfma_f32_16x16x32_bf16 a[208:211], a[216:219], v[192:195], a[208:211]\n" \
"v_mul_f32_e32 v205, v56, v205\n" \
"v_mul_f32_e32 v206, v56, v206\n" \
"v_mfma_f32_32x32x16_bf16 v[144:159], a[216:219], a[216:219], 0\n" \
"v_mul_f32_e32 v207, v56, v207\n" \
"v_mul_f32_e32 v208, v56, v208\n" \
"v_mul_f32_e32 v209, v56, v209\n" \
"v_max_f32_e32 v62, v62, v57\n" \
"v_cvt_pk_bf16_f32 v202, v204, v205\n" \
"v_cvt_pk_bf16_f32 v203, v206, v207\n" \
"buffer_store_dwordx4 v[200:203], v20, s[56:59], s70 offen offset:1024 sc1\n" \
"v_mfma_f32_32x32x16_bf16 v[160:175], a[216:219], a[216:219], 0\n" \
"v_cvt_pk_bf16_f32 v208, v208, v209\n" \
"v_mul_f32_e32 v210, v56, v210\n" \
"v_mul_f32_e32 v211, v56, v211\n" \
"v_rcp_f32_e32 v57, v57\n" \
"v_cvt_pk_bf16_f32 v209, v210, v211\n" \
"v_mfma_f32_16x16x32_bf16 a[208:211], a[216:219], v[200:203], a[208:211]\n" \
"v_mul_f32_e32 v212, v56, v212\n" \
"v_mul_f32_e32 v213, v56, v213\n" \
"v_mfma_f32_32x32x16_bf16 v[176:191], a[216:219], a[216:219], 0\n" \
"v_mul_f32_e32 v214, v56, v214\n" \
"v_mul_f32_e32 v215, v56, v215\n" \
"v_mul_f32_e32 v216, v56, v216\n" \
"v_mul_f32_e32 v217, v56, v217\n" \
"v_cvt_pk_bf16_f32 v210, v212, v213\n" \
"v_cvt_pk_bf16_f32 v211, v214, v215\n" \
"s_waitcnt vmcnt(21)\n" \
"s_barrier\n" \
"buffer_store_dwordx4 v[208:211], v20, s[56:59], s70 offen offset:2048 sc1\n" \
"v_mfma_f32_16x16x32_bf16 v[64:67], a[128:131], a[64:67], v[32:35]\n" \
"s_xor_b32 m0, s65, 0x18000\n" \
"v_cvt_pk_bf16_f32 v216, v216, v217\n" \
"v_mfma_f32_16x16x32_bf16 v[64:67], a[132:135], a[68:71], v[64:67]\n" \
"buffer_load_dwordx4 v52, s[8:11], s64 offen offset:0 sc1 lds\n" \
"v_mfma_f32_16x16x32_bf16 v[64:67], a[136:139], a[72:75], v[64:67]\n" \
"v_mul_f32_e32 v218, v56, v218\n" \
"v_mul_f32_e32 v219, v56, v219\n" \
"v_mfma_f32_16x16x32_bf16 v[64:67], a[140:143], a[76:79], v[64:67]\n" \
"buffer_load_dwordx4 v52, s[8:11], s64 offen offset:1024 sc1 lds\n" \
"v_mfma_f32_16x16x32_bf16 v[68:71], a[156:159], a[76:79], v[32:35]\n" \
"v_mul_f32_e32 v57, v57, v61\n" \
"v_cvt_pk_bf16_f32 v217, v218, v219\n" \
"v_mfma_f32_16x16x32_bf16 v[68:71], a[152:155], a[72:75], v[68:71]\n" \
"v_mul_f32_e32 v220, v56, v220\n" \
"v_mul_f32_e32 v221, v56, v221\n" \
"v_mfma_f32_16x16x32_bf16 v[68:71], a[148:151], a[68:71], v[68:71]\n" \
"v_mul_f32_e32 v222, v56, v222\n" \
"v_mul_f32_e32 v223, v56, v223\n" \
"v_mfma_f32_16x16x32_bf16 v[68:71], a[144:147], a[64:67], v[68:71]\n" \
"buffer_load_dwordx4 v52, s[8:11], s64 offen offset:2048 sc1 lds\n" \
"v_mfma_f32_16x16x32_bf16 v[72:75], a[128:131], a[80:83], v[36:39]\n" \
"v_mul_f32_e32 v224, v57, v224\n" \
"v_mul_f32_e32 v225, v57, v225\n" \
"v_mfma_f32_16x16x32_bf16 v[72:75], a[132:135], a[84:87], v[72:75]\n" \
"v_cvt_pk_bf16_f32 v218, v220, v221\n" \
"v_cvt_pk_bf16_f32 v219, v222, v223\n" \
"buffer_store_dwordx4 v[216:219], v20, s[56:59], s70 offen offset:3072 sc1\n" \
"v_mfma_f32_16x16x32_bf16 v[72:75], a[136:139], a[88:91], v[72:75]\n" \
"v_cvt_pk_bf16_f32 v224, v224, v225\n" \
"v_mul_f32_e32 v226, v57, v226\n" \
"v_mfma_f32_16x16x32_bf16 v[72:75], a[140:143], a[92:95], v[72:75]\n" \
"buffer_load_dwordx4 v52, s[8:11], s64 offen offset:3072 sc1 lds\n" \
"v_mfma_f32_16x16x32_bf16 v[76:79], a[156:159], a[92:95], v[36:39]\n" \
"s_mov_b32 m0, s65\n" \
"v_mul_f32_e32 v227, v57, v227\n" \
"v_mfma_f32_16x16x32_bf16 v[76:79], a[152:155], a[88:91], v[76:79]\n" \
"v_cvt_pk_bf16_f32 v225, v226, v227\n" \
"v_mul_f32_e32 v228, v57, v228\n" \
"v_mfma_f32_16x16x32_bf16 v[76:79], a[148:151], a[84:87], v[76:79]\n" \
"v_mul_f32_e32 v229, v57, v229\n" \
"v_mul_f32_e32 v230, v57, v230\n" \
"v_mfma_f32_16x16x32_bf16 v[76:79], a[144:147], a[80:83], v[76:79]\n" \
"buffer_load_dwordx4 v53, s[8:11], s64 offen offset:0 sc1 lds\n" \
"v_mfma_f32_16x16x32_bf16 v[80:83], a[128:131], a[96:99], v[40:43]\n" \
"v_mul_f32_e32 v231, v57, v231\n" \
"v_mul_f32_e32 v232, v57, v232\n" \
"v_mfma_f32_16x16x32_bf16 v[80:83], a[132:135], a[100:103], v[80:83]\n" \
"v_mul_f32_e32 v233, v57, v233\n" \
"v_cvt_pk_bf16_f32 v226, v228, v229\n" \
"v_mfma_f32_16x16x32_bf16 v[80:83], a[136:139], a[104:107], v[80:83]\n" \
"buffer_load_dwordx4 v53, s[8:11], s64 offen offset:1024 sc1 lds\n" \
"v_mfma_f32_16x16x32_bf16 v[80:83], a[140:143], a[108:111], v[80:83]\n" \
"v_cvt_pk_bf16_f32 v227, v230, v231\n" \
"v_cvt_pk_bf16_f32 v232, v232, v233\n" \
"buffer_store_dwordx4 v[224:227], v20, s[56:59], s71 offen offset:0 sc1\n" \
"v_mfma_f32_16x16x32_bf16 v[84:87], a[156:159], a[108:111], v[40:43]\n" \
"v_mul_f32_e32 v234, v57, v234\n" \
"v_mul_f32_e32 v235, v57, v235\n" \
"v_mfma_f32_16x16x32_bf16 v[84:87], a[152:155], a[104:107], v[84:87]\n" \
"v_cvt_pk_bf16_f32 v233, v234, v235\n" \
"v_mul_f32_e32 v236, v57, v236\n" \
"v_mfma_f32_16x16x32_bf16 v[84:87], a[148:151], a[100:103], v[84:87]\n" \
"buffer_load_dwordx4 v53, s[8:11], s64 offen offset:2048 sc1 lds\n" \
"v_mfma_f32_16x16x32_bf16 v[84:87], a[144:147], a[96:99], v[84:87]\n" \
"v_mul_f32_e32 v237, v57, v237\n" \
"v_mul_f32_e32 v238, v57, v238\n" \
"v_mfma_f32_16x16x32_bf16 v[88:91], a[128:131], a[112:115], v[44:47]\n" \
"ds_read_b128 a[128:131], v48 offset:8192\n" \
"v_mul_f32_e32 v239, v57, v239\n" \
"v_mfma_f32_16x16x32_bf16 v[88:91], a[132:135], a[116:119], v[88:91]\n" \
"ds_read_b128 a[132:135], v48 offset:9216\n" \
"v_mul_f32_e32 v240, v57, v240\n" \
"v_mfma_f32_16x16x32_bf16 v[88:91], a[136:139], a[120:123], v[88:91]\n" \
"ds_read_b128 a[136:139], v48 offset:10240\n" \
"v_mul_f32_e32 v241, v57, v241\n" \
"v_mfma_f32_16x16x32_bf16 v[88:91], a[140:143], a[124:127], v[88:91]\n" \
"ds_read_b128 a[140:143], v48 offset:11264\n" \
"v_cvt_pk_bf16_f32 v234, v236, v237\n" \
"v_mfma_f32_16x16x32_bf16 v[92:95], a[156:159], a[124:127], v[44:47]\n" \
"ds_read_b128 a[156:159], v48 offset:15360\n" \
"v_cvt_pk_bf16_f32 v235, v238, v239\n" \
"buffer_store_dwordx4 v[232:235], v20, s[56:59], s71 offen offset:1024 sc1\n" \
"v_mfma_f32_16x16x32_bf16 v[92:95], a[152:155], a[120:123], v[92:95]\n" \
"ds_read_b128 a[152:155], v48 offset:14336\n" \
"v_cvt_pk_bf16_f32 v240, v240, v241\n" \
"v_mfma_f32_16x16x32_bf16 v[92:95], a[148:151], a[116:119], v[92:95]\n" \
"ds_read_b128 a[148:151], v48 offset:13312\n" \
"v_mul_f32_e32 v242, v57, v242\n" \
"v_mfma_f32_16x16x32_bf16 v[92:95], a[144:147], a[112:115], v[92:95]\n" \
"ds_read_b128 a[144:147], v48 offset:12288\n" \
"v_mul_f32_e32 v243, v57, v243\n" \
"v_mfma_f32_16x16x32_bf16 a[208:211], a[216:219], v[208:211], a[208:211]\n" \
"v_cvt_pk_bf16_f32 v241, v242, v243\n" \
"v_mul_f32_e32 v244, v57, v244\n" \
"v_mfma_f32_16x16x32_bf16 a[208:211], a[216:219], v[216:219], a[208:211]\n" \
"v_mul_f32_e32 v245, v57, v245\n" \
"v_mul_f32_e32 v246, v57, v246\n" \
"v_mfma_f32_16x16x32_bf16 a[208:211], a[216:219], v[224:227], a[208:211]\n" \
"v_mul_f32_e32 v247, v57, v247\n" \
"v_mul_f32_e32 v248, v57, v248\n" \
"v_mfma_f32_16x16x32_bf16 a[208:211], a[216:219], v[232:235], a[208:211]\n" \
"v_mul_f32_e32 v249, v57, v249\n" \
"v_cvt_pk_bf16_f32 v242, v244, v245\n" \
"v_mfma_f32_32x32x16_bf16 v[192:207], a[216:219], a[216:219], 0\n" \
"v_cvt_pk_bf16_f32 v243, v246, v247\n" \
"buffer_store_dwordx4 v[240:243], v20, s[56:59], s71 offen offset:2048 sc1\n" \
"v_cvt_pk_bf16_f32 v248, v248, v249\n" \
"v_mfma_f32_16x16x32_bf16 a[208:211], a[216:219], v[240:243], a[208:211]\n" \
"v_mul_f32_e32 v250, v57, v250\n" \
"v_mul_f32_e32 v251, v57, v251\n" \
"v_mfma_f32_32x32x16_bf16 v[208:223], a[216:219], a[216:219], 0\n" \
"v_cvt_pk_bf16_f32 v249, v250, v251\n" \
"v_mul_f32_e32 v252, v57, v252\n" \
"v_mul_f32_e32 v253, v57, v253\n" \
"v_mul_f32_e32 v254, v57, v254\n" \
"v_mul_f32_e32 v255, v57, v255\n" \
"v_cvt_pk_bf16_f32 v250, v252, v253\n" \
"v_mfma_f32_32x32x16_bf16 v[224:239], a[216:219], a[216:219], 0\n" \
"v_cvt_pk_bf16_f32 v251, v254, v255\n" \
"buffer_store_dwordx4 v[248:251], v20, s[56:59], s71 offen offset:3072 sc1\n" \
"buffer_load_dwordx4 v53, s[8:11], s64 offen offset:3072 sc1 lds\n" \
"v_mfma_f32_16x16x32_bf16 a[208:211], a[216:219], v[248:251], a[208:211]\n" \
"v_cmp_lt_f32_e64 s[72:73], s100, v62\n" \
"v_readlane_b32 s95, v11, 2\n" \
"v_mfma_f32_32x32x16_bf16 v[240:255], a[216:219], a[216:219], 0\n" \
"v_exp_f32_e32 v64, v64\n" \
"v_exp_f32_e32 v65, v65\n" \
"v_exp_f32_e32 v66, v66\n" \
"v_mfma_f32_32x32x16_bf16 a[192:207], a[216:219], a[216:219], 0\n" \
"v_exp_f32_e32 v67, v67\n" \
"v_accvgpr_read_b32 v63, a208\n" \
"v_cmp_u_f32_e32 vcc, v63, v63\n" \
"v_exp_f32_e32 v68, v68\n" \
"s_or_b64 vcc, s[72:73], vcc\n" \
"s_cselect_b32 s95, 0x40000000, s95\n" \
"s_cselect_b32 s94, 0x7f800000, 0\n" \
"v_exp_f32_e32 v69, v69\n" \
"v_exp_f32_e32 v70, v70\n" \
"v_exp_f32_e32 v71, v71\n" \
"v_exp_f32_e32 v72, v72\n" \
"v_exp_f32_e32 v73, v73\n" \
"v_exp_f32_e32 v74, v74\n" \
"v_exp_f32_e32 v75, v75\n" \
"v_exp_f32_e32 v76, v76\n" \
"v_exp_f32_e32 v77, v77\n" \
"v_exp_f32_e32 v78, v78\n" \
"v_exp_f32_e32 v79, v79\n" \
"v_exp_f32_e32 v80, v80\n" \
"v_exp_f32_e32 v81, v81\n" \
"v_exp_f32_e32 v82, v82\n" \
"v_exp_f32_e32 v83, v83\n" \
"v_exp_f32_e32 v84, v84\n" \
"v_exp_f32_e32 v85, v85\n" \
"v_exp_f32_e32 v86, v86\n" \
"v_exp_f32_e32 v87, v87\n" \
"v_add_u32_e32 v49, 0x10000, v48\n" \
"s_xor_b32 s65, s65, 0x8000\n" \
"s_add_u32 s64, s64, 0x10000\n" \
"v_exp_f32_e32 v88, v88\n" \
"v_exp_f32_e32 v89, v89\n" \
"v_exp_f32_e32 v90, v90\n" \
"v_exp_f32_e32 v91, v91\n" \
"v_cvt_pk_bf16_f32 v96, v64, v65\n" \
"v_cvt_pk_bf16_f32 v97, v66, v67\n" \
"v_cvt_pk_bf16_f32 v98, v68, v69\n" \
"v_cvt_pk_bf16_f32 v99, v70, v71\n" \
"v_cvt_pk_bf16_f32 v104, v72, v73\n" \
"s_waitcnt lgkmcnt(0)\n" \
"v_cvt_pk_bf16_f32 v105, v74, v75\n" \
"v_cvt_pk_bf16_f32 v106, v76, v77\n" \
"v_cvt_pk_bf16_f32 v107, v78, v79\n" \
"v_cvt_pk_bf16_f32 v112, v80, v81\n" \
"v_cvt_pk_bf16_f32 v113, v82, v83\n" \
"v_cvt_pk_bf16_f32 v114, v84, v85\n" \
"v_cvt_pk_bf16_f32 v115, v86, v87\n" \
"v_xor_b32_e32 v48, 0x8000, v48\n" \
"s_sub_u32 s66, s19, 1\n" \
"s_cbranch_scc0 L_2_%=\n" \
"s_waitcnt vmcnt(14)\n" \
"s_barrier\n" \
"L_f1_%=:\n" \
"L_pf18:\n" \
"s_cbranch_execz L_retA\n" \
"s_nop 0\n" \
"ds_read_b128 a[160:163], v49 offset:0\n" \
"ds_read_b128 a[164:167], v49 offset:1024\n" \
"ds_read_b128 a[168:171], v49 offset:2048\n" \
"ds_read_b128 a[172:175], v49 offset:3072\n" \
"ds_read_b128 a[176:179], v49 offset:8192\n" \
"ds_read_b128 a[180:183], v49 offset:9216\n" \
"v_mfma_f32_16x16x32_bf16 v[72:75], a[128:131], a[80:83], v[36:39]\n" \
"s_add_u32 s68, s61, 0x0\n" \
"s_add_u32 s69, s61, 0x49000\n" \
"ds_read_b128 a[184:187], v49 offset:10240\n" \
"v_mfma_f32_16x16x32_bf16 v[72:75], a[132:135], a[84:87], v[72:75]\n" \
"ds_read_b128 a[188:191], v49 offset:11264\n" \
"v_add_u32_e32 v56, 0x49000, v20\n" \
"v_mfma_f32_16x16x32_bf16 v[72:75], a[136:139], a[88:91], v[72:75]\n" \
"v_readlane_b32 s97, v11, 32\n" \
"v_readlane_b32 s98, v11, 33\n" \
"v_mfma_f32_16x16x32_bf16 v[72:75], a[140:143], a[92:95], v[72:75]\n" \
"v_and_b32_e32 v96, v12, v96\n" \
"v_and_b32_e32 v97, v13, v97\n" \
"v_mfma_f32_16x16x32_bf16 v[76:79], a[156:159], a[92:95], v[36:39]\n" \
"buffer_load_dwordx4 a[32:35], v20, s[56:59], s97 offen offset:0 sc1\n" \
"v_mfma_f32_16x16x32_bf16 v[76:79], a[152:155], a[88:91], v[76:79]\n" \
"v_and_b32_e32 v98, v14, v98\n" \
"v_and_b32_e32 v99, v15, v99\n" \
"v_mfma_f32_16x16x32_bf16 v[76:79], a[148:151], a[84:87], v[76:79]\n" \
"ds_read_b128 a[0:3], v20 offset:8192\n" \
"v_mov_b32_e32 v61, s62\n" \
"v_mfma_f32_16x16x32_bf16 v[76:79], a[144:147], a[80:83], v[76:79]\n" \
"buffer_load_dwordx4 a[40:43], v56, s[56:59], s97 offen offset:0 sc1\n" \
"ds_read_b128 a[4:7], v20 offset:9216\n" \
"ds_read_b128 a[8:11], v20 offset:10240\n" \
"s_waitcnt lgkmcnt(10)\n" \
"v_mfma_f32_16x16x32_bf16 v[128:131], a[160:163], v[96:99], v[128:131]\n" \
"ds_read_b128 a[12:15], v20 offset:11264\n" \
"s_waitcnt lgkmcnt(10)\n" \
"v_mfma_f32_16x16x32_bf16 v[132:135], a[164:167], v[96:99], v[132:135]\n" \
"ds_read_b128 a[28:31], v20 offset:15360\n" \
"buffer_load_dwordx4 a[48:51], v20, s[56:59], s98 offen offset:0 sc1\n" \
"s_waitcnt lgkmcnt(10)\n" \
"v_mfma_f32_16x16x32_bf16 v[136:139], a[168:171], v[96:99], v[136:139]\n" \
"ds_read_b128 a[24:27], v20 offset:14336\n" \
"s_waitcnt lgkmcnt(10)\n" \
"v_mfma_f32_16x16x32_bf16 v[140:143], a[172:175], v[96:99], v[140:143]\n" \
"ds_read_b128 a[20:23], v20 offset:13312\n" \
"buffer_load_dwordx4 a[56:59], v56, s[56:59], s98 offen offset:0 sc1\n" \
"s_waitcnt lgkmcnt(9)\n" \
"v_mfma_f32_16x16x32_bf16 v[144:147], a[176:179], v[96:99], v[144:147]\n" \
"ds_read_b128 a[16:19], v20 offset:12288\n" \
"v_add_u32_e32 v50, s15, v48\n" \
"v_mfma_f32_16x16x32_bf16 v[148:151], a[180:183], v[96:99], v[148:151]\n" \
"v_xor_b32_e32 v48, 0x4000, v49\n" \
"s_waitcnt lgkmcnt(8)\n" \
"v_mfma_f32_16x16x32_bf16 v[152:155], a[184:187], v[96:99], v[152:155]\n" \
"buffer_load_dwordx4 a[36:39], v20, s[56:59], s97 offen offset:1024 sc1\n" \
"v_mfma_f32_16x16x32_bf16 v[156:159], a[188:191], v[96:99], v[156:159]\n" \
"ds_read_b32 v58, v18 offset:256\n" \
"ds_read_b32 v59, v18 offset:384\n" \
"v_mfma_f32_16x16x32_bf16 a[192:195], a[252:255], v[96:99], a[192:195]\n" \
"ds_read_b128 a[96:99], v48 offset:4096\n" \
"ds_read_b128 a[100:103], v48 offset:5120\n" \
"buffer_load_dwordx4 a[44:47], v56, s[56:59], s97 offen offset:1024 sc1\n" \
"v_mfma_f32_16x16x32_bf16 v[160:163], a[160:163], v[104:107], v[160:163]\n" \
"ds_read_b128 a[104:107], v48 offset:6144\n" \
"ds_read_b128 a[108:111], v48 offset:7168\n" \
"v_mfma_f32_16x16x32_bf16 v[164:167], a[164:167], v[104:107], v[164:167]\n" \
"ds_read_b128 a[112:115], v48 offset:12288\n" \
"ds_read_b128 a[116:119], v48 offset:13312\n" \
"buffer_load_dwordx4 a[52:55], v20, s[56:59], s98 offen offset:1024 sc1\n" \
"v_mfma_f32_16x16x32_bf16 v[168:171], a[168:171], v[104:107], v[168:171]\n" \
"ds_read_b128 a[120:123], v48 offset:14336\n" \
"ds_read_b128 a[124:127], v48 offset:15360\n" \
"v_mfma_f32_16x16x32_bf16 v[172:175], a[172:175], v[104:107], v[172:175]\n" \
"ds_read_b128 a[128:131], v49 offset:4096\n" \
"ds_read_b128 a[132:135], v49 offset:5120\n" \
"buffer_load_dwordx4 a[60:63], v56, s[56:59], s98 offen offset:1024 sc1\n" \
"v_mfma_f32_16x16x32_bf16 v[176:179], a[176:179], v[104:107], v[176:179]\n" \
"ds_read_b128 a[136:139], v49 offset:6144\n" \
"ds_read_b128 a[140:143], v49 offset:7168\n" \
"v_mfma_f32_16x16x32_bf16 v[180:183], a[180:183], v[104:107], v[180:183]\n" \
"ds_read_b128 a[144:147], v49 offset:12288\n" \
"ds_read_b128 a[148:151], v49 offset:13312\n" \
"s_waitcnt vmcnt(8)\n" \
"s_mov_b64 exec, 1\n" \
"buffer_store_dword v61, off, s[56:59], s95 offset:0 sc1\n" \
"s_mov_b64 exec, -1\n" \
"v_mfma_f32_16x16x32_bf16 v[184:187], a[184:187], v[104:107], v[184:187]\n" \
"ds_read_b128 a[152:155], v49 offset:14336\n" \
"ds_read_b128 a[156:159], v49 offset:15360\n" \
"v_mfma_f32_16x16x32_bf16 v[188:191], a[188:191], v[104:107], v[188:191]\n" \
"v_accvgpr_read_b32 v40, a200\n" \
"v_accvgpr_read_b32 v23, a201\n" \
"v_mfma_f32_16x16x32_bf16 a[196:199], a[252:255], v[104:107], a[196:199]\n" \
"v_add_f32_e32 v40, v40, v23\n" \
"s_waitcnt lgkmcnt(15)\n" \
"v_mfma_f32_16x16x32_bf16 v[80:83], a[0:3], a[64:67], v[32:35]\n" \
"buffer_load_dwordx4 a[220:223], v20, s[56:59], s60 offen offset:0 sc1\n" \
"v_mfma_f32_16x16x32_bf16 v[80:83], a[4:7], a[68:71], v[80:83]\n" \
"v_accvgpr_read_b32 v41, a204\n" \
"v_accvgpr_read_b32 v23, a205\n" \
"v_mfma_f32_16x16x32_bf16 v[80:83], a[8:11], a[72:75], v[80:83]\n" \
"v_add_f32_e32 v41, v41, v23\n" \
"v_add_u32_e32 v51, 0x10000, v50\n" \
"v_mfma_f32_16x16x32_bf16 v[80:83], a[12:15], a[76:79], v[80:83]\n" \
"buffer_load_dwordx4 a[236:239], v56, s[56:59], s60 offen offset:0 sc1\n" \
"v_mfma_f32_16x16x32_bf16 v[84:87], a[28:31], a[76:79], v[32:35]\n" \
"v_add_f32_e32 v40, s94, v40\n" \
"v_add_f32_e32 v41, s94, v41\n" \
"v_mfma_f32_16x16x32_bf16 v[84:87], a[24:27], a[72:75], v[84:87]\n" \
"ds_write_b128 v50, v[192:195] offset:0\n" \
"buffer_load_dwordx4 a[224:227], v20, s[56:59], s60 offen offset:1024 sc1\n" \
"v_mfma_f32_16x16x32_bf16 v[84:87], a[20:23], a[68:71], v[84:87]\n" \
"ds_write_b128 v50, v[196:199] offset:1024\n" \
"v_mfma_f32_16x16x32_bf16 v[84:87], a[16:19], a[64:67], v[84:87]\n" \
"ds_write_b128 v50, v[200:203] offset:2048\n" \
"buffer_load_dwordx4 a[240:243], v56, s[56:59], s60 offen offset:1024 sc1\n" \
"L_pf19:\n" \
"s_cbranch_execz L_retA\n" \
"s_nop 0\n" \
"v_mfma_f32_16x16x32_bf16 v[88:91], a[0:3], a[80:83], v[36:39]\n" \
"ds_write_b128 v50, v[204:207] offset:3072\n" \
"v_mfma_f32_16x16x32_bf16 v[88:91], a[4:7], a[84:87], v[88:91]\n" \
"ds_write_b128 v50, v[208:211] offset:8192\n" \
"buffer_load_dwordx4 a[228:231], v20, s[56:59], s60 offen offset:2048 sc1\n" \
"v_mfma_f32_16x16x32_bf16 v[88:91], a[8:11], a[88:91], v[88:91]\n" \
"ds_write_b128 v50, v[212:215] offset:9216\n" \
"v_mfma_f32_16x16x32_bf16 v[88:91], a[12:15], a[92:95], v[88:91]\n" \
"ds_write_b128 v50, v[216:219] offset:10240\n" \
"buffer_load_dwordx4 a[244:247], v56, s[56:59], s60 offen offset:2048 sc1\n" \
"v_mfma_f32_16x16x32_bf16 v[92:95], a[28:31], a[92:95], v[36:39]\n" \
"ds_write_b128 v50, v[220:223] offset:11264\n" \
"ds_write_b128 v51, v[224:227] offset:0\n" \
"ds_write_b128 v51, v[228:231] offset:1024\n" \
"buffer_load_dwordx4 a[232:235], v20, s[56:59], s60 offen offset:3072 sc1\n" \
"ds_write_b128 v51, v[232:235] offset:2048\n" \
"ds_write_b128 v51, v[236:239] offset:3072\n" \
"ds_write_b128 v51, v[240:243] offset:8192\n" \
"ds_write_b128 v51, v[244:247] offset:9216\n" \
"buffer_load_dwordx4 a[248:251], v56, s[56:59], s60 offen offset:3072 sc1\n" \
"ds_write_b128 v51, v[248:251] offset:10240\n" \
"ds_write_b128 v51, v[252:255] offset:11264\n" \
"ds_write_b64 v19, v[40:41]\n" \
"v_mfma_f32_16x16x32_bf16 v[92:95], a[24:27], a[88:91], v[92:95]\n" \
"v_xor_b32_e32 v52, 0x4000, v50\n" \
"v_xor_b32_e32 v53, 0x4000, v51\n" \
"v_mfma_f32_16x16x32_bf16 v[92:95], a[20:23], a[84:87], v[92:95]\n" \
"v_exp_f32_e32 v72, v72\n" \
"v_mfma_f32_16x16x32_bf16 v[92:95], a[16:19], a[80:83], v[92:95]\n" \
"v_exp_f32_e32 v73, v73\n" \
"v_exp_f32_e32 v74, v74\n" \
"v_exp_f32_e32 v75, v75\n" \
"v_exp_f32_e32 v76, v76\n" \
"v_exp_f32_e32 v77, v77\n" \
"v_exp_f32_e32 v78, v78\n" \
"v_exp_f32_e32 v79, v79\n" \
"v_cvt_pk_bf16_f32 v108, v72, v73\n" \
"v_cvt_pk_bf16_f32 v109, v74, v75\n" \
"v_cvt_pk_bf16_f32 v110, v76, v77\n" \
"v_cvt_pk_bf16_f32 v111, v78, v79\n" \
"v_and_b32_e32 v108, v12, v108\n" \
"v_and_b32_e32 v109, v13, v109\n" \
"v_and_b32_e32 v110, v14, v110\n" \
"v_and_b32_e32 v111, v15, v111\n" \
"v_readlane_b32 s93, v11, 18\n" \
"v_readlane_b32 s84, v11, 13\n" \
"v_readlane_b32 s85, v11, 14\n" \
"v_readlane_b32 s86, v11, 15\n" \
"s_mov_b32 s87, 0x20000\n" \
"v_readlane_b32 s92, v11, 16\n" \
"s_add_u32 m0, s93, 0x0\n" \
"v_xor_b32_e32 v63, 0x400, v19\n" \
"s_waitcnt lgkmcnt(0)\n" \
"s_barrier\n" \
"ds_read_b128 v[192:195], v52 offset:0\n" \
"ds_read_b128 v[196:199], v52 offset:1024\n" \
"ds_read_b128 v[200:203], v52 offset:2048\n" \
"ds_read_b128 v[204:207], v52 offset:3072\n" \
"ds_read_b128 v[208:211], v52 offset:8192\n" \
"ds_read_b128 v[212:215], v52 offset:9216\n" \
"ds_read_b128 v[216:219], v52 offset:10240\n" \
"ds_read_b128 v[220:223], v52 offset:11264\n" \
"ds_read_b128 v[224:227], v53 offset:0\n" \
"ds_read_b128 v[228:231], v53 offset:1024\n" \
"ds_read_b128 v[232:235], v53 offset:2048\n" \
"ds_read_b128 v[236:239], v53 offset:3072\n" \
"ds_read_b128 v[240:243], v53 offset:8192\n" \
"ds_read_b128 v[244:247], v53 offset:9216\n" \
"ds_read_b128 v[248:251], v53 offset:10240\n" \
"ds_read_b128 v[252:255], v53 offset:11264\n" \
"v_mfma_f32_16x16x32_bf16 v[160:163], a[128:131], v[108:111], v[160:163]\n" \
"buffer_load_dwordx4 v0, s[84:87], s92 offen offset:0 lds\n" \
"v_mfma_f32_16x16x32_bf16 v[164:167], a[132:135], v[108:111], v[164:167]\n" \
"v_exp_f32_e32 v80, v80\n" \
"v_mfma_f32_16x16x32_bf16 v[168:171], a[136:139], v[108:111], v[168:171]\n" \
"v_exp_f32_e32 v81, v81\n" \
"v_mfma_f32_16x16x32_bf16 v[172:175], a[140:143], v[108:111], v[172:175]\n" \
"buffer_load_dwordx4 v1, s[84:87], s92 offen offset:1024 lds\n" \
"v_mfma_f32_16x16x32_bf16 v[176:179], a[144:147], v[108:111], v[176:179]\n" \
"ds_read_b64 v[42:43], v63\n" \
"v_cvt_pk_bf16_f32 v116, v80, v81\n" \
"v_mfma_f32_16x16x32_bf16 v[180:183], a[148:151], v[108:111], v[180:183]\n" \
"v_exp_f32_e32 v82, v82\n" \
"v_mfma_f32_16x16x32_bf16 v[184:187], a[152:155], v[108:111], v[184:187]\n" \
"buffer_load_dwordx4 v2, s[84:87], s92 offen offset:2048 lds\n" \
"v_mfma_f32_16x16x32_bf16 v[188:191], a[156:159], v[108:111], v[188:191]\n" \
"v_exp_f32_e32 v83, v83\n" \
"v_mfma_f32_16x16x32_bf16 a[196:199], a[252:255], v[108:111], a[196:199]\n" \
"v_exp_f32_e32 v84, v84\n" \
"v_exp_f32_e32 v85, v85\n" \
"v_exp_f32_e32 v86, v86\n" \
"s_waitcnt lgkmcnt(1)\n" \
"s_barrier\n" \
"buffer_load_dwordx4 v3, s[84:87], s92 offen offset:3072 lds\n" \
"s_add_u32 m0, s93, 0x1000\n" \
"v_exp_f32_e32 v87, v87\n" \
"v_cvt_pk_bf16_f32 v117, v82, v83\n" \
"v_cvt_pk_bf16_f32 v118, v84, v85\n" \
"v_cvt_pk_bf16_f32 v119, v86, v87\n" \
"v_exp_f32_e32 v88, v88\n" \
"v_exp_f32_e32 v89, v89\n" \
"v_mfma_f32_16x16x32_bf16 v[128:131], a[96:99], v[116:119], v[128:131]\n" \
"buffer_load_dwordx4 v4, s[84:87], s92 offen offset:0 lds\n" \
"v_mfma_f32_16x16x32_bf16 v[132:135], a[100:103], v[116:119], v[132:135]\n" \
"v_exp_f32_e32 v90, v90\n" \
"v_mfma_f32_16x16x32_bf16 v[136:139], a[104:107], v[116:119], v[136:139]\n" \
"v_exp_f32_e32 v91, v91\n" \
"v_mfma_f32_16x16x32_bf16 v[140:143], a[108:111], v[116:119], v[140:143]\n" \
"buffer_load_dwordx4 v5, s[84:87], s92 offen offset:1024 lds\n" \
"v_mfma_f32_16x16x32_bf16 v[144:147], a[112:115], v[116:119], v[144:147]\n" \
"v_exp_f32_e32 v92, v92\n" \
"v_mfma_f32_16x16x32_bf16 v[148:151], a[116:119], v[116:119], v[148:151]\n" \
"v_exp_f32_e32 v93, v93\n" \
"v_mfma_f32_16x16x32_bf16 v[152:155], a[120:123], v[116:119], v[152:155]\n" \
"buffer_load_dwordx4 v6, s[84:87], s92 offen offset:2048 lds\n" \
"v_mfma_f32_16x16x32_bf16 v[156:159], a[124:127], v[116:119], v[156:159]\n" \
"v_exp_f32_e32 v94, v94\n" \
"v_mfma_f32_16x16x32_bf16 a[192:195], a[252:255], v[116:119], a[192:195]\n" \
"v_exp_f32_e32 v95, v95\n" \
"v_cvt_pk_bf16_f32 v124, v88, v89\n" \
"v_cvt_pk_bf16_f32 v125, v90, v91\n" \
"v_cvt_pk_bf16_f32 v126, v92, v93\n" \
"v_cvt_pk_bf16_f32 v127, v94, v95\n" \
"v_add_f32_e32 v160, v160, v224\n" \
"v_add_f32_e32 v161, v161, v225\n" \
"v_add_f32_e32 v162, v162, v226\n" \
"v_add_f32_e32 v163, v163, v227\n" \
"v_add_f32_e32 v164, v164, v228\n" \
"v_add_f32_e32 v165, v165, v229\n" \
"v_mfma_f32_16x16x32_bf16 v[160:163], a[96:99], v[124:127], v[160:163]\n" \
"buffer_load_dwordx4 v7, s[84:87], s92 offen offset:3072 lds\n" \
"s_add_u32 m0, s93, 0x2000\n" \
"v_add_f32_e32 v166, v166, v230\n" \
"v_add_f32_e32 v167, v167, v231\n" \
"v_add_f32_e32 v168, v168, v232\n" \
"v_add_f32_e32 v169, v169, v233\n" \
"v_mfma_f32_16x16x32_bf16 v[164:167], a[100:103], v[124:127], v[164:167]\n" \
"v_add_f32_e32 v170, v170, v234\n" \
"v_add_f32_e32 v171, v171, v235\n" \
"v_add_f32_e32 v172, v172, v236\n" \
"v_add_f32_e32 v173, v173, v237\n" \
"v_mfma_f32_16x16x32_bf16 v[168:171], a[104:107], v[124:127], v[168:171]\n" \
"buffer_load_dwordx4 v8, s[84:87], s92 offen offset:0 lds\n" \
"v_add_f32_e32 v174, v174, v238\n" \
"v_add_f32_e32 v175, v175, v239\n" \
"v_accvgpr_read_b32 v54, a192\n" \
"v_accvgpr_read_b32 v23, a193\n" \
"v_mfma_f32_16x16x32_bf16 v[172:175], a[108:111], v[124:127], v[172:175]\n" \
"v_add_f32_e32 v54, v54, v23\n" \
"s_waitcnt lgkmcnt(0)\n" \
"v_add_f32_e32 v54, v54, v42\n" \
"v_max_f32_e32 v62, v62, v54\n" \
"v_add_f32_e32 v176, v176, v240\n" \
"v_add_f32_e32 v177, v177, v241\n" \
"v_add_f32_e32 v178, v178, v242\n" \
"v_add_f32_e32 v179, v179, v243\n" \
"v_rcp_f32_e32 v54, v54\n" \
"v_add_f32_e32 v128, v128, v192\n" \
"v_mfma_f32_16x16x32_bf16 v[176:179], a[112:115], v[124:127], v[176:179]\n" \
"v_add_f32_e32 v129, v129, v193\n" \
"v_add_f32_e32 v130, v130, v194\n" \
"v_add_f32_e32 v131, v131, v195\n" \
"v_add_f32_e32 v132, v132, v196\n" \
"v_add_f32_e32 v133, v133, v197\n" \
"v_add_f32_e32 v134, v134, v198\n" \
"v_add_f32_e32 v135, v135, v199\n" \
"v_mul_f32_e32 v54, v54, v58\n" \
"v_mul_f32_e32 v128, v54, v128\n" \
"v_mul_f32_e32 v129, v54, v129\n" \
"v_mul_f32_e32 v130, v54, v130\n" \
"v_mul_f32_e32 v131, v54, v131\n" \
"v_mul_f32_e32 v132, v54, v132\n" \
"v_mul_f32_e32 v133, v54, v133\n" \
"v_mul_f32_e32 v134, v54, v134\n" \
"v_mul_f32_e32 v135, v54, v135\n" \
"v_add_f32_e32 v180, v180, v244\n" \
"v_add_f32_e32 v181, v181, v245\n" \
"v_add_f32_e32 v182, v182, v246\n" \
"v_add_f32_e32 v183, v183, v247\n" \
"v_add_f32_e32 v136, v136, v200\n" \
"v_add_f32_e32 v137, v137, v201\n" \
"v_add_f32_e32 v138, v138, v202\n" \
"v_add_f32_e32 v139, v139, v203\n" \
"v_add_f32_e32 v140, v140, v204\n" \
"v_add_f32_e32 v141, v141, v205\n" \
"v_add_f32_e32 v142, v142, v206\n" \
"v_add_f32_e32 v143, v143, v207\n" \
"v_cvt_pk_bf16_f32 v64, v128, v129\n" \
"v_cvt_pk_bf16_f32 v65, v130, v131\n" \
"v_cvt_pk_bf16_f32 v66, v132, v133\n" \
"v_cvt_pk_bf16_f32 v67, v134, v135\n" \
"buffer_store_dwordx4 v[64:67], v20, s[56:59], s68 offen offset:0 sc1\n" \
"v_mfma_f32_16x16x32_bf16 v[180:183], a[116:119], v[124:127], v[180:183]\n" \
"v_mul_f32_e32 v136, v54, v136\n" \
"v_mul_f32_e32 v137, v54, v137\n" \
"v_mfma_f32_16x16x32_bf16 a[208:211], a[216:219], v[64:67], a[208:211]\n" \
"v_mul_f32_e32 v138, v54, v138\n" \
"v_mul_f32_e32 v139, v54, v139\n" \
"v_mul_f32_e32 v140, v54, v140\n" \
"v_mul_f32_e32 v141, v54, v141\n" \
"v_mul_f32_e32 v142, v54, v142\n" \
"v_mul_f32_e32 v143, v54, v143\n" \
"v_add_f32_e32 v144, v144, v208\n" \
"v_add_f32_e32 v145, v145, v209\n" \
"v_add_f32_e32 v146, v146, v210\n" \
"v_add_f32_e32 v147, v147, v211\n" \
"v_add_f32_e32 v148, v148, v212\n" \
"v_add_f32_e32 v149, v149, v213\n" \
"v_add_f32_e32 v150, v150, v214\n" \
"v_add_f32_e32 v151, v151, v215\n" \
"v_cvt_pk_bf16_f32 v68, v136, v137\n" \
"v_cvt_pk_bf16_f32 v69, v138, v139\n" \
"v_cvt_pk_bf16_f32 v70, v140, v141\n" \
"v_cvt_pk_bf16_f32 v71, v142, v143\n" \
"buffer_store_dwordx4 v[68:71], v20, s[56:59], s68 offen offset:1024 sc1\n" \
"v_mul_f32_e32 v144, v54, v144\n" \
"v_mfma_f32_16x16x32_bf16 a[208:211], a[216:219], v[68:71], a[208:211]\n" \
"v_mul_f32_e32 v145, v54, v145\n" \
"v_mul_f32_e32 v146, v54, v146\n" \
"v_mul_f32_e32 v147, v54, v147\n" \
"v_mul_f32_e32 v148, v54, v148\n" \
"v_mul_f32_e32 v149, v54, v149\n" \
"v_mul_f32_e32 v150, v54, v150\n" \
"v_mul_f32_e32 v151, v54, v151\n" \
"v_add_f32_e32 v184, v184, v248\n" \
"v_add_f32_e32 v185, v185, v249\n" \
"v_add_f32_e32 v186, v186, v250\n" \
"v_add_f32_e32 v187, v187, v251\n" \
"v_add_f32_e32 v152, v152, v216\n" \
"v_add_f32_e32 v153, v153, v217\n" \
"v_add_f32_e32 v154, v154, v218\n" \
"v_add_f32_e32 v155, v155, v219\n" \
"v_add_f32_e32 v156, v156, v220\n" \
"v_add_f32_e32 v157, v157, v221\n" \
"v_add_f32_e32 v158, v158, v222\n" \
"v_add_f32_e32 v159, v159, v223\n" \
"v_cvt_pk_bf16_f32 v72, v144, v145\n" \
"v_cvt_pk_bf16_f32 v73, v146, v147\n" \
"v_cvt_pk_bf16_f32 v74, v148, v149\n" \
"v_cvt_pk_bf16_f32 v75, v150, v151\n" \
"buffer_store_dwordx4 v[72:75], v20, s[56:59], s68 offen offset:2048 sc1\n" \
"v_mfma_f32_16x16x32_bf16 v[184:187], a[120:123], v[124:127], v[184:187]\n" \
"v_mul_f32_e32 v152, v54, v152\n" \
"v_mul_f32_e32 v153, v54, v153\n" \
"v_mfma_f32_16x16x32_bf16 a[208:211], a[216:219], v[72:75], a[208:211]\n" \
"v_mul_f32_e32 v154, v54, v154\n" \
"v_mul_f32_e32 v155, v54, v155\n" \
"v_mul_f32_e32 v156, v54, v156\n" \
"v_mul_f32_e32 v157, v54, v157\n" \
"v_mul_f32_e32 v158, v54, v158\n" \
"v_mul_f32_e32 v159, v54, v159\n" \
"v_cvt_pk_bf16_f32 v76, v152, v153\n" \
"v_cvt_pk_bf16_f32 v77, v154, v155\n" \
"v_cvt_pk_bf16_f32 v78, v156, v157\n" \
"v_cvt_pk_bf16_f32 v79, v158, v159\n" \
"buffer_store_dwordx4 v[76:79], v20, s[56:59], s68 offen offset:3072 sc1\n" \
"v_add_f32_e32 v188, v188, v252\n" \
"v_mfma_f32_16x16x32_bf16 a[208:211], a[216:219], v[76:79], a[208:211]\n" \
"v_add_f32_e32 v189, v189, v253\n" \
"v_add_f32_e32 v190, v190, v254\n" \
"v_add_f32_e32 v191, v191, v255\n" \
"s_nop 1\n" \
"v_mfma_f32_16x16x32_bf16 v[188:191], a[124:127], v[124:127], v[188:191]\n" \
"v_mfma_f32_16x16x32_bf16 a[196:199], a[252:255], v[124:127], a[196:199]\n" \
"s_nop 7\n" \
"v_accvgpr_read_b32 v55, a196\n" \
"v_accvgpr_read_b32 v23, a197\n" \
"v_add_f32_e32 v55, v55, v23\n" \
"v_add_f32_e32 v55, v55, v43\n" \
"v_max_f32_e32 v62, v62, v55\n" \
"v_rcp_f32_e32 v55, v55\n" \
"v_cmp_lt_f32_e64 s[72:73], s100, v62\n" \
"v_mul_f32_e32 v55, v55, v59\n" \
"v_mul_f32_e32 v160, v55, v160\n" \
"v_mul_f32_e32 v161, v55, v161\n" \
"v_mul_f32_e32 v162, v55, v162\n" \
"v_mul_f32_e32 v163, v55, v163\n" \
"v_mul_f32_e32 v164, v55, v164\n" \
"v_mul_f32_e32 v165, v55, v165\n" \
"v_mul_f32_e32 v166, v55, v166\n" \
"v_mul_f32_e32 v167, v55, v167\n" \
"v_cvt_pk_bf16_f32 v80, v160, v161\n" \
"v_cvt_pk_bf16_f32 v81, v162, v163\n" \
"v_cvt_pk_bf16_f32 v82, v164, v165\n" \
"v_cvt_pk_bf16_f32 v83, v166, v167\n" \
"buffer_store_dwordx4 v[80:83], v20, s[56:59], s69 offen offset:0 sc1\n" \
"v_mul_f32_e32 v168, v55, v168\n" \
"v_mfma_f32_16x16x32_bf16 a[208:211], a[216:219], v[80:83], a[208:211]\n" \
"v_mul_f32_e32 v169, v55, v169\n" \
"v_mul_f32_e32 v170, v55, v170\n" \
"v_mul_f32_e32 v171, v55, v171\n" \
"v_mul_f32_e32 v172, v55, v172\n" \
"v_mul_f32_e32 v173, v55, v173\n" \
"v_mul_f32_e32 v174, v55, v174\n" \
"v_mul_f32_e32 v175, v55, v175\n" \
"v_cvt_pk_bf16_f32 v84, v168, v169\n" \
"v_cvt_pk_bf16_f32 v85, v170, v171\n" \
"v_cvt_pk_bf16_f32 v86, v172, v173\n" \
"v_cvt_pk_bf16_f32 v87, v174, v175\n" \
"buffer_store_dwordx4 v[84:87], v20, s[56:59], s69 offen offset:1024 sc1\n" \
"v_mul_f32_e32 v176, v55, v176\n" \
"v_mfma_f32_16x16x32_bf16 a[208:211], a[216:219], v[84:87], a[208:211]\n" \
"v_mul_f32_e32 v177, v55, v177\n" \
"v_mul_f32_e32 v178, v55, v178\n" \
"v_mul_f32_e32 v179, v55, v179\n" \
"v_mul_f32_e32 v180, v55, v180\n" \
"v_mul_f32_e32 v181, v55, v181\n" \
"v_mul_f32_e32 v182, v55, v182\n" \
"v_mul_f32_e32 v183, v55, v183\n" \
"v_cvt_pk_bf16_f32 v96, v176, v177\n" \
"v_cvt_pk_bf16_f32 v97, v178, v179\n" \
"v_cvt_pk_bf16_f32 v98, v180, v181\n" \
"v_cvt_pk_bf16_f32 v99, v182, v183\n" \
"buffer_store_dwordx4 v[96:99], v20, s[56:59], s69 offen offset:2048 sc1\n" \
"v_mul_f32_e32 v184, v55, v184\n" \
"v_mfma_f32_16x16x32_bf16 a[208:211], a[216:219], v[96:99], a[208:211]\n" \
"v_mul_f32_e32 v185, v55, v185\n" \
"v_mul_f32_e32 v186, v55, v186\n" \
"v_mul_f32_e32 v187, v55, v187\n" \
"v_mul_f32_e32 v188, v55, v188\n" \
"v_mul_f32_e32 v189, v55, v189\n" \
"v_mul_f32_e32 v190, v55, v190\n" \
"v_mul_f32_e32 v191, v55, v191\n" \
"v_cvt_pk_bf16_f32 v100, v184, v185\n" \
"v_cvt_pk_bf16_f32 v101, v186, v187\n" \
"v_cvt_pk_bf16_f32 v102, v188, v189\n" \
"v_cvt_pk_bf16_f32 v103, v190, v191\n" \
"buffer_store_dwordx4 v[100:103], v20, s[56:59], s69 offen offset:3072 sc1\n" \
"s_nop 0\n" \
"v_mfma_f32_16x16x32_bf16 a[208:211], a[216:219], v[100:103], a[208:211]\n" \
"s_nop 7\n" \
"v_accvgpr_read_b32 v63, a208\n" \
"v_cmp_u_f32_e32 vcc, v63, v63\n" \
"s_nop 0\n" \
"s_or_b64 vcc, s[72:73], vcc\n" \
"s_cbranch_scc1 L_end_%=\n" \
"L_out_%=:\n" \
"L_pf20:\n" \
"s_cbranch_execz L_retA\n" \
"s_nop 0\n" \
"v_add_u32_e32 v34, 0x20000, v10\n" \
"v_mov_b32_e32 v39, s62\n" \
"v_readlane_b32 s70, v11, 4\n" \
"v_bfe_u32 v38, v20, 2, 4\n" \
"v_readlane_b32 s64, v11, 1\n" \
"v_readlane_b32 s71, v11, 3\n" \
"v_add_u32_e32 v33, 0xfc00, v10\n" \
"v_readlane_b32 s99, v11, 19\n" \
"v_readlane_b32 s80, v11, 9\n" \
"v_readlane_b32 s81, v11, 10\n" \
"s_mov_b32 s82, 0x47fca0\n" \
"s_mov_b32 s83, 0x20000\n" \
"ds_read_b64_tr_b16 a[0:1], v34 offset:0\n" \
"ds_read_b64_tr_b16 a[2:3], v34 offset:4608\n" \
"ds_read_b64_tr_b16 a[4:5], v34 offset:9216\n" \
"ds_read_b64_tr_b16 a[6:7], v34 offset:13824\n" \
"ds_read_b64_tr_b16 a[8:9], v34 offset:18432\n" \
"ds_read_b64_tr_b16 a[10:11], v34 offset:23040\n" \
"ds_read_b64_tr_b16 a[12:13], v34 offset:32\n" \
"ds_read_b64_tr_b16 a[14:15], v34 offset:4640\n" \
"ds_read_b64_tr_b16 a[16:17], v34 offset:9248\n" \
"ds_read_b64_tr_b16 a[18:19], v34 offset:13856\n" \
"buffer_load_dword v37, v38, s[56:59], s64 offen sc1\n" \
"ds_read_b64_tr_b16 a[20:21], v34 offset:18464\n" \
"ds_read_b64_tr_b16 a[22:23], v34 offset:23072\n" \
"ds_read_b64_tr_b16 a[24:25], v34 offset:64\n" \
"ds_read_b64_tr_b16 a[26:27], v34 offset:4672\n" \
"ds_read_b64_tr_b16 a[28:29], v34 offset:9280\n" \
"ds_read_b64_tr_b16 a[30:31], v34 offset:13888\n" \
"ds_read_b64_tr_b16 a[192:193], v34 offset:18496\n" \
"ds_read_b64_tr_b16 a[194:195], v34 offset:23104\n" \
"ds_read_b64_tr_b16 a[196:197], v34 offset:96\n" \
"ds_read_b64_tr_b16 a[198:199], v34 offset:4704\n" \
"ds_read_b64_tr_b16 a[200:201], v34 offset:9312\n" \
"ds_read_b64_tr_b16 a[202:203], v34 offset:13920\n" \
"ds_read_b64_tr_b16 a[204:205], v34 offset:18528\n" \
"ds_read_b64_tr_b16 a[206:207], v34 offset:23136\n" \
"ds_read_b64_tr_b16 a[208:209], v34 offset:128\n" \
"ds_read_b64_tr_b16 a[210:211], v34 offset:4736\n" \
"ds_read_b64_tr_b16 a[212:213], v34 offset:9344\n" \
"ds_read_b64_tr_b16 a[214:215], v34 offset:13952\n" \
"ds_read_b64_tr_b16 a[216:217], v34 offset:18560\n" \
"ds_read_b64_tr_b16 a[218:219], v34 offset:23168\n" \
"v_readlane_b32 s65, v11, 23\n" \
"s_add_u32 m0, s99, 0x6c00\n" \
"v_readlane_b32 s66, v11, 26\n" \
"v_add_u32_e32 v32, 0x49000, v20\n" \
"v_lshrrev_b32_e32 v44, 9, v20\n" \
"v_mul_u32_u24_e32 v44, 0xb40, v44\n" \
"v_add_u32_e32 v35, v44, v10\n" \
"v_readlane_b32 s67, v11, 24\n" \
"v_readlane_b32 s92, v11, 20\n" \
"v_readlane_b32 s73, v11, 25\n" \
"s_waitcnt vmcnt(18) lgkmcnt(15)\n" \
"v_mfma_f32_16x16x32_bf16 v[112:115], a[0:3], a[220:223], 0\n" \
"buffer_load_dwordx4 v0, s[80:83], s65 offen offset:0 lds\n" \
"v_mfma_f32_16x16x32_bf16 v[112:115], a[4:7], a[224:227], v[112:115]\n" \
"v_readlane_b32 s94, v11, 21\n" \
"v_readlane_b32 s84, v11, 7\n" \
"v_mfma_f32_16x16x32_bf16 v[112:115], a[8:11], a[228:231], v[112:115]\n" \
"v_readlane_b32 s85, v11, 8\n" \
"s_mov_b32 s86, 0xa1ca0\n" \
"v_mfma_f32_16x16x32_bf16 v[116:119], a[8:11], a[244:247], 0\n" \
"s_mov_b32 s87, 0x20000\n" \
"v_readlane_b32 s68, v11, 27\n" \
"v_mfma_f32_16x16x32_bf16 v[116:119], a[4:7], a[240:243], v[116:119]\n" \
"v_readlane_b32 s69, v11, 28\n" \
"v_readlane_b32 s72, v11, 30\n" \
"v_mfma_f32_16x16x32_bf16 v[116:119], a[0:3], a[236:239], v[116:119]\n" \
"buffer_load_dwordx4 v1, s[80:83], s65 offen offset:1024 lds\n" \
"v_mfma_f32_16x16x32_bf16 v[184:187], a[0:3], v[64:67], 0\n" \
"v_readlane_b32 s93, v11, 31\n" \
"v_lshlrev_b32_e32 v41, 12, v20\n" \
"v_mfma_f32_16x16x32_bf16 v[184:187], a[4:7], v[68:71], v[184:187]\n" \
"v_readlane_b32 s63, v11, 37\n" \
"v_bfe_u32 v40, v20, 2, 7\n" \
"v_mfma_f32_16x16x32_bf16 v[184:187], a[8:11], v[72:75], v[184:187]\n" \
"s_add_u32 s74, s63, 0x3800\n" \
"v_readlane_b32 s95, v11, 34\n" \
"v_mfma_f32_16x16x32_bf16 v[188:191], a[8:11], v[96:99], 0\n" \
"ds_read_b64_tr_b16 a[8:9], v34 offset:18592\n" \
"ds_read_b64_tr_b16 a[10:11], v34 offset:23200\n" \
"v_mfma_f32_16x16x32_bf16 v[188:191], a[4:7], v[84:87], v[188:191]\n" \
"ds_read_b64_tr_b16 a[4:5], v34 offset:9376\n" \
"ds_read_b64_tr_b16 a[6:7], v34 offset:13984\n" \
"v_mfma_f32_16x16x32_bf16 v[188:191], a[0:3], v[80:83], v[188:191]\n" \
"ds_read_b64_tr_b16 a[0:1], v34 offset:160\n" \
"ds_read_b64_tr_b16 a[2:3], v34 offset:4768\n" \
"v_mfma_f32_16x16x32_bf16 v[120:123], a[12:15], a[220:223], 0\n" \
"buffer_load_dwordx4 v2, s[80:83], s65 offen offset:2048 lds\n" \
"v_mfma_f32_16x16x32_bf16 v[120:123], a[16:19], a[224:227], v[120:123]\n" \
"v_readlane_b32 s97, v11, 36\n" \
"v_bfe_u32 v44, v20, 4, 4\n" \
"v_mfma_f32_16x16x32_bf16 v[120:123], a[20:23], a[228:231], v[120:123]\n" \
"v_bfe_u32 v45, v20, 8, 2\n" \
"v_mul_u32_u24_e32 v42, 0x480, v44\n" \
"v_mfma_f32_16x16x32_bf16 v[124:127], a[20:23], a[244:247], 0\n" \
"v_lshl_add_u32 v42, v45, 3, v42\n" \
"v_and_b32_e32 v43, 1, v45\n" \
"v_mfma_f32_16x16x32_bf16 v[124:127], a[16:19], a[240:243], v[124:127]\n" \
"v_lshrrev_b32_e32 v44, 1, v44\n" \
"v_lshlrev_b32_e32 v45, 1, v45\n" \
"v_mfma_f32_16x16x32_bf16 v[124:127], a[12:15], a[236:239], v[124:127]\n" \
"buffer_load_dwordx4 v3, s[80:83], s65 offen offset:3072 lds\n" \
"v_mfma_f32_16x16x32_bf16 v[192:195], a[12:15], v[64:67], 0\n" \
"s_add_u32 m0, s99, 0x7c00\n" \
"v_sub_u32_e32 v44, v44, v45\n" \
"v_mfma_f32_16x16x32_bf16 v[192:195], a[16:19], v[68:71], v[192:195]\n" \
"v_and_b32_e32 v45, 16, v20\n" \
"v_mov_b32_e32 v46, 0x3f80\n" \
"v_mfma_f32_16x16x32_bf16 v[192:195], a[20:23], v[72:75], v[192:195]\n" \
"v_lshlrev_b32_e32 v45, v45, v46\n" \
"v_mov_b32_e32 v46, s62\n" \
"v_mfma_f32_16x16x32_bf16 v[196:199], a[20:23], v[96:99], 0\n" \
"ds_read_b64_tr_b16 a[20:21], v34 offset:18624\n" \
"ds_read_b64_tr_b16 a[22:23], v34 offset:23232\n" \
"v_mfma_f32_16x16x32_bf16 v[196:199], a[16:19], v[84:87], v[196:199]\n" \
"ds_read_b64_tr_b16 a[16:17], v34 offset:9408\n" \
"ds_read_b64_tr_b16 a[18:19], v34 offset:14016\n" \
"v_mfma_f32_16x16x32_bf16 v[196:199], a[12:15], v[80:83], v[196:199]\n" \
"ds_read_b64_tr_b16 a[12:13], v34 offset:192\n" \
"ds_read_b64_tr_b16 a[14:15], v34 offset:4800\n" \
"v_mfma_f32_16x16x32_bf16 v[128:131], a[24:27], a[220:223], 0\n" \
"v_readlane_b32 s96, v11, 35\n" \
"s_waitcnt lgkmcnt(8)\n" \
"v_mfma_f32_16x16x32_bf16 v[128:131], a[28:31], a[224:227], v[128:131]\n" \
"buffer_load_dwordx4 v4, s[80:83], s65 offen offset:0 lds\n" \
"v_mfma_f32_16x16x32_bf16 v[128:131], a[192:195], a[228:231], v[128:131]\n" \
"v_readlane_b32 s100, v11, 6\n" \
"v_readlane_b32 s75, v11, 38\n" \
"v_mfma_f32_16x16x32_bf16 v[132:135], a[192:195], a[244:247], 0\n" \
"v_add_u32_e32 v54, 0x3000, v20\n" \
"v_mov_b32_e32 v52, 0\n" \
"v_mfma_f32_16x16x32_bf16 v[132:135], a[28:31], a[240:243], v[132:135]\n" \
"v_mov_b32_e32 v53, 0\n" \
"v_mov_b32_e32 v48, 0\n" \
"v_mfma_f32_16x16x32_bf16 v[132:135], a[24:27], a[236:239], v[132:135]\n" \
"v_mov_b32_e32 v49, 0\n" \
"v_mul_u32_u24_e32 v43, 0x8ff8, v43\n" \
"v_mfma_f32_16x16x32_bf16 v[200:203], a[24:27], v[64:67], 0\n" \
"buffer_load_dwordx4 v5, s[80:83], s65 offen offset:1024 lds\n" \
"v_mfma_f32_16x16x32_bf16 v[200:203], a[28:31], v[68:71], v[200:203]\n" \
"v_add_u32_e32 v42, v42, v43\n" \
"v_readlane_b32 s88, v11, 11\n" \
"v_mfma_f32_16x16x32_bf16 v[200:203], a[192:195], v[72:75], v[200:203]\n" \
"v_readlane_b32 s89, v11, 12\n" \
"s_mov_b32 s90, 0x240000\n" \
"v_mfma_f32_16x16x32_bf16 v[204:207], a[192:195], v[96:99], 0\n" \
"ds_read_b64_tr_b16 a[192:193], v34 offset:18656\n" \
"ds_read_b64_tr_b16 a[194:195], v34 offset:23264\n" \
"v_mfma_f32_16x16x32_bf16 v[204:207], a[28:31], v[84:87], v[204:207]\n" \
"ds_read_b64_tr_b16 a[28:29], v34 offset:9440\n" \
"ds_read_b64_tr_b16 a[30:31], v34 offset:14048\n" \
"v_mfma_f32_16x16x32_bf16 v[204:207], a[24:27], v[80:83], v[204:207]\n" \
"ds_read_b64_tr_b16 a[24:25], v34 offset:224\n" \
"ds_read_b64_tr_b16 a[26:27], v34 offset:4832\n" \
"v_mfma_f32_16x16x32_bf16 v[136:139], a[196:199], a[220:223], 0\n" \
"buffer_load_dwordx4 v6, s[80:83], s65 offen offset:2048 lds\n" \
"v_mfma_f32_16x16x32_bf16 v[136:139], a[200:203], a[224:227], v[136:139]\n" \
"s_mov_b32 s91, 0x20000\n" \
"v_mfma_f32_16x16x32_bf16 v[136:139], a[204:207], a[228:231], v[136:139]\n" \
"v_mfma_f32_16x16x32_bf16 v[140:143], a[204:207], a[244:247], 0\n" \
"v_mfma_f32_16x16x32_bf16 v[140:143], a[200:203], a[240:243], v[140:143]\n" \
"v_mfma_f32_16x16x32_bf16 v[140:143], a[196:199], a[236:239], v[140:143]\n" \
"buffer_load_dwordx4 v7, s[80:83], s65 offen offset:3072 lds\n" \
"v_mfma_f32_16x16x32_bf16 v[208:211], a[196:199], v[64:67], 0\n" \
"s_add_u32 m0, s99, 0x8c00\n" \
"v_mfma_f32_16x16x32_bf16 v[208:211], a[200:203], v[68:71], v[208:211]\n" \
"v_mfma_f32_16x16x32_bf16 v[208:211], a[204:207], v[72:75], v[208:211]\n" \
"v_mfma_f32_16x16x32_bf16 v[212:215], a[204:207], v[96:99], 0\n" \
"ds_read_b64_tr_b16 a[204:205], v34 offset:18688\n" \
"ds_read_b64_tr_b16 a[206:207], v34 offset:23296\n" \
"v_mfma_f32_16x16x32_bf16 v[212:215], a[200:203], v[84:87], v[212:215]\n" \
"ds_read_b64_tr_b16 a[200:201], v34 offset:9472\n" \
"ds_read_b64_tr_b16 a[202:203], v34 offset:14080\n" \
"v_mfma_f32_16x16x32_bf16 v[212:215], a[196:199], v[80:83], v[212:215]\n" \
"ds_read_b64_tr_b16 a[196:197], v34 offset:256\n" \
"ds_read_b64_tr_b16 a[198:199], v34 offset:4864\n" \
"v_mfma_f32_16x16x32_bf16 v[144:147], a[208:211], a[220:223], 0\n" \
"buffer_load_dwordx4 v8, s[80:83], s65 offen offset:0 lds\n" \
"v_mfma_f32_16x16x32_bf16 v[144:147], a[212:215], a[224:227], v[144:147]\n" \
"s_waitcnt vmcnt(9)\n" \
"v_cmp_ne_u32_e32 vcc, s62, v37\n" \
"v_mfma_f32_16x16x32_bf16 v[144:147], a[216:219], a[228:231], v[144:147]\n" \
"s_add_u32 m0, s99, 0x14400\n" \
"v_readlane_b32 s65, v11, 42\n" \
"v_mfma_f32_16x16x32_bf16 v[148:151], a[216:219], a[244:247], 0\n" \
"v_mfma_f32_16x16x32_bf16 v[148:151], a[212:215], a[240:243], v[148:151]\n" \
"s_cbranch_vccnz L_sa_%=\n" \
"L_ra_%=:\n" \
"v_readlane_b32 s64, v11, 29\n" \
"v_mfma_f32_16x16x32_bf16 v[148:151], a[208:211], a[236:239], v[148:151]\n" \
"buffer_load_dwordx4 a[64:67], v20, s[56:59], s66 offen offset:0 sc1\n" \
"v_mfma_f32_16x16x32_bf16 v[216:219], a[208:211], v[64:67], 0\n" \
"v_mfma_f32_16x16x32_bf16 v[216:219], a[212:215], v[68:71], v[216:219]\n" \
"v_mfma_f32_16x16x32_bf16 v[216:219], a[216:219], v[72:75], v[216:219]\n" \
"v_mfma_f32_16x16x32_bf16 v[220:223], a[216:219], v[96:99], 0\n" \
"v_mfma_f32_16x16x32_bf16 v[220:223], a[212:215], v[84:87], v[220:223]\n" \
"buffer_load_dwordx4 a[68:71], v20, s[56:59], s66 offen offset:1024 sc1\n" \
"v_mfma_f32_16x16x32_bf16 v[220:223], a[208:211], v[80:83], v[220:223]\n" \
"s_waitcnt lgkmcnt(5)\n" \
"v_mfma_f32_16x16x32_bf16 v[152:155], a[0:3], a[220:223], 0\n" \
"v_mfma_f32_16x16x32_bf16 v[152:155], a[4:7], a[224:227], v[152:155]\n" \
"v_mfma_f32_16x16x32_bf16 v[152:155], a[8:11], a[228:231], v[152:155]\n" \
"v_mfma_f32_16x16x32_bf16 v[156:159], a[8:11], a[244:247], 0\n" \
"buffer_load_dwordx4 a[84:87], v32, s[56:59], s66 offen offset:1024 sc1\n" \
"v_mfma_f32_16x16x32_bf16 v[156:159], a[4:7], a[240:243], v[156:159]\n" \
"v_mfma_f32_16x16x32_bf16 v[156:159], a[0:3], a[236:239], v[156:159]\n" \
"v_mfma_f32_16x16x32_bf16 v[224:227], a[0:3], v[64:67], 0\n" \
"v_mfma_f32_16x16x32_bf16 v[224:227], a[4:7], v[68:71], v[224:227]\n" \
"v_mfma_f32_16x16x32_bf16 v[224:227], a[8:11], v[72:75], v[224:227]\n" \
"buffer_load_dwordx4 a[80:83], v32, s[56:59], s66 offen offset:0 sc1\n" \
"v_mfma_f32_16x16x32_bf16 v[228:231], a[8:11], v[96:99], 0\n" \
"v_mfma_f32_16x16x32_bf16 v[228:231], a[4:7], v[84:87], v[228:231]\n" \
"v_mfma_f32_16x16x32_bf16 v[228:231], a[0:3], v[80:83], v[228:231]\n" \
"v_mfma_f32_16x16x32_bf16 v[160:163], a[12:15], a[220:223], 0\n" \
"v_mfma_f32_16x16x32_bf16 v[160:163], a[16:19], a[224:227], v[160:163]\n" \
"buffer_load_dwordx4 a[72:75], v20, s[56:59], s66 offen offset:2048 sc1\n" \
"v_mfma_f32_16x16x32_bf16 v[160:163], a[20:23], a[228:231], v[160:163]\n" \
"v_mfma_f32_16x16x32_bf16 v[164:167], a[20:23], a[244:247], 0\n" \
"v_mfma_f32_16x16x32_bf16 v[164:167], a[16:19], a[240:243], v[164:167]\n" \
"v_mfma_f32_16x16x32_bf16 v[164:167], a[12:15], a[236:239], v[164:167]\n" \
"v_mfma_f32_16x16x32_bf16 v[232:235], a[12:15], v[64:67], 0\n" \
"buffer_load_dwordx4 a[76:79], v20, s[56:59], s66 offen offset:3072 sc1\n" \
"v_mfma_f32_16x16x32_bf16 v[232:235], a[16:19], v[68:71], v[232:235]\n" \
"v_mfma_f32_16x16x32_bf16 v[232:235], a[20:23], v[72:75], v[232:235]\n" \
"v_mfma_f32_16x16x32_bf16 v[236:239], a[20:23], v[96:99], 0\n" \
"v_mfma_f32_16x16x32_bf16 v[236:239], a[16:19], v[84:87], v[236:239]\n" \
"v_mfma_f32_16x16x32_bf16 v[236:239], a[12:15], v[80:83], v[236:239]\n" \
"buffer_load_dwordx4 a[92:95], v32, s[56:59], s66 offen offset:3072 sc1\n" \
"s_waitcnt lgkmcnt(0)\n" \
"s_barrier\n" \
"v_mfma_f32_16x16x32_bf16 v[168:171], a[24:27], a[220:223], 0\n" \
"ds_read_b64_tr_b16 a[208:209], v33 offset:9216\n" \
"ds_read_b64_tr_b16 a[210:211], v33 offset:13824\n" \
"v_mfma_f32_16x16x32_bf16 v[168:171], a[28:31], a[224:227], v[168:171]\n" \
"ds_read_b64_tr_b16 a[212:213], v33 offset:9248\n" \
"ds_read_b64_tr_b16 a[214:215], v33 offset:13856\n" \
"v_mfma_f32_16x16x32_bf16 v[168:171], a[192:195], a[228:231], v[168:171]\n" \
"ds_read_b64_tr_b16 a[216:217], v33 offset:9280\n" \
"ds_read_b64_tr_b16 a[218:219], v33 offset:13888\n" \
"s_mov_b64 exec, 1\n" \
"buffer_store_dword v39, off, s[56:59], s70 offset:0 sc1\n" \
"s_mov_b64 exec, -1\n" \
"v_mfma_f32_16x16x32_bf16 v[172:175], a[192:195], a[244:247], 0\n" \
"ds_read_b64_tr_b16 a[0:1], v33 offset:9312\n" \
"ds_read_b64_tr_b16 a[2:3], v33 offset:13920\n" \
"v_mfma_f32_16x16x32_bf16 v[172:175], a[28:31], a[240:243], v[172:175]\n" \
"ds_read_b64_tr_b16 a[4:5], v33 offset:9344\n" \
"ds_read_b64_tr_b16 a[6:7], v33 offset:13952\n" \
"v_mfma_f32_16x16x32_bf16 v[172:175], a[24:27], a[236:239], v[172:175]\n" \
"ds_read_b64_tr_b16 a[8:9], v33 offset:9376\n" \
"ds_read_b64_tr_b16 a[10:11], v33 offset:13984\n" \
"v_mfma_f32_16x16x32_bf16 v[240:243], a[24:27], v[64:67], 0\n" \
"ds_read_b64_tr_b16 a[12:13], v33 offset:9408\n" \
"ds_read_b64_tr_b16 a[14:15], v33 offset:14016\n" \
"v_mfma_f32_16x16x32_bf16 v[240:243], a[28:31], v[68:71], v[240:243]\n" \
"ds_read_b64_tr_b16 a[16:17], v33 offset:9440\n" \
"ds_read_b64_tr_b16 a[18:19], v33 offset:14048\n" \
"v_mfma_f32_16x16x32_bf16 v[240:243], a[192:195], v[72:75], v[240:243]\n" \
"ds_read_b64_tr_b16 a[20:21], v33 offset:9472\n" \
"ds_read_b64_tr_b16 a[22:23], v33 offset:14080\n" \
"v_mfma_f32_16x16x32_bf16 v[244:247], a[192:195], v[96:99], 0\n" \
"ds_read_b64_tr_b16 a[192:193], v35 offset:32\n" \
"ds_read_b64_tr_b16 a[194:195], v35 offset:2336\n" \
"v_mfma_f32_16x16x32_bf16 v[244:247], a[28:31], v[84:87], v[244:247]\n" \
"ds_read_b64_tr_b16 a[28:29], v35 offset:10368\n" \
"ds_read_b64_tr_b16 a[30:31], v35 offset:12672\n" \
"v_mfma_f32_16x16x32_bf16 v[244:247], a[24:27], v[80:83], v[244:247]\n" \
"ds_read_b64_tr_b16 a[24:25], v35 offset:0\n" \
"ds_read_b64_tr_b16 a[26:27], v35 offset:2304\n" \
"v_mfma_f32_16x16x32_bf16 v[176:179], a[196:199], a[220:223], 0\n" \
"buffer_load_dwordx4 a[88:91], v32, s[56:59], s66 offen offset:2048 sc1\n" \
"v_mfma_f32_16x16x32_bf16 v[176:179], a[200:203], a[224:227], v[176:179]\n" \
"v_readlane_b32 s66, v11, 43\n" \
"v_readlane_b32 s70, v11, 5\n" \
"v_mfma_f32_16x16x32_bf16 v[176:179], a[204:207], a[228:231], v[176:179]\n" \
"v_mfma_f32_16x16x32_bf16 v[180:183], a[204:207], a[244:247], 0\n" \
"L_pf21:\n" \
"s_cbranch_execz L_pf23\n" \
"s_nop 0\n" \
"v_mfma_f32_16x16x32_bf16 v[180:183], a[200:203], a[240:243], v[180:183]\n" \
"v_mfma_f32_16x16x32_bf16 v[180:183], a[196:199], a[236:239], v[180:183]\n" \
"buffer_load_dwordx4 v0, s[80:83], s67 offen offset:0 lds\n" \
"v_mfma_f32_16x16x32_bf16 v[248:251], a[196:199], v[64:67], 0\n" \
"v_mfma_f32_16x16x32_bf16 v[248:251], a[200:203], v[68:71], v[248:251]\n" \
"v_mfma_f32_16x16x32_bf16 v[248:251], a[204:207], v[72:75], v[248:251]\n" \
"v_mfma_f32_16x16x32_bf16 v[252:255], a[204:207], v[96:99], 0\n" \
"ds_read_b64_tr_b16 a[204:205], v35 offset:10432\n" \
"ds_read_b64_tr_b16 a[206:207], v35 offset:12736\n" \
"v_mfma_f32_16x16x32_bf16 v[252:255], a[200:203], v[84:87], v[252:255]\n" \
"ds_read_b64_tr_b16 a[200:201], v35 offset:64\n" \
"ds_read_b64_tr_b16 a[202:203], v35 offset:2368\n" \
"v_mfma_f32_16x16x32_bf16 v[252:255], a[196:199], v[80:83], v[252:255]\n" \
"ds_read_b64_tr_b16 a[196:197], v35 offset:10400\n" \
"s_waitcnt lgkmcnt(9)\n" \
"v_mfma_f32_16x16x32_bf16 v[112:115], a[208:211], a[232:235], v[112:115]\n" \
"buffer_load_dwordx4 v1, s[80:83], s67 offen offset:1024 lds\n" \
"v_mfma_f32_16x16x32_bf16 v[116:119], a[208:211], a[248:251], v[116:119]\n" \
"ds_read_b64_tr_b16 a[198:199], v35 offset:12704\n" \
"v_mfma_f32_16x16x32_bf16 v[184:187], a[208:211], v[76:79], v[184:187]\n" \
"v_mfma_f32_16x16x32_bf16 v[188:191], a[208:211], v[100:103], v[188:191]\n" \
"ds_read_b64_tr_b16 a[208:209], v35 offset:96\n" \
"ds_read_b64_tr_b16 a[210:211], v35 offset:2400\n" \
"v_mfma_f32_16x16x32_bf16 v[120:123], a[212:215], a[232:235], v[120:123]\n" \
"v_mfma_f32_16x16x32_bf16 v[124:127], a[212:215], a[248:251], v[124:127]\n" \
"buffer_load_dwordx4 v2, s[80:83], s67 offen offset:2048 lds\n" \
"v_mfma_f32_16x16x32_bf16 v[192:195], a[212:215], v[76:79], v[192:195]\n" \
"v_mfma_f32_16x16x32_bf16 v[196:199], a[212:215], v[100:103], v[196:199]\n" \
"ds_read_b64_tr_b16 a[212:213], v35 offset:10464\n" \
"ds_read_b64_tr_b16 a[214:215], v35 offset:12768\n" \
"v_mfma_f32_16x16x32_bf16 v[128:131], a[216:219], a[232:235], v[128:131]\n" \
"v_mfma_f32_16x16x32_bf16 v[132:135], a[216:219], a[248:251], v[132:135]\n" \
"v_mfma_f32_16x16x32_bf16 v[200:203], a[216:219], v[76:79], v[200:203]\n" \
"buffer_load_dwordx4 v3, s[80:83], s67 offen offset:3072 lds\n" \
"v_mfma_f32_16x16x32_bf16 v[204:207], a[216:219], v[100:103], v[204:207]\n" \
"ds_read_b64_tr_b16 a[216:217], v35 offset:128\n" \
"ds_read_b64_tr_b16 a[218:219], v35 offset:2432\n" \
"v_mfma_f32_16x16x32_bf16 v[136:139], a[0:3], a[232:235], v[136:139]\n" \
"s_add_u32 m0, s99, 0x15400\n" \
"v_mfma_f32_16x16x32_bf16 v[140:143], a[0:3], a[248:251], v[140:143]\n" \
"v_mfma_f32_16x16x32_bf16 v[208:211], a[0:3], v[76:79], v[208:211]\n" \
"v_mfma_f32_16x16x32_bf16 v[212:215], a[0:3], v[100:103], v[212:215]\n" \
"ds_read_b64_tr_b16 a[0:1], v35 offset:10496\n" \
"ds_read_b64_tr_b16 a[2:3], v35 offset:12800\n" \
"v_mfma_f32_16x16x32_bf16 v[144:147], a[4:7], a[232:235], v[144:147]\n" \
"buffer_load_dwordx4 v4, s[80:83], s67 offen offset:0 lds\n" \
"v_mfma_f32_16x16x32_bf16 v[148:151], a[4:7], a[248:251], v[148:151]\n" \
"v_mfma_f32_16x16x32_bf16 v[216:219], a[4:7], v[76:79], v[216:219]\n" \
"v_mfma_f32_16x16x32_bf16 v[220:223], a[4:7], v[100:103], v[220:223]\n" \
"ds_read_b64_tr_b16 a[4:5], v35 offset:160\n" \
"ds_read_b64_tr_b16 a[6:7], v35 offset:2464\n" \
"v_mfma_f32_16x16x32_bf16 v[152:155], a[8:11], a[232:235], v[152:155]\n" \
"v_mfma_f32_16x16x32_bf16 v[156:159], a[8:11], a[248:251], v[156:159]\n" \
"buffer_load_dwordx4 v5, s[80:83], s67 offen offset:1024 lds\n" \
"v_mfma_f32_16x16x32_bf16 v[224:227], a[8:11], v[76:79], v[224:227]\n" \
"v_mfma_f32_16x16x32_bf16 v[228:231], a[8:11], v[100:103], v[228:231]\n" \
"ds_read_b64_tr_b16 a[8:9], v35 offset:10528\n" \
"ds_read_b64_tr_b16 a[10:11], v35 offset:12832\n" \
"v_mfma_f32_16x16x32_bf16 v[160:163], a[12:15], a[232:235], v[160:163]\n" \
"v_mfma_f32_16x16x32_bf16 v[164:167], a[12:15], a[248:251], v[164:167]\n" \
"v_mfma_f32_16x16x32_bf16 v[232:235], a[12:15], v[76:79], v[232:235]\n" \
"buffer_load_dwordx4 v6, s[80:83], s67 offen offset:2048 lds\n" \
"v_mfma_f32_16x16x32_bf16 v[236:239], a[12:15], v[100:103], v[236:239]\n" \
"ds_read_b64_tr_b16 a[12:13], v35 offset:192\n" \
"ds_read_b64_tr_b16 a[14:15], v35 offset:2496\n" \
"v_mfma_f32_16x16x32_bf16 v[168:171], a[16:19], a[232:235], v[168:171]\n" \
"v_mfma_f32_16x16x32_bf16 v[172:175], a[16:19], a[248:251], v[172:175]\n" \
"v_mfma_f32_16x16x32_bf16 v[240:243], a[16:19], v[76:79], v[240:243]\n" \
"v_mfma_f32_16x16x32_bf16 v[244:247], a[16:19], v[100:103], v[244:247]\n" \
"buffer_load_dwordx4 v7, s[80:83], s67 offen offset:3072 lds\n" \
"v_mfma_f32_16x16x32_bf16 v[176:179], a[20:23], a[232:235], v[176:179]\n" \
"s_add_u32 m0, s99, 0x16400\n" \
"ds_read_b64_tr_b16 a[16:17], v35 offset:10560\n" \
"v_mfma_f32_16x16x32_bf16 v[180:183], a[20:23], a[248:251], v[180:183]\n" \
"ds_read_b64_tr_b16 a[18:19], v35 offset:12864\n" \
"v_mfma_f32_16x16x32_bf16 v[248:251], a[20:23], v[76:79], v[248:251]\n" \
"v_mfma_f32_16x16x32_bf16 v[252:255], a[20:23], v[100:103], v[252:255]\n" \
"ds_read_b64_tr_b16 a[20:21], v35 offset:224\n" \
"s_waitcnt lgkmcnt(7)\n" \
"v_mfma_f32_16x16x32_bf16 v[112:115], a[24:27], a[32:35], v[112:115]\n" \
"buffer_load_dwordx4 v8, s[80:83], s67 offen offset:0 lds\n" \
"v_mfma_f32_16x16x32_bf16 v[112:115], a[28:31], a[36:39], v[112:115]\n" \
"s_add_u32 m0, s92, 0x0\n" \
"ds_read_b64_tr_b16 a[22:23], v35 offset:2528\n" \
"v_mfma_f32_16x16x32_bf16 v[116:119], a[28:31], a[44:47], v[116:119]\n" \
"v_readlane_b32 s67, v11, 44\n" \
"v_mfma_f32_16x16x32_bf16 v[116:119], a[24:27], a[40:43], v[116:119]\n" \
"v_mfma_f32_16x16x32_bf16 v[184:187], a[24:27], a[48:51], v[184:187]\n" \
"v_mfma_f32_16x16x32_bf16 v[184:187], a[28:31], a[52:55], v[184:187]\n" \
"buffer_load_dword v47, v38, s[56:59], s71 offen sc1\n" \
"v_mfma_f32_16x16x32_bf16 v[188:191], a[28:31], a[60:63], v[188:191]\n" \
"ds_read_b64_tr_b16 a[28:29], v35 offset:256\n" \
"ds_read_b64_tr_b16 a[30:31], v35 offset:2560\n" \
"v_mfma_f32_16x16x32_bf16 v[188:191], a[24:27], a[56:59], v[188:191]\n" \
"ds_read_b64_tr_b16 a[24:25], v35 offset:10592\n" \
"ds_read_b64_tr_b16 a[26:27], v35 offset:12896\n" \
"v_mfma_f32_16x16x32_bf16 v[120:123], a[192:195], a[32:35], v[120:123]\n" \
"v_mfma_f32_16x16x32_bf16 v[120:123], a[196:199], a[36:39], v[120:123]\n" \
"v_mfma_f32_16x16x32_bf16 v[124:127], a[196:199], a[44:47], v[124:127]\n" \
"buffer_load_dwordx4 v0, s[80:83], s73 offen offset:0 lds\n" \
"v_mfma_f32_16x16x32_bf16 v[124:127], a[192:195], a[40:43], v[124:127]\n" \
"v_mfma_f32_16x16x32_bf16 v[192:195], a[192:195], a[48:51], v[192:195]\n" \
"v_mfma_f32_16x16x32_bf16 v[192:195], a[196:199], a[52:55], v[192:195]\n" \
"v_mfma_f32_16x16x32_bf16 v[196:199], a[196:199], a[60:63], v[196:199]\n" \
"v_mfma_f32_16x16x32_bf16 v[196:199], a[192:195], a[56:59], v[196:199]\n" \
"buffer_load_dwordx4 v1, s[80:83], s73 offen offset:1024 lds\n" \
"v_mfma_f32_16x16x32_bf16 v[128:131], a[200:203], a[32:35], v[128:131]\n" \
"ds_read_b64_tr_b16 a[192:193], v35 offset:10624\n" \
"ds_read_b64_tr_b16 a[194:195], v35 offset:12928\n" \
"v_mfma_f32_16x16x32_bf16 v[128:131], a[204:207], a[36:39], v[128:131]\n" \
"v_mfma_f32_16x16x32_bf16 v[132:135], a[204:207], a[44:47], v[132:135]\n" \
"v_mfma_f32_16x16x32_bf16 v[132:135], a[200:203], a[40:43], v[132:135]\n" \
"v_mfma_f32_16x16x32_bf16 v[200:203], a[200:203], a[48:51], v[200:203]\n" \
"buffer_load_dwordx4 v2, s[80:83], s73 offen offset:2048 lds\n" \
"v_mfma_f32_16x16x32_bf16 v[200:203], a[204:207], a[52:55], v[200:203]\n" \
"v_mfma_f32_16x16x32_bf16 v[204:207], a[204:207], a[60:63], v[204:207]\n" \
"v_mfma_f32_16x16x32_bf16 v[204:207], a[200:203], a[56:59], v[204:207]\n" \
"v_mfma_f32_16x16x32_bf16 v[136:139], a[208:211], a[32:35], v[136:139]\n" \
"v_mfma_f32_16x16x32_bf16 v[136:139], a[212:215], a[36:39], v[136:139]\n" \
"buffer_load_dwordx4 v3, s[80:83], s73 offen offset:3072 lds\n" \
"v_mfma_f32_16x16x32_bf16 v[140:143], a[212:215], a[44:47], v[140:143]\n" \
"s_add_u32 m0, s92, 0x1000\n" \
"v_mfma_f32_16x16x32_bf16 v[140:143], a[208:211], a[40:43], v[140:143]\n" \
"v_mfma_f32_16x16x32_bf16 v[208:211], a[208:211], a[48:51], v[208:211]\n" \
"v_mfma_f32_16x16x32_bf16 v[208:211], a[212:215], a[52:55], v[208:211]\n" \
"v_mfma_f32_16x16x32_bf16 v[212:215], a[212:215], a[60:63], v[212:215]\n" \
"buffer_load_dwordx4 v4, s[80:83], s73 offen offset:0 lds\n" \
"v_mfma_f32_16x16x32_bf16 v[212:215], a[208:211], a[56:59], v[212:215]\n" \
"v_mfma_f32_16x16x32_bf16 v[144:147], a[216:219], a[32:35], v[144:147]\n" \
"v_mfma_f32_16x16x32_bf16 v[144:147], a[0:3], a[36:39], v[144:147]\n" \
"v_mfma_f32_16x16x32_bf16 v[148:151], a[0:3], a[44:47], v[148:151]\n" \
"v_mfma_f32_16x16x32_bf16 v[148:151], a[216:219], a[40:43], v[148:151]\n" \
"buffer_load_dwordx4 v5, s[80:83], s73 offen offset:1024 lds\n" \
"v_mfma_f32_16x16x32_bf16 v[216:219], a[216:219], a[48:51], v[216:219]\n" \
"v_mfma_f32_16x16x32_bf16 v[216:219], a[0:3], a[52:55], v[216:219]\n" \
"v_mfma_f32_16x16x32_bf16 v[220:223], a[0:3], a[60:63], v[220:223]\n" \
"v_mfma_f32_16x16x32_bf16 v[220:223], a[216:219], a[56:59], v[220:223]\n" \
"v_mfma_f32_16x16x32_bf16 v[152:155], a[4:7], a[32:35], v[152:155]\n" \
"buffer_load_dwordx4 v6, s[80:83], s73 offen offset:2048 lds\n" \
"s_waitcnt lgkmcnt(0)\n" \
"v_mfma_f32_16x16x32_bf16 v[152:155], a[8:11], a[36:39], v[152:155]\n" \
"v_mfma_f32_16x16x32_bf16 v[156:159], a[8:11], a[44:47], v[156:159]\n" \
"v_mfma_f32_16x16x32_bf16 v[156:159], a[4:7], a[40:43], v[156:159]\n" \
"v_mfma_f32_16x16x32_bf16 v[224:227], a[4:7], a[48:51], v[224:227]\n" \
"buffer_load_dwordx4 v7, s[80:83], s73 offen offset:3072 lds\n" \
"s_add_u32 m0, s92, 0x2000\n" \
"v_mfma_f32_16x16x32_bf16 v[224:227], a[8:11], a[52:55], v[224:227]\n" \
"v_mfma_f32_16x16x32_bf16 v[228:231], a[8:11], a[60:63], v[228:231]\n" \
"v_mfma_f32_16x16x32_bf16 v[228:231], a[4:7], a[56:59], v[228:231]\n" \
"s_waitcnt vmcnt(23)\n" \
"s_barrier\n" \
"buffer_load_dwordx4 v8, s[80:83], s73 offen offset:0 lds\n" \
"s_mov_b32 m0, s94\n" \
"v_mfma_f32_16x16x32_bf16 v[160:163], a[12:15], a[32:35], v[160:163]\n" \
"ds_read_b64_tr_b16 a[196:197], v10 offset:27648\n" \
"ds_read_b64_tr_b16 a[198:199], v10 offset:32256\n" \
"v_mfma_f32_16x16x32_bf16 v[160:163], a[16:19], a[36:39], v[160:163]\n" \
"ds_read_b64_tr_b16 a[200:201], v10 offset:36864\n" \
"ds_read_b64_tr_b16 a[202:203], v10 offset:41472\n" \
"v_mfma_f32_16x16x32_bf16 v[164:167], a[16:19], a[44:47], v[164:167]\n" \
"ds_read_b64_tr_b16 a[204:205], v10 offset:27680\n" \
"ds_read_b64_tr_b16 a[206:207], v10 offset:32288\n" \
"v_mfma_f32_16x16x32_bf16 v[164:167], a[12:15], a[40:43], v[164:167]\n" \
"buffer_load_dwordx4 v9, s[84:87], 0 offen lds\n" \
"v_mfma_f32_16x16x32_bf16 v[232:235], a[12:15], a[48:51], v[232:235]\n" \
"ds_read_b64_tr_b16 a[208:209], v10 offset:36896\n" \
"ds_read_b64_tr_b16 a[210:211], v10 offset:41504\n" \
"v_mfma_f32_16x16x32_bf16 v[232:235], a[16:19], a[52:55], v[232:235]\n" \
"ds_read_b64_tr_b16 a[212:213], v10 offset:27712\n" \
"ds_read_b64_tr_b16 a[214:215], v10 offset:32320\n" \
"v_mfma_f32_16x16x32_bf16 v[236:239], a[16:19], a[60:63], v[236:239]\n" \
"ds_read_b64_tr_b16 a[216:217], v10 offset:36928\n" \
"ds_read_b64_tr_b16 a[218:219], v10 offset:41536\n" \
"v_mfma_f32_16x16x32_bf16 v[236:239], a[12:15], a[56:59], v[236:239]\n" \
"ds_read_b64_tr_b16 a[0:1], v10 offset:27744\n" \
"ds_read_b64_tr_b16 a[2:3], v10 offset:32352\n" \
"buffer_load_dwordx4 a[128:131], v20, s[56:59], s68 offen offset:0 sc1\n" \
"v_mfma_f32_16x16x32_bf16 v[168:171], a[20:23], a[32:35], v[168:171]\n" \
"ds_read_b64_tr_b16 a[4:5], v10 offset:36960\n" \
"ds_read_b64_tr_b16 a[6:7], v10 offset:41568\n" \
"v_mfma_f32_16x16x32_bf16 v[168:171], a[24:27], a[36:39], v[168:171]\n" \
"ds_read_b64_tr_b16 a[8:9], v10 offset:27776\n" \
"ds_read_b64_tr_b16 a[10:11], v10 offset:32384\n" \
"v_mfma_f32_16x16x32_bf16 v[172:175], a[24:27], a[44:47], v[172:175]\n" \
"ds_read_b64_tr_b16 a[12:13], v10 offset:36992\n" \
"ds_read_b64_tr_b16 a[14:15], v10 offset:41600\n" \
"v_mfma_f32_16x16x32_bf16 v[172:175], a[20:23], a[40:43], v[172:175]\n" \
"ds_read_b64_tr_b16 a[16:17], v10 offset:27808\n" \
"ds_read_b64_tr_b16 a[18:19], v10 offset:32416\n" \
"buffer_load_dwordx4 a[132:135], v20, s[56:59], s68 offen offset:1024 sc1\n" \
"v_mfma_f32_16x16x32_bf16 v[240:243], a[20:23], a[48:51], v[240:243]\n" \
"s_waitcnt vmcnt(12)\n" \
"v_cmp_ne_u32_e32 vcc, s62, v47\n" \
"v_mfma_f32_16x16x32_bf16 v[240:243], a[24:27], a[52:55], v[240:243]\n" \
"v_readlane_b32 s94, v11, 47\n" \
"s_mov_b32 s84, 0\n" \
"v_mfma_f32_16x16x32_bf16 v[244:247], a[24:27], a[60:63], v[244:247]\n" \
"ds_read_b64_tr_b16 a[24:25], v10 offset:27840\n" \
"ds_read_b64_tr_b16 a[26:27], v10 offset:32448\n" \
"v_mfma_f32_16x16x32_bf16 v[244:247], a[20:23], a[56:59], v[244:247]\n" \
"ds_read_b64_tr_b16 a[20:21], v10 offset:37024\n" \
"ds_read_b64_tr_b16 a[22:23], v10 offset:41632\n" \
"buffer_load_dwordx4 a[136:139], v20, s[56:59], s68 offen offset:2048 sc1\n" \
"v_mfma_f32_16x16x32_bf16 v[176:179], a[28:31], a[32:35], v[176:179]\n" \
"s_cbranch_vccnz L_sb_%=\n" \
"L_rb_%=:\n" \
"v_readlane_b32 s71, v11, 46\n" \
"v_mfma_f32_16x16x32_bf16 v[176:179], a[192:195], a[36:39], v[176:179]\n" \
"v_add_u32_e32 v38, 0x1000, v20\n" \
"v_add_u32_e32 v47, 0x2000, v20\n" \
"v_mfma_f32_16x16x32_bf16 v[180:183], a[192:195], a[44:47], v[180:183]\n" \
"v_cmp_eq_u32_e32 vcc, 0, v44\n" \
"v_mfma_f32_16x16x32_bf16 v[180:183], a[28:31], a[40:43], v[180:183]\n" \
"v_mfma_f32_16x16x32_bf16 v[248:251], a[28:31], a[48:51], v[248:251]\n" \
"buffer_load_dwordx4 a[140:143], v20, s[56:59], s68 offen offset:3072 sc1\n" \
"v_mfma_f32_16x16x32_bf16 v[248:251], a[192:195], a[52:55], v[248:251]\n" \
"v_cndmask_b32_e32 v50, 0, v45, vcc\n" \
"v_cmp_eq_u32_e32 vcc, 1, v44\n" \
"v_mfma_f32_16x16x32_bf16 v[252:255], a[192:195], a[60:63], v[252:255]\n" \
"ds_read_b64_tr_b16 a[192:193], v10 offset:27872\n" \
"ds_read_b64_tr_b16 a[194:195], v10 offset:32480\n" \
"v_mfma_f32_16x16x32_bf16 v[252:255], a[28:31], a[56:59], v[252:255]\n" \
"ds_read_b64_tr_b16 a[28:29], v10 offset:37056\n" \
"s_waitcnt lgkmcnt(9)\n" \
"v_mfma_f32_16x16x32_bf16 v[112:115], a[196:199], a[64:67], v[112:115]\n" \
"ds_read_b64_tr_b16 a[30:31], v10 offset:41664\n" \
"v_cndmask_b32_e32 v51, 0, v45, vcc\n" \
"v_mfma_f32_16x16x32_bf16 v[112:115], a[200:203], a[68:71], v[112:115]\n" \
"buffer_load_dwordx4 a[156:159], v32, s[56:59], s68 offen offset:3072 sc1\n" \
"v_mfma_f32_16x16x32_bf16 v[116:119], a[200:203], a[84:87], v[116:119]\n" \
"ds_read_b64_tr_b16 a[200:201], v10 offset:27904\n" \
"ds_read_b64_tr_b16 a[202:203], v10 offset:32512\n" \
"v_mfma_f32_16x16x32_bf16 v[116:119], a[196:199], a[80:83], v[116:119]\n" \
"ds_read_b64_tr_b16 a[196:197], v10 offset:37088\n" \
"ds_read_b64_tr_b16 a[198:199], v10 offset:41696\n" \
"v_mfma_f32_16x16x32_bf16 v[120:123], a[204:207], a[64:67], v[120:123]\n" \
"v_bfe_u32 v45, v20, 1, 5\n" \
"v_and_b32_e32 v45, 24, v45\n" \
"v_mfma_f32_16x16x32_bf16 v[120:123], a[208:211], a[68:71], v[120:123]\n" \
"v_and_b32_e32 v44, 0x3c0, v20\n" \
"v_add_u32_e32 v45, 0x1d400, v45\n" \
"v_mfma_f32_16x16x32_bf16 v[124:127], a[208:211], a[84:87], v[124:127]\n" \
"buffer_load_dwordx4 a[152:155], v32, s[56:59], s68 offen offset:2048 sc1\n" \
"v_mfma_f32_16x16x32_bf16 v[124:127], a[204:207], a[80:83], v[124:127]\n" \
"ds_read_b64_tr_b16 a[204:205], v10 offset:37120\n" \
"ds_read_b64_tr_b16 a[206:207], v10 offset:41728\n" \
"v_mfma_f32_16x16x32_bf16 v[128:131], a[212:215], a[64:67], v[128:131]\n" \
"ds_read_b64_tr_b16 a[208:209], v10 offset:46080\n" \
"ds_read_b64_tr_b16 a[210:211], v10 offset:50688\n" \
"v_mfma_f32_16x16x32_bf16 v[128:131], a[216:219], a[68:71], v[128:131]\n" \
"v_add_u32_e32 v36, v44, v45\n" \
"v_mfma_f32_16x16x32_bf16 v[132:135], a[216:219], a[84:87], v[132:135]\n" \
"ds_read_b64_tr_b16 a[216:217], v10 offset:46112\n" \
"ds_read_b64_tr_b16 a[218:219], v10 offset:50720\n" \
"buffer_load_dwordx4 a[148:151], v32, s[56:59], s68 offen offset:1024 sc1\n" \
"v_mfma_f32_16x16x32_bf16 v[132:135], a[212:215], a[80:83], v[132:135]\n" \
"ds_read_b64_tr_b16 a[212:213], v10 offset:55296\n" \
"ds_read_b64_tr_b16 a[214:215], v10 offset:59904\n" \
"v_mfma_f32_16x16x32_bf16 v[136:139], a[0:3], a[64:67], v[136:139]\n" \
"v_mfma_f32_16x16x32_bf16 v[136:139], a[4:7], a[68:71], v[136:139]\n" \
"v_mfma_f32_16x16x32_bf16 v[140:143], a[4:7], a[84:87], v[140:143]\n" \
"ds_read_b64_tr_b16 a[4:5], v10 offset:46144\n" \
"ds_read_b64_tr_b16 a[6:7], v10 offset:50752\n" \
"buffer_load_dwordx4 a[144:147], v32, s[56:59], s68 offen offset:0 sc1\n" \
"v_mfma_f32_16x16x32_bf16 v[140:143], a[0:3], a[80:83], v[140:143]\n" \
"ds_read_b64_tr_b16 a[0:1], v10 offset:55328\n" \
"ds_read_b64_tr_b16 a[2:3], v10 offset:59936\n" \
"v_mfma_f32_16x16x32_bf16 v[144:147], a[8:11], a[64:67], v[144:147]\n" \
"v_readlane_b32 s68, v11, 45\n" \
"v_mfma_f32_16x16x32_bf16 v[144:147], a[12:15], a[68:71], v[144:147]\n" \
"v_mfma_f32_16x16x32_bf16 v[148:151], a[12:15], a[84:87], v[148:151]\n" \
"ds_read_b64_tr_b16 a[12:13], v10 offset:46176\n" \
"ds_read_b64_tr_b16 a[14:15], v10 offset:50784\n" \
"buffer_load_dwordx4 a[96:99], v20, s[56:59], s69 offen offset:0 sc1\n" \
"v_mfma_f32_16x16x32_bf16 v[148:151], a[8:11], a[80:83], v[148:151]\n" \
"ds_read_b64_tr_b16 a[8:9], v10 offset:55360\n" \
"s_waitcnt lgkmcnt(15)\n" \
"v_mfma_f32_16x16x32_bf16 v[152:155], a[16:19], a[64:67], v[152:155]\n" \
"ds_read_b64_tr_b16 a[10:11], v10 offset:59968\n" \
"v_mfma_f32_16x16x32_bf16 v[152:155], a[20:23], a[68:71], v[152:155]\n" \
"v_mfma_f32_16x16x32_bf16 v[156:159], a[20:23], a[84:87], v[156:159]\n" \
"ds_read_b64_tr_b16 a[20:21], v10 offset:46208\n" \
"ds_read_b64_tr_b16 a[22:23], v10 offset:50816\n" \
"buffer_load_dwordx4 a[100:103], v20, s[56:59], s69 offen offset:1024 sc1\n" \
"v_mfma_f32_16x16x32_bf16 v[156:159], a[16:19], a[80:83], v[156:159]\n" \
"ds_read_b64_tr_b16 a[16:17], v10 offset:55392\n" \
"ds_read_b64_tr_b16 a[18:19], v10 offset:60000\n" \
"v_mfma_f32_16x16x32_bf16 v[160:163], a[24:27], a[64:67], v[160:163]\n" \
"v_mfma_f32_16x16x32_bf16 v[160:163], a[28:31], a[68:71], v[160:163]\n" \
"v_mfma_f32_16x16x32_bf16 v[164:167], a[28:31], a[84:87], v[164:167]\n" \
"ds_read_b64_tr_b16 a[28:29], v10 offset:46240\n" \
"ds_read_b64_tr_b16 a[30:31], v10 offset:50848\n" \
"buffer_load_dwordx4 a[104:107], v20, s[56:59], s69 offen offset:2048 sc1\n" \
"v_mfma_f32_16x16x32_bf16 v[164:167], a[24:27], a[80:83], v[164:167]\n" \
"ds_read_b64_tr_b16 a[24:25], v10 offset:55424\n" \
"ds_read_b64_tr_b16 a[26:27], v10 offset:60032\n" \
"v_mfma_f32_16x16x32_bf16 v[168:171], a[192:195], a[64:67], v[168:171]\n" \
"v_mfma_f32_16x16x32_bf16 v[168:171], a[196:199], a[68:71], v[168:171]\n" \
"v_mfma_f32_16x16x32_bf16 v[172:175], a[196:199], a[84:87], v[172:175]\n" \
"ds_read_b64_tr_b16 a[196:197], v10 offset:46272\n" \
"ds_read_b64_tr_b16 a[198:199], v10 offset:50880\n" \
"buffer_load_dwordx4 a[108:111], v20, s[56:59], s69 offen offset:3072 sc1\n" \
"v_mfma_f32_16x16x32_bf16 v[172:175], a[192:195], a[80:83], v[172:175]\n" \
"ds_read_b64_tr_b16 a[192:193], v10 offset:55456\n" \
"ds_read_b64_tr_b16 a[194:195], v10 offset:60064\n" \
"v_mfma_f32_16x16x32_bf16 v[176:179], a[200:203], a[64:67], v[176:179]\n" \
"s_waitcnt lgkmcnt(13)\n" \
"v_mfma_f32_16x16x32_bf16 v[176:179], a[204:207], a[68:71], v[176:179]\n" \
"v_mfma_f32_16x16x32_bf16 v[180:183], a[204:207], a[84:87], v[180:183]\n" \
"ds_read_b64_tr_b16 a[204:205], v10 offset:46304\n" \
"ds_read_b64_tr_b16 a[206:207], v10 offset:50912\n" \
"buffer_load_dwordx4 a[124:127], v32, s[56:59], s69 offen offset:3072 sc1\n" \
"v_mfma_f32_16x16x32_bf16 v[180:183], a[200:203], a[80:83], v[180:183]\n" \
"ds_read_b64_tr_b16 a[200:201], v10 offset:55488\n" \
"ds_read_b64_tr_b16 a[202:203], v10 offset:60096\n" \
"v_mfma_f32_16x16x32_bf16 v[112:115], a[208:211], a[72:75], v[112:115]\n" \
"v_mfma_f32_16x16x32_bf16 v[112:115], a[212:215], a[76:79], v[112:115]\n" \
"v_mfma_f32_16x16x32_bf16 v[116:119], a[212:215], a[92:95], v[116:119]\n" \
"ds_read_b64_tr_b16 a[212:213], v10 offset:46336\n" \
"ds_read_b64_tr_b16 a[214:215], v10 offset:50944\n" \
"buffer_load_dwordx4 a[120:123], v32, s[56:59], s69 offen offset:2048 sc1\n" \
"v_mfma_f32_16x16x32_bf16 v[116:119], a[208:211], a[88:91], v[116:119]\n" \
"ds_read_b64_tr_b16 a[208:209], v10 offset:55520\n" \
"ds_read_b64_tr_b16 a[210:211], v10 offset:60128\n" \
"v_mfma_f32_16x16x32_bf16 v[120:123], a[216:219], a[72:75], v[120:123]\n" \
"v_mfma_f32_16x16x32_bf16 v[120:123], a[0:3], a[76:79], v[120:123]\n" \
"v_mfma_f32_16x16x32_bf16 v[124:127], a[0:3], a[92:95], v[124:127]\n" \
"v_mfma_f32_16x16x32_bf16 v[124:127], a[216:219], a[88:91], v[124:127]\n" \
"buffer_load_dwordx4 a[116:119], v32, s[56:59], s69 offen offset:1024 sc1\n" \
"v_mfma_f32_16x16x32_bf16 v[128:131], a[4:7], a[72:75], v[128:131]\n" \
"ds_read_b64_tr_b16 a[216:217], v10 offset:55552\n" \
"s_waitcnt lgkmcnt(9)\n" \
"v_mfma_f32_16x16x32_bf16 v[128:131], a[8:11], a[76:79], v[128:131]\n" \
"ds_read_b64_tr_b16 a[218:219], v10 offset:60160\n" \
"v_mfma_f32_16x16x32_bf16 v[132:135], a[8:11], a[92:95], v[132:135]\n" \
"v_mfma_f32_16x16x32_bf16 v[132:135], a[4:7], a[88:91], v[132:135]\n" \
"v_mfma_f32_16x16x32_bf16 v[136:139], a[12:15], a[72:75], v[136:139]\n" \
"buffer_load_dwordx4 a[112:115], v32, s[56:59], s69 offen offset:0 sc1\n" \
"v_mfma_f32_16x16x32_bf16 v[136:139], a[16:19], a[76:79], v[136:139]\n" \
"v_readlane_b32 s69, v11, 49\n" \
"v_mfma_f32_16x16x32_bf16 v[140:143], a[16:19], a[92:95], v[140:143]\n" \
"v_mfma_f32_16x16x32_bf16 v[140:143], a[12:15], a[88:91], v[140:143]\n" \
"s_waitcnt vmcnt(16)\n" \
"s_barrier\n" \
"buffer_load_dwordx4 a[32:35], v20, s[56:59], s64 offen offset:0 sc1\n" \
"v_mfma_f32_16x16x32_bf16 v[144:147], a[20:23], a[72:75], v[144:147]\n" \
"ds_read_b64_tr_b16 a[0:1], v33 offset:18432\n" \
"ds_read_b64_tr_b16 a[2:3], v33 offset:23040\n" \
"v_mfma_f32_16x16x32_bf16 v[144:147], a[24:27], a[76:79], v[144:147]\n" \
"ds_read_b64_tr_b16 a[4:5], v33 offset:27648\n" \
"ds_read_b64_tr_b16 a[6:7], v33 offset:32256\n" \
"v_mfma_f32_16x16x32_bf16 v[148:151], a[24:27], a[92:95], v[148:151]\n" \
"ds_read_b64_tr_b16 a[8:9], v33 offset:36864\n" \
"ds_read_b64_tr_b16 a[10:11], v33 offset:41472\n" \
"v_mfma_f32_16x16x32_bf16 v[148:151], a[20:23], a[88:91], v[148:151]\n" \
"ds_read_b64_tr_b16 a[12:13], v33 offset:46080\n" \
"ds_read_b64_tr_b16 a[14:15], v33 offset:50688\n" \
"buffer_load_dwordx4 a[36:39], v20, s[56:59], s64 offen offset:1024 sc1\n" \
"v_mfma_f32_16x16x32_bf16 v[152:155], a[28:31], a[72:75], v[152:155]\n" \
"ds_read_b64_tr_b16 a[16:17], v34 offset:0\n" \
"ds_read_b64_tr_b16 a[18:19], v34 offset:4608\n" \
"v_mfma_f32_16x16x32_bf16 v[152:155], a[192:195], a[76:79], v[152:155]\n" \
"ds_read_b64_tr_b16 a[20:21], v34 offset:9216\n" \
"ds_read_b64_tr_b16 a[22:23], v34 offset:13824\n" \
"v_mfma_f32_16x16x32_bf16 v[156:159], a[192:195], a[92:95], v[156:159]\n" \
"ds_read_b64_tr_b16 a[24:25], v34 offset:18432\n" \
"ds_read_b64_tr_b16 a[26:27], v34 offset:23040\n" \
"v_mfma_f32_16x16x32_bf16 v[156:159], a[28:31], a[88:91], v[156:159]\n" \
"ds_read_b64_tr_b16 a[28:29], v33 offset:0\n" \
"ds_read_b64_tr_b16 a[30:31], v33 offset:4608\n" \
"buffer_load_dwordx4 a[40:43], v20, s[56:59], s64 offen offset:2048 sc1\n" \
"v_mfma_f32_16x16x32_bf16 v[160:163], a[196:199], a[72:75], v[160:163]\n" \
"ds_read_b64_tr_b16 a[192:193], v33 offset:18464\n" \
"s_waitcnt lgkmcnt(15)\n" \
"v_mfma_f32_16x16x32_bf16 v[160:163], a[200:203], a[76:79], v[160:163]\n" \
"ds_read_b64_tr_b16 a[194:195], v33 offset:23072\n" \
"v_mfma_f32_16x16x32_bf16 v[164:167], a[200:203], a[92:95], v[164:167]\n" \
"buffer_load_dwordx4 a[44:47], v20, s[56:59], s64 offen offset:3072 sc1\n" \
"v_mfma_f32_16x16x32_bf16 v[164:167], a[196:199], a[88:91], v[164:167]\n" \
"ds_read_b64_tr_b16 a[196:197], v33 offset:27680\n" \
"ds_read_b64_tr_b16 a[198:199], v33 offset:32288\n" \
"v_mfma_f32_16x16x32_bf16 v[168:171], a[204:207], a[72:75], v[168:171]\n" \
"ds_read_b64_tr_b16 a[200:201], v33 offset:36896\n" \
"ds_read_b64_tr_b16 a[202:203], v33 offset:41504\n" \
"v_mfma_f32_16x16x32_bf16 v[168:171], a[208:211], a[76:79], v[168:171]\n" \
"v_mfma_f32_16x16x32_bf16 v[172:175], a[208:211], a[92:95], v[172:175]\n" \
"ds_read_b64_tr_b16 a[208:209], v34 offset:32\n" \
"ds_read_b64_tr_b16 a[210:211], v34 offset:4640\n" \
"buffer_load_dwordx4 a[60:63], v32, s[56:59], s64 offen offset:3072 sc1\n" \
"v_mfma_f32_16x16x32_bf16 v[172:175], a[204:207], a[88:91], v[172:175]\n" \
"ds_read_b64_tr_b16 a[204:205], v33 offset:46112\n" \
"ds_read_b64_tr_b16 a[206:207], v33 offset:50720\n" \
"v_mfma_f32_16x16x32_bf16 v[176:179], a[212:215], a[72:75], v[176:179]\n" \
"v_mfma_f32_16x16x32_bf16 v[176:179], a[216:219], a[76:79], v[176:179]\n" \
"v_mfma_f32_16x16x32_bf16 v[180:183], a[216:219], a[92:95], v[180:183]\n" \
"ds_read_b64_tr_b16 a[216:217], v34 offset:18464\n" \
"ds_read_b64_tr_b16 a[218:219], v34 offset:23072\n" \
"buffer_load_dwordx4 a[56:59], v32, s[56:59], s64 offen offset:2048 sc1\n" \
"v_mfma_f32_16x16x32_bf16 v[180:183], a[212:215], a[88:91], v[180:183]\n" \
"ds_read_b64_tr_b16 a[212:213], v34 offset:9248\n" \
"s_waitcnt vmcnt(21)\n" \
"v_mfma_f32_16x16x32_bf16 v[112:115], a[0:3], a[128:131], v[112:115]\n" \
"ds_read_b64_tr_b16 a[214:215], v34 offset:13856\n" \
"s_waitcnt vmcnt(20) lgkmcnt(13)\n" \
"v_mfma_f32_16x16x32_bf16 v[112:115], a[4:7], a[132:135], v[112:115]\n" \
"s_waitcnt vmcnt(19)\n" \
"v_mfma_f32_16x16x32_bf16 v[112:115], a[8:11], a[136:139], v[112:115]\n" \
"s_waitcnt vmcnt(18)\n" \
"v_mfma_f32_16x16x32_bf16 v[112:115], a[12:15], a[140:143], v[112:115]\n" \
"buffer_load_dwordx4 a[52:55], v32, s[56:59], s64 offen offset:1024 sc1\n" \
"s_waitcnt vmcnt(18)\n" \
"v_mfma_f32_16x16x32_bf16 v[116:119], a[12:15], a[156:159], v[116:119]\n" \
"ds_read_b64_tr_b16 a[12:13], v33 offset:36928\n" \
"s_waitcnt vmcnt(17)\n" \
"v_mfma_f32_16x16x32_bf16 v[116:119], a[8:11], a[152:155], v[116:119]\n" \
"ds_read_b64_tr_b16 a[8:9], v33 offset:27712\n" \
"ds_read_b64_tr_b16 a[10:11], v33 offset:32320\n" \
"ds_read_b64_tr_b16 a[14:15], v33 offset:41536\n" \
"s_waitcnt vmcnt(16)\n" \
"v_mfma_f32_16x16x32_bf16 v[116:119], a[4:7], a[148:151], v[116:119]\n" \
"buffer_load_dwordx4 a[48:51], v32, s[56:59], s64 offen offset:0 sc1\n" \
"ds_read_b64_tr_b16 a[4:5], v33 offset:18496\n" \
"ds_read_b64_tr_b16 a[6:7], v33 offset:23104\n" \
"v_readlane_b32 s64, v11, 43\n" \
"s_waitcnt vmcnt(16)\n" \
"v_mfma_f32_16x16x32_bf16 v[116:119], a[0:3], a[144:147], v[116:119]\n" \
"buffer_load_dwordx4 a[160:163], v20, s[56:59], s72 offen offset:0 sc1\n" \
"ds_read_b64_tr_b16 a[0:1], v33 offset:32\n" \
"ds_read_b64_tr_b16 a[2:3], v33 offset:4640\n" \
"s_waitcnt vmcnt(16)\n" \
"v_mfma_f32_16x16x32_bf16 v[112:115], a[16:19], a[96:99], v[112:115]\n" \
"buffer_load_dwordx4 a[164:167], v20, s[56:59], s72 offen offset:1024 sc1\n" \
"s_waitcnt vmcnt(16)\n" \
"v_mfma_f32_16x16x32_bf16 v[112:115], a[20:23], a[100:103], v[112:115]\n" \
"buffer_load_dwordx4 a[168:171], v20, s[56:59], s72 offen offset:2048 sc1\n" \
"s_waitcnt vmcnt(16)\n" \
"v_mfma_f32_16x16x32_bf16 v[112:115], a[24:27], a[104:107], v[112:115]\n" \
"buffer_load_dwordx4 a[172:175], v20, s[56:59], s72 offen offset:3072 sc1\n" \
"s_waitcnt vmcnt(16)\n" \
"v_mfma_f32_16x16x32_bf16 v[112:115], a[28:31], a[108:111], v[112:115]\n" \
"buffer_load_dwordx4 a[188:191], v32, s[56:59], s72 offen offset:3072 sc1\n" \
"s_waitcnt vmcnt(16)\n" \
"v_mfma_f32_16x16x32_bf16 v[116:119], a[28:31], a[124:127], v[116:119]\n" \
"buffer_load_dwordx4 a[184:187], v32, s[56:59], s72 offen offset:2048 sc1\n" \
"ds_read_b64_tr_b16 a[28:29], v34 offset:18496\n" \
"ds_read_b64_tr_b16 a[30:31], v34 offset:23104\n" \
"s_waitcnt vmcnt(16)\n" \
"v_mfma_f32_16x16x32_bf16 v[116:119], a[24:27], a[120:123], v[116:119]\n" \
"v_cvt_pk_bf16_f32 v112, v112, v113\n" \
"v_cvt_pk_bf16_f32 v113, v114, v115\n" \
"s_waitcnt vmcnt(15)\n" \
"v_mfma_f32_16x16x32_bf16 v[116:119], a[20:23], a[116:119], v[116:119]\n" \
"buffer_load_dwordx4 a[180:183], v32, s[56:59], s72 offen offset:1024 sc1\n" \
"ds_read_b64_tr_b16 a[20:21], v34 offset:64\n" \
"ds_read_b64_tr_b16 a[22:23], v34 offset:4672\n" \
"ds_read_b64_tr_b16 a[24:25], v34 offset:9280\n" \
"ds_read_b64_tr_b16 a[26:27], v34 offset:13888\n" \
"s_waitcnt vmcnt(15)\n" \
"v_mfma_f32_16x16x32_bf16 v[116:119], a[16:19], a[112:115], v[116:119]\n" \
"ds_read_b64_tr_b16 a[16:17], v33 offset:46144\n" \
"buffer_load_dwordx4 a[176:179], v32, s[56:59], s72 offen offset:0 sc1\n" \
"s_waitcnt lgkmcnt(5)\n" \
"v_mfma_f32_16x16x32_bf16 v[120:123], a[192:195], a[128:131], v[120:123]\n" \
"ds_read_b64_tr_b16 a[18:19], v33 offset:50752\n" \
"v_mfma_f32_16x16x32_bf16 v[120:123], a[196:199], a[132:135], v[120:123]\n" \
"v_mfma_f32_16x16x32_bf16 v[120:123], a[200:203], a[136:139], v[120:123]\n" \
"v_mfma_f32_16x16x32_bf16 v[120:123], a[204:207], a[140:143], v[120:123]\n" \
"v_cvt_pk_bf16_f32 v114, v116, v117\n" \
"v_cvt_pk_bf16_f32 v115, v118, v119\n" \
"v_mfma_f32_16x16x32_bf16 v[124:127], a[204:207], a[156:159], v[124:127]\n" \
"buffer_store_dwordx4 v[112:115], v20, s[56:59], s65 offen offset:0 sc1\n" \
"s_add_u32 s65, s65, 0x3800\n" \
"v_mfma_f32_16x16x32_bf16 v[124:127], a[200:203], a[152:155], v[124:127]\n" \
"ds_read_b64_tr_b16 a[200:201], v33 offset:27744\n" \
"ds_read_b64_tr_b16 a[202:203], v33 offset:32352\n" \
"v_mfma_f32_16x16x32_bf16 v[124:127], a[196:199], a[148:151], v[124:127]\n" \
"ds_read_b64_tr_b16 a[196:197], v33 offset:18528\n" \
"ds_read_b64_tr_b16 a[198:199], v33 offset:23136\n" \
"v_mfma_f32_16x16x32_bf16 v[124:127], a[192:195], a[144:147], v[124:127]\n" \
"ds_read_b64_tr_b16 a[192:193], v33 offset:64\n" \
"ds_read_b64_tr_b16 a[194:195], v33 offset:4672\n" \
"v_mfma_f32_16x16x32_bf16 v[120:123], a[208:211], a[96:99], v[120:123]\n" \
"buffer_load_dwordx4 a[220:223], v20, s[56:59], s93 offen offset:0 sc1\n" \
"v_mfma_f32_16x16x32_bf16 v[120:123], a[212:215], a[100:103], v[120:123]\n" \
"ds_read_b64_tr_b16 a[204:205], v33 offset:36960\n" \
"ds_read_b64_tr_b16 a[206:207], v33 offset:41568\n" \
"v_mfma_f32_16x16x32_bf16 v[120:123], a[216:219], a[104:107], v[120:123]\n" \
"v_mfma_f32_16x16x32_bf16 v[120:123], a[0:3], a[108:111], v[120:123]\n" \
"v_mfma_f32_16x16x32_bf16 v[124:127], a[0:3], a[124:127], v[124:127]\n" \
"ds_read_b64_tr_b16 a[0:1], v34 offset:18528\n" \
"ds_read_b64_tr_b16 a[2:3], v34 offset:23136\n" \
"buffer_load_dwordx4 a[224:227], v20, s[56:59], s93 offen offset:1024 sc1\n" \
"v_mfma_f32_16x16x32_bf16 v[124:127], a[216:219], a[120:123], v[124:127]\n" \
"ds_read_b64_tr_b16 a[216:217], v34 offset:9312\n" \
"ds_read_b64_tr_b16 a[218:219], v34 offset:13920\n" \
"v_mfma_f32_16x16x32_bf16 v[124:127], a[212:215], a[116:119], v[124:127]\n" \
"ds_read_b64_tr_b16 a[212:213], v34 offset:96\n" \
"v_cvt_pk_bf16_f32 v120, v120, v121\n" \
"v_cvt_pk_bf16_f32 v121, v122, v123\n" \
"v_mfma_f32_16x16x32_bf16 v[124:127], a[208:211], a[112:115], v[124:127]\n" \
"ds_read_b64_tr_b16 a[208:209], v33 offset:46176\n" \
"ds_read_b64_tr_b16 a[210:211], v33 offset:50784\n" \
"v_mfma_f32_16x16x32_bf16 v[128:131], a[4:7], a[128:131], v[128:131]\n" \
"buffer_load_dwordx4 a[228:231], v20, s[56:59], s93 offen offset:2048 sc1\n" \
"v_mfma_f32_16x16x32_bf16 v[128:131], a[8:11], a[132:135], v[128:131]\n" \
"ds_read_b64_tr_b16 a[214:215], v34 offset:4704\n" \
"v_mfma_f32_16x16x32_bf16 v[128:131], a[12:15], a[136:139], v[128:131]\n" \
"s_waitcnt lgkmcnt(15)\n" \
"v_mfma_f32_16x16x32_bf16 v[128:131], a[16:19], a[140:143], v[128:131]\n" \
"v_cvt_pk_bf16_f32 v122, v124, v125\n" \
"v_cvt_pk_bf16_f32 v123, v126, v127\n" \
"v_mfma_f32_16x16x32_bf16 v[132:135], a[16:19], a[156:159], v[132:135]\n" \
"buffer_store_dwordx4 v[120:123], v20, s[56:59], s66 offen offset:0 sc1\n" \
"s_add_u32 s66, s66, 0x3000\n" \
"v_mfma_f32_16x16x32_bf16 v[132:135], a[12:15], a[152:155], v[132:135]\n" \
"ds_read_b64_tr_b16 a[12:13], v10 offset:37120\n" \
"ds_read_b64_tr_b16 a[14:15], v10 offset:41728\n" \
"v_mfma_f32_16x16x32_bf16 v[132:135], a[8:11], a[148:151], v[132:135]\n" \
"ds_read_b64_tr_b16 a[8:9], v10 offset:27904\n" \
"ds_read_b64_tr_b16 a[10:11], v10 offset:32512\n" \
"v_mfma_f32_16x16x32_bf16 v[132:135], a[4:7], a[144:147], v[132:135]\n" \
"ds_read_b64_tr_b16 a[4:5], v33 offset:96\n" \
"ds_read_b64_tr_b16 a[6:7], v33 offset:4704\n" \
"v_mfma_f32_16x16x32_bf16 v[128:131], a[20:23], a[96:99], v[128:131]\n" \
"buffer_load_dwordx4 a[232:235], v20, s[56:59], s93 offen offset:3072 sc1\n" \
"v_mfma_f32_16x16x32_bf16 v[128:131], a[24:27], a[100:103], v[128:131]\n" \
"ds_read_b64_tr_b16 a[16:17], v10 offset:46336\n" \
"ds_read_b64_tr_b16 a[18:19], v10 offset:50944\n" \
"v_mfma_f32_16x16x32_bf16 v[128:131], a[28:31], a[104:107], v[128:131]\n" \
"s_waitcnt lgkmcnt(14)\n" \
"v_mfma_f32_16x16x32_bf16 v[128:131], a[192:195], a[108:111], v[128:131]\n" \
"v_mfma_f32_16x16x32_bf16 v[132:135], a[192:195], a[124:127], v[132:135]\n" \
"ds_read_b64_tr_b16 a[192:193], v33 offset:37120\n" \
"ds_read_b64_tr_b16 a[194:195], v33 offset:41728\n" \
"L_pf22:\n" \
"s_cbranch_execz L_retB\n" \
"s_nop 0\n" \
"buffer_load_dwordx4 a[248:251], v32, s[56:59], s93 offen offset:3072 sc1\n" \
"v_mfma_f32_16x16x32_bf16 v[132:135], a[28:31], a[120:123], v[132:135]\n" \
"ds_read_b64_tr_b16 a[28:29], v33 offset:27904\n" \
"ds_read_b64_tr_b16 a[30:31], v33 offset:32512\n" \
"v_mfma_f32_16x16x32_bf16 v[132:135], a[24:27], a[116:119], v[132:135]\n" \
"ds_read_b64_tr_b16 a[24:25], v33 offset:18688\n" \
"v_cvt_pk_bf16_f32 v128, v128, v129\n" \
"v_cvt_pk_bf16_f32 v129, v130, v131\n" \
"v_mfma_f32_16x16x32_bf16 v[132:135], a[20:23], a[112:115], v[132:135]\n" \
"ds_read_b64_tr_b16 a[20:21], v10 offset:55552\n" \
"ds_read_b64_tr_b16 a[22:23], v10 offset:60160\n" \
"v_mfma_f32_16x16x32_bf16 v[136:139], a[196:199], a[128:131], v[136:139]\n" \
"buffer_load_dwordx4 a[244:247], v32, s[56:59], s93 offen offset:2048 sc1\n" \
"v_mfma_f32_16x16x32_bf16 v[136:139], a[200:203], a[132:135], v[136:139]\n" \
"ds_read_b64_tr_b16 a[26:27], v33 offset:23296\n" \
"v_mfma_f32_16x16x32_bf16 v[136:139], a[204:207], a[136:139], v[136:139]\n" \
"s_waitcnt lgkmcnt(15)\n" \
"v_mfma_f32_16x16x32_bf16 v[136:139], a[208:211], a[140:143], v[136:139]\n" \
"v_cvt_pk_bf16_f32 v130, v132, v133\n" \
"v_cvt_pk_bf16_f32 v131, v134, v135\n" \
"v_mfma_f32_16x16x32_bf16 v[140:143], a[208:211], a[156:159], v[140:143]\n" \
"buffer_store_dwordx4 v[128:131], v20, s[56:59], s67 offen offset:0 sc1\n" \
"s_add_u32 s67, s67, 0x2800\n" \
"v_mfma_f32_16x16x32_bf16 v[140:143], a[204:207], a[152:155], v[140:143]\n" \
"ds_read_b64_tr_b16 a[204:205], v34 offset:9472\n" \
"ds_read_b64_tr_b16 a[206:207], v34 offset:14080\n" \
"v_mfma_f32_16x16x32_bf16 v[140:143], a[200:203], a[148:151], v[140:143]\n" \
"ds_read_b64_tr_b16 a[200:201], v34 offset:256\n" \
"ds_read_b64_tr_b16 a[202:203], v34 offset:4864\n" \
"v_mfma_f32_16x16x32_bf16 v[140:143], a[196:199], a[144:147], v[140:143]\n" \
"ds_read_b64_tr_b16 a[196:197], v33 offset:46336\n" \
"ds_read_b64_tr_b16 a[198:199], v33 offset:50944\n" \
"v_mfma_f32_16x16x32_bf16 v[136:139], a[212:215], a[96:99], v[136:139]\n" \
"buffer_load_dwordx4 a[240:243], v32, s[56:59], s93 offen offset:1024 sc1\n" \
"v_mfma_f32_16x16x32_bf16 v[136:139], a[216:219], a[100:103], v[136:139]\n" \
"ds_read_b64_tr_b16 a[208:209], v34 offset:18688\n" \
"ds_read_b64_tr_b16 a[210:211], v34 offset:23296\n" \
"v_mfma_f32_16x16x32_bf16 v[136:139], a[0:3], a[104:107], v[136:139]\n" \
"s_waitcnt lgkmcnt(14)\n" \
"v_mfma_f32_16x16x32_bf16 v[136:139], a[4:7], a[108:111], v[136:139]\n" \
"v_mfma_f32_16x16x32_bf16 v[140:143], a[4:7], a[124:127], v[140:143]\n" \
"ds_read_b64_tr_b16 a[4:5], v10 offset:46208\n" \
"ds_read_b64_tr_b16 a[6:7], v10 offset:50816\n" \
"buffer_load_dwordx4 a[236:239], v32, s[56:59], s93 offen offset:0 sc1\n" \
"v_mfma_f32_16x16x32_bf16 v[140:143], a[0:3], a[120:123], v[140:143]\n" \
"ds_read_b64_tr_b16 a[0:1], v10 offset:36992\n" \
"ds_read_b64_tr_b16 a[2:3], v10 offset:41600\n" \
"v_mfma_f32_16x16x32_bf16 v[140:143], a[216:219], a[116:119], v[140:143]\n" \
"ds_read_b64_tr_b16 a[216:217], v10 offset:27776\n" \
"v_cvt_pk_bf16_f32 v136, v136, v137\n" \
"v_cvt_pk_bf16_f32 v137, v138, v139\n" \
"v_mfma_f32_16x16x32_bf16 v[140:143], a[212:215], a[112:115], v[140:143]\n" \
"ds_read_b64_tr_b16 a[212:213], v33 offset:256\n" \
"s_waitcnt vmcnt(26)\n" \
"v_mfma_f32_16x16x32_bf16 v[248:251], a[8:11], a[32:35], v[248:251]\n" \
"ds_read_b64_tr_b16 a[214:215], v33 offset:4864\n" \
"s_waitcnt vmcnt(25)\n" \
"v_mfma_f32_16x16x32_bf16 v[248:251], a[12:15], a[36:39], v[248:251]\n" \
"ds_read_b64_tr_b16 a[218:219], v10 offset:32384\n" \
"s_waitcnt vmcnt(24)\n" \
"v_mfma_f32_16x16x32_bf16 v[248:251], a[16:19], a[40:43], v[248:251]\n" \
"v_cvt_pk_bf16_f32 v138, v140, v141\n" \
"v_cvt_pk_bf16_f32 v139, v142, v143\n" \
"s_waitcnt vmcnt(23) lgkmcnt(14)\n" \
"v_mfma_f32_16x16x32_bf16 v[248:251], a[20:23], a[44:47], v[248:251]\n" \
"buffer_store_dwordx4 v[136:139], v20, s[56:59], s68 offen offset:0 sc1\n" \
"s_add_u32 s68, s68, 0x2000\n" \
"s_waitcnt vmcnt(23)\n" \
"v_mfma_f32_16x16x32_bf16 v[252:255], a[20:23], a[60:63], v[252:255]\n" \
"ds_read_b64_tr_b16 a[20:21], v33 offset:36992\n" \
"s_waitcnt vmcnt(22)\n" \
"v_mfma_f32_16x16x32_bf16 v[252:255], a[16:19], a[56:59], v[252:255]\n" \
"ds_read_b64_tr_b16 a[16:17], v33 offset:27776\n" \
"s_waitcnt vmcnt(21)\n" \
"v_mfma_f32_16x16x32_bf16 v[252:255], a[12:15], a[52:55], v[252:255]\n" \
"ds_read_b64_tr_b16 a[12:13], v33 offset:18560\n" \
"s_waitcnt vmcnt(20)\n" \
"v_mfma_f32_16x16x32_bf16 v[252:255], a[8:11], a[48:51], v[252:255]\n" \
"ds_read_b64_tr_b16 a[8:9], v10 offset:55424\n" \
"ds_read_b64_tr_b16 a[10:11], v10 offset:60032\n" \
"v_mfma_f32_16x16x32_bf16 v[176:179], a[24:27], a[128:131], v[176:179]\n" \
"ds_read_b64_tr_b16 a[14:15], v33 offset:23168\n" \
"ds_read_b64_tr_b16 a[18:19], v33 offset:32384\n" \
"v_mfma_f32_16x16x32_bf16 v[176:179], a[28:31], a[132:135], v[176:179]\n" \
"buffer_load_dwordx4 v[64:67], v20, s[56:59], s95 offen offset:0 sc1\n" \
"v_mfma_f32_16x16x32_bf16 v[176:179], a[192:195], a[136:139], v[176:179]\n" \
"ds_read_b64_tr_b16 a[22:23], v33 offset:41600\n" \
"s_waitcnt lgkmcnt(12)\n" \
"v_mfma_f32_16x16x32_bf16 v[176:179], a[196:199], a[140:143], v[176:179]\n" \
"v_mfma_f32_16x16x32_bf16 v[180:183], a[196:199], a[156:159], v[180:183]\n" \
"v_mfma_f32_16x16x32_bf16 v[180:183], a[192:195], a[152:155], v[180:183]\n" \
"v_mfma_f32_16x16x32_bf16 v[180:183], a[28:31], a[148:151], v[180:183]\n" \
"buffer_load_dwordx4 v[68:71], v20, s[56:59], s95 offen offset:1024 sc1\n" \
"v_mfma_f32_16x16x32_bf16 v[180:183], a[24:27], a[144:147], v[180:183]\n" \
"s_waitcnt vmcnt(21)\n" \
"v_mfma_f32_16x16x32_bf16 v[248:251], a[24:27], a[160:163], v[248:251]\n" \
"s_waitcnt vmcnt(13)\n" \
"s_mov_b64 exec, 1\n" \
"buffer_store_dword v39, off, s[56:59], s65 offset:0 sc1\n" \
"s_mov_b64 exec, -1\n" \
"v_mfma_f32_16x16x32_bf16 v[248:251], a[28:31], a[164:167], v[248:251]\n" \
"v_mfma_f32_16x16x32_bf16 v[248:251], a[192:195], a[168:171], v[248:251]\n" \
"v_mfma_f32_16x16x32_bf16 v[248:251], a[196:199], a[172:175], v[248:251]\n" \
"v_mfma_f32_16x16x32_bf16 v[252:255], a[196:199], a[188:191], v[252:255]\n" \
"ds_read_b64_tr_b16 a[196:197], v34 offset:18560\n" \
"ds_read_b64_tr_b16 a[198:199], v34 offset:23168\n" \
"v_mfma_f32_16x16x32_bf16 v[252:255], a[192:195], a[184:187], v[252:255]\n" \
"ds_read_b64_tr_b16 a[192:193], v34 offset:9344\n" \
"ds_read_b64_tr_b16 a[194:195], v34 offset:13952\n" \
"v_mfma_f32_16x16x32_bf16 v[252:255], a[28:31], a[180:183], v[252:255]\n" \
"ds_read_b64_tr_b16 a[28:29], v34 offset:128\n" \
"ds_read_b64_tr_b16 a[30:31], v34 offset:4736\n" \
"v_mfma_f32_16x16x32_bf16 v[252:255], a[24:27], a[176:179], v[252:255]\n" \
"ds_read_b64_tr_b16 a[24:25], v33 offset:46208\n" \
"ds_read_b64_tr_b16 a[26:27], v33 offset:50816\n" \
"v_mfma_f32_16x16x32_bf16 v[176:179], a[200:203], a[96:99], v[176:179]\n" \
"buffer_load_dwordx4 v[72:75], v32, s[56:59], s95 offen offset:0 sc1\n" \
"v_mfma_f32_16x16x32_bf16 v[176:179], a[204:207], a[100:103], v[176:179]\n" \
"v_mfma_f32_16x16x32_bf16 v[176:179], a[208:211], a[104:107], v[176:179]\n" \
"s_waitcnt lgkmcnt(8)\n" \
"v_mfma_f32_16x16x32_bf16 v[176:179], a[212:215], a[108:111], v[176:179]\n" \
"v_mfma_f32_16x16x32_bf16 v[180:183], a[212:215], a[124:127], v[180:183]\n" \
"v_mfma_f32_16x16x32_bf16 v[180:183], a[208:211], a[120:123], v[180:183]\n" \
"buffer_load_dwordx4 v[76:79], v32, s[56:59], s95 offen offset:1024 sc1\n" \
"v_mfma_f32_16x16x32_bf16 v[180:183], a[204:207], a[116:119], v[180:183]\n" \
"v_readlane_b32 s95, v11, 48\n" \
"v_mfma_f32_16x16x32_bf16 v[180:183], a[200:203], a[112:115], v[180:183]\n" \
"s_waitcnt vmcnt(15)\n" \
"v_mfma_f32_16x16x32_bf16 v[248:251], a[200:203], a[220:223], v[248:251]\n" \
"s_waitcnt vmcnt(12)\n" \
"s_mov_b64 exec, 1\n" \
"buffer_store_dword v39, off, s[56:59], s66 offset:4 sc1\n" \
"s_mov_b64 exec, -1\n" \
"v_cvt_pk_bf16_f32 v176, v176, v177\n" \
"v_cvt_pk_bf16_f32 v177, v178, v179\n" \
"v_cvt_pk_bf16_f32 v178, v180, v181\n" \
"v_cvt_pk_bf16_f32 v179, v182, v183\n" \
"v_mfma_f32_16x16x32_bf16 v[248:251], a[204:207], a[224:227], v[248:251]\n" \
"v_readlane_b32 s66, v11, 44\n" \
"v_mfma_f32_16x16x32_bf16 v[248:251], a[208:211], a[228:231], v[248:251]\n" \
"buffer_store_dwordx4 v[176:179], v20, s[56:59], s69 offen offset:0 sc1\n" \
"s_waitcnt vmcnt(10)\n" \
"s_mov_b64 exec, 1\n" \
"buffer_store_dword v39, off, s[56:59], s67 offset:8 sc1\n" \
"s_mov_b64 exec, -1\n" \
"v_mfma_f32_16x16x32_bf16 v[248:251], a[212:215], a[232:235], v[248:251]\n" \
"v_readlane_b32 s67, v11, 45\n" \
"v_mfma_f32_16x16x32_bf16 v[252:255], a[212:215], a[248:251], v[252:255]\n" \
"ds_read_b64_tr_b16 a[212:213], v10 offset:46240\n" \
"ds_read_b64_tr_b16 a[214:215], v10 offset:50848\n" \
"v_mfma_f32_16x16x32_bf16 v[252:255], a[208:211], a[244:247], v[252:255]\n" \
"ds_read_b64_tr_b16 a[208:209], v10 offset:37024\n" \
"ds_read_b64_tr_b16 a[210:211], v10 offset:41632\n" \
"s_waitcnt vmcnt(10)\n" \
"v_mfma_f32_16x16x32_bf16 v[252:255], a[204:207], a[240:243], v[252:255]\n" \
"v_cvt_pk_bf16_f32 v248, v248, v249\n" \
"v_cvt_pk_bf16_f32 v249, v250, v251\n" \
"s_waitcnt vmcnt(9)\n" \
"v_mfma_f32_16x16x32_bf16 v[252:255], a[200:203], a[236:239], v[252:255]\n" \
"ds_read_b64_tr_b16 a[200:201], v33 offset:128\n" \
"ds_read_b64_tr_b16 a[202:203], v33 offset:4736\n" \
"s_waitcnt vmcnt(8)\n" \
"s_mov_b64 exec, 1\n" \
"buffer_store_dword v39, off, s[56:59], s68 offset:12 sc1\n" \
"s_mov_b64 exec, -1\n" \
"v_mfma_f32_16x16x32_bf16 v[216:219], a[216:219], a[32:35], v[216:219]\n" \
"ds_read_b64_tr_b16 a[204:205], v10 offset:27808\n" \
"v_cvt_pk_bf16_f32 v250, v252, v253\n" \
"v_cvt_pk_bf16_f32 v251, v254, v255\n" \
"v_mfma_f32_16x16x32_bf16 v[216:219], a[0:3], a[36:39], v[216:219]\n" \
"buffer_store_dwordx4 v[248:251], v20, s[56:59], s69 offen offset:1024 sc1\n" \
"v_mfma_f32_16x16x32_bf16 v[216:219], a[4:7], a[40:43], v[216:219]\n" \
"ds_read_b64_tr_b16 a[206:207], v10 offset:32416\n" \
"v_mfma_f32_16x16x32_bf16 v[216:219], a[8:11], a[44:47], v[216:219]\n" \
"v_mfma_f32_16x16x32_bf16 v[220:223], a[8:11], a[60:63], v[220:223]\n" \
"ds_read_b64_tr_b16 a[8:9], v33 offset:37024\n" \
"ds_read_b64_tr_b16 a[10:11], v33 offset:41632\n" \
"v_mfma_f32_16x16x32_bf16 v[220:223], a[4:7], a[56:59], v[220:223]\n" \
"ds_read_b64_tr_b16 a[4:5], v33 offset:27808\n" \
"ds_read_b64_tr_b16 a[6:7], v33 offset:32416\n" \
"v_mfma_f32_16x16x32_bf16 v[220:223], a[0:3], a[52:55], v[220:223]\n" \
"ds_read_b64_tr_b16 a[0:1], v33 offset:18592\n" \
"ds_read_b64_tr_b16 a[2:3], v33 offset:23200\n" \
"v_mfma_f32_16x16x32_bf16 v[220:223], a[216:219], a[48:51], v[220:223]\n" \
"ds_read_b64_tr_b16 a[216:217], v10 offset:55456\n" \
"ds_read_b64_tr_b16 a[218:219], v10 offset:60064\n" \
"v_mfma_f32_16x16x32_bf16 v[144:147], a[12:15], a[128:131], v[144:147]\n" \
"buffer_load_dwordx4 v[96:99], v20, s[56:59], s97 offen offset:0 sc1\n" \
"v_mfma_f32_16x16x32_bf16 v[144:147], a[16:19], a[132:135], v[144:147]\n" \
"v_mfma_f32_16x16x32_bf16 v[144:147], a[20:23], a[136:139], v[144:147]\n" \
"s_waitcnt lgkmcnt(12)\n" \
"v_mfma_f32_16x16x32_bf16 v[144:147], a[24:27], a[140:143], v[144:147]\n" \
"buffer_load_dword v37, v40, s[56:59], s74 offen sc1\n" \
"v_mfma_f32_16x16x32_bf16 v[148:151], a[24:27], a[156:159], v[148:151]\n" \
"v_mfma_f32_16x16x32_bf16 v[148:151], a[20:23], a[152:155], v[148:151]\n" \
"v_mfma_f32_16x16x32_bf16 v[148:151], a[16:19], a[148:151], v[148:151]\n" \
"v_mfma_f32_16x16x32_bf16 v[148:151], a[12:15], a[144:147], v[148:151]\n" \
"v_mfma_f32_16x16x32_bf16 v[216:219], a[12:15], a[160:163], v[216:219]\n" \
"buffer_load_dwordx4 v[100:103], v20, s[56:59], s97 offen offset:1024 sc1\n" \
"v_mfma_f32_16x16x32_bf16 v[216:219], a[16:19], a[164:167], v[216:219]\n" \
"v_mfma_f32_16x16x32_bf16 v[216:219], a[20:23], a[168:171], v[216:219]\n" \
"v_mfma_f32_16x16x32_bf16 v[216:219], a[24:27], a[172:175], v[216:219]\n" \
"v_mfma_f32_16x16x32_bf16 v[220:223], a[24:27], a[188:191], v[220:223]\n" \
"ds_read_b64_tr_b16 a[24:25], v34 offset:18592\n" \
"ds_read_b64_tr_b16 a[26:27], v34 offset:23200\n" \
"v_mfma_f32_16x16x32_bf16 v[220:223], a[20:23], a[184:187], v[220:223]\n" \
"ds_read_b64_tr_b16 a[20:21], v34 offset:9376\n" \
"ds_read_b64_tr_b16 a[22:23], v34 offset:13984\n" \
"v_mfma_f32_16x16x32_bf16 v[220:223], a[16:19], a[180:183], v[220:223]\n" \
"ds_read_b64_tr_b16 a[16:17], v34 offset:160\n" \
"ds_read_b64_tr_b16 a[18:19], v34 offset:4768\n" \
"v_mfma_f32_16x16x32_bf16 v[220:223], a[12:15], a[176:179], v[220:223]\n" \
"ds_read_b64_tr_b16 a[12:13], v33 offset:46240\n" \
"ds_read_b64_tr_b16 a[14:15], v33 offset:50848\n" \
"v_mfma_f32_16x16x32_bf16 v[144:147], a[28:31], a[96:99], v[144:147]\n" \
"buffer_load_dwordx4 v[104:107], v32, s[56:59], s97 offen offset:0 sc1\n" \
"v_mfma_f32_16x16x32_bf16 v[144:147], a[192:195], a[100:103], v[144:147]\n" \
"v_mfma_f32_16x16x32_bf16 v[144:147], a[196:199], a[104:107], v[144:147]\n" \
"s_waitcnt lgkmcnt(8)\n" \
"v_mfma_f32_16x16x32_bf16 v[144:147], a[200:203], a[108:111], v[144:147]\n" \
"v_mfma_f32_16x16x32_bf16 v[148:151], a[200:203], a[124:127], v[148:151]\n" \
"v_mfma_f32_16x16x32_bf16 v[148:151], a[196:199], a[120:123], v[148:151]\n" \
"buffer_load_dwordx4 v[108:111], v32, s[56:59], s97 offen offset:1024 sc1\n" \
"v_mfma_f32_16x16x32_bf16 v[148:151], a[192:195], a[116:119], v[148:151]\n" \
"v_readlane_b32 s97, v11, 42\n" \
"v_mfma_f32_16x16x32_bf16 v[148:151], a[28:31], a[112:115], v[148:151]\n" \
"v_mfma_f32_16x16x32_bf16 v[216:219], a[28:31], a[220:223], v[216:219]\n" \
"v_mfma_f32_16x16x32_bf16 v[216:219], a[192:195], a[224:227], v[216:219]\n" \
"v_cvt_pk_bf16_f32 v144, v144, v145\n" \
"v_cvt_pk_bf16_f32 v145, v146, v147\n" \
"v_mfma_f32_16x16x32_bf16 v[216:219], a[196:199], a[228:231], v[216:219]\n" \
"buffer_load_dwordx4 v[80:83], v20, s[56:59], s96 offen offset:0 sc1\n" \
"v_mfma_f32_16x16x32_bf16 v[216:219], a[200:203], a[232:235], v[216:219]\n" \
"v_mfma_f32_16x16x32_bf16 v[220:223], a[200:203], a[248:251], v[220:223]\n" \
"v_cvt_pk_bf16_f32 v146, v148, v149\n" \
"v_cvt_pk_bf16_f32 v147, v150, v151\n" \
"v_mfma_f32_16x16x32_bf16 v[220:223], a[196:199], a[244:247], v[220:223]\n" \
"buffer_store_dwordx4 v[144:147], v20, s[56:59], s71 offen offset:0 sc1\n" \
"v_mfma_f32_16x16x32_bf16 v[220:223], a[192:195], a[240:243], v[220:223]\n" \
"ds_read_b64_tr_b16 a[192:193], v10 offset:27840\n" \
"ds_read_b64_tr_b16 a[194:195], v10 offset:32448\n" \
"v_cvt_pk_bf16_f32 v216, v216, v217\n" \
"v_cvt_pk_bf16_f32 v217, v218, v219\n" \
"v_mfma_f32_16x16x32_bf16 v[220:223], a[28:31], a[236:239], v[220:223]\n" \
"ds_read_b64_tr_b16 a[28:29], v33 offset:160\n" \
"ds_read_b64_tr_b16 a[30:31], v33 offset:4768\n" \
"v_mfma_f32_16x16x32_bf16 v[224:227], a[204:207], a[32:35], v[224:227]\n" \
"ds_read_b64_tr_b16 a[196:197], v10 offset:37056\n" \
"ds_read_b64_tr_b16 a[198:199], v10 offset:41664\n" \
"v_mfma_f32_16x16x32_bf16 v[224:227], a[208:211], a[36:39], v[224:227]\n" \
"ds_read_b64_tr_b16 a[200:201], v10 offset:46272\n" \
"ds_read_b64_tr_b16 a[202:203], v10 offset:50880\n" \
"v_cvt_pk_bf16_f32 v218, v220, v221\n" \
"v_cvt_pk_bf16_f32 v219, v222, v223\n" \
"v_mfma_f32_16x16x32_bf16 v[224:227], a[212:215], a[40:43], v[224:227]\n" \
"buffer_store_dwordx4 v[216:219], v20, s[56:59], s71 offen offset:1024 sc1\n" \
"s_add_u32 s71, s71, 0x1800\n" \
"v_mfma_f32_16x16x32_bf16 v[224:227], a[216:219], a[44:47], v[224:227]\n" \
"v_mfma_f32_16x16x32_bf16 v[228:231], a[216:219], a[60:63], v[228:231]\n" \
"ds_read_b64_tr_b16 a[216:217], v33 offset:37056\n" \
"ds_read_b64_tr_b16 a[218:219], v33 offset:41664\n" \
"v_mfma_f32_16x16x32_bf16 v[228:231], a[212:215], a[56:59], v[228:231]\n" \
"ds_read_b64_tr_b16 a[212:213], v33 offset:27840\n" \
"ds_read_b64_tr_b16 a[214:215], v33 offset:32448\n" \
"v_mfma_f32_16x16x32_bf16 v[228:231], a[208:211], a[52:55], v[228:231]\n" \
"ds_read_b64_tr_b16 a[208:209], v33 offset:18624\n" \
"ds_read_b64_tr_b16 a[210:211], v33 offset:23232\n" \
"v_mfma_f32_16x16x32_bf16 v[228:231], a[204:207], a[48:51], v[228:231]\n" \
"ds_read_b64_tr_b16 a[204:205], v10 offset:55488\n" \
"ds_read_b64_tr_b16 a[206:207], v10 offset:60096\n" \
"v_mfma_f32_16x16x32_bf16 v[152:155], a[0:3], a[128:131], v[152:155]\n" \
"buffer_load_dwordx4 v[88:91], v32, s[56:59], s96 offen offset:0 sc1\n" \
"v_mfma_f32_16x16x32_bf16 v[152:155], a[4:7], a[132:135], v[152:155]\n" \
"v_mfma_f32_16x16x32_bf16 v[152:155], a[8:11], a[136:139], v[152:155]\n" \
"s_waitcnt lgkmcnt(15)\n" \
"v_mfma_f32_16x16x32_bf16 v[152:155], a[12:15], a[140:143], v[152:155]\n" \
"v_mfma_f32_16x16x32_bf16 v[156:159], a[12:15], a[156:159], v[156:159]\n" \
"v_mfma_f32_16x16x32_bf16 v[156:159], a[8:11], a[152:155], v[156:159]\n" \
"buffer_load_dwordx4 v[84:87], v20, s[56:59], s96 offen offset:1024 sc1\n" \
"v_mfma_f32_16x16x32_bf16 v[156:159], a[4:7], a[148:151], v[156:159]\n" \
"v_mfma_f32_16x16x32_bf16 v[156:159], a[0:3], a[144:147], v[156:159]\n" \
"v_mfma_f32_16x16x32_bf16 v[224:227], a[0:3], a[160:163], v[224:227]\n" \
"v_mfma_f32_16x16x32_bf16 v[224:227], a[4:7], a[164:167], v[224:227]\n" \
"v_mfma_f32_16x16x32_bf16 v[224:227], a[8:11], a[168:171], v[224:227]\n" \
"buffer_load_dwordx4 v[92:95], v32, s[56:59], s96 offen offset:1024 sc1\n" \
"v_mfma_f32_16x16x32_bf16 v[224:227], a[12:15], a[172:175], v[224:227]\n" \
"v_mfma_f32_16x16x32_bf16 v[228:231], a[12:15], a[188:191], v[228:231]\n" \
"ds_read_b64_tr_b16 a[12:13], v34 offset:18624\n" \
"ds_read_b64_tr_b16 a[14:15], v34 offset:23232\n" \
"v_mfma_f32_16x16x32_bf16 v[228:231], a[8:11], a[184:187], v[228:231]\n" \
"ds_read_b64_tr_b16 a[8:9], v34 offset:9408\n" \
"ds_read_b64_tr_b16 a[10:11], v34 offset:14016\n" \
"v_mfma_f32_16x16x32_bf16 v[228:231], a[4:7], a[180:183], v[228:231]\n" \
"ds_read_b64_tr_b16 a[4:5], v34 offset:192\n" \
"ds_read_b64_tr_b16 a[6:7], v34 offset:4800\n" \
"v_mfma_f32_16x16x32_bf16 v[228:231], a[0:3], a[176:179], v[228:231]\n" \
"ds_read_b64_tr_b16 a[0:1], v33 offset:46272\n" \
"ds_read_b64_tr_b16 a[2:3], v33 offset:50880\n" \
"s_waitcnt vmcnt(11)\n" \
"s_mov_b64 exec, 3\n" \
"buffer_store_dword v39, v41, s[56:59], s70 offen sc1\n" \
"s_mov_b64 exec, -1\n" \
"v_mfma_f32_16x16x32_bf16 v[152:155], a[16:19], a[96:99], v[152:155]\n" \
"v_mfma_f32_16x16x32_bf16 v[152:155], a[20:23], a[100:103], v[152:155]\n" \
"v_mfma_f32_16x16x32_bf16 v[152:155], a[24:27], a[104:107], v[152:155]\n" \
"s_waitcnt vmcnt(10)\n" \
"v_cmp_eq_u32_e64 s[98:99], s62, v37\n" \
"s_waitcnt lgkmcnt(8)\n" \
"v_mfma_f32_16x16x32_bf16 v[152:155], a[28:31], a[108:111], v[152:155]\n" \
"s_and_b32 s99, s98, 0xf\n" \
"s_or_b32 s84, s84, s99\n" \
"s_bitcmp1_b32 s84, 0\n" \
"s_cselect_b32 s72, s63, 0x40000000\n" \
"s_bitcmp1_b32 s84, 1\n" \
"s_cselect_b32 s73, s63, 0x40000000\n" \
"s_bitcmp1_b32 s84, 2\n" \
"s_cselect_b32 s92, s63, 0x40000000\n" \
"s_bitcmp1_b32 s84, 3\n" \
"s_cselect_b32 s93, s63, 0x40000000\n" \
"v_cvt_pk_bf16_f32 v152, v152, v153\n" \
"v_cvt_pk_bf16_f32 v153, v154, v155\n" \
"v_mfma_f32_16x16x32_bf16 v[156:159], a[28:31], a[124:127], v[156:159]\n" \
"buffer_load_dwordx4 v[116:119], v20, s[56:59], s72 offen offset:0 sc1\n" \
"v_mfma_f32_16x16x32_bf16 v[156:159], a[24:27], a[120:123], v[156:159]\n" \
"v_mfma_f32_16x16x32_bf16 v[156:159], a[20:23], a[116:119], v[156:159]\n" \
"v_mfma_f32_16x16x32_bf16 v[156:159], a[16:19], a[112:115], v[156:159]\n" \
"v_mfma_f32_16x16x32_bf16 v[224:227], a[16:19], a[220:223], v[224:227]\n" \
"v_mfma_f32_16x16x32_bf16 v[224:227], a[20:23], a[224:227], v[224:227]\n" \
"buffer_load_dwordx4 v[124:127], v20, s[56:59], s73 offen offset:2048 sc1\n" \
"v_mfma_f32_16x16x32_bf16 v[224:227], a[24:27], a[228:231], v[224:227]\n" \
"v_mfma_f32_16x16x32_bf16 v[224:227], a[28:31], a[232:235], v[224:227]\n" \
"v_mfma_f32_16x16x32_bf16 v[228:231], a[28:31], a[248:251], v[228:231]\n" \
"ds_read_b64_tr_b16 a[28:29], v10 offset:46080\n" \
"ds_read_b64_tr_b16 a[30:31], v10 offset:50688\n" \
"v_cvt_pk_bf16_f32 v154, v156, v157\n" \
"v_cvt_pk_bf16_f32 v155, v158, v159\n" \
"v_mfma_f32_16x16x32_bf16 v[228:231], a[24:27], a[244:247], v[228:231]\n" \
"buffer_store_dwordx4 v[152:155], v20, s[56:59], s94 offen offset:0 sc1\n" \
"v_mfma_f32_16x16x32_bf16 v[228:231], a[20:23], a[240:243], v[228:231]\n" \
"ds_read_b64_tr_b16 a[20:21], v10 offset:27648\n" \
"v_cvt_pk_bf16_f32 v224, v224, v225\n" \
"v_cvt_pk_bf16_f32 v225, v226, v227\n" \
"v_mfma_f32_16x16x32_bf16 v[228:231], a[16:19], a[236:239], v[228:231]\n" \
"ds_read_b64_tr_b16 a[16:17], v33 offset:192\n" \
"ds_read_b64_tr_b16 a[18:19], v33 offset:4800\n" \
"v_mfma_f32_16x16x32_bf16 v[232:235], a[192:195], a[32:35], v[232:235]\n" \
"ds_read_b64_tr_b16 a[22:23], v10 offset:32256\n" \
"ds_read_b64_tr_b16 a[24:25], v10 offset:36864\n" \
"v_mfma_f32_16x16x32_bf16 v[232:235], a[196:199], a[36:39], v[232:235]\n" \
"ds_read_b64_tr_b16 a[26:27], v10 offset:41472\n" \
"buffer_load_dwordx4 v[132:135], v38, s[56:59], s92 offen offset:0 sc1\n" \
"v_cvt_pk_bf16_f32 v226, v228, v229\n" \
"v_cvt_pk_bf16_f32 v227, v230, v231\n" \
"v_mfma_f32_16x16x32_bf16 v[232:235], a[200:203], a[40:43], v[232:235]\n" \
"v_readlane_b32 s92, v11, 50\n" \
"v_mfma_f32_16x16x32_bf16 v[232:235], a[204:207], a[44:47], v[232:235]\n" \
"buffer_store_dwordx4 v[224:227], v20, s[56:59], s94 offen offset:1024 sc1\n" \
"s_add_u32 s94, s94, 0x1000\n" \
"v_mfma_f32_16x16x32_bf16 v[236:239], a[204:207], a[60:63], v[236:239]\n" \
"ds_read_b64_tr_b16 a[204:205], v33 offset:36864\n" \
"ds_read_b64_tr_b16 a[206:207], v33 offset:41472\n" \
"v_mfma_f32_16x16x32_bf16 v[236:239], a[200:203], a[56:59], v[236:239]\n" \
"ds_read_b64_tr_b16 a[200:201], v33 offset:27648\n" \
"ds_read_b64_tr_b16 a[202:203], v33 offset:32256\n" \
"v_mfma_f32_16x16x32_bf16 v[236:239], a[196:199], a[52:55], v[236:239]\n" \
"ds_read_b64_tr_b16 a[196:197], v33 offset:18432\n" \
"ds_read_b64_tr_b16 a[198:199], v33 offset:23040\n" \
"v_mfma_f32_16x16x32_bf16 v[236:239], a[192:195], a[48:51], v[236:239]\n" \
"ds_read_b64_tr_b16 a[192:193], v10 offset:55296\n" \
"ds_read_b64_tr_b16 a[194:195], v10 offset:59904\n" \
"buffer_load_dwordx4 v[140:143], v38, s[56:59], s93 offen offset:2048 sc1\n" \
"v_mfma_f32_16x16x32_bf16 v[160:163], a[208:211], a[128:131], v[160:163]\n" \
"v_readlane_b32 s93, v11, 41\n" \
"v_mfma_f32_16x16x32_bf16 v[160:163], a[212:215], a[132:135], v[160:163]\n" \
"v_mfma_f32_16x16x32_bf16 v[160:163], a[216:219], a[136:139], v[160:163]\n" \
"s_waitcnt lgkmcnt(14)\n" \
"v_mfma_f32_16x16x32_bf16 v[160:163], a[0:3], a[140:143], v[160:163]\n" \
"buffer_load_dword v37, v40, s[56:59], s74 offen sc1\n" \
"v_mfma_f32_16x16x32_bf16 v[164:167], a[0:3], a[156:159], v[164:167]\n" \
"v_mfma_f32_16x16x32_bf16 v[164:167], a[216:219], a[152:155], v[164:167]\n" \
"v_mfma_f32_16x16x32_bf16 v[164:167], a[212:215], a[148:151], v[164:167]\n" \
"v_mfma_f32_16x16x32_bf16 v[164:167], a[208:211], a[144:147], v[164:167]\n" \
"v_mfma_f32_16x16x32_bf16 v[232:235], a[208:211], a[160:163], v[232:235]\n" \
"v_mfma_f32_16x16x32_bf16 v[232:235], a[212:215], a[164:167], v[232:235]\n" \
"v_mfma_f32_16x16x32_bf16 v[232:235], a[216:219], a[168:171], v[232:235]\n" \
"v_mfma_f32_16x16x32_bf16 v[232:235], a[0:3], a[172:175], v[232:235]\n" \
"s_waitcnt vmcnt(11)\n" \
"s_mov_b64 exec, 1\n" \
"buffer_store_dword v39, off, s[56:59], s71 offset:20 sc1\n" \
"s_mov_b64 exec, -1\n" \
"v_mfma_f32_16x16x32_bf16 v[236:239], a[0:3], a[188:191], v[236:239]\n" \
"ds_read_b64_tr_b16 a[0:1], v34 offset:18432\n" \
"ds_read_b64_tr_b16 a[2:3], v34 offset:23040\n" \
"v_mfma_f32_16x16x32_bf16 v[236:239], a[216:219], a[184:187], v[236:239]\n" \
"ds_read_b64_tr_b16 a[216:217], v34 offset:9216\n" \
"ds_read_b64_tr_b16 a[218:219], v34 offset:13824\n" \
"v_mfma_f32_16x16x32_bf16 v[236:239], a[212:215], a[180:183], v[236:239]\n" \
"ds_read_b64_tr_b16 a[212:213], v34 offset:0\n" \
"ds_read_b64_tr_b16 a[214:215], v34 offset:4608\n" \
"v_mfma_f32_16x16x32_bf16 v[236:239], a[208:211], a[176:179], v[236:239]\n" \
"ds_read_b64_tr_b16 a[208:209], v33 offset:46080\n" \
"ds_read_b64_tr_b16 a[210:211], v33 offset:50688\n" \
"v_mfma_f32_16x16x32_bf16 v[160:163], a[4:7], a[96:99], v[160:163]\n" \
"v_mfma_f32_16x16x32_bf16 v[160:163], a[8:11], a[100:103], v[160:163]\n" \
"v_mfma_f32_16x16x32_bf16 v[160:163], a[12:15], a[104:107], v[160:163]\n" \
"s_waitcnt lgkmcnt(8)\n" \
"v_mfma_f32_16x16x32_bf16 v[160:163], a[16:19], a[108:111], v[160:163]\n" \
"v_mfma_f32_16x16x32_bf16 v[164:167], a[16:19], a[124:127], v[164:167]\n" \
"v_mfma_f32_16x16x32_bf16 v[164:167], a[12:15], a[120:123], v[164:167]\n" \
"v_mfma_f32_16x16x32_bf16 v[164:167], a[8:11], a[116:119], v[164:167]\n" \
"v_mfma_f32_16x16x32_bf16 v[164:167], a[4:7], a[112:115], v[164:167]\n" \
"v_mfma_f32_16x16x32_bf16 v[232:235], a[4:7], a[220:223], v[232:235]\n" \
"v_mfma_f32_16x16x32_bf16 v[232:235], a[8:11], a[224:227], v[232:235]\n" \
"v_mfma_f32_16x16x32_bf16 v[232:235], a[12:15], a[228:231], v[232:235]\n" \
"v_mfma_f32_16x16x32_bf16 v[232:235], a[16:19], a[232:235], v[232:235]\n" \
"v_cvt_pk_bf16_f32 v160, v160, v161\n" \
"v_cvt_pk_bf16_f32 v161, v162, v163\n" \
"v_mfma_f32_16x16x32_bf16 v[236:239], a[16:19], a[248:251], v[236:239]\n" \
"ds_read_b64_tr_b16 a[16:17], v10 offset:46112\n" \
"v_cvt_pk_bf16_f32 v162, v164, v165\n" \
"v_cvt_pk_bf16_f32 v163, v166, v167\n" \
"v_mfma_f32_16x16x32_bf16 v[236:239], a[12:15], a[244:247], v[236:239]\n" \
"buffer_store_dwordx4 v[160:163], v20, s[56:59], s95 offen offset:0 sc1\n" \
"v_cvt_pk_bf16_f32 v232, v232, v233\n" \
"v_cvt_pk_bf16_f32 v233, v234, v235\n" \
"v_mfma_f32_16x16x32_bf16 v[236:239], a[8:11], a[240:243], v[236:239]\n" \
"ds_read_b64_tr_b16 a[8:9], v10 offset:27680\n" \
"ds_read_b64_tr_b16 a[10:11], v10 offset:32288\n" \
"v_mfma_f32_16x16x32_bf16 v[236:239], a[4:7], a[236:239], v[236:239]\n" \
"ds_read_b64_tr_b16 a[4:5], v33 offset:0\n" \
"ds_read_b64_tr_b16 a[6:7], v33 offset:4608\n" \
"v_mfma_f32_16x16x32_bf16 v[184:187], a[20:23], a[32:35], v[184:187]\n" \
"ds_read_b64_tr_b16 a[12:13], v10 offset:36896\n" \
"ds_read_b64_tr_b16 a[14:15], v10 offset:41504\n" \
"v_mfma_f32_16x16x32_bf16 v[184:187], a[24:27], a[36:39], v[184:187]\n" \
"ds_read_b64_tr_b16 a[18:19], v10 offset:50720\n" \
"v_mfma_f32_16x16x32_bf16 v[184:187], a[28:31], a[40:43], v[184:187]\n" \
"v_cvt_pk_bf16_f32 v234, v236, v237\n" \
"v_cvt_pk_bf16_f32 v235, v238, v239\n" \
"v_mfma_f32_16x16x32_bf16 v[184:187], a[192:195], a[44:47], v[184:187]\n" \
"buffer_store_dwordx4 v[232:235], v20, s[56:59], s95 offen offset:1024 sc1\n" \
"s_add_u32 s95, s95, 0x800\n" \
"v_mfma_f32_16x16x32_bf16 v[188:191], a[192:195], a[60:63], v[188:191]\n" \
"ds_read_b64_tr_b16 a[192:193], v33 offset:36896\n" \
"ds_read_b64_tr_b16 a[194:195], v33 offset:41504\n" \
"v_mfma_f32_16x16x32_bf16 v[188:191], a[28:31], a[56:59], v[188:191]\n" \
"ds_read_b64_tr_b16 a[28:29], v33 offset:27680\n" \
"ds_read_b64_tr_b16 a[30:31], v33 offset:32288\n" \
"v_mfma_f32_16x16x32_bf16 v[188:191], a[24:27], a[52:55], v[188:191]\n" \
"ds_read_b64_tr_b16 a[24:25], v33 offset:18464\n" \
"ds_read_b64_tr_b16 a[26:27], v33 offset:23072\n" \
"s_mov_b64 exec, 1\n" \
"buffer_store_dword v46, off, s[56:59], s92 offset:0 sc1\n" \
"s_mov_b64 exec, -1\n" \
"v_mfma_f32_16x16x32_bf16 v[188:191], a[20:23], a[48:51], v[188:191]\n" \
"ds_read_b64_tr_b16 a[20:21], v10 offset:55328\n" \
"ds_read_b64_tr_b16 a[22:23], v10 offset:59936\n" \
"v_mfma_f32_16x16x32_bf16 v[184:187], a[196:199], a[160:163], v[184:187]\n" \
"v_mfma_f32_16x16x32_bf16 v[184:187], a[200:203], a[164:167], v[184:187]\n" \
"v_mfma_f32_16x16x32_bf16 v[184:187], a[204:207], a[168:171], v[184:187]\n" \
"s_waitcnt lgkmcnt(15)\n" \
"v_mfma_f32_16x16x32_bf16 v[184:187], a[208:211], a[172:175], v[184:187]\n" \
"v_mfma_f32_16x16x32_bf16 v[188:191], a[208:211], a[188:191], v[188:191]\n" \
"ds_read_b64_tr_b16 a[208:209], v34 offset:18464\n" \
"ds_read_b64_tr_b16 a[210:211], v34 offset:23072\n" \
"v_mfma_f32_16x16x32_bf16 v[188:191], a[204:207], a[184:187], v[188:191]\n" \
"ds_read_b64_tr_b16 a[204:205], v34 offset:9248\n" \
"ds_read_b64_tr_b16 a[206:207], v34 offset:13856\n" \
"v_mfma_f32_16x16x32_bf16 v[188:191], a[200:203], a[180:183], v[188:191]\n" \
"ds_read_b64_tr_b16 a[200:201], v34 offset:32\n" \
"ds_read_b64_tr_b16 a[202:203], v34 offset:4640\n" \
"v_mfma_f32_16x16x32_bf16 v[188:191], a[196:199], a[176:179], v[188:191]\n" \
"ds_read_b64_tr_b16 a[196:197], v33 offset:46112\n" \
"ds_read_b64_tr_b16 a[198:199], v33 offset:50720\n" \
"v_mfma_f32_16x16x32_bf16 v[184:187], a[212:215], a[220:223], v[184:187]\n" \
"v_mfma_f32_16x16x32_bf16 v[184:187], a[216:219], a[224:227], v[184:187]\n" \
"v_mfma_f32_16x16x32_bf16 v[184:187], a[0:3], a[228:231], v[184:187]\n" \
"s_waitcnt lgkmcnt(13)\n" \
"v_mfma_f32_16x16x32_bf16 v[184:187], a[4:7], a[232:235], v[184:187]\n" \
"v_mfma_f32_16x16x32_bf16 v[188:191], a[4:7], a[248:251], v[188:191]\n" \
"ds_read_b64_tr_b16 a[4:5], v10 offset:46144\n" \
"ds_read_b64_tr_b16 a[6:7], v10 offset:50752\n" \
"v_mfma_f32_16x16x32_bf16 v[188:191], a[0:3], a[244:247], v[188:191]\n" \
"ds_read_b64_tr_b16 a[0:1], v10 offset:36928\n" \
"ds_read_b64_tr_b16 a[2:3], v10 offset:41536\n" \
"v_mfma_f32_16x16x32_bf16 v[188:191], a[216:219], a[240:243], v[188:191]\n" \
"ds_read_b64_tr_b16 a[216:217], v10 offset:27712\n" \
"v_cvt_pk_bf16_f32 v184, v184, v185\n" \
"v_cvt_pk_bf16_f32 v185, v186, v187\n" \
"v_mfma_f32_16x16x32_bf16 v[188:191], a[212:215], a[236:239], v[188:191]\n" \
"ds_read_b64_tr_b16 a[212:213], v33 offset:32\n" \
"ds_read_b64_tr_b16 a[214:215], v33 offset:4640\n" \
"s_waitcnt vmcnt(6)\n" \
"s_mov_b64 exec, 1\n" \
"buffer_store_dword v39, off, s[56:59], s94 offset:24 sc1\n" \
"s_mov_b64 exec, -1\n" \
"s_waitcnt vmcnt(5)\n" \
"v_cmp_eq_u32_e64 s[98:99], s62, v37\n" \
"v_cvt_pk_bf16_f32 v186, v188, v189\n" \
"v_cvt_pk_bf16_f32 v187, v190, v191\n" \
"v_mfma_f32_16x16x32_bf16 v[192:195], a[8:11], a[32:35], v[192:195]\n" \
"ds_read_b64_tr_b16 a[218:219], v10 offset:32320\n" \
"s_and_b32 s99, s98, 0x0\n" \
"buffer_store_dwordx4 v[184:187], v20, s[56:59], s97 offen offset:1024 sc1\n" \
"s_add_u32 s97, s97, 0x3800\n" \
"v_mfma_f32_16x16x32_bf16 v[192:195], a[12:15], a[36:39], v[192:195]\n" \
"s_or_b32 s84, s84, s99\n" \
"v_readlane_b32 s94, v11, 39\n" \
"v_mfma_f32_16x16x32_bf16 v[192:195], a[16:19], a[40:43], v[192:195]\n" \
"s_waitcnt lgkmcnt(8)\n" \
"v_mfma_f32_16x16x32_bf16 v[192:195], a[20:23], a[44:47], v[192:195]\n" \
"s_andn2_b32 s99, s100, s98\n" \
"s_cselect_b32 s99, 0, 16\n" \
"v_mfma_f32_16x16x32_bf16 v[196:199], a[20:23], a[60:63], v[196:199]\n" \
"ds_read_b64_tr_b16 a[20:21], v33 offset:36928\n" \
"ds_read_b64_tr_b16 a[22:23], v33 offset:41536\n" \
"v_mfma_f32_16x16x32_bf16 v[196:199], a[16:19], a[56:59], v[196:199]\n" \
"ds_read_b64_tr_b16 a[16:17], v33 offset:27712\n" \
"ds_read_b64_tr_b16 a[18:19], v33 offset:32320\n" \
"v_mfma_f32_16x16x32_bf16 v[196:199], a[12:15], a[52:55], v[196:199]\n" \
"ds_read_b64_tr_b16 a[12:13], v33 offset:18496\n" \
"ds_read_b64_tr_b16 a[14:15], v33 offset:23104\n" \
"v_mfma_f32_16x16x32_bf16 v[196:199], a[8:11], a[48:51], v[196:199]\n" \
"ds_read_b64_tr_b16 a[8:9], v10 offset:55360\n" \
"ds_read_b64_tr_b16 a[10:11], v10 offset:59968\n" \
"v_mfma_f32_16x16x32_bf16 v[192:195], a[24:27], a[160:163], v[192:195]\n" \
"s_or_b32 s84, s84, s99\n" \
"s_bitcmp1_b32 s84, 4\n" \
"s_cselect_b32 s96, s75, 0x40000000\n" \
"buffer_load_dwordx4 v[176:179], v20, s[56:59], s96 offen offset:0 sc1\n" \
"v_mfma_f32_16x16x32_bf16 v[192:195], a[28:31], a[164:167], v[192:195]\n" \
"v_mfma_f32_16x16x32_bf16 v[192:195], a[192:195], a[168:171], v[192:195]\n" \
"v_mfma_f32_16x16x32_bf16 v[192:195], a[196:199], a[172:175], v[192:195]\n" \
"buffer_load_dword v37, v40, s[56:59], s74 offen sc1\n" \
"v_mfma_f32_16x16x32_bf16 v[196:199], a[196:199], a[188:191], v[196:199]\n" \
"ds_read_b64_tr_b16 a[196:197], v34 offset:18496\n" \
"ds_read_b64_tr_b16 a[198:199], v34 offset:23104\n" \
"v_mfma_f32_16x16x32_bf16 v[196:199], a[192:195], a[184:187], v[196:199]\n" \
"ds_read_b64_tr_b16 a[192:193], v34 offset:9280\n" \
"ds_read_b64_tr_b16 a[194:195], v34 offset:13888\n" \
"v_mfma_f32_16x16x32_bf16 v[196:199], a[28:31], a[180:183], v[196:199]\n" \
"buffer_load_dwordx4 v[248:251], v20, s[56:59], s96 offen offset:2048 sc1\n" \
"v_mfma_f32_16x16x32_bf16 v[196:199], a[24:27], a[176:179], v[196:199]\n" \
"ds_read_b64_tr_b16 a[24:25], v33 offset:46144\n" \
"L_pf23:\n" \
"s_cbranch_execz L_retB\n" \
"s_nop 0\n" \
"ds_read_b64_tr_b16 a[26:27], v33 offset:50752\n" \
"v_mfma_f32_16x16x32_bf16 v[192:195], a[200:203], a[220:223], v[192:195]\n" \
"ds_read_b64_tr_b16 a[28:29], v34 offset:64\n" \
"ds_read_b64_tr_b16 a[30:31], v34 offset:4672\n" \
"v_mfma_f32_16x16x32_bf16 v[192:195], a[204:207], a[224:227], v[192:195]\n" \
"buffer_load_dwordx4 v[180:183], v38, s[56:59], s96 offen offset:0 sc1\n" \
"v_mfma_f32_16x16x32_bf16 v[192:195], a[208:211], a[228:231], v[192:195]\n" \
"s_waitcnt lgkmcnt(14)\n" \
"v_mfma_f32_16x16x32_bf16 v[192:195], a[212:215], a[232:235], v[192:195]\n" \
"v_mfma_f32_16x16x32_bf16 v[196:199], a[212:215], a[248:251], v[196:199]\n" \
"buffer_load_dwordx4 v[252:255], v38, s[56:59], s96 offen offset:2048 sc1\n" \
"v_mfma_f32_16x16x32_bf16 v[196:199], a[208:211], a[244:247], v[196:199]\n" \
"ds_read_b64_tr_b16 a[208:209], v10 offset:36960\n" \
"ds_read_b64_tr_b16 a[210:211], v10 offset:41568\n" \
"v_mfma_f32_16x16x32_bf16 v[196:199], a[204:207], a[240:243], v[196:199]\n" \
"ds_read_b64_tr_b16 a[204:205], v10 offset:27744\n" \
"ds_read_b64_tr_b16 a[206:207], v10 offset:32352\n" \
"v_cvt_pk_bf16_f32 v192, v192, v193\n" \
"v_cvt_pk_bf16_f32 v193, v194, v195\n" \
"v_mfma_f32_16x16x32_bf16 v[196:199], a[200:203], a[236:239], v[196:199]\n" \
"buffer_load_dwordx4 v[112:115], v47, s[56:59], s96 offen offset:0 sc1\n" \
"v_mfma_f32_16x16x32_bf16 v[200:203], a[216:219], a[32:35], v[200:203]\n" \
"ds_read_b64_tr_b16 a[200:201], v33 offset:64\n" \
"ds_read_b64_tr_b16 a[202:203], v33 offset:4672\n" \
"v_mfma_f32_16x16x32_bf16 v[200:203], a[0:3], a[36:39], v[200:203]\n" \
"ds_read_b64_tr_b16 a[212:213], v10 offset:46176\n" \
"ds_read_b64_tr_b16 a[214:215], v10 offset:50784\n" \
"v_mfma_f32_16x16x32_bf16 v[200:203], a[4:7], a[40:43], v[200:203]\n" \
"v_cvt_pk_bf16_f32 v194, v196, v197\n" \
"v_cvt_pk_bf16_f32 v195, v198, v199\n" \
"s_waitcnt lgkmcnt(13)\n" \
"v_mfma_f32_16x16x32_bf16 v[200:203], a[8:11], a[44:47], v[200:203]\n" \
"buffer_store_dwordx4 v[192:195], v20, s[56:59], s64 offen offset:1024 sc1\n" \
"s_add_u32 s64, s64, 0x3000\n" \
"v_mfma_f32_16x16x32_bf16 v[204:207], a[8:11], a[60:63], v[204:207]\n" \
"ds_read_b64_tr_b16 a[8:9], v33 offset:36960\n" \
"ds_read_b64_tr_b16 a[10:11], v33 offset:41568\n" \
"v_mfma_f32_16x16x32_bf16 v[204:207], a[4:7], a[56:59], v[204:207]\n" \
"ds_read_b64_tr_b16 a[4:5], v33 offset:27744\n" \
"ds_read_b64_tr_b16 a[6:7], v33 offset:32352\n" \
"v_mfma_f32_16x16x32_bf16 v[204:207], a[0:3], a[52:55], v[204:207]\n" \
"buffer_load_dwordx4 v[120:123], v47, s[56:59], s96 offen offset:2048 sc1\n" \
"v_mfma_f32_16x16x32_bf16 v[204:207], a[216:219], a[48:51], v[204:207]\n" \
"ds_read_b64_tr_b16 a[216:217], v10 offset:55392\n" \
"ds_read_b64_tr_b16 a[218:219], v10 offset:60000\n" \
"v_mfma_f32_16x16x32_bf16 v[200:203], a[12:15], a[160:163], v[200:203]\n" \
"ds_read_b64_tr_b16 a[0:1], v33 offset:18528\n" \
"ds_read_b64_tr_b16 a[2:3], v33 offset:23136\n" \
"v_mfma_f32_16x16x32_bf16 v[200:203], a[16:19], a[164:167], v[200:203]\n" \
"buffer_load_dwordx4 v[128:131], v54, s[56:59], s96 offen offset:0 sc1\n" \
"v_mfma_f32_16x16x32_bf16 v[200:203], a[20:23], a[168:171], v[200:203]\n" \
"s_waitcnt lgkmcnt(15)\n" \
"v_mfma_f32_16x16x32_bf16 v[200:203], a[24:27], a[172:175], v[200:203]\n" \
"v_mfma_f32_16x16x32_bf16 v[204:207], a[24:27], a[188:191], v[204:207]\n" \
"buffer_load_dwordx4 v[136:139], v54, s[56:59], s96 offen offset:2048 sc1\n" \
"s_waitcnt vmcnt(13)\n" \
"s_mov_b64 exec, 1\n" \
"buffer_store_dword v39, off, s[56:59], s95 offset:28 sc1\n" \
"s_mov_b64 exec, -1\n" \
"v_mfma_f32_16x16x32_bf16 v[204:207], a[20:23], a[184:187], v[204:207]\n" \
"ds_read_b64_tr_b16 a[20:21], v34 offset:9312\n" \
"ds_read_b64_tr_b16 a[22:23], v34 offset:13920\n" \
"v_mfma_f32_16x16x32_bf16 v[204:207], a[16:19], a[180:183], v[204:207]\n" \
"ds_read_b64_tr_b16 a[16:17], v34 offset:96\n" \
"ds_read_b64_tr_b16 a[18:19], v34 offset:4704\n" \
"v_mfma_f32_16x16x32_bf16 v[204:207], a[12:15], a[176:179], v[204:207]\n" \
"ds_read_b64_tr_b16 a[12:13], v33 offset:46176\n" \
"ds_read_b64_tr_b16 a[14:15], v33 offset:50784\n" \
"v_mfma_f32_16x16x32_bf16 v[200:203], a[28:31], a[220:223], v[200:203]\n" \
"ds_read_b64_tr_b16 a[24:25], v34 offset:18528\n" \
"ds_read_b64_tr_b16 a[26:27], v34 offset:23136\n" \
"v_mfma_f32_16x16x32_bf16 v[200:203], a[192:195], a[224:227], v[200:203]\n" \
"v_readlane_b32 s95, v11, 40\n" \
"v_mfma_f32_16x16x32_bf16 v[200:203], a[196:199], a[228:231], v[200:203]\n" \
"s_waitcnt lgkmcnt(12)\n" \
"v_mfma_f32_16x16x32_bf16 v[200:203], a[200:203], a[232:235], v[200:203]\n" \
"v_mfma_f32_16x16x32_bf16 v[204:207], a[200:203], a[248:251], v[204:207]\n" \
"ds_read_b64_tr_b16 a[200:201], v10 offset:46304\n" \
"ds_read_b64_tr_b16 a[202:203], v10 offset:50912\n" \
"v_mfma_f32_16x16x32_bf16 v[204:207], a[196:199], a[244:247], v[204:207]\n" \
"ds_read_b64_tr_b16 a[196:197], v10 offset:37088\n" \
"ds_read_b64_tr_b16 a[198:199], v10 offset:41696\n" \
"v_mfma_f32_16x16x32_bf16 v[204:207], a[192:195], a[240:243], v[204:207]\n" \
"ds_read_b64_tr_b16 a[192:193], v10 offset:27872\n" \
"v_cvt_pk_bf16_f32 v200, v200, v201\n" \
"v_cvt_pk_bf16_f32 v201, v202, v203\n" \
"v_mfma_f32_16x16x32_bf16 v[204:207], a[28:31], a[236:239], v[204:207]\n" \
"ds_read_b64_tr_b16 a[28:29], v33 offset:96\n" \
"ds_read_b64_tr_b16 a[30:31], v33 offset:4704\n" \
"v_mfma_f32_16x16x32_bf16 v[208:211], a[204:207], a[32:35], v[208:211]\n" \
"ds_read_b64_tr_b16 a[194:195], v10 offset:32480\n" \
"v_mfma_f32_16x16x32_bf16 v[208:211], a[208:211], a[36:39], v[208:211]\n" \
"v_mfma_f32_16x16x32_bf16 v[208:211], a[212:215], a[40:43], v[208:211]\n" \
"s_waitcnt lgkmcnt(15)\n" \
"v_mfma_f32_16x16x32_bf16 v[208:211], a[216:219], a[44:47], v[208:211]\n" \
"v_cvt_pk_bf16_f32 v202, v204, v205\n" \
"v_cvt_pk_bf16_f32 v203, v206, v207\n" \
"v_mfma_f32_16x16x32_bf16 v[212:215], a[216:219], a[60:63], v[212:215]\n" \
"buffer_store_dwordx4 v[200:203], v20, s[56:59], s66 offen offset:1024 sc1\n" \
"s_add_u32 s66, s66, 0x2800\n" \
"v_mfma_f32_16x16x32_bf16 v[212:215], a[212:215], a[56:59], v[212:215]\n" \
"ds_read_b64_tr_b16 a[212:213], v33 offset:27872\n" \
"ds_read_b64_tr_b16 a[214:215], v33 offset:32480\n" \
"v_mfma_f32_16x16x32_bf16 v[212:215], a[208:211], a[52:55], v[212:215]\n" \
"ds_read_b64_tr_b16 a[208:209], v33 offset:18656\n" \
"ds_read_b64_tr_b16 a[210:211], v33 offset:23264\n" \
"v_mfma_f32_16x16x32_bf16 v[212:215], a[204:207], a[48:51], v[212:215]\n" \
"ds_read_b64_tr_b16 a[204:205], v10 offset:55520\n" \
"ds_read_b64_tr_b16 a[206:207], v10 offset:60128\n" \
"v_mfma_f32_16x16x32_bf16 v[208:211], a[0:3], a[160:163], v[208:211]\n" \
"ds_read_b64_tr_b16 a[216:217], v33 offset:37088\n" \
"ds_read_b64_tr_b16 a[218:219], v33 offset:41696\n" \
"v_mfma_f32_16x16x32_bf16 v[208:211], a[4:7], a[164:167], v[208:211]\n" \
"v_mfma_f32_16x16x32_bf16 v[208:211], a[8:11], a[168:171], v[208:211]\n" \
"s_waitcnt lgkmcnt(14)\n" \
"v_mfma_f32_16x16x32_bf16 v[208:211], a[12:15], a[172:175], v[208:211]\n" \
"v_mfma_f32_16x16x32_bf16 v[212:215], a[12:15], a[188:191], v[212:215]\n" \
"ds_read_b64_tr_b16 a[12:13], v34 offset:18656\n" \
"ds_read_b64_tr_b16 a[14:15], v34 offset:23264\n" \
"s_waitcnt vmcnt(12)\n" \
"s_mov_b64 exec, 1\n" \
"buffer_store_dword v39, off, s[56:59], s97 offset:32 sc1\n" \
"s_mov_b64 exec, -1\n" \
"v_mfma_f32_16x16x32_bf16 v[212:215], a[8:11], a[184:187], v[212:215]\n" \
"ds_read_b64_tr_b16 a[8:9], v34 offset:9440\n" \
"ds_read_b64_tr_b16 a[10:11], v34 offset:14048\n" \
"v_mfma_f32_16x16x32_bf16 v[212:215], a[4:7], a[180:183], v[212:215]\n" \
"ds_read_b64_tr_b16 a[4:5], v34 offset:224\n" \
"ds_read_b64_tr_b16 a[6:7], v34 offset:4832\n" \
"v_mfma_f32_16x16x32_bf16 v[212:215], a[0:3], a[176:179], v[212:215]\n" \
"ds_read_b64_tr_b16 a[0:1], v33 offset:46304\n" \
"ds_read_b64_tr_b16 a[2:3], v33 offset:50912\n" \
"v_mfma_f32_16x16x32_bf16 v[208:211], a[16:19], a[220:223], v[208:211]\n" \
"s_waitcnt vmcnt(11)\n" \
"v_cmp_eq_u32_e64 s[98:99], s62, v37\n" \
"buffer_load_dword v37, v40, s[56:59], s74 offen sc1\n" \
"v_mfma_f32_16x16x32_bf16 v[208:211], a[20:23], a[224:227], v[208:211]\n" \
"s_and_b32 s99, s98, 0x20\n" \
"s_or_b32 s84, s84, s99\n" \
"s_bitcmp1_b32 s84, 5\n" \
"s_cselect_b32 s65, s63, 0x40000000\n" \
"v_mfma_f32_16x16x32_bf16 v[208:211], a[24:27], a[228:231], v[208:211]\n" \
"s_waitcnt lgkmcnt(12)\n" \
"v_mfma_f32_16x16x32_bf16 v[208:211], a[28:31], a[232:235], v[208:211]\n" \
"v_mfma_f32_16x16x32_bf16 v[212:215], a[28:31], a[248:251], v[212:215]\n" \
"buffer_load_dwordx4 v[148:151], v47, s[56:59], s65 offen offset:0 sc1\n" \
"v_mfma_f32_16x16x32_bf16 v[212:215], a[24:27], a[244:247], v[212:215]\n" \
"ds_read_b64_tr_b16 a[24:25], v36 offset:2048\n" \
"ds_read_b64_tr_b16 a[26:27], v36 offset:3072\n" \
"v_mfma_f32_16x16x32_bf16 v[212:215], a[20:23], a[240:243], v[212:215]\n" \
"ds_read_b64_tr_b16 a[20:21], v36 offset:0\n" \
"ds_read_b64_tr_b16 a[22:23], v36 offset:1024\n" \
"v_cvt_pk_bf16_f32 v208, v208, v209\n" \
"v_cvt_pk_bf16_f32 v209, v210, v211\n" \
"v_mfma_f32_16x16x32_bf16 v[212:215], a[16:19], a[236:239], v[212:215]\n" \
"ds_read_b64_tr_b16 a[16:17], v33 offset:224\n" \
"ds_read_b64_tr_b16 a[18:19], v33 offset:4832\n" \
"v_mfma_f32_16x16x32_bf16 v[240:243], a[192:195], a[32:35], v[240:243]\n" \
"buffer_load_dwordx4 v[220:223], v47, s[56:59], s65 offen offset:1024 sc1\n" \
"v_mfma_f32_16x16x32_bf16 v[240:243], a[196:199], a[36:39], v[240:243]\n" \
"ds_read_b64_tr_b16 a[28:29], v36 offset:32\n" \
"ds_read_b64_tr_b16 a[30:31], v36 offset:1056\n" \
"v_mfma_f32_16x16x32_bf16 v[240:243], a[200:203], a[40:43], v[240:243]\n" \
"v_cvt_pk_bf16_f32 v210, v212, v213\n" \
"v_cvt_pk_bf16_f32 v211, v214, v215\n" \
"s_waitcnt lgkmcnt(10)\n" \
"v_mfma_f32_16x16x32_bf16 v[240:243], a[204:207], a[44:47], v[240:243]\n" \
"buffer_store_dwordx4 v[208:211], v20, s[56:59], s67 offen offset:1024 sc1\n" \
"s_add_u32 s67, s67, 0x2000\n" \
"v_mfma_f32_16x16x32_bf16 v[244:247], a[204:207], a[60:63], v[244:247]\n" \
"v_mfma_f32_16x16x32_bf16 v[244:247], a[200:203], a[56:59], v[244:247]\n" \
"v_mfma_f32_16x16x32_bf16 v[244:247], a[196:199], a[52:55], v[244:247]\n" \
"v_mfma_f32_16x16x32_bf16 v[244:247], a[192:195], a[48:51], v[244:247]\n" \
"ds_read_b64_tr_b16 a[192:193], v36 offset:2080\n" \
"ds_read_b64_tr_b16 a[194:195], v36 offset:3104\n" \
"v_mfma_f32_16x16x32_bf16 v[168:171], a[208:211], a[128:131], v[168:171]\n" \
"v_mfma_f32_16x16x32_bf16 v[168:171], a[212:215], a[132:135], v[168:171]\n" \
"v_mfma_f32_16x16x32_bf16 v[168:171], a[216:219], a[136:139], v[168:171]\n" \
"s_waitcnt lgkmcnt(9)\n" \
"v_mfma_f32_16x16x32_bf16 v[168:171], a[0:3], a[140:143], v[168:171]\n" \
"v_mfma_f32_16x16x32_bf16 v[172:175], a[0:3], a[156:159], v[172:175]\n" \
"v_mfma_f32_16x16x32_bf16 v[172:175], a[216:219], a[152:155], v[172:175]\n" \
"s_waitcnt vmcnt(10)\n" \
"s_mov_b64 exec, 1\n" \
"buffer_store_dword v39, off, s[56:59], s64 offset:36 sc1\n" \
"s_mov_b64 exec, -1\n" \
"v_mfma_f32_16x16x32_bf16 v[172:175], a[212:215], a[148:151], v[172:175]\n" \
"v_mfma_f32_16x16x32_bf16 v[172:175], a[208:211], a[144:147], v[172:175]\n" \
"v_mfma_f32_16x16x32_bf16 v[240:243], a[208:211], a[160:163], v[240:243]\n" \
"v_mfma_f32_16x16x32_bf16 v[240:243], a[212:215], a[164:167], v[240:243]\n" \
"v_mfma_f32_16x16x32_bf16 v[240:243], a[216:219], a[168:171], v[240:243]\n" \
"v_mfma_f32_16x16x32_bf16 v[240:243], a[0:3], a[172:175], v[240:243]\n" \
"v_mfma_f32_16x16x32_bf16 v[244:247], a[0:3], a[188:191], v[244:247]\n" \
"v_mfma_f32_16x16x32_bf16 v[244:247], a[216:219], a[184:187], v[244:247]\n" \
"v_mfma_f32_16x16x32_bf16 v[244:247], a[212:215], a[180:183], v[244:247]\n" \
"v_mfma_f32_16x16x32_bf16 v[244:247], a[208:211], a[176:179], v[244:247]\n" \
"v_mfma_f32_16x16x32_bf16 v[168:171], a[4:7], a[96:99], v[168:171]\n" \
"v_mfma_f32_16x16x32_bf16 v[168:171], a[8:11], a[100:103], v[168:171]\n" \
"v_mfma_f32_16x16x32_bf16 v[168:171], a[12:15], a[104:107], v[168:171]\n" \
"s_waitcnt vmcnt(6)\n" \
"s_mov_b64 exec, 1\n" \
"buffer_store_dword v39, off, s[56:59], s66 offset:40 sc1\n" \
"s_mov_b64 exec, -1\n" \
"s_waitcnt lgkmcnt(0)\n" \
"v_mfma_f32_16x16x32_bf16 v[168:171], a[16:19], a[108:111], v[168:171]\n" \
"v_mfma_f32_16x16x32_bf16 v[172:175], a[16:19], a[124:127], v[172:175]\n" \
"v_mfma_f32_16x16x32_bf16 v[172:175], a[12:15], a[120:123], v[172:175]\n" \
"v_mfma_f32_16x16x32_bf16 v[172:175], a[8:11], a[116:119], v[172:175]\n" \
"v_mfma_f32_16x16x32_bf16 v[172:175], a[4:7], a[112:115], v[172:175]\n" \
"v_mfma_f32_16x16x32_bf16 v[240:243], a[4:7], a[220:223], v[240:243]\n" \
"v_mfma_f32_16x16x32_bf16 v[240:243], a[8:11], a[224:227], v[240:243]\n" \
"v_mfma_f32_16x16x32_bf16 v[240:243], a[12:15], a[228:231], v[240:243]\n" \
"v_mfma_f32_16x16x32_bf16 v[240:243], a[16:19], a[232:235], v[240:243]\n" \
"v_mfma_f32_16x16x32_bf16 v[244:247], a[16:19], a[248:251], v[244:247]\n" \
"v_mfma_f32_16x16x32_bf16 v[244:247], a[12:15], a[244:247], v[244:247]\n" \
"v_mfma_f32_16x16x32_bf16 v[244:247], a[8:11], a[240:243], v[244:247]\n" \
"v_mfma_f32_16x16x32_bf16 v[244:247], a[4:7], a[236:239], v[244:247]\n" \
"v_mfma_f32_16x16x32_bf16 v[168:171], a[20:23], v[64:67], v[168:171]\n" \
"v_mfma_f32_16x16x32_bf16 v[172:175], a[20:23], v[72:75], v[172:175]\n" \
"v_mfma_f32_16x16x32_bf16 v[240:243], a[20:23], v[80:83], v[240:243]\n" \
"v_mfma_f32_16x16x32_bf16 v[244:247], a[20:23], v[88:91], v[244:247]\n" \
"v_mfma_f32_16x16x32_bf16 v[168:171], a[24:27], v[68:71], v[168:171]\n" \
"v_mfma_f32_16x16x32_bf16 v[172:175], a[24:27], v[76:79], v[172:175]\n" \
"v_mfma_f32_16x16x32_bf16 v[240:243], a[24:27], v[84:87], v[240:243]\n" \
"s_waitcnt vmcnt(5)\n" \
"v_cmp_eq_u32_e64 s[98:99], s62, v37\n" \
"v_mfma_f32_16x16x32_bf16 v[244:247], a[24:27], v[92:95], v[244:247]\n" \
"s_and_b32 s99, s98, 0xfc0\n" \
"s_or_b32 s84, s84, s99\n" \
"s_bitcmp1_b32 s84, 6\n" \
"s_cselect_b32 s68, s63, 0x40000000\n" \
"s_bitcmp1_b32 s84, 7\n" \
"s_cselect_b32 s69, s63, 0x40000000\n" \
"s_bitcmp1_b32 s84, 8\n" \
"s_cselect_b32 s70, s63, 0x40000000\n" \
"s_bitcmp1_b32 s84, 9\n" \
"s_cselect_b32 s71, s63, 0x40000000\n" \
"s_bitcmp1_b32 s84, 10\n" \
"s_cselect_b32 s72, s63, 0x40000000\n" \
"s_bitcmp1_b32 s84, 11\n" \
"s_cselect_b32 s73, s63, 0x40000000\n" \
"buffer_load_dwordx4 v[156:159], v47, s[56:59], s68 offen offset:2048 sc1\n" \
"v_mfma_f32_16x16x32_bf16 v[56:59], a[28:31], v[96:99], 0\n" \
"s_waitcnt vmcnt(3)\n" \
"s_mov_b64 exec, 1\n" \
"buffer_store_dword v39, off, s[56:59], s67 offset:44 sc1\n" \
"s_mov_b64 exec, -1\n" \
"v_mfma_f32_16x16x32_bf16 v[60:63], a[28:31], v[104:107], 0\n" \
"v_mfma_f32_16x16x32_bf16 v[56:59], a[192:195], v[100:103], v[56:59]\n" \
"v_mfma_f32_16x16x32_bf16 v[60:63], a[192:195], v[108:111], v[60:63]\n" \
"buffer_load_dwordx4 v[228:231], v47, s[56:59], s68 offen offset:3072 sc1\n" \
"buffer_load_dwordx4 v[164:167], v54, s[56:59], s69 offen offset:0 sc1\n" \
"buffer_load_dwordx4 v[236:239], v54, s[56:59], s69 offen offset:1024 sc1\n" \
"buffer_load_dwordx4 v[188:191], v20, s[56:59], s70 offen offset:1024 sc1\n" \
"buffer_load_dwordx4 v[196:199], v20, s[56:59], s71 offen offset:3072 sc1\n" \
"buffer_load_dwordx4 v[204:207], v38, s[56:59], s72 offen offset:1024 sc1\n" \
"buffer_load_dwordx4 v[212:215], v38, s[56:59], s73 offen offset:3072 sc1\n" \
"s_andn2_b32 s99, 0xff, s84\n" \
"s_cbranch_scc1 L_sx0_%=\n" \
"L_rx0_%=:\n" \
"v_mfma_f32_16x16x32_bf16 v[168:171], v[50:53], v[116:119], v[168:171]\n" \
"v_mfma_f32_16x16x32_bf16 v[172:175], v[48:51], v[116:119], v[172:175]\n" \
"v_mfma_f32_16x16x32_bf16 v[168:171], v[50:53], v[124:127], v[168:171]\n" \
"v_mfma_f32_16x16x32_bf16 v[172:175], v[48:51], v[124:127], v[172:175]\n" \
"v_mfma_f32_16x16x32_bf16 v[168:171], v[50:53], v[132:135], v[168:171]\n" \
"v_mfma_f32_16x16x32_bf16 v[172:175], v[48:51], v[132:135], v[172:175]\n" \
"v_mfma_f32_16x16x32_bf16 v[168:171], v[50:53], v[140:143], v[168:171]\n" \
"v_mfma_f32_16x16x32_bf16 v[172:175], v[48:51], v[140:143], v[172:175]\n" \
"v_mfma_f32_16x16x32_bf16 v[56:59], v[50:53], v[176:179], v[56:59]\n" \
"v_mfma_f32_16x16x32_bf16 v[60:63], v[48:51], v[176:179], v[60:63]\n" \
"v_mfma_f32_16x16x32_bf16 v[56:59], v[50:53], v[248:251], v[56:59]\n" \
"v_mfma_f32_16x16x32_bf16 v[60:63], v[48:51], v[248:251], v[60:63]\n" \
"v_mfma_f32_16x16x32_bf16 v[56:59], v[50:53], v[180:183], v[56:59]\n" \
"v_mfma_f32_16x16x32_bf16 v[60:63], v[48:51], v[180:183], v[60:63]\n" \
"v_mfma_f32_16x16x32_bf16 v[56:59], v[50:53], v[252:255], v[56:59]\n" \
"v_mfma_f32_16x16x32_bf16 v[60:63], v[48:51], v[252:255], v[60:63]\n" \
"v_mfma_f32_16x16x32_bf16 v[56:59], v[50:53], v[112:115], v[56:59]\n" \
"v_mfma_f32_16x16x32_bf16 v[60:63], v[48:51], v[112:115], v[60:63]\n" \
"v_mfma_f32_16x16x32_bf16 v[56:59], v[50:53], v[120:123], v[56:59]\n" \
"v_mfma_f32_16x16x32_bf16 v[60:63], v[48:51], v[120:123], v[60:63]\n" \
"v_mfma_f32_16x16x32_bf16 v[56:59], v[50:53], v[128:131], v[56:59]\n" \
"v_mfma_f32_16x16x32_bf16 v[60:63], v[48:51], v[128:131], v[60:63]\n" \
"v_mfma_f32_16x16x32_bf16 v[56:59], v[50:53], v[136:139], v[56:59]\n" \
"v_mfma_f32_16x16x32_bf16 v[60:63], v[48:51], v[136:139], v[60:63]\n" \
"v_mfma_f32_16x16x32_bf16 v[168:171], v[50:53], v[148:151], v[168:171]\n" \
"v_mfma_f32_16x16x32_bf16 v[172:175], v[48:51], v[148:151], v[172:175]\n" \
"v_mfma_f32_16x16x32_bf16 v[240:243], v[50:53], v[220:223], v[240:243]\n" \
"v_mfma_f32_16x16x32_bf16 v[244:247], v[48:51], v[220:223], v[244:247]\n" \
"s_waitcnt vmcnt(8)\n" \
"v_mfma_f32_16x16x32_bf16 v[168:171], v[50:53], v[156:159], v[168:171]\n" \
"v_mfma_f32_16x16x32_bf16 v[172:175], v[48:51], v[156:159], v[172:175]\n" \
"v_cvt_pk_bf16_f32 v56, v56, v57\n" \
"v_cvt_pk_bf16_f32 v57, v58, v59\n" \
"v_cvt_pk_bf16_f32 v58, v60, v61\n" \
"v_cvt_pk_bf16_f32 v59, v62, v63\n" \
"s_waitcnt vmcnt(6)\n" \
"v_mfma_f32_16x16x32_bf16 v[240:243], v[50:53], v[228:231], v[240:243]\n" \
"v_permlane16_swap_b32_e32 v56, v58\n" \
"v_mfma_f32_16x16x32_bf16 v[244:247], v[48:51], v[228:231], v[244:247]\n" \
"v_permlane16_swap_b32_e32 v57, v59\n" \
"buffer_store_dwordx4 v[56:59], v42, s[88:91], s93 offen sc0 sc1\n" \
"s_waitcnt vmcnt(6)\n" \
"v_mfma_f32_16x16x32_bf16 v[168:171], v[50:53], v[164:167], v[168:171]\n" \
"v_mfma_f32_16x16x32_bf16 v[172:175], v[48:51], v[164:167], v[172:175]\n" \
"s_waitcnt vmcnt(5)\n" \
"v_mfma_f32_16x16x32_bf16 v[240:243], v[50:53], v[236:239], v[240:243]\n" \
"v_mfma_f32_16x16x32_bf16 v[244:247], v[48:51], v[236:239], v[244:247]\n" \
"s_andn2_b32 s99, 0xf00, s84\n" \
"s_cbranch_scc1 L_sx1_%=\n" \
"L_rx1_%=:\n" \
"s_waitcnt vmcnt(4)\n" \
"v_mfma_f32_16x16x32_bf16 v[240:243], v[50:53], v[188:191], v[240:243]\n" \
"v_cvt_pk_bf16_f32 v168, v168, v169\n" \
"v_cvt_pk_bf16_f32 v169, v170, v171\n" \
"v_mfma_f32_16x16x32_bf16 v[244:247], v[48:51], v[188:191], v[244:247]\n" \
"v_cvt_pk_bf16_f32 v170, v172, v173\n" \
"v_cvt_pk_bf16_f32 v171, v174, v175\n" \
"s_waitcnt vmcnt(3)\n" \
"v_mfma_f32_16x16x32_bf16 v[240:243], v[50:53], v[196:199], v[240:243]\n" \
"v_permlane16_swap_b32_e32 v168, v170\n" \
"v_mfma_f32_16x16x32_bf16 v[244:247], v[48:51], v[196:199], v[244:247]\n" \
"v_permlane16_swap_b32_e32 v169, v171\n" \
"buffer_store_dwordx4 v[168:171], v42, s[88:91], s94 offen sc0 sc1\n" \
"s_waitcnt vmcnt(3)\n" \
"v_mfma_f32_16x16x32_bf16 v[240:243], v[50:53], v[204:207], v[240:243]\n" \
"v_mfma_f32_16x16x32_bf16 v[244:247], v[48:51], v[204:207], v[244:247]\n" \
"s_waitcnt vmcnt(2)\n" \
"v_mfma_f32_16x16x32_bf16 v[240:243], v[50:53], v[212:215], v[240:243]\n" \
"v_mfma_f32_16x16x32_bf16 v[244:247], v[48:51], v[212:215], v[244:247]\n" \
"s_nop 6\n" \
"v_cvt_pk_bf16_f32 v240, v240, v241\n" \
"v_cvt_pk_bf16_f32 v241, v242, v243\n" \
"v_cvt_pk_bf16_f32 v242, v244, v245\n" \
"v_cvt_pk_bf16_f32 v243, v246, v247\n" \
"s_nop 0\n" \
"v_permlane16_swap_b32_e32 v240, v242\n" \
"v_permlane16_swap_b32_e32 v241, v243\n" \
"buffer_store_dwordx4 v[240:243], v42, s[88:91], s95 offen sc0 sc1\n"
#define ATT_VCLOB "v21","v22","v23","v32","v33","v34","v35","v36","v37","v38","v39","v40","v41","v42","v43","v44","v45","v46","v47","v48","v49","v50","v51","v52","v53","v54","v55","v56","v57","v58","v59","v60","v61","v62","v63","v64","v65","v66","v67","v68","v69","v70","v71","v72","v73","v74","v75","v76","v77","v78","v79","v80","v81","v82","v83","v84","v85","v86","v87","v88","v89","v90","v91","v92","v93","v94","v95","v96","v97","v98","v99","v100","v101","v102","v103","v104","v105","v106","v107","v108","v109","v110","v111","v112","v113","v114","v115","v116","v117","v118","v119","v120","v121","v122","v123","v124","v125","v126","v127","v128","v129","v130","v131","v132","v133","v134","v135","v136","v137","v138","v139","v140","v141","v142","v143","v144","v145","v146","v147","v148","v149","v150","v151","v152","v153","v154","v155","v156","v157","v158","v159","v160","v161","v162","v163","v164","v165","v166","v167","v168","v169","v170","v171","v172","v173","v174","v175","v176","v177","v178","v179","v180","v181","v182","v183","v184","v185","v186","v187","v188","v189","v190","v191","v192","v193","v194","v195","v196","v197","v198","v199","v200","v201","v202","v203","v204","v205","v206","v207","v208","v209","v210","v211","v212","v213","v214","v215","v216","v217","v218","v219","v220","v221","v222","v223","v224","v225","v226","v227","v228","v229","v230","v231","v232","v233","v234","v235","v236","v237","v238","v239","v240","v241","v242","v243","v244","v245","v246","v247","v248","v249","v250","v251","v252","v253","v254","v255"
#define ATT_SCLOB "s63","s64","s65","s66","s67","s68","s69","s70","s71","s72","s73","s74","s75","s76","s77","s78","s79","s80","s81","s82","s83","s84","s85","s86","s87","s88","s89","s90","s91","s92","s93","s95","s96","s97","s98","s99","s100","s101"
enum { T_fo, T_fa, T_fas, T_fb, T_fbs, T_f8, T_mk8, T_wol, T_woh, T_wbl, T_wbh, T_outl, T_outh, T_r1l, T_r1h, T_r1n, T_r1s, T_m0, T_m1, T_mw, T_m4, T_ml, T_w0, T_w1, T_w2, T_w3, T_A1, T_A2, T_A3, T_B1, T_B2, T_B3, T_Ao, T_Bo, T_Al, T_Bl, T_l8, T_xr, T_x8r, T_oA, T_oB, T_o8, T_x0, T_x1, T_x2, T_x3, T_x4, T_x5, T_x6, T_x8, T_ep, T_xf, T_kf };

__device__ __forceinline__ void out_consts(u32x4 (&vp)[3], const u16* wo, const u16* wob, const u16* out, const unsigned b, const unsigned w, const unsigned lane) {
    const unsigned sp = b & 7u, nt = (b >> 3) & 3u, j = b >> 5, head = 4u * sp + nt, OOR = 0x40000000u;
    unsigned vw[9];
#pragma unroll
    for (unsigned i = 0; i < 9u; ++i) {
        const unsigned q = 64u * i + lane, row = __umul24(q, 3641u) >> 16, c = q - __umul24(row, 18u);
        vw[i] = __umul24(row, DM * 2) + (c < 16u ? (c * 16u + sp * 32u + 32u) & 255u : c * 16u) - (i & 3u) * 1024u;
    }
    const unsigned row = 16u * w + (lane >> 2), r9 = __umul24(16u * (row >> 5) + (row & 12u) + 2u * ((row >> 4) & 1u) + ((row >> 1) & 1u), 18u) + 16u + (row & 1u);
    unsigned vwl = __umul24(r9, DM * 2) + ((lane & 2u) ? 256u : sp * 32u) + (lane & 1u) * 16u, vrd = __umul24(lane >> 2, 288u) + (lane & 3u) * 8u;
    asm volatile("" : "+v"(vw[0]), "+v"(vw[1]), "+v"(vw[2]), "+v"(vw[3]), "+v"(vw[4]), "+v"(vw[5]), "+v"(vw[6]), "+v"(vw[7]), "+v"(vw[8]), "+v"(vwl), "+v"(vrd));
    unsigned tab = 0;
#define TB(n, v) asm("v_writelane_b32 %0, %1, %2" : "+v"(tab) : "s"((unsigned)(v)), "n"((int)T_##n))
    const unsigned FJ = (unsigned)WS_F + (F_J + 640u * j) * 4u, mt = 2u * (w & 2u) + (w & 1u), w2 = w < 2u ? w : 2u, hw = head * 147456u;
    const unsigned FR = (unsigned)WS_F + F_R * 4u + (8u * j + sp) * 1024u;
    TB(fo, FJ + 4u * head); TB(fa, FR + (w | 2u) * 256u); TB(fas, FR + w * 256u + nt * 4u);
    TB(fb, FR + 128u + w * 256u); TB(fbs, FR + 128u + w * 256u + nt * 4u);
    TB(ep, b + w ? OOR : (unsigned)WS_F + F_EPOCH * 4u);
    const u16* pwo = wo + nt * 144u; const u16* pwb = wob + nt * 144u; const u16* pr1 = w < 3u ? pwo : pwb;
    TB(wol, PLO(pwo)); TB(woh, PHI(pwo) & 0xffffu); TB(wbl, PLO(pwb)); TB(wbh, PHI(pwb) & 0xffffu); TB(outl, PLO(out)); TB(outh, PHI(out) & 0xffffu); TB(r1l, PLO(pr1)); TB(r1h, PHI(pr1) & 0xffffu);
    TB(r1n, (w < 3u ? 72u * sp + 71u : 4095u) * 1152u + 288u); TB(r1s, w < 3u ? (72u * sp + 32u * w) * 1152u : hw + 110592u);
    TB(m0, 131072u + 9216u * w2); TB(w0, hw + 36864u * w2); TB(m1, w < 3u ? 9216u * w : 73728u); TB(mw, 9216u * w); TB(m4, w < 3u ? 131072u + 9216u * w : 64512u); TB(ml, 119808u + 1024u * w);
    const unsigned tA = (unsigned)WS_AF + (8u * j + mt) * (KS_TOT * 1024u), tB = (unsigned)WS_AF + (120u - 8u * j + mt) * (KS_TOT * 1024u);
#define HK(k) (4u * sp + ((nt + k) & 3u))
    TB(w1, HK(1u) * 147456u + w * 36864u); TB(w2, HK(2u) * 147456u + w * 36864u); TB(w3, HK(3u) * 147456u + w * 36864u);
    TB(A1, tA + 18432u + HK(1u) * 4096u); TB(A2, tA + 18432u + HK(2u) * 4096u); TB(A3, tA + 18432u + HK(3u) * 4096u);
    TB(B1, tB + 18432u + HK(1u) * 4096u); TB(B2, tB + 18432u + HK(2u) * 4096u); TB(B3, tB + 18432u + HK(3u) * 4096u);
    TB(Ao, tA + sp * 2048u); TB(Bo, tB + sp * 2048u); TB(Al, tA + 16384u); TB(Bl, tB + 16384u);
    const bool red = w == (sp >> 1), blk = sp & 1u;
    const unsigned xg = (unsigned)WS_X + (4u * j + nt) * 589824u + w * 16384u, oA = (128u * j + 16u * mt) * 1152u + nt * 288u, oB = (1920u - 128u * j + 16u * mt) * 1152u + nt * 288u;
    TB(l8, red ? (blk ? tB : tA) + 16384u : OOR); TB(xr, xg + sp * 65536u); TB(x8r, red ? xg + 524288u + (sp & 1u) * 1024u : OOR);
    TB(oA, oA + sp * 32u); TB(oB, oB + sp * 32u); TB(o8, red ? (blk ? oB : oA) + 256u : OOR); TB(x8, xg + 524288u + sp * 2048u);
    TB(f8, xg + w * 131072u + 14400u + sp * 4u); TB(mk8, red ? 0xff0000u : 0u); TB(xf, xg + sp * 65536u + 14336u);
    TB(kf, (unsigned)WS_F + (F_KV + head * 64u) * 4u);
#define XI(i) TB(x##i, xg + ((sp + 1u + i) & 7u) * 65536u + i * 2048u)
    XI(0); XI(1); XI(2); XI(3); XI(4); XI(5); XI(6);
#undef XI
#undef HK
#undef TB
    vp[0] = (u32x4){vw[0], vw[1], vw[2], vw[3]}; vp[1] = (u32x4){vw[4], vw[5], vw[6], vw[7]}; vp[2] = (u32x4){vw[8], vwl, vrd, tab};
}
__global__ __launch_bounds__(256, 1) __attribute__((aligned(4096))) void fused_kernel(const u16* __restrict__ x, const u16* __restrict__ ckv, const u16* __restrict__ wq, const u16* __restrict__ wuk,
                                                       const u16* __restrict__ wuv, const u16* __restrict__ wqkv, const u16* __restrict__ gw,
                                                       const u16* __restrict__ wo, const u16* __restrict__ wob, char* __restrict__ ws, u16* __restrict__ out) {
    using namespace att;
    extern __shared__ __attribute__((aligned(16))) char lds[];
    unsigned wid = rfl(threadIdx.x >> 6);
    code_touch((blockIdx.x >> 3) * 4u + wid, (unsigned)(size_t)lds + 151552u + wid * 512u);
    SBAR();
    asm volatile("" :: "s"(x), "s"(ckv), "s"(wq), "s"(wuk), "s"(wuv), "s"(wqkv), "s"(gw), "s"(wo), "s"(wob), "s"(ws), "s"(out));
    unsigned tag;
    int b = blockIdx.x;
    u32x4 vp[5], sa[5]; unsigned sd[2];
    const unsigned vl = proj_phase(x, ckv, wq, wuk, wuv, wqkv, gw, wo, wob, out, ws, lds, wid, tag, vp, sa, sd);
    asm volatile("" : "+s"(ws));
    unsigned* const flags = (unsigned*)(ws + WS_F);
    RTS(2);
#define AST(L) do {} while (0)
#define SD(J8, H, W) ((unsigned)WS_AF + (((J8) + 2u * ((W) & 2u) + ((W) & 1u)) * KS_TOT + 18u + 4u * (H)) * 1024u)
#define SRDA (u32x4){PLO(ws), PHI(ws) & 0xffffu, (unsigned)WS_L, 0x00020000u}
#define ATT_TAIL "s_endpgm\n"
    unsigned abad;
    asm volatile(ATT_ASM ATT_TAIL "L_end_%=:\n s_nop 1"
                 : "=&{s94}"(abad)
                 : "{v[0:3]}"(vp[0]), "{v[4:7]}"(vp[1]), "{v[8:11]}"(vp[2]), "{v[12:15]}"(vp[3]), "{v[16:19]}"(vp[4]), "{v20}"(vl), "{s[56:59]}"(sa[4]), "{s60}"(sd[0]), "{s61}"(sd[1]), "{s62}"(tag),
                   "{s[8:11]}"(sa[0]), "{s[12:15]}"(sa[1]), "{s[16:19]}"(sa[2]), "{s[20:23]}"(sa[3])
                 : "memory", "m0", "vcc", "scc", ATT_VCLOB, ATT_SCLOB, ACLOB_LO, ACLOB_HI);
    {
        const u16* const __attribute__((address_space(4)))* ka = (const u16* const __attribute__((address_space(4)))*)__builtin_amdgcn_kernarg_segment_ptr();
        unsigned b2 = b, w2 = wid;
        asm volatile("s_waitcnt vmcnt(0)" : "+s"(ka), "+s"(b2), "+s"(w2), "+s"(ws) :: "memory");
        const unsigned j2 = b2 >> 5, p2 = w2 & 1u, h2 = (b2 & 7u) * 4u + ((b2 >> 3) & 3u);
#pragma unroll 1
        for (unsigned i = w2 & 2u; i < ((w2 & 2u) ? (abad ? 8u : 4u) : 2u); ++i)
            att_exact(ka[0], ka[2], ws, h2, (i < 4u ? 1920u - 128u * j2 : 128u * j2 - 128u) + 32u * i + 16u * p2, *reinterpret_cast<const float*>(lds + LDS_G + (32u * i + 16u * p2 + (lane_id() & 15u)) * 4u));
        asm volatile("s_waitcnt vmcnt(0)" ::: "memory");
        unsigned* fa = (unsigned*)(ws + WS_F) + F_R + (8u * j2 + (b2 & 7u)) * 256u + (2u + p2) * 64u + ((b2 >> 3) & 3u);
        if (w2 & 2u) st_flag(fa, tag);
        wait_flags(fa, ld_flag(fa), tag);
        const unsigned l2 = lane_id(); u32x4 c[3]; out_consts(c, ka[7], ka[8], ka[10], b2, w2, l2);
        asm volatile("s_waitcnt vmcnt(0) lgkmcnt(0)\n\ts_nop 7\n\ts_branch L_att_stub" :: "{v[0:3]}"(c[0]), "{v[4:7]}"(c[1]), "{v[8:11]}"(c[2]), "{v20}"(l2 * 16u), "{s[56:59]}"(SRDA), "{s60}"(SD(8u * j2, h2, w2)),
                     "{s61}"(SD(120u - 8u * j2, h2, w2)), "{s62}"(tag) : "memory");
    }
    RTS(5);
}

extern "C" void kernel_launch(void* const* d_in, const int* in_sizes, int n_in, void* d_out, int out_size, void* d_ws, size_t ws_size,
                              hipStream_t stream) {
    (void)hipFuncSetAttribute((const void*)fused_kernel, hipFuncAttributeMaxDynamicSharedMemorySize, att::LDS_BYTES);
    fused_kernel<<<dim3(256), 256, att::LDS_BYTES, stream>>>((const u16*)d_in[0], (const u16*)d_in[4], (const u16*)d_in[5], (const u16*)d_in[6], (const u16*)d_in[7], (const u16*)d_in[1],
                                                             (const u16*)d_in[9], (const u16*)d_in[2], (const u16*)d_in[8], (char*)d_ws, (u16*)d_out);
}
```

```cpp
#include <hip/hip_runtime.h>
#include <hip/hip_bf16.h>
#include <stdint.h>
#include <utility>

typedef short bf16x8 __attribute__((ext_vector_type(8)));
typedef short s16x4 __attribute__((ext_vector_type(4)));
typedef float f32x16 __attribute__((ext_vector_type(16)));
typedef float f32x4 __attribute__((ext_vector_type(4)));
typedef unsigned u32x4 __attribute__((ext_vector_type(4)));
typedef unsigned short u16;

constexpr int S = 2048, DM = 576, NH = 32, HD = 128, DC = 512, NQKV = 1728, DHA = 18;
constexpr int NBIG = NH * HD;
constexpr int KS_TOT = 146;

#define SBAR() __builtin_amdgcn_sched_barrier(0)
constexpr size_t WS_Q = 0, WS_K = WS_Q + (size_t)S * NBIG * 2, WS_V = WS_K + (size_t)S * NBIG * 2, WS_AF = WS_V + (size_t)S * NBIG * 2, WS_X = WS_AF + (size_t)128 * KS_TOT * 1024,
                 WS_P = WS_X + (size_t)64 * 16 * 9216, WS_F = WS_P + (size_t)128 * (4 * 32 * 256 + 1024) * 4;
constexpr size_t WS_L = (size_t)96 << 20;
static_assert(WS_F == 96206848, "workspace map is baked into the generated asm");
constexpr int F_EPOCH = 0, F_J = 1024, F_ST = 8192, F_KV = 40960, F_R = 65536;
constexpr unsigned TAG_STEP = 0x9E3779B1u;
__device__ __forceinline__ unsigned ld_flag(const unsigned* p) { return __hip_atomic_load(p, __ATOMIC_RELAXED, __HIP_MEMORY_SCOPE_AGENT); }
__device__ __forceinline__ void st_flag(unsigned* p, unsigned v) { __hip_atomic_store(p, v, __ATOMIC_RELAXED, __HIP_MEMORY_SCOPE_AGENT); }
__device__ __forceinline__ unsigned ld_flag_async(const unsigned* p) { unsigned v; asm volatile("global_load_dword %0, %1, off sc1" : "=v"(v) : "v"(p) : "memory"); return v; }
#define WAITV(v, N) asm volatile("s_waitcnt vmcnt(" #N ")" : "+v"(v) :: "memory")
__device__ __forceinline__ void wait_flags(const unsigned* p, unsigned v, unsigned tag) {
    if (__builtin_expect(__builtin_amdgcn_ballot_w64(v != tag) != 0ull, 0)) {
        unsigned spins = 0;
        do { __builtin_amdgcn_s_sleep(1); } while (__builtin_amdgcn_ballot_w64(ld_flag(p) != tag) != 0ull && ++spins < (1u << 22));
    }
    asm volatile("" ::: "memory");
}

__device__ __forceinline__ unsigned cvtpk(float lo, float hi) {
    unsigned r; asm volatile("v_cvt_pk_bf16_f32 %0, %1, %2" : "=v"(r) : "v"(lo), "v"(hi)); return r;
}
__device__ __forceinline__ float bf2f(u16 v) { return __uint_as_float(((unsigned)v) << 16); }
__device__ __forceinline__ int crow(int r, int hi) { return (r & 3) + 8 * (r >> 2) + 4 * hi; }

typedef __attribute__((address_space(3))) s16x4* lds_s16x4_p;
__device__ __forceinline__ s16x4 trrd(unsigned addr) { return __builtin_amdgcn_ds_read_tr16_b64_v4i16((lds_s16x4_p)addr); }

__device__ __forceinline__ unsigned lane_id() { unsigned l; asm volatile("v_mbcnt_lo_u32_b32 %0, -1, 0\n\tv_mbcnt_hi_u32_b32 %0, -1, %0" : "=v"(l)); return l; }
#define RTS(slot) do {} while (0)
#define CT_L 5u
__device__ __forceinline__ void code_touch(unsigned wi, unsigned lds_scratch) {
    unsigned long long pc; asm volatile("s_getpc_b64 %0" : "=s"(pc));
    asm volatile("s_mov_b32 m0, %0\n\ts_mov_b64 exec, %3\n\tglobal_load_lds_dword %1, %2\n\ts_mov_b64 exec, -1" :: "s"(lds_scratch), "v"(((threadIdx.x & 63u) * 128u + wi) * 128u), "s"(pc & ~127ull), "n"((1u << CT_L) - 1u) : "m0", "memory");
}
#define ACLOB_LO "a0","a1","a2","a3","a4","a5","a6","a7","a8","a9","a10","a11","a12","a13","a14","a15","a16","a17","a18","a19","a20","a21","a22","a23","a24","a25","a26","a27","a28","a29","a30","a31","a32","a33","a34","a35","a36","a37","a38","a39","a40","a41","a42","a43","a44","a45","a46","a47","a48","a49","a50","a51","a52","a53","a54","a55","a56","a57","a58","a59","a60","a61","a62","a63","a64","a65","a66","a67","a68","a69","a70","a71","a72","a73","a74","a75","a76","a77","a78","a79","a80","a81","a82","a83","a84","a85","a86","a87","a88","a89","a90","a91","a92","a93","a94","a95","a96","a97","a98","a99","a100","a101","a102","a103","a104","a105","a106","a107","a108","a109","a110","a111","a112","a113","a114","a115","a116","a117","a118","a119","a120","a121","a122","a123","a124","a125","a126","a127"
#define ACLOB_HI "a128","a129","a130","a131","a132","a133","a134","a135","a136","a137","a138","a139","a140","a141","a142","a143","a144","a145","a146","a147","a148","a149","a150","a151","a152","a153","a154","a155","a156","a157","a158","a159","a160","a161","a162","a163","a164","a165","a166","a167","a168","a169","a170","a171","a172","a173","a174","a175","a176","a177","a178","a179","a180","a181","a182","a183","a184","a185","a186","a187","a188","a189","a190","a191","a192","a193","a194","a195","a196","a197","a198","a199","a200","a201","a202","a203","a204","a205","a206","a207","a208","a209","a210","a211","a212","a213","a214","a215","a216","a217","a218","a219","a220","a221","a222","a223","a224","a225","a226","a227","a228","a229","a230","a231","a232","a233","a234","a235","a236","a237","a238","a239","a240","a241","a242","a243","a244","a245","a246","a247","a248","a249","a250","a251","a252","a253","a254","a255"
#define PROJ_PRE0 \
"s_add_u32 s62, %[rc], 0x0\n" \
"s_and_b32 s62, s62, 0x3ff\n" \
"s_lshl_b32 s63, s62, 12\n" \
"s_add_u32 m0, %[mA], 0x0\n" \
"s_add_u32 s85, s62, 0x0\n" \
"buffer_load_dwordx4 v22, s[64:67], s85 offen lds\n" \
"s_add_u32 m0, %[mA], 0x400\n" \
"s_add_u32 s84, s62, 0x2000\n" \
"buffer_load_dwordx4 v23, s[64:67], s84 offen lds\n" \
"s_add_u32 m0, %[mB], 0x0\n" \
"s_add_u32 s85, s63, 0x0\n" \
"buffer_load_dwordx4 v26, s[68:71], s85 offen lds\n" \
"s_add_u32 m0, %[mB], 0x4000\n" \
"s_add_u32 s84, s63, 0x0\n" \
"buffer_load_dwordx4 v26, s[72:75], s84 offen lds\n" \
"s_add_u32 m0, %[mA], 0x800\n" \
"s_add_u32 s85, s62, 0x4000\n" \
"buffer_load_dwordx4 v22, s[64:67], s85 offen lds\n" \
"s_add_u32 m0, %[mA], 0xc00\n" \
"s_add_u32 s84, s62, 0x6000\n" \
"buffer_load_dwordx4 v23, s[64:67], s84 offen lds\n" \
"s_add_u32 m0, %[mB], 0x400\n" \
"s_add_u32 s85, s63, 0x8000\n" \
"buffer_load_dwordx4 v26, s[68:71], s85 offen lds\n" \
"s_add_u32 m0, %[mB], 0x4400\n" \
"s_add_u32 s84, s63, 0x8000\n" \
"buffer_load_dwordx4 v26, s[72:75], s84 offen lds\n"
#define PROJ_PRE1 \
"s_add_u32 s62, %[rc], 0x0\n" \
"s_and_b32 s62, s62, 0x3ff\n" \
"s_lshl_b32 s63, s62, 12\n" \
"s_add_u32 m0, %[mA], 0x1000\n" \
"s_add_u32 s85, s62, 0x8000\n" \
"buffer_load_dwordx4 v22, s[64:67], s85 offen lds\n" \
"s_add_u32 m0, %[mA], 0x1400\n" \
"s_add_u32 s84, s62, 0xa000\n" \
"buffer_load_dwordx4 v23, s[64:67], s84 offen lds\n" \
"s_add_u32 m0, %[mB], 0x800\n" \
"s_add_u32 s85, s63, 0x10000\n" \
"buffer_load_dwordx4 v27, s[68:71], s85 offen lds\n" \
"s_add_u32 m0, %[mB], 0x4800\n" \
"s_add_u32 s84, s63, 0x10000\n" \
"buffer_load_dwordx4 v27, s[72:75], s84 offen lds\n" \
"s_add_u32 m0, %[mA], 0x1800\n" \
"s_add_u32 s85, s62, 0xc000\n" \
"buffer_load_dwordx4 v22, s[64:67], s85 offen lds\n" \
"s_add_u32 m0, %[mA], 0x1c00\n" \
"s_add_u32 s84, s62, 0xe000\n" \
"buffer_load_dwordx4 v23, s[64:67], s84 offen lds\n" \
"s_add_u32 m0, %[mB], 0xc00\n" \
"s_add_u32 s85, s63, 0x18000\n" \
"buffer_load_dwordx4 v27, s[68:71], s85 offen lds\n" \
"s_add_u32 m0, %[mB], 0x4c00\n" \
"s_add_u32 s84, s63, 0x18000\n" \
"buffer_load_dwordx4 v27, s[72:75], s84 offen lds\n"
#define PROJ_PRE2 \
"s_add_u32 s62, %[rc], 0x80\n" \
"s_and_b32 s62, s62, 0x3ff\n" \
"s_lshl_b32 s63, s62, 12\n" \
"s_add_u32 m0, %[mA], 0x8000\n" \
"s_add_u32 s85, s62, 0x0\n" \
"buffer_load_dwordx4 v22, s[64:67], s85 offen lds\n" \
"s_add_u32 m0, %[mA], 0x8400\n" \
"s_add_u32 s84, s62, 0x2000\n" \
"buffer_load_dwordx4 v23, s[64:67], s84 offen lds\n"
#define PROJ_PRE3 \
"s_add_u32 s62, %[rc], 0x80\n" \
"s_and_b32 s62, s62, 0x3ff\n" \
"s_lshl_b32 s63, s62, 12\n" \
"s_add_u32 m0, %[mB], 0x8000\n" \
"s_add_u32 s85, s63, 0x0\n" \
"buffer_load_dwordx4 v26, s[68:71], s85 offen lds\n" \
"s_add_u32 m0, %[mB], 0xc000\n" \
"s_add_u32 s84, s63, 0x0\n" \
"buffer_load_dwordx4 v26, s[72:75], s84 offen lds\n" \
"s_add_u32 m0, %[mA], 0x8800\n" \
"s_add_u32 s85, s62, 0x4000\n" \
"buffer_load_dwordx4 v22, s[64:67], s85 offen lds\n" \
"s_add_u32 m0, %[mA], 0x8c00\n" \
"s_add_u32 s84, s62, 0x6000\n" \
"buffer_load_dwordx4 v23, s[64:67], s84 offen lds\n" \
"s_add_u32 m0, %[mB], 0x8400\n" \
"s_add_u32 s85, s63, 0x8000\n" \
"buffer_load_dwordx4 v26, s[68:71], s85 offen lds\n" \
"s_add_u32 m0, %[mB], 0xc400\n" \
"s_add_u32 s84, s63, 0x8000\n" \
"buffer_load_dwordx4 v26, s[72:75], s84 offen lds\n" \
"s_add_u32 m0, %[mA], 0x9000\n" \
"s_add_u32 s85, s62, 0x8000\n" \
"buffer_load_dwordx4 v22, s[64:67], s85 offen lds\n" \
"s_add_u32 m0, %[mA], 0x9400\n" \
"s_add_u32 s84, s62, 0xa000\n" \
"buffer_load_dwordx4 v23, s[64:67], s84 offen lds\n"
#define PROJ_TKE 0
#define PROJ_T0 0
#define PROJ_ROT 2*(b+mt)+((b>>2)&1)
#define PROJ_TC 0
#define PROJ_ASM \
".p2align 3\n" \
"s_branch L_go_%=\n" \
"L_pl_%=:\n" \
"s_mov_b32 s63, 0\n" \
"L_ql_%=:\n" \
"s_sleep 1\n" \
"v_bfe_u32 v44, v20, 2, 5\n" \
"buffer_load_dword v44, v44, s[80:83], s39 offen offset:64 sc1\n" \
"s_waitcnt vmcnt(0)\n" \
"v_cmp_ne_u32_e32 vcc, %[tag], v44\n" \
"s_add_u32 s63, s63, 1\n" \
"s_nop 3\n" \
"s_cbranch_vccz L_rl_%=\n" \
"s_cmp_gt_u32 s63, 0x400000\n" \
"s_cbranch_scc0 L_ql_%=\n" \
"s_branch L_rl_%=\n" \
"L_pa_%=:\n" \
"s_mov_b32 s63, 0\n" \
"L_qa_%=:\n" \
"s_sleep 1\n" \
"v_bfe_u32 v44, v20, 2, 5\n" \
"buffer_load_dword v44, v44, s[80:83], s39 offen offset:0 sc1\n" \
"s_waitcnt vmcnt(0)\n" \
"v_cmp_ne_u32_e32 vcc, %[tag], v44\n" \
"s_add_u32 s63, s63, 1\n" \
"s_nop 3\n" \
"s_cbranch_vccz L_ra_%=\n" \
"s_cmp_gt_u32 s63, 0x400000\n" \
"s_cbranch_scc0 L_qa_%=\n" \
"s_branch L_ra_%=\n" \
"L_go_%=:\n" \
"v_xor_b32_e32 v33, 0x20, v32\n" \
"s_mov_b32 s80, %[wsl]\n" \
"s_and_b32 s81, %[wsh], 0xffff\n" \
"s_mov_b32 s82, 0x10000000\n" \
"s_mov_b32 s83, 0x20000\n" \
"s_mov_b32 s84, 0x5bc0000\n" \
"s_add_u32 s62, s22, 0x80\n" \
"buffer_load_dword v180, off, s[80:83], s84 sc1\n" \
"s_and_b32 s62, s62, 0x3ff\n" \
"s_lshl_b32 s63, s62, 12\n" \
"v_xor_b32_e64 v34, 0x40, v32\n" \
"v_xor_b32_e32 v35, 0x60, v32\n" \
"s_waitcnt vmcnt(11)\n" \
"s_barrier\n" \
"ds_read_b64_tr_b16 v[120:121], v32 offset:8192\n" \
"ds_read_b64_tr_b16 v[122:123], v32 offset:9216\n" \
"ds_read_b128 v[56:59], v29 offset:16384\n" \
"ds_read_b64_tr_b16 v[116:117], v32 offset:0\n" \
"ds_read_b64_tr_b16 v[118:119], v32 offset:1024\n" \
"ds_read_b128 v[52:55], v28 offset:16384\n" \
"s_add_u32 m0, %[mB], 0x8800\n" \
"s_add_u32 s85, s63, 0x10000\n" \
"s_mov_b32 s76, s12\n" \
"ds_read_b128 v[60:63], v28 offset:18432\n" \
"s_and_b32 s77, s13, 0xffff\n" \
"s_mov_b32 s78, 0x1e6000\n" \
"s_mov_b32 s79, 0x20000\n" \
"ds_read_b128 v[64:67], v29 offset:18432\n" \
"s_waitcnt lgkmcnt(5)\n" \
"v_mfma_f32_16x16x32_bf16 a[0:3], v[120:123], v[56:59], 0\n" \
"ds_read_b128 v[72:75], v29 offset:20480\n" \
"ds_read_b128 v[68:71], v28 offset:20480\n" \
"ds_read_b128 v[76:79], v28 offset:22528\n" \
"buffer_load_dwordx4 v27, s[68:71], s85 offen lds\n" \
"ds_read_b128 v[80:83], v29 offset:22528\n" \
"s_waitcnt lgkmcnt(6)\n" \
"v_mfma_f32_16x16x32_bf16 a[0:3], v[116:119], v[52:55], a[0:3]\n" \
"ds_read_b64_tr_b16 v[128:129], v33 offset:8192\n" \
"s_waitcnt lgkmcnt(6)\n" \
"v_mfma_f32_16x16x32_bf16 a[32:35], v[116:119], v[60:63], 0\n" \
"ds_read_b64_tr_b16 v[130:131], v33 offset:9216\n" \
"ds_read_b64_tr_b16 v[124:125], v33 offset:0\n" \
"ds_read_b64_tr_b16 v[126:127], v33 offset:1024\n" \
"s_waitcnt lgkmcnt(8)\n" \
"s_nop 0\n" \
"v_mfma_f32_16x16x32_bf16 a[32:35], v[120:123], v[64:67], a[32:35]\n" \
"ds_read_b64_tr_b16 v[136:137], v34 offset:8192\n" \
"ds_read_b64_tr_b16 v[138:139], v34 offset:9216\n" \
"ds_read_b64_tr_b16 v[132:133], v34 offset:0\n" \
"s_add_u32 m0, %[mB], 0xc800\n" \
"ds_read_b64_tr_b16 v[134:135], v34 offset:1024\n" \
"s_add_u32 s84, s63, 0x10000\n" \
"buffer_load_dwordx4 v27, s[72:75], s84 offen lds\n" \
"ds_read_b64_tr_b16 v[144:145], v35 offset:8192\n" \
"ds_read_b64_tr_b16 v[146:147], v35 offset:9216\n" \
"ds_read_b64_tr_b16 v[140:141], v35 offset:0\n" \
"s_lshr_b32 s95, %[b], 3\n" \
"ds_read_b64_tr_b16 v[142:143], v35 offset:1024\n" \
"s_lshl_b32 s96, s95, 2\n" \
"ds_read_b64_tr_b16 v[152:153], v32 offset:24576\n" \
"s_add_u32 s97, s96, %[w]\n" \
"ds_read_b64_tr_b16 v[154:155], v32 offset:25600\n" \
"v_lshrrev_b32_e32 v212, 4, v20\n" \
"s_mul_i32 s98, 27, s97\n" \
"v_min_u32_e32 v213, 26, v212\n" \
"v_add_u32_e64 v214, s98, v213\n" \
"v_mul_u32_u24_e32 v215, 0xaaab, v214\n" \
"s_waitcnt lgkmcnt(15)\n" \
"v_mfma_f32_16x16x32_bf16 a[64:67], v[120:123], v[72:75], 0\n" \
"ds_read_b64_tr_b16 v[148:149], v32 offset:16384\n" \
"ds_read_b64_tr_b16 v[150:151], v32 offset:17408\n" \
"v_mfma_f32_16x16x32_bf16 a[64:67], v[116:119], v[68:71], a[64:67]\n" \
"s_add_u32 m0, %[mA], 0x9800\n" \
"s_add_u32 s85, s62, 0xc000\n" \
"v_mfma_f32_16x16x32_bf16 a[96:99], v[116:119], v[76:79], 0\n" \
"ds_read_b64_tr_b16 v[160:161], v33 offset:24576\n" \
"s_waitcnt lgkmcnt(15)\n" \
"v_mfma_f32_16x16x32_bf16 a[96:99], v[120:123], v[80:83], a[96:99]\n" \
"buffer_load_dwordx4 v22, s[64:67], s85 offen lds\n" \
"v_mfma_f32_16x16x32_bf16 a[4:7], v[128:131], v[56:59], 0\n" \
"s_add_u32 m0, %[mA], 0x9c00\n" \
"s_waitcnt lgkmcnt(13)\n" \
"s_nop 0\n" \
"v_mfma_f32_16x16x32_bf16 a[4:7], v[124:127], v[52:55], a[4:7]\n" \
"s_add_u32 s84, s62, 0xe000\n" \
"ds_read_b64_tr_b16 v[162:163], v33 offset:25600\n" \
"v_mfma_f32_16x16x32_bf16 a[36:39], v[124:127], v[60:63], 0\n" \
"buffer_load_dwordx4 v23, s[64:67], s84 offen lds\n" \
"v_mfma_f32_16x16x32_bf16 a[36:39], v[128:131], v[64:67], a[36:39]\n" \
"s_add_u32 m0, %[mB], 0x8c00\n" \
"s_add_u32 s85, s63, 0x18000\n" \
"v_mfma_f32_16x16x32_bf16 a[68:71], v[128:131], v[72:75], 0\n" \
"ds_read_b64_tr_b16 v[156:157], v33 offset:16384\n" \
"ds_read_b64_tr_b16 v[158:159], v33 offset:17408\n" \
"v_mfma_f32_16x16x32_bf16 a[68:71], v[124:127], v[68:71], a[68:71]\n" \
"buffer_load_dwordx4 v27, s[68:71], s85 offen lds\n" \
"v_mfma_f32_16x16x32_bf16 a[100:103], v[124:127], v[76:79], 0\n" \
"s_add_u32 m0, %[mB], 0xcc00\n" \
"s_add_u32 s84, s63, 0x18000\n" \
"v_mfma_f32_16x16x32_bf16 a[100:103], v[128:131], v[80:83], a[100:103]\n" \
"ds_read_b64_tr_b16 v[168:169], v34 offset:24576\n" \
"s_waitcnt lgkmcnt(7)\n" \
"v_mfma_f32_16x16x32_bf16 a[8:11], v[136:139], v[56:59], 0\n" \
"buffer_load_dwordx4 v27, s[72:75], s84 offen lds\n" \
"v_mfma_f32_16x16x32_bf16 a[8:11], v[132:135], v[52:55], a[8:11]\n" \
"ds_read_b64_tr_b16 v[170:171], v34 offset:25600\n" \
"ds_read_b64_tr_b16 v[164:165], v34 offset:16384\n" \
"v_mfma_f32_16x16x32_bf16 a[40:43], v[132:135], v[60:63], 0\n" \
"ds_read_b64_tr_b16 v[166:167], v34 offset:17408\n" \
"v_lshrrev_b32_e32 v216, 18, v215\n" \
"v_mfma_f32_16x16x32_bf16 a[40:43], v[136:139], v[64:67], a[40:43]\n" \
"v_mul_u32_u24_e32 v218, 6, v216\n" \
"v_sub_u32_e32 v219, v214, v218\n" \
"v_mfma_f32_16x16x32_bf16 a[72:75], v[136:139], v[72:75], 0\n" \
"v_lshrrev_b32_e32 v220, 1, v219\n" \
"v_mul_u32_u24_e32 v217, 27, v216\n" \
"v_mfma_f32_16x16x32_bf16 a[72:75], v[132:135], v[68:71], a[72:75]\n" \
"v_mul_u32_u24_e32 v221, 9, v220\n" \
"v_add_u32_e32 v222, v217, v221\n" \
"v_mfma_f32_16x16x32_bf16 a[104:107], v[132:135], v[76:79], 0\n" \
"v_and_b32_e32 v223, 1, v219\n" \
"s_and_b32 s94, 7, %[b]\n" \
"v_mfma_f32_16x16x32_bf16 a[104:107], v[136:139], v[80:83], a[104:107]\n" \
"ds_read_b64_tr_b16 v[176:177], v35 offset:24576\n" \
"v_add_u32_e64 v224, v222, v223\n" \
"v_mfma_f32_16x16x32_bf16 a[12:15], v[144:147], v[56:59], 0\n" \
"ds_read_b64_tr_b16 v[178:179], v35 offset:25600\n" \
"v_add_u32_e64 v225, s94, v224\n" \
"v_mfma_f32_16x16x32_bf16 a[12:15], v[140:143], v[52:55], a[12:15]\n" \
"ds_read_b64_tr_b16 v[172:173], v35 offset:16384\n" \
"v_lshlrev_b32_e32 v186, 7, v225\n" \
"v_mfma_f32_16x16x32_bf16 a[44:47], v[140:143], v[60:63], 0\n" \
"ds_read_b64_tr_b16 v[174:175], v35 offset:17408\n" \
"ds_read_b128 v[88:91], v29 offset:0\n" \
"v_mfma_f32_16x16x32_bf16 a[44:47], v[144:147], v[64:67], a[44:47]\n" \
"ds_read_b128 v[84:87], v28 offset:0\n" \
"ds_read_b128 v[92:95], v28 offset:2048\n" \
"v_mfma_f32_16x16x32_bf16 a[76:79], v[144:147], v[72:75], 0\n" \
"ds_read_b128 v[96:99], v29 offset:2048\n" \
"s_mul_i32 s89, 6, s97\n" \
"v_mfma_f32_16x16x32_bf16 a[76:79], v[140:143], v[68:71], a[76:79]\n" \
"ds_read_b128 v[104:107], v29 offset:4096\n" \
"s_add_u32 s95, 0xffffffdc, s89\n" \
"v_mfma_f32_16x16x32_bf16 a[108:111], v[140:143], v[76:79], 0\n" \
"ds_read_b128 v[100:103], v28 offset:4096\n" \
"s_mul_i32 s99, 0x1200, s94\n" \
"v_mfma_f32_16x16x32_bf16 a[108:111], v[144:147], v[80:83], a[108:111]\n" \
"ds_read_b128 v[108:111], v28 offset:6144\n" \
"s_mul_i32 s100, 36, s97\n" \
"v_mfma_f32_16x16x32_bf16 a[16:19], v[56:59], v[152:155], 0\n" \
"ds_read_b128 v[112:115], v29 offset:6144\n" \
"s_waitcnt lgkmcnt(12)\n" \
"v_mfma_f32_16x16x32_bf16 a[16:19], v[52:55], v[148:151], a[16:19]\n" \
"v_add_u32_e32 v227, s95, v212\n" \
"s_add_u32 s101, s99, s100\n" \
"v_mfma_f32_16x16x32_bf16 a[48:51], v[60:63], v[148:151], 0\n" \
"s_mul_i32 s88, 0x288, s94\n" \
"v_min_u32_e32 v228, 0x287, v227\n" \
"v_mfma_f32_16x16x32_bf16 a[48:51], v[64:67], v[152:155], a[48:51]\n" \
"s_mov_b32 m0, %[mS]\n" \
"s_mov_b32 s86, s12\n" \
"v_mfma_f32_16x16x32_bf16 a[80:83], v[72:75], v[152:155], 0\n" \
"s_mov_b32 s87, s13\n" \
"v_add_u32_e32 v226, s101, v212\n" \
"v_mfma_f32_16x16x32_bf16 a[80:83], v[68:71], v[148:151], a[80:83]\n" \
"v_add_u32_e32 v229, s88, v228\n" \
"v_cmp_gt_u32_e32 vcc, 36, v212\n" \
"v_mfma_f32_16x16x32_bf16 a[112:115], v[76:79], v[148:151], 0\n" \
"s_lshl_b32 s96, %[w], 6\n" \
"v_cndmask_b32_e32 v230, v229, v226, vcc\n" \
"v_mfma_f32_16x16x32_bf16 a[112:115], v[80:83], v[152:155], a[112:115]\n" \
"v_lshlrev_b32_e32 v16, 7, v230\n" \
"v_lshrrev_b32_e32 v212, 4, v20\n" \
"v_mfma_f32_16x16x32_bf16 a[20:23], v[56:59], v[160:163], 0\n" \
"v_add_u32_e64 v213, s96, v212\n" \
"v_mul_u32_u24_e32 v214, 0x2aab, v213\n" \
"v_mfma_f32_16x16x32_bf16 a[20:23], v[52:55], v[156:159], a[20:23]\n" \
"v_lshrrev_b32_e32 v215, 16, v214\n" \
"s_lshr_b32 s94, %[b], 5\n" \
"s_mov_b32 exec_lo, 0x7ffffff\n" \
"s_mov_b32 exec_hi, 0x0\n" \
"global_load_lds_dword v186, s[86:87]\n" \
"s_mov_b64 exec, -1\n" \
"v_mfma_f32_16x16x32_bf16 a[52:55], v[60:63], v[156:159], 0\n" \
"v_mul_u32_u24_e32 v218, 6, v215\n" \
"s_mul_i32 s95, 0x48, s94\n" \
"v_mfma_f32_16x16x32_bf16 a[52:55], v[64:67], v[160:163], a[52:55]\n" \
"v_sub_u32_e32 v219, v213, v218\n" \
"s_lshl_b32 s41, s97, 7\n" \
"v_mfma_f32_16x16x32_bf16 a[84:87], v[72:75], v[160:163], 0\n" \
"v_add_u32_e32 v216, s95, v215\n" \
"v_lshrrev_b32_e32 v220, 1, v219\n" \
"v_mfma_f32_16x16x32_bf16 a[84:87], v[68:71], v[156:159], a[84:87]\n" \
"s_and_b32 s100, 7, %[b]\n" \
"s_lshr_b32 s88, %[b], 3\n" \
"v_mfma_f32_16x16x32_bf16 a[116:119], v[76:79], v[156:159], 0\n" \
"v_mul_u32_u24_e32 v217, 0xd80, v216\n" \
"v_mul_u32_u24_e32 v221, 0x480, v220\n" \
"v_mfma_f32_16x16x32_bf16 a[116:119], v[80:83], v[160:163], a[116:119]\n" \
"v_and_b32_e32 v223, 1, v219\n" \
"s_add_u32 s97, 0x100, s96\n" \
"v_mfma_f32_16x16x32_bf16 a[24:27], v[56:59], v[168:171], 0\n" \
"s_lshl_b32 s101, s100, 2\n" \
"s_and_b32 s89, 3, s88\n" \
"v_mfma_f32_16x16x32_bf16 a[24:27], v[52:55], v[164:167], a[24:27]\n" \
"v_add_u32_e32 v222, v217, v221\n" \
"v_lshlrev_b32_e32 v224, 4, v223\n" \
"v_mfma_f32_16x16x32_bf16 a[56:59], v[60:63], v[164:167], 0\n" \
"s_add_u32 s96, s101, s89\n" \
"v_add_u32_e32 v192, v222, v224\n" \
"v_mfma_f32_16x16x32_bf16 a[56:59], v[64:67], v[168:171], a[56:59]\n" \
"s_mul_i32 s56, 36, s96\n" \
"v_add_u32_e32 v225, s97, v212\n" \
"v_mfma_f32_16x16x32_bf16 a[88:91], v[72:75], v[168:171], 0\n" \
"v_mul_u32_u24_e32 v226, 0x2aab, v225\n" \
"v_lshrrev_b32_e32 v227, 16, v226\n" \
"v_mfma_f32_16x16x32_bf16 a[88:91], v[68:71], v[164:167], a[88:91]\n" \
"v_mul_u32_u24_e32 v230, 6, v227\n" \
"v_sub_u32_e32 v231, v225, v230\n" \
"v_mfma_f32_16x16x32_bf16 a[120:123], v[76:79], v[164:167], 0\n" \
"v_add_u32_e32 v228, s95, v227\n" \
"v_lshrrev_b32_e32 v232, 1, v231\n" \
"v_mfma_f32_16x16x32_bf16 a[120:123], v[80:83], v[168:171], a[120:123]\n" \
"v_mul_u32_u24_e32 v229, 0xd80, v228\n" \
"s_waitcnt lgkmcnt(0)\n" \
"v_mfma_f32_16x16x32_bf16 a[28:31], v[56:59], v[176:179], 0\n" \
"v_mul_u32_u24_e32 v233, 0x480, v232\n" \
"v_and_b32_e64 v235, 1, v231\n" \
"v_mfma_f32_16x16x32_bf16 a[28:31], v[52:55], v[172:175], a[28:31]\n" \
"v_add_u32_e32 v234, v229, v233\n" \
"v_lshlrev_b32_e32 v236, 4, v235\n" \
"v_mfma_f32_16x16x32_bf16 a[60:63], v[60:63], v[172:175], 0\n" \
"v_add_u32_e64 v237, v234, v236\n" \
"v_cmp_gt_u32_e32 vcc, 0x1b0, v225\n" \
"v_mfma_f32_16x16x32_bf16 a[60:63], v[64:67], v[176:179], a[60:63]\n" \
"s_mul_i32 s98, 18, %[w]\n" \
"v_cndmask_b32_e32 v193, 0x40000000, v237, vcc\n" \
"v_mfma_f32_16x16x32_bf16 a[92:95], v[72:75], v[176:179], 0\n" \
"s_add_u32 s99, s95, s98\n" \
"v_add_u32_e32 v238, s99, v212\n" \
"v_mfma_f32_16x16x32_bf16 a[92:95], v[68:71], v[172:175], a[92:95]\n" \
"v_mul_u32_u24_e32 v239, 0xd80, v238\n" \
"v_cmp_gt_u32_e32 vcc, 18, v212\n" \
"v_mfma_f32_16x16x32_bf16 a[124:127], v[76:79], v[172:175], 0\n" \
"v_lshlrev_b32_e32 v240, 1, v238\n" \
"v_cndmask_b32_e32 v194, 0x40000000, v239, vcc\n" \
"v_mfma_f32_16x16x32_bf16 a[124:127], v[80:83], v[176:179], a[124:127]\n" \
"v_cmp_gt_u32_e32 vcc, 18, v212\n" \
"v_lshrrev_b32_e32 v230, 1, v238\n" \
"v_mfma_f32_16x16x32_bf16 a[144:147], v[88:91], v[152:155], 0\n" \
"v_cndmask_b32_e32 v195, 0x40000000, v240, vcc\n" \
"v_and_b32_e32 v227, 0xff8, v238\n" \
"v_mfma_f32_16x16x32_bf16 a[144:147], v[84:87], v[148:151], a[144:147]\n" \
"v_and_b32_e32 v232, 2, v230\n" \
"v_add_u32_e32 v229, v227, v232\n" \
"v_mfma_f32_16x16x32_bf16 a[176:179], v[92:95], v[148:151], 0\n" \
"v_and_b32_e32 v233, 1, v238\n" \
"v_add_u32_e32 v235, v229, v233\n" \
"v_mfma_f32_16x16x32_bf16 a[176:179], v[96:99], v[152:155], a[176:179]\n" \
"v_lshrrev_b32_e32 v242, 1, v216\n" \
"v_lshrrev_b32_e32 v244, 2, v216\n" \
"v_mfma_f32_16x16x32_bf16 a[208:211], v[104:107], v[152:155], 0\n" \
"v_lshrrev_b32_e32 v214, 1, v228\n" \
"v_lshrrev_b32_e32 v213, 2, v228\n" \
"v_mfma_f32_16x16x32_bf16 a[208:211], v[100:103], v[148:151], a[208:211]\n" \
"v_lshrrev_b32_e32 v236, 1, v235\n" \
"v_lshrrev_b32_e32 v239, 2, v235\n" \
"v_mfma_f32_16x16x32_bf16 a[240:243], v[108:111], v[148:151], 0\n" \
"s_lshl_b32 s98, %[w], 10\n" \
"s_mul_i32 s101, 0x12000, s96\n" \
"v_mfma_f32_16x16x32_bf16 a[240:243], v[112:115], v[152:155], a[240:243]\n" \
"v_and_b32_e32 v243, 1, v242\n" \
"v_and_b32_e64 v245, 2, v244\n" \
"v_mfma_f32_16x16x32_bf16 a[148:151], v[88:91], v[160:163], 0\n" \
"v_and_b32_e32 v215, 1, v214\n" \
"v_and_b32_e32 v218, 2, v213\n" \
"v_mfma_f32_16x16x32_bf16 a[148:151], v[84:87], v[156:159], a[148:151]\n" \
"v_and_b32_e32 v237, 1, v236\n" \
"v_and_b32_e32 v240, 2, v239\n" \
"v_mfma_f32_16x16x32_bf16 a[180:183], v[92:95], v[156:159], 0\n" \
"s_add_u32 s99, 0x22000, s98\n" \
"s_add_u32 s89, 0x6000000, s101\n" \
"v_mfma_f32_16x16x32_bf16 a[180:183], v[96:99], v[160:163], a[180:183]\n" \
"s_mul_i32 s97, 0x2400, s94\n" \
"v_lshlrev_b32_e32 v241, 7, v216\n" \
"v_mfma_f32_16x16x32_bf16 a[212:215], v[104:107], v[160:163], 0\n" \
"v_or_b32_e32 v246, v243, v245\n" \
"v_or_b32_e32 v220, v215, v218\n" \
"v_mfma_f32_16x16x32_bf16 a[212:215], v[100:103], v[156:159], a[212:215]\n" \
"v_or_b32_e32 v242, v237, v240\n" \
"v_add_u32_e64 v199, s99, v20\n" \
"v_mfma_f32_16x16x32_bf16 a[244:247], v[108:111], v[156:159], 0\n" \
"s_add_u32 s37, s89, s98\n" \
"v_lshlrev_b32_e32 v247, 1, v246\n" \
"v_mfma_f32_16x16x32_bf16 a[244:247], v[112:115], v[160:163], a[244:247]\n" \
"v_lshlrev_b32_e32 v217, 1, v220\n" \
"v_xor_b32_e32 v216, 3, v242\n" \
"v_mfma_f32_16x16x32_bf16 a[152:155], v[88:91], v[168:171], 0\n" \
"s_add_u32 s57, s37, s97\n" \
"s_lshl_b32 s99, s88, 2\n" \
"v_mfma_f32_16x16x32_bf16 a[152:155], v[84:87], v[164:167], a[152:155]\n" \
"v_xor_b32_e32 v248, v219, v247\n" \
"v_xor_b32_e32 v221, v231, v217\n" \
"v_mfma_f32_16x16x32_bf16 a[184:187], v[92:95], v[164:167], 0\n" \
"v_lshlrev_b32_e32 v234, 7, v235\n" \
"v_lshlrev_b32_e32 v244, 5, v216\n" \
"v_mfma_f32_16x16x32_bf16 a[184:187], v[96:99], v[168:171], a[184:187]\n" \
"v_and_b32_e64 v245, 2, v238\n" \
"s_add_u32 s94, 0x2000, s57\n" \
"v_mfma_f32_16x16x32_bf16 a[216:219], v[104:107], v[168:171], 0\n" \
"s_add_u32 s96, s99, %[w]\n" \
"s_lshl_b32 s89, %[b], 2\n" \
"v_mfma_f32_16x16x32_bf16 a[216:219], v[100:103], v[164:167], a[216:219]\n" \
"s_sub_u32 s95, 0x22000, s97\n" \
"v_lshlrev_b32_e32 v249, 4, v248\n" \
"v_mfma_f32_16x16x32_bf16 a[248:251], v[108:111], v[164:167], 0\n" \
"v_lshlrev_b32_e32 v251, 7, v228\n" \
"v_lshlrev_b32_e32 v223, 4, v221\n" \
"v_mfma_f32_16x16x32_bf16 a[248:251], v[112:115], v[168:171], a[248:251]\n" \
"v_add_u32_e32 v243, v234, v244\n" \
"v_lshlrev_b32_e32 v246, 1, v245\n" \
"v_mfma_f32_16x16x32_bf16 a[156:159], v[88:91], v[176:179], 0\n" \
"s_cmp_eq_u32 %[w], 0\n" \
"s_cselect_b32 s58, s94, 0x40000000\n" \
"v_mfma_f32_16x16x32_bf16 a[156:159], v[84:87], v[172:175], a[156:159]\n" \
"s_mul_i32 s101, 36, s96\n" \
"s_add_u32 s98, s89, %[w]\n" \
"v_mfma_f32_16x16x32_bf16 a[188:191], v[92:95], v[172:175], 0\n" \
"v_add_u32_e32 v250, v241, v249\n" \
"v_add_u32_e64 v222, v251, v223\n" \
"v_mfma_f32_16x16x32_bf16 a[188:191], v[96:99], v[176:179], a[188:191]\n" \
"v_add_u32_e32 v219, v243, v246\n" \
"v_add_u32_e32 v248, s101, v212\n" \
"v_mfma_f32_16x16x32_bf16 a[220:223], v[104:107], v[176:179], 0\n" \
"s_mul_i32 s97, 18, s98\n" \
"v_add_u32_e32 v196, s95, v250\n" \
"v_mfma_f32_16x16x32_bf16 a[220:223], v[100:103], v[172:175], a[220:223]\n" \
"v_add_u32_e32 v224, s95, v222\n" \
"v_add_u32_e32 v247, s95, v219\n" \
"v_mfma_f32_16x16x32_bf16 a[252:255], v[108:111], v[172:175], 0\n" \
"v_lshrrev_b32_e32 v241, 3, v248\n" \
"s_add_u32 s94, 0xffffffdc, s97\n" \
"v_mfma_f32_16x16x32_bf16 a[252:255], v[112:115], v[176:179], a[252:255]\n" \
"v_min_u32_e32 v213, 53, v212\n" \
"s_lshl_b32 s95, s100, 3\n" \
"v_mfma_f32_16x16x32_bf16 a[128:131], v[120:123], v[88:91], 0\n" \
"v_lshlrev_b32_e32 v249, 6, v241\n" \
"v_add_u32_e64 v215, s94, v213\n" \
"v_mfma_f32_16x16x32_bf16 a[128:131], v[116:119], v[84:87], a[128:131]\n" \
"s_and_b32 s97, 7, %[b]\n" \
"s_lshr_b32 s99, %[b], 3\n" \
"v_mfma_f32_16x16x32_bf16 a[160:163], v[116:119], v[92:95], 0\n" \
"v_add_u32_e32 v250, s95, v249\n" \
"s_lshr_b32 s94, %[b], 5\n" \
"v_mfma_f32_16x16x32_bf16 a[160:163], v[120:123], v[96:99], a[160:163]\n" \
"s_lshl_b32 s98, s97, 2\n" \
"s_and_b32 s100, 3, s99\n" \
"v_mfma_f32_16x16x32_bf16 a[192:195], v[120:123], v[104:107], 0\n" \
"s_mul_i32 s95, 0xa00, s94\n" \
"s_add_u32 s101, s98, s100\n" \
"v_mfma_f32_16x16x32_bf16 a[192:195], v[116:119], v[100:103], a[192:195]\n" \
"s_add_u32 s96, 0x5bc1000, s95\n" \
"s_lshl_b32 s88, s101, 2\n" \
"v_mfma_f32_16x16x32_bf16 a[224:227], v[116:119], v[108:111], 0\n" \
"s_add_u32 s89, s96, s88\n" \
"s_lshl_b32 s99, s94, 3\n" \
"v_mfma_f32_16x16x32_bf16 a[224:227], v[120:123], v[112:115], a[224:227]\n" \
"s_cmp_eq_u32 %[w], 0\n" \
"s_cselect_b32 s95, s89, 0x40000000\n" \
"v_mfma_f32_16x16x32_bf16 a[132:135], v[128:131], v[88:91], 0\n" \
"s_add_u32 s62, s22, 0x100\n" \
"s_and_b32 s62, s62, 0x3ff\n" \
"v_mfma_f32_16x16x32_bf16 a[132:135], v[124:127], v[84:87], a[132:135]\n" \
"s_lshl_b32 s63, s62, 12\n" \
"s_add_u32 s88, s99, s97\n" \
"v_mfma_f32_16x16x32_bf16 a[164:167], v[124:127], v[92:95], 0\n" \
"v_writelane_b32 v11, s95, 0\n" \
"s_lshl_b32 s89, s88, 10\n" \
"v_mfma_f32_16x16x32_bf16 a[164:167], v[128:131], v[96:99], a[164:167]\n" \
"s_add_u32 s95, 0x5c00000, s89\n" \
"s_lshl_b32 s99, %[w], 8\n" \
"v_mfma_f32_16x16x32_bf16 a[196:199], v[128:131], v[104:107], 0\n" \
"s_add_u32 s88, s95, s99\n" \
"v_add_u32_e32 v226, 0x24400, v20\n" \
"v_mfma_f32_16x16x32_bf16 a[196:199], v[124:127], v[100:103], a[196:199]\n" \
"v_cmp_gt_u32_e32 vcc, 0x1b0, v225\n" \
"s_or_b32 s89, 0x200, s88\n" \
"v_mfma_f32_16x16x32_bf16 a[228:231], v[124:127], v[108:111], 0\n" \
"s_lshl_b32 s95, s100, 2\n" \
"v_cndmask_b32_e32 v197, v226, v224, vcc\n" \
"v_mfma_f32_16x16x32_bf16 a[228:231], v[128:131], v[112:115], a[228:231]\n" \
"v_writelane_b32 v11, s89, 1\n" \
"s_add_u32 s99, s88, s95\n" \
"s_waitcnt vmcnt(1)\n" \
"s_barrier\n" \
"v_mfma_f32_16x16x32_bf16 a[136:139], v[136:139], v[88:91], 0\n" \
"ds_read_b64_tr_b16 v[152:153], v32 offset:57344\n" \
"ds_read_b64_tr_b16 v[154:155], v32 offset:58368\n" \
"v_mfma_f32_16x16x32_bf16 a[136:139], v[132:135], v[84:87], a[136:139]\n" \
"ds_read_b128 v[56:59], v29 offset:49152\n" \
"s_add_u32 m0, %[mA], 0x0\n" \
"v_mfma_f32_16x16x32_bf16 a[168:171], v[132:135], v[92:95], 0\n" \
"s_add_u32 s85, s62, 0x0\n" \
"ds_read_b64_tr_b16 v[148:149], v32 offset:49152\n" \
"v_mfma_f32_16x16x32_bf16 a[168:171], v[136:139], v[96:99], a[168:171]\n" \
"buffer_load_dwordx4 v22, s[64:67], s85 offen lds\n" \
"ds_read_b64_tr_b16 v[150:151], v32 offset:50176\n" \
"ds_read_b128 v[52:55], v28 offset:49152\n" \
"v_mfma_f32_16x16x32_bf16 a[200:203], v[136:139], v[104:107], 0\n" \
"L_pf0:\n" \
"s_cbranch_execz L_pf4\n" \
"s_nop 0\n" \
"ds_read_b128 v[60:63], v28 offset:51200\n" \
"ds_read_b128 v[64:67], v29 offset:51200\n" \
"v_mfma_f32_16x16x32_bf16 a[200:203], v[132:135], v[100:103], a[200:203]\n" \
"s_add_u32 m0, %[mA], 0x400\n" \
"s_add_u32 s84, s62, 0x2000\n" \
"v_mfma_f32_16x16x32_bf16 a[232:235], v[132:135], v[108:111], 0\n" \
"buffer_load_dwordx4 v23, s[64:67], s84 offen lds\n" \
"ds_read_b128 v[72:75], v29 offset:53248\n" \
"v_mfma_f32_16x16x32_bf16 a[232:235], v[136:139], v[112:115], a[232:235]\n" \
"ds_read_b128 v[68:71], v28 offset:53248\n" \
"ds_read_b128 v[76:79], v28 offset:55296\n" \
"v_mfma_f32_16x16x32_bf16 a[140:143], v[144:147], v[88:91], 0\n" \
"ds_read_b128 v[80:83], v29 offset:55296\n" \
"s_add_u32 m0, %[mB], 0x0\n" \
"v_mfma_f32_16x16x32_bf16 a[140:143], v[140:143], v[84:87], a[140:143]\n" \
"s_add_u32 s85, s63, 0x0\n" \
"ds_read_b64_tr_b16 v[160:161], v33 offset:57344\n" \
"v_mfma_f32_16x16x32_bf16 a[172:175], v[140:143], v[92:95], 0\n" \
"buffer_load_dwordx4 v26, s[68:71], s85 offen lds\n" \
"ds_read_b64_tr_b16 v[162:163], v33 offset:58368\n" \
"v_mfma_f32_16x16x32_bf16 a[172:175], v[144:147], v[96:99], a[172:175]\n" \
"ds_read_b64_tr_b16 v[156:157], v33 offset:49152\n" \
"ds_read_b64_tr_b16 v[158:159], v33 offset:50176\n" \
"v_mfma_f32_16x16x32_bf16 a[204:207], v[144:147], v[104:107], 0\n" \
"s_add_u32 m0, %[mB], 0x4000\n" \
"s_add_u32 s84, s63, 0x0\n" \
"v_mfma_f32_16x16x32_bf16 a[204:207], v[140:143], v[100:103], a[204:207]\n" \
"buffer_load_dwordx4 v26, s[72:75], s84 offen lds\n" \
"v_mfma_f32_16x16x32_bf16 a[236:239], v[140:143], v[108:111], 0\n" \
"s_add_u32 m0, %[mA], 0x800\n" \
"s_add_u32 s85, s62, 0x4000\n" \
"v_mfma_f32_16x16x32_bf16 a[236:239], v[144:147], v[112:115], a[236:239]\n" \
"ds_read_b64_tr_b16 v[168:169], v34 offset:57344\n" \
"s_waitcnt lgkmcnt(13)\n" \
"v_mfma_f32_16x16x32_bf16 a[16:19], v[56:59], v[152:155], a[16:19]\n" \
"ds_read_b64_tr_b16 v[170:171], v34 offset:58368\n" \
"s_waitcnt lgkmcnt(12)\n" \
"v_mfma_f32_16x16x32_bf16 a[16:19], v[52:55], v[148:151], a[16:19]\n" \
"ds_read_b64_tr_b16 v[164:165], v34 offset:49152\n" \
"s_waitcnt lgkmcnt(12)\n" \
"v_mfma_f32_16x16x32_bf16 a[48:51], v[60:63], v[148:151], a[48:51]\n" \
"ds_read_b64_tr_b16 v[166:167], v34 offset:50176\n" \
"buffer_load_dwordx4 v22, s[64:67], s85 offen lds\n" \
"s_waitcnt lgkmcnt(12)\n" \
"v_mfma_f32_16x16x32_bf16 a[48:51], v[64:67], v[152:155], a[48:51]\n" \
"s_add_u32 m0, %[mA], 0xc00\n" \
"s_waitcnt lgkmcnt(11)\n" \
"v_mfma_f32_16x16x32_bf16 a[80:83], v[72:75], v[152:155], a[80:83]\n" \
"s_add_u32 s84, s62, 0x6000\n" \
"s_waitcnt lgkmcnt(10)\n" \
"v_mfma_f32_16x16x32_bf16 a[80:83], v[68:71], v[148:151], a[80:83]\n" \
"s_add_u32 s85, s63, 0x8000\n" \
"buffer_load_dwordx4 v23, s[64:67], s84 offen lds\n" \
"s_waitcnt lgkmcnt(9)\n" \
"v_mfma_f32_16x16x32_bf16 a[112:115], v[76:79], v[148:151], a[112:115]\n" \
"s_add_u32 m0, %[mB], 0x400\n" \
"s_waitcnt lgkmcnt(8)\n" \
"v_mfma_f32_16x16x32_bf16 a[112:115], v[80:83], v[152:155], a[112:115]\n" \
"ds_read_b64_tr_b16 v[176:177], v35 offset:57344\n" \
"s_waitcnt lgkmcnt(7)\n" \
"v_mfma_f32_16x16x32_bf16 a[20:23], v[56:59], v[160:163], a[20:23]\n" \
"ds_read_b64_tr_b16 v[178:179], v35 offset:58368\n" \
"s_waitcnt lgkmcnt(6)\n" \
"s_nop 0\n" \
"v_mfma_f32_16x16x32_bf16 a[20:23], v[52:55], v[156:159], a[20:23]\n" \
"buffer_load_dwordx4 v26, s[68:71], s85 offen lds\n" \
"v_mfma_f32_16x16x32_bf16 a[52:55], v[60:63], v[156:159], a[52:55]\n" \
"ds_read_b64_tr_b16 v[172:173], v35 offset:49152\n" \
"ds_read_b64_tr_b16 v[174:175], v35 offset:50176\n" \
"v_mfma_f32_16x16x32_bf16 a[52:55], v[64:67], v[160:163], a[52:55]\n" \
"s_add_u32 m0, %[mB], 0x4400\n" \
"s_add_u32 s84, s63, 0x8000\n" \
"v_mfma_f32_16x16x32_bf16 a[84:87], v[72:75], v[160:163], a[84:87]\n" \
"buffer_load_dwordx4 v26, s[72:75], s84 offen lds\n" \
"v_mfma_f32_16x16x32_bf16 a[84:87], v[68:71], v[156:159], a[84:87]\n" \
"s_add_u32 m0, %[mA], 0x1000\n" \
"s_add_u32 s85, s62, 0x8000\n" \
"v_mfma_f32_16x16x32_bf16 a[116:119], v[76:79], v[156:159], a[116:119]\n" \
"ds_read_b64_tr_b16 v[120:121], v32 offset:40960\n" \
"ds_read_b64_tr_b16 v[122:123], v32 offset:41984\n" \
"v_mfma_f32_16x16x32_bf16 a[116:119], v[80:83], v[160:163], a[116:119]\n" \
"ds_read_b64_tr_b16 v[116:117], v32 offset:32768\n" \
"s_waitcnt lgkmcnt(7)\n" \
"s_nop 0\n" \
"v_mfma_f32_16x16x32_bf16 a[24:27], v[56:59], v[168:171], a[24:27]\n" \
"buffer_load_dwordx4 v22, s[64:67], s85 offen lds\n" \
"v_mfma_f32_16x16x32_bf16 a[24:27], v[52:55], v[164:167], a[24:27]\n" \
"ds_read_b64_tr_b16 v[118:119], v32 offset:33792\n" \
"s_add_u32 m0, %[mA], 0x1400\n" \
"v_mfma_f32_16x16x32_bf16 a[56:59], v[60:63], v[164:167], a[56:59]\n" \
"s_add_u32 s84, s62, 0xa000\n" \
"ds_read_b64_tr_b16 v[128:129], v33 offset:40960\n" \
"v_mfma_f32_16x16x32_bf16 a[56:59], v[64:67], v[168:171], a[56:59]\n" \
"buffer_load_dwordx4 v23, s[64:67], s84 offen lds\n" \
"v_mfma_f32_16x16x32_bf16 a[88:91], v[72:75], v[168:171], a[88:91]\n" \
"s_add_u32 m0, %[mB], 0x800\n" \
"s_add_u32 s85, s63, 0x10000\n" \
"v_mfma_f32_16x16x32_bf16 a[88:91], v[68:71], v[164:167], a[88:91]\n" \
"ds_read_b64_tr_b16 v[130:131], v33 offset:41984\n" \
"ds_read_b64_tr_b16 v[124:125], v33 offset:32768\n" \
"v_mfma_f32_16x16x32_bf16 a[120:123], v[76:79], v[164:167], a[120:123]\n" \
"buffer_load_dwordx4 v27, s[68:71], s85 offen lds\n" \
"v_mfma_f32_16x16x32_bf16 a[120:123], v[80:83], v[168:171], a[120:123]\n" \
"ds_read_b64_tr_b16 v[126:127], v33 offset:33792\n" \
"s_waitcnt lgkmcnt(10)\n" \
"v_mfma_f32_16x16x32_bf16 a[28:31], v[56:59], v[176:179], a[28:31]\n" \
"s_add_u32 m0, %[mB], 0x4800\n" \
"s_waitcnt lgkmcnt(8)\n" \
"v_mfma_f32_16x16x32_bf16 a[28:31], v[52:55], v[172:175], a[28:31]\n" \
"s_add_u32 s84, s63, 0x10000\n" \
"ds_read_b64_tr_b16 v[136:137], v34 offset:40960\n" \
"v_mfma_f32_16x16x32_bf16 a[60:63], v[60:63], v[172:175], a[60:63]\n" \
"buffer_load_dwordx4 v27, s[72:75], s84 offen lds\n" \
"v_mfma_f32_16x16x32_bf16 a[60:63], v[64:67], v[176:179], a[60:63]\n" \
"ds_read_b64_tr_b16 v[138:139], v34 offset:41984\n" \
"ds_read_b64_tr_b16 v[132:133], v34 offset:32768\n" \
"v_mfma_f32_16x16x32_bf16 a[92:95], v[72:75], v[176:179], a[92:95]\n" \
"s_add_u32 m0, %[mA], 0x1800\n" \
"s_add_u32 s85, s62, 0xc000\n" \
"v_mfma_f32_16x16x32_bf16 a[92:95], v[68:71], v[172:175], a[92:95]\n" \
"buffer_load_dwordx4 v22, s[64:67], s85 offen lds\n" \
"v_mfma_f32_16x16x32_bf16 a[124:127], v[76:79], v[172:175], a[124:127]\n" \
"ds_read_b64_tr_b16 v[134:135], v34 offset:33792\n" \
"ds_read_b64_tr_b16 v[144:145], v35 offset:40960\n" \
"v_mfma_f32_16x16x32_bf16 a[124:127], v[80:83], v[176:179], a[124:127]\n" \
"s_add_u32 m0, %[mA], 0x1c00\n" \
"s_waitcnt lgkmcnt(9)\n" \
"s_nop 0\n" \
"v_mfma_f32_16x16x32_bf16 a[0:3], v[120:123], v[56:59], a[0:3]\n" \
"s_add_u32 s84, s62, 0xe000\n" \
"ds_read_b64_tr_b16 v[146:147], v35 offset:41984\n" \
"v_mfma_f32_16x16x32_bf16 a[0:3], v[116:119], v[52:55], a[0:3]\n" \
"buffer_load_dwordx4 v23, s[64:67], s84 offen lds\n" \
"v_mfma_f32_16x16x32_bf16 a[32:35], v[116:119], v[60:63], a[32:35]\n" \
"ds_read_b64_tr_b16 v[140:141], v35 offset:32768\n" \
"ds_read_b64_tr_b16 v[142:143], v35 offset:33792\n" \
"v_mfma_f32_16x16x32_bf16 a[32:35], v[120:123], v[64:67], a[32:35]\n" \
"s_add_u32 m0, %[mB], 0xc00\n" \
"s_add_u32 s85, s63, 0x18000\n" \
"v_mfma_f32_16x16x32_bf16 a[64:67], v[120:123], v[72:75], a[64:67]\n" \
"buffer_load_dwordx4 v27, s[68:71], s85 offen lds\n" \
"v_mfma_f32_16x16x32_bf16 a[64:67], v[116:119], v[68:71], a[64:67]\n" \
"ds_read_b128 v[88:91], v29 offset:32768\n" \
"ds_read_b128 v[84:87], v28 offset:32768\n" \
"v_mfma_f32_16x16x32_bf16 a[96:99], v[116:119], v[76:79], a[96:99]\n" \
"ds_read_b128 v[92:95], v28 offset:34816\n" \
"ds_read_b128 v[96:99], v29 offset:34816\n" \
"v_mfma_f32_16x16x32_bf16 a[96:99], v[120:123], v[80:83], a[96:99]\n" \
"s_add_u32 m0, %[mB], 0x4c00\n" \
"s_waitcnt lgkmcnt(11)\n" \
"v_mfma_f32_16x16x32_bf16 a[4:7], v[128:131], v[56:59], a[4:7]\n" \
"s_add_u32 s84, s63, 0x18000\n" \
"ds_read_b128 v[104:107], v29 offset:36864\n" \
"v_mfma_f32_16x16x32_bf16 a[4:7], v[124:127], v[52:55], a[4:7]\n" \
"buffer_load_dwordx4 v27, s[72:75], s84 offen lds\n" \
"v_mfma_f32_16x16x32_bf16 a[36:39], v[124:127], v[60:63], a[36:39]\n" \
"ds_read_b128 v[100:103], v28 offset:36864\n" \
"ds_read_b128 v[108:111], v28 offset:38912\n" \
"v_mfma_f32_16x16x32_bf16 a[36:39], v[128:131], v[64:67], a[36:39]\n" \
"s_mov_b32 m0, %[mS]\n" \
"s_mov_b32 s86, s18\n" \
"v_mfma_f32_16x16x32_bf16 a[68:71], v[128:131], v[72:75], a[68:71]\n" \
"s_mov_b32 s87, s19\n" \
"ds_read_b128 v[112:115], v29 offset:38912\n" \
"v_mfma_f32_16x16x32_bf16 a[68:71], v[124:127], v[68:71], a[68:71]\n" \
"v_cmp_gt_u32_e32 vcc, 18, v212\n" \
"v_writelane_b32 v11, s99, 2\n" \
"v_mfma_f32_16x16x32_bf16 a[100:103], v[124:127], v[76:79], a[100:103]\n" \
"s_add_u32 s89, 0x80, s88\n" \
"v_cndmask_b32_e32 v198, v226, v247, vcc\n" \
"v_mfma_f32_16x16x32_bf16 a[100:103], v[128:131], v[80:83], a[100:103]\n" \
"v_and_b32_e32 v214, 7, v248\n" \
"s_waitcnt lgkmcnt(10)\n" \
"v_mfma_f32_16x16x32_bf16 a[8:11], v[136:139], v[56:59], a[8:11]\n" \
"v_writelane_b32 v11, s89, 3\n" \
"s_lshl_b32 s88, s101, 8\n" \
"v_mfma_f32_16x16x32_bf16 a[8:11], v[132:135], v[52:55], a[8:11]\n" \
"v_add_u32_e32 v228, v250, v214\n" \
"v_cmp_gt_u32_e32 vcc, 36, v212\n" \
"s_mov_b32 exec_lo, 0x3ffff\n" \
"s_mov_b32 exec_hi, 0x0\n" \
"global_load_lds_dword v16, s[86:87]\n" \
"s_mov_b64 exec, -1\n" \
"v_mfma_f32_16x16x32_bf16 a[40:43], v[132:135], v[60:63], a[40:43]\n" \
"s_add_u32 s95, 0x80, s99\n" \
"s_add_u32 s39, 0x5be8000, s88\n" \
"v_mfma_f32_16x16x32_bf16 a[40:43], v[136:139], v[64:67], a[40:43]\n" \
"s_lshl_b32 s89, s94, 2\n" \
"v_cndmask_b32_e32 v218, v215, v228, vcc\n" \
"v_mfma_f32_16x16x32_bf16 a[72:75], v[136:139], v[72:75], a[72:75]\n" \
"s_add_u32 s99, s39, s89\n" \
"v_lshrrev_b32_e32 v212, 4, v20\n" \
"v_mfma_f32_16x16x32_bf16 a[72:75], v[132:135], v[68:71], a[72:75]\n" \
"v_writelane_b32 v11, s95, 4\n" \
"v_lshlrev_b32_e32 v213, 2, v212\n" \
"v_mfma_f32_16x16x32_bf16 a[104:107], v[132:135], v[76:79], a[104:107]\n" \
"s_cmp_eq_u32 %[w], 3\n" \
"s_cselect_b32 s38, s99, 0x40000000\n" \
"v_mfma_f32_16x16x32_bf16 a[104:107], v[136:139], v[80:83], a[104:107]\n" \
"v_and_b32_e32 v215, 7, v212\n" \
"s_add_u32 s101, s89, s100\n" \
"v_mfma_f32_16x16x32_bf16 a[12:15], v[144:147], v[56:59], a[12:15]\n" \
"v_add_u32_e32 v214, s96, v213\n" \
"s_waitcnt lgkmcnt(1)\n" \
"v_mfma_f32_16x16x32_bf16 a[12:15], v[140:143], v[52:55], a[12:15]\n" \
"s_add_u32 s95, 0x5be8020, s88\n" \
"v_lshlrev_b32_e32 v216, 2, v215\n" \
"v_mfma_f32_16x16x32_bf16 a[44:47], v[140:143], v[60:63], a[44:47]\n" \
"s_mul_i32 s94, 0x90000, s101\n" \
"v_add_u32_e32 v217, s95, v216\n" \
"v_mfma_f32_16x16x32_bf16 a[44:47], v[144:147], v[64:67], a[44:47]\n" \
"s_add_u32 s99, 0x4240000, s94\n" \
"s_lshl_b32 s96, %[w], 14\n" \
"v_mfma_f32_16x16x32_bf16 a[76:79], v[144:147], v[72:75], a[76:79]\n" \
"s_add_u32 s88, s99, s96\n" \
"s_lshl_b32 s95, s97, 16\n" \
"v_mfma_f32_16x16x32_bf16 a[76:79], v[140:143], v[68:71], a[76:79]\n" \
"v_writelane_b32 v11, s39, 52\n" \
"s_add_u32 s89, s88, s95\n" \
"v_mfma_f32_16x16x32_bf16 a[108:111], v[140:143], v[76:79], a[108:111]\n" \
"s_lshl_b32 s101, s97, 11\n" \
"v_writelane_b32 v11, s89, 37\n" \
"v_mfma_f32_16x16x32_bf16 a[108:111], v[144:147], v[80:83], a[108:111]\n" \
"s_add_u32 s100, 0x3800, s89\n" \
"s_add_u32 s94, s88, s101\n" \
"v_mfma_f32_16x16x32_bf16 a[128:131], v[120:123], v[88:91], a[128:131]\n" \
"v_writelane_b32 v11, s100, 51\n" \
"s_add_u32 s99, 0x80000, s94\n" \
"v_mfma_f32_16x16x32_bf16 a[128:131], v[116:119], v[84:87], a[128:131]\n" \
"s_lshl_b32 s96, %[w], 17\n" \
"v_writelane_b32 v11, s99, 49\n" \
"v_mfma_f32_16x16x32_bf16 a[160:163], v[116:119], v[92:95], a[160:163]\n" \
"s_add_u32 s95, s88, s96\n" \
"s_and_b32 s94, 1, s97\n" \
"v_mfma_f32_16x16x32_bf16 a[160:163], v[120:123], v[96:99], a[160:163]\n" \
"s_add_u32 s89, s95, s98\n" \
"s_lshl_b32 s99, s94, 10\n" \
"v_mfma_f32_16x16x32_bf16 a[192:195], v[120:123], v[104:107], a[192:195]\n" \
"s_add_u32 s100, 0x3840, s89\n" \
"s_add_u32 s96, s88, s99\n" \
"v_mfma_f32_16x16x32_bf16 a[192:195], v[116:119], v[100:103], a[192:195]\n" \
"v_writelane_b32 v11, s100, 5\n" \
"s_lshr_b32 s101, s97, 1\n" \
"v_mfma_f32_16x16x32_bf16 a[224:227], v[116:119], v[108:111], a[224:227]\n" \
"s_add_u32 s95, 0x80000, s96\n" \
"s_waitcnt lgkmcnt(0)\n" \
"v_mfma_f32_16x16x32_bf16 a[224:227], v[120:123], v[112:115], a[224:227]\n" \
"s_add_u32 s89, 1, s97\n" \
"s_and_b32 s100, 7, s89\n" \
"v_mfma_f32_16x16x32_bf16 a[132:135], v[128:131], v[88:91], a[132:135]\n" \
"s_cmp_eq_u32 %[w], s101\n" \
"s_cselect_b32 s98, s95, 0x40000000\n" \
"v_mfma_f32_16x16x32_bf16 a[132:135], v[124:127], v[84:87], a[132:135]\n" \
"s_lshl_b32 s94, s100, 16\n" \
"s_add_u32 s96, 2, s97\n" \
"v_mfma_f32_16x16x32_bf16 a[164:167], v[124:127], v[92:95], a[164:167]\n" \
"v_writelane_b32 v11, s98, 38\n" \
"s_add_u32 s99, s88, s94\n" \
"v_mfma_f32_16x16x32_bf16 a[164:167], v[128:131], v[96:99], a[164:167]\n" \
"s_and_b32 s101, 7, s96\n" \
"s_add_u32 s100, 3, s97\n" \
"v_mfma_f32_16x16x32_bf16 a[196:199], v[128:131], v[104:107], a[196:199]\n" \
"v_writelane_b32 v11, s99, 42\n" \
"s_lshl_b32 s95, s101, 16\n" \
"v_mfma_f32_16x16x32_bf16 a[196:199], v[124:127], v[100:103], a[196:199]\n" \
"s_and_b32 s94, 7, s100\n" \
"s_add_u32 s98, s88, s95\n" \
"v_mfma_f32_16x16x32_bf16 a[228:231], v[124:127], v[108:111], a[228:231]\n" \
"s_lshl_b32 s99, s94, 16\n" \
"s_add_u32 s89, 0x800, s98\n" \
"v_mfma_f32_16x16x32_bf16 a[228:231], v[128:131], v[112:115], a[228:231]\n" \
"s_add_u32 s96, s88, s99\n" \
"s_add_u32 s95, 4, s97\n" \
"v_mfma_f32_16x16x32_bf16 a[136:139], v[136:139], v[88:91], a[136:139]\n" \
"v_writelane_b32 v11, s89, 43\n" \
"s_add_u32 s101, 0x1000, s96\n" \
"v_mfma_f32_16x16x32_bf16 a[136:139], v[132:135], v[84:87], a[136:139]\n" \
"s_and_b32 s98, 7, s95\n" \
"s_add_u32 s99, 5, s97\n" \
"v_mfma_f32_16x16x32_bf16 a[168:171], v[132:135], v[92:95], a[168:171]\n" \
"v_writelane_b32 v11, s101, 44\n" \
"s_lshl_b32 s89, s98, 16\n" \
"v_mfma_f32_16x16x32_bf16 a[168:171], v[136:139], v[96:99], a[168:171]\n" \
"s_and_b32 s96, 7, s99\n" \
"s_add_u32 s100, s88, s89\n" \
"v_mfma_f32_16x16x32_bf16 a[200:203], v[136:139], v[104:107], a[200:203]\n" \
"s_lshl_b32 s101, s96, 16\n" \
"s_add_u32 s94, 0x1800, s100\n" \
"v_mfma_f32_16x16x32_bf16 a[200:203], v[132:135], v[100:103], a[200:203]\n" \
"s_add_u32 s95, s88, s101\n" \
"s_add_u32 s89, 6, s97\n" \
"v_mfma_f32_16x16x32_bf16 a[232:235], v[132:135], v[108:111], a[232:235]\n" \
"v_writelane_b32 v11, s94, 45\n" \
"s_add_u32 s98, 0x2000, s95\n" \
"v_mfma_f32_16x16x32_bf16 a[232:235], v[136:139], v[112:115], a[232:235]\n" \
"s_and_b32 s100, 7, s89\n" \
"s_add_u32 s101, 7, s97\n" \
"v_mfma_f32_16x16x32_bf16 a[140:143], v[144:147], v[88:91], a[140:143]\n" \
"s_add_u32 s62, s22, 0x180\n" \
"s_and_b32 s62, s62, 0x3ff\n" \
"v_mfma_f32_16x16x32_bf16 a[140:143], v[140:143], v[84:87], a[140:143]\n" \
"s_lshl_b32 s63, s62, 12\n" \
"v_writelane_b32 v11, s98, 46\n" \
"v_mfma_f32_16x16x32_bf16 a[172:175], v[140:143], v[92:95], a[172:175]\n" \
"s_lshl_b32 s94, s100, 16\n" \
"s_and_b32 s95, 7, s101\n" \
"v_mfma_f32_16x16x32_bf16 a[172:175], v[144:147], v[96:99], a[172:175]\n" \
"s_add_u32 s99, s88, s94\n" \
"s_lshl_b32 s98, s95, 16\n" \
"v_mfma_f32_16x16x32_bf16 a[204:207], v[144:147], v[104:107], a[204:207]\n" \
"s_add_u32 s96, 0x2800, s99\n" \
"s_add_u32 s89, s88, s98\n" \
"v_mfma_f32_16x16x32_bf16 a[204:207], v[140:143], v[100:103], a[204:207]\n" \
"v_writelane_b32 v11, s96, 47\n" \
"s_add_u32 s100, 0x3000, s89\n" \
"v_mfma_f32_16x16x32_bf16 a[236:239], v[140:143], v[108:111], a[236:239]\n" \
"v_writelane_b32 v11, s100, 48\n" \
"s_mov_b32 s87, 0x5040100\n" \
"v_mfma_f32_16x16x32_bf16 a[236:239], v[144:147], v[112:115], a[236:239]\n" \
"v_lshlrev_b32_e32 v188, 7, v218\n" \
"v_readfirstlane_b32 %[tag], v180\n" \
"s_waitcnt vmcnt(1)\n" \
"s_barrier\n" \
"v_mfma_f32_16x16x32_bf16 a[144:147], v[88:91], v[152:155], a[144:147]\n" \
"s_add_u32 m0, %[mA], 0x8000\n" \
"s_add_u32 s85, s62, 0x0\n" \
"v_mfma_f32_16x16x32_bf16 a[144:147], v[84:87], v[148:151], a[144:147]\n" \
"buffer_load_dwordx4 v22, s[64:67], s85 offen lds\n" \
"v_mfma_f32_16x16x32_bf16 a[176:179], v[92:95], v[148:151], a[176:179]\n" \
"s_add_u32 m0, %[mA], 0x8400\n" \
"s_add_u32 s84, s62, 0x2000\n" \
"v_mfma_f32_16x16x32_bf16 a[176:179], v[96:99], v[152:155], a[176:179]\n" \
"ds_read_b64_tr_b16 v[120:121], v32 offset:8192\n" \
"ds_read_b64_tr_b16 v[122:123], v32 offset:9216\n" \
"v_mfma_f32_16x16x32_bf16 a[208:211], v[104:107], v[152:155], a[208:211]\n" \
"buffer_load_dwordx4 v23, s[64:67], s84 offen lds\n" \
"v_mfma_f32_16x16x32_bf16 a[208:211], v[100:103], v[148:151], a[208:211]\n" \
"ds_read_b128 v[56:59], v29 offset:16384\n" \
"ds_read_b64_tr_b16 v[116:117], v32 offset:0\n" \
"v_mfma_f32_16x16x32_bf16 a[240:243], v[108:111], v[148:151], a[240:243]\n" \
"s_add_u32 m0, %[mB], 0x8000\n" \
"s_add_u32 s85, s63, 0x0\n" \
"v_mfma_f32_16x16x32_bf16 a[240:243], v[112:115], v[152:155], a[240:243]\n" \
"buffer_load_dwordx4 v26, s[68:71], s85 offen lds\n" \
"v_mfma_f32_16x16x32_bf16 a[148:151], v[88:91], v[160:163], a[148:151]\n" \
"ds_read_b64_tr_b16 v[118:119], v32 offset:1024\n" \
"ds_read_b128 v[52:55], v28 offset:16384\n" \
"v_mfma_f32_16x16x32_bf16 a[148:151], v[84:87], v[156:159], a[148:151]\n" \
"ds_read_b128 v[60:63], v28 offset:18432\n" \
"ds_read_b128 v[64:67], v29 offset:18432\n" \
"v_mfma_f32_16x16x32_bf16 a[180:183], v[92:95], v[156:159], a[180:183]\n" \
"s_add_u32 m0, %[mB], 0xc000\n" \
"s_add_u32 s84, s63, 0x0\n" \
"v_mfma_f32_16x16x32_bf16 a[180:183], v[96:99], v[160:163], a[180:183]\n" \
"buffer_load_dwordx4 v26, s[72:75], s84 offen lds\n" \
"v_mfma_f32_16x16x32_bf16 a[212:215], v[104:107], v[160:163], a[212:215]\n" \
"ds_read_b128 v[72:75], v29 offset:20480\n" \
"ds_read_b128 v[68:71], v28 offset:20480\n" \
"v_mfma_f32_16x16x32_bf16 a[212:215], v[100:103], v[156:159], a[212:215]\n" \
"ds_read_b128 v[76:79], v28 offset:22528\n" \
"ds_read_b128 v[80:83], v29 offset:22528\n" \
"v_mfma_f32_16x16x32_bf16 a[244:247], v[108:111], v[156:159], a[244:247]\n" \
"s_add_u32 m0, %[mA], 0x8800\n" \
"s_add_u32 s85, s62, 0x4000\n" \
"v_mfma_f32_16x16x32_bf16 a[244:247], v[112:115], v[160:163], a[244:247]\n" \
"buffer_load_dwordx4 v22, s[64:67], s85 offen lds\n" \
"v_mfma_f32_16x16x32_bf16 a[152:155], v[88:91], v[168:171], a[152:155]\n" \
"ds_read_b64_tr_b16 v[128:129], v33 offset:8192\n" \
"ds_read_b64_tr_b16 v[130:131], v33 offset:9216\n" \
"v_mfma_f32_16x16x32_bf16 a[152:155], v[84:87], v[164:167], a[152:155]\n" \
"s_add_u32 m0, %[mA], 0x8c00\n" \
"s_add_u32 s84, s62, 0x6000\n" \
"v_mfma_f32_16x16x32_bf16 a[184:187], v[92:95], v[164:167], a[184:187]\n" \
"buffer_load_dwordx4 v23, s[64:67], s84 offen lds\n" \
"v_mfma_f32_16x16x32_bf16 a[184:187], v[96:99], v[168:171], a[184:187]\n" \
"ds_read_b64_tr_b16 v[124:125], v33 offset:0\n" \
"ds_read_b64_tr_b16 v[126:127], v33 offset:1024\n" \
"v_mfma_f32_16x16x32_bf16 a[216:219], v[104:107], v[168:171], a[216:219]\n" \
"ds_read_b64_tr_b16 v[136:137], v34 offset:8192\n" \
"ds_read_b64_tr_b16 v[138:139], v34 offset:9216\n" \
"v_mfma_f32_16x16x32_bf16 a[216:219], v[100:103], v[164:167], a[216:219]\n" \
"s_add_u32 m0, %[mB], 0x8400\n" \
"s_add_u32 s85, s63, 0x8000\n" \
"v_mfma_f32_16x16x32_bf16 a[248:251], v[108:111], v[164:167], a[248:251]\n" \
"buffer_load_dwordx4 v26, s[68:71], s85 offen lds\n" \
"v_mfma_f32_16x16x32_bf16 a[248:251], v[112:115], v[168:171], a[248:251]\n" \
"ds_read_b64_tr_b16 v[132:133], v34 offset:0\n" \
"ds_read_b64_tr_b16 v[134:135], v34 offset:1024\n" \
"v_mfma_f32_16x16x32_bf16 a[156:159], v[88:91], v[176:179], a[156:159]\n" \
"ds_read_b64_tr_b16 v[144:145], v35 offset:8192\n" \
"ds_read_b64_tr_b16 v[146:147], v35 offset:9216\n" \
"v_mfma_f32_16x16x32_bf16 a[156:159], v[84:87], v[172:175], a[156:159]\n" \
"s_add_u32 m0, %[mB], 0xc400\n" \
"s_add_u32 s84, s63, 0x8000\n" \
"v_mfma_f32_16x16x32_bf16 a[188:191], v[92:95], v[172:175], a[188:191]\n" \
"buffer_load_dwordx4 v26, s[72:75], s84 offen lds\n" \
"v_mfma_f32_16x16x32_bf16 a[188:191], v[96:99], v[176:179], a[188:191]\n" \
"ds_read_b64_tr_b16 v[140:141], v35 offset:0\n" \
"ds_read_b64_tr_b16 v[142:143], v35 offset:1024\n" \
"v_mfma_f32_16x16x32_bf16 a[220:223], v[104:107], v[176:179], a[220:223]\n" \
"ds_read_b64_tr_b16 v[152:153], v32 offset:24576\n" \
"ds_read_b64_tr_b16 v[154:155], v32 offset:25600\n" \
"v_mfma_f32_16x16x32_bf16 a[220:223], v[100:103], v[172:175], a[220:223]\n" \
"s_add_u32 m0, %[mA], 0x9000\n" \
"s_add_u32 s85, s62, 0x8000\n" \
"v_mfma_f32_16x16x32_bf16 a[252:255], v[108:111], v[172:175], a[252:255]\n" \
"buffer_load_dwordx4 v22, s[64:67], s85 offen lds\n" \
"v_mfma_f32_16x16x32_bf16 a[252:255], v[112:115], v[176:179], a[252:255]\n" \
"ds_read_b64_tr_b16 v[148:149], v32 offset:16384\n" \
"s_waitcnt lgkmcnt(13)\n" \
"s_nop 0\n" \
"v_mfma_f32_16x16x32_bf16 a[0:3], v[120:123], v[56:59], a[0:3]\n" \
"s_add_u32 m0, %[mA], 0x9400\n" \
"s_add_u32 s84, s62, 0xa000\n" \
"v_mfma_f32_16x16x32_bf16 a[0:3], v[116:119], v[52:55], a[0:3]\n" \
"buffer_load_dwordx4 v23, s[64:67], s84 offen lds\n" \
"v_mfma_f32_16x16x32_bf16 a[32:35], v[116:119], v[60:63], a[32:35]\n" \
"ds_read_b64_tr_b16 v[150:151], v32 offset:17408\n" \
"ds_read_b64_tr_b16 v[160:161], v33 offset:24576\n" \
"v_mfma_f32_16x16x32_bf16 a[32:35], v[120:123], v[64:67], a[32:35]\n" \
"ds_read_b64_tr_b16 v[162:163], v33 offset:25600\n" \
"ds_read_b64_tr_b16 v[156:157], v33 offset:16384\n" \
"v_mfma_f32_16x16x32_bf16 a[64:67], v[120:123], v[72:75], a[64:67]\n" \
"s_add_u32 m0, %[mB], 0x8800\n" \
"s_add_u32 s85, s63, 0x10000\n" \
"v_mfma_f32_16x16x32_bf16 a[64:67], v[116:119], v[68:71], a[64:67]\n" \
"buffer_load_dwordx4 v27, s[68:71], s85 offen lds\n" \
"v_mfma_f32_16x16x32_bf16 a[96:99], v[116:119], v[76:79], a[96:99]\n" \
"ds_read_b64_tr_b16 v[158:159], v33 offset:17408\n" \
"ds_read_b64_tr_b16 v[168:169], v34 offset:24576\n" \
"v_mfma_f32_16x16x32_bf16 a[96:99], v[120:123], v[80:83], a[96:99]\n" \
"ds_read_b64_tr_b16 v[170:171], v34 offset:25600\n" \
"ds_read_b64_tr_b16 v[164:165], v34 offset:16384\n" \
"v_mfma_f32_16x16x32_bf16 a[4:7], v[128:131], v[56:59], a[4:7]\n" \
"s_add_u32 m0, %[mB], 0xc800\n" \
"s_waitcnt lgkmcnt(13)\n" \
"s_nop 0\n" \
"v_mfma_f32_16x16x32_bf16 a[4:7], v[124:127], v[52:55], a[4:7]\n" \
"s_add_u32 s84, s63, 0x10000\n" \
"ds_read_b64_tr_b16 v[166:167], v34 offset:17408\n" \
"v_mfma_f32_16x16x32_bf16 a[36:39], v[124:127], v[60:63], a[36:39]\n" \
"buffer_load_dwordx4 v27, s[72:75], s84 offen lds\n" \
"v_mfma_f32_16x16x32_bf16 a[36:39], v[128:131], v[64:67], a[36:39]\n" \
"ds_read_b64_tr_b16 v[176:177], v35 offset:24576\n" \
"ds_read_b64_tr_b16 v[178:179], v35 offset:25600\n" \
"v_mfma_f32_16x16x32_bf16 a[68:71], v[128:131], v[72:75], a[68:71]\n" \
"s_add_u32 m0, %[mA], 0x9800\n" \
"s_add_u32 s85, s62, 0xc000\n" \
"v_mfma_f32_16x16x32_bf16 a[68:71], v[124:127], v[68:71], a[68:71]\n" \
"buffer_load_dwordx4 v22, s[64:67], s85 offen lds\n" \
"v_mfma_f32_16x16x32_bf16 a[100:103], v[124:127], v[76:79], a[100:103]\n" \
"ds_read_b64_tr_b16 v[172:173], v35 offset:16384\n" \
"ds_read_b64_tr_b16 v[174:175], v35 offset:17408\n" \
"v_mfma_f32_16x16x32_bf16 a[100:103], v[128:131], v[80:83], a[100:103]\n" \
"s_add_u32 m0, %[mA], 0x9c00\n" \
"s_add_u32 s84, s62, 0xe000\n" \
"v_mfma_f32_16x16x32_bf16 a[8:11], v[136:139], v[56:59], a[8:11]\n" \
"buffer_load_dwordx4 v23, s[64:67], s84 offen lds\n" \
"v_mfma_f32_16x16x32_bf16 a[8:11], v[132:135], v[52:55], a[8:11]\n" \
"ds_read_b128 v[88:91], v29 offset:0\n" \
"ds_read_b128 v[84:87], v28 offset:0\n" \
"v_mfma_f32_16x16x32_bf16 a[40:43], v[132:135], v[60:63], a[40:43]\n" \
"ds_read_b128 v[92:95], v28 offset:2048\n" \
"ds_read_b128 v[96:99], v29 offset:2048\n" \
"v_mfma_f32_16x16x32_bf16 a[40:43], v[136:139], v[64:67], a[40:43]\n" \
"s_add_u32 m0, %[mB], 0x8c00\n" \
"s_add_u32 s85, s63, 0x18000\n" \
"v_mfma_f32_16x16x32_bf16 a[72:75], v[136:139], v[72:75], a[72:75]\n" \
"buffer_load_dwordx4 v27, s[68:71], s85 offen lds\n" \
"v_mfma_f32_16x16x32_bf16 a[72:75], v[132:135], v[68:71], a[72:75]\n" \
"ds_read_b128 v[104:107], v29 offset:4096\n" \
"ds_read_b128 v[100:103], v28 offset:4096\n" \
"v_mfma_f32_16x16x32_bf16 a[104:107], v[132:135], v[76:79], a[104:107]\n" \
"ds_read_b128 v[108:111], v28 offset:6144\n" \
"ds_read_b128 v[112:115], v29 offset:6144\n" \
"v_mfma_f32_16x16x32_bf16 a[104:107], v[136:139], v[80:83], a[104:107]\n" \
"s_add_u32 m0, %[mB], 0xcc00\n" \
"s_add_u32 s84, s63, 0x18000\n" \
"v_mfma_f32_16x16x32_bf16 a[12:15], v[144:147], v[56:59], a[12:15]\n" \
"s_add_u32 %[tag], %[tag], 0x9E3779B1\n" \
"s_waitcnt lgkmcnt(12)\n" \
"v_mfma_f32_16x16x32_bf16 a[12:15], v[140:143], v[52:55], a[12:15]\n" \
"buffer_load_dwordx4 v27, s[72:75], s84 offen lds\n" \
"v_mfma_f32_16x16x32_bf16 a[44:47], v[140:143], v[60:63], a[44:47]\n" \
"v_mov_b32_e32 v45, %[tag]\n" \
"s_lshr_b32 s94, %[b], 5\n" \
"v_mfma_f32_16x16x32_bf16 a[44:47], v[144:147], v[64:67], a[44:47]\n" \
"s_sub_u32 s95, 15, s94\n" \
"s_and_b32 s100, 1, %[w]\n" \
"v_mfma_f32_16x16x32_bf16 a[76:79], v[144:147], v[72:75], a[76:79]\n" \
"buffer_load_dwordx4 v[200:203], v192, s[76:79], s56 offen offset:0\n" \
"v_mfma_f32_16x16x32_bf16 a[76:79], v[140:143], v[68:71], a[76:79]\n" \
"s_cmp_lt_u32 %[w], 2\n" \
"s_cselect_b32 s96, s95, s94\n" \
"v_mfma_f32_16x16x32_bf16 a[108:111], v[140:143], v[76:79], a[108:111]\n" \
"s_lshl_b32 s97, s96, 7\n" \
"s_add_u32 s98, 0xfffffff0, s97\n" \
"v_mfma_f32_16x16x32_bf16 a[108:111], v[144:147], v[80:83], a[108:111]\n" \
"s_cmp_eq_u32 s97, 0\n" \
"s_cselect_b32 s99, 0, s98\n" \
"v_mfma_f32_16x16x32_bf16 a[16:19], v[56:59], v[152:155], a[16:19]\n" \
"v_cmp_gt_u32_e32 vcc, 32, v212\n" \
"s_lshl_b32 s101, s100, 3\n" \
"v_mfma_f32_16x16x32_bf16 a[16:19], v[52:55], v[148:151], a[16:19]\n" \
"buffer_load_dwordx4 v[204:207], v193, s[76:79], s56 offen offset:0\n" \
"v_mfma_f32_16x16x32_bf16 a[48:51], v[60:63], v[148:151], a[48:51]\n" \
"s_add_u32 s88, s99, s101\n" \
"v_lshrrev_b32_e32 v212, 4, v20\n" \
"v_mfma_f32_16x16x32_bf16 a[48:51], v[64:67], v[152:155], a[48:51]\n" \
"v_cndmask_b32_e32 v17, v217, v214, vcc\n" \
"v_lshrrev_b32_e32 v213, 3, v212\n" \
"v_mfma_f32_16x16x32_bf16 a[80:83], v[72:75], v[152:155], a[80:83]\n" \
"s_lshr_b32 s99, %[b], 3\n" \
"v_add_u32_e64 v214, s88, v213\n" \
"v_mfma_f32_16x16x32_bf16 a[80:83], v[68:71], v[148:151], a[80:83]\n" \
"s_lshl_b32 s96, s100, 6\n" \
"s_and_b32 s101, 3, s99\n" \
"v_mfma_f32_16x16x32_bf16 a[112:115], v[76:79], v[148:151], a[112:115]\n" \
"s_add_u32 s98, s97, s96\n" \
"s_add_u32 s88, s101, s94\n" \
"v_mfma_f32_16x16x32_bf16 a[112:115], v[80:83], v[152:155], a[112:115]\n" \
"buffer_load_dword v208, v194, s[76:79], s56 offen offset:32\n" \
"v_mfma_f32_16x16x32_bf16 a[20:23], v[56:59], v[160:163], a[20:23]\n" \
"v_add_u32_e32 v221, s98, v213\n" \
"s_add_u32 s96, 0xfffffff7, s88\n" \
"v_mfma_f32_16x16x32_bf16 a[20:23], v[52:55], v[156:159], a[20:23]\n" \
"s_cmp_lt_u32 s88, 9\n" \
"s_cselect_b32 s98, s88, s96\n" \
"v_mfma_f32_16x16x32_bf16 a[52:55], v[60:63], v[156:159], a[52:55]\n" \
"s_lshl_b32 s89, s100, 2\n" \
"s_add_u32 s99, s97, s100\n" \
"v_mfma_f32_16x16x32_bf16 a[52:55], v[64:67], v[160:163], a[52:55]\n" \
"s_lshr_b32 s88, s97, 4\n" \
"s_lshl_b32 s23, s98, 7\n" \
"v_mfma_f32_16x16x32_bf16 a[84:87], v[72:75], v[160:163], a[84:87]\n" \
"s_cmp_eq_u32 s99, 0\n" \
"s_cselect_b32 s49, 0xff800000, 0\n" \
"v_mfma_f32_16x16x32_bf16 a[84:87], v[68:71], v[156:159], a[84:87]\n" \
"buffer_load_dword v209, v194, s[76:79], s56 offen offset:1184\n" \
"v_mfma_f32_16x16x32_bf16 a[116:119], v[76:79], v[156:159], a[116:119]\n" \
"s_add_u32 s96, s88, s89\n" \
"v_lshrrev_b32_e32 v217, 4, v212\n" \
"v_mfma_f32_16x16x32_bf16 a[116:119], v[80:83], v[160:163], a[116:119]\n" \
"s_mul_i32 s98, 0x24800, s96\n" \
"s_and_b32 s97, 7, %[b]\n" \
"v_mfma_f32_16x16x32_bf16 a[24:27], v[56:59], v[168:171], a[24:27]\n" \
"v_and_b32_e32 v216, 7, v212\n" \
"v_add_u32_e32 v218, s89, v217\n" \
"v_mfma_f32_16x16x32_bf16 a[24:27], v[52:55], v[164:167], a[24:27]\n" \
"s_add_u32 s99, 0x3000000, s98\n" \
"s_lshl_b32 s88, s97, 8\n" \
"v_mfma_f32_16x16x32_bf16 a[56:59], v[60:63], v[164:167], a[56:59]\n" \
"v_xor_b32_e32 v219, v216, v218\n" \
"s_add_u32 s89, s99, s88\n" \
"v_mfma_f32_16x16x32_bf16 a[56:59], v[64:67], v[168:171], a[56:59]\n" \
"buffer_load_dword v211, v194, s[76:79], s56 offen offset:2336\n" \
"v_mfma_f32_16x16x32_bf16 a[88:91], v[72:75], v[168:171], a[88:91]\n" \
"s_mov_b32 s76, s14\n" \
"s_and_b32 s77, s15, 0xffff\n" \
"v_mfma_f32_16x16x32_bf16 a[88:91], v[68:71], v[164:167], a[88:91]\n" \
"s_mov_b32 s78, 0x480\n" \
"s_add_u32 s56, s57, 0x1000\n" \
"v_mfma_f32_16x16x32_bf16 a[120:123], v[76:79], v[164:167], a[120:123]\n" \
"s_lshl_b32 s96, s101, 2\n" \
"v_mul_u32_u24_e32 v215, 0x480, v214\n" \
"v_mfma_f32_16x16x32_bf16 a[120:123], v[80:83], v[168:171], a[120:123]\n" \
"v_lshlrev_b32_e32 v220, 4, v219\n" \
"s_waitcnt lgkmcnt(0)\n" \
"v_mfma_f32_16x16x32_bf16 a[28:31], v[56:59], v[176:179], a[28:31]\n" \
"v_lshrrev_b32_e32 v229, 2, v212\n" \
"v_and_b32_e64 v234, 3, v212\n" \
"v_mfma_f32_16x16x32_bf16 a[28:31], v[52:55], v[172:175], a[28:31]\n" \
"buffer_load_ushort v210, v195, s[76:79], 0 offen\n" \
"v_mfma_f32_16x16x32_bf16 a[60:63], v[60:63], v[172:175], a[60:63]\n" \
"v_and_b32_e32 v249, 2, v212\n" \
"s_add_u32 s98, s89, s96\n" \
"v_mfma_f32_16x16x32_bf16 a[60:63], v[64:67], v[176:179], a[60:63]\n" \
"v_add_u32_e32 v21, v215, v220\n" \
"v_add_u32_e32 v225, 4, v217\n" \
"v_mfma_f32_16x16x32_bf16 a[92:95], v[72:75], v[176:179], a[92:95]\n" \
"s_add_u32 s51, 0x4000, s98\n" \
"s_lshl_b32 s88, s97, 2\n" \
"v_mfma_f32_16x16x32_bf16 a[92:95], v[68:71], v[172:175], a[92:95]\n" \
"v_lshrrev_b32_e32 v212, 4, v20\n" \
"v_xor_b32_e64 v223, v216, v217\n" \
"v_mfma_f32_16x16x32_bf16 a[124:127], v[76:79], v[172:175], a[124:127]\n" \
"v_xor_b32_e64 v226, v216, v225\n" \
"L_pf1:\n" \
"s_cbranch_execz L_pf5\n" \
"s_nop 0\n" \
"v_mov_b32_e32 v250, 0xeeeeeeee\n" \
"v_mfma_f32_16x16x32_bf16 a[124:127], v[80:83], v[176:179], a[124:127]\n" \
"v_mov_b32_e32 v251, 0x44444444\n" \
"v_cmp_ne_u32_e32 vcc, 0, v249\n" \
"v_mfma_f32_16x16x32_bf16 a[144:147], v[88:91], v[152:155], a[144:147]\n" \
"s_add_u32 s89, s88, s101\n" \
"s_lshl_b32 s98, %[w], 10\n" \
"v_mfma_f32_16x16x32_bf16 a[144:147], v[84:87], v[148:151], a[144:147]\n" \
"v_and_b32_e32 v215, 15, v212\n" \
"v_cndmask_b32_e32 v50, v251, v250, vcc\n" \
"v_mfma_f32_16x16x32_bf16 a[176:179], v[92:95], v[148:151], a[176:179]\n" \
"s_lshl_b32 s96, s89, 9\n" \
"s_add_u32 s40, 0x14000, s98\n" \
"v_mfma_f32_16x16x32_bf16 a[176:179], v[96:99], v[152:155], a[176:179]\n" \
"s_lshl_b32 s97, s89, 20\n" \
"v_lshrrev_b32_e32 v213, 4, v212\n" \
"v_mfma_f32_16x16x32_bf16 a[208:211], v[104:107], v[152:155], a[208:211]\n" \
"v_and_b32_e32 v216, 1, v215\n" \
"v_lshlrev_b32_e32 v228, 3, v217\n" \
"v_mfma_f32_16x16x32_bf16 a[208:211], v[100:103], v[148:151], a[208:211]\n" \
"v_and_b32_e32 v238, 1, v217\n" \
"v_lshlrev_b32_e32 v242, 4, v217\n" \
"v_mfma_f32_16x16x32_bf16 a[240:243], v[108:111], v[148:151], a[240:243]\n" \
"s_add_u32 s50, s99, s96\n" \
"s_add_u32 s88, 0x1000000, s97\n" \
"v_mfma_f32_16x16x32_bf16 a[240:243], v[112:115], v[152:155], a[240:243]\n" \
"s_lshl_b32 s101, %[w], 12\n" \
"s_lshr_b32 s98, %[w], 1\n" \
"v_mfma_f32_16x16x32_bf16 a[148:151], v[88:91], v[160:163], a[148:151]\n" \
"s_add_u32 s62, s22, 0x200\n" \
"s_and_b32 s62, s62, 0x3ff\n" \
"v_mfma_f32_16x16x32_bf16 a[148:151], v[84:87], v[156:159], a[148:151]\n" \
"s_lshl_b32 s63, s62, 12\n" \
"v_lshlrev_b32_e32 v214, 2, v213\n" \
"v_mfma_f32_16x16x32_bf16 a[180:183], v[92:95], v[156:159], a[180:183]\n" \
"v_mov_b32_e32 v218, 0x1000c0c\n" \
"v_mov_b32_e32 v219, 0xc0c0100\n" \
"v_mfma_f32_16x16x32_bf16 a[180:183], v[96:99], v[160:163], a[180:183]\n" \
"v_cmp_ne_u32_e32 vcc, 0, v216\n" \
"v_mul_u32_u24_e32 v222, 0x480, v221\n" \
"v_mfma_f32_16x16x32_bf16 a[212:215], v[104:107], v[160:163], a[212:215]\n" \
"s_add_u32 s52, s88, s101\n" \
"s_lshl_b32 s99, %[w], 11\n" \
"v_mfma_f32_16x16x32_bf16 a[212:215], v[100:103], v[156:159], a[212:215]\n" \
"s_lshl_b32 s89, s98, 15\n" \
"v_add_u32_e32 v217, v214, v216\n" \
"v_mfma_f32_16x16x32_bf16 a[244:247], v[108:111], v[156:159], a[244:247]\n" \
"v_cndmask_b32_e32 v220, v219, v218, vcc\n" \
"v_lshlrev_b32_e32 v224, 4, v223\n" \
"v_mfma_f32_16x16x32_bf16 a[244:247], v[112:115], v[160:163], a[244:247]\n" \
"s_add_u32 s96, s88, s99\n" \
"s_add_u32 s97, s88, s89\n" \
"s_waitcnt vmcnt(6)\n" \
"s_barrier\n" \
"v_mfma_f32_16x16x32_bf16 a[152:155], v[88:91], v[168:171], a[152:155]\n" \
"s_add_u32 m0, %[mA], 0x0\n" \
"s_add_u32 s85, s62, 0x0\n" \
"v_mfma_f32_16x16x32_bf16 a[152:155], v[84:87], v[164:167], a[152:155]\n" \
"buffer_load_dwordx4 v22, s[64:67], s85 offen lds\n" \
"v_mfma_f32_16x16x32_bf16 a[184:187], v[92:95], v[164:167], a[184:187]\n" \
"s_add_u32 m0, %[mA], 0x400\n" \
"s_add_u32 s84, s62, 0x2000\n" \
"v_mfma_f32_16x16x32_bf16 a[184:187], v[96:99], v[168:171], a[184:187]\n" \
"ds_read_b64_tr_b16 v[152:153], v32 offset:57344\n" \
"ds_read_b64_tr_b16 v[154:155], v32 offset:58368\n" \
"v_mfma_f32_16x16x32_bf16 a[216:219], v[104:107], v[168:171], a[216:219]\n" \
"buffer_load_dwordx4 v23, s[64:67], s84 offen lds\n" \
"v_mfma_f32_16x16x32_bf16 a[216:219], v[100:103], v[164:167], a[216:219]\n" \
"ds_read_b128 v[56:59], v29 offset:49152\n" \
"ds_read_b64_tr_b16 v[148:149], v32 offset:49152\n" \
"v_mfma_f32_16x16x32_bf16 a[248:251], v[108:111], v[164:167], a[248:251]\n" \
"s_add_u32 m0, %[mB], 0x0\n" \
"s_add_u32 s85, s63, 0x0\n" \
"v_mfma_f32_16x16x32_bf16 a[248:251], v[112:115], v[168:171], a[248:251]\n" \
"buffer_load_dwordx4 v26, s[68:71], s85 offen lds\n" \
"v_mfma_f32_16x16x32_bf16 a[156:159], v[88:91], v[176:179], a[156:159]\n" \
"ds_read_b64_tr_b16 v[150:151], v32 offset:50176\n" \
"ds_read_b128 v[52:55], v28 offset:49152\n" \
"v_mfma_f32_16x16x32_bf16 a[156:159], v[84:87], v[172:175], a[156:159]\n" \
"ds_read_b128 v[60:63], v28 offset:51200\n" \
"ds_read_b128 v[64:67], v29 offset:51200\n" \
"v_mfma_f32_16x16x32_bf16 a[188:191], v[92:95], v[172:175], a[188:191]\n" \
"s_add_u32 m0, %[mB], 0x4000\n" \
"s_add_u32 s84, s63, 0x0\n" \
"s_nop 0\n" \
"v_mfma_f32_16x16x32_bf16 a[188:191], v[96:99], v[176:179], a[188:191]\n" \
"buffer_load_dwordx4 v26, s[72:75], s84 offen lds\n" \
"v_mfma_f32_16x16x32_bf16 a[220:223], v[104:107], v[176:179], a[220:223]\n" \
"ds_read_b128 v[72:75], v29 offset:53248\n" \
"ds_read_b128 v[68:71], v28 offset:53248\n" \
"v_mfma_f32_16x16x32_bf16 a[220:223], v[100:103], v[172:175], a[220:223]\n" \
"ds_read_b128 v[76:79], v28 offset:55296\n" \
"ds_read_b128 v[80:83], v29 offset:55296\n" \
"v_mfma_f32_16x16x32_bf16 a[252:255], v[108:111], v[172:175], a[252:255]\n" \
"s_add_u32 m0, %[mA], 0x800\n" \
"s_add_u32 s85, s62, 0x4000\n" \
"v_mfma_f32_16x16x32_bf16 a[252:255], v[112:115], v[176:179], a[252:255]\n" \
"buffer_load_dwordx4 v22, s[64:67], s85 offen lds\n" \
"v_mfma_f32_16x16x32_bf16 a[128:131], v[120:123], v[88:91], a[128:131]\n" \
"ds_read_b64_tr_b16 v[160:161], v33 offset:57344\n" \
"ds_read_b64_tr_b16 v[162:163], v33 offset:58368\n" \
"v_mfma_f32_16x16x32_bf16 a[128:131], v[116:119], v[84:87], a[128:131]\n" \
"s_add_u32 m0, %[mA], 0xc00\n" \
"s_add_u32 s84, s62, 0x6000\n" \
"v_mfma_f32_16x16x32_bf16 a[160:163], v[116:119], v[92:95], a[160:163]\n" \
"buffer_load_dwordx4 v23, s[64:67], s84 offen lds\n" \
"v_mfma_f32_16x16x32_bf16 a[160:163], v[120:123], v[96:99], a[160:163]\n" \
"ds_read_b64_tr_b16 v[156:157], v33 offset:49152\n" \
"ds_read_b64_tr_b16 v[158:159], v33 offset:50176\n" \
"v_mfma_f32_16x16x32_bf16 a[192:195], v[120:123], v[104:107], a[192:195]\n" \
"ds_read_b64_tr_b16 v[168:169], v34 offset:57344\n" \
"ds_read_b64_tr_b16 v[170:171], v34 offset:58368\n" \
"v_mfma_f32_16x16x32_bf16 a[192:195], v[116:119], v[100:103], a[192:195]\n" \
"s_add_u32 m0, %[mB], 0x400\n" \
"s_add_u32 s85, s63, 0x8000\n" \
"v_mfma_f32_16x16x32_bf16 a[224:227], v[116:119], v[108:111], a[224:227]\n" \
"buffer_load_dwordx4 v26, s[68:71], s85 offen lds\n" \
"v_mfma_f32_16x16x32_bf16 a[224:227], v[120:123], v[112:115], a[224:227]\n" \
"ds_read_b64_tr_b16 v[164:165], v34 offset:49152\n" \
"ds_read_b64_tr_b16 v[166:167], v34 offset:50176\n" \
"v_mfma_f32_16x16x32_bf16 a[132:135], v[128:131], v[88:91], a[132:135]\n" \
"ds_read_b64_tr_b16 v[176:177], v35 offset:57344\n" \
"ds_read_b64_tr_b16 v[178:179], v35 offset:58368\n" \
"v_mfma_f32_16x16x32_bf16 a[132:135], v[124:127], v[84:87], a[132:135]\n" \
"s_add_u32 m0, %[mB], 0x4400\n" \
"s_add_u32 s84, s63, 0x8000\n" \
"v_mfma_f32_16x16x32_bf16 a[164:167], v[124:127], v[92:95], a[164:167]\n" \
"buffer_load_dwordx4 v26, s[72:75], s84 offen lds\n" \
"v_mfma_f32_16x16x32_bf16 a[164:167], v[128:131], v[96:99], a[164:167]\n" \
"ds_read_b64_tr_b16 v[172:173], v35 offset:49152\n" \
"ds_read_b64_tr_b16 v[174:175], v35 offset:50176\n" \
"v_mfma_f32_16x16x32_bf16 a[196:199], v[128:131], v[104:107], a[196:199]\n" \
"ds_read_b64_tr_b16 v[120:121], v32 offset:40960\n" \
"ds_read_b64_tr_b16 v[122:123], v32 offset:41984\n" \
"v_mfma_f32_16x16x32_bf16 a[196:199], v[124:127], v[100:103], a[196:199]\n" \
"s_add_u32 m0, %[mA], 0x1000\n" \
"s_add_u32 s85, s62, 0x8000\n" \
"v_mfma_f32_16x16x32_bf16 a[228:231], v[124:127], v[108:111], a[228:231]\n" \
"buffer_load_dwordx4 v22, s[64:67], s85 offen lds\n" \
"v_mfma_f32_16x16x32_bf16 a[228:231], v[128:131], v[112:115], a[228:231]\n" \
"ds_read_b64_tr_b16 v[116:117], v32 offset:32768\n" \
"ds_read_b64_tr_b16 v[118:119], v32 offset:33792\n" \
"v_mfma_f32_16x16x32_bf16 a[136:139], v[136:139], v[88:91], a[136:139]\n" \
"s_add_u32 m0, %[mA], 0x1400\n" \
"s_add_u32 s84, s62, 0xa000\n" \
"v_mfma_f32_16x16x32_bf16 a[136:139], v[132:135], v[84:87], a[136:139]\n" \
"buffer_load_dwordx4 v23, s[64:67], s84 offen lds\n" \
"v_mfma_f32_16x16x32_bf16 a[168:171], v[132:135], v[92:95], a[168:171]\n" \
"ds_read_b64_tr_b16 v[128:129], v33 offset:40960\n" \
"ds_read_b64_tr_b16 v[130:131], v33 offset:41984\n" \
"v_mfma_f32_16x16x32_bf16 a[168:171], v[136:139], v[96:99], a[168:171]\n" \
"ds_read_b64_tr_b16 v[124:125], v33 offset:32768\n" \
"ds_read_b64_tr_b16 v[126:127], v33 offset:33792\n" \
"v_mfma_f32_16x16x32_bf16 a[200:203], v[136:139], v[104:107], a[200:203]\n" \
"s_add_u32 m0, %[mB], 0x800\n" \
"s_add_u32 s85, s63, 0x10000\n" \
"v_mfma_f32_16x16x32_bf16 a[200:203], v[132:135], v[100:103], a[200:203]\n" \
"buffer_load_dwordx4 v27, s[68:71], s85 offen lds\n" \
"v_mfma_f32_16x16x32_bf16 a[232:235], v[132:135], v[108:111], a[232:235]\n" \
"ds_read_b64_tr_b16 v[132:133], v34 offset:32768\n" \
"ds_read_b64_tr_b16 v[134:135], v34 offset:33792\n" \
"v_mfma_f32_16x16x32_bf16 a[232:235], v[136:139], v[112:115], a[232:235]\n" \
"ds_read_b64_tr_b16 v[136:137], v34 offset:40960\n" \
"ds_read_b64_tr_b16 v[138:139], v34 offset:41984\n" \
"v_mfma_f32_16x16x32_bf16 a[140:143], v[144:147], v[88:91], a[140:143]\n" \
"s_add_u32 m0, %[mB], 0x4800\n" \
"s_add_u32 s84, s63, 0x10000\n" \
"v_mfma_f32_16x16x32_bf16 a[140:143], v[140:143], v[84:87], a[140:143]\n" \
"buffer_load_dwordx4 v27, s[72:75], s84 offen lds\n" \
"v_mfma_f32_16x16x32_bf16 a[172:175], v[140:143], v[92:95], a[172:175]\n" \
"ds_read_b128 v[84:87], v28 offset:32768\n" \
"ds_read_b128 v[88:91], v29 offset:32768\n" \
"v_mfma_f32_16x16x32_bf16 a[172:175], v[144:147], v[96:99], a[172:175]\n" \
"ds_read_b128 v[92:95], v28 offset:34816\n" \
"ds_read_b128 v[96:99], v29 offset:34816\n" \
"v_mfma_f32_16x16x32_bf16 a[204:207], v[144:147], v[104:107], a[204:207]\n" \
"s_add_u32 m0, %[mA], 0x1800\n" \
"s_add_u32 s85, s62, 0xc000\n" \
"v_mfma_f32_16x16x32_bf16 a[204:207], v[140:143], v[100:103], a[204:207]\n" \
"buffer_load_dwordx4 v22, s[64:67], s85 offen lds\n" \
"v_mfma_f32_16x16x32_bf16 a[236:239], v[140:143], v[108:111], a[236:239]\n" \
"ds_read_b64_tr_b16 v[140:141], v35 offset:32768\n" \
"ds_read_b64_tr_b16 v[142:143], v35 offset:33792\n" \
"v_mfma_f32_16x16x32_bf16 a[236:239], v[144:147], v[112:115], a[236:239]\n" \
"s_add_u32 m0, %[mA], 0x1c00\n" \
"s_waitcnt lgkmcnt(14)\n" \
"s_nop 0\n" \
"v_mfma_f32_16x16x32_bf16 a[16:19], v[56:59], v[152:155], a[16:19]\n" \
"s_add_u32 s84, s62, 0xe000\n" \
"ds_read_b64_tr_b16 v[144:145], v35 offset:40960\n" \
"v_mfma_f32_16x16x32_bf16 a[16:19], v[52:55], v[148:151], a[16:19]\n" \
"buffer_load_dwordx4 v23, s[64:67], s84 offen lds\n" \
"v_mfma_f32_16x16x32_bf16 a[48:51], v[60:63], v[148:151], a[48:51]\n" \
"ds_read_b64_tr_b16 v[146:147], v35 offset:41984\n" \
"ds_read_b128 v[100:103], v28 offset:36864\n" \
"v_mfma_f32_16x16x32_bf16 a[48:51], v[64:67], v[152:155], a[48:51]\n" \
"s_add_u32 m0, %[mB], 0xc00\n" \
"s_add_u32 s85, s63, 0x18000\n" \
"v_mfma_f32_16x16x32_bf16 a[80:83], v[72:75], v[152:155], a[80:83]\n" \
"buffer_load_dwordx4 v27, s[68:71], s85 offen lds\n" \
"v_mfma_f32_16x16x32_bf16 a[80:83], v[68:71], v[148:151], a[80:83]\n" \
"ds_read_b128 v[104:107], v29 offset:36864\n" \
"ds_read_b128 v[108:111], v28 offset:38912\n" \
"v_mfma_f32_16x16x32_bf16 a[112:115], v[76:79], v[148:151], a[112:115]\n" \
"ds_read_b128 v[112:115], v29 offset:38912\n" \
"s_lshl_b32 s101, s100, 11\n" \
"v_mfma_f32_16x16x32_bf16 a[112:115], v[80:83], v[152:155], a[112:115]\n" \
"s_add_u32 m0, %[mB], 0x4c00\n" \
"s_add_u32 s84, s63, 0x18000\n" \
"v_mfma_f32_16x16x32_bf16 a[20:23], v[56:59], v[160:163], a[20:23]\n" \
"buffer_load_dwordx4 v27, s[72:75], s84 offen lds\n" \
"v_mfma_f32_16x16x32_bf16 a[20:23], v[52:55], v[156:159], a[20:23]\n" \
"s_mov_b32 m0, %[mS]\n" \
"v_mov_b32_e32 v221, 0xc0c0c0c\n" \
"v_mfma_f32_16x16x32_bf16 a[52:55], v[60:63], v[156:159], a[52:55]\n" \
"v_cmp_le_u32_e32 vcc, v217, v215\n" \
"v_add_u32_e32 v24, v222, v224\n" \
"v_mfma_f32_16x16x32_bf16 a[52:55], v[64:67], v[160:163], a[52:55]\n" \
"v_lshlrev_b32_e32 v227, 4, v226\n" \
"s_add_u32 s99, s97, s101\n" \
"v_mfma_f32_16x16x32_bf16 a[84:87], v[72:75], v[160:163], a[84:87]\n" \
"s_lshl_b32 s98, s95, 16\n" \
"v_cndmask_b32_e32 v40, v220, v221, vcc\n" \
"v_mfma_f32_16x16x32_bf16 a[84:87], v[68:71], v[156:159], a[84:87]\n" \
"v_add_u32_e64 v25, v222, v227\n" \
"s_add_u32 s54, 0x10000, s96\n" \
"v_mfma_f32_16x16x32_bf16 a[116:119], v[76:79], v[156:159], a[116:119]\n" \
"s_add_u32 s43, s99, s98\n" \
"s_mul_i32 s88, 0x1800, s100\n" \
"v_mfma_f32_16x16x32_bf16 a[116:119], v[80:83], v[160:163], a[116:119]\n" \
"v_mov_b32_e32 v223, 0x3020c0c\n" \
"v_mov_b32_e32 v224, 0xc0c0302\n" \
"v_mfma_f32_16x16x32_bf16 a[24:27], v[56:59], v[168:171], a[24:27]\n" \
"v_cmp_ne_u32_e32 vcc, 0, v216\n" \
"s_lshl_b32 s96, s94, 16\n" \
"v_mfma_f32_16x16x32_bf16 a[24:27], v[52:55], v[164:167], a[24:27]\n" \
"s_add_u32 s97, s43, s88\n" \
"v_add_u32_e64 v222, 2, v217\n" \
"v_mfma_f32_16x16x32_bf16 a[56:59], v[60:63], v[164:167], a[56:59]\n" \
"v_cndmask_b32_e32 v225, v224, v223, vcc\n" \
"s_add_u32 s42, s99, s96\n" \
"v_mfma_f32_16x16x32_bf16 a[56:59], v[64:67], v[168:171], a[56:59]\n" \
"s_add_u32 s45, 0x4000, s97\n" \
"s_lshr_b32 s94, %[w], 1\n" \
"v_mfma_f32_16x16x32_bf16 a[88:91], v[72:75], v[168:171], a[88:91]\n" \
"s_mov_b32 s86, s10\n" \
"s_and_b32 s95, 1, %[w]\n" \
"v_mfma_f32_16x16x32_bf16 a[88:91], v[68:71], v[164:167], a[88:91]\n" \
"v_mov_b32_e32 v226, 0xc0c0c0c\n" \
"v_cmp_le_u32_e32 vcc, v222, v215\n" \
"s_mov_b32 exec_lo, 0x15\n" \
"s_mov_b32 exec_hi, 0x180000\n" \
"buffer_load_dword v11, s[80:83], 0 offen sc1 lds\n" \
"s_mov_b64 exec, -1\n" \
"v_mfma_f32_16x16x32_bf16 a[120:123], v[76:79], v[164:167], a[120:123]\n" \
"s_mov_b32 m0, %[mS]\n" \
"s_add_u32 s96, s94, s95\n" \
"v_mfma_f32_16x16x32_bf16 a[120:123], v[80:83], v[168:171], a[120:123]\n" \
"s_add_u32 s97, 1, s94\n" \
"v_cndmask_b32_e32 v41, v225, v226, vcc\n" \
"v_mfma_f32_16x16x32_bf16 a[28:31], v[56:59], v[176:179], a[28:31]\n" \
"v_and_b32_e32 v230, 3, v229\n" \
"s_add_u32 s89, s42, s88\n" \
"v_mfma_f32_16x16x32_bf16 a[28:31], v[52:55], v[172:175], a[28:31]\n" \
"s_lshl_b32 s59, s96, 13\n" \
"s_sub_u32 s98, s97, s95\n" \
"v_mfma_f32_16x16x32_bf16 a[60:63], v[60:63], v[172:175], a[60:63]\n" \
"s_lshl_b32 s99, s94, 13\n" \
"s_mul_i32 s101, 0x1800, s94\n" \
"v_mfma_f32_16x16x32_bf16 a[60:63], v[64:67], v[176:179], a[60:63]\n" \
"v_mov_b32_e32 v227, 0xc0c0c0c\n" \
"v_cmp_le_u32_e32 vcc, v217, v215\n" \
"v_mfma_f32_16x16x32_bf16 a[92:95], v[72:75], v[176:179], a[92:95]\n" \
"v_add_u32_e32 v231, v228, v230\n" \
"s_lshl_b32 s60, s98, 13\n" \
"v_mfma_f32_16x16x32_bf16 a[92:95], v[68:71], v[172:175], a[92:95]\n" \
"s_add_u32 s100, 0x1800, s99\n" \
"s_sub_u32 s88, 0x17000, s101\n" \
"v_mfma_f32_16x16x32_bf16 a[124:127], v[76:79], v[172:175], a[124:127]\n" \
"s_lshl_b32 s96, s94, 15\n" \
"v_cndmask_b32_e32 v42, v227, v220, vcc\n" \
"v_mfma_f32_16x16x32_bf16 a[124:127], v[80:83], v[176:179], a[124:127]\n" \
"v_lshlrev_b32_e32 v232, 7, v231\n" \
"s_add_u32 s97, 0x10000, s96\n" \
"v_mfma_f32_16x16x32_bf16 a[0:3], v[120:123], v[56:59], a[0:3]\n" \
"s_cmp_lg_u32 s95, 0\n" \
"s_cselect_b32 s61, s100, s88\n" \
"v_mfma_f32_16x16x32_bf16 a[0:3], v[116:119], v[52:55], a[0:3]\n" \
"s_lshl_b32 s98, s95, 11\n" \
"v_mov_b32_e32 v228, 0xc0c0c0c\n" \
"v_mfma_f32_16x16x32_bf16 a[32:35], v[116:119], v[60:63], a[32:35]\n" \
"v_cmp_le_u32_e32 vcc, v222, v215\n" \
"v_add_u32_e32 v233, 0x14000, v232\n" \
"v_mfma_f32_16x16x32_bf16 a[32:35], v[120:123], v[64:67], a[32:35]\n" \
"v_lshlrev_b32_e32 v235, 3, v234\n" \
"s_add_u32 s101, s97, s98\n" \
"v_mfma_f32_16x16x32_bf16 a[64:67], v[120:123], v[72:75], a[64:67]\n" \
"v_cndmask_b32_e32 v43, v228, v225, vcc\n" \
"v_and_b32_e64 v231, 2, v212\n" \
"v_mfma_f32_16x16x32_bf16 a[64:67], v[116:119], v[68:71], a[64:67]\n" \
"s_waitcnt vmcnt(19)\n" \
"v_perm_b32 v252, v209, v208, s87\n" \
"s_mov_b32 s87, 0x7060302\n" \
"v_perm_b32 v253, v209, v208, s87\n" \
"s_mov_b32 s87, 0x5040100\n" \
"s_waitcnt vmcnt(17)\n" \
"v_perm_b32 v254, v211, v210, s87\n" \
"s_mov_b32 s87, 0x7060100\n" \
"v_perm_b32 v255, v211, v210, s87\n" \
"s_mov_b32 s87, s11\n" \
"s_mov_b32 exec_lo, 0x3ffff\n" \
"s_mov_b32 exec_hi, 0x0\n" \
"global_load_lds_dword v188, s[86:87]\n" \
"s_mov_b64 exec, -1\n" \
"v_mfma_f32_16x16x32_bf16 a[96:99], v[116:119], v[76:79], a[96:99]\n" \
"ds_write_b128 v196, v[200:203]\n" \
"v_mfma_f32_16x16x32_bf16 a[96:99], v[120:123], v[80:83], a[96:99]\n" \
"ds_write2_b32 v198, v252, v253 offset0:0 offset1:2\n" \
"s_waitcnt lgkmcnt(2)\n" \
"v_mfma_f32_16x16x32_bf16 a[4:7], v[128:131], v[56:59], a[4:7]\n" \
"ds_write2_b32 v198, v254, v255 offset0:4 offset1:6\n" \
"v_add_u32_e64 v236, v233, v235\n" \
"v_mfma_f32_16x16x32_bf16 a[4:7], v[124:127], v[52:55], a[4:7]\n" \
"v_lshrrev_b32_e32 v237, 1, v230\n" \
"v_add_u32_e32 v243, v242, v230\n" \
"v_mfma_f32_16x16x32_bf16 a[36:39], v[124:127], v[60:63], a[36:39]\n" \
"s_add_u32 s100, s101, s99\n" \
"v_and_b32_e32 v229, 3, v212\n" \
"v_mfma_f32_16x16x32_bf16 a[36:39], v[128:131], v[64:67], a[36:39]\n" \
"ds_write_b128 v197, v[204:207]\n" \
"v_mfma_f32_16x16x32_bf16 a[68:71], v[128:131], v[72:75], a[68:71]\n" \
"v_mov_b32_e32 v232, 0x3f80\n" \
"v_cmp_eq_u32_e32 vcc, 0, v231\n" \
"v_mfma_f32_16x16x32_bf16 a[68:71], v[124:127], v[68:71], a[68:71]\n" \
"v_lshlrev_b32_e32 v239, 1, v238\n" \
"v_lshlrev_b32_e32 v244, 7, v243\n" \
"v_mfma_f32_16x16x32_bf16 a[100:103], v[124:127], v[76:79], a[100:103]\n" \
"s_add_u32 s88, 0x4000, s100\n" \
"v_or_b32_e32 v230, 2, v229\n" \
"v_mfma_f32_16x16x32_bf16 a[100:103], v[128:131], v[80:83], a[100:103]\n" \
"v_cndmask_b32_e32 v233, 0, v232, vcc\n" \
"v_or_b32_e32 v240, v237, v239\n" \
"v_mfma_f32_16x16x32_bf16 a[8:11], v[136:139], v[56:59], a[8:11]\n" \
"v_add_u32_e32 v245, 0x14000, v244\n" \
"v_cmp_eq_u32_e32 vcc, v230, v213\n" \
"v_mfma_f32_16x16x32_bf16 a[8:11], v[132:135], v[52:55], a[8:11]\n" \
"s_cmp_lg_u32 s95, 0\n" \
"s_cselect_b32 s47, s88, s101\n" \
"v_mfma_f32_16x16x32_bf16 a[40:43], v[132:135], v[60:63], a[40:43]\n" \
"v_lshlrev_b32_e32 v241, 5, v240\n" \
"v_add_u32_e64 v246, v245, v235\n" \
"v_mfma_f32_16x16x32_bf16 a[40:43], v[136:139], v[64:67], a[40:43]\n" \
"v_cndmask_b32_e32 v46, 0, v233, vcc\n" \
"v_and_b32_e32 v234, 51, v212\n" \
"v_mfma_f32_16x16x32_bf16 a[72:75], v[136:139], v[72:75], a[72:75]\n" \
"v_add_u32_e64 v190, v236, v241\n" \
"v_mov_b32_e32 v235, 0x3f80\n" \
"v_mfma_f32_16x16x32_bf16 a[72:75], v[132:135], v[68:71], a[72:75]\n" \
"v_cmp_eq_u32_e32 vcc, 2, v234\n" \
"s_lshl_b32 s88, s95, 4\n" \
"v_mfma_f32_16x16x32_bf16 a[104:107], v[132:135], v[76:79], a[104:107]\n" \
"v_xor_b32_e32 v247, 3, v237\n" \
"v_cndmask_b32_e32 v47, 0, v235, vcc\n" \
"v_mfma_f32_16x16x32_bf16 a[104:107], v[136:139], v[80:83], a[104:107]\n" \
"v_add_u32_e64 v236, s88, v215\n" \
"v_mov_b32_e32 v237, 0xffff\n" \
"v_mfma_f32_16x16x32_bf16 a[12:15], v[144:147], v[56:59], a[12:15]\n" \
"s_add_u32 s62, s22, 0x280\n" \
"s_and_b32 s62, s62, 0x3ff\n" \
"v_mfma_f32_16x16x32_bf16 a[12:15], v[140:143], v[52:55], a[12:15]\n" \
"s_lshl_b32 s63, s62, 12\n" \
"v_cmp_le_u32_e32 vcc, v214, v236\n" \
"v_mfma_f32_16x16x32_bf16 a[44:47], v[140:143], v[60:63], a[44:47]\n" \
"v_mov_b32_e32 v239, 0xffff0000\n" \
"v_cndmask_b32_e32 v238, 0, v237, vcc\n" \
"v_mfma_f32_16x16x32_bf16 a[44:47], v[144:147], v[64:67], a[44:47]\n" \
"v_cmp_lt_u32_e32 vcc, v214, v236\n" \
"v_add_u32_e64 v241, 2, v214\n" \
"v_mfma_f32_16x16x32_bf16 a[76:79], v[144:147], v[72:75], a[76:79]\n" \
"v_cndmask_b32_e32 v240, 0, v239, vcc\n" \
"v_mov_b32_e32 v242, 0xffff\n" \
"v_mfma_f32_16x16x32_bf16 a[76:79], v[140:143], v[68:71], a[76:79]\n" \
"v_cmp_le_u32_e32 vcc, v241, v236\n" \
"v_lshlrev_b32_e32 v248, 5, v247\n" \
"v_mfma_f32_16x16x32_bf16 a[108:111], v[140:143], v[76:79], a[108:111]\n" \
"v_cndmask_b32_e32 v243, 0, v242, vcc\n" \
"v_add_u32_e64 v39, v246, v248\n" \
"v_mfma_f32_16x16x32_bf16 a[108:111], v[144:147], v[80:83], a[108:111]\n" \
"v_mov_b32_e32 v244, 0xffff0000\n" \
"v_cmp_lt_u32_e32 vcc, v241, v236\n" \
"s_waitcnt vmcnt(2) lgkmcnt(0)\n" \
"s_barrier\n" \
"v_mfma_f32_16x16x32_bf16 a[128:131], v[120:123], v[88:91], a[128:131]\n" \
"s_add_u32 m0, %[mA], 0x8000\n" \
"s_add_u32 s85, s62, 0x0\n" \
"v_mfma_f32_16x16x32_bf16 a[128:131], v[116:119], v[84:87], a[128:131]\n" \
"buffer_load_dwordx4 v22, s[64:67], s85 offen lds\n" \
"v_mfma_f32_16x16x32_bf16 a[160:163], v[116:119], v[92:95], a[160:163]\n" \
"ds_read_b128 v[56:59], v29 offset:16384\n" \
"ds_read_b128 v[52:55], v28 offset:16384\n" \
"v_mfma_f32_16x16x32_bf16 a[160:163], v[120:123], v[96:99], a[160:163]\n" \
"s_add_u32 m0, %[mA], 0x8400\n" \
"s_add_u32 s84, s62, 0x2000\n" \
"v_mfma_f32_16x16x32_bf16 a[192:195], v[120:123], v[104:107], a[192:195]\n" \
"buffer_load_dwordx4 v23, s[64:67], s84 offen lds\n" \
"v_mfma_f32_16x16x32_bf16 a[192:195], v[116:119], v[100:103], a[192:195]\n" \
"ds_read_b128 v[60:63], v28 offset:18432\n" \
"ds_read_b128 v[64:67], v29 offset:18432\n" \
"v_mfma_f32_16x16x32_bf16 a[224:227], v[116:119], v[108:111], a[224:227]\n" \
"ds_read_b64_tr_b16 v[116:117], v32 offset:0\n" \
"ds_read_b64_tr_b16 v[118:119], v32 offset:1024\n" \
"v_mfma_f32_16x16x32_bf16 a[224:227], v[120:123], v[112:115], a[224:227]\n" \
"s_add_u32 m0, %[mB], 0x8000\n" \
"s_add_u32 s85, s63, 0x0\n" \
"v_mfma_f32_16x16x32_bf16 a[132:135], v[128:131], v[88:91], a[132:135]\n" \
"buffer_load_dwordx4 v26, s[68:71], s85 offen lds\n" \
"v_mfma_f32_16x16x32_bf16 a[132:135], v[124:127], v[84:87], a[132:135]\n" \
"ds_read_b64_tr_b16 v[120:121], v32 offset:8192\n" \
"ds_read_b64_tr_b16 v[122:123], v32 offset:9216\n" \
"v_mfma_f32_16x16x32_bf16 a[164:167], v[124:127], v[92:95], a[164:167]\n" \
"ds_read_b128 v[72:75], v29 offset:20480\n" \
"ds_read_b128 v[68:71], v28 offset:20480\n" \
"v_mfma_f32_16x16x32_bf16 a[164:167], v[128:131], v[96:99], a[164:167]\n" \
"s_add_u32 m0, %[mB], 0xc000\n" \
"s_add_u32 s84, s63, 0x0\n" \
"v_mfma_f32_16x16x32_bf16 a[196:199], v[128:131], v[104:107], a[196:199]\n" \
"buffer_load_dwordx4 v26, s[72:75], s84 offen lds\n" \
"v_mfma_f32_16x16x32_bf16 a[196:199], v[124:127], v[100:103], a[196:199]\n" \
"ds_read_b128 v[76:79], v28 offset:22528\n" \
"ds_read_b128 v[80:83], v29 offset:22528\n" \
"v_mfma_f32_16x16x32_bf16 a[228:231], v[124:127], v[108:111], a[228:231]\n" \
"ds_read_b64_tr_b16 v[124:125], v33 offset:0\n" \
"ds_read_b64_tr_b16 v[126:127], v33 offset:1024\n" \
"v_mfma_f32_16x16x32_bf16 a[228:231], v[128:131], v[112:115], a[228:231]\n" \
"s_add_u32 m0, %[mA], 0x8800\n" \
"s_add_u32 s85, s62, 0x4000\n" \
"v_mfma_f32_16x16x32_bf16 a[136:139], v[136:139], v[88:91], a[136:139]\n" \
"buffer_load_dwordx4 v22, s[64:67], s85 offen lds\n" \
"v_mfma_f32_16x16x32_bf16 a[136:139], v[132:135], v[84:87], a[136:139]\n" \
"ds_read_b64_tr_b16 v[128:129], v33 offset:8192\n" \
"ds_read_b64_tr_b16 v[130:131], v33 offset:9216\n" \
"v_mfma_f32_16x16x32_bf16 a[168:171], v[132:135], v[92:95], a[168:171]\n" \
"ds_read_b128 v[200:203], v199 offset:0\n" \
"ds_read_b128 v[204:207], v199 offset:4096\n" \
"v_mfma_f32_16x16x32_bf16 a[168:171], v[136:139], v[96:99], a[168:171]\n" \
"s_add_u32 m0, %[mA], 0x8c00\n" \
"s_add_u32 s84, s62, 0x6000\n" \
"v_mfma_f32_16x16x32_bf16 a[200:203], v[136:139], v[104:107], a[200:203]\n" \
"buffer_load_dwordx4 v23, s[64:67], s84 offen lds\n" \
"v_mfma_f32_16x16x32_bf16 a[200:203], v[132:135], v[100:103], a[200:203]\n" \
"ds_read_b128 v[208:211], v199 offset:8192\n" \
"v_cndmask_b32_e32 v245, 0, v244, vcc\n" \
"s_nop 0\n" \
"v_mfma_f32_16x16x32_bf16 a[232:235], v[132:135], v[108:111], a[232:235]\n" \
"ds_read_b64_tr_b16 v[132:133], v34 offset:0\n" \
"ds_read_b64_tr_b16 v[134:135], v34 offset:1024\n" \
"v_mfma_f32_16x16x32_bf16 a[232:235], v[136:139], v[112:115], a[232:235]\n" \
"s_add_u32 m0, %[mB], 0x8400\n" \
"s_add_u32 s85, s63, 0x8000\n" \
"v_mfma_f32_16x16x32_bf16 a[140:143], v[144:147], v[88:91], a[140:143]\n" \
"buffer_load_dwordx4 v26, s[68:71], s85 offen lds\n" \
"v_mfma_f32_16x16x32_bf16 a[140:143], v[140:143], v[84:87], a[140:143]\n" \
"ds_read_b64_tr_b16 v[136:137], v34 offset:8192\n" \
"ds_read_b64_tr_b16 v[138:139], v34 offset:9216\n" \
"v_mfma_f32_16x16x32_bf16 a[172:175], v[140:143], v[92:95], a[172:175]\n" \
"v_add_u32_e64 v246, 16, v214\n" \
"v_mov_b32_e32 v247, 0xffff\n" \
"v_mfma_f32_16x16x32_bf16 a[172:175], v[144:147], v[96:99], a[172:175]\n" \
"s_add_u32 m0, %[mB], 0xc400\n" \
"s_add_u32 s84, s63, 0x8000\n" \
"v_mfma_f32_16x16x32_bf16 a[204:207], v[144:147], v[104:107], a[204:207]\n" \
"buffer_load_dwordx4 v26, s[72:75], s84 offen lds\n" \
"v_mfma_f32_16x16x32_bf16 a[204:207], v[140:143], v[100:103], a[204:207]\n" \
"v_cmp_le_u32_e32 vcc, v246, v236\n" \
"s_nop 0\n" \
"s_add_u32 s44, 0x4000, s89\n" \
"v_mfma_f32_16x16x32_bf16 a[236:239], v[140:143], v[108:111], a[236:239]\n" \
"ds_read_b64_tr_b16 v[140:141], v35 offset:0\n" \
"ds_read_b64_tr_b16 v[142:143], v35 offset:1024\n" \
"v_mfma_f32_16x16x32_bf16 a[236:239], v[144:147], v[112:115], a[236:239]\n" \
"s_add_u32 m0, %[mA], 0x9000\n" \
"s_add_u32 s85, s62, 0x8000\n" \
"v_mfma_f32_16x16x32_bf16 a[144:147], v[88:91], v[152:155], a[144:147]\n" \
"buffer_load_dwordx4 v22, s[64:67], s85 offen lds\n" \
"v_mfma_f32_16x16x32_bf16 a[144:147], v[84:87], v[148:151], a[144:147]\n" \
"ds_read_b64_tr_b16 v[144:145], v35 offset:8192\n" \
"ds_read_b64_tr_b16 v[146:147], v35 offset:9216\n" \
"v_mfma_f32_16x16x32_bf16 a[176:179], v[92:95], v[148:151], a[176:179]\n" \
"v_cndmask_b32_e32 v248, 0, v247, vcc\n" \
"v_mov_b32_e32 v249, 0xffff0000\n" \
"v_mfma_f32_16x16x32_bf16 a[176:179], v[96:99], v[152:155], a[176:179]\n" \
"s_add_u32 m0, %[mA], 0x9400\n" \
"s_add_u32 s84, s62, 0xa000\n" \
"v_mfma_f32_16x16x32_bf16 a[208:211], v[104:107], v[152:155], a[208:211]\n" \
"buffer_load_dwordx4 v23, s[64:67], s84 offen lds\n" \
"v_mfma_f32_16x16x32_bf16 a[208:211], v[100:103], v[148:151], a[208:211]\n" \
"v_cmp_lt_u32_e32 vcc, v246, v236\n" \
"s_lshl_b32 s89, %[w], 8\n" \
"s_nop 0\n" \
"v_mfma_f32_16x16x32_bf16 a[240:243], v[108:111], v[148:151], a[240:243]\n" \
"ds_read_b64_tr_b16 v[148:149], v32 offset:16384\n" \
"ds_read_b64_tr_b16 v[150:151], v32 offset:17408\n" \
"v_mfma_f32_16x16x32_bf16 a[240:243], v[112:115], v[152:155], a[240:243]\n" \
"s_add_u32 m0, %[mB], 0x8800\n" \
"s_add_u32 s85, s63, 0x10000\n" \
"v_mfma_f32_16x16x32_bf16 a[148:151], v[88:91], v[160:163], a[148:151]\n" \
"buffer_load_dwordx4 v27, s[68:71], s85 offen lds\n" \
"v_mfma_f32_16x16x32_bf16 a[148:151], v[84:87], v[156:159], a[148:151]\n" \
"ds_read_b64_tr_b16 v[152:153], v32 offset:24576\n" \
"ds_read_b64_tr_b16 v[154:155], v32 offset:25600\n" \
"v_mfma_f32_16x16x32_bf16 a[180:183], v[92:95], v[156:159], a[180:183]\n" \
"v_cndmask_b32_e32 v250, 0, v249, vcc\n" \
"v_add_u32_e32 v251, 18, v214\n" \
"v_mfma_f32_16x16x32_bf16 a[180:183], v[96:99], v[160:163], a[180:183]\n" \
"s_add_u32 m0, %[mB], 0xc800\n" \
"s_add_u32 s84, s63, 0x10000\n" \
"v_mfma_f32_16x16x32_bf16 a[212:215], v[104:107], v[160:163], a[212:215]\n" \
"buffer_load_dwordx4 v27, s[72:75], s84 offen lds\n" \
"v_mfma_f32_16x16x32_bf16 a[212:215], v[100:103], v[156:159], a[212:215]\n" \
"s_add_u32 s46, 0x27000, s89\n" \
"v_mov_b32_e32 v218, 0xffff\n" \
"v_mfma_f32_16x16x32_bf16 a[244:247], v[108:111], v[156:159], a[244:247]\n" \
"ds_read_b64_tr_b16 v[156:157], v33 offset:16384\n" \
"ds_read_b64_tr_b16 v[158:159], v33 offset:17408\n" \
"v_mfma_f32_16x16x32_bf16 a[244:247], v[112:115], v[160:163], a[244:247]\n" \
"ds_read_b64_tr_b16 v[160:161], v33 offset:24576\n" \
"ds_read_b64_tr_b16 v[162:163], v33 offset:25600\n" \
"v_mfma_f32_16x16x32_bf16 a[152:155], v[88:91], v[168:171], a[152:155]\n" \
"v_cmp_le_u32_e32 vcc, v251, v236\n" \
"s_add_u32 s89, 0xffffc000, s101\n" \
"v_mfma_f32_16x16x32_bf16 a[152:155], v[84:87], v[164:167], a[152:155]\n" \
"v_cndmask_b32_e32 v219, 0, v218, vcc\n" \
"v_lshlrev_b32_e32 v224, 3, v212\n" \
"v_mfma_f32_16x16x32_bf16 a[184:187], v[92:95], v[164:167], a[184:187]\n" \
"s_cmp_lg_u32 s95, 0\n" \
"s_cselect_b32 s48, s89, s100\n" \
"v_mfma_f32_16x16x32_bf16 a[184:187], v[96:99], v[168:171], a[184:187]\n" \
"s_lshr_b32 s94, %[b], 5\n" \
"s_mov_b32 s96, 0xc000\n" \
"v_mfma_f32_16x16x32_bf16 a[216:219], v[104:107], v[168:171], a[216:219]\n" \
"s_mov_b32 s97, 0x8000\n" \
"v_mov_b32_e32 v221, 0xffff0000\n" \
"v_mfma_f32_16x16x32_bf16 a[216:219], v[100:103], v[164:167], a[216:219]\n" \
"v_cmp_lt_u32_e32 vcc, v251, v236\n" \
"v_lshrrev_b32_e32 v212, 4, v20\n" \
"v_mfma_f32_16x16x32_bf16 a[248:251], v[108:111], v[164:167], a[248:251]\n" \
"ds_read_b64_tr_b16 v[164:165], v34 offset:16384\n" \
"ds_read_b64_tr_b16 v[166:167], v34 offset:17408\n" \
"v_mfma_f32_16x16x32_bf16 a[248:251], v[112:115], v[168:171], a[248:251]\n" \
"ds_read_b64_tr_b16 v[168:169], v34 offset:24576\n" \
"ds_read_b64_tr_b16 v[170:171], v34 offset:25600\n" \
"v_mfma_f32_16x16x32_bf16 a[156:159], v[88:91], v[176:179], a[156:159]\n" \
"ds_read_b128 v[88:91], v29 offset:0\n" \
"v_cndmask_b32_e32 v216, 0, v221, vcc\n" \
"v_mfma_f32_16x16x32_bf16 a[156:159], v[84:87], v[172:175], a[156:159]\n" \
"ds_read_b128 v[84:87], v28 offset:0\n" \
"v_lshlrev_b32_e32 v223, 2, v236\n" \
"v_mfma_f32_16x16x32_bf16 a[188:191], v[92:95], v[172:175], a[188:191]\n" \
"ds_read_b128 v[92:95], v28 offset:2048\n" \
"s_lshl_b32 s101, %[w], 9\n" \
"v_mfma_f32_16x16x32_bf16 a[188:191], v[96:99], v[176:179], a[188:191]\n" \
"ds_read_b128 v[96:99], v29 offset:2048\n" \
"v_mul_u32_u24_e32 v213, 0xe39, v212\n" \
"v_mfma_f32_16x16x32_bf16 a[220:223], v[104:107], v[176:179], a[220:223]\n" \
"ds_read_b128 v[104:107], v29 offset:4096\n" \
"v_or_b32_e32 v15, v219, v216\n" \
"v_mfma_f32_16x16x32_bf16 a[220:223], v[100:103], v[172:175], a[220:223]\n" \
"ds_read_b128 v[100:103], v28 offset:4096\n" \
"v_add_u32_e32 v18, 0x27000, v223\n" \
"v_mfma_f32_16x16x32_bf16 a[252:255], v[108:111], v[172:175], a[252:255]\n" \
"ds_read_b64_tr_b16 v[172:173], v35 offset:16384\n" \
"ds_read_b64_tr_b16 v[174:175], v35 offset:17408\n" \
"v_mfma_f32_16x16x32_bf16 a[252:255], v[112:115], v[176:179], a[252:255]\n" \
"s_add_u32 m0, %[mA], 0x9800\n" \
"s_waitcnt lgkmcnt(12)\n" \
"s_nop 0\n" \
"v_mfma_f32_16x16x32_bf16 a[0:3], v[120:123], v[56:59], a[0:3]\n" \
"s_add_u32 s85, s62, 0xc000\n" \
"ds_read_b64_tr_b16 v[176:177], v35 offset:24576\n" \
"v_mfma_f32_16x16x32_bf16 a[0:3], v[116:119], v[52:55], a[0:3]\n" \
"buffer_load_dwordx4 v22, s[64:67], s85 offen lds\n" \
"v_mfma_f32_16x16x32_bf16 a[32:35], v[116:119], v[60:63], a[32:35]\n" \
"L_pf2:\n" \
"s_cbranch_execz L_pf6\n" \
"s_nop 0\n" \
"s_add_u32 m0, %[mA], 0x9c00\n" \
"s_add_u32 s84, s62, 0xe000\n" \
"v_mfma_f32_16x16x32_bf16 a[32:35], v[120:123], v[64:67], a[32:35]\n" \
"ds_read_b64_tr_b16 v[178:179], v35 offset:25600\n" \
"ds_read_b128 v[108:111], v28 offset:6144\n" \
"v_mfma_f32_16x16x32_bf16 a[64:67], v[120:123], v[72:75], a[64:67]\n" \
"buffer_load_dwordx4 v23, s[64:67], s84 offen lds\n" \
"v_mfma_f32_16x16x32_bf16 a[64:67], v[116:119], v[68:71], a[64:67]\n" \
"s_add_u32 m0, %[mB], 0x8c00\n" \
"s_add_u32 s85, s63, 0x18000\n" \
"v_mfma_f32_16x16x32_bf16 a[96:99], v[116:119], v[76:79], a[96:99]\n" \
"ds_read_b128 v[112:115], v29 offset:6144\n" \
"s_add_u32 s89, 0x27800, s101\n" \
"v_mfma_f32_16x16x32_bf16 a[96:99], v[120:123], v[80:83], a[96:99]\n" \
"buffer_load_dwordx4 v27, s[68:71], s85 offen lds\n" \
"v_mfma_f32_16x16x32_bf16 a[4:7], v[128:131], v[56:59], a[4:7]\n" \
"s_add_u32 m0, %[mB], 0xcc00\n" \
"s_add_u32 s84, s63, 0x18000\n" \
"v_mfma_f32_16x16x32_bf16 a[4:7], v[124:127], v[52:55], a[4:7]\n" \
"s_cmp_lg_u32 s94, 0\n" \
"s_cselect_b32 s53, s96, 0x8000\n" \
"v_mfma_f32_16x16x32_bf16 a[36:39], v[124:127], v[60:63], a[36:39]\n" \
"buffer_load_dwordx4 v27, s[72:75], s84 offen lds\n" \
"v_mfma_f32_16x16x32_bf16 a[36:39], v[128:131], v[64:67], a[36:39]\n" \
"s_cmp_lg_u32 s94, 0\n" \
"s_cselect_b32 s98, s97, 0x4000\n" \
"v_mfma_f32_16x16x32_bf16 a[68:71], v[128:131], v[72:75], a[68:71]\n" \
"v_lshrrev_b32_e32 v214, 16, v213\n" \
"v_add_u32_e32 v222, 64, v212\n" \
"v_mfma_f32_16x16x32_bf16 a[68:71], v[124:127], v[68:71], a[68:71]\n" \
"buffer_store_dwordx4 v[200:203], v20, s[80:83], s57 offen sc1\n" \
"v_mfma_f32_16x16x32_bf16 a[100:103], v[124:127], v[76:79], a[100:103]\n" \
"v_add_u32_e32 v19, s89, v224\n" \
"v_mul_u32_u24_e32 v216, 18, v214\n" \
"v_mfma_f32_16x16x32_bf16 a[100:103], v[128:131], v[80:83], a[100:103]\n" \
"s_and_b32 s94, 7, %[b]\n" \
"v_mul_u32_u24_e32 v223, 0xe39, v222\n" \
"v_mfma_f32_16x16x32_bf16 a[8:11], v[136:139], v[56:59], a[8:11]\n" \
"v_sub_u32_e32 v217, v212, v216\n" \
"s_lshl_b32 s95, s94, 5\n" \
"v_mfma_f32_16x16x32_bf16 a[8:11], v[132:135], v[52:55], a[8:11]\n" \
"v_lshrrev_b32_e32 v224, 16, v223\n" \
"v_add_u32_e32 v233, 0x80, v212\n" \
"v_mfma_f32_16x16x32_bf16 a[40:43], v[132:135], v[60:63], a[40:43]\n" \
"buffer_store_dwordx4 v[204:207], v20, s[80:83], s56 offen sc1\n" \
"v_mfma_f32_16x16x32_bf16 a[40:43], v[136:139], v[64:67], a[40:43]\n" \
"s_add_u32 s96, 32, s95\n" \
"v_lshlrev_b32_e32 v218, 4, v217\n" \
"v_mfma_f32_16x16x32_bf16 a[72:75], v[136:139], v[72:75], a[72:75]\n" \
"v_mul_u32_u24_e32 v226, 18, v224\n" \
"v_mul_u32_u24_e32 v234, 0xe39, v233\n" \
"v_mfma_f32_16x16x32_bf16 a[72:75], v[132:135], v[68:71], a[72:75]\n" \
"v_or_b32_e32 v13, v243, v245\n" \
"v_add_u32_e64 v219, s96, v218\n" \
"v_mfma_f32_16x16x32_bf16 a[104:107], v[132:135], v[76:79], a[104:107]\n" \
"v_sub_u32_e32 v227, v222, v226\n" \
"v_lshrrev_b32_e32 v235, 16, v234\n" \
"v_mfma_f32_16x16x32_bf16 a[104:107], v[136:139], v[80:83], a[104:107]\n" \
"v_add_u32_e32 v244, 0xc0, v212\n" \
"v_or_b32_e32 v12, v238, v240\n" \
"v_mfma_f32_16x16x32_bf16 a[12:15], v[144:147], v[56:59], a[12:15]\n" \
"buffer_store_dwordx4 v[208:211], v20, s[80:83], s58 offen sc1\n" \
"v_mfma_f32_16x16x32_bf16 a[12:15], v[140:143], v[52:55], a[12:15]\n" \
"v_and_b32_e32 v220, 0xff, v219\n" \
"v_cmp_gt_u32_e32 vcc, 16, v217\n" \
"v_mfma_f32_16x16x32_bf16 a[44:47], v[140:143], v[60:63], a[44:47]\n" \
"v_lshlrev_b32_e32 v228, 4, v227\n" \
"v_mul_u32_u24_e32 v237, 18, v235\n" \
"v_mfma_f32_16x16x32_bf16 a[44:47], v[144:147], v[64:67], a[44:47]\n" \
"v_mul_u32_u24_e32 v245, 0xe39, v244\n" \
"v_or_b32_e32 v14, v248, v250\n" \
"v_mfma_f32_16x16x32_bf16 a[76:79], v[144:147], v[72:75], a[76:79]\n" \
"v_cndmask_b32_e32 v221, v218, v220, vcc\n" \
"v_add_u32_e64 v229, s96, v228\n" \
"v_mfma_f32_16x16x32_bf16 a[76:79], v[140:143], v[68:71], a[76:79]\n" \
"v_sub_u32_e32 v238, v233, v237\n" \
"v_lshrrev_b32_e32 v246, 16, v245\n" \
"v_mfma_f32_16x16x32_bf16 a[108:111], v[140:143], v[76:79], a[108:111]\n" \
"s_mov_b32 m0, %[mS]\n" \
"s_mov_b32 s86, s10\n" \
"v_mfma_f32_16x16x32_bf16 a[108:111], v[144:147], v[80:83], a[108:111]\n" \
"s_mov_b32 s87, s11\n" \
"v_add_u32_e32 v219, 0x100, v212\n" \
"v_mfma_f32_16x16x32_bf16 a[16:19], v[56:59], v[152:155], a[16:19]\n" \
"v_mul_u32_u24_e32 v215, 0x480, v214\n" \
"v_and_b32_e32 v230, 0xff, v229\n" \
"v_mfma_f32_16x16x32_bf16 a[16:19], v[52:55], v[148:151], a[16:19]\n" \
"v_cmp_gt_u32_e32 vcc, 16, v227\n" \
"v_lshlrev_b32_e32 v239, 4, v238\n" \
"v_mfma_f32_16x16x32_bf16 a[48:51], v[60:63], v[148:151], a[48:51]\n" \
"v_mul_u32_u24_e32 v248, 18, v246\n" \
"v_mul_u32_u24_e32 v217, 0xe39, v219\n" \
"v_mfma_f32_16x16x32_bf16 a[48:51], v[64:67], v[152:155], a[48:51]\n" \
"v_add_u32_e32 v0, v215, v221\n" \
"v_cndmask_b32_e32 v231, v228, v230, vcc\n" \
"v_mfma_f32_16x16x32_bf16 a[80:83], v[72:75], v[152:155], a[80:83]\n" \
"v_add_u32_e32 v240, s96, v239\n" \
"v_sub_u32_e32 v249, v244, v248\n" \
"s_mov_b32 exec_lo, 0xfffc0000\n" \
"s_mov_b32 exec_hi, 0xf\n" \
"global_load_lds_dword v188, s[86:87]\n" \
"s_mov_b64 exec, -1\n" \
"v_mfma_f32_16x16x32_bf16 a[80:83], v[68:71], v[148:151], a[80:83]\n" \
"v_lshrrev_b32_e32 v220, 16, v217\n" \
"v_add_u32_e32 v229, 0x140, v212\n" \
"v_mfma_f32_16x16x32_bf16 a[112:115], v[76:79], v[148:151], a[112:115]\n" \
"v_mul_u32_u24_e32 v225, 0x480, v224\n" \
"v_and_b32_e32 v241, 0xff, v240\n" \
"v_mfma_f32_16x16x32_bf16 a[112:115], v[80:83], v[152:155], a[112:115]\n" \
"v_cmp_gt_u32_e32 vcc, 16, v238\n" \
"v_lshlrev_b32_e32 v250, 4, v249\n" \
"v_mfma_f32_16x16x32_bf16 a[20:23], v[56:59], v[160:163], a[20:23]\n" \
"v_mul_u32_u24_e32 v215, 18, v220\n" \
"v_mul_u32_u24_e32 v227, 0xe39, v229\n" \
"v_mfma_f32_16x16x32_bf16 a[20:23], v[52:55], v[156:159], a[20:23]\n" \
"v_add_u32_e32 v232, v225, v231\n" \
"v_cndmask_b32_e32 v242, v239, v241, vcc\n" \
"v_mfma_f32_16x16x32_bf16 a[52:55], v[60:63], v[156:159], a[52:55]\n" \
"v_add_u32_e32 v251, s96, v250\n" \
"v_sub_u32_e32 v221, v219, v215\n" \
"v_mfma_f32_16x16x32_bf16 a[52:55], v[64:67], v[160:163], a[52:55]\n" \
"v_lshrrev_b32_e32 v230, 16, v227\n" \
"v_add_u32_e32 v240, 0x180, v212\n" \
"v_mfma_f32_16x16x32_bf16 a[84:87], v[72:75], v[160:163], a[84:87]\n" \
"v_mul_u32_u24_e32 v236, 0x480, v235\n" \
"v_and_b32_e32 v213, 0xff, v251\n" \
"v_mfma_f32_16x16x32_bf16 a[84:87], v[68:71], v[156:159], a[84:87]\n" \
"v_cmp_gt_u32_e32 vcc, 16, v249\n" \
"v_lshlrev_b32_e32 v223, 4, v221\n" \
"v_mfma_f32_16x16x32_bf16 a[116:119], v[76:79], v[156:159], a[116:119]\n" \
"v_mul_u32_u24_e32 v225, 18, v230\n" \
"v_mul_u32_u24_e32 v238, 0xe39, v240\n" \
"v_mfma_f32_16x16x32_bf16 a[116:119], v[80:83], v[160:163], a[116:119]\n" \
"v_add_u32_e32 v1, 0xfffffc00, v232\n" \
"s_waitcnt lgkmcnt(0)\n" \
"v_mfma_f32_16x16x32_bf16 a[24:27], v[56:59], v[168:171], a[24:27]\n" \
"v_add_u32_e32 v243, v236, v242\n" \
"v_cndmask_b32_e32 v214, v250, v213, vcc\n" \
"v_mfma_f32_16x16x32_bf16 a[24:27], v[52:55], v[164:167], a[24:27]\n" \
"v_add_u32_e32 v224, s96, v223\n" \
"v_sub_u32_e32 v231, v229, v225\n" \
"v_mfma_f32_16x16x32_bf16 a[56:59], v[60:63], v[164:167], a[56:59]\n" \
"v_lshrrev_b32_e32 v241, 16, v238\n" \
"v_add_u32_e32 v251, 0x1c0, v212\n" \
"v_mfma_f32_16x16x32_bf16 a[56:59], v[64:67], v[168:171], a[56:59]\n" \
"v_mul_u32_u24_e32 v247, 0x480, v246\n" \
"v_and_b32_e32 v222, 0xff, v224\n" \
"v_mfma_f32_16x16x32_bf16 a[88:91], v[72:75], v[168:171], a[88:91]\n" \
"v_cmp_gt_u32_e32 vcc, 16, v221\n" \
"v_lshlrev_b32_e32 v232, 4, v231\n" \
"v_mfma_f32_16x16x32_bf16 a[88:91], v[68:71], v[164:167], a[88:91]\n" \
"v_mul_u32_u24_e32 v236, 18, v241\n" \
"v_mul_u32_u24_e32 v249, 0xe39, v251\n" \
"v_mfma_f32_16x16x32_bf16 a[120:123], v[76:79], v[164:167], a[120:123]\n" \
"v_add_u32_e32 v2, 0xfffff800, v243\n" \
"v_add_u32_e32 v216, v247, v214\n" \
"v_mfma_f32_16x16x32_bf16 a[120:123], v[80:83], v[168:171], a[120:123]\n" \
"v_cndmask_b32_e32 v226, v223, v222, vcc\n" \
"v_add_u32_e32 v234, s96, v232\n" \
"v_mfma_f32_16x16x32_bf16 a[28:31], v[56:59], v[176:179], a[28:31]\n" \
"s_add_u32 s62, s22, 0x300\n" \
"s_and_b32 s62, s62, 0x3ff\n" \
"v_mfma_f32_16x16x32_bf16 a[28:31], v[52:55], v[172:175], a[28:31]\n" \
"s_lshl_b32 s63, s62, 12\n" \
"v_sub_u32_e32 v242, v240, v236\n" \
"v_mfma_f32_16x16x32_bf16 a[60:63], v[60:63], v[172:175], a[60:63]\n" \
"v_lshrrev_b32_e32 v213, 16, v249\n" \
"v_add_u32_e32 v224, 0x200, v212\n" \
"v_mfma_f32_16x16x32_bf16 a[60:63], v[64:67], v[176:179], a[60:63]\n" \
"v_mul_u32_u24_e32 v218, 0x480, v220\n" \
"v_and_b32_e32 v235, 0xff, v234\n" \
"v_mfma_f32_16x16x32_bf16 a[92:95], v[72:75], v[176:179], a[92:95]\n" \
"v_cmp_gt_u32_e32 vcc, 16, v231\n" \
"v_lshlrev_b32_e32 v243, 4, v242\n" \
"v_mfma_f32_16x16x32_bf16 a[92:95], v[68:71], v[172:175], a[92:95]\n" \
"v_mul_u32_u24_e32 v247, 18, v213\n" \
"v_mul_u32_u24_e32 v221, 0xe39, v224\n" \
"v_mfma_f32_16x16x32_bf16 a[124:127], v[76:79], v[172:175], a[124:127]\n" \
"v_add_u32_e32 v3, 0xfffff400, v216\n" \
"v_add_u32_e64 v4, v218, v226\n" \
"v_mfma_f32_16x16x32_bf16 a[124:127], v[80:83], v[176:179], a[124:127]\n" \
"v_cndmask_b32_e32 v233, v232, v235, vcc\n" \
"v_add_u32_e32 v245, s96, v243\n" \
"s_waitcnt vmcnt(4)\n" \
"s_barrier\n" \
"v_mfma_f32_16x16x32_bf16 a[144:147], v[88:91], v[152:155], a[144:147]\n" \
"s_add_u32 m0, %[mA], 0x0\n" \
"s_add_u32 s85, s62, 0x0\n" \
"v_mfma_f32_16x16x32_bf16 a[144:147], v[84:87], v[148:151], a[144:147]\n" \
"buffer_load_dwordx4 v22, s[64:67], s85 offen lds\n" \
"v_mfma_f32_16x16x32_bf16 a[176:179], v[92:95], v[148:151], a[176:179]\n" \
"ds_read_b128 v[56:59], v29 offset:49152\n" \
"ds_read_b128 v[52:55], v28 offset:49152\n" \
"v_mfma_f32_16x16x32_bf16 a[176:179], v[96:99], v[152:155], a[176:179]\n" \
"s_add_u32 m0, %[mA], 0x400\n" \
"s_add_u32 s84, s62, 0x2000\n" \
"v_mfma_f32_16x16x32_bf16 a[208:211], v[104:107], v[152:155], a[208:211]\n" \
"buffer_load_dwordx4 v23, s[64:67], s84 offen lds\n" \
"v_mfma_f32_16x16x32_bf16 a[208:211], v[100:103], v[148:151], a[208:211]\n" \
"ds_read_b128 v[60:63], v28 offset:51200\n" \
"ds_read_b128 v[64:67], v29 offset:51200\n" \
"v_mfma_f32_16x16x32_bf16 a[240:243], v[108:111], v[148:151], a[240:243]\n" \
"ds_read_b64_tr_b16 v[148:149], v32 offset:49152\n" \
"ds_read_b64_tr_b16 v[150:151], v32 offset:50176\n" \
"v_mfma_f32_16x16x32_bf16 a[240:243], v[112:115], v[152:155], a[240:243]\n" \
"s_add_u32 m0, %[mB], 0x0\n" \
"s_add_u32 s85, s63, 0x0\n" \
"v_mfma_f32_16x16x32_bf16 a[148:151], v[88:91], v[160:163], a[148:151]\n" \
"buffer_load_dwordx4 v26, s[68:71], s85 offen lds\n" \
"v_mfma_f32_16x16x32_bf16 a[148:151], v[84:87], v[156:159], a[148:151]\n" \
"ds_read_b64_tr_b16 v[152:153], v32 offset:57344\n" \
"ds_read_b64_tr_b16 v[154:155], v32 offset:58368\n" \
"v_mfma_f32_16x16x32_bf16 a[180:183], v[92:95], v[156:159], a[180:183]\n" \
"ds_read_b128 v[72:75], v29 offset:53248\n" \
"ds_read_b128 v[68:71], v28 offset:53248\n" \
"v_mfma_f32_16x16x32_bf16 a[180:183], v[96:99], v[160:163], a[180:183]\n" \
"s_add_u32 m0, %[mB], 0x4000\n" \
"s_add_u32 s84, s63, 0x0\n" \
"s_nop 0\n" \
"v_mfma_f32_16x16x32_bf16 a[212:215], v[104:107], v[160:163], a[212:215]\n" \
"buffer_load_dwordx4 v26, s[72:75], s84 offen lds\n" \
"v_mfma_f32_16x16x32_bf16 a[212:215], v[100:103], v[156:159], a[212:215]\n" \
"ds_read_b128 v[76:79], v28 offset:55296\n" \
"ds_read_b128 v[80:83], v29 offset:55296\n" \
"v_mfma_f32_16x16x32_bf16 a[244:247], v[108:111], v[156:159], a[244:247]\n" \
"ds_read_b64_tr_b16 v[156:157], v33 offset:49152\n" \
"ds_read_b64_tr_b16 v[158:159], v33 offset:50176\n" \
"v_mfma_f32_16x16x32_bf16 a[244:247], v[112:115], v[160:163], a[244:247]\n" \
"s_add_u32 m0, %[mA], 0x800\n" \
"s_add_u32 s85, s62, 0x4000\n" \
"v_mfma_f32_16x16x32_bf16 a[152:155], v[88:91], v[168:171], a[152:155]\n" \
"buffer_load_dwordx4 v22, s[64:67], s85 offen lds\n" \
"v_mfma_f32_16x16x32_bf16 a[152:155], v[84:87], v[164:167], a[152:155]\n" \
"ds_read_b64_tr_b16 v[160:161], v33 offset:57344\n" \
"ds_read_b64_tr_b16 v[162:163], v33 offset:58368\n" \
"v_mfma_f32_16x16x32_bf16 a[184:187], v[92:95], v[164:167], a[184:187]\n" \
"v_sub_u32_e32 v214, v251, v247\n" \
"v_lshrrev_b32_e32 v222, 16, v221\n" \
"v_mfma_f32_16x16x32_bf16 a[184:187], v[96:99], v[168:171], a[184:187]\n" \
"s_add_u32 m0, %[mA], 0xc00\n" \
"s_add_u32 s84, s62, 0x6000\n" \
"v_mfma_f32_16x16x32_bf16 a[216:219], v[104:107], v[168:171], a[216:219]\n" \
"buffer_load_dwordx4 v23, s[64:67], s84 offen lds\n" \
"v_mfma_f32_16x16x32_bf16 a[216:219], v[100:103], v[164:167], a[216:219]\n" \
"v_and_b32_e32 v246, 0xff, v245\n" \
"v_cmp_gt_u32_e32 vcc, 16, v242\n" \
"s_nop 0\n" \
"v_mfma_f32_16x16x32_bf16 a[248:251], v[108:111], v[164:167], a[248:251]\n" \
"ds_read_b64_tr_b16 v[164:165], v34 offset:49152\n" \
"ds_read_b64_tr_b16 v[166:167], v34 offset:50176\n" \
"v_mfma_f32_16x16x32_bf16 a[248:251], v[112:115], v[168:171], a[248:251]\n" \
"s_add_u32 m0, %[mB], 0x400\n" \
"s_add_u32 s85, s63, 0x8000\n" \
"v_mfma_f32_16x16x32_bf16 a[156:159], v[88:91], v[176:179], a[156:159]\n" \
"buffer_load_dwordx4 v26, s[68:71], s85 offen lds\n" \
"v_mfma_f32_16x16x32_bf16 a[156:159], v[84:87], v[172:175], a[156:159]\n" \
"ds_read_b64_tr_b16 v[168:169], v34 offset:57344\n" \
"ds_read_b64_tr_b16 v[170:171], v34 offset:58368\n" \
"v_mfma_f32_16x16x32_bf16 a[188:191], v[92:95], v[172:175], a[188:191]\n" \
"v_lshlrev_b32_e32 v216, 4, v214\n" \
"v_mul_u32_u24_e32 v218, 18, v222\n" \
"v_mfma_f32_16x16x32_bf16 a[188:191], v[96:99], v[176:179], a[188:191]\n" \
"s_add_u32 m0, %[mB], 0x4400\n" \
"s_add_u32 s84, s63, 0x8000\n" \
"v_mfma_f32_16x16x32_bf16 a[220:223], v[104:107], v[176:179], a[220:223]\n" \
"buffer_load_dwordx4 v26, s[72:75], s84 offen lds\n" \
"v_mfma_f32_16x16x32_bf16 a[220:223], v[100:103], v[172:175], a[220:223]\n" \
"v_cndmask_b32_e32 v244, v243, v246, vcc\n" \
"v_add_u32_e32 v217, s96, v216\n" \
"v_mfma_f32_16x16x32_bf16 a[252:255], v[108:111], v[172:175], a[252:255]\n" \
"ds_read_b64_tr_b16 v[172:173], v35 offset:49152\n" \
"ds_read_b64_tr_b16 v[174:175], v35 offset:50176\n" \
"v_mfma_f32_16x16x32_bf16 a[252:255], v[112:115], v[176:179], a[252:255]\n" \
"s_add_u32 m0, %[mA], 0x1000\n" \
"s_add_u32 s85, s62, 0x8000\n" \
"v_mfma_f32_16x16x32_bf16 a[128:131], v[120:123], v[88:91], a[128:131]\n" \
"buffer_load_dwordx4 v22, s[64:67], s85 offen lds\n" \
"v_mfma_f32_16x16x32_bf16 a[128:131], v[116:119], v[84:87], a[128:131]\n" \
"ds_read_b64_tr_b16 v[176:177], v35 offset:57344\n" \
"ds_read_b64_tr_b16 v[178:179], v35 offset:58368\n" \
"v_mfma_f32_16x16x32_bf16 a[160:163], v[116:119], v[92:95], a[160:163]\n" \
"v_sub_u32_e32 v226, v224, v218\n" \
"v_mul_u32_u24_e32 v228, 0x480, v230\n" \
"v_mfma_f32_16x16x32_bf16 a[160:163], v[120:123], v[96:99], a[160:163]\n" \
"s_add_u32 m0, %[mA], 0x1400\n" \
"s_add_u32 s84, s62, 0xa000\n" \
"v_mfma_f32_16x16x32_bf16 a[192:195], v[120:123], v[104:107], a[192:195]\n" \
"buffer_load_dwordx4 v23, s[64:67], s84 offen lds\n" \
"v_mfma_f32_16x16x32_bf16 a[192:195], v[116:119], v[100:103], a[192:195]\n" \
"v_and_b32_e32 v220, 0xff, v217\n" \
"v_cmp_gt_u32_e32 vcc, 16, v214\n" \
"v_mfma_f32_16x16x32_bf16 a[224:227], v[116:119], v[108:111], a[224:227]\n" \
"ds_read_b64_tr_b16 v[116:117], v32 offset:32768\n" \
"ds_read_b64_tr_b16 v[118:119], v32 offset:33792\n" \
"v_mfma_f32_16x16x32_bf16 a[224:227], v[120:123], v[112:115], a[224:227]\n" \
"s_add_u32 m0, %[mB], 0x800\n" \
"s_add_u32 s85, s63, 0x10000\n" \
"v_mfma_f32_16x16x32_bf16 a[132:135], v[128:131], v[88:91], a[132:135]\n" \
"buffer_load_dwordx4 v27, s[68:71], s85 offen lds\n" \
"v_mfma_f32_16x16x32_bf16 a[132:135], v[124:127], v[84:87], a[132:135]\n" \
"ds_read_b64_tr_b16 v[120:121], v32 offset:40960\n" \
"ds_read_b64_tr_b16 v[122:123], v32 offset:41984\n" \
"v_mfma_f32_16x16x32_bf16 a[164:167], v[124:127], v[92:95], a[164:167]\n" \
"v_lshlrev_b32_e32 v227, 4, v226\n" \
"v_cndmask_b32_e32 v219, v216, v220, vcc\n" \
"v_mfma_f32_16x16x32_bf16 a[164:167], v[128:131], v[96:99], a[164:167]\n" \
"s_add_u32 m0, %[mB], 0x4800\n" \
"s_add_u32 s84, s63, 0x10000\n" \
"v_mfma_f32_16x16x32_bf16 a[196:199], v[128:131], v[104:107], a[196:199]\n" \
"buffer_load_dwordx4 v27, s[72:75], s84 offen lds\n" \
"v_mfma_f32_16x16x32_bf16 a[196:199], v[124:127], v[100:103], a[196:199]\n" \
"v_add_u32_e64 v230, s96, v227\n" \
"v_and_b32_e32 v229, 0xff, v230\n" \
"v_mfma_f32_16x16x32_bf16 a[228:231], v[124:127], v[108:111], a[228:231]\n" \
"ds_read_b64_tr_b16 v[124:125], v33 offset:32768\n" \
"ds_read_b64_tr_b16 v[126:127], v33 offset:33792\n" \
"v_mfma_f32_16x16x32_bf16 a[228:231], v[128:131], v[112:115], a[228:231]\n" \
"ds_read_b64_tr_b16 v[128:129], v33 offset:40960\n" \
"ds_read_b64_tr_b16 v[130:131], v33 offset:41984\n" \
"v_mfma_f32_16x16x32_bf16 a[136:139], v[136:139], v[88:91], a[136:139]\n" \
"v_cmp_gt_u32_e32 vcc, 16, v226\n" \
"v_mul_u32_u24_e32 v250, 0x480, v213\n" \
"v_mfma_f32_16x16x32_bf16 a[136:139], v[132:135], v[84:87], a[136:139]\n" \
"v_cndmask_b32_e32 v225, v227, v229, vcc\n" \
"v_and_b32_e64 v249, 2, v212\n" \
"v_mfma_f32_16x16x32_bf16 a[168:171], v[132:135], v[92:95], a[168:171]\n" \
"v_mov_b32_e32 v213, 0x100\n" \
"v_mov_b32_e64 v251, s95\n" \
"v_mfma_f32_16x16x32_bf16 a[168:171], v[136:139], v[96:99], a[168:171]\n" \
"v_cmp_ne_u32_e32 vcc, 0, v249\n" \
"s_mov_b32 s76, s8\n" \
"v_mfma_f32_16x16x32_bf16 a[200:203], v[136:139], v[104:107], a[200:203]\n" \
"v_cndmask_b32_e32 v247, v251, v213, vcc\n" \
"s_and_b32 s77, s9, 0xffff\n" \
"v_mfma_f32_16x16x32_bf16 a[200:203], v[132:135], v[100:103], a[200:203]\n" \
"s_mov_b32 s78, 0x240000\n" \
"v_xor_b32_e32 v37, 0x20, v190\n" \
"v_mfma_f32_16x16x32_bf16 a[232:235], v[132:135], v[108:111], a[232:235]\n" \
"ds_read_b64_tr_b16 v[132:133], v34 offset:32768\n" \
"ds_read_b64_tr_b16 v[134:135], v34 offset:33792\n" \
"v_mfma_f32_16x16x32_bf16 a[232:235], v[136:139], v[112:115], a[232:235]\n" \
"ds_read_b64_tr_b16 v[136:137], v34 offset:40960\n" \
"ds_read_b64_tr_b16 v[138:139], v34 offset:41984\n" \
"v_mfma_f32_16x16x32_bf16 a[140:143], v[144:147], v[88:91], a[140:143]\n" \
"ds_read_b128 v[88:91], v29 offset:32768\n" \
"v_xor_b32_e64 v38, 0x40, v190\n" \
"v_mfma_f32_16x16x32_bf16 a[140:143], v[140:143], v[84:87], a[140:143]\n" \
"ds_read_b128 v[84:87], v28 offset:32768\n" \
"v_add_u32_e64 v48, s60, v28\n" \
"v_mfma_f32_16x16x32_bf16 a[172:175], v[140:143], v[92:95], a[172:175]\n" \
"ds_read_b128 v[92:95], v28 offset:34816\n" \
"v_add_u32_e64 v49, s60, v29\n" \
"v_mfma_f32_16x16x32_bf16 a[172:175], v[144:147], v[96:99], a[172:175]\n" \
"ds_read_b128 v[96:99], v29 offset:34816\n" \
"v_xor_b32_e64 v36, 0x0, v190\n" \
"v_mfma_f32_16x16x32_bf16 a[204:207], v[144:147], v[104:107], a[204:207]\n" \
"ds_read_b128 v[104:107], v29 offset:36864\n" \
"v_add_u32_e64 v237, v228, v233\n" \
"v_mfma_f32_16x16x32_bf16 a[204:207], v[140:143], v[100:103], a[204:207]\n" \
"ds_read_b128 v[100:103], v28 offset:36864\n" \
"s_lshl_b32 s97, %[w], 4\n" \
"v_mfma_f32_16x16x32_bf16 a[236:239], v[140:143], v[108:111], a[236:239]\n" \
"ds_read_b64_tr_b16 v[140:141], v35 offset:32768\n" \
"ds_read_b64_tr_b16 v[142:143], v35 offset:33792\n" \
"v_mfma_f32_16x16x32_bf16 a[236:239], v[144:147], v[112:115], a[236:239]\n" \
"s_add_u32 m0, %[mA], 0x1800\n" \
"s_waitcnt lgkmcnt(12)\n" \
"v_mfma_f32_16x16x32_bf16 a[16:19], v[56:59], v[152:155], a[16:19]\n" \
"s_add_u32 s85, s62, 0xc000\n" \
"ds_read_b64_tr_b16 v[144:145], v35 offset:40960\n" \
"v_mfma_f32_16x16x32_bf16 a[16:19], v[52:55], v[148:151], a[16:19]\n" \
"buffer_load_dwordx4 v22, s[64:67], s85 offen lds\n" \
"v_mfma_f32_16x16x32_bf16 a[48:51], v[60:63], v[148:151], a[48:51]\n" \
"s_add_u32 m0, %[mA], 0x1c00\n" \
"s_add_u32 s84, s62, 0xe000\n" \
"v_mfma_f32_16x16x32_bf16 a[48:51], v[64:67], v[152:155], a[48:51]\n" \
"ds_read_b64_tr_b16 v[146:147], v35 offset:41984\n" \
"ds_read_b128 v[108:111], v28 offset:38912\n" \
"v_mfma_f32_16x16x32_bf16 a[80:83], v[72:75], v[152:155], a[80:83]\n" \
"buffer_load_dwordx4 v23, s[64:67], s84 offen lds\n" \
"v_mfma_f32_16x16x32_bf16 a[80:83], v[68:71], v[148:151], a[80:83]\n" \
"s_add_u32 m0, %[mB], 0xc00\n" \
"s_add_u32 s85, s63, 0x18000\n" \
"v_mfma_f32_16x16x32_bf16 a[112:115], v[76:79], v[148:151], a[112:115]\n" \
"ds_read_b128 v[112:115], v29 offset:38912\n" \
"v_lshrrev_b32_e32 v234, 2, v212\n" \
"v_mfma_f32_16x16x32_bf16 a[112:115], v[80:83], v[152:155], a[112:115]\n" \
"buffer_load_dwordx4 v27, s[68:71], s85 offen lds\n" \
"v_mfma_f32_16x16x32_bf16 a[20:23], v[56:59], v[160:163], a[20:23]\n" \
"s_add_u32 m0, %[mB], 0x4c00\n" \
"s_add_u32 s84, s63, 0x18000\n" \
"v_mfma_f32_16x16x32_bf16 a[20:23], v[52:55], v[156:159], a[20:23]\n" \
"v_add_u32_e32 v5, 0xfffffc00, v237\n" \
"v_add_u32_e32 v231, s97, v234\n" \
"v_mfma_f32_16x16x32_bf16 a[52:55], v[60:63], v[156:159], a[52:55]\n" \
"buffer_load_dwordx4 v27, s[72:75], s84 offen lds\n" \
"v_mfma_f32_16x16x32_bf16 a[52:55], v[64:67], v[160:163], a[52:55]\n" \
"v_lshrrev_b32_e32 v235, 5, v231\n" \
"v_lshrrev_b32_e32 v237, 4, v231\n" \
"v_mfma_f32_16x16x32_bf16 a[84:87], v[72:75], v[160:163], a[84:87]\n" \
"v_mul_u32_u24_e32 v239, 0x480, v241\n" \
"v_lshlrev_b32_e32 v232, 4, v235\n" \
"v_mfma_f32_16x16x32_bf16 a[84:87], v[68:71], v[156:159], a[84:87]\n" \
"v_and_b32_e32 v228, 12, v231\n" \
"v_and_b32_e64 v238, 1, v237\n" \
"v_mfma_f32_16x16x32_bf16 a[116:119], v[76:79], v[156:159], a[116:119]\n" \
"v_add_u32_e32 v233, v232, v228\n" \
"v_lshlrev_b32_e32 v241, 1, v238\n" \
"v_mfma_f32_16x16x32_bf16 a[116:119], v[80:83], v[160:163], a[116:119]\n" \
"s_mov_b32 m0, %[mS]\n" \
"s_mov_b32 s86, s8\n" \
"v_mfma_f32_16x16x32_bf16 a[24:27], v[56:59], v[168:171], a[24:27]\n" \
"s_mov_b32 s87, s9\n" \
"v_lshrrev_b32_e32 v236, 1, v231\n" \
"v_mfma_f32_16x16x32_bf16 a[24:27], v[52:55], v[164:167], a[24:27]\n" \
"v_add_u32_e32 v240, v233, v241\n" \
"v_and_b32_e32 v245, 1, v236\n" \
"v_mfma_f32_16x16x32_bf16 a[56:59], v[60:63], v[164:167], a[56:59]\n" \
"v_add_u32_e32 v242, v240, v245\n" \
"v_add_u32_e32 v248, v239, v244\n" \
"v_mfma_f32_16x16x32_bf16 a[56:59], v[64:67], v[168:171], a[56:59]\n" \
"v_mul_u32_u24_e32 v246, 18, v242\n" \
"v_add_u32_e32 v243, 16, v246\n" \
"v_mfma_f32_16x16x32_bf16 a[88:91], v[72:75], v[168:171], a[88:91]\n" \
"v_and_b32_e64 v239, 1, v231\n" \
"v_add_u32_e32 v6, 0xfffff800, v248\n" \
"v_mfma_f32_16x16x32_bf16 a[88:91], v[68:71], v[164:167], a[88:91]\n" \
"v_add_u32_e64 v244, v243, v239\n" \
"v_mul_u32_u24_e32 v248, 0x480, v244\n" \
"s_mov_b32 exec_lo, 0x0\n" \
"s_mov_b32 exec_hi, 0x3ffff0\n" \
"global_load_lds_dword v188, s[86:87]\n" \
"s_mov_b64 exec, -1\n" \
"v_mfma_f32_16x16x32_bf16 a[120:123], v[76:79], v[164:167], a[120:123]\n" \
"v_add_u32_e32 v215, v250, v219\n" \
"v_add_u32_e32 v217, v248, v247\n" \
"v_mfma_f32_16x16x32_bf16 a[120:123], v[80:83], v[168:171], a[120:123]\n" \
"v_and_b32_e64 v214, 16, v20\n" \
"v_add_u32_e32 v7, 0xfffff400, v215\n" \
"v_mfma_f32_16x16x32_bf16 a[28:31], v[56:59], v[176:179], a[28:31]\n" \
"v_add_u32_e32 v9, v217, v214\n" \
"v_and_b32_e32 v216, 3, v212\n" \
"v_mfma_f32_16x16x32_bf16 a[28:31], v[52:55], v[172:175], a[28:31]\n" \
"v_lshlrev_b32_e32 v250, 3, v216\n" \
"v_mul_u32_u24_e32 v220, 0x120, v234\n" \
"v_mfma_f32_16x16x32_bf16 a[60:63], v[60:63], v[172:175], a[60:63]\n" \
"v_mul_u32_u24_e32 v223, 0x480, v222\n" \
"v_add_u32_e32 v10, v220, v250\n" \
"v_mfma_f32_16x16x32_bf16 a[60:63], v[64:67], v[176:179], a[60:63]\n" \
"v_add_u32_e32 v8, v223, v225\n" \
"s_lshl_b32 s99, %[w], 11\n" \
"v_mfma_f32_16x16x32_bf16 a[92:95], v[72:75], v[176:179], a[92:95]\n" \
"s_add_u32 s55, s98, s99\n" \
"s_nop 0\n" \
"v_mfma_f32_16x16x32_bf16 a[92:95], v[68:71], v[172:175], a[92:95]\n" \
"v_mfma_f32_16x16x32_bf16 a[124:127], v[76:79], v[172:175], a[124:127]\n" \
"v_mfma_f32_16x16x32_bf16 a[124:127], v[80:83], v[176:179], a[124:127]\n" \
"v_mfma_f32_16x16x32_bf16 a[0:3], v[120:123], v[56:59], a[0:3]\n" \
"v_mfma_f32_16x16x32_bf16 a[0:3], v[116:119], v[52:55], a[0:3]\n" \
"v_mfma_f32_16x16x32_bf16 a[32:35], v[116:119], v[60:63], a[32:35]\n" \
"v_mfma_f32_16x16x32_bf16 a[32:35], v[120:123], v[64:67], a[32:35]\n" \
"v_mfma_f32_16x16x32_bf16 a[64:67], v[120:123], v[72:75], a[64:67]\n" \
"v_mfma_f32_16x16x32_bf16 a[64:67], v[116:119], v[68:71], a[64:67]\n" \
"v_mfma_f32_16x16x32_bf16 a[96:99], v[116:119], v[76:79], a[96:99]\n" \
"v_mfma_f32_16x16x32_bf16 a[96:99], v[120:123], v[80:83], a[96:99]\n" \
"v_mfma_f32_16x16x32_bf16 a[4:7], v[128:131], v[56:59], a[4:7]\n" \
"v_mfma_f32_16x16x32_bf16 a[4:7], v[124:127], v[52:55], a[4:7]\n" \
"v_mfma_f32_16x16x32_bf16 a[36:39], v[124:127], v[60:63], a[36:39]\n" \
"v_mfma_f32_16x16x32_bf16 a[36:39], v[128:131], v[64:67], a[36:39]\n" \
"v_mfma_f32_16x16x32_bf16 a[68:71], v[128:131], v[72:75], a[68:71]\n" \
"v_mfma_f32_16x16x32_bf16 a[68:71], v[124:127], v[68:71], a[68:71]\n" \
"v_mfma_f32_16x16x32_bf16 a[100:103], v[124:127], v[76:79], a[100:103]\n" \
"v_mfma_f32_16x16x32_bf16 a[100:103], v[128:131], v[80:83], a[100:103]\n" \
"s_waitcnt lgkmcnt(0)\n" \
"s_nop 0\n" \
"v_mfma_f32_16x16x32_bf16 a[8:11], v[136:139], v[56:59], a[8:11]\n" \
"v_mfma_f32_16x16x32_bf16 a[8:11], v[132:135], v[52:55], a[8:11]\n" \
"v_mfma_f32_16x16x32_bf16 a[40:43], v[132:135], v[60:63], a[40:43]\n" \
"v_mfma_f32_16x16x32_bf16 a[40:43], v[136:139], v[64:67], a[40:43]\n" \
"v_mfma_f32_16x16x32_bf16 a[72:75], v[136:139], v[72:75], a[72:75]\n" \
"v_mfma_f32_16x16x32_bf16 a[72:75], v[132:135], v[68:71], a[72:75]\n" \
"v_mfma_f32_16x16x32_bf16 a[104:107], v[132:135], v[76:79], a[104:107]\n" \
"v_mfma_f32_16x16x32_bf16 a[104:107], v[136:139], v[80:83], a[104:107]\n" \
"v_mfma_f32_16x16x32_bf16 a[12:15], v[144:147], v[56:59], a[12:15]\n" \
"s_add_u32 s62, s22, 0x380\n" \
"s_and_b32 s62, s62, 0x3ff\n" \
"v_mfma_f32_16x16x32_bf16 a[12:15], v[140:143], v[52:55], a[12:15]\n" \
"s_lshl_b32 s63, s62, 12\n" \
"v_mfma_f32_16x16x32_bf16 a[44:47], v[140:143], v[60:63], a[44:47]\n" \
"v_mfma_f32_16x16x32_bf16 a[44:47], v[144:147], v[64:67], a[44:47]\n" \
"v_mfma_f32_16x16x32_bf16 a[76:79], v[144:147], v[72:75], a[76:79]\n" \
"v_mfma_f32_16x16x32_bf16 a[76:79], v[140:143], v[68:71], a[76:79]\n" \
"v_mfma_f32_16x16x32_bf16 a[108:111], v[140:143], v[76:79], a[108:111]\n" \
"v_mfma_f32_16x16x32_bf16 a[108:111], v[144:147], v[80:83], a[108:111]\n" \
"s_waitcnt vmcnt(1)\n" \
"s_barrier\n" \
"s_mov_b64 exec, 1\n" \
"buffer_store_dword v45, off, s[80:83], s38 offset:64 sc1\n" \
"s_mov_b64 exec, -1\n" \
"v_mfma_f32_16x16x32_bf16 a[128:131], v[120:123], v[88:91], a[128:131]\n" \
"s_add_u32 m0, %[mA], 0x8000\n" \
"s_add_u32 s85, s62, 0x0\n" \
"v_mfma_f32_16x16x32_bf16 a[128:131], v[116:119], v[84:87], a[128:131]\n" \
"ds_read_b128 v[56:59], v29 offset:16384\n" \
"ds_read_b128 v[52:55], v28 offset:16384\n" \
"v_mfma_f32_16x16x32_bf16 a[160:163], v[116:119], v[92:95], a[160:163]\n" \
"buffer_load_dwordx4 v22, s[64:67], s85 offen lds\n" \
"v_mfma_f32_16x16x32_bf16 a[160:163], v[120:123], v[96:99], a[160:163]\n" \
"s_add_u32 m0, %[mA], 0x8400\n" \
"s_add_u32 s84, s62, 0x2000\n" \
"v_mfma_f32_16x16x32_bf16 a[192:195], v[120:123], v[104:107], a[192:195]\n" \
"ds_read_b128 v[60:63], v28 offset:18432\n" \
"ds_read_b128 v[64:67], v29 offset:18432\n" \
"v_mfma_f32_16x16x32_bf16 a[192:195], v[116:119], v[100:103], a[192:195]\n" \
"buffer_load_dwordx4 v23, s[64:67], s84 offen lds\n" \
"v_mfma_f32_16x16x32_bf16 a[224:227], v[116:119], v[108:111], a[224:227]\n" \
"ds_read_b64_tr_b16 v[116:117], v32 offset:0\n" \
"ds_read_b64_tr_b16 v[118:119], v32 offset:1024\n" \
"v_mfma_f32_16x16x32_bf16 a[224:227], v[120:123], v[112:115], a[224:227]\n" \
"s_add_u32 m0, %[mB], 0x8000\n" \
"s_add_u32 s85, s63, 0x0\n" \
"v_mfma_f32_16x16x32_bf16 a[132:135], v[128:131], v[88:91], a[132:135]\n" \
"buffer_load_dwordx4 v26, s[68:71], s85 offen lds\n" \
"v_mfma_f32_16x16x32_bf16 a[132:135], v[124:127], v[84:87], a[132:135]\n" \
"ds_read_b64_tr_b16 v[120:121], v32 offset:8192\n" \
"ds_read_b64_tr_b16 v[122:123], v32 offset:9216\n" \
"v_mfma_f32_16x16x32_bf16 a[164:167], v[124:127], v[92:95], a[164:167]\n" \
"ds_read_b128 v[72:75], v29 offset:20480\n" \
"ds_read_b128 v[68:71], v28 offset:20480\n" \
"v_mfma_f32_16x16x32_bf16 a[164:167], v[128:131], v[96:99], a[164:167]\n" \
"s_add_u32 m0, %[mB], 0xc000\n" \
"s_add_u32 s84, s63, 0x0\n" \
"v_mfma_f32_16x16x32_bf16 a[196:199], v[128:131], v[104:107], a[196:199]\n" \
"buffer_load_dwordx4 v26, s[72:75], s84 offen lds\n" \
"v_mfma_f32_16x16x32_bf16 a[196:199], v[124:127], v[100:103], a[196:199]\n" \
"ds_read_b128 v[76:79], v28 offset:22528\n" \
"ds_read_b128 v[80:83], v29 offset:22528\n" \
"v_mfma_f32_16x16x32_bf16 a[228:231], v[124:127], v[108:111], a[228:231]\n" \
"ds_read_b64_tr_b16 v[124:125], v33 offset:0\n" \
"ds_read_b64_tr_b16 v[126:127], v33 offset:1024\n" \
"v_mfma_f32_16x16x32_bf16 a[228:231], v[128:131], v[112:115], a[228:231]\n" \
"s_add_u32 m0, %[mA], 0x8800\n" \
"s_add_u32 s85, s62, 0x4000\n" \
"v_mfma_f32_16x16x32_bf16 a[136:139], v[136:139], v[88:91], a[136:139]\n" \
"buffer_load_dwordx4 v22, s[64:67], s85 offen lds\n" \
"v_mfma_f32_16x16x32_bf16 a[136:139], v[132:135], v[84:87], a[136:139]\n" \
"ds_read_b64_tr_b16 v[128:129], v33 offset:8192\n" \
"ds_read_b64_tr_b16 v[130:131], v33 offset:9216\n" \
"v_mfma_f32_16x16x32_bf16 a[168:171], v[132:135], v[92:95], a[168:171]\n" \
"v_mov_b32_e64 v45, %[tag]\n" \
"v_mfma_f32_16x16x32_bf16 a[168:171], v[136:139], v[96:99], a[168:171]\n" \
"s_add_u32 m0, %[mA], 0x8c00\n" \
"s_add_u32 s84, s62, 0x6000\n" \
"v_mfma_f32_16x16x32_bf16 a[200:203], v[136:139], v[104:107], a[200:203]\n" \
"buffer_load_dwordx4 v23, s[64:67], s84 offen lds\n" \
"v_mfma_f32_16x16x32_bf16 a[200:203], v[132:135], v[100:103], a[200:203]\n" \
"v_mfma_f32_16x16x32_bf16 a[232:235], v[132:135], v[108:111], a[232:235]\n" \
"ds_read_b64_tr_b16 v[132:133], v34 offset:0\n" \
"ds_read_b64_tr_b16 v[134:135], v34 offset:1024\n" \
"v_mfma_f32_16x16x32_bf16 a[232:235], v[136:139], v[112:115], a[232:235]\n" \
"s_add_u32 m0, %[mB], 0x8400\n" \
"s_add_u32 s85, s63, 0x8000\n" \
"v_mfma_f32_16x16x32_bf16 a[140:143], v[144:147], v[88:91], a[140:143]\n" \
"buffer_load_dwordx4 v26, s[68:71], s85 offen lds\n" \
"v_mfma_f32_16x16x32_bf16 a[140:143], v[140:143], v[84:87], a[140:143]\n" \
"ds_read_b64_tr_b16 v[136:137], v34 offset:8192\n" \
"ds_read_b64_tr_b16 v[138:139], v34 offset:9216\n" \
"v_mfma_f32_16x16x32_bf16 a[172:175], v[140:143], v[92:95], a[172:175]\n" \
"v_mfma_f32_16x16x32_bf16 a[172:175], v[144:147], v[96:99], a[172:175]\n" \
"s_add_u32 m0, %[mB], 0xc400\n" \
"s_add_u32 s84, s63, 0x8000\n" \
"v_mfma_f32_16x16x32_bf16 a[204:207], v[144:147], v[104:107], a[204:207]\n" \
"buffer_load_dwordx4 v26, s[72:75], s84 offen lds\n" \
"v_mfma_f32_16x16x32_bf16 a[204:207], v[140:143], v[100:103], a[204:207]\n" \
"v_mfma_f32_16x16x32_bf16 a[236:239], v[140:143], v[108:111], a[236:239]\n" \
"ds_read_b64_tr_b16 v[140:141], v35 offset:0\n" \
"ds_read_b64_tr_b16 v[142:143], v35 offset:1024\n" \
"v_mfma_f32_16x16x32_bf16 a[236:239], v[144:147], v[112:115], a[236:239]\n" \
"s_add_u32 m0, %[mA], 0x9000\n" \
"L_pf3:\n" \
"s_cbranch_execz L_pf7\n" \
"s_nop 0\n" \
"s_add_u32 s85, s62, 0x8000\n" \
"v_mfma_f32_16x16x32_bf16 a[144:147], v[88:91], v[152:155], a[144:147]\n" \
"buffer_load_dwordx4 v22, s[64:67], s85 offen lds\n" \
"v_mfma_f32_16x16x32_bf16 a[144:147], v[84:87], v[148:151], a[144:147]\n" \
"ds_read_b64_tr_b16 v[144:145], v35 offset:8192\n" \
"ds_read_b64_tr_b16 v[146:147], v35 offset:9216\n" \
"v_mfma_f32_16x16x32_bf16 a[176:179], v[92:95], v[148:151], a[176:179]\n" \
"v_mfma_f32_16x16x32_bf16 a[176:179], v[96:99], v[152:155], a[176:179]\n" \
"s_add_u32 m0, %[mA], 0x9400\n" \
"s_add_u32 s84, s62, 0xa000\n" \
"v_mfma_f32_16x16x32_bf16 a[208:211], v[104:107], v[152:155], a[208:211]\n" \
"buffer_load_dwordx4 v23, s[64:67], s84 offen lds\n" \
"v_mfma_f32_16x16x32_bf16 a[208:211], v[100:103], v[148:151], a[208:211]\n" \
"v_mfma_f32_16x16x32_bf16 a[240:243], v[108:111], v[148:151], a[240:243]\n" \
"ds_read_b64_tr_b16 v[148:149], v32 offset:16384\n" \
"ds_read_b64_tr_b16 v[150:151], v32 offset:17408\n" \
"v_mfma_f32_16x16x32_bf16 a[240:243], v[112:115], v[152:155], a[240:243]\n" \
"s_add_u32 m0, %[mB], 0x8800\n" \
"s_add_u32 s85, s63, 0x10000\n" \
"v_mfma_f32_16x16x32_bf16 a[148:151], v[88:91], v[160:163], a[148:151]\n" \
"buffer_load_dwordx4 v27, s[68:71], s85 offen lds\n" \
"v_mfma_f32_16x16x32_bf16 a[148:151], v[84:87], v[156:159], a[148:151]\n" \
"ds_read_b64_tr_b16 v[152:153], v32 offset:24576\n" \
"ds_read_b64_tr_b16 v[154:155], v32 offset:25600\n" \
"v_mfma_f32_16x16x32_bf16 a[180:183], v[92:95], v[156:159], a[180:183]\n" \
"v_mfma_f32_16x16x32_bf16 a[180:183], v[96:99], v[160:163], a[180:183]\n" \
"s_add_u32 m0, %[mB], 0xc800\n" \
"s_add_u32 s84, s63, 0x10000\n" \
"v_mfma_f32_16x16x32_bf16 a[212:215], v[104:107], v[160:163], a[212:215]\n" \
"buffer_load_dwordx4 v27, s[72:75], s84 offen lds\n" \
"v_mfma_f32_16x16x32_bf16 a[212:215], v[100:103], v[156:159], a[212:215]\n" \
"v_mfma_f32_16x16x32_bf16 a[244:247], v[108:111], v[156:159], a[244:247]\n" \
"ds_read_b64_tr_b16 v[156:157], v33 offset:16384\n" \
"ds_read_b64_tr_b16 v[158:159], v33 offset:17408\n" \
"v_mfma_f32_16x16x32_bf16 a[244:247], v[112:115], v[160:163], a[244:247]\n" \
"ds_read_b64_tr_b16 v[160:161], v33 offset:24576\n" \
"ds_read_b64_tr_b16 v[162:163], v33 offset:25600\n" \
"v_mfma_f32_16x16x32_bf16 a[152:155], v[88:91], v[168:171], a[152:155]\n" \
"v_mfma_f32_16x16x32_bf16 a[152:155], v[84:87], v[164:167], a[152:155]\n" \
"v_mfma_f32_16x16x32_bf16 a[184:187], v[92:95], v[164:167], a[184:187]\n" \
"v_mfma_f32_16x16x32_bf16 a[184:187], v[96:99], v[168:171], a[184:187]\n" \
"v_mfma_f32_16x16x32_bf16 a[216:219], v[104:107], v[168:171], a[216:219]\n" \
"v_mfma_f32_16x16x32_bf16 a[216:219], v[100:103], v[164:167], a[216:219]\n" \
"v_mfma_f32_16x16x32_bf16 a[248:251], v[108:111], v[164:167], a[248:251]\n" \
"ds_read_b64_tr_b16 v[164:165], v34 offset:16384\n" \
"ds_read_b64_tr_b16 v[166:167], v34 offset:17408\n" \
"v_mfma_f32_16x16x32_bf16 a[248:251], v[112:115], v[168:171], a[248:251]\n" \
"ds_read_b64_tr_b16 v[168:169], v34 offset:24576\n" \
"ds_read_b64_tr_b16 v[170:171], v34 offset:25600\n" \
"v_mfma_f32_16x16x32_bf16 a[156:159], v[88:91], v[176:179], a[156:159]\n" \
"ds_read_b128 v[88:91], v29 offset:0\n" \
"v_mfma_f32_16x16x32_bf16 a[156:159], v[84:87], v[172:175], a[156:159]\n" \
"ds_read_b128 v[84:87], v28 offset:0\n" \
"v_mfma_f32_16x16x32_bf16 a[188:191], v[92:95], v[172:175], a[188:191]\n" \
"ds_read_b128 v[92:95], v28 offset:2048\n" \
"v_mfma_f32_16x16x32_bf16 a[188:191], v[96:99], v[176:179], a[188:191]\n" \
"ds_read_b128 v[96:99], v29 offset:2048\n" \
"v_mfma_f32_16x16x32_bf16 a[220:223], v[104:107], v[176:179], a[220:223]\n" \
"ds_read_b128 v[104:107], v29 offset:4096\n" \
"v_mfma_f32_16x16x32_bf16 a[220:223], v[100:103], v[172:175], a[220:223]\n" \
"ds_read_b128 v[100:103], v28 offset:4096\n" \
"v_mfma_f32_16x16x32_bf16 a[252:255], v[108:111], v[172:175], a[252:255]\n" \
"ds_read_b64_tr_b16 v[172:173], v35 offset:16384\n" \
"ds_read_b64_tr_b16 v[174:175], v35 offset:17408\n" \
"v_mfma_f32_16x16x32_bf16 a[252:255], v[112:115], v[176:179], a[252:255]\n" \
"s_add_u32 m0, %[mA], 0x9800\n" \
"s_waitcnt lgkmcnt(12)\n" \
"s_nop 0\n" \
"v_mfma_f32_16x16x32_bf16 a[0:3], v[120:123], v[56:59], a[0:3]\n" \
"s_add_u32 s85, s62, 0xc000\n" \
"ds_read_b64_tr_b16 v[176:177], v35 offset:24576\n" \
"v_mfma_f32_16x16x32_bf16 a[0:3], v[116:119], v[52:55], a[0:3]\n" \
"buffer_load_dwordx4 v22, s[64:67], s85 offen lds\n" \
"v_mfma_f32_16x16x32_bf16 a[32:35], v[116:119], v[60:63], a[32:35]\n" \
"s_add_u32 m0, %[mA], 0x9c00\n" \
"s_add_u32 s84, s62, 0xe000\n" \
"v_mfma_f32_16x16x32_bf16 a[32:35], v[120:123], v[64:67], a[32:35]\n" \
"ds_read_b64_tr_b16 v[178:179], v35 offset:25600\n" \
"ds_read_b128 v[108:111], v28 offset:6144\n" \
"v_mfma_f32_16x16x32_bf16 a[64:67], v[120:123], v[72:75], a[64:67]\n" \
"buffer_load_dwordx4 v23, s[64:67], s84 offen lds\n" \
"v_mfma_f32_16x16x32_bf16 a[64:67], v[116:119], v[68:71], a[64:67]\n" \
"s_add_u32 m0, %[mB], 0x8c00\n" \
"s_add_u32 s85, s63, 0x18000\n" \
"v_mfma_f32_16x16x32_bf16 a[96:99], v[116:119], v[76:79], a[96:99]\n" \
"ds_read_b128 v[112:115], v29 offset:6144\n" \
"s_mov_b32 s62, s23\n" \
"v_mfma_f32_16x16x32_bf16 a[96:99], v[120:123], v[80:83], a[96:99]\n" \
"buffer_load_dwordx4 v27, s[68:71], s85 offen lds\n" \
"v_mfma_f32_16x16x32_bf16 a[4:7], v[128:131], v[56:59], a[4:7]\n" \
"s_add_u32 m0, %[mB], 0xcc00\n" \
"s_add_u32 s84, s63, 0x18000\n" \
"v_mfma_f32_16x16x32_bf16 a[4:7], v[124:127], v[52:55], a[4:7]\n" \
"s_mov_b32 s68, s10\n" \
"s_and_b32 s69, s11, 0xffff\n" \
"v_mfma_f32_16x16x32_bf16 a[36:39], v[124:127], v[60:63], a[36:39]\n" \
"buffer_load_dwordx4 v27, s[72:75], s84 offen lds\n" \
"s_mov_b32 s73, s52\n" \
"s_add_u32 s74, s52, 0x4000\n" \
"s_add_u32 s75, s52, 0x8000\n" \
"v_mfma_f32_16x16x32_bf16 a[36:39], v[128:131], v[64:67], a[36:39]\n" \
"s_lshl_b32 s72, s62, 6\n" \
"s_add_u32 s72, s72, s37\n" \
"v_mfma_f32_16x16x32_bf16 a[68:71], v[128:131], v[72:75], a[68:71]\n" \
"v_bfe_u32 v44, v20, 2, 5\n" \
"s_mov_b32 s70, 0x480000\n" \
"v_mfma_f32_16x16x32_bf16 a[68:71], v[124:127], v[68:71], a[68:71]\n" \
"buffer_load_dword v44, v44, s[80:83], s39 offen offset:64 sc1\n" \
"v_mfma_f32_16x16x32_bf16 a[100:103], v[124:127], v[76:79], a[100:103]\n" \
"v_add_u32_e32 v22, s61, v28\n" \
"v_add_u32_e64 v23, s61, v29\n" \
"v_mfma_f32_16x16x32_bf16 a[100:103], v[128:131], v[80:83], a[100:103]\n" \
"s_mov_b32 m0, %[mS]\n" \
"s_mov_b32 s86, s18\n" \
"v_mfma_f32_16x16x32_bf16 a[8:11], v[136:139], v[56:59], a[8:11]\n" \
"s_mov_b32 s87, s19\n" \
"v_mfma_f32_16x16x32_bf16 a[8:11], v[132:135], v[52:55], a[8:11]\n" \
"v_mfma_f32_16x16x32_bf16 a[40:43], v[132:135], v[60:63], a[40:43]\n" \
"v_mfma_f32_16x16x32_bf16 a[40:43], v[136:139], v[64:67], a[40:43]\n" \
"v_mfma_f32_16x16x32_bf16 a[72:75], v[136:139], v[72:75], a[72:75]\n" \
"v_mfma_f32_16x16x32_bf16 a[72:75], v[132:135], v[68:71], a[72:75]\n" \
"s_mov_b32 exec_lo, 0xfffc0000\n" \
"s_mov_b32 exec_hi, 0xf\n" \
"global_load_lds_dword v16, s[86:87]\n" \
"s_mov_b64 exec, -1\n" \
"v_mfma_f32_16x16x32_bf16 a[104:107], v[132:135], v[76:79], a[104:107]\n" \
"s_mov_b32 m0, %[mS]\n" \
"s_mov_b32 s86, s16\n" \
"v_mfma_f32_16x16x32_bf16 a[104:107], v[136:139], v[80:83], a[104:107]\n" \
"s_mov_b32 s87, s17\n" \
"v_mfma_f32_16x16x32_bf16 a[12:15], v[144:147], v[56:59], a[12:15]\n" \
"v_mfma_f32_16x16x32_bf16 a[12:15], v[140:143], v[52:55], a[12:15]\n" \
"v_mfma_f32_16x16x32_bf16 a[44:47], v[140:143], v[60:63], a[44:47]\n" \
"s_mov_b32 exec_lo, 0x0\n" \
"s_mov_b32 exec_hi, 0x3f0\n" \
"global_load_lds_dword v16, s[86:87]\n" \
"s_mov_b64 exec, -1\n" \
"v_mfma_f32_16x16x32_bf16 a[44:47], v[144:147], v[64:67], a[44:47]\n" \
"v_mfma_f32_16x16x32_bf16 a[76:79], v[144:147], v[72:75], a[76:79]\n" \
"v_mfma_f32_16x16x32_bf16 a[76:79], v[140:143], v[68:71], a[76:79]\n" \
"v_mfma_f32_16x16x32_bf16 a[108:111], v[140:143], v[76:79], a[108:111]\n" \
"v_mfma_f32_16x16x32_bf16 a[108:111], v[144:147], v[80:83], a[108:111]\n" \
"v_mfma_f32_16x16x32_bf16 a[16:19], v[56:59], v[152:155], a[16:19]\n" \
"v_mfma_f32_16x16x32_bf16 a[16:19], v[52:55], v[148:151], a[16:19]\n" \
"v_mfma_f32_16x16x32_bf16 a[48:51], v[60:63], v[148:151], a[48:51]\n" \
"v_mfma_f32_16x16x32_bf16 a[48:51], v[64:67], v[152:155], a[48:51]\n" \
"v_mfma_f32_16x16x32_bf16 a[80:83], v[72:75], v[152:155], a[80:83]\n" \
"v_mfma_f32_16x16x32_bf16 a[80:83], v[68:71], v[148:151], a[80:83]\n" \
"v_mfma_f32_16x16x32_bf16 a[112:115], v[76:79], v[148:151], a[112:115]\n" \
"v_mfma_f32_16x16x32_bf16 a[112:115], v[80:83], v[152:155], a[112:115]\n" \
"v_mfma_f32_16x16x32_bf16 a[20:23], v[56:59], v[160:163], a[20:23]\n" \
"v_mfma_f32_16x16x32_bf16 a[20:23], v[52:55], v[156:159], a[20:23]\n" \
"v_mfma_f32_16x16x32_bf16 a[52:55], v[60:63], v[156:159], a[52:55]\n" \
"v_mfma_f32_16x16x32_bf16 a[52:55], v[64:67], v[160:163], a[52:55]\n" \
"v_mfma_f32_16x16x32_bf16 a[84:87], v[72:75], v[160:163], a[84:87]\n" \
"v_mfma_f32_16x16x32_bf16 a[84:87], v[68:71], v[156:159], a[84:87]\n" \
"v_mfma_f32_16x16x32_bf16 a[116:119], v[76:79], v[156:159], a[116:119]\n" \
"v_mfma_f32_16x16x32_bf16 a[116:119], v[80:83], v[160:163], a[116:119]\n" \
"s_waitcnt lgkmcnt(0)\n" \
"v_mfma_f32_16x16x32_bf16 a[24:27], v[56:59], v[168:171], a[24:27]\n" \
"v_mfma_f32_16x16x32_bf16 a[24:27], v[52:55], v[164:167], a[24:27]\n" \
"v_mfma_f32_16x16x32_bf16 a[56:59], v[60:63], v[164:167], a[56:59]\n" \
"v_mfma_f32_16x16x32_bf16 a[56:59], v[64:67], v[168:171], a[56:59]\n" \
"v_mfma_f32_16x16x32_bf16 a[88:91], v[72:75], v[168:171], a[88:91]\n" \
"v_mfma_f32_16x16x32_bf16 a[88:91], v[68:71], v[164:167], a[88:91]\n" \
"v_mfma_f32_16x16x32_bf16 a[120:123], v[76:79], v[164:167], a[120:123]\n" \
"v_mfma_f32_16x16x32_bf16 a[120:123], v[80:83], v[168:171], a[120:123]\n" \
"v_mfma_f32_16x16x32_bf16 a[28:31], v[56:59], v[176:179], a[28:31]\n" \
"s_waitcnt vmcnt(2)\n" \
"v_cmp_ne_u32_e32 vcc, %[tag], v44\n" \
"v_mfma_f32_16x16x32_bf16 a[28:31], v[52:55], v[172:175], a[28:31]\n" \
"v_mfma_f32_16x16x32_bf16 a[60:63], v[60:63], v[172:175], a[60:63]\n" \
"v_mfma_f32_16x16x32_bf16 a[60:63], v[64:67], v[176:179], a[60:63]\n" \
"v_mfma_f32_16x16x32_bf16 a[92:95], v[72:75], v[176:179], a[92:95]\n" \
"v_mfma_f32_16x16x32_bf16 a[92:95], v[68:71], v[172:175], a[92:95]\n" \
"s_cbranch_vccnz L_pl_%=\n" \
"L_rl_%=:\n" \
"s_lshl_b32 s63, s62, 12\n" \
"v_mfma_f32_16x16x32_bf16 a[124:127], v[76:79], v[172:175], a[124:127]\n" \
"v_mfma_f32_16x16x32_bf16 a[124:127], v[80:83], v[176:179], a[124:127]\n" \
"s_barrier\n" \
"v_mfma_f32_16x16x32_bf16 a[144:147], v[88:91], v[152:155], a[144:147]\n" \
"s_add_u32 m0, s40, 0x0\n" \
"s_add_u32 s85, s72, 0x0\n" \
"v_mfma_f32_16x16x32_bf16 a[144:147], v[84:87], v[148:151], a[144:147]\n" \
"buffer_load_dwordx4 v20, s[80:83], s85 offen sc1 lds\n" \
"v_mfma_f32_16x16x32_bf16 a[176:179], v[92:95], v[148:151], a[176:179]\n" \
"s_add_u32 m0, s40, 0x1000\n" \
"s_add_u32 s84, s72, 0x1000\n" \
"v_mfma_f32_16x16x32_bf16 a[176:179], v[96:99], v[152:155], a[176:179]\n" \
"ds_read_b128 v[56:59], v29 offset:49152\n" \
"ds_read_b128 v[52:55], v28 offset:49152\n" \
"v_mfma_f32_16x16x32_bf16 a[208:211], v[104:107], v[152:155], a[208:211]\n" \
"buffer_load_dwordx4 v20, s[80:83], s84 offen sc1 lds\n" \
"v_mfma_f32_16x16x32_bf16 a[208:211], v[100:103], v[148:151], a[208:211]\n" \
"ds_read_b128 v[60:63], v28 offset:51200\n" \
"ds_read_b128 v[64:67], v29 offset:51200\n" \
"v_mfma_f32_16x16x32_bf16 a[240:243], v[108:111], v[148:151], a[240:243]\n" \
"s_add_u32 m0, s40, 0x3000\n" \
"s_add_u32 s85, s62, 0x0\n" \
"v_mfma_f32_16x16x32_bf16 a[240:243], v[112:115], v[152:155], a[240:243]\n" \
"buffer_load_dwordx4 v21, s[76:79], s85 offen lds\n" \
"v_mfma_f32_16x16x32_bf16 a[148:151], v[88:91], v[160:163], a[148:151]\n" \
"ds_read_b64_tr_b16 v[152:153], v32 offset:57344\n" \
"ds_read_b64_tr_b16 v[154:155], v32 offset:58368\n" \
"v_mfma_f32_16x16x32_bf16 a[148:151], v[84:87], v[156:159], a[148:151]\n" \
"ds_read_b64_tr_b16 v[148:149], v32 offset:49152\n" \
"ds_read_b64_tr_b16 v[150:151], v32 offset:50176\n" \
"v_mfma_f32_16x16x32_bf16 a[180:183], v[92:95], v[156:159], a[180:183]\n" \
"s_add_u32 m0, %[mA], 0x0\n" \
"s_add_u32 s84, s62, 0x0\n" \
"s_nop 0\n" \
"v_mfma_f32_16x16x32_bf16 a[180:183], v[96:99], v[160:163], a[180:183]\n" \
"buffer_load_dwordx4 v24, s[76:79], s84 offen lds\n" \
"v_mfma_f32_16x16x32_bf16 a[212:215], v[104:107], v[160:163], a[212:215]\n" \
"ds_read_b128 v[72:75], v29 offset:53248\n" \
"ds_read_b128 v[68:71], v28 offset:53248\n" \
"v_mfma_f32_16x16x32_bf16 a[212:215], v[100:103], v[156:159], a[212:215]\n" \
"s_add_u32 m0, %[mA], 0x400\n" \
"s_add_u32 s85, s62, 0x2400\n" \
"v_mfma_f32_16x16x32_bf16 a[244:247], v[108:111], v[156:159], a[244:247]\n" \
"buffer_load_dwordx4 v25, s[76:79], s85 offen lds\n" \
"v_mfma_f32_16x16x32_bf16 a[244:247], v[112:115], v[160:163], a[244:247]\n" \
"ds_read_b128 v[76:79], v28 offset:55296\n" \
"ds_read_b128 v[80:83], v29 offset:55296\n" \
"v_mfma_f32_16x16x32_bf16 a[152:155], v[88:91], v[168:171], a[152:155]\n" \
"ds_read_b64_tr_b16 v[160:161], v33 offset:57344\n" \
"ds_read_b64_tr_b16 v[162:163], v33 offset:58368\n" \
"v_mfma_f32_16x16x32_bf16 a[152:155], v[84:87], v[164:167], a[152:155]\n" \
"s_add_u32 m0, %[mB], 0x0\n" \
"s_add_u32 s84, s63, 0x0\n" \
"v_mfma_f32_16x16x32_bf16 a[184:187], v[92:95], v[164:167], a[184:187]\n" \
"buffer_load_dwordx4 v26, s[68:71], s84 offen lds\n" \
"v_mfma_f32_16x16x32_bf16 a[184:187], v[96:99], v[168:171], a[184:187]\n" \
"ds_read_b64_tr_b16 v[156:157], v33 offset:49152\n" \
"ds_read_b64_tr_b16 v[158:159], v33 offset:50176\n" \
"v_mfma_f32_16x16x32_bf16 a[216:219], v[104:107], v[168:171], a[216:219]\n" \
"v_mfma_f32_16x16x32_bf16 a[216:219], v[100:103], v[164:167], a[216:219]\n" \
"s_add_u32 m0, %[mA], 0x800\n" \
"s_add_u32 s85, s62, 0x4800\n" \
"v_mfma_f32_16x16x32_bf16 a[248:251], v[108:111], v[164:167], a[248:251]\n" \
"buffer_load_dwordx4 v24, s[76:79], s85 offen lds\n" \
"v_mfma_f32_16x16x32_bf16 a[248:251], v[112:115], v[168:171], a[248:251]\n" \
"ds_read_b64_tr_b16 v[168:169], v34 offset:57344\n" \
"ds_read_b64_tr_b16 v[170:171], v34 offset:58368\n" \
"v_mfma_f32_16x16x32_bf16 a[156:159], v[88:91], v[176:179], a[156:159]\n" \
"s_add_u32 m0, %[mA], 0xc00\n" \
"s_add_u32 s84, s62, 0x6c00\n" \
"v_mfma_f32_16x16x32_bf16 a[156:159], v[84:87], v[172:175], a[156:159]\n" \
"buffer_load_dwordx4 v25, s[76:79], s84 offen lds\n" \
"v_mfma_f32_16x16x32_bf16 a[188:191], v[92:95], v[172:175], a[188:191]\n" \
"ds_read_b64_tr_b16 v[164:165], v34 offset:49152\n" \
"ds_read_b64_tr_b16 v[166:167], v34 offset:50176\n" \
"v_mfma_f32_16x16x32_bf16 a[188:191], v[96:99], v[176:179], a[188:191]\n" \
"v_mfma_f32_16x16x32_bf16 a[220:223], v[104:107], v[176:179], a[220:223]\n" \
"s_add_u32 m0, %[mB], 0x400\n" \
"s_add_u32 s85, s63, 0x8000\n" \
"v_mfma_f32_16x16x32_bf16 a[220:223], v[100:103], v[172:175], a[220:223]\n" \
"buffer_load_dwordx4 v26, s[68:71], s85 offen lds\n" \
"v_mfma_f32_16x16x32_bf16 a[252:255], v[108:111], v[172:175], a[252:255]\n" \
"ds_read_b64_tr_b16 v[172:173], v35 offset:49152\n" \
"ds_read_b64_tr_b16 v[174:175], v35 offset:50176\n" \
"v_mfma_f32_16x16x32_bf16 a[252:255], v[112:115], v[176:179], a[252:255]\n" \
"ds_read_b64_tr_b16 v[176:177], v35 offset:57344\n" \
"ds_read_b64_tr_b16 v[178:179], v35 offset:58368\n" \
"v_mfma_f32_16x16x32_bf16 a[128:131], v[120:123], v[88:91], a[128:131]\n" \
"s_add_u32 m0, %[mA], 0x1000\n" \
"s_add_u32 s84, s62, 0x9000\n" \
"v_mfma_f32_16x16x32_bf16 a[128:131], v[116:119], v[84:87], a[128:131]\n" \
"buffer_load_dwordx4 v24, s[76:79], s84 offen lds\n" \
"v_mfma_f32_16x16x32_bf16 a[160:163], v[116:119], v[92:95], a[160:163]\n" \
"v_mfma_f32_16x16x32_bf16 a[160:163], v[120:123], v[96:99], a[160:163]\n" \
"s_add_u32 m0, %[mA], 0x1400\n" \
"s_add_u32 s85, s62, 0xb400\n" \
"v_mfma_f32_16x16x32_bf16 a[192:195], v[120:123], v[104:107], a[192:195]\n" \
"buffer_load_dwordx4 v25, s[76:79], s85 offen lds\n" \
"v_mfma_f32_16x16x32_bf16 a[192:195], v[116:119], v[100:103], a[192:195]\n" \
"v_mfma_f32_16x16x32_bf16 a[224:227], v[116:119], v[108:111], a[224:227]\n" \
"ds_read_b64_tr_b16 v[116:117], v32 offset:32768\n" \
"ds_read_b64_tr_b16 v[118:119], v32 offset:33792\n" \
"v_mfma_f32_16x16x32_bf16 a[224:227], v[120:123], v[112:115], a[224:227]\n" \
"s_add_u32 m0, %[mB], 0x800\n" \
"s_add_u32 s84, s63, 0x10000\n" \
"v_mfma_f32_16x16x32_bf16 a[132:135], v[128:131], v[88:91], a[132:135]\n" \
"buffer_load_dwordx4 v27, s[68:71], s84 offen lds\n" \
"v_mfma_f32_16x16x32_bf16 a[132:135], v[124:127], v[84:87], a[132:135]\n" \
"ds_read_b64_tr_b16 v[120:121], v32 offset:40960\n" \
"ds_read_b64_tr_b16 v[122:123], v32 offset:41984\n" \
"v_mfma_f32_16x16x32_bf16 a[164:167], v[124:127], v[92:95], a[164:167]\n" \
"v_mfma_f32_16x16x32_bf16 a[164:167], v[128:131], v[96:99], a[164:167]\n" \
"s_add_u32 m0, %[mA], 0x1800\n" \
"s_add_u32 s85, s62, 0xd800\n" \
"v_mfma_f32_16x16x32_bf16 a[196:199], v[128:131], v[104:107], a[196:199]\n" \
"buffer_load_dwordx4 v24, s[76:79], s85 offen lds\n" \
"v_mfma_f32_16x16x32_bf16 a[196:199], v[124:127], v[100:103], a[196:199]\n" \
"v_mfma_f32_16x16x32_bf16 a[228:231], v[124:127], v[108:111], a[228:231]\n" \
"ds_read_b64_tr_b16 v[124:125], v33 offset:32768\n" \
"ds_read_b64_tr_b16 v[126:127], v33 offset:33792\n" \
"v_mfma_f32_16x16x32_bf16 a[228:231], v[128:131], v[112:115], a[228:231]\n" \
"ds_read_b64_tr_b16 v[128:129], v33 offset:40960\n" \
"ds_read_b64_tr_b16 v[130:131], v33 offset:41984\n" \
"v_mfma_f32_16x16x32_bf16 a[136:139], v[136:139], v[88:91], a[136:139]\n" \
"v_mfma_f32_16x16x32_bf16 a[136:139], v[132:135], v[84:87], a[136:139]\n" \
"v_mfma_f32_16x16x32_bf16 a[168:171], v[132:135], v[92:95], a[168:171]\n" \
"v_mfma_f32_16x16x32_bf16 a[168:171], v[136:139], v[96:99], a[168:171]\n" \
"v_mfma_f32_16x16x32_bf16 a[200:203], v[136:139], v[104:107], a[200:203]\n" \
"v_mfma_f32_16x16x32_bf16 a[200:203], v[132:135], v[100:103], a[200:203]\n" \
"v_mfma_f32_16x16x32_bf16 a[232:235], v[132:135], v[108:111], a[232:235]\n" \
"ds_read_b64_tr_b16 v[132:133], v34 offset:32768\n" \
"ds_read_b64_tr_b16 v[134:135], v34 offset:33792\n" \
"v_mfma_f32_16x16x32_bf16 a[232:235], v[136:139], v[112:115], a[232:235]\n" \
"ds_read_b64_tr_b16 v[136:137], v34 offset:40960\n" \
"ds_read_b64_tr_b16 v[138:139], v34 offset:41984\n" \
"v_mfma_f32_16x16x32_bf16 a[140:143], v[144:147], v[88:91], a[140:143]\n" \
"ds_read_b128 v[88:91], v29 offset:32768\n" \
"v_mfma_f32_16x16x32_bf16 a[140:143], v[140:143], v[84:87], a[140:143]\n" \
"ds_read_b128 v[84:87], v28 offset:32768\n" \
"v_mfma_f32_16x16x32_bf16 a[172:175], v[140:143], v[92:95], a[172:175]\n" \
"ds_read_b128 v[92:95], v28 offset:34816\n" \
"v_mfma_f32_16x16x32_bf16 a[172:175], v[144:147], v[96:99], a[172:175]\n" \
"ds_read_b128 v[96:99], v29 offset:34816\n" \
"v_mfma_f32_16x16x32_bf16 a[204:207], v[144:147], v[104:107], a[204:207]\n" \
"ds_read_b128 v[104:107], v29 offset:36864\n" \
"v_mfma_f32_16x16x32_bf16 a[204:207], v[140:143], v[100:103], a[204:207]\n" \
"ds_read_b128 v[100:103], v28 offset:36864\n" \
"v_mfma_f32_16x16x32_bf16 a[236:239], v[140:143], v[108:111], a[236:239]\n" \
"ds_read_b64_tr_b16 v[140:141], v35 offset:32768\n" \
"ds_read_b64_tr_b16 v[142:143], v35 offset:33792\n" \
"v_mfma_f32_16x16x32_bf16 a[236:239], v[144:147], v[112:115], a[236:239]\n" \
"s_add_u32 m0, %[mA], 0x1c00\n" \
"s_waitcnt lgkmcnt(12)\n" \
"s_nop 0\n" \
"v_mfma_f32_16x16x32_bf16 a[16:19], v[56:59], v[152:155], a[16:19]\n" \
"s_add_u32 s85, s62, 0xfc00\n" \
"ds_read_b64_tr_b16 v[144:145], v35 offset:40960\n" \
"v_mfma_f32_16x16x32_bf16 a[16:19], v[52:55], v[148:151], a[16:19]\n" \
"buffer_load_dwordx4 v25, s[76:79], s85 offen lds\n" \
"v_mfma_f32_16x16x32_bf16 a[48:51], v[60:63], v[148:151], a[48:51]\n" \
"s_add_u32 m0, %[mB], 0xc00\n" \
"s_add_u32 s84, s63, 0x18000\n" \
"v_mfma_f32_16x16x32_bf16 a[48:51], v[64:67], v[152:155], a[48:51]\n" \
"ds_read_b64_tr_b16 v[146:147], v35 offset:41984\n" \
"ds_read_b128 v[108:111], v28 offset:38912\n" \
"v_mfma_f32_16x16x32_bf16 a[80:83], v[72:75], v[152:155], a[80:83]\n" \
"buffer_load_dwordx4 v27, s[68:71], s84 offen lds\n" \
"v_mfma_f32_16x16x32_bf16 a[80:83], v[68:71], v[148:151], a[80:83]\n" \
"ds_read_b128 v[112:115], v29 offset:38912\n" \
"s_add_u32 s62, s62, 0x80\n" \
"v_mfma_f32_16x16x32_bf16 a[112:115], v[76:79], v[148:151], a[112:115]\n" \
"s_cmp_lt_u32 s62, 0x480\n" \
"s_cselect_b32 s62, s62, 0\n" \
"v_mfma_f32_16x16x32_bf16 a[112:115], v[80:83], v[152:155], a[112:115]\n" \
"s_lshl_b32 s72, s62, 6\n" \
"s_add_u32 s72, s72, s37\n" \
"v_mfma_f32_16x16x32_bf16 a[20:23], v[56:59], v[160:163], a[20:23]\n" \
"s_lshl_b32 s63, s62, 12\n" \
"v_add_u32_e64 v28, s59, v28\n" \
"v_mfma_f32_16x16x32_bf16 a[20:23], v[52:55], v[156:159], a[20:23]\n" \
"v_add_u32_e64 v29, s59, v29\n" \
"v_accvgpr_read_b32 v184, a16\n" \
"v_mfma_f32_16x16x32_bf16 a[52:55], v[60:63], v[156:159], a[52:55]\n" \
"v_accvgpr_read_b32 v185, a17\n" \
"v_accvgpr_read_b32 v186, a18\n" \
"v_mfma_f32_16x16x32_bf16 a[52:55], v[64:67], v[160:163], a[52:55]\n" \
"v_accvgpr_read_b32 v187, a19\n" \
"v_accvgpr_write_b32 a16, 0\n" \
"v_mfma_f32_16x16x32_bf16 a[84:87], v[72:75], v[160:163], a[84:87]\n" \
"v_accvgpr_write_b32 a17, 0\n" \
"v_accvgpr_write_b32 a18, 0\n" \
"v_mfma_f32_16x16x32_bf16 a[84:87], v[68:71], v[156:159], a[84:87]\n" \
"v_accvgpr_write_b32 a19, 0\n" \
"v_accvgpr_read_b32 v180, a48\n" \
"v_mfma_f32_16x16x32_bf16 a[116:119], v[76:79], v[156:159], a[116:119]\n" \
"v_accvgpr_read_b32 v181, a49\n" \
"v_accvgpr_read_b32 v182, a50\n" \
"v_mfma_f32_16x16x32_bf16 a[116:119], v[80:83], v[160:163], a[116:119]\n" \
"v_accvgpr_read_b32 v183, a51\n" \
"v_accvgpr_read_b32 v188, a80\n" \
"v_mfma_f32_16x16x32_bf16 a[24:27], v[56:59], v[168:171], a[24:27]\n" \
"v_accvgpr_read_b32 v189, a81\n" \
"v_accvgpr_read_b32 v190, a82\n" \
"v_mfma_f32_16x16x32_bf16 a[24:27], v[52:55], v[164:167], a[24:27]\n" \
"v_accvgpr_read_b32 v191, a83\n" \
"v_cvt_pk_bf16_f32 v184, v184, v185\n" \
"v_mfma_f32_16x16x32_bf16 a[56:59], v[60:63], v[164:167], a[56:59]\n" \
"v_cvt_pk_bf16_f32 v185, v186, v187\n" \
"v_cvt_pk_bf16_f32 v186, v180, v181\n" \
"v_mfma_f32_16x16x32_bf16 a[56:59], v[64:67], v[168:171], a[56:59]\n" \
"v_cvt_pk_bf16_f32 v187, v182, v183\n" \
"v_accvgpr_read_b32 v180, a112\n" \
"v_mfma_f32_16x16x32_bf16 a[88:91], v[72:75], v[168:171], a[88:91]\n" \
"v_accvgpr_read_b32 v181, a113\n" \
"v_accvgpr_read_b32 v182, a114\n" \
"v_mfma_f32_16x16x32_bf16 a[88:91], v[68:71], v[164:167], a[88:91]\n" \
"v_accvgpr_read_b32 v183, a115\n" \
"v_accvgpr_write_b32 a112, 0\n" \
"v_mfma_f32_16x16x32_bf16 a[120:123], v[76:79], v[164:167], a[120:123]\n" \
"v_accvgpr_write_b32 a113, 0\n" \
"v_accvgpr_write_b32 a114, 0\n" \
"v_mfma_f32_16x16x32_bf16 a[120:123], v[80:83], v[168:171], a[120:123]\n" \
"v_accvgpr_write_b32 a115, 0\n" \
"v_accvgpr_read_b32 v192, a20\n" \
"v_mfma_f32_16x16x32_bf16 a[28:31], v[56:59], v[176:179], a[28:31]\n" \
"v_accvgpr_read_b32 v193, a21\n" \
"v_accvgpr_read_b32 v194, a22\n" \
"v_mfma_f32_16x16x32_bf16 a[28:31], v[52:55], v[172:175], a[28:31]\n" \
"v_accvgpr_read_b32 v195, a23\n" \
"v_cvt_pk_bf16_f32 v188, v188, v189\n" \
"v_mfma_f32_16x16x32_bf16 a[60:63], v[60:63], v[172:175], a[60:63]\n" \
"v_cvt_pk_bf16_f32 v189, v190, v191\n" \
"v_cvt_pk_bf16_f32 v190, v180, v181\n" \
"v_mfma_f32_16x16x32_bf16 a[60:63], v[64:67], v[176:179], a[60:63]\n" \
"v_cvt_pk_bf16_f32 v191, v182, v183\n" \
"v_accvgpr_read_b32 v180, a52\n" \
"v_mfma_f32_16x16x32_bf16 a[92:95], v[72:75], v[176:179], a[92:95]\n" \
"v_accvgpr_read_b32 v181, a53\n" \
"v_accvgpr_read_b32 v182, a54\n" \
"v_mfma_f32_16x16x32_bf16 a[92:95], v[68:71], v[172:175], a[92:95]\n" \
"v_accvgpr_read_b32 v183, a55\n" \
"v_accvgpr_read_b32 v196, a84\n" \
"v_mfma_f32_16x16x32_bf16 a[124:127], v[76:79], v[172:175], a[124:127]\n" \
"v_accvgpr_read_b32 v197, a85\n" \
"v_accvgpr_read_b32 v198, a86\n" \
"v_mfma_f32_16x16x32_bf16 a[124:127], v[80:83], v[176:179], a[124:127]\n" \
"v_accvgpr_read_b32 v199, a87\n" \
"v_cvt_pk_bf16_f32 v192, v192, v193\n" \
"v_mfma_f32_16x16x32_bf16 a[0:3], v[120:123], v[56:59], a[0:3]\n" \
"v_cvt_pk_bf16_f32 v193, v194, v195\n" \
"v_cvt_pk_bf16_f32 v194, v180, v181\n" \
"v_mfma_f32_16x16x32_bf16 a[0:3], v[116:119], v[52:55], a[0:3]\n" \
"v_cvt_pk_bf16_f32 v195, v182, v183\n" \
"v_accvgpr_read_b32 v180, a116\n" \
"v_mfma_f32_16x16x32_bf16 a[32:35], v[116:119], v[60:63], a[32:35]\n" \
"v_accvgpr_read_b32 v181, a117\n" \
"v_accvgpr_read_b32 v182, a118\n" \
"v_mfma_f32_16x16x32_bf16 a[32:35], v[120:123], v[64:67], a[32:35]\n" \
"v_accvgpr_read_b32 v183, a119\n" \
"v_accvgpr_write_b32 a116, 0\n" \
"v_mfma_f32_16x16x32_bf16 a[64:67], v[120:123], v[72:75], a[64:67]\n" \
"v_accvgpr_write_b32 a117, 0\n" \
"v_accvgpr_write_b32 a118, 0\n" \
"v_mfma_f32_16x16x32_bf16 a[64:67], v[116:119], v[68:71], a[64:67]\n" \
"v_accvgpr_write_b32 a119, 0\n" \
"v_accvgpr_read_b32 v200, a24\n" \
"v_mfma_f32_16x16x32_bf16 a[96:99], v[116:119], v[76:79], a[96:99]\n" \
"v_accvgpr_read_b32 v201, a25\n" \
"v_accvgpr_read_b32 v202, a26\n" \
"v_mfma_f32_16x16x32_bf16 a[96:99], v[120:123], v[80:83], a[96:99]\n" \
"v_accvgpr_read_b32 v203, a27\n" \
"v_cvt_pk_bf16_f32 v196, v196, v197\n" \
"v_mfma_f32_16x16x32_bf16 a[4:7], v[128:131], v[56:59], a[4:7]\n" \
"v_cvt_pk_bf16_f32 v197, v198, v199\n" \
"v_cvt_pk_bf16_f32 v198, v180, v181\n" \
"v_mfma_f32_16x16x32_bf16 a[4:7], v[124:127], v[52:55], a[4:7]\n" \
"v_cvt_pk_bf16_f32 v199, v182, v183\n" \
"v_accvgpr_read_b32 v180, a56\n" \
"v_mfma_f32_16x16x32_bf16 a[36:39], v[124:127], v[60:63], a[36:39]\n" \
"v_accvgpr_read_b32 v181, a57\n" \
"v_accvgpr_read_b32 v182, a58\n" \
"v_mfma_f32_16x16x32_bf16 a[36:39], v[128:131], v[64:67], a[36:39]\n" \
"v_accvgpr_read_b32 v183, a59\n" \
"v_accvgpr_read_b32 v204, a88\n" \
"v_mfma_f32_16x16x32_bf16 a[68:71], v[128:131], v[72:75], a[68:71]\n" \
"v_accvgpr_read_b32 v205, a89\n" \
"v_accvgpr_read_b32 v206, a90\n" \
"v_mfma_f32_16x16x32_bf16 a[68:71], v[124:127], v[68:71], a[68:71]\n" \
"v_accvgpr_read_b32 v207, a91\n" \
"v_cvt_pk_bf16_f32 v200, v200, v201\n" \
"v_mfma_f32_16x16x32_bf16 a[100:103], v[124:127], v[76:79], a[100:103]\n" \
"v_cvt_pk_bf16_f32 v201, v202, v203\n" \
"v_cvt_pk_bf16_f32 v202, v180, v181\n" \
"v_mfma_f32_16x16x32_bf16 a[100:103], v[128:131], v[80:83], a[100:103]\n" \
"v_cvt_pk_bf16_f32 v203, v182, v183\n" \
"s_waitcnt lgkmcnt(0)\n" \
"s_nop 0\n" \
"v_mfma_f32_16x16x32_bf16 a[8:11], v[136:139], v[56:59], a[8:11]\n" \
"v_accvgpr_read_b32 v180, a120\n" \
"v_accvgpr_read_b32 v181, a121\n" \
"v_mfma_f32_16x16x32_bf16 a[8:11], v[132:135], v[52:55], a[8:11]\n" \
"v_accvgpr_read_b32 v182, a122\n" \
"v_accvgpr_read_b32 v183, a123\n" \
"v_mfma_f32_16x16x32_bf16 a[40:43], v[132:135], v[60:63], a[40:43]\n" \
"v_accvgpr_write_b32 a120, 0\n" \
"v_accvgpr_write_b32 a121, 0\n" \
"v_mfma_f32_16x16x32_bf16 a[40:43], v[136:139], v[64:67], a[40:43]\n" \
"v_accvgpr_write_b32 a122, 0\n" \
"v_accvgpr_write_b32 a123, 0\n" \
"v_mfma_f32_16x16x32_bf16 a[72:75], v[136:139], v[72:75], a[72:75]\n" \
"v_accvgpr_read_b32 v208, a28\n" \
"v_accvgpr_read_b32 v209, a29\n" \
"v_mfma_f32_16x16x32_bf16 a[72:75], v[132:135], v[68:71], a[72:75]\n" \
"v_accvgpr_read_b32 v210, a30\n" \
"v_accvgpr_read_b32 v211, a31\n" \
"v_mfma_f32_16x16x32_bf16 a[104:107], v[132:135], v[76:79], a[104:107]\n" \
"v_cvt_pk_bf16_f32 v204, v204, v205\n" \
"v_cvt_pk_bf16_f32 v205, v206, v207\n" \
"v_mfma_f32_16x16x32_bf16 a[104:107], v[136:139], v[80:83], a[104:107]\n" \
"v_cvt_pk_bf16_f32 v206, v180, v181\n" \
"v_cvt_pk_bf16_f32 v207, v182, v183\n" \
"v_mfma_f32_16x16x32_bf16 a[12:15], v[144:147], v[56:59], a[12:15]\n" \
"v_accvgpr_read_b32 v180, a60\n" \
"v_accvgpr_read_b32 v181, a61\n" \
"v_mfma_f32_16x16x32_bf16 a[12:15], v[140:143], v[52:55], a[12:15]\n" \
"v_accvgpr_read_b32 v182, a62\n" \
"v_accvgpr_read_b32 v183, a63\n" \
"v_mfma_f32_16x16x32_bf16 a[44:47], v[140:143], v[60:63], a[44:47]\n" \
"v_accvgpr_read_b32 v212, a92\n" \
"v_accvgpr_read_b32 v213, a93\n" \
"v_mfma_f32_16x16x32_bf16 a[44:47], v[144:147], v[64:67], a[44:47]\n" \
"v_accvgpr_read_b32 v214, a94\n" \
"v_accvgpr_read_b32 v215, a95\n" \
"v_mfma_f32_16x16x32_bf16 a[76:79], v[144:147], v[72:75], a[76:79]\n" \
"v_cvt_pk_bf16_f32 v208, v208, v209\n" \
"v_cvt_pk_bf16_f32 v209, v210, v211\n" \
"v_mfma_f32_16x16x32_bf16 a[76:79], v[140:143], v[68:71], a[76:79]\n" \
"v_cvt_pk_bf16_f32 v210, v180, v181\n" \
"v_cvt_pk_bf16_f32 v211, v182, v183\n" \
"v_mfma_f32_16x16x32_bf16 a[108:111], v[140:143], v[76:79], a[108:111]\n" \
"v_accvgpr_read_b32 v180, a124\n" \
"v_accvgpr_read_b32 v181, a125\n" \
"v_mfma_f32_16x16x32_bf16 a[108:111], v[144:147], v[80:83], a[108:111]\n" \
"v_accvgpr_read_b32 v182, a126\n" \
"v_accvgpr_read_b32 v183, a127\n" \
"s_waitcnt vmcnt(0)\n" \
"s_barrier\n" \
"v_mfma_f32_16x16x32_bf16 a[128:131], v[120:123], v[88:91], a[128:131]\n" \
"s_add_u32 m0, s40, 0x8000\n" \
"s_add_u32 s85, s72, 0x0\n" \
"s_nop 0\n" \
"v_mfma_f32_16x16x32_bf16 a[128:131], v[116:119], v[84:87], a[128:131]\n" \
"buffer_load_dwordx4 v20, s[80:83], s85 offen sc1 lds\n" \
"v_mfma_f32_16x16x32_bf16 a[160:163], v[116:119], v[92:95], a[160:163]\n" \
"s_add_u32 m0, s40, 0x9000\n" \
"s_add_u32 s84, s72, 0x1000\n" \
"L_pf4:\n" \
"s_cbranch_execz L_pf8\n" \
"s_nop 0\n" \
"v_mfma_f32_16x16x32_bf16 a[160:163], v[120:123], v[96:99], a[160:163]\n" \
"ds_read_b128 v[52:55], v28 offset:0\n" \
"ds_read_b128 v[56:59], v29 offset:0\n" \
"v_mfma_f32_16x16x32_bf16 a[192:195], v[120:123], v[104:107], a[192:195]\n" \
"buffer_load_dwordx4 v20, s[80:83], s84 offen sc1 lds\n" \
"v_mfma_f32_16x16x32_bf16 a[192:195], v[116:119], v[100:103], a[192:195]\n" \
"ds_read_b128 v[60:63], v28 offset:2048\n" \
"ds_read_b128 v[64:67], v29 offset:2048\n" \
"v_mfma_f32_16x16x32_bf16 a[224:227], v[116:119], v[108:111], a[224:227]\n" \
"s_add_u32 m0, s40, 0xb000\n" \
"s_add_u32 s85, s62, 0x0\n" \
"v_mfma_f32_16x16x32_bf16 a[224:227], v[120:123], v[112:115], a[224:227]\n" \
"buffer_load_dwordx4 v21, s[76:79], s85 offen lds\n" \
"v_mfma_f32_16x16x32_bf16 a[132:135], v[128:131], v[88:91], a[132:135]\n" \
"ds_read_b64_tr_b16 v[116:117], v36 offset:0\n" \
"ds_read_b64_tr_b16 v[118:119], v36 offset:512\n" \
"v_mfma_f32_16x16x32_bf16 a[132:135], v[124:127], v[84:87], a[132:135]\n" \
"ds_read_b64_tr_b16 v[120:121], v36 offset:4096\n" \
"ds_read_b64_tr_b16 v[122:123], v36 offset:4608\n" \
"v_mfma_f32_16x16x32_bf16 a[164:167], v[124:127], v[92:95], a[164:167]\n" \
"s_add_u32 m0, %[mA], 0x8000\n" \
"s_add_u32 s84, s62, 0x0\n" \
"v_mfma_f32_16x16x32_bf16 a[164:167], v[128:131], v[96:99], a[164:167]\n" \
"buffer_load_dwordx4 v24, s[76:79], s84 offen lds\n" \
"v_mfma_f32_16x16x32_bf16 a[196:199], v[128:131], v[104:107], a[196:199]\n" \
"ds_read_b128 v[68:71], v28 offset:4096\n" \
"ds_read_b128 v[72:75], v29 offset:4096\n" \
"v_mfma_f32_16x16x32_bf16 a[196:199], v[124:127], v[100:103], a[196:199]\n" \
"s_add_u32 m0, %[mA], 0x8400\n" \
"s_add_u32 s85, s62, 0x2400\n" \
"v_mfma_f32_16x16x32_bf16 a[228:231], v[124:127], v[108:111], a[228:231]\n" \
"buffer_load_dwordx4 v25, s[76:79], s85 offen lds\n" \
"v_mfma_f32_16x16x32_bf16 a[228:231], v[128:131], v[112:115], a[228:231]\n" \
"ds_read_b64_tr_b16 v[128:129], v37 offset:4096\n" \
"ds_read_b64_tr_b16 v[130:131], v37 offset:4608\n" \
"v_mfma_f32_16x16x32_bf16 a[136:139], v[136:139], v[88:91], a[136:139]\n" \
"ds_read_b64_tr_b16 v[124:125], v37 offset:0\n" \
"ds_read_b64_tr_b16 v[126:127], v37 offset:512\n" \
"v_mfma_f32_16x16x32_bf16 a[136:139], v[132:135], v[84:87], a[136:139]\n" \
"s_add_u32 m0, %[mB], 0x8000\n" \
"s_add_u32 s84, s63, 0x0\n" \
"s_nop 0\n" \
"v_mfma_f32_16x16x32_bf16 a[168:171], v[132:135], v[92:95], a[168:171]\n" \
"buffer_load_dwordx4 v26, s[68:71], s84 offen lds\n" \
"v_mfma_f32_16x16x32_bf16 a[168:171], v[136:139], v[96:99], a[168:171]\n" \
"ds_read_b128 v[76:79], v28 offset:6144\n" \
"ds_read_b128 v[80:83], v29 offset:6144\n" \
"v_mfma_f32_16x16x32_bf16 a[200:203], v[136:139], v[104:107], a[200:203]\n" \
"v_accvgpr_write_b32 a124, 0\n" \
"v_accvgpr_write_b32 a125, 0\n" \
"v_mfma_f32_16x16x32_bf16 a[200:203], v[132:135], v[100:103], a[200:203]\n" \
"s_add_u32 m0, %[mA], 0x8800\n" \
"s_add_u32 s85, s62, 0x4800\n" \
"v_mfma_f32_16x16x32_bf16 a[232:235], v[132:135], v[108:111], a[232:235]\n" \
"buffer_load_dwordx4 v24, s[76:79], s85 offen lds\n" \
"v_mfma_f32_16x16x32_bf16 a[232:235], v[136:139], v[112:115], a[232:235]\n" \
"ds_read_b64_tr_b16 v[132:133], v38 offset:0\n" \
"ds_read_b64_tr_b16 v[134:135], v38 offset:512\n" \
"v_mfma_f32_16x16x32_bf16 a[140:143], v[144:147], v[88:91], a[140:143]\n" \
"s_add_u32 m0, %[mA], 0x8c00\n" \
"s_add_u32 s84, s62, 0x6c00\n" \
"v_mfma_f32_16x16x32_bf16 a[140:143], v[140:143], v[84:87], a[140:143]\n" \
"buffer_load_dwordx4 v25, s[76:79], s84 offen lds\n" \
"v_mfma_f32_16x16x32_bf16 a[172:175], v[140:143], v[92:95], a[172:175]\n" \
"ds_read_b64_tr_b16 v[136:137], v38 offset:4096\n" \
"ds_read_b64_tr_b16 v[138:139], v38 offset:4608\n" \
"v_mfma_f32_16x16x32_bf16 a[172:175], v[144:147], v[96:99], a[172:175]\n" \
"v_accvgpr_write_b32 a126, 0\n" \
"v_accvgpr_write_b32 a127, 0\n" \
"v_mfma_f32_16x16x32_bf16 a[204:207], v[144:147], v[104:107], a[204:207]\n" \
"s_add_u32 m0, %[mB], 0x8400\n" \
"s_add_u32 s85, s63, 0x8000\n" \
"v_mfma_f32_16x16x32_bf16 a[204:207], v[140:143], v[100:103], a[204:207]\n" \
"buffer_load_dwordx4 v26, s[68:71], s85 offen lds\n" \
"v_mfma_f32_16x16x32_bf16 a[236:239], v[140:143], v[108:111], a[236:239]\n" \
"ds_read_b64_tr_b16 v[140:141], v39 offset:0\n" \
"ds_read_b64_tr_b16 v[142:143], v39 offset:960\n" \
"v_mfma_f32_16x16x32_bf16 a[236:239], v[144:147], v[112:115], a[236:239]\n" \
"v_accvgpr_read_b32 v216, a0\n" \
"v_accvgpr_read_b32 v217, a1\n" \
"v_mfma_f32_16x16x32_bf16 a[144:147], v[88:91], v[152:155], a[144:147]\n" \
"s_add_u32 m0, %[mA], 0x9000\n" \
"s_add_u32 s84, s62, 0x9000\n" \
"v_mfma_f32_16x16x32_bf16 a[144:147], v[84:87], v[148:151], a[144:147]\n" \
"buffer_load_dwordx4 v24, s[76:79], s84 offen lds\n" \
"v_mfma_f32_16x16x32_bf16 a[148:151], v[84:87], v[156:159], a[148:151]\n" \
"v_accvgpr_read_b32 v218, a2\n" \
"v_accvgpr_read_b32 v219, a3\n" \
"v_mfma_f32_16x16x32_bf16 a[148:151], v[88:91], v[160:163], a[148:151]\n" \
"s_add_u32 m0, %[mA], 0x9400\n" \
"s_add_u32 s85, s62, 0xb400\n" \
"v_mfma_f32_16x16x32_bf16 a[152:155], v[88:91], v[168:171], a[152:155]\n" \
"buffer_load_dwordx4 v25, s[76:79], s85 offen lds\n" \
"v_mfma_f32_16x16x32_bf16 a[152:155], v[84:87], v[164:167], a[152:155]\n" \
"v_cvt_pk_bf16_f32 v212, v212, v213\n" \
"v_cvt_pk_bf16_f32 v213, v214, v215\n" \
"v_mfma_f32_16x16x32_bf16 a[156:159], v[84:87], v[172:175], a[156:159]\n" \
"ds_read_b128 v[84:87], v22 offset:0\n" \
"v_cvt_pk_bf16_f32 v214, v180, v181\n" \
"v_mfma_f32_16x16x32_bf16 a[156:159], v[88:91], v[176:179], a[156:159]\n" \
"s_add_u32 m0, %[mB], 0x8800\n" \
"s_add_u32 s84, s63, 0x10000\n" \
"v_mfma_f32_16x16x32_bf16 a[176:179], v[96:99], v[152:155], a[176:179]\n" \
"buffer_load_dwordx4 v27, s[68:71], s84 offen lds\n" \
"v_mfma_f32_16x16x32_bf16 a[176:179], v[92:95], v[148:151], a[176:179]\n" \
"ds_read_b128 v[88:91], v23 offset:0\n" \
"v_cvt_pk_bf16_f32 v215, v182, v183\n" \
"v_mfma_f32_16x16x32_bf16 a[180:183], v[92:95], v[156:159], a[180:183]\n" \
"v_accvgpr_read_b32 v180, a4\n" \
"v_accvgpr_read_b32 v181, a5\n" \
"v_mfma_f32_16x16x32_bf16 a[180:183], v[96:99], v[160:163], a[180:183]\n" \
"s_add_u32 m0, %[mA], 0x9800\n" \
"s_add_u32 s85, s62, 0xd800\n" \
"v_mfma_f32_16x16x32_bf16 a[184:187], v[96:99], v[168:171], a[184:187]\n" \
"buffer_load_dwordx4 v24, s[76:79], s85 offen lds\n" \
"v_mfma_f32_16x16x32_bf16 a[184:187], v[92:95], v[164:167], a[184:187]\n" \
"v_accvgpr_read_b32 v182, a6\n" \
"v_accvgpr_read_b32 v183, a7\n" \
"v_mfma_f32_16x16x32_bf16 a[188:191], v[92:95], v[172:175], a[188:191]\n" \
"v_cvt_pk_bf16_f32 v216, v216, v217\n" \
"v_cvt_pk_bf16_f32 v217, v218, v219\n" \
"v_mfma_f32_16x16x32_bf16 a[188:191], v[96:99], v[176:179], a[188:191]\n" \
"v_accvgpr_read_b32 v220, a32\n" \
"v_accvgpr_read_b32 v221, a33\n" \
"v_mfma_f32_16x16x32_bf16 a[208:211], v[104:107], v[152:155], a[208:211]\n" \
"v_cvt_pk_bf16_f32 v218, v180, v181\n" \
"v_cvt_pk_bf16_f32 v219, v182, v183\n" \
"v_mfma_f32_16x16x32_bf16 a[208:211], v[100:103], v[148:151], a[208:211]\n" \
"v_accvgpr_read_b32 v222, a34\n" \
"v_accvgpr_read_b32 v223, a35\n" \
"v_mfma_f32_16x16x32_bf16 a[212:215], v[100:103], v[156:159], a[212:215]\n" \
"v_cvt_pk_bf16_f32 v220, v220, v221\n" \
"v_accvgpr_read_b32 v180, a36\n" \
"v_mfma_f32_16x16x32_bf16 a[212:215], v[104:107], v[160:163], a[212:215]\n" \
"v_accvgpr_read_b32 v181, a37\n" \
"v_accvgpr_read_b32 v182, a38\n" \
"v_mfma_f32_16x16x32_bf16 a[216:219], v[104:107], v[168:171], a[216:219]\n" \
"v_accvgpr_read_b32 v183, a39\n" \
"v_cvt_pk_bf16_f32 v221, v222, v223\n" \
"v_mfma_f32_16x16x32_bf16 a[216:219], v[100:103], v[164:167], a[216:219]\n" \
"v_accvgpr_read_b32 v224, a64\n" \
"v_accvgpr_read_b32 v225, a65\n" \
"v_mfma_f32_16x16x32_bf16 a[220:223], v[100:103], v[172:175], a[220:223]\n" \
"v_cvt_pk_bf16_f32 v222, v180, v181\n" \
"v_cvt_pk_bf16_f32 v223, v182, v183\n" \
"v_mfma_f32_16x16x32_bf16 a[220:223], v[104:107], v[176:179], a[220:223]\n" \
"v_accvgpr_read_b32 v226, a66\n" \
"v_accvgpr_read_b32 v227, a67\n" \
"v_mfma_f32_16x16x32_bf16 a[240:243], v[112:115], v[152:155], a[240:243]\n" \
"ds_read_b64_tr_b16 v[152:153], v32 offset:8192\n" \
"ds_read_b64_tr_b16 v[154:155], v32 offset:9216\n" \
"v_mfma_f32_16x16x32_bf16 a[240:243], v[108:111], v[148:151], a[240:243]\n" \
"ds_read_b64_tr_b16 v[148:149], v32 offset:0\n" \
"ds_read_b64_tr_b16 v[150:151], v32 offset:1024\n" \
"v_mfma_f32_16x16x32_bf16 a[244:247], v[108:111], v[156:159], a[244:247]\n" \
"ds_read_b64_tr_b16 v[156:157], v33 offset:0\n" \
"ds_read_b64_tr_b16 v[158:159], v33 offset:1024\n" \
"v_mfma_f32_16x16x32_bf16 a[244:247], v[112:115], v[160:163], a[244:247]\n" \
"ds_read_b64_tr_b16 v[160:161], v33 offset:8192\n" \
"ds_read_b64_tr_b16 v[162:163], v33 offset:9216\n" \
"v_mfma_f32_16x16x32_bf16 a[248:251], v[112:115], v[168:171], a[248:251]\n" \
"ds_read_b64_tr_b16 v[168:169], v34 offset:8192\n" \
"ds_read_b64_tr_b16 v[170:171], v34 offset:9216\n" \
"v_mfma_f32_16x16x32_bf16 a[248:251], v[108:111], v[164:167], a[248:251]\n" \
"ds_read_b64_tr_b16 v[164:165], v34 offset:0\n" \
"ds_read_b64_tr_b16 v[166:167], v34 offset:1024\n" \
"v_mfma_f32_16x16x32_bf16 a[252:255], v[108:111], v[172:175], a[252:255]\n" \
"ds_read_b64_tr_b16 v[172:173], v35 offset:0\n" \
"ds_read_b64_tr_b16 v[174:175], v35 offset:1024\n" \
"v_mfma_f32_16x16x32_bf16 a[252:255], v[112:115], v[176:179], a[252:255]\n" \
"s_add_u32 m0, %[mA], 0x9c00\n" \
"s_waitcnt lgkmcnt(14)\n" \
"s_nop 0\n" \
"v_smfmac_f32_16x16x64_bf16 a[16:19], v[140:143], v[84:91], v50\n" \
"v_mfma_f32_16x16x32_bf16 a[48:51], v[128:131], v[88:91], 0\n" \
"s_add_u32 s85, s62, 0xfc00\n" \
"ds_read_b64_tr_b16 v[176:177], v35 offset:8192\n" \
"v_mfma_f32_16x16x32_bf16 a[48:51], v[124:127], v[84:87], a[48:51]\n" \
"buffer_load_dwordx4 v25, s[76:79], s85 offen lds\n" \
"v_mfma_f32_16x16x32_bf16 a[80:83], v[84:87], v[132:135], 0\n" \
"s_add_u32 m0, %[mB], 0x8c00\n" \
"s_add_u32 s84, s63, 0x18000\n" \
"v_mfma_f32_16x16x32_bf16 a[80:83], v[88:91], v[136:139], a[80:83]\n" \
"ds_read_b64_tr_b16 v[178:179], v35 offset:9216\n" \
"v_cvt_pk_bf16_f32 v224, v224, v225\n" \
"v_smfmac_f32_16x16x64_bf16 a[112:115], v[140:143], v[52:59], v50\n" \
"v_mfma_f32_16x16x32_bf16 a[20:23], v[128:131], v[56:59], 0\n" \
"buffer_load_dwordx4 v27, s[68:71], s84 offen lds\n" \
"v_mfma_f32_16x16x32_bf16 a[20:23], v[124:127], v[52:55], a[20:23]\n" \
"s_add_u32 s88, s44, 0x0\n" \
"s_nop 0\n" \
"v_accvgpr_read_b32 v180, a68\n" \
"v_mfma_f32_16x16x32_bf16 a[52:55], v[116:119], v[52:55], 0\n" \
"v_accvgpr_read_b32 v181, a69\n" \
"v_accvgpr_read_b32 v182, a70\n" \
"v_mfma_f32_16x16x32_bf16 a[52:55], v[120:123], v[56:59], a[52:55]\n" \
"buffer_store_dwordx4 v[184:187], v20, s[80:83], s88 offen offset:0 sc1\n" \
"v_mfma_f32_16x16x32_bf16 a[84:87], v[56:59], v[136:139], 0\n" \
"v_accvgpr_read_b32 v183, a71\n" \
"v_cvt_pk_bf16_f32 v225, v226, v227\n" \
"v_mfma_f32_16x16x32_bf16 a[84:87], v[52:55], v[132:135], a[84:87]\n" \
"v_accvgpr_read_b32 v228, a96\n" \
"v_accvgpr_read_b32 v229, a97\n" \
"v_smfmac_f32_16x16x64_bf16 a[116:119], v[140:143], v[60:67], v50\n" \
"v_mfma_f32_16x16x32_bf16 a[24:27], v[124:127], v[60:63], 0\n" \
"v_cvt_pk_bf16_f32 v226, v180, v181\n" \
"v_cvt_pk_bf16_f32 v227, v182, v183\n" \
"v_mfma_f32_16x16x32_bf16 a[24:27], v[128:131], v[64:67], a[24:27]\n" \
"s_add_u32 s89, s44, 0x1000\n" \
"v_accvgpr_read_b32 v230, a98\n" \
"v_mfma_f32_16x16x32_bf16 a[56:59], v[120:123], v[64:67], 0\n" \
"buffer_store_dwordx4 v[188:191], v20, s[80:83], s89 offen offset:0 sc1\n" \
"v_mfma_f32_16x16x32_bf16 a[56:59], v[116:119], v[60:63], a[56:59]\n" \
"v_accvgpr_read_b32 v231, a99\n" \
"v_cvt_pk_bf16_f32 v228, v228, v229\n" \
"v_mfma_f32_16x16x32_bf16 a[88:91], v[60:63], v[132:135], 0\n" \
"v_accvgpr_read_b32 v180, a100\n" \
"v_accvgpr_read_b32 v181, a101\n" \
"v_mfma_f32_16x16x32_bf16 a[88:91], v[64:67], v[136:139], a[88:91]\n" \
"v_accvgpr_read_b32 v182, a102\n" \
"v_accvgpr_read_b32 v183, a103\n" \
"v_smfmac_f32_16x16x64_bf16 a[120:123], v[140:143], v[68:75], v50\n" \
"v_mfma_f32_16x16x32_bf16 a[28:31], v[128:131], v[72:75], 0\n" \
"v_cvt_pk_bf16_f32 v229, v230, v231\n" \
"v_accvgpr_read_b32 v232, a8\n" \
"v_mfma_f32_16x16x32_bf16 a[28:31], v[124:127], v[68:71], a[28:31]\n" \
"v_accvgpr_read_b32 v233, a9\n" \
"v_cvt_pk_bf16_f32 v230, v180, v181\n" \
"v_mfma_f32_16x16x32_bf16 a[60:63], v[116:119], v[68:71], 0\n" \
"s_add_u32 s88, s44, 0x0\n" \
"s_nop 0\n" \
"v_cvt_pk_bf16_f32 v231, v182, v183\n" \
"v_mfma_f32_16x16x32_bf16 a[60:63], v[120:123], v[72:75], a[60:63]\n" \
"buffer_store_dwordx4 v[192:195], v20, s[80:83], s88 offen offset:1024 sc1\n" \
"v_mfma_f32_16x16x32_bf16 a[92:95], v[72:75], v[136:139], 0\n" \
"v_accvgpr_read_b32 v234, a10\n" \
"v_accvgpr_read_b32 v235, a11\n" \
"v_mfma_f32_16x16x32_bf16 a[92:95], v[68:71], v[132:135], a[92:95]\n" \
"v_cvt_pk_bf16_f32 v232, v232, v233\n" \
"v_accvgpr_read_b32 v180, a12\n" \
"v_smfmac_f32_16x16x64_bf16 a[124:127], v[140:143], v[76:83], v50\n" \
"v_mfma_f32_16x16x32_bf16 a[0:3], v[124:127], v[76:79], 0\n" \
"v_accvgpr_read_b32 v181, a13\n" \
"v_accvgpr_read_b32 v182, a14\n" \
"v_mfma_f32_16x16x32_bf16 a[0:3], v[128:131], v[80:83], a[0:3]\n" \
"v_accvgpr_read_b32 v183, a15\n" \
"v_cvt_pk_bf16_f32 v233, v234, v235\n" \
"v_mfma_f32_16x16x32_bf16 a[4:7], v[120:123], v[80:83], 0\n" \
"v_accvgpr_read_b32 v236, a40\n" \
"v_accvgpr_read_b32 v237, a41\n" \
"v_mfma_f32_16x16x32_bf16 a[4:7], v[116:119], v[76:79], a[4:7]\n" \
"v_cvt_pk_bf16_f32 v234, v180, v181\n" \
"v_cvt_pk_bf16_f32 v235, v182, v183\n" \
"v_mfma_f32_16x16x32_bf16 a[32:35], v[76:79], v[132:135], 0\n" \
"v_accvgpr_read_b32 v238, a42\n" \
"v_accvgpr_read_b32 v239, a43\n" \
"v_mfma_f32_16x16x32_bf16 a[32:35], v[80:83], v[136:139], a[32:35]\n" \
"v_cvt_pk_bf16_f32 v236, v236, v237\n" \
"v_accvgpr_read_b32 v180, a44\n" \
"v_accvgpr_read_b32 v181, a45\n" \
"v_accvgpr_read_b32 v182, a46\n" \
"v_accvgpr_read_b32 v183, a47\n" \
"v_cvt_pk_bf16_f32 v237, v238, v239\n" \
"v_accvgpr_read_b32 v240, a72\n" \
"v_accvgpr_read_b32 v241, a73\n" \
"v_cvt_pk_bf16_f32 v238, v180, v181\n" \
"v_cvt_pk_bf16_f32 v239, v182, v183\n" \
"v_accvgpr_read_b32 v242, a74\n" \
"v_accvgpr_read_b32 v243, a75\n" \
"v_cvt_pk_bf16_f32 v240, v240, v241\n" \
"v_accvgpr_read_b32 v180, a76\n" \
"v_accvgpr_read_b32 v181, a77\n" \
"v_accvgpr_read_b32 v182, a78\n" \
"v_accvgpr_read_b32 v183, a79\n" \
"v_cvt_pk_bf16_f32 v241, v242, v243\n" \
"v_accvgpr_read_b32 v244, a104\n" \
"v_accvgpr_read_b32 v245, a105\n" \
"v_cvt_pk_bf16_f32 v242, v180, v181\n" \
"v_cvt_pk_bf16_f32 v243, v182, v183\n" \
"v_accvgpr_read_b32 v246, a106\n" \
"v_accvgpr_read_b32 v247, a107\n" \
"v_cvt_pk_bf16_f32 v244, v244, v245\n" \
"v_accvgpr_read_b32 v248, a128\n" \
"v_accvgpr_read_b32 v249, a129\n" \
"v_accvgpr_read_b32 v250, a130\n" \
"v_accvgpr_read_b32 v251, a131\n" \
"s_waitcnt lgkmcnt(0)\n" \
"s_nop 0\n" \
"v_mfma_f32_16x16x32_bf16 a[128:131], v[152:155], v[56:59], 0\n" \
"v_accvgpr_read_b32 v180, a108\n" \
"v_accvgpr_read_b32 v181, a109\n" \
"v_accvgpr_read_b32 v182, a110\n" \
"v_accvgpr_read_b32 v183, a111\n" \
"v_cvt_pk_bf16_f32 v245, v246, v247\n" \
"v_cvt_pk_bf16_f32 v246, v180, v181\n" \
"v_cvt_pk_bf16_f32 v247, v182, v183\n" \
"v_cvt_pk_bf16_f32 v248, v248, v249\n" \
"v_mfma_f32_16x16x32_bf16 a[128:131], v[148:151], v[52:55], a[128:131]\n" \
"v_accvgpr_read_b32 v180, a132\n" \
"v_accvgpr_read_b32 v181, a133\n" \
"v_accvgpr_read_b32 v182, a134\n" \
"v_accvgpr_read_b32 v183, a135\n" \
"v_cvt_pk_bf16_f32 v249, v250, v251\n" \
"v_accvgpr_read_b32 v252, a160\n" \
"v_accvgpr_read_b32 v253, a161\n" \
"v_cvt_pk_bf16_f32 v250, v180, v181\n" \
"v_cvt_pk_bf16_f32 v251, v182, v183\n" \
"v_accvgpr_read_b32 v254, a162\n" \
"v_accvgpr_read_b32 v255, a163\n" \
"v_cvt_pk_bf16_f32 v252, v252, v253\n" \
"v_accvgpr_read_b32 v180, a164\n" \
"v_accvgpr_read_b32 v181, a165\n" \
"v_accvgpr_read_b32 v182, a166\n" \
"v_accvgpr_read_b32 v183, a167\n" \
"v_cvt_pk_bf16_f32 v253, v254, v255\n" \
"v_cvt_pk_bf16_f32 v254, v180, v181\n" \
"v_cvt_pk_bf16_f32 v255, v182, v183\n" \
"v_accvgpr_write_b32 a36, v252\n" \
"v_accvgpr_write_b32 a37, v253\n" \
"v_mfma_f32_16x16x32_bf16 a[132:135], v[156:159], v[52:55], 0\n" \
"v_accvgpr_write_b32 a38, v254\n" \
"v_accvgpr_write_b32 a39, v255\n" \
"v_accvgpr_read_b32 v252, a192\n" \
"v_accvgpr_read_b32 v253, a193\n" \
"v_accvgpr_read_b32 v254, a194\n" \
"v_accvgpr_read_b32 v255, a195\n" \
"v_cvt_pk_bf16_f32 v252, v252, v253\n" \
"v_accvgpr_read_b32 v180, a196\n" \
"v_accvgpr_read_b32 v181, a197\n" \
"v_accvgpr_read_b32 v182, a198\n" \
"v_accvgpr_read_b32 v183, a199\n" \
"v_cvt_pk_bf16_f32 v253, v254, v255\n" \
"v_cvt_pk_bf16_f32 v254, v180, v181\n" \
"v_cvt_pk_bf16_f32 v255, v182, v183\n" \
"v_accvgpr_write_b32 a64, v252\n" \
"v_accvgpr_write_b32 a65, v253\n" \
"v_mfma_f32_16x16x32_bf16 a[132:135], v[160:163], v[56:59], a[132:135]\n" \
"v_accvgpr_write_b32 a66, v254\n" \
"v_accvgpr_write_b32 a67, v255\n" \
"v_accvgpr_read_b32 v252, a224\n" \
"v_accvgpr_read_b32 v253, a225\n" \
"v_accvgpr_read_b32 v254, a226\n" \
"v_accvgpr_read_b32 v255, a227\n" \
"v_cvt_pk_bf16_f32 v252, v252, v253\n" \
"v_accvgpr_read_b32 v180, a228\n" \
"v_accvgpr_read_b32 v181, a229\n" \
"v_accvgpr_read_b32 v182, a230\n" \
"v_accvgpr_read_b32 v183, a231\n" \
"v_cvt_pk_bf16_f32 v253, v254, v255\n" \
"v_cvt_pk_bf16_f32 v254, v180, v181\n" \
"v_cvt_pk_bf16_f32 v255, v182, v183\n" \
"v_accvgpr_write_b32 a68, v252\n" \
"v_accvgpr_write_b32 a69, v253\n" \
"v_accvgpr_write_b32 a70, v254\n" \
"v_accvgpr_write_b32 a71, v255\n" \
"v_accvgpr_read_b32 v252, a136\n" \
"v_accvgpr_read_b32 v253, a137\n" \
"v_accvgpr_read_b32 v254, a138\n" \
"v_accvgpr_read_b32 v255, a139\n" \
"v_mfma_f32_16x16x32_bf16 a[136:139], v[168:171], v[56:59], 0\n" \
"ds_read_b128 v[84:87], v48 offset:0\n" \
"ds_read_b128 v[88:91], v49 offset:0\n" \
"ds_read_b128 v[92:95], v48 offset:2048\n" \
"v_mfma_f32_16x16x32_bf16 a[136:139], v[164:167], v[52:55], a[136:139]\n" \
"ds_read_b128 v[96:99], v49 offset:2048\n" \
"v_accvgpr_read_b32 v180, a140\n" \
"v_accvgpr_read_b32 v181, a141\n" \
"v_accvgpr_read_b32 v182, a142\n" \
"v_accvgpr_read_b32 v183, a143\n" \
"v_mfma_f32_16x16x32_bf16 a[140:143], v[172:175], v[52:55], 0\n" \
"ds_read_b128 v[100:103], v48 offset:4096\n" \
"ds_read_b128 v[104:107], v49 offset:4096\n" \
"s_add_u32 s89, s44, 0x1000\n" \
"buffer_store_dwordx4 v[196:199], v20, s[80:83], s89 offen offset:1024 sc1\n" \
"ds_read_b128 v[108:111], v48 offset:6144\n" \
"v_mfma_f32_16x16x32_bf16 a[140:143], v[176:179], v[56:59], a[140:143]\n" \
"v_cvt_pk_bf16_f32 v252, v252, v253\n" \
"v_cvt_pk_bf16_f32 v253, v254, v255\n" \
"v_mfma_f32_16x16x32_bf16 a[160:163], v[152:155], v[64:67], 0\n" \
"ds_read_b128 v[112:115], v49 offset:6144\n" \
"v_cvt_pk_bf16_f32 v254, v180, v181\n" \
"v_mfma_f32_16x16x32_bf16 a[160:163], v[148:151], v[60:63], a[160:163]\n" \
"v_cvt_pk_bf16_f32 v255, v182, v183\n" \
"v_accvgpr_write_b32 a96, v252\n" \
"v_mfma_f32_16x16x32_bf16 a[164:167], v[156:159], v[60:63], 0\n" \
"v_accvgpr_write_b32 a97, v253\n" \
"v_accvgpr_write_b32 a98, v254\n" \
"v_mfma_f32_16x16x32_bf16 a[164:167], v[160:163], v[64:67], a[164:167]\n" \
"v_accvgpr_write_b32 a99, v255\n" \
"v_accvgpr_read_b32 v252, a168\n" \
"v_accvgpr_read_b32 v253, a169\n" \
"v_accvgpr_read_b32 v254, a170\n" \
"v_accvgpr_read_b32 v255, a171\n" \
"v_mfma_f32_16x16x32_bf16 a[168:171], v[168:171], v[64:67], 0\n" \
"v_accvgpr_read_b32 v180, a172\n" \
"v_accvgpr_read_b32 v181, a173\n" \
"v_mfma_f32_16x16x32_bf16 a[168:171], v[164:167], v[60:63], a[168:171]\n" \
"v_accvgpr_read_b32 v182, a174\n" \
"v_accvgpr_read_b32 v183, a175\n" \
"v_mfma_f32_16x16x32_bf16 a[172:175], v[172:175], v[60:63], 0\n" \
"v_cvt_pk_bf16_f32 v252, v252, v253\n" \
"v_cvt_pk_bf16_f32 v253, v254, v255\n" \
"v_mfma_f32_16x16x32_bf16 a[172:175], v[176:179], v[64:67], a[172:175]\n" \
"v_cvt_pk_bf16_f32 v254, v180, v181\n" \
"v_cvt_pk_bf16_f32 v255, v182, v183\n" \
"v_mfma_f32_16x16x32_bf16 a[192:195], v[152:155], v[72:75], 0\n" \
"v_accvgpr_write_b32 a100, v252\n" \
"v_accvgpr_write_b32 a101, v253\n" \
"v_mfma_f32_16x16x32_bf16 a[192:195], v[148:151], v[68:71], a[192:195]\n" \
"v_accvgpr_write_b32 a102, v254\n" \
"v_accvgpr_write_b32 a103, v255\n" \
"s_add_u32 s62, s62, 0x80\n" \
"v_accvgpr_read_b32 v252, a200\n" \
"v_accvgpr_read_b32 v253, a201\n" \
"v_accvgpr_read_b32 v254, a202\n" \
"s_waitcnt vmcnt(4) lgkmcnt(0)\n" \
"s_barrier\n" \
"v_mfma_f32_16x16x32_bf16 a[196:199], v[156:159], v[68:71], 0\n" \
"v_accvgpr_read_b32 v255, a203\n" \
"s_add_u32 m0, s40, 0x0\n" \
"v_mfma_f32_16x16x32_bf16 a[196:199], v[160:163], v[72:75], a[196:199]\n" \
"s_cmp_lt_u32 s62, 0x480\n" \
"s_cselect_b32 s62, s62, 0\n" \
"v_mfma_f32_16x16x32_bf16 a[200:203], v[168:171], v[72:75], 0\n" \
"s_lshl_b32 s72, s62, 6\n" \
"s_add_u32 s72, s72, s37\n" \
"v_mfma_f32_16x16x32_bf16 a[200:203], v[164:167], v[68:71], a[200:203]\n" \
"s_add_u32 s85, s72, 0x0\n" \
"s_nop 0\n" \
"buffer_load_dwordx4 v20, s[80:83], s85 offen sc1 lds\n" \
"v_accvgpr_read_b32 v180, a204\n" \
"v_accvgpr_read_b32 v181, a205\n" \
"v_accvgpr_read_b32 v182, a206\n" \
"v_accvgpr_read_b32 v183, a207\n" \
"v_mfma_f32_16x16x32_bf16 a[204:207], v[172:175], v[68:71], 0\n" \
"s_add_u32 m0, s40, 0x1000\n" \
"s_add_u32 s84, s72, 0x1000\n" \
"v_mfma_f32_16x16x32_bf16 a[204:207], v[176:179], v[72:75], a[204:207]\n" \
"buffer_load_dwordx4 v20, s[80:83], s84 offen sc1 lds\n" \
"v_mfma_f32_16x16x32_bf16 a[224:227], v[152:155], v[80:83], 0\n" \
"v_cvt_pk_bf16_f32 v252, v252, v253\n" \
"v_cvt_pk_bf16_f32 v253, v254, v255\n" \
"v_mfma_f32_16x16x32_bf16 a[224:227], v[148:151], v[76:79], a[224:227]\n" \
"v_cvt_pk_bf16_f32 v254, v180, v181\n" \
"v_cvt_pk_bf16_f32 v255, v182, v183\n" \
"v_mfma_f32_16x16x32_bf16 a[228:231], v[156:159], v[76:79], 0\n" \
"v_accvgpr_write_b32 a8, v252\n" \
"v_accvgpr_write_b32 a9, v253\n" \
"v_mfma_f32_16x16x32_bf16 a[228:231], v[160:163], v[80:83], a[228:231]\n" \
"v_accvgpr_write_b32 a10, v254\n" \
"v_accvgpr_write_b32 a11, v255\n" \
"v_accvgpr_read_b32 v252, a232\n" \
"v_accvgpr_read_b32 v253, a233\n" \
"v_accvgpr_read_b32 v254, a234\n" \
"v_accvgpr_read_b32 v255, a235\n" \
"v_mfma_f32_16x16x32_bf16 a[232:235], v[168:171], v[80:83], 0\n" \
"s_add_u32 m0, s40, 0x3000\n" \
"s_add_u32 s85, s62, 0x0\n" \
"buffer_load_dwordx4 v21, s[76:79], s85 offen lds\n" \
"v_mfma_f32_16x16x32_bf16 a[232:235], v[164:167], v[76:79], a[232:235]\n" \
"v_accvgpr_read_b32 v180, a236\n" \
"v_accvgpr_read_b32 v181, a237\n" \
"v_accvgpr_read_b32 v182, a238\n" \
"v_accvgpr_read_b32 v183, a239\n" \
"v_mfma_f32_16x16x32_bf16 a[236:239], v[172:175], v[76:79], 0\n" \
"v_cvt_pk_bf16_f32 v252, v252, v253\n" \
"s_add_u32 m0, %[mA], 0x0\n" \
"v_mfma_f32_16x16x32_bf16 a[236:239], v[176:179], v[80:83], a[236:239]\n" \
"s_add_u32 s84, s62, 0x0\n" \
"s_nop 0\n" \
"v_cvt_pk_bf16_f32 v253, v254, v255\n" \
"v_cvt_pk_bf16_f32 v254, v180, v181\n" \
"v_cvt_pk_bf16_f32 v255, v182, v183\n" \
"v_accvgpr_write_b32 a12, v252\n" \
"v_accvgpr_write_b32 a13, v253\n" \
"v_accvgpr_write_b32 a14, v254\n" \
"v_accvgpr_write_b32 a15, v255\n" \
"v_accvgpr_read_b32 v252, a144\n" \
"v_accvgpr_read_b32 v253, a145\n" \
"v_accvgpr_read_b32 v254, a146\n" \
"v_accvgpr_read_b32 v255, a147\n" \
"v_mfma_f32_16x16x32_bf16 a[144:147], v[152:155], v[88:91], 0\n" \
"buffer_load_dwordx4 v24, s[76:79], s84 offen lds\n" \
"v_mfma_f32_16x16x32_bf16 a[144:147], v[148:151], v[84:87], a[144:147]\n" \
"v_cvt_pk_bf16_f32 v252, v252, v253\n" \
"v_accvgpr_read_b32 v180, a176\n" \
"v_accvgpr_read_b32 v181, a177\n" \
"v_accvgpr_read_b32 v182, a178\n" \
"v_accvgpr_read_b32 v183, a179\n" \
"v_cvt_pk_bf16_f32 v253, v254, v255\n" \
"v_cvt_pk_bf16_f32 v254, v180, v181\n" \
"v_cvt_pk_bf16_f32 v255, v182, v183\n" \
"v_accvgpr_write_b32 a40, v252\n" \
"v_accvgpr_write_b32 a41, v253\n" \
"v_accvgpr_write_b32 a42, v254\n" \
"v_accvgpr_write_b32 a43, v255\n" \
"v_accvgpr_read_b32 v252, a148\n" \
"v_accvgpr_read_b32 v253, a149\n" \
"v_accvgpr_read_b32 v254, a150\n" \
"v_accvgpr_read_b32 v255, a151\n" \
"v_mfma_f32_16x16x32_bf16 a[148:151], v[156:159], v[84:87], 0\n" \
"v_cvt_pk_bf16_f32 v252, v252, v253\n" \
"s_add_u32 m0, %[mA], 0x400\n" \
"v_mfma_f32_16x16x32_bf16 a[148:151], v[160:163], v[88:91], a[148:151]\n" \
"s_add_u32 s85, s62, 0x2400\n" \
"v_accvgpr_read_b32 v180, a180\n" \
"v_accvgpr_read_b32 v181, a181\n" \
"v_accvgpr_read_b32 v182, a182\n" \
"v_accvgpr_read_b32 v183, a183\n" \
"v_cvt_pk_bf16_f32 v253, v254, v255\n" \
"v_cvt_pk_bf16_f32 v254, v180, v181\n" \
"v_cvt_pk_bf16_f32 v255, v182, v183\n" \
"v_accvgpr_write_b32 a44, v252\n" \
"v_accvgpr_write_b32 a45, v253\n" \
"v_accvgpr_write_b32 a46, v254\n" \
"v_accvgpr_write_b32 a47, v255\n" \
"v_accvgpr_read_b32 v252, a152\n" \
"v_accvgpr_read_b32 v253, a153\n" \
"v_accvgpr_read_b32 v254, a154\n" \
"v_accvgpr_read_b32 v255, a155\n" \
"v_mfma_f32_16x16x32_bf16 a[152:155], v[168:171], v[88:91], 0\n" \
"buffer_load_dwordx4 v25, s[76:79], s85 offen lds\n" \
"v_mfma_f32_16x16x32_bf16 a[152:155], v[164:167], v[84:87], a[152:155]\n" \
"L_pf5:\n" \
"s_cbranch_execz L_pf9\n" \
"s_nop 0\n" \
"v_cvt_pk_bf16_f32 v252, v252, v253\n" \
"v_accvgpr_read_b32 v180, a184\n" \
"v_accvgpr_read_b32 v181, a185\n" \
"v_accvgpr_read_b32 v182, a186\n" \
"v_accvgpr_read_b32 v183, a187\n" \
"v_cvt_pk_bf16_f32 v253, v254, v255\n" \
"v_cvt_pk_bf16_f32 v254, v180, v181\n" \
"v_cvt_pk_bf16_f32 v255, v182, v183\n" \
"s_lshl_b32 s63, s62, 12\n" \
"v_accvgpr_write_b32 a72, v252\n" \
"v_accvgpr_write_b32 a73, v253\n" \
"v_accvgpr_write_b32 a74, v254\n" \
"v_accvgpr_write_b32 a75, v255\n" \
"v_accvgpr_read_b32 v252, a156\n" \
"v_accvgpr_read_b32 v253, a157\n" \
"v_accvgpr_read_b32 v254, a158\n" \
"v_accvgpr_read_b32 v255, a159\n" \
"v_mfma_f32_16x16x32_bf16 a[156:159], v[172:175], v[84:87], 0\n" \
"s_add_u32 m0, %[mB], 0x0\n" \
"s_add_u32 s84, s63, 0x0\n" \
"s_nop 0\n" \
"buffer_load_dwordx4 v26, s[68:71], s84 offen lds\n" \
"v_mfma_f32_16x16x32_bf16 a[156:159], v[176:179], v[88:91], a[156:159]\n" \
"s_add_u32 m0, %[mA], 0x800\n" \
"s_add_u32 s85, s62, 0x4800\n" \
"v_mfma_f32_16x16x32_bf16 a[176:179], v[152:155], v[96:99], 0\n" \
"v_accvgpr_read_b32 v180, a188\n" \
"v_accvgpr_read_b32 v181, a189\n" \
"v_mfma_f32_16x16x32_bf16 a[176:179], v[148:151], v[92:95], a[176:179]\n" \
"buffer_load_dwordx4 v24, s[76:79], s85 offen lds\n" \
"v_mfma_f32_16x16x32_bf16 a[180:183], v[156:159], v[92:95], 0\n" \
"s_add_u32 m0, %[mA], 0xc00\n" \
"s_add_u32 s84, s62, 0x6c00\n" \
"v_mfma_f32_16x16x32_bf16 a[180:183], v[160:163], v[96:99], a[180:183]\n" \
"v_accvgpr_read_b32 v182, a190\n" \
"v_accvgpr_read_b32 v183, a191\n" \
"v_mfma_f32_16x16x32_bf16 a[184:187], v[168:171], v[96:99], 0\n" \
"buffer_load_dwordx4 v25, s[76:79], s84 offen lds\n" \
"v_mfma_f32_16x16x32_bf16 a[184:187], v[164:167], v[92:95], a[184:187]\n" \
"v_cvt_pk_bf16_f32 v252, v252, v253\n" \
"v_cvt_pk_bf16_f32 v253, v254, v255\n" \
"v_mfma_f32_16x16x32_bf16 a[188:191], v[172:175], v[92:95], 0\n" \
"v_cvt_pk_bf16_f32 v254, v180, v181\n" \
"v_cvt_pk_bf16_f32 v255, v182, v183\n" \
"v_mfma_f32_16x16x32_bf16 a[188:191], v[176:179], v[96:99], a[188:191]\n" \
"v_accvgpr_write_b32 a76, v252\n" \
"v_accvgpr_write_b32 a77, v253\n" \
"v_accvgpr_write_b32 a78, v254\n" \
"v_accvgpr_write_b32 a79, v255\n" \
"v_accvgpr_read_b32 v252, a208\n" \
"v_accvgpr_read_b32 v253, a209\n" \
"v_accvgpr_read_b32 v254, a210\n" \
"v_accvgpr_read_b32 v255, a211\n" \
"v_mfma_f32_16x16x32_bf16 a[208:211], v[152:155], v[104:107], 0\n" \
"v_cvt_pk_bf16_f32 v252, v252, v253\n" \
"s_add_u32 m0, %[mB], 0x400\n" \
"v_mfma_f32_16x16x32_bf16 a[208:211], v[148:151], v[100:103], a[208:211]\n" \
"s_add_u32 s85, s63, 0x8000\n" \
"v_accvgpr_read_b32 v180, a240\n" \
"v_accvgpr_read_b32 v181, a241\n" \
"v_accvgpr_read_b32 v182, a242\n" \
"v_accvgpr_read_b32 v183, a243\n" \
"v_cvt_pk_bf16_f32 v253, v254, v255\n" \
"v_cvt_pk_bf16_f32 v254, v180, v181\n" \
"v_cvt_pk_bf16_f32 v255, v182, v183\n" \
"v_accvgpr_write_b32 a104, v252\n" \
"v_accvgpr_write_b32 a105, v253\n" \
"v_accvgpr_write_b32 a106, v254\n" \
"v_accvgpr_write_b32 a107, v255\n" \
"v_accvgpr_read_b32 v252, a212\n" \
"v_accvgpr_read_b32 v253, a213\n" \
"v_accvgpr_read_b32 v254, a214\n" \
"v_accvgpr_read_b32 v255, a215\n" \
"v_mfma_f32_16x16x32_bf16 a[212:215], v[156:159], v[100:103], 0\n" \
"buffer_load_dwordx4 v26, s[68:71], s85 offen lds\n" \
"ds_read_b64_tr_b16 v[140:141], v39 offset:32768\n" \
"ds_read_b64_tr_b16 v[142:143], v39 offset:33728\n" \
"ds_read_b128 v[84:87], v22 offset:32768\n" \
"ds_read_b128 v[88:91], v23 offset:32768\n" \
"v_mfma_f32_16x16x32_bf16 a[212:215], v[160:163], v[104:107], a[212:215]\n" \
"ds_read_b64_tr_b16 v[128:129], v37 offset:36864\n" \
"ds_read_b64_tr_b16 v[130:131], v37 offset:37376\n" \
"v_accvgpr_read_b32 v184, a216\n" \
"v_accvgpr_read_b32 v185, a217\n" \
"v_accvgpr_read_b32 v186, a218\n" \
"v_accvgpr_read_b32 v187, a219\n" \
"v_mfma_f32_16x16x32_bf16 a[216:219], v[168:171], v[104:107], 0\n" \
"s_add_u32 m0, %[mA], 0x1000\n" \
"s_add_u32 s84, s62, 0x9000\n" \
"buffer_load_dwordx4 v24, s[76:79], s84 offen lds\n" \
"ds_read_b64_tr_b16 v[124:125], v37 offset:32768\n" \
"ds_read_b64_tr_b16 v[126:127], v37 offset:33280\n" \
"v_mfma_f32_16x16x32_bf16 a[216:219], v[164:167], v[100:103], a[216:219]\n" \
"v_cvt_pk_bf16_f32 v252, v252, v253\n" \
"v_accvgpr_read_b32 v180, a244\n" \
"v_accvgpr_read_b32 v181, a245\n" \
"v_accvgpr_read_b32 v182, a246\n" \
"v_accvgpr_read_b32 v183, a247\n" \
"v_cvt_pk_bf16_f32 v253, v254, v255\n" \
"v_cvt_pk_bf16_f32 v254, v180, v181\n" \
"v_cvt_pk_bf16_f32 v255, v182, v183\n" \
"v_accvgpr_write_b32 a108, v252\n" \
"v_accvgpr_write_b32 a109, v253\n" \
"v_accvgpr_write_b32 a110, v254\n" \
"v_accvgpr_write_b32 a111, v255\n" \
"v_accvgpr_read_b32 v252, a220\n" \
"v_accvgpr_read_b32 v253, a221\n" \
"v_accvgpr_read_b32 v254, a222\n" \
"v_accvgpr_read_b32 v255, a223\n" \
"v_mfma_f32_16x16x32_bf16 a[220:223], v[172:175], v[100:103], 0\n" \
"ds_read_b64_tr_b16 v[132:133], v38 offset:32768\n" \
"ds_read_b64_tr_b16 v[134:135], v38 offset:33280\n" \
"ds_read_b64_tr_b16 v[136:137], v38 offset:36864\n" \
"ds_read_b64_tr_b16 v[138:139], v38 offset:37376\n" \
"v_mfma_f32_16x16x32_bf16 a[220:223], v[176:179], v[104:107], a[220:223]\n" \
"ds_read_b128 v[52:55], v28 offset:32768\n" \
"ds_read_b128 v[56:59], v29 offset:32768\n" \
"v_mfma_f32_16x16x32_bf16 a[240:243], v[152:155], v[112:115], 0\n" \
"s_add_u32 m0, %[mA], 0x1400\n" \
"s_add_u32 s85, s62, 0xb400\n" \
"buffer_load_dwordx4 v25, s[76:79], s85 offen lds\n" \
"v_mfma_f32_16x16x32_bf16 a[240:243], v[148:151], v[108:111], a[240:243]\n" \
"ds_read_b64_tr_b16 v[116:117], v36 offset:32768\n" \
"ds_read_b64_tr_b16 v[118:119], v36 offset:33280\n" \
"v_mfma_f32_16x16x32_bf16 a[244:247], v[156:159], v[108:111], 0\n" \
"s_add_u32 m0, %[mB], 0x800\n" \
"s_add_u32 s84, s63, 0x10000\n" \
"v_mfma_f32_16x16x32_bf16 a[244:247], v[160:163], v[112:115], a[244:247]\n" \
"buffer_load_dwordx4 v27, s[68:71], s84 offen lds\n" \
"ds_read_b64_tr_b16 v[120:121], v36 offset:36864\n" \
"ds_read_b64_tr_b16 v[122:123], v36 offset:37376\n" \
"v_accvgpr_read_b32 v180, a248\n" \
"v_accvgpr_read_b32 v181, a249\n" \
"v_accvgpr_read_b32 v182, a250\n" \
"v_accvgpr_read_b32 v183, a251\n" \
"v_mfma_f32_16x16x32_bf16 a[248:251], v[168:171], v[112:115], 0\n" \
"ds_read_b128 v[60:63], v28 offset:34816\n" \
"v_cvt_pk_bf16_f32 v184, v184, v185\n" \
"v_mfma_f32_16x16x32_bf16 a[248:251], v[164:167], v[108:111], a[248:251]\n" \
"v_cvt_pk_bf16_f32 v185, v186, v187\n" \
"v_cvt_pk_bf16_f32 v186, v180, v181\n" \
"v_cvt_pk_bf16_f32 v187, v182, v183\n" \
"v_accvgpr_read_b32 v180, a252\n" \
"v_accvgpr_read_b32 v181, a253\n" \
"v_accvgpr_read_b32 v182, a254\n" \
"v_accvgpr_read_b32 v183, a255\n" \
"v_mfma_f32_16x16x32_bf16 a[252:255], v[172:175], v[108:111], 0\n" \
"ds_read_b128 v[64:67], v29 offset:34816\n" \
"s_add_u32 m0, %[mA], 0x1800\n" \
"v_mfma_f32_16x16x32_bf16 a[252:255], v[176:179], v[112:115], a[252:255]\n" \
"s_add_u32 s85, s62, 0xd800\n" \
"buffer_load_dwordx4 v24, s[76:79], s85 offen lds\n" \
"s_waitcnt lgkmcnt(12)\n" \
"v_smfmac_f32_16x16x64_bf16 a[16:19], v[140:143], v[84:91], v50\n" \
"v_mfma_f32_16x16x32_bf16 a[48:51], v[128:131], v[88:91], a[48:51]\n" \
"s_add_u32 m0, %[mA], 0x1c00\n" \
"s_add_u32 s85, s62, 0xfc00\n" \
"v_mfma_f32_16x16x32_bf16 a[48:51], v[124:127], v[84:87], a[48:51]\n" \
"ds_read_b128 v[68:71], v28 offset:36864\n" \
"s_waitcnt lgkmcnt(8)\n" \
"v_mfma_f32_16x16x32_bf16 a[80:83], v[84:87], v[132:135], a[80:83]\n" \
"buffer_load_dwordx4 v25, s[76:79], s85 offen lds\n" \
"v_mfma_f32_16x16x32_bf16 a[80:83], v[88:91], v[136:139], a[80:83]\n" \
"ds_read_b128 v[72:75], v29 offset:36864\n" \
"s_waitcnt lgkmcnt(6)\n" \
"v_smfmac_f32_16x16x64_bf16 a[112:115], v[140:143], v[52:59], v50\n" \
"v_mfma_f32_16x16x32_bf16 a[20:23], v[128:131], v[56:59], a[20:23]\n" \
"s_add_u32 m0, %[mB], 0xc00\n" \
"s_add_u32 s84, s63, 0x18000\n" \
"buffer_load_dwordx4 v27, s[68:71], s84 offen lds\n" \
"v_mfma_f32_16x16x32_bf16 a[20:23], v[124:127], v[52:55], a[20:23]\n" \
"ds_read_b128 v[76:79], v28 offset:38912\n" \
"ds_read_b128 v[80:83], v29 offset:38912\n" \
"v_mfma_f32_16x16x32_bf16 a[52:55], v[116:119], v[52:55], a[52:55]\n" \
"s_add_u32 s88, s44, 0x0\n" \
"s_waitcnt lgkmcnt(5)\n" \
"v_mfma_f32_16x16x32_bf16 a[52:55], v[120:123], v[56:59], a[52:55]\n" \
"buffer_store_dwordx4 v[200:203], v20, s[80:83], s88 offen offset:2048 sc1\n" \
"v_mfma_f32_16x16x32_bf16 a[84:87], v[56:59], v[136:139], a[84:87]\n" \
"ds_read_b64_tr_b16 v[152:153], v32 offset:40960\n" \
"ds_read_b64_tr_b16 v[154:155], v32 offset:41984\n" \
"v_mfma_f32_16x16x32_bf16 a[84:87], v[52:55], v[132:135], a[84:87]\n" \
"ds_read_b64_tr_b16 v[148:149], v32 offset:32768\n" \
"s_waitcnt lgkmcnt(7)\n" \
"v_smfmac_f32_16x16x64_bf16 a[116:119], v[140:143], v[60:67], v50\n" \
"v_mfma_f32_16x16x32_bf16 a[24:27], v[124:127], v[60:63], a[24:27]\n" \
"ds_read_b64_tr_b16 v[150:151], v32 offset:33792\n" \
"ds_read_b64_tr_b16 v[156:157], v33 offset:32768\n" \
"v_mfma_f32_16x16x32_bf16 a[24:27], v[128:131], v[64:67], a[24:27]\n" \
"ds_read_b64_tr_b16 v[158:159], v33 offset:33792\n" \
"ds_read_b64_tr_b16 v[160:161], v33 offset:40960\n" \
"v_mfma_f32_16x16x32_bf16 a[56:59], v[120:123], v[64:67], a[56:59]\n" \
"ds_read_b64_tr_b16 v[162:163], v33 offset:41984\n" \
"s_add_u32 s89, s44, 0x1000\n" \
"v_mfma_f32_16x16x32_bf16 a[56:59], v[116:119], v[60:63], a[56:59]\n" \
"buffer_store_dwordx4 v[204:207], v20, s[80:83], s89 offen offset:2048 sc1\n" \
"v_mfma_f32_16x16x32_bf16 a[88:91], v[60:63], v[132:135], a[88:91]\n" \
"ds_read_b64_tr_b16 v[168:169], v34 offset:40960\n" \
"ds_read_b64_tr_b16 v[170:171], v34 offset:41984\n" \
"v_mfma_f32_16x16x32_bf16 a[88:91], v[64:67], v[136:139], a[88:91]\n" \
"ds_read_b64_tr_b16 v[164:165], v34 offset:32768\n" \
"s_waitcnt lgkmcnt(13)\n" \
"v_smfmac_f32_16x16x64_bf16 a[120:123], v[140:143], v[68:75], v50\n" \
"v_mfma_f32_16x16x32_bf16 a[28:31], v[128:131], v[72:75], a[28:31]\n" \
"ds_read_b64_tr_b16 v[166:167], v34 offset:33792\n" \
"ds_read_b64_tr_b16 v[172:173], v35 offset:32768\n" \
"v_mfma_f32_16x16x32_bf16 a[28:31], v[124:127], v[68:71], a[28:31]\n" \
"ds_read_b64_tr_b16 v[174:175], v35 offset:33792\n" \
"ds_read_b64_tr_b16 v[176:177], v35 offset:40960\n" \
"v_mfma_f32_16x16x32_bf16 a[60:63], v[116:119], v[68:71], a[60:63]\n" \
"ds_read_b64_tr_b16 v[178:179], v35 offset:41984\n" \
"s_add_u32 s88, s44, 0x0\n" \
"v_mfma_f32_16x16x32_bf16 a[60:63], v[120:123], v[72:75], a[60:63]\n" \
"buffer_store_dwordx4 v[208:211], v20, s[80:83], s88 offen offset:3072 sc1\n" \
"v_mfma_f32_16x16x32_bf16 a[92:95], v[72:75], v[136:139], a[92:95]\n" \
"ds_read_b128 v[84:87], v48 offset:32768\n" \
"ds_read_b128 v[88:91], v49 offset:32768\n" \
"v_mfma_f32_16x16x32_bf16 a[92:95], v[68:71], v[132:135], a[92:95]\n" \
"ds_read_b128 v[92:95], v48 offset:34816\n" \
"s_waitcnt lgkmcnt(15)\n" \
"s_nop 0\n" \
"v_smfmac_f32_16x16x64_bf16 a[124:127], v[140:143], v[76:83], v50\n" \
"v_mfma_f32_16x16x32_bf16 a[0:3], v[124:127], v[76:79], a[0:3]\n" \
"ds_read_b128 v[96:99], v49 offset:34816\n" \
"ds_read_b128 v[100:103], v48 offset:36864\n" \
"v_mfma_f32_16x16x32_bf16 a[0:3], v[128:131], v[80:83], a[0:3]\n" \
"ds_read_b128 v[104:107], v49 offset:36864\n" \
"ds_read_b128 v[108:111], v48 offset:38912\n" \
"v_mfma_f32_16x16x32_bf16 a[4:7], v[120:123], v[80:83], a[4:7]\n" \
"s_add_u32 s62, s62, 0x80\n" \
"ds_read_b128 v[112:115], v49 offset:38912\n" \
"v_mfma_f32_16x16x32_bf16 a[4:7], v[116:119], v[76:79], a[4:7]\n" \
"s_cmp_lt_u32 s62, 0x480\n" \
"s_cselect_b32 s62, s62, 0\n" \
"v_mfma_f32_16x16x32_bf16 a[32:35], v[76:79], v[132:135], a[32:35]\n" \
"s_lshl_b32 s72, s62, 6\n" \
"s_add_u32 s72, s72, s37\n" \
"v_mfma_f32_16x16x32_bf16 a[32:35], v[80:83], v[136:139], a[32:35]\n" \
"s_lshl_b32 s63, s62, 12\n" \
"v_cvt_pk_bf16_f32 v252, v252, v253\n" \
"v_mfma_f32_16x16x32_bf16 a[128:131], v[152:155], v[56:59], a[128:131]\n" \
"v_cvt_pk_bf16_f32 v253, v254, v255\n" \
"v_cvt_pk_bf16_f32 v254, v180, v181\n" \
"v_mfma_f32_16x16x32_bf16 a[128:131], v[148:151], v[52:55], a[128:131]\n" \
"v_cvt_pk_bf16_f32 v255, v182, v183\n" \
"s_waitcnt lgkmcnt(11)\n" \
"v_mfma_f32_16x16x32_bf16 a[132:135], v[156:159], v[52:55], a[132:135]\n" \
"v_mfma_f32_16x16x32_bf16 a[132:135], v[160:163], v[56:59], a[132:135]\n" \
"v_mfma_f32_16x16x32_bf16 a[136:139], v[168:171], v[56:59], a[136:139]\n" \
"v_mfma_f32_16x16x32_bf16 a[136:139], v[164:167], v[52:55], a[136:139]\n" \
"s_waitcnt lgkmcnt(7)\n" \
"v_mfma_f32_16x16x32_bf16 a[140:143], v[172:175], v[52:55], a[140:143]\n" \
"v_mfma_f32_16x16x32_bf16 a[140:143], v[176:179], v[56:59], a[140:143]\n" \
"v_mfma_f32_16x16x32_bf16 a[160:163], v[152:155], v[64:67], a[160:163]\n" \
"v_mfma_f32_16x16x32_bf16 a[160:163], v[148:151], v[60:63], a[160:163]\n" \
"v_mfma_f32_16x16x32_bf16 a[164:167], v[156:159], v[60:63], a[164:167]\n" \
"v_mfma_f32_16x16x32_bf16 a[164:167], v[160:163], v[64:67], a[164:167]\n" \
"v_mfma_f32_16x16x32_bf16 a[168:171], v[168:171], v[64:67], a[168:171]\n" \
"v_mfma_f32_16x16x32_bf16 a[168:171], v[164:167], v[60:63], a[168:171]\n" \
"v_mfma_f32_16x16x32_bf16 a[172:175], v[172:175], v[60:63], a[172:175]\n" \
"v_mfma_f32_16x16x32_bf16 a[172:175], v[176:179], v[64:67], a[172:175]\n" \
"v_mfma_f32_16x16x32_bf16 a[192:195], v[152:155], v[72:75], a[192:195]\n" \
"v_mfma_f32_16x16x32_bf16 a[192:195], v[148:151], v[68:71], a[192:195]\n" \
"s_waitcnt vmcnt(3) lgkmcnt(0)\n" \
"s_barrier\n" \
"v_mfma_f32_16x16x32_bf16 a[196:199], v[156:159], v[68:71], a[196:199]\n" \
"s_add_u32 m0, s40, 0x8000\n" \
"s_add_u32 s85, s72, 0x0\n" \
"s_nop 0\n" \
"v_mfma_f32_16x16x32_bf16 a[196:199], v[160:163], v[72:75], a[196:199]\n" \
"buffer_load_dwordx4 v20, s[80:83], s85 offen sc1 lds\n" \
"v_mfma_f32_16x16x32_bf16 a[200:203], v[168:171], v[72:75], a[200:203]\n" \
"s_add_u32 m0, s40, 0x9000\n" \
"s_add_u32 s84, s72, 0x1000\n" \
"v_mfma_f32_16x16x32_bf16 a[200:203], v[164:167], v[68:71], a[200:203]\n" \
"ds_read_b64_tr_b16 v[140:141], v39 offset:0\n" \
"ds_read_b64_tr_b16 v[142:143], v39 offset:960\n" \
"v_mfma_f32_16x16x32_bf16 a[204:207], v[172:175], v[68:71], a[204:207]\n" \
"buffer_load_dwordx4 v20, s[80:83], s84 offen sc1 lds\n" \
"v_mfma_f32_16x16x32_bf16 a[204:207], v[176:179], v[72:75], a[204:207]\n" \
"ds_read_b64_tr_b16 v[128:129], v37 offset:4096\n" \
"ds_read_b64_tr_b16 v[130:131], v37 offset:4608\n" \
"v_mfma_f32_16x16x32_bf16 a[224:227], v[152:155], v[80:83], a[224:227]\n" \
"s_add_u32 m0, s40, 0xb000\n" \
"s_add_u32 s85, s62, 0x0\n" \
"v_mfma_f32_16x16x32_bf16 a[224:227], v[148:151], v[76:79], a[224:227]\n" \
"buffer_load_dwordx4 v21, s[76:79], s85 offen lds\n" \
"v_mfma_f32_16x16x32_bf16 a[228:231], v[156:159], v[76:79], a[228:231]\n" \
"ds_read_b64_tr_b16 v[124:125], v37 offset:0\n" \
"ds_read_b64_tr_b16 v[126:127], v37 offset:512\n" \
"v_mfma_f32_16x16x32_bf16 a[228:231], v[160:163], v[80:83], a[228:231]\n" \
"s_add_u32 m0, %[mA], 0x8000\n" \
"s_add_u32 s84, s62, 0x0\n" \
"v_mfma_f32_16x16x32_bf16 a[232:235], v[168:171], v[80:83], a[232:235]\n" \
"buffer_load_dwordx4 v24, s[76:79], s84 offen lds\n" \
"v_mfma_f32_16x16x32_bf16 a[232:235], v[164:167], v[76:79], a[232:235]\n" \
"ds_read_b64_tr_b16 v[132:133], v38 offset:0\n" \
"ds_read_b64_tr_b16 v[134:135], v38 offset:512\n" \
"v_mfma_f32_16x16x32_bf16 a[236:239], v[172:175], v[76:79], a[236:239]\n" \
"ds_read_b64_tr_b16 v[136:137], v38 offset:4096\n" \
"ds_read_b64_tr_b16 v[138:139], v38 offset:4608\n" \
"v_mfma_f32_16x16x32_bf16 a[236:239], v[176:179], v[80:83], a[236:239]\n" \
"s_add_u32 m0, %[mA], 0x8400\n" \
"s_add_u32 s85, s62, 0x2400\n" \
"v_mfma_f32_16x16x32_bf16 a[144:147], v[152:155], v[88:91], a[144:147]\n" \
"buffer_load_dwordx4 v25, s[76:79], s85 offen lds\n" \
"v_mfma_f32_16x16x32_bf16 a[144:147], v[148:151], v[84:87], a[144:147]\n" \
"ds_read_b128 v[52:55], v28 offset:0\n" \
"ds_read_b128 v[56:59], v29 offset:0\n" \
"v_mfma_f32_16x16x32_bf16 a[148:151], v[156:159], v[84:87], a[148:151]\n" \
"s_add_u32 m0, %[mB], 0x8000\n" \
"s_add_u32 s84, s63, 0x0\n" \
"s_nop 0\n" \
"v_mfma_f32_16x16x32_bf16 a[148:151], v[160:163], v[88:91], a[148:151]\n" \
"buffer_load_dwordx4 v26, s[68:71], s84 offen lds\n" \
"v_mfma_f32_16x16x32_bf16 a[152:155], v[168:171], v[88:91], a[152:155]\n" \
"ds_read_b64_tr_b16 v[116:117], v36 offset:0\n" \
"ds_read_b64_tr_b16 v[118:119], v36 offset:512\n" \
"v_mfma_f32_16x16x32_bf16 a[152:155], v[164:167], v[84:87], a[152:155]\n" \
"ds_read_b64_tr_b16 v[120:121], v36 offset:4096\n" \
"ds_read_b64_tr_b16 v[122:123], v36 offset:4608\n" \
"v_mfma_f32_16x16x32_bf16 a[156:159], v[172:175], v[84:87], a[156:159]\n" \
"s_add_u32 m0, %[mA], 0x8800\n" \
"s_add_u32 s85, s62, 0x4800\n" \
"v_mfma_f32_16x16x32_bf16 a[156:159], v[176:179], v[88:91], a[156:159]\n" \
"buffer_load_dwordx4 v24, s[76:79], s85 offen lds\n" \
"v_mfma_f32_16x16x32_bf16 a[176:179], v[152:155], v[96:99], a[176:179]\n" \
"ds_read_b128 v[84:87], v22 offset:0\n" \
"ds_read_b128 v[88:91], v23 offset:0\n" \
"v_mfma_f32_16x16x32_bf16 a[176:179], v[148:151], v[92:95], a[176:179]\n" \
"s_add_u32 m0, %[mA], 0x8c00\n" \
"s_add_u32 s84, s62, 0x6c00\n" \
"v_mfma_f32_16x16x32_bf16 a[180:183], v[156:159], v[92:95], a[180:183]\n" \
"buffer_load_dwordx4 v25, s[76:79], s84 offen lds\n" \
"v_mfma_f32_16x16x32_bf16 a[180:183], v[160:163], v[96:99], a[180:183]\n" \
"ds_read_b128 v[60:63], v28 offset:2048\n" \
"ds_read_b128 v[64:67], v29 offset:2048\n" \
"v_mfma_f32_16x16x32_bf16 a[184:187], v[168:171], v[96:99], a[184:187]\n" \
"ds_read_b128 v[68:71], v28 offset:4096\n" \
"ds_read_b128 v[72:75], v29 offset:4096\n" \
"v_mfma_f32_16x16x32_bf16 a[184:187], v[164:167], v[92:95], a[184:187]\n" \
"s_add_u32 m0, %[mB], 0x8400\n" \
"s_add_u32 s85, s63, 0x8000\n" \
"v_mfma_f32_16x16x32_bf16 a[188:191], v[172:175], v[92:95], a[188:191]\n" \
"buffer_load_dwordx4 v26, s[68:71], s85 offen lds\n" \
"v_mfma_f32_16x16x32_bf16 a[188:191], v[176:179], v[96:99], a[188:191]\n" \
"ds_read_b128 v[76:79], v28 offset:6144\n" \
"ds_read_b128 v[80:83], v29 offset:6144\n" \
"v_mfma_f32_16x16x32_bf16 a[208:211], v[152:155], v[104:107], a[208:211]\n" \
"s_add_u32 m0, %[mA], 0x9000\n" \
"s_add_u32 s84, s62, 0x9000\n" \
"v_mfma_f32_16x16x32_bf16 a[208:211], v[148:151], v[100:103], a[208:211]\n" \
"buffer_load_dwordx4 v24, s[76:79], s84 offen lds\n" \
"v_mfma_f32_16x16x32_bf16 a[212:215], v[156:159], v[100:103], a[212:215]\n" \
"ds_read_b128 v[92:95], v48 offset:2048\n" \
"ds_read_b128 v[96:99], v49 offset:2048\n" \
"v_mfma_f32_16x16x32_bf16 a[212:215], v[160:163], v[104:107], a[212:215]\n" \
"v_mfma_f32_16x16x32_bf16 a[216:219], v[168:171], v[104:107], a[216:219]\n" \
"s_add_u32 m0, %[mA], 0x9400\n" \
"s_add_u32 s85, s62, 0xb400\n" \
"v_mfma_f32_16x16x32_bf16 a[216:219], v[164:167], v[100:103], a[216:219]\n" \
"buffer_load_dwordx4 v25, s[76:79], s85 offen lds\n" \
"v_mfma_f32_16x16x32_bf16 a[220:223], v[172:175], v[100:103], a[220:223]\n" \
"ds_read_b128 v[100:103], v48 offset:4096\n" \
"v_mfma_f32_16x16x32_bf16 a[220:223], v[176:179], v[104:107], a[220:223]\n" \
"s_add_u32 m0, %[mB], 0x8800\n" \
"s_add_u32 s84, s63, 0x10000\n" \
"v_mfma_f32_16x16x32_bf16 a[240:243], v[152:155], v[112:115], a[240:243]\n" \
"buffer_load_dwordx4 v27, s[68:71], s84 offen lds\n" \
"v_mfma_f32_16x16x32_bf16 a[240:243], v[148:151], v[108:111], a[240:243]\n" \
"ds_read_b64_tr_b16 v[152:153], v32 offset:8192\n" \
"ds_read_b64_tr_b16 v[154:155], v32 offset:9216\n" \
"v_mfma_f32_16x16x32_bf16 a[244:247], v[156:159], v[108:111], a[244:247]\n" \
"ds_read_b64_tr_b16 v[148:149], v32 offset:0\n" \
"ds_read_b64_tr_b16 v[150:151], v32 offset:1024\n" \
"v_mfma_f32_16x16x32_bf16 a[244:247], v[160:163], v[112:115], a[244:247]\n" \
"s_add_u32 m0, %[mA], 0x9800\n" \
"s_add_u32 s85, s62, 0xd800\n" \
"v_mfma_f32_16x16x32_bf16 a[248:251], v[168:171], v[112:115], a[248:251]\n" \
"buffer_load_dwordx4 v24, s[76:79], s85 offen lds\n" \
"v_mfma_f32_16x16x32_bf16 a[248:251], v[164:167], v[108:111], a[248:251]\n" \
"ds_read_b64_tr_b16 v[156:157], v33 offset:0\n" \
"ds_read_b64_tr_b16 v[158:159], v33 offset:1024\n" \
"v_mfma_f32_16x16x32_bf16 a[252:255], v[172:175], v[108:111], a[252:255]\n" \
"ds_read_b64_tr_b16 v[160:161], v33 offset:8192\n" \
"ds_read_b64_tr_b16 v[162:163], v33 offset:9216\n" \
"v_mfma_f32_16x16x32_bf16 a[252:255], v[176:179], v[112:115], a[252:255]\n" \
"s_add_u32 m0, %[mA], 0x9c00\n" \
"s_waitcnt lgkmcnt(11)\n" \
"s_nop 0\n" \
"v_smfmac_f32_16x16x64_bf16 a[16:19], v[140:143], v[84:91], v50\n" \
"v_mfma_f32_16x16x32_bf16 a[48:51], v[128:131], v[88:91], a[48:51]\n" \
"s_add_u32 s85, s62, 0xfc00\n" \
"ds_read_b64_tr_b16 v[168:169], v34 offset:8192\n" \
"v_mfma_f32_16x16x32_bf16 a[48:51], v[124:127], v[84:87], a[48:51]\n" \
"buffer_load_dwordx4 v25, s[76:79], s85 offen lds\n" \
"v_mfma_f32_16x16x32_bf16 a[80:83], v[84:87], v[132:135], a[80:83]\n" \
"s_add_u32 m0, %[mB], 0x8c00\n" \
"s_add_u32 s84, s63, 0x18000\n" \
"v_mfma_f32_16x16x32_bf16 a[80:83], v[88:91], v[136:139], a[80:83]\n" \
"ds_read_b64_tr_b16 v[170:171], v34 offset:9216\n" \
"ds_read_b64_tr_b16 v[164:165], v34 offset:0\n" \
"v_smfmac_f32_16x16x64_bf16 a[112:115], v[140:143], v[52:59], v50\n" \
"v_mfma_f32_16x16x32_bf16 a[20:23], v[128:131], v[56:59], a[20:23]\n" \
"buffer_load_dwordx4 v27, s[68:71], s84 offen lds\n" \
"v_mfma_f32_16x16x32_bf16 a[20:23], v[124:127], v[52:55], a[20:23]\n" \
"s_add_u32 s89, s44, 0x1000\n" \
"ds_read_b64_tr_b16 v[166:167], v34 offset:1024\n" \
"v_mfma_f32_16x16x32_bf16 a[52:55], v[116:119], v[52:55], a[52:55]\n" \
"ds_read_b64_tr_b16 v[172:173], v35 offset:0\n" \
"ds_read_b64_tr_b16 v[174:175], v35 offset:1024\n" \
"v_mfma_f32_16x16x32_bf16 a[52:55], v[120:123], v[56:59], a[52:55]\n" \
"buffer_store_dwordx4 v[212:215], v20, s[80:83], s89 offen offset:3072 sc1\n" \
"v_mfma_f32_16x16x32_bf16 a[84:87], v[56:59], v[136:139], a[84:87]\n" \
"ds_read_b64_tr_b16 v[176:177], v35 offset:8192\n" \
"ds_read_b64_tr_b16 v[178:179], v35 offset:9216\n" \
"v_mfma_f32_16x16x32_bf16 a[84:87], v[52:55], v[132:135], a[84:87]\n" \
"ds_read_b128 v[84:87], v48 offset:0\n" \
"ds_read_b128 v[88:91], v49 offset:0\n" \
"v_smfmac_f32_16x16x64_bf16 a[116:119], v[140:143], v[60:67], v50\n" \
"v_mfma_f32_16x16x32_bf16 a[24:27], v[124:127], v[60:63], a[24:27]\n" \
"ds_read_b128 v[104:107], v49 offset:4096\n" \
"ds_read_b128 v[108:111], v48 offset:6144\n" \
"v_mfma_f32_16x16x32_bf16 a[24:27], v[128:131], v[64:67], a[24:27]\n" \
"s_add_u32 s88, s42, 0x0\n" \
"ds_read_b128 v[112:115], v49 offset:6144\n" \
"v_mfma_f32_16x16x32_bf16 a[56:59], v[120:123], v[64:67], a[56:59]\n" \
"buffer_store_dwordx4 v[216:219], v20, s[80:83], s88 offen offset:0 sc1\n" \
"v_mfma_f32_16x16x32_bf16 a[56:59], v[116:119], v[60:63], a[56:59]\n" \
"s_add_u32 s62, s62, 0x80\n" \
"s_add_u32 s94, %[b], %[w]\n" \
"v_mfma_f32_16x16x32_bf16 a[88:91], v[60:63], v[132:135], a[88:91]\n" \
"s_cmp_lt_u32 s62, 0x480\n" \
"s_cselect_b32 s62, s62, 0\n" \
"v_mfma_f32_16x16x32_bf16 a[88:91], v[64:67], v[136:139], a[88:91]\n" \
"s_lshl_b32 s72, s62, 6\n" \
"s_add_u32 s72, s72, s37\n" \
"v_smfmac_f32_16x16x64_bf16 a[120:123], v[140:143], v[68:75], v50\n" \
"v_mfma_f32_16x16x32_bf16 a[28:31], v[128:131], v[72:75], a[28:31]\n" \
"s_lshl_b32 s63, s62, 12\n" \
"s_and_b32 s96, 7, %[b]\n" \
"v_mfma_f32_16x16x32_bf16 a[28:31], v[124:127], v[68:71], a[28:31]\n" \
"s_cmp_lg_u32 s94, 0\n" \
"s_cselect_b32 s95, 0x40000000, 0x5bc0000\n" \
"v_mfma_f32_16x16x32_bf16 a[60:63], v[116:119], v[68:71], a[60:63]\n" \
"s_add_u32 s89, s42, 0x1000\n" \
"s_mul_i32 s97, 0x48, s96\n" \
"v_mfma_f32_16x16x32_bf16 a[60:63], v[120:123], v[72:75], a[60:63]\n" \
"buffer_store_dwordx4 v[220:223], v20, s[80:83], s89 offen offset:0 sc1\n" \
"v_mfma_f32_16x16x32_bf16 a[92:95], v[72:75], v[136:139], a[92:95]\n" \
"s_add_u32 s98, 0x47, s97\n" \
"v_writelane_b32 v11, s95, 50\n" \
"v_mfma_f32_16x16x32_bf16 a[92:95], v[68:71], v[132:135], a[92:95]\n" \
"s_cmp_lt_u32 %[w], 3\n" \
"s_cselect_b32 s99, s98, 0xfff\n" \
"v_smfmac_f32_16x16x64_bf16 a[124:127], v[140:143], v[76:83], v50\n" \
"v_mfma_f32_16x16x32_bf16 a[0:3], v[124:127], v[76:79], a[0:3]\n" \
"s_lshr_b32 s94, %[b], 3\n" \
"s_lshl_b32 s67, s96, 2\n" \
"v_mfma_f32_16x16x32_bf16 a[0:3], v[128:131], v[80:83], a[0:3]\n" \
"s_and_b32 s95, 3, s94\n" \
"s_mul_i32 s100, 0x480, s99\n" \
"v_mfma_f32_16x16x32_bf16 a[4:7], v[120:123], v[80:83], a[4:7]\n" \
"s_lshl_b32 s64, %[w], 5\n" \
"s_add_u32 s98, s67, s95\n" \
"v_mfma_f32_16x16x32_bf16 a[4:7], v[116:119], v[76:79], a[4:7]\n" \
"s_add_u32 s101, 0x120, s100\n" \
"s_add_u32 s65, s97, s64\n" \
"v_mfma_f32_16x16x32_bf16 a[32:35], v[76:79], v[132:135], a[32:35]\n" \
"s_mul_i32 s99, 0x24000, s98\n" \
"v_writelane_b32 v11, s101, 15\n" \
"v_mfma_f32_16x16x32_bf16 a[32:35], v[80:83], v[136:139], a[32:35]\n" \
"s_mul_i32 s66, 0x480, s65\n" \
"s_waitcnt lgkmcnt(0)\n" \
"v_mfma_f32_16x16x32_bf16 a[128:131], v[152:155], v[56:59], a[128:131]\n" \
"s_add_u32 s100, 0x1b000, s99\n" \
"s_min_u32 s97, 2, %[w]\n" \
"v_mfma_f32_16x16x32_bf16 a[128:131], v[148:151], v[52:55], a[128:131]\n" \
"s_cmp_lt_u32 %[w], 3\n" \
"s_cselect_b32 s101, s66, s100\n" \
"v_mfma_f32_16x16x32_bf16 a[132:135], v[156:159], v[52:55], a[132:135]\n" \
"s_mul_i32 s64, 0x2400, s97\n" \
"v_writelane_b32 v11, s101, 16\n" \
"v_mfma_f32_16x16x32_bf16 a[132:135], v[160:163], v[56:59], a[132:135]\n" \
"s_add_u32 s65, 0x20000, s64\n" \
"s_mul_i32 s94, 0x9000, s97\n" \
"v_mfma_f32_16x16x32_bf16 a[136:139], v[168:171], v[56:59], a[136:139]\n" \
"s_mul_i32 s95, 0x2400, %[w]\n" \
"v_writelane_b32 v11, s65, 17\n" \
"v_mfma_f32_16x16x32_bf16 a[136:139], v[164:167], v[52:55], a[136:139]\n" \
"s_add_u32 s67, s99, s94\n" \
"v_writelane_b32 v11, s67, 22\n" \
"v_mfma_f32_16x16x32_bf16 a[140:143], v[172:175], v[52:55], a[140:143]\n" \
"s_cmp_lt_u32 %[w], 3\n" \
"s_cselect_b32 s98, s95, 0x12000\n" \
"v_mfma_f32_16x16x32_bf16 a[140:143], v[176:179], v[56:59], a[140:143]\n" \
"s_add_u32 s66, 0x20000, s95\n" \
"v_writelane_b32 v11, s98, 18\n" \
"v_mfma_f32_16x16x32_bf16 a[160:163], v[152:155], v[64:67], a[160:163]\n" \
"s_cmp_lt_u32 %[w], 3\n" \
"s_cselect_b32 s100, s66, 0xfc00\n" \
"v_mfma_f32_16x16x32_bf16 a[160:163], v[148:151], v[60:63], a[160:163]\n" \
"v_writelane_b32 v11, s95, 19\n" \
"s_lshl_b32 s101, %[w], 10\n" \
"v_mfma_f32_16x16x32_bf16 a[164:167], v[156:159], v[60:63], a[164:167]\n" \
"s_lshr_b32 s65, s96, 1\n" \
"v_writelane_b32 v11, s100, 20\n" \
"v_mfma_f32_16x16x32_bf16 a[164:167], v[160:163], v[64:67], a[164:167]\n" \
"s_cmp_eq_u32 %[w], s65\n" \
"s_cselect_b32 s97, 0xff0000, 0\n" \
"v_mfma_f32_16x16x32_bf16 a[168:171], v[168:171], v[64:67], a[168:171]\n" \
"s_add_u32 s64, 0x1d400, s101\n" \
"v_writelane_b32 v11, s64, 21\n" \
"v_mfma_f32_16x16x32_bf16 a[168:171], v[164:167], v[60:63], a[168:171]\n" \
"v_writelane_b32 v11, s97, 6\n" \
"v_mfma_f32_16x16x32_bf16 a[172:175], v[172:175], v[60:63], a[172:175]\n" \
"v_mfma_f32_16x16x32_bf16 a[172:175], v[176:179], v[64:67], a[172:175]\n" \
"v_mfma_f32_16x16x32_bf16 a[192:195], v[152:155], v[72:75], a[192:195]\n" \
"v_mfma_f32_16x16x32_bf16 a[192:195], v[148:151], v[68:71], a[192:195]\n" \
"s_waitcnt vmcnt(3)\n" \
"s_barrier\n" \
"v_mfma_f32_16x16x32_bf16 a[196:199], v[156:159], v[68:71], a[196:199]\n" \
"s_add_u32 m0, s40, 0x0\n" \
"s_add_u32 s85, s72, 0x0\n" \
"s_nop 0\n" \
"v_mfma_f32_16x16x32_bf16 a[196:199], v[160:163], v[72:75], a[196:199]\n" \
"buffer_load_dwordx4 v20, s[80:83], s85 offen sc1 lds\n" \
"v_mfma_f32_16x16x32_bf16 a[200:203], v[168:171], v[72:75], a[200:203]\n" \
"s_add_u32 m0, s40, 0x1000\n" \
"s_add_u32 s84, s72, 0x1000\n" \
"v_mfma_f32_16x16x32_bf16 a[200:203], v[164:167], v[68:71], a[200:203]\n" \
"ds_read_b64_tr_b16 v[140:141], v39 offset:32768\n" \
"ds_read_b64_tr_b16 v[142:143], v39 offset:33728\n" \
"v_mfma_f32_16x16x32_bf16 a[204:207], v[172:175], v[68:71], a[204:207]\n" \
"buffer_load_dwordx4 v20, s[80:83], s84 offen sc1 lds\n" \
"v_mfma_f32_16x16x32_bf16 a[204:207], v[176:179], v[72:75], a[204:207]\n" \
"ds_read_b64_tr_b16 v[128:129], v37 offset:36864\n" \
"ds_read_b64_tr_b16 v[130:131], v37 offset:37376\n" \
"v_mfma_f32_16x16x32_bf16 a[224:227], v[152:155], v[80:83], a[224:227]\n" \
"s_add_u32 m0, s40, 0x3000\n" \
"s_add_u32 s85, s62, 0x0\n" \
"v_mfma_f32_16x16x32_bf16 a[224:227], v[148:151], v[76:79], a[224:227]\n" \
"L_pf6:\n" \
"s_cbranch_execz L_pf10\n" \
"s_nop 0\n" \
"buffer_load_dwordx4 v21, s[76:79], s85 offen lds\n" \
"v_mfma_f32_16x16x32_bf16 a[228:231], v[156:159], v[76:79], a[228:231]\n" \
"ds_read_b64_tr_b16 v[124:125], v37 offset:32768\n" \
"ds_read_b64_tr_b16 v[126:127], v37 offset:33280\n" \
"v_mfma_f32_16x16x32_bf16 a[228:231], v[160:163], v[80:83], a[228:231]\n" \
"s_add_u32 m0, %[mA], 0x0\n" \
"s_add_u32 s84, s62, 0x0\n" \
"s_nop 0\n" \
"v_mfma_f32_16x16x32_bf16 a[232:235], v[168:171], v[80:83], a[232:235]\n" \
"buffer_load_dwordx4 v24, s[76:79], s84 offen lds\n" \
"v_mfma_f32_16x16x32_bf16 a[232:235], v[164:167], v[76:79], a[232:235]\n" \
"ds_read_b64_tr_b16 v[132:133], v38 offset:32768\n" \
"ds_read_b64_tr_b16 v[134:135], v38 offset:33280\n" \
"v_mfma_f32_16x16x32_bf16 a[236:239], v[172:175], v[76:79], a[236:239]\n" \
"ds_read_b64_tr_b16 v[136:137], v38 offset:36864\n" \
"ds_read_b64_tr_b16 v[138:139], v38 offset:37376\n" \
"v_mfma_f32_16x16x32_bf16 a[236:239], v[176:179], v[80:83], a[236:239]\n" \
"s_add_u32 m0, %[mA], 0x400\n" \
"s_add_u32 s85, s62, 0x2400\n" \
"v_mfma_f32_16x16x32_bf16 a[144:147], v[152:155], v[88:91], a[144:147]\n" \
"buffer_load_dwordx4 v25, s[76:79], s85 offen lds\n" \
"v_mfma_f32_16x16x32_bf16 a[144:147], v[148:151], v[84:87], a[144:147]\n" \
"ds_read_b128 v[52:55], v28 offset:32768\n" \
"ds_read_b128 v[56:59], v29 offset:32768\n" \
"v_mfma_f32_16x16x32_bf16 a[148:151], v[156:159], v[84:87], a[148:151]\n" \
"s_add_u32 m0, %[mB], 0x0\n" \
"s_add_u32 s84, s63, 0x0\n" \
"v_mfma_f32_16x16x32_bf16 a[148:151], v[160:163], v[88:91], a[148:151]\n" \
"buffer_load_dwordx4 v26, s[68:71], s84 offen lds\n" \
"v_mfma_f32_16x16x32_bf16 a[152:155], v[168:171], v[88:91], a[152:155]\n" \
"ds_read_b64_tr_b16 v[116:117], v36 offset:32768\n" \
"ds_read_b64_tr_b16 v[118:119], v36 offset:33280\n" \
"v_mfma_f32_16x16x32_bf16 a[152:155], v[164:167], v[84:87], a[152:155]\n" \
"ds_read_b64_tr_b16 v[120:121], v36 offset:36864\n" \
"ds_read_b64_tr_b16 v[122:123], v36 offset:37376\n" \
"v_mfma_f32_16x16x32_bf16 a[156:159], v[172:175], v[84:87], a[156:159]\n" \
"s_add_u32 m0, %[mA], 0x800\n" \
"s_add_u32 s85, s62, 0x4800\n" \
"v_mfma_f32_16x16x32_bf16 a[156:159], v[176:179], v[88:91], a[156:159]\n" \
"buffer_load_dwordx4 v24, s[76:79], s85 offen lds\n" \
"v_mfma_f32_16x16x32_bf16 a[176:179], v[152:155], v[96:99], a[176:179]\n" \
"ds_read_b128 v[84:87], v22 offset:32768\n" \
"ds_read_b128 v[88:91], v23 offset:32768\n" \
"v_mfma_f32_16x16x32_bf16 a[176:179], v[148:151], v[92:95], a[176:179]\n" \
"s_add_u32 m0, %[mA], 0xc00\n" \
"s_add_u32 s84, s62, 0x6c00\n" \
"v_mfma_f32_16x16x32_bf16 a[180:183], v[156:159], v[92:95], a[180:183]\n" \
"buffer_load_dwordx4 v25, s[76:79], s84 offen lds\n" \
"v_mfma_f32_16x16x32_bf16 a[180:183], v[160:163], v[96:99], a[180:183]\n" \
"ds_read_b128 v[60:63], v28 offset:34816\n" \
"ds_read_b128 v[64:67], v29 offset:34816\n" \
"v_mfma_f32_16x16x32_bf16 a[184:187], v[168:171], v[96:99], a[184:187]\n" \
"ds_read_b128 v[68:71], v28 offset:36864\n" \
"ds_read_b128 v[72:75], v29 offset:36864\n" \
"v_mfma_f32_16x16x32_bf16 a[184:187], v[164:167], v[92:95], a[184:187]\n" \
"s_add_u32 m0, %[mB], 0x400\n" \
"s_add_u32 s85, s63, 0x8000\n" \
"v_mfma_f32_16x16x32_bf16 a[188:191], v[172:175], v[92:95], a[188:191]\n" \
"buffer_load_dwordx4 v26, s[68:71], s85 offen lds\n" \
"v_mfma_f32_16x16x32_bf16 a[188:191], v[176:179], v[96:99], a[188:191]\n" \
"ds_read_b128 v[76:79], v28 offset:38912\n" \
"ds_read_b128 v[80:83], v29 offset:38912\n" \
"v_mfma_f32_16x16x32_bf16 a[208:211], v[152:155], v[104:107], a[208:211]\n" \
"s_add_u32 m0, %[mA], 0x1000\n" \
"s_add_u32 s84, s62, 0x9000\n" \
"v_mfma_f32_16x16x32_bf16 a[208:211], v[148:151], v[100:103], a[208:211]\n" \
"buffer_load_dwordx4 v24, s[76:79], s84 offen lds\n" \
"v_mfma_f32_16x16x32_bf16 a[212:215], v[156:159], v[100:103], a[212:215]\n" \
"ds_read_b128 v[92:95], v48 offset:34816\n" \
"ds_read_b128 v[96:99], v49 offset:34816\n" \
"v_mfma_f32_16x16x32_bf16 a[212:215], v[160:163], v[104:107], a[212:215]\n" \
"v_mfma_f32_16x16x32_bf16 a[216:219], v[168:171], v[104:107], a[216:219]\n" \
"s_add_u32 m0, %[mA], 0x1400\n" \
"s_add_u32 s85, s62, 0xb400\n" \
"v_mfma_f32_16x16x32_bf16 a[216:219], v[164:167], v[100:103], a[216:219]\n" \
"buffer_load_dwordx4 v25, s[76:79], s85 offen lds\n" \
"v_mfma_f32_16x16x32_bf16 a[220:223], v[172:175], v[100:103], a[220:223]\n" \
"ds_read_b128 v[100:103], v48 offset:36864\n" \
"v_mfma_f32_16x16x32_bf16 a[220:223], v[176:179], v[104:107], a[220:223]\n" \
"s_add_u32 m0, %[mB], 0x800\n" \
"s_add_u32 s84, s63, 0x10000\n" \
"v_mfma_f32_16x16x32_bf16 a[240:243], v[152:155], v[112:115], a[240:243]\n" \
"buffer_load_dwordx4 v27, s[68:71], s84 offen lds\n" \
"v_mfma_f32_16x16x32_bf16 a[240:243], v[148:151], v[108:111], a[240:243]\n" \
"ds_read_b64_tr_b16 v[152:153], v32 offset:40960\n" \
"ds_read_b64_tr_b16 v[154:155], v32 offset:41984\n" \
"v_mfma_f32_16x16x32_bf16 a[244:247], v[156:159], v[108:111], a[244:247]\n" \
"ds_read_b64_tr_b16 v[148:149], v32 offset:32768\n" \
"ds_read_b64_tr_b16 v[150:151], v32 offset:33792\n" \
"v_mfma_f32_16x16x32_bf16 a[244:247], v[160:163], v[112:115], a[244:247]\n" \
"s_add_u32 m0, %[mA], 0x1800\n" \
"s_add_u32 s85, s62, 0xd800\n" \
"v_mfma_f32_16x16x32_bf16 a[248:251], v[168:171], v[112:115], a[248:251]\n" \
"buffer_load_dwordx4 v24, s[76:79], s85 offen lds\n" \
"v_mfma_f32_16x16x32_bf16 a[248:251], v[164:167], v[108:111], a[248:251]\n" \
"ds_read_b64_tr_b16 v[156:157], v33 offset:32768\n" \
"ds_read_b64_tr_b16 v[158:159], v33 offset:33792\n" \
"v_mfma_f32_16x16x32_bf16 a[252:255], v[172:175], v[108:111], a[252:255]\n" \
"ds_read_b64_tr_b16 v[160:161], v33 offset:40960\n" \
"ds_read_b64_tr_b16 v[162:163], v33 offset:41984\n" \
"v_mfma_f32_16x16x32_bf16 a[252:255], v[176:179], v[112:115], a[252:255]\n" \
"s_add_u32 m0, %[mA], 0x1c00\n" \
"s_waitcnt lgkmcnt(11)\n" \
"s_nop 0\n" \
"v_smfmac_f32_16x16x64_bf16 a[16:19], v[140:143], v[84:91], v50\n" \
"v_mfma_f32_16x16x32_bf16 a[48:51], v[128:131], v[88:91], a[48:51]\n" \
"s_add_u32 s85, s62, 0xfc00\n" \
"ds_read_b64_tr_b16 v[168:169], v34 offset:40960\n" \
"v_mfma_f32_16x16x32_bf16 a[48:51], v[124:127], v[84:87], a[48:51]\n" \
"buffer_load_dwordx4 v25, s[76:79], s85 offen lds\n" \
"v_mfma_f32_16x16x32_bf16 a[80:83], v[84:87], v[132:135], a[80:83]\n" \
"s_add_u32 m0, %[mB], 0xc00\n" \
"s_add_u32 s84, s63, 0x18000\n" \
"v_mfma_f32_16x16x32_bf16 a[80:83], v[88:91], v[136:139], a[80:83]\n" \
"ds_read_b64_tr_b16 v[170:171], v34 offset:41984\n" \
"ds_read_b64_tr_b16 v[164:165], v34 offset:32768\n" \
"v_smfmac_f32_16x16x64_bf16 a[112:115], v[140:143], v[52:59], v50\n" \
"v_mfma_f32_16x16x32_bf16 a[20:23], v[128:131], v[56:59], a[20:23]\n" \
"buffer_load_dwordx4 v27, s[68:71], s84 offen lds\n" \
"v_mfma_f32_16x16x32_bf16 a[20:23], v[124:127], v[52:55], a[20:23]\n" \
"s_add_u32 s88, s42, 0x2000\n" \
"ds_read_b64_tr_b16 v[166:167], v34 offset:33792\n" \
"v_mfma_f32_16x16x32_bf16 a[52:55], v[116:119], v[52:55], a[52:55]\n" \
"ds_read_b64_tr_b16 v[172:173], v35 offset:32768\n" \
"ds_read_b64_tr_b16 v[174:175], v35 offset:33792\n" \
"v_mfma_f32_16x16x32_bf16 a[52:55], v[120:123], v[56:59], a[52:55]\n" \
"buffer_store_dwordx4 v[224:227], v20, s[80:83], s88 offen offset:0 sc1\n" \
"v_mfma_f32_16x16x32_bf16 a[84:87], v[56:59], v[136:139], a[84:87]\n" \
"ds_read_b64_tr_b16 v[176:177], v35 offset:40960\n" \
"ds_read_b64_tr_b16 v[178:179], v35 offset:41984\n" \
"v_mfma_f32_16x16x32_bf16 a[84:87], v[52:55], v[132:135], a[84:87]\n" \
"ds_read_b128 v[84:87], v48 offset:32768\n" \
"ds_read_b128 v[88:91], v49 offset:32768\n" \
"v_smfmac_f32_16x16x64_bf16 a[116:119], v[140:143], v[60:67], v50\n" \
"v_mfma_f32_16x16x32_bf16 a[24:27], v[124:127], v[60:63], a[24:27]\n" \
"ds_read_b128 v[104:107], v49 offset:36864\n" \
"ds_read_b128 v[108:111], v48 offset:38912\n" \
"v_mfma_f32_16x16x32_bf16 a[24:27], v[128:131], v[64:67], a[24:27]\n" \
"s_add_u32 s89, s42, 0x3000\n" \
"ds_read_b128 v[112:115], v49 offset:38912\n" \
"v_mfma_f32_16x16x32_bf16 a[56:59], v[120:123], v[64:67], a[56:59]\n" \
"buffer_store_dwordx4 v[228:231], v20, s[80:83], s89 offen offset:0 sc1\n" \
"v_mfma_f32_16x16x32_bf16 a[56:59], v[116:119], v[60:63], a[56:59]\n" \
"s_add_u32 s62, s62, 0x80\n" \
"s_lshr_b32 s96, %[b], 3\n" \
"v_mfma_f32_16x16x32_bf16 a[88:91], v[60:63], v[132:135], a[88:91]\n" \
"s_cmp_lt_u32 s62, 0x480\n" \
"s_cselect_b32 s62, s62, 0\n" \
"v_mfma_f32_16x16x32_bf16 a[88:91], v[64:67], v[136:139], a[88:91]\n" \
"s_lshl_b32 s72, s62, 6\n" \
"s_add_u32 s72, s72, s37\n" \
"v_smfmac_f32_16x16x64_bf16 a[120:123], v[140:143], v[68:75], v50\n" \
"v_mfma_f32_16x16x32_bf16 a[28:31], v[128:131], v[72:75], a[28:31]\n" \
"s_lshl_b32 s63, s62, 12\n" \
"s_and_b32 s97, 3, s96\n" \
"v_mfma_f32_16x16x32_bf16 a[28:31], v[124:127], v[68:71], a[28:31]\n" \
"s_and_b32 s94, 7, %[b]\n" \
"s_add_u32 s98, 1, s97\n" \
"v_mfma_f32_16x16x32_bf16 a[60:63], v[116:119], v[68:71], a[60:63]\n" \
"s_add_u32 s88, s42, 0x0\n" \
"s_lshl_b32 s95, s94, 2\n" \
"v_mfma_f32_16x16x32_bf16 a[60:63], v[120:123], v[72:75], a[60:63]\n" \
"buffer_store_dwordx4 v[232:235], v20, s[80:83], s88 offen offset:1024 sc1\n" \
"v_mfma_f32_16x16x32_bf16 a[92:95], v[72:75], v[136:139], a[92:95]\n" \
"s_and_b32 s99, 3, s98\n" \
"s_add_u32 s100, s95, s99\n" \
"v_mfma_f32_16x16x32_bf16 a[92:95], v[68:71], v[132:135], a[92:95]\n" \
"s_mul_i32 s101, 0x24000, s100\n" \
"s_mul_i32 s64, 0x9000, %[w]\n" \
"v_smfmac_f32_16x16x64_bf16 a[124:127], v[140:143], v[76:83], v50\n" \
"v_mfma_f32_16x16x32_bf16 a[0:3], v[124:127], v[76:79], a[0:3]\n" \
"s_and_b32 s96, 2, %[w]\n" \
"s_add_u32 s65, s101, s64\n" \
"v_mfma_f32_16x16x32_bf16 a[0:3], v[128:131], v[80:83], a[0:3]\n" \
"s_lshr_b32 s66, %[b], 5\n" \
"s_lshl_b32 s98, s96, 1\n" \
"v_mfma_f32_16x16x32_bf16 a[4:7], v[120:123], v[80:83], a[4:7]\n" \
"s_and_b32 s99, 1, %[w]\n" \
"v_writelane_b32 v11, s65, 23\n" \
"v_mfma_f32_16x16x32_bf16 a[4:7], v[116:119], v[76:79], a[4:7]\n" \
"s_lshl_b32 s67, s66, 3\n" \
"s_add_u32 s101, s98, s99\n" \
"v_mfma_f32_16x16x32_bf16 a[32:35], v[76:79], v[132:135], a[32:35]\n" \
"s_add_u32 s65, s67, s101\n" \
"s_mul_i32 s66, 0x24800, s65\n" \
"v_mfma_f32_16x16x32_bf16 a[32:35], v[80:83], v[136:139], a[32:35]\n" \
"s_lshl_b32 s98, s100, 12\n" \
"s_waitcnt lgkmcnt(0)\n" \
"v_mfma_f32_16x16x32_bf16 a[128:131], v[152:155], v[56:59], a[128:131]\n" \
"s_add_u32 s96, 0x3000000, s66\n" \
"s_add_u32 s99, 0x4800, s98\n" \
"v_mfma_f32_16x16x32_bf16 a[128:131], v[148:151], v[52:55], a[128:131]\n" \
"s_add_u32 s65, s96, s99\n" \
"s_sub_u32 s100, 0x78, s67\n" \
"v_mfma_f32_16x16x32_bf16 a[132:135], v[156:159], v[52:55], a[132:135]\n" \
"v_writelane_b32 v11, s65, 26\n" \
"s_add_u32 s98, s100, s101\n" \
"v_mfma_f32_16x16x32_bf16 a[132:135], v[160:163], v[56:59], a[132:135]\n" \
"s_mul_i32 s65, 0x24800, s98\n" \
"s_add_u32 s67, 0x3000000, s65\n" \
"v_mfma_f32_16x16x32_bf16 a[136:139], v[168:171], v[56:59], a[136:139]\n" \
"s_add_u32 s101, 2, s97\n" \
"s_add_u32 s100, s67, s99\n" \
"v_mfma_f32_16x16x32_bf16 a[136:139], v[164:167], v[52:55], a[136:139]\n" \
"s_and_b32 s98, 3, s101\n" \
"v_writelane_b32 v11, s100, 29\n" \
"v_mfma_f32_16x16x32_bf16 a[140:143], v[172:175], v[52:55], a[140:143]\n" \
"s_add_u32 s99, s95, s98\n" \
"s_mul_i32 s100, 0x24000, s99\n" \
"v_mfma_f32_16x16x32_bf16 a[140:143], v[176:179], v[56:59], a[140:143]\n" \
"s_add_u32 s101, s100, s64\n" \
"s_lshl_b32 s98, s99, 12\n" \
"v_mfma_f32_16x16x32_bf16 a[160:163], v[152:155], v[64:67], a[160:163]\n" \
"v_writelane_b32 v11, s101, 24\n" \
"s_add_u32 s100, 0x4800, s98\n" \
"v_mfma_f32_16x16x32_bf16 a[160:163], v[148:151], v[60:63], a[160:163]\n" \
"s_add_u32 s101, s96, s100\n" \
"v_writelane_b32 v11, s101, 27\n" \
"v_mfma_f32_16x16x32_bf16 a[164:167], v[156:159], v[60:63], a[164:167]\n" \
"s_add_u32 s98, 3, s97\n" \
"s_add_u32 s99, s67, s100\n" \
"v_mfma_f32_16x16x32_bf16 a[164:167], v[160:163], v[64:67], a[164:167]\n" \
"s_and_b32 s101, 3, s98\n" \
"v_writelane_b32 v11, s99, 30\n" \
"v_mfma_f32_16x16x32_bf16 a[168:171], v[168:171], v[64:67], a[168:171]\n" \
"s_add_u32 s100, s95, s101\n" \
"s_mul_i32 s99, 0x24000, s100\n" \
"v_mfma_f32_16x16x32_bf16 a[168:171], v[164:167], v[60:63], a[168:171]\n" \
"s_lshl_b32 s98, s100, 12\n" \
"s_add_u32 s97, s99, s64\n" \
"v_mfma_f32_16x16x32_bf16 a[172:175], v[172:175], v[60:63], a[172:175]\n" \
"s_add_u32 s95, 0x4800, s98\n" \
"v_writelane_b32 v11, s97, 25\n" \
"v_mfma_f32_16x16x32_bf16 a[172:175], v[176:179], v[64:67], a[172:175]\n" \
"s_add_u32 s101, s96, s95\n" \
"v_writelane_b32 v11, s101, 28\n" \
"v_mfma_f32_16x16x32_bf16 a[192:195], v[152:155], v[72:75], a[192:195]\n" \
"s_add_u32 s99, s67, s95\n" \
"s_lshl_b32 s64, s94, 11\n" \
"v_mfma_f32_16x16x32_bf16 a[192:195], v[148:151], v[68:71], a[192:195]\n" \
"v_writelane_b32 v11, s99, 31\n" \
"s_add_u32 s97, s96, s64\n" \
"s_waitcnt vmcnt(3)\n" \
"s_barrier\n" \
"v_mfma_f32_16x16x32_bf16 a[196:199], v[156:159], v[68:71], a[196:199]\n" \
"s_add_u32 m0, s40, 0x8000\n" \
"s_add_u32 s85, s72, 0x0\n" \
"v_mfma_f32_16x16x32_bf16 a[196:199], v[160:163], v[72:75], a[196:199]\n" \
"buffer_load_dwordx4 v20, s[80:83], s85 offen sc1 lds\n" \
"v_mfma_f32_16x16x32_bf16 a[200:203], v[168:171], v[72:75], a[200:203]\n" \
"s_add_u32 m0, s40, 0x9000\n" \
"s_add_u32 s84, s72, 0x1000\n" \
"v_mfma_f32_16x16x32_bf16 a[200:203], v[164:167], v[68:71], a[200:203]\n" \
"ds_read_b64_tr_b16 v[140:141], v39 offset:0\n" \
"ds_read_b64_tr_b16 v[142:143], v39 offset:960\n" \
"v_mfma_f32_16x16x32_bf16 a[204:207], v[172:175], v[68:71], a[204:207]\n" \
"buffer_load_dwordx4 v20, s[80:83], s84 offen sc1 lds\n" \
"v_mfma_f32_16x16x32_bf16 a[204:207], v[176:179], v[72:75], a[204:207]\n" \
"ds_read_b64_tr_b16 v[128:129], v37 offset:4096\n" \
"ds_read_b64_tr_b16 v[130:131], v37 offset:4608\n" \
"v_mfma_f32_16x16x32_bf16 a[224:227], v[152:155], v[80:83], a[224:227]\n" \
"s_add_u32 m0, s40, 0xb000\n" \
"s_add_u32 s85, s62, 0x0\n" \
"v_mfma_f32_16x16x32_bf16 a[224:227], v[148:151], v[76:79], a[224:227]\n" \
"buffer_load_dwordx4 v21, s[76:79], s85 offen lds\n" \
"v_mfma_f32_16x16x32_bf16 a[228:231], v[156:159], v[76:79], a[228:231]\n" \
"ds_read_b64_tr_b16 v[124:125], v37 offset:0\n" \
"ds_read_b64_tr_b16 v[126:127], v37 offset:512\n" \
"v_mfma_f32_16x16x32_bf16 a[228:231], v[160:163], v[80:83], a[228:231]\n" \
"s_add_u32 m0, %[mA], 0x8000\n" \
"s_add_u32 s84, s62, 0x0\n" \
"v_mfma_f32_16x16x32_bf16 a[232:235], v[168:171], v[80:83], a[232:235]\n" \
"buffer_load_dwordx4 v24, s[76:79], s84 offen lds\n" \
"v_mfma_f32_16x16x32_bf16 a[232:235], v[164:167], v[76:79], a[232:235]\n" \
"ds_read_b64_tr_b16 v[132:133], v38 offset:0\n" \
"ds_read_b64_tr_b16 v[134:135], v38 offset:512\n" \
"v_mfma_f32_16x16x32_bf16 a[236:239], v[172:175], v[76:79], a[236:239]\n" \
"ds_read_b64_tr_b16 v[136:137], v38 offset:4096\n" \
"ds_read_b64_tr_b16 v[138:139], v38 offset:4608\n" \
"v_mfma_f32_16x16x32_bf16 a[236:239], v[176:179], v[80:83], a[236:239]\n" \
"s_add_u32 m0, %[mA], 0x8400\n" \
"s_add_u32 s85, s62, 0x2400\n" \
"v_mfma_f32_16x16x32_bf16 a[144:147], v[152:155], v[88:91], a[144:147]\n" \
"buffer_load_dwordx4 v25, s[76:79], s85 offen lds\n" \
"v_mfma_f32_16x16x32_bf16 a[144:147], v[148:151], v[84:87], a[144:147]\n" \
"ds_read_b128 v[52:55], v28 offset:0\n" \
"ds_read_b128 v[56:59], v29 offset:0\n" \
"v_mfma_f32_16x16x32_bf16 a[148:151], v[156:159], v[84:87], a[148:151]\n" \
"s_add_u32 m0, %[mB], 0x8000\n" \
"s_add_u32 s84, s63, 0x0\n" \
"s_nop 0\n" \
"v_mfma_f32_16x16x32_bf16 a[148:151], v[160:163], v[88:91], a[148:151]\n" \
"buffer_load_dwordx4 v26, s[68:71], s84 offen lds\n" \
"v_mfma_f32_16x16x32_bf16 a[152:155], v[168:171], v[88:91], a[152:155]\n" \
"ds_read_b64_tr_b16 v[116:117], v36 offset:0\n" \
"ds_read_b64_tr_b16 v[118:119], v36 offset:512\n" \
"v_mfma_f32_16x16x32_bf16 a[152:155], v[164:167], v[84:87], a[152:155]\n" \
"ds_read_b64_tr_b16 v[120:121], v36 offset:4096\n" \
"ds_read_b64_tr_b16 v[122:123], v36 offset:4608\n" \
"v_mfma_f32_16x16x32_bf16 a[156:159], v[172:175], v[84:87], a[156:159]\n" \
"s_add_u32 m0, %[mA], 0x8800\n" \
"s_add_u32 s85, s62, 0x4800\n" \
"v_mfma_f32_16x16x32_bf16 a[156:159], v[176:179], v[88:91], a[156:159]\n" \
"buffer_load_dwordx4 v24, s[76:79], s85 offen lds\n" \
"v_mfma_f32_16x16x32_bf16 a[176:179], v[152:155], v[96:99], a[176:179]\n" \
"ds_read_b128 v[84:87], v22 offset:0\n" \
"ds_read_b128 v[88:91], v23 offset:0\n" \
"v_mfma_f32_16x16x32_bf16 a[176:179], v[148:151], v[92:95], a[176:179]\n" \
"s_add_u32 m0, %[mA], 0x8c00\n" \
"s_add_u32 s84, s62, 0x6c00\n" \
"v_mfma_f32_16x16x32_bf16 a[180:183], v[156:159], v[92:95], a[180:183]\n" \
"buffer_load_dwordx4 v25, s[76:79], s84 offen lds\n" \
"v_mfma_f32_16x16x32_bf16 a[180:183], v[160:163], v[96:99], a[180:183]\n" \
"ds_read_b128 v[60:63], v28 offset:2048\n" \
"ds_read_b128 v[64:67], v29 offset:2048\n" \
"v_mfma_f32_16x16x32_bf16 a[184:187], v[168:171], v[96:99], a[184:187]\n" \
"ds_read_b128 v[68:71], v28 offset:4096\n" \
"ds_read_b128 v[72:75], v29 offset:4096\n" \
"v_mfma_f32_16x16x32_bf16 a[184:187], v[164:167], v[92:95], a[184:187]\n" \
"s_add_u32 m0, %[mB], 0x8400\n" \
"s_add_u32 s85, s63, 0x8000\n" \
"v_mfma_f32_16x16x32_bf16 a[188:191], v[172:175], v[92:95], a[188:191]\n" \
"buffer_load_dwordx4 v26, s[68:71], s85 offen lds\n" \
"v_mfma_f32_16x16x32_bf16 a[188:191], v[176:179], v[96:99], a[188:191]\n" \
"ds_read_b128 v[76:79], v28 offset:6144\n" \
"ds_read_b128 v[80:83], v29 offset:6144\n" \
"v_mfma_f32_16x16x32_bf16 a[208:211], v[152:155], v[104:107], a[208:211]\n" \
"s_add_u32 m0, %[mA], 0x9000\n" \
"s_add_u32 s84, s62, 0x9000\n" \
"v_mfma_f32_16x16x32_bf16 a[208:211], v[148:151], v[100:103], a[208:211]\n" \
"buffer_load_dwordx4 v24, s[76:79], s84 offen lds\n" \
"v_mfma_f32_16x16x32_bf16 a[212:215], v[156:159], v[100:103], a[212:215]\n" \
"ds_read_b128 v[92:95], v48 offset:2048\n" \
"ds_read_b128 v[96:99], v49 offset:2048\n" \
"v_mfma_f32_16x16x32_bf16 a[212:215], v[160:163], v[104:107], a[212:215]\n" \
"v_writelane_b32 v11, s97, 32\n" \
"s_and_b32 s99, 1, s94\n" \
"v_mfma_f32_16x16x32_bf16 a[216:219], v[168:171], v[104:107], a[216:219]\n" \
"s_add_u32 m0, %[mA], 0x9400\n" \
"s_add_u32 s85, s62, 0xb400\n" \
"v_mfma_f32_16x16x32_bf16 a[216:219], v[164:167], v[100:103], a[216:219]\n" \
"buffer_load_dwordx4 v25, s[76:79], s85 offen lds\n" \
"v_mfma_f32_16x16x32_bf16 a[220:223], v[172:175], v[100:103], a[220:223]\n" \
"ds_read_b128 v[100:103], v48 offset:4096\n" \
"s_add_u32 s100, s67, s64\n" \
"v_mfma_f32_16x16x32_bf16 a[220:223], v[176:179], v[104:107], a[220:223]\n" \
"s_add_u32 m0, %[mB], 0x8800\n" \
"s_add_u32 s84, s63, 0x10000\n" \
"v_mfma_f32_16x16x32_bf16 a[240:243], v[152:155], v[112:115], a[240:243]\n" \
"buffer_load_dwordx4 v27, s[68:71], s84 offen lds\n" \
"v_mfma_f32_16x16x32_bf16 a[240:243], v[148:151], v[108:111], a[240:243]\n" \
"ds_read_b64_tr_b16 v[152:153], v32 offset:8192\n" \
"ds_read_b64_tr_b16 v[154:155], v32 offset:9216\n" \
"v_mfma_f32_16x16x32_bf16 a[244:247], v[156:159], v[108:111], a[244:247]\n" \
"ds_read_b64_tr_b16 v[148:149], v32 offset:0\n" \
"ds_read_b64_tr_b16 v[150:151], v32 offset:1024\n" \
"v_mfma_f32_16x16x32_bf16 a[244:247], v[160:163], v[112:115], a[244:247]\n" \
"s_add_u32 m0, %[mA], 0x9800\n" \
"s_add_u32 s85, s62, 0xd800\n" \
"v_mfma_f32_16x16x32_bf16 a[248:251], v[168:171], v[112:115], a[248:251]\n" \
"buffer_load_dwordx4 v24, s[76:79], s85 offen lds\n" \
"v_mfma_f32_16x16x32_bf16 a[248:251], v[164:167], v[108:111], a[248:251]\n" \
"ds_read_b64_tr_b16 v[156:157], v33 offset:0\n" \
"ds_read_b64_tr_b16 v[158:159], v33 offset:1024\n" \
"v_mfma_f32_16x16x32_bf16 a[252:255], v[172:175], v[108:111], a[252:255]\n" \
"ds_read_b64_tr_b16 v[160:161], v33 offset:8192\n" \
"ds_read_b64_tr_b16 v[162:163], v33 offset:9216\n" \
"v_mfma_f32_16x16x32_bf16 a[252:255], v[176:179], v[112:115], a[252:255]\n" \
"s_add_u32 m0, %[mA], 0x9c00\n" \
"s_waitcnt lgkmcnt(11)\n" \
"s_nop 0\n" \
"v_smfmac_f32_16x16x64_bf16 a[16:19], v[140:143], v[84:91], v50\n" \
"v_mfma_f32_16x16x32_bf16 a[48:51], v[128:131], v[88:91], a[48:51]\n" \
"s_add_u32 s85, s62, 0xfc00\n" \
"ds_read_b64_tr_b16 v[168:169], v34 offset:8192\n" \
"v_mfma_f32_16x16x32_bf16 a[48:51], v[124:127], v[84:87], a[48:51]\n" \
"buffer_load_dwordx4 v25, s[76:79], s85 offen lds\n" \
"v_mfma_f32_16x16x32_bf16 a[80:83], v[84:87], v[132:135], a[80:83]\n" \
"s_add_u32 m0, %[mB], 0x8c00\n" \
"s_add_u32 s84, s63, 0x18000\n" \
"v_mfma_f32_16x16x32_bf16 a[80:83], v[88:91], v[136:139], a[80:83]\n" \
"ds_read_b64_tr_b16 v[170:171], v34 offset:9216\n" \
"ds_read_b64_tr_b16 v[164:165], v34 offset:0\n" \
"v_smfmac_f32_16x16x64_bf16 a[112:115], v[140:143], v[52:59], v50\n" \
"v_mfma_f32_16x16x32_bf16 a[20:23], v[128:131], v[56:59], a[20:23]\n" \
"buffer_load_dwordx4 v27, s[68:71], s84 offen lds\n" \
"v_mfma_f32_16x16x32_bf16 a[20:23], v[124:127], v[52:55], a[20:23]\n" \
"s_add_u32 s89, s42, 0x1000\n" \
"ds_read_b64_tr_b16 v[166:167], v34 offset:1024\n" \
"v_mfma_f32_16x16x32_bf16 a[52:55], v[116:119], v[52:55], a[52:55]\n" \
"ds_read_b64_tr_b16 v[172:173], v35 offset:0\n" \
"ds_read_b64_tr_b16 v[174:175], v35 offset:1024\n" \
"v_mfma_f32_16x16x32_bf16 a[52:55], v[120:123], v[56:59], a[52:55]\n" \
"buffer_store_dwordx4 v[236:239], v20, s[80:83], s89 offen offset:1024 sc1\n" \
"v_mfma_f32_16x16x32_bf16 a[84:87], v[56:59], v[136:139], a[84:87]\n" \
"ds_read_b64_tr_b16 v[176:177], v35 offset:8192\n" \
"ds_read_b64_tr_b16 v[178:179], v35 offset:9216\n" \
"v_mfma_f32_16x16x32_bf16 a[84:87], v[52:55], v[132:135], a[84:87]\n" \
"ds_read_b128 v[84:87], v48 offset:0\n" \
"ds_read_b128 v[88:91], v49 offset:0\n" \
"v_smfmac_f32_16x16x64_bf16 a[116:119], v[140:143], v[60:67], v50\n" \
"v_mfma_f32_16x16x32_bf16 a[24:27], v[124:127], v[60:63], a[24:27]\n" \
"ds_read_b128 v[104:107], v49 offset:4096\n" \
"ds_read_b128 v[108:111], v48 offset:6144\n" \
"v_mfma_f32_16x16x32_bf16 a[24:27], v[128:131], v[64:67], a[24:27]\n" \
"s_add_u32 s88, s42, 0x2000\n" \
"ds_read_b128 v[112:115], v49 offset:6144\n" \
"v_mfma_f32_16x16x32_bf16 a[56:59], v[120:123], v[64:67], a[56:59]\n" \
"buffer_store_dwordx4 v[240:243], v20, s[80:83], s88 offen offset:1024 sc1\n" \
"v_mfma_f32_16x16x32_bf16 a[56:59], v[116:119], v[60:63], a[56:59]\n" \
"s_add_u32 s62, s62, 0x80\n" \
"v_writelane_b32 v11, s100, 33\n" \
"v_mfma_f32_16x16x32_bf16 a[88:91], v[60:63], v[132:135], a[88:91]\n" \
"s_cmp_lt_u32 s62, 0x480\n" \
"s_cselect_b32 s62, s62, 0\n" \
"v_mfma_f32_16x16x32_bf16 a[88:91], v[64:67], v[136:139], a[88:91]\n" \
"s_lshl_b32 s72, s62, 6\n" \
"s_add_u32 s72, s72, s37\n" \
"v_smfmac_f32_16x16x64_bf16 a[120:123], v[140:143], v[68:75], v50\n" \
"v_mfma_f32_16x16x32_bf16 a[28:31], v[128:131], v[72:75], a[28:31]\n" \
"s_lshl_b32 s63, s62, 12\n" \
"s_add_u32 s98, 0x3004000, s66\n" \
"v_mfma_f32_16x16x32_bf16 a[28:31], v[124:127], v[68:71], a[28:31]\n" \
"s_cmp_lg_u32 s99, 0\n" \
"s_cselect_b32 s97, s67, s96\n" \
"v_mfma_f32_16x16x32_bf16 a[60:63], v[116:119], v[68:71], a[60:63]\n" \
"s_add_u32 s89, s42, 0x3000\n" \
"s_lshr_b32 s95, s94, 1\n" \
"v_mfma_f32_16x16x32_bf16 a[60:63], v[120:123], v[72:75], a[60:63]\n" \
"buffer_store_dwordx4 v[244:247], v20, s[80:83], s89 offen offset:1024 sc1\n" \
"v_mfma_f32_16x16x32_bf16 a[92:95], v[72:75], v[136:139], a[92:95]\n" \
"s_add_u32 s64, 0x4000, s97\n" \
"v_writelane_b32 v11, s98, 34\n" \
"v_mfma_f32_16x16x32_bf16 a[92:95], v[68:71], v[132:135], a[92:95]\n" \
"s_add_u32 s101, 0x3004000, s65\n" \
"s_and_b32 s96, 2, %[w]\n" \
"v_smfmac_f32_16x16x64_bf16 a[124:127], v[140:143], v[76:83], v50\n" \
"v_mfma_f32_16x16x32_bf16 a[0:3], v[124:127], v[76:79], a[0:3]\n" \
"s_cmp_eq_u32 %[w], s95\n" \
"s_cselect_b32 s100, s64, 0x40000000\n" \
"v_mfma_f32_16x16x32_bf16 a[0:3], v[128:131], v[80:83], a[0:3]\n" \
"v_writelane_b32 v11, s101, 35\n" \
"s_lshl_b32 s97, s96, 1\n" \
"v_mfma_f32_16x16x32_bf16 a[4:7], v[120:123], v[80:83], a[4:7]\n" \
"s_and_b32 s98, 1, %[w]\n" \
"v_writelane_b32 v11, s100, 36\n" \
"v_mfma_f32_16x16x32_bf16 a[4:7], v[116:119], v[76:79], a[4:7]\n" \
"s_lshr_b32 s94, %[b], 5\n" \
"s_add_u32 s99, s97, s98\n" \
"v_mfma_f32_16x16x32_bf16 a[32:35], v[76:79], v[132:135], a[32:35]\n" \
"s_lshl_b32 s95, s94, 7\n" \
"s_lshl_b32 s100, s99, 4\n" \
"v_mfma_f32_16x16x32_bf16 a[32:35], v[80:83], v[136:139], a[32:35]\n" \
"s_add_u32 s101, s95, s100\n" \
"s_waitcnt lgkmcnt(0)\n" \
"v_mfma_f32_16x16x32_bf16 a[128:131], v[152:155], v[56:59], a[128:131]\n" \
"s_mul_i32 s64, 0x480, s101\n" \
"s_lshr_b32 s65, %[b], 3\n" \
"v_mfma_f32_16x16x32_bf16 a[128:131], v[148:151], v[52:55], a[128:131]\n" \
"s_sub_u32 s99, 0x780, s95\n" \
"s_and_b32 s66, 3, s65\n" \
"v_mfma_f32_16x16x32_bf16 a[132:135], v[156:159], v[52:55], a[132:135]\n" \
"s_add_u32 s101, s99, s100\n" \
"s_mul_i32 s67, 0x120, s66\n" \
"v_mfma_f32_16x16x32_bf16 a[132:135], v[160:163], v[56:59], a[132:135]\n" \
"s_and_b32 s96, 7, %[b]\n" \
"s_mul_i32 s65, 0x480, s101\n" \
"v_mfma_f32_16x16x32_bf16 a[136:139], v[168:171], v[56:59], a[136:139]\n" \
"s_add_u32 s94, s64, s67\n" \
"s_lshl_b32 s97, s96, 5\n" \
"v_mfma_f32_16x16x32_bf16 a[136:139], v[164:167], v[52:55], a[136:139]\n" \
"s_add_u32 s66, s65, s67\n" \
"s_and_b32 s95, 1, s96\n" \
"v_mfma_f32_16x16x32_bf16 a[140:143], v[172:175], v[52:55], a[140:143]\n" \
"s_add_u32 s98, s94, s97\n" \
"v_writelane_b32 v11, s98, 39\n" \
"v_mfma_f32_16x16x32_bf16 a[140:143], v[176:179], v[56:59], a[140:143]\n" \
"s_cmp_lg_u32 s95, 0\n" \
"s_cselect_b32 s99, s66, s94\n" \
"v_mfma_f32_16x16x32_bf16 a[160:163], v[152:155], v[64:67], a[160:163]\n" \
"s_lshr_b32 s98, s96, 1\n" \
"s_add_u32 s100, 0x100, s99\n" \
"v_mfma_f32_16x16x32_bf16 a[160:163], v[148:151], v[60:63], a[160:163]\n" \
"s_add_u32 s64, s66, s97\n" \
"s_mov_b32 s94, s67\n" \
"v_mfma_f32_16x16x32_bf16 a[164:167], v[156:159], v[60:63], a[164:167]\n" \
"s_cmp_eq_u32 %[w], s98\n" \
"s_cselect_b32 s101, s100, 0x40000000\n" \
"v_mfma_f32_16x16x32_bf16 a[164:167], v[160:163], v[64:67], a[164:167]\n" \
"v_writelane_b32 v11, s64, 40\n" \
"v_writelane_b32 v11, s101, 41\n" \
"v_mfma_f32_16x16x32_bf16 a[168:171], v[168:171], v[64:67], a[168:171]\n" \
"s_add_u32 s96, s16, s94\n" \
"s_addc_u32 s97, s17, 0\n" \
"v_mfma_f32_16x16x32_bf16 a[168:171], v[164:167], v[60:63], a[168:171]\n" \
"s_add_u32 s98, s18, s94\n" \
"s_addc_u32 s99, s19, 0\n" \
"v_mfma_f32_16x16x32_bf16 a[172:175], v[172:175], v[60:63], a[172:175]\n" \
"s_and_b32 s97, s97, 0xffff\n" \
"v_writelane_b32 v11, s96, 7\n" \
"v_mfma_f32_16x16x32_bf16 a[172:175], v[176:179], v[64:67], a[172:175]\n" \
"v_writelane_b32 v11, s97, 8\n" \
"s_and_b32 s99, s99, 0xffff\n" \
"v_mfma_f32_16x16x32_bf16 a[192:195], v[152:155], v[72:75], a[192:195]\n" \
"v_writelane_b32 v11, s98, 9\n" \
"v_writelane_b32 v11, s99, 10\n" \
"v_mfma_f32_16x16x32_bf16 a[192:195], v[148:151], v[68:71], a[192:195]\n" \
"s_cmp_lt_u32 %[w], 3\n" \
"s_cselect_b64 s[96:97], s[96:97], s[98:99]\n" \
"s_waitcnt vmcnt(3)\n" \
"s_barrier\n" \
"v_mfma_f32_16x16x32_bf16 a[196:199], v[156:159], v[68:71], a[196:199]\n" \
"s_add_u32 m0, s40, 0x0\n" \
"s_add_u32 s85, s72, 0x0\n" \
"v_mfma_f32_16x16x32_bf16 a[196:199], v[160:163], v[72:75], a[196:199]\n" \
"buffer_load_dwordx4 v20, s[80:83], s85 offen sc1 lds\n" \
"v_mfma_f32_16x16x32_bf16 a[200:203], v[168:171], v[72:75], a[200:203]\n" \
"s_add_u32 m0, s40, 0x1000\n" \
"s_add_u32 s84, s72, 0x1000\n" \
"v_mfma_f32_16x16x32_bf16 a[200:203], v[164:167], v[68:71], a[200:203]\n" \
"ds_read_b64_tr_b16 v[140:141], v39 offset:32768\n" \
"ds_read_b64_tr_b16 v[142:143], v39 offset:33728\n" \
"v_mfma_f32_16x16x32_bf16 a[204:207], v[172:175], v[68:71], a[204:207]\n" \
"buffer_load_dwordx4 v20, s[80:83], s84 offen sc1 lds\n" \
"v_mfma_f32_16x16x32_bf16 a[204:207], v[176:179], v[72:75], a[204:207]\n" \
"ds_read_b64_tr_b16 v[128:129], v37 offset:36864\n" \
"ds_read_b64_tr_b16 v[130:131], v37 offset:37376\n" \
"v_mfma_f32_16x16x32_bf16 a[224:227], v[152:155], v[80:83], a[224:227]\n" \
"s_add_u32 m0, s40, 0x3000\n" \
"s_add_u32 s85, s62, 0x0\n" \
"v_mfma_f32_16x16x32_bf16 a[224:227], v[148:151], v[76:79], a[224:227]\n" \
"buffer_load_dwordx4 v21, s[76:79], s85 offen lds\n" \
"v_mfma_f32_16x16x32_bf16 a[228:231], v[156:159], v[76:79], a[228:231]\n" \
"ds_read_b64_tr_b16 v[124:125], v37 offset:32768\n" \
"ds_read_b64_tr_b16 v[126:127], v37 offset:33280\n" \
"v_mfma_f32_16x16x32_bf16 a[228:231], v[160:163], v[80:83], a[228:231]\n" \
"s_add_u32 m0, %[mA], 0x0\n" \
"s_add_u32 s84, s62, 0x0\n" \
"s_nop 0\n" \
"v_mfma_f32_16x16x32_bf16 a[232:235], v[168:171], v[80:83], a[232:235]\n" \
"buffer_load_dwordx4 v24, s[76:79], s84 offen lds\n" \
"v_mfma_f32_16x16x32_bf16 a[232:235], v[164:167], v[76:79], a[232:235]\n" \
"ds_read_b64_tr_b16 v[132:133], v38 offset:32768\n" \
"ds_read_b64_tr_b16 v[134:135], v38 offset:33280\n" \
"v_mfma_f32_16x16x32_bf16 a[236:239], v[172:175], v[76:79], a[236:239]\n" \
"ds_read_b64_tr_b16 v[136:137], v38 offset:36864\n" \
"ds_read_b64_tr_b16 v[138:139], v38 offset:37376\n" \
"v_mfma_f32_16x16x32_bf16 a[236:239], v[176:179], v[80:83], a[236:239]\n" \
"s_add_u32 m0, %[mA], 0x400\n" \
"s_add_u32 s85, s62, 0x2400\n" \
"v_mfma_f32_16x16x32_bf16 a[144:147], v[152:155], v[88:91], a[144:147]\n" \
"buffer_load_dwordx4 v25, s[76:79], s85 offen lds\n" \
"v_mfma_f32_16x16x32_bf16 a[144:147], v[148:151], v[84:87], a[144:147]\n" \
"ds_read_b128 v[52:55], v28 offset:32768\n" \
"ds_read_b128 v[56:59], v29 offset:32768\n" \
"v_mfma_f32_16x16x32_bf16 a[148:151], v[156:159], v[84:87], a[148:151]\n" \
"s_add_u32 m0, %[mB], 0x0\n" \
"s_add_u32 s84, s63, 0x0\n" \
"v_mfma_f32_16x16x32_bf16 a[148:151], v[160:163], v[88:91], a[148:151]\n" \
"buffer_load_dwordx4 v26, s[68:71], s84 offen lds\n" \
"v_mfma_f32_16x16x32_bf16 a[152:155], v[168:171], v[88:91], a[152:155]\n" \
"ds_read_b64_tr_b16 v[116:117], v36 offset:32768\n" \
"ds_read_b64_tr_b16 v[118:119], v36 offset:33280\n" \
"v_mfma_f32_16x16x32_bf16 a[152:155], v[164:167], v[84:87], a[152:155]\n" \
"ds_read_b64_tr_b16 v[120:121], v36 offset:36864\n" \
"L_pf7:\n" \
"s_cbranch_execz L_pf11\n" \
"s_nop 0\n" \
"ds_read_b64_tr_b16 v[122:123], v36 offset:37376\n" \
"v_mfma_f32_16x16x32_bf16 a[156:159], v[172:175], v[84:87], a[156:159]\n" \
"s_add_u32 m0, %[mA], 0x800\n" \
"s_add_u32 s85, s62, 0x4800\n" \
"v_mfma_f32_16x16x32_bf16 a[156:159], v[176:179], v[88:91], a[156:159]\n" \
"buffer_load_dwordx4 v24, s[76:79], s85 offen lds\n" \
"v_mfma_f32_16x16x32_bf16 a[176:179], v[152:155], v[96:99], a[176:179]\n" \
"ds_read_b128 v[84:87], v22 offset:32768\n" \
"ds_read_b128 v[88:91], v23 offset:32768\n" \
"v_mfma_f32_16x16x32_bf16 a[176:179], v[148:151], v[92:95], a[176:179]\n" \
"s_add_u32 m0, %[mA], 0xc00\n" \
"s_add_u32 s84, s62, 0x6c00\n" \
"v_mfma_f32_16x16x32_bf16 a[180:183], v[156:159], v[92:95], a[180:183]\n" \
"buffer_load_dwordx4 v25, s[76:79], s84 offen lds\n" \
"v_mfma_f32_16x16x32_bf16 a[180:183], v[160:163], v[96:99], a[180:183]\n" \
"ds_read_b128 v[60:63], v28 offset:34816\n" \
"ds_read_b128 v[64:67], v29 offset:34816\n" \
"v_mfma_f32_16x16x32_bf16 a[184:187], v[168:171], v[96:99], a[184:187]\n" \
"ds_read_b128 v[68:71], v28 offset:36864\n" \
"ds_read_b128 v[72:75], v29 offset:36864\n" \
"v_mfma_f32_16x16x32_bf16 a[184:187], v[164:167], v[92:95], a[184:187]\n" \
"s_add_u32 m0, %[mB], 0x400\n" \
"s_add_u32 s85, s63, 0x8000\n" \
"v_mfma_f32_16x16x32_bf16 a[188:191], v[172:175], v[92:95], a[188:191]\n" \
"buffer_load_dwordx4 v26, s[68:71], s85 offen lds\n" \
"v_mfma_f32_16x16x32_bf16 a[188:191], v[176:179], v[96:99], a[188:191]\n" \
"ds_read_b128 v[76:79], v28 offset:38912\n" \
"ds_read_b128 v[80:83], v29 offset:38912\n" \
"v_mfma_f32_16x16x32_bf16 a[208:211], v[152:155], v[104:107], a[208:211]\n" \
"s_add_u32 m0, %[mA], 0x1000\n" \
"s_add_u32 s84, s62, 0x9000\n" \
"v_mfma_f32_16x16x32_bf16 a[208:211], v[148:151], v[100:103], a[208:211]\n" \
"buffer_load_dwordx4 v24, s[76:79], s84 offen lds\n" \
"v_mfma_f32_16x16x32_bf16 a[212:215], v[156:159], v[100:103], a[212:215]\n" \
"ds_read_b128 v[92:95], v48 offset:34816\n" \
"ds_read_b128 v[96:99], v49 offset:34816\n" \
"v_mfma_f32_16x16x32_bf16 a[212:215], v[160:163], v[104:107], a[212:215]\n" \
"v_writelane_b32 v11, s96, 13\n" \
"v_writelane_b32 v11, s97, 14\n" \
"v_mfma_f32_16x16x32_bf16 a[216:219], v[168:171], v[104:107], a[216:219]\n" \
"s_add_u32 m0, %[mA], 0x1400\n" \
"s_add_u32 s85, s62, 0xb400\n" \
"v_mfma_f32_16x16x32_bf16 a[216:219], v[164:167], v[100:103], a[216:219]\n" \
"buffer_load_dwordx4 v25, s[76:79], s85 offen lds\n" \
"v_mfma_f32_16x16x32_bf16 a[220:223], v[172:175], v[100:103], a[220:223]\n" \
"ds_read_b128 v[100:103], v48 offset:36864\n" \
"v_writelane_b32 v11, s20, 11\n" \
"v_mfma_f32_16x16x32_bf16 a[220:223], v[176:179], v[104:107], a[220:223]\n" \
"s_add_u32 m0, %[mB], 0x800\n" \
"s_add_u32 s84, s63, 0x10000\n" \
"v_mfma_f32_16x16x32_bf16 a[240:243], v[152:155], v[112:115], a[240:243]\n" \
"buffer_load_dwordx4 v27, s[68:71], s84 offen lds\n" \
"v_mfma_f32_16x16x32_bf16 a[240:243], v[148:151], v[108:111], a[240:243]\n" \
"ds_read_b64_tr_b16 v[152:153], v32 offset:40960\n" \
"ds_read_b64_tr_b16 v[154:155], v32 offset:41984\n" \
"v_mfma_f32_16x16x32_bf16 a[244:247], v[156:159], v[108:111], a[244:247]\n" \
"ds_read_b64_tr_b16 v[148:149], v32 offset:32768\n" \
"ds_read_b64_tr_b16 v[150:151], v32 offset:33792\n" \
"v_mfma_f32_16x16x32_bf16 a[244:247], v[160:163], v[112:115], a[244:247]\n" \
"s_add_u32 m0, %[mA], 0x1800\n" \
"s_add_u32 s85, s62, 0xd800\n" \
"v_mfma_f32_16x16x32_bf16 a[248:251], v[168:171], v[112:115], a[248:251]\n" \
"buffer_load_dwordx4 v24, s[76:79], s85 offen lds\n" \
"v_mfma_f32_16x16x32_bf16 a[248:251], v[164:167], v[108:111], a[248:251]\n" \
"ds_read_b64_tr_b16 v[156:157], v33 offset:32768\n" \
"ds_read_b64_tr_b16 v[158:159], v33 offset:33792\n" \
"v_mfma_f32_16x16x32_bf16 a[252:255], v[172:175], v[108:111], a[252:255]\n" \
"ds_read_b64_tr_b16 v[160:161], v33 offset:40960\n" \
"ds_read_b64_tr_b16 v[162:163], v33 offset:41984\n" \
"v_mfma_f32_16x16x32_bf16 a[252:255], v[176:179], v[112:115], a[252:255]\n" \
"s_add_u32 m0, %[mA], 0x1c00\n" \
"s_waitcnt lgkmcnt(11)\n" \
"s_nop 0\n" \
"v_smfmac_f32_16x16x64_bf16 a[16:19], v[140:143], v[84:91], v50\n" \
"v_mfma_f32_16x16x32_bf16 a[48:51], v[128:131], v[88:91], a[48:51]\n" \
"s_add_u32 s85, s62, 0xfc00\n" \
"ds_read_b64_tr_b16 v[168:169], v34 offset:40960\n" \
"v_mfma_f32_16x16x32_bf16 a[48:51], v[124:127], v[84:87], a[48:51]\n" \
"buffer_load_dwordx4 v25, s[76:79], s85 offen lds\n" \
"v_mfma_f32_16x16x32_bf16 a[80:83], v[84:87], v[132:135], a[80:83]\n" \
"s_add_u32 m0, %[mB], 0xc00\n" \
"s_add_u32 s84, s63, 0x18000\n" \
"v_mfma_f32_16x16x32_bf16 a[80:83], v[88:91], v[136:139], a[80:83]\n" \
"ds_read_b64_tr_b16 v[170:171], v34 offset:41984\n" \
"ds_read_b64_tr_b16 v[164:165], v34 offset:32768\n" \
"v_smfmac_f32_16x16x64_bf16 a[112:115], v[140:143], v[52:59], v50\n" \
"v_mfma_f32_16x16x32_bf16 a[20:23], v[128:131], v[56:59], a[20:23]\n" \
"buffer_load_dwordx4 v27, s[68:71], s84 offen lds\n" \
"v_mfma_f32_16x16x32_bf16 a[20:23], v[124:127], v[52:55], a[20:23]\n" \
"s_add_u32 s88, s43, 0x0\n" \
"s_nop 0\n" \
"ds_read_b64_tr_b16 v[166:167], v34 offset:33792\n" \
"v_mfma_f32_16x16x32_bf16 a[52:55], v[116:119], v[52:55], a[52:55]\n" \
"ds_read_b64_tr_b16 v[172:173], v35 offset:32768\n" \
"ds_read_b64_tr_b16 v[174:175], v35 offset:33792\n" \
"v_mfma_f32_16x16x32_bf16 a[52:55], v[120:123], v[56:59], a[52:55]\n" \
"buffer_store_dwordx4 v[248:251], v20, s[80:83], s88 offen offset:0 sc1\n" \
"v_mfma_f32_16x16x32_bf16 a[84:87], v[56:59], v[136:139], a[84:87]\n" \
"ds_read_b64_tr_b16 v[176:177], v35 offset:40960\n" \
"ds_read_b64_tr_b16 v[178:179], v35 offset:41984\n" \
"v_mfma_f32_16x16x32_bf16 a[84:87], v[52:55], v[132:135], a[84:87]\n" \
"ds_read_b128 v[84:87], v48 offset:32768\n" \
"ds_read_b128 v[88:91], v49 offset:32768\n" \
"v_smfmac_f32_16x16x64_bf16 a[116:119], v[140:143], v[60:67], v50\n" \
"v_mfma_f32_16x16x32_bf16 a[24:27], v[124:127], v[60:63], a[24:27]\n" \
"ds_read_b128 v[104:107], v49 offset:36864\n" \
"ds_read_b128 v[108:111], v48 offset:38912\n" \
"v_mfma_f32_16x16x32_bf16 a[24:27], v[128:131], v[64:67], a[24:27]\n" \
"s_add_u32 s89, s45, 0x1000\n" \
"ds_read_b128 v[112:115], v49 offset:38912\n" \
"v_mfma_f32_16x16x32_bf16 a[56:59], v[120:123], v[64:67], a[56:59]\n" \
"buffer_store_dwordx4 v[184:187], v20, s[80:83], s89 offen offset:2048 sc1\n" \
"v_mfma_f32_16x16x32_bf16 a[56:59], v[116:119], v[60:63], a[56:59]\n" \
"s_add_u32 s62, s62, 0x80\n" \
"s_and_b32 s95, s21, 0xffff\n" \
"v_mfma_f32_16x16x32_bf16 a[88:91], v[60:63], v[132:135], a[88:91]\n" \
"s_cmp_lt_u32 s62, 0x480\n" \
"s_cselect_b32 s62, s62, 0\n" \
"v_mfma_f32_16x16x32_bf16 a[88:91], v[64:67], v[136:139], a[88:91]\n" \
"s_lshl_b32 s72, s62, 6\n" \
"s_add_u32 s72, s72, s37\n" \
"v_smfmac_f32_16x16x64_bf16 a[120:123], v[140:143], v[68:75], v50\n" \
"v_mfma_f32_16x16x32_bf16 a[28:31], v[128:131], v[72:75], a[28:31]\n" \
"s_lshl_b32 s63, s62, 12\n" \
"v_writelane_b32 v11, s95, 12\n" \
"v_mfma_f32_16x16x32_bf16 a[28:31], v[124:127], v[68:71], a[28:31]\n" \
"s_and_b32 s95, 1, %[w]\n" \
"s_lshr_b32 s96, %[w], 1\n" \
"v_mfma_f32_16x16x32_bf16 a[60:63], v[116:119], v[68:71], a[60:63]\n" \
"s_add_u32 s88, s45, 0x1000\n" \
"s_lshl_b32 s94, %[w], 11\n" \
"v_mfma_f32_16x16x32_bf16 a[60:63], v[120:123], v[72:75], a[60:63]\n" \
"buffer_store_dwordx4 v[252:255], v20, s[80:83], s88 offen offset:3072 sc1\n" \
"v_mfma_f32_16x16x32_bf16 a[92:95], v[72:75], v[136:139], a[92:95]\n" \
"s_lshl_b32 s15, s95, 12\n" \
"s_lshl_b32 s16, s96, 14\n" \
"v_mfma_f32_16x16x32_bf16 a[92:95], v[68:71], v[132:135], a[92:95]\n" \
"s_add_u32 s12, 0xc000, s94\n" \
"s_add_u32 s97, s16, s15\n" \
"v_smfmac_f32_16x16x64_bf16 a[124:127], v[140:143], v[76:83], v50\n" \
"v_mfma_f32_16x16x32_bf16 a[0:3], v[124:127], v[76:79], a[0:3]\n" \
"s_lshr_b32 s100, %[b], 5\n" \
"s_and_b32 s65, 7, %[b]\n" \
"v_mfma_f32_16x16x32_bf16 a[0:3], v[128:131], v[80:83], a[0:3]\n" \
"s_lshr_b32 s67, %[b], 3\n" \
"s_xor_b32 s13, 0x8000, s97\n" \
"v_mfma_f32_16x16x32_bf16 a[4:7], v[120:123], v[80:83], a[4:7]\n" \
"s_add_u32 s20, 0x14000, s97\n" \
"s_mul_i32 s98, 0xe000, s96\n" \
"v_mfma_f32_16x16x32_bf16 a[4:7], v[116:119], v[76:79], a[4:7]\n" \
"s_lshl_b32 s18, s100, 1\n" \
"s_lshl_b32 s66, s65, 2\n" \
"v_mfma_f32_16x16x32_bf16 a[32:35], v[76:79], v[132:135], a[32:35]\n" \
"s_and_b32 s94, 3, s67\n" \
"s_add_u32 s99, s98, s15\n" \
"v_mfma_f32_16x16x32_bf16 a[32:35], v[80:83], v[136:139], a[32:35]\n" \
"s_add_u32 s101, s18, s96\n" \
"s_waitcnt lgkmcnt(0)\n" \
"v_mfma_f32_16x16x32_bf16 a[128:131], v[152:155], v[56:59], a[128:131]\n" \
"s_mov_b32 s64, 0x4000\n" \
"s_add_u32 s97, s66, s94\n" \
"v_mfma_f32_16x16x32_bf16 a[128:131], v[148:151], v[52:55], a[128:131]\n" \
"s_add_u32 s21, 0x10000, s99\n" \
"s_lshl_b32 s14, s101, 15\n" \
"v_mfma_f32_16x16x32_bf16 a[132:135], v[156:159], v[52:55], a[132:135]\n" \
"s_cmp_lg_u32 s100, 0\n" \
"s_cselect_b32 s17, s64, 0x8000\n" \
"v_mfma_f32_16x16x32_bf16 a[132:135], v[160:163], v[56:59], a[132:135]\n" \
"s_lshl_b32 s98, s97, 8\n" \
"s_and_b32 s67, 2, %[w]\n" \
"v_mfma_f32_16x16x32_bf16 a[136:139], v[168:171], v[56:59], a[136:139]\n" \
"s_add_u32 s99, 0x5be8020, s98\n" \
"s_lshl_b32 s101, s100, 2\n" \
"v_mfma_f32_16x16x32_bf16 a[136:139], v[164:167], v[52:55], a[136:139]\n" \
"s_lshl_b32 s66, s67, 1\n" \
"s_add_u32 s64, s99, s101\n" \
"v_mfma_f32_16x16x32_bf16 a[140:143], v[172:175], v[52:55], a[140:143]\n" \
"s_lshl_b32 s65, s100, 3\n" \
"s_add_u32 s94, s66, s95\n" \
"v_mfma_f32_16x16x32_bf16 a[140:143], v[176:179], v[56:59], a[140:143]\n" \
"s_cmp_eq_u32 %[w], 1\n" \
"s_cselect_b32 s22, s64, 0x40000000\n" \
"v_mfma_f32_16x16x32_bf16 a[160:163], v[152:155], v[64:67], a[160:163]\n" \
"s_add_u32 s98, s65, s94\n" \
"s_mul_i32 s99, 0x24800, s98\n" \
"v_mfma_f32_16x16x32_bf16 a[160:163], v[148:151], v[60:63], a[160:163]\n" \
"s_lshl_b32 s101, s97, 12\n" \
"s_sub_u32 s67, 0x78, s65\n" \
"v_mfma_f32_16x16x32_bf16 a[164:167], v[156:159], v[60:63], a[164:167]\n" \
"s_add_u32 s64, s99, s101\n" \
"s_add_u32 s66, s67, s94\n" \
"v_mfma_f32_16x16x32_bf16 a[164:167], v[160:163], v[64:67], a[164:167]\n" \
"s_add_u32 s60, 0x3004800, s64\n" \
"s_mul_i32 s95, 0x24800, s66\n" \
"v_mfma_f32_16x16x32_bf16 a[168:171], v[168:171], v[64:67], a[168:171]\n" \
"s_lshl_b32 s99, s96, 1\n" \
"s_add_u32 s98, s95, s101\n" \
"v_mfma_f32_16x16x32_bf16 a[168:171], v[164:167], v[60:63], a[168:171]\n" \
"s_add_u32 s64, s100, s99\n" \
"s_mov_b32 s65, 0x18000000\n" \
"v_mfma_f32_16x16x32_bf16 a[172:175], v[172:175], v[60:63], a[172:175]\n" \
"s_mul_i32 s94, 6, s96\n" \
"s_add_u32 s66, s96, s94\n" \
"v_mfma_f32_16x16x32_bf16 a[172:175], v[176:179], v[64:67], a[172:175]\n" \
"s_cmp_lt_u32 s64, 2\n" \
"s_cselect_b32 s67, s65, 0x30000000\n" \
"v_mfma_f32_16x16x32_bf16 a[192:195], v[152:155], v[72:75], a[192:195]\n" \
"s_mov_b32 s95, 0x80000\n" \
"s_mov_b32 s64, 0xaaaaaaaa\n" \
"v_mfma_f32_16x16x32_bf16 a[192:195], v[148:151], v[68:71], a[192:195]\n" \
"s_cmp_gt_u32 s66, s100\n" \
"s_cselect_b32 s101, s95, 0x40000\n" \
"s_waitcnt vmcnt(3)\n" \
"s_barrier\n" \
"s_mov_b64 exec, 1\n" \
"buffer_store_dword v45, off, s[80:83], s38 offset:0 sc1\n" \
"s_mov_b64 exec, -1\n" \
"v_mfma_f32_16x16x32_bf16 a[196:199], v[156:159], v[68:71], a[196:199]\n" \
"s_add_u32 m0, s40, 0x8000\n" \
"s_add_u32 s85, s72, 0x0\n" \
"s_nop 0\n" \
"v_mfma_f32_16x16x32_bf16 a[196:199], v[160:163], v[72:75], a[196:199]\n" \
"ds_read_b64_tr_b16 v[140:141], v39 offset:0\n" \
"ds_read_b64_tr_b16 v[142:143], v39 offset:960\n" \
"v_mfma_f32_16x16x32_bf16 a[200:203], v[168:171], v[72:75], a[200:203]\n" \
"buffer_load_dwordx4 v20, s[80:83], s85 offen sc1 lds\n" \
"v_mfma_f32_16x16x32_bf16 a[200:203], v[164:167], v[68:71], a[200:203]\n" \
"s_add_u32 m0, s40, 0x9000\n" \
"s_add_u32 s84, s72, 0x1000\n" \
"v_mfma_f32_16x16x32_bf16 a[204:207], v[172:175], v[68:71], a[204:207]\n" \
"ds_read_b64_tr_b16 v[128:129], v37 offset:4096\n" \
"ds_read_b64_tr_b16 v[130:131], v37 offset:4608\n" \
"v_mfma_f32_16x16x32_bf16 a[204:207], v[176:179], v[72:75], a[204:207]\n" \
"buffer_load_dwordx4 v20, s[80:83], s84 offen sc1 lds\n" \
"v_mfma_f32_16x16x32_bf16 a[224:227], v[152:155], v[80:83], a[224:227]\n" \
"s_add_u32 m0, s40, 0xb000\n" \
"s_add_u32 s85, s62, 0x0\n" \
"v_mfma_f32_16x16x32_bf16 a[224:227], v[148:151], v[76:79], a[224:227]\n" \
"ds_read_b64_tr_b16 v[124:125], v37 offset:0\n" \
"ds_read_b64_tr_b16 v[126:127], v37 offset:512\n" \
"v_mfma_f32_16x16x32_bf16 a[228:231], v[156:159], v[76:79], a[228:231]\n" \
"buffer_load_dwordx4 v21, s[76:79], s85 offen lds\n" \
"v_mfma_f32_16x16x32_bf16 a[228:231], v[160:163], v[80:83], a[228:231]\n" \
"s_add_u32 m0, %[mA], 0x8000\n" \
"s_add_u32 s84, s62, 0x0\n" \
"v_mfma_f32_16x16x32_bf16 a[232:235], v[168:171], v[80:83], a[232:235]\n" \
"ds_read_b64_tr_b16 v[132:133], v38 offset:0\n" \
"ds_read_b64_tr_b16 v[134:135], v38 offset:512\n" \
"v_mfma_f32_16x16x32_bf16 a[232:235], v[164:167], v[76:79], a[232:235]\n" \
"buffer_load_dwordx4 v24, s[76:79], s84 offen lds\n" \
"v_mfma_f32_16x16x32_bf16 a[236:239], v[172:175], v[76:79], a[236:239]\n" \
"ds_read_b64_tr_b16 v[136:137], v38 offset:4096\n" \
"ds_read_b64_tr_b16 v[138:139], v38 offset:4608\n" \
"v_mfma_f32_16x16x32_bf16 a[236:239], v[176:179], v[80:83], a[236:239]\n" \
"s_add_u32 m0, %[mA], 0x8400\n" \
"s_add_u32 s85, s62, 0x2400\n" \
"v_mfma_f32_16x16x32_bf16 a[144:147], v[152:155], v[88:91], a[144:147]\n" \
"buffer_load_dwordx4 v25, s[76:79], s85 offen lds\n" \
"v_mfma_f32_16x16x32_bf16 a[144:147], v[148:151], v[84:87], a[144:147]\n" \
"ds_read_b128 v[52:55], v28 offset:0\n" \
"ds_read_b128 v[56:59], v29 offset:0\n" \
"v_mfma_f32_16x16x32_bf16 a[148:151], v[156:159], v[84:87], a[148:151]\n" \
"s_add_u32 m0, %[mB], 0x8000\n" \
"s_add_u32 s84, s63, 0x0\n" \
"s_nop 0\n" \
"v_mfma_f32_16x16x32_bf16 a[148:151], v[160:163], v[88:91], a[148:151]\n" \
"buffer_load_dwordx4 v26, s[68:71], s84 offen lds\n" \
"v_mfma_f32_16x16x32_bf16 a[152:155], v[168:171], v[88:91], a[152:155]\n" \
"ds_read_b64_tr_b16 v[116:117], v36 offset:0\n" \
"ds_read_b64_tr_b16 v[118:119], v36 offset:512\n" \
"v_mfma_f32_16x16x32_bf16 a[152:155], v[164:167], v[84:87], a[152:155]\n" \
"ds_read_b64_tr_b16 v[120:121], v36 offset:4096\n" \
"ds_read_b64_tr_b16 v[122:123], v36 offset:4608\n" \
"v_mfma_f32_16x16x32_bf16 a[156:159], v[172:175], v[84:87], a[156:159]\n" \
"s_add_u32 m0, %[mA], 0x8800\n" \
"s_add_u32 s85, s62, 0x4800\n" \
"v_mfma_f32_16x16x32_bf16 a[156:159], v[176:179], v[88:91], a[156:159]\n" \
"buffer_load_dwordx4 v24, s[76:79], s85 offen lds\n" \
"v_mfma_f32_16x16x32_bf16 a[176:179], v[152:155], v[96:99], a[176:179]\n" \
"ds_read_b128 v[84:87], v22 offset:0\n" \
"ds_read_b128 v[88:91], v23 offset:0\n" \
"v_mfma_f32_16x16x32_bf16 a[176:179], v[148:151], v[92:95], a[176:179]\n" \
"s_add_u32 m0, %[mA], 0x8c00\n" \
"s_add_u32 s84, s62, 0x6c00\n" \
"v_mfma_f32_16x16x32_bf16 a[180:183], v[156:159], v[92:95], a[180:183]\n" \
"buffer_load_dwordx4 v25, s[76:79], s84 offen lds\n" \
"v_mfma_f32_16x16x32_bf16 a[180:183], v[160:163], v[96:99], a[180:183]\n" \
"ds_read_b128 v[60:63], v28 offset:2048\n" \
"ds_read_b128 v[64:67], v29 offset:2048\n" \
"v_mfma_f32_16x16x32_bf16 a[184:187], v[168:171], v[96:99], a[184:187]\n" \
"ds_read_b128 v[68:71], v28 offset:4096\n" \
"ds_read_b128 v[72:75], v29 offset:4096\n" \
"v_mfma_f32_16x16x32_bf16 a[184:187], v[164:167], v[92:95], a[184:187]\n" \
"s_add_u32 m0, %[mB], 0x8400\n" \
"s_add_u32 s85, s63, 0x8000\n" \
"v_mfma_f32_16x16x32_bf16 a[188:191], v[172:175], v[92:95], a[188:191]\n" \
"buffer_load_dwordx4 v26, s[68:71], s85 offen lds\n" \
"v_mfma_f32_16x16x32_bf16 a[188:191], v[176:179], v[96:99], a[188:191]\n" \
"ds_read_b128 v[76:79], v28 offset:6144\n" \
"ds_read_b128 v[80:83], v29 offset:6144\n" \
"v_mfma_f32_16x16x32_bf16 a[208:211], v[152:155], v[104:107], a[208:211]\n" \
"s_add_u32 m0, %[mA], 0x9000\n" \
"s_add_u32 s84, s62, 0x9000\n" \
"v_mfma_f32_16x16x32_bf16 a[208:211], v[148:151], v[100:103], a[208:211]\n" \
"buffer_load_dwordx4 v24, s[76:79], s84 offen lds\n" \
"v_mfma_f32_16x16x32_bf16 a[212:215], v[156:159], v[100:103], a[212:215]\n" \
"ds_read_b128 v[92:95], v48 offset:2048\n" \
"ds_read_b128 v[96:99], v49 offset:2048\n" \
"v_mfma_f32_16x16x32_bf16 a[212:215], v[160:163], v[104:107], a[212:215]\n" \
"s_mov_b32 s65, 0xaaaaaaaa\n" \
"s_add_u32 s23, s67, s101\n" \
"s_nop 0\n" \
"v_mfma_f32_16x16x32_bf16 a[216:219], v[168:171], v[104:107], a[216:219]\n" \
"s_add_u32 m0, %[mA], 0x9400\n" \
"s_add_u32 s85, s62, 0xb400\n" \
"v_mfma_f32_16x16x32_bf16 a[216:219], v[164:167], v[100:103], a[216:219]\n" \
"buffer_load_dwordx4 v25, s[76:79], s85 offen lds\n" \
"v_mfma_f32_16x16x32_bf16 a[220:223], v[172:175], v[100:103], a[220:223]\n" \
"ds_read_b128 v[100:103], v48 offset:4096\n" \
"s_mov_b32 s66, 0xfff0ffff\n" \
"v_mfma_f32_16x16x32_bf16 a[220:223], v[176:179], v[104:107], a[220:223]\n" \
"s_add_u32 m0, %[mB], 0x8800\n" \
"s_add_u32 s84, s63, 0x10000\n" \
"v_mfma_f32_16x16x32_bf16 a[240:243], v[152:155], v[112:115], a[240:243]\n" \
"buffer_load_dwordx4 v27, s[68:71], s84 offen lds\n" \
"v_mfma_f32_16x16x32_bf16 a[240:243], v[148:151], v[108:111], a[240:243]\n" \
"ds_read_b64_tr_b16 v[152:153], v32 offset:8192\n" \
"ds_read_b64_tr_b16 v[154:155], v32 offset:9216\n" \
"v_mfma_f32_16x16x32_bf16 a[244:247], v[156:159], v[108:111], a[244:247]\n" \
"ds_read_b64_tr_b16 v[148:149], v32 offset:0\n" \
"ds_read_b64_tr_b16 v[150:151], v32 offset:1024\n" \
"v_mfma_f32_16x16x32_bf16 a[244:247], v[160:163], v[112:115], a[244:247]\n" \
"s_add_u32 m0, %[mA], 0x9800\n" \
"s_add_u32 s85, s62, 0xd800\n" \
"v_mfma_f32_16x16x32_bf16 a[248:251], v[168:171], v[112:115], a[248:251]\n" \
"buffer_load_dwordx4 v24, s[76:79], s85 offen lds\n" \
"v_mfma_f32_16x16x32_bf16 a[248:251], v[164:167], v[108:111], a[248:251]\n" \
"ds_read_b64_tr_b16 v[156:157], v33 offset:0\n" \
"ds_read_b64_tr_b16 v[158:159], v33 offset:1024\n" \
"v_mfma_f32_16x16x32_bf16 a[252:255], v[172:175], v[108:111], a[252:255]\n" \
"ds_read_b64_tr_b16 v[160:161], v33 offset:8192\n" \
"ds_read_b64_tr_b16 v[162:163], v33 offset:9216\n" \
"v_mfma_f32_16x16x32_bf16 a[252:255], v[176:179], v[112:115], a[252:255]\n" \
"s_add_u32 m0, %[mA], 0x9c00\n" \
"s_waitcnt lgkmcnt(11)\n" \
"s_nop 0\n" \
"v_smfmac_f32_16x16x64_bf16 a[16:19], v[140:143], v[84:91], v50\n" \
"v_mfma_f32_16x16x32_bf16 a[48:51], v[128:131], v[88:91], a[48:51]\n" \
"s_add_u32 s85, s62, 0xfc00\n" \
"ds_read_b64_tr_b16 v[168:169], v34 offset:8192\n" \
"v_mfma_f32_16x16x32_bf16 a[48:51], v[124:127], v[84:87], a[48:51]\n" \
"buffer_load_dwordx4 v25, s[76:79], s85 offen lds\n" \
"v_mfma_f32_16x16x32_bf16 a[80:83], v[84:87], v[132:135], a[80:83]\n" \
"s_add_u32 m0, %[mB], 0x8c00\n" \
"s_add_u32 s84, s63, 0x18000\n" \
"v_mfma_f32_16x16x32_bf16 a[80:83], v[88:91], v[136:139], a[80:83]\n" \
"ds_read_b64_tr_b16 v[170:171], v34 offset:9216\n" \
"ds_read_b64_tr_b16 v[164:165], v34 offset:0\n" \
"v_smfmac_f32_16x16x64_bf16 a[112:115], v[140:143], v[52:59], v50\n" \
"v_mfma_f32_16x16x32_bf16 a[20:23], v[128:131], v[56:59], a[20:23]\n" \
"buffer_load_dwordx4 v27, s[68:71], s84 offen lds\n" \
"v_mfma_f32_16x16x32_bf16 a[20:23], v[124:127], v[52:55], a[20:23]\n" \
"s_add_u32 s89, s43, 0x1000\n" \
"ds_read_b64_tr_b16 v[166:167], v34 offset:1024\n" \
"v_mfma_f32_16x16x32_bf16 a[52:55], v[116:119], v[52:55], a[52:55]\n" \
"ds_read_b64_tr_b16 v[172:173], v35 offset:0\n" \
"ds_read_b64_tr_b16 v[174:175], v35 offset:1024\n" \
"v_mfma_f32_16x16x32_bf16 a[52:55], v[120:123], v[56:59], a[52:55]\n" \
"buffer_store_dwordx4 a[36:39], v20, s[80:83], s89 offen offset:0 sc1\n" \
"v_mfma_f32_16x16x32_bf16 a[84:87], v[56:59], v[136:139], a[84:87]\n" \
"ds_read_b64_tr_b16 v[176:177], v35 offset:8192\n" \
"ds_read_b64_tr_b16 v[178:179], v35 offset:9216\n" \
"v_mfma_f32_16x16x32_bf16 a[84:87], v[52:55], v[132:135], a[84:87]\n" \
"ds_read_b128 v[84:87], v48 offset:0\n" \
"ds_read_b128 v[88:91], v49 offset:0\n" \
"v_smfmac_f32_16x16x64_bf16 a[116:119], v[140:143], v[60:67], v50\n" \
"v_mfma_f32_16x16x32_bf16 a[24:27], v[124:127], v[60:63], a[24:27]\n" \
"ds_read_b128 v[104:107], v49 offset:4096\n" \
"ds_read_b128 v[108:111], v48 offset:6144\n" \
"v_mfma_f32_16x16x32_bf16 a[24:27], v[128:131], v[64:67], a[24:27]\n" \
"s_add_u32 s88, s43, 0x2000\n" \
"ds_read_b128 v[112:115], v49 offset:6144\n" \
"v_mfma_f32_16x16x32_bf16 a[56:59], v[120:123], v[64:67], a[56:59]\n" \
"buffer_store_dwordx4 a[64:67], v20, s[80:83], s88 offen offset:0 sc1\n" \
"v_mfma_f32_16x16x32_bf16 a[56:59], v[116:119], v[60:63], a[56:59]\n" \
"s_add_u32 s62, s62, 0x80\n" \
"s_mov_b32 s67, 0xf000ff00\n" \
"v_mfma_f32_16x16x32_bf16 a[88:91], v[60:63], v[132:135], a[88:91]\n" \
"s_cmp_lt_u32 s62, 0x480\n" \
"s_cselect_b32 s62, s62, 0\n" \
"v_mfma_f32_16x16x32_bf16 a[88:91], v[64:67], v[136:139], a[88:91]\n" \
"s_lshl_b32 s72, s62, 6\n" \
"s_add_u32 s72, s72, s37\n" \
"v_smfmac_f32_16x16x64_bf16 a[120:123], v[140:143], v[68:75], v50\n" \
"v_mfma_f32_16x16x32_bf16 a[28:31], v[128:131], v[72:75], a[28:31]\n" \
"s_lshl_b32 s63, s62, 12\n" \
"s_add_u32 s61, 0x3004800, s98\n" \
"v_mfma_f32_16x16x32_bf16 a[28:31], v[124:127], v[68:71], a[28:31]\n" \
"s_lshl_b32 s99, s97, 20\n" \
"s_lshl_b32 s98, s100, 16\n" \
"v_mfma_f32_16x16x32_bf16 a[60:63], v[116:119], v[68:71], a[60:63]\n" \
"s_add_u32 s89, s43, 0x3000\n" \
"s_add_u32 s8, 0x1000000, s99\n" \
"v_mfma_f32_16x16x32_bf16 a[60:63], v[120:123], v[72:75], a[60:63]\n" \
"buffer_store_dwordx4 a[68:71], v20, s[80:83], s89 offen offset:0 sc1\n" \
"v_mfma_f32_16x16x32_bf16 a[92:95], v[72:75], v[136:139], a[92:95]\n" \
"s_sub_u32 s19, 14, s18\n" \
"s_sub_u32 s10, 0x100000, s98\n" \
"v_mfma_f32_16x16x32_bf16 a[92:95], v[68:71], v[132:135], a[92:95]\n" \
"s_mov_b32 s11, 0x20000\n" \
"s_mov_b32 s58, 0x6000000\n" \
"v_smfmac_f32_16x16x64_bf16 a[124:127], v[140:143], v[76:83], v50\n" \
"v_mfma_f32_16x16x32_bf16 a[0:3], v[124:127], v[76:79], a[0:3]\n" \
"s_mov_b32 s59, 0x20000\n" \
"s_mov_b64 s[56:57], s[80:81]\n" \
"v_mfma_f32_16x16x32_bf16 a[0:3], v[128:131], v[80:83], a[0:3]\n" \
"s_add_u32 s8, s8, s80\n" \
"s_addc_u32 s9, s81, 0\n" \
"v_mfma_f32_16x16x32_bf16 a[4:7], v[120:123], v[80:83], a[4:7]\n" \
"v_mfma_f32_16x16x32_bf16 a[4:7], v[116:119], v[76:79], a[4:7]\n" \
"v_mfma_f32_16x16x32_bf16 a[32:35], v[76:79], v[132:135], a[32:35]\n" \
"v_mfma_f32_16x16x32_bf16 a[32:35], v[80:83], v[136:139], a[32:35]\n" \
"s_waitcnt lgkmcnt(0)\n" \
"s_nop 0\n" \
"v_mfma_f32_16x16x32_bf16 a[128:131], v[152:155], v[56:59], a[128:131]\n" \
"v_mfma_f32_16x16x32_bf16 a[128:131], v[148:151], v[52:55], a[128:131]\n" \
"v_mfma_f32_16x16x32_bf16 a[132:135], v[156:159], v[52:55], a[132:135]\n" \
"v_mfma_f32_16x16x32_bf16 a[132:135], v[160:163], v[56:59], a[132:135]\n" \
"v_mfma_f32_16x16x32_bf16 a[136:139], v[168:171], v[56:59], a[136:139]\n" \
"v_mfma_f32_16x16x32_bf16 a[136:139], v[164:167], v[52:55], a[136:139]\n" \
"v_mfma_f32_16x16x32_bf16 a[140:143], v[172:175], v[52:55], a[140:143]\n" \
"v_mfma_f32_16x16x32_bf16 a[140:143], v[176:179], v[56:59], a[140:143]\n" \
"v_mfma_f32_16x16x32_bf16 a[160:163], v[152:155], v[64:67], a[160:163]\n" \
"v_mfma_f32_16x16x32_bf16 a[160:163], v[148:151], v[60:63], a[160:163]\n" \
"v_mfma_f32_16x16x32_bf16 a[164:167], v[156:159], v[60:63], a[164:167]\n" \
"v_mfma_f32_16x16x32_bf16 a[164:167], v[160:163], v[64:67], a[164:167]\n" \
"v_mfma_f32_16x16x32_bf16 a[168:171], v[168:171], v[64:67], a[168:171]\n" \
"v_mfma_f32_16x16x32_bf16 a[168:171], v[164:167], v[60:63], a[168:171]\n" \
"v_mfma_f32_16x16x32_bf16 a[172:175], v[172:175], v[60:63], a[172:175]\n" \
"v_mfma_f32_16x16x32_bf16 a[172:175], v[176:179], v[64:67], a[172:175]\n" \
"v_mfma_f32_16x16x32_bf16 a[192:195], v[152:155], v[72:75], a[192:195]\n" \
"v_mfma_f32_16x16x32_bf16 a[192:195], v[148:151], v[68:71], a[192:195]\n" \
"s_waitcnt vmcnt(3)\n" \
"s_barrier\n" \
"v_mfma_f32_16x16x32_bf16 a[196:199], v[156:159], v[68:71], a[196:199]\n" \
"s_add_u32 m0, s40, 0x0\n" \
"s_add_u32 s85, s72, 0x0\n" \
"v_mfma_f32_16x16x32_bf16 a[196:199], v[160:163], v[72:75], a[196:199]\n" \
"buffer_load_dwordx4 v20, s[80:83], s85 offen sc1 lds\n" \
"v_mfma_f32_16x16x32_bf16 a[200:203], v[168:171], v[72:75], a[200:203]\n" \
"s_add_u32 m0, s40, 0x1000\n" \
"s_add_u32 s84, s72, 0x1000\n" \
"v_mfma_f32_16x16x32_bf16 a[200:203], v[164:167], v[68:71], a[200:203]\n" \
"ds_read_b64_tr_b16 v[140:141], v39 offset:32768\n" \
"ds_read_b64_tr_b16 v[142:143], v39 offset:33728\n" \
"v_mfma_f32_16x16x32_bf16 a[204:207], v[172:175], v[68:71], a[204:207]\n" \
"buffer_load_dwordx4 v20, s[80:83], s84 offen sc1 lds\n" \
"v_mfma_f32_16x16x32_bf16 a[204:207], v[176:179], v[72:75], a[204:207]\n" \
"ds_read_b64_tr_b16 v[128:129], v37 offset:36864\n" \
"ds_read_b64_tr_b16 v[130:131], v37 offset:37376\n" \
"v_mfma_f32_16x16x32_bf16 a[224:227], v[152:155], v[80:83], a[224:227]\n" \
"s_add_u32 m0, s40, 0x3000\n" \
"s_add_u32 s85, s62, 0x0\n" \
"v_mfma_f32_16x16x32_bf16 a[224:227], v[148:151], v[76:79], a[224:227]\n" \
"buffer_load_dwordx4 v21, s[76:79], s85 offen lds\n" \
"v_mfma_f32_16x16x32_bf16 a[228:231], v[156:159], v[76:79], a[228:231]\n" \
"ds_read_b64_tr_b16 v[124:125], v37 offset:32768\n" \
"ds_read_b64_tr_b16 v[126:127], v37 offset:33280\n" \
"v_mfma_f32_16x16x32_bf16 a[228:231], v[160:163], v[80:83], a[228:231]\n" \
"s_add_u32 m0, %[mA], 0x0\n" \
"s_add_u32 s84, s62, 0x0\n" \
"s_nop 0\n" \
"v_mfma_f32_16x16x32_bf16 a[232:235], v[168:171], v[80:83], a[232:235]\n" \
"buffer_load_dwordx4 v24, s[76:79], s84 offen lds\n" \
"v_mfma_f32_16x16x32_bf16 a[232:235], v[164:167], v[76:79], a[232:235]\n" \
"ds_read_b64_tr_b16 v[132:133], v38 offset:32768\n" \
"ds_read_b64_tr_b16 v[134:135], v38 offset:33280\n" \
"v_mfma_f32_16x16x32_bf16 a[236:239], v[172:175], v[76:79], a[236:239]\n" \
"ds_read_b64_tr_b16 v[136:137], v38 offset:36864\n" \
"ds_read_b64_tr_b16 v[138:139], v38 offset:37376\n" \
"v_mfma_f32_16x16x32_bf16 a[236:239], v[176:179], v[80:83], a[236:239]\n" \
"s_add_u32 m0, %[mA], 0x400\n" \
"s_add_u32 s85, s62, 0x2400\n" \
"v_mfma_f32_16x16x32_bf16 a[144:147], v[152:155], v[88:91], a[144:147]\n" \
"buffer_load_dwordx4 v25, s[76:79], s85 offen lds\n" \
"v_mfma_f32_16x16x32_bf16 a[144:147], v[148:151], v[84:87], a[144:147]\n" \
"ds_read_b128 v[52:55], v28 offset:32768\n" \
"ds_read_b128 v[56:59], v29 offset:32768\n" \
"v_mfma_f32_16x16x32_bf16 a[148:151], v[156:159], v[84:87], a[148:151]\n" \
"s_add_u32 m0, %[mB], 0x0\n" \
"s_add_u32 s84, s63, 0x0\n" \
"v_mfma_f32_16x16x32_bf16 a[148:151], v[160:163], v[88:91], a[148:151]\n" \
"buffer_load_dwordx4 v26, s[68:71], s84 offen lds\n" \
"v_mfma_f32_16x16x32_bf16 a[152:155], v[168:171], v[88:91], a[152:155]\n" \
"ds_read_b64_tr_b16 v[116:117], v36 offset:32768\n" \
"ds_read_b64_tr_b16 v[118:119], v36 offset:33280\n" \
"v_mfma_f32_16x16x32_bf16 a[152:155], v[164:167], v[84:87], a[152:155]\n" \
"ds_read_b64_tr_b16 v[120:121], v36 offset:36864\n" \
"ds_read_b64_tr_b16 v[122:123], v36 offset:37376\n" \
"v_mfma_f32_16x16x32_bf16 a[156:159], v[172:175], v[84:87], a[156:159]\n" \
"s_add_u32 m0, %[mA], 0x800\n" \
"s_add_u32 s85, s62, 0x4800\n" \
"v_mfma_f32_16x16x32_bf16 a[156:159], v[176:179], v[88:91], a[156:159]\n" \
"buffer_load_dwordx4 v24, s[76:79], s85 offen lds\n" \
"v_mfma_f32_16x16x32_bf16 a[176:179], v[152:155], v[96:99], a[176:179]\n" \
"ds_read_b128 v[84:87], v22 offset:32768\n" \
"ds_read_b128 v[88:91], v23 offset:32768\n" \
"v_mfma_f32_16x16x32_bf16 a[176:179], v[148:151], v[92:95], a[176:179]\n" \
"s_add_u32 m0, %[mA], 0xc00\n" \
"s_add_u32 s84, s62, 0x6c00\n" \
"v_mfma_f32_16x16x32_bf16 a[180:183], v[156:159], v[92:95], a[180:183]\n" \
"buffer_load_dwordx4 v25, s[76:79], s84 offen lds\n" \
"v_mfma_f32_16x16x32_bf16 a[180:183], v[160:163], v[96:99], a[180:183]\n" \
"ds_read_b128 v[60:63], v28 offset:34816\n" \
"ds_read_b128 v[64:67], v29 offset:34816\n" \
"v_mfma_f32_16x16x32_bf16 a[184:187], v[168:171], v[96:99], a[184:187]\n" \
"ds_read_b128 v[68:71], v28 offset:36864\n" \
"ds_read_b128 v[72:75], v29 offset:36864\n" \
"v_mfma_f32_16x16x32_bf16 a[184:187], v[164:167], v[92:95], a[184:187]\n" \
"s_add_u32 m0, %[mB], 0x400\n" \
"s_add_u32 s85, s63, 0x8000\n" \
"v_mfma_f32_16x16x32_bf16 a[188:191], v[172:175], v[92:95], a[188:191]\n" \
"buffer_load_dwordx4 v26, s[68:71], s85 offen lds\n" \
"v_mfma_f32_16x16x32_bf16 a[188:191], v[176:179], v[96:99], a[188:191]\n" \
"ds_read_b128 v[76:79], v28 offset:38912\n" \
"ds_read_b128 v[80:83], v29 offset:38912\n" \
"v_mfma_f32_16x16x32_bf16 a[208:211], v[152:155], v[104:107], a[208:211]\n" \
"s_add_u32 m0, %[mA], 0x1000\n" \
"s_add_u32 s84, s62, 0x9000\n" \
"v_mfma_f32_16x16x32_bf16 a[208:211], v[148:151], v[100:103], a[208:211]\n" \
"buffer_load_dwordx4 v24, s[76:79], s84 offen lds\n" \
"v_mfma_f32_16x16x32_bf16 a[212:215], v[156:159], v[100:103], a[212:215]\n" \
"ds_read_b128 v[92:95], v48 offset:34816\n" \
"ds_read_b128 v[96:99], v49 offset:34816\n" \
"v_mfma_f32_16x16x32_bf16 a[212:215], v[160:163], v[104:107], a[212:215]\n" \
"s_mov_b32 s72, 0x3eae1a93\n" \
"v_and_b32_e32 v26, 0x1ff, v20\n" \
"v_mfma_f32_16x16x32_bf16 a[216:219], v[168:171], v[104:107], a[216:219]\n" \
"s_add_u32 m0, %[mA], 0x1400\n" \
"s_add_u32 s85, s62, 0xb400\n" \
"v_mfma_f32_16x16x32_bf16 a[216:219], v[164:167], v[100:103], a[216:219]\n" \
"buffer_load_dwordx4 v25, s[76:79], s85 offen lds\n" \
"v_mfma_f32_16x16x32_bf16 a[220:223], v[172:175], v[100:103], a[220:223]\n" \
"ds_read_b128 v[100:103], v48 offset:36864\n" \
"v_add_u32_e64 v21, s48, v20\n" \
"v_mfma_f32_16x16x32_bf16 a[220:223], v[176:179], v[104:107], a[220:223]\n" \
"s_add_u32 m0, %[mB], 0x800\n" \
"s_add_u32 s84, s63, 0x10000\n" \
"v_mfma_f32_16x16x32_bf16 a[240:243], v[152:155], v[112:115], a[240:243]\n" \
"buffer_load_dwordx4 v27, s[68:71], s84 offen lds\n" \
"v_mfma_f32_16x16x32_bf16 a[240:243], v[148:151], v[108:111], a[240:243]\n" \
"ds_read_b64_tr_b16 v[152:153], v32 offset:40960\n" \
"L_pf8:\n" \
"s_cbranch_execz L_pf12\n" \
"s_nop 0\n" \
"ds_read_b64_tr_b16 v[154:155], v32 offset:41984\n" \
"v_mfma_f32_16x16x32_bf16 a[244:247], v[156:159], v[108:111], a[244:247]\n" \
"ds_read_b64_tr_b16 v[148:149], v32 offset:32768\n" \
"ds_read_b64_tr_b16 v[150:151], v32 offset:33792\n" \
"v_mfma_f32_16x16x32_bf16 a[244:247], v[160:163], v[112:115], a[244:247]\n" \
"s_add_u32 m0, %[mA], 0x1800\n" \
"s_add_u32 s85, s62, 0xd800\n" \
"v_mfma_f32_16x16x32_bf16 a[248:251], v[168:171], v[112:115], a[248:251]\n" \
"buffer_load_dwordx4 v24, s[76:79], s85 offen lds\n" \
"v_mfma_f32_16x16x32_bf16 a[248:251], v[164:167], v[108:111], a[248:251]\n" \
"ds_read_b64_tr_b16 v[156:157], v33 offset:32768\n" \
"ds_read_b64_tr_b16 v[158:159], v33 offset:33792\n" \
"v_mfma_f32_16x16x32_bf16 a[252:255], v[172:175], v[108:111], a[252:255]\n" \
"ds_read_b64_tr_b16 v[160:161], v33 offset:40960\n" \
"ds_read_b64_tr_b16 v[162:163], v33 offset:41984\n" \
"v_mfma_f32_16x16x32_bf16 a[252:255], v[176:179], v[112:115], a[252:255]\n" \
"s_add_u32 m0, %[mA], 0x1c00\n" \
"s_waitcnt lgkmcnt(11)\n" \
"v_smfmac_f32_16x16x64_bf16 a[16:19], v[140:143], v[84:91], v50\n" \
"v_mfma_f32_16x16x32_bf16 a[48:51], v[128:131], v[88:91], a[48:51]\n" \
"s_add_u32 s85, s62, 0xfc00\n" \
"ds_read_b64_tr_b16 v[168:169], v34 offset:40960\n" \
"v_mfma_f32_16x16x32_bf16 a[48:51], v[124:127], v[84:87], a[48:51]\n" \
"buffer_load_dwordx4 v25, s[76:79], s85 offen lds\n" \
"s_mov_b32 s76, s54\n" \
"v_mfma_f32_16x16x32_bf16 a[80:83], v[84:87], v[132:135], a[80:83]\n" \
"s_add_u32 m0, %[mB], 0xc00\n" \
"s_add_u32 s84, s63, 0x18000\n" \
"v_mfma_f32_16x16x32_bf16 a[80:83], v[88:91], v[136:139], a[80:83]\n" \
"ds_read_b64_tr_b16 v[170:171], v34 offset:41984\n" \
"ds_read_b64_tr_b16 v[164:165], v34 offset:32768\n" \
"v_smfmac_f32_16x16x64_bf16 a[112:115], v[140:143], v[52:59], v50\n" \
"v_mfma_f32_16x16x32_bf16 a[20:23], v[128:131], v[56:59], a[20:23]\n" \
"buffer_load_dwordx4 v27, s[68:71], s84 offen lds\n" \
"v_mfma_f32_16x16x32_bf16 a[20:23], v[124:127], v[52:55], a[20:23]\n" \
"s_add_u32 s88, s43, 0x0\n" \
"s_nop 0\n" \
"ds_read_b64_tr_b16 v[166:167], v34 offset:33792\n" \
"v_mfma_f32_16x16x32_bf16 a[52:55], v[116:119], v[52:55], a[52:55]\n" \
"ds_read_b64_tr_b16 v[172:173], v35 offset:32768\n" \
"ds_read_b64_tr_b16 v[174:175], v35 offset:33792\n" \
"v_mfma_f32_16x16x32_bf16 a[52:55], v[120:123], v[56:59], a[52:55]\n" \
"buffer_store_dwordx4 a[96:99], v20, s[80:83], s88 offen offset:1024 sc1\n" \
"v_mfma_f32_16x16x32_bf16 a[84:87], v[56:59], v[136:139], a[84:87]\n" \
"ds_read_b64_tr_b16 v[176:177], v35 offset:40960\n" \
"ds_read_b64_tr_b16 v[178:179], v35 offset:41984\n" \
"v_mfma_f32_16x16x32_bf16 a[84:87], v[52:55], v[132:135], a[84:87]\n" \
"ds_read_b128 v[84:87], v48 offset:32768\n" \
"ds_read_b128 v[88:91], v49 offset:32768\n" \
"v_smfmac_f32_16x16x64_bf16 a[116:119], v[140:143], v[60:67], v50\n" \
"v_mfma_f32_16x16x32_bf16 a[24:27], v[124:127], v[60:63], a[24:27]\n" \
"ds_read_b128 v[104:107], v49 offset:36864\n" \
"ds_read_b128 v[108:111], v48 offset:38912\n" \
"v_mfma_f32_16x16x32_bf16 a[24:27], v[128:131], v[64:67], a[24:27]\n" \
"s_add_u32 s89, s43, 0x1000\n" \
"ds_read_b128 v[112:115], v49 offset:38912\n" \
"v_mfma_f32_16x16x32_bf16 a[56:59], v[120:123], v[64:67], a[56:59]\n" \
"buffer_store_dwordx4 a[100:103], v20, s[80:83], s89 offen offset:1024 sc1\n" \
"s_add_u32 s89, s43, 0x3000\n" \
"v_mfma_f32_16x16x32_bf16 a[56:59], v[116:119], v[60:63], a[56:59]\n" \
"v_accvgpr_read_b32 v248, a16\n" \
"v_accvgpr_read_b32 v249, a17\n" \
"v_mfma_f32_16x16x32_bf16 a[88:91], v[60:63], v[132:135], a[88:91]\n" \
"v_accvgpr_read_b32 v250, a18\n" \
"v_accvgpr_read_b32 v251, a19\n" \
"v_mfma_f32_16x16x32_bf16 a[88:91], v[64:67], v[136:139], a[88:91]\n" \
"v_accvgpr_read_b32 v180, a48\n" \
"v_accvgpr_read_b32 v181, a49\n" \
"v_smfmac_f32_16x16x64_bf16 a[120:123], v[140:143], v[68:75], v50\n" \
"v_mfma_f32_16x16x32_bf16 a[28:31], v[128:131], v[72:75], a[28:31]\n" \
"v_accvgpr_read_b32 v182, a50\n" \
"v_accvgpr_read_b32 v183, a51\n" \
"v_mfma_f32_16x16x32_bf16 a[28:31], v[124:127], v[68:71], a[28:31]\n" \
"v_accvgpr_read_b32 v252, a80\n" \
"v_accvgpr_read_b32 v253, a81\n" \
"v_mfma_f32_16x16x32_bf16 a[60:63], v[116:119], v[68:71], a[60:63]\n" \
"s_add_u32 s88, s43, 0x2000\n" \
"v_accvgpr_read_b32 v254, a82\n" \
"v_mfma_f32_16x16x32_bf16 a[60:63], v[120:123], v[72:75], a[60:63]\n" \
"buffer_store_dwordx4 a[8:11], v20, s[80:83], s88 offen offset:1024 sc1\n" \
"s_add_u32 s88, s45, 0x0\n" \
"s_nop 0\n" \
"v_mfma_f32_16x16x32_bf16 a[92:95], v[72:75], v[136:139], a[92:95]\n" \
"v_accvgpr_read_b32 v255, a83\n" \
"v_accvgpr_read_b32 v196, a112\n" \
"v_mfma_f32_16x16x32_bf16 a[92:95], v[68:71], v[132:135], a[92:95]\n" \
"v_accvgpr_read_b32 v197, a113\n" \
"v_accvgpr_read_b32 v198, a114\n" \
"v_smfmac_f32_16x16x64_bf16 a[124:127], v[140:143], v[76:83], v50\n" \
"v_mfma_f32_16x16x32_bf16 a[0:3], v[124:127], v[76:79], a[0:3]\n" \
"v_accvgpr_read_b32 v199, a115\n" \
"v_accvgpr_read_b32 v188, a20\n" \
"v_mfma_f32_16x16x32_bf16 a[0:3], v[128:131], v[80:83], a[0:3]\n" \
"v_accvgpr_read_b32 v189, a21\n" \
"v_accvgpr_read_b32 v190, a22\n" \
"v_mfma_f32_16x16x32_bf16 a[4:7], v[120:123], v[80:83], a[4:7]\n" \
"v_accvgpr_read_b32 v191, a23\n" \
"v_accvgpr_read_b32 v184, a52\n" \
"v_mfma_f32_16x16x32_bf16 a[4:7], v[116:119], v[76:79], a[4:7]\n" \
"v_lshlrev_b32_e32 v45, 10, v20\n" \
"v_add_u32_e32 v45, s41, v45\n" \
"v_mfma_f32_16x16x32_bf16 a[32:35], v[76:79], v[132:135], a[32:35]\n" \
"s_mov_b32 m0, %[mS]\n" \
"v_accvgpr_read_b32 v185, a53\n" \
"v_mfma_f32_16x16x32_bf16 a[32:35], v[80:83], v[136:139], a[32:35]\n" \
"v_accvgpr_read_b32 v186, a54\n" \
"s_waitcnt lgkmcnt(0)\n" \
"v_mfma_f32_16x16x32_bf16 a[128:131], v[152:155], v[56:59], a[128:131]\n" \
"v_accvgpr_read_b32 v187, a55\n" \
"v_accvgpr_read_b32 v192, a84\n" \
"v_mfma_f32_16x16x32_bf16 a[128:131], v[148:151], v[52:55], a[128:131]\n" \
"v_accvgpr_read_b32 v193, a85\n" \
"v_accvgpr_read_b32 v194, a86\n" \
"v_mfma_f32_16x16x32_bf16 a[132:135], v[156:159], v[52:55], a[132:135]\n" \
"v_accvgpr_read_b32 v195, a87\n" \
"v_accvgpr_read_b32 v212, a116\n" \
"s_getpc_b64 s[86:87]\n" \
"L_ct15_%=:\n" \
"s_add_u32 s86, s86, L_end_%=-L_ct15_%=\n" \
"s_addc_u32 s87, s87, 0\n" \
"v_mfma_f32_16x16x32_bf16 a[132:135], v[160:163], v[56:59], a[132:135]\n" \
"v_accvgpr_read_b32 v213, a117\n" \
"v_accvgpr_read_b32 v214, a118\n" \
"v_mfma_f32_16x16x32_bf16 a[136:139], v[168:171], v[56:59], a[136:139]\n" \
"v_accvgpr_read_b32 v215, a119\n" \
"v_accvgpr_read_b32 v204, a24\n" \
"v_mfma_f32_16x16x32_bf16 a[136:139], v[164:167], v[52:55], a[136:139]\n" \
"v_accvgpr_read_b32 v205, a25\n" \
"v_accvgpr_read_b32 v206, a26\n" \
"v_mfma_f32_16x16x32_bf16 a[140:143], v[172:175], v[52:55], a[140:143]\n" \
"v_accvgpr_read_b32 v207, a27\n" \
"v_accvgpr_read_b32 v200, a56\n" \
"v_mfma_f32_16x16x32_bf16 a[140:143], v[176:179], v[56:59], a[140:143]\n" \
"v_accvgpr_read_b32 v201, a57\n" \
"v_accvgpr_read_b32 v202, a58\n" \
"v_mfma_f32_16x16x32_bf16 a[160:163], v[152:155], v[64:67], a[160:163]\n" \
"v_accvgpr_read_b32 v203, a59\n" \
"v_accvgpr_read_b32 v208, a88\n" \
"v_mfma_f32_16x16x32_bf16 a[160:163], v[148:151], v[60:63], a[160:163]\n" \
"v_accvgpr_read_b32 v209, a89\n" \
"v_accvgpr_read_b32 v210, a90\n" \
"v_mfma_f32_16x16x32_bf16 a[164:167], v[156:159], v[60:63], a[164:167]\n" \
"v_accvgpr_read_b32 v211, a91\n" \
"v_accvgpr_read_b32 v228, a120\n" \
"v_mfma_f32_16x16x32_bf16 a[164:167], v[160:163], v[64:67], a[164:167]\n" \
"v_accvgpr_read_b32 v229, a121\n" \
"v_accvgpr_read_b32 v230, a122\n" \
"v_mfma_f32_16x16x32_bf16 a[168:171], v[168:171], v[64:67], a[168:171]\n" \
"v_accvgpr_read_b32 v231, a123\n" \
"v_accvgpr_read_b32 v220, a28\n" \
"v_mfma_f32_16x16x32_bf16 a[168:171], v[164:167], v[60:63], a[168:171]\n" \
"v_accvgpr_read_b32 v221, a29\n" \
"v_accvgpr_read_b32 v222, a30\n" \
"v_mfma_f32_16x16x32_bf16 a[172:175], v[172:175], v[60:63], a[172:175]\n" \
"v_accvgpr_read_b32 v223, a31\n" \
"v_accvgpr_read_b32 v216, a60\n" \
"v_mfma_f32_16x16x32_bf16 a[172:175], v[176:179], v[64:67], a[172:175]\n" \
"v_accvgpr_read_b32 v217, a61\n" \
"v_accvgpr_read_b32 v218, a62\n" \
"v_mfma_f32_16x16x32_bf16 a[192:195], v[152:155], v[72:75], a[192:195]\n" \
"v_accvgpr_read_b32 v219, a63\n" \
"v_accvgpr_read_b32 v224, a92\n" \
"v_mfma_f32_16x16x32_bf16 a[192:195], v[148:151], v[68:71], a[192:195]\n" \
"v_accvgpr_read_b32 v225, a93\n" \
"v_accvgpr_read_b32 v226, a94\n" \
"s_waitcnt vmcnt(3)\n" \
"s_barrier\n" \
"s_mov_b64 exec, 3\n" \
"global_load_lds_dword v45, s[86:87]\n" \
"s_mov_b64 exec, -1\n" \
"s_sub_u32 m0, %[mB], 0x10000\n" \
"v_mfma_f32_16x16x32_bf16 a[196:199], v[156:159], v[68:71], a[196:199]\n" \
"ds_read_b64_tr_b16 v[140:141], v39 offset:0\n" \
"ds_read_b64_tr_b16 v[142:143], v39 offset:960\n" \
"v_mfma_f32_16x16x32_bf16 a[196:199], v[160:163], v[72:75], a[196:199]\n" \
"ds_read_b64_tr_b16 v[128:129], v37 offset:4096\n" \
"ds_read_b64_tr_b16 v[130:131], v37 offset:4608\n" \
"v_mfma_f32_16x16x32_bf16 a[200:203], v[168:171], v[72:75], a[200:203]\n" \
"ds_read_b64_tr_b16 v[124:125], v37 offset:0\n" \
"ds_read_b64_tr_b16 v[126:127], v37 offset:512\n" \
"v_mfma_f32_16x16x32_bf16 a[200:203], v[164:167], v[68:71], a[200:203]\n" \
"ds_read_b64_tr_b16 v[132:133], v38 offset:0\n" \
"ds_read_b64_tr_b16 v[134:135], v38 offset:512\n" \
"v_mfma_f32_16x16x32_bf16 a[204:207], v[172:175], v[68:71], a[204:207]\n" \
"ds_read_b64_tr_b16 v[136:137], v38 offset:4096\n" \
"ds_read_b64_tr_b16 v[138:139], v38 offset:4608\n" \
"v_mfma_f32_16x16x32_bf16 a[204:207], v[176:179], v[72:75], a[204:207]\n" \
"ds_read_b128 v[52:55], v28 offset:0\n" \
"ds_read_b128 v[56:59], v29 offset:0\n" \
"v_mfma_f32_16x16x32_bf16 a[224:227], v[152:155], v[80:83], a[224:227]\n" \
"ds_read_b64_tr_b16 v[116:117], v36 offset:0\n" \
"ds_read_b64_tr_b16 v[118:119], v36 offset:512\n" \
"v_mfma_f32_16x16x32_bf16 a[224:227], v[148:151], v[76:79], a[224:227]\n" \
"ds_read_b64_tr_b16 v[120:121], v36 offset:4096\n" \
"ds_read_b64_tr_b16 v[122:123], v36 offset:4608\n" \
"v_mfma_f32_16x16x32_bf16 a[228:231], v[156:159], v[76:79], a[228:231]\n" \
"ds_read_b128 v[60:63], v28 offset:2048\n" \
"ds_read_b128 v[64:67], v29 offset:2048\n" \
"v_mfma_f32_16x16x32_bf16 a[228:231], v[160:163], v[80:83], a[228:231]\n" \
"v_accvgpr_read_b32 v227, a95\n" \
"v_accvgpr_read_b32 v244, a124\n" \
"v_mfma_f32_16x16x32_bf16 a[232:235], v[168:171], v[80:83], a[232:235]\n" \
"ds_read_b128 v[68:71], v28 offset:4096\n" \
"ds_read_b128 v[72:75], v29 offset:4096\n" \
"v_mfma_f32_16x16x32_bf16 a[232:235], v[164:167], v[76:79], a[232:235]\n" \
"v_accvgpr_read_b32 v245, a125\n" \
"v_accvgpr_read_b32 v246, a126\n" \
"v_mfma_f32_16x16x32_bf16 a[236:239], v[172:175], v[76:79], a[236:239]\n" \
"ds_read_b128 v[76:79], v28 offset:6144\n" \
"v_accvgpr_read_b32 v247, a127\n" \
"v_mfma_f32_16x16x32_bf16 a[236:239], v[176:179], v[80:83], a[236:239]\n" \
"ds_read_b128 v[80:83], v29 offset:6144\n" \
"v_accvgpr_read_b32 v236, a0\n" \
"v_mfma_f32_16x16x32_bf16 a[144:147], v[152:155], v[88:91], a[144:147]\n" \
"v_accvgpr_read_b32 v237, a1\n" \
"v_accvgpr_read_b32 v238, a2\n" \
"v_mfma_f32_16x16x32_bf16 a[144:147], v[148:151], v[84:87], a[144:147]\n" \
"v_accvgpr_read_b32 v239, a3\n" \
"v_accvgpr_read_b32 v232, a4\n" \
"v_mfma_f32_16x16x32_bf16 a[148:151], v[156:159], v[84:87], a[148:151]\n" \
"v_accvgpr_read_b32 v233, a5\n" \
"v_accvgpr_read_b32 v234, a6\n" \
"v_mfma_f32_16x16x32_bf16 a[148:151], v[160:163], v[88:91], a[148:151]\n" \
"v_accvgpr_read_b32 v235, a7\n" \
"v_accvgpr_read_b32 v240, a32\n" \
"v_mfma_f32_16x16x32_bf16 a[152:155], v[168:171], v[88:91], a[152:155]\n" \
"v_accvgpr_read_b32 v241, a33\n" \
"v_accvgpr_read_b32 v242, a34\n" \
"v_mfma_f32_16x16x32_bf16 a[152:155], v[164:167], v[84:87], a[152:155]\n" \
"v_accvgpr_read_b32 v243, a35\n" \
"s_mov_b32 s78, -1\n" \
"v_mfma_f32_16x16x32_bf16 a[156:159], v[172:175], v[84:87], a[156:159]\n" \
"ds_read_b128 v[84:87], v22 offset:0\n" \
"s_mov_b32 s79, 0\n" \
"v_mfma_f32_16x16x32_bf16 a[156:159], v[176:179], v[88:91], a[156:159]\n" \
"ds_read_b128 v[88:91], v23 offset:0\n" \
"s_mov_b32 s68, 0xffc0fffc\n" \
"v_mfma_f32_16x16x32_bf16 a[176:179], v[152:155], v[96:99], a[176:179]\n" \
"s_mov_b32 s69, 0xc000fc00\n" \
"v_lshrrev_b32_e32 v24, 9, v20\n" \
"v_mfma_f32_16x16x32_bf16 a[176:179], v[148:151], v[92:95], a[176:179]\n" \
"v_lshrrev_b32_e32 v25, 8, v20\n" \
"v_mul_u32_u24_e32 v24, 0x24800, v24\n" \
"v_mfma_f32_16x16x32_bf16 a[180:183], v[156:159], v[92:95], a[180:183]\n" \
"v_mul_u32_u24_e32 v25, 0x24700, v25\n" \
"v_add_u32_e64 v24, v24, v26\n" \
"v_mfma_f32_16x16x32_bf16 a[180:183], v[160:163], v[96:99], a[180:183]\n" \
"v_add_u32_e32 v25, v25, v20\n" \
"s_add_u32 s85, s50, 0x0\n" \
"v_mfma_f32_16x16x32_bf16 a[184:187], v[168:171], v[96:99], a[184:187]\n" \
"s_add_u32 s87, s50, 0x49000\n" \
"s_mov_b32 s70, 0x3e0293ee\n" \
"v_mfma_f32_16x16x32_bf16 a[184:187], v[164:167], v[92:95], a[184:187]\n" \
"s_mov_b32 s71, 0x3e0293ee\n" \
"v_add_u32_e64 v27, s47, v20\n" \
"v_mfma_f32_16x16x32_bf16 a[188:191], v[172:175], v[92:95], a[188:191]\n" \
"ds_read_b128 v[92:95], v48 offset:2048\n" \
"v_lshrrev_b32_e32 v26, 2, v20\n" \
"v_mfma_f32_16x16x32_bf16 a[188:191], v[176:179], v[96:99], a[188:191]\n" \
"ds_read_b128 v[96:99], v49 offset:2048\n" \
"v_add_u32_e32 v26, s46, v26\n" \
"v_mfma_f32_16x16x32_bf16 a[208:211], v[152:155], v[104:107], a[208:211]\n" \
"v_mfma_f32_16x16x32_bf16 a[208:211], v[148:151], v[100:103], a[208:211]\n" \
"v_mfma_f32_16x16x32_bf16 a[212:215], v[156:159], v[100:103], a[212:215]\n" \
"v_mfma_f32_16x16x32_bf16 a[212:215], v[160:163], v[104:107], a[212:215]\n" \
"v_mfma_f32_16x16x32_bf16 a[216:219], v[168:171], v[104:107], a[216:219]\n" \
"v_mfma_f32_16x16x32_bf16 a[216:219], v[164:167], v[100:103], a[216:219]\n" \
"v_mfma_f32_16x16x32_bf16 a[220:223], v[172:175], v[100:103], a[220:223]\n" \
"ds_read_b128 v[100:103], v48 offset:4096\n" \
"v_mfma_f32_16x16x32_bf16 a[220:223], v[176:179], v[104:107], a[220:223]\n" \
"ds_read_b128 v[104:107], v49 offset:4096\n" \
"v_mfma_f32_16x16x32_bf16 a[240:243], v[152:155], v[112:115], a[240:243]\n" \
"ds_read_b64_tr_b16 v[152:153], v32 offset:8192\n" \
"ds_read_b64_tr_b16 v[154:155], v32 offset:9216\n" \
"v_mfma_f32_16x16x32_bf16 a[240:243], v[148:151], v[108:111], a[240:243]\n" \
"ds_read_b64_tr_b16 v[148:149], v32 offset:0\n" \
"ds_read_b64_tr_b16 v[150:151], v32 offset:1024\n" \
"v_mfma_f32_16x16x32_bf16 a[244:247], v[156:159], v[108:111], a[244:247]\n" \
"ds_read_b64_tr_b16 v[156:157], v33 offset:0\n" \
"ds_read_b64_tr_b16 v[158:159], v33 offset:1024\n" \
"v_mfma_f32_16x16x32_bf16 a[244:247], v[160:163], v[112:115], a[244:247]\n" \
"ds_read_b64_tr_b16 v[160:161], v33 offset:8192\n" \
"ds_read_b64_tr_b16 v[162:163], v33 offset:9216\n" \
"v_mfma_f32_16x16x32_bf16 a[248:251], v[168:171], v[112:115], a[248:251]\n" \
"ds_read_b64_tr_b16 v[168:169], v34 offset:8192\n" \
"ds_read_b64_tr_b16 v[170:171], v34 offset:9216\n" \
"v_mfma_f32_16x16x32_bf16 a[248:251], v[164:167], v[108:111], a[248:251]\n" \
"ds_read_b64_tr_b16 v[164:165], v34 offset:0\n" \
"ds_read_b64_tr_b16 v[166:167], v34 offset:1024\n" \
"v_mfma_f32_16x16x32_bf16 a[252:255], v[172:175], v[108:111], a[252:255]\n" \
"ds_read_b64_tr_b16 v[172:173], v35 offset:0\n" \
"ds_read_b64_tr_b16 v[174:175], v35 offset:1024\n" \
"v_mfma_f32_16x16x32_bf16 a[252:255], v[176:179], v[112:115], a[252:255]\n" \
"ds_read_b64_tr_b16 v[176:177], v35 offset:8192\n" \
"s_waitcnt lgkmcnt(15)\n" \
"s_nop 0\n" \
"v_smfmac_f32_16x16x64_bf16 v[248:251], v[140:143], v[84:91], v50\n" \
"v_mfma_f32_16x16x32_bf16 v[180:183], v[128:131], v[88:91], v[180:183]\n" \
"ds_read_b64_tr_b16 v[178:179], v35 offset:9216\n" \
"v_bfe_u32 v44, v20, 2, 5\n" \
"v_mfma_f32_16x16x32_bf16 v[180:183], v[124:127], v[84:87], v[180:183]\n" \
"buffer_load_dword v44, v44, s[80:83], s39 offen offset:0 sc1\n" \
"v_mfma_f32_16x16x32_bf16 v[252:255], v[84:87], v[132:135], v[252:255]\n" \
"ds_read_b128 v[84:87], v48 offset:0\n" \
"ds_read_b128 v[108:111], v48 offset:6144\n" \
"v_mfma_f32_16x16x32_bf16 v[252:255], v[88:91], v[136:139], v[252:255]\n" \
"ds_read_b128 v[88:91], v49 offset:0\n" \
"ds_read_b128 v[112:115], v49 offset:6144\n" \
"v_smfmac_f32_16x16x64_bf16 v[196:199], v[140:143], v[52:59], v50\n" \
"v_mfma_f32_16x16x32_bf16 v[188:191], v[128:131], v[56:59], v[188:191]\n" \
"v_cvt_pk_bf16_f32 v180, v180, v181\n" \
"v_cvt_pk_bf16_f32 v181, v182, v183\n" \
"v_mfma_f32_16x16x32_bf16 v[188:191], v[124:127], v[52:55], v[188:191]\n" \
"v_add_f32_e64 v249, v249, v251\n" \
"v_cvt_pk_bf16_f32 v182, v249, 0\n" \
"v_mfma_f32_16x16x32_bf16 v[184:187], v[116:119], v[52:55], v[184:187]\n" \
"v_mov_b32_e32 v183, 0x3f80\n" \
"v_add_f32_e64 v196, v196, v198\n" \
"v_mfma_f32_16x16x32_bf16 v[184:187], v[120:123], v[56:59], v[184:187]\n" \
"v_mfma_f32_16x16x16_bf16 v[248:251], v[182:183], v[46:47], 0\n" \
"v_add_f32_e64 v197, v197, v199\n" \
"v_cvt_pk_bf16_f32 v188, v188, v189\n" \
"v_mfma_f32_16x16x32_bf16 v[192:195], v[56:59], v[136:139], v[192:195]\n" \
"v_cvt_pk_bf16_f32 v189, v190, v191\n" \
"v_cvt_pk_bf16_f32 v190, v197, 0\n" \
"v_mfma_f32_16x16x32_bf16 v[192:195], v[52:55], v[132:135], v[192:195]\n" \
"v_mov_b32_e32 v191, 0x3f80\n" \
"v_cvt_pk_bf16_f32 v184, v184, v185\n" \
"v_smfmac_f32_16x16x64_bf16 v[212:215], v[140:143], v[60:67], v50\n" \
"v_mfma_f32_16x16x32_bf16 v[204:207], v[124:127], v[60:63], v[204:207]\n" \
"v_cvt_pk_bf16_f32 v185, v186, v187\n" \
"v_cndmask_b32_e64 v186, 0, v196, s[78:79]\n" \
"v_mfma_f32_16x16x32_bf16 v[204:207], v[128:131], v[64:67], v[204:207]\n" \
"v_mov_b32_e64 v187, 0\n" \
"v_cvt_pk_bf16_f32 v186, v186, 0\n" \
"v_mfma_f32_16x16x32_bf16 v[200:203], v[120:123], v[64:67], v[200:203]\n" \
"v_mfma_f32_16x16x32_bf16 v[200:203], v[116:119], v[60:63], v[200:203]\n" \
"v_add_f32_e32 v212, v212, v214\n" \
"v_add_f32_e32 v213, v213, v215\n" \
"v_mfma_f32_16x16x32_bf16 v[208:211], v[60:63], v[132:135], v[208:211]\n" \
"v_mfma_f32_16x16x32_bf16 v[208:211], v[64:67], v[136:139], v[208:211]\n" \
"v_cvt_pk_bf16_f32 v204, v204, v205\n" \
"v_cvt_pk_bf16_f32 v205, v206, v207\n" \
"v_smfmac_f32_16x16x64_bf16 v[228:231], v[140:143], v[68:75], v50\n" \
"v_mfma_f32_16x16x32_bf16 v[220:223], v[128:131], v[72:75], v[220:223]\n" \
"v_cvt_pk_bf16_f32 v200, v200, v201\n" \
"v_cvt_pk_bf16_f32 v201, v202, v203\n" \
"v_mfma_f32_16x16x32_bf16 v[220:223], v[124:127], v[68:71], v[220:223]\n" \
"v_cndmask_b32_e64 v202, 0, v212, s[78:79]\n" \
"v_cvt_pk_bf16_f32 v206, v213, 0\n" \
"v_mfma_f32_16x16x32_bf16 v[216:219], v[116:119], v[68:71], v[216:219]\n" \
"v_mov_b32_e32 v207, 0x3f80\n" \
"v_add_f32_e64 v228, v228, v230\n" \
"v_mfma_f32_16x16x32_bf16 v[216:219], v[120:123], v[72:75], v[216:219]\n" \
"v_add_f32_e32 v229, v229, v231\n" \
"v_mov_b32_e32 v203, 0\n" \
"v_mfma_f32_16x16x32_bf16 v[224:227], v[72:75], v[136:139], v[224:227]\n" \
"v_cvt_pk_bf16_f32 v220, v220, v221\n" \
"v_cvt_pk_bf16_f32 v221, v222, v223\n" \
"v_mfma_f32_16x16x32_bf16 v[224:227], v[68:71], v[132:135], v[224:227]\n" \
"v_cvt_pk_bf16_f32 v202, v202, 0\n" \
"v_cvt_pk_bf16_f32 v222, v229, 0\n" \
"v_smfmac_f32_16x16x64_bf16 v[244:247], v[140:143], v[76:83], v50\n" \
"v_mfma_f32_16x16x32_bf16 v[236:239], v[124:127], v[76:79], v[236:239]\n" \
"v_cvt_pk_bf16_f32 v216, v216, v217\n" \
"v_cvt_pk_bf16_f32 v217, v218, v219\n" \
"v_mfma_f32_16x16x32_bf16 v[236:239], v[128:131], v[80:83], v[236:239]\n" \
"v_cndmask_b32_e64 v218, 0, v228, s[78:79]\n" \
"v_cvt_pk_bf16_f32 v126, v192, v193\n" \
"v_mfma_f32_16x16x32_bf16 v[232:235], v[120:123], v[80:83], v[232:235]\n" \
"v_cvt_pk_bf16_f32 v127, v194, v195\n" \
"v_add_f32_e64 v244, v244, v246\n" \
"v_mfma_f32_16x16x32_bf16 v[232:235], v[116:119], v[76:79], v[232:235]\n" \
"v_add_f32_e64 v245, v245, v247\n" \
"v_cvt_pk_bf16_f32 v128, v208, v209\n" \
"v_mfma_f32_16x16x32_bf16 v[240:243], v[76:79], v[132:135], v[240:243]\n" \
"v_cvt_pk_bf16_f32 v236, v236, v237\n" \
"v_cvt_pk_bf16_f32 v237, v238, v239\n" \
"v_mfma_f32_16x16x32_bf16 v[240:243], v[80:83], v[136:139], v[240:243]\n" \
"v_cvt_pk_bf16_f32 v129, v210, v211\n" \
"v_mov_b32_e32 v223, 0x3f80\n" \
"s_waitcnt lgkmcnt(0)\n" \
"s_barrier\n" \
"v_mfma_f32_16x16x32_bf16 a[128:131], v[152:155], v[56:59], a[128:131]\n" \
"v_cvt_pk_bf16_f32 v232, v232, v233\n" \
"v_cvt_pk_bf16_f32 v233, v234, v235\n" \
"v_mfma_f32_16x16x32_bf16 a[128:131], v[148:151], v[52:55], a[128:131]\n" \
"v_cndmask_b32_e64 v234, 0, v244, s[78:79]\n" \
"v_mov_b32_e64 v219, 0\n" \
"v_mfma_f32_16x16x32_bf16 a[132:135], v[156:159], v[52:55], a[132:135]\n" \
"v_cvt_pk_bf16_f32 v218, v218, 0\n" \
"v_cvt_pk_bf16_f32 v130, v224, v225\n" \
"v_mfma_f32_16x16x32_bf16 a[132:135], v[160:163], v[56:59], a[132:135]\n" \
"v_cvt_pk_bf16_f32 v131, v226, v227\n" \
"v_cvt_pk_bf16_f32 v238, v245, 0\n" \
"v_mfma_f32_16x16x32_bf16 a[136:139], v[168:171], v[56:59], a[136:139]\n" \
"v_mov_b32_e32 v239, 0x3f80\n" \
"v_mov_b32_e64 v235, 0\n" \
"v_mfma_f32_16x16x32_bf16 a[136:139], v[164:167], v[52:55], a[136:139]\n" \
"v_cvt_pk_bf16_f32 v234, v234, 0\n" \
"v_cvt_pk_bf16_f32 v132, v240, v241\n" \
"v_mfma_f32_16x16x32_bf16 a[140:143], v[172:175], v[52:55], a[140:143]\n" \
"v_cvt_pk_bf16_f32 v133, v242, v243\n" \
"v_cvt_pk_bf16_f32 v134, v248, v249\n" \
"v_mfma_f32_16x16x32_bf16 a[140:143], v[176:179], v[56:59], a[140:143]\n" \
"v_mfma_f32_16x16x32_bf16 v[116:119], v[180:183], v[184:187], 0\n" \
"v_mfma_f32_16x16x32_bf16 v[192:195], v[188:191], v[184:187], 0\n" \
"v_mfma_f32_16x16x32_bf16 v[208:211], v[204:207], v[200:203], 0\n" \
"v_mfma_f32_16x16x32_bf16 v[120:123], v[188:191], v[200:203], 0\n" \
"s_waitcnt vmcnt(0)\n" \
"v_cmp_ne_u32_e32 vcc, %[tag], v44\n" \
"v_mfma_f32_16x16x32_bf16 a[160:163], v[152:155], v[64:67], a[160:163]\n" \
"v_mfma_f32_16x16x32_bf16 v[224:227], v[220:223], v[216:219], 0\n" \
"v_mfma_f32_16x16x32_bf16 v[36:39], v[204:207], v[216:219], 0\n" \
"v_mfma_f32_16x16x32_bf16 v[240:243], v[236:239], v[232:235], 0\n" \
"v_mfma_f32_16x16x32_bf16 v[48:51], v[220:223], v[232:235], 0\n" \
"s_cbranch_vccnz L_pa_%=\n" \
"L_ra_%=:\n" \
"v_cndmask_b32_e64 v116, v116, v117, s[64:65]\n" \
"buffer_load_dwordx4 v20, s[80:83], s73 offen offset:0 sc1 lds\n" \
"v_mfma_f32_16x16x32_bf16 a[160:163], v[148:151], v[60:63], a[160:163]\n" \
"v_cndmask_b32_e64 v118, v118, v119, s[64:65]\n" \
"v_cndmask_b32_e64 v192, v192, v193, s[64:65]\n" \
"v_mfma_f32_16x16x32_bf16 a[164:167], v[156:159], v[60:63], a[164:167]\n" \
"v_add_f32_e32 v116, s49, v116\n" \
"v_cndmask_b32_e64 v194, v194, v195, s[64:65]\n" \
"v_mfma_f32_16x16x32_bf16 a[164:167], v[160:163], v[64:67], a[164:167]\n" \
"v_add_f32_e64 v118, s49, v118\n" \
"v_cndmask_b32_e64 v208, v208, v209, s[64:65]\n" \
"v_mfma_f32_16x16x32_bf16 a[168:171], v[168:171], v[64:67], a[168:171]\n" \
"buffer_load_dwordx4 v20, s[80:83], s73 offen offset:1024 sc1 lds\n" \
"v_mfma_f32_16x16x32_bf16 a[168:171], v[164:167], v[60:63], a[168:171]\n" \
"v_cndmask_b32_e64 v120, v120, v121, s[64:65]\n" \
"v_cndmask_b32_e64 v210, v210, v211, s[64:65]\n" \
"v_mfma_f32_16x16x32_bf16 a[172:175], v[172:175], v[60:63], a[172:175]\n" \
"v_cndmask_b32_e64 v122, v122, v123, s[64:65]\n" \
"v_cndmask_b32_e64 v224, v224, v225, s[64:65]\n" \
"v_mfma_f32_16x16x32_bf16 a[172:175], v[176:179], v[64:67], a[172:175]\n" \
"v_cndmask_b32_e64 v36, v36, v37, s[64:65]\n" \
"v_cndmask_b32_e64 v226, v226, v227, s[64:65]\n" \
"v_mfma_f32_16x16x32_bf16 a[192:195], v[152:155], v[72:75], a[192:195]\n" \
"buffer_load_dwordx4 v20, s[80:83], s73 offen offset:2048 sc1 lds\n" \
"v_mfma_f32_16x16x32_bf16 a[192:195], v[148:151], v[68:71], a[192:195]\n" \
"v_cndmask_b32_e64 v38, v38, v39, s[64:65]\n" \
"v_cndmask_b32_e64 v240, v240, v241, s[64:65]\n" \
"v_mfma_f32_16x16x32_bf16 a[196:199], v[156:159], v[68:71], a[196:199]\n" \
"v_cndmask_b32_e64 v48, v48, v49, s[64:65]\n" \
"v_cndmask_b32_e64 v242, v242, v243, s[64:65]\n" \
"v_mfma_f32_16x16x32_bf16 a[196:199], v[160:163], v[72:75], a[196:199]\n" \
"v_cndmask_b32_e64 v50, v50, v51, s[64:65]\n" \
"v_cndmask_b32_e64 v192, v116, v192, s[66:67]\n" \
"v_mfma_f32_16x16x32_bf16 a[200:203], v[168:171], v[72:75], a[200:203]\n" \
"buffer_load_dwordx4 v20, s[80:83], s73 offen offset:3072 sc1 lds\n" \
"v_mfma_f32_16x16x32_bf16 a[200:203], v[164:167], v[68:71], a[200:203]\n" \
"s_sub_u32 m0, %[mB], s53\n" \
"s_nop 0\n" \
"v_cndmask_b32_e64 v194, v118, v194, s[68:69]\n" \
"v_mfma_f32_16x16x32_bf16 a[204:207], v[172:175], v[68:71], a[204:207]\n" \
"v_cndmask_b32_e64 v208, v120, v208, s[66:67]\n" \
"v_cndmask_b32_e64 v210, v122, v210, s[68:69]\n" \
"v_mfma_f32_16x16x32_bf16 a[204:207], v[176:179], v[72:75], a[204:207]\n" \
"v_cndmask_b32_e64 v224, v36, v224, s[66:67]\n" \
"v_cndmask_b32_e64 v226, v38, v226, s[68:69]\n" \
"v_mfma_f32_16x16x32_bf16 a[224:227], v[152:155], v[80:83], a[224:227]\n" \
"buffer_load_dwordx4 v20, s[80:83], s74 offen offset:0 sc1 lds\n" \
"v_mfma_f32_16x16x32_bf16 a[224:227], v[148:151], v[76:79], a[224:227]\n" \
"v_cndmask_b32_e64 v240, v48, v240, s[66:67]\n" \
"v_cndmask_b32_e64 v242, v50, v242, s[68:69]\n" \
"v_mfma_f32_16x16x32_bf16 a[228:231], v[156:159], v[76:79], a[228:231]\n" \
"v_max_f32_e32 v193, v192, v194\n" \
"v_max_f32_e32 v209, v208, v210\n" \
"v_mfma_f32_16x16x32_bf16 a[228:231], v[160:163], v[80:83], a[228:231]\n" \
"v_max_f32_e32 v225, v224, v226\n" \
"v_max_f32_e32 v241, v240, v242\n" \
"v_mfma_f32_16x16x32_bf16 a[232:235], v[168:171], v[80:83], a[232:235]\n" \
"buffer_load_dwordx4 v20, s[80:83], s74 offen offset:1024 sc1 lds\n" \
"v_mfma_f32_16x16x32_bf16 a[232:235], v[164:167], v[76:79], a[232:235]\n" \
"v_permlane32_swap_b32_e32 v193, v225\n" \
"v_permlane32_swap_b32_e32 v209, v241\n" \
"v_mfma_f32_16x16x32_bf16 a[236:239], v[172:175], v[76:79], a[236:239]\n" \
"v_max_f32_e32 v193, v193, v225\n" \
"v_max_f32_e32 v209, v209, v241\n" \
"v_permlane32_swap_b32_e32 v196, v228\n" \
"v_permlane32_swap_b32_e32 v212, v244\n" \
"buffer_load_dwordx4 v20, s[80:83], s74 offen offset:2048 sc1 lds\n" \
"v_permlane16_swap_b32_e32 v193, v209\n" \
"v_permlane16_swap_b32_e32 v228, v244\n" \
"v_mfma_f32_16x16x32_bf16 a[236:239], v[176:179], v[80:83], a[236:239]\n" \
"v_max_f32_e32 v193, v193, v209\n" \
"v_mul_f32_e32 v193, s72, v193\n" \
"v_mfma_f32_16x16x32_bf16 a[144:147], v[152:155], v[88:91], a[144:147]\n" \
"v_mul_f32_e32 v45, 0xbfb8aa3b, v228\n" \
"v_mov_b32_e64 v209, v193\n" \
"v_mfma_f32_16x16x16_bf16 v[196:199], v[190:191], v[46:47], 0\n" \
"buffer_load_dwordx4 v20, s[80:83], s74 offen offset:3072 sc1 lds\n" \
"v_mfma_f32_16x16x32_bf16 a[144:147], v[148:151], v[84:87], a[144:147]\n" \
"s_sub_u32 m0, %[mB], 0x4000\n" \
"v_cvt_pk_bf16_f32 v135, v250, v251\n" \
"v_mfma_f32_16x16x16_bf16 v[212:215], v[206:207], v[46:47], 0\n" \
"v_mfma_f32_16x16x16_bf16 v[228:231], v[222:223], v[46:47], 0\n" \
"v_permlane16_swap_b32_e32 v193, v209\n" \
"v_mfma_f32_16x16x16_bf16 v[244:247], v[238:239], v[46:47], 0\n" \
"buffer_load_dwordx4 v20, s[80:83], s75 offen offset:0 sc1 lds\n" \
"v_mfma_f32_16x16x32_bf16 a[148:151], v[156:159], v[84:87], a[148:151]\n" \
"v_mov_b32_e32 v225, v193\n" \
"v_mov_b32_e64 v241, v209\n" \
"v_mfma_f32_16x16x32_bf16 a[148:151], v[160:163], v[88:91], a[148:151]\n" \
"v_permlane32_swap_b32_e32 v193, v225\n" \
"v_permlane32_swap_b32_e32 v209, v241\n" \
"v_mfma_f32_16x16x32_bf16 a[152:155], v[168:171], v[88:91], a[152:155]\n" \
"v_fma_f32 v192, v192, s72, -v193\n" \
"v_fma_f32 v194, v194, s72, -v193\n" \
"buffer_load_dwordx4 v20, s[80:83], s75 offen offset:1024 sc1 lds\n" \
"v_mfma_f32_16x16x32_bf16 a[152:155], v[164:167], v[84:87], a[152:155]\n" \
"v_fma_f32 v208, v208, s72, -v209\n" \
"v_fma_f32 v210, v210, s72, -v209\n" \
"v_exp_f32_e32 v192, v192\n" \
"v_exp_f32_e32 v194, v194\n" \
"v_exp_f32_e32 v208, v208\n" \
"v_exp_f32_e32 v210, v210\n" \
"v_mfma_f32_16x16x32_bf16 a[156:159], v[172:175], v[84:87], a[156:159]\n" \
"buffer_load_dwordx4 v20, s[80:83], s75 offen offset:2048 sc1 lds\n" \
"v_mfma_f32_16x16x32_bf16 a[156:159], v[176:179], v[88:91], a[156:159]\n" \
"v_fma_f32 v224, v224, s72, -v225\n" \
"v_fma_f32 v226, v226, s72, -v225\n" \
"v_exp_f32_e32 v224, v224\n" \
"v_exp_f32_e32 v226, v226\n" \
"v_mfma_f32_16x16x32_bf16 a[176:179], v[152:155], v[96:99], a[176:179]\n" \
"v_fma_f32 v240, v240, s72, -v241\n" \
"v_fma_f32 v242, v242, s72, -v241\n" \
"v_mfma_f32_16x16x32_bf16 a[176:179], v[148:151], v[92:95], a[176:179]\n" \
"buffer_load_dwordx4 v20, s[80:83], s75 offen offset:3072 sc1 lds\n" \
"v_mfma_f32_16x16x32_bf16 a[180:183], v[156:159], v[92:95], a[180:183]\n" \
"s_mov_b32 m0, s55\n" \
"v_cvt_pk_bf16_f32 v192, v192, v194\n" \
"v_mfma_f32_16x16x32_bf16 a[180:183], v[160:163], v[96:99], a[180:183]\n" \
"v_cvt_pk_bf16_f32 v208, v208, v210\n" \
"v_cvt_pk_bf16_f32 v136, v196, v197\n" \
"v_exp_f32_e32 v240, v240\n" \
"v_exp_f32_e64 v242, v242\n" \
"v_mfma_f32_16x16x32_bf16 a[184:187], v[168:171], v[96:99], a[184:187]\n" \
"buffer_load_dwordx4 v20, s[80:83], s76 offen offset:0 sc1 lds\n" \
"v_mfma_f32_16x16x32_bf16 a[184:187], v[164:167], v[92:95], a[184:187]\n" \
"v_cvt_pk_bf16_f32 v137, v198, v199\n" \
"v_cvt_pk_bf16_f32 v138, v212, v213\n" \
"L_pf9:\n" \
"s_cbranch_execz L_pf13\n" \
"s_nop 0\n" \
"v_mfma_f32_16x16x32_bf16 a[188:191], v[172:175], v[92:95], a[188:191]\n" \
"v_cvt_pk_bf16_f32 v139, v214, v215\n" \
"v_perm_b32 v184, v192, v192, v40\n" \
"v_mfma_f32_16x16x32_bf16 a[188:191], v[176:179], v[96:99], a[188:191]\n" \
"v_perm_b32 v185, v192, v192, v41\n" \
"v_perm_b32 v186, v192, v192, v42\n" \
"v_perm_b32 v187, v192, v192, v43\n" \
"buffer_load_dwordx4 v20, s[80:83], s76 offen offset:1024 sc1 lds\n" \
"v_perm_b32 v200, v208, v208, v40\n" \
"v_perm_b32 v201, v208, v208, v41\n" \
"v_perm_b32 v202, v208, v208, v42\n" \
"v_perm_b32 v203, v208, v208, v43\n" \
"v_cvt_pk_bf16_f32 v224, v224, v226\n" \
"v_cvt_pk_bf16_f32 v140, v228, v229\n" \
"v_cvt_pk_bf16_f32 v141, v230, v231\n" \
"v_mfma_f32_16x16x32_bf16 v[116:119], v[134:137], v[184:187], 0\n" \
"v_mfma_f32_16x16x32_bf16 v[120:123], v[136:139], v[200:203], 0\n" \
"v_perm_b32 v216, v224, v224, v40\n" \
"v_perm_b32 v217, v224, v224, v41\n" \
"v_perm_b32 v218, v224, v224, v42\n" \
"v_perm_b32 v219, v224, v224, v43\n" \
"v_cvt_pk_bf16_f32 v240, v240, v242\n" \
"v_cvt_pk_bf16_f32 v142, v244, v245\n" \
"v_cvt_pk_bf16_f32 v143, v246, v247\n" \
"v_mfma_f32_16x16x32_bf16 v[36:39], v[138:141], v[216:219], 0\n" \
"v_perm_b32 v232, v240, v240, v40\n" \
"v_perm_b32 v233, v240, v240, v41\n" \
"v_perm_b32 v234, v240, v240, v42\n" \
"v_perm_b32 v235, v240, v240, v43\n" \
"v_exp_f32_e64 v45, v45\n" \
"v_mov_b32_dpp v118, v122 quad_perm:[0,1,2,3] row_mask:0x2 bank_mask:0xf\n" \
"v_mfma_f32_16x16x32_bf16 v[48:51], v[140:143], v[232:235], 0\n" \
"v_cvt_pk_bf16_f32 v124, v252, v253\n" \
"v_cvt_pk_bf16_f32 v125, v254, v255\n" \
"v_mov_b32_dpp v118, v38 quad_perm:[0,1,2,3] row_mask:0x4 bank_mask:0xf\n" \
"v_add_f32_e64 v45, 1.0, v45\n" \
"v_mfma_f32_16x16x32_bf16 v[192:195], v[124:127], v[184:187], 0\n" \
"v_mfma_f32_16x16x32_bf16 v[224:227], v[128:131], v[216:219], 0\n" \
"v_rcp_f32_e64 v45, v45\n" \
"v_mov_b32_dpp v116, v120 quad_perm:[0,1,2,3] row_mask:0x2 bank_mask:0xf\n" \
"v_mov_b32_dpp v118, v50 quad_perm:[0,1,2,3] row_mask:0x8 bank_mask:0xf\n" \
"v_mov_b32_dpp v117, v121 quad_perm:[0,1,2,3] row_mask:0x2 bank_mask:0xf\n" \
"v_rcp_f32_e64 v118, v118\n" \
"v_mfma_f32_16x16x32_bf16 v[208:211], v[126:129], v[200:203], 0\n" \
"v_mfma_f32_16x16x32_bf16 v[240:243], v[130:133], v[232:235], 0\n" \
"v_mov_b32_dpp v116, v36 quad_perm:[0,1,2,3] row_mask:0x4 bank_mask:0xf\n" \
"v_mov_b32_dpp v117, v37 quad_perm:[0,1,2,3] row_mask:0x4 bank_mask:0xf\n" \
"v_mul_f32_e64 v118, v45, v118\n" \
"v_mov_b32_dpp v116, v48 quad_perm:[0,1,2,3] row_mask:0x8 bank_mask:0xf\n" \
"v_mov_b32_dpp v117, v49 quad_perm:[0,1,2,3] row_mask:0x8 bank_mask:0xf\n" \
"v_mul_f32_e32 v116, v118, v116\n" \
"v_mul_f32_e32 v117, v118, v117\n" \
"buffer_store_dwordx4 a[12:15], v20, s[80:83], s89 offen offset:1024 sc1\n" \
"s_add_u32 s89, s45, 0x0\n" \
"v_mov_b32_e32 v122, v118\n" \
"v_cvt_pk_bf16_f32 v116, v116, v117\n" \
"v_accvgpr_read_b32 v52, a128\n" \
"v_permlane16_swap_b32_e32 v118, v122\n" \
"v_mov_b32_e32 v38, v118\n" \
"v_mov_b32_e64 v50, v122\n" \
"v_accvgpr_read_b32 v53, a129\n" \
"v_permlane32_swap_b32_e32 v118, v38\n" \
"v_permlane32_swap_b32_e32 v122, v50\n" \
"v_mul_f32_e32 v192, v118, v192\n" \
"v_mul_f32_e32 v193, v118, v193\n" \
"v_mul_f32_e32 v224, v38, v224\n" \
"v_mul_f32_e32 v225, v38, v225\n" \
"v_mul_f32_e32 v194, v118, v194\n" \
"v_mul_f32_e32 v195, v118, v195\n" \
"v_cvt_pk_bf16_f32 v192, v192, v193\n" \
"v_mul_f32_e32 v226, v38, v226\n" \
"v_mul_f32_e32 v227, v38, v227\n" \
"v_cvt_pk_bf16_f32 v224, v224, v225\n" \
"v_cvt_pk_bf16_f32 v193, v194, v195\n" \
"v_mul_f32_e32 v208, v122, v208\n" \
"v_mul_f32_e32 v209, v122, v209\n" \
"v_mul_f32_e32 v210, v122, v210\n" \
"v_mul_f32_e32 v211, v122, v211\n" \
"v_cvt_pk_bf16_f32 v225, v226, v227\n" \
"v_mul_f32_e32 v240, v50, v240\n" \
"v_mul_f32_e32 v241, v50, v241\n" \
"v_mul_f32_e32 v242, v50, v242\n" \
"v_mul_f32_e32 v243, v50, v243\n" \
"v_cvt_pk_bf16_f32 v194, v208, v209\n" \
"v_cvt_pk_bf16_f32 v195, v210, v211\n" \
"v_cvt_pk_bf16_f32 v226, v240, v241\n" \
"v_cvt_pk_bf16_f32 v227, v242, v243\n" \
"v_permlane32_swap_b32_e32 v192, v194\n" \
"v_permlane32_swap_b32_e32 v193, v195\n" \
"v_permlane32_swap_b32_e32 v224, v226\n" \
"v_permlane32_swap_b32_e32 v225, v227\n" \
"v_accvgpr_read_b32 v54, a130\n" \
"v_accvgpr_read_b32 v55, a131\n" \
"v_pk_mul_f32 v[52:53], v[52:53], s[70:71]\n" \
"v_accvgpr_read_b32 v56, a132\n" \
"v_accvgpr_read_b32 v57, a133\n" \
"v_accvgpr_read_b32 v58, a134\n" \
"v_accvgpr_read_b32 v59, a135\n" \
"v_pk_mul_f32 v[54:55], v[54:55], s[70:71]\n" \
"v_cvt_pk_bf16_f32 v52, v52, v53\n" \
"v_pk_mul_f32 v[56:57], v[56:57], s[70:71]\n" \
"v_pk_mul_f32 v[58:59], v[58:59], s[70:71]\n" \
"v_cvt_pk_bf16_f32 v53, v54, v55\n" \
"v_cvt_pk_bf16_f32 v54, v56, v57\n" \
"v_cvt_pk_bf16_f32 v55, v58, v59\n" \
"ds_write_b128 v27, v[52:55] offset:0\n" \
"buffer_store_dwordx4 a[40:43], v20, s[80:83], s88 offen offset:0 sc1\n" \
"s_add_u32 s88, s45, 0x0\n" \
"s_nop 0\n" \
"buffer_store_dword v116, v25, s[80:83], s51 offen sc1\n" \
"buffer_store_dwordx4 v[192:195], v24, s[80:83], s85 offen sc1\n" \
"buffer_store_dwordx4 v[224:227], v24, s[80:83], s87 offen sc1\n" \
"v_accvgpr_read_b32 v76, a224\n" \
"v_accvgpr_read_b32 v77, a225\n" \
"v_accvgpr_read_b32 v78, a226\n" \
"v_accvgpr_read_b32 v79, a227\n" \
"v_pk_mul_f32 v[76:77], v[76:77], s[70:71]\n" \
"v_accvgpr_read_b32 v52, a136\n" \
"v_accvgpr_read_b32 v53, a137\n" \
"v_accvgpr_read_b32 v80, a228\n" \
"v_accvgpr_read_b32 v81, a229\n" \
"v_accvgpr_read_b32 v82, a230\n" \
"v_accvgpr_read_b32 v83, a231\n" \
"v_pk_mul_f32 v[78:79], v[78:79], s[70:71]\n" \
"v_cvt_pk_bf16_f32 v76, v76, v77\n" \
"v_accvgpr_read_b32 v54, a138\n" \
"v_accvgpr_read_b32 v55, a139\n" \
"v_pk_mul_f32 v[52:53], v[52:53], s[70:71]\n" \
"v_pk_mul_f32 v[80:81], v[80:81], s[70:71]\n" \
"v_pk_mul_f32 v[82:83], v[82:83], s[70:71]\n" \
"v_cvt_pk_bf16_f32 v77, v78, v79\n" \
"v_accvgpr_read_b32 v56, a140\n" \
"v_accvgpr_read_b32 v57, a141\n" \
"v_accvgpr_read_b32 v58, a142\n" \
"v_accvgpr_read_b32 v59, a143\n" \
"v_pk_mul_f32 v[54:55], v[54:55], s[70:71]\n" \
"v_cvt_pk_bf16_f32 v52, v52, v53\n" \
"v_cvt_pk_bf16_f32 v78, v80, v81\n" \
"v_cvt_pk_bf16_f32 v79, v82, v83\n" \
"ds_write_b128 v27, v[76:79] offset:12288\n" \
"v_pk_mul_f32 v[56:57], v[56:57], s[70:71]\n" \
"v_pk_mul_f32 v[58:59], v[58:59], s[70:71]\n" \
"v_cvt_pk_bf16_f32 v53, v54, v55\n" \
"v_cvt_pk_bf16_f32 v54, v56, v57\n" \
"v_cvt_pk_bf16_f32 v55, v58, v59\n" \
"ds_write_b128 v27, v[52:55] offset:1024\n" \
"v_accvgpr_read_b32 v76, a208\n" \
"v_accvgpr_read_b32 v77, a209\n" \
"v_accvgpr_read_b32 v78, a210\n" \
"v_accvgpr_read_b32 v79, a211\n" \
"v_accvgpr_read_b32 v52, a168\n" \
"v_accvgpr_read_b32 v53, a169\n" \
"v_mfma_f32_16x16x32_bf16 v[76:79], v[152:155], v[104:107], v[76:79]\n" \
"v_accvgpr_read_b32 v54, a170\n" \
"v_accvgpr_read_b32 v55, a171\n" \
"v_mfma_f32_16x16x32_bf16 v[76:79], v[148:151], v[100:103], v[76:79]\n" \
"v_accvgpr_read_b32 v56, a172\n" \
"v_accvgpr_read_b32 v57, a173\n" \
"v_accvgpr_read_b32 v58, a174\n" \
"v_accvgpr_read_b32 v59, a175\n" \
"v_pk_mul_f32 v[52:53], v[52:53], s[70:71]\n" \
"v_pk_mul_f32 v[54:55], v[54:55], s[70:71]\n" \
"v_cvt_pk_bf16_f32 v52, v52, v53\n" \
"v_pk_mul_f32 v[56:57], v[56:57], s[70:71]\n" \
"v_pk_mul_f32 v[58:59], v[58:59], s[70:71]\n" \
"buffer_store_dwordx4 a[44:47], v20, s[80:83], s89 offen offset:1024 sc1\n" \
"s_add_u32 s89, s45, 0x0\n" \
"s_nop 0\n" \
"v_cvt_pk_bf16_f32 v53, v54, v55\n" \
"v_cvt_pk_bf16_f32 v54, v56, v57\n" \
"v_cvt_pk_bf16_f32 v55, v58, v59\n" \
"ds_write_b128 v27, v[52:55] offset:5120\n" \
"v_accvgpr_read_b32 v80, a212\n" \
"v_accvgpr_read_b32 v81, a213\n" \
"v_accvgpr_read_b32 v82, a214\n" \
"v_accvgpr_read_b32 v83, a215\n" \
"v_accvgpr_read_b32 v52, a232\n" \
"v_accvgpr_read_b32 v53, a233\n" \
"v_mfma_f32_16x16x32_bf16 v[80:83], v[156:159], v[100:103], v[80:83]\n" \
"v_accvgpr_read_b32 v54, a234\n" \
"v_accvgpr_read_b32 v55, a235\n" \
"v_mfma_f32_16x16x32_bf16 v[80:83], v[160:163], v[104:107], v[80:83]\n" \
"v_accvgpr_read_b32 v56, a236\n" \
"v_accvgpr_read_b32 v57, a237\n" \
"v_accvgpr_read_b32 v58, a238\n" \
"v_accvgpr_read_b32 v59, a239\n" \
"v_pk_mul_f32 v[52:53], v[52:53], s[70:71]\n" \
"v_pk_mul_f32 v[54:55], v[54:55], s[70:71]\n" \
"v_cvt_pk_bf16_f32 v52, v52, v53\n" \
"v_pk_mul_f32 v[56:57], v[56:57], s[70:71]\n" \
"v_pk_mul_f32 v[58:59], v[58:59], s[70:71]\n" \
"v_cvt_pk_bf16_f32 v53, v54, v55\n" \
"v_cvt_pk_bf16_f32 v54, v56, v57\n" \
"v_cvt_pk_bf16_f32 v55, v58, v59\n" \
"ds_write_b128 v27, v[52:55] offset:13312\n" \
"v_accvgpr_read_b32 v52, a216\n" \
"v_accvgpr_read_b32 v53, a217\n" \
"v_accvgpr_read_b32 v54, a218\n" \
"v_accvgpr_read_b32 v55, a219\n" \
"v_accvgpr_read_b32 v60, a160\n" \
"v_accvgpr_read_b32 v61, a161\n" \
"v_mfma_f32_16x16x32_bf16 v[52:55], v[168:171], v[104:107], v[52:55]\n" \
"v_accvgpr_read_b32 v62, a162\n" \
"v_accvgpr_read_b32 v63, a163\n" \
"v_mfma_f32_16x16x32_bf16 v[52:55], v[164:167], v[100:103], v[52:55]\n" \
"v_accvgpr_read_b32 v56, a220\n" \
"v_accvgpr_read_b32 v57, a221\n" \
"v_accvgpr_read_b32 v58, a222\n" \
"v_accvgpr_read_b32 v59, a223\n" \
"v_pk_mul_f32 v[60:61], v[60:61], s[70:71]\n" \
"v_accvgpr_read_b32 v64, a164\n" \
"v_mfma_f32_16x16x32_bf16 v[56:59], v[172:175], v[100:103], v[56:59]\n" \
"v_accvgpr_read_b32 v65, a165\n" \
"v_accvgpr_read_b32 v66, a166\n" \
"v_mfma_f32_16x16x32_bf16 v[56:59], v[176:179], v[104:107], v[56:59]\n" \
"v_accvgpr_read_b32 v67, a167\n" \
"buffer_store_dwordx4 a[72:75], v20, s[80:83], s88 offen offset:2048 sc1\n" \
"s_add_u32 s88, s45, 0x1000\n" \
"v_pk_mul_f32 v[62:63], v[62:63], s[70:71]\n" \
"v_cvt_pk_bf16_f32 v60, v60, v61\n" \
"v_pk_mul_f32 v[64:65], v[64:65], s[70:71]\n" \
"v_pk_mul_f32 v[66:67], v[66:67], s[70:71]\n" \
"v_cvt_pk_bf16_f32 v61, v62, v63\n" \
"v_cvt_pk_bf16_f32 v62, v64, v65\n" \
"v_cvt_pk_bf16_f32 v63, v66, v67\n" \
"ds_write_b128 v27, v[60:63] offset:4096\n" \
"v_accvgpr_read_b32 v68, a192\n" \
"v_accvgpr_read_b32 v69, a193\n" \
"v_accvgpr_read_b32 v60, a200\n" \
"v_accvgpr_read_b32 v61, a201\n" \
"v_accvgpr_read_b32 v70, a194\n" \
"v_accvgpr_read_b32 v71, a195\n" \
"v_pk_mul_f32 v[68:69], v[68:69], s[70:71]\n" \
"v_accvgpr_read_b32 v62, a202\n" \
"v_accvgpr_read_b32 v63, a203\n" \
"v_pk_mul_f32 v[60:61], v[60:61], s[70:71]\n" \
"v_accvgpr_read_b32 v72, a196\n" \
"v_accvgpr_read_b32 v73, a197\n" \
"v_accvgpr_read_b32 v74, a198\n" \
"v_accvgpr_read_b32 v75, a199\n" \
"v_pk_mul_f32 v[70:71], v[70:71], s[70:71]\n" \
"v_cvt_pk_bf16_f32 v68, v68, v69\n" \
"v_accvgpr_read_b32 v64, a204\n" \
"v_accvgpr_read_b32 v65, a205\n" \
"v_accvgpr_read_b32 v66, a206\n" \
"v_accvgpr_read_b32 v67, a207\n" \
"v_pk_mul_f32 v[62:63], v[62:63], s[70:71]\n" \
"v_cvt_pk_bf16_f32 v60, v60, v61\n" \
"v_accvgpr_read_b32 v100, a240\n" \
"v_accvgpr_read_b32 v101, a241\n" \
"v_accvgpr_read_b32 v102, a242\n" \
"v_accvgpr_read_b32 v103, a243\n" \
"v_pk_mul_f32 v[72:73], v[72:73], s[70:71]\n" \
"v_pk_mul_f32 v[74:75], v[74:75], s[70:71]\n" \
"v_mfma_f32_16x16x32_bf16 v[100:103], v[152:155], v[112:115], v[100:103]\n" \
"v_cvt_pk_bf16_f32 v69, v70, v71\n" \
"v_cvt_pk_bf16_f32 v61, v62, v63\n" \
"v_mfma_f32_16x16x32_bf16 v[100:103], v[148:151], v[108:111], v[100:103]\n" \
"v_cvt_pk_bf16_f32 v70, v72, v73\n" \
"v_cvt_pk_bf16_f32 v71, v74, v75\n" \
"ds_write_b128 v27, v[68:71] offset:8192\n" \
"v_pk_mul_f32 v[64:65], v[64:65], s[70:71]\n" \
"v_pk_mul_f32 v[66:67], v[66:67], s[70:71]\n" \
"v_cvt_pk_bf16_f32 v62, v64, v65\n" \
"v_cvt_pk_bf16_f32 v63, v66, v67\n" \
"ds_write_b128 v27, v[60:63] offset:9216\n" \
"v_accvgpr_read_b32 v84, a144\n" \
"buffer_store_dwordx4 a[76:79], v20, s[80:83], s89 offen offset:3072 sc1\n" \
"s_add_u32 s89, s45, 0x1000\n" \
"v_accvgpr_read_b32 v85, a145\n" \
"v_accvgpr_read_b32 v68, a152\n" \
"v_accvgpr_read_b32 v69, a153\n" \
"v_accvgpr_read_b32 v92, a176\n" \
"v_accvgpr_read_b32 v93, a177\n" \
"v_accvgpr_read_b32 v60, a184\n" \
"v_accvgpr_read_b32 v61, a185\n" \
"v_accvgpr_read_b32 v86, a146\n" \
"v_accvgpr_read_b32 v87, a147\n" \
"v_pk_mul_f32 v[84:85], v[84:85], s[70:71]\n" \
"v_accvgpr_read_b32 v70, a154\n" \
"v_accvgpr_read_b32 v71, a155\n" \
"v_pk_mul_f32 v[68:69], v[68:69], s[70:71]\n" \
"v_accvgpr_read_b32 v94, a178\n" \
"v_accvgpr_read_b32 v95, a179\n" \
"v_pk_mul_f32 v[92:93], v[92:93], s[70:71]\n" \
"v_accvgpr_read_b32 v62, a186\n" \
"v_accvgpr_read_b32 v63, a187\n" \
"v_pk_mul_f32 v[60:61], v[60:61], s[70:71]\n" \
"v_pk_mul_f32 v[76:77], v[76:77], s[70:71]\n" \
"v_pk_mul_f32 v[52:53], v[52:53], s[70:71]\n" \
"v_pk_mul_f32 v[100:101], v[100:101], s[70:71]\n" \
"v_accvgpr_read_b32 v88, a148\n" \
"v_accvgpr_read_b32 v89, a149\n" \
"v_accvgpr_read_b32 v90, a150\n" \
"v_accvgpr_read_b32 v91, a151\n" \
"v_pk_mul_f32 v[86:87], v[86:87], s[70:71]\n" \
"v_cvt_pk_bf16_f32 v84, v84, v85\n" \
"v_accvgpr_read_b32 v72, a156\n" \
"v_accvgpr_read_b32 v73, a157\n" \
"v_accvgpr_read_b32 v74, a158\n" \
"v_accvgpr_read_b32 v75, a159\n" \
"v_pk_mul_f32 v[70:71], v[70:71], s[70:71]\n" \
"v_cvt_pk_bf16_f32 v68, v68, v69\n" \
"v_accvgpr_read_b32 v96, a180\n" \
"v_accvgpr_read_b32 v97, a181\n" \
"v_accvgpr_read_b32 v98, a182\n" \
"v_accvgpr_read_b32 v99, a183\n" \
"v_pk_mul_f32 v[94:95], v[94:95], s[70:71]\n" \
"v_cvt_pk_bf16_f32 v92, v92, v93\n" \
"v_accvgpr_read_b32 v64, a188\n" \
"v_accvgpr_read_b32 v65, a189\n" \
"v_accvgpr_read_b32 v66, a190\n" \
"v_accvgpr_read_b32 v67, a191\n" \
"v_pk_mul_f32 v[62:63], v[62:63], s[70:71]\n" \
"v_cvt_pk_bf16_f32 v60, v60, v61\n" \
"v_pk_mul_f32 v[78:79], v[78:79], s[70:71]\n" \
"v_cvt_pk_bf16_f32 v76, v76, v77\n" \
"v_pk_mul_f32 v[54:55], v[54:55], s[70:71]\n" \
"v_cvt_pk_bf16_f32 v52, v52, v53\n" \
"v_pk_mul_f32 v[102:103], v[102:103], s[70:71]\n" \
"v_cvt_pk_bf16_f32 v100, v100, v101\n" \
"v_pk_mul_f32 v[88:89], v[88:89], s[70:71]\n" \
"v_pk_mul_f32 v[90:91], v[90:91], s[70:71]\n" \
"v_cvt_pk_bf16_f32 v85, v86, v87\n" \
"v_pk_mul_f32 v[72:73], v[72:73], s[70:71]\n" \
"v_pk_mul_f32 v[74:75], v[74:75], s[70:71]\n" \
"v_cvt_pk_bf16_f32 v69, v70, v71\n" \
"v_pk_mul_f32 v[96:97], v[96:97], s[70:71]\n" \
"v_pk_mul_f32 v[98:99], v[98:99], s[70:71]\n" \
"buffer_store_dwordx4 a[104:107], v20, s[80:83], s88 offen offset:0 sc1\n" \
"v_cvt_pk_bf16_f32 v93, v94, v95\n" \
"v_pk_mul_f32 v[64:65], v[64:65], s[70:71]\n" \
"v_pk_mul_f32 v[66:67], v[66:67], s[70:71]\n" \
"v_cvt_pk_bf16_f32 v61, v62, v63\n" \
"v_pk_mul_f32 v[80:81], v[80:81], s[70:71]\n" \
"v_pk_mul_f32 v[82:83], v[82:83], s[70:71]\n" \
"v_cvt_pk_bf16_f32 v77, v78, v79\n" \
"v_pk_mul_f32 v[56:57], v[56:57], s[70:71]\n" \
"v_pk_mul_f32 v[58:59], v[58:59], s[70:71]\n" \
"v_cvt_pk_bf16_f32 v53, v54, v55\n" \
"v_accvgpr_read_b32 v104, a244\n" \
"v_accvgpr_read_b32 v105, a245\n" \
"v_accvgpr_read_b32 v106, a246\n" \
"v_accvgpr_read_b32 v107, a247\n" \
"v_cvt_pk_bf16_f32 v101, v102, v103\n" \
"v_cvt_pk_bf16_f32 v86, v88, v89\n" \
"v_mfma_f32_16x16x32_bf16 v[104:107], v[156:159], v[108:111], v[104:107]\n" \
"v_cvt_pk_bf16_f32 v87, v90, v91\n" \
"v_cvt_pk_bf16_f32 v70, v72, v73\n" \
"ds_write_b128 v21, v[84:87] offset:16384\n" \
"v_mfma_f32_16x16x32_bf16 v[104:107], v[160:163], v[112:115], v[104:107]\n" \
"v_cvt_pk_bf16_f32 v71, v74, v75\n" \
"ds_write_b128 v21, v[68:71] offset:17408\n" \
"v_cvt_pk_bf16_f32 v94, v96, v97\n" \
"v_cvt_pk_bf16_f32 v95, v98, v99\n" \
"ds_write_b128 v21, v[92:95] offset:20480\n" \
"v_cvt_pk_bf16_f32 v62, v64, v65\n" \
"v_cvt_pk_bf16_f32 v63, v66, v67\n" \
"ds_write_b128 v21, v[60:63] offset:21504\n" \
"v_pk_mul_f32 v[104:105], v[104:105], s[70:71]\n" \
"v_pk_mul_f32 v[106:107], v[106:107], s[70:71]\n" \
"v_cvt_pk_bf16_f32 v78, v80, v81\n" \
"v_cvt_pk_bf16_f32 v79, v82, v83\n" \
"ds_write_b128 v21, v[76:79] offset:24576\n" \
"v_cvt_pk_bf16_f32 v54, v56, v57\n" \
"v_cvt_pk_bf16_f32 v55, v58, v59\n" \
"ds_write_b128 v21, v[52:55] offset:25600\n" \
"v_cvt_pk_bf16_f32 v102, v104, v105\n" \
"v_cvt_pk_bf16_f32 v103, v106, v107\n" \
"ds_write_b128 v21, v[100:103] offset:28672\n" \
"buffer_store_dwordx4 a[108:111], v20, s[80:83], s89 offen offset:1024 sc1\n" \
"v_accvgpr_read_b32 v84, a248\n" \
"v_accvgpr_read_b32 v85, a249\n" \
"v_accvgpr_read_b32 v86, a250\n" \
"v_accvgpr_read_b32 v87, a251\n" \
"v_accvgpr_read_b32 v88, a252\n" \
"v_accvgpr_read_b32 v89, a253\n" \
"v_mfma_f32_16x16x32_bf16 v[84:87], v[168:171], v[112:115], v[84:87]\n" \
"v_accvgpr_read_b32 v90, a254\n" \
"v_accvgpr_read_b32 v91, a255\n" \
"v_mfma_f32_16x16x32_bf16 v[84:87], v[164:167], v[108:111], v[84:87]\n" \
"v_sub_f32_e32 v44, 1.0, v45\n" \
"v_mfma_f32_16x16x32_bf16 v[88:91], v[172:175], v[108:111], v[88:91]\n" \
"ds_write_b32 v26, v44\n" \
"v_mfma_f32_16x16x32_bf16 v[88:91], v[176:179], v[112:115], v[88:91]\n" \
"s_nop 3\n" \
"v_pk_mul_f32 v[84:85], v[84:85], s[70:71]\n" \
"v_pk_mul_f32 v[86:87], v[86:87], s[70:71]\n" \
"v_cvt_pk_bf16_f32 v84, v84, v85\n" \
"v_cvt_pk_bf16_f32 v85, v86, v87\n" \
"v_pk_mul_f32 v[88:89], v[88:89], s[70:71]\n" \
"v_pk_mul_f32 v[90:91], v[90:91], s[70:71]\n" \
"v_cvt_pk_bf16_f32 v86, v88, v89\n" \
"v_cvt_pk_bf16_f32 v87, v90, v91\n" \
"ds_write_b128 v21, v[84:87] offset:29696\n" \
"L_end_%=:\n"
#define PROJ_VCLOB "v21","v24","v25","v33","v34","v35","v36","v37","v38","v39","v40","v41","v42","v43","v44","v45","v46","v47","v48","v49","v50","v51","v52","v53","v54","v55","v56","v57","v58","v59","v60","v61","v62","v63","v64","v65","v66","v67","v68","v69","v70","v71","v72","v73","v74","v75","v76","v77","v78","v79","v80","v81","v82","v83","v84","v85","v86","v87","v88","v89","v90","v91","v92","v93","v94","v95","v96","v97","v98","v99","v100","v101","v102","v103","v104","v105","v106","v107","v108","v109","v110","v111","v112","v113","v114","v115","v116","v117","v118","v119","v120","v121","v122","v123","v124","v125","v126","v127","v128","v129","v130","v131","v132","v133","v134","v135","v136","v137","v138","v139","v140","v141","v142","v143","v144","v145","v146","v147","v148","v149","v150","v151","v152","v153","v154","v155","v156","v157","v158","v159","v160","v161","v162","v163","v164","v165","v166","v167","v168","v169","v170","v171","v172","v173","v174","v175","v176","v177","v178","v179","v180","v181","v182","v183","v184","v185","v186","v187","v188","v189","v190","v191","v192","v193","v194","v195","v196","v197","v198","v199","v200","v201","v202","v203","v204","v205","v206","v207","v208","v209","v210","v211","v212","v213","v214","v215","v216","v217","v218","v219","v220","v221","v222","v223","v224","v225","v226","v227","v228","v229","v230","v231","v232","v233","v234","v235","v236","v237","v238","v239","v240","v241","v242","v243","v244","v245","v246","v247","v248","v249","v250","v251","v252","v253","v254","v255"
#define PROJ_SCLOB "s37","s38","s39","s40","s41","s42","s43","s44","s45","s46","s47","s48","s49","s50","s51","s52","s53","s54","s55","s62","s63","s76","s77","s78","s79","s80","s81","s82","s83","s84","s85","s86","s87","s88","s89","s94","s95","s96","s97","s98","s99","s100","s101"

#define PLO(p) ((unsigned)(size_t)(p))
#define PHI(p) ((unsigned)((size_t)(p) >> 32))
__device__ __forceinline__ unsigned proj_phase(const u16* __restrict__ x, const u16* __restrict__ ckv, const u16* __restrict__ wq, const u16* __restrict__ wuk,
                                                       const u16* __restrict__ wuv, const u16* __restrict__ wqkv, const u16* __restrict__ gw, const u16* wo, const u16* wob, const u16* out,
                                                       char* __restrict__ ws, char* lds, const unsigned wid, unsigned& tag, u32x4 (&vp)[5], u32x4 (&sa)[5], unsigned (&sd)[2]) {
    typedef unsigned u32x2 __attribute__((ext_vector_type(2)));
    unsigned* const flags = (unsigned*)(ws + WS_F);
    const int b = blockIdx.x, h = (b & 7) * 4 + ((b >> 3) & 3), mt = b >> 5;
    const unsigned L0 = (unsigned)(size_t)lds, rb = (wid < 2u ? 15u - mt : mt) * 128u;
    RTS(0);
#define PST() do {} while (0)
    const unsigned lane = threadIdx.x & 63u, wr = wid >> 1, wc = wid & 1u;
    PST();
    unsigned vAc[2], vB[2];
    const unsigned sp = b & 7, nt = (b >> 3) & 3, rot = (PROJ_ROT) & 7u;
#pragma unroll
    for (int par = 0; par < 2; ++par) {
        const unsigned lr = lane >> 3, cc = (lane & 7u) ^ (4u * par + (lr >> 1)), r0 = rb + wc * 64u + lr;
        vAc[par] = r0 * (DC * 2) + cc * 16u;
        const unsigned br = lane >> 4, p16 = lane & 15u, sc = (((p16 >> 1) ^ (br | (par << 2))) << 1) | (p16 & 1u);
        vB[par] = (16u * wid + br) * (NBIG * 2) + h * (HD * 2) + sc * 16u;
    }
#define SRD(p, n) (u32x4){PLO(p), PHI(p) & 0xffffu, n, 0x00020000u}
    u32x4 sC = SRD(ckv, S * DC * 2), sK = SRD(wuk, DC * NBIG * 2), sV = SRD(wuv, DC * NBIG * 2);
#define PRE(n) do { SBAR(); asm volatile(PROJ_PRE##n :: "{v22}"(vAc[0]), "{v23}"(vAc[1]), "{v26}"(vB[0]), "{v27}"(vB[1]), "{s[64:67]}"(sC), "{s[68:71]}"(sK), "{s[72:75]}"(sV), \
                 [mA] "s"(L0 + wid * 8192u), [mB] "s"(L0 + 65536u + wid * 4096u), [rc] "s"(rot * 128u) : "memory", "m0", "scc", "s62", "s63", "s84", "s85"); SBAR(); } while (0)
    PRE(0);
    const unsigned fr = lane & 15u, fg = lane >> 4, r4 = (lane >> 2) & 3u, sB = r4 | ((fg & 1u) << 2);
    const unsigned aA0 = L0 + (wr * 64u + fr) * 128u + ((fg ^ (fr >> 1)) << 4);
    unsigned vl = lane * 16u, r32 = L0 + 65536u + (8u * fg + r4) * 256u + (lane & 3u) * 8u + ((sB ^ (4u * wc)) << 5);
    u32x2 r28 = {aA0, aA0 ^ 64u};
    asm volatile("" : "+v"(vl), "+v"(r28), "+v"(r32));
    PST();
    PRE(1);
    PST();
    PRE(2);
    PST();
    asm volatile("s_mov_b64 exec, 0\n s_cmp_lt_u32 %0, 2\n s_cbranch_scc1 L_chA\n s_cmp_eq_u32 %0, 2\n s_cbranch_scc1 L_pf2\n s_branch L_pf3\n"
                 "L_chA: s_cmp_eq_u32 %0, 0\n s_cbranch_scc1 L_pf0\n s_branch L_pf1\n L_retA: s_mov_b64 exec, -1" :: "s"(wid) : "scc", "memory");
    PRE(3);
    PST();
    sa[0] = (u32x4){PLO(x), PHI(x), PLO(wq), PHI(wq)}; sa[1] = (u32x4){PLO(wqkv), PHI(wqkv), PLO(gw), PHI(gw)}; sa[2] = (u32x4){PLO(wo), PHI(wo), PLO(wob), PHI(wob)}; sa[3] = (u32x4){PLO(out), PHI(out), rot * 128u, 0u};
    asm volatile(PROJ_ASM
                 : [tag] "=&s"(tag), "+{s[8:11]}"(sa[0]), "+{s[12:15]}"(sa[1]), "+{s[16:19]}"(sa[2]), "+{s[20:23]}"(sa[3]), "=&{s[56:59]}"(sa[4]), "=&{s60}"(sd[0]), "=&{s61}"(sd[1]),
                   "+{s[64:67]}"(sC), "+{s[68:71]}"(sK), "+{s[72:75]}"(sV), "+{v22}"(vAc[0]), "+{v23}"(vAc[1]), "+{v26}"(vB[0]), "+{v27}"(vB[1]), "+{v[28:29]}"(r28), "+{v32}"(r32),
                   "={v[0:3]}"(vp[0]), "={v[4:7]}"(vp[1]), "={v[8:11]}"(vp[2]), "={v[12:15]}"(vp[3]), "={v[16:19]}"(vp[4])
                 : "{v20}"(vl), [wsl] "s"(PLO(ws)), [wsh] "s"(PHI(ws)), [b] "s"(b), [w] "s"(wid), [mA] "s"(L0 + wid * 8192u), [mB] "s"(L0 + 65536u + wid * 4096u), [mS] "s"(L0 + 151552u + wid * 512u)
                 : "memory", "m0", "vcc", "scc", PROJ_VCLOB, PROJ_SCLOB, ACLOB_LO, ACLOB_HI);
    RTS(1);
    return vl;
}

namespace att {
#define DI __device__ __forceinline__
constexpr int LDS_BYTES = 163840;
constexpr unsigned LDS_G = 159744u, LDS_L = 161792u;
typedef __bf16 bfx8 __attribute__((ext_vector_type(8)));
DI unsigned rfl(unsigned x) { return __builtin_amdgcn_readfirstlane(x); }
DI f32x4 mfma(u32x4 a, u32x4 b, f32x4 c) { return __builtin_amdgcn_mfma_f32_16x16x32_bf16(__builtin_bit_cast(bfx8, a), __builtin_bit_cast(bfx8, b), c, 0, 0, 0); }
typedef __bf16 bfx2 __attribute__((ext_vector_type(2)));
typedef float f32x2 __attribute__((ext_vector_type(2)));
DI unsigned cvtpk2(float lo, float hi) { f32x2 v = {lo, hi}; bfx2 b = __builtin_convertvector(v, bfx2); return __builtin_bit_cast(unsigned, b); }
DI void att_exact(const u16* __restrict__ x, const u16* __restrict__ wq, char* __restrict__ ws, unsigned h, unsigned r0, float g1) {
    const unsigned lane = lane_id(), g = lane >> 4, c = lane & 15u;
    f32x4 o[8];
#pragma unroll
    for (int n = 0; n < 8; ++n) o[n] = (f32x4){0.f, 0.f, 0.f, 0.f};
#pragma unroll 1
    for (unsigned kk = 0; kk < (unsigned)DM; kk += 32u) {
        const unsigned k = kk + 8u * g;
        const u32x4 xf = *reinterpret_cast<const u32x4*>(x + (r0 + c) * DM + k);
#pragma unroll
        for (int n = 0; n < 8; ++n) {
            const u16* w = wq + k * NBIG + h * HD + n * 16 + c; u32x4 wf;
#pragma unroll
            for (int e = 0; e < 4; ++e) wf[e] = (unsigned)w[2 * e * NBIG] | ((unsigned)w[(2 * e + 1) * NBIG] << 16);
            o[n] = mfma(wf, xf, o[n]);
        }
    }
    u32x4 q[4];
#pragma unroll
    for (int p = 0; p < 4; ++p) {
        const f32x4 a = o[2 * p] * 0.12751743082f, b = o[2 * p + 1] * 0.12751743082f;
        q[p] = (u32x4){cvtpk2(a[0], a[1]), cvtpk2(a[2], a[3]), cvtpk2(b[0], b[1]), cvtpk2(b[2], b[3])};
    }
#pragma unroll
    for (int n = 0; n < 8; ++n) o[n] = (f32x4){0.f, 0.f, 0.f, 0.f};
    const __amdgpu_buffer_rsrc_t srd = __builtin_amdgcn_make_buffer_rsrc((void*)(ws + WS_K + (size_t)h * 1048576), (short)0, 1048576, 0x00020000);
    float m = -3.0e38f, l = 0.f;
#pragma unroll 1
    for (unsigned k0 = 0; k0 <= r0 + 15u; k0 += 32u) {
        const unsigned tb = (k0 >> 6) * 32768u + (k0 & 32u) * 256u;
        f32x4 s[2]; float mx = m;
#pragma unroll
        for (int kt = 0; kt < 2; ++kt) {
            s[kt] = (f32x4){0.f, 0.f, 0.f, 0.f};
#pragma unroll
            for (int p = 0; p < 4; ++p) s[kt] = mfma(__builtin_amdgcn_raw_buffer_load_b128(srd, lane * 16u + (kt * 4 + p) * 1024u, tb, 16), q[p], s[kt]);
#pragma unroll
            for (int e = 0; e < 4; ++e) { if (k0 + 16u * kt + 4u * g + e > r0 + c) s[kt][e] = -3.0e38f; mx = fmaxf(mx, s[kt][e]); }
        }
        mx = fmaxf(mx, __shfl_xor(mx, 16)); mx = fmaxf(mx, __shfl_xor(mx, 32));
        const float al = __builtin_amdgcn_exp2f(m - mx); m = mx;
        u32x4 pf;
#pragma unroll
        for (int e = 0; e < 4; ++e) pf[e] = cvtpk2(__builtin_amdgcn_exp2f(s[e >> 1][2 * (e & 1)] - m), __builtin_amdgcn_exp2f(s[e >> 1][2 * (e & 1) + 1] - m));
        l *= al;
#pragma unroll
        for (int e = 0; e < 4; ++e) l += __uint_as_float(pf[e] << 16) + __uint_as_float(pf[e] & 0xffff0000u);
#pragma unroll
        for (int n = 0; n < 8; ++n) o[n] = mfma(__builtin_amdgcn_raw_buffer_load_b128(srd, lane * 16u + 16384u + ((n >> 2) * 8 + (n & 3)) * 1024u, tb - (k0 & 32u) * 128u, 16), pf, o[n] * al);
    }
    l += __shfl_xor(l, 16); l += __shfl_xor(l, 32);
    const float f = g1 / l;
    const __amdgpu_buffer_rsrc_t srdA = __builtin_amdgcn_make_buffer_rsrc((void*)(ws + WS_AF), (short)0, 128 * KS_TOT * 1024, 0x00020000);
#pragma unroll
    for (int p = 0; p < 4; ++p) {
        const f32x4 a = o[2 * p] * f, b = o[2 * p + 1] * f;
        __builtin_amdgcn_raw_buffer_store_b128((u32x4){cvtpk2(a[0], a[1]), cvtpk2(a[2], a[3]), cvtpk2(b[0], b[1]), cvtpk2(b[2], b[3])}, srdA, lane * 16u + p * 1024u, ((r0 >> 4) * KS_TOT + 18u + 4u * h) * 1024u, 16);
    }
}
}

#define ATT_ASM \
"L_att0:\n" \
"s_mov_b32 s101, 0\n" \
"s_waitcnt vmcnt(20) lgkmcnt(0)\n" \
"s_barrier\n" \
"v_accvgpr_write_b32 a216, 0\n" \
"v_accvgpr_write_b32 a217, 0\n" \
"v_accvgpr_write_b32 a218, 0\n" \
"v_accvgpr_write_b32 a219, 0\n" \
"v_and_b32_e32 v63, 0x300, v20\n" \
"v_add_u32_e32 v58, s20, v20\n" \
"v_mfma_f32_32x32x16_bf16 v[128:143], a[216:219], a[216:219], 0\n" \
"ds_read_b128 v[112:115], v63 offset:0\n" \
"ds_read_b128 a[0:3], v58 offset:0\n" \
"ds_read_b128 a[16:19], v58 offset:8192\n" \
"ds_read_b128 v[116:119], v63 offset:1024\n" \
"ds_read_b128 a[4:7], v58 offset:1024\n" \
"v_add_u32_e32 v59, s21, v20\n" \
"v_mfma_f32_32x32x16_bf16 v[144:159], a[216:219], a[216:219], 0\n" \
"ds_read_b128 a[32:35], v59 offset:0\n" \
"ds_read_b128 a[48:51], v59 offset:8192\n" \
"s_sub_u32 s67, s12, 0xc000\n" \
"s_mov_b32 s65, s12\n" \
"v_add_u32_e32 v55, s67, v20\n" \
"v_add_u32_e32 v52, 0xc000, v55\n" \
"v_mfma_f32_32x32x16_bf16 v[160:175], a[216:219], a[216:219], 0\n" \
"ds_read_b128 a[20:23], v58 offset:9216\n" \
"s_xor_b32 m0, s65, 0x10000\n" \
"ds_read_b128 a[36:39], v59 offset:1024\n" \
"buffer_load_dwordx4 v52, s[8:11], 0 offen offset:0 sc1 lds\n" \
"ds_read_b128 a[52:55], v59 offset:9216\n" \
"v_mfma_f32_32x32x16_bf16 v[176:191], a[216:219], a[216:219], 0\n" \
"buffer_load_dwordx4 v52, s[8:11], 0 offen offset:1024 sc1 lds\n" \
"ds_read_b128 v[120:123], v63 offset:2048\n" \
"v_add_u32_e32 v60, s15, v20\n" \
"v_add_u32_e32 v60, 0x10000, v60\n" \
"v_mov_b32_e32 v56, s62\n" \
"v_mfma_f32_32x32x16_bf16 v[192:207], a[216:219], a[216:219], 0\n" \
"ds_read_b128 a[8:11], v58 offset:2048\n" \
"ds_read_b128 a[24:27], v58 offset:10240\n" \
"v_readlane_b32 s68, v11, 0\n" \
"v_bfe_i32 v21, v20, 4, 1\n" \
"v_mov_b32_e32 v54, 0x3f803f80\n" \
"v_mov_b32_e32 v57, 0x3f003f00\n" \
"v_mfma_f32_32x32x16_bf16 v[208:223], a[216:219], a[216:219], 0\n" \
"ds_read_b128 a[40:43], v59 offset:2048\n" \
"ds_read_b128 a[56:59], v59 offset:10240\n" \
"s_waitcnt lgkmcnt(14)\n" \
"v_xor_b32_e32 v112, 0x80008000, v112\n" \
"v_xor_b32_e32 v113, 0x80008000, v113\n" \
"v_xor_b32_e32 v114, 0x80008000, v114\n" \
"v_mfma_f32_32x32x16_bf16 v[224:239], a[216:219], a[216:219], 0\n" \
"ds_read_b128 v[124:127], v63 offset:3072\n" \
"ds_read_b128 a[12:15], v58 offset:3072\n" \
"v_xor_b32_e32 v115, 0x80008000, v115\n" \
"v_and_b32_e32 v21, 0xaaaaaaaa, v21\n" \
"v_accvgpr_write_b32 a220, v112\n" \
"s_waitcnt lgkmcnt(13)\n" \
"v_xor_b32_e32 v116, 0x80008000, v116\n" \
"v_mfma_f32_32x32x16_bf16 v[240:255], a[216:219], a[216:219], 0\n" \
"ds_read_b128 a[28:31], v58 offset:11264\n" \
"ds_read_b128 a[44:47], v59 offset:3072\n" \
"v_xor_b32_e32 v117, 0x80008000, v117\n" \
"v_xor_b32_e32 v118, 0x80008000, v118\n" \
"v_xor_b32_e32 v119, 0x80008000, v119\n" \
"v_accvgpr_write_b32 a221, v113\n" \
"v_mfma_f32_32x32x16_bf16 a[192:207], a[216:219], a[216:219], 0\n" \
"ds_read_b128 a[60:63], v59 offset:11264\n" \
"ds_read_b128 a[160:163], v20 offset:0\n" \
"v_accvgpr_write_b32 a222, v114\n" \
"v_accvgpr_write_b32 a223, v115\n" \
"v_readlane_b32 s80, v11, 9\n" \
"v_readlane_b32 s81, v11, 10\n" \
"v_mfma_f32_16x16x32_bf16 v[32:35], v[112:115], a[0:3], 0\n" \
"ds_read_b128 a[164:167], v20 offset:1024\n" \
"ds_read_b128 a[168:171], v20 offset:2048\n" \
"v_mfma_f32_16x16x32_bf16 v[36:39], v[112:115], a[16:19], 0\n" \
"ds_read_b128 a[172:175], v20 offset:3072\n" \
"s_waitcnt lgkmcnt(15)\n" \
"v_mfma_f32_16x16x32_bf16 v[40:43], v[112:115], a[32:35], 0\n" \
"ds_read_b128 a[188:191], v20 offset:7168\n" \
"s_waitcnt lgkmcnt(14)\n" \
"v_xor_b32_e32 v120, 0x80008000, v120\n" \
"v_mfma_f32_16x16x32_bf16 v[44:47], v[112:115], a[48:51], 0\n" \
"ds_read_b128 a[184:187], v20 offset:6144\n" \
"v_xor_b32_e32 v121, 0x80008000, v121\n" \
"v_mfma_f32_16x16x32_bf16 v[32:35], v[116:119], a[4:7], v[32:35]\n" \
"v_xor_b32_e32 v122, 0x80008000, v122\n" \
"v_xor_b32_e32 v123, 0x80008000, v123\n" \
"v_mfma_f32_16x16x32_bf16 v[36:39], v[116:119], a[20:23], v[36:39]\n" \
"ds_read_b128 a[180:183], v20 offset:5120\n" \
"ds_read_b128 a[176:179], v20 offset:4096\n" \
"v_mfma_f32_16x16x32_bf16 v[40:43], v[116:119], a[36:39], v[40:43]\n" \
"ds_read_b128 a[128:131], v20 offset:8192\n" \
"s_mov_b32 s82, 0x47fca0\n" \
"v_mfma_f32_16x16x32_bf16 v[44:47], v[116:119], a[52:55], v[44:47]\n" \
"s_waitcnt lgkmcnt(13)\n" \
"v_xor_b32_e32 v124, 0x80008000, v124\n" \
"v_mfma_f32_16x16x32_bf16 v[32:35], v[120:123], a[8:11], v[32:35]\n" \
"v_xor_b32_e32 v125, 0x80008000, v125\n" \
"v_xor_b32_e32 v126, 0x80008000, v126\n" \
"v_mfma_f32_16x16x32_bf16 v[36:39], v[120:123], a[24:27], v[36:39]\n" \
"ds_read_b128 a[132:135], v20 offset:9216\n" \
"v_xor_b32_e32 v127, 0x80008000, v127\n" \
"v_mfma_f32_16x16x32_bf16 v[40:43], v[120:123], a[40:43], v[40:43]\n" \
"ds_read_b128 a[136:139], v20 offset:10240\n" \
"ds_read_b128 a[140:143], v20 offset:11264\n" \
"v_mfma_f32_16x16x32_bf16 v[44:47], v[120:123], a[56:59], v[44:47]\n" \
"ds_read_b128 a[144:147], v20 offset:12288\n" \
"s_waitcnt lgkmcnt(15)\n" \
"v_mfma_f32_16x16x32_bf16 v[32:35], v[124:127], a[12:15], v[32:35]\n" \
"ds_read_b128 a[148:151], v20 offset:13312\n" \
"ds_read_b128 a[152:155], v20 offset:14336\n" \
"v_mfma_f32_16x16x32_bf16 v[36:39], v[124:127], a[28:31], v[36:39]\n" \
"ds_read_b128 a[156:159], v20 offset:15360\n" \
"s_waitcnt lgkmcnt(15)\n" \
"v_mfma_f32_16x16x32_bf16 v[40:43], v[124:127], a[44:47], v[40:43]\n" \
"ds_read_b128 a[64:67], v60 offset:16384\n" \
"s_mov_b32 s83, 0x20000\n" \
"v_mfma_f32_16x16x32_bf16 v[44:47], v[124:127], a[60:63], v[44:47]\n" \
"ds_read_b128 a[68:71], v60 offset:17408\n" \
"s_mov_b32 s100, 0x71800000\n" \
"v_mfma_f32_16x16x32_bf16 v[64:67], a[160:163], a[0:3], v[32:35]\n" \
"ds_read_b128 a[72:75], v60 offset:18432\n" \
"s_waitcnt lgkmcnt(15)\n" \
"v_mfma_f32_16x16x32_bf16 v[64:67], a[164:167], a[4:7], v[64:67]\n" \
"ds_read_b128 a[76:79], v60 offset:19456\n" \
"s_mov_b32 s63, s23\n" \
"v_mfma_f32_16x16x32_bf16 v[64:67], a[168:171], a[8:11], v[64:67]\n" \
"ds_read_b128 a[80:83], v60 offset:24576\n" \
"v_mov_b32_e32 v62, 0\n" \
"v_mfma_f32_16x16x32_bf16 v[64:67], a[172:175], a[12:15], v[64:67]\n" \
"ds_read_b128 a[84:87], v60 offset:25600\n" \
"s_waitcnt lgkmcnt(15)\n" \
"v_mfma_f32_16x16x32_bf16 v[68:71], a[188:191], a[12:15], v[32:35]\n" \
"ds_read_b128 a[88:91], v60 offset:26624\n" \
"v_accvgpr_write_b32 a212, v54\n" \
"v_mfma_f32_16x16x32_bf16 v[68:71], a[184:187], a[8:11], v[68:71]\n" \
"ds_read_b128 a[92:95], v60 offset:27648\n" \
"v_accvgpr_write_b32 a208, 0\n" \
"v_mfma_f32_16x16x32_bf16 v[68:71], a[180:183], a[4:7], v[68:71]\n" \
"ds_read_b128 a[96:99], v60 offset:0\n" \
"s_waitcnt lgkmcnt(15)\n" \
"v_mfma_f32_16x16x32_bf16 v[68:71], a[176:179], a[0:3], v[68:71]\n" \
"ds_read_b128 a[100:103], v60 offset:1024\n" \
"L_pf10:\n" \
"s_cbranch_execz L_pf14\n" \
"s_nop 0\n" \
"v_accvgpr_write_b32 a213, v54\n" \
"v_mfma_f32_16x16x32_bf16 v[72:75], a[160:163], a[16:19], v[36:39]\n" \
"ds_read_b128 a[104:107], v60 offset:2048\n" \
"v_accvgpr_write_b32 a209, 0\n" \
"v_mfma_f32_16x16x32_bf16 v[72:75], a[164:167], a[20:23], v[72:75]\n" \
"ds_read_b128 a[108:111], v60 offset:3072\n" \
"v_accvgpr_write_b32 a214, v54\n" \
"v_mfma_f32_16x16x32_bf16 v[72:75], a[168:171], a[24:27], v[72:75]\n" \
"ds_read_b128 a[112:115], v60 offset:8192\n" \
"v_accvgpr_write_b32 a210, 0\n" \
"v_mfma_f32_16x16x32_bf16 v[72:75], a[172:175], a[28:31], v[72:75]\n" \
"ds_read_b128 a[116:119], v60 offset:9216\n" \
"v_accvgpr_write_b32 a215, v54\n" \
"v_mfma_f32_16x16x32_bf16 v[76:79], a[188:191], a[28:31], v[36:39]\n" \
"ds_read_b128 a[120:123], v60 offset:10240\n" \
"v_accvgpr_write_b32 a211, 0\n" \
"v_mfma_f32_16x16x32_bf16 v[76:79], a[184:187], a[24:27], v[76:79]\n" \
"ds_read_b128 a[124:127], v60 offset:11264\n" \
"v_or_b32_e32 v21, 0x44444444, v21\n" \
"v_mfma_f32_16x16x32_bf16 v[76:79], a[180:183], a[20:23], v[76:79]\n" \
"v_exp_f32_e32 v64, v64\n" \
"v_mfma_f32_16x16x32_bf16 v[76:79], a[176:179], a[16:19], v[76:79]\n" \
"v_exp_f32_e32 v65, v65\n" \
"v_mfma_f32_16x16x32_bf16 v[80:83], a[160:163], a[32:35], v[40:43]\n" \
"v_exp_f32_e32 v66, v66\n" \
"v_mfma_f32_16x16x32_bf16 v[80:83], a[164:167], a[36:39], v[80:83]\n" \
"v_exp_f32_e32 v67, v67\n" \
"v_mfma_f32_16x16x32_bf16 v[80:83], a[168:171], a[40:43], v[80:83]\n" \
"v_exp_f32_e32 v68, v68\n" \
"v_mfma_f32_16x16x32_bf16 v[80:83], a[172:175], a[44:47], v[80:83]\n" \
"v_exp_f32_e32 v69, v69\n" \
"v_mfma_f32_16x16x32_bf16 v[84:87], a[188:191], a[44:47], v[40:43]\n" \
"v_exp_f32_e32 v70, v70\n" \
"v_mfma_f32_16x16x32_bf16 v[84:87], a[184:187], a[40:43], v[84:87]\n" \
"v_exp_f32_e32 v71, v71\n" \
"v_mfma_f32_16x16x32_bf16 v[84:87], a[180:183], a[36:39], v[84:87]\n" \
"v_exp_f32_e32 v72, v72\n" \
"v_mfma_f32_16x16x32_bf16 v[84:87], a[176:179], a[32:35], v[84:87]\n" \
"v_exp_f32_e32 v73, v73\n" \
"v_mfma_f32_16x16x32_bf16 v[88:91], a[160:163], a[48:51], v[44:47]\n" \
"v_exp_f32_e32 v74, v74\n" \
"v_mfma_f32_16x16x32_bf16 v[88:91], a[164:167], a[52:55], v[88:91]\n" \
"v_exp_f32_e32 v75, v75\n" \
"v_mfma_f32_16x16x32_bf16 v[88:91], a[168:171], a[56:59], v[88:91]\n" \
"v_exp_f32_e32 v76, v76\n" \
"v_mfma_f32_16x16x32_bf16 v[88:91], a[172:175], a[60:63], v[88:91]\n" \
"v_exp_f32_e32 v77, v77\n" \
"v_mfma_f32_16x16x32_bf16 v[92:95], a[188:191], a[60:63], v[44:47]\n" \
"v_exp_f32_e32 v78, v78\n" \
"v_mfma_f32_16x16x32_bf16 v[92:95], a[184:187], a[56:59], v[92:95]\n" \
"v_exp_f32_e32 v79, v79\n" \
"v_mfma_f32_16x16x32_bf16 v[92:95], a[180:183], a[52:55], v[92:95]\n" \
"v_exp_f32_e32 v80, v80\n" \
"v_mfma_f32_16x16x32_bf16 v[92:95], a[176:179], a[48:51], v[92:95]\n" \
"v_exp_f32_e32 v81, v81\n" \
"v_exp_f32_e32 v82, v82\n" \
"v_exp_f32_e32 v83, v83\n" \
"v_exp_f32_e32 v84, v84\n" \
"v_exp_f32_e32 v85, v85\n" \
"v_exp_f32_e32 v86, v86\n" \
"v_exp_f32_e32 v87, v87\n" \
"v_accvgpr_write_b32 a252, v57\n" \
"v_accvgpr_write_b32 a253, v57\n" \
"v_accvgpr_write_b32 a254, v57\n" \
"v_accvgpr_write_b32 a255, v57\n" \
"s_mov_b32 s64, 0x8000\n" \
"v_add_u32_e32 v50, 0x6000, v55\n" \
"v_add_u32_e32 v51, 0xa000, v55\n" \
"v_add_u32_e32 v53, 0x10000, v55\n" \
"v_add_u32_e32 v48, 0xc000, v20\n" \
"v_add_u32_e32 v49, s17, v20\n" \
"v_accvgpr_write_b32 a224, v116\n" \
"v_accvgpr_write_b32 a225, v117\n" \
"v_accvgpr_write_b32 a226, v118\n" \
"v_accvgpr_write_b32 a227, v119\n" \
"v_accvgpr_write_b32 a228, v120\n" \
"v_accvgpr_write_b32 a229, v121\n" \
"v_accvgpr_write_b32 a230, v122\n" \
"v_accvgpr_write_b32 a231, v123\n" \
"v_accvgpr_write_b32 a232, v124\n" \
"v_accvgpr_write_b32 a233, v125\n" \
"v_accvgpr_write_b32 a234, v126\n" \
"v_accvgpr_write_b32 a235, v127\n" \
"v_exp_f32_e32 v88, v88\n" \
"v_exp_f32_e32 v89, v89\n" \
"v_exp_f32_e32 v90, v90\n" \
"v_exp_f32_e32 v91, v91\n" \
"v_cvt_pk_bf16_f32 v96, v64, v65\n" \
"v_cvt_pk_bf16_f32 v97, v66, v67\n" \
"v_cvt_pk_bf16_f32 v98, v68, v69\n" \
"v_cvt_pk_bf16_f32 v99, v70, v71\n" \
"v_cvt_pk_bf16_f32 v104, v72, v73\n" \
"v_cvt_pk_bf16_f32 v105, v74, v75\n" \
"v_cvt_pk_bf16_f32 v106, v76, v77\n" \
"v_cvt_pk_bf16_f32 v107, v78, v79\n" \
"v_cvt_pk_bf16_f32 v112, v80, v81\n" \
"v_cvt_pk_bf16_f32 v113, v82, v83\n" \
"v_cvt_pk_bf16_f32 v114, v84, v85\n" \
"v_cvt_pk_bf16_f32 v115, v86, v87\n" \
"s_sub_u32 s66, s18, 1\n" \
"s_waitcnt vmcnt(2) lgkmcnt(0)\n" \
"s_barrier\n" \
"s_mov_b64 exec, 1\n" \
"buffer_store_dword v56, off, s[56:59], s68 offset:0 sc1\n" \
"buffer_store_dword v56, off, s[56:59], s22 sc1\n" \
"s_mov_b64 exec, -1\n" \
"s_cbranch_scc1 L_tr_%=\n" \
"s_branch L_1_%=\n" \
"L_t1_%=:\n" \
"v_mfma_f32_16x16x32_bf16 v[252:255], a[188:191], v[124:127], v[252:255]\n" \
"v_mfma_f32_16x16x32_bf16 v[252:255], a[172:175], v[120:123], v[252:255]\n" \
"v_smfmac_f32_16x16x64_bf16 a[204:207], a[212:215], v[120:127], v21\n" \
"s_waitcnt lgkmcnt(0)\n" \
"L_1_%=:\n" \
"v_mfma_f32_16x16x32_bf16 v[64:67], a[128:131], a[0:3], v[32:35]\n" \
"ds_read_b128 a[160:163], v49 offset:0\n" \
"ds_read_b128 a[176:179], v49 offset:4096\n" \
"v_mfma_f32_16x16x32_bf16 v[64:67], a[132:135], a[4:7], v[64:67]\n" \
"v_cvt_pk_bf16_f32 v120, v88, v89\n" \
"v_cvt_pk_bf16_f32 v121, v90, v91\n" \
"v_mfma_f32_16x16x32_bf16 v[64:67], a[136:139], a[8:11], v[64:67]\n" \
"ds_read_b128 a[180:183], v49 offset:5120\n" \
"ds_read_b128 a[164:167], v49 offset:1024\n" \
"v_mfma_f32_16x16x32_bf16 v[64:67], a[140:143], a[12:15], v[64:67]\n" \
"v_exp_f32_e32 v92, v92\n" \
"v_mfma_f32_16x16x32_bf16 v[68:71], a[156:159], a[12:15], v[32:35]\n" \
"ds_read_b128 a[168:171], v49 offset:2048\n" \
"ds_read_b128 a[184:187], v49 offset:6144\n" \
"v_mfma_f32_16x16x32_bf16 v[68:71], a[152:155], a[8:11], v[68:71]\n" \
"v_exp_f32_e32 v93, v93\n" \
"v_mfma_f32_16x16x32_bf16 v[68:71], a[148:151], a[4:7], v[68:71]\n" \
"ds_read_b128 a[188:191], v49 offset:7168\n" \
"ds_read_b128 a[172:175], v49 offset:3072\n" \
"v_mfma_f32_16x16x32_bf16 v[68:71], a[144:147], a[0:3], v[68:71]\n" \
"v_exp_f32_e32 v94, v94\n" \
"v_mfma_f32_16x16x32_bf16 v[72:75], a[128:131], a[16:19], v[36:39]\n" \
"v_exp_f32_e32 v95, v95\n" \
"v_mfma_f32_16x16x32_bf16 v[72:75], a[132:135], a[20:23], v[72:75]\n" \
"v_cvt_pk_bf16_f32 v122, v92, v93\n" \
"v_cvt_pk_bf16_f32 v123, v94, v95\n" \
"v_mfma_f32_16x16x32_bf16 v[72:75], a[136:139], a[24:27], v[72:75]\n" \
"v_exp_f32_e32 v64, v64\n" \
"v_mfma_f32_16x16x32_bf16 v[72:75], a[140:143], a[28:31], v[72:75]\n" \
"v_exp_f32_e32 v65, v65\n" \
"v_mfma_f32_16x16x32_bf16 v[76:79], a[156:159], a[28:31], v[36:39]\n" \
"v_exp_f32_e32 v66, v66\n" \
"v_mfma_f32_16x16x32_bf16 v[76:79], a[152:155], a[24:27], v[76:79]\n" \
"v_exp_f32_e32 v67, v67\n" \
"v_mfma_f32_16x16x32_bf16 v[76:79], a[148:151], a[20:23], v[76:79]\n" \
"v_exp_f32_e32 v68, v68\n" \
"v_mfma_f32_16x16x32_bf16 v[76:79], a[144:147], a[16:19], v[76:79]\n" \
"v_exp_f32_e32 v69, v69\n" \
"v_mfma_f32_16x16x32_bf16 v[80:83], a[128:131], a[32:35], v[40:43]\n" \
"v_exp_f32_e32 v70, v70\n" \
"v_mfma_f32_16x16x32_bf16 v[80:83], a[132:135], a[36:39], v[80:83]\n" \
"v_exp_f32_e32 v71, v71\n" \
"v_mfma_f32_16x16x32_bf16 v[80:83], a[136:139], a[40:43], v[80:83]\n" \
"v_cvt_pk_bf16_f32 v100, v64, v65\n" \
"v_cvt_pk_bf16_f32 v101, v66, v67\n" \
"v_mfma_f32_16x16x32_bf16 v[80:83], a[140:143], a[44:47], v[80:83]\n" \
"v_cvt_pk_bf16_f32 v102, v68, v69\n" \
"v_cvt_pk_bf16_f32 v103, v70, v71\n" \
"v_mfma_f32_16x16x32_bf16 v[84:87], a[156:159], a[44:47], v[40:43]\n" \
"v_exp_f32_e32 v72, v72\n" \
"v_mfma_f32_16x16x32_bf16 v[84:87], a[152:155], a[40:43], v[84:87]\n" \
"v_exp_f32_e32 v73, v73\n" \
"v_mfma_f32_16x16x32_bf16 v[84:87], a[148:151], a[36:39], v[84:87]\n" \
"v_exp_f32_e32 v74, v74\n" \
"v_mfma_f32_16x16x32_bf16 v[84:87], a[144:147], a[32:35], v[84:87]\n" \
"v_exp_f32_e32 v75, v75\n" \
"v_mfma_f32_16x16x32_bf16 v[88:91], a[128:131], a[48:51], v[44:47]\n" \
"v_exp_f32_e32 v76, v76\n" \
"v_mfma_f32_16x16x32_bf16 v[88:91], a[132:135], a[52:55], v[88:91]\n" \
"v_exp_f32_e32 v77, v77\n" \
"v_mfma_f32_16x16x32_bf16 v[88:91], a[136:139], a[56:59], v[88:91]\n" \
"v_exp_f32_e32 v78, v78\n" \
"v_mfma_f32_16x16x32_bf16 v[88:91], a[140:143], a[60:63], v[88:91]\n" \
"v_exp_f32_e32 v79, v79\n" \
"v_mfma_f32_16x16x32_bf16 v[92:95], a[156:159], a[60:63], v[44:47]\n" \
"v_cvt_pk_bf16_f32 v108, v72, v73\n" \
"v_cvt_pk_bf16_f32 v109, v74, v75\n" \
"v_mfma_f32_16x16x32_bf16 v[92:95], a[152:155], a[56:59], v[92:95]\n" \
"v_cvt_pk_bf16_f32 v110, v76, v77\n" \
"v_cvt_pk_bf16_f32 v111, v78, v79\n" \
"v_mfma_f32_16x16x32_bf16 v[92:95], a[148:151], a[52:55], v[92:95]\n" \
"ds_read_b128 a[128:131], v48 offset:0\n" \
"ds_read_b128 a[132:135], v48 offset:1024\n" \
"v_mfma_f32_16x16x32_bf16 v[92:95], a[144:147], a[48:51], v[92:95]\n" \
"s_add_u32 s63, s63, s63\n" \
"s_waitcnt lgkmcnt(2)\n" \
"v_mfma_f32_16x16x32_bf16 v[128:131], a[160:163], v[96:99], v[128:131]\n" \
"ds_read_b128 a[136:139], v48 offset:2048\n" \
"ds_read_b128 a[140:143], v48 offset:3072\n" \
"v_mfma_f32_16x16x32_bf16 v[128:131], a[176:179], v[100:103], v[128:131]\n" \
"v_exp_f32_e32 v80, v80\n" \
"v_mfma_f32_16x16x32_bf16 v[132:135], a[180:183], v[100:103], v[132:135]\n" \
"ds_read_b128 a[144:147], v48 offset:4096\n" \
"ds_read_b128 a[148:151], v48 offset:5120\n" \
"v_mfma_f32_16x16x32_bf16 v[132:135], a[164:167], v[96:99], v[132:135]\n" \
"v_exp_f32_e32 v81, v81\n" \
"v_mfma_f32_16x16x32_bf16 v[136:139], a[168:171], v[96:99], v[136:139]\n" \
"ds_read_b128 a[152:155], v48 offset:6144\n" \
"ds_read_b128 a[156:159], v48 offset:7168\n" \
"v_mfma_f32_16x16x32_bf16 v[136:139], a[184:187], v[100:103], v[136:139]\n" \
"v_exp_f32_e32 v82, v82\n" \
"v_mfma_f32_16x16x32_bf16 v[140:143], a[188:191], v[100:103], v[140:143]\n" \
"v_exp_f32_e32 v83, v83\n" \
"v_mfma_f32_16x16x32_bf16 v[140:143], a[172:175], v[96:99], v[140:143]\n" \
"v_exp_f32_e32 v84, v84\n" \
"v_smfmac_f32_16x16x64_bf16 a[192:195], a[212:215], v[96:103], v21\n" \
"v_mfma_f32_16x16x32_bf16 v[160:163], a[160:163], v[104:107], v[160:163]\n" \
"v_exp_f32_e32 v85, v85\n" \
"v_mfma_f32_16x16x32_bf16 v[160:163], a[176:179], v[108:111], v[160:163]\n" \
"v_exp_f32_e32 v86, v86\n" \
"v_mfma_f32_16x16x32_bf16 v[164:167], a[180:183], v[108:111], v[164:167]\n" \
"v_exp_f32_e32 v87, v87\n" \
"v_mfma_f32_16x16x32_bf16 v[164:167], a[164:167], v[104:107], v[164:167]\n" \
"v_cvt_pk_bf16_f32 v116, v80, v81\n" \
"v_cvt_pk_bf16_f32 v117, v82, v83\n" \
"v_mfma_f32_16x16x32_bf16 v[168:171], a[168:171], v[104:107], v[168:171]\n" \
"v_cvt_pk_bf16_f32 v118, v84, v85\n" \
"v_cvt_pk_bf16_f32 v119, v86, v87\n" \
"v_mfma_f32_16x16x32_bf16 v[168:171], a[184:187], v[108:111], v[168:171]\n" \
"v_exp_f32_e32 v88, v88\n" \
"v_mfma_f32_16x16x32_bf16 v[172:175], a[188:191], v[108:111], v[172:175]\n" \
"v_exp_f32_e32 v89, v89\n" \
"v_mfma_f32_16x16x32_bf16 v[172:175], a[172:175], v[104:107], v[172:175]\n" \
"v_exp_f32_e32 v90, v90\n" \
"v_smfmac_f32_16x16x64_bf16 a[196:199], a[212:215], v[104:111], v21\n" \
"v_mfma_f32_16x16x32_bf16 v[192:195], a[160:163], v[112:115], v[192:195]\n" \
"v_exp_f32_e32 v91, v91\n" \
"v_mfma_f32_16x16x32_bf16 v[192:195], a[176:179], v[116:119], v[192:195]\n" \
"v_exp_f32_e32 v92, v92\n" \
"v_mfma_f32_16x16x32_bf16 v[196:199], a[180:183], v[116:119], v[196:199]\n" \
"v_exp_f32_e32 v93, v93\n" \
"v_mfma_f32_16x16x32_bf16 v[196:199], a[164:167], v[112:115], v[196:199]\n" \
"v_exp_f32_e32 v94, v94\n" \
"v_mfma_f32_16x16x32_bf16 v[200:203], a[168:171], v[112:115], v[200:203]\n" \
"v_exp_f32_e32 v95, v95\n" \
"v_mfma_f32_16x16x32_bf16 v[200:203], a[184:187], v[116:119], v[200:203]\n" \
"v_cvt_pk_bf16_f32 v124, v88, v89\n" \
"v_cvt_pk_bf16_f32 v125, v90, v91\n" \
"v_mfma_f32_16x16x32_bf16 v[204:207], a[188:191], v[116:119], v[204:207]\n" \
"v_cvt_pk_bf16_f32 v126, v92, v93\n" \
"v_cvt_pk_bf16_f32 v127, v94, v95\n" \
"v_mfma_f32_16x16x32_bf16 v[204:207], a[172:175], v[112:115], v[204:207]\n" \
"v_smfmac_f32_16x16x64_bf16 a[200:203], a[212:215], v[112:119], v21\n" \
"s_waitcnt vmcnt(0) lgkmcnt(0)\n" \
"s_barrier\n" \
"v_mfma_f32_16x16x32_bf16 v[224:227], a[160:163], v[120:123], v[224:227]\n" \
"s_cbranch_scc1 L_h1_%=\n" \
"L_hr1_%=:\n" \
"v_mfma_f32_16x16x32_bf16 v[224:227], a[176:179], v[124:127], v[224:227]\n" \
"s_xor_b32 m0, s65, 0x12000\n" \
"ds_read_b128 a[160:163], v49 offset:8192\n" \
"v_mfma_f32_16x16x32_bf16 v[228:231], a[180:183], v[124:127], v[228:231]\n" \
"buffer_load_dwordx4 v50, s[8:11], s64 offen offset:0 sc1 lds\n" \
"v_mfma_f32_16x16x32_bf16 v[228:231], a[164:167], v[120:123], v[228:231]\n" \
"ds_read_b128 a[176:179], v49 offset:12288\n" \
"ds_read_b128 a[180:183], v49 offset:13312\n" \
"v_mfma_f32_16x16x32_bf16 v[232:235], a[168:171], v[120:123], v[232:235]\n" \
"v_mfma_f32_16x16x32_bf16 v[232:235], a[184:187], v[124:127], v[232:235]\n" \
"ds_read_b128 a[164:167], v49 offset:9216\n" \
"ds_read_b128 a[168:171], v49 offset:10240\n" \
"v_mfma_f32_16x16x32_bf16 v[236:239], a[188:191], v[124:127], v[236:239]\n" \
"v_mfma_f32_16x16x32_bf16 v[236:239], a[172:175], v[120:123], v[236:239]\n" \
"ds_read_b128 a[184:187], v49 offset:14336\n" \
"ds_read_b128 a[188:191], v49 offset:15360\n" \
"v_mfma_f32_16x16x32_bf16 v[64:67], a[128:131], a[0:3], v[32:35]\n" \
"v_mfma_f32_16x16x32_bf16 v[64:67], a[132:135], a[4:7], v[64:67]\n" \
"buffer_load_dwordx4 v50, s[8:11], s64 offen offset:1024 sc1 lds\n" \
"v_mfma_f32_16x16x32_bf16 v[64:67], a[136:139], a[8:11], v[64:67]\n" \
"s_xor_b32 m0, s65, 0x6000\n" \
"ds_read_b128 a[172:175], v49 offset:11264\n" \
"v_mfma_f32_16x16x32_bf16 v[64:67], a[140:143], a[12:15], v[64:67]\n" \
"v_add_u32_e32 v49, 0x10000, v48\n" \
"v_mfma_f32_16x16x32_bf16 v[68:71], a[156:159], a[12:15], v[32:35]\n" \
"v_mfma_f32_16x16x32_bf16 v[68:71], a[152:155], a[8:11], v[68:71]\n" \
"v_mfma_f32_16x16x32_bf16 v[68:71], a[148:151], a[4:7], v[68:71]\n" \
"v_mfma_f32_16x16x32_bf16 v[68:71], a[144:147], a[0:3], v[68:71]\n" \
"v_mfma_f32_16x16x32_bf16 v[72:75], a[128:131], a[16:19], v[36:39]\n" \
"v_mfma_f32_16x16x32_bf16 v[72:75], a[132:135], a[20:23], v[72:75]\n" \
"v_mfma_f32_16x16x32_bf16 v[72:75], a[136:139], a[24:27], v[72:75]\n" \
"buffer_load_dwordx4 v51, s[8:11], s64 offen offset:0 sc1 lds\n" \
"v_mfma_f32_16x16x32_bf16 v[72:75], a[140:143], a[28:31], v[72:75]\n" \
"v_exp_f32_e32 v64, v64\n" \
"v_mfma_f32_16x16x32_bf16 v[76:79], a[156:159], a[28:31], v[36:39]\n" \
"v_exp_f32_e32 v65, v65\n" \
"v_mfma_f32_16x16x32_bf16 v[76:79], a[152:155], a[24:27], v[76:79]\n" \
"v_exp_f32_e32 v66, v66\n" \
"v_mfma_f32_16x16x32_bf16 v[76:79], a[148:151], a[20:23], v[76:79]\n" \
"v_exp_f32_e32 v67, v67\n" \
"v_mfma_f32_16x16x32_bf16 v[76:79], a[144:147], a[16:19], v[76:79]\n" \
"v_exp_f32_e32 v68, v68\n" \
"v_mfma_f32_16x16x32_bf16 v[80:83], a[128:131], a[32:35], v[40:43]\n" \
"v_exp_f32_e32 v69, v69\n" \
"v_mfma_f32_16x16x32_bf16 v[80:83], a[132:135], a[36:39], v[80:83]\n" \
"v_exp_f32_e32 v70, v70\n" \
"v_mfma_f32_16x16x32_bf16 v[80:83], a[136:139], a[40:43], v[80:83]\n" \
"v_exp_f32_e32 v71, v71\n" \
"v_mfma_f32_16x16x32_bf16 v[80:83], a[140:143], a[44:47], v[80:83]\n" \
"buffer_load_dwordx4 v51, s[8:11], s64 offen offset:1024 sc1 lds\n" \
"v_mfma_f32_16x16x32_bf16 v[84:87], a[156:159], a[44:47], v[40:43]\n" \
"v_exp_f32_e32 v72, v72\n" \
"v_mfma_f32_16x16x32_bf16 v[84:87], a[152:155], a[40:43], v[84:87]\n" \
"v_exp_f32_e32 v73, v73\n" \
"v_mfma_f32_16x16x32_bf16 v[84:87], a[148:151], a[36:39], v[84:87]\n" \
"v_exp_f32_e32 v74, v74\n" \
"v_mfma_f32_16x16x32_bf16 v[84:87], a[144:147], a[32:35], v[84:87]\n" \
"v_exp_f32_e32 v75, v75\n" \
"v_mfma_f32_16x16x32_bf16 v[88:91], a[128:131], a[48:51], v[44:47]\n" \
"s_xor_b32 m0, s65, 0x14000\n" \
"ds_read_b128 a[128:131], v48 offset:8192\n" \
"v_mfma_f32_16x16x32_bf16 v[88:91], a[132:135], a[52:55], v[88:91]\n" \
"v_exp_f32_e32 v76, v76\n" \
"v_mfma_f32_16x16x32_bf16 v[88:91], a[136:139], a[56:59], v[88:91]\n" \
"ds_read_b128 a[132:135], v48 offset:9216\n" \
"ds_read_b128 a[136:139], v48 offset:10240\n" \
"v_mfma_f32_16x16x32_bf16 v[88:91], a[140:143], a[60:63], v[88:91]\n" \
"v_exp_f32_e32 v77, v77\n" \
"v_mfma_f32_16x16x32_bf16 v[92:95], a[156:159], a[60:63], v[44:47]\n" \
"buffer_load_dwordx4 v52, s[8:11], s64 offen offset:0 sc1 lds\n" \
"v_mfma_f32_16x16x32_bf16 v[92:95], a[152:155], a[56:59], v[92:95]\n" \
"ds_read_b128 a[140:143], v48 offset:11264\n" \
"ds_read_b128 a[152:155], v48 offset:14336\n" \
"v_mfma_f32_16x16x32_bf16 v[92:95], a[148:151], a[52:55], v[92:95]\n" \
"v_exp_f32_e32 v78, v78\n" \
"v_mfma_f32_16x16x32_bf16 v[92:95], a[144:147], a[48:51], v[92:95]\n" \
"ds_read_b128 a[144:147], v48 offset:12288\n" \
"s_waitcnt lgkmcnt(6)\n" \
"v_mfma_f32_16x16x32_bf16 v[144:147], a[160:163], v[96:99], v[144:147]\n" \
"ds_read_b128 a[148:151], v48 offset:13312\n" \
"ds_read_b128 a[156:159], v48 offset:15360\n" \
"v_mfma_f32_16x16x32_bf16 v[144:147], a[176:179], v[100:103], v[144:147]\n" \
"v_exp_f32_e32 v79, v79\n" \
"v_mfma_f32_16x16x32_bf16 v[148:151], a[180:183], v[100:103], v[148:151]\n" \
"v_exp_f32_e32 v80, v80\n" \
"v_mfma_f32_16x16x32_bf16 v[148:151], a[164:167], v[96:99], v[148:151]\n" \
"v_exp_f32_e32 v81, v81\n" \
"v_mfma_f32_16x16x32_bf16 v[152:155], a[168:171], v[96:99], v[152:155]\n" \
"buffer_load_dwordx4 v52, s[8:11], s64 offen offset:1024 sc1 lds\n" \
"v_mfma_f32_16x16x32_bf16 v[152:155], a[184:187], v[100:103], v[152:155]\n" \
"s_mov_b32 m0, s65\n" \
"s_xor_b32 s65, s65, 0x4000\n" \
"v_mfma_f32_16x16x32_bf16 v[156:159], a[188:191], v[100:103], v[156:159]\n" \
"v_exp_f32_e32 v82, v82\n" \
"v_mfma_f32_16x16x32_bf16 v[156:159], a[172:175], v[96:99], v[156:159]\n" \
"v_exp_f32_e32 v83, v83\n" \
"v_mfma_f32_16x16x32_bf16 v[176:179], a[160:163], v[104:107], v[176:179]\n" \
"v_exp_f32_e32 v84, v84\n" \
"v_mfma_f32_16x16x32_bf16 v[176:179], a[176:179], v[108:111], v[176:179]\n" \
"v_exp_f32_e32 v85, v85\n" \
"v_mfma_f32_16x16x32_bf16 v[180:183], a[180:183], v[108:111], v[180:183]\n" \
"v_exp_f32_e32 v86, v86\n" \
"v_mfma_f32_16x16x32_bf16 v[180:183], a[164:167], v[104:107], v[180:183]\n" \
"v_exp_f32_e32 v87, v87\n" \
"v_mfma_f32_16x16x32_bf16 v[184:187], a[168:171], v[104:107], v[184:187]\n" \
"v_exp_f32_e32 v88, v88\n" \
"v_mfma_f32_16x16x32_bf16 v[184:187], a[184:187], v[108:111], v[184:187]\n" \
"buffer_load_dwordx4 v53, s[8:11], s64 offen offset:0 sc1 lds\n" \
"v_mfma_f32_16x16x32_bf16 v[188:191], a[188:191], v[108:111], v[188:191]\n" \
"v_exp_f32_e32 v89, v89\n" \
"v_mfma_f32_16x16x32_bf16 v[188:191], a[172:175], v[104:107], v[188:191]\n" \
"v_exp_f32_e32 v90, v90\n" \
"v_mfma_f32_16x16x32_bf16 v[208:211], a[160:163], v[112:115], v[208:211]\n" \
"v_exp_f32_e32 v91, v91\n" \
"v_mfma_f32_16x16x32_bf16 v[208:211], a[176:179], v[116:119], v[208:211]\n" \
"v_cvt_pk_bf16_f32 v96, v64, v65\n" \
"v_cvt_pk_bf16_f32 v97, v66, v67\n" \
"v_mfma_f32_16x16x32_bf16 v[212:215], a[180:183], v[116:119], v[212:215]\n" \
"v_cvt_pk_bf16_f32 v98, v68, v69\n" \
"v_cvt_pk_bf16_f32 v99, v70, v71\n" \
"v_mfma_f32_16x16x32_bf16 v[212:215], a[164:167], v[112:115], v[212:215]\n" \
"v_cvt_pk_bf16_f32 v104, v72, v73\n" \
"v_cvt_pk_bf16_f32 v105, v74, v75\n" \
"v_mfma_f32_16x16x32_bf16 v[216:219], a[168:171], v[112:115], v[216:219]\n" \
"v_cvt_pk_bf16_f32 v106, v76, v77\n" \
"v_cvt_pk_bf16_f32 v107, v78, v79\n" \
"v_mfma_f32_16x16x32_bf16 v[216:219], a[184:187], v[116:119], v[216:219]\n" \
"v_xor_b32_e32 v48, 0x4000, v48\n" \
"v_mfma_f32_16x16x32_bf16 v[220:223], a[188:191], v[116:119], v[220:223]\n" \
"buffer_load_dwordx4 v53, s[8:11], s64 offen offset:1024 sc1 lds\n" \
"v_mfma_f32_16x16x32_bf16 v[220:223], a[172:175], v[112:115], v[220:223]\n" \
"s_add_u32 s64, s64, 0x8000\n" \
"s_sub_u32 s66, s66, 1\n" \
"v_mfma_f32_16x16x32_bf16 v[240:243], a[160:163], v[120:123], v[240:243]\n" \
"v_cvt_pk_bf16_f32 v112, v80, v81\n" \
"v_cvt_pk_bf16_f32 v113, v82, v83\n" \
"v_mfma_f32_16x16x32_bf16 v[240:243], a[176:179], v[124:127], v[240:243]\n" \
"v_cvt_pk_bf16_f32 v114, v84, v85\n" \
"v_cvt_pk_bf16_f32 v115, v86, v87\n" \
"v_mfma_f32_16x16x32_bf16 v[244:247], a[180:183], v[124:127], v[244:247]\n" \
"v_mfma_f32_16x16x32_bf16 v[244:247], a[164:167], v[120:123], v[244:247]\n" \
"v_mfma_f32_16x16x32_bf16 v[248:251], a[168:171], v[120:123], v[248:251]\n" \
"v_mfma_f32_16x16x32_bf16 v[248:251], a[184:187], v[124:127], v[248:251]\n" \
"s_cbranch_scc0 L_t1_%=\n" \
"v_mfma_f32_16x16x32_bf16 v[252:255], a[188:191], v[124:127], v[252:255]\n" \
"v_mfma_f32_16x16x32_bf16 v[252:255], a[172:175], v[120:123], v[252:255]\n" \
"v_smfmac_f32_16x16x64_bf16 a[204:207], a[212:215], v[120:127], v21\n" \
"s_waitcnt lgkmcnt(0)\n" \
"s_barrier\n" \
"L_tr_%=:\n" \
"L_pf11:\n" \
"s_cbranch_execz L_pf15\n" \
"s_nop 0\n" \
"s_cmp_lg_u32 s16, 0\n" \
"s_cbranch_scc1 L_sw_%=\n" \
"v_mfma_f32_16x16x32_bf16 v[64:67], a[128:131], a[0:3], v[32:35]\n" \
"s_xor_b32 m0, s65, 0x12000\n" \
"ds_read_b128 a[160:163], v49 offset:0\n" \
"v_mfma_f32_16x16x32_bf16 v[64:67], a[132:135], a[4:7], v[64:67]\n" \
"buffer_load_dwordx4 v50, s[8:11], s64 offen offset:0 sc1 lds\n" \
"v_mfma_f32_16x16x32_bf16 v[64:67], a[136:139], a[8:11], v[64:67]\n" \
"ds_read_b128 a[176:179], v49 offset:4096\n" \
"ds_read_b128 a[180:183], v49 offset:5120\n" \
"v_mfma_f32_16x16x32_bf16 v[64:67], a[140:143], a[12:15], v[64:67]\n" \
"s_mov_b32 s65, s13\n" \
"v_add_u32_e32 v63, s15, v20\n" \
"v_mfma_f32_16x16x32_bf16 v[68:71], a[156:159], a[12:15], v[32:35]\n" \
"s_add_u32 s67, s14, 40960\n" \
"ds_read_b128 a[164:167], v49 offset:1024\n" \
"v_mfma_f32_16x16x32_bf16 v[68:71], a[152:155], a[8:11], v[68:71]\n" \
"ds_read_b128 a[168:171], v49 offset:2048\n" \
"ds_read_b128 a[184:187], v49 offset:6144\n" \
"v_mfma_f32_16x16x32_bf16 v[68:71], a[148:151], a[4:7], v[68:71]\n" \
"buffer_load_dwordx4 v50, s[8:11], s64 offen offset:1024 sc1 lds\n" \
"v_mfma_f32_16x16x32_bf16 v[68:71], a[144:147], a[0:3], v[68:71]\n" \
"v_add_u32_e32 v50, 0x6000, v63\n" \
"s_xor_b32 m0, s65, 0x8000\n" \
"v_mfma_f32_16x16x32_bf16 v[72:75], a[128:131], a[16:19], v[36:39]\n" \
"v_cvt_pk_bf16_f32 v120, v88, v89\n" \
"v_cvt_pk_bf16_f32 v121, v90, v91\n" \
"v_mfma_f32_16x16x32_bf16 v[72:75], a[132:135], a[20:23], v[72:75]\n" \
"ds_read_b128 a[188:191], v49 offset:7168\n" \
"ds_read_b128 a[172:175], v49 offset:3072\n" \
"v_mfma_f32_16x16x32_bf16 v[72:75], a[136:139], a[24:27], v[72:75]\n" \
"v_exp_f32_e32 v92, v92\n" \
"v_mfma_f32_16x16x32_bf16 v[72:75], a[140:143], a[28:31], v[72:75]\n" \
"v_exp_f32_e32 v93, v93\n" \
"v_mfma_f32_16x16x32_bf16 v[76:79], a[156:159], a[28:31], v[36:39]\n" \
"buffer_load_dwordx4 v50, s[8:11], s67 offen offset:0 sc1 lds\n" \
"v_mfma_f32_16x16x32_bf16 v[76:79], a[152:155], a[24:27], v[76:79]\n" \
"v_exp_f32_e32 v94, v94\n" \
"v_mfma_f32_16x16x32_bf16 v[76:79], a[148:151], a[20:23], v[76:79]\n" \
"v_exp_f32_e32 v95, v95\n" \
"v_mfma_f32_16x16x32_bf16 v[76:79], a[144:147], a[16:19], v[76:79]\n" \
"v_cvt_pk_bf16_f32 v122, v92, v93\n" \
"v_cvt_pk_bf16_f32 v123, v94, v95\n" \
"v_mfma_f32_16x16x32_bf16 v[80:83], a[128:131], a[32:35], v[40:43]\n" \
"v_exp_f32_e32 v64, v64\n" \
"v_mfma_f32_16x16x32_bf16 v[80:83], a[132:135], a[36:39], v[80:83]\n" \
"v_exp_f32_e32 v65, v65\n" \
"v_mfma_f32_16x16x32_bf16 v[80:83], a[136:139], a[40:43], v[80:83]\n" \
"buffer_load_dwordx4 v50, s[8:11], s67 offen offset:1024 sc1 lds\n" \
"v_mfma_f32_16x16x32_bf16 v[80:83], a[140:143], a[44:47], v[80:83]\n" \
"v_exp_f32_e32 v66, v66\n" \
"v_mfma_f32_16x16x32_bf16 v[84:87], a[156:159], a[44:47], v[40:43]\n" \
"v_exp_f32_e32 v67, v67\n" \
"v_mfma_f32_16x16x32_bf16 v[84:87], a[152:155], a[40:43], v[84:87]\n" \
"v_exp_f32_e32 v68, v68\n" \
"v_mfma_f32_16x16x32_bf16 v[84:87], a[148:151], a[36:39], v[84:87]\n" \
"v_exp_f32_e32 v69, v69\n" \
"v_mfma_f32_16x16x32_bf16 v[84:87], a[144:147], a[32:35], v[84:87]\n" \
"v_exp_f32_e32 v70, v70\n" \
"v_mfma_f32_16x16x32_bf16 v[88:91], a[128:131], a[48:51], v[44:47]\n" \
"v_exp_f32_e32 v71, v71\n" \
"v_mfma_f32_16x16x32_bf16 v[88:91], a[132:135], a[52:55], v[88:91]\n" \
"v_cvt_pk_bf16_f32 v100, v64, v65\n" \
"v_cvt_pk_bf16_f32 v101, v66, v67\n" \
"v_mfma_f32_16x16x32_bf16 v[88:91], a[136:139], a[56:59], v[88:91]\n" \
"v_cvt_pk_bf16_f32 v102, v68, v69\n" \
"v_cvt_pk_bf16_f32 v103, v70, v71\n" \
"v_mfma_f32_16x16x32_bf16 v[88:91], a[140:143], a[60:63], v[88:91]\n" \
"buffer_load_dwordx4 v50, s[8:11], s67 offen offset:2048 sc1 lds\n" \
"v_mfma_f32_16x16x32_bf16 v[92:95], a[156:159], a[60:63], v[44:47]\n" \
"v_exp_f32_e32 v72, v72\n" \
"v_mfma_f32_16x16x32_bf16 v[92:95], a[152:155], a[56:59], v[92:95]\n" \
"v_exp_f32_e32 v73, v73\n" \
"v_mfma_f32_16x16x32_bf16 v[92:95], a[148:151], a[52:55], v[92:95]\n" \
"v_exp_f32_e32 v74, v74\n" \
"v_mfma_f32_16x16x32_bf16 v[92:95], a[144:147], a[48:51], v[92:95]\n" \
"v_cvt_pk_bf16_f32 v108, v72, v73\n" \
"s_waitcnt lgkmcnt(0)\n" \
"v_mfma_f32_16x16x32_bf16 v[128:131], a[160:163], v[96:99], v[128:131]\n" \
"buffer_load_dwordx4 v50, s[8:11], s67 offen offset:3072 sc1 lds\n" \
"v_mfma_f32_16x16x32_bf16 v[128:131], a[176:179], v[100:103], v[128:131]\n" \
"v_exp_f32_e32 v75, v75\n" \
"v_mfma_f32_16x16x32_bf16 v[132:135], a[180:183], v[100:103], v[132:135]\n" \
"v_exp_f32_e32 v76, v76\n" \
"v_mfma_f32_16x16x32_bf16 v[132:135], a[164:167], v[96:99], v[132:135]\n" \
"v_exp_f32_e32 v77, v77\n" \
"v_mfma_f32_16x16x32_bf16 v[136:139], a[168:171], v[96:99], v[136:139]\n" \
"v_exp_f32_e32 v78, v78\n" \
"v_mfma_f32_16x16x32_bf16 v[136:139], a[184:187], v[100:103], v[136:139]\n" \
"v_exp_f32_e32 v79, v79\n" \
"v_mfma_f32_16x16x32_bf16 v[140:143], a[188:191], v[100:103], v[140:143]\n" \
"v_cvt_pk_bf16_f32 v109, v74, v75\n" \
"v_cvt_pk_bf16_f32 v110, v76, v77\n" \
"v_mfma_f32_16x16x32_bf16 v[140:143], a[172:175], v[96:99], v[140:143]\n" \
"v_cvt_pk_bf16_f32 v111, v78, v79\n" \
"s_mov_b32 s64, s14\n" \
"v_smfmac_f32_16x16x64_bf16 a[192:195], a[212:215], v[96:103], v21\n" \
"v_mfma_f32_16x16x32_bf16 v[160:163], a[160:163], v[104:107], v[160:163]\n" \
"v_add_u32_e32 v51, 0x12000, v63\n" \
"s_xor_b32 m0, s65, 0xa000\n" \
"v_mfma_f32_16x16x32_bf16 v[160:163], a[176:179], v[108:111], v[160:163]\n" \
"buffer_load_dwordx4 v51, s[8:11], s64 offen offset:0 sc1 lds\n" \
"v_mfma_f32_16x16x32_bf16 v[164:167], a[180:183], v[108:111], v[164:167]\n" \
"v_exp_f32_e32 v80, v80\n" \
"v_mfma_f32_16x16x32_bf16 v[164:167], a[164:167], v[104:107], v[164:167]\n" \
"v_exp_f32_e32 v81, v81\n" \
"v_mfma_f32_16x16x32_bf16 v[168:171], a[168:171], v[104:107], v[168:171]\n" \
"v_exp_f32_e32 v82, v82\n" \
"v_mfma_f32_16x16x32_bf16 v[168:171], a[184:187], v[108:111], v[168:171]\n" \
"v_exp_f32_e32 v83, v83\n" \
"v_mfma_f32_16x16x32_bf16 v[172:175], a[188:191], v[108:111], v[172:175]\n" \
"v_exp_f32_e32 v84, v84\n" \
"v_mfma_f32_16x16x32_bf16 v[172:175], a[172:175], v[104:107], v[172:175]\n" \
"v_exp_f32_e32 v85, v85\n" \
"v_smfmac_f32_16x16x64_bf16 a[196:199], a[212:215], v[104:111], v21\n" \
"v_mfma_f32_16x16x32_bf16 v[192:195], a[160:163], v[112:115], v[192:195]\n" \
"v_exp_f32_e32 v86, v86\n" \
"v_exp_f32_e32 v87, v87\n" \
"v_cvt_pk_bf16_f32 v116, v80, v81\n" \
"v_cvt_pk_bf16_f32 v117, v82, v83\n" \
"v_cvt_pk_bf16_f32 v118, v84, v85\n" \
"v_cvt_pk_bf16_f32 v119, v86, v87\n" \
"v_exp_f32_e32 v88, v88\n" \
"v_exp_f32_e32 v89, v89\n" \
"v_mfma_f32_16x16x32_bf16 v[192:195], a[176:179], v[116:119], v[192:195]\n" \
"buffer_load_dwordx4 v51, s[8:11], s64 offen offset:1024 sc1 lds\n" \
"v_mfma_f32_16x16x32_bf16 v[196:199], a[180:183], v[116:119], v[196:199]\n" \
"ds_read_b128 a[128:131], v48 offset:0\n" \
"ds_read_b128 a[132:135], v48 offset:1024\n" \
"v_mfma_f32_16x16x32_bf16 v[196:199], a[164:167], v[112:115], v[196:199]\n" \
"ds_read_b128 a[136:139], v48 offset:2048\n" \
"ds_read_b128 a[140:143], v48 offset:3072\n" \
"ds_read_b128 a[144:147], v48 offset:4096\n" \
"ds_read_b128 a[148:151], v48 offset:5120\n" \
"ds_read_b128 a[152:155], v48 offset:6144\n" \
"ds_read_b128 a[156:159], v48 offset:7168\n" \
"v_mfma_f32_16x16x32_bf16 v[200:203], a[168:171], v[112:115], v[200:203]\n" \
"v_exp_f32_e32 v90, v90\n" \
"v_mfma_f32_16x16x32_bf16 v[200:203], a[184:187], v[116:119], v[200:203]\n" \
"v_exp_f32_e32 v91, v91\n" \
"v_mfma_f32_16x16x32_bf16 v[204:207], a[188:191], v[116:119], v[204:207]\n" \
"v_exp_f32_e32 v92, v92\n" \
"v_mfma_f32_16x16x32_bf16 v[204:207], a[172:175], v[112:115], v[204:207]\n" \
"v_exp_f32_e32 v93, v93\n" \
"v_smfmac_f32_16x16x64_bf16 a[200:203], a[212:215], v[112:119], v21\n" \
"v_mfma_f32_16x16x32_bf16 v[224:227], a[160:163], v[120:123], v[224:227]\n" \
"v_exp_f32_e32 v94, v94\n" \
"v_exp_f32_e32 v95, v95\n" \
"v_cvt_pk_bf16_f32 v124, v88, v89\n" \
"v_cvt_pk_bf16_f32 v125, v90, v91\n" \
"v_cvt_pk_bf16_f32 v126, v92, v93\n" \
"v_cvt_pk_bf16_f32 v127, v94, v95\n" \
"v_add_u32_e32 v52, 0x14000, v63\n" \
"v_add_u32_e32 v53, 0x20000, v63\n" \
"v_mfma_f32_16x16x32_bf16 v[224:227], a[176:179], v[124:127], v[224:227]\n" \
"buffer_load_dwordx4 v51, s[8:11], s64 offen offset:2048 sc1 lds\n" \
"v_mfma_f32_16x16x32_bf16 v[228:231], a[180:183], v[124:127], v[228:231]\n" \
"buffer_load_dwordx4 v51, s[8:11], s64 offen offset:3072 sc1 lds\n" \
"v_mfma_f32_16x16x32_bf16 v[228:231], a[164:167], v[120:123], v[228:231]\n" \
"v_readlane_b32 s93, v11, 17\n" \
"v_readlane_b32 s92, v11, 22\n" \
"v_mfma_f32_16x16x32_bf16 v[232:235], a[168:171], v[120:123], v[232:235]\n" \
"s_mov_b32 s66, s19\n" \
"v_mfma_f32_16x16x32_bf16 v[232:235], a[184:187], v[124:127], v[232:235]\n" \
"v_mfma_f32_16x16x32_bf16 v[236:239], a[188:191], v[124:127], v[236:239]\n" \
"v_mfma_f32_16x16x32_bf16 v[236:239], a[172:175], v[120:123], v[236:239]\n" \
"s_waitcnt vmcnt(10) lgkmcnt(0)\n" \
"s_barrier\n" \
"v_mfma_f32_16x16x32_bf16 v[64:67], a[128:131], a[0:3], v[32:35]\n" \
"s_xor_b32 m0, s65, 0x18000\n" \
"ds_read_b128 a[160:163], v49 offset:8192\n" \
"v_mfma_f32_16x16x32_bf16 v[64:67], a[132:135], a[4:7], v[64:67]\n" \
"buffer_load_dwordx4 v52, s[8:11], s64 offen offset:0 sc1 lds\n" \
"v_mfma_f32_16x16x32_bf16 v[64:67], a[136:139], a[8:11], v[64:67]\n" \
"ds_read_b128 a[176:179], v49 offset:12288\n" \
"ds_read_b128 a[180:183], v49 offset:13312\n" \
"v_mfma_f32_16x16x32_bf16 v[64:67], a[140:143], a[12:15], v[64:67]\n" \
"buffer_load_dwordx4 v52, s[8:11], s64 offen offset:1024 sc1 lds\n" \
"v_mfma_f32_16x16x32_bf16 v[68:71], a[156:159], a[12:15], v[32:35]\n" \
"ds_read_b128 a[164:167], v49 offset:9216\n" \
"ds_read_b128 a[168:171], v49 offset:10240\n" \
"v_mfma_f32_16x16x32_bf16 v[68:71], a[152:155], a[8:11], v[68:71]\n" \
"ds_read_b128 a[184:187], v49 offset:14336\n" \
"ds_read_b128 a[188:191], v49 offset:15360\n" \
"v_mfma_f32_16x16x32_bf16 v[68:71], a[148:151], a[4:7], v[68:71]\n" \
"v_exp_f32_e32 v64, v64\n" \
"v_mfma_f32_16x16x32_bf16 v[68:71], a[144:147], a[0:3], v[68:71]\n" \
"buffer_load_dwordx4 v52, s[8:11], s64 offen offset:2048 sc1 lds\n" \
"v_mfma_f32_16x16x32_bf16 v[72:75], a[128:131], a[16:19], v[36:39]\n" \
"v_exp_f32_e32 v65, v65\n" \
"v_mfma_f32_16x16x32_bf16 v[72:75], a[132:135], a[20:23], v[72:75]\n" \
"v_exp_f32_e32 v66, v66\n" \
"L_pf12:\n" \
"s_cbranch_execz L_pf16\n" \
"s_nop 0\n" \
"v_mfma_f32_16x16x32_bf16 v[72:75], a[136:139], a[24:27], v[72:75]\n" \
"buffer_load_dwordx4 v52, s[8:11], s64 offen offset:3072 sc1 lds\n" \
"v_mfma_f32_16x16x32_bf16 v[72:75], a[140:143], a[28:31], v[72:75]\n" \
"s_mov_b32 m0, s65\n" \
"ds_read_b128 a[172:175], v49 offset:11264\n" \
"v_mfma_f32_16x16x32_bf16 v[76:79], a[156:159], a[28:31], v[36:39]\n" \
"v_exp_f32_e32 v67, v67\n" \
"v_mfma_f32_16x16x32_bf16 v[76:79], a[152:155], a[24:27], v[76:79]\n" \
"v_exp_f32_e32 v68, v68\n" \
"v_mfma_f32_16x16x32_bf16 v[76:79], a[148:151], a[20:23], v[76:79]\n" \
"buffer_load_dwordx4 v53, s[8:11], s64 offen offset:0 sc1 lds\n" \
"v_mfma_f32_16x16x32_bf16 v[76:79], a[144:147], a[16:19], v[76:79]\n" \
"v_exp_f32_e32 v69, v69\n" \
"v_mfma_f32_16x16x32_bf16 v[80:83], a[128:131], a[32:35], v[40:43]\n" \
"v_exp_f32_e32 v70, v70\n" \
"v_mfma_f32_16x16x32_bf16 v[80:83], a[132:135], a[36:39], v[80:83]\n" \
"buffer_load_dwordx4 v53, s[8:11], s64 offen offset:1024 sc1 lds\n" \
"v_mfma_f32_16x16x32_bf16 v[80:83], a[136:139], a[40:43], v[80:83]\n" \
"v_exp_f32_e32 v71, v71\n" \
"v_mfma_f32_16x16x32_bf16 v[80:83], a[140:143], a[44:47], v[80:83]\n" \
"v_exp_f32_e32 v72, v72\n" \
"v_mfma_f32_16x16x32_bf16 v[84:87], a[156:159], a[44:47], v[40:43]\n" \
"v_exp_f32_e32 v73, v73\n" \
"v_mfma_f32_16x16x32_bf16 v[84:87], a[152:155], a[40:43], v[84:87]\n" \
"buffer_load_dwordx4 v53, s[8:11], s64 offen offset:2048 sc1 lds\n" \
"v_mfma_f32_16x16x32_bf16 v[84:87], a[148:151], a[36:39], v[84:87]\n" \
"v_exp_f32_e32 v74, v74\n" \
"v_mfma_f32_16x16x32_bf16 v[84:87], a[144:147], a[32:35], v[84:87]\n" \
"v_exp_f32_e32 v75, v75\n" \
"v_mfma_f32_16x16x32_bf16 v[88:91], a[128:131], a[48:51], v[44:47]\n" \
"buffer_load_dwordx4 v53, s[8:11], s64 offen offset:3072 sc1 lds\n" \
"v_mfma_f32_16x16x32_bf16 v[88:91], a[132:135], a[52:55], v[88:91]\n" \
"s_add_u32 m0, s93, 0x0\n" \
"ds_read_b128 a[128:131], v48 offset:8192\n" \
"v_mfma_f32_16x16x32_bf16 v[88:91], a[136:139], a[56:59], v[88:91]\n" \
"ds_read_b128 a[132:135], v48 offset:9216\n" \
"ds_read_b128 a[136:139], v48 offset:10240\n" \
"v_mfma_f32_16x16x32_bf16 v[88:91], a[140:143], a[60:63], v[88:91]\n" \
"ds_read_b128 a[140:143], v48 offset:11264\n" \
"v_add_u32_e32 v49, 0x10000, v48\n" \
"v_mfma_f32_16x16x32_bf16 v[92:95], a[156:159], a[60:63], v[44:47]\n" \
"buffer_load_dwordx4 v0, s[80:83], s92 offen offset:0 lds\n" \
"v_mfma_f32_16x16x32_bf16 v[92:95], a[152:155], a[56:59], v[92:95]\n" \
"ds_read_b128 a[152:155], v48 offset:14336\n" \
"ds_read_b128 a[156:159], v48 offset:15360\n" \
"v_mfma_f32_16x16x32_bf16 v[92:95], a[148:151], a[52:55], v[92:95]\n" \
"ds_read_b128 a[148:151], v48 offset:13312\n" \
"s_xor_b32 s65, s65, 0x8000\n" \
"v_mfma_f32_16x16x32_bf16 v[92:95], a[144:147], a[48:51], v[92:95]\n" \
"ds_read_b128 a[144:147], v48 offset:12288\n" \
"s_waitcnt lgkmcnt(8)\n" \
"v_mfma_f32_16x16x32_bf16 v[144:147], a[160:163], v[96:99], v[144:147]\n" \
"buffer_load_dwordx4 v1, s[80:83], s92 offen offset:1024 lds\n" \
"v_mfma_f32_16x16x32_bf16 v[144:147], a[176:179], v[100:103], v[144:147]\n" \
"v_exp_f32_e32 v76, v76\n" \
"v_mfma_f32_16x16x32_bf16 v[148:151], a[180:183], v[100:103], v[148:151]\n" \
"v_exp_f32_e32 v77, v77\n" \
"v_mfma_f32_16x16x32_bf16 v[148:151], a[164:167], v[96:99], v[148:151]\n" \
"buffer_load_dwordx4 v2, s[80:83], s92 offen offset:2048 lds\n" \
"v_mfma_f32_16x16x32_bf16 v[152:155], a[168:171], v[96:99], v[152:155]\n" \
"v_exp_f32_e32 v78, v78\n" \
"v_mfma_f32_16x16x32_bf16 v[152:155], a[184:187], v[100:103], v[152:155]\n" \
"v_exp_f32_e32 v79, v79\n" \
"v_mfma_f32_16x16x32_bf16 v[156:159], a[188:191], v[100:103], v[156:159]\n" \
"buffer_load_dwordx4 v3, s[80:83], s92 offen offset:3072 lds\n" \
"v_mfma_f32_16x16x32_bf16 v[156:159], a[172:175], v[96:99], v[156:159]\n" \
"s_add_u32 m0, s93, 0x1000\n" \
"v_cvt_pk_bf16_f32 v96, v64, v65\n" \
"v_mfma_f32_16x16x32_bf16 v[176:179], a[160:163], v[104:107], v[176:179]\n" \
"v_exp_f32_e32 v80, v80\n" \
"v_mfma_f32_16x16x32_bf16 v[176:179], a[176:179], v[108:111], v[176:179]\n" \
"v_exp_f32_e32 v81, v81\n" \
"v_mfma_f32_16x16x32_bf16 v[180:183], a[180:183], v[108:111], v[180:183]\n" \
"buffer_load_dwordx4 v4, s[80:83], s92 offen offset:0 lds\n" \
"v_mfma_f32_16x16x32_bf16 v[180:183], a[164:167], v[104:107], v[180:183]\n" \
"v_exp_f32_e32 v82, v82\n" \
"v_mfma_f32_16x16x32_bf16 v[184:187], a[168:171], v[104:107], v[184:187]\n" \
"v_exp_f32_e32 v83, v83\n" \
"v_mfma_f32_16x16x32_bf16 v[184:187], a[184:187], v[108:111], v[184:187]\n" \
"buffer_load_dwordx4 v5, s[80:83], s92 offen offset:1024 lds\n" \
"v_mfma_f32_16x16x32_bf16 v[188:191], a[188:191], v[108:111], v[188:191]\n" \
"v_exp_f32_e32 v84, v84\n" \
"v_mfma_f32_16x16x32_bf16 v[188:191], a[172:175], v[104:107], v[188:191]\n" \
"v_exp_f32_e32 v85, v85\n" \
"v_mfma_f32_16x16x32_bf16 v[208:211], a[160:163], v[112:115], v[208:211]\n" \
"v_exp_f32_e32 v86, v86\n" \
"v_mfma_f32_16x16x32_bf16 v[208:211], a[176:179], v[116:119], v[208:211]\n" \
"buffer_load_dwordx4 v6, s[80:83], s92 offen offset:2048 lds\n" \
"v_mfma_f32_16x16x32_bf16 v[212:215], a[180:183], v[116:119], v[212:215]\n" \
"v_exp_f32_e32 v87, v87\n" \
"v_mfma_f32_16x16x32_bf16 v[212:215], a[164:167], v[112:115], v[212:215]\n" \
"v_exp_f32_e32 v88, v88\n" \
"v_mfma_f32_16x16x32_bf16 v[216:219], a[168:171], v[112:115], v[216:219]\n" \
"buffer_load_dwordx4 v7, s[80:83], s92 offen offset:3072 lds\n" \
"v_mfma_f32_16x16x32_bf16 v[216:219], a[184:187], v[116:119], v[216:219]\n" \
"s_add_u32 m0, s93, 0x2000\n" \
"v_cvt_pk_bf16_f32 v97, v66, v67\n" \
"v_mfma_f32_16x16x32_bf16 v[220:223], a[188:191], v[116:119], v[220:223]\n" \
"v_exp_f32_e32 v89, v89\n" \
"v_mfma_f32_16x16x32_bf16 v[220:223], a[172:175], v[112:115], v[220:223]\n" \
"v_exp_f32_e32 v90, v90\n" \
"v_mfma_f32_16x16x32_bf16 v[240:243], a[160:163], v[120:123], v[240:243]\n" \
"buffer_load_dwordx4 v8, s[80:83], s92 offen offset:0 lds\n" \
"v_mfma_f32_16x16x32_bf16 v[240:243], a[176:179], v[124:127], v[240:243]\n" \
"v_exp_f32_e32 v91, v91\n" \
"v_mfma_f32_16x16x32_bf16 v[244:247], a[180:183], v[124:127], v[244:247]\n" \
"v_cvt_pk_bf16_f32 v98, v68, v69\n" \
"v_cvt_pk_bf16_f32 v99, v70, v71\n" \
"v_mfma_f32_16x16x32_bf16 v[244:247], a[164:167], v[120:123], v[244:247]\n" \
"v_cvt_pk_bf16_f32 v104, v72, v73\n" \
"v_cvt_pk_bf16_f32 v105, v74, v75\n" \
"v_mfma_f32_16x16x32_bf16 v[248:251], a[168:171], v[120:123], v[248:251]\n" \
"v_cvt_pk_bf16_f32 v106, v76, v77\n" \
"v_cvt_pk_bf16_f32 v107, v78, v79\n" \
"v_mfma_f32_16x16x32_bf16 v[248:251], a[184:187], v[124:127], v[248:251]\n" \
"v_cvt_pk_bf16_f32 v112, v80, v81\n" \
"v_cvt_pk_bf16_f32 v113, v82, v83\n" \
"v_mfma_f32_16x16x32_bf16 v[252:255], a[188:191], v[124:127], v[252:255]\n" \
"v_cvt_pk_bf16_f32 v114, v84, v85\n" \
"v_cvt_pk_bf16_f32 v115, v86, v87\n" \
"v_mfma_f32_16x16x32_bf16 v[252:255], a[172:175], v[120:123], v[252:255]\n" \
"v_add_u32_e32 v48, s16, v20\n" \
"s_add_u32 s64, s64, 0x10000\n" \
"v_smfmac_f32_16x16x64_bf16 a[204:207], a[212:215], v[120:127], v21\n" \
"s_waitcnt vmcnt(17) lgkmcnt(0)\n" \
"s_barrier\n" \
"s_mov_b64 exec, 0\n" \
"s_cmp_lg_u32 s15, 0\n" \
"s_cbranch_scc1 L_pf22\n" \
"s_branch L_pf21\n" \
"L_retB:\n" \
"s_mov_b64 exec, -1\n" \
"s_branch L_2_%=\n" \
"L_t2_%=:\n" \
"v_mfma_f32_16x16x32_bf16 v[252:255], a[188:191], v[124:127], v[252:255]\n" \
"v_cvt_pk_bf16_f32 v114, v84, v85\n" \
"v_cvt_pk_bf16_f32 v115, v86, v87\n" \
"v_mfma_f32_16x16x32_bf16 v[252:255], a[172:175], v[120:123], v[252:255]\n" \
"v_xor_b32_e32 v48, 0x8000, v48\n" \
"v_smfmac_f32_16x16x64_bf16 a[204:207], a[212:215], v[120:127], v21\n" \
"s_waitcnt lgkmcnt(0)\n" \
"L_2_%=:\n" \
"v_mfma_f32_16x16x32_bf16 v[64:67], a[128:131], a[64:67], v[32:35]\n" \
"ds_read_b128 a[160:163], v49 offset:0\n" \
"ds_read_b128 a[176:179], v49 offset:4096\n" \
"v_mfma_f32_16x16x32_bf16 v[64:67], a[132:135], a[68:71], v[64:67]\n" \
"v_cvt_pk_bf16_f32 v120, v88, v89\n" \
"v_cvt_pk_bf16_f32 v121, v90, v91\n" \
"v_mfma_f32_16x16x32_bf16 v[64:67], a[136:139], a[72:75], v[64:67]\n" \
"ds_read_b128 a[180:183], v49 offset:5120\n" \
"ds_read_b128 a[164:167], v49 offset:1024\n" \
"v_mfma_f32_16x16x32_bf16 v[64:67], a[140:143], a[76:79], v[64:67]\n" \
"v_exp_f32_e32 v92, v92\n" \
"v_mfma_f32_16x16x32_bf16 v[68:71], a[156:159], a[76:79], v[32:35]\n" \
"ds_read_b128 a[168:171], v49 offset:2048\n" \
"ds_read_b128 a[184:187], v49 offset:6144\n" \
"v_mfma_f32_16x16x32_bf16 v[68:71], a[152:155], a[72:75], v[68:71]\n" \
"v_exp_f32_e32 v93, v93\n" \
"v_mfma_f32_16x16x32_bf16 v[68:71], a[148:151], a[68:71], v[68:71]\n" \
"ds_read_b128 a[188:191], v49 offset:7168\n" \
"ds_read_b128 a[172:175], v49 offset:3072\n" \
"v_mfma_f32_16x16x32_bf16 v[68:71], a[144:147], a[64:67], v[68:71]\n" \
"v_exp_f32_e32 v94, v94\n" \
"v_mfma_f32_16x16x32_bf16 v[72:75], a[128:131], a[80:83], v[36:39]\n" \
"v_exp_f32_e32 v95, v95\n" \
"v_mfma_f32_16x16x32_bf16 v[72:75], a[132:135], a[84:87], v[72:75]\n" \
"v_cvt_pk_bf16_f32 v122, v92, v93\n" \
"v_cvt_pk_bf16_f32 v123, v94, v95\n" \
"v_mfma_f32_16x16x32_bf16 v[72:75], a[136:139], a[88:91], v[72:75]\n" \
"v_exp_f32_e32 v64, v64\n" \
"v_mfma_f32_16x16x32_bf16 v[72:75], a[140:143], a[92:95], v[72:75]\n" \
"v_exp_f32_e32 v65, v65\n" \
"v_mfma_f32_16x16x32_bf16 v[76:79], a[156:159], a[92:95], v[36:39]\n" \
"v_exp_f32_e32 v66, v66\n" \
"v_mfma_f32_16x16x32_bf16 v[76:79], a[152:155], a[88:91], v[76:79]\n" \
"v_exp_f32_e32 v67, v67\n" \
"v_mfma_f32_16x16x32_bf16 v[76:79], a[148:151], a[84:87], v[76:79]\n" \
"v_exp_f32_e32 v68, v68\n" \
"v_mfma_f32_16x16x32_bf16 v[76:79], a[144:147], a[80:83], v[76:79]\n" \
"v_exp_f32_e32 v69, v69\n" \
"v_mfma_f32_16x16x32_bf16 v[80:83], a[128:131], a[96:99], v[40:43]\n" \
"v_exp_f32_e32 v70, v70\n" \
"v_mfma_f32_16x16x32_bf16 v[80:83], a[132:135], a[100:103], v[80:83]\n" \
"v_exp_f32_e32 v71, v71\n" \
"v_mfma_f32_16x16x32_bf16 v[80:83], a[136:139], a[104:107], v[80:83]\n" \
"v_cvt_pk_bf16_f32 v100, v64, v65\n" \
"v_cvt_pk_bf16_f32 v101, v66, v67\n" \
"v_mfma_f32_16x16x32_bf16 v[80:83], a[140:143], a[108:111], v[80:83]\n" \
"v_cvt_pk_bf16_f32 v102, v68, v69\n" \
"v_cvt_pk_bf16_f32 v103, v70, v71\n" \
"v_mfma_f32_16x16x32_bf16 v[84:87], a[156:159], a[108:111], v[40:43]\n" \
"v_exp_f32_e32 v72, v72\n" \
"v_mfma_f32_16x16x32_bf16 v[84:87], a[152:155], a[104:107], v[84:87]\n" \
"v_exp_f32_e32 v73, v73\n" \
"v_mfma_f32_16x16x32_bf16 v[84:87], a[148:151], a[100:103], v[84:87]\n" \
"v_exp_f32_e32 v74, v74\n" \
"v_mfma_f32_16x16x32_bf16 v[84:87], a[144:147], a[96:99], v[84:87]\n" \
"v_exp_f32_e32 v75, v75\n" \
"v_mfma_f32_16x16x32_bf16 v[88:91], a[128:131], a[112:115], v[44:47]\n" \
"v_exp_f32_e32 v76, v76\n" \
"v_mfma_f32_16x16x32_bf16 v[88:91], a[132:135], a[116:119], v[88:91]\n" \
"v_exp_f32_e32 v77, v77\n" \
"v_mfma_f32_16x16x32_bf16 v[88:91], a[136:139], a[120:123], v[88:91]\n" \
"v_exp_f32_e32 v78, v78\n" \
"v_mfma_f32_16x16x32_bf16 v[88:91], a[140:143], a[124:127], v[88:91]\n" \
"v_exp_f32_e32 v79, v79\n" \
"v_mfma_f32_16x16x32_bf16 v[92:95], a[156:159], a[124:127], v[44:47]\n" \
"v_cvt_pk_bf16_f32 v108, v72, v73\n" \
"v_cvt_pk_bf16_f32 v109, v74, v75\n" \
"v_mfma_f32_16x16x32_bf16 v[92:95], a[152:155], a[120:123], v[92:95]\n" \
"v_cvt_pk_bf16_f32 v110, v76, v77\n" \
"v_cvt_pk_bf16_f32 v111, v78, v79\n" \
"v_mfma_f32_16x16x32_bf16 v[92:95], a[148:151], a[116:119], v[92:95]\n" \
"ds_read_b128 a[128:131], v48 offset:0\n" \
"ds_read_b128 a[132:135], v48 offset:1024\n" \
"v_mfma_f32_16x16x32_bf16 v[92:95], a[144:147], a[112:115], v[92:95]\n" \
"s_add_u32 s63, s63, s63\n" \
"s_waitcnt lgkmcnt(2)\n" \
"v_mfma_f32_16x16x32_bf16 v[128:131], a[160:163], v[96:99], v[128:131]\n" \
"ds_read_b128 a[136:139], v48 offset:2048\n" \
"ds_read_b128 a[140:143], v48 offset:3072\n" \
"v_mfma_f32_16x16x32_bf16 v[128:131], a[176:179], v[100:103], v[128:131]\n" \
"v_exp_f32_e32 v80, v80\n" \
"v_mfma_f32_16x16x32_bf16 v[132:135], a[180:183], v[100:103], v[132:135]\n" \
"ds_read_b128 a[144:147], v48 offset:4096\n" \
"ds_read_b128 a[148:151], v48 offset:5120\n" \
"v_mfma_f32_16x16x32_bf16 v[132:135], a[164:167], v[96:99], v[132:135]\n" \
"v_exp_f32_e32 v81, v81\n" \
"v_mfma_f32_16x16x32_bf16 v[136:139], a[168:171], v[96:99], v[136:139]\n" \
"ds_read_b128 a[152:155], v48 offset:6144\n" \
"ds_read_b128 a[156:159], v48 offset:7168\n" \
"v_mfma_f32_16x16x32_bf16 v[136:139], a[184:187], v[100:103], v[136:139]\n" \
"v_exp_f32_e32 v82, v82\n" \
"v_mfma_f32_16x16x32_bf16 v[140:143], a[188:191], v[100:103], v[140:143]\n" \
"v_exp_f32_e32 v83, v83\n" \
"v_mfma_f32_16x16x32_bf16 v[140:143], a[172:175], v[96:99], v[140:143]\n" \
"v_exp_f32_e32 v84, v84\n" \
"v_smfmac_f32_16x16x64_bf16 a[192:195], a[212:215], v[96:103], v21\n" \
"v_mfma_f32_16x16x32_bf16 v[160:163], a[160:163], v[104:107], v[160:163]\n" \
"v_exp_f32_e32 v85, v85\n" \
"v_mfma_f32_16x16x32_bf16 v[160:163], a[176:179], v[108:111], v[160:163]\n" \
"v_exp_f32_e32 v86, v86\n" \
"v_mfma_f32_16x16x32_bf16 v[164:167], a[180:183], v[108:111], v[164:167]\n" \
"v_exp_f32_e32 v87, v87\n" \
"v_mfma_f32_16x16x32_bf16 v[164:167], a[164:167], v[104:107], v[164:167]\n" \
"v_cvt_pk_bf16_f32 v116, v80, v81\n" \
"v_cvt_pk_bf16_f32 v117, v82, v83\n" \
"v_mfma_f32_16x16x32_bf16 v[168:171], a[168:171], v[104:107], v[168:171]\n" \
"v_cvt_pk_bf16_f32 v118, v84, v85\n" \
"v_cvt_pk_bf16_f32 v119, v86, v87\n" \
"v_mfma_f32_16x16x32_bf16 v[168:171], a[184:187], v[108:111], v[168:171]\n" \
"v_exp_f32_e32 v88, v88\n" \
"v_mfma_f32_16x16x32_bf16 v[172:175], a[188:191], v[108:111], v[172:175]\n" \
"v_exp_f32_e32 v89, v89\n" \
"v_mfma_f32_16x16x32_bf16 v[172:175], a[172:175], v[104:107], v[172:175]\n" \
"v_exp_f32_e32 v90, v90\n" \
"v_smfmac_f32_16x16x64_bf16 a[196:199], a[212:215], v[104:111], v21\n" \
"v_mfma_f32_16x16x32_bf16 v[192:195], a[160:163], v[112:115], v[192:195]\n" \
"v_exp_f32_e32 v91, v91\n" \
"v_mfma_f32_16x16x32_bf16 v[192:195], a[176:179], v[116:119], v[192:195]\n" \
"v_exp_f32_e32 v92, v92\n" \
"v_mfma_f32_16x16x32_bf16 v[196:199], a[180:183], v[116:119], v[196:199]\n" \
"v_exp_f32_e32 v93, v93\n" \
"v_mfma_f32_16x16x32_bf16 v[196:199], a[164:167], v[112:115], v[196:199]\n" \
"v_exp_f32_e32 v94, v94\n" \
"v_mfma_f32_16x16x32_bf16 v[200:203], a[168:171], v[112:115], v[200:203]\n" \
"v_exp_f32_e32 v95, v95\n" \
"v_mfma_f32_16x16x32_bf16 v[200:203], a[184:187], v[116:119], v[200:203]\n" \
"v_cvt_pk_bf16_f32 v124, v88, v89\n" \
"v_cvt_pk_bf16_f32 v125, v90, v91\n" \
"v_mfma_f32_16x16x32_bf16 v[204:207], a[188:191], v[116:119], v[204:207]\n" \
"v_cvt_pk_bf16_f32 v126, v92, v93\n" \
"v_cvt_pk_bf16_f32 v127, v94, v95\n" \
"v_mfma_f32_16x16x32_bf16 v[204:207], a[172:175], v[112:115], v[204:207]\n" \
"v_smfmac_f32_16x16x64_bf16 a[200:203], a[212:215], v[112:119], v21\n" \
"s_waitcnt vmcnt(0) lgkmcnt(0)\n" \
"s_barrier\n" \
"v_mfma_f32_16x16x32_bf16 v[224:227], a[160:163], v[120:123], v[224:227]\n" \
"s_cbranch_scc1 L_h2_%=\n" \
"L_hr2_%=:\n" \
"v_mfma_f32_16x16x32_bf16 v[224:227], a[176:179], v[124:127], v[224:227]\n" \
"s_xor_b32 m0, s65, 0x12000\n" \
"ds_read_b128 a[160:163], v49 offset:8192\n" \
"v_mfma_f32_16x16x32_bf16 v[228:231], a[180:183], v[124:127], v[228:231]\n" \
"buffer_load_dwordx4 v50, s[8:11], s64 offen offset:0 sc1 lds\n" \
"v_mfma_f32_16x16x32_bf16 v[228:231], a[164:167], v[120:123], v[228:231]\n" \
"ds_read_b128 a[176:179], v49 offset:12288\n" \
"ds_read_b128 a[180:183], v49 offset:13312\n" \
"v_mfma_f32_16x16x32_bf16 v[232:235], a[168:171], v[120:123], v[232:235]\n" \
"v_mfma_f32_16x16x32_bf16 v[232:235], a[184:187], v[124:127], v[232:235]\n" \
"buffer_load_dwordx4 v50, s[8:11], s64 offen offset:1024 sc1 lds\n" \
"v_mfma_f32_16x16x32_bf16 v[236:239], a[188:191], v[124:127], v[236:239]\n" \
"ds_read_b128 a[164:167], v49 offset:9216\n" \
"ds_read_b128 a[168:171], v49 offset:10240\n" \
"v_mfma_f32_16x16x32_bf16 v[236:239], a[172:175], v[120:123], v[236:239]\n" \
"v_mfma_f32_16x16x32_bf16 v[64:67], a[128:131], a[64:67], v[32:35]\n" \
"ds_read_b128 a[184:187], v49 offset:14336\n" \
"ds_read_b128 a[188:191], v49 offset:15360\n" \
"v_mfma_f32_16x16x32_bf16 v[64:67], a[132:135], a[68:71], v[64:67]\n" \
"buffer_load_dwordx4 v50, s[8:11], s64 offen offset:2048 sc1 lds\n" \
"v_mfma_f32_16x16x32_bf16 v[64:67], a[136:139], a[72:75], v[64:67]\n" \
"ds_read_b128 a[172:175], v49 offset:11264\n" \
"v_add_u32_e32 v49, 0x10000, v48\n" \
"v_mfma_f32_16x16x32_bf16 v[64:67], a[140:143], a[76:79], v[64:67]\n" \
"v_mfma_f32_16x16x32_bf16 v[68:71], a[156:159], a[76:79], v[32:35]\n" \
"v_mfma_f32_16x16x32_bf16 v[68:71], a[152:155], a[72:75], v[68:71]\n" \
"v_mfma_f32_16x16x32_bf16 v[68:71], a[148:151], a[68:71], v[68:71]\n" \
"buffer_load_dwordx4 v50, s[8:11], s64 offen offset:3072 sc1 lds\n" \
"v_mfma_f32_16x16x32_bf16 v[68:71], a[144:147], a[64:67], v[68:71]\n" \
"s_xor_b32 m0, s65, 0xa000\n" \
"v_mfma_f32_16x16x32_bf16 v[72:75], a[128:131], a[80:83], v[36:39]\n" \
"v_mfma_f32_16x16x32_bf16 v[72:75], a[132:135], a[84:87], v[72:75]\n" \
"v_exp_f32_e32 v64, v64\n" \
"v_mfma_f32_16x16x32_bf16 v[72:75], a[136:139], a[88:91], v[72:75]\n" \
"buffer_load_dwordx4 v51, s[8:11], s64 offen offset:0 sc1 lds\n" \
"v_mfma_f32_16x16x32_bf16 v[72:75], a[140:143], a[92:95], v[72:75]\n" \
"v_exp_f32_e32 v65, v65\n" \
"v_mfma_f32_16x16x32_bf16 v[76:79], a[156:159], a[92:95], v[36:39]\n" \
"v_exp_f32_e32 v66, v66\n" \
"v_mfma_f32_16x16x32_bf16 v[76:79], a[152:155], a[88:91], v[76:79]\n" \
"v_exp_f32_e32 v67, v67\n" \
"v_mfma_f32_16x16x32_bf16 v[76:79], a[148:151], a[84:87], v[76:79]\n" \
"buffer_load_dwordx4 v51, s[8:11], s64 offen offset:1024 sc1 lds\n" \
"v_mfma_f32_16x16x32_bf16 v[76:79], a[144:147], a[80:83], v[76:79]\n" \
"v_exp_f32_e32 v68, v68\n" \
"v_mfma_f32_16x16x32_bf16 v[80:83], a[128:131], a[96:99], v[40:43]\n" \
"v_exp_f32_e32 v69, v69\n" \
"v_mfma_f32_16x16x32_bf16 v[80:83], a[132:135], a[100:103], v[80:83]\n" \
"v_exp_f32_e32 v70, v70\n" \
"v_mfma_f32_16x16x32_bf16 v[80:83], a[136:139], a[104:107], v[80:83]\n" \
"v_exp_f32_e32 v71, v71\n" \
"v_mfma_f32_16x16x32_bf16 v[80:83], a[140:143], a[108:111], v[80:83]\n" \
"buffer_load_dwordx4 v51, s[8:11], s64 offen offset:2048 sc1 lds\n" \
"v_mfma_f32_16x16x32_bf16 v[84:87], a[156:159], a[108:111], v[40:43]\n" \
"v_exp_f32_e32 v72, v72\n" \
"v_mfma_f32_16x16x32_bf16 v[84:87], a[152:155], a[104:107], v[84:87]\n" \
"v_exp_f32_e32 v73, v73\n" \
"v_mfma_f32_16x16x32_bf16 v[84:87], a[148:151], a[100:103], v[84:87]\n" \
"v_exp_f32_e32 v74, v74\n" \
"v_mfma_f32_16x16x32_bf16 v[84:87], a[144:147], a[96:99], v[84:87]\n" \
"buffer_load_dwordx4 v51, s[8:11], s64 offen offset:3072 sc1 lds\n" \
"v_mfma_f32_16x16x32_bf16 v[88:91], a[128:131], a[112:115], v[44:47]\n" \
"s_xor_b32 m0, s65, 0x18000\n" \
"ds_read_b128 a[128:131], v48 offset:8192\n" \
"v_mfma_f32_16x16x32_bf16 v[88:91], a[132:135], a[116:119], v[88:91]\n" \
"v_exp_f32_e32 v75, v75\n" \
"v_mfma_f32_16x16x32_bf16 v[88:91], a[136:139], a[120:123], v[88:91]\n" \
"ds_read_b128 a[132:135], v48 offset:9216\n" \
"ds_read_b128 a[136:139], v48 offset:10240\n" \
"v_mfma_f32_16x16x32_bf16 v[88:91], a[140:143], a[124:127], v[88:91]\n" \
"v_exp_f32_e32 v76, v76\n" \
"v_mfma_f32_16x16x32_bf16 v[92:95], a[156:159], a[124:127], v[44:47]\n" \
"buffer_load_dwordx4 v52, s[8:11], s64 offen offset:0 sc1 lds\n" \
"v_mfma_f32_16x16x32_bf16 v[92:95], a[152:155], a[120:123], v[92:95]\n" \
"ds_read_b128 a[140:143], v48 offset:11264\n" \
"ds_read_b128 a[152:155], v48 offset:14336\n" \
"v_mfma_f32_16x16x32_bf16 v[92:95], a[148:151], a[116:119], v[92:95]\n" \
"v_exp_f32_e32 v77, v77\n" \
"v_mfma_f32_16x16x32_bf16 v[92:95], a[144:147], a[112:115], v[92:95]\n" \
"ds_read_b128 a[144:147], v48 offset:12288\n" \
"s_waitcnt lgkmcnt(6)\n" \
"v_mfma_f32_16x16x32_bf16 v[144:147], a[160:163], v[96:99], v[144:147]\n" \
"buffer_load_dwordx4 v52, s[8:11], s64 offen offset:1024 sc1 lds\n" \
"v_mfma_f32_16x16x32_bf16 v[144:147], a[176:179], v[100:103], v[144:147]\n" \
"ds_read_b128 a[148:151], v48 offset:13312\n" \
"ds_read_b128 a[156:159], v48 offset:15360\n" \
"v_mfma_f32_16x16x32_bf16 v[148:151], a[180:183], v[100:103], v[148:151]\n" \
"v_exp_f32_e32 v78, v78\n" \
"v_mfma_f32_16x16x32_bf16 v[148:151], a[164:167], v[96:99], v[148:151]\n" \
"v_exp_f32_e32 v79, v79\n" \
"v_mfma_f32_16x16x32_bf16 v[152:155], a[168:171], v[96:99], v[152:155]\n" \
"buffer_load_dwordx4 v52, s[8:11], s64 offen offset:2048 sc1 lds\n" \
"v_mfma_f32_16x16x32_bf16 v[152:155], a[184:187], v[100:103], v[152:155]\n" \
"v_exp_f32_e32 v80, v80\n" \
"v_mfma_f32_16x16x32_bf16 v[156:159], a[188:191], v[100:103], v[156:159]\n" \
"v_exp_f32_e32 v81, v81\n" \
"v_mfma_f32_16x16x32_bf16 v[156:159], a[172:175], v[96:99], v[156:159]\n" \
"v_exp_f32_e32 v82, v82\n" \
"v_mfma_f32_16x16x32_bf16 v[176:179], a[160:163], v[104:107], v[176:179]\n" \
"v_exp_f32_e32 v83, v83\n" \
"v_mfma_f32_16x16x32_bf16 v[176:179], a[176:179], v[108:111], v[176:179]\n" \
"buffer_load_dwordx4 v52, s[8:11], s64 offen offset:3072 sc1 lds\n" \
"v_mfma_f32_16x16x32_bf16 v[180:183], a[180:183], v[108:111], v[180:183]\n" \
"s_mov_b32 m0, s65\n" \
"s_xor_b32 s65, s65, 0x8000\n" \
"v_mfma_f32_16x16x32_bf16 v[180:183], a[164:167], v[104:107], v[180:183]\n" \
"v_exp_f32_e32 v84, v84\n" \
"v_mfma_f32_16x16x32_bf16 v[184:187], a[168:171], v[104:107], v[184:187]\n" \
"v_exp_f32_e32 v85, v85\n" \
"v_mfma_f32_16x16x32_bf16 v[184:187], a[184:187], v[108:111], v[184:187]\n" \
"buffer_load_dwordx4 v53, s[8:11], s64 offen offset:0 sc1 lds\n" \
"v_mfma_f32_16x16x32_bf16 v[188:191], a[188:191], v[108:111], v[188:191]\n" \
"v_exp_f32_e32 v86, v86\n" \
"v_mfma_f32_16x16x32_bf16 v[188:191], a[172:175], v[104:107], v[188:191]\n" \
"v_exp_f32_e32 v87, v87\n" \
"v_mfma_f32_16x16x32_bf16 v[208:211], a[160:163], v[112:115], v[208:211]\n" \
"v_exp_f32_e32 v88, v88\n" \
"v_mfma_f32_16x16x32_bf16 v[208:211], a[176:179], v[116:119], v[208:211]\n" \
"buffer_load_dwordx4 v53, s[8:11], s64 offen offset:1024 sc1 lds\n" \
"v_mfma_f32_16x16x32_bf16 v[212:215], a[180:183], v[116:119], v[212:215]\n" \
"v_exp_f32_e32 v89, v89\n" \
"v_mfma_f32_16x16x32_bf16 v[212:215], a[164:167], v[112:115], v[212:215]\n" \
"v_exp_f32_e32 v90, v90\n" \
"v_mfma_f32_16x16x32_bf16 v[216:219], a[168:171], v[112:115], v[216:219]\n" \
"v_exp_f32_e32 v91, v91\n" \
"v_mfma_f32_16x16x32_bf16 v[216:219], a[184:187], v[116:119], v[216:219]\n" \
"v_cvt_pk_bf16_f32 v96, v64, v65\n" \
"v_cvt_pk_bf16_f32 v97, v66, v67\n" \
"v_mfma_f32_16x16x32_bf16 v[220:223], a[188:191], v[116:119], v[220:223]\n" \
"buffer_load_dwordx4 v53, s[8:11], s64 offen offset:2048 sc1 lds\n" \
"v_mfma_f32_16x16x32_bf16 v[220:223], a[172:175], v[112:115], v[220:223]\n" \
"v_cvt_pk_bf16_f32 v98, v68, v69\n" \
"v_cvt_pk_bf16_f32 v99, v70, v71\n" \
"v_mfma_f32_16x16x32_bf16 v[240:243], a[160:163], v[120:123], v[240:243]\n" \
"v_cvt_pk_bf16_f32 v104, v72, v73\n" \
"v_cvt_pk_bf16_f32 v105, v74, v75\n" \
"v_mfma_f32_16x16x32_bf16 v[240:243], a[176:179], v[124:127], v[240:243]\n" \
"v_cvt_pk_bf16_f32 v106, v76, v77\n" \
"v_cvt_pk_bf16_f32 v107, v78, v79\n" \
"v_mfma_f32_16x16x32_bf16 v[244:247], a[180:183], v[124:127], v[244:247]\n" \
"buffer_load_dwordx4 v53, s[8:11], s64 offen offset:3072 sc1 lds\n" \
"v_mfma_f32_16x16x32_bf16 v[244:247], a[164:167], v[120:123], v[244:247]\n" \
"s_add_u32 s64, s64, 0x10000\n" \
"s_sub_u32 s66, s66, 1\n" \
"v_mfma_f32_16x16x32_bf16 v[248:251], a[168:171], v[120:123], v[248:251]\n" \
"v_cvt_pk_bf16_f32 v112, v80, v81\n" \
"v_cvt_pk_bf16_f32 v113, v82, v83\n" \
"v_mfma_f32_16x16x32_bf16 v[248:251], a[184:187], v[124:127], v[248:251]\n" \
"s_cbranch_scc0 L_t2_%=\n" \
"v_mfma_f32_16x16x32_bf16 v[252:255], a[188:191], v[124:127], v[252:255]\n" \
"v_cvt_pk_bf16_f32 v114, v84, v85\n" \
"v_cvt_pk_bf16_f32 v115, v86, v87\n" \
"v_mfma_f32_16x16x32_bf16 v[252:255], a[172:175], v[120:123], v[252:255]\n" \
"v_xor_b32_e32 v48, 0x8000, v48\n" \
"v_smfmac_f32_16x16x64_bf16 a[204:207], a[212:215], v[120:127], v21\n" \
"s_waitcnt lgkmcnt(0)\n" \
"s_waitcnt vmcnt(12)\n" \
"s_barrier\n" \
"s_cmp_lg_u32 s16, 0\n" \
"s_cbranch_scc1 L_f1_%=\n" \
"ds_read_b128 a[160:163], v49 offset:0\n" \
"ds_read_b128 a[164:167], v49 offset:1024\n" \
"ds_read_b128 a[168:171], v49 offset:2048\n" \
"ds_read_b128 a[172:175], v49 offset:3072\n" \
"ds_read_b128 a[176:179], v49 offset:8192\n" \
"ds_read_b128 a[180:183], v49 offset:9216\n" \
"v_cvt_pk_bf16_f32 v120, v88, v89\n" \
"v_cvt_pk_bf16_f32 v121, v90, v91\n" \
"v_mfma_f32_16x16x32_bf16 v[88:91], a[128:131], a[112:115], v[44:47]\n" \
"s_add_u32 s70, s61, 0x0\n" \
"s_add_u32 s71, s61, 0x49000\n" \
"ds_read_b128 a[184:187], v49 offset:10240\n" \
"v_mfma_f32_16x16x32_bf16 v[88:91], a[132:135], a[116:119], v[88:91]\n" \
"ds_read_b128 a[188:191], v49 offset:11264\n" \
"v_add_u32_e32 v54, 0x49000, v20\n" \
"v_mfma_f32_16x16x32_bf16 v[88:91], a[136:139], a[120:123], v[88:91]\n" \
"v_exp_f32_e32 v92, v92\n" \
"v_mfma_f32_16x16x32_bf16 v[88:91], a[140:143], a[124:127], v[88:91]\n" \
"v_exp_f32_e32 v93, v93\n" \
"v_exp_f32_e32 v94, v94\n" \
"v_exp_f32_e32 v95, v95\n" \
"v_cvt_pk_bf16_f32 v122, v92, v93\n" \
"v_cvt_pk_bf16_f32 v123, v94, v95\n" \
"v_mfma_f32_16x16x32_bf16 v[92:95], a[156:159], a[124:127], v[44:47]\n" \
"buffer_load_dwordx4 a[220:223], v20, s[56:59], s60 offen offset:0 sc1\n" \
"v_mfma_f32_16x16x32_bf16 v[92:95], a[152:155], a[120:123], v[92:95]\n" \
"v_readlane_b32 s97, v11, 32\n" \
"v_readlane_b32 s98, v11, 33\n" \
"v_mfma_f32_16x16x32_bf16 v[92:95], a[148:151], a[116:119], v[92:95]\n" \
"v_and_b32_e32 v112, v12, v112\n" \
"v_and_b32_e32 v113, v13, v113\n" \
"v_mfma_f32_16x16x32_bf16 v[92:95], a[144:147], a[112:115], v[92:95]\n" \
"v_and_b32_e32 v114, v14, v114\n" \
"s_waitcnt lgkmcnt(7)\n" \
"v_mfma_f32_16x16x32_bf16 v[128:131], a[160:163], v[96:99], v[128:131]\n" \
"v_and_b32_e32 v115, v15, v115\n" \
"s_waitcnt lgkmcnt(6)\n" \
"v_mfma_f32_16x16x32_bf16 v[132:135], a[164:167], v[96:99], v[132:135]\n" \
"ds_read_b32 v60, v18 offset:0\n" \
"s_waitcnt lgkmcnt(6)\n" \
"v_mfma_f32_16x16x32_bf16 v[136:139], a[168:171], v[96:99], v[136:139]\n" \
"ds_read_b32 v61, v18 offset:128\n" \
"s_waitcnt lgkmcnt(6)\n" \
"v_mfma_f32_16x16x32_bf16 v[140:143], a[172:175], v[96:99], v[140:143]\n" \
"ds_read_b128 a[0:3], v49 offset:4096\n" \
"s_waitcnt lgkmcnt(5)\n" \
"v_mfma_f32_16x16x32_bf16 v[144:147], a[176:179], v[96:99], v[144:147]\n" \
"buffer_load_dwordx4 a[236:239], v54, s[56:59], s60 offen offset:0 sc1\n" \
"v_mfma_f32_16x16x32_bf16 v[148:151], a[180:183], v[96:99], v[148:151]\n" \
"ds_read_b128 a[4:7], v49 offset:5120\n" \
"s_waitcnt lgkmcnt(5)\n" \
"v_mfma_f32_16x16x32_bf16 v[152:155], a[184:187], v[96:99], v[152:155]\n" \
"ds_read_b128 a[8:11], v49 offset:6144\n" \
"s_waitcnt lgkmcnt(5)\n" \
"v_mfma_f32_16x16x32_bf16 v[156:159], a[188:191], v[96:99], v[156:159]\n" \
"buffer_load_dwordx4 a[224:227], v20, s[56:59], s60 offen offset:1024 sc1\n" \
"v_mfma_f32_16x16x32_bf16 a[192:195], a[252:255], v[96:99], a[192:195]\n" \
"ds_read_b128 a[12:15], v49 offset:7168\n" \
"ds_read_b128 a[16:19], v49 offset:12288\n" \
"v_mfma_f32_16x16x32_bf16 v[160:163], a[160:163], v[104:107], v[160:163]\n" \
"ds_read_b128 a[20:23], v49 offset:13312\n" \
"ds_read_b128 a[24:27], v49 offset:14336\n" \
"buffer_load_dwordx4 a[240:243], v54, s[56:59], s60 offen offset:1024 sc1\n" \
"v_mfma_f32_16x16x32_bf16 v[164:167], a[164:167], v[104:107], v[164:167]\n" \
"ds_read_b128 a[28:31], v49 offset:15360\n" \
"v_accvgpr_read_b32 v40, a192\n" \
"v_mfma_f32_16x16x32_bf16 v[168:171], a[168:171], v[104:107], v[168:171]\n" \
"v_accvgpr_read_b32 v23, a193\n" \
"v_add_f32_e32 v40, v40, v23\n" \
"v_mfma_f32_16x16x32_bf16 v[172:175], a[172:175], v[104:107], v[172:175]\n" \
"buffer_load_dwordx4 a[228:231], v20, s[56:59], s60 offen offset:2048 sc1\n" \
"v_mfma_f32_16x16x32_bf16 v[176:179], a[176:179], v[104:107], v[176:179]\n" \
"v_add_u32_e32 v50, s15, v48\n" \
"v_add_u32_e32 v51, 0x10000, v50\n" \
"v_mfma_f32_16x16x32_bf16 v[180:183], a[180:183], v[104:107], v[180:183]\n" \
"s_waitcnt vmcnt(5)\n" \
"ds_write_b128 v50, v[128:131] offset:0\n" \
"buffer_load_dwordx4 a[244:247], v54, s[56:59], s60 offen offset:2048 sc1\n" \
"v_mfma_f32_16x16x32_bf16 v[184:187], a[184:187], v[104:107], v[184:187]\n" \
"ds_write_b128 v50, v[132:135] offset:1024\n" \
"v_mfma_f32_16x16x32_bf16 v[188:191], a[188:191], v[104:107], v[188:191]\n" \
"ds_write_b128 v50, v[136:139] offset:2048\n" \
"buffer_load_dwordx4 a[232:235], v20, s[56:59], s60 offen offset:3072 sc1\n" \
"v_mfma_f32_16x16x32_bf16 a[196:199], a[252:255], v[104:107], a[196:199]\n" \
"ds_write_b128 v50, v[140:143] offset:3072\n" \
"v_mfma_f32_16x16x32_bf16 v[192:195], a[160:163], v[112:115], v[192:195]\n" \
"ds_write_b128 v50, v[144:147] offset:8192\n" \
"buffer_load_dwordx4 a[248:251], v54, s[56:59], s60 offen offset:3072 sc1\n" \
"v_mfma_f32_16x16x32_bf16 v[196:199], a[164:167], v[112:115], v[196:199]\n" \
"ds_write_b128 v50, v[148:151] offset:9216\n" \
"v_mfma_f32_16x16x32_bf16 v[200:203], a[168:171], v[112:115], v[200:203]\n" \
"ds_write_b128 v50, v[152:155] offset:10240\n" \
"buffer_load_dwordx4 a[32:35], v20, s[56:59], s97 offen offset:0 sc1\n" \
"v_mfma_f32_16x16x32_bf16 v[204:207], a[172:175], v[112:115], v[204:207]\n" \
"v_accvgpr_read_b32 v41, a196\n" \
"v_accvgpr_read_b32 v23, a197\n" \
"v_mfma_f32_16x16x32_bf16 v[208:211], a[176:179], v[112:115], v[208:211]\n" \
"v_add_f32_e32 v41, v41, v23\n" \
"v_xor_b32_e32 v52, 0x4000, v50\n" \
"v_mfma_f32_16x16x32_bf16 v[212:215], a[180:183], v[112:115], v[212:215]\n" \
"buffer_load_dwordx4 a[40:43], v54, s[56:59], s97 offen offset:0 sc1\n" \
"v_mfma_f32_16x16x32_bf16 v[216:219], a[184:187], v[112:115], v[216:219]\n" \
"ds_write_b128 v50, v[156:159] offset:11264\n" \
"v_mfma_f32_16x16x32_bf16 v[220:223], a[188:191], v[112:115], v[220:223]\n" \
"ds_write_b128 v51, v[160:163] offset:0\n" \
"buffer_load_dwordx4 a[48:51], v20, s[56:59], s98 offen offset:0 sc1\n" \
"v_mfma_f32_16x16x32_bf16 a[200:203], a[252:255], v[112:115], a[200:203]\n" \
"ds_write_b128 v51, v[164:167] offset:1024\n" \
"ds_write_b128 v51, v[168:171] offset:2048\n" \
"ds_write_b128 v51, v[172:175] offset:3072\n" \
"buffer_load_dwordx4 a[56:59], v54, s[56:59], s98 offen offset:0 sc1\n" \
"ds_write_b128 v51, v[176:179] offset:8192\n" \
"ds_write_b128 v51, v[180:183] offset:9216\n" \
"ds_write_b128 v51, v[184:187] offset:10240\n" \
"ds_write_b128 v51, v[188:191] offset:11264\n" \
"buffer_load_dwordx4 a[36:39], v20, s[56:59], s97 offen offset:1024 sc1\n" \
"ds_write_b64 v19, v[40:41]\n" \
"v_xor_b32_e32 v53, 0x4000, v51\n" \
"v_xor_b32_e32 v63, 0x400, v19\n" \
"v_readlane_b32 s93, v11, 18\n" \
"v_readlane_b32 s84, v11, 13\n" \
"v_readlane_b32 s85, v11, 14\n" \
"v_readlane_b32 s86, v11, 15\n" \
"s_mov_b32 s87, 0x20000\n" \
"v_readlane_b32 s92, v11, 16\n" \
"s_add_u32 m0, s93, 0x0\n" \
"v_exp_f32_e32 v88, v88\n" \
"v_exp_f32_e32 v89, v89\n" \
"v_exp_f32_e32 v90, v90\n" \
"v_exp_f32_e32 v91, v91\n" \
"v_exp_f32_e32 v92, v92\n" \
"v_exp_f32_e32 v93, v93\n" \
"v_exp_f32_e32 v94, v94\n" \
"v_exp_f32_e32 v95, v95\n" \
"v_cvt_pk_bf16_f32 v124, v88, v89\n" \
"v_cvt_pk_bf16_f32 v125, v90, v91\n" \
"v_cvt_pk_bf16_f32 v126, v92, v93\n" \
"v_cvt_pk_bf16_f32 v127, v94, v95\n" \
"v_and_b32_e32 v124, v12, v124\n" \
"v_and_b32_e32 v125, v13, v125\n" \
"v_and_b32_e32 v126, v14, v126\n" \
"v_and_b32_e32 v127, v15, v127\n" \
"v_accvgpr_read_b32 v56, a200\n" \
"v_accvgpr_read_b32 v23, a201\n" \
"v_add_f32_e32 v56, v56, v23\n" \
"s_waitcnt lgkmcnt(0)\n" \
"s_barrier\n" \
"buffer_load_dwordx4 a[44:47], v54, s[56:59], s97 offen offset:1024 sc1\n" \
"ds_read_b128 v[128:131], v52 offset:0\n" \
"ds_read_b128 v[132:135], v52 offset:1024\n" \
"ds_read_b128 v[136:139], v52 offset:2048\n" \
"L_pf13:\n" \
"s_cbranch_execz L_pf17\n" \
"s_nop 0\n" \
"ds_read_b128 v[140:143], v52 offset:3072\n" \
"ds_read_b128 v[144:147], v52 offset:8192\n" \
"ds_read_b128 v[148:151], v52 offset:9216\n" \
"ds_read_b128 v[152:155], v52 offset:10240\n" \
"ds_read_b128 v[156:159], v52 offset:11264\n" \
"buffer_load_dwordx4 a[52:55], v20, s[56:59], s98 offen offset:1024 sc1\n" \
"ds_read_b128 v[160:163], v53 offset:0\n" \
"ds_read_b128 v[164:167], v53 offset:1024\n" \
"ds_read_b128 v[168:171], v53 offset:2048\n" \
"ds_read_b128 v[172:175], v53 offset:3072\n" \
"ds_read_b128 v[176:179], v53 offset:8192\n" \
"ds_read_b128 v[180:183], v53 offset:9216\n" \
"ds_read_b128 v[184:187], v53 offset:10240\n" \
"ds_read_b128 v[188:191], v53 offset:11264\n" \
"buffer_load_dwordx4 a[60:63], v54, s[56:59], s98 offen offset:1024 sc1\n" \
"ds_read_b64 v[42:43], v63\n" \
"v_mfma_f32_16x16x32_bf16 v[224:227], a[160:163], v[120:123], v[224:227]\n" \
"v_mfma_f32_16x16x32_bf16 v[228:231], a[164:167], v[120:123], v[228:231]\n" \
"v_mfma_f32_16x16x32_bf16 v[232:235], a[168:171], v[120:123], v[232:235]\n" \
"buffer_load_dwordx4 v0, s[84:87], s92 offen offset:0 lds\n" \
"v_mfma_f32_16x16x32_bf16 v[236:239], a[172:175], v[120:123], v[236:239]\n" \
"v_mfma_f32_16x16x32_bf16 v[240:243], a[176:179], v[120:123], v[240:243]\n" \
"v_mfma_f32_16x16x32_bf16 v[244:247], a[180:183], v[120:123], v[244:247]\n" \
"buffer_load_dwordx4 v1, s[84:87], s92 offen offset:1024 lds\n" \
"v_mfma_f32_16x16x32_bf16 v[248:251], a[184:187], v[120:123], v[248:251]\n" \
"s_waitcnt lgkmcnt(15)\n" \
"v_add_f32_e32 v192, v192, v128\n" \
"v_mfma_f32_16x16x32_bf16 v[252:255], a[188:191], v[120:123], v[252:255]\n" \
"v_add_f32_e32 v193, v193, v129\n" \
"v_add_f32_e32 v194, v194, v130\n" \
"v_mfma_f32_16x16x32_bf16 a[204:207], a[252:255], v[120:123], a[204:207]\n" \
"buffer_load_dwordx4 v2, s[84:87], s92 offen offset:2048 lds\n" \
"v_add_f32_e32 v195, v195, v131\n" \
"s_waitcnt lgkmcnt(1)\n" \
"s_barrier\n" \
"v_mfma_f32_16x16x32_bf16 v[224:227], a[0:3], v[124:127], v[224:227]\n" \
"buffer_load_dwordx4 v3, s[84:87], s92 offen offset:3072 lds\n" \
"s_add_u32 m0, s93, 0x1000\n" \
"v_mfma_f32_16x16x32_bf16 v[228:231], a[4:7], v[124:127], v[228:231]\n" \
"s_waitcnt lgkmcnt(0)\n" \
"v_add_f32_e32 v56, v56, v42\n" \
"v_mfma_f32_16x16x32_bf16 v[232:235], a[8:11], v[124:127], v[232:235]\n" \
"v_max_f32_e32 v62, v62, v56\n" \
"v_add_f32_e32 v196, v196, v132\n" \
"v_mfma_f32_16x16x32_bf16 v[236:239], a[12:15], v[124:127], v[236:239]\n" \
"buffer_load_dwordx4 v4, s[84:87], s92 offen offset:0 lds\n" \
"v_rcp_f32_e32 v56, v56\n" \
"v_mfma_f32_16x16x32_bf16 v[240:243], a[16:19], v[124:127], v[240:243]\n" \
"v_add_f32_e32 v197, v197, v133\n" \
"v_add_f32_e32 v198, v198, v134\n" \
"v_add_f32_e32 v199, v199, v135\n" \
"v_mul_f32_e32 v56, v56, v60\n" \
"v_mul_f32_e32 v192, v56, v192\n" \
"v_mul_f32_e32 v193, v56, v193\n" \
"v_mul_f32_e32 v194, v56, v194\n" \
"v_mul_f32_e32 v195, v56, v195\n" \
"v_mul_f32_e32 v196, v56, v196\n" \
"v_mul_f32_e32 v197, v56, v197\n" \
"v_mul_f32_e32 v198, v56, v198\n" \
"v_mul_f32_e32 v199, v56, v199\n" \
"v_add_f32_e32 v200, v200, v136\n" \
"v_add_f32_e32 v201, v201, v137\n" \
"v_add_f32_e32 v202, v202, v138\n" \
"v_add_f32_e32 v203, v203, v139\n" \
"v_add_f32_e32 v204, v204, v140\n" \
"v_add_f32_e32 v205, v205, v141\n" \
"v_add_f32_e32 v206, v206, v142\n" \
"v_add_f32_e32 v207, v207, v143\n" \
"v_cvt_pk_bf16_f32 v64, v192, v193\n" \
"v_cvt_pk_bf16_f32 v65, v194, v195\n" \
"v_cvt_pk_bf16_f32 v66, v196, v197\n" \
"v_cvt_pk_bf16_f32 v67, v198, v199\n" \
"buffer_store_dwordx4 v[64:67], v20, s[56:59], s70 offen offset:0 sc1\n" \
"v_mfma_f32_16x16x32_bf16 v[244:247], a[20:23], v[124:127], v[244:247]\n" \
"v_mul_f32_e32 v200, v56, v200\n" \
"v_mul_f32_e32 v201, v56, v201\n" \
"v_mul_f32_e32 v202, v56, v202\n" \
"v_mul_f32_e32 v203, v56, v203\n" \
"v_mul_f32_e32 v204, v56, v204\n" \
"v_mul_f32_e32 v205, v56, v205\n" \
"v_mul_f32_e32 v206, v56, v206\n" \
"v_mul_f32_e32 v207, v56, v207\n" \
"v_add_f32_e32 v208, v208, v144\n" \
"v_add_f32_e32 v209, v209, v145\n" \
"v_add_f32_e32 v210, v210, v146\n" \
"v_add_f32_e32 v211, v211, v147\n" \
"v_add_f32_e32 v212, v212, v148\n" \
"v_add_f32_e32 v213, v213, v149\n" \
"v_add_f32_e32 v214, v214, v150\n" \
"v_add_f32_e32 v215, v215, v151\n" \
"v_cvt_pk_bf16_f32 v68, v200, v201\n" \
"v_cvt_pk_bf16_f32 v69, v202, v203\n" \
"v_cvt_pk_bf16_f32 v70, v204, v205\n" \
"v_cvt_pk_bf16_f32 v71, v206, v207\n" \
"buffer_store_dwordx4 v[68:71], v20, s[56:59], s70 offen offset:1024 sc1\n" \
"v_mul_f32_e32 v208, v56, v208\n" \
"v_mul_f32_e32 v209, v56, v209\n" \
"v_mul_f32_e32 v210, v56, v210\n" \
"v_mul_f32_e32 v211, v56, v211\n" \
"v_mul_f32_e32 v212, v56, v212\n" \
"v_mul_f32_e32 v213, v56, v213\n" \
"v_mul_f32_e32 v214, v56, v214\n" \
"v_mul_f32_e32 v215, v56, v215\n" \
"v_add_f32_e32 v216, v216, v152\n" \
"v_add_f32_e32 v217, v217, v153\n" \
"v_add_f32_e32 v218, v218, v154\n" \
"v_add_f32_e32 v219, v219, v155\n" \
"v_add_f32_e32 v220, v220, v156\n" \
"v_add_f32_e32 v221, v221, v157\n" \
"v_add_f32_e32 v222, v222, v158\n" \
"v_add_f32_e32 v223, v223, v159\n" \
"v_cvt_pk_bf16_f32 v72, v208, v209\n" \
"v_cvt_pk_bf16_f32 v73, v210, v211\n" \
"v_cvt_pk_bf16_f32 v74, v212, v213\n" \
"v_cvt_pk_bf16_f32 v75, v214, v215\n" \
"buffer_store_dwordx4 v[72:75], v20, s[56:59], s70 offen offset:2048 sc1\n" \
"v_mfma_f32_16x16x32_bf16 v[248:251], a[24:27], v[124:127], v[248:251]\n" \
"v_mul_f32_e32 v216, v56, v216\n" \
"v_mul_f32_e32 v217, v56, v217\n" \
"v_mul_f32_e32 v218, v56, v218\n" \
"v_mul_f32_e32 v219, v56, v219\n" \
"v_mul_f32_e32 v220, v56, v220\n" \
"v_mul_f32_e32 v221, v56, v221\n" \
"v_mul_f32_e32 v222, v56, v222\n" \
"v_mul_f32_e32 v223, v56, v223\n" \
"v_cvt_pk_bf16_f32 v76, v216, v217\n" \
"v_cvt_pk_bf16_f32 v77, v218, v219\n" \
"v_cvt_pk_bf16_f32 v78, v220, v221\n" \
"v_cvt_pk_bf16_f32 v79, v222, v223\n" \
"buffer_store_dwordx4 v[76:79], v20, s[56:59], s70 offen offset:3072 sc1\n" \
"v_add_f32_e32 v224, v224, v160\n" \
"v_add_f32_e32 v225, v225, v161\n" \
"v_add_f32_e32 v226, v226, v162\n" \
"v_add_f32_e32 v227, v227, v163\n" \
"v_add_f32_e32 v228, v228, v164\n" \
"v_add_f32_e32 v229, v229, v165\n" \
"v_add_f32_e32 v230, v230, v166\n" \
"v_add_f32_e32 v231, v231, v167\n" \
"v_add_f32_e32 v232, v232, v168\n" \
"v_add_f32_e32 v233, v233, v169\n" \
"v_add_f32_e32 v234, v234, v170\n" \
"v_add_f32_e32 v235, v235, v171\n" \
"v_add_f32_e32 v236, v236, v172\n" \
"v_add_f32_e32 v237, v237, v173\n" \
"v_add_f32_e32 v238, v238, v174\n" \
"v_add_f32_e32 v239, v239, v175\n" \
"v_mfma_f32_16x16x32_bf16 v[252:255], a[28:31], v[124:127], v[252:255]\n" \
"buffer_load_dwordx4 v5, s[84:87], s92 offen offset:1024 lds\n" \
"v_add_f32_e32 v240, v240, v176\n" \
"v_add_f32_e32 v241, v241, v177\n" \
"v_add_f32_e32 v242, v242, v178\n" \
"v_add_f32_e32 v243, v243, v179\n" \
"v_add_f32_e32 v244, v244, v180\n" \
"v_add_f32_e32 v245, v245, v181\n" \
"v_add_f32_e32 v246, v246, v182\n" \
"v_add_f32_e32 v247, v247, v183\n" \
"v_add_f32_e32 v248, v248, v184\n" \
"v_add_f32_e32 v249, v249, v185\n" \
"v_add_f32_e32 v250, v250, v186\n" \
"v_add_f32_e32 v251, v251, v187\n" \
"v_add_f32_e32 v252, v252, v188\n" \
"v_add_f32_e32 v253, v253, v189\n" \
"v_add_f32_e32 v254, v254, v190\n" \
"v_add_f32_e32 v255, v255, v191\n" \
"v_mfma_f32_16x16x32_bf16 a[204:207], a[252:255], v[124:127], a[204:207]\n" \
"buffer_load_dwordx4 v6, s[84:87], s92 offen offset:2048 lds\n" \
"v_mfma_f32_16x16x32_bf16 a[208:211], a[216:219], v[64:67], a[208:211]\n" \
"v_mfma_f32_16x16x32_bf16 a[208:211], a[216:219], v[68:71], a[208:211]\n" \
"v_mfma_f32_16x16x32_bf16 a[208:211], a[216:219], v[72:75], a[208:211]\n" \
"buffer_load_dwordx4 v7, s[84:87], s92 offen offset:3072 lds\n" \
"v_mfma_f32_16x16x32_bf16 a[208:211], a[216:219], v[76:79], a[208:211]\n" \
"s_add_u32 m0, s93, 0x2000\n" \
"s_nop 0\n" \
"buffer_load_dwordx4 v8, s[84:87], s92 offen offset:0 lds\n" \
"v_accvgpr_read_b32 v57, a204\n" \
"v_accvgpr_read_b32 v23, a205\n" \
"v_add_f32_e32 v57, v57, v23\n" \
"v_add_f32_e32 v57, v57, v43\n" \
"v_max_f32_e32 v62, v62, v57\n" \
"v_rcp_f32_e32 v57, v57\n" \
"v_cmp_lt_f32_e64 s[72:73], s100, v62\n" \
"v_mul_f32_e32 v57, v57, v61\n" \
"v_mul_f32_e32 v224, v57, v224\n" \
"v_mul_f32_e32 v225, v57, v225\n" \
"v_mul_f32_e32 v226, v57, v226\n" \
"v_mul_f32_e32 v227, v57, v227\n" \
"v_mul_f32_e32 v228, v57, v228\n" \
"v_mul_f32_e32 v229, v57, v229\n" \
"v_mul_f32_e32 v230, v57, v230\n" \
"v_mul_f32_e32 v231, v57, v231\n" \
"v_cvt_pk_bf16_f32 v80, v224, v225\n" \
"v_cvt_pk_bf16_f32 v81, v226, v227\n" \
"v_cvt_pk_bf16_f32 v82, v228, v229\n" \
"v_cvt_pk_bf16_f32 v83, v230, v231\n" \
"buffer_store_dwordx4 v[80:83], v20, s[56:59], s71 offen offset:0 sc1\n" \
"v_mul_f32_e32 v232, v57, v232\n" \
"v_mfma_f32_16x16x32_bf16 a[208:211], a[216:219], v[80:83], a[208:211]\n" \
"v_mul_f32_e32 v233, v57, v233\n" \
"v_mul_f32_e32 v234, v57, v234\n" \
"v_mul_f32_e32 v235, v57, v235\n" \
"v_mul_f32_e32 v236, v57, v236\n" \
"v_mul_f32_e32 v237, v57, v237\n" \
"v_mul_f32_e32 v238, v57, v238\n" \
"v_mul_f32_e32 v239, v57, v239\n" \
"v_cvt_pk_bf16_f32 v84, v232, v233\n" \
"v_cvt_pk_bf16_f32 v85, v234, v235\n" \
"v_cvt_pk_bf16_f32 v86, v236, v237\n" \
"v_cvt_pk_bf16_f32 v87, v238, v239\n" \
"buffer_store_dwordx4 v[84:87], v20, s[56:59], s71 offen offset:1024 sc1\n" \
"v_mul_f32_e32 v240, v57, v240\n" \
"v_mfma_f32_16x16x32_bf16 a[208:211], a[216:219], v[84:87], a[208:211]\n" \
"v_mul_f32_e32 v241, v57, v241\n" \
"v_mul_f32_e32 v242, v57, v242\n" \
"v_mul_f32_e32 v243, v57, v243\n" \
"v_mul_f32_e32 v244, v57, v244\n" \
"v_mul_f32_e32 v245, v57, v245\n" \
"v_mul_f32_e32 v246, v57, v246\n" \
"v_mul_f32_e32 v247, v57, v247\n" \
"v_cvt_pk_bf16_f32 v96, v240, v241\n" \
"v_cvt_pk_bf16_f32 v97, v242, v243\n" \
"v_cvt_pk_bf16_f32 v98, v244, v245\n" \
"v_cvt_pk_bf16_f32 v99, v246, v247\n" \
"buffer_store_dwordx4 v[96:99], v20, s[56:59], s71 offen offset:2048 sc1\n" \
"v_mul_f32_e32 v248, v57, v248\n" \
"v_mfma_f32_16x16x32_bf16 a[208:211], a[216:219], v[96:99], a[208:211]\n" \
"v_mul_f32_e32 v249, v57, v249\n" \
"v_mul_f32_e32 v250, v57, v250\n" \
"v_mul_f32_e32 v251, v57, v251\n" \
"v_mul_f32_e32 v252, v57, v252\n" \
"v_mul_f32_e32 v253, v57, v253\n" \
"v_mul_f32_e32 v254, v57, v254\n" \
"v_mul_f32_e32 v255, v57, v255\n" \
"v_cvt_pk_bf16_f32 v100, v248, v249\n" \
"v_cvt_pk_bf16_f32 v101, v250, v251\n" \
"v_cvt_pk_bf16_f32 v102, v252, v253\n" \
"v_cvt_pk_bf16_f32 v103, v254, v255\n" \
"buffer_store_dwordx4 v[100:103], v20, s[56:59], s71 offen offset:3072 sc1\n" \
"s_nop 0\n" \
"v_mfma_f32_16x16x32_bf16 a[208:211], a[216:219], v[100:103], a[208:211]\n" \
"s_nop 7\n" \
"v_accvgpr_read_b32 v63, a208\n" \
"v_cmp_u_f32_e32 vcc, v63, v63\n" \
"s_nop 0\n" \
"s_or_b64 vcc, s[72:73], vcc\n" \
"s_cbranch_scc0 L_out_%=\n" \
"s_branch L_end_%=\n" \
"L_h1_%=:\n" \
"L_pf14:\n" \
"s_cbranch_execz L_pf18\n" \
"s_nop 0\n" \
"s_cmp_gt_i32 s63, 0\n" \
"s_cbranch_scc1 L_hc1_%=\n" \
"s_cmp_lt_i32 s63, 0\n" \
"s_cbranch_scc1 L_hn1_%=\n" \
"s_mov_b32 exec_hi, 0xf\n" \
"buffer_load_dword v22, v16, s[80:83], 0 offen\n" \
"s_mov_b32 exec_hi, -1\n" \
"s_branch L_hr1_%=\n" \
"L_hn1_%=:\n" \
"buffer_load_dword v22, v17, s[56:59], 0 offen sc1\n" \
"s_branch L_hr1_%=\n" \
"L_hs1_%=:\n" \
"s_sleep 1\n" \
"buffer_load_dword v22, v17, s[56:59], 0 offen sc1\n" \
"s_add_u32 s101, s101, 1\n" \
"s_waitcnt vmcnt(0)\n" \
"s_cmp_gt_u32 s101, 0x100000\n" \
"s_cbranch_scc1 L_hr1_%=\n" \
"L_hc1_%=:\n" \
"v_cmp_ne_u32_e32 vcc, s62, v22\n" \
"s_nop 4\n" \
"s_cbranch_vccnz L_hs1_%=\n" \
"s_branch L_hr1_%=\n" \
"L_h2_%=:\n" \
"s_cmp_gt_i32 s63, 0\n" \
"s_cbranch_scc1 L_hc2_%=\n" \
"s_cmp_lt_i32 s63, 0\n" \
"s_cbranch_scc1 L_hn2_%=\n" \
"s_mov_b32 exec_hi, 0xf\n" \
"buffer_load_dword v22, v16, s[80:83], 0 offen\n" \
"s_mov_b32 exec_hi, -1\n" \
"s_branch L_hr2_%=\n" \
"L_hn2_%=:\n" \
"buffer_load_dword v22, v17, s[56:59], 0 offen sc1\n" \
"s_branch L_hr2_%=\n" \
"L_hs2_%=:\n" \
"s_sleep 1\n" \
"buffer_load_dword v22, v17, s[56:59], 0 offen sc1\n" \
"s_add_u32 s101, s101, 1\n" \
"s_waitcnt vmcnt(0)\n" \
"s_cmp_gt_u32 s101, 0x100000\n" \
"s_cbranch_scc1 L_hr2_%=\n" \
"L_hc2_%=:\n" \
"v_cmp_ne_u32_e32 vcc, s62, v22\n" \
"s_nop 4\n" \
"s_cbranch_vccnz L_hs2_%=\n" \
"s_branch L_hr2_%=\n" \
"L_sa_%=:\n" \
"s_sleep 1\n" \
"buffer_load_dword v37, v38, s[56:59], s64 offen sc1\n" \
"s_add_u32 s101, s101, 1\n" \
"s_waitcnt vmcnt(0)\n" \
"v_cmp_ne_u32_e32 vcc, s62, v37\n" \
"s_cmp_gt_u32 s101, 0x100000\n" \
"s_cbranch_scc1 L_ra_%=\n" \
"s_nop 2\n" \
"s_cbranch_vccnz L_sa_%=\n" \
"s_branch L_ra_%=\n" \
"L_sb_%=:\n" \
"s_sleep 1\n" \
"buffer_load_dword v47, v38, s[56:59], s71 offen sc1\n" \
"s_add_u32 s101, s101, 1\n" \
"s_waitcnt vmcnt(0)\n" \
"v_cmp_ne_u32_e32 vcc, s62, v47\n" \
"s_cmp_gt_u32 s101, 0x100000\n" \
"s_cbranch_scc1 L_rb_%=\n" \
"s_nop 2\n" \
"s_cbranch_vccnz L_sb_%=\n" \
"s_branch L_rb_%=\n" \
"L_sp0_%=:\n" \
"s_sleep 1\n" \
"L_sx0_%=:\n" \
"L_pf15:\n" \
"s_cbranch_execz L_pf19\n" \
"s_nop 0\n" \
"buffer_load_dword v37, v40, s[56:59], s74 offen sc1\n" \
"s_add_u32 s101, s101, 1\n" \
"s_waitcnt vmcnt(0)\n" \
"v_cmp_eq_u32_e32 vcc, s62, v37\n" \
"s_nop 1\n" \
"s_and_b32 s98, vcc_lo, 0xfef\n" \
"s_andn2_b32 s99, s100, vcc_lo\n" \
"s_cselect_b32 s99, 0, 16\n" \
"s_or_b32 s98, s98, s99\n" \
"s_andn2_b32 s98, s98, s84\n" \
"s_cbranch_scc0 L_sn0_%=\n" \
"s_or_b32 s84, s84, s98\n" \
"s_bitcmp1_b32 s98, 0\n" \
"s_cbranch_scc0 L_xs00_%=\n" \
"buffer_load_dwordx4 v[116:119], v20, s[56:59], s63 offen offset:0 sc1\n" \
"L_xs00_%=:\n" \
"s_bitcmp1_b32 s98, 1\n" \
"s_cbranch_scc0 L_xs01_%=\n" \
"buffer_load_dwordx4 v[124:127], v20, s[56:59], s63 offen offset:2048 sc1\n" \
"L_xs01_%=:\n" \
"s_bitcmp1_b32 s98, 2\n" \
"s_cbranch_scc0 L_xs02_%=\n" \
"buffer_load_dwordx4 v[132:135], v38, s[56:59], s63 offen offset:0 sc1\n" \
"L_xs02_%=:\n" \
"s_bitcmp1_b32 s98, 3\n" \
"s_cbranch_scc0 L_xs03_%=\n" \
"buffer_load_dwordx4 v[140:143], v38, s[56:59], s63 offen offset:2048 sc1\n" \
"L_xs03_%=:\n" \
"s_bitcmp1_b32 s98, 4\n" \
"s_cbranch_scc0 L_xs04_%=\n" \
"buffer_load_dwordx4 v[176:179], v20, s[56:59], s75 offen offset:0 sc1\n" \
"buffer_load_dwordx4 v[248:251], v20, s[56:59], s75 offen offset:2048 sc1\n" \
"buffer_load_dwordx4 v[180:183], v38, s[56:59], s75 offen offset:0 sc1\n" \
"buffer_load_dwordx4 v[252:255], v38, s[56:59], s75 offen offset:2048 sc1\n" \
"buffer_load_dwordx4 v[112:115], v47, s[56:59], s75 offen offset:0 sc1\n" \
"buffer_load_dwordx4 v[120:123], v47, s[56:59], s75 offen offset:2048 sc1\n" \
"buffer_load_dwordx4 v[128:131], v54, s[56:59], s75 offen offset:0 sc1\n" \
"buffer_load_dwordx4 v[136:139], v54, s[56:59], s75 offen offset:2048 sc1\n" \
"L_xs04_%=:\n" \
"s_bitcmp1_b32 s98, 5\n" \
"s_cbranch_scc0 L_xs05_%=\n" \
"buffer_load_dwordx4 v[148:151], v47, s[56:59], s63 offen offset:0 sc1\n" \
"buffer_load_dwordx4 v[220:223], v47, s[56:59], s63 offen offset:1024 sc1\n" \
"L_xs05_%=:\n" \
"s_bitcmp1_b32 s98, 6\n" \
"s_cbranch_scc0 L_xs06_%=\n" \
"buffer_load_dwordx4 v[156:159], v47, s[56:59], s63 offen offset:2048 sc1\n" \
"buffer_load_dwordx4 v[228:231], v47, s[56:59], s63 offen offset:3072 sc1\n" \
"L_xs06_%=:\n" \
"s_bitcmp1_b32 s98, 7\n" \
"s_cbranch_scc0 L_xs07_%=\n" \
"buffer_load_dwordx4 v[164:167], v54, s[56:59], s63 offen offset:0 sc1\n" \
"buffer_load_dwordx4 v[236:239], v54, s[56:59], s63 offen offset:1024 sc1\n" \
"L_xs07_%=:\n" \
"s_bitcmp1_b32 s98, 8\n" \
"s_cbranch_scc0 L_xs08_%=\n" \
"buffer_load_dwordx4 v[188:191], v20, s[56:59], s63 offen offset:1024 sc1\n" \
"L_xs08_%=:\n" \
"s_bitcmp1_b32 s98, 9\n" \
"s_cbranch_scc0 L_xs09_%=\n" \
"buffer_load_dwordx4 v[196:199], v20, s[56:59], s63 offen offset:3072 sc1\n" \
"L_xs09_%=:\n" \
"s_bitcmp1_b32 s98, 10\n" \
"s_cbranch_scc0 L_xs010_%=\n" \
"buffer_load_dwordx4 v[204:207], v38, s[56:59], s63 offen offset:1024 sc1\n" \
"L_xs010_%=:\n" \
"s_bitcmp1_b32 s98, 11\n" \
"s_cbranch_scc0 L_xs011_%=\n" \
"buffer_load_dwordx4 v[212:215], v38, s[56:59], s63 offen offset:3072 sc1\n" \
"L_xs011_%=:\n" \
"L_sn0_%=:\n" \
"s_cmp_gt_u32 s101, 0x100000\n" \
"s_cbranch_scc1 L_se0_%=\n" \
"s_andn2_b32 s99, 0xff, s84\n" \
"s_cbranch_scc1 L_sp0_%=\n" \
"L_se0_%=:\n" \
"s_waitcnt vmcnt(0)\n" \
"s_branch L_rx0_%=\n" \
"L_sp1_%=:\n" \
"s_sleep 1\n" \
"L_sx1_%=:\n" \
"buffer_load_dword v37, v40, s[56:59], s74 offen sc1\n" \
"s_add_u32 s101, s101, 1\n" \
"s_waitcnt vmcnt(0)\n" \
"v_cmp_eq_u32_e32 vcc, s62, v37\n" \
"s_nop 1\n" \
"s_and_b32 s98, vcc_lo, 0xf00\n" \
"s_andn2_b32 s98, s98, s84\n" \
"s_cbranch_scc0 L_sn1_%=\n" \
"s_or_b32 s84, s84, s98\n" \
"s_bitcmp1_b32 s98, 8\n" \
"s_cbranch_scc0 L_xs18_%=\n" \
"buffer_load_dwordx4 v[188:191], v20, s[56:59], s63 offen offset:1024 sc1\n" \
"L_xs18_%=:\n" \
"s_bitcmp1_b32 s98, 9\n" \
"s_cbranch_scc0 L_xs19_%=\n" \
"buffer_load_dwordx4 v[196:199], v20, s[56:59], s63 offen offset:3072 sc1\n" \
"L_xs19_%=:\n" \
"s_bitcmp1_b32 s98, 10\n" \
"s_cbranch_scc0 L_xs110_%=\n" \
"buffer_load_dwordx4 v[204:207], v38, s[56:59], s63 offen offset:1024 sc1\n" \
"L_xs110_%=:\n" \
"s_bitcmp1_b32 s98, 11\n" \
"s_cbranch_scc0 L_xs111_%=\n" \
"buffer_load_dwordx4 v[212:215], v38, s[56:59], s63 offen offset:3072 sc1\n" \
"L_xs111_%=:\n" \
"L_sn1_%=:\n" \
"s_cmp_gt_u32 s101, 0x100000\n" \
"s_cbranch_scc1 L_se1_%=\n" \
"s_andn2_b32 s99, 0xf00, s84\n" \
"s_cbranch_scc1 L_sp1_%=\n" \
"L_se1_%=:\n" \
"s_waitcnt vmcnt(0)\n" \
"s_branch L_rx1_%=\n" \
"L_att_stub:\n" \
"s_mov_b32 s101, 0\n" \
"v_add_u32_e32 v32, 0x49000, v20\n" \
"v_readlane_b32 s96, v11, 32\n" \
"v_readlane_b32 s97, v11, 33\n" \
"buffer_load_dwordx4 a[220:223], v20, s[56:59], s60 offen offset:0 sc1\n" \
"buffer_load_dwordx4 a[236:239], v32, s[56:59], s60 offen offset:0 sc1\n" \
"buffer_load_dwordx4 a[224:227], v20, s[56:59], s60 offen offset:1024 sc1\n" \
"buffer_load_dwordx4 a[240:243], v32, s[56:59], s60 offen offset:1024 sc1\n" \
"buffer_load_dwordx4 a[228:231], v20, s[56:59], s60 offen offset:2048 sc1\n" \
"buffer_load_dwordx4 a[244:247], v32, s[56:59], s60 offen offset:2048 sc1\n" \
"buffer_load_dwordx4 a[232:235], v20, s[56:59], s60 offen offset:3072 sc1\n" \
"buffer_load_dwordx4 a[248:251], v32, s[56:59], s60 offen offset:3072 sc1\n" \
"buffer_load_dwordx4 a[32:35], v20, s[56:59], s96 offen offset:0 sc1\n" \
"buffer_load_dwordx4 a[40:43], v32, s[56:59], s96 offen offset:0 sc1\n" \
"buffer_load_dwordx4 a[48:51], v20, s[56:59], s97 offen offset:0 sc1\n" \
"buffer_load_dwordx4 a[56:59], v32, s[56:59], s97 offen offset:0 sc1\n" \
"buffer_load_dwordx4 a[36:39], v20, s[56:59], s96 offen offset:1024 sc1\n" \
"buffer_load_dwordx4 a[44:47], v32, s[56:59], s96 offen offset:1024 sc1\n" \
"buffer_load_dwordx4 a[52:55], v20, s[56:59], s97 offen offset:1024 sc1\n" \
"buffer_load_dwordx4 a[60:63], v32, s[56:59], s97 offen offset:1024 sc1\n" \
"buffer_load_dwordx4 v[64:67], v20, s[56:59], s61 offen offset:0 sc1\n" \
"buffer_load_dwordx4 v[68:71], v20, s[56:59], s61 offen offset:1024 sc1\n" \
"buffer_load_dwordx4 v[72:75], v20, s[56:59], s61 offen offset:2048 sc1\n" \
"buffer_load_dwordx4 v[76:79], v20, s[56:59], s61 offen offset:3072 sc1\n" \
"buffer_load_dwordx4 v[80:83], v32, s[56:59], s61 offen offset:0 sc1\n" \
"buffer_load_dwordx4 v[84:87], v32, s[56:59], s61 offen offset:1024 sc1\n" \
"buffer_load_dwordx4 v[96:99], v32, s[56:59], s61 offen offset:2048 sc1\n" \
"buffer_load_dwordx4 v[100:103], v32, s[56:59], s61 offen offset:3072 sc1\n" \
"s_waitcnt vmcnt(0)\n" \
"s_branch L_out_%=\n" \
"L_sw_%=:\n" \
"L_pf16:\n" \
"s_cbranch_execz L_pf20\n" \
"s_nop 0\n" \
"v_mfma_f32_16x16x32_bf16 v[72:75], a[128:131], a[16:19], v[36:39]\n" \
"s_add_u32 s68, s60, 0xfff6e000\n" \
"s_add_u32 s69, s60, 0xfffb7000\n" \
"s_add_u32 s70, s60, 0x0\n" \
"s_add_u32 s71, s60, 0x49000\n" \
"v_mfma_f32_16x16x32_bf16 v[72:75], a[132:135], a[20:23], v[72:75]\n" \
"s_xor_b32 m0, s65, 0x12000\n" \
"ds_read_b128 a[160:163], v49 offset:0\n" \
"v_mfma_f32_16x16x32_bf16 v[72:75], a[136:139], a[24:27], v[72:75]\n" \
"buffer_load_dwordx4 v50, s[8:11], s64 offen offset:0 sc1 lds\n" \
"v_mfma_f32_16x16x32_bf16 v[72:75], a[140:143], a[28:31], v[72:75]\n" \
"ds_read_b128 a[164:167], v49 offset:1024\n" \
"ds_read_b128 a[168:171], v49 offset:2048\n" \
"v_mfma_f32_16x16x32_bf16 v[76:79], a[156:159], a[28:31], v[36:39]\n" \
"ds_read_b128 a[172:175], v49 offset:3072\n" \
"v_cvt_pk_bf16_f32 v120, v88, v89\n" \
"v_mfma_f32_16x16x32_bf16 v[76:79], a[152:155], a[24:27], v[76:79]\n" \
"buffer_load_dwordx4 v50, s[8:11], s64 offen offset:1024 sc1 lds\n" \
"v_mfma_f32_16x16x32_bf16 v[76:79], a[148:151], a[20:23], v[76:79]\n" \
"ds_read_b128 a[176:179], v49 offset:4096\n" \
"v_cvt_pk_bf16_f32 v121, v90, v91\n" \
"v_mfma_f32_16x16x32_bf16 v[76:79], a[144:147], a[16:19], v[76:79]\n" \
"ds_read_b128 a[180:183], v49 offset:5120\n" \
"s_mov_b32 s65, s13\n" \
"v_mfma_f32_16x16x32_bf16 v[80:83], a[128:131], a[32:35], v[40:43]\n" \
"v_add_u32_e32 v63, s15, v20\n" \
"s_add_u32 s67, s14, 40960\n" \
"v_mfma_f32_16x16x32_bf16 v[80:83], a[132:135], a[36:39], v[80:83]\n" \
"v_add_u32_e32 v50, 0x6000, v63\n" \
"s_xor_b32 m0, s65, 0x8000\n" \
"v_mfma_f32_16x16x32_bf16 v[80:83], a[136:139], a[40:43], v[80:83]\n" \
"v_exp_f32_e32 v92, v92\n" \
"v_mfma_f32_16x16x32_bf16 v[80:83], a[140:143], a[44:47], v[80:83]\n" \
"buffer_load_dwordx4 v50, s[8:11], s67 offen offset:0 sc1 lds\n" \
"v_mfma_f32_16x16x32_bf16 v[84:87], a[156:159], a[44:47], v[40:43]\n" \
"v_exp_f32_e32 v93, v93\n" \
"v_mfma_f32_16x16x32_bf16 v[84:87], a[152:155], a[40:43], v[84:87]\n" \
"v_exp_f32_e32 v94, v94\n" \
"v_mfma_f32_16x16x32_bf16 v[84:87], a[148:151], a[36:39], v[84:87]\n" \
"v_exp_f32_e32 v95, v95\n" \
"v_mfma_f32_16x16x32_bf16 v[84:87], a[144:147], a[32:35], v[84:87]\n" \
"v_cvt_pk_bf16_f32 v122, v92, v93\n" \
"v_cvt_pk_bf16_f32 v123, v94, v95\n" \
"v_mfma_f32_16x16x32_bf16 v[88:91], a[128:131], a[48:51], v[44:47]\n" \
"ds_read_b128 a[184:187], v49 offset:6144\n" \
"ds_read_b128 a[188:191], v49 offset:7168\n" \
"v_mfma_f32_16x16x32_bf16 v[88:91], a[132:135], a[52:55], v[88:91]\n" \
"v_and_b32_e32 v96, v12, v96\n" \
"v_and_b32_e32 v97, v13, v97\n" \
"v_mfma_f32_16x16x32_bf16 v[88:91], a[136:139], a[56:59], v[88:91]\n" \
"v_and_b32_e32 v98, v14, v98\n" \
"v_and_b32_e32 v99, v15, v99\n" \
"v_mfma_f32_16x16x32_bf16 v[88:91], a[140:143], a[60:63], v[88:91]\n" \
"buffer_load_dwordx4 v50, s[8:11], s67 offen offset:1024 sc1 lds\n" \
"v_mfma_f32_16x16x32_bf16 v[92:95], a[156:159], a[60:63], v[44:47]\n" \
"v_exp_f32_e32 v72, v72\n" \
"v_mfma_f32_16x16x32_bf16 v[92:95], a[152:155], a[56:59], v[92:95]\n" \
"v_exp_f32_e32 v73, v73\n" \
"v_mfma_f32_16x16x32_bf16 v[92:95], a[148:151], a[52:55], v[92:95]\n" \
"v_exp_f32_e32 v74, v74\n" \
"v_mfma_f32_16x16x32_bf16 v[92:95], a[144:147], a[48:51], v[92:95]\n" \
"v_cvt_pk_bf16_f32 v108, v72, v73\n" \
"s_waitcnt lgkmcnt(2)\n" \
"v_mfma_f32_16x16x32_bf16 v[128:131], a[160:163], v[96:99], v[128:131]\n" \
"v_exp_f32_e32 v75, v75\n" \
"v_mfma_f32_16x16x32_bf16 v[132:135], a[164:167], v[96:99], v[132:135]\n" \
"v_exp_f32_e32 v76, v76\n" \
"v_mfma_f32_16x16x32_bf16 v[136:139], a[168:171], v[96:99], v[136:139]\n" \
"v_exp_f32_e32 v77, v77\n" \
"v_mfma_f32_16x16x32_bf16 v[140:143], a[172:175], v[96:99], v[140:143]\n" \
"v_exp_f32_e32 v78, v78\n" \
"v_mfma_f32_16x16x32_bf16 a[192:195], a[252:255], v[96:99], a[192:195]\n" \
"v_exp_f32_e32 v79, v79\n" \
"v_mfma_f32_16x16x32_bf16 v[160:163], a[160:163], v[104:107], v[160:163]\n" \
"v_cvt_pk_bf16_f32 v109, v74, v75\n" \
"v_cvt_pk_bf16_f32 v110, v76, v77\n" \
"v_cvt_pk_bf16_f32 v111, v78, v79\n" \
"v_and_b32_e32 v108, v12, v108\n" \
"v_and_b32_e32 v109, v13, v109\n" \
"v_and_b32_e32 v110, v14, v110\n" \
"v_and_b32_e32 v111, v15, v111\n" \
"v_exp_f32_e32 v80, v80\n" \
"v_exp_f32_e32 v81, v81\n" \
"v_mfma_f32_16x16x32_bf16 v[160:163], a[176:179], v[108:111], v[160:163]\n" \
"buffer_load_dwordx4 v50, s[8:11], s67 offen offset:2048 sc1 lds\n" \
"v_mfma_f32_16x16x32_bf16 v[164:167], a[180:183], v[108:111], v[164:167]\n" \
"buffer_load_dwordx4 v50, s[8:11], s67 offen offset:3072 sc1 lds\n" \
"v_mfma_f32_16x16x32_bf16 v[164:167], a[164:167], v[104:107], v[164:167]\n" \
"v_exp_f32_e32 v82, v82\n" \
"v_mfma_f32_16x16x32_bf16 v[168:171], a[168:171], v[104:107], v[168:171]\n" \
"v_cvt_pk_bf16_f32 v116, v80, v81\n" \
"s_waitcnt lgkmcnt(0)\n" \
"v_mfma_f32_16x16x32_bf16 v[168:171], a[184:187], v[108:111], v[168:171]\n" \
"v_exp_f32_e32 v83, v83\n" \
"v_mfma_f32_16x16x32_bf16 v[172:175], a[188:191], v[108:111], v[172:175]\n" \
"v_exp_f32_e32 v84, v84\n" \
"v_mfma_f32_16x16x32_bf16 v[172:175], a[172:175], v[104:107], v[172:175]\n" \
"v_exp_f32_e32 v85, v85\n" \
"v_smfmac_f32_16x16x64_bf16 a[196:199], a[212:215], v[104:111], v21\n" \
"v_mfma_f32_16x16x32_bf16 v[192:195], a[160:163], v[112:115], v[192:195]\n" \
"v_exp_f32_e32 v86, v86\n" \
"v_exp_f32_e32 v87, v87\n" \
"v_cvt_pk_bf16_f32 v117, v82, v83\n" \
"v_cvt_pk_bf16_f32 v118, v84, v85\n" \
"v_cvt_pk_bf16_f32 v119, v86, v87\n" \
"s_mov_b32 s64, s14\n" \
"v_add_u32_e32 v51, 0x12000, v63\n" \
"v_mfma_f32_16x16x32_bf16 v[192:195], a[176:179], v[116:119], v[192:195]\n" \
"s_xor_b32 m0, s65, 0xa000\n" \
"ds_read_b32 v58, v18 offset:512\n" \
"v_mfma_f32_16x16x32_bf16 v[196:199], a[180:183], v[116:119], v[196:199]\n" \
"buffer_load_dwordx4 v51, s[8:11], s64 offen offset:0 sc1 lds\n" \
"v_mfma_f32_16x16x32_bf16 v[196:199], a[164:167], v[112:115], v[196:199]\n" \
"v_exp_f32_e32 v88, v88\n" \
"v_mfma_f32_16x16x32_bf16 v[200:203], a[168:171], v[112:115], v[200:203]\n" \
"v_exp_f32_e32 v89, v89\n" \
"v_mfma_f32_16x16x32_bf16 v[200:203], a[184:187], v[116:119], v[200:203]\n" \
"v_exp_f32_e32 v90, v90\n" \
"v_mfma_f32_16x16x32_bf16 v[204:207], a[188:191], v[116:119], v[204:207]\n" \
"v_exp_f32_e32 v91, v91\n" \
"v_mfma_f32_16x16x32_bf16 v[204:207], a[172:175], v[112:115], v[204:207]\n" \
"v_exp_f32_e32 v92, v92\n" \
"v_smfmac_f32_16x16x64_bf16 a[200:203], a[212:215], v[112:119], v21\n" \
"v_mfma_f32_16x16x32_bf16 v[224:227], a[160:163], v[120:123], v[224:227]\n" \
"v_exp_f32_e32 v93, v93\n" \
"v_exp_f32_e32 v94, v94\n" \
"v_exp_f32_e32 v95, v95\n" \
"v_cvt_pk_bf16_f32 v124, v88, v89\n" \
"v_cvt_pk_bf16_f32 v125, v90, v91\n" \
"v_cvt_pk_bf16_f32 v126, v92, v93\n" \
"v_cvt_pk_bf16_f32 v127, v94, v95\n" \
"v_add_u32_e32 v52, 0x14000, v63\n" \
"v_add_u32_e32 v53, 0x20000, v63\n" \
"v_mfma_f32_16x16x32_bf16 v[224:227], a[176:179], v[124:127], v[224:227]\n" \
"buffer_load_dwordx4 v51, s[8:11], s64 offen offset:1024 sc1 lds\n" \
"v_mfma_f32_16x16x32_bf16 v[228:231], a[180:183], v[124:127], v[228:231]\n" \
"ds_read_b32 v59, v18 offset:640\n" \
"ds_read_b32 v60, v18 offset:768\n" \
"v_mfma_f32_16x16x32_bf16 v[228:231], a[164:167], v[120:123], v[228:231]\n" \
"ds_read_b32 v61, v18 offset:896\n" \
"ds_read_b128 a[128:131], v48 offset:0\n" \
"v_mfma_f32_16x16x32_bf16 v[232:235], a[168:171], v[120:123], v[232:235]\n" \
"ds_read_b128 a[132:135], v48 offset:1024\n" \
"ds_read_b128 a[136:139], v48 offset:2048\n" \
"v_mfma_f32_16x16x32_bf16 v[232:235], a[184:187], v[124:127], v[232:235]\n" \
"ds_read_b128 a[140:143], v48 offset:3072\n" \
"ds_read_b128 a[144:147], v48 offset:4096\n" \
"ds_read_b128 a[148:151], v48 offset:5120\n" \
"ds_read_b128 a[152:155], v48 offset:6144\n" \
"ds_read_b128 a[156:159], v48 offset:7168\n" \
"v_mfma_f32_16x16x32_bf16 v[236:239], a[188:191], v[124:127], v[236:239]\n" \
"buffer_load_dwordx4 v51, s[8:11], s64 offen offset:2048 sc1 lds\n" \
"v_mfma_f32_16x16x32_bf16 v[236:239], a[172:175], v[120:123], v[236:239]\n" \
"buffer_load_dwordx4 v51, s[8:11], s64 offen offset:3072 sc1 lds\n" \
"v_readlane_b32 s93, v11, 17\n" \
"v_readlane_b32 s92, v11, 22\n" \
"v_accvgpr_read_b32 v54, a192\n" \
"v_accvgpr_read_b32 v23, a193\n" \
"v_add_f32_e32 v54, v54, v23\n" \
"v_accvgpr_read_b32 v55, a196\n" \
"v_accvgpr_read_b32 v23, a197\n" \
"v_max_f32_e32 v62, v62, v54\n" \
"v_add_f32_e32 v55, v55, v23\n" \
"v_max_f32_e32 v62, v62, v55\n" \
"v_rcp_f32_e32 v54, v54\n" \
"v_rcp_f32_e32 v55, v55\n" \
"s_waitcnt lgkmcnt(11)\n" \
"v_mul_f32_e32 v54, v54, v58\n" \
"s_waitcnt lgkmcnt(10)\n" \
"v_mul_f32_e32 v55, v55, v59\n" \
"v_mul_f32_e32 v128, v54, v128\n" \
"v_mul_f32_e32 v129, v54, v129\n" \
"v_mul_f32_e32 v136, v54, v136\n" \
"v_mul_f32_e32 v137, v54, v137\n" \
"v_mul_f32_e32 v160, v55, v160\n" \
"v_mul_f32_e32 v161, v55, v161\n" \
"v_mul_f32_e32 v168, v55, v168\n" \
"v_mul_f32_e32 v169, v55, v169\n" \
"v_cvt_pk_bf16_f32 v128, v128, v129\n" \
"v_mul_f32_e32 v130, v54, v130\n" \
"v_mul_f32_e32 v131, v54, v131\n" \
"v_cvt_pk_bf16_f32 v136, v136, v137\n" \
"v_mul_f32_e32 v138, v54, v138\n" \
"v_mul_f32_e32 v139, v54, v139\n" \
"s_waitcnt vmcnt(10) lgkmcnt(0)\n" \
"s_barrier\n" \
"v_mfma_f32_16x16x32_bf16 v[80:83], a[128:131], a[32:35], v[40:43]\n" \
"ds_read_b128 a[160:163], v49 offset:8192\n" \
"ds_read_b128 a[164:167], v49 offset:9216\n" \
"v_mfma_f32_16x16x32_bf16 v[80:83], a[132:135], a[36:39], v[80:83]\n" \
"ds_read_b128 a[168:171], v49 offset:10240\n" \
"ds_read_b128 a[172:175], v49 offset:11264\n" \
"v_mfma_f32_16x16x32_bf16 v[80:83], a[136:139], a[40:43], v[80:83]\n" \
"s_xor_b32 m0, s65, 0x18000\n" \
"ds_read_b128 a[176:179], v49 offset:12288\n" \
"v_mfma_f32_16x16x32_bf16 v[80:83], a[140:143], a[44:47], v[80:83]\n" \
"buffer_load_dwordx4 v52, s[8:11], s64 offen offset:0 sc1 lds\n" \
"v_mfma_f32_16x16x32_bf16 v[84:87], a[156:159], a[44:47], v[40:43]\n" \
"ds_read_b128 a[180:183], v49 offset:13312\n" \
"ds_read_b128 a[184:187], v49 offset:14336\n" \
"v_mfma_f32_16x16x32_bf16 v[84:87], a[152:155], a[40:43], v[84:87]\n" \
"buffer_load_dwordx4 v52, s[8:11], s64 offen offset:1024 sc1 lds\n" \
"v_mfma_f32_16x16x32_bf16 v[84:87], a[148:151], a[36:39], v[84:87]\n" \
"ds_read_b128 a[188:191], v49 offset:15360\n" \
"v_add_u32_e32 v49, 0x10000, v48\n" \
"v_mfma_f32_16x16x32_bf16 v[84:87], a[144:147], a[32:35], v[84:87]\n" \
"buffer_load_dwordx4 v52, s[8:11], s64 offen offset:2048 sc1 lds\n" \
"v_mfma_f32_16x16x32_bf16 v[88:91], a[128:131], a[48:51], v[44:47]\n" \
"ds_read_b128 a[128:131], v48 offset:8192\n" \
"v_cvt_pk_bf16_f32 v160, v160, v161\n" \
"v_mfma_f32_16x16x32_bf16 v[88:91], a[132:135], a[52:55], v[88:91]\n" \
"ds_read_b128 a[132:135], v48 offset:9216\n" \
"v_mul_f32_e32 v162, v55, v162\n" \
"v_mfma_f32_16x16x32_bf16 v[88:91], a[136:139], a[56:59], v[88:91]\n" \
"buffer_load_dwordx4 v52, s[8:11], s64 offen offset:3072 sc1 lds\n" \
"v_mfma_f32_16x16x32_bf16 v[88:91], a[140:143], a[60:63], v[88:91]\n" \
"s_mov_b32 m0, s65\n" \
"ds_read_b128 a[136:139], v48 offset:10240\n" \
"v_mfma_f32_16x16x32_bf16 v[92:95], a[156:159], a[60:63], v[44:47]\n" \
"ds_read_b128 a[140:143], v48 offset:11264\n" \
"ds_read_b128 a[156:159], v48 offset:15360\n" \
"v_mfma_f32_16x16x32_bf16 v[92:95], a[152:155], a[56:59], v[92:95]\n" \
"buffer_load_dwordx4 v53, s[8:11], s64 offen offset:0 sc1 lds\n" \
"v_mfma_f32_16x16x32_bf16 v[92:95], a[148:151], a[52:55], v[92:95]\n" \
"ds_read_b128 a[148:151], v48 offset:13312\n" \
"ds_read_b128 a[152:155], v48 offset:14336\n" \
"v_mfma_f32_16x16x32_bf16 v[92:95], a[144:147], a[48:51], v[92:95]\n" \
"ds_read_b128 a[144:147], v48 offset:12288\n" \
"s_waitcnt lgkmcnt(12)\n" \
"v_mfma_f32_16x16x32_bf16 v[144:147], a[160:163], v[96:99], v[144:147]\n" \
"buffer_load_dwordx4 v53, s[8:11], s64 offen offset:1024 sc1 lds\n" \
"v_mfma_f32_16x16x32_bf16 v[148:151], a[164:167], v[96:99], v[148:151]\n" \
"v_exp_f32_e32 v88, v88\n" \
"v_mfma_f32_16x16x32_bf16 v[152:155], a[168:171], v[96:99], v[152:155]\n" \
"v_exp_f32_e32 v89, v89\n" \
"v_mfma_f32_16x16x32_bf16 v[156:159], a[172:175], v[96:99], v[156:159]\n" \
"v_exp_f32_e32 v90, v90\n" \
"v_mfma_f32_16x16x32_bf16 v[176:179], a[160:163], v[104:107], v[176:179]\n" \
"s_xor_b32 s65, s65, 0x8000\n" \
"s_waitcnt lgkmcnt(8)\n" \
"v_mfma_f32_16x16x32_bf16 v[176:179], a[176:179], v[108:111], v[176:179]\n" \
"buffer_load_dwordx4 v53, s[8:11], s64 offen offset:2048 sc1 lds\n" \
"v_mfma_f32_16x16x32_bf16 v[180:183], a[180:183], v[108:111], v[180:183]\n" \
"v_exp_f32_e32 v91, v91\n" \
"v_mfma_f32_16x16x32_bf16 v[180:183], a[164:167], v[104:107], v[180:183]\n" \
"buffer_load_dwordx4 v53, s[8:11], s64 offen offset:3072 sc1 lds\n" \
"L_pf17:\n" \
"s_cbranch_execz L_retA\n" \
"s_nop 0\n" \
"v_mfma_f32_16x16x32_bf16 v[184:187], a[168:171], v[104:107], v[184:187]\n" \
"s_add_u32 m0, s93, 0x0\n" \
"s_add_u32 s64, s64, 0x10000\n" \
"v_mfma_f32_16x16x32_bf16 v[184:187], a[184:187], v[108:111], v[184:187]\n" \
"v_exp_f32_e32 v92, v92\n" \
"v_mfma_f32_16x16x32_bf16 v[188:191], a[188:191], v[108:111], v[188:191]\n" \
"buffer_load_dwordx4 v0, s[80:83], s92 offen offset:0 lds\n" \
"v_mfma_f32_16x16x32_bf16 v[188:191], a[172:175], v[104:107], v[188:191]\n" \
"v_exp_f32_e32 v93, v93\n" \
"v_mfma_f32_16x16x32_bf16 v[208:211], a[160:163], v[112:115], v[208:211]\n" \
"v_exp_f32_e32 v94, v94\n" \
"v_mfma_f32_16x16x32_bf16 v[208:211], a[176:179], v[116:119], v[208:211]\n" \
"v_exp_f32_e32 v95, v95\n" \
"v_mfma_f32_16x16x32_bf16 v[212:215], a[180:183], v[116:119], v[212:215]\n" \
"buffer_load_dwordx4 v1, s[80:83], s92 offen offset:1024 lds\n" \
"v_mfma_f32_16x16x32_bf16 v[212:215], a[164:167], v[112:115], v[212:215]\n" \
"v_exp_f32_e32 v80, v80\n" \
"v_mfma_f32_16x16x32_bf16 v[216:219], a[168:171], v[112:115], v[216:219]\n" \
"v_exp_f32_e32 v81, v81\n" \
"v_mfma_f32_16x16x32_bf16 v[216:219], a[184:187], v[116:119], v[216:219]\n" \
"buffer_load_dwordx4 v2, s[80:83], s92 offen offset:2048 lds\n" \
"v_mfma_f32_16x16x32_bf16 v[220:223], a[188:191], v[116:119], v[220:223]\n" \
"v_exp_f32_e32 v82, v82\n" \
"v_mfma_f32_16x16x32_bf16 v[220:223], a[172:175], v[112:115], v[220:223]\n" \
"v_exp_f32_e32 v83, v83\n" \
"v_mfma_f32_16x16x32_bf16 v[240:243], a[160:163], v[120:123], v[240:243]\n" \
"buffer_load_dwordx4 v3, s[80:83], s92 offen offset:3072 lds\n" \
"v_mfma_f32_16x16x32_bf16 v[240:243], a[176:179], v[124:127], v[240:243]\n" \
"v_exp_f32_e32 v84, v84\n" \
"v_mfma_f32_16x16x32_bf16 v[244:247], a[180:183], v[124:127], v[244:247]\n" \
"v_exp_f32_e32 v85, v85\n" \
"v_mfma_f32_16x16x32_bf16 v[244:247], a[164:167], v[120:123], v[244:247]\n" \
"v_exp_f32_e32 v86, v86\n" \
"v_mfma_f32_16x16x32_bf16 v[248:251], a[168:171], v[120:123], v[248:251]\n" \
"v_exp_f32_e32 v87, v87\n" \
"v_mfma_f32_16x16x32_bf16 v[248:251], a[184:187], v[124:127], v[248:251]\n" \
"v_cvt_pk_bf16_f32 v112, v80, v81\n" \
"v_cvt_pk_bf16_f32 v113, v82, v83\n" \
"v_mfma_f32_16x16x32_bf16 v[252:255], a[188:191], v[124:127], v[252:255]\n" \
"v_cvt_pk_bf16_f32 v114, v84, v85\n" \
"v_cvt_pk_bf16_f32 v115, v86, v87\n" \
"v_mfma_f32_16x16x32_bf16 v[252:255], a[172:175], v[120:123], v[252:255]\n" \
"v_and_b32_e32 v112, v12, v112\n" \
"v_and_b32_e32 v113, v13, v113\n" \
"v_smfmac_f32_16x16x64_bf16 a[204:207], a[212:215], v[120:127], v21\n" \
"v_cvt_pk_bf16_f32 v120, v88, v89\n" \
"v_cvt_pk_bf16_f32 v121, v90, v91\n" \
"v_cvt_pk_bf16_f32 v122, v92, v93\n" \
"v_cvt_pk_bf16_f32 v123, v94, v95\n" \
"v_and_b32_e32 v114, v14, v114\n" \
"v_and_b32_e32 v115, v15, v115\n" \
"v_add_u32_e32 v48, s16, v20\n" \
"v_mul_f32_e32 v144, v54, v144\n" \
"v_mul_f32_e32 v145, v54, v145\n" \
"v_mul_f32_e32 v152, v54, v152\n" \
"v_mul_f32_e32 v153, v54, v153\n" \
"v_mul_f32_e32 v176, v55, v176\n" \
"v_mul_f32_e32 v177, v55, v177\n" \
"v_mul_f32_e32 v184, v55, v184\n" \
"v_mul_f32_e32 v185, v55, v185\n" \
"v_mul_f32_e32 v163, v55, v163\n" \
"v_cvt_pk_bf16_f32 v168, v168, v169\n" \
"v_mul_f32_e32 v170, v55, v170\n" \
"v_mul_f32_e32 v171, v55, v171\n" \
"v_cvt_pk_bf16_f32 v144, v144, v145\n" \
"v_mul_f32_e32 v146, v54, v146\n" \
"v_mul_f32_e32 v147, v54, v147\n" \
"v_cvt_pk_bf16_f32 v152, v152, v153\n" \
"v_mul_f32_e32 v154, v54, v154\n" \
"v_mul_f32_e32 v155, v54, v155\n" \
"v_cvt_pk_bf16_f32 v176, v176, v177\n" \
"v_mul_f32_e32 v178, v55, v178\n" \
"v_mul_f32_e32 v179, v55, v179\n" \
"v_cvt_pk_bf16_f32 v184, v184, v185\n" \
"v_mul_f32_e32 v186, v55, v186\n" \
"v_mul_f32_e32 v187, v55, v187\n" \
"v_cvt_pk_bf16_f32 v129, v130, v131\n" \
"v_mul_f32_e32 v132, v54, v132\n" \
"v_mul_f32_e32 v133, v54, v133\n" \
"v_mul_f32_e32 v134, v54, v134\n" \
"v_mul_f32_e32 v135, v54, v135\n" \
"v_cvt_pk_bf16_f32 v137, v138, v139\n" \
"v_mul_f32_e32 v140, v54, v140\n" \
"v_mul_f32_e32 v141, v54, v141\n" \
"v_mul_f32_e32 v142, v54, v142\n" \
"v_mul_f32_e32 v143, v54, v143\n" \
"v_cvt_pk_bf16_f32 v161, v162, v163\n" \
"v_mul_f32_e32 v164, v55, v164\n" \
"v_mul_f32_e32 v165, v55, v165\n" \
"v_mul_f32_e32 v166, v55, v166\n" \
"v_mul_f32_e32 v167, v55, v167\n" \
"v_cvt_pk_bf16_f32 v169, v170, v171\n" \
"v_mul_f32_e32 v172, v55, v172\n" \
"v_mul_f32_e32 v173, v55, v173\n" \
"v_mul_f32_e32 v174, v55, v174\n" \
"v_mul_f32_e32 v175, v55, v175\n" \
"v_cvt_pk_bf16_f32 v145, v146, v147\n" \
"v_mul_f32_e32 v148, v54, v148\n" \
"v_mul_f32_e32 v149, v54, v149\n" \
"v_mul_f32_e32 v150, v54, v150\n" \
"v_mul_f32_e32 v151, v54, v151\n" \
"v_cvt_pk_bf16_f32 v153, v154, v155\n" \
"v_mul_f32_e32 v156, v54, v156\n" \
"v_mul_f32_e32 v157, v54, v157\n" \
"v_mul_f32_e32 v158, v54, v158\n" \
"v_mul_f32_e32 v159, v54, v159\n" \
"v_cvt_pk_bf16_f32 v177, v178, v179\n" \
"v_mul_f32_e32 v180, v55, v180\n" \
"v_mul_f32_e32 v181, v55, v181\n" \
"v_mul_f32_e32 v182, v55, v182\n" \
"v_mul_f32_e32 v183, v55, v183\n" \
"v_cvt_pk_bf16_f32 v185, v186, v187\n" \
"v_mul_f32_e32 v188, v55, v188\n" \
"v_mul_f32_e32 v189, v55, v189\n" \
"v_mul_f32_e32 v190, v55, v190\n" \
"v_mul_f32_e32 v191, v55, v191\n" \
"v_cvt_pk_bf16_f32 v130, v132, v133\n" \
"v_cvt_pk_bf16_f32 v131, v134, v135\n" \
"buffer_store_dwordx4 v[128:131], v20, s[56:59], s68 offen offset:0 sc1\n" \
"v_cvt_pk_bf16_f32 v138, v140, v141\n" \
"v_mfma_f32_16x16x32_bf16 a[208:211], a[216:219], v[128:131], a[208:211]\n" \
"v_cvt_pk_bf16_f32 v139, v142, v143\n" \
"buffer_store_dwordx4 v[136:139], v20, s[56:59], s68 offen offset:1024 sc1\n" \
"s_waitcnt vmcnt(14) lgkmcnt(0)\n" \
"s_barrier\n" \
"v_mfma_f32_16x16x32_bf16 v[88:91], a[128:131], a[48:51], v[44:47]\n" \
"s_xor_b32 m0, s65, 0x12000\n" \
"ds_read_b128 a[160:163], v49 offset:0\n" \
"v_mfma_f32_16x16x32_bf16 v[88:91], a[132:135], a[52:55], v[88:91]\n" \
"buffer_load_dwordx4 v50, s[8:11], s64 offen offset:0 sc1 lds\n" \
"v_mfma_f32_16x16x32_bf16 v[88:91], a[136:139], a[56:59], v[88:91]\n" \
"ds_read_b128 a[164:167], v49 offset:1024\n" \
"ds_read_b128 a[168:171], v49 offset:2048\n" \
"v_mfma_f32_16x16x32_bf16 v[88:91], a[140:143], a[60:63], v[88:91]\n" \
"buffer_load_dwordx4 v50, s[8:11], s64 offen offset:1024 sc1 lds\n" \
"v_mfma_f32_16x16x32_bf16 v[92:95], a[156:159], a[60:63], v[44:47]\n" \
"ds_read_b128 a[172:175], v49 offset:3072\n" \
"ds_read_b128 a[176:179], v49 offset:8192\n" \
"v_mfma_f32_16x16x32_bf16 v[92:95], a[152:155], a[56:59], v[92:95]\n" \
"ds_read_b128 a[180:183], v49 offset:9216\n" \
"ds_read_b128 a[184:187], v49 offset:10240\n" \
"v_mfma_f32_16x16x32_bf16 v[92:95], a[148:151], a[52:55], v[92:95]\n" \
"ds_read_b128 a[188:191], v49 offset:11264\n" \
"ds_read_b128 a[0:3], v49 offset:4096\n" \
"v_mfma_f32_16x16x32_bf16 v[92:95], a[144:147], a[48:51], v[92:95]\n" \
"buffer_load_dwordx4 v50, s[8:11], s64 offen offset:2048 sc1 lds\n" \
"v_mfma_f32_16x16x32_bf16 v[32:35], a[220:223], a[64:67], 0\n" \
"ds_read_b128 a[4:7], v49 offset:5120\n" \
"ds_read_b128 a[8:11], v49 offset:6144\n" \
"v_mfma_f32_16x16x32_bf16 v[36:39], a[220:223], a[80:83], 0\n" \
"ds_read_b128 a[12:15], v49 offset:7168\n" \
"ds_read_b128 a[16:19], v49 offset:12288\n" \
"v_mfma_f32_16x16x32_bf16 v[40:43], a[220:223], a[96:99], 0\n" \
"ds_read_b128 a[20:23], v49 offset:13312\n" \
"ds_read_b128 a[24:27], v49 offset:14336\n" \
"v_mfma_f32_16x16x32_bf16 v[44:47], a[220:223], a[112:115], 0\n" \
"buffer_load_dwordx4 v50, s[8:11], s64 offen offset:3072 sc1 lds\n" \
"v_mfma_f32_16x16x32_bf16 v[32:35], a[224:227], a[68:71], v[32:35]\n" \
"s_xor_b32 m0, s65, 0xa000\n" \
"ds_read_b128 a[28:31], v49 offset:15360\n" \
"v_mfma_f32_16x16x32_bf16 v[36:39], a[224:227], a[84:87], v[36:39]\n" \
"v_exp_f32_e32 v88, v88\n" \
"v_mfma_f32_16x16x32_bf16 v[40:43], a[224:227], a[100:103], v[40:43]\n" \
"buffer_load_dwordx4 v51, s[8:11], s64 offen offset:0 sc1 lds\n" \
"v_mfma_f32_16x16x32_bf16 v[44:47], a[224:227], a[116:119], v[44:47]\n" \
"v_exp_f32_e32 v89, v89\n" \
"v_mfma_f32_16x16x32_bf16 v[32:35], a[228:231], a[72:75], v[32:35]\n" \
"v_exp_f32_e32 v90, v90\n" \
"v_mfma_f32_16x16x32_bf16 v[36:39], a[228:231], a[88:91], v[36:39]\n" \
"v_exp_f32_e32 v91, v91\n" \
"v_mfma_f32_16x16x32_bf16 v[40:43], a[228:231], a[104:107], v[40:43]\n" \
"buffer_load_dwordx4 v51, s[8:11], s64 offen offset:1024 sc1 lds\n" \
"v_mfma_f32_16x16x32_bf16 v[44:47], a[228:231], a[120:123], v[44:47]\n" \
"v_exp_f32_e32 v92, v92\n" \
"v_mfma_f32_16x16x32_bf16 v[32:35], a[232:235], a[76:79], v[32:35]\n" \
"v_exp_f32_e32 v93, v93\n" \
"v_mfma_f32_16x16x32_bf16 v[36:39], a[232:235], a[92:95], v[36:39]\n" \
"v_exp_f32_e32 v94, v94\n" \
"v_mfma_f32_16x16x32_bf16 v[40:43], a[232:235], a[108:111], v[40:43]\n" \
"buffer_load_dwordx4 v51, s[8:11], s64 offen offset:2048 sc1 lds\n" \
"v_mfma_f32_16x16x32_bf16 v[44:47], a[232:235], a[124:127], v[44:47]\n" \
"v_cvt_pk_bf16_f32 v124, v88, v89\n" \
"s_waitcnt lgkmcnt(3)\n" \
"v_mfma_f32_16x16x32_bf16 v[192:195], a[160:163], v[112:115], v[192:195]\n" \
"v_exp_f32_e32 v95, v95\n" \
"v_mfma_f32_16x16x32_bf16 v[196:199], a[164:167], v[112:115], v[196:199]\n" \
"buffer_load_dwordx4 v51, s[8:11], s64 offen offset:3072 sc1 lds\n" \
"v_mfma_f32_16x16x32_bf16 v[200:203], a[168:171], v[112:115], v[200:203]\n" \
"s_add_u32 m0, s93, 0x1000\n" \
"v_cvt_pk_bf16_f32 v125, v90, v91\n" \
"v_mfma_f32_16x16x32_bf16 v[204:207], a[172:175], v[112:115], v[204:207]\n" \
"v_cvt_pk_bf16_f32 v126, v92, v93\n" \
"v_cvt_pk_bf16_f32 v127, v94, v95\n" \
"v_mfma_f32_16x16x32_bf16 v[208:211], a[176:179], v[112:115], v[208:211]\n" \
"v_and_b32_e32 v124, v12, v124\n" \
"v_and_b32_e32 v125, v13, v125\n" \
"v_mfma_f32_16x16x32_bf16 v[212:215], a[180:183], v[112:115], v[212:215]\n" \
"buffer_load_dwordx4 v4, s[80:83], s92 offen offset:0 lds\n" \
"v_mfma_f32_16x16x32_bf16 v[216:219], a[184:187], v[112:115], v[216:219]\n" \
"v_and_b32_e32 v126, v14, v126\n" \
"v_and_b32_e32 v127, v15, v127\n" \
"v_mfma_f32_16x16x32_bf16 v[220:223], a[188:191], v[112:115], v[220:223]\n" \
"ds_read_b128 a[128:131], v48 offset:0\n" \
"ds_read_b128 a[132:135], v48 offset:1024\n" \
"v_mfma_f32_16x16x32_bf16 a[200:203], a[252:255], v[112:115], a[200:203]\n" \
"ds_read_b128 a[136:139], v48 offset:2048\n" \
"ds_read_b128 a[140:143], v48 offset:3072\n" \
"v_mfma_f32_16x16x32_bf16 v[224:227], a[160:163], v[120:123], v[224:227]\n" \
"buffer_load_dwordx4 v5, s[80:83], s92 offen offset:1024 lds\n" \
"v_mfma_f32_16x16x32_bf16 v[228:231], a[164:167], v[120:123], v[228:231]\n" \
"ds_read_b128 a[144:147], v48 offset:4096\n" \
"ds_read_b128 a[148:151], v48 offset:5120\n" \
"v_mfma_f32_16x16x32_bf16 v[232:235], a[168:171], v[120:123], v[232:235]\n" \
"ds_read_b128 a[152:155], v48 offset:6144\n" \
"ds_read_b128 a[156:159], v48 offset:7168\n" \
"v_mfma_f32_16x16x32_bf16 v[236:239], a[172:175], v[120:123], v[236:239]\n" \
"buffer_load_dwordx4 v6, s[80:83], s92 offen offset:2048 lds\n" \
"v_mfma_f32_16x16x32_bf16 v[240:243], a[176:179], v[120:123], v[240:243]\n" \
"v_cvt_pk_bf16_f32 v162, v164, v165\n" \
"v_cvt_pk_bf16_f32 v163, v166, v167\n" \
"buffer_store_dwordx4 v[160:163], v20, s[56:59], s69 offen offset:0 sc1\n" \
"v_mfma_f32_16x16x32_bf16 v[244:247], a[180:183], v[120:123], v[244:247]\n" \
"v_cvt_pk_bf16_f32 v170, v172, v173\n" \
"v_cvt_pk_bf16_f32 v171, v174, v175\n" \
"buffer_store_dwordx4 v[168:171], v20, s[56:59], s69 offen offset:1024 sc1\n" \
"v_mfma_f32_16x16x32_bf16 v[248:251], a[184:187], v[120:123], v[248:251]\n" \
"v_cvt_pk_bf16_f32 v146, v148, v149\n" \
"v_cvt_pk_bf16_f32 v147, v150, v151\n" \
"buffer_store_dwordx4 v[144:147], v20, s[56:59], s68 offen offset:2048 sc1\n" \
"v_mfma_f32_16x16x32_bf16 v[252:255], a[188:191], v[120:123], v[252:255]\n" \
"buffer_load_dwordx4 v7, s[80:83], s92 offen offset:3072 lds\n" \
"v_mfma_f32_16x16x32_bf16 a[204:207], a[252:255], v[120:123], a[204:207]\n" \
"v_cvt_pk_bf16_f32 v154, v156, v157\n" \
"v_cvt_pk_bf16_f32 v155, v158, v159\n" \
"buffer_store_dwordx4 v[152:155], v20, s[56:59], s68 offen offset:3072 sc1\n" \
"v_mfma_f32_16x16x32_bf16 v[224:227], a[0:3], v[124:127], v[224:227]\n" \
"v_cvt_pk_bf16_f32 v178, v180, v181\n" \
"v_cvt_pk_bf16_f32 v179, v182, v183\n" \
"buffer_store_dwordx4 v[176:179], v20, s[56:59], s69 offen offset:2048 sc1\n" \
"v_mfma_f32_16x16x32_bf16 v[228:231], a[4:7], v[124:127], v[228:231]\n" \
"v_cvt_pk_bf16_f32 v186, v188, v189\n" \
"v_cvt_pk_bf16_f32 v187, v190, v191\n" \
"buffer_store_dwordx4 v[184:187], v20, s[56:59], s69 offen offset:3072 sc1\n" \
"v_mfma_f32_16x16x32_bf16 v[232:235], a[8:11], v[124:127], v[232:235]\n" \
"s_add_u32 m0, s93, 0x2000\n" \
"v_accvgpr_read_b32 v56, a200\n" \
"v_mfma_f32_16x16x32_bf16 v[236:239], a[12:15], v[124:127], v[236:239]\n" \
"buffer_load_dwordx4 v8, s[80:83], s92 offen offset:0 lds\n" \
"v_mfma_f32_16x16x32_bf16 v[240:243], a[16:19], v[124:127], v[240:243]\n" \
"v_accvgpr_read_b32 v23, a201\n" \
"s_waitcnt lgkmcnt(0)\n" \
"v_mfma_f32_16x16x32_bf16 v[244:247], a[20:23], v[124:127], v[244:247]\n" \
"v_add_f32_e32 v56, v56, v23\n" \
"v_max_f32_e32 v62, v62, v56\n" \
"v_mfma_f32_16x16x32_bf16 v[248:251], a[24:27], v[124:127], v[248:251]\n" \
"v_rcp_f32_e32 v56, v56\n" \
"v_mfma_f32_16x16x32_bf16 v[252:255], a[28:31], v[124:127], v[252:255]\n" \
"v_mul_f32_e32 v56, v56, v60\n" \
"v_mul_f32_e32 v192, v56, v192\n" \
"v_mfma_f32_16x16x32_bf16 a[204:207], a[252:255], v[124:127], a[204:207]\n" \
"v_mul_f32_e32 v193, v56, v193\n" \
"v_cvt_pk_bf16_f32 v192, v192, v193\n" \
"v_mfma_f32_16x16x32_bf16 a[208:211], a[216:219], v[136:139], a[208:211]\n" \
"v_mul_f32_e32 v194, v56, v194\n" \
"v_mul_f32_e32 v195, v56, v195\n" \
"v_mfma_f32_16x16x32_bf16 a[208:211], a[216:219], v[160:163], a[208:211]\n" \
"v_cvt_pk_bf16_f32 v193, v194, v195\n" \
"v_mul_f32_e32 v196, v56, v196\n" \
"v_mfma_f32_16x16x32_bf16 a[208:211], a[216:219], v[168:171], a[208:211]\n" \
"v_mul_f32_e32 v197, v56, v197\n" \
"v_mul_f32_e32 v198, v56, v198\n" \
"v_mfma_f32_16x16x32_bf16 a[208:211], a[216:219], v[144:147], a[208:211]\n" \
"v_mul_f32_e32 v199, v56, v199\n" \
"v_mul_f32_e32 v200, v56, v200\n" \
"v_mfma_f32_16x16x32_bf16 a[208:211], a[216:219], v[152:155], a[208:211]\n" \
"v_mul_f32_e32 v201, v56, v201\n" \
"v_accvgpr_read_b32 v57, a204\n" \
"v_mfma_f32_16x16x32_bf16 a[208:211], a[216:219], v[176:179], a[208:211]\n" \
"v_accvgpr_read_b32 v23, a205\n" \
"v_cvt_pk_bf16_f32 v194, v196, v197\n" \
"v_mfma_f32_16x16x32_bf16 a[208:211], a[216:219], v[184:187], a[208:211]\n" \
"v_cvt_pk_bf16_f32 v195, v198, v199\n" \
"buffer_store_dwordx4 v[192:195], v20, s[56:59], s70 offen offset:0 sc1\n" \
"v_mfma_f32_32x32x16_bf16 v[128:143], a[216:219], a[216:219], 0\n" \
"v_cvt_pk_bf16_f32 v200, v200, v201\n" \
"v_mul_f32_e32 v202, v56, v202\n" \
"v_mul_f32_e32 v203, v56, v203\n" \
"v_add_f32_e32 v57, v57, v23\n" \
"v_cvt_pk_bf16_f32 v201, v202, v203\n" \
"v_mul_f32_e32 v204, v56, v204\n" \
"v_mfma_f32_16x16x32_bf16 a[208:211], a[216:219], v[192:195], a[208:211]\n" \
"v_mul_f32_e32 v205, v56, v205\n" \
"v_mul_f32_e32 v206, v56, v206\n" \
"v_mfma_f32_32x32x16_bf16 v[144:159], a[216:219], a[216:219], 0\n" \
"v_mul_f32_e32 v207, v56, v207\n" \
"v_mul_f32_e32 v208, v56, v208\n" \
"v_mul_f32_e32 v209, v56, v209\n" \
"v_max_f32_e32 v62, v62, v57\n" \
"v_cvt_pk_bf16_f32 v202, v204, v205\n" \
"v_cvt_pk_bf16_f32 v203, v206, v207\n" \
"buffer_store_dwordx4 v[200:203], v20, s[56:59], s70 offen offset:1024 sc1\n" \
"v_mfma_f32_32x32x16_bf16 v[160:175], a[216:219], a[216:219], 0\n" \
"v_cvt_pk_bf16_f32 v208, v208, v209\n" \
"v_mul_f32_e32 v210, v56, v210\n" \
"v_mul_f32_e32 v211, v56, v211\n" \
"v_rcp_f32_e32 v57, v57\n" \
"v_cvt_pk_bf16_f32 v209, v210, v211\n" \
"v_mfma_f32_16x16x32_bf16 a[208:211], a[216:219], v[200:203], a[208:211]\n" \
"v_mul_f32_e32 v212, v56, v212\n" \
"v_mul_f32_e32 v213, v56, v213\n" \
"v_mfma_f32_32x32x16_bf16 v[176:191], a[216:219], a[216:219], 0\n" \
"v_mul_f32_e32 v214, v56, v214\n" \
"v_mul_f32_e32 v215, v56, v215\n" \
"v_mul_f32_e32 v216, v56, v216\n" \
"v_mul_f32_e32 v217, v56, v217\n" \
"v_cvt_pk_bf16_f32 v210, v212, v213\n" \
"v_cvt_pk_bf16_f32 v211, v214, v215\n" \
"s_waitcnt vmcnt(21)\n" \
"s_barrier\n" \
"buffer_store_dwordx4 v[208:211], v20, s[56:59], s70 offen offset:2048 sc1\n" \
"v_mfma_f32_16x16x32_bf16 v[64:67], a[128:131], a[64:67], v[32:35]\n" \
"s_xor_b32 m0, s65, 0x18000\n" \
"v_cvt_pk_bf16_f32 v216, v216, v217\n" \
"v_mfma_f32_16x16x32_bf16 v[64:67], a[132:135], a[68:71], v[64:67]\n" \
"buffer_load_dwordx4 v52, s[8:11], s64 offen offset:0 sc1 lds\n" \
"v_mfma_f32_16x16x32_bf16 v[64:67], a[136:139], a[72:75], v[64:67]\n" \
"v_mul_f32_e32 v218, v56, v218\n" \
"v_mul_f32_e32 v219, v56, v219\n" \
"v_mfma_f32_16x16x32_bf16 v[64:67], a[140:143], a[76:79], v[64:67]\n" \
"buffer_load_dwordx4 v52, s[8:11], s64 offen offset:1024 sc1 lds\n" \
"v_mfma_f32_16x16x32_bf16 v[68:71], a[156:159], a[76:79], v[32:35]\n" \
"v_mul_f32_e32 v57, v57, v61\n" \
"v_cvt_pk_bf16_f32 v217, v218, v219\n" \
"v_mfma_f32_16x16x32_bf16 v[68:71], a[152:155], a[72:75], v[68:71]\n" \
"v_mul_f32_e32 v220, v56, v220\n" \
"v_mul_f32_e32 v221, v56, v221\n" \
"v_mfma_f32_16x16x32_bf16 v[68:71], a[148:151], a[68:71], v[68:71]\n" \
"v_mul_f32_e32 v222, v56, v222\n" \
"v_mul_f32_e32 v223, v56, v223\n" \
"v_mfma_f32_16x16x32_bf16 v[68:71], a[144:147], a[64:67], v[68:71]\n" \
"buffer_load_dwordx4 v52, s[8:11], s64 offen offset:2048 sc1 lds\n" \
"v_mfma_f32_16x16x32_bf16 v[72:75], a[128:131], a[80:83], v[36:39]\n" \
"v_mul_f32_e32 v224, v57, v224\n" \
"v_mul_f32_e32 v225, v57, v225\n" \
"v_mfma_f32_16x16x32_bf16 v[72:75], a[132:135], a[84:87], v[72:75]\n" \
"v_cvt_pk_bf16_f32 v218, v220, v221\n" \
"v_cvt_pk_bf16_f32 v219, v222, v223\n" \
"buffer_store_dwordx4 v[216:219], v20, s[56:59], s70 offen offset:3072 sc1\n" \
"v_mfma_f32_16x16x32_bf16 v[72:75], a[136:139], a[88:91], v[72:75]\n" \
"v_cvt_pk_bf16_f32 v224, v224, v225\n" \
"v_mul_f32_e32 v226, v57, v226\n" \
"v_mfma_f32_16x16x32_bf16 v[72:75], a[140:143], a[92:95], v[72:75]\n" \
"buffer_load_dwordx4 v52, s[8:11], s64 offen offset:3072 sc1 lds\n" \
"v_mfma_f32_16x16x32_bf16 v[76:79], a[156:159], a[92:95], v[36:39]\n" \
"s_mov_b32 m0, s65\n" \
"v_mul_f32_e32 v227, v57, v227\n" \
"v_mfma_f32_16x16x32_bf16 v[76:79], a[152:155], a[88:91], v[76:79]\n" \
"v_cvt_pk_bf16_f32 v225, v226, v227\n" \
"v_mul_f32_e32 v228, v57, v228\n" \
"v_mfma_f32_16x16x32_bf16 v[76:79], a[148:151], a[84:87], v[76:79]\n" \
"v_mul_f32_e32 v229, v57, v229\n" \
"v_mul_f32_e32 v230, v57, v230\n" \
"v_mfma_f32_16x16x32_bf16 v[76:79], a[144:147], a[80:83], v[76:79]\n" \
"buffer_load_dwordx4 v53, s[8:11], s64 offen offset:0 sc1 lds\n" \
"v_mfma_f32_16x16x32_bf16 v[80:83], a[128:131], a[96:99], v[40:43]\n" \
"v_mul_f32_e32 v231, v57, v231\n" \
"v_mul_f32_e32 v232, v57, v232\n" \
"v_mfma_f32_16x16x32_bf16 v[80:83], a[132:135], a[100:103], v[80:83]\n" \
"v_mul_f32_e32 v233, v57, v233\n" \
"v_cvt_pk_bf16_f32 v226, v228, v229\n" \
"v_mfma_f32_16x16x32_bf16 v[80:83], a[136:139], a[104:107], v[80:83]\n" \
"buffer_load_dwordx4 v53, s[8:11], s64 offen offset:1024 sc1 lds\n" \
"v_mfma_f32_16x16x32_bf16 v[80:83], a[140:143], a[108:111], v[80:83]\n" \
"v_cvt_pk_bf16_f32 v227, v230, v231\n" \
"v_cvt_pk_bf16_f32 v232, v232, v233\n" \
"buffer_store_dwordx4 v[224:227], v20, s[56:59], s71 offen offset:0 sc1\n" \
"v_mfma_f32_16x16x32_bf16 v[84:87], a[156:159], a[108:111], v[40:43]\n" \
"v_mul_f32_e32 v234, v57, v234\n" \
"v_mul_f32_e32 v235, v57, v235\n" \
"v_mfma_f32_16x16x32_bf16 v[84:87], a[152:155], a[104:107], v[84:87]\n" \
"v_cvt_pk_bf16_f32 v233, v234, v235\n" \
"v_mul_f32_e32 v236, v57, v236\n" \
"v_mfma_f32_16x16x32_bf16 v[84:87], a[148:151], a[100:103], v[84:87]\n" \
"buffer_load_dwordx4 v53, s[8:11], s64 offen offset:2048 sc1 lds\n" \
"v_mfma_f32_16x16x32_bf16 v[84:87], a[144:147], a[96:99], v[84:87]\n" \
"v_mul_f32_e32 v237, v57, v237\n" \
"v_mul_f32_e32 v238, v57, v238\n" \
"v_mfma_f32_16x16x32_bf16 v[88:91], a[128:131], a[112:115], v[44:47]\n" \
"ds_read_b128 a[128:131], v48 offset:8192\n" \
"v_mul_f32_e32 v239, v57, v239\n" \
"v_mfma_f32_16x16x32_bf16 v[88:91], a[132:135], a[116:119], v[88:91]\n" \
"ds_read_b128 a[132:135], v48 offset:9216\n" \
"v_mul_f32_e32 v240, v57, v240\n" \
"v_mfma_f32_16x16x32_bf16 v[88:91], a[136:139], a[120:123], v[88:91]\n" \
"ds_read_b128 a[136:139], v48 offset:10240\n" \
"v_mul_f32_e32 v241, v57, v241\n" \
"v_mfma_f32_16x16x32_bf16 v[88:91], a[140:143], a[124:127], v[88:91]\n" \
"ds_read_b128 a[140:143], v48 offset:11264\n" \
"v_cvt_pk_bf16_f32 v234, v236, v237\n" \
"v_mfma_f32_16x16x32_bf16 v[92:95], a[156:159], a[124:127], v[44:47]\n" \
"ds_read_b128 a[156:159], v48 offset:15360\n" \
"v_cvt_pk_bf16_f32 v235, v238, v239\n" \
"buffer_store_dwordx4 v[232:235], v20, s[56:59], s71 offen offset:1024 sc1\n" \
"v_mfma_f32_16x16x32_bf16 v[92:95], a[152:155], a[120:123], v[92:95]\n" \
"ds_read_b128 a[152:155], v48 offset:14336\n" \
"v_cvt_pk_bf16_f32 v240, v240, v241\n" \
"v_mfma_f32_16x16x32_bf16 v[92:95], a[148:151], a[116:119], v[92:95]\n" \
"ds_read_b128 a[148:151], v48 offset:13312\n" \
"v_mul_f32_e32 v242, v57, v242\n" \
"v_mfma_f32_16x16x32_bf16 v[92:95], a[144:147], a[112:115], v[92:95]\n" \
"ds_read_b128 a[144:147], v48 offset:12288\n" \
"v_mul_f32_e32 v243, v57, v243\n" \
"v_mfma_f32_16x16x32_bf16 a[208:211], a[216:219], v[208:211], a[208:211]\n" \
"v_cvt_pk_bf16_f32 v241, v242, v243\n" \
"v_mul_f32_e32 v244, v57, v244\n" \
"v_mfma_f32_16x16x32_bf16 a[208:211], a[216:219], v[216:219], a[208:211]\n" \
"v_mul_f32_e32 v245, v57, v245\n" \
"v_mul_f32_e32 v246, v57, v246\n" \
"v_mfma_f32_16x16x32_bf16 a[208:211], a[216:219], v[224:227], a[208:211]\n" \
"v_mul_f32_e32 v247, v57, v247\n" \
"v_mul_f32_e32 v248, v57, v248\n" \
"v_mfma_f32_16x16x32_bf16 a[208:211], a[216:219], v[232:235], a[208:211]\n" \
"v_mul_f32_e32 v249, v57, v249\n" \
"v_cvt_pk_bf16_f32 v242, v244, v245\n" \
"v_mfma_f32_32x32x16_bf16 v[192:207], a[216:219], a[216:219], 0\n" \
"v_cvt_pk_bf16_f32 v243, v246, v247\n" \
"buffer_store_dwordx4 v[240:243], v20, s[56:59], s71 offen offset:2048 sc1\n" \
"v_cvt_pk_bf16_f32 v248, v248, v249\n" \
"v_mfma_f32_16x16x32_bf16 a[208:211], a[216:219], v[240:243], a[208:211]\n" \
"v_mul_f32_e32 v250, v57, v250\n" \
"v_mul_f32_e32 v251, v57, v251\n" \
"v_mfma_f32_32x32x16_bf16 v[208:223], a[216:219], a[216:219], 0\n" \
"v_cvt_pk_bf16_f32 v249, v250, v251\n" \
"v_mul_f32_e32 v252, v57, v252\n" \
"v_mul_f32_e32 v253, v57, v253\n" \
"v_mul_f32_e32 v254, v57, v254\n" \
"v_mul_f32_e32 v255, v57, v255\n" \
"v_cvt_pk_bf16_f32 v250, v252, v253\n" \
"v_mfma_f32_32x32x16_bf16 v[224:239], a[216:219], a[216:219], 0\n" \
"v_cvt_pk_bf16_f32 v251, v254, v255\n" \
"buffer_store_dwordx4 v[248:251], v20, s[56:59], s71 offen offset:3072 sc1\n" \
"buffer_load_dwordx4 v53, s[8:11], s64 offen offset:3072 sc1 lds\n" \
"v_mfma_f32_16x16x32_bf16 a[208:211], a[216:219], v[248:251], a[208:211]\n" \
"v_cmp_lt_f32_e64 s[72:73], s100, v62\n" \
"v_readlane_b32 s95, v11, 2\n" \
"v_mfma_f32_32x32x16_bf16 v[240:255], a[216:219], a[216:219], 0\n" \
"v_exp_f32_e32 v64, v64\n" \
"v_exp_f32_e32 v65, v65\n" \
"v_exp_f32_e32 v66, v66\n" \
"v_mfma_f32_32x32x16_bf16 a[192:207], a[216:219], a[216:219], 0\n" \
"v_exp_f32_e32 v67, v67\n" \
"v_accvgpr_read_b32 v63, a208\n" \
"v_cmp_u_f32_e32 vcc, v63, v63\n" \
"v_exp_f32_e32 v68, v68\n" \
"s_or_b64 vcc, s[72:73], vcc\n" \
"s_cselect_b32 s95, 0x40000000, s95\n" \
"s_cselect_b32 s94, 0x7f800000, 0\n" \
"v_exp_f32_e32 v69, v69\n" \
"v_exp_f32_e32 v70, v70\n" \
"v_exp_f32_e32 v71, v71\n" \
"v_exp_f32_e32 v72, v72\n" \
"v_exp_f32_e32 v73, v73\n" \
"v_exp_f32_e32 v74, v74\n" \
"v_exp_f32_e32 v75, v75\n" \
"v_exp_f32_e32 v76, v76\n" \
"v_exp_f32_e32 v77, v77\n" \
"v_exp_f32_e32 v78, v78\n" \
"v_exp_f32_e32 v79, v79\n" \
"v_exp_f32_e32 v80, v80\n" \
"v_exp_f32_e32 v81, v81\n" \
"v_exp_f32_e32 v82, v82\n" \
"v_exp_f32_e32 v83, v83\n" \
"v_exp_f32_e32 v84, v84\n" \
"v_exp_f32_e32 v85, v85\n" \
"v_exp_f32_e32 v86, v86\n" \
"v_exp_f32_e32 v87, v87\n" \
"v_add_u32_e32 v49, 0x10000, v48\n" \
"s_xor_b32 s65, s65, 0x8000\n" \
"s_add_u32 s64, s64, 0x10000\n" \
"v_exp_f32_e32 v88, v88\n" \
"v_exp_f32_e32 v89, v89\n" \
"v_exp_f32_e32 v90, v90\n" \
"v_exp_f32_e32 v91, v91\n" \
"v_cvt_pk_bf16_f32 v96, v64, v65\n" \
"v_cvt_pk_bf16_f32 v97, v66, v67\n" \
"v_cvt_pk_bf16_f32 v98, v68, v69\n" \
"v_cvt_pk_bf16_f32 v99, v70, v71\n" \
"v_cvt_pk_bf16_f32 v104, v72, v73\n" \
"s_waitcnt lgkmcnt(0)\n" \
"v_cvt_pk_bf16_f32 v105, v74, v75\n" \
"v_cvt_pk_bf16_f32 v106, v76, v77\n" \
"v_cvt_pk_bf16_f32 v107, v78, v79\n" \
"v_cvt_pk_bf16_f32 v112, v80, v81\n" \
"v_cvt_pk_bf16_f32 v113, v82, v83\n" \
"v_cvt_pk_bf16_f32 v114, v84, v85\n" \
"v_cvt_pk_bf16_f32 v115, v86, v87\n" \
"v_xor_b32_e32 v48, 0x8000, v48\n" \
"s_sub_u32 s66, s19, 1\n" \
"s_cbranch_scc0 L_2_%=\n" \
"s_waitcnt vmcnt(14)\n" \
"s_barrier\n" \
"L_f1_%=:\n" \
"L_pf18:\n" \
"s_cbranch_execz L_retA\n" \
"s_nop 0\n" \
"ds_read_b128 a[160:163], v49 offset:0\n" \
"ds_read_b128 a[164:167], v49 offset:1024\n" \
"ds_read_b128 a[168:171], v49 offset:2048\n" \
"ds_read_b128 a[172:175], v49 offset:3072\n" \
"ds_read_b128 a[176:179], v49 offset:8192\n" \
"ds_read_b128 a[180:183], v49 offset:9216\n" \
"v_mfma_f32_16x16x32_bf16 v[72:75], a[128:131], a[80:83], v[36:39]\n" \
"s_add_u32 s68, s61, 0x0\n" \
"s_add_u32 s69, s61, 0x49000\n" \
"ds_read_b128 a[184:187], v49 offset:10240\n" \
"v_mfma_f32_16x16x32_bf16 v[72:75], a[132:135], a[84:87], v[72:75]\n" \
"ds_read_b128 a[188:191], v49 offset:11264\n" \
"v_add_u32_e32 v56, 0x49000, v20\n" \
"v_mfma_f32_16x16x32_bf16 v[72:75], a[136:139], a[88:91], v[72:75]\n" \
"v_readlane_b32 s97, v11, 32\n" \
"v_readlane_b32 s98, v11, 33\n" \
"v_mfma_f32_16x16x32_bf16 v[72:75], a[140:143], a[92:95], v[72:75]\n" \
"v_and_b32_e32 v96, v12, v96\n" \
"v_and_b32_e32 v97, v13, v97\n" \
"v_mfma_f32_16x16x32_bf16 v[76:79], a[156:159], a[92:95], v[36:39]\n" \
"buffer_load_dwordx4 a[32:35], v20, s[56:59], s97 offen offset:0 sc1\n" \
"v_mfma_f32_16x16x32_bf16 v[76:79], a[152:155], a[88:91], v[76:79]\n" \
"v_and_b32_e32 v98, v14, v98\n" \
"v_and_b32_e32 v99, v15, v99\n" \
"v_mfma_f32_16x16x32_bf16 v[76:79], a[148:151], a[84:87], v[76:79]\n" \
"ds_read_b128 a[0:3], v20 offset:8192\n" \
"v_mov_b32_e32 v61, s62\n" \
"v_mfma_f32_16x16x32_bf16 v[76:79], a[144:147], a[80:83], v[76:79]\n" \
"buffer_load_dwordx4 a[40:43], v56, s[56:59], s97 offen offset:0 sc1\n" \
"ds_read_b128 a[4:7], v20 offset:9216\n" \
"ds_read_b128 a[8:11], v20 offset:10240\n" \
"s_waitcnt lgkmcnt(10)\n" \
"v_mfma_f32_16x16x32_bf16 v[128:131], a[160:163], v[96:99], v[128:131]\n" \
"ds_read_b128 a[12:15], v20 offset:11264\n" \
"s_waitcnt lgkmcnt(10)\n" \
"v_mfma_f32_16x16x32_bf16 v[132:135], a[164:167], v[96:99], v[132:135]\n" \
"ds_read_b128 a[28:31], v20 offset:15360\n" \
"buffer_load_dwordx4 a[48:51], v20, s[56:59], s98 offen offset:0 sc1\n" \
"s_waitcnt lgkmcnt(10)\n" \
"v_mfma_f32_16x16x32_bf16 v[136:139], a[168:171], v[96:99], v[136:139]\n" \
"ds_read_b128 a[24:27], v20 offset:14336\n" \
"s_waitcnt lgkmcnt(10)\n" \
"v_mfma_f32_16x16x32_bf16 v[140:143], a[172:175], v[96:99], v[140:143]\n" \
"ds_read_b128 a[20:23], v20 offset:13312\n" \
"buffer_load_dwordx4 a[56:59], v56, s[56:59], s98 offen offset:0 sc1\n" \
"s_waitcnt lgkmcnt(9)\n" \
"v_mfma_f32_16x16x32_bf16 v[144:147], a[176:179], v[96:99], v[144:147]\n" \
"ds_read_b128 a[16:19], v20 offset:12288\n" \
"v_add_u32_e32 v50, s15, v48\n" \
"v_mfma_f32_16x16x32_bf16 v[148:151], a[180:183], v[96:99], v[148:151]\n" \
"v_xor_b32_e32 v48, 0x4000, v49\n" \
"s_waitcnt lgkmcnt(8)\n" \
"v_mfma_f32_16x16x32_bf16 v[152:155], a[184:187], v[96:99], v[152:155]\n" \
"buffer_load_dwordx4 a[36:39], v20, s[56:59], s97 offen offset:1024 sc1\n" \
"v_mfma_f32_16x16x32_bf16 v[156:159], a[188:191], v[96:99], v[156:159]\n" \
"ds_read_b32 v58, v18 offset:256\n" \
"ds_read_b32 v59, v18 offset:384\n" \
"v_mfma_f32_16x16x32_bf16 a[192:195], a[252:255], v[96:99], a[192:195]\n" \
"ds_read_b128 a[96:99], v48 offset:4096\n" \
"ds_read_b128 a[100:103], v48 offset:5120\n" \
"buffer_load_dwordx4 a[44:47], v56, s[56:59], s97 offen offset:1024 sc1\n" \
"v_mfma_f32_16x16x32_bf16 v[160:163], a[160:163], v[104:107], v[160:163]\n" \
"ds_read_b128 a[104:107], v48 offset:6144\n" \
"ds_read_b128 a[108:111], v48 offset:7168\n" \
"v_mfma_f32_16x16x32_bf16 v[164:167], a[164:167], v[104:107], v[164:167]\n" \
"ds_read_b128 a[112:115], v48 offset:12288\n" \
"ds_read_b128 a[116:119], v48 offset:13312\n" \
"buffer_load_dwordx4 a[52:55], v20, s[56:59], s98 offen offset:1024 sc1\n" \
"v_mfma_f32_16x16x32_bf16 v[168:171], a[168:171], v[104:107], v[168:171]\n" \
"ds_read_b128 a[120:123], v48 offset:14336\n" \
"ds_read_b128 a[124:127], v48 offset:15360\n" \
"v_mfma_f32_16x16x32_bf16 v[172:175], a[172:175], v[104:107], v[172:175]\n" \
"ds_read_b128 a[128:131], v49 offset:4096\n" \
"ds_read_b128 a[132:135], v49 offset:5120\n" \
"buffer_load_dwordx4 a[60:63], v56, s[56:59], s98 offen offset:1024 sc1\n" \
"v_mfma_f32_16x16x32_bf16 v[176:179], a[176:179], v[104:107], v[176:179]\n" \
"ds_read_b128 a[136:139], v49 offset:6144\n" \
"ds_read_b128 a[140:143], v49 offset:7168\n" \
"v_mfma_f32_16x16x32_bf16 v[180:183], a[180:183], v[104:107], v[180:183]\n" \
"ds_read_b128 a[144:147], v49 offset:12288\n" \
"ds_read_b128 a[148:151], v49 offset:13312\n" \
"s_waitcnt vmcnt(8)\n" \
"s_mov_b64 exec, 1\n" \
"buffer_store_dword v61, off, s[56:59], s95 offset:0 sc1\n" \
"s_mov_b64 exec, -1\n" \
"v_mfma_f32_16x16x32_bf16 v[184:187], a[184:187], v[104:107], v[184:187]\n" \
"ds_read_b128 a[152:155], v49 offset:14336\n" \
"ds_read_b128 a[156:159], v49 offset:15360\n" \
"v_mfma_f32_16x16x32_bf16 v[188:191], a[188:191], v[104:107], v[188:191]\n" \
"v_accvgpr_read_b32 v40, a200\n" \
"v_accvgpr_read_b32 v23, a201\n" \
"v_mfma_f32_16x16x32_bf16 a[196:199], a[252:255], v[104:107], a[196:199]\n" \
"v_add_f32_e32 v40, v40, v23\n" \
"s_waitcnt lgkmcnt(15)\n" \
"v_mfma_f32_16x16x32_bf16 v[80:83], a[0:3], a[64:67], v[32:35]\n" \
"buffer_load_dwordx4 a[220:223], v20, s[56:59], s60 offen offset:0 sc1\n" \
"v_mfma_f32_16x16x32_bf16 v[80:83], a[4:7], a[68:71], v[80:83]\n" \
"v_accvgpr_read_b32 v41, a204\n" \
"v_accvgpr_read_b32 v23, a205\n" \
"v_mfma_f32_16x16x32_bf16 v[80:83], a[8:11], a[72:75], v[80:83]\n" \
"v_add_f32_e32 v41, v41, v23\n" \
"v_add_u32_e32 v51, 0x10000, v50\n" \
"v_mfma_f32_16x16x32_bf16 v[80:83], a[12:15], a[76:79], v[80:83]\n" \
"buffer_load_dwordx4 a[236:239], v56, s[56:59], s60 offen offset:0 sc1\n" \
"v_mfma_f32_16x16x32_bf16 v[84:87], a[28:31], a[76:79], v[32:35]\n" \
"v_add_f32_e32 v40, s94, v40\n" \
"v_add_f32_e32 v41, s94, v41\n" \
"v_mfma_f32_16x16x32_bf16 v[84:87], a[24:27], a[72:75], v[84:87]\n" \
"ds_write_b128 v50, v[192:195] offset:0\n" \
"buffer_load_dwordx4 a[224:227], v20, s[56:59], s60 offen offset:1024 sc1\n" \
"v_mfma_f32_16x16x32_bf16 v[84:87], a[20:23], a[68:71], v[84:87]\n" \
"ds_write_b128 v50, v[196:199] offset:1024\n" \
"v_mfma_f32_16x16x32_bf16 v[84:87], a[16:19], a[64:67], v[84:87]\n" \
"ds_write_b128 v50, v[200:203] offset:2048\n" \
"buffer_load_dwordx4 a[240:243], v56, s[56:59], s60 offen offset:1024 sc1\n" \
"v_mfma_f32_16x16x32_bf16 v[88:91], a[0:3], a[80:83], v[36:39]\n" \
"ds_write_b128 v50, v[204:207] offset:3072\n" \
"L_pf19:\n" \
"s_cbranch_execz L_retA\n" \
"s_nop 0\n" \
"v_mfma_f32_16x16x32_bf16 v[88:91], a[4:7], a[84:87], v[88:91]\n" \
"ds_write_b128 v50, v[208:211] offset:8192\n" \
"buffer_load_dwordx4 a[228:231], v20, s[56:59], s60 offen offset:2048 sc1\n" \
"v_mfma_f32_16x16x32_bf16 v[88:91], a[8:11], a[88:91], v[88:91]\n" \
"ds_write_b128 v50, v[212:215] offset:9216\n" \
"v_mfma_f32_16x16x32_bf16 v[88:91], a[12:15], a[92:95], v[88:91]\n" \
"ds_write_b128 v50, v[216:219] offset:10240\n" \
"buffer_load_dwordx4 a[244:247], v56, s[56:59], s60 offen offset:2048 sc1\n" \
"v_mfma_f32_16x16x32_bf16 v[92:95], a[28:31], a[92:95], v[36:39]\n" \
"ds_write_b128 v50, v[220:223] offset:11264\n" \
"ds_write_b128 v51, v[224:227] offset:0\n" \
"ds_write_b128 v51, v[228:231] offset:1024\n" \
"buffer_load_dwordx4 a[232:235], v20, s[56:59], s60 offen offset:3072 sc1\n" \
"ds_write_b128 v51, v[232:235] offset:2048\n" \
"ds_write_b128 v51, v[236:239] offset:3072\n" \
"ds_write_b128 v51, v[240:243] offset:8192\n" \
"ds_write_b128 v51, v[244:247] offset:9216\n" \
"buffer_load_dwordx4 a[248:251], v56, s[56:59], s60 offen offset:3072 sc1\n" \
"ds_write_b128 v51, v[248:251] offset:10240\n" \
"ds_write_b128 v51, v[252:255] offset:11264\n" \
"ds_write_b64 v19, v[40:41]\n" \
"v_mfma_f32_16x16x32_bf16 v[92:95], a[24:27], a[88:91], v[92:95]\n" \
"v_xor_b32_e32 v52, 0x4000, v50\n" \
"v_xor_b32_e32 v53, 0x4000, v51\n" \
"v_mfma_f32_16x16x32_bf16 v[92:95], a[20:23], a[84:87], v[92:95]\n" \
"v_exp_f32_e32 v72, v72\n" \
"v_mfma_f32_16x16x32_bf16 v[92:95], a[16:19], a[80:83], v[92:95]\n" \
"v_exp_f32_e32 v73, v73\n" \
"v_exp_f32_e32 v74, v74\n" \
"v_exp_f32_e32 v75, v75\n" \
"v_exp_f32_e32 v76, v76\n" \
"v_exp_f32_e32 v77, v77\n" \
"v_exp_f32_e32 v78, v78\n" \
"v_exp_f32_e32 v79, v79\n" \
"v_cvt_pk_bf16_f32 v108, v72, v73\n" \
"v_cvt_pk_bf16_f32 v109, v74, v75\n" \
"v_cvt_pk_bf16_f32 v110, v76, v77\n" \
"v_cvt_pk_bf16_f32 v111, v78, v79\n" \
"v_and_b32_e32 v108, v12, v108\n" \
"v_and_b32_e32 v109, v13, v109\n" \
"v_and_b32_e32 v110, v14, v110\n" \
"v_and_b32_e32 v111, v15, v111\n" \
"v_readlane_b32 s93, v11, 18\n" \
"v_readlane_b32 s84, v11, 13\n" \
"v_readlane_b32 s85, v11, 14\n" \
"v_readlane_b32 s86, v11, 15\n" \
"s_mov_b32 s87, 0x20000\n" \
"v_readlane_b32 s92, v11, 16\n" \
"s_add_u32 m0, s93, 0x0\n" \
"v_xor_b32_e32 v63, 0x400, v19\n" \
"s_waitcnt lgkmcnt(0)\n" \
"s_barrier\n" \
"ds_read_b128 v[192:195], v52 offset:0\n" \
"ds_read_b128 v[196:199], v52 offset:1024\n" \
"ds_read_b128 v[200:203], v52 offset:2048\n" \
"ds_read_b128 v[204:207], v52 offset:3072\n" \
"ds_read_b128 v[208:211], v52 offset:8192\n" \
"ds_read_b128 v[212:215], v52 offset:9216\n" \
"ds_read_b128 v[216:219], v52 offset:10240\n" \
"ds_read_b128 v[220:223], v52 offset:11264\n" \
"ds_read_b128 v[224:227], v53 offset:0\n" \
"ds_read_b128 v[228:231], v53 offset:1024\n" \
"ds_read_b128 v[232:235], v53 offset:2048\n" \
"ds_read_b128 v[236:239], v53 offset:3072\n" \
"ds_read_b128 v[240:243], v53 offset:8192\n" \
"ds_read_b128 v[244:247], v53 offset:9216\n" \
"ds_read_b128 v[248:251], v53 offset:10240\n" \
"ds_read_b128 v[252:255], v53 offset:11264\n" \
"v_mfma_f32_16x16x32_bf16 v[160:163], a[128:131], v[108:111], v[160:163]\n" \
"buffer_load_dwordx4 v0, s[84:87], s92 offen offset:0 lds\n" \
"v_mfma_f32_16x16x32_bf16 v[164:167], a[132:135], v[108:111], v[164:167]\n" \
"v_exp_f32_e32 v80, v80\n" \
"v_mfma_f32_16x16x32_bf16 v[168:171], a[136:139], v[108:111], v[168:171]\n" \
"v_exp_f32_e32 v81, v81\n" \
"v_mfma_f32_16x16x32_bf16 v[172:175], a[140:143], v[108:111], v[172:175]\n" \
"buffer_load_dwordx4 v1, s[84:87], s92 offen offset:1024 lds\n" \
"v_mfma_f32_16x16x32_bf16 v[176:179], a[144:147], v[108:111], v[176:179]\n" \
"ds_read_b64 v[42:43], v63\n" \
"v_cvt_pk_bf16_f32 v116, v80, v81\n" \
"v_mfma_f32_16x16x32_bf16 v[180:183], a[148:151], v[108:111], v[180:183]\n" \
"v_exp_f32_e32 v82, v82\n" \
"v_mfma_f32_16x16x32_bf16 v[184:187], a[152:155], v[108:111], v[184:187]\n" \
"buffer_load_dwordx4 v2, s[84:87], s92 offen offset:2048 lds\n" \
"v_mfma_f32_16x16x32_bf16 v[188:191], a[156:159], v[108:111], v[188:191]\n" \
"v_exp_f32_e32 v83, v83\n" \
"v_mfma_f32_16x16x32_bf16 a[196:199], a[252:255], v[108:111], a[196:199]\n" \
"v_exp_f32_e32 v84, v84\n" \
"v_exp_f32_e32 v85, v85\n" \
"v_exp_f32_e32 v86, v86\n" \
"s_waitcnt lgkmcnt(1)\n" \
"s_barrier\n" \
"buffer_load_dwordx4 v3, s[84:87], s92 offen offset:3072 lds\n" \
"s_add_u32 m0, s93, 0x1000\n" \
"v_exp_f32_e32 v87, v87\n" \
"v_cvt_pk_bf16_f32 v117, v82, v83\n" \
"v_cvt_pk_bf16_f32 v118, v84, v85\n" \
"v_cvt_pk_bf16_f32 v119, v86, v87\n" \
"v_exp_f32_e32 v88, v88\n" \
"v_exp_f32_e32 v89, v89\n" \
"v_mfma_f32_16x16x32_bf16 v[128:131], a[96:99], v[116:119], v[128:131]\n" \
"buffer_load_dwordx4 v4, s[84:87], s92 offen offset:0 lds\n" \
"v_mfma_f32_16x16x32_bf16 v[132:135], a[100:103], v[116:119], v[132:135]\n" \
"v_exp_f32_e32 v90, v90\n" \
"v_mfma_f32_16x16x32_bf16 v[136:139], a[104:107], v[116:119], v[136:139]\n" \
"v_exp_f32_e32 v91, v91\n" \
"v_mfma_f32_16x16x32_bf16 v[140:143], a[108:111], v[116:119], v[140:143]\n" \
"buffer_load_dwordx4 v5, s[84:87], s92 offen offset:1024 lds\n" \
"v_mfma_f32_16x16x32_bf16 v[144:147], a[112:115], v[116:119], v[144:147]\n" \
"v_exp_f32_e32 v92, v92\n" \
"v_mfma_f32_16x16x32_bf16 v[148:151], a[116:119], v[116:119], v[148:151]\n" \
"v_exp_f32_e32 v93, v93\n" \
"v_mfma_f32_16x16x32_bf16 v[152:155], a[120:123], v[116:119], v[152:155]\n" \
"buffer_load_dwordx4 v6, s[84:87], s92 offen offset:2048 lds\n" \
"v_mfma_f32_16x16x32_bf16 v[156:159], a[124:127], v[116:119], v[156:159]\n" \
"v_exp_f32_e32 v94, v94\n" \
"v_mfma_f32_16x16x32_bf16 a[192:195], a[252:255], v[116:119], a[192:195]\n" \
"v_exp_f32_e32 v95, v95\n" \
"v_cvt_pk_bf16_f32 v124, v88, v89\n" \
"v_cvt_pk_bf16_f32 v125, v90, v91\n" \
"v_cvt_pk_bf16_f32 v126, v92, v93\n" \
"v_cvt_pk_bf16_f32 v127, v94, v95\n" \
"v_add_f32_e32 v160, v160, v224\n" \
"v_add_f32_e32 v161, v161, v225\n" \
"v_add_f32_e32 v162, v162, v226\n" \
"v_add_f32_e32 v163, v163, v227\n" \
"v_add_f32_e32 v164, v164, v228\n" \
"v_add_f32_e32 v165, v165, v229\n" \
"v_mfma_f32_16x16x32_bf16 v[160:163], a[96:99], v[124:127], v[160:163]\n" \
"buffer_load_dwordx4 v7, s[84:87], s92 offen offset:3072 lds\n" \
"s_add_u32 m0, s93, 0x2000\n" \
"v_add_f32_e32 v166, v166, v230\n" \
"v_add_f32_e32 v167, v167, v231\n" \
"v_add_f32_e32 v168, v168, v232\n" \
"v_add_f32_e32 v169, v169, v233\n" \
"v_mfma_f32_16x16x32_bf16 v[164:167], a[100:103], v[124:127], v[164:167]\n" \
"v_add_f32_e32 v170, v170, v234\n" \
"v_add_f32_e32 v171, v171, v235\n" \
"v_add_f32_e32 v172, v172, v236\n" \
"v_add_f32_e32 v173, v173, v237\n" \
"v_mfma_f32_16x16x32_bf16 v[168:171], a[104:107], v[124:127], v[168:171]\n" \
"buffer_load_dwordx4 v8, s[84:87], s92 offen offset:0 lds\n" \
"v_add_f32_e32 v174, v174, v238\n" \
"v_add_f32_e32 v175, v175, v239\n" \
"v_accvgpr_read_b32 v54, a192\n" \
"v_accvgpr_read_b32 v23, a193\n" \
"v_mfma_f32_16x16x32_bf16 v[172:175], a[108:111], v[124:127], v[172:175]\n" \
"v_add_f32_e32 v54, v54, v23\n" \
"s_waitcnt lgkmcnt(0)\n" \
"v_add_f32_e32 v54, v54, v42\n" \
"v_max_f32_e32 v62, v62, v54\n" \
"v_add_f32_e32 v176, v176, v240\n" \
"v_add_f32_e32 v177, v177, v241\n" \
"v_add_f32_e32 v178, v178, v242\n" \
"v_add_f32_e32 v179, v179, v243\n" \
"v_rcp_f32_e32 v54, v54\n" \
"v_add_f32_e32 v128, v128, v192\n" \
"v_mfma_f32_16x16x32_bf16 v[176:179], a[112:115], v[124:127], v[176:179]\n" \
"v_add_f32_e32 v129, v129, v193\n" \
"v_add_f32_e32 v130, v130, v194\n" \
"v_add_f32_e32 v131, v131, v195\n" \
"v_add_f32_e32 v132, v132, v196\n" \
"v_add_f32_e32 v133, v133, v197\n" \
"v_add_f32_e32 v134, v134, v198\n" \
"v_add_f32_e32 v135, v135, v199\n" \
"v_mul_f32_e32 v54, v54, v58\n" \
"v_mul_f32_e32 v128, v54, v128\n" \
"v_mul_f32_e32 v129, v54, v129\n" \
"v_mul_f32_e32 v130, v54, v130\n" \
"v_mul_f32_e32 v131, v54, v131\n" \
"v_mul_f32_e32 v132, v54, v132\n" \
"v_mul_f32_e32 v133, v54, v133\n" \
"v_mul_f32_e32 v134, v54, v134\n" \
"v_mul_f32_e32 v135, v54, v135\n" \
"v_add_f32_e32 v180, v180, v244\n" \
"v_add_f32_e32 v181, v181, v245\n" \
"v_add_f32_e32 v182, v182, v246\n" \
"v_add_f32_e32 v183, v183, v247\n" \
"v_add_f32_e32 v136, v136, v200\n" \
"v_add_f32_e32 v137, v137, v201\n" \
"v_add_f32_e32 v138, v138, v202\n" \
"v_add_f32_e32 v139, v139, v203\n" \
"v_add_f32_e32 v140, v140, v204\n" \
"v_add_f32_e32 v141, v141, v205\n" \
"v_add_f32_e32 v142, v142, v206\n" \
"v_add_f32_e32 v143, v143, v207\n" \
"v_cvt_pk_bf16_f32 v64, v128, v129\n" \
"v_cvt_pk_bf16_f32 v65, v130, v131\n" \
"v_cvt_pk_bf16_f32 v66, v132, v133\n" \
"v_cvt_pk_bf16_f32 v67, v134, v135\n" \
"buffer_store_dwordx4 v[64:67], v20, s[56:59], s68 offen offset:0 sc1\n" \
"v_mfma_f32_16x16x32_bf16 v[180:183], a[116:119], v[124:127], v[180:183]\n" \
"v_mul_f32_e32 v136, v54, v136\n" \
"v_mul_f32_e32 v137, v54, v137\n" \
"v_mfma_f32_16x16x32_bf16 a[208:211], a[216:219], v[64:67], a[208:211]\n" \
"v_mul_f32_e32 v138, v54, v138\n" \
"v_mul_f32_e32 v139, v54, v139\n" \
"v_mul_f32_e32 v140, v54, v140\n" \
"v_mul_f32_e32 v141, v54, v141\n" \
"v_mul_f32_e32 v142, v54, v142\n" \
"v_mul_f32_e32 v143, v54, v143\n" \
"v_add_f32_e32 v144, v144, v208\n" \
"v_add_f32_e32 v145, v145, v209\n" \
"v_add_f32_e32 v146, v146, v210\n" \
"v_add_f32_e32 v147, v147, v211\n" \
"v_add_f32_e32 v148, v148, v212\n" \
"v_add_f32_e32 v149, v149, v213\n" \
"v_add_f32_e32 v150, v150, v214\n" \
"v_add_f32_e32 v151, v151, v215\n" \
"v_cvt_pk_bf16_f32 v68, v136, v137\n" \
"v_cvt_pk_bf16_f32 v69, v138, v139\n" \
"v_cvt_pk_bf16_f32 v70, v140, v141\n" \
"v_cvt_pk_bf16_f32 v71, v142, v143\n" \
"buffer_store_dwordx4 v[68:71], v20, s[56:59], s68 offen offset:1024 sc1\n" \
"v_mul_f32_e32 v144, v54, v144\n" \
"v_mfma_f32_16x16x32_bf16 a[208:211], a[216:219], v[68:71], a[208:211]\n" \
"v_mul_f32_e32 v145, v54, v145\n" \
"v_mul_f32_e32 v146, v54, v146\n" \
"v_mul_f32_e32 v147, v54, v147\n" \
"v_mul_f32_e32 v148, v54, v148\n" \
"v_mul_f32_e32 v149, v54, v149\n" \
"v_mul_f32_e32 v150, v54, v150\n" \
"v_mul_f32_e32 v151, v54, v151\n" \
"v_add_f32_e32 v184, v184, v248\n" \
"v_add_f32_e32 v185, v185, v249\n" \
"v_add_f32_e32 v186, v186, v250\n" \
"v_add_f32_e32 v187, v187, v251\n" \
"v_add_f32_e32 v152, v152, v216\n" \
"v_add_f32_e32 v153, v153, v217\n" \
"v_add_f32_e32 v154, v154, v218\n" \
"v_add_f32_e32 v155, v155, v219\n" \
"v_add_f32_e32 v156, v156, v220\n" \
"v_add_f32_e32 v157, v157, v221\n" \
"v_add_f32_e32 v158, v158, v222\n" \
"v_add_f32_e32 v159, v159, v223\n" \
"v_cvt_pk_bf16_f32 v72, v144, v145\n" \
"v_cvt_pk_bf16_f32 v73, v146, v147\n" \
"v_cvt_pk_bf16_f32 v74, v148, v149\n" \
"v_cvt_pk_bf16_f32 v75, v150, v151\n" \
"buffer_store_dwordx4 v[72:75], v20, s[56:59], s68 offen offset:2048 sc1\n" \
"v_mfma_f32_16x16x32_bf16 v[184:187], a[120:123], v[124:127], v[184:187]\n" \
"v_mul_f32_e32 v152, v54, v152\n" \
"v_mul_f32_e32 v153, v54, v153\n" \
"v_mfma_f32_16x16x32_bf16 a[208:211], a[216:219], v[72:75], a[208:211]\n" \
"v_mul_f32_e32 v154, v54, v154\n" \
"v_mul_f32_e32 v155, v54, v155\n" \
"v_mul_f32_e32 v156, v54, v156\n" \
"v_mul_f32_e32 v157, v54, v157\n" \
"v_mul_f32_e32 v158, v54, v158\n" \
"v_mul_f32_e32 v159, v54, v159\n" \
"v_cvt_pk_bf16_f32 v76, v152, v153\n" \
"v_cvt_pk_bf16_f32 v77, v154, v155\n" \
"v_cvt_pk_bf16_f32 v78, v156, v157\n" \
"v_cvt_pk_bf16_f32 v79, v158, v159\n" \
"buffer_store_dwordx4 v[76:79], v20, s[56:59], s68 offen offset:3072 sc1\n" \
"v_add_f32_e32 v188, v188, v252\n" \
"v_mfma_f32_16x16x32_bf16 a[208:211], a[216:219], v[76:79], a[208:211]\n" \
"v_add_f32_e32 v189, v189, v253\n" \
"v_add_f32_e32 v190, v190, v254\n" \
"v_add_f32_e32 v191, v191, v255\n" \
"s_nop 1\n" \
"v_mfma_f32_16x16x32_bf16 v[188:191], a[124:127], v[124:127], v[188:191]\n" \
"v_mfma_f32_16x16x32_bf16 a[196:199], a[252:255], v[124:127], a[196:199]\n" \
"s_nop 7\n" \
"v_accvgpr_read_b32 v55, a196\n" \
"v_accvgpr_read_b32 v23, a197\n" \
"v_add_f32_e32 v55, v55, v23\n" \
"v_add_f32_e32 v55, v55, v43\n" \
"v_max_f32_e32 v62, v62, v55\n" \
"v_rcp_f32_e32 v55, v55\n" \
"v_cmp_lt_f32_e64 s[72:73], s100, v62\n" \
"v_mul_f32_e32 v55, v55, v59\n" \
"v_mul_f32_e32 v160, v55, v160\n" \
"v_mul_f32_e32 v161, v55, v161\n" \
"v_mul_f32_e32 v162, v55, v162\n" \
"v_mul_f32_e32 v163, v55, v163\n" \
"v_mul_f32_e32 v164, v55, v164\n" \
"v_mul_f32_e32 v165, v55, v165\n" \
"v_mul_f32_e32 v166, v55, v166\n" \
"v_mul_f32_e32 v167, v55, v167\n" \
"v_cvt_pk_bf16_f32 v80, v160, v161\n" \
"v_cvt_pk_bf16_f32 v81, v162, v163\n" \
"v_cvt_pk_bf16_f32 v82, v164, v165\n" \
"v_cvt_pk_bf16_f32 v83, v166, v167\n" \
"buffer_store_dwordx4 v[80:83], v20, s[56:59], s69 offen offset:0 sc1\n" \
"v_mul_f32_e32 v168, v55, v168\n" \
"v_mfma_f32_16x16x32_bf16 a[208:211], a[216:219], v[80:83], a[208:211]\n" \
"v_mul_f32_e32 v169, v55, v169\n" \
"v_mul_f32_e32 v170, v55, v170\n" \
"v_mul_f32_e32 v171, v55, v171\n" \
"v_mul_f32_e32 v172, v55, v172\n" \
"v_mul_f32_e32 v173, v55, v173\n" \
"v_mul_f32_e32 v174, v55, v174\n" \
"v_mul_f32_e32 v175, v55, v175\n" \
"v_cvt_pk_bf16_f32 v84, v168, v169\n" \
"v_cvt_pk_bf16_f32 v85, v170, v171\n" \
"v_cvt_pk_bf16_f32 v86, v172, v173\n" \
"v_cvt_pk_bf16_f32 v87, v174, v175\n" \
"buffer_store_dwordx4 v[84:87], v20, s[56:59], s69 offen offset:1024 sc1\n" \
"v_mul_f32_e32 v176, v55, v176\n" \
"v_mfma_f32_16x16x32_bf16 a[208:211], a[216:219], v[84:87], a[208:211]\n" \
"v_mul_f32_e32 v177, v55, v177\n" \
"v_mul_f32_e32 v178, v55, v178\n" \
"v_mul_f32_e32 v179, v55, v179\n" \
"v_mul_f32_e32 v180, v55, v180\n" \
"v_mul_f32_e32 v181, v55, v181\n" \
"v_mul_f32_e32 v182, v55, v182\n" \
"v_mul_f32_e32 v183, v55, v183\n" \
"v_cvt_pk_bf16_f32 v96, v176, v177\n" \
"v_cvt_pk_bf16_f32 v97, v178, v179\n" \
"v_cvt_pk_bf16_f32 v98, v180, v181\n" \
"v_cvt_pk_bf16_f32 v99, v182, v183\n" \
"buffer_store_dwordx4 v[96:99], v20, s[56:59], s69 offen offset:2048 sc1\n" \
"v_mul_f32_e32 v184, v55, v184\n" \
"v_mfma_f32_16x16x32_bf16 a[208:211], a[216:219], v[96:99], a[208:211]\n" \
"v_mul_f32_e32 v185, v55, v185\n" \
"v_mul_f32_e32 v186, v55, v186\n" \
"v_mul_f32_e32 v187, v55, v187\n" \
"v_mul_f32_e32 v188, v55, v188\n" \
"v_mul_f32_e32 v189, v55, v189\n" \
"v_mul_f32_e32 v190, v55, v190\n" \
"v_mul_f32_e32 v191, v55, v191\n" \
"v_cvt_pk_bf16_f32 v100, v184, v185\n" \
"v_cvt_pk_bf16_f32 v101, v186, v187\n" \
"v_cvt_pk_bf16_f32 v102, v188, v189\n" \
"v_cvt_pk_bf16_f32 v103, v190, v191\n" \
"buffer_store_dwordx4 v[100:103], v20, s[56:59], s69 offen offset:3072 sc1\n" \
"s_nop 0\n" \
"v_mfma_f32_16x16x32_bf16 a[208:211], a[216:219], v[100:103], a[208:211]\n" \
"s_nop 7\n" \
"v_accvgpr_read_b32 v63, a208\n" \
"v_cmp_u_f32_e32 vcc, v63, v63\n" \
"s_nop 0\n" \
"s_or_b64 vcc, s[72:73], vcc\n" \
"s_cbranch_scc1 L_end_%=\n" \
"L_out_%=:\n" \
"L_pf20:\n" \
"s_cbranch_execz L_retA\n" \
"s_nop 0\n" \
"v_add_u32_e32 v34, 0x20000, v10\n" \
"v_mov_b32_e32 v39, s62\n" \
"v_readlane_b32 s70, v11, 4\n" \
"v_bfe_u32 v38, v20, 2, 4\n" \
"v_readlane_b32 s64, v11, 1\n" \
"v_readlane_b32 s71, v11, 3\n" \
"v_add_u32_e32 v33, 0xfc00, v10\n" \
"v_readlane_b32 s99, v11, 19\n" \
"v_readlane_b32 s80, v11, 9\n" \
"v_readlane_b32 s81, v11, 10\n" \
"s_mov_b32 s82, 0x47fca0\n" \
"s_mov_b32 s83, 0x20000\n" \
"ds_read_b64_tr_b16 a[0:1], v34 offset:0\n" \
"ds_read_b64_tr_b16 a[2:3], v34 offset:4608\n" \
"ds_read_b64_tr_b16 a[4:5], v34 offset:9216\n" \
"ds_read_b64_tr_b16 a[6:7], v34 offset:13824\n" \
"ds_read_b64_tr_b16 a[8:9], v34 offset:18432\n" \
"ds_read_b64_tr_b16 a[10:11], v34 offset:23040\n" \
"ds_read_b64_tr_b16 a[12:13], v34 offset:32\n" \
"ds_read_b64_tr_b16 a[14:15], v34 offset:4640\n" \
"ds_read_b64_tr_b16 a[16:17], v34 offset:9248\n" \
"ds_read_b64_tr_b16 a[18:19], v34 offset:13856\n" \
"buffer_load_dword v37, v38, s[56:59], s64 offen sc1\n" \
"ds_read_b64_tr_b16 a[20:21], v34 offset:18464\n" \
"ds_read_b64_tr_b16 a[22:23], v34 offset:23072\n" \
"ds_read_b64_tr_b16 a[24:25], v34 offset:64\n" \
"ds_read_b64_tr_b16 a[26:27], v34 offset:4672\n" \
"ds_read_b64_tr_b16 a[28:29], v34 offset:9280\n" \
"ds_read_b64_tr_b16 a[30:31], v34 offset:13888\n" \
"ds_read_b64_tr_b16 a[192:193], v34 offset:18496\n" \
"ds_read_b64_tr_b16 a[194:195], v34 offset:23104\n" \
"ds_read_b64_tr_b16 a[196:197], v34 offset:96\n" \
"ds_read_b64_tr_b16 a[198:199], v34 offset:4704\n" \
"ds_read_b64_tr_b16 a[200:201], v34 offset:9312\n" \
"ds_read_b64_tr_b16 a[202:203], v34 offset:13920\n" \
"ds_read_b64_tr_b16 a[204:205], v34 offset:18528\n" \
"ds_read_b64_tr_b16 a[206:207], v34 offset:23136\n" \
"ds_read_b64_tr_b16 a[208:209], v34 offset:128\n" \
"ds_read_b64_tr_b16 a[210:211], v34 offset:4736\n" \
"ds_read_b64_tr_b16 a[212:213], v34 offset:9344\n" \
"ds_read_b64_tr_b16 a[214:215], v34 offset:13952\n" \
"ds_read_b64_tr_b16 a[216:217], v34 offset:18560\n" \
"ds_read_b64_tr_b16 a[218:219], v34 offset:23168\n" \
"v_readlane_b32 s65, v11, 23\n" \
"s_add_u32 m0, s99, 0x6c00\n" \
"v_readlane_b32 s66, v11, 26\n" \
"v_add_u32_e32 v32, 0x49000, v20\n" \
"v_lshrrev_b32_e32 v44, 9, v20\n" \
"v_mul_u32_u24_e32 v44, 0xb40, v44\n" \
"v_add_u32_e32 v35, v44, v10\n" \
"v_readlane_b32 s67, v11, 24\n" \
"v_readlane_b32 s92, v11, 20\n" \
"v_readlane_b32 s73, v11, 25\n" \
"s_waitcnt vmcnt(18) lgkmcnt(15)\n" \
"v_mfma_f32_16x16x32_bf16 v[112:115], a[0:3], a[220:223], 0\n" \
"buffer_load_dwordx4 v0, s[80:83], s65 offen offset:0 lds\n" \
"v_mfma_f32_16x16x32_bf16 v[112:115], a[4:7], a[224:227], v[112:115]\n" \
"v_readlane_b32 s94, v11, 21\n" \
"v_readlane_b32 s84, v11, 7\n" \
"v_mfma_f32_16x16x32_bf16 v[112:115], a[8:11], a[228:231], v[112:115]\n" \
"v_readlane_b32 s85, v11, 8\n" \
"s_mov_b32 s86, 0xa1ca0\n" \
"v_mfma_f32_16x16x32_bf16 v[116:119], a[8:11], a[244:247], 0\n" \
"s_mov_b32 s87, 0x20000\n" \
"v_readlane_b32 s68, v11, 27\n" \
"v_mfma_f32_16x16x32_bf16 v[116:119], a[4:7], a[240:243], v[116:119]\n" \
"v_readlane_b32 s69, v11, 28\n" \
"v_readlane_b32 s72, v11, 30\n" \
"v_mfma_f32_16x16x32_bf16 v[116:119], a[0:3], a[236:239], v[116:119]\n" \
"buffer_load_dwordx4 v1, s[80:83], s65 offen offset:1024 lds\n" \
"v_mfma_f32_16x16x32_bf16 v[184:187], a[0:3], v[64:67], 0\n" \
"v_readlane_b32 s93, v11, 31\n" \
"v_lshlrev_b32_e32 v41, 12, v20\n" \
"v_mfma_f32_16x16x32_bf16 v[184:187], a[4:7], v[68:71], v[184:187]\n" \
"v_readlane_b32 s63, v11, 37\n" \
"v_bfe_u32 v40, v20, 2, 7\n" \
"v_mfma_f32_16x16x32_bf16 v[184:187], a[8:11], v[72:75], v[184:187]\n" \
"s_add_u32 s74, s63, 0x3800\n" \
"v_readlane_b32 s95, v11, 34\n" \
"v_mfma_f32_16x16x32_bf16 v[188:191], a[8:11], v[96:99], 0\n" \
"ds_read_b64_tr_b16 a[8:9], v34 offset:18592\n" \
"ds_read_b64_tr_b16 a[10:11], v34 offset:23200\n" \
"v_mfma_f32_16x16x32_bf16 v[188:191], a[4:7], v[84:87], v[188:191]\n" \
"ds_read_b64_tr_b16 a[4:5], v34 offset:9376\n" \
"ds_read_b64_tr_b16 a[6:7], v34 offset:13984\n" \
"v_mfma_f32_16x16x32_bf16 v[188:191], a[0:3], v[80:83], v[188:191]\n" \
"ds_read_b64_tr_b16 a[0:1], v34 offset:160\n" \
"ds_read_b64_tr_b16 a[2:3], v34 offset:4768\n" \
"v_mfma_f32_16x16x32_bf16 v[120:123], a[12:15], a[220:223], 0\n" \
"buffer_load_dwordx4 v2, s[80:83], s65 offen offset:2048 lds\n" \
"v_mfma_f32_16x16x32_bf16 v[120:123], a[16:19], a[224:227], v[120:123]\n" \
"v_readlane_b32 s97, v11, 36\n" \
"v_bfe_u32 v44, v20, 4, 4\n" \
"v_mfma_f32_16x16x32_bf16 v[120:123], a[20:23], a[228:231], v[120:123]\n" \
"v_bfe_u32 v45, v20, 8, 2\n" \
"v_mul_u32_u24_e32 v42, 0x480, v44\n" \
"v_mfma_f32_16x16x32_bf16 v[124:127], a[20:23], a[244:247], 0\n" \
"v_lshl_add_u32 v42, v45, 3, v42\n" \
"v_and_b32_e32 v43, 1, v45\n" \
"v_mfma_f32_16x16x32_bf16 v[124:127], a[16:19], a[240:243], v[124:127]\n" \
"v_lshrrev_b32_e32 v44, 1, v44\n" \
"v_lshlrev_b32_e32 v45, 1, v45\n" \
"v_mfma_f32_16x16x32_bf16 v[124:127], a[12:15], a[236:239], v[124:127]\n" \
"buffer_load_dwordx4 v3, s[80:83], s65 offen offset:3072 lds\n" \
"v_mfma_f32_16x16x32_bf16 v[192:195], a[12:15], v[64:67], 0\n" \
"s_add_u32 m0, s99, 0x7c00\n" \
"v_sub_u32_e32 v44, v44, v45\n" \
"v_mfma_f32_16x16x32_bf16 v[192:195], a[16:19], v[68:71], v[192:195]\n" \
"v_and_b32_e32 v45, 16, v20\n" \
"v_mov_b32_e32 v46, 0x3f80\n" \
"v_mfma_f32_16x16x32_bf16 v[192:195], a[20:23], v[72:75], v[192:195]\n" \
"v_lshlrev_b32_e32 v45, v45, v46\n" \
"v_mov_b32_e32 v46, s62\n" \
"v_mfma_f32_16x16x32_bf16 v[196:199], a[20:23], v[96:99], 0\n" \
"ds_read_b64_tr_b16 a[20:21], v34 offset:18624\n" \
"ds_read_b64_tr_b16 a[22:23], v34 offset:23232\n" \
"v_mfma_f32_16x16x32_bf16 v[196:199], a[16:19], v[84:87], v[196:199]\n" \
"ds_read_b64_tr_b16 a[16:17], v34 offset:9408\n" \
"ds_read_b64_tr_b16 a[18:19], v34 offset:14016\n" \
"v_mfma_f32_16x16x32_bf16 v[196:199], a[12:15], v[80:83], v[196:199]\n" \
"ds_read_b64_tr_b16 a[12:13], v34 offset:192\n" \
"ds_read_b64_tr_b16 a[14:15], v34 offset:4800\n" \
"v_mfma_f32_16x16x32_bf16 v[128:131], a[24:27], a[220:223], 0\n" \
"v_readlane_b32 s96, v11, 35\n" \
"s_waitcnt lgkmcnt(8)\n" \
"v_mfma_f32_16x16x32_bf16 v[128:131], a[28:31], a[224:227], v[128:131]\n" \
"buffer_load_dwordx4 v4, s[80:83], s65 offen offset:0 lds\n" \
"v_mfma_f32_16x16x32_bf16 v[128:131], a[192:195], a[228:231], v[128:131]\n" \
"v_readlane_b32 s100, v11, 6\n" \
"v_readlane_b32 s75, v11, 38\n" \
"v_mfma_f32_16x16x32_bf16 v[132:135], a[192:195], a[244:247], 0\n" \
"v_add_u32_e32 v54, 0x3000, v20\n" \
"v_mov_b32_e32 v52, 0\n" \
"v_mfma_f32_16x16x32_bf16 v[132:135], a[28:31], a[240:243], v[132:135]\n" \
"v_mov_b32_e32 v53, 0\n" \
"v_mov_b32_e32 v48, 0\n" \
"v_mfma_f32_16x16x32_bf16 v[132:135], a[24:27], a[236:239], v[132:135]\n" \
"v_mov_b32_e32 v49, 0\n" \
"v_mul_u32_u24_e32 v43, 0x8ff8, v43\n" \
"v_mfma_f32_16x16x32_bf16 v[200:203], a[24:27], v[64:67], 0\n" \
"buffer_load_dwordx4 v5, s[80:83], s65 offen offset:1024 lds\n" \
"v_mfma_f32_16x16x32_bf16 v[200:203], a[28:31], v[68:71], v[200:203]\n" \
"v_add_u32_e32 v42, v42, v43\n" \
"v_readlane_b32 s88, v11, 11\n" \
"v_mfma_f32_16x16x32_bf16 v[200:203], a[192:195], v[72:75], v[200:203]\n" \
"v_readlane_b32 s89, v11, 12\n" \
"s_mov_b32 s90, 0x240000\n" \
"v_mfma_f32_16x16x32_bf16 v[204:207], a[192:195], v[96:99], 0\n" \
"ds_read_b64_tr_b16 a[192:193], v34 offset:18656\n" \
"ds_read_b64_tr_b16 a[194:195], v34 offset:23264\n" \
"v_mfma_f32_16x16x32_bf16 v[204:207], a[28:31], v[84:87], v[204:207]\n" \
"ds_read_b64_tr_b16 a[28:29], v34 offset:9440\n" \
"ds_read_b64_tr_b16 a[30:31], v34 offset:14048\n" \
"v_mfma_f32_16x16x32_bf16 v[204:207], a[24:27], v[80:83], v[204:207]\n" \
"ds_read_b64_tr_b16 a[24:25], v34 offset:224\n" \
"ds_read_b64_tr_b16 a[26:27], v34 offset:4832\n" \
"v_mfma_f32_16x16x32_bf16 v[136:139], a[196:199], a[220:223], 0\n" \
"buffer_load_dwordx4 v6, s[80:83], s65 offen offset:2048 lds\n" \
"v_mfma_f32_16x16x32_bf16 v[136:139], a[200:203], a[224:227], v[136:139]\n" \
"s_mov_b32 s91, 0x20000\n" \
"v_mfma_f32_16x16x32_bf16 v[136:139], a[204:207], a[228:231], v[136:139]\n" \
"v_mfma_f32_16x16x32_bf16 v[140:143], a[204:207], a[244:247], 0\n" \
"v_mfma_f32_16x16x32_bf16 v[140:143], a[200:203], a[240:243], v[140:143]\n" \
"v_mfma_f32_16x16x32_bf16 v[140:143], a[196:199], a[236:239], v[140:143]\n" \
"buffer_load_dwordx4 v7, s[80:83], s65 offen offset:3072 lds\n" \
"v_mfma_f32_16x16x32_bf16 v[208:211], a[196:199], v[64:67], 0\n" \
"s_add_u32 m0, s99, 0x8c00\n" \
"v_mfma_f32_16x16x32_bf16 v[208:211], a[200:203], v[68:71], v[208:211]\n" \
"v_mfma_f32_16x16x32_bf16 v[208:211], a[204:207], v[72:75], v[208:211]\n" \
"v_mfma_f32_16x16x32_bf16 v[212:215], a[204:207], v[96:99], 0\n" \
"ds_read_b64_tr_b16 a[204:205], v34 offset:18688\n" \
"ds_read_b64_tr_b16 a[206:207], v34 offset:23296\n" \
"v_mfma_f32_16x16x32_bf16 v[212:215], a[200:203], v[84:87], v[212:215]\n" \
"ds_read_b64_tr_b16 a[200:201], v34 offset:9472\n" \
"ds_read_b64_tr_b16 a[202:203], v34 offset:14080\n" \
"v_mfma_f32_16x16x32_bf16 v[212:215], a[196:199], v[80:83], v[212:215]\n" \
"ds_read_b64_tr_b16 a[196:197], v34 offset:256\n" \
"ds_read_b64_tr_b16 a[198:199], v34 offset:4864\n" \
"v_mfma_f32_16x16x32_bf16 v[144:147], a[208:211], a[220:223], 0\n" \
"buffer_load_dwordx4 v8, s[80:83], s65 offen offset:0 lds\n" \
"v_mfma_f32_16x16x32_bf16 v[144:147], a[212:215], a[224:227], v[144:147]\n" \
"s_waitcnt vmcnt(9)\n" \
"v_cmp_ne_u32_e32 vcc, s62, v37\n" \
"v_mfma_f32_16x16x32_bf16 v[144:147], a[216:219], a[228:231], v[144:147]\n" \
"s_add_u32 m0, s99, 0x14400\n" \
"v_readlane_b32 s65, v11, 42\n" \
"v_mfma_f32_16x16x32_bf16 v[148:151], a[216:219], a[244:247], 0\n" \
"v_mfma_f32_16x16x32_bf16 v[148:151], a[212:215], a[240:243], v[148:151]\n" \
"s_cbranch_vccnz L_sa_%=\n" \
"L_ra_%=:\n" \
"v_readlane_b32 s64, v11, 29\n" \
"v_mfma_f32_16x16x32_bf16 v[148:151], a[208:211], a[236:239], v[148:151]\n" \
"buffer_load_dwordx4 a[64:67], v20, s[56:59], s66 offen offset:0 sc1\n" \
"v_mfma_f32_16x16x32_bf16 v[216:219], a[208:211], v[64:67], 0\n" \
"v_mfma_f32_16x16x32_bf16 v[216:219], a[212:215], v[68:71], v[216:219]\n" \
"v_mfma_f32_16x16x32_bf16 v[216:219], a[216:219], v[72:75], v[216:219]\n" \
"v_mfma_f32_16x16x32_bf16 v[220:223], a[216:219], v[96:99], 0\n" \
"v_mfma_f32_16x16x32_bf16 v[220:223], a[212:215], v[84:87], v[220:223]\n" \
"buffer_load_dwordx4 a[68:71], v20, s[56:59], s66 offen offset:1024 sc1\n" \
"v_mfma_f32_16x16x32_bf16 v[220:223], a[208:211], v[80:83], v[220:223]\n" \
"s_waitcnt lgkmcnt(5)\n" \
"v_mfma_f32_16x16x32_bf16 v[152:155], a[0:3], a[220:223], 0\n" \
"v_mfma_f32_16x16x32_bf16 v[152:155], a[4:7], a[224:227], v[152:155]\n" \
"v_mfma_f32_16x16x32_bf16 v[152:155], a[8:11], a[228:231], v[152:155]\n" \
"v_mfma_f32_16x16x32_bf16 v[156:159], a[8:11], a[244:247], 0\n" \
"buffer_load_dwordx4 a[84:87], v32, s[56:59], s66 offen offset:1024 sc1\n" \
"v_mfma_f32_16x16x32_bf16 v[156:159], a[4:7], a[240:243], v[156:159]\n" \
"v_mfma_f32_16x16x32_bf16 v[156:159], a[0:3], a[236:239], v[156:159]\n" \
"v_mfma_f32_16x16x32_bf16 v[224:227], a[0:3], v[64:67], 0\n" \
"v_mfma_f32_16x16x32_bf16 v[224:227], a[4:7], v[68:71], v[224:227]\n" \
"v_mfma_f32_16x16x32_bf16 v[224:227], a[8:11], v[72:75], v[224:227]\n" \
"buffer_load_dwordx4 a[80:83], v32, s[56:59], s66 offen offset:0 sc1\n" \
"v_mfma_f32_16x16x32_bf16 v[228:231], a[8:11], v[96:99], 0\n" \
"v_mfma_f32_16x16x32_bf16 v[228:231], a[4:7], v[84:87], v[228:231]\n" \
"v_mfma_f32_16x16x32_bf16 v[228:231], a[0:3], v[80:83], v[228:231]\n" \
"v_mfma_f32_16x16x32_bf16 v[160:163], a[12:15], a[220:223], 0\n" \
"v_mfma_f32_16x16x32_bf16 v[160:163], a[16:19], a[224:227], v[160:163]\n" \
"buffer_load_dwordx4 a[72:75], v20, s[56:59], s66 offen offset:2048 sc1\n" \
"v_mfma_f32_16x16x32_bf16 v[160:163], a[20:23], a[228:231], v[160:163]\n" \
"v_mfma_f32_16x16x32_bf16 v[164:167], a[20:23], a[244:247], 0\n" \
"v_mfma_f32_16x16x32_bf16 v[164:167], a[16:19], a[240:243], v[164:167]\n" \
"v_mfma_f32_16x16x32_bf16 v[164:167], a[12:15], a[236:239], v[164:167]\n" \
"v_mfma_f32_16x16x32_bf16 v[232:235], a[12:15], v[64:67], 0\n" \
"buffer_load_dwordx4 a[76:79], v20, s[56:59], s66 offen offset:3072 sc1\n" \
"v_mfma_f32_16x16x32_bf16 v[232:235], a[16:19], v[68:71], v[232:235]\n" \
"v_mfma_f32_16x16x32_bf16 v[232:235], a[20:23], v[72:75], v[232:235]\n" \
"v_mfma_f32_16x16x32_bf16 v[236:239], a[20:23], v[96:99], 0\n" \
"v_mfma_f32_16x16x32_bf16 v[236:239], a[16:19], v[84:87], v[236:239]\n" \
"v_mfma_f32_16x16x32_bf16 v[236:239], a[12:15], v[80:83], v[236:239]\n" \
"buffer_load_dwordx4 a[92:95], v32, s[56:59], s66 offen offset:3072 sc1\n" \
"s_waitcnt lgkmcnt(0)\n" \
"s_barrier\n" \
"v_mfma_f32_16x16x32_bf16 v[168:171], a[24:27], a[220:223], 0\n" \
"ds_read_b64_tr_b16 a[208:209], v33 offset:9216\n" \
"ds_read_b64_tr_b16 a[210:211], v33 offset:13824\n" \
"v_mfma_f32_16x16x32_bf16 v[168:171], a[28:31], a[224:227], v[168:171]\n" \
"ds_read_b64_tr_b16 a[212:213], v33 offset:9248\n" \
"ds_read_b64_tr_b16 a[214:215], v33 offset:13856\n" \
"v_mfma_f32_16x16x32_bf16 v[168:171], a[192:195], a[228:231], v[168:171]\n" \
"ds_read_b64_tr_b16 a[216:217], v33 offset:9280\n" \
"ds_read_b64_tr_b16 a[218:219], v33 offset:13888\n" \
"s_mov_b64 exec, 1\n" \
"buffer_store_dword v39, off, s[56:59], s70 offset:0 sc1\n" \
"s_mov_b64 exec, -1\n" \
"v_mfma_f32_16x16x32_bf16 v[172:175], a[192:195], a[244:247], 0\n" \
"ds_read_b64_tr_b16 a[0:1], v33 offset:9312\n" \
"ds_read_b64_tr_b16 a[2:3], v33 offset:13920\n" \
"v_mfma_f32_16x16x32_bf16 v[172:175], a[28:31], a[240:243], v[172:175]\n" \
"ds_read_b64_tr_b16 a[4:5], v33 offset:9344\n" \
"ds_read_b64_tr_b16 a[6:7], v33 offset:13952\n" \
"v_mfma_f32_16x16x32_bf16 v[172:175], a[24:27], a[236:239], v[172:175]\n" \
"ds_read_b64_tr_b16 a[8:9], v33 offset:9376\n" \
"ds_read_b64_tr_b16 a[10:11], v33 offset:13984\n" \
"v_mfma_f32_16x16x32_bf16 v[240:243], a[24:27], v[64:67], 0\n" \
"ds_read_b64_tr_b16 a[12:13], v33 offset:9408\n" \
"ds_read_b64_tr_b16 a[14:15], v33 offset:14016\n" \
"v_mfma_f32_16x16x32_bf16 v[240:243], a[28:31], v[68:71], v[240:243]\n" \
"ds_read_b64_tr_b16 a[16:17], v33 offset:9440\n" \
"ds_read_b64_tr_b16 a[18:19], v33 offset:14048\n" \
"v_mfma_f32_16x16x32_bf16 v[240:243], a[192:195], v[72:75], v[240:243]\n" \
"ds_read_b64_tr_b16 a[20:21], v33 offset:9472\n" \
"ds_read_b64_tr_b16 a[22:23], v33 offset:14080\n" \
"v_mfma_f32_16x16x32_bf16 v[244:247], a[192:195], v[96:99], 0\n" \
"ds_read_b64_tr_b16 a[192:193], v35 offset:32\n" \
"ds_read_b64_tr_b16 a[194:195], v35 offset:2336\n" \
"v_mfma_f32_16x16x32_bf16 v[244:247], a[28:31], v[84:87], v[244:247]\n" \
"ds_read_b64_tr_b16 a[28:29], v35 offset:10368\n" \
"ds_read_b64_tr_b16 a[30:31], v35 offset:12672\n" \
"v_mfma_f32_16x16x32_bf16 v[244:247], a[24:27], v[80:83], v[244:247]\n" \
"ds_read_b64_tr_b16 a[24:25], v35 offset:0\n" \
"ds_read_b64_tr_b16 a[26:27], v35 offset:2304\n" \
"v_mfma_f32_16x16x32_bf16 v[176:179], a[196:199], a[220:223], 0\n" \
"buffer_load_dwordx4 a[88:91], v32, s[56:59], s66 offen offset:2048 sc1\n" \
"v_mfma_f32_16x16x32_bf16 v[176:179], a[200:203], a[224:227], v[176:179]\n" \
"v_readlane_b32 s66, v11, 43\n" \
"v_readlane_b32 s70, v11, 5\n" \
"v_mfma_f32_16x16x32_bf16 v[176:179], a[204:207], a[228:231], v[176:179]\n" \
"v_mfma_f32_16x16x32_bf16 v[180:183], a[204:207], a[244:247], 0\n" \
"v_mfma_f32_16x16x32_bf16 v[180:183], a[200:203], a[240:243], v[180:183]\n" \
"v_mfma_f32_16x16x32_bf16 v[180:183], a[196:199], a[236:239], v[180:183]\n" \
"L_pf21:\n" \
"s_cbranch_execz L_pf23\n" \
"s_nop 0\n" \
"buffer_load_dwordx4 v0, s[80:83], s67 offen offset:0 lds\n" \
"v_mfma_f32_16x16x32_bf16 v[248:251], a[196:199], v[64:67], 0\n" \
"v_mfma_f32_16x16x32_bf16 v[248:251], a[200:203], v[68:71], v[248:251]\n" \
"v_mfma_f32_16x16x32_bf16 v[248:251], a[204:207], v[72:75], v[248:251]\n" \
"v_mfma_f32_16x16x32_bf16 v[252:255], a[204:207], v[96:99], 0\n" \
"ds_read_b64_tr_b16 a[204:205], v35 offset:10432\n" \
"ds_read_b64_tr_b16 a[206:207], v35 offset:12736\n" \
"v_mfma_f32_16x16x32_bf16 v[252:255], a[200:203], v[84:87], v[252:255]\n" \
"ds_read_b64_tr_b16 a[200:201], v35 offset:64\n" \
"ds_read_b64_tr_b16 a[202:203], v35 offset:2368\n" \
"v_mfma_f32_16x16x32_bf16 v[252:255], a[196:199], v[80:83], v[252:255]\n" \
"ds_read_b64_tr_b16 a[196:197], v35 offset:10400\n" \
"s_waitcnt lgkmcnt(9)\n" \
"v_mfma_f32_16x16x32_bf16 v[112:115], a[208:211], a[232:235], v[112:115]\n" \
"buffer_load_dwordx4 v1, s[80:83], s67 offen offset:1024 lds\n" \
"v_mfma_f32_16x16x32_bf16 v[116:119], a[208:211], a[248:251], v[116:119]\n" \
"ds_read_b64_tr_b16 a[198:199], v35 offset:12704\n" \
"v_mfma_f32_16x16x32_bf16 v[184:187], a[208:211], v[76:79], v[184:187]\n" \
"v_mfma_f32_16x16x32_bf16 v[188:191], a[208:211], v[100:103], v[188:191]\n" \
"ds_read_b64_tr_b16 a[208:209], v35 offset:96\n" \
"ds_read_b64_tr_b16 a[210:211], v35 offset:2400\n" \
"v_mfma_f32_16x16x32_bf16 v[120:123], a[212:215], a[232:235], v[120:123]\n" \
"v_mfma_f32_16x16x32_bf16 v[124:127], a[212:215], a[248:251], v[124:127]\n" \
"buffer_load_dwordx4 v2, s[80:83], s67 offen offset:2048 lds\n" \
"v_mfma_f32_16x16x32_bf16 v[192:195], a[212:215], v[76:79], v[192:195]\n" \
"v_mfma_f32_16x16x32_bf16 v[196:199], a[212:215], v[100:103], v[196:199]\n" \
"ds_read_b64_tr_b16 a[212:213], v35 offset:10464\n" \
"ds_read_b64_tr_b16 a[214:215], v35 offset:12768\n" \
"v_mfma_f32_16x16x32_bf16 v[128:131], a[216:219], a[232:235], v[128:131]\n" \
"v_mfma_f32_16x16x32_bf16 v[132:135], a[216:219], a[248:251], v[132:135]\n" \
"v_mfma_f32_16x16x32_bf16 v[200:203], a[216:219], v[76:79], v[200:203]\n" \
"buffer_load_dwordx4 v3, s[80:83], s67 offen offset:3072 lds\n" \
"v_mfma_f32_16x16x32_bf16 v[204:207], a[216:219], v[100:103], v[204:207]\n" \
"ds_read_b64_tr_b16 a[216:217], v35 offset:128\n" \
"ds_read_b64_tr_b16 a[218:219], v35 offset:2432\n" \
"v_mfma_f32_16x16x32_bf16 v[136:139], a[0:3], a[232:235], v[136:139]\n" \
"s_add_u32 m0, s99, 0x15400\n" \
"v_mfma_f32_16x16x32_bf16 v[140:143], a[0:3], a[248:251], v[140:143]\n" \
"v_mfma_f32_16x16x32_bf16 v[208:211], a[0:3], v[76:79], v[208:211]\n" \
"v_mfma_f32_16x16x32_bf16 v[212:215], a[0:3], v[100:103], v[212:215]\n" \
"ds_read_b64_tr_b16 a[0:1], v35 offset:10496\n" \
"ds_read_b64_tr_b16 a[2:3], v35 offset:12800\n" \
"v_mfma_f32_16x16x32_bf16 v[144:147], a[4:7], a[232:235], v[144:147]\n" \
"buffer_load_dwordx4 v4, s[80:83], s67 offen offset:0 lds\n" \
"v_mfma_f32_16x16x32_bf16 v[148:151], a[4:7], a[248:251], v[148:151]\n" \
"v_mfma_f32_16x16x32_bf16 v[216:219], a[4:7], v[76:79], v[216:219]\n" \
"v_mfma_f32_16x16x32_bf16 v[220:223], a[4:7], v[100:103], v[220:223]\n" \
"ds_read_b64_tr_b16 a[4:5], v35 offset:160\n" \
"ds_read_b64_tr_b16 a[6:7], v35 offset:2464\n" \
"v_mfma_f32_16x16x32_bf16 v[152:155], a[8:11], a[232:235], v[152:155]\n" \
"v_mfma_f32_16x16x32_bf16 v[156:159], a[8:11], a[248:251], v[156:159]\n" \
"buffer_load_dwordx4 v5, s[80:83], s67 offen offset:1024 lds\n" \
"v_mfma_f32_16x16x32_bf16 v[224:227], a[8:11], v[76:79], v[224:227]\n" \
"v_mfma_f32_16x16x32_bf16 v[228:231], a[8:11], v[100:103], v[228:231]\n" \
"ds_read_b64_tr_b16 a[8:9], v35 offset:10528\n" \
"ds_read_b64_tr_b16 a[10:11], v35 offset:12832\n" \
"v_mfma_f32_16x16x32_bf16 v[160:163], a[12:15], a[232:235], v[160:163]\n" \
"v_mfma_f32_16x16x32_bf16 v[164:167], a[12:15], a[248:251], v[164:167]\n" \
"v_mfma_f32_16x16x32_bf16 v[232:235], a[12:15], v[76:79], v[232:235]\n" \
"buffer_load_dwordx4 v6, s[80:83], s67 offen offset:2048 lds\n" \
"v_mfma_f32_16x16x32_bf16 v[236:239], a[12:15], v[100:103], v[236:239]\n" \
"ds_read_b64_tr_b16 a[12:13], v35 offset:192\n" \
"ds_read_b64_tr_b16 a[14:15], v35 offset:2496\n" \
"v_mfma_f32_16x16x32_bf16 v[168:171], a[16:19], a[232:235], v[168:171]\n" \
"v_mfma_f32_16x16x32_bf16 v[172:175], a[16:19], a[248:251], v[172:175]\n" \
"v_mfma_f32_16x16x32_bf16 v[240:243], a[16:19], v[76:79], v[240:243]\n" \
"v_mfma_f32_16x16x32_bf16 v[244:247], a[16:19], v[100:103], v[244:247]\n" \
"buffer_load_dwordx4 v7, s[80:83], s67 offen offset:3072 lds\n" \
"v_mfma_f32_16x16x32_bf16 v[176:179], a[20:23], a[232:235], v[176:179]\n" \
"s_add_u32 m0, s99, 0x16400\n" \
"ds_read_b64_tr_b16 a[16:17], v35 offset:10560\n" \
"v_mfma_f32_16x16x32_bf16 v[180:183], a[20:23], a[248:251], v[180:183]\n" \
"ds_read_b64_tr_b16 a[18:19], v35 offset:12864\n" \
"v_mfma_f32_16x16x32_bf16 v[248:251], a[20:23], v[76:79], v[248:251]\n" \
"v_mfma_f32_16x16x32_bf16 v[252:255], a[20:23], v[100:103], v[252:255]\n" \
"ds_read_b64_tr_b16 a[20:21], v35 offset:224\n" \
"s_waitcnt lgkmcnt(7)\n" \
"v_mfma_f32_16x16x32_bf16 v[112:115], a[24:27], a[32:35], v[112:115]\n" \
"buffer_load_dwordx4 v8, s[80:83], s67 offen offset:0 lds\n" \
"v_mfma_f32_16x16x32_bf16 v[112:115], a[28:31], a[36:39], v[112:115]\n" \
"s_add_u32 m0, s92, 0x0\n" \
"ds_read_b64_tr_b16 a[22:23], v35 offset:2528\n" \
"v_mfma_f32_16x16x32_bf16 v[116:119], a[28:31], a[44:47], v[116:119]\n" \
"v_readlane_b32 s67, v11, 44\n" \
"v_mfma_f32_16x16x32_bf16 v[116:119], a[24:27], a[40:43], v[116:119]\n" \
"v_mfma_f32_16x16x32_bf16 v[184:187], a[24:27], a[48:51], v[184:187]\n" \
"v_mfma_f32_16x16x32_bf16 v[184:187], a[28:31], a[52:55], v[184:187]\n" \
"buffer_load_dword v47, v38, s[56:59], s71 offen sc1\n" \
"v_mfma_f32_16x16x32_bf16 v[188:191], a[28:31], a[60:63], v[188:191]\n" \
"ds_read_b64_tr_b16 a[28:29], v35 offset:256\n" \
"ds_read_b64_tr_b16 a[30:31], v35 offset:2560\n" \
"v_mfma_f32_16x16x32_bf16 v[188:191], a[24:27], a[56:59], v[188:191]\n" \
"ds_read_b64_tr_b16 a[24:25], v35 offset:10592\n" \
"ds_read_b64_tr_b16 a[26:27], v35 offset:12896\n" \
"v_mfma_f32_16x16x32_bf16 v[120:123], a[192:195], a[32:35], v[120:123]\n" \
"v_mfma_f32_16x16x32_bf16 v[120:123], a[196:199], a[36:39], v[120:123]\n" \
"v_mfma_f32_16x16x32_bf16 v[124:127], a[196:199], a[44:47], v[124:127]\n" \
"buffer_load_dwordx4 v0, s[80:83], s73 offen offset:0 lds\n" \
"v_mfma_f32_16x16x32_bf16 v[124:127], a[192:195], a[40:43], v[124:127]\n" \
"v_mfma_f32_16x16x32_bf16 v[192:195], a[192:195], a[48:51], v[192:195]\n" \
"v_mfma_f32_16x16x32_bf16 v[192:195], a[196:199], a[52:55], v[192:195]\n" \
"v_mfma_f32_16x16x32_bf16 v[196:199], a[196:199], a[60:63], v[196:199]\n" \
"v_mfma_f32_16x16x32_bf16 v[196:199], a[192:195], a[56:59], v[196:199]\n" \
"buffer_load_dwordx4 v1, s[80:83], s73 offen offset:1024 lds\n" \
"v_mfma_f32_16x16x32_bf16 v[128:131], a[200:203], a[32:35], v[128:131]\n" \
"ds_read_b64_tr_b16 a[192:193], v35 offset:10624\n" \
"ds_read_b64_tr_b16 a[194:195], v35 offset:12928\n" \
"v_mfma_f32_16x16x32_bf16 v[128:131], a[204:207], a[36:39], v[128:131]\n" \
"v_mfma_f32_16x16x32_bf16 v[132:135], a[204:207], a[44:47], v[132:135]\n" \
"v_mfma_f32_16x16x32_bf16 v[132:135], a[200:203], a[40:43], v[132:135]\n" \
"v_mfma_f32_16x16x32_bf16 v[200:203], a[200:203], a[48:51], v[200:203]\n" \
"buffer_load_dwordx4 v2, s[80:83], s73 offen offset:2048 lds\n" \
"v_mfma_f32_16x16x32_bf16 v[200:203], a[204:207], a[52:55], v[200:203]\n" \
"v_mfma_f32_16x16x32_bf16 v[204:207], a[204:207], a[60:63], v[204:207]\n" \
"v_mfma_f32_16x16x32_bf16 v[204:207], a[200:203], a[56:59], v[204:207]\n" \
"v_mfma_f32_16x16x32_bf16 v[136:139], a[208:211], a[32:35], v[136:139]\n" \
"v_mfma_f32_16x16x32_bf16 v[136:139], a[212:215], a[36:39], v[136:139]\n" \
"buffer_load_dwordx4 v3, s[80:83], s73 offen offset:3072 lds\n" \
"v_mfma_f32_16x16x32_bf16 v[140:143], a[212:215], a[44:47], v[140:143]\n" \
"s_add_u32 m0, s92, 0x1000\n" \
"v_mfma_f32_16x16x32_bf16 v[140:143], a[208:211], a[40:43], v[140:143]\n" \
"v_mfma_f32_16x16x32_bf16 v[208:211], a[208:211], a[48:51], v[208:211]\n" \
"v_mfma_f32_16x16x32_bf16 v[208:211], a[212:215], a[52:55], v[208:211]\n" \
"v_mfma_f32_16x16x32_bf16 v[212:215], a[212:215], a[60:63], v[212:215]\n" \
"buffer_load_dwordx4 v4, s[80:83], s73 offen offset:0 lds\n" \
"v_mfma_f32_16x16x32_bf16 v[212:215], a[208:211], a[56:59], v[212:215]\n" \
"v_mfma_f32_16x16x32_bf16 v[144:147], a[216:219], a[32:35], v[144:147]\n" \
"v_mfma_f32_16x16x32_bf16 v[144:147], a[0:3], a[36:39], v[144:147]\n" \
"v_mfma_f32_16x16x32_bf16 v[148:151], a[0:3], a[44:47], v[148:151]\n" \
"v_mfma_f32_16x16x32_bf16 v[148:151], a[216:219], a[40:43], v[148:151]\n" \
"buffer_load_dwordx4 v5, s[80:83], s73 offen offset:1024 lds\n" \
"v_mfma_f32_16x16x32_bf16 v[216:219], a[216:219], a[48:51], v[216:219]\n" \
"v_mfma_f32_16x16x32_bf16 v[216:219], a[0:3], a[52:55], v[216:219]\n" \
"v_mfma_f32_16x16x32_bf16 v[220:223], a[0:3], a[60:63], v[220:223]\n" \
"v_mfma_f32_16x16x32_bf16 v[220:223], a[216:219], a[56:59], v[220:223]\n" \
"v_mfma_f32_16x16x32_bf16 v[152:155], a[4:7], a[32:35], v[152:155]\n" \
"buffer_load_dwordx4 v6, s[80:83], s73 offen offset:2048 lds\n" \
"s_waitcnt lgkmcnt(0)\n" \
"v_mfma_f32_16x16x32_bf16 v[152:155], a[8:11], a[36:39], v[152:155]\n" \
"v_mfma_f32_16x16x32_bf16 v[156:159], a[8:11], a[44:47], v[156:159]\n" \
"v_mfma_f32_16x16x32_bf16 v[156:159], a[4:7], a[40:43], v[156:159]\n" \
"v_mfma_f32_16x16x32_bf16 v[224:227], a[4:7], a[48:51], v[224:227]\n" \
"buffer_load_dwordx4 v7, s[80:83], s73 offen offset:3072 lds\n" \
"s_add_u32 m0, s92, 0x2000\n" \
"v_mfma_f32_16x16x32_bf16 v[224:227], a[8:11], a[52:55], v[224:227]\n" \
"v_mfma_f32_16x16x32_bf16 v[228:231], a[8:11], a[60:63], v[228:231]\n" \
"v_mfma_f32_16x16x32_bf16 v[228:231], a[4:7], a[56:59], v[228:231]\n" \
"s_waitcnt vmcnt(23)\n" \
"s_barrier\n" \
"buffer_load_dwordx4 v8, s[80:83], s73 offen offset:0 lds\n" \
"s_mov_b32 m0, s94\n" \
"v_mfma_f32_16x16x32_bf16 v[160:163], a[12:15], a[32:35], v[160:163]\n" \
"ds_read_b64_tr_b16 a[196:197], v10 offset:27648\n" \
"ds_read_b64_tr_b16 a[198:199], v10 offset:32256\n" \
"v_mfma_f32_16x16x32_bf16 v[160:163], a[16:19], a[36:39], v[160:163]\n" \
"ds_read_b64_tr_b16 a[200:201], v10 offset:36864\n" \
"ds_read_b64_tr_b16 a[202:203], v10 offset:41472\n" \
"v_mfma_f32_16x16x32_bf16 v[164:167], a[16:19], a[44:47], v[164:167]\n" \
"ds_read_b64_tr_b16 a[204:205], v10 offset:27680\n" \
"ds_read_b64_tr_b16 a[206:207], v10 offset:32288\n" \
"v_mfma_f32_16x16x32_bf16 v[164:167], a[12:15], a[40:43], v[164:167]\n" \
"buffer_load_dwordx4 v9, s[84:87], 0 offen lds\n" \
"v_mfma_f32_16x16x32_bf16 v[232:235], a[12:15], a[48:51], v[232:235]\n" \
"ds_read_b64_tr_b16 a[208:209], v10 offset:36896\n" \
"ds_read_b64_tr_b16 a[210:211], v10 offset:41504\n" \
"v_mfma_f32_16x16x32_bf16 v[232:235], a[16:19], a[52:55], v[232:235]\n" \
"ds_read_b64_tr_b16 a[212:213], v10 offset:27712\n" \
"ds_read_b64_tr_b16 a[214:215], v10 offset:32320\n" \
"v_mfma_f32_16x16x32_bf16 v[236:239], a[16:19], a[60:63], v[236:239]\n" \
"ds_read_b64_tr_b16 a[216:217], v10 offset:36928\n" \
"ds_read_b64_tr_b16 a[218:219], v10 offset:41536\n" \
"v_mfma_f32_16x16x32_bf16 v[236:239], a[12:15], a[56:59], v[236:239]\n" \
"ds_read_b64_tr_b16 a[0:1], v10 offset:27744\n" \
"ds_read_b64_tr_b16 a[2:3], v10 offset:32352\n" \
"buffer_load_dwordx4 a[128:131], v20, s[56:59], s68 offen offset:0 sc1\n" \
"v_mfma_f32_16x16x32_bf16 v[168:171], a[20:23], a[32:35], v[168:171]\n" \
"ds_read_b64_tr_b16 a[4:5], v10 offset:36960\n" \
"ds_read_b64_tr_b16 a[6:7], v10 offset:41568\n" \
"v_mfma_f32_16x16x32_bf16 v[168:171], a[24:27], a[36:39], v[168:171]\n" \
"ds_read_b64_tr_b16 a[8:9], v10 offset:27776\n" \
"ds_read_b64_tr_b16 a[10:11], v10 offset:32384\n" \
"v_mfma_f32_16x16x32_bf16 v[172:175], a[24:27], a[44:47], v[172:175]\n" \
"ds_read_b64_tr_b16 a[12:13], v10 offset:36992\n" \
"ds_read_b64_tr_b16 a[14:15], v10 offset:41600\n" \
"v_mfma_f32_16x16x32_bf16 v[172:175], a[20:23], a[40:43], v[172:175]\n" \
"ds_read_b64_tr_b16 a[16:17], v10 offset:27808\n" \
"ds_read_b64_tr_b16 a[18:19], v10 offset:32416\n" \
"buffer_load_dwordx4 a[132:135], v20, s[56:59], s68 offen offset:1024 sc1\n" \
"v_mfma_f32_16x16x32_bf16 v[240:243], a[20:23], a[48:51], v[240:243]\n" \
"s_waitcnt vmcnt(12)\n" \
"v_cmp_ne_u32_e32 vcc, s62, v47\n" \
"v_mfma_f32_16x16x32_bf16 v[240:243], a[24:27], a[52:55], v[240:243]\n" \
"v_readlane_b32 s94, v11, 47\n" \
"s_mov_b32 s84, 0\n" \
"v_mfma_f32_16x16x32_bf16 v[244:247], a[24:27], a[60:63], v[244:247]\n" \
"ds_read_b64_tr_b16 a[24:25], v10 offset:27840\n" \
"ds_read_b64_tr_b16 a[26:27], v10 offset:32448\n" \
"v_mfma_f32_16x16x32_bf16 v[244:247], a[20:23], a[56:59], v[244:247]\n" \
"ds_read_b64_tr_b16 a[20:21], v10 offset:37024\n" \
"ds_read_b64_tr_b16 a[22:23], v10 offset:41632\n" \
"buffer_load_dwordx4 a[136:139], v20, s[56:59], s68 offen offset:2048 sc1\n" \
"v_mfma_f32_16x16x32_bf16 v[176:179], a[28:31], a[32:35], v[176:179]\n" \
"s_cbranch_vccnz L_sb_%=\n" \
"L_rb_%=:\n" \
"v_readlane_b32 s71, v11, 46\n" \
"v_mfma_f32_16x16x32_bf16 v[176:179], a[192:195], a[36:39], v[176:179]\n" \
"v_add_u32_e32 v38, 0x1000, v20\n" \
"v_add_u32_e32 v47, 0x2000, v20\n" \
"v_mfma_f32_16x16x32_bf16 v[180:183], a[192:195], a[44:47], v[180:183]\n" \
"v_cmp_eq_u32_e32 vcc, 0, v44\n" \
"v_mfma_f32_16x16x32_bf16 v[180:183], a[28:31], a[40:43], v[180:183]\n" \
"v_mfma_f32_16x16x32_bf16 v[248:251], a[28:31], a[48:51], v[248:251]\n" \
"buffer_load_dwordx4 a[140:143], v20, s[56:59], s68 offen offset:3072 sc1\n" \
"v_mfma_f32_16x16x32_bf16 v[248:251], a[192:195], a[52:55], v[248:251]\n" \
"v_cndmask_b32_e32 v50, 0, v45, vcc\n" \
"v_cmp_eq_u32_e32 vcc, 1, v44\n" \
"v_mfma_f32_16x16x32_bf16 v[252:255], a[192:195], a[60:63], v[252:255]\n" \
"ds_read_b64_tr_b16 a[192:193], v10 offset:27872\n" \
"ds_read_b64_tr_b16 a[194:195], v10 offset:32480\n" \
"v_mfma_f32_16x16x32_bf16 v[252:255], a[28:31], a[56:59], v[252:255]\n" \
"ds_read_b64_tr_b16 a[28:29], v10 offset:37056\n" \
"s_waitcnt lgkmcnt(9)\n" \
"v_mfma_f32_16x16x32_bf16 v[112:115], a[196:199], a[64:67], v[112:115]\n" \
"ds_read_b64_tr_b16 a[30:31], v10 offset:41664\n" \
"v_cndmask_b32_e32 v51, 0, v45, vcc\n" \
"v_mfma_f32_16x16x32_bf16 v[112:115], a[200:203], a[68:71], v[112:115]\n" \
"buffer_load_dwordx4 a[156:159], v32, s[56:59], s68 offen offset:3072 sc1\n" \
"v_mfma_f32_16x16x32_bf16 v[116:119], a[200:203], a[84:87], v[116:119]\n" \
"ds_read_b64_tr_b16 a[200:201], v10 offset:27904\n" \
"ds_read_b64_tr_b16 a[202:203], v10 offset:32512\n" \
"v_mfma_f32_16x16x32_bf16 v[116:119], a[196:199], a[80:83], v[116:119]\n" \
"ds_read_b64_tr_b16 a[196:197], v10 offset:37088\n" \
"ds_read_b64_tr_b16 a[198:199], v10 offset:41696\n" \
"v_mfma_f32_16x16x32_bf16 v[120:123], a[204:207], a[64:67], v[120:123]\n" \
"v_bfe_u32 v45, v20, 1, 5\n" \
"v_and_b32_e32 v45, 24, v45\n" \
"v_mfma_f32_16x16x32_bf16 v[120:123], a[208:211], a[68:71], v[120:123]\n" \
"v_and_b32_e32 v44, 0x3c0, v20\n" \
"v_add_u32_e32 v45, 0x1d400, v45\n" \
"v_mfma_f32_16x16x32_bf16 v[124:127], a[208:211], a[84:87], v[124:127]\n" \
"buffer_load_dwordx4 a[152:155], v32, s[56:59], s68 offen offset:2048 sc1\n" \
"v_mfma_f32_16x16x32_bf16 v[124:127], a[204:207], a[80:83], v[124:127]\n" \
"ds_read_b64_tr_b16 a[204:205], v10 offset:37120\n" \
"ds_read_b64_tr_b16 a[206:207], v10 offset:41728\n" \
"v_mfma_f32_16x16x32_bf16 v[128:131], a[212:215], a[64:67], v[128:131]\n" \
"ds_read_b64_tr_b16 a[208:209], v10 offset:46080\n" \
"ds_read_b64_tr_b16 a[210:211], v10 offset:50688\n" \
"v_mfma_f32_16x16x32_bf16 v[128:131], a[216:219], a[68:71], v[128:131]\n" \
"v_add_u32_e32 v36, v44, v45\n" \
"v_mfma_f32_16x16x32_bf16 v[132:135], a[216:219], a[84:87], v[132:135]\n" \
"ds_read_b64_tr_b16 a[216:217], v10 offset:46112\n" \
"ds_read_b64_tr_b16 a[218:219], v10 offset:50720\n" \
"buffer_load_dwordx4 a[148:151], v32, s[56:59], s68 offen offset:1024 sc1\n" \
"v_mfma_f32_16x16x32_bf16 v[132:135], a[212:215], a[80:83], v[132:135]\n" \
"ds_read_b64_tr_b16 a[212:213], v10 offset:55296\n" \
"ds_read_b64_tr_b16 a[214:215], v10 offset:59904\n" \
"v_mfma_f32_16x16x32_bf16 v[136:139], a[0:3], a[64:67], v[136:139]\n" \
"v_mfma_f32_16x16x32_bf16 v[136:139], a[4:7], a[68:71], v[136:139]\n" \
"v_mfma_f32_16x16x32_bf16 v[140:143], a[4:7], a[84:87], v[140:143]\n" \
"ds_read_b64_tr_b16 a[4:5], v10 offset:46144\n" \
"ds_read_b64_tr_b16 a[6:7], v10 offset:50752\n" \
"buffer_load_dwordx4 a[144:147], v32, s[56:59], s68 offen offset:0 sc1\n" \
"v_mfma_f32_16x16x32_bf16 v[140:143], a[0:3], a[80:83], v[140:143]\n" \
"ds_read_b64_tr_b16 a[0:1], v10 offset:55328\n" \
"ds_read_b64_tr_b16 a[2:3], v10 offset:59936\n" \
"v_mfma_f32_16x16x32_bf16 v[144:147], a[8:11], a[64:67], v[144:147]\n" \
"v_readlane_b32 s68, v11, 45\n" \
"v_mfma_f32_16x16x32_bf16 v[144:147], a[12:15], a[68:71], v[144:147]\n" \
"v_mfma_f32_16x16x32_bf16 v[148:151], a[12:15], a[84:87], v[148:151]\n" \
"ds_read_b64_tr_b16 a[12:13], v10 offset:46176\n" \
"ds_read_b64_tr_b16 a[14:15], v10 offset:50784\n" \
"buffer_load_dwordx4 a[96:99], v20, s[56:59], s69 offen offset:0 sc1\n" \
"v_mfma_f32_16x16x32_bf16 v[148:151], a[8:11], a[80:83], v[148:151]\n" \
"ds_read_b64_tr_b16 a[8:9], v10 offset:55360\n" \
"s_waitcnt lgkmcnt(15)\n" \
"v_mfma_f32_16x16x32_bf16 v[152:155], a[16:19], a[64:67], v[152:155]\n" \
"ds_read_b64_tr_b16 a[10:11], v10 offset:59968\n" \
"v_mfma_f32_16x16x32_bf16 v[152:155], a[20:23], a[68:71], v[152:155]\n" \
"v_mfma_f32_16x16x32_bf16 v[156:159], a[20:23], a[84:87], v[156:159]\n" \
"ds_read_b64_tr_b16 a[20:21], v10 offset:46208\n" \
"ds_read_b64_tr_b16 a[22:23], v10 offset:50816\n" \
"buffer_load_dwordx4 a[100:103], v20, s[56:59], s69 offen offset:1024 sc1\n" \
"v_mfma_f32_16x16x32_bf16 v[156:159], a[16:19], a[80:83], v[156:159]\n" \
"ds_read_b64_tr_b16 a[16:17], v10 offset:55392\n" \
"ds_read_b64_tr_b16 a[18:19], v10 offset:60000\n" \
"v_mfma_f32_16x16x32_bf16 v[160:163], a[24:27], a[64:67], v[160:163]\n" \
"v_mfma_f32_16x16x32_bf16 v[160:163], a[28:31], a[68:71], v[160:163]\n" \
"v_mfma_f32_16x16x32_bf16 v[164:167], a[28:31], a[84:87], v[164:167]\n" \
"ds_read_b64_tr_b16 a[28:29], v10 offset:46240\n" \
"ds_read_b64_tr_b16 a[30:31], v10 offset:50848\n" \
"buffer_load_dwordx4 a[104:107], v20, s[56:59], s69 offen offset:2048 sc1\n" \
"v_mfma_f32_16x16x32_bf16 v[164:167], a[24:27], a[80:83], v[164:167]\n" \
"ds_read_b64_tr_b16 a[24:25], v10 offset:55424\n" \
"ds_read_b64_tr_b16 a[26:27], v10 offset:60032\n" \
"v_mfma_f32_16x16x32_bf16 v[168:171], a[192:195], a[64:67], v[168:171]\n" \
"v_mfma_f32_16x16x32_bf16 v[168:171], a[196:199], a[68:71], v[168:171]\n" \
"v_mfma_f32_16x16x32_bf16 v[172:175], a[196:199], a[84:87], v[172:175]\n" \
"ds_read_b64_tr_b16 a[196:197], v10 offset:46272\n" \
"ds_read_b64_tr_b16 a[198:199], v10 offset:50880\n" \
"buffer_load_dwordx4 a[108:111], v20, s[56:59], s69 offen offset:3072 sc1\n" \
"v_mfma_f32_16x16x32_bf16 v[172:175], a[192:195], a[80:83], v[172:175]\n" \
"ds_read_b64_tr_b16 a[192:193], v10 offset:55456\n" \
"ds_read_b64_tr_b16 a[194:195], v10 offset:60064\n" \
"v_mfma_f32_16x16x32_bf16 v[176:179], a[200:203], a[64:67], v[176:179]\n" \
"s_waitcnt lgkmcnt(13)\n" \
"v_mfma_f32_16x16x32_bf16 v[176:179], a[204:207], a[68:71], v[176:179]\n" \
"v_mfma_f32_16x16x32_bf16 v[180:183], a[204:207], a[84:87], v[180:183]\n" \
"ds_read_b64_tr_b16 a[204:205], v10 offset:46304\n" \
"ds_read_b64_tr_b16 a[206:207], v10 offset:50912\n" \
"buffer_load_dwordx4 a[124:127], v32, s[56:59], s69 offen offset:3072 sc1\n" \
"v_mfma_f32_16x16x32_bf16 v[180:183], a[200:203], a[80:83], v[180:183]\n" \
"ds_read_b64_tr_b16 a[200:201], v10 offset:55488\n" \
"ds_read_b64_tr_b16 a[202:203], v10 offset:60096\n" \
"v_mfma_f32_16x16x32_bf16 v[112:115], a[208:211], a[72:75], v[112:115]\n" \
"v_mfma_f32_16x16x32_bf16 v[112:115], a[212:215], a[76:79], v[112:115]\n" \
"v_mfma_f32_16x16x32_bf16 v[116:119], a[212:215], a[92:95], v[116:119]\n" \
"ds_read_b64_tr_b16 a[212:213], v10 offset:46336\n" \
"ds_read_b64_tr_b16 a[214:215], v10 offset:50944\n" \
"buffer_load_dwordx4 a[120:123], v32, s[56:59], s69 offen offset:2048 sc1\n" \
"v_mfma_f32_16x16x32_bf16 v[116:119], a[208:211], a[88:91], v[116:119]\n" \
"ds_read_b64_tr_b16 a[208:209], v10 offset:55520\n" \
"ds_read_b64_tr_b16 a[210:211], v10 offset:60128\n" \
"v_mfma_f32_16x16x32_bf16 v[120:123], a[216:219], a[72:75], v[120:123]\n" \
"v_mfma_f32_16x16x32_bf16 v[120:123], a[0:3], a[76:79], v[120:123]\n" \
"v_mfma_f32_16x16x32_bf16 v[124:127], a[0:3], a[92:95], v[124:127]\n" \
"v_mfma_f32_16x16x32_bf16 v[124:127], a[216:219], a[88:91], v[124:127]\n" \
"buffer_load_dwordx4 a[116:119], v32, s[56:59], s69 offen offset:1024 sc1\n" \
"v_mfma_f32_16x16x32_bf16 v[128:131], a[4:7], a[72:75], v[128:131]\n" \
"ds_read_b64_tr_b16 a[216:217], v10 offset:55552\n" \
"s_waitcnt lgkmcnt(9)\n" \
"v_mfma_f32_16x16x32_bf16 v[128:131], a[8:11], a[76:79], v[128:131]\n" \
"ds_read_b64_tr_b16 a[218:219], v10 offset:60160\n" \
"v_mfma_f32_16x16x32_bf16 v[132:135], a[8:11], a[92:95], v[132:135]\n" \
"v_mfma_f32_16x16x32_bf16 v[132:135], a[4:7], a[88:91], v[132:135]\n" \
"v_mfma_f32_16x16x32_bf16 v[136:139], a[12:15], a[72:75], v[136:139]\n" \
"buffer_load_dwordx4 a[112:115], v32, s[56:59], s69 offen offset:0 sc1\n" \
"v_mfma_f32_16x16x32_bf16 v[136:139], a[16:19], a[76:79], v[136:139]\n" \
"v_readlane_b32 s69, v11, 49\n" \
"v_mfma_f32_16x16x32_bf16 v[140:143], a[16:19], a[92:95], v[140:143]\n" \
"v_mfma_f32_16x16x32_bf16 v[140:143], a[12:15], a[88:91], v[140:143]\n" \
"s_waitcnt vmcnt(16)\n" \
"s_barrier\n" \
"buffer_load_dwordx4 a[32:35], v20, s[56:59], s64 offen offset:0 sc1\n" \
"v_mfma_f32_16x16x32_bf16 v[144:147], a[20:23], a[72:75], v[144:147]\n" \
"ds_read_b64_tr_b16 a[0:1], v33 offset:18432\n" \
"ds_read_b64_tr_b16 a[2:3], v33 offset:23040\n" \
"v_mfma_f32_16x16x32_bf16 v[144:147], a[24:27], a[76:79], v[144:147]\n" \
"ds_read_b64_tr_b16 a[4:5], v33 offset:27648\n" \
"ds_read_b64_tr_b16 a[6:7], v33 offset:32256\n" \
"v_mfma_f32_16x16x32_bf16 v[148:151], a[24:27], a[92:95], v[148:151]\n" \
"ds_read_b64_tr_b16 a[8:9], v33 offset:36864\n" \
"ds_read_b64_tr_b16 a[10:11], v33 offset:41472\n" \
"v_mfma_f32_16x16x32_bf16 v[148:151], a[20:23], a[88:91], v[148:151]\n" \
"ds_read_b64_tr_b16 a[12:13], v33 offset:46080\n" \
"ds_read_b64_tr_b16 a[14:15], v33 offset:50688\n" \
"buffer_load_dwordx4 a[36:39], v20, s[56:59], s64 offen offset:1024 sc1\n" \
"v_mfma_f32_16x16x32_bf16 v[152:155], a[28:31], a[72:75], v[152:155]\n" \
"ds_read_b64_tr_b16 a[16:17], v34 offset:0\n" \
"ds_read_b64_tr_b16 a[18:19], v34 offset:4608\n" \
"v_mfma_f32_16x16x32_bf16 v[152:155], a[192:195], a[76:79], v[152:155]\n" \
"ds_read_b64_tr_b16 a[20:21], v34 offset:9216\n" \
"ds_read_b64_tr_b16 a[22:23], v34 offset:13824\n" \
"v_mfma_f32_16x16x32_bf16 v[156:159], a[192:195], a[92:95], v[156:159]\n" \
"ds_read_b64_tr_b16 a[24:25], v34 offset:18432\n" \
"ds_read_b64_tr_b16 a[26:27], v34 offset:23040\n" \
"v_mfma_f32_16x16x32_bf16 v[156:159], a[28:31], a[88:91], v[156:159]\n" \
"ds_read_b64_tr_b16 a[28:29], v33 offset:0\n" \
"ds_read_b64_tr_b16 a[30:31], v33 offset:4608\n" \
"buffer_load_dwordx4 a[40:43], v20, s[56:59], s64 offen offset:2048 sc1\n" \
"v_mfma_f32_16x16x32_bf16 v[160:163], a[196:199], a[72:75], v[160:163]\n" \
"ds_read_b64_tr_b16 a[192:193], v33 offset:18464\n" \
"s_waitcnt lgkmcnt(15)\n" \
"v_mfma_f32_16x16x32_bf16 v[160:163], a[200:203], a[76:79], v[160:163]\n" \
"ds_read_b64_tr_b16 a[194:195], v33 offset:23072\n" \
"v_mfma_f32_16x16x32_bf16 v[164:167], a[200:203], a[92:95], v[164:167]\n" \
"buffer_load_dwordx4 a[44:47], v20, s[56:59], s64 offen offset:3072 sc1\n" \
"v_mfma_f32_16x16x32_bf16 v[164:167], a[196:199], a[88:91], v[164:167]\n" \
"ds_read_b64_tr_b16 a[196:197], v33 offset:27680\n" \
"ds_read_b64_tr_b16 a[198:199], v33 offset:32288\n" \
"v_mfma_f32_16x16x32_bf16 v[168:171], a[204:207], a[72:75], v[168:171]\n" \
"ds_read_b64_tr_b16 a[200:201], v33 offset:36896\n" \
"ds_read_b64_tr_b16 a[202:203], v33 offset:41504\n" \
"v_mfma_f32_16x16x32_bf16 v[168:171], a[208:211], a[76:79], v[168:171]\n" \
"v_mfma_f32_16x16x32_bf16 v[172:175], a[208:211], a[92:95], v[172:175]\n" \
"ds_read_b64_tr_b16 a[208:209], v34 offset:32\n" \
"ds_read_b64_tr_b16 a[210:211], v34 offset:4640\n" \
"buffer_load_dwordx4 a[60:63], v32, s[56:59], s64 offen offset:3072 sc1\n" \
"v_mfma_f32_16x16x32_bf16 v[172:175], a[204:207], a[88:91], v[172:175]\n" \
"ds_read_b64_tr_b16 a[204:205], v33 offset:46112\n" \
"ds_read_b64_tr_b16 a[206:207], v33 offset:50720\n" \
"v_mfma_f32_16x16x32_bf16 v[176:179], a[212:215], a[72:75], v[176:179]\n" \
"v_mfma_f32_16x16x32_bf16 v[176:179], a[216:219], a[76:79], v[176:179]\n" \
"v_mfma_f32_16x16x32_bf16 v[180:183], a[216:219], a[92:95], v[180:183]\n" \
"ds_read_b64_tr_b16 a[216:217], v34 offset:18464\n" \
"ds_read_b64_tr_b16 a[218:219], v34 offset:23072\n" \
"buffer_load_dwordx4 a[56:59], v32, s[56:59], s64 offen offset:2048 sc1\n" \
"v_mfma_f32_16x16x32_bf16 v[180:183], a[212:215], a[88:91], v[180:183]\n" \
"ds_read_b64_tr_b16 a[212:213], v34 offset:9248\n" \
"s_waitcnt vmcnt(21)\n" \
"v_mfma_f32_16x16x32_bf16 v[112:115], a[0:3], a[128:131], v[112:115]\n" \
"ds_read_b64_tr_b16 a[214:215], v34 offset:13856\n" \
"s_waitcnt vmcnt(20) lgkmcnt(13)\n" \
"v_mfma_f32_16x16x32_bf16 v[112:115], a[4:7], a[132:135], v[112:115]\n" \
"s_waitcnt vmcnt(19)\n" \
"v_mfma_f32_16x16x32_bf16 v[112:115], a[8:11], a[136:139], v[112:115]\n" \
"s_waitcnt vmcnt(18)\n" \
"v_mfma_f32_16x16x32_bf16 v[112:115], a[12:15], a[140:143], v[112:115]\n" \
"buffer_load_dwordx4 a[52:55], v32, s[56:59], s64 offen offset:1024 sc1\n" \
"s_waitcnt vmcnt(18)\n" \
"v_mfma_f32_16x16x32_bf16 v[116:119], a[12:15], a[156:159], v[116:119]\n" \
"ds_read_b64_tr_b16 a[12:13], v33 offset:36928\n" \
"s_waitcnt vmcnt(17)\n" \
"v_mfma_f32_16x16x32_bf16 v[116:119], a[8:11], a[152:155], v[116:119]\n" \
"ds_read_b64_tr_b16 a[8:9], v33 offset:27712\n" \
"ds_read_b64_tr_b16 a[10:11], v33 offset:32320\n" \
"ds_read_b64_tr_b16 a[14:15], v33 offset:41536\n" \
"s_waitcnt vmcnt(16)\n" \
"v_mfma_f32_16x16x32_bf16 v[116:119], a[4:7], a[148:151], v[116:119]\n" \
"buffer_load_dwordx4 a[48:51], v32, s[56:59], s64 offen offset:0 sc1\n" \
"ds_read_b64_tr_b16 a[4:5], v33 offset:18496\n" \
"ds_read_b64_tr_b16 a[6:7], v33 offset:23104\n" \
"v_readlane_b32 s64, v11, 43\n" \
"s_waitcnt vmcnt(16)\n" \
"v_mfma_f32_16x16x32_bf16 v[116:119], a[0:3], a[144:147], v[116:119]\n" \
"buffer_load_dwordx4 a[160:163], v20, s[56:59], s72 offen offset:0 sc1\n" \
"ds_read_b64_tr_b16 a[0:1], v33 offset:32\n" \
"ds_read_b64_tr_b16 a[2:3], v33 offset:4640\n" \
"s_waitcnt vmcnt(16)\n" \
"v_mfma_f32_16x16x32_bf16 v[112:115], a[16:19], a[96:99], v[112:115]\n" \
"buffer_load_dwordx4 a[164:167], v20, s[56:59], s72 offen offset:1024 sc1\n" \
"s_waitcnt vmcnt(16)\n" \
"v_mfma_f32_16x16x32_bf16 v[112:115], a[20:23], a[100:103], v[112:115]\n" \
"buffer_load_dwordx4 a[168:171], v20, s[56:59], s72 offen offset:2048 sc1\n" \
"s_waitcnt vmcnt(16)\n" \
"v_mfma_f32_16x16x32_bf16 v[112:115], a[24:27], a[104:107], v[112:115]\n" \
"buffer_load_dwordx4 a[172:175], v20, s[56:59], s72 offen offset:3072 sc1\n" \
"s_waitcnt vmcnt(16)\n" \
"v_mfma_f32_16x16x32_bf16 v[112:115], a[28:31], a[108:111], v[112:115]\n" \
"buffer_load_dwordx4 a[188:191], v32, s[56:59], s72 offen offset:3072 sc1\n" \
"s_waitcnt vmcnt(16)\n" \
"v_mfma_f32_16x16x32_bf16 v[116:119], a[28:31], a[124:127], v[116:119]\n" \
"buffer_load_dwordx4 a[184:187], v32, s[56:59], s72 offen offset:2048 sc1\n" \
"ds_read_b64_tr_b16 a[28:29], v34 offset:18496\n" \
"ds_read_b64_tr_b16 a[30:31], v34 offset:23104\n" \
"s_waitcnt vmcnt(16)\n" \
"v_mfma_f32_16x16x32_bf16 v[116:119], a[24:27], a[120:123], v[116:119]\n" \
"v_cvt_pk_bf16_f32 v112, v112, v113\n" \
"v_cvt_pk_bf16_f32 v113, v114, v115\n" \
"s_waitcnt vmcnt(15)\n" \
"v_mfma_f32_16x16x32_bf16 v[116:119], a[20:23], a[116:119], v[116:119]\n" \
"buffer_load_dwordx4 a[180:183], v32, s[56:59], s72 offen offset:1024 sc1\n" \
"ds_read_b64_tr_b16 a[20:21], v34 offset:64\n" \
"ds_read_b64_tr_b16 a[22:23], v34 offset:4672\n" \
"ds_read_b64_tr_b16 a[24:25], v34 offset:9280\n" \
"ds_read_b64_tr_b16 a[26:27], v34 offset:13888\n" \
"s_waitcnt vmcnt(15)\n" \
"v_mfma_f32_16x16x32_bf16 v[116:119], a[16:19], a[112:115], v[116:119]\n" \
"ds_read_b64_tr_b16 a[16:17], v33 offset:46144\n" \
"buffer_load_dwordx4 a[176:179], v32, s[56:59], s72 offen offset:0 sc1\n" \
"s_waitcnt lgkmcnt(5)\n" \
"v_mfma_f32_16x16x32_bf16 v[120:123], a[192:195], a[128:131], v[120:123]\n" \
"ds_read_b64_tr_b16 a[18:19], v33 offset:50752\n" \
"v_mfma_f32_16x16x32_bf16 v[120:123], a[196:199], a[132:135], v[120:123]\n" \
"v_mfma_f32_16x16x32_bf16 v[120:123], a[200:203], a[136:139], v[120:123]\n" \
"v_mfma_f32_16x16x32_bf16 v[120:123], a[204:207], a[140:143], v[120:123]\n" \
"v_cvt_pk_bf16_f32 v114, v116, v117\n" \
"v_cvt_pk_bf16_f32 v115, v118, v119\n" \
"v_mfma_f32_16x16x32_bf16 v[124:127], a[204:207], a[156:159], v[124:127]\n" \
"buffer_store_dwordx4 v[112:115], v20, s[56:59], s65 offen offset:0 sc1\n" \
"s_add_u32 s65, s65, 0x3800\n" \
"v_mfma_f32_16x16x32_bf16 v[124:127], a[200:203], a[152:155], v[124:127]\n" \
"ds_read_b64_tr_b16 a[200:201], v33 offset:27744\n" \
"ds_read_b64_tr_b16 a[202:203], v33 offset:32352\n" \
"v_mfma_f32_16x16x32_bf16 v[124:127], a[196:199], a[148:151], v[124:127]\n" \
"ds_read_b64_tr_b16 a[196:197], v33 offset:18528\n" \
"ds_read_b64_tr_b16 a[198:199], v33 offset:23136\n" \
"v_mfma_f32_16x16x32_bf16 v[124:127], a[192:195], a[144:147], v[124:127]\n" \
"ds_read_b64_tr_b16 a[192:193], v33 offset:64\n" \
"ds_read_b64_tr_b16 a[194:195], v33 offset:4672\n" \
"v_mfma_f32_16x16x32_bf16 v[120:123], a[208:211], a[96:99], v[120:123]\n" \
"buffer_load_dwordx4 a[220:223], v20, s[56:59], s93 offen offset:0 sc1\n" \
"v_mfma_f32_16x16x32_bf16 v[120:123], a[212:215], a[100:103], v[120:123]\n" \
"ds_read_b64_tr_b16 a[204:205], v33 offset:36960\n" \
"ds_read_b64_tr_b16 a[206:207], v33 offset:41568\n" \
"v_mfma_f32_16x16x32_bf16 v[120:123], a[216:219], a[104:107], v[120:123]\n" \
"v_mfma_f32_16x16x32_bf16 v[120:123], a[0:3], a[108:111], v[120:123]\n" \
"v_mfma_f32_16x16x32_bf16 v[124:127], a[0:3], a[124:127], v[124:127]\n" \
"ds_read_b64_tr_b16 a[0:1], v34 offset:18528\n" \
"ds_read_b64_tr_b16 a[2:3], v34 offset:23136\n" \
"buffer_load_dwordx4 a[224:227], v20, s[56:59], s93 offen offset:1024 sc1\n" \
"v_mfma_f32_16x16x32_bf16 v[124:127], a[216:219], a[120:123], v[124:127]\n" \
"ds_read_b64_tr_b16 a[216:217], v34 offset:9312\n" \
"ds_read_b64_tr_b16 a[218:219], v34 offset:13920\n" \
"v_mfma_f32_16x16x32_bf16 v[124:127], a[212:215], a[116:119], v[124:127]\n" \
"ds_read_b64_tr_b16 a[212:213], v34 offset:96\n" \
"v_cvt_pk_bf16_f32 v120, v120, v121\n" \
"v_cvt_pk_bf16_f32 v121, v122, v123\n" \
"v_mfma_f32_16x16x32_bf16 v[124:127], a[208:211], a[112:115], v[124:127]\n" \
"ds_read_b64_tr_b16 a[208:209], v33 offset:46176\n" \
"ds_read_b64_tr_b16 a[210:211], v33 offset:50784\n" \
"v_mfma_f32_16x16x32_bf16 v[128:131], a[4:7], a[128:131], v[128:131]\n" \
"buffer_load_dwordx4 a[228:231], v20, s[56:59], s93 offen offset:2048 sc1\n" \
"v_mfma_f32_16x16x32_bf16 v[128:131], a[8:11], a[132:135], v[128:131]\n" \
"ds_read_b64_tr_b16 a[214:215], v34 offset:4704\n" \
"v_mfma_f32_16x16x32_bf16 v[128:131], a[12:15], a[136:139], v[128:131]\n" \
"s_waitcnt lgkmcnt(15)\n" \
"v_mfma_f32_16x16x32_bf16 v[128:131], a[16:19], a[140:143], v[128:131]\n" \
"v_cvt_pk_bf16_f32 v122, v124, v125\n" \
"v_cvt_pk_bf16_f32 v123, v126, v127\n" \
"v_mfma_f32_16x16x32_bf16 v[132:135], a[16:19], a[156:159], v[132:135]\n" \
"buffer_store_dwordx4 v[120:123], v20, s[56:59], s66 offen offset:0 sc1\n" \
"s_add_u32 s66, s66, 0x3000\n" \
"v_mfma_f32_16x16x32_bf16 v[132:135], a[12:15], a[152:155], v[132:135]\n" \
"ds_read_b64_tr_b16 a[12:13], v10 offset:37120\n" \
"ds_read_b64_tr_b16 a[14:15], v10 offset:41728\n" \
"v_mfma_f32_16x16x32_bf16 v[132:135], a[8:11], a[148:151], v[132:135]\n" \
"ds_read_b64_tr_b16 a[8:9], v10 offset:27904\n" \
"ds_read_b64_tr_b16 a[10:11], v10 offset:32512\n" \
"v_mfma_f32_16x16x32_bf16 v[132:135], a[4:7], a[144:147], v[132:135]\n" \
"ds_read_b64_tr_b16 a[4:5], v33 offset:96\n" \
"ds_read_b64_tr_b16 a[6:7], v33 offset:4704\n" \
"v_mfma_f32_16x16x32_bf16 v[128:131], a[20:23], a[96:99], v[128:131]\n" \
"buffer_load_dwordx4 a[232:235], v20, s[56:59], s93 offen offset:3072 sc1\n" \
"v_mfma_f32_16x16x32_bf16 v[128:131], a[24:27], a[100:103], v[128:131]\n" \
"ds_read_b64_tr_b16 a[16:17], v10 offset:46336\n" \
"ds_read_b64_tr_b16 a[18:19], v10 offset:50944\n" \
"v_mfma_f32_16x16x32_bf16 v[128:131], a[28:31], a[104:107], v[128:131]\n" \
"s_waitcnt lgkmcnt(14)\n" \
"v_mfma_f32_16x16x32_bf16 v[128:131], a[192:195], a[108:111], v[128:131]\n" \
"v_mfma_f32_16x16x32_bf16 v[132:135], a[192:195], a[124:127], v[132:135]\n" \
"ds_read_b64_tr_b16 a[192:193], v33 offset:37120\n" \
"ds_read_b64_tr_b16 a[194:195], v33 offset:41728\n" \
"buffer_load_dwordx4 a[248:251], v32, s[56:59], s93 offen offset:3072 sc1\n" \
"v_mfma_f32_16x16x32_bf16 v[132:135], a[28:31], a[120:123], v[132:135]\n" \
"L_pf22:\n" \
"s_cbranch_execz L_retB\n" \
"s_nop 0\n" \
"ds_read_b64_tr_b16 a[28:29], v33 offset:27904\n" \
"ds_read_b64_tr_b16 a[30:31], v33 offset:32512\n" \
"v_mfma_f32_16x16x32_bf16 v[132:135], a[24:27], a[116:119], v[132:135]\n" \
"ds_read_b64_tr_b16 a[24:25], v33 offset:18688\n" \
"v_cvt_pk_bf16_f32 v128, v128, v129\n" \
"v_cvt_pk_bf16_f32 v129, v130, v131\n" \
"v_mfma_f32_16x16x32_bf16 v[132:135], a[20:23], a[112:115], v[132:135]\n" \
"ds_read_b64_tr_b16 a[20:21], v10 offset:55552\n" \
"ds_read_b64_tr_b16 a[22:23], v10 offset:60160\n" \
"v_mfma_f32_16x16x32_bf16 v[136:139], a[196:199], a[128:131], v[136:139]\n" \
"buffer_load_dwordx4 a[244:247], v32, s[56:59], s93 offen offset:2048 sc1\n" \
"v_mfma_f32_16x16x32_bf16 v[136:139], a[200:203], a[132:135], v[136:139]\n" \
"ds_read_b64_tr_b16 a[26:27], v33 offset:23296\n" \
"v_mfma_f32_16x16x32_bf16 v[136:139], a[204:207], a[136:139], v[136:139]\n" \
"s_waitcnt lgkmcnt(15)\n" \
"v_mfma_f32_16x16x32_bf16 v[136:139], a[208:211], a[140:143], v[136:139]\n" \
"v_cvt_pk_bf16_f32 v130, v132, v133\n" \
"v_cvt_pk_bf16_f32 v131, v134, v135\n" \
"v_mfma_f32_16x16x32_bf16 v[140:143], a[208:211], a[156:159], v[140:143]\n" \
"buffer_store_dwordx4 v[128:131], v20, s[56:59], s67 offen offset:0 sc1\n" \
"s_add_u32 s67, s67, 0x2800\n" \
"v_mfma_f32_16x16x32_bf16 v[140:143], a[204:207], a[152:155], v[140:143]\n" \
"ds_read_b64_tr_b16 a[204:205], v34 offset:9472\n" \
"ds_read_b64_tr_b16 a[206:207], v34 offset:14080\n" \
"v_mfma_f32_16x16x32_bf16 v[140:143], a[200:203], a[148:151], v[140:143]\n" \
"ds_read_b64_tr_b16 a[200:201], v34 offset:256\n" \
"ds_read_b64_tr_b16 a[202:203], v34 offset:4864\n" \
"v_mfma_f32_16x16x32_bf16 v[140:143], a[196:199], a[144:147], v[140:143]\n" \
"ds_read_b64_tr_b16 a[196:197], v33 offset:46336\n" \
"ds_read_b64_tr_b16 a[198:199], v33 offset:50944\n" \
"v_mfma_f32_16x16x32_bf16 v[136:139], a[212:215], a[96:99], v[136:139]\n" \
"buffer_load_dwordx4 a[240:243], v32, s[56:59], s93 offen offset:1024 sc1\n" \
"v_mfma_f32_16x16x32_bf16 v[136:139], a[216:219], a[100:103], v[136:139]\n" \
"ds_read_b64_tr_b16 a[208:209], v34 offset:18688\n" \
"ds_read_b64_tr_b16 a[210:211], v34 offset:23296\n" \
"v_mfma_f32_16x16x32_bf16 v[136:139], a[0:3], a[104:107], v[136:139]\n" \
"s_waitcnt lgkmcnt(14)\n" \
"v_mfma_f32_16x16x32_bf16 v[136:139], a[4:7], a[108:111], v[136:139]\n" \
"v_mfma_f32_16x16x32_bf16 v[140:143], a[4:7], a[124:127], v[140:143]\n" \
"ds_read_b64_tr_b16 a[4:5], v10 offset:46208\n" \
"ds_read_b64_tr_b16 a[6:7], v10 offset:50816\n" \
"buffer_load_dwordx4 a[236:239], v32, s[56:59], s93 offen offset:0 sc1\n" \
"v_mfma_f32_16x16x32_bf16 v[140:143], a[0:3], a[120:123], v[140:143]\n" \
"ds_read_b64_tr_b16 a[0:1], v10 offset:36992\n" \
"ds_read_b64_tr_b16 a[2:3], v10 offset:41600\n" \
"v_mfma_f32_16x16x32_bf16 v[140:143], a[216:219], a[116:119], v[140:143]\n" \
"ds_read_b64_tr_b16 a[216:217], v10 offset:27776\n" \
"v_cvt_pk_bf16_f32 v136, v136, v137\n" \
"v_cvt_pk_bf16_f32 v137, v138, v139\n" \
"v_mfma_f32_16x16x32_bf16 v[140:143], a[212:215], a[112:115], v[140:143]\n" \
"ds_read_b64_tr_b16 a[212:213], v33 offset:256\n" \
"s_waitcnt vmcnt(26)\n" \
"v_mfma_f32_16x16x32_bf16 v[248:251], a[8:11], a[32:35], v[248:251]\n" \
"ds_read_b64_tr_b16 a[214:215], v33 offset:4864\n" \
"s_waitcnt vmcnt(25)\n" \
"v_mfma_f32_16x16x32_bf16 v[248:251], a[12:15], a[36:39], v[248:251]\n" \
"ds_read_b64_tr_b16 a[218:219], v10 offset:32384\n" \
"s_waitcnt vmcnt(24)\n" \
"v_mfma_f32_16x16x32_bf16 v[248:251], a[16:19], a[40:43], v[248:251]\n" \
"v_cvt_pk_bf16_f32 v138, v140, v141\n" \
"v_cvt_pk_bf16_f32 v139, v142, v143\n" \
"s_waitcnt vmcnt(23) lgkmcnt(14)\n" \
"v_mfma_f32_16x16x32_bf16 v[248:251], a[20:23], a[44:47], v[248:251]\n" \
"buffer_store_dwordx4 v[136:139], v20, s[56:59], s68 offen offset:0 sc1\n" \
"s_add_u32 s68, s68, 0x2000\n" \
"s_waitcnt vmcnt(23)\n" \
"v_mfma_f32_16x16x32_bf16 v[252:255], a[20:23], a[60:63], v[252:255]\n" \
"ds_read_b64_tr_b16 a[20:21], v33 offset:36992\n" \
"s_waitcnt vmcnt(22)\n" \
"v_mfma_f32_16x16x32_bf16 v[252:255], a[16:19], a[56:59], v[252:255]\n" \
"ds_read_b64_tr_b16 a[16:17], v33 offset:27776\n" \
"s_waitcnt vmcnt(21)\n" \
"v_mfma_f32_16x16x32_bf16 v[252:255], a[12:15], a[52:55], v[252:255]\n" \
"ds_read_b64_tr_b16 a[12:13], v33 offset:18560\n" \
"s_waitcnt vmcnt(20)\n" \
"v_mfma_f32_16x16x32_bf16 v[252:255], a[8:11], a[48:51], v[252:255]\n" \
"ds_read_b64_tr_b16 a[8:9], v10 offset:55424\n" \
"ds_read_b64_tr_b16 a[10:11], v10 offset:60032\n" \
"v_mfma_f32_16x16x32_bf16 v[176:179], a[24:27], a[128:131], v[176:179]\n" \
"ds_read_b64_tr_b16 a[14:15], v33 offset:23168\n" \
"ds_read_b64_tr_b16 a[18:19], v33 offset:32384\n" \
"v_mfma_f32_16x16x32_bf16 v[176:179], a[28:31], a[132:135], v[176:179]\n" \
"buffer_load_dwordx4 v[64:67], v20, s[56:59], s95 offen offset:0 sc1\n" \
"v_mfma_f32_16x16x32_bf16 v[176:179], a[192:195], a[136:139], v[176:179]\n" \
"ds_read_b64_tr_b16 a[22:23], v33 offset:41600\n" \
"s_waitcnt lgkmcnt(12)\n" \
"v_mfma_f32_16x16x32_bf16 v[176:179], a[196:199], a[140:143], v[176:179]\n" \
"v_mfma_f32_16x16x32_bf16 v[180:183], a[196:199], a[156:159], v[180:183]\n" \
"v_mfma_f32_16x16x32_bf16 v[180:183], a[192:195], a[152:155], v[180:183]\n" \
"v_mfma_f32_16x16x32_bf16 v[180:183], a[28:31], a[148:151], v[180:183]\n" \
"buffer_load_dwordx4 v[68:71], v20, s[56:59], s95 offen offset:1024 sc1\n" \
"v_mfma_f32_16x16x32_bf16 v[180:183], a[24:27], a[144:147], v[180:183]\n" \
"s_waitcnt vmcnt(21)\n" \
"v_mfma_f32_16x16x32_bf16 v[248:251], a[24:27], a[160:163], v[248:251]\n" \
"s_waitcnt vmcnt(13)\n" \
"s_mov_b64 exec, 1\n" \
"buffer_store_dword v39, off, s[56:59], s65 offset:0 sc1\n" \
"s_mov_b64 exec, -1\n" \
"v_mfma_f32_16x16x32_bf16 v[248:251], a[28:31], a[164:167], v[248:251]\n" \
"v_mfma_f32_16x16x32_bf16 v[248:251], a[192:195], a[168:171], v[248:251]\n" \
"v_mfma_f32_16x16x32_bf16 v[248:251], a[196:199], a[172:175], v[248:251]\n" \
"v_mfma_f32_16x16x32_bf16 v[252:255], a[196:199], a[188:191], v[252:255]\n" \
"ds_read_b64_tr_b16 a[196:197], v34 offset:18560\n" \
"ds_read_b64_tr_b16 a[198:199], v34 offset:23168\n" \
"v_mfma_f32_16x16x32_bf16 v[252:255], a[192:195], a[184:187], v[252:255]\n" \
"ds_read_b64_tr_b16 a[192:193], v34 offset:9344\n" \
"ds_read_b64_tr_b16 a[194:195], v34 offset:13952\n" \
"v_mfma_f32_16x16x32_bf16 v[252:255], a[28:31], a[180:183], v[252:255]\n" \
"ds_read_b64_tr_b16 a[28:29], v34 offset:128\n" \
"ds_read_b64_tr_b16 a[30:31], v34 offset:4736\n" \
"v_mfma_f32_16x16x32_bf16 v[252:255], a[24:27], a[176:179], v[252:255]\n" \
"ds_read_b64_tr_b16 a[24:25], v33 offset:46208\n" \
"ds_read_b64_tr_b16 a[26:27], v33 offset:50816\n" \
"v_mfma_f32_16x16x32_bf16 v[176:179], a[200:203], a[96:99], v[176:179]\n" \
"buffer_load_dwordx4 v[72:75], v32, s[56:59], s95 offen offset:0 sc1\n" \
"v_mfma_f32_16x16x32_bf16 v[176:179], a[204:207], a[100:103], v[176:179]\n" \
"v_mfma_f32_16x16x32_bf16 v[176:179], a[208:211], a[104:107], v[176:179]\n" \
"s_waitcnt lgkmcnt(8)\n" \
"v_mfma_f32_16x16x32_bf16 v[176:179], a[212:215], a[108:111], v[176:179]\n" \
"v_mfma_f32_16x16x32_bf16 v[180:183], a[212:215], a[124:127], v[180:183]\n" \
"v_mfma_f32_16x16x32_bf16 v[180:183], a[208:211], a[120:123], v[180:183]\n" \
"buffer_load_dwordx4 v[76:79], v32, s[56:59], s95 offen offset:1024 sc1\n" \
"v_mfma_f32_16x16x32_bf16 v[180:183], a[204:207], a[116:119], v[180:183]\n" \
"v_readlane_b32 s95, v11, 48\n" \
"v_mfma_f32_16x16x32_bf16 v[180:183], a[200:203], a[112:115], v[180:183]\n" \
"s_waitcnt vmcnt(15)\n" \
"v_mfma_f32_16x16x32_bf16 v[248:251], a[200:203], a[220:223], v[248:251]\n" \
"s_waitcnt vmcnt(12)\n" \
"s_mov_b64 exec, 1\n" \
"buffer_store_dword v39, off, s[56:59], s66 offset:4 sc1\n" \
"s_mov_b64 exec, -1\n" \
"v_cvt_pk_bf16_f32 v176, v176, v177\n" \
"v_cvt_pk_bf16_f32 v177, v178, v179\n" \
"v_cvt_pk_bf16_f32 v178, v180, v181\n" \
"v_cvt_pk_bf16_f32 v179, v182, v183\n" \
"v_mfma_f32_16x16x32_bf16 v[248:251], a[204:207], a[224:227], v[248:251]\n" \
"v_readlane_b32 s66, v11, 44\n" \
"v_mfma_f32_16x16x32_bf16 v[248:251], a[208:211], a[228:231], v[248:251]\n" \
"buffer_store_dwordx4 v[176:179], v20, s[56:59], s69 offen offset:0 sc1\n" \
"s_waitcnt vmcnt(10)\n" \
"s_mov_b64 exec, 1\n" \
"buffer_store_dword v39, off, s[56:59], s67 offset:8 sc1\n" \
"s_mov_b64 exec, -1\n" \
"v_mfma_f32_16x16x32_bf16 v[248:251], a[212:215], a[232:235], v[248:251]\n" \
"v_readlane_b32 s67, v11, 45\n" \
"v_mfma_f32_16x16x32_bf16 v[252:255], a[212:215], a[248:251], v[252:255]\n" \
"ds_read_b64_tr_b16 a[212:213], v10 offset:46240\n" \
"ds_read_b64_tr_b16 a[214:215], v10 offset:50848\n" \
"v_mfma_f32_16x16x32_bf16 v[252:255], a[208:211], a[244:247], v[252:255]\n" \
"ds_read_b64_tr_b16 a[208:209], v10 offset:37024\n" \
"ds_read_b64_tr_b16 a[210:211], v10 offset:41632\n" \
"s_waitcnt vmcnt(10)\n" \
"v_mfma_f32_16x16x32_bf16 v[252:255], a[204:207], a[240:243], v[252:255]\n" \
"v_cvt_pk_bf16_f32 v248, v248, v249\n" \
"v_cvt_pk_bf16_f32 v249, v250, v251\n" \
"s_waitcnt vmcnt(9)\n" \
"v_mfma_f32_16x16x32_bf16 v[252:255], a[200:203], a[236:239], v[252:255]\n" \
"ds_read_b64_tr_b16 a[200:201], v33 offset:128\n" \
"ds_read_b64_tr_b16 a[202:203], v33 offset:4736\n" \
"s_waitcnt vmcnt(8)\n" \
"s_mov_b64 exec, 1\n" \
"buffer_store_dword v39, off, s[56:59], s68 offset:12 sc1\n" \
"s_mov_b64 exec, -1\n" \
"v_mfma_f32_16x16x32_bf16 v[216:219], a[216:219], a[32:35], v[216:219]\n" \
"ds_read_b64_tr_b16 a[204:205], v10 offset:27808\n" \
"v_cvt_pk_bf16_f32 v250, v252, v253\n" \
"v_cvt_pk_bf16_f32 v251, v254, v255\n" \
"v_mfma_f32_16x16x32_bf16 v[216:219], a[0:3], a[36:39], v[216:219]\n" \
"buffer_store_dwordx4 v[248:251], v20, s[56:59], s69 offen offset:1024 sc1\n" \
"v_mfma_f32_16x16x32_bf16 v[216:219], a[4:7], a[40:43], v[216:219]\n" \
"ds_read_b64_tr_b16 a[206:207], v10 offset:32416\n" \
"v_mfma_f32_16x16x32_bf16 v[216:219], a[8:11], a[44:47], v[216:219]\n" \
"v_mfma_f32_16x16x32_bf16 v[220:223], a[8:11], a[60:63], v[220:223]\n" \
"ds_read_b64_tr_b16 a[8:9], v33 offset:37024\n" \
"ds_read_b64_tr_b16 a[10:11], v33 offset:41632\n" \
"v_mfma_f32_16x16x32_bf16 v[220:223], a[4:7], a[56:59], v[220:223]\n" \
"ds_read_b64_tr_b16 a[4:5], v33 offset:27808\n" \
"ds_read_b64_tr_b16 a[6:7], v33 offset:32416\n" \
"v_mfma_f32_16x16x32_bf16 v[220:223], a[0:3], a[52:55], v[220:223]\n" \
"ds_read_b64_tr_b16 a[0:1], v33 offset:18592\n" \
"ds_read_b64_tr_b16 a[2:3], v33 offset:23200\n" \
"v_mfma_f32_16x16x32_bf16 v[220:223], a[216:219], a[48:51], v[220:223]\n" \
"ds_read_b64_tr_b16 a[216:217], v10 offset:55456\n" \
"ds_read_b64_tr_b16 a[218:219], v10 offset:60064\n" \
"v_mfma_f32_16x16x32_bf16 v[144:147], a[12:15], a[128:131], v[144:147]\n" \
"buffer_load_dwordx4 v[96:99], v20, s[56:59], s97 offen offset:0 sc1\n" \
"v_mfma_f32_16x16x32_bf16 v[144:147], a[16:19], a[132:135], v[144:147]\n" \
"v_mfma_f32_16x16x32_bf16 v[144:147], a[20:23], a[136:139], v[144:147]\n" \
"s_waitcnt lgkmcnt(12)\n" \
"v_mfma_f32_16x16x32_bf16 v[144:147], a[24:27], a[140:143], v[144:147]\n" \
"buffer_load_dword v37, v40, s[56:59], s74 offen sc1\n" \
"v_mfma_f32_16x16x32_bf16 v[148:151], a[24:27], a[156:159], v[148:151]\n" \
"v_mfma_f32_16x16x32_bf16 v[148:151], a[20:23], a[152:155], v[148:151]\n" \
"v_mfma_f32_16x16x32_bf16 v[148:151], a[16:19], a[148:151], v[148:151]\n" \
"v_mfma_f32_16x16x32_bf16 v[148:151], a[12:15], a[144:147], v[148:151]\n" \
"v_mfma_f32_16x16x32_bf16 v[216:219], a[12:15], a[160:163], v[216:219]\n" \
"buffer_load_dwordx4 v[100:103], v20, s[56:59], s97 offen offset:1024 sc1\n" \
"v_mfma_f32_16x16x32_bf16 v[216:219], a[16:19], a[164:167], v[216:219]\n" \
"v_mfma_f32_16x16x32_bf16 v[216:219], a[20:23], a[168:171], v[216:219]\n" \
"v_mfma_f32_16x16x32_bf16 v[216:219], a[24:27], a[172:175], v[216:219]\n" \
"v_mfma_f32_16x16x32_bf16 v[220:223], a[24:27], a[188:191], v[220:223]\n" \
"ds_read_b64_tr_b16 a[24:25], v34 offset:18592\n" \
"ds_read_b64_tr_b16 a[26:27], v34 offset:23200\n" \
"v_mfma_f32_16x16x32_bf16 v[220:223], a[20:23], a[184:187], v[220:223]\n" \
"ds_read_b64_tr_b16 a[20:21], v34 offset:9376\n" \
"ds_read_b64_tr_b16 a[22:23], v34 offset:13984\n" \
"v_mfma_f32_16x16x32_bf16 v[220:223], a[16:19], a[180:183], v[220:223]\n" \
"ds_read_b64_tr_b16 a[16:17], v34 offset:160\n" \
"ds_read_b64_tr_b16 a[18:19], v34 offset:4768\n" \
"v_mfma_f32_16x16x32_bf16 v[220:223], a[12:15], a[176:179], v[220:223]\n" \
"ds_read_b64_tr_b16 a[12:13], v33 offset:46240\n" \
"ds_read_b64_tr_b16 a[14:15], v33 offset:50848\n" \
"v_mfma_f32_16x16x32_bf16 v[144:147], a[28:31], a[96:99], v[144:147]\n" \
"buffer_load_dwordx4 v[104:107], v32, s[56:59], s97 offen offset:0 sc1\n" \
"v_mfma_f32_16x16x32_bf16 v[144:147], a[192:195], a[100:103], v[144:147]\n" \
"v_mfma_f32_16x16x32_bf16 v[144:147], a[196:199], a[104:107], v[144:147]\n" \
"s_waitcnt lgkmcnt(8)\n" \
"v_mfma_f32_16x16x32_bf16 v[144:147], a[200:203], a[108:111], v[144:147]\n" \
"v_mfma_f32_16x16x32_bf16 v[148:151], a[200:203], a[124:127], v[148:151]\n" \
"v_mfma_f32_16x16x32_bf16 v[148:151], a[196:199], a[120:123], v[148:151]\n" \
"buffer_load_dwordx4 v[108:111], v32, s[56:59], s97 offen offset:1024 sc1\n" \
"v_mfma_f32_16x16x32_bf16 v[148:151], a[192:195], a[116:119], v[148:151]\n" \
"v_readlane_b32 s97, v11, 42\n" \
"v_mfma_f32_16x16x32_bf16 v[148:151], a[28:31], a[112:115], v[148:151]\n" \
"v_mfma_f32_16x16x32_bf16 v[216:219], a[28:31], a[220:223], v[216:219]\n" \
"v_mfma_f32_16x16x32_bf16 v[216:219], a[192:195], a[224:227], v[216:219]\n" \
"v_cvt_pk_bf16_f32 v144, v144, v145\n" \
"v_cvt_pk_bf16_f32 v145, v146, v147\n" \
"v_mfma_f32_16x16x32_bf16 v[216:219], a[196:199], a[228:231], v[216:219]\n" \
"buffer_load_dwordx4 v[80:83], v20, s[56:59], s96 offen offset:0 sc1\n" \
"v_mfma_f32_16x16x32_bf16 v[216:219], a[200:203], a[232:235], v[216:219]\n" \
"v_mfma_f32_16x16x32_bf16 v[220:223], a[200:203], a[248:251], v[220:223]\n" \
"v_cvt_pk_bf16_f32 v146, v148, v149\n" \
"v_cvt_pk_bf16_f32 v147, v150, v151\n" \
"v_mfma_f32_16x16x32_bf16 v[220:223], a[196:199], a[244:247], v[220:223]\n" \
"buffer_store_dwordx4 v[144:147], v20, s[56:59], s71 offen offset:0 sc1\n" \
"v_mfma_f32_16x16x32_bf16 v[220:223], a[192:195], a[240:243], v[220:223]\n" \
"ds_read_b64_tr_b16 a[192:193], v10 offset:27840\n" \
"ds_read_b64_tr_b16 a[194:195], v10 offset:32448\n" \
"v_cvt_pk_bf16_f32 v216, v216, v217\n" \
"v_cvt_pk_bf16_f32 v217, v218, v219\n" \
"v_mfma_f32_16x16x32_bf16 v[220:223], a[28:31], a[236:239], v[220:223]\n" \
"ds_read_b64_tr_b16 a[28:29], v33 offset:160\n" \
"ds_read_b64_tr_b16 a[30:31], v33 offset:4768\n" \
"v_mfma_f32_16x16x32_bf16 v[224:227], a[204:207], a[32:35], v[224:227]\n" \
"ds_read_b64_tr_b16 a[196:197], v10 offset:37056\n" \
"ds_read_b64_tr_b16 a[198:199], v10 offset:41664\n" \
"v_mfma_f32_16x16x32_bf16 v[224:227], a[208:211], a[36:39], v[224:227]\n" \
"ds_read_b64_tr_b16 a[200:201], v10 offset:46272\n" \
"ds_read_b64_tr_b16 a[202:203], v10 offset:50880\n" \
"v_cvt_pk_bf16_f32 v218, v220, v221\n" \
"v_cvt_pk_bf16_f32 v219, v222, v223\n" \
"v_mfma_f32_16x16x32_bf16 v[224:227], a[212:215], a[40:43], v[224:227]\n" \
"buffer_store_dwordx4 v[216:219], v20, s[56:59], s71 offen offset:1024 sc1\n" \
"s_add_u32 s71, s71, 0x1800\n" \
"v_mfma_f32_16x16x32_bf16 v[224:227], a[216:219], a[44:47], v[224:227]\n" \
"v_mfma_f32_16x16x32_bf16 v[228:231], a[216:219], a[60:63], v[228:231]\n" \
"ds_read_b64_tr_b16 a[216:217], v33 offset:37056\n" \
"ds_read_b64_tr_b16 a[218:219], v33 offset:41664\n" \
"v_mfma_f32_16x16x32_bf16 v[228:231], a[212:215], a[56:59], v[228:231]\n" \
"ds_read_b64_tr_b16 a[212:213], v33 offset:27840\n" \
"ds_read_b64_tr_b16 a[214:215], v33 offset:32448\n" \
"v_mfma_f32_16x16x32_bf16 v[228:231], a[208:211], a[52:55], v[228:231]\n" \
"ds_read_b64_tr_b16 a[208:209], v33 offset:18624\n" \
"ds_read_b64_tr_b16 a[210:211], v33 offset:23232\n" \
"v_mfma_f32_16x16x32_bf16 v[228:231], a[204:207], a[48:51], v[228:231]\n" \
"ds_read_b64_tr_b16 a[204:205], v10 offset:55488\n" \
"ds_read_b64_tr_b16 a[206:207], v10 offset:60096\n" \
"v_mfma_f32_16x16x32_bf16 v[152:155], a[0:3], a[128:131], v[152:155]\n" \
"buffer_load_dwordx4 v[88:91], v32, s[56:59], s96 offen offset:0 sc1\n" \
"v_mfma_f32_16x16x32_bf16 v[152:155], a[4:7], a[132:135], v[152:155]\n" \
"v_mfma_f32_16x16x32_bf16 v[152:155], a[8:11], a[136:139], v[152:155]\n" \
"s_waitcnt lgkmcnt(15)\n" \
"v_mfma_f32_16x16x32_bf16 v[152:155], a[12:15], a[140:143], v[152:155]\n" \
"v_mfma_f32_16x16x32_bf16 v[156:159], a[12:15], a[156:159], v[156:159]\n" \
"v_mfma_f32_16x16x32_bf16 v[156:159], a[8:11], a[152:155], v[156:159]\n" \
"buffer_load_dwordx4 v[84:87], v20, s[56:59], s96 offen offset:1024 sc1\n" \
"v_mfma_f32_16x16x32_bf16 v[156:159], a[4:7], a[148:151], v[156:159]\n" \
"v_mfma_f32_16x16x32_bf16 v[156:159], a[0:3], a[144:147], v[156:159]\n" \
"v_mfma_f32_16x16x32_bf16 v[224:227], a[0:3], a[160:163], v[224:227]\n" \
"v_mfma_f32_16x16x32_bf16 v[224:227], a[4:7], a[164:167], v[224:227]\n" \
"v_mfma_f32_16x16x32_bf16 v[224:227], a[8:11], a[168:171], v[224:227]\n" \
"buffer_load_dwordx4 v[92:95], v32, s[56:59], s96 offen offset:1024 sc1\n" \
"v_mfma_f32_16x16x32_bf16 v[224:227], a[12:15], a[172:175], v[224:227]\n" \
"v_mfma_f32_16x16x32_bf16 v[228:231], a[12:15], a[188:191], v[228:231]\n" \
"ds_read_b64_tr_b16 a[12:13], v34 offset:18624\n" \
"ds_read_b64_tr_b16 a[14:15], v34 offset:23232\n" \
"v_mfma_f32_16x16x32_bf16 v[228:231], a[8:11], a[184:187], v[228:231]\n" \
"ds_read_b64_tr_b16 a[8:9], v34 offset:9408\n" \
"ds_read_b64_tr_b16 a[10:11], v34 offset:14016\n" \
"v_mfma_f32_16x16x32_bf16 v[228:231], a[4:7], a[180:183], v[228:231]\n" \
"ds_read_b64_tr_b16 a[4:5], v34 offset:192\n" \
"ds_read_b64_tr_b16 a[6:7], v34 offset:4800\n" \
"v_mfma_f32_16x16x32_bf16 v[228:231], a[0:3], a[176:179], v[228:231]\n" \
"ds_read_b64_tr_b16 a[0:1], v33 offset:46272\n" \
"ds_read_b64_tr_b16 a[2:3], v33 offset:50880\n" \
"s_waitcnt vmcnt(11)\n" \
"s_mov_b64 exec, 3\n" \
"buffer_store_dword v39, v41, s[56:59], s70 offen sc1\n" \
"s_mov_b64 exec, -1\n" \
"v_mfma_f32_16x16x32_bf16 v[152:155], a[16:19], a[96:99], v[152:155]\n" \
"v_mfma_f32_16x16x32_bf16 v[152:155], a[20:23], a[100:103], v[152:155]\n" \
"v_mfma_f32_16x16x32_bf16 v[152:155], a[24:27], a[104:107], v[152:155]\n" \
"s_waitcnt vmcnt(10)\n" \
"v_cmp_eq_u32_e64 s[98:99], s62, v37\n" \
"s_waitcnt lgkmcnt(8)\n" \
"v_mfma_f32_16x16x32_bf16 v[152:155], a[28:31], a[108:111], v[152:155]\n" \
"s_and_b32 s99, s98, 0xf\n" \
"s_or_b32 s84, s84, s99\n" \
"s_bitcmp1_b32 s84, 0\n" \
"s_cselect_b32 s72, s63, 0x40000000\n" \
"s_bitcmp1_b32 s84, 1\n" \
"s_cselect_b32 s73, s63, 0x40000000\n" \
"s_bitcmp1_b32 s84, 2\n" \
"s_cselect_b32 s92, s63, 0x40000000\n" \
"s_bitcmp1_b32 s84, 3\n" \
"s_cselect_b32 s93, s63, 0x40000000\n" \
"v_cvt_pk_bf16_f32 v152, v152, v153\n" \
"v_cvt_pk_bf16_f32 v153, v154, v155\n" \
"v_mfma_f32_16x16x32_bf16 v[156:159], a[28:31], a[124:127], v[156:159]\n" \
"buffer_load_dwordx4 v[116:119], v20, s[56:59], s72 offen offset:0 sc1\n" \
"v_mfma_f32_16x16x32_bf16 v[156:159], a[24:27], a[120:123], v[156:159]\n" \
"v_mfma_f32_16x16x32_bf16 v[156:159], a[20:23], a[116:119], v[156:159]\n" \
"v_mfma_f32_16x16x32_bf16 v[156:159], a[16:19], a[112:115], v[156:159]\n" \
"v_mfma_f32_16x16x32_bf16 v[224:227], a[16:19], a[220:223], v[224:227]\n" \
"v_mfma_f32_16x16x32_bf16 v[224:227], a[20:23], a[224:227], v[224:227]\n" \
"buffer_load_dwordx4 v[124:127], v20, s[56:59], s73 offen offset:2048 sc1\n" \
"v_mfma_f32_16x16x32_bf16 v[224:227], a[24:27], a[228:231], v[224:227]\n" \
"v_mfma_f32_16x16x32_bf16 v[224:227], a[28:31], a[232:235], v[224:227]\n" \
"v_mfma_f32_16x16x32_bf16 v[228:231], a[28:31], a[248:251], v[228:231]\n" \
"ds_read_b64_tr_b16 a[28:29], v10 offset:46080\n" \
"ds_read_b64_tr_b16 a[30:31], v10 offset:50688\n" \
"v_cvt_pk_bf16_f32 v154, v156, v157\n" \
"v_cvt_pk_bf16_f32 v155, v158, v159\n" \
"v_mfma_f32_16x16x32_bf16 v[228:231], a[24:27], a[244:247], v[228:231]\n" \
"buffer_store_dwordx4 v[152:155], v20, s[56:59], s94 offen offset:0 sc1\n" \
"v_mfma_f32_16x16x32_bf16 v[228:231], a[20:23], a[240:243], v[228:231]\n" \
"ds_read_b64_tr_b16 a[20:21], v10 offset:27648\n" \
"v_cvt_pk_bf16_f32 v224, v224, v225\n" \
"v_cvt_pk_bf16_f32 v225, v226, v227\n" \
"v_mfma_f32_16x16x32_bf16 v[228:231], a[16:19], a[236:239], v[228:231]\n" \
"ds_read_b64_tr_b16 a[16:17], v33 offset:192\n" \
"ds_read_b64_tr_b16 a[18:19], v33 offset:4800\n" \
"v_mfma_f32_16x16x32_bf16 v[232:235], a[192:195], a[32:35], v[232:235]\n" \
"ds_read_b64_tr_b16 a[22:23], v10 offset:32256\n" \
"ds_read_b64_tr_b16 a[24:25], v10 offset:36864\n" \
"v_mfma_f32_16x16x32_bf16 v[232:235], a[196:199], a[36:39], v[232:235]\n" \
"ds_read_b64_tr_b16 a[26:27], v10 offset:41472\n" \
"buffer_load_dwordx4 v[132:135], v38, s[56:59], s92 offen offset:0 sc1\n" \
"v_cvt_pk_bf16_f32 v226, v228, v229\n" \
"v_cvt_pk_bf16_f32 v227, v230, v231\n" \
"v_mfma_f32_16x16x32_bf16 v[232:235], a[200:203], a[40:43], v[232:235]\n" \
"v_readlane_b32 s92, v11, 50\n" \
"v_mfma_f32_16x16x32_bf16 v[232:235], a[204:207], a[44:47], v[232:235]\n" \
"buffer_store_dwordx4 v[224:227], v20, s[56:59], s94 offen offset:1024 sc1\n" \
"s_add_u32 s94, s94, 0x1000\n" \
"v_mfma_f32_16x16x32_bf16 v[236:239], a[204:207], a[60:63], v[236:239]\n" \
"ds_read_b64_tr_b16 a[204:205], v33 offset:36864\n" \
"ds_read_b64_tr_b16 a[206:207], v33 offset:41472\n" \
"v_mfma_f32_16x16x32_bf16 v[236:239], a[200:203], a[56:59], v[236:239]\n" \
"ds_read_b64_tr_b16 a[200:201], v33 offset:27648\n" \
"ds_read_b64_tr_b16 a[202:203], v33 offset:32256\n" \
"v_mfma_f32_16x16x32_bf16 v[236:239], a[196:199], a[52:55], v[236:239]\n" \
"ds_read_b64_tr_b16 a[196:197], v33 offset:18432\n" \
"ds_read_b64_tr_b16 a[198:199], v33 offset:23040\n" \
"v_mfma_f32_16x16x32_bf16 v[236:239], a[192:195], a[48:51], v[236:239]\n" \
"ds_read_b64_tr_b16 a[192:193], v10 offset:55296\n" \
"ds_read_b64_tr_b16 a[194:195], v10 offset:59904\n" \
"buffer_load_dwordx4 v[140:143], v38, s[56:59], s93 offen offset:2048 sc1\n" \
"v_mfma_f32_16x16x32_bf16 v[160:163], a[208:211], a[128:131], v[160:163]\n" \
"v_readlane_b32 s93, v11, 41\n" \
"v_mfma_f32_16x16x32_bf16 v[160:163], a[212:215], a[132:135], v[160:163]\n" \
"v_mfma_f32_16x16x32_bf16 v[160:163], a[216:219], a[136:139], v[160:163]\n" \
"s_waitcnt lgkmcnt(14)\n" \
"v_mfma_f32_16x16x32_bf16 v[160:163], a[0:3], a[140:143], v[160:163]\n" \
"buffer_load_dword v37, v40, s[56:59], s74 offen sc1\n" \
"v_mfma_f32_16x16x32_bf16 v[164:167], a[0:3], a[156:159], v[164:167]\n" \
"v_mfma_f32_16x16x32_bf16 v[164:167], a[216:219], a[152:155], v[164:167]\n" \
"v_mfma_f32_16x16x32_bf16 v[164:167], a[212:215], a[148:151], v[164:167]\n" \
"v_mfma_f32_16x16x32_bf16 v[164:167], a[208:211], a[144:147], v[164:167]\n" \
"v_mfma_f32_16x16x32_bf16 v[232:235], a[208:211], a[160:163], v[232:235]\n" \
"v_mfma_f32_16x16x32_bf16 v[232:235], a[212:215], a[164:167], v[232:235]\n" \
"v_mfma_f32_16x16x32_bf16 v[232:235], a[216:219], a[168:171], v[232:235]\n" \
"v_mfma_f32_16x16x32_bf16 v[232:235], a[0:3], a[172:175], v[232:235]\n" \
"s_waitcnt vmcnt(11)\n" \
"s_mov_b64 exec, 1\n" \
"buffer_store_dword v39, off, s[56:59], s71 offset:20 sc1\n" \
"s_mov_b64 exec, -1\n" \
"v_mfma_f32_16x16x32_bf16 v[236:239], a[0:3], a[188:191], v[236:239]\n" \
"ds_read_b64_tr_b16 a[0:1], v34 offset:18432\n" \
"ds_read_b64_tr_b16 a[2:3], v34 offset:23040\n" \
"v_mfma_f32_16x16x32_bf16 v[236:239], a[216:219], a[184:187], v[236:239]\n" \
"ds_read_b64_tr_b16 a[216:217], v34 offset:9216\n" \
"ds_read_b64_tr_b16 a[218:219], v34 offset:13824\n" \
"v_mfma_f32_16x16x32_bf16 v[236:239], a[212:215], a[180:183], v[236:239]\n" \
"ds_read_b64_tr_b16 a[212:213], v34 offset:0\n" \
"ds_read_b64_tr_b16 a[214:215], v34 offset:4608\n" \
"v_mfma_f32_16x16x32_bf16 v[236:239], a[208:211], a[176:179], v[236:239]\n" \
"ds_read_b64_tr_b16 a[208:209], v33 offset:46080\n" \
"ds_read_b64_tr_b16 a[210:211], v33 offset:50688\n" \
"v_mfma_f32_16x16x32_bf16 v[160:163], a[4:7], a[96:99], v[160:163]\n" \
"v_mfma_f32_16x16x32_bf16 v[160:163], a[8:11], a[100:103], v[160:163]\n" \
"v_mfma_f32_16x16x32_bf16 v[160:163], a[12:15], a[104:107], v[160:163]\n" \
"s_waitcnt lgkmcnt(8)\n" \
"v_mfma_f32_16x16x32_bf16 v[160:163], a[16:19], a[108:111], v[160:163]\n" \
"v_mfma_f32_16x16x32_bf16 v[164:167], a[16:19], a[124:127], v[164:167]\n" \
"v_mfma_f32_16x16x32_bf16 v[164:167], a[12:15], a[120:123], v[164:167]\n" \
"v_mfma_f32_16x16x32_bf16 v[164:167], a[8:11], a[116:119], v[164:167]\n" \
"v_mfma_f32_16x16x32_bf16 v[164:167], a[4:7], a[112:115], v[164:167]\n" \
"v_mfma_f32_16x16x32_bf16 v[232:235], a[4:7], a[220:223], v[232:235]\n" \
"v_mfma_f32_16x16x32_bf16 v[232:235], a[8:11], a[224:227], v[232:235]\n" \
"v_mfma_f32_16x16x32_bf16 v[232:235], a[12:15], a[228:231], v[232:235]\n" \
"v_mfma_f32_16x16x32_bf16 v[232:235], a[16:19], a[232:235], v[232:235]\n" \
"v_cvt_pk_bf16_f32 v160, v160, v161\n" \
"v_cvt_pk_bf16_f32 v161, v162, v163\n" \
"v_mfma_f32_16x16x32_bf16 v[236:239], a[16:19], a[248:251], v[236:239]\n" \
"ds_read_b64_tr_b16 a[16:17], v10 offset:46112\n" \
"v_cvt_pk_bf16_f32 v162, v164, v165\n" \
"v_cvt_pk_bf16_f32 v163, v166, v167\n" \
"v_mfma_f32_16x16x32_bf16 v[236:239], a[12:15], a[244:247], v[236:239]\n" \
"buffer_store_dwordx4 v[160:163], v20, s[56:59], s95 offen offset:0 sc1\n" \
"v_cvt_pk_bf16_f32 v232, v232, v233\n" \
"v_cvt_pk_bf16_f32 v233, v234, v235\n" \
"v_mfma_f32_16x16x32_bf16 v[236:239], a[8:11], a[240:243], v[236:239]\n" \
"ds_read_b64_tr_b16 a[8:9], v10 offset:27680\n" \
"ds_read_b64_tr_b16 a[10:11], v10 offset:32288\n" \
"v_mfma_f32_16x16x32_bf16 v[236:239], a[4:7], a[236:239], v[236:239]\n" \
"ds_read_b64_tr_b16 a[4:5], v33 offset:0\n" \
"ds_read_b64_tr_b16 a[6:7], v33 offset:4608\n" \
"v_mfma_f32_16x16x32_bf16 v[184:187], a[20:23], a[32:35], v[184:187]\n" \
"ds_read_b64_tr_b16 a[12:13], v10 offset:36896\n" \
"ds_read_b64_tr_b16 a[14:15], v10 offset:41504\n" \
"v_mfma_f32_16x16x32_bf16 v[184:187], a[24:27], a[36:39], v[184:187]\n" \
"ds_read_b64_tr_b16 a[18:19], v10 offset:50720\n" \
"v_mfma_f32_16x16x32_bf16 v[184:187], a[28:31], a[40:43], v[184:187]\n" \
"v_cvt_pk_bf16_f32 v234, v236, v237\n" \
"v_cvt_pk_bf16_f32 v235, v238, v239\n" \
"v_mfma_f32_16x16x32_bf16 v[184:187], a[192:195], a[44:47], v[184:187]\n" \
"buffer_store_dwordx4 v[232:235], v20, s[56:59], s95 offen offset:1024 sc1\n" \
"s_add_u32 s95, s95, 0x800\n" \
"v_mfma_f32_16x16x32_bf16 v[188:191], a[192:195], a[60:63], v[188:191]\n" \
"ds_read_b64_tr_b16 a[192:193], v33 offset:36896\n" \
"ds_read_b64_tr_b16 a[194:195], v33 offset:41504\n" \
"v_mfma_f32_16x16x32_bf16 v[188:191], a[28:31], a[56:59], v[188:191]\n" \
"ds_read_b64_tr_b16 a[28:29], v33 offset:27680\n" \
"ds_read_b64_tr_b16 a[30:31], v33 offset:32288\n" \
"v_mfma_f32_16x16x32_bf16 v[188:191], a[24:27], a[52:55], v[188:191]\n" \
"ds_read_b64_tr_b16 a[24:25], v33 offset:18464\n" \
"ds_read_b64_tr_b16 a[26:27], v33 offset:23072\n" \
"s_mov_b64 exec, 1\n" \
"buffer_store_dword v46, off, s[56:59], s92 offset:0 sc1\n" \
"s_mov_b64 exec, -1\n" \
"v_mfma_f32_16x16x32_bf16 v[188:191], a[20:23], a[48:51], v[188:191]\n" \
"ds_read_b64_tr_b16 a[20:21], v10 offset:55328\n" \
"ds_read_b64_tr_b16 a[22:23], v10 offset:59936\n" \
"v_mfma_f32_16x16x32_bf16 v[184:187], a[196:199], a[160:163], v[184:187]\n" \
"v_mfma_f32_16x16x32_bf16 v[184:187], a[200:203], a[164:167], v[184:187]\n" \
"v_mfma_f32_16x16x32_bf16 v[184:187], a[204:207], a[168:171], v[184:187]\n" \
"s_waitcnt lgkmcnt(15)\n" \
"v_mfma_f32_16x16x32_bf16 v[184:187], a[208:211], a[172:175], v[184:187]\n" \
"v_mfma_f32_16x16x32_bf16 v[188:191], a[208:211], a[188:191], v[188:191]\n" \
"ds_read_b64_tr_b16 a[208:209], v34 offset:18464\n" \
"ds_read_b64_tr_b16 a[210:211], v34 offset:23072\n" \
"v_mfma_f32_16x16x32_bf16 v[188:191], a[204:207], a[184:187], v[188:191]\n" \
"ds_read_b64_tr_b16 a[204:205], v34 offset:9248\n" \
"ds_read_b64_tr_b16 a[206:207], v34 offset:13856\n" \
"v_mfma_f32_16x16x32_bf16 v[188:191], a[200:203], a[180:183], v[188:191]\n" \
"ds_read_b64_tr_b16 a[200:201], v34 offset:32\n" \
"ds_read_b64_tr_b16 a[202:203], v34 offset:4640\n" \
"v_mfma_f32_16x16x32_bf16 v[188:191], a[196:199], a[176:179], v[188:191]\n" \
"ds_read_b64_tr_b16 a[196:197], v33 offset:46112\n" \
"ds_read_b64_tr_b16 a[198:199], v33 offset:50720\n" \
"v_mfma_f32_16x16x32_bf16 v[184:187], a[212:215], a[220:223], v[184:187]\n" \
"v_mfma_f32_16x16x32_bf16 v[184:187], a[216:219], a[224:227], v[184:187]\n" \
"v_mfma_f32_16x16x32_bf16 v[184:187], a[0:3], a[228:231], v[184:187]\n" \
"s_waitcnt lgkmcnt(13)\n" \
"v_mfma_f32_16x16x32_bf16 v[184:187], a[4:7], a[232:235], v[184:187]\n" \
"v_mfma_f32_16x16x32_bf16 v[188:191], a[4:7], a[248:251], v[188:191]\n" \
"ds_read_b64_tr_b16 a[4:5], v10 offset:46144\n" \
"ds_read_b64_tr_b16 a[6:7], v10 offset:50752\n" \
"v_mfma_f32_16x16x32_bf16 v[188:191], a[0:3], a[244:247], v[188:191]\n" \
"ds_read_b64_tr_b16 a[0:1], v10 offset:36928\n" \
"ds_read_b64_tr_b16 a[2:3], v10 offset:41536\n" \
"v_mfma_f32_16x16x32_bf16 v[188:191], a[216:219], a[240:243], v[188:191]\n" \
"ds_read_b64_tr_b16 a[216:217], v10 offset:27712\n" \
"v_cvt_pk_bf16_f32 v184, v184, v185\n" \
"v_cvt_pk_bf16_f32 v185, v186, v187\n" \
"v_mfma_f32_16x16x32_bf16 v[188:191], a[212:215], a[236:239], v[188:191]\n" \
"ds_read_b64_tr_b16 a[212:213], v33 offset:32\n" \
"ds_read_b64_tr_b16 a[214:215], v33 offset:4640\n" \
"s_waitcnt vmcnt(6)\n" \
"s_mov_b64 exec, 1\n" \
"buffer_store_dword v39, off, s[56:59], s94 offset:24 sc1\n" \
"s_mov_b64 exec, -1\n" \
"s_waitcnt vmcnt(5)\n" \
"v_cmp_eq_u32_e64 s[98:99], s62, v37\n" \
"v_cvt_pk_bf16_f32 v186, v188, v189\n" \
"v_cvt_pk_bf16_f32 v187, v190, v191\n" \
"v_mfma_f32_16x16x32_bf16 v[192:195], a[8:11], a[32:35], v[192:195]\n" \
"ds_read_b64_tr_b16 a[218:219], v10 offset:32320\n" \
"s_and_b32 s99, s98, 0x0\n" \
"buffer_store_dwordx4 v[184:187], v20, s[56:59], s97 offen offset:1024 sc1\n" \
"s_add_u32 s97, s97, 0x3800\n" \
"v_mfma_f32_16x16x32_bf16 v[192:195], a[12:15], a[36:39], v[192:195]\n" \
"s_or_b32 s84, s84, s99\n" \
"v_readlane_b32 s94, v11, 39\n" \
"v_mfma_f32_16x16x32_bf16 v[192:195], a[16:19], a[40:43], v[192:195]\n" \
"s_waitcnt lgkmcnt(8)\n" \
"v_mfma_f32_16x16x32_bf16 v[192:195], a[20:23], a[44:47], v[192:195]\n" \
"s_andn2_b32 s99, s100, s98\n" \
"s_cselect_b32 s99, 0, 16\n" \
"v_mfma_f32_16x16x32_bf16 v[196:199], a[20:23], a[60:63], v[196:199]\n" \
"ds_read_b64_tr_b16 a[20:21], v33 offset:36928\n" \
"ds_read_b64_tr_b16 a[22:23], v33 offset:41536\n" \
"v_mfma_f32_16x16x32_bf16 v[196:199], a[16:19], a[56:59], v[196:199]\n" \
"ds_read_b64_tr_b16 a[16:17], v33 offset:27712\n" \
"ds_read_b64_tr_b16 a[18:19], v33 offset:32320\n" \
"v_mfma_f32_16x16x32_bf16 v[196:199], a[12:15], a[52:55], v[196:199]\n" \
"ds_read_b64_tr_b16 a[12:13], v33 offset:18496\n" \
"ds_read_b64_tr_b16 a[14:15], v33 offset:23104\n" \
"v_mfma_f32_16x16x32_bf16 v[196:199], a[8:11], a[48:51], v[196:199]\n" \
"ds_read_b64_tr_b16 a[8:9], v10 offset:55360\n" \
"ds_read_b64_tr_b16 a[10:11], v10 offset:59968\n" \
"v_mfma_f32_16x16x32_bf16 v[192:195], a[24:27], a[160:163], v[192:195]\n" \
"s_or_b32 s84, s84, s99\n" \
"s_bitcmp1_b32 s84, 4\n" \
"s_cselect_b32 s96, s75, 0x40000000\n" \
"buffer_load_dwordx4 v[176:179], v20, s[56:59], s96 offen offset:0 sc1\n" \
"v_mfma_f32_16x16x32_bf16 v[192:195], a[28:31], a[164:167], v[192:195]\n" \
"v_mfma_f32_16x16x32_bf16 v[192:195], a[192:195], a[168:171], v[192:195]\n" \
"v_mfma_f32_16x16x32_bf16 v[192:195], a[196:199], a[172:175], v[192:195]\n" \
"buffer_load_dword v37, v40, s[56:59], s74 offen sc1\n" \
"v_mfma_f32_16x16x32_bf16 v[196:199], a[196:199], a[188:191], v[196:199]\n" \
"ds_read_b64_tr_b16 a[196:197], v34 offset:18496\n" \
"ds_read_b64_tr_b16 a[198:199], v34 offset:23104\n" \
"v_mfma_f32_16x16x32_bf16 v[196:199], a[192:195], a[184:187], v[196:199]\n" \
"ds_read_b64_tr_b16 a[192:193], v34 offset:9280\n" \
"ds_read_b64_tr_b16 a[194:195], v34 offset:13888\n" \
"v_mfma_f32_16x16x32_bf16 v[196:199], a[28:31], a[180:183], v[196:199]\n" \
"buffer_load_dwordx4 v[248:251], v20, s[56:59], s96 offen offset:2048 sc1\n" \
"v_mfma_f32_16x16x32_bf16 v[196:199], a[24:27], a[176:179], v[196:199]\n" \
"ds_read_b64_tr_b16 a[24:25], v33 offset:46144\n" \
"ds_read_b64_tr_b16 a[26:27], v33 offset:50752\n" \
"v_mfma_f32_16x16x32_bf16 v[192:195], a[200:203], a[220:223], v[192:195]\n" \
"L_pf23:\n" \
"s_cbranch_execz L_retB\n" \
"s_nop 0\n" \
"ds_read_b64_tr_b16 a[28:29], v34 offset:64\n" \
"ds_read_b64_tr_b16 a[30:31], v34 offset:4672\n" \
"v_mfma_f32_16x16x32_bf16 v[192:195], a[204:207], a[224:227], v[192:195]\n" \
"buffer_load_dwordx4 v[180:183], v38, s[56:59], s96 offen offset:0 sc1\n" \
"v_mfma_f32_16x16x32_bf16 v[192:195], a[208:211], a[228:231], v[192:195]\n" \
"s_waitcnt lgkmcnt(14)\n" \
"v_mfma_f32_16x16x32_bf16 v[192:195], a[212:215], a[232:235], v[192:195]\n" \
"v_mfma_f32_16x16x32_bf16 v[196:199], a[212:215], a[248:251], v[196:199]\n" \
"buffer_load_dwordx4 v[252:255], v38, s[56:59], s96 offen offset:2048 sc1\n" \
"v_mfma_f32_16x16x32_bf16 v[196:199], a[208:211], a[244:247], v[196:199]\n" \
"ds_read_b64_tr_b16 a[208:209], v10 offset:36960\n" \
"ds_read_b64_tr_b16 a[210:211], v10 offset:41568\n" \
"v_mfma_f32_16x16x32_bf16 v[196:199], a[204:207], a[240:243], v[196:199]\n" \
"ds_read_b64_tr_b16 a[204:205], v10 offset:27744\n" \
"ds_read_b64_tr_b16 a[206:207], v10 offset:32352\n" \
"v_cvt_pk_bf16_f32 v192, v192, v193\n" \
"v_cvt_pk_bf16_f32 v193, v194, v195\n" \
"v_mfma_f32_16x16x32_bf16 v[196:199], a[200:203], a[236:239], v[196:199]\n" \
"buffer_load_dwordx4 v[112:115], v47, s[56:59], s96 offen offset:0 sc1\n" \
"v_mfma_f32_16x16x32_bf16 v[200:203], a[216:219], a[32:35], v[200:203]\n" \
"ds_read_b64_tr_b16 a[200:201], v33 offset:64\n" \
"ds_read_b64_tr_b16 a[202:203], v33 offset:4672\n" \
"v_mfma_f32_16x16x32_bf16 v[200:203], a[0:3], a[36:39], v[200:203]\n" \
"ds_read_b64_tr_b16 a[212:213], v10 offset:46176\n" \
"ds_read_b64_tr_b16 a[214:215], v10 offset:50784\n" \
"v_mfma_f32_16x16x32_bf16 v[200:203], a[4:7], a[40:43], v[200:203]\n" \
"v_cvt_pk_bf16_f32 v194, v196, v197\n" \
"v_cvt_pk_bf16_f32 v195, v198, v199\n" \
"s_waitcnt lgkmcnt(13)\n" \
"v_mfma_f32_16x16x32_bf16 v[200:203], a[8:11], a[44:47], v[200:203]\n" \
"buffer_store_dwordx4 v[192:195], v20, s[56:59], s64 offen offset:1024 sc1\n" \
"s_add_u32 s64, s64, 0x3000\n" \
"v_mfma_f32_16x16x32_bf16 v[204:207], a[8:11], a[60:63], v[204:207]\n" \
"ds_read_b64_tr_b16 a[8:9], v33 offset:36960\n" \
"ds_read_b64_tr_b16 a[10:11], v33 offset:41568\n" \
"v_mfma_f32_16x16x32_bf16 v[204:207], a[4:7], a[56:59], v[204:207]\n" \
"ds_read_b64_tr_b16 a[4:5], v33 offset:27744\n" \
"ds_read_b64_tr_b16 a[6:7], v33 offset:32352\n" \
"v_mfma_f32_16x16x32_bf16 v[204:207], a[0:3], a[52:55], v[204:207]\n" \
"buffer_load_dwordx4 v[120:123], v47, s[56:59], s96 offen offset:2048 sc1\n" \
"v_mfma_f32_16x16x32_bf16 v[204:207], a[216:219], a[48:51], v[204:207]\n" \
"ds_read_b64_tr_b16 a[216:217], v10 offset:55392\n" \
"ds_read_b64_tr_b16 a[218:219], v10 offset:60000\n" \
"v_mfma_f32_16x16x32_bf16 v[200:203], a[12:15], a[160:163], v[200:203]\n" \
"ds_read_b64_tr_b16 a[0:1], v33 offset:18528\n" \
"ds_read_b64_tr_b16 a[2:3], v33 offset:23136\n" \
"v_mfma_f32_16x16x32_bf16 v[200:203], a[16:19], a[164:167], v[200:203]\n" \
"buffer_load_dwordx4 v[128:131], v54, s[56:59], s96 offen offset:0 sc1\n" \
"v_mfma_f32_16x16x32_bf16 v[200:203], a[20:23], a[168:171], v[200:203]\n" \
"s_waitcnt lgkmcnt(15)\n" \
"v_mfma_f32_16x16x32_bf16 v[200:203], a[24:27], a[172:175], v[200:203]\n" \
"v_mfma_f32_16x16x32_bf16 v[204:207], a[24:27], a[188:191], v[204:207]\n" \
"buffer_load_dwordx4 v[136:139], v54, s[56:59], s96 offen offset:2048 sc1\n" \
"s_waitcnt vmcnt(13)\n" \
"s_mov_b64 exec, 1\n" \
"buffer_store_dword v39, off, s[56:59], s95 offset:28 sc1\n" \
"s_mov_b64 exec, -1\n" \
"v_mfma_f32_16x16x32_bf16 v[204:207], a[20:23], a[184:187], v[204:207]\n" \
"ds_read_b64_tr_b16 a[20:21], v34 offset:9312\n" \
"ds_read_b64_tr_b16 a[22:23], v34 offset:13920\n" \
"v_mfma_f32_16x16x32_bf16 v[204:207], a[16:19], a[180:183], v[204:207]\n" \
"ds_read_b64_tr_b16 a[16:17], v34 offset:96\n" \
"ds_read_b64_tr_b16 a[18:19], v34 offset:4704\n" \
"v_mfma_f32_16x16x32_bf16 v[204:207], a[12:15], a[176:179], v[204:207]\n" \
"ds_read_b64_tr_b16 a[12:13], v33 offset:46176\n" \
"ds_read_b64_tr_b16 a[14:15], v33 offset:50784\n" \
"v_mfma_f32_16x16x32_bf16 v[200:203], a[28:31], a[220:223], v[200:203]\n" \
"ds_read_b64_tr_b16 a[24:25], v34 offset:18528\n" \
"ds_read_b64_tr_b16 a[26:27], v34 offset:23136\n" \
"v_mfma_f32_16x16x32_bf16 v[200:203], a[192:195], a[224:227], v[200:203]\n" \
"v_readlane_b32 s95, v11, 40\n" \
"v_mfma_f32_16x16x32_bf16 v[200:203], a[196:199], a[228:231], v[200:203]\n" \
"s_waitcnt lgkmcnt(12)\n" \
"v_mfma_f32_16x16x32_bf16 v[200:203], a[200:203], a[232:235], v[200:203]\n" \
"v_mfma_f32_16x16x32_bf16 v[204:207], a[200:203], a[248:251], v[204:207]\n" \
"ds_read_b64_tr_b16 a[200:201], v10 offset:46304\n" \
"ds_read_b64_tr_b16 a[202:203], v10 offset:50912\n" \
"v_mfma_f32_16x16x32_bf16 v[204:207], a[196:199], a[244:247], v[204:207]\n" \
"ds_read_b64_tr_b16 a[196:197], v10 offset:37088\n" \
"ds_read_b64_tr_b16 a[198:199], v10 offset:41696\n" \
"v_mfma_f32_16x16x32_bf16 v[204:207], a[192:195], a[240:243], v[204:207]\n" \
"ds_read_b64_tr_b16 a[192:193], v10 offset:27872\n" \
"v_cvt_pk_bf16_f32 v200, v200, v201\n" \
"v_cvt_pk_bf16_f32 v201, v202, v203\n" \
"v_mfma_f32_16x16x32_bf16 v[204:207], a[28:31], a[236:239], v[204:207]\n" \
"ds_read_b64_tr_b16 a[28:29], v33 offset:96\n" \
"ds_read_b64_tr_b16 a[30:31], v33 offset:4704\n" \
"v_mfma_f32_16x16x32_bf16 v[208:211], a[204:207], a[32:35], v[208:211]\n" \
"ds_read_b64_tr_b16 a[194:195], v10 offset:32480\n" \
"v_mfma_f32_16x16x32_bf16 v[208:211], a[208:211], a[36:39], v[208:211]\n" \
"v_mfma_f32_16x16x32_bf16 v[208:211], a[212:215], a[40:43], v[208:211]\n" \
"s_waitcnt lgkmcnt(15)\n" \
"v_mfma_f32_16x16x32_bf16 v[208:211], a[216:219], a[44:47], v[208:211]\n" \
"v_cvt_pk_bf16_f32 v202, v204, v205\n" \
"v_cvt_pk_bf16_f32 v203, v206, v207\n" \
"v_mfma_f32_16x16x32_bf16 v[212:215], a[216:219], a[60:63], v[212:215]\n" \
"buffer_store_dwordx4 v[200:203], v20, s[56:59], s66 offen offset:1024 sc1\n" \
"s_add_u32 s66, s66, 0x2800\n" \
"v_mfma_f32_16x16x32_bf16 v[212:215], a[212:215], a[56:59], v[212:215]\n" \
"ds_read_b64_tr_b16 a[212:213], v33 offset:27872\n" \
"ds_read_b64_tr_b16 a[214:215], v33 offset:32480\n" \
"v_mfma_f32_16x16x32_bf16 v[212:215], a[208:211], a[52:55], v[212:215]\n" \
"ds_read_b64_tr_b16 a[208:209], v33 offset:18656\n" \
"ds_read_b64_tr_b16 a[210:211], v33 offset:23264\n" \
"v_mfma_f32_16x16x32_bf16 v[212:215], a[204:207], a[48:51], v[212:215]\n" \
"ds_read_b64_tr_b16 a[204:205], v10 offset:55520\n" \
"ds_read_b64_tr_b16 a[206:207], v10 offset:60128\n" \
"v_mfma_f32_16x16x32_bf16 v[208:211], a[0:3], a[160:163], v[208:211]\n" \
"ds_read_b64_tr_b16 a[216:217], v33 offset:37088\n" \
"ds_read_b64_tr_b16 a[218:219], v33 offset:41696\n" \
"v_mfma_f32_16x16x32_bf16 v[208:211], a[4:7], a[164:167], v[208:211]\n" \
"v_mfma_f32_16x16x32_bf16 v[208:211], a[8:11], a[168:171], v[208:211]\n" \
"s_waitcnt lgkmcnt(14)\n" \
"v_mfma_f32_16x16x32_bf16 v[208:211], a[12:15], a[172:175], v[208:211]\n" \
"v_mfma_f32_16x16x32_bf16 v[212:215], a[12:15], a[188:191], v[212:215]\n" \
"ds_read_b64_tr_b16 a[12:13], v34 offset:18656\n" \
"ds_read_b64_tr_b16 a[14:15], v34 offset:23264\n" \
"s_waitcnt vmcnt(12)\n" \
"s_mov_b64 exec, 1\n" \
"buffer_store_dword v39, off, s[56:59], s97 offset:32 sc1\n" \
"s_mov_b64 exec, -1\n" \
"v_mfma_f32_16x16x32_bf16 v[212:215], a[8:11], a[184:187], v[212:215]\n" \
"ds_read_b64_tr_b16 a[8:9], v34 offset:9440\n" \
"ds_read_b64_tr_b16 a[10:11], v34 offset:14048\n" \
"v_mfma_f32_16x16x32_bf16 v[212:215], a[4:7], a[180:183], v[212:215]\n" \
"ds_read_b64_tr_b16 a[4:5], v34 offset:224\n" \
"ds_read_b64_tr_b16 a[6:7], v34 offset:4832\n" \
"v_mfma_f32_16x16x32_bf16 v[212:215], a[0:3], a[176:179], v[212:215]\n" \
"ds_read_b64_tr_b16 a[0:1], v33 offset:46304\n" \
"ds_read_b64_tr_b16 a[2:3], v33 offset:50912\n" \
"v_mfma_f32_16x16x32_bf16 v[208:211], a[16:19], a[220:223], v[208:211]\n" \
"s_waitcnt vmcnt(11)\n" \
"v_cmp_eq_u32_e64 s[98:99], s62, v37\n" \
"buffer_load_dword v37, v40, s[56:59], s74 offen sc1\n" \
"v_mfma_f32_16x16x32_bf16 v[208:211], a[20:23], a[224:227], v[208:211]\n" \
"s_and_b32 s99, s98, 0x20\n" \
"s_or_b32 s84, s84, s99\n" \
"s_bitcmp1_b32 s84, 5\n" \
"s_cselect_b32 s65, s63, 0x40000000\n" \
"v_mfma_f32_16x16x32_bf16 v[208:211], a[24:27], a[228:231], v[208:211]\n" \
"s_waitcnt lgkmcnt(12)\n" \
"v_mfma_f32_16x16x32_bf16 v[208:211], a[28:31], a[232:235], v[208:211]\n" \
"v_mfma_f32_16x16x32_bf16 v[212:215], a[28:31], a[248:251], v[212:215]\n" \
"buffer_load_dwordx4 v[148:151], v47, s[56:59], s65 offen offset:0 sc1\n" \
"v_mfma_f32_16x16x32_bf16 v[212:215], a[24:27], a[244:247], v[212:215]\n" \
"ds_read_b64_tr_b16 a[24:25], v36 offset:2048\n" \
"ds_read_b64_tr_b16 a[26:27], v36 offset:3072\n" \
"v_mfma_f32_16x16x32_bf16 v[212:215], a[20:23], a[240:243], v[212:215]\n" \
"ds_read_b64_tr_b16 a[20:21], v36 offset:0\n" \
"ds_read_b64_tr_b16 a[22:23], v36 offset:1024\n" \
"v_cvt_pk_bf16_f32 v208, v208, v209\n" \
"v_cvt_pk_bf16_f32 v209, v210, v211\n" \
"v_mfma_f32_16x16x32_bf16 v[212:215], a[16:19], a[236:239], v[212:215]\n" \
"ds_read_b64_tr_b16 a[16:17], v33 offset:224\n" \
"ds_read_b64_tr_b16 a[18:19], v33 offset:4832\n" \
"v_mfma_f32_16x16x32_bf16 v[240:243], a[192:195], a[32:35], v[240:243]\n" \
"buffer_load_dwordx4 v[220:223], v47, s[56:59], s65 offen offset:1024 sc1\n" \
"v_mfma_f32_16x16x32_bf16 v[240:243], a[196:199], a[36:39], v[240:243]\n" \
"ds_read_b64_tr_b16 a[28:29], v36 offset:32\n" \
"ds_read_b64_tr_b16 a[30:31], v36 offset:1056\n" \
"v_mfma_f32_16x16x32_bf16 v[240:243], a[200:203], a[40:43], v[240:243]\n" \
"v_cvt_pk_bf16_f32 v210, v212, v213\n" \
"v_cvt_pk_bf16_f32 v211, v214, v215\n" \
"s_waitcnt lgkmcnt(10)\n" \
"v_mfma_f32_16x16x32_bf16 v[240:243], a[204:207], a[44:47], v[240:243]\n" \
"buffer_store_dwordx4 v[208:211], v20, s[56:59], s67 offen offset:1024 sc1\n" \
"s_add_u32 s67, s67, 0x2000\n" \
"v_mfma_f32_16x16x32_bf16 v[244:247], a[204:207], a[60:63], v[244:247]\n" \
"v_mfma_f32_16x16x32_bf16 v[244:247], a[200:203], a[56:59], v[244:247]\n" \
"v_mfma_f32_16x16x32_bf16 v[244:247], a[196:199], a[52:55], v[244:247]\n" \
"v_mfma_f32_16x16x32_bf16 v[244:247], a[192:195], a[48:51], v[244:247]\n" \
"ds_read_b64_tr_b16 a[192:193], v36 offset:2080\n" \
"ds_read_b64_tr_b16 a[194:195], v36 offset:3104\n" \
"v_mfma_f32_16x16x32_bf16 v[168:171], a[208:211], a[128:131], v[168:171]\n" \
"v_mfma_f32_16x16x32_bf16 v[168:171], a[212:215], a[132:135], v[168:171]\n" \
"v_mfma_f32_16x16x32_bf16 v[168:171], a[216:219], a[136:139], v[168:171]\n" \
"s_waitcnt lgkmcnt(9)\n" \
"v_mfma_f32_16x16x32_bf16 v[168:171], a[0:3], a[140:143], v[168:171]\n" \
"v_mfma_f32_16x16x32_bf16 v[172:175], a[0:3], a[156:159], v[172:175]\n" \
"v_mfma_f32_16x16x32_bf16 v[172:175], a[216:219], a[152:155], v[172:175]\n" \
"s_waitcnt vmcnt(10)\n" \
"s_mov_b64 exec, 1\n" \
"buffer_store_dword v39, off, s[56:59], s64 offset:36 sc1\n" \
"s_mov_b64 exec, -1\n" \
"v_mfma_f32_16x16x32_bf16 v[172:175], a[212:215], a[148:151], v[172:175]\n" \
"v_mfma_f32_16x16x32_bf16 v[172:175], a[208:211], a[144:147], v[172:175]\n" \
"v_mfma_f32_16x16x32_bf16 v[240:243], a[208:211], a[160:163], v[240:243]\n" \
"v_mfma_f32_16x16x32_bf16 v[240:243], a[212:215], a[164:167], v[240:243]\n" \
"v_mfma_f32_16x16x32_bf16 v[240:243], a[216:219], a[168:171], v[240:243]\n" \
"v_mfma_f32_16x16x32_bf16 v[240:243], a[0:3], a[172:175], v[240:243]\n" \
"v_mfma_f32_16x16x32_bf16 v[244:247], a[0:3], a[188:191], v[244:247]\n" \
"v_mfma_f32_16x16x32_bf16 v[244:247], a[216:219], a[184:187], v[244:247]\n" \
"v_mfma_f32_16x16x32_bf16 v[244:247], a[212:215], a[180:183], v[244:247]\n" \
"v_mfma_f32_16x16x32_bf16 v[244:247], a[208:211], a[176:179], v[244:247]\n" \
"v_mfma_f32_16x16x32_bf16 v[168:171], a[4:7], a[96:99], v[168:171]\n" \
"v_mfma_f32_16x16x32_bf16 v[168:171], a[8:11], a[100:103], v[168:171]\n" \
"v_mfma_f32_16x16x32_bf16 v[168:171], a[12:15], a[104:107], v[168:171]\n" \
"s_waitcnt vmcnt(6)\n" \
"s_mov_b64 exec, 1\n" \
"buffer_store_dword v39, off, s[56:59], s66 offset:40 sc1\n" \
"s_mov_b64 exec, -1\n" \
"s_waitcnt lgkmcnt(0)\n" \
"v_mfma_f32_16x16x32_bf16 v[168:171], a[16:19], a[108:111], v[168:171]\n" \
"v_mfma_f32_16x16x32_bf16 v[172:175], a[16:19], a[124:127], v[172:175]\n" \
"v_mfma_f32_16x16x32_bf16 v[172:175], a[12:15], a[120:123], v[172:175]\n" \
"v_mfma_f32_16x16x32_bf16 v[172:175], a[8:11], a[116:119], v[172:175]\n" \
"v_mfma_f32_16x16x32_bf16 v[172:175], a[4:7], a[112:115], v[172:175]\n" \
"v_mfma_f32_16x16x32_bf16 v[240:243], a[4:7], a[220:223], v[240:243]\n" \
"v_mfma_f32_16x16x32_bf16 v[240:243], a[8:11], a[224:227], v[240:243]\n" \
"v_mfma_f32_16x16x32_bf16 v[240:243], a[12:15], a[228:231], v[240:243]\n" \
"v_mfma_f32_16x16x32_bf16 v[240:243], a[16:19], a[232:235], v[240:243]\n" \
"v_mfma_f32_16x16x32_bf16 v[244:247], a[16:19], a[248:251], v[244:247]\n" \
"v_mfma_f32_16x16x32_bf16 v[244:247], a[12:15], a[244:247], v[244:247]\n" \
"v_mfma_f32_16x16x32_bf16 v[244:247], a[8:11], a[240:243], v[244:247]\n" \
"v_mfma_f32_16x16x32_bf16 v[244:247], a[4:7], a[236:239], v[244:247]\n" \
"v_mfma_f32_16x16x32_bf16 v[168:171], a[20:23], v[64:67], v[168:171]\n" \
"v_mfma_f32_16x16x32_bf16 v[172:175], a[20:23], v[72:75], v[172:175]\n" \
"v_mfma_f32_16x16x32_bf16 v[240:243], a[20:23], v[80:83], v[240:243]\n" \
"v_mfma_f32_16x16x32_bf16 v[244:247], a[20:23], v[88:91], v[244:247]\n" \
"v_mfma_f32_16x16x32_bf16 v[168:171], a[24:27], v[68:71], v[168:171]\n" \
"v_mfma_f32_16x16x32_bf16 v[172:175], a[24:27], v[76:79], v[172:175]\n" \
"v_mfma_f32_16x16x32_bf16 v[240:243], a[24:27], v[84:87], v[240:243]\n" \
"s_waitcnt vmcnt(5)\n" \
"v_cmp_eq_u32_e64 s[98:99], s62, v37\n" \
"v_mfma_f32_16x16x32_bf16 v[244:247], a[24:27], v[92:95], v[244:247]\n" \
"s_and_b32 s99, s98, 0xfc0\n" \
"s_or_b32 s84, s84, s99\n" \
"s_bitcmp1_b32 s84, 6\n" \
"s_cselect_b32 s68, s63, 0x40000000\n" \
"s_bitcmp1_b32 s84, 7\n" \
"s_cselect_b32 s69, s63, 0x40000000\n" \
"s_bitcmp1_b32 s84, 8\n" \
"s_cselect_b32 s70, s63, 0x40000000\n" \
"s_bitcmp1_b32 s84, 9\n" \
"s_cselect_b32 s71, s63, 0x40000000\n" \
"s_bitcmp1_b32 s84, 10\n" \
"s_cselect_b32 s72, s63, 0x40000000\n" \
"s_bitcmp1_b32 s84, 11\n" \
"s_cselect_b32 s73, s63, 0x40000000\n" \
"buffer_load_dwordx4 v[156:159], v47, s[56:59], s68 offen offset:2048 sc1\n" \
"v_mfma_f32_16x16x32_bf16 v[56:59], a[28:31], v[96:99], 0\n" \
"s_waitcnt vmcnt(3)\n" \
"s_mov_b64 exec, 1\n" \
"buffer_store_dword v39, off, s[56:59], s67 offset:44 sc1\n" \
"s_mov_b64 exec, -1\n" \
"v_mfma_f32_16x16x32_bf16 v[60:63], a[28:31], v[104:107], 0\n" \
"v_mfma_f32_16x16x32_bf16 v[56:59], a[192:195], v[100:103], v[56:59]\n" \
"v_mfma_f32_16x16x32_bf16 v[60:63], a[192:195], v[108:111], v[60:63]\n" \
"buffer_load_dwordx4 v[228:231], v47, s[56:59], s68 offen offset:3072 sc1\n" \
"buffer_load_dwordx4 v[164:167], v54, s[56:59], s69 offen offset:0 sc1\n" \
"buffer_load_dwordx4 v[236:239], v54, s[56:59], s69 offen offset:1024 sc1\n" \
"buffer_load_dwordx4 v[188:191], v20, s[56:59], s70 offen offset:1024 sc1\n" \
"buffer_load_dwordx4 v[196:199], v20, s[56:59], s71 offen offset:3072 sc1\n" \
"buffer_load_dwordx4 v[204:207], v38, s[56:59], s72 offen offset:1024 sc1\n" \
"buffer_load_dwordx4 v[212:215], v38, s[56:59], s73 offen offset:3072 sc1\n" \
"s_andn2_b32 s99, 0xff, s84\n" \
"s_cbranch_scc1 L_sx0_%=\n" \
"L_rx0_%=:\n" \
"v_mfma_f32_16x16x32_bf16 v[168:171], v[50:53], v[116:119], v[168:171]\n" \
"v_mfma_f32_16x16x32_bf16 v[172:175], v[48:51], v[116:119], v[172:175]\n" \
"v_mfma_f32_16x16x32_bf16 v[168:171], v[50:53], v[124:127], v[168:171]\n" \
"v_mfma_f32_16x16x32_bf16 v[172:175], v[48:51], v[124:127], v[172:175]\n" \
"v_mfma_f32_16x16x32_bf16 v[168:171], v[50:53], v[132:135], v[168:171]\n" \
"v_mfma_f32_16x16x32_bf16 v[172:175], v[48:51], v[132:135], v[172:175]\n" \
"v_mfma_f32_16x16x32_bf16 v[168:171], v[50:53], v[140:143], v[168:171]\n" \
"v_mfma_f32_16x16x32_bf16 v[172:175], v[48:51], v[140:143], v[172:175]\n" \
"v_mfma_f32_16x16x32_bf16 v[56:59], v[50:53], v[176:179], v[56:59]\n" \
"v_mfma_f32_16x16x32_bf16 v[60:63], v[48:51], v[176:179], v[60:63]\n" \
"v_mfma_f32_16x16x32_bf16 v[56:59], v[50:53], v[248:251], v[56:59]\n" \
"v_mfma_f32_16x16x32_bf16 v[60:63], v[48:51], v[248:251], v[60:63]\n" \
"v_mfma_f32_16x16x32_bf16 v[56:59], v[50:53], v[180:183], v[56:59]\n" \
"v_mfma_f32_16x16x32_bf16 v[60:63], v[48:51], v[180:183], v[60:63]\n" \
"v_mfma_f32_16x16x32_bf16 v[56:59], v[50:53], v[252:255], v[56:59]\n" \
"v_mfma_f32_16x16x32_bf16 v[60:63], v[48:51], v[252:255], v[60:63]\n" \
"v_mfma_f32_16x16x32_bf16 v[56:59], v[50:53], v[112:115], v[56:59]\n" \
"v_mfma_f32_16x16x32_bf16 v[60:63], v[48:51], v[112:115], v[60:63]\n" \
"v_mfma_f32_16x16x32_bf16 v[56:59], v[50:53], v[120:123], v[56:59]\n" \
"v_mfma_f32_16x16x32_bf16 v[60:63], v[48:51], v[120:123], v[60:63]\n" \
"v_mfma_f32_16x16x32_bf16 v[56:59], v[50:53], v[128:131], v[56:59]\n" \
"v_mfma_f32_16x16x32_bf16 v[60:63], v[48:51], v[128:131], v[60:63]\n" \
"v_mfma_f32_16x16x32_bf16 v[56:59], v[50:53], v[136:139], v[56:59]\n" \
"v_mfma_f32_16x16x32_bf16 v[60:63], v[48:51], v[136:139], v[60:63]\n" \
"v_mfma_f32_16x16x32_bf16 v[168:171], v[50:53], v[148:151], v[168:171]\n" \
"v_mfma_f32_16x16x32_bf16 v[172:175], v[48:51], v[148:151], v[172:175]\n" \
"v_mfma_f32_16x16x32_bf16 v[240:243], v[50:53], v[220:223], v[240:243]\n" \
"v_mfma_f32_16x16x32_bf16 v[244:247], v[48:51], v[220:223], v[244:247]\n" \
"s_waitcnt vmcnt(8)\n" \
"v_mfma_f32_16x16x32_bf16 v[168:171], v[50:53], v[156:159], v[168:171]\n" \
"v_mfma_f32_16x16x32_bf16 v[172:175], v[48:51], v[156:159], v[172:175]\n" \
"v_cvt_pk_bf16_f32 v56, v56, v57\n" \
"v_cvt_pk_bf16_f32 v57, v58, v59\n" \
"v_cvt_pk_bf16_f32 v58, v60, v61\n" \
"v_cvt_pk_bf16_f32 v59, v62, v63\n" \
"s_waitcnt vmcnt(6)\n" \
"v_mfma_f32_16x16x32_bf16 v[240:243], v[50:53], v[228:231], v[240:243]\n" \
"v_permlane16_swap_b32_e32 v56, v58\n" \
"v_mfma_f32_16x16x32_bf16 v[244:247], v[48:51], v[228:231], v[244:247]\n" \
"v_permlane16_swap_b32_e32 v57, v59\n" \
"buffer_store_dwordx4 v[56:59], v42, s[88:91], s93 offen sc0 sc1\n" \
"s_waitcnt vmcnt(6)\n" \
"v_mfma_f32_16x16x32_bf16 v[168:171], v[50:53], v[164:167], v[168:171]\n" \
"v_mfma_f32_16x16x32_bf16 v[172:175], v[48:51], v[164:167], v[172:175]\n" \
"s_waitcnt vmcnt(5)\n" \
"v_mfma_f32_16x16x32_bf16 v[240:243], v[50:53], v[236:239], v[240:243]\n" \
"v_mfma_f32_16x16x32_bf16 v[244:247], v[48:51], v[236:239], v[244:247]\n" \
"s_andn2_b32 s99, 0xf00, s84\n" \
"s_cbranch_scc1 L_sx1_%=\n" \
"L_rx1_%=:\n" \
"s_waitcnt vmcnt(4)\n" \
"v_mfma_f32_16x16x32_bf16 v[240:243], v[50:53], v[188:191], v[240:243]\n" \
"v_cvt_pk_bf16_f32 v168, v168, v169\n" \
"v_cvt_pk_bf16_f32 v169, v170, v171\n" \
"v_mfma_f32_16x16x32_bf16 v[244:247], v[48:51], v[188:191], v[244:247]\n" \
"v_cvt_pk_bf16_f32 v170, v172, v173\n" \
"v_cvt_pk_bf16_f32 v171, v174, v175\n" \
"s_waitcnt vmcnt(3)\n" \
"v_mfma_f32_16x16x32_bf16 v[240:243], v[50:53], v[196:199], v[240:243]\n" \
"v_permlane16_swap_b32_e32 v168, v170\n" \
"v_mfma_f32_16x16x32_bf16 v[244:247], v[48:51], v[196:199], v[244:247]\n" \
"v_permlane16_swap_b32_e32 v169, v171\n" \
"buffer_store_dwordx4 v[168:171], v42, s[88:91], s94 offen sc0 sc1\n" \
"s_waitcnt vmcnt(3)\n" \
"v_mfma_f32_16x16x32_bf16 v[240:243], v[50:53], v[204:207], v[240:243]\n" \
"v_mfma_f32_16x16x32_bf16 v[244:247], v[48:51], v[204:207], v[244:247]\n" \
"s_waitcnt vmcnt(2)\n" \
"v_mfma_f32_16x16x32_bf16 v[240:243], v[50:53], v[212:215], v[240:243]\n" \
"v_mfma_f32_16x16x32_bf16 v[244:247], v[48:51], v[212:215], v[244:247]\n" \
"s_nop 6\n" \
"v_cvt_pk_bf16_f32 v240, v240, v241\n" \
"v_cvt_pk_bf16_f32 v241, v242, v243\n" \
"v_cvt_pk_bf16_f32 v242, v244, v245\n" \
"v_cvt_pk_bf16_f32 v243, v246, v247\n" \
"s_nop 0\n" \
"v_permlane16_swap_b32_e32 v240, v242\n" \
"v_permlane16_swap_b32_e32 v241, v243\n" \
"buffer_store_dwordx4 v[240:243], v42, s[88:91], s95 offen sc0 sc1\n"
#define ATT_VCLOB "v21","v22","v23","v32","v33","v34","v35","v36","v37","v38","v39","v40","v41","v42","v43","v44","v45","v46","v47","v48","v49","v50","v51","v52","v53","v54","v55","v56","v57","v58","v59","v60","v61","v62","v63","v64","v65","v66","v67","v68","v69","v70","v71","v72","v73","v74","v75","v76","v77","v78","v79","v80","v81","v82","v83","v84","v85","v86","v87","v88","v89","v90","v91","v92","v93","v94","v95","v96","v97","v98","v99","v100","v101","v102","v103","v104","v105","v106","v107","v108","v109","v110","v111","v112","v113","v114","v115","v116","v117","v118","v119","v120","v121","v122","v123","v124","v125","v126","v127","v128","v129","v130","v131","v132","v133","v134","v135","v136","v137","v138","v139","v140","v141","v142","v143","v144","v145","v146","v147","v148","v149","v150","v151","v152","v153","v154","v155","v156","v157","v158","v159","v160","v161","v162","v163","v164","v165","v166","v167","v168","v169","v170","v171","v172","v173","v174","v175","v176","v177","v178","v179","v180","v181","v182","v183","v184","v185","v186","v187","v188","v189","v190","v191","v192","v193","v194","v195","v196","v197","v198","v199","v200","v201","v202","v203","v204","v205","v206","v207","v208","v209","v210","v211","v212","v213","v214","v215","v216","v217","v218","v219","v220","v221","v222","v223","v224","v225","v226","v227","v228","v229","v230","v231","v232","v233","v234","v235","v236","v237","v238","v239","v240","v241","v242","v243","v244","v245","v246","v247","v248","v249","v250","v251","v252","v253","v254","v255"
#define ATT_SCLOB "s63","s64","s65","s66","s67","s68","s69","s70","s71","s72","s73","s74","s75","s76","s77","s78","s79","s80","s81","s82","s83","s84","s85","s86","s87","s88","s89","s90","s91","s92","s93","s95","s96","s97","s98","s99","s100","s101"
enum { T_fo, T_fa, T_fas, T_fb, T_fbs, T_f8, T_mk8, T_wol, T_woh, T_wbl, T_wbh, T_outl, T_outh, T_r1l, T_r1h, T_r1n, T_r1s, T_m0, T_m1, T_mw, T_m4, T_ml, T_w0, T_w1, T_w2, T_w3, T_A1, T_A2, T_A3, T_B1, T_B2, T_B3, T_Ao, T_Bo, T_Al, T_Bl, T_l8, T_xr, T_x8r, T_oA, T_oB, T_o8, T_x0, T_x1, T_x2, T_x3, T_x4, T_x5, T_x6, T_x8, T_ep, T_xf, T_kf };

__device__ __forceinline__ void out_consts(u32x4 (&vp)[3], const u16* wo, const u16* wob, const u16* out, const unsigned b, const unsigned w, const unsigned lane) {
    const unsigned sp = b & 7u, nt = (b >> 3) & 3u, j = b >> 5, head = 4u * sp + nt, OOR = 0x40000000u;
    unsigned vw[9];
#pragma unroll
    for (unsigned i = 0; i < 9u; ++i) {
        const unsigned q = 64u * i + lane, row = __umul24(q, 3641u) >> 16, c = q - __umul24(row, 18u);
        vw[i] = __umul24(row, DM * 2) + (c < 16u ? (c * 16u + sp * 32u + 32u) & 255u : c * 16u) - (i & 3u) * 1024u;
    }
    const unsigned row = 16u * w + (lane >> 2), r9 = __umul24(16u * (row >> 5) + (row & 12u) + 2u * ((row >> 4) & 1u) + ((row >> 1) & 1u), 18u) + 16u + (row & 1u);
    unsigned vwl = __umul24(r9, DM * 2) + ((lane & 2u) ? 256u : sp * 32u) + (lane & 1u) * 16u, vrd = __umul24(lane >> 2, 288u) + (lane & 3u) * 8u;
    asm volatile("" : "+v"(vw[0]), "+v"(vw[1]), "+v"(vw[2]), "+v"(vw[3]), "+v"(vw[4]), "+v"(vw[5]), "+v"(vw[6]), "+v"(vw[7]), "+v"(vw[8]), "+v"(vwl), "+v"(vrd));
    unsigned tab = 0;
#define TB(n, v) asm("v_writelane_b32 %0, %1, %2" : "+v"(tab) : "s"((unsigned)(v)), "n"((int)T_##n))
    const unsigned FJ = (unsigned)WS_F + (F_J + 640u * j) * 4u, mt = 2u * (w & 2u) + (w & 1u), w2 = w < 2u ? w : 2u, hw = head * 147456u;
    const unsigned FR = (unsigned)WS_F + F_R * 4u + (8u * j + sp) * 1024u;
    TB(fo, FJ + 4u * head); TB(fa, FR + (w | 2u) * 256u); TB(fas, FR + w * 256u + nt * 4u);
    TB(fb, FR + 128u + w * 256u); TB(fbs, FR + 128u + w * 256u + nt * 4u);
    TB(ep, b + w ? OOR : (unsigned)WS_F + F_EPOCH * 4u);
    const u16* pwo = wo + nt * 144u; const u16* pwb = wob + nt * 144u; const u16* pr1 = w < 3u ? pwo : pwb;
    TB(wol, PLO(pwo)); TB(woh, PHI(pwo) & 0xffffu); TB(wbl, PLO(pwb)); TB(wbh, PHI(pwb) & 0xffffu); TB(outl, PLO(out)); TB(outh, PHI(out) & 0xffffu); TB(r1l, PLO(pr1)); TB(r1h, PHI(pr1) & 0xffffu);
    TB(r1n, (w < 3u ? 72u * sp + 71u : 4095u) * 1152u + 288u); TB(r1s, w < 3u ? (72u * sp + 32u * w) * 1152u : hw + 110592u);
    TB(m0, 131072u + 9216u * w2); TB(w0, hw + 36864u * w2); TB(m1, w < 3u ? 9216u * w : 73728u); TB(mw, 9216u * w); TB(m4, w < 3u ? 131072u + 9216u * w : 64512u); TB(ml, 119808u + 1024u * w);
    const unsigned tA = (unsigned)WS_AF + (8u * j + mt) * (KS_TOT * 1024u), tB = (unsigned)WS_AF + (120u - 8u * j + mt) * (KS_TOT * 1024u);
#define HK(k) (4u * sp + ((nt + k) & 3u))
    TB(w1, HK(1u) * 147456u + w * 36864u); TB(w2, HK(2u) * 147456u + w * 36864u); TB(w3, HK(3u) * 147456u + w * 36864u);
    TB(A1, tA + 18432u + HK(1u) * 4096u); TB(A2, tA + 18432u + HK(2u) * 4096u); TB(A3, tA + 18432u + HK(3u) * 4096u);
    TB(B1, tB + 18432u + HK(1u) * 4096u); TB(B2, tB + 18432u + HK(2u) * 4096u); TB(B3, tB + 18432u + HK(3u) * 4096u);
    TB(Ao, tA + sp * 2048u); TB(Bo, tB + sp * 2048u); TB(Al, tA + 16384u); TB(Bl, tB + 16384u);
    const bool red = w == (sp >> 1), blk = sp & 1u;
    const unsigned xg = (unsigned)WS_X + (4u * j + nt) * 589824u + w * 16384u, oA = (128u * j + 16u * mt) * 1152u + nt * 288u, oB = (1920u - 128u * j + 16u * mt) * 1152u + nt * 288u;
    TB(l8, red ? (blk ? tB : tA) + 16384u : OOR); TB(xr, xg + sp * 65536u); TB(x8r, red ? xg + 524288u + (sp & 1u) * 1024u : OOR);
    TB(oA, oA + sp * 32u); TB(oB, oB + sp * 32u); TB(o8, red ? (blk ? oB : oA) + 256u : OOR); TB(x8, xg + 524288u + sp * 2048u);
    TB(f8, xg + w * 131072u + 14400u + sp * 4u); TB(mk8, red ? 0xff0000u : 0u); TB(xf, xg + sp * 65536u + 14336u);
    TB(kf, (unsigned)WS_F + (F_KV + head * 64u) * 4u);
#define XI(i) TB(x##i, xg + ((sp + 1u + i) & 7u) * 65536u + i * 2048u)
    XI(0); XI(1); XI(2); XI(3); XI(4); XI(5); XI(6);
#undef XI
#undef HK
#undef TB
    vp[0] = (u32x4){vw[0], vw[1], vw[2], vw[3]}; vp[1] = (u32x4){vw[4], vw[5], vw[6], vw[7]}; vp[2] = (u32x4){vw[8], vwl, vrd, tab};
}
__global__ __launch_bounds__(256, 1) __attribute__((aligned(4096))) void fused_kernel(const u16* __restrict__ x, const u16* __restrict__ ckv, const u16* __restrict__ wq, const u16* __restrict__ wuk,
                                                       const u16* __restrict__ wuv, const u16* __restrict__ wqkv, const u16* __restrict__ gw,
                                                       const u16* __restrict__ wo, const u16* __restrict__ wob, char* __restrict__ ws, u16* __restrict__ out) {
    using namespace att;
    extern __shared__ __attribute__((aligned(16))) char lds[];
    unsigned wid = rfl(threadIdx.x >> 6);
    code_touch((blockIdx.x >> 3) * 4u + wid, (unsigned)(size_t)lds + 151552u + wid * 512u);
    SBAR();
    asm volatile("" :: "s"(x), "s"(ckv), "s"(wq), "s"(wuk), "s"(wuv), "s"(wqkv), "s"(gw), "s"(wo), "s"(wob), "s"(ws), "s"(out));
    unsigned tag;
    int b = blockIdx.x;
    u32x4 vp[5], sa[5]; unsigned sd[2];
    const unsigned vl = proj_phase(x, ckv, wq, wuk, wuv, wqkv, gw, wo, wob, out, ws, lds, wid, tag, vp, sa, sd);
    asm volatile("" : "+s"(ws));
    unsigned* const flags = (unsigned*)(ws + WS_F);
    RTS(2);
#define AST(L) do {} while (0)
#define SD(J8, H, W) ((unsigned)WS_AF + (((J8) + 2u * ((W) & 2u) + ((W) & 1u)) * KS_TOT + 18u + 4u * (H)) * 1024u)
#define SRDA (u32x4){PLO(ws), PHI(ws) & 0xffffu, (unsigned)WS_L, 0x00020000u}
#define ATT_TAIL "s_endpgm\n"
    unsigned abad;
    asm volatile(ATT_ASM ATT_TAIL "L_end_%=:\n s_nop 1"
                 : "=&{s94}"(abad)
                 : "{v[0:3]}"(vp[0]), "{v[4:7]}"(vp[1]), "{v[8:11]}"(vp[2]), "{v[12:15]}"(vp[3]), "{v[16:19]}"(vp[4]), "{v20}"(vl), "{s[56:59]}"(sa[4]), "{s60}"(sd[0]), "{s61}"(sd[1]), "{s62}"(tag),
                   "{s[8:11]}"(sa[0]), "{s[12:15]}"(sa[1]), "{s[16:19]}"(sa[2]), "{s[20:23]}"(sa[3])
                 : "memory", "m0", "vcc", "scc", ATT_VCLOB, ATT_SCLOB, ACLOB_LO, ACLOB_HI);
    {
        const u16* const __attribute__((address_space(4)))* ka = (const u16* const __attribute__((address_space(4)))*)__builtin_amdgcn_kernarg_segment_ptr();
        unsigned b2 = b, w2 = wid;
        asm volatile("s_waitcnt vmcnt(0)" : "+s"(ka), "+s"(b2), "+s"(w2), "+s"(ws) :: "memory");
        const unsigned j2 = b2 >> 5, p2 = w2 & 1u, h2 = (b2 & 7u) * 4u + ((b2 >> 3) & 3u);
#pragma unroll 1
        for (unsigned i = w2 & 2u; i < ((w2 & 2u) ? (abad ? 8u : 4u) : 2u); ++i)
            att_exact(ka[0], ka[2], ws, h2, (i < 4u ? 1920u - 128u * j2 : 128u * j2 - 128u) + 32u * i + 16u * p2, *reinterpret_cast<const float*>(lds + LDS_G + (32u * i + 16u * p2 + (lane_id() & 15u)) * 4u));
        asm volatile("s_waitcnt vmcnt(0)" ::: "memory");
        unsigned* fa = (unsigned*)(ws + WS_F) + F_R + (8u * j2 + (b2 & 7u)) * 256u + (2u + p2) * 64u + ((b2 >> 3) & 3u);
        if (w2 & 2u) st_flag(fa, tag);
        wait_flags(fa, ld_flag(fa), tag);
        const unsigned l2 = lane_id(); u32x4 c[3]; out_consts(c, ka[7], ka[8], ka[10], b2, w2, l2);
        asm volatile("s_waitcnt vmcnt(0) lgkmcnt(0)\n\ts_nop 7\n\ts_branch L_att_stub" :: "{v[0:3]}"(c[0]), "{v[4:7]}"(c[1]), "{v[8:11]}"(c[2]), "{v20}"(l2 * 16u), "{s[56:59]}"(SRDA), "{s60}"(SD(8u * j2, h2, w2)),
                     "{s61}"(SD(120u - 8u * j2, h2, w2)), "{s62}"(tag) : "memory");
    }
    RTS(5);
}

extern "C" void kernel_launch(void* const* d_in, const int* in_sizes, int n_in, void* d_out, int out_size, void* d_ws, size_t ws_size,
                              hipStream_t stream) {
    (void)hipFuncSetAttribute((const void*)fused_kernel, hipFuncAttributeMaxDynamicSharedMemorySize, att::LDS_BYTES);
    fused_kernel<<<dim3(256), 256, att::LDS_BYTES, stream>>>((const u16*)d_in[0], (const u16*)d_in[4], (const u16*)d_in[5], (const u16*)d_in[6], (const u16*)d_in[7], (const u16*)d_in[1],
                                                             (const u16*)d_in[9], (const u16*)d_in[2], (const u16*)d_in[8], (char*)d_ws, (u16*)d_out);
}
```

```cpp
#include <hip/hip_runtime.h>
#include <hip/hip_bf16.h>
#include <stdint.h>
#include <utility>

typedef short bf16x8 __attribute__((ext_vector_type(8)));
typedef short s16x4 __attribute__((ext_vector_type(4)));
typedef float f32x16 __attribute__((ext_vector_type(16)));
typedef float f32x4 __attribute__((ext_vector_type(4)));
typedef unsigned u32x4 __attribute__((ext_vector_type(4)));
typedef unsigned short u16;

constexpr int S = 2048, DM = 576, NH = 32, HD = 128, DC = 512, NQKV = 1728, DHA = 18;
constexpr int NBIG = NH * HD;
constexpr int KS_TOT = 146;

#define SBAR() __builtin_amdgcn_sched_barrier(0)
constexpr size_t WS_Q = 0, WS_K = WS_Q + (size_t)S * NBIG * 2, WS_V = WS_K + (size_t)S * NBIG * 2, WS_AF = WS_V + (size_t)S * NBIG * 2, WS_X = WS_AF + (size_t)128 * KS_TOT * 1024,
                 WS_P = WS_X + (size_t)64 * 16 * 9216, WS_F = WS_P + (size_t)128 * (4 * 32 * 256 + 1024) * 4;
constexpr size_t WS_L = (size_t)96 << 20;
static_assert(WS_F == 96206848, "workspace map is baked into the generated asm");
constexpr int F_EPOCH = 0, F_J = 1024, F_ST = 8192, F_KV = 40960, F_R = 65536;
constexpr unsigned TAG_STEP = 0x9E3779B1u;
__device__ __forceinline__ unsigned ld_flag(const unsigned* p) { return __hip_atomic_load(p, __ATOMIC_RELAXED, __HIP_MEMORY_SCOPE_AGENT); }
__device__ __forceinline__ void st_flag(unsigned* p, unsigned v) { __hip_atomic_store(p, v, __ATOMIC_RELAXED, __HIP_MEMORY_SCOPE_AGENT); }
__device__ __forceinline__ unsigned ld_flag_async(const unsigned* p) { unsigned v; asm volatile("global_load_dword %0, %1, off sc1" : "=v"(v) : "v"(p) : "memory"); return v; }
#define WAITV(v, N) asm volatile("s_waitcnt vmcnt(" #N ")" : "+v"(v) :: "memory")
__device__ __forceinline__ void wait_flags(const unsigned* p, unsigned v, unsigned tag) {
    if (__builtin_expect(__builtin_amdgcn_ballot_w64(v != tag) != 0ull, 0)) {
        unsigned spins = 0;
        do { __builtin_amdgcn_s_sleep(1); } while (__builtin_amdgcn_ballot_w64(ld_flag(p) != tag) != 0ull && ++spins < (1u << 22));
    }
    asm volatile("" ::: "memory");
}

__device__ __forceinline__ unsigned cvtpk(float lo, float hi) {
    unsigned r; asm volatile("v_cvt_pk_bf16_f32 %0, %1, %2" : "=v"(r) : "v"(lo), "v"(hi)); return r;
}
__device__ __forceinline__ float bf2f(u16 v) { return __uint_as_float(((unsigned)v) << 16); }
__device__ __forceinline__ int crow(int r, int hi) { return (r & 3) + 8 * (r >> 2) + 4 * hi; }

typedef __attribute__((address_space(3))) s16x4* lds_s16x4_p;
__device__ __forceinline__ s16x4 trrd(unsigned addr) { return __builtin_amdgcn_ds_read_tr16_b64_v4i16((lds_s16x4_p)addr); }

__device__ __forceinline__ unsigned lane_id() { unsigned l; asm volatile("v_mbcnt_lo_u32_b32 %0, -1, 0\n\tv_mbcnt_hi_u32_b32 %0, -1, %0" : "=v"(l)); return l; }
#define RTS(slot) do {} while (0)
#define CT_L 5u
__device__ __forceinline__ void code_touch(unsigned wi, unsigned lds_scratch) {
    unsigned long long pc; asm volatile("s_getpc_b64 %0" : "=s"(pc));
    asm volatile("s_mov_b32 m0, %0\n\ts_mov_b64 exec, %3\n\tglobal_load_lds_dword %1, %2\n\ts_mov_b64 exec, -1" :: "s"(lds_scratch), "v"(((threadIdx.x & 63u) * 128u + wi) * 128u), "s"(pc & ~127ull), "n"((1u << CT_L) - 1u) : "m0", "memory");
}
#define ACLOB_LO "a0","a1","a2","a3","a4","a5","a6","a7","a8","a9","a10","a11","a12","a13","a14","a15","a16","a17","a18","a19","a20","a21","a22","a23","a24","a25","a26","a27","a28","a29","a30","a31","a32","a33","a34","a35","a36","a37","a38","a39","a40","a41","a42","a43","a44","a45","a46","a47","a48","a49","a50","a51","a52","a53","a54","a55","a56","a57","a58","a59","a60","a61","a62","a63","a64","a65","a66","a67","a68","a69","a70","a71","a72","a73","a74","a75","a76","a77","a78","a79","a80","a81","a82","a83","a84","a85","a86","a87","a88","a89","a90","a91","a92","a93","a94","a95","a96","a97","a98","a99","a100","a101","a102","a103","a104","a105","a106","a107","a108","a109","a110","a111","a112","a113","a114","a115","a116","a117","a118","a119","a120","a121","a122","a123","a124","a125","a126","a127"
#define ACLOB_HI "a128","a129","a130","a131","a132","a133","a134","a135","a136","a137","a138","a139","a140","a141","a142","a143","a144","a145","a146","a147","a148","a149","a150","a151","a152","a153","a154","a155","a156","a157","a158","a159","a160","a161","a162","a163","a164","a165","a166","a167","a168","a169","a170","a171","a172","a173","a174","a175","a176","a177","a178","a179","a180","a181","a182","a183","a184","a185","a186","a187","a188","a189","a190","a191","a192","a193","a194","a195","a196","a197","a198","a199","a200","a201","a202","a203","a204","a205","a206","a207","a208","a209","a210","a211","a212","a213","a214","a215","a216","a217","a218","a219","a220","a221","a222","a223","a224","a225","a226","a227","a228","a229","a230","a231","a232","a233","a234","a235","a236","a237","a238","a239","a240","a241","a242","a243","a244","a245","a246","a247","a248","a249","a250","a251","a252","a253","a254","a255"
#define PROJ_PRE0 \
"s_add_u32 s62, %[rc], 0x0\n" \
"s_and_b32 s62, s62, 0x3ff\n" \
"s_lshl_b32 s63, s62, 12\n" \
"s_add_u32 m0, %[mA], 0x0\n" \
"s_add_u32 s85, s62, 0x0\n" \
"buffer_load_dwordx4 v22, s[64:67], s85 offen lds\n" \
"s_add_u32 m0, %[mA], 0x400\n" \
"s_add_u32 s84, s62, 0x2000\n" \
"buffer_load_dwordx4 v23, s[64:67], s84 offen lds\n" \
"s_add_u32 m0, %[mB], 0x0\n" \
"s_add_u32 s85, s63, 0x0\n" \
"buffer_load_dwordx4 v26, s[68:71], s85 offen lds\n" \
"s_add_u32 m0, %[mB], 0x4000\n" \
"s_add_u32 s84, s63, 0x0\n" \
"buffer_load_dwordx4 v26, s[72:75], s84 offen lds\n" \
"s_add_u32 m0, %[mA], 0x800\n" \
"s_add_u32 s85, s62, 0x4000\n" \
"buffer_load_dwordx4 v22, s[64:67], s85 offen lds\n" \
"s_add_u32 m0, %[mA], 0xc00\n" \
"s_add_u32 s84, s62, 0x6000\n" \
"buffer_load_dwordx4 v23, s[64:67], s84 offen lds\n" \
"s_add_u32 m0, %[mB], 0x400\n" \
"s_add_u32 s85, s63, 0x8000\n" \
"buffer_load_dwordx4 v26, s[68:71], s85 offen lds\n" \
"s_add_u32 m0, %[mB], 0x4400\n" \
"s_add_u32 s84, s63, 0x8000\n" \
"buffer_load_dwordx4 v26, s[72:75], s84 offen lds\n"
#define PROJ_PRE1 \
"s_add_u32 s62, %[rc], 0x0\n" \
"s_and_b32 s62, s62, 0x3ff\n" \
"s_lshl_b32 s63, s62, 12\n" \
"s_add_u32 m0, %[mA], 0x1000\n" \
"s_add_u32 s85, s62, 0x8000\n" \
"buffer_load_dwordx4 v22, s[64:67], s85 offen lds\n" \
"s_add_u32 m0, %[mA], 0x1400\n" \
"s_add_u32 s84, s62, 0xa000\n" \
"buffer_load_dwordx4 v23, s[64:67], s84 offen lds\n" \
"s_add_u32 m0, %[mB], 0x800\n" \
"s_add_u32 s85, s63, 0x10000\n" \
"buffer_load_dwordx4 v27, s[68:71], s85 offen lds\n" \
"s_add_u32 m0, %[mB], 0x4800\n" \
"s_add_u32 s84, s63, 0x10000\n" \
"buffer_load_dwordx4 v27, s[72:75], s84 offen lds\n" \
"s_add_u32 m0, %[mA], 0x1800\n" \
"s_add_u32 s85, s62, 0xc000\n" \
"buffer_load_dwordx4 v22, s[64:67], s85 offen lds\n" \
"s_add_u32 m0, %[mA], 0x1c00\n" \
"s_add_u32 s84, s62, 0xe000\n" \
"buffer_load_dwordx4 v23, s[64:67], s84 offen lds\n" \
"s_add_u32 m0, %[mB], 0xc00\n" \
"s_add_u32 s85, s63, 0x18000\n" \
"buffer_load_dwordx4 v27, s[68:71], s85 offen lds\n" \
"s_add_u32 m0, %[mB], 0x4c00\n" \
"s_add_u32 s84, s63, 0x18000\n" \
"buffer_load_dwordx4 v27, s[72:75], s84 offen lds\n"
#define PROJ_PRE2 \
"s_add_u32 s62, %[rc], 0x80\n" \
"s_and_b32 s62, s62, 0x3ff\n" \
"s_lshl_b32 s63, s62, 12\n" \
"s_add_u32 m0, %[mA], 0x8000\n" \
"s_add_u32 s85, s62, 0x0\n" \
"buffer_load_dwordx4 v22, s[64:67], s85 offen lds\n" \
"s_add_u32 m0, %[mA], 0x8400\n" \
"s_add_u32 s84, s62, 0x2000\n" \
"buffer_load_dwordx4 v23, s[64:67], s84 offen lds\n"
#define PROJ_PRE3 \
"s_add_u32 s62, %[rc], 0x80\n" \
"s_and_b32 s62, s62, 0x3ff\n" \
"s_lshl_b32 s63, s62, 12\n" \
"s_add_u32 m0, %[mB], 0x8000\n" \
"s_add_u32 s85, s63, 0x0\n" \
"buffer_load_dwordx4 v26, s[68:71], s85 offen lds\n" \
"s_add_u32 m0, %[mB], 0xc000\n" \
"s_add_u32 s84, s63, 0x0\n" \
"buffer_load_dwordx4 v26, s[72:75], s84 offen lds\n" \
"s_add_u32 m0, %[mA], 0x8800\n" \
"s_add_u32 s85, s62, 0x4000\n" \
"buffer_load_dwordx4 v22, s[64:67], s85 offen lds\n" \
"s_add_u32 m0, %[mA], 0x8c00\n" \
"s_add_u32 s84, s62, 0x6000\n" \
"buffer_load_dwordx4 v23, s[64:67], s84 offen lds\n" \
"s_add_u32 m0, %[mB], 0x8400\n" \
"s_add_u32 s85, s63, 0x8000\n" \
"buffer_load_dwordx4 v26, s[68:71], s85 offen lds\n" \
"s_add_u32 m0, %[mB], 0xc400\n" \
"s_add_u32 s84, s63, 0x8000\n" \
"buffer_load_dwordx4 v26, s[72:75], s84 offen lds\n" \
"s_add_u32 m0, %[mA], 0x9000\n" \
"s_add_u32 s85, s62, 0x8000\n" \
"buffer_load_dwordx4 v22, s[64:67], s85 offen lds\n" \
"s_add_u32 m0, %[mA], 0x9400\n" \
"s_add_u32 s84, s62, 0xa000\n" \
"buffer_load_dwordx4 v23, s[64:67], s84 offen lds\n"
#define PROJ_TKE 0
#define PROJ_T0 0
#define PROJ_ROT 2*(sp+mt)
#define PROJ_TC 0
#define PROJ_ASM \
".p2align 3\n" \
"s_branch L_go_%=\n" \
"L_pl_%=:\n" \
"s_mov_b32 s63, 0\n" \
"L_ql_%=:\n" \
"s_sleep 1\n" \
"v_bfe_u32 v44, v20, 2, 5\n" \
"buffer_load_dword v44, v44, s[80:83], s39 offen offset:64 sc1\n" \
"s_waitcnt vmcnt(0)\n" \
"v_cmp_ne_u32_e32 vcc, %[tag], v44\n" \
"s_add_u32 s63, s63, 1\n" \
"s_nop 3\n" \
"s_cbranch_vccz L_rl_%=\n" \
"s_cmp_gt_u32 s63, 0x400000\n" \
"s_cbranch_scc0 L_ql_%=\n" \
"s_branch L_rl_%=\n" \
"L_pa_%=:\n" \
"s_mov_b32 s63, 0\n" \
"L_qa_%=:\n" \
"s_sleep 1\n" \
"v_bfe_u32 v44, v20, 2, 5\n" \
"buffer_load_dword v44, v44, s[80:83], s39 offen offset:0 sc1\n" \
"s_waitcnt vmcnt(0)\n" \
"v_cmp_ne_u32_e32 vcc, %[tag], v44\n" \
"s_add_u32 s63, s63, 1\n" \
"s_nop 3\n" \
"s_cbranch_vccz L_ra_%=\n" \
"s_cmp_gt_u32 s63, 0x400000\n" \
"s_cbranch_scc0 L_qa_%=\n" \
"s_branch L_ra_%=\n" \
"L_go_%=:\n" \
"v_xor_b32_e32 v33, 0x20, v32\n" \
"s_mov_b32 s80, %[wsl]\n" \
"s_and_b32 s81, %[wsh], 0xffff\n" \
"s_mov_b32 s82, 0x10000000\n" \
"s_mov_b32 s83, 0x20000\n" \
"s_mov_b32 s84, 0x5bc0000\n" \
"s_add_u32 s62, s22, 0x80\n" \
"buffer_load_dword v180, off, s[80:83], s84 sc1\n" \
"s_and_b32 s62, s62, 0x3ff\n" \
"s_lshl_b32 s63, s62, 12\n" \
"v_xor_b32_e64 v34, 0x40, v32\n" \
"v_xor_b32_e32 v35, 0x60, v32\n" \
"s_waitcnt vmcnt(11)\n" \
"s_barrier\n" \
"ds_read_b64_tr_b16 v[120:121], v32 offset:8192\n" \
"ds_read_b64_tr_b16 v[122:123], v32 offset:9216\n" \
"ds_read_b128 v[56:59], v29 offset:16384\n" \
"ds_read_b64_tr_b16 v[116:117], v32 offset:0\n" \
"ds_read_b64_tr_b16 v[118:119], v32 offset:1024\n" \
"ds_read_b128 v[52:55], v28 offset:16384\n" \
"s_add_u32 m0, %[mB], 0x8800\n" \
"s_add_u32 s85, s63, 0x10000\n" \
"s_mov_b32 s76, s12\n" \
"ds_read_b128 v[60:63], v28 offset:18432\n" \
"s_and_b32 s77, s13, 0xffff\n" \
"s_mov_b32 s78, 0x1e6000\n" \
"s_mov_b32 s79, 0x20000\n" \
"ds_read_b128 v[64:67], v29 offset:18432\n" \
"s_waitcnt lgkmcnt(5)\n" \
"v_mfma_f32_16x16x32_bf16 a[0:3], v[120:123], v[56:59], 0\n" \
"ds_read_b128 v[72:75], v29 offset:20480\n" \
"ds_read_b128 v[68:71], v28 offset:20480\n" \
"ds_read_b128 v[76:79], v28 offset:22528\n" \
"buffer_load_dwordx4 v27, s[68:71], s85 offen lds\n" \
"ds_read_b128 v[80:83], v29 offset:22528\n" \
"s_waitcnt lgkmcnt(6)\n" \
"v_mfma_f32_16x16x32_bf16 a[0:3], v[116:119], v[52:55], a[0:3]\n" \
"ds_read_b64_tr_b16 v[128:129], v33 offset:8192\n" \
"s_waitcnt lgkmcnt(6)\n" \
"v_mfma_f32_16x16x32_bf16 a[32:35], v[116:119], v[60:63], 0\n" \
"ds_read_b64_tr_b16 v[130:131], v33 offset:9216\n" \
"ds_read_b64_tr_b16 v[124:125], v33 offset:0\n" \
"ds_read_b64_tr_b16 v[126:127], v33 offset:1024\n" \
"s_waitcnt lgkmcnt(8)\n" \
"s_nop 0\n" \
"v_mfma_f32_16x16x32_bf16 a[32:35], v[120:123], v[64:67], a[32:35]\n" \
"ds_read_b64_tr_b16 v[136:137], v34 offset:8192\n" \
"ds_read_b64_tr_b16 v[138:139], v34 offset:9216\n" \
"ds_read_b64_tr_b16 v[132:133], v34 offset:0\n" \
"s_add_u32 m0, %[mB], 0xc800\n" \
"ds_read_b64_tr_b16 v[134:135], v34 offset:1024\n" \
"s_add_u32 s84, s63, 0x10000\n" \
"buffer_load_dwordx4 v27, s[72:75], s84 offen lds\n" \
"ds_read_b64_tr_b16 v[144:145], v35 offset:8192\n" \
"ds_read_b64_tr_b16 v[146:147], v35 offset:9216\n" \
"ds_read_b64_tr_b16 v[140:141], v35 offset:0\n" \
"s_lshr_b32 s95, %[b], 3\n" \
"ds_read_b64_tr_b16 v[142:143], v35 offset:1024\n" \
"s_lshl_b32 s96, s95, 2\n" \
"ds_read_b64_tr_b16 v[152:153], v32 offset:24576\n" \
"s_add_u32 s97, s96, %[w]\n" \
"ds_read_b64_tr_b16 v[154:155], v32 offset:25600\n" \
"v_lshrrev_b32_e32 v212, 4, v20\n" \
"s_mul_i32 s98, 27, s97\n" \
"v_min_u32_e32 v213, 26, v212\n" \
"v_add_u32_e64 v214, s98, v213\n" \
"v_mul_u32_u24_e32 v215, 0xaaab, v214\n" \
"s_waitcnt lgkmcnt(15)\n" \
"v_mfma_f32_16x16x32_bf16 a[64:67], v[120:123], v[72:75], 0\n" \
"ds_read_b64_tr_b16 v[148:149], v32 offset:16384\n" \
"ds_read_b64_tr_b16 v[150:151], v32 offset:17408\n" \
"v_mfma_f32_16x16x32_bf16 a[64:67], v[116:119], v[68:71], a[64:67]\n" \
"s_add_u32 m0, %[mA], 0x9800\n" \
"s_add_u32 s85, s62, 0xc000\n" \
"v_mfma_f32_16x16x32_bf16 a[96:99], v[116:119], v[76:79], 0\n" \
"ds_read_b64_tr_b16 v[160:161], v33 offset:24576\n" \
"s_waitcnt lgkmcnt(15)\n" \
"v_mfma_f32_16x16x32_bf16 a[96:99], v[120:123], v[80:83], a[96:99]\n" \
"buffer_load_dwordx4 v22, s[64:67], s85 offen lds\n" \
"v_mfma_f32_16x16x32_bf16 a[4:7], v[128:131], v[56:59], 0\n" \
"s_add_u32 m0, %[mA], 0x9c00\n" \
"s_waitcnt lgkmcnt(13)\n" \
"s_nop 0\n" \
"v_mfma_f32_16x16x32_bf16 a[4:7], v[124:127], v[52:55], a[4:7]\n" \
"s_add_u32 s84, s62, 0xe000\n" \
"ds_read_b64_tr_b16 v[162:163], v33 offset:25600\n" \
"v_mfma_f32_16x16x32_bf16 a[36:39], v[124:127], v[60:63], 0\n" \
"buffer_load_dwordx4 v23, s[64:67], s84 offen lds\n" \
"v_mfma_f32_16x16x32_bf16 a[36:39], v[128:131], v[64:67], a[36:39]\n" \
"s_add_u32 m0, %[mB], 0x8c00\n" \
"s_add_u32 s85, s63, 0x18000\n" \
"v_mfma_f32_16x16x32_bf16 a[68:71], v[128:131], v[72:75], 0\n" \
"ds_read_b64_tr_b16 v[156:157], v33 offset:16384\n" \
"ds_read_b64_tr_b16 v[158:159], v33 offset:17408\n" \
"v_mfma_f32_16x16x32_bf16 a[68:71], v[124:127], v[68:71], a[68:71]\n" \
"buffer_load_dwordx4 v27, s[68:71], s85 offen lds\n" \
"v_mfma_f32_16x16x32_bf16 a[100:103], v[124:127], v[76:79], 0\n" \
"s_add_u32 m0, %[mB], 0xcc00\n" \
"s_add_u32 s84, s63, 0x18000\n" \
"v_mfma_f32_16x16x32_bf16 a[100:103], v[128:131], v[80:83], a[100:103]\n" \
"ds_read_b64_tr_b16 v[168:169], v34 offset:24576\n" \
"s_waitcnt lgkmcnt(7)\n" \
"s_nop 0\n" \
"v_mfma_f32_16x16x32_bf16 a[8:11], v[136:139], v[56:59], 0\n" \
"buffer_load_dwordx4 v27, s[72:75], s84 offen lds\n" \
"v_mfma_f32_16x16x32_bf16 a[8:11], v[132:135], v[52:55], a[8:11]\n" \
"ds_read_b64_tr_b16 v[170:171], v34 offset:25600\n" \
"ds_read_b64_tr_b16 v[164:165], v34 offset:16384\n" \
"v_mfma_f32_16x16x32_bf16 a[40:43], v[132:135], v[60:63], 0\n" \
"ds_read_b64_tr_b16 v[166:167], v34 offset:17408\n" \
"ds_read_b64_tr_b16 v[176:177], v35 offset:24576\n" \
"v_mfma_f32_16x16x32_bf16 a[40:43], v[136:139], v[64:67], a[40:43]\n" \
"ds_read_b64_tr_b16 v[178:179], v35 offset:25600\n" \
"ds_read_b64_tr_b16 v[172:173], v35 offset:16384\n" \
"v_mfma_f32_16x16x32_bf16 a[72:75], v[136:139], v[72:75], 0\n" \
"ds_read_b64_tr_b16 v[174:175], v35 offset:17408\n" \
"ds_read_b128 v[88:91], v29 offset:0\n" \
"v_mfma_f32_16x16x32_bf16 a[72:75], v[132:135], v[68:71], a[72:75]\n" \
"ds_read_b128 v[84:87], v28 offset:0\n" \
"ds_read_b128 v[92:95], v28 offset:2048\n" \
"v_mfma_f32_16x16x32_bf16 a[104:107], v[132:135], v[76:79], 0\n" \
"ds_read_b128 v[96:99], v29 offset:2048\n" \
"v_lshrrev_b32_e32 v216, 18, v215\n" \
"v_mfma_f32_16x16x32_bf16 a[104:107], v[136:139], v[80:83], a[104:107]\n" \
"ds_read_b128 v[104:107], v29 offset:4096\n" \
"v_mul_u32_u24_e32 v218, 6, v216\n" \
"v_mfma_f32_16x16x32_bf16 a[12:15], v[144:147], v[56:59], 0\n" \
"ds_read_b128 v[100:103], v28 offset:4096\n" \
"v_sub_u32_e32 v219, v214, v218\n" \
"v_mfma_f32_16x16x32_bf16 a[12:15], v[140:143], v[52:55], a[12:15]\n" \
"ds_read_b128 v[108:111], v28 offset:6144\n" \
"v_lshrrev_b32_e32 v220, 1, v219\n" \
"v_mfma_f32_16x16x32_bf16 a[44:47], v[140:143], v[60:63], 0\n" \
"ds_read_b128 v[112:115], v29 offset:6144\n" \
"v_mul_u32_u24_e32 v217, 27, v216\n" \
"v_mfma_f32_16x16x32_bf16 a[44:47], v[144:147], v[64:67], a[44:47]\n" \
"v_mul_u32_u24_e32 v221, 9, v220\n" \
"v_add_u32_e64 v222, v217, v221\n" \
"v_mfma_f32_16x16x32_bf16 a[76:79], v[144:147], v[72:75], 0\n" \
"v_and_b32_e32 v223, 1, v219\n" \
"s_and_b32 s94, 7, %[b]\n" \
"v_mfma_f32_16x16x32_bf16 a[76:79], v[140:143], v[68:71], a[76:79]\n" \
"v_add_u32_e32 v224, v222, v223\n" \
"v_add_u32_e32 v225, s94, v224\n" \
"v_mfma_f32_16x16x32_bf16 a[108:111], v[140:143], v[76:79], 0\n" \
"v_lshlrev_b32_e32 v186, 7, v225\n" \
"s_mul_i32 s89, 6, s97\n" \
"v_mfma_f32_16x16x32_bf16 a[108:111], v[144:147], v[80:83], a[108:111]\n" \
"s_add_u32 s95, 0xffffffdc, s89\n" \
"s_mul_i32 s99, 0x1200, s94\n" \
"v_mfma_f32_16x16x32_bf16 a[16:19], v[56:59], v[152:155], 0\n" \
"s_mul_i32 s100, 36, s97\n" \
"s_waitcnt lgkmcnt(11)\n" \
"v_mfma_f32_16x16x32_bf16 a[16:19], v[52:55], v[148:151], a[16:19]\n" \
"v_add_u32_e32 v227, s95, v212\n" \
"s_add_u32 s101, s99, s100\n" \
"v_mfma_f32_16x16x32_bf16 a[48:51], v[60:63], v[148:151], 0\n" \
"s_mul_i32 s88, 0x288, s94\n" \
"v_min_u32_e32 v228, 0x287, v227\n" \
"v_mfma_f32_16x16x32_bf16 a[48:51], v[64:67], v[152:155], a[48:51]\n" \
"v_add_u32_e32 v226, s101, v212\n" \
"v_add_u32_e32 v229, s88, v228\n" \
"v_mfma_f32_16x16x32_bf16 a[80:83], v[72:75], v[152:155], 0\n" \
"v_cmp_gt_u32_e32 vcc, 36, v212\n" \
"s_lshl_b32 s96, %[w], 6\n" \
"v_mfma_f32_16x16x32_bf16 a[80:83], v[68:71], v[148:151], a[80:83]\n" \
"v_cndmask_b32_e32 v230, v229, v226, vcc\n" \
"v_lshlrev_b32_e32 v16, 7, v230\n" \
"v_mfma_f32_16x16x32_bf16 a[112:115], v[76:79], v[148:151], 0\n" \
"v_lshrrev_b32_e32 v212, 4, v20\n" \
"v_add_u32_e32 v213, s96, v212\n" \
"v_mfma_f32_16x16x32_bf16 a[112:115], v[80:83], v[152:155], a[112:115]\n" \
"v_mul_u32_u24_e32 v214, 0x2aab, v213\n" \
"v_lshrrev_b32_e32 v215, 16, v214\n" \
"v_mfma_f32_16x16x32_bf16 a[20:23], v[56:59], v[160:163], 0\n" \
"s_lshr_b32 s94, %[b], 5\n" \
"v_mul_u32_u24_e32 v218, 6, v215\n" \
"v_mfma_f32_16x16x32_bf16 a[20:23], v[52:55], v[156:159], a[20:23]\n" \
"s_mul_i32 s95, 0x48, s94\n" \
"v_sub_u32_e32 v219, v213, v218\n" \
"v_mfma_f32_16x16x32_bf16 a[52:55], v[60:63], v[156:159], 0\n" \
"s_lshl_b32 s41, s97, 7\n" \
"v_add_u32_e32 v216, s95, v215\n" \
"v_mfma_f32_16x16x32_bf16 a[52:55], v[64:67], v[160:163], a[52:55]\n" \
"v_lshrrev_b32_e32 v220, 1, v219\n" \
"s_and_b32 s100, 7, %[b]\n" \
"v_mfma_f32_16x16x32_bf16 a[84:87], v[72:75], v[160:163], 0\n" \
"s_lshr_b32 s88, %[b], 3\n" \
"v_mul_u32_u24_e32 v217, 0xd80, v216\n" \
"v_mfma_f32_16x16x32_bf16 a[84:87], v[68:71], v[156:159], a[84:87]\n" \
"v_mul_u32_u24_e32 v221, 0x480, v220\n" \
"v_and_b32_e32 v223, 1, v219\n" \
"v_mfma_f32_16x16x32_bf16 a[116:119], v[76:79], v[156:159], 0\n" \
"s_add_u32 s97, 0x100, s96\n" \
"s_lshl_b32 s101, s100, 2\n" \
"v_mfma_f32_16x16x32_bf16 a[116:119], v[80:83], v[160:163], a[116:119]\n" \
"s_and_b32 s89, 3, s88\n" \
"v_add_u32_e64 v222, v217, v221\n" \
"v_mfma_f32_16x16x32_bf16 a[24:27], v[56:59], v[168:171], 0\n" \
"v_lshlrev_b32_e32 v224, 4, v223\n" \
"s_add_u32 s96, s101, s89\n" \
"v_mfma_f32_16x16x32_bf16 a[24:27], v[52:55], v[164:167], a[24:27]\n" \
"v_add_u32_e32 v192, v222, v224\n" \
"s_mul_i32 s56, 36, s96\n" \
"v_mfma_f32_16x16x32_bf16 a[56:59], v[60:63], v[164:167], 0\n" \
"v_add_u32_e64 v225, s97, v212\n" \
"v_mul_u32_u24_e32 v226, 0x2aab, v225\n" \
"v_mfma_f32_16x16x32_bf16 a[56:59], v[64:67], v[168:171], a[56:59]\n" \
"v_lshrrev_b32_e32 v227, 16, v226\n" \
"v_mul_u32_u24_e32 v230, 6, v227\n" \
"v_mfma_f32_16x16x32_bf16 a[88:91], v[72:75], v[168:171], 0\n" \
"v_sub_u32_e32 v231, v225, v230\n" \
"v_add_u32_e32 v228, s95, v227\n" \
"v_mfma_f32_16x16x32_bf16 a[88:91], v[68:71], v[164:167], a[88:91]\n" \
"v_lshrrev_b32_e32 v232, 1, v231\n" \
"v_mul_u32_u24_e32 v229, 0xd80, v228\n" \
"v_mfma_f32_16x16x32_bf16 a[120:123], v[76:79], v[164:167], 0\n" \
"v_mul_u32_u24_e32 v233, 0x480, v232\n" \
"v_and_b32_e32 v235, 1, v231\n" \
"v_mfma_f32_16x16x32_bf16 a[120:123], v[80:83], v[168:171], a[120:123]\n" \
"v_add_u32_e32 v234, v229, v233\n" \
"s_waitcnt lgkmcnt(0)\n" \
"v_mfma_f32_16x16x32_bf16 a[28:31], v[56:59], v[176:179], 0\n" \
"v_lshlrev_b32_e32 v236, 4, v235\n" \
"v_add_u32_e32 v237, v234, v236\n" \
"v_mfma_f32_16x16x32_bf16 a[28:31], v[52:55], v[172:175], a[28:31]\n" \
"v_cmp_gt_u32_e32 vcc, 0x1b0, v225\n" \
"s_mul_i32 s98, 18, %[w]\n" \
"v_mfma_f32_16x16x32_bf16 a[60:63], v[60:63], v[172:175], 0\n" \
"v_cndmask_b32_e32 v193, 0x40000000, v237, vcc\n" \
"s_add_u32 s99, s95, s98\n" \
"v_mfma_f32_16x16x32_bf16 a[60:63], v[64:67], v[176:179], a[60:63]\n" \
"v_add_u32_e32 v238, s99, v212\n" \
"v_mul_u32_u24_e32 v239, 0xd80, v238\n" \
"v_mfma_f32_16x16x32_bf16 a[92:95], v[72:75], v[176:179], 0\n" \
"v_cmp_gt_u32_e32 vcc, 18, v212\n" \
"v_lshlrev_b32_e32 v240, 1, v238\n" \
"v_mfma_f32_16x16x32_bf16 a[92:95], v[68:71], v[172:175], a[92:95]\n" \
"v_cndmask_b32_e32 v194, 0x40000000, v239, vcc\n" \
"v_cmp_gt_u32_e32 vcc, 18, v212\n" \
"v_mfma_f32_16x16x32_bf16 a[124:127], v[76:79], v[172:175], 0\n" \
"v_lshrrev_b32_e32 v230, 1, v238\n" \
"v_cndmask_b32_e32 v195, 0x40000000, v240, vcc\n" \
"v_mfma_f32_16x16x32_bf16 a[124:127], v[80:83], v[176:179], a[124:127]\n" \
"v_and_b32_e32 v227, 0xff8, v238\n" \
"v_and_b32_e64 v232, 2, v230\n" \
"v_mfma_f32_16x16x32_bf16 a[144:147], v[88:91], v[152:155], 0\n" \
"v_add_u32_e32 v229, v227, v232\n" \
"v_and_b32_e32 v233, 1, v238\n" \
"v_mfma_f32_16x16x32_bf16 a[144:147], v[84:87], v[148:151], a[144:147]\n" \
"v_add_u32_e32 v235, v229, v233\n" \
"v_lshrrev_b32_e32 v242, 1, v216\n" \
"v_mfma_f32_16x16x32_bf16 a[176:179], v[92:95], v[148:151], 0\n" \
"v_lshrrev_b32_e32 v244, 2, v216\n" \
"v_lshrrev_b32_e32 v214, 1, v228\n" \
"v_mfma_f32_16x16x32_bf16 a[176:179], v[96:99], v[152:155], a[176:179]\n" \
"v_lshrrev_b32_e32 v213, 2, v228\n" \
"v_lshrrev_b32_e32 v236, 1, v235\n" \
"v_mfma_f32_16x16x32_bf16 a[208:211], v[104:107], v[152:155], 0\n" \
"v_lshrrev_b32_e32 v239, 2, v235\n" \
"s_lshl_b32 s98, %[w], 10\n" \
"v_mfma_f32_16x16x32_bf16 a[208:211], v[100:103], v[148:151], a[208:211]\n" \
"s_mul_i32 s101, 0x12000, s96\n" \
"v_and_b32_e64 v243, 1, v242\n" \
"v_mfma_f32_16x16x32_bf16 a[240:243], v[108:111], v[148:151], 0\n" \
"v_and_b32_e32 v245, 2, v244\n" \
"v_and_b32_e32 v215, 1, v214\n" \
"v_mfma_f32_16x16x32_bf16 a[240:243], v[112:115], v[152:155], a[240:243]\n" \
"v_and_b32_e32 v218, 2, v213\n" \
"v_and_b32_e32 v237, 1, v236\n" \
"v_mfma_f32_16x16x32_bf16 a[148:151], v[88:91], v[160:163], 0\n" \
"v_and_b32_e64 v240, 2, v239\n" \
"s_add_u32 s99, 0x22000, s98\n" \
"v_mfma_f32_16x16x32_bf16 a[148:151], v[84:87], v[156:159], a[148:151]\n" \
"s_add_u32 s89, 0x6000000, s101\n" \
"s_mul_i32 s97, 0x2400, s94\n" \
"v_mfma_f32_16x16x32_bf16 a[180:183], v[92:95], v[156:159], 0\n" \
"v_lshlrev_b32_e32 v241, 7, v216\n" \
"v_or_b32_e32 v246, v243, v245\n" \
"v_mfma_f32_16x16x32_bf16 a[180:183], v[96:99], v[160:163], a[180:183]\n" \
"v_or_b32_e32 v220, v215, v218\n" \
"v_or_b32_e32 v242, v237, v240\n" \
"v_mfma_f32_16x16x32_bf16 a[212:215], v[104:107], v[160:163], 0\n" \
"v_add_u32_e32 v199, s99, v20\n" \
"s_add_u32 s37, s89, s98\n" \
"v_mfma_f32_16x16x32_bf16 a[212:215], v[100:103], v[156:159], a[212:215]\n" \
"v_lshlrev_b32_e32 v247, 1, v246\n" \
"v_lshlrev_b32_e32 v217, 1, v220\n" \
"v_mfma_f32_16x16x32_bf16 a[244:247], v[108:111], v[156:159], 0\n" \
"v_xor_b32_e32 v216, 3, v242\n" \
"s_add_u32 s57, s37, s97\n" \
"v_mfma_f32_16x16x32_bf16 a[244:247], v[112:115], v[160:163], a[244:247]\n" \
"s_lshl_b32 s99, s88, 2\n" \
"v_xor_b32_e32 v248, v219, v247\n" \
"v_mfma_f32_16x16x32_bf16 a[152:155], v[88:91], v[168:171], 0\n" \
"v_xor_b32_e32 v221, v231, v217\n" \
"v_lshlrev_b32_e32 v234, 7, v235\n" \
"v_mfma_f32_16x16x32_bf16 a[152:155], v[84:87], v[164:167], a[152:155]\n" \
"v_lshlrev_b32_e32 v244, 5, v216\n" \
"v_and_b32_e32 v245, 2, v238\n" \
"v_mfma_f32_16x16x32_bf16 a[184:187], v[92:95], v[164:167], 0\n" \
"s_add_u32 s94, 0x2000, s57\n" \
"s_add_u32 s96, s99, %[w]\n" \
"v_mfma_f32_16x16x32_bf16 a[184:187], v[96:99], v[168:171], a[184:187]\n" \
"s_lshl_b32 s89, %[b], 2\n" \
"s_sub_u32 s95, 0x22000, s97\n" \
"v_mfma_f32_16x16x32_bf16 a[216:219], v[104:107], v[168:171], 0\n" \
"v_lshlrev_b32_e32 v249, 4, v248\n" \
"v_lshlrev_b32_e32 v251, 7, v228\n" \
"v_mfma_f32_16x16x32_bf16 a[216:219], v[100:103], v[164:167], a[216:219]\n" \
"v_lshlrev_b32_e32 v223, 4, v221\n" \
"v_add_u32_e32 v243, v234, v244\n" \
"v_mfma_f32_16x16x32_bf16 a[248:251], v[108:111], v[164:167], 0\n" \
"v_lshlrev_b32_e32 v246, 1, v245\n" \
"s_mul_i32 s101, 36, s96\n" \
"v_mfma_f32_16x16x32_bf16 a[248:251], v[112:115], v[168:171], a[248:251]\n" \
"s_cmp_eq_u32 %[w], 0\n" \
"s_cselect_b32 s58, s94, 0x40000000\n" \
"v_mfma_f32_16x16x32_bf16 a[156:159], v[88:91], v[176:179], 0\n" \
"s_add_u32 s98, s89, %[w]\n" \
"v_add_u32_e32 v250, v241, v249\n" \
"v_mfma_f32_16x16x32_bf16 a[156:159], v[84:87], v[172:175], a[156:159]\n" \
"v_add_u32_e32 v222, v251, v223\n" \
"v_add_u32_e32 v219, v243, v246\n" \
"v_mfma_f32_16x16x32_bf16 a[188:191], v[92:95], v[172:175], 0\n" \
"v_add_u32_e32 v248, s101, v212\n" \
"s_mul_i32 s97, 18, s98\n" \
"v_mfma_f32_16x16x32_bf16 a[188:191], v[96:99], v[176:179], a[188:191]\n" \
"v_add_u32_e32 v196, s95, v250\n" \
"v_add_u32_e32 v224, s95, v222\n" \
"v_mfma_f32_16x16x32_bf16 a[220:223], v[104:107], v[176:179], 0\n" \
"v_add_u32_e32 v247, s95, v219\n" \
"v_lshrrev_b32_e32 v241, 3, v248\n" \
"v_mfma_f32_16x16x32_bf16 a[220:223], v[100:103], v[172:175], a[220:223]\n" \
"s_add_u32 s94, 0xffffffdc, s97\n" \
"v_min_u32_e32 v213, 53, v212\n" \
"v_mfma_f32_16x16x32_bf16 a[252:255], v[108:111], v[172:175], 0\n" \
"s_lshl_b32 s95, s100, 3\n" \
"v_lshlrev_b32_e32 v249, 6, v241\n" \
"v_mfma_f32_16x16x32_bf16 a[252:255], v[112:115], v[176:179], a[252:255]\n" \
"v_add_u32_e32 v215, s94, v213\n" \
"s_and_b32 s97, 7, %[b]\n" \
"v_mfma_f32_16x16x32_bf16 a[128:131], v[120:123], v[88:91], 0\n" \
"s_lshr_b32 s99, %[b], 3\n" \
"v_add_u32_e64 v250, s95, v249\n" \
"v_mfma_f32_16x16x32_bf16 a[128:131], v[116:119], v[84:87], a[128:131]\n" \
"s_lshr_b32 s94, %[b], 5\n" \
"s_lshl_b32 s98, s97, 2\n" \
"v_mfma_f32_16x16x32_bf16 a[160:163], v[116:119], v[92:95], 0\n" \
"s_and_b32 s100, 3, s99\n" \
"s_mul_i32 s95, 0xa00, s94\n" \
"v_mfma_f32_16x16x32_bf16 a[160:163], v[120:123], v[96:99], a[160:163]\n" \
"s_mov_b32 m0, %[mS]\n" \
"s_mov_b32 s86, s12\n" \
"v_mfma_f32_16x16x32_bf16 a[192:195], v[120:123], v[104:107], 0\n" \
"s_mov_b32 s87, s13\n" \
"s_add_u32 s101, s98, s100\n" \
"v_mfma_f32_16x16x32_bf16 a[192:195], v[116:119], v[100:103], a[192:195]\n" \
"s_add_u32 s96, 0x5bc1000, s95\n" \
"s_lshl_b32 s88, s101, 2\n" \
"v_mfma_f32_16x16x32_bf16 a[224:227], v[116:119], v[108:111], 0\n" \
"s_add_u32 s89, s96, s88\n" \
"s_lshl_b32 s99, s94, 3\n" \
"v_mfma_f32_16x16x32_bf16 a[224:227], v[120:123], v[112:115], a[224:227]\n" \
"s_cmp_eq_u32 %[w], 0\n" \
"s_cselect_b32 s95, s89, 0x40000000\n" \
"v_mfma_f32_16x16x32_bf16 a[132:135], v[128:131], v[88:91], 0\n" \
"s_add_u32 s62, s22, 0x100\n" \
"s_and_b32 s62, s62, 0x3ff\n" \
"v_mfma_f32_16x16x32_bf16 a[132:135], v[124:127], v[84:87], a[132:135]\n" \
"s_lshl_b32 s63, s62, 12\n" \
"s_add_u32 s88, s99, s97\n" \
"s_mov_b32 exec_lo, 0x7ffffff\n" \
"s_mov_b32 exec_hi, 0x0\n" \
"global_load_lds_dword v186, s[86:87]\n" \
"s_mov_b64 exec, -1\n" \
"v_mfma_f32_16x16x32_bf16 a[164:167], v[124:127], v[92:95], 0\n" \
"v_writelane_b32 v11, s95, 0\n" \
"s_lshl_b32 s89, s88, 10\n" \
"v_mfma_f32_16x16x32_bf16 a[164:167], v[128:131], v[96:99], a[164:167]\n" \
"s_add_u32 s95, 0x5c00000, s89\n" \
"s_lshl_b32 s99, %[w], 8\n" \
"v_mfma_f32_16x16x32_bf16 a[196:199], v[128:131], v[104:107], 0\n" \
"s_add_u32 s88, s95, s99\n" \
"v_add_u32_e32 v226, 0x24400, v20\n" \
"v_mfma_f32_16x16x32_bf16 a[196:199], v[124:127], v[100:103], a[196:199]\n" \
"v_cmp_gt_u32_e32 vcc, 0x1b0, v225\n" \
"s_or_b32 s89, 0x200, s88\n" \
"v_mfma_f32_16x16x32_bf16 a[228:231], v[124:127], v[108:111], 0\n" \
"s_lshl_b32 s95, s100, 2\n" \
"v_cndmask_b32_e32 v197, v226, v224, vcc\n" \
"v_mfma_f32_16x16x32_bf16 a[228:231], v[128:131], v[112:115], a[228:231]\n" \
"v_writelane_b32 v11, s89, 1\n" \
"s_add_u32 s99, s88, s95\n" \
"s_waitcnt vmcnt(1)\n" \
"s_barrier\n" \
"v_mfma_f32_16x16x32_bf16 a[136:139], v[136:139], v[88:91], 0\n" \
"ds_read_b64_tr_b16 v[152:153], v32 offset:57344\n" \
"ds_read_b64_tr_b16 v[154:155], v32 offset:58368\n" \
"v_mfma_f32_16x16x32_bf16 a[136:139], v[132:135], v[84:87], a[136:139]\n" \
"ds_read_b128 v[56:59], v29 offset:49152\n" \
"s_add_u32 m0, %[mA], 0x0\n" \
"v_mfma_f32_16x16x32_bf16 a[168:171], v[132:135], v[92:95], 0\n" \
"s_add_u32 s85, s62, 0x0\n" \
"ds_read_b64_tr_b16 v[148:149], v32 offset:49152\n" \
"v_mfma_f32_16x16x32_bf16 a[168:171], v[136:139], v[96:99], a[168:171]\n" \
"buffer_load_dwordx4 v22, s[64:67], s85 offen lds\n" \
"ds_read_b64_tr_b16 v[150:151], v32 offset:50176\n" \
"ds_read_b128 v[52:55], v28 offset:49152\n" \
"v_mfma_f32_16x16x32_bf16 a[200:203], v[136:139], v[104:107], 0\n" \
"ds_read_b128 v[60:63], v28 offset:51200\n" \
"ds_read_b128 v[64:67], v29 offset:51200\n" \
"v_mfma_f32_16x16x32_bf16 a[200:203], v[132:135], v[100:103], a[200:203]\n" \
"L_pf0:\n" \
"s_cbranch_execz L_pf4\n" \
"s_nop 0\n" \
"s_add_u32 m0, %[mA], 0x400\n" \
"s_add_u32 s84, s62, 0x2000\n" \
"v_mfma_f32_16x16x32_bf16 a[232:235], v[132:135], v[108:111], 0\n" \
"buffer_load_dwordx4 v23, s[64:67], s84 offen lds\n" \
"ds_read_b128 v[72:75], v29 offset:53248\n" \
"v_mfma_f32_16x16x32_bf16 a[232:235], v[136:139], v[112:115], a[232:235]\n" \
"ds_read_b128 v[68:71], v28 offset:53248\n" \
"ds_read_b128 v[76:79], v28 offset:55296\n" \
"v_mfma_f32_16x16x32_bf16 a[140:143], v[144:147], v[88:91], 0\n" \
"ds_read_b128 v[80:83], v29 offset:55296\n" \
"s_add_u32 m0, %[mB], 0x0\n" \
"v_mfma_f32_16x16x32_bf16 a[140:143], v[140:143], v[84:87], a[140:143]\n" \
"s_add_u32 s85, s63, 0x0\n" \
"ds_read_b64_tr_b16 v[160:161], v33 offset:57344\n" \
"v_mfma_f32_16x16x32_bf16 a[172:175], v[140:143], v[92:95], 0\n" \
"buffer_load_dwordx4 v26, s[68:71], s85 offen lds\n" \
"ds_read_b64_tr_b16 v[162:163], v33 offset:58368\n" \
"v_mfma_f32_16x16x32_bf16 a[172:175], v[144:147], v[96:99], a[172:175]\n" \
"ds_read_b64_tr_b16 v[156:157], v33 offset:49152\n" \
"ds_read_b64_tr_b16 v[158:159], v33 offset:50176\n" \
"v_mfma_f32_16x16x32_bf16 a[204:207], v[144:147], v[104:107], 0\n" \
"s_add_u32 m0, %[mB], 0x4000\n" \
"s_add_u32 s84, s63, 0x0\n" \
"v_mfma_f32_16x16x32_bf16 a[204:207], v[140:143], v[100:103], a[204:207]\n" \
"buffer_load_dwordx4 v26, s[72:75], s84 offen lds\n" \
"v_mfma_f32_16x16x32_bf16 a[236:239], v[140:143], v[108:111], 0\n" \
"s_add_u32 m0, %[mA], 0x800\n" \
"s_add_u32 s85, s62, 0x4000\n" \
"v_mfma_f32_16x16x32_bf16 a[236:239], v[144:147], v[112:115], a[236:239]\n" \
"ds_read_b64_tr_b16 v[168:169], v34 offset:57344\n" \
"s_waitcnt lgkmcnt(13)\n" \
"v_mfma_f32_16x16x32_bf16 a[16:19], v[56:59], v[152:155], a[16:19]\n" \
"ds_read_b64_tr_b16 v[170:171], v34 offset:58368\n" \
"s_waitcnt lgkmcnt(12)\n" \
"v_mfma_f32_16x16x32_bf16 a[16:19], v[52:55], v[148:151], a[16:19]\n" \
"ds_read_b64_tr_b16 v[164:165], v34 offset:49152\n" \
"s_waitcnt lgkmcnt(12)\n" \
"v_mfma_f32_16x16x32_bf16 a[48:51], v[60:63], v[148:151], a[48:51]\n" \
"ds_read_b64_tr_b16 v[166:167], v34 offset:50176\n" \
"buffer_load_dwordx4 v22, s[64:67], s85 offen lds\n" \
"s_waitcnt lgkmcnt(12)\n" \
"v_mfma_f32_16x16x32_bf16 a[48:51], v[64:67], v[152:155], a[48:51]\n" \
"s_add_u32 m0, %[mA], 0xc00\n" \
"s_waitcnt lgkmcnt(11)\n" \
"v_mfma_f32_16x16x32_bf16 a[80:83], v[72:75], v[152:155], a[80:83]\n" \
"s_add_u32 s84, s62, 0x6000\n" \
"s_waitcnt lgkmcnt(10)\n" \
"v_mfma_f32_16x16x32_bf16 a[80:83], v[68:71], v[148:151], a[80:83]\n" \
"s_add_u32 s85, s63, 0x8000\n" \
"buffer_load_dwordx4 v23, s[64:67], s84 offen lds\n" \
"s_waitcnt lgkmcnt(9)\n" \
"v_mfma_f32_16x16x32_bf16 a[112:115], v[76:79], v[148:151], a[112:115]\n" \
"s_add_u32 m0, %[mB], 0x400\n" \
"s_waitcnt lgkmcnt(8)\n" \
"v_mfma_f32_16x16x32_bf16 a[112:115], v[80:83], v[152:155], a[112:115]\n" \
"ds_read_b64_tr_b16 v[176:177], v35 offset:57344\n" \
"s_waitcnt lgkmcnt(7)\n" \
"v_mfma_f32_16x16x32_bf16 a[20:23], v[56:59], v[160:163], a[20:23]\n" \
"ds_read_b64_tr_b16 v[178:179], v35 offset:58368\n" \
"s_waitcnt lgkmcnt(6)\n" \
"s_nop 0\n" \
"v_mfma_f32_16x16x32_bf16 a[20:23], v[52:55], v[156:159], a[20:23]\n" \
"buffer_load_dwordx4 v26, s[68:71], s85 offen lds\n" \
"v_mfma_f32_16x16x32_bf16 a[52:55], v[60:63], v[156:159], a[52:55]\n" \
"ds_read_b64_tr_b16 v[172:173], v35 offset:49152\n" \
"ds_read_b64_tr_b16 v[174:175], v35 offset:50176\n" \
"v_mfma_f32_16x16x32_bf16 a[52:55], v[64:67], v[160:163], a[52:55]\n" \
"s_add_u32 m0, %[mB], 0x4400\n" \
"s_add_u32 s84, s63, 0x8000\n" \
"v_mfma_f32_16x16x32_bf16 a[84:87], v[72:75], v[160:163], a[84:87]\n" \
"buffer_load_dwordx4 v26, s[72:75], s84 offen lds\n" \
"v_mfma_f32_16x16x32_bf16 a[84:87], v[68:71], v[156:159], a[84:87]\n" \
"s_add_u32 m0, %[mA], 0x1000\n" \
"s_add_u32 s85, s62, 0x8000\n" \
"v_mfma_f32_16x16x32_bf16 a[116:119], v[76:79], v[156:159], a[116:119]\n" \
"ds_read_b64_tr_b16 v[120:121], v32 offset:40960\n" \
"ds_read_b64_tr_b16 v[122:123], v32 offset:41984\n" \
"v_mfma_f32_16x16x32_bf16 a[116:119], v[80:83], v[160:163], a[116:119]\n" \
"ds_read_b64_tr_b16 v[116:117], v32 offset:32768\n" \
"s_waitcnt lgkmcnt(7)\n" \
"s_nop 0\n" \
"v_mfma_f32_16x16x32_bf16 a[24:27], v[56:59], v[168:171], a[24:27]\n" \
"buffer_load_dwordx4 v22, s[64:67], s85 offen lds\n" \
"v_mfma_f32_16x16x32_bf16 a[24:27], v[52:55], v[164:167], a[24:27]\n" \
"ds_read_b64_tr_b16 v[118:119], v32 offset:33792\n" \
"s_add_u32 m0, %[mA], 0x1400\n" \
"v_mfma_f32_16x16x32_bf16 a[56:59], v[60:63], v[164:167], a[56:59]\n" \
"s_add_u32 s84, s62, 0xa000\n" \
"ds_read_b64_tr_b16 v[128:129], v33 offset:40960\n" \
"v_mfma_f32_16x16x32_bf16 a[56:59], v[64:67], v[168:171], a[56:59]\n" \
"buffer_load_dwordx4 v23, s[64:67], s84 offen lds\n" \
"v_mfma_f32_16x16x32_bf16 a[88:91], v[72:75], v[168:171], a[88:91]\n" \
"s_add_u32 m0, %[mB], 0x800\n" \
"s_add_u32 s85, s63, 0x10000\n" \
"v_mfma_f32_16x16x32_bf16 a[88:91], v[68:71], v[164:167], a[88:91]\n" \
"ds_read_b64_tr_b16 v[130:131], v33 offset:41984\n" \
"ds_read_b64_tr_b16 v[124:125], v33 offset:32768\n" \
"v_mfma_f32_16x16x32_bf16 a[120:123], v[76:79], v[164:167], a[120:123]\n" \
"buffer_load_dwordx4 v27, s[68:71], s85 offen lds\n" \
"v_mfma_f32_16x16x32_bf16 a[120:123], v[80:83], v[168:171], a[120:123]\n" \
"ds_read_b64_tr_b16 v[126:127], v33 offset:33792\n" \
"s_waitcnt lgkmcnt(10)\n" \
"v_mfma_f32_16x16x32_bf16 a[28:31], v[56:59], v[176:179], a[28:31]\n" \
"s_add_u32 m0, %[mB], 0x4800\n" \
"s_waitcnt lgkmcnt(8)\n" \
"v_mfma_f32_16x16x32_bf16 a[28:31], v[52:55], v[172:175], a[28:31]\n" \
"s_add_u32 s84, s63, 0x10000\n" \
"ds_read_b64_tr_b16 v[136:137], v34 offset:40960\n" \
"v_mfma_f32_16x16x32_bf16 a[60:63], v[60:63], v[172:175], a[60:63]\n" \
"buffer_load_dwordx4 v27, s[72:75], s84 offen lds\n" \
"v_mfma_f32_16x16x32_bf16 a[60:63], v[64:67], v[176:179], a[60:63]\n" \
"ds_read_b64_tr_b16 v[138:139], v34 offset:41984\n" \
"ds_read_b64_tr_b16 v[132:133], v34 offset:32768\n" \
"v_mfma_f32_16x16x32_bf16 a[92:95], v[72:75], v[176:179], a[92:95]\n" \
"s_add_u32 m0, %[mA], 0x1800\n" \
"s_add_u32 s85, s62, 0xc000\n" \
"v_mfma_f32_16x16x32_bf16 a[92:95], v[68:71], v[172:175], a[92:95]\n" \
"buffer_load_dwordx4 v22, s[64:67], s85 offen lds\n" \
"v_mfma_f32_16x16x32_bf16 a[124:127], v[76:79], v[172:175], a[124:127]\n" \
"ds_read_b64_tr_b16 v[134:135], v34 offset:33792\n" \
"ds_read_b64_tr_b16 v[144:145], v35 offset:40960\n" \
"v_mfma_f32_16x16x32_bf16 a[124:127], v[80:83], v[176:179], a[124:127]\n" \
"s_add_u32 m0, %[mA], 0x1c00\n" \
"s_waitcnt lgkmcnt(9)\n" \
"s_nop 0\n" \
"v_mfma_f32_16x16x32_bf16 a[0:3], v[120:123], v[56:59], a[0:3]\n" \
"s_add_u32 s84, s62, 0xe000\n" \
"ds_read_b64_tr_b16 v[146:147], v35 offset:41984\n" \
"v_mfma_f32_16x16x32_bf16 a[0:3], v[116:119], v[52:55], a[0:3]\n" \
"buffer_load_dwordx4 v23, s[64:67], s84 offen lds\n" \
"v_mfma_f32_16x16x32_bf16 a[32:35], v[116:119], v[60:63], a[32:35]\n" \
"ds_read_b64_tr_b16 v[140:141], v35 offset:32768\n" \
"ds_read_b64_tr_b16 v[142:143], v35 offset:33792\n" \
"v_mfma_f32_16x16x32_bf16 a[32:35], v[120:123], v[64:67], a[32:35]\n" \
"s_add_u32 m0, %[mB], 0xc00\n" \
"s_add_u32 s85, s63, 0x18000\n" \
"v_mfma_f32_16x16x32_bf16 a[64:67], v[120:123], v[72:75], a[64:67]\n" \
"buffer_load_dwordx4 v27, s[68:71], s85 offen lds\n" \
"v_mfma_f32_16x16x32_bf16 a[64:67], v[116:119], v[68:71], a[64:67]\n" \
"ds_read_b128 v[88:91], v29 offset:32768\n" \
"ds_read_b128 v[84:87], v28 offset:32768\n" \
"v_mfma_f32_16x16x32_bf16 a[96:99], v[116:119], v[76:79], a[96:99]\n" \
"ds_read_b128 v[92:95], v28 offset:34816\n" \
"ds_read_b128 v[96:99], v29 offset:34816\n" \
"v_mfma_f32_16x16x32_bf16 a[96:99], v[120:123], v[80:83], a[96:99]\n" \
"s_add_u32 m0, %[mB], 0x4c00\n" \
"s_waitcnt lgkmcnt(11)\n" \
"v_mfma_f32_16x16x32_bf16 a[4:7], v[128:131], v[56:59], a[4:7]\n" \
"s_add_u32 s84, s63, 0x18000\n" \
"ds_read_b128 v[104:107], v29 offset:36864\n" \
"v_mfma_f32_16x16x32_bf16 a[4:7], v[124:127], v[52:55], a[4:7]\n" \
"buffer_load_dwordx4 v27, s[72:75], s84 offen lds\n" \
"v_mfma_f32_16x16x32_bf16 a[36:39], v[124:127], v[60:63], a[36:39]\n" \
"ds_read_b128 v[100:103], v28 offset:36864\n" \
"ds_read_b128 v[108:111], v28 offset:38912\n" \
"v_mfma_f32_16x16x32_bf16 a[36:39], v[128:131], v[64:67], a[36:39]\n" \
"s_mov_b32 m0, %[mS]\n" \
"s_mov_b32 s86, s18\n" \
"v_mfma_f32_16x16x32_bf16 a[68:71], v[128:131], v[72:75], a[68:71]\n" \
"s_mov_b32 s87, s19\n" \
"ds_read_b128 v[112:115], v29 offset:38912\n" \
"v_mfma_f32_16x16x32_bf16 a[68:71], v[124:127], v[68:71], a[68:71]\n" \
"v_cmp_gt_u32_e32 vcc, 18, v212\n" \
"v_writelane_b32 v11, s99, 2\n" \
"v_mfma_f32_16x16x32_bf16 a[100:103], v[124:127], v[76:79], a[100:103]\n" \
"s_add_u32 s89, 0x80, s88\n" \
"v_cndmask_b32_e32 v198, v226, v247, vcc\n" \
"v_mfma_f32_16x16x32_bf16 a[100:103], v[128:131], v[80:83], a[100:103]\n" \
"v_and_b32_e32 v214, 7, v248\n" \
"s_waitcnt lgkmcnt(10)\n" \
"v_mfma_f32_16x16x32_bf16 a[8:11], v[136:139], v[56:59], a[8:11]\n" \
"v_writelane_b32 v11, s89, 3\n" \
"s_lshl_b32 s88, s101, 8\n" \
"v_mfma_f32_16x16x32_bf16 a[8:11], v[132:135], v[52:55], a[8:11]\n" \
"v_add_u32_e32 v228, v250, v214\n" \
"v_cmp_gt_u32_e32 vcc, 36, v212\n" \
"s_mov_b32 exec_lo, 0x3ffff\n" \
"s_mov_b32 exec_hi, 0x0\n" \
"global_load_lds_dword v16, s[86:87]\n" \
"s_mov_b64 exec, -1\n" \
"v_mfma_f32_16x16x32_bf16 a[40:43], v[132:135], v[60:63], a[40:43]\n" \
"s_add_u32 s95, 0x80, s99\n" \
"s_add_u32 s39, 0x5be8000, s88\n" \
"v_mfma_f32_16x16x32_bf16 a[40:43], v[136:139], v[64:67], a[40:43]\n" \
"s_lshl_b32 s89, s94, 2\n" \
"v_cndmask_b32_e32 v218, v215, v228, vcc\n" \
"v_mfma_f32_16x16x32_bf16 a[72:75], v[136:139], v[72:75], a[72:75]\n" \
"s_add_u32 s99, s39, s89\n" \
"v_lshrrev_b32_e32 v212, 4, v20\n" \
"v_mfma_f32_16x16x32_bf16 a[72:75], v[132:135], v[68:71], a[72:75]\n" \
"v_writelane_b32 v11, s95, 4\n" \
"v_lshlrev_b32_e32 v213, 2, v212\n" \
"v_mfma_f32_16x16x32_bf16 a[104:107], v[132:135], v[76:79], a[104:107]\n" \
"s_cmp_eq_u32 %[w], 3\n" \
"s_cselect_b32 s38, s99, 0x40000000\n" \
"v_mfma_f32_16x16x32_bf16 a[104:107], v[136:139], v[80:83], a[104:107]\n" \
"v_and_b32_e32 v215, 7, v212\n" \
"s_add_u32 s101, s89, s100\n" \
"v_mfma_f32_16x16x32_bf16 a[12:15], v[144:147], v[56:59], a[12:15]\n" \
"v_add_u32_e32 v214, s96, v213\n" \
"s_waitcnt lgkmcnt(1)\n" \
"v_mfma_f32_16x16x32_bf16 a[12:15], v[140:143], v[52:55], a[12:15]\n" \
"s_add_u32 s95, 0x5be8020, s88\n" \
"v_lshlrev_b32_e32 v216, 2, v215\n" \
"v_mfma_f32_16x16x32_bf16 a[44:47], v[140:143], v[60:63], a[44:47]\n" \
"s_mul_i32 s94, 0x90000, s101\n" \
"v_add_u32_e32 v217, s95, v216\n" \
"v_mfma_f32_16x16x32_bf16 a[44:47], v[144:147], v[64:67], a[44:47]\n" \
"s_add_u32 s99, 0x4240000, s94\n" \
"s_lshl_b32 s96, %[w], 14\n" \
"v_mfma_f32_16x16x32_bf16 a[76:79], v[144:147], v[72:75], a[76:79]\n" \
"s_add_u32 s88, s99, s96\n" \
"s_lshl_b32 s95, s97, 16\n" \
"v_mfma_f32_16x16x32_bf16 a[76:79], v[140:143], v[68:71], a[76:79]\n" \
"v_writelane_b32 v11, s39, 52\n" \
"s_add_u32 s89, s88, s95\n" \
"v_mfma_f32_16x16x32_bf16 a[108:111], v[140:143], v[76:79], a[108:111]\n" \
"s_lshl_b32 s101, s97, 11\n" \
"v_writelane_b32 v11, s89, 37\n" \
"v_mfma_f32_16x16x32_bf16 a[108:111], v[144:147], v[80:83], a[108:111]\n" \
"s_add_u32 s100, 0x3800, s89\n" \
"s_add_u32 s94, s88, s101\n" \
"v_mfma_f32_16x16x32_bf16 a[128:131], v[120:123], v[88:91], a[128:131]\n" \
"v_writelane_b32 v11, s100, 51\n" \
"s_add_u32 s99, 0x80000, s94\n" \
"v_mfma_f32_16x16x32_bf16 a[128:131], v[116:119], v[84:87], a[128:131]\n" \
"s_lshl_b32 s96, %[w], 17\n" \
"v_writelane_b32 v11, s99, 49\n" \
"v_mfma_f32_16x16x32_bf16 a[160:163], v[116:119], v[92:95], a[160:163]\n" \
"s_add_u32 s95, s88, s96\n" \
"s_and_b32 s94, 1, s97\n" \
"v_mfma_f32_16x16x32_bf16 a[160:163], v[120:123], v[96:99], a[160:163]\n" \
"s_add_u32 s89, s95, s98\n" \
"s_lshl_b32 s99, s94, 10\n" \
"v_mfma_f32_16x16x32_bf16 a[192:195], v[120:123], v[104:107], a[192:195]\n" \
"s_add_u32 s100, 0x3840, s89\n" \
"s_add_u32 s96, s88, s99\n" \
"v_mfma_f32_16x16x32_bf16 a[192:195], v[116:119], v[100:103], a[192:195]\n" \
"v_writelane_b32 v11, s100, 5\n" \
"s_lshr_b32 s101, s97, 1\n" \
"v_mfma_f32_16x16x32_bf16 a[224:227], v[116:119], v[108:111], a[224:227]\n" \
"s_add_u32 s95, 0x80000, s96\n" \
"s_waitcnt lgkmcnt(0)\n" \
"v_mfma_f32_16x16x32_bf16 a[224:227], v[120:123], v[112:115], a[224:227]\n" \
"s_add_u32 s89, 1, s97\n" \
"s_and_b32 s100, 7, s89\n" \
"v_mfma_f32_16x16x32_bf16 a[132:135], v[128:131], v[88:91], a[132:135]\n" \
"s_cmp_eq_u32 %[w], s101\n" \
"s_cselect_b32 s98, s95, 0x40000000\n" \
"v_mfma_f32_16x16x32_bf16 a[132:135], v[124:127], v[84:87], a[132:135]\n" \
"s_lshl_b32 s94, s100, 16\n" \
"s_add_u32 s96, 2, s97\n" \
"v_mfma_f32_16x16x32_bf16 a[164:167], v[124:127], v[92:95], a[164:167]\n" \
"v_writelane_b32 v11, s98, 38\n" \
"s_add_u32 s99, s88, s94\n" \
"v_mfma_f32_16x16x32_bf16 a[164:167], v[128:131], v[96:99], a[164:167]\n" \
"s_and_b32 s101, 7, s96\n" \
"s_add_u32 s100, 3, s97\n" \
"v_mfma_f32_16x16x32_bf16 a[196:199], v[128:131], v[104:107], a[196:199]\n" \
"v_writelane_b32 v11, s99, 42\n" \
"s_lshl_b32 s95, s101, 16\n" \
"v_mfma_f32_16x16x32_bf16 a[196:199], v[124:127], v[100:103], a[196:199]\n" \
"s_and_b32 s94, 7, s100\n" \
"s_add_u32 s98, s88, s95\n" \
"v_mfma_f32_16x16x32_bf16 a[228:231], v[124:127], v[108:111], a[228:231]\n" \
"s_lshl_b32 s99, s94, 16\n" \
"s_add_u32 s89, 0x800, s98\n" \
"v_mfma_f32_16x16x32_bf16 a[228:231], v[128:131], v[112:115], a[228:231]\n" \
"s_add_u32 s96, s88, s99\n" \
"s_add_u32 s95, 4, s97\n" \
"v_mfma_f32_16x16x32_bf16 a[136:139], v[136:139], v[88:91], a[136:139]\n" \
"v_writelane_b32 v11, s89, 43\n" \
"s_add_u32 s101, 0x1000, s96\n" \
"v_mfma_f32_16x16x32_bf16 a[136:139], v[132:135], v[84:87], a[136:139]\n" \
"s_and_b32 s98, 7, s95\n" \
"s_add_u32 s99, 5, s97\n" \
"v_mfma_f32_16x16x32_bf16 a[168:171], v[132:135], v[92:95], a[168:171]\n" \
"v_writelane_b32 v11, s101, 44\n" \
"s_lshl_b32 s89, s98, 16\n" \
"v_mfma_f32_16x16x32_bf16 a[168:171], v[136:139], v[96:99], a[168:171]\n" \
"s_and_b32 s96, 7, s99\n" \
"s_add_u32 s100, s88, s89\n" \
"v_mfma_f32_16x16x32_bf16 a[200:203], v[136:139], v[104:107], a[200:203]\n" \
"s_lshl_b32 s101, s96, 16\n" \
"s_add_u32 s94, 0x1800, s100\n" \
"v_mfma_f32_16x16x32_bf16 a[200:203], v[132:135], v[100:103], a[200:203]\n" \
"s_add_u32 s95, s88, s101\n" \
"s_add_u32 s89, 6, s97\n" \
"v_mfma_f32_16x16x32_bf16 a[232:235], v[132:135], v[108:111], a[232:235]\n" \
"v_writelane_b32 v11, s94, 45\n" \
"s_add_u32 s98, 0x2000, s95\n" \
"v_mfma_f32_16x16x32_bf16 a[232:235], v[136:139], v[112:115], a[232:235]\n" \
"s_and_b32 s100, 7, s89\n" \
"s_add_u32 s101, 7, s97\n" \
"v_mfma_f32_16x16x32_bf16 a[140:143], v[144:147], v[88:91], a[140:143]\n" \
"s_add_u32 s62, s22, 0x180\n" \
"s_and_b32 s62, s62, 0x3ff\n" \
"v_mfma_f32_16x16x32_bf16 a[140:143], v[140:143], v[84:87], a[140:143]\n" \
"s_lshl_b32 s63, s62, 12\n" \
"v_writelane_b32 v11, s98, 46\n" \
"v_mfma_f32_16x16x32_bf16 a[172:175], v[140:143], v[92:95], a[172:175]\n" \
"s_lshl_b32 s94, s100, 16\n" \
"s_and_b32 s95, 7, s101\n" \
"v_mfma_f32_16x16x32_bf16 a[172:175], v[144:147], v[96:99], a[172:175]\n" \
"s_add_u32 s99, s88, s94\n" \
"s_lshl_b32 s98, s95, 16\n" \
"v_mfma_f32_16x16x32_bf16 a[204:207], v[144:147], v[104:107], a[204:207]\n" \
"s_add_u32 s96, 0x2800, s99\n" \
"s_add_u32 s89, s88, s98\n" \
"v_mfma_f32_16x16x32_bf16 a[204:207], v[140:143], v[100:103], a[204:207]\n" \
"v_writelane_b32 v11, s96, 47\n" \
"s_add_u32 s100, 0x3000, s89\n" \
"v_mfma_f32_16x16x32_bf16 a[236:239], v[140:143], v[108:111], a[236:239]\n" \
"v_writelane_b32 v11, s100, 48\n" \
"s_mov_b32 s87, 0x5040100\n" \
"v_mfma_f32_16x16x32_bf16 a[236:239], v[144:147], v[112:115], a[236:239]\n" \
"v_lshlrev_b32_e32 v188, 7, v218\n" \
"v_readfirstlane_b32 %[tag], v180\n" \
"s_waitcnt vmcnt(1)\n" \
"s_sleep 5\n" \
"s_barrier\n" \
"v_mfma_f32_16x16x32_bf16 a[144:147], v[88:91], v[152:155], a[144:147]\n" \
"s_add_u32 m0, %[mA], 0x8000\n" \
"s_add_u32 s85, s62, 0x0\n" \
"s_nop 0\n" \
"v_mfma_f32_16x16x32_bf16 a[144:147], v[84:87], v[148:151], a[144:147]\n" \
"buffer_load_dwordx4 v22, s[64:67], s85 offen lds\n" \
"v_mfma_f32_16x16x32_bf16 a[176:179], v[92:95], v[148:151], a[176:179]\n" \
"s_add_u32 m0, %[mA], 0x8400\n" \
"s_add_u32 s84, s62, 0x2000\n" \
"v_mfma_f32_16x16x32_bf16 a[176:179], v[96:99], v[152:155], a[176:179]\n" \
"ds_read_b64_tr_b16 v[120:121], v32 offset:8192\n" \
"ds_read_b64_tr_b16 v[122:123], v32 offset:9216\n" \
"v_mfma_f32_16x16x32_bf16 a[208:211], v[104:107], v[152:155], a[208:211]\n" \
"buffer_load_dwordx4 v23, s[64:67], s84 offen lds\n" \
"v_mfma_f32_16x16x32_bf16 a[208:211], v[100:103], v[148:151], a[208:211]\n" \
"ds_read_b128 v[56:59], v29 offset:16384\n" \
"ds_read_b64_tr_b16 v[116:117], v32 offset:0\n" \
"v_mfma_f32_16x16x32_bf16 a[240:243], v[108:111], v[148:151], a[240:243]\n" \
"s_add_u32 m0, %[mB], 0x8000\n" \
"s_add_u32 s85, s63, 0x0\n" \
"v_mfma_f32_16x16x32_bf16 a[240:243], v[112:115], v[152:155], a[240:243]\n" \
"buffer_load_dwordx4 v26, s[68:71], s85 offen lds\n" \
"v_mfma_f32_16x16x32_bf16 a[148:151], v[88:91], v[160:163], a[148:151]\n" \
"ds_read_b64_tr_b16 v[118:119], v32 offset:1024\n" \
"ds_read_b128 v[52:55], v28 offset:16384\n" \
"v_mfma_f32_16x16x32_bf16 a[148:151], v[84:87], v[156:159], a[148:151]\n" \
"ds_read_b128 v[60:63], v28 offset:18432\n" \
"ds_read_b128 v[64:67], v29 offset:18432\n" \
"v_mfma_f32_16x16x32_bf16 a[180:183], v[92:95], v[156:159], a[180:183]\n" \
"s_add_u32 m0, %[mB], 0xc000\n" \
"s_add_u32 s84, s63, 0x0\n" \
"v_mfma_f32_16x16x32_bf16 a[180:183], v[96:99], v[160:163], a[180:183]\n" \
"buffer_load_dwordx4 v26, s[72:75], s84 offen lds\n" \
"v_mfma_f32_16x16x32_bf16 a[212:215], v[104:107], v[160:163], a[212:215]\n" \
"ds_read_b128 v[72:75], v29 offset:20480\n" \
"ds_read_b128 v[68:71], v28 offset:20480\n" \
"v_mfma_f32_16x16x32_bf16 a[212:215], v[100:103], v[156:159], a[212:215]\n" \
"ds_read_b128 v[76:79], v28 offset:22528\n" \
"ds_read_b128 v[80:83], v29 offset:22528\n" \
"v_mfma_f32_16x16x32_bf16 a[244:247], v[108:111], v[156:159], a[244:247]\n" \
"s_add_u32 m0, %[mA], 0x8800\n" \
"s_add_u32 s85, s62, 0x4000\n" \
"v_mfma_f32_16x16x32_bf16 a[244:247], v[112:115], v[160:163], a[244:247]\n" \
"buffer_load_dwordx4 v22, s[64:67], s85 offen lds\n" \
"v_mfma_f32_16x16x32_bf16 a[152:155], v[88:91], v[168:171], a[152:155]\n" \
"ds_read_b64_tr_b16 v[128:129], v33 offset:8192\n" \
"ds_read_b64_tr_b16 v[130:131], v33 offset:9216\n" \
"v_mfma_f32_16x16x32_bf16 a[152:155], v[84:87], v[164:167], a[152:155]\n" \
"s_add_u32 m0, %[mA], 0x8c00\n" \
"s_add_u32 s84, s62, 0x6000\n" \
"v_mfma_f32_16x16x32_bf16 a[184:187], v[92:95], v[164:167], a[184:187]\n" \
"buffer_load_dwordx4 v23, s[64:67], s84 offen lds\n" \
"v_mfma_f32_16x16x32_bf16 a[184:187], v[96:99], v[168:171], a[184:187]\n" \
"ds_read_b64_tr_b16 v[124:125], v33 offset:0\n" \
"ds_read_b64_tr_b16 v[126:127], v33 offset:1024\n" \
"v_mfma_f32_16x16x32_bf16 a[216:219], v[104:107], v[168:171], a[216:219]\n" \
"ds_read_b64_tr_b16 v[136:137], v34 offset:8192\n" \
"ds_read_b64_tr_b16 v[138:139], v34 offset:9216\n" \
"v_mfma_f32_16x16x32_bf16 a[216:219], v[100:103], v[164:167], a[216:219]\n" \
"s_add_u32 m0, %[mB], 0x8400\n" \
"s_add_u32 s85, s63, 0x8000\n" \
"v_mfma_f32_16x16x32_bf16 a[248:251], v[108:111], v[164:167], a[248:251]\n" \
"buffer_load_dwordx4 v26, s[68:71], s85 offen lds\n" \
"v_mfma_f32_16x16x32_bf16 a[248:251], v[112:115], v[168:171], a[248:251]\n" \
"ds_read_b64_tr_b16 v[132:133], v34 offset:0\n" \
"ds_read_b64_tr_b16 v[134:135], v34 offset:1024\n" \
"v_mfma_f32_16x16x32_bf16 a[156:159], v[88:91], v[176:179], a[156:159]\n" \
"ds_read_b64_tr_b16 v[144:145], v35 offset:8192\n" \
"ds_read_b64_tr_b16 v[146:147], v35 offset:9216\n" \
"v_mfma_f32_16x16x32_bf16 a[156:159], v[84:87], v[172:175], a[156:159]\n" \
"s_add_u32 m0, %[mB], 0xc400\n" \
"s_add_u32 s84, s63, 0x8000\n" \
"v_mfma_f32_16x16x32_bf16 a[188:191], v[92:95], v[172:175], a[188:191]\n" \
"buffer_load_dwordx4 v26, s[72:75], s84 offen lds\n" \
"v_mfma_f32_16x16x32_bf16 a[188:191], v[96:99], v[176:179], a[188:191]\n" \
"ds_read_b64_tr_b16 v[140:141], v35 offset:0\n" \
"ds_read_b64_tr_b16 v[142:143], v35 offset:1024\n" \
"v_mfma_f32_16x16x32_bf16 a[220:223], v[104:107], v[176:179], a[220:223]\n" \
"ds_read_b64_tr_b16 v[152:153], v32 offset:24576\n" \
"ds_read_b64_tr_b16 v[154:155], v32 offset:25600\n" \
"v_mfma_f32_16x16x32_bf16 a[220:223], v[100:103], v[172:175], a[220:223]\n" \
"s_add_u32 m0, %[mA], 0x9000\n" \
"s_add_u32 s85, s62, 0x8000\n" \
"v_mfma_f32_16x16x32_bf16 a[252:255], v[108:111], v[172:175], a[252:255]\n" \
"buffer_load_dwordx4 v22, s[64:67], s85 offen lds\n" \
"v_mfma_f32_16x16x32_bf16 a[252:255], v[112:115], v[176:179], a[252:255]\n" \
"ds_read_b64_tr_b16 v[148:149], v32 offset:16384\n" \
"s_waitcnt lgkmcnt(13)\n" \
"s_nop 0\n" \
"v_mfma_f32_16x16x32_bf16 a[0:3], v[120:123], v[56:59], a[0:3]\n" \
"s_add_u32 m0, %[mA], 0x9400\n" \
"s_add_u32 s84, s62, 0xa000\n" \
"v_mfma_f32_16x16x32_bf16 a[0:3], v[116:119], v[52:55], a[0:3]\n" \
"buffer_load_dwordx4 v23, s[64:67], s84 offen lds\n" \
"v_mfma_f32_16x16x32_bf16 a[32:35], v[116:119], v[60:63], a[32:35]\n" \
"ds_read_b64_tr_b16 v[150:151], v32 offset:17408\n" \
"ds_read_b64_tr_b16 v[160:161], v33 offset:24576\n" \
"v_mfma_f32_16x16x32_bf16 a[32:35], v[120:123], v[64:67], a[32:35]\n" \
"ds_read_b64_tr_b16 v[162:163], v33 offset:25600\n" \
"ds_read_b64_tr_b16 v[156:157], v33 offset:16384\n" \
"v_mfma_f32_16x16x32_bf16 a[64:67], v[120:123], v[72:75], a[64:67]\n" \
"s_add_u32 m0, %[mB], 0x8800\n" \
"s_add_u32 s85, s63, 0x10000\n" \
"v_mfma_f32_16x16x32_bf16 a[64:67], v[116:119], v[68:71], a[64:67]\n" \
"buffer_load_dwordx4 v27, s[68:71], s85 offen lds\n" \
"v_mfma_f32_16x16x32_bf16 a[96:99], v[116:119], v[76:79], a[96:99]\n" \
"ds_read_b64_tr_b16 v[158:159], v33 offset:17408\n" \
"ds_read_b64_tr_b16 v[168:169], v34 offset:24576\n" \
"v_mfma_f32_16x16x32_bf16 a[96:99], v[120:123], v[80:83], a[96:99]\n" \
"ds_read_b64_tr_b16 v[170:171], v34 offset:25600\n" \
"ds_read_b64_tr_b16 v[164:165], v34 offset:16384\n" \
"v_mfma_f32_16x16x32_bf16 a[4:7], v[128:131], v[56:59], a[4:7]\n" \
"s_add_u32 m0, %[mB], 0xc800\n" \
"s_waitcnt lgkmcnt(13)\n" \
"s_nop 0\n" \
"v_mfma_f32_16x16x32_bf16 a[4:7], v[124:127], v[52:55], a[4:7]\n" \
"s_add_u32 s84, s63, 0x10000\n" \
"ds_read_b64_tr_b16 v[166:167], v34 offset:17408\n" \
"v_mfma_f32_16x16x32_bf16 a[36:39], v[124:127], v[60:63], a[36:39]\n" \
"buffer_load_dwordx4 v27, s[72:75], s84 offen lds\n" \
"v_mfma_f32_16x16x32_bf16 a[36:39], v[128:131], v[64:67], a[36:39]\n" \
"ds_read_b64_tr_b16 v[176:177], v35 offset:24576\n" \
"ds_read_b64_tr_b16 v[178:179], v35 offset:25600\n" \
"v_mfma_f32_16x16x32_bf16 a[68:71], v[128:131], v[72:75], a[68:71]\n" \
"s_add_u32 m0, %[mA], 0x9800\n" \
"s_add_u32 s85, s62, 0xc000\n" \
"v_mfma_f32_16x16x32_bf16 a[68:71], v[124:127], v[68:71], a[68:71]\n" \
"buffer_load_dwordx4 v22, s[64:67], s85 offen lds\n" \
"v_mfma_f32_16x16x32_bf16 a[100:103], v[124:127], v[76:79], a[100:103]\n" \
"ds_read_b64_tr_b16 v[172:173], v35 offset:16384\n" \
"ds_read_b64_tr_b16 v[174:175], v35 offset:17408\n" \
"v_mfma_f32_16x16x32_bf16 a[100:103], v[128:131], v[80:83], a[100:103]\n" \
"s_add_u32 m0, %[mA], 0x9c00\n" \
"s_add_u32 s84, s62, 0xe000\n" \
"v_mfma_f32_16x16x32_bf16 a[8:11], v[136:139], v[56:59], a[8:11]\n" \
"buffer_load_dwordx4 v23, s[64:67], s84 offen lds\n" \
"v_mfma_f32_16x16x32_bf16 a[8:11], v[132:135], v[52:55], a[8:11]\n" \
"ds_read_b128 v[88:91], v29 offset:0\n" \
"ds_read_b128 v[84:87], v28 offset:0\n" \
"v_mfma_f32_16x16x32_bf16 a[40:43], v[132:135], v[60:63], a[40:43]\n" \
"ds_read_b128 v[92:95], v28 offset:2048\n" \
"ds_read_b128 v[96:99], v29 offset:2048\n" \
"v_mfma_f32_16x16x32_bf16 a[40:43], v[136:139], v[64:67], a[40:43]\n" \
"s_add_u32 m0, %[mB], 0x8c00\n" \
"s_add_u32 s85, s63, 0x18000\n" \
"v_mfma_f32_16x16x32_bf16 a[72:75], v[136:139], v[72:75], a[72:75]\n" \
"buffer_load_dwordx4 v27, s[68:71], s85 offen lds\n" \
"v_mfma_f32_16x16x32_bf16 a[72:75], v[132:135], v[68:71], a[72:75]\n" \
"ds_read_b128 v[104:107], v29 offset:4096\n" \
"ds_read_b128 v[100:103], v28 offset:4096\n" \
"v_mfma_f32_16x16x32_bf16 a[104:107], v[132:135], v[76:79], a[104:107]\n" \
"ds_read_b128 v[108:111], v28 offset:6144\n" \
"ds_read_b128 v[112:115], v29 offset:6144\n" \
"v_mfma_f32_16x16x32_bf16 a[104:107], v[136:139], v[80:83], a[104:107]\n" \
"s_add_u32 m0, %[mB], 0xcc00\n" \
"s_add_u32 s84, s63, 0x18000\n" \
"v_mfma_f32_16x16x32_bf16 a[12:15], v[144:147], v[56:59], a[12:15]\n" \
"s_add_u32 %[tag], %[tag], 0x9E3779B1\n" \
"s_waitcnt lgkmcnt(12)\n" \
"v_mfma_f32_16x16x32_bf16 a[12:15], v[140:143], v[52:55], a[12:15]\n" \
"buffer_load_dwordx4 v27, s[72:75], s84 offen lds\n" \
"v_mfma_f32_16x16x32_bf16 a[44:47], v[140:143], v[60:63], a[44:47]\n" \
"v_mov_b32_e32 v45, %[tag]\n" \
"s_lshr_b32 s94, %[b], 5\n" \
"v_mfma_f32_16x16x32_bf16 a[44:47], v[144:147], v[64:67], a[44:47]\n" \
"s_sub_u32 s95, 15, s94\n" \
"s_and_b32 s100, 1, %[w]\n" \
"v_mfma_f32_16x16x32_bf16 a[76:79], v[144:147], v[72:75], a[76:79]\n" \
"buffer_load_dwordx4 v[200:203], v192, s[76:79], s56 offen offset:0\n" \
"v_mfma_f32_16x16x32_bf16 a[76:79], v[140:143], v[68:71], a[76:79]\n" \
"s_cmp_lt_u32 %[w], 2\n" \
"s_cselect_b32 s96, s95, s94\n" \
"v_mfma_f32_16x16x32_bf16 a[108:111], v[140:143], v[76:79], a[108:111]\n" \
"s_lshl_b32 s97, s96, 7\n" \
"s_add_u32 s98, 0xfffffff0, s97\n" \
"v_mfma_f32_16x16x32_bf16 a[108:111], v[144:147], v[80:83], a[108:111]\n" \
"s_cmp_eq_u32 s97, 0\n" \
"s_cselect_b32 s99, 0, s98\n" \
"v_mfma_f32_16x16x32_bf16 a[16:19], v[56:59], v[152:155], a[16:19]\n" \
"v_cmp_gt_u32_e32 vcc, 32, v212\n" \
"s_lshl_b32 s101, s100, 3\n" \
"v_mfma_f32_16x16x32_bf16 a[16:19], v[52:55], v[148:151], a[16:19]\n" \
"buffer_load_dwordx4 v[204:207], v193, s[76:79], s56 offen offset:0\n" \
"v_mfma_f32_16x16x32_bf16 a[48:51], v[60:63], v[148:151], a[48:51]\n" \
"s_add_u32 s88, s99, s101\n" \
"v_lshrrev_b32_e32 v212, 4, v20\n" \
"v_mfma_f32_16x16x32_bf16 a[48:51], v[64:67], v[152:155], a[48:51]\n" \
"v_cndmask_b32_e32 v17, v217, v214, vcc\n" \
"v_lshrrev_b32_e32 v213, 3, v212\n" \
"v_mfma_f32_16x16x32_bf16 a[80:83], v[72:75], v[152:155], a[80:83]\n" \
"s_lshr_b32 s99, %[b], 3\n" \
"v_add_u32_e64 v214, s88, v213\n" \
"v_mfma_f32_16x16x32_bf16 a[80:83], v[68:71], v[148:151], a[80:83]\n" \
"s_lshl_b32 s96, s100, 6\n" \
"s_and_b32 s101, 3, s99\n" \
"v_mfma_f32_16x16x32_bf16 a[112:115], v[76:79], v[148:151], a[112:115]\n" \
"s_add_u32 s98, s97, s96\n" \
"s_add_u32 s88, s101, s94\n" \
"v_mfma_f32_16x16x32_bf16 a[112:115], v[80:83], v[152:155], a[112:115]\n" \
"buffer_load_dword v208, v194, s[76:79], s56 offen offset:32\n" \
"v_mfma_f32_16x16x32_bf16 a[20:23], v[56:59], v[160:163], a[20:23]\n" \
"v_add_u32_e32 v221, s98, v213\n" \
"s_add_u32 s96, 0xfffffff7, s88\n" \
"v_mfma_f32_16x16x32_bf16 a[20:23], v[52:55], v[156:159], a[20:23]\n" \
"s_cmp_lt_u32 s88, 9\n" \
"s_cselect_b32 s98, s88, s96\n" \
"v_mfma_f32_16x16x32_bf16 a[52:55], v[60:63], v[156:159], a[52:55]\n" \
"s_lshl_b32 s89, s100, 2\n" \
"s_add_u32 s99, s97, s100\n" \
"v_mfma_f32_16x16x32_bf16 a[52:55], v[64:67], v[160:163], a[52:55]\n" \
"s_lshr_b32 s88, s97, 4\n" \
"s_lshl_b32 s23, s98, 7\n" \
"v_mfma_f32_16x16x32_bf16 a[84:87], v[72:75], v[160:163], a[84:87]\n" \
"s_cmp_eq_u32 s99, 0\n" \
"s_cselect_b32 s49, 0xff800000, 0\n" \
"v_mfma_f32_16x16x32_bf16 a[84:87], v[68:71], v[156:159], a[84:87]\n" \
"buffer_load_dword v209, v194, s[76:79], s56 offen offset:1184\n" \
"v_mfma_f32_16x16x32_bf16 a[116:119], v[76:79], v[156:159], a[116:119]\n" \
"s_add_u32 s96, s88, s89\n" \
"v_lshrrev_b32_e32 v217, 4, v212\n" \
"v_mfma_f32_16x16x32_bf16 a[116:119], v[80:83], v[160:163], a[116:119]\n" \
"s_mul_i32 s98, 0x24800, s96\n" \
"s_and_b32 s97, 7, %[b]\n" \
"v_mfma_f32_16x16x32_bf16 a[24:27], v[56:59], v[168:171], a[24:27]\n" \
"v_and_b32_e32 v216, 7, v212\n" \
"v_add_u32_e32 v218, s89, v217\n" \
"v_mfma_f32_16x16x32_bf16 a[24:27], v[52:55], v[164:167], a[24:27]\n" \
"s_add_u32 s99, 0x3000000, s98\n" \
"s_lshl_b32 s88, s97, 8\n" \
"v_mfma_f32_16x16x32_bf16 a[56:59], v[60:63], v[164:167], a[56:59]\n" \
"v_xor_b32_e32 v219, v216, v218\n" \
"s_add_u32 s89, s99, s88\n" \
"v_mfma_f32_16x16x32_bf16 a[56:59], v[64:67], v[168:171], a[56:59]\n" \
"buffer_load_dword v211, v194, s[76:79], s56 offen offset:2336\n" \
"v_mfma_f32_16x16x32_bf16 a[88:91], v[72:75], v[168:171], a[88:91]\n" \
"s_mov_b32 s76, s14\n" \
"s_and_b32 s77, s15, 0xffff\n" \
"v_mfma_f32_16x16x32_bf16 a[88:91], v[68:71], v[164:167], a[88:91]\n" \
"s_mov_b32 s78, 0x480\n" \
"s_add_u32 s56, s57, 0x1000\n" \
"v_mfma_f32_16x16x32_bf16 a[120:123], v[76:79], v[164:167], a[120:123]\n" \
"s_lshl_b32 s96, s101, 2\n" \
"v_mul_u32_u24_e32 v215, 0x480, v214\n" \
"v_mfma_f32_16x16x32_bf16 a[120:123], v[80:83], v[168:171], a[120:123]\n" \
"v_lshlrev_b32_e32 v220, 4, v219\n" \
"s_waitcnt lgkmcnt(0)\n" \
"v_mfma_f32_16x16x32_bf16 a[28:31], v[56:59], v[176:179], a[28:31]\n" \
"v_lshrrev_b32_e32 v229, 2, v212\n" \
"v_and_b32_e64 v234, 3, v212\n" \
"v_mfma_f32_16x16x32_bf16 a[28:31], v[52:55], v[172:175], a[28:31]\n" \
"buffer_load_ushort v210, v195, s[76:79], 0 offen\n" \
"v_mfma_f32_16x16x32_bf16 a[60:63], v[60:63], v[172:175], a[60:63]\n" \
"v_and_b32_e32 v249, 2, v212\n" \
"s_add_u32 s98, s89, s96\n" \
"v_mfma_f32_16x16x32_bf16 a[60:63], v[64:67], v[176:179], a[60:63]\n" \
"v_add_u32_e32 v21, v215, v220\n" \
"v_add_u32_e32 v225, 4, v217\n" \
"v_mfma_f32_16x16x32_bf16 a[92:95], v[72:75], v[176:179], a[92:95]\n" \
"s_add_u32 s51, 0x4000, s98\n" \
"s_lshl_b32 s88, s97, 2\n" \
"v_mfma_f32_16x16x32_bf16 a[92:95], v[68:71], v[172:175], a[92:95]\n" \
"v_lshrrev_b32_e32 v212, 4, v20\n" \
"v_xor_b32_e64 v223, v216, v217\n" \
"v_mfma_f32_16x16x32_bf16 a[124:127], v[76:79], v[172:175], a[124:127]\n" \
"v_xor_b32_e64 v226, v216, v225\n" \
"v_mov_b32_e32 v250, 0xeeeeeeee\n" \
"v_mfma_f32_16x16x32_bf16 a[124:127], v[80:83], v[176:179], a[124:127]\n" \
"L_pf1:\n" \
"s_cbranch_execz L_pf5\n" \
"s_nop 0\n" \
"v_mov_b32_e32 v251, 0x44444444\n" \
"v_cmp_ne_u32_e32 vcc, 0, v249\n" \
"v_mfma_f32_16x16x32_bf16 a[144:147], v[88:91], v[152:155], a[144:147]\n" \
"s_add_u32 s89, s88, s101\n" \
"s_lshl_b32 s98, %[w], 10\n" \
"v_mfma_f32_16x16x32_bf16 a[144:147], v[84:87], v[148:151], a[144:147]\n" \
"v_and_b32_e32 v215, 15, v212\n" \
"v_cndmask_b32_e32 v50, v251, v250, vcc\n" \
"v_mfma_f32_16x16x32_bf16 a[176:179], v[92:95], v[148:151], a[176:179]\n" \
"s_lshl_b32 s96, s89, 9\n" \
"s_add_u32 s40, 0x14000, s98\n" \
"v_mfma_f32_16x16x32_bf16 a[176:179], v[96:99], v[152:155], a[176:179]\n" \
"s_lshl_b32 s97, s89, 20\n" \
"v_lshrrev_b32_e32 v213, 4, v212\n" \
"v_mfma_f32_16x16x32_bf16 a[208:211], v[104:107], v[152:155], a[208:211]\n" \
"v_and_b32_e32 v216, 1, v215\n" \
"v_lshlrev_b32_e32 v228, 3, v217\n" \
"v_mfma_f32_16x16x32_bf16 a[208:211], v[100:103], v[148:151], a[208:211]\n" \
"v_and_b32_e32 v238, 1, v217\n" \
"v_lshlrev_b32_e32 v242, 4, v217\n" \
"v_mfma_f32_16x16x32_bf16 a[240:243], v[108:111], v[148:151], a[240:243]\n" \
"s_add_u32 s50, s99, s96\n" \
"s_add_u32 s88, 0x1000000, s97\n" \
"v_mfma_f32_16x16x32_bf16 a[240:243], v[112:115], v[152:155], a[240:243]\n" \
"s_lshl_b32 s101, %[w], 12\n" \
"s_lshr_b32 s98, %[w], 1\n" \
"v_mfma_f32_16x16x32_bf16 a[148:151], v[88:91], v[160:163], a[148:151]\n" \
"s_add_u32 s62, s22, 0x200\n" \
"s_and_b32 s62, s62, 0x3ff\n" \
"v_mfma_f32_16x16x32_bf16 a[148:151], v[84:87], v[156:159], a[148:151]\n" \
"s_lshl_b32 s63, s62, 12\n" \
"v_lshlrev_b32_e32 v214, 2, v213\n" \
"v_mfma_f32_16x16x32_bf16 a[180:183], v[92:95], v[156:159], a[180:183]\n" \
"v_mov_b32_e32 v218, 0x1000c0c\n" \
"v_mov_b32_e32 v219, 0xc0c0100\n" \
"v_mfma_f32_16x16x32_bf16 a[180:183], v[96:99], v[160:163], a[180:183]\n" \
"v_cmp_ne_u32_e32 vcc, 0, v216\n" \
"v_mul_u32_u24_e32 v222, 0x480, v221\n" \
"v_mfma_f32_16x16x32_bf16 a[212:215], v[104:107], v[160:163], a[212:215]\n" \
"s_add_u32 s52, s88, s101\n" \
"s_lshl_b32 s99, %[w], 11\n" \
"v_mfma_f32_16x16x32_bf16 a[212:215], v[100:103], v[156:159], a[212:215]\n" \
"s_lshl_b32 s89, s98, 15\n" \
"v_add_u32_e32 v217, v214, v216\n" \
"v_mfma_f32_16x16x32_bf16 a[244:247], v[108:111], v[156:159], a[244:247]\n" \
"v_cndmask_b32_e32 v220, v219, v218, vcc\n" \
"v_lshlrev_b32_e32 v224, 4, v223\n" \
"v_mfma_f32_16x16x32_bf16 a[244:247], v[112:115], v[160:163], a[244:247]\n" \
"s_add_u32 s96, s88, s99\n" \
"s_add_u32 s97, s88, s89\n" \
"s_waitcnt vmcnt(6)\n" \
"s_barrier\n" \
"v_mfma_f32_16x16x32_bf16 a[152:155], v[88:91], v[168:171], a[152:155]\n" \
"s_add_u32 m0, %[mA], 0x0\n" \
"s_add_u32 s85, s62, 0x0\n" \
"v_mfma_f32_16x16x32_bf16 a[152:155], v[84:87], v[164:167], a[152:155]\n" \
"buffer_load_dwordx4 v22, s[64:67], s85 offen lds\n" \
"v_mfma_f32_16x16x32_bf16 a[184:187], v[92:95], v[164:167], a[184:187]\n" \
"s_add_u32 m0, %[mA], 0x400\n" \
"s_add_u32 s84, s62, 0x2000\n" \
"v_mfma_f32_16x16x32_bf16 a[184:187], v[96:99], v[168:171], a[184:187]\n" \
"ds_read_b64_tr_b16 v[152:153], v32 offset:57344\n" \
"ds_read_b64_tr_b16 v[154:155], v32 offset:58368\n" \
"v_mfma_f32_16x16x32_bf16 a[216:219], v[104:107], v[168:171], a[216:219]\n" \
"buffer_load_dwordx4 v23, s[64:67], s84 offen lds\n" \
"v_mfma_f32_16x16x32_bf16 a[216:219], v[100:103], v[164:167], a[216:219]\n" \
"ds_read_b128 v[56:59], v29 offset:49152\n" \
"ds_read_b64_tr_b16 v[148:149], v32 offset:49152\n" \
"v_mfma_f32_16x16x32_bf16 a[248:251], v[108:111], v[164:167], a[248:251]\n" \
"s_add_u32 m0, %[mB], 0x0\n" \
"s_add_u32 s85, s63, 0x0\n" \
"v_mfma_f32_16x16x32_bf16 a[248:251], v[112:115], v[168:171], a[248:251]\n" \
"buffer_load_dwordx4 v26, s[68:71], s85 offen lds\n" \
"v_mfma_f32_16x16x32_bf16 a[156:159], v[88:91], v[176:179], a[156:159]\n" \
"ds_read_b64_tr_b16 v[150:151], v32 offset:50176\n" \
"ds_read_b128 v[52:55], v28 offset:49152\n" \
"v_mfma_f32_16x16x32_bf16 a[156:159], v[84:87], v[172:175], a[156:159]\n" \
"ds_read_b128 v[60:63], v28 offset:51200\n" \
"ds_read_b128 v[64:67], v29 offset:51200\n" \
"v_mfma_f32_16x16x32_bf16 a[188:191], v[92:95], v[172:175], a[188:191]\n" \
"s_add_u32 m0, %[mB], 0x4000\n" \
"s_add_u32 s84, s63, 0x0\n" \
"s_nop 0\n" \
"v_mfma_f32_16x16x32_bf16 a[188:191], v[96:99], v[176:179], a[188:191]\n" \
"buffer_load_dwordx4 v26, s[72:75], s84 offen lds\n" \
"v_mfma_f32_16x16x32_bf16 a[220:223], v[104:107], v[176:179], a[220:223]\n" \
"ds_read_b128 v[72:75], v29 offset:53248\n" \
"ds_read_b128 v[68:71], v28 offset:53248\n" \
"v_mfma_f32_16x16x32_bf16 a[220:223], v[100:103], v[172:175], a[220:223]\n" \
"ds_read_b128 v[76:79], v28 offset:55296\n" \
"ds_read_b128 v[80:83], v29 offset:55296\n" \
"v_mfma_f32_16x16x32_bf16 a[252:255], v[108:111], v[172:175], a[252:255]\n" \
"s_add_u32 m0, %[mA], 0x800\n" \
"s_add_u32 s85, s62, 0x4000\n" \
"v_mfma_f32_16x16x32_bf16 a[252:255], v[112:115], v[176:179], a[252:255]\n" \
"buffer_load_dwordx4 v22, s[64:67], s85 offen lds\n" \
"v_mfma_f32_16x16x32_bf16 a[128:131], v[120:123], v[88:91], a[128:131]\n" \
"ds_read_b64_tr_b16 v[160:161], v33 offset:57344\n" \
"ds_read_b64_tr_b16 v[162:163], v33 offset:58368\n" \
"v_mfma_f32_16x16x32_bf16 a[128:131], v[116:119], v[84:87], a[128:131]\n" \
"s_add_u32 m0, %[mA], 0xc00\n" \
"s_add_u32 s84, s62, 0x6000\n" \
"v_mfma_f32_16x16x32_bf16 a[160:163], v[116:119], v[92:95], a[160:163]\n" \
"buffer_load_dwordx4 v23, s[64:67], s84 offen lds\n" \
"v_mfma_f32_16x16x32_bf16 a[160:163], v[120:123], v[96:99], a[160:163]\n" \
"ds_read_b64_tr_b16 v[156:157], v33 offset:49152\n" \
"ds_read_b64_tr_b16 v[158:159], v33 offset:50176\n" \
"v_mfma_f32_16x16x32_bf16 a[192:195], v[120:123], v[104:107], a[192:195]\n" \
"ds_read_b64_tr_b16 v[168:169], v34 offset:57344\n" \
"ds_read_b64_tr_b16 v[170:171], v34 offset:58368\n" \
"v_mfma_f32_16x16x32_bf16 a[192:195], v[116:119], v[100:103], a[192:195]\n" \
"s_add_u32 m0, %[mB], 0x400\n" \
"s_add_u32 s85, s63, 0x8000\n" \
"v_mfma_f32_16x16x32_bf16 a[224:227], v[116:119], v[108:111], a[224:227]\n" \
"buffer_load_dwordx4 v26, s[68:71], s85 offen lds\n" \
"v_mfma_f32_16x16x32_bf16 a[224:227], v[120:123], v[112:115], a[224:227]\n" \
"ds_read_b64_tr_b16 v[164:165], v34 offset:49152\n" \
"ds_read_b64_tr_b16 v[166:167], v34 offset:50176\n" \
"v_mfma_f32_16x16x32_bf16 a[132:135], v[128:131], v[88:91], a[132:135]\n" \
"ds_read_b64_tr_b16 v[176:177], v35 offset:57344\n" \
"ds_read_b64_tr_b16 v[178:179], v35 offset:58368\n" \
"v_mfma_f32_16x16x32_bf16 a[132:135], v[124:127], v[84:87], a[132:135]\n" \
"s_add_u32 m0, %[mB], 0x4400\n" \
"s_add_u32 s84, s63, 0x8000\n" \
"v_mfma_f32_16x16x32_bf16 a[164:167], v[124:127], v[92:95], a[164:167]\n" \
"buffer_load_dwordx4 v26, s[72:75], s84 offen lds\n" \
"v_mfma_f32_16x16x32_bf16 a[164:167], v[128:131], v[96:99], a[164:167]\n" \
"ds_read_b64_tr_b16 v[172:173], v35 offset:49152\n" \
"ds_read_b64_tr_b16 v[174:175], v35 offset:50176\n" \
"v_mfma_f32_16x16x32_bf16 a[196:199], v[128:131], v[104:107], a[196:199]\n" \
"ds_read_b64_tr_b16 v[120:121], v32 offset:40960\n" \
"ds_read_b64_tr_b16 v[122:123], v32 offset:41984\n" \
"v_mfma_f32_16x16x32_bf16 a[196:199], v[124:127], v[100:103], a[196:199]\n" \
"s_add_u32 m0, %[mA], 0x1000\n" \
"s_add_u32 s85, s62, 0x8000\n" \
"v_mfma_f32_16x16x32_bf16 a[228:231], v[124:127], v[108:111], a[228:231]\n" \
"buffer_load_dwordx4 v22, s[64:67], s85 offen lds\n" \
"v_mfma_f32_16x16x32_bf16 a[228:231], v[128:131], v[112:115], a[228:231]\n" \
"ds_read_b64_tr_b16 v[116:117], v32 offset:32768\n" \
"ds_read_b64_tr_b16 v[118:119], v32 offset:33792\n" \
"v_mfma_f32_16x16x32_bf16 a[136:139], v[136:139], v[88:91], a[136:139]\n" \
"s_add_u32 m0, %[mA], 0x1400\n" \
"s_add_u32 s84, s62, 0xa000\n" \
"v_mfma_f32_16x16x32_bf16 a[136:139], v[132:135], v[84:87], a[136:139]\n" \
"buffer_load_dwordx4 v23, s[64:67], s84 offen lds\n" \
"v_mfma_f32_16x16x32_bf16 a[168:171], v[132:135], v[92:95], a[168:171]\n" \
"ds_read_b64_tr_b16 v[128:129], v33 offset:40960\n" \
"ds_read_b64_tr_b16 v[130:131], v33 offset:41984\n" \
"v_mfma_f32_16x16x32_bf16 a[168:171], v[136:139], v[96:99], a[168:171]\n" \
"ds_read_b64_tr_b16 v[124:125], v33 offset:32768\n" \
"ds_read_b64_tr_b16 v[126:127], v33 offset:33792\n" \
"v_mfma_f32_16x16x32_bf16 a[200:203], v[136:139], v[104:107], a[200:203]\n" \
"s_add_u32 m0, %[mB], 0x800\n" \
"s_add_u32 s85, s63, 0x10000\n" \
"v_mfma_f32_16x16x32_bf16 a[200:203], v[132:135], v[100:103], a[200:203]\n" \
"buffer_load_dwordx4 v27, s[68:71], s85 offen lds\n" \
"v_mfma_f32_16x16x32_bf16 a[232:235], v[132:135], v[108:111], a[232:235]\n" \
"ds_read_b64_tr_b16 v[132:133], v34 offset:32768\n" \
"ds_read_b64_tr_b16 v[134:135], v34 offset:33792\n" \
"v_mfma_f32_16x16x32_bf16 a[232:235], v[136:139], v[112:115], a[232:235]\n" \
"ds_read_b64_tr_b16 v[136:137], v34 offset:40960\n" \
"ds_read_b64_tr_b16 v[138:139], v34 offset:41984\n" \
"v_mfma_f32_16x16x32_bf16 a[140:143], v[144:147], v[88:91], a[140:143]\n" \
"s_add_u32 m0, %[mB], 0x4800\n" \
"s_add_u32 s84, s63, 0x10000\n" \
"v_mfma_f32_16x16x32_bf16 a[140:143], v[140:143], v[84:87], a[140:143]\n" \
"buffer_load_dwordx4 v27, s[72:75], s84 offen lds\n" \
"v_mfma_f32_16x16x32_bf16 a[172:175], v[140:143], v[92:95], a[172:175]\n" \
"ds_read_b128 v[84:87], v28 offset:32768\n" \
"ds_read_b128 v[88:91], v29 offset:32768\n" \
"v_mfma_f32_16x16x32_bf16 a[172:175], v[144:147], v[96:99], a[172:175]\n" \
"ds_read_b128 v[92:95], v28 offset:34816\n" \
"ds_read_b128 v[96:99], v29 offset:34816\n" \
"v_mfma_f32_16x16x32_bf16 a[204:207], v[144:147], v[104:107], a[204:207]\n" \
"s_add_u32 m0, %[mA], 0x1800\n" \
"s_add_u32 s85, s62, 0xc000\n" \
"v_mfma_f32_16x16x32_bf16 a[204:207], v[140:143], v[100:103], a[204:207]\n" \
"buffer_load_dwordx4 v22, s[64:67], s85 offen lds\n" \
"v_mfma_f32_16x16x32_bf16 a[236:239], v[140:143], v[108:111], a[236:239]\n" \
"ds_read_b64_tr_b16 v[140:141], v35 offset:32768\n" \
"ds_read_b64_tr_b16 v[142:143], v35 offset:33792\n" \
"v_mfma_f32_16x16x32_bf16 a[236:239], v[144:147], v[112:115], a[236:239]\n" \
"s_add_u32 m0, %[mA], 0x1c00\n" \
"s_waitcnt lgkmcnt(14)\n" \
"s_nop 0\n" \
"v_mfma_f32_16x16x32_bf16 a[16:19], v[56:59], v[152:155], a[16:19]\n" \
"s_add_u32 s84, s62, 0xe000\n" \
"ds_read_b64_tr_b16 v[144:145], v35 offset:40960\n" \
"v_mfma_f32_16x16x32_bf16 a[16:19], v[52:55], v[148:151], a[16:19]\n" \
"buffer_load_dwordx4 v23, s[64:67], s84 offen lds\n" \
"v_mfma_f32_16x16x32_bf16 a[48:51], v[60:63], v[148:151], a[48:51]\n" \
"ds_read_b64_tr_b16 v[146:147], v35 offset:41984\n" \
"ds_read_b128 v[100:103], v28 offset:36864\n" \
"v_mfma_f32_16x16x32_bf16 a[48:51], v[64:67], v[152:155], a[48:51]\n" \
"s_add_u32 m0, %[mB], 0xc00\n" \
"s_add_u32 s85, s63, 0x18000\n" \
"v_mfma_f32_16x16x32_bf16 a[80:83], v[72:75], v[152:155], a[80:83]\n" \
"buffer_load_dwordx4 v27, s[68:71], s85 offen lds\n" \
"v_mfma_f32_16x16x32_bf16 a[80:83], v[68:71], v[148:151], a[80:83]\n" \
"ds_read_b128 v[104:107], v29 offset:36864\n" \
"ds_read_b128 v[108:111], v28 offset:38912\n" \
"v_mfma_f32_16x16x32_bf16 a[112:115], v[76:79], v[148:151], a[112:115]\n" \
"ds_read_b128 v[112:115], v29 offset:38912\n" \
"s_lshl_b32 s101, s100, 11\n" \
"v_mfma_f32_16x16x32_bf16 a[112:115], v[80:83], v[152:155], a[112:115]\n" \
"s_add_u32 m0, %[mB], 0x4c00\n" \
"s_add_u32 s84, s63, 0x18000\n" \
"v_mfma_f32_16x16x32_bf16 a[20:23], v[56:59], v[160:163], a[20:23]\n" \
"buffer_load_dwordx4 v27, s[72:75], s84 offen lds\n" \
"v_mfma_f32_16x16x32_bf16 a[20:23], v[52:55], v[156:159], a[20:23]\n" \
"s_mov_b32 m0, %[mS]\n" \
"v_mov_b32_e32 v221, 0xc0c0c0c\n" \
"v_mfma_f32_16x16x32_bf16 a[52:55], v[60:63], v[156:159], a[52:55]\n" \
"v_cmp_le_u32_e32 vcc, v217, v215\n" \
"v_add_u32_e32 v24, v222, v224\n" \
"v_mfma_f32_16x16x32_bf16 a[52:55], v[64:67], v[160:163], a[52:55]\n" \
"v_lshlrev_b32_e32 v227, 4, v226\n" \
"s_add_u32 s99, s97, s101\n" \
"v_mfma_f32_16x16x32_bf16 a[84:87], v[72:75], v[160:163], a[84:87]\n" \
"s_lshl_b32 s98, s95, 16\n" \
"v_cndmask_b32_e32 v40, v220, v221, vcc\n" \
"v_mfma_f32_16x16x32_bf16 a[84:87], v[68:71], v[156:159], a[84:87]\n" \
"v_add_u32_e64 v25, v222, v227\n" \
"s_add_u32 s54, 0x10000, s96\n" \
"v_mfma_f32_16x16x32_bf16 a[116:119], v[76:79], v[156:159], a[116:119]\n" \
"s_add_u32 s43, s99, s98\n" \
"s_mul_i32 s88, 0x1800, s100\n" \
"v_mfma_f32_16x16x32_bf16 a[116:119], v[80:83], v[160:163], a[116:119]\n" \
"v_mov_b32_e32 v223, 0x3020c0c\n" \
"v_mov_b32_e32 v224, 0xc0c0302\n" \
"v_mfma_f32_16x16x32_bf16 a[24:27], v[56:59], v[168:171], a[24:27]\n" \
"v_cmp_ne_u32_e32 vcc, 0, v216\n" \
"s_lshl_b32 s96, s94, 16\n" \
"v_mfma_f32_16x16x32_bf16 a[24:27], v[52:55], v[164:167], a[24:27]\n" \
"s_add_u32 s97, s43, s88\n" \
"v_add_u32_e64 v222, 2, v217\n" \
"v_mfma_f32_16x16x32_bf16 a[56:59], v[60:63], v[164:167], a[56:59]\n" \
"v_cndmask_b32_e32 v225, v224, v223, vcc\n" \
"s_add_u32 s42, s99, s96\n" \
"v_mfma_f32_16x16x32_bf16 a[56:59], v[64:67], v[168:171], a[56:59]\n" \
"s_add_u32 s45, 0x4000, s97\n" \
"s_lshr_b32 s94, %[w], 1\n" \
"v_mfma_f32_16x16x32_bf16 a[88:91], v[72:75], v[168:171], a[88:91]\n" \
"s_mov_b32 s86, s10\n" \
"s_and_b32 s95, 1, %[w]\n" \
"v_mfma_f32_16x16x32_bf16 a[88:91], v[68:71], v[164:167], a[88:91]\n" \
"v_mov_b32_e32 v226, 0xc0c0c0c\n" \
"v_cmp_le_u32_e32 vcc, v222, v215\n" \
"s_mov_b32 exec_lo, 0x15\n" \
"s_mov_b32 exec_hi, 0x180000\n" \
"buffer_load_dword v11, s[80:83], 0 offen sc1 lds\n" \
"s_mov_b64 exec, -1\n" \
"v_mfma_f32_16x16x32_bf16 a[120:123], v[76:79], v[164:167], a[120:123]\n" \
"s_mov_b32 m0, %[mS]\n" \
"s_add_u32 s96, s94, s95\n" \
"v_mfma_f32_16x16x32_bf16 a[120:123], v[80:83], v[168:171], a[120:123]\n" \
"s_add_u32 s97, 1, s94\n" \
"v_cndmask_b32_e32 v41, v225, v226, vcc\n" \
"v_mfma_f32_16x16x32_bf16 a[28:31], v[56:59], v[176:179], a[28:31]\n" \
"v_and_b32_e32 v230, 3, v229\n" \
"s_add_u32 s89, s42, s88\n" \
"v_mfma_f32_16x16x32_bf16 a[28:31], v[52:55], v[172:175], a[28:31]\n" \
"s_lshl_b32 s59, s96, 13\n" \
"s_sub_u32 s98, s97, s95\n" \
"v_mfma_f32_16x16x32_bf16 a[60:63], v[60:63], v[172:175], a[60:63]\n" \
"s_lshl_b32 s99, s94, 13\n" \
"s_mul_i32 s101, 0x1800, s94\n" \
"v_mfma_f32_16x16x32_bf16 a[60:63], v[64:67], v[176:179], a[60:63]\n" \
"v_mov_b32_e32 v227, 0xc0c0c0c\n" \
"v_cmp_le_u32_e32 vcc, v217, v215\n" \
"v_mfma_f32_16x16x32_bf16 a[92:95], v[72:75], v[176:179], a[92:95]\n" \
"v_add_u32_e32 v231, v228, v230\n" \
"s_lshl_b32 s60, s98, 13\n" \
"v_mfma_f32_16x16x32_bf16 a[92:95], v[68:71], v[172:175], a[92:95]\n" \
"s_add_u32 s100, 0x1800, s99\n" \
"s_sub_u32 s88, 0x17000, s101\n" \
"v_mfma_f32_16x16x32_bf16 a[124:127], v[76:79], v[172:175], a[124:127]\n" \
"s_lshl_b32 s96, s94, 15\n" \
"v_cndmask_b32_e32 v42, v227, v220, vcc\n" \
"v_mfma_f32_16x16x32_bf16 a[124:127], v[80:83], v[176:179], a[124:127]\n" \
"v_lshlrev_b32_e32 v232, 7, v231\n" \
"s_add_u32 s97, 0x10000, s96\n" \
"v_mfma_f32_16x16x32_bf16 a[0:3], v[120:123], v[56:59], a[0:3]\n" \
"s_cmp_lg_u32 s95, 0\n" \
"s_cselect_b32 s61, s100, s88\n" \
"v_mfma_f32_16x16x32_bf16 a[0:3], v[116:119], v[52:55], a[0:3]\n" \
"s_lshl_b32 s98, s95, 11\n" \
"v_mov_b32_e32 v228, 0xc0c0c0c\n" \
"v_mfma_f32_16x16x32_bf16 a[32:35], v[116:119], v[60:63], a[32:35]\n" \
"v_cmp_le_u32_e32 vcc, v222, v215\n" \
"v_add_u32_e32 v233, 0x14000, v232\n" \
"v_mfma_f32_16x16x32_bf16 a[32:35], v[120:123], v[64:67], a[32:35]\n" \
"v_lshlrev_b32_e32 v235, 3, v234\n" \
"s_add_u32 s101, s97, s98\n" \
"v_mfma_f32_16x16x32_bf16 a[64:67], v[120:123], v[72:75], a[64:67]\n" \
"v_cndmask_b32_e32 v43, v228, v225, vcc\n" \
"v_and_b32_e64 v231, 2, v212\n" \
"v_mfma_f32_16x16x32_bf16 a[64:67], v[116:119], v[68:71], a[64:67]\n" \
"s_waitcnt vmcnt(19)\n" \
"v_perm_b32 v252, v209, v208, s87\n" \
"s_mov_b32 s87, 0x7060302\n" \
"v_perm_b32 v253, v209, v208, s87\n" \
"s_mov_b32 s87, 0x5040100\n" \
"s_waitcnt vmcnt(17)\n" \
"v_perm_b32 v254, v211, v210, s87\n" \
"s_mov_b32 s87, 0x7060100\n" \
"v_perm_b32 v255, v211, v210, s87\n" \
"s_mov_b32 s87, s11\n" \
"s_mov_b32 exec_lo, 0x3ffff\n" \
"s_mov_b32 exec_hi, 0x0\n" \
"global_load_lds_dword v188, s[86:87]\n" \
"s_mov_b64 exec, -1\n" \
"v_mfma_f32_16x16x32_bf16 a[96:99], v[116:119], v[76:79], a[96:99]\n" \
"ds_write_b128 v196, v[200:203]\n" \
"v_mfma_f32_16x16x32_bf16 a[96:99], v[120:123], v[80:83], a[96:99]\n" \
"ds_write2_b32 v198, v252, v253 offset0:0 offset1:2\n" \
"s_waitcnt lgkmcnt(2)\n" \
"v_mfma_f32_16x16x32_bf16 a[4:7], v[128:131], v[56:59], a[4:7]\n" \
"ds_write2_b32 v198, v254, v255 offset0:4 offset1:6\n" \
"v_add_u32_e64 v236, v233, v235\n" \
"v_mfma_f32_16x16x32_bf16 a[4:7], v[124:127], v[52:55], a[4:7]\n" \
"v_lshrrev_b32_e32 v237, 1, v230\n" \
"v_add_u32_e32 v243, v242, v230\n" \
"v_mfma_f32_16x16x32_bf16 a[36:39], v[124:127], v[60:63], a[36:39]\n" \
"s_add_u32 s100, s101, s99\n" \
"v_and_b32_e32 v229, 3, v212\n" \
"v_mfma_f32_16x16x32_bf16 a[36:39], v[128:131], v[64:67], a[36:39]\n" \
"ds_write_b128 v197, v[204:207]\n" \
"v_mfma_f32_16x16x32_bf16 a[68:71], v[128:131], v[72:75], a[68:71]\n" \
"v_mov_b32_e32 v232, 0x3f80\n" \
"v_cmp_eq_u32_e32 vcc, 0, v231\n" \
"v_mfma_f32_16x16x32_bf16 a[68:71], v[124:127], v[68:71], a[68:71]\n" \
"v_lshlrev_b32_e32 v239, 1, v238\n" \
"v_lshlrev_b32_e32 v244, 7, v243\n" \
"v_mfma_f32_16x16x32_bf16 a[100:103], v[124:127], v[76:79], a[100:103]\n" \
"s_add_u32 s88, 0x4000, s100\n" \
"v_or_b32_e32 v230, 2, v229\n" \
"v_mfma_f32_16x16x32_bf16 a[100:103], v[128:131], v[80:83], a[100:103]\n" \
"v_cndmask_b32_e32 v233, 0, v232, vcc\n" \
"v_or_b32_e32 v240, v237, v239\n" \
"v_mfma_f32_16x16x32_bf16 a[8:11], v[136:139], v[56:59], a[8:11]\n" \
"v_add_u32_e32 v245, 0x14000, v244\n" \
"v_cmp_eq_u32_e32 vcc, v230, v213\n" \
"v_mfma_f32_16x16x32_bf16 a[8:11], v[132:135], v[52:55], a[8:11]\n" \
"s_cmp_lg_u32 s95, 0\n" \
"s_cselect_b32 s47, s88, s101\n" \
"v_mfma_f32_16x16x32_bf16 a[40:43], v[132:135], v[60:63], a[40:43]\n" \
"v_lshlrev_b32_e32 v241, 5, v240\n" \
"v_add_u32_e64 v246, v245, v235\n" \
"v_mfma_f32_16x16x32_bf16 a[40:43], v[136:139], v[64:67], a[40:43]\n" \
"v_cndmask_b32_e32 v46, 0, v233, vcc\n" \
"v_and_b32_e32 v234, 51, v212\n" \
"v_mfma_f32_16x16x32_bf16 a[72:75], v[136:139], v[72:75], a[72:75]\n" \
"v_add_u32_e64 v190, v236, v241\n" \
"v_mov_b32_e32 v235, 0x3f80\n" \
"v_mfma_f32_16x16x32_bf16 a[72:75], v[132:135], v[68:71], a[72:75]\n" \
"v_cmp_eq_u32_e32 vcc, 2, v234\n" \
"s_lshl_b32 s88, s95, 4\n" \
"v_mfma_f32_16x16x32_bf16 a[104:107], v[132:135], v[76:79], a[104:107]\n" \
"v_xor_b32_e32 v247, 3, v237\n" \
"v_cndmask_b32_e32 v47, 0, v235, vcc\n" \
"v_mfma_f32_16x16x32_bf16 a[104:107], v[136:139], v[80:83], a[104:107]\n" \
"v_add_u32_e64 v236, s88, v215\n" \
"v_mov_b32_e32 v237, 0xffff\n" \
"v_mfma_f32_16x16x32_bf16 a[12:15], v[144:147], v[56:59], a[12:15]\n" \
"s_add_u32 s62, s22, 0x280\n" \
"s_and_b32 s62, s62, 0x3ff\n" \
"v_mfma_f32_16x16x32_bf16 a[12:15], v[140:143], v[52:55], a[12:15]\n" \
"s_lshl_b32 s63, s62, 12\n" \
"v_cmp_le_u32_e32 vcc, v214, v236\n" \
"v_mfma_f32_16x16x32_bf16 a[44:47], v[140:143], v[60:63], a[44:47]\n" \
"v_mov_b32_e32 v239, 0xffff0000\n" \
"v_cndmask_b32_e32 v238, 0, v237, vcc\n" \
"v_mfma_f32_16x16x32_bf16 a[44:47], v[144:147], v[64:67], a[44:47]\n" \
"v_cmp_lt_u32_e32 vcc, v214, v236\n" \
"v_add_u32_e64 v241, 2, v214\n" \
"v_mfma_f32_16x16x32_bf16 a[76:79], v[144:147], v[72:75], a[76:79]\n" \
"v_cndmask_b32_e32 v240, 0, v239, vcc\n" \
"v_mov_b32_e32 v242, 0xffff\n" \
"v_mfma_f32_16x16x32_bf16 a[76:79], v[140:143], v[68:71], a[76:79]\n" \
"v_cmp_le_u32_e32 vcc, v241, v236\n" \
"v_lshlrev_b32_e32 v248, 5, v247\n" \
"v_mfma_f32_16x16x32_bf16 a[108:111], v[140:143], v[76:79], a[108:111]\n" \
"v_cndmask_b32_e32 v243, 0, v242, vcc\n" \
"v_add_u32_e64 v39, v246, v248\n" \
"v_mfma_f32_16x16x32_bf16 a[108:111], v[144:147], v[80:83], a[108:111]\n" \
"v_mov_b32_e32 v244, 0xffff0000\n" \
"v_cmp_lt_u32_e32 vcc, v241, v236\n" \
"s_waitcnt vmcnt(2) lgkmcnt(0)\n" \
"s_barrier\n" \
"v_mfma_f32_16x16x32_bf16 a[128:131], v[120:123], v[88:91], a[128:131]\n" \
"s_add_u32 m0, %[mA], 0x8000\n" \
"s_add_u32 s85, s62, 0x0\n" \
"v_mfma_f32_16x16x32_bf16 a[128:131], v[116:119], v[84:87], a[128:131]\n" \
"buffer_load_dwordx4 v22, s[64:67], s85 offen lds\n" \
"v_mfma_f32_16x16x32_bf16 a[160:163], v[116:119], v[92:95], a[160:163]\n" \
"ds_read_b128 v[56:59], v29 offset:16384\n" \
"ds_read_b128 v[52:55], v28 offset:16384\n" \
"v_mfma_f32_16x16x32_bf16 a[160:163], v[120:123], v[96:99], a[160:163]\n" \
"s_add_u32 m0, %[mA], 0x8400\n" \
"s_add_u32 s84, s62, 0x2000\n" \
"v_mfma_f32_16x16x32_bf16 a[192:195], v[120:123], v[104:107], a[192:195]\n" \
"buffer_load_dwordx4 v23, s[64:67], s84 offen lds\n" \
"v_mfma_f32_16x16x32_bf16 a[192:195], v[116:119], v[100:103], a[192:195]\n" \
"ds_read_b128 v[60:63], v28 offset:18432\n" \
"ds_read_b128 v[64:67], v29 offset:18432\n" \
"v_mfma_f32_16x16x32_bf16 a[224:227], v[116:119], v[108:111], a[224:227]\n" \
"ds_read_b64_tr_b16 v[116:117], v32 offset:0\n" \
"ds_read_b64_tr_b16 v[118:119], v32 offset:1024\n" \
"v_mfma_f32_16x16x32_bf16 a[224:227], v[120:123], v[112:115], a[224:227]\n" \
"s_add_u32 m0, %[mB], 0x8000\n" \
"s_add_u32 s85, s63, 0x0\n" \
"v_mfma_f32_16x16x32_bf16 a[132:135], v[128:131], v[88:91], a[132:135]\n" \
"buffer_load_dwordx4 v26, s[68:71], s85 offen lds\n" \
"v_mfma_f32_16x16x32_bf16 a[132:135], v[124:127], v[84:87], a[132:135]\n" \
"ds_read_b64_tr_b16 v[120:121], v32 offset:8192\n" \
"ds_read_b64_tr_b16 v[122:123], v32 offset:9216\n" \
"v_mfma_f32_16x16x32_bf16 a[164:167], v[124:127], v[92:95], a[164:167]\n" \
"ds_read_b128 v[72:75], v29 offset:20480\n" \
"ds_read_b128 v[68:71], v28 offset:20480\n" \
"v_mfma_f32_16x16x32_bf16 a[164:167], v[128:131], v[96:99], a[164:167]\n" \
"s_add_u32 m0, %[mB], 0xc000\n" \
"s_add_u32 s84, s63, 0x0\n" \
"v_mfma_f32_16x16x32_bf16 a[196:199], v[128:131], v[104:107], a[196:199]\n" \
"buffer_load_dwordx4 v26, s[72:75], s84 offen lds\n" \
"v_mfma_f32_16x16x32_bf16 a[196:199], v[124:127], v[100:103], a[196:199]\n" \
"ds_read_b128 v[76:79], v28 offset:22528\n" \
"ds_read_b128 v[80:83], v29 offset:22528\n" \
"v_mfma_f32_16x16x32_bf16 a[228:231], v[124:127], v[108:111], a[228:231]\n" \
"ds_read_b64_tr_b16 v[124:125], v33 offset:0\n" \
"ds_read_b64_tr_b16 v[126:127], v33 offset:1024\n" \
"v_mfma_f32_16x16x32_bf16 a[228:231], v[128:131], v[112:115], a[228:231]\n" \
"s_add_u32 m0, %[mA], 0x8800\n" \
"s_add_u32 s85, s62, 0x4000\n" \
"v_mfma_f32_16x16x32_bf16 a[136:139], v[136:139], v[88:91], a[136:139]\n" \
"buffer_load_dwordx4 v22, s[64:67], s85 offen lds\n" \
"v_mfma_f32_16x16x32_bf16 a[136:139], v[132:135], v[84:87], a[136:139]\n" \
"ds_read_b64_tr_b16 v[128:129], v33 offset:8192\n" \
"ds_read_b64_tr_b16 v[130:131], v33 offset:9216\n" \
"v_mfma_f32_16x16x32_bf16 a[168:171], v[132:135], v[92:95], a[168:171]\n" \
"ds_read_b128 v[200:203], v199 offset:0\n" \
"ds_read_b128 v[204:207], v199 offset:4096\n" \
"v_mfma_f32_16x16x32_bf16 a[168:171], v[136:139], v[96:99], a[168:171]\n" \
"s_add_u32 m0, %[mA], 0x8c00\n" \
"s_add_u32 s84, s62, 0x6000\n" \
"v_mfma_f32_16x16x32_bf16 a[200:203], v[136:139], v[104:107], a[200:203]\n" \
"buffer_load_dwordx4 v23, s[64:67], s84 offen lds\n" \
"v_mfma_f32_16x16x32_bf16 a[200:203], v[132:135], v[100:103], a[200:203]\n" \
"ds_read_b128 v[208:211], v199 offset:8192\n" \
"v_cndmask_b32_e32 v245, 0, v244, vcc\n" \
"s_nop 0\n" \
"v_mfma_f32_16x16x32_bf16 a[232:235], v[132:135], v[108:111], a[232:235]\n" \
"ds_read_b64_tr_b16 v[132:133], v34 offset:0\n" \
"ds_read_b64_tr_b16 v[134:135], v34 offset:1024\n" \
"v_mfma_f32_16x16x32_bf16 a[232:235], v[136:139], v[112:115], a[232:235]\n" \
"s_add_u32 m0, %[mB], 0x8400\n" \
"s_add_u32 s85, s63, 0x8000\n" \
"v_mfma_f32_16x16x32_bf16 a[140:143], v[144:147], v[88:91], a[140:143]\n" \
"buffer_load_dwordx4 v26, s[68:71], s85 offen lds\n" \
"v_mfma_f32_16x16x32_bf16 a[140:143], v[140:143], v[84:87], a[140:143]\n" \
"ds_read_b64_tr_b16 v[136:137], v34 offset:8192\n" \
"ds_read_b64_tr_b16 v[138:139], v34 offset:9216\n" \
"v_mfma_f32_16x16x32_bf16 a[172:175], v[140:143], v[92:95], a[172:175]\n" \
"v_add_u32_e64 v246, 16, v214\n" \
"v_mov_b32_e32 v247, 0xffff\n" \
"v_mfma_f32_16x16x32_bf16 a[172:175], v[144:147], v[96:99], a[172:175]\n" \
"s_add_u32 m0, %[mB], 0xc400\n" \
"s_add_u32 s84, s63, 0x8000\n" \
"v_mfma_f32_16x16x32_bf16 a[204:207], v[144:147], v[104:107], a[204:207]\n" \
"buffer_load_dwordx4 v26, s[72:75], s84 offen lds\n" \
"v_mfma_f32_16x16x32_bf16 a[204:207], v[140:143], v[100:103], a[204:207]\n" \
"v_cmp_le_u32_e32 vcc, v246, v236\n" \
"s_nop 0\n" \
"s_add_u32 s44, 0x4000, s89\n" \
"v_mfma_f32_16x16x32_bf16 a[236:239], v[140:143], v[108:111], a[236:239]\n" \
"ds_read_b64_tr_b16 v[140:141], v35 offset:0\n" \
"ds_read_b64_tr_b16 v[142:143], v35 offset:1024\n" \
"v_mfma_f32_16x16x32_bf16 a[236:239], v[144:147], v[112:115], a[236:239]\n" \
"s_add_u32 m0, %[mA], 0x9000\n" \
"s_add_u32 s85, s62, 0x8000\n" \
"v_mfma_f32_16x16x32_bf16 a[144:147], v[88:91], v[152:155], a[144:147]\n" \
"buffer_load_dwordx4 v22, s[64:67], s85 offen lds\n" \
"v_mfma_f32_16x16x32_bf16 a[144:147], v[84:87], v[148:151], a[144:147]\n" \
"ds_read_b64_tr_b16 v[144:145], v35 offset:8192\n" \
"ds_read_b64_tr_b16 v[146:147], v35 offset:9216\n" \
"v_mfma_f32_16x16x32_bf16 a[176:179], v[92:95], v[148:151], a[176:179]\n" \
"v_cndmask_b32_e32 v248, 0, v247, vcc\n" \
"v_mov_b32_e32 v249, 0xffff0000\n" \
"v_mfma_f32_16x16x32_bf16 a[176:179], v[96:99], v[152:155], a[176:179]\n" \
"s_add_u32 m0, %[mA], 0x9400\n" \
"s_add_u32 s84, s62, 0xa000\n" \
"v_mfma_f32_16x16x32_bf16 a[208:211], v[104:107], v[152:155], a[208:211]\n" \
"buffer_load_dwordx4 v23, s[64:67], s84 offen lds\n" \
"v_mfma_f32_16x16x32_bf16 a[208:211], v[100:103], v[148:151], a[208:211]\n" \
"v_cmp_lt_u32_e32 vcc, v246, v236\n" \
"s_lshl_b32 s89, %[w], 8\n" \
"s_nop 0\n" \
"v_mfma_f32_16x16x32_bf16 a[240:243], v[108:111], v[148:151], a[240:243]\n" \
"ds_read_b64_tr_b16 v[148:149], v32 offset:16384\n" \
"ds_read_b64_tr_b16 v[150:151], v32 offset:17408\n" \
"v_mfma_f32_16x16x32_bf16 a[240:243], v[112:115], v[152:155], a[240:243]\n" \
"s_add_u32 m0, %[mB], 0x8800\n" \
"s_add_u32 s85, s63, 0x10000\n" \
"v_mfma_f32_16x16x32_bf16 a[148:151], v[88:91], v[160:163], a[148:151]\n" \
"buffer_load_dwordx4 v27, s[68:71], s85 offen lds\n" \
"v_mfma_f32_16x16x32_bf16 a[148:151], v[84:87], v[156:159], a[148:151]\n" \
"ds_read_b64_tr_b16 v[152:153], v32 offset:24576\n" \
"ds_read_b64_tr_b16 v[154:155], v32 offset:25600\n" \
"v_mfma_f32_16x16x32_bf16 a[180:183], v[92:95], v[156:159], a[180:183]\n" \
"v_cndmask_b32_e32 v250, 0, v249, vcc\n" \
"v_add_u32_e32 v251, 18, v214\n" \
"v_mfma_f32_16x16x32_bf16 a[180:183], v[96:99], v[160:163], a[180:183]\n" \
"s_add_u32 m0, %[mB], 0xc800\n" \
"s_add_u32 s84, s63, 0x10000\n" \
"v_mfma_f32_16x16x32_bf16 a[212:215], v[104:107], v[160:163], a[212:215]\n" \
"buffer_load_dwordx4 v27, s[72:75], s84 offen lds\n" \
"v_mfma_f32_16x16x32_bf16 a[212:215], v[100:103], v[156:159], a[212:215]\n" \
"s_add_u32 s46, 0x27000, s89\n" \
"v_mov_b32_e32 v218, 0xffff\n" \
"v_mfma_f32_16x16x32_bf16 a[244:247], v[108:111], v[156:159], a[244:247]\n" \
"ds_read_b64_tr_b16 v[156:157], v33 offset:16384\n" \
"ds_read_b64_tr_b16 v[158:159], v33 offset:17408\n" \
"v_mfma_f32_16x16x32_bf16 a[244:247], v[112:115], v[160:163], a[244:247]\n" \
"ds_read_b64_tr_b16 v[160:161], v33 offset:24576\n" \
"ds_read_b64_tr_b16 v[162:163], v33 offset:25600\n" \
"v_mfma_f32_16x16x32_bf16 a[152:155], v[88:91], v[168:171], a[152:155]\n" \
"v_cmp_le_u32_e32 vcc, v251, v236\n" \
"s_add_u32 s89, 0xffffc000, s101\n" \
"v_mfma_f32_16x16x32_bf16 a[152:155], v[84:87], v[164:167], a[152:155]\n" \
"v_cndmask_b32_e32 v219, 0, v218, vcc\n" \
"v_lshlrev_b32_e32 v224, 3, v212\n" \
"v_mfma_f32_16x16x32_bf16 a[184:187], v[92:95], v[164:167], a[184:187]\n" \
"s_cmp_lg_u32 s95, 0\n" \
"s_cselect_b32 s48, s89, s100\n" \
"v_mfma_f32_16x16x32_bf16 a[184:187], v[96:99], v[168:171], a[184:187]\n" \
"s_lshr_b32 s94, %[b], 5\n" \
"s_mov_b32 s96, 0xc000\n" \
"v_mfma_f32_16x16x32_bf16 a[216:219], v[104:107], v[168:171], a[216:219]\n" \
"s_mov_b32 s97, 0x8000\n" \
"v_mov_b32_e32 v221, 0xffff0000\n" \
"v_mfma_f32_16x16x32_bf16 a[216:219], v[100:103], v[164:167], a[216:219]\n" \
"v_cmp_lt_u32_e32 vcc, v251, v236\n" \
"v_lshrrev_b32_e32 v212, 4, v20\n" \
"v_mfma_f32_16x16x32_bf16 a[248:251], v[108:111], v[164:167], a[248:251]\n" \
"ds_read_b64_tr_b16 v[164:165], v34 offset:16384\n" \
"ds_read_b64_tr_b16 v[166:167], v34 offset:17408\n" \
"v_mfma_f32_16x16x32_bf16 a[248:251], v[112:115], v[168:171], a[248:251]\n" \
"ds_read_b64_tr_b16 v[168:169], v34 offset:24576\n" \
"ds_read_b64_tr_b16 v[170:171], v34 offset:25600\n" \
"v_mfma_f32_16x16x32_bf16 a[156:159], v[88:91], v[176:179], a[156:159]\n" \
"ds_read_b128 v[88:91], v29 offset:0\n" \
"v_cndmask_b32_e32 v216, 0, v221, vcc\n" \
"v_mfma_f32_16x16x32_bf16 a[156:159], v[84:87], v[172:175], a[156:159]\n" \
"ds_read_b128 v[84:87], v28 offset:0\n" \
"v_lshlrev_b32_e32 v223, 2, v236\n" \
"v_mfma_f32_16x16x32_bf16 a[188:191], v[92:95], v[172:175], a[188:191]\n" \
"ds_read_b128 v[92:95], v28 offset:2048\n" \
"s_lshl_b32 s101, %[w], 9\n" \
"v_mfma_f32_16x16x32_bf16 a[188:191], v[96:99], v[176:179], a[188:191]\n" \
"ds_read_b128 v[96:99], v29 offset:2048\n" \
"v_mul_u32_u24_e32 v213, 0xe39, v212\n" \
"v_mfma_f32_16x16x32_bf16 a[220:223], v[104:107], v[176:179], a[220:223]\n" \
"ds_read_b128 v[104:107], v29 offset:4096\n" \
"v_or_b32_e32 v15, v219, v216\n" \
"v_mfma_f32_16x16x32_bf16 a[220:223], v[100:103], v[172:175], a[220:223]\n" \
"ds_read_b128 v[100:103], v28 offset:4096\n" \
"v_add_u32_e32 v18, 0x27000, v223\n" \
"v_mfma_f32_16x16x32_bf16 a[252:255], v[108:111], v[172:175], a[252:255]\n" \
"ds_read_b64_tr_b16 v[172:173], v35 offset:16384\n" \
"ds_read_b64_tr_b16 v[174:175], v35 offset:17408\n" \
"v_mfma_f32_16x16x32_bf16 a[252:255], v[112:115], v[176:179], a[252:255]\n" \
"s_add_u32 m0, %[mA], 0x9800\n" \
"s_waitcnt lgkmcnt(12)\n" \
"s_nop 0\n" \
"v_mfma_f32_16x16x32_bf16 a[0:3], v[120:123], v[56:59], a[0:3]\n" \
"s_add_u32 s85, s62, 0xc000\n" \
"ds_read_b64_tr_b16 v[176:177], v35 offset:24576\n" \
"v_mfma_f32_16x16x32_bf16 a[0:3], v[116:119], v[52:55], a[0:3]\n" \
"buffer_load_dwordx4 v22, s[64:67], s85 offen lds\n" \
"v_mfma_f32_16x16x32_bf16 a[32:35], v[116:119], v[60:63], a[32:35]\n" \
"s_add_u32 m0, %[mA], 0x9c00\n" \
"s_add_u32 s84, s62, 0xe000\n" \
"L_pf2:\n" \
"s_cbranch_execz L_pf6\n" \
"s_nop 0\n" \
"v_mfma_f32_16x16x32_bf16 a[32:35], v[120:123], v[64:67], a[32:35]\n" \
"ds_read_b64_tr_b16 v[178:179], v35 offset:25600\n" \
"ds_read_b128 v[108:111], v28 offset:6144\n" \
"v_mfma_f32_16x16x32_bf16 a[64:67], v[120:123], v[72:75], a[64:67]\n" \
"buffer_load_dwordx4 v23, s[64:67], s84 offen lds\n" \
"v_mfma_f32_16x16x32_bf16 a[64:67], v[116:119], v[68:71], a[64:67]\n" \
"s_add_u32 m0, %[mB], 0x8c00\n" \
"s_add_u32 s85, s63, 0x18000\n" \
"v_mfma_f32_16x16x32_bf16 a[96:99], v[116:119], v[76:79], a[96:99]\n" \
"ds_read_b128 v[112:115], v29 offset:6144\n" \
"s_add_u32 s89, 0x27800, s101\n" \
"v_mfma_f32_16x16x32_bf16 a[96:99], v[120:123], v[80:83], a[96:99]\n" \
"buffer_load_dwordx4 v27, s[68:71], s85 offen lds\n" \
"v_mfma_f32_16x16x32_bf16 a[4:7], v[128:131], v[56:59], a[4:7]\n" \
"s_add_u32 m0, %[mB], 0xcc00\n" \
"s_add_u32 s84, s63, 0x18000\n" \
"v_mfma_f32_16x16x32_bf16 a[4:7], v[124:127], v[52:55], a[4:7]\n" \
"s_cmp_lg_u32 s94, 0\n" \
"s_cselect_b32 s53, s96, 0x8000\n" \
"v_mfma_f32_16x16x32_bf16 a[36:39], v[124:127], v[60:63], a[36:39]\n" \
"buffer_load_dwordx4 v27, s[72:75], s84 offen lds\n" \
"v_mfma_f32_16x16x32_bf16 a[36:39], v[128:131], v[64:67], a[36:39]\n" \
"s_cmp_lg_u32 s94, 0\n" \
"s_cselect_b32 s98, s97, 0x4000\n" \
"v_mfma_f32_16x16x32_bf16 a[68:71], v[128:131], v[72:75], a[68:71]\n" \
"v_lshrrev_b32_e32 v214, 16, v213\n" \
"v_add_u32_e32 v222, 64, v212\n" \
"v_mfma_f32_16x16x32_bf16 a[68:71], v[124:127], v[68:71], a[68:71]\n" \
"buffer_store_dwordx4 v[200:203], v20, s[80:83], s57 offen sc1\n" \
"v_mfma_f32_16x16x32_bf16 a[100:103], v[124:127], v[76:79], a[100:103]\n" \
"v_add_u32_e32 v19, s89, v224\n" \
"v_mul_u32_u24_e32 v216, 18, v214\n" \
"v_mfma_f32_16x16x32_bf16 a[100:103], v[128:131], v[80:83], a[100:103]\n" \
"s_and_b32 s94, 7, %[b]\n" \
"v_mul_u32_u24_e32 v223, 0xe39, v222\n" \
"v_mfma_f32_16x16x32_bf16 a[8:11], v[136:139], v[56:59], a[8:11]\n" \
"v_sub_u32_e32 v217, v212, v216\n" \
"s_lshl_b32 s95, s94, 5\n" \
"v_mfma_f32_16x16x32_bf16 a[8:11], v[132:135], v[52:55], a[8:11]\n" \
"v_lshrrev_b32_e32 v224, 16, v223\n" \
"v_add_u32_e32 v233, 0x80, v212\n" \
"v_mfma_f32_16x16x32_bf16 a[40:43], v[132:135], v[60:63], a[40:43]\n" \
"buffer_store_dwordx4 v[204:207], v20, s[80:83], s56 offen sc1\n" \
"v_mfma_f32_16x16x32_bf16 a[40:43], v[136:139], v[64:67], a[40:43]\n" \
"s_add_u32 s96, 32, s95\n" \
"v_lshlrev_b32_e32 v218, 4, v217\n" \
"v_mfma_f32_16x16x32_bf16 a[72:75], v[136:139], v[72:75], a[72:75]\n" \
"v_mul_u32_u24_e32 v226, 18, v224\n" \
"v_mul_u32_u24_e32 v234, 0xe39, v233\n" \
"v_mfma_f32_16x16x32_bf16 a[72:75], v[132:135], v[68:71], a[72:75]\n" \
"v_or_b32_e32 v13, v243, v245\n" \
"v_add_u32_e64 v219, s96, v218\n" \
"v_mfma_f32_16x16x32_bf16 a[104:107], v[132:135], v[76:79], a[104:107]\n" \
"v_sub_u32_e32 v227, v222, v226\n" \
"v_lshrrev_b32_e32 v235, 16, v234\n" \
"v_mfma_f32_16x16x32_bf16 a[104:107], v[136:139], v[80:83], a[104:107]\n" \
"v_add_u32_e32 v244, 0xc0, v212\n" \
"v_or_b32_e32 v12, v238, v240\n" \
"v_mfma_f32_16x16x32_bf16 a[12:15], v[144:147], v[56:59], a[12:15]\n" \
"buffer_store_dwordx4 v[208:211], v20, s[80:83], s58 offen sc1\n" \
"v_mfma_f32_16x16x32_bf16 a[12:15], v[140:143], v[52:55], a[12:15]\n" \
"v_and_b32_e32 v220, 0xff, v219\n" \
"v_cmp_gt_u32_e32 vcc, 16, v217\n" \
"v_mfma_f32_16x16x32_bf16 a[44:47], v[140:143], v[60:63], a[44:47]\n" \
"v_lshlrev_b32_e32 v228, 4, v227\n" \
"v_mul_u32_u24_e32 v237, 18, v235\n" \
"v_mfma_f32_16x16x32_bf16 a[44:47], v[144:147], v[64:67], a[44:47]\n" \
"v_mul_u32_u24_e32 v245, 0xe39, v244\n" \
"v_or_b32_e32 v14, v248, v250\n" \
"v_mfma_f32_16x16x32_bf16 a[76:79], v[144:147], v[72:75], a[76:79]\n" \
"v_cndmask_b32_e32 v221, v218, v220, vcc\n" \
"v_add_u32_e64 v229, s96, v228\n" \
"v_mfma_f32_16x16x32_bf16 a[76:79], v[140:143], v[68:71], a[76:79]\n" \
"v_sub_u32_e32 v238, v233, v237\n" \
"v_lshrrev_b32_e32 v246, 16, v245\n" \
"v_mfma_f32_16x16x32_bf16 a[108:111], v[140:143], v[76:79], a[108:111]\n" \
"s_mov_b32 m0, %[mS]\n" \
"s_mov_b32 s86, s10\n" \
"v_mfma_f32_16x16x32_bf16 a[108:111], v[144:147], v[80:83], a[108:111]\n" \
"s_mov_b32 s87, s11\n" \
"v_add_u32_e32 v219, 0x100, v212\n" \
"v_mfma_f32_16x16x32_bf16 a[16:19], v[56:59], v[152:155], a[16:19]\n" \
"v_mul_u32_u24_e32 v215, 0x480, v214\n" \
"v_and_b32_e32 v230, 0xff, v229\n" \
"v_mfma_f32_16x16x32_bf16 a[16:19], v[52:55], v[148:151], a[16:19]\n" \
"v_cmp_gt_u32_e32 vcc, 16, v227\n" \
"v_lshlrev_b32_e32 v239, 4, v238\n" \
"v_mfma_f32_16x16x32_bf16 a[48:51], v[60:63], v[148:151], a[48:51]\n" \
"v_mul_u32_u24_e32 v248, 18, v246\n" \
"v_mul_u32_u24_e32 v217, 0xe39, v219\n" \
"v_mfma_f32_16x16x32_bf16 a[48:51], v[64:67], v[152:155], a[48:51]\n" \
"v_add_u32_e32 v0, v215, v221\n" \
"v_cndmask_b32_e32 v231, v228, v230, vcc\n" \
"v_mfma_f32_16x16x32_bf16 a[80:83], v[72:75], v[152:155], a[80:83]\n" \
"v_add_u32_e32 v240, s96, v239\n" \
"v_sub_u32_e32 v249, v244, v248\n" \
"s_mov_b32 exec_lo, 0xfffc0000\n" \
"s_mov_b32 exec_hi, 0xf\n" \
"global_load_lds_dword v188, s[86:87]\n" \
"s_mov_b64 exec, -1\n" \
"v_mfma_f32_16x16x32_bf16 a[80:83], v[68:71], v[148:151], a[80:83]\n" \
"v_lshrrev_b32_e32 v220, 16, v217\n" \
"v_add_u32_e32 v229, 0x140, v212\n" \
"v_mfma_f32_16x16x32_bf16 a[112:115], v[76:79], v[148:151], a[112:115]\n" \
"v_mul_u32_u24_e32 v225, 0x480, v224\n" \
"v_and_b32_e32 v241, 0xff, v240\n" \
"v_mfma_f32_16x16x32_bf16 a[112:115], v[80:83], v[152:155], a[112:115]\n" \
"v_cmp_gt_u32_e32 vcc, 16, v238\n" \
"v_lshlrev_b32_e32 v250, 4, v249\n" \
"v_mfma_f32_16x16x32_bf16 a[20:23], v[56:59], v[160:163], a[20:23]\n" \
"v_mul_u32_u24_e32 v215, 18, v220\n" \
"v_mul_u32_u24_e32 v227, 0xe39, v229\n" \
"v_mfma_f32_16x16x32_bf16 a[20:23], v[52:55], v[156:159], a[20:23]\n" \
"v_add_u32_e32 v232, v225, v231\n" \
"v_cndmask_b32_e32 v242, v239, v241, vcc\n" \
"v_mfma_f32_16x16x32_bf16 a[52:55], v[60:63], v[156:159], a[52:55]\n" \
"v_add_u32_e32 v251, s96, v250\n" \
"v_sub_u32_e32 v221, v219, v215\n" \
"v_mfma_f32_16x16x32_bf16 a[52:55], v[64:67], v[160:163], a[52:55]\n" \
"v_lshrrev_b32_e32 v230, 16, v227\n" \
"v_add_u32_e32 v240, 0x180, v212\n" \
"v_mfma_f32_16x16x32_bf16 a[84:87], v[72:75], v[160:163], a[84:87]\n" \
"v_mul_u32_u24_e32 v236, 0x480, v235\n" \
"v_and_b32_e32 v213, 0xff, v251\n" \
"v_mfma_f32_16x16x32_bf16 a[84:87], v[68:71], v[156:159], a[84:87]\n" \
"v_cmp_gt_u32_e32 vcc, 16, v249\n" \
"v_lshlrev_b32_e32 v223, 4, v221\n" \
"v_mfma_f32_16x16x32_bf16 a[116:119], v[76:79], v[156:159], a[116:119]\n" \
"v_mul_u32_u24_e32 v225, 18, v230\n" \
"v_mul_u32_u24_e32 v238, 0xe39, v240\n" \
"v_mfma_f32_16x16x32_bf16 a[116:119], v[80:83], v[160:163], a[116:119]\n" \
"v_add_u32_e32 v1, 0xfffffc00, v232\n" \
"s_waitcnt lgkmcnt(0)\n" \
"v_mfma_f32_16x16x32_bf16 a[24:27], v[56:59], v[168:171], a[24:27]\n" \
"v_add_u32_e32 v243, v236, v242\n" \
"v_cndmask_b32_e32 v214, v250, v213, vcc\n" \
"v_mfma_f32_16x16x32_bf16 a[24:27], v[52:55], v[164:167], a[24:27]\n" \
"v_add_u32_e32 v224, s96, v223\n" \
"v_sub_u32_e32 v231, v229, v225\n" \
"v_mfma_f32_16x16x32_bf16 a[56:59], v[60:63], v[164:167], a[56:59]\n" \
"v_lshrrev_b32_e32 v241, 16, v238\n" \
"v_add_u32_e32 v251, 0x1c0, v212\n" \
"v_mfma_f32_16x16x32_bf16 a[56:59], v[64:67], v[168:171], a[56:59]\n" \
"v_mul_u32_u24_e32 v247, 0x480, v246\n" \
"v_and_b32_e32 v222, 0xff, v224\n" \
"v_mfma_f32_16x16x32_bf16 a[88:91], v[72:75], v[168:171], a[88:91]\n" \
"v_cmp_gt_u32_e32 vcc, 16, v221\n" \
"v_lshlrev_b32_e32 v232, 4, v231\n" \
"v_mfma_f32_16x16x32_bf16 a[88:91], v[68:71], v[164:167], a[88:91]\n" \
"v_mul_u32_u24_e32 v236, 18, v241\n" \
"v_mul_u32_u24_e32 v249, 0xe39, v251\n" \
"v_mfma_f32_16x16x32_bf16 a[120:123], v[76:79], v[164:167], a[120:123]\n" \
"v_add_u32_e32 v2, 0xfffff800, v243\n" \
"v_add_u32_e32 v216, v247, v214\n" \
"v_mfma_f32_16x16x32_bf16 a[120:123], v[80:83], v[168:171], a[120:123]\n" \
"v_cndmask_b32_e32 v226, v223, v222, vcc\n" \
"v_add_u32_e32 v234, s96, v232\n" \
"v_mfma_f32_16x16x32_bf16 a[28:31], v[56:59], v[176:179], a[28:31]\n" \
"s_add_u32 s62, s22, 0x300\n" \
"s_and_b32 s62, s62, 0x3ff\n" \
"v_mfma_f32_16x16x32_bf16 a[28:31], v[52:55], v[172:175], a[28:31]\n" \
"s_lshl_b32 s63, s62, 12\n" \
"v_sub_u32_e32 v242, v240, v236\n" \
"v_mfma_f32_16x16x32_bf16 a[60:63], v[60:63], v[172:175], a[60:63]\n" \
"v_lshrrev_b32_e32 v213, 16, v249\n" \
"v_add_u32_e32 v224, 0x200, v212\n" \
"v_mfma_f32_16x16x32_bf16 a[60:63], v[64:67], v[176:179], a[60:63]\n" \
"v_mul_u32_u24_e32 v218, 0x480, v220\n" \
"v_and_b32_e32 v235, 0xff, v234\n" \
"v_mfma_f32_16x16x32_bf16 a[92:95], v[72:75], v[176:179], a[92:95]\n" \
"v_cmp_gt_u32_e32 vcc, 16, v231\n" \
"v_lshlrev_b32_e32 v243, 4, v242\n" \
"v_mfma_f32_16x16x32_bf16 a[92:95], v[68:71], v[172:175], a[92:95]\n" \
"v_mul_u32_u24_e32 v247, 18, v213\n" \
"v_mul_u32_u24_e32 v221, 0xe39, v224\n" \
"v_mfma_f32_16x16x32_bf16 a[124:127], v[76:79], v[172:175], a[124:127]\n" \
"v_add_u32_e32 v3, 0xfffff400, v216\n" \
"v_add_u32_e64 v4, v218, v226\n" \
"v_mfma_f32_16x16x32_bf16 a[124:127], v[80:83], v[176:179], a[124:127]\n" \
"v_cndmask_b32_e32 v233, v232, v235, vcc\n" \
"v_add_u32_e32 v245, s96, v243\n" \
"s_waitcnt vmcnt(4)\n" \
"s_barrier\n" \
"v_mfma_f32_16x16x32_bf16 a[144:147], v[88:91], v[152:155], a[144:147]\n" \
"s_add_u32 m0, %[mA], 0x0\n" \
"s_add_u32 s85, s62, 0x0\n" \
"v_mfma_f32_16x16x32_bf16 a[144:147], v[84:87], v[148:151], a[144:147]\n" \
"buffer_load_dwordx4 v22, s[64:67], s85 offen lds\n" \
"v_mfma_f32_16x16x32_bf16 a[176:179], v[92:95], v[148:151], a[176:179]\n" \
"ds_read_b128 v[56:59], v29 offset:49152\n" \
"ds_read_b128 v[52:55], v28 offset:49152\n" \
"v_mfma_f32_16x16x32_bf16 a[176:179], v[96:99], v[152:155], a[176:179]\n" \
"s_add_u32 m0, %[mA], 0x400\n" \
"s_add_u32 s84, s62, 0x2000\n" \
"v_mfma_f32_16x16x32_bf16 a[208:211], v[104:107], v[152:155], a[208:211]\n" \
"buffer_load_dwordx4 v23, s[64:67], s84 offen lds\n" \
"v_mfma_f32_16x16x32_bf16 a[208:211], v[100:103], v[148:151], a[208:211]\n" \
"ds_read_b128 v[60:63], v28 offset:51200\n" \
"ds_read_b128 v[64:67], v29 offset:51200\n" \
"v_mfma_f32_16x16x32_bf16 a[240:243], v[108:111], v[148:151], a[240:243]\n" \
"ds_read_b64_tr_b16 v[148:149], v32 offset:49152\n" \
"ds_read_b64_tr_b16 v[150:151], v32 offset:50176\n" \
"v_mfma_f32_16x16x32_bf16 a[240:243], v[112:115], v[152:155], a[240:243]\n" \
"s_add_u32 m0, %[mB], 0x0\n" \
"s_add_u32 s85, s63, 0x0\n" \
"v_mfma_f32_16x16x32_bf16 a[148:151], v[88:91], v[160:163], a[148:151]\n" \
"buffer_load_dwordx4 v26, s[68:71], s85 offen lds\n" \
"v_mfma_f32_16x16x32_bf16 a[148:151], v[84:87], v[156:159], a[148:151]\n" \
"ds_read_b64_tr_b16 v[152:153], v32 offset:57344\n" \
"ds_read_b64_tr_b16 v[154:155], v32 offset:58368\n" \
"v_mfma_f32_16x16x32_bf16 a[180:183], v[92:95], v[156:159], a[180:183]\n" \
"ds_read_b128 v[72:75], v29 offset:53248\n" \
"ds_read_b128 v[68:71], v28 offset:53248\n" \
"v_mfma_f32_16x16x32_bf16 a[180:183], v[96:99], v[160:163], a[180:183]\n" \
"s_add_u32 m0, %[mB], 0x4000\n" \
"s_add_u32 s84, s63, 0x0\n" \
"s_nop 0\n" \
"v_mfma_f32_16x16x32_bf16 a[212:215], v[104:107], v[160:163], a[212:215]\n" \
"buffer_load_dwordx4 v26, s[72:75], s84 offen lds\n" \
"v_mfma_f32_16x16x32_bf16 a[212:215], v[100:103], v[156:159], a[212:215]\n" \
"ds_read_b128 v[76:79], v28 offset:55296\n" \
"ds_read_b128 v[80:83], v29 offset:55296\n" \
"v_mfma_f32_16x16x32_bf16 a[244:247], v[108:111], v[156:159], a[244:247]\n" \
"ds_read_b64_tr_b16 v[156:157], v33 offset:49152\n" \
"ds_read_b64_tr_b16 v[158:159], v33 offset:50176\n" \
"v_mfma_f32_16x16x32_bf16 a[244:247], v[112:115], v[160:163], a[244:247]\n" \
"s_add_u32 m0, %[mA], 0x800\n" \
"s_add_u32 s85, s62, 0x4000\n" \
"v_mfma_f32_16x16x32_bf16 a[152:155], v[88:91], v[168:171], a[152:155]\n" \
"buffer_load_dwordx4 v22, s[64:67], s85 offen lds\n" \
"v_mfma_f32_16x16x32_bf16 a[152:155], v[84:87], v[164:167], a[152:155]\n" \
"ds_read_b64_tr_b16 v[160:161], v33 offset:57344\n" \
"ds_read_b64_tr_b16 v[162:163], v33 offset:58368\n" \
"v_mfma_f32_16x16x32_bf16 a[184:187], v[92:95], v[164:167], a[184:187]\n" \
"v_sub_u32_e32 v214, v251, v247\n" \
"v_lshrrev_b32_e32 v222, 16, v221\n" \
"v_mfma_f32_16x16x32_bf16 a[184:187], v[96:99], v[168:171], a[184:187]\n" \
"s_add_u32 m0, %[mA], 0xc00\n" \
"s_add_u32 s84, s62, 0x6000\n" \
"v_mfma_f32_16x16x32_bf16 a[216:219], v[104:107], v[168:171], a[216:219]\n" \
"buffer_load_dwordx4 v23, s[64:67], s84 offen lds\n" \
"v_mfma_f32_16x16x32_bf16 a[216:219], v[100:103], v[164:167], a[216:219]\n" \
"v_and_b32_e32 v246, 0xff, v245\n" \
"v_cmp_gt_u32_e32 vcc, 16, v242\n" \
"s_nop 0\n" \
"v_mfma_f32_16x16x32_bf16 a[248:251], v[108:111], v[164:167], a[248:251]\n" \
"ds_read_b64_tr_b16 v[164:165], v34 offset:49152\n" \
"ds_read_b64_tr_b16 v[166:167], v34 offset:50176\n" \
"v_mfma_f32_16x16x32_bf16 a[248:251], v[112:115], v[168:171], a[248:251]\n" \
"s_add_u32 m0, %[mB], 0x400\n" \
"s_add_u32 s85, s63, 0x8000\n" \
"v_mfma_f32_16x16x32_bf16 a[156:159], v[88:91], v[176:179], a[156:159]\n" \
"buffer_load_dwordx4 v26, s[68:71], s85 offen lds\n" \
"v_mfma_f32_16x16x32_bf16 a[156:159], v[84:87], v[172:175], a[156:159]\n" \
"ds_read_b64_tr_b16 v[168:169], v34 offset:57344\n" \
"ds_read_b64_tr_b16 v[170:171], v34 offset:58368\n" \
"v_mfma_f32_16x16x32_bf16 a[188:191], v[92:95], v[172:175], a[188:191]\n" \
"v_lshlrev_b32_e32 v216, 4, v214\n" \
"v_mul_u32_u24_e32 v218, 18, v222\n" \
"v_mfma_f32_16x16x32_bf16 a[188:191], v[96:99], v[176:179], a[188:191]\n" \
"s_add_u32 m0, %[mB], 0x4400\n" \
"s_add_u32 s84, s63, 0x8000\n" \
"v_mfma_f32_16x16x32_bf16 a[220:223], v[104:107], v[176:179], a[220:223]\n" \
"buffer_load_dwordx4 v26, s[72:75], s84 offen lds\n" \
"v_mfma_f32_16x16x32_bf16 a[220:223], v[100:103], v[172:175], a[220:223]\n" \
"v_cndmask_b32_e32 v244, v243, v246, vcc\n" \
"v_add_u32_e32 v217, s96, v216\n" \
"v_mfma_f32_16x16x32_bf16 a[252:255], v[108:111], v[172:175], a[252:255]\n" \
"ds_read_b64_tr_b16 v[172:173], v35 offset:49152\n" \
"ds_read_b64_tr_b16 v[174:175], v35 offset:50176\n" \
"v_mfma_f32_16x16x32_bf16 a[252:255], v[112:115], v[176:179], a[252:255]\n" \
"s_add_u32 m0, %[mA], 0x1000\n" \
"s_add_u32 s85, s62, 0x8000\n" \
"v_mfma_f32_16x16x32_bf16 a[128:131], v[120:123], v[88:91], a[128:131]\n" \
"buffer_load_dwordx4 v22, s[64:67], s85 offen lds\n" \
"v_mfma_f32_16x16x32_bf16 a[128:131], v[116:119], v[84:87], a[128:131]\n" \
"ds_read_b64_tr_b16 v[176:177], v35 offset:57344\n" \
"ds_read_b64_tr_b16 v[178:179], v35 offset:58368\n" \
"v_mfma_f32_16x16x32_bf16 a[160:163], v[116:119], v[92:95], a[160:163]\n" \
"v_sub_u32_e32 v226, v224, v218\n" \
"v_mul_u32_u24_e32 v228, 0x480, v230\n" \
"v_mfma_f32_16x16x32_bf16 a[160:163], v[120:123], v[96:99], a[160:163]\n" \
"s_add_u32 m0, %[mA], 0x1400\n" \
"s_add_u32 s84, s62, 0xa000\n" \
"v_mfma_f32_16x16x32_bf16 a[192:195], v[120:123], v[104:107], a[192:195]\n" \
"buffer_load_dwordx4 v23, s[64:67], s84 offen lds\n" \
"v_mfma_f32_16x16x32_bf16 a[192:195], v[116:119], v[100:103], a[192:195]\n" \
"v_and_b32_e32 v220, 0xff, v217\n" \
"v_cmp_gt_u32_e32 vcc, 16, v214\n" \
"v_mfma_f32_16x16x32_bf16 a[224:227], v[116:119], v[108:111], a[224:227]\n" \
"ds_read_b64_tr_b16 v[116:117], v32 offset:32768\n" \
"ds_read_b64_tr_b16 v[118:119], v32 offset:33792\n" \
"v_mfma_f32_16x16x32_bf16 a[224:227], v[120:123], v[112:115], a[224:227]\n" \
"s_add_u32 m0, %[mB], 0x800\n" \
"s_add_u32 s85, s63, 0x10000\n" \
"v_mfma_f32_16x16x32_bf16 a[132:135], v[128:131], v[88:91], a[132:135]\n" \
"buffer_load_dwordx4 v27, s[68:71], s85 offen lds\n" \
"v_mfma_f32_16x16x32_bf16 a[132:135], v[124:127], v[84:87], a[132:135]\n" \
"ds_read_b64_tr_b16 v[120:121], v32 offset:40960\n" \
"ds_read_b64_tr_b16 v[122:123], v32 offset:41984\n" \
"v_mfma_f32_16x16x32_bf16 a[164:167], v[124:127], v[92:95], a[164:167]\n" \
"v_lshlrev_b32_e32 v227, 4, v226\n" \
"v_cndmask_b32_e32 v219, v216, v220, vcc\n" \
"v_mfma_f32_16x16x32_bf16 a[164:167], v[128:131], v[96:99], a[164:167]\n" \
"s_add_u32 m0, %[mB], 0x4800\n" \
"s_add_u32 s84, s63, 0x10000\n" \
"v_mfma_f32_16x16x32_bf16 a[196:199], v[128:131], v[104:107], a[196:199]\n" \
"buffer_load_dwordx4 v27, s[72:75], s84 offen lds\n" \
"v_mfma_f32_16x16x32_bf16 a[196:199], v[124:127], v[100:103], a[196:199]\n" \
"v_add_u32_e64 v230, s96, v227\n" \
"v_and_b32_e32 v229, 0xff, v230\n" \
"v_mfma_f32_16x16x32_bf16 a[228:231], v[124:127], v[108:111], a[228:231]\n" \
"ds_read_b64_tr_b16 v[124:125], v33 offset:32768\n" \
"ds_read_b64_tr_b16 v[126:127], v33 offset:33792\n" \
"v_mfma_f32_16x16x32_bf16 a[228:231], v[128:131], v[112:115], a[228:231]\n" \
"ds_read_b64_tr_b16 v[128:129], v33 offset:40960\n" \
"ds_read_b64_tr_b16 v[130:131], v33 offset:41984\n" \
"v_mfma_f32_16x16x32_bf16 a[136:139], v[136:139], v[88:91], a[136:139]\n" \
"v_cmp_gt_u32_e32 vcc, 16, v226\n" \
"v_mul_u32_u24_e32 v250, 0x480, v213\n" \
"v_mfma_f32_16x16x32_bf16 a[136:139], v[132:135], v[84:87], a[136:139]\n" \
"v_cndmask_b32_e32 v225, v227, v229, vcc\n" \
"v_and_b32_e64 v249, 2, v212\n" \
"v_mfma_f32_16x16x32_bf16 a[168:171], v[132:135], v[92:95], a[168:171]\n" \
"v_mov_b32_e32 v213, 0x100\n" \
"v_mov_b32_e64 v251, s95\n" \
"v_mfma_f32_16x16x32_bf16 a[168:171], v[136:139], v[96:99], a[168:171]\n" \
"v_cmp_ne_u32_e32 vcc, 0, v249\n" \
"s_mov_b32 s76, s8\n" \
"v_mfma_f32_16x16x32_bf16 a[200:203], v[136:139], v[104:107], a[200:203]\n" \
"v_cndmask_b32_e32 v247, v251, v213, vcc\n" \
"s_and_b32 s77, s9, 0xffff\n" \
"v_mfma_f32_16x16x32_bf16 a[200:203], v[132:135], v[100:103], a[200:203]\n" \
"s_mov_b32 s78, 0x240000\n" \
"v_xor_b32_e32 v37, 0x20, v190\n" \
"v_mfma_f32_16x16x32_bf16 a[232:235], v[132:135], v[108:111], a[232:235]\n" \
"ds_read_b64_tr_b16 v[132:133], v34 offset:32768\n" \
"ds_read_b64_tr_b16 v[134:135], v34 offset:33792\n" \
"v_mfma_f32_16x16x32_bf16 a[232:235], v[136:139], v[112:115], a[232:235]\n" \
"ds_read_b64_tr_b16 v[136:137], v34 offset:40960\n" \
"ds_read_b64_tr_b16 v[138:139], v34 offset:41984\n" \
"v_mfma_f32_16x16x32_bf16 a[140:143], v[144:147], v[88:91], a[140:143]\n" \
"ds_read_b128 v[88:91], v29 offset:32768\n" \
"v_xor_b32_e64 v38, 0x40, v190\n" \
"v_mfma_f32_16x16x32_bf16 a[140:143], v[140:143], v[84:87], a[140:143]\n" \
"ds_read_b128 v[84:87], v28 offset:32768\n" \
"v_add_u32_e64 v48, s60, v28\n" \
"v_mfma_f32_16x16x32_bf16 a[172:175], v[140:143], v[92:95], a[172:175]\n" \
"ds_read_b128 v[92:95], v28 offset:34816\n" \
"v_add_u32_e64 v49, s60, v29\n" \
"v_mfma_f32_16x16x32_bf16 a[172:175], v[144:147], v[96:99], a[172:175]\n" \
"ds_read_b128 v[96:99], v29 offset:34816\n" \
"v_xor_b32_e64 v36, 0x0, v190\n" \
"v_mfma_f32_16x16x32_bf16 a[204:207], v[144:147], v[104:107], a[204:207]\n" \
"ds_read_b128 v[104:107], v29 offset:36864\n" \
"v_add_u32_e64 v237, v228, v233\n" \
"v_mfma_f32_16x16x32_bf16 a[204:207], v[140:143], v[100:103], a[204:207]\n" \
"ds_read_b128 v[100:103], v28 offset:36864\n" \
"s_lshl_b32 s97, %[w], 4\n" \
"v_mfma_f32_16x16x32_bf16 a[236:239], v[140:143], v[108:111], a[236:239]\n" \
"ds_read_b64_tr_b16 v[140:141], v35 offset:32768\n" \
"ds_read_b64_tr_b16 v[142:143], v35 offset:33792\n" \
"v_mfma_f32_16x16x32_bf16 a[236:239], v[144:147], v[112:115], a[236:239]\n" \
"s_add_u32 m0, %[mA], 0x1800\n" \
"s_waitcnt lgkmcnt(12)\n" \
"v_mfma_f32_16x16x32_bf16 a[16:19], v[56:59], v[152:155], a[16:19]\n" \
"s_add_u32 s85, s62, 0xc000\n" \
"ds_read_b64_tr_b16 v[144:145], v35 offset:40960\n" \
"v_mfma_f32_16x16x32_bf16 a[16:19], v[52:55], v[148:151], a[16:19]\n" \
"buffer_load_dwordx4 v22, s[64:67], s85 offen lds\n" \
"v_mfma_f32_16x16x32_bf16 a[48:51], v[60:63], v[148:151], a[48:51]\n" \
"s_add_u32 m0, %[mA], 0x1c00\n" \
"s_add_u32 s84, s62, 0xe000\n" \
"v_mfma_f32_16x16x32_bf16 a[48:51], v[64:67], v[152:155], a[48:51]\n" \
"ds_read_b64_tr_b16 v[146:147], v35 offset:41984\n" \
"ds_read_b128 v[108:111], v28 offset:38912\n" \
"v_mfma_f32_16x16x32_bf16 a[80:83], v[72:75], v[152:155], a[80:83]\n" \
"buffer_load_dwordx4 v23, s[64:67], s84 offen lds\n" \
"v_mfma_f32_16x16x32_bf16 a[80:83], v[68:71], v[148:151], a[80:83]\n" \
"s_add_u32 m0, %[mB], 0xc00\n" \
"s_add_u32 s85, s63, 0x18000\n" \
"v_mfma_f32_16x16x32_bf16 a[112:115], v[76:79], v[148:151], a[112:115]\n" \
"ds_read_b128 v[112:115], v29 offset:38912\n" \
"v_lshrrev_b32_e32 v234, 2, v212\n" \
"v_mfma_f32_16x16x32_bf16 a[112:115], v[80:83], v[152:155], a[112:115]\n" \
"buffer_load_dwordx4 v27, s[68:71], s85 offen lds\n" \
"v_mfma_f32_16x16x32_bf16 a[20:23], v[56:59], v[160:163], a[20:23]\n" \
"s_add_u32 m0, %[mB], 0x4c00\n" \
"s_add_u32 s84, s63, 0x18000\n" \
"v_mfma_f32_16x16x32_bf16 a[20:23], v[52:55], v[156:159], a[20:23]\n" \
"v_add_u32_e32 v5, 0xfffffc00, v237\n" \
"v_add_u32_e32 v231, s97, v234\n" \
"v_mfma_f32_16x16x32_bf16 a[52:55], v[60:63], v[156:159], a[52:55]\n" \
"buffer_load_dwordx4 v27, s[72:75], s84 offen lds\n" \
"v_mfma_f32_16x16x32_bf16 a[52:55], v[64:67], v[160:163], a[52:55]\n" \
"v_lshrrev_b32_e32 v235, 5, v231\n" \
"v_lshrrev_b32_e32 v237, 4, v231\n" \
"v_mfma_f32_16x16x32_bf16 a[84:87], v[72:75], v[160:163], a[84:87]\n" \
"v_mul_u32_u24_e32 v239, 0x480, v241\n" \
"v_lshlrev_b32_e32 v232, 4, v235\n" \
"v_mfma_f32_16x16x32_bf16 a[84:87], v[68:71], v[156:159], a[84:87]\n" \
"v_and_b32_e32 v228, 12, v231\n" \
"v_and_b32_e64 v238, 1, v237\n" \
"v_mfma_f32_16x16x32_bf16 a[116:119], v[76:79], v[156:159], a[116:119]\n" \
"v_add_u32_e32 v233, v232, v228\n" \
"v_lshlrev_b32_e32 v241, 1, v238\n" \
"v_mfma_f32_16x16x32_bf16 a[116:119], v[80:83], v[160:163], a[116:119]\n" \
"s_mov_b32 m0, %[mS]\n" \
"s_mov_b32 s86, s8\n" \
"v_mfma_f32_16x16x32_bf16 a[24:27], v[56:59], v[168:171], a[24:27]\n" \
"s_mov_b32 s87, s9\n" \
"v_lshrrev_b32_e32 v236, 1, v231\n" \
"v_mfma_f32_16x16x32_bf16 a[24:27], v[52:55], v[164:167], a[24:27]\n" \
"v_add_u32_e32 v240, v233, v241\n" \
"v_and_b32_e32 v245, 1, v236\n" \
"v_mfma_f32_16x16x32_bf16 a[56:59], v[60:63], v[164:167], a[56:59]\n" \
"v_add_u32_e32 v242, v240, v245\n" \
"v_add_u32_e32 v248, v239, v244\n" \
"v_mfma_f32_16x16x32_bf16 a[56:59], v[64:67], v[168:171], a[56:59]\n" \
"v_mul_u32_u24_e32 v246, 18, v242\n" \
"v_add_u32_e32 v243, 16, v246\n" \
"v_mfma_f32_16x16x32_bf16 a[88:91], v[72:75], v[168:171], a[88:91]\n" \
"v_and_b32_e64 v239, 1, v231\n" \
"v_add_u32_e32 v6, 0xfffff800, v248\n" \
"v_mfma_f32_16x16x32_bf16 a[88:91], v[68:71], v[164:167], a[88:91]\n" \
"v_add_u32_e64 v244, v243, v239\n" \
"v_mul_u32_u24_e32 v248, 0x480, v244\n" \
"s_mov_b32 exec_lo, 0x0\n" \
"s_mov_b32 exec_hi, 0x3ffff0\n" \
"global_load_lds_dword v188, s[86:87]\n" \
"s_mov_b64 exec, -1\n" \
"v_mfma_f32_16x16x32_bf16 a[120:123], v[76:79], v[164:167], a[120:123]\n" \
"v_add_u32_e32 v215, v250, v219\n" \
"v_add_u32_e32 v217, v248, v247\n" \
"v_mfma_f32_16x16x32_bf16 a[120:123], v[80:83], v[168:171], a[120:123]\n" \
"v_and_b32_e64 v214, 16, v20\n" \
"v_add_u32_e32 v7, 0xfffff400, v215\n" \
"v_mfma_f32_16x16x32_bf16 a[28:31], v[56:59], v[176:179], a[28:31]\n" \
"v_add_u32_e32 v9, v217, v214\n" \
"v_and_b32_e32 v216, 3, v212\n" \
"v_mfma_f32_16x16x32_bf16 a[28:31], v[52:55], v[172:175], a[28:31]\n" \
"v_lshlrev_b32_e32 v250, 3, v216\n" \
"v_mul_u32_u24_e32 v220, 0x120, v234\n" \
"v_mfma_f32_16x16x32_bf16 a[60:63], v[60:63], v[172:175], a[60:63]\n" \
"v_mul_u32_u24_e32 v223, 0x480, v222\n" \
"v_add_u32_e32 v10, v220, v250\n" \
"v_mfma_f32_16x16x32_bf16 a[60:63], v[64:67], v[176:179], a[60:63]\n" \
"v_add_u32_e32 v8, v223, v225\n" \
"s_lshl_b32 s99, %[w], 11\n" \
"v_mfma_f32_16x16x32_bf16 a[92:95], v[72:75], v[176:179], a[92:95]\n" \
"s_add_u32 s55, s98, s99\n" \
"s_nop 0\n" \
"v_mfma_f32_16x16x32_bf16 a[92:95], v[68:71], v[172:175], a[92:95]\n" \
"v_mfma_f32_16x16x32_bf16 a[124:127], v[76:79], v[172:175], a[124:127]\n" \
"v_mfma_f32_16x16x32_bf16 a[124:127], v[80:83], v[176:179], a[124:127]\n" \
"v_mfma_f32_16x16x32_bf16 a[0:3], v[120:123], v[56:59], a[0:3]\n" \
"v_mfma_f32_16x16x32_bf16 a[0:3], v[116:119], v[52:55], a[0:3]\n" \
"v_mfma_f32_16x16x32_bf16 a[32:35], v[116:119], v[60:63], a[32:35]\n" \
"v_mfma_f32_16x16x32_bf16 a[32:35], v[120:123], v[64:67], a[32:35]\n" \
"v_mfma_f32_16x16x32_bf16 a[64:67], v[120:123], v[72:75], a[64:67]\n" \
"v_mfma_f32_16x16x32_bf16 a[64:67], v[116:119], v[68:71], a[64:67]\n" \
"v_mfma_f32_16x16x32_bf16 a[96:99], v[116:119], v[76:79], a[96:99]\n" \
"v_mfma_f32_16x16x32_bf16 a[96:99], v[120:123], v[80:83], a[96:99]\n" \
"v_mfma_f32_16x16x32_bf16 a[4:7], v[128:131], v[56:59], a[4:7]\n" \
"v_mfma_f32_16x16x32_bf16 a[4:7], v[124:127], v[52:55], a[4:7]\n" \
"v_mfma_f32_16x16x32_bf16 a[36:39], v[124:127], v[60:63], a[36:39]\n" \
"v_mfma_f32_16x16x32_bf16 a[36:39], v[128:131], v[64:67], a[36:39]\n" \
"v_mfma_f32_16x16x32_bf16 a[68:71], v[128:131], v[72:75], a[68:71]\n" \
"v_mfma_f32_16x16x32_bf16 a[68:71], v[124:127], v[68:71], a[68:71]\n" \
"v_mfma_f32_16x16x32_bf16 a[100:103], v[124:127], v[76:79], a[100:103]\n" \
"v_mfma_f32_16x16x32_bf16 a[100:103], v[128:131], v[80:83], a[100:103]\n" \
"s_waitcnt lgkmcnt(0)\n" \
"s_nop 0\n" \
"v_mfma_f32_16x16x32_bf16 a[8:11], v[136:139], v[56:59], a[8:11]\n" \
"v_mfma_f32_16x16x32_bf16 a[8:11], v[132:135], v[52:55], a[8:11]\n" \
"v_mfma_f32_16x16x32_bf16 a[40:43], v[132:135], v[60:63], a[40:43]\n" \
"v_mfma_f32_16x16x32_bf16 a[40:43], v[136:139], v[64:67], a[40:43]\n" \
"v_mfma_f32_16x16x32_bf16 a[72:75], v[136:139], v[72:75], a[72:75]\n" \
"v_mfma_f32_16x16x32_bf16 a[72:75], v[132:135], v[68:71], a[72:75]\n" \
"v_mfma_f32_16x16x32_bf16 a[104:107], v[132:135], v[76:79], a[104:107]\n" \
"v_mfma_f32_16x16x32_bf16 a[104:107], v[136:139], v[80:83], a[104:107]\n" \
"v_mfma_f32_16x16x32_bf16 a[12:15], v[144:147], v[56:59], a[12:15]\n" \
"s_add_u32 s62, s22, 0x380\n" \
"s_and_b32 s62, s62, 0x3ff\n" \
"v_mfma_f32_16x16x32_bf16 a[12:15], v[140:143], v[52:55], a[12:15]\n" \
"s_lshl_b32 s63, s62, 12\n" \
"v_mfma_f32_16x16x32_bf16 a[44:47], v[140:143], v[60:63], a[44:47]\n" \
"v_mfma_f32_16x16x32_bf16 a[44:47], v[144:147], v[64:67], a[44:47]\n" \
"v_mfma_f32_16x16x32_bf16 a[76:79], v[144:147], v[72:75], a[76:79]\n" \
"v_mfma_f32_16x16x32_bf16 a[76:79], v[140:143], v[68:71], a[76:79]\n" \
"v_mfma_f32_16x16x32_bf16 a[108:111], v[140:143], v[76:79], a[108:111]\n" \
"v_mfma_f32_16x16x32_bf16 a[108:111], v[144:147], v[80:83], a[108:111]\n" \
"s_waitcnt vmcnt(1)\n" \
"s_barrier\n" \
"s_mov_b64 exec, 1\n" \
"buffer_store_dword v45, off, s[80:83], s38 offset:64 sc1\n" \
"s_mov_b64 exec, -1\n" \
"v_mfma_f32_16x16x32_bf16 a[128:131], v[120:123], v[88:91], a[128:131]\n" \
"s_add_u32 m0, %[mA], 0x8000\n" \
"s_add_u32 s85, s62, 0x0\n" \
"v_mfma_f32_16x16x32_bf16 a[128:131], v[116:119], v[84:87], a[128:131]\n" \
"ds_read_b128 v[56:59], v29 offset:16384\n" \
"ds_read_b128 v[52:55], v28 offset:16384\n" \
"v_mfma_f32_16x16x32_bf16 a[160:163], v[116:119], v[92:95], a[160:163]\n" \
"buffer_load_dwordx4 v22, s[64:67], s85 offen lds\n" \
"v_mfma_f32_16x16x32_bf16 a[160:163], v[120:123], v[96:99], a[160:163]\n" \
"s_add_u32 m0, %[mA], 0x8400\n" \
"s_add_u32 s84, s62, 0x2000\n" \
"v_mfma_f32_16x16x32_bf16 a[192:195], v[120:123], v[104:107], a[192:195]\n" \
"ds_read_b128 v[60:63], v28 offset:18432\n" \
"ds_read_b128 v[64:67], v29 offset:18432\n" \
"v_mfma_f32_16x16x32_bf16 a[192:195], v[116:119], v[100:103], a[192:195]\n" \
"buffer_load_dwordx4 v23, s[64:67], s84 offen lds\n" \
"v_mfma_f32_16x16x32_bf16 a[224:227], v[116:119], v[108:111], a[224:227]\n" \
"ds_read_b64_tr_b16 v[116:117], v32 offset:0\n" \
"ds_read_b64_tr_b16 v[118:119], v32 offset:1024\n" \
"v_mfma_f32_16x16x32_bf16 a[224:227], v[120:123], v[112:115], a[224:227]\n" \
"s_add_u32 m0, %[mB], 0x8000\n" \
"s_add_u32 s85, s63, 0x0\n" \
"v_mfma_f32_16x16x32_bf16 a[132:135], v[128:131], v[88:91], a[132:135]\n" \
"buffer_load_dwordx4 v26, s[68:71], s85 offen lds\n" \
"v_mfma_f32_16x16x32_bf16 a[132:135], v[124:127], v[84:87], a[132:135]\n" \
"ds_read_b64_tr_b16 v[120:121], v32 offset:8192\n" \
"ds_read_b64_tr_b16 v[122:123], v32 offset:9216\n" \
"v_mfma_f32_16x16x32_bf16 a[164:167], v[124:127], v[92:95], a[164:167]\n" \
"ds_read_b128 v[72:75], v29 offset:20480\n" \
"ds_read_b128 v[68:71], v28 offset:20480\n" \
"v_mfma_f32_16x16x32_bf16 a[164:167], v[128:131], v[96:99], a[164:167]\n" \
"s_add_u32 m0, %[mB], 0xc000\n" \
"s_add_u32 s84, s63, 0x0\n" \
"v_mfma_f32_16x16x32_bf16 a[196:199], v[128:131], v[104:107], a[196:199]\n" \
"buffer_load_dwordx4 v26, s[72:75], s84 offen lds\n" \
"v_mfma_f32_16x16x32_bf16 a[196:199], v[124:127], v[100:103], a[196:199]\n" \
"ds_read_b128 v[76:79], v28 offset:22528\n" \
"ds_read_b128 v[80:83], v29 offset:22528\n" \
"v_mfma_f32_16x16x32_bf16 a[228:231], v[124:127], v[108:111], a[228:231]\n" \
"ds_read_b64_tr_b16 v[124:125], v33 offset:0\n" \
"ds_read_b64_tr_b16 v[126:127], v33 offset:1024\n" \
"v_mfma_f32_16x16x32_bf16 a[228:231], v[128:131], v[112:115], a[228:231]\n" \
"s_add_u32 m0, %[mA], 0x8800\n" \
"s_add_u32 s85, s62, 0x4000\n" \
"v_mfma_f32_16x16x32_bf16 a[136:139], v[136:139], v[88:91], a[136:139]\n" \
"buffer_load_dwordx4 v22, s[64:67], s85 offen lds\n" \
"v_mfma_f32_16x16x32_bf16 a[136:139], v[132:135], v[84:87], a[136:139]\n" \
"ds_read_b64_tr_b16 v[128:129], v33 offset:8192\n" \
"ds_read_b64_tr_b16 v[130:131], v33 offset:9216\n" \
"v_mfma_f32_16x16x32_bf16 a[168:171], v[132:135], v[92:95], a[168:171]\n" \
"v_mov_b32_e64 v45, %[tag]\n" \
"v_mfma_f32_16x16x32_bf16 a[168:171], v[136:139], v[96:99], a[168:171]\n" \
"s_add_u32 m0, %[mA], 0x8c00\n" \
"s_add_u32 s84, s62, 0x6000\n" \
"v_mfma_f32_16x16x32_bf16 a[200:203], v[136:139], v[104:107], a[200:203]\n" \
"buffer_load_dwordx4 v23, s[64:67], s84 offen lds\n" \
"v_mfma_f32_16x16x32_bf16 a[200:203], v[132:135], v[100:103], a[200:203]\n" \
"v_mfma_f32_16x16x32_bf16 a[232:235], v[132:135], v[108:111], a[232:235]\n" \
"ds_read_b64_tr_b16 v[132:133], v34 offset:0\n" \
"ds_read_b64_tr_b16 v[134:135], v34 offset:1024\n" \
"v_mfma_f32_16x16x32_bf16 a[232:235], v[136:139], v[112:115], a[232:235]\n" \
"s_add_u32 m0, %[mB], 0x8400\n" \
"s_add_u32 s85, s63, 0x8000\n" \
"v_mfma_f32_16x16x32_bf16 a[140:143], v[144:147], v[88:91], a[140:143]\n" \
"buffer_load_dwordx4 v26, s[68:71], s85 offen lds\n" \
"v_mfma_f32_16x16x32_bf16 a[140:143], v[140:143], v[84:87], a[140:143]\n" \
"ds_read_b64_tr_b16 v[136:137], v34 offset:8192\n" \
"ds_read_b64_tr_b16 v[138:139], v34 offset:9216\n" \
"v_mfma_f32_16x16x32_bf16 a[172:175], v[140:143], v[92:95], a[172:175]\n" \
"v_mfma_f32_16x16x32_bf16 a[172:175], v[144:147], v[96:99], a[172:175]\n" \
"s_add_u32 m0, %[mB], 0xc400\n" \
"s_add_u32 s84, s63, 0x8000\n" \
"v_mfma_f32_16x16x32_bf16 a[204:207], v[144:147], v[104:107], a[204:207]\n" \
"buffer_load_dwordx4 v26, s[72:75], s84 offen lds\n" \
"v_mfma_f32_16x16x32_bf16 a[204:207], v[140:143], v[100:103], a[204:207]\n" \
"v_mfma_f32_16x16x32_bf16 a[236:239], v[140:143], v[108:111], a[236:239]\n" \
"ds_read_b64_tr_b16 v[140:141], v35 offset:0\n" \
"ds_read_b64_tr_b16 v[142:143], v35 offset:1024\n" \
"v_mfma_f32_16x16x32_bf16 a[236:239], v[144:147], v[112:115], a[236:239]\n" \
"s_add_u32 m0, %[mA], 0x9000\n" \
"s_add_u32 s85, s62, 0x8000\n" \
"v_mfma_f32_16x16x32_bf16 a[144:147], v[88:91], v[152:155], a[144:147]\n" \
"L_pf3:\n" \
"s_cbranch_execz L_pf7\n" \
"s_nop 0\n" \
"buffer_load_dwordx4 v22, s[64:67], s85 offen lds\n" \
"v_mfma_f32_16x16x32_bf16 a[144:147], v[84:87], v[148:151], a[144:147]\n" \
"ds_read_b64_tr_b16 v[144:145], v35 offset:8192\n" \
"ds_read_b64_tr_b16 v[146:147], v35 offset:9216\n" \
"v_mfma_f32_16x16x32_bf16 a[176:179], v[92:95], v[148:151], a[176:179]\n" \
"v_mfma_f32_16x16x32_bf16 a[176:179], v[96:99], v[152:155], a[176:179]\n" \
"s_add_u32 m0, %[mA], 0x9400\n" \
"s_add_u32 s84, s62, 0xa000\n" \
"v_mfma_f32_16x16x32_bf16 a[208:211], v[104:107], v[152:155], a[208:211]\n" \
"buffer_load_dwordx4 v23, s[64:67], s84 offen lds\n" \
"v_mfma_f32_16x16x32_bf16 a[208:211], v[100:103], v[148:151], a[208:211]\n" \
"v_mfma_f32_16x16x32_bf16 a[240:243], v[108:111], v[148:151], a[240:243]\n" \
"ds_read_b64_tr_b16 v[148:149], v32 offset:16384\n" \
"ds_read_b64_tr_b16 v[150:151], v32 offset:17408\n" \
"v_mfma_f32_16x16x32_bf16 a[240:243], v[112:115], v[152:155], a[240:243]\n" \
"s_add_u32 m0, %[mB], 0x8800\n" \
"s_add_u32 s85, s63, 0x10000\n" \
"v_mfma_f32_16x16x32_bf16 a[148:151], v[88:91], v[160:163], a[148:151]\n" \
"buffer_load_dwordx4 v27, s[68:71], s85 offen lds\n" \
"v_mfma_f32_16x16x32_bf16 a[148:151], v[84:87], v[156:159], a[148:151]\n" \
"ds_read_b64_tr_b16 v[152:153], v32 offset:24576\n" \
"ds_read_b64_tr_b16 v[154:155], v32 offset:25600\n" \
"v_mfma_f32_16x16x32_bf16 a[180:183], v[92:95], v[156:159], a[180:183]\n" \
"v_mfma_f32_16x16x32_bf16 a[180:183], v[96:99], v[160:163], a[180:183]\n" \
"s_add_u32 m0, %[mB], 0xc800\n" \
"s_add_u32 s84, s63, 0x10000\n" \
"v_mfma_f32_16x16x32_bf16 a[212:215], v[104:107], v[160:163], a[212:215]\n" \
"buffer_load_dwordx4 v27, s[72:75], s84 offen lds\n" \
"v_mfma_f32_16x16x32_bf16 a[212:215], v[100:103], v[156:159], a[212:215]\n" \
"v_mfma_f32_16x16x32_bf16 a[244:247], v[108:111], v[156:159], a[244:247]\n" \
"ds_read_b64_tr_b16 v[156:157], v33 offset:16384\n" \
"ds_read_b64_tr_b16 v[158:159], v33 offset:17408\n" \
"v_mfma_f32_16x16x32_bf16 a[244:247], v[112:115], v[160:163], a[244:247]\n" \
"ds_read_b64_tr_b16 v[160:161], v33 offset:24576\n" \
"ds_read_b64_tr_b16 v[162:163], v33 offset:25600\n" \
"v_mfma_f32_16x16x32_bf16 a[152:155], v[88:91], v[168:171], a[152:155]\n" \
"v_mfma_f32_16x16x32_bf16 a[152:155], v[84:87], v[164:167], a[152:155]\n" \
"v_mfma_f32_16x16x32_bf16 a[184:187], v[92:95], v[164:167], a[184:187]\n" \
"v_mfma_f32_16x16x32_bf16 a[184:187], v[96:99], v[168:171], a[184:187]\n" \
"v_mfma_f32_16x16x32_bf16 a[216:219], v[104:107], v[168:171], a[216:219]\n" \
"v_mfma_f32_16x16x32_bf16 a[216:219], v[100:103], v[164:167], a[216:219]\n" \
"v_mfma_f32_16x16x32_bf16 a[248:251], v[108:111], v[164:167], a[248:251]\n" \
"ds_read_b64_tr_b16 v[164:165], v34 offset:16384\n" \
"ds_read_b64_tr_b16 v[166:167], v34 offset:17408\n" \
"v_mfma_f32_16x16x32_bf16 a[248:251], v[112:115], v[168:171], a[248:251]\n" \
"ds_read_b64_tr_b16 v[168:169], v34 offset:24576\n" \
"ds_read_b64_tr_b16 v[170:171], v34 offset:25600\n" \
"v_mfma_f32_16x16x32_bf16 a[156:159], v[88:91], v[176:179], a[156:159]\n" \
"ds_read_b128 v[88:91], v29 offset:0\n" \
"v_mfma_f32_16x16x32_bf16 a[156:159], v[84:87], v[172:175], a[156:159]\n" \
"ds_read_b128 v[84:87], v28 offset:0\n" \
"v_mfma_f32_16x16x32_bf16 a[188:191], v[92:95], v[172:175], a[188:191]\n" \
"ds_read_b128 v[92:95], v28 offset:2048\n" \
"v_mfma_f32_16x16x32_bf16 a[188:191], v[96:99], v[176:179], a[188:191]\n" \
"ds_read_b128 v[96:99], v29 offset:2048\n" \
"v_mfma_f32_16x16x32_bf16 a[220:223], v[104:107], v[176:179], a[220:223]\n" \
"ds_read_b128 v[104:107], v29 offset:4096\n" \
"v_mfma_f32_16x16x32_bf16 a[220:223], v[100:103], v[172:175], a[220:223]\n" \
"ds_read_b128 v[100:103], v28 offset:4096\n" \
"v_mfma_f32_16x16x32_bf16 a[252:255], v[108:111], v[172:175], a[252:255]\n" \
"ds_read_b64_tr_b16 v[172:173], v35 offset:16384\n" \
"ds_read_b64_tr_b16 v[174:175], v35 offset:17408\n" \
"v_mfma_f32_16x16x32_bf16 a[252:255], v[112:115], v[176:179], a[252:255]\n" \
"s_add_u32 m0, %[mA], 0x9800\n" \
"s_waitcnt lgkmcnt(12)\n" \
"s_nop 0\n" \
"v_mfma_f32_16x16x32_bf16 a[0:3], v[120:123], v[56:59], a[0:3]\n" \
"s_add_u32 s85, s62, 0xc000\n" \
"ds_read_b64_tr_b16 v[176:177], v35 offset:24576\n" \
"v_mfma_f32_16x16x32_bf16 a[0:3], v[116:119], v[52:55], a[0:3]\n" \
"buffer_load_dwordx4 v22, s[64:67], s85 offen lds\n" \
"v_mfma_f32_16x16x32_bf16 a[32:35], v[116:119], v[60:63], a[32:35]\n" \
"s_add_u32 m0, %[mA], 0x9c00\n" \
"s_add_u32 s84, s62, 0xe000\n" \
"v_mfma_f32_16x16x32_bf16 a[32:35], v[120:123], v[64:67], a[32:35]\n" \
"ds_read_b64_tr_b16 v[178:179], v35 offset:25600\n" \
"ds_read_b128 v[108:111], v28 offset:6144\n" \
"v_mfma_f32_16x16x32_bf16 a[64:67], v[120:123], v[72:75], a[64:67]\n" \
"buffer_load_dwordx4 v23, s[64:67], s84 offen lds\n" \
"v_mfma_f32_16x16x32_bf16 a[64:67], v[116:119], v[68:71], a[64:67]\n" \
"s_add_u32 m0, %[mB], 0x8c00\n" \
"s_add_u32 s85, s63, 0x18000\n" \
"v_mfma_f32_16x16x32_bf16 a[96:99], v[116:119], v[76:79], a[96:99]\n" \
"ds_read_b128 v[112:115], v29 offset:6144\n" \
"s_mov_b32 s62, s23\n" \
"v_mfma_f32_16x16x32_bf16 a[96:99], v[120:123], v[80:83], a[96:99]\n" \
"buffer_load_dwordx4 v27, s[68:71], s85 offen lds\n" \
"v_mfma_f32_16x16x32_bf16 a[4:7], v[128:131], v[56:59], a[4:7]\n" \
"s_add_u32 m0, %[mB], 0xcc00\n" \
"s_add_u32 s84, s63, 0x18000\n" \
"v_mfma_f32_16x16x32_bf16 a[4:7], v[124:127], v[52:55], a[4:7]\n" \
"s_mov_b32 s68, s10\n" \
"s_and_b32 s69, s11, 0xffff\n" \
"v_mfma_f32_16x16x32_bf16 a[36:39], v[124:127], v[60:63], a[36:39]\n" \
"buffer_load_dwordx4 v27, s[72:75], s84 offen lds\n" \
"s_mov_b32 s73, s52\n" \
"s_add_u32 s74, s52, 0x4000\n" \
"s_add_u32 s75, s52, 0x8000\n" \
"v_mfma_f32_16x16x32_bf16 a[36:39], v[128:131], v[64:67], a[36:39]\n" \
"s_lshl_b32 s72, s62, 6\n" \
"s_add_u32 s72, s72, s37\n" \
"v_mfma_f32_16x16x32_bf16 a[68:71], v[128:131], v[72:75], a[68:71]\n" \
"v_bfe_u32 v44, v20, 2, 5\n" \
"s_mov_b32 s70, 0x480000\n" \
"v_mfma_f32_16x16x32_bf16 a[68:71], v[124:127], v[68:71], a[68:71]\n" \
"buffer_load_dword v44, v44, s[80:83], s39 offen offset:64 sc1\n" \
"v_mfma_f32_16x16x32_bf16 a[100:103], v[124:127], v[76:79], a[100:103]\n" \
"v_add_u32_e32 v22, s61, v28\n" \
"v_add_u32_e64 v23, s61, v29\n" \
"v_mfma_f32_16x16x32_bf16 a[100:103], v[128:131], v[80:83], a[100:103]\n" \
"s_mov_b32 m0, %[mS]\n" \
"s_mov_b32 s86, s18\n" \
"v_mfma_f32_16x16x32_bf16 a[8:11], v[136:139], v[56:59], a[8:11]\n" \
"s_mov_b32 s87, s19\n" \
"v_mfma_f32_16x16x32_bf16 a[8:11], v[132:135], v[52:55], a[8:11]\n" \
"v_mfma_f32_16x16x32_bf16 a[40:43], v[132:135], v[60:63], a[40:43]\n" \
"v_mfma_f32_16x16x32_bf16 a[40:43], v[136:139], v[64:67], a[40:43]\n" \
"v_mfma_f32_16x16x32_bf16 a[72:75], v[136:139], v[72:75], a[72:75]\n" \
"v_mfma_f32_16x16x32_bf16 a[72:75], v[132:135], v[68:71], a[72:75]\n" \
"s_mov_b32 exec_lo, 0xfffc0000\n" \
"s_mov_b32 exec_hi, 0xf\n" \
"global_load_lds_dword v16, s[86:87]\n" \
"s_mov_b64 exec, -1\n" \
"v_mfma_f32_16x16x32_bf16 a[104:107], v[132:135], v[76:79], a[104:107]\n" \
"s_mov_b32 m0, %[mS]\n" \
"s_mov_b32 s86, s16\n" \
"v_mfma_f32_16x16x32_bf16 a[104:107], v[136:139], v[80:83], a[104:107]\n" \
"s_mov_b32 s87, s17\n" \
"v_mfma_f32_16x16x32_bf16 a[12:15], v[144:147], v[56:59], a[12:15]\n" \
"v_mfma_f32_16x16x32_bf16 a[12:15], v[140:143], v[52:55], a[12:15]\n" \
"v_mfma_f32_16x16x32_bf16 a[44:47], v[140:143], v[60:63], a[44:47]\n" \
"s_mov_b32 exec_lo, 0x0\n" \
"s_mov_b32 exec_hi, 0x3f0\n" \
"global_load_lds_dword v16, s[86:87]\n" \
"s_mov_b64 exec, -1\n" \
"v_mfma_f32_16x16x32_bf16 a[44:47], v[144:147], v[64:67], a[44:47]\n" \
"v_mfma_f32_16x16x32_bf16 a[76:79], v[144:147], v[72:75], a[76:79]\n" \
"v_mfma_f32_16x16x32_bf16 a[76:79], v[140:143], v[68:71], a[76:79]\n" \
"v_mfma_f32_16x16x32_bf16 a[108:111], v[140:143], v[76:79], a[108:111]\n" \
"v_mfma_f32_16x16x32_bf16 a[108:111], v[144:147], v[80:83], a[108:111]\n" \
"v_mfma_f32_16x16x32_bf16 a[16:19], v[56:59], v[152:155], a[16:19]\n" \
"v_mfma_f32_16x16x32_bf16 a[16:19], v[52:55], v[148:151], a[16:19]\n" \
"v_mfma_f32_16x16x32_bf16 a[48:51], v[60:63], v[148:151], a[48:51]\n" \
"v_mfma_f32_16x16x32_bf16 a[48:51], v[64:67], v[152:155], a[48:51]\n" \
"v_mfma_f32_16x16x32_bf16 a[80:83], v[72:75], v[152:155], a[80:83]\n" \
"v_mfma_f32_16x16x32_bf16 a[80:83], v[68:71], v[148:151], a[80:83]\n" \
"v_mfma_f32_16x16x32_bf16 a[112:115], v[76:79], v[148:151], a[112:115]\n" \
"v_mfma_f32_16x16x32_bf16 a[112:115], v[80:83], v[152:155], a[112:115]\n" \
"v_mfma_f32_16x16x32_bf16 a[20:23], v[56:59], v[160:163], a[20:23]\n" \
"v_mfma_f32_16x16x32_bf16 a[20:23], v[52:55], v[156:159], a[20:23]\n" \
"v_mfma_f32_16x16x32_bf16 a[52:55], v[60:63], v[156:159], a[52:55]\n" \
"v_mfma_f32_16x16x32_bf16 a[52:55], v[64:67], v[160:163], a[52:55]\n" \
"v_mfma_f32_16x16x32_bf16 a[84:87], v[72:75], v[160:163], a[84:87]\n" \
"v_mfma_f32_16x16x32_bf16 a[84:87], v[68:71], v[156:159], a[84:87]\n" \
"v_mfma_f32_16x16x32_bf16 a[116:119], v[76:79], v[156:159], a[116:119]\n" \
"v_mfma_f32_16x16x32_bf16 a[116:119], v[80:83], v[160:163], a[116:119]\n" \
"s_waitcnt lgkmcnt(0)\n" \
"v_mfma_f32_16x16x32_bf16 a[24:27], v[56:59], v[168:171], a[24:27]\n" \
"v_mfma_f32_16x16x32_bf16 a[24:27], v[52:55], v[164:167], a[24:27]\n" \
"v_mfma_f32_16x16x32_bf16 a[56:59], v[60:63], v[164:167], a[56:59]\n" \
"v_mfma_f32_16x16x32_bf16 a[56:59], v[64:67], v[168:171], a[56:59]\n" \
"v_mfma_f32_16x16x32_bf16 a[88:91], v[72:75], v[168:171], a[88:91]\n" \
"v_mfma_f32_16x16x32_bf16 a[88:91], v[68:71], v[164:167], a[88:91]\n" \
"v_mfma_f32_16x16x32_bf16 a[120:123], v[76:79], v[164:167], a[120:123]\n" \
"v_mfma_f32_16x16x32_bf16 a[120:123], v[80:83], v[168:171], a[120:123]\n" \
"v_mfma_f32_16x16x32_bf16 a[28:31], v[56:59], v[176:179], a[28:31]\n" \
"s_waitcnt vmcnt(2)\n" \
"v_cmp_ne_u32_e32 vcc, %[tag], v44\n" \
"v_mfma_f32_16x16x32_bf16 a[28:31], v[52:55], v[172:175], a[28:31]\n" \
"v_mfma_f32_16x16x32_bf16 a[60:63], v[60:63], v[172:175], a[60:63]\n" \
"v_mfma_f32_16x16x32_bf16 a[60:63], v[64:67], v[176:179], a[60:63]\n" \
"v_mfma_f32_16x16x32_bf16 a[92:95], v[72:75], v[176:179], a[92:95]\n" \
"v_mfma_f32_16x16x32_bf16 a[92:95], v[68:71], v[172:175], a[92:95]\n" \
"s_cbranch_vccnz L_pl_%=\n" \
"L_rl_%=:\n" \
"s_lshl_b32 s63, s62, 12\n" \
"v_mfma_f32_16x16x32_bf16 a[124:127], v[76:79], v[172:175], a[124:127]\n" \
"v_mfma_f32_16x16x32_bf16 a[124:127], v[80:83], v[176:179], a[124:127]\n" \
"s_barrier\n" \
"v_mfma_f32_16x16x32_bf16 a[144:147], v[88:91], v[152:155], a[144:147]\n" \
"s_add_u32 m0, s40, 0x0\n" \
"s_add_u32 s85, s72, 0x0\n" \
"v_mfma_f32_16x16x32_bf16 a[144:147], v[84:87], v[148:151], a[144:147]\n" \
"buffer_load_dwordx4 v20, s[80:83], s85 offen sc1 lds\n" \
"v_mfma_f32_16x16x32_bf16 a[176:179], v[92:95], v[148:151], a[176:179]\n" \
"s_add_u32 m0, s40, 0x1000\n" \
"s_add_u32 s84, s72, 0x1000\n" \
"v_mfma_f32_16x16x32_bf16 a[176:179], v[96:99], v[152:155], a[176:179]\n" \
"ds_read_b128 v[56:59], v29 offset:49152\n" \
"ds_read_b128 v[52:55], v28 offset:49152\n" \
"v_mfma_f32_16x16x32_bf16 a[208:211], v[104:107], v[152:155], a[208:211]\n" \
"buffer_load_dwordx4 v20, s[80:83], s84 offen sc1 lds\n" \
"v_mfma_f32_16x16x32_bf16 a[208:211], v[100:103], v[148:151], a[208:211]\n" \
"ds_read_b128 v[60:63], v28 offset:51200\n" \
"ds_read_b128 v[64:67], v29 offset:51200\n" \
"v_mfma_f32_16x16x32_bf16 a[240:243], v[108:111], v[148:151], a[240:243]\n" \
"s_add_u32 m0, s40, 0x3000\n" \
"s_add_u32 s85, s62, 0x0\n" \
"v_mfma_f32_16x16x32_bf16 a[240:243], v[112:115], v[152:155], a[240:243]\n" \
"buffer_load_dwordx4 v21, s[76:79], s85 offen lds\n" \
"v_mfma_f32_16x16x32_bf16 a[148:151], v[88:91], v[160:163], a[148:151]\n" \
"ds_read_b64_tr_b16 v[152:153], v32 offset:57344\n" \
"ds_read_b64_tr_b16 v[154:155], v32 offset:58368\n" \
"v_mfma_f32_16x16x32_bf16 a[148:151], v[84:87], v[156:159], a[148:151]\n" \
"ds_read_b64_tr_b16 v[148:149], v32 offset:49152\n" \
"ds_read_b64_tr_b16 v[150:151], v32 offset:50176\n" \
"v_mfma_f32_16x16x32_bf16 a[180:183], v[92:95], v[156:159], a[180:183]\n" \
"s_add_u32 m0, %[mA], 0x0\n" \
"s_add_u32 s84, s62, 0x0\n" \
"s_nop 0\n" \
"v_mfma_f32_16x16x32_bf16 a[180:183], v[96:99], v[160:163], a[180:183]\n" \
"buffer_load_dwordx4 v24, s[76:79], s84 offen lds\n" \
"v_mfma_f32_16x16x32_bf16 a[212:215], v[104:107], v[160:163], a[212:215]\n" \
"ds_read_b128 v[72:75], v29 offset:53248\n" \
"ds_read_b128 v[68:71], v28 offset:53248\n" \
"v_mfma_f32_16x16x32_bf16 a[212:215], v[100:103], v[156:159], a[212:215]\n" \
"s_add_u32 m0, %[mA], 0x400\n" \
"s_add_u32 s85, s62, 0x2400\n" \
"v_mfma_f32_16x16x32_bf16 a[244:247], v[108:111], v[156:159], a[244:247]\n" \
"buffer_load_dwordx4 v25, s[76:79], s85 offen lds\n" \
"v_mfma_f32_16x16x32_bf16 a[244:247], v[112:115], v[160:163], a[244:247]\n" \
"ds_read_b128 v[76:79], v28 offset:55296\n" \
"ds_read_b128 v[80:83], v29 offset:55296\n" \
"v_mfma_f32_16x16x32_bf16 a[152:155], v[88:91], v[168:171], a[152:155]\n" \
"ds_read_b64_tr_b16 v[160:161], v33 offset:57344\n" \
"ds_read_b64_tr_b16 v[162:163], v33 offset:58368\n" \
"v_mfma_f32_16x16x32_bf16 a[152:155], v[84:87], v[164:167], a[152:155]\n" \
"s_add_u32 m0, %[mB], 0x0\n" \
"s_add_u32 s84, s63, 0x0\n" \
"v_mfma_f32_16x16x32_bf16 a[184:187], v[92:95], v[164:167], a[184:187]\n" \
"buffer_load_dwordx4 v26, s[68:71], s84 offen lds\n" \
"v_mfma_f32_16x16x32_bf16 a[184:187], v[96:99], v[168:171], a[184:187]\n" \
"ds_read_b64_tr_b16 v[156:157], v33 offset:49152\n" \
"ds_read_b64_tr_b16 v[158:159], v33 offset:50176\n" \
"v_mfma_f32_16x16x32_bf16 a[216:219], v[104:107], v[168:171], a[216:219]\n" \
"v_mfma_f32_16x16x32_bf16 a[216:219], v[100:103], v[164:167], a[216:219]\n" \
"s_add_u32 m0, %[mA], 0x800\n" \
"s_add_u32 s85, s62, 0x4800\n" \
"v_mfma_f32_16x16x32_bf16 a[248:251], v[108:111], v[164:167], a[248:251]\n" \
"buffer_load_dwordx4 v24, s[76:79], s85 offen lds\n" \
"v_mfma_f32_16x16x32_bf16 a[248:251], v[112:115], v[168:171], a[248:251]\n" \
"ds_read_b64_tr_b16 v[168:169], v34 offset:57344\n" \
"ds_read_b64_tr_b16 v[170:171], v34 offset:58368\n" \
"v_mfma_f32_16x16x32_bf16 a[156:159], v[88:91], v[176:179], a[156:159]\n" \
"s_add_u32 m0, %[mA], 0xc00\n" \
"s_add_u32 s84, s62, 0x6c00\n" \
"v_mfma_f32_16x16x32_bf16 a[156:159], v[84:87], v[172:175], a[156:159]\n" \
"buffer_load_dwordx4 v25, s[76:79], s84 offen lds\n" \
"v_mfma_f32_16x16x32_bf16 a[188:191], v[92:95], v[172:175], a[188:191]\n" \
"ds_read_b64_tr_b16 v[164:165], v34 offset:49152\n" \
"ds_read_b64_tr_b16 v[166:167], v34 offset:50176\n" \
"v_mfma_f32_16x16x32_bf16 a[188:191], v[96:99], v[176:179], a[188:191]\n" \
"v_mfma_f32_16x16x32_bf16 a[220:223], v[104:107], v[176:179], a[220:223]\n" \
"s_add_u32 m0, %[mB], 0x400\n" \
"s_add_u32 s85, s63, 0x8000\n" \
"v_mfma_f32_16x16x32_bf16 a[220:223], v[100:103], v[172:175], a[220:223]\n" \
"buffer_load_dwordx4 v26, s[68:71], s85 offen lds\n" \
"v_mfma_f32_16x16x32_bf16 a[252:255], v[108:111], v[172:175], a[252:255]\n" \
"ds_read_b64_tr_b16 v[172:173], v35 offset:49152\n" \
"ds_read_b64_tr_b16 v[174:175], v35 offset:50176\n" \
"v_mfma_f32_16x16x32_bf16 a[252:255], v[112:115], v[176:179], a[252:255]\n" \
"ds_read_b64_tr_b16 v[176:177], v35 offset:57344\n" \
"ds_read_b64_tr_b16 v[178:179], v35 offset:58368\n" \
"v_mfma_f32_16x16x32_bf16 a[128:131], v[120:123], v[88:91], a[128:131]\n" \
"s_add_u32 m0, %[mA], 0x1000\n" \
"s_add_u32 s84, s62, 0x9000\n" \
"v_mfma_f32_16x16x32_bf16 a[128:131], v[116:119], v[84:87], a[128:131]\n" \
"buffer_load_dwordx4 v24, s[76:79], s84 offen lds\n" \
"v_mfma_f32_16x16x32_bf16 a[160:163], v[116:119], v[92:95], a[160:163]\n" \
"v_mfma_f32_16x16x32_bf16 a[160:163], v[120:123], v[96:99], a[160:163]\n" \
"s_add_u32 m0, %[mA], 0x1400\n" \
"s_add_u32 s85, s62, 0xb400\n" \
"v_mfma_f32_16x16x32_bf16 a[192:195], v[120:123], v[104:107], a[192:195]\n" \
"buffer_load_dwordx4 v25, s[76:79], s85 offen lds\n" \
"v_mfma_f32_16x16x32_bf16 a[192:195], v[116:119], v[100:103], a[192:195]\n" \
"v_mfma_f32_16x16x32_bf16 a[224:227], v[116:119], v[108:111], a[224:227]\n" \
"ds_read_b64_tr_b16 v[116:117], v32 offset:32768\n" \
"ds_read_b64_tr_b16 v[118:119], v32 offset:33792\n" \
"v_mfma_f32_16x16x32_bf16 a[224:227], v[120:123], v[112:115], a[224:227]\n" \
"s_add_u32 m0, %[mB], 0x800\n" \
"s_add_u32 s84, s63, 0x10000\n" \
"v_mfma_f32_16x16x32_bf16 a[132:135], v[128:131], v[88:91], a[132:135]\n" \
"buffer_load_dwordx4 v27, s[68:71], s84 offen lds\n" \
"v_mfma_f32_16x16x32_bf16 a[132:135], v[124:127], v[84:87], a[132:135]\n" \
"ds_read_b64_tr_b16 v[120:121], v32 offset:40960\n" \
"ds_read_b64_tr_b16 v[122:123], v32 offset:41984\n" \
"v_mfma_f32_16x16x32_bf16 a[164:167], v[124:127], v[92:95], a[164:167]\n" \
"v_mfma_f32_16x16x32_bf16 a[164:167], v[128:131], v[96:99], a[164:167]\n" \
"s_add_u32 m0, %[mA], 0x1800\n" \
"s_add_u32 s85, s62, 0xd800\n" \
"v_mfma_f32_16x16x32_bf16 a[196:199], v[128:131], v[104:107], a[196:199]\n" \
"buffer_load_dwordx4 v24, s[76:79], s85 offen lds\n" \
"v_mfma_f32_16x16x32_bf16 a[196:199], v[124:127], v[100:103], a[196:199]\n" \
"v_mfma_f32_16x16x32_bf16 a[228:231], v[124:127], v[108:111], a[228:231]\n" \
"ds_read_b64_tr_b16 v[124:125], v33 offset:32768\n" \
"ds_read_b64_tr_b16 v[126:127], v33 offset:33792\n" \
"v_mfma_f32_16x16x32_bf16 a[228:231], v[128:131], v[112:115], a[228:231]\n" \
"ds_read_b64_tr_b16 v[128:129], v33 offset:40960\n" \
"ds_read_b64_tr_b16 v[130:131], v33 offset:41984\n" \
"v_mfma_f32_16x16x32_bf16 a[136:139], v[136:139], v[88:91], a[136:139]\n" \
"v_mfma_f32_16x16x32_bf16 a[136:139], v[132:135], v[84:87], a[136:139]\n" \
"v_mfma_f32_16x16x32_bf16 a[168:171], v[132:135], v[92:95], a[168:171]\n" \
"v_mfma_f32_16x16x32_bf16 a[168:171], v[136:139], v[96:99], a[168:171]\n" \
"v_mfma_f32_16x16x32_bf16 a[200:203], v[136:139], v[104:107], a[200:203]\n" \
"v_mfma_f32_16x16x32_bf16 a[200:203], v[132:135], v[100:103], a[200:203]\n" \
"v_mfma_f32_16x16x32_bf16 a[232:235], v[132:135], v[108:111], a[232:235]\n" \
"ds_read_b64_tr_b16 v[132:133], v34 offset:32768\n" \
"ds_read_b64_tr_b16 v[134:135], v34 offset:33792\n" \
"v_mfma_f32_16x16x32_bf16 a[232:235], v[136:139], v[112:115], a[232:235]\n" \
"ds_read_b64_tr_b16 v[136:137], v34 offset:40960\n" \
"ds_read_b64_tr_b16 v[138:139], v34 offset:41984\n" \
"v_mfma_f32_16x16x32_bf16 a[140:143], v[144:147], v[88:91], a[140:143]\n" \
"ds_read_b128 v[88:91], v29 offset:32768\n" \
"v_mfma_f32_16x16x32_bf16 a[140:143], v[140:143], v[84:87], a[140:143]\n" \
"ds_read_b128 v[84:87], v28 offset:32768\n" \
"v_mfma_f32_16x16x32_bf16 a[172:175], v[140:143], v[92:95], a[172:175]\n" \
"ds_read_b128 v[92:95], v28 offset:34816\n" \
"v_mfma_f32_16x16x32_bf16 a[172:175], v[144:147], v[96:99], a[172:175]\n" \
"ds_read_b128 v[96:99], v29 offset:34816\n" \
"v_mfma_f32_16x16x32_bf16 a[204:207], v[144:147], v[104:107], a[204:207]\n" \
"ds_read_b128 v[104:107], v29 offset:36864\n" \
"v_mfma_f32_16x16x32_bf16 a[204:207], v[140:143], v[100:103], a[204:207]\n" \
"ds_read_b128 v[100:103], v28 offset:36864\n" \
"v_mfma_f32_16x16x32_bf16 a[236:239], v[140:143], v[108:111], a[236:239]\n" \
"ds_read_b64_tr_b16 v[140:141], v35 offset:32768\n" \
"ds_read_b64_tr_b16 v[142:143], v35 offset:33792\n" \
"v_mfma_f32_16x16x32_bf16 a[236:239], v[144:147], v[112:115], a[236:239]\n" \
"s_add_u32 m0, %[mA], 0x1c00\n" \
"s_waitcnt lgkmcnt(12)\n" \
"s_nop 0\n" \
"v_mfma_f32_16x16x32_bf16 a[16:19], v[56:59], v[152:155], a[16:19]\n" \
"s_add_u32 s85, s62, 0xfc00\n" \
"ds_read_b64_tr_b16 v[144:145], v35 offset:40960\n" \
"v_mfma_f32_16x16x32_bf16 a[16:19], v[52:55], v[148:151], a[16:19]\n" \
"buffer_load_dwordx4 v25, s[76:79], s85 offen lds\n" \
"v_mfma_f32_16x16x32_bf16 a[48:51], v[60:63], v[148:151], a[48:51]\n" \
"s_add_u32 m0, %[mB], 0xc00\n" \
"s_add_u32 s84, s63, 0x18000\n" \
"v_mfma_f32_16x16x32_bf16 a[48:51], v[64:67], v[152:155], a[48:51]\n" \
"ds_read_b64_tr_b16 v[146:147], v35 offset:41984\n" \
"ds_read_b128 v[108:111], v28 offset:38912\n" \
"v_mfma_f32_16x16x32_bf16 a[80:83], v[72:75], v[152:155], a[80:83]\n" \
"buffer_load_dwordx4 v27, s[68:71], s84 offen lds\n" \
"v_mfma_f32_16x16x32_bf16 a[80:83], v[68:71], v[148:151], a[80:83]\n" \
"ds_read_b128 v[112:115], v29 offset:38912\n" \
"s_add_u32 s62, s62, 0x80\n" \
"v_mfma_f32_16x16x32_bf16 a[112:115], v[76:79], v[148:151], a[112:115]\n" \
"s_cmp_lt_u32 s62, 0x480\n" \
"s_cselect_b32 s62, s62, 0\n" \
"v_mfma_f32_16x16x32_bf16 a[112:115], v[80:83], v[152:155], a[112:115]\n" \
"s_lshl_b32 s72, s62, 6\n" \
"s_add_u32 s72, s72, s37\n" \
"v_mfma_f32_16x16x32_bf16 a[20:23], v[56:59], v[160:163], a[20:23]\n" \
"s_lshl_b32 s63, s62, 12\n" \
"v_add_u32_e64 v28, s59, v28\n" \
"v_mfma_f32_16x16x32_bf16 a[20:23], v[52:55], v[156:159], a[20:23]\n" \
"v_add_u32_e64 v29, s59, v29\n" \
"v_accvgpr_read_b32 v184, a16\n" \
"v_mfma_f32_16x16x32_bf16 a[52:55], v[60:63], v[156:159], a[52:55]\n" \
"v_accvgpr_read_b32 v185, a17\n" \
"v_accvgpr_read_b32 v186, a18\n" \
"v_mfma_f32_16x16x32_bf16 a[52:55], v[64:67], v[160:163], a[52:55]\n" \
"v_accvgpr_read_b32 v187, a19\n" \
"v_accvgpr_write_b32 a16, 0\n" \
"v_mfma_f32_16x16x32_bf16 a[84:87], v[72:75], v[160:163], a[84:87]\n" \
"v_accvgpr_write_b32 a17, 0\n" \
"v_accvgpr_write_b32 a18, 0\n" \
"v_mfma_f32_16x16x32_bf16 a[84:87], v[68:71], v[156:159], a[84:87]\n" \
"v_accvgpr_write_b32 a19, 0\n" \
"v_accvgpr_read_b32 v180, a48\n" \
"v_mfma_f32_16x16x32_bf16 a[116:119], v[76:79], v[156:159], a[116:119]\n" \
"v_accvgpr_read_b32 v181, a49\n" \
"v_accvgpr_read_b32 v182, a50\n" \
"v_mfma_f32_16x16x32_bf16 a[116:119], v[80:83], v[160:163], a[116:119]\n" \
"v_accvgpr_read_b32 v183, a51\n" \
"v_accvgpr_read_b32 v188, a80\n" \
"v_mfma_f32_16x16x32_bf16 a[24:27], v[56:59], v[168:171], a[24:27]\n" \
"v_accvgpr_read_b32 v189, a81\n" \
"v_accvgpr_read_b32 v190, a82\n" \
"v_mfma_f32_16x16x32_bf16 a[24:27], v[52:55], v[164:167], a[24:27]\n" \
"v_accvgpr_read_b32 v191, a83\n" \
"v_cvt_pk_bf16_f32 v184, v184, v185\n" \
"v_mfma_f32_16x16x32_bf16 a[56:59], v[60:63], v[164:167], a[56:59]\n" \
"v_cvt_pk_bf16_f32 v185, v186, v187\n" \
"v_cvt_pk_bf16_f32 v186, v180, v181\n" \
"v_mfma_f32_16x16x32_bf16 a[56:59], v[64:67], v[168:171], a[56:59]\n" \
"v_cvt_pk_bf16_f32 v187, v182, v183\n" \
"v_accvgpr_read_b32 v180, a112\n" \
"v_mfma_f32_16x16x32_bf16 a[88:91], v[72:75], v[168:171], a[88:91]\n" \
"v_accvgpr_read_b32 v181, a113\n" \
"v_accvgpr_read_b32 v182, a114\n" \
"v_mfma_f32_16x16x32_bf16 a[88:91], v[68:71], v[164:167], a[88:91]\n" \
"v_accvgpr_read_b32 v183, a115\n" \
"v_accvgpr_write_b32 a112, 0\n" \
"v_mfma_f32_16x16x32_bf16 a[120:123], v[76:79], v[164:167], a[120:123]\n" \
"v_accvgpr_write_b32 a113, 0\n" \
"v_accvgpr_write_b32 a114, 0\n" \
"v_mfma_f32_16x16x32_bf16 a[120:123], v[80:83], v[168:171], a[120:123]\n" \
"v_accvgpr_write_b32 a115, 0\n" \
"v_accvgpr_read_b32 v192, a20\n" \
"v_mfma_f32_16x16x32_bf16 a[28:31], v[56:59], v[176:179], a[28:31]\n" \
"v_accvgpr_read_b32 v193, a21\n" \
"v_accvgpr_read_b32 v194, a22\n" \
"v_mfma_f32_16x16x32_bf16 a[28:31], v[52:55], v[172:175], a[28:31]\n" \
"v_accvgpr_read_b32 v195, a23\n" \
"v_cvt_pk_bf16_f32 v188, v188, v189\n" \
"v_mfma_f32_16x16x32_bf16 a[60:63], v[60:63], v[172:175], a[60:63]\n" \
"v_cvt_pk_bf16_f32 v189, v190, v191\n" \
"v_cvt_pk_bf16_f32 v190, v180, v181\n" \
"v_mfma_f32_16x16x32_bf16 a[60:63], v[64:67], v[176:179], a[60:63]\n" \
"v_cvt_pk_bf16_f32 v191, v182, v183\n" \
"v_accvgpr_read_b32 v180, a52\n" \
"v_mfma_f32_16x16x32_bf16 a[92:95], v[72:75], v[176:179], a[92:95]\n" \
"v_accvgpr_read_b32 v181, a53\n" \
"v_accvgpr_read_b32 v182, a54\n" \
"v_mfma_f32_16x16x32_bf16 a[92:95], v[68:71], v[172:175], a[92:95]\n" \
"v_accvgpr_read_b32 v183, a55\n" \
"v_accvgpr_read_b32 v196, a84\n" \
"v_mfma_f32_16x16x32_bf16 a[124:127], v[76:79], v[172:175], a[124:127]\n" \
"v_accvgpr_read_b32 v197, a85\n" \
"v_accvgpr_read_b32 v198, a86\n" \
"v_mfma_f32_16x16x32_bf16 a[124:127], v[80:83], v[176:179], a[124:127]\n" \
"v_accvgpr_read_b32 v199, a87\n" \
"v_cvt_pk_bf16_f32 v192, v192, v193\n" \
"v_mfma_f32_16x16x32_bf16 a[0:3], v[120:123], v[56:59], a[0:3]\n" \
"v_cvt_pk_bf16_f32 v193, v194, v195\n" \
"v_cvt_pk_bf16_f32 v194, v180, v181\n" \
"v_mfma_f32_16x16x32_bf16 a[0:3], v[116:119], v[52:55], a[0:3]\n" \
"v_cvt_pk_bf16_f32 v195, v182, v183\n" \
"v_accvgpr_read_b32 v180, a116\n" \
"v_mfma_f32_16x16x32_bf16 a[32:35], v[116:119], v[60:63], a[32:35]\n" \
"v_accvgpr_read_b32 v181, a117\n" \
"v_accvgpr_read_b32 v182, a118\n" \
"v_mfma_f32_16x16x32_bf16 a[32:35], v[120:123], v[64:67], a[32:35]\n" \
"v_accvgpr_read_b32 v183, a119\n" \
"v_accvgpr_write_b32 a116, 0\n" \
"v_mfma_f32_16x16x32_bf16 a[64:67], v[120:123], v[72:75], a[64:67]\n" \
"v_accvgpr_write_b32 a117, 0\n" \
"v_accvgpr_write_b32 a118, 0\n" \
"v_mfma_f32_16x16x32_bf16 a[64:67], v[116:119], v[68:71], a[64:67]\n" \
"v_accvgpr_write_b32 a119, 0\n" \
"v_accvgpr_read_b32 v200, a24\n" \
"v_mfma_f32_16x16x32_bf16 a[96:99], v[116:119], v[76:79], a[96:99]\n" \
"v_accvgpr_read_b32 v201, a25\n" \
"v_accvgpr_read_b32 v202, a26\n" \
"v_mfma_f32_16x16x32_bf16 a[96:99], v[120:123], v[80:83], a[96:99]\n" \
"v_accvgpr_read_b32 v203, a27\n" \
"v_cvt_pk_bf16_f32 v196, v196, v197\n" \
"v_mfma_f32_16x16x32_bf16 a[4:7], v[128:131], v[56:59], a[4:7]\n" \
"v_cvt_pk_bf16_f32 v197, v198, v199\n" \
"v_cvt_pk_bf16_f32 v198, v180, v181\n" \
"v_mfma_f32_16x16x32_bf16 a[4:7], v[124:127], v[52:55], a[4:7]\n" \
"v_cvt_pk_bf16_f32 v199, v182, v183\n" \
"v_accvgpr_read_b32 v180, a56\n" \
"v_mfma_f32_16x16x32_bf16 a[36:39], v[124:127], v[60:63], a[36:39]\n" \
"v_accvgpr_read_b32 v181, a57\n" \
"v_accvgpr_read_b32 v182, a58\n" \
"v_mfma_f32_16x16x32_bf16 a[36:39], v[128:131], v[64:67], a[36:39]\n" \
"v_accvgpr_read_b32 v183, a59\n" \
"v_accvgpr_read_b32 v204, a88\n" \
"v_mfma_f32_16x16x32_bf16 a[68:71], v[128:131], v[72:75], a[68:71]\n" \
"v_accvgpr_read_b32 v205, a89\n" \
"v_accvgpr_read_b32 v206, a90\n" \
"v_mfma_f32_16x16x32_bf16 a[68:71], v[124:127], v[68:71], a[68:71]\n" \
"v_accvgpr_read_b32 v207, a91\n" \
"v_cvt_pk_bf16_f32 v200, v200, v201\n" \
"v_mfma_f32_16x16x32_bf16 a[100:103], v[124:127], v[76:79], a[100:103]\n" \
"v_cvt_pk_bf16_f32 v201, v202, v203\n" \
"v_cvt_pk_bf16_f32 v202, v180, v181\n" \
"v_mfma_f32_16x16x32_bf16 a[100:103], v[128:131], v[80:83], a[100:103]\n" \
"v_cvt_pk_bf16_f32 v203, v182, v183\n" \
"s_waitcnt lgkmcnt(0)\n" \
"s_nop 0\n" \
"v_mfma_f32_16x16x32_bf16 a[8:11], v[136:139], v[56:59], a[8:11]\n" \
"v_accvgpr_read_b32 v180, a120\n" \
"v_accvgpr_read_b32 v181, a121\n" \
"v_mfma_f32_16x16x32_bf16 a[8:11], v[132:135], v[52:55], a[8:11]\n" \
"v_accvgpr_read_b32 v182, a122\n" \
"v_accvgpr_read_b32 v183, a123\n" \
"v_mfma_f32_16x16x32_bf16 a[40:43], v[132:135], v[60:63], a[40:43]\n" \
"v_accvgpr_write_b32 a120, 0\n" \
"v_accvgpr_write_b32 a121, 0\n" \
"v_mfma_f32_16x16x32_bf16 a[40:43], v[136:139], v[64:67], a[40:43]\n" \
"v_accvgpr_write_b32 a122, 0\n" \
"v_accvgpr_write_b32 a123, 0\n" \
"v_mfma_f32_16x16x32_bf16 a[72:75], v[136:139], v[72:75], a[72:75]\n" \
"v_accvgpr_read_b32 v208, a28\n" \
"v_accvgpr_read_b32 v209, a29\n" \
"v_mfma_f32_16x16x32_bf16 a[72:75], v[132:135], v[68:71], a[72:75]\n" \
"v_accvgpr_read_b32 v210, a30\n" \
"v_accvgpr_read_b32 v211, a31\n" \
"v_mfma_f32_16x16x32_bf16 a[104:107], v[132:135], v[76:79], a[104:107]\n" \
"v_cvt_pk_bf16_f32 v204, v204, v205\n" \
"v_cvt_pk_bf16_f32 v205, v206, v207\n" \
"v_mfma_f32_16x16x32_bf16 a[104:107], v[136:139], v[80:83], a[104:107]\n" \
"v_cvt_pk_bf16_f32 v206, v180, v181\n" \
"v_cvt_pk_bf16_f32 v207, v182, v183\n" \
"v_mfma_f32_16x16x32_bf16 a[12:15], v[144:147], v[56:59], a[12:15]\n" \
"v_accvgpr_read_b32 v180, a60\n" \
"v_accvgpr_read_b32 v181, a61\n" \
"v_mfma_f32_16x16x32_bf16 a[12:15], v[140:143], v[52:55], a[12:15]\n" \
"v_accvgpr_read_b32 v182, a62\n" \
"v_accvgpr_read_b32 v183, a63\n" \
"v_mfma_f32_16x16x32_bf16 a[44:47], v[140:143], v[60:63], a[44:47]\n" \
"v_accvgpr_read_b32 v212, a92\n" \
"v_accvgpr_read_b32 v213, a93\n" \
"v_mfma_f32_16x16x32_bf16 a[44:47], v[144:147], v[64:67], a[44:47]\n" \
"v_accvgpr_read_b32 v214, a94\n" \
"v_accvgpr_read_b32 v215, a95\n" \
"v_mfma_f32_16x16x32_bf16 a[76:79], v[144:147], v[72:75], a[76:79]\n" \
"v_cvt_pk_bf16_f32 v208, v208, v209\n" \
"v_cvt_pk_bf16_f32 v209, v210, v211\n" \
"v_mfma_f32_16x16x32_bf16 a[76:79], v[140:143], v[68:71], a[76:79]\n" \
"v_cvt_pk_bf16_f32 v210, v180, v181\n" \
"v_cvt_pk_bf16_f32 v211, v182, v183\n" \
"v_mfma_f32_16x16x32_bf16 a[108:111], v[140:143], v[76:79], a[108:111]\n" \
"v_accvgpr_read_b32 v180, a124\n" \
"v_accvgpr_read_b32 v181, a125\n" \
"v_mfma_f32_16x16x32_bf16 a[108:111], v[144:147], v[80:83], a[108:111]\n" \
"v_accvgpr_read_b32 v182, a126\n" \
"v_accvgpr_read_b32 v183, a127\n" \
"s_waitcnt vmcnt(0)\n" \
"s_barrier\n" \
"v_mfma_f32_16x16x32_bf16 a[128:131], v[120:123], v[88:91], a[128:131]\n" \
"s_add_u32 m0, s40, 0x8000\n" \
"s_add_u32 s85, s72, 0x0\n" \
"s_nop 0\n" \
"v_mfma_f32_16x16x32_bf16 a[128:131], v[116:119], v[84:87], a[128:131]\n" \
"buffer_load_dwordx4 v20, s[80:83], s85 offen sc1 lds\n" \
"v_mfma_f32_16x16x32_bf16 a[160:163], v[116:119], v[92:95], a[160:163]\n" \
"s_add_u32 m0, s40, 0x9000\n" \
"s_add_u32 s84, s72, 0x1000\n" \
"v_mfma_f32_16x16x32_bf16 a[160:163], v[120:123], v[96:99], a[160:163]\n" \
"ds_read_b128 v[52:55], v28 offset:0\n" \
"L_pf4:\n" \
"s_cbranch_execz L_pf8\n" \
"s_nop 0\n" \
"ds_read_b128 v[56:59], v29 offset:0\n" \
"v_mfma_f32_16x16x32_bf16 a[192:195], v[120:123], v[104:107], a[192:195]\n" \
"buffer_load_dwordx4 v20, s[80:83], s84 offen sc1 lds\n" \
"v_mfma_f32_16x16x32_bf16 a[192:195], v[116:119], v[100:103], a[192:195]\n" \
"ds_read_b128 v[60:63], v28 offset:2048\n" \
"ds_read_b128 v[64:67], v29 offset:2048\n" \
"v_mfma_f32_16x16x32_bf16 a[224:227], v[116:119], v[108:111], a[224:227]\n" \
"s_add_u32 m0, s40, 0xb000\n" \
"s_add_u32 s85, s62, 0x0\n" \
"v_mfma_f32_16x16x32_bf16 a[224:227], v[120:123], v[112:115], a[224:227]\n" \
"buffer_load_dwordx4 v21, s[76:79], s85 offen lds\n" \
"v_mfma_f32_16x16x32_bf16 a[132:135], v[128:131], v[88:91], a[132:135]\n" \
"ds_read_b64_tr_b16 v[116:117], v36 offset:0\n" \
"ds_read_b64_tr_b16 v[118:119], v36 offset:512\n" \
"v_mfma_f32_16x16x32_bf16 a[132:135], v[124:127], v[84:87], a[132:135]\n" \
"ds_read_b64_tr_b16 v[120:121], v36 offset:4096\n" \
"ds_read_b64_tr_b16 v[122:123], v36 offset:4608\n" \
"v_mfma_f32_16x16x32_bf16 a[164:167], v[124:127], v[92:95], a[164:167]\n" \
"s_add_u32 m0, %[mA], 0x8000\n" \
"s_add_u32 s84, s62, 0x0\n" \
"v_mfma_f32_16x16x32_bf16 a[164:167], v[128:131], v[96:99], a[164:167]\n" \
"buffer_load_dwordx4 v24, s[76:79], s84 offen lds\n" \
"v_mfma_f32_16x16x32_bf16 a[196:199], v[128:131], v[104:107], a[196:199]\n" \
"ds_read_b128 v[68:71], v28 offset:4096\n" \
"ds_read_b128 v[72:75], v29 offset:4096\n" \
"v_mfma_f32_16x16x32_bf16 a[196:199], v[124:127], v[100:103], a[196:199]\n" \
"s_add_u32 m0, %[mA], 0x8400\n" \
"s_add_u32 s85, s62, 0x2400\n" \
"v_mfma_f32_16x16x32_bf16 a[228:231], v[124:127], v[108:111], a[228:231]\n" \
"buffer_load_dwordx4 v25, s[76:79], s85 offen lds\n" \
"v_mfma_f32_16x16x32_bf16 a[228:231], v[128:131], v[112:115], a[228:231]\n" \
"ds_read_b64_tr_b16 v[128:129], v37 offset:4096\n" \
"ds_read_b64_tr_b16 v[130:131], v37 offset:4608\n" \
"v_mfma_f32_16x16x32_bf16 a[136:139], v[136:139], v[88:91], a[136:139]\n" \
"ds_read_b64_tr_b16 v[124:125], v37 offset:0\n" \
"ds_read_b64_tr_b16 v[126:127], v37 offset:512\n" \
"v_mfma_f32_16x16x32_bf16 a[136:139], v[132:135], v[84:87], a[136:139]\n" \
"s_add_u32 m0, %[mB], 0x8000\n" \
"s_add_u32 s84, s63, 0x0\n" \
"s_nop 0\n" \
"v_mfma_f32_16x16x32_bf16 a[168:171], v[132:135], v[92:95], a[168:171]\n" \
"buffer_load_dwordx4 v26, s[68:71], s84 offen lds\n" \
"v_mfma_f32_16x16x32_bf16 a[168:171], v[136:139], v[96:99], a[168:171]\n" \
"ds_read_b128 v[76:79], v28 offset:6144\n" \
"ds_read_b128 v[80:83], v29 offset:6144\n" \
"v_mfma_f32_16x16x32_bf16 a[200:203], v[136:139], v[104:107], a[200:203]\n" \
"v_accvgpr_write_b32 a124, 0\n" \
"v_accvgpr_write_b32 a125, 0\n" \
"v_mfma_f32_16x16x32_bf16 a[200:203], v[132:135], v[100:103], a[200:203]\n" \
"s_add_u32 m0, %[mA], 0x8800\n" \
"s_add_u32 s85, s62, 0x4800\n" \
"v_mfma_f32_16x16x32_bf16 a[232:235], v[132:135], v[108:111], a[232:235]\n" \
"buffer_load_dwordx4 v24, s[76:79], s85 offen lds\n" \
"v_mfma_f32_16x16x32_bf16 a[232:235], v[136:139], v[112:115], a[232:235]\n" \
"ds_read_b64_tr_b16 v[132:133], v38 offset:0\n" \
"ds_read_b64_tr_b16 v[134:135], v38 offset:512\n" \
"v_mfma_f32_16x16x32_bf16 a[140:143], v[144:147], v[88:91], a[140:143]\n" \
"s_add_u32 m0, %[mA], 0x8c00\n" \
"s_add_u32 s84, s62, 0x6c00\n" \
"v_mfma_f32_16x16x32_bf16 a[140:143], v[140:143], v[84:87], a[140:143]\n" \
"buffer_load_dwordx4 v25, s[76:79], s84 offen lds\n" \
"v_mfma_f32_16x16x32_bf16 a[172:175], v[140:143], v[92:95], a[172:175]\n" \
"ds_read_b64_tr_b16 v[136:137], v38 offset:4096\n" \
"ds_read_b64_tr_b16 v[138:139], v38 offset:4608\n" \
"v_mfma_f32_16x16x32_bf16 a[172:175], v[144:147], v[96:99], a[172:175]\n" \
"v_accvgpr_write_b32 a126, 0\n" \
"v_accvgpr_write_b32 a127, 0\n" \
"v_mfma_f32_16x16x32_bf16 a[204:207], v[144:147], v[104:107], a[204:207]\n" \
"s_add_u32 m0, %[mB], 0x8400\n" \
"s_add_u32 s85, s63, 0x8000\n" \
"v_mfma_f32_16x16x32_bf16 a[204:207], v[140:143], v[100:103], a[204:207]\n" \
"buffer_load_dwordx4 v26, s[68:71], s85 offen lds\n" \
"v_mfma_f32_16x16x32_bf16 a[236:239], v[140:143], v[108:111], a[236:239]\n" \
"ds_read_b64_tr_b16 v[140:141], v39 offset:0\n" \
"ds_read_b64_tr_b16 v[142:143], v39 offset:960\n" \
"v_mfma_f32_16x16x32_bf16 a[236:239], v[144:147], v[112:115], a[236:239]\n" \
"v_accvgpr_read_b32 v216, a0\n" \
"v_accvgpr_read_b32 v217, a1\n" \
"v_mfma_f32_16x16x32_bf16 a[144:147], v[88:91], v[152:155], a[144:147]\n" \
"s_add_u32 m0, %[mA], 0x9000\n" \
"s_add_u32 s84, s62, 0x9000\n" \
"v_mfma_f32_16x16x32_bf16 a[144:147], v[84:87], v[148:151], a[144:147]\n" \
"buffer_load_dwordx4 v24, s[76:79], s84 offen lds\n" \
"v_mfma_f32_16x16x32_bf16 a[148:151], v[84:87], v[156:159], a[148:151]\n" \
"v_accvgpr_read_b32 v218, a2\n" \
"v_accvgpr_read_b32 v219, a3\n" \
"v_mfma_f32_16x16x32_bf16 a[148:151], v[88:91], v[160:163], a[148:151]\n" \
"s_add_u32 m0, %[mA], 0x9400\n" \
"s_add_u32 s85, s62, 0xb400\n" \
"v_mfma_f32_16x16x32_bf16 a[152:155], v[88:91], v[168:171], a[152:155]\n" \
"buffer_load_dwordx4 v25, s[76:79], s85 offen lds\n" \
"v_mfma_f32_16x16x32_bf16 a[152:155], v[84:87], v[164:167], a[152:155]\n" \
"v_cvt_pk_bf16_f32 v212, v212, v213\n" \
"v_cvt_pk_bf16_f32 v213, v214, v215\n" \
"v_mfma_f32_16x16x32_bf16 a[156:159], v[84:87], v[172:175], a[156:159]\n" \
"ds_read_b128 v[84:87], v22 offset:0\n" \
"v_cvt_pk_bf16_f32 v214, v180, v181\n" \
"v_mfma_f32_16x16x32_bf16 a[156:159], v[88:91], v[176:179], a[156:159]\n" \
"s_add_u32 m0, %[mB], 0x8800\n" \
"s_add_u32 s84, s63, 0x10000\n" \
"v_mfma_f32_16x16x32_bf16 a[176:179], v[96:99], v[152:155], a[176:179]\n" \
"buffer_load_dwordx4 v27, s[68:71], s84 offen lds\n" \
"v_mfma_f32_16x16x32_bf16 a[176:179], v[92:95], v[148:151], a[176:179]\n" \
"ds_read_b128 v[88:91], v23 offset:0\n" \
"v_cvt_pk_bf16_f32 v215, v182, v183\n" \
"v_mfma_f32_16x16x32_bf16 a[180:183], v[92:95], v[156:159], a[180:183]\n" \
"v_accvgpr_read_b32 v180, a4\n" \
"v_accvgpr_read_b32 v181, a5\n" \
"v_mfma_f32_16x16x32_bf16 a[180:183], v[96:99], v[160:163], a[180:183]\n" \
"s_add_u32 m0, %[mA], 0x9800\n" \
"s_add_u32 s85, s62, 0xd800\n" \
"v_mfma_f32_16x16x32_bf16 a[184:187], v[96:99], v[168:171], a[184:187]\n" \
"buffer_load_dwordx4 v24, s[76:79], s85 offen lds\n" \
"v_mfma_f32_16x16x32_bf16 a[184:187], v[92:95], v[164:167], a[184:187]\n" \
"v_accvgpr_read_b32 v182, a6\n" \
"v_accvgpr_read_b32 v183, a7\n" \
"v_mfma_f32_16x16x32_bf16 a[188:191], v[92:95], v[172:175], a[188:191]\n" \
"v_cvt_pk_bf16_f32 v216, v216, v217\n" \
"v_cvt_pk_bf16_f32 v217, v218, v219\n" \
"v_mfma_f32_16x16x32_bf16 a[188:191], v[96:99], v[176:179], a[188:191]\n" \
"v_accvgpr_read_b32 v220, a32\n" \
"v_accvgpr_read_b32 v221, a33\n" \
"v_mfma_f32_16x16x32_bf16 a[208:211], v[104:107], v[152:155], a[208:211]\n" \
"v_cvt_pk_bf16_f32 v218, v180, v181\n" \
"v_cvt_pk_bf16_f32 v219, v182, v183\n" \
"v_mfma_f32_16x16x32_bf16 a[208:211], v[100:103], v[148:151], a[208:211]\n" \
"v_accvgpr_read_b32 v222, a34\n" \
"v_accvgpr_read_b32 v223, a35\n" \
"v_mfma_f32_16x16x32_bf16 a[212:215], v[100:103], v[156:159], a[212:215]\n" \
"v_cvt_pk_bf16_f32 v220, v220, v221\n" \
"v_accvgpr_read_b32 v180, a36\n" \
"v_mfma_f32_16x16x32_bf16 a[212:215], v[104:107], v[160:163], a[212:215]\n" \
"v_accvgpr_read_b32 v181, a37\n" \
"v_accvgpr_read_b32 v182, a38\n" \
"v_mfma_f32_16x16x32_bf16 a[216:219], v[104:107], v[168:171], a[216:219]\n" \
"v_accvgpr_read_b32 v183, a39\n" \
"v_cvt_pk_bf16_f32 v221, v222, v223\n" \
"v_mfma_f32_16x16x32_bf16 a[216:219], v[100:103], v[164:167], a[216:219]\n" \
"v_accvgpr_read_b32 v224, a64\n" \
"v_accvgpr_read_b32 v225, a65\n" \
"v_mfma_f32_16x16x32_bf16 a[220:223], v[100:103], v[172:175], a[220:223]\n" \
"v_cvt_pk_bf16_f32 v222, v180, v181\n" \
"v_cvt_pk_bf16_f32 v223, v182, v183\n" \
"v_mfma_f32_16x16x32_bf16 a[220:223], v[104:107], v[176:179], a[220:223]\n" \
"v_accvgpr_read_b32 v226, a66\n" \
"v_accvgpr_read_b32 v227, a67\n" \
"v_mfma_f32_16x16x32_bf16 a[240:243], v[112:115], v[152:155], a[240:243]\n" \
"ds_read_b64_tr_b16 v[152:153], v32 offset:8192\n" \
"ds_read_b64_tr_b16 v[154:155], v32 offset:9216\n" \
"v_mfma_f32_16x16x32_bf16 a[240:243], v[108:111], v[148:151], a[240:243]\n" \
"ds_read_b64_tr_b16 v[148:149], v32 offset:0\n" \
"ds_read_b64_tr_b16 v[150:151], v32 offset:1024\n" \
"v_mfma_f32_16x16x32_bf16 a[244:247], v[108:111], v[156:159], a[244:247]\n" \
"ds_read_b64_tr_b16 v[156:157], v33 offset:0\n" \
"ds_read_b64_tr_b16 v[158:159], v33 offset:1024\n" \
"v_mfma_f32_16x16x32_bf16 a[244:247], v[112:115], v[160:163], a[244:247]\n" \
"ds_read_b64_tr_b16 v[160:161], v33 offset:8192\n" \
"ds_read_b64_tr_b16 v[162:163], v33 offset:9216\n" \
"v_mfma_f32_16x16x32_bf16 a[248:251], v[112:115], v[168:171], a[248:251]\n" \
"ds_read_b64_tr_b16 v[168:169], v34 offset:8192\n" \
"ds_read_b64_tr_b16 v[170:171], v34 offset:9216\n" \
"v_mfma_f32_16x16x32_bf16 a[248:251], v[108:111], v[164:167], a[248:251]\n" \
"ds_read_b64_tr_b16 v[164:165], v34 offset:0\n" \
"ds_read_b64_tr_b16 v[166:167], v34 offset:1024\n" \
"v_mfma_f32_16x16x32_bf16 a[252:255], v[108:111], v[172:175], a[252:255]\n" \
"ds_read_b64_tr_b16 v[172:173], v35 offset:0\n" \
"ds_read_b64_tr_b16 v[174:175], v35 offset:1024\n" \
"v_mfma_f32_16x16x32_bf16 a[252:255], v[112:115], v[176:179], a[252:255]\n" \
"s_add_u32 m0, %[mA], 0x9c00\n" \
"s_waitcnt lgkmcnt(14)\n" \
"s_nop 0\n" \
"v_smfmac_f32_16x16x64_bf16 a[16:19], v[140:143], v[84:91], v50\n" \
"v_mfma_f32_16x16x32_bf16 a[48:51], v[128:131], v[88:91], 0\n" \
"s_add_u32 s85, s62, 0xfc00\n" \
"ds_read_b64_tr_b16 v[176:177], v35 offset:8192\n" \
"v_mfma_f32_16x16x32_bf16 a[48:51], v[124:127], v[84:87], a[48:51]\n" \
"buffer_load_dwordx4 v25, s[76:79], s85 offen lds\n" \
"v_mfma_f32_16x16x32_bf16 a[80:83], v[84:87], v[132:135], 0\n" \
"s_add_u32 m0, %[mB], 0x8c00\n" \
"s_add_u32 s84, s63, 0x18000\n" \
"v_mfma_f32_16x16x32_bf16 a[80:83], v[88:91], v[136:139], a[80:83]\n" \
"ds_read_b64_tr_b16 v[178:179], v35 offset:9216\n" \
"v_cvt_pk_bf16_f32 v224, v224, v225\n" \
"v_smfmac_f32_16x16x64_bf16 a[112:115], v[140:143], v[52:59], v50\n" \
"v_mfma_f32_16x16x32_bf16 a[20:23], v[128:131], v[56:59], 0\n" \
"buffer_load_dwordx4 v27, s[68:71], s84 offen lds\n" \
"v_mfma_f32_16x16x32_bf16 a[20:23], v[124:127], v[52:55], a[20:23]\n" \
"s_add_u32 s88, s44, 0x0\n" \
"s_nop 0\n" \
"v_accvgpr_read_b32 v180, a68\n" \
"v_mfma_f32_16x16x32_bf16 a[52:55], v[116:119], v[52:55], 0\n" \
"v_accvgpr_read_b32 v181, a69\n" \
"v_accvgpr_read_b32 v182, a70\n" \
"v_mfma_f32_16x16x32_bf16 a[52:55], v[120:123], v[56:59], a[52:55]\n" \
"buffer_store_dwordx4 v[184:187], v20, s[80:83], s88 offen offset:0 sc1\n" \
"v_mfma_f32_16x16x32_bf16 a[84:87], v[56:59], v[136:139], 0\n" \
"v_accvgpr_read_b32 v183, a71\n" \
"v_cvt_pk_bf16_f32 v225, v226, v227\n" \
"v_mfma_f32_16x16x32_bf16 a[84:87], v[52:55], v[132:135], a[84:87]\n" \
"v_accvgpr_read_b32 v228, a96\n" \
"v_accvgpr_read_b32 v229, a97\n" \
"v_smfmac_f32_16x16x64_bf16 a[116:119], v[140:143], v[60:67], v50\n" \
"v_mfma_f32_16x16x32_bf16 a[24:27], v[124:127], v[60:63], 0\n" \
"v_cvt_pk_bf16_f32 v226, v180, v181\n" \
"v_cvt_pk_bf16_f32 v227, v182, v183\n" \
"v_mfma_f32_16x16x32_bf16 a[24:27], v[128:131], v[64:67], a[24:27]\n" \
"s_add_u32 s89, s44, 0x1000\n" \
"v_accvgpr_read_b32 v230, a98\n" \
"v_mfma_f32_16x16x32_bf16 a[56:59], v[120:123], v[64:67], 0\n" \
"buffer_store_dwordx4 v[188:191], v20, s[80:83], s89 offen offset:0 sc1\n" \
"v_mfma_f32_16x16x32_bf16 a[56:59], v[116:119], v[60:63], a[56:59]\n" \
"v_accvgpr_read_b32 v231, a99\n" \
"v_cvt_pk_bf16_f32 v228, v228, v229\n" \
"v_mfma_f32_16x16x32_bf16 a[88:91], v[60:63], v[132:135], 0\n" \
"v_accvgpr_read_b32 v180, a100\n" \
"v_accvgpr_read_b32 v181, a101\n" \
"v_mfma_f32_16x16x32_bf16 a[88:91], v[64:67], v[136:139], a[88:91]\n" \
"v_accvgpr_read_b32 v182, a102\n" \
"v_accvgpr_read_b32 v183, a103\n" \
"v_smfmac_f32_16x16x64_bf16 a[120:123], v[140:143], v[68:75], v50\n" \
"v_mfma_f32_16x16x32_bf16 a[28:31], v[128:131], v[72:75], 0\n" \
"v_cvt_pk_bf16_f32 v229, v230, v231\n" \
"v_accvgpr_read_b32 v232, a8\n" \
"v_mfma_f32_16x16x32_bf16 a[28:31], v[124:127], v[68:71], a[28:31]\n" \
"v_accvgpr_read_b32 v233, a9\n" \
"v_cvt_pk_bf16_f32 v230, v180, v181\n" \
"v_mfma_f32_16x16x32_bf16 a[60:63], v[116:119], v[68:71], 0\n" \
"s_add_u32 s88, s44, 0x0\n" \
"s_nop 0\n" \
"v_cvt_pk_bf16_f32 v231, v182, v183\n" \
"v_mfma_f32_16x16x32_bf16 a[60:63], v[120:123], v[72:75], a[60:63]\n" \
"buffer_store_dwordx4 v[192:195], v20, s[80:83], s88 offen offset:1024 sc1\n" \
"v_mfma_f32_16x16x32_bf16 a[92:95], v[72:75], v[136:139], 0\n" \
"v_accvgpr_read_b32 v234, a10\n" \
"v_accvgpr_read_b32 v235, a11\n" \
"v_mfma_f32_16x16x32_bf16 a[92:95], v[68:71], v[132:135], a[92:95]\n" \
"v_cvt_pk_bf16_f32 v232, v232, v233\n" \
"v_accvgpr_read_b32 v180, a12\n" \
"v_smfmac_f32_16x16x64_bf16 a[124:127], v[140:143], v[76:83], v50\n" \
"v_mfma_f32_16x16x32_bf16 a[0:3], v[124:127], v[76:79], 0\n" \
"v_accvgpr_read_b32 v181, a13\n" \
"v_accvgpr_read_b32 v182, a14\n" \
"v_mfma_f32_16x16x32_bf16 a[0:3], v[128:131], v[80:83], a[0:3]\n" \
"v_accvgpr_read_b32 v183, a15\n" \
"v_cvt_pk_bf16_f32 v233, v234, v235\n" \
"v_mfma_f32_16x16x32_bf16 a[4:7], v[120:123], v[80:83], 0\n" \
"v_accvgpr_read_b32 v236, a40\n" \
"v_accvgpr_read_b32 v237, a41\n" \
"v_mfma_f32_16x16x32_bf16 a[4:7], v[116:119], v[76:79], a[4:7]\n" \
"v_cvt_pk_bf16_f32 v234, v180, v181\n" \
"v_cvt_pk_bf16_f32 v235, v182, v183\n" \
"v_mfma_f32_16x16x32_bf16 a[32:35], v[76:79], v[132:135], 0\n" \
"v_accvgpr_read_b32 v238, a42\n" \
"v_accvgpr_read_b32 v239, a43\n" \
"v_mfma_f32_16x16x32_bf16 a[32:35], v[80:83], v[136:139], a[32:35]\n" \
"v_cvt_pk_bf16_f32 v236, v236, v237\n" \
"v_accvgpr_read_b32 v180, a44\n" \
"v_accvgpr_read_b32 v181, a45\n" \
"v_accvgpr_read_b32 v182, a46\n" \
"v_accvgpr_read_b32 v183, a47\n" \
"v_cvt_pk_bf16_f32 v237, v238, v239\n" \
"v_accvgpr_read_b32 v240, a72\n" \
"v_accvgpr_read_b32 v241, a73\n" \
"v_cvt_pk_bf16_f32 v238, v180, v181\n" \
"v_cvt_pk_bf16_f32 v239, v182, v183\n" \
"v_accvgpr_read_b32 v242, a74\n" \
"v_accvgpr_read_b32 v243, a75\n" \
"v_cvt_pk_bf16_f32 v240, v240, v241\n" \
"v_accvgpr_read_b32 v180, a76\n" \
"v_accvgpr_read_b32 v181, a77\n" \
"v_accvgpr_read_b32 v182, a78\n" \
"v_accvgpr_read_b32 v183, a79\n" \
"v_cvt_pk_bf16_f32 v241, v242, v243\n" \
"v_accvgpr_read_b32 v244, a104\n" \
"v_accvgpr_read_b32 v245, a105\n" \
"v_cvt_pk_bf16_f32 v242, v180, v181\n" \
"v_cvt_pk_bf16_f32 v243, v182, v183\n" \
"v_accvgpr_read_b32 v246, a106\n" \
"v_accvgpr_read_b32 v247, a107\n" \
"v_cvt_pk_bf16_f32 v244, v244, v245\n" \
"v_accvgpr_read_b32 v248, a128\n" \
"v_accvgpr_read_b32 v249, a129\n" \
"v_accvgpr_read_b32 v250, a130\n" \
"v_accvgpr_read_b32 v251, a131\n" \
"s_waitcnt lgkmcnt(0)\n" \
"s_nop 0\n" \
"v_mfma_f32_16x16x32_bf16 a[128:131], v[152:155], v[56:59], 0\n" \
"v_accvgpr_read_b32 v180, a108\n" \
"v_accvgpr_read_b32 v181, a109\n" \
"v_accvgpr_read_b32 v182, a110\n" \
"v_accvgpr_read_b32 v183, a111\n" \
"v_cvt_pk_bf16_f32 v245, v246, v247\n" \
"v_cvt_pk_bf16_f32 v246, v180, v181\n" \
"v_cvt_pk_bf16_f32 v247, v182, v183\n" \
"v_cvt_pk_bf16_f32 v248, v248, v249\n" \
"v_mfma_f32_16x16x32_bf16 a[128:131], v[148:151], v[52:55], a[128:131]\n" \
"v_accvgpr_read_b32 v180, a132\n" \
"v_accvgpr_read_b32 v181, a133\n" \
"v_accvgpr_read_b32 v182, a134\n" \
"v_accvgpr_read_b32 v183, a135\n" \
"v_cvt_pk_bf16_f32 v249, v250, v251\n" \
"v_accvgpr_read_b32 v252, a160\n" \
"v_accvgpr_read_b32 v253, a161\n" \
"v_cvt_pk_bf16_f32 v250, v180, v181\n" \
"v_cvt_pk_bf16_f32 v251, v182, v183\n" \
"v_accvgpr_read_b32 v254, a162\n" \
"v_accvgpr_read_b32 v255, a163\n" \
"v_cvt_pk_bf16_f32 v252, v252, v253\n" \
"v_accvgpr_read_b32 v180, a164\n" \
"v_accvgpr_read_b32 v181, a165\n" \
"v_accvgpr_read_b32 v182, a166\n" \
"v_accvgpr_read_b32 v183, a167\n" \
"v_cvt_pk_bf16_f32 v253, v254, v255\n" \
"v_cvt_pk_bf16_f32 v254, v180, v181\n" \
"v_cvt_pk_bf16_f32 v255, v182, v183\n" \
"v_accvgpr_write_b32 a36, v252\n" \
"v_accvgpr_write_b32 a37, v253\n" \
"v_mfma_f32_16x16x32_bf16 a[132:135], v[156:159], v[52:55], 0\n" \
"v_accvgpr_write_b32 a38, v254\n" \
"v_accvgpr_write_b32 a39, v255\n" \
"v_accvgpr_read_b32 v252, a192\n" \
"v_accvgpr_read_b32 v253, a193\n" \
"v_accvgpr_read_b32 v254, a194\n" \
"v_accvgpr_read_b32 v255, a195\n" \
"v_cvt_pk_bf16_f32 v252, v252, v253\n" \
"v_accvgpr_read_b32 v180, a196\n" \
"v_accvgpr_read_b32 v181, a197\n" \
"v_accvgpr_read_b32 v182, a198\n" \
"v_accvgpr_read_b32 v183, a199\n" \
"v_cvt_pk_bf16_f32 v253, v254, v255\n" \
"v_cvt_pk_bf16_f32 v254, v180, v181\n" \
"v_cvt_pk_bf16_f32 v255, v182, v183\n" \
"v_accvgpr_write_b32 a64, v252\n" \
"v_accvgpr_write_b32 a65, v253\n" \
"v_mfma_f32_16x16x32_bf16 a[132:135], v[160:163], v[56:59], a[132:135]\n" \
"v_accvgpr_write_b32 a66, v254\n" \
"v_accvgpr_write_b32 a67, v255\n" \
"v_accvgpr_read_b32 v252, a224\n" \
"v_accvgpr_read_b32 v253, a225\n" \
"v_accvgpr_read_b32 v254, a226\n" \
"v_accvgpr_read_b32 v255, a227\n" \
"v_cvt_pk_bf16_f32 v252, v252, v253\n" \
"v_accvgpr_read_b32 v180, a228\n" \
"v_accvgpr_read_b32 v181, a229\n" \
"v_accvgpr_read_b32 v182, a230\n" \
"v_accvgpr_read_b32 v183, a231\n" \
"v_cvt_pk_bf16_f32 v253, v254, v255\n" \
"v_cvt_pk_bf16_f32 v254, v180, v181\n" \
"v_cvt_pk_bf16_f32 v255, v182, v183\n" \
"v_accvgpr_write_b32 a68, v252\n" \
"v_accvgpr_write_b32 a69, v253\n" \
"v_accvgpr_write_b32 a70, v254\n" \
"v_accvgpr_write_b32 a71, v255\n" \
"v_accvgpr_read_b32 v252, a136\n" \
"v_accvgpr_read_b32 v253, a137\n" \
"v_accvgpr_read_b32 v254, a138\n" \
"v_accvgpr_read_b32 v255, a139\n" \
"v_mfma_f32_16x16x32_bf16 a[136:139], v[168:171], v[56:59], 0\n" \
"ds_read_b128 v[84:87], v48 offset:0\n" \
"ds_read_b128 v[88:91], v49 offset:0\n" \
"ds_read_b128 v[92:95], v48 offset:2048\n" \
"v_mfma_f32_16x16x32_bf16 a[136:139], v[164:167], v[52:55], a[136:139]\n" \
"ds_read_b128 v[96:99], v49 offset:2048\n" \
"v_accvgpr_read_b32 v180, a140\n" \
"v_accvgpr_read_b32 v181, a141\n" \
"v_accvgpr_read_b32 v182, a142\n" \
"v_accvgpr_read_b32 v183, a143\n" \
"v_mfma_f32_16x16x32_bf16 a[140:143], v[172:175], v[52:55], 0\n" \
"ds_read_b128 v[100:103], v48 offset:4096\n" \
"ds_read_b128 v[104:107], v49 offset:4096\n" \
"s_add_u32 s89, s44, 0x1000\n" \
"buffer_store_dwordx4 v[196:199], v20, s[80:83], s89 offen offset:1024 sc1\n" \
"ds_read_b128 v[108:111], v48 offset:6144\n" \
"v_mfma_f32_16x16x32_bf16 a[140:143], v[176:179], v[56:59], a[140:143]\n" \
"v_cvt_pk_bf16_f32 v252, v252, v253\n" \
"v_cvt_pk_bf16_f32 v253, v254, v255\n" \
"v_mfma_f32_16x16x32_bf16 a[160:163], v[152:155], v[64:67], 0\n" \
"ds_read_b128 v[112:115], v49 offset:6144\n" \
"v_cvt_pk_bf16_f32 v254, v180, v181\n" \
"v_mfma_f32_16x16x32_bf16 a[160:163], v[148:151], v[60:63], a[160:163]\n" \
"v_cvt_pk_bf16_f32 v255, v182, v183\n" \
"v_accvgpr_write_b32 a96, v252\n" \
"v_mfma_f32_16x16x32_bf16 a[164:167], v[156:159], v[60:63], 0\n" \
"v_accvgpr_write_b32 a97, v253\n" \
"v_accvgpr_write_b32 a98, v254\n" \
"v_mfma_f32_16x16x32_bf16 a[164:167], v[160:163], v[64:67], a[164:167]\n" \
"v_accvgpr_write_b32 a99, v255\n" \
"v_accvgpr_read_b32 v252, a168\n" \
"v_accvgpr_read_b32 v253, a169\n" \
"v_accvgpr_read_b32 v254, a170\n" \
"v_accvgpr_read_b32 v255, a171\n" \
"v_mfma_f32_16x16x32_bf16 a[168:171], v[168:171], v[64:67], 0\n" \
"v_accvgpr_read_b32 v180, a172\n" \
"v_accvgpr_read_b32 v181, a173\n" \
"v_mfma_f32_16x16x32_bf16 a[168:171], v[164:167], v[60:63], a[168:171]\n" \
"v_accvgpr_read_b32 v182, a174\n" \
"v_accvgpr_read_b32 v183, a175\n" \
"v_mfma_f32_16x16x32_bf16 a[172:175], v[172:175], v[60:63], 0\n" \
"v_cvt_pk_bf16_f32 v252, v252, v253\n" \
"v_cvt_pk_bf16_f32 v253, v254, v255\n" \
"v_mfma_f32_16x16x32_bf16 a[172:175], v[176:179], v[64:67], a[172:175]\n" \
"v_cvt_pk_bf16_f32 v254, v180, v181\n" \
"v_cvt_pk_bf16_f32 v255, v182, v183\n" \
"v_mfma_f32_16x16x32_bf16 a[192:195], v[152:155], v[72:75], 0\n" \
"v_accvgpr_write_b32 a100, v252\n" \
"v_accvgpr_write_b32 a101, v253\n" \
"v_mfma_f32_16x16x32_bf16 a[192:195], v[148:151], v[68:71], a[192:195]\n" \
"v_accvgpr_write_b32 a102, v254\n" \
"v_accvgpr_write_b32 a103, v255\n" \
"s_add_u32 s62, s62, 0x80\n" \
"v_accvgpr_read_b32 v252, a200\n" \
"v_accvgpr_read_b32 v253, a201\n" \
"v_accvgpr_read_b32 v254, a202\n" \
"s_waitcnt vmcnt(4) lgkmcnt(0)\n" \
"s_barrier\n" \
"v_mfma_f32_16x16x32_bf16 a[196:199], v[156:159], v[68:71], 0\n" \
"v_accvgpr_read_b32 v255, a203\n" \
"s_add_u32 m0, s40, 0x0\n" \
"v_mfma_f32_16x16x32_bf16 a[196:199], v[160:163], v[72:75], a[196:199]\n" \
"s_cmp_lt_u32 s62, 0x480\n" \
"s_cselect_b32 s62, s62, 0\n" \
"v_mfma_f32_16x16x32_bf16 a[200:203], v[168:171], v[72:75], 0\n" \
"s_lshl_b32 s72, s62, 6\n" \
"s_add_u32 s72, s72, s37\n" \
"v_mfma_f32_16x16x32_bf16 a[200:203], v[164:167], v[68:71], a[200:203]\n" \
"s_add_u32 s85, s72, 0x0\n" \
"s_nop 0\n" \
"buffer_load_dwordx4 v20, s[80:83], s85 offen sc1 lds\n" \
"v_accvgpr_read_b32 v180, a204\n" \
"v_accvgpr_read_b32 v181, a205\n" \
"v_accvgpr_read_b32 v182, a206\n" \
"v_accvgpr_read_b32 v183, a207\n" \
"v_mfma_f32_16x16x32_bf16 a[204:207], v[172:175], v[68:71], 0\n" \
"s_add_u32 m0, s40, 0x1000\n" \
"s_add_u32 s84, s72, 0x1000\n" \
"v_mfma_f32_16x16x32_bf16 a[204:207], v[176:179], v[72:75], a[204:207]\n" \
"buffer_load_dwordx4 v20, s[80:83], s84 offen sc1 lds\n" \
"v_mfma_f32_16x16x32_bf16 a[224:227], v[152:155], v[80:83], 0\n" \
"v_cvt_pk_bf16_f32 v252, v252, v253\n" \
"v_cvt_pk_bf16_f32 v253, v254, v255\n" \
"v_mfma_f32_16x16x32_bf16 a[224:227], v[148:151], v[76:79], a[224:227]\n" \
"v_cvt_pk_bf16_f32 v254, v180, v181\n" \
"v_cvt_pk_bf16_f32 v255, v182, v183\n" \
"v_mfma_f32_16x16x32_bf16 a[228:231], v[156:159], v[76:79], 0\n" \
"v_accvgpr_write_b32 a8, v252\n" \
"v_accvgpr_write_b32 a9, v253\n" \
"v_mfma_f32_16x16x32_bf16 a[228:231], v[160:163], v[80:83], a[228:231]\n" \
"v_accvgpr_write_b32 a10, v254\n" \
"v_accvgpr_write_b32 a11, v255\n" \
"v_accvgpr_read_b32 v252, a232\n" \
"v_accvgpr_read_b32 v253, a233\n" \
"v_accvgpr_read_b32 v254, a234\n" \
"v_accvgpr_read_b32 v255, a235\n" \
"v_mfma_f32_16x16x32_bf16 a[232:235], v[168:171], v[80:83], 0\n" \
"s_add_u32 m0, s40, 0x3000\n" \
"s_add_u32 s85, s62, 0x0\n" \
"buffer_load_dwordx4 v21, s[76:79], s85 offen lds\n" \
"v_mfma_f32_16x16x32_bf16 a[232:235], v[164:167], v[76:79], a[232:235]\n" \
"v_accvgpr_read_b32 v180, a236\n" \
"v_accvgpr_read_b32 v181, a237\n" \
"v_accvgpr_read_b32 v182, a238\n" \
"v_accvgpr_read_b32 v183, a239\n" \
"v_mfma_f32_16x16x32_bf16 a[236:239], v[172:175], v[76:79], 0\n" \
"v_cvt_pk_bf16_f32 v252, v252, v253\n" \
"s_add_u32 m0, %[mA], 0x0\n" \
"v_mfma_f32_16x16x32_bf16 a[236:239], v[176:179], v[80:83], a[236:239]\n" \
"s_add_u32 s84, s62, 0x0\n" \
"s_nop 0\n" \
"v_cvt_pk_bf16_f32 v253, v254, v255\n" \
"v_cvt_pk_bf16_f32 v254, v180, v181\n" \
"v_cvt_pk_bf16_f32 v255, v182, v183\n" \
"v_accvgpr_write_b32 a12, v252\n" \
"v_accvgpr_write_b32 a13, v253\n" \
"v_accvgpr_write_b32 a14, v254\n" \
"v_accvgpr_write_b32 a15, v255\n" \
"v_accvgpr_read_b32 v252, a144\n" \
"v_accvgpr_read_b32 v253, a145\n" \
"v_accvgpr_read_b32 v254, a146\n" \
"v_accvgpr_read_b32 v255, a147\n" \
"v_mfma_f32_16x16x32_bf16 a[144:147], v[152:155], v[88:91], 0\n" \
"buffer_load_dwordx4 v24, s[76:79], s84 offen lds\n" \
"v_mfma_f32_16x16x32_bf16 a[144:147], v[148:151], v[84:87], a[144:147]\n" \
"v_cvt_pk_bf16_f32 v252, v252, v253\n" \
"v_accvgpr_read_b32 v180, a176\n" \
"v_accvgpr_read_b32 v181, a177\n" \
"v_accvgpr_read_b32 v182, a178\n" \
"v_accvgpr_read_b32 v183, a179\n" \
"v_cvt_pk_bf16_f32 v253, v254, v255\n" \
"v_cvt_pk_bf16_f32 v254, v180, v181\n" \
"v_cvt_pk_bf16_f32 v255, v182, v183\n" \
"v_accvgpr_write_b32 a40, v252\n" \
"v_accvgpr_write_b32 a41, v253\n" \
"v_accvgpr_write_b32 a42, v254\n" \
"v_accvgpr_write_b32 a43, v255\n" \
"v_accvgpr_read_b32 v252, a148\n" \
"v_accvgpr_read_b32 v253, a149\n" \
"v_accvgpr_read_b32 v254, a150\n" \
"v_accvgpr_read_b32 v255, a151\n" \
"v_mfma_f32_16x16x32_bf16 a[148:151], v[156:159], v[84:87], 0\n" \
"v_cvt_pk_bf16_f32 v252, v252, v253\n" \
"s_add_u32 m0, %[mA], 0x400\n" \
"v_mfma_f32_16x16x32_bf16 a[148:151], v[160:163], v[88:91], a[148:151]\n" \
"s_add_u32 s85, s62, 0x2400\n" \
"v_accvgpr_read_b32 v180, a180\n" \
"v_accvgpr_read_b32 v181, a181\n" \
"v_accvgpr_read_b32 v182, a182\n" \
"v_accvgpr_read_b32 v183, a183\n" \
"v_cvt_pk_bf16_f32 v253, v254, v255\n" \
"v_cvt_pk_bf16_f32 v254, v180, v181\n" \
"v_cvt_pk_bf16_f32 v255, v182, v183\n" \
"v_accvgpr_write_b32 a44, v252\n" \
"v_accvgpr_write_b32 a45, v253\n" \
"v_accvgpr_write_b32 a46, v254\n" \
"v_accvgpr_write_b32 a47, v255\n" \
"v_accvgpr_read_b32 v252, a152\n" \
"v_accvgpr_read_b32 v253, a153\n" \
"v_accvgpr_read_b32 v254, a154\n" \
"v_accvgpr_read_b32 v255, a155\n" \
"v_mfma_f32_16x16x32_bf16 a[152:155], v[168:171], v[88:91], 0\n" \
"buffer_load_dwordx4 v25, s[76:79], s85 offen lds\n" \
"v_mfma_f32_16x16x32_bf16 a[152:155], v[164:167], v[84:87], a[152:155]\n" \
"v_cvt_pk_bf16_f32 v252, v252, v253\n" \
"v_accvgpr_read_b32 v180, a184\n" \
"L_pf5:\n" \
"s_cbranch_execz L_pf9\n" \
"s_nop 0\n" \
"v_accvgpr_read_b32 v181, a185\n" \
"v_accvgpr_read_b32 v182, a186\n" \
"v_accvgpr_read_b32 v183, a187\n" \
"v_cvt_pk_bf16_f32 v253, v254, v255\n" \
"v_cvt_pk_bf16_f32 v254, v180, v181\n" \
"v_cvt_pk_bf16_f32 v255, v182, v183\n" \
"s_lshl_b32 s63, s62, 12\n" \
"v_accvgpr_write_b32 a72, v252\n" \
"v_accvgpr_write_b32 a73, v253\n" \
"v_accvgpr_write_b32 a74, v254\n" \
"v_accvgpr_write_b32 a75, v255\n" \
"v_accvgpr_read_b32 v252, a156\n" \
"v_accvgpr_read_b32 v253, a157\n" \
"v_accvgpr_read_b32 v254, a158\n" \
"v_accvgpr_read_b32 v255, a159\n" \
"v_mfma_f32_16x16x32_bf16 a[156:159], v[172:175], v[84:87], 0\n" \
"s_add_u32 m0, %[mB], 0x0\n" \
"s_add_u32 s84, s63, 0x0\n" \
"s_nop 0\n" \
"buffer_load_dwordx4 v26, s[68:71], s84 offen lds\n" \
"v_mfma_f32_16x16x32_bf16 a[156:159], v[176:179], v[88:91], a[156:159]\n" \
"s_add_u32 m0, %[mA], 0x800\n" \
"s_add_u32 s85, s62, 0x4800\n" \
"v_mfma_f32_16x16x32_bf16 a[176:179], v[152:155], v[96:99], 0\n" \
"v_accvgpr_read_b32 v180, a188\n" \
"v_accvgpr_read_b32 v181, a189\n" \
"v_mfma_f32_16x16x32_bf16 a[176:179], v[148:151], v[92:95], a[176:179]\n" \
"buffer_load_dwordx4 v24, s[76:79], s85 offen lds\n" \
"v_mfma_f32_16x16x32_bf16 a[180:183], v[156:159], v[92:95], 0\n" \
"s_add_u32 m0, %[mA], 0xc00\n" \
"s_add_u32 s84, s62, 0x6c00\n" \
"v_mfma_f32_16x16x32_bf16 a[180:183], v[160:163], v[96:99], a[180:183]\n" \
"v_accvgpr_read_b32 v182, a190\n" \
"v_accvgpr_read_b32 v183, a191\n" \
"v_mfma_f32_16x16x32_bf16 a[184:187], v[168:171], v[96:99], 0\n" \
"buffer_load_dwordx4 v25, s[76:79], s84 offen lds\n" \
"v_mfma_f32_16x16x32_bf16 a[184:187], v[164:167], v[92:95], a[184:187]\n" \
"v_cvt_pk_bf16_f32 v252, v252, v253\n" \
"v_cvt_pk_bf16_f32 v253, v254, v255\n" \
"v_mfma_f32_16x16x32_bf16 a[188:191], v[172:175], v[92:95], 0\n" \
"v_cvt_pk_bf16_f32 v254, v180, v181\n" \
"v_cvt_pk_bf16_f32 v255, v182, v183\n" \
"v_mfma_f32_16x16x32_bf16 a[188:191], v[176:179], v[96:99], a[188:191]\n" \
"v_accvgpr_write_b32 a76, v252\n" \
"v_accvgpr_write_b32 a77, v253\n" \
"v_accvgpr_write_b32 a78, v254\n" \
"v_accvgpr_write_b32 a79, v255\n" \
"v_accvgpr_read_b32 v252, a208\n" \
"v_accvgpr_read_b32 v253, a209\n" \
"v_accvgpr_read_b32 v254, a210\n" \
"v_accvgpr_read_b32 v255, a211\n" \
"v_mfma_f32_16x16x32_bf16 a[208:211], v[152:155], v[104:107], 0\n" \
"v_cvt_pk_bf16_f32 v252, v252, v253\n" \
"s_add_u32 m0, %[mB], 0x400\n" \
"v_mfma_f32_16x16x32_bf16 a[208:211], v[148:151], v[100:103], a[208:211]\n" \
"s_add_u32 s85, s63, 0x8000\n" \
"v_accvgpr_read_b32 v180, a240\n" \
"v_accvgpr_read_b32 v181, a241\n" \
"v_accvgpr_read_b32 v182, a242\n" \
"v_accvgpr_read_b32 v183, a243\n" \
"v_cvt_pk_bf16_f32 v253, v254, v255\n" \
"v_cvt_pk_bf16_f32 v254, v180, v181\n" \
"v_cvt_pk_bf16_f32 v255, v182, v183\n" \
"v_accvgpr_write_b32 a104, v252\n" \
"v_accvgpr_write_b32 a105, v253\n" \
"v_accvgpr_write_b32 a106, v254\n" \
"v_accvgpr_write_b32 a107, v255\n" \
"v_accvgpr_read_b32 v252, a212\n" \
"v_accvgpr_read_b32 v253, a213\n" \
"v_accvgpr_read_b32 v254, a214\n" \
"v_accvgpr_read_b32 v255, a215\n" \
"v_mfma_f32_16x16x32_bf16 a[212:215], v[156:159], v[100:103], 0\n" \
"buffer_load_dwordx4 v26, s[68:71], s85 offen lds\n" \
"ds_read_b64_tr_b16 v[140:141], v39 offset:32768\n" \
"ds_read_b64_tr_b16 v[142:143], v39 offset:33728\n" \
"ds_read_b128 v[84:87], v22 offset:32768\n" \
"ds_read_b128 v[88:91], v23 offset:32768\n" \
"v_mfma_f32_16x16x32_bf16 a[212:215], v[160:163], v[104:107], a[212:215]\n" \
"ds_read_b64_tr_b16 v[128:129], v37 offset:36864\n" \
"ds_read_b64_tr_b16 v[130:131], v37 offset:37376\n" \
"v_accvgpr_read_b32 v184, a216\n" \
"v_accvgpr_read_b32 v185, a217\n" \
"v_accvgpr_read_b32 v186, a218\n" \
"v_accvgpr_read_b32 v187, a219\n" \
"v_mfma_f32_16x16x32_bf16 a[216:219], v[168:171], v[104:107], 0\n" \
"s_add_u32 m0, %[mA], 0x1000\n" \
"s_add_u32 s84, s62, 0x9000\n" \
"buffer_load_dwordx4 v24, s[76:79], s84 offen lds\n" \
"ds_read_b64_tr_b16 v[124:125], v37 offset:32768\n" \
"ds_read_b64_tr_b16 v[126:127], v37 offset:33280\n" \
"v_mfma_f32_16x16x32_bf16 a[216:219], v[164:167], v[100:103], a[216:219]\n" \
"v_cvt_pk_bf16_f32 v252, v252, v253\n" \
"v_accvgpr_read_b32 v180, a244\n" \
"v_accvgpr_read_b32 v181, a245\n" \
"v_accvgpr_read_b32 v182, a246\n" \
"v_accvgpr_read_b32 v183, a247\n" \
"v_cvt_pk_bf16_f32 v253, v254, v255\n" \
"v_cvt_pk_bf16_f32 v254, v180, v181\n" \
"v_cvt_pk_bf16_f32 v255, v182, v183\n" \
"v_accvgpr_write_b32 a108, v252\n" \
"v_accvgpr_write_b32 a109, v253\n" \
"v_accvgpr_write_b32 a110, v254\n" \
"v_accvgpr_write_b32 a111, v255\n" \
"v_accvgpr_read_b32 v252, a220\n" \
"v_accvgpr_read_b32 v253, a221\n" \
"v_accvgpr_read_b32 v254, a222\n" \
"v_accvgpr_read_b32 v255, a223\n" \
"v_mfma_f32_16x16x32_bf16 a[220:223], v[172:175], v[100:103], 0\n" \
"ds_read_b64_tr_b16 v[132:133], v38 offset:32768\n" \
"ds_read_b64_tr_b16 v[134:135], v38 offset:33280\n" \
"ds_read_b64_tr_b16 v[136:137], v38 offset:36864\n" \
"ds_read_b64_tr_b16 v[138:139], v38 offset:37376\n" \
"v_mfma_f32_16x16x32_bf16 a[220:223], v[176:179], v[104:107], a[220:223]\n" \
"ds_read_b128 v[52:55], v28 offset:32768\n" \
"ds_read_b128 v[56:59], v29 offset:32768\n" \
"v_mfma_f32_16x16x32_bf16 a[240:243], v[152:155], v[112:115], 0\n" \
"s_add_u32 m0, %[mA], 0x1400\n" \
"s_add_u32 s85, s62, 0xb400\n" \
"buffer_load_dwordx4 v25, s[76:79], s85 offen lds\n" \
"v_mfma_f32_16x16x32_bf16 a[240:243], v[148:151], v[108:111], a[240:243]\n" \
"ds_read_b64_tr_b16 v[116:117], v36 offset:32768\n" \
"ds_read_b64_tr_b16 v[118:119], v36 offset:33280\n" \
"v_mfma_f32_16x16x32_bf16 a[244:247], v[156:159], v[108:111], 0\n" \
"s_add_u32 m0, %[mB], 0x800\n" \
"s_add_u32 s84, s63, 0x10000\n" \
"v_mfma_f32_16x16x32_bf16 a[244:247], v[160:163], v[112:115], a[244:247]\n" \
"buffer_load_dwordx4 v27, s[68:71], s84 offen lds\n" \
"ds_read_b64_tr_b16 v[120:121], v36 offset:36864\n" \
"ds_read_b64_tr_b16 v[122:123], v36 offset:37376\n" \
"v_accvgpr_read_b32 v180, a248\n" \
"v_accvgpr_read_b32 v181, a249\n" \
"v_accvgpr_read_b32 v182, a250\n" \
"v_accvgpr_read_b32 v183, a251\n" \
"v_mfma_f32_16x16x32_bf16 a[248:251], v[168:171], v[112:115], 0\n" \
"ds_read_b128 v[60:63], v28 offset:34816\n" \
"v_cvt_pk_bf16_f32 v184, v184, v185\n" \
"v_mfma_f32_16x16x32_bf16 a[248:251], v[164:167], v[108:111], a[248:251]\n" \
"v_cvt_pk_bf16_f32 v185, v186, v187\n" \
"v_cvt_pk_bf16_f32 v186, v180, v181\n" \
"v_cvt_pk_bf16_f32 v187, v182, v183\n" \
"v_accvgpr_read_b32 v180, a252\n" \
"v_accvgpr_read_b32 v181, a253\n" \
"v_accvgpr_read_b32 v182, a254\n" \
"v_accvgpr_read_b32 v183, a255\n" \
"v_mfma_f32_16x16x32_bf16 a[252:255], v[172:175], v[108:111], 0\n" \
"ds_read_b128 v[64:67], v29 offset:34816\n" \
"s_add_u32 m0, %[mA], 0x1800\n" \
"v_mfma_f32_16x16x32_bf16 a[252:255], v[176:179], v[112:115], a[252:255]\n" \
"s_add_u32 s85, s62, 0xd800\n" \
"buffer_load_dwordx4 v24, s[76:79], s85 offen lds\n" \
"s_waitcnt lgkmcnt(12)\n" \
"v_smfmac_f32_16x16x64_bf16 a[16:19], v[140:143], v[84:91], v50\n" \
"v_mfma_f32_16x16x32_bf16 a[48:51], v[128:131], v[88:91], a[48:51]\n" \
"s_add_u32 m0, %[mA], 0x1c00\n" \
"s_add_u32 s85, s62, 0xfc00\n" \
"v_mfma_f32_16x16x32_bf16 a[48:51], v[124:127], v[84:87], a[48:51]\n" \
"ds_read_b128 v[68:71], v28 offset:36864\n" \
"s_waitcnt lgkmcnt(8)\n" \
"v_mfma_f32_16x16x32_bf16 a[80:83], v[84:87], v[132:135], a[80:83]\n" \
"buffer_load_dwordx4 v25, s[76:79], s85 offen lds\n" \
"v_mfma_f32_16x16x32_bf16 a[80:83], v[88:91], v[136:139], a[80:83]\n" \
"ds_read_b128 v[72:75], v29 offset:36864\n" \
"s_waitcnt lgkmcnt(6)\n" \
"v_smfmac_f32_16x16x64_bf16 a[112:115], v[140:143], v[52:59], v50\n" \
"v_mfma_f32_16x16x32_bf16 a[20:23], v[128:131], v[56:59], a[20:23]\n" \
"s_add_u32 m0, %[mB], 0xc00\n" \
"s_add_u32 s84, s63, 0x18000\n" \
"buffer_load_dwordx4 v27, s[68:71], s84 offen lds\n" \
"v_mfma_f32_16x16x32_bf16 a[20:23], v[124:127], v[52:55], a[20:23]\n" \
"ds_read_b128 v[76:79], v28 offset:38912\n" \
"ds_read_b128 v[80:83], v29 offset:38912\n" \
"v_mfma_f32_16x16x32_bf16 a[52:55], v[116:119], v[52:55], a[52:55]\n" \
"s_add_u32 s88, s44, 0x0\n" \
"s_waitcnt lgkmcnt(5)\n" \
"v_mfma_f32_16x16x32_bf16 a[52:55], v[120:123], v[56:59], a[52:55]\n" \
"buffer_store_dwordx4 v[200:203], v20, s[80:83], s88 offen offset:2048 sc1\n" \
"v_mfma_f32_16x16x32_bf16 a[84:87], v[56:59], v[136:139], a[84:87]\n" \
"ds_read_b64_tr_b16 v[152:153], v32 offset:40960\n" \
"ds_read_b64_tr_b16 v[154:155], v32 offset:41984\n" \
"v_mfma_f32_16x16x32_bf16 a[84:87], v[52:55], v[132:135], a[84:87]\n" \
"ds_read_b64_tr_b16 v[148:149], v32 offset:32768\n" \
"s_waitcnt lgkmcnt(7)\n" \
"v_smfmac_f32_16x16x64_bf16 a[116:119], v[140:143], v[60:67], v50\n" \
"v_mfma_f32_16x16x32_bf16 a[24:27], v[124:127], v[60:63], a[24:27]\n" \
"ds_read_b64_tr_b16 v[150:151], v32 offset:33792\n" \
"ds_read_b64_tr_b16 v[156:157], v33 offset:32768\n" \
"v_mfma_f32_16x16x32_bf16 a[24:27], v[128:131], v[64:67], a[24:27]\n" \
"ds_read_b64_tr_b16 v[158:159], v33 offset:33792\n" \
"ds_read_b64_tr_b16 v[160:161], v33 offset:40960\n" \
"v_mfma_f32_16x16x32_bf16 a[56:59], v[120:123], v[64:67], a[56:59]\n" \
"ds_read_b64_tr_b16 v[162:163], v33 offset:41984\n" \
"s_add_u32 s89, s44, 0x1000\n" \
"v_mfma_f32_16x16x32_bf16 a[56:59], v[116:119], v[60:63], a[56:59]\n" \
"buffer_store_dwordx4 v[204:207], v20, s[80:83], s89 offen offset:2048 sc1\n" \
"v_mfma_f32_16x16x32_bf16 a[88:91], v[60:63], v[132:135], a[88:91]\n" \
"ds_read_b64_tr_b16 v[168:169], v34 offset:40960\n" \
"ds_read_b64_tr_b16 v[170:171], v34 offset:41984\n" \
"v_mfma_f32_16x16x32_bf16 a[88:91], v[64:67], v[136:139], a[88:91]\n" \
"ds_read_b64_tr_b16 v[164:165], v34 offset:32768\n" \
"s_waitcnt lgkmcnt(13)\n" \
"v_smfmac_f32_16x16x64_bf16 a[120:123], v[140:143], v[68:75], v50\n" \
"v_mfma_f32_16x16x32_bf16 a[28:31], v[128:131], v[72:75], a[28:31]\n" \
"ds_read_b64_tr_b16 v[166:167], v34 offset:33792\n" \
"ds_read_b64_tr_b16 v[172:173], v35 offset:32768\n" \
"v_mfma_f32_16x16x32_bf16 a[28:31], v[124:127], v[68:71], a[28:31]\n" \
"ds_read_b64_tr_b16 v[174:175], v35 offset:33792\n" \
"ds_read_b64_tr_b16 v[176:177], v35 offset:40960\n" \
"v_mfma_f32_16x16x32_bf16 a[60:63], v[116:119], v[68:71], a[60:63]\n" \
"ds_read_b64_tr_b16 v[178:179], v35 offset:41984\n" \
"s_add_u32 s88, s44, 0x0\n" \
"v_mfma_f32_16x16x32_bf16 a[60:63], v[120:123], v[72:75], a[60:63]\n" \
"buffer_store_dwordx4 v[208:211], v20, s[80:83], s88 offen offset:3072 sc1\n" \
"v_mfma_f32_16x16x32_bf16 a[92:95], v[72:75], v[136:139], a[92:95]\n" \
"ds_read_b128 v[84:87], v48 offset:32768\n" \
"ds_read_b128 v[88:91], v49 offset:32768\n" \
"v_mfma_f32_16x16x32_bf16 a[92:95], v[68:71], v[132:135], a[92:95]\n" \
"ds_read_b128 v[92:95], v48 offset:34816\n" \
"s_waitcnt lgkmcnt(15)\n" \
"s_nop 0\n" \
"v_smfmac_f32_16x16x64_bf16 a[124:127], v[140:143], v[76:83], v50\n" \
"v_mfma_f32_16x16x32_bf16 a[0:3], v[124:127], v[76:79], a[0:3]\n" \
"ds_read_b128 v[96:99], v49 offset:34816\n" \
"ds_read_b128 v[100:103], v48 offset:36864\n" \
"v_mfma_f32_16x16x32_bf16 a[0:3], v[128:131], v[80:83], a[0:3]\n" \
"ds_read_b128 v[104:107], v49 offset:36864\n" \
"ds_read_b128 v[108:111], v48 offset:38912\n" \
"v_mfma_f32_16x16x32_bf16 a[4:7], v[120:123], v[80:83], a[4:7]\n" \
"s_add_u32 s62, s62, 0x80\n" \
"ds_read_b128 v[112:115], v49 offset:38912\n" \
"v_mfma_f32_16x16x32_bf16 a[4:7], v[116:119], v[76:79], a[4:7]\n" \
"s_cmp_lt_u32 s62, 0x480\n" \
"s_cselect_b32 s62, s62, 0\n" \
"v_mfma_f32_16x16x32_bf16 a[32:35], v[76:79], v[132:135], a[32:35]\n" \
"s_lshl_b32 s72, s62, 6\n" \
"s_add_u32 s72, s72, s37\n" \
"v_mfma_f32_16x16x32_bf16 a[32:35], v[80:83], v[136:139], a[32:35]\n" \
"s_lshl_b32 s63, s62, 12\n" \
"v_cvt_pk_bf16_f32 v252, v252, v253\n" \
"v_mfma_f32_16x16x32_bf16 a[128:131], v[152:155], v[56:59], a[128:131]\n" \
"v_cvt_pk_bf16_f32 v253, v254, v255\n" \
"v_cvt_pk_bf16_f32 v254, v180, v181\n" \
"v_mfma_f32_16x16x32_bf16 a[128:131], v[148:151], v[52:55], a[128:131]\n" \
"v_cvt_pk_bf16_f32 v255, v182, v183\n" \
"s_waitcnt lgkmcnt(11)\n" \
"v_mfma_f32_16x16x32_bf16 a[132:135], v[156:159], v[52:55], a[132:135]\n" \
"v_mfma_f32_16x16x32_bf16 a[132:135], v[160:163], v[56:59], a[132:135]\n" \
"v_mfma_f32_16x16x32_bf16 a[136:139], v[168:171], v[56:59], a[136:139]\n" \
"v_mfma_f32_16x16x32_bf16 a[136:139], v[164:167], v[52:55], a[136:139]\n" \
"s_waitcnt lgkmcnt(7)\n" \
"v_mfma_f32_16x16x32_bf16 a[140:143], v[172:175], v[52:55], a[140:143]\n" \
"v_mfma_f32_16x16x32_bf16 a[140:143], v[176:179], v[56:59], a[140:143]\n" \
"v_mfma_f32_16x16x32_bf16 a[160:163], v[152:155], v[64:67], a[160:163]\n" \
"v_mfma_f32_16x16x32_bf16 a[160:163], v[148:151], v[60:63], a[160:163]\n" \
"v_mfma_f32_16x16x32_bf16 a[164:167], v[156:159], v[60:63], a[164:167]\n" \
"v_mfma_f32_16x16x32_bf16 a[164:167], v[160:163], v[64:67], a[164:167]\n" \
"v_mfma_f32_16x16x32_bf16 a[168:171], v[168:171], v[64:67], a[168:171]\n" \
"v_mfma_f32_16x16x32_bf16 a[168:171], v[164:167], v[60:63], a[168:171]\n" \
"v_mfma_f32_16x16x32_bf16 a[172:175], v[172:175], v[60:63], a[172:175]\n" \
"v_mfma_f32_16x16x32_bf16 a[172:175], v[176:179], v[64:67], a[172:175]\n" \
"v_mfma_f32_16x16x32_bf16 a[192:195], v[152:155], v[72:75], a[192:195]\n" \
"v_mfma_f32_16x16x32_bf16 a[192:195], v[148:151], v[68:71], a[192:195]\n" \
"s_waitcnt vmcnt(3) lgkmcnt(0)\n" \
"s_barrier\n" \
"v_mfma_f32_16x16x32_bf16 a[196:199], v[156:159], v[68:71], a[196:199]\n" \
"s_add_u32 m0, s40, 0x8000\n" \
"s_add_u32 s85, s72, 0x0\n" \
"s_nop 0\n" \
"v_mfma_f32_16x16x32_bf16 a[196:199], v[160:163], v[72:75], a[196:199]\n" \
"buffer_load_dwordx4 v20, s[80:83], s85 offen sc1 lds\n" \
"v_mfma_f32_16x16x32_bf16 a[200:203], v[168:171], v[72:75], a[200:203]\n" \
"s_add_u32 m0, s40, 0x9000\n" \
"s_add_u32 s84, s72, 0x1000\n" \
"v_mfma_f32_16x16x32_bf16 a[200:203], v[164:167], v[68:71], a[200:203]\n" \
"ds_read_b64_tr_b16 v[140:141], v39 offset:0\n" \
"ds_read_b64_tr_b16 v[142:143], v39 offset:960\n" \
"v_mfma_f32_16x16x32_bf16 a[204:207], v[172:175], v[68:71], a[204:207]\n" \
"buffer_load_dwordx4 v20, s[80:83], s84 offen sc1 lds\n" \
"v_mfma_f32_16x16x32_bf16 a[204:207], v[176:179], v[72:75], a[204:207]\n" \
"ds_read_b64_tr_b16 v[128:129], v37 offset:4096\n" \
"ds_read_b64_tr_b16 v[130:131], v37 offset:4608\n" \
"v_mfma_f32_16x16x32_bf16 a[224:227], v[152:155], v[80:83], a[224:227]\n" \
"s_add_u32 m0, s40, 0xb000\n" \
"s_add_u32 s85, s62, 0x0\n" \
"v_mfma_f32_16x16x32_bf16 a[224:227], v[148:151], v[76:79], a[224:227]\n" \
"buffer_load_dwordx4 v21, s[76:79], s85 offen lds\n" \
"v_mfma_f32_16x16x32_bf16 a[228:231], v[156:159], v[76:79], a[228:231]\n" \
"ds_read_b64_tr_b16 v[124:125], v37 offset:0\n" \
"ds_read_b64_tr_b16 v[126:127], v37 offset:512\n" \
"v_mfma_f32_16x16x32_bf16 a[228:231], v[160:163], v[80:83], a[228:231]\n" \
"s_add_u32 m0, %[mA], 0x8000\n" \
"s_add_u32 s84, s62, 0x0\n" \
"v_mfma_f32_16x16x32_bf16 a[232:235], v[168:171], v[80:83], a[232:235]\n" \
"buffer_load_dwordx4 v24, s[76:79], s84 offen lds\n" \
"v_mfma_f32_16x16x32_bf16 a[232:235], v[164:167], v[76:79], a[232:235]\n" \
"ds_read_b64_tr_b16 v[132:133], v38 offset:0\n" \
"ds_read_b64_tr_b16 v[134:135], v38 offset:512\n" \
"v_mfma_f32_16x16x32_bf16 a[236:239], v[172:175], v[76:79], a[236:239]\n" \
"ds_read_b64_tr_b16 v[136:137], v38 offset:4096\n" \
"ds_read_b64_tr_b16 v[138:139], v38 offset:4608\n" \
"v_mfma_f32_16x16x32_bf16 a[236:239], v[176:179], v[80:83], a[236:239]\n" \
"s_add_u32 m0, %[mA], 0x8400\n" \
"s_add_u32 s85, s62, 0x2400\n" \
"v_mfma_f32_16x16x32_bf16 a[144:147], v[152:155], v[88:91], a[144:147]\n" \
"buffer_load_dwordx4 v25, s[76:79], s85 offen lds\n" \
"v_mfma_f32_16x16x32_bf16 a[144:147], v[148:151], v[84:87], a[144:147]\n" \
"ds_read_b128 v[52:55], v28 offset:0\n" \
"ds_read_b128 v[56:59], v29 offset:0\n" \
"v_mfma_f32_16x16x32_bf16 a[148:151], v[156:159], v[84:87], a[148:151]\n" \
"s_add_u32 m0, %[mB], 0x8000\n" \
"s_add_u32 s84, s63, 0x0\n" \
"s_nop 0\n" \
"v_mfma_f32_16x16x32_bf16 a[148:151], v[160:163], v[88:91], a[148:151]\n" \
"buffer_load_dwordx4 v26, s[68:71], s84 offen lds\n" \
"v_mfma_f32_16x16x32_bf16 a[152:155], v[168:171], v[88:91], a[152:155]\n" \
"ds_read_b64_tr_b16 v[116:117], v36 offset:0\n" \
"ds_read_b64_tr_b16 v[118:119], v36 offset:512\n" \
"v_mfma_f32_16x16x32_bf16 a[152:155], v[164:167], v[84:87], a[152:155]\n" \
"ds_read_b64_tr_b16 v[120:121], v36 offset:4096\n" \
"ds_read_b64_tr_b16 v[122:123], v36 offset:4608\n" \
"v_mfma_f32_16x16x32_bf16 a[156:159], v[172:175], v[84:87], a[156:159]\n" \
"s_add_u32 m0, %[mA], 0x8800\n" \
"s_add_u32 s85, s62, 0x4800\n" \
"v_mfma_f32_16x16x32_bf16 a[156:159], v[176:179], v[88:91], a[156:159]\n" \
"buffer_load_dwordx4 v24, s[76:79], s85 offen lds\n" \
"v_mfma_f32_16x16x32_bf16 a[176:179], v[152:155], v[96:99], a[176:179]\n" \
"ds_read_b128 v[84:87], v22 offset:0\n" \
"ds_read_b128 v[88:91], v23 offset:0\n" \
"v_mfma_f32_16x16x32_bf16 a[176:179], v[148:151], v[92:95], a[176:179]\n" \
"s_add_u32 m0, %[mA], 0x8c00\n" \
"s_add_u32 s84, s62, 0x6c00\n" \
"v_mfma_f32_16x16x32_bf16 a[180:183], v[156:159], v[92:95], a[180:183]\n" \
"buffer_load_dwordx4 v25, s[76:79], s84 offen lds\n" \
"v_mfma_f32_16x16x32_bf16 a[180:183], v[160:163], v[96:99], a[180:183]\n" \
"ds_read_b128 v[60:63], v28 offset:2048\n" \
"ds_read_b128 v[64:67], v29 offset:2048\n" \
"v_mfma_f32_16x16x32_bf16 a[184:187], v[168:171], v[96:99], a[184:187]\n" \
"ds_read_b128 v[68:71], v28 offset:4096\n" \
"ds_read_b128 v[72:75], v29 offset:4096\n" \
"v_mfma_f32_16x16x32_bf16 a[184:187], v[164:167], v[92:95], a[184:187]\n" \
"s_add_u32 m0, %[mB], 0x8400\n" \
"s_add_u32 s85, s63, 0x8000\n" \
"v_mfma_f32_16x16x32_bf16 a[188:191], v[172:175], v[92:95], a[188:191]\n" \
"buffer_load_dwordx4 v26, s[68:71], s85 offen lds\n" \
"v_mfma_f32_16x16x32_bf16 a[188:191], v[176:179], v[96:99], a[188:191]\n" \
"ds_read_b128 v[76:79], v28 offset:6144\n" \
"ds_read_b128 v[80:83], v29 offset:6144\n" \
"v_mfma_f32_16x16x32_bf16 a[208:211], v[152:155], v[104:107], a[208:211]\n" \
"s_add_u32 m0, %[mA], 0x9000\n" \
"s_add_u32 s84, s62, 0x9000\n" \
"v_mfma_f32_16x16x32_bf16 a[208:211], v[148:151], v[100:103], a[208:211]\n" \
"buffer_load_dwordx4 v24, s[76:79], s84 offen lds\n" \
"v_mfma_f32_16x16x32_bf16 a[212:215], v[156:159], v[100:103], a[212:215]\n" \
"ds_read_b128 v[92:95], v48 offset:2048\n" \
"ds_read_b128 v[96:99], v49 offset:2048\n" \
"v_mfma_f32_16x16x32_bf16 a[212:215], v[160:163], v[104:107], a[212:215]\n" \
"v_mfma_f32_16x16x32_bf16 a[216:219], v[168:171], v[104:107], a[216:219]\n" \
"s_add_u32 m0, %[mA], 0x9400\n" \
"s_add_u32 s85, s62, 0xb400\n" \
"v_mfma_f32_16x16x32_bf16 a[216:219], v[164:167], v[100:103], a[216:219]\n" \
"buffer_load_dwordx4 v25, s[76:79], s85 offen lds\n" \
"v_mfma_f32_16x16x32_bf16 a[220:223], v[172:175], v[100:103], a[220:223]\n" \
"ds_read_b128 v[100:103], v48 offset:4096\n" \
"v_mfma_f32_16x16x32_bf16 a[220:223], v[176:179], v[104:107], a[220:223]\n" \
"s_add_u32 m0, %[mB], 0x8800\n" \
"s_add_u32 s84, s63, 0x10000\n" \
"v_mfma_f32_16x16x32_bf16 a[240:243], v[152:155], v[112:115], a[240:243]\n" \
"buffer_load_dwordx4 v27, s[68:71], s84 offen lds\n" \
"v_mfma_f32_16x16x32_bf16 a[240:243], v[148:151], v[108:111], a[240:243]\n" \
"ds_read_b64_tr_b16 v[152:153], v32 offset:8192\n" \
"ds_read_b64_tr_b16 v[154:155], v32 offset:9216\n" \
"v_mfma_f32_16x16x32_bf16 a[244:247], v[156:159], v[108:111], a[244:247]\n" \
"ds_read_b64_tr_b16 v[148:149], v32 offset:0\n" \
"ds_read_b64_tr_b16 v[150:151], v32 offset:1024\n" \
"v_mfma_f32_16x16x32_bf16 a[244:247], v[160:163], v[112:115], a[244:247]\n" \
"s_add_u32 m0, %[mA], 0x9800\n" \
"s_add_u32 s85, s62, 0xd800\n" \
"v_mfma_f32_16x16x32_bf16 a[248:251], v[168:171], v[112:115], a[248:251]\n" \
"buffer_load_dwordx4 v24, s[76:79], s85 offen lds\n" \
"v_mfma_f32_16x16x32_bf16 a[248:251], v[164:167], v[108:111], a[248:251]\n" \
"ds_read_b64_tr_b16 v[156:157], v33 offset:0\n" \
"ds_read_b64_tr_b16 v[158:159], v33 offset:1024\n" \
"v_mfma_f32_16x16x32_bf16 a[252:255], v[172:175], v[108:111], a[252:255]\n" \
"ds_read_b64_tr_b16 v[160:161], v33 offset:8192\n" \
"ds_read_b64_tr_b16 v[162:163], v33 offset:9216\n" \
"v_mfma_f32_16x16x32_bf16 a[252:255], v[176:179], v[112:115], a[252:255]\n" \
"s_add_u32 m0, %[mA], 0x9c00\n" \
"s_waitcnt lgkmcnt(11)\n" \
"s_nop 0\n" \
"v_smfmac_f32_16x16x64_bf16 a[16:19], v[140:143], v[84:91], v50\n" \
"v_mfma_f32_16x16x32_bf16 a[48:51], v[128:131], v[88:91], a[48:51]\n" \
"s_add_u32 s85, s62, 0xfc00\n" \
"ds_read_b64_tr_b16 v[168:169], v34 offset:8192\n" \
"v_mfma_f32_16x16x32_bf16 a[48:51], v[124:127], v[84:87], a[48:51]\n" \
"buffer_load_dwordx4 v25, s[76:79], s85 offen lds\n" \
"v_mfma_f32_16x16x32_bf16 a[80:83], v[84:87], v[132:135], a[80:83]\n" \
"s_add_u32 m0, %[mB], 0x8c00\n" \
"s_add_u32 s84, s63, 0x18000\n" \
"v_mfma_f32_16x16x32_bf16 a[80:83], v[88:91], v[136:139], a[80:83]\n" \
"ds_read_b64_tr_b16 v[170:171], v34 offset:9216\n" \
"ds_read_b64_tr_b16 v[164:165], v34 offset:0\n" \
"v_smfmac_f32_16x16x64_bf16 a[112:115], v[140:143], v[52:59], v50\n" \
"v_mfma_f32_16x16x32_bf16 a[20:23], v[128:131], v[56:59], a[20:23]\n" \
"buffer_load_dwordx4 v27, s[68:71], s84 offen lds\n" \
"v_mfma_f32_16x16x32_bf16 a[20:23], v[124:127], v[52:55], a[20:23]\n" \
"s_add_u32 s89, s44, 0x1000\n" \
"ds_read_b64_tr_b16 v[166:167], v34 offset:1024\n" \
"v_mfma_f32_16x16x32_bf16 a[52:55], v[116:119], v[52:55], a[52:55]\n" \
"ds_read_b64_tr_b16 v[172:173], v35 offset:0\n" \
"ds_read_b64_tr_b16 v[174:175], v35 offset:1024\n" \
"v_mfma_f32_16x16x32_bf16 a[52:55], v[120:123], v[56:59], a[52:55]\n" \
"buffer_store_dwordx4 v[212:215], v20, s[80:83], s89 offen offset:3072 sc1\n" \
"v_mfma_f32_16x16x32_bf16 a[84:87], v[56:59], v[136:139], a[84:87]\n" \
"ds_read_b64_tr_b16 v[176:177], v35 offset:8192\n" \
"ds_read_b64_tr_b16 v[178:179], v35 offset:9216\n" \
"v_mfma_f32_16x16x32_bf16 a[84:87], v[52:55], v[132:135], a[84:87]\n" \
"ds_read_b128 v[84:87], v48 offset:0\n" \
"ds_read_b128 v[88:91], v49 offset:0\n" \
"v_smfmac_f32_16x16x64_bf16 a[116:119], v[140:143], v[60:67], v50\n" \
"v_mfma_f32_16x16x32_bf16 a[24:27], v[124:127], v[60:63], a[24:27]\n" \
"ds_read_b128 v[104:107], v49 offset:4096\n" \
"ds_read_b128 v[108:111], v48 offset:6144\n" \
"v_mfma_f32_16x16x32_bf16 a[24:27], v[128:131], v[64:67], a[24:27]\n" \
"s_add_u32 s88, s42, 0x0\n" \
"ds_read_b128 v[112:115], v49 offset:6144\n" \
"v_mfma_f32_16x16x32_bf16 a[56:59], v[120:123], v[64:67], a[56:59]\n" \
"buffer_store_dwordx4 v[216:219], v20, s[80:83], s88 offen offset:0 sc1\n" \
"v_mfma_f32_16x16x32_bf16 a[56:59], v[116:119], v[60:63], a[56:59]\n" \
"s_add_u32 s62, s62, 0x80\n" \
"s_add_u32 s94, %[b], %[w]\n" \
"v_mfma_f32_16x16x32_bf16 a[88:91], v[60:63], v[132:135], a[88:91]\n" \
"s_cmp_lt_u32 s62, 0x480\n" \
"s_cselect_b32 s62, s62, 0\n" \
"v_mfma_f32_16x16x32_bf16 a[88:91], v[64:67], v[136:139], a[88:91]\n" \
"s_lshl_b32 s72, s62, 6\n" \
"s_add_u32 s72, s72, s37\n" \
"v_smfmac_f32_16x16x64_bf16 a[120:123], v[140:143], v[68:75], v50\n" \
"v_mfma_f32_16x16x32_bf16 a[28:31], v[128:131], v[72:75], a[28:31]\n" \
"s_lshl_b32 s63, s62, 12\n" \
"s_and_b32 s96, 7, %[b]\n" \
"v_mfma_f32_16x16x32_bf16 a[28:31], v[124:127], v[68:71], a[28:31]\n" \
"s_cmp_lg_u32 s94, 0\n" \
"s_cselect_b32 s95, 0x40000000, 0x5bc0000\n" \
"v_mfma_f32_16x16x32_bf16 a[60:63], v[116:119], v[68:71], a[60:63]\n" \
"s_add_u32 s89, s42, 0x1000\n" \
"s_mul_i32 s97, 0x48, s96\n" \
"v_mfma_f32_16x16x32_bf16 a[60:63], v[120:123], v[72:75], a[60:63]\n" \
"buffer_store_dwordx4 v[220:223], v20, s[80:83], s89 offen offset:0 sc1\n" \
"v_mfma_f32_16x16x32_bf16 a[92:95], v[72:75], v[136:139], a[92:95]\n" \
"s_add_u32 s98, 0x47, s97\n" \
"v_writelane_b32 v11, s95, 50\n" \
"v_mfma_f32_16x16x32_bf16 a[92:95], v[68:71], v[132:135], a[92:95]\n" \
"s_cmp_lt_u32 %[w], 3\n" \
"s_cselect_b32 s99, s98, 0xfff\n" \
"v_smfmac_f32_16x16x64_bf16 a[124:127], v[140:143], v[76:83], v50\n" \
"v_mfma_f32_16x16x32_bf16 a[0:3], v[124:127], v[76:79], a[0:3]\n" \
"s_lshr_b32 s94, %[b], 3\n" \
"s_lshl_b32 s67, s96, 2\n" \
"v_mfma_f32_16x16x32_bf16 a[0:3], v[128:131], v[80:83], a[0:3]\n" \
"s_and_b32 s95, 3, s94\n" \
"s_mul_i32 s100, 0x480, s99\n" \
"v_mfma_f32_16x16x32_bf16 a[4:7], v[120:123], v[80:83], a[4:7]\n" \
"s_lshl_b32 s64, %[w], 5\n" \
"s_add_u32 s98, s67, s95\n" \
"v_mfma_f32_16x16x32_bf16 a[4:7], v[116:119], v[76:79], a[4:7]\n" \
"s_add_u32 s101, 0x120, s100\n" \
"s_add_u32 s65, s97, s64\n" \
"v_mfma_f32_16x16x32_bf16 a[32:35], v[76:79], v[132:135], a[32:35]\n" \
"s_mul_i32 s99, 0x24000, s98\n" \
"v_writelane_b32 v11, s101, 15\n" \
"v_mfma_f32_16x16x32_bf16 a[32:35], v[80:83], v[136:139], a[32:35]\n" \
"s_mul_i32 s66, 0x480, s65\n" \
"s_waitcnt lgkmcnt(0)\n" \
"v_mfma_f32_16x16x32_bf16 a[128:131], v[152:155], v[56:59], a[128:131]\n" \
"s_add_u32 s100, 0x1b000, s99\n" \
"s_min_u32 s97, 2, %[w]\n" \
"v_mfma_f32_16x16x32_bf16 a[128:131], v[148:151], v[52:55], a[128:131]\n" \
"s_cmp_lt_u32 %[w], 3\n" \
"s_cselect_b32 s101, s66, s100\n" \
"v_mfma_f32_16x16x32_bf16 a[132:135], v[156:159], v[52:55], a[132:135]\n" \
"s_mul_i32 s64, 0x2400, s97\n" \
"v_writelane_b32 v11, s101, 16\n" \
"v_mfma_f32_16x16x32_bf16 a[132:135], v[160:163], v[56:59], a[132:135]\n" \
"s_add_u32 s65, 0x20000, s64\n" \
"s_mul_i32 s94, 0x9000, s97\n" \
"v_mfma_f32_16x16x32_bf16 a[136:139], v[168:171], v[56:59], a[136:139]\n" \
"s_mul_i32 s95, 0x2400, %[w]\n" \
"v_writelane_b32 v11, s65, 17\n" \
"v_mfma_f32_16x16x32_bf16 a[136:139], v[164:167], v[52:55], a[136:139]\n" \
"s_add_u32 s67, s99, s94\n" \
"v_writelane_b32 v11, s67, 22\n" \
"v_mfma_f32_16x16x32_bf16 a[140:143], v[172:175], v[52:55], a[140:143]\n" \
"s_cmp_lt_u32 %[w], 3\n" \
"s_cselect_b32 s98, s95, 0x12000\n" \
"v_mfma_f32_16x16x32_bf16 a[140:143], v[176:179], v[56:59], a[140:143]\n" \
"s_add_u32 s66, 0x20000, s95\n" \
"v_writelane_b32 v11, s98, 18\n" \
"v_mfma_f32_16x16x32_bf16 a[160:163], v[152:155], v[64:67], a[160:163]\n" \
"s_cmp_lt_u32 %[w], 3\n" \
"s_cselect_b32 s100, s66, 0xfc00\n" \
"v_mfma_f32_16x16x32_bf16 a[160:163], v[148:151], v[60:63], a[160:163]\n" \
"v_writelane_b32 v11, s95, 19\n" \
"s_lshl_b32 s101, %[w], 10\n" \
"v_mfma_f32_16x16x32_bf16 a[164:167], v[156:159], v[60:63], a[164:167]\n" \
"s_lshr_b32 s65, s96, 1\n" \
"v_writelane_b32 v11, s100, 20\n" \
"v_mfma_f32_16x16x32_bf16 a[164:167], v[160:163], v[64:67], a[164:167]\n" \
"s_cmp_eq_u32 %[w], s65\n" \
"s_cselect_b32 s97, 0xff0000, 0\n" \
"v_mfma_f32_16x16x32_bf16 a[168:171], v[168:171], v[64:67], a[168:171]\n" \
"s_add_u32 s64, 0x1d400, s101\n" \
"v_writelane_b32 v11, s64, 21\n" \
"v_mfma_f32_16x16x32_bf16 a[168:171], v[164:167], v[60:63], a[168:171]\n" \
"v_writelane_b32 v11, s97, 6\n" \
"v_mfma_f32_16x16x32_bf16 a[172:175], v[172:175], v[60:63], a[172:175]\n" \
"v_mfma_f32_16x16x32_bf16 a[172:175], v[176:179], v[64:67], a[172:175]\n" \
"v_mfma_f32_16x16x32_bf16 a[192:195], v[152:155], v[72:75], a[192:195]\n" \
"v_mfma_f32_16x16x32_bf16 a[192:195], v[148:151], v[68:71], a[192:195]\n" \
"s_waitcnt vmcnt(3)\n" \
"s_barrier\n" \
"v_mfma_f32_16x16x32_bf16 a[196:199], v[156:159], v[68:71], a[196:199]\n" \
"s_add_u32 m0, s40, 0x0\n" \
"s_add_u32 s85, s72, 0x0\n" \
"s_nop 0\n" \
"v_mfma_f32_16x16x32_bf16 a[196:199], v[160:163], v[72:75], a[196:199]\n" \
"buffer_load_dwordx4 v20, s[80:83], s85 offen sc1 lds\n" \
"v_mfma_f32_16x16x32_bf16 a[200:203], v[168:171], v[72:75], a[200:203]\n" \
"s_add_u32 m0, s40, 0x1000\n" \
"s_add_u32 s84, s72, 0x1000\n" \
"v_mfma_f32_16x16x32_bf16 a[200:203], v[164:167], v[68:71], a[200:203]\n" \
"ds_read_b64_tr_b16 v[140:141], v39 offset:32768\n" \
"ds_read_b64_tr_b16 v[142:143], v39 offset:33728\n" \
"v_mfma_f32_16x16x32_bf16 a[204:207], v[172:175], v[68:71], a[204:207]\n" \
"buffer_load_dwordx4 v20, s[80:83], s84 offen sc1 lds\n" \
"v_mfma_f32_16x16x32_bf16 a[204:207], v[176:179], v[72:75], a[204:207]\n" \
"ds_read_b64_tr_b16 v[128:129], v37 offset:36864\n" \
"ds_read_b64_tr_b16 v[130:131], v37 offset:37376\n" \
"v_mfma_f32_16x16x32_bf16 a[224:227], v[152:155], v[80:83], a[224:227]\n" \
"s_add_u32 m0, s40, 0x3000\n" \
"s_add_u32 s85, s62, 0x0\n" \
"v_mfma_f32_16x16x32_bf16 a[224:227], v[148:151], v[76:79], a[224:227]\n" \
"buffer_load_dwordx4 v21, s[76:79], s85 offen lds\n" \
"v_mfma_f32_16x16x32_bf16 a[228:231], v[156:159], v[76:79], a[228:231]\n" \
"L_pf6:\n" \
"s_cbranch_execz L_pf10\n" \
"s_nop 0\n" \
"ds_read_b64_tr_b16 v[124:125], v37 offset:32768\n" \
"ds_read_b64_tr_b16 v[126:127], v37 offset:33280\n" \
"v_mfma_f32_16x16x32_bf16 a[228:231], v[160:163], v[80:83], a[228:231]\n" \
"s_add_u32 m0, %[mA], 0x0\n" \
"s_add_u32 s84, s62, 0x0\n" \
"s_nop 0\n" \
"v_mfma_f32_16x16x32_bf16 a[232:235], v[168:171], v[80:83], a[232:235]\n" \
"buffer_load_dwordx4 v24, s[76:79], s84 offen lds\n" \
"v_mfma_f32_16x16x32_bf16 a[232:235], v[164:167], v[76:79], a[232:235]\n" \
"ds_read_b64_tr_b16 v[132:133], v38 offset:32768\n" \
"ds_read_b64_tr_b16 v[134:135], v38 offset:33280\n" \
"v_mfma_f32_16x16x32_bf16 a[236:239], v[172:175], v[76:79], a[236:239]\n" \
"ds_read_b64_tr_b16 v[136:137], v38 offset:36864\n" \
"ds_read_b64_tr_b16 v[138:139], v38 offset:37376\n" \
"v_mfma_f32_16x16x32_bf16 a[236:239], v[176:179], v[80:83], a[236:239]\n" \
"s_add_u32 m0, %[mA], 0x400\n" \
"s_add_u32 s85, s62, 0x2400\n" \
"v_mfma_f32_16x16x32_bf16 a[144:147], v[152:155], v[88:91], a[144:147]\n" \
"buffer_load_dwordx4 v25, s[76:79], s85 offen lds\n" \
"v_mfma_f32_16x16x32_bf16 a[144:147], v[148:151], v[84:87], a[144:147]\n" \
"ds_read_b128 v[52:55], v28 offset:32768\n" \
"ds_read_b128 v[56:59], v29 offset:32768\n" \
"v_mfma_f32_16x16x32_bf16 a[148:151], v[156:159], v[84:87], a[148:151]\n" \
"s_add_u32 m0, %[mB], 0x0\n" \
"s_add_u32 s84, s63, 0x0\n" \
"v_mfma_f32_16x16x32_bf16 a[148:151], v[160:163], v[88:91], a[148:151]\n" \
"buffer_load_dwordx4 v26, s[68:71], s84 offen lds\n" \
"v_mfma_f32_16x16x32_bf16 a[152:155], v[168:171], v[88:91], a[152:155]\n" \
"ds_read_b64_tr_b16 v[116:117], v36 offset:32768\n" \
"ds_read_b64_tr_b16 v[118:119], v36 offset:33280\n" \
"v_mfma_f32_16x16x32_bf16 a[152:155], v[164:167], v[84:87], a[152:155]\n" \
"ds_read_b64_tr_b16 v[120:121], v36 offset:36864\n" \
"ds_read_b64_tr_b16 v[122:123], v36 offset:37376\n" \
"v_mfma_f32_16x16x32_bf16 a[156:159], v[172:175], v[84:87], a[156:159]\n" \
"s_add_u32 m0, %[mA], 0x800\n" \
"s_add_u32 s85, s62, 0x4800\n" \
"v_mfma_f32_16x16x32_bf16 a[156:159], v[176:179], v[88:91], a[156:159]\n" \
"buffer_load_dwordx4 v24, s[76:79], s85 offen lds\n" \
"v_mfma_f32_16x16x32_bf16 a[176:179], v[152:155], v[96:99], a[176:179]\n" \
"ds_read_b128 v[84:87], v22 offset:32768\n" \
"ds_read_b128 v[88:91], v23 offset:32768\n" \
"v_mfma_f32_16x16x32_bf16 a[176:179], v[148:151], v[92:95], a[176:179]\n" \
"s_add_u32 m0, %[mA], 0xc00\n" \
"s_add_u32 s84, s62, 0x6c00\n" \
"v_mfma_f32_16x16x32_bf16 a[180:183], v[156:159], v[92:95], a[180:183]\n" \
"buffer_load_dwordx4 v25, s[76:79], s84 offen lds\n" \
"v_mfma_f32_16x16x32_bf16 a[180:183], v[160:163], v[96:99], a[180:183]\n" \
"ds_read_b128 v[60:63], v28 offset:34816\n" \
"ds_read_b128 v[64:67], v29 offset:34816\n" \
"v_mfma_f32_16x16x32_bf16 a[184:187], v[168:171], v[96:99], a[184:187]\n" \
"ds_read_b128 v[68:71], v28 offset:36864\n" \
"ds_read_b128 v[72:75], v29 offset:36864\n" \
"v_mfma_f32_16x16x32_bf16 a[184:187], v[164:167], v[92:95], a[184:187]\n" \
"s_add_u32 m0, %[mB], 0x400\n" \
"s_add_u32 s85, s63, 0x8000\n" \
"v_mfma_f32_16x16x32_bf16 a[188:191], v[172:175], v[92:95], a[188:191]\n" \
"buffer_load_dwordx4 v26, s[68:71], s85 offen lds\n" \
"v_mfma_f32_16x16x32_bf16 a[188:191], v[176:179], v[96:99], a[188:191]\n" \
"ds_read_b128 v[76:79], v28 offset:38912\n" \
"ds_read_b128 v[80:83], v29 offset:38912\n" \
"v_mfma_f32_16x16x32_bf16 a[208:211], v[152:155], v[104:107], a[208:211]\n" \
"s_add_u32 m0, %[mA], 0x1000\n" \
"s_add_u32 s84, s62, 0x9000\n" \
"v_mfma_f32_16x16x32_bf16 a[208:211], v[148:151], v[100:103], a[208:211]\n" \
"buffer_load_dwordx4 v24, s[76:79], s84 offen lds\n" \
"v_mfma_f32_16x16x32_bf16 a[212:215], v[156:159], v[100:103], a[212:215]\n" \
"ds_read_b128 v[92:95], v48 offset:34816\n" \
"ds_read_b128 v[96:99], v49 offset:34816\n" \
"v_mfma_f32_16x16x32_bf16 a[212:215], v[160:163], v[104:107], a[212:215]\n" \
"v_mfma_f32_16x16x32_bf16 a[216:219], v[168:171], v[104:107], a[216:219]\n" \
"s_add_u32 m0, %[mA], 0x1400\n" \
"s_add_u32 s85, s62, 0xb400\n" \
"v_mfma_f32_16x16x32_bf16 a[216:219], v[164:167], v[100:103], a[216:219]\n" \
"buffer_load_dwordx4 v25, s[76:79], s85 offen lds\n" \
"v_mfma_f32_16x16x32_bf16 a[220:223], v[172:175], v[100:103], a[220:223]\n" \
"ds_read_b128 v[100:103], v48 offset:36864\n" \
"v_mfma_f32_16x16x32_bf16 a[220:223], v[176:179], v[104:107], a[220:223]\n" \
"s_add_u32 m0, %[mB], 0x800\n" \
"s_add_u32 s84, s63, 0x10000\n" \
"v_mfma_f32_16x16x32_bf16 a[240:243], v[152:155], v[112:115], a[240:243]\n" \
"buffer_load_dwordx4 v27, s[68:71], s84 offen lds\n" \
"v_mfma_f32_16x16x32_bf16 a[240:243], v[148:151], v[108:111], a[240:243]\n" \
"ds_read_b64_tr_b16 v[152:153], v32 offset:40960\n" \
"ds_read_b64_tr_b16 v[154:155], v32 offset:41984\n" \
"v_mfma_f32_16x16x32_bf16 a[244:247], v[156:159], v[108:111], a[244:247]\n" \
"ds_read_b64_tr_b16 v[148:149], v32 offset:32768\n" \
"ds_read_b64_tr_b16 v[150:151], v32 offset:33792\n" \
"v_mfma_f32_16x16x32_bf16 a[244:247], v[160:163], v[112:115], a[244:247]\n" \
"s_add_u32 m0, %[mA], 0x1800\n" \
"s_add_u32 s85, s62, 0xd800\n" \
"v_mfma_f32_16x16x32_bf16 a[248:251], v[168:171], v[112:115], a[248:251]\n" \
"buffer_load_dwordx4 v24, s[76:79], s85 offen lds\n" \
"v_mfma_f32_16x16x32_bf16 a[248:251], v[164:167], v[108:111], a[248:251]\n" \
"ds_read_b64_tr_b16 v[156:157], v33 offset:32768\n" \
"ds_read_b64_tr_b16 v[158:159], v33 offset:33792\n" \
"v_mfma_f32_16x16x32_bf16 a[252:255], v[172:175], v[108:111], a[252:255]\n" \
"ds_read_b64_tr_b16 v[160:161], v33 offset:40960\n" \
"ds_read_b64_tr_b16 v[162:163], v33 offset:41984\n" \
"v_mfma_f32_16x16x32_bf16 a[252:255], v[176:179], v[112:115], a[252:255]\n" \
"s_add_u32 m0, %[mA], 0x1c00\n" \
"s_waitcnt lgkmcnt(11)\n" \
"s_nop 0\n" \
"v_smfmac_f32_16x16x64_bf16 a[16:19], v[140:143], v[84:91], v50\n" \
"v_mfma_f32_16x16x32_bf16 a[48:51], v[128:131], v[88:91], a[48:51]\n" \
"s_add_u32 s85, s62, 0xfc00\n" \
"ds_read_b64_tr_b16 v[168:169], v34 offset:40960\n" \
"v_mfma_f32_16x16x32_bf16 a[48:51], v[124:127], v[84:87], a[48:51]\n" \
"buffer_load_dwordx4 v25, s[76:79], s85 offen lds\n" \
"v_mfma_f32_16x16x32_bf16 a[80:83], v[84:87], v[132:135], a[80:83]\n" \
"s_add_u32 m0, %[mB], 0xc00\n" \
"s_add_u32 s84, s63, 0x18000\n" \
"v_mfma_f32_16x16x32_bf16 a[80:83], v[88:91], v[136:139], a[80:83]\n" \
"ds_read_b64_tr_b16 v[170:171], v34 offset:41984\n" \
"ds_read_b64_tr_b16 v[164:165], v34 offset:32768\n" \
"v_smfmac_f32_16x16x64_bf16 a[112:115], v[140:143], v[52:59], v50\n" \
"v_mfma_f32_16x16x32_bf16 a[20:23], v[128:131], v[56:59], a[20:23]\n" \
"buffer_load_dwordx4 v27, s[68:71], s84 offen lds\n" \
"v_mfma_f32_16x16x32_bf16 a[20:23], v[124:127], v[52:55], a[20:23]\n" \
"s_add_u32 s88, s42, 0x2000\n" \
"ds_read_b64_tr_b16 v[166:167], v34 offset:33792\n" \
"v_mfma_f32_16x16x32_bf16 a[52:55], v[116:119], v[52:55], a[52:55]\n" \
"ds_read_b64_tr_b16 v[172:173], v35 offset:32768\n" \
"ds_read_b64_tr_b16 v[174:175], v35 offset:33792\n" \
"v_mfma_f32_16x16x32_bf16 a[52:55], v[120:123], v[56:59], a[52:55]\n" \
"buffer_store_dwordx4 v[224:227], v20, s[80:83], s88 offen offset:0 sc1\n" \
"v_mfma_f32_16x16x32_bf16 a[84:87], v[56:59], v[136:139], a[84:87]\n" \
"ds_read_b64_tr_b16 v[176:177], v35 offset:40960\n" \
"ds_read_b64_tr_b16 v[178:179], v35 offset:41984\n" \
"v_mfma_f32_16x16x32_bf16 a[84:87], v[52:55], v[132:135], a[84:87]\n" \
"ds_read_b128 v[84:87], v48 offset:32768\n" \
"ds_read_b128 v[88:91], v49 offset:32768\n" \
"v_smfmac_f32_16x16x64_bf16 a[116:119], v[140:143], v[60:67], v50\n" \
"v_mfma_f32_16x16x32_bf16 a[24:27], v[124:127], v[60:63], a[24:27]\n" \
"ds_read_b128 v[104:107], v49 offset:36864\n" \
"ds_read_b128 v[108:111], v48 offset:38912\n" \
"v_mfma_f32_16x16x32_bf16 a[24:27], v[128:131], v[64:67], a[24:27]\n" \
"s_add_u32 s89, s42, 0x3000\n" \
"ds_read_b128 v[112:115], v49 offset:38912\n" \
"v_mfma_f32_16x16x32_bf16 a[56:59], v[120:123], v[64:67], a[56:59]\n" \
"buffer_store_dwordx4 v[228:231], v20, s[80:83], s89 offen offset:0 sc1\n" \
"v_mfma_f32_16x16x32_bf16 a[56:59], v[116:119], v[60:63], a[56:59]\n" \
"s_add_u32 s62, s62, 0x80\n" \
"s_lshr_b32 s96, %[b], 3\n" \
"v_mfma_f32_16x16x32_bf16 a[88:91], v[60:63], v[132:135], a[88:91]\n" \
"s_cmp_lt_u32 s62, 0x480\n" \
"s_cselect_b32 s62, s62, 0\n" \
"v_mfma_f32_16x16x32_bf16 a[88:91], v[64:67], v[136:139], a[88:91]\n" \
"s_lshl_b32 s72, s62, 6\n" \
"s_add_u32 s72, s72, s37\n" \
"v_smfmac_f32_16x16x64_bf16 a[120:123], v[140:143], v[68:75], v50\n" \
"v_mfma_f32_16x16x32_bf16 a[28:31], v[128:131], v[72:75], a[28:31]\n" \
"s_lshl_b32 s63, s62, 12\n" \
"s_and_b32 s97, 3, s96\n" \
"v_mfma_f32_16x16x32_bf16 a[28:31], v[124:127], v[68:71], a[28:31]\n" \
"s_and_b32 s94, 7, %[b]\n" \
"s_add_u32 s98, 1, s97\n" \
"v_mfma_f32_16x16x32_bf16 a[60:63], v[116:119], v[68:71], a[60:63]\n" \
"s_add_u32 s88, s42, 0x0\n" \
"s_lshl_b32 s95, s94, 2\n" \
"v_mfma_f32_16x16x32_bf16 a[60:63], v[120:123], v[72:75], a[60:63]\n" \
"buffer_store_dwordx4 v[232:235], v20, s[80:83], s88 offen offset:1024 sc1\n" \
"v_mfma_f32_16x16x32_bf16 a[92:95], v[72:75], v[136:139], a[92:95]\n" \
"s_and_b32 s99, 3, s98\n" \
"s_add_u32 s100, s95, s99\n" \
"v_mfma_f32_16x16x32_bf16 a[92:95], v[68:71], v[132:135], a[92:95]\n" \
"s_mul_i32 s101, 0x24000, s100\n" \
"s_mul_i32 s64, 0x9000, %[w]\n" \
"v_smfmac_f32_16x16x64_bf16 a[124:127], v[140:143], v[76:83], v50\n" \
"v_mfma_f32_16x16x32_bf16 a[0:3], v[124:127], v[76:79], a[0:3]\n" \
"s_and_b32 s96, 2, %[w]\n" \
"s_add_u32 s65, s101, s64\n" \
"v_mfma_f32_16x16x32_bf16 a[0:3], v[128:131], v[80:83], a[0:3]\n" \
"s_lshr_b32 s66, %[b], 5\n" \
"s_lshl_b32 s98, s96, 1\n" \
"v_mfma_f32_16x16x32_bf16 a[4:7], v[120:123], v[80:83], a[4:7]\n" \
"s_and_b32 s99, 1, %[w]\n" \
"v_writelane_b32 v11, s65, 23\n" \
"v_mfma_f32_16x16x32_bf16 a[4:7], v[116:119], v[76:79], a[4:7]\n" \
"s_lshl_b32 s67, s66, 3\n" \
"s_add_u32 s101, s98, s99\n" \
"v_mfma_f32_16x16x32_bf16 a[32:35], v[76:79], v[132:135], a[32:35]\n" \
"s_add_u32 s65, s67, s101\n" \
"s_mul_i32 s66, 0x24800, s65\n" \
"v_mfma_f32_16x16x32_bf16 a[32:35], v[80:83], v[136:139], a[32:35]\n" \
"s_lshl_b32 s98, s100, 12\n" \
"s_waitcnt lgkmcnt(0)\n" \
"v_mfma_f32_16x16x32_bf16 a[128:131], v[152:155], v[56:59], a[128:131]\n" \
"s_add_u32 s96, 0x3000000, s66\n" \
"s_add_u32 s99, 0x4800, s98\n" \
"v_mfma_f32_16x16x32_bf16 a[128:131], v[148:151], v[52:55], a[128:131]\n" \
"s_add_u32 s65, s96, s99\n" \
"s_sub_u32 s100, 0x78, s67\n" \
"v_mfma_f32_16x16x32_bf16 a[132:135], v[156:159], v[52:55], a[132:135]\n" \
"v_writelane_b32 v11, s65, 26\n" \
"s_add_u32 s98, s100, s101\n" \
"v_mfma_f32_16x16x32_bf16 a[132:135], v[160:163], v[56:59], a[132:135]\n" \
"s_mul_i32 s65, 0x24800, s98\n" \
"s_add_u32 s67, 0x3000000, s65\n" \
"v_mfma_f32_16x16x32_bf16 a[136:139], v[168:171], v[56:59], a[136:139]\n" \
"s_add_u32 s101, 2, s97\n" \
"s_add_u32 s100, s67, s99\n" \
"v_mfma_f32_16x16x32_bf16 a[136:139], v[164:167], v[52:55], a[136:139]\n" \
"s_and_b32 s98, 3, s101\n" \
"v_writelane_b32 v11, s100, 29\n" \
"v_mfma_f32_16x16x32_bf16 a[140:143], v[172:175], v[52:55], a[140:143]\n" \
"s_add_u32 s99, s95, s98\n" \
"s_mul_i32 s100, 0x24000, s99\n" \
"v_mfma_f32_16x16x32_bf16 a[140:143], v[176:179], v[56:59], a[140:143]\n" \
"s_add_u32 s101, s100, s64\n" \
"s_lshl_b32 s98, s99, 12\n" \
"v_mfma_f32_16x16x32_bf16 a[160:163], v[152:155], v[64:67], a[160:163]\n" \
"v_writelane_b32 v11, s101, 24\n" \
"s_add_u32 s100, 0x4800, s98\n" \
"v_mfma_f32_16x16x32_bf16 a[160:163], v[148:151], v[60:63], a[160:163]\n" \
"s_add_u32 s101, s96, s100\n" \
"v_writelane_b32 v11, s101, 27\n" \
"v_mfma_f32_16x16x32_bf16 a[164:167], v[156:159], v[60:63], a[164:167]\n" \
"s_add_u32 s98, 3, s97\n" \
"s_add_u32 s99, s67, s100\n" \
"v_mfma_f32_16x16x32_bf16 a[164:167], v[160:163], v[64:67], a[164:167]\n" \
"s_and_b32 s101, 3, s98\n" \
"v_writelane_b32 v11, s99, 30\n" \
"v_mfma_f32_16x16x32_bf16 a[168:171], v[168:171], v[64:67], a[168:171]\n" \
"s_add_u32 s100, s95, s101\n" \
"s_mul_i32 s99, 0x24000, s100\n" \
"v_mfma_f32_16x16x32_bf16 a[168:171], v[164:167], v[60:63], a[168:171]\n" \
"s_lshl_b32 s98, s100, 12\n" \
"s_add_u32 s97, s99, s64\n" \
"v_mfma_f32_16x16x32_bf16 a[172:175], v[172:175], v[60:63], a[172:175]\n" \
"s_add_u32 s95, 0x4800, s98\n" \
"v_writelane_b32 v11, s97, 25\n" \
"v_mfma_f32_16x16x32_bf16 a[172:175], v[176:179], v[64:67], a[172:175]\n" \
"s_add_u32 s101, s96, s95\n" \
"v_writelane_b32 v11, s101, 28\n" \
"v_mfma_f32_16x16x32_bf16 a[192:195], v[152:155], v[72:75], a[192:195]\n" \
"s_add_u32 s99, s67, s95\n" \
"s_lshl_b32 s64, s94, 11\n" \
"v_mfma_f32_16x16x32_bf16 a[192:195], v[148:151], v[68:71], a[192:195]\n" \
"v_writelane_b32 v11, s99, 31\n" \
"s_add_u32 s97, s96, s64\n" \
"s_waitcnt vmcnt(3)\n" \
"s_barrier\n" \
"v_mfma_f32_16x16x32_bf16 a[196:199], v[156:159], v[68:71], a[196:199]\n" \
"s_add_u32 m0, s40, 0x8000\n" \
"s_add_u32 s85, s72, 0x0\n" \
"v_mfma_f32_16x16x32_bf16 a[196:199], v[160:163], v[72:75], a[196:199]\n" \
"buffer_load_dwordx4 v20, s[80:83], s85 offen sc1 lds\n" \
"v_mfma_f32_16x16x32_bf16 a[200:203], v[168:171], v[72:75], a[200:203]\n" \
"s_add_u32 m0, s40, 0x9000\n" \
"s_add_u32 s84, s72, 0x1000\n" \
"v_mfma_f32_16x16x32_bf16 a[200:203], v[164:167], v[68:71], a[200:203]\n" \
"ds_read_b64_tr_b16 v[140:141], v39 offset:0\n" \
"ds_read_b64_tr_b16 v[142:143], v39 offset:960\n" \
"v_mfma_f32_16x16x32_bf16 a[204:207], v[172:175], v[68:71], a[204:207]\n" \
"buffer_load_dwordx4 v20, s[80:83], s84 offen sc1 lds\n" \
"v_mfma_f32_16x16x32_bf16 a[204:207], v[176:179], v[72:75], a[204:207]\n" \
"ds_read_b64_tr_b16 v[128:129], v37 offset:4096\n" \
"ds_read_b64_tr_b16 v[130:131], v37 offset:4608\n" \
"v_mfma_f32_16x16x32_bf16 a[224:227], v[152:155], v[80:83], a[224:227]\n" \
"s_add_u32 m0, s40, 0xb000\n" \
"s_add_u32 s85, s62, 0x0\n" \
"v_mfma_f32_16x16x32_bf16 a[224:227], v[148:151], v[76:79], a[224:227]\n" \
"buffer_load_dwordx4 v21, s[76:79], s85 offen lds\n" \
"v_mfma_f32_16x16x32_bf16 a[228:231], v[156:159], v[76:79], a[228:231]\n" \
"ds_read_b64_tr_b16 v[124:125], v37 offset:0\n" \
"ds_read_b64_tr_b16 v[126:127], v37 offset:512\n" \
"v_mfma_f32_16x16x32_bf16 a[228:231], v[160:163], v[80:83], a[228:231]\n" \
"s_add_u32 m0, %[mA], 0x8000\n" \
"s_add_u32 s84, s62, 0x0\n" \
"v_mfma_f32_16x16x32_bf16 a[232:235], v[168:171], v[80:83], a[232:235]\n" \
"buffer_load_dwordx4 v24, s[76:79], s84 offen lds\n" \
"v_mfma_f32_16x16x32_bf16 a[232:235], v[164:167], v[76:79], a[232:235]\n" \
"ds_read_b64_tr_b16 v[132:133], v38 offset:0\n" \
"ds_read_b64_tr_b16 v[134:135], v38 offset:512\n" \
"v_mfma_f32_16x16x32_bf16 a[236:239], v[172:175], v[76:79], a[236:239]\n" \
"ds_read_b64_tr_b16 v[136:137], v38 offset:4096\n" \
"ds_read_b64_tr_b16 v[138:139], v38 offset:4608\n" \
"v_mfma_f32_16x16x32_bf16 a[236:239], v[176:179], v[80:83], a[236:239]\n" \
"s_add_u32 m0, %[mA], 0x8400\n" \
"s_add_u32 s85, s62, 0x2400\n" \
"v_mfma_f32_16x16x32_bf16 a[144:147], v[152:155], v[88:91], a[144:147]\n" \
"buffer_load_dwordx4 v25, s[76:79], s85 offen lds\n" \
"v_mfma_f32_16x16x32_bf16 a[144:147], v[148:151], v[84:87], a[144:147]\n" \
"ds_read_b128 v[52:55], v28 offset:0\n" \
"ds_read_b128 v[56:59], v29 offset:0\n" \
"v_mfma_f32_16x16x32_bf16 a[148:151], v[156:159], v[84:87], a[148:151]\n" \
"s_add_u32 m0, %[mB], 0x8000\n" \
"s_add_u32 s84, s63, 0x0\n" \
"s_nop 0\n" \
"v_mfma_f32_16x16x32_bf16 a[148:151], v[160:163], v[88:91], a[148:151]\n" \
"buffer_load_dwordx4 v26, s[68:71], s84 offen lds\n" \
"v_mfma_f32_16x16x32_bf16 a[152:155], v[168:171], v[88:91], a[152:155]\n" \
"ds_read_b64_tr_b16 v[116:117], v36 offset:0\n" \
"ds_read_b64_tr_b16 v[118:119], v36 offset:512\n" \
"v_mfma_f32_16x16x32_bf16 a[152:155], v[164:167], v[84:87], a[152:155]\n" \
"ds_read_b64_tr_b16 v[120:121], v36 offset:4096\n" \
"ds_read_b64_tr_b16 v[122:123], v36 offset:4608\n" \
"v_mfma_f32_16x16x32_bf16 a[156:159], v[172:175], v[84:87], a[156:159]\n" \
"s_add_u32 m0, %[mA], 0x8800\n" \
"s_add_u32 s85, s62, 0x4800\n" \
"v_mfma_f32_16x16x32_bf16 a[156:159], v[176:179], v[88:91], a[156:159]\n" \
"buffer_load_dwordx4 v24, s[76:79], s85 offen lds\n" \
"v_mfma_f32_16x16x32_bf16 a[176:179], v[152:155], v[96:99], a[176:179]\n" \
"ds_read_b128 v[84:87], v22 offset:0\n" \
"ds_read_b128 v[88:91], v23 offset:0\n" \
"v_mfma_f32_16x16x32_bf16 a[176:179], v[148:151], v[92:95], a[176:179]\n" \
"s_add_u32 m0, %[mA], 0x8c00\n" \
"s_add_u32 s84, s62, 0x6c00\n" \
"v_mfma_f32_16x16x32_bf16 a[180:183], v[156:159], v[92:95], a[180:183]\n" \
"buffer_load_dwordx4 v25, s[76:79], s84 offen lds\n" \
"v_mfma_f32_16x16x32_bf16 a[180:183], v[160:163], v[96:99], a[180:183]\n" \
"ds_read_b128 v[60:63], v28 offset:2048\n" \
"ds_read_b128 v[64:67], v29 offset:2048\n" \
"v_mfma_f32_16x16x32_bf16 a[184:187], v[168:171], v[96:99], a[184:187]\n" \
"ds_read_b128 v[68:71], v28 offset:4096\n" \
"ds_read_b128 v[72:75], v29 offset:4096\n" \
"v_mfma_f32_16x16x32_bf16 a[184:187], v[164:167], v[92:95], a[184:187]\n" \
"s_add_u32 m0, %[mB], 0x8400\n" \
"s_add_u32 s85, s63, 0x8000\n" \
"v_mfma_f32_16x16x32_bf16 a[188:191], v[172:175], v[92:95], a[188:191]\n" \
"buffer_load_dwordx4 v26, s[68:71], s85 offen lds\n" \
"v_mfma_f32_16x16x32_bf16 a[188:191], v[176:179], v[96:99], a[188:191]\n" \
"ds_read_b128 v[76:79], v28 offset:6144\n" \
"ds_read_b128 v[80:83], v29 offset:6144\n" \
"v_mfma_f32_16x16x32_bf16 a[208:211], v[152:155], v[104:107], a[208:211]\n" \
"s_add_u32 m0, %[mA], 0x9000\n" \
"s_add_u32 s84, s62, 0x9000\n" \
"v_mfma_f32_16x16x32_bf16 a[208:211], v[148:151], v[100:103], a[208:211]\n" \
"buffer_load_dwordx4 v24, s[76:79], s84 offen lds\n" \
"v_mfma_f32_16x16x32_bf16 a[212:215], v[156:159], v[100:103], a[212:215]\n" \
"ds_read_b128 v[92:95], v48 offset:2048\n" \
"ds_read_b128 v[96:99], v49 offset:2048\n" \
"v_mfma_f32_16x16x32_bf16 a[212:215], v[160:163], v[104:107], a[212:215]\n" \
"v_writelane_b32 v11, s97, 32\n" \
"s_and_b32 s99, 1, s94\n" \
"v_mfma_f32_16x16x32_bf16 a[216:219], v[168:171], v[104:107], a[216:219]\n" \
"s_add_u32 m0, %[mA], 0x9400\n" \
"s_add_u32 s85, s62, 0xb400\n" \
"v_mfma_f32_16x16x32_bf16 a[216:219], v[164:167], v[100:103], a[216:219]\n" \
"buffer_load_dwordx4 v25, s[76:79], s85 offen lds\n" \
"v_mfma_f32_16x16x32_bf16 a[220:223], v[172:175], v[100:103], a[220:223]\n" \
"ds_read_b128 v[100:103], v48 offset:4096\n" \
"s_add_u32 s100, s67, s64\n" \
"v_mfma_f32_16x16x32_bf16 a[220:223], v[176:179], v[104:107], a[220:223]\n" \
"s_add_u32 m0, %[mB], 0x8800\n" \
"s_add_u32 s84, s63, 0x10000\n" \
"v_mfma_f32_16x16x32_bf16 a[240:243], v[152:155], v[112:115], a[240:243]\n" \
"buffer_load_dwordx4 v27, s[68:71], s84 offen lds\n" \
"v_mfma_f32_16x16x32_bf16 a[240:243], v[148:151], v[108:111], a[240:243]\n" \
"ds_read_b64_tr_b16 v[152:153], v32 offset:8192\n" \
"ds_read_b64_tr_b16 v[154:155], v32 offset:9216\n" \
"v_mfma_f32_16x16x32_bf16 a[244:247], v[156:159], v[108:111], a[244:247]\n" \
"ds_read_b64_tr_b16 v[148:149], v32 offset:0\n" \
"ds_read_b64_tr_b16 v[150:151], v32 offset:1024\n" \
"v_mfma_f32_16x16x32_bf16 a[244:247], v[160:163], v[112:115], a[244:247]\n" \
"s_add_u32 m0, %[mA], 0x9800\n" \
"s_add_u32 s85, s62, 0xd800\n" \
"v_mfma_f32_16x16x32_bf16 a[248:251], v[168:171], v[112:115], a[248:251]\n" \
"buffer_load_dwordx4 v24, s[76:79], s85 offen lds\n" \
"v_mfma_f32_16x16x32_bf16 a[248:251], v[164:167], v[108:111], a[248:251]\n" \
"ds_read_b64_tr_b16 v[156:157], v33 offset:0\n" \
"ds_read_b64_tr_b16 v[158:159], v33 offset:1024\n" \
"v_mfma_f32_16x16x32_bf16 a[252:255], v[172:175], v[108:111], a[252:255]\n" \
"ds_read_b64_tr_b16 v[160:161], v33 offset:8192\n" \
"ds_read_b64_tr_b16 v[162:163], v33 offset:9216\n" \
"v_mfma_f32_16x16x32_bf16 a[252:255], v[176:179], v[112:115], a[252:255]\n" \
"s_add_u32 m0, %[mA], 0x9c00\n" \
"s_waitcnt lgkmcnt(11)\n" \
"s_nop 0\n" \
"v_smfmac_f32_16x16x64_bf16 a[16:19], v[140:143], v[84:91], v50\n" \
"v_mfma_f32_16x16x32_bf16 a[48:51], v[128:131], v[88:91], a[48:51]\n" \
"s_add_u32 s85, s62, 0xfc00\n" \
"ds_read_b64_tr_b16 v[168:169], v34 offset:8192\n" \
"v_mfma_f32_16x16x32_bf16 a[48:51], v[124:127], v[84:87], a[48:51]\n" \
"buffer_load_dwordx4 v25, s[76:79], s85 offen lds\n" \
"v_mfma_f32_16x16x32_bf16 a[80:83], v[84:87], v[132:135], a[80:83]\n" \
"s_add_u32 m0, %[mB], 0x8c00\n" \
"s_add_u32 s84, s63, 0x18000\n" \
"v_mfma_f32_16x16x32_bf16 a[80:83], v[88:91], v[136:139], a[80:83]\n" \
"ds_read_b64_tr_b16 v[170:171], v34 offset:9216\n" \
"ds_read_b64_tr_b16 v[164:165], v34 offset:0\n" \
"v_smfmac_f32_16x16x64_bf16 a[112:115], v[140:143], v[52:59], v50\n" \
"v_mfma_f32_16x16x32_bf16 a[20:23], v[128:131], v[56:59], a[20:23]\n" \
"buffer_load_dwordx4 v27, s[68:71], s84 offen lds\n" \
"v_mfma_f32_16x16x32_bf16 a[20:23], v[124:127], v[52:55], a[20:23]\n" \
"s_add_u32 s89, s42, 0x1000\n" \
"ds_read_b64_tr_b16 v[166:167], v34 offset:1024\n" \
"v_mfma_f32_16x16x32_bf16 a[52:55], v[116:119], v[52:55], a[52:55]\n" \
"ds_read_b64_tr_b16 v[172:173], v35 offset:0\n" \
"ds_read_b64_tr_b16 v[174:175], v35 offset:1024\n" \
"v_mfma_f32_16x16x32_bf16 a[52:55], v[120:123], v[56:59], a[52:55]\n" \
"buffer_store_dwordx4 v[236:239], v20, s[80:83], s89 offen offset:1024 sc1\n" \
"v_mfma_f32_16x16x32_bf16 a[84:87], v[56:59], v[136:139], a[84:87]\n" \
"ds_read_b64_tr_b16 v[176:177], v35 offset:8192\n" \
"ds_read_b64_tr_b16 v[178:179], v35 offset:9216\n" \
"v_mfma_f32_16x16x32_bf16 a[84:87], v[52:55], v[132:135], a[84:87]\n" \
"ds_read_b128 v[84:87], v48 offset:0\n" \
"ds_read_b128 v[88:91], v49 offset:0\n" \
"v_smfmac_f32_16x16x64_bf16 a[116:119], v[140:143], v[60:67], v50\n" \
"v_mfma_f32_16x16x32_bf16 a[24:27], v[124:127], v[60:63], a[24:27]\n" \
"ds_read_b128 v[104:107], v49 offset:4096\n" \
"ds_read_b128 v[108:111], v48 offset:6144\n" \
"v_mfma_f32_16x16x32_bf16 a[24:27], v[128:131], v[64:67], a[24:27]\n" \
"s_add_u32 s88, s42, 0x2000\n" \
"ds_read_b128 v[112:115], v49 offset:6144\n" \
"v_mfma_f32_16x16x32_bf16 a[56:59], v[120:123], v[64:67], a[56:59]\n" \
"buffer_store_dwordx4 v[240:243], v20, s[80:83], s88 offen offset:1024 sc1\n" \
"v_mfma_f32_16x16x32_bf16 a[56:59], v[116:119], v[60:63], a[56:59]\n" \
"s_add_u32 s62, s62, 0x80\n" \
"v_writelane_b32 v11, s100, 33\n" \
"v_mfma_f32_16x16x32_bf16 a[88:91], v[60:63], v[132:135], a[88:91]\n" \
"s_cmp_lt_u32 s62, 0x480\n" \
"s_cselect_b32 s62, s62, 0\n" \
"v_mfma_f32_16x16x32_bf16 a[88:91], v[64:67], v[136:139], a[88:91]\n" \
"s_lshl_b32 s72, s62, 6\n" \
"s_add_u32 s72, s72, s37\n" \
"v_smfmac_f32_16x16x64_bf16 a[120:123], v[140:143], v[68:75], v50\n" \
"v_mfma_f32_16x16x32_bf16 a[28:31], v[128:131], v[72:75], a[28:31]\n" \
"s_lshl_b32 s63, s62, 12\n" \
"s_add_u32 s98, 0x3004000, s66\n" \
"v_mfma_f32_16x16x32_bf16 a[28:31], v[124:127], v[68:71], a[28:31]\n" \
"s_cmp_lg_u32 s99, 0\n" \
"s_cselect_b32 s97, s67, s96\n" \
"v_mfma_f32_16x16x32_bf16 a[60:63], v[116:119], v[68:71], a[60:63]\n" \
"s_add_u32 s89, s42, 0x3000\n" \
"s_lshr_b32 s95, s94, 1\n" \
"v_mfma_f32_16x16x32_bf16 a[60:63], v[120:123], v[72:75], a[60:63]\n" \
"buffer_store_dwordx4 v[244:247], v20, s[80:83], s89 offen offset:1024 sc1\n" \
"v_mfma_f32_16x16x32_bf16 a[92:95], v[72:75], v[136:139], a[92:95]\n" \
"s_add_u32 s64, 0x4000, s97\n" \
"v_writelane_b32 v11, s98, 34\n" \
"v_mfma_f32_16x16x32_bf16 a[92:95], v[68:71], v[132:135], a[92:95]\n" \
"s_add_u32 s101, 0x3004000, s65\n" \
"s_and_b32 s96, 2, %[w]\n" \
"v_smfmac_f32_16x16x64_bf16 a[124:127], v[140:143], v[76:83], v50\n" \
"v_mfma_f32_16x16x32_bf16 a[0:3], v[124:127], v[76:79], a[0:3]\n" \
"s_cmp_eq_u32 %[w], s95\n" \
"s_cselect_b32 s100, s64, 0x40000000\n" \
"v_mfma_f32_16x16x32_bf16 a[0:3], v[128:131], v[80:83], a[0:3]\n" \
"v_writelane_b32 v11, s101, 35\n" \
"s_lshl_b32 s97, s96, 1\n" \
"v_mfma_f32_16x16x32_bf16 a[4:7], v[120:123], v[80:83], a[4:7]\n" \
"s_and_b32 s98, 1, %[w]\n" \
"v_writelane_b32 v11, s100, 36\n" \
"v_mfma_f32_16x16x32_bf16 a[4:7], v[116:119], v[76:79], a[4:7]\n" \
"s_lshr_b32 s94, %[b], 5\n" \
"s_add_u32 s99, s97, s98\n" \
"v_mfma_f32_16x16x32_bf16 a[32:35], v[76:79], v[132:135], a[32:35]\n" \
"s_lshl_b32 s95, s94, 7\n" \
"s_lshl_b32 s100, s99, 4\n" \
"v_mfma_f32_16x16x32_bf16 a[32:35], v[80:83], v[136:139], a[32:35]\n" \
"s_add_u32 s101, s95, s100\n" \
"s_waitcnt lgkmcnt(0)\n" \
"v_mfma_f32_16x16x32_bf16 a[128:131], v[152:155], v[56:59], a[128:131]\n" \
"s_mul_i32 s64, 0x480, s101\n" \
"s_lshr_b32 s65, %[b], 3\n" \
"v_mfma_f32_16x16x32_bf16 a[128:131], v[148:151], v[52:55], a[128:131]\n" \
"s_sub_u32 s99, 0x780, s95\n" \
"s_and_b32 s66, 3, s65\n" \
"v_mfma_f32_16x16x32_bf16 a[132:135], v[156:159], v[52:55], a[132:135]\n" \
"s_add_u32 s101, s99, s100\n" \
"s_mul_i32 s67, 0x120, s66\n" \
"v_mfma_f32_16x16x32_bf16 a[132:135], v[160:163], v[56:59], a[132:135]\n" \
"s_and_b32 s96, 7, %[b]\n" \
"s_mul_i32 s65, 0x480, s101\n" \
"v_mfma_f32_16x16x32_bf16 a[136:139], v[168:171], v[56:59], a[136:139]\n" \
"s_add_u32 s94, s64, s67\n" \
"s_lshl_b32 s97, s96, 5\n" \
"v_mfma_f32_16x16x32_bf16 a[136:139], v[164:167], v[52:55], a[136:139]\n" \
"s_add_u32 s66, s65, s67\n" \
"s_and_b32 s95, 1, s96\n" \
"v_mfma_f32_16x16x32_bf16 a[140:143], v[172:175], v[52:55], a[140:143]\n" \
"s_add_u32 s98, s94, s97\n" \
"v_writelane_b32 v11, s98, 39\n" \
"v_mfma_f32_16x16x32_bf16 a[140:143], v[176:179], v[56:59], a[140:143]\n" \
"s_cmp_lg_u32 s95, 0\n" \
"s_cselect_b32 s99, s66, s94\n" \
"v_mfma_f32_16x16x32_bf16 a[160:163], v[152:155], v[64:67], a[160:163]\n" \
"s_lshr_b32 s98, s96, 1\n" \
"s_add_u32 s100, 0x100, s99\n" \
"v_mfma_f32_16x16x32_bf16 a[160:163], v[148:151], v[60:63], a[160:163]\n" \
"s_add_u32 s64, s66, s97\n" \
"s_mov_b32 s94, s67\n" \
"v_mfma_f32_16x16x32_bf16 a[164:167], v[156:159], v[60:63], a[164:167]\n" \
"s_cmp_eq_u32 %[w], s98\n" \
"s_cselect_b32 s101, s100, 0x40000000\n" \
"v_mfma_f32_16x16x32_bf16 a[164:167], v[160:163], v[64:67], a[164:167]\n" \
"v_writelane_b32 v11, s64, 40\n" \
"v_writelane_b32 v11, s101, 41\n" \
"v_mfma_f32_16x16x32_bf16 a[168:171], v[168:171], v[64:67], a[168:171]\n" \
"s_add_u32 s96, s16, s94\n" \
"s_addc_u32 s97, s17, 0\n" \
"v_mfma_f32_16x16x32_bf16 a[168:171], v[164:167], v[60:63], a[168:171]\n" \
"s_add_u32 s98, s18, s94\n" \
"s_addc_u32 s99, s19, 0\n" \
"v_mfma_f32_16x16x32_bf16 a[172:175], v[172:175], v[60:63], a[172:175]\n" \
"s_and_b32 s97, s97, 0xffff\n" \
"v_writelane_b32 v11, s96, 7\n" \
"v_mfma_f32_16x16x32_bf16 a[172:175], v[176:179], v[64:67], a[172:175]\n" \
"v_writelane_b32 v11, s97, 8\n" \
"s_and_b32 s99, s99, 0xffff\n" \
"v_mfma_f32_16x16x32_bf16 a[192:195], v[152:155], v[72:75], a[192:195]\n" \
"v_writelane_b32 v11, s98, 9\n" \
"v_writelane_b32 v11, s99, 10\n" \
"v_mfma_f32_16x16x32_bf16 a[192:195], v[148:151], v[68:71], a[192:195]\n" \
"s_cmp_lt_u32 %[w], 3\n" \
"s_cselect_b64 s[96:97], s[96:97], s[98:99]\n" \
"s_waitcnt vmcnt(3)\n" \
"s_barrier\n" \
"v_mfma_f32_16x16x32_bf16 a[196:199], v[156:159], v[68:71], a[196:199]\n" \
"s_add_u32 m0, s40, 0x0\n" \
"s_add_u32 s85, s72, 0x0\n" \
"v_mfma_f32_16x16x32_bf16 a[196:199], v[160:163], v[72:75], a[196:199]\n" \
"buffer_load_dwordx4 v20, s[80:83], s85 offen sc1 lds\n" \
"v_mfma_f32_16x16x32_bf16 a[200:203], v[168:171], v[72:75], a[200:203]\n" \
"s_add_u32 m0, s40, 0x1000\n" \
"s_add_u32 s84, s72, 0x1000\n" \
"v_mfma_f32_16x16x32_bf16 a[200:203], v[164:167], v[68:71], a[200:203]\n" \
"ds_read_b64_tr_b16 v[140:141], v39 offset:32768\n" \
"ds_read_b64_tr_b16 v[142:143], v39 offset:33728\n" \
"v_mfma_f32_16x16x32_bf16 a[204:207], v[172:175], v[68:71], a[204:207]\n" \
"buffer_load_dwordx4 v20, s[80:83], s84 offen sc1 lds\n" \
"v_mfma_f32_16x16x32_bf16 a[204:207], v[176:179], v[72:75], a[204:207]\n" \
"ds_read_b64_tr_b16 v[128:129], v37 offset:36864\n" \
"ds_read_b64_tr_b16 v[130:131], v37 offset:37376\n" \
"v_mfma_f32_16x16x32_bf16 a[224:227], v[152:155], v[80:83], a[224:227]\n" \
"s_add_u32 m0, s40, 0x3000\n" \
"s_add_u32 s85, s62, 0x0\n" \
"v_mfma_f32_16x16x32_bf16 a[224:227], v[148:151], v[76:79], a[224:227]\n" \
"buffer_load_dwordx4 v21, s[76:79], s85 offen lds\n" \
"v_mfma_f32_16x16x32_bf16 a[228:231], v[156:159], v[76:79], a[228:231]\n" \
"ds_read_b64_tr_b16 v[124:125], v37 offset:32768\n" \
"ds_read_b64_tr_b16 v[126:127], v37 offset:33280\n" \
"v_mfma_f32_16x16x32_bf16 a[228:231], v[160:163], v[80:83], a[228:231]\n" \
"s_add_u32 m0, %[mA], 0x0\n" \
"s_add_u32 s84, s62, 0x0\n" \
"s_nop 0\n" \
"v_mfma_f32_16x16x32_bf16 a[232:235], v[168:171], v[80:83], a[232:235]\n" \
"buffer_load_dwordx4 v24, s[76:79], s84 offen lds\n" \
"v_mfma_f32_16x16x32_bf16 a[232:235], v[164:167], v[76:79], a[232:235]\n" \
"ds_read_b64_tr_b16 v[132:133], v38 offset:32768\n" \
"ds_read_b64_tr_b16 v[134:135], v38 offset:33280\n" \
"v_mfma_f32_16x16x32_bf16 a[236:239], v[172:175], v[76:79], a[236:239]\n" \
"ds_read_b64_tr_b16 v[136:137], v38 offset:36864\n" \
"ds_read_b64_tr_b16 v[138:139], v38 offset:37376\n" \
"v_mfma_f32_16x16x32_bf16 a[236:239], v[176:179], v[80:83], a[236:239]\n" \
"s_add_u32 m0, %[mA], 0x400\n" \
"s_add_u32 s85, s62, 0x2400\n" \
"v_mfma_f32_16x16x32_bf16 a[144:147], v[152:155], v[88:91], a[144:147]\n" \
"buffer_load_dwordx4 v25, s[76:79], s85 offen lds\n" \
"v_mfma_f32_16x16x32_bf16 a[144:147], v[148:151], v[84:87], a[144:147]\n" \
"ds_read_b128 v[52:55], v28 offset:32768\n" \
"ds_read_b128 v[56:59], v29 offset:32768\n" \
"v_mfma_f32_16x16x32_bf16 a[148:151], v[156:159], v[84:87], a[148:151]\n" \
"s_add_u32 m0, %[mB], 0x0\n" \
"s_add_u32 s84, s63, 0x0\n" \
"v_mfma_f32_16x16x32_bf16 a[148:151], v[160:163], v[88:91], a[148:151]\n" \
"buffer_load_dwordx4 v26, s[68:71], s84 offen lds\n" \
"v_mfma_f32_16x16x32_bf16 a[152:155], v[168:171], v[88:91], a[152:155]\n" \
"ds_read_b64_tr_b16 v[116:117], v36 offset:32768\n" \
"ds_read_b64_tr_b16 v[118:119], v36 offset:33280\n" \
"v_mfma_f32_16x16x32_bf16 a[152:155], v[164:167], v[84:87], a[152:155]\n" \
"ds_read_b64_tr_b16 v[120:121], v36 offset:36864\n" \
"ds_read_b64_tr_b16 v[122:123], v36 offset:37376\n" \
"v_mfma_f32_16x16x32_bf16 a[156:159], v[172:175], v[84:87], a[156:159]\n" \
"L_pf7:\n" \
"s_cbranch_execz L_pf11\n" \
"s_nop 0\n" \
"s_add_u32 m0, %[mA], 0x800\n" \
"s_add_u32 s85, s62, 0x4800\n" \
"v_mfma_f32_16x16x32_bf16 a[156:159], v[176:179], v[88:91], a[156:159]\n" \
"buffer_load_dwordx4 v24, s[76:79], s85 offen lds\n" \
"v_mfma_f32_16x16x32_bf16 a[176:179], v[152:155], v[96:99], a[176:179]\n" \
"ds_read_b128 v[84:87], v22 offset:32768\n" \
"ds_read_b128 v[88:91], v23 offset:32768\n" \
"v_mfma_f32_16x16x32_bf16 a[176:179], v[148:151], v[92:95], a[176:179]\n" \
"s_add_u32 m0, %[mA], 0xc00\n" \
"s_add_u32 s84, s62, 0x6c00\n" \
"v_mfma_f32_16x16x32_bf16 a[180:183], v[156:159], v[92:95], a[180:183]\n" \
"buffer_load_dwordx4 v25, s[76:79], s84 offen lds\n" \
"v_mfma_f32_16x16x32_bf16 a[180:183], v[160:163], v[96:99], a[180:183]\n" \
"ds_read_b128 v[60:63], v28 offset:34816\n" \
"ds_read_b128 v[64:67], v29 offset:34816\n" \
"v_mfma_f32_16x16x32_bf16 a[184:187], v[168:171], v[96:99], a[184:187]\n" \
"ds_read_b128 v[68:71], v28 offset:36864\n" \
"ds_read_b128 v[72:75], v29 offset:36864\n" \
"v_mfma_f32_16x16x32_bf16 a[184:187], v[164:167], v[92:95], a[184:187]\n" \
"s_add_u32 m0, %[mB], 0x400\n" \
"s_add_u32 s85, s63, 0x8000\n" \
"v_mfma_f32_16x16x32_bf16 a[188:191], v[172:175], v[92:95], a[188:191]\n" \
"buffer_load_dwordx4 v26, s[68:71], s85 offen lds\n" \
"v_mfma_f32_16x16x32_bf16 a[188:191], v[176:179], v[96:99], a[188:191]\n" \
"ds_read_b128 v[76:79], v28 offset:38912\n" \
"ds_read_b128 v[80:83], v29 offset:38912\n" \
"v_mfma_f32_16x16x32_bf16 a[208:211], v[152:155], v[104:107], a[208:211]\n" \
"s_add_u32 m0, %[mA], 0x1000\n" \
"s_add_u32 s84, s62, 0x9000\n" \
"v_mfma_f32_16x16x32_bf16 a[208:211], v[148:151], v[100:103], a[208:211]\n" \
"buffer_load_dwordx4 v24, s[76:79], s84 offen lds\n" \
"v_mfma_f32_16x16x32_bf16 a[212:215], v[156:159], v[100:103], a[212:215]\n" \
"ds_read_b128 v[92:95], v48 offset:34816\n" \
"ds_read_b128 v[96:99], v49 offset:34816\n" \
"v_mfma_f32_16x16x32_bf16 a[212:215], v[160:163], v[104:107], a[212:215]\n" \
"v_writelane_b32 v11, s96, 13\n" \
"v_writelane_b32 v11, s97, 14\n" \
"v_mfma_f32_16x16x32_bf16 a[216:219], v[168:171], v[104:107], a[216:219]\n" \
"s_add_u32 m0, %[mA], 0x1400\n" \
"s_add_u32 s85, s62, 0xb400\n" \
"v_mfma_f32_16x16x32_bf16 a[216:219], v[164:167], v[100:103], a[216:219]\n" \
"buffer_load_dwordx4 v25, s[76:79], s85 offen lds\n" \
"v_mfma_f32_16x16x32_bf16 a[220:223], v[172:175], v[100:103], a[220:223]\n" \
"ds_read_b128 v[100:103], v48 offset:36864\n" \
"v_writelane_b32 v11, s20, 11\n" \
"v_mfma_f32_16x16x32_bf16 a[220:223], v[176:179], v[104:107], a[220:223]\n" \
"s_add_u32 m0, %[mB], 0x800\n" \
"s_add_u32 s84, s63, 0x10000\n" \
"v_mfma_f32_16x16x32_bf16 a[240:243], v[152:155], v[112:115], a[240:243]\n" \
"buffer_load_dwordx4 v27, s[68:71], s84 offen lds\n" \
"v_mfma_f32_16x16x32_bf16 a[240:243], v[148:151], v[108:111], a[240:243]\n" \
"ds_read_b64_tr_b16 v[152:153], v32 offset:40960\n" \
"ds_read_b64_tr_b16 v[154:155], v32 offset:41984\n" \
"v_mfma_f32_16x16x32_bf16 a[244:247], v[156:159], v[108:111], a[244:247]\n" \
"ds_read_b64_tr_b16 v[148:149], v32 offset:32768\n" \
"ds_read_b64_tr_b16 v[150:151], v32 offset:33792\n" \
"v_mfma_f32_16x16x32_bf16 a[244:247], v[160:163], v[112:115], a[244:247]\n" \
"s_add_u32 m0, %[mA], 0x1800\n" \
"s_add_u32 s85, s62, 0xd800\n" \
"v_mfma_f32_16x16x32_bf16 a[248:251], v[168:171], v[112:115], a[248:251]\n" \
"buffer_load_dwordx4 v24, s[76:79], s85 offen lds\n" \
"v_mfma_f32_16x16x32_bf16 a[248:251], v[164:167], v[108:111], a[248:251]\n" \
"ds_read_b64_tr_b16 v[156:157], v33 offset:32768\n" \
"ds_read_b64_tr_b16 v[158:159], v33 offset:33792\n" \
"v_mfma_f32_16x16x32_bf16 a[252:255], v[172:175], v[108:111], a[252:255]\n" \
"ds_read_b64_tr_b16 v[160:161], v33 offset:40960\n" \
"ds_read_b64_tr_b16 v[162:163], v33 offset:41984\n" \
"v_mfma_f32_16x16x32_bf16 a[252:255], v[176:179], v[112:115], a[252:255]\n" \
"s_add_u32 m0, %[mA], 0x1c00\n" \
"s_waitcnt lgkmcnt(11)\n" \
"s_nop 0\n" \
"v_smfmac_f32_16x16x64_bf16 a[16:19], v[140:143], v[84:91], v50\n" \
"v_mfma_f32_16x16x32_bf16 a[48:51], v[128:131], v[88:91], a[48:51]\n" \
"s_add_u32 s85, s62, 0xfc00\n" \
"ds_read_b64_tr_b16 v[168:169], v34 offset:40960\n" \
"v_mfma_f32_16x16x32_bf16 a[48:51], v[124:127], v[84:87], a[48:51]\n" \
"buffer_load_dwordx4 v25, s[76:79], s85 offen lds\n" \
"v_mfma_f32_16x16x32_bf16 a[80:83], v[84:87], v[132:135], a[80:83]\n" \
"s_add_u32 m0, %[mB], 0xc00\n" \
"s_add_u32 s84, s63, 0x18000\n" \
"v_mfma_f32_16x16x32_bf16 a[80:83], v[88:91], v[136:139], a[80:83]\n" \
"ds_read_b64_tr_b16 v[170:171], v34 offset:41984\n" \
"ds_read_b64_tr_b16 v[164:165], v34 offset:32768\n" \
"v_smfmac_f32_16x16x64_bf16 a[112:115], v[140:143], v[52:59], v50\n" \
"v_mfma_f32_16x16x32_bf16 a[20:23], v[128:131], v[56:59], a[20:23]\n" \
"buffer_load_dwordx4 v27, s[68:71], s84 offen lds\n" \
"v_mfma_f32_16x16x32_bf16 a[20:23], v[124:127], v[52:55], a[20:23]\n" \
"s_add_u32 s88, s43, 0x0\n" \
"s_nop 0\n" \
"ds_read_b64_tr_b16 v[166:167], v34 offset:33792\n" \
"v_mfma_f32_16x16x32_bf16 a[52:55], v[116:119], v[52:55], a[52:55]\n" \
"ds_read_b64_tr_b16 v[172:173], v35 offset:32768\n" \
"ds_read_b64_tr_b16 v[174:175], v35 offset:33792\n" \
"v_mfma_f32_16x16x32_bf16 a[52:55], v[120:123], v[56:59], a[52:55]\n" \
"buffer_store_dwordx4 v[248:251], v20, s[80:83], s88 offen offset:0 sc1\n" \
"v_mfma_f32_16x16x32_bf16 a[84:87], v[56:59], v[136:139], a[84:87]\n" \
"ds_read_b64_tr_b16 v[176:177], v35 offset:40960\n" \
"ds_read_b64_tr_b16 v[178:179], v35 offset:41984\n" \
"v_mfma_f32_16x16x32_bf16 a[84:87], v[52:55], v[132:135], a[84:87]\n" \
"ds_read_b128 v[84:87], v48 offset:32768\n" \
"ds_read_b128 v[88:91], v49 offset:32768\n" \
"v_smfmac_f32_16x16x64_bf16 a[116:119], v[140:143], v[60:67], v50\n" \
"v_mfma_f32_16x16x32_bf16 a[24:27], v[124:127], v[60:63], a[24:27]\n" \
"ds_read_b128 v[104:107], v49 offset:36864\n" \
"ds_read_b128 v[108:111], v48 offset:38912\n" \
"v_mfma_f32_16x16x32_bf16 a[24:27], v[128:131], v[64:67], a[24:27]\n" \
"s_add_u32 s89, s45, 0x1000\n" \
"ds_read_b128 v[112:115], v49 offset:38912\n" \
"v_mfma_f32_16x16x32_bf16 a[56:59], v[120:123], v[64:67], a[56:59]\n" \
"buffer_store_dwordx4 v[184:187], v20, s[80:83], s89 offen offset:2048 sc1\n" \
"v_mfma_f32_16x16x32_bf16 a[56:59], v[116:119], v[60:63], a[56:59]\n" \
"s_add_u32 s62, s62, 0x80\n" \
"s_and_b32 s95, s21, 0xffff\n" \
"v_mfma_f32_16x16x32_bf16 a[88:91], v[60:63], v[132:135], a[88:91]\n" \
"s_cmp_lt_u32 s62, 0x480\n" \
"s_cselect_b32 s62, s62, 0\n" \
"v_mfma_f32_16x16x32_bf16 a[88:91], v[64:67], v[136:139], a[88:91]\n" \
"s_lshl_b32 s72, s62, 6\n" \
"s_add_u32 s72, s72, s37\n" \
"v_smfmac_f32_16x16x64_bf16 a[120:123], v[140:143], v[68:75], v50\n" \
"v_mfma_f32_16x16x32_bf16 a[28:31], v[128:131], v[72:75], a[28:31]\n" \
"s_lshl_b32 s63, s62, 12\n" \
"v_writelane_b32 v11, s95, 12\n" \
"v_mfma_f32_16x16x32_bf16 a[28:31], v[124:127], v[68:71], a[28:31]\n" \
"s_and_b32 s95, 1, %[w]\n" \
"s_lshr_b32 s96, %[w], 1\n" \
"v_mfma_f32_16x16x32_bf16 a[60:63], v[116:119], v[68:71], a[60:63]\n" \
"s_add_u32 s88, s45, 0x1000\n" \
"s_lshl_b32 s94, %[w], 11\n" \
"v_mfma_f32_16x16x32_bf16 a[60:63], v[120:123], v[72:75], a[60:63]\n" \
"buffer_store_dwordx4 v[252:255], v20, s[80:83], s88 offen offset:3072 sc1\n" \
"v_mfma_f32_16x16x32_bf16 a[92:95], v[72:75], v[136:139], a[92:95]\n" \
"s_lshl_b32 s15, s95, 12\n" \
"s_lshl_b32 s16, s96, 14\n" \
"v_mfma_f32_16x16x32_bf16 a[92:95], v[68:71], v[132:135], a[92:95]\n" \
"s_add_u32 s12, 0xc000, s94\n" \
"s_add_u32 s97, s16, s15\n" \
"v_smfmac_f32_16x16x64_bf16 a[124:127], v[140:143], v[76:83], v50\n" \
"v_mfma_f32_16x16x32_bf16 a[0:3], v[124:127], v[76:79], a[0:3]\n" \
"s_lshr_b32 s100, %[b], 5\n" \
"s_and_b32 s65, 7, %[b]\n" \
"v_mfma_f32_16x16x32_bf16 a[0:3], v[128:131], v[80:83], a[0:3]\n" \
"s_lshr_b32 s67, %[b], 3\n" \
"s_xor_b32 s13, 0x8000, s97\n" \
"v_mfma_f32_16x16x32_bf16 a[4:7], v[120:123], v[80:83], a[4:7]\n" \
"s_add_u32 s20, 0x14000, s97\n" \
"s_mul_i32 s98, 0xe000, s96\n" \
"v_mfma_f32_16x16x32_bf16 a[4:7], v[116:119], v[76:79], a[4:7]\n" \
"s_lshl_b32 s18, s100, 1\n" \
"s_lshl_b32 s66, s65, 2\n" \
"v_mfma_f32_16x16x32_bf16 a[32:35], v[76:79], v[132:135], a[32:35]\n" \
"s_and_b32 s94, 3, s67\n" \
"s_add_u32 s99, s98, s15\n" \
"v_mfma_f32_16x16x32_bf16 a[32:35], v[80:83], v[136:139], a[32:35]\n" \
"s_add_u32 s101, s18, s96\n" \
"s_waitcnt lgkmcnt(0)\n" \
"v_mfma_f32_16x16x32_bf16 a[128:131], v[152:155], v[56:59], a[128:131]\n" \
"s_mov_b32 s64, 0x4000\n" \
"s_add_u32 s97, s66, s94\n" \
"v_mfma_f32_16x16x32_bf16 a[128:131], v[148:151], v[52:55], a[128:131]\n" \
"s_add_u32 s21, 0x10000, s99\n" \
"s_lshl_b32 s14, s101, 15\n" \
"v_mfma_f32_16x16x32_bf16 a[132:135], v[156:159], v[52:55], a[132:135]\n" \
"s_cmp_lg_u32 s100, 0\n" \
"s_cselect_b32 s17, s64, 0x8000\n" \
"v_mfma_f32_16x16x32_bf16 a[132:135], v[160:163], v[56:59], a[132:135]\n" \
"s_lshl_b32 s98, s97, 8\n" \
"s_and_b32 s67, 2, %[w]\n" \
"v_mfma_f32_16x16x32_bf16 a[136:139], v[168:171], v[56:59], a[136:139]\n" \
"s_add_u32 s99, 0x5be8020, s98\n" \
"s_lshl_b32 s101, s100, 2\n" \
"v_mfma_f32_16x16x32_bf16 a[136:139], v[164:167], v[52:55], a[136:139]\n" \
"s_lshl_b32 s66, s67, 1\n" \
"s_add_u32 s64, s99, s101\n" \
"v_mfma_f32_16x16x32_bf16 a[140:143], v[172:175], v[52:55], a[140:143]\n" \
"s_lshl_b32 s65, s100, 3\n" \
"s_add_u32 s94, s66, s95\n" \
"v_mfma_f32_16x16x32_bf16 a[140:143], v[176:179], v[56:59], a[140:143]\n" \
"s_cmp_eq_u32 %[w], 1\n" \
"s_cselect_b32 s22, s64, 0x40000000\n" \
"v_mfma_f32_16x16x32_bf16 a[160:163], v[152:155], v[64:67], a[160:163]\n" \
"s_add_u32 s98, s65, s94\n" \
"s_mul_i32 s99, 0x24800, s98\n" \
"v_mfma_f32_16x16x32_bf16 a[160:163], v[148:151], v[60:63], a[160:163]\n" \
"s_lshl_b32 s101, s97, 12\n" \
"s_sub_u32 s67, 0x78, s65\n" \
"v_mfma_f32_16x16x32_bf16 a[164:167], v[156:159], v[60:63], a[164:167]\n" \
"s_add_u32 s64, s99, s101\n" \
"s_add_u32 s66, s67, s94\n" \
"v_mfma_f32_16x16x32_bf16 a[164:167], v[160:163], v[64:67], a[164:167]\n" \
"s_add_u32 s60, 0x3004800, s64\n" \
"s_mul_i32 s95, 0x24800, s66\n" \
"v_mfma_f32_16x16x32_bf16 a[168:171], v[168:171], v[64:67], a[168:171]\n" \
"s_lshl_b32 s99, s96, 1\n" \
"s_add_u32 s98, s95, s101\n" \
"v_mfma_f32_16x16x32_bf16 a[168:171], v[164:167], v[60:63], a[168:171]\n" \
"s_add_u32 s64, s100, s99\n" \
"s_mov_b32 s65, 0x18000000\n" \
"v_mfma_f32_16x16x32_bf16 a[172:175], v[172:175], v[60:63], a[172:175]\n" \
"s_mul_i32 s94, 6, s96\n" \
"s_add_u32 s66, s96, s94\n" \
"v_mfma_f32_16x16x32_bf16 a[172:175], v[176:179], v[64:67], a[172:175]\n" \
"s_cmp_lt_u32 s64, 2\n" \
"s_cselect_b32 s67, s65, 0x30000000\n" \
"v_mfma_f32_16x16x32_bf16 a[192:195], v[152:155], v[72:75], a[192:195]\n" \
"s_mov_b32 s95, 0x80000\n" \
"s_mov_b32 s64, 0xaaaaaaaa\n" \
"v_mfma_f32_16x16x32_bf16 a[192:195], v[148:151], v[68:71], a[192:195]\n" \
"s_cmp_gt_u32 s66, s100\n" \
"s_cselect_b32 s101, s95, 0x40000\n" \
"s_waitcnt vmcnt(3)\n" \
"s_barrier\n" \
"s_mov_b64 exec, 1\n" \
"buffer_store_dword v45, off, s[80:83], s38 offset:0 sc1\n" \
"s_mov_b64 exec, -1\n" \
"v_mfma_f32_16x16x32_bf16 a[196:199], v[156:159], v[68:71], a[196:199]\n" \
"s_add_u32 m0, s40, 0x8000\n" \
"s_add_u32 s85, s72, 0x0\n" \
"s_nop 0\n" \
"v_mfma_f32_16x16x32_bf16 a[196:199], v[160:163], v[72:75], a[196:199]\n" \
"ds_read_b64_tr_b16 v[140:141], v39 offset:0\n" \
"ds_read_b64_tr_b16 v[142:143], v39 offset:960\n" \
"v_mfma_f32_16x16x32_bf16 a[200:203], v[168:171], v[72:75], a[200:203]\n" \
"buffer_load_dwordx4 v20, s[80:83], s85 offen sc1 lds\n" \
"v_mfma_f32_16x16x32_bf16 a[200:203], v[164:167], v[68:71], a[200:203]\n" \
"s_add_u32 m0, s40, 0x9000\n" \
"s_add_u32 s84, s72, 0x1000\n" \
"v_mfma_f32_16x16x32_bf16 a[204:207], v[172:175], v[68:71], a[204:207]\n" \
"ds_read_b64_tr_b16 v[128:129], v37 offset:4096\n" \
"ds_read_b64_tr_b16 v[130:131], v37 offset:4608\n" \
"v_mfma_f32_16x16x32_bf16 a[204:207], v[176:179], v[72:75], a[204:207]\n" \
"buffer_load_dwordx4 v20, s[80:83], s84 offen sc1 lds\n" \
"v_mfma_f32_16x16x32_bf16 a[224:227], v[152:155], v[80:83], a[224:227]\n" \
"s_add_u32 m0, s40, 0xb000\n" \
"s_add_u32 s85, s62, 0x0\n" \
"v_mfma_f32_16x16x32_bf16 a[224:227], v[148:151], v[76:79], a[224:227]\n" \
"ds_read_b64_tr_b16 v[124:125], v37 offset:0\n" \
"ds_read_b64_tr_b16 v[126:127], v37 offset:512\n" \
"v_mfma_f32_16x16x32_bf16 a[228:231], v[156:159], v[76:79], a[228:231]\n" \
"buffer_load_dwordx4 v21, s[76:79], s85 offen lds\n" \
"v_mfma_f32_16x16x32_bf16 a[228:231], v[160:163], v[80:83], a[228:231]\n" \
"s_add_u32 m0, %[mA], 0x8000\n" \
"s_add_u32 s84, s62, 0x0\n" \
"v_mfma_f32_16x16x32_bf16 a[232:235], v[168:171], v[80:83], a[232:235]\n" \
"ds_read_b64_tr_b16 v[132:133], v38 offset:0\n" \
"ds_read_b64_tr_b16 v[134:135], v38 offset:512\n" \
"v_mfma_f32_16x16x32_bf16 a[232:235], v[164:167], v[76:79], a[232:235]\n" \
"buffer_load_dwordx4 v24, s[76:79], s84 offen lds\n" \
"v_mfma_f32_16x16x32_bf16 a[236:239], v[172:175], v[76:79], a[236:239]\n" \
"ds_read_b64_tr_b16 v[136:137], v38 offset:4096\n" \
"ds_read_b64_tr_b16 v[138:139], v38 offset:4608\n" \
"v_mfma_f32_16x16x32_bf16 a[236:239], v[176:179], v[80:83], a[236:239]\n" \
"s_add_u32 m0, %[mA], 0x8400\n" \
"s_add_u32 s85, s62, 0x2400\n" \
"v_mfma_f32_16x16x32_bf16 a[144:147], v[152:155], v[88:91], a[144:147]\n" \
"buffer_load_dwordx4 v25, s[76:79], s85 offen lds\n" \
"v_mfma_f32_16x16x32_bf16 a[144:147], v[148:151], v[84:87], a[144:147]\n" \
"ds_read_b128 v[52:55], v28 offset:0\n" \
"ds_read_b128 v[56:59], v29 offset:0\n" \
"v_mfma_f32_16x16x32_bf16 a[148:151], v[156:159], v[84:87], a[148:151]\n" \
"s_add_u32 m0, %[mB], 0x8000\n" \
"s_add_u32 s84, s63, 0x0\n" \
"s_nop 0\n" \
"v_mfma_f32_16x16x32_bf16 a[148:151], v[160:163], v[88:91], a[148:151]\n" \
"buffer_load_dwordx4 v26, s[68:71], s84 offen lds\n" \
"v_mfma_f32_16x16x32_bf16 a[152:155], v[168:171], v[88:91], a[152:155]\n" \
"ds_read_b64_tr_b16 v[116:117], v36 offset:0\n" \
"ds_read_b64_tr_b16 v[118:119], v36 offset:512\n" \
"v_mfma_f32_16x16x32_bf16 a[152:155], v[164:167], v[84:87], a[152:155]\n" \
"ds_read_b64_tr_b16 v[120:121], v36 offset:4096\n" \
"ds_read_b64_tr_b16 v[122:123], v36 offset:4608\n" \
"v_mfma_f32_16x16x32_bf16 a[156:159], v[172:175], v[84:87], a[156:159]\n" \
"s_add_u32 m0, %[mA], 0x8800\n" \
"s_add_u32 s85, s62, 0x4800\n" \
"v_mfma_f32_16x16x32_bf16 a[156:159], v[176:179], v[88:91], a[156:159]\n" \
"buffer_load_dwordx4 v24, s[76:79], s85 offen lds\n" \
"v_mfma_f32_16x16x32_bf16 a[176:179], v[152:155], v[96:99], a[176:179]\n" \
"ds_read_b128 v[84:87], v22 offset:0\n" \
"ds_read_b128 v[88:91], v23 offset:0\n" \
"v_mfma_f32_16x16x32_bf16 a[176:179], v[148:151], v[92:95], a[176:179]\n" \
"s_add_u32 m0, %[mA], 0x8c00\n" \
"s_add_u32 s84, s62, 0x6c00\n" \
"v_mfma_f32_16x16x32_bf16 a[180:183], v[156:159], v[92:95], a[180:183]\n" \
"buffer_load_dwordx4 v25, s[76:79], s84 offen lds\n" \
"v_mfma_f32_16x16x32_bf16 a[180:183], v[160:163], v[96:99], a[180:183]\n" \
"ds_read_b128 v[60:63], v28 offset:2048\n" \
"ds_read_b128 v[64:67], v29 offset:2048\n" \
"v_mfma_f32_16x16x32_bf16 a[184:187], v[168:171], v[96:99], a[184:187]\n" \
"ds_read_b128 v[68:71], v28 offset:4096\n" \
"ds_read_b128 v[72:75], v29 offset:4096\n" \
"v_mfma_f32_16x16x32_bf16 a[184:187], v[164:167], v[92:95], a[184:187]\n" \
"s_add_u32 m0, %[mB], 0x8400\n" \
"s_add_u32 s85, s63, 0x8000\n" \
"v_mfma_f32_16x16x32_bf16 a[188:191], v[172:175], v[92:95], a[188:191]\n" \
"buffer_load_dwordx4 v26, s[68:71], s85 offen lds\n" \
"v_mfma_f32_16x16x32_bf16 a[188:191], v[176:179], v[96:99], a[188:191]\n" \
"ds_read_b128 v[76:79], v28 offset:6144\n" \
"ds_read_b128 v[80:83], v29 offset:6144\n" \
"v_mfma_f32_16x16x32_bf16 a[208:211], v[152:155], v[104:107], a[208:211]\n" \
"s_add_u32 m0, %[mA], 0x9000\n" \
"s_add_u32 s84, s62, 0x9000\n" \
"v_mfma_f32_16x16x32_bf16 a[208:211], v[148:151], v[100:103], a[208:211]\n" \
"buffer_load_dwordx4 v24, s[76:79], s84 offen lds\n" \
"v_mfma_f32_16x16x32_bf16 a[212:215], v[156:159], v[100:103], a[212:215]\n" \
"ds_read_b128 v[92:95], v48 offset:2048\n" \
"ds_read_b128 v[96:99], v49 offset:2048\n" \
"v_mfma_f32_16x16x32_bf16 a[212:215], v[160:163], v[104:107], a[212:215]\n" \
"s_mov_b32 s65, 0xaaaaaaaa\n" \
"s_add_u32 s23, s67, s101\n" \
"s_nop 0\n" \
"v_mfma_f32_16x16x32_bf16 a[216:219], v[168:171], v[104:107], a[216:219]\n" \
"s_add_u32 m0, %[mA], 0x9400\n" \
"s_add_u32 s85, s62, 0xb400\n" \
"v_mfma_f32_16x16x32_bf16 a[216:219], v[164:167], v[100:103], a[216:219]\n" \
"buffer_load_dwordx4 v25, s[76:79], s85 offen lds\n" \
"v_mfma_f32_16x16x32_bf16 a[220:223], v[172:175], v[100:103], a[220:223]\n" \
"ds_read_b128 v[100:103], v48 offset:4096\n" \
"s_mov_b32 s66, 0xfff0ffff\n" \
"v_mfma_f32_16x16x32_bf16 a[220:223], v[176:179], v[104:107], a[220:223]\n" \
"s_add_u32 m0, %[mB], 0x8800\n" \
"s_add_u32 s84, s63, 0x10000\n" \
"v_mfma_f32_16x16x32_bf16 a[240:243], v[152:155], v[112:115], a[240:243]\n" \
"buffer_load_dwordx4 v27, s[68:71], s84 offen lds\n" \
"v_mfma_f32_16x16x32_bf16 a[240:243], v[148:151], v[108:111], a[240:243]\n" \
"ds_read_b64_tr_b16 v[152:153], v32 offset:8192\n" \
"ds_read_b64_tr_b16 v[154:155], v32 offset:9216\n" \
"v_mfma_f32_16x16x32_bf16 a[244:247], v[156:159], v[108:111], a[244:247]\n" \
"ds_read_b64_tr_b16 v[148:149], v32 offset:0\n" \
"ds_read_b64_tr_b16 v[150:151], v32 offset:1024\n" \
"v_mfma_f32_16x16x32_bf16 a[244:247], v[160:163], v[112:115], a[244:247]\n" \
"s_add_u32 m0, %[mA], 0x9800\n" \
"s_add_u32 s85, s62, 0xd800\n" \
"v_mfma_f32_16x16x32_bf16 a[248:251], v[168:171], v[112:115], a[248:251]\n" \
"buffer_load_dwordx4 v24, s[76:79], s85 offen lds\n" \
"v_mfma_f32_16x16x32_bf16 a[248:251], v[164:167], v[108:111], a[248:251]\n" \
"ds_read_b64_tr_b16 v[156:157], v33 offset:0\n" \
"ds_read_b64_tr_b16 v[158:159], v33 offset:1024\n" \
"v_mfma_f32_16x16x32_bf16 a[252:255], v[172:175], v[108:111], a[252:255]\n" \
"ds_read_b64_tr_b16 v[160:161], v33 offset:8192\n" \
"ds_read_b64_tr_b16 v[162:163], v33 offset:9216\n" \
"v_mfma_f32_16x16x32_bf16 a[252:255], v[176:179], v[112:115], a[252:255]\n" \
"s_add_u32 m0, %[mA], 0x9c00\n" \
"s_waitcnt lgkmcnt(11)\n" \
"s_nop 0\n" \
"v_smfmac_f32_16x16x64_bf16 a[16:19], v[140:143], v[84:91], v50\n" \
"v_mfma_f32_16x16x32_bf16 a[48:51], v[128:131], v[88:91], a[48:51]\n" \
"s_add_u32 s85, s62, 0xfc00\n" \
"ds_read_b64_tr_b16 v[168:169], v34 offset:8192\n" \
"v_mfma_f32_16x16x32_bf16 a[48:51], v[124:127], v[84:87], a[48:51]\n" \
"buffer_load_dwordx4 v25, s[76:79], s85 offen lds\n" \
"v_mfma_f32_16x16x32_bf16 a[80:83], v[84:87], v[132:135], a[80:83]\n" \
"s_add_u32 m0, %[mB], 0x8c00\n" \
"s_add_u32 s84, s63, 0x18000\n" \
"v_mfma_f32_16x16x32_bf16 a[80:83], v[88:91], v[136:139], a[80:83]\n" \
"ds_read_b64_tr_b16 v[170:171], v34 offset:9216\n" \
"ds_read_b64_tr_b16 v[164:165], v34 offset:0\n" \
"v_smfmac_f32_16x16x64_bf16 a[112:115], v[140:143], v[52:59], v50\n" \
"v_mfma_f32_16x16x32_bf16 a[20:23], v[128:131], v[56:59], a[20:23]\n" \
"buffer_load_dwordx4 v27, s[68:71], s84 offen lds\n" \
"v_mfma_f32_16x16x32_bf16 a[20:23], v[124:127], v[52:55], a[20:23]\n" \
"s_add_u32 s89, s43, 0x1000\n" \
"ds_read_b64_tr_b16 v[166:167], v34 offset:1024\n" \
"v_mfma_f32_16x16x32_bf16 a[52:55], v[116:119], v[52:55], a[52:55]\n" \
"ds_read_b64_tr_b16 v[172:173], v35 offset:0\n" \
"ds_read_b64_tr_b16 v[174:175], v35 offset:1024\n" \
"v_mfma_f32_16x16x32_bf16 a[52:55], v[120:123], v[56:59], a[52:55]\n" \
"buffer_store_dwordx4 a[36:39], v20, s[80:83], s89 offen offset:0 sc1\n" \
"v_mfma_f32_16x16x32_bf16 a[84:87], v[56:59], v[136:139], a[84:87]\n" \
"ds_read_b64_tr_b16 v[176:177], v35 offset:8192\n" \
"ds_read_b64_tr_b16 v[178:179], v35 offset:9216\n" \
"v_mfma_f32_16x16x32_bf16 a[84:87], v[52:55], v[132:135], a[84:87]\n" \
"ds_read_b128 v[84:87], v48 offset:0\n" \
"ds_read_b128 v[88:91], v49 offset:0\n" \
"v_smfmac_f32_16x16x64_bf16 a[116:119], v[140:143], v[60:67], v50\n" \
"v_mfma_f32_16x16x32_bf16 a[24:27], v[124:127], v[60:63], a[24:27]\n" \
"ds_read_b128 v[104:107], v49 offset:4096\n" \
"ds_read_b128 v[108:111], v48 offset:6144\n" \
"v_mfma_f32_16x16x32_bf16 a[24:27], v[128:131], v[64:67], a[24:27]\n" \
"s_add_u32 s88, s43, 0x2000\n" \
"ds_read_b128 v[112:115], v49 offset:6144\n" \
"v_mfma_f32_16x16x32_bf16 a[56:59], v[120:123], v[64:67], a[56:59]\n" \
"buffer_store_dwordx4 a[64:67], v20, s[80:83], s88 offen offset:0 sc1\n" \
"v_mfma_f32_16x16x32_bf16 a[56:59], v[116:119], v[60:63], a[56:59]\n" \
"s_add_u32 s62, s62, 0x80\n" \
"s_mov_b32 s67, 0xf000ff00\n" \
"v_mfma_f32_16x16x32_bf16 a[88:91], v[60:63], v[132:135], a[88:91]\n" \
"s_cmp_lt_u32 s62, 0x480\n" \
"s_cselect_b32 s62, s62, 0\n" \
"v_mfma_f32_16x16x32_bf16 a[88:91], v[64:67], v[136:139], a[88:91]\n" \
"s_lshl_b32 s72, s62, 6\n" \
"s_add_u32 s72, s72, s37\n" \
"v_smfmac_f32_16x16x64_bf16 a[120:123], v[140:143], v[68:75], v50\n" \
"v_mfma_f32_16x16x32_bf16 a[28:31], v[128:131], v[72:75], a[28:31]\n" \
"s_lshl_b32 s63, s62, 12\n" \
"s_add_u32 s61, 0x3004800, s98\n" \
"v_mfma_f32_16x16x32_bf16 a[28:31], v[124:127], v[68:71], a[28:31]\n" \
"s_lshl_b32 s99, s97, 20\n" \
"s_lshl_b32 s98, s100, 16\n" \
"v_mfma_f32_16x16x32_bf16 a[60:63], v[116:119], v[68:71], a[60:63]\n" \
"s_add_u32 s89, s43, 0x3000\n" \
"s_add_u32 s8, 0x1000000, s99\n" \
"v_mfma_f32_16x16x32_bf16 a[60:63], v[120:123], v[72:75], a[60:63]\n" \
"buffer_store_dwordx4 a[68:71], v20, s[80:83], s89 offen offset:0 sc1\n" \
"v_mfma_f32_16x16x32_bf16 a[92:95], v[72:75], v[136:139], a[92:95]\n" \
"s_sub_u32 s19, 14, s18\n" \
"s_sub_u32 s10, 0x100000, s98\n" \
"v_mfma_f32_16x16x32_bf16 a[92:95], v[68:71], v[132:135], a[92:95]\n" \
"s_mov_b32 s11, 0x20000\n" \
"s_mov_b32 s58, 0x6000000\n" \
"v_smfmac_f32_16x16x64_bf16 a[124:127], v[140:143], v[76:83], v50\n" \
"v_mfma_f32_16x16x32_bf16 a[0:3], v[124:127], v[76:79], a[0:3]\n" \
"s_mov_b32 s59, 0x20000\n" \
"s_mov_b64 s[56:57], s[80:81]\n" \
"v_mfma_f32_16x16x32_bf16 a[0:3], v[128:131], v[80:83], a[0:3]\n" \
"s_add_u32 s8, s8, s80\n" \
"s_addc_u32 s9, s81, 0\n" \
"v_mfma_f32_16x16x32_bf16 a[4:7], v[120:123], v[80:83], a[4:7]\n" \
"v_mfma_f32_16x16x32_bf16 a[4:7], v[116:119], v[76:79], a[4:7]\n" \
"v_mfma_f32_16x16x32_bf16 a[32:35], v[76:79], v[132:135], a[32:35]\n" \
"v_mfma_f32_16x16x32_bf16 a[32:35], v[80:83], v[136:139], a[32:35]\n" \
"s_waitcnt lgkmcnt(0)\n" \
"s_nop 0\n" \
"v_mfma_f32_16x16x32_bf16 a[128:131], v[152:155], v[56:59], a[128:131]\n" \
"v_mfma_f32_16x16x32_bf16 a[128:131], v[148:151], v[52:55], a[128:131]\n" \
"v_mfma_f32_16x16x32_bf16 a[132:135], v[156:159], v[52:55], a[132:135]\n" \
"v_mfma_f32_16x16x32_bf16 a[132:135], v[160:163], v[56:59], a[132:135]\n" \
"v_mfma_f32_16x16x32_bf16 a[136:139], v[168:171], v[56:59], a[136:139]\n" \
"v_mfma_f32_16x16x32_bf16 a[136:139], v[164:167], v[52:55], a[136:139]\n" \
"v_mfma_f32_16x16x32_bf16 a[140:143], v[172:175], v[52:55], a[140:143]\n" \
"v_mfma_f32_16x16x32_bf16 a[140:143], v[176:179], v[56:59], a[140:143]\n" \
"v_mfma_f32_16x16x32_bf16 a[160:163], v[152:155], v[64:67], a[160:163]\n" \
"v_mfma_f32_16x16x32_bf16 a[160:163], v[148:151], v[60:63], a[160:163]\n" \
"v_mfma_f32_16x16x32_bf16 a[164:167], v[156:159], v[60:63], a[164:167]\n" \
"v_mfma_f32_16x16x32_bf16 a[164:167], v[160:163], v[64:67], a[164:167]\n" \
"v_mfma_f32_16x16x32_bf16 a[168:171], v[168:171], v[64:67], a[168:171]\n" \
"v_mfma_f32_16x16x32_bf16 a[168:171], v[164:167], v[60:63], a[168:171]\n" \
"v_mfma_f32_16x16x32_bf16 a[172:175], v[172:175], v[60:63], a[172:175]\n" \
"v_mfma_f32_16x16x32_bf16 a[172:175], v[176:179], v[64:67], a[172:175]\n" \
"v_mfma_f32_16x16x32_bf16 a[192:195], v[152:155], v[72:75], a[192:195]\n" \
"v_mfma_f32_16x16x32_bf16 a[192:195], v[148:151], v[68:71], a[192:195]\n" \
"s_waitcnt vmcnt(3)\n" \
"s_barrier\n" \
"v_mfma_f32_16x16x32_bf16 a[196:199], v[156:159], v[68:71], a[196:199]\n" \
"s_add_u32 m0, s40, 0x0\n" \
"s_add_u32 s85, s72, 0x0\n" \
"v_mfma_f32_16x16x32_bf16 a[196:199], v[160:163], v[72:75], a[196:199]\n" \
"buffer_load_dwordx4 v20, s[80:83], s85 offen sc1 lds\n" \
"v_mfma_f32_16x16x32_bf16 a[200:203], v[168:171], v[72:75], a[200:203]\n" \
"s_add_u32 m0, s40, 0x1000\n" \
"s_add_u32 s84, s72, 0x1000\n" \
"v_mfma_f32_16x16x32_bf16 a[200:203], v[164:167], v[68:71], a[200:203]\n" \
"ds_read_b64_tr_b16 v[140:141], v39 offset:32768\n" \
"ds_read_b64_tr_b16 v[142:143], v39 offset:33728\n" \
"v_mfma_f32_16x16x32_bf16 a[204:207], v[172:175], v[68:71], a[204:207]\n" \
"buffer_load_dwordx4 v20, s[80:83], s84 offen sc1 lds\n" \
"v_mfma_f32_16x16x32_bf16 a[204:207], v[176:179], v[72:75], a[204:207]\n" \
"ds_read_b64_tr_b16 v[128:129], v37 offset:36864\n" \
"ds_read_b64_tr_b16 v[130:131], v37 offset:37376\n" \
"v_mfma_f32_16x16x32_bf16 a[224:227], v[152:155], v[80:83], a[224:227]\n" \
"s_add_u32 m0, s40, 0x3000\n" \
"s_add_u32 s85, s62, 0x0\n" \
"v_mfma_f32_16x16x32_bf16 a[224:227], v[148:151], v[76:79], a[224:227]\n" \
"buffer_load_dwordx4 v21, s[76:79], s85 offen lds\n" \
"v_mfma_f32_16x16x32_bf16 a[228:231], v[156:159], v[76:79], a[228:231]\n" \
"ds_read_b64_tr_b16 v[124:125], v37 offset:32768\n" \
"ds_read_b64_tr_b16 v[126:127], v37 offset:33280\n" \
"v_mfma_f32_16x16x32_bf16 a[228:231], v[160:163], v[80:83], a[228:231]\n" \
"s_add_u32 m0, %[mA], 0x0\n" \
"s_add_u32 s84, s62, 0x0\n" \
"s_nop 0\n" \
"v_mfma_f32_16x16x32_bf16 a[232:235], v[168:171], v[80:83], a[232:235]\n" \
"buffer_load_dwordx4 v24, s[76:79], s84 offen lds\n" \
"v_mfma_f32_16x16x32_bf16 a[232:235], v[164:167], v[76:79], a[232:235]\n" \
"ds_read_b64_tr_b16 v[132:133], v38 offset:32768\n" \
"ds_read_b64_tr_b16 v[134:135], v38 offset:33280\n" \
"v_mfma_f32_16x16x32_bf16 a[236:239], v[172:175], v[76:79], a[236:239]\n" \
"ds_read_b64_tr_b16 v[136:137], v38 offset:36864\n" \
"ds_read_b64_tr_b16 v[138:139], v38 offset:37376\n" \
"v_mfma_f32_16x16x32_bf16 a[236:239], v[176:179], v[80:83], a[236:239]\n" \
"s_add_u32 m0, %[mA], 0x400\n" \
"s_add_u32 s85, s62, 0x2400\n" \
"v_mfma_f32_16x16x32_bf16 a[144:147], v[152:155], v[88:91], a[144:147]\n" \
"buffer_load_dwordx4 v25, s[76:79], s85 offen lds\n" \
"v_mfma_f32_16x16x32_bf16 a[144:147], v[148:151], v[84:87], a[144:147]\n" \
"ds_read_b128 v[52:55], v28 offset:32768\n" \
"ds_read_b128 v[56:59], v29 offset:32768\n" \
"v_mfma_f32_16x16x32_bf16 a[148:151], v[156:159], v[84:87], a[148:151]\n" \
"s_add_u32 m0, %[mB], 0x0\n" \
"s_add_u32 s84, s63, 0x0\n" \
"v_mfma_f32_16x16x32_bf16 a[148:151], v[160:163], v[88:91], a[148:151]\n" \
"buffer_load_dwordx4 v26, s[68:71], s84 offen lds\n" \
"v_mfma_f32_16x16x32_bf16 a[152:155], v[168:171], v[88:91], a[152:155]\n" \
"ds_read_b64_tr_b16 v[116:117], v36 offset:32768\n" \
"ds_read_b64_tr_b16 v[118:119], v36 offset:33280\n" \
"v_mfma_f32_16x16x32_bf16 a[152:155], v[164:167], v[84:87], a[152:155]\n" \
"ds_read_b64_tr_b16 v[120:121], v36 offset:36864\n" \
"ds_read_b64_tr_b16 v[122:123], v36 offset:37376\n" \
"v_mfma_f32_16x16x32_bf16 a[156:159], v[172:175], v[84:87], a[156:159]\n" \
"s_add_u32 m0, %[mA], 0x800\n" \
"s_add_u32 s85, s62, 0x4800\n" \
"v_mfma_f32_16x16x32_bf16 a[156:159], v[176:179], v[88:91], a[156:159]\n" \
"buffer_load_dwordx4 v24, s[76:79], s85 offen lds\n" \
"v_mfma_f32_16x16x32_bf16 a[176:179], v[152:155], v[96:99], a[176:179]\n" \
"ds_read_b128 v[84:87], v22 offset:32768\n" \
"ds_read_b128 v[88:91], v23 offset:32768\n" \
"v_mfma_f32_16x16x32_bf16 a[176:179], v[148:151], v[92:95], a[176:179]\n" \
"s_add_u32 m0, %[mA], 0xc00\n" \
"s_add_u32 s84, s62, 0x6c00\n" \
"v_mfma_f32_16x16x32_bf16 a[180:183], v[156:159], v[92:95], a[180:183]\n" \
"buffer_load_dwordx4 v25, s[76:79], s84 offen lds\n" \
"v_mfma_f32_16x16x32_bf16 a[180:183], v[160:163], v[96:99], a[180:183]\n" \
"ds_read_b128 v[60:63], v28 offset:34816\n" \
"ds_read_b128 v[64:67], v29 offset:34816\n" \
"v_mfma_f32_16x16x32_bf16 a[184:187], v[168:171], v[96:99], a[184:187]\n" \
"ds_read_b128 v[68:71], v28 offset:36864\n" \
"ds_read_b128 v[72:75], v29 offset:36864\n" \
"v_mfma_f32_16x16x32_bf16 a[184:187], v[164:167], v[92:95], a[184:187]\n" \
"s_add_u32 m0, %[mB], 0x400\n" \
"s_add_u32 s85, s63, 0x8000\n" \
"v_mfma_f32_16x16x32_bf16 a[188:191], v[172:175], v[92:95], a[188:191]\n" \
"buffer_load_dwordx4 v26, s[68:71], s85 offen lds\n" \
"v_mfma_f32_16x16x32_bf16 a[188:191], v[176:179], v[96:99], a[188:191]\n" \
"ds_read_b128 v[76:79], v28 offset:38912\n" \
"ds_read_b128 v[80:83], v29 offset:38912\n" \
"v_mfma_f32_16x16x32_bf16 a[208:211], v[152:155], v[104:107], a[208:211]\n" \
"s_add_u32 m0, %[mA], 0x1000\n" \
"s_add_u32 s84, s62, 0x9000\n" \
"v_mfma_f32_16x16x32_bf16 a[208:211], v[148:151], v[100:103], a[208:211]\n" \
"buffer_load_dwordx4 v24, s[76:79], s84 offen lds\n" \
"v_mfma_f32_16x16x32_bf16 a[212:215], v[156:159], v[100:103], a[212:215]\n" \
"ds_read_b128 v[92:95], v48 offset:34816\n" \
"ds_read_b128 v[96:99], v49 offset:34816\n" \
"v_mfma_f32_16x16x32_bf16 a[212:215], v[160:163], v[104:107], a[212:215]\n" \
"s_mov_b32 s72, 0x3eae1a93\n" \
"v_and_b32_e32 v26, 0x1ff, v20\n" \
"v_mfma_f32_16x16x32_bf16 a[216:219], v[168:171], v[104:107], a[216:219]\n" \
"s_add_u32 m0, %[mA], 0x1400\n" \
"s_add_u32 s85, s62, 0xb400\n" \
"v_mfma_f32_16x16x32_bf16 a[216:219], v[164:167], v[100:103], a[216:219]\n" \
"buffer_load_dwordx4 v25, s[76:79], s85 offen lds\n" \
"v_mfma_f32_16x16x32_bf16 a[220:223], v[172:175], v[100:103], a[220:223]\n" \
"ds_read_b128 v[100:103], v48 offset:36864\n" \
"v_add_u32_e64 v21, s48, v20\n" \
"v_mfma_f32_16x16x32_bf16 a[220:223], v[176:179], v[104:107], a[220:223]\n" \
"s_add_u32 m0, %[mB], 0x800\n" \
"s_add_u32 s84, s63, 0x10000\n" \
"v_mfma_f32_16x16x32_bf16 a[240:243], v[152:155], v[112:115], a[240:243]\n" \
"buffer_load_dwordx4 v27, s[68:71], s84 offen lds\n" \
"v_mfma_f32_16x16x32_bf16 a[240:243], v[148:151], v[108:111], a[240:243]\n" \
"ds_read_b64_tr_b16 v[152:153], v32 offset:40960\n" \
"ds_read_b64_tr_b16 v[154:155], v32 offset:41984\n" \
"v_mfma_f32_16x16x32_bf16 a[244:247], v[156:159], v[108:111], a[244:247]\n" \
"L_pf8:\n" \
"s_cbranch_execz L_pf12\n" \
"s_nop 0\n" \
"ds_read_b64_tr_b16 v[148:149], v32 offset:32768\n" \
"ds_read_b64_tr_b16 v[150:151], v32 offset:33792\n" \
"v_mfma_f32_16x16x32_bf16 a[244:247], v[160:163], v[112:115], a[244:247]\n" \
"s_add_u32 m0, %[mA], 0x1800\n" \
"s_add_u32 s85, s62, 0xd800\n" \
"v_mfma_f32_16x16x32_bf16 a[248:251], v[168:171], v[112:115], a[248:251]\n" \
"buffer_load_dwordx4 v24, s[76:79], s85 offen lds\n" \
"v_mfma_f32_16x16x32_bf16 a[248:251], v[164:167], v[108:111], a[248:251]\n" \
"ds_read_b64_tr_b16 v[156:157], v33 offset:32768\n" \
"ds_read_b64_tr_b16 v[158:159], v33 offset:33792\n" \
"v_mfma_f32_16x16x32_bf16 a[252:255], v[172:175], v[108:111], a[252:255]\n" \
"ds_read_b64_tr_b16 v[160:161], v33 offset:40960\n" \
"ds_read_b64_tr_b16 v[162:163], v33 offset:41984\n" \
"v_mfma_f32_16x16x32_bf16 a[252:255], v[176:179], v[112:115], a[252:255]\n" \
"s_add_u32 m0, %[mA], 0x1c00\n" \
"s_waitcnt lgkmcnt(11)\n" \
"v_smfmac_f32_16x16x64_bf16 a[16:19], v[140:143], v[84:91], v50\n" \
"v_mfma_f32_16x16x32_bf16 a[48:51], v[128:131], v[88:91], a[48:51]\n" \
"s_add_u32 s85, s62, 0xfc00\n" \
"ds_read_b64_tr_b16 v[168:169], v34 offset:40960\n" \
"v_mfma_f32_16x16x32_bf16 a[48:51], v[124:127], v[84:87], a[48:51]\n" \
"buffer_load_dwordx4 v25, s[76:79], s85 offen lds\n" \
"s_mov_b32 s76, s54\n" \
"v_mfma_f32_16x16x32_bf16 a[80:83], v[84:87], v[132:135], a[80:83]\n" \
"s_add_u32 m0, %[mB], 0xc00\n" \
"s_add_u32 s84, s63, 0x18000\n" \
"v_mfma_f32_16x16x32_bf16 a[80:83], v[88:91], v[136:139], a[80:83]\n" \
"ds_read_b64_tr_b16 v[170:171], v34 offset:41984\n" \
"ds_read_b64_tr_b16 v[164:165], v34 offset:32768\n" \
"v_smfmac_f32_16x16x64_bf16 a[112:115], v[140:143], v[52:59], v50\n" \
"v_mfma_f32_16x16x32_bf16 a[20:23], v[128:131], v[56:59], a[20:23]\n" \
"buffer_load_dwordx4 v27, s[68:71], s84 offen lds\n" \
"v_mfma_f32_16x16x32_bf16 a[20:23], v[124:127], v[52:55], a[20:23]\n" \
"s_add_u32 s88, s43, 0x0\n" \
"s_nop 0\n" \
"ds_read_b64_tr_b16 v[166:167], v34 offset:33792\n" \
"v_mfma_f32_16x16x32_bf16 a[52:55], v[116:119], v[52:55], a[52:55]\n" \
"ds_read_b64_tr_b16 v[172:173], v35 offset:32768\n" \
"ds_read_b64_tr_b16 v[174:175], v35 offset:33792\n" \
"v_mfma_f32_16x16x32_bf16 a[52:55], v[120:123], v[56:59], a[52:55]\n" \
"buffer_store_dwordx4 a[96:99], v20, s[80:83], s88 offen offset:1024 sc1\n" \
"v_mfma_f32_16x16x32_bf16 a[84:87], v[56:59], v[136:139], a[84:87]\n" \
"ds_read_b64_tr_b16 v[176:177], v35 offset:40960\n" \
"ds_read_b64_tr_b16 v[178:179], v35 offset:41984\n" \
"v_mfma_f32_16x16x32_bf16 a[84:87], v[52:55], v[132:135], a[84:87]\n" \
"ds_read_b128 v[84:87], v48 offset:32768\n" \
"ds_read_b128 v[88:91], v49 offset:32768\n" \
"v_smfmac_f32_16x16x64_bf16 a[116:119], v[140:143], v[60:67], v50\n" \
"v_mfma_f32_16x16x32_bf16 a[24:27], v[124:127], v[60:63], a[24:27]\n" \
"ds_read_b128 v[104:107], v49 offset:36864\n" \
"ds_read_b128 v[108:111], v48 offset:38912\n" \
"v_mfma_f32_16x16x32_bf16 a[24:27], v[128:131], v[64:67], a[24:27]\n" \
"s_add_u32 s89, s43, 0x1000\n" \
"ds_read_b128 v[112:115], v49 offset:38912\n" \
"v_mfma_f32_16x16x32_bf16 a[56:59], v[120:123], v[64:67], a[56:59]\n" \
"buffer_store_dwordx4 a[100:103], v20, s[80:83], s89 offen offset:1024 sc1\n" \
"s_add_u32 s89, s43, 0x3000\n" \
"v_mfma_f32_16x16x32_bf16 a[56:59], v[116:119], v[60:63], a[56:59]\n" \
"v_accvgpr_read_b32 v248, a16\n" \
"v_accvgpr_read_b32 v249, a17\n" \
"v_mfma_f32_16x16x32_bf16 a[88:91], v[60:63], v[132:135], a[88:91]\n" \
"v_accvgpr_read_b32 v250, a18\n" \
"v_accvgpr_read_b32 v251, a19\n" \
"v_mfma_f32_16x16x32_bf16 a[88:91], v[64:67], v[136:139], a[88:91]\n" \
"v_accvgpr_read_b32 v180, a48\n" \
"v_accvgpr_read_b32 v181, a49\n" \
"v_smfmac_f32_16x16x64_bf16 a[120:123], v[140:143], v[68:75], v50\n" \
"v_mfma_f32_16x16x32_bf16 a[28:31], v[128:131], v[72:75], a[28:31]\n" \
"v_accvgpr_read_b32 v182, a50\n" \
"v_accvgpr_read_b32 v183, a51\n" \
"v_mfma_f32_16x16x32_bf16 a[28:31], v[124:127], v[68:71], a[28:31]\n" \
"v_accvgpr_read_b32 v252, a80\n" \
"v_accvgpr_read_b32 v253, a81\n" \
"v_mfma_f32_16x16x32_bf16 a[60:63], v[116:119], v[68:71], a[60:63]\n" \
"s_add_u32 s88, s43, 0x2000\n" \
"v_accvgpr_read_b32 v254, a82\n" \
"v_mfma_f32_16x16x32_bf16 a[60:63], v[120:123], v[72:75], a[60:63]\n" \
"buffer_store_dwordx4 a[8:11], v20, s[80:83], s88 offen offset:1024 sc1\n" \
"s_add_u32 s88, s45, 0x0\n" \
"s_nop 0\n" \
"v_mfma_f32_16x16x32_bf16 a[92:95], v[72:75], v[136:139], a[92:95]\n" \
"v_accvgpr_read_b32 v255, a83\n" \
"v_accvgpr_read_b32 v196, a112\n" \
"v_mfma_f32_16x16x32_bf16 a[92:95], v[68:71], v[132:135], a[92:95]\n" \
"v_accvgpr_read_b32 v197, a113\n" \
"v_accvgpr_read_b32 v198, a114\n" \
"v_smfmac_f32_16x16x64_bf16 a[124:127], v[140:143], v[76:83], v50\n" \
"v_mfma_f32_16x16x32_bf16 a[0:3], v[124:127], v[76:79], a[0:3]\n" \
"v_accvgpr_read_b32 v199, a115\n" \
"v_accvgpr_read_b32 v188, a20\n" \
"v_mfma_f32_16x16x32_bf16 a[0:3], v[128:131], v[80:83], a[0:3]\n" \
"v_accvgpr_read_b32 v189, a21\n" \
"v_accvgpr_read_b32 v190, a22\n" \
"v_mfma_f32_16x16x32_bf16 a[4:7], v[120:123], v[80:83], a[4:7]\n" \
"v_accvgpr_read_b32 v191, a23\n" \
"v_accvgpr_read_b32 v184, a52\n" \
"v_mfma_f32_16x16x32_bf16 a[4:7], v[116:119], v[76:79], a[4:7]\n" \
"v_lshlrev_b32_e32 v45, 10, v20\n" \
"v_add_u32_e32 v45, s41, v45\n" \
"v_mfma_f32_16x16x32_bf16 a[32:35], v[76:79], v[132:135], a[32:35]\n" \
"s_mov_b32 m0, %[mS]\n" \
"v_accvgpr_read_b32 v185, a53\n" \
"v_mfma_f32_16x16x32_bf16 a[32:35], v[80:83], v[136:139], a[32:35]\n" \
"v_accvgpr_read_b32 v186, a54\n" \
"s_waitcnt lgkmcnt(0)\n" \
"v_mfma_f32_16x16x32_bf16 a[128:131], v[152:155], v[56:59], a[128:131]\n" \
"v_accvgpr_read_b32 v187, a55\n" \
"v_accvgpr_read_b32 v192, a84\n" \
"v_mfma_f32_16x16x32_bf16 a[128:131], v[148:151], v[52:55], a[128:131]\n" \
"v_accvgpr_read_b32 v193, a85\n" \
"v_accvgpr_read_b32 v194, a86\n" \
"v_mfma_f32_16x16x32_bf16 a[132:135], v[156:159], v[52:55], a[132:135]\n" \
"v_accvgpr_read_b32 v195, a87\n" \
"v_accvgpr_read_b32 v212, a116\n" \
"s_getpc_b64 s[86:87]\n" \
"L_ct15_%=:\n" \
"s_add_u32 s86, s86, L_end_%=-L_ct15_%=\n" \
"s_addc_u32 s87, s87, 0\n" \
"v_mfma_f32_16x16x32_bf16 a[132:135], v[160:163], v[56:59], a[132:135]\n" \
"v_accvgpr_read_b32 v213, a117\n" \
"v_accvgpr_read_b32 v214, a118\n" \
"v_mfma_f32_16x16x32_bf16 a[136:139], v[168:171], v[56:59], a[136:139]\n" \
"v_accvgpr_read_b32 v215, a119\n" \
"v_accvgpr_read_b32 v204, a24\n" \
"v_mfma_f32_16x16x32_bf16 a[136:139], v[164:167], v[52:55], a[136:139]\n" \
"v_accvgpr_read_b32 v205, a25\n" \
"v_accvgpr_read_b32 v206, a26\n" \
"v_mfma_f32_16x16x32_bf16 a[140:143], v[172:175], v[52:55], a[140:143]\n" \
"v_accvgpr_read_b32 v207, a27\n" \
"v_accvgpr_read_b32 v200, a56\n" \
"v_mfma_f32_16x16x32_bf16 a[140:143], v[176:179], v[56:59], a[140:143]\n" \
"v_accvgpr_read_b32 v201, a57\n" \
"v_accvgpr_read_b32 v202, a58\n" \
"v_mfma_f32_16x16x32_bf16 a[160:163], v[152:155], v[64:67], a[160:163]\n" \
"v_accvgpr_read_b32 v203, a59\n" \
"v_accvgpr_read_b32 v208, a88\n" \
"v_mfma_f32_16x16x32_bf16 a[160:163], v[148:151], v[60:63], a[160:163]\n" \
"v_accvgpr_read_b32 v209, a89\n" \
"v_accvgpr_read_b32 v210, a90\n" \
"v_mfma_f32_16x16x32_bf16 a[164:167], v[156:159], v[60:63], a[164:167]\n" \
"v_accvgpr_read_b32 v211, a91\n" \
"v_accvgpr_read_b32 v228, a120\n" \
"v_mfma_f32_16x16x32_bf16 a[164:167], v[160:163], v[64:67], a[164:167]\n" \
"v_accvgpr_read_b32 v229, a121\n" \
"v_accvgpr_read_b32 v230, a122\n" \
"v_mfma_f32_16x16x32_bf16 a[168:171], v[168:171], v[64:67], a[168:171]\n" \
"v_accvgpr_read_b32 v231, a123\n" \
"v_accvgpr_read_b32 v220, a28\n" \
"v_mfma_f32_16x16x32_bf16 a[168:171], v[164:167], v[60:63], a[168:171]\n" \
"v_accvgpr_read_b32 v221, a29\n" \
"v_accvgpr_read_b32 v222, a30\n" \
"v_mfma_f32_16x16x32_bf16 a[172:175], v[172:175], v[60:63], a[172:175]\n" \
"v_accvgpr_read_b32 v223, a31\n" \
"v_accvgpr_read_b32 v216, a60\n" \
"v_mfma_f32_16x16x32_bf16 a[172:175], v[176:179], v[64:67], a[172:175]\n" \
"v_accvgpr_read_b32 v217, a61\n" \
"v_accvgpr_read_b32 v218, a62\n" \
"v_mfma_f32_16x16x32_bf16 a[192:195], v[152:155], v[72:75], a[192:195]\n" \
"v_accvgpr_read_b32 v219, a63\n" \
"v_accvgpr_read_b32 v224, a92\n" \
"v_mfma_f32_16x16x32_bf16 a[192:195], v[148:151], v[68:71], a[192:195]\n" \
"v_accvgpr_read_b32 v225, a93\n" \
"v_accvgpr_read_b32 v226, a94\n" \
"s_waitcnt vmcnt(3)\n" \
"s_barrier\n" \
"s_mov_b64 exec, 3\n" \
"global_load_lds_dword v45, s[86:87]\n" \
"s_mov_b64 exec, -1\n" \
"s_sub_u32 m0, %[mB], 0x10000\n" \
"v_mfma_f32_16x16x32_bf16 a[196:199], v[156:159], v[68:71], a[196:199]\n" \
"ds_read_b64_tr_b16 v[140:141], v39 offset:0\n" \
"ds_read_b64_tr_b16 v[142:143], v39 offset:960\n" \
"v_mfma_f32_16x16x32_bf16 a[196:199], v[160:163], v[72:75], a[196:199]\n" \
"ds_read_b64_tr_b16 v[128:129], v37 offset:4096\n" \
"ds_read_b64_tr_b16 v[130:131], v37 offset:4608\n" \
"v_mfma_f32_16x16x32_bf16 a[200:203], v[168:171], v[72:75], a[200:203]\n" \
"ds_read_b64_tr_b16 v[124:125], v37 offset:0\n" \
"ds_read_b64_tr_b16 v[126:127], v37 offset:512\n" \
"v_mfma_f32_16x16x32_bf16 a[200:203], v[164:167], v[68:71], a[200:203]\n" \
"ds_read_b64_tr_b16 v[132:133], v38 offset:0\n" \
"ds_read_b64_tr_b16 v[134:135], v38 offset:512\n" \
"v_mfma_f32_16x16x32_bf16 a[204:207], v[172:175], v[68:71], a[204:207]\n" \
"ds_read_b64_tr_b16 v[136:137], v38 offset:4096\n" \
"ds_read_b64_tr_b16 v[138:139], v38 offset:4608\n" \
"v_mfma_f32_16x16x32_bf16 a[204:207], v[176:179], v[72:75], a[204:207]\n" \
"ds_read_b128 v[52:55], v28 offset:0\n" \
"ds_read_b128 v[56:59], v29 offset:0\n" \
"v_mfma_f32_16x16x32_bf16 a[224:227], v[152:155], v[80:83], a[224:227]\n" \
"ds_read_b64_tr_b16 v[116:117], v36 offset:0\n" \
"ds_read_b64_tr_b16 v[118:119], v36 offset:512\n" \
"v_mfma_f32_16x16x32_bf16 a[224:227], v[148:151], v[76:79], a[224:227]\n" \
"ds_read_b64_tr_b16 v[120:121], v36 offset:4096\n" \
"ds_read_b64_tr_b16 v[122:123], v36 offset:4608\n" \
"v_mfma_f32_16x16x32_bf16 a[228:231], v[156:159], v[76:79], a[228:231]\n" \
"ds_read_b128 v[60:63], v28 offset:2048\n" \
"ds_read_b128 v[64:67], v29 offset:2048\n" \
"v_mfma_f32_16x16x32_bf16 a[228:231], v[160:163], v[80:83], a[228:231]\n" \
"v_accvgpr_read_b32 v227, a95\n" \
"v_accvgpr_read_b32 v244, a124\n" \
"v_mfma_f32_16x16x32_bf16 a[232:235], v[168:171], v[80:83], a[232:235]\n" \
"ds_read_b128 v[68:71], v28 offset:4096\n" \
"ds_read_b128 v[72:75], v29 offset:4096\n" \
"v_mfma_f32_16x16x32_bf16 a[232:235], v[164:167], v[76:79], a[232:235]\n" \
"v_accvgpr_read_b32 v245, a125\n" \
"v_accvgpr_read_b32 v246, a126\n" \
"v_mfma_f32_16x16x32_bf16 a[236:239], v[172:175], v[76:79], a[236:239]\n" \
"ds_read_b128 v[76:79], v28 offset:6144\n" \
"v_accvgpr_read_b32 v247, a127\n" \
"v_mfma_f32_16x16x32_bf16 a[236:239], v[176:179], v[80:83], a[236:239]\n" \
"ds_read_b128 v[80:83], v29 offset:6144\n" \
"v_accvgpr_read_b32 v236, a0\n" \
"v_mfma_f32_16x16x32_bf16 a[144:147], v[152:155], v[88:91], a[144:147]\n" \
"v_accvgpr_read_b32 v237, a1\n" \
"v_accvgpr_read_b32 v238, a2\n" \
"v_mfma_f32_16x16x32_bf16 a[144:147], v[148:151], v[84:87], a[144:147]\n" \
"v_accvgpr_read_b32 v239, a3\n" \
"v_accvgpr_read_b32 v232, a4\n" \
"v_mfma_f32_16x16x32_bf16 a[148:151], v[156:159], v[84:87], a[148:151]\n" \
"v_accvgpr_read_b32 v233, a5\n" \
"v_accvgpr_read_b32 v234, a6\n" \
"v_mfma_f32_16x16x32_bf16 a[148:151], v[160:163], v[88:91], a[148:151]\n" \
"v_accvgpr_read_b32 v235, a7\n" \
"v_accvgpr_read_b32 v240, a32\n" \
"v_mfma_f32_16x16x32_bf16 a[152:155], v[168:171], v[88:91], a[152:155]\n" \
"v_accvgpr_read_b32 v241, a33\n" \
"v_accvgpr_read_b32 v242, a34\n" \
"v_mfma_f32_16x16x32_bf16 a[152:155], v[164:167], v[84:87], a[152:155]\n" \
"v_accvgpr_read_b32 v243, a35\n" \
"s_mov_b32 s78, -1\n" \
"v_mfma_f32_16x16x32_bf16 a[156:159], v[172:175], v[84:87], a[156:159]\n" \
"ds_read_b128 v[84:87], v22 offset:0\n" \
"s_mov_b32 s79, 0\n" \
"v_mfma_f32_16x16x32_bf16 a[156:159], v[176:179], v[88:91], a[156:159]\n" \
"ds_read_b128 v[88:91], v23 offset:0\n" \
"s_mov_b32 s68, 0xffc0fffc\n" \
"v_mfma_f32_16x16x32_bf16 a[176:179], v[152:155], v[96:99], a[176:179]\n" \
"s_mov_b32 s69, 0xc000fc00\n" \
"v_lshrrev_b32_e32 v24, 9, v20\n" \
"v_mfma_f32_16x16x32_bf16 a[176:179], v[148:151], v[92:95], a[176:179]\n" \
"v_lshrrev_b32_e32 v25, 8, v20\n" \
"v_mul_u32_u24_e32 v24, 0x24800, v24\n" \
"v_mfma_f32_16x16x32_bf16 a[180:183], v[156:159], v[92:95], a[180:183]\n" \
"v_mul_u32_u24_e32 v25, 0x24700, v25\n" \
"v_add_u32_e64 v24, v24, v26\n" \
"v_mfma_f32_16x16x32_bf16 a[180:183], v[160:163], v[96:99], a[180:183]\n" \
"v_add_u32_e32 v25, v25, v20\n" \
"s_add_u32 s85, s50, 0x0\n" \
"v_mfma_f32_16x16x32_bf16 a[184:187], v[168:171], v[96:99], a[184:187]\n" \
"s_add_u32 s87, s50, 0x49000\n" \
"s_mov_b32 s70, 0x3e0293ee\n" \
"v_mfma_f32_16x16x32_bf16 a[184:187], v[164:167], v[92:95], a[184:187]\n" \
"s_mov_b32 s71, 0x3e0293ee\n" \
"v_add_u32_e64 v27, s47, v20\n" \
"v_mfma_f32_16x16x32_bf16 a[188:191], v[172:175], v[92:95], a[188:191]\n" \
"ds_read_b128 v[92:95], v48 offset:2048\n" \
"v_lshrrev_b32_e32 v26, 2, v20\n" \
"v_mfma_f32_16x16x32_bf16 a[188:191], v[176:179], v[96:99], a[188:191]\n" \
"ds_read_b128 v[96:99], v49 offset:2048\n" \
"v_add_u32_e32 v26, s46, v26\n" \
"v_mfma_f32_16x16x32_bf16 a[208:211], v[152:155], v[104:107], a[208:211]\n" \
"v_mfma_f32_16x16x32_bf16 a[208:211], v[148:151], v[100:103], a[208:211]\n" \
"v_mfma_f32_16x16x32_bf16 a[212:215], v[156:159], v[100:103], a[212:215]\n" \
"v_mfma_f32_16x16x32_bf16 a[212:215], v[160:163], v[104:107], a[212:215]\n" \
"v_mfma_f32_16x16x32_bf16 a[216:219], v[168:171], v[104:107], a[216:219]\n" \
"v_mfma_f32_16x16x32_bf16 a[216:219], v[164:167], v[100:103], a[216:219]\n" \
"v_mfma_f32_16x16x32_bf16 a[220:223], v[172:175], v[100:103], a[220:223]\n" \
"ds_read_b128 v[100:103], v48 offset:4096\n" \
"v_mfma_f32_16x16x32_bf16 a[220:223], v[176:179], v[104:107], a[220:223]\n" \
"ds_read_b128 v[104:107], v49 offset:4096\n" \
"v_mfma_f32_16x16x32_bf16 a[240:243], v[152:155], v[112:115], a[240:243]\n" \
"ds_read_b64_tr_b16 v[152:153], v32 offset:8192\n" \
"ds_read_b64_tr_b16 v[154:155], v32 offset:9216\n" \
"v_mfma_f32_16x16x32_bf16 a[240:243], v[148:151], v[108:111], a[240:243]\n" \
"ds_read_b64_tr_b16 v[148:149], v32 offset:0\n" \
"ds_read_b64_tr_b16 v[150:151], v32 offset:1024\n" \
"v_mfma_f32_16x16x32_bf16 a[244:247], v[156:159], v[108:111], a[244:247]\n" \
"ds_read_b64_tr_b16 v[156:157], v33 offset:0\n" \
"ds_read_b64_tr_b16 v[158:159], v33 offset:1024\n" \
"v_mfma_f32_16x16x32_bf16 a[244:247], v[160:163], v[112:115], a[244:247]\n" \
"ds_read_b64_tr_b16 v[160:161], v33 offset:8192\n" \
"ds_read_b64_tr_b16 v[162:163], v33 offset:9216\n" \
"v_mfma_f32_16x16x32_bf16 a[248:251], v[168:171], v[112:115], a[248:251]\n" \
"ds_read_b64_tr_b16 v[168:169], v34 offset:8192\n" \
"ds_read_b64_tr_b16 v[170:171], v34 offset:9216\n" \
"v_mfma_f32_16x16x32_bf16 a[248:251], v[164:167], v[108:111], a[248:251]\n" \
"ds_read_b64_tr_b16 v[164:165], v34 offset:0\n" \
"ds_read_b64_tr_b16 v[166:167], v34 offset:1024\n" \
"v_mfma_f32_16x16x32_bf16 a[252:255], v[172:175], v[108:111], a[252:255]\n" \
"ds_read_b64_tr_b16 v[172:173], v35 offset:0\n" \
"ds_read_b64_tr_b16 v[174:175], v35 offset:1024\n" \
"v_mfma_f32_16x16x32_bf16 a[252:255], v[176:179], v[112:115], a[252:255]\n" \
"ds_read_b64_tr_b16 v[176:177], v35 offset:8192\n" \
"s_waitcnt lgkmcnt(15)\n" \
"s_nop 0\n" \
"v_smfmac_f32_16x16x64_bf16 v[248:251], v[140:143], v[84:91], v50\n" \
"v_mfma_f32_16x16x32_bf16 v[180:183], v[128:131], v[88:91], v[180:183]\n" \
"ds_read_b64_tr_b16 v[178:179], v35 offset:9216\n" \
"v_bfe_u32 v44, v20, 2, 5\n" \
"v_mfma_f32_16x16x32_bf16 v[180:183], v[124:127], v[84:87], v[180:183]\n" \
"buffer_load_dword v44, v44, s[80:83], s39 offen offset:0 sc1\n" \
"v_mfma_f32_16x16x32_bf16 v[252:255], v[84:87], v[132:135], v[252:255]\n" \
"ds_read_b128 v[84:87], v48 offset:0\n" \
"ds_read_b128 v[108:111], v48 offset:6144\n" \
"v_mfma_f32_16x16x32_bf16 v[252:255], v[88:91], v[136:139], v[252:255]\n" \
"ds_read_b128 v[88:91], v49 offset:0\n" \
"ds_read_b128 v[112:115], v49 offset:6144\n" \
"v_smfmac_f32_16x16x64_bf16 v[196:199], v[140:143], v[52:59], v50\n" \
"v_mfma_f32_16x16x32_bf16 v[188:191], v[128:131], v[56:59], v[188:191]\n" \
"v_cvt_pk_bf16_f32 v180, v180, v181\n" \
"v_cvt_pk_bf16_f32 v181, v182, v183\n" \
"v_mfma_f32_16x16x32_bf16 v[188:191], v[124:127], v[52:55], v[188:191]\n" \
"v_add_f32_e64 v249, v249, v251\n" \
"v_cvt_pk_bf16_f32 v182, v249, 0\n" \
"v_mfma_f32_16x16x32_bf16 v[184:187], v[116:119], v[52:55], v[184:187]\n" \
"v_mov_b32_e32 v183, 0x3f80\n" \
"v_add_f32_e64 v196, v196, v198\n" \
"v_mfma_f32_16x16x32_bf16 v[184:187], v[120:123], v[56:59], v[184:187]\n" \
"v_mfma_f32_16x16x16_bf16 v[248:251], v[182:183], v[46:47], 0\n" \
"v_add_f32_e64 v197, v197, v199\n" \
"v_cvt_pk_bf16_f32 v188, v188, v189\n" \
"v_mfma_f32_16x16x32_bf16 v[192:195], v[56:59], v[136:139], v[192:195]\n" \
"v_cvt_pk_bf16_f32 v189, v190, v191\n" \
"v_cvt_pk_bf16_f32 v190, v197, 0\n" \
"v_mfma_f32_16x16x32_bf16 v[192:195], v[52:55], v[132:135], v[192:195]\n" \
"v_mov_b32_e32 v191, 0x3f80\n" \
"v_cvt_pk_bf16_f32 v184, v184, v185\n" \
"v_smfmac_f32_16x16x64_bf16 v[212:215], v[140:143], v[60:67], v50\n" \
"v_mfma_f32_16x16x32_bf16 v[204:207], v[124:127], v[60:63], v[204:207]\n" \
"v_cvt_pk_bf16_f32 v185, v186, v187\n" \
"v_cndmask_b32_e64 v186, 0, v196, s[78:79]\n" \
"v_mfma_f32_16x16x32_bf16 v[204:207], v[128:131], v[64:67], v[204:207]\n" \
"v_mov_b32_e64 v187, 0\n" \
"v_cvt_pk_bf16_f32 v186, v186, 0\n" \
"v_mfma_f32_16x16x32_bf16 v[200:203], v[120:123], v[64:67], v[200:203]\n" \
"v_mfma_f32_16x16x32_bf16 v[200:203], v[116:119], v[60:63], v[200:203]\n" \
"v_add_f32_e32 v212, v212, v214\n" \
"v_add_f32_e32 v213, v213, v215\n" \
"v_mfma_f32_16x16x32_bf16 v[208:211], v[60:63], v[132:135], v[208:211]\n" \
"v_mfma_f32_16x16x32_bf16 v[208:211], v[64:67], v[136:139], v[208:211]\n" \
"v_cvt_pk_bf16_f32 v204, v204, v205\n" \
"v_cvt_pk_bf16_f32 v205, v206, v207\n" \
"v_smfmac_f32_16x16x64_bf16 v[228:231], v[140:143], v[68:75], v50\n" \
"v_mfma_f32_16x16x32_bf16 v[220:223], v[128:131], v[72:75], v[220:223]\n" \
"v_cvt_pk_bf16_f32 v200, v200, v201\n" \
"v_cvt_pk_bf16_f32 v201, v202, v203\n" \
"v_mfma_f32_16x16x32_bf16 v[220:223], v[124:127], v[68:71], v[220:223]\n" \
"v_cndmask_b32_e64 v202, 0, v212, s[78:79]\n" \
"v_cvt_pk_bf16_f32 v206, v213, 0\n" \
"v_mfma_f32_16x16x32_bf16 v[216:219], v[116:119], v[68:71], v[216:219]\n" \
"v_mov_b32_e32 v207, 0x3f80\n" \
"v_add_f32_e64 v228, v228, v230\n" \
"v_mfma_f32_16x16x32_bf16 v[216:219], v[120:123], v[72:75], v[216:219]\n" \
"v_add_f32_e32 v229, v229, v231\n" \
"v_mov_b32_e32 v203, 0\n" \
"v_mfma_f32_16x16x32_bf16 v[224:227], v[72:75], v[136:139], v[224:227]\n" \
"v_cvt_pk_bf16_f32 v220, v220, v221\n" \
"v_cvt_pk_bf16_f32 v221, v222, v223\n" \
"v_mfma_f32_16x16x32_bf16 v[224:227], v[68:71], v[132:135], v[224:227]\n" \
"v_cvt_pk_bf16_f32 v202, v202, 0\n" \
"v_cvt_pk_bf16_f32 v222, v229, 0\n" \
"v_smfmac_f32_16x16x64_bf16 v[244:247], v[140:143], v[76:83], v50\n" \
"v_mfma_f32_16x16x32_bf16 v[236:239], v[124:127], v[76:79], v[236:239]\n" \
"v_cvt_pk_bf16_f32 v216, v216, v217\n" \
"v_cvt_pk_bf16_f32 v217, v218, v219\n" \
"v_mfma_f32_16x16x32_bf16 v[236:239], v[128:131], v[80:83], v[236:239]\n" \
"v_cndmask_b32_e64 v218, 0, v228, s[78:79]\n" \
"v_cvt_pk_bf16_f32 v126, v192, v193\n" \
"v_mfma_f32_16x16x32_bf16 v[232:235], v[120:123], v[80:83], v[232:235]\n" \
"v_cvt_pk_bf16_f32 v127, v194, v195\n" \
"v_add_f32_e64 v244, v244, v246\n" \
"v_mfma_f32_16x16x32_bf16 v[232:235], v[116:119], v[76:79], v[232:235]\n" \
"v_add_f32_e64 v245, v245, v247\n" \
"v_cvt_pk_bf16_f32 v128, v208, v209\n" \
"v_mfma_f32_16x16x32_bf16 v[240:243], v[76:79], v[132:135], v[240:243]\n" \
"v_cvt_pk_bf16_f32 v236, v236, v237\n" \
"v_cvt_pk_bf16_f32 v237, v238, v239\n" \
"v_mfma_f32_16x16x32_bf16 v[240:243], v[80:83], v[136:139], v[240:243]\n" \
"v_cvt_pk_bf16_f32 v129, v210, v211\n" \
"v_mov_b32_e32 v223, 0x3f80\n" \
"s_waitcnt lgkmcnt(0)\n" \
"s_barrier\n" \
"v_mfma_f32_16x16x32_bf16 a[128:131], v[152:155], v[56:59], a[128:131]\n" \
"v_cvt_pk_bf16_f32 v232, v232, v233\n" \
"v_cvt_pk_bf16_f32 v233, v234, v235\n" \
"v_mfma_f32_16x16x32_bf16 a[128:131], v[148:151], v[52:55], a[128:131]\n" \
"v_cndmask_b32_e64 v234, 0, v244, s[78:79]\n" \
"v_mov_b32_e64 v219, 0\n" \
"v_mfma_f32_16x16x32_bf16 a[132:135], v[156:159], v[52:55], a[132:135]\n" \
"v_cvt_pk_bf16_f32 v218, v218, 0\n" \
"v_cvt_pk_bf16_f32 v130, v224, v225\n" \
"v_mfma_f32_16x16x32_bf16 a[132:135], v[160:163], v[56:59], a[132:135]\n" \
"v_cvt_pk_bf16_f32 v131, v226, v227\n" \
"v_cvt_pk_bf16_f32 v238, v245, 0\n" \
"v_mfma_f32_16x16x32_bf16 a[136:139], v[168:171], v[56:59], a[136:139]\n" \
"v_mov_b32_e32 v239, 0x3f80\n" \
"v_mov_b32_e64 v235, 0\n" \
"v_mfma_f32_16x16x32_bf16 a[136:139], v[164:167], v[52:55], a[136:139]\n" \
"v_cvt_pk_bf16_f32 v234, v234, 0\n" \
"v_cvt_pk_bf16_f32 v132, v240, v241\n" \
"v_mfma_f32_16x16x32_bf16 a[140:143], v[172:175], v[52:55], a[140:143]\n" \
"v_cvt_pk_bf16_f32 v133, v242, v243\n" \
"v_cvt_pk_bf16_f32 v134, v248, v249\n" \
"v_mfma_f32_16x16x32_bf16 a[140:143], v[176:179], v[56:59], a[140:143]\n" \
"v_mfma_f32_16x16x32_bf16 v[116:119], v[180:183], v[184:187], 0\n" \
"v_mfma_f32_16x16x32_bf16 v[192:195], v[188:191], v[184:187], 0\n" \
"v_mfma_f32_16x16x32_bf16 v[208:211], v[204:207], v[200:203], 0\n" \
"v_mfma_f32_16x16x32_bf16 v[120:123], v[188:191], v[200:203], 0\n" \
"s_waitcnt vmcnt(0)\n" \
"v_cmp_ne_u32_e32 vcc, %[tag], v44\n" \
"v_mfma_f32_16x16x32_bf16 a[160:163], v[152:155], v[64:67], a[160:163]\n" \
"v_mfma_f32_16x16x32_bf16 v[224:227], v[220:223], v[216:219], 0\n" \
"v_mfma_f32_16x16x32_bf16 v[36:39], v[204:207], v[216:219], 0\n" \
"v_mfma_f32_16x16x32_bf16 v[240:243], v[236:239], v[232:235], 0\n" \
"v_mfma_f32_16x16x32_bf16 v[48:51], v[220:223], v[232:235], 0\n" \
"s_cbranch_vccnz L_pa_%=\n" \
"L_ra_%=:\n" \
"v_cndmask_b32_e64 v116, v116, v117, s[64:65]\n" \
"buffer_load_dwordx4 v20, s[80:83], s73 offen offset:0 sc1 lds\n" \
"v_mfma_f32_16x16x32_bf16 a[160:163], v[148:151], v[60:63], a[160:163]\n" \
"v_cndmask_b32_e64 v118, v118, v119, s[64:65]\n" \
"v_cndmask_b32_e64 v192, v192, v193, s[64:65]\n" \
"v_mfma_f32_16x16x32_bf16 a[164:167], v[156:159], v[60:63], a[164:167]\n" \
"v_add_f32_e32 v116, s49, v116\n" \
"v_cndmask_b32_e64 v194, v194, v195, s[64:65]\n" \
"v_mfma_f32_16x16x32_bf16 a[164:167], v[160:163], v[64:67], a[164:167]\n" \
"v_add_f32_e64 v118, s49, v118\n" \
"v_cndmask_b32_e64 v208, v208, v209, s[64:65]\n" \
"v_mfma_f32_16x16x32_bf16 a[168:171], v[168:171], v[64:67], a[168:171]\n" \
"buffer_load_dwordx4 v20, s[80:83], s73 offen offset:1024 sc1 lds\n" \
"v_mfma_f32_16x16x32_bf16 a[168:171], v[164:167], v[60:63], a[168:171]\n" \
"v_cndmask_b32_e64 v120, v120, v121, s[64:65]\n" \
"v_cndmask_b32_e64 v210, v210, v211, s[64:65]\n" \
"v_mfma_f32_16x16x32_bf16 a[172:175], v[172:175], v[60:63], a[172:175]\n" \
"v_cndmask_b32_e64 v122, v122, v123, s[64:65]\n" \
"v_cndmask_b32_e64 v224, v224, v225, s[64:65]\n" \
"v_mfma_f32_16x16x32_bf16 a[172:175], v[176:179], v[64:67], a[172:175]\n" \
"v_cndmask_b32_e64 v36, v36, v37, s[64:65]\n" \
"v_cndmask_b32_e64 v226, v226, v227, s[64:65]\n" \
"v_mfma_f32_16x16x32_bf16 a[192:195], v[152:155], v[72:75], a[192:195]\n" \
"buffer_load_dwordx4 v20, s[80:83], s73 offen offset:2048 sc1 lds\n" \
"v_mfma_f32_16x16x32_bf16 a[192:195], v[148:151], v[68:71], a[192:195]\n" \
"v_cndmask_b32_e64 v38, v38, v39, s[64:65]\n" \
"v_cndmask_b32_e64 v240, v240, v241, s[64:65]\n" \
"v_mfma_f32_16x16x32_bf16 a[196:199], v[156:159], v[68:71], a[196:199]\n" \
"v_cndmask_b32_e64 v48, v48, v49, s[64:65]\n" \
"v_cndmask_b32_e64 v242, v242, v243, s[64:65]\n" \
"v_mfma_f32_16x16x32_bf16 a[196:199], v[160:163], v[72:75], a[196:199]\n" \
"v_cndmask_b32_e64 v50, v50, v51, s[64:65]\n" \
"v_cndmask_b32_e64 v192, v116, v192, s[66:67]\n" \
"v_mfma_f32_16x16x32_bf16 a[200:203], v[168:171], v[72:75], a[200:203]\n" \
"buffer_load_dwordx4 v20, s[80:83], s73 offen offset:3072 sc1 lds\n" \
"v_mfma_f32_16x16x32_bf16 a[200:203], v[164:167], v[68:71], a[200:203]\n" \
"s_sub_u32 m0, %[mB], s53\n" \
"s_nop 0\n" \
"v_cndmask_b32_e64 v194, v118, v194, s[68:69]\n" \
"v_mfma_f32_16x16x32_bf16 a[204:207], v[172:175], v[68:71], a[204:207]\n" \
"v_cndmask_b32_e64 v208, v120, v208, s[66:67]\n" \
"v_cndmask_b32_e64 v210, v122, v210, s[68:69]\n" \
"v_mfma_f32_16x16x32_bf16 a[204:207], v[176:179], v[72:75], a[204:207]\n" \
"v_cndmask_b32_e64 v224, v36, v224, s[66:67]\n" \
"v_cndmask_b32_e64 v226, v38, v226, s[68:69]\n" \
"v_mfma_f32_16x16x32_bf16 a[224:227], v[152:155], v[80:83], a[224:227]\n" \
"buffer_load_dwordx4 v20, s[80:83], s74 offen offset:0 sc1 lds\n" \
"v_mfma_f32_16x16x32_bf16 a[224:227], v[148:151], v[76:79], a[224:227]\n" \
"v_cndmask_b32_e64 v240, v48, v240, s[66:67]\n" \
"v_cndmask_b32_e64 v242, v50, v242, s[68:69]\n" \
"v_mfma_f32_16x16x32_bf16 a[228:231], v[156:159], v[76:79], a[228:231]\n" \
"v_max_f32_e32 v193, v192, v194\n" \
"v_max_f32_e32 v209, v208, v210\n" \
"v_mfma_f32_16x16x32_bf16 a[228:231], v[160:163], v[80:83], a[228:231]\n" \
"v_max_f32_e32 v225, v224, v226\n" \
"v_max_f32_e32 v241, v240, v242\n" \
"v_mfma_f32_16x16x32_bf16 a[232:235], v[168:171], v[80:83], a[232:235]\n" \
"buffer_load_dwordx4 v20, s[80:83], s74 offen offset:1024 sc1 lds\n" \
"v_mfma_f32_16x16x32_bf16 a[232:235], v[164:167], v[76:79], a[232:235]\n" \
"v_permlane32_swap_b32_e32 v193, v225\n" \
"v_permlane32_swap_b32_e32 v209, v241\n" \
"v_mfma_f32_16x16x32_bf16 a[236:239], v[172:175], v[76:79], a[236:239]\n" \
"v_max_f32_e32 v193, v193, v225\n" \
"v_max_f32_e32 v209, v209, v241\n" \
"v_permlane32_swap_b32_e32 v196, v228\n" \
"v_permlane32_swap_b32_e32 v212, v244\n" \
"buffer_load_dwordx4 v20, s[80:83], s74 offen offset:2048 sc1 lds\n" \
"v_permlane16_swap_b32_e32 v193, v209\n" \
"v_permlane16_swap_b32_e32 v228, v244\n" \
"v_mfma_f32_16x16x32_bf16 a[236:239], v[176:179], v[80:83], a[236:239]\n" \
"v_max_f32_e32 v193, v193, v209\n" \
"v_mul_f32_e32 v193, s72, v193\n" \
"v_mfma_f32_16x16x32_bf16 a[144:147], v[152:155], v[88:91], a[144:147]\n" \
"v_mul_f32_e32 v45, 0xbfb8aa3b, v228\n" \
"v_mov_b32_e64 v209, v193\n" \
"v_mfma_f32_16x16x16_bf16 v[196:199], v[190:191], v[46:47], 0\n" \
"buffer_load_dwordx4 v20, s[80:83], s74 offen offset:3072 sc1 lds\n" \
"v_mfma_f32_16x16x32_bf16 a[144:147], v[148:151], v[84:87], a[144:147]\n" \
"s_sub_u32 m0, %[mB], 0x4000\n" \
"v_cvt_pk_bf16_f32 v135, v250, v251\n" \
"v_mfma_f32_16x16x16_bf16 v[212:215], v[206:207], v[46:47], 0\n" \
"v_mfma_f32_16x16x16_bf16 v[228:231], v[222:223], v[46:47], 0\n" \
"v_permlane16_swap_b32_e32 v193, v209\n" \
"v_mfma_f32_16x16x16_bf16 v[244:247], v[238:239], v[46:47], 0\n" \
"buffer_load_dwordx4 v20, s[80:83], s75 offen offset:0 sc1 lds\n" \
"v_mfma_f32_16x16x32_bf16 a[148:151], v[156:159], v[84:87], a[148:151]\n" \
"v_mov_b32_e32 v225, v193\n" \
"v_mov_b32_e64 v241, v209\n" \
"v_mfma_f32_16x16x32_bf16 a[148:151], v[160:163], v[88:91], a[148:151]\n" \
"v_permlane32_swap_b32_e32 v193, v225\n" \
"v_permlane32_swap_b32_e32 v209, v241\n" \
"v_mfma_f32_16x16x32_bf16 a[152:155], v[168:171], v[88:91], a[152:155]\n" \
"v_fma_f32 v192, v192, s72, -v193\n" \
"v_fma_f32 v194, v194, s72, -v193\n" \
"buffer_load_dwordx4 v20, s[80:83], s75 offen offset:1024 sc1 lds\n" \
"v_mfma_f32_16x16x32_bf16 a[152:155], v[164:167], v[84:87], a[152:155]\n" \
"v_fma_f32 v208, v208, s72, -v209\n" \
"v_fma_f32 v210, v210, s72, -v209\n" \
"v_exp_f32_e32 v192, v192\n" \
"v_exp_f32_e32 v194, v194\n" \
"v_exp_f32_e32 v208, v208\n" \
"v_exp_f32_e32 v210, v210\n" \
"v_mfma_f32_16x16x32_bf16 a[156:159], v[172:175], v[84:87], a[156:159]\n" \
"buffer_load_dwordx4 v20, s[80:83], s75 offen offset:2048 sc1 lds\n" \
"v_mfma_f32_16x16x32_bf16 a[156:159], v[176:179], v[88:91], a[156:159]\n" \
"v_fma_f32 v224, v224, s72, -v225\n" \
"v_fma_f32 v226, v226, s72, -v225\n" \
"v_exp_f32_e32 v224, v224\n" \
"v_exp_f32_e32 v226, v226\n" \
"v_mfma_f32_16x16x32_bf16 a[176:179], v[152:155], v[96:99], a[176:179]\n" \
"v_fma_f32 v240, v240, s72, -v241\n" \
"v_fma_f32 v242, v242, s72, -v241\n" \
"v_mfma_f32_16x16x32_bf16 a[176:179], v[148:151], v[92:95], a[176:179]\n" \
"buffer_load_dwordx4 v20, s[80:83], s75 offen offset:3072 sc1 lds\n" \
"v_mfma_f32_16x16x32_bf16 a[180:183], v[156:159], v[92:95], a[180:183]\n" \
"s_mov_b32 m0, s55\n" \
"v_cvt_pk_bf16_f32 v192, v192, v194\n" \
"v_mfma_f32_16x16x32_bf16 a[180:183], v[160:163], v[96:99], a[180:183]\n" \
"v_cvt_pk_bf16_f32 v208, v208, v210\n" \
"v_cvt_pk_bf16_f32 v136, v196, v197\n" \
"v_exp_f32_e32 v240, v240\n" \
"v_exp_f32_e64 v242, v242\n" \
"v_mfma_f32_16x16x32_bf16 a[184:187], v[168:171], v[96:99], a[184:187]\n" \
"buffer_load_dwordx4 v20, s[80:83], s76 offen offset:0 sc1 lds\n" \
"v_mfma_f32_16x16x32_bf16 a[184:187], v[164:167], v[92:95], a[184:187]\n" \
"v_cvt_pk_bf16_f32 v137, v198, v199\n" \
"v_cvt_pk_bf16_f32 v138, v212, v213\n" \
"v_mfma_f32_16x16x32_bf16 a[188:191], v[172:175], v[92:95], a[188:191]\n" \
"v_cvt_pk_bf16_f32 v139, v214, v215\n" \
"L_pf9:\n" \
"s_cbranch_execz L_pf13\n" \
"s_nop 0\n" \
"v_perm_b32 v184, v192, v192, v40\n" \
"v_mfma_f32_16x16x32_bf16 a[188:191], v[176:179], v[96:99], a[188:191]\n" \
"v_perm_b32 v185, v192, v192, v41\n" \
"v_perm_b32 v186, v192, v192, v42\n" \
"v_perm_b32 v187, v192, v192, v43\n" \
"buffer_load_dwordx4 v20, s[80:83], s76 offen offset:1024 sc1 lds\n" \
"v_perm_b32 v200, v208, v208, v40\n" \
"v_perm_b32 v201, v208, v208, v41\n" \
"v_perm_b32 v202, v208, v208, v42\n" \
"v_perm_b32 v203, v208, v208, v43\n" \
"v_cvt_pk_bf16_f32 v224, v224, v226\n" \
"v_cvt_pk_bf16_f32 v140, v228, v229\n" \
"v_cvt_pk_bf16_f32 v141, v230, v231\n" \
"v_mfma_f32_16x16x32_bf16 v[116:119], v[134:137], v[184:187], 0\n" \
"v_mfma_f32_16x16x32_bf16 v[120:123], v[136:139], v[200:203], 0\n" \
"v_perm_b32 v216, v224, v224, v40\n" \
"v_perm_b32 v217, v224, v224, v41\n" \
"v_perm_b32 v218, v224, v224, v42\n" \
"v_perm_b32 v219, v224, v224, v43\n" \
"v_cvt_pk_bf16_f32 v240, v240, v242\n" \
"v_cvt_pk_bf16_f32 v142, v244, v245\n" \
"v_cvt_pk_bf16_f32 v143, v246, v247\n" \
"v_mfma_f32_16x16x32_bf16 v[36:39], v[138:141], v[216:219], 0\n" \
"v_perm_b32 v232, v240, v240, v40\n" \
"v_perm_b32 v233, v240, v240, v41\n" \
"v_perm_b32 v234, v240, v240, v42\n" \
"v_perm_b32 v235, v240, v240, v43\n" \
"v_exp_f32_e64 v45, v45\n" \
"v_mov_b32_dpp v118, v122 quad_perm:[0,1,2,3] row_mask:0x2 bank_mask:0xf\n" \
"v_mfma_f32_16x16x32_bf16 v[48:51], v[140:143], v[232:235], 0\n" \
"v_cvt_pk_bf16_f32 v124, v252, v253\n" \
"v_cvt_pk_bf16_f32 v125, v254, v255\n" \
"v_mov_b32_dpp v118, v38 quad_perm:[0,1,2,3] row_mask:0x4 bank_mask:0xf\n" \
"v_add_f32_e64 v45, 1.0, v45\n" \
"v_mfma_f32_16x16x32_bf16 v[192:195], v[124:127], v[184:187], 0\n" \
"v_mfma_f32_16x16x32_bf16 v[224:227], v[128:131], v[216:219], 0\n" \
"v_rcp_f32_e64 v45, v45\n" \
"v_mov_b32_dpp v116, v120 quad_perm:[0,1,2,3] row_mask:0x2 bank_mask:0xf\n" \
"v_mov_b32_dpp v118, v50 quad_perm:[0,1,2,3] row_mask:0x8 bank_mask:0xf\n" \
"v_mov_b32_dpp v117, v121 quad_perm:[0,1,2,3] row_mask:0x2 bank_mask:0xf\n" \
"v_rcp_f32_e64 v118, v118\n" \
"v_mfma_f32_16x16x32_bf16 v[208:211], v[126:129], v[200:203], 0\n" \
"v_mfma_f32_16x16x32_bf16 v[240:243], v[130:133], v[232:235], 0\n" \
"v_mov_b32_dpp v116, v36 quad_perm:[0,1,2,3] row_mask:0x4 bank_mask:0xf\n" \
"v_mov_b32_dpp v117, v37 quad_perm:[0,1,2,3] row_mask:0x4 bank_mask:0xf\n" \
"v_mul_f32_e64 v118, v45, v118\n" \
"v_mov_b32_dpp v116, v48 quad_perm:[0,1,2,3] row_mask:0x8 bank_mask:0xf\n" \
"v_mov_b32_dpp v117, v49 quad_perm:[0,1,2,3] row_mask:0x8 bank_mask:0xf\n" \
"v_mul_f32_e32 v116, v118, v116\n" \
"v_mul_f32_e32 v117, v118, v117\n" \
"buffer_store_dwordx4 a[12:15], v20, s[80:83], s89 offen offset:1024 sc1\n" \
"s_add_u32 s89, s45, 0x0\n" \
"v_mov_b32_e32 v122, v118\n" \
"v_cvt_pk_bf16_f32 v116, v116, v117\n" \
"v_accvgpr_read_b32 v52, a128\n" \
"v_permlane16_swap_b32_e32 v118, v122\n" \
"v_mov_b32_e32 v38, v118\n" \
"v_mov_b32_e64 v50, v122\n" \
"v_accvgpr_read_b32 v53, a129\n" \
"v_permlane32_swap_b32_e32 v118, v38\n" \
"v_permlane32_swap_b32_e32 v122, v50\n" \
"v_mul_f32_e32 v192, v118, v192\n" \
"v_mul_f32_e32 v193, v118, v193\n" \
"v_mul_f32_e32 v224, v38, v224\n" \
"v_mul_f32_e32 v225, v38, v225\n" \
"v_mul_f32_e32 v194, v118, v194\n" \
"v_mul_f32_e32 v195, v118, v195\n" \
"v_cvt_pk_bf16_f32 v192, v192, v193\n" \
"v_mul_f32_e32 v226, v38, v226\n" \
"v_mul_f32_e32 v227, v38, v227\n" \
"v_cvt_pk_bf16_f32 v224, v224, v225\n" \
"v_cvt_pk_bf16_f32 v193, v194, v195\n" \
"v_mul_f32_e32 v208, v122, v208\n" \
"v_mul_f32_e32 v209, v122, v209\n" \
"v_mul_f32_e32 v210, v122, v210\n" \
"v_mul_f32_e32 v211, v122, v211\n" \
"v_cvt_pk_bf16_f32 v225, v226, v227\n" \
"v_mul_f32_e32 v240, v50, v240\n" \
"v_mul_f32_e32 v241, v50, v241\n" \
"v_mul_f32_e32 v242, v50, v242\n" \
"v_mul_f32_e32 v243, v50, v243\n" \
"v_cvt_pk_bf16_f32 v194, v208, v209\n" \
"v_cvt_pk_bf16_f32 v195, v210, v211\n" \
"v_cvt_pk_bf16_f32 v226, v240, v241\n" \
"v_cvt_pk_bf16_f32 v227, v242, v243\n" \
"v_permlane32_swap_b32_e32 v192, v194\n" \
"v_permlane32_swap_b32_e32 v193, v195\n" \
"v_permlane32_swap_b32_e32 v224, v226\n" \
"v_permlane32_swap_b32_e32 v225, v227\n" \
"v_accvgpr_read_b32 v54, a130\n" \
"v_accvgpr_read_b32 v55, a131\n" \
"v_pk_mul_f32 v[52:53], v[52:53], s[70:71]\n" \
"v_accvgpr_read_b32 v56, a132\n" \
"v_accvgpr_read_b32 v57, a133\n" \
"v_accvgpr_read_b32 v58, a134\n" \
"v_accvgpr_read_b32 v59, a135\n" \
"v_pk_mul_f32 v[54:55], v[54:55], s[70:71]\n" \
"v_cvt_pk_bf16_f32 v52, v52, v53\n" \
"v_pk_mul_f32 v[56:57], v[56:57], s[70:71]\n" \
"v_pk_mul_f32 v[58:59], v[58:59], s[70:71]\n" \
"v_cvt_pk_bf16_f32 v53, v54, v55\n" \
"v_cvt_pk_bf16_f32 v54, v56, v57\n" \
"v_cvt_pk_bf16_f32 v55, v58, v59\n" \
"ds_write_b128 v27, v[52:55] offset:0\n" \
"buffer_store_dwordx4 a[40:43], v20, s[80:83], s88 offen offset:0 sc1\n" \
"s_add_u32 s88, s45, 0x0\n" \
"s_nop 0\n" \
"buffer_store_dword v116, v25, s[80:83], s51 offen sc1\n" \
"buffer_store_dwordx4 v[192:195], v24, s[80:83], s85 offen sc1\n" \
"buffer_store_dwordx4 v[224:227], v24, s[80:83], s87 offen sc1\n" \
"v_accvgpr_read_b32 v76, a224\n" \
"v_accvgpr_read_b32 v77, a225\n" \
"v_accvgpr_read_b32 v78, a226\n" \
"v_accvgpr_read_b32 v79, a227\n" \
"v_pk_mul_f32 v[76:77], v[76:77], s[70:71]\n" \
"v_accvgpr_read_b32 v52, a136\n" \
"v_accvgpr_read_b32 v53, a137\n" \
"v_accvgpr_read_b32 v80, a228\n" \
"v_accvgpr_read_b32 v81, a229\n" \
"v_accvgpr_read_b32 v82, a230\n" \
"v_accvgpr_read_b32 v83, a231\n" \
"v_pk_mul_f32 v[78:79], v[78:79], s[70:71]\n" \
"v_cvt_pk_bf16_f32 v76, v76, v77\n" \
"v_accvgpr_read_b32 v54, a138\n" \
"v_accvgpr_read_b32 v55, a139\n" \
"v_pk_mul_f32 v[52:53], v[52:53], s[70:71]\n" \
"v_pk_mul_f32 v[80:81], v[80:81], s[70:71]\n" \
"v_pk_mul_f32 v[82:83], v[82:83], s[70:71]\n" \
"v_cvt_pk_bf16_f32 v77, v78, v79\n" \
"v_accvgpr_read_b32 v56, a140\n" \
"v_accvgpr_read_b32 v57, a141\n" \
"v_accvgpr_read_b32 v58, a142\n" \
"v_accvgpr_read_b32 v59, a143\n" \
"v_pk_mul_f32 v[54:55], v[54:55], s[70:71]\n" \
"v_cvt_pk_bf16_f32 v52, v52, v53\n" \
"v_cvt_pk_bf16_f32 v78, v80, v81\n" \
"v_cvt_pk_bf16_f32 v79, v82, v83\n" \
"ds_write_b128 v27, v[76:79] offset:12288\n" \
"v_pk_mul_f32 v[56:57], v[56:57], s[70:71]\n" \
"v_pk_mul_f32 v[58:59], v[58:59], s[70:71]\n" \
"v_cvt_pk_bf16_f32 v53, v54, v55\n" \
"v_cvt_pk_bf16_f32 v54, v56, v57\n" \
"v_cvt_pk_bf16_f32 v55, v58, v59\n" \
"ds_write_b128 v27, v[52:55] offset:1024\n" \
"v_accvgpr_read_b32 v76, a208\n" \
"v_accvgpr_read_b32 v77, a209\n" \
"v_accvgpr_read_b32 v78, a210\n" \
"v_accvgpr_read_b32 v79, a211\n" \
"v_accvgpr_read_b32 v52, a168\n" \
"v_accvgpr_read_b32 v53, a169\n" \
"v_mfma_f32_16x16x32_bf16 v[76:79], v[152:155], v[104:107], v[76:79]\n" \
"v_accvgpr_read_b32 v54, a170\n" \
"v_accvgpr_read_b32 v55, a171\n" \
"v_mfma_f32_16x16x32_bf16 v[76:79], v[148:151], v[100:103], v[76:79]\n" \
"v_accvgpr_read_b32 v56, a172\n" \
"v_accvgpr_read_b32 v57, a173\n" \
"v_accvgpr_read_b32 v58, a174\n" \
"v_accvgpr_read_b32 v59, a175\n" \
"v_pk_mul_f32 v[52:53], v[52:53], s[70:71]\n" \
"v_pk_mul_f32 v[54:55], v[54:55], s[70:71]\n" \
"v_cvt_pk_bf16_f32 v52, v52, v53\n" \
"v_pk_mul_f32 v[56:57], v[56:57], s[70:71]\n" \
"v_pk_mul_f32 v[58:59], v[58:59], s[70:71]\n" \
"buffer_store_dwordx4 a[44:47], v20, s[80:83], s89 offen offset:1024 sc1\n" \
"s_add_u32 s89, s45, 0x0\n" \
"s_nop 0\n" \
"v_cvt_pk_bf16_f32 v53, v54, v55\n" \
"v_cvt_pk_bf16_f32 v54, v56, v57\n" \
"v_cvt_pk_bf16_f32 v55, v58, v59\n" \
"ds_write_b128 v27, v[52:55] offset:5120\n" \
"v_accvgpr_read_b32 v80, a212\n" \
"v_accvgpr_read_b32 v81, a213\n" \
"v_accvgpr_read_b32 v82, a214\n" \
"v_accvgpr_read_b32 v83, a215\n" \
"v_accvgpr_read_b32 v52, a232\n" \
"v_accvgpr_read_b32 v53, a233\n" \
"v_mfma_f32_16x16x32_bf16 v[80:83], v[156:159], v[100:103], v[80:83]\n" \
"v_accvgpr_read_b32 v54, a234\n" \
"v_accvgpr_read_b32 v55, a235\n" \
"v_mfma_f32_16x16x32_bf16 v[80:83], v[160:163], v[104:107], v[80:83]\n" \
"v_accvgpr_read_b32 v56, a236\n" \
"v_accvgpr_read_b32 v57, a237\n" \
"v_accvgpr_read_b32 v58, a238\n" \
"v_accvgpr_read_b32 v59, a239\n" \
"v_pk_mul_f32 v[52:53], v[52:53], s[70:71]\n" \
"v_pk_mul_f32 v[54:55], v[54:55], s[70:71]\n" \
"v_cvt_pk_bf16_f32 v52, v52, v53\n" \
"v_pk_mul_f32 v[56:57], v[56:57], s[70:71]\n" \
"v_pk_mul_f32 v[58:59], v[58:59], s[70:71]\n" \
"v_cvt_pk_bf16_f32 v53, v54, v55\n" \
"v_cvt_pk_bf16_f32 v54, v56, v57\n" \
"v_cvt_pk_bf16_f32 v55, v58, v59\n" \
"ds_write_b128 v27, v[52:55] offset:13312\n" \
"v_accvgpr_read_b32 v52, a216\n" \
"v_accvgpr_read_b32 v53, a217\n" \
"v_accvgpr_read_b32 v54, a218\n" \
"v_accvgpr_read_b32 v55, a219\n" \
"v_accvgpr_read_b32 v60, a160\n" \
"v_accvgpr_read_b32 v61, a161\n" \
"v_mfma_f32_16x16x32_bf16 v[52:55], v[168:171], v[104:107], v[52:55]\n" \
"v_accvgpr_read_b32 v62, a162\n" \
"v_accvgpr_read_b32 v63, a163\n" \
"v_mfma_f32_16x16x32_bf16 v[52:55], v[164:167], v[100:103], v[52:55]\n" \
"v_accvgpr_read_b32 v56, a220\n" \
"v_accvgpr_read_b32 v57, a221\n" \
"v_accvgpr_read_b32 v58, a222\n" \
"v_accvgpr_read_b32 v59, a223\n" \
"v_pk_mul_f32 v[60:61], v[60:61], s[70:71]\n" \
"v_accvgpr_read_b32 v64, a164\n" \
"v_mfma_f32_16x16x32_bf16 v[56:59], v[172:175], v[100:103], v[56:59]\n" \
"v_accvgpr_read_b32 v65, a165\n" \
"v_accvgpr_read_b32 v66, a166\n" \
"v_mfma_f32_16x16x32_bf16 v[56:59], v[176:179], v[104:107], v[56:59]\n" \
"v_accvgpr_read_b32 v67, a167\n" \
"buffer_store_dwordx4 a[72:75], v20, s[80:83], s88 offen offset:2048 sc1\n" \
"s_add_u32 s88, s45, 0x1000\n" \
"v_pk_mul_f32 v[62:63], v[62:63], s[70:71]\n" \
"v_cvt_pk_bf16_f32 v60, v60, v61\n" \
"v_pk_mul_f32 v[64:65], v[64:65], s[70:71]\n" \
"v_pk_mul_f32 v[66:67], v[66:67], s[70:71]\n" \
"v_cvt_pk_bf16_f32 v61, v62, v63\n" \
"v_cvt_pk_bf16_f32 v62, v64, v65\n" \
"v_cvt_pk_bf16_f32 v63, v66, v67\n" \
"ds_write_b128 v27, v[60:63] offset:4096\n" \
"v_accvgpr_read_b32 v68, a192\n" \
"v_accvgpr_read_b32 v69, a193\n" \
"v_accvgpr_read_b32 v60, a200\n" \
"v_accvgpr_read_b32 v61, a201\n" \
"v_accvgpr_read_b32 v70, a194\n" \
"v_accvgpr_read_b32 v71, a195\n" \
"v_pk_mul_f32 v[68:69], v[68:69], s[70:71]\n" \
"v_accvgpr_read_b32 v62, a202\n" \
"v_accvgpr_read_b32 v63, a203\n" \
"v_pk_mul_f32 v[60:61], v[60:61], s[70:71]\n" \
"v_accvgpr_read_b32 v72, a196\n" \
"v_accvgpr_read_b32 v73, a197\n" \
"v_accvgpr_read_b32 v74, a198\n" \
"v_accvgpr_read_b32 v75, a199\n" \
"v_pk_mul_f32 v[70:71], v[70:71], s[70:71]\n" \
"v_cvt_pk_bf16_f32 v68, v68, v69\n" \
"v_accvgpr_read_b32 v64, a204\n" \
"v_accvgpr_read_b32 v65, a205\n" \
"v_accvgpr_read_b32 v66, a206\n" \
"v_accvgpr_read_b32 v67, a207\n" \
"v_pk_mul_f32 v[62:63], v[62:63], s[70:71]\n" \
"v_cvt_pk_bf16_f32 v60, v60, v61\n" \
"v_accvgpr_read_b32 v100, a240\n" \
"v_accvgpr_read_b32 v101, a241\n" \
"v_accvgpr_read_b32 v102, a242\n" \
"v_accvgpr_read_b32 v103, a243\n" \
"v_pk_mul_f32 v[72:73], v[72:73], s[70:71]\n" \
"v_pk_mul_f32 v[74:75], v[74:75], s[70:71]\n" \
"v_mfma_f32_16x16x32_bf16 v[100:103], v[152:155], v[112:115], v[100:103]\n" \
"v_cvt_pk_bf16_f32 v69, v70, v71\n" \
"v_cvt_pk_bf16_f32 v61, v62, v63\n" \
"v_mfma_f32_16x16x32_bf16 v[100:103], v[148:151], v[108:111], v[100:103]\n" \
"v_cvt_pk_bf16_f32 v70, v72, v73\n" \
"v_cvt_pk_bf16_f32 v71, v74, v75\n" \
"ds_write_b128 v27, v[68:71] offset:8192\n" \
"v_pk_mul_f32 v[64:65], v[64:65], s[70:71]\n" \
"v_pk_mul_f32 v[66:67], v[66:67], s[70:71]\n" \
"v_cvt_pk_bf16_f32 v62, v64, v65\n" \
"v_cvt_pk_bf16_f32 v63, v66, v67\n" \
"ds_write_b128 v27, v[60:63] offset:9216\n" \
"v_accvgpr_read_b32 v84, a144\n" \
"buffer_store_dwordx4 a[76:79], v20, s[80:83], s89 offen offset:3072 sc1\n" \
"s_add_u32 s89, s45, 0x1000\n" \
"v_accvgpr_read_b32 v85, a145\n" \
"v_accvgpr_read_b32 v68, a152\n" \
"v_accvgpr_read_b32 v69, a153\n" \
"v_accvgpr_read_b32 v92, a176\n" \
"v_accvgpr_read_b32 v93, a177\n" \
"v_accvgpr_read_b32 v60, a184\n" \
"v_accvgpr_read_b32 v61, a185\n" \
"v_accvgpr_read_b32 v86, a146\n" \
"v_accvgpr_read_b32 v87, a147\n" \
"v_pk_mul_f32 v[84:85], v[84:85], s[70:71]\n" \
"v_accvgpr_read_b32 v70, a154\n" \
"v_accvgpr_read_b32 v71, a155\n" \
"v_pk_mul_f32 v[68:69], v[68:69], s[70:71]\n" \
"v_accvgpr_read_b32 v94, a178\n" \
"v_accvgpr_read_b32 v95, a179\n" \
"v_pk_mul_f32 v[92:93], v[92:93], s[70:71]\n" \
"v_accvgpr_read_b32 v62, a186\n" \
"v_accvgpr_read_b32 v63, a187\n" \
"v_pk_mul_f32 v[60:61], v[60:61], s[70:71]\n" \
"v_pk_mul_f32 v[76:77], v[76:77], s[70:71]\n" \
"v_pk_mul_f32 v[52:53], v[52:53], s[70:71]\n" \
"v_pk_mul_f32 v[100:101], v[100:101], s[70:71]\n" \
"v_accvgpr_read_b32 v88, a148\n" \
"v_accvgpr_read_b32 v89, a149\n" \
"v_accvgpr_read_b32 v90, a150\n" \
"v_accvgpr_read_b32 v91, a151\n" \
"v_pk_mul_f32 v[86:87], v[86:87], s[70:71]\n" \
"v_cvt_pk_bf16_f32 v84, v84, v85\n" \
"v_accvgpr_read_b32 v72, a156\n" \
"v_accvgpr_read_b32 v73, a157\n" \
"v_accvgpr_read_b32 v74, a158\n" \
"v_accvgpr_read_b32 v75, a159\n" \
"v_pk_mul_f32 v[70:71], v[70:71], s[70:71]\n" \
"v_cvt_pk_bf16_f32 v68, v68, v69\n" \
"v_accvgpr_read_b32 v96, a180\n" \
"v_accvgpr_read_b32 v97, a181\n" \
"v_accvgpr_read_b32 v98, a182\n" \
"v_accvgpr_read_b32 v99, a183\n" \
"v_pk_mul_f32 v[94:95], v[94:95], s[70:71]\n" \
"v_cvt_pk_bf16_f32 v92, v92, v93\n" \
"v_accvgpr_read_b32 v64, a188\n" \
"v_accvgpr_read_b32 v65, a189\n" \
"v_accvgpr_read_b32 v66, a190\n" \
"v_accvgpr_read_b32 v67, a191\n" \
"v_pk_mul_f32 v[62:63], v[62:63], s[70:71]\n" \
"v_cvt_pk_bf16_f32 v60, v60, v61\n" \
"v_pk_mul_f32 v[78:79], v[78:79], s[70:71]\n" \
"v_cvt_pk_bf16_f32 v76, v76, v77\n" \
"v_pk_mul_f32 v[54:55], v[54:55], s[70:71]\n" \
"v_cvt_pk_bf16_f32 v52, v52, v53\n" \
"v_pk_mul_f32 v[102:103], v[102:103], s[70:71]\n" \
"v_cvt_pk_bf16_f32 v100, v100, v101\n" \
"v_pk_mul_f32 v[88:89], v[88:89], s[70:71]\n" \
"v_pk_mul_f32 v[90:91], v[90:91], s[70:71]\n" \
"v_cvt_pk_bf16_f32 v85, v86, v87\n" \
"v_pk_mul_f32 v[72:73], v[72:73], s[70:71]\n" \
"v_pk_mul_f32 v[74:75], v[74:75], s[70:71]\n" \
"v_cvt_pk_bf16_f32 v69, v70, v71\n" \
"v_pk_mul_f32 v[96:97], v[96:97], s[70:71]\n" \
"v_pk_mul_f32 v[98:99], v[98:99], s[70:71]\n" \
"buffer_store_dwordx4 a[104:107], v20, s[80:83], s88 offen offset:0 sc1\n" \
"v_cvt_pk_bf16_f32 v93, v94, v95\n" \
"v_pk_mul_f32 v[64:65], v[64:65], s[70:71]\n" \
"v_pk_mul_f32 v[66:67], v[66:67], s[70:71]\n" \
"v_cvt_pk_bf16_f32 v61, v62, v63\n" \
"v_pk_mul_f32 v[80:81], v[80:81], s[70:71]\n" \
"v_pk_mul_f32 v[82:83], v[82:83], s[70:71]\n" \
"v_cvt_pk_bf16_f32 v77, v78, v79\n" \
"v_pk_mul_f32 v[56:57], v[56:57], s[70:71]\n" \
"v_pk_mul_f32 v[58:59], v[58:59], s[70:71]\n" \
"v_cvt_pk_bf16_f32 v53, v54, v55\n" \
"v_accvgpr_read_b32 v104, a244\n" \
"v_accvgpr_read_b32 v105, a245\n" \
"v_accvgpr_read_b32 v106, a246\n" \
"v_accvgpr_read_b32 v107, a247\n" \
"v_cvt_pk_bf16_f32 v101, v102, v103\n" \
"v_cvt_pk_bf16_f32 v86, v88, v89\n" \
"v_mfma_f32_16x16x32_bf16 v[104:107], v[156:159], v[108:111], v[104:107]\n" \
"v_cvt_pk_bf16_f32 v87, v90, v91\n" \
"v_cvt_pk_bf16_f32 v70, v72, v73\n" \
"ds_write_b128 v21, v[84:87] offset:16384\n" \
"v_mfma_f32_16x16x32_bf16 v[104:107], v[160:163], v[112:115], v[104:107]\n" \
"v_cvt_pk_bf16_f32 v71, v74, v75\n" \
"ds_write_b128 v21, v[68:71] offset:17408\n" \
"v_cvt_pk_bf16_f32 v94, v96, v97\n" \
"v_cvt_pk_bf16_f32 v95, v98, v99\n" \
"ds_write_b128 v21, v[92:95] offset:20480\n" \
"v_cvt_pk_bf16_f32 v62, v64, v65\n" \
"v_cvt_pk_bf16_f32 v63, v66, v67\n" \
"ds_write_b128 v21, v[60:63] offset:21504\n" \
"v_pk_mul_f32 v[104:105], v[104:105], s[70:71]\n" \
"v_pk_mul_f32 v[106:107], v[106:107], s[70:71]\n" \
"v_cvt_pk_bf16_f32 v78, v80, v81\n" \
"v_cvt_pk_bf16_f32 v79, v82, v83\n" \
"ds_write_b128 v21, v[76:79] offset:24576\n" \
"v_cvt_pk_bf16_f32 v54, v56, v57\n" \
"v_cvt_pk_bf16_f32 v55, v58, v59\n" \
"ds_write_b128 v21, v[52:55] offset:25600\n" \
"v_cvt_pk_bf16_f32 v102, v104, v105\n" \
"v_cvt_pk_bf16_f32 v103, v106, v107\n" \
"ds_write_b128 v21, v[100:103] offset:28672\n" \
"buffer_store_dwordx4 a[108:111], v20, s[80:83], s89 offen offset:1024 sc1\n" \
"v_accvgpr_read_b32 v84, a248\n" \
"v_accvgpr_read_b32 v85, a249\n" \
"v_accvgpr_read_b32 v86, a250\n" \
"v_accvgpr_read_b32 v87, a251\n" \
"v_accvgpr_read_b32 v88, a252\n" \
"v_accvgpr_read_b32 v89, a253\n" \
"v_mfma_f32_16x16x32_bf16 v[84:87], v[168:171], v[112:115], v[84:87]\n" \
"v_accvgpr_read_b32 v90, a254\n" \
"v_accvgpr_read_b32 v91, a255\n" \
"v_mfma_f32_16x16x32_bf16 v[84:87], v[164:167], v[108:111], v[84:87]\n" \
"v_sub_f32_e32 v44, 1.0, v45\n" \
"v_mfma_f32_16x16x32_bf16 v[88:91], v[172:175], v[108:111], v[88:91]\n" \
"ds_write_b32 v26, v44\n" \
"v_mfma_f32_16x16x32_bf16 v[88:91], v[176:179], v[112:115], v[88:91]\n" \
"s_nop 3\n" \
"v_pk_mul_f32 v[84:85], v[84:85], s[70:71]\n" \
"v_pk_mul_f32 v[86:87], v[86:87], s[70:71]\n" \
"v_cvt_pk_bf16_f32 v84, v84, v85\n" \
"v_cvt_pk_bf16_f32 v85, v86, v87\n" \
"v_pk_mul_f32 v[88:89], v[88:89], s[70:71]\n" \
"v_pk_mul_f32 v[90:91], v[90:91], s[70:71]\n" \
"v_cvt_pk_bf16_f32 v86, v88, v89\n" \
"v_cvt_pk_bf16_f32 v87, v90, v91\n" \
"ds_write_b128 v21, v[84:87] offset:29696\n" \
"L_end_%=:\n"
#define PROJ_VCLOB "v21","v24","v25","v33","v34","v35","v36","v37","v38","v39","v40","v41","v42","v43","v44","v45","v46","v47","v48","v49","v50","v51","v52","v53","v54","v55","v56","v57","v58","v59","v60","v61","v62","v63","v64","v65","v66","v67","v68","v69","v70","v71","v72","v73","v74","v75","v76","v77","v78","v79","v80","v81","v82","v83","v84","v85","v86","v87","v88","v89","v90","v91","v92","v93","v94","v95","v96","v97","v98","v99","v100","v101","v102","v103","v104","v105","v106","v107","v108","v109","v110","v111","v112","v113","v114","v115","v116","v117","v118","v119","v120","v121","v122","v123","v124","v125","v126","v127","v128","v129","v130","v131","v132","v133","v134","v135","v136","v137","v138","v139","v140","v141","v142","v143","v144","v145","v146","v147","v148","v149","v150","v151","v152","v153","v154","v155","v156","v157","v158","v159","v160","v161","v162","v163","v164","v165","v166","v167","v168","v169","v170","v171","v172","v173","v174","v175","v176","v177","v178","v179","v180","v181","v182","v183","v184","v185","v186","v187","v188","v189","v190","v191","v192","v193","v194","v195","v196","v197","v198","v199","v200","v201","v202","v203","v204","v205","v206","v207","v208","v209","v210","v211","v212","v213","v214","v215","v216","v217","v218","v219","v220","v221","v222","v223","v224","v225","v226","v227","v228","v229","v230","v231","v232","v233","v234","v235","v236","v237","v238","v239","v240","v241","v242","v243","v244","v245","v246","v247","v248","v249","v250","v251","v252","v253","v254","v255"
#define PROJ_SCLOB "s37","s38","s39","s40","s41","s42","s43","s44","s45","s46","s47","s48","s49","s50","s51","s52","s53","s54","s55","s62","s63","s76","s77","s78","s79","s80","s81","s82","s83","s84","s85","s86","s87","s88","s89","s94","s95","s96","s97","s98","s99","s100","s101"

#define PLO(p) ((unsigned)(size_t)(p))
#define PHI(p) ((unsigned)((size_t)(p) >> 32))
__device__ __forceinline__ unsigned proj_phase(const u16* __restrict__ x, const u16* __restrict__ ckv, const u16* __restrict__ wq, const u16* __restrict__ wuk,
                                                       const u16* __restrict__ wuv, const u16* __restrict__ wqkv, const u16* __restrict__ gw, const u16* wo, const u16* wob, const u16* out,
                                                       char* __restrict__ ws, char* lds, const unsigned wid, unsigned& tag, u32x4 (&vp)[5], u32x4 (&sa)[5], unsigned (&sd)[2]) {
    typedef unsigned u32x2 __attribute__((ext_vector_type(2)));
    unsigned* const flags = (unsigned*)(ws + WS_F);
    const int b = blockIdx.x, h = (b & 7) * 4 + ((b >> 3) & 3), mt = b >> 5;
    const unsigned L0 = (unsigned)(size_t)lds, rb = (wid < 2u ? 15u - mt : mt) * 128u;
    RTS(0);
#define PST() do {} while (0)
    const unsigned lane = threadIdx.x & 63u, wr = wid >> 1, wc = wid & 1u;
    PST();
    unsigned vAc[2], vB[2];
    const unsigned sp = b & 7, nt = (b >> 3) & 3, rot = (PROJ_ROT) & 7u;
#pragma unroll
    for (int par = 0; par < 2; ++par) {
        const unsigned lr = lane >> 3, cc = (lane & 7u) ^ (4u * par + (lr >> 1)), r0 = rb + wc * 64u + lr;
        vAc[par] = r0 * (DC * 2) + cc * 16u;
        const unsigned br = lane >> 4, p16 = lane & 15u, sc = (((p16 >> 1) ^ (br | (par << 2))) << 1) | (p16 & 1u);
        vB[par] = (16u * wid + br) * (NBIG * 2) + h * (HD * 2) + sc * 16u;
    }
#define SRD(p, n) (u32x4){PLO(p), PHI(p) & 0xffffu, n, 0x00020000u}
    u32x4 sC = SRD(ckv, S * DC * 2), sK = SRD(wuk, DC * NBIG * 2), sV = SRD(wuv, DC * NBIG * 2);
#define PRE(n) do { SBAR(); asm volatile(PROJ_PRE##n :: "{v22}"(vAc[0]), "{v23}"(vAc[1]), "{v26}"(vB[0]), "{v27}"(vB[1]), "{s[64:67]}"(sC), "{s[68:71]}"(sK), "{s[72:75]}"(sV), \
                 [mA] "s"(L0 + wid * 8192u), [mB] "s"(L0 + 65536u + wid * 4096u), [rc] "s"(rot * 128u) : "memory", "m0", "scc", "s62", "s63", "s84", "s85"); SBAR(); } while (0)
    PRE(0);
    const unsigned fr = lane & 15u, fg = lane >> 4, r4 = (lane >> 2) & 3u, sB = r4 | ((fg & 1u) << 2);
    const unsigned aA0 = L0 + (wr * 64u + fr) * 128u + ((fg ^ (fr >> 1)) << 4);
    unsigned vl = lane * 16u, r32 = L0 + 65536u + (8u * fg + r4) * 256u + (lane & 3u) * 8u + ((sB ^ (4u * wc)) << 5);
    u32x2 r28 = {aA0, aA0 ^ 64u};
    asm volatile("" : "+v"(vl), "+v"(r28), "+v"(r32));
    PST();
    PRE(1);
    PST();
    PRE(2);
    PST();
    asm volatile("s_mov_b64 exec, 0\n s_cmp_lt_u32 %0, 2\n s_cbranch_scc1 L_chA\n s_cmp_eq_u32 %0, 2\n s_cbranch_scc1 L_pf2\n s_branch L_pf3\n"
                 "L_chA: s_cmp_eq_u32 %0, 0\n s_cbranch_scc1 L_pf0\n s_branch L_pf1\n L_retA: s_mov_b64 exec, -1" :: "s"(wid) : "scc", "memory");
    PRE(3);
    PST();
    sa[0] = (u32x4){PLO(x), PHI(x), PLO(wq), PHI(wq)}; sa[1] = (u32x4){PLO(wqkv), PHI(wqkv), PLO(gw), PHI(gw)}; sa[2] = (u32x4){PLO(wo), PHI(wo), PLO(wob), PHI(wob)}; sa[3] = (u32x4){PLO(out), PHI(out), rot * 128u, 0u};
    asm volatile(PROJ_ASM
                 : [tag] "=&s"(tag), "+{s[8:11]}"(sa[0]), "+{s[12:15]}"(sa[1]), "+{s[16:19]}"(sa[2]), "+{s[20:23]}"(sa[3]), "=&{s[56:59]}"(sa[4]), "=&{s60}"(sd[0]), "=&{s61}"(sd[1]),
                   "+{s[64:67]}"(sC), "+{s[68:71]}"(sK), "+{s[72:75]}"(sV), "+{v22}"(vAc[0]), "+{v23}"(vAc[1]), "+{v26}"(vB[0]), "+{v27}"(vB[1]), "+{v[28:29]}"(r28), "+{v32}"(r32),
                   "={v[0:3]}"(vp[0]), "={v[4:7]}"(vp[1]), "={v[8:11]}"(vp[2]), "={v[12:15]}"(vp[3]), "={v[16:19]}"(vp[4])
                 : "{v20}"(vl), [wsl] "s"(PLO(ws)), [wsh] "s"(PHI(ws)), [b] "s"(b), [w] "s"(wid), [mA] "s"(L0 + wid * 8192u), [mB] "s"(L0 + 65536u + wid * 4096u), [mS] "s"(L0 + 151552u + wid * 512u)
                 : "memory", "m0", "vcc", "scc", PROJ_VCLOB, PROJ_SCLOB, ACLOB_LO, ACLOB_HI);
    RTS(1);
    return vl;
}

namespace att {
#define DI __device__ __forceinline__
constexpr int LDS_BYTES = 163840;
constexpr unsigned LDS_G = 159744u, LDS_L = 161792u;
typedef __bf16 bfx8 __attribute__((ext_vector_type(8)));
DI unsigned rfl(unsigned x) { return __builtin_amdgcn_readfirstlane(x); }
DI f32x4 mfma(u32x4 a, u32x4 b, f32x4 c) { return __builtin_amdgcn_mfma_f32_16x16x32_bf16(__builtin_bit_cast(bfx8, a), __builtin_bit_cast(bfx8, b), c, 0, 0, 0); }
typedef __bf16 bfx2 __attribute__((ext_vector_type(2)));
typedef float f32x2 __attribute__((ext_vector_type(2)));
DI unsigned cvtpk2(float lo, float hi) { f32x2 v = {lo, hi}; bfx2 b = __builtin_convertvector(v, bfx2); return __builtin_bit_cast(unsigned, b); }
DI void att_exact(const u16* __restrict__ x, const u16* __restrict__ wq, char* __restrict__ ws, unsigned h, unsigned r0, float g1) {
    const unsigned lane = lane_id(), g = lane >> 4, c = lane & 15u;
    f32x4 o[8];
#pragma unroll
    for (int n = 0; n < 8; ++n) o[n] = (f32x4){0.f, 0.f, 0.f, 0.f};
#pragma unroll 1
    for (unsigned kk = 0; kk < (unsigned)DM; kk += 32u) {
        const unsigned k = kk + 8u * g;
        const u32x4 xf = *reinterpret_cast<const u32x4*>(x + (r0 + c) * DM + k);
#pragma unroll
        for (int n = 0; n < 8; ++n) {
            const u16* w = wq + k * NBIG + h * HD + n * 16 + c; u32x4 wf;
#pragma unroll
            for (int e = 0; e < 4; ++e) wf[e] = (unsigned)w[2 * e * NBIG] | ((unsigned)w[(2 * e + 1) * NBIG] << 16);
            o[n] = mfma(wf, xf, o[n]);
        }
    }
    u32x4 q[4];
#pragma unroll
    for (int p = 0; p < 4; ++p) {
        const f32x4 a = o[2 * p] * 0.12751743082f, b = o[2 * p + 1] * 0.12751743082f;
        q[p] = (u32x4){cvtpk2(a[0], a[1]), cvtpk2(a[2], a[3]), cvtpk2(b[0], b[1]), cvtpk2(b[2], b[3])};
    }
#pragma unroll
    for (int n = 0; n < 8; ++n) o[n] = (f32x4){0.f, 0.f, 0.f, 0.f};
    const __amdgpu_buffer_rsrc_t srd = __builtin_amdgcn_make_buffer_rsrc((void*)(ws + WS_K + (size_t)h * 1048576), (short)0, 1048576, 0x00020000);
    float m = -3.0e38f, l = 0.f;
#pragma unroll 1
    for (unsigned k0 = 0; k0 <= r0 + 15u; k0 += 32u) {
        const unsigned tb = (k0 >> 6) * 32768u + (k0 & 32u) * 256u;
        f32x4 s[2]; float mx = m;
#pragma unroll
        for (int kt = 0; kt < 2; ++kt) {
            s[kt] = (f32x4){0.f, 0.f, 0.f, 0.f};
#pragma unroll
            for (int p = 0; p < 4; ++p) s[kt] = mfma(__builtin_amdgcn_raw_buffer_load_b128(srd, lane * 16u + (kt * 4 + p) * 1024u, tb, 16), q[p], s[kt]);
#pragma unroll
            for (int e = 0; e < 4; ++e) { if (k0 + 16u * kt + 4u * g + e > r0 + c) s[kt][e] = -3.0e38f; mx = fmaxf(mx, s[kt][e]); }
        }
        mx = fmaxf(mx, __shfl_xor(mx, 16)); mx = fmaxf(mx, __shfl_xor(mx, 32));
        const float al = __builtin_amdgcn_exp2f(m - mx); m = mx;
        u32x4 pf;
#pragma unroll
        for (int e = 0; e < 4; ++e) pf[e] = cvtpk2(__builtin_amdgcn_exp2f(s[e >> 1][2 * (e & 1)] - m), __builtin_amdgcn_exp2f(s[e >> 1][2 * (e & 1) + 1] - m));
        l *= al;
#pragma unroll
        for (int e = 0; e < 4; ++e) l += __uint_as_float(pf[e] << 16) + __uint_as_float(pf[e] & 0xffff0000u);
#pragma unroll
        for (int n = 0; n < 8; ++n) o[n] = mfma(__builtin_amdgcn_raw_buffer_load_b128(srd, lane * 16u + 16384u + ((n >> 2) * 8 + (n & 3)) * 1024u, tb - (k0 & 32u) * 128u, 16), pf, o[n] * al);
    }
    l += __shfl_xor(l, 16); l += __shfl_xor(l, 32);
    const float f = g1 / l;
    const __amdgpu_buffer_rsrc_t srdA = __builtin_amdgcn_make_buffer_rsrc((void*)(ws + WS_AF), (short)0, 128 * KS_TOT * 1024, 0x00020000);
#pragma unroll
    for (int p = 0; p < 4; ++p) {
        const f32x4 a = o[2 * p] * f, b = o[2 * p + 1] * f;
        __builtin_amdgcn_raw_buffer_store_b128((u32x4){cvtpk2(a[0], a[1]), cvtpk2(a[2], a[3]), cvtpk2(b[0], b[1]), cvtpk2(b[2], b[3])}, srdA, lane * 16u + p * 1024u, ((r0 >> 4) * KS_TOT + 18u + 4u * h) * 1024u, 16);
    }
}
}

#define ATT_ASM \
"L_att0:\n" \
"s_mov_b32 s101, 0\n" \
"s_waitcnt vmcnt(20) lgkmcnt(0)\n" \
"s_barrier\n" \
"v_accvgpr_write_b32 a216, 0\n" \
"v_accvgpr_write_b32 a217, 0\n" \
"v_accvgpr_write_b32 a218, 0\n" \
"v_accvgpr_write_b32 a219, 0\n" \
"v_and_b32_e32 v63, 0x300, v20\n" \
"v_add_u32_e32 v58, s20, v20\n" \
"v_mfma_f32_32x32x16_bf16 v[128:143], a[216:219], a[216:219], 0\n" \
"ds_read_b128 v[112:115], v63 offset:0\n" \
"ds_read_b128 a[0:3], v58 offset:0\n" \
"ds_read_b128 a[16:19], v58 offset:8192\n" \
"ds_read_b128 v[116:119], v63 offset:1024\n" \
"ds_read_b128 a[4:7], v58 offset:1024\n" \
"v_add_u32_e32 v59, s21, v20\n" \
"v_mfma_f32_32x32x16_bf16 v[144:159], a[216:219], a[216:219], 0\n" \
"ds_read_b128 a[32:35], v59 offset:0\n" \
"ds_read_b128 a[48:51], v59 offset:8192\n" \
"s_sub_u32 s67, s12, 0xc000\n" \
"s_mov_b32 s65, s12\n" \
"v_add_u32_e32 v55, s67, v20\n" \
"v_add_u32_e32 v52, 0xc000, v55\n" \
"v_mfma_f32_32x32x16_bf16 v[160:175], a[216:219], a[216:219], 0\n" \
"ds_read_b128 a[20:23], v58 offset:9216\n" \
"s_xor_b32 m0, s65, 0x10000\n" \
"ds_read_b128 a[36:39], v59 offset:1024\n" \
"buffer_load_dwordx4 v52, s[8:11], 0 offen offset:0 sc1 lds\n" \
"ds_read_b128 a[52:55], v59 offset:9216\n" \
"v_mfma_f32_32x32x16_bf16 v[176:191], a[216:219], a[216:219], 0\n" \
"buffer_load_dwordx4 v52, s[8:11], 0 offen offset:1024 sc1 lds\n" \
"ds_read_b128 v[120:123], v63 offset:2048\n" \
"v_add_u32_e32 v60, s15, v20\n" \
"v_add_u32_e32 v60, 0x10000, v60\n" \
"v_mov_b32_e32 v56, s62\n" \
"v_mfma_f32_32x32x16_bf16 v[192:207], a[216:219], a[216:219], 0\n" \
"ds_read_b128 a[8:11], v58 offset:2048\n" \
"ds_read_b128 a[24:27], v58 offset:10240\n" \
"v_readlane_b32 s68, v11, 0\n" \
"v_bfe_i32 v21, v20, 4, 1\n" \
"v_mov_b32_e32 v54, 0x3f803f80\n" \
"v_mov_b32_e32 v57, 0x3f003f00\n" \
"v_mfma_f32_32x32x16_bf16 v[208:223], a[216:219], a[216:219], 0\n" \
"ds_read_b128 a[40:43], v59 offset:2048\n" \
"ds_read_b128 a[56:59], v59 offset:10240\n" \
"s_waitcnt lgkmcnt(14)\n" \
"v_xor_b32_e32 v112, 0x80008000, v112\n" \
"v_xor_b32_e32 v113, 0x80008000, v113\n" \
"v_xor_b32_e32 v114, 0x80008000, v114\n" \
"v_mfma_f32_32x32x16_bf16 v[224:239], a[216:219], a[216:219], 0\n" \
"ds_read_b128 v[124:127], v63 offset:3072\n" \
"ds_read_b128 a[12:15], v58 offset:3072\n" \
"v_xor_b32_e32 v115, 0x80008000, v115\n" \
"v_and_b32_e32 v21, 0xaaaaaaaa, v21\n" \
"v_accvgpr_write_b32 a220, v112\n" \
"s_waitcnt lgkmcnt(13)\n" \
"v_xor_b32_e32 v116, 0x80008000, v116\n" \
"v_mfma_f32_32x32x16_bf16 v[240:255], a[216:219], a[216:219], 0\n" \
"ds_read_b128 a[28:31], v58 offset:11264\n" \
"ds_read_b128 a[44:47], v59 offset:3072\n" \
"v_xor_b32_e32 v117, 0x80008000, v117\n" \
"v_xor_b32_e32 v118, 0x80008000, v118\n" \
"v_xor_b32_e32 v119, 0x80008000, v119\n" \
"v_accvgpr_write_b32 a221, v113\n" \
"v_mfma_f32_32x32x16_bf16 a[192:207], a[216:219], a[216:219], 0\n" \
"ds_read_b128 a[60:63], v59 offset:11264\n" \
"ds_read_b128 a[160:163], v20 offset:0\n" \
"v_accvgpr_write_b32 a222, v114\n" \
"v_accvgpr_write_b32 a223, v115\n" \
"v_readlane_b32 s80, v11, 9\n" \
"v_readlane_b32 s81, v11, 10\n" \
"v_mfma_f32_16x16x32_bf16 v[32:35], v[112:115], a[0:3], 0\n" \
"ds_read_b128 a[164:167], v20 offset:1024\n" \
"ds_read_b128 a[168:171], v20 offset:2048\n" \
"v_mfma_f32_16x16x32_bf16 v[36:39], v[112:115], a[16:19], 0\n" \
"ds_read_b128 a[172:175], v20 offset:3072\n" \
"s_waitcnt lgkmcnt(15)\n" \
"v_mfma_f32_16x16x32_bf16 v[40:43], v[112:115], a[32:35], 0\n" \
"ds_read_b128 a[188:191], v20 offset:7168\n" \
"s_waitcnt lgkmcnt(14)\n" \
"v_xor_b32_e32 v120, 0x80008000, v120\n" \
"v_mfma_f32_16x16x32_bf16 v[44:47], v[112:115], a[48:51], 0\n" \
"ds_read_b128 a[184:187], v20 offset:6144\n" \
"v_xor_b32_e32 v121, 0x80008000, v121\n" \
"v_mfma_f32_16x16x32_bf16 v[32:35], v[116:119], a[4:7], v[32:35]\n" \
"v_xor_b32_e32 v122, 0x80008000, v122\n" \
"v_xor_b32_e32 v123, 0x80008000, v123\n" \
"v_mfma_f32_16x16x32_bf16 v[36:39], v[116:119], a[20:23], v[36:39]\n" \
"ds_read_b128 a[180:183], v20 offset:5120\n" \
"ds_read_b128 a[176:179], v20 offset:4096\n" \
"v_mfma_f32_16x16x32_bf16 v[40:43], v[116:119], a[36:39], v[40:43]\n" \
"ds_read_b128 a[128:131], v20 offset:8192\n" \
"s_mov_b32 s82, 0x47fca0\n" \
"v_mfma_f32_16x16x32_bf16 v[44:47], v[116:119], a[52:55], v[44:47]\n" \
"s_waitcnt lgkmcnt(13)\n" \
"v_xor_b32_e32 v124, 0x80008000, v124\n" \
"v_mfma_f32_16x16x32_bf16 v[32:35], v[120:123], a[8:11], v[32:35]\n" \
"v_xor_b32_e32 v125, 0x80008000, v125\n" \
"v_xor_b32_e32 v126, 0x80008000, v126\n" \
"v_mfma_f32_16x16x32_bf16 v[36:39], v[120:123], a[24:27], v[36:39]\n" \
"ds_read_b128 a[132:135], v20 offset:9216\n" \
"v_xor_b32_e32 v127, 0x80008000, v127\n" \
"v_mfma_f32_16x16x32_bf16 v[40:43], v[120:123], a[40:43], v[40:43]\n" \
"ds_read_b128 a[136:139], v20 offset:10240\n" \
"ds_read_b128 a[140:143], v20 offset:11264\n" \
"v_mfma_f32_16x16x32_bf16 v[44:47], v[120:123], a[56:59], v[44:47]\n" \
"ds_read_b128 a[144:147], v20 offset:12288\n" \
"s_waitcnt lgkmcnt(15)\n" \
"v_mfma_f32_16x16x32_bf16 v[32:35], v[124:127], a[12:15], v[32:35]\n" \
"ds_read_b128 a[148:151], v20 offset:13312\n" \
"ds_read_b128 a[152:155], v20 offset:14336\n" \
"v_mfma_f32_16x16x32_bf16 v[36:39], v[124:127], a[28:31], v[36:39]\n" \
"ds_read_b128 a[156:159], v20 offset:15360\n" \
"s_waitcnt lgkmcnt(15)\n" \
"v_mfma_f32_16x16x32_bf16 v[40:43], v[124:127], a[44:47], v[40:43]\n" \
"ds_read_b128 a[64:67], v60 offset:16384\n" \
"s_mov_b32 s83, 0x20000\n" \
"v_mfma_f32_16x16x32_bf16 v[44:47], v[124:127], a[60:63], v[44:47]\n" \
"ds_read_b128 a[68:71], v60 offset:17408\n" \
"s_mov_b32 s100, 0x71800000\n" \
"v_mfma_f32_16x16x32_bf16 v[64:67], a[160:163], a[0:3], v[32:35]\n" \
"ds_read_b128 a[72:75], v60 offset:18432\n" \
"s_waitcnt lgkmcnt(15)\n" \
"v_mfma_f32_16x16x32_bf16 v[64:67], a[164:167], a[4:7], v[64:67]\n" \
"ds_read_b128 a[76:79], v60 offset:19456\n" \
"s_mov_b32 s63, s23\n" \
"v_mfma_f32_16x16x32_bf16 v[64:67], a[168:171], a[8:11], v[64:67]\n" \
"ds_read_b128 a[80:83], v60 offset:24576\n" \
"v_mov_b32_e32 v62, 0\n" \
"v_mfma_f32_16x16x32_bf16 v[64:67], a[172:175], a[12:15], v[64:67]\n" \
"ds_read_b128 a[84:87], v60 offset:25600\n" \
"s_waitcnt lgkmcnt(15)\n" \
"v_mfma_f32_16x16x32_bf16 v[68:71], a[188:191], a[12:15], v[32:35]\n" \
"ds_read_b128 a[88:91], v60 offset:26624\n" \
"v_accvgpr_write_b32 a212, v54\n" \
"v_mfma_f32_16x16x32_bf16 v[68:71], a[184:187], a[8:11], v[68:71]\n" \
"ds_read_b128 a[92:95], v60 offset:27648\n" \
"v_accvgpr_write_b32 a208, 0\n" \
"v_mfma_f32_16x16x32_bf16 v[68:71], a[180:183], a[4:7], v[68:71]\n" \
"ds_read_b128 a[96:99], v60 offset:0\n" \
"s_waitcnt lgkmcnt(15)\n" \
"v_mfma_f32_16x16x32_bf16 v[68:71], a[176:179], a[0:3], v[68:71]\n" \
"ds_read_b128 a[100:103], v60 offset:1024\n" \
"v_accvgpr_write_b32 a213, v54\n" \
"v_mfma_f32_16x16x32_bf16 v[72:75], a[160:163], a[16:19], v[36:39]\n" \
"L_pf10:\n" \
"s_cbranch_execz L_pf14\n" \
"s_nop 0\n" \
"ds_read_b128 a[104:107], v60 offset:2048\n" \
"v_accvgpr_write_b32 a209, 0\n" \
"v_mfma_f32_16x16x32_bf16 v[72:75], a[164:167], a[20:23], v[72:75]\n" \
"ds_read_b128 a[108:111], v60 offset:3072\n" \
"v_accvgpr_write_b32 a214, v54\n" \
"v_mfma_f32_16x16x32_bf16 v[72:75], a[168:171], a[24:27], v[72:75]\n" \
"ds_read_b128 a[112:115], v60 offset:8192\n" \
"v_accvgpr_write_b32 a210, 0\n" \
"v_mfma_f32_16x16x32_bf16 v[72:75], a[172:175], a[28:31], v[72:75]\n" \
"ds_read_b128 a[116:119], v60 offset:9216\n" \
"v_accvgpr_write_b32 a215, v54\n" \
"v_mfma_f32_16x16x32_bf16 v[76:79], a[188:191], a[28:31], v[36:39]\n" \
"ds_read_b128 a[120:123], v60 offset:10240\n" \
"v_accvgpr_write_b32 a211, 0\n" \
"v_mfma_f32_16x16x32_bf16 v[76:79], a[184:187], a[24:27], v[76:79]\n" \
"ds_read_b128 a[124:127], v60 offset:11264\n" \
"v_or_b32_e32 v21, 0x44444444, v21\n" \
"v_mfma_f32_16x16x32_bf16 v[76:79], a[180:183], a[20:23], v[76:79]\n" \
"v_exp_f32_e32 v64, v64\n" \
"v_mfma_f32_16x16x32_bf16 v[76:79], a[176:179], a[16:19], v[76:79]\n" \
"v_exp_f32_e32 v65, v65\n" \
"v_mfma_f32_16x16x32_bf16 v[80:83], a[160:163], a[32:35], v[40:43]\n" \
"v_exp_f32_e32 v66, v66\n" \
"v_mfma_f32_16x16x32_bf16 v[80:83], a[164:167], a[36:39], v[80:83]\n" \
"v_exp_f32_e32 v67, v67\n" \
"v_mfma_f32_16x16x32_bf16 v[80:83], a[168:171], a[40:43], v[80:83]\n" \
"v_exp_f32_e32 v68, v68\n" \
"v_mfma_f32_16x16x32_bf16 v[80:83], a[172:175], a[44:47], v[80:83]\n" \
"v_exp_f32_e32 v69, v69\n" \
"v_mfma_f32_16x16x32_bf16 v[84:87], a[188:191], a[44:47], v[40:43]\n" \
"v_exp_f32_e32 v70, v70\n" \
"v_mfma_f32_16x16x32_bf16 v[84:87], a[184:187], a[40:43], v[84:87]\n" \
"v_exp_f32_e32 v71, v71\n" \
"v_mfma_f32_16x16x32_bf16 v[84:87], a[180:183], a[36:39], v[84:87]\n" \
"v_exp_f32_e32 v72, v72\n" \
"v_mfma_f32_16x16x32_bf16 v[84:87], a[176:179], a[32:35], v[84:87]\n" \
"v_exp_f32_e32 v73, v73\n" \
"v_mfma_f32_16x16x32_bf16 v[88:91], a[160:163], a[48:51], v[44:47]\n" \
"v_exp_f32_e32 v74, v74\n" \
"v_mfma_f32_16x16x32_bf16 v[88:91], a[164:167], a[52:55], v[88:91]\n" \
"v_exp_f32_e32 v75, v75\n" \
"v_mfma_f32_16x16x32_bf16 v[88:91], a[168:171], a[56:59], v[88:91]\n" \
"v_exp_f32_e32 v76, v76\n" \
"v_mfma_f32_16x16x32_bf16 v[88:91], a[172:175], a[60:63], v[88:91]\n" \
"v_exp_f32_e32 v77, v77\n" \
"v_mfma_f32_16x16x32_bf16 v[92:95], a[188:191], a[60:63], v[44:47]\n" \
"v_exp_f32_e32 v78, v78\n" \
"v_mfma_f32_16x16x32_bf16 v[92:95], a[184:187], a[56:59], v[92:95]\n" \
"v_exp_f32_e32 v79, v79\n" \
"v_mfma_f32_16x16x32_bf16 v[92:95], a[180:183], a[52:55], v[92:95]\n" \
"v_exp_f32_e32 v80, v80\n" \
"v_mfma_f32_16x16x32_bf16 v[92:95], a[176:179], a[48:51], v[92:95]\n" \
"v_exp_f32_e32 v81, v81\n" \
"v_exp_f32_e32 v82, v82\n" \
"v_exp_f32_e32 v83, v83\n" \
"v_exp_f32_e32 v84, v84\n" \
"v_exp_f32_e32 v85, v85\n" \
"v_exp_f32_e32 v86, v86\n" \
"v_exp_f32_e32 v87, v87\n" \
"v_accvgpr_write_b32 a252, v57\n" \
"v_accvgpr_write_b32 a253, v57\n" \
"v_accvgpr_write_b32 a254, v57\n" \
"v_accvgpr_write_b32 a255, v57\n" \
"s_mov_b32 s64, 0x8000\n" \
"v_add_u32_e32 v50, 0x6000, v55\n" \
"v_add_u32_e32 v51, 0xa000, v55\n" \
"v_add_u32_e32 v53, 0x10000, v55\n" \
"v_add_u32_e32 v48, 0xc000, v20\n" \
"v_add_u32_e32 v49, s17, v20\n" \
"v_accvgpr_write_b32 a224, v116\n" \
"v_accvgpr_write_b32 a225, v117\n" \
"v_accvgpr_write_b32 a226, v118\n" \
"v_accvgpr_write_b32 a227, v119\n" \
"v_accvgpr_write_b32 a228, v120\n" \
"v_accvgpr_write_b32 a229, v121\n" \
"v_accvgpr_write_b32 a230, v122\n" \
"v_accvgpr_write_b32 a231, v123\n" \
"v_accvgpr_write_b32 a232, v124\n" \
"v_accvgpr_write_b32 a233, v125\n" \
"v_accvgpr_write_b32 a234, v126\n" \
"v_accvgpr_write_b32 a235, v127\n" \
"v_exp_f32_e32 v88, v88\n" \
"v_exp_f32_e32 v89, v89\n" \
"v_exp_f32_e32 v90, v90\n" \
"v_exp_f32_e32 v91, v91\n" \
"v_cvt_pk_bf16_f32 v96, v64, v65\n" \
"v_cvt_pk_bf16_f32 v97, v66, v67\n" \
"v_cvt_pk_bf16_f32 v98, v68, v69\n" \
"v_cvt_pk_bf16_f32 v99, v70, v71\n" \
"v_cvt_pk_bf16_f32 v104, v72, v73\n" \
"v_cvt_pk_bf16_f32 v105, v74, v75\n" \
"v_cvt_pk_bf16_f32 v106, v76, v77\n" \
"v_cvt_pk_bf16_f32 v107, v78, v79\n" \
"v_cvt_pk_bf16_f32 v112, v80, v81\n" \
"v_cvt_pk_bf16_f32 v113, v82, v83\n" \
"v_cvt_pk_bf16_f32 v114, v84, v85\n" \
"v_cvt_pk_bf16_f32 v115, v86, v87\n" \
"s_sub_u32 s66, s18, 1\n" \
"s_waitcnt vmcnt(2) lgkmcnt(0)\n" \
"s_barrier\n" \
"s_mov_b64 exec, 1\n" \
"buffer_store_dword v56, off, s[56:59], s68 offset:0 sc1\n" \
"buffer_store_dword v56, off, s[56:59], s22 sc1\n" \
"s_mov_b64 exec, -1\n" \
"s_cbranch_scc1 L_tr_%=\n" \
"s_branch L_1_%=\n" \
"L_t1_%=:\n" \
"v_mfma_f32_16x16x32_bf16 v[252:255], a[188:191], v[124:127], v[252:255]\n" \
"v_mfma_f32_16x16x32_bf16 v[252:255], a[172:175], v[120:123], v[252:255]\n" \
"v_smfmac_f32_16x16x64_bf16 a[204:207], a[212:215], v[120:127], v21\n" \
"s_waitcnt lgkmcnt(0)\n" \
"L_1_%=:\n" \
"v_mfma_f32_16x16x32_bf16 v[64:67], a[128:131], a[0:3], v[32:35]\n" \
"ds_read_b128 a[160:163], v49 offset:0\n" \
"ds_read_b128 a[176:179], v49 offset:4096\n" \
"v_mfma_f32_16x16x32_bf16 v[64:67], a[132:135], a[4:7], v[64:67]\n" \
"v_cvt_pk_bf16_f32 v120, v88, v89\n" \
"v_cvt_pk_bf16_f32 v121, v90, v91\n" \
"v_mfma_f32_16x16x32_bf16 v[64:67], a[136:139], a[8:11], v[64:67]\n" \
"ds_read_b128 a[180:183], v49 offset:5120\n" \
"ds_read_b128 a[164:167], v49 offset:1024\n" \
"v_mfma_f32_16x16x32_bf16 v[64:67], a[140:143], a[12:15], v[64:67]\n" \
"v_exp_f32_e32 v92, v92\n" \
"v_mfma_f32_16x16x32_bf16 v[68:71], a[156:159], a[12:15], v[32:35]\n" \
"ds_read_b128 a[168:171], v49 offset:2048\n" \
"ds_read_b128 a[184:187], v49 offset:6144\n" \
"v_mfma_f32_16x16x32_bf16 v[68:71], a[152:155], a[8:11], v[68:71]\n" \
"v_exp_f32_e32 v93, v93\n" \
"v_mfma_f32_16x16x32_bf16 v[68:71], a[148:151], a[4:7], v[68:71]\n" \
"ds_read_b128 a[188:191], v49 offset:7168\n" \
"ds_read_b128 a[172:175], v49 offset:3072\n" \
"v_mfma_f32_16x16x32_bf16 v[68:71], a[144:147], a[0:3], v[68:71]\n" \
"v_exp_f32_e32 v94, v94\n" \
"v_mfma_f32_16x16x32_bf16 v[72:75], a[128:131], a[16:19], v[36:39]\n" \
"v_exp_f32_e32 v95, v95\n" \
"v_mfma_f32_16x16x32_bf16 v[72:75], a[132:135], a[20:23], v[72:75]\n" \
"v_cvt_pk_bf16_f32 v122, v92, v93\n" \
"v_cvt_pk_bf16_f32 v123, v94, v95\n" \
"v_mfma_f32_16x16x32_bf16 v[72:75], a[136:139], a[24:27], v[72:75]\n" \
"v_exp_f32_e32 v64, v64\n" \
"v_mfma_f32_16x16x32_bf16 v[72:75], a[140:143], a[28:31], v[72:75]\n" \
"v_exp_f32_e32 v65, v65\n" \
"v_mfma_f32_16x16x32_bf16 v[76:79], a[156:159], a[28:31], v[36:39]\n" \
"v_exp_f32_e32 v66, v66\n" \
"v_mfma_f32_16x16x32_bf16 v[76:79], a[152:155], a[24:27], v[76:79]\n" \
"v_exp_f32_e32 v67, v67\n" \
"v_mfma_f32_16x16x32_bf16 v[76:79], a[148:151], a[20:23], v[76:79]\n" \
"v_exp_f32_e32 v68, v68\n" \
"v_mfma_f32_16x16x32_bf16 v[76:79], a[144:147], a[16:19], v[76:79]\n" \
"v_exp_f32_e32 v69, v69\n" \
"v_mfma_f32_16x16x32_bf16 v[80:83], a[128:131], a[32:35], v[40:43]\n" \
"v_exp_f32_e32 v70, v70\n" \
"v_mfma_f32_16x16x32_bf16 v[80:83], a[132:135], a[36:39], v[80:83]\n" \
"v_exp_f32_e32 v71, v71\n" \
"v_mfma_f32_16x16x32_bf16 v[80:83], a[136:139], a[40:43], v[80:83]\n" \
"v_cvt_pk_bf16_f32 v100, v64, v65\n" \
"v_cvt_pk_bf16_f32 v101, v66, v67\n" \
"v_mfma_f32_16x16x32_bf16 v[80:83], a[140:143], a[44:47], v[80:83]\n" \
"v_cvt_pk_bf16_f32 v102, v68, v69\n" \
"v_cvt_pk_bf16_f32 v103, v70, v71\n" \
"v_mfma_f32_16x16x32_bf16 v[84:87], a[156:159], a[44:47], v[40:43]\n" \
"v_exp_f32_e32 v72, v72\n" \
"v_mfma_f32_16x16x32_bf16 v[84:87], a[152:155], a[40:43], v[84:87]\n" \
"v_exp_f32_e32 v73, v73\n" \
"v_mfma_f32_16x16x32_bf16 v[84:87], a[148:151], a[36:39], v[84:87]\n" \
"v_exp_f32_e32 v74, v74\n" \
"v_mfma_f32_16x16x32_bf16 v[84:87], a[144:147], a[32:35], v[84:87]\n" \
"v_exp_f32_e32 v75, v75\n" \
"v_mfma_f32_16x16x32_bf16 v[88:91], a[128:131], a[48:51], v[44:47]\n" \
"v_exp_f32_e32 v76, v76\n" \
"v_mfma_f32_16x16x32_bf16 v[88:91], a[132:135], a[52:55], v[88:91]\n" \
"v_exp_f32_e32 v77, v77\n" \
"v_mfma_f32_16x16x32_bf16 v[88:91], a[136:139], a[56:59], v[88:91]\n" \
"v_exp_f32_e32 v78, v78\n" \
"v_mfma_f32_16x16x32_bf16 v[88:91], a[140:143], a[60:63], v[88:91]\n" \
"v_exp_f32_e32 v79, v79\n" \
"v_mfma_f32_16x16x32_bf16 v[92:95], a[156:159], a[60:63], v[44:47]\n" \
"v_cvt_pk_bf16_f32 v108, v72, v73\n" \
"v_cvt_pk_bf16_f32 v109, v74, v75\n" \
"v_mfma_f32_16x16x32_bf16 v[92:95], a[152:155], a[56:59], v[92:95]\n" \
"v_cvt_pk_bf16_f32 v110, v76, v77\n" \
"v_cvt_pk_bf16_f32 v111, v78, v79\n" \
"v_mfma_f32_16x16x32_bf16 v[92:95], a[148:151], a[52:55], v[92:95]\n" \
"ds_read_b128 a[128:131], v48 offset:0\n" \
"ds_read_b128 a[132:135], v48 offset:1024\n" \
"v_mfma_f32_16x16x32_bf16 v[92:95], a[144:147], a[48:51], v[92:95]\n" \
"s_add_u32 s63, s63, s63\n" \
"s_waitcnt lgkmcnt(2)\n" \
"v_mfma_f32_16x16x32_bf16 v[128:131], a[160:163], v[96:99], v[128:131]\n" \
"ds_read_b128 a[136:139], v48 offset:2048\n" \
"ds_read_b128 a[140:143], v48 offset:3072\n" \
"v_mfma_f32_16x16x32_bf16 v[128:131], a[176:179], v[100:103], v[128:131]\n" \
"v_exp_f32_e32 v80, v80\n" \
"v_mfma_f32_16x16x32_bf16 v[132:135], a[180:183], v[100:103], v[132:135]\n" \
"ds_read_b128 a[144:147], v48 offset:4096\n" \
"ds_read_b128 a[148:151], v48 offset:5120\n" \
"v_mfma_f32_16x16x32_bf16 v[132:135], a[164:167], v[96:99], v[132:135]\n" \
"v_exp_f32_e32 v81, v81\n" \
"v_mfma_f32_16x16x32_bf16 v[136:139], a[168:171], v[96:99], v[136:139]\n" \
"ds_read_b128 a[152:155], v48 offset:6144\n" \
"ds_read_b128 a[156:159], v48 offset:7168\n" \
"v_mfma_f32_16x16x32_bf16 v[136:139], a[184:187], v[100:103], v[136:139]\n" \
"v_exp_f32_e32 v82, v82\n" \
"v_mfma_f32_16x16x32_bf16 v[140:143], a[188:191], v[100:103], v[140:143]\n" \
"v_exp_f32_e32 v83, v83\n" \
"v_mfma_f32_16x16x32_bf16 v[140:143], a[172:175], v[96:99], v[140:143]\n" \
"v_exp_f32_e32 v84, v84\n" \
"v_smfmac_f32_16x16x64_bf16 a[192:195], a[212:215], v[96:103], v21\n" \
"v_mfma_f32_16x16x32_bf16 v[160:163], a[160:163], v[104:107], v[160:163]\n" \
"v_exp_f32_e32 v85, v85\n" \
"v_mfma_f32_16x16x32_bf16 v[160:163], a[176:179], v[108:111], v[160:163]\n" \
"v_exp_f32_e32 v86, v86\n" \
"v_mfma_f32_16x16x32_bf16 v[164:167], a[180:183], v[108:111], v[164:167]\n" \
"v_exp_f32_e32 v87, v87\n" \
"v_mfma_f32_16x16x32_bf16 v[164:167], a[164:167], v[104:107], v[164:167]\n" \
"v_cvt_pk_bf16_f32 v116, v80, v81\n" \
"v_cvt_pk_bf16_f32 v117, v82, v83\n" \
"v_mfma_f32_16x16x32_bf16 v[168:171], a[168:171], v[104:107], v[168:171]\n" \
"v_cvt_pk_bf16_f32 v118, v84, v85\n" \
"v_cvt_pk_bf16_f32 v119, v86, v87\n" \
"v_mfma_f32_16x16x32_bf16 v[168:171], a[184:187], v[108:111], v[168:171]\n" \
"v_exp_f32_e32 v88, v88\n" \
"v_mfma_f32_16x16x32_bf16 v[172:175], a[188:191], v[108:111], v[172:175]\n" \
"v_exp_f32_e32 v89, v89\n" \
"v_mfma_f32_16x16x32_bf16 v[172:175], a[172:175], v[104:107], v[172:175]\n" \
"v_exp_f32_e32 v90, v90\n" \
"v_smfmac_f32_16x16x64_bf16 a[196:199], a[212:215], v[104:111], v21\n" \
"v_mfma_f32_16x16x32_bf16 v[192:195], a[160:163], v[112:115], v[192:195]\n" \
"v_exp_f32_e32 v91, v91\n" \
"v_mfma_f32_16x16x32_bf16 v[192:195], a[176:179], v[116:119], v[192:195]\n" \
"v_exp_f32_e32 v92, v92\n" \
"v_mfma_f32_16x16x32_bf16 v[196:199], a[180:183], v[116:119], v[196:199]\n" \
"v_exp_f32_e32 v93, v93\n" \
"v_mfma_f32_16x16x32_bf16 v[196:199], a[164:167], v[112:115], v[196:199]\n" \
"v_exp_f32_e32 v94, v94\n" \
"v_mfma_f32_16x16x32_bf16 v[200:203], a[168:171], v[112:115], v[200:203]\n" \
"v_exp_f32_e32 v95, v95\n" \
"v_mfma_f32_16x16x32_bf16 v[200:203], a[184:187], v[116:119], v[200:203]\n" \
"v_cvt_pk_bf16_f32 v124, v88, v89\n" \
"v_cvt_pk_bf16_f32 v125, v90, v91\n" \
"v_mfma_f32_16x16x32_bf16 v[204:207], a[188:191], v[116:119], v[204:207]\n" \
"v_cvt_pk_bf16_f32 v126, v92, v93\n" \
"v_cvt_pk_bf16_f32 v127, v94, v95\n" \
"v_mfma_f32_16x16x32_bf16 v[204:207], a[172:175], v[112:115], v[204:207]\n" \
"v_smfmac_f32_16x16x64_bf16 a[200:203], a[212:215], v[112:119], v21\n" \
"s_waitcnt vmcnt(0) lgkmcnt(0)\n" \
"s_barrier\n" \
"v_mfma_f32_16x16x32_bf16 v[224:227], a[160:163], v[120:123], v[224:227]\n" \
"s_cbranch_scc1 L_h1_%=\n" \
"L_hr1_%=:\n" \
"v_mfma_f32_16x16x32_bf16 v[224:227], a[176:179], v[124:127], v[224:227]\n" \
"s_xor_b32 m0, s65, 0x12000\n" \
"ds_read_b128 a[160:163], v49 offset:8192\n" \
"v_mfma_f32_16x16x32_bf16 v[228:231], a[180:183], v[124:127], v[228:231]\n" \
"buffer_load_dwordx4 v50, s[8:11], s64 offen offset:0 sc1 lds\n" \
"v_mfma_f32_16x16x32_bf16 v[228:231], a[164:167], v[120:123], v[228:231]\n" \
"ds_read_b128 a[176:179], v49 offset:12288\n" \
"ds_read_b128 a[180:183], v49 offset:13312\n" \
"v_mfma_f32_16x16x32_bf16 v[232:235], a[168:171], v[120:123], v[232:235]\n" \
"v_mfma_f32_16x16x32_bf16 v[232:235], a[184:187], v[124:127], v[232:235]\n" \
"ds_read_b128 a[164:167], v49 offset:9216\n" \
"ds_read_b128 a[168:171], v49 offset:10240\n" \
"v_mfma_f32_16x16x32_bf16 v[236:239], a[188:191], v[124:127], v[236:239]\n" \
"v_mfma_f32_16x16x32_bf16 v[236:239], a[172:175], v[120:123], v[236:239]\n" \
"ds_read_b128 a[184:187], v49 offset:14336\n" \
"ds_read_b128 a[188:191], v49 offset:15360\n" \
"v_mfma_f32_16x16x32_bf16 v[64:67], a[128:131], a[0:3], v[32:35]\n" \
"v_mfma_f32_16x16x32_bf16 v[64:67], a[132:135], a[4:7], v[64:67]\n" \
"buffer_load_dwordx4 v50, s[8:11], s64 offen offset:1024 sc1 lds\n" \
"v_mfma_f32_16x16x32_bf16 v[64:67], a[136:139], a[8:11], v[64:67]\n" \
"s_xor_b32 m0, s65, 0x6000\n" \
"ds_read_b128 a[172:175], v49 offset:11264\n" \
"v_mfma_f32_16x16x32_bf16 v[64:67], a[140:143], a[12:15], v[64:67]\n" \
"v_add_u32_e32 v49, 0x10000, v48\n" \
"v_mfma_f32_16x16x32_bf16 v[68:71], a[156:159], a[12:15], v[32:35]\n" \
"v_mfma_f32_16x16x32_bf16 v[68:71], a[152:155], a[8:11], v[68:71]\n" \
"v_mfma_f32_16x16x32_bf16 v[68:71], a[148:151], a[4:7], v[68:71]\n" \
"v_mfma_f32_16x16x32_bf16 v[68:71], a[144:147], a[0:3], v[68:71]\n" \
"v_mfma_f32_16x16x32_bf16 v[72:75], a[128:131], a[16:19], v[36:39]\n" \
"v_mfma_f32_16x16x32_bf16 v[72:75], a[132:135], a[20:23], v[72:75]\n" \
"v_mfma_f32_16x16x32_bf16 v[72:75], a[136:139], a[24:27], v[72:75]\n" \
"buffer_load_dwordx4 v51, s[8:11], s64 offen offset:0 sc1 lds\n" \
"v_mfma_f32_16x16x32_bf16 v[72:75], a[140:143], a[28:31], v[72:75]\n" \
"v_exp_f32_e32 v64, v64\n" \
"v_mfma_f32_16x16x32_bf16 v[76:79], a[156:159], a[28:31], v[36:39]\n" \
"v_exp_f32_e32 v65, v65\n" \
"v_mfma_f32_16x16x32_bf16 v[76:79], a[152:155], a[24:27], v[76:79]\n" \
"v_exp_f32_e32 v66, v66\n" \
"v_mfma_f32_16x16x32_bf16 v[76:79], a[148:151], a[20:23], v[76:79]\n" \
"v_exp_f32_e32 v67, v67\n" \
"v_mfma_f32_16x16x32_bf16 v[76:79], a[144:147], a[16:19], v[76:79]\n" \
"v_exp_f32_e32 v68, v68\n" \
"v_mfma_f32_16x16x32_bf16 v[80:83], a[128:131], a[32:35], v[40:43]\n" \
"v_exp_f32_e32 v69, v69\n" \
"v_mfma_f32_16x16x32_bf16 v[80:83], a[132:135], a[36:39], v[80:83]\n" \
"v_exp_f32_e32 v70, v70\n" \
"v_mfma_f32_16x16x32_bf16 v[80:83], a[136:139], a[40:43], v[80:83]\n" \
"v_exp_f32_e32 v71, v71\n" \
"v_mfma_f32_16x16x32_bf16 v[80:83], a[140:143], a[44:47], v[80:83]\n" \
"buffer_load_dwordx4 v51, s[8:11], s64 offen offset:1024 sc1 lds\n" \
"v_mfma_f32_16x16x32_bf16 v[84:87], a[156:159], a[44:47], v[40:43]\n" \
"v_exp_f32_e32 v72, v72\n" \
"v_mfma_f32_16x16x32_bf16 v[84:87], a[152:155], a[40:43], v[84:87]\n" \
"v_exp_f32_e32 v73, v73\n" \
"v_mfma_f32_16x16x32_bf16 v[84:87], a[148:151], a[36:39], v[84:87]\n" \
"v_exp_f32_e32 v74, v74\n" \
"v_mfma_f32_16x16x32_bf16 v[84:87], a[144:147], a[32:35], v[84:87]\n" \
"v_exp_f32_e32 v75, v75\n" \
"v_mfma_f32_16x16x32_bf16 v[88:91], a[128:131], a[48:51], v[44:47]\n" \
"s_xor_b32 m0, s65, 0x14000\n" \
"ds_read_b128 a[128:131], v48 offset:8192\n" \
"v_mfma_f32_16x16x32_bf16 v[88:91], a[132:135], a[52:55], v[88:91]\n" \
"v_exp_f32_e32 v76, v76\n" \
"v_mfma_f32_16x16x32_bf16 v[88:91], a[136:139], a[56:59], v[88:91]\n" \
"ds_read_b128 a[132:135], v48 offset:9216\n" \
"ds_read_b128 a[136:139], v48 offset:10240\n" \
"v_mfma_f32_16x16x32_bf16 v[88:91], a[140:143], a[60:63], v[88:91]\n" \
"v_exp_f32_e32 v77, v77\n" \
"v_mfma_f32_16x16x32_bf16 v[92:95], a[156:159], a[60:63], v[44:47]\n" \
"buffer_load_dwordx4 v52, s[8:11], s64 offen offset:0 sc1 lds\n" \
"v_mfma_f32_16x16x32_bf16 v[92:95], a[152:155], a[56:59], v[92:95]\n" \
"ds_read_b128 a[140:143], v48 offset:11264\n" \
"ds_read_b128 a[152:155], v48 offset:14336\n" \
"v_mfma_f32_16x16x32_bf16 v[92:95], a[148:151], a[52:55], v[92:95]\n" \
"v_exp_f32_e32 v78, v78\n" \
"v_mfma_f32_16x16x32_bf16 v[92:95], a[144:147], a[48:51], v[92:95]\n" \
"ds_read_b128 a[144:147], v48 offset:12288\n" \
"s_waitcnt lgkmcnt(6)\n" \
"v_mfma_f32_16x16x32_bf16 v[144:147], a[160:163], v[96:99], v[144:147]\n" \
"ds_read_b128 a[148:151], v48 offset:13312\n" \
"ds_read_b128 a[156:159], v48 offset:15360\n" \
"v_mfma_f32_16x16x32_bf16 v[144:147], a[176:179], v[100:103], v[144:147]\n" \
"v_exp_f32_e32 v79, v79\n" \
"v_mfma_f32_16x16x32_bf16 v[148:151], a[180:183], v[100:103], v[148:151]\n" \
"v_exp_f32_e32 v80, v80\n" \
"v_mfma_f32_16x16x32_bf16 v[148:151], a[164:167], v[96:99], v[148:151]\n" \
"v_exp_f32_e32 v81, v81\n" \
"v_mfma_f32_16x16x32_bf16 v[152:155], a[168:171], v[96:99], v[152:155]\n" \
"buffer_load_dwordx4 v52, s[8:11], s64 offen offset:1024 sc1 lds\n" \
"v_mfma_f32_16x16x32_bf16 v[152:155], a[184:187], v[100:103], v[152:155]\n" \
"s_mov_b32 m0, s65\n" \
"s_xor_b32 s65, s65, 0x4000\n" \
"v_mfma_f32_16x16x32_bf16 v[156:159], a[188:191], v[100:103], v[156:159]\n" \
"v_exp_f32_e32 v82, v82\n" \
"v_mfma_f32_16x16x32_bf16 v[156:159], a[172:175], v[96:99], v[156:159]\n" \
"v_exp_f32_e32 v83, v83\n" \
"v_mfma_f32_16x16x32_bf16 v[176:179], a[160:163], v[104:107], v[176:179]\n" \
"v_exp_f32_e32 v84, v84\n" \
"v_mfma_f32_16x16x32_bf16 v[176:179], a[176:179], v[108:111], v[176:179]\n" \
"v_exp_f32_e32 v85, v85\n" \
"v_mfma_f32_16x16x32_bf16 v[180:183], a[180:183], v[108:111], v[180:183]\n" \
"v_exp_f32_e32 v86, v86\n" \
"v_mfma_f32_16x16x32_bf16 v[180:183], a[164:167], v[104:107], v[180:183]\n" \
"v_exp_f32_e32 v87, v87\n" \
"v_mfma_f32_16x16x32_bf16 v[184:187], a[168:171], v[104:107], v[184:187]\n" \
"v_exp_f32_e32 v88, v88\n" \
"v_mfma_f32_16x16x32_bf16 v[184:187], a[184:187], v[108:111], v[184:187]\n" \
"buffer_load_dwordx4 v53, s[8:11], s64 offen offset:0 sc1 lds\n" \
"v_mfma_f32_16x16x32_bf16 v[188:191], a[188:191], v[108:111], v[188:191]\n" \
"v_exp_f32_e32 v89, v89\n" \
"v_mfma_f32_16x16x32_bf16 v[188:191], a[172:175], v[104:107], v[188:191]\n" \
"v_exp_f32_e32 v90, v90\n" \
"v_mfma_f32_16x16x32_bf16 v[208:211], a[160:163], v[112:115], v[208:211]\n" \
"v_exp_f32_e32 v91, v91\n" \
"v_mfma_f32_16x16x32_bf16 v[208:211], a[176:179], v[116:119], v[208:211]\n" \
"v_cvt_pk_bf16_f32 v96, v64, v65\n" \
"v_cvt_pk_bf16_f32 v97, v66, v67\n" \
"v_mfma_f32_16x16x32_bf16 v[212:215], a[180:183], v[116:119], v[212:215]\n" \
"v_cvt_pk_bf16_f32 v98, v68, v69\n" \
"v_cvt_pk_bf16_f32 v99, v70, v71\n" \
"v_mfma_f32_16x16x32_bf16 v[212:215], a[164:167], v[112:115], v[212:215]\n" \
"v_cvt_pk_bf16_f32 v104, v72, v73\n" \
"v_cvt_pk_bf16_f32 v105, v74, v75\n" \
"v_mfma_f32_16x16x32_bf16 v[216:219], a[168:171], v[112:115], v[216:219]\n" \
"v_cvt_pk_bf16_f32 v106, v76, v77\n" \
"v_cvt_pk_bf16_f32 v107, v78, v79\n" \
"v_mfma_f32_16x16x32_bf16 v[216:219], a[184:187], v[116:119], v[216:219]\n" \
"v_xor_b32_e32 v48, 0x4000, v48\n" \
"v_mfma_f32_16x16x32_bf16 v[220:223], a[188:191], v[116:119], v[220:223]\n" \
"buffer_load_dwordx4 v53, s[8:11], s64 offen offset:1024 sc1 lds\n" \
"v_mfma_f32_16x16x32_bf16 v[220:223], a[172:175], v[112:115], v[220:223]\n" \
"s_add_u32 s64, s64, 0x8000\n" \
"s_sub_u32 s66, s66, 1\n" \
"v_mfma_f32_16x16x32_bf16 v[240:243], a[160:163], v[120:123], v[240:243]\n" \
"v_cvt_pk_bf16_f32 v112, v80, v81\n" \
"v_cvt_pk_bf16_f32 v113, v82, v83\n" \
"v_mfma_f32_16x16x32_bf16 v[240:243], a[176:179], v[124:127], v[240:243]\n" \
"v_cvt_pk_bf16_f32 v114, v84, v85\n" \
"v_cvt_pk_bf16_f32 v115, v86, v87\n" \
"v_mfma_f32_16x16x32_bf16 v[244:247], a[180:183], v[124:127], v[244:247]\n" \
"v_mfma_f32_16x16x32_bf16 v[244:247], a[164:167], v[120:123], v[244:247]\n" \
"v_mfma_f32_16x16x32_bf16 v[248:251], a[168:171], v[120:123], v[248:251]\n" \
"v_mfma_f32_16x16x32_bf16 v[248:251], a[184:187], v[124:127], v[248:251]\n" \
"s_cbranch_scc0 L_t1_%=\n" \
"v_mfma_f32_16x16x32_bf16 v[252:255], a[188:191], v[124:127], v[252:255]\n" \
"v_mfma_f32_16x16x32_bf16 v[252:255], a[172:175], v[120:123], v[252:255]\n" \
"v_smfmac_f32_16x16x64_bf16 a[204:207], a[212:215], v[120:127], v21\n" \
"s_waitcnt lgkmcnt(0)\n" \
"s_barrier\n" \
"L_tr_%=:\n" \
"L_pf11:\n" \
"s_cbranch_execz L_pf15\n" \
"s_nop 0\n" \
"s_cmp_lg_u32 s16, 0\n" \
"s_cbranch_scc1 L_sw_%=\n" \
"v_mfma_f32_16x16x32_bf16 v[64:67], a[128:131], a[0:3], v[32:35]\n" \
"s_xor_b32 m0, s65, 0x12000\n" \
"ds_read_b128 a[160:163], v49 offset:0\n" \
"v_mfma_f32_16x16x32_bf16 v[64:67], a[132:135], a[4:7], v[64:67]\n" \
"buffer_load_dwordx4 v50, s[8:11], s64 offen offset:0 sc1 lds\n" \
"v_mfma_f32_16x16x32_bf16 v[64:67], a[136:139], a[8:11], v[64:67]\n" \
"ds_read_b128 a[176:179], v49 offset:4096\n" \
"ds_read_b128 a[180:183], v49 offset:5120\n" \
"v_mfma_f32_16x16x32_bf16 v[64:67], a[140:143], a[12:15], v[64:67]\n" \
"s_mov_b32 s65, s13\n" \
"v_add_u32_e32 v63, s15, v20\n" \
"v_mfma_f32_16x16x32_bf16 v[68:71], a[156:159], a[12:15], v[32:35]\n" \
"s_add_u32 s67, s14, 40960\n" \
"ds_read_b128 a[164:167], v49 offset:1024\n" \
"v_mfma_f32_16x16x32_bf16 v[68:71], a[152:155], a[8:11], v[68:71]\n" \
"ds_read_b128 a[168:171], v49 offset:2048\n" \
"ds_read_b128 a[184:187], v49 offset:6144\n" \
"v_mfma_f32_16x16x32_bf16 v[68:71], a[148:151], a[4:7], v[68:71]\n" \
"buffer_load_dwordx4 v50, s[8:11], s64 offen offset:1024 sc1 lds\n" \
"v_mfma_f32_16x16x32_bf16 v[68:71], a[144:147], a[0:3], v[68:71]\n" \
"v_add_u32_e32 v50, 0x6000, v63\n" \
"s_xor_b32 m0, s65, 0x8000\n" \
"v_mfma_f32_16x16x32_bf16 v[72:75], a[128:131], a[16:19], v[36:39]\n" \
"v_cvt_pk_bf16_f32 v120, v88, v89\n" \
"v_cvt_pk_bf16_f32 v121, v90, v91\n" \
"v_mfma_f32_16x16x32_bf16 v[72:75], a[132:135], a[20:23], v[72:75]\n" \
"ds_read_b128 a[188:191], v49 offset:7168\n" \
"ds_read_b128 a[172:175], v49 offset:3072\n" \
"v_mfma_f32_16x16x32_bf16 v[72:75], a[136:139], a[24:27], v[72:75]\n" \
"v_exp_f32_e32 v92, v92\n" \
"v_mfma_f32_16x16x32_bf16 v[72:75], a[140:143], a[28:31], v[72:75]\n" \
"v_exp_f32_e32 v93, v93\n" \
"v_mfma_f32_16x16x32_bf16 v[76:79], a[156:159], a[28:31], v[36:39]\n" \
"buffer_load_dwordx4 v50, s[8:11], s67 offen offset:0 sc1 lds\n" \
"v_mfma_f32_16x16x32_bf16 v[76:79], a[152:155], a[24:27], v[76:79]\n" \
"v_exp_f32_e32 v94, v94\n" \
"v_mfma_f32_16x16x32_bf16 v[76:79], a[148:151], a[20:23], v[76:79]\n" \
"v_exp_f32_e32 v95, v95\n" \
"v_mfma_f32_16x16x32_bf16 v[76:79], a[144:147], a[16:19], v[76:79]\n" \
"v_cvt_pk_bf16_f32 v122, v92, v93\n" \
"v_cvt_pk_bf16_f32 v123, v94, v95\n" \
"v_mfma_f32_16x16x32_bf16 v[80:83], a[128:131], a[32:35], v[40:43]\n" \
"v_exp_f32_e32 v64, v64\n" \
"v_mfma_f32_16x16x32_bf16 v[80:83], a[132:135], a[36:39], v[80:83]\n" \
"v_exp_f32_e32 v65, v65\n" \
"v_mfma_f32_16x16x32_bf16 v[80:83], a[136:139], a[40:43], v[80:83]\n" \
"buffer_load_dwordx4 v50, s[8:11], s67 offen offset:1024 sc1 lds\n" \
"v_mfma_f32_16x16x32_bf16 v[80:83], a[140:143], a[44:47], v[80:83]\n" \
"v_exp_f32_e32 v66, v66\n" \
"v_mfma_f32_16x16x32_bf16 v[84:87], a[156:159], a[44:47], v[40:43]\n" \
"v_exp_f32_e32 v67, v67\n" \
"v_mfma_f32_16x16x32_bf16 v[84:87], a[152:155], a[40:43], v[84:87]\n" \
"v_exp_f32_e32 v68, v68\n" \
"v_mfma_f32_16x16x32_bf16 v[84:87], a[148:151], a[36:39], v[84:87]\n" \
"v_exp_f32_e32 v69, v69\n" \
"v_mfma_f32_16x16x32_bf16 v[84:87], a[144:147], a[32:35], v[84:87]\n" \
"v_exp_f32_e32 v70, v70\n" \
"v_mfma_f32_16x16x32_bf16 v[88:91], a[128:131], a[48:51], v[44:47]\n" \
"v_exp_f32_e32 v71, v71\n" \
"v_mfma_f32_16x16x32_bf16 v[88:91], a[132:135], a[52:55], v[88:91]\n" \
"v_cvt_pk_bf16_f32 v100, v64, v65\n" \
"v_cvt_pk_bf16_f32 v101, v66, v67\n" \
"v_mfma_f32_16x16x32_bf16 v[88:91], a[136:139], a[56:59], v[88:91]\n" \
"v_cvt_pk_bf16_f32 v102, v68, v69\n" \
"v_cvt_pk_bf16_f32 v103, v70, v71\n" \
"v_mfma_f32_16x16x32_bf16 v[88:91], a[140:143], a[60:63], v[88:91]\n" \
"buffer_load_dwordx4 v50, s[8:11], s67 offen offset:2048 sc1 lds\n" \
"v_mfma_f32_16x16x32_bf16 v[92:95], a[156:159], a[60:63], v[44:47]\n" \
"v_exp_f32_e32 v72, v72\n" \
"v_mfma_f32_16x16x32_bf16 v[92:95], a[152:155], a[56:59], v[92:95]\n" \
"v_exp_f32_e32 v73, v73\n" \
"v_mfma_f32_16x16x32_bf16 v[92:95], a[148:151], a[52:55], v[92:95]\n" \
"v_exp_f32_e32 v74, v74\n" \
"v_mfma_f32_16x16x32_bf16 v[92:95], a[144:147], a[48:51], v[92:95]\n" \
"v_cvt_pk_bf16_f32 v108, v72, v73\n" \
"s_waitcnt lgkmcnt(0)\n" \
"v_mfma_f32_16x16x32_bf16 v[128:131], a[160:163], v[96:99], v[128:131]\n" \
"buffer_load_dwordx4 v50, s[8:11], s67 offen offset:3072 sc1 lds\n" \
"v_mfma_f32_16x16x32_bf16 v[128:131], a[176:179], v[100:103], v[128:131]\n" \
"v_exp_f32_e32 v75, v75\n" \
"v_mfma_f32_16x16x32_bf16 v[132:135], a[180:183], v[100:103], v[132:135]\n" \
"v_exp_f32_e32 v76, v76\n" \
"v_mfma_f32_16x16x32_bf16 v[132:135], a[164:167], v[96:99], v[132:135]\n" \
"v_exp_f32_e32 v77, v77\n" \
"v_mfma_f32_16x16x32_bf16 v[136:139], a[168:171], v[96:99], v[136:139]\n" \
"v_exp_f32_e32 v78, v78\n" \
"v_mfma_f32_16x16x32_bf16 v[136:139], a[184:187], v[100:103], v[136:139]\n" \
"v_exp_f32_e32 v79, v79\n" \
"v_mfma_f32_16x16x32_bf16 v[140:143], a[188:191], v[100:103], v[140:143]\n" \
"v_cvt_pk_bf16_f32 v109, v74, v75\n" \
"v_cvt_pk_bf16_f32 v110, v76, v77\n" \
"v_mfma_f32_16x16x32_bf16 v[140:143], a[172:175], v[96:99], v[140:143]\n" \
"v_cvt_pk_bf16_f32 v111, v78, v79\n" \
"s_mov_b32 s64, s14\n" \
"v_smfmac_f32_16x16x64_bf16 a[192:195], a[212:215], v[96:103], v21\n" \
"v_mfma_f32_16x16x32_bf16 v[160:163], a[160:163], v[104:107], v[160:163]\n" \
"v_add_u32_e32 v51, 0x12000, v63\n" \
"s_xor_b32 m0, s65, 0xa000\n" \
"v_mfma_f32_16x16x32_bf16 v[160:163], a[176:179], v[108:111], v[160:163]\n" \
"buffer_load_dwordx4 v51, s[8:11], s64 offen offset:0 sc1 lds\n" \
"v_mfma_f32_16x16x32_bf16 v[164:167], a[180:183], v[108:111], v[164:167]\n" \
"v_exp_f32_e32 v80, v80\n" \
"v_mfma_f32_16x16x32_bf16 v[164:167], a[164:167], v[104:107], v[164:167]\n" \
"v_exp_f32_e32 v81, v81\n" \
"v_mfma_f32_16x16x32_bf16 v[168:171], a[168:171], v[104:107], v[168:171]\n" \
"v_exp_f32_e32 v82, v82\n" \
"v_mfma_f32_16x16x32_bf16 v[168:171], a[184:187], v[108:111], v[168:171]\n" \
"v_exp_f32_e32 v83, v83\n" \
"v_mfma_f32_16x16x32_bf16 v[172:175], a[188:191], v[108:111], v[172:175]\n" \
"v_exp_f32_e32 v84, v84\n" \
"v_mfma_f32_16x16x32_bf16 v[172:175], a[172:175], v[104:107], v[172:175]\n" \
"v_exp_f32_e32 v85, v85\n" \
"v_smfmac_f32_16x16x64_bf16 a[196:199], a[212:215], v[104:111], v21\n" \
"v_mfma_f32_16x16x32_bf16 v[192:195], a[160:163], v[112:115], v[192:195]\n" \
"v_exp_f32_e32 v86, v86\n" \
"v_exp_f32_e32 v87, v87\n" \
"v_cvt_pk_bf16_f32 v116, v80, v81\n" \
"v_cvt_pk_bf16_f32 v117, v82, v83\n" \
"v_cvt_pk_bf16_f32 v118, v84, v85\n" \
"v_cvt_pk_bf16_f32 v119, v86, v87\n" \
"v_exp_f32_e32 v88, v88\n" \
"v_exp_f32_e32 v89, v89\n" \
"v_mfma_f32_16x16x32_bf16 v[192:195], a[176:179], v[116:119], v[192:195]\n" \
"buffer_load_dwordx4 v51, s[8:11], s64 offen offset:1024 sc1 lds\n" \
"v_mfma_f32_16x16x32_bf16 v[196:199], a[180:183], v[116:119], v[196:199]\n" \
"ds_read_b128 a[128:131], v48 offset:0\n" \
"ds_read_b128 a[132:135], v48 offset:1024\n" \
"v_mfma_f32_16x16x32_bf16 v[196:199], a[164:167], v[112:115], v[196:199]\n" \
"ds_read_b128 a[136:139], v48 offset:2048\n" \
"ds_read_b128 a[140:143], v48 offset:3072\n" \
"ds_read_b128 a[144:147], v48 offset:4096\n" \
"ds_read_b128 a[148:151], v48 offset:5120\n" \
"ds_read_b128 a[152:155], v48 offset:6144\n" \
"ds_read_b128 a[156:159], v48 offset:7168\n" \
"v_mfma_f32_16x16x32_bf16 v[200:203], a[168:171], v[112:115], v[200:203]\n" \
"v_exp_f32_e32 v90, v90\n" \
"v_mfma_f32_16x16x32_bf16 v[200:203], a[184:187], v[116:119], v[200:203]\n" \
"v_exp_f32_e32 v91, v91\n" \
"v_mfma_f32_16x16x32_bf16 v[204:207], a[188:191], v[116:119], v[204:207]\n" \
"v_exp_f32_e32 v92, v92\n" \
"v_mfma_f32_16x16x32_bf16 v[204:207], a[172:175], v[112:115], v[204:207]\n" \
"v_exp_f32_e32 v93, v93\n" \
"v_smfmac_f32_16x16x64_bf16 a[200:203], a[212:215], v[112:119], v21\n" \
"v_mfma_f32_16x16x32_bf16 v[224:227], a[160:163], v[120:123], v[224:227]\n" \
"v_exp_f32_e32 v94, v94\n" \
"v_exp_f32_e32 v95, v95\n" \
"v_cvt_pk_bf16_f32 v124, v88, v89\n" \
"v_cvt_pk_bf16_f32 v125, v90, v91\n" \
"v_cvt_pk_bf16_f32 v126, v92, v93\n" \
"v_cvt_pk_bf16_f32 v127, v94, v95\n" \
"v_add_u32_e32 v52, 0x14000, v63\n" \
"v_add_u32_e32 v53, 0x20000, v63\n" \
"v_mfma_f32_16x16x32_bf16 v[224:227], a[176:179], v[124:127], v[224:227]\n" \
"buffer_load_dwordx4 v51, s[8:11], s64 offen offset:2048 sc1 lds\n" \
"v_mfma_f32_16x16x32_bf16 v[228:231], a[180:183], v[124:127], v[228:231]\n" \
"buffer_load_dwordx4 v51, s[8:11], s64 offen offset:3072 sc1 lds\n" \
"v_mfma_f32_16x16x32_bf16 v[228:231], a[164:167], v[120:123], v[228:231]\n" \
"v_readlane_b32 s93, v11, 17\n" \
"v_readlane_b32 s92, v11, 22\n" \
"v_mfma_f32_16x16x32_bf16 v[232:235], a[168:171], v[120:123], v[232:235]\n" \
"s_mov_b32 s66, s19\n" \
"v_mfma_f32_16x16x32_bf16 v[232:235], a[184:187], v[124:127], v[232:235]\n" \
"v_mfma_f32_16x16x32_bf16 v[236:239], a[188:191], v[124:127], v[236:239]\n" \
"v_mfma_f32_16x16x32_bf16 v[236:239], a[172:175], v[120:123], v[236:239]\n" \
"s_waitcnt vmcnt(10) lgkmcnt(0)\n" \
"s_barrier\n" \
"v_mfma_f32_16x16x32_bf16 v[64:67], a[128:131], a[0:3], v[32:35]\n" \
"s_xor_b32 m0, s65, 0x18000\n" \
"ds_read_b128 a[160:163], v49 offset:8192\n" \
"v_mfma_f32_16x16x32_bf16 v[64:67], a[132:135], a[4:7], v[64:67]\n" \
"buffer_load_dwordx4 v52, s[8:11], s64 offen offset:0 sc1 lds\n" \
"v_mfma_f32_16x16x32_bf16 v[64:67], a[136:139], a[8:11], v[64:67]\n" \
"ds_read_b128 a[176:179], v49 offset:12288\n" \
"ds_read_b128 a[180:183], v49 offset:13312\n" \
"v_mfma_f32_16x16x32_bf16 v[64:67], a[140:143], a[12:15], v[64:67]\n" \
"buffer_load_dwordx4 v52, s[8:11], s64 offen offset:1024 sc1 lds\n" \
"v_mfma_f32_16x16x32_bf16 v[68:71], a[156:159], a[12:15], v[32:35]\n" \
"ds_read_b128 a[164:167], v49 offset:9216\n" \
"ds_read_b128 a[168:171], v49 offset:10240\n" \
"v_mfma_f32_16x16x32_bf16 v[68:71], a[152:155], a[8:11], v[68:71]\n" \
"ds_read_b128 a[184:187], v49 offset:14336\n" \
"ds_read_b128 a[188:191], v49 offset:15360\n" \
"v_mfma_f32_16x16x32_bf16 v[68:71], a[148:151], a[4:7], v[68:71]\n" \
"v_exp_f32_e32 v64, v64\n" \
"v_mfma_f32_16x16x32_bf16 v[68:71], a[144:147], a[0:3], v[68:71]\n" \
"buffer_load_dwordx4 v52, s[8:11], s64 offen offset:2048 sc1 lds\n" \
"v_mfma_f32_16x16x32_bf16 v[72:75], a[128:131], a[16:19], v[36:39]\n" \
"v_exp_f32_e32 v65, v65\n" \
"v_mfma_f32_16x16x32_bf16 v[72:75], a[132:135], a[20:23], v[72:75]\n" \
"v_exp_f32_e32 v66, v66\n" \
"v_mfma_f32_16x16x32_bf16 v[72:75], a[136:139], a[24:27], v[72:75]\n" \
"buffer_load_dwordx4 v52, s[8:11], s64 offen offset:3072 sc1 lds\n" \
"L_pf12:\n" \
"s_cbranch_execz L_pf16\n" \
"s_nop 0\n" \
"v_mfma_f32_16x16x32_bf16 v[72:75], a[140:143], a[28:31], v[72:75]\n" \
"s_mov_b32 m0, s65\n" \
"ds_read_b128 a[172:175], v49 offset:11264\n" \
"v_mfma_f32_16x16x32_bf16 v[76:79], a[156:159], a[28:31], v[36:39]\n" \
"v_exp_f32_e32 v67, v67\n" \
"v_mfma_f32_16x16x32_bf16 v[76:79], a[152:155], a[24:27], v[76:79]\n" \
"v_exp_f32_e32 v68, v68\n" \
"v_mfma_f32_16x16x32_bf16 v[76:79], a[148:151], a[20:23], v[76:79]\n" \
"buffer_load_dwordx4 v53, s[8:11], s64 offen offset:0 sc1 lds\n" \
"v_mfma_f32_16x16x32_bf16 v[76:79], a[144:147], a[16:19], v[76:79]\n" \
"v_exp_f32_e32 v69, v69\n" \
"v_mfma_f32_16x16x32_bf16 v[80:83], a[128:131], a[32:35], v[40:43]\n" \
"v_exp_f32_e32 v70, v70\n" \
"v_mfma_f32_16x16x32_bf16 v[80:83], a[132:135], a[36:39], v[80:83]\n" \
"buffer_load_dwordx4 v53, s[8:11], s64 offen offset:1024 sc1 lds\n" \
"v_mfma_f32_16x16x32_bf16 v[80:83], a[136:139], a[40:43], v[80:83]\n" \
"v_exp_f32_e32 v71, v71\n" \
"v_mfma_f32_16x16x32_bf16 v[80:83], a[140:143], a[44:47], v[80:83]\n" \
"v_exp_f32_e32 v72, v72\n" \
"v_mfma_f32_16x16x32_bf16 v[84:87], a[156:159], a[44:47], v[40:43]\n" \
"v_exp_f32_e32 v73, v73\n" \
"v_mfma_f32_16x16x32_bf16 v[84:87], a[152:155], a[40:43], v[84:87]\n" \
"buffer_load_dwordx4 v53, s[8:11], s64 offen offset:2048 sc1 lds\n" \
"v_mfma_f32_16x16x32_bf16 v[84:87], a[148:151], a[36:39], v[84:87]\n" \
"v_exp_f32_e32 v74, v74\n" \
"v_mfma_f32_16x16x32_bf16 v[84:87], a[144:147], a[32:35], v[84:87]\n" \
"v_exp_f32_e32 v75, v75\n" \
"v_mfma_f32_16x16x32_bf16 v[88:91], a[128:131], a[48:51], v[44:47]\n" \
"buffer_load_dwordx4 v53, s[8:11], s64 offen offset:3072 sc1 lds\n" \
"v_mfma_f32_16x16x32_bf16 v[88:91], a[132:135], a[52:55], v[88:91]\n" \
"s_add_u32 m0, s93, 0x0\n" \
"ds_read_b128 a[128:131], v48 offset:8192\n" \
"v_mfma_f32_16x16x32_bf16 v[88:91], a[136:139], a[56:59], v[88:91]\n" \
"ds_read_b128 a[132:135], v48 offset:9216\n" \
"ds_read_b128 a[136:139], v48 offset:10240\n" \
"v_mfma_f32_16x16x32_bf16 v[88:91], a[140:143], a[60:63], v[88:91]\n" \
"ds_read_b128 a[140:143], v48 offset:11264\n" \
"v_add_u32_e32 v49, 0x10000, v48\n" \
"v_mfma_f32_16x16x32_bf16 v[92:95], a[156:159], a[60:63], v[44:47]\n" \
"buffer_load_dwordx4 v0, s[80:83], s92 offen offset:0 lds\n" \
"v_mfma_f32_16x16x32_bf16 v[92:95], a[152:155], a[56:59], v[92:95]\n" \
"ds_read_b128 a[152:155], v48 offset:14336\n" \
"ds_read_b128 a[156:159], v48 offset:15360\n" \
"v_mfma_f32_16x16x32_bf16 v[92:95], a[148:151], a[52:55], v[92:95]\n" \
"ds_read_b128 a[148:151], v48 offset:13312\n" \
"s_xor_b32 s65, s65, 0x8000\n" \
"v_mfma_f32_16x16x32_bf16 v[92:95], a[144:147], a[48:51], v[92:95]\n" \
"ds_read_b128 a[144:147], v48 offset:12288\n" \
"s_waitcnt lgkmcnt(8)\n" \
"v_mfma_f32_16x16x32_bf16 v[144:147], a[160:163], v[96:99], v[144:147]\n" \
"buffer_load_dwordx4 v1, s[80:83], s92 offen offset:1024 lds\n" \
"v_mfma_f32_16x16x32_bf16 v[144:147], a[176:179], v[100:103], v[144:147]\n" \
"v_exp_f32_e32 v76, v76\n" \
"v_mfma_f32_16x16x32_bf16 v[148:151], a[180:183], v[100:103], v[148:151]\n" \
"v_exp_f32_e32 v77, v77\n" \
"v_mfma_f32_16x16x32_bf16 v[148:151], a[164:167], v[96:99], v[148:151]\n" \
"buffer_load_dwordx4 v2, s[80:83], s92 offen offset:2048 lds\n" \
"v_mfma_f32_16x16x32_bf16 v[152:155], a[168:171], v[96:99], v[152:155]\n" \
"v_exp_f32_e32 v78, v78\n" \
"v_mfma_f32_16x16x32_bf16 v[152:155], a[184:187], v[100:103], v[152:155]\n" \
"v_exp_f32_e32 v79, v79\n" \
"v_mfma_f32_16x16x32_bf16 v[156:159], a[188:191], v[100:103], v[156:159]\n" \
"buffer_load_dwordx4 v3, s[80:83], s92 offen offset:3072 lds\n" \
"v_mfma_f32_16x16x32_bf16 v[156:159], a[172:175], v[96:99], v[156:159]\n" \
"s_add_u32 m0, s93, 0x1000\n" \
"v_cvt_pk_bf16_f32 v96, v64, v65\n" \
"v_mfma_f32_16x16x32_bf16 v[176:179], a[160:163], v[104:107], v[176:179]\n" \
"v_exp_f32_e32 v80, v80\n" \
"v_mfma_f32_16x16x32_bf16 v[176:179], a[176:179], v[108:111], v[176:179]\n" \
"v_exp_f32_e32 v81, v81\n" \
"v_mfma_f32_16x16x32_bf16 v[180:183], a[180:183], v[108:111], v[180:183]\n" \
"buffer_load_dwordx4 v4, s[80:83], s92 offen offset:0 lds\n" \
"v_mfma_f32_16x16x32_bf16 v[180:183], a[164:167], v[104:107], v[180:183]\n" \
"v_exp_f32_e32 v82, v82\n" \
"v_mfma_f32_16x16x32_bf16 v[184:187], a[168:171], v[104:107], v[184:187]\n" \
"v_exp_f32_e32 v83, v83\n" \
"v_mfma_f32_16x16x32_bf16 v[184:187], a[184:187], v[108:111], v[184:187]\n" \
"buffer_load_dwordx4 v5, s[80:83], s92 offen offset:1024 lds\n" \
"v_mfma_f32_16x16x32_bf16 v[188:191], a[188:191], v[108:111], v[188:191]\n" \
"v_exp_f32_e32 v84, v84\n" \
"v_mfma_f32_16x16x32_bf16 v[188:191], a[172:175], v[104:107], v[188:191]\n" \
"v_exp_f32_e32 v85, v85\n" \
"v_mfma_f32_16x16x32_bf16 v[208:211], a[160:163], v[112:115], v[208:211]\n" \
"v_exp_f32_e32 v86, v86\n" \
"v_mfma_f32_16x16x32_bf16 v[208:211], a[176:179], v[116:119], v[208:211]\n" \
"buffer_load_dwordx4 v6, s[80:83], s92 offen offset:2048 lds\n" \
"v_mfma_f32_16x16x32_bf16 v[212:215], a[180:183], v[116:119], v[212:215]\n" \
"v_exp_f32_e32 v87, v87\n" \
"v_mfma_f32_16x16x32_bf16 v[212:215], a[164:167], v[112:115], v[212:215]\n" \
"v_exp_f32_e32 v88, v88\n" \
"v_mfma_f32_16x16x32_bf16 v[216:219], a[168:171], v[112:115], v[216:219]\n" \
"buffer_load_dwordx4 v7, s[80:83], s92 offen offset:3072 lds\n" \
"v_mfma_f32_16x16x32_bf16 v[216:219], a[184:187], v[116:119], v[216:219]\n" \
"s_add_u32 m0, s93, 0x2000\n" \
"v_cvt_pk_bf16_f32 v97, v66, v67\n" \
"v_mfma_f32_16x16x32_bf16 v[220:223], a[188:191], v[116:119], v[220:223]\n" \
"v_exp_f32_e32 v89, v89\n" \
"v_mfma_f32_16x16x32_bf16 v[220:223], a[172:175], v[112:115], v[220:223]\n" \
"v_exp_f32_e32 v90, v90\n" \
"v_mfma_f32_16x16x32_bf16 v[240:243], a[160:163], v[120:123], v[240:243]\n" \
"buffer_load_dwordx4 v8, s[80:83], s92 offen offset:0 lds\n" \
"v_mfma_f32_16x16x32_bf16 v[240:243], a[176:179], v[124:127], v[240:243]\n" \
"v_exp_f32_e32 v91, v91\n" \
"v_mfma_f32_16x16x32_bf16 v[244:247], a[180:183], v[124:127], v[244:247]\n" \
"v_cvt_pk_bf16_f32 v98, v68, v69\n" \
"v_cvt_pk_bf16_f32 v99, v70, v71\n" \
"v_mfma_f32_16x16x32_bf16 v[244:247], a[164:167], v[120:123], v[244:247]\n" \
"v_cvt_pk_bf16_f32 v104, v72, v73\n" \
"v_cvt_pk_bf16_f32 v105, v74, v75\n" \
"v_mfma_f32_16x16x32_bf16 v[248:251], a[168:171], v[120:123], v[248:251]\n" \
"v_cvt_pk_bf16_f32 v106, v76, v77\n" \
"v_cvt_pk_bf16_f32 v107, v78, v79\n" \
"v_mfma_f32_16x16x32_bf16 v[248:251], a[184:187], v[124:127], v[248:251]\n" \
"v_cvt_pk_bf16_f32 v112, v80, v81\n" \
"v_cvt_pk_bf16_f32 v113, v82, v83\n" \
"v_mfma_f32_16x16x32_bf16 v[252:255], a[188:191], v[124:127], v[252:255]\n" \
"v_cvt_pk_bf16_f32 v114, v84, v85\n" \
"v_cvt_pk_bf16_f32 v115, v86, v87\n" \
"v_mfma_f32_16x16x32_bf16 v[252:255], a[172:175], v[120:123], v[252:255]\n" \
"v_add_u32_e32 v48, s16, v20\n" \
"s_add_u32 s64, s64, 0x10000\n" \
"v_smfmac_f32_16x16x64_bf16 a[204:207], a[212:215], v[120:127], v21\n" \
"s_waitcnt vmcnt(17) lgkmcnt(0)\n" \
"s_barrier\n" \
"s_mov_b64 exec, 0\n" \
"s_cmp_lg_u32 s15, 0\n" \
"s_cbranch_scc1 L_pf22\n" \
"s_branch L_pf21\n" \
"L_retB:\n" \
"s_mov_b64 exec, -1\n" \
"s_branch L_2_%=\n" \
"L_t2_%=:\n" \
"v_mfma_f32_16x16x32_bf16 v[252:255], a[188:191], v[124:127], v[252:255]\n" \
"v_cvt_pk_bf16_f32 v114, v84, v85\n" \
"v_cvt_pk_bf16_f32 v115, v86, v87\n" \
"v_mfma_f32_16x16x32_bf16 v[252:255], a[172:175], v[120:123], v[252:255]\n" \
"v_xor_b32_e32 v48, 0x8000, v48\n" \
"v_smfmac_f32_16x16x64_bf16 a[204:207], a[212:215], v[120:127], v21\n" \
"s_waitcnt lgkmcnt(0)\n" \
"L_2_%=:\n" \
"v_mfma_f32_16x16x32_bf16 v[64:67], a[128:131], a[64:67], v[32:35]\n" \
"ds_read_b128 a[160:163], v49 offset:0\n" \
"ds_read_b128 a[176:179], v49 offset:4096\n" \
"v_mfma_f32_16x16x32_bf16 v[64:67], a[132:135], a[68:71], v[64:67]\n" \
"v_cvt_pk_bf16_f32 v120, v88, v89\n" \
"v_cvt_pk_bf16_f32 v121, v90, v91\n" \
"v_mfma_f32_16x16x32_bf16 v[64:67], a[136:139], a[72:75], v[64:67]\n" \
"ds_read_b128 a[180:183], v49 offset:5120\n" \
"ds_read_b128 a[164:167], v49 offset:1024\n" \
"v_mfma_f32_16x16x32_bf16 v[64:67], a[140:143], a[76:79], v[64:67]\n" \
"v_exp_f32_e32 v92, v92\n" \
"v_mfma_f32_16x16x32_bf16 v[68:71], a[156:159], a[76:79], v[32:35]\n" \
"ds_read_b128 a[168:171], v49 offset:2048\n" \
"ds_read_b128 a[184:187], v49 offset:6144\n" \
"v_mfma_f32_16x16x32_bf16 v[68:71], a[152:155], a[72:75], v[68:71]\n" \
"v_exp_f32_e32 v93, v93\n" \
"v_mfma_f32_16x16x32_bf16 v[68:71], a[148:151], a[68:71], v[68:71]\n" \
"ds_read_b128 a[188:191], v49 offset:7168\n" \
"ds_read_b128 a[172:175], v49 offset:3072\n" \
"v_mfma_f32_16x16x32_bf16 v[68:71], a[144:147], a[64:67], v[68:71]\n" \
"v_exp_f32_e32 v94, v94\n" \
"v_mfma_f32_16x16x32_bf16 v[72:75], a[128:131], a[80:83], v[36:39]\n" \
"v_exp_f32_e32 v95, v95\n" \
"v_mfma_f32_16x16x32_bf16 v[72:75], a[132:135], a[84:87], v[72:75]\n" \
"v_cvt_pk_bf16_f32 v122, v92, v93\n" \
"v_cvt_pk_bf16_f32 v123, v94, v95\n" \
"v_mfma_f32_16x16x32_bf16 v[72:75], a[136:139], a[88:91], v[72:75]\n" \
"v_exp_f32_e32 v64, v64\n" \
"v_mfma_f32_16x16x32_bf16 v[72:75], a[140:143], a[92:95], v[72:75]\n" \
"v_exp_f32_e32 v65, v65\n" \
"v_mfma_f32_16x16x32_bf16 v[76:79], a[156:159], a[92:95], v[36:39]\n" \
"v_exp_f32_e32 v66, v66\n" \
"v_mfma_f32_16x16x32_bf16 v[76:79], a[152:155], a[88:91], v[76:79]\n" \
"v_exp_f32_e32 v67, v67\n" \
"v_mfma_f32_16x16x32_bf16 v[76:79], a[148:151], a[84:87], v[76:79]\n" \
"v_exp_f32_e32 v68, v68\n" \
"v_mfma_f32_16x16x32_bf16 v[76:79], a[144:147], a[80:83], v[76:79]\n" \
"v_exp_f32_e32 v69, v69\n" \
"v_mfma_f32_16x16x32_bf16 v[80:83], a[128:131], a[96:99], v[40:43]\n" \
"v_exp_f32_e32 v70, v70\n" \
"v_mfma_f32_16x16x32_bf16 v[80:83], a[132:135], a[100:103], v[80:83]\n" \
"v_exp_f32_e32 v71, v71\n" \
"v_mfma_f32_16x16x32_bf16 v[80:83], a[136:139], a[104:107], v[80:83]\n" \
"v_cvt_pk_bf16_f32 v100, v64, v65\n" \
"v_cvt_pk_bf16_f32 v101, v66, v67\n" \
"v_mfma_f32_16x16x32_bf16 v[80:83], a[140:143], a[108:111], v[80:83]\n" \
"v_cvt_pk_bf16_f32 v102, v68, v69\n" \
"v_cvt_pk_bf16_f32 v103, v70, v71\n" \
"v_mfma_f32_16x16x32_bf16 v[84:87], a[156:159], a[108:111], v[40:43]\n" \
"v_exp_f32_e32 v72, v72\n" \
"v_mfma_f32_16x16x32_bf16 v[84:87], a[152:155], a[104:107], v[84:87]\n" \
"v_exp_f32_e32 v73, v73\n" \
"v_mfma_f32_16x16x32_bf16 v[84:87], a[148:151], a[100:103], v[84:87]\n" \
"v_exp_f32_e32 v74, v74\n" \
"v_mfma_f32_16x16x32_bf16 v[84:87], a[144:147], a[96:99], v[84:87]\n" \
"v_exp_f32_e32 v75, v75\n" \
"v_mfma_f32_16x16x32_bf16 v[88:91], a[128:131], a[112:115], v[44:47]\n" \
"v_exp_f32_e32 v76, v76\n" \
"v_mfma_f32_16x16x32_bf16 v[88:91], a[132:135], a[116:119], v[88:91]\n" \
"v_exp_f32_e32 v77, v77\n" \
"v_mfma_f32_16x16x32_bf16 v[88:91], a[136:139], a[120:123], v[88:91]\n" \
"v_exp_f32_e32 v78, v78\n" \
"v_mfma_f32_16x16x32_bf16 v[88:91], a[140:143], a[124:127], v[88:91]\n" \
"v_exp_f32_e32 v79, v79\n" \
"v_mfma_f32_16x16x32_bf16 v[92:95], a[156:159], a[124:127], v[44:47]\n" \
"v_cvt_pk_bf16_f32 v108, v72, v73\n" \
"v_cvt_pk_bf16_f32 v109, v74, v75\n" \
"v_mfma_f32_16x16x32_bf16 v[92:95], a[152:155], a[120:123], v[92:95]\n" \
"v_cvt_pk_bf16_f32 v110, v76, v77\n" \
"v_cvt_pk_bf16_f32 v111, v78, v79\n" \
"v_mfma_f32_16x16x32_bf16 v[92:95], a[148:151], a[116:119], v[92:95]\n" \
"ds_read_b128 a[128:131], v48 offset:0\n" \
"ds_read_b128 a[132:135], v48 offset:1024\n" \
"v_mfma_f32_16x16x32_bf16 v[92:95], a[144:147], a[112:115], v[92:95]\n" \
"s_add_u32 s63, s63, s63\n" \
"s_waitcnt lgkmcnt(2)\n" \
"v_mfma_f32_16x16x32_bf16 v[128:131], a[160:163], v[96:99], v[128:131]\n" \
"ds_read_b128 a[136:139], v48 offset:2048\n" \
"ds_read_b128 a[140:143], v48 offset:3072\n" \
"v_mfma_f32_16x16x32_bf16 v[128:131], a[176:179], v[100:103], v[128:131]\n" \
"v_exp_f32_e32 v80, v80\n" \
"v_mfma_f32_16x16x32_bf16 v[132:135], a[180:183], v[100:103], v[132:135]\n" \
"ds_read_b128 a[144:147], v48 offset:4096\n" \
"ds_read_b128 a[148:151], v48 offset:5120\n" \
"v_mfma_f32_16x16x32_bf16 v[132:135], a[164:167], v[96:99], v[132:135]\n" \
"v_exp_f32_e32 v81, v81\n" \
"v_mfma_f32_16x16x32_bf16 v[136:139], a[168:171], v[96:99], v[136:139]\n" \
"ds_read_b128 a[152:155], v48 offset:6144\n" \
"ds_read_b128 a[156:159], v48 offset:7168\n" \
"v_mfma_f32_16x16x32_bf16 v[136:139], a[184:187], v[100:103], v[136:139]\n" \
"v_exp_f32_e32 v82, v82\n" \
"v_mfma_f32_16x16x32_bf16 v[140:143], a[188:191], v[100:103], v[140:143]\n" \
"v_exp_f32_e32 v83, v83\n" \
"v_mfma_f32_16x16x32_bf16 v[140:143], a[172:175], v[96:99], v[140:143]\n" \
"v_exp_f32_e32 v84, v84\n" \
"v_smfmac_f32_16x16x64_bf16 a[192:195], a[212:215], v[96:103], v21\n" \
"v_mfma_f32_16x16x32_bf16 v[160:163], a[160:163], v[104:107], v[160:163]\n" \
"v_exp_f32_e32 v85, v85\n" \
"v_mfma_f32_16x16x32_bf16 v[160:163], a[176:179], v[108:111], v[160:163]\n" \
"v_exp_f32_e32 v86, v86\n" \
"v_mfma_f32_16x16x32_bf16 v[164:167], a[180:183], v[108:111], v[164:167]\n" \
"v_exp_f32_e32 v87, v87\n" \
"v_mfma_f32_16x16x32_bf16 v[164:167], a[164:167], v[104:107], v[164:167]\n" \
"v_cvt_pk_bf16_f32 v116, v80, v81\n" \
"v_cvt_pk_bf16_f32 v117, v82, v83\n" \
"v_mfma_f32_16x16x32_bf16 v[168:171], a[168:171], v[104:107], v[168:171]\n" \
"v_cvt_pk_bf16_f32 v118, v84, v85\n" \
"v_cvt_pk_bf16_f32 v119, v86, v87\n" \
"v_mfma_f32_16x16x32_bf16 v[168:171], a[184:187], v[108:111], v[168:171]\n" \
"v_exp_f32_e32 v88, v88\n" \
"v_mfma_f32_16x16x32_bf16 v[172:175], a[188:191], v[108:111], v[172:175]\n" \
"v_exp_f32_e32 v89, v89\n" \
"v_mfma_f32_16x16x32_bf16 v[172:175], a[172:175], v[104:107], v[172:175]\n" \
"v_exp_f32_e32 v90, v90\n" \
"v_smfmac_f32_16x16x64_bf16 a[196:199], a[212:215], v[104:111], v21\n" \
"v_mfma_f32_16x16x32_bf16 v[192:195], a[160:163], v[112:115], v[192:195]\n" \
"v_exp_f32_e32 v91, v91\n" \
"v_mfma_f32_16x16x32_bf16 v[192:195], a[176:179], v[116:119], v[192:195]\n" \
"v_exp_f32_e32 v92, v92\n" \
"v_mfma_f32_16x16x32_bf16 v[196:199], a[180:183], v[116:119], v[196:199]\n" \
"v_exp_f32_e32 v93, v93\n" \
"v_mfma_f32_16x16x32_bf16 v[196:199], a[164:167], v[112:115], v[196:199]\n" \
"v_exp_f32_e32 v94, v94\n" \
"v_mfma_f32_16x16x32_bf16 v[200:203], a[168:171], v[112:115], v[200:203]\n" \
"v_exp_f32_e32 v95, v95\n" \
"v_mfma_f32_16x16x32_bf16 v[200:203], a[184:187], v[116:119], v[200:203]\n" \
"v_cvt_pk_bf16_f32 v124, v88, v89\n" \
"v_cvt_pk_bf16_f32 v125, v90, v91\n" \
"v_mfma_f32_16x16x32_bf16 v[204:207], a[188:191], v[116:119], v[204:207]\n" \
"v_cvt_pk_bf16_f32 v126, v92, v93\n" \
"v_cvt_pk_bf16_f32 v127, v94, v95\n" \
"v_mfma_f32_16x16x32_bf16 v[204:207], a[172:175], v[112:115], v[204:207]\n" \
"v_smfmac_f32_16x16x64_bf16 a[200:203], a[212:215], v[112:119], v21\n" \
"s_waitcnt vmcnt(0) lgkmcnt(0)\n" \
"s_barrier\n" \
"v_mfma_f32_16x16x32_bf16 v[224:227], a[160:163], v[120:123], v[224:227]\n" \
"s_cbranch_scc1 L_h2_%=\n" \
"L_hr2_%=:\n" \
"v_mfma_f32_16x16x32_bf16 v[224:227], a[176:179], v[124:127], v[224:227]\n" \
"s_xor_b32 m0, s65, 0x12000\n" \
"ds_read_b128 a[160:163], v49 offset:8192\n" \
"v_mfma_f32_16x16x32_bf16 v[228:231], a[180:183], v[124:127], v[228:231]\n" \
"buffer_load_dwordx4 v50, s[8:11], s64 offen offset:0 sc1 lds\n" \
"v_mfma_f32_16x16x32_bf16 v[228:231], a[164:167], v[120:123], v[228:231]\n" \
"ds_read_b128 a[176:179], v49 offset:12288\n" \
"ds_read_b128 a[180:183], v49 offset:13312\n" \
"v_mfma_f32_16x16x32_bf16 v[232:235], a[168:171], v[120:123], v[232:235]\n" \
"v_mfma_f32_16x16x32_bf16 v[232:235], a[184:187], v[124:127], v[232:235]\n" \
"buffer_load_dwordx4 v50, s[8:11], s64 offen offset:1024 sc1 lds\n" \
"v_mfma_f32_16x16x32_bf16 v[236:239], a[188:191], v[124:127], v[236:239]\n" \
"ds_read_b128 a[164:167], v49 offset:9216\n" \
"ds_read_b128 a[168:171], v49 offset:10240\n" \
"v_mfma_f32_16x16x32_bf16 v[236:239], a[172:175], v[120:123], v[236:239]\n" \
"v_mfma_f32_16x16x32_bf16 v[64:67], a[128:131], a[64:67], v[32:35]\n" \
"ds_read_b128 a[184:187], v49 offset:14336\n" \
"ds_read_b128 a[188:191], v49 offset:15360\n" \
"v_mfma_f32_16x16x32_bf16 v[64:67], a[132:135], a[68:71], v[64:67]\n" \
"buffer_load_dwordx4 v50, s[8:11], s64 offen offset:2048 sc1 lds\n" \
"v_mfma_f32_16x16x32_bf16 v[64:67], a[136:139], a[72:75], v[64:67]\n" \
"ds_read_b128 a[172:175], v49 offset:11264\n" \
"v_add_u32_e32 v49, 0x10000, v48\n" \
"v_mfma_f32_16x16x32_bf16 v[64:67], a[140:143], a[76:79], v[64:67]\n" \
"v_mfma_f32_16x16x32_bf16 v[68:71], a[156:159], a[76:79], v[32:35]\n" \
"v_mfma_f32_16x16x32_bf16 v[68:71], a[152:155], a[72:75], v[68:71]\n" \
"v_mfma_f32_16x16x32_bf16 v[68:71], a[148:151], a[68:71], v[68:71]\n" \
"buffer_load_dwordx4 v50, s[8:11], s64 offen offset:3072 sc1 lds\n" \
"v_mfma_f32_16x16x32_bf16 v[68:71], a[144:147], a[64:67], v[68:71]\n" \
"s_xor_b32 m0, s65, 0xa000\n" \
"v_mfma_f32_16x16x32_bf16 v[72:75], a[128:131], a[80:83], v[36:39]\n" \
"v_mfma_f32_16x16x32_bf16 v[72:75], a[132:135], a[84:87], v[72:75]\n" \
"v_exp_f32_e32 v64, v64\n" \
"v_mfma_f32_16x16x32_bf16 v[72:75], a[136:139], a[88:91], v[72:75]\n" \
"buffer_load_dwordx4 v51, s[8:11], s64 offen offset:0 sc1 lds\n" \
"v_mfma_f32_16x16x32_bf16 v[72:75], a[140:143], a[92:95], v[72:75]\n" \
"v_exp_f32_e32 v65, v65\n" \
"v_mfma_f32_16x16x32_bf16 v[76:79], a[156:159], a[92:95], v[36:39]\n" \
"v_exp_f32_e32 v66, v66\n" \
"v_mfma_f32_16x16x32_bf16 v[76:79], a[152:155], a[88:91], v[76:79]\n" \
"v_exp_f32_e32 v67, v67\n" \
"v_mfma_f32_16x16x32_bf16 v[76:79], a[148:151], a[84:87], v[76:79]\n" \
"buffer_load_dwordx4 v51, s[8:11], s64 offen offset:1024 sc1 lds\n" \
"v_mfma_f32_16x16x32_bf16 v[76:79], a[144:147], a[80:83], v[76:79]\n" \
"v_exp_f32_e32 v68, v68\n" \
"v_mfma_f32_16x16x32_bf16 v[80:83], a[128:131], a[96:99], v[40:43]\n" \
"v_exp_f32_e32 v69, v69\n" \
"v_mfma_f32_16x16x32_bf16 v[80:83], a[132:135], a[100:103], v[80:83]\n" \
"v_exp_f32_e32 v70, v70\n" \
"v_mfma_f32_16x16x32_bf16 v[80:83], a[136:139], a[104:107], v[80:83]\n" \
"v_exp_f32_e32 v71, v71\n" \
"v_mfma_f32_16x16x32_bf16 v[80:83], a[140:143], a[108:111], v[80:83]\n" \
"buffer_load_dwordx4 v51, s[8:11], s64 offen offset:2048 sc1 lds\n" \
"v_mfma_f32_16x16x32_bf16 v[84:87], a[156:159], a[108:111], v[40:43]\n" \
"v_exp_f32_e32 v72, v72\n" \
"v_mfma_f32_16x16x32_bf16 v[84:87], a[152:155], a[104:107], v[84:87]\n" \
"v_exp_f32_e32 v73, v73\n" \
"v_mfma_f32_16x16x32_bf16 v[84:87], a[148:151], a[100:103], v[84:87]\n" \
"v_exp_f32_e32 v74, v74\n" \
"v_mfma_f32_16x16x32_bf16 v[84:87], a[144:147], a[96:99], v[84:87]\n" \
"buffer_load_dwordx4 v51, s[8:11], s64 offen offset:3072 sc1 lds\n" \
"v_mfma_f32_16x16x32_bf16 v[88:91], a[128:131], a[112:115], v[44:47]\n" \
"s_xor_b32 m0, s65, 0x18000\n" \
"ds_read_b128 a[128:131], v48 offset:8192\n" \
"v_mfma_f32_16x16x32_bf16 v[88:91], a[132:135], a[116:119], v[88:91]\n" \
"v_exp_f32_e32 v75, v75\n" \
"v_mfma_f32_16x16x32_bf16 v[88:91], a[136:139], a[120:123], v[88:91]\n" \
"ds_read_b128 a[132:135], v48 offset:9216\n" \
"ds_read_b128 a[136:139], v48 offset:10240\n" \
"v_mfma_f32_16x16x32_bf16 v[88:91], a[140:143], a[124:127], v[88:91]\n" \
"v_exp_f32_e32 v76, v76\n" \
"v_mfma_f32_16x16x32_bf16 v[92:95], a[156:159], a[124:127], v[44:47]\n" \
"buffer_load_dwordx4 v52, s[8:11], s64 offen offset:0 sc1 lds\n" \
"v_mfma_f32_16x16x32_bf16 v[92:95], a[152:155], a[120:123], v[92:95]\n" \
"ds_read_b128 a[140:143], v48 offset:11264\n" \
"ds_read_b128 a[152:155], v48 offset:14336\n" \
"v_mfma_f32_16x16x32_bf16 v[92:95], a[148:151], a[116:119], v[92:95]\n" \
"v_exp_f32_e32 v77, v77\n" \
"v_mfma_f32_16x16x32_bf16 v[92:95], a[144:147], a[112:115], v[92:95]\n" \
"ds_read_b128 a[144:147], v48 offset:12288\n" \
"s_waitcnt lgkmcnt(6)\n" \
"v_mfma_f32_16x16x32_bf16 v[144:147], a[160:163], v[96:99], v[144:147]\n" \
"buffer_load_dwordx4 v52, s[8:11], s64 offen offset:1024 sc1 lds\n" \
"v_mfma_f32_16x16x32_bf16 v[144:147], a[176:179], v[100:103], v[144:147]\n" \
"ds_read_b128 a[148:151], v48 offset:13312\n" \
"ds_read_b128 a[156:159], v48 offset:15360\n" \
"v_mfma_f32_16x16x32_bf16 v[148:151], a[180:183], v[100:103], v[148:151]\n" \
"v_exp_f32_e32 v78, v78\n" \
"v_mfma_f32_16x16x32_bf16 v[148:151], a[164:167], v[96:99], v[148:151]\n" \
"v_exp_f32_e32 v79, v79\n" \
"v_mfma_f32_16x16x32_bf16 v[152:155], a[168:171], v[96:99], v[152:155]\n" \
"buffer_load_dwordx4 v52, s[8:11], s64 offen offset:2048 sc1 lds\n" \
"v_mfma_f32_16x16x32_bf16 v[152:155], a[184:187], v[100:103], v[152:155]\n" \
"v_exp_f32_e32 v80, v80\n" \
"v_mfma_f32_16x16x32_bf16 v[156:159], a[188:191], v[100:103], v[156:159]\n" \
"v_exp_f32_e32 v81, v81\n" \
"v_mfma_f32_16x16x32_bf16 v[156:159], a[172:175], v[96:99], v[156:159]\n" \
"v_exp_f32_e32 v82, v82\n" \
"v_mfma_f32_16x16x32_bf16 v[176:179], a[160:163], v[104:107], v[176:179]\n" \
"v_exp_f32_e32 v83, v83\n" \
"v_mfma_f32_16x16x32_bf16 v[176:179], a[176:179], v[108:111], v[176:179]\n" \
"buffer_load_dwordx4 v52, s[8:11], s64 offen offset:3072 sc1 lds\n" \
"v_mfma_f32_16x16x32_bf16 v[180:183], a[180:183], v[108:111], v[180:183]\n" \
"s_mov_b32 m0, s65\n" \
"s_xor_b32 s65, s65, 0x8000\n" \
"v_mfma_f32_16x16x32_bf16 v[180:183], a[164:167], v[104:107], v[180:183]\n" \
"v_exp_f32_e32 v84, v84\n" \
"v_mfma_f32_16x16x32_bf16 v[184:187], a[168:171], v[104:107], v[184:187]\n" \
"v_exp_f32_e32 v85, v85\n" \
"v_mfma_f32_16x16x32_bf16 v[184:187], a[184:187], v[108:111], v[184:187]\n" \
"buffer_load_dwordx4 v53, s[8:11], s64 offen offset:0 sc1 lds\n" \
"v_mfma_f32_16x16x32_bf16 v[188:191], a[188:191], v[108:111], v[188:191]\n" \
"v_exp_f32_e32 v86, v86\n" \
"v_mfma_f32_16x16x32_bf16 v[188:191], a[172:175], v[104:107], v[188:191]\n" \
"v_exp_f32_e32 v87, v87\n" \
"v_mfma_f32_16x16x32_bf16 v[208:211], a[160:163], v[112:115], v[208:211]\n" \
"v_exp_f32_e32 v88, v88\n" \
"v_mfma_f32_16x16x32_bf16 v[208:211], a[176:179], v[116:119], v[208:211]\n" \
"buffer_load_dwordx4 v53, s[8:11], s64 offen offset:1024 sc1 lds\n" \
"v_mfma_f32_16x16x32_bf16 v[212:215], a[180:183], v[116:119], v[212:215]\n" \
"v_exp_f32_e32 v89, v89\n" \
"v_mfma_f32_16x16x32_bf16 v[212:215], a[164:167], v[112:115], v[212:215]\n" \
"v_exp_f32_e32 v90, v90\n" \
"v_mfma_f32_16x16x32_bf16 v[216:219], a[168:171], v[112:115], v[216:219]\n" \
"v_exp_f32_e32 v91, v91\n" \
"v_mfma_f32_16x16x32_bf16 v[216:219], a[184:187], v[116:119], v[216:219]\n" \
"v_cvt_pk_bf16_f32 v96, v64, v65\n" \
"v_cvt_pk_bf16_f32 v97, v66, v67\n" \
"v_mfma_f32_16x16x32_bf16 v[220:223], a[188:191], v[116:119], v[220:223]\n" \
"buffer_load_dwordx4 v53, s[8:11], s64 offen offset:2048 sc1 lds\n" \
"v_mfma_f32_16x16x32_bf16 v[220:223], a[172:175], v[112:115], v[220:223]\n" \
"v_cvt_pk_bf16_f32 v98, v68, v69\n" \
"v_cvt_pk_bf16_f32 v99, v70, v71\n" \
"v_mfma_f32_16x16x32_bf16 v[240:243], a[160:163], v[120:123], v[240:243]\n" \
"v_cvt_pk_bf16_f32 v104, v72, v73\n" \
"v_cvt_pk_bf16_f32 v105, v74, v75\n" \
"v_mfma_f32_16x16x32_bf16 v[240:243], a[176:179], v[124:127], v[240:243]\n" \
"v_cvt_pk_bf16_f32 v106, v76, v77\n" \
"v_cvt_pk_bf16_f32 v107, v78, v79\n" \
"v_mfma_f32_16x16x32_bf16 v[244:247], a[180:183], v[124:127], v[244:247]\n" \
"buffer_load_dwordx4 v53, s[8:11], s64 offen offset:3072 sc1 lds\n" \
"v_mfma_f32_16x16x32_bf16 v[244:247], a[164:167], v[120:123], v[244:247]\n" \
"s_add_u32 s64, s64, 0x10000\n" \
"s_sub_u32 s66, s66, 1\n" \
"v_mfma_f32_16x16x32_bf16 v[248:251], a[168:171], v[120:123], v[248:251]\n" \
"v_cvt_pk_bf16_f32 v112, v80, v81\n" \
"v_cvt_pk_bf16_f32 v113, v82, v83\n" \
"v_mfma_f32_16x16x32_bf16 v[248:251], a[184:187], v[124:127], v[248:251]\n" \
"s_cbranch_scc0 L_t2_%=\n" \
"v_mfma_f32_16x16x32_bf16 v[252:255], a[188:191], v[124:127], v[252:255]\n" \
"v_cvt_pk_bf16_f32 v114, v84, v85\n" \
"v_cvt_pk_bf16_f32 v115, v86, v87\n" \
"v_mfma_f32_16x16x32_bf16 v[252:255], a[172:175], v[120:123], v[252:255]\n" \
"v_xor_b32_e32 v48, 0x8000, v48\n" \
"v_smfmac_f32_16x16x64_bf16 a[204:207], a[212:215], v[120:127], v21\n" \
"s_waitcnt lgkmcnt(0)\n" \
"s_waitcnt vmcnt(12)\n" \
"s_barrier\n" \
"s_cmp_lg_u32 s16, 0\n" \
"s_cbranch_scc1 L_f1_%=\n" \
"ds_read_b128 a[160:163], v49 offset:0\n" \
"ds_read_b128 a[164:167], v49 offset:1024\n" \
"ds_read_b128 a[168:171], v49 offset:2048\n" \
"ds_read_b128 a[172:175], v49 offset:3072\n" \
"ds_read_b128 a[176:179], v49 offset:8192\n" \
"ds_read_b128 a[180:183], v49 offset:9216\n" \
"v_cvt_pk_bf16_f32 v120, v88, v89\n" \
"v_cvt_pk_bf16_f32 v121, v90, v91\n" \
"v_mfma_f32_16x16x32_bf16 v[88:91], a[128:131], a[112:115], v[44:47]\n" \
"s_add_u32 s70, s61, 0x0\n" \
"s_add_u32 s71, s61, 0x49000\n" \
"ds_read_b128 a[184:187], v49 offset:10240\n" \
"v_mfma_f32_16x16x32_bf16 v[88:91], a[132:135], a[116:119], v[88:91]\n" \
"ds_read_b128 a[188:191], v49 offset:11264\n" \
"v_add_u32_e32 v54, 0x49000, v20\n" \
"v_mfma_f32_16x16x32_bf16 v[88:91], a[136:139], a[120:123], v[88:91]\n" \
"v_exp_f32_e32 v92, v92\n" \
"v_mfma_f32_16x16x32_bf16 v[88:91], a[140:143], a[124:127], v[88:91]\n" \
"v_exp_f32_e32 v93, v93\n" \
"v_exp_f32_e32 v94, v94\n" \
"v_exp_f32_e32 v95, v95\n" \
"v_cvt_pk_bf16_f32 v122, v92, v93\n" \
"v_cvt_pk_bf16_f32 v123, v94, v95\n" \
"v_mfma_f32_16x16x32_bf16 v[92:95], a[156:159], a[124:127], v[44:47]\n" \
"buffer_load_dwordx4 a[220:223], v20, s[56:59], s60 offen offset:0 sc1\n" \
"v_mfma_f32_16x16x32_bf16 v[92:95], a[152:155], a[120:123], v[92:95]\n" \
"v_readlane_b32 s97, v11, 32\n" \
"v_readlane_b32 s98, v11, 33\n" \
"v_mfma_f32_16x16x32_bf16 v[92:95], a[148:151], a[116:119], v[92:95]\n" \
"v_and_b32_e32 v112, v12, v112\n" \
"v_and_b32_e32 v113, v13, v113\n" \
"v_mfma_f32_16x16x32_bf16 v[92:95], a[144:147], a[112:115], v[92:95]\n" \
"v_and_b32_e32 v114, v14, v114\n" \
"s_waitcnt lgkmcnt(7)\n" \
"v_mfma_f32_16x16x32_bf16 v[128:131], a[160:163], v[96:99], v[128:131]\n" \
"v_and_b32_e32 v115, v15, v115\n" \
"s_waitcnt lgkmcnt(6)\n" \
"v_mfma_f32_16x16x32_bf16 v[132:135], a[164:167], v[96:99], v[132:135]\n" \
"ds_read_b32 v60, v18 offset:0\n" \
"s_waitcnt lgkmcnt(6)\n" \
"v_mfma_f32_16x16x32_bf16 v[136:139], a[168:171], v[96:99], v[136:139]\n" \
"ds_read_b32 v61, v18 offset:128\n" \
"s_waitcnt lgkmcnt(6)\n" \
"v_mfma_f32_16x16x32_bf16 v[140:143], a[172:175], v[96:99], v[140:143]\n" \
"ds_read_b128 a[0:3], v49 offset:4096\n" \
"s_waitcnt lgkmcnt(5)\n" \
"v_mfma_f32_16x16x32_bf16 v[144:147], a[176:179], v[96:99], v[144:147]\n" \
"buffer_load_dwordx4 a[236:239], v54, s[56:59], s60 offen offset:0 sc1\n" \
"v_mfma_f32_16x16x32_bf16 v[148:151], a[180:183], v[96:99], v[148:151]\n" \
"ds_read_b128 a[4:7], v49 offset:5120\n" \
"s_waitcnt lgkmcnt(5)\n" \
"v_mfma_f32_16x16x32_bf16 v[152:155], a[184:187], v[96:99], v[152:155]\n" \
"ds_read_b128 a[8:11], v49 offset:6144\n" \
"s_waitcnt lgkmcnt(5)\n" \
"v_mfma_f32_16x16x32_bf16 v[156:159], a[188:191], v[96:99], v[156:159]\n" \
"buffer_load_dwordx4 a[224:227], v20, s[56:59], s60 offen offset:1024 sc1\n" \
"v_mfma_f32_16x16x32_bf16 a[192:195], a[252:255], v[96:99], a[192:195]\n" \
"ds_read_b128 a[12:15], v49 offset:7168\n" \
"ds_read_b128 a[16:19], v49 offset:12288\n" \
"v_mfma_f32_16x16x32_bf16 v[160:163], a[160:163], v[104:107], v[160:163]\n" \
"ds_read_b128 a[20:23], v49 offset:13312\n" \
"ds_read_b128 a[24:27], v49 offset:14336\n" \
"buffer_load_dwordx4 a[240:243], v54, s[56:59], s60 offen offset:1024 sc1\n" \
"v_mfma_f32_16x16x32_bf16 v[164:167], a[164:167], v[104:107], v[164:167]\n" \
"ds_read_b128 a[28:31], v49 offset:15360\n" \
"v_accvgpr_read_b32 v40, a192\n" \
"v_mfma_f32_16x16x32_bf16 v[168:171], a[168:171], v[104:107], v[168:171]\n" \
"v_accvgpr_read_b32 v23, a193\n" \
"v_add_f32_e32 v40, v40, v23\n" \
"v_mfma_f32_16x16x32_bf16 v[172:175], a[172:175], v[104:107], v[172:175]\n" \
"buffer_load_dwordx4 a[228:231], v20, s[56:59], s60 offen offset:2048 sc1\n" \
"v_mfma_f32_16x16x32_bf16 v[176:179], a[176:179], v[104:107], v[176:179]\n" \
"v_add_u32_e32 v50, s15, v48\n" \
"v_add_u32_e32 v51, 0x10000, v50\n" \
"v_mfma_f32_16x16x32_bf16 v[180:183], a[180:183], v[104:107], v[180:183]\n" \
"s_waitcnt vmcnt(5)\n" \
"ds_write_b128 v50, v[128:131] offset:0\n" \
"buffer_load_dwordx4 a[244:247], v54, s[56:59], s60 offen offset:2048 sc1\n" \
"v_mfma_f32_16x16x32_bf16 v[184:187], a[184:187], v[104:107], v[184:187]\n" \
"ds_write_b128 v50, v[132:135] offset:1024\n" \
"v_mfma_f32_16x16x32_bf16 v[188:191], a[188:191], v[104:107], v[188:191]\n" \
"ds_write_b128 v50, v[136:139] offset:2048\n" \
"buffer_load_dwordx4 a[232:235], v20, s[56:59], s60 offen offset:3072 sc1\n" \
"v_mfma_f32_16x16x32_bf16 a[196:199], a[252:255], v[104:107], a[196:199]\n" \
"ds_write_b128 v50, v[140:143] offset:3072\n" \
"v_mfma_f32_16x16x32_bf16 v[192:195], a[160:163], v[112:115], v[192:195]\n" \
"ds_write_b128 v50, v[144:147] offset:8192\n" \
"buffer_load_dwordx4 a[248:251], v54, s[56:59], s60 offen offset:3072 sc1\n" \
"v_mfma_f32_16x16x32_bf16 v[196:199], a[164:167], v[112:115], v[196:199]\n" \
"ds_write_b128 v50, v[148:151] offset:9216\n" \
"v_mfma_f32_16x16x32_bf16 v[200:203], a[168:171], v[112:115], v[200:203]\n" \
"ds_write_b128 v50, v[152:155] offset:10240\n" \
"buffer_load_dwordx4 a[32:35], v20, s[56:59], s97 offen offset:0 sc1\n" \
"v_mfma_f32_16x16x32_bf16 v[204:207], a[172:175], v[112:115], v[204:207]\n" \
"v_accvgpr_read_b32 v41, a196\n" \
"v_accvgpr_read_b32 v23, a197\n" \
"v_mfma_f32_16x16x32_bf16 v[208:211], a[176:179], v[112:115], v[208:211]\n" \
"v_add_f32_e32 v41, v41, v23\n" \
"v_xor_b32_e32 v52, 0x4000, v50\n" \
"v_mfma_f32_16x16x32_bf16 v[212:215], a[180:183], v[112:115], v[212:215]\n" \
"buffer_load_dwordx4 a[40:43], v54, s[56:59], s97 offen offset:0 sc1\n" \
"v_mfma_f32_16x16x32_bf16 v[216:219], a[184:187], v[112:115], v[216:219]\n" \
"ds_write_b128 v50, v[156:159] offset:11264\n" \
"v_mfma_f32_16x16x32_bf16 v[220:223], a[188:191], v[112:115], v[220:223]\n" \
"ds_write_b128 v51, v[160:163] offset:0\n" \
"buffer_load_dwordx4 a[48:51], v20, s[56:59], s98 offen offset:0 sc1\n" \
"v_mfma_f32_16x16x32_bf16 a[200:203], a[252:255], v[112:115], a[200:203]\n" \
"ds_write_b128 v51, v[164:167] offset:1024\n" \
"ds_write_b128 v51, v[168:171] offset:2048\n" \
"ds_write_b128 v51, v[172:175] offset:3072\n" \
"buffer_load_dwordx4 a[56:59], v54, s[56:59], s98 offen offset:0 sc1\n" \
"ds_write_b128 v51, v[176:179] offset:8192\n" \
"ds_write_b128 v51, v[180:183] offset:9216\n" \
"ds_write_b128 v51, v[184:187] offset:10240\n" \
"ds_write_b128 v51, v[188:191] offset:11264\n" \
"buffer_load_dwordx4 a[36:39], v20, s[56:59], s97 offen offset:1024 sc1\n" \
"ds_write_b64 v19, v[40:41]\n" \
"v_xor_b32_e32 v53, 0x4000, v51\n" \
"v_xor_b32_e32 v63, 0x400, v19\n" \
"v_readlane_b32 s93, v11, 18\n" \
"v_readlane_b32 s84, v11, 13\n" \
"v_readlane_b32 s85, v11, 14\n" \
"v_readlane_b32 s86, v11, 15\n" \
"s_mov_b32 s87, 0x20000\n" \
"v_readlane_b32 s92, v11, 16\n" \
"s_add_u32 m0, s93, 0x0\n" \
"v_exp_f32_e32 v88, v88\n" \
"v_exp_f32_e32 v89, v89\n" \
"v_exp_f32_e32 v90, v90\n" \
"v_exp_f32_e32 v91, v91\n" \
"v_exp_f32_e32 v92, v92\n" \
"v_exp_f32_e32 v93, v93\n" \
"v_exp_f32_e32 v94, v94\n" \
"v_exp_f32_e32 v95, v95\n" \
"v_cvt_pk_bf16_f32 v124, v88, v89\n" \
"v_cvt_pk_bf16_f32 v125, v90, v91\n" \
"v_cvt_pk_bf16_f32 v126, v92, v93\n" \
"v_cvt_pk_bf16_f32 v127, v94, v95\n" \
"v_and_b32_e32 v124, v12, v124\n" \
"v_and_b32_e32 v125, v13, v125\n" \
"v_and_b32_e32 v126, v14, v126\n" \
"v_and_b32_e32 v127, v15, v127\n" \
"v_accvgpr_read_b32 v56, a200\n" \
"v_accvgpr_read_b32 v23, a201\n" \
"v_add_f32_e32 v56, v56, v23\n" \
"s_waitcnt lgkmcnt(0)\n" \
"s_barrier\n" \
"buffer_load_dwordx4 a[44:47], v54, s[56:59], s97 offen offset:1024 sc1\n" \
"ds_read_b128 v[128:131], v52 offset:0\n" \
"ds_read_b128 v[132:135], v52 offset:1024\n" \
"ds_read_b128 v[136:139], v52 offset:2048\n" \
"ds_read_b128 v[140:143], v52 offset:3072\n" \
"ds_read_b128 v[144:147], v52 offset:8192\n" \
"L_pf13:\n" \
"s_cbranch_execz L_pf17\n" \
"s_nop 0\n" \
"ds_read_b128 v[148:151], v52 offset:9216\n" \
"ds_read_b128 v[152:155], v52 offset:10240\n" \
"ds_read_b128 v[156:159], v52 offset:11264\n" \
"buffer_load_dwordx4 a[52:55], v20, s[56:59], s98 offen offset:1024 sc1\n" \
"ds_read_b128 v[160:163], v53 offset:0\n" \
"ds_read_b128 v[164:167], v53 offset:1024\n" \
"ds_read_b128 v[168:171], v53 offset:2048\n" \
"ds_read_b128 v[172:175], v53 offset:3072\n" \
"ds_read_b128 v[176:179], v53 offset:8192\n" \
"ds_read_b128 v[180:183], v53 offset:9216\n" \
"ds_read_b128 v[184:187], v53 offset:10240\n" \
"ds_read_b128 v[188:191], v53 offset:11264\n" \
"buffer_load_dwordx4 a[60:63], v54, s[56:59], s98 offen offset:1024 sc1\n" \
"ds_read_b64 v[42:43], v63\n" \
"v_mfma_f32_16x16x32_bf16 v[224:227], a[160:163], v[120:123], v[224:227]\n" \
"v_mfma_f32_16x16x32_bf16 v[228:231], a[164:167], v[120:123], v[228:231]\n" \
"v_mfma_f32_16x16x32_bf16 v[232:235], a[168:171], v[120:123], v[232:235]\n" \
"buffer_load_dwordx4 v0, s[84:87], s92 offen offset:0 lds\n" \
"v_mfma_f32_16x16x32_bf16 v[236:239], a[172:175], v[120:123], v[236:239]\n" \
"v_mfma_f32_16x16x32_bf16 v[240:243], a[176:179], v[120:123], v[240:243]\n" \
"v_mfma_f32_16x16x32_bf16 v[244:247], a[180:183], v[120:123], v[244:247]\n" \
"buffer_load_dwordx4 v1, s[84:87], s92 offen offset:1024 lds\n" \
"v_mfma_f32_16x16x32_bf16 v[248:251], a[184:187], v[120:123], v[248:251]\n" \
"s_waitcnt lgkmcnt(15)\n" \
"v_add_f32_e32 v192, v192, v128\n" \
"v_mfma_f32_16x16x32_bf16 v[252:255], a[188:191], v[120:123], v[252:255]\n" \
"v_add_f32_e32 v193, v193, v129\n" \
"v_add_f32_e32 v194, v194, v130\n" \
"v_mfma_f32_16x16x32_bf16 a[204:207], a[252:255], v[120:123], a[204:207]\n" \
"buffer_load_dwordx4 v2, s[84:87], s92 offen offset:2048 lds\n" \
"v_add_f32_e32 v195, v195, v131\n" \
"s_waitcnt lgkmcnt(1)\n" \
"s_barrier\n" \
"v_mfma_f32_16x16x32_bf16 v[224:227], a[0:3], v[124:127], v[224:227]\n" \
"buffer_load_dwordx4 v3, s[84:87], s92 offen offset:3072 lds\n" \
"s_add_u32 m0, s93, 0x1000\n" \
"v_mfma_f32_16x16x32_bf16 v[228:231], a[4:7], v[124:127], v[228:231]\n" \
"s_waitcnt lgkmcnt(0)\n" \
"v_add_f32_e32 v56, v56, v42\n" \
"v_mfma_f32_16x16x32_bf16 v[232:235], a[8:11], v[124:127], v[232:235]\n" \
"v_max_f32_e32 v62, v62, v56\n" \
"v_add_f32_e32 v196, v196, v132\n" \
"v_mfma_f32_16x16x32_bf16 v[236:239], a[12:15], v[124:127], v[236:239]\n" \
"buffer_load_dwordx4 v4, s[84:87], s92 offen offset:0 lds\n" \
"v_rcp_f32_e32 v56, v56\n" \
"v_mfma_f32_16x16x32_bf16 v[240:243], a[16:19], v[124:127], v[240:243]\n" \
"v_add_f32_e32 v197, v197, v133\n" \
"v_add_f32_e32 v198, v198, v134\n" \
"v_add_f32_e32 v199, v199, v135\n" \
"v_mul_f32_e32 v56, v56, v60\n" \
"v_mul_f32_e32 v192, v56, v192\n" \
"v_mul_f32_e32 v193, v56, v193\n" \
"v_mul_f32_e32 v194, v56, v194\n" \
"v_mul_f32_e32 v195, v56, v195\n" \
"v_mul_f32_e32 v196, v56, v196\n" \
"v_mul_f32_e32 v197, v56, v197\n" \
"v_mul_f32_e32 v198, v56, v198\n" \
"v_mul_f32_e32 v199, v56, v199\n" \
"v_add_f32_e32 v200, v200, v136\n" \
"v_add_f32_e32 v201, v201, v137\n" \
"v_add_f32_e32 v202, v202, v138\n" \
"v_add_f32_e32 v203, v203, v139\n" \
"v_add_f32_e32 v204, v204, v140\n" \
"v_add_f32_e32 v205, v205, v141\n" \
"v_add_f32_e32 v206, v206, v142\n" \
"v_add_f32_e32 v207, v207, v143\n" \
"v_cvt_pk_bf16_f32 v64, v192, v193\n" \
"v_cvt_pk_bf16_f32 v65, v194, v195\n" \
"v_cvt_pk_bf16_f32 v66, v196, v197\n" \
"v_cvt_pk_bf16_f32 v67, v198, v199\n" \
"buffer_store_dwordx4 v[64:67], v20, s[56:59], s70 offen offset:0 sc1\n" \
"v_mfma_f32_16x16x32_bf16 v[244:247], a[20:23], v[124:127], v[244:247]\n" \
"v_mul_f32_e32 v200, v56, v200\n" \
"v_mul_f32_e32 v201, v56, v201\n" \
"v_mul_f32_e32 v202, v56, v202\n" \
"v_mul_f32_e32 v203, v56, v203\n" \
"v_mul_f32_e32 v204, v56, v204\n" \
"v_mul_f32_e32 v205, v56, v205\n" \
"v_mul_f32_e32 v206, v56, v206\n" \
"v_mul_f32_e32 v207, v56, v207\n" \
"v_add_f32_e32 v208, v208, v144\n" \
"v_add_f32_e32 v209, v209, v145\n" \
"v_add_f32_e32 v210, v210, v146\n" \
"v_add_f32_e32 v211, v211, v147\n" \
"v_add_f32_e32 v212, v212, v148\n" \
"v_add_f32_e32 v213, v213, v149\n" \
"v_add_f32_e32 v214, v214, v150\n" \
"v_add_f32_e32 v215, v215, v151\n" \
"v_cvt_pk_bf16_f32 v68, v200, v201\n" \
"v_cvt_pk_bf16_f32 v69, v202, v203\n" \
"v_cvt_pk_bf16_f32 v70, v204, v205\n" \
"v_cvt_pk_bf16_f32 v71, v206, v207\n" \
"buffer_store_dwordx4 v[68:71], v20, s[56:59], s70 offen offset:1024 sc1\n" \
"v_mul_f32_e32 v208, v56, v208\n" \
"v_mul_f32_e32 v209, v56, v209\n" \
"v_mul_f32_e32 v210, v56, v210\n" \
"v_mul_f32_e32 v211, v56, v211\n" \
"v_mul_f32_e32 v212, v56, v212\n" \
"v_mul_f32_e32 v213, v56, v213\n" \
"v_mul_f32_e32 v214, v56, v214\n" \
"v_mul_f32_e32 v215, v56, v215\n" \
"v_add_f32_e32 v216, v216, v152\n" \
"v_add_f32_e32 v217, v217, v153\n" \
"v_add_f32_e32 v218, v218, v154\n" \
"v_add_f32_e32 v219, v219, v155\n" \
"v_add_f32_e32 v220, v220, v156\n" \
"v_add_f32_e32 v221, v221, v157\n" \
"v_add_f32_e32 v222, v222, v158\n" \
"v_add_f32_e32 v223, v223, v159\n" \
"v_cvt_pk_bf16_f32 v72, v208, v209\n" \
"v_cvt_pk_bf16_f32 v73, v210, v211\n" \
"v_cvt_pk_bf16_f32 v74, v212, v213\n" \
"v_cvt_pk_bf16_f32 v75, v214, v215\n" \
"buffer_store_dwordx4 v[72:75], v20, s[56:59], s70 offen offset:2048 sc1\n" \
"v_mfma_f32_16x16x32_bf16 v[248:251], a[24:27], v[124:127], v[248:251]\n" \
"v_mul_f32_e32 v216, v56, v216\n" \
"v_mul_f32_e32 v217, v56, v217\n" \
"v_mul_f32_e32 v218, v56, v218\n" \
"v_mul_f32_e32 v219, v56, v219\n" \
"v_mul_f32_e32 v220, v56, v220\n" \
"v_mul_f32_e32 v221, v56, v221\n" \
"v_mul_f32_e32 v222, v56, v222\n" \
"v_mul_f32_e32 v223, v56, v223\n" \
"v_cvt_pk_bf16_f32 v76, v216, v217\n" \
"v_cvt_pk_bf16_f32 v77, v218, v219\n" \
"v_cvt_pk_bf16_f32 v78, v220, v221\n" \
"v_cvt_pk_bf16_f32 v79, v222, v223\n" \
"buffer_store_dwordx4 v[76:79], v20, s[56:59], s70 offen offset:3072 sc1\n" \
"v_add_f32_e32 v224, v224, v160\n" \
"v_add_f32_e32 v225, v225, v161\n" \
"v_add_f32_e32 v226, v226, v162\n" \
"v_add_f32_e32 v227, v227, v163\n" \
"v_add_f32_e32 v228, v228, v164\n" \
"v_add_f32_e32 v229, v229, v165\n" \
"v_add_f32_e32 v230, v230, v166\n" \
"v_add_f32_e32 v231, v231, v167\n" \
"v_add_f32_e32 v232, v232, v168\n" \
"v_add_f32_e32 v233, v233, v169\n" \
"v_add_f32_e32 v234, v234, v170\n" \
"v_add_f32_e32 v235, v235, v171\n" \
"v_add_f32_e32 v236, v236, v172\n" \
"v_add_f32_e32 v237, v237, v173\n" \
"v_add_f32_e32 v238, v238, v174\n" \
"v_add_f32_e32 v239, v239, v175\n" \
"v_mfma_f32_16x16x32_bf16 v[252:255], a[28:31], v[124:127], v[252:255]\n" \
"buffer_load_dwordx4 v5, s[84:87], s92 offen offset:1024 lds\n" \
"v_add_f32_e32 v240, v240, v176\n" \
"v_add_f32_e32 v241, v241, v177\n" \
"v_add_f32_e32 v242, v242, v178\n" \
"v_add_f32_e32 v243, v243, v179\n" \
"v_add_f32_e32 v244, v244, v180\n" \
"v_add_f32_e32 v245, v245, v181\n" \
"v_add_f32_e32 v246, v246, v182\n" \
"v_add_f32_e32 v247, v247, v183\n" \
"v_add_f32_e32 v248, v248, v184\n" \
"v_add_f32_e32 v249, v249, v185\n" \
"v_add_f32_e32 v250, v250, v186\n" \
"v_add_f32_e32 v251, v251, v187\n" \
"v_add_f32_e32 v252, v252, v188\n" \
"v_add_f32_e32 v253, v253, v189\n" \
"v_add_f32_e32 v254, v254, v190\n" \
"v_add_f32_e32 v255, v255, v191\n" \
"v_mfma_f32_16x16x32_bf16 a[204:207], a[252:255], v[124:127], a[204:207]\n" \
"buffer_load_dwordx4 v6, s[84:87], s92 offen offset:2048 lds\n" \
"v_mfma_f32_16x16x32_bf16 a[208:211], a[216:219], v[64:67], a[208:211]\n" \
"v_mfma_f32_16x16x32_bf16 a[208:211], a[216:219], v[68:71], a[208:211]\n" \
"v_mfma_f32_16x16x32_bf16 a[208:211], a[216:219], v[72:75], a[208:211]\n" \
"buffer_load_dwordx4 v7, s[84:87], s92 offen offset:3072 lds\n" \
"v_mfma_f32_16x16x32_bf16 a[208:211], a[216:219], v[76:79], a[208:211]\n" \
"s_add_u32 m0, s93, 0x2000\n" \
"s_nop 0\n" \
"buffer_load_dwordx4 v8, s[84:87], s92 offen offset:0 lds\n" \
"v_accvgpr_read_b32 v57, a204\n" \
"v_accvgpr_read_b32 v23, a205\n" \
"v_add_f32_e32 v57, v57, v23\n" \
"v_add_f32_e32 v57, v57, v43\n" \
"v_max_f32_e32 v62, v62, v57\n" \
"v_rcp_f32_e32 v57, v57\n" \
"v_cmp_lt_f32_e64 s[72:73], s100, v62\n" \
"v_mul_f32_e32 v57, v57, v61\n" \
"v_mul_f32_e32 v224, v57, v224\n" \
"v_mul_f32_e32 v225, v57, v225\n" \
"v_mul_f32_e32 v226, v57, v226\n" \
"v_mul_f32_e32 v227, v57, v227\n" \
"v_mul_f32_e32 v228, v57, v228\n" \
"v_mul_f32_e32 v229, v57, v229\n" \
"v_mul_f32_e32 v230, v57, v230\n" \
"v_mul_f32_e32 v231, v57, v231\n" \
"v_cvt_pk_bf16_f32 v80, v224, v225\n" \
"v_cvt_pk_bf16_f32 v81, v226, v227\n" \
"v_cvt_pk_bf16_f32 v82, v228, v229\n" \
"v_cvt_pk_bf16_f32 v83, v230, v231\n" \
"buffer_store_dwordx4 v[80:83], v20, s[56:59], s71 offen offset:0 sc1\n" \
"v_mul_f32_e32 v232, v57, v232\n" \
"v_mfma_f32_16x16x32_bf16 a[208:211], a[216:219], v[80:83], a[208:211]\n" \
"v_mul_f32_e32 v233, v57, v233\n" \
"v_mul_f32_e32 v234, v57, v234\n" \
"v_mul_f32_e32 v235, v57, v235\n" \
"v_mul_f32_e32 v236, v57, v236\n" \
"v_mul_f32_e32 v237, v57, v237\n" \
"v_mul_f32_e32 v238, v57, v238\n" \
"v_mul_f32_e32 v239, v57, v239\n" \
"v_cvt_pk_bf16_f32 v84, v232, v233\n" \
"v_cvt_pk_bf16_f32 v85, v234, v235\n" \
"v_cvt_pk_bf16_f32 v86, v236, v237\n" \
"v_cvt_pk_bf16_f32 v87, v238, v239\n" \
"buffer_store_dwordx4 v[84:87], v20, s[56:59], s71 offen offset:1024 sc1\n" \
"v_mul_f32_e32 v240, v57, v240\n" \
"v_mfma_f32_16x16x32_bf16 a[208:211], a[216:219], v[84:87], a[208:211]\n" \
"v_mul_f32_e32 v241, v57, v241\n" \
"v_mul_f32_e32 v242, v57, v242\n" \
"v_mul_f32_e32 v243, v57, v243\n" \
"v_mul_f32_e32 v244, v57, v244\n" \
"v_mul_f32_e32 v245, v57, v245\n" \
"v_mul_f32_e32 v246, v57, v246\n" \
"v_mul_f32_e32 v247, v57, v247\n" \
"v_cvt_pk_bf16_f32 v96, v240, v241\n" \
"v_cvt_pk_bf16_f32 v97, v242, v243\n" \
"v_cvt_pk_bf16_f32 v98, v244, v245\n" \
"v_cvt_pk_bf16_f32 v99, v246, v247\n" \
"buffer_store_dwordx4 v[96:99], v20, s[56:59], s71 offen offset:2048 sc1\n" \
"v_mul_f32_e32 v248, v57, v248\n" \
"v_mfma_f32_16x16x32_bf16 a[208:211], a[216:219], v[96:99], a[208:211]\n" \
"v_mul_f32_e32 v249, v57, v249\n" \
"v_mul_f32_e32 v250, v57, v250\n" \
"v_mul_f32_e32 v251, v57, v251\n" \
"v_mul_f32_e32 v252, v57, v252\n" \
"v_mul_f32_e32 v253, v57, v253\n" \
"v_mul_f32_e32 v254, v57, v254\n" \
"v_mul_f32_e32 v255, v57, v255\n" \
"v_cvt_pk_bf16_f32 v100, v248, v249\n" \
"v_cvt_pk_bf16_f32 v101, v250, v251\n" \
"v_cvt_pk_bf16_f32 v102, v252, v253\n" \
"v_cvt_pk_bf16_f32 v103, v254, v255\n" \
"buffer_store_dwordx4 v[100:103], v20, s[56:59], s71 offen offset:3072 sc1\n" \
"s_nop 0\n" \
"v_mfma_f32_16x16x32_bf16 a[208:211], a[216:219], v[100:103], a[208:211]\n" \
"s_nop 7\n" \
"v_accvgpr_read_b32 v63, a208\n" \
"v_cmp_u_f32_e32 vcc, v63, v63\n" \
"s_nop 0\n" \
"s_or_b64 vcc, s[72:73], vcc\n" \
"s_cbranch_scc0 L_out_%=\n" \
"s_branch L_end_%=\n" \
"L_h1_%=:\n" \
"L_pf14:\n" \
"s_cbranch_execz L_pf18\n" \
"s_nop 0\n" \
"s_cmp_gt_i32 s63, 0\n" \
"s_cbranch_scc1 L_hc1_%=\n" \
"s_cmp_lt_i32 s63, 0\n" \
"s_cbranch_scc1 L_hn1_%=\n" \
"s_mov_b32 exec_hi, 0xf\n" \
"buffer_load_dword v22, v16, s[80:83], 0 offen\n" \
"s_mov_b32 exec_hi, -1\n" \
"s_branch L_hr1_%=\n" \
"L_hn1_%=:\n" \
"buffer_load_dword v22, v17, s[56:59], 0 offen sc1\n" \
"s_branch L_hr1_%=\n" \
"L_hs1_%=:\n" \
"s_sleep 1\n" \
"buffer_load_dword v22, v17, s[56:59], 0 offen sc1\n" \
"s_add_u32 s101, s101, 1\n" \
"s_waitcnt vmcnt(0)\n" \
"s_cmp_gt_u32 s101, 0x100000\n" \
"s_cbranch_scc1 L_hr1_%=\n" \
"L_hc1_%=:\n" \
"v_cmp_ne_u32_e32 vcc, s62, v22\n" \
"s_nop 4\n" \
"s_cbranch_vccnz L_hs1_%=\n" \
"s_branch L_hr1_%=\n" \
"L_h2_%=:\n" \
"s_cmp_gt_i32 s63, 0\n" \
"s_cbranch_scc1 L_hc2_%=\n" \
"s_cmp_lt_i32 s63, 0\n" \
"s_cbranch_scc1 L_hn2_%=\n" \
"s_mov_b32 exec_hi, 0xf\n" \
"buffer_load_dword v22, v16, s[80:83], 0 offen\n" \
"s_mov_b32 exec_hi, -1\n" \
"s_branch L_hr2_%=\n" \
"L_hn2_%=:\n" \
"buffer_load_dword v22, v17, s[56:59], 0 offen sc1\n" \
"s_branch L_hr2_%=\n" \
"L_hs2_%=:\n" \
"s_sleep 1\n" \
"buffer_load_dword v22, v17, s[56:59], 0 offen sc1\n" \
"s_add_u32 s101, s101, 1\n" \
"s_waitcnt vmcnt(0)\n" \
"s_cmp_gt_u32 s101, 0x100000\n" \
"s_cbranch_scc1 L_hr2_%=\n" \
"L_hc2_%=:\n" \
"v_cmp_ne_u32_e32 vcc, s62, v22\n" \
"s_nop 4\n" \
"s_cbranch_vccnz L_hs2_%=\n" \
"s_branch L_hr2_%=\n" \
"L_sa_%=:\n" \
"s_sleep 1\n" \
"buffer_load_dword v37, v38, s[56:59], s64 offen sc1\n" \
"s_add_u32 s101, s101, 1\n" \
"s_waitcnt vmcnt(0)\n" \
"v_cmp_ne_u32_e32 vcc, s62, v37\n" \
"s_cmp_gt_u32 s101, 0x100000\n" \
"s_cbranch_scc1 L_ra_%=\n" \
"s_nop 2\n" \
"s_cbranch_vccnz L_sa_%=\n" \
"s_branch L_ra_%=\n" \
"L_sb_%=:\n" \
"s_sleep 1\n" \
"buffer_load_dword v47, v38, s[56:59], s71 offen sc1\n" \
"s_add_u32 s101, s101, 1\n" \
"s_waitcnt vmcnt(0)\n" \
"v_cmp_ne_u32_e32 vcc, s62, v47\n" \
"s_cmp_gt_u32 s101, 0x100000\n" \
"s_cbranch_scc1 L_rb_%=\n" \
"s_nop 2\n" \
"s_cbranch_vccnz L_sb_%=\n" \
"s_branch L_rb_%=\n" \
"L_sp0_%=:\n" \
"s_sleep 1\n" \
"L_sx0_%=:\n" \
"L_pf15:\n" \
"s_cbranch_execz L_pf19\n" \
"s_nop 0\n" \
"buffer_load_dword v37, v40, s[56:59], s74 offen sc1\n" \
"s_add_u32 s101, s101, 1\n" \
"s_waitcnt vmcnt(0)\n" \
"v_cmp_eq_u32_e32 vcc, s62, v37\n" \
"s_nop 1\n" \
"s_and_b32 s98, vcc_lo, 0xfef\n" \
"s_andn2_b32 s99, s100, vcc_lo\n" \
"s_cselect_b32 s99, 0, 16\n" \
"s_or_b32 s98, s98, s99\n" \
"s_andn2_b32 s98, s98, s84\n" \
"s_cbranch_scc0 L_sn0_%=\n" \
"s_or_b32 s84, s84, s98\n" \
"s_bitcmp1_b32 s98, 0\n" \
"s_cbranch_scc0 L_xs00_%=\n" \
"buffer_load_dwordx4 v[116:119], v20, s[56:59], s63 offen offset:0 sc1\n" \
"L_xs00_%=:\n" \
"s_bitcmp1_b32 s98, 1\n" \
"s_cbranch_scc0 L_xs01_%=\n" \
"buffer_load_dwordx4 v[124:127], v20, s[56:59], s63 offen offset:2048 sc1\n" \
"L_xs01_%=:\n" \
"s_bitcmp1_b32 s98, 2\n" \
"s_cbranch_scc0 L_xs02_%=\n" \
"buffer_load_dwordx4 v[132:135], v38, s[56:59], s63 offen offset:0 sc1\n" \
"L_xs02_%=:\n" \
"s_bitcmp1_b32 s98, 3\n" \
"s_cbranch_scc0 L_xs03_%=\n" \
"buffer_load_dwordx4 v[140:143], v38, s[56:59], s63 offen offset:2048 sc1\n" \
"L_xs03_%=:\n" \
"s_bitcmp1_b32 s98, 4\n" \
"s_cbranch_scc0 L_xs04_%=\n" \
"buffer_load_dwordx4 v[176:179], v20, s[56:59], s75 offen offset:0 sc1\n" \
"buffer_load_dwordx4 v[248:251], v20, s[56:59], s75 offen offset:2048 sc1\n" \
"buffer_load_dwordx4 v[180:183], v38, s[56:59], s75 offen offset:0 sc1\n" \
"buffer_load_dwordx4 v[252:255], v38, s[56:59], s75 offen offset:2048 sc1\n" \
"buffer_load_dwordx4 v[112:115], v47, s[56:59], s75 offen offset:0 sc1\n" \
"buffer_load_dwordx4 v[120:123], v47, s[56:59], s75 offen offset:2048 sc1\n" \
"buffer_load_dwordx4 v[128:131], v54, s[56:59], s75 offen offset:0 sc1\n" \
"buffer_load_dwordx4 v[136:139], v54, s[56:59], s75 offen offset:2048 sc1\n" \
"L_xs04_%=:\n" \
"s_bitcmp1_b32 s98, 5\n" \
"s_cbranch_scc0 L_xs05_%=\n" \
"buffer_load_dwordx4 v[148:151], v47, s[56:59], s63 offen offset:0 sc1\n" \
"buffer_load_dwordx4 v[220:223], v47, s[56:59], s63 offen offset:1024 sc1\n" \
"L_xs05_%=:\n" \
"s_bitcmp1_b32 s98, 6\n" \
"s_cbranch_scc0 L_xs06_%=\n" \
"buffer_load_dwordx4 v[156:159], v47, s[56:59], s63 offen offset:2048 sc1\n" \
"buffer_load_dwordx4 v[228:231], v47, s[56:59], s63 offen offset:3072 sc1\n" \
"L_xs06_%=:\n" \
"s_bitcmp1_b32 s98, 7\n" \
"s_cbranch_scc0 L_xs07_%=\n" \
"buffer_load_dwordx4 v[164:167], v54, s[56:59], s63 offen offset:0 sc1\n" \
"buffer_load_dwordx4 v[236:239], v54, s[56:59], s63 offen offset:1024 sc1\n" \
"L_xs07_%=:\n" \
"s_bitcmp1_b32 s98, 8\n" \
"s_cbranch_scc0 L_xs08_%=\n" \
"buffer_load_dwordx4 v[188:191], v20, s[56:59], s63 offen offset:1024 sc1\n" \
"L_xs08_%=:\n" \
"s_bitcmp1_b32 s98, 9\n" \
"s_cbranch_scc0 L_xs09_%=\n" \
"buffer_load_dwordx4 v[196:199], v20, s[56:59], s63 offen offset:3072 sc1\n" \
"L_xs09_%=:\n" \
"s_bitcmp1_b32 s98, 10\n" \
"s_cbranch_scc0 L_xs010_%=\n" \
"buffer_load_dwordx4 v[204:207], v38, s[56:59], s63 offen offset:1024 sc1\n" \
"L_xs010_%=:\n" \
"s_bitcmp1_b32 s98, 11\n" \
"s_cbranch_scc0 L_xs011_%=\n" \
"buffer_load_dwordx4 v[212:215], v38, s[56:59], s63 offen offset:3072 sc1\n" \
"L_xs011_%=:\n" \
"L_sn0_%=:\n" \
"s_cmp_gt_u32 s101, 0x100000\n" \
"s_cbranch_scc1 L_se0_%=\n" \
"s_andn2_b32 s99, 0xff, s84\n" \
"s_cbranch_scc1 L_sp0_%=\n" \
"L_se0_%=:\n" \
"s_waitcnt vmcnt(0)\n" \
"s_branch L_rx0_%=\n" \
"L_sp1_%=:\n" \
"s_sleep 1\n" \
"L_sx1_%=:\n" \
"buffer_load_dword v37, v40, s[56:59], s74 offen sc1\n" \
"s_add_u32 s101, s101, 1\n" \
"s_waitcnt vmcnt(0)\n" \
"v_cmp_eq_u32_e32 vcc, s62, v37\n" \
"s_nop 1\n" \
"s_and_b32 s98, vcc_lo, 0xf00\n" \
"s_andn2_b32 s98, s98, s84\n" \
"s_cbranch_scc0 L_sn1_%=\n" \
"s_or_b32 s84, s84, s98\n" \
"s_bitcmp1_b32 s98, 8\n" \
"s_cbranch_scc0 L_xs18_%=\n" \
"buffer_load_dwordx4 v[188:191], v20, s[56:59], s63 offen offset:1024 sc1\n" \
"L_xs18_%=:\n" \
"s_bitcmp1_b32 s98, 9\n" \
"s_cbranch_scc0 L_xs19_%=\n" \
"buffer_load_dwordx4 v[196:199], v20, s[56:59], s63 offen offset:3072 sc1\n" \
"L_xs19_%=:\n" \
"s_bitcmp1_b32 s98, 10\n" \
"s_cbranch_scc0 L_xs110_%=\n" \
"buffer_load_dwordx4 v[204:207], v38, s[56:59], s63 offen offset:1024 sc1\n" \
"L_xs110_%=:\n" \
"s_bitcmp1_b32 s98, 11\n" \
"s_cbranch_scc0 L_xs111_%=\n" \
"buffer_load_dwordx4 v[212:215], v38, s[56:59], s63 offen offset:3072 sc1\n" \
"L_xs111_%=:\n" \
"L_sn1_%=:\n" \
"s_cmp_gt_u32 s101, 0x100000\n" \
"s_cbranch_scc1 L_se1_%=\n" \
"s_andn2_b32 s99, 0xf00, s84\n" \
"s_cbranch_scc1 L_sp1_%=\n" \
"L_se1_%=:\n" \
"s_waitcnt vmcnt(0)\n" \
"s_branch L_rx1_%=\n" \
"L_att_stub:\n" \
"s_mov_b32 s101, 0\n" \
"v_add_u32_e32 v32, 0x49000, v20\n" \
"v_readlane_b32 s96, v11, 32\n" \
"v_readlane_b32 s97, v11, 33\n" \
"buffer_load_dwordx4 a[220:223], v20, s[56:59], s60 offen offset:0 sc1\n" \
"buffer_load_dwordx4 a[236:239], v32, s[56:59], s60 offen offset:0 sc1\n" \
"buffer_load_dwordx4 a[224:227], v20, s[56:59], s60 offen offset:1024 sc1\n" \
"buffer_load_dwordx4 a[240:243], v32, s[56:59], s60 offen offset:1024 sc1\n" \
"buffer_load_dwordx4 a[228:231], v20, s[56:59], s60 offen offset:2048 sc1\n" \
"buffer_load_dwordx4 a[244:247], v32, s[56:59], s60 offen offset:2048 sc1\n" \
"buffer_load_dwordx4 a[232:235], v20, s[56:59], s60 offen offset:3072 sc1\n" \
"buffer_load_dwordx4 a[248:251], v32, s[56:59], s60 offen offset:3072 sc1\n" \
"buffer_load_dwordx4 a[32:35], v20, s[56:59], s96 offen offset:0 sc1\n" \
"buffer_load_dwordx4 a[40:43], v32, s[56:59], s96 offen offset:0 sc1\n" \
"buffer_load_dwordx4 a[48:51], v20, s[56:59], s97 offen offset:0 sc1\n" \
"buffer_load_dwordx4 a[56:59], v32, s[56:59], s97 offen offset:0 sc1\n" \
"buffer_load_dwordx4 a[36:39], v20, s[56:59], s96 offen offset:1024 sc1\n" \
"buffer_load_dwordx4 a[44:47], v32, s[56:59], s96 offen offset:1024 sc1\n" \
"buffer_load_dwordx4 a[52:55], v20, s[56:59], s97 offen offset:1024 sc1\n" \
"buffer_load_dwordx4 a[60:63], v32, s[56:59], s97 offen offset:1024 sc1\n" \
"buffer_load_dwordx4 v[64:67], v20, s[56:59], s61 offen offset:0 sc1\n" \
"buffer_load_dwordx4 v[68:71], v20, s[56:59], s61 offen offset:1024 sc1\n" \
"buffer_load_dwordx4 v[72:75], v20, s[56:59], s61 offen offset:2048 sc1\n" \
"buffer_load_dwordx4 v[76:79], v20, s[56:59], s61 offen offset:3072 sc1\n" \
"buffer_load_dwordx4 v[80:83], v32, s[56:59], s61 offen offset:0 sc1\n" \
"buffer_load_dwordx4 v[84:87], v32, s[56:59], s61 offen offset:1024 sc1\n" \
"buffer_load_dwordx4 v[96:99], v32, s[56:59], s61 offen offset:2048 sc1\n" \
"buffer_load_dwordx4 v[100:103], v32, s[56:59], s61 offen offset:3072 sc1\n" \
"s_waitcnt vmcnt(0)\n" \
"s_branch L_out_%=\n" \
"L_sw_%=:\n" \
"L_pf16:\n" \
"s_cbranch_execz L_pf20\n" \
"s_nop 0\n" \
"v_mfma_f32_16x16x32_bf16 v[72:75], a[128:131], a[16:19], v[36:39]\n" \
"s_add_u32 s68, s60, 0xfff6e000\n" \
"s_add_u32 s69, s60, 0xfffb7000\n" \
"s_add_u32 s70, s60, 0x0\n" \
"s_add_u32 s71, s60, 0x49000\n" \
"v_mfma_f32_16x16x32_bf16 v[72:75], a[132:135], a[20:23], v[72:75]\n" \
"s_xor_b32 m0, s65, 0x12000\n" \
"ds_read_b128 a[160:163], v49 offset:0\n" \
"v_mfma_f32_16x16x32_bf16 v[72:75], a[136:139], a[24:27], v[72:75]\n" \
"buffer_load_dwordx4 v50, s[8:11], s64 offen offset:0 sc1 lds\n" \
"v_mfma_f32_16x16x32_bf16 v[72:75], a[140:143], a[28:31], v[72:75]\n" \
"ds_read_b128 a[164:167], v49 offset:1024\n" \
"ds_read_b128 a[168:171], v49 offset:2048\n" \
"v_mfma_f32_16x16x32_bf16 v[76:79], a[156:159], a[28:31], v[36:39]\n" \
"ds_read_b128 a[172:175], v49 offset:3072\n" \
"v_cvt_pk_bf16_f32 v120, v88, v89\n" \
"v_mfma_f32_16x16x32_bf16 v[76:79], a[152:155], a[24:27], v[76:79]\n" \
"buffer_load_dwordx4 v50, s[8:11], s64 offen offset:1024 sc1 lds\n" \
"v_mfma_f32_16x16x32_bf16 v[76:79], a[148:151], a[20:23], v[76:79]\n" \
"ds_read_b128 a[176:179], v49 offset:4096\n" \
"v_cvt_pk_bf16_f32 v121, v90, v91\n" \
"v_mfma_f32_16x16x32_bf16 v[76:79], a[144:147], a[16:19], v[76:79]\n" \
"ds_read_b128 a[180:183], v49 offset:5120\n" \
"s_mov_b32 s65, s13\n" \
"v_mfma_f32_16x16x32_bf16 v[80:83], a[128:131], a[32:35], v[40:43]\n" \
"v_add_u32_e32 v63, s15, v20\n" \
"s_add_u32 s67, s14, 40960\n" \
"v_mfma_f32_16x16x32_bf16 v[80:83], a[132:135], a[36:39], v[80:83]\n" \
"v_add_u32_e32 v50, 0x6000, v63\n" \
"s_xor_b32 m0, s65, 0x8000\n" \
"v_mfma_f32_16x16x32_bf16 v[80:83], a[136:139], a[40:43], v[80:83]\n" \
"v_exp_f32_e32 v92, v92\n" \
"v_mfma_f32_16x16x32_bf16 v[80:83], a[140:143], a[44:47], v[80:83]\n" \
"buffer_load_dwordx4 v50, s[8:11], s67 offen offset:0 sc1 lds\n" \
"v_mfma_f32_16x16x32_bf16 v[84:87], a[156:159], a[44:47], v[40:43]\n" \
"v_exp_f32_e32 v93, v93\n" \
"v_mfma_f32_16x16x32_bf16 v[84:87], a[152:155], a[40:43], v[84:87]\n" \
"v_exp_f32_e32 v94, v94\n" \
"v_mfma_f32_16x16x32_bf16 v[84:87], a[148:151], a[36:39], v[84:87]\n" \
"v_exp_f32_e32 v95, v95\n" \
"v_mfma_f32_16x16x32_bf16 v[84:87], a[144:147], a[32:35], v[84:87]\n" \
"v_cvt_pk_bf16_f32 v122, v92, v93\n" \
"v_cvt_pk_bf16_f32 v123, v94, v95\n" \
"v_mfma_f32_16x16x32_bf16 v[88:91], a[128:131], a[48:51], v[44:47]\n" \
"ds_read_b128 a[184:187], v49 offset:6144\n" \
"ds_read_b128 a[188:191], v49 offset:7168\n" \
"v_mfma_f32_16x16x32_bf16 v[88:91], a[132:135], a[52:55], v[88:91]\n" \
"v_and_b32_e32 v96, v12, v96\n" \
"v_and_b32_e32 v97, v13, v97\n" \
"v_mfma_f32_16x16x32_bf16 v[88:91], a[136:139], a[56:59], v[88:91]\n" \
"v_and_b32_e32 v98, v14, v98\n" \
"v_and_b32_e32 v99, v15, v99\n" \
"v_mfma_f32_16x16x32_bf16 v[88:91], a[140:143], a[60:63], v[88:91]\n" \
"buffer_load_dwordx4 v50, s[8:11], s67 offen offset:1024 sc1 lds\n" \
"v_mfma_f32_16x16x32_bf16 v[92:95], a[156:159], a[60:63], v[44:47]\n" \
"v_exp_f32_e32 v72, v72\n" \
"v_mfma_f32_16x16x32_bf16 v[92:95], a[152:155], a[56:59], v[92:95]\n" \
"v_exp_f32_e32 v73, v73\n" \
"v_mfma_f32_16x16x32_bf16 v[92:95], a[148:151], a[52:55], v[92:95]\n" \
"v_exp_f32_e32 v74, v74\n" \
"v_mfma_f32_16x16x32_bf16 v[92:95], a[144:147], a[48:51], v[92:95]\n" \
"v_cvt_pk_bf16_f32 v108, v72, v73\n" \
"s_waitcnt lgkmcnt(2)\n" \
"v_mfma_f32_16x16x32_bf16 v[128:131], a[160:163], v[96:99], v[128:131]\n" \
"v_exp_f32_e32 v75, v75\n" \
"v_mfma_f32_16x16x32_bf16 v[132:135], a[164:167], v[96:99], v[132:135]\n" \
"v_exp_f32_e32 v76, v76\n" \
"v_mfma_f32_16x16x32_bf16 v[136:139], a[168:171], v[96:99], v[136:139]\n" \
"v_exp_f32_e32 v77, v77\n" \
"v_mfma_f32_16x16x32_bf16 v[140:143], a[172:175], v[96:99], v[140:143]\n" \
"v_exp_f32_e32 v78, v78\n" \
"v_mfma_f32_16x16x32_bf16 a[192:195], a[252:255], v[96:99], a[192:195]\n" \
"v_exp_f32_e32 v79, v79\n" \
"v_mfma_f32_16x16x32_bf16 v[160:163], a[160:163], v[104:107], v[160:163]\n" \
"v_cvt_pk_bf16_f32 v109, v74, v75\n" \
"v_cvt_pk_bf16_f32 v110, v76, v77\n" \
"v_cvt_pk_bf16_f32 v111, v78, v79\n" \
"v_and_b32_e32 v108, v12, v108\n" \
"v_and_b32_e32 v109, v13, v109\n" \
"v_and_b32_e32 v110, v14, v110\n" \
"v_and_b32_e32 v111, v15, v111\n" \
"v_exp_f32_e32 v80, v80\n" \
"v_exp_f32_e32 v81, v81\n" \
"v_mfma_f32_16x16x32_bf16 v[160:163], a[176:179], v[108:111], v[160:163]\n" \
"buffer_load_dwordx4 v50, s[8:11], s67 offen offset:2048 sc1 lds\n" \
"v_mfma_f32_16x16x32_bf16 v[164:167], a[180:183], v[108:111], v[164:167]\n" \
"buffer_load_dwordx4 v50, s[8:11], s67 offen offset:3072 sc1 lds\n" \
"v_mfma_f32_16x16x32_bf16 v[164:167], a[164:167], v[104:107], v[164:167]\n" \
"v_exp_f32_e32 v82, v82\n" \
"v_mfma_f32_16x16x32_bf16 v[168:171], a[168:171], v[104:107], v[168:171]\n" \
"v_cvt_pk_bf16_f32 v116, v80, v81\n" \
"s_waitcnt lgkmcnt(0)\n" \
"v_mfma_f32_16x16x32_bf16 v[168:171], a[184:187], v[108:111], v[168:171]\n" \
"v_exp_f32_e32 v83, v83\n" \
"v_mfma_f32_16x16x32_bf16 v[172:175], a[188:191], v[108:111], v[172:175]\n" \
"v_exp_f32_e32 v84, v84\n" \
"v_mfma_f32_16x16x32_bf16 v[172:175], a[172:175], v[104:107], v[172:175]\n" \
"v_exp_f32_e32 v85, v85\n" \
"v_smfmac_f32_16x16x64_bf16 a[196:199], a[212:215], v[104:111], v21\n" \
"v_mfma_f32_16x16x32_bf16 v[192:195], a[160:163], v[112:115], v[192:195]\n" \
"v_exp_f32_e32 v86, v86\n" \
"v_exp_f32_e32 v87, v87\n" \
"v_cvt_pk_bf16_f32 v117, v82, v83\n" \
"v_cvt_pk_bf16_f32 v118, v84, v85\n" \
"v_cvt_pk_bf16_f32 v119, v86, v87\n" \
"s_mov_b32 s64, s14\n" \
"v_add_u32_e32 v51, 0x12000, v63\n" \
"v_mfma_f32_16x16x32_bf16 v[192:195], a[176:179], v[116:119], v[192:195]\n" \
"s_xor_b32 m0, s65, 0xa000\n" \
"ds_read_b32 v58, v18 offset:512\n" \
"v_mfma_f32_16x16x32_bf16 v[196:199], a[180:183], v[116:119], v[196:199]\n" \
"buffer_load_dwordx4 v51, s[8:11], s64 offen offset:0 sc1 lds\n" \
"v_mfma_f32_16x16x32_bf16 v[196:199], a[164:167], v[112:115], v[196:199]\n" \
"v_exp_f32_e32 v88, v88\n" \
"v_mfma_f32_16x16x32_bf16 v[200:203], a[168:171], v[112:115], v[200:203]\n" \
"v_exp_f32_e32 v89, v89\n" \
"v_mfma_f32_16x16x32_bf16 v[200:203], a[184:187], v[116:119], v[200:203]\n" \
"v_exp_f32_e32 v90, v90\n" \
"v_mfma_f32_16x16x32_bf16 v[204:207], a[188:191], v[116:119], v[204:207]\n" \
"v_exp_f32_e32 v91, v91\n" \
"v_mfma_f32_16x16x32_bf16 v[204:207], a[172:175], v[112:115], v[204:207]\n" \
"v_exp_f32_e32 v92, v92\n" \
"v_smfmac_f32_16x16x64_bf16 a[200:203], a[212:215], v[112:119], v21\n" \
"v_mfma_f32_16x16x32_bf16 v[224:227], a[160:163], v[120:123], v[224:227]\n" \
"v_exp_f32_e32 v93, v93\n" \
"v_exp_f32_e32 v94, v94\n" \
"v_exp_f32_e32 v95, v95\n" \
"v_cvt_pk_bf16_f32 v124, v88, v89\n" \
"v_cvt_pk_bf16_f32 v125, v90, v91\n" \
"v_cvt_pk_bf16_f32 v126, v92, v93\n" \
"v_cvt_pk_bf16_f32 v127, v94, v95\n" \
"v_add_u32_e32 v52, 0x14000, v63\n" \
"v_add_u32_e32 v53, 0x20000, v63\n" \
"v_mfma_f32_16x16x32_bf16 v[224:227], a[176:179], v[124:127], v[224:227]\n" \
"buffer_load_dwordx4 v51, s[8:11], s64 offen offset:1024 sc1 lds\n" \
"v_mfma_f32_16x16x32_bf16 v[228:231], a[180:183], v[124:127], v[228:231]\n" \
"ds_read_b32 v59, v18 offset:640\n" \
"ds_read_b32 v60, v18 offset:768\n" \
"v_mfma_f32_16x16x32_bf16 v[228:231], a[164:167], v[120:123], v[228:231]\n" \
"ds_read_b32 v61, v18 offset:896\n" \
"ds_read_b128 a[128:131], v48 offset:0\n" \
"v_mfma_f32_16x16x32_bf16 v[232:235], a[168:171], v[120:123], v[232:235]\n" \
"ds_read_b128 a[132:135], v48 offset:1024\n" \
"ds_read_b128 a[136:139], v48 offset:2048\n" \
"v_mfma_f32_16x16x32_bf16 v[232:235], a[184:187], v[124:127], v[232:235]\n" \
"ds_read_b128 a[140:143], v48 offset:3072\n" \
"ds_read_b128 a[144:147], v48 offset:4096\n" \
"ds_read_b128 a[148:151], v48 offset:5120\n" \
"ds_read_b128 a[152:155], v48 offset:6144\n" \
"ds_read_b128 a[156:159], v48 offset:7168\n" \
"v_mfma_f32_16x16x32_bf16 v[236:239], a[188:191], v[124:127], v[236:239]\n" \
"buffer_load_dwordx4 v51, s[8:11], s64 offen offset:2048 sc1 lds\n" \
"v_mfma_f32_16x16x32_bf16 v[236:239], a[172:175], v[120:123], v[236:239]\n" \
"buffer_load_dwordx4 v51, s[8:11], s64 offen offset:3072 sc1 lds\n" \
"v_readlane_b32 s93, v11, 17\n" \
"v_readlane_b32 s92, v11, 22\n" \
"v_accvgpr_read_b32 v54, a192\n" \
"v_accvgpr_read_b32 v23, a193\n" \
"v_add_f32_e32 v54, v54, v23\n" \
"v_accvgpr_read_b32 v55, a196\n" \
"v_accvgpr_read_b32 v23, a197\n" \
"v_max_f32_e32 v62, v62, v54\n" \
"v_add_f32_e32 v55, v55, v23\n" \
"v_max_f32_e32 v62, v62, v55\n" \
"v_rcp_f32_e32 v54, v54\n" \
"v_rcp_f32_e32 v55, v55\n" \
"s_waitcnt lgkmcnt(11)\n" \
"v_mul_f32_e32 v54, v54, v58\n" \
"s_waitcnt lgkmcnt(10)\n" \
"v_mul_f32_e32 v55, v55, v59\n" \
"v_mul_f32_e32 v128, v54, v128\n" \
"v_mul_f32_e32 v129, v54, v129\n" \
"v_mul_f32_e32 v136, v54, v136\n" \
"v_mul_f32_e32 v137, v54, v137\n" \
"v_mul_f32_e32 v160, v55, v160\n" \
"v_mul_f32_e32 v161, v55, v161\n" \
"v_mul_f32_e32 v168, v55, v168\n" \
"v_mul_f32_e32 v169, v55, v169\n" \
"v_cvt_pk_bf16_f32 v128, v128, v129\n" \
"v_mul_f32_e32 v130, v54, v130\n" \
"v_mul_f32_e32 v131, v54, v131\n" \
"v_cvt_pk_bf16_f32 v136, v136, v137\n" \
"v_mul_f32_e32 v138, v54, v138\n" \
"v_mul_f32_e32 v139, v54, v139\n" \
"s_waitcnt vmcnt(10) lgkmcnt(0)\n" \
"s_barrier\n" \
"v_mfma_f32_16x16x32_bf16 v[80:83], a[128:131], a[32:35], v[40:43]\n" \
"ds_read_b128 a[160:163], v49 offset:8192\n" \
"ds_read_b128 a[164:167], v49 offset:9216\n" \
"v_mfma_f32_16x16x32_bf16 v[80:83], a[132:135], a[36:39], v[80:83]\n" \
"ds_read_b128 a[168:171], v49 offset:10240\n" \
"ds_read_b128 a[172:175], v49 offset:11264\n" \
"v_mfma_f32_16x16x32_bf16 v[80:83], a[136:139], a[40:43], v[80:83]\n" \
"s_xor_b32 m0, s65, 0x18000\n" \
"ds_read_b128 a[176:179], v49 offset:12288\n" \
"v_mfma_f32_16x16x32_bf16 v[80:83], a[140:143], a[44:47], v[80:83]\n" \
"buffer_load_dwordx4 v52, s[8:11], s64 offen offset:0 sc1 lds\n" \
"v_mfma_f32_16x16x32_bf16 v[84:87], a[156:159], a[44:47], v[40:43]\n" \
"ds_read_b128 a[180:183], v49 offset:13312\n" \
"ds_read_b128 a[184:187], v49 offset:14336\n" \
"v_mfma_f32_16x16x32_bf16 v[84:87], a[152:155], a[40:43], v[84:87]\n" \
"buffer_load_dwordx4 v52, s[8:11], s64 offen offset:1024 sc1 lds\n" \
"v_mfma_f32_16x16x32_bf16 v[84:87], a[148:151], a[36:39], v[84:87]\n" \
"ds_read_b128 a[188:191], v49 offset:15360\n" \
"v_add_u32_e32 v49, 0x10000, v48\n" \
"v_mfma_f32_16x16x32_bf16 v[84:87], a[144:147], a[32:35], v[84:87]\n" \
"buffer_load_dwordx4 v52, s[8:11], s64 offen offset:2048 sc1 lds\n" \
"v_mfma_f32_16x16x32_bf16 v[88:91], a[128:131], a[48:51], v[44:47]\n" \
"ds_read_b128 a[128:131], v48 offset:8192\n" \
"v_cvt_pk_bf16_f32 v160, v160, v161\n" \
"v_mfma_f32_16x16x32_bf16 v[88:91], a[132:135], a[52:55], v[88:91]\n" \
"ds_read_b128 a[132:135], v48 offset:9216\n" \
"v_mul_f32_e32 v162, v55, v162\n" \
"v_mfma_f32_16x16x32_bf16 v[88:91], a[136:139], a[56:59], v[88:91]\n" \
"buffer_load_dwordx4 v52, s[8:11], s64 offen offset:3072 sc1 lds\n" \
"v_mfma_f32_16x16x32_bf16 v[88:91], a[140:143], a[60:63], v[88:91]\n" \
"s_mov_b32 m0, s65\n" \
"ds_read_b128 a[136:139], v48 offset:10240\n" \
"v_mfma_f32_16x16x32_bf16 v[92:95], a[156:159], a[60:63], v[44:47]\n" \
"ds_read_b128 a[140:143], v48 offset:11264\n" \
"ds_read_b128 a[156:159], v48 offset:15360\n" \
"v_mfma_f32_16x16x32_bf16 v[92:95], a[152:155], a[56:59], v[92:95]\n" \
"buffer_load_dwordx4 v53, s[8:11], s64 offen offset:0 sc1 lds\n" \
"v_mfma_f32_16x16x32_bf16 v[92:95], a[148:151], a[52:55], v[92:95]\n" \
"ds_read_b128 a[148:151], v48 offset:13312\n" \
"ds_read_b128 a[152:155], v48 offset:14336\n" \
"v_mfma_f32_16x16x32_bf16 v[92:95], a[144:147], a[48:51], v[92:95]\n" \
"ds_read_b128 a[144:147], v48 offset:12288\n" \
"s_waitcnt lgkmcnt(12)\n" \
"v_mfma_f32_16x16x32_bf16 v[144:147], a[160:163], v[96:99], v[144:147]\n" \
"buffer_load_dwordx4 v53, s[8:11], s64 offen offset:1024 sc1 lds\n" \
"v_mfma_f32_16x16x32_bf16 v[148:151], a[164:167], v[96:99], v[148:151]\n" \
"v_exp_f32_e32 v88, v88\n" \
"v_mfma_f32_16x16x32_bf16 v[152:155], a[168:171], v[96:99], v[152:155]\n" \
"v_exp_f32_e32 v89, v89\n" \
"v_mfma_f32_16x16x32_bf16 v[156:159], a[172:175], v[96:99], v[156:159]\n" \
"v_exp_f32_e32 v90, v90\n" \
"v_mfma_f32_16x16x32_bf16 v[176:179], a[160:163], v[104:107], v[176:179]\n" \
"s_xor_b32 s65, s65, 0x8000\n" \
"s_waitcnt lgkmcnt(8)\n" \
"v_mfma_f32_16x16x32_bf16 v[176:179], a[176:179], v[108:111], v[176:179]\n" \
"buffer_load_dwordx4 v53, s[8:11], s64 offen offset:2048 sc1 lds\n" \
"v_mfma_f32_16x16x32_bf16 v[180:183], a[180:183], v[108:111], v[180:183]\n" \
"v_exp_f32_e32 v91, v91\n" \
"v_mfma_f32_16x16x32_bf16 v[180:183], a[164:167], v[104:107], v[180:183]\n" \
"buffer_load_dwordx4 v53, s[8:11], s64 offen offset:3072 sc1 lds\n" \
"v_mfma_f32_16x16x32_bf16 v[184:187], a[168:171], v[104:107], v[184:187]\n" \
"s_add_u32 m0, s93, 0x0\n" \
"L_pf17:\n" \
"s_cbranch_execz L_retA\n" \
"s_nop 0\n" \
"s_add_u32 s64, s64, 0x10000\n" \
"v_mfma_f32_16x16x32_bf16 v[184:187], a[184:187], v[108:111], v[184:187]\n" \
"v_exp_f32_e32 v92, v92\n" \
"v_mfma_f32_16x16x32_bf16 v[188:191], a[188:191], v[108:111], v[188:191]\n" \
"buffer_load_dwordx4 v0, s[80:83], s92 offen offset:0 lds\n" \
"v_mfma_f32_16x16x32_bf16 v[188:191], a[172:175], v[104:107], v[188:191]\n" \
"v_exp_f32_e32 v93, v93\n" \
"v_mfma_f32_16x16x32_bf16 v[208:211], a[160:163], v[112:115], v[208:211]\n" \
"v_exp_f32_e32 v94, v94\n" \
"v_mfma_f32_16x16x32_bf16 v[208:211], a[176:179], v[116:119], v[208:211]\n" \
"v_exp_f32_e32 v95, v95\n" \
"v_mfma_f32_16x16x32_bf16 v[212:215], a[180:183], v[116:119], v[212:215]\n" \
"buffer_load_dwordx4 v1, s[80:83], s92 offen offset:1024 lds\n" \
"v_mfma_f32_16x16x32_bf16 v[212:215], a[164:167], v[112:115], v[212:215]\n" \
"v_exp_f32_e32 v80, v80\n" \
"v_mfma_f32_16x16x32_bf16 v[216:219], a[168:171], v[112:115], v[216:219]\n" \
"v_exp_f32_e32 v81, v81\n" \
"v_mfma_f32_16x16x32_bf16 v[216:219], a[184:187], v[116:119], v[216:219]\n" \
"buffer_load_dwordx4 v2, s[80:83], s92 offen offset:2048 lds\n" \
"v_mfma_f32_16x16x32_bf16 v[220:223], a[188:191], v[116:119], v[220:223]\n" \
"v_exp_f32_e32 v82, v82\n" \
"v_mfma_f32_16x16x32_bf16 v[220:223], a[172:175], v[112:115], v[220:223]\n" \
"v_exp_f32_e32 v83, v83\n" \
"v_mfma_f32_16x16x32_bf16 v[240:243], a[160:163], v[120:123], v[240:243]\n" \
"buffer_load_dwordx4 v3, s[80:83], s92 offen offset:3072 lds\n" \
"v_mfma_f32_16x16x32_bf16 v[240:243], a[176:179], v[124:127], v[240:243]\n" \
"v_exp_f32_e32 v84, v84\n" \
"v_mfma_f32_16x16x32_bf16 v[244:247], a[180:183], v[124:127], v[244:247]\n" \
"v_exp_f32_e32 v85, v85\n" \
"v_mfma_f32_16x16x32_bf16 v[244:247], a[164:167], v[120:123], v[244:247]\n" \
"v_exp_f32_e32 v86, v86\n" \
"v_mfma_f32_16x16x32_bf16 v[248:251], a[168:171], v[120:123], v[248:251]\n" \
"v_exp_f32_e32 v87, v87\n" \
"v_mfma_f32_16x16x32_bf16 v[248:251], a[184:187], v[124:127], v[248:251]\n" \
"v_cvt_pk_bf16_f32 v112, v80, v81\n" \
"v_cvt_pk_bf16_f32 v113, v82, v83\n" \
"v_mfma_f32_16x16x32_bf16 v[252:255], a[188:191], v[124:127], v[252:255]\n" \
"v_cvt_pk_bf16_f32 v114, v84, v85\n" \
"v_cvt_pk_bf16_f32 v115, v86, v87\n" \
"v_mfma_f32_16x16x32_bf16 v[252:255], a[172:175], v[120:123], v[252:255]\n" \
"v_and_b32_e32 v112, v12, v112\n" \
"v_and_b32_e32 v113, v13, v113\n" \
"v_smfmac_f32_16x16x64_bf16 a[204:207], a[212:215], v[120:127], v21\n" \
"v_cvt_pk_bf16_f32 v120, v88, v89\n" \
"v_cvt_pk_bf16_f32 v121, v90, v91\n" \
"v_cvt_pk_bf16_f32 v122, v92, v93\n" \
"v_cvt_pk_bf16_f32 v123, v94, v95\n" \
"v_and_b32_e32 v114, v14, v114\n" \
"v_and_b32_e32 v115, v15, v115\n" \
"v_add_u32_e32 v48, s16, v20\n" \
"v_mul_f32_e32 v144, v54, v144\n" \
"v_mul_f32_e32 v145, v54, v145\n" \
"v_mul_f32_e32 v152, v54, v152\n" \
"v_mul_f32_e32 v153, v54, v153\n" \
"v_mul_f32_e32 v176, v55, v176\n" \
"v_mul_f32_e32 v177, v55, v177\n" \
"v_mul_f32_e32 v184, v55, v184\n" \
"v_mul_f32_e32 v185, v55, v185\n" \
"v_mul_f32_e32 v163, v55, v163\n" \
"v_cvt_pk_bf16_f32 v168, v168, v169\n" \
"v_mul_f32_e32 v170, v55, v170\n" \
"v_mul_f32_e32 v171, v55, v171\n" \
"v_cvt_pk_bf16_f32 v144, v144, v145\n" \
"v_mul_f32_e32 v146, v54, v146\n" \
"v_mul_f32_e32 v147, v54, v147\n" \
"v_cvt_pk_bf16_f32 v152, v152, v153\n" \
"v_mul_f32_e32 v154, v54, v154\n" \
"v_mul_f32_e32 v155, v54, v155\n" \
"v_cvt_pk_bf16_f32 v176, v176, v177\n" \
"v_mul_f32_e32 v178, v55, v178\n" \
"v_mul_f32_e32 v179, v55, v179\n" \
"v_cvt_pk_bf16_f32 v184, v184, v185\n" \
"v_mul_f32_e32 v186, v55, v186\n" \
"v_mul_f32_e32 v187, v55, v187\n" \
"v_cvt_pk_bf16_f32 v129, v130, v131\n" \
"v_mul_f32_e32 v132, v54, v132\n" \
"v_mul_f32_e32 v133, v54, v133\n" \
"v_mul_f32_e32 v134, v54, v134\n" \
"v_mul_f32_e32 v135, v54, v135\n" \
"v_cvt_pk_bf16_f32 v137, v138, v139\n" \
"v_mul_f32_e32 v140, v54, v140\n" \
"v_mul_f32_e32 v141, v54, v141\n" \
"v_mul_f32_e32 v142, v54, v142\n" \
"v_mul_f32_e32 v143, v54, v143\n" \
"v_cvt_pk_bf16_f32 v161, v162, v163\n" \
"v_mul_f32_e32 v164, v55, v164\n" \
"v_mul_f32_e32 v165, v55, v165\n" \
"v_mul_f32_e32 v166, v55, v166\n" \
"v_mul_f32_e32 v167, v55, v167\n" \
"v_cvt_pk_bf16_f32 v169, v170, v171\n" \
"v_mul_f32_e32 v172, v55, v172\n" \
"v_mul_f32_e32 v173, v55, v173\n" \
"v_mul_f32_e32 v174, v55, v174\n" \
"v_mul_f32_e32 v175, v55, v175\n" \
"v_cvt_pk_bf16_f32 v145, v146, v147\n" \
"v_mul_f32_e32 v148, v54, v148\n" \
"v_mul_f32_e32 v149, v54, v149\n" \
"v_mul_f32_e32 v150, v54, v150\n" \
"v_mul_f32_e32 v151, v54, v151\n" \
"v_cvt_pk_bf16_f32 v153, v154, v155\n" \
"v_mul_f32_e32 v156, v54, v156\n" \
"v_mul_f32_e32 v157, v54, v157\n" \
"v_mul_f32_e32 v158, v54, v158\n" \
"v_mul_f32_e32 v159, v54, v159\n" \
"v_cvt_pk_bf16_f32 v177, v178, v179\n" \
"v_mul_f32_e32 v180, v55, v180\n" \
"v_mul_f32_e32 v181, v55, v181\n" \
"v_mul_f32_e32 v182, v55, v182\n" \
"v_mul_f32_e32 v183, v55, v183\n" \
"v_cvt_pk_bf16_f32 v185, v186, v187\n" \
"v_mul_f32_e32 v188, v55, v188\n" \
"v_mul_f32_e32 v189, v55, v189\n" \
"v_mul_f32_e32 v190, v55, v190\n" \
"v_mul_f32_e32 v191, v55, v191\n" \
"v_cvt_pk_bf16_f32 v130, v132, v133\n" \
"v_cvt_pk_bf16_f32 v131, v134, v135\n" \
"buffer_store_dwordx4 v[128:131], v20, s[56:59], s68 offen offset:0 sc1\n" \
"v_cvt_pk_bf16_f32 v138, v140, v141\n" \
"v_mfma_f32_16x16x32_bf16 a[208:211], a[216:219], v[128:131], a[208:211]\n" \
"v_cvt_pk_bf16_f32 v139, v142, v143\n" \
"buffer_store_dwordx4 v[136:139], v20, s[56:59], s68 offen offset:1024 sc1\n" \
"s_waitcnt vmcnt(14) lgkmcnt(0)\n" \
"s_barrier\n" \
"v_mfma_f32_16x16x32_bf16 v[88:91], a[128:131], a[48:51], v[44:47]\n" \
"s_xor_b32 m0, s65, 0x12000\n" \
"ds_read_b128 a[160:163], v49 offset:0\n" \
"v_mfma_f32_16x16x32_bf16 v[88:91], a[132:135], a[52:55], v[88:91]\n" \
"buffer_load_dwordx4 v50, s[8:11], s64 offen offset:0 sc1 lds\n" \
"v_mfma_f32_16x16x32_bf16 v[88:91], a[136:139], a[56:59], v[88:91]\n" \
"ds_read_b128 a[164:167], v49 offset:1024\n" \
"ds_read_b128 a[168:171], v49 offset:2048\n" \
"v_mfma_f32_16x16x32_bf16 v[88:91], a[140:143], a[60:63], v[88:91]\n" \
"buffer_load_dwordx4 v50, s[8:11], s64 offen offset:1024 sc1 lds\n" \
"v_mfma_f32_16x16x32_bf16 v[92:95], a[156:159], a[60:63], v[44:47]\n" \
"ds_read_b128 a[172:175], v49 offset:3072\n" \
"ds_read_b128 a[176:179], v49 offset:8192\n" \
"v_mfma_f32_16x16x32_bf16 v[92:95], a[152:155], a[56:59], v[92:95]\n" \
"ds_read_b128 a[180:183], v49 offset:9216\n" \
"ds_read_b128 a[184:187], v49 offset:10240\n" \
"v_mfma_f32_16x16x32_bf16 v[92:95], a[148:151], a[52:55], v[92:95]\n" \
"ds_read_b128 a[188:191], v49 offset:11264\n" \
"ds_read_b128 a[0:3], v49 offset:4096\n" \
"v_mfma_f32_16x16x32_bf16 v[92:95], a[144:147], a[48:51], v[92:95]\n" \
"buffer_load_dwordx4 v50, s[8:11], s64 offen offset:2048 sc1 lds\n" \
"v_mfma_f32_16x16x32_bf16 v[32:35], a[220:223], a[64:67], 0\n" \
"ds_read_b128 a[4:7], v49 offset:5120\n" \
"ds_read_b128 a[8:11], v49 offset:6144\n" \
"v_mfma_f32_16x16x32_bf16 v[36:39], a[220:223], a[80:83], 0\n" \
"ds_read_b128 a[12:15], v49 offset:7168\n" \
"ds_read_b128 a[16:19], v49 offset:12288\n" \
"v_mfma_f32_16x16x32_bf16 v[40:43], a[220:223], a[96:99], 0\n" \
"ds_read_b128 a[20:23], v49 offset:13312\n" \
"ds_read_b128 a[24:27], v49 offset:14336\n" \
"v_mfma_f32_16x16x32_bf16 v[44:47], a[220:223], a[112:115], 0\n" \
"buffer_load_dwordx4 v50, s[8:11], s64 offen offset:3072 sc1 lds\n" \
"v_mfma_f32_16x16x32_bf16 v[32:35], a[224:227], a[68:71], v[32:35]\n" \
"s_xor_b32 m0, s65, 0xa000\n" \
"ds_read_b128 a[28:31], v49 offset:15360\n" \
"v_mfma_f32_16x16x32_bf16 v[36:39], a[224:227], a[84:87], v[36:39]\n" \
"v_exp_f32_e32 v88, v88\n" \
"v_mfma_f32_16x16x32_bf16 v[40:43], a[224:227], a[100:103], v[40:43]\n" \
"buffer_load_dwordx4 v51, s[8:11], s64 offen offset:0 sc1 lds\n" \
"v_mfma_f32_16x16x32_bf16 v[44:47], a[224:227], a[116:119], v[44:47]\n" \
"v_exp_f32_e32 v89, v89\n" \
"v_mfma_f32_16x16x32_bf16 v[32:35], a[228:231], a[72:75], v[32:35]\n" \
"v_exp_f32_e32 v90, v90\n" \
"v_mfma_f32_16x16x32_bf16 v[36:39], a[228:231], a[88:91], v[36:39]\n" \
"v_exp_f32_e32 v91, v91\n" \
"v_mfma_f32_16x16x32_bf16 v[40:43], a[228:231], a[104:107], v[40:43]\n" \
"buffer_load_dwordx4 v51, s[8:11], s64 offen offset:1024 sc1 lds\n" \
"v_mfma_f32_16x16x32_bf16 v[44:47], a[228:231], a[120:123], v[44:47]\n" \
"v_exp_f32_e32 v92, v92\n" \
"v_mfma_f32_16x16x32_bf16 v[32:35], a[232:235], a[76:79], v[32:35]\n" \
"v_exp_f32_e32 v93, v93\n" \
"v_mfma_f32_16x16x32_bf16 v[36:39], a[232:235], a[92:95], v[36:39]\n" \
"v_exp_f32_e32 v94, v94\n" \
"v_mfma_f32_16x16x32_bf16 v[40:43], a[232:235], a[108:111], v[40:43]\n" \
"buffer_load_dwordx4 v51, s[8:11], s64 offen offset:2048 sc1 lds\n" \
"v_mfma_f32_16x16x32_bf16 v[44:47], a[232:235], a[124:127], v[44:47]\n" \
"v_cvt_pk_bf16_f32 v124, v88, v89\n" \
"s_waitcnt lgkmcnt(3)\n" \
"v_mfma_f32_16x16x32_bf16 v[192:195], a[160:163], v[112:115], v[192:195]\n" \
"v_exp_f32_e32 v95, v95\n" \
"v_mfma_f32_16x16x32_bf16 v[196:199], a[164:167], v[112:115], v[196:199]\n" \
"buffer_load_dwordx4 v51, s[8:11], s64 offen offset:3072 sc1 lds\n" \
"v_mfma_f32_16x16x32_bf16 v[200:203], a[168:171], v[112:115], v[200:203]\n" \
"s_add_u32 m0, s93, 0x1000\n" \
"v_cvt_pk_bf16_f32 v125, v90, v91\n" \
"v_mfma_f32_16x16x32_bf16 v[204:207], a[172:175], v[112:115], v[204:207]\n" \
"v_cvt_pk_bf16_f32 v126, v92, v93\n" \
"v_cvt_pk_bf16_f32 v127, v94, v95\n" \
"v_mfma_f32_16x16x32_bf16 v[208:211], a[176:179], v[112:115], v[208:211]\n" \
"v_and_b32_e32 v124, v12, v124\n" \
"v_and_b32_e32 v125, v13, v125\n" \
"v_mfma_f32_16x16x32_bf16 v[212:215], a[180:183], v[112:115], v[212:215]\n" \
"buffer_load_dwordx4 v4, s[80:83], s92 offen offset:0 lds\n" \
"v_mfma_f32_16x16x32_bf16 v[216:219], a[184:187], v[112:115], v[216:219]\n" \
"v_and_b32_e32 v126, v14, v126\n" \
"v_and_b32_e32 v127, v15, v127\n" \
"v_mfma_f32_16x16x32_bf16 v[220:223], a[188:191], v[112:115], v[220:223]\n" \
"ds_read_b128 a[128:131], v48 offset:0\n" \
"ds_read_b128 a[132:135], v48 offset:1024\n" \
"v_mfma_f32_16x16x32_bf16 a[200:203], a[252:255], v[112:115], a[200:203]\n" \
"ds_read_b128 a[136:139], v48 offset:2048\n" \
"ds_read_b128 a[140:143], v48 offset:3072\n" \
"v_mfma_f32_16x16x32_bf16 v[224:227], a[160:163], v[120:123], v[224:227]\n" \
"buffer_load_dwordx4 v5, s[80:83], s92 offen offset:1024 lds\n" \
"v_mfma_f32_16x16x32_bf16 v[228:231], a[164:167], v[120:123], v[228:231]\n" \
"ds_read_b128 a[144:147], v48 offset:4096\n" \
"ds_read_b128 a[148:151], v48 offset:5120\n" \
"v_mfma_f32_16x16x32_bf16 v[232:235], a[168:171], v[120:123], v[232:235]\n" \
"ds_read_b128 a[152:155], v48 offset:6144\n" \
"ds_read_b128 a[156:159], v48 offset:7168\n" \
"v_mfma_f32_16x16x32_bf16 v[236:239], a[172:175], v[120:123], v[236:239]\n" \
"buffer_load_dwordx4 v6, s[80:83], s92 offen offset:2048 lds\n" \
"v_mfma_f32_16x16x32_bf16 v[240:243], a[176:179], v[120:123], v[240:243]\n" \
"v_cvt_pk_bf16_f32 v162, v164, v165\n" \
"v_cvt_pk_bf16_f32 v163, v166, v167\n" \
"buffer_store_dwordx4 v[160:163], v20, s[56:59], s69 offen offset:0 sc1\n" \
"v_mfma_f32_16x16x32_bf16 v[244:247], a[180:183], v[120:123], v[244:247]\n" \
"v_cvt_pk_bf16_f32 v170, v172, v173\n" \
"v_cvt_pk_bf16_f32 v171, v174, v175\n" \
"buffer_store_dwordx4 v[168:171], v20, s[56:59], s69 offen offset:1024 sc1\n" \
"v_mfma_f32_16x16x32_bf16 v[248:251], a[184:187], v[120:123], v[248:251]\n" \
"v_cvt_pk_bf16_f32 v146, v148, v149\n" \
"v_cvt_pk_bf16_f32 v147, v150, v151\n" \
"buffer_store_dwordx4 v[144:147], v20, s[56:59], s68 offen offset:2048 sc1\n" \
"v_mfma_f32_16x16x32_bf16 v[252:255], a[188:191], v[120:123], v[252:255]\n" \
"buffer_load_dwordx4 v7, s[80:83], s92 offen offset:3072 lds\n" \
"v_mfma_f32_16x16x32_bf16 a[204:207], a[252:255], v[120:123], a[204:207]\n" \
"v_cvt_pk_bf16_f32 v154, v156, v157\n" \
"v_cvt_pk_bf16_f32 v155, v158, v159\n" \
"buffer_store_dwordx4 v[152:155], v20, s[56:59], s68 offen offset:3072 sc1\n" \
"v_mfma_f32_16x16x32_bf16 v[224:227], a[0:3], v[124:127], v[224:227]\n" \
"v_cvt_pk_bf16_f32 v178, v180, v181\n" \
"v_cvt_pk_bf16_f32 v179, v182, v183\n" \
"buffer_store_dwordx4 v[176:179], v20, s[56:59], s69 offen offset:2048 sc1\n" \
"v_mfma_f32_16x16x32_bf16 v[228:231], a[4:7], v[124:127], v[228:231]\n" \
"v_cvt_pk_bf16_f32 v186, v188, v189\n" \
"v_cvt_pk_bf16_f32 v187, v190, v191\n" \
"buffer_store_dwordx4 v[184:187], v20, s[56:59], s69 offen offset:3072 sc1\n" \
"v_mfma_f32_16x16x32_bf16 v[232:235], a[8:11], v[124:127], v[232:235]\n" \
"s_add_u32 m0, s93, 0x2000\n" \
"v_accvgpr_read_b32 v56, a200\n" \
"v_mfma_f32_16x16x32_bf16 v[236:239], a[12:15], v[124:127], v[236:239]\n" \
"buffer_load_dwordx4 v8, s[80:83], s92 offen offset:0 lds\n" \
"v_mfma_f32_16x16x32_bf16 v[240:243], a[16:19], v[124:127], v[240:243]\n" \
"v_accvgpr_read_b32 v23, a201\n" \
"s_waitcnt lgkmcnt(0)\n" \
"v_mfma_f32_16x16x32_bf16 v[244:247], a[20:23], v[124:127], v[244:247]\n" \
"v_add_f32_e32 v56, v56, v23\n" \
"v_max_f32_e32 v62, v62, v56\n" \
"v_mfma_f32_16x16x32_bf16 v[248:251], a[24:27], v[124:127], v[248:251]\n" \
"v_rcp_f32_e32 v56, v56\n" \
"v_mfma_f32_16x16x32_bf16 v[252:255], a[28:31], v[124:127], v[252:255]\n" \
"v_mul_f32_e32 v56, v56, v60\n" \
"v_mul_f32_e32 v192, v56, v192\n" \
"v_mfma_f32_16x16x32_bf16 a[204:207], a[252:255], v[124:127], a[204:207]\n" \
"v_mul_f32_e32 v193, v56, v193\n" \
"v_cvt_pk_bf16_f32 v192, v192, v193\n" \
"v_mfma_f32_16x16x32_bf16 a[208:211], a[216:219], v[136:139], a[208:211]\n" \
"v_mul_f32_e32 v194, v56, v194\n" \
"v_mul_f32_e32 v195, v56, v195\n" \
"v_mfma_f32_16x16x32_bf16 a[208:211], a[216:219], v[160:163], a[208:211]\n" \
"v_cvt_pk_bf16_f32 v193, v194, v195\n" \
"v_mul_f32_e32 v196, v56, v196\n" \
"v_mfma_f32_16x16x32_bf16 a[208:211], a[216:219], v[168:171], a[208:211]\n" \
"v_mul_f32_e32 v197, v56, v197\n" \
"v_mul_f32_e32 v198, v56, v198\n" \
"v_mfma_f32_16x16x32_bf16 a[208:211], a[216:219], v[144:147], a[208:211]\n" \
"v_mul_f32_e32 v199, v56, v199\n" \
"v_mul_f32_e32 v200, v56, v200\n" \
"v_mfma_f32_16x16x32_bf16 a[208:211], a[216:219], v[152:155], a[208:211]\n" \
"v_mul_f32_e32 v201, v56, v201\n" \
"v_accvgpr_read_b32 v57, a204\n" \
"v_mfma_f32_16x16x32_bf16 a[208:211], a[216:219], v[176:179], a[208:211]\n" \
"v_accvgpr_read_b32 v23, a205\n" \
"v_cvt_pk_bf16_f32 v194, v196, v197\n" \
"v_mfma_f32_16x16x32_bf16 a[208:211], a[216:219], v[184:187], a[208:211]\n" \
"v_cvt_pk_bf16_f32 v195, v198, v199\n" \
"buffer_store_dwordx4 v[192:195], v20, s[56:59], s70 offen offset:0 sc1\n" \
"v_mfma_f32_32x32x16_bf16 v[128:143], a[216:219], a[216:219], 0\n" \
"v_cvt_pk_bf16_f32 v200, v200, v201\n" \
"v_mul_f32_e32 v202, v56, v202\n" \
"v_mul_f32_e32 v203, v56, v203\n" \
"v_add_f32_e32 v57, v57, v23\n" \
"v_cvt_pk_bf16_f32 v201, v202, v203\n" \
"v_mul_f32_e32 v204, v56, v204\n" \
"v_mfma_f32_16x16x32_bf16 a[208:211], a[216:219], v[192:195], a[208:211]\n" \
"v_mul_f32_e32 v205, v56, v205\n" \
"v_mul_f32_e32 v206, v56, v206\n" \
"v_mfma_f32_32x32x16_bf16 v[144:159], a[216:219], a[216:219], 0\n" \
"v_mul_f32_e32 v207, v56, v207\n" \
"v_mul_f32_e32 v208, v56, v208\n" \
"v_mul_f32_e32 v209, v56, v209\n" \
"v_max_f32_e32 v62, v62, v57\n" \
"v_cvt_pk_bf16_f32 v202, v204, v205\n" \
"v_cvt_pk_bf16_f32 v203, v206, v207\n" \
"buffer_store_dwordx4 v[200:203], v20, s[56:59], s70 offen offset:1024 sc1\n" \
"v_mfma_f32_32x32x16_bf16 v[160:175], a[216:219], a[216:219], 0\n" \
"v_cvt_pk_bf16_f32 v208, v208, v209\n" \
"v_mul_f32_e32 v210, v56, v210\n" \
"v_mul_f32_e32 v211, v56, v211\n" \
"v_rcp_f32_e32 v57, v57\n" \
"v_cvt_pk_bf16_f32 v209, v210, v211\n" \
"v_mfma_f32_16x16x32_bf16 a[208:211], a[216:219], v[200:203], a[208:211]\n" \
"v_mul_f32_e32 v212, v56, v212\n" \
"v_mul_f32_e32 v213, v56, v213\n" \
"v_mfma_f32_32x32x16_bf16 v[176:191], a[216:219], a[216:219], 0\n" \
"v_mul_f32_e32 v214, v56, v214\n" \
"v_mul_f32_e32 v215, v56, v215\n" \
"v_mul_f32_e32 v216, v56, v216\n" \
"v_mul_f32_e32 v217, v56, v217\n" \
"v_cvt_pk_bf16_f32 v210, v212, v213\n" \
"v_cvt_pk_bf16_f32 v211, v214, v215\n" \
"s_waitcnt vmcnt(21)\n" \
"s_barrier\n" \
"buffer_store_dwordx4 v[208:211], v20, s[56:59], s70 offen offset:2048 sc1\n" \
"v_mfma_f32_16x16x32_bf16 v[64:67], a[128:131], a[64:67], v[32:35]\n" \
"s_xor_b32 m0, s65, 0x18000\n" \
"v_cvt_pk_bf16_f32 v216, v216, v217\n" \
"v_mfma_f32_16x16x32_bf16 v[64:67], a[132:135], a[68:71], v[64:67]\n" \
"buffer_load_dwordx4 v52, s[8:11], s64 offen offset:0 sc1 lds\n" \
"v_mfma_f32_16x16x32_bf16 v[64:67], a[136:139], a[72:75], v[64:67]\n" \
"v_mul_f32_e32 v218, v56, v218\n" \
"v_mul_f32_e32 v219, v56, v219\n" \
"v_mfma_f32_16x16x32_bf16 v[64:67], a[140:143], a[76:79], v[64:67]\n" \
"buffer_load_dwordx4 v52, s[8:11], s64 offen offset:1024 sc1 lds\n" \
"v_mfma_f32_16x16x32_bf16 v[68:71], a[156:159], a[76:79], v[32:35]\n" \
"v_mul_f32_e32 v57, v57, v61\n" \
"v_cvt_pk_bf16_f32 v217, v218, v219\n" \
"v_mfma_f32_16x16x32_bf16 v[68:71], a[152:155], a[72:75], v[68:71]\n" \
"v_mul_f32_e32 v220, v56, v220\n" \
"v_mul_f32_e32 v221, v56, v221\n" \
"v_mfma_f32_16x16x32_bf16 v[68:71], a[148:151], a[68:71], v[68:71]\n" \
"v_mul_f32_e32 v222, v56, v222\n" \
"v_mul_f32_e32 v223, v56, v223\n" \
"v_mfma_f32_16x16x32_bf16 v[68:71], a[144:147], a[64:67], v[68:71]\n" \
"buffer_load_dwordx4 v52, s[8:11], s64 offen offset:2048 sc1 lds\n" \
"v_mfma_f32_16x16x32_bf16 v[72:75], a[128:131], a[80:83], v[36:39]\n" \
"v_mul_f32_e32 v224, v57, v224\n" \
"v_mul_f32_e32 v225, v57, v225\n" \
"v_mfma_f32_16x16x32_bf16 v[72:75], a[132:135], a[84:87], v[72:75]\n" \
"v_cvt_pk_bf16_f32 v218, v220, v221\n" \
"v_cvt_pk_bf16_f32 v219, v222, v223\n" \
"buffer_store_dwordx4 v[216:219], v20, s[56:59], s70 offen offset:3072 sc1\n" \
"v_mfma_f32_16x16x32_bf16 v[72:75], a[136:139], a[88:91], v[72:75]\n" \
"v_cvt_pk_bf16_f32 v224, v224, v225\n" \
"v_mul_f32_e32 v226, v57, v226\n" \
"v_mfma_f32_16x16x32_bf16 v[72:75], a[140:143], a[92:95], v[72:75]\n" \
"buffer_load_dwordx4 v52, s[8:11], s64 offen offset:3072 sc1 lds\n" \
"v_mfma_f32_16x16x32_bf16 v[76:79], a[156:159], a[92:95], v[36:39]\n" \
"s_mov_b32 m0, s65\n" \
"v_mul_f32_e32 v227, v57, v227\n" \
"v_mfma_f32_16x16x32_bf16 v[76:79], a[152:155], a[88:91], v[76:79]\n" \
"v_cvt_pk_bf16_f32 v225, v226, v227\n" \
"v_mul_f32_e32 v228, v57, v228\n" \
"v_mfma_f32_16x16x32_bf16 v[76:79], a[148:151], a[84:87], v[76:79]\n" \
"v_mul_f32_e32 v229, v57, v229\n" \
"v_mul_f32_e32 v230, v57, v230\n" \
"v_mfma_f32_16x16x32_bf16 v[76:79], a[144:147], a[80:83], v[76:79]\n" \
"buffer_load_dwordx4 v53, s[8:11], s64 offen offset:0 sc1 lds\n" \
"v_mfma_f32_16x16x32_bf16 v[80:83], a[128:131], a[96:99], v[40:43]\n" \
"v_mul_f32_e32 v231, v57, v231\n" \
"v_mul_f32_e32 v232, v57, v232\n" \
"v_mfma_f32_16x16x32_bf16 v[80:83], a[132:135], a[100:103], v[80:83]\n" \
"v_mul_f32_e32 v233, v57, v233\n" \
"v_cvt_pk_bf16_f32 v226, v228, v229\n" \
"v_mfma_f32_16x16x32_bf16 v[80:83], a[136:139], a[104:107], v[80:83]\n" \
"buffer_load_dwordx4 v53, s[8:11], s64 offen offset:1024 sc1 lds\n" \
"v_mfma_f32_16x16x32_bf16 v[80:83], a[140:143], a[108:111], v[80:83]\n" \
"v_cvt_pk_bf16_f32 v227, v230, v231\n" \
"v_cvt_pk_bf16_f32 v232, v232, v233\n" \
"buffer_store_dwordx4 v[224:227], v20, s[56:59], s71 offen offset:0 sc1\n" \
"v_mfma_f32_16x16x32_bf16 v[84:87], a[156:159], a[108:111], v[40:43]\n" \
"v_mul_f32_e32 v234, v57, v234\n" \
"v_mul_f32_e32 v235, v57, v235\n" \
"v_mfma_f32_16x16x32_bf16 v[84:87], a[152:155], a[104:107], v[84:87]\n" \
"v_cvt_pk_bf16_f32 v233, v234, v235\n" \
"v_mul_f32_e32 v236, v57, v236\n" \
"v_mfma_f32_16x16x32_bf16 v[84:87], a[148:151], a[100:103], v[84:87]\n" \
"buffer_load_dwordx4 v53, s[8:11], s64 offen offset:2048 sc1 lds\n" \
"v_mfma_f32_16x16x32_bf16 v[84:87], a[144:147], a[96:99], v[84:87]\n" \
"v_mul_f32_e32 v237, v57, v237\n" \
"v_mul_f32_e32 v238, v57, v238\n" \
"v_mfma_f32_16x16x32_bf16 v[88:91], a[128:131], a[112:115], v[44:47]\n" \
"ds_read_b128 a[128:131], v48 offset:8192\n" \
"v_mul_f32_e32 v239, v57, v239\n" \
"v_mfma_f32_16x16x32_bf16 v[88:91], a[132:135], a[116:119], v[88:91]\n" \
"ds_read_b128 a[132:135], v48 offset:9216\n" \
"v_mul_f32_e32 v240, v57, v240\n" \
"v_mfma_f32_16x16x32_bf16 v[88:91], a[136:139], a[120:123], v[88:91]\n" \
"ds_read_b128 a[136:139], v48 offset:10240\n" \
"v_mul_f32_e32 v241, v57, v241\n" \
"v_mfma_f32_16x16x32_bf16 v[88:91], a[140:143], a[124:127], v[88:91]\n" \
"ds_read_b128 a[140:143], v48 offset:11264\n" \
"v_cvt_pk_bf16_f32 v234, v236, v237\n" \
"v_mfma_f32_16x16x32_bf16 v[92:95], a[156:159], a[124:127], v[44:47]\n" \
"ds_read_b128 a[156:159], v48 offset:15360\n" \
"v_cvt_pk_bf16_f32 v235, v238, v239\n" \
"buffer_store_dwordx4 v[232:235], v20, s[56:59], s71 offen offset:1024 sc1\n" \
"v_mfma_f32_16x16x32_bf16 v[92:95], a[152:155], a[120:123], v[92:95]\n" \
"ds_read_b128 a[152:155], v48 offset:14336\n" \
"v_cvt_pk_bf16_f32 v240, v240, v241\n" \
"v_mfma_f32_16x16x32_bf16 v[92:95], a[148:151], a[116:119], v[92:95]\n" \
"ds_read_b128 a[148:151], v48 offset:13312\n" \
"v_mul_f32_e32 v242, v57, v242\n" \
"v_mfma_f32_16x16x32_bf16 v[92:95], a[144:147], a[112:115], v[92:95]\n" \
"ds_read_b128 a[144:147], v48 offset:12288\n" \
"v_mul_f32_e32 v243, v57, v243\n" \
"v_mfma_f32_16x16x32_bf16 a[208:211], a[216:219], v[208:211], a[208:211]\n" \
"v_cvt_pk_bf16_f32 v241, v242, v243\n" \
"v_mul_f32_e32 v244, v57, v244\n" \
"v_mfma_f32_16x16x32_bf16 a[208:211], a[216:219], v[216:219], a[208:211]\n" \
"v_mul_f32_e32 v245, v57, v245\n" \
"v_mul_f32_e32 v246, v57, v246\n" \
"v_mfma_f32_16x16x32_bf16 a[208:211], a[216:219], v[224:227], a[208:211]\n" \
"v_mul_f32_e32 v247, v57, v247\n" \
"v_mul_f32_e32 v248, v57, v248\n" \
"v_mfma_f32_16x16x32_bf16 a[208:211], a[216:219], v[232:235], a[208:211]\n" \
"v_mul_f32_e32 v249, v57, v249\n" \
"v_cvt_pk_bf16_f32 v242, v244, v245\n" \
"v_mfma_f32_32x32x16_bf16 v[192:207], a[216:219], a[216:219], 0\n" \
"v_cvt_pk_bf16_f32 v243, v246, v247\n" \
"buffer_store_dwordx4 v[240:243], v20, s[56:59], s71 offen offset:2048 sc1\n" \
"v_cvt_pk_bf16_f32 v248, v248, v249\n" \
"v_mfma_f32_16x16x32_bf16 a[208:211], a[216:219], v[240:243], a[208:211]\n" \
"v_mul_f32_e32 v250, v57, v250\n" \
"v_mul_f32_e32 v251, v57, v251\n" \
"v_mfma_f32_32x32x16_bf16 v[208:223], a[216:219], a[216:219], 0\n" \
"v_cvt_pk_bf16_f32 v249, v250, v251\n" \
"v_mul_f32_e32 v252, v57, v252\n" \
"v_mul_f32_e32 v253, v57, v253\n" \
"v_mul_f32_e32 v254, v57, v254\n" \
"v_mul_f32_e32 v255, v57, v255\n" \
"v_cvt_pk_bf16_f32 v250, v252, v253\n" \
"v_mfma_f32_32x32x16_bf16 v[224:239], a[216:219], a[216:219], 0\n" \
"v_cvt_pk_bf16_f32 v251, v254, v255\n" \
"buffer_store_dwordx4 v[248:251], v20, s[56:59], s71 offen offset:3072 sc1\n" \
"buffer_load_dwordx4 v53, s[8:11], s64 offen offset:3072 sc1 lds\n" \
"v_mfma_f32_16x16x32_bf16 a[208:211], a[216:219], v[248:251], a[208:211]\n" \
"v_cmp_lt_f32_e64 s[72:73], s100, v62\n" \
"v_readlane_b32 s95, v11, 2\n" \
"v_mfma_f32_32x32x16_bf16 v[240:255], a[216:219], a[216:219], 0\n" \
"v_exp_f32_e32 v64, v64\n" \
"v_exp_f32_e32 v65, v65\n" \
"v_exp_f32_e32 v66, v66\n" \
"v_mfma_f32_32x32x16_bf16 a[192:207], a[216:219], a[216:219], 0\n" \
"v_exp_f32_e32 v67, v67\n" \
"v_accvgpr_read_b32 v63, a208\n" \
"v_cmp_u_f32_e32 vcc, v63, v63\n" \
"v_exp_f32_e32 v68, v68\n" \
"s_or_b64 vcc, s[72:73], vcc\n" \
"s_cselect_b32 s95, 0x40000000, s95\n" \
"s_cselect_b32 s94, 0x7f800000, 0\n" \
"v_exp_f32_e32 v69, v69\n" \
"v_exp_f32_e32 v70, v70\n" \
"v_exp_f32_e32 v71, v71\n" \
"v_exp_f32_e32 v72, v72\n" \
"v_exp_f32_e32 v73, v73\n" \
"v_exp_f32_e32 v74, v74\n" \
"v_exp_f32_e32 v75, v75\n" \
"v_exp_f32_e32 v76, v76\n" \
"v_exp_f32_e32 v77, v77\n" \
"v_exp_f32_e32 v78, v78\n" \
"v_exp_f32_e32 v79, v79\n" \
"v_exp_f32_e32 v80, v80\n" \
"v_exp_f32_e32 v81, v81\n" \
"v_exp_f32_e32 v82, v82\n" \
"v_exp_f32_e32 v83, v83\n" \
"v_exp_f32_e32 v84, v84\n" \
"v_exp_f32_e32 v85, v85\n" \
"v_exp_f32_e32 v86, v86\n" \
"v_exp_f32_e32 v87, v87\n" \
"v_add_u32_e32 v49, 0x10000, v48\n" \
"s_xor_b32 s65, s65, 0x8000\n" \
"s_add_u32 s64, s64, 0x10000\n" \
"v_exp_f32_e32 v88, v88\n" \
"v_exp_f32_e32 v89, v89\n" \
"v_exp_f32_e32 v90, v90\n" \
"v_exp_f32_e32 v91, v91\n" \
"v_cvt_pk_bf16_f32 v96, v64, v65\n" \
"v_cvt_pk_bf16_f32 v97, v66, v67\n" \
"v_cvt_pk_bf16_f32 v98, v68, v69\n" \
"v_cvt_pk_bf16_f32 v99, v70, v71\n" \
"v_cvt_pk_bf16_f32 v104, v72, v73\n" \
"s_waitcnt lgkmcnt(0)\n" \
"v_cvt_pk_bf16_f32 v105, v74, v75\n" \
"v_cvt_pk_bf16_f32 v106, v76, v77\n" \
"v_cvt_pk_bf16_f32 v107, v78, v79\n" \
"v_cvt_pk_bf16_f32 v112, v80, v81\n" \
"v_cvt_pk_bf16_f32 v113, v82, v83\n" \
"v_cvt_pk_bf16_f32 v114, v84, v85\n" \
"v_cvt_pk_bf16_f32 v115, v86, v87\n" \
"v_xor_b32_e32 v48, 0x8000, v48\n" \
"s_sub_u32 s66, s19, 1\n" \
"s_cbranch_scc0 L_2_%=\n" \
"s_waitcnt vmcnt(14)\n" \
"s_barrier\n" \
"L_f1_%=:\n" \
"L_pf18:\n" \
"s_cbranch_execz L_retA\n" \
"s_nop 0\n" \
"ds_read_b128 a[160:163], v49 offset:0\n" \
"ds_read_b128 a[164:167], v49 offset:1024\n" \
"ds_read_b128 a[168:171], v49 offset:2048\n" \
"ds_read_b128 a[172:175], v49 offset:3072\n" \
"ds_read_b128 a[176:179], v49 offset:8192\n" \
"ds_read_b128 a[180:183], v49 offset:9216\n" \
"v_mfma_f32_16x16x32_bf16 v[72:75], a[128:131], a[80:83], v[36:39]\n" \
"s_add_u32 s68, s61, 0x0\n" \
"s_add_u32 s69, s61, 0x49000\n" \
"ds_read_b128 a[184:187], v49 offset:10240\n" \
"v_mfma_f32_16x16x32_bf16 v[72:75], a[132:135], a[84:87], v[72:75]\n" \
"ds_read_b128 a[188:191], v49 offset:11264\n" \
"v_add_u32_e32 v56, 0x49000, v20\n" \
"v_mfma_f32_16x16x32_bf16 v[72:75], a[136:139], a[88:91], v[72:75]\n" \
"v_readlane_b32 s97, v11, 32\n" \
"v_readlane_b32 s98, v11, 33\n" \
"v_mfma_f32_16x16x32_bf16 v[72:75], a[140:143], a[92:95], v[72:75]\n" \
"v_and_b32_e32 v96, v12, v96\n" \
"v_and_b32_e32 v97, v13, v97\n" \
"v_mfma_f32_16x16x32_bf16 v[76:79], a[156:159], a[92:95], v[36:39]\n" \
"buffer_load_dwordx4 a[32:35], v20, s[56:59], s97 offen offset:0 sc1\n" \
"v_mfma_f32_16x16x32_bf16 v[76:79], a[152:155], a[88:91], v[76:79]\n" \
"v_and_b32_e32 v98, v14, v98\n" \
"v_and_b32_e32 v99, v15, v99\n" \
"v_mfma_f32_16x16x32_bf16 v[76:79], a[148:151], a[84:87], v[76:79]\n" \
"ds_read_b128 a[0:3], v20 offset:8192\n" \
"v_mov_b32_e32 v61, s62\n" \
"v_mfma_f32_16x16x32_bf16 v[76:79], a[144:147], a[80:83], v[76:79]\n" \
"buffer_load_dwordx4 a[40:43], v56, s[56:59], s97 offen offset:0 sc1\n" \
"ds_read_b128 a[4:7], v20 offset:9216\n" \
"ds_read_b128 a[8:11], v20 offset:10240\n" \
"s_waitcnt lgkmcnt(10)\n" \
"v_mfma_f32_16x16x32_bf16 v[128:131], a[160:163], v[96:99], v[128:131]\n" \
"ds_read_b128 a[12:15], v20 offset:11264\n" \
"s_waitcnt lgkmcnt(10)\n" \
"v_mfma_f32_16x16x32_bf16 v[132:135], a[164:167], v[96:99], v[132:135]\n" \
"ds_read_b128 a[28:31], v20 offset:15360\n" \
"buffer_load_dwordx4 a[48:51], v20, s[56:59], s98 offen offset:0 sc1\n" \
"s_waitcnt lgkmcnt(10)\n" \
"v_mfma_f32_16x16x32_bf16 v[136:139], a[168:171], v[96:99], v[136:139]\n" \
"ds_read_b128 a[24:27], v20 offset:14336\n" \
"s_waitcnt lgkmcnt(10)\n" \
"v_mfma_f32_16x16x32_bf16 v[140:143], a[172:175], v[96:99], v[140:143]\n" \
"ds_read_b128 a[20:23], v20 offset:13312\n" \
"buffer_load_dwordx4 a[56:59], v56, s[56:59], s98 offen offset:0 sc1\n" \
"s_waitcnt lgkmcnt(9)\n" \
"v_mfma_f32_16x16x32_bf16 v[144:147], a[176:179], v[96:99], v[144:147]\n" \
"ds_read_b128 a[16:19], v20 offset:12288\n" \
"v_add_u32_e32 v50, s15, v48\n" \
"v_mfma_f32_16x16x32_bf16 v[148:151], a[180:183], v[96:99], v[148:151]\n" \
"v_xor_b32_e32 v48, 0x4000, v49\n" \
"s_waitcnt lgkmcnt(8)\n" \
"v_mfma_f32_16x16x32_bf16 v[152:155], a[184:187], v[96:99], v[152:155]\n" \
"buffer_load_dwordx4 a[36:39], v20, s[56:59], s97 offen offset:1024 sc1\n" \
"v_mfma_f32_16x16x32_bf16 v[156:159], a[188:191], v[96:99], v[156:159]\n" \
"ds_read_b32 v58, v18 offset:256\n" \
"ds_read_b32 v59, v18 offset:384\n" \
"v_mfma_f32_16x16x32_bf16 a[192:195], a[252:255], v[96:99], a[192:195]\n" \
"ds_read_b128 a[96:99], v48 offset:4096\n" \
"ds_read_b128 a[100:103], v48 offset:5120\n" \
"buffer_load_dwordx4 a[44:47], v56, s[56:59], s97 offen offset:1024 sc1\n" \
"v_mfma_f32_16x16x32_bf16 v[160:163], a[160:163], v[104:107], v[160:163]\n" \
"ds_read_b128 a[104:107], v48 offset:6144\n" \
"ds_read_b128 a[108:111], v48 offset:7168\n" \
"v_mfma_f32_16x16x32_bf16 v[164:167], a[164:167], v[104:107], v[164:167]\n" \
"ds_read_b128 a[112:115], v48 offset:12288\n" \
"ds_read_b128 a[116:119], v48 offset:13312\n" \
"buffer_load_dwordx4 a[52:55], v20, s[56:59], s98 offen offset:1024 sc1\n" \
"v_mfma_f32_16x16x32_bf16 v[168:171], a[168:171], v[104:107], v[168:171]\n" \
"ds_read_b128 a[120:123], v48 offset:14336\n" \
"ds_read_b128 a[124:127], v48 offset:15360\n" \
"v_mfma_f32_16x16x32_bf16 v[172:175], a[172:175], v[104:107], v[172:175]\n" \
"ds_read_b128 a[128:131], v49 offset:4096\n" \
"ds_read_b128 a[132:135], v49 offset:5120\n" \
"buffer_load_dwordx4 a[60:63], v56, s[56:59], s98 offen offset:1024 sc1\n" \
"v_mfma_f32_16x16x32_bf16 v[176:179], a[176:179], v[104:107], v[176:179]\n" \
"ds_read_b128 a[136:139], v49 offset:6144\n" \
"ds_read_b128 a[140:143], v49 offset:7168\n" \
"v_mfma_f32_16x16x32_bf16 v[180:183], a[180:183], v[104:107], v[180:183]\n" \
"ds_read_b128 a[144:147], v49 offset:12288\n" \
"ds_read_b128 a[148:151], v49 offset:13312\n" \
"s_waitcnt vmcnt(8)\n" \
"s_mov_b64 exec, 1\n" \
"buffer_store_dword v61, off, s[56:59], s95 offset:0 sc1\n" \
"s_mov_b64 exec, -1\n" \
"v_mfma_f32_16x16x32_bf16 v[184:187], a[184:187], v[104:107], v[184:187]\n" \
"ds_read_b128 a[152:155], v49 offset:14336\n" \
"ds_read_b128 a[156:159], v49 offset:15360\n" \
"v_mfma_f32_16x16x32_bf16 v[188:191], a[188:191], v[104:107], v[188:191]\n" \
"v_accvgpr_read_b32 v40, a200\n" \
"v_accvgpr_read_b32 v23, a201\n" \
"v_mfma_f32_16x16x32_bf16 a[196:199], a[252:255], v[104:107], a[196:199]\n" \
"v_add_f32_e32 v40, v40, v23\n" \
"s_waitcnt lgkmcnt(15)\n" \
"v_mfma_f32_16x16x32_bf16 v[80:83], a[0:3], a[64:67], v[32:35]\n" \
"buffer_load_dwordx4 a[220:223], v20, s[56:59], s60 offen offset:0 sc1\n" \
"v_mfma_f32_16x16x32_bf16 v[80:83], a[4:7], a[68:71], v[80:83]\n" \
"v_accvgpr_read_b32 v41, a204\n" \
"v_accvgpr_read_b32 v23, a205\n" \
"v_mfma_f32_16x16x32_bf16 v[80:83], a[8:11], a[72:75], v[80:83]\n" \
"v_add_f32_e32 v41, v41, v23\n" \
"v_add_u32_e32 v51, 0x10000, v50\n" \
"v_mfma_f32_16x16x32_bf16 v[80:83], a[12:15], a[76:79], v[80:83]\n" \
"buffer_load_dwordx4 a[236:239], v56, s[56:59], s60 offen offset:0 sc1\n" \
"v_mfma_f32_16x16x32_bf16 v[84:87], a[28:31], a[76:79], v[32:35]\n" \
"v_add_f32_e32 v40, s94, v40\n" \
"v_add_f32_e32 v41, s94, v41\n" \
"v_mfma_f32_16x16x32_bf16 v[84:87], a[24:27], a[72:75], v[84:87]\n" \
"ds_write_b128 v50, v[192:195] offset:0\n" \
"buffer_load_dwordx4 a[224:227], v20, s[56:59], s60 offen offset:1024 sc1\n" \
"v_mfma_f32_16x16x32_bf16 v[84:87], a[20:23], a[68:71], v[84:87]\n" \
"ds_write_b128 v50, v[196:199] offset:1024\n" \
"v_mfma_f32_16x16x32_bf16 v[84:87], a[16:19], a[64:67], v[84:87]\n" \
"ds_write_b128 v50, v[200:203] offset:2048\n" \
"buffer_load_dwordx4 a[240:243], v56, s[56:59], s60 offen offset:1024 sc1\n" \
"v_mfma_f32_16x16x32_bf16 v[88:91], a[0:3], a[80:83], v[36:39]\n" \
"ds_write_b128 v50, v[204:207] offset:3072\n" \
"v_mfma_f32_16x16x32_bf16 v[88:91], a[4:7], a[84:87], v[88:91]\n" \
"ds_write_b128 v50, v[208:211] offset:8192\n" \
"L_pf19:\n" \
"s_cbranch_execz L_retA\n" \
"s_nop 0\n" \
"buffer_load_dwordx4 a[228:231], v20, s[56:59], s60 offen offset:2048 sc1\n" \
"v_mfma_f32_16x16x32_bf16 v[88:91], a[8:11], a[88:91], v[88:91]\n" \
"ds_write_b128 v50, v[212:215] offset:9216\n" \
"v_mfma_f32_16x16x32_bf16 v[88:91], a[12:15], a[92:95], v[88:91]\n" \
"ds_write_b128 v50, v[216:219] offset:10240\n" \
"buffer_load_dwordx4 a[244:247], v56, s[56:59], s60 offen offset:2048 sc1\n" \
"v_mfma_f32_16x16x32_bf16 v[92:95], a[28:31], a[92:95], v[36:39]\n" \
"ds_write_b128 v50, v[220:223] offset:11264\n" \
"ds_write_b128 v51, v[224:227] offset:0\n" \
"ds_write_b128 v51, v[228:231] offset:1024\n" \
"buffer_load_dwordx4 a[232:235], v20, s[56:59], s60 offen offset:3072 sc1\n" \
"ds_write_b128 v51, v[232:235] offset:2048\n" \
"ds_write_b128 v51, v[236:239] offset:3072\n" \
"ds_write_b128 v51, v[240:243] offset:8192\n" \
"ds_write_b128 v51, v[244:247] offset:9216\n" \
"buffer_load_dwordx4 a[248:251], v56, s[56:59], s60 offen offset:3072 sc1\n" \
"ds_write_b128 v51, v[248:251] offset:10240\n" \
"ds_write_b128 v51, v[252:255] offset:11264\n" \
"ds_write_b64 v19, v[40:41]\n" \
"v_mfma_f32_16x16x32_bf16 v[92:95], a[24:27], a[88:91], v[92:95]\n" \
"v_xor_b32_e32 v52, 0x4000, v50\n" \
"v_xor_b32_e32 v53, 0x4000, v51\n" \
"v_mfma_f32_16x16x32_bf16 v[92:95], a[20:23], a[84:87], v[92:95]\n" \
"v_exp_f32_e32 v72, v72\n" \
"v_mfma_f32_16x16x32_bf16 v[92:95], a[16:19], a[80:83], v[92:95]\n" \
"v_exp_f32_e32 v73, v73\n" \
"v_exp_f32_e32 v74, v74\n" \
"v_exp_f32_e32 v75, v75\n" \
"v_exp_f32_e32 v76, v76\n" \
"v_exp_f32_e32 v77, v77\n" \
"v_exp_f32_e32 v78, v78\n" \
"v_exp_f32_e32 v79, v79\n" \
"v_cvt_pk_bf16_f32 v108, v72, v73\n" \
"v_cvt_pk_bf16_f32 v109, v74, v75\n" \
"v_cvt_pk_bf16_f32 v110, v76, v77\n" \
"v_cvt_pk_bf16_f32 v111, v78, v79\n" \
"v_and_b32_e32 v108, v12, v108\n" \
"v_and_b32_e32 v109, v13, v109\n" \
"v_and_b32_e32 v110, v14, v110\n" \
"v_and_b32_e32 v111, v15, v111\n" \
"v_readlane_b32 s93, v11, 18\n" \
"v_readlane_b32 s84, v11, 13\n" \
"v_readlane_b32 s85, v11, 14\n" \
"v_readlane_b32 s86, v11, 15\n" \
"s_mov_b32 s87, 0x20000\n" \
"v_readlane_b32 s92, v11, 16\n" \
"s_add_u32 m0, s93, 0x0\n" \
"v_xor_b32_e32 v63, 0x400, v19\n" \
"s_waitcnt lgkmcnt(0)\n" \
"s_barrier\n" \
"ds_read_b128 v[192:195], v52 offset:0\n" \
"ds_read_b128 v[196:199], v52 offset:1024\n" \
"ds_read_b128 v[200:203], v52 offset:2048\n" \
"ds_read_b128 v[204:207], v52 offset:3072\n" \
"ds_read_b128 v[208:211], v52 offset:8192\n" \
"ds_read_b128 v[212:215], v52 offset:9216\n" \
"ds_read_b128 v[216:219], v52 offset:10240\n" \
"ds_read_b128 v[220:223], v52 offset:11264\n" \
"ds_read_b128 v[224:227], v53 offset:0\n" \
"ds_read_b128 v[228:231], v53 offset:1024\n" \
"ds_read_b128 v[232:235], v53 offset:2048\n" \
"ds_read_b128 v[236:239], v53 offset:3072\n" \
"ds_read_b128 v[240:243], v53 offset:8192\n" \
"ds_read_b128 v[244:247], v53 offset:9216\n" \
"ds_read_b128 v[248:251], v53 offset:10240\n" \
"ds_read_b128 v[252:255], v53 offset:11264\n" \
"v_mfma_f32_16x16x32_bf16 v[160:163], a[128:131], v[108:111], v[160:163]\n" \
"buffer_load_dwordx4 v0, s[84:87], s92 offen offset:0 lds\n" \
"v_mfma_f32_16x16x32_bf16 v[164:167], a[132:135], v[108:111], v[164:167]\n" \
"v_exp_f32_e32 v80, v80\n" \
"v_mfma_f32_16x16x32_bf16 v[168:171], a[136:139], v[108:111], v[168:171]\n" \
"v_exp_f32_e32 v81, v81\n" \
"v_mfma_f32_16x16x32_bf16 v[172:175], a[140:143], v[108:111], v[172:175]\n" \
"buffer_load_dwordx4 v1, s[84:87], s92 offen offset:1024 lds\n" \
"v_mfma_f32_16x16x32_bf16 v[176:179], a[144:147], v[108:111], v[176:179]\n" \
"ds_read_b64 v[42:43], v63\n" \
"v_cvt_pk_bf16_f32 v116, v80, v81\n" \
"v_mfma_f32_16x16x32_bf16 v[180:183], a[148:151], v[108:111], v[180:183]\n" \
"v_exp_f32_e32 v82, v82\n" \
"v_mfma_f32_16x16x32_bf16 v[184:187], a[152:155], v[108:111], v[184:187]\n" \
"buffer_load_dwordx4 v2, s[84:87], s92 offen offset:2048 lds\n" \
"v_mfma_f32_16x16x32_bf16 v[188:191], a[156:159], v[108:111], v[188:191]\n" \
"v_exp_f32_e32 v83, v83\n" \
"v_mfma_f32_16x16x32_bf16 a[196:199], a[252:255], v[108:111], a[196:199]\n" \
"v_exp_f32_e32 v84, v84\n" \
"v_exp_f32_e32 v85, v85\n" \
"v_exp_f32_e32 v86, v86\n" \
"s_waitcnt lgkmcnt(1)\n" \
"s_barrier\n" \
"buffer_load_dwordx4 v3, s[84:87], s92 offen offset:3072 lds\n" \
"s_add_u32 m0, s93, 0x1000\n" \
"v_exp_f32_e32 v87, v87\n" \
"v_cvt_pk_bf16_f32 v117, v82, v83\n" \
"v_cvt_pk_bf16_f32 v118, v84, v85\n" \
"v_cvt_pk_bf16_f32 v119, v86, v87\n" \
"v_exp_f32_e32 v88, v88\n" \
"v_exp_f32_e32 v89, v89\n" \
"v_mfma_f32_16x16x32_bf16 v[128:131], a[96:99], v[116:119], v[128:131]\n" \
"buffer_load_dwordx4 v4, s[84:87], s92 offen offset:0 lds\n" \
"v_mfma_f32_16x16x32_bf16 v[132:135], a[100:103], v[116:119], v[132:135]\n" \
"v_exp_f32_e32 v90, v90\n" \
"v_mfma_f32_16x16x32_bf16 v[136:139], a[104:107], v[116:119], v[136:139]\n" \
"v_exp_f32_e32 v91, v91\n" \
"v_mfma_f32_16x16x32_bf16 v[140:143], a[108:111], v[116:119], v[140:143]\n" \
"buffer_load_dwordx4 v5, s[84:87], s92 offen offset:1024 lds\n" \
"v_mfma_f32_16x16x32_bf16 v[144:147], a[112:115], v[116:119], v[144:147]\n" \
"v_exp_f32_e32 v92, v92\n" \
"v_mfma_f32_16x16x32_bf16 v[148:151], a[116:119], v[116:119], v[148:151]\n" \
"v_exp_f32_e32 v93, v93\n" \
"v_mfma_f32_16x16x32_bf16 v[152:155], a[120:123], v[116:119], v[152:155]\n" \
"buffer_load_dwordx4 v6, s[84:87], s92 offen offset:2048 lds\n" \
"v_mfma_f32_16x16x32_bf16 v[156:159], a[124:127], v[116:119], v[156:159]\n" \
"v_exp_f32_e32 v94, v94\n" \
"v_mfma_f32_16x16x32_bf16 a[192:195], a[252:255], v[116:119], a[192:195]\n" \
"v_exp_f32_e32 v95, v95\n" \
"v_cvt_pk_bf16_f32 v124, v88, v89\n" \
"v_cvt_pk_bf16_f32 v125, v90, v91\n" \
"v_cvt_pk_bf16_f32 v126, v92, v93\n" \
"v_cvt_pk_bf16_f32 v127, v94, v95\n" \
"v_add_f32_e32 v160, v160, v224\n" \
"v_add_f32_e32 v161, v161, v225\n" \
"v_add_f32_e32 v162, v162, v226\n" \
"v_add_f32_e32 v163, v163, v227\n" \
"v_add_f32_e32 v164, v164, v228\n" \
"v_add_f32_e32 v165, v165, v229\n" \
"v_mfma_f32_16x16x32_bf16 v[160:163], a[96:99], v[124:127], v[160:163]\n" \
"buffer_load_dwordx4 v7, s[84:87], s92 offen offset:3072 lds\n" \
"s_add_u32 m0, s93, 0x2000\n" \
"v_add_f32_e32 v166, v166, v230\n" \
"v_add_f32_e32 v167, v167, v231\n" \
"v_add_f32_e32 v168, v168, v232\n" \
"v_add_f32_e32 v169, v169, v233\n" \
"v_mfma_f32_16x16x32_bf16 v[164:167], a[100:103], v[124:127], v[164:167]\n" \
"v_add_f32_e32 v170, v170, v234\n" \
"v_add_f32_e32 v171, v171, v235\n" \
"v_add_f32_e32 v172, v172, v236\n" \
"v_add_f32_e32 v173, v173, v237\n" \
"v_mfma_f32_16x16x32_bf16 v[168:171], a[104:107], v[124:127], v[168:171]\n" \
"buffer_load_dwordx4 v8, s[84:87], s92 offen offset:0 lds\n" \
"v_add_f32_e32 v174, v174, v238\n" \
"v_add_f32_e32 v175, v175, v239\n" \
"v_accvgpr_read_b32 v54, a192\n" \
"v_accvgpr_read_b32 v23, a193\n" \
"v_mfma_f32_16x16x32_bf16 v[172:175], a[108:111], v[124:127], v[172:175]\n" \
"v_add_f32_e32 v54, v54, v23\n" \
"s_waitcnt lgkmcnt(0)\n" \
"v_add_f32_e32 v54, v54, v42\n" \
"v_max_f32_e32 v62, v62, v54\n" \
"v_add_f32_e32 v176, v176, v240\n" \
"v_add_f32_e32 v177, v177, v241\n" \
"v_add_f32_e32 v178, v178, v242\n" \
"v_add_f32_e32 v179, v179, v243\n" \
"v_rcp_f32_e32 v54, v54\n" \
"v_add_f32_e32 v128, v128, v192\n" \
"v_mfma_f32_16x16x32_bf16 v[176:179], a[112:115], v[124:127], v[176:179]\n" \
"v_add_f32_e32 v129, v129, v193\n" \
"v_add_f32_e32 v130, v130, v194\n" \
"v_add_f32_e32 v131, v131, v195\n" \
"v_add_f32_e32 v132, v132, v196\n" \
"v_add_f32_e32 v133, v133, v197\n" \
"v_add_f32_e32 v134, v134, v198\n" \
"v_add_f32_e32 v135, v135, v199\n" \
"v_mul_f32_e32 v54, v54, v58\n" \
"v_mul_f32_e32 v128, v54, v128\n" \
"v_mul_f32_e32 v129, v54, v129\n" \
"v_mul_f32_e32 v130, v54, v130\n" \
"v_mul_f32_e32 v131, v54, v131\n" \
"v_mul_f32_e32 v132, v54, v132\n" \
"v_mul_f32_e32 v133, v54, v133\n" \
"v_mul_f32_e32 v134, v54, v134\n" \
"v_mul_f32_e32 v135, v54, v135\n" \
"v_add_f32_e32 v180, v180, v244\n" \
"v_add_f32_e32 v181, v181, v245\n" \
"v_add_f32_e32 v182, v182, v246\n" \
"v_add_f32_e32 v183, v183, v247\n" \
"v_add_f32_e32 v136, v136, v200\n" \
"v_add_f32_e32 v137, v137, v201\n" \
"v_add_f32_e32 v138, v138, v202\n" \
"v_add_f32_e32 v139, v139, v203\n" \
"v_add_f32_e32 v140, v140, v204\n" \
"v_add_f32_e32 v141, v141, v205\n" \
"v_add_f32_e32 v142, v142, v206\n" \
"v_add_f32_e32 v143, v143, v207\n" \
"v_cvt_pk_bf16_f32 v64, v128, v129\n" \
"v_cvt_pk_bf16_f32 v65, v130, v131\n" \
"v_cvt_pk_bf16_f32 v66, v132, v133\n" \
"v_cvt_pk_bf16_f32 v67, v134, v135\n" \
"buffer_store_dwordx4 v[64:67], v20, s[56:59], s68 offen offset:0 sc1\n" \
"v_mfma_f32_16x16x32_bf16 v[180:183], a[116:119], v[124:127], v[180:183]\n" \
"v_mul_f32_e32 v136, v54, v136\n" \
"v_mul_f32_e32 v137, v54, v137\n" \
"v_mfma_f32_16x16x32_bf16 a[208:211], a[216:219], v[64:67], a[208:211]\n" \
"v_mul_f32_e32 v138, v54, v138\n" \
"v_mul_f32_e32 v139, v54, v139\n" \
"v_mul_f32_e32 v140, v54, v140\n" \
"v_mul_f32_e32 v141, v54, v141\n" \
"v_mul_f32_e32 v142, v54, v142\n" \
"v_mul_f32_e32 v143, v54, v143\n" \
"v_add_f32_e32 v144, v144, v208\n" \
"v_add_f32_e32 v145, v145, v209\n" \
"v_add_f32_e32 v146, v146, v210\n" \
"v_add_f32_e32 v147, v147, v211\n" \
"v_add_f32_e32 v148, v148, v212\n" \
"v_add_f32_e32 v149, v149, v213\n" \
"v_add_f32_e32 v150, v150, v214\n" \
"v_add_f32_e32 v151, v151, v215\n" \
"v_cvt_pk_bf16_f32 v68, v136, v137\n" \
"v_cvt_pk_bf16_f32 v69, v138, v139\n" \
"v_cvt_pk_bf16_f32 v70, v140, v141\n" \
"v_cvt_pk_bf16_f32 v71, v142, v143\n" \
"buffer_store_dwordx4 v[68:71], v20, s[56:59], s68 offen offset:1024 sc1\n" \
"v_mul_f32_e32 v144, v54, v144\n" \
"v_mfma_f32_16x16x32_bf16 a[208:211], a[216:219], v[68:71], a[208:211]\n" \
"v_mul_f32_e32 v145, v54, v145\n" \
"v_mul_f32_e32 v146, v54, v146\n" \
"v_mul_f32_e32 v147, v54, v147\n" \
"v_mul_f32_e32 v148, v54, v148\n" \
"v_mul_f32_e32 v149, v54, v149\n" \
"v_mul_f32_e32 v150, v54, v150\n" \
"v_mul_f32_e32 v151, v54, v151\n" \
"v_add_f32_e32 v184, v184, v248\n" \
"v_add_f32_e32 v185, v185, v249\n" \
"v_add_f32_e32 v186, v186, v250\n" \
"v_add_f32_e32 v187, v187, v251\n" \
"v_add_f32_e32 v152, v152, v216\n" \
"v_add_f32_e32 v153, v153, v217\n" \
"v_add_f32_e32 v154, v154, v218\n" \
"v_add_f32_e32 v155, v155, v219\n" \
"v_add_f32_e32 v156, v156, v220\n" \
"v_add_f32_e32 v157, v157, v221\n" \
"v_add_f32_e32 v158, v158, v222\n" \
"v_add_f32_e32 v159, v159, v223\n" \
"v_cvt_pk_bf16_f32 v72, v144, v145\n" \
"v_cvt_pk_bf16_f32 v73, v146, v147\n" \
"v_cvt_pk_bf16_f32 v74, v148, v149\n" \
"v_cvt_pk_bf16_f32 v75, v150, v151\n" \
"buffer_store_dwordx4 v[72:75], v20, s[56:59], s68 offen offset:2048 sc1\n" \
"v_mfma_f32_16x16x32_bf16 v[184:187], a[120:123], v[124:127], v[184:187]\n" \
"v_mul_f32_e32 v152, v54, v152\n" \
"v_mul_f32_e32 v153, v54, v153\n" \
"v_mfma_f32_16x16x32_bf16 a[208:211], a[216:219], v[72:75], a[208:211]\n" \
"v_mul_f32_e32 v154, v54, v154\n" \
"v_mul_f32_e32 v155, v54, v155\n" \
"v_mul_f32_e32 v156, v54, v156\n" \
"v_mul_f32_e32 v157, v54, v157\n" \
"v_mul_f32_e32 v158, v54, v158\n" \
"v_mul_f32_e32 v159, v54, v159\n" \
"v_cvt_pk_bf16_f32 v76, v152, v153\n" \
"v_cvt_pk_bf16_f32 v77, v154, v155\n" \
"v_cvt_pk_bf16_f32 v78, v156, v157\n" \
"v_cvt_pk_bf16_f32 v79, v158, v159\n" \
"buffer_store_dwordx4 v[76:79], v20, s[56:59], s68 offen offset:3072 sc1\n" \
"v_add_f32_e32 v188, v188, v252\n" \
"v_mfma_f32_16x16x32_bf16 a[208:211], a[216:219], v[76:79], a[208:211]\n" \
"v_add_f32_e32 v189, v189, v253\n" \
"v_add_f32_e32 v190, v190, v254\n" \
"v_add_f32_e32 v191, v191, v255\n" \
"s_nop 1\n" \
"v_mfma_f32_16x16x32_bf16 v[188:191], a[124:127], v[124:127], v[188:191]\n" \
"v_mfma_f32_16x16x32_bf16 a[196:199], a[252:255], v[124:127], a[196:199]\n" \
"s_nop 7\n" \
"v_accvgpr_read_b32 v55, a196\n" \
"v_accvgpr_read_b32 v23, a197\n" \
"v_add_f32_e32 v55, v55, v23\n" \
"v_add_f32_e32 v55, v55, v43\n" \
"v_max_f32_e32 v62, v62, v55\n" \
"v_rcp_f32_e32 v55, v55\n" \
"v_cmp_lt_f32_e64 s[72:73], s100, v62\n" \
"v_mul_f32_e32 v55, v55, v59\n" \
"v_mul_f32_e32 v160, v55, v160\n" \
"v_mul_f32_e32 v161, v55, v161\n" \
"v_mul_f32_e32 v162, v55, v162\n" \
"v_mul_f32_e32 v163, v55, v163\n" \
"v_mul_f32_e32 v164, v55, v164\n" \
"v_mul_f32_e32 v165, v55, v165\n" \
"v_mul_f32_e32 v166, v55, v166\n" \
"v_mul_f32_e32 v167, v55, v167\n" \
"v_cvt_pk_bf16_f32 v80, v160, v161\n" \
"v_cvt_pk_bf16_f32 v81, v162, v163\n" \
"v_cvt_pk_bf16_f32 v82, v164, v165\n" \
"v_cvt_pk_bf16_f32 v83, v166, v167\n" \
"buffer_store_dwordx4 v[80:83], v20, s[56:59], s69 offen offset:0 sc1\n" \
"v_mul_f32_e32 v168, v55, v168\n" \
"v_mfma_f32_16x16x32_bf16 a[208:211], a[216:219], v[80:83], a[208:211]\n" \
"v_mul_f32_e32 v169, v55, v169\n" \
"v_mul_f32_e32 v170, v55, v170\n" \
"v_mul_f32_e32 v171, v55, v171\n" \
"v_mul_f32_e32 v172, v55, v172\n" \
"v_mul_f32_e32 v173, v55, v173\n" \
"v_mul_f32_e32 v174, v55, v174\n" \
"v_mul_f32_e32 v175, v55, v175\n" \
"v_cvt_pk_bf16_f32 v84, v168, v169\n" \
"v_cvt_pk_bf16_f32 v85, v170, v171\n" \
"v_cvt_pk_bf16_f32 v86, v172, v173\n" \
"v_cvt_pk_bf16_f32 v87, v174, v175\n" \
"buffer_store_dwordx4 v[84:87], v20, s[56:59], s69 offen offset:1024 sc1\n" \
"v_mul_f32_e32 v176, v55, v176\n" \
"v_mfma_f32_16x16x32_bf16 a[208:211], a[216:219], v[84:87], a[208:211]\n" \
"v_mul_f32_e32 v177, v55, v177\n" \
"v_mul_f32_e32 v178, v55, v178\n" \
"v_mul_f32_e32 v179, v55, v179\n" \
"v_mul_f32_e32 v180, v55, v180\n" \
"v_mul_f32_e32 v181, v55, v181\n" \
"v_mul_f32_e32 v182, v55, v182\n" \
"v_mul_f32_e32 v183, v55, v183\n" \
"v_cvt_pk_bf16_f32 v96, v176, v177\n" \
"v_cvt_pk_bf16_f32 v97, v178, v179\n" \
"v_cvt_pk_bf16_f32 v98, v180, v181\n" \
"v_cvt_pk_bf16_f32 v99, v182, v183\n" \
"buffer_store_dwordx4 v[96:99], v20, s[56:59], s69 offen offset:2048 sc1\n" \
"v_mul_f32_e32 v184, v55, v184\n" \
"v_mfma_f32_16x16x32_bf16 a[208:211], a[216:219], v[96:99], a[208:211]\n" \
"v_mul_f32_e32 v185, v55, v185\n" \
"v_mul_f32_e32 v186, v55, v186\n" \
"v_mul_f32_e32 v187, v55, v187\n" \
"v_mul_f32_e32 v188, v55, v188\n" \
"v_mul_f32_e32 v189, v55, v189\n" \
"v_mul_f32_e32 v190, v55, v190\n" \
"v_mul_f32_e32 v191, v55, v191\n" \
"v_cvt_pk_bf16_f32 v100, v184, v185\n" \
"v_cvt_pk_bf16_f32 v101, v186, v187\n" \
"v_cvt_pk_bf16_f32 v102, v188, v189\n" \
"v_cvt_pk_bf16_f32 v103, v190, v191\n" \
"buffer_store_dwordx4 v[100:103], v20, s[56:59], s69 offen offset:3072 sc1\n" \
"s_nop 0\n" \
"v_mfma_f32_16x16x32_bf16 a[208:211], a[216:219], v[100:103], a[208:211]\n" \
"s_nop 7\n" \
"v_accvgpr_read_b32 v63, a208\n" \
"v_cmp_u_f32_e32 vcc, v63, v63\n" \
"s_nop 0\n" \
"s_or_b64 vcc, s[72:73], vcc\n" \
"s_cbranch_scc1 L_end_%=\n" \
"L_out_%=:\n" \
"L_pf20:\n" \
"s_cbranch_execz L_retA\n" \
"s_nop 0\n" \
"v_add_u32_e32 v34, 0x20000, v10\n" \
"v_mov_b32_e32 v39, s62\n" \
"v_readlane_b32 s70, v11, 4\n" \
"v_bfe_u32 v38, v20, 2, 4\n" \
"v_readlane_b32 s64, v11, 1\n" \
"v_readlane_b32 s71, v11, 3\n" \
"v_add_u32_e32 v33, 0xfc00, v10\n" \
"v_readlane_b32 s99, v11, 19\n" \
"v_readlane_b32 s80, v11, 9\n" \
"v_readlane_b32 s81, v11, 10\n" \
"s_mov_b32 s82, 0x47fca0\n" \
"s_mov_b32 s83, 0x20000\n" \
"ds_read_b64_tr_b16 a[0:1], v34 offset:0\n" \
"ds_read_b64_tr_b16 a[2:3], v34 offset:4608\n" \
"ds_read_b64_tr_b16 a[4:5], v34 offset:9216\n" \
"ds_read_b64_tr_b16 a[6:7], v34 offset:13824\n" \
"ds_read_b64_tr_b16 a[8:9], v34 offset:18432\n" \
"ds_read_b64_tr_b16 a[10:11], v34 offset:23040\n" \
"ds_read_b64_tr_b16 a[12:13], v34 offset:32\n" \
"ds_read_b64_tr_b16 a[14:15], v34 offset:4640\n" \
"ds_read_b64_tr_b16 a[16:17], v34 offset:9248\n" \
"ds_read_b64_tr_b16 a[18:19], v34 offset:13856\n" \
"buffer_load_dword v37, v38, s[56:59], s64 offen sc1\n" \
"ds_read_b64_tr_b16 a[20:21], v34 offset:18464\n" \
"ds_read_b64_tr_b16 a[22:23], v34 offset:23072\n" \
"ds_read_b64_tr_b16 a[24:25], v34 offset:64\n" \
"ds_read_b64_tr_b16 a[26:27], v34 offset:4672\n" \
"ds_read_b64_tr_b16 a[28:29], v34 offset:9280\n" \
"ds_read_b64_tr_b16 a[30:31], v34 offset:13888\n" \
"ds_read_b64_tr_b16 a[192:193], v34 offset:18496\n" \
"ds_read_b64_tr_b16 a[194:195], v34 offset:23104\n" \
"ds_read_b64_tr_b16 a[196:197], v34 offset:96\n" \
"ds_read_b64_tr_b16 a[198:199], v34 offset:4704\n" \
"ds_read_b64_tr_b16 a[200:201], v34 offset:9312\n" \
"ds_read_b64_tr_b16 a[202:203], v34 offset:13920\n" \
"ds_read_b64_tr_b16 a[204:205], v34 offset:18528\n" \
"ds_read_b64_tr_b16 a[206:207], v34 offset:23136\n" \
"ds_read_b64_tr_b16 a[208:209], v34 offset:128\n" \
"ds_read_b64_tr_b16 a[210:211], v34 offset:4736\n" \
"ds_read_b64_tr_b16 a[212:213], v34 offset:9344\n" \
"ds_read_b64_tr_b16 a[214:215], v34 offset:13952\n" \
"ds_read_b64_tr_b16 a[216:217], v34 offset:18560\n" \
"ds_read_b64_tr_b16 a[218:219], v34 offset:23168\n" \
"v_readlane_b32 s65, v11, 23\n" \
"s_add_u32 m0, s99, 0x6c00\n" \
"v_readlane_b32 s66, v11, 26\n" \
"v_add_u32_e32 v32, 0x49000, v20\n" \
"v_lshrrev_b32_e32 v44, 9, v20\n" \
"v_mul_u32_u24_e32 v44, 0xb40, v44\n" \
"v_add_u32_e32 v35, v44, v10\n" \
"v_readlane_b32 s67, v11, 24\n" \
"v_readlane_b32 s92, v11, 20\n" \
"v_readlane_b32 s73, v11, 25\n" \
"s_waitcnt vmcnt(18) lgkmcnt(15)\n" \
"v_mfma_f32_16x16x32_bf16 v[112:115], a[0:3], a[220:223], 0\n" \
"buffer_load_dwordx4 v0, s[80:83], s65 offen offset:0 lds\n" \
"v_mfma_f32_16x16x32_bf16 v[112:115], a[4:7], a[224:227], v[112:115]\n" \
"v_readlane_b32 s94, v11, 21\n" \
"v_readlane_b32 s84, v11, 7\n" \
"v_mfma_f32_16x16x32_bf16 v[112:115], a[8:11], a[228:231], v[112:115]\n" \
"v_readlane_b32 s85, v11, 8\n" \
"s_mov_b32 s86, 0xa1ca0\n" \
"v_mfma_f32_16x16x32_bf16 v[116:119], a[8:11], a[244:247], 0\n" \
"s_mov_b32 s87, 0x20000\n" \
"v_readlane_b32 s68, v11, 27\n" \
"v_mfma_f32_16x16x32_bf16 v[116:119], a[4:7], a[240:243], v[116:119]\n" \
"v_readlane_b32 s69, v11, 28\n" \
"v_readlane_b32 s72, v11, 30\n" \
"v_mfma_f32_16x16x32_bf16 v[116:119], a[0:3], a[236:239], v[116:119]\n" \
"buffer_load_dwordx4 v1, s[80:83], s65 offen offset:1024 lds\n" \
"v_mfma_f32_16x16x32_bf16 v[184:187], a[0:3], v[64:67], 0\n" \
"v_readlane_b32 s93, v11, 31\n" \
"v_lshlrev_b32_e32 v41, 12, v20\n" \
"v_mfma_f32_16x16x32_bf16 v[184:187], a[4:7], v[68:71], v[184:187]\n" \
"v_readlane_b32 s63, v11, 37\n" \
"v_bfe_u32 v40, v20, 2, 7\n" \
"v_mfma_f32_16x16x32_bf16 v[184:187], a[8:11], v[72:75], v[184:187]\n" \
"s_add_u32 s74, s63, 0x3800\n" \
"v_readlane_b32 s95, v11, 34\n" \
"v_mfma_f32_16x16x32_bf16 v[188:191], a[8:11], v[96:99], 0\n" \
"ds_read_b64_tr_b16 a[8:9], v34 offset:18592\n" \
"ds_read_b64_tr_b16 a[10:11], v34 offset:23200\n" \
"v_mfma_f32_16x16x32_bf16 v[188:191], a[4:7], v[84:87], v[188:191]\n" \
"ds_read_b64_tr_b16 a[4:5], v34 offset:9376\n" \
"ds_read_b64_tr_b16 a[6:7], v34 offset:13984\n" \
"v_mfma_f32_16x16x32_bf16 v[188:191], a[0:3], v[80:83], v[188:191]\n" \
"ds_read_b64_tr_b16 a[0:1], v34 offset:160\n" \
"ds_read_b64_tr_b16 a[2:3], v34 offset:4768\n" \
"v_mfma_f32_16x16x32_bf16 v[120:123], a[12:15], a[220:223], 0\n" \
"buffer_load_dwordx4 v2, s[80:83], s65 offen offset:2048 lds\n" \
"v_mfma_f32_16x16x32_bf16 v[120:123], a[16:19], a[224:227], v[120:123]\n" \
"v_readlane_b32 s97, v11, 36\n" \
"v_bfe_u32 v44, v20, 4, 4\n" \
"v_mfma_f32_16x16x32_bf16 v[120:123], a[20:23], a[228:231], v[120:123]\n" \
"v_bfe_u32 v45, v20, 8, 2\n" \
"v_mul_u32_u24_e32 v42, 0x480, v44\n" \
"v_mfma_f32_16x16x32_bf16 v[124:127], a[20:23], a[244:247], 0\n" \
"v_lshl_add_u32 v42, v45, 3, v42\n" \
"v_and_b32_e32 v43, 1, v45\n" \
"v_mfma_f32_16x16x32_bf16 v[124:127], a[16:19], a[240:243], v[124:127]\n" \
"v_lshrrev_b32_e32 v44, 1, v44\n" \
"v_lshlrev_b32_e32 v45, 1, v45\n" \
"v_mfma_f32_16x16x32_bf16 v[124:127], a[12:15], a[236:239], v[124:127]\n" \
"buffer_load_dwordx4 v3, s[80:83], s65 offen offset:3072 lds\n" \
"v_mfma_f32_16x16x32_bf16 v[192:195], a[12:15], v[64:67], 0\n" \
"s_add_u32 m0, s99, 0x7c00\n" \
"v_sub_u32_e32 v44, v44, v45\n" \
"v_mfma_f32_16x16x32_bf16 v[192:195], a[16:19], v[68:71], v[192:195]\n" \
"v_and_b32_e32 v45, 16, v20\n" \
"v_mov_b32_e32 v46, 0x3f80\n" \
"v_mfma_f32_16x16x32_bf16 v[192:195], a[20:23], v[72:75], v[192:195]\n" \
"v_lshlrev_b32_e32 v45, v45, v46\n" \
"v_mov_b32_e32 v46, s62\n" \
"v_mfma_f32_16x16x32_bf16 v[196:199], a[20:23], v[96:99], 0\n" \
"ds_read_b64_tr_b16 a[20:21], v34 offset:18624\n" \
"ds_read_b64_tr_b16 a[22:23], v34 offset:23232\n" \
"v_mfma_f32_16x16x32_bf16 v[196:199], a[16:19], v[84:87], v[196:199]\n" \
"ds_read_b64_tr_b16 a[16:17], v34 offset:9408\n" \
"ds_read_b64_tr_b16 a[18:19], v34 offset:14016\n" \
"v_mfma_f32_16x16x32_bf16 v[196:199], a[12:15], v[80:83], v[196:199]\n" \
"ds_read_b64_tr_b16 a[12:13], v34 offset:192\n" \
"ds_read_b64_tr_b16 a[14:15], v34 offset:4800\n" \
"v_mfma_f32_16x16x32_bf16 v[128:131], a[24:27], a[220:223], 0\n" \
"v_readlane_b32 s96, v11, 35\n" \
"s_waitcnt lgkmcnt(8)\n" \
"v_mfma_f32_16x16x32_bf16 v[128:131], a[28:31], a[224:227], v[128:131]\n" \
"buffer_load_dwordx4 v4, s[80:83], s65 offen offset:0 lds\n" \
"v_mfma_f32_16x16x32_bf16 v[128:131], a[192:195], a[228:231], v[128:131]\n" \
"v_readlane_b32 s100, v11, 6\n" \
"v_readlane_b32 s75, v11, 38\n" \
"v_mfma_f32_16x16x32_bf16 v[132:135], a[192:195], a[244:247], 0\n" \
"v_add_u32_e32 v54, 0x3000, v20\n" \
"v_mov_b32_e32 v52, 0\n" \
"v_mfma_f32_16x16x32_bf16 v[132:135], a[28:31], a[240:243], v[132:135]\n" \
"v_mov_b32_e32 v53, 0\n" \
"v_mov_b32_e32 v48, 0\n" \
"v_mfma_f32_16x16x32_bf16 v[132:135], a[24:27], a[236:239], v[132:135]\n" \
"v_mov_b32_e32 v49, 0\n" \
"v_mul_u32_u24_e32 v43, 0x8ff8, v43\n" \
"v_mfma_f32_16x16x32_bf16 v[200:203], a[24:27], v[64:67], 0\n" \
"buffer_load_dwordx4 v5, s[80:83], s65 offen offset:1024 lds\n" \
"v_mfma_f32_16x16x32_bf16 v[200:203], a[28:31], v[68:71], v[200:203]\n" \
"v_add_u32_e32 v42, v42, v43\n" \
"v_readlane_b32 s88, v11, 11\n" \
"v_mfma_f32_16x16x32_bf16 v[200:203], a[192:195], v[72:75], v[200:203]\n" \
"v_readlane_b32 s89, v11, 12\n" \
"s_mov_b32 s90, 0x240000\n" \
"v_mfma_f32_16x16x32_bf16 v[204:207], a[192:195], v[96:99], 0\n" \
"ds_read_b64_tr_b16 a[192:193], v34 offset:18656\n" \
"ds_read_b64_tr_b16 a[194:195], v34 offset:23264\n" \
"v_mfma_f32_16x16x32_bf16 v[204:207], a[28:31], v[84:87], v[204:207]\n" \
"ds_read_b64_tr_b16 a[28:29], v34 offset:9440\n" \
"ds_read_b64_tr_b16 a[30:31], v34 offset:14048\n" \
"v_mfma_f32_16x16x32_bf16 v[204:207], a[24:27], v[80:83], v[204:207]\n" \
"ds_read_b64_tr_b16 a[24:25], v34 offset:224\n" \
"ds_read_b64_tr_b16 a[26:27], v34 offset:4832\n" \
"v_mfma_f32_16x16x32_bf16 v[136:139], a[196:199], a[220:223], 0\n" \
"buffer_load_dwordx4 v6, s[80:83], s65 offen offset:2048 lds\n" \
"v_mfma_f32_16x16x32_bf16 v[136:139], a[200:203], a[224:227], v[136:139]\n" \
"s_mov_b32 s91, 0x20000\n" \
"v_mfma_f32_16x16x32_bf16 v[136:139], a[204:207], a[228:231], v[136:139]\n" \
"v_mfma_f32_16x16x32_bf16 v[140:143], a[204:207], a[244:247], 0\n" \
"v_mfma_f32_16x16x32_bf16 v[140:143], a[200:203], a[240:243], v[140:143]\n" \
"v_mfma_f32_16x16x32_bf16 v[140:143], a[196:199], a[236:239], v[140:143]\n" \
"buffer_load_dwordx4 v7, s[80:83], s65 offen offset:3072 lds\n" \
"v_mfma_f32_16x16x32_bf16 v[208:211], a[196:199], v[64:67], 0\n" \
"s_add_u32 m0, s99, 0x8c00\n" \
"v_mfma_f32_16x16x32_bf16 v[208:211], a[200:203], v[68:71], v[208:211]\n" \
"v_mfma_f32_16x16x32_bf16 v[208:211], a[204:207], v[72:75], v[208:211]\n" \
"v_mfma_f32_16x16x32_bf16 v[212:215], a[204:207], v[96:99], 0\n" \
"ds_read_b64_tr_b16 a[204:205], v34 offset:18688\n" \
"ds_read_b64_tr_b16 a[206:207], v34 offset:23296\n" \
"v_mfma_f32_16x16x32_bf16 v[212:215], a[200:203], v[84:87], v[212:215]\n" \
"ds_read_b64_tr_b16 a[200:201], v34 offset:9472\n" \
"ds_read_b64_tr_b16 a[202:203], v34 offset:14080\n" \
"v_mfma_f32_16x16x32_bf16 v[212:215], a[196:199], v[80:83], v[212:215]\n" \
"ds_read_b64_tr_b16 a[196:197], v34 offset:256\n" \
"ds_read_b64_tr_b16 a[198:199], v34 offset:4864\n" \
"v_mfma_f32_16x16x32_bf16 v[144:147], a[208:211], a[220:223], 0\n" \
"buffer_load_dwordx4 v8, s[80:83], s65 offen offset:0 lds\n" \
"v_mfma_f32_16x16x32_bf16 v[144:147], a[212:215], a[224:227], v[144:147]\n" \
"s_waitcnt vmcnt(9)\n" \
"v_cmp_ne_u32_e32 vcc, s62, v37\n" \
"v_mfma_f32_16x16x32_bf16 v[144:147], a[216:219], a[228:231], v[144:147]\n" \
"s_add_u32 m0, s99, 0x14400\n" \
"v_readlane_b32 s65, v11, 42\n" \
"v_mfma_f32_16x16x32_bf16 v[148:151], a[216:219], a[244:247], 0\n" \
"v_mfma_f32_16x16x32_bf16 v[148:151], a[212:215], a[240:243], v[148:151]\n" \
"s_cbranch_vccnz L_sa_%=\n" \
"L_ra_%=:\n" \
"v_readlane_b32 s64, v11, 29\n" \
"v_mfma_f32_16x16x32_bf16 v[148:151], a[208:211], a[236:239], v[148:151]\n" \
"buffer_load_dwordx4 a[64:67], v20, s[56:59], s66 offen offset:0 sc1\n" \
"v_mfma_f32_16x16x32_bf16 v[216:219], a[208:211], v[64:67], 0\n" \
"v_mfma_f32_16x16x32_bf16 v[216:219], a[212:215], v[68:71], v[216:219]\n" \
"v_mfma_f32_16x16x32_bf16 v[216:219], a[216:219], v[72:75], v[216:219]\n" \
"v_mfma_f32_16x16x32_bf16 v[220:223], a[216:219], v[96:99], 0\n" \
"v_mfma_f32_16x16x32_bf16 v[220:223], a[212:215], v[84:87], v[220:223]\n" \
"buffer_load_dwordx4 a[68:71], v20, s[56:59], s66 offen offset:1024 sc1\n" \
"v_mfma_f32_16x16x32_bf16 v[220:223], a[208:211], v[80:83], v[220:223]\n" \
"s_waitcnt lgkmcnt(5)\n" \
"v_mfma_f32_16x16x32_bf16 v[152:155], a[0:3], a[220:223], 0\n" \
"v_mfma_f32_16x16x32_bf16 v[152:155], a[4:7], a[224:227], v[152:155]\n" \
"v_mfma_f32_16x16x32_bf16 v[152:155], a[8:11], a[228:231], v[152:155]\n" \
"v_mfma_f32_16x16x32_bf16 v[156:159], a[8:11], a[244:247], 0\n" \
"buffer_load_dwordx4 a[84:87], v32, s[56:59], s66 offen offset:1024 sc1\n" \
"v_mfma_f32_16x16x32_bf16 v[156:159], a[4:7], a[240:243], v[156:159]\n" \
"v_mfma_f32_16x16x32_bf16 v[156:159], a[0:3], a[236:239], v[156:159]\n" \
"v_mfma_f32_16x16x32_bf16 v[224:227], a[0:3], v[64:67], 0\n" \
"v_mfma_f32_16x16x32_bf16 v[224:227], a[4:7], v[68:71], v[224:227]\n" \
"v_mfma_f32_16x16x32_bf16 v[224:227], a[8:11], v[72:75], v[224:227]\n" \
"buffer_load_dwordx4 a[80:83], v32, s[56:59], s66 offen offset:0 sc1\n" \
"v_mfma_f32_16x16x32_bf16 v[228:231], a[8:11], v[96:99], 0\n" \
"v_mfma_f32_16x16x32_bf16 v[228:231], a[4:7], v[84:87], v[228:231]\n" \
"v_mfma_f32_16x16x32_bf16 v[228:231], a[0:3], v[80:83], v[228:231]\n" \
"v_mfma_f32_16x16x32_bf16 v[160:163], a[12:15], a[220:223], 0\n" \
"v_mfma_f32_16x16x32_bf16 v[160:163], a[16:19], a[224:227], v[160:163]\n" \
"buffer_load_dwordx4 a[72:75], v20, s[56:59], s66 offen offset:2048 sc1\n" \
"v_mfma_f32_16x16x32_bf16 v[160:163], a[20:23], a[228:231], v[160:163]\n" \
"v_mfma_f32_16x16x32_bf16 v[164:167], a[20:23], a[244:247], 0\n" \
"v_mfma_f32_16x16x32_bf16 v[164:167], a[16:19], a[240:243], v[164:167]\n" \
"v_mfma_f32_16x16x32_bf16 v[164:167], a[12:15], a[236:239], v[164:167]\n" \
"v_mfma_f32_16x16x32_bf16 v[232:235], a[12:15], v[64:67], 0\n" \
"buffer_load_dwordx4 a[76:79], v20, s[56:59], s66 offen offset:3072 sc1\n" \
"v_mfma_f32_16x16x32_bf16 v[232:235], a[16:19], v[68:71], v[232:235]\n" \
"v_mfma_f32_16x16x32_bf16 v[232:235], a[20:23], v[72:75], v[232:235]\n" \
"v_mfma_f32_16x16x32_bf16 v[236:239], a[20:23], v[96:99], 0\n" \
"v_mfma_f32_16x16x32_bf16 v[236:239], a[16:19], v[84:87], v[236:239]\n" \
"v_mfma_f32_16x16x32_bf16 v[236:239], a[12:15], v[80:83], v[236:239]\n" \
"buffer_load_dwordx4 a[92:95], v32, s[56:59], s66 offen offset:3072 sc1\n" \
"s_waitcnt lgkmcnt(0)\n" \
"s_barrier\n" \
"v_mfma_f32_16x16x32_bf16 v[168:171], a[24:27], a[220:223], 0\n" \
"ds_read_b64_tr_b16 a[208:209], v33 offset:9216\n" \
"ds_read_b64_tr_b16 a[210:211], v33 offset:13824\n" \
"v_mfma_f32_16x16x32_bf16 v[168:171], a[28:31], a[224:227], v[168:171]\n" \
"ds_read_b64_tr_b16 a[212:213], v33 offset:9248\n" \
"ds_read_b64_tr_b16 a[214:215], v33 offset:13856\n" \
"v_mfma_f32_16x16x32_bf16 v[168:171], a[192:195], a[228:231], v[168:171]\n" \
"ds_read_b64_tr_b16 a[216:217], v33 offset:9280\n" \
"ds_read_b64_tr_b16 a[218:219], v33 offset:13888\n" \
"s_mov_b64 exec, 1\n" \
"buffer_store_dword v39, off, s[56:59], s70 offset:0 sc1\n" \
"s_mov_b64 exec, -1\n" \
"v_mfma_f32_16x16x32_bf16 v[172:175], a[192:195], a[244:247], 0\n" \
"ds_read_b64_tr_b16 a[0:1], v33 offset:9312\n" \
"ds_read_b64_tr_b16 a[2:3], v33 offset:13920\n" \
"v_mfma_f32_16x16x32_bf16 v[172:175], a[28:31], a[240:243], v[172:175]\n" \
"ds_read_b64_tr_b16 a[4:5], v33 offset:9344\n" \
"ds_read_b64_tr_b16 a[6:7], v33 offset:13952\n" \
"v_mfma_f32_16x16x32_bf16 v[172:175], a[24:27], a[236:239], v[172:175]\n" \
"ds_read_b64_tr_b16 a[8:9], v33 offset:9376\n" \
"ds_read_b64_tr_b16 a[10:11], v33 offset:13984\n" \
"v_mfma_f32_16x16x32_bf16 v[240:243], a[24:27], v[64:67], 0\n" \
"ds_read_b64_tr_b16 a[12:13], v33 offset:9408\n" \
"ds_read_b64_tr_b16 a[14:15], v33 offset:14016\n" \
"v_mfma_f32_16x16x32_bf16 v[240:243], a[28:31], v[68:71], v[240:243]\n" \
"ds_read_b64_tr_b16 a[16:17], v33 offset:9440\n" \
"ds_read_b64_tr_b16 a[18:19], v33 offset:14048\n" \
"v_mfma_f32_16x16x32_bf16 v[240:243], a[192:195], v[72:75], v[240:243]\n" \
"ds_read_b64_tr_b16 a[20:21], v33 offset:9472\n" \
"ds_read_b64_tr_b16 a[22:23], v33 offset:14080\n" \
"v_mfma_f32_16x16x32_bf16 v[244:247], a[192:195], v[96:99], 0\n" \
"ds_read_b64_tr_b16 a[192:193], v35 offset:32\n" \
"ds_read_b64_tr_b16 a[194:195], v35 offset:2336\n" \
"v_mfma_f32_16x16x32_bf16 v[244:247], a[28:31], v[84:87], v[244:247]\n" \
"ds_read_b64_tr_b16 a[28:29], v35 offset:10368\n" \
"ds_read_b64_tr_b16 a[30:31], v35 offset:12672\n" \
"v_mfma_f32_16x16x32_bf16 v[244:247], a[24:27], v[80:83], v[244:247]\n" \
"ds_read_b64_tr_b16 a[24:25], v35 offset:0\n" \
"ds_read_b64_tr_b16 a[26:27], v35 offset:2304\n" \
"v_mfma_f32_16x16x32_bf16 v[176:179], a[196:199], a[220:223], 0\n" \
"buffer_load_dwordx4 a[88:91], v32, s[56:59], s66 offen offset:2048 sc1\n" \
"v_mfma_f32_16x16x32_bf16 v[176:179], a[200:203], a[224:227], v[176:179]\n" \
"v_readlane_b32 s66, v11, 43\n" \
"v_readlane_b32 s70, v11, 5\n" \
"v_mfma_f32_16x16x32_bf16 v[176:179], a[204:207], a[228:231], v[176:179]\n" \
"v_mfma_f32_16x16x32_bf16 v[180:183], a[204:207], a[244:247], 0\n" \
"v_mfma_f32_16x16x32_bf16 v[180:183], a[200:203], a[240:243], v[180:183]\n" \
"v_mfma_f32_16x16x32_bf16 v[180:183], a[196:199], a[236:239], v[180:183]\n" \
"buffer_load_dwordx4 v0, s[80:83], s67 offen offset:0 lds\n" \
"v_mfma_f32_16x16x32_bf16 v[248:251], a[196:199], v[64:67], 0\n" \
"L_pf21:\n" \
"s_cbranch_execz L_pf23\n" \
"s_nop 0\n" \
"v_mfma_f32_16x16x32_bf16 v[248:251], a[200:203], v[68:71], v[248:251]\n" \
"v_mfma_f32_16x16x32_bf16 v[248:251], a[204:207], v[72:75], v[248:251]\n" \
"v_mfma_f32_16x16x32_bf16 v[252:255], a[204:207], v[96:99], 0\n" \
"ds_read_b64_tr_b16 a[204:205], v35 offset:10432\n" \
"ds_read_b64_tr_b16 a[206:207], v35 offset:12736\n" \
"v_mfma_f32_16x16x32_bf16 v[252:255], a[200:203], v[84:87], v[252:255]\n" \
"ds_read_b64_tr_b16 a[200:201], v35 offset:64\n" \
"ds_read_b64_tr_b16 a[202:203], v35 offset:2368\n" \
"v_mfma_f32_16x16x32_bf16 v[252:255], a[196:199], v[80:83], v[252:255]\n" \
"ds_read_b64_tr_b16 a[196:197], v35 offset:10400\n" \
"s_waitcnt lgkmcnt(9)\n" \
"v_mfma_f32_16x16x32_bf16 v[112:115], a[208:211], a[232:235], v[112:115]\n" \
"buffer_load_dwordx4 v1, s[80:83], s67 offen offset:1024 lds\n" \
"v_mfma_f32_16x16x32_bf16 v[116:119], a[208:211], a[248:251], v[116:119]\n" \
"ds_read_b64_tr_b16 a[198:199], v35 offset:12704\n" \
"v_mfma_f32_16x16x32_bf16 v[184:187], a[208:211], v[76:79], v[184:187]\n" \
"v_mfma_f32_16x16x32_bf16 v[188:191], a[208:211], v[100:103], v[188:191]\n" \
"ds_read_b64_tr_b16 a[208:209], v35 offset:96\n" \
"ds_read_b64_tr_b16 a[210:211], v35 offset:2400\n" \
"v_mfma_f32_16x16x32_bf16 v[120:123], a[212:215], a[232:235], v[120:123]\n" \
"v_mfma_f32_16x16x32_bf16 v[124:127], a[212:215], a[248:251], v[124:127]\n" \
"buffer_load_dwordx4 v2, s[80:83], s67 offen offset:2048 lds\n" \
"v_mfma_f32_16x16x32_bf16 v[192:195], a[212:215], v[76:79], v[192:195]\n" \
"v_mfma_f32_16x16x32_bf16 v[196:199], a[212:215], v[100:103], v[196:199]\n" \
"ds_read_b64_tr_b16 a[212:213], v35 offset:10464\n" \
"ds_read_b64_tr_b16 a[214:215], v35 offset:12768\n" \
"v_mfma_f32_16x16x32_bf16 v[128:131], a[216:219], a[232:235], v[128:131]\n" \
"v_mfma_f32_16x16x32_bf16 v[132:135], a[216:219], a[248:251], v[132:135]\n" \
"v_mfma_f32_16x16x32_bf16 v[200:203], a[216:219], v[76:79], v[200:203]\n" \
"buffer_load_dwordx4 v3, s[80:83], s67 offen offset:3072 lds\n" \
"v_mfma_f32_16x16x32_bf16 v[204:207], a[216:219], v[100:103], v[204:207]\n" \
"ds_read_b64_tr_b16 a[216:217], v35 offset:128\n" \
"ds_read_b64_tr_b16 a[218:219], v35 offset:2432\n" \
"v_mfma_f32_16x16x32_bf16 v[136:139], a[0:3], a[232:235], v[136:139]\n" \
"s_add_u32 m0, s99, 0x15400\n" \
"v_mfma_f32_16x16x32_bf16 v[140:143], a[0:3], a[248:251], v[140:143]\n" \
"v_mfma_f32_16x16x32_bf16 v[208:211], a[0:3], v[76:79], v[208:211]\n" \
"v_mfma_f32_16x16x32_bf16 v[212:215], a[0:3], v[100:103], v[212:215]\n" \
"ds_read_b64_tr_b16 a[0:1], v35 offset:10496\n" \
"ds_read_b64_tr_b16 a[2:3], v35 offset:12800\n" \
"v_mfma_f32_16x16x32_bf16 v[144:147], a[4:7], a[232:235], v[144:147]\n" \
"buffer_load_dwordx4 v4, s[80:83], s67 offen offset:0 lds\n" \
"v_mfma_f32_16x16x32_bf16 v[148:151], a[4:7], a[248:251], v[148:151]\n" \
"v_mfma_f32_16x16x32_bf16 v[216:219], a[4:7], v[76:79], v[216:219]\n" \
"v_mfma_f32_16x16x32_bf16 v[220:223], a[4:7], v[100:103], v[220:223]\n" \
"ds_read_b64_tr_b16 a[4:5], v35 offset:160\n" \
"ds_read_b64_tr_b16 a[6:7], v35 offset:2464\n" \
"v_mfma_f32_16x16x32_bf16 v[152:155], a[8:11], a[232:235], v[152:155]\n" \
"v_mfma_f32_16x16x32_bf16 v[156:159], a[8:11], a[248:251], v[156:159]\n" \
"buffer_load_dwordx4 v5, s[80:83], s67 offen offset:1024 lds\n" \
"v_mfma_f32_16x16x32_bf16 v[224:227], a[8:11], v[76:79], v[224:227]\n" \
"v_mfma_f32_16x16x32_bf16 v[228:231], a[8:11], v[100:103], v[228:231]\n" \
"ds_read_b64_tr_b16 a[8:9], v35 offset:10528\n" \
"ds_read_b64_tr_b16 a[10:11], v35 offset:12832\n" \
"v_mfma_f32_16x16x32_bf16 v[160:163], a[12:15], a[232:235], v[160:163]\n" \
"v_mfma_f32_16x16x32_bf16 v[164:167], a[12:15], a[248:251], v[164:167]\n" \
"v_mfma_f32_16x16x32_bf16 v[232:235], a[12:15], v[76:79], v[232:235]\n" \
"buffer_load_dwordx4 v6, s[80:83], s67 offen offset:2048 lds\n" \
"v_mfma_f32_16x16x32_bf16 v[236:239], a[12:15], v[100:103], v[236:239]\n" \
"ds_read_b64_tr_b16 a[12:13], v35 offset:192\n" \
"ds_read_b64_tr_b16 a[14:15], v35 offset:2496\n" \
"v_mfma_f32_16x16x32_bf16 v[168:171], a[16:19], a[232:235], v[168:171]\n" \
"v_mfma_f32_16x16x32_bf16 v[172:175], a[16:19], a[248:251], v[172:175]\n" \
"v_mfma_f32_16x16x32_bf16 v[240:243], a[16:19], v[76:79], v[240:243]\n" \
"v_mfma_f32_16x16x32_bf16 v[244:247], a[16:19], v[100:103], v[244:247]\n" \
"buffer_load_dwordx4 v7, s[80:83], s67 offen offset:3072 lds\n" \
"v_mfma_f32_16x16x32_bf16 v[176:179], a[20:23], a[232:235], v[176:179]\n" \
"s_add_u32 m0, s99, 0x16400\n" \
"ds_read_b64_tr_b16 a[16:17], v35 offset:10560\n" \
"v_mfma_f32_16x16x32_bf16 v[180:183], a[20:23], a[248:251], v[180:183]\n" \
"ds_read_b64_tr_b16 a[18:19], v35 offset:12864\n" \
"v_mfma_f32_16x16x32_bf16 v[248:251], a[20:23], v[76:79], v[248:251]\n" \
"v_mfma_f32_16x16x32_bf16 v[252:255], a[20:23], v[100:103], v[252:255]\n" \
"ds_read_b64_tr_b16 a[20:21], v35 offset:224\n" \
"s_waitcnt lgkmcnt(7)\n" \
"v_mfma_f32_16x16x32_bf16 v[112:115], a[24:27], a[32:35], v[112:115]\n" \
"buffer_load_dwordx4 v8, s[80:83], s67 offen offset:0 lds\n" \
"v_mfma_f32_16x16x32_bf16 v[112:115], a[28:31], a[36:39], v[112:115]\n" \
"s_add_u32 m0, s92, 0x0\n" \
"ds_read_b64_tr_b16 a[22:23], v35 offset:2528\n" \
"v_mfma_f32_16x16x32_bf16 v[116:119], a[28:31], a[44:47], v[116:119]\n" \
"v_readlane_b32 s67, v11, 44\n" \
"v_mfma_f32_16x16x32_bf16 v[116:119], a[24:27], a[40:43], v[116:119]\n" \
"v_mfma_f32_16x16x32_bf16 v[184:187], a[24:27], a[48:51], v[184:187]\n" \
"v_mfma_f32_16x16x32_bf16 v[184:187], a[28:31], a[52:55], v[184:187]\n" \
"buffer_load_dword v47, v38, s[56:59], s71 offen sc1\n" \
"v_mfma_f32_16x16x32_bf16 v[188:191], a[28:31], a[60:63], v[188:191]\n" \
"ds_read_b64_tr_b16 a[28:29], v35 offset:256\n" \
"ds_read_b64_tr_b16 a[30:31], v35 offset:2560\n" \
"v_mfma_f32_16x16x32_bf16 v[188:191], a[24:27], a[56:59], v[188:191]\n" \
"ds_read_b64_tr_b16 a[24:25], v35 offset:10592\n" \
"ds_read_b64_tr_b16 a[26:27], v35 offset:12896\n" \
"v_mfma_f32_16x16x32_bf16 v[120:123], a[192:195], a[32:35], v[120:123]\n" \
"v_mfma_f32_16x16x32_bf16 v[120:123], a[196:199], a[36:39], v[120:123]\n" \
"v_mfma_f32_16x16x32_bf16 v[124:127], a[196:199], a[44:47], v[124:127]\n" \
"buffer_load_dwordx4 v0, s[80:83], s73 offen offset:0 lds\n" \
"v_mfma_f32_16x16x32_bf16 v[124:127], a[192:195], a[40:43], v[124:127]\n" \
"v_mfma_f32_16x16x32_bf16 v[192:195], a[192:195], a[48:51], v[192:195]\n" \
"v_mfma_f32_16x16x32_bf16 v[192:195], a[196:199], a[52:55], v[192:195]\n" \
"v_mfma_f32_16x16x32_bf16 v[196:199], a[196:199], a[60:63], v[196:199]\n" \
"v_mfma_f32_16x16x32_bf16 v[196:199], a[192:195], a[56:59], v[196:199]\n" \
"buffer_load_dwordx4 v1, s[80:83], s73 offen offset:1024 lds\n" \
"v_mfma_f32_16x16x32_bf16 v[128:131], a[200:203], a[32:35], v[128:131]\n" \
"ds_read_b64_tr_b16 a[192:193], v35 offset:10624\n" \
"ds_read_b64_tr_b16 a[194:195], v35 offset:12928\n" \
"v_mfma_f32_16x16x32_bf16 v[128:131], a[204:207], a[36:39], v[128:131]\n" \
"v_mfma_f32_16x16x32_bf16 v[132:135], a[204:207], a[44:47], v[132:135]\n" \
"v_mfma_f32_16x16x32_bf16 v[132:135], a[200:203], a[40:43], v[132:135]\n" \
"v_mfma_f32_16x16x32_bf16 v[200:203], a[200:203], a[48:51], v[200:203]\n" \
"buffer_load_dwordx4 v2, s[80:83], s73 offen offset:2048 lds\n" \
"v_mfma_f32_16x16x32_bf16 v[200:203], a[204:207], a[52:55], v[200:203]\n" \
"v_mfma_f32_16x16x32_bf16 v[204:207], a[204:207], a[60:63], v[204:207]\n" \
"v_mfma_f32_16x16x32_bf16 v[204:207], a[200:203], a[56:59], v[204:207]\n" \
"v_mfma_f32_16x16x32_bf16 v[136:139], a[208:211], a[32:35], v[136:139]\n" \
"v_mfma_f32_16x16x32_bf16 v[136:139], a[212:215], a[36:39], v[136:139]\n" \
"buffer_load_dwordx4 v3, s[80:83], s73 offen offset:3072 lds\n" \
"v_mfma_f32_16x16x32_bf16 v[140:143], a[212:215], a[44:47], v[140:143]\n" \
"s_add_u32 m0, s92, 0x1000\n" \
"v_mfma_f32_16x16x32_bf16 v[140:143], a[208:211], a[40:43], v[140:143]\n" \
"v_mfma_f32_16x16x32_bf16 v[208:211], a[208:211], a[48:51], v[208:211]\n" \
"v_mfma_f32_16x16x32_bf16 v[208:211], a[212:215], a[52:55], v[208:211]\n" \
"v_mfma_f32_16x16x32_bf16 v[212:215], a[212:215], a[60:63], v[212:215]\n" \
"buffer_load_dwordx4 v4, s[80:83], s73 offen offset:0 lds\n" \
"v_mfma_f32_16x16x32_bf16 v[212:215], a[208:211], a[56:59], v[212:215]\n" \
"v_mfma_f32_16x16x32_bf16 v[144:147], a[216:219], a[32:35], v[144:147]\n" \
"v_mfma_f32_16x16x32_bf16 v[144:147], a[0:3], a[36:39], v[144:147]\n" \
"v_mfma_f32_16x16x32_bf16 v[148:151], a[0:3], a[44:47], v[148:151]\n" \
"v_mfma_f32_16x16x32_bf16 v[148:151], a[216:219], a[40:43], v[148:151]\n" \
"buffer_load_dwordx4 v5, s[80:83], s73 offen offset:1024 lds\n" \
"v_mfma_f32_16x16x32_bf16 v[216:219], a[216:219], a[48:51], v[216:219]\n" \
"v_mfma_f32_16x16x32_bf16 v[216:219], a[0:3], a[52:55], v[216:219]\n" \
"v_mfma_f32_16x16x32_bf16 v[220:223], a[0:3], a[60:63], v[220:223]\n" \
"v_mfma_f32_16x16x32_bf16 v[220:223], a[216:219], a[56:59], v[220:223]\n" \
"v_mfma_f32_16x16x32_bf16 v[152:155], a[4:7], a[32:35], v[152:155]\n" \
"buffer_load_dwordx4 v6, s[80:83], s73 offen offset:2048 lds\n" \
"s_waitcnt lgkmcnt(0)\n" \
"v_mfma_f32_16x16x32_bf16 v[152:155], a[8:11], a[36:39], v[152:155]\n" \
"v_mfma_f32_16x16x32_bf16 v[156:159], a[8:11], a[44:47], v[156:159]\n" \
"v_mfma_f32_16x16x32_bf16 v[156:159], a[4:7], a[40:43], v[156:159]\n" \
"v_mfma_f32_16x16x32_bf16 v[224:227], a[4:7], a[48:51], v[224:227]\n" \
"buffer_load_dwordx4 v7, s[80:83], s73 offen offset:3072 lds\n" \
"s_add_u32 m0, s92, 0x2000\n" \
"v_mfma_f32_16x16x32_bf16 v[224:227], a[8:11], a[52:55], v[224:227]\n" \
"v_mfma_f32_16x16x32_bf16 v[228:231], a[8:11], a[60:63], v[228:231]\n" \
"v_mfma_f32_16x16x32_bf16 v[228:231], a[4:7], a[56:59], v[228:231]\n" \
"s_waitcnt vmcnt(23)\n" \
"s_barrier\n" \
"buffer_load_dwordx4 v8, s[80:83], s73 offen offset:0 lds\n" \
"s_mov_b32 m0, s94\n" \
"v_mfma_f32_16x16x32_bf16 v[160:163], a[12:15], a[32:35], v[160:163]\n" \
"ds_read_b64_tr_b16 a[196:197], v10 offset:27648\n" \
"ds_read_b64_tr_b16 a[198:199], v10 offset:32256\n" \
"v_mfma_f32_16x16x32_bf16 v[160:163], a[16:19], a[36:39], v[160:163]\n" \
"ds_read_b64_tr_b16 a[200:201], v10 offset:36864\n" \
"ds_read_b64_tr_b16 a[202:203], v10 offset:41472\n" \
"v_mfma_f32_16x16x32_bf16 v[164:167], a[16:19], a[44:47], v[164:167]\n" \
"ds_read_b64_tr_b16 a[204:205], v10 offset:27680\n" \
"ds_read_b64_tr_b16 a[206:207], v10 offset:32288\n" \
"v_mfma_f32_16x16x32_bf16 v[164:167], a[12:15], a[40:43], v[164:167]\n" \
"buffer_load_dwordx4 v9, s[84:87], 0 offen lds\n" \
"v_mfma_f32_16x16x32_bf16 v[232:235], a[12:15], a[48:51], v[232:235]\n" \
"ds_read_b64_tr_b16 a[208:209], v10 offset:36896\n" \
"ds_read_b64_tr_b16 a[210:211], v10 offset:41504\n" \
"v_mfma_f32_16x16x32_bf16 v[232:235], a[16:19], a[52:55], v[232:235]\n" \
"ds_read_b64_tr_b16 a[212:213], v10 offset:27712\n" \
"ds_read_b64_tr_b16 a[214:215], v10 offset:32320\n" \
"v_mfma_f32_16x16x32_bf16 v[236:239], a[16:19], a[60:63], v[236:239]\n" \
"ds_read_b64_tr_b16 a[216:217], v10 offset:36928\n" \
"ds_read_b64_tr_b16 a[218:219], v10 offset:41536\n" \
"v_mfma_f32_16x16x32_bf16 v[236:239], a[12:15], a[56:59], v[236:239]\n" \
"ds_read_b64_tr_b16 a[0:1], v10 offset:27744\n" \
"ds_read_b64_tr_b16 a[2:3], v10 offset:32352\n" \
"buffer_load_dwordx4 a[128:131], v20, s[56:59], s68 offen offset:0 sc1\n" \
"v_mfma_f32_16x16x32_bf16 v[168:171], a[20:23], a[32:35], v[168:171]\n" \
"ds_read_b64_tr_b16 a[4:5], v10 offset:36960\n" \
"ds_read_b64_tr_b16 a[6:7], v10 offset:41568\n" \
"v_mfma_f32_16x16x32_bf16 v[168:171], a[24:27], a[36:39], v[168:171]\n" \
"ds_read_b64_tr_b16 a[8:9], v10 offset:27776\n" \
"ds_read_b64_tr_b16 a[10:11], v10 offset:32384\n" \
"v_mfma_f32_16x16x32_bf16 v[172:175], a[24:27], a[44:47], v[172:175]\n" \
"ds_read_b64_tr_b16 a[12:13], v10 offset:36992\n" \
"ds_read_b64_tr_b16 a[14:15], v10 offset:41600\n" \
"v_mfma_f32_16x16x32_bf16 v[172:175], a[20:23], a[40:43], v[172:175]\n" \
"ds_read_b64_tr_b16 a[16:17], v10 offset:27808\n" \
"ds_read_b64_tr_b16 a[18:19], v10 offset:32416\n" \
"buffer_load_dwordx4 a[132:135], v20, s[56:59], s68 offen offset:1024 sc1\n" \
"v_mfma_f32_16x16x32_bf16 v[240:243], a[20:23], a[48:51], v[240:243]\n" \
"s_waitcnt vmcnt(12)\n" \
"v_cmp_ne_u32_e32 vcc, s62, v47\n" \
"v_mfma_f32_16x16x32_bf16 v[240:243], a[24:27], a[52:55], v[240:243]\n" \
"v_readlane_b32 s94, v11, 47\n" \
"s_mov_b32 s84, 0\n" \
"v_mfma_f32_16x16x32_bf16 v[244:247], a[24:27], a[60:63], v[244:247]\n" \
"ds_read_b64_tr_b16 a[24:25], v10 offset:27840\n" \
"ds_read_b64_tr_b16 a[26:27], v10 offset:32448\n" \
"v_mfma_f32_16x16x32_bf16 v[244:247], a[20:23], a[56:59], v[244:247]\n" \
"ds_read_b64_tr_b16 a[20:21], v10 offset:37024\n" \
"ds_read_b64_tr_b16 a[22:23], v10 offset:41632\n" \
"buffer_load_dwordx4 a[136:139], v20, s[56:59], s68 offen offset:2048 sc1\n" \
"v_mfma_f32_16x16x32_bf16 v[176:179], a[28:31], a[32:35], v[176:179]\n" \
"s_cbranch_vccnz L_sb_%=\n" \
"L_rb_%=:\n" \
"v_readlane_b32 s71, v11, 46\n" \
"v_mfma_f32_16x16x32_bf16 v[176:179], a[192:195], a[36:39], v[176:179]\n" \
"v_add_u32_e32 v38, 0x1000, v20\n" \
"v_add_u32_e32 v47, 0x2000, v20\n" \
"v_mfma_f32_16x16x32_bf16 v[180:183], a[192:195], a[44:47], v[180:183]\n" \
"v_cmp_eq_u32_e32 vcc, 0, v44\n" \
"v_mfma_f32_16x16x32_bf16 v[180:183], a[28:31], a[40:43], v[180:183]\n" \
"v_mfma_f32_16x16x32_bf16 v[248:251], a[28:31], a[48:51], v[248:251]\n" \
"buffer_load_dwordx4 a[140:143], v20, s[56:59], s68 offen offset:3072 sc1\n" \
"v_mfma_f32_16x16x32_bf16 v[248:251], a[192:195], a[52:55], v[248:251]\n" \
"v_cndmask_b32_e32 v50, 0, v45, vcc\n" \
"v_cmp_eq_u32_e32 vcc, 1, v44\n" \
"v_mfma_f32_16x16x32_bf16 v[252:255], a[192:195], a[60:63], v[252:255]\n" \
"ds_read_b64_tr_b16 a[192:193], v10 offset:27872\n" \
"ds_read_b64_tr_b16 a[194:195], v10 offset:32480\n" \
"v_mfma_f32_16x16x32_bf16 v[252:255], a[28:31], a[56:59], v[252:255]\n" \
"ds_read_b64_tr_b16 a[28:29], v10 offset:37056\n" \
"s_waitcnt lgkmcnt(9)\n" \
"v_mfma_f32_16x16x32_bf16 v[112:115], a[196:199], a[64:67], v[112:115]\n" \
"ds_read_b64_tr_b16 a[30:31], v10 offset:41664\n" \
"v_cndmask_b32_e32 v51, 0, v45, vcc\n" \
"v_mfma_f32_16x16x32_bf16 v[112:115], a[200:203], a[68:71], v[112:115]\n" \
"buffer_load_dwordx4 a[156:159], v32, s[56:59], s68 offen offset:3072 sc1\n" \
"v_mfma_f32_16x16x32_bf16 v[116:119], a[200:203], a[84:87], v[116:119]\n" \
"ds_read_b64_tr_b16 a[200:201], v10 offset:27904\n" \
"ds_read_b64_tr_b16 a[202:203], v10 offset:32512\n" \
"v_mfma_f32_16x16x32_bf16 v[116:119], a[196:199], a[80:83], v[116:119]\n" \
"ds_read_b64_tr_b16 a[196:197], v10 offset:37088\n" \
"ds_read_b64_tr_b16 a[198:199], v10 offset:41696\n" \
"v_mfma_f32_16x16x32_bf16 v[120:123], a[204:207], a[64:67], v[120:123]\n" \
"v_bfe_u32 v45, v20, 1, 5\n" \
"v_and_b32_e32 v45, 24, v45\n" \
"v_mfma_f32_16x16x32_bf16 v[120:123], a[208:211], a[68:71], v[120:123]\n" \
"v_and_b32_e32 v44, 0x3c0, v20\n" \
"v_add_u32_e32 v45, 0x1d400, v45\n" \
"v_mfma_f32_16x16x32_bf16 v[124:127], a[208:211], a[84:87], v[124:127]\n" \
"buffer_load_dwordx4 a[152:155], v32, s[56:59], s68 offen offset:2048 sc1\n" \
"v_mfma_f32_16x16x32_bf16 v[124:127], a[204:207], a[80:83], v[124:127]\n" \
"ds_read_b64_tr_b16 a[204:205], v10 offset:37120\n" \
"ds_read_b64_tr_b16 a[206:207], v10 offset:41728\n" \
"v_mfma_f32_16x16x32_bf16 v[128:131], a[212:215], a[64:67], v[128:131]\n" \
"ds_read_b64_tr_b16 a[208:209], v10 offset:46080\n" \
"ds_read_b64_tr_b16 a[210:211], v10 offset:50688\n" \
"v_mfma_f32_16x16x32_bf16 v[128:131], a[216:219], a[68:71], v[128:131]\n" \
"v_add_u32_e32 v36, v44, v45\n" \
"v_mfma_f32_16x16x32_bf16 v[132:135], a[216:219], a[84:87], v[132:135]\n" \
"ds_read_b64_tr_b16 a[216:217], v10 offset:46112\n" \
"ds_read_b64_tr_b16 a[218:219], v10 offset:50720\n" \
"buffer_load_dwordx4 a[148:151], v32, s[56:59], s68 offen offset:1024 sc1\n" \
"v_mfma_f32_16x16x32_bf16 v[132:135], a[212:215], a[80:83], v[132:135]\n" \
"ds_read_b64_tr_b16 a[212:213], v10 offset:55296\n" \
"ds_read_b64_tr_b16 a[214:215], v10 offset:59904\n" \
"v_mfma_f32_16x16x32_bf16 v[136:139], a[0:3], a[64:67], v[136:139]\n" \
"v_mfma_f32_16x16x32_bf16 v[136:139], a[4:7], a[68:71], v[136:139]\n" \
"v_mfma_f32_16x16x32_bf16 v[140:143], a[4:7], a[84:87], v[140:143]\n" \
"ds_read_b64_tr_b16 a[4:5], v10 offset:46144\n" \
"ds_read_b64_tr_b16 a[6:7], v10 offset:50752\n" \
"buffer_load_dwordx4 a[144:147], v32, s[56:59], s68 offen offset:0 sc1\n" \
"v_mfma_f32_16x16x32_bf16 v[140:143], a[0:3], a[80:83], v[140:143]\n" \
"ds_read_b64_tr_b16 a[0:1], v10 offset:55328\n" \
"ds_read_b64_tr_b16 a[2:3], v10 offset:59936\n" \
"v_mfma_f32_16x16x32_bf16 v[144:147], a[8:11], a[64:67], v[144:147]\n" \
"v_readlane_b32 s68, v11, 45\n" \
"v_mfma_f32_16x16x32_bf16 v[144:147], a[12:15], a[68:71], v[144:147]\n" \
"v_mfma_f32_16x16x32_bf16 v[148:151], a[12:15], a[84:87], v[148:151]\n" \
"ds_read_b64_tr_b16 a[12:13], v10 offset:46176\n" \
"ds_read_b64_tr_b16 a[14:15], v10 offset:50784\n" \
"buffer_load_dwordx4 a[96:99], v20, s[56:59], s69 offen offset:0 sc1\n" \
"v_mfma_f32_16x16x32_bf16 v[148:151], a[8:11], a[80:83], v[148:151]\n" \
"ds_read_b64_tr_b16 a[8:9], v10 offset:55360\n" \
"s_waitcnt lgkmcnt(15)\n" \
"v_mfma_f32_16x16x32_bf16 v[152:155], a[16:19], a[64:67], v[152:155]\n" \
"ds_read_b64_tr_b16 a[10:11], v10 offset:59968\n" \
"v_mfma_f32_16x16x32_bf16 v[152:155], a[20:23], a[68:71], v[152:155]\n" \
"v_mfma_f32_16x16x32_bf16 v[156:159], a[20:23], a[84:87], v[156:159]\n" \
"ds_read_b64_tr_b16 a[20:21], v10 offset:46208\n" \
"ds_read_b64_tr_b16 a[22:23], v10 offset:50816\n" \
"buffer_load_dwordx4 a[100:103], v20, s[56:59], s69 offen offset:1024 sc1\n" \
"v_mfma_f32_16x16x32_bf16 v[156:159], a[16:19], a[80:83], v[156:159]\n" \
"ds_read_b64_tr_b16 a[16:17], v10 offset:55392\n" \
"ds_read_b64_tr_b16 a[18:19], v10 offset:60000\n" \
"v_mfma_f32_16x16x32_bf16 v[160:163], a[24:27], a[64:67], v[160:163]\n" \
"v_mfma_f32_16x16x32_bf16 v[160:163], a[28:31], a[68:71], v[160:163]\n" \
"v_mfma_f32_16x16x32_bf16 v[164:167], a[28:31], a[84:87], v[164:167]\n" \
"ds_read_b64_tr_b16 a[28:29], v10 offset:46240\n" \
"ds_read_b64_tr_b16 a[30:31], v10 offset:50848\n" \
"buffer_load_dwordx4 a[104:107], v20, s[56:59], s69 offen offset:2048 sc1\n" \
"v_mfma_f32_16x16x32_bf16 v[164:167], a[24:27], a[80:83], v[164:167]\n" \
"ds_read_b64_tr_b16 a[24:25], v10 offset:55424\n" \
"ds_read_b64_tr_b16 a[26:27], v10 offset:60032\n" \
"v_mfma_f32_16x16x32_bf16 v[168:171], a[192:195], a[64:67], v[168:171]\n" \
"v_mfma_f32_16x16x32_bf16 v[168:171], a[196:199], a[68:71], v[168:171]\n" \
"v_mfma_f32_16x16x32_bf16 v[172:175], a[196:199], a[84:87], v[172:175]\n" \
"ds_read_b64_tr_b16 a[196:197], v10 offset:46272\n" \
"ds_read_b64_tr_b16 a[198:199], v10 offset:50880\n" \
"buffer_load_dwordx4 a[108:111], v20, s[56:59], s69 offen offset:3072 sc1\n" \
"v_mfma_f32_16x16x32_bf16 v[172:175], a[192:195], a[80:83], v[172:175]\n" \
"ds_read_b64_tr_b16 a[192:193], v10 offset:55456\n" \
"ds_read_b64_tr_b16 a[194:195], v10 offset:60064\n" \
"v_mfma_f32_16x16x32_bf16 v[176:179], a[200:203], a[64:67], v[176:179]\n" \
"s_waitcnt lgkmcnt(13)\n" \
"v_mfma_f32_16x16x32_bf16 v[176:179], a[204:207], a[68:71], v[176:179]\n" \
"v_mfma_f32_16x16x32_bf16 v[180:183], a[204:207], a[84:87], v[180:183]\n" \
"ds_read_b64_tr_b16 a[204:205], v10 offset:46304\n" \
"ds_read_b64_tr_b16 a[206:207], v10 offset:50912\n" \
"buffer_load_dwordx4 a[124:127], v32, s[56:59], s69 offen offset:3072 sc1\n" \
"v_mfma_f32_16x16x32_bf16 v[180:183], a[200:203], a[80:83], v[180:183]\n" \
"ds_read_b64_tr_b16 a[200:201], v10 offset:55488\n" \
"ds_read_b64_tr_b16 a[202:203], v10 offset:60096\n" \
"v_mfma_f32_16x16x32_bf16 v[112:115], a[208:211], a[72:75], v[112:115]\n" \
"v_mfma_f32_16x16x32_bf16 v[112:115], a[212:215], a[76:79], v[112:115]\n" \
"v_mfma_f32_16x16x32_bf16 v[116:119], a[212:215], a[92:95], v[116:119]\n" \
"ds_read_b64_tr_b16 a[212:213], v10 offset:46336\n" \
"ds_read_b64_tr_b16 a[214:215], v10 offset:50944\n" \
"buffer_load_dwordx4 a[120:123], v32, s[56:59], s69 offen offset:2048 sc1\n" \
"v_mfma_f32_16x16x32_bf16 v[116:119], a[208:211], a[88:91], v[116:119]\n" \
"ds_read_b64_tr_b16 a[208:209], v10 offset:55520\n" \
"ds_read_b64_tr_b16 a[210:211], v10 offset:60128\n" \
"v_mfma_f32_16x16x32_bf16 v[120:123], a[216:219], a[72:75], v[120:123]\n" \
"v_mfma_f32_16x16x32_bf16 v[120:123], a[0:3], a[76:79], v[120:123]\n" \
"v_mfma_f32_16x16x32_bf16 v[124:127], a[0:3], a[92:95], v[124:127]\n" \
"v_mfma_f32_16x16x32_bf16 v[124:127], a[216:219], a[88:91], v[124:127]\n" \
"buffer_load_dwordx4 a[116:119], v32, s[56:59], s69 offen offset:1024 sc1\n" \
"v_mfma_f32_16x16x32_bf16 v[128:131], a[4:7], a[72:75], v[128:131]\n" \
"ds_read_b64_tr_b16 a[216:217], v10 offset:55552\n" \
"s_waitcnt lgkmcnt(9)\n" \
"v_mfma_f32_16x16x32_bf16 v[128:131], a[8:11], a[76:79], v[128:131]\n" \
"ds_read_b64_tr_b16 a[218:219], v10 offset:60160\n" \
"v_mfma_f32_16x16x32_bf16 v[132:135], a[8:11], a[92:95], v[132:135]\n" \
"v_mfma_f32_16x16x32_bf16 v[132:135], a[4:7], a[88:91], v[132:135]\n" \
"v_mfma_f32_16x16x32_bf16 v[136:139], a[12:15], a[72:75], v[136:139]\n" \
"buffer_load_dwordx4 a[112:115], v32, s[56:59], s69 offen offset:0 sc1\n" \
"v_mfma_f32_16x16x32_bf16 v[136:139], a[16:19], a[76:79], v[136:139]\n" \
"v_readlane_b32 s69, v11, 49\n" \
"v_mfma_f32_16x16x32_bf16 v[140:143], a[16:19], a[92:95], v[140:143]\n" \
"v_mfma_f32_16x16x32_bf16 v[140:143], a[12:15], a[88:91], v[140:143]\n" \
"s_waitcnt vmcnt(16)\n" \
"s_barrier\n" \
"buffer_load_dwordx4 a[32:35], v20, s[56:59], s64 offen offset:0 sc1\n" \
"v_mfma_f32_16x16x32_bf16 v[144:147], a[20:23], a[72:75], v[144:147]\n" \
"ds_read_b64_tr_b16 a[0:1], v33 offset:18432\n" \
"ds_read_b64_tr_b16 a[2:3], v33 offset:23040\n" \
"v_mfma_f32_16x16x32_bf16 v[144:147], a[24:27], a[76:79], v[144:147]\n" \
"ds_read_b64_tr_b16 a[4:5], v33 offset:27648\n" \
"ds_read_b64_tr_b16 a[6:7], v33 offset:32256\n" \
"v_mfma_f32_16x16x32_bf16 v[148:151], a[24:27], a[92:95], v[148:151]\n" \
"ds_read_b64_tr_b16 a[8:9], v33 offset:36864\n" \
"ds_read_b64_tr_b16 a[10:11], v33 offset:41472\n" \
"v_mfma_f32_16x16x32_bf16 v[148:151], a[20:23], a[88:91], v[148:151]\n" \
"ds_read_b64_tr_b16 a[12:13], v33 offset:46080\n" \
"ds_read_b64_tr_b16 a[14:15], v33 offset:50688\n" \
"buffer_load_dwordx4 a[36:39], v20, s[56:59], s64 offen offset:1024 sc1\n" \
"v_mfma_f32_16x16x32_bf16 v[152:155], a[28:31], a[72:75], v[152:155]\n" \
"ds_read_b64_tr_b16 a[16:17], v34 offset:0\n" \
"ds_read_b64_tr_b16 a[18:19], v34 offset:4608\n" \
"v_mfma_f32_16x16x32_bf16 v[152:155], a[192:195], a[76:79], v[152:155]\n" \
"ds_read_b64_tr_b16 a[20:21], v34 offset:9216\n" \
"ds_read_b64_tr_b16 a[22:23], v34 offset:13824\n" \
"v_mfma_f32_16x16x32_bf16 v[156:159], a[192:195], a[92:95], v[156:159]\n" \
"ds_read_b64_tr_b16 a[24:25], v34 offset:18432\n" \
"ds_read_b64_tr_b16 a[26:27], v34 offset:23040\n" \
"v_mfma_f32_16x16x32_bf16 v[156:159], a[28:31], a[88:91], v[156:159]\n" \
"ds_read_b64_tr_b16 a[28:29], v33 offset:0\n" \
"ds_read_b64_tr_b16 a[30:31], v33 offset:4608\n" \
"buffer_load_dwordx4 a[40:43], v20, s[56:59], s64 offen offset:2048 sc1\n" \
"v_mfma_f32_16x16x32_bf16 v[160:163], a[196:199], a[72:75], v[160:163]\n" \
"ds_read_b64_tr_b16 a[192:193], v33 offset:18464\n" \
"s_waitcnt lgkmcnt(15)\n" \
"v_mfma_f32_16x16x32_bf16 v[160:163], a[200:203], a[76:79], v[160:163]\n" \
"ds_read_b64_tr_b16 a[194:195], v33 offset:23072\n" \
"v_mfma_f32_16x16x32_bf16 v[164:167], a[200:203], a[92:95], v[164:167]\n" \
"buffer_load_dwordx4 a[44:47], v20, s[56:59], s64 offen offset:3072 sc1\n" \
"v_mfma_f32_16x16x32_bf16 v[164:167], a[196:199], a[88:91], v[164:167]\n" \
"ds_read_b64_tr_b16 a[196:197], v33 offset:27680\n" \
"ds_read_b64_tr_b16 a[198:199], v33 offset:32288\n" \
"v_mfma_f32_16x16x32_bf16 v[168:171], a[204:207], a[72:75], v[168:171]\n" \
"ds_read_b64_tr_b16 a[200:201], v33 offset:36896\n" \
"ds_read_b64_tr_b16 a[202:203], v33 offset:41504\n" \
"v_mfma_f32_16x16x32_bf16 v[168:171], a[208:211], a[76:79], v[168:171]\n" \
"v_mfma_f32_16x16x32_bf16 v[172:175], a[208:211], a[92:95], v[172:175]\n" \
"ds_read_b64_tr_b16 a[208:209], v34 offset:32\n" \
"ds_read_b64_tr_b16 a[210:211], v34 offset:4640\n" \
"buffer_load_dwordx4 a[60:63], v32, s[56:59], s64 offen offset:3072 sc1\n" \
"v_mfma_f32_16x16x32_bf16 v[172:175], a[204:207], a[88:91], v[172:175]\n" \
"ds_read_b64_tr_b16 a[204:205], v33 offset:46112\n" \
"ds_read_b64_tr_b16 a[206:207], v33 offset:50720\n" \
"v_mfma_f32_16x16x32_bf16 v[176:179], a[212:215], a[72:75], v[176:179]\n" \
"v_mfma_f32_16x16x32_bf16 v[176:179], a[216:219], a[76:79], v[176:179]\n" \
"v_mfma_f32_16x16x32_bf16 v[180:183], a[216:219], a[92:95], v[180:183]\n" \
"ds_read_b64_tr_b16 a[216:217], v34 offset:18464\n" \
"ds_read_b64_tr_b16 a[218:219], v34 offset:23072\n" \
"buffer_load_dwordx4 a[56:59], v32, s[56:59], s64 offen offset:2048 sc1\n" \
"v_mfma_f32_16x16x32_bf16 v[180:183], a[212:215], a[88:91], v[180:183]\n" \
"ds_read_b64_tr_b16 a[212:213], v34 offset:9248\n" \
"s_waitcnt vmcnt(21)\n" \
"v_mfma_f32_16x16x32_bf16 v[112:115], a[0:3], a[128:131], v[112:115]\n" \
"ds_read_b64_tr_b16 a[214:215], v34 offset:13856\n" \
"s_waitcnt vmcnt(20) lgkmcnt(13)\n" \
"v_mfma_f32_16x16x32_bf16 v[112:115], a[4:7], a[132:135], v[112:115]\n" \
"s_waitcnt vmcnt(19)\n" \
"v_mfma_f32_16x16x32_bf16 v[112:115], a[8:11], a[136:139], v[112:115]\n" \
"s_waitcnt vmcnt(18)\n" \
"v_mfma_f32_16x16x32_bf16 v[112:115], a[12:15], a[140:143], v[112:115]\n" \
"buffer_load_dwordx4 a[52:55], v32, s[56:59], s64 offen offset:1024 sc1\n" \
"s_waitcnt vmcnt(18)\n" \
"v_mfma_f32_16x16x32_bf16 v[116:119], a[12:15], a[156:159], v[116:119]\n" \
"ds_read_b64_tr_b16 a[12:13], v33 offset:36928\n" \
"s_waitcnt vmcnt(17)\n" \
"v_mfma_f32_16x16x32_bf16 v[116:119], a[8:11], a[152:155], v[116:119]\n" \
"ds_read_b64_tr_b16 a[8:9], v33 offset:27712\n" \
"ds_read_b64_tr_b16 a[10:11], v33 offset:32320\n" \
"ds_read_b64_tr_b16 a[14:15], v33 offset:41536\n" \
"s_waitcnt vmcnt(16)\n" \
"v_mfma_f32_16x16x32_bf16 v[116:119], a[4:7], a[148:151], v[116:119]\n" \
"buffer_load_dwordx4 a[48:51], v32, s[56:59], s64 offen offset:0 sc1\n" \
"ds_read_b64_tr_b16 a[4:5], v33 offset:18496\n" \
"ds_read_b64_tr_b16 a[6:7], v33 offset:23104\n" \
"v_readlane_b32 s64, v11, 43\n" \
"s_waitcnt vmcnt(16)\n" \
"v_mfma_f32_16x16x32_bf16 v[116:119], a[0:3], a[144:147], v[116:119]\n" \
"buffer_load_dwordx4 a[160:163], v20, s[56:59], s72 offen offset:0 sc1\n" \
"ds_read_b64_tr_b16 a[0:1], v33 offset:32\n" \
"ds_read_b64_tr_b16 a[2:3], v33 offset:4640\n" \
"s_waitcnt vmcnt(16)\n" \
"v_mfma_f32_16x16x32_bf16 v[112:115], a[16:19], a[96:99], v[112:115]\n" \
"buffer_load_dwordx4 a[164:167], v20, s[56:59], s72 offen offset:1024 sc1\n" \
"s_waitcnt vmcnt(16)\n" \
"v_mfma_f32_16x16x32_bf16 v[112:115], a[20:23], a[100:103], v[112:115]\n" \
"buffer_load_dwordx4 a[168:171], v20, s[56:59], s72 offen offset:2048 sc1\n" \
"s_waitcnt vmcnt(16)\n" \
"v_mfma_f32_16x16x32_bf16 v[112:115], a[24:27], a[104:107], v[112:115]\n" \
"buffer_load_dwordx4 a[172:175], v20, s[56:59], s72 offen offset:3072 sc1\n" \
"s_waitcnt vmcnt(16)\n" \
"v_mfma_f32_16x16x32_bf16 v[112:115], a[28:31], a[108:111], v[112:115]\n" \
"buffer_load_dwordx4 a[188:191], v32, s[56:59], s72 offen offset:3072 sc1\n" \
"s_waitcnt vmcnt(16)\n" \
"v_mfma_f32_16x16x32_bf16 v[116:119], a[28:31], a[124:127], v[116:119]\n" \
"buffer_load_dwordx4 a[184:187], v32, s[56:59], s72 offen offset:2048 sc1\n" \
"ds_read_b64_tr_b16 a[28:29], v34 offset:18496\n" \
"ds_read_b64_tr_b16 a[30:31], v34 offset:23104\n" \
"s_waitcnt vmcnt(16)\n" \
"v_mfma_f32_16x16x32_bf16 v[116:119], a[24:27], a[120:123], v[116:119]\n" \
"v_cvt_pk_bf16_f32 v112, v112, v113\n" \
"v_cvt_pk_bf16_f32 v113, v114, v115\n" \
"s_waitcnt vmcnt(15)\n" \
"v_mfma_f32_16x16x32_bf16 v[116:119], a[20:23], a[116:119], v[116:119]\n" \
"buffer_load_dwordx4 a[180:183], v32, s[56:59], s72 offen offset:1024 sc1\n" \
"ds_read_b64_tr_b16 a[20:21], v34 offset:64\n" \
"ds_read_b64_tr_b16 a[22:23], v34 offset:4672\n" \
"ds_read_b64_tr_b16 a[24:25], v34 offset:9280\n" \
"ds_read_b64_tr_b16 a[26:27], v34 offset:13888\n" \
"s_waitcnt vmcnt(15)\n" \
"v_mfma_f32_16x16x32_bf16 v[116:119], a[16:19], a[112:115], v[116:119]\n" \
"ds_read_b64_tr_b16 a[16:17], v33 offset:46144\n" \
"buffer_load_dwordx4 a[176:179], v32, s[56:59], s72 offen offset:0 sc1\n" \
"s_waitcnt lgkmcnt(5)\n" \
"v_mfma_f32_16x16x32_bf16 v[120:123], a[192:195], a[128:131], v[120:123]\n" \
"ds_read_b64_tr_b16 a[18:19], v33 offset:50752\n" \
"v_mfma_f32_16x16x32_bf16 v[120:123], a[196:199], a[132:135], v[120:123]\n" \
"v_mfma_f32_16x16x32_bf16 v[120:123], a[200:203], a[136:139], v[120:123]\n" \
"v_mfma_f32_16x16x32_bf16 v[120:123], a[204:207], a[140:143], v[120:123]\n" \
"v_cvt_pk_bf16_f32 v114, v116, v117\n" \
"v_cvt_pk_bf16_f32 v115, v118, v119\n" \
"v_mfma_f32_16x16x32_bf16 v[124:127], a[204:207], a[156:159], v[124:127]\n" \
"buffer_store_dwordx4 v[112:115], v20, s[56:59], s65 offen offset:0 sc1\n" \
"s_add_u32 s65, s65, 0x3800\n" \
"v_mfma_f32_16x16x32_bf16 v[124:127], a[200:203], a[152:155], v[124:127]\n" \
"ds_read_b64_tr_b16 a[200:201], v33 offset:27744\n" \
"ds_read_b64_tr_b16 a[202:203], v33 offset:32352\n" \
"v_mfma_f32_16x16x32_bf16 v[124:127], a[196:199], a[148:151], v[124:127]\n" \
"ds_read_b64_tr_b16 a[196:197], v33 offset:18528\n" \
"ds_read_b64_tr_b16 a[198:199], v33 offset:23136\n" \
"v_mfma_f32_16x16x32_bf16 v[124:127], a[192:195], a[144:147], v[124:127]\n" \
"ds_read_b64_tr_b16 a[192:193], v33 offset:64\n" \
"ds_read_b64_tr_b16 a[194:195], v33 offset:4672\n" \
"v_mfma_f32_16x16x32_bf16 v[120:123], a[208:211], a[96:99], v[120:123]\n" \
"buffer_load_dwordx4 a[220:223], v20, s[56:59], s93 offen offset:0 sc1\n" \
"v_mfma_f32_16x16x32_bf16 v[120:123], a[212:215], a[100:103], v[120:123]\n" \
"ds_read_b64_tr_b16 a[204:205], v33 offset:36960\n" \
"ds_read_b64_tr_b16 a[206:207], v33 offset:41568\n" \
"v_mfma_f32_16x16x32_bf16 v[120:123], a[216:219], a[104:107], v[120:123]\n" \
"v_mfma_f32_16x16x32_bf16 v[120:123], a[0:3], a[108:111], v[120:123]\n" \
"v_mfma_f32_16x16x32_bf16 v[124:127], a[0:3], a[124:127], v[124:127]\n" \
"ds_read_b64_tr_b16 a[0:1], v34 offset:18528\n" \
"ds_read_b64_tr_b16 a[2:3], v34 offset:23136\n" \
"buffer_load_dwordx4 a[224:227], v20, s[56:59], s93 offen offset:1024 sc1\n" \
"v_mfma_f32_16x16x32_bf16 v[124:127], a[216:219], a[120:123], v[124:127]\n" \
"ds_read_b64_tr_b16 a[216:217], v34 offset:9312\n" \
"ds_read_b64_tr_b16 a[218:219], v34 offset:13920\n" \
"v_mfma_f32_16x16x32_bf16 v[124:127], a[212:215], a[116:119], v[124:127]\n" \
"ds_read_b64_tr_b16 a[212:213], v34 offset:96\n" \
"v_cvt_pk_bf16_f32 v120, v120, v121\n" \
"v_cvt_pk_bf16_f32 v121, v122, v123\n" \
"v_mfma_f32_16x16x32_bf16 v[124:127], a[208:211], a[112:115], v[124:127]\n" \
"ds_read_b64_tr_b16 a[208:209], v33 offset:46176\n" \
"ds_read_b64_tr_b16 a[210:211], v33 offset:50784\n" \
"v_mfma_f32_16x16x32_bf16 v[128:131], a[4:7], a[128:131], v[128:131]\n" \
"buffer_load_dwordx4 a[228:231], v20, s[56:59], s93 offen offset:2048 sc1\n" \
"v_mfma_f32_16x16x32_bf16 v[128:131], a[8:11], a[132:135], v[128:131]\n" \
"ds_read_b64_tr_b16 a[214:215], v34 offset:4704\n" \
"v_mfma_f32_16x16x32_bf16 v[128:131], a[12:15], a[136:139], v[128:131]\n" \
"s_waitcnt lgkmcnt(15)\n" \
"v_mfma_f32_16x16x32_bf16 v[128:131], a[16:19], a[140:143], v[128:131]\n" \
"v_cvt_pk_bf16_f32 v122, v124, v125\n" \
"v_cvt_pk_bf16_f32 v123, v126, v127\n" \
"v_mfma_f32_16x16x32_bf16 v[132:135], a[16:19], a[156:159], v[132:135]\n" \
"buffer_store_dwordx4 v[120:123], v20, s[56:59], s66 offen offset:0 sc1\n" \
"s_add_u32 s66, s66, 0x3000\n" \
"v_mfma_f32_16x16x32_bf16 v[132:135], a[12:15], a[152:155], v[132:135]\n" \
"ds_read_b64_tr_b16 a[12:13], v10 offset:37120\n" \
"ds_read_b64_tr_b16 a[14:15], v10 offset:41728\n" \
"v_mfma_f32_16x16x32_bf16 v[132:135], a[8:11], a[148:151], v[132:135]\n" \
"ds_read_b64_tr_b16 a[8:9], v10 offset:27904\n" \
"ds_read_b64_tr_b16 a[10:11], v10 offset:32512\n" \
"v_mfma_f32_16x16x32_bf16 v[132:135], a[4:7], a[144:147], v[132:135]\n" \
"ds_read_b64_tr_b16 a[4:5], v33 offset:96\n" \
"ds_read_b64_tr_b16 a[6:7], v33 offset:4704\n" \
"v_mfma_f32_16x16x32_bf16 v[128:131], a[20:23], a[96:99], v[128:131]\n" \
"buffer_load_dwordx4 a[232:235], v20, s[56:59], s93 offen offset:3072 sc1\n" \
"v_mfma_f32_16x16x32_bf16 v[128:131], a[24:27], a[100:103], v[128:131]\n" \
"ds_read_b64_tr_b16 a[16:17], v10 offset:46336\n" \
"ds_read_b64_tr_b16 a[18:19], v10 offset:50944\n" \
"v_mfma_f32_16x16x32_bf16 v[128:131], a[28:31], a[104:107], v[128:131]\n" \
"s_waitcnt lgkmcnt(14)\n" \
"v_mfma_f32_16x16x32_bf16 v[128:131], a[192:195], a[108:111], v[128:131]\n" \
"v_mfma_f32_16x16x32_bf16 v[132:135], a[192:195], a[124:127], v[132:135]\n" \
"ds_read_b64_tr_b16 a[192:193], v33 offset:37120\n" \
"ds_read_b64_tr_b16 a[194:195], v33 offset:41728\n" \
"buffer_load_dwordx4 a[248:251], v32, s[56:59], s93 offen offset:3072 sc1\n" \
"v_mfma_f32_16x16x32_bf16 v[132:135], a[28:31], a[120:123], v[132:135]\n" \
"ds_read_b64_tr_b16 a[28:29], v33 offset:27904\n" \
"ds_read_b64_tr_b16 a[30:31], v33 offset:32512\n" \
"L_pf22:\n" \
"s_cbranch_execz L_retB\n" \
"s_nop 0\n" \
"v_mfma_f32_16x16x32_bf16 v[132:135], a[24:27], a[116:119], v[132:135]\n" \
"ds_read_b64_tr_b16 a[24:25], v33 offset:18688\n" \
"v_cvt_pk_bf16_f32 v128, v128, v129\n" \
"v_cvt_pk_bf16_f32 v129, v130, v131\n" \
"v_mfma_f32_16x16x32_bf16 v[132:135], a[20:23], a[112:115], v[132:135]\n" \
"ds_read_b64_tr_b16 a[20:21], v10 offset:55552\n" \
"ds_read_b64_tr_b16 a[22:23], v10 offset:60160\n" \
"v_mfma_f32_16x16x32_bf16 v[136:139], a[196:199], a[128:131], v[136:139]\n" \
"buffer_load_dwordx4 a[244:247], v32, s[56:59], s93 offen offset:2048 sc1\n" \
"v_mfma_f32_16x16x32_bf16 v[136:139], a[200:203], a[132:135], v[136:139]\n" \
"ds_read_b64_tr_b16 a[26:27], v33 offset:23296\n" \
"v_mfma_f32_16x16x32_bf16 v[136:139], a[204:207], a[136:139], v[136:139]\n" \
"s_waitcnt lgkmcnt(15)\n" \
"v_mfma_f32_16x16x32_bf16 v[136:139], a[208:211], a[140:143], v[136:139]\n" \
"v_cvt_pk_bf16_f32 v130, v132, v133\n" \
"v_cvt_pk_bf16_f32 v131, v134, v135\n" \
"v_mfma_f32_16x16x32_bf16 v[140:143], a[208:211], a[156:159], v[140:143]\n" \
"buffer_store_dwordx4 v[128:131], v20, s[56:59], s67 offen offset:0 sc1\n" \
"s_add_u32 s67, s67, 0x2800\n" \
"v_mfma_f32_16x16x32_bf16 v[140:143], a[204:207], a[152:155], v[140:143]\n" \
"ds_read_b64_tr_b16 a[204:205], v34 offset:9472\n" \
"ds_read_b64_tr_b16 a[206:207], v34 offset:14080\n" \
"v_mfma_f32_16x16x32_bf16 v[140:143], a[200:203], a[148:151], v[140:143]\n" \
"ds_read_b64_tr_b16 a[200:201], v34 offset:256\n" \
"ds_read_b64_tr_b16 a[202:203], v34 offset:4864\n" \
"v_mfma_f32_16x16x32_bf16 v[140:143], a[196:199], a[144:147], v[140:143]\n" \
"ds_read_b64_tr_b16 a[196:197], v33 offset:46336\n" \
"ds_read_b64_tr_b16 a[198:199], v33 offset:50944\n" \
"v_mfma_f32_16x16x32_bf16 v[136:139], a[212:215], a[96:99], v[136:139]\n" \
"buffer_load_dwordx4 a[240:243], v32, s[56:59], s93 offen offset:1024 sc1\n" \
"v_mfma_f32_16x16x32_bf16 v[136:139], a[216:219], a[100:103], v[136:139]\n" \
"ds_read_b64_tr_b16 a[208:209], v34 offset:18688\n" \
"ds_read_b64_tr_b16 a[210:211], v34 offset:23296\n" \
"v_mfma_f32_16x16x32_bf16 v[136:139], a[0:3], a[104:107], v[136:139]\n" \
"s_waitcnt lgkmcnt(14)\n" \
"v_mfma_f32_16x16x32_bf16 v[136:139], a[4:7], a[108:111], v[136:139]\n" \
"v_mfma_f32_16x16x32_bf16 v[140:143], a[4:7], a[124:127], v[140:143]\n" \
"ds_read_b64_tr_b16 a[4:5], v10 offset:46208\n" \
"ds_read_b64_tr_b16 a[6:7], v10 offset:50816\n" \
"buffer_load_dwordx4 a[236:239], v32, s[56:59], s93 offen offset:0 sc1\n" \
"v_mfma_f32_16x16x32_bf16 v[140:143], a[0:3], a[120:123], v[140:143]\n" \
"ds_read_b64_tr_b16 a[0:1], v10 offset:36992\n" \
"ds_read_b64_tr_b16 a[2:3], v10 offset:41600\n" \
"v_mfma_f32_16x16x32_bf16 v[140:143], a[216:219], a[116:119], v[140:143]\n" \
"ds_read_b64_tr_b16 a[216:217], v10 offset:27776\n" \
"v_cvt_pk_bf16_f32 v136, v136, v137\n" \
"v_cvt_pk_bf16_f32 v137, v138, v139\n" \
"v_mfma_f32_16x16x32_bf16 v[140:143], a[212:215], a[112:115], v[140:143]\n" \
"ds_read_b64_tr_b16 a[212:213], v33 offset:256\n" \
"s_waitcnt vmcnt(26)\n" \
"v_mfma_f32_16x16x32_bf16 v[248:251], a[8:11], a[32:35], v[248:251]\n" \
"ds_read_b64_tr_b16 a[214:215], v33 offset:4864\n" \
"s_waitcnt vmcnt(25)\n" \
"v_mfma_f32_16x16x32_bf16 v[248:251], a[12:15], a[36:39], v[248:251]\n" \
"ds_read_b64_tr_b16 a[218:219], v10 offset:32384\n" \
"s_waitcnt vmcnt(24)\n" \
"v_mfma_f32_16x16x32_bf16 v[248:251], a[16:19], a[40:43], v[248:251]\n" \
"v_cvt_pk_bf16_f32 v138, v140, v141\n" \
"v_cvt_pk_bf16_f32 v139, v142, v143\n" \
"s_waitcnt vmcnt(23) lgkmcnt(14)\n" \
"v_mfma_f32_16x16x32_bf16 v[248:251], a[20:23], a[44:47], v[248:251]\n" \
"buffer_store_dwordx4 v[136:139], v20, s[56:59], s68 offen offset:0 sc1\n" \
"s_add_u32 s68, s68, 0x2000\n" \
"s_waitcnt vmcnt(23)\n" \
"v_mfma_f32_16x16x32_bf16 v[252:255], a[20:23], a[60:63], v[252:255]\n" \
"ds_read_b64_tr_b16 a[20:21], v33 offset:36992\n" \
"s_waitcnt vmcnt(22)\n" \
"v_mfma_f32_16x16x32_bf16 v[252:255], a[16:19], a[56:59], v[252:255]\n" \
"ds_read_b64_tr_b16 a[16:17], v33 offset:27776\n" \
"s_waitcnt vmcnt(21)\n" \
"v_mfma_f32_16x16x32_bf16 v[252:255], a[12:15], a[52:55], v[252:255]\n" \
"ds_read_b64_tr_b16 a[12:13], v33 offset:18560\n" \
"s_waitcnt vmcnt(20)\n" \
"v_mfma_f32_16x16x32_bf16 v[252:255], a[8:11], a[48:51], v[252:255]\n" \
"ds_read_b64_tr_b16 a[8:9], v10 offset:55424\n" \
"ds_read_b64_tr_b16 a[10:11], v10 offset:60032\n" \
"v_mfma_f32_16x16x32_bf16 v[176:179], a[24:27], a[128:131], v[176:179]\n" \
"ds_read_b64_tr_b16 a[14:15], v33 offset:23168\n" \
"ds_read_b64_tr_b16 a[18:19], v33 offset:32384\n" \
"v_mfma_f32_16x16x32_bf16 v[176:179], a[28:31], a[132:135], v[176:179]\n" \
"buffer_load_dwordx4 v[64:67], v20, s[56:59], s95 offen offset:0 sc1\n" \
"v_mfma_f32_16x16x32_bf16 v[176:179], a[192:195], a[136:139], v[176:179]\n" \
"ds_read_b64_tr_b16 a[22:23], v33 offset:41600\n" \
"s_waitcnt lgkmcnt(12)\n" \
"v_mfma_f32_16x16x32_bf16 v[176:179], a[196:199], a[140:143], v[176:179]\n" \
"v_mfma_f32_16x16x32_bf16 v[180:183], a[196:199], a[156:159], v[180:183]\n" \
"v_mfma_f32_16x16x32_bf16 v[180:183], a[192:195], a[152:155], v[180:183]\n" \
"v_mfma_f32_16x16x32_bf16 v[180:183], a[28:31], a[148:151], v[180:183]\n" \
"buffer_load_dwordx4 v[68:71], v20, s[56:59], s95 offen offset:1024 sc1\n" \
"v_mfma_f32_16x16x32_bf16 v[180:183], a[24:27], a[144:147], v[180:183]\n" \
"s_waitcnt vmcnt(21)\n" \
"v_mfma_f32_16x16x32_bf16 v[248:251], a[24:27], a[160:163], v[248:251]\n" \
"s_waitcnt vmcnt(13)\n" \
"s_mov_b64 exec, 1\n" \
"buffer_store_dword v39, off, s[56:59], s65 offset:0 sc1\n" \
"s_mov_b64 exec, -1\n" \
"v_mfma_f32_16x16x32_bf16 v[248:251], a[28:31], a[164:167], v[248:251]\n" \
"v_mfma_f32_16x16x32_bf16 v[248:251], a[192:195], a[168:171], v[248:251]\n" \
"v_mfma_f32_16x16x32_bf16 v[248:251], a[196:199], a[172:175], v[248:251]\n" \
"v_mfma_f32_16x16x32_bf16 v[252:255], a[196:199], a[188:191], v[252:255]\n" \
"ds_read_b64_tr_b16 a[196:197], v34 offset:18560\n" \
"ds_read_b64_tr_b16 a[198:199], v34 offset:23168\n" \
"v_mfma_f32_16x16x32_bf16 v[252:255], a[192:195], a[184:187], v[252:255]\n" \
"ds_read_b64_tr_b16 a[192:193], v34 offset:9344\n" \
"ds_read_b64_tr_b16 a[194:195], v34 offset:13952\n" \
"v_mfma_f32_16x16x32_bf16 v[252:255], a[28:31], a[180:183], v[252:255]\n" \
"ds_read_b64_tr_b16 a[28:29], v34 offset:128\n" \
"ds_read_b64_tr_b16 a[30:31], v34 offset:4736\n" \
"v_mfma_f32_16x16x32_bf16 v[252:255], a[24:27], a[176:179], v[252:255]\n" \
"ds_read_b64_tr_b16 a[24:25], v33 offset:46208\n" \
"ds_read_b64_tr_b16 a[26:27], v33 offset:50816\n" \
"v_mfma_f32_16x16x32_bf16 v[176:179], a[200:203], a[96:99], v[176:179]\n" \
"buffer_load_dwordx4 v[72:75], v32, s[56:59], s95 offen offset:0 sc1\n" \
"v_mfma_f32_16x16x32_bf16 v[176:179], a[204:207], a[100:103], v[176:179]\n" \
"v_mfma_f32_16x16x32_bf16 v[176:179], a[208:211], a[104:107], v[176:179]\n" \
"s_waitcnt lgkmcnt(8)\n" \
"v_mfma_f32_16x16x32_bf16 v[176:179], a[212:215], a[108:111], v[176:179]\n" \
"v_mfma_f32_16x16x32_bf16 v[180:183], a[212:215], a[124:127], v[180:183]\n" \
"v_mfma_f32_16x16x32_bf16 v[180:183], a[208:211], a[120:123], v[180:183]\n" \
"buffer_load_dwordx4 v[76:79], v32, s[56:59], s95 offen offset:1024 sc1\n" \
"v_mfma_f32_16x16x32_bf16 v[180:183], a[204:207], a[116:119], v[180:183]\n" \
"v_readlane_b32 s95, v11, 48\n" \
"v_mfma_f32_16x16x32_bf16 v[180:183], a[200:203], a[112:115], v[180:183]\n" \
"s_waitcnt vmcnt(15)\n" \
"v_mfma_f32_16x16x32_bf16 v[248:251], a[200:203], a[220:223], v[248:251]\n" \
"s_waitcnt vmcnt(12)\n" \
"s_mov_b64 exec, 1\n" \
"buffer_store_dword v39, off, s[56:59], s66 offset:4 sc1\n" \
"s_mov_b64 exec, -1\n" \
"v_cvt_pk_bf16_f32 v176, v176, v177\n" \
"v_cvt_pk_bf16_f32 v177, v178, v179\n" \
"v_cvt_pk_bf16_f32 v178, v180, v181\n" \
"v_cvt_pk_bf16_f32 v179, v182, v183\n" \
"v_mfma_f32_16x16x32_bf16 v[248:251], a[204:207], a[224:227], v[248:251]\n" \
"v_readlane_b32 s66, v11, 44\n" \
"v_mfma_f32_16x16x32_bf16 v[248:251], a[208:211], a[228:231], v[248:251]\n" \
"buffer_store_dwordx4 v[176:179], v20, s[56:59], s69 offen offset:0 sc1\n" \
"s_waitcnt vmcnt(10)\n" \
"s_mov_b64 exec, 1\n" \
"buffer_store_dword v39, off, s[56:59], s67 offset:8 sc1\n" \
"s_mov_b64 exec, -1\n" \
"v_mfma_f32_16x16x32_bf16 v[248:251], a[212:215], a[232:235], v[248:251]\n" \
"v_readlane_b32 s67, v11, 45\n" \
"v_mfma_f32_16x16x32_bf16 v[252:255], a[212:215], a[248:251], v[252:255]\n" \
"ds_read_b64_tr_b16 a[212:213], v10 offset:46240\n" \
"ds_read_b64_tr_b16 a[214:215], v10 offset:50848\n" \
"v_mfma_f32_16x16x32_bf16 v[252:255], a[208:211], a[244:247], v[252:255]\n" \
"ds_read_b64_tr_b16 a[208:209], v10 offset:37024\n" \
"ds_read_b64_tr_b16 a[210:211], v10 offset:41632\n" \
"s_waitcnt vmcnt(10)\n" \
"v_mfma_f32_16x16x32_bf16 v[252:255], a[204:207], a[240:243], v[252:255]\n" \
"v_cvt_pk_bf16_f32 v248, v248, v249\n" \
"v_cvt_pk_bf16_f32 v249, v250, v251\n" \
"s_waitcnt vmcnt(9)\n" \
"v_mfma_f32_16x16x32_bf16 v[252:255], a[200:203], a[236:239], v[252:255]\n" \
"ds_read_b64_tr_b16 a[200:201], v33 offset:128\n" \
"ds_read_b64_tr_b16 a[202:203], v33 offset:4736\n" \
"s_waitcnt vmcnt(8)\n" \
"s_mov_b64 exec, 1\n" \
"buffer_store_dword v39, off, s[56:59], s68 offset:12 sc1\n" \
"s_mov_b64 exec, -1\n" \
"v_mfma_f32_16x16x32_bf16 v[216:219], a[216:219], a[32:35], v[216:219]\n" \
"ds_read_b64_tr_b16 a[204:205], v10 offset:27808\n" \
"v_cvt_pk_bf16_f32 v250, v252, v253\n" \
"v_cvt_pk_bf16_f32 v251, v254, v255\n" \
"v_mfma_f32_16x16x32_bf16 v[216:219], a[0:3], a[36:39], v[216:219]\n" \
"buffer_store_dwordx4 v[248:251], v20, s[56:59], s69 offen offset:1024 sc1\n" \
"v_mfma_f32_16x16x32_bf16 v[216:219], a[4:7], a[40:43], v[216:219]\n" \
"ds_read_b64_tr_b16 a[206:207], v10 offset:32416\n" \
"v_mfma_f32_16x16x32_bf16 v[216:219], a[8:11], a[44:47], v[216:219]\n" \
"v_mfma_f32_16x16x32_bf16 v[220:223], a[8:11], a[60:63], v[220:223]\n" \
"ds_read_b64_tr_b16 a[8:9], v33 offset:37024\n" \
"ds_read_b64_tr_b16 a[10:11], v33 offset:41632\n" \
"v_mfma_f32_16x16x32_bf16 v[220:223], a[4:7], a[56:59], v[220:223]\n" \
"ds_read_b64_tr_b16 a[4:5], v33 offset:27808\n" \
"ds_read_b64_tr_b16 a[6:7], v33 offset:32416\n" \
"v_mfma_f32_16x16x32_bf16 v[220:223], a[0:3], a[52:55], v[220:223]\n" \
"ds_read_b64_tr_b16 a[0:1], v33 offset:18592\n" \
"ds_read_b64_tr_b16 a[2:3], v33 offset:23200\n" \
"v_mfma_f32_16x16x32_bf16 v[220:223], a[216:219], a[48:51], v[220:223]\n" \
"ds_read_b64_tr_b16 a[216:217], v10 offset:55456\n" \
"ds_read_b64_tr_b16 a[218:219], v10 offset:60064\n" \
"v_mfma_f32_16x16x32_bf16 v[144:147], a[12:15], a[128:131], v[144:147]\n" \
"buffer_load_dwordx4 v[96:99], v20, s[56:59], s97 offen offset:0 sc1\n" \
"v_mfma_f32_16x16x32_bf16 v[144:147], a[16:19], a[132:135], v[144:147]\n" \
"v_mfma_f32_16x16x32_bf16 v[144:147], a[20:23], a[136:139], v[144:147]\n" \
"s_waitcnt lgkmcnt(12)\n" \
"v_mfma_f32_16x16x32_bf16 v[144:147], a[24:27], a[140:143], v[144:147]\n" \
"buffer_load_dword v37, v40, s[56:59], s74 offen sc1\n" \
"v_mfma_f32_16x16x32_bf16 v[148:151], a[24:27], a[156:159], v[148:151]\n" \
"v_mfma_f32_16x16x32_bf16 v[148:151], a[20:23], a[152:155], v[148:151]\n" \
"v_mfma_f32_16x16x32_bf16 v[148:151], a[16:19], a[148:151], v[148:151]\n" \
"v_mfma_f32_16x16x32_bf16 v[148:151], a[12:15], a[144:147], v[148:151]\n" \
"v_mfma_f32_16x16x32_bf16 v[216:219], a[12:15], a[160:163], v[216:219]\n" \
"buffer_load_dwordx4 v[100:103], v20, s[56:59], s97 offen offset:1024 sc1\n" \
"v_mfma_f32_16x16x32_bf16 v[216:219], a[16:19], a[164:167], v[216:219]\n" \
"v_mfma_f32_16x16x32_bf16 v[216:219], a[20:23], a[168:171], v[216:219]\n" \
"v_mfma_f32_16x16x32_bf16 v[216:219], a[24:27], a[172:175], v[216:219]\n" \
"v_mfma_f32_16x16x32_bf16 v[220:223], a[24:27], a[188:191], v[220:223]\n" \
"ds_read_b64_tr_b16 a[24:25], v34 offset:18592\n" \
"ds_read_b64_tr_b16 a[26:27], v34 offset:23200\n" \
"v_mfma_f32_16x16x32_bf16 v[220:223], a[20:23], a[184:187], v[220:223]\n" \
"ds_read_b64_tr_b16 a[20:21], v34 offset:9376\n" \
"ds_read_b64_tr_b16 a[22:23], v34 offset:13984\n" \
"v_mfma_f32_16x16x32_bf16 v[220:223], a[16:19], a[180:183], v[220:223]\n" \
"ds_read_b64_tr_b16 a[16:17], v34 offset:160\n" \
"ds_read_b64_tr_b16 a[18:19], v34 offset:4768\n" \
"v_mfma_f32_16x16x32_bf16 v[220:223], a[12:15], a[176:179], v[220:223]\n" \
"ds_read_b64_tr_b16 a[12:13], v33 offset:46240\n" \
"ds_read_b64_tr_b16 a[14:15], v33 offset:50848\n" \
"v_mfma_f32_16x16x32_bf16 v[144:147], a[28:31], a[96:99], v[144:147]\n" \
"buffer_load_dwordx4 v[104:107], v32, s[56:59], s97 offen offset:0 sc1\n" \
"v_mfma_f32_16x16x32_bf16 v[144:147], a[192:195], a[100:103], v[144:147]\n" \
"v_mfma_f32_16x16x32_bf16 v[144:147], a[196:199], a[104:107], v[144:147]\n" \
"s_waitcnt lgkmcnt(8)\n" \
"v_mfma_f32_16x16x32_bf16 v[144:147], a[200:203], a[108:111], v[144:147]\n" \
"v_mfma_f32_16x16x32_bf16 v[148:151], a[200:203], a[124:127], v[148:151]\n" \
"v_mfma_f32_16x16x32_bf16 v[148:151], a[196:199], a[120:123], v[148:151]\n" \
"buffer_load_dwordx4 v[108:111], v32, s[56:59], s97 offen offset:1024 sc1\n" \
"v_mfma_f32_16x16x32_bf16 v[148:151], a[192:195], a[116:119], v[148:151]\n" \
"v_readlane_b32 s97, v11, 42\n" \
"v_mfma_f32_16x16x32_bf16 v[148:151], a[28:31], a[112:115], v[148:151]\n" \
"v_mfma_f32_16x16x32_bf16 v[216:219], a[28:31], a[220:223], v[216:219]\n" \
"v_mfma_f32_16x16x32_bf16 v[216:219], a[192:195], a[224:227], v[216:219]\n" \
"v_cvt_pk_bf16_f32 v144, v144, v145\n" \
"v_cvt_pk_bf16_f32 v145, v146, v147\n" \
"v_mfma_f32_16x16x32_bf16 v[216:219], a[196:199], a[228:231], v[216:219]\n" \
"buffer_load_dwordx4 v[80:83], v20, s[56:59], s96 offen offset:0 sc1\n" \
"v_mfma_f32_16x16x32_bf16 v[216:219], a[200:203], a[232:235], v[216:219]\n" \
"v_mfma_f32_16x16x32_bf16 v[220:223], a[200:203], a[248:251], v[220:223]\n" \
"v_cvt_pk_bf16_f32 v146, v148, v149\n" \
"v_cvt_pk_bf16_f32 v147, v150, v151\n" \
"v_mfma_f32_16x16x32_bf16 v[220:223], a[196:199], a[244:247], v[220:223]\n" \
"buffer_store_dwordx4 v[144:147], v20, s[56:59], s71 offen offset:0 sc1\n" \
"v_mfma_f32_16x16x32_bf16 v[220:223], a[192:195], a[240:243], v[220:223]\n" \
"ds_read_b64_tr_b16 a[192:193], v10 offset:27840\n" \
"ds_read_b64_tr_b16 a[194:195], v10 offset:32448\n" \
"v_cvt_pk_bf16_f32 v216, v216, v217\n" \
"v_cvt_pk_bf16_f32 v217, v218, v219\n" \
"v_mfma_f32_16x16x32_bf16 v[220:223], a[28:31], a[236:239], v[220:223]\n" \
"ds_read_b64_tr_b16 a[28:29], v33 offset:160\n" \
"ds_read_b64_tr_b16 a[30:31], v33 offset:4768\n" \
"v_mfma_f32_16x16x32_bf16 v[224:227], a[204:207], a[32:35], v[224:227]\n" \
"ds_read_b64_tr_b16 a[196:197], v10 offset:37056\n" \
"ds_read_b64_tr_b16 a[198:199], v10 offset:41664\n" \
"v_mfma_f32_16x16x32_bf16 v[224:227], a[208:211], a[36:39], v[224:227]\n" \
"ds_read_b64_tr_b16 a[200:201], v10 offset:46272\n" \
"ds_read_b64_tr_b16 a[202:203], v10 offset:50880\n" \
"v_cvt_pk_bf16_f32 v218, v220, v221\n" \
"v_cvt_pk_bf16_f32 v219, v222, v223\n" \
"v_mfma_f32_16x16x32_bf16 v[224:227], a[212:215], a[40:43], v[224:227]\n" \
"buffer_store_dwordx4 v[216:219], v20, s[56:59], s71 offen offset:1024 sc1\n" \
"s_add_u32 s71, s71, 0x1800\n" \
"v_mfma_f32_16x16x32_bf16 v[224:227], a[216:219], a[44:47], v[224:227]\n" \
"v_mfma_f32_16x16x32_bf16 v[228:231], a[216:219], a[60:63], v[228:231]\n" \
"ds_read_b64_tr_b16 a[216:217], v33 offset:37056\n" \
"ds_read_b64_tr_b16 a[218:219], v33 offset:41664\n" \
"v_mfma_f32_16x16x32_bf16 v[228:231], a[212:215], a[56:59], v[228:231]\n" \
"ds_read_b64_tr_b16 a[212:213], v33 offset:27840\n" \
"ds_read_b64_tr_b16 a[214:215], v33 offset:32448\n" \
"v_mfma_f32_16x16x32_bf16 v[228:231], a[208:211], a[52:55], v[228:231]\n" \
"ds_read_b64_tr_b16 a[208:209], v33 offset:18624\n" \
"ds_read_b64_tr_b16 a[210:211], v33 offset:23232\n" \
"v_mfma_f32_16x16x32_bf16 v[228:231], a[204:207], a[48:51], v[228:231]\n" \
"ds_read_b64_tr_b16 a[204:205], v10 offset:55488\n" \
"ds_read_b64_tr_b16 a[206:207], v10 offset:60096\n" \
"v_mfma_f32_16x16x32_bf16 v[152:155], a[0:3], a[128:131], v[152:155]\n" \
"buffer_load_dwordx4 v[88:91], v32, s[56:59], s96 offen offset:0 sc1\n" \
"v_mfma_f32_16x16x32_bf16 v[152:155], a[4:7], a[132:135], v[152:155]\n" \
"v_mfma_f32_16x16x32_bf16 v[152:155], a[8:11], a[136:139], v[152:155]\n" \
"s_waitcnt lgkmcnt(15)\n" \
"v_mfma_f32_16x16x32_bf16 v[152:155], a[12:15], a[140:143], v[152:155]\n" \
"v_mfma_f32_16x16x32_bf16 v[156:159], a[12:15], a[156:159], v[156:159]\n" \
"v_mfma_f32_16x16x32_bf16 v[156:159], a[8:11], a[152:155], v[156:159]\n" \
"buffer_load_dwordx4 v[84:87], v20, s[56:59], s96 offen offset:1024 sc1\n" \
"v_mfma_f32_16x16x32_bf16 v[156:159], a[4:7], a[148:151], v[156:159]\n" \
"v_mfma_f32_16x16x32_bf16 v[156:159], a[0:3], a[144:147], v[156:159]\n" \
"v_mfma_f32_16x16x32_bf16 v[224:227], a[0:3], a[160:163], v[224:227]\n" \
"v_mfma_f32_16x16x32_bf16 v[224:227], a[4:7], a[164:167], v[224:227]\n" \
"v_mfma_f32_16x16x32_bf16 v[224:227], a[8:11], a[168:171], v[224:227]\n" \
"buffer_load_dwordx4 v[92:95], v32, s[56:59], s96 offen offset:1024 sc1\n" \
"v_mfma_f32_16x16x32_bf16 v[224:227], a[12:15], a[172:175], v[224:227]\n" \
"v_mfma_f32_16x16x32_bf16 v[228:231], a[12:15], a[188:191], v[228:231]\n" \
"ds_read_b64_tr_b16 a[12:13], v34 offset:18624\n" \
"ds_read_b64_tr_b16 a[14:15], v34 offset:23232\n" \
"v_mfma_f32_16x16x32_bf16 v[228:231], a[8:11], a[184:187], v[228:231]\n" \
"ds_read_b64_tr_b16 a[8:9], v34 offset:9408\n" \
"ds_read_b64_tr_b16 a[10:11], v34 offset:14016\n" \
"v_mfma_f32_16x16x32_bf16 v[228:231], a[4:7], a[180:183], v[228:231]\n" \
"ds_read_b64_tr_b16 a[4:5], v34 offset:192\n" \
"ds_read_b64_tr_b16 a[6:7], v34 offset:4800\n" \
"v_mfma_f32_16x16x32_bf16 v[228:231], a[0:3], a[176:179], v[228:231]\n" \
"ds_read_b64_tr_b16 a[0:1], v33 offset:46272\n" \
"ds_read_b64_tr_b16 a[2:3], v33 offset:50880\n" \
"s_waitcnt vmcnt(11)\n" \
"s_mov_b64 exec, 3\n" \
"buffer_store_dword v39, v41, s[56:59], s70 offen sc1\n" \
"s_mov_b64 exec, -1\n" \
"v_mfma_f32_16x16x32_bf16 v[152:155], a[16:19], a[96:99], v[152:155]\n" \
"v_mfma_f32_16x16x32_bf16 v[152:155], a[20:23], a[100:103], v[152:155]\n" \
"v_mfma_f32_16x16x32_bf16 v[152:155], a[24:27], a[104:107], v[152:155]\n" \
"s_waitcnt vmcnt(10)\n" \
"v_cmp_eq_u32_e64 s[98:99], s62, v37\n" \
"s_waitcnt lgkmcnt(8)\n" \
"v_mfma_f32_16x16x32_bf16 v[152:155], a[28:31], a[108:111], v[152:155]\n" \
"s_and_b32 s99, s98, 0xf\n" \
"s_or_b32 s84, s84, s99\n" \
"s_bitcmp1_b32 s84, 0\n" \
"s_cselect_b32 s72, s63, 0x40000000\n" \
"s_bitcmp1_b32 s84, 1\n" \
"s_cselect_b32 s73, s63, 0x40000000\n" \
"s_bitcmp1_b32 s84, 2\n" \
"s_cselect_b32 s92, s63, 0x40000000\n" \
"s_bitcmp1_b32 s84, 3\n" \
"s_cselect_b32 s93, s63, 0x40000000\n" \
"v_cvt_pk_bf16_f32 v152, v152, v153\n" \
"v_cvt_pk_bf16_f32 v153, v154, v155\n" \
"v_mfma_f32_16x16x32_bf16 v[156:159], a[28:31], a[124:127], v[156:159]\n" \
"buffer_load_dwordx4 v[116:119], v20, s[56:59], s72 offen offset:0 sc1\n" \
"v_mfma_f32_16x16x32_bf16 v[156:159], a[24:27], a[120:123], v[156:159]\n" \
"v_mfma_f32_16x16x32_bf16 v[156:159], a[20:23], a[116:119], v[156:159]\n" \
"v_mfma_f32_16x16x32_bf16 v[156:159], a[16:19], a[112:115], v[156:159]\n" \
"v_mfma_f32_16x16x32_bf16 v[224:227], a[16:19], a[220:223], v[224:227]\n" \
"v_mfma_f32_16x16x32_bf16 v[224:227], a[20:23], a[224:227], v[224:227]\n" \
"buffer_load_dwordx4 v[124:127], v20, s[56:59], s73 offen offset:2048 sc1\n" \
"v_mfma_f32_16x16x32_bf16 v[224:227], a[24:27], a[228:231], v[224:227]\n" \
"v_mfma_f32_16x16x32_bf16 v[224:227], a[28:31], a[232:235], v[224:227]\n" \
"v_mfma_f32_16x16x32_bf16 v[228:231], a[28:31], a[248:251], v[228:231]\n" \
"ds_read_b64_tr_b16 a[28:29], v10 offset:46080\n" \
"ds_read_b64_tr_b16 a[30:31], v10 offset:50688\n" \
"v_cvt_pk_bf16_f32 v154, v156, v157\n" \
"v_cvt_pk_bf16_f32 v155, v158, v159\n" \
"v_mfma_f32_16x16x32_bf16 v[228:231], a[24:27], a[244:247], v[228:231]\n" \
"buffer_store_dwordx4 v[152:155], v20, s[56:59], s94 offen offset:0 sc1\n" \
"v_mfma_f32_16x16x32_bf16 v[228:231], a[20:23], a[240:243], v[228:231]\n" \
"ds_read_b64_tr_b16 a[20:21], v10 offset:27648\n" \
"v_cvt_pk_bf16_f32 v224, v224, v225\n" \
"v_cvt_pk_bf16_f32 v225, v226, v227\n" \
"v_mfma_f32_16x16x32_bf16 v[228:231], a[16:19], a[236:239], v[228:231]\n" \
"ds_read_b64_tr_b16 a[16:17], v33 offset:192\n" \
"ds_read_b64_tr_b16 a[18:19], v33 offset:4800\n" \
"v_mfma_f32_16x16x32_bf16 v[232:235], a[192:195], a[32:35], v[232:235]\n" \
"ds_read_b64_tr_b16 a[22:23], v10 offset:32256\n" \
"ds_read_b64_tr_b16 a[24:25], v10 offset:36864\n" \
"v_mfma_f32_16x16x32_bf16 v[232:235], a[196:199], a[36:39], v[232:235]\n" \
"ds_read_b64_tr_b16 a[26:27], v10 offset:41472\n" \
"buffer_load_dwordx4 v[132:135], v38, s[56:59], s92 offen offset:0 sc1\n" \
"v_cvt_pk_bf16_f32 v226, v228, v229\n" \
"v_cvt_pk_bf16_f32 v227, v230, v231\n" \
"v_mfma_f32_16x16x32_bf16 v[232:235], a[200:203], a[40:43], v[232:235]\n" \
"v_readlane_b32 s92, v11, 50\n" \
"v_mfma_f32_16x16x32_bf16 v[232:235], a[204:207], a[44:47], v[232:235]\n" \
"buffer_store_dwordx4 v[224:227], v20, s[56:59], s94 offen offset:1024 sc1\n" \
"s_add_u32 s94, s94, 0x1000\n" \
"v_mfma_f32_16x16x32_bf16 v[236:239], a[204:207], a[60:63], v[236:239]\n" \
"ds_read_b64_tr_b16 a[204:205], v33 offset:36864\n" \
"ds_read_b64_tr_b16 a[206:207], v33 offset:41472\n" \
"v_mfma_f32_16x16x32_bf16 v[236:239], a[200:203], a[56:59], v[236:239]\n" \
"ds_read_b64_tr_b16 a[200:201], v33 offset:27648\n" \
"ds_read_b64_tr_b16 a[202:203], v33 offset:32256\n" \
"v_mfma_f32_16x16x32_bf16 v[236:239], a[196:199], a[52:55], v[236:239]\n" \
"ds_read_b64_tr_b16 a[196:197], v33 offset:18432\n" \
"ds_read_b64_tr_b16 a[198:199], v33 offset:23040\n" \
"v_mfma_f32_16x16x32_bf16 v[236:239], a[192:195], a[48:51], v[236:239]\n" \
"ds_read_b64_tr_b16 a[192:193], v10 offset:55296\n" \
"ds_read_b64_tr_b16 a[194:195], v10 offset:59904\n" \
"buffer_load_dwordx4 v[140:143], v38, s[56:59], s93 offen offset:2048 sc1\n" \
"v_mfma_f32_16x16x32_bf16 v[160:163], a[208:211], a[128:131], v[160:163]\n" \
"v_readlane_b32 s93, v11, 41\n" \
"v_mfma_f32_16x16x32_bf16 v[160:163], a[212:215], a[132:135], v[160:163]\n" \
"v_mfma_f32_16x16x32_bf16 v[160:163], a[216:219], a[136:139], v[160:163]\n" \
"s_waitcnt lgkmcnt(14)\n" \
"v_mfma_f32_16x16x32_bf16 v[160:163], a[0:3], a[140:143], v[160:163]\n" \
"buffer_load_dword v37, v40, s[56:59], s74 offen sc1\n" \
"v_mfma_f32_16x16x32_bf16 v[164:167], a[0:3], a[156:159], v[164:167]\n" \
"v_mfma_f32_16x16x32_bf16 v[164:167], a[216:219], a[152:155], v[164:167]\n" \
"v_mfma_f32_16x16x32_bf16 v[164:167], a[212:215], a[148:151], v[164:167]\n" \
"v_mfma_f32_16x16x32_bf16 v[164:167], a[208:211], a[144:147], v[164:167]\n" \
"v_mfma_f32_16x16x32_bf16 v[232:235], a[208:211], a[160:163], v[232:235]\n" \
"v_mfma_f32_16x16x32_bf16 v[232:235], a[212:215], a[164:167], v[232:235]\n" \
"v_mfma_f32_16x16x32_bf16 v[232:235], a[216:219], a[168:171], v[232:235]\n" \
"v_mfma_f32_16x16x32_bf16 v[232:235], a[0:3], a[172:175], v[232:235]\n" \
"s_waitcnt vmcnt(11)\n" \
"s_mov_b64 exec, 1\n" \
"buffer_store_dword v39, off, s[56:59], s71 offset:20 sc1\n" \
"s_mov_b64 exec, -1\n" \
"v_mfma_f32_16x16x32_bf16 v[236:239], a[0:3], a[188:191], v[236:239]\n" \
"ds_read_b64_tr_b16 a[0:1], v34 offset:18432\n" \
"ds_read_b64_tr_b16 a[2:3], v34 offset:23040\n" \
"v_mfma_f32_16x16x32_bf16 v[236:239], a[216:219], a[184:187], v[236:239]\n" \
"ds_read_b64_tr_b16 a[216:217], v34 offset:9216\n" \
"ds_read_b64_tr_b16 a[218:219], v34 offset:13824\n" \
"v_mfma_f32_16x16x32_bf16 v[236:239], a[212:215], a[180:183], v[236:239]\n" \
"ds_read_b64_tr_b16 a[212:213], v34 offset:0\n" \
"ds_read_b64_tr_b16 a[214:215], v34 offset:4608\n" \
"v_mfma_f32_16x16x32_bf16 v[236:239], a[208:211], a[176:179], v[236:239]\n" \
"ds_read_b64_tr_b16 a[208:209], v33 offset:46080\n" \
"ds_read_b64_tr_b16 a[210:211], v33 offset:50688\n" \
"v_mfma_f32_16x16x32_bf16 v[160:163], a[4:7], a[96:99], v[160:163]\n" \
"v_mfma_f32_16x16x32_bf16 v[160:163], a[8:11], a[100:103], v[160:163]\n" \
"v_mfma_f32_16x16x32_bf16 v[160:163], a[12:15], a[104:107], v[160:163]\n" \
"s_waitcnt lgkmcnt(8)\n" \
"v_mfma_f32_16x16x32_bf16 v[160:163], a[16:19], a[108:111], v[160:163]\n" \
"v_mfma_f32_16x16x32_bf16 v[164:167], a[16:19], a[124:127], v[164:167]\n" \
"v_mfma_f32_16x16x32_bf16 v[164:167], a[12:15], a[120:123], v[164:167]\n" \
"v_mfma_f32_16x16x32_bf16 v[164:167], a[8:11], a[116:119], v[164:167]\n" \
"v_mfma_f32_16x16x32_bf16 v[164:167], a[4:7], a[112:115], v[164:167]\n" \
"v_mfma_f32_16x16x32_bf16 v[232:235], a[4:7], a[220:223], v[232:235]\n" \
"v_mfma_f32_16x16x32_bf16 v[232:235], a[8:11], a[224:227], v[232:235]\n" \
"v_mfma_f32_16x16x32_bf16 v[232:235], a[12:15], a[228:231], v[232:235]\n" \
"v_mfma_f32_16x16x32_bf16 v[232:235], a[16:19], a[232:235], v[232:235]\n" \
"v_cvt_pk_bf16_f32 v160, v160, v161\n" \
"v_cvt_pk_bf16_f32 v161, v162, v163\n" \
"v_mfma_f32_16x16x32_bf16 v[236:239], a[16:19], a[248:251], v[236:239]\n" \
"ds_read_b64_tr_b16 a[16:17], v10 offset:46112\n" \
"v_cvt_pk_bf16_f32 v162, v164, v165\n" \
"v_cvt_pk_bf16_f32 v163, v166, v167\n" \
"v_mfma_f32_16x16x32_bf16 v[236:239], a[12:15], a[244:247], v[236:239]\n" \
"buffer_store_dwordx4 v[160:163], v20, s[56:59], s95 offen offset:0 sc1\n" \
"v_cvt_pk_bf16_f32 v232, v232, v233\n" \
"v_cvt_pk_bf16_f32 v233, v234, v235\n" \
"v_mfma_f32_16x16x32_bf16 v[236:239], a[8:11], a[240:243], v[236:239]\n" \
"ds_read_b64_tr_b16 a[8:9], v10 offset:27680\n" \
"ds_read_b64_tr_b16 a[10:11], v10 offset:32288\n" \
"v_mfma_f32_16x16x32_bf16 v[236:239], a[4:7], a[236:239], v[236:239]\n" \
"ds_read_b64_tr_b16 a[4:5], v33 offset:0\n" \
"ds_read_b64_tr_b16 a[6:7], v33 offset:4608\n" \
"v_mfma_f32_16x16x32_bf16 v[184:187], a[20:23], a[32:35], v[184:187]\n" \
"ds_read_b64_tr_b16 a[12:13], v10 offset:36896\n" \
"ds_read_b64_tr_b16 a[14:15], v10 offset:41504\n" \
"v_mfma_f32_16x16x32_bf16 v[184:187], a[24:27], a[36:39], v[184:187]\n" \
"ds_read_b64_tr_b16 a[18:19], v10 offset:50720\n" \
"v_mfma_f32_16x16x32_bf16 v[184:187], a[28:31], a[40:43], v[184:187]\n" \
"v_cvt_pk_bf16_f32 v234, v236, v237\n" \
"v_cvt_pk_bf16_f32 v235, v238, v239\n" \
"v_mfma_f32_16x16x32_bf16 v[184:187], a[192:195], a[44:47], v[184:187]\n" \
"buffer_store_dwordx4 v[232:235], v20, s[56:59], s95 offen offset:1024 sc1\n" \
"s_add_u32 s95, s95, 0x800\n" \
"v_mfma_f32_16x16x32_bf16 v[188:191], a[192:195], a[60:63], v[188:191]\n" \
"ds_read_b64_tr_b16 a[192:193], v33 offset:36896\n" \
"ds_read_b64_tr_b16 a[194:195], v33 offset:41504\n" \
"v_mfma_f32_16x16x32_bf16 v[188:191], a[28:31], a[56:59], v[188:191]\n" \
"ds_read_b64_tr_b16 a[28:29], v33 offset:27680\n" \
"ds_read_b64_tr_b16 a[30:31], v33 offset:32288\n" \
"v_mfma_f32_16x16x32_bf16 v[188:191], a[24:27], a[52:55], v[188:191]\n" \
"ds_read_b64_tr_b16 a[24:25], v33 offset:18464\n" \
"ds_read_b64_tr_b16 a[26:27], v33 offset:23072\n" \
"s_mov_b64 exec, 1\n" \
"buffer_store_dword v46, off, s[56:59], s92 offset:0 sc1\n" \
"s_mov_b64 exec, -1\n" \
"v_mfma_f32_16x16x32_bf16 v[188:191], a[20:23], a[48:51], v[188:191]\n" \
"ds_read_b64_tr_b16 a[20:21], v10 offset:55328\n" \
"ds_read_b64_tr_b16 a[22:23], v10 offset:59936\n" \
"v_mfma_f32_16x16x32_bf16 v[184:187], a[196:199], a[160:163], v[184:187]\n" \
"v_mfma_f32_16x16x32_bf16 v[184:187], a[200:203], a[164:167], v[184:187]\n" \
"v_mfma_f32_16x16x32_bf16 v[184:187], a[204:207], a[168:171], v[184:187]\n" \
"s_waitcnt lgkmcnt(15)\n" \
"v_mfma_f32_16x16x32_bf16 v[184:187], a[208:211], a[172:175], v[184:187]\n" \
"v_mfma_f32_16x16x32_bf16 v[188:191], a[208:211], a[188:191], v[188:191]\n" \
"ds_read_b64_tr_b16 a[208:209], v34 offset:18464\n" \
"ds_read_b64_tr_b16 a[210:211], v34 offset:23072\n" \
"v_mfma_f32_16x16x32_bf16 v[188:191], a[204:207], a[184:187], v[188:191]\n" \
"ds_read_b64_tr_b16 a[204:205], v34 offset:9248\n" \
"ds_read_b64_tr_b16 a[206:207], v34 offset:13856\n" \
"v_mfma_f32_16x16x32_bf16 v[188:191], a[200:203], a[180:183], v[188:191]\n" \
"ds_read_b64_tr_b16 a[200:201], v34 offset:32\n" \
"ds_read_b64_tr_b16 a[202:203], v34 offset:4640\n" \
"v_mfma_f32_16x16x32_bf16 v[188:191], a[196:199], a[176:179], v[188:191]\n" \
"ds_read_b64_tr_b16 a[196:197], v33 offset:46112\n" \
"ds_read_b64_tr_b16 a[198:199], v33 offset:50720\n" \
"v_mfma_f32_16x16x32_bf16 v[184:187], a[212:215], a[220:223], v[184:187]\n" \
"v_mfma_f32_16x16x32_bf16 v[184:187], a[216:219], a[224:227], v[184:187]\n" \
"v_mfma_f32_16x16x32_bf16 v[184:187], a[0:3], a[228:231], v[184:187]\n" \
"s_waitcnt lgkmcnt(13)\n" \
"v_mfma_f32_16x16x32_bf16 v[184:187], a[4:7], a[232:235], v[184:187]\n" \
"v_mfma_f32_16x16x32_bf16 v[188:191], a[4:7], a[248:251], v[188:191]\n" \
"ds_read_b64_tr_b16 a[4:5], v10 offset:46144\n" \
"ds_read_b64_tr_b16 a[6:7], v10 offset:50752\n" \
"v_mfma_f32_16x16x32_bf16 v[188:191], a[0:3], a[244:247], v[188:191]\n" \
"ds_read_b64_tr_b16 a[0:1], v10 offset:36928\n" \
"ds_read_b64_tr_b16 a[2:3], v10 offset:41536\n" \
"v_mfma_f32_16x16x32_bf16 v[188:191], a[216:219], a[240:243], v[188:191]\n" \
"ds_read_b64_tr_b16 a[216:217], v10 offset:27712\n" \
"v_cvt_pk_bf16_f32 v184, v184, v185\n" \
"v_cvt_pk_bf16_f32 v185, v186, v187\n" \
"v_mfma_f32_16x16x32_bf16 v[188:191], a[212:215], a[236:239], v[188:191]\n" \
"ds_read_b64_tr_b16 a[212:213], v33 offset:32\n" \
"ds_read_b64_tr_b16 a[214:215], v33 offset:4640\n" \
"s_waitcnt vmcnt(6)\n" \
"s_mov_b64 exec, 1\n" \
"buffer_store_dword v39, off, s[56:59], s94 offset:24 sc1\n" \
"s_mov_b64 exec, -1\n" \
"s_waitcnt vmcnt(5)\n" \
"v_cmp_eq_u32_e64 s[98:99], s62, v37\n" \
"v_cvt_pk_bf16_f32 v186, v188, v189\n" \
"v_cvt_pk_bf16_f32 v187, v190, v191\n" \
"v_mfma_f32_16x16x32_bf16 v[192:195], a[8:11], a[32:35], v[192:195]\n" \
"ds_read_b64_tr_b16 a[218:219], v10 offset:32320\n" \
"s_and_b32 s99, s98, 0x0\n" \
"buffer_store_dwordx4 v[184:187], v20, s[56:59], s97 offen offset:1024 sc1\n" \
"s_add_u32 s97, s97, 0x3800\n" \
"v_mfma_f32_16x16x32_bf16 v[192:195], a[12:15], a[36:39], v[192:195]\n" \
"s_or_b32 s84, s84, s99\n" \
"v_readlane_b32 s94, v11, 39\n" \
"v_mfma_f32_16x16x32_bf16 v[192:195], a[16:19], a[40:43], v[192:195]\n" \
"s_waitcnt lgkmcnt(8)\n" \
"v_mfma_f32_16x16x32_bf16 v[192:195], a[20:23], a[44:47], v[192:195]\n" \
"s_andn2_b32 s99, s100, s98\n" \
"s_cselect_b32 s99, 0, 16\n" \
"v_mfma_f32_16x16x32_bf16 v[196:199], a[20:23], a[60:63], v[196:199]\n" \
"ds_read_b64_tr_b16 a[20:21], v33 offset:36928\n" \
"ds_read_b64_tr_b16 a[22:23], v33 offset:41536\n" \
"v_mfma_f32_16x16x32_bf16 v[196:199], a[16:19], a[56:59], v[196:199]\n" \
"ds_read_b64_tr_b16 a[16:17], v33 offset:27712\n" \
"ds_read_b64_tr_b16 a[18:19], v33 offset:32320\n" \
"v_mfma_f32_16x16x32_bf16 v[196:199], a[12:15], a[52:55], v[196:199]\n" \
"ds_read_b64_tr_b16 a[12:13], v33 offset:18496\n" \
"ds_read_b64_tr_b16 a[14:15], v33 offset:23104\n" \
"v_mfma_f32_16x16x32_bf16 v[196:199], a[8:11], a[48:51], v[196:199]\n" \
"ds_read_b64_tr_b16 a[8:9], v10 offset:55360\n" \
"ds_read_b64_tr_b16 a[10:11], v10 offset:59968\n" \
"v_mfma_f32_16x16x32_bf16 v[192:195], a[24:27], a[160:163], v[192:195]\n" \
"s_or_b32 s84, s84, s99\n" \
"s_bitcmp1_b32 s84, 4\n" \
"s_cselect_b32 s96, s75, 0x40000000\n" \
"buffer_load_dwordx4 v[176:179], v20, s[56:59], s96 offen offset:0 sc1\n" \
"v_mfma_f32_16x16x32_bf16 v[192:195], a[28:31], a[164:167], v[192:195]\n" \
"v_mfma_f32_16x16x32_bf16 v[192:195], a[192:195], a[168:171], v[192:195]\n" \
"v_mfma_f32_16x16x32_bf16 v[192:195], a[196:199], a[172:175], v[192:195]\n" \
"buffer_load_dword v37, v40, s[56:59], s74 offen sc1\n" \
"v_mfma_f32_16x16x32_bf16 v[196:199], a[196:199], a[188:191], v[196:199]\n" \
"ds_read_b64_tr_b16 a[196:197], v34 offset:18496\n" \
"ds_read_b64_tr_b16 a[198:199], v34 offset:23104\n" \
"v_mfma_f32_16x16x32_bf16 v[196:199], a[192:195], a[184:187], v[196:199]\n" \
"ds_read_b64_tr_b16 a[192:193], v34 offset:9280\n" \
"ds_read_b64_tr_b16 a[194:195], v34 offset:13888\n" \
"v_mfma_f32_16x16x32_bf16 v[196:199], a[28:31], a[180:183], v[196:199]\n" \
"buffer_load_dwordx4 v[248:251], v20, s[56:59], s96 offen offset:2048 sc1\n" \
"v_mfma_f32_16x16x32_bf16 v[196:199], a[24:27], a[176:179], v[196:199]\n" \
"ds_read_b64_tr_b16 a[24:25], v33 offset:46144\n" \
"ds_read_b64_tr_b16 a[26:27], v33 offset:50752\n" \
"v_mfma_f32_16x16x32_bf16 v[192:195], a[200:203], a[220:223], v[192:195]\n" \
"ds_read_b64_tr_b16 a[28:29], v34 offset:64\n" \
"ds_read_b64_tr_b16 a[30:31], v34 offset:4672\n" \
"L_pf23:\n" \
"s_cbranch_execz L_retB\n" \
"s_nop 0\n" \
"v_mfma_f32_16x16x32_bf16 v[192:195], a[204:207], a[224:227], v[192:195]\n" \
"buffer_load_dwordx4 v[180:183], v38, s[56:59], s96 offen offset:0 sc1\n" \
"v_mfma_f32_16x16x32_bf16 v[192:195], a[208:211], a[228:231], v[192:195]\n" \
"s_waitcnt lgkmcnt(14)\n" \
"v_mfma_f32_16x16x32_bf16 v[192:195], a[212:215], a[232:235], v[192:195]\n" \
"v_mfma_f32_16x16x32_bf16 v[196:199], a[212:215], a[248:251], v[196:199]\n" \
"buffer_load_dwordx4 v[252:255], v38, s[56:59], s96 offen offset:2048 sc1\n" \
"v_mfma_f32_16x16x32_bf16 v[196:199], a[208:211], a[244:247], v[196:199]\n" \
"ds_read_b64_tr_b16 a[208:209], v10 offset:36960\n" \
"ds_read_b64_tr_b16 a[210:211], v10 offset:41568\n" \
"v_mfma_f32_16x16x32_bf16 v[196:199], a[204:207], a[240:243], v[196:199]\n" \
"ds_read_b64_tr_b16 a[204:205], v10 offset:27744\n" \
"ds_read_b64_tr_b16 a[206:207], v10 offset:32352\n" \
"v_cvt_pk_bf16_f32 v192, v192, v193\n" \
"v_cvt_pk_bf16_f32 v193, v194, v195\n" \
"v_mfma_f32_16x16x32_bf16 v[196:199], a[200:203], a[236:239], v[196:199]\n" \
"buffer_load_dwordx4 v[112:115], v47, s[56:59], s96 offen offset:0 sc1\n" \
"v_mfma_f32_16x16x32_bf16 v[200:203], a[216:219], a[32:35], v[200:203]\n" \
"ds_read_b64_tr_b16 a[200:201], v33 offset:64\n" \
"ds_read_b64_tr_b16 a[202:203], v33 offset:4672\n" \
"v_mfma_f32_16x16x32_bf16 v[200:203], a[0:3], a[36:39], v[200:203]\n" \
"ds_read_b64_tr_b16 a[212:213], v10 offset:46176\n" \
"ds_read_b64_tr_b16 a[214:215], v10 offset:50784\n" \
"v_mfma_f32_16x16x32_bf16 v[200:203], a[4:7], a[40:43], v[200:203]\n" \
"v_cvt_pk_bf16_f32 v194, v196, v197\n" \
"v_cvt_pk_bf16_f32 v195, v198, v199\n" \
"s_waitcnt lgkmcnt(13)\n" \
"v_mfma_f32_16x16x32_bf16 v[200:203], a[8:11], a[44:47], v[200:203]\n" \
"buffer_store_dwordx4 v[192:195], v20, s[56:59], s64 offen offset:1024 sc1\n" \
"s_add_u32 s64, s64, 0x3000\n" \
"v_mfma_f32_16x16x32_bf16 v[204:207], a[8:11], a[60:63], v[204:207]\n" \
"ds_read_b64_tr_b16 a[8:9], v33 offset:36960\n" \
"ds_read_b64_tr_b16 a[10:11], v33 offset:41568\n" \
"v_mfma_f32_16x16x32_bf16 v[204:207], a[4:7], a[56:59], v[204:207]\n" \
"ds_read_b64_tr_b16 a[4:5], v33 offset:27744\n" \
"ds_read_b64_tr_b16 a[6:7], v33 offset:32352\n" \
"v_mfma_f32_16x16x32_bf16 v[204:207], a[0:3], a[52:55], v[204:207]\n" \
"buffer_load_dwordx4 v[120:123], v47, s[56:59], s96 offen offset:2048 sc1\n" \
"v_mfma_f32_16x16x32_bf16 v[204:207], a[216:219], a[48:51], v[204:207]\n" \
"ds_read_b64_tr_b16 a[216:217], v10 offset:55392\n" \
"ds_read_b64_tr_b16 a[218:219], v10 offset:60000\n" \
"v_mfma_f32_16x16x32_bf16 v[200:203], a[12:15], a[160:163], v[200:203]\n" \
"ds_read_b64_tr_b16 a[0:1], v33 offset:18528\n" \
"ds_read_b64_tr_b16 a[2:3], v33 offset:23136\n" \
"v_mfma_f32_16x16x32_bf16 v[200:203], a[16:19], a[164:167], v[200:203]\n" \
"buffer_load_dwordx4 v[128:131], v54, s[56:59], s96 offen offset:0 sc1\n" \
"v_mfma_f32_16x16x32_bf16 v[200:203], a[20:23], a[168:171], v[200:203]\n" \
"s_waitcnt lgkmcnt(15)\n" \
"v_mfma_f32_16x16x32_bf16 v[200:203], a[24:27], a[172:175], v[200:203]\n" \
"v_mfma_f32_16x16x32_bf16 v[204:207], a[24:27], a[188:191], v[204:207]\n" \
"buffer_load_dwordx4 v[136:139], v54, s[56:59], s96 offen offset:2048 sc1\n" \
"s_waitcnt vmcnt(13)\n" \
"s_mov_b64 exec, 1\n" \
"buffer_store_dword v39, off, s[56:59], s95 offset:28 sc1\n" \
"s_mov_b64 exec, -1\n" \
"v_mfma_f32_16x16x32_bf16 v[204:207], a[20:23], a[184:187], v[204:207]\n" \
"ds_read_b64_tr_b16 a[20:21], v34 offset:9312\n" \
"ds_read_b64_tr_b16 a[22:23], v34 offset:13920\n" \
"v_mfma_f32_16x16x32_bf16 v[204:207], a[16:19], a[180:183], v[204:207]\n" \
"ds_read_b64_tr_b16 a[16:17], v34 offset:96\n" \
"ds_read_b64_tr_b16 a[18:19], v34 offset:4704\n" \
"v_mfma_f32_16x16x32_bf16 v[204:207], a[12:15], a[176:179], v[204:207]\n" \
"ds_read_b64_tr_b16 a[12:13], v33 offset:46176\n" \
"ds_read_b64_tr_b16 a[14:15], v33 offset:50784\n" \
"v_mfma_f32_16x16x32_bf16 v[200:203], a[28:31], a[220:223], v[200:203]\n" \
"ds_read_b64_tr_b16 a[24:25], v34 offset:18528\n" \
"ds_read_b64_tr_b16 a[26:27], v34 offset:23136\n" \
"v_mfma_f32_16x16x32_bf16 v[200:203], a[192:195], a[224:227], v[200:203]\n" \
"v_readlane_b32 s95, v11, 40\n" \
"v_mfma_f32_16x16x32_bf16 v[200:203], a[196:199], a[228:231], v[200:203]\n" \
"s_waitcnt lgkmcnt(12)\n" \
"v_mfma_f32_16x16x32_bf16 v[200:203], a[200:203], a[232:235], v[200:203]\n" \
"v_mfma_f32_16x16x32_bf16 v[204:207], a[200:203], a[248:251], v[204:207]\n" \
"ds_read_b64_tr_b16 a[200:201], v10 offset:46304\n" \
"ds_read_b64_tr_b16 a[202:203], v10 offset:50912\n" \
"v_mfma_f32_16x16x32_bf16 v[204:207], a[196:199], a[244:247], v[204:207]\n" \
"ds_read_b64_tr_b16 a[196:197], v10 offset:37088\n" \
"ds_read_b64_tr_b16 a[198:199], v10 offset:41696\n" \
"v_mfma_f32_16x16x32_bf16 v[204:207], a[192:195], a[240:243], v[204:207]\n" \
"ds_read_b64_tr_b16 a[192:193], v10 offset:27872\n" \
"v_cvt_pk_bf16_f32 v200, v200, v201\n" \
"v_cvt_pk_bf16_f32 v201, v202, v203\n" \
"v_mfma_f32_16x16x32_bf16 v[204:207], a[28:31], a[236:239], v[204:207]\n" \
"ds_read_b64_tr_b16 a[28:29], v33 offset:96\n" \
"ds_read_b64_tr_b16 a[30:31], v33 offset:4704\n" \
"v_mfma_f32_16x16x32_bf16 v[208:211], a[204:207], a[32:35], v[208:211]\n" \
"ds_read_b64_tr_b16 a[194:195], v10 offset:32480\n" \
"v_mfma_f32_16x16x32_bf16 v[208:211], a[208:211], a[36:39], v[208:211]\n" \
"v_mfma_f32_16x16x32_bf16 v[208:211], a[212:215], a[40:43], v[208:211]\n" \
"s_waitcnt lgkmcnt(15)\n" \
"v_mfma_f32_16x16x32_bf16 v[208:211], a[216:219], a[44:47], v[208:211]\n" \
"v_cvt_pk_bf16_f32 v202, v204, v205\n" \
"v_cvt_pk_bf16_f32 v203, v206, v207\n" \
"v_mfma_f32_16x16x32_bf16 v[212:215], a[216:219], a[60:63], v[212:215]\n" \
"buffer_store_dwordx4 v[200:203], v20, s[56:59], s66 offen offset:1024 sc1\n" \
"s_add_u32 s66, s66, 0x2800\n" \
"v_mfma_f32_16x16x32_bf16 v[212:215], a[212:215], a[56:59], v[212:215]\n" \
"ds_read_b64_tr_b16 a[212:213], v33 offset:27872\n" \
"ds_read_b64_tr_b16 a[214:215], v33 offset:32480\n" \
"v_mfma_f32_16x16x32_bf16 v[212:215], a[208:211], a[52:55], v[212:215]\n" \
"ds_read_b64_tr_b16 a[208:209], v33 offset:18656\n" \
"ds_read_b64_tr_b16 a[210:211], v33 offset:23264\n" \
"v_mfma_f32_16x16x32_bf16 v[212:215], a[204:207], a[48:51], v[212:215]\n" \
"ds_read_b64_tr_b16 a[204:205], v10 offset:55520\n" \
"ds_read_b64_tr_b16 a[206:207], v10 offset:60128\n" \
"v_mfma_f32_16x16x32_bf16 v[208:211], a[0:3], a[160:163], v[208:211]\n" \
"ds_read_b64_tr_b16 a[216:217], v33 offset:37088\n" \
"ds_read_b64_tr_b16 a[218:219], v33 offset:41696\n" \
"v_mfma_f32_16x16x32_bf16 v[208:211], a[4:7], a[164:167], v[208:211]\n" \
"v_mfma_f32_16x16x32_bf16 v[208:211], a[8:11], a[168:171], v[208:211]\n" \
"s_waitcnt lgkmcnt(14)\n" \
"v_mfma_f32_16x16x32_bf16 v[208:211], a[12:15], a[172:175], v[208:211]\n" \
"v_mfma_f32_16x16x32_bf16 v[212:215], a[12:15], a[188:191], v[212:215]\n" \
"ds_read_b64_tr_b16 a[12:13], v34 offset:18656\n" \
"ds_read_b64_tr_b16 a[14:15], v34 offset:23264\n" \
"s_waitcnt vmcnt(12)\n" \
"s_mov_b64 exec, 1\n" \
"buffer_store_dword v39, off, s[56:59], s97 offset:32 sc1\n" \
"s_mov_b64 exec, -1\n" \
"v_mfma_f32_16x16x32_bf16 v[212:215], a[8:11], a[184:187], v[212:215]\n" \
"ds_read_b64_tr_b16 a[8:9], v34 offset:9440\n" \
"ds_read_b64_tr_b16 a[10:11], v34 offset:14048\n" \
"v_mfma_f32_16x16x32_bf16 v[212:215], a[4:7], a[180:183], v[212:215]\n" \
"ds_read_b64_tr_b16 a[4:5], v34 offset:224\n" \
"ds_read_b64_tr_b16 a[6:7], v34 offset:4832\n" \
"v_mfma_f32_16x16x32_bf16 v[212:215], a[0:3], a[176:179], v[212:215]\n" \
"ds_read_b64_tr_b16 a[0:1], v33 offset:46304\n" \
"ds_read_b64_tr_b16 a[2:3], v33 offset:50912\n" \
"v_mfma_f32_16x16x32_bf16 v[208:211], a[16:19], a[220:223], v[208:211]\n" \
"s_waitcnt vmcnt(11)\n" \
"v_cmp_eq_u32_e64 s[98:99], s62, v37\n" \
"buffer_load_dword v37, v40, s[56:59], s74 offen sc1\n" \
"v_mfma_f32_16x16x32_bf16 v[208:211], a[20:23], a[224:227], v[208:211]\n" \
"s_and_b32 s99, s98, 0x20\n" \
"s_or_b32 s84, s84, s99\n" \
"s_bitcmp1_b32 s84, 5\n" \
"s_cselect_b32 s65, s63, 0x40000000\n" \
"v_mfma_f32_16x16x32_bf16 v[208:211], a[24:27], a[228:231], v[208:211]\n" \
"s_waitcnt lgkmcnt(12)\n" \
"v_mfma_f32_16x16x32_bf16 v[208:211], a[28:31], a[232:235], v[208:211]\n" \
"v_mfma_f32_16x16x32_bf16 v[212:215], a[28:31], a[248:251], v[212:215]\n" \
"buffer_load_dwordx4 v[148:151], v47, s[56:59], s65 offen offset:0 sc1\n" \
"v_mfma_f32_16x16x32_bf16 v[212:215], a[24:27], a[244:247], v[212:215]\n" \
"ds_read_b64_tr_b16 a[24:25], v36 offset:2048\n" \
"ds_read_b64_tr_b16 a[26:27], v36 offset:3072\n" \
"v_mfma_f32_16x16x32_bf16 v[212:215], a[20:23], a[240:243], v[212:215]\n" \
"ds_read_b64_tr_b16 a[20:21], v36 offset:0\n" \
"ds_read_b64_tr_b16 a[22:23], v36 offset:1024\n" \
"v_cvt_pk_bf16_f32 v208, v208, v209\n" \
"v_cvt_pk_bf16_f32 v209, v210, v211\n" \
"v_mfma_f32_16x16x32_bf16 v[212:215], a[16:19], a[236:239], v[212:215]\n" \
"ds_read_b64_tr_b16 a[16:17], v33 offset:224\n" \
"ds_read_b64_tr_b16 a[18:19], v33 offset:4832\n" \
"v_mfma_f32_16x16x32_bf16 v[240:243], a[192:195], a[32:35], v[240:243]\n" \
"buffer_load_dwordx4 v[220:223], v47, s[56:59], s65 offen offset:1024 sc1\n" \
"v_mfma_f32_16x16x32_bf16 v[240:243], a[196:199], a[36:39], v[240:243]\n" \
"ds_read_b64_tr_b16 a[28:29], v36 offset:32\n" \
"ds_read_b64_tr_b16 a[30:31], v36 offset:1056\n" \
"v_mfma_f32_16x16x32_bf16 v[240:243], a[200:203], a[40:43], v[240:243]\n" \
"v_cvt_pk_bf16_f32 v210, v212, v213\n" \
"v_cvt_pk_bf16_f32 v211, v214, v215\n" \
"s_waitcnt lgkmcnt(10)\n" \
"v_mfma_f32_16x16x32_bf16 v[240:243], a[204:207], a[44:47], v[240:243]\n" \
"buffer_store_dwordx4 v[208:211], v20, s[56:59], s67 offen offset:1024 sc1\n" \
"s_add_u32 s67, s67, 0x2000\n" \
"v_mfma_f32_16x16x32_bf16 v[244:247], a[204:207], a[60:63], v[244:247]\n" \
"v_mfma_f32_16x16x32_bf16 v[244:247], a[200:203], a[56:59], v[244:247]\n" \
"v_mfma_f32_16x16x32_bf16 v[244:247], a[196:199], a[52:55], v[244:247]\n" \
"v_mfma_f32_16x16x32_bf16 v[244:247], a[192:195], a[48:51], v[244:247]\n" \
"ds_read_b64_tr_b16 a[192:193], v36 offset:2080\n" \
"ds_read_b64_tr_b16 a[194:195], v36 offset:3104\n" \
"v_mfma_f32_16x16x32_bf16 v[168:171], a[208:211], a[128:131], v[168:171]\n" \
"v_mfma_f32_16x16x32_bf16 v[168:171], a[212:215], a[132:135], v[168:171]\n" \
"v_mfma_f32_16x16x32_bf16 v[168:171], a[216:219], a[136:139], v[168:171]\n" \
"s_waitcnt lgkmcnt(9)\n" \
"v_mfma_f32_16x16x32_bf16 v[168:171], a[0:3], a[140:143], v[168:171]\n" \
"v_mfma_f32_16x16x32_bf16 v[172:175], a[0:3], a[156:159], v[172:175]\n" \
"v_mfma_f32_16x16x32_bf16 v[172:175], a[216:219], a[152:155], v[172:175]\n" \
"s_waitcnt vmcnt(10)\n" \
"s_mov_b64 exec, 1\n" \
"buffer_store_dword v39, off, s[56:59], s64 offset:36 sc1\n" \
"s_mov_b64 exec, -1\n" \
"v_mfma_f32_16x16x32_bf16 v[172:175], a[212:215], a[148:151], v[172:175]\n" \
"v_mfma_f32_16x16x32_bf16 v[172:175], a[208:211], a[144:147], v[172:175]\n" \
"v_mfma_f32_16x16x32_bf16 v[240:243], a[208:211], a[160:163], v[240:243]\n" \
"v_mfma_f32_16x16x32_bf16 v[240:243], a[212:215], a[164:167], v[240:243]\n" \
"v_mfma_f32_16x16x32_bf16 v[240:243], a[216:219], a[168:171], v[240:243]\n" \
"v_mfma_f32_16x16x32_bf16 v[240:243], a[0:3], a[172:175], v[240:243]\n" \
"v_mfma_f32_16x16x32_bf16 v[244:247], a[0:3], a[188:191], v[244:247]\n" \
"v_mfma_f32_16x16x32_bf16 v[244:247], a[216:219], a[184:187], v[244:247]\n" \
"v_mfma_f32_16x16x32_bf16 v[244:247], a[212:215], a[180:183], v[244:247]\n" \
"v_mfma_f32_16x16x32_bf16 v[244:247], a[208:211], a[176:179], v[244:247]\n" \
"v_mfma_f32_16x16x32_bf16 v[168:171], a[4:7], a[96:99], v[168:171]\n" \
"v_mfma_f32_16x16x32_bf16 v[168:171], a[8:11], a[100:103], v[168:171]\n" \
"v_mfma_f32_16x16x32_bf16 v[168:171], a[12:15], a[104:107], v[168:171]\n" \
"s_waitcnt vmcnt(6)\n" \
"s_mov_b64 exec, 1\n" \
"buffer_store_dword v39, off, s[56:59], s66 offset:40 sc1\n" \
"s_mov_b64 exec, -1\n" \
"s_waitcnt lgkmcnt(0)\n" \
"v_mfma_f32_16x16x32_bf16 v[168:171], a[16:19], a[108:111], v[168:171]\n" \
"v_mfma_f32_16x16x32_bf16 v[172:175], a[16:19], a[124:127], v[172:175]\n" \
"v_mfma_f32_16x16x32_bf16 v[172:175], a[12:15], a[120:123], v[172:175]\n" \
"v_mfma_f32_16x16x32_bf16 v[172:175], a[8:11], a[116:119], v[172:175]\n" \
"v_mfma_f32_16x16x32_bf16 v[172:175], a[4:7], a[112:115], v[172:175]\n" \
"v_mfma_f32_16x16x32_bf16 v[240:243], a[4:7], a[220:223], v[240:243]\n" \
"v_mfma_f32_16x16x32_bf16 v[240:243], a[8:11], a[224:227], v[240:243]\n" \
"v_mfma_f32_16x16x32_bf16 v[240:243], a[12:15], a[228:231], v[240:243]\n" \
"v_mfma_f32_16x16x32_bf16 v[240:243], a[16:19], a[232:235], v[240:243]\n" \
"v_mfma_f32_16x16x32_bf16 v[244:247], a[16:19], a[248:251], v[244:247]\n" \
"v_mfma_f32_16x16x32_bf16 v[244:247], a[12:15], a[244:247], v[244:247]\n" \
"v_mfma_f32_16x16x32_bf16 v[244:247], a[8:11], a[240:243], v[244:247]\n" \
"v_mfma_f32_16x16x32_bf16 v[244:247], a[4:7], a[236:239], v[244:247]\n" \
"v_mfma_f32_16x16x32_bf16 v[168:171], a[20:23], v[64:67], v[168:171]\n" \
"v_mfma_f32_16x16x32_bf16 v[172:175], a[20:23], v[72:75], v[172:175]\n" \
"v_mfma_f32_16x16x32_bf16 v[240:243], a[20:23], v[80:83], v[240:243]\n" \
"v_mfma_f32_16x16x32_bf16 v[244:247], a[20:23], v[88:91], v[244:247]\n" \
"v_mfma_f32_16x16x32_bf16 v[168:171], a[24:27], v[68:71], v[168:171]\n" \
"v_mfma_f32_16x16x32_bf16 v[172:175], a[24:27], v[76:79], v[172:175]\n" \
"v_mfma_f32_16x16x32_bf16 v[240:243], a[24:27], v[84:87], v[240:243]\n" \
"s_waitcnt vmcnt(5)\n" \
"v_cmp_eq_u32_e64 s[98:99], s62, v37\n" \
"v_mfma_f32_16x16x32_bf16 v[244:247], a[24:27], v[92:95], v[244:247]\n" \
"s_and_b32 s99, s98, 0xfc0\n" \
"s_or_b32 s84, s84, s99\n" \
"s_bitcmp1_b32 s84, 6\n" \
"s_cselect_b32 s68, s63, 0x40000000\n" \
"s_bitcmp1_b32 s84, 7\n" \
"s_cselect_b32 s69, s63, 0x40000000\n" \
"s_bitcmp1_b32 s84, 8\n" \
"s_cselect_b32 s70, s63, 0x40000000\n" \
"s_bitcmp1_b32 s84, 9\n" \
"s_cselect_b32 s71, s63, 0x40000000\n" \
"s_bitcmp1_b32 s84, 10\n" \
"s_cselect_b32 s72, s63, 0x40000000\n" \
"s_bitcmp1_b32 s84, 11\n" \
"s_cselect_b32 s73, s63, 0x40000000\n" \
"buffer_load_dwordx4 v[156:159], v47, s[56:59], s68 offen offset:2048 sc1\n" \
"v_mfma_f32_16x16x32_bf16 v[56:59], a[28:31], v[96:99], 0\n" \
"s_waitcnt vmcnt(3)\n" \
"s_mov_b64 exec, 1\n" \
"buffer_store_dword v39, off, s[56:59], s67 offset:44 sc1\n" \
"s_mov_b64 exec, -1\n" \
"v_mfma_f32_16x16x32_bf16 v[60:63], a[28:31], v[104:107], 0\n" \
"v_mfma_f32_16x16x32_bf16 v[56:59], a[192:195], v[100:103], v[56:59]\n" \
"v_mfma_f32_16x16x32_bf16 v[60:63], a[192:195], v[108:111], v[60:63]\n" \
"buffer_load_dwordx4 v[228:231], v47, s[56:59], s68 offen offset:3072 sc1\n" \
"buffer_load_dwordx4 v[164:167], v54, s[56:59], s69 offen offset:0 sc1\n" \
"buffer_load_dwordx4 v[236:239], v54, s[56:59], s69 offen offset:1024 sc1\n" \
"buffer_load_dwordx4 v[188:191], v20, s[56:59], s70 offen offset:1024 sc1\n" \
"buffer_load_dwordx4 v[196:199], v20, s[56:59], s71 offen offset:3072 sc1\n" \
"buffer_load_dwordx4 v[204:207], v38, s[56:59], s72 offen offset:1024 sc1\n" \
"buffer_load_dwordx4 v[212:215], v38, s[56:59], s73 offen offset:3072 sc1\n" \
"s_andn2_b32 s99, 0xff, s84\n" \
"s_cbranch_scc1 L_sx0_%=\n" \
"L_rx0_%=:\n" \
"v_mfma_f32_16x16x32_bf16 v[168:171], v[50:53], v[116:119], v[168:171]\n" \
"v_mfma_f32_16x16x32_bf16 v[172:175], v[48:51], v[116:119], v[172:175]\n" \
"v_mfma_f32_16x16x32_bf16 v[168:171], v[50:53], v[124:127], v[168:171]\n" \
"v_mfma_f32_16x16x32_bf16 v[172:175], v[48:51], v[124:127], v[172:175]\n" \
"v_mfma_f32_16x16x32_bf16 v[168:171], v[50:53], v[132:135], v[168:171]\n" \
"v_mfma_f32_16x16x32_bf16 v[172:175], v[48:51], v[132:135], v[172:175]\n" \
"v_mfma_f32_16x16x32_bf16 v[168:171], v[50:53], v[140:143], v[168:171]\n" \
"v_mfma_f32_16x16x32_bf16 v[172:175], v[48:51], v[140:143], v[172:175]\n" \
"v_mfma_f32_16x16x32_bf16 v[56:59], v[50:53], v[176:179], v[56:59]\n" \
"v_mfma_f32_16x16x32_bf16 v[60:63], v[48:51], v[176:179], v[60:63]\n" \
"v_mfma_f32_16x16x32_bf16 v[56:59], v[50:53], v[248:251], v[56:59]\n" \
"v_mfma_f32_16x16x32_bf16 v[60:63], v[48:51], v[248:251], v[60:63]\n" \
"v_mfma_f32_16x16x32_bf16 v[56:59], v[50:53], v[180:183], v[56:59]\n" \
"v_mfma_f32_16x16x32_bf16 v[60:63], v[48:51], v[180:183], v[60:63]\n" \
"v_mfma_f32_16x16x32_bf16 v[56:59], v[50:53], v[252:255], v[56:59]\n" \
"v_mfma_f32_16x16x32_bf16 v[60:63], v[48:51], v[252:255], v[60:63]\n" \
"v_mfma_f32_16x16x32_bf16 v[56:59], v[50:53], v[112:115], v[56:59]\n" \
"v_mfma_f32_16x16x32_bf16 v[60:63], v[48:51], v[112:115], v[60:63]\n" \
"v_mfma_f32_16x16x32_bf16 v[56:59], v[50:53], v[120:123], v[56:59]\n" \
"v_mfma_f32_16x16x32_bf16 v[60:63], v[48:51], v[120:123], v[60:63]\n" \
"v_mfma_f32_16x16x32_bf16 v[56:59], v[50:53], v[128:131], v[56:59]\n" \
"v_mfma_f32_16x16x32_bf16 v[60:63], v[48:51], v[128:131], v[60:63]\n" \
"v_mfma_f32_16x16x32_bf16 v[56:59], v[50:53], v[136:139], v[56:59]\n" \
"v_mfma_f32_16x16x32_bf16 v[60:63], v[48:51], v[136:139], v[60:63]\n" \
"v_mfma_f32_16x16x32_bf16 v[168:171], v[50:53], v[148:151], v[168:171]\n" \
"v_mfma_f32_16x16x32_bf16 v[172:175], v[48:51], v[148:151], v[172:175]\n" \
"v_mfma_f32_16x16x32_bf16 v[240:243], v[50:53], v[220:223], v[240:243]\n" \
"v_mfma_f32_16x16x32_bf16 v[244:247], v[48:51], v[220:223], v[244:247]\n" \
"s_waitcnt vmcnt(8)\n" \
"v_mfma_f32_16x16x32_bf16 v[168:171], v[50:53], v[156:159], v[168:171]\n" \
"v_mfma_f32_16x16x32_bf16 v[172:175], v[48:51], v[156:159], v[172:175]\n" \
"v_cvt_pk_bf16_f32 v56, v56, v57\n" \
"v_cvt_pk_bf16_f32 v57, v58, v59\n" \
"v_cvt_pk_bf16_f32 v58, v60, v61\n" \
"v_cvt_pk_bf16_f32 v59, v62, v63\n" \
"s_waitcnt vmcnt(6)\n" \
"v_mfma_f32_16x16x32_bf16 v[240:243], v[50:53], v[228:231], v[240:243]\n" \
"v_permlane16_swap_b32_e32 v56, v58\n" \
"v_mfma_f32_16x16x32_bf16 v[244:247], v[48:51], v[228:231], v[244:247]\n" \
"v_permlane16_swap_b32_e32 v57, v59\n" \
"buffer_store_dwordx4 v[56:59], v42, s[88:91], s93 offen sc0 sc1\n" \
"s_waitcnt vmcnt(6)\n" \
"v_mfma_f32_16x16x32_bf16 v[168:171], v[50:53], v[164:167], v[168:171]\n" \
"v_mfma_f32_16x16x32_bf16 v[172:175], v[48:51], v[164:167], v[172:175]\n" \
"s_waitcnt vmcnt(5)\n" \
"v_mfma_f32_16x16x32_bf16 v[240:243], v[50:53], v[236:239], v[240:243]\n" \
"v_mfma_f32_16x16x32_bf16 v[244:247], v[48:51], v[236:239], v[244:247]\n" \
"s_andn2_b32 s99, 0xf00, s84\n" \
"s_cbranch_scc1 L_sx1_%=\n" \
"L_rx1_%=:\n" \
"s_waitcnt vmcnt(4)\n" \
"v_mfma_f32_16x16x32_bf16 v[240:243], v[50:53], v[188:191], v[240:243]\n" \
"v_cvt_pk_bf16_f32 v168, v168, v169\n" \
"v_cvt_pk_bf16_f32 v169, v170, v171\n" \
"v_mfma_f32_16x16x32_bf16 v[244:247], v[48:51], v[188:191], v[244:247]\n" \
"v_cvt_pk_bf16_f32 v170, v172, v173\n" \
"v_cvt_pk_bf16_f32 v171, v174, v175\n" \
"s_waitcnt vmcnt(3)\n" \
"v_mfma_f32_16x16x32_bf16 v[240:243], v[50:53], v[196:199], v[240:243]\n" \
"v_permlane16_swap_b32_e32 v168, v170\n" \
"v_mfma_f32_16x16x32_bf16 v[244:247], v[48:51], v[196:199], v[244:247]\n" \
"v_permlane16_swap_b32_e32 v169, v171\n" \
"buffer_store_dwordx4 v[168:171], v42, s[88:91], s94 offen sc0 sc1\n" \
"s_waitcnt vmcnt(3)\n" \
"v_mfma_f32_16x16x32_bf16 v[240:243], v[50:53], v[204:207], v[240:243]\n" \
"v_mfma_f32_16x16x32_bf16 v[244:247], v[48:51], v[204:207], v[244:247]\n" \
"s_waitcnt vmcnt(2)\n" \
"v_mfma_f32_16x16x32_bf16 v[240:243], v[50:53], v[212:215], v[240:243]\n" \
"v_mfma_f32_16x16x32_bf16 v[244:247], v[48:51], v[212:215], v[244:247]\n" \
"s_nop 6\n" \
"v_cvt_pk_bf16_f32 v240, v240, v241\n" \
"v_cvt_pk_bf16_f32 v241, v242, v243\n" \
"v_cvt_pk_bf16_f32 v242, v244, v245\n" \
"v_cvt_pk_bf16_f32 v243, v246, v247\n" \
"s_nop 0\n" \
"v_permlane16_swap_b32_e32 v240, v242\n" \
"v_permlane16_swap_b32_e32 v241, v243\n" \
"buffer_store_dwordx4 v[240:243], v42, s[88:91], s95 offen sc0 sc1\n"
#define ATT_VCLOB "v21","v22","v23","v32","v33","v34","v35","v36","v37","v38","v39","v40","v41","v42","v43","v44","v45","v46","v47","v48","v49","v50","v51","v52","v53","v54","v55","v56","v57","v58","v59","v60","v61","v62","v63","v64","v65","v66","v67","v68","v69","v70","v71","v72","v73","v74","v75","v76","v77","v78","v79","v80","v81","v82","v83","v84","v85","v86","v87","v88","v89","v90","v91","v92","v93","v94","v95","v96","v97","v98","v99","v100","v101","v102","v103","v104","v105","v106","v107","v108","v109","v110","v111","v112","v113","v114","v115","v116","v117","v118","v119","v120","v121","v122","v123","v124","v125","v126","v127","v128","v129","v130","v131","v132","v133","v134","v135","v136","v137","v138","v139","v140","v141","v142","v143","v144","v145","v146","v147","v148","v149","v150","v151","v152","v153","v154","v155","v156","v157","v158","v159","v160","v161","v162","v163","v164","v165","v166","v167","v168","v169","v170","v171","v172","v173","v174","v175","v176","v177","v178","v179","v180","v181","v182","v183","v184","v185","v186","v187","v188","v189","v190","v191","v192","v193","v194","v195","v196","v197","v198","v199","v200","v201","v202","v203","v204","v205","v206","v207","v208","v209","v210","v211","v212","v213","v214","v215","v216","v217","v218","v219","v220","v221","v222","v223","v224","v225","v226","v227","v228","v229","v230","v231","v232","v233","v234","v235","v236","v237","v238","v239","v240","v241","v242","v243","v244","v245","v246","v247","v248","v249","v250","v251","v252","v253","v254","v255"
#define ATT_SCLOB "s63","s64","s65","s66","s67","s68","s69","s70","s71","s72","s73","s74","s75","s76","s77","s78","s79","s80","s81","s82","s83","s84","s85","s86","s87","s88","s89","s90","s91","s92","s93","s95","s96","s97","s98","s99","s100","s101"
enum { T_fo, T_fa, T_fas, T_fb, T_fbs, T_f8, T_mk8, T_wol, T_woh, T_wbl, T_wbh, T_outl, T_outh, T_r1l, T_r1h, T_r1n, T_r1s, T_m0, T_m1, T_mw, T_m4, T_ml, T_w0, T_w1, T_w2, T_w3, T_A1, T_A2, T_A3, T_B1, T_B2, T_B3, T_Ao, T_Bo, T_Al, T_Bl, T_l8, T_xr, T_x8r, T_oA, T_oB, T_o8, T_x0, T_x1, T_x2, T_x3, T_x4, T_x5, T_x6, T_x8, T_ep, T_xf, T_kf };

__device__ __forceinline__ void out_consts(u32x4 (&vp)[3], const u16* wo, const u16* wob, const u16* out, const unsigned b, const unsigned w, const unsigned lane) {
    const unsigned sp = b & 7u, nt = (b >> 3) & 3u, j = b >> 5, head = 4u * sp + nt, OOR = 0x40000000u;
    unsigned vw[9];
#pragma unroll
    for (unsigned i = 0; i < 9u; ++i) {
        const unsigned q = 64u * i + lane, row = __umul24(q, 3641u) >> 16, c = q - __umul24(row, 18u);
        vw[i] = __umul24(row, DM * 2) + (c < 16u ? (c * 16u + sp * 32u + 32u) & 255u : c * 16u) - (i & 3u) * 1024u;
    }
    const unsigned row = 16u * w + (lane >> 2), r9 = __umul24(16u * (row >> 5) + (row & 12u) + 2u * ((row >> 4) & 1u) + ((row >> 1) & 1u), 18u) + 16u + (row & 1u);
    unsigned vwl = __umul24(r9, DM * 2) + ((lane & 2u) ? 256u : sp * 32u) + (lane & 1u) * 16u, vrd = __umul24(lane >> 2, 288u) + (lane & 3u) * 8u;
    asm volatile("" : "+v"(vw[0]), "+v"(vw[1]), "+v"(vw[2]), "+v"(vw[3]), "+v"(vw[4]), "+v"(vw[5]), "+v"(vw[6]), "+v"(vw[7]), "+v"(vw[8]), "+v"(vwl), "+v"(vrd));
    unsigned tab = 0;
#define TB(n, v) asm("v_writelane_b32 %0, %1, %2" : "+v"(tab) : "s"((unsigned)(v)), "n"((int)T_##n))
    const unsigned FJ = (unsigned)WS_F + (F_J + 640u * j) * 4u, mt = 2u * (w & 2u) + (w & 1u), w2 = w < 2u ? w : 2u, hw = head * 147456u;
    const unsigned FR = (unsigned)WS_F + F_R * 4u + (8u * j + sp) * 1024u;
    TB(fo, FJ + 4u * head); TB(fa, FR + (w | 2u) * 256u); TB(fas, FR + w * 256u + nt * 4u);
    TB(fb, FR + 128u + w * 256u); TB(fbs, FR + 128u + w * 256u + nt * 4u);
    TB(ep, b + w ? OOR : (unsigned)WS_F + F_EPOCH * 4u);
    const u16* pwo = wo + nt * 144u; const u16* pwb = wob + nt * 144u; const u16* pr1 = w < 3u ? pwo : pwb;
    TB(wol, PLO(pwo)); TB(woh, PHI(pwo) & 0xffffu); TB(wbl, PLO(pwb)); TB(wbh, PHI(pwb) & 0xffffu); TB(outl, PLO(out)); TB(outh, PHI(out) & 0xffffu); TB(r1l, PLO(pr1)); TB(r1h, PHI(pr1) & 0xffffu);
    TB(r1n, (w < 3u ? 72u * sp + 71u : 4095u) * 1152u + 288u); TB(r1s, w < 3u ? (72u * sp + 32u * w) * 1152u : hw + 110592u);
    TB(m0, 131072u + 9216u * w2); TB(w0, hw + 36864u * w2); TB(m1, w < 3u ? 9216u * w : 73728u); TB(mw, 9216u * w); TB(m4, w < 3u ? 131072u + 9216u * w : 64512u); TB(ml, 119808u + 1024u * w);
    const unsigned tA = (unsigned)WS_AF + (8u * j + mt) * (KS_TOT * 1024u), tB = (unsigned)WS_AF + (120u - 8u * j + mt) * (KS_TOT * 1024u);
#define HK(k) (4u * sp + ((nt + k) & 3u))
    TB(w1, HK(1u) * 147456u + w * 36864u); TB(w2, HK(2u) * 147456u + w * 36864u); TB(w3, HK(3u) * 147456u + w * 36864u);
    TB(A1, tA + 18432u + HK(1u) * 4096u); TB(A2, tA + 18432u + HK(2u) * 4096u); TB(A3, tA + 18432u + HK(3u) * 4096u);
    TB(B1, tB + 18432u + HK(1u) * 4096u); TB(B2, tB + 18432u + HK(2u) * 4096u); TB(B3, tB + 18432u + HK(3u) * 4096u);
    TB(Ao, tA + sp * 2048u); TB(Bo, tB + sp * 2048u); TB(Al, tA + 16384u); TB(Bl, tB + 16384u);
    const bool red = w == (sp >> 1), blk = sp & 1u;
    const unsigned xg = (unsigned)WS_X + (4u * j + nt) * 589824u + w * 16384u, oA = (128u * j + 16u * mt) * 1152u + nt * 288u, oB = (1920u - 128u * j + 16u * mt) * 1152u + nt * 288u;
    TB(l8, red ? (blk ? tB : tA) + 16384u : OOR); TB(xr, xg + sp * 65536u); TB(x8r, red ? xg + 524288u + (sp & 1u) * 1024u : OOR);
    TB(oA, oA + sp * 32u); TB(oB, oB + sp * 32u); TB(o8, red ? (blk ? oB : oA) + 256u : OOR); TB(x8, xg + 524288u + sp * 2048u);
    TB(f8, xg + w * 131072u + 14400u + sp * 4u); TB(mk8, red ? 0xff0000u : 0u); TB(xf, xg + sp * 65536u + 14336u);
    TB(kf, (unsigned)WS_F + (F_KV + head * 64u) * 4u);
#define XI(i) TB(x##i, xg + ((sp + 1u + i) & 7u) * 65536u + i * 2048u)
    XI(0); XI(1); XI(2); XI(3); XI(4); XI(5); XI(6);
#undef XI
#undef HK
#undef TB
    vp[0] = (u32x4){vw[0], vw[1], vw[2], vw[3]}; vp[1] = (u32x4){vw[4], vw[5], vw[6], vw[7]}; vp[2] = (u32x4){vw[8], vwl, vrd, tab};
}
__global__ __launch_bounds__(256, 1) __attribute__((aligned(4096))) void fused_kernel(const u16* __restrict__ x, const u16* __restrict__ ckv, const u16* __restrict__ wq, const u16* __restrict__ wuk,
                                                       const u16* __restrict__ wuv, const u16* __restrict__ wqkv, const u16* __restrict__ gw,
                                                       const u16* __restrict__ wo, const u16* __restrict__ wob, char* __restrict__ ws, u16* __restrict__ out) {
    using namespace att;
    extern __shared__ __attribute__((aligned(16))) char lds[];
    unsigned wid = rfl(threadIdx.x >> 6);
    code_touch((blockIdx.x >> 3) * 4u + wid, (unsigned)(size_t)lds + 151552u + wid * 512u);
    SBAR();
    asm volatile("" :: "s"(x), "s"(ckv), "s"(wq), "s"(wuk), "s"(wuv), "s"(wqkv), "s"(gw), "s"(wo), "s"(wob), "s"(ws), "s"(out));
    unsigned tag;
    int b = blockIdx.x;
    u32x4 vp[5], sa[5]; unsigned sd[2];
    const unsigned vl = proj_phase(x, ckv, wq, wuk, wuv, wqkv, gw, wo, wob, out, ws, lds, wid, tag, vp, sa, sd);
    asm volatile("" : "+s"(ws));
    unsigned* const flags = (unsigned*)(ws + WS_F);
    RTS(2);
#define AST(L) do {} while (0)
#define SD(J8, H, W) ((unsigned)WS_AF + (((J8) + 2u * ((W) & 2u) + ((W) & 1u)) * KS_TOT + 18u + 4u * (H)) * 1024u)
#define SRDA (u32x4){PLO(ws), PHI(ws) & 0xffffu, (unsigned)WS_L, 0x00020000u}
#define ATT_TAIL "s_endpgm\n"
    unsigned abad;
    asm volatile(ATT_ASM ATT_TAIL "L_end_%=:\n s_nop 1"
                 : "=&{s94}"(abad)
                 : "{v[0:3]}"(vp[0]), "{v[4:7]}"(vp[1]), "{v[8:11]}"(vp[2]), "{v[12:15]}"(vp[3]), "{v[16:19]}"(vp[4]), "{v20}"(vl), "{s[56:59]}"(sa[4]), "{s60}"(sd[0]), "{s61}"(sd[1]), "{s62}"(tag),
                   "{s[8:11]}"(sa[0]), "{s[12:15]}"(sa[1]), "{s[16:19]}"(sa[2]), "{s[20:23]}"(sa[3])
                 : "memory", "m0", "vcc", "scc", ATT_VCLOB, ATT_SCLOB, ACLOB_LO, ACLOB_HI);
    {
        const u16* const __attribute__((address_space(4)))* ka = (const u16* const __attribute__((address_space(4)))*)__builtin_amdgcn_kernarg_segment_ptr();
        unsigned b2 = b, w2 = wid;
        asm volatile("s_waitcnt vmcnt(0)" : "+s"(ka), "+s"(b2), "+s"(w2), "+s"(ws) :: "memory");
        const unsigned j2 = b2 >> 5, p2 = w2 & 1u, h2 = (b2 & 7u) * 4u + ((b2 >> 3) & 3u);
#pragma unroll 1
        for (unsigned i = w2 & 2u; i < ((w2 & 2u) ? (abad ? 8u : 4u) : 2u); ++i)
            att_exact(ka[0], ka[2], ws, h2, (i < 4u ? 1920u - 128u * j2 : 128u * j2 - 128u) + 32u * i + 16u * p2, *reinterpret_cast<const float*>(lds + LDS_G + (32u * i + 16u * p2 + (lane_id() & 15u)) * 4u));
        asm volatile("s_waitcnt vmcnt(0)" ::: "memory");
        unsigned* fa = (unsigned*)(ws + WS_F) + F_R + (8u * j2 + (b2 & 7u)) * 256u + (2u + p2) * 64u + ((b2 >> 3) & 3u);
        if (w2 & 2u) st_flag(fa, tag);
        wait_flags(fa, ld_flag(fa), tag);
        const unsigned l2 = lane_id(); u32x4 c[3]; out_consts(c, ka[7], ka[8], ka[10], b2, w2, l2);
        asm volatile("s_waitcnt vmcnt(0) lgkmcnt(0)\n\ts_nop 7\n\ts_branch L_att_stub" :: "{v[0:3]}"(c[0]), "{v[4:7]}"(c[1]), "{v[8:11]}"(c[2]), "{v20}"(l2 * 16u), "{s[56:59]}"(SRDA), "{s60}"(SD(8u * j2, h2, w2)),
                     "{s61}"(SD(120u - 8u * j2, h2, w2)), "{s62}"(tag) : "memory");
    }
    RTS(5);
}

extern "C" void kernel_launch(void* const* d_in, const int* in_sizes, int n_in, void* d_out, int out_size, void* d_ws, size_t ws_size,
                              hipStream_t stream) {
    (void)hipFuncSetAttribute((const void*)fused_kernel, hipFuncAttributeMaxDynamicSharedMemorySize, att::LDS_BYTES);
    fused_kernel<<<dim3(256), 256, att::LDS_BYTES, stream>>>((const u16*)d_in[0], (const u16*)d_in[4], (const u16*)d_in[5], (const u16*)d_in[6], (const u16*)d_in[7], (const u16*)d_in[1],
                                                             (const u16*)d_in[9], (const u16*)d_in[2], (const u16*)d_in[8], (char*)d_ws, (u16*)d_out);
}
```

```cpp
#include <hip/hip_runtime.h>
#include <hip/hip_bf16.h>
#include <stdint.h>
#include <utility>

typedef short bf16x8 __attribute__((ext_vector_type(8)));
typedef short s16x4 __attribute__((ext_vector_type(4)));
typedef float f32x16 __attribute__((ext_vector_type(16)));
typedef float f32x4 __attribute__((ext_vector_type(4)));
typedef unsigned u32x4 __attribute__((ext_vector_type(4)));
typedef unsigned short u16;

constexpr int S = 2048, DM = 576, NH = 32, HD = 128, DC = 512, NQKV = 1728, DHA = 18;
constexpr int NBIG = NH * HD;
constexpr int KS_TOT = 146;

#define SBAR() __builtin_amdgcn_sched_barrier(0)
constexpr size_t WS_Q = 0, WS_K = WS_Q + (size_t)S * NBIG * 2, WS_V = WS_K + (size_t)S * NBIG * 2, WS_AF = WS_V + (size_t)S * NBIG * 2, WS_X = WS_AF + (size_t)128 * KS_TOT * 1024,
                 WS_P = WS_X + (size_t)64 * 16 * 9216, WS_F = WS_P + (size_t)128 * (4 * 32 * 256 + 1024) * 4;
constexpr size_t WS_L = (size_t)96 << 20;
static_assert(WS_F == 96206848, "workspace map is baked into the generated asm");
constexpr int F_EPOCH = 0, F_J = 1024, F_ST = 8192, F_KV = 40960, F_R = 65536;
constexpr unsigned TAG_STEP = 0x9E3779B1u;
__device__ __forceinline__ unsigned ld_flag(const unsigned* p) { return __hip_atomic_load(p, __ATOMIC_RELAXED, __HIP_MEMORY_SCOPE_AGENT); }
__device__ __forceinline__ void st_flag(unsigned* p, unsigned v) { __hip_atomic_store(p, v, __ATOMIC_RELAXED, __HIP_MEMORY_SCOPE_AGENT); }
__device__ __forceinline__ unsigned ld_flag_async(const unsigned* p) { unsigned v; asm volatile("global_load_dword %0, %1, off sc1" : "=v"(v) : "v"(p) : "memory"); return v; }
#define WAITV(v, N) asm volatile("s_waitcnt vmcnt(" #N ")" : "+v"(v) :: "memory")
__device__ __forceinline__ void wait_flags(const unsigned* p, unsigned v, unsigned tag) {
    if (__builtin_expect(__builtin_amdgcn_ballot_w64(v != tag) != 0ull, 0)) {
        unsigned spins = 0;
        do { __builtin_amdgcn_s_sleep(1); } while (__builtin_amdgcn_ballot_w64(ld_flag(p) != tag) != 0ull && ++spins < (1u << 22));
    }
    asm volatile("" ::: "memory");
}

__device__ __forceinline__ unsigned cvtpk(float lo, float hi) {
    unsigned r; asm volatile("v_cvt_pk_bf16_f32 %0, %1, %2" : "=v"(r) : "v"(lo), "v"(hi)); return r;
}
__device__ __forceinline__ float bf2f(u16 v) { return __uint_as_float(((unsigned)v) << 16); }
__device__ __forceinline__ int crow(int r, int hi) { return (r & 3) + 8 * (r >> 2) + 4 * hi; }

typedef __attribute__((address_space(3))) s16x4* lds_s16x4_p;
__device__ __forceinline__ s16x4 trrd(unsigned addr) { return __builtin_amdgcn_ds_read_tr16_b64_v4i16((lds_s16x4_p)addr); }

__device__ __forceinline__ unsigned lane_id() { unsigned l; asm volatile("v_mbcnt_lo_u32_b32 %0, -1, 0\n\tv_mbcnt_hi_u32_b32 %0, -1, %0" : "=v"(l)); return l; }
#define RTS(slot) do {} while (0)
#define CT_L 5u
__device__ __forceinline__ void code_touch(unsigned wi, unsigned lds_scratch) {
    unsigned long long pc; asm volatile("s_getpc_b64 %0" : "=s"(pc));
    asm volatile("s_mov_b32 m0, %0\n\ts_mov_b64 exec, %3\n\tglobal_load_lds_dword %1, %2\n\ts_mov_b64 exec, -1" :: "s"(lds_scratch), "v"(((threadIdx.x & 63u) * 128u + wi) * 128u), "s"(pc & ~127ull), "n"((1u << CT_L) - 1u) : "m0", "memory");
}
#define ACLOB_LO "a0","a1","a2","a3","a4","a5","a6","a7","a8","a9","a10","a11","a12","a13","a14","a15","a16","a17","a18","a19","a20","a21","a22","a23","a24","a25","a26","a27","a28","a29","a30","a31","a32","a33","a34","a35","a36","a37","a38","a39","a40","a41","a42","a43","a44","a45","a46","a47","a48","a49","a50","a51","a52","a53","a54","a55","a56","a57","a58","a59","a60","a61","a62","a63","a64","a65","a66","a67","a68","a69","a70","a71","a72","a73","a74","a75","a76","a77","a78","a79","a80","a81","a82","a83","a84","a85","a86","a87","a88","a89","a90","a91","a92","a93","a94","a95","a96","a97","a98","a99","a100","a101","a102","a103","a104","a105","a106","a107","a108","a109","a110","a111","a112","a113","a114","a115","a116","a117","a118","a119","a120","a121","a122","a123","a124","a125","a126","a127"
#define ACLOB_HI "a128","a129","a130","a131","a132","a133","a134","a135","a136","a137","a138","a139","a140","a141","a142","a143","a144","a145","a146","a147","a148","a149","a150","a151","a152","a153","a154","a155","a156","a157","a158","a159","a160","a161","a162","a163","a164","a165","a166","a167","a168","a169","a170","a171","a172","a173","a174","a175","a176","a177","a178","a179","a180","a181","a182","a183","a184","a185","a186","a187","a188","a189","a190","a191","a192","a193","a194","a195","a196","a197","a198","a199","a200","a201","a202","a203","a204","a205","a206","a207","a208","a209","a210","a211","a212","a213","a214","a215","a216","a217","a218","a219","a220","a221","a222","a223","a224","a225","a226","a227","a228","a229","a230","a231","a232","a233","a234","a235","a236","a237","a238","a239","a240","a241","a242","a243","a244","a245","a246","a247","a248","a249","a250","a251","a252","a253","a254","a255"
#define PROJ_PRE0 \
"s_add_u32 s62, %[rc], 0x0\n" \
"s_and_b32 s62, s62, 0x3ff\n" \
"s_lshl_b32 s63, s62, 12\n" \
"s_add_u32 m0, %[mA], 0x0\n" \
"s_add_u32 s85, s62, 0x0\n" \
"buffer_load_dwordx4 v22, s[64:67], s85 offen lds\n" \
"s_add_u32 m0, %[mA], 0x400\n" \
"s_add_u32 s84, s62, 0x2000\n" \
"buffer_load_dwordx4 v23, s[64:67], s84 offen lds\n" \
"s_add_u32 m0, %[mB], 0x0\n" \
"s_add_u32 s85, s63, 0x0\n" \
"buffer_load_dwordx4 v26, s[68:71], s85 offen lds\n" \
"s_add_u32 m0, %[mB], 0x4000\n" \
"s_add_u32 s84, s63, 0x0\n" \
"buffer_load_dwordx4 v26, s[72:75], s84 offen lds\n" \
"s_add_u32 m0, %[mA], 0x800\n" \
"s_add_u32 s85, s62, 0x4000\n" \
"buffer_load_dwordx4 v22, s[64:67], s85 offen lds\n" \
"s_add_u32 m0, %[mA], 0xc00\n" \
"s_add_u32 s84, s62, 0x6000\n" \
"buffer_load_dwordx4 v23, s[64:67], s84 offen lds\n" \
"s_add_u32 m0, %[mB], 0x400\n" \
"s_add_u32 s85, s63, 0x8000\n" \
"buffer_load_dwordx4 v26, s[68:71], s85 offen lds\n" \
"s_add_u32 m0, %[mB], 0x4400\n" \
"s_add_u32 s84, s63, 0x8000\n" \
"buffer_load_dwordx4 v26, s[72:75], s84 offen lds\n"
#define PROJ_PRE1 \
"s_add_u32 s62, %[rc], 0x0\n" \
"s_and_b32 s62, s62, 0x3ff\n" \
"s_lshl_b32 s63, s62, 12\n" \
"s_add_u32 m0, %[mA], 0x1000\n" \
"s_add_u32 s85, s62, 0x8000\n" \
"buffer_load_dwordx4 v22, s[64:67], s85 offen lds\n" \
"s_add_u32 m0, %[mA], 0x1400\n" \
"s_add_u32 s84, s62, 0xa000\n" \
"buffer_load_dwordx4 v23, s[64:67], s84 offen lds\n" \
"s_add_u32 m0, %[mB], 0x800\n" \
"s_add_u32 s85, s63, 0x10000\n" \
"buffer_load_dwordx4 v27, s[68:71], s85 offen lds\n" \
"s_add_u32 m0, %[mB], 0x4800\n" \
"s_add_u32 s84, s63, 0x10000\n" \
"buffer_load_dwordx4 v27, s[72:75], s84 offen lds\n" \
"s_add_u32 m0, %[mA], 0x1800\n" \
"s_add_u32 s85, s62, 0xc000\n" \
"buffer_load_dwordx4 v22, s[64:67], s85 offen lds\n" \
"s_add_u32 m0, %[mA], 0x1c00\n" \
"s_add_u32 s84, s62, 0xe000\n" \
"buffer_load_dwordx4 v23, s[64:67], s84 offen lds\n" \
"s_add_u32 m0, %[mB], 0xc00\n" \
"s_add_u32 s85, s63, 0x18000\n" \
"buffer_load_dwordx4 v27, s[68:71], s85 offen lds\n" \
"s_add_u32 m0, %[mB], 0x4c00\n" \
"s_add_u32 s84, s63, 0x18000\n" \
"buffer_load_dwordx4 v27, s[72:75], s84 offen lds\n"
#define PROJ_PRE2 \
"s_add_u32 s62, %[rc], 0x80\n" \
"s_and_b32 s62, s62, 0x3ff\n" \
"s_lshl_b32 s63, s62, 12\n" \
"s_add_u32 m0, %[mA], 0x8000\n" \
"s_add_u32 s85, s62, 0x0\n" \
"buffer_load_dwordx4 v22, s[64:67], s85 offen lds\n" \
"s_add_u32 m0, %[mA], 0x8400\n" \
"s_add_u32 s84, s62, 0x2000\n" \
"buffer_load_dwordx4 v23, s[64:67], s84 offen lds\n"
#define PROJ_PRE3 \
"s_add_u32 s62, %[rc], 0x80\n" \
"s_and_b32 s62, s62, 0x3ff\n" \
"s_lshl_b32 s63, s62, 12\n" \
"s_add_u32 m0, %[mB], 0x8000\n" \
"s_add_u32 s85, s63, 0x0\n" \
"buffer_load_dwordx4 v26, s[68:71], s85 offen lds\n" \
"s_add_u32 m0, %[mB], 0xc000\n" \
"s_add_u32 s84, s63, 0x0\n" \
"buffer_load_dwordx4 v26, s[72:75], s84 offen lds\n" \
"s_add_u32 m0, %[mA], 0x8800\n" \
"s_add_u32 s85, s62, 0x4000\n" \
"buffer_load_dwordx4 v22, s[64:67], s85 offen lds\n" \
"s_add_u32 m0, %[mA], 0x8c00\n" \
"s_add_u32 s84, s62, 0x6000\n" \
"buffer_load_dwordx4 v23, s[64:67], s84 offen lds\n" \
"s_add_u32 m0, %[mB], 0x8400\n" \
"s_add_u32 s85, s63, 0x8000\n" \
"buffer_load_dwordx4 v26, s[68:71], s85 offen lds\n" \
"s_add_u32 m0, %[mB], 0xc400\n" \
"s_add_u32 s84, s63, 0x8000\n" \
"buffer_load_dwordx4 v26, s[72:75], s84 offen lds\n" \
"s_add_u32 m0, %[mA], 0x9000\n" \
"s_add_u32 s85, s62, 0x8000\n" \
"buffer_load_dwordx4 v22, s[64:67], s85 offen lds\n" \
"s_add_u32 m0, %[mA], 0x9400\n" \
"s_add_u32 s84, s62, 0xa000\n" \
"buffer_load_dwordx4 v23, s[64:67], s84 offen lds\n"
#define PROJ_TKE 0
#define PROJ_T0 0
#define PROJ_ROT 2*(sp+mt)
#define PROJ_TC 0
#define PROJ_ASM \
".p2align 3\n" \
"s_branch L_go_%=\n" \
"L_pl_%=:\n" \
"s_mov_b32 s63, 0\n" \
"L_ql_%=:\n" \
"s_sleep 1\n" \
"v_bfe_u32 v44, v20, 2, 5\n" \
"buffer_load_dword v44, v44, s[80:83], s39 offen offset:64 sc1\n" \
"s_waitcnt vmcnt(0)\n" \
"v_cmp_ne_u32_e32 vcc, %[tag], v44\n" \
"s_add_u32 s63, s63, 1\n" \
"s_nop 3\n" \
"s_cbranch_vccz L_rl_%=\n" \
"s_cmp_gt_u32 s63, 0x400000\n" \
"s_cbranch_scc0 L_ql_%=\n" \
"s_branch L_rl_%=\n" \
"L_pa_%=:\n" \
"s_mov_b32 s63, 0\n" \
"L_qa_%=:\n" \
"s_sleep 1\n" \
"v_bfe_u32 v44, v20, 2, 5\n" \
"buffer_load_dword v44, v44, s[80:83], s39 offen offset:0 sc1\n" \
"s_waitcnt vmcnt(0)\n" \
"v_cmp_ne_u32_e32 vcc, %[tag], v44\n" \
"s_add_u32 s63, s63, 1\n" \
"s_nop 3\n" \
"s_cbranch_vccz L_ra_%=\n" \
"s_cmp_gt_u32 s63, 0x400000\n" \
"s_cbranch_scc0 L_qa_%=\n" \
"s_branch L_ra_%=\n" \
"L_go_%=:\n" \
"v_xor_b32_e32 v33, 0x20, v32\n" \
"s_mov_b32 s80, %[wsl]\n" \
"s_and_b32 s81, %[wsh], 0xffff\n" \
"s_mov_b32 s82, 0x10000000\n" \
"s_mov_b32 s83, 0x20000\n" \
"s_mov_b32 s84, 0x5bc0000\n" \
"s_add_u32 s62, s22, 0x80\n" \
"buffer_load_dword v180, off, s[80:83], s84 sc1\n" \
"s_and_b32 s62, s62, 0x3ff\n" \
"s_lshl_b32 s63, s62, 12\n" \
"v_xor_b32_e64 v34, 0x40, v32\n" \
"v_xor_b32_e32 v35, 0x60, v32\n" \
"s_waitcnt vmcnt(11)\n" \
"s_barrier\n" \
"ds_read_b64_tr_b16 v[120:121], v32 offset:8192\n" \
"ds_read_b64_tr_b16 v[122:123], v32 offset:9216\n" \
"ds_read_b128 v[56:59], v29 offset:16384\n" \
"ds_read_b64_tr_b16 v[116:117], v32 offset:0\n" \
"ds_read_b64_tr_b16 v[118:119], v32 offset:1024\n" \
"ds_read_b128 v[52:55], v28 offset:16384\n" \
"s_add_u32 m0, %[mB], 0x8800\n" \
"s_add_u32 s85, s63, 0x10000\n" \
"s_mov_b32 s76, s12\n" \
"ds_read_b128 v[60:63], v28 offset:18432\n" \
"s_and_b32 s77, s13, 0xffff\n" \
"s_mov_b32 s78, 0x1e6000\n" \
"s_mov_b32 s79, 0x20000\n" \
"ds_read_b128 v[64:67], v29 offset:18432\n" \
"s_waitcnt lgkmcnt(5)\n" \
"v_mfma_f32_16x16x32_bf16 a[0:3], v[120:123], v[56:59], 0\n" \
"ds_read_b128 v[72:75], v29 offset:20480\n" \
"ds_read_b128 v[68:71], v28 offset:20480\n" \
"ds_read_b128 v[76:79], v28 offset:22528\n" \
"buffer_load_dwordx4 v27, s[68:71], s85 offen lds\n" \
"ds_read_b128 v[80:83], v29 offset:22528\n" \
"s_waitcnt lgkmcnt(6)\n" \
"v_mfma_f32_16x16x32_bf16 a[0:3], v[116:119], v[52:55], a[0:3]\n" \
"ds_read_b64_tr_b16 v[128:129], v33 offset:8192\n" \
"s_waitcnt lgkmcnt(6)\n" \
"v_mfma_f32_16x16x32_bf16 a[32:35], v[116:119], v[60:63], 0\n" \
"ds_read_b64_tr_b16 v[130:131], v33 offset:9216\n" \
"ds_read_b64_tr_b16 v[124:125], v33 offset:0\n" \
"ds_read_b64_tr_b16 v[126:127], v33 offset:1024\n" \
"s_waitcnt lgkmcnt(8)\n" \
"s_nop 0\n" \
"v_mfma_f32_16x16x32_bf16 a[32:35], v[120:123], v[64:67], a[32:35]\n" \
"ds_read_b64_tr_b16 v[136:137], v34 offset:8192\n" \
"ds_read_b64_tr_b16 v[138:139], v34 offset:9216\n" \
"ds_read_b64_tr_b16 v[132:133], v34 offset:0\n" \
"s_add_u32 m0, %[mB], 0xc800\n" \
"ds_read_b64_tr_b16 v[134:135], v34 offset:1024\n" \
"s_add_u32 s84, s63, 0x10000\n" \
"buffer_load_dwordx4 v27, s[72:75], s84 offen lds\n" \
"ds_read_b64_tr_b16 v[144:145], v35 offset:8192\n" \
"ds_read_b64_tr_b16 v[146:147], v35 offset:9216\n" \
"ds_read_b64_tr_b16 v[140:141], v35 offset:0\n" \
"s_lshr_b32 s95, %[b], 3\n" \
"ds_read_b64_tr_b16 v[142:143], v35 offset:1024\n" \
"s_lshl_b32 s96, s95, 2\n" \
"ds_read_b64_tr_b16 v[152:153], v32 offset:24576\n" \
"s_add_u32 s97, s96, %[w]\n" \
"ds_read_b64_tr_b16 v[154:155], v32 offset:25600\n" \
"v_lshrrev_b32_e32 v212, 4, v20\n" \
"s_mul_i32 s98, 27, s97\n" \
"v_min_u32_e32 v213, 26, v212\n" \
"v_add_u32_e64 v214, s98, v213\n" \
"v_mul_u32_u24_e32 v215, 0xaaab, v214\n" \
"s_waitcnt lgkmcnt(15)\n" \
"v_mfma_f32_16x16x32_bf16 a[64:67], v[120:123], v[72:75], 0\n" \
"ds_read_b64_tr_b16 v[148:149], v32 offset:16384\n" \
"ds_read_b64_tr_b16 v[150:151], v32 offset:17408\n" \
"v_mfma_f32_16x16x32_bf16 a[64:67], v[116:119], v[68:71], a[64:67]\n" \
"s_add_u32 m0, %[mA], 0x9800\n" \
"s_add_u32 s85, s62, 0xc000\n" \
"v_mfma_f32_16x16x32_bf16 a[96:99], v[116:119], v[76:79], 0\n" \
"ds_read_b64_tr_b16 v[160:161], v33 offset:24576\n" \
"s_waitcnt lgkmcnt(15)\n" \
"v_mfma_f32_16x16x32_bf16 a[96:99], v[120:123], v[80:83], a[96:99]\n" \
"buffer_load_dwordx4 v22, s[64:67], s85 offen lds\n" \
"v_mfma_f32_16x16x32_bf16 a[4:7], v[128:131], v[56:59], 0\n" \
"s_add_u32 m0, %[mA], 0x9c00\n" \
"s_waitcnt lgkmcnt(13)\n" \
"s_nop 0\n" \
"v_mfma_f32_16x16x32_bf16 a[4:7], v[124:127], v[52:55], a[4:7]\n" \
"s_add_u32 s84, s62, 0xe000\n" \
"ds_read_b64_tr_b16 v[162:163], v33 offset:25600\n" \
"v_mfma_f32_16x16x32_bf16 a[36:39], v[124:127], v[60:63], 0\n" \
"buffer_load_dwordx4 v23, s[64:67], s84 offen lds\n" \
"v_mfma_f32_16x16x32_bf16 a[36:39], v[128:131], v[64:67], a[36:39]\n" \
"s_add_u32 m0, %[mB], 0x8c00\n" \
"s_add_u32 s85, s63, 0x18000\n" \
"v_mfma_f32_16x16x32_bf16 a[68:71], v[128:131], v[72:75], 0\n" \
"ds_read_b64_tr_b16 v[156:157], v33 offset:16384\n" \
"ds_read_b64_tr_b16 v[158:159], v33 offset:17408\n" \
"v_mfma_f32_16x16x32_bf16 a[68:71], v[124:127], v[68:71], a[68:71]\n" \
"buffer_load_dwordx4 v27, s[68:71], s85 offen lds\n" \
"v_mfma_f32_16x16x32_bf16 a[100:103], v[124:127], v[76:79], 0\n" \
"s_add_u32 m0, %[mB], 0xcc00\n" \
"s_add_u32 s84, s63, 0x18000\n" \
"v_mfma_f32_16x16x32_bf16 a[100:103], v[128:131], v[80:83], a[100:103]\n" \
"ds_read_b64_tr_b16 v[168:169], v34 offset:24576\n" \
"s_waitcnt lgkmcnt(7)\n" \
"s_nop 0\n" \
"v_mfma_f32_16x16x32_bf16 a[8:11], v[136:139], v[56:59], 0\n" \
"buffer_load_dwordx4 v27, s[72:75], s84 offen lds\n" \
"v_mfma_f32_16x16x32_bf16 a[8:11], v[132:135], v[52:55], a[8:11]\n" \
"ds_read_b64_tr_b16 v[170:171], v34 offset:25600\n" \
"ds_read_b64_tr_b16 v[164:165], v34 offset:16384\n" \
"v_mfma_f32_16x16x32_bf16 a[40:43], v[132:135], v[60:63], 0\n" \
"ds_read_b64_tr_b16 v[166:167], v34 offset:17408\n" \
"ds_read_b64_tr_b16 v[176:177], v35 offset:24576\n" \
"v_mfma_f32_16x16x32_bf16 a[40:43], v[136:139], v[64:67], a[40:43]\n" \
"ds_read_b64_tr_b16 v[178:179], v35 offset:25600\n" \
"ds_read_b64_tr_b16 v[172:173], v35 offset:16384\n" \
"v_mfma_f32_16x16x32_bf16 a[72:75], v[136:139], v[72:75], 0\n" \
"ds_read_b64_tr_b16 v[174:175], v35 offset:17408\n" \
"ds_read_b128 v[88:91], v29 offset:0\n" \
"v_mfma_f32_16x16x32_bf16 a[72:75], v[132:135], v[68:71], a[72:75]\n" \
"ds_read_b128 v[84:87], v28 offset:0\n" \
"ds_read_b128 v[92:95], v28 offset:2048\n" \
"v_mfma_f32_16x16x32_bf16 a[104:107], v[132:135], v[76:79], 0\n" \
"ds_read_b128 v[96:99], v29 offset:2048\n" \
"v_lshrrev_b32_e32 v216, 18, v215\n" \
"v_mfma_f32_16x16x32_bf16 a[104:107], v[136:139], v[80:83], a[104:107]\n" \
"ds_read_b128 v[104:107], v29 offset:4096\n" \
"v_mul_u32_u24_e32 v218, 6, v216\n" \
"v_mfma_f32_16x16x32_bf16 a[12:15], v[144:147], v[56:59], 0\n" \
"ds_read_b128 v[100:103], v28 offset:4096\n" \
"v_sub_u32_e32 v219, v214, v218\n" \
"v_mfma_f32_16x16x32_bf16 a[12:15], v[140:143], v[52:55], a[12:15]\n" \
"ds_read_b128 v[108:111], v28 offset:6144\n" \
"v_lshrrev_b32_e32 v220, 1, v219\n" \
"v_mfma_f32_16x16x32_bf16 a[44:47], v[140:143], v[60:63], 0\n" \
"ds_read_b128 v[112:115], v29 offset:6144\n" \
"v_mul_u32_u24_e32 v217, 27, v216\n" \
"v_mfma_f32_16x16x32_bf16 a[44:47], v[144:147], v[64:67], a[44:47]\n" \
"v_mul_u32_u24_e32 v221, 9, v220\n" \
"v_add_u32_e64 v222, v217, v221\n" \
"v_mfma_f32_16x16x32_bf16 a[76:79], v[144:147], v[72:75], 0\n" \
"v_and_b32_e32 v223, 1, v219\n" \
"s_and_b32 s94, 7, %[b]\n" \
"v_mfma_f32_16x16x32_bf16 a[76:79], v[140:143], v[68:71], a[76:79]\n" \
"v_add_u32_e32 v224, v222, v223\n" \
"v_add_u32_e32 v225, s94, v224\n" \
"v_mfma_f32_16x16x32_bf16 a[108:111], v[140:143], v[76:79], 0\n" \
"v_lshlrev_b32_e32 v186, 7, v225\n" \
"s_mul_i32 s99, 0x1200, s94\n" \
"v_mfma_f32_16x16x32_bf16 a[108:111], v[144:147], v[80:83], a[108:111]\n" \
"s_mul_i32 s100, 36, s97\n" \
"s_mul_i32 s89, 6, s97\n" \
"v_mfma_f32_16x16x32_bf16 a[16:19], v[56:59], v[152:155], 0\n" \
"s_add_u32 s101, s99, s100\n" \
"s_waitcnt lgkmcnt(11)\n" \
"v_mfma_f32_16x16x32_bf16 a[16:19], v[52:55], v[148:151], a[16:19]\n" \
"s_add_u32 s95, 0xffffffdc, s89\n" \
"v_add_u32_e32 v226, s101, v212\n" \
"v_mfma_f32_16x16x32_bf16 a[48:51], v[60:63], v[148:151], 0\n" \
"v_add_u32_e32 v227, s95, v212\n" \
"v_cmp_gt_u32_e32 vcc, 36, v212\n" \
"v_mfma_f32_16x16x32_bf16 a[48:51], v[64:67], v[152:155], a[48:51]\n" \
"s_lshl_b32 s96, %[w], 6\n" \
"v_lshrrev_b32_e32 v212, 4, v20\n" \
"v_mfma_f32_16x16x32_bf16 a[80:83], v[72:75], v[152:155], 0\n" \
"v_add_u32_e64 v213, s96, v212\n" \
"v_mul_u32_u24_e32 v214, 0x2aab, v213\n" \
"v_mfma_f32_16x16x32_bf16 a[80:83], v[68:71], v[148:151], a[80:83]\n" \
"s_mul_i32 s88, 0x288, s94\n" \
"v_lshrrev_b32_e32 v215, 16, v214\n" \
"v_mfma_f32_16x16x32_bf16 a[112:115], v[76:79], v[148:151], 0\n" \
"v_min_u32_e32 v228, 0x287, v227\n" \
"s_lshr_b32 s94, %[b], 5\n" \
"v_mfma_f32_16x16x32_bf16 a[112:115], v[80:83], v[152:155], a[112:115]\n" \
"v_mul_u32_u24_e32 v218, 6, v215\n" \
"v_add_u32_e32 v229, s88, v228\n" \
"v_mfma_f32_16x16x32_bf16 a[20:23], v[56:59], v[160:163], 0\n" \
"s_mul_i32 s95, 0x48, s94\n" \
"v_sub_u32_e32 v219, v213, v218\n" \
"v_mfma_f32_16x16x32_bf16 a[20:23], v[52:55], v[156:159], a[20:23]\n" \
"s_lshl_b32 s41, s97, 7\n" \
"v_add_u32_e64 v216, s95, v215\n" \
"v_mfma_f32_16x16x32_bf16 a[52:55], v[60:63], v[156:159], 0\n" \
"v_lshrrev_b32_e32 v220, 1, v219\n" \
"s_and_b32 s100, 7, %[b]\n" \
"v_mfma_f32_16x16x32_bf16 a[52:55], v[64:67], v[160:163], a[52:55]\n" \
"s_lshr_b32 s88, %[b], 3\n" \
"v_mul_u32_u24_e32 v217, 0xd80, v216\n" \
"v_mfma_f32_16x16x32_bf16 a[84:87], v[72:75], v[160:163], 0\n" \
"v_mul_u32_u24_e32 v221, 0x480, v220\n" \
"v_and_b32_e32 v223, 1, v219\n" \
"v_mfma_f32_16x16x32_bf16 a[84:87], v[68:71], v[156:159], a[84:87]\n" \
"s_add_u32 s97, 0x100, s96\n" \
"s_lshl_b32 s101, s100, 2\n" \
"v_mfma_f32_16x16x32_bf16 a[116:119], v[76:79], v[156:159], 0\n" \
"s_and_b32 s89, 3, s88\n" \
"v_add_u32_e64 v222, v217, v221\n" \
"v_mfma_f32_16x16x32_bf16 a[116:119], v[80:83], v[160:163], a[116:119]\n" \
"v_lshlrev_b32_e32 v224, 4, v223\n" \
"s_add_u32 s96, s101, s89\n" \
"v_mfma_f32_16x16x32_bf16 a[24:27], v[56:59], v[168:171], 0\n" \
"v_add_u32_e32 v192, v222, v224\n" \
"s_mul_i32 s56, 36, s96\n" \
"v_mfma_f32_16x16x32_bf16 a[24:27], v[52:55], v[164:167], a[24:27]\n" \
"v_cndmask_b32_e32 v230, v229, v226, vcc\n" \
"v_add_u32_e32 v225, s97, v212\n" \
"v_mfma_f32_16x16x32_bf16 a[56:59], v[60:63], v[164:167], 0\n" \
"v_mul_u32_u24_e32 v226, 0x2aab, v225\n" \
"v_lshlrev_b32_e32 v16, 7, v230\n" \
"v_mfma_f32_16x16x32_bf16 a[56:59], v[64:67], v[168:171], a[56:59]\n" \
"v_lshrrev_b32_e32 v227, 16, v226\n" \
"v_mul_u32_u24_e32 v230, 6, v227\n" \
"v_mfma_f32_16x16x32_bf16 a[88:91], v[72:75], v[168:171], 0\n" \
"v_sub_u32_e32 v231, v225, v230\n" \
"v_add_u32_e64 v228, s95, v227\n" \
"v_mfma_f32_16x16x32_bf16 a[88:91], v[68:71], v[164:167], a[88:91]\n" \
"v_lshrrev_b32_e32 v232, 1, v231\n" \
"v_mul_u32_u24_e32 v229, 0xd80, v228\n" \
"v_mfma_f32_16x16x32_bf16 a[120:123], v[76:79], v[164:167], 0\n" \
"v_mul_u32_u24_e32 v233, 0x480, v232\n" \
"v_and_b32_e32 v235, 1, v231\n" \
"v_mfma_f32_16x16x32_bf16 a[120:123], v[80:83], v[168:171], a[120:123]\n" \
"v_add_u32_e32 v234, v229, v233\n" \
"s_waitcnt lgkmcnt(0)\n" \
"v_mfma_f32_16x16x32_bf16 a[28:31], v[56:59], v[176:179], 0\n" \
"v_lshlrev_b32_e32 v236, 4, v235\n" \
"v_add_u32_e32 v237, v234, v236\n" \
"v_mfma_f32_16x16x32_bf16 a[28:31], v[52:55], v[172:175], a[28:31]\n" \
"v_cmp_gt_u32_e32 vcc, 0x1b0, v225\n" \
"s_mul_i32 s98, 18, %[w]\n" \
"v_mfma_f32_16x16x32_bf16 a[60:63], v[60:63], v[172:175], 0\n" \
"v_cndmask_b32_e32 v193, 0x40000000, v237, vcc\n" \
"s_add_u32 s99, s95, s98\n" \
"v_mfma_f32_16x16x32_bf16 a[60:63], v[64:67], v[176:179], a[60:63]\n" \
"v_add_u32_e32 v238, s99, v212\n" \
"v_mul_u32_u24_e32 v239, 0xd80, v238\n" \
"v_mfma_f32_16x16x32_bf16 a[92:95], v[72:75], v[176:179], 0\n" \
"v_cmp_gt_u32_e32 vcc, 18, v212\n" \
"v_lshlrev_b32_e32 v240, 1, v238\n" \
"v_mfma_f32_16x16x32_bf16 a[92:95], v[68:71], v[172:175], a[92:95]\n" \
"v_cndmask_b32_e32 v194, 0x40000000, v239, vcc\n" \
"v_cmp_gt_u32_e32 vcc, 18, v212\n" \
"v_mfma_f32_16x16x32_bf16 a[124:127], v[76:79], v[172:175], 0\n" \
"v_lshrrev_b32_e32 v230, 1, v238\n" \
"v_cndmask_b32_e32 v195, 0x40000000, v240, vcc\n" \
"v_mfma_f32_16x16x32_bf16 a[124:127], v[80:83], v[176:179], a[124:127]\n" \
"v_and_b32_e32 v227, 0xff8, v238\n" \
"v_and_b32_e64 v232, 2, v230\n" \
"v_mfma_f32_16x16x32_bf16 a[144:147], v[88:91], v[152:155], 0\n" \
"v_add_u32_e32 v229, v227, v232\n" \
"v_and_b32_e32 v233, 1, v238\n" \
"v_mfma_f32_16x16x32_bf16 a[144:147], v[84:87], v[148:151], a[144:147]\n" \
"v_add_u32_e32 v235, v229, v233\n" \
"v_lshrrev_b32_e32 v242, 1, v216\n" \
"v_mfma_f32_16x16x32_bf16 a[176:179], v[92:95], v[148:151], 0\n" \
"v_lshrrev_b32_e32 v244, 2, v216\n" \
"v_lshrrev_b32_e32 v214, 1, v228\n" \
"v_mfma_f32_16x16x32_bf16 a[176:179], v[96:99], v[152:155], a[176:179]\n" \
"v_lshrrev_b32_e32 v213, 2, v228\n" \
"v_lshrrev_b32_e32 v236, 1, v235\n" \
"v_mfma_f32_16x16x32_bf16 a[208:211], v[104:107], v[152:155], 0\n" \
"v_lshrrev_b32_e32 v239, 2, v235\n" \
"s_lshl_b32 s98, %[w], 10\n" \
"v_mfma_f32_16x16x32_bf16 a[208:211], v[100:103], v[148:151], a[208:211]\n" \
"s_mul_i32 s101, 0x12000, s96\n" \
"v_and_b32_e64 v243, 1, v242\n" \
"v_mfma_f32_16x16x32_bf16 a[240:243], v[108:111], v[148:151], 0\n" \
"v_and_b32_e32 v245, 2, v244\n" \
"v_and_b32_e32 v215, 1, v214\n" \
"v_mfma_f32_16x16x32_bf16 a[240:243], v[112:115], v[152:155], a[240:243]\n" \
"v_and_b32_e32 v218, 2, v213\n" \
"v_and_b32_e32 v237, 1, v236\n" \
"v_mfma_f32_16x16x32_bf16 a[148:151], v[88:91], v[160:163], 0\n" \
"v_and_b32_e64 v240, 2, v239\n" \
"s_add_u32 s99, 0x22000, s98\n" \
"v_mfma_f32_16x16x32_bf16 a[148:151], v[84:87], v[156:159], a[148:151]\n" \
"s_add_u32 s89, 0x6000000, s101\n" \
"s_mul_i32 s97, 0x2400, s94\n" \
"v_mfma_f32_16x16x32_bf16 a[180:183], v[92:95], v[156:159], 0\n" \
"v_lshlrev_b32_e32 v241, 7, v216\n" \
"v_or_b32_e32 v246, v243, v245\n" \
"v_mfma_f32_16x16x32_bf16 a[180:183], v[96:99], v[160:163], a[180:183]\n" \
"v_or_b32_e32 v220, v215, v218\n" \
"v_or_b32_e32 v242, v237, v240\n" \
"v_mfma_f32_16x16x32_bf16 a[212:215], v[104:107], v[160:163], 0\n" \
"v_add_u32_e32 v199, s99, v20\n" \
"s_add_u32 s37, s89, s98\n" \
"v_mfma_f32_16x16x32_bf16 a[212:215], v[100:103], v[156:159], a[212:215]\n" \
"v_lshlrev_b32_e32 v247, 1, v246\n" \
"v_lshlrev_b32_e32 v217, 1, v220\n" \
"v_mfma_f32_16x16x32_bf16 a[244:247], v[108:111], v[156:159], 0\n" \
"v_xor_b32_e32 v216, 3, v242\n" \
"s_add_u32 s57, s37, s97\n" \
"v_mfma_f32_16x16x32_bf16 a[244:247], v[112:115], v[160:163], a[244:247]\n" \
"s_lshl_b32 s99, s88, 2\n" \
"v_xor_b32_e32 v248, v219, v247\n" \
"v_mfma_f32_16x16x32_bf16 a[152:155], v[88:91], v[168:171], 0\n" \
"v_xor_b32_e32 v221, v231, v217\n" \
"v_lshlrev_b32_e32 v234, 7, v235\n" \
"v_mfma_f32_16x16x32_bf16 a[152:155], v[84:87], v[164:167], a[152:155]\n" \
"v_lshlrev_b32_e32 v244, 5, v216\n" \
"v_and_b32_e32 v245, 2, v238\n" \
"v_mfma_f32_16x16x32_bf16 a[184:187], v[92:95], v[164:167], 0\n" \
"s_add_u32 s94, 0x2000, s57\n" \
"s_add_u32 s96, s99, %[w]\n" \
"v_mfma_f32_16x16x32_bf16 a[184:187], v[96:99], v[168:171], a[184:187]\n" \
"s_lshl_b32 s89, %[b], 2\n" \
"s_sub_u32 s95, 0x22000, s97\n" \
"v_mfma_f32_16x16x32_bf16 a[216:219], v[104:107], v[168:171], 0\n" \
"v_lshlrev_b32_e32 v249, 4, v248\n" \
"v_lshlrev_b32_e32 v251, 7, v228\n" \
"v_mfma_f32_16x16x32_bf16 a[216:219], v[100:103], v[164:167], a[216:219]\n" \
"v_lshlrev_b32_e32 v223, 4, v221\n" \
"v_add_u32_e32 v243, v234, v244\n" \
"v_mfma_f32_16x16x32_bf16 a[248:251], v[108:111], v[164:167], 0\n" \
"v_lshlrev_b32_e32 v246, 1, v245\n" \
"s_mul_i32 s101, 36, s96\n" \
"v_mfma_f32_16x16x32_bf16 a[248:251], v[112:115], v[168:171], a[248:251]\n" \
"s_cmp_eq_u32 %[w], 0\n" \
"s_cselect_b32 s58, s94, 0x40000000\n" \
"v_mfma_f32_16x16x32_bf16 a[156:159], v[88:91], v[176:179], 0\n" \
"s_add_u32 s98, s89, %[w]\n" \
"v_add_u32_e32 v250, v241, v249\n" \
"v_mfma_f32_16x16x32_bf16 a[156:159], v[84:87], v[172:175], a[156:159]\n" \
"v_add_u32_e32 v222, v251, v223\n" \
"v_add_u32_e32 v219, v243, v246\n" \
"v_mfma_f32_16x16x32_bf16 a[188:191], v[92:95], v[172:175], 0\n" \
"v_add_u32_e32 v248, s101, v212\n" \
"s_mul_i32 s97, 18, s98\n" \
"v_mfma_f32_16x16x32_bf16 a[188:191], v[96:99], v[176:179], a[188:191]\n" \
"v_add_u32_e32 v196, s95, v250\n" \
"v_add_u32_e32 v224, s95, v222\n" \
"v_mfma_f32_16x16x32_bf16 a[220:223], v[104:107], v[176:179], 0\n" \
"v_add_u32_e32 v247, s95, v219\n" \
"v_lshrrev_b32_e32 v241, 3, v248\n" \
"v_mfma_f32_16x16x32_bf16 a[220:223], v[100:103], v[172:175], a[220:223]\n" \
"s_add_u32 s94, 0xffffffdc, s97\n" \
"v_min_u32_e32 v213, 53, v212\n" \
"v_mfma_f32_16x16x32_bf16 a[252:255], v[108:111], v[172:175], 0\n" \
"s_lshl_b32 s95, s100, 3\n" \
"v_lshlrev_b32_e32 v249, 6, v241\n" \
"v_mfma_f32_16x16x32_bf16 a[252:255], v[112:115], v[176:179], a[252:255]\n" \
"v_add_u32_e32 v215, s94, v213\n" \
"s_and_b32 s97, 7, %[b]\n" \
"v_mfma_f32_16x16x32_bf16 a[128:131], v[120:123], v[88:91], 0\n" \
"s_lshr_b32 s99, %[b], 3\n" \
"v_add_u32_e64 v250, s95, v249\n" \
"v_mfma_f32_16x16x32_bf16 a[128:131], v[116:119], v[84:87], a[128:131]\n" \
"s_lshr_b32 s94, %[b], 5\n" \
"s_lshl_b32 s98, s97, 2\n" \
"v_mfma_f32_16x16x32_bf16 a[160:163], v[116:119], v[92:95], 0\n" \
"s_and_b32 s100, 3, s99\n" \
"s_mul_i32 s95, 0xa00, s94\n" \
"v_mfma_f32_16x16x32_bf16 a[160:163], v[120:123], v[96:99], a[160:163]\n" \
"s_add_u32 s101, s98, s100\n" \
"s_add_u32 s96, 0x5bc1000, s95\n" \
"v_mfma_f32_16x16x32_bf16 a[192:195], v[120:123], v[104:107], 0\n" \
"s_lshl_b32 s88, s101, 2\n" \
"s_add_u32 s89, s96, s88\n" \
"v_mfma_f32_16x16x32_bf16 a[192:195], v[116:119], v[100:103], a[192:195]\n" \
"s_cmp_eq_u32 %[w], 0\n" \
"s_cselect_b32 s95, s89, 0x40000000\n" \
"v_mfma_f32_16x16x32_bf16 a[224:227], v[116:119], v[108:111], 0\n" \
"s_lshl_b32 s99, s94, 3\n" \
"s_add_u32 s88, s99, s97\n" \
"v_mfma_f32_16x16x32_bf16 a[224:227], v[120:123], v[112:115], a[224:227]\n" \
"v_writelane_b32 v11, s95, 0\n" \
"s_lshl_b32 s89, s88, 10\n" \
"v_mfma_f32_16x16x32_bf16 a[132:135], v[128:131], v[88:91], 0\n" \
"s_add_u32 s62, s22, 0x100\n" \
"s_and_b32 s62, s62, 0x3ff\n" \
"v_mfma_f32_16x16x32_bf16 a[132:135], v[124:127], v[84:87], a[132:135]\n" \
"s_lshl_b32 s63, s62, 12\n" \
"s_add_u32 s95, 0x5c00000, s89\n" \
"v_mfma_f32_16x16x32_bf16 a[164:167], v[124:127], v[92:95], 0\n" \
"s_lshl_b32 s99, %[w], 8\n" \
"s_add_u32 s88, s95, s99\n" \
"v_mfma_f32_16x16x32_bf16 a[164:167], v[128:131], v[96:99], a[164:167]\n" \
"v_add_u32_e32 v226, 0x24400, v20\n" \
"v_cmp_gt_u32_e32 vcc, 0x1b0, v225\n" \
"v_mfma_f32_16x16x32_bf16 a[196:199], v[128:131], v[104:107], 0\n" \
"s_or_b32 s89, 0x200, s88\n" \
"s_lshl_b32 s95, s100, 2\n" \
"v_mfma_f32_16x16x32_bf16 a[196:199], v[124:127], v[100:103], a[196:199]\n" \
"v_cndmask_b32_e32 v197, v226, v224, vcc\n" \
"v_writelane_b32 v11, s89, 1\n" \
"v_mfma_f32_16x16x32_bf16 a[228:231], v[124:127], v[108:111], 0\n" \
"s_add_u32 s99, s88, s95\n" \
"v_cmp_gt_u32_e32 vcc, 18, v212\n" \
"v_mfma_f32_16x16x32_bf16 a[228:231], v[128:131], v[112:115], a[228:231]\n" \
"v_writelane_b32 v11, s99, 2\n" \
"s_add_u32 s89, 0x80, s88\n" \
"s_waitcnt vmcnt(0)\n" \
"s_sleep 16\n" \
"s_barrier\n" \
"v_mfma_f32_16x16x32_bf16 a[136:139], v[136:139], v[88:91], 0\n" \
"ds_read_b64_tr_b16 v[152:153], v32 offset:57344\n" \
"ds_read_b64_tr_b16 v[154:155], v32 offset:58368\n" \
"v_mfma_f32_16x16x32_bf16 a[136:139], v[132:135], v[84:87], a[136:139]\n" \
"ds_read_b128 v[56:59], v29 offset:49152\n" \
"s_add_u32 m0, %[mA], 0x0\n" \
"v_mfma_f32_16x16x32_bf16 a[168:171], v[132:135], v[92:95], 0\n" \
"s_add_u32 s85, s62, 0x0\n" \
"s_nop 0\n" \
"ds_read_b64_tr_b16 v[148:149], v32 offset:49152\n" \
"v_mfma_f32_16x16x32_bf16 a[168:171], v[136:139], v[96:99], a[168:171]\n" \
"buffer_load_dwordx4 v22, s[64:67], s85 offen lds\n" \
"ds_read_b64_tr_b16 v[150:151], v32 offset:50176\n" \
"ds_read_b128 v[52:55], v28 offset:49152\n" \
"v_mfma_f32_16x16x32_bf16 a[200:203], v[136:139], v[104:107], 0\n" \
"ds_read_b128 v[60:63], v28 offset:51200\n" \
"ds_read_b128 v[64:67], v29 offset:51200\n" \
"v_mfma_f32_16x16x32_bf16 a[200:203], v[132:135], v[100:103], a[200:203]\n" \
"L_pf0:\n" \
"s_cbranch_execz L_pf4\n" \
"s_nop 0\n" \
"s_add_u32 m0, %[mA], 0x400\n" \
"s_add_u32 s84, s62, 0x2000\n" \
"v_mfma_f32_16x16x32_bf16 a[232:235], v[132:135], v[108:111], 0\n" \
"buffer_load_dwordx4 v23, s[64:67], s84 offen lds\n" \
"ds_read_b128 v[72:75], v29 offset:53248\n" \
"v_mfma_f32_16x16x32_bf16 a[232:235], v[136:139], v[112:115], a[232:235]\n" \
"ds_read_b128 v[68:71], v28 offset:53248\n" \
"ds_read_b128 v[76:79], v28 offset:55296\n" \
"v_mfma_f32_16x16x32_bf16 a[140:143], v[144:147], v[88:91], 0\n" \
"ds_read_b128 v[80:83], v29 offset:55296\n" \
"s_add_u32 m0, %[mB], 0x0\n" \
"v_mfma_f32_16x16x32_bf16 a[140:143], v[140:143], v[84:87], a[140:143]\n" \
"s_add_u32 s85, s63, 0x0\n" \
"ds_read_b64_tr_b16 v[160:161], v33 offset:57344\n" \
"v_mfma_f32_16x16x32_bf16 a[172:175], v[140:143], v[92:95], 0\n" \
"buffer_load_dwordx4 v26, s[68:71], s85 offen lds\n" \
"ds_read_b64_tr_b16 v[162:163], v33 offset:58368\n" \
"v_mfma_f32_16x16x32_bf16 a[172:175], v[144:147], v[96:99], a[172:175]\n" \
"ds_read_b64_tr_b16 v[156:157], v33 offset:49152\n" \
"ds_read_b64_tr_b16 v[158:159], v33 offset:50176\n" \
"v_mfma_f32_16x16x32_bf16 a[204:207], v[144:147], v[104:107], 0\n" \
"s_add_u32 m0, %[mB], 0x4000\n" \
"s_add_u32 s84, s63, 0x0\n" \
"v_mfma_f32_16x16x32_bf16 a[204:207], v[140:143], v[100:103], a[204:207]\n" \
"buffer_load_dwordx4 v26, s[72:75], s84 offen lds\n" \
"v_mfma_f32_16x16x32_bf16 a[236:239], v[140:143], v[108:111], 0\n" \
"s_add_u32 m0, %[mA], 0x800\n" \
"s_add_u32 s85, s62, 0x4000\n" \
"v_mfma_f32_16x16x32_bf16 a[236:239], v[144:147], v[112:115], a[236:239]\n" \
"ds_read_b64_tr_b16 v[168:169], v34 offset:57344\n" \
"s_waitcnt lgkmcnt(13)\n" \
"v_mfma_f32_16x16x32_bf16 a[16:19], v[56:59], v[152:155], a[16:19]\n" \
"ds_read_b64_tr_b16 v[170:171], v34 offset:58368\n" \
"s_waitcnt lgkmcnt(12)\n" \
"v_mfma_f32_16x16x32_bf16 a[16:19], v[52:55], v[148:151], a[16:19]\n" \
"ds_read_b64_tr_b16 v[164:165], v34 offset:49152\n" \
"s_waitcnt lgkmcnt(12)\n" \
"v_mfma_f32_16x16x32_bf16 a[48:51], v[60:63], v[148:151], a[48:51]\n" \
"ds_read_b64_tr_b16 v[166:167], v34 offset:50176\n" \
"buffer_load_dwordx4 v22, s[64:67], s85 offen lds\n" \
"s_waitcnt lgkmcnt(12)\n" \
"v_mfma_f32_16x16x32_bf16 a[48:51], v[64:67], v[152:155], a[48:51]\n" \
"s_add_u32 m0, %[mA], 0xc00\n" \
"s_waitcnt lgkmcnt(11)\n" \
"v_mfma_f32_16x16x32_bf16 a[80:83], v[72:75], v[152:155], a[80:83]\n" \
"s_add_u32 s84, s62, 0x6000\n" \
"s_waitcnt lgkmcnt(10)\n" \
"v_mfma_f32_16x16x32_bf16 a[80:83], v[68:71], v[148:151], a[80:83]\n" \
"s_add_u32 s85, s63, 0x8000\n" \
"buffer_load_dwordx4 v23, s[64:67], s84 offen lds\n" \
"s_waitcnt lgkmcnt(9)\n" \
"v_mfma_f32_16x16x32_bf16 a[112:115], v[76:79], v[148:151], a[112:115]\n" \
"s_add_u32 m0, %[mB], 0x400\n" \
"s_waitcnt lgkmcnt(8)\n" \
"v_mfma_f32_16x16x32_bf16 a[112:115], v[80:83], v[152:155], a[112:115]\n" \
"ds_read_b64_tr_b16 v[176:177], v35 offset:57344\n" \
"s_waitcnt lgkmcnt(7)\n" \
"v_mfma_f32_16x16x32_bf16 a[20:23], v[56:59], v[160:163], a[20:23]\n" \
"ds_read_b64_tr_b16 v[178:179], v35 offset:58368\n" \
"s_waitcnt lgkmcnt(6)\n" \
"s_nop 0\n" \
"v_mfma_f32_16x16x32_bf16 a[20:23], v[52:55], v[156:159], a[20:23]\n" \
"buffer_load_dwordx4 v26, s[68:71], s85 offen lds\n" \
"v_mfma_f32_16x16x32_bf16 a[52:55], v[60:63], v[156:159], a[52:55]\n" \
"ds_read_b64_tr_b16 v[172:173], v35 offset:49152\n" \
"ds_read_b64_tr_b16 v[174:175], v35 offset:50176\n" \
"v_mfma_f32_16x16x32_bf16 a[52:55], v[64:67], v[160:163], a[52:55]\n" \
"s_add_u32 m0, %[mB], 0x4400\n" \
"s_add_u32 s84, s63, 0x8000\n" \
"v_mfma_f32_16x16x32_bf16 a[84:87], v[72:75], v[160:163], a[84:87]\n" \
"buffer_load_dwordx4 v26, s[72:75], s84 offen lds\n" \
"v_mfma_f32_16x16x32_bf16 a[84:87], v[68:71], v[156:159], a[84:87]\n" \
"s_add_u32 m0, %[mA], 0x1000\n" \
"s_add_u32 s85, s62, 0x8000\n" \
"v_mfma_f32_16x16x32_bf16 a[116:119], v[76:79], v[156:159], a[116:119]\n" \
"ds_read_b64_tr_b16 v[120:121], v32 offset:40960\n" \
"ds_read_b64_tr_b16 v[122:123], v32 offset:41984\n" \
"v_mfma_f32_16x16x32_bf16 a[116:119], v[80:83], v[160:163], a[116:119]\n" \
"ds_read_b64_tr_b16 v[116:117], v32 offset:32768\n" \
"s_waitcnt lgkmcnt(7)\n" \
"s_nop 0\n" \
"v_mfma_f32_16x16x32_bf16 a[24:27], v[56:59], v[168:171], a[24:27]\n" \
"buffer_load_dwordx4 v22, s[64:67], s85 offen lds\n" \
"v_mfma_f32_16x16x32_bf16 a[24:27], v[52:55], v[164:167], a[24:27]\n" \
"ds_read_b64_tr_b16 v[118:119], v32 offset:33792\n" \
"s_add_u32 m0, %[mA], 0x1400\n" \
"v_mfma_f32_16x16x32_bf16 a[56:59], v[60:63], v[164:167], a[56:59]\n" \
"s_add_u32 s84, s62, 0xa000\n" \
"ds_read_b64_tr_b16 v[128:129], v33 offset:40960\n" \
"v_mfma_f32_16x16x32_bf16 a[56:59], v[64:67], v[168:171], a[56:59]\n" \
"buffer_load_dwordx4 v23, s[64:67], s84 offen lds\n" \
"v_mfma_f32_16x16x32_bf16 a[88:91], v[72:75], v[168:171], a[88:91]\n" \
"s_add_u32 m0, %[mB], 0x800\n" \
"s_add_u32 s85, s63, 0x10000\n" \
"v_mfma_f32_16x16x32_bf16 a[88:91], v[68:71], v[164:167], a[88:91]\n" \
"ds_read_b64_tr_b16 v[130:131], v33 offset:41984\n" \
"ds_read_b64_tr_b16 v[124:125], v33 offset:32768\n" \
"v_mfma_f32_16x16x32_bf16 a[120:123], v[76:79], v[164:167], a[120:123]\n" \
"buffer_load_dwordx4 v27, s[68:71], s85 offen lds\n" \
"v_mfma_f32_16x16x32_bf16 a[120:123], v[80:83], v[168:171], a[120:123]\n" \
"ds_read_b64_tr_b16 v[126:127], v33 offset:33792\n" \
"s_waitcnt lgkmcnt(10)\n" \
"v_mfma_f32_16x16x32_bf16 a[28:31], v[56:59], v[176:179], a[28:31]\n" \
"s_add_u32 m0, %[mB], 0x4800\n" \
"s_waitcnt lgkmcnt(8)\n" \
"v_mfma_f32_16x16x32_bf16 a[28:31], v[52:55], v[172:175], a[28:31]\n" \
"s_add_u32 s84, s63, 0x10000\n" \
"ds_read_b64_tr_b16 v[136:137], v34 offset:40960\n" \
"v_mfma_f32_16x16x32_bf16 a[60:63], v[60:63], v[172:175], a[60:63]\n" \
"buffer_load_dwordx4 v27, s[72:75], s84 offen lds\n" \
"v_mfma_f32_16x16x32_bf16 a[60:63], v[64:67], v[176:179], a[60:63]\n" \
"ds_read_b64_tr_b16 v[138:139], v34 offset:41984\n" \
"ds_read_b64_tr_b16 v[132:133], v34 offset:32768\n" \
"v_mfma_f32_16x16x32_bf16 a[92:95], v[72:75], v[176:179], a[92:95]\n" \
"s_add_u32 m0, %[mA], 0x1800\n" \
"s_add_u32 s85, s62, 0xc000\n" \
"v_mfma_f32_16x16x32_bf16 a[92:95], v[68:71], v[172:175], a[92:95]\n" \
"buffer_load_dwordx4 v22, s[64:67], s85 offen lds\n" \
"v_mfma_f32_16x16x32_bf16 a[124:127], v[76:79], v[172:175], a[124:127]\n" \
"ds_read_b64_tr_b16 v[134:135], v34 offset:33792\n" \
"ds_read_b64_tr_b16 v[144:145], v35 offset:40960\n" \
"v_mfma_f32_16x16x32_bf16 a[124:127], v[80:83], v[176:179], a[124:127]\n" \
"s_add_u32 m0, %[mA], 0x1c00\n" \
"s_waitcnt lgkmcnt(9)\n" \
"s_nop 0\n" \
"v_mfma_f32_16x16x32_bf16 a[0:3], v[120:123], v[56:59], a[0:3]\n" \
"s_add_u32 s84, s62, 0xe000\n" \
"ds_read_b64_tr_b16 v[146:147], v35 offset:41984\n" \
"v_mfma_f32_16x16x32_bf16 a[0:3], v[116:119], v[52:55], a[0:3]\n" \
"buffer_load_dwordx4 v23, s[64:67], s84 offen lds\n" \
"v_mfma_f32_16x16x32_bf16 a[32:35], v[116:119], v[60:63], a[32:35]\n" \
"ds_read_b64_tr_b16 v[140:141], v35 offset:32768\n" \
"ds_read_b64_tr_b16 v[142:143], v35 offset:33792\n" \
"v_mfma_f32_16x16x32_bf16 a[32:35], v[120:123], v[64:67], a[32:35]\n" \
"s_add_u32 m0, %[mB], 0xc00\n" \
"s_add_u32 s85, s63, 0x18000\n" \
"v_mfma_f32_16x16x32_bf16 a[64:67], v[120:123], v[72:75], a[64:67]\n" \
"buffer_load_dwordx4 v27, s[68:71], s85 offen lds\n" \
"v_mfma_f32_16x16x32_bf16 a[64:67], v[116:119], v[68:71], a[64:67]\n" \
"ds_read_b128 v[88:91], v29 offset:32768\n" \
"ds_read_b128 v[84:87], v28 offset:32768\n" \
"v_mfma_f32_16x16x32_bf16 a[96:99], v[116:119], v[76:79], a[96:99]\n" \
"ds_read_b128 v[92:95], v28 offset:34816\n" \
"ds_read_b128 v[96:99], v29 offset:34816\n" \
"v_mfma_f32_16x16x32_bf16 a[96:99], v[120:123], v[80:83], a[96:99]\n" \
"s_add_u32 m0, %[mB], 0x4c00\n" \
"s_waitcnt lgkmcnt(11)\n" \
"v_mfma_f32_16x16x32_bf16 a[4:7], v[128:131], v[56:59], a[4:7]\n" \
"s_add_u32 s84, s63, 0x18000\n" \
"ds_read_b128 v[104:107], v29 offset:36864\n" \
"v_mfma_f32_16x16x32_bf16 a[4:7], v[124:127], v[52:55], a[4:7]\n" \
"buffer_load_dwordx4 v27, s[72:75], s84 offen lds\n" \
"v_mfma_f32_16x16x32_bf16 a[36:39], v[124:127], v[60:63], a[36:39]\n" \
"ds_read_b128 v[100:103], v28 offset:36864\n" \
"ds_read_b128 v[108:111], v28 offset:38912\n" \
"v_mfma_f32_16x16x32_bf16 a[36:39], v[128:131], v[64:67], a[36:39]\n" \
"s_mov_b32 m0, %[mS]\n" \
"s_mov_b32 s86, s12\n" \
"v_mfma_f32_16x16x32_bf16 a[68:71], v[128:131], v[72:75], a[68:71]\n" \
"s_mov_b32 s87, s13\n" \
"ds_read_b128 v[112:115], v29 offset:38912\n" \
"v_mfma_f32_16x16x32_bf16 a[68:71], v[124:127], v[68:71], a[68:71]\n" \
"v_cndmask_b32_e32 v198, v226, v247, vcc\n" \
"v_and_b32_e32 v214, 7, v248\n" \
"v_mfma_f32_16x16x32_bf16 a[100:103], v[124:127], v[76:79], a[100:103]\n" \
"v_writelane_b32 v11, s89, 3\n" \
"s_lshl_b32 s88, s101, 8\n" \
"v_mfma_f32_16x16x32_bf16 a[100:103], v[128:131], v[80:83], a[100:103]\n" \
"v_add_u32_e32 v228, v250, v214\n" \
"s_waitcnt lgkmcnt(10)\n" \
"v_mfma_f32_16x16x32_bf16 a[8:11], v[136:139], v[56:59], a[8:11]\n" \
"v_cmp_gt_u32_e32 vcc, 36, v212\n" \
"s_add_u32 s95, 0x80, s99\n" \
"v_mfma_f32_16x16x32_bf16 a[8:11], v[132:135], v[52:55], a[8:11]\n" \
"s_add_u32 s39, 0x5be8000, s88\n" \
"s_lshl_b32 s89, s94, 2\n" \
"s_mov_b32 exec_lo, 0x7ffffff\n" \
"s_mov_b32 exec_hi, 0x0\n" \
"global_load_lds_dword v186, s[86:87]\n" \
"s_mov_b64 exec, -1\n" \
"v_mfma_f32_16x16x32_bf16 a[40:43], v[132:135], v[60:63], a[40:43]\n" \
"v_cndmask_b32_e32 v218, v215, v228, vcc\n" \
"s_add_u32 s99, s39, s89\n" \
"v_mfma_f32_16x16x32_bf16 a[40:43], v[136:139], v[64:67], a[40:43]\n" \
"v_lshrrev_b32_e32 v212, 4, v20\n" \
"v_writelane_b32 v11, s95, 4\n" \
"v_mfma_f32_16x16x32_bf16 a[72:75], v[136:139], v[72:75], a[72:75]\n" \
"s_cmp_eq_u32 %[w], 3\n" \
"s_cselect_b32 s38, s99, 0x40000000\n" \
"v_mfma_f32_16x16x32_bf16 a[72:75], v[132:135], v[68:71], a[72:75]\n" \
"v_lshlrev_b32_e32 v213, 2, v212\n" \
"v_and_b32_e32 v215, 7, v212\n" \
"v_mfma_f32_16x16x32_bf16 a[104:107], v[132:135], v[76:79], a[104:107]\n" \
"s_add_u32 s101, s89, s100\n" \
"v_add_u32_e32 v214, s96, v213\n" \
"v_mfma_f32_16x16x32_bf16 a[104:107], v[136:139], v[80:83], a[104:107]\n" \
"s_add_u32 s95, 0x5be8020, s88\n" \
"v_lshlrev_b32_e32 v216, 2, v215\n" \
"v_mfma_f32_16x16x32_bf16 a[12:15], v[144:147], v[56:59], a[12:15]\n" \
"s_mul_i32 s94, 0x90000, s101\n" \
"s_waitcnt lgkmcnt(1)\n" \
"v_mfma_f32_16x16x32_bf16 a[12:15], v[140:143], v[52:55], a[12:15]\n" \
"v_add_u32_e64 v217, s95, v216\n" \
"s_add_u32 s99, 0x4240000, s94\n" \
"v_mfma_f32_16x16x32_bf16 a[44:47], v[140:143], v[60:63], a[44:47]\n" \
"s_lshl_b32 s96, %[w], 14\n" \
"s_add_u32 s88, s99, s96\n" \
"v_mfma_f32_16x16x32_bf16 a[44:47], v[144:147], v[64:67], a[44:47]\n" \
"s_lshl_b32 s95, s97, 16\n" \
"v_writelane_b32 v11, s39, 52\n" \
"v_mfma_f32_16x16x32_bf16 a[76:79], v[144:147], v[72:75], a[76:79]\n" \
"s_add_u32 s89, s88, s95\n" \
"s_lshl_b32 s101, s97, 11\n" \
"v_mfma_f32_16x16x32_bf16 a[76:79], v[140:143], v[68:71], a[76:79]\n" \
"v_writelane_b32 v11, s89, 37\n" \
"s_add_u32 s100, 0x3800, s89\n" \
"v_mfma_f32_16x16x32_bf16 a[108:111], v[140:143], v[76:79], a[108:111]\n" \
"s_add_u32 s94, s88, s101\n" \
"v_writelane_b32 v11, s100, 51\n" \
"v_mfma_f32_16x16x32_bf16 a[108:111], v[144:147], v[80:83], a[108:111]\n" \
"s_add_u32 s99, 0x80000, s94\n" \
"s_lshl_b32 s96, %[w], 17\n" \
"v_mfma_f32_16x16x32_bf16 a[128:131], v[120:123], v[88:91], a[128:131]\n" \
"v_writelane_b32 v11, s99, 49\n" \
"s_add_u32 s95, s88, s96\n" \
"v_mfma_f32_16x16x32_bf16 a[128:131], v[116:119], v[84:87], a[128:131]\n" \
"s_and_b32 s94, 1, s97\n" \
"s_add_u32 s89, s95, s98\n" \
"v_mfma_f32_16x16x32_bf16 a[160:163], v[116:119], v[92:95], a[160:163]\n" \
"s_lshl_b32 s99, s94, 10\n" \
"s_add_u32 s100, 0x3840, s89\n" \
"v_mfma_f32_16x16x32_bf16 a[160:163], v[120:123], v[96:99], a[160:163]\n" \
"s_add_u32 s96, s88, s99\n" \
"v_writelane_b32 v11, s100, 5\n" \
"v_mfma_f32_16x16x32_bf16 a[192:195], v[120:123], v[104:107], a[192:195]\n" \
"s_lshr_b32 s101, s97, 1\n" \
"s_add_u32 s95, 0x80000, s96\n" \
"v_mfma_f32_16x16x32_bf16 a[192:195], v[116:119], v[100:103], a[192:195]\n" \
"s_add_u32 s89, 1, s97\n" \
"s_and_b32 s100, 7, s89\n" \
"v_mfma_f32_16x16x32_bf16 a[224:227], v[116:119], v[108:111], a[224:227]\n" \
"s_lshl_b32 s94, s100, 16\n" \
"s_waitcnt lgkmcnt(0)\n" \
"v_mfma_f32_16x16x32_bf16 a[224:227], v[120:123], v[112:115], a[224:227]\n" \
"s_cmp_eq_u32 %[w], s101\n" \
"s_cselect_b32 s98, s95, 0x40000000\n" \
"v_mfma_f32_16x16x32_bf16 a[132:135], v[128:131], v[88:91], a[132:135]\n" \
"s_add_u32 s96, 2, s97\n" \
"v_writelane_b32 v11, s98, 38\n" \
"v_mfma_f32_16x16x32_bf16 a[132:135], v[124:127], v[84:87], a[132:135]\n" \
"s_add_u32 s99, s88, s94\n" \
"s_and_b32 s101, 7, s96\n" \
"v_mfma_f32_16x16x32_bf16 a[164:167], v[124:127], v[92:95], a[164:167]\n" \
"s_add_u32 s100, 3, s97\n" \
"v_writelane_b32 v11, s99, 42\n" \
"v_mfma_f32_16x16x32_bf16 a[164:167], v[128:131], v[96:99], a[164:167]\n" \
"s_lshl_b32 s95, s101, 16\n" \
"s_and_b32 s94, 7, s100\n" \
"v_mfma_f32_16x16x32_bf16 a[196:199], v[128:131], v[104:107], a[196:199]\n" \
"s_add_u32 s98, s88, s95\n" \
"s_lshl_b32 s99, s94, 16\n" \
"v_mfma_f32_16x16x32_bf16 a[196:199], v[124:127], v[100:103], a[196:199]\n" \
"s_add_u32 s89, 0x800, s98\n" \
"s_add_u32 s96, s88, s99\n" \
"v_mfma_f32_16x16x32_bf16 a[228:231], v[124:127], v[108:111], a[228:231]\n" \
"s_add_u32 s95, 4, s97\n" \
"v_writelane_b32 v11, s89, 43\n" \
"v_mfma_f32_16x16x32_bf16 a[228:231], v[128:131], v[112:115], a[228:231]\n" \
"s_add_u32 s101, 0x1000, s96\n" \
"s_and_b32 s98, 7, s95\n" \
"v_mfma_f32_16x16x32_bf16 a[136:139], v[136:139], v[88:91], a[136:139]\n" \
"s_add_u32 s99, 5, s97\n" \
"v_writelane_b32 v11, s101, 44\n" \
"v_mfma_f32_16x16x32_bf16 a[136:139], v[132:135], v[84:87], a[136:139]\n" \
"s_lshl_b32 s89, s98, 16\n" \
"s_and_b32 s96, 7, s99\n" \
"v_mfma_f32_16x16x32_bf16 a[168:171], v[132:135], v[92:95], a[168:171]\n" \
"s_add_u32 s100, s88, s89\n" \
"s_lshl_b32 s101, s96, 16\n" \
"v_mfma_f32_16x16x32_bf16 a[168:171], v[136:139], v[96:99], a[168:171]\n" \
"s_add_u32 s94, 0x1800, s100\n" \
"s_add_u32 s95, s88, s101\n" \
"v_mfma_f32_16x16x32_bf16 a[200:203], v[136:139], v[104:107], a[200:203]\n" \
"s_add_u32 s89, 6, s97\n" \
"v_writelane_b32 v11, s94, 45\n" \
"v_mfma_f32_16x16x32_bf16 a[200:203], v[132:135], v[100:103], a[200:203]\n" \
"s_add_u32 s98, 0x2000, s95\n" \
"s_and_b32 s100, 7, s89\n" \
"v_mfma_f32_16x16x32_bf16 a[232:235], v[132:135], v[108:111], a[232:235]\n" \
"s_add_u32 s101, 7, s97\n" \
"v_writelane_b32 v11, s98, 46\n" \
"v_mfma_f32_16x16x32_bf16 a[232:235], v[136:139], v[112:115], a[232:235]\n" \
"s_lshl_b32 s94, s100, 16\n" \
"s_and_b32 s95, 7, s101\n" \
"v_mfma_f32_16x16x32_bf16 a[140:143], v[144:147], v[88:91], a[140:143]\n" \
"s_add_u32 s62, s22, 0x180\n" \
"s_and_b32 s62, s62, 0x3ff\n" \
"v_mfma_f32_16x16x32_bf16 a[140:143], v[140:143], v[84:87], a[140:143]\n" \
"s_lshl_b32 s63, s62, 12\n" \
"s_add_u32 s99, s88, s94\n" \
"v_mfma_f32_16x16x32_bf16 a[172:175], v[140:143], v[92:95], a[172:175]\n" \
"s_lshl_b32 s98, s95, 16\n" \
"s_add_u32 s96, 0x2800, s99\n" \
"v_mfma_f32_16x16x32_bf16 a[172:175], v[144:147], v[96:99], a[172:175]\n" \
"s_add_u32 s89, s88, s98\n" \
"v_writelane_b32 v11, s96, 47\n" \
"v_mfma_f32_16x16x32_bf16 a[204:207], v[144:147], v[104:107], a[204:207]\n" \
"s_add_u32 s100, 0x3000, s89\n" \
"v_writelane_b32 v11, s100, 48\n" \
"v_mfma_f32_16x16x32_bf16 a[204:207], v[140:143], v[100:103], a[204:207]\n" \
"s_mov_b32 s87, 0x5040100\n" \
"v_lshlrev_b32_e32 v188, 7, v218\n" \
"v_mfma_f32_16x16x32_bf16 a[236:239], v[140:143], v[108:111], a[236:239]\n" \
"v_readfirstlane_b32 %[tag], v180\n" \
"s_lshr_b32 s94, %[b], 5\n" \
"v_mfma_f32_16x16x32_bf16 a[236:239], v[144:147], v[112:115], a[236:239]\n" \
"s_add_u32 %[tag], %[tag], 0x9E3779B1\n" \
"v_mov_b32_e32 v45, %[tag]\n" \
"s_waitcnt vmcnt(1)\n" \
"s_sleep 8\n" \
"s_barrier\n" \
"v_mfma_f32_16x16x32_bf16 a[144:147], v[88:91], v[152:155], a[144:147]\n" \
"s_add_u32 m0, %[mA], 0x8000\n" \
"s_add_u32 s85, s62, 0x0\n" \
"s_nop 0\n" \
"v_mfma_f32_16x16x32_bf16 a[144:147], v[84:87], v[148:151], a[144:147]\n" \
"buffer_load_dwordx4 v22, s[64:67], s85 offen lds\n" \
"v_mfma_f32_16x16x32_bf16 a[176:179], v[92:95], v[148:151], a[176:179]\n" \
"s_add_u32 m0, %[mA], 0x8400\n" \
"s_add_u32 s84, s62, 0x2000\n" \
"v_mfma_f32_16x16x32_bf16 a[176:179], v[96:99], v[152:155], a[176:179]\n" \
"ds_read_b64_tr_b16 v[120:121], v32 offset:8192\n" \
"ds_read_b64_tr_b16 v[122:123], v32 offset:9216\n" \
"v_mfma_f32_16x16x32_bf16 a[208:211], v[104:107], v[152:155], a[208:211]\n" \
"buffer_load_dwordx4 v23, s[64:67], s84 offen lds\n" \
"v_mfma_f32_16x16x32_bf16 a[208:211], v[100:103], v[148:151], a[208:211]\n" \
"ds_read_b128 v[56:59], v29 offset:16384\n" \
"ds_read_b64_tr_b16 v[116:117], v32 offset:0\n" \
"v_mfma_f32_16x16x32_bf16 a[240:243], v[108:111], v[148:151], a[240:243]\n" \
"s_add_u32 m0, %[mB], 0x8000\n" \
"s_add_u32 s85, s63, 0x0\n" \
"v_mfma_f32_16x16x32_bf16 a[240:243], v[112:115], v[152:155], a[240:243]\n" \
"buffer_load_dwordx4 v26, s[68:71], s85 offen lds\n" \
"v_mfma_f32_16x16x32_bf16 a[148:151], v[88:91], v[160:163], a[148:151]\n" \
"ds_read_b64_tr_b16 v[118:119], v32 offset:1024\n" \
"ds_read_b128 v[52:55], v28 offset:16384\n" \
"v_mfma_f32_16x16x32_bf16 a[148:151], v[84:87], v[156:159], a[148:151]\n" \
"ds_read_b128 v[60:63], v28 offset:18432\n" \
"ds_read_b128 v[64:67], v29 offset:18432\n" \
"v_mfma_f32_16x16x32_bf16 a[180:183], v[92:95], v[156:159], a[180:183]\n" \
"s_add_u32 m0, %[mB], 0xc000\n" \
"s_add_u32 s84, s63, 0x0\n" \
"v_mfma_f32_16x16x32_bf16 a[180:183], v[96:99], v[160:163], a[180:183]\n" \
"buffer_load_dwordx4 v26, s[72:75], s84 offen lds\n" \
"v_mfma_f32_16x16x32_bf16 a[212:215], v[104:107], v[160:163], a[212:215]\n" \
"ds_read_b128 v[72:75], v29 offset:20480\n" \
"ds_read_b128 v[68:71], v28 offset:20480\n" \
"v_mfma_f32_16x16x32_bf16 a[212:215], v[100:103], v[156:159], a[212:215]\n" \
"ds_read_b128 v[76:79], v28 offset:22528\n" \
"ds_read_b128 v[80:83], v29 offset:22528\n" \
"v_mfma_f32_16x16x32_bf16 a[244:247], v[108:111], v[156:159], a[244:247]\n" \
"s_add_u32 m0, %[mA], 0x8800\n" \
"s_add_u32 s85, s62, 0x4000\n" \
"v_mfma_f32_16x16x32_bf16 a[244:247], v[112:115], v[160:163], a[244:247]\n" \
"buffer_load_dwordx4 v22, s[64:67], s85 offen lds\n" \
"v_mfma_f32_16x16x32_bf16 a[152:155], v[88:91], v[168:171], a[152:155]\n" \
"ds_read_b64_tr_b16 v[128:129], v33 offset:8192\n" \
"ds_read_b64_tr_b16 v[130:131], v33 offset:9216\n" \
"v_mfma_f32_16x16x32_bf16 a[152:155], v[84:87], v[164:167], a[152:155]\n" \
"s_add_u32 m0, %[mA], 0x8c00\n" \
"s_add_u32 s84, s62, 0x6000\n" \
"v_mfma_f32_16x16x32_bf16 a[184:187], v[92:95], v[164:167], a[184:187]\n" \
"buffer_load_dwordx4 v23, s[64:67], s84 offen lds\n" \
"v_mfma_f32_16x16x32_bf16 a[184:187], v[96:99], v[168:171], a[184:187]\n" \
"ds_read_b64_tr_b16 v[124:125], v33 offset:0\n" \
"ds_read_b64_tr_b16 v[126:127], v33 offset:1024\n" \
"v_mfma_f32_16x16x32_bf16 a[216:219], v[104:107], v[168:171], a[216:219]\n" \
"ds_read_b64_tr_b16 v[136:137], v34 offset:8192\n" \
"ds_read_b64_tr_b16 v[138:139], v34 offset:9216\n" \
"v_mfma_f32_16x16x32_bf16 a[216:219], v[100:103], v[164:167], a[216:219]\n" \
"s_add_u32 m0, %[mB], 0x8400\n" \
"s_add_u32 s85, s63, 0x8000\n" \
"v_mfma_f32_16x16x32_bf16 a[248:251], v[108:111], v[164:167], a[248:251]\n" \
"buffer_load_dwordx4 v26, s[68:71], s85 offen lds\n" \
"v_mfma_f32_16x16x32_bf16 a[248:251], v[112:115], v[168:171], a[248:251]\n" \
"ds_read_b64_tr_b16 v[132:133], v34 offset:0\n" \
"ds_read_b64_tr_b16 v[134:135], v34 offset:1024\n" \
"v_mfma_f32_16x16x32_bf16 a[156:159], v[88:91], v[176:179], a[156:159]\n" \
"ds_read_b64_tr_b16 v[144:145], v35 offset:8192\n" \
"ds_read_b64_tr_b16 v[146:147], v35 offset:9216\n" \
"v_mfma_f32_16x16x32_bf16 a[156:159], v[84:87], v[172:175], a[156:159]\n" \
"s_add_u32 m0, %[mB], 0xc400\n" \
"s_add_u32 s84, s63, 0x8000\n" \
"v_mfma_f32_16x16x32_bf16 a[188:191], v[92:95], v[172:175], a[188:191]\n" \
"buffer_load_dwordx4 v26, s[72:75], s84 offen lds\n" \
"v_mfma_f32_16x16x32_bf16 a[188:191], v[96:99], v[176:179], a[188:191]\n" \
"ds_read_b64_tr_b16 v[140:141], v35 offset:0\n" \
"ds_read_b64_tr_b16 v[142:143], v35 offset:1024\n" \
"v_mfma_f32_16x16x32_bf16 a[220:223], v[104:107], v[176:179], a[220:223]\n" \
"ds_read_b64_tr_b16 v[152:153], v32 offset:24576\n" \
"ds_read_b64_tr_b16 v[154:155], v32 offset:25600\n" \
"v_mfma_f32_16x16x32_bf16 a[220:223], v[100:103], v[172:175], a[220:223]\n" \
"s_add_u32 m0, %[mA], 0x9000\n" \
"s_add_u32 s85, s62, 0x8000\n" \
"v_mfma_f32_16x16x32_bf16 a[252:255], v[108:111], v[172:175], a[252:255]\n" \
"buffer_load_dwordx4 v22, s[64:67], s85 offen lds\n" \
"v_mfma_f32_16x16x32_bf16 a[252:255], v[112:115], v[176:179], a[252:255]\n" \
"ds_read_b64_tr_b16 v[148:149], v32 offset:16384\n" \
"s_waitcnt lgkmcnt(13)\n" \
"s_nop 0\n" \
"v_mfma_f32_16x16x32_bf16 a[0:3], v[120:123], v[56:59], a[0:3]\n" \
"s_add_u32 m0, %[mA], 0x9400\n" \
"s_add_u32 s84, s62, 0xa000\n" \
"v_mfma_f32_16x16x32_bf16 a[0:3], v[116:119], v[52:55], a[0:3]\n" \
"buffer_load_dwordx4 v23, s[64:67], s84 offen lds\n" \
"v_mfma_f32_16x16x32_bf16 a[32:35], v[116:119], v[60:63], a[32:35]\n" \
"ds_read_b64_tr_b16 v[150:151], v32 offset:17408\n" \
"ds_read_b64_tr_b16 v[160:161], v33 offset:24576\n" \
"v_mfma_f32_16x16x32_bf16 a[32:35], v[120:123], v[64:67], a[32:35]\n" \
"ds_read_b64_tr_b16 v[162:163], v33 offset:25600\n" \
"ds_read_b64_tr_b16 v[156:157], v33 offset:16384\n" \
"v_mfma_f32_16x16x32_bf16 a[64:67], v[120:123], v[72:75], a[64:67]\n" \
"s_add_u32 m0, %[mB], 0x8800\n" \
"s_add_u32 s85, s63, 0x10000\n" \
"v_mfma_f32_16x16x32_bf16 a[64:67], v[116:119], v[68:71], a[64:67]\n" \
"buffer_load_dwordx4 v27, s[68:71], s85 offen lds\n" \
"v_mfma_f32_16x16x32_bf16 a[96:99], v[116:119], v[76:79], a[96:99]\n" \
"ds_read_b64_tr_b16 v[158:159], v33 offset:17408\n" \
"ds_read_b64_tr_b16 v[168:169], v34 offset:24576\n" \
"v_mfma_f32_16x16x32_bf16 a[96:99], v[120:123], v[80:83], a[96:99]\n" \
"ds_read_b64_tr_b16 v[170:171], v34 offset:25600\n" \
"ds_read_b64_tr_b16 v[164:165], v34 offset:16384\n" \
"v_mfma_f32_16x16x32_bf16 a[4:7], v[128:131], v[56:59], a[4:7]\n" \
"s_add_u32 m0, %[mB], 0xc800\n" \
"s_waitcnt lgkmcnt(13)\n" \
"s_nop 0\n" \
"v_mfma_f32_16x16x32_bf16 a[4:7], v[124:127], v[52:55], a[4:7]\n" \
"s_add_u32 s84, s63, 0x10000\n" \
"ds_read_b64_tr_b16 v[166:167], v34 offset:17408\n" \
"v_mfma_f32_16x16x32_bf16 a[36:39], v[124:127], v[60:63], a[36:39]\n" \
"buffer_load_dwordx4 v27, s[72:75], s84 offen lds\n" \
"v_mfma_f32_16x16x32_bf16 a[36:39], v[128:131], v[64:67], a[36:39]\n" \
"ds_read_b64_tr_b16 v[176:177], v35 offset:24576\n" \
"ds_read_b64_tr_b16 v[178:179], v35 offset:25600\n" \
"v_mfma_f32_16x16x32_bf16 a[68:71], v[128:131], v[72:75], a[68:71]\n" \
"s_add_u32 m0, %[mA], 0x9800\n" \
"s_add_u32 s85, s62, 0xc000\n" \
"v_mfma_f32_16x16x32_bf16 a[68:71], v[124:127], v[68:71], a[68:71]\n" \
"buffer_load_dwordx4 v22, s[64:67], s85 offen lds\n" \
"v_mfma_f32_16x16x32_bf16 a[100:103], v[124:127], v[76:79], a[100:103]\n" \
"ds_read_b64_tr_b16 v[172:173], v35 offset:16384\n" \
"ds_read_b64_tr_b16 v[174:175], v35 offset:17408\n" \
"v_mfma_f32_16x16x32_bf16 a[100:103], v[128:131], v[80:83], a[100:103]\n" \
"s_add_u32 m0, %[mA], 0x9c00\n" \
"s_add_u32 s84, s62, 0xe000\n" \
"v_mfma_f32_16x16x32_bf16 a[8:11], v[136:139], v[56:59], a[8:11]\n" \
"buffer_load_dwordx4 v23, s[64:67], s84 offen lds\n" \
"v_mfma_f32_16x16x32_bf16 a[8:11], v[132:135], v[52:55], a[8:11]\n" \
"ds_read_b128 v[88:91], v29 offset:0\n" \
"ds_read_b128 v[84:87], v28 offset:0\n" \
"v_mfma_f32_16x16x32_bf16 a[40:43], v[132:135], v[60:63], a[40:43]\n" \
"ds_read_b128 v[92:95], v28 offset:2048\n" \
"ds_read_b128 v[96:99], v29 offset:2048\n" \
"v_mfma_f32_16x16x32_bf16 a[40:43], v[136:139], v[64:67], a[40:43]\n" \
"s_add_u32 m0, %[mB], 0x8c00\n" \
"s_add_u32 s85, s63, 0x18000\n" \
"v_mfma_f32_16x16x32_bf16 a[72:75], v[136:139], v[72:75], a[72:75]\n" \
"buffer_load_dwordx4 v27, s[68:71], s85 offen lds\n" \
"v_mfma_f32_16x16x32_bf16 a[72:75], v[132:135], v[68:71], a[72:75]\n" \
"ds_read_b128 v[104:107], v29 offset:4096\n" \
"ds_read_b128 v[100:103], v28 offset:4096\n" \
"v_mfma_f32_16x16x32_bf16 a[104:107], v[132:135], v[76:79], a[104:107]\n" \
"ds_read_b128 v[108:111], v28 offset:6144\n" \
"ds_read_b128 v[112:115], v29 offset:6144\n" \
"v_mfma_f32_16x16x32_bf16 a[104:107], v[136:139], v[80:83], a[104:107]\n" \
"s_add_u32 m0, %[mB], 0xcc00\n" \
"s_add_u32 s84, s63, 0x18000\n" \
"v_mfma_f32_16x16x32_bf16 a[12:15], v[144:147], v[56:59], a[12:15]\n" \
"s_sub_u32 s95, 15, s94\n" \
"s_waitcnt lgkmcnt(12)\n" \
"v_mfma_f32_16x16x32_bf16 a[12:15], v[140:143], v[52:55], a[12:15]\n" \
"buffer_load_dwordx4 v27, s[72:75], s84 offen lds\n" \
"v_mfma_f32_16x16x32_bf16 a[44:47], v[140:143], v[60:63], a[44:47]\n" \
"s_cmp_lt_u32 %[w], 2\n" \
"s_cselect_b32 s96, s95, s94\n" \
"v_mfma_f32_16x16x32_bf16 a[44:47], v[144:147], v[64:67], a[44:47]\n" \
"s_lshl_b32 s97, s96, 7\n" \
"s_add_u32 s98, 0xfffffff0, s97\n" \
"v_mfma_f32_16x16x32_bf16 a[76:79], v[144:147], v[72:75], a[76:79]\n" \
"buffer_load_dwordx4 v[200:203], v192, s[76:79], s56 offen offset:0\n" \
"v_mfma_f32_16x16x32_bf16 a[76:79], v[140:143], v[68:71], a[76:79]\n" \
"s_cmp_eq_u32 s97, 0\n" \
"s_cselect_b32 s99, 0, s98\n" \
"v_mfma_f32_16x16x32_bf16 a[108:111], v[140:143], v[76:79], a[108:111]\n" \
"s_and_b32 s100, 1, %[w]\n" \
"v_cmp_gt_u32_e32 vcc, 32, v212\n" \
"v_mfma_f32_16x16x32_bf16 a[108:111], v[144:147], v[80:83], a[108:111]\n" \
"s_lshl_b32 s101, s100, 3\n" \
"s_add_u32 s88, s99, s101\n" \
"v_mfma_f32_16x16x32_bf16 a[16:19], v[56:59], v[152:155], a[16:19]\n" \
"v_lshrrev_b32_e32 v212, 4, v20\n" \
"v_cndmask_b32_e32 v17, v217, v214, vcc\n" \
"v_mfma_f32_16x16x32_bf16 a[16:19], v[52:55], v[148:151], a[16:19]\n" \
"buffer_load_dwordx4 v[204:207], v193, s[76:79], s56 offen offset:0\n" \
"v_mfma_f32_16x16x32_bf16 a[48:51], v[60:63], v[148:151], a[48:51]\n" \
"v_lshrrev_b32_e32 v213, 3, v212\n" \
"s_lshr_b32 s99, %[b], 3\n" \
"v_mfma_f32_16x16x32_bf16 a[48:51], v[64:67], v[152:155], a[48:51]\n" \
"v_add_u32_e32 v214, s88, v213\n" \
"s_lshl_b32 s96, s100, 6\n" \
"v_mfma_f32_16x16x32_bf16 a[80:83], v[72:75], v[152:155], a[80:83]\n" \
"s_and_b32 s101, 3, s99\n" \
"s_add_u32 s98, s97, s96\n" \
"v_mfma_f32_16x16x32_bf16 a[80:83], v[68:71], v[148:151], a[80:83]\n" \
"s_add_u32 s88, s101, s94\n" \
"v_add_u32_e32 v221, s98, v213\n" \
"v_mfma_f32_16x16x32_bf16 a[112:115], v[76:79], v[148:151], a[112:115]\n" \
"s_add_u32 s96, 0xfffffff7, s88\n" \
"s_lshl_b32 s89, s100, 2\n" \
"v_mfma_f32_16x16x32_bf16 a[112:115], v[80:83], v[152:155], a[112:115]\n" \
"buffer_load_dword v208, v194, s[76:79], s56 offen offset:32\n" \
"v_mfma_f32_16x16x32_bf16 a[20:23], v[56:59], v[160:163], a[20:23]\n" \
"s_cmp_lt_u32 s88, 9\n" \
"s_cselect_b32 s98, s88, s96\n" \
"v_mfma_f32_16x16x32_bf16 a[20:23], v[52:55], v[156:159], a[20:23]\n" \
"s_add_u32 s99, s97, s100\n" \
"s_lshr_b32 s88, s97, 4\n" \
"v_mfma_f32_16x16x32_bf16 a[52:55], v[60:63], v[156:159], a[52:55]\n" \
"s_lshl_b32 s23, s98, 7\n" \
"s_add_u32 s96, s88, s89\n" \
"v_mfma_f32_16x16x32_bf16 a[52:55], v[64:67], v[160:163], a[52:55]\n" \
"s_cmp_eq_u32 s99, 0\n" \
"s_cselect_b32 s49, 0xff800000, 0\n" \
"v_mfma_f32_16x16x32_bf16 a[84:87], v[72:75], v[160:163], a[84:87]\n" \
"v_lshrrev_b32_e32 v217, 4, v212\n" \
"s_mul_i32 s98, 0x24800, s96\n" \
"v_mfma_f32_16x16x32_bf16 a[84:87], v[68:71], v[156:159], a[84:87]\n" \
"buffer_load_dword v209, v194, s[76:79], s56 offen offset:1184\n" \
"v_mfma_f32_16x16x32_bf16 a[116:119], v[76:79], v[156:159], a[116:119]\n" \
"s_and_b32 s97, 7, %[b]\n" \
"v_and_b32_e32 v216, 7, v212\n" \
"v_mfma_f32_16x16x32_bf16 a[116:119], v[80:83], v[160:163], a[116:119]\n" \
"v_add_u32_e64 v218, s89, v217\n" \
"s_add_u32 s99, 0x3000000, s98\n" \
"v_mfma_f32_16x16x32_bf16 a[24:27], v[56:59], v[168:171], a[24:27]\n" \
"s_lshl_b32 s88, s97, 8\n" \
"v_xor_b32_e32 v219, v216, v218\n" \
"v_mfma_f32_16x16x32_bf16 a[24:27], v[52:55], v[164:167], a[24:27]\n" \
"s_add_u32 s89, s99, s88\n" \
"s_lshl_b32 s96, s101, 2\n" \
"v_mfma_f32_16x16x32_bf16 a[56:59], v[60:63], v[164:167], a[56:59]\n" \
"v_mul_u32_u24_e32 v215, 0x480, v214\n" \
"v_lshlrev_b32_e32 v220, 4, v219\n" \
"v_mfma_f32_16x16x32_bf16 a[56:59], v[64:67], v[168:171], a[56:59]\n" \
"buffer_load_dword v211, v194, s[76:79], s56 offen offset:2336\n" \
"v_mfma_f32_16x16x32_bf16 a[88:91], v[72:75], v[168:171], a[88:91]\n" \
"s_mov_b32 s76, s14\n" \
"s_and_b32 s77, s15, 0xffff\n" \
"v_mfma_f32_16x16x32_bf16 a[88:91], v[68:71], v[164:167], a[88:91]\n" \
"s_mov_b32 s78, 0x480\n" \
"s_add_u32 s56, s57, 0x1000\n" \
"v_mfma_f32_16x16x32_bf16 a[120:123], v[76:79], v[164:167], a[120:123]\n" \
"v_lshrrev_b32_e32 v229, 2, v212\n" \
"v_and_b32_e32 v234, 3, v212\n" \
"v_mfma_f32_16x16x32_bf16 a[120:123], v[80:83], v[168:171], a[120:123]\n" \
"v_and_b32_e32 v249, 2, v212\n" \
"s_waitcnt lgkmcnt(0)\n" \
"v_mfma_f32_16x16x32_bf16 a[28:31], v[56:59], v[176:179], a[28:31]\n" \
"s_add_u32 s98, s89, s96\n" \
"v_add_u32_e32 v21, v215, v220\n" \
"v_mfma_f32_16x16x32_bf16 a[28:31], v[52:55], v[172:175], a[28:31]\n" \
"buffer_load_ushort v210, v195, s[76:79], 0 offen\n" \
"v_mfma_f32_16x16x32_bf16 a[60:63], v[60:63], v[172:175], a[60:63]\n" \
"v_add_u32_e64 v225, 4, v217\n" \
"s_add_u32 s51, 0x4000, s98\n" \
"v_mfma_f32_16x16x32_bf16 a[60:63], v[64:67], v[176:179], a[60:63]\n" \
"s_lshl_b32 s88, s97, 2\n" \
"v_lshrrev_b32_e32 v212, 4, v20\n" \
"v_mfma_f32_16x16x32_bf16 a[92:95], v[72:75], v[176:179], a[92:95]\n" \
"v_xor_b32_e32 v223, v216, v217\n" \
"v_xor_b32_e32 v226, v216, v225\n" \
"v_mfma_f32_16x16x32_bf16 a[92:95], v[68:71], v[172:175], a[92:95]\n" \
"v_mov_b32_e32 v250, 0xeeeeeeee\n" \
"v_mov_b32_e32 v251, 0x44444444\n" \
"v_mfma_f32_16x16x32_bf16 a[124:127], v[76:79], v[172:175], a[124:127]\n" \
"v_cmp_ne_u32_e32 vcc, 0, v249\n" \
"s_add_u32 s89, s88, s101\n" \
"v_mfma_f32_16x16x32_bf16 a[124:127], v[80:83], v[176:179], a[124:127]\n" \
"s_lshl_b32 s98, %[w], 10\n" \
"v_and_b32_e32 v215, 15, v212\n" \
"L_pf1:\n" \
"s_cbranch_execz L_pf5\n" \
"s_nop 0\n" \
"v_mfma_f32_16x16x32_bf16 a[144:147], v[88:91], v[152:155], a[144:147]\n" \
"v_cndmask_b32_e32 v50, v251, v250, vcc\n" \
"s_lshl_b32 s96, s89, 9\n" \
"v_mfma_f32_16x16x32_bf16 a[144:147], v[84:87], v[148:151], a[144:147]\n" \
"s_add_u32 s40, 0x14000, s98\n" \
"s_lshl_b32 s97, s89, 20\n" \
"v_mfma_f32_16x16x32_bf16 a[176:179], v[92:95], v[148:151], a[176:179]\n" \
"v_lshrrev_b32_e32 v213, 4, v212\n" \
"v_and_b32_e64 v216, 1, v215\n" \
"v_mfma_f32_16x16x32_bf16 a[176:179], v[96:99], v[152:155], a[176:179]\n" \
"v_lshlrev_b32_e32 v228, 3, v217\n" \
"v_and_b32_e32 v238, 1, v217\n" \
"v_mfma_f32_16x16x32_bf16 a[208:211], v[104:107], v[152:155], a[208:211]\n" \
"v_lshlrev_b32_e32 v242, 4, v217\n" \
"s_add_u32 s50, s99, s96\n" \
"v_mfma_f32_16x16x32_bf16 a[208:211], v[100:103], v[148:151], a[208:211]\n" \
"s_add_u32 s88, 0x1000000, s97\n" \
"s_lshl_b32 s101, %[w], 12\n" \
"v_mfma_f32_16x16x32_bf16 a[240:243], v[108:111], v[148:151], a[240:243]\n" \
"s_lshr_b32 s98, %[w], 1\n" \
"v_lshlrev_b32_e32 v214, 2, v213\n" \
"v_mfma_f32_16x16x32_bf16 a[240:243], v[112:115], v[152:155], a[240:243]\n" \
"v_mov_b32_e32 v218, 0x1000c0c\n" \
"v_mov_b32_e32 v219, 0xc0c0100\n" \
"v_mfma_f32_16x16x32_bf16 a[148:151], v[88:91], v[160:163], a[148:151]\n" \
"s_add_u32 s62, s22, 0x200\n" \
"s_and_b32 s62, s62, 0x3ff\n" \
"v_mfma_f32_16x16x32_bf16 a[148:151], v[84:87], v[156:159], a[148:151]\n" \
"s_lshl_b32 s63, s62, 12\n" \
"v_cmp_ne_u32_e32 vcc, 0, v216\n" \
"v_mfma_f32_16x16x32_bf16 a[180:183], v[92:95], v[156:159], a[180:183]\n" \
"v_mul_u32_u24_e32 v222, 0x480, v221\n" \
"s_add_u32 s52, s88, s101\n" \
"v_mfma_f32_16x16x32_bf16 a[180:183], v[96:99], v[160:163], a[180:183]\n" \
"s_lshl_b32 s99, %[w], 11\n" \
"s_lshl_b32 s89, s98, 15\n" \
"v_mfma_f32_16x16x32_bf16 a[212:215], v[104:107], v[160:163], a[212:215]\n" \
"v_add_u32_e32 v217, v214, v216\n" \
"v_cndmask_b32_e32 v220, v219, v218, vcc\n" \
"v_mfma_f32_16x16x32_bf16 a[212:215], v[100:103], v[156:159], a[212:215]\n" \
"v_lshlrev_b32_e32 v224, 4, v223\n" \
"s_add_u32 s96, s88, s99\n" \
"v_mfma_f32_16x16x32_bf16 a[244:247], v[108:111], v[156:159], a[244:247]\n" \
"s_add_u32 s97, s88, s89\n" \
"s_lshl_b32 s101, s100, 11\n" \
"v_mfma_f32_16x16x32_bf16 a[244:247], v[112:115], v[160:163], a[244:247]\n" \
"v_mov_b32_e32 v221, 0xc0c0c0c\n" \
"v_cmp_le_u32_e32 vcc, v217, v215\n" \
"s_waitcnt vmcnt(6)\n" \
"s_sleep 4\n" \
"s_barrier\n" \
"v_mfma_f32_16x16x32_bf16 a[152:155], v[88:91], v[168:171], a[152:155]\n" \
"s_add_u32 m0, %[mA], 0x0\n" \
"s_add_u32 s85, s62, 0x0\n" \
"v_mfma_f32_16x16x32_bf16 a[152:155], v[84:87], v[164:167], a[152:155]\n" \
"buffer_load_dwordx4 v22, s[64:67], s85 offen lds\n" \
"v_mfma_f32_16x16x32_bf16 a[184:187], v[92:95], v[164:167], a[184:187]\n" \
"s_add_u32 m0, %[mA], 0x400\n" \
"s_add_u32 s84, s62, 0x2000\n" \
"v_mfma_f32_16x16x32_bf16 a[184:187], v[96:99], v[168:171], a[184:187]\n" \
"ds_read_b64_tr_b16 v[152:153], v32 offset:57344\n" \
"ds_read_b64_tr_b16 v[154:155], v32 offset:58368\n" \
"v_mfma_f32_16x16x32_bf16 a[216:219], v[104:107], v[168:171], a[216:219]\n" \
"buffer_load_dwordx4 v23, s[64:67], s84 offen lds\n" \
"v_mfma_f32_16x16x32_bf16 a[216:219], v[100:103], v[164:167], a[216:219]\n" \
"ds_read_b128 v[56:59], v29 offset:49152\n" \
"ds_read_b64_tr_b16 v[148:149], v32 offset:49152\n" \
"v_mfma_f32_16x16x32_bf16 a[248:251], v[108:111], v[164:167], a[248:251]\n" \
"s_add_u32 m0, %[mB], 0x0\n" \
"s_add_u32 s85, s63, 0x0\n" \
"v_mfma_f32_16x16x32_bf16 a[248:251], v[112:115], v[168:171], a[248:251]\n" \
"buffer_load_dwordx4 v26, s[68:71], s85 offen lds\n" \
"v_mfma_f32_16x16x32_bf16 a[156:159], v[88:91], v[176:179], a[156:159]\n" \
"ds_read_b64_tr_b16 v[150:151], v32 offset:50176\n" \
"ds_read_b128 v[52:55], v28 offset:49152\n" \
"v_mfma_f32_16x16x32_bf16 a[156:159], v[84:87], v[172:175], a[156:159]\n" \
"ds_read_b128 v[60:63], v28 offset:51200\n" \
"ds_read_b128 v[64:67], v29 offset:51200\n" \
"v_mfma_f32_16x16x32_bf16 a[188:191], v[92:95], v[172:175], a[188:191]\n" \
"s_add_u32 m0, %[mB], 0x4000\n" \
"s_add_u32 s84, s63, 0x0\n" \
"s_nop 0\n" \
"v_mfma_f32_16x16x32_bf16 a[188:191], v[96:99], v[176:179], a[188:191]\n" \
"buffer_load_dwordx4 v26, s[72:75], s84 offen lds\n" \
"v_mfma_f32_16x16x32_bf16 a[220:223], v[104:107], v[176:179], a[220:223]\n" \
"ds_read_b128 v[72:75], v29 offset:53248\n" \
"ds_read_b128 v[68:71], v28 offset:53248\n" \
"v_mfma_f32_16x16x32_bf16 a[220:223], v[100:103], v[172:175], a[220:223]\n" \
"ds_read_b128 v[76:79], v28 offset:55296\n" \
"ds_read_b128 v[80:83], v29 offset:55296\n" \
"v_mfma_f32_16x16x32_bf16 a[252:255], v[108:111], v[172:175], a[252:255]\n" \
"s_add_u32 m0, %[mA], 0x800\n" \
"s_add_u32 s85, s62, 0x4000\n" \
"v_mfma_f32_16x16x32_bf16 a[252:255], v[112:115], v[176:179], a[252:255]\n" \
"buffer_load_dwordx4 v22, s[64:67], s85 offen lds\n" \
"v_mfma_f32_16x16x32_bf16 a[128:131], v[120:123], v[88:91], a[128:131]\n" \
"ds_read_b64_tr_b16 v[160:161], v33 offset:57344\n" \
"ds_read_b64_tr_b16 v[162:163], v33 offset:58368\n" \
"v_mfma_f32_16x16x32_bf16 a[128:131], v[116:119], v[84:87], a[128:131]\n" \
"s_add_u32 m0, %[mA], 0xc00\n" \
"s_add_u32 s84, s62, 0x6000\n" \
"v_mfma_f32_16x16x32_bf16 a[160:163], v[116:119], v[92:95], a[160:163]\n" \
"buffer_load_dwordx4 v23, s[64:67], s84 offen lds\n" \
"v_mfma_f32_16x16x32_bf16 a[160:163], v[120:123], v[96:99], a[160:163]\n" \
"ds_read_b64_tr_b16 v[156:157], v33 offset:49152\n" \
"ds_read_b64_tr_b16 v[158:159], v33 offset:50176\n" \
"v_mfma_f32_16x16x32_bf16 a[192:195], v[120:123], v[104:107], a[192:195]\n" \
"ds_read_b64_tr_b16 v[168:169], v34 offset:57344\n" \
"ds_read_b64_tr_b16 v[170:171], v34 offset:58368\n" \
"v_mfma_f32_16x16x32_bf16 a[192:195], v[116:119], v[100:103], a[192:195]\n" \
"s_add_u32 m0, %[mB], 0x400\n" \
"s_add_u32 s85, s63, 0x8000\n" \
"v_mfma_f32_16x16x32_bf16 a[224:227], v[116:119], v[108:111], a[224:227]\n" \
"buffer_load_dwordx4 v26, s[68:71], s85 offen lds\n" \
"v_mfma_f32_16x16x32_bf16 a[224:227], v[120:123], v[112:115], a[224:227]\n" \
"ds_read_b64_tr_b16 v[164:165], v34 offset:49152\n" \
"ds_read_b64_tr_b16 v[166:167], v34 offset:50176\n" \
"v_mfma_f32_16x16x32_bf16 a[132:135], v[128:131], v[88:91], a[132:135]\n" \
"ds_read_b64_tr_b16 v[176:177], v35 offset:57344\n" \
"ds_read_b64_tr_b16 v[178:179], v35 offset:58368\n" \
"v_mfma_f32_16x16x32_bf16 a[132:135], v[124:127], v[84:87], a[132:135]\n" \
"s_add_u32 m0, %[mB], 0x4400\n" \
"s_add_u32 s84, s63, 0x8000\n" \
"v_mfma_f32_16x16x32_bf16 a[164:167], v[124:127], v[92:95], a[164:167]\n" \
"buffer_load_dwordx4 v26, s[72:75], s84 offen lds\n" \
"v_mfma_f32_16x16x32_bf16 a[164:167], v[128:131], v[96:99], a[164:167]\n" \
"ds_read_b64_tr_b16 v[172:173], v35 offset:49152\n" \
"ds_read_b64_tr_b16 v[174:175], v35 offset:50176\n" \
"v_mfma_f32_16x16x32_bf16 a[196:199], v[128:131], v[104:107], a[196:199]\n" \
"ds_read_b64_tr_b16 v[120:121], v32 offset:40960\n" \
"ds_read_b64_tr_b16 v[122:123], v32 offset:41984\n" \
"v_mfma_f32_16x16x32_bf16 a[196:199], v[124:127], v[100:103], a[196:199]\n" \
"s_add_u32 m0, %[mA], 0x1000\n" \
"s_add_u32 s85, s62, 0x8000\n" \
"v_mfma_f32_16x16x32_bf16 a[228:231], v[124:127], v[108:111], a[228:231]\n" \
"buffer_load_dwordx4 v22, s[64:67], s85 offen lds\n" \
"v_mfma_f32_16x16x32_bf16 a[228:231], v[128:131], v[112:115], a[228:231]\n" \
"ds_read_b64_tr_b16 v[116:117], v32 offset:32768\n" \
"ds_read_b64_tr_b16 v[118:119], v32 offset:33792\n" \
"v_mfma_f32_16x16x32_bf16 a[136:139], v[136:139], v[88:91], a[136:139]\n" \
"s_add_u32 m0, %[mA], 0x1400\n" \
"s_add_u32 s84, s62, 0xa000\n" \
"v_mfma_f32_16x16x32_bf16 a[136:139], v[132:135], v[84:87], a[136:139]\n" \
"buffer_load_dwordx4 v23, s[64:67], s84 offen lds\n" \
"v_mfma_f32_16x16x32_bf16 a[168:171], v[132:135], v[92:95], a[168:171]\n" \
"ds_read_b64_tr_b16 v[128:129], v33 offset:40960\n" \
"ds_read_b64_tr_b16 v[130:131], v33 offset:41984\n" \
"v_mfma_f32_16x16x32_bf16 a[168:171], v[136:139], v[96:99], a[168:171]\n" \
"ds_read_b64_tr_b16 v[124:125], v33 offset:32768\n" \
"ds_read_b64_tr_b16 v[126:127], v33 offset:33792\n" \
"v_mfma_f32_16x16x32_bf16 a[200:203], v[136:139], v[104:107], a[200:203]\n" \
"s_add_u32 m0, %[mB], 0x800\n" \
"s_add_u32 s85, s63, 0x10000\n" \
"v_mfma_f32_16x16x32_bf16 a[200:203], v[132:135], v[100:103], a[200:203]\n" \
"buffer_load_dwordx4 v27, s[68:71], s85 offen lds\n" \
"v_mfma_f32_16x16x32_bf16 a[232:235], v[132:135], v[108:111], a[232:235]\n" \
"ds_read_b64_tr_b16 v[132:133], v34 offset:32768\n" \
"ds_read_b64_tr_b16 v[134:135], v34 offset:33792\n" \
"v_mfma_f32_16x16x32_bf16 a[232:235], v[136:139], v[112:115], a[232:235]\n" \
"ds_read_b64_tr_b16 v[136:137], v34 offset:40960\n" \
"ds_read_b64_tr_b16 v[138:139], v34 offset:41984\n" \
"v_mfma_f32_16x16x32_bf16 a[140:143], v[144:147], v[88:91], a[140:143]\n" \
"s_add_u32 m0, %[mB], 0x4800\n" \
"s_add_u32 s84, s63, 0x10000\n" \
"v_mfma_f32_16x16x32_bf16 a[140:143], v[140:143], v[84:87], a[140:143]\n" \
"buffer_load_dwordx4 v27, s[72:75], s84 offen lds\n" \
"v_mfma_f32_16x16x32_bf16 a[172:175], v[140:143], v[92:95], a[172:175]\n" \
"ds_read_b128 v[84:87], v28 offset:32768\n" \
"ds_read_b128 v[88:91], v29 offset:32768\n" \
"v_mfma_f32_16x16x32_bf16 a[172:175], v[144:147], v[96:99], a[172:175]\n" \
"ds_read_b128 v[92:95], v28 offset:34816\n" \
"ds_read_b128 v[96:99], v29 offset:34816\n" \
"v_mfma_f32_16x16x32_bf16 a[204:207], v[144:147], v[104:107], a[204:207]\n" \
"s_add_u32 m0, %[mA], 0x1800\n" \
"s_add_u32 s85, s62, 0xc000\n" \
"v_mfma_f32_16x16x32_bf16 a[204:207], v[140:143], v[100:103], a[204:207]\n" \
"buffer_load_dwordx4 v22, s[64:67], s85 offen lds\n" \
"v_mfma_f32_16x16x32_bf16 a[236:239], v[140:143], v[108:111], a[236:239]\n" \
"ds_read_b64_tr_b16 v[140:141], v35 offset:32768\n" \
"ds_read_b64_tr_b16 v[142:143], v35 offset:33792\n" \
"v_mfma_f32_16x16x32_bf16 a[236:239], v[144:147], v[112:115], a[236:239]\n" \
"s_add_u32 m0, %[mA], 0x1c00\n" \
"s_waitcnt lgkmcnt(14)\n" \
"s_nop 0\n" \
"v_mfma_f32_16x16x32_bf16 a[16:19], v[56:59], v[152:155], a[16:19]\n" \
"s_add_u32 s84, s62, 0xe000\n" \
"ds_read_b64_tr_b16 v[144:145], v35 offset:40960\n" \
"v_mfma_f32_16x16x32_bf16 a[16:19], v[52:55], v[148:151], a[16:19]\n" \
"buffer_load_dwordx4 v23, s[64:67], s84 offen lds\n" \
"v_mfma_f32_16x16x32_bf16 a[48:51], v[60:63], v[148:151], a[48:51]\n" \
"ds_read_b64_tr_b16 v[146:147], v35 offset:41984\n" \
"ds_read_b128 v[100:103], v28 offset:36864\n" \
"v_mfma_f32_16x16x32_bf16 a[48:51], v[64:67], v[152:155], a[48:51]\n" \
"s_add_u32 m0, %[mB], 0xc00\n" \
"s_add_u32 s85, s63, 0x18000\n" \
"v_mfma_f32_16x16x32_bf16 a[80:83], v[72:75], v[152:155], a[80:83]\n" \
"buffer_load_dwordx4 v27, s[68:71], s85 offen lds\n" \
"v_mfma_f32_16x16x32_bf16 a[80:83], v[68:71], v[148:151], a[80:83]\n" \
"ds_read_b128 v[104:107], v29 offset:36864\n" \
"ds_read_b128 v[108:111], v28 offset:38912\n" \
"v_mfma_f32_16x16x32_bf16 a[112:115], v[76:79], v[148:151], a[112:115]\n" \
"ds_read_b128 v[112:115], v29 offset:38912\n" \
"v_add_u32_e64 v24, v222, v224\n" \
"v_mfma_f32_16x16x32_bf16 a[112:115], v[80:83], v[152:155], a[112:115]\n" \
"s_add_u32 m0, %[mB], 0x4c00\n" \
"s_add_u32 s84, s63, 0x18000\n" \
"v_mfma_f32_16x16x32_bf16 a[20:23], v[56:59], v[160:163], a[20:23]\n" \
"buffer_load_dwordx4 v27, s[72:75], s84 offen lds\n" \
"v_mfma_f32_16x16x32_bf16 a[20:23], v[52:55], v[156:159], a[20:23]\n" \
"s_mov_b32 m0, %[mS]\n" \
"v_lshlrev_b32_e32 v227, 4, v226\n" \
"v_mfma_f32_16x16x32_bf16 a[52:55], v[60:63], v[156:159], a[52:55]\n" \
"s_add_u32 s99, s97, s101\n" \
"s_lshl_b32 s98, s95, 16\n" \
"v_mfma_f32_16x16x32_bf16 a[52:55], v[64:67], v[160:163], a[52:55]\n" \
"v_cndmask_b32_e32 v40, v220, v221, vcc\n" \
"v_add_u32_e32 v25, v222, v227\n" \
"v_mfma_f32_16x16x32_bf16 a[84:87], v[72:75], v[160:163], a[84:87]\n" \
"s_add_u32 s54, 0x10000, s96\n" \
"s_add_u32 s43, s99, s98\n" \
"v_mfma_f32_16x16x32_bf16 a[84:87], v[68:71], v[156:159], a[84:87]\n" \
"s_mul_i32 s88, 0x1800, s100\n" \
"v_mov_b32_e32 v223, 0x3020c0c\n" \
"v_mfma_f32_16x16x32_bf16 a[116:119], v[76:79], v[156:159], a[116:119]\n" \
"v_mov_b32_e32 v224, 0xc0c0302\n" \
"v_cmp_ne_u32_e32 vcc, 0, v216\n" \
"v_mfma_f32_16x16x32_bf16 a[116:119], v[80:83], v[160:163], a[116:119]\n" \
"s_lshl_b32 s96, s94, 16\n" \
"s_add_u32 s97, s43, s88\n" \
"v_mfma_f32_16x16x32_bf16 a[24:27], v[56:59], v[168:171], a[24:27]\n" \
"v_add_u32_e32 v222, 2, v217\n" \
"v_cndmask_b32_e32 v225, v224, v223, vcc\n" \
"v_mfma_f32_16x16x32_bf16 a[24:27], v[52:55], v[164:167], a[24:27]\n" \
"s_add_u32 s42, s99, s96\n" \
"s_add_u32 s45, 0x4000, s97\n" \
"v_mfma_f32_16x16x32_bf16 a[56:59], v[60:63], v[164:167], a[56:59]\n" \
"s_lshr_b32 s94, %[w], 1\n" \
"s_and_b32 s95, 1, %[w]\n" \
"v_mfma_f32_16x16x32_bf16 a[56:59], v[64:67], v[168:171], a[56:59]\n" \
"v_mov_b32_e32 v226, 0xc0c0c0c\n" \
"v_cmp_le_u32_e32 vcc, v222, v215\n" \
"v_mfma_f32_16x16x32_bf16 a[88:91], v[72:75], v[168:171], a[88:91]\n" \
"s_mov_b32 s86, s10\n" \
"s_add_u32 s96, s94, s95\n" \
"v_mfma_f32_16x16x32_bf16 a[88:91], v[68:71], v[164:167], a[88:91]\n" \
"s_add_u32 s97, 1, s94\n" \
"v_cndmask_b32_e32 v41, v225, v226, vcc\n" \
"s_mov_b32 exec_lo, 0x15\n" \
"s_mov_b32 exec_hi, 0x180000\n" \
"buffer_load_dword v11, s[80:83], 0 offen sc1 lds\n" \
"s_mov_b64 exec, -1\n" \
"v_mfma_f32_16x16x32_bf16 a[120:123], v[76:79], v[164:167], a[120:123]\n" \
"s_mov_b32 m0, %[mS]\n" \
"v_and_b32_e32 v230, 3, v229\n" \
"v_mfma_f32_16x16x32_bf16 a[120:123], v[80:83], v[168:171], a[120:123]\n" \
"s_add_u32 s89, s42, s88\n" \
"s_lshl_b32 s59, s96, 13\n" \
"v_mfma_f32_16x16x32_bf16 a[28:31], v[56:59], v[176:179], a[28:31]\n" \
"s_sub_u32 s98, s97, s95\n" \
"s_lshl_b32 s99, s94, 13\n" \
"v_mfma_f32_16x16x32_bf16 a[28:31], v[52:55], v[172:175], a[28:31]\n" \
"s_mul_i32 s101, 0x1800, s94\n" \
"v_mov_b32_e32 v227, 0xc0c0c0c\n" \
"v_mfma_f32_16x16x32_bf16 a[60:63], v[60:63], v[172:175], a[60:63]\n" \
"v_cmp_le_u32_e32 vcc, v217, v215\n" \
"v_add_u32_e32 v231, v228, v230\n" \
"v_mfma_f32_16x16x32_bf16 a[60:63], v[64:67], v[176:179], a[60:63]\n" \
"s_lshl_b32 s60, s98, 13\n" \
"s_add_u32 s100, 0x1800, s99\n" \
"v_mfma_f32_16x16x32_bf16 a[92:95], v[72:75], v[176:179], a[92:95]\n" \
"s_sub_u32 s88, 0x17000, s101\n" \
"s_lshl_b32 s96, s94, 15\n" \
"v_mfma_f32_16x16x32_bf16 a[92:95], v[68:71], v[172:175], a[92:95]\n" \
"v_cndmask_b32_e32 v42, v227, v220, vcc\n" \
"v_lshlrev_b32_e32 v232, 7, v231\n" \
"v_mfma_f32_16x16x32_bf16 a[124:127], v[76:79], v[172:175], a[124:127]\n" \
"s_cmp_lg_u32 s95, 0\n" \
"s_cselect_b32 s61, s100, s88\n" \
"v_mfma_f32_16x16x32_bf16 a[124:127], v[80:83], v[176:179], a[124:127]\n" \
"s_add_u32 s97, 0x10000, s96\n" \
"s_lshl_b32 s98, s95, 11\n" \
"v_mfma_f32_16x16x32_bf16 a[0:3], v[120:123], v[56:59], a[0:3]\n" \
"v_mov_b32_e32 v228, 0xc0c0c0c\n" \
"v_cmp_le_u32_e32 vcc, v222, v215\n" \
"v_mfma_f32_16x16x32_bf16 a[0:3], v[116:119], v[52:55], a[0:3]\n" \
"v_add_u32_e32 v233, 0x14000, v232\n" \
"v_lshlrev_b32_e32 v235, 3, v234\n" \
"v_mfma_f32_16x16x32_bf16 a[32:35], v[116:119], v[60:63], a[32:35]\n" \
"s_add_u32 s101, s97, s98\n" \
"v_cndmask_b32_e32 v43, v228, v225, vcc\n" \
"v_mfma_f32_16x16x32_bf16 a[32:35], v[120:123], v[64:67], a[32:35]\n" \
"v_and_b32_e32 v231, 2, v212\n" \
"v_add_u32_e64 v236, v233, v235\n" \
"v_mfma_f32_16x16x32_bf16 a[64:67], v[120:123], v[72:75], a[64:67]\n" \
"v_lshrrev_b32_e32 v237, 1, v230\n" \
"v_add_u32_e32 v243, v242, v230\n" \
"v_mfma_f32_16x16x32_bf16 a[64:67], v[116:119], v[68:71], a[64:67]\n" \
"s_waitcnt vmcnt(19)\n" \
"v_perm_b32 v252, v209, v208, s87\n" \
"s_mov_b32 s87, 0x7060302\n" \
"v_perm_b32 v253, v209, v208, s87\n" \
"s_mov_b32 s87, 0x5040100\n" \
"s_waitcnt vmcnt(17)\n" \
"v_perm_b32 v254, v211, v210, s87\n" \
"s_mov_b32 s87, 0x7060100\n" \
"v_perm_b32 v255, v211, v210, s87\n" \
"s_mov_b32 s87, s11\n" \
"s_mov_b32 exec_lo, 0x3ffff\n" \
"s_mov_b32 exec_hi, 0x0\n" \
"global_load_lds_dword v188, s[86:87]\n" \
"s_mov_b64 exec, -1\n" \
"v_mfma_f32_16x16x32_bf16 a[96:99], v[116:119], v[76:79], a[96:99]\n" \
"ds_write_b128 v196, v[200:203]\n" \
"v_mfma_f32_16x16x32_bf16 a[96:99], v[120:123], v[80:83], a[96:99]\n" \
"ds_write2_b32 v198, v252, v253 offset0:0 offset1:2\n" \
"s_waitcnt lgkmcnt(2)\n" \
"v_mfma_f32_16x16x32_bf16 a[4:7], v[128:131], v[56:59], a[4:7]\n" \
"ds_write2_b32 v198, v254, v255 offset0:4 offset1:6\n" \
"s_add_u32 s100, s101, s99\n" \
"v_mfma_f32_16x16x32_bf16 a[4:7], v[124:127], v[52:55], a[4:7]\n" \
"v_and_b32_e32 v229, 3, v212\n" \
"v_mov_b32_e32 v232, 0x3f80\n" \
"v_mfma_f32_16x16x32_bf16 a[36:39], v[124:127], v[60:63], a[36:39]\n" \
"v_cmp_eq_u32_e32 vcc, 0, v231\n" \
"v_lshlrev_b32_e32 v239, 1, v238\n" \
"v_mfma_f32_16x16x32_bf16 a[36:39], v[128:131], v[64:67], a[36:39]\n" \
"ds_write_b128 v197, v[204:207]\n" \
"v_mfma_f32_16x16x32_bf16 a[68:71], v[128:131], v[72:75], a[68:71]\n" \
"v_lshlrev_b32_e32 v244, 7, v243\n" \
"s_add_u32 s88, 0x4000, s100\n" \
"v_mfma_f32_16x16x32_bf16 a[68:71], v[124:127], v[68:71], a[68:71]\n" \
"v_or_b32_e32 v230, 2, v229\n" \
"v_cndmask_b32_e32 v233, 0, v232, vcc\n" \
"v_mfma_f32_16x16x32_bf16 a[100:103], v[124:127], v[76:79], a[100:103]\n" \
"v_or_b32_e32 v240, v237, v239\n" \
"v_add_u32_e32 v245, 0x14000, v244\n" \
"v_mfma_f32_16x16x32_bf16 a[100:103], v[128:131], v[80:83], a[100:103]\n" \
"s_cmp_lg_u32 s95, 0\n" \
"s_cselect_b32 s47, s88, s101\n" \
"v_mfma_f32_16x16x32_bf16 a[8:11], v[136:139], v[56:59], a[8:11]\n" \
"v_cmp_eq_u32_e32 vcc, v230, v213\n" \
"v_lshlrev_b32_e32 v241, 5, v240\n" \
"v_mfma_f32_16x16x32_bf16 a[8:11], v[132:135], v[52:55], a[8:11]\n" \
"v_add_u32_e32 v246, v245, v235\n" \
"v_cndmask_b32_e32 v46, 0, v233, vcc\n" \
"v_mfma_f32_16x16x32_bf16 a[40:43], v[132:135], v[60:63], a[40:43]\n" \
"v_and_b32_e32 v234, 51, v212\n" \
"v_add_u32_e32 v190, v236, v241\n" \
"v_mfma_f32_16x16x32_bf16 a[40:43], v[136:139], v[64:67], a[40:43]\n" \
"v_mov_b32_e32 v235, 0x3f80\n" \
"v_cmp_eq_u32_e32 vcc, 2, v234\n" \
"v_mfma_f32_16x16x32_bf16 a[72:75], v[136:139], v[72:75], a[72:75]\n" \
"s_lshl_b32 s88, s95, 4\n" \
"v_xor_b32_e64 v247, 3, v237\n" \
"v_mfma_f32_16x16x32_bf16 a[72:75], v[132:135], v[68:71], a[72:75]\n" \
"v_cndmask_b32_e32 v47, 0, v235, vcc\n" \
"v_add_u32_e32 v236, s88, v215\n" \
"v_mfma_f32_16x16x32_bf16 a[104:107], v[132:135], v[76:79], a[104:107]\n" \
"v_mov_b32_e32 v237, 0xffff\n" \
"v_cmp_le_u32_e32 vcc, v214, v236\n" \
"v_mfma_f32_16x16x32_bf16 a[104:107], v[136:139], v[80:83], a[104:107]\n" \
"v_mov_b32_e32 v239, 0xffff0000\n" \
"v_cndmask_b32_e32 v238, 0, v237, vcc\n" \
"v_mfma_f32_16x16x32_bf16 a[12:15], v[144:147], v[56:59], a[12:15]\n" \
"s_add_u32 s62, s22, 0x280\n" \
"s_and_b32 s62, s62, 0x3ff\n" \
"v_mfma_f32_16x16x32_bf16 a[12:15], v[140:143], v[52:55], a[12:15]\n" \
"s_lshl_b32 s63, s62, 12\n" \
"v_cmp_lt_u32_e32 vcc, v214, v236\n" \
"v_mfma_f32_16x16x32_bf16 a[44:47], v[140:143], v[60:63], a[44:47]\n" \
"v_add_u32_e32 v241, 2, v214\n" \
"v_cndmask_b32_e32 v240, 0, v239, vcc\n" \
"v_mfma_f32_16x16x32_bf16 a[44:47], v[144:147], v[64:67], a[44:47]\n" \
"v_mov_b32_e32 v242, 0xffff\n" \
"v_cmp_le_u32_e32 vcc, v241, v236\n" \
"v_mfma_f32_16x16x32_bf16 a[76:79], v[144:147], v[72:75], a[76:79]\n" \
"v_lshlrev_b32_e32 v248, 5, v247\n" \
"v_cndmask_b32_e32 v243, 0, v242, vcc\n" \
"v_mfma_f32_16x16x32_bf16 a[76:79], v[140:143], v[68:71], a[76:79]\n" \
"v_add_u32_e32 v39, v246, v248\n" \
"v_mov_b32_e32 v244, 0xffff0000\n" \
"v_mfma_f32_16x16x32_bf16 a[108:111], v[140:143], v[76:79], a[108:111]\n" \
"v_cmp_lt_u32_e32 vcc, v241, v236\n" \
"v_add_u32_e32 v246, 16, v214\n" \
"v_mfma_f32_16x16x32_bf16 a[108:111], v[144:147], v[80:83], a[108:111]\n" \
"v_cndmask_b32_e32 v245, 0, v244, vcc\n" \
"v_mov_b32_e32 v247, 0xffff\n" \
"s_waitcnt vmcnt(2) lgkmcnt(0)\n" \
"s_barrier\n" \
"v_mfma_f32_16x16x32_bf16 a[128:131], v[120:123], v[88:91], a[128:131]\n" \
"s_add_u32 m0, %[mA], 0x8000\n" \
"s_add_u32 s85, s62, 0x0\n" \
"v_mfma_f32_16x16x32_bf16 a[128:131], v[116:119], v[84:87], a[128:131]\n" \
"buffer_load_dwordx4 v22, s[64:67], s85 offen lds\n" \
"v_mfma_f32_16x16x32_bf16 a[160:163], v[116:119], v[92:95], a[160:163]\n" \
"ds_read_b128 v[56:59], v29 offset:16384\n" \
"ds_read_b128 v[52:55], v28 offset:16384\n" \
"v_mfma_f32_16x16x32_bf16 a[160:163], v[120:123], v[96:99], a[160:163]\n" \
"s_add_u32 m0, %[mA], 0x8400\n" \
"s_add_u32 s84, s62, 0x2000\n" \
"v_mfma_f32_16x16x32_bf16 a[192:195], v[120:123], v[104:107], a[192:195]\n" \
"buffer_load_dwordx4 v23, s[64:67], s84 offen lds\n" \
"v_mfma_f32_16x16x32_bf16 a[192:195], v[116:119], v[100:103], a[192:195]\n" \
"ds_read_b128 v[60:63], v28 offset:18432\n" \
"ds_read_b128 v[64:67], v29 offset:18432\n" \
"v_mfma_f32_16x16x32_bf16 a[224:227], v[116:119], v[108:111], a[224:227]\n" \
"ds_read_b64_tr_b16 v[116:117], v32 offset:0\n" \
"ds_read_b64_tr_b16 v[118:119], v32 offset:1024\n" \
"v_mfma_f32_16x16x32_bf16 a[224:227], v[120:123], v[112:115], a[224:227]\n" \
"s_add_u32 m0, %[mB], 0x8000\n" \
"s_add_u32 s85, s63, 0x0\n" \
"v_mfma_f32_16x16x32_bf16 a[132:135], v[128:131], v[88:91], a[132:135]\n" \
"buffer_load_dwordx4 v26, s[68:71], s85 offen lds\n" \
"v_mfma_f32_16x16x32_bf16 a[132:135], v[124:127], v[84:87], a[132:135]\n" \
"ds_read_b64_tr_b16 v[120:121], v32 offset:8192\n" \
"ds_read_b64_tr_b16 v[122:123], v32 offset:9216\n" \
"v_mfma_f32_16x16x32_bf16 a[164:167], v[124:127], v[92:95], a[164:167]\n" \
"ds_read_b128 v[72:75], v29 offset:20480\n" \
"ds_read_b128 v[68:71], v28 offset:20480\n" \
"v_mfma_f32_16x16x32_bf16 a[164:167], v[128:131], v[96:99], a[164:167]\n" \
"s_add_u32 m0, %[mB], 0xc000\n" \
"s_add_u32 s84, s63, 0x0\n" \
"v_mfma_f32_16x16x32_bf16 a[196:199], v[128:131], v[104:107], a[196:199]\n" \
"buffer_load_dwordx4 v26, s[72:75], s84 offen lds\n" \
"v_mfma_f32_16x16x32_bf16 a[196:199], v[124:127], v[100:103], a[196:199]\n" \
"ds_read_b128 v[76:79], v28 offset:22528\n" \
"ds_read_b128 v[80:83], v29 offset:22528\n" \
"v_mfma_f32_16x16x32_bf16 a[228:231], v[124:127], v[108:111], a[228:231]\n" \
"ds_read_b64_tr_b16 v[124:125], v33 offset:0\n" \
"ds_read_b64_tr_b16 v[126:127], v33 offset:1024\n" \
"v_mfma_f32_16x16x32_bf16 a[228:231], v[128:131], v[112:115], a[228:231]\n" \
"s_add_u32 m0, %[mA], 0x8800\n" \
"s_add_u32 s85, s62, 0x4000\n" \
"v_mfma_f32_16x16x32_bf16 a[136:139], v[136:139], v[88:91], a[136:139]\n" \
"buffer_load_dwordx4 v22, s[64:67], s85 offen lds\n" \
"v_mfma_f32_16x16x32_bf16 a[136:139], v[132:135], v[84:87], a[136:139]\n" \
"ds_read_b64_tr_b16 v[128:129], v33 offset:8192\n" \
"ds_read_b64_tr_b16 v[130:131], v33 offset:9216\n" \
"v_mfma_f32_16x16x32_bf16 a[168:171], v[132:135], v[92:95], a[168:171]\n" \
"ds_read_b128 v[200:203], v199 offset:0\n" \
"ds_read_b128 v[204:207], v199 offset:4096\n" \
"v_mfma_f32_16x16x32_bf16 a[168:171], v[136:139], v[96:99], a[168:171]\n" \
"s_add_u32 m0, %[mA], 0x8c00\n" \
"s_add_u32 s84, s62, 0x6000\n" \
"v_mfma_f32_16x16x32_bf16 a[200:203], v[136:139], v[104:107], a[200:203]\n" \
"buffer_load_dwordx4 v23, s[64:67], s84 offen lds\n" \
"v_mfma_f32_16x16x32_bf16 a[200:203], v[132:135], v[100:103], a[200:203]\n" \
"ds_read_b128 v[208:211], v199 offset:8192\n" \
"v_cmp_le_u32_e32 vcc, v246, v236\n" \
"s_nop 0\n" \
"v_mfma_f32_16x16x32_bf16 a[232:235], v[132:135], v[108:111], a[232:235]\n" \
"ds_read_b64_tr_b16 v[132:133], v34 offset:0\n" \
"ds_read_b64_tr_b16 v[134:135], v34 offset:1024\n" \
"v_mfma_f32_16x16x32_bf16 a[232:235], v[136:139], v[112:115], a[232:235]\n" \
"s_add_u32 m0, %[mB], 0x8400\n" \
"s_add_u32 s85, s63, 0x8000\n" \
"v_mfma_f32_16x16x32_bf16 a[140:143], v[144:147], v[88:91], a[140:143]\n" \
"buffer_load_dwordx4 v26, s[68:71], s85 offen lds\n" \
"v_mfma_f32_16x16x32_bf16 a[140:143], v[140:143], v[84:87], a[140:143]\n" \
"ds_read_b64_tr_b16 v[136:137], v34 offset:8192\n" \
"ds_read_b64_tr_b16 v[138:139], v34 offset:9216\n" \
"v_mfma_f32_16x16x32_bf16 a[172:175], v[140:143], v[92:95], a[172:175]\n" \
"v_cndmask_b32_e32 v248, 0, v247, vcc\n" \
"s_add_u32 s44, 0x4000, s89\n" \
"v_mfma_f32_16x16x32_bf16 a[172:175], v[144:147], v[96:99], a[172:175]\n" \
"s_add_u32 m0, %[mB], 0xc400\n" \
"s_add_u32 s84, s63, 0x8000\n" \
"v_mfma_f32_16x16x32_bf16 a[204:207], v[144:147], v[104:107], a[204:207]\n" \
"buffer_load_dwordx4 v26, s[72:75], s84 offen lds\n" \
"v_mfma_f32_16x16x32_bf16 a[204:207], v[140:143], v[100:103], a[204:207]\n" \
"v_mov_b32_e32 v249, 0xffff0000\n" \
"v_cmp_lt_u32_e32 vcc, v246, v236\n" \
"v_mfma_f32_16x16x32_bf16 a[236:239], v[140:143], v[108:111], a[236:239]\n" \
"ds_read_b64_tr_b16 v[140:141], v35 offset:0\n" \
"ds_read_b64_tr_b16 v[142:143], v35 offset:1024\n" \
"v_mfma_f32_16x16x32_bf16 a[236:239], v[144:147], v[112:115], a[236:239]\n" \
"s_add_u32 m0, %[mA], 0x9000\n" \
"s_add_u32 s85, s62, 0x8000\n" \
"v_mfma_f32_16x16x32_bf16 a[144:147], v[88:91], v[152:155], a[144:147]\n" \
"buffer_load_dwordx4 v22, s[64:67], s85 offen lds\n" \
"v_mfma_f32_16x16x32_bf16 a[144:147], v[84:87], v[148:151], a[144:147]\n" \
"ds_read_b64_tr_b16 v[144:145], v35 offset:8192\n" \
"ds_read_b64_tr_b16 v[146:147], v35 offset:9216\n" \
"v_mfma_f32_16x16x32_bf16 a[176:179], v[92:95], v[148:151], a[176:179]\n" \
"s_lshl_b32 s89, %[w], 8\n" \
"v_cndmask_b32_e32 v250, 0, v249, vcc\n" \
"v_mfma_f32_16x16x32_bf16 a[176:179], v[96:99], v[152:155], a[176:179]\n" \
"s_add_u32 m0, %[mA], 0x9400\n" \
"s_add_u32 s84, s62, 0xa000\n" \
"v_mfma_f32_16x16x32_bf16 a[208:211], v[104:107], v[152:155], a[208:211]\n" \
"buffer_load_dwordx4 v23, s[64:67], s84 offen lds\n" \
"v_mfma_f32_16x16x32_bf16 a[208:211], v[100:103], v[148:151], a[208:211]\n" \
"v_add_u32_e64 v251, 18, v214\n" \
"s_add_u32 s46, 0x27000, s89\n" \
"v_mfma_f32_16x16x32_bf16 a[240:243], v[108:111], v[148:151], a[240:243]\n" \
"ds_read_b64_tr_b16 v[148:149], v32 offset:16384\n" \
"ds_read_b64_tr_b16 v[150:151], v32 offset:17408\n" \
"v_mfma_f32_16x16x32_bf16 a[240:243], v[112:115], v[152:155], a[240:243]\n" \
"s_add_u32 m0, %[mB], 0x8800\n" \
"s_add_u32 s85, s63, 0x10000\n" \
"v_mfma_f32_16x16x32_bf16 a[148:151], v[88:91], v[160:163], a[148:151]\n" \
"buffer_load_dwordx4 v27, s[68:71], s85 offen lds\n" \
"v_mfma_f32_16x16x32_bf16 a[148:151], v[84:87], v[156:159], a[148:151]\n" \
"ds_read_b64_tr_b16 v[152:153], v32 offset:24576\n" \
"ds_read_b64_tr_b16 v[154:155], v32 offset:25600\n" \
"v_mfma_f32_16x16x32_bf16 a[180:183], v[92:95], v[156:159], a[180:183]\n" \
"v_mov_b32_e32 v218, 0xffff\n" \
"v_cmp_le_u32_e32 vcc, v251, v236\n" \
"v_mfma_f32_16x16x32_bf16 a[180:183], v[96:99], v[160:163], a[180:183]\n" \
"s_add_u32 m0, %[mB], 0xc800\n" \
"s_add_u32 s84, s63, 0x10000\n" \
"v_mfma_f32_16x16x32_bf16 a[212:215], v[104:107], v[160:163], a[212:215]\n" \
"buffer_load_dwordx4 v27, s[72:75], s84 offen lds\n" \
"v_mfma_f32_16x16x32_bf16 a[212:215], v[100:103], v[156:159], a[212:215]\n" \
"s_add_u32 s89, 0xffffc000, s101\n" \
"v_cndmask_b32_e32 v219, 0, v218, vcc\n" \
"v_mfma_f32_16x16x32_bf16 a[244:247], v[108:111], v[156:159], a[244:247]\n" \
"ds_read_b64_tr_b16 v[156:157], v33 offset:16384\n" \
"ds_read_b64_tr_b16 v[158:159], v33 offset:17408\n" \
"v_mfma_f32_16x16x32_bf16 a[244:247], v[112:115], v[160:163], a[244:247]\n" \
"ds_read_b64_tr_b16 v[160:161], v33 offset:24576\n" \
"ds_read_b64_tr_b16 v[162:163], v33 offset:25600\n" \
"v_mfma_f32_16x16x32_bf16 a[152:155], v[88:91], v[168:171], a[152:155]\n" \
"v_lshlrev_b32_e32 v224, 3, v212\n" \
"s_lshr_b32 s94, %[b], 5\n" \
"v_mfma_f32_16x16x32_bf16 a[152:155], v[84:87], v[164:167], a[152:155]\n" \
"s_cmp_lg_u32 s95, 0\n" \
"s_cselect_b32 s48, s89, s100\n" \
"v_mfma_f32_16x16x32_bf16 a[184:187], v[92:95], v[164:167], a[184:187]\n" \
"s_mov_b32 s96, 0xc000\n" \
"s_mov_b32 s97, 0x8000\n" \
"v_mfma_f32_16x16x32_bf16 a[184:187], v[96:99], v[168:171], a[184:187]\n" \
"v_mov_b32_e32 v221, 0xffff0000\n" \
"v_cmp_lt_u32_e32 vcc, v251, v236\n" \
"v_mfma_f32_16x16x32_bf16 a[216:219], v[104:107], v[168:171], a[216:219]\n" \
"v_lshrrev_b32_e32 v212, 4, v20\n" \
"v_cndmask_b32_e32 v216, 0, v221, vcc\n" \
"v_mfma_f32_16x16x32_bf16 a[216:219], v[100:103], v[164:167], a[216:219]\n" \
"s_cmp_lg_u32 s94, 0\n" \
"s_cselect_b32 s53, s96, 0x8000\n" \
"v_mfma_f32_16x16x32_bf16 a[248:251], v[108:111], v[164:167], a[248:251]\n" \
"ds_read_b64_tr_b16 v[164:165], v34 offset:16384\n" \
"ds_read_b64_tr_b16 v[166:167], v34 offset:17408\n" \
"v_mfma_f32_16x16x32_bf16 a[248:251], v[112:115], v[168:171], a[248:251]\n" \
"ds_read_b64_tr_b16 v[168:169], v34 offset:24576\n" \
"ds_read_b64_tr_b16 v[170:171], v34 offset:25600\n" \
"v_mfma_f32_16x16x32_bf16 a[156:159], v[88:91], v[176:179], a[156:159]\n" \
"ds_read_b128 v[88:91], v29 offset:0\n" \
"v_lshlrev_b32_e32 v223, 2, v236\n" \
"v_mfma_f32_16x16x32_bf16 a[156:159], v[84:87], v[172:175], a[156:159]\n" \
"ds_read_b128 v[84:87], v28 offset:0\n" \
"s_lshl_b32 s101, %[w], 9\n" \
"v_mfma_f32_16x16x32_bf16 a[188:191], v[92:95], v[172:175], a[188:191]\n" \
"ds_read_b128 v[92:95], v28 offset:2048\n" \
"v_mul_u32_u24_e32 v213, 0xe39, v212\n" \
"v_mfma_f32_16x16x32_bf16 a[188:191], v[96:99], v[176:179], a[188:191]\n" \
"ds_read_b128 v[96:99], v29 offset:2048\n" \
"v_or_b32_e32 v15, v219, v216\n" \
"s_nop 0\n" \
"v_mfma_f32_16x16x32_bf16 a[220:223], v[104:107], v[176:179], a[220:223]\n" \
"ds_read_b128 v[104:107], v29 offset:4096\n" \
"v_add_u32_e32 v18, 0x27000, v223\n" \
"v_mfma_f32_16x16x32_bf16 a[220:223], v[100:103], v[172:175], a[220:223]\n" \
"ds_read_b128 v[100:103], v28 offset:4096\n" \
"s_add_u32 s89, 0x27800, s101\n" \
"v_mfma_f32_16x16x32_bf16 a[252:255], v[108:111], v[172:175], a[252:255]\n" \
"ds_read_b64_tr_b16 v[172:173], v35 offset:16384\n" \
"ds_read_b64_tr_b16 v[174:175], v35 offset:17408\n" \
"v_mfma_f32_16x16x32_bf16 a[252:255], v[112:115], v[176:179], a[252:255]\n" \
"s_add_u32 m0, %[mA], 0x9800\n" \
"s_waitcnt lgkmcnt(12)\n" \
"s_nop 0\n" \
"v_mfma_f32_16x16x32_bf16 a[0:3], v[120:123], v[56:59], a[0:3]\n" \
"s_add_u32 s85, s62, 0xc000\n" \
"ds_read_b64_tr_b16 v[176:177], v35 offset:24576\n" \
"v_mfma_f32_16x16x32_bf16 a[0:3], v[116:119], v[52:55], a[0:3]\n" \
"buffer_load_dwordx4 v22, s[64:67], s85 offen lds\n" \
"v_mfma_f32_16x16x32_bf16 a[32:35], v[116:119], v[60:63], a[32:35]\n" \
"s_add_u32 m0, %[mA], 0x9c00\n" \
"s_add_u32 s84, s62, 0xe000\n" \
"v_mfma_f32_16x16x32_bf16 a[32:35], v[120:123], v[64:67], a[32:35]\n" \
"ds_read_b64_tr_b16 v[178:179], v35 offset:25600\n" \
"ds_read_b128 v[108:111], v28 offset:6144\n" \
"L_pf2:\n" \
"s_cbranch_execz L_pf6\n" \
"s_nop 0\n" \
"v_mfma_f32_16x16x32_bf16 a[64:67], v[120:123], v[72:75], a[64:67]\n" \
"buffer_load_dwordx4 v23, s[64:67], s84 offen lds\n" \
"v_mfma_f32_16x16x32_bf16 a[64:67], v[116:119], v[68:71], a[64:67]\n" \
"s_add_u32 m0, %[mB], 0x8c00\n" \
"s_add_u32 s85, s63, 0x18000\n" \
"v_mfma_f32_16x16x32_bf16 a[96:99], v[116:119], v[76:79], a[96:99]\n" \
"ds_read_b128 v[112:115], v29 offset:6144\n" \
"v_lshrrev_b32_e32 v214, 16, v213\n" \
"v_mfma_f32_16x16x32_bf16 a[96:99], v[120:123], v[80:83], a[96:99]\n" \
"buffer_load_dwordx4 v27, s[68:71], s85 offen lds\n" \
"v_mfma_f32_16x16x32_bf16 a[4:7], v[128:131], v[56:59], a[4:7]\n" \
"s_add_u32 m0, %[mB], 0xcc00\n" \
"s_add_u32 s84, s63, 0x18000\n" \
"v_mfma_f32_16x16x32_bf16 a[4:7], v[124:127], v[52:55], a[4:7]\n" \
"s_cmp_lg_u32 s94, 0\n" \
"s_cselect_b32 s98, s97, 0x4000\n" \
"v_mfma_f32_16x16x32_bf16 a[36:39], v[124:127], v[60:63], a[36:39]\n" \
"buffer_load_dwordx4 v27, s[72:75], s84 offen lds\n" \
"v_mfma_f32_16x16x32_bf16 a[36:39], v[128:131], v[64:67], a[36:39]\n" \
"v_add_u32_e32 v222, 64, v212\n" \
"v_add_u32_e32 v19, s89, v224\n" \
"v_mfma_f32_16x16x32_bf16 a[68:71], v[128:131], v[72:75], a[68:71]\n" \
"v_mul_u32_u24_e32 v216, 18, v214\n" \
"s_and_b32 s94, 7, %[b]\n" \
"v_mfma_f32_16x16x32_bf16 a[68:71], v[124:127], v[68:71], a[68:71]\n" \
"buffer_store_dwordx4 v[200:203], v20, s[80:83], s57 offen sc1\n" \
"v_mfma_f32_16x16x32_bf16 a[100:103], v[124:127], v[76:79], a[100:103]\n" \
"v_mul_u32_u24_e32 v223, 0xe39, v222\n" \
"v_sub_u32_e32 v217, v212, v216\n" \
"v_mfma_f32_16x16x32_bf16 a[100:103], v[128:131], v[80:83], a[100:103]\n" \
"s_lshl_b32 s95, s94, 5\n" \
"v_lshrrev_b32_e32 v224, 16, v223\n" \
"v_mfma_f32_16x16x32_bf16 a[8:11], v[136:139], v[56:59], a[8:11]\n" \
"v_add_u32_e32 v233, 0x80, v212\n" \
"s_add_u32 s96, 32, s95\n" \
"v_mfma_f32_16x16x32_bf16 a[8:11], v[132:135], v[52:55], a[8:11]\n" \
"v_lshlrev_b32_e32 v218, 4, v217\n" \
"v_mul_u32_u24_e32 v226, 18, v224\n" \
"v_mfma_f32_16x16x32_bf16 a[40:43], v[132:135], v[60:63], a[40:43]\n" \
"buffer_store_dwordx4 v[204:207], v20, s[80:83], s56 offen sc1\n" \
"v_mfma_f32_16x16x32_bf16 a[40:43], v[136:139], v[64:67], a[40:43]\n" \
"v_mul_u32_u24_e32 v234, 0xe39, v233\n" \
"v_or_b32_e32 v13, v243, v245\n" \
"v_mfma_f32_16x16x32_bf16 a[72:75], v[136:139], v[72:75], a[72:75]\n" \
"v_add_u32_e32 v219, s96, v218\n" \
"v_sub_u32_e32 v227, v222, v226\n" \
"v_mfma_f32_16x16x32_bf16 a[72:75], v[132:135], v[68:71], a[72:75]\n" \
"v_lshrrev_b32_e32 v235, 16, v234\n" \
"v_add_u32_e32 v244, 0xc0, v212\n" \
"v_mfma_f32_16x16x32_bf16 a[104:107], v[132:135], v[76:79], a[104:107]\n" \
"v_or_b32_e32 v12, v238, v240\n" \
"v_and_b32_e32 v220, 0xff, v219\n" \
"v_mfma_f32_16x16x32_bf16 a[104:107], v[136:139], v[80:83], a[104:107]\n" \
"v_cmp_gt_u32_e32 vcc, 16, v217\n" \
"v_lshlrev_b32_e32 v228, 4, v227\n" \
"v_mfma_f32_16x16x32_bf16 a[12:15], v[144:147], v[56:59], a[12:15]\n" \
"buffer_store_dwordx4 v[208:211], v20, s[80:83], s58 offen sc1\n" \
"v_mfma_f32_16x16x32_bf16 a[12:15], v[140:143], v[52:55], a[12:15]\n" \
"v_mul_u32_u24_e32 v237, 18, v235\n" \
"v_mul_u32_u24_e32 v245, 0xe39, v244\n" \
"v_mfma_f32_16x16x32_bf16 a[44:47], v[140:143], v[60:63], a[44:47]\n" \
"v_or_b32_e32 v14, v248, v250\n" \
"v_cndmask_b32_e32 v221, v218, v220, vcc\n" \
"v_mfma_f32_16x16x32_bf16 a[44:47], v[144:147], v[64:67], a[44:47]\n" \
"v_add_u32_e32 v229, s96, v228\n" \
"v_sub_u32_e32 v238, v233, v237\n" \
"v_mfma_f32_16x16x32_bf16 a[76:79], v[144:147], v[72:75], a[76:79]\n" \
"v_lshrrev_b32_e32 v246, 16, v245\n" \
"v_add_u32_e32 v219, 0x100, v212\n" \
"v_mfma_f32_16x16x32_bf16 a[76:79], v[140:143], v[68:71], a[76:79]\n" \
"v_mul_u32_u24_e32 v215, 0x480, v214\n" \
"v_and_b32_e32 v230, 0xff, v229\n" \
"v_mfma_f32_16x16x32_bf16 a[108:111], v[140:143], v[76:79], a[108:111]\n" \
"s_mov_b32 m0, %[mS]\n" \
"s_mov_b32 s86, s10\n" \
"v_mfma_f32_16x16x32_bf16 a[108:111], v[144:147], v[80:83], a[108:111]\n" \
"s_mov_b32 s87, s11\n" \
"v_cmp_gt_u32_e32 vcc, 16, v227\n" \
"v_mfma_f32_16x16x32_bf16 a[16:19], v[56:59], v[152:155], a[16:19]\n" \
"v_lshlrev_b32_e32 v239, 4, v238\n" \
"v_mul_u32_u24_e32 v248, 18, v246\n" \
"v_mfma_f32_16x16x32_bf16 a[16:19], v[52:55], v[148:151], a[16:19]\n" \
"v_mul_u32_u24_e32 v217, 0xe39, v219\n" \
"v_add_u32_e32 v0, v215, v221\n" \
"v_mfma_f32_16x16x32_bf16 a[48:51], v[60:63], v[148:151], a[48:51]\n" \
"v_cndmask_b32_e32 v231, v228, v230, vcc\n" \
"v_add_u32_e32 v240, s96, v239\n" \
"v_mfma_f32_16x16x32_bf16 a[48:51], v[64:67], v[152:155], a[48:51]\n" \
"v_sub_u32_e32 v249, v244, v248\n" \
"v_lshrrev_b32_e32 v220, 16, v217\n" \
"v_mfma_f32_16x16x32_bf16 a[80:83], v[72:75], v[152:155], a[80:83]\n" \
"v_add_u32_e32 v229, 0x140, v212\n" \
"v_mul_u32_u24_e32 v225, 0x480, v224\n" \
"s_mov_b32 exec_lo, 0xfffc0000\n" \
"s_mov_b32 exec_hi, 0xf\n" \
"global_load_lds_dword v188, s[86:87]\n" \
"s_mov_b64 exec, -1\n" \
"v_mfma_f32_16x16x32_bf16 a[80:83], v[68:71], v[148:151], a[80:83]\n" \
"v_and_b32_e32 v241, 0xff, v240\n" \
"v_cmp_gt_u32_e32 vcc, 16, v238\n" \
"v_mfma_f32_16x16x32_bf16 a[112:115], v[76:79], v[148:151], a[112:115]\n" \
"v_lshlrev_b32_e32 v250, 4, v249\n" \
"v_mul_u32_u24_e32 v215, 18, v220\n" \
"v_mfma_f32_16x16x32_bf16 a[112:115], v[80:83], v[152:155], a[112:115]\n" \
"v_mul_u32_u24_e32 v227, 0xe39, v229\n" \
"v_add_u32_e32 v232, v225, v231\n" \
"v_mfma_f32_16x16x32_bf16 a[20:23], v[56:59], v[160:163], a[20:23]\n" \
"v_cndmask_b32_e32 v242, v239, v241, vcc\n" \
"v_add_u32_e32 v251, s96, v250\n" \
"v_mfma_f32_16x16x32_bf16 a[20:23], v[52:55], v[156:159], a[20:23]\n" \
"v_sub_u32_e32 v221, v219, v215\n" \
"v_lshrrev_b32_e32 v230, 16, v227\n" \
"v_mfma_f32_16x16x32_bf16 a[52:55], v[60:63], v[156:159], a[52:55]\n" \
"v_add_u32_e32 v240, 0x180, v212\n" \
"v_mul_u32_u24_e32 v236, 0x480, v235\n" \
"v_mfma_f32_16x16x32_bf16 a[52:55], v[64:67], v[160:163], a[52:55]\n" \
"v_and_b32_e32 v213, 0xff, v251\n" \
"v_cmp_gt_u32_e32 vcc, 16, v249\n" \
"v_mfma_f32_16x16x32_bf16 a[84:87], v[72:75], v[160:163], a[84:87]\n" \
"v_lshlrev_b32_e32 v223, 4, v221\n" \
"v_mul_u32_u24_e32 v225, 18, v230\n" \
"v_mfma_f32_16x16x32_bf16 a[84:87], v[68:71], v[156:159], a[84:87]\n" \
"v_mul_u32_u24_e32 v238, 0xe39, v240\n" \
"v_add_u32_e32 v1, 0xfffffc00, v232\n" \
"v_mfma_f32_16x16x32_bf16 a[116:119], v[76:79], v[156:159], a[116:119]\n" \
"v_add_u32_e32 v243, v236, v242\n" \
"v_cndmask_b32_e32 v214, v250, v213, vcc\n" \
"v_mfma_f32_16x16x32_bf16 a[116:119], v[80:83], v[160:163], a[116:119]\n" \
"v_add_u32_e32 v224, s96, v223\n" \
"s_waitcnt lgkmcnt(0)\n" \
"v_mfma_f32_16x16x32_bf16 a[24:27], v[56:59], v[168:171], a[24:27]\n" \
"v_sub_u32_e32 v231, v229, v225\n" \
"v_lshrrev_b32_e32 v241, 16, v238\n" \
"v_mfma_f32_16x16x32_bf16 a[24:27], v[52:55], v[164:167], a[24:27]\n" \
"v_add_u32_e32 v251, 0x1c0, v212\n" \
"v_mul_u32_u24_e32 v247, 0x480, v246\n" \
"v_mfma_f32_16x16x32_bf16 a[56:59], v[60:63], v[164:167], a[56:59]\n" \
"v_and_b32_e32 v222, 0xff, v224\n" \
"v_cmp_gt_u32_e32 vcc, 16, v221\n" \
"v_mfma_f32_16x16x32_bf16 a[56:59], v[64:67], v[168:171], a[56:59]\n" \
"v_lshlrev_b32_e32 v232, 4, v231\n" \
"v_mul_u32_u24_e32 v236, 18, v241\n" \
"v_mfma_f32_16x16x32_bf16 a[88:91], v[72:75], v[168:171], a[88:91]\n" \
"v_mul_u32_u24_e32 v249, 0xe39, v251\n" \
"v_add_u32_e32 v2, 0xfffff800, v243\n" \
"v_mfma_f32_16x16x32_bf16 a[88:91], v[68:71], v[164:167], a[88:91]\n" \
"v_add_u32_e32 v216, v247, v214\n" \
"v_cndmask_b32_e32 v226, v223, v222, vcc\n" \
"v_mfma_f32_16x16x32_bf16 a[120:123], v[76:79], v[164:167], a[120:123]\n" \
"v_add_u32_e32 v234, s96, v232\n" \
"v_sub_u32_e32 v242, v240, v236\n" \
"v_mfma_f32_16x16x32_bf16 a[120:123], v[80:83], v[168:171], a[120:123]\n" \
"v_lshrrev_b32_e32 v213, 16, v249\n" \
"v_add_u32_e32 v224, 0x200, v212\n" \
"v_mfma_f32_16x16x32_bf16 a[28:31], v[56:59], v[176:179], a[28:31]\n" \
"s_add_u32 s62, s22, 0x300\n" \
"s_and_b32 s62, s62, 0x3ff\n" \
"v_mfma_f32_16x16x32_bf16 a[28:31], v[52:55], v[172:175], a[28:31]\n" \
"s_lshl_b32 s63, s62, 12\n" \
"v_mul_u32_u24_e32 v218, 0x480, v220\n" \
"v_mfma_f32_16x16x32_bf16 a[60:63], v[60:63], v[172:175], a[60:63]\n" \
"v_and_b32_e32 v235, 0xff, v234\n" \
"v_cmp_gt_u32_e32 vcc, 16, v231\n" \
"v_mfma_f32_16x16x32_bf16 a[60:63], v[64:67], v[176:179], a[60:63]\n" \
"v_lshlrev_b32_e32 v243, 4, v242\n" \
"v_mul_u32_u24_e32 v247, 18, v213\n" \
"v_mfma_f32_16x16x32_bf16 a[92:95], v[72:75], v[176:179], a[92:95]\n" \
"v_mul_u32_u24_e32 v221, 0xe39, v224\n" \
"v_add_u32_e32 v3, 0xfffff400, v216\n" \
"v_mfma_f32_16x16x32_bf16 a[92:95], v[68:71], v[172:175], a[92:95]\n" \
"v_add_u32_e32 v4, v218, v226\n" \
"v_cndmask_b32_e32 v233, v232, v235, vcc\n" \
"v_mfma_f32_16x16x32_bf16 a[124:127], v[76:79], v[172:175], a[124:127]\n" \
"v_add_u32_e32 v245, s96, v243\n" \
"v_sub_u32_e32 v214, v251, v247\n" \
"v_mfma_f32_16x16x32_bf16 a[124:127], v[80:83], v[176:179], a[124:127]\n" \
"v_lshrrev_b32_e32 v222, 16, v221\n" \
"v_and_b32_e32 v246, 0xff, v245\n" \
"s_waitcnt vmcnt(4)\n" \
"s_barrier\n" \
"v_mfma_f32_16x16x32_bf16 a[144:147], v[88:91], v[152:155], a[144:147]\n" \
"s_add_u32 m0, %[mA], 0x0\n" \
"s_add_u32 s85, s62, 0x0\n" \
"v_mfma_f32_16x16x32_bf16 a[144:147], v[84:87], v[148:151], a[144:147]\n" \
"buffer_load_dwordx4 v22, s[64:67], s85 offen lds\n" \
"v_mfma_f32_16x16x32_bf16 a[176:179], v[92:95], v[148:151], a[176:179]\n" \
"ds_read_b128 v[56:59], v29 offset:49152\n" \
"ds_read_b128 v[52:55], v28 offset:49152\n" \
"v_mfma_f32_16x16x32_bf16 a[176:179], v[96:99], v[152:155], a[176:179]\n" \
"s_add_u32 m0, %[mA], 0x400\n" \
"s_add_u32 s84, s62, 0x2000\n" \
"v_mfma_f32_16x16x32_bf16 a[208:211], v[104:107], v[152:155], a[208:211]\n" \
"buffer_load_dwordx4 v23, s[64:67], s84 offen lds\n" \
"v_mfma_f32_16x16x32_bf16 a[208:211], v[100:103], v[148:151], a[208:211]\n" \
"ds_read_b128 v[60:63], v28 offset:51200\n" \
"ds_read_b128 v[64:67], v29 offset:51200\n" \
"v_mfma_f32_16x16x32_bf16 a[240:243], v[108:111], v[148:151], a[240:243]\n" \
"ds_read_b64_tr_b16 v[148:149], v32 offset:49152\n" \
"ds_read_b64_tr_b16 v[150:151], v32 offset:50176\n" \
"v_mfma_f32_16x16x32_bf16 a[240:243], v[112:115], v[152:155], a[240:243]\n" \
"s_add_u32 m0, %[mB], 0x0\n" \
"s_add_u32 s85, s63, 0x0\n" \
"v_mfma_f32_16x16x32_bf16 a[148:151], v[88:91], v[160:163], a[148:151]\n" \
"buffer_load_dwordx4 v26, s[68:71], s85 offen lds\n" \
"v_mfma_f32_16x16x32_bf16 a[148:151], v[84:87], v[156:159], a[148:151]\n" \
"ds_read_b64_tr_b16 v[152:153], v32 offset:57344\n" \
"ds_read_b64_tr_b16 v[154:155], v32 offset:58368\n" \
"v_mfma_f32_16x16x32_bf16 a[180:183], v[92:95], v[156:159], a[180:183]\n" \
"ds_read_b128 v[72:75], v29 offset:53248\n" \
"ds_read_b128 v[68:71], v28 offset:53248\n" \
"v_mfma_f32_16x16x32_bf16 a[180:183], v[96:99], v[160:163], a[180:183]\n" \
"s_add_u32 m0, %[mB], 0x4000\n" \
"s_add_u32 s84, s63, 0x0\n" \
"s_nop 0\n" \
"v_mfma_f32_16x16x32_bf16 a[212:215], v[104:107], v[160:163], a[212:215]\n" \
"buffer_load_dwordx4 v26, s[72:75], s84 offen lds\n" \
"v_mfma_f32_16x16x32_bf16 a[212:215], v[100:103], v[156:159], a[212:215]\n" \
"ds_read_b128 v[76:79], v28 offset:55296\n" \
"ds_read_b128 v[80:83], v29 offset:55296\n" \
"v_mfma_f32_16x16x32_bf16 a[244:247], v[108:111], v[156:159], a[244:247]\n" \
"ds_read_b64_tr_b16 v[156:157], v33 offset:49152\n" \
"ds_read_b64_tr_b16 v[158:159], v33 offset:50176\n" \
"v_mfma_f32_16x16x32_bf16 a[244:247], v[112:115], v[160:163], a[244:247]\n" \
"s_add_u32 m0, %[mA], 0x800\n" \
"s_add_u32 s85, s62, 0x4000\n" \
"v_mfma_f32_16x16x32_bf16 a[152:155], v[88:91], v[168:171], a[152:155]\n" \
"buffer_load_dwordx4 v22, s[64:67], s85 offen lds\n" \
"v_mfma_f32_16x16x32_bf16 a[152:155], v[84:87], v[164:167], a[152:155]\n" \
"ds_read_b64_tr_b16 v[160:161], v33 offset:57344\n" \
"ds_read_b64_tr_b16 v[162:163], v33 offset:58368\n" \
"v_mfma_f32_16x16x32_bf16 a[184:187], v[92:95], v[164:167], a[184:187]\n" \
"v_cmp_gt_u32_e32 vcc, 16, v242\n" \
"v_lshlrev_b32_e32 v216, 4, v214\n" \
"v_mfma_f32_16x16x32_bf16 a[184:187], v[96:99], v[168:171], a[184:187]\n" \
"s_add_u32 m0, %[mA], 0xc00\n" \
"s_add_u32 s84, s62, 0x6000\n" \
"v_mfma_f32_16x16x32_bf16 a[216:219], v[104:107], v[168:171], a[216:219]\n" \
"buffer_load_dwordx4 v23, s[64:67], s84 offen lds\n" \
"v_mfma_f32_16x16x32_bf16 a[216:219], v[100:103], v[164:167], a[216:219]\n" \
"v_mul_u32_u24_e32 v218, 18, v222\n" \
"v_cndmask_b32_e32 v244, v243, v246, vcc\n" \
"v_mfma_f32_16x16x32_bf16 a[248:251], v[108:111], v[164:167], a[248:251]\n" \
"ds_read_b64_tr_b16 v[164:165], v34 offset:49152\n" \
"ds_read_b64_tr_b16 v[166:167], v34 offset:50176\n" \
"v_mfma_f32_16x16x32_bf16 a[248:251], v[112:115], v[168:171], a[248:251]\n" \
"s_add_u32 m0, %[mB], 0x400\n" \
"s_add_u32 s85, s63, 0x8000\n" \
"v_mfma_f32_16x16x32_bf16 a[156:159], v[88:91], v[176:179], a[156:159]\n" \
"buffer_load_dwordx4 v26, s[68:71], s85 offen lds\n" \
"v_mfma_f32_16x16x32_bf16 a[156:159], v[84:87], v[172:175], a[156:159]\n" \
"ds_read_b64_tr_b16 v[168:169], v34 offset:57344\n" \
"ds_read_b64_tr_b16 v[170:171], v34 offset:58368\n" \
"v_mfma_f32_16x16x32_bf16 a[188:191], v[92:95], v[172:175], a[188:191]\n" \
"v_add_u32_e32 v217, s96, v216\n" \
"v_sub_u32_e32 v226, v224, v218\n" \
"v_mfma_f32_16x16x32_bf16 a[188:191], v[96:99], v[176:179], a[188:191]\n" \
"s_add_u32 m0, %[mB], 0x4400\n" \
"s_add_u32 s84, s63, 0x8000\n" \
"v_mfma_f32_16x16x32_bf16 a[220:223], v[104:107], v[176:179], a[220:223]\n" \
"buffer_load_dwordx4 v26, s[72:75], s84 offen lds\n" \
"v_mfma_f32_16x16x32_bf16 a[220:223], v[100:103], v[172:175], a[220:223]\n" \
"v_mul_u32_u24_e32 v228, 0x480, v230\n" \
"v_and_b32_e32 v220, 0xff, v217\n" \
"v_mfma_f32_16x16x32_bf16 a[252:255], v[108:111], v[172:175], a[252:255]\n" \
"ds_read_b64_tr_b16 v[172:173], v35 offset:49152\n" \
"ds_read_b64_tr_b16 v[174:175], v35 offset:50176\n" \
"v_mfma_f32_16x16x32_bf16 a[252:255], v[112:115], v[176:179], a[252:255]\n" \
"s_add_u32 m0, %[mA], 0x1000\n" \
"s_add_u32 s85, s62, 0x8000\n" \
"v_mfma_f32_16x16x32_bf16 a[128:131], v[120:123], v[88:91], a[128:131]\n" \
"buffer_load_dwordx4 v22, s[64:67], s85 offen lds\n" \
"v_mfma_f32_16x16x32_bf16 a[128:131], v[116:119], v[84:87], a[128:131]\n" \
"ds_read_b64_tr_b16 v[176:177], v35 offset:57344\n" \
"ds_read_b64_tr_b16 v[178:179], v35 offset:58368\n" \
"v_mfma_f32_16x16x32_bf16 a[160:163], v[116:119], v[92:95], a[160:163]\n" \
"v_cmp_gt_u32_e32 vcc, 16, v214\n" \
"v_lshlrev_b32_e32 v227, 4, v226\n" \
"v_mfma_f32_16x16x32_bf16 a[160:163], v[120:123], v[96:99], a[160:163]\n" \
"s_add_u32 m0, %[mA], 0x1400\n" \
"s_add_u32 s84, s62, 0xa000\n" \
"v_mfma_f32_16x16x32_bf16 a[192:195], v[120:123], v[104:107], a[192:195]\n" \
"buffer_load_dwordx4 v23, s[64:67], s84 offen lds\n" \
"v_mfma_f32_16x16x32_bf16 a[192:195], v[116:119], v[100:103], a[192:195]\n" \
"v_cndmask_b32_e32 v219, v216, v220, vcc\n" \
"v_add_u32_e32 v230, s96, v227\n" \
"v_mfma_f32_16x16x32_bf16 a[224:227], v[116:119], v[108:111], a[224:227]\n" \
"ds_read_b64_tr_b16 v[116:117], v32 offset:32768\n" \
"ds_read_b64_tr_b16 v[118:119], v32 offset:33792\n" \
"v_mfma_f32_16x16x32_bf16 a[224:227], v[120:123], v[112:115], a[224:227]\n" \
"s_add_u32 m0, %[mB], 0x800\n" \
"s_add_u32 s85, s63, 0x10000\n" \
"v_mfma_f32_16x16x32_bf16 a[132:135], v[128:131], v[88:91], a[132:135]\n" \
"buffer_load_dwordx4 v27, s[68:71], s85 offen lds\n" \
"v_mfma_f32_16x16x32_bf16 a[132:135], v[124:127], v[84:87], a[132:135]\n" \
"ds_read_b64_tr_b16 v[120:121], v32 offset:40960\n" \
"ds_read_b64_tr_b16 v[122:123], v32 offset:41984\n" \
"v_mfma_f32_16x16x32_bf16 a[164:167], v[124:127], v[92:95], a[164:167]\n" \
"v_and_b32_e32 v229, 0xff, v230\n" \
"v_cmp_gt_u32_e32 vcc, 16, v226\n" \
"v_mfma_f32_16x16x32_bf16 a[164:167], v[128:131], v[96:99], a[164:167]\n" \
"s_add_u32 m0, %[mB], 0x4800\n" \
"s_add_u32 s84, s63, 0x10000\n" \
"v_mfma_f32_16x16x32_bf16 a[196:199], v[128:131], v[104:107], a[196:199]\n" \
"buffer_load_dwordx4 v27, s[72:75], s84 offen lds\n" \
"v_mfma_f32_16x16x32_bf16 a[196:199], v[124:127], v[100:103], a[196:199]\n" \
"v_mul_u32_u24_e32 v250, 0x480, v213\n" \
"v_cndmask_b32_e32 v225, v227, v229, vcc\n" \
"v_mfma_f32_16x16x32_bf16 a[228:231], v[124:127], v[108:111], a[228:231]\n" \
"ds_read_b64_tr_b16 v[124:125], v33 offset:32768\n" \
"ds_read_b64_tr_b16 v[126:127], v33 offset:33792\n" \
"v_mfma_f32_16x16x32_bf16 a[228:231], v[128:131], v[112:115], a[228:231]\n" \
"ds_read_b64_tr_b16 v[128:129], v33 offset:40960\n" \
"ds_read_b64_tr_b16 v[130:131], v33 offset:41984\n" \
"v_mfma_f32_16x16x32_bf16 a[136:139], v[136:139], v[88:91], a[136:139]\n" \
"v_and_b32_e64 v249, 2, v212\n" \
"v_mov_b32_e32 v213, 0x100\n" \
"v_mfma_f32_16x16x32_bf16 a[136:139], v[132:135], v[84:87], a[136:139]\n" \
"v_mov_b32_e32 v251, s95\n" \
"v_cmp_ne_u32_e32 vcc, 0, v249\n" \
"v_mfma_f32_16x16x32_bf16 a[168:171], v[132:135], v[92:95], a[168:171]\n" \
"s_mov_b32 s76, s8\n" \
"v_cndmask_b32_e32 v247, v251, v213, vcc\n" \
"v_mfma_f32_16x16x32_bf16 a[168:171], v[136:139], v[96:99], a[168:171]\n" \
"s_and_b32 s77, s9, 0xffff\n" \
"s_mov_b32 s78, 0x240000\n" \
"v_mfma_f32_16x16x32_bf16 a[200:203], v[136:139], v[104:107], a[200:203]\n" \
"v_xor_b32_e32 v37, 0x20, v190\n" \
"v_xor_b32_e32 v38, 0x40, v190\n" \
"v_mfma_f32_16x16x32_bf16 a[200:203], v[132:135], v[100:103], a[200:203]\n" \
"v_add_u32_e32 v48, s60, v28\n" \
"v_add_u32_e32 v49, s60, v29\n" \
"v_mfma_f32_16x16x32_bf16 a[232:235], v[132:135], v[108:111], a[232:235]\n" \
"ds_read_b64_tr_b16 v[132:133], v34 offset:32768\n" \
"ds_read_b64_tr_b16 v[134:135], v34 offset:33792\n" \
"v_mfma_f32_16x16x32_bf16 a[232:235], v[136:139], v[112:115], a[232:235]\n" \
"ds_read_b64_tr_b16 v[136:137], v34 offset:40960\n" \
"ds_read_b64_tr_b16 v[138:139], v34 offset:41984\n" \
"v_mfma_f32_16x16x32_bf16 a[140:143], v[144:147], v[88:91], a[140:143]\n" \
"ds_read_b128 v[88:91], v29 offset:32768\n" \
"v_xor_b32_e64 v36, 0x0, v190\n" \
"v_mfma_f32_16x16x32_bf16 a[140:143], v[140:143], v[84:87], a[140:143]\n" \
"ds_read_b128 v[84:87], v28 offset:32768\n" \
"v_add_u32_e64 v237, v228, v233\n" \
"v_mfma_f32_16x16x32_bf16 a[172:175], v[140:143], v[92:95], a[172:175]\n" \
"ds_read_b128 v[92:95], v28 offset:34816\n" \
"s_lshl_b32 s97, %[w], 4\n" \
"v_mfma_f32_16x16x32_bf16 a[172:175], v[144:147], v[96:99], a[172:175]\n" \
"ds_read_b128 v[96:99], v29 offset:34816\n" \
"v_lshrrev_b32_e32 v234, 2, v212\n" \
"v_mfma_f32_16x16x32_bf16 a[204:207], v[144:147], v[104:107], a[204:207]\n" \
"ds_read_b128 v[104:107], v29 offset:36864\n" \
"v_add_u32_e32 v5, 0xfffffc00, v237\n" \
"v_mfma_f32_16x16x32_bf16 a[204:207], v[140:143], v[100:103], a[204:207]\n" \
"ds_read_b128 v[100:103], v28 offset:36864\n" \
"v_add_u32_e64 v231, s97, v234\n" \
"v_mfma_f32_16x16x32_bf16 a[236:239], v[140:143], v[108:111], a[236:239]\n" \
"ds_read_b64_tr_b16 v[140:141], v35 offset:32768\n" \
"ds_read_b64_tr_b16 v[142:143], v35 offset:33792\n" \
"v_mfma_f32_16x16x32_bf16 a[236:239], v[144:147], v[112:115], a[236:239]\n" \
"s_add_u32 m0, %[mA], 0x1800\n" \
"s_waitcnt lgkmcnt(12)\n" \
"s_nop 0\n" \
"v_mfma_f32_16x16x32_bf16 a[16:19], v[56:59], v[152:155], a[16:19]\n" \
"s_add_u32 s85, s62, 0xc000\n" \
"ds_read_b64_tr_b16 v[144:145], v35 offset:40960\n" \
"v_mfma_f32_16x16x32_bf16 a[16:19], v[52:55], v[148:151], a[16:19]\n" \
"buffer_load_dwordx4 v22, s[64:67], s85 offen lds\n" \
"v_mfma_f32_16x16x32_bf16 a[48:51], v[60:63], v[148:151], a[48:51]\n" \
"s_add_u32 m0, %[mA], 0x1c00\n" \
"s_add_u32 s84, s62, 0xe000\n" \
"v_mfma_f32_16x16x32_bf16 a[48:51], v[64:67], v[152:155], a[48:51]\n" \
"ds_read_b64_tr_b16 v[146:147], v35 offset:41984\n" \
"ds_read_b128 v[108:111], v28 offset:38912\n" \
"v_mfma_f32_16x16x32_bf16 a[80:83], v[72:75], v[152:155], a[80:83]\n" \
"buffer_load_dwordx4 v23, s[64:67], s84 offen lds\n" \
"v_mfma_f32_16x16x32_bf16 a[80:83], v[68:71], v[148:151], a[80:83]\n" \
"s_add_u32 m0, %[mB], 0xc00\n" \
"s_add_u32 s85, s63, 0x18000\n" \
"v_mfma_f32_16x16x32_bf16 a[112:115], v[76:79], v[148:151], a[112:115]\n" \
"ds_read_b128 v[112:115], v29 offset:38912\n" \
"v_lshrrev_b32_e32 v235, 5, v231\n" \
"v_mfma_f32_16x16x32_bf16 a[112:115], v[80:83], v[152:155], a[112:115]\n" \
"buffer_load_dwordx4 v27, s[68:71], s85 offen lds\n" \
"v_mfma_f32_16x16x32_bf16 a[20:23], v[56:59], v[160:163], a[20:23]\n" \
"s_add_u32 m0, %[mB], 0x4c00\n" \
"s_add_u32 s84, s63, 0x18000\n" \
"v_mfma_f32_16x16x32_bf16 a[20:23], v[52:55], v[156:159], a[20:23]\n" \
"v_lshrrev_b32_e32 v237, 4, v231\n" \
"v_mul_u32_u24_e32 v239, 0x480, v241\n" \
"v_mfma_f32_16x16x32_bf16 a[52:55], v[60:63], v[156:159], a[52:55]\n" \
"buffer_load_dwordx4 v27, s[72:75], s84 offen lds\n" \
"v_mfma_f32_16x16x32_bf16 a[52:55], v[64:67], v[160:163], a[52:55]\n" \
"v_lshlrev_b32_e32 v232, 4, v235\n" \
"v_and_b32_e32 v228, 12, v231\n" \
"v_mfma_f32_16x16x32_bf16 a[84:87], v[72:75], v[160:163], a[84:87]\n" \
"v_and_b32_e32 v238, 1, v237\n" \
"v_add_u32_e32 v233, v232, v228\n" \
"v_mfma_f32_16x16x32_bf16 a[84:87], v[68:71], v[156:159], a[84:87]\n" \
"v_lshlrev_b32_e32 v241, 1, v238\n" \
"v_lshrrev_b32_e32 v236, 1, v231\n" \
"v_mfma_f32_16x16x32_bf16 a[116:119], v[76:79], v[156:159], a[116:119]\n" \
"v_add_u32_e32 v240, v233, v241\n" \
"v_and_b32_e32 v245, 1, v236\n" \
"v_mfma_f32_16x16x32_bf16 a[116:119], v[80:83], v[160:163], a[116:119]\n" \
"s_mov_b32 m0, %[mS]\n" \
"s_mov_b32 s86, s8\n" \
"v_mfma_f32_16x16x32_bf16 a[24:27], v[56:59], v[168:171], a[24:27]\n" \
"s_mov_b32 s87, s9\n" \
"v_add_u32_e32 v242, v240, v245\n" \
"v_mfma_f32_16x16x32_bf16 a[24:27], v[52:55], v[164:167], a[24:27]\n" \
"v_add_u32_e32 v248, v239, v244\n" \
"v_mul_u32_u24_e32 v246, 18, v242\n" \
"v_mfma_f32_16x16x32_bf16 a[56:59], v[60:63], v[164:167], a[56:59]\n" \
"v_add_u32_e32 v243, 16, v246\n" \
"v_and_b32_e32 v239, 1, v231\n" \
"v_mfma_f32_16x16x32_bf16 a[56:59], v[64:67], v[168:171], a[56:59]\n" \
"v_add_u32_e32 v6, 0xfffff800, v248\n" \
"v_add_u32_e64 v244, v243, v239\n" \
"v_mfma_f32_16x16x32_bf16 a[88:91], v[72:75], v[168:171], a[88:91]\n" \
"v_mul_u32_u24_e32 v248, 0x480, v244\n" \
"v_add_u32_e64 v215, v250, v219\n" \
"v_mfma_f32_16x16x32_bf16 a[88:91], v[68:71], v[164:167], a[88:91]\n" \
"v_add_u32_e32 v217, v248, v247\n" \
"v_and_b32_e32 v214, 16, v20\n" \
"s_mov_b32 exec_lo, 0x0\n" \
"s_mov_b32 exec_hi, 0x3ffff0\n" \
"global_load_lds_dword v188, s[86:87]\n" \
"s_mov_b64 exec, -1\n" \
"v_mfma_f32_16x16x32_bf16 a[120:123], v[76:79], v[164:167], a[120:123]\n" \
"s_mov_b32 m0, %[mS]\n" \
"s_mov_b32 s86, s18\n" \
"v_mfma_f32_16x16x32_bf16 a[120:123], v[80:83], v[168:171], a[120:123]\n" \
"s_mov_b32 s87, s19\n" \
"v_add_u32_e32 v7, 0xfffff400, v215\n" \
"v_mfma_f32_16x16x32_bf16 a[28:31], v[56:59], v[176:179], a[28:31]\n" \
"v_add_u32_e32 v9, v217, v214\n" \
"v_and_b32_e64 v216, 3, v212\n" \
"v_mfma_f32_16x16x32_bf16 a[28:31], v[52:55], v[172:175], a[28:31]\n" \
"v_lshlrev_b32_e32 v250, 3, v216\n" \
"v_mul_u32_u24_e32 v220, 0x120, v234\n" \
"v_mfma_f32_16x16x32_bf16 a[60:63], v[60:63], v[172:175], a[60:63]\n" \
"v_mul_u32_u24_e32 v223, 0x480, v222\n" \
"v_add_u32_e32 v10, v220, v250\n" \
"s_mov_b32 exec_lo, 0x3ffff\n" \
"s_mov_b32 exec_hi, 0x0\n" \
"global_load_lds_dword v16, s[86:87]\n" \
"s_mov_b64 exec, -1\n" \
"v_mfma_f32_16x16x32_bf16 a[60:63], v[64:67], v[176:179], a[60:63]\n" \
"v_add_u32_e32 v8, v223, v225\n" \
"s_lshl_b32 s99, %[w], 11\n" \
"v_mfma_f32_16x16x32_bf16 a[92:95], v[72:75], v[176:179], a[92:95]\n" \
"s_add_u32 s55, s98, s99\n" \
"s_nop 0\n" \
"v_mfma_f32_16x16x32_bf16 a[92:95], v[68:71], v[172:175], a[92:95]\n" \
"v_mfma_f32_16x16x32_bf16 a[124:127], v[76:79], v[172:175], a[124:127]\n" \
"v_mfma_f32_16x16x32_bf16 a[124:127], v[80:83], v[176:179], a[124:127]\n" \
"v_mfma_f32_16x16x32_bf16 a[0:3], v[120:123], v[56:59], a[0:3]\n" \
"v_mfma_f32_16x16x32_bf16 a[0:3], v[116:119], v[52:55], a[0:3]\n" \
"v_mfma_f32_16x16x32_bf16 a[32:35], v[116:119], v[60:63], a[32:35]\n" \
"v_mfma_f32_16x16x32_bf16 a[32:35], v[120:123], v[64:67], a[32:35]\n" \
"v_mfma_f32_16x16x32_bf16 a[64:67], v[120:123], v[72:75], a[64:67]\n" \
"v_mfma_f32_16x16x32_bf16 a[64:67], v[116:119], v[68:71], a[64:67]\n" \
"v_mfma_f32_16x16x32_bf16 a[96:99], v[116:119], v[76:79], a[96:99]\n" \
"v_mfma_f32_16x16x32_bf16 a[96:99], v[120:123], v[80:83], a[96:99]\n" \
"v_mfma_f32_16x16x32_bf16 a[4:7], v[128:131], v[56:59], a[4:7]\n" \
"v_mfma_f32_16x16x32_bf16 a[4:7], v[124:127], v[52:55], a[4:7]\n" \
"v_mfma_f32_16x16x32_bf16 a[36:39], v[124:127], v[60:63], a[36:39]\n" \
"v_mfma_f32_16x16x32_bf16 a[36:39], v[128:131], v[64:67], a[36:39]\n" \
"v_mfma_f32_16x16x32_bf16 a[68:71], v[128:131], v[72:75], a[68:71]\n" \
"v_mfma_f32_16x16x32_bf16 a[68:71], v[124:127], v[68:71], a[68:71]\n" \
"v_mfma_f32_16x16x32_bf16 a[100:103], v[124:127], v[76:79], a[100:103]\n" \
"v_mfma_f32_16x16x32_bf16 a[100:103], v[128:131], v[80:83], a[100:103]\n" \
"s_waitcnt lgkmcnt(0)\n" \
"s_nop 0\n" \
"v_mfma_f32_16x16x32_bf16 a[8:11], v[136:139], v[56:59], a[8:11]\n" \
"v_mfma_f32_16x16x32_bf16 a[8:11], v[132:135], v[52:55], a[8:11]\n" \
"v_mfma_f32_16x16x32_bf16 a[40:43], v[132:135], v[60:63], a[40:43]\n" \
"v_mfma_f32_16x16x32_bf16 a[40:43], v[136:139], v[64:67], a[40:43]\n" \
"v_mfma_f32_16x16x32_bf16 a[72:75], v[136:139], v[72:75], a[72:75]\n" \
"v_mfma_f32_16x16x32_bf16 a[72:75], v[132:135], v[68:71], a[72:75]\n" \
"v_mfma_f32_16x16x32_bf16 a[104:107], v[132:135], v[76:79], a[104:107]\n" \
"v_mfma_f32_16x16x32_bf16 a[104:107], v[136:139], v[80:83], a[104:107]\n" \
"v_mfma_f32_16x16x32_bf16 a[12:15], v[144:147], v[56:59], a[12:15]\n" \
"s_add_u32 s62, s22, 0x380\n" \
"s_and_b32 s62, s62, 0x3ff\n" \
"v_mfma_f32_16x16x32_bf16 a[12:15], v[140:143], v[52:55], a[12:15]\n" \
"s_lshl_b32 s63, s62, 12\n" \
"v_mfma_f32_16x16x32_bf16 a[44:47], v[140:143], v[60:63], a[44:47]\n" \
"v_mfma_f32_16x16x32_bf16 a[44:47], v[144:147], v[64:67], a[44:47]\n" \
"v_mfma_f32_16x16x32_bf16 a[76:79], v[144:147], v[72:75], a[76:79]\n" \
"v_mfma_f32_16x16x32_bf16 a[76:79], v[140:143], v[68:71], a[76:79]\n" \
"v_mfma_f32_16x16x32_bf16 a[108:111], v[140:143], v[76:79], a[108:111]\n" \
"v_mfma_f32_16x16x32_bf16 a[108:111], v[144:147], v[80:83], a[108:111]\n" \
"s_waitcnt vmcnt(2)\n" \
"s_barrier\n" \
"s_mov_b64 exec, 1\n" \
"buffer_store_dword v45, off, s[80:83], s38 offset:64 sc1\n" \
"s_mov_b64 exec, -1\n" \
"v_mfma_f32_16x16x32_bf16 a[128:131], v[120:123], v[88:91], a[128:131]\n" \
"s_add_u32 m0, %[mA], 0x8000\n" \
"s_add_u32 s85, s62, 0x0\n" \
"v_mfma_f32_16x16x32_bf16 a[128:131], v[116:119], v[84:87], a[128:131]\n" \
"ds_read_b128 v[56:59], v29 offset:16384\n" \
"ds_read_b128 v[52:55], v28 offset:16384\n" \
"v_mfma_f32_16x16x32_bf16 a[160:163], v[116:119], v[92:95], a[160:163]\n" \
"buffer_load_dwordx4 v22, s[64:67], s85 offen lds\n" \
"v_mfma_f32_16x16x32_bf16 a[160:163], v[120:123], v[96:99], a[160:163]\n" \
"s_add_u32 m0, %[mA], 0x8400\n" \
"s_add_u32 s84, s62, 0x2000\n" \
"v_mfma_f32_16x16x32_bf16 a[192:195], v[120:123], v[104:107], a[192:195]\n" \
"ds_read_b128 v[60:63], v28 offset:18432\n" \
"ds_read_b128 v[64:67], v29 offset:18432\n" \
"v_mfma_f32_16x16x32_bf16 a[192:195], v[116:119], v[100:103], a[192:195]\n" \
"buffer_load_dwordx4 v23, s[64:67], s84 offen lds\n" \
"v_mfma_f32_16x16x32_bf16 a[224:227], v[116:119], v[108:111], a[224:227]\n" \
"ds_read_b64_tr_b16 v[116:117], v32 offset:0\n" \
"ds_read_b64_tr_b16 v[118:119], v32 offset:1024\n" \
"v_mfma_f32_16x16x32_bf16 a[224:227], v[120:123], v[112:115], a[224:227]\n" \
"s_add_u32 m0, %[mB], 0x8000\n" \
"s_add_u32 s85, s63, 0x0\n" \
"v_mfma_f32_16x16x32_bf16 a[132:135], v[128:131], v[88:91], a[132:135]\n" \
"buffer_load_dwordx4 v26, s[68:71], s85 offen lds\n" \
"v_mfma_f32_16x16x32_bf16 a[132:135], v[124:127], v[84:87], a[132:135]\n" \
"ds_read_b64_tr_b16 v[120:121], v32 offset:8192\n" \
"ds_read_b64_tr_b16 v[122:123], v32 offset:9216\n" \
"v_mfma_f32_16x16x32_bf16 a[164:167], v[124:127], v[92:95], a[164:167]\n" \
"ds_read_b128 v[72:75], v29 offset:20480\n" \
"ds_read_b128 v[68:71], v28 offset:20480\n" \
"v_mfma_f32_16x16x32_bf16 a[164:167], v[128:131], v[96:99], a[164:167]\n" \
"s_add_u32 m0, %[mB], 0xc000\n" \
"s_add_u32 s84, s63, 0x0\n" \
"v_mfma_f32_16x16x32_bf16 a[196:199], v[128:131], v[104:107], a[196:199]\n" \
"buffer_load_dwordx4 v26, s[72:75], s84 offen lds\n" \
"v_mfma_f32_16x16x32_bf16 a[196:199], v[124:127], v[100:103], a[196:199]\n" \
"ds_read_b128 v[76:79], v28 offset:22528\n" \
"ds_read_b128 v[80:83], v29 offset:22528\n" \
"v_mfma_f32_16x16x32_bf16 a[228:231], v[124:127], v[108:111], a[228:231]\n" \
"ds_read_b64_tr_b16 v[124:125], v33 offset:0\n" \
"ds_read_b64_tr_b16 v[126:127], v33 offset:1024\n" \
"v_mfma_f32_16x16x32_bf16 a[228:231], v[128:131], v[112:115], a[228:231]\n" \
"s_add_u32 m0, %[mA], 0x8800\n" \
"s_add_u32 s85, s62, 0x4000\n" \
"v_mfma_f32_16x16x32_bf16 a[136:139], v[136:139], v[88:91], a[136:139]\n" \
"buffer_load_dwordx4 v22, s[64:67], s85 offen lds\n" \
"v_mfma_f32_16x16x32_bf16 a[136:139], v[132:135], v[84:87], a[136:139]\n" \
"ds_read_b64_tr_b16 v[128:129], v33 offset:8192\n" \
"ds_read_b64_tr_b16 v[130:131], v33 offset:9216\n" \
"v_mfma_f32_16x16x32_bf16 a[168:171], v[132:135], v[92:95], a[168:171]\n" \
"v_mov_b32_e64 v45, %[tag]\n" \
"v_mfma_f32_16x16x32_bf16 a[168:171], v[136:139], v[96:99], a[168:171]\n" \
"s_add_u32 m0, %[mA], 0x8c00\n" \
"s_add_u32 s84, s62, 0x6000\n" \
"v_mfma_f32_16x16x32_bf16 a[200:203], v[136:139], v[104:107], a[200:203]\n" \
"buffer_load_dwordx4 v23, s[64:67], s84 offen lds\n" \
"v_mfma_f32_16x16x32_bf16 a[200:203], v[132:135], v[100:103], a[200:203]\n" \
"v_mfma_f32_16x16x32_bf16 a[232:235], v[132:135], v[108:111], a[232:235]\n" \
"ds_read_b64_tr_b16 v[132:133], v34 offset:0\n" \
"ds_read_b64_tr_b16 v[134:135], v34 offset:1024\n" \
"v_mfma_f32_16x16x32_bf16 a[232:235], v[136:139], v[112:115], a[232:235]\n" \
"s_add_u32 m0, %[mB], 0x8400\n" \
"s_add_u32 s85, s63, 0x8000\n" \
"v_mfma_f32_16x16x32_bf16 a[140:143], v[144:147], v[88:91], a[140:143]\n" \
"buffer_load_dwordx4 v26, s[68:71], s85 offen lds\n" \
"v_mfma_f32_16x16x32_bf16 a[140:143], v[140:143], v[84:87], a[140:143]\n" \
"ds_read_b64_tr_b16 v[136:137], v34 offset:8192\n" \
"ds_read_b64_tr_b16 v[138:139], v34 offset:9216\n" \
"v_mfma_f32_16x16x32_bf16 a[172:175], v[140:143], v[92:95], a[172:175]\n" \
"v_mfma_f32_16x16x32_bf16 a[172:175], v[144:147], v[96:99], a[172:175]\n" \
"s_add_u32 m0, %[mB], 0xc400\n" \
"s_add_u32 s84, s63, 0x8000\n" \
"v_mfma_f32_16x16x32_bf16 a[204:207], v[144:147], v[104:107], a[204:207]\n" \
"buffer_load_dwordx4 v26, s[72:75], s84 offen lds\n" \
"v_mfma_f32_16x16x32_bf16 a[204:207], v[140:143], v[100:103], a[204:207]\n" \
"v_mfma_f32_16x16x32_bf16 a[236:239], v[140:143], v[108:111], a[236:239]\n" \
"ds_read_b64_tr_b16 v[140:141], v35 offset:0\n" \
"ds_read_b64_tr_b16 v[142:143], v35 offset:1024\n" \
"v_mfma_f32_16x16x32_bf16 a[236:239], v[144:147], v[112:115], a[236:239]\n" \
"s_add_u32 m0, %[mA], 0x9000\n" \
"s_add_u32 s85, s62, 0x8000\n" \
"v_mfma_f32_16x16x32_bf16 a[144:147], v[88:91], v[152:155], a[144:147]\n" \
"buffer_load_dwordx4 v22, s[64:67], s85 offen lds\n" \
"v_mfma_f32_16x16x32_bf16 a[144:147], v[84:87], v[148:151], a[144:147]\n" \
"ds_read_b64_tr_b16 v[144:145], v35 offset:8192\n" \
"ds_read_b64_tr_b16 v[146:147], v35 offset:9216\n" \
"v_mfma_f32_16x16x32_bf16 a[176:179], v[92:95], v[148:151], a[176:179]\n" \
"L_pf3:\n" \
"s_cbranch_execz L_pf7\n" \
"s_nop 0\n" \
"v_mfma_f32_16x16x32_bf16 a[176:179], v[96:99], v[152:155], a[176:179]\n" \
"s_add_u32 m0, %[mA], 0x9400\n" \
"s_add_u32 s84, s62, 0xa000\n" \
"v_mfma_f32_16x16x32_bf16 a[208:211], v[104:107], v[152:155], a[208:211]\n" \
"buffer_load_dwordx4 v23, s[64:67], s84 offen lds\n" \
"v_mfma_f32_16x16x32_bf16 a[208:211], v[100:103], v[148:151], a[208:211]\n" \
"v_mfma_f32_16x16x32_bf16 a[240:243], v[108:111], v[148:151], a[240:243]\n" \
"ds_read_b64_tr_b16 v[148:149], v32 offset:16384\n" \
"ds_read_b64_tr_b16 v[150:151], v32 offset:17408\n" \
"v_mfma_f32_16x16x32_bf16 a[240:243], v[112:115], v[152:155], a[240:243]\n" \
"s_add_u32 m0, %[mB], 0x8800\n" \
"s_add_u32 s85, s63, 0x10000\n" \
"v_mfma_f32_16x16x32_bf16 a[148:151], v[88:91], v[160:163], a[148:151]\n" \
"buffer_load_dwordx4 v27, s[68:71], s85 offen lds\n" \
"v_mfma_f32_16x16x32_bf16 a[148:151], v[84:87], v[156:159], a[148:151]\n" \
"ds_read_b64_tr_b16 v[152:153], v32 offset:24576\n" \
"ds_read_b64_tr_b16 v[154:155], v32 offset:25600\n" \
"v_mfma_f32_16x16x32_bf16 a[180:183], v[92:95], v[156:159], a[180:183]\n" \
"v_mfma_f32_16x16x32_bf16 a[180:183], v[96:99], v[160:163], a[180:183]\n" \
"s_add_u32 m0, %[mB], 0xc800\n" \
"s_add_u32 s84, s63, 0x10000\n" \
"v_mfma_f32_16x16x32_bf16 a[212:215], v[104:107], v[160:163], a[212:215]\n" \
"buffer_load_dwordx4 v27, s[72:75], s84 offen lds\n" \
"v_mfma_f32_16x16x32_bf16 a[212:215], v[100:103], v[156:159], a[212:215]\n" \
"v_mfma_f32_16x16x32_bf16 a[244:247], v[108:111], v[156:159], a[244:247]\n" \
"ds_read_b64_tr_b16 v[156:157], v33 offset:16384\n" \
"ds_read_b64_tr_b16 v[158:159], v33 offset:17408\n" \
"v_mfma_f32_16x16x32_bf16 a[244:247], v[112:115], v[160:163], a[244:247]\n" \
"ds_read_b64_tr_b16 v[160:161], v33 offset:24576\n" \
"ds_read_b64_tr_b16 v[162:163], v33 offset:25600\n" \
"v_mfma_f32_16x16x32_bf16 a[152:155], v[88:91], v[168:171], a[152:155]\n" \
"v_mfma_f32_16x16x32_bf16 a[152:155], v[84:87], v[164:167], a[152:155]\n" \
"v_mfma_f32_16x16x32_bf16 a[184:187], v[92:95], v[164:167], a[184:187]\n" \
"v_mfma_f32_16x16x32_bf16 a[184:187], v[96:99], v[168:171], a[184:187]\n" \
"v_mfma_f32_16x16x32_bf16 a[216:219], v[104:107], v[168:171], a[216:219]\n" \
"v_mfma_f32_16x16x32_bf16 a[216:219], v[100:103], v[164:167], a[216:219]\n" \
"v_mfma_f32_16x16x32_bf16 a[248:251], v[108:111], v[164:167], a[248:251]\n" \
"ds_read_b64_tr_b16 v[164:165], v34 offset:16384\n" \
"ds_read_b64_tr_b16 v[166:167], v34 offset:17408\n" \
"v_mfma_f32_16x16x32_bf16 a[248:251], v[112:115], v[168:171], a[248:251]\n" \
"ds_read_b64_tr_b16 v[168:169], v34 offset:24576\n" \
"ds_read_b64_tr_b16 v[170:171], v34 offset:25600\n" \
"v_mfma_f32_16x16x32_bf16 a[156:159], v[88:91], v[176:179], a[156:159]\n" \
"ds_read_b128 v[88:91], v29 offset:0\n" \
"v_mfma_f32_16x16x32_bf16 a[156:159], v[84:87], v[172:175], a[156:159]\n" \
"ds_read_b128 v[84:87], v28 offset:0\n" \
"v_mfma_f32_16x16x32_bf16 a[188:191], v[92:95], v[172:175], a[188:191]\n" \
"ds_read_b128 v[92:95], v28 offset:2048\n" \
"v_mfma_f32_16x16x32_bf16 a[188:191], v[96:99], v[176:179], a[188:191]\n" \
"ds_read_b128 v[96:99], v29 offset:2048\n" \
"v_mfma_f32_16x16x32_bf16 a[220:223], v[104:107], v[176:179], a[220:223]\n" \
"ds_read_b128 v[104:107], v29 offset:4096\n" \
"v_mfma_f32_16x16x32_bf16 a[220:223], v[100:103], v[172:175], a[220:223]\n" \
"ds_read_b128 v[100:103], v28 offset:4096\n" \
"v_mfma_f32_16x16x32_bf16 a[252:255], v[108:111], v[172:175], a[252:255]\n" \
"ds_read_b64_tr_b16 v[172:173], v35 offset:16384\n" \
"ds_read_b64_tr_b16 v[174:175], v35 offset:17408\n" \
"v_mfma_f32_16x16x32_bf16 a[252:255], v[112:115], v[176:179], a[252:255]\n" \
"s_add_u32 m0, %[mA], 0x9800\n" \
"s_waitcnt lgkmcnt(12)\n" \
"s_nop 0\n" \
"v_mfma_f32_16x16x32_bf16 a[0:3], v[120:123], v[56:59], a[0:3]\n" \
"s_add_u32 s85, s62, 0xc000\n" \
"ds_read_b64_tr_b16 v[176:177], v35 offset:24576\n" \
"v_mfma_f32_16x16x32_bf16 a[0:3], v[116:119], v[52:55], a[0:3]\n" \
"buffer_load_dwordx4 v22, s[64:67], s85 offen lds\n" \
"v_mfma_f32_16x16x32_bf16 a[32:35], v[116:119], v[60:63], a[32:35]\n" \
"s_add_u32 m0, %[mA], 0x9c00\n" \
"s_add_u32 s84, s62, 0xe000\n" \
"v_mfma_f32_16x16x32_bf16 a[32:35], v[120:123], v[64:67], a[32:35]\n" \
"ds_read_b64_tr_b16 v[178:179], v35 offset:25600\n" \
"ds_read_b128 v[108:111], v28 offset:6144\n" \
"v_mfma_f32_16x16x32_bf16 a[64:67], v[120:123], v[72:75], a[64:67]\n" \
"buffer_load_dwordx4 v23, s[64:67], s84 offen lds\n" \
"v_mfma_f32_16x16x32_bf16 a[64:67], v[116:119], v[68:71], a[64:67]\n" \
"s_add_u32 m0, %[mB], 0x8c00\n" \
"s_add_u32 s85, s63, 0x18000\n" \
"v_mfma_f32_16x16x32_bf16 a[96:99], v[116:119], v[76:79], a[96:99]\n" \
"ds_read_b128 v[112:115], v29 offset:6144\n" \
"s_mov_b32 s62, s23\n" \
"v_mfma_f32_16x16x32_bf16 a[96:99], v[120:123], v[80:83], a[96:99]\n" \
"buffer_load_dwordx4 v27, s[68:71], s85 offen lds\n" \
"v_mfma_f32_16x16x32_bf16 a[4:7], v[128:131], v[56:59], a[4:7]\n" \
"s_add_u32 m0, %[mB], 0xcc00\n" \
"s_add_u32 s84, s63, 0x18000\n" \
"v_mfma_f32_16x16x32_bf16 a[4:7], v[124:127], v[52:55], a[4:7]\n" \
"s_mov_b32 s68, s10\n" \
"s_and_b32 s69, s11, 0xffff\n" \
"v_mfma_f32_16x16x32_bf16 a[36:39], v[124:127], v[60:63], a[36:39]\n" \
"buffer_load_dwordx4 v27, s[72:75], s84 offen lds\n" \
"s_mov_b32 s73, s52\n" \
"s_add_u32 s74, s52, 0x4000\n" \
"s_add_u32 s75, s52, 0x8000\n" \
"v_mfma_f32_16x16x32_bf16 a[36:39], v[128:131], v[64:67], a[36:39]\n" \
"s_lshl_b32 s72, s62, 6\n" \
"s_add_u32 s72, s72, s37\n" \
"v_mfma_f32_16x16x32_bf16 a[68:71], v[128:131], v[72:75], a[68:71]\n" \
"v_bfe_u32 v44, v20, 2, 5\n" \
"s_mov_b32 s70, 0x480000\n" \
"v_mfma_f32_16x16x32_bf16 a[68:71], v[124:127], v[68:71], a[68:71]\n" \
"buffer_load_dword v44, v44, s[80:83], s39 offen offset:64 sc1\n" \
"v_mfma_f32_16x16x32_bf16 a[100:103], v[124:127], v[76:79], a[100:103]\n" \
"v_add_u32_e32 v22, s61, v28\n" \
"v_add_u32_e64 v23, s61, v29\n" \
"v_mfma_f32_16x16x32_bf16 a[100:103], v[128:131], v[80:83], a[100:103]\n" \
"s_mov_b32 m0, %[mS]\n" \
"s_mov_b32 s86, s18\n" \
"v_mfma_f32_16x16x32_bf16 a[8:11], v[136:139], v[56:59], a[8:11]\n" \
"s_mov_b32 s87, s19\n" \
"v_mfma_f32_16x16x32_bf16 a[8:11], v[132:135], v[52:55], a[8:11]\n" \
"v_mfma_f32_16x16x32_bf16 a[40:43], v[132:135], v[60:63], a[40:43]\n" \
"v_mfma_f32_16x16x32_bf16 a[40:43], v[136:139], v[64:67], a[40:43]\n" \
"v_mfma_f32_16x16x32_bf16 a[72:75], v[136:139], v[72:75], a[72:75]\n" \
"v_mfma_f32_16x16x32_bf16 a[72:75], v[132:135], v[68:71], a[72:75]\n" \
"s_mov_b32 exec_lo, 0xfffc0000\n" \
"s_mov_b32 exec_hi, 0xf\n" \
"global_load_lds_dword v16, s[86:87]\n" \
"s_mov_b64 exec, -1\n" \
"v_mfma_f32_16x16x32_bf16 a[104:107], v[132:135], v[76:79], a[104:107]\n" \
"s_mov_b32 m0, %[mS]\n" \
"s_mov_b32 s86, s16\n" \
"v_mfma_f32_16x16x32_bf16 a[104:107], v[136:139], v[80:83], a[104:107]\n" \
"s_mov_b32 s87, s17\n" \
"v_mfma_f32_16x16x32_bf16 a[12:15], v[144:147], v[56:59], a[12:15]\n" \
"v_mfma_f32_16x16x32_bf16 a[12:15], v[140:143], v[52:55], a[12:15]\n" \
"v_mfma_f32_16x16x32_bf16 a[44:47], v[140:143], v[60:63], a[44:47]\n" \
"s_mov_b32 exec_lo, 0x0\n" \
"s_mov_b32 exec_hi, 0x3f0\n" \
"global_load_lds_dword v16, s[86:87]\n" \
"s_mov_b64 exec, -1\n" \
"v_mfma_f32_16x16x32_bf16 a[44:47], v[144:147], v[64:67], a[44:47]\n" \
"v_mfma_f32_16x16x32_bf16 a[76:79], v[144:147], v[72:75], a[76:79]\n" \
"v_mfma_f32_16x16x32_bf16 a[76:79], v[140:143], v[68:71], a[76:79]\n" \
"v_mfma_f32_16x16x32_bf16 a[108:111], v[140:143], v[76:79], a[108:111]\n" \
"v_mfma_f32_16x16x32_bf16 a[108:111], v[144:147], v[80:83], a[108:111]\n" \
"v_mfma_f32_16x16x32_bf16 a[16:19], v[56:59], v[152:155], a[16:19]\n" \
"v_mfma_f32_16x16x32_bf16 a[16:19], v[52:55], v[148:151], a[16:19]\n" \
"v_mfma_f32_16x16x32_bf16 a[48:51], v[60:63], v[148:151], a[48:51]\n" \
"v_mfma_f32_16x16x32_bf16 a[48:51], v[64:67], v[152:155], a[48:51]\n" \
"v_mfma_f32_16x16x32_bf16 a[80:83], v[72:75], v[152:155], a[80:83]\n" \
"v_mfma_f32_16x16x32_bf16 a[80:83], v[68:71], v[148:151], a[80:83]\n" \
"v_mfma_f32_16x16x32_bf16 a[112:115], v[76:79], v[148:151], a[112:115]\n" \
"v_mfma_f32_16x16x32_bf16 a[112:115], v[80:83], v[152:155], a[112:115]\n" \
"v_mfma_f32_16x16x32_bf16 a[20:23], v[56:59], v[160:163], a[20:23]\n" \
"v_mfma_f32_16x16x32_bf16 a[20:23], v[52:55], v[156:159], a[20:23]\n" \
"v_mfma_f32_16x16x32_bf16 a[52:55], v[60:63], v[156:159], a[52:55]\n" \
"v_mfma_f32_16x16x32_bf16 a[52:55], v[64:67], v[160:163], a[52:55]\n" \
"v_mfma_f32_16x16x32_bf16 a[84:87], v[72:75], v[160:163], a[84:87]\n" \
"v_mfma_f32_16x16x32_bf16 a[84:87], v[68:71], v[156:159], a[84:87]\n" \
"v_mfma_f32_16x16x32_bf16 a[116:119], v[76:79], v[156:159], a[116:119]\n" \
"v_mfma_f32_16x16x32_bf16 a[116:119], v[80:83], v[160:163], a[116:119]\n" \
"s_waitcnt lgkmcnt(0)\n" \
"v_mfma_f32_16x16x32_bf16 a[24:27], v[56:59], v[168:171], a[24:27]\n" \
"v_mfma_f32_16x16x32_bf16 a[24:27], v[52:55], v[164:167], a[24:27]\n" \
"v_mfma_f32_16x16x32_bf16 a[56:59], v[60:63], v[164:167], a[56:59]\n" \
"v_mfma_f32_16x16x32_bf16 a[56:59], v[64:67], v[168:171], a[56:59]\n" \
"v_mfma_f32_16x16x32_bf16 a[88:91], v[72:75], v[168:171], a[88:91]\n" \
"v_mfma_f32_16x16x32_bf16 a[88:91], v[68:71], v[164:167], a[88:91]\n" \
"v_mfma_f32_16x16x32_bf16 a[120:123], v[76:79], v[164:167], a[120:123]\n" \
"v_mfma_f32_16x16x32_bf16 a[120:123], v[80:83], v[168:171], a[120:123]\n" \
"v_mfma_f32_16x16x32_bf16 a[28:31], v[56:59], v[176:179], a[28:31]\n" \
"s_waitcnt vmcnt(2)\n" \
"v_cmp_ne_u32_e32 vcc, %[tag], v44\n" \
"v_mfma_f32_16x16x32_bf16 a[28:31], v[52:55], v[172:175], a[28:31]\n" \
"v_mfma_f32_16x16x32_bf16 a[60:63], v[60:63], v[172:175], a[60:63]\n" \
"v_mfma_f32_16x16x32_bf16 a[60:63], v[64:67], v[176:179], a[60:63]\n" \
"v_mfma_f32_16x16x32_bf16 a[92:95], v[72:75], v[176:179], a[92:95]\n" \
"v_mfma_f32_16x16x32_bf16 a[92:95], v[68:71], v[172:175], a[92:95]\n" \
"s_cbranch_vccnz L_pl_%=\n" \
"L_rl_%=:\n" \
"s_lshl_b32 s63, s62, 12\n" \
"v_mfma_f32_16x16x32_bf16 a[124:127], v[76:79], v[172:175], a[124:127]\n" \
"v_mfma_f32_16x16x32_bf16 a[124:127], v[80:83], v[176:179], a[124:127]\n" \
"s_barrier\n" \
"v_mfma_f32_16x16x32_bf16 a[144:147], v[88:91], v[152:155], a[144:147]\n" \
"s_add_u32 m0, s40, 0x0\n" \
"s_add_u32 s85, s72, 0x0\n" \
"v_mfma_f32_16x16x32_bf16 a[144:147], v[84:87], v[148:151], a[144:147]\n" \
"buffer_load_dwordx4 v20, s[80:83], s85 offen sc1 lds\n" \
"v_mfma_f32_16x16x32_bf16 a[176:179], v[92:95], v[148:151], a[176:179]\n" \
"s_add_u32 m0, s40, 0x1000\n" \
"s_add_u32 s84, s72, 0x1000\n" \
"v_mfma_f32_16x16x32_bf16 a[176:179], v[96:99], v[152:155], a[176:179]\n" \
"ds_read_b128 v[56:59], v29 offset:49152\n" \
"ds_read_b128 v[52:55], v28 offset:49152\n" \
"v_mfma_f32_16x16x32_bf16 a[208:211], v[104:107], v[152:155], a[208:211]\n" \
"buffer_load_dwordx4 v20, s[80:83], s84 offen sc1 lds\n" \
"v_mfma_f32_16x16x32_bf16 a[208:211], v[100:103], v[148:151], a[208:211]\n" \
"ds_read_b128 v[60:63], v28 offset:51200\n" \
"ds_read_b128 v[64:67], v29 offset:51200\n" \
"v_mfma_f32_16x16x32_bf16 a[240:243], v[108:111], v[148:151], a[240:243]\n" \
"s_add_u32 m0, s40, 0x3000\n" \
"s_add_u32 s85, s62, 0x0\n" \
"v_mfma_f32_16x16x32_bf16 a[240:243], v[112:115], v[152:155], a[240:243]\n" \
"buffer_load_dwordx4 v21, s[76:79], s85 offen lds\n" \
"v_mfma_f32_16x16x32_bf16 a[148:151], v[88:91], v[160:163], a[148:151]\n" \
"ds_read_b64_tr_b16 v[152:153], v32 offset:57344\n" \
"ds_read_b64_tr_b16 v[154:155], v32 offset:58368\n" \
"v_mfma_f32_16x16x32_bf16 a[148:151], v[84:87], v[156:159], a[148:151]\n" \
"ds_read_b64_tr_b16 v[148:149], v32 offset:49152\n" \
"ds_read_b64_tr_b16 v[150:151], v32 offset:50176\n" \
"v_mfma_f32_16x16x32_bf16 a[180:183], v[92:95], v[156:159], a[180:183]\n" \
"s_add_u32 m0, %[mA], 0x0\n" \
"s_add_u32 s84, s62, 0x0\n" \
"s_nop 0\n" \
"v_mfma_f32_16x16x32_bf16 a[180:183], v[96:99], v[160:163], a[180:183]\n" \
"buffer_load_dwordx4 v24, s[76:79], s84 offen lds\n" \
"v_mfma_f32_16x16x32_bf16 a[212:215], v[104:107], v[160:163], a[212:215]\n" \
"ds_read_b128 v[72:75], v29 offset:53248\n" \
"ds_read_b128 v[68:71], v28 offset:53248\n" \
"v_mfma_f32_16x16x32_bf16 a[212:215], v[100:103], v[156:159], a[212:215]\n" \
"s_add_u32 m0, %[mA], 0x400\n" \
"s_add_u32 s85, s62, 0x2400\n" \
"v_mfma_f32_16x16x32_bf16 a[244:247], v[108:111], v[156:159], a[244:247]\n" \
"buffer_load_dwordx4 v25, s[76:79], s85 offen lds\n" \
"v_mfma_f32_16x16x32_bf16 a[244:247], v[112:115], v[160:163], a[244:247]\n" \
"ds_read_b128 v[76:79], v28 offset:55296\n" \
"ds_read_b128 v[80:83], v29 offset:55296\n" \
"v_mfma_f32_16x16x32_bf16 a[152:155], v[88:91], v[168:171], a[152:155]\n" \
"ds_read_b64_tr_b16 v[160:161], v33 offset:57344\n" \
"ds_read_b64_tr_b16 v[162:163], v33 offset:58368\n" \
"v_mfma_f32_16x16x32_bf16 a[152:155], v[84:87], v[164:167], a[152:155]\n" \
"s_add_u32 m0, %[mB], 0x0\n" \
"s_add_u32 s84, s63, 0x0\n" \
"v_mfma_f32_16x16x32_bf16 a[184:187], v[92:95], v[164:167], a[184:187]\n" \
"buffer_load_dwordx4 v26, s[68:71], s84 offen lds\n" \
"v_mfma_f32_16x16x32_bf16 a[184:187], v[96:99], v[168:171], a[184:187]\n" \
"ds_read_b64_tr_b16 v[156:157], v33 offset:49152\n" \
"ds_read_b64_tr_b16 v[158:159], v33 offset:50176\n" \
"v_mfma_f32_16x16x32_bf16 a[216:219], v[104:107], v[168:171], a[216:219]\n" \
"v_mfma_f32_16x16x32_bf16 a[216:219], v[100:103], v[164:167], a[216:219]\n" \
"s_add_u32 m0, %[mA], 0x800\n" \
"s_add_u32 s85, s62, 0x4800\n" \
"v_mfma_f32_16x16x32_bf16 a[248:251], v[108:111], v[164:167], a[248:251]\n" \
"buffer_load_dwordx4 v24, s[76:79], s85 offen lds\n" \
"v_mfma_f32_16x16x32_bf16 a[248:251], v[112:115], v[168:171], a[248:251]\n" \
"ds_read_b64_tr_b16 v[168:169], v34 offset:57344\n" \
"ds_read_b64_tr_b16 v[170:171], v34 offset:58368\n" \
"v_mfma_f32_16x16x32_bf16 a[156:159], v[88:91], v[176:179], a[156:159]\n" \
"s_add_u32 m0, %[mA], 0xc00\n" \
"s_add_u32 s84, s62, 0x6c00\n" \
"v_mfma_f32_16x16x32_bf16 a[156:159], v[84:87], v[172:175], a[156:159]\n" \
"buffer_load_dwordx4 v25, s[76:79], s84 offen lds\n" \
"v_mfma_f32_16x16x32_bf16 a[188:191], v[92:95], v[172:175], a[188:191]\n" \
"ds_read_b64_tr_b16 v[164:165], v34 offset:49152\n" \
"ds_read_b64_tr_b16 v[166:167], v34 offset:50176\n" \
"v_mfma_f32_16x16x32_bf16 a[188:191], v[96:99], v[176:179], a[188:191]\n" \
"v_mfma_f32_16x16x32_bf16 a[220:223], v[104:107], v[176:179], a[220:223]\n" \
"s_add_u32 m0, %[mB], 0x400\n" \
"s_add_u32 s85, s63, 0x8000\n" \
"v_mfma_f32_16x16x32_bf16 a[220:223], v[100:103], v[172:175], a[220:223]\n" \
"buffer_load_dwordx4 v26, s[68:71], s85 offen lds\n" \
"v_mfma_f32_16x16x32_bf16 a[252:255], v[108:111], v[172:175], a[252:255]\n" \
"ds_read_b64_tr_b16 v[172:173], v35 offset:49152\n" \
"ds_read_b64_tr_b16 v[174:175], v35 offset:50176\n" \
"v_mfma_f32_16x16x32_bf16 a[252:255], v[112:115], v[176:179], a[252:255]\n" \
"ds_read_b64_tr_b16 v[176:177], v35 offset:57344\n" \
"ds_read_b64_tr_b16 v[178:179], v35 offset:58368\n" \
"v_mfma_f32_16x16x32_bf16 a[128:131], v[120:123], v[88:91], a[128:131]\n" \
"s_add_u32 m0, %[mA], 0x1000\n" \
"s_add_u32 s84, s62, 0x9000\n" \
"v_mfma_f32_16x16x32_bf16 a[128:131], v[116:119], v[84:87], a[128:131]\n" \
"buffer_load_dwordx4 v24, s[76:79], s84 offen lds\n" \
"v_mfma_f32_16x16x32_bf16 a[160:163], v[116:119], v[92:95], a[160:163]\n" \
"v_mfma_f32_16x16x32_bf16 a[160:163], v[120:123], v[96:99], a[160:163]\n" \
"s_add_u32 m0, %[mA], 0x1400\n" \
"s_add_u32 s85, s62, 0xb400\n" \
"v_mfma_f32_16x16x32_bf16 a[192:195], v[120:123], v[104:107], a[192:195]\n" \
"buffer_load_dwordx4 v25, s[76:79], s85 offen lds\n" \
"v_mfma_f32_16x16x32_bf16 a[192:195], v[116:119], v[100:103], a[192:195]\n" \
"v_mfma_f32_16x16x32_bf16 a[224:227], v[116:119], v[108:111], a[224:227]\n" \
"ds_read_b64_tr_b16 v[116:117], v32 offset:32768\n" \
"ds_read_b64_tr_b16 v[118:119], v32 offset:33792\n" \
"v_mfma_f32_16x16x32_bf16 a[224:227], v[120:123], v[112:115], a[224:227]\n" \
"s_add_u32 m0, %[mB], 0x800\n" \
"s_add_u32 s84, s63, 0x10000\n" \
"v_mfma_f32_16x16x32_bf16 a[132:135], v[128:131], v[88:91], a[132:135]\n" \
"buffer_load_dwordx4 v27, s[68:71], s84 offen lds\n" \
"v_mfma_f32_16x16x32_bf16 a[132:135], v[124:127], v[84:87], a[132:135]\n" \
"ds_read_b64_tr_b16 v[120:121], v32 offset:40960\n" \
"ds_read_b64_tr_b16 v[122:123], v32 offset:41984\n" \
"v_mfma_f32_16x16x32_bf16 a[164:167], v[124:127], v[92:95], a[164:167]\n" \
"v_mfma_f32_16x16x32_bf16 a[164:167], v[128:131], v[96:99], a[164:167]\n" \
"s_add_u32 m0, %[mA], 0x1800\n" \
"s_add_u32 s85, s62, 0xd800\n" \
"v_mfma_f32_16x16x32_bf16 a[196:199], v[128:131], v[104:107], a[196:199]\n" \
"buffer_load_dwordx4 v24, s[76:79], s85 offen lds\n" \
"v_mfma_f32_16x16x32_bf16 a[196:199], v[124:127], v[100:103], a[196:199]\n" \
"v_mfma_f32_16x16x32_bf16 a[228:231], v[124:127], v[108:111], a[228:231]\n" \
"ds_read_b64_tr_b16 v[124:125], v33 offset:32768\n" \
"ds_read_b64_tr_b16 v[126:127], v33 offset:33792\n" \
"v_mfma_f32_16x16x32_bf16 a[228:231], v[128:131], v[112:115], a[228:231]\n" \
"ds_read_b64_tr_b16 v[128:129], v33 offset:40960\n" \
"ds_read_b64_tr_b16 v[130:131], v33 offset:41984\n" \
"v_mfma_f32_16x16x32_bf16 a[136:139], v[136:139], v[88:91], a[136:139]\n" \
"v_mfma_f32_16x16x32_bf16 a[136:139], v[132:135], v[84:87], a[136:139]\n" \
"v_mfma_f32_16x16x32_bf16 a[168:171], v[132:135], v[92:95], a[168:171]\n" \
"v_mfma_f32_16x16x32_bf16 a[168:171], v[136:139], v[96:99], a[168:171]\n" \
"v_mfma_f32_16x16x32_bf16 a[200:203], v[136:139], v[104:107], a[200:203]\n" \
"v_mfma_f32_16x16x32_bf16 a[200:203], v[132:135], v[100:103], a[200:203]\n" \
"v_mfma_f32_16x16x32_bf16 a[232:235], v[132:135], v[108:111], a[232:235]\n" \
"ds_read_b64_tr_b16 v[132:133], v34 offset:32768\n" \
"ds_read_b64_tr_b16 v[134:135], v34 offset:33792\n" \
"v_mfma_f32_16x16x32_bf16 a[232:235], v[136:139], v[112:115], a[232:235]\n" \
"ds_read_b64_tr_b16 v[136:137], v34 offset:40960\n" \
"ds_read_b64_tr_b16 v[138:139], v34 offset:41984\n" \
"v_mfma_f32_16x16x32_bf16 a[140:143], v[144:147], v[88:91], a[140:143]\n" \
"ds_read_b128 v[88:91], v29 offset:32768\n" \
"v_mfma_f32_16x16x32_bf16 a[140:143], v[140:143], v[84:87], a[140:143]\n" \
"ds_read_b128 v[84:87], v28 offset:32768\n" \
"v_mfma_f32_16x16x32_bf16 a[172:175], v[140:143], v[92:95], a[172:175]\n" \
"ds_read_b128 v[92:95], v28 offset:34816\n" \
"v_mfma_f32_16x16x32_bf16 a[172:175], v[144:147], v[96:99], a[172:175]\n" \
"ds_read_b128 v[96:99], v29 offset:34816\n" \
"v_mfma_f32_16x16x32_bf16 a[204:207], v[144:147], v[104:107], a[204:207]\n" \
"ds_read_b128 v[104:107], v29 offset:36864\n" \
"v_mfma_f32_16x16x32_bf16 a[204:207], v[140:143], v[100:103], a[204:207]\n" \
"ds_read_b128 v[100:103], v28 offset:36864\n" \
"v_mfma_f32_16x16x32_bf16 a[236:239], v[140:143], v[108:111], a[236:239]\n" \
"ds_read_b64_tr_b16 v[140:141], v35 offset:32768\n" \
"ds_read_b64_tr_b16 v[142:143], v35 offset:33792\n" \
"v_mfma_f32_16x16x32_bf16 a[236:239], v[144:147], v[112:115], a[236:239]\n" \
"s_add_u32 m0, %[mA], 0x1c00\n" \
"s_waitcnt lgkmcnt(12)\n" \
"s_nop 0\n" \
"v_mfma_f32_16x16x32_bf16 a[16:19], v[56:59], v[152:155], a[16:19]\n" \
"s_add_u32 s85, s62, 0xfc00\n" \
"ds_read_b64_tr_b16 v[144:145], v35 offset:40960\n" \
"v_mfma_f32_16x16x32_bf16 a[16:19], v[52:55], v[148:151], a[16:19]\n" \
"buffer_load_dwordx4 v25, s[76:79], s85 offen lds\n" \
"v_mfma_f32_16x16x32_bf16 a[48:51], v[60:63], v[148:151], a[48:51]\n" \
"s_add_u32 m0, %[mB], 0xc00\n" \
"s_add_u32 s84, s63, 0x18000\n" \
"v_mfma_f32_16x16x32_bf16 a[48:51], v[64:67], v[152:155], a[48:51]\n" \
"ds_read_b64_tr_b16 v[146:147], v35 offset:41984\n" \
"ds_read_b128 v[108:111], v28 offset:38912\n" \
"v_mfma_f32_16x16x32_bf16 a[80:83], v[72:75], v[152:155], a[80:83]\n" \
"buffer_load_dwordx4 v27, s[68:71], s84 offen lds\n" \
"v_mfma_f32_16x16x32_bf16 a[80:83], v[68:71], v[148:151], a[80:83]\n" \
"ds_read_b128 v[112:115], v29 offset:38912\n" \
"s_add_u32 s62, s62, 0x80\n" \
"v_mfma_f32_16x16x32_bf16 a[112:115], v[76:79], v[148:151], a[112:115]\n" \
"s_cmp_lt_u32 s62, 0x480\n" \
"s_cselect_b32 s62, s62, 0\n" \
"v_mfma_f32_16x16x32_bf16 a[112:115], v[80:83], v[152:155], a[112:115]\n" \
"s_lshl_b32 s72, s62, 6\n" \
"s_add_u32 s72, s72, s37\n" \
"v_mfma_f32_16x16x32_bf16 a[20:23], v[56:59], v[160:163], a[20:23]\n" \
"s_lshl_b32 s63, s62, 12\n" \
"v_add_u32_e64 v28, s59, v28\n" \
"v_mfma_f32_16x16x32_bf16 a[20:23], v[52:55], v[156:159], a[20:23]\n" \
"v_add_u32_e64 v29, s59, v29\n" \
"v_accvgpr_read_b32 v184, a16\n" \
"v_mfma_f32_16x16x32_bf16 a[52:55], v[60:63], v[156:159], a[52:55]\n" \
"v_accvgpr_read_b32 v185, a17\n" \
"v_accvgpr_read_b32 v186, a18\n" \
"v_mfma_f32_16x16x32_bf16 a[52:55], v[64:67], v[160:163], a[52:55]\n" \
"v_accvgpr_read_b32 v187, a19\n" \
"v_accvgpr_write_b32 a16, 0\n" \
"v_mfma_f32_16x16x32_bf16 a[84:87], v[72:75], v[160:163], a[84:87]\n" \
"v_accvgpr_write_b32 a17, 0\n" \
"v_accvgpr_write_b32 a18, 0\n" \
"v_mfma_f32_16x16x32_bf16 a[84:87], v[68:71], v[156:159], a[84:87]\n" \
"v_accvgpr_write_b32 a19, 0\n" \
"v_accvgpr_read_b32 v180, a48\n" \
"v_mfma_f32_16x16x32_bf16 a[116:119], v[76:79], v[156:159], a[116:119]\n" \
"v_accvgpr_read_b32 v181, a49\n" \
"v_accvgpr_read_b32 v182, a50\n" \
"v_mfma_f32_16x16x32_bf16 a[116:119], v[80:83], v[160:163], a[116:119]\n" \
"v_accvgpr_read_b32 v183, a51\n" \
"v_accvgpr_read_b32 v188, a80\n" \
"v_mfma_f32_16x16x32_bf16 a[24:27], v[56:59], v[168:171], a[24:27]\n" \
"v_accvgpr_read_b32 v189, a81\n" \
"v_accvgpr_read_b32 v190, a82\n" \
"v_mfma_f32_16x16x32_bf16 a[24:27], v[52:55], v[164:167], a[24:27]\n" \
"v_accvgpr_read_b32 v191, a83\n" \
"v_cvt_pk_bf16_f32 v184, v184, v185\n" \
"v_mfma_f32_16x16x32_bf16 a[56:59], v[60:63], v[164:167], a[56:59]\n" \
"v_cvt_pk_bf16_f32 v185, v186, v187\n" \
"v_cvt_pk_bf16_f32 v186, v180, v181\n" \
"v_mfma_f32_16x16x32_bf16 a[56:59], v[64:67], v[168:171], a[56:59]\n" \
"v_cvt_pk_bf16_f32 v187, v182, v183\n" \
"v_accvgpr_read_b32 v180, a112\n" \
"v_mfma_f32_16x16x32_bf16 a[88:91], v[72:75], v[168:171], a[88:91]\n" \
"v_accvgpr_read_b32 v181, a113\n" \
"v_accvgpr_read_b32 v182, a114\n" \
"v_mfma_f32_16x16x32_bf16 a[88:91], v[68:71], v[164:167], a[88:91]\n" \
"v_accvgpr_read_b32 v183, a115\n" \
"v_accvgpr_write_b32 a112, 0\n" \
"v_mfma_f32_16x16x32_bf16 a[120:123], v[76:79], v[164:167], a[120:123]\n" \
"v_accvgpr_write_b32 a113, 0\n" \
"v_accvgpr_write_b32 a114, 0\n" \
"v_mfma_f32_16x16x32_bf16 a[120:123], v[80:83], v[168:171], a[120:123]\n" \
"v_accvgpr_write_b32 a115, 0\n" \
"v_accvgpr_read_b32 v192, a20\n" \
"v_mfma_f32_16x16x32_bf16 a[28:31], v[56:59], v[176:179], a[28:31]\n" \
"v_accvgpr_read_b32 v193, a21\n" \
"v_accvgpr_read_b32 v194, a22\n" \
"v_mfma_f32_16x16x32_bf16 a[28:31], v[52:55], v[172:175], a[28:31]\n" \
"v_accvgpr_read_b32 v195, a23\n" \
"v_cvt_pk_bf16_f32 v188, v188, v189\n" \
"v_mfma_f32_16x16x32_bf16 a[60:63], v[60:63], v[172:175], a[60:63]\n" \
"v_cvt_pk_bf16_f32 v189, v190, v191\n" \
"v_cvt_pk_bf16_f32 v190, v180, v181\n" \
"v_mfma_f32_16x16x32_bf16 a[60:63], v[64:67], v[176:179], a[60:63]\n" \
"v_cvt_pk_bf16_f32 v191, v182, v183\n" \
"v_accvgpr_read_b32 v180, a52\n" \
"v_mfma_f32_16x16x32_bf16 a[92:95], v[72:75], v[176:179], a[92:95]\n" \
"v_accvgpr_read_b32 v181, a53\n" \
"v_accvgpr_read_b32 v182, a54\n" \
"v_mfma_f32_16x16x32_bf16 a[92:95], v[68:71], v[172:175], a[92:95]\n" \
"v_accvgpr_read_b32 v183, a55\n" \
"v_accvgpr_read_b32 v196, a84\n" \
"v_mfma_f32_16x16x32_bf16 a[124:127], v[76:79], v[172:175], a[124:127]\n" \
"v_accvgpr_read_b32 v197, a85\n" \
"v_accvgpr_read_b32 v198, a86\n" \
"v_mfma_f32_16x16x32_bf16 a[124:127], v[80:83], v[176:179], a[124:127]\n" \
"v_accvgpr_read_b32 v199, a87\n" \
"v_cvt_pk_bf16_f32 v192, v192, v193\n" \
"v_mfma_f32_16x16x32_bf16 a[0:3], v[120:123], v[56:59], a[0:3]\n" \
"v_cvt_pk_bf16_f32 v193, v194, v195\n" \
"v_cvt_pk_bf16_f32 v194, v180, v181\n" \
"v_mfma_f32_16x16x32_bf16 a[0:3], v[116:119], v[52:55], a[0:3]\n" \
"v_cvt_pk_bf16_f32 v195, v182, v183\n" \
"v_accvgpr_read_b32 v180, a116\n" \
"v_mfma_f32_16x16x32_bf16 a[32:35], v[116:119], v[60:63], a[32:35]\n" \
"v_accvgpr_read_b32 v181, a117\n" \
"v_accvgpr_read_b32 v182, a118\n" \
"v_mfma_f32_16x16x32_bf16 a[32:35], v[120:123], v[64:67], a[32:35]\n" \
"v_accvgpr_read_b32 v183, a119\n" \
"v_accvgpr_write_b32 a116, 0\n" \
"v_mfma_f32_16x16x32_bf16 a[64:67], v[120:123], v[72:75], a[64:67]\n" \
"v_accvgpr_write_b32 a117, 0\n" \
"v_accvgpr_write_b32 a118, 0\n" \
"v_mfma_f32_16x16x32_bf16 a[64:67], v[116:119], v[68:71], a[64:67]\n" \
"v_accvgpr_write_b32 a119, 0\n" \
"v_accvgpr_read_b32 v200, a24\n" \
"v_mfma_f32_16x16x32_bf16 a[96:99], v[116:119], v[76:79], a[96:99]\n" \
"v_accvgpr_read_b32 v201, a25\n" \
"v_accvgpr_read_b32 v202, a26\n" \
"v_mfma_f32_16x16x32_bf16 a[96:99], v[120:123], v[80:83], a[96:99]\n" \
"v_accvgpr_read_b32 v203, a27\n" \
"v_cvt_pk_bf16_f32 v196, v196, v197\n" \
"v_mfma_f32_16x16x32_bf16 a[4:7], v[128:131], v[56:59], a[4:7]\n" \
"v_cvt_pk_bf16_f32 v197, v198, v199\n" \
"v_cvt_pk_bf16_f32 v198, v180, v181\n" \
"v_mfma_f32_16x16x32_bf16 a[4:7], v[124:127], v[52:55], a[4:7]\n" \
"v_cvt_pk_bf16_f32 v199, v182, v183\n" \
"v_accvgpr_read_b32 v180, a56\n" \
"v_mfma_f32_16x16x32_bf16 a[36:39], v[124:127], v[60:63], a[36:39]\n" \
"v_accvgpr_read_b32 v181, a57\n" \
"v_accvgpr_read_b32 v182, a58\n" \
"v_mfma_f32_16x16x32_bf16 a[36:39], v[128:131], v[64:67], a[36:39]\n" \
"v_accvgpr_read_b32 v183, a59\n" \
"v_accvgpr_read_b32 v204, a88\n" \
"v_mfma_f32_16x16x32_bf16 a[68:71], v[128:131], v[72:75], a[68:71]\n" \
"v_accvgpr_read_b32 v205, a89\n" \
"v_accvgpr_read_b32 v206, a90\n" \
"v_mfma_f32_16x16x32_bf16 a[68:71], v[124:127], v[68:71], a[68:71]\n" \
"v_accvgpr_read_b32 v207, a91\n" \
"v_cvt_pk_bf16_f32 v200, v200, v201\n" \
"v_mfma_f32_16x16x32_bf16 a[100:103], v[124:127], v[76:79], a[100:103]\n" \
"v_cvt_pk_bf16_f32 v201, v202, v203\n" \
"v_cvt_pk_bf16_f32 v202, v180, v181\n" \
"v_mfma_f32_16x16x32_bf16 a[100:103], v[128:131], v[80:83], a[100:103]\n" \
"v_cvt_pk_bf16_f32 v203, v182, v183\n" \
"s_waitcnt lgkmcnt(0)\n" \
"s_nop 0\n" \
"v_mfma_f32_16x16x32_bf16 a[8:11], v[136:139], v[56:59], a[8:11]\n" \
"v_accvgpr_read_b32 v180, a120\n" \
"v_accvgpr_read_b32 v181, a121\n" \
"v_mfma_f32_16x16x32_bf16 a[8:11], v[132:135], v[52:55], a[8:11]\n" \
"v_accvgpr_read_b32 v182, a122\n" \
"v_accvgpr_read_b32 v183, a123\n" \
"v_mfma_f32_16x16x32_bf16 a[40:43], v[132:135], v[60:63], a[40:43]\n" \
"v_accvgpr_write_b32 a120, 0\n" \
"v_accvgpr_write_b32 a121, 0\n" \
"v_mfma_f32_16x16x32_bf16 a[40:43], v[136:139], v[64:67], a[40:43]\n" \
"v_accvgpr_write_b32 a122, 0\n" \
"v_accvgpr_write_b32 a123, 0\n" \
"v_mfma_f32_16x16x32_bf16 a[72:75], v[136:139], v[72:75], a[72:75]\n" \
"v_accvgpr_read_b32 v208, a28\n" \
"v_accvgpr_read_b32 v209, a29\n" \
"v_mfma_f32_16x16x32_bf16 a[72:75], v[132:135], v[68:71], a[72:75]\n" \
"v_accvgpr_read_b32 v210, a30\n" \
"v_accvgpr_read_b32 v211, a31\n" \
"v_mfma_f32_16x16x32_bf16 a[104:107], v[132:135], v[76:79], a[104:107]\n" \
"v_cvt_pk_bf16_f32 v204, v204, v205\n" \
"v_cvt_pk_bf16_f32 v205, v206, v207\n" \
"v_mfma_f32_16x16x32_bf16 a[104:107], v[136:139], v[80:83], a[104:107]\n" \
"v_cvt_pk_bf16_f32 v206, v180, v181\n" \
"v_cvt_pk_bf16_f32 v207, v182, v183\n" \
"v_mfma_f32_16x16x32_bf16 a[12:15], v[144:147], v[56:59], a[12:15]\n" \
"v_accvgpr_read_b32 v180, a60\n" \
"v_accvgpr_read_b32 v181, a61\n" \
"v_mfma_f32_16x16x32_bf16 a[12:15], v[140:143], v[52:55], a[12:15]\n" \
"v_accvgpr_read_b32 v182, a62\n" \
"v_accvgpr_read_b32 v183, a63\n" \
"v_mfma_f32_16x16x32_bf16 a[44:47], v[140:143], v[60:63], a[44:47]\n" \
"v_accvgpr_read_b32 v212, a92\n" \
"v_accvgpr_read_b32 v213, a93\n" \
"v_mfma_f32_16x16x32_bf16 a[44:47], v[144:147], v[64:67], a[44:47]\n" \
"v_accvgpr_read_b32 v214, a94\n" \
"v_accvgpr_read_b32 v215, a95\n" \
"v_mfma_f32_16x16x32_bf16 a[76:79], v[144:147], v[72:75], a[76:79]\n" \
"v_cvt_pk_bf16_f32 v208, v208, v209\n" \
"v_cvt_pk_bf16_f32 v209, v210, v211\n" \
"v_mfma_f32_16x16x32_bf16 a[76:79], v[140:143], v[68:71], a[76:79]\n" \
"v_cvt_pk_bf16_f32 v210, v180, v181\n" \
"v_cvt_pk_bf16_f32 v211, v182, v183\n" \
"v_mfma_f32_16x16x32_bf16 a[108:111], v[140:143], v[76:79], a[108:111]\n" \
"v_accvgpr_read_b32 v180, a124\n" \
"v_accvgpr_read_b32 v181, a125\n" \
"v_mfma_f32_16x16x32_bf16 a[108:111], v[144:147], v[80:83], a[108:111]\n" \
"v_accvgpr_read_b32 v182, a126\n" \
"v_accvgpr_read_b32 v183, a127\n" \
"s_waitcnt vmcnt(0)\n" \
"s_barrier\n" \
"v_mfma_f32_16x16x32_bf16 a[128:131], v[120:123], v[88:91], a[128:131]\n" \
"s_add_u32 m0, s40, 0x8000\n" \
"s_add_u32 s85, s72, 0x0\n" \
"s_nop 0\n" \
"v_mfma_f32_16x16x32_bf16 a[128:131], v[116:119], v[84:87], a[128:131]\n" \
"buffer_load_dwordx4 v20, s[80:83], s85 offen sc1 lds\n" \
"v_mfma_f32_16x16x32_bf16 a[160:163], v[116:119], v[92:95], a[160:163]\n" \
"s_add_u32 m0, s40, 0x9000\n" \
"s_add_u32 s84, s72, 0x1000\n" \
"v_mfma_f32_16x16x32_bf16 a[160:163], v[120:123], v[96:99], a[160:163]\n" \
"ds_read_b128 v[52:55], v28 offset:0\n" \
"ds_read_b128 v[56:59], v29 offset:0\n" \
"v_mfma_f32_16x16x32_bf16 a[192:195], v[120:123], v[104:107], a[192:195]\n" \
"buffer_load_dwordx4 v20, s[80:83], s84 offen sc1 lds\n" \
"v_mfma_f32_16x16x32_bf16 a[192:195], v[116:119], v[100:103], a[192:195]\n" \
"ds_read_b128 v[60:63], v28 offset:2048\n" \
"L_pf4:\n" \
"s_cbranch_execz L_pf8\n" \
"s_nop 0\n" \
"ds_read_b128 v[64:67], v29 offset:2048\n" \
"v_mfma_f32_16x16x32_bf16 a[224:227], v[116:119], v[108:111], a[224:227]\n" \
"s_add_u32 m0, s40, 0xb000\n" \
"s_add_u32 s85, s62, 0x0\n" \
"v_mfma_f32_16x16x32_bf16 a[224:227], v[120:123], v[112:115], a[224:227]\n" \
"buffer_load_dwordx4 v21, s[76:79], s85 offen lds\n" \
"v_mfma_f32_16x16x32_bf16 a[132:135], v[128:131], v[88:91], a[132:135]\n" \
"ds_read_b64_tr_b16 v[116:117], v36 offset:0\n" \
"ds_read_b64_tr_b16 v[118:119], v36 offset:512\n" \
"v_mfma_f32_16x16x32_bf16 a[132:135], v[124:127], v[84:87], a[132:135]\n" \
"ds_read_b64_tr_b16 v[120:121], v36 offset:4096\n" \
"ds_read_b64_tr_b16 v[122:123], v36 offset:4608\n" \
"v_mfma_f32_16x16x32_bf16 a[164:167], v[124:127], v[92:95], a[164:167]\n" \
"s_add_u32 m0, %[mA], 0x8000\n" \
"s_add_u32 s84, s62, 0x0\n" \
"v_mfma_f32_16x16x32_bf16 a[164:167], v[128:131], v[96:99], a[164:167]\n" \
"buffer_load_dwordx4 v24, s[76:79], s84 offen lds\n" \
"v_mfma_f32_16x16x32_bf16 a[196:199], v[128:131], v[104:107], a[196:199]\n" \
"ds_read_b128 v[68:71], v28 offset:4096\n" \
"ds_read_b128 v[72:75], v29 offset:4096\n" \
"v_mfma_f32_16x16x32_bf16 a[196:199], v[124:127], v[100:103], a[196:199]\n" \
"s_add_u32 m0, %[mA], 0x8400\n" \
"s_add_u32 s85, s62, 0x2400\n" \
"v_mfma_f32_16x16x32_bf16 a[228:231], v[124:127], v[108:111], a[228:231]\n" \
"buffer_load_dwordx4 v25, s[76:79], s85 offen lds\n" \
"v_mfma_f32_16x16x32_bf16 a[228:231], v[128:131], v[112:115], a[228:231]\n" \
"ds_read_b64_tr_b16 v[128:129], v37 offset:4096\n" \
"ds_read_b64_tr_b16 v[130:131], v37 offset:4608\n" \
"v_mfma_f32_16x16x32_bf16 a[136:139], v[136:139], v[88:91], a[136:139]\n" \
"ds_read_b64_tr_b16 v[124:125], v37 offset:0\n" \
"ds_read_b64_tr_b16 v[126:127], v37 offset:512\n" \
"v_mfma_f32_16x16x32_bf16 a[136:139], v[132:135], v[84:87], a[136:139]\n" \
"s_add_u32 m0, %[mB], 0x8000\n" \
"s_add_u32 s84, s63, 0x0\n" \
"s_nop 0\n" \
"v_mfma_f32_16x16x32_bf16 a[168:171], v[132:135], v[92:95], a[168:171]\n" \
"buffer_load_dwordx4 v26, s[68:71], s84 offen lds\n" \
"v_mfma_f32_16x16x32_bf16 a[168:171], v[136:139], v[96:99], a[168:171]\n" \
"ds_read_b128 v[76:79], v28 offset:6144\n" \
"ds_read_b128 v[80:83], v29 offset:6144\n" \
"v_mfma_f32_16x16x32_bf16 a[200:203], v[136:139], v[104:107], a[200:203]\n" \
"v_accvgpr_write_b32 a124, 0\n" \
"v_accvgpr_write_b32 a125, 0\n" \
"v_mfma_f32_16x16x32_bf16 a[200:203], v[132:135], v[100:103], a[200:203]\n" \
"s_add_u32 m0, %[mA], 0x8800\n" \
"s_add_u32 s85, s62, 0x4800\n" \
"v_mfma_f32_16x16x32_bf16 a[232:235], v[132:135], v[108:111], a[232:235]\n" \
"buffer_load_dwordx4 v24, s[76:79], s85 offen lds\n" \
"v_mfma_f32_16x16x32_bf16 a[232:235], v[136:139], v[112:115], a[232:235]\n" \
"ds_read_b64_tr_b16 v[132:133], v38 offset:0\n" \
"ds_read_b64_tr_b16 v[134:135], v38 offset:512\n" \
"v_mfma_f32_16x16x32_bf16 a[140:143], v[144:147], v[88:91], a[140:143]\n" \
"s_add_u32 m0, %[mA], 0x8c00\n" \
"s_add_u32 s84, s62, 0x6c00\n" \
"v_mfma_f32_16x16x32_bf16 a[140:143], v[140:143], v[84:87], a[140:143]\n" \
"buffer_load_dwordx4 v25, s[76:79], s84 offen lds\n" \
"v_mfma_f32_16x16x32_bf16 a[172:175], v[140:143], v[92:95], a[172:175]\n" \
"ds_read_b64_tr_b16 v[136:137], v38 offset:4096\n" \
"ds_read_b64_tr_b16 v[138:139], v38 offset:4608\n" \
"v_mfma_f32_16x16x32_bf16 a[172:175], v[144:147], v[96:99], a[172:175]\n" \
"v_accvgpr_write_b32 a126, 0\n" \
"v_accvgpr_write_b32 a127, 0\n" \
"v_mfma_f32_16x16x32_bf16 a[204:207], v[144:147], v[104:107], a[204:207]\n" \
"s_add_u32 m0, %[mB], 0x8400\n" \
"s_add_u32 s85, s63, 0x8000\n" \
"v_mfma_f32_16x16x32_bf16 a[204:207], v[140:143], v[100:103], a[204:207]\n" \
"buffer_load_dwordx4 v26, s[68:71], s85 offen lds\n" \
"v_mfma_f32_16x16x32_bf16 a[236:239], v[140:143], v[108:111], a[236:239]\n" \
"ds_read_b64_tr_b16 v[140:141], v39 offset:0\n" \
"ds_read_b64_tr_b16 v[142:143], v39 offset:960\n" \
"v_mfma_f32_16x16x32_bf16 a[236:239], v[144:147], v[112:115], a[236:239]\n" \
"v_accvgpr_read_b32 v216, a0\n" \
"v_accvgpr_read_b32 v217, a1\n" \
"v_mfma_f32_16x16x32_bf16 a[144:147], v[88:91], v[152:155], a[144:147]\n" \
"s_add_u32 m0, %[mA], 0x9000\n" \
"s_add_u32 s84, s62, 0x9000\n" \
"v_mfma_f32_16x16x32_bf16 a[144:147], v[84:87], v[148:151], a[144:147]\n" \
"buffer_load_dwordx4 v24, s[76:79], s84 offen lds\n" \
"v_mfma_f32_16x16x32_bf16 a[148:151], v[84:87], v[156:159], a[148:151]\n" \
"v_accvgpr_read_b32 v218, a2\n" \
"v_accvgpr_read_b32 v219, a3\n" \
"v_mfma_f32_16x16x32_bf16 a[148:151], v[88:91], v[160:163], a[148:151]\n" \
"s_add_u32 m0, %[mA], 0x9400\n" \
"s_add_u32 s85, s62, 0xb400\n" \
"v_mfma_f32_16x16x32_bf16 a[152:155], v[88:91], v[168:171], a[152:155]\n" \
"buffer_load_dwordx4 v25, s[76:79], s85 offen lds\n" \
"v_mfma_f32_16x16x32_bf16 a[152:155], v[84:87], v[164:167], a[152:155]\n" \
"v_cvt_pk_bf16_f32 v212, v212, v213\n" \
"v_cvt_pk_bf16_f32 v213, v214, v215\n" \
"v_mfma_f32_16x16x32_bf16 a[156:159], v[84:87], v[172:175], a[156:159]\n" \
"ds_read_b128 v[84:87], v22 offset:0\n" \
"v_cvt_pk_bf16_f32 v214, v180, v181\n" \
"v_mfma_f32_16x16x32_bf16 a[156:159], v[88:91], v[176:179], a[156:159]\n" \
"s_add_u32 m0, %[mB], 0x8800\n" \
"s_add_u32 s84, s63, 0x10000\n" \
"v_mfma_f32_16x16x32_bf16 a[176:179], v[96:99], v[152:155], a[176:179]\n" \
"buffer_load_dwordx4 v27, s[68:71], s84 offen lds\n" \
"v_mfma_f32_16x16x32_bf16 a[176:179], v[92:95], v[148:151], a[176:179]\n" \
"ds_read_b128 v[88:91], v23 offset:0\n" \
"v_cvt_pk_bf16_f32 v215, v182, v183\n" \
"v_mfma_f32_16x16x32_bf16 a[180:183], v[92:95], v[156:159], a[180:183]\n" \
"v_accvgpr_read_b32 v180, a4\n" \
"v_accvgpr_read_b32 v181, a5\n" \
"v_mfma_f32_16x16x32_bf16 a[180:183], v[96:99], v[160:163], a[180:183]\n" \
"s_add_u32 m0, %[mA], 0x9800\n" \
"s_add_u32 s85, s62, 0xd800\n" \
"v_mfma_f32_16x16x32_bf16 a[184:187], v[96:99], v[168:171], a[184:187]\n" \
"buffer_load_dwordx4 v24, s[76:79], s85 offen lds\n" \
"v_mfma_f32_16x16x32_bf16 a[184:187], v[92:95], v[164:167], a[184:187]\n" \
"v_accvgpr_read_b32 v182, a6\n" \
"v_accvgpr_read_b32 v183, a7\n" \
"v_mfma_f32_16x16x32_bf16 a[188:191], v[92:95], v[172:175], a[188:191]\n" \
"v_cvt_pk_bf16_f32 v216, v216, v217\n" \
"v_cvt_pk_bf16_f32 v217, v218, v219\n" \
"v_mfma_f32_16x16x32_bf16 a[188:191], v[96:99], v[176:179], a[188:191]\n" \
"v_accvgpr_read_b32 v220, a32\n" \
"v_accvgpr_read_b32 v221, a33\n" \
"v_mfma_f32_16x16x32_bf16 a[208:211], v[104:107], v[152:155], a[208:211]\n" \
"v_cvt_pk_bf16_f32 v218, v180, v181\n" \
"v_cvt_pk_bf16_f32 v219, v182, v183\n" \
"v_mfma_f32_16x16x32_bf16 a[208:211], v[100:103], v[148:151], a[208:211]\n" \
"v_accvgpr_read_b32 v222, a34\n" \
"v_accvgpr_read_b32 v223, a35\n" \
"v_mfma_f32_16x16x32_bf16 a[212:215], v[100:103], v[156:159], a[212:215]\n" \
"v_cvt_pk_bf16_f32 v220, v220, v221\n" \
"v_accvgpr_read_b32 v180, a36\n" \
"v_mfma_f32_16x16x32_bf16 a[212:215], v[104:107], v[160:163], a[212:215]\n" \
"v_accvgpr_read_b32 v181, a37\n" \
"v_accvgpr_read_b32 v182, a38\n" \
"v_mfma_f32_16x16x32_bf16 a[216:219], v[104:107], v[168:171], a[216:219]\n" \
"v_accvgpr_read_b32 v183, a39\n" \
"v_cvt_pk_bf16_f32 v221, v222, v223\n" \
"v_mfma_f32_16x16x32_bf16 a[216:219], v[100:103], v[164:167], a[216:219]\n" \
"v_accvgpr_read_b32 v224, a64\n" \
"v_accvgpr_read_b32 v225, a65\n" \
"v_mfma_f32_16x16x32_bf16 a[220:223], v[100:103], v[172:175], a[220:223]\n" \
"v_cvt_pk_bf16_f32 v222, v180, v181\n" \
"v_cvt_pk_bf16_f32 v223, v182, v183\n" \
"v_mfma_f32_16x16x32_bf16 a[220:223], v[104:107], v[176:179], a[220:223]\n" \
"v_accvgpr_read_b32 v226, a66\n" \
"v_accvgpr_read_b32 v227, a67\n" \
"v_mfma_f32_16x16x32_bf16 a[240:243], v[112:115], v[152:155], a[240:243]\n" \
"ds_read_b64_tr_b16 v[152:153], v32 offset:8192\n" \
"ds_read_b64_tr_b16 v[154:155], v32 offset:9216\n" \
"v_mfma_f32_16x16x32_bf16 a[240:243], v[108:111], v[148:151], a[240:243]\n" \
"ds_read_b64_tr_b16 v[148:149], v32 offset:0\n" \
"ds_read_b64_tr_b16 v[150:151], v32 offset:1024\n" \
"v_mfma_f32_16x16x32_bf16 a[244:247], v[108:111], v[156:159], a[244:247]\n" \
"ds_read_b64_tr_b16 v[156:157], v33 offset:0\n" \
"ds_read_b64_tr_b16 v[158:159], v33 offset:1024\n" \
"v_mfma_f32_16x16x32_bf16 a[244:247], v[112:115], v[160:163], a[244:247]\n" \
"ds_read_b64_tr_b16 v[160:161], v33 offset:8192\n" \
"ds_read_b64_tr_b16 v[162:163], v33 offset:9216\n" \
"v_mfma_f32_16x16x32_bf16 a[248:251], v[112:115], v[168:171], a[248:251]\n" \
"ds_read_b64_tr_b16 v[168:169], v34 offset:8192\n" \
"ds_read_b64_tr_b16 v[170:171], v34 offset:9216\n" \
"v_mfma_f32_16x16x32_bf16 a[248:251], v[108:111], v[164:167], a[248:251]\n" \
"ds_read_b64_tr_b16 v[164:165], v34 offset:0\n" \
"ds_read_b64_tr_b16 v[166:167], v34 offset:1024\n" \
"v_mfma_f32_16x16x32_bf16 a[252:255], v[108:111], v[172:175], a[252:255]\n" \
"ds_read_b64_tr_b16 v[172:173], v35 offset:0\n" \
"ds_read_b64_tr_b16 v[174:175], v35 offset:1024\n" \
"v_mfma_f32_16x16x32_bf16 a[252:255], v[112:115], v[176:179], a[252:255]\n" \
"s_add_u32 m0, %[mA], 0x9c00\n" \
"s_waitcnt lgkmcnt(14)\n" \
"s_nop 0\n" \
"v_smfmac_f32_16x16x64_bf16 a[16:19], v[140:143], v[84:91], v50\n" \
"v_mfma_f32_16x16x32_bf16 a[48:51], v[128:131], v[88:91], 0\n" \
"s_add_u32 s85, s62, 0xfc00\n" \
"ds_read_b64_tr_b16 v[176:177], v35 offset:8192\n" \
"v_mfma_f32_16x16x32_bf16 a[48:51], v[124:127], v[84:87], a[48:51]\n" \
"buffer_load_dwordx4 v25, s[76:79], s85 offen lds\n" \
"v_mfma_f32_16x16x32_bf16 a[80:83], v[84:87], v[132:135], 0\n" \
"s_add_u32 m0, %[mB], 0x8c00\n" \
"s_add_u32 s84, s63, 0x18000\n" \
"v_mfma_f32_16x16x32_bf16 a[80:83], v[88:91], v[136:139], a[80:83]\n" \
"ds_read_b64_tr_b16 v[178:179], v35 offset:9216\n" \
"v_cvt_pk_bf16_f32 v224, v224, v225\n" \
"v_smfmac_f32_16x16x64_bf16 a[112:115], v[140:143], v[52:59], v50\n" \
"v_mfma_f32_16x16x32_bf16 a[20:23], v[128:131], v[56:59], 0\n" \
"buffer_load_dwordx4 v27, s[68:71], s84 offen lds\n" \
"v_mfma_f32_16x16x32_bf16 a[20:23], v[124:127], v[52:55], a[20:23]\n" \
"s_add_u32 s88, s44, 0x0\n" \
"s_nop 0\n" \
"v_accvgpr_read_b32 v180, a68\n" \
"v_mfma_f32_16x16x32_bf16 a[52:55], v[116:119], v[52:55], 0\n" \
"v_accvgpr_read_b32 v181, a69\n" \
"v_accvgpr_read_b32 v182, a70\n" \
"v_mfma_f32_16x16x32_bf16 a[52:55], v[120:123], v[56:59], a[52:55]\n" \
"buffer_store_dwordx4 v[184:187], v20, s[80:83], s88 offen offset:0 sc1\n" \
"v_mfma_f32_16x16x32_bf16 a[84:87], v[56:59], v[136:139], 0\n" \
"v_accvgpr_read_b32 v183, a71\n" \
"v_cvt_pk_bf16_f32 v225, v226, v227\n" \
"v_mfma_f32_16x16x32_bf16 a[84:87], v[52:55], v[132:135], a[84:87]\n" \
"v_accvgpr_read_b32 v228, a96\n" \
"v_accvgpr_read_b32 v229, a97\n" \
"v_smfmac_f32_16x16x64_bf16 a[116:119], v[140:143], v[60:67], v50\n" \
"v_mfma_f32_16x16x32_bf16 a[24:27], v[124:127], v[60:63], 0\n" \
"v_cvt_pk_bf16_f32 v226, v180, v181\n" \
"v_cvt_pk_bf16_f32 v227, v182, v183\n" \
"v_mfma_f32_16x16x32_bf16 a[24:27], v[128:131], v[64:67], a[24:27]\n" \
"s_add_u32 s89, s44, 0x1000\n" \
"v_accvgpr_read_b32 v230, a98\n" \
"v_mfma_f32_16x16x32_bf16 a[56:59], v[120:123], v[64:67], 0\n" \
"buffer_store_dwordx4 v[188:191], v20, s[80:83], s89 offen offset:0 sc1\n" \
"v_mfma_f32_16x16x32_bf16 a[56:59], v[116:119], v[60:63], a[56:59]\n" \
"v_accvgpr_read_b32 v231, a99\n" \
"v_cvt_pk_bf16_f32 v228, v228, v229\n" \
"v_mfma_f32_16x16x32_bf16 a[88:91], v[60:63], v[132:135], 0\n" \
"v_accvgpr_read_b32 v180, a100\n" \
"v_accvgpr_read_b32 v181, a101\n" \
"v_mfma_f32_16x16x32_bf16 a[88:91], v[64:67], v[136:139], a[88:91]\n" \
"v_accvgpr_read_b32 v182, a102\n" \
"v_accvgpr_read_b32 v183, a103\n" \
"v_smfmac_f32_16x16x64_bf16 a[120:123], v[140:143], v[68:75], v50\n" \
"v_mfma_f32_16x16x32_bf16 a[28:31], v[128:131], v[72:75], 0\n" \
"v_cvt_pk_bf16_f32 v229, v230, v231\n" \
"v_accvgpr_read_b32 v232, a8\n" \
"v_mfma_f32_16x16x32_bf16 a[28:31], v[124:127], v[68:71], a[28:31]\n" \
"v_accvgpr_read_b32 v233, a9\n" \
"v_cvt_pk_bf16_f32 v230, v180, v181\n" \
"v_mfma_f32_16x16x32_bf16 a[60:63], v[116:119], v[68:71], 0\n" \
"s_add_u32 s88, s44, 0x0\n" \
"s_nop 0\n" \
"v_cvt_pk_bf16_f32 v231, v182, v183\n" \
"v_mfma_f32_16x16x32_bf16 a[60:63], v[120:123], v[72:75], a[60:63]\n" \
"buffer_store_dwordx4 v[192:195], v20, s[80:83], s88 offen offset:1024 sc1\n" \
"v_mfma_f32_16x16x32_bf16 a[92:95], v[72:75], v[136:139], 0\n" \
"v_accvgpr_read_b32 v234, a10\n" \
"v_accvgpr_read_b32 v235, a11\n" \
"v_mfma_f32_16x16x32_bf16 a[92:95], v[68:71], v[132:135], a[92:95]\n" \
"v_cvt_pk_bf16_f32 v232, v232, v233\n" \
"v_accvgpr_read_b32 v180, a12\n" \
"v_smfmac_f32_16x16x64_bf16 a[124:127], v[140:143], v[76:83], v50\n" \
"v_mfma_f32_16x16x32_bf16 a[0:3], v[124:127], v[76:79], 0\n" \
"v_accvgpr_read_b32 v181, a13\n" \
"v_accvgpr_read_b32 v182, a14\n" \
"v_mfma_f32_16x16x32_bf16 a[0:3], v[128:131], v[80:83], a[0:3]\n" \
"v_accvgpr_read_b32 v183, a15\n" \
"v_cvt_pk_bf16_f32 v233, v234, v235\n" \
"v_mfma_f32_16x16x32_bf16 a[4:7], v[120:123], v[80:83], 0\n" \
"v_accvgpr_read_b32 v236, a40\n" \
"v_accvgpr_read_b32 v237, a41\n" \
"v_mfma_f32_16x16x32_bf16 a[4:7], v[116:119], v[76:79], a[4:7]\n" \
"v_cvt_pk_bf16_f32 v234, v180, v181\n" \
"v_cvt_pk_bf16_f32 v235, v182, v183\n" \
"v_mfma_f32_16x16x32_bf16 a[32:35], v[76:79], v[132:135], 0\n" \
"v_accvgpr_read_b32 v238, a42\n" \
"v_accvgpr_read_b32 v239, a43\n" \
"v_mfma_f32_16x16x32_bf16 a[32:35], v[80:83], v[136:139], a[32:35]\n" \
"v_cvt_pk_bf16_f32 v236, v236, v237\n" \
"v_accvgpr_read_b32 v180, a44\n" \
"v_accvgpr_read_b32 v181, a45\n" \
"v_accvgpr_read_b32 v182, a46\n" \
"v_accvgpr_read_b32 v183, a47\n" \
"v_cvt_pk_bf16_f32 v237, v238, v239\n" \
"v_accvgpr_read_b32 v240, a72\n" \
"v_accvgpr_read_b32 v241, a73\n" \
"v_cvt_pk_bf16_f32 v238, v180, v181\n" \
"v_cvt_pk_bf16_f32 v239, v182, v183\n" \
"v_accvgpr_read_b32 v242, a74\n" \
"v_accvgpr_read_b32 v243, a75\n" \
"v_cvt_pk_bf16_f32 v240, v240, v241\n" \
"v_accvgpr_read_b32 v180, a76\n" \
"v_accvgpr_read_b32 v181, a77\n" \
"v_accvgpr_read_b32 v182, a78\n" \
"v_accvgpr_read_b32 v183, a79\n" \
"v_cvt_pk_bf16_f32 v241, v242, v243\n" \
"v_accvgpr_read_b32 v244, a104\n" \
"v_accvgpr_read_b32 v245, a105\n" \
"v_cvt_pk_bf16_f32 v242, v180, v181\n" \
"v_cvt_pk_bf16_f32 v243, v182, v183\n" \
"v_accvgpr_read_b32 v246, a106\n" \
"v_accvgpr_read_b32 v247, a107\n" \
"v_cvt_pk_bf16_f32 v244, v244, v245\n" \
"v_accvgpr_read_b32 v248, a128\n" \
"v_accvgpr_read_b32 v249, a129\n" \
"v_accvgpr_read_b32 v250, a130\n" \
"v_accvgpr_read_b32 v251, a131\n" \
"s_waitcnt lgkmcnt(0)\n" \
"s_nop 0\n" \
"v_mfma_f32_16x16x32_bf16 a[128:131], v[152:155], v[56:59], 0\n" \
"v_accvgpr_read_b32 v180, a108\n" \
"v_accvgpr_read_b32 v181, a109\n" \
"v_accvgpr_read_b32 v182, a110\n" \
"v_accvgpr_read_b32 v183, a111\n" \
"v_cvt_pk_bf16_f32 v245, v246, v247\n" \
"v_cvt_pk_bf16_f32 v246, v180, v181\n" \
"v_cvt_pk_bf16_f32 v247, v182, v183\n" \
"v_cvt_pk_bf16_f32 v248, v248, v249\n" \
"v_mfma_f32_16x16x32_bf16 a[128:131], v[148:151], v[52:55], a[128:131]\n" \
"v_accvgpr_read_b32 v180, a132\n" \
"v_accvgpr_read_b32 v181, a133\n" \
"v_accvgpr_read_b32 v182, a134\n" \
"v_accvgpr_read_b32 v183, a135\n" \
"v_cvt_pk_bf16_f32 v249, v250, v251\n" \
"v_accvgpr_read_b32 v252, a160\n" \
"v_accvgpr_read_b32 v253, a161\n" \
"v_cvt_pk_bf16_f32 v250, v180, v181\n" \
"v_cvt_pk_bf16_f32 v251, v182, v183\n" \
"v_accvgpr_read_b32 v254, a162\n" \
"v_accvgpr_read_b32 v255, a163\n" \
"v_cvt_pk_bf16_f32 v252, v252, v253\n" \
"v_accvgpr_read_b32 v180, a164\n" \
"v_accvgpr_read_b32 v181, a165\n" \
"v_accvgpr_read_b32 v182, a166\n" \
"v_accvgpr_read_b32 v183, a167\n" \
"v_cvt_pk_bf16_f32 v253, v254, v255\n" \
"v_cvt_pk_bf16_f32 v254, v180, v181\n" \
"v_cvt_pk_bf16_f32 v255, v182, v183\n" \
"v_accvgpr_write_b32 a36, v252\n" \
"v_accvgpr_write_b32 a37, v253\n" \
"v_mfma_f32_16x16x32_bf16 a[132:135], v[156:159], v[52:55], 0\n" \
"v_accvgpr_write_b32 a38, v254\n" \
"v_accvgpr_write_b32 a39, v255\n" \
"v_accvgpr_read_b32 v252, a192\n" \
"v_accvgpr_read_b32 v253, a193\n" \
"v_accvgpr_read_b32 v254, a194\n" \
"v_accvgpr_read_b32 v255, a195\n" \
"v_cvt_pk_bf16_f32 v252, v252, v253\n" \
"v_accvgpr_read_b32 v180, a196\n" \
"v_accvgpr_read_b32 v181, a197\n" \
"v_accvgpr_read_b32 v182, a198\n" \
"v_accvgpr_read_b32 v183, a199\n" \
"v_cvt_pk_bf16_f32 v253, v254, v255\n" \
"v_cvt_pk_bf16_f32 v254, v180, v181\n" \
"v_cvt_pk_bf16_f32 v255, v182, v183\n" \
"v_accvgpr_write_b32 a64, v252\n" \
"v_accvgpr_write_b32 a65, v253\n" \
"v_mfma_f32_16x16x32_bf16 a[132:135], v[160:163], v[56:59], a[132:135]\n" \
"v_accvgpr_write_b32 a66, v254\n" \
"v_accvgpr_write_b32 a67, v255\n" \
"v_accvgpr_read_b32 v252, a224\n" \
"v_accvgpr_read_b32 v253, a225\n" \
"v_accvgpr_read_b32 v254, a226\n" \
"v_accvgpr_read_b32 v255, a227\n" \
"v_cvt_pk_bf16_f32 v252, v252, v253\n" \
"v_accvgpr_read_b32 v180, a228\n" \
"v_accvgpr_read_b32 v181, a229\n" \
"v_accvgpr_read_b32 v182, a230\n" \
"v_accvgpr_read_b32 v183, a231\n" \
"v_cvt_pk_bf16_f32 v253, v254, v255\n" \
"v_cvt_pk_bf16_f32 v254, v180, v181\n" \
"v_cvt_pk_bf16_f32 v255, v182, v183\n" \
"v_accvgpr_write_b32 a68, v252\n" \
"v_accvgpr_write_b32 a69, v253\n" \
"v_accvgpr_write_b32 a70, v254\n" \
"v_accvgpr_write_b32 a71, v255\n" \
"v_accvgpr_read_b32 v252, a136\n" \
"v_accvgpr_read_b32 v253, a137\n" \
"v_accvgpr_read_b32 v254, a138\n" \
"v_accvgpr_read_b32 v255, a139\n" \
"v_mfma_f32_16x16x32_bf16 a[136:139], v[168:171], v[56:59], 0\n" \
"ds_read_b128 v[84:87], v48 offset:0\n" \
"ds_read_b128 v[88:91], v49 offset:0\n" \
"ds_read_b128 v[92:95], v48 offset:2048\n" \
"v_mfma_f32_16x16x32_bf16 a[136:139], v[164:167], v[52:55], a[136:139]\n" \
"ds_read_b128 v[96:99], v49 offset:2048\n" \
"v_accvgpr_read_b32 v180, a140\n" \
"v_accvgpr_read_b32 v181, a141\n" \
"v_accvgpr_read_b32 v182, a142\n" \
"v_accvgpr_read_b32 v183, a143\n" \
"v_mfma_f32_16x16x32_bf16 a[140:143], v[172:175], v[52:55], 0\n" \
"ds_read_b128 v[100:103], v48 offset:4096\n" \
"ds_read_b128 v[104:107], v49 offset:4096\n" \
"s_add_u32 s89, s44, 0x1000\n" \
"buffer_store_dwordx4 v[196:199], v20, s[80:83], s89 offen offset:1024 sc1\n" \
"ds_read_b128 v[108:111], v48 offset:6144\n" \
"v_mfma_f32_16x16x32_bf16 a[140:143], v[176:179], v[56:59], a[140:143]\n" \
"v_cvt_pk_bf16_f32 v252, v252, v253\n" \
"v_cvt_pk_bf16_f32 v253, v254, v255\n" \
"v_mfma_f32_16x16x32_bf16 a[160:163], v[152:155], v[64:67], 0\n" \
"ds_read_b128 v[112:115], v49 offset:6144\n" \
"v_cvt_pk_bf16_f32 v254, v180, v181\n" \
"v_mfma_f32_16x16x32_bf16 a[160:163], v[148:151], v[60:63], a[160:163]\n" \
"v_cvt_pk_bf16_f32 v255, v182, v183\n" \
"v_accvgpr_write_b32 a96, v252\n" \
"v_mfma_f32_16x16x32_bf16 a[164:167], v[156:159], v[60:63], 0\n" \
"v_accvgpr_write_b32 a97, v253\n" \
"v_accvgpr_write_b32 a98, v254\n" \
"v_mfma_f32_16x16x32_bf16 a[164:167], v[160:163], v[64:67], a[164:167]\n" \
"v_accvgpr_write_b32 a99, v255\n" \
"v_accvgpr_read_b32 v252, a168\n" \
"v_accvgpr_read_b32 v253, a169\n" \
"v_accvgpr_read_b32 v254, a170\n" \
"v_accvgpr_read_b32 v255, a171\n" \
"v_mfma_f32_16x16x32_bf16 a[168:171], v[168:171], v[64:67], 0\n" \
"v_accvgpr_read_b32 v180, a172\n" \
"v_accvgpr_read_b32 v181, a173\n" \
"v_mfma_f32_16x16x32_bf16 a[168:171], v[164:167], v[60:63], a[168:171]\n" \
"v_accvgpr_read_b32 v182, a174\n" \
"v_accvgpr_read_b32 v183, a175\n" \
"v_mfma_f32_16x16x32_bf16 a[172:175], v[172:175], v[60:63], 0\n" \
"v_cvt_pk_bf16_f32 v252, v252, v253\n" \
"v_cvt_pk_bf16_f32 v253, v254, v255\n" \
"v_mfma_f32_16x16x32_bf16 a[172:175], v[176:179], v[64:67], a[172:175]\n" \
"v_cvt_pk_bf16_f32 v254, v180, v181\n" \
"v_cvt_pk_bf16_f32 v255, v182, v183\n" \
"v_mfma_f32_16x16x32_bf16 a[192:195], v[152:155], v[72:75], 0\n" \
"v_accvgpr_write_b32 a100, v252\n" \
"v_accvgpr_write_b32 a101, v253\n" \
"v_mfma_f32_16x16x32_bf16 a[192:195], v[148:151], v[68:71], a[192:195]\n" \
"v_accvgpr_write_b32 a102, v254\n" \
"v_accvgpr_write_b32 a103, v255\n" \
"s_add_u32 s62, s62, 0x80\n" \
"v_accvgpr_read_b32 v252, a200\n" \
"v_accvgpr_read_b32 v253, a201\n" \
"v_accvgpr_read_b32 v254, a202\n" \
"s_waitcnt vmcnt(4) lgkmcnt(0)\n" \
"s_barrier\n" \
"v_mfma_f32_16x16x32_bf16 a[196:199], v[156:159], v[68:71], 0\n" \
"v_accvgpr_read_b32 v255, a203\n" \
"s_add_u32 m0, s40, 0x0\n" \
"v_mfma_f32_16x16x32_bf16 a[196:199], v[160:163], v[72:75], a[196:199]\n" \
"s_cmp_lt_u32 s62, 0x480\n" \
"s_cselect_b32 s62, s62, 0\n" \
"v_mfma_f32_16x16x32_bf16 a[200:203], v[168:171], v[72:75], 0\n" \
"s_lshl_b32 s72, s62, 6\n" \
"s_add_u32 s72, s72, s37\n" \
"v_mfma_f32_16x16x32_bf16 a[200:203], v[164:167], v[68:71], a[200:203]\n" \
"s_add_u32 s85, s72, 0x0\n" \
"s_nop 0\n" \
"buffer_load_dwordx4 v20, s[80:83], s85 offen sc1 lds\n" \
"v_accvgpr_read_b32 v180, a204\n" \
"v_accvgpr_read_b32 v181, a205\n" \
"v_accvgpr_read_b32 v182, a206\n" \
"v_accvgpr_read_b32 v183, a207\n" \
"v_mfma_f32_16x16x32_bf16 a[204:207], v[172:175], v[68:71], 0\n" \
"s_add_u32 m0, s40, 0x1000\n" \
"s_add_u32 s84, s72, 0x1000\n" \
"v_mfma_f32_16x16x32_bf16 a[204:207], v[176:179], v[72:75], a[204:207]\n" \
"buffer_load_dwordx4 v20, s[80:83], s84 offen sc1 lds\n" \
"v_mfma_f32_16x16x32_bf16 a[224:227], v[152:155], v[80:83], 0\n" \
"v_cvt_pk_bf16_f32 v252, v252, v253\n" \
"v_cvt_pk_bf16_f32 v253, v254, v255\n" \
"v_mfma_f32_16x16x32_bf16 a[224:227], v[148:151], v[76:79], a[224:227]\n" \
"v_cvt_pk_bf16_f32 v254, v180, v181\n" \
"v_cvt_pk_bf16_f32 v255, v182, v183\n" \
"v_mfma_f32_16x16x32_bf16 a[228:231], v[156:159], v[76:79], 0\n" \
"v_accvgpr_write_b32 a8, v252\n" \
"v_accvgpr_write_b32 a9, v253\n" \
"v_mfma_f32_16x16x32_bf16 a[228:231], v[160:163], v[80:83], a[228:231]\n" \
"v_accvgpr_write_b32 a10, v254\n" \
"v_accvgpr_write_b32 a11, v255\n" \
"v_accvgpr_read_b32 v252, a232\n" \
"v_accvgpr_read_b32 v253, a233\n" \
"v_accvgpr_read_b32 v254, a234\n" \
"v_accvgpr_read_b32 v255, a235\n" \
"v_mfma_f32_16x16x32_bf16 a[232:235], v[168:171], v[80:83], 0\n" \
"s_add_u32 m0, s40, 0x3000\n" \
"s_add_u32 s85, s62, 0x0\n" \
"buffer_load_dwordx4 v21, s[76:79], s85 offen lds\n" \
"v_mfma_f32_16x16x32_bf16 a[232:235], v[164:167], v[76:79], a[232:235]\n" \
"v_accvgpr_read_b32 v180, a236\n" \
"v_accvgpr_read_b32 v181, a237\n" \
"v_accvgpr_read_b32 v182, a238\n" \
"v_accvgpr_read_b32 v183, a239\n" \
"v_mfma_f32_16x16x32_bf16 a[236:239], v[172:175], v[76:79], 0\n" \
"v_cvt_pk_bf16_f32 v252, v252, v253\n" \
"s_add_u32 m0, %[mA], 0x0\n" \
"v_mfma_f32_16x16x32_bf16 a[236:239], v[176:179], v[80:83], a[236:239]\n" \
"s_add_u32 s84, s62, 0x0\n" \
"s_nop 0\n" \
"v_cvt_pk_bf16_f32 v253, v254, v255\n" \
"v_cvt_pk_bf16_f32 v254, v180, v181\n" \
"v_cvt_pk_bf16_f32 v255, v182, v183\n" \
"v_accvgpr_write_b32 a12, v252\n" \
"v_accvgpr_write_b32 a13, v253\n" \
"v_accvgpr_write_b32 a14, v254\n" \
"v_accvgpr_write_b32 a15, v255\n" \
"v_accvgpr_read_b32 v252, a144\n" \
"v_accvgpr_read_b32 v253, a145\n" \
"v_accvgpr_read_b32 v254, a146\n" \
"v_accvgpr_read_b32 v255, a147\n" \
"v_mfma_f32_16x16x32_bf16 a[144:147], v[152:155], v[88:91], 0\n" \
"buffer_load_dwordx4 v24, s[76:79], s84 offen lds\n" \
"v_mfma_f32_16x16x32_bf16 a[144:147], v[148:151], v[84:87], a[144:147]\n" \
"v_cvt_pk_bf16_f32 v252, v252, v253\n" \
"v_accvgpr_read_b32 v180, a176\n" \
"v_accvgpr_read_b32 v181, a177\n" \
"v_accvgpr_read_b32 v182, a178\n" \
"v_accvgpr_read_b32 v183, a179\n" \
"v_cvt_pk_bf16_f32 v253, v254, v255\n" \
"v_cvt_pk_bf16_f32 v254, v180, v181\n" \
"v_cvt_pk_bf16_f32 v255, v182, v183\n" \
"v_accvgpr_write_b32 a40, v252\n" \
"v_accvgpr_write_b32 a41, v253\n" \
"v_accvgpr_write_b32 a42, v254\n" \
"v_accvgpr_write_b32 a43, v255\n" \
"v_accvgpr_read_b32 v252, a148\n" \
"v_accvgpr_read_b32 v253, a149\n" \
"v_accvgpr_read_b32 v254, a150\n" \
"v_accvgpr_read_b32 v255, a151\n" \
"v_mfma_f32_16x16x32_bf16 a[148:151], v[156:159], v[84:87], 0\n" \
"v_cvt_pk_bf16_f32 v252, v252, v253\n" \
"s_add_u32 m0, %[mA], 0x400\n" \
"v_mfma_f32_16x16x32_bf16 a[148:151], v[160:163], v[88:91], a[148:151]\n" \
"s_add_u32 s85, s62, 0x2400\n" \
"v_accvgpr_read_b32 v180, a180\n" \
"v_accvgpr_read_b32 v181, a181\n" \
"v_accvgpr_read_b32 v182, a182\n" \
"v_accvgpr_read_b32 v183, a183\n" \
"v_cvt_pk_bf16_f32 v253, v254, v255\n" \
"v_cvt_pk_bf16_f32 v254, v180, v181\n" \
"v_cvt_pk_bf16_f32 v255, v182, v183\n" \
"v_accvgpr_write_b32 a44, v252\n" \
"v_accvgpr_write_b32 a45, v253\n" \
"v_accvgpr_write_b32 a46, v254\n" \
"v_accvgpr_write_b32 a47, v255\n" \
"v_accvgpr_read_b32 v252, a152\n" \
"v_accvgpr_read_b32 v253, a153\n" \
"v_accvgpr_read_b32 v254, a154\n" \
"v_accvgpr_read_b32 v255, a155\n" \
"v_mfma_f32_16x16x32_bf16 a[152:155], v[168:171], v[88:91], 0\n" \
"buffer_load_dwordx4 v25, s[76:79], s85 offen lds\n" \
"v_mfma_f32_16x16x32_bf16 a[152:155], v[164:167], v[84:87], a[152:155]\n" \
"v_cvt_pk_bf16_f32 v252, v252, v253\n" \
"v_accvgpr_read_b32 v180, a184\n" \
"v_accvgpr_read_b32 v181, a185\n" \
"v_accvgpr_read_b32 v182, a186\n" \
"v_accvgpr_read_b32 v183, a187\n" \
"v_cvt_pk_bf16_f32 v253, v254, v255\n" \
"v_cvt_pk_bf16_f32 v254, v180, v181\n" \
"L_pf5:\n" \
"s_cbranch_execz L_pf9\n" \
"s_nop 0\n" \
"v_cvt_pk_bf16_f32 v255, v182, v183\n" \
"s_lshl_b32 s63, s62, 12\n" \
"v_accvgpr_write_b32 a72, v252\n" \
"v_accvgpr_write_b32 a73, v253\n" \
"v_accvgpr_write_b32 a74, v254\n" \
"v_accvgpr_write_b32 a75, v255\n" \
"v_accvgpr_read_b32 v252, a156\n" \
"v_accvgpr_read_b32 v253, a157\n" \
"v_accvgpr_read_b32 v254, a158\n" \
"v_accvgpr_read_b32 v255, a159\n" \
"v_mfma_f32_16x16x32_bf16 a[156:159], v[172:175], v[84:87], 0\n" \
"s_add_u32 m0, %[mB], 0x0\n" \
"s_add_u32 s84, s63, 0x0\n" \
"s_nop 0\n" \
"buffer_load_dwordx4 v26, s[68:71], s84 offen lds\n" \
"v_mfma_f32_16x16x32_bf16 a[156:159], v[176:179], v[88:91], a[156:159]\n" \
"s_add_u32 m0, %[mA], 0x800\n" \
"s_add_u32 s85, s62, 0x4800\n" \
"v_mfma_f32_16x16x32_bf16 a[176:179], v[152:155], v[96:99], 0\n" \
"v_accvgpr_read_b32 v180, a188\n" \
"v_accvgpr_read_b32 v181, a189\n" \
"v_mfma_f32_16x16x32_bf16 a[176:179], v[148:151], v[92:95], a[176:179]\n" \
"buffer_load_dwordx4 v24, s[76:79], s85 offen lds\n" \
"v_mfma_f32_16x16x32_bf16 a[180:183], v[156:159], v[92:95], 0\n" \
"s_add_u32 m0, %[mA], 0xc00\n" \
"s_add_u32 s84, s62, 0x6c00\n" \
"v_mfma_f32_16x16x32_bf16 a[180:183], v[160:163], v[96:99], a[180:183]\n" \
"v_accvgpr_read_b32 v182, a190\n" \
"v_accvgpr_read_b32 v183, a191\n" \
"v_mfma_f32_16x16x32_bf16 a[184:187], v[168:171], v[96:99], 0\n" \
"buffer_load_dwordx4 v25, s[76:79], s84 offen lds\n" \
"v_mfma_f32_16x16x32_bf16 a[184:187], v[164:167], v[92:95], a[184:187]\n" \
"v_cvt_pk_bf16_f32 v252, v252, v253\n" \
"v_cvt_pk_bf16_f32 v253, v254, v255\n" \
"v_mfma_f32_16x16x32_bf16 a[188:191], v[172:175], v[92:95], 0\n" \
"v_cvt_pk_bf16_f32 v254, v180, v181\n" \
"v_cvt_pk_bf16_f32 v255, v182, v183\n" \
"v_mfma_f32_16x16x32_bf16 a[188:191], v[176:179], v[96:99], a[188:191]\n" \
"v_accvgpr_write_b32 a76, v252\n" \
"v_accvgpr_write_b32 a77, v253\n" \
"v_accvgpr_write_b32 a78, v254\n" \
"v_accvgpr_write_b32 a79, v255\n" \
"v_accvgpr_read_b32 v252, a208\n" \
"v_accvgpr_read_b32 v253, a209\n" \
"v_accvgpr_read_b32 v254, a210\n" \
"v_accvgpr_read_b32 v255, a211\n" \
"v_mfma_f32_16x16x32_bf16 a[208:211], v[152:155], v[104:107], 0\n" \
"v_cvt_pk_bf16_f32 v252, v252, v253\n" \
"s_add_u32 m0, %[mB], 0x400\n" \
"v_mfma_f32_16x16x32_bf16 a[208:211], v[148:151], v[100:103], a[208:211]\n" \
"s_add_u32 s85, s63, 0x8000\n" \
"v_accvgpr_read_b32 v180, a240\n" \
"v_accvgpr_read_b32 v181, a241\n" \
"v_accvgpr_read_b32 v182, a242\n" \
"v_accvgpr_read_b32 v183, a243\n" \
"v_cvt_pk_bf16_f32 v253, v254, v255\n" \
"v_cvt_pk_bf16_f32 v254, v180, v181\n" \
"v_cvt_pk_bf16_f32 v255, v182, v183\n" \
"v_accvgpr_write_b32 a104, v252\n" \
"v_accvgpr_write_b32 a105, v253\n" \
"v_accvgpr_write_b32 a106, v254\n" \
"v_accvgpr_write_b32 a107, v255\n" \
"v_accvgpr_read_b32 v252, a212\n" \
"v_accvgpr_read_b32 v253, a213\n" \
"v_accvgpr_read_b32 v254, a214\n" \
"v_accvgpr_read_b32 v255, a215\n" \
"v_mfma_f32_16x16x32_bf16 a[212:215], v[156:159], v[100:103], 0\n" \
"buffer_load_dwordx4 v26, s[68:71], s85 offen lds\n" \
"ds_read_b64_tr_b16 v[140:141], v39 offset:32768\n" \
"ds_read_b64_tr_b16 v[142:143], v39 offset:33728\n" \
"ds_read_b128 v[84:87], v22 offset:32768\n" \
"ds_read_b128 v[88:91], v23 offset:32768\n" \
"v_mfma_f32_16x16x32_bf16 a[212:215], v[160:163], v[104:107], a[212:215]\n" \
"ds_read_b64_tr_b16 v[128:129], v37 offset:36864\n" \
"ds_read_b64_tr_b16 v[130:131], v37 offset:37376\n" \
"v_accvgpr_read_b32 v184, a216\n" \
"v_accvgpr_read_b32 v185, a217\n" \
"v_accvgpr_read_b32 v186, a218\n" \
"v_accvgpr_read_b32 v187, a219\n" \
"v_mfma_f32_16x16x32_bf16 a[216:219], v[168:171], v[104:107], 0\n" \
"s_add_u32 m0, %[mA], 0x1000\n" \
"s_add_u32 s84, s62, 0x9000\n" \
"buffer_load_dwordx4 v24, s[76:79], s84 offen lds\n" \
"ds_read_b64_tr_b16 v[124:125], v37 offset:32768\n" \
"ds_read_b64_tr_b16 v[126:127], v37 offset:33280\n" \
"v_mfma_f32_16x16x32_bf16 a[216:219], v[164:167], v[100:103], a[216:219]\n" \
"v_cvt_pk_bf16_f32 v252, v252, v253\n" \
"v_accvgpr_read_b32 v180, a244\n" \
"v_accvgpr_read_b32 v181, a245\n" \
"v_accvgpr_read_b32 v182, a246\n" \
"v_accvgpr_read_b32 v183, a247\n" \
"v_cvt_pk_bf16_f32 v253, v254, v255\n" \
"v_cvt_pk_bf16_f32 v254, v180, v181\n" \
"v_cvt_pk_bf16_f32 v255, v182, v183\n" \
"v_accvgpr_write_b32 a108, v252\n" \
"v_accvgpr_write_b32 a109, v253\n" \
"v_accvgpr_write_b32 a110, v254\n" \
"v_accvgpr_write_b32 a111, v255\n" \
"v_accvgpr_read_b32 v252, a220\n" \
"v_accvgpr_read_b32 v253, a221\n" \
"v_accvgpr_read_b32 v254, a222\n" \
"v_accvgpr_read_b32 v255, a223\n" \
"v_mfma_f32_16x16x32_bf16 a[220:223], v[172:175], v[100:103], 0\n" \
"ds_read_b64_tr_b16 v[132:133], v38 offset:32768\n" \
"ds_read_b64_tr_b16 v[134:135], v38 offset:33280\n" \
"ds_read_b64_tr_b16 v[136:137], v38 offset:36864\n" \
"ds_read_b64_tr_b16 v[138:139], v38 offset:37376\n" \
"v_mfma_f32_16x16x32_bf16 a[220:223], v[176:179], v[104:107], a[220:223]\n" \
"ds_read_b128 v[52:55], v28 offset:32768\n" \
"ds_read_b128 v[56:59], v29 offset:32768\n" \
"v_mfma_f32_16x16x32_bf16 a[240:243], v[152:155], v[112:115], 0\n" \
"s_add_u32 m0, %[mA], 0x1400\n" \
"s_add_u32 s85, s62, 0xb400\n" \
"buffer_load_dwordx4 v25, s[76:79], s85 offen lds\n" \
"v_mfma_f32_16x16x32_bf16 a[240:243], v[148:151], v[108:111], a[240:243]\n" \
"ds_read_b64_tr_b16 v[116:117], v36 offset:32768\n" \
"ds_read_b64_tr_b16 v[118:119], v36 offset:33280\n" \
"v_mfma_f32_16x16x32_bf16 a[244:247], v[156:159], v[108:111], 0\n" \
"s_add_u32 m0, %[mB], 0x800\n" \
"s_add_u32 s84, s63, 0x10000\n" \
"v_mfma_f32_16x16x32_bf16 a[244:247], v[160:163], v[112:115], a[244:247]\n" \
"buffer_load_dwordx4 v27, s[68:71], s84 offen lds\n" \
"ds_read_b64_tr_b16 v[120:121], v36 offset:36864\n" \
"ds_read_b64_tr_b16 v[122:123], v36 offset:37376\n" \
"v_accvgpr_read_b32 v180, a248\n" \
"v_accvgpr_read_b32 v181, a249\n" \
"v_accvgpr_read_b32 v182, a250\n" \
"v_accvgpr_read_b32 v183, a251\n" \
"v_mfma_f32_16x16x32_bf16 a[248:251], v[168:171], v[112:115], 0\n" \
"ds_read_b128 v[60:63], v28 offset:34816\n" \
"v_cvt_pk_bf16_f32 v184, v184, v185\n" \
"v_mfma_f32_16x16x32_bf16 a[248:251], v[164:167], v[108:111], a[248:251]\n" \
"v_cvt_pk_bf16_f32 v185, v186, v187\n" \
"v_cvt_pk_bf16_f32 v186, v180, v181\n" \
"v_cvt_pk_bf16_f32 v187, v182, v183\n" \
"v_accvgpr_read_b32 v180, a252\n" \
"v_accvgpr_read_b32 v181, a253\n" \
"v_accvgpr_read_b32 v182, a254\n" \
"v_accvgpr_read_b32 v183, a255\n" \
"v_mfma_f32_16x16x32_bf16 a[252:255], v[172:175], v[108:111], 0\n" \
"ds_read_b128 v[64:67], v29 offset:34816\n" \
"s_add_u32 m0, %[mA], 0x1800\n" \
"v_mfma_f32_16x16x32_bf16 a[252:255], v[176:179], v[112:115], a[252:255]\n" \
"s_add_u32 s85, s62, 0xd800\n" \
"buffer_load_dwordx4 v24, s[76:79], s85 offen lds\n" \
"s_waitcnt lgkmcnt(12)\n" \
"v_smfmac_f32_16x16x64_bf16 a[16:19], v[140:143], v[84:91], v50\n" \
"v_mfma_f32_16x16x32_bf16 a[48:51], v[128:131], v[88:91], a[48:51]\n" \
"s_add_u32 m0, %[mA], 0x1c00\n" \
"s_add_u32 s85, s62, 0xfc00\n" \
"v_mfma_f32_16x16x32_bf16 a[48:51], v[124:127], v[84:87], a[48:51]\n" \
"ds_read_b128 v[68:71], v28 offset:36864\n" \
"s_waitcnt lgkmcnt(8)\n" \
"v_mfma_f32_16x16x32_bf16 a[80:83], v[84:87], v[132:135], a[80:83]\n" \
"buffer_load_dwordx4 v25, s[76:79], s85 offen lds\n" \
"v_mfma_f32_16x16x32_bf16 a[80:83], v[88:91], v[136:139], a[80:83]\n" \
"ds_read_b128 v[72:75], v29 offset:36864\n" \
"s_waitcnt lgkmcnt(6)\n" \
"v_smfmac_f32_16x16x64_bf16 a[112:115], v[140:143], v[52:59], v50\n" \
"v_mfma_f32_16x16x32_bf16 a[20:23], v[128:131], v[56:59], a[20:23]\n" \
"s_add_u32 m0, %[mB], 0xc00\n" \
"s_add_u32 s84, s63, 0x18000\n" \
"buffer_load_dwordx4 v27, s[68:71], s84 offen lds\n" \
"v_mfma_f32_16x16x32_bf16 a[20:23], v[124:127], v[52:55], a[20:23]\n" \
"ds_read_b128 v[76:79], v28 offset:38912\n" \
"ds_read_b128 v[80:83], v29 offset:38912\n" \
"v_mfma_f32_16x16x32_bf16 a[52:55], v[116:119], v[52:55], a[52:55]\n" \
"s_add_u32 s88, s44, 0x0\n" \
"s_waitcnt lgkmcnt(5)\n" \
"v_mfma_f32_16x16x32_bf16 a[52:55], v[120:123], v[56:59], a[52:55]\n" \
"buffer_store_dwordx4 v[200:203], v20, s[80:83], s88 offen offset:2048 sc1\n" \
"v_mfma_f32_16x16x32_bf16 a[84:87], v[56:59], v[136:139], a[84:87]\n" \
"ds_read_b64_tr_b16 v[152:153], v32 offset:40960\n" \
"ds_read_b64_tr_b16 v[154:155], v32 offset:41984\n" \
"v_mfma_f32_16x16x32_bf16 a[84:87], v[52:55], v[132:135], a[84:87]\n" \
"ds_read_b64_tr_b16 v[148:149], v32 offset:32768\n" \
"s_waitcnt lgkmcnt(7)\n" \
"v_smfmac_f32_16x16x64_bf16 a[116:119], v[140:143], v[60:67], v50\n" \
"v_mfma_f32_16x16x32_bf16 a[24:27], v[124:127], v[60:63], a[24:27]\n" \
"ds_read_b64_tr_b16 v[150:151], v32 offset:33792\n" \
"ds_read_b64_tr_b16 v[156:157], v33 offset:32768\n" \
"v_mfma_f32_16x16x32_bf16 a[24:27], v[128:131], v[64:67], a[24:27]\n" \
"ds_read_b64_tr_b16 v[158:159], v33 offset:33792\n" \
"ds_read_b64_tr_b16 v[160:161], v33 offset:40960\n" \
"v_mfma_f32_16x16x32_bf16 a[56:59], v[120:123], v[64:67], a[56:59]\n" \
"ds_read_b64_tr_b16 v[162:163], v33 offset:41984\n" \
"s_add_u32 s89, s44, 0x1000\n" \
"v_mfma_f32_16x16x32_bf16 a[56:59], v[116:119], v[60:63], a[56:59]\n" \
"buffer_store_dwordx4 v[204:207], v20, s[80:83], s89 offen offset:2048 sc1\n" \
"v_mfma_f32_16x16x32_bf16 a[88:91], v[60:63], v[132:135], a[88:91]\n" \
"ds_read_b64_tr_b16 v[168:169], v34 offset:40960\n" \
"ds_read_b64_tr_b16 v[170:171], v34 offset:41984\n" \
"v_mfma_f32_16x16x32_bf16 a[88:91], v[64:67], v[136:139], a[88:91]\n" \
"ds_read_b64_tr_b16 v[164:165], v34 offset:32768\n" \
"s_waitcnt lgkmcnt(13)\n" \
"v_smfmac_f32_16x16x64_bf16 a[120:123], v[140:143], v[68:75], v50\n" \
"v_mfma_f32_16x16x32_bf16 a[28:31], v[128:131], v[72:75], a[28:31]\n" \
"ds_read_b64_tr_b16 v[166:167], v34 offset:33792\n" \
"ds_read_b64_tr_b16 v[172:173], v35 offset:32768\n" \
"v_mfma_f32_16x16x32_bf16 a[28:31], v[124:127], v[68:71], a[28:31]\n" \
"ds_read_b64_tr_b16 v[174:175], v35 offset:33792\n" \
"ds_read_b64_tr_b16 v[176:177], v35 offset:40960\n" \
"v_mfma_f32_16x16x32_bf16 a[60:63], v[116:119], v[68:71], a[60:63]\n" \
"ds_read_b64_tr_b16 v[178:179], v35 offset:41984\n" \
"s_add_u32 s88, s44, 0x0\n" \
"v_mfma_f32_16x16x32_bf16 a[60:63], v[120:123], v[72:75], a[60:63]\n" \
"buffer_store_dwordx4 v[208:211], v20, s[80:83], s88 offen offset:3072 sc1\n" \
"v_mfma_f32_16x16x32_bf16 a[92:95], v[72:75], v[136:139], a[92:95]\n" \
"ds_read_b128 v[84:87], v48 offset:32768\n" \
"ds_read_b128 v[88:91], v49 offset:32768\n" \
"v_mfma_f32_16x16x32_bf16 a[92:95], v[68:71], v[132:135], a[92:95]\n" \
"ds_read_b128 v[92:95], v48 offset:34816\n" \
"s_waitcnt lgkmcnt(15)\n" \
"s_nop 0\n" \
"v_smfmac_f32_16x16x64_bf16 a[124:127], v[140:143], v[76:83], v50\n" \
"v_mfma_f32_16x16x32_bf16 a[0:3], v[124:127], v[76:79], a[0:3]\n" \
"ds_read_b128 v[96:99], v49 offset:34816\n" \
"ds_read_b128 v[100:103], v48 offset:36864\n" \
"v_mfma_f32_16x16x32_bf16 a[0:3], v[128:131], v[80:83], a[0:3]\n" \
"ds_read_b128 v[104:107], v49 offset:36864\n" \
"ds_read_b128 v[108:111], v48 offset:38912\n" \
"v_mfma_f32_16x16x32_bf16 a[4:7], v[120:123], v[80:83], a[4:7]\n" \
"s_add_u32 s62, s62, 0x80\n" \
"ds_read_b128 v[112:115], v49 offset:38912\n" \
"v_mfma_f32_16x16x32_bf16 a[4:7], v[116:119], v[76:79], a[4:7]\n" \
"s_cmp_lt_u32 s62, 0x480\n" \
"s_cselect_b32 s62, s62, 0\n" \
"v_mfma_f32_16x16x32_bf16 a[32:35], v[76:79], v[132:135], a[32:35]\n" \
"s_lshl_b32 s72, s62, 6\n" \
"s_add_u32 s72, s72, s37\n" \
"v_mfma_f32_16x16x32_bf16 a[32:35], v[80:83], v[136:139], a[32:35]\n" \
"s_lshl_b32 s63, s62, 12\n" \
"v_cvt_pk_bf16_f32 v252, v252, v253\n" \
"v_mfma_f32_16x16x32_bf16 a[128:131], v[152:155], v[56:59], a[128:131]\n" \
"v_cvt_pk_bf16_f32 v253, v254, v255\n" \
"v_cvt_pk_bf16_f32 v254, v180, v181\n" \
"v_mfma_f32_16x16x32_bf16 a[128:131], v[148:151], v[52:55], a[128:131]\n" \
"v_cvt_pk_bf16_f32 v255, v182, v183\n" \
"s_waitcnt lgkmcnt(11)\n" \
"v_mfma_f32_16x16x32_bf16 a[132:135], v[156:159], v[52:55], a[132:135]\n" \
"v_mfma_f32_16x16x32_bf16 a[132:135], v[160:163], v[56:59], a[132:135]\n" \
"v_mfma_f32_16x16x32_bf16 a[136:139], v[168:171], v[56:59], a[136:139]\n" \
"v_mfma_f32_16x16x32_bf16 a[136:139], v[164:167], v[52:55], a[136:139]\n" \
"s_waitcnt lgkmcnt(7)\n" \
"v_mfma_f32_16x16x32_bf16 a[140:143], v[172:175], v[52:55], a[140:143]\n" \
"v_mfma_f32_16x16x32_bf16 a[140:143], v[176:179], v[56:59], a[140:143]\n" \
"v_mfma_f32_16x16x32_bf16 a[160:163], v[152:155], v[64:67], a[160:163]\n" \
"v_mfma_f32_16x16x32_bf16 a[160:163], v[148:151], v[60:63], a[160:163]\n" \
"v_mfma_f32_16x16x32_bf16 a[164:167], v[156:159], v[60:63], a[164:167]\n" \
"v_mfma_f32_16x16x32_bf16 a[164:167], v[160:163], v[64:67], a[164:167]\n" \
"v_mfma_f32_16x16x32_bf16 a[168:171], v[168:171], v[64:67], a[168:171]\n" \
"v_mfma_f32_16x16x32_bf16 a[168:171], v[164:167], v[60:63], a[168:171]\n" \
"v_mfma_f32_16x16x32_bf16 a[172:175], v[172:175], v[60:63], a[172:175]\n" \
"v_mfma_f32_16x16x32_bf16 a[172:175], v[176:179], v[64:67], a[172:175]\n" \
"v_mfma_f32_16x16x32_bf16 a[192:195], v[152:155], v[72:75], a[192:195]\n" \
"v_mfma_f32_16x16x32_bf16 a[192:195], v[148:151], v[68:71], a[192:195]\n" \
"s_waitcnt vmcnt(3) lgkmcnt(0)\n" \
"s_barrier\n" \
"v_mfma_f32_16x16x32_bf16 a[196:199], v[156:159], v[68:71], a[196:199]\n" \
"s_add_u32 m0, s40, 0x8000\n" \
"s_add_u32 s85, s72, 0x0\n" \
"s_nop 0\n" \
"v_mfma_f32_16x16x32_bf16 a[196:199], v[160:163], v[72:75], a[196:199]\n" \
"buffer_load_dwordx4 v20, s[80:83], s85 offen sc1 lds\n" \
"v_mfma_f32_16x16x32_bf16 a[200:203], v[168:171], v[72:75], a[200:203]\n" \
"s_add_u32 m0, s40, 0x9000\n" \
"s_add_u32 s84, s72, 0x1000\n" \
"v_mfma_f32_16x16x32_bf16 a[200:203], v[164:167], v[68:71], a[200:203]\n" \
"ds_read_b64_tr_b16 v[140:141], v39 offset:0\n" \
"ds_read_b64_tr_b16 v[142:143], v39 offset:960\n" \
"v_mfma_f32_16x16x32_bf16 a[204:207], v[172:175], v[68:71], a[204:207]\n" \
"buffer_load_dwordx4 v20, s[80:83], s84 offen sc1 lds\n" \
"v_mfma_f32_16x16x32_bf16 a[204:207], v[176:179], v[72:75], a[204:207]\n" \
"ds_read_b64_tr_b16 v[128:129], v37 offset:4096\n" \
"ds_read_b64_tr_b16 v[130:131], v37 offset:4608\n" \
"v_mfma_f32_16x16x32_bf16 a[224:227], v[152:155], v[80:83], a[224:227]\n" \
"s_add_u32 m0, s40, 0xb000\n" \
"s_add_u32 s85, s62, 0x0\n" \
"v_mfma_f32_16x16x32_bf16 a[224:227], v[148:151], v[76:79], a[224:227]\n" \
"buffer_load_dwordx4 v21, s[76:79], s85 offen lds\n" \
"v_mfma_f32_16x16x32_bf16 a[228:231], v[156:159], v[76:79], a[228:231]\n" \
"ds_read_b64_tr_b16 v[124:125], v37 offset:0\n" \
"ds_read_b64_tr_b16 v[126:127], v37 offset:512\n" \
"v_mfma_f32_16x16x32_bf16 a[228:231], v[160:163], v[80:83], a[228:231]\n" \
"s_add_u32 m0, %[mA], 0x8000\n" \
"s_add_u32 s84, s62, 0x0\n" \
"v_mfma_f32_16x16x32_bf16 a[232:235], v[168:171], v[80:83], a[232:235]\n" \
"buffer_load_dwordx4 v24, s[76:79], s84 offen lds\n" \
"v_mfma_f32_16x16x32_bf16 a[232:235], v[164:167], v[76:79], a[232:235]\n" \
"ds_read_b64_tr_b16 v[132:133], v38 offset:0\n" \
"ds_read_b64_tr_b16 v[134:135], v38 offset:512\n" \
"v_mfma_f32_16x16x32_bf16 a[236:239], v[172:175], v[76:79], a[236:239]\n" \
"ds_read_b64_tr_b16 v[136:137], v38 offset:4096\n" \
"ds_read_b64_tr_b16 v[138:139], v38 offset:4608\n" \
"v_mfma_f32_16x16x32_bf16 a[236:239], v[176:179], v[80:83], a[236:239]\n" \
"s_add_u32 m0, %[mA], 0x8400\n" \
"s_add_u32 s85, s62, 0x2400\n" \
"v_mfma_f32_16x16x32_bf16 a[144:147], v[152:155], v[88:91], a[144:147]\n" \
"buffer_load_dwordx4 v25, s[76:79], s85 offen lds\n" \
"v_mfma_f32_16x16x32_bf16 a[144:147], v[148:151], v[84:87], a[144:147]\n" \
"ds_read_b128 v[52:55], v28 offset:0\n" \
"ds_read_b128 v[56:59], v29 offset:0\n" \
"v_mfma_f32_16x16x32_bf16 a[148:151], v[156:159], v[84:87], a[148:151]\n" \
"s_add_u32 m0, %[mB], 0x8000\n" \
"s_add_u32 s84, s63, 0x0\n" \
"s_nop 0\n" \
"v_mfma_f32_16x16x32_bf16 a[148:151], v[160:163], v[88:91], a[148:151]\n" \
"buffer_load_dwordx4 v26, s[68:71], s84 offen lds\n" \
"v_mfma_f32_16x16x32_bf16 a[152:155], v[168:171], v[88:91], a[152:155]\n" \
"ds_read_b64_tr_b16 v[116:117], v36 offset:0\n" \
"ds_read_b64_tr_b16 v[118:119], v36 offset:512\n" \
"v_mfma_f32_16x16x32_bf16 a[152:155], v[164:167], v[84:87], a[152:155]\n" \
"ds_read_b64_tr_b16 v[120:121], v36 offset:4096\n" \
"ds_read_b64_tr_b16 v[122:123], v36 offset:4608\n" \
"v_mfma_f32_16x16x32_bf16 a[156:159], v[172:175], v[84:87], a[156:159]\n" \
"s_add_u32 m0, %[mA], 0x8800\n" \
"s_add_u32 s85, s62, 0x4800\n" \
"v_mfma_f32_16x16x32_bf16 a[156:159], v[176:179], v[88:91], a[156:159]\n" \
"buffer_load_dwordx4 v24, s[76:79], s85 offen lds\n" \
"v_mfma_f32_16x16x32_bf16 a[176:179], v[152:155], v[96:99], a[176:179]\n" \
"ds_read_b128 v[84:87], v22 offset:0\n" \
"ds_read_b128 v[88:91], v23 offset:0\n" \
"v_mfma_f32_16x16x32_bf16 a[176:179], v[148:151], v[92:95], a[176:179]\n" \
"s_add_u32 m0, %[mA], 0x8c00\n" \
"s_add_u32 s84, s62, 0x6c00\n" \
"v_mfma_f32_16x16x32_bf16 a[180:183], v[156:159], v[92:95], a[180:183]\n" \
"buffer_load_dwordx4 v25, s[76:79], s84 offen lds\n" \
"v_mfma_f32_16x16x32_bf16 a[180:183], v[160:163], v[96:99], a[180:183]\n" \
"ds_read_b128 v[60:63], v28 offset:2048\n" \
"ds_read_b128 v[64:67], v29 offset:2048\n" \
"v_mfma_f32_16x16x32_bf16 a[184:187], v[168:171], v[96:99], a[184:187]\n" \
"ds_read_b128 v[68:71], v28 offset:4096\n" \
"ds_read_b128 v[72:75], v29 offset:4096\n" \
"v_mfma_f32_16x16x32_bf16 a[184:187], v[164:167], v[92:95], a[184:187]\n" \
"s_add_u32 m0, %[mB], 0x8400\n" \
"s_add_u32 s85, s63, 0x8000\n" \
"v_mfma_f32_16x16x32_bf16 a[188:191], v[172:175], v[92:95], a[188:191]\n" \
"buffer_load_dwordx4 v26, s[68:71], s85 offen lds\n" \
"v_mfma_f32_16x16x32_bf16 a[188:191], v[176:179], v[96:99], a[188:191]\n" \
"ds_read_b128 v[76:79], v28 offset:6144\n" \
"ds_read_b128 v[80:83], v29 offset:6144\n" \
"v_mfma_f32_16x16x32_bf16 a[208:211], v[152:155], v[104:107], a[208:211]\n" \
"s_add_u32 m0, %[mA], 0x9000\n" \
"s_add_u32 s84, s62, 0x9000\n" \
"v_mfma_f32_16x16x32_bf16 a[208:211], v[148:151], v[100:103], a[208:211]\n" \
"buffer_load_dwordx4 v24, s[76:79], s84 offen lds\n" \
"v_mfma_f32_16x16x32_bf16 a[212:215], v[156:159], v[100:103], a[212:215]\n" \
"ds_read_b128 v[92:95], v48 offset:2048\n" \
"ds_read_b128 v[96:99], v49 offset:2048\n" \
"v_mfma_f32_16x16x32_bf16 a[212:215], v[160:163], v[104:107], a[212:215]\n" \
"v_mfma_f32_16x16x32_bf16 a[216:219], v[168:171], v[104:107], a[216:219]\n" \
"s_add_u32 m0, %[mA], 0x9400\n" \
"s_add_u32 s85, s62, 0xb400\n" \
"v_mfma_f32_16x16x32_bf16 a[216:219], v[164:167], v[100:103], a[216:219]\n" \
"buffer_load_dwordx4 v25, s[76:79], s85 offen lds\n" \
"v_mfma_f32_16x16x32_bf16 a[220:223], v[172:175], v[100:103], a[220:223]\n" \
"ds_read_b128 v[100:103], v48 offset:4096\n" \
"v_mfma_f32_16x16x32_bf16 a[220:223], v[176:179], v[104:107], a[220:223]\n" \
"s_add_u32 m0, %[mB], 0x8800\n" \
"s_add_u32 s84, s63, 0x10000\n" \
"v_mfma_f32_16x16x32_bf16 a[240:243], v[152:155], v[112:115], a[240:243]\n" \
"buffer_load_dwordx4 v27, s[68:71], s84 offen lds\n" \
"v_mfma_f32_16x16x32_bf16 a[240:243], v[148:151], v[108:111], a[240:243]\n" \
"ds_read_b64_tr_b16 v[152:153], v32 offset:8192\n" \
"ds_read_b64_tr_b16 v[154:155], v32 offset:9216\n" \
"v_mfma_f32_16x16x32_bf16 a[244:247], v[156:159], v[108:111], a[244:247]\n" \
"ds_read_b64_tr_b16 v[148:149], v32 offset:0\n" \
"ds_read_b64_tr_b16 v[150:151], v32 offset:1024\n" \
"v_mfma_f32_16x16x32_bf16 a[244:247], v[160:163], v[112:115], a[244:247]\n" \
"s_add_u32 m0, %[mA], 0x9800\n" \
"s_add_u32 s85, s62, 0xd800\n" \
"v_mfma_f32_16x16x32_bf16 a[248:251], v[168:171], v[112:115], a[248:251]\n" \
"buffer_load_dwordx4 v24, s[76:79], s85 offen lds\n" \
"v_mfma_f32_16x16x32_bf16 a[248:251], v[164:167], v[108:111], a[248:251]\n" \
"ds_read_b64_tr_b16 v[156:157], v33 offset:0\n" \
"ds_read_b64_tr_b16 v[158:159], v33 offset:1024\n" \
"v_mfma_f32_16x16x32_bf16 a[252:255], v[172:175], v[108:111], a[252:255]\n" \
"ds_read_b64_tr_b16 v[160:161], v33 offset:8192\n" \
"ds_read_b64_tr_b16 v[162:163], v33 offset:9216\n" \
"v_mfma_f32_16x16x32_bf16 a[252:255], v[176:179], v[112:115], a[252:255]\n" \
"s_add_u32 m0, %[mA], 0x9c00\n" \
"s_waitcnt lgkmcnt(11)\n" \
"s_nop 0\n" \
"v_smfmac_f32_16x16x64_bf16 a[16:19], v[140:143], v[84:91], v50\n" \
"v_mfma_f32_16x16x32_bf16 a[48:51], v[128:131], v[88:91], a[48:51]\n" \
"s_add_u32 s85, s62, 0xfc00\n" \
"ds_read_b64_tr_b16 v[168:169], v34 offset:8192\n" \
"v_mfma_f32_16x16x32_bf16 a[48:51], v[124:127], v[84:87], a[48:51]\n" \
"buffer_load_dwordx4 v25, s[76:79], s85 offen lds\n" \
"v_mfma_f32_16x16x32_bf16 a[80:83], v[84:87], v[132:135], a[80:83]\n" \
"s_add_u32 m0, %[mB], 0x8c00\n" \
"s_add_u32 s84, s63, 0x18000\n" \
"v_mfma_f32_16x16x32_bf16 a[80:83], v[88:91], v[136:139], a[80:83]\n" \
"ds_read_b64_tr_b16 v[170:171], v34 offset:9216\n" \
"ds_read_b64_tr_b16 v[164:165], v34 offset:0\n" \
"v_smfmac_f32_16x16x64_bf16 a[112:115], v[140:143], v[52:59], v50\n" \
"v_mfma_f32_16x16x32_bf16 a[20:23], v[128:131], v[56:59], a[20:23]\n" \
"buffer_load_dwordx4 v27, s[68:71], s84 offen lds\n" \
"v_mfma_f32_16x16x32_bf16 a[20:23], v[124:127], v[52:55], a[20:23]\n" \
"s_add_u32 s89, s44, 0x1000\n" \
"ds_read_b64_tr_b16 v[166:167], v34 offset:1024\n" \
"v_mfma_f32_16x16x32_bf16 a[52:55], v[116:119], v[52:55], a[52:55]\n" \
"ds_read_b64_tr_b16 v[172:173], v35 offset:0\n" \
"ds_read_b64_tr_b16 v[174:175], v35 offset:1024\n" \
"v_mfma_f32_16x16x32_bf16 a[52:55], v[120:123], v[56:59], a[52:55]\n" \
"buffer_store_dwordx4 v[212:215], v20, s[80:83], s89 offen offset:3072 sc1\n" \
"v_mfma_f32_16x16x32_bf16 a[84:87], v[56:59], v[136:139], a[84:87]\n" \
"ds_read_b64_tr_b16 v[176:177], v35 offset:8192\n" \
"ds_read_b64_tr_b16 v[178:179], v35 offset:9216\n" \
"v_mfma_f32_16x16x32_bf16 a[84:87], v[52:55], v[132:135], a[84:87]\n" \
"ds_read_b128 v[84:87], v48 offset:0\n" \
"ds_read_b128 v[88:91], v49 offset:0\n" \
"v_smfmac_f32_16x16x64_bf16 a[116:119], v[140:143], v[60:67], v50\n" \
"v_mfma_f32_16x16x32_bf16 a[24:27], v[124:127], v[60:63], a[24:27]\n" \
"ds_read_b128 v[104:107], v49 offset:4096\n" \
"ds_read_b128 v[108:111], v48 offset:6144\n" \
"v_mfma_f32_16x16x32_bf16 a[24:27], v[128:131], v[64:67], a[24:27]\n" \
"s_add_u32 s88, s42, 0x0\n" \
"ds_read_b128 v[112:115], v49 offset:6144\n" \
"v_mfma_f32_16x16x32_bf16 a[56:59], v[120:123], v[64:67], a[56:59]\n" \
"buffer_store_dwordx4 v[216:219], v20, s[80:83], s88 offen offset:0 sc1\n" \
"v_mfma_f32_16x16x32_bf16 a[56:59], v[116:119], v[60:63], a[56:59]\n" \
"s_add_u32 s62, s62, 0x80\n" \
"s_add_u32 s94, %[b], %[w]\n" \
"v_mfma_f32_16x16x32_bf16 a[88:91], v[60:63], v[132:135], a[88:91]\n" \
"s_cmp_lt_u32 s62, 0x480\n" \
"s_cselect_b32 s62, s62, 0\n" \
"v_mfma_f32_16x16x32_bf16 a[88:91], v[64:67], v[136:139], a[88:91]\n" \
"s_lshl_b32 s72, s62, 6\n" \
"s_add_u32 s72, s72, s37\n" \
"v_smfmac_f32_16x16x64_bf16 a[120:123], v[140:143], v[68:75], v50\n" \
"v_mfma_f32_16x16x32_bf16 a[28:31], v[128:131], v[72:75], a[28:31]\n" \
"s_lshl_b32 s63, s62, 12\n" \
"s_and_b32 s96, 7, %[b]\n" \
"v_mfma_f32_16x16x32_bf16 a[28:31], v[124:127], v[68:71], a[28:31]\n" \
"s_cmp_lg_u32 s94, 0\n" \
"s_cselect_b32 s95, 0x40000000, 0x5bc0000\n" \
"v_mfma_f32_16x16x32_bf16 a[60:63], v[116:119], v[68:71], a[60:63]\n" \
"s_add_u32 s89, s42, 0x1000\n" \
"s_mul_i32 s97, 0x48, s96\n" \
"v_mfma_f32_16x16x32_bf16 a[60:63], v[120:123], v[72:75], a[60:63]\n" \
"buffer_store_dwordx4 v[220:223], v20, s[80:83], s89 offen offset:0 sc1\n" \
"v_mfma_f32_16x16x32_bf16 a[92:95], v[72:75], v[136:139], a[92:95]\n" \
"s_add_u32 s98, 0x47, s97\n" \
"v_writelane_b32 v11, s95, 50\n" \
"v_mfma_f32_16x16x32_bf16 a[92:95], v[68:71], v[132:135], a[92:95]\n" \
"s_cmp_lt_u32 %[w], 3\n" \
"s_cselect_b32 s99, s98, 0xfff\n" \
"v_smfmac_f32_16x16x64_bf16 a[124:127], v[140:143], v[76:83], v50\n" \
"v_mfma_f32_16x16x32_bf16 a[0:3], v[124:127], v[76:79], a[0:3]\n" \
"s_lshr_b32 s94, %[b], 3\n" \
"s_lshl_b32 s67, s96, 2\n" \
"v_mfma_f32_16x16x32_bf16 a[0:3], v[128:131], v[80:83], a[0:3]\n" \
"s_and_b32 s95, 3, s94\n" \
"s_mul_i32 s100, 0x480, s99\n" \
"v_mfma_f32_16x16x32_bf16 a[4:7], v[120:123], v[80:83], a[4:7]\n" \
"s_lshl_b32 s64, %[w], 5\n" \
"s_add_u32 s98, s67, s95\n" \
"v_mfma_f32_16x16x32_bf16 a[4:7], v[116:119], v[76:79], a[4:7]\n" \
"s_add_u32 s101, 0x120, s100\n" \
"s_add_u32 s65, s97, s64\n" \
"v_mfma_f32_16x16x32_bf16 a[32:35], v[76:79], v[132:135], a[32:35]\n" \
"s_mul_i32 s99, 0x24000, s98\n" \
"v_writelane_b32 v11, s101, 15\n" \
"v_mfma_f32_16x16x32_bf16 a[32:35], v[80:83], v[136:139], a[32:35]\n" \
"s_mul_i32 s66, 0x480, s65\n" \
"s_waitcnt lgkmcnt(0)\n" \
"v_mfma_f32_16x16x32_bf16 a[128:131], v[152:155], v[56:59], a[128:131]\n" \
"s_add_u32 s100, 0x1b000, s99\n" \
"s_min_u32 s97, 2, %[w]\n" \
"v_mfma_f32_16x16x32_bf16 a[128:131], v[148:151], v[52:55], a[128:131]\n" \
"s_cmp_lt_u32 %[w], 3\n" \
"s_cselect_b32 s101, s66, s100\n" \
"v_mfma_f32_16x16x32_bf16 a[132:135], v[156:159], v[52:55], a[132:135]\n" \
"s_mul_i32 s64, 0x2400, s97\n" \
"v_writelane_b32 v11, s101, 16\n" \
"v_mfma_f32_16x16x32_bf16 a[132:135], v[160:163], v[56:59], a[132:135]\n" \
"s_add_u32 s65, 0x20000, s64\n" \
"s_mul_i32 s94, 0x9000, s97\n" \
"v_mfma_f32_16x16x32_bf16 a[136:139], v[168:171], v[56:59], a[136:139]\n" \
"s_mul_i32 s95, 0x2400, %[w]\n" \
"v_writelane_b32 v11, s65, 17\n" \
"v_mfma_f32_16x16x32_bf16 a[136:139], v[164:167], v[52:55], a[136:139]\n" \
"s_add_u32 s67, s99, s94\n" \
"v_writelane_b32 v11, s67, 22\n" \
"v_mfma_f32_16x16x32_bf16 a[140:143], v[172:175], v[52:55], a[140:143]\n" \
"s_cmp_lt_u32 %[w], 3\n" \
"s_cselect_b32 s98, s95, 0x12000\n" \
"v_mfma_f32_16x16x32_bf16 a[140:143], v[176:179], v[56:59], a[140:143]\n" \
"s_add_u32 s66, 0x20000, s95\n" \
"v_writelane_b32 v11, s98, 18\n" \
"v_mfma_f32_16x16x32_bf16 a[160:163], v[152:155], v[64:67], a[160:163]\n" \
"s_cmp_lt_u32 %[w], 3\n" \
"s_cselect_b32 s100, s66, 0xfc00\n" \
"v_mfma_f32_16x16x32_bf16 a[160:163], v[148:151], v[60:63], a[160:163]\n" \
"v_writelane_b32 v11, s95, 19\n" \
"s_lshl_b32 s101, %[w], 10\n" \
"v_mfma_f32_16x16x32_bf16 a[164:167], v[156:159], v[60:63], a[164:167]\n" \
"s_lshr_b32 s65, s96, 1\n" \
"v_writelane_b32 v11, s100, 20\n" \
"v_mfma_f32_16x16x32_bf16 a[164:167], v[160:163], v[64:67], a[164:167]\n" \
"s_cmp_eq_u32 %[w], s65\n" \
"s_cselect_b32 s97, 0xff0000, 0\n" \
"v_mfma_f32_16x16x32_bf16 a[168:171], v[168:171], v[64:67], a[168:171]\n" \
"s_add_u32 s64, 0x1d400, s101\n" \
"v_writelane_b32 v11, s64, 21\n" \
"v_mfma_f32_16x16x32_bf16 a[168:171], v[164:167], v[60:63], a[168:171]\n" \
"v_writelane_b32 v11, s97, 6\n" \
"v_mfma_f32_16x16x32_bf16 a[172:175], v[172:175], v[60:63], a[172:175]\n" \
"v_mfma_f32_16x16x32_bf16 a[172:175], v[176:179], v[64:67], a[172:175]\n" \
"v_mfma_f32_16x16x32_bf16 a[192:195], v[152:155], v[72:75], a[192:195]\n" \
"v_mfma_f32_16x16x32_bf16 a[192:195], v[148:151], v[68:71], a[192:195]\n" \
"s_waitcnt vmcnt(3)\n" \
"s_barrier\n" \
"v_mfma_f32_16x16x32_bf16 a[196:199], v[156:159], v[68:71], a[196:199]\n" \
"s_add_u32 m0, s40, 0x0\n" \
"s_add_u32 s85, s72, 0x0\n" \
"s_nop 0\n" \
"v_mfma_f32_16x16x32_bf16 a[196:199], v[160:163], v[72:75], a[196:199]\n" \
"buffer_load_dwordx4 v20, s[80:83], s85 offen sc1 lds\n" \
"v_mfma_f32_16x16x32_bf16 a[200:203], v[168:171], v[72:75], a[200:203]\n" \
"s_add_u32 m0, s40, 0x1000\n" \
"s_add_u32 s84, s72, 0x1000\n" \
"v_mfma_f32_16x16x32_bf16 a[200:203], v[164:167], v[68:71], a[200:203]\n" \
"ds_read_b64_tr_b16 v[140:141], v39 offset:32768\n" \
"ds_read_b64_tr_b16 v[142:143], v39 offset:33728\n" \
"v_mfma_f32_16x16x32_bf16 a[204:207], v[172:175], v[68:71], a[204:207]\n" \
"buffer_load_dwordx4 v20, s[80:83], s84 offen sc1 lds\n" \
"v_mfma_f32_16x16x32_bf16 a[204:207], v[176:179], v[72:75], a[204:207]\n" \
"ds_read_b64_tr_b16 v[128:129], v37 offset:36864\n" \
"ds_read_b64_tr_b16 v[130:131], v37 offset:37376\n" \
"v_mfma_f32_16x16x32_bf16 a[224:227], v[152:155], v[80:83], a[224:227]\n" \
"s_add_u32 m0, s40, 0x3000\n" \
"s_add_u32 s85, s62, 0x0\n" \
"v_mfma_f32_16x16x32_bf16 a[224:227], v[148:151], v[76:79], a[224:227]\n" \
"buffer_load_dwordx4 v21, s[76:79], s85 offen lds\n" \
"v_mfma_f32_16x16x32_bf16 a[228:231], v[156:159], v[76:79], a[228:231]\n" \
"ds_read_b64_tr_b16 v[124:125], v37 offset:32768\n" \
"ds_read_b64_tr_b16 v[126:127], v37 offset:33280\n" \
"v_mfma_f32_16x16x32_bf16 a[228:231], v[160:163], v[80:83], a[228:231]\n" \
"s_add_u32 m0, %[mA], 0x0\n" \
"s_add_u32 s84, s62, 0x0\n" \
"s_nop 0\n" \
"L_pf6:\n" \
"s_cbranch_execz L_pf10\n" \
"s_nop 0\n" \
"v_mfma_f32_16x16x32_bf16 a[232:235], v[168:171], v[80:83], a[232:235]\n" \
"buffer_load_dwordx4 v24, s[76:79], s84 offen lds\n" \
"v_mfma_f32_16x16x32_bf16 a[232:235], v[164:167], v[76:79], a[232:235]\n" \
"ds_read_b64_tr_b16 v[132:133], v38 offset:32768\n" \
"ds_read_b64_tr_b16 v[134:135], v38 offset:33280\n" \
"v_mfma_f32_16x16x32_bf16 a[236:239], v[172:175], v[76:79], a[236:239]\n" \
"ds_read_b64_tr_b16 v[136:137], v38 offset:36864\n" \
"ds_read_b64_tr_b16 v[138:139], v38 offset:37376\n" \
"v_mfma_f32_16x16x32_bf16 a[236:239], v[176:179], v[80:83], a[236:239]\n" \
"s_add_u32 m0, %[mA], 0x400\n" \
"s_add_u32 s85, s62, 0x2400\n" \
"v_mfma_f32_16x16x32_bf16 a[144:147], v[152:155], v[88:91], a[144:147]\n" \
"buffer_load_dwordx4 v25, s[76:79], s85 offen lds\n" \
"v_mfma_f32_16x16x32_bf16 a[144:147], v[148:151], v[84:87], a[144:147]\n" \
"ds_read_b128 v[52:55], v28 offset:32768\n" \
"ds_read_b128 v[56:59], v29 offset:32768\n" \
"v_mfma_f32_16x16x32_bf16 a[148:151], v[156:159], v[84:87], a[148:151]\n" \
"s_add_u32 m0, %[mB], 0x0\n" \
"s_add_u32 s84, s63, 0x0\n" \
"v_mfma_f32_16x16x32_bf16 a[148:151], v[160:163], v[88:91], a[148:151]\n" \
"buffer_load_dwordx4 v26, s[68:71], s84 offen lds\n" \
"v_mfma_f32_16x16x32_bf16 a[152:155], v[168:171], v[88:91], a[152:155]\n" \
"ds_read_b64_tr_b16 v[116:117], v36 offset:32768\n" \
"ds_read_b64_tr_b16 v[118:119], v36 offset:33280\n" \
"v_mfma_f32_16x16x32_bf16 a[152:155], v[164:167], v[84:87], a[152:155]\n" \
"ds_read_b64_tr_b16 v[120:121], v36 offset:36864\n" \
"ds_read_b64_tr_b16 v[122:123], v36 offset:37376\n" \
"v_mfma_f32_16x16x32_bf16 a[156:159], v[172:175], v[84:87], a[156:159]\n" \
"s_add_u32 m0, %[mA], 0x800\n" \
"s_add_u32 s85, s62, 0x4800\n" \
"v_mfma_f32_16x16x32_bf16 a[156:159], v[176:179], v[88:91], a[156:159]\n" \
"buffer_load_dwordx4 v24, s[76:79], s85 offen lds\n" \
"v_mfma_f32_16x16x32_bf16 a[176:179], v[152:155], v[96:99], a[176:179]\n" \
"ds_read_b128 v[84:87], v22 offset:32768\n" \
"ds_read_b128 v[88:91], v23 offset:32768\n" \
"v_mfma_f32_16x16x32_bf16 a[176:179], v[148:151], v[92:95], a[176:179]\n" \
"s_add_u32 m0, %[mA], 0xc00\n" \
"s_add_u32 s84, s62, 0x6c00\n" \
"v_mfma_f32_16x16x32_bf16 a[180:183], v[156:159], v[92:95], a[180:183]\n" \
"buffer_load_dwordx4 v25, s[76:79], s84 offen lds\n" \
"v_mfma_f32_16x16x32_bf16 a[180:183], v[160:163], v[96:99], a[180:183]\n" \
"ds_read_b128 v[60:63], v28 offset:34816\n" \
"ds_read_b128 v[64:67], v29 offset:34816\n" \
"v_mfma_f32_16x16x32_bf16 a[184:187], v[168:171], v[96:99], a[184:187]\n" \
"ds_read_b128 v[68:71], v28 offset:36864\n" \
"ds_read_b128 v[72:75], v29 offset:36864\n" \
"v_mfma_f32_16x16x32_bf16 a[184:187], v[164:167], v[92:95], a[184:187]\n" \
"s_add_u32 m0, %[mB], 0x400\n" \
"s_add_u32 s85, s63, 0x8000\n" \
"v_mfma_f32_16x16x32_bf16 a[188:191], v[172:175], v[92:95], a[188:191]\n" \
"buffer_load_dwordx4 v26, s[68:71], s85 offen lds\n" \
"v_mfma_f32_16x16x32_bf16 a[188:191], v[176:179], v[96:99], a[188:191]\n" \
"ds_read_b128 v[76:79], v28 offset:38912\n" \
"ds_read_b128 v[80:83], v29 offset:38912\n" \
"v_mfma_f32_16x16x32_bf16 a[208:211], v[152:155], v[104:107], a[208:211]\n" \
"s_add_u32 m0, %[mA], 0x1000\n" \
"s_add_u32 s84, s62, 0x9000\n" \
"v_mfma_f32_16x16x32_bf16 a[208:211], v[148:151], v[100:103], a[208:211]\n" \
"buffer_load_dwordx4 v24, s[76:79], s84 offen lds\n" \
"v_mfma_f32_16x16x32_bf16 a[212:215], v[156:159], v[100:103], a[212:215]\n" \
"ds_read_b128 v[92:95], v48 offset:34816\n" \
"ds_read_b128 v[96:99], v49 offset:34816\n" \
"v_mfma_f32_16x16x32_bf16 a[212:215], v[160:163], v[104:107], a[212:215]\n" \
"v_mfma_f32_16x16x32_bf16 a[216:219], v[168:171], v[104:107], a[216:219]\n" \
"s_add_u32 m0, %[mA], 0x1400\n" \
"s_add_u32 s85, s62, 0xb400\n" \
"v_mfma_f32_16x16x32_bf16 a[216:219], v[164:167], v[100:103], a[216:219]\n" \
"buffer_load_dwordx4 v25, s[76:79], s85 offen lds\n" \
"v_mfma_f32_16x16x32_bf16 a[220:223], v[172:175], v[100:103], a[220:223]\n" \
"ds_read_b128 v[100:103], v48 offset:36864\n" \
"v_mfma_f32_16x16x32_bf16 a[220:223], v[176:179], v[104:107], a[220:223]\n" \
"s_add_u32 m0, %[mB], 0x800\n" \
"s_add_u32 s84, s63, 0x10000\n" \
"v_mfma_f32_16x16x32_bf16 a[240:243], v[152:155], v[112:115], a[240:243]\n" \
"buffer_load_dwordx4 v27, s[68:71], s84 offen lds\n" \
"v_mfma_f32_16x16x32_bf16 a[240:243], v[148:151], v[108:111], a[240:243]\n" \
"ds_read_b64_tr_b16 v[152:153], v32 offset:40960\n" \
"ds_read_b64_tr_b16 v[154:155], v32 offset:41984\n" \
"v_mfma_f32_16x16x32_bf16 a[244:247], v[156:159], v[108:111], a[244:247]\n" \
"ds_read_b64_tr_b16 v[148:149], v32 offset:32768\n" \
"ds_read_b64_tr_b16 v[150:151], v32 offset:33792\n" \
"v_mfma_f32_16x16x32_bf16 a[244:247], v[160:163], v[112:115], a[244:247]\n" \
"s_add_u32 m0, %[mA], 0x1800\n" \
"s_add_u32 s85, s62, 0xd800\n" \
"v_mfma_f32_16x16x32_bf16 a[248:251], v[168:171], v[112:115], a[248:251]\n" \
"buffer_load_dwordx4 v24, s[76:79], s85 offen lds\n" \
"v_mfma_f32_16x16x32_bf16 a[248:251], v[164:167], v[108:111], a[248:251]\n" \
"ds_read_b64_tr_b16 v[156:157], v33 offset:32768\n" \
"ds_read_b64_tr_b16 v[158:159], v33 offset:33792\n" \
"v_mfma_f32_16x16x32_bf16 a[252:255], v[172:175], v[108:111], a[252:255]\n" \
"ds_read_b64_tr_b16 v[160:161], v33 offset:40960\n" \
"ds_read_b64_tr_b16 v[162:163], v33 offset:41984\n" \
"v_mfma_f32_16x16x32_bf16 a[252:255], v[176:179], v[112:115], a[252:255]\n" \
"s_add_u32 m0, %[mA], 0x1c00\n" \
"s_waitcnt lgkmcnt(11)\n" \
"s_nop 0\n" \
"v_smfmac_f32_16x16x64_bf16 a[16:19], v[140:143], v[84:91], v50\n" \
"v_mfma_f32_16x16x32_bf16 a[48:51], v[128:131], v[88:91], a[48:51]\n" \
"s_add_u32 s85, s62, 0xfc00\n" \
"ds_read_b64_tr_b16 v[168:169], v34 offset:40960\n" \
"v_mfma_f32_16x16x32_bf16 a[48:51], v[124:127], v[84:87], a[48:51]\n" \
"buffer_load_dwordx4 v25, s[76:79], s85 offen lds\n" \
"v_mfma_f32_16x16x32_bf16 a[80:83], v[84:87], v[132:135], a[80:83]\n" \
"s_add_u32 m0, %[mB], 0xc00\n" \
"s_add_u32 s84, s63, 0x18000\n" \
"v_mfma_f32_16x16x32_bf16 a[80:83], v[88:91], v[136:139], a[80:83]\n" \
"ds_read_b64_tr_b16 v[170:171], v34 offset:41984\n" \
"ds_read_b64_tr_b16 v[164:165], v34 offset:32768\n" \
"v_smfmac_f32_16x16x64_bf16 a[112:115], v[140:143], v[52:59], v50\n" \
"v_mfma_f32_16x16x32_bf16 a[20:23], v[128:131], v[56:59], a[20:23]\n" \
"buffer_load_dwordx4 v27, s[68:71], s84 offen lds\n" \
"v_mfma_f32_16x16x32_bf16 a[20:23], v[124:127], v[52:55], a[20:23]\n" \
"s_add_u32 s88, s42, 0x2000\n" \
"ds_read_b64_tr_b16 v[166:167], v34 offset:33792\n" \
"v_mfma_f32_16x16x32_bf16 a[52:55], v[116:119], v[52:55], a[52:55]\n" \
"ds_read_b64_tr_b16 v[172:173], v35 offset:32768\n" \
"ds_read_b64_tr_b16 v[174:175], v35 offset:33792\n" \
"v_mfma_f32_16x16x32_bf16 a[52:55], v[120:123], v[56:59], a[52:55]\n" \
"buffer_store_dwordx4 v[224:227], v20, s[80:83], s88 offen offset:0 sc1\n" \
"v_mfma_f32_16x16x32_bf16 a[84:87], v[56:59], v[136:139], a[84:87]\n" \
"ds_read_b64_tr_b16 v[176:177], v35 offset:40960\n" \
"ds_read_b64_tr_b16 v[178:179], v35 offset:41984\n" \
"v_mfma_f32_16x16x32_bf16 a[84:87], v[52:55], v[132:135], a[84:87]\n" \
"ds_read_b128 v[84:87], v48 offset:32768\n" \
"ds_read_b128 v[88:91], v49 offset:32768\n" \
"v_smfmac_f32_16x16x64_bf16 a[116:119], v[140:143], v[60:67], v50\n" \
"v_mfma_f32_16x16x32_bf16 a[24:27], v[124:127], v[60:63], a[24:27]\n" \
"ds_read_b128 v[104:107], v49 offset:36864\n" \
"ds_read_b128 v[108:111], v48 offset:38912\n" \
"v_mfma_f32_16x16x32_bf16 a[24:27], v[128:131], v[64:67], a[24:27]\n" \
"s_add_u32 s89, s42, 0x3000\n" \
"ds_read_b128 v[112:115], v49 offset:38912\n" \
"v_mfma_f32_16x16x32_bf16 a[56:59], v[120:123], v[64:67], a[56:59]\n" \
"buffer_store_dwordx4 v[228:231], v20, s[80:83], s89 offen offset:0 sc1\n" \
"v_mfma_f32_16x16x32_bf16 a[56:59], v[116:119], v[60:63], a[56:59]\n" \
"s_add_u32 s62, s62, 0x80\n" \
"s_lshr_b32 s96, %[b], 3\n" \
"v_mfma_f32_16x16x32_bf16 a[88:91], v[60:63], v[132:135], a[88:91]\n" \
"s_cmp_lt_u32 s62, 0x480\n" \
"s_cselect_b32 s62, s62, 0\n" \
"v_mfma_f32_16x16x32_bf16 a[88:91], v[64:67], v[136:139], a[88:91]\n" \
"s_lshl_b32 s72, s62, 6\n" \
"s_add_u32 s72, s72, s37\n" \
"v_smfmac_f32_16x16x64_bf16 a[120:123], v[140:143], v[68:75], v50\n" \
"v_mfma_f32_16x16x32_bf16 a[28:31], v[128:131], v[72:75], a[28:31]\n" \
"s_lshl_b32 s63, s62, 12\n" \
"s_and_b32 s97, 3, s96\n" \
"v_mfma_f32_16x16x32_bf16 a[28:31], v[124:127], v[68:71], a[28:31]\n" \
"s_and_b32 s94, 7, %[b]\n" \
"s_add_u32 s98, 1, s97\n" \
"v_mfma_f32_16x16x32_bf16 a[60:63], v[116:119], v[68:71], a[60:63]\n" \
"s_add_u32 s88, s42, 0x0\n" \
"s_lshl_b32 s95, s94, 2\n" \
"v_mfma_f32_16x16x32_bf16 a[60:63], v[120:123], v[72:75], a[60:63]\n" \
"buffer_store_dwordx4 v[232:235], v20, s[80:83], s88 offen offset:1024 sc1\n" \
"v_mfma_f32_16x16x32_bf16 a[92:95], v[72:75], v[136:139], a[92:95]\n" \
"s_and_b32 s99, 3, s98\n" \
"s_add_u32 s100, s95, s99\n" \
"v_mfma_f32_16x16x32_bf16 a[92:95], v[68:71], v[132:135], a[92:95]\n" \
"s_mul_i32 s101, 0x24000, s100\n" \
"s_mul_i32 s64, 0x9000, %[w]\n" \
"v_smfmac_f32_16x16x64_bf16 a[124:127], v[140:143], v[76:83], v50\n" \
"v_mfma_f32_16x16x32_bf16 a[0:3], v[124:127], v[76:79], a[0:3]\n" \
"s_and_b32 s96, 2, %[w]\n" \
"s_add_u32 s65, s101, s64\n" \
"v_mfma_f32_16x16x32_bf16 a[0:3], v[128:131], v[80:83], a[0:3]\n" \
"s_lshr_b32 s66, %[b], 5\n" \
"s_lshl_b32 s98, s96, 1\n" \
"v_mfma_f32_16x16x32_bf16 a[4:7], v[120:123], v[80:83], a[4:7]\n" \
"s_and_b32 s99, 1, %[w]\n" \
"v_writelane_b32 v11, s65, 23\n" \
"v_mfma_f32_16x16x32_bf16 a[4:7], v[116:119], v[76:79], a[4:7]\n" \
"s_lshl_b32 s67, s66, 3\n" \
"s_add_u32 s101, s98, s99\n" \
"v_mfma_f32_16x16x32_bf16 a[32:35], v[76:79], v[132:135], a[32:35]\n" \
"s_add_u32 s65, s67, s101\n" \
"s_mul_i32 s66, 0x24800, s65\n" \
"v_mfma_f32_16x16x32_bf16 a[32:35], v[80:83], v[136:139], a[32:35]\n" \
"s_lshl_b32 s98, s100, 12\n" \
"s_waitcnt lgkmcnt(0)\n" \
"v_mfma_f32_16x16x32_bf16 a[128:131], v[152:155], v[56:59], a[128:131]\n" \
"s_add_u32 s96, 0x3000000, s66\n" \
"s_add_u32 s99, 0x4800, s98\n" \
"v_mfma_f32_16x16x32_bf16 a[128:131], v[148:151], v[52:55], a[128:131]\n" \
"s_add_u32 s65, s96, s99\n" \
"s_sub_u32 s100, 0x78, s67\n" \
"v_mfma_f32_16x16x32_bf16 a[132:135], v[156:159], v[52:55], a[132:135]\n" \
"v_writelane_b32 v11, s65, 26\n" \
"s_add_u32 s98, s100, s101\n" \
"v_mfma_f32_16x16x32_bf16 a[132:135], v[160:163], v[56:59], a[132:135]\n" \
"s_mul_i32 s65, 0x24800, s98\n" \
"s_add_u32 s67, 0x3000000, s65\n" \
"v_mfma_f32_16x16x32_bf16 a[136:139], v[168:171], v[56:59], a[136:139]\n" \
"s_add_u32 s101, 2, s97\n" \
"s_add_u32 s100, s67, s99\n" \
"v_mfma_f32_16x16x32_bf16 a[136:139], v[164:167], v[52:55], a[136:139]\n" \
"s_and_b32 s98, 3, s101\n" \
"v_writelane_b32 v11, s100, 29\n" \
"v_mfma_f32_16x16x32_bf16 a[140:143], v[172:175], v[52:55], a[140:143]\n" \
"s_add_u32 s99, s95, s98\n" \
"s_mul_i32 s100, 0x24000, s99\n" \
"v_mfma_f32_16x16x32_bf16 a[140:143], v[176:179], v[56:59], a[140:143]\n" \
"s_add_u32 s101, s100, s64\n" \
"s_lshl_b32 s98, s99, 12\n" \
"v_mfma_f32_16x16x32_bf16 a[160:163], v[152:155], v[64:67], a[160:163]\n" \
"v_writelane_b32 v11, s101, 24\n" \
"s_add_u32 s100, 0x4800, s98\n" \
"v_mfma_f32_16x16x32_bf16 a[160:163], v[148:151], v[60:63], a[160:163]\n" \
"s_add_u32 s101, s96, s100\n" \
"v_writelane_b32 v11, s101, 27\n" \
"v_mfma_f32_16x16x32_bf16 a[164:167], v[156:159], v[60:63], a[164:167]\n" \
"s_add_u32 s98, 3, s97\n" \
"s_add_u32 s99, s67, s100\n" \
"v_mfma_f32_16x16x32_bf16 a[164:167], v[160:163], v[64:67], a[164:167]\n" \
"s_and_b32 s101, 3, s98\n" \
"v_writelane_b32 v11, s99, 30\n" \
"v_mfma_f32_16x16x32_bf16 a[168:171], v[168:171], v[64:67], a[168:171]\n" \
"s_add_u32 s100, s95, s101\n" \
"s_mul_i32 s99, 0x24000, s100\n" \
"v_mfma_f32_16x16x32_bf16 a[168:171], v[164:167], v[60:63], a[168:171]\n" \
"s_lshl_b32 s98, s100, 12\n" \
"s_add_u32 s97, s99, s64\n" \
"v_mfma_f32_16x16x32_bf16 a[172:175], v[172:175], v[60:63], a[172:175]\n" \
"s_add_u32 s95, 0x4800, s98\n" \
"v_writelane_b32 v11, s97, 25\n" \
"v_mfma_f32_16x16x32_bf16 a[172:175], v[176:179], v[64:67], a[172:175]\n" \
"s_add_u32 s101, s96, s95\n" \
"v_writelane_b32 v11, s101, 28\n" \
"v_mfma_f32_16x16x32_bf16 a[192:195], v[152:155], v[72:75], a[192:195]\n" \
"s_add_u32 s99, s67, s95\n" \
"s_lshl_b32 s64, s94, 11\n" \
"v_mfma_f32_16x16x32_bf16 a[192:195], v[148:151], v[68:71], a[192:195]\n" \
"v_writelane_b32 v11, s99, 31\n" \
"s_add_u32 s97, s96, s64\n" \
"s_waitcnt vmcnt(3)\n" \
"s_barrier\n" \
"v_mfma_f32_16x16x32_bf16 a[196:199], v[156:159], v[68:71], a[196:199]\n" \
"s_add_u32 m0, s40, 0x8000\n" \
"s_add_u32 s85, s72, 0x0\n" \
"v_mfma_f32_16x16x32_bf16 a[196:199], v[160:163], v[72:75], a[196:199]\n" \
"buffer_load_dwordx4 v20, s[80:83], s85 offen sc1 lds\n" \
"v_mfma_f32_16x16x32_bf16 a[200:203], v[168:171], v[72:75], a[200:203]\n" \
"s_add_u32 m0, s40, 0x9000\n" \
"s_add_u32 s84, s72, 0x1000\n" \
"v_mfma_f32_16x16x32_bf16 a[200:203], v[164:167], v[68:71], a[200:203]\n" \
"ds_read_b64_tr_b16 v[140:141], v39 offset:0\n" \
"ds_read_b64_tr_b16 v[142:143], v39 offset:960\n" \
"v_mfma_f32_16x16x32_bf16 a[204:207], v[172:175], v[68:71], a[204:207]\n" \
"buffer_load_dwordx4 v20, s[80:83], s84 offen sc1 lds\n" \
"v_mfma_f32_16x16x32_bf16 a[204:207], v[176:179], v[72:75], a[204:207]\n" \
"ds_read_b64_tr_b16 v[128:129], v37 offset:4096\n" \
"ds_read_b64_tr_b16 v[130:131], v37 offset:4608\n" \
"v_mfma_f32_16x16x32_bf16 a[224:227], v[152:155], v[80:83], a[224:227]\n" \
"s_add_u32 m0, s40, 0xb000\n" \
"s_add_u32 s85, s62, 0x0\n" \
"v_mfma_f32_16x16x32_bf16 a[224:227], v[148:151], v[76:79], a[224:227]\n" \
"buffer_load_dwordx4 v21, s[76:79], s85 offen lds\n" \
"v_mfma_f32_16x16x32_bf16 a[228:231], v[156:159], v[76:79], a[228:231]\n" \
"ds_read_b64_tr_b16 v[124:125], v37 offset:0\n" \
"ds_read_b64_tr_b16 v[126:127], v37 offset:512\n" \
"v_mfma_f32_16x16x32_bf16 a[228:231], v[160:163], v[80:83], a[228:231]\n" \
"s_add_u32 m0, %[mA], 0x8000\n" \
"s_add_u32 s84, s62, 0x0\n" \
"v_mfma_f32_16x16x32_bf16 a[232:235], v[168:171], v[80:83], a[232:235]\n" \
"buffer_load_dwordx4 v24, s[76:79], s84 offen lds\n" \
"v_mfma_f32_16x16x32_bf16 a[232:235], v[164:167], v[76:79], a[232:235]\n" \
"ds_read_b64_tr_b16 v[132:133], v38 offset:0\n" \
"ds_read_b64_tr_b16 v[134:135], v38 offset:512\n" \
"v_mfma_f32_16x16x32_bf16 a[236:239], v[172:175], v[76:79], a[236:239]\n" \
"ds_read_b64_tr_b16 v[136:137], v38 offset:4096\n" \
"ds_read_b64_tr_b16 v[138:139], v38 offset:4608\n" \
"v_mfma_f32_16x16x32_bf16 a[236:239], v[176:179], v[80:83], a[236:239]\n" \
"s_add_u32 m0, %[mA], 0x8400\n" \
"s_add_u32 s85, s62, 0x2400\n" \
"v_mfma_f32_16x16x32_bf16 a[144:147], v[152:155], v[88:91], a[144:147]\n" \
"buffer_load_dwordx4 v25, s[76:79], s85 offen lds\n" \
"v_mfma_f32_16x16x32_bf16 a[144:147], v[148:151], v[84:87], a[144:147]\n" \
"ds_read_b128 v[52:55], v28 offset:0\n" \
"ds_read_b128 v[56:59], v29 offset:0\n" \
"v_mfma_f32_16x16x32_bf16 a[148:151], v[156:159], v[84:87], a[148:151]\n" \
"s_add_u32 m0, %[mB], 0x8000\n" \
"s_add_u32 s84, s63, 0x0\n" \
"s_nop 0\n" \
"v_mfma_f32_16x16x32_bf16 a[148:151], v[160:163], v[88:91], a[148:151]\n" \
"buffer_load_dwordx4 v26, s[68:71], s84 offen lds\n" \
"v_mfma_f32_16x16x32_bf16 a[152:155], v[168:171], v[88:91], a[152:155]\n" \
"ds_read_b64_tr_b16 v[116:117], v36 offset:0\n" \
"ds_read_b64_tr_b16 v[118:119], v36 offset:512\n" \
"v_mfma_f32_16x16x32_bf16 a[152:155], v[164:167], v[84:87], a[152:155]\n" \
"ds_read_b64_tr_b16 v[120:121], v36 offset:4096\n" \
"ds_read_b64_tr_b16 v[122:123], v36 offset:4608\n" \
"v_mfma_f32_16x16x32_bf16 a[156:159], v[172:175], v[84:87], a[156:159]\n" \
"s_add_u32 m0, %[mA], 0x8800\n" \
"s_add_u32 s85, s62, 0x4800\n" \
"v_mfma_f32_16x16x32_bf16 a[156:159], v[176:179], v[88:91], a[156:159]\n" \
"buffer_load_dwordx4 v24, s[76:79], s85 offen lds\n" \
"v_mfma_f32_16x16x32_bf16 a[176:179], v[152:155], v[96:99], a[176:179]\n" \
"ds_read_b128 v[84:87], v22 offset:0\n" \
"ds_read_b128 v[88:91], v23 offset:0\n" \
"v_mfma_f32_16x16x32_bf16 a[176:179], v[148:151], v[92:95], a[176:179]\n" \
"s_add_u32 m0, %[mA], 0x8c00\n" \
"s_add_u32 s84, s62, 0x6c00\n" \
"v_mfma_f32_16x16x32_bf16 a[180:183], v[156:159], v[92:95], a[180:183]\n" \
"buffer_load_dwordx4 v25, s[76:79], s84 offen lds\n" \
"v_mfma_f32_16x16x32_bf16 a[180:183], v[160:163], v[96:99], a[180:183]\n" \
"ds_read_b128 v[60:63], v28 offset:2048\n" \
"ds_read_b128 v[64:67], v29 offset:2048\n" \
"v_mfma_f32_16x16x32_bf16 a[184:187], v[168:171], v[96:99], a[184:187]\n" \
"ds_read_b128 v[68:71], v28 offset:4096\n" \
"ds_read_b128 v[72:75], v29 offset:4096\n" \
"v_mfma_f32_16x16x32_bf16 a[184:187], v[164:167], v[92:95], a[184:187]\n" \
"s_add_u32 m0, %[mB], 0x8400\n" \
"s_add_u32 s85, s63, 0x8000\n" \
"v_mfma_f32_16x16x32_bf16 a[188:191], v[172:175], v[92:95], a[188:191]\n" \
"buffer_load_dwordx4 v26, s[68:71], s85 offen lds\n" \
"v_mfma_f32_16x16x32_bf16 a[188:191], v[176:179], v[96:99], a[188:191]\n" \
"ds_read_b128 v[76:79], v28 offset:6144\n" \
"ds_read_b128 v[80:83], v29 offset:6144\n" \
"v_mfma_f32_16x16x32_bf16 a[208:211], v[152:155], v[104:107], a[208:211]\n" \
"s_add_u32 m0, %[mA], 0x9000\n" \
"s_add_u32 s84, s62, 0x9000\n" \
"v_mfma_f32_16x16x32_bf16 a[208:211], v[148:151], v[100:103], a[208:211]\n" \
"buffer_load_dwordx4 v24, s[76:79], s84 offen lds\n" \
"v_mfma_f32_16x16x32_bf16 a[212:215], v[156:159], v[100:103], a[212:215]\n" \
"ds_read_b128 v[92:95], v48 offset:2048\n" \
"ds_read_b128 v[96:99], v49 offset:2048\n" \
"v_mfma_f32_16x16x32_bf16 a[212:215], v[160:163], v[104:107], a[212:215]\n" \
"v_writelane_b32 v11, s97, 32\n" \
"s_and_b32 s99, 1, s94\n" \
"v_mfma_f32_16x16x32_bf16 a[216:219], v[168:171], v[104:107], a[216:219]\n" \
"s_add_u32 m0, %[mA], 0x9400\n" \
"s_add_u32 s85, s62, 0xb400\n" \
"v_mfma_f32_16x16x32_bf16 a[216:219], v[164:167], v[100:103], a[216:219]\n" \
"buffer_load_dwordx4 v25, s[76:79], s85 offen lds\n" \
"v_mfma_f32_16x16x32_bf16 a[220:223], v[172:175], v[100:103], a[220:223]\n" \
"ds_read_b128 v[100:103], v48 offset:4096\n" \
"s_add_u32 s100, s67, s64\n" \
"v_mfma_f32_16x16x32_bf16 a[220:223], v[176:179], v[104:107], a[220:223]\n" \
"s_add_u32 m0, %[mB], 0x8800\n" \
"s_add_u32 s84, s63, 0x10000\n" \
"v_mfma_f32_16x16x32_bf16 a[240:243], v[152:155], v[112:115], a[240:243]\n" \
"buffer_load_dwordx4 v27, s[68:71], s84 offen lds\n" \
"v_mfma_f32_16x16x32_bf16 a[240:243], v[148:151], v[108:111], a[240:243]\n" \
"ds_read_b64_tr_b16 v[152:153], v32 offset:8192\n" \
"ds_read_b64_tr_b16 v[154:155], v32 offset:9216\n" \
"v_mfma_f32_16x16x32_bf16 a[244:247], v[156:159], v[108:111], a[244:247]\n" \
"ds_read_b64_tr_b16 v[148:149], v32 offset:0\n" \
"ds_read_b64_tr_b16 v[150:151], v32 offset:1024\n" \
"v_mfma_f32_16x16x32_bf16 a[244:247], v[160:163], v[112:115], a[244:247]\n" \
"s_add_u32 m0, %[mA], 0x9800\n" \
"s_add_u32 s85, s62, 0xd800\n" \
"v_mfma_f32_16x16x32_bf16 a[248:251], v[168:171], v[112:115], a[248:251]\n" \
"buffer_load_dwordx4 v24, s[76:79], s85 offen lds\n" \
"v_mfma_f32_16x16x32_bf16 a[248:251], v[164:167], v[108:111], a[248:251]\n" \
"ds_read_b64_tr_b16 v[156:157], v33 offset:0\n" \
"ds_read_b64_tr_b16 v[158:159], v33 offset:1024\n" \
"v_mfma_f32_16x16x32_bf16 a[252:255], v[172:175], v[108:111], a[252:255]\n" \
"ds_read_b64_tr_b16 v[160:161], v33 offset:8192\n" \
"ds_read_b64_tr_b16 v[162:163], v33 offset:9216\n" \
"v_mfma_f32_16x16x32_bf16 a[252:255], v[176:179], v[112:115], a[252:255]\n" \
"s_add_u32 m0, %[mA], 0x9c00\n" \
"s_waitcnt lgkmcnt(11)\n" \
"s_nop 0\n" \
"v_smfmac_f32_16x16x64_bf16 a[16:19], v[140:143], v[84:91], v50\n" \
"v_mfma_f32_16x16x32_bf16 a[48:51], v[128:131], v[88:91], a[48:51]\n" \
"s_add_u32 s85, s62, 0xfc00\n" \
"ds_read_b64_tr_b16 v[168:169], v34 offset:8192\n" \
"v_mfma_f32_16x16x32_bf16 a[48:51], v[124:127], v[84:87], a[48:51]\n" \
"buffer_load_dwordx4 v25, s[76:79], s85 offen lds\n" \
"v_mfma_f32_16x16x32_bf16 a[80:83], v[84:87], v[132:135], a[80:83]\n" \
"s_add_u32 m0, %[mB], 0x8c00\n" \
"s_add_u32 s84, s63, 0x18000\n" \
"v_mfma_f32_16x16x32_bf16 a[80:83], v[88:91], v[136:139], a[80:83]\n" \
"ds_read_b64_tr_b16 v[170:171], v34 offset:9216\n" \
"ds_read_b64_tr_b16 v[164:165], v34 offset:0\n" \
"v_smfmac_f32_16x16x64_bf16 a[112:115], v[140:143], v[52:59], v50\n" \
"v_mfma_f32_16x16x32_bf16 a[20:23], v[128:131], v[56:59], a[20:23]\n" \
"buffer_load_dwordx4 v27, s[68:71], s84 offen lds\n" \
"v_mfma_f32_16x16x32_bf16 a[20:23], v[124:127], v[52:55], a[20:23]\n" \
"s_add_u32 s89, s42, 0x1000\n" \
"ds_read_b64_tr_b16 v[166:167], v34 offset:1024\n" \
"v_mfma_f32_16x16x32_bf16 a[52:55], v[116:119], v[52:55], a[52:55]\n" \
"ds_read_b64_tr_b16 v[172:173], v35 offset:0\n" \
"ds_read_b64_tr_b16 v[174:175], v35 offset:1024\n" \
"v_mfma_f32_16x16x32_bf16 a[52:55], v[120:123], v[56:59], a[52:55]\n" \
"buffer_store_dwordx4 v[236:239], v20, s[80:83], s89 offen offset:1024 sc1\n" \
"v_mfma_f32_16x16x32_bf16 a[84:87], v[56:59], v[136:139], a[84:87]\n" \
"ds_read_b64_tr_b16 v[176:177], v35 offset:8192\n" \
"ds_read_b64_tr_b16 v[178:179], v35 offset:9216\n" \
"v_mfma_f32_16x16x32_bf16 a[84:87], v[52:55], v[132:135], a[84:87]\n" \
"ds_read_b128 v[84:87], v48 offset:0\n" \
"ds_read_b128 v[88:91], v49 offset:0\n" \
"v_smfmac_f32_16x16x64_bf16 a[116:119], v[140:143], v[60:67], v50\n" \
"v_mfma_f32_16x16x32_bf16 a[24:27], v[124:127], v[60:63], a[24:27]\n" \
"ds_read_b128 v[104:107], v49 offset:4096\n" \
"ds_read_b128 v[108:111], v48 offset:6144\n" \
"v_mfma_f32_16x16x32_bf16 a[24:27], v[128:131], v[64:67], a[24:27]\n" \
"s_add_u32 s88, s42, 0x2000\n" \
"ds_read_b128 v[112:115], v49 offset:6144\n" \
"v_mfma_f32_16x16x32_bf16 a[56:59], v[120:123], v[64:67], a[56:59]\n" \
"buffer_store_dwordx4 v[240:243], v20, s[80:83], s88 offen offset:1024 sc1\n" \
"v_mfma_f32_16x16x32_bf16 a[56:59], v[116:119], v[60:63], a[56:59]\n" \
"s_add_u32 s62, s62, 0x80\n" \
"v_writelane_b32 v11, s100, 33\n" \
"v_mfma_f32_16x16x32_bf16 a[88:91], v[60:63], v[132:135], a[88:91]\n" \
"s_cmp_lt_u32 s62, 0x480\n" \
"s_cselect_b32 s62, s62, 0\n" \
"v_mfma_f32_16x16x32_bf16 a[88:91], v[64:67], v[136:139], a[88:91]\n" \
"s_lshl_b32 s72, s62, 6\n" \
"s_add_u32 s72, s72, s37\n" \
"v_smfmac_f32_16x16x64_bf16 a[120:123], v[140:143], v[68:75], v50\n" \
"v_mfma_f32_16x16x32_bf16 a[28:31], v[128:131], v[72:75], a[28:31]\n" \
"s_lshl_b32 s63, s62, 12\n" \
"s_add_u32 s98, 0x3004000, s66\n" \
"v_mfma_f32_16x16x32_bf16 a[28:31], v[124:127], v[68:71], a[28:31]\n" \
"s_cmp_lg_u32 s99, 0\n" \
"s_cselect_b32 s97, s67, s96\n" \
"v_mfma_f32_16x16x32_bf16 a[60:63], v[116:119], v[68:71], a[60:63]\n" \
"s_add_u32 s89, s42, 0x3000\n" \
"s_lshr_b32 s95, s94, 1\n" \
"v_mfma_f32_16x16x32_bf16 a[60:63], v[120:123], v[72:75], a[60:63]\n" \
"buffer_store_dwordx4 v[244:247], v20, s[80:83], s89 offen offset:1024 sc1\n" \
"v_mfma_f32_16x16x32_bf16 a[92:95], v[72:75], v[136:139], a[92:95]\n" \
"s_add_u32 s64, 0x4000, s97\n" \
"v_writelane_b32 v11, s98, 34\n" \
"v_mfma_f32_16x16x32_bf16 a[92:95], v[68:71], v[132:135], a[92:95]\n" \
"s_add_u32 s101, 0x3004000, s65\n" \
"s_and_b32 s96, 2, %[w]\n" \
"v_smfmac_f32_16x16x64_bf16 a[124:127], v[140:143], v[76:83], v50\n" \
"v_mfma_f32_16x16x32_bf16 a[0:3], v[124:127], v[76:79], a[0:3]\n" \
"s_cmp_eq_u32 %[w], s95\n" \
"s_cselect_b32 s100, s64, 0x40000000\n" \
"v_mfma_f32_16x16x32_bf16 a[0:3], v[128:131], v[80:83], a[0:3]\n" \
"v_writelane_b32 v11, s101, 35\n" \
"s_lshl_b32 s97, s96, 1\n" \
"v_mfma_f32_16x16x32_bf16 a[4:7], v[120:123], v[80:83], a[4:7]\n" \
"s_and_b32 s98, 1, %[w]\n" \
"v_writelane_b32 v11, s100, 36\n" \
"v_mfma_f32_16x16x32_bf16 a[4:7], v[116:119], v[76:79], a[4:7]\n" \
"s_lshr_b32 s94, %[b], 5\n" \
"s_add_u32 s99, s97, s98\n" \
"v_mfma_f32_16x16x32_bf16 a[32:35], v[76:79], v[132:135], a[32:35]\n" \
"s_lshl_b32 s95, s94, 7\n" \
"s_lshl_b32 s100, s99, 4\n" \
"v_mfma_f32_16x16x32_bf16 a[32:35], v[80:83], v[136:139], a[32:35]\n" \
"s_add_u32 s101, s95, s100\n" \
"s_waitcnt lgkmcnt(0)\n" \
"v_mfma_f32_16x16x32_bf16 a[128:131], v[152:155], v[56:59], a[128:131]\n" \
"s_mul_i32 s64, 0x480, s101\n" \
"s_lshr_b32 s65, %[b], 3\n" \
"v_mfma_f32_16x16x32_bf16 a[128:131], v[148:151], v[52:55], a[128:131]\n" \
"s_sub_u32 s99, 0x780, s95\n" \
"s_and_b32 s66, 3, s65\n" \
"v_mfma_f32_16x16x32_bf16 a[132:135], v[156:159], v[52:55], a[132:135]\n" \
"s_add_u32 s101, s99, s100\n" \
"s_mul_i32 s67, 0x120, s66\n" \
"v_mfma_f32_16x16x32_bf16 a[132:135], v[160:163], v[56:59], a[132:135]\n" \
"s_and_b32 s96, 7, %[b]\n" \
"s_mul_i32 s65, 0x480, s101\n" \
"v_mfma_f32_16x16x32_bf16 a[136:139], v[168:171], v[56:59], a[136:139]\n" \
"s_add_u32 s94, s64, s67\n" \
"s_lshl_b32 s97, s96, 5\n" \
"v_mfma_f32_16x16x32_bf16 a[136:139], v[164:167], v[52:55], a[136:139]\n" \
"s_add_u32 s66, s65, s67\n" \
"s_and_b32 s95, 1, s96\n" \
"v_mfma_f32_16x16x32_bf16 a[140:143], v[172:175], v[52:55], a[140:143]\n" \
"s_add_u32 s98, s94, s97\n" \
"v_writelane_b32 v11, s98, 39\n" \
"v_mfma_f32_16x16x32_bf16 a[140:143], v[176:179], v[56:59], a[140:143]\n" \
"s_cmp_lg_u32 s95, 0\n" \
"s_cselect_b32 s99, s66, s94\n" \
"v_mfma_f32_16x16x32_bf16 a[160:163], v[152:155], v[64:67], a[160:163]\n" \
"s_lshr_b32 s98, s96, 1\n" \
"s_add_u32 s100, 0x100, s99\n" \
"v_mfma_f32_16x16x32_bf16 a[160:163], v[148:151], v[60:63], a[160:163]\n" \
"s_add_u32 s64, s66, s97\n" \
"s_mov_b32 s94, s67\n" \
"v_mfma_f32_16x16x32_bf16 a[164:167], v[156:159], v[60:63], a[164:167]\n" \
"s_cmp_eq_u32 %[w], s98\n" \
"s_cselect_b32 s101, s100, 0x40000000\n" \
"v_mfma_f32_16x16x32_bf16 a[164:167], v[160:163], v[64:67], a[164:167]\n" \
"v_writelane_b32 v11, s64, 40\n" \
"v_writelane_b32 v11, s101, 41\n" \
"v_mfma_f32_16x16x32_bf16 a[168:171], v[168:171], v[64:67], a[168:171]\n" \
"s_add_u32 s96, s16, s94\n" \
"s_addc_u32 s97, s17, 0\n" \
"v_mfma_f32_16x16x32_bf16 a[168:171], v[164:167], v[60:63], a[168:171]\n" \
"s_add_u32 s98, s18, s94\n" \
"s_addc_u32 s99, s19, 0\n" \
"v_mfma_f32_16x16x32_bf16 a[172:175], v[172:175], v[60:63], a[172:175]\n" \
"s_and_b32 s97, s97, 0xffff\n" \
"v_writelane_b32 v11, s96, 7\n" \
"v_mfma_f32_16x16x32_bf16 a[172:175], v[176:179], v[64:67], a[172:175]\n" \
"v_writelane_b32 v11, s97, 8\n" \
"s_and_b32 s99, s99, 0xffff\n" \
"v_mfma_f32_16x16x32_bf16 a[192:195], v[152:155], v[72:75], a[192:195]\n" \
"v_writelane_b32 v11, s98, 9\n" \
"v_writelane_b32 v11, s99, 10\n" \
"v_mfma_f32_16x16x32_bf16 a[192:195], v[148:151], v[68:71], a[192:195]\n" \
"s_cmp_lt_u32 %[w], 3\n" \
"s_cselect_b64 s[96:97], s[96:97], s[98:99]\n" \
"s_waitcnt vmcnt(3)\n" \
"s_barrier\n" \
"v_mfma_f32_16x16x32_bf16 a[196:199], v[156:159], v[68:71], a[196:199]\n" \
"s_add_u32 m0, s40, 0x0\n" \
"s_add_u32 s85, s72, 0x0\n" \
"v_mfma_f32_16x16x32_bf16 a[196:199], v[160:163], v[72:75], a[196:199]\n" \
"buffer_load_dwordx4 v20, s[80:83], s85 offen sc1 lds\n" \
"v_mfma_f32_16x16x32_bf16 a[200:203], v[168:171], v[72:75], a[200:203]\n" \
"s_add_u32 m0, s40, 0x1000\n" \
"s_add_u32 s84, s72, 0x1000\n" \
"v_mfma_f32_16x16x32_bf16 a[200:203], v[164:167], v[68:71], a[200:203]\n" \
"ds_read_b64_tr_b16 v[140:141], v39 offset:32768\n" \
"ds_read_b64_tr_b16 v[142:143], v39 offset:33728\n" \
"v_mfma_f32_16x16x32_bf16 a[204:207], v[172:175], v[68:71], a[204:207]\n" \
"buffer_load_dwordx4 v20, s[80:83], s84 offen sc1 lds\n" \
"v_mfma_f32_16x16x32_bf16 a[204:207], v[176:179], v[72:75], a[204:207]\n" \
"ds_read_b64_tr_b16 v[128:129], v37 offset:36864\n" \
"ds_read_b64_tr_b16 v[130:131], v37 offset:37376\n" \
"v_mfma_f32_16x16x32_bf16 a[224:227], v[152:155], v[80:83], a[224:227]\n" \
"s_add_u32 m0, s40, 0x3000\n" \
"s_add_u32 s85, s62, 0x0\n" \
"v_mfma_f32_16x16x32_bf16 a[224:227], v[148:151], v[76:79], a[224:227]\n" \
"buffer_load_dwordx4 v21, s[76:79], s85 offen lds\n" \
"v_mfma_f32_16x16x32_bf16 a[228:231], v[156:159], v[76:79], a[228:231]\n" \
"ds_read_b64_tr_b16 v[124:125], v37 offset:32768\n" \
"ds_read_b64_tr_b16 v[126:127], v37 offset:33280\n" \
"v_mfma_f32_16x16x32_bf16 a[228:231], v[160:163], v[80:83], a[228:231]\n" \
"s_add_u32 m0, %[mA], 0x0\n" \
"s_add_u32 s84, s62, 0x0\n" \
"s_nop 0\n" \
"v_mfma_f32_16x16x32_bf16 a[232:235], v[168:171], v[80:83], a[232:235]\n" \
"buffer_load_dwordx4 v24, s[76:79], s84 offen lds\n" \
"v_mfma_f32_16x16x32_bf16 a[232:235], v[164:167], v[76:79], a[232:235]\n" \
"ds_read_b64_tr_b16 v[132:133], v38 offset:32768\n" \
"ds_read_b64_tr_b16 v[134:135], v38 offset:33280\n" \
"v_mfma_f32_16x16x32_bf16 a[236:239], v[172:175], v[76:79], a[236:239]\n" \
"ds_read_b64_tr_b16 v[136:137], v38 offset:36864\n" \
"ds_read_b64_tr_b16 v[138:139], v38 offset:37376\n" \
"v_mfma_f32_16x16x32_bf16 a[236:239], v[176:179], v[80:83], a[236:239]\n" \
"s_add_u32 m0, %[mA], 0x400\n" \
"s_add_u32 s85, s62, 0x2400\n" \
"v_mfma_f32_16x16x32_bf16 a[144:147], v[152:155], v[88:91], a[144:147]\n" \
"buffer_load_dwordx4 v25, s[76:79], s85 offen lds\n" \
"v_mfma_f32_16x16x32_bf16 a[144:147], v[148:151], v[84:87], a[144:147]\n" \
"ds_read_b128 v[52:55], v28 offset:32768\n" \
"ds_read_b128 v[56:59], v29 offset:32768\n" \
"v_mfma_f32_16x16x32_bf16 a[148:151], v[156:159], v[84:87], a[148:151]\n" \
"s_add_u32 m0, %[mB], 0x0\n" \
"s_add_u32 s84, s63, 0x0\n" \
"v_mfma_f32_16x16x32_bf16 a[148:151], v[160:163], v[88:91], a[148:151]\n" \
"buffer_load_dwordx4 v26, s[68:71], s84 offen lds\n" \
"v_mfma_f32_16x16x32_bf16 a[152:155], v[168:171], v[88:91], a[152:155]\n" \
"ds_read_b64_tr_b16 v[116:117], v36 offset:32768\n" \
"ds_read_b64_tr_b16 v[118:119], v36 offset:33280\n" \
"v_mfma_f32_16x16x32_bf16 a[152:155], v[164:167], v[84:87], a[152:155]\n" \
"ds_read_b64_tr_b16 v[120:121], v36 offset:36864\n" \
"ds_read_b64_tr_b16 v[122:123], v36 offset:37376\n" \
"v_mfma_f32_16x16x32_bf16 a[156:159], v[172:175], v[84:87], a[156:159]\n" \
"s_add_u32 m0, %[mA], 0x800\n" \
"s_add_u32 s85, s62, 0x4800\n" \
"v_mfma_f32_16x16x32_bf16 a[156:159], v[176:179], v[88:91], a[156:159]\n" \
"buffer_load_dwordx4 v24, s[76:79], s85 offen lds\n" \
"v_mfma_f32_16x16x32_bf16 a[176:179], v[152:155], v[96:99], a[176:179]\n" \
"L_pf7:\n" \
"s_cbranch_execz L_pf11\n" \
"s_nop 0\n" \
"ds_read_b128 v[84:87], v22 offset:32768\n" \
"ds_read_b128 v[88:91], v23 offset:32768\n" \
"v_mfma_f32_16x16x32_bf16 a[176:179], v[148:151], v[92:95], a[176:179]\n" \
"s_add_u32 m0, %[mA], 0xc00\n" \
"s_add_u32 s84, s62, 0x6c00\n" \
"v_mfma_f32_16x16x32_bf16 a[180:183], v[156:159], v[92:95], a[180:183]\n" \
"buffer_load_dwordx4 v25, s[76:79], s84 offen lds\n" \
"v_mfma_f32_16x16x32_bf16 a[180:183], v[160:163], v[96:99], a[180:183]\n" \
"ds_read_b128 v[60:63], v28 offset:34816\n" \
"ds_read_b128 v[64:67], v29 offset:34816\n" \
"v_mfma_f32_16x16x32_bf16 a[184:187], v[168:171], v[96:99], a[184:187]\n" \
"ds_read_b128 v[68:71], v28 offset:36864\n" \
"ds_read_b128 v[72:75], v29 offset:36864\n" \
"v_mfma_f32_16x16x32_bf16 a[184:187], v[164:167], v[92:95], a[184:187]\n" \
"s_add_u32 m0, %[mB], 0x400\n" \
"s_add_u32 s85, s63, 0x8000\n" \
"v_mfma_f32_16x16x32_bf16 a[188:191], v[172:175], v[92:95], a[188:191]\n" \
"buffer_load_dwordx4 v26, s[68:71], s85 offen lds\n" \
"v_mfma_f32_16x16x32_bf16 a[188:191], v[176:179], v[96:99], a[188:191]\n" \
"ds_read_b128 v[76:79], v28 offset:38912\n" \
"ds_read_b128 v[80:83], v29 offset:38912\n" \
"v_mfma_f32_16x16x32_bf16 a[208:211], v[152:155], v[104:107], a[208:211]\n" \
"s_add_u32 m0, %[mA], 0x1000\n" \
"s_add_u32 s84, s62, 0x9000\n" \
"v_mfma_f32_16x16x32_bf16 a[208:211], v[148:151], v[100:103], a[208:211]\n" \
"buffer_load_dwordx4 v24, s[76:79], s84 offen lds\n" \
"v_mfma_f32_16x16x32_bf16 a[212:215], v[156:159], v[100:103], a[212:215]\n" \
"ds_read_b128 v[92:95], v48 offset:34816\n" \
"ds_read_b128 v[96:99], v49 offset:34816\n" \
"v_mfma_f32_16x16x32_bf16 a[212:215], v[160:163], v[104:107], a[212:215]\n" \
"v_writelane_b32 v11, s96, 13\n" \
"v_writelane_b32 v11, s97, 14\n" \
"v_mfma_f32_16x16x32_bf16 a[216:219], v[168:171], v[104:107], a[216:219]\n" \
"s_add_u32 m0, %[mA], 0x1400\n" \
"s_add_u32 s85, s62, 0xb400\n" \
"v_mfma_f32_16x16x32_bf16 a[216:219], v[164:167], v[100:103], a[216:219]\n" \
"buffer_load_dwordx4 v25, s[76:79], s85 offen lds\n" \
"v_mfma_f32_16x16x32_bf16 a[220:223], v[172:175], v[100:103], a[220:223]\n" \
"ds_read_b128 v[100:103], v48 offset:36864\n" \
"v_writelane_b32 v11, s20, 11\n" \
"v_mfma_f32_16x16x32_bf16 a[220:223], v[176:179], v[104:107], a[220:223]\n" \
"s_add_u32 m0, %[mB], 0x800\n" \
"s_add_u32 s84, s63, 0x10000\n" \
"v_mfma_f32_16x16x32_bf16 a[240:243], v[152:155], v[112:115], a[240:243]\n" \
"buffer_load_dwordx4 v27, s[68:71], s84 offen lds\n" \
"v_mfma_f32_16x16x32_bf16 a[240:243], v[148:151], v[108:111], a[240:243]\n" \
"ds_read_b64_tr_b16 v[152:153], v32 offset:40960\n" \
"ds_read_b64_tr_b16 v[154:155], v32 offset:41984\n" \
"v_mfma_f32_16x16x32_bf16 a[244:247], v[156:159], v[108:111], a[244:247]\n" \
"ds_read_b64_tr_b16 v[148:149], v32 offset:32768\n" \
"ds_read_b64_tr_b16 v[150:151], v32 offset:33792\n" \
"v_mfma_f32_16x16x32_bf16 a[244:247], v[160:163], v[112:115], a[244:247]\n" \
"s_add_u32 m0, %[mA], 0x1800\n" \
"s_add_u32 s85, s62, 0xd800\n" \
"v_mfma_f32_16x16x32_bf16 a[248:251], v[168:171], v[112:115], a[248:251]\n" \
"buffer_load_dwordx4 v24, s[76:79], s85 offen lds\n" \
"v_mfma_f32_16x16x32_bf16 a[248:251], v[164:167], v[108:111], a[248:251]\n" \
"ds_read_b64_tr_b16 v[156:157], v33 offset:32768\n" \
"ds_read_b64_tr_b16 v[158:159], v33 offset:33792\n" \
"v_mfma_f32_16x16x32_bf16 a[252:255], v[172:175], v[108:111], a[252:255]\n" \
"ds_read_b64_tr_b16 v[160:161], v33 offset:40960\n" \
"ds_read_b64_tr_b16 v[162:163], v33 offset:41984\n" \
"v_mfma_f32_16x16x32_bf16 a[252:255], v[176:179], v[112:115], a[252:255]\n" \
"s_add_u32 m0, %[mA], 0x1c00\n" \
"s_waitcnt lgkmcnt(11)\n" \
"s_nop 0\n" \
"v_smfmac_f32_16x16x64_bf16 a[16:19], v[140:143], v[84:91], v50\n" \
"v_mfma_f32_16x16x32_bf16 a[48:51], v[128:131], v[88:91], a[48:51]\n" \
"s_add_u32 s85, s62, 0xfc00\n" \
"ds_read_b64_tr_b16 v[168:169], v34 offset:40960\n" \
"v_mfma_f32_16x16x32_bf16 a[48:51], v[124:127], v[84:87], a[48:51]\n" \
"buffer_load_dwordx4 v25, s[76:79], s85 offen lds\n" \
"v_mfma_f32_16x16x32_bf16 a[80:83], v[84:87], v[132:135], a[80:83]\n" \
"s_add_u32 m0, %[mB], 0xc00\n" \
"s_add_u32 s84, s63, 0x18000\n" \
"v_mfma_f32_16x16x32_bf16 a[80:83], v[88:91], v[136:139], a[80:83]\n" \
"ds_read_b64_tr_b16 v[170:171], v34 offset:41984\n" \
"ds_read_b64_tr_b16 v[164:165], v34 offset:32768\n" \
"v_smfmac_f32_16x16x64_bf16 a[112:115], v[140:143], v[52:59], v50\n" \
"v_mfma_f32_16x16x32_bf16 a[20:23], v[128:131], v[56:59], a[20:23]\n" \
"buffer_load_dwordx4 v27, s[68:71], s84 offen lds\n" \
"v_mfma_f32_16x16x32_bf16 a[20:23], v[124:127], v[52:55], a[20:23]\n" \
"s_add_u32 s88, s43, 0x0\n" \
"s_nop 0\n" \
"ds_read_b64_tr_b16 v[166:167], v34 offset:33792\n" \
"v_mfma_f32_16x16x32_bf16 a[52:55], v[116:119], v[52:55], a[52:55]\n" \
"ds_read_b64_tr_b16 v[172:173], v35 offset:32768\n" \
"ds_read_b64_tr_b16 v[174:175], v35 offset:33792\n" \
"v_mfma_f32_16x16x32_bf16 a[52:55], v[120:123], v[56:59], a[52:55]\n" \
"buffer_store_dwordx4 v[248:251], v20, s[80:83], s88 offen offset:0 sc1\n" \
"v_mfma_f32_16x16x32_bf16 a[84:87], v[56:59], v[136:139], a[84:87]\n" \
"ds_read_b64_tr_b16 v[176:177], v35 offset:40960\n" \
"ds_read_b64_tr_b16 v[178:179], v35 offset:41984\n" \
"v_mfma_f32_16x16x32_bf16 a[84:87], v[52:55], v[132:135], a[84:87]\n" \
"ds_read_b128 v[84:87], v48 offset:32768\n" \
"ds_read_b128 v[88:91], v49 offset:32768\n" \
"v_smfmac_f32_16x16x64_bf16 a[116:119], v[140:143], v[60:67], v50\n" \
"v_mfma_f32_16x16x32_bf16 a[24:27], v[124:127], v[60:63], a[24:27]\n" \
"ds_read_b128 v[104:107], v49 offset:36864\n" \
"ds_read_b128 v[108:111], v48 offset:38912\n" \
"v_mfma_f32_16x16x32_bf16 a[24:27], v[128:131], v[64:67], a[24:27]\n" \
"s_add_u32 s89, s45, 0x1000\n" \
"ds_read_b128 v[112:115], v49 offset:38912\n" \
"v_mfma_f32_16x16x32_bf16 a[56:59], v[120:123], v[64:67], a[56:59]\n" \
"buffer_store_dwordx4 v[184:187], v20, s[80:83], s89 offen offset:2048 sc1\n" \
"v_mfma_f32_16x16x32_bf16 a[56:59], v[116:119], v[60:63], a[56:59]\n" \
"s_add_u32 s62, s62, 0x80\n" \
"s_and_b32 s95, s21, 0xffff\n" \
"v_mfma_f32_16x16x32_bf16 a[88:91], v[60:63], v[132:135], a[88:91]\n" \
"s_cmp_lt_u32 s62, 0x480\n" \
"s_cselect_b32 s62, s62, 0\n" \
"v_mfma_f32_16x16x32_bf16 a[88:91], v[64:67], v[136:139], a[88:91]\n" \
"s_lshl_b32 s72, s62, 6\n" \
"s_add_u32 s72, s72, s37\n" \
"v_smfmac_f32_16x16x64_bf16 a[120:123], v[140:143], v[68:75], v50\n" \
"v_mfma_f32_16x16x32_bf16 a[28:31], v[128:131], v[72:75], a[28:31]\n" \
"s_lshl_b32 s63, s62, 12\n" \
"v_writelane_b32 v11, s95, 12\n" \
"v_mfma_f32_16x16x32_bf16 a[28:31], v[124:127], v[68:71], a[28:31]\n" \
"s_and_b32 s95, 1, %[w]\n" \
"s_lshr_b32 s96, %[w], 1\n" \
"v_mfma_f32_16x16x32_bf16 a[60:63], v[116:119], v[68:71], a[60:63]\n" \
"s_add_u32 s88, s45, 0x1000\n" \
"s_lshl_b32 s94, %[w], 11\n" \
"v_mfma_f32_16x16x32_bf16 a[60:63], v[120:123], v[72:75], a[60:63]\n" \
"buffer_store_dwordx4 v[252:255], v20, s[80:83], s88 offen offset:3072 sc1\n" \
"v_mfma_f32_16x16x32_bf16 a[92:95], v[72:75], v[136:139], a[92:95]\n" \
"s_lshl_b32 s15, s95, 12\n" \
"s_lshl_b32 s16, s96, 14\n" \
"v_mfma_f32_16x16x32_bf16 a[92:95], v[68:71], v[132:135], a[92:95]\n" \
"s_add_u32 s12, 0xc000, s94\n" \
"s_add_u32 s97, s16, s15\n" \
"v_smfmac_f32_16x16x64_bf16 a[124:127], v[140:143], v[76:83], v50\n" \
"v_mfma_f32_16x16x32_bf16 a[0:3], v[124:127], v[76:79], a[0:3]\n" \
"s_lshr_b32 s100, %[b], 5\n" \
"s_and_b32 s65, 7, %[b]\n" \
"v_mfma_f32_16x16x32_bf16 a[0:3], v[128:131], v[80:83], a[0:3]\n" \
"s_lshr_b32 s67, %[b], 3\n" \
"s_xor_b32 s13, 0x8000, s97\n" \
"v_mfma_f32_16x16x32_bf16 a[4:7], v[120:123], v[80:83], a[4:7]\n" \
"s_add_u32 s20, 0x14000, s97\n" \
"s_mul_i32 s98, 0xe000, s96\n" \
"v_mfma_f32_16x16x32_bf16 a[4:7], v[116:119], v[76:79], a[4:7]\n" \
"s_lshl_b32 s18, s100, 1\n" \
"s_lshl_b32 s66, s65, 2\n" \
"v_mfma_f32_16x16x32_bf16 a[32:35], v[76:79], v[132:135], a[32:35]\n" \
"s_and_b32 s94, 3, s67\n" \
"s_add_u32 s99, s98, s15\n" \
"v_mfma_f32_16x16x32_bf16 a[32:35], v[80:83], v[136:139], a[32:35]\n" \
"s_add_u32 s101, s18, s96\n" \
"s_waitcnt lgkmcnt(0)\n" \
"v_mfma_f32_16x16x32_bf16 a[128:131], v[152:155], v[56:59], a[128:131]\n" \
"s_mov_b32 s64, 0x4000\n" \
"s_add_u32 s97, s66, s94\n" \
"v_mfma_f32_16x16x32_bf16 a[128:131], v[148:151], v[52:55], a[128:131]\n" \
"s_add_u32 s21, 0x10000, s99\n" \
"s_lshl_b32 s14, s101, 15\n" \
"v_mfma_f32_16x16x32_bf16 a[132:135], v[156:159], v[52:55], a[132:135]\n" \
"s_cmp_lg_u32 s100, 0\n" \
"s_cselect_b32 s17, s64, 0x8000\n" \
"v_mfma_f32_16x16x32_bf16 a[132:135], v[160:163], v[56:59], a[132:135]\n" \
"s_lshl_b32 s98, s97, 8\n" \
"s_and_b32 s67, 2, %[w]\n" \
"v_mfma_f32_16x16x32_bf16 a[136:139], v[168:171], v[56:59], a[136:139]\n" \
"s_add_u32 s99, 0x5be8020, s98\n" \
"s_lshl_b32 s101, s100, 2\n" \
"v_mfma_f32_16x16x32_bf16 a[136:139], v[164:167], v[52:55], a[136:139]\n" \
"s_lshl_b32 s66, s67, 1\n" \
"s_add_u32 s64, s99, s101\n" \
"v_mfma_f32_16x16x32_bf16 a[140:143], v[172:175], v[52:55], a[140:143]\n" \
"s_lshl_b32 s65, s100, 3\n" \
"s_add_u32 s94, s66, s95\n" \
"v_mfma_f32_16x16x32_bf16 a[140:143], v[176:179], v[56:59], a[140:143]\n" \
"s_cmp_eq_u32 %[w], 1\n" \
"s_cselect_b32 s22, s64, 0x40000000\n" \
"v_mfma_f32_16x16x32_bf16 a[160:163], v[152:155], v[64:67], a[160:163]\n" \
"s_add_u32 s98, s65, s94\n" \
"s_mul_i32 s99, 0x24800, s98\n" \
"v_mfma_f32_16x16x32_bf16 a[160:163], v[148:151], v[60:63], a[160:163]\n" \
"s_lshl_b32 s101, s97, 12\n" \
"s_sub_u32 s67, 0x78, s65\n" \
"v_mfma_f32_16x16x32_bf16 a[164:167], v[156:159], v[60:63], a[164:167]\n" \
"s_add_u32 s64, s99, s101\n" \
"s_add_u32 s66, s67, s94\n" \
"v_mfma_f32_16x16x32_bf16 a[164:167], v[160:163], v[64:67], a[164:167]\n" \
"s_add_u32 s60, 0x3004800, s64\n" \
"s_mul_i32 s95, 0x24800, s66\n" \
"v_mfma_f32_16x16x32_bf16 a[168:171], v[168:171], v[64:67], a[168:171]\n" \
"s_lshl_b32 s99, s96, 1\n" \
"s_add_u32 s98, s95, s101\n" \
"v_mfma_f32_16x16x32_bf16 a[168:171], v[164:167], v[60:63], a[168:171]\n" \
"s_add_u32 s64, s100, s99\n" \
"s_mov_b32 s65, 0x18000000\n" \
"v_mfma_f32_16x16x32_bf16 a[172:175], v[172:175], v[60:63], a[172:175]\n" \
"s_mul_i32 s94, 6, s96\n" \
"s_add_u32 s66, s96, s94\n" \
"v_mfma_f32_16x16x32_bf16 a[172:175], v[176:179], v[64:67], a[172:175]\n" \
"s_cmp_lt_u32 s64, 2\n" \
"s_cselect_b32 s67, s65, 0x30000000\n" \
"v_mfma_f32_16x16x32_bf16 a[192:195], v[152:155], v[72:75], a[192:195]\n" \
"s_mov_b32 s95, 0x80000\n" \
"s_mov_b32 s64, 0xaaaaaaaa\n" \
"v_mfma_f32_16x16x32_bf16 a[192:195], v[148:151], v[68:71], a[192:195]\n" \
"s_cmp_gt_u32 s66, s100\n" \
"s_cselect_b32 s101, s95, 0x40000\n" \
"s_waitcnt vmcnt(3)\n" \
"s_barrier\n" \
"s_mov_b64 exec, 1\n" \
"buffer_store_dword v45, off, s[80:83], s38 offset:0 sc1\n" \
"s_mov_b64 exec, -1\n" \
"v_mfma_f32_16x16x32_bf16 a[196:199], v[156:159], v[68:71], a[196:199]\n" \
"s_add_u32 m0, s40, 0x8000\n" \
"s_add_u32 s85, s72, 0x0\n" \
"s_nop 0\n" \
"v_mfma_f32_16x16x32_bf16 a[196:199], v[160:163], v[72:75], a[196:199]\n" \
"ds_read_b64_tr_b16 v[140:141], v39 offset:0\n" \
"ds_read_b64_tr_b16 v[142:143], v39 offset:960\n" \
"v_mfma_f32_16x16x32_bf16 a[200:203], v[168:171], v[72:75], a[200:203]\n" \
"buffer_load_dwordx4 v20, s[80:83], s85 offen sc1 lds\n" \
"v_mfma_f32_16x16x32_bf16 a[200:203], v[164:167], v[68:71], a[200:203]\n" \
"s_add_u32 m0, s40, 0x9000\n" \
"s_add_u32 s84, s72, 0x1000\n" \
"v_mfma_f32_16x16x32_bf16 a[204:207], v[172:175], v[68:71], a[204:207]\n" \
"ds_read_b64_tr_b16 v[128:129], v37 offset:4096\n" \
"ds_read_b64_tr_b16 v[130:131], v37 offset:4608\n" \
"v_mfma_f32_16x16x32_bf16 a[204:207], v[176:179], v[72:75], a[204:207]\n" \
"buffer_load_dwordx4 v20, s[80:83], s84 offen sc1 lds\n" \
"v_mfma_f32_16x16x32_bf16 a[224:227], v[152:155], v[80:83], a[224:227]\n" \
"s_add_u32 m0, s40, 0xb000\n" \
"s_add_u32 s85, s62, 0x0\n" \
"v_mfma_f32_16x16x32_bf16 a[224:227], v[148:151], v[76:79], a[224:227]\n" \
"ds_read_b64_tr_b16 v[124:125], v37 offset:0\n" \
"ds_read_b64_tr_b16 v[126:127], v37 offset:512\n" \
"v_mfma_f32_16x16x32_bf16 a[228:231], v[156:159], v[76:79], a[228:231]\n" \
"buffer_load_dwordx4 v21, s[76:79], s85 offen lds\n" \
"v_mfma_f32_16x16x32_bf16 a[228:231], v[160:163], v[80:83], a[228:231]\n" \
"s_add_u32 m0, %[mA], 0x8000\n" \
"s_add_u32 s84, s62, 0x0\n" \
"v_mfma_f32_16x16x32_bf16 a[232:235], v[168:171], v[80:83], a[232:235]\n" \
"ds_read_b64_tr_b16 v[132:133], v38 offset:0\n" \
"ds_read_b64_tr_b16 v[134:135], v38 offset:512\n" \
"v_mfma_f32_16x16x32_bf16 a[232:235], v[164:167], v[76:79], a[232:235]\n" \
"buffer_load_dwordx4 v24, s[76:79], s84 offen lds\n" \
"v_mfma_f32_16x16x32_bf16 a[236:239], v[172:175], v[76:79], a[236:239]\n" \
"ds_read_b64_tr_b16 v[136:137], v38 offset:4096\n" \
"ds_read_b64_tr_b16 v[138:139], v38 offset:4608\n" \
"v_mfma_f32_16x16x32_bf16 a[236:239], v[176:179], v[80:83], a[236:239]\n" \
"s_add_u32 m0, %[mA], 0x8400\n" \
"s_add_u32 s85, s62, 0x2400\n" \
"v_mfma_f32_16x16x32_bf16 a[144:147], v[152:155], v[88:91], a[144:147]\n" \
"buffer_load_dwordx4 v25, s[76:79], s85 offen lds\n" \
"v_mfma_f32_16x16x32_bf16 a[144:147], v[148:151], v[84:87], a[144:147]\n" \
"ds_read_b128 v[52:55], v28 offset:0\n" \
"ds_read_b128 v[56:59], v29 offset:0\n" \
"v_mfma_f32_16x16x32_bf16 a[148:151], v[156:159], v[84:87], a[148:151]\n" \
"s_add_u32 m0, %[mB], 0x8000\n" \
"s_add_u32 s84, s63, 0x0\n" \
"s_nop 0\n" \
"v_mfma_f32_16x16x32_bf16 a[148:151], v[160:163], v[88:91], a[148:151]\n" \
"buffer_load_dwordx4 v26, s[68:71], s84 offen lds\n" \
"v_mfma_f32_16x16x32_bf16 a[152:155], v[168:171], v[88:91], a[152:155]\n" \
"ds_read_b64_tr_b16 v[116:117], v36 offset:0\n" \
"ds_read_b64_tr_b16 v[118:119], v36 offset:512\n" \
"v_mfma_f32_16x16x32_bf16 a[152:155], v[164:167], v[84:87], a[152:155]\n" \
"ds_read_b64_tr_b16 v[120:121], v36 offset:4096\n" \
"ds_read_b64_tr_b16 v[122:123], v36 offset:4608\n" \
"v_mfma_f32_16x16x32_bf16 a[156:159], v[172:175], v[84:87], a[156:159]\n" \
"s_add_u32 m0, %[mA], 0x8800\n" \
"s_add_u32 s85, s62, 0x4800\n" \
"v_mfma_f32_16x16x32_bf16 a[156:159], v[176:179], v[88:91], a[156:159]\n" \
"buffer_load_dwordx4 v24, s[76:79], s85 offen lds\n" \
"v_mfma_f32_16x16x32_bf16 a[176:179], v[152:155], v[96:99], a[176:179]\n" \
"ds_read_b128 v[84:87], v22 offset:0\n" \
"ds_read_b128 v[88:91], v23 offset:0\n" \
"v_mfma_f32_16x16x32_bf16 a[176:179], v[148:151], v[92:95], a[176:179]\n" \
"s_add_u32 m0, %[mA], 0x8c00\n" \
"s_add_u32 s84, s62, 0x6c00\n" \
"v_mfma_f32_16x16x32_bf16 a[180:183], v[156:159], v[92:95], a[180:183]\n" \
"buffer_load_dwordx4 v25, s[76:79], s84 offen lds\n" \
"v_mfma_f32_16x16x32_bf16 a[180:183], v[160:163], v[96:99], a[180:183]\n" \
"ds_read_b128 v[60:63], v28 offset:2048\n" \
"ds_read_b128 v[64:67], v29 offset:2048\n" \
"v_mfma_f32_16x16x32_bf16 a[184:187], v[168:171], v[96:99], a[184:187]\n" \
"ds_read_b128 v[68:71], v28 offset:4096\n" \
"ds_read_b128 v[72:75], v29 offset:4096\n" \
"v_mfma_f32_16x16x32_bf16 a[184:187], v[164:167], v[92:95], a[184:187]\n" \
"s_add_u32 m0, %[mB], 0x8400\n" \
"s_add_u32 s85, s63, 0x8000\n" \
"v_mfma_f32_16x16x32_bf16 a[188:191], v[172:175], v[92:95], a[188:191]\n" \
"buffer_load_dwordx4 v26, s[68:71], s85 offen lds\n" \
"v_mfma_f32_16x16x32_bf16 a[188:191], v[176:179], v[96:99], a[188:191]\n" \
"ds_read_b128 v[76:79], v28 offset:6144\n" \
"ds_read_b128 v[80:83], v29 offset:6144\n" \
"v_mfma_f32_16x16x32_bf16 a[208:211], v[152:155], v[104:107], a[208:211]\n" \
"s_add_u32 m0, %[mA], 0x9000\n" \
"s_add_u32 s84, s62, 0x9000\n" \
"v_mfma_f32_16x16x32_bf16 a[208:211], v[148:151], v[100:103], a[208:211]\n" \
"buffer_load_dwordx4 v24, s[76:79], s84 offen lds\n" \
"v_mfma_f32_16x16x32_bf16 a[212:215], v[156:159], v[100:103], a[212:215]\n" \
"ds_read_b128 v[92:95], v48 offset:2048\n" \
"ds_read_b128 v[96:99], v49 offset:2048\n" \
"v_mfma_f32_16x16x32_bf16 a[212:215], v[160:163], v[104:107], a[212:215]\n" \
"s_mov_b32 s65, 0xaaaaaaaa\n" \
"s_add_u32 s23, s67, s101\n" \
"s_nop 0\n" \
"v_mfma_f32_16x16x32_bf16 a[216:219], v[168:171], v[104:107], a[216:219]\n" \
"s_add_u32 m0, %[mA], 0x9400\n" \
"s_add_u32 s85, s62, 0xb400\n" \
"v_mfma_f32_16x16x32_bf16 a[216:219], v[164:167], v[100:103], a[216:219]\n" \
"buffer_load_dwordx4 v25, s[76:79], s85 offen lds\n" \
"v_mfma_f32_16x16x32_bf16 a[220:223], v[172:175], v[100:103], a[220:223]\n" \
"ds_read_b128 v[100:103], v48 offset:4096\n" \
"s_mov_b32 s66, 0xfff0ffff\n" \
"v_mfma_f32_16x16x32_bf16 a[220:223], v[176:179], v[104:107], a[220:223]\n" \
"s_add_u32 m0, %[mB], 0x8800\n" \
"s_add_u32 s84, s63, 0x10000\n" \
"v_mfma_f32_16x16x32_bf16 a[240:243], v[152:155], v[112:115], a[240:243]\n" \
"buffer_load_dwordx4 v27, s[68:71], s84 offen lds\n" \
"v_mfma_f32_16x16x32_bf16 a[240:243], v[148:151], v[108:111], a[240:243]\n" \
"ds_read_b64_tr_b16 v[152:153], v32 offset:8192\n" \
"ds_read_b64_tr_b16 v[154:155], v32 offset:9216\n" \
"v_mfma_f32_16x16x32_bf16 a[244:247], v[156:159], v[108:111], a[244:247]\n" \
"ds_read_b64_tr_b16 v[148:149], v32 offset:0\n" \
"ds_read_b64_tr_b16 v[150:151], v32 offset:1024\n" \
"v_mfma_f32_16x16x32_bf16 a[244:247], v[160:163], v[112:115], a[244:247]\n" \
"s_add_u32 m0, %[mA], 0x9800\n" \
"s_add_u32 s85, s62, 0xd800\n" \
"v_mfma_f32_16x16x32_bf16 a[248:251], v[168:171], v[112:115], a[248:251]\n" \
"buffer_load_dwordx4 v24, s[76:79], s85 offen lds\n" \
"v_mfma_f32_16x16x32_bf16 a[248:251], v[164:167], v[108:111], a[248:251]\n" \
"ds_read_b64_tr_b16 v[156:157], v33 offset:0\n" \
"ds_read_b64_tr_b16 v[158:159], v33 offset:1024\n" \
"v_mfma_f32_16x16x32_bf16 a[252:255], v[172:175], v[108:111], a[252:255]\n" \
"ds_read_b64_tr_b16 v[160:161], v33 offset:8192\n" \
"ds_read_b64_tr_b16 v[162:163], v33 offset:9216\n" \
"v_mfma_f32_16x16x32_bf16 a[252:255], v[176:179], v[112:115], a[252:255]\n" \
"s_add_u32 m0, %[mA], 0x9c00\n" \
"s_waitcnt lgkmcnt(11)\n" \
"s_nop 0\n" \
"v_smfmac_f32_16x16x64_bf16 a[16:19], v[140:143], v[84:91], v50\n" \
"v_mfma_f32_16x16x32_bf16 a[48:51], v[128:131], v[88:91], a[48:51]\n" \
"s_add_u32 s85, s62, 0xfc00\n" \
"ds_read_b64_tr_b16 v[168:169], v34 offset:8192\n" \
"v_mfma_f32_16x16x32_bf16 a[48:51], v[124:127], v[84:87], a[48:51]\n" \
"buffer_load_dwordx4 v25, s[76:79], s85 offen lds\n" \
"v_mfma_f32_16x16x32_bf16 a[80:83], v[84:87], v[132:135], a[80:83]\n" \
"s_add_u32 m0, %[mB], 0x8c00\n" \
"s_add_u32 s84, s63, 0x18000\n" \
"v_mfma_f32_16x16x32_bf16 a[80:83], v[88:91], v[136:139], a[80:83]\n" \
"ds_read_b64_tr_b16 v[170:171], v34 offset:9216\n" \
"ds_read_b64_tr_b16 v[164:165], v34 offset:0\n" \
"v_smfmac_f32_16x16x64_bf16 a[112:115], v[140:143], v[52:59], v50\n" \
"v_mfma_f32_16x16x32_bf16 a[20:23], v[128:131], v[56:59], a[20:23]\n" \
"buffer_load_dwordx4 v27, s[68:71], s84 offen lds\n" \
"v_mfma_f32_16x16x32_bf16 a[20:23], v[124:127], v[52:55], a[20:23]\n" \
"s_add_u32 s89, s43, 0x1000\n" \
"ds_read_b64_tr_b16 v[166:167], v34 offset:1024\n" \
"v_mfma_f32_16x16x32_bf16 a[52:55], v[116:119], v[52:55], a[52:55]\n" \
"ds_read_b64_tr_b16 v[172:173], v35 offset:0\n" \
"ds_read_b64_tr_b16 v[174:175], v35 offset:1024\n" \
"v_mfma_f32_16x16x32_bf16 a[52:55], v[120:123], v[56:59], a[52:55]\n" \
"buffer_store_dwordx4 a[36:39], v20, s[80:83], s89 offen offset:0 sc1\n" \
"v_mfma_f32_16x16x32_bf16 a[84:87], v[56:59], v[136:139], a[84:87]\n" \
"ds_read_b64_tr_b16 v[176:177], v35 offset:8192\n" \
"ds_read_b64_tr_b16 v[178:179], v35 offset:9216\n" \
"v_mfma_f32_16x16x32_bf16 a[84:87], v[52:55], v[132:135], a[84:87]\n" \
"ds_read_b128 v[84:87], v48 offset:0\n" \
"ds_read_b128 v[88:91], v49 offset:0\n" \
"v_smfmac_f32_16x16x64_bf16 a[116:119], v[140:143], v[60:67], v50\n" \
"v_mfma_f32_16x16x32_bf16 a[24:27], v[124:127], v[60:63], a[24:27]\n" \
"ds_read_b128 v[104:107], v49 offset:4096\n" \
"ds_read_b128 v[108:111], v48 offset:6144\n" \
"v_mfma_f32_16x16x32_bf16 a[24:27], v[128:131], v[64:67], a[24:27]\n" \
"s_add_u32 s88, s43, 0x2000\n" \
"ds_read_b128 v[112:115], v49 offset:6144\n" \
"v_mfma_f32_16x16x32_bf16 a[56:59], v[120:123], v[64:67], a[56:59]\n" \
"buffer_store_dwordx4 a[64:67], v20, s[80:83], s88 offen offset:0 sc1\n" \
"v_mfma_f32_16x16x32_bf16 a[56:59], v[116:119], v[60:63], a[56:59]\n" \
"s_add_u32 s62, s62, 0x80\n" \
"s_mov_b32 s67, 0xf000ff00\n" \
"v_mfma_f32_16x16x32_bf16 a[88:91], v[60:63], v[132:135], a[88:91]\n" \
"s_cmp_lt_u32 s62, 0x480\n" \
"s_cselect_b32 s62, s62, 0\n" \
"v_mfma_f32_16x16x32_bf16 a[88:91], v[64:67], v[136:139], a[88:91]\n" \
"s_lshl_b32 s72, s62, 6\n" \
"s_add_u32 s72, s72, s37\n" \
"v_smfmac_f32_16x16x64_bf16 a[120:123], v[140:143], v[68:75], v50\n" \
"v_mfma_f32_16x16x32_bf16 a[28:31], v[128:131], v[72:75], a[28:31]\n" \
"s_lshl_b32 s63, s62, 12\n" \
"s_add_u32 s61, 0x3004800, s98\n" \
"v_mfma_f32_16x16x32_bf16 a[28:31], v[124:127], v[68:71], a[28:31]\n" \
"s_lshl_b32 s99, s97, 20\n" \
"s_lshl_b32 s98, s100, 16\n" \
"v_mfma_f32_16x16x32_bf16 a[60:63], v[116:119], v[68:71], a[60:63]\n" \
"s_add_u32 s89, s43, 0x3000\n" \
"s_add_u32 s8, 0x1000000, s99\n" \
"v_mfma_f32_16x16x32_bf16 a[60:63], v[120:123], v[72:75], a[60:63]\n" \
"buffer_store_dwordx4 a[68:71], v20, s[80:83], s89 offen offset:0 sc1\n" \
"v_mfma_f32_16x16x32_bf16 a[92:95], v[72:75], v[136:139], a[92:95]\n" \
"s_sub_u32 s19, 14, s18\n" \
"s_sub_u32 s10, 0x100000, s98\n" \
"v_mfma_f32_16x16x32_bf16 a[92:95], v[68:71], v[132:135], a[92:95]\n" \
"s_mov_b32 s11, 0x20000\n" \
"s_mov_b32 s58, 0x6000000\n" \
"v_smfmac_f32_16x16x64_bf16 a[124:127], v[140:143], v[76:83], v50\n" \
"v_mfma_f32_16x16x32_bf16 a[0:3], v[124:127], v[76:79], a[0:3]\n" \
"s_mov_b32 s59, 0x20000\n" \
"s_mov_b64 s[56:57], s[80:81]\n" \
"v_mfma_f32_16x16x32_bf16 a[0:3], v[128:131], v[80:83], a[0:3]\n" \
"s_add_u32 s8, s8, s80\n" \
"s_addc_u32 s9, s81, 0\n" \
"v_mfma_f32_16x16x32_bf16 a[4:7], v[120:123], v[80:83], a[4:7]\n" \
"v_mfma_f32_16x16x32_bf16 a[4:7], v[116:119], v[76:79], a[4:7]\n" \
"v_mfma_f32_16x16x32_bf16 a[32:35], v[76:79], v[132:135], a[32:35]\n" \
"v_mfma_f32_16x16x32_bf16 a[32:35], v[80:83], v[136:139], a[32:35]\n" \
"s_waitcnt lgkmcnt(0)\n" \
"s_nop 0\n" \
"v_mfma_f32_16x16x32_bf16 a[128:131], v[152:155], v[56:59], a[128:131]\n" \
"v_mfma_f32_16x16x32_bf16 a[128:131], v[148:151], v[52:55], a[128:131]\n" \
"v_mfma_f32_16x16x32_bf16 a[132:135], v[156:159], v[52:55], a[132:135]\n" \
"v_mfma_f32_16x16x32_bf16 a[132:135], v[160:163], v[56:59], a[132:135]\n" \
"v_mfma_f32_16x16x32_bf16 a[136:139], v[168:171], v[56:59], a[136:139]\n" \
"v_mfma_f32_16x16x32_bf16 a[136:139], v[164:167], v[52:55], a[136:139]\n" \
"v_mfma_f32_16x16x32_bf16 a[140:143], v[172:175], v[52:55], a[140:143]\n" \
"v_mfma_f32_16x16x32_bf16 a[140:143], v[176:179], v[56:59], a[140:143]\n" \
"v_mfma_f32_16x16x32_bf16 a[160:163], v[152:155], v[64:67], a[160:163]\n" \
"v_mfma_f32_16x16x32_bf16 a[160:163], v[148:151], v[60:63], a[160:163]\n" \
"v_mfma_f32_16x16x32_bf16 a[164:167], v[156:159], v[60:63], a[164:167]\n" \
"v_mfma_f32_16x16x32_bf16 a[164:167], v[160:163], v[64:67], a[164:167]\n" \
"v_mfma_f32_16x16x32_bf16 a[168:171], v[168:171], v[64:67], a[168:171]\n" \
"v_mfma_f32_16x16x32_bf16 a[168:171], v[164:167], v[60:63], a[168:171]\n" \
"v_mfma_f32_16x16x32_bf16 a[172:175], v[172:175], v[60:63], a[172:175]\n" \
"v_mfma_f32_16x16x32_bf16 a[172:175], v[176:179], v[64:67], a[172:175]\n" \
"v_mfma_f32_16x16x32_bf16 a[192:195], v[152:155], v[72:75], a[192:195]\n" \
"v_mfma_f32_16x16x32_bf16 a[192:195], v[148:151], v[68:71], a[192:195]\n" \
"s_waitcnt vmcnt(3)\n" \
"s_barrier\n" \
"v_mfma_f32_16x16x32_bf16 a[196:199], v[156:159], v[68:71], a[196:199]\n" \
"s_add_u32 m0, s40, 0x0\n" \
"s_add_u32 s85, s72, 0x0\n" \
"v_mfma_f32_16x16x32_bf16 a[196:199], v[160:163], v[72:75], a[196:199]\n" \
"buffer_load_dwordx4 v20, s[80:83], s85 offen sc1 lds\n" \
"v_mfma_f32_16x16x32_bf16 a[200:203], v[168:171], v[72:75], a[200:203]\n" \
"s_add_u32 m0, s40, 0x1000\n" \
"s_add_u32 s84, s72, 0x1000\n" \
"v_mfma_f32_16x16x32_bf16 a[200:203], v[164:167], v[68:71], a[200:203]\n" \
"ds_read_b64_tr_b16 v[140:141], v39 offset:32768\n" \
"ds_read_b64_tr_b16 v[142:143], v39 offset:33728\n" \
"v_mfma_f32_16x16x32_bf16 a[204:207], v[172:175], v[68:71], a[204:207]\n" \
"buffer_load_dwordx4 v20, s[80:83], s84 offen sc1 lds\n" \
"v_mfma_f32_16x16x32_bf16 a[204:207], v[176:179], v[72:75], a[204:207]\n" \
"ds_read_b64_tr_b16 v[128:129], v37 offset:36864\n" \
"ds_read_b64_tr_b16 v[130:131], v37 offset:37376\n" \
"v_mfma_f32_16x16x32_bf16 a[224:227], v[152:155], v[80:83], a[224:227]\n" \
"s_add_u32 m0, s40, 0x3000\n" \
"s_add_u32 s85, s62, 0x0\n" \
"v_mfma_f32_16x16x32_bf16 a[224:227], v[148:151], v[76:79], a[224:227]\n" \
"buffer_load_dwordx4 v21, s[76:79], s85 offen lds\n" \
"v_mfma_f32_16x16x32_bf16 a[228:231], v[156:159], v[76:79], a[228:231]\n" \
"ds_read_b64_tr_b16 v[124:125], v37 offset:32768\n" \
"ds_read_b64_tr_b16 v[126:127], v37 offset:33280\n" \
"v_mfma_f32_16x16x32_bf16 a[228:231], v[160:163], v[80:83], a[228:231]\n" \
"s_add_u32 m0, %[mA], 0x0\n" \
"s_add_u32 s84, s62, 0x0\n" \
"s_nop 0\n" \
"v_mfma_f32_16x16x32_bf16 a[232:235], v[168:171], v[80:83], a[232:235]\n" \
"buffer_load_dwordx4 v24, s[76:79], s84 offen lds\n" \
"v_mfma_f32_16x16x32_bf16 a[232:235], v[164:167], v[76:79], a[232:235]\n" \
"ds_read_b64_tr_b16 v[132:133], v38 offset:32768\n" \
"ds_read_b64_tr_b16 v[134:135], v38 offset:33280\n" \
"v_mfma_f32_16x16x32_bf16 a[236:239], v[172:175], v[76:79], a[236:239]\n" \
"ds_read_b64_tr_b16 v[136:137], v38 offset:36864\n" \
"ds_read_b64_tr_b16 v[138:139], v38 offset:37376\n" \
"v_mfma_f32_16x16x32_bf16 a[236:239], v[176:179], v[80:83], a[236:239]\n" \
"s_add_u32 m0, %[mA], 0x400\n" \
"s_add_u32 s85, s62, 0x2400\n" \
"v_mfma_f32_16x16x32_bf16 a[144:147], v[152:155], v[88:91], a[144:147]\n" \
"buffer_load_dwordx4 v25, s[76:79], s85 offen lds\n" \
"v_mfma_f32_16x16x32_bf16 a[144:147], v[148:151], v[84:87], a[144:147]\n" \
"ds_read_b128 v[52:55], v28 offset:32768\n" \
"ds_read_b128 v[56:59], v29 offset:32768\n" \
"v_mfma_f32_16x16x32_bf16 a[148:151], v[156:159], v[84:87], a[148:151]\n" \
"s_add_u32 m0, %[mB], 0x0\n" \
"s_add_u32 s84, s63, 0x0\n" \
"v_mfma_f32_16x16x32_bf16 a[148:151], v[160:163], v[88:91], a[148:151]\n" \
"buffer_load_dwordx4 v26, s[68:71], s84 offen lds\n" \
"v_mfma_f32_16x16x32_bf16 a[152:155], v[168:171], v[88:91], a[152:155]\n" \
"ds_read_b64_tr_b16 v[116:117], v36 offset:32768\n" \
"ds_read_b64_tr_b16 v[118:119], v36 offset:33280\n" \
"v_mfma_f32_16x16x32_bf16 a[152:155], v[164:167], v[84:87], a[152:155]\n" \
"ds_read_b64_tr_b16 v[120:121], v36 offset:36864\n" \
"ds_read_b64_tr_b16 v[122:123], v36 offset:37376\n" \
"v_mfma_f32_16x16x32_bf16 a[156:159], v[172:175], v[84:87], a[156:159]\n" \
"s_add_u32 m0, %[mA], 0x800\n" \
"s_add_u32 s85, s62, 0x4800\n" \
"v_mfma_f32_16x16x32_bf16 a[156:159], v[176:179], v[88:91], a[156:159]\n" \
"buffer_load_dwordx4 v24, s[76:79], s85 offen lds\n" \
"v_mfma_f32_16x16x32_bf16 a[176:179], v[152:155], v[96:99], a[176:179]\n" \
"ds_read_b128 v[84:87], v22 offset:32768\n" \
"ds_read_b128 v[88:91], v23 offset:32768\n" \
"v_mfma_f32_16x16x32_bf16 a[176:179], v[148:151], v[92:95], a[176:179]\n" \
"s_add_u32 m0, %[mA], 0xc00\n" \
"s_add_u32 s84, s62, 0x6c00\n" \
"v_mfma_f32_16x16x32_bf16 a[180:183], v[156:159], v[92:95], a[180:183]\n" \
"buffer_load_dwordx4 v25, s[76:79], s84 offen lds\n" \
"v_mfma_f32_16x16x32_bf16 a[180:183], v[160:163], v[96:99], a[180:183]\n" \
"ds_read_b128 v[60:63], v28 offset:34816\n" \
"ds_read_b128 v[64:67], v29 offset:34816\n" \
"v_mfma_f32_16x16x32_bf16 a[184:187], v[168:171], v[96:99], a[184:187]\n" \
"ds_read_b128 v[68:71], v28 offset:36864\n" \
"ds_read_b128 v[72:75], v29 offset:36864\n" \
"v_mfma_f32_16x16x32_bf16 a[184:187], v[164:167], v[92:95], a[184:187]\n" \
"s_add_u32 m0, %[mB], 0x400\n" \
"s_add_u32 s85, s63, 0x8000\n" \
"v_mfma_f32_16x16x32_bf16 a[188:191], v[172:175], v[92:95], a[188:191]\n" \
"buffer_load_dwordx4 v26, s[68:71], s85 offen lds\n" \
"v_mfma_f32_16x16x32_bf16 a[188:191], v[176:179], v[96:99], a[188:191]\n" \
"ds_read_b128 v[76:79], v28 offset:38912\n" \
"ds_read_b128 v[80:83], v29 offset:38912\n" \
"v_mfma_f32_16x16x32_bf16 a[208:211], v[152:155], v[104:107], a[208:211]\n" \
"s_add_u32 m0, %[mA], 0x1000\n" \
"s_add_u32 s84, s62, 0x9000\n" \
"v_mfma_f32_16x16x32_bf16 a[208:211], v[148:151], v[100:103], a[208:211]\n" \
"buffer_load_dwordx4 v24, s[76:79], s84 offen lds\n" \
"v_mfma_f32_16x16x32_bf16 a[212:215], v[156:159], v[100:103], a[212:215]\n" \
"ds_read_b128 v[92:95], v48 offset:34816\n" \
"ds_read_b128 v[96:99], v49 offset:34816\n" \
"v_mfma_f32_16x16x32_bf16 a[212:215], v[160:163], v[104:107], a[212:215]\n" \
"s_mov_b32 s72, 0x3eae1a93\n" \
"v_and_b32_e32 v26, 0x1ff, v20\n" \
"v_mfma_f32_16x16x32_bf16 a[216:219], v[168:171], v[104:107], a[216:219]\n" \
"s_add_u32 m0, %[mA], 0x1400\n" \
"s_add_u32 s85, s62, 0xb400\n" \
"v_mfma_f32_16x16x32_bf16 a[216:219], v[164:167], v[100:103], a[216:219]\n" \
"buffer_load_dwordx4 v25, s[76:79], s85 offen lds\n" \
"v_mfma_f32_16x16x32_bf16 a[220:223], v[172:175], v[100:103], a[220:223]\n" \
"ds_read_b128 v[100:103], v48 offset:36864\n" \
"v_add_u32_e64 v21, s48, v20\n" \
"v_mfma_f32_16x16x32_bf16 a[220:223], v[176:179], v[104:107], a[220:223]\n" \
"s_add_u32 m0, %[mB], 0x800\n" \
"s_add_u32 s84, s63, 0x10000\n" \
"v_mfma_f32_16x16x32_bf16 a[240:243], v[152:155], v[112:115], a[240:243]\n" \
"buffer_load_dwordx4 v27, s[68:71], s84 offen lds\n" \
"v_mfma_f32_16x16x32_bf16 a[240:243], v[148:151], v[108:111], a[240:243]\n" \
"ds_read_b64_tr_b16 v[152:153], v32 offset:40960\n" \
"ds_read_b64_tr_b16 v[154:155], v32 offset:41984\n" \
"v_mfma_f32_16x16x32_bf16 a[244:247], v[156:159], v[108:111], a[244:247]\n" \
"ds_read_b64_tr_b16 v[148:149], v32 offset:32768\n" \
"ds_read_b64_tr_b16 v[150:151], v32 offset:33792\n" \
"v_mfma_f32_16x16x32_bf16 a[244:247], v[160:163], v[112:115], a[244:247]\n" \
"s_add_u32 m0, %[mA], 0x1800\n" \
"s_add_u32 s85, s62, 0xd800\n" \
"L_pf8:\n" \
"s_cbranch_execz L_pf12\n" \
"s_nop 0\n" \
"v_mfma_f32_16x16x32_bf16 a[248:251], v[168:171], v[112:115], a[248:251]\n" \
"buffer_load_dwordx4 v24, s[76:79], s85 offen lds\n" \
"v_mfma_f32_16x16x32_bf16 a[248:251], v[164:167], v[108:111], a[248:251]\n" \
"ds_read_b64_tr_b16 v[156:157], v33 offset:32768\n" \
"ds_read_b64_tr_b16 v[158:159], v33 offset:33792\n" \
"v_mfma_f32_16x16x32_bf16 a[252:255], v[172:175], v[108:111], a[252:255]\n" \
"ds_read_b64_tr_b16 v[160:161], v33 offset:40960\n" \
"ds_read_b64_tr_b16 v[162:163], v33 offset:41984\n" \
"v_mfma_f32_16x16x32_bf16 a[252:255], v[176:179], v[112:115], a[252:255]\n" \
"s_add_u32 m0, %[mA], 0x1c00\n" \
"s_waitcnt lgkmcnt(11)\n" \
"v_smfmac_f32_16x16x64_bf16 a[16:19], v[140:143], v[84:91], v50\n" \
"v_mfma_f32_16x16x32_bf16 a[48:51], v[128:131], v[88:91], a[48:51]\n" \
"s_add_u32 s85, s62, 0xfc00\n" \
"ds_read_b64_tr_b16 v[168:169], v34 offset:40960\n" \
"v_mfma_f32_16x16x32_bf16 a[48:51], v[124:127], v[84:87], a[48:51]\n" \
"buffer_load_dwordx4 v25, s[76:79], s85 offen lds\n" \
"s_mov_b32 s76, s54\n" \
"v_mfma_f32_16x16x32_bf16 a[80:83], v[84:87], v[132:135], a[80:83]\n" \
"s_add_u32 m0, %[mB], 0xc00\n" \
"s_add_u32 s84, s63, 0x18000\n" \
"v_mfma_f32_16x16x32_bf16 a[80:83], v[88:91], v[136:139], a[80:83]\n" \
"ds_read_b64_tr_b16 v[170:171], v34 offset:41984\n" \
"ds_read_b64_tr_b16 v[164:165], v34 offset:32768\n" \
"v_smfmac_f32_16x16x64_bf16 a[112:115], v[140:143], v[52:59], v50\n" \
"v_mfma_f32_16x16x32_bf16 a[20:23], v[128:131], v[56:59], a[20:23]\n" \
"buffer_load_dwordx4 v27, s[68:71], s84 offen lds\n" \
"v_mfma_f32_16x16x32_bf16 a[20:23], v[124:127], v[52:55], a[20:23]\n" \
"s_add_u32 s88, s43, 0x0\n" \
"s_nop 0\n" \
"ds_read_b64_tr_b16 v[166:167], v34 offset:33792\n" \
"v_mfma_f32_16x16x32_bf16 a[52:55], v[116:119], v[52:55], a[52:55]\n" \
"ds_read_b64_tr_b16 v[172:173], v35 offset:32768\n" \
"ds_read_b64_tr_b16 v[174:175], v35 offset:33792\n" \
"v_mfma_f32_16x16x32_bf16 a[52:55], v[120:123], v[56:59], a[52:55]\n" \
"buffer_store_dwordx4 a[96:99], v20, s[80:83], s88 offen offset:1024 sc1\n" \
"v_mfma_f32_16x16x32_bf16 a[84:87], v[56:59], v[136:139], a[84:87]\n" \
"ds_read_b64_tr_b16 v[176:177], v35 offset:40960\n" \
"ds_read_b64_tr_b16 v[178:179], v35 offset:41984\n" \
"v_mfma_f32_16x16x32_bf16 a[84:87], v[52:55], v[132:135], a[84:87]\n" \
"ds_read_b128 v[84:87], v48 offset:32768\n" \
"ds_read_b128 v[88:91], v49 offset:32768\n" \
"v_smfmac_f32_16x16x64_bf16 a[116:119], v[140:143], v[60:67], v50\n" \
"v_mfma_f32_16x16x32_bf16 a[24:27], v[124:127], v[60:63], a[24:27]\n" \
"ds_read_b128 v[104:107], v49 offset:36864\n" \
"ds_read_b128 v[108:111], v48 offset:38912\n" \
"v_mfma_f32_16x16x32_bf16 a[24:27], v[128:131], v[64:67], a[24:27]\n" \
"s_add_u32 s89, s43, 0x1000\n" \
"ds_read_b128 v[112:115], v49 offset:38912\n" \
"v_mfma_f32_16x16x32_bf16 a[56:59], v[120:123], v[64:67], a[56:59]\n" \
"buffer_store_dwordx4 a[100:103], v20, s[80:83], s89 offen offset:1024 sc1\n" \
"s_add_u32 s89, s43, 0x3000\n" \
"v_mfma_f32_16x16x32_bf16 a[56:59], v[116:119], v[60:63], a[56:59]\n" \
"v_accvgpr_read_b32 v248, a16\n" \
"v_accvgpr_read_b32 v249, a17\n" \
"v_mfma_f32_16x16x32_bf16 a[88:91], v[60:63], v[132:135], a[88:91]\n" \
"v_accvgpr_read_b32 v250, a18\n" \
"v_accvgpr_read_b32 v251, a19\n" \
"v_mfma_f32_16x16x32_bf16 a[88:91], v[64:67], v[136:139], a[88:91]\n" \
"v_accvgpr_read_b32 v180, a48\n" \
"v_accvgpr_read_b32 v181, a49\n" \
"v_smfmac_f32_16x16x64_bf16 a[120:123], v[140:143], v[68:75], v50\n" \
"v_mfma_f32_16x16x32_bf16 a[28:31], v[128:131], v[72:75], a[28:31]\n" \
"v_accvgpr_read_b32 v182, a50\n" \
"v_accvgpr_read_b32 v183, a51\n" \
"v_mfma_f32_16x16x32_bf16 a[28:31], v[124:127], v[68:71], a[28:31]\n" \
"v_accvgpr_read_b32 v252, a80\n" \
"v_accvgpr_read_b32 v253, a81\n" \
"v_mfma_f32_16x16x32_bf16 a[60:63], v[116:119], v[68:71], a[60:63]\n" \
"s_add_u32 s88, s43, 0x2000\n" \
"v_accvgpr_read_b32 v254, a82\n" \
"v_mfma_f32_16x16x32_bf16 a[60:63], v[120:123], v[72:75], a[60:63]\n" \
"buffer_store_dwordx4 a[8:11], v20, s[80:83], s88 offen offset:1024 sc1\n" \
"s_add_u32 s88, s45, 0x0\n" \
"s_nop 0\n" \
"v_mfma_f32_16x16x32_bf16 a[92:95], v[72:75], v[136:139], a[92:95]\n" \
"v_accvgpr_read_b32 v255, a83\n" \
"v_accvgpr_read_b32 v196, a112\n" \
"v_mfma_f32_16x16x32_bf16 a[92:95], v[68:71], v[132:135], a[92:95]\n" \
"v_accvgpr_read_b32 v197, a113\n" \
"v_accvgpr_read_b32 v198, a114\n" \
"v_smfmac_f32_16x16x64_bf16 a[124:127], v[140:143], v[76:83], v50\n" \
"v_mfma_f32_16x16x32_bf16 a[0:3], v[124:127], v[76:79], a[0:3]\n" \
"v_accvgpr_read_b32 v199, a115\n" \
"v_accvgpr_read_b32 v188, a20\n" \
"v_mfma_f32_16x16x32_bf16 a[0:3], v[128:131], v[80:83], a[0:3]\n" \
"v_accvgpr_read_b32 v189, a21\n" \
"v_accvgpr_read_b32 v190, a22\n" \
"v_mfma_f32_16x16x32_bf16 a[4:7], v[120:123], v[80:83], a[4:7]\n" \
"v_accvgpr_read_b32 v191, a23\n" \
"v_accvgpr_read_b32 v184, a52\n" \
"v_mfma_f32_16x16x32_bf16 a[4:7], v[116:119], v[76:79], a[4:7]\n" \
"v_lshlrev_b32_e32 v45, 10, v20\n" \
"v_add_u32_e32 v45, s41, v45\n" \
"v_mfma_f32_16x16x32_bf16 a[32:35], v[76:79], v[132:135], a[32:35]\n" \
"s_mov_b32 m0, %[mS]\n" \
"v_accvgpr_read_b32 v185, a53\n" \
"v_mfma_f32_16x16x32_bf16 a[32:35], v[80:83], v[136:139], a[32:35]\n" \
"v_accvgpr_read_b32 v186, a54\n" \
"s_waitcnt lgkmcnt(0)\n" \
"v_mfma_f32_16x16x32_bf16 a[128:131], v[152:155], v[56:59], a[128:131]\n" \
"v_accvgpr_read_b32 v187, a55\n" \
"v_accvgpr_read_b32 v192, a84\n" \
"v_mfma_f32_16x16x32_bf16 a[128:131], v[148:151], v[52:55], a[128:131]\n" \
"v_accvgpr_read_b32 v193, a85\n" \
"v_accvgpr_read_b32 v194, a86\n" \
"v_mfma_f32_16x16x32_bf16 a[132:135], v[156:159], v[52:55], a[132:135]\n" \
"v_accvgpr_read_b32 v195, a87\n" \
"v_accvgpr_read_b32 v212, a116\n" \
"s_getpc_b64 s[86:87]\n" \
"L_ct15_%=:\n" \
"s_add_u32 s86, s86, L_end_%=-L_ct15_%=\n" \
"s_addc_u32 s87, s87, 0\n" \
"v_mfma_f32_16x16x32_bf16 a[132:135], v[160:163], v[56:59], a[132:135]\n" \
"v_accvgpr_read_b32 v213, a117\n" \
"v_accvgpr_read_b32 v214, a118\n" \
"v_mfma_f32_16x16x32_bf16 a[136:139], v[168:171], v[56:59], a[136:139]\n" \
"v_accvgpr_read_b32 v215, a119\n" \
"v_accvgpr_read_b32 v204, a24\n" \
"v_mfma_f32_16x16x32_bf16 a[136:139], v[164:167], v[52:55], a[136:139]\n" \
"v_accvgpr_read_b32 v205, a25\n" \
"v_accvgpr_read_b32 v206, a26\n" \
"v_mfma_f32_16x16x32_bf16 a[140:143], v[172:175], v[52:55], a[140:143]\n" \
"v_accvgpr_read_b32 v207, a27\n" \
"v_accvgpr_read_b32 v200, a56\n" \
"v_mfma_f32_16x16x32_bf16 a[140:143], v[176:179], v[56:59], a[140:143]\n" \
"v_accvgpr_read_b32 v201, a57\n" \
"v_accvgpr_read_b32 v202, a58\n" \
"v_mfma_f32_16x16x32_bf16 a[160:163], v[152:155], v[64:67], a[160:163]\n" \
"v_accvgpr_read_b32 v203, a59\n" \
"v_accvgpr_read_b32 v208, a88\n" \
"v_mfma_f32_16x16x32_bf16 a[160:163], v[148:151], v[60:63], a[160:163]\n" \
"v_accvgpr_read_b32 v209, a89\n" \
"v_accvgpr_read_b32 v210, a90\n" \
"v_mfma_f32_16x16x32_bf16 a[164:167], v[156:159], v[60:63], a[164:167]\n" \
"v_accvgpr_read_b32 v211, a91\n" \
"v_accvgpr_read_b32 v228, a120\n" \
"v_mfma_f32_16x16x32_bf16 a[164:167], v[160:163], v[64:67], a[164:167]\n" \
"v_accvgpr_read_b32 v229, a121\n" \
"v_accvgpr_read_b32 v230, a122\n" \
"v_mfma_f32_16x16x32_bf16 a[168:171], v[168:171], v[64:67], a[168:171]\n" \
"v_accvgpr_read_b32 v231, a123\n" \
"v_accvgpr_read_b32 v220, a28\n" \
"v_mfma_f32_16x16x32_bf16 a[168:171], v[164:167], v[60:63], a[168:171]\n" \
"v_accvgpr_read_b32 v221, a29\n" \
"v_accvgpr_read_b32 v222, a30\n" \
"v_mfma_f32_16x16x32_bf16 a[172:175], v[172:175], v[60:63], a[172:175]\n" \
"v_accvgpr_read_b32 v223, a31\n" \
"v_accvgpr_read_b32 v216, a60\n" \
"v_mfma_f32_16x16x32_bf16 a[172:175], v[176:179], v[64:67], a[172:175]\n" \
"v_accvgpr_read_b32 v217, a61\n" \
"v_accvgpr_read_b32 v218, a62\n" \
"v_mfma_f32_16x16x32_bf16 a[192:195], v[152:155], v[72:75], a[192:195]\n" \
"v_accvgpr_read_b32 v219, a63\n" \
"v_accvgpr_read_b32 v224, a92\n" \
"v_mfma_f32_16x16x32_bf16 a[192:195], v[148:151], v[68:71], a[192:195]\n" \
"v_accvgpr_read_b32 v225, a93\n" \
"v_accvgpr_read_b32 v226, a94\n" \
"s_waitcnt vmcnt(3)\n" \
"s_barrier\n" \
"s_mov_b64 exec, 3\n" \
"global_load_lds_dword v45, s[86:87]\n" \
"s_mov_b64 exec, -1\n" \
"s_sub_u32 m0, %[mB], 0x10000\n" \
"v_mfma_f32_16x16x32_bf16 a[196:199], v[156:159], v[68:71], a[196:199]\n" \
"ds_read_b64_tr_b16 v[140:141], v39 offset:0\n" \
"ds_read_b64_tr_b16 v[142:143], v39 offset:960\n" \
"v_mfma_f32_16x16x32_bf16 a[196:199], v[160:163], v[72:75], a[196:199]\n" \
"ds_read_b64_tr_b16 v[128:129], v37 offset:4096\n" \
"ds_read_b64_tr_b16 v[130:131], v37 offset:4608\n" \
"v_mfma_f32_16x16x32_bf16 a[200:203], v[168:171], v[72:75], a[200:203]\n" \
"ds_read_b64_tr_b16 v[124:125], v37 offset:0\n" \
"ds_read_b64_tr_b16 v[126:127], v37 offset:512\n" \
"v_mfma_f32_16x16x32_bf16 a[200:203], v[164:167], v[68:71], a[200:203]\n" \
"ds_read_b64_tr_b16 v[132:133], v38 offset:0\n" \
"ds_read_b64_tr_b16 v[134:135], v38 offset:512\n" \
"v_mfma_f32_16x16x32_bf16 a[204:207], v[172:175], v[68:71], a[204:207]\n" \
"ds_read_b64_tr_b16 v[136:137], v38 offset:4096\n" \
"ds_read_b64_tr_b16 v[138:139], v38 offset:4608\n" \
"v_mfma_f32_16x16x32_bf16 a[204:207], v[176:179], v[72:75], a[204:207]\n" \
"ds_read_b128 v[52:55], v28 offset:0\n" \
"ds_read_b128 v[56:59], v29 offset:0\n" \
"v_mfma_f32_16x16x32_bf16 a[224:227], v[152:155], v[80:83], a[224:227]\n" \
"ds_read_b64_tr_b16 v[116:117], v36 offset:0\n" \
"ds_read_b64_tr_b16 v[118:119], v36 offset:512\n" \
"v_mfma_f32_16x16x32_bf16 a[224:227], v[148:151], v[76:79], a[224:227]\n" \
"ds_read_b64_tr_b16 v[120:121], v36 offset:4096\n" \
"ds_read_b64_tr_b16 v[122:123], v36 offset:4608\n" \
"v_mfma_f32_16x16x32_bf16 a[228:231], v[156:159], v[76:79], a[228:231]\n" \
"ds_read_b128 v[60:63], v28 offset:2048\n" \
"ds_read_b128 v[64:67], v29 offset:2048\n" \
"v_mfma_f32_16x16x32_bf16 a[228:231], v[160:163], v[80:83], a[228:231]\n" \
"v_accvgpr_read_b32 v227, a95\n" \
"v_accvgpr_read_b32 v244, a124\n" \
"v_mfma_f32_16x16x32_bf16 a[232:235], v[168:171], v[80:83], a[232:235]\n" \
"ds_read_b128 v[68:71], v28 offset:4096\n" \
"ds_read_b128 v[72:75], v29 offset:4096\n" \
"v_mfma_f32_16x16x32_bf16 a[232:235], v[164:167], v[76:79], a[232:235]\n" \
"v_accvgpr_read_b32 v245, a125\n" \
"v_accvgpr_read_b32 v246, a126\n" \
"v_mfma_f32_16x16x32_bf16 a[236:239], v[172:175], v[76:79], a[236:239]\n" \
"ds_read_b128 v[76:79], v28 offset:6144\n" \
"v_accvgpr_read_b32 v247, a127\n" \
"v_mfma_f32_16x16x32_bf16 a[236:239], v[176:179], v[80:83], a[236:239]\n" \
"ds_read_b128 v[80:83], v29 offset:6144\n" \
"v_accvgpr_read_b32 v236, a0\n" \
"v_mfma_f32_16x16x32_bf16 a[144:147], v[152:155], v[88:91], a[144:147]\n" \
"v_accvgpr_read_b32 v237, a1\n" \
"v_accvgpr_read_b32 v238, a2\n" \
"v_mfma_f32_16x16x32_bf16 a[144:147], v[148:151], v[84:87], a[144:147]\n" \
"v_accvgpr_read_b32 v239, a3\n" \
"v_accvgpr_read_b32 v232, a4\n" \
"v_mfma_f32_16x16x32_bf16 a[148:151], v[156:159], v[84:87], a[148:151]\n" \
"v_accvgpr_read_b32 v233, a5\n" \
"v_accvgpr_read_b32 v234, a6\n" \
"v_mfma_f32_16x16x32_bf16 a[148:151], v[160:163], v[88:91], a[148:151]\n" \
"v_accvgpr_read_b32 v235, a7\n" \
"v_accvgpr_read_b32 v240, a32\n" \
"v_mfma_f32_16x16x32_bf16 a[152:155], v[168:171], v[88:91], a[152:155]\n" \
"v_accvgpr_read_b32 v241, a33\n" \
"v_accvgpr_read_b32 v242, a34\n" \
"v_mfma_f32_16x16x32_bf16 a[152:155], v[164:167], v[84:87], a[152:155]\n" \
"v_accvgpr_read_b32 v243, a35\n" \
"s_mov_b32 s78, -1\n" \
"v_mfma_f32_16x16x32_bf16 a[156:159], v[172:175], v[84:87], a[156:159]\n" \
"ds_read_b128 v[84:87], v22 offset:0\n" \
"s_mov_b32 s79, 0\n" \
"v_mfma_f32_16x16x32_bf16 a[156:159], v[176:179], v[88:91], a[156:159]\n" \
"ds_read_b128 v[88:91], v23 offset:0\n" \
"s_mov_b32 s68, 0xffc0fffc\n" \
"v_mfma_f32_16x16x32_bf16 a[176:179], v[152:155], v[96:99], a[176:179]\n" \
"s_mov_b32 s69, 0xc000fc00\n" \
"v_lshrrev_b32_e32 v24, 9, v20\n" \
"v_mfma_f32_16x16x32_bf16 a[176:179], v[148:151], v[92:95], a[176:179]\n" \
"v_lshrrev_b32_e32 v25, 8, v20\n" \
"v_mul_u32_u24_e32 v24, 0x24800, v24\n" \
"v_mfma_f32_16x16x32_bf16 a[180:183], v[156:159], v[92:95], a[180:183]\n" \
"v_mul_u32_u24_e32 v25, 0x24700, v25\n" \
"v_add_u32_e64 v24, v24, v26\n" \
"v_mfma_f32_16x16x32_bf16 a[180:183], v[160:163], v[96:99], a[180:183]\n" \
"v_add_u32_e32 v25, v25, v20\n" \
"s_add_u32 s85, s50, 0x0\n" \
"v_mfma_f32_16x16x32_bf16 a[184:187], v[168:171], v[96:99], a[184:187]\n" \
"s_add_u32 s87, s50, 0x49000\n" \
"s_mov_b32 s70, 0x3e0293ee\n" \
"v_mfma_f32_16x16x32_bf16 a[184:187], v[164:167], v[92:95], a[184:187]\n" \
"s_mov_b32 s71, 0x3e0293ee\n" \
"v_add_u32_e64 v27, s47, v20\n" \
"v_mfma_f32_16x16x32_bf16 a[188:191], v[172:175], v[92:95], a[188:191]\n" \
"ds_read_b128 v[92:95], v48 offset:2048\n" \
"v_lshrrev_b32_e32 v26, 2, v20\n" \
"v_mfma_f32_16x16x32_bf16 a[188:191], v[176:179], v[96:99], a[188:191]\n" \
"ds_read_b128 v[96:99], v49 offset:2048\n" \
"v_add_u32_e32 v26, s46, v26\n" \
"v_mfma_f32_16x16x32_bf16 a[208:211], v[152:155], v[104:107], a[208:211]\n" \
"v_mfma_f32_16x16x32_bf16 a[208:211], v[148:151], v[100:103], a[208:211]\n" \
"v_mfma_f32_16x16x32_bf16 a[212:215], v[156:159], v[100:103], a[212:215]\n" \
"v_mfma_f32_16x16x32_bf16 a[212:215], v[160:163], v[104:107], a[212:215]\n" \
"v_mfma_f32_16x16x32_bf16 a[216:219], v[168:171], v[104:107], a[216:219]\n" \
"v_mfma_f32_16x16x32_bf16 a[216:219], v[164:167], v[100:103], a[216:219]\n" \
"v_mfma_f32_16x16x32_bf16 a[220:223], v[172:175], v[100:103], a[220:223]\n" \
"ds_read_b128 v[100:103], v48 offset:4096\n" \
"v_mfma_f32_16x16x32_bf16 a[220:223], v[176:179], v[104:107], a[220:223]\n" \
"ds_read_b128 v[104:107], v49 offset:4096\n" \
"v_mfma_f32_16x16x32_bf16 a[240:243], v[152:155], v[112:115], a[240:243]\n" \
"ds_read_b64_tr_b16 v[152:153], v32 offset:8192\n" \
"ds_read_b64_tr_b16 v[154:155], v32 offset:9216\n" \
"v_mfma_f32_16x16x32_bf16 a[240:243], v[148:151], v[108:111], a[240:243]\n" \
"ds_read_b64_tr_b16 v[148:149], v32 offset:0\n" \
"ds_read_b64_tr_b16 v[150:151], v32 offset:1024\n" \
"v_mfma_f32_16x16x32_bf16 a[244:247], v[156:159], v[108:111], a[244:247]\n" \
"ds_read_b64_tr_b16 v[156:157], v33 offset:0\n" \
"ds_read_b64_tr_b16 v[158:159], v33 offset:1024\n" \
"v_mfma_f32_16x16x32_bf16 a[244:247], v[160:163], v[112:115], a[244:247]\n" \
"ds_read_b64_tr_b16 v[160:161], v33 offset:8192\n" \
"ds_read_b64_tr_b16 v[162:163], v33 offset:9216\n" \
"v_mfma_f32_16x16x32_bf16 a[248:251], v[168:171], v[112:115], a[248:251]\n" \
"ds_read_b64_tr_b16 v[168:169], v34 offset:8192\n" \
"ds_read_b64_tr_b16 v[170:171], v34 offset:9216\n" \
"v_mfma_f32_16x16x32_bf16 a[248:251], v[164:167], v[108:111], a[248:251]\n" \
"ds_read_b64_tr_b16 v[164:165], v34 offset:0\n" \
"ds_read_b64_tr_b16 v[166:167], v34 offset:1024\n" \
"v_mfma_f32_16x16x32_bf16 a[252:255], v[172:175], v[108:111], a[252:255]\n" \
"ds_read_b64_tr_b16 v[172:173], v35 offset:0\n" \
"ds_read_b64_tr_b16 v[174:175], v35 offset:1024\n" \
"v_mfma_f32_16x16x32_bf16 a[252:255], v[176:179], v[112:115], a[252:255]\n" \
"ds_read_b64_tr_b16 v[176:177], v35 offset:8192\n" \
"s_waitcnt lgkmcnt(15)\n" \
"s_nop 0\n" \
"v_smfmac_f32_16x16x64_bf16 v[248:251], v[140:143], v[84:91], v50\n" \
"v_mfma_f32_16x16x32_bf16 v[180:183], v[128:131], v[88:91], v[180:183]\n" \
"ds_read_b64_tr_b16 v[178:179], v35 offset:9216\n" \
"v_bfe_u32 v44, v20, 2, 5\n" \
"v_mfma_f32_16x16x32_bf16 v[180:183], v[124:127], v[84:87], v[180:183]\n" \
"buffer_load_dword v44, v44, s[80:83], s39 offen offset:0 sc1\n" \
"v_mfma_f32_16x16x32_bf16 v[252:255], v[84:87], v[132:135], v[252:255]\n" \
"ds_read_b128 v[84:87], v48 offset:0\n" \
"ds_read_b128 v[108:111], v48 offset:6144\n" \
"v_mfma_f32_16x16x32_bf16 v[252:255], v[88:91], v[136:139], v[252:255]\n" \
"ds_read_b128 v[88:91], v49 offset:0\n" \
"ds_read_b128 v[112:115], v49 offset:6144\n" \
"v_smfmac_f32_16x16x64_bf16 v[196:199], v[140:143], v[52:59], v50\n" \
"v_mfma_f32_16x16x32_bf16 v[188:191], v[128:131], v[56:59], v[188:191]\n" \
"v_cvt_pk_bf16_f32 v180, v180, v181\n" \
"v_cvt_pk_bf16_f32 v181, v182, v183\n" \
"v_mfma_f32_16x16x32_bf16 v[188:191], v[124:127], v[52:55], v[188:191]\n" \
"v_add_f32_e64 v249, v249, v251\n" \
"v_cvt_pk_bf16_f32 v182, v249, 0\n" \
"v_mfma_f32_16x16x32_bf16 v[184:187], v[116:119], v[52:55], v[184:187]\n" \
"v_mov_b32_e32 v183, 0x3f80\n" \
"v_add_f32_e64 v196, v196, v198\n" \
"v_mfma_f32_16x16x32_bf16 v[184:187], v[120:123], v[56:59], v[184:187]\n" \
"v_mfma_f32_16x16x16_bf16 v[248:251], v[182:183], v[46:47], 0\n" \
"v_add_f32_e64 v197, v197, v199\n" \
"v_cvt_pk_bf16_f32 v188, v188, v189\n" \
"v_mfma_f32_16x16x32_bf16 v[192:195], v[56:59], v[136:139], v[192:195]\n" \
"v_cvt_pk_bf16_f32 v189, v190, v191\n" \
"v_cvt_pk_bf16_f32 v190, v197, 0\n" \
"v_mfma_f32_16x16x32_bf16 v[192:195], v[52:55], v[132:135], v[192:195]\n" \
"v_mov_b32_e32 v191, 0x3f80\n" \
"v_cvt_pk_bf16_f32 v184, v184, v185\n" \
"v_smfmac_f32_16x16x64_bf16 v[212:215], v[140:143], v[60:67], v50\n" \
"v_mfma_f32_16x16x32_bf16 v[204:207], v[124:127], v[60:63], v[204:207]\n" \
"v_cvt_pk_bf16_f32 v185, v186, v187\n" \
"v_cndmask_b32_e64 v186, 0, v196, s[78:79]\n" \
"v_mfma_f32_16x16x32_bf16 v[204:207], v[128:131], v[64:67], v[204:207]\n" \
"v_mov_b32_e64 v187, 0\n" \
"v_cvt_pk_bf16_f32 v186, v186, 0\n" \
"v_mfma_f32_16x16x32_bf16 v[200:203], v[120:123], v[64:67], v[200:203]\n" \
"v_mfma_f32_16x16x32_bf16 v[200:203], v[116:119], v[60:63], v[200:203]\n" \
"v_add_f32_e32 v212, v212, v214\n" \
"v_add_f32_e32 v213, v213, v215\n" \
"v_mfma_f32_16x16x32_bf16 v[208:211], v[60:63], v[132:135], v[208:211]\n" \
"v_mfma_f32_16x16x32_bf16 v[208:211], v[64:67], v[136:139], v[208:211]\n" \
"v_cvt_pk_bf16_f32 v204, v204, v205\n" \
"v_cvt_pk_bf16_f32 v205, v206, v207\n" \
"v_smfmac_f32_16x16x64_bf16 v[228:231], v[140:143], v[68:75], v50\n" \
"v_mfma_f32_16x16x32_bf16 v[220:223], v[128:131], v[72:75], v[220:223]\n" \
"v_cvt_pk_bf16_f32 v200, v200, v201\n" \
"v_cvt_pk_bf16_f32 v201, v202, v203\n" \
"v_mfma_f32_16x16x32_bf16 v[220:223], v[124:127], v[68:71], v[220:223]\n" \
"v_cndmask_b32_e64 v202, 0, v212, s[78:79]\n" \
"v_cvt_pk_bf16_f32 v206, v213, 0\n" \
"v_mfma_f32_16x16x32_bf16 v[216:219], v[116:119], v[68:71], v[216:219]\n" \
"v_mov_b32_e32 v207, 0x3f80\n" \
"v_add_f32_e64 v228, v228, v230\n" \
"v_mfma_f32_16x16x32_bf16 v[216:219], v[120:123], v[72:75], v[216:219]\n" \
"v_add_f32_e32 v229, v229, v231\n" \
"v_mov_b32_e32 v203, 0\n" \
"v_mfma_f32_16x16x32_bf16 v[224:227], v[72:75], v[136:139], v[224:227]\n" \
"v_cvt_pk_bf16_f32 v220, v220, v221\n" \
"v_cvt_pk_bf16_f32 v221, v222, v223\n" \
"v_mfma_f32_16x16x32_bf16 v[224:227], v[68:71], v[132:135], v[224:227]\n" \
"v_cvt_pk_bf16_f32 v202, v202, 0\n" \
"v_cvt_pk_bf16_f32 v222, v229, 0\n" \
"v_smfmac_f32_16x16x64_bf16 v[244:247], v[140:143], v[76:83], v50\n" \
"v_mfma_f32_16x16x32_bf16 v[236:239], v[124:127], v[76:79], v[236:239]\n" \
"v_cvt_pk_bf16_f32 v216, v216, v217\n" \
"v_cvt_pk_bf16_f32 v217, v218, v219\n" \
"v_mfma_f32_16x16x32_bf16 v[236:239], v[128:131], v[80:83], v[236:239]\n" \
"v_cndmask_b32_e64 v218, 0, v228, s[78:79]\n" \
"v_cvt_pk_bf16_f32 v126, v192, v193\n" \
"v_mfma_f32_16x16x32_bf16 v[232:235], v[120:123], v[80:83], v[232:235]\n" \
"v_cvt_pk_bf16_f32 v127, v194, v195\n" \
"v_add_f32_e64 v244, v244, v246\n" \
"v_mfma_f32_16x16x32_bf16 v[232:235], v[116:119], v[76:79], v[232:235]\n" \
"v_add_f32_e64 v245, v245, v247\n" \
"v_cvt_pk_bf16_f32 v128, v208, v209\n" \
"v_mfma_f32_16x16x32_bf16 v[240:243], v[76:79], v[132:135], v[240:243]\n" \
"v_cvt_pk_bf16_f32 v236, v236, v237\n" \
"v_cvt_pk_bf16_f32 v237, v238, v239\n" \
"v_mfma_f32_16x16x32_bf16 v[240:243], v[80:83], v[136:139], v[240:243]\n" \
"v_cvt_pk_bf16_f32 v129, v210, v211\n" \
"v_mov_b32_e32 v223, 0x3f80\n" \
"s_waitcnt lgkmcnt(0)\n" \
"s_barrier\n" \
"v_mfma_f32_16x16x32_bf16 a[128:131], v[152:155], v[56:59], a[128:131]\n" \
"v_cvt_pk_bf16_f32 v232, v232, v233\n" \
"v_cvt_pk_bf16_f32 v233, v234, v235\n" \
"v_mfma_f32_16x16x32_bf16 a[128:131], v[148:151], v[52:55], a[128:131]\n" \
"v_cndmask_b32_e64 v234, 0, v244, s[78:79]\n" \
"v_mov_b32_e64 v219, 0\n" \
"v_mfma_f32_16x16x32_bf16 a[132:135], v[156:159], v[52:55], a[132:135]\n" \
"v_cvt_pk_bf16_f32 v218, v218, 0\n" \
"v_cvt_pk_bf16_f32 v130, v224, v225\n" \
"v_mfma_f32_16x16x32_bf16 a[132:135], v[160:163], v[56:59], a[132:135]\n" \
"v_cvt_pk_bf16_f32 v131, v226, v227\n" \
"v_cvt_pk_bf16_f32 v238, v245, 0\n" \
"v_mfma_f32_16x16x32_bf16 a[136:139], v[168:171], v[56:59], a[136:139]\n" \
"v_mov_b32_e32 v239, 0x3f80\n" \
"v_mov_b32_e64 v235, 0\n" \
"v_mfma_f32_16x16x32_bf16 a[136:139], v[164:167], v[52:55], a[136:139]\n" \
"v_cvt_pk_bf16_f32 v234, v234, 0\n" \
"v_cvt_pk_bf16_f32 v132, v240, v241\n" \
"v_mfma_f32_16x16x32_bf16 a[140:143], v[172:175], v[52:55], a[140:143]\n" \
"v_cvt_pk_bf16_f32 v133, v242, v243\n" \
"v_cvt_pk_bf16_f32 v134, v248, v249\n" \
"v_mfma_f32_16x16x32_bf16 a[140:143], v[176:179], v[56:59], a[140:143]\n" \
"v_mfma_f32_16x16x32_bf16 v[116:119], v[180:183], v[184:187], 0\n" \
"v_mfma_f32_16x16x32_bf16 v[192:195], v[188:191], v[184:187], 0\n" \
"v_mfma_f32_16x16x32_bf16 v[208:211], v[204:207], v[200:203], 0\n" \
"v_mfma_f32_16x16x32_bf16 v[120:123], v[188:191], v[200:203], 0\n" \
"s_waitcnt vmcnt(0)\n" \
"v_cmp_ne_u32_e32 vcc, %[tag], v44\n" \
"v_mfma_f32_16x16x32_bf16 a[160:163], v[152:155], v[64:67], a[160:163]\n" \
"v_mfma_f32_16x16x32_bf16 v[224:227], v[220:223], v[216:219], 0\n" \
"v_mfma_f32_16x16x32_bf16 v[36:39], v[204:207], v[216:219], 0\n" \
"v_mfma_f32_16x16x32_bf16 v[240:243], v[236:239], v[232:235], 0\n" \
"v_mfma_f32_16x16x32_bf16 v[48:51], v[220:223], v[232:235], 0\n" \
"s_cbranch_vccnz L_pa_%=\n" \
"L_ra_%=:\n" \
"v_cndmask_b32_e64 v116, v116, v117, s[64:65]\n" \
"buffer_load_dwordx4 v20, s[80:83], s73 offen offset:0 sc1 lds\n" \
"v_mfma_f32_16x16x32_bf16 a[160:163], v[148:151], v[60:63], a[160:163]\n" \
"v_cndmask_b32_e64 v118, v118, v119, s[64:65]\n" \
"v_cndmask_b32_e64 v192, v192, v193, s[64:65]\n" \
"v_mfma_f32_16x16x32_bf16 a[164:167], v[156:159], v[60:63], a[164:167]\n" \
"v_add_f32_e32 v116, s49, v116\n" \
"v_cndmask_b32_e64 v194, v194, v195, s[64:65]\n" \
"v_mfma_f32_16x16x32_bf16 a[164:167], v[160:163], v[64:67], a[164:167]\n" \
"v_add_f32_e64 v118, s49, v118\n" \
"v_cndmask_b32_e64 v208, v208, v209, s[64:65]\n" \
"v_mfma_f32_16x16x32_bf16 a[168:171], v[168:171], v[64:67], a[168:171]\n" \
"buffer_load_dwordx4 v20, s[80:83], s73 offen offset:1024 sc1 lds\n" \
"v_mfma_f32_16x16x32_bf16 a[168:171], v[164:167], v[60:63], a[168:171]\n" \
"v_cndmask_b32_e64 v120, v120, v121, s[64:65]\n" \
"v_cndmask_b32_e64 v210, v210, v211, s[64:65]\n" \
"v_mfma_f32_16x16x32_bf16 a[172:175], v[172:175], v[60:63], a[172:175]\n" \
"v_cndmask_b32_e64 v122, v122, v123, s[64:65]\n" \
"v_cndmask_b32_e64 v224, v224, v225, s[64:65]\n" \
"v_mfma_f32_16x16x32_bf16 a[172:175], v[176:179], v[64:67], a[172:175]\n" \
"v_cndmask_b32_e64 v36, v36, v37, s[64:65]\n" \
"v_cndmask_b32_e64 v226, v226, v227, s[64:65]\n" \
"v_mfma_f32_16x16x32_bf16 a[192:195], v[152:155], v[72:75], a[192:195]\n" \
"buffer_load_dwordx4 v20, s[80:83], s73 offen offset:2048 sc1 lds\n" \
"v_mfma_f32_16x16x32_bf16 a[192:195], v[148:151], v[68:71], a[192:195]\n" \
"v_cndmask_b32_e64 v38, v38, v39, s[64:65]\n" \
"v_cndmask_b32_e64 v240, v240, v241, s[64:65]\n" \
"v_mfma_f32_16x16x32_bf16 a[196:199], v[156:159], v[68:71], a[196:199]\n" \
"v_cndmask_b32_e64 v48, v48, v49, s[64:65]\n" \
"v_cndmask_b32_e64 v242, v242, v243, s[64:65]\n" \
"v_mfma_f32_16x16x32_bf16 a[196:199], v[160:163], v[72:75], a[196:199]\n" \
"v_cndmask_b32_e64 v50, v50, v51, s[64:65]\n" \
"v_cndmask_b32_e64 v192, v116, v192, s[66:67]\n" \
"v_mfma_f32_16x16x32_bf16 a[200:203], v[168:171], v[72:75], a[200:203]\n" \
"buffer_load_dwordx4 v20, s[80:83], s73 offen offset:3072 sc1 lds\n" \
"v_mfma_f32_16x16x32_bf16 a[200:203], v[164:167], v[68:71], a[200:203]\n" \
"s_sub_u32 m0, %[mB], s53\n" \
"s_nop 0\n" \
"v_cndmask_b32_e64 v194, v118, v194, s[68:69]\n" \
"v_mfma_f32_16x16x32_bf16 a[204:207], v[172:175], v[68:71], a[204:207]\n" \
"v_cndmask_b32_e64 v208, v120, v208, s[66:67]\n" \
"v_cndmask_b32_e64 v210, v122, v210, s[68:69]\n" \
"v_mfma_f32_16x16x32_bf16 a[204:207], v[176:179], v[72:75], a[204:207]\n" \
"v_cndmask_b32_e64 v224, v36, v224, s[66:67]\n" \
"v_cndmask_b32_e64 v226, v38, v226, s[68:69]\n" \
"v_mfma_f32_16x16x32_bf16 a[224:227], v[152:155], v[80:83], a[224:227]\n" \
"buffer_load_dwordx4 v20, s[80:83], s74 offen offset:0 sc1 lds\n" \
"v_mfma_f32_16x16x32_bf16 a[224:227], v[148:151], v[76:79], a[224:227]\n" \
"v_cndmask_b32_e64 v240, v48, v240, s[66:67]\n" \
"v_cndmask_b32_e64 v242, v50, v242, s[68:69]\n" \
"v_mfma_f32_16x16x32_bf16 a[228:231], v[156:159], v[76:79], a[228:231]\n" \
"v_max_f32_e32 v193, v192, v194\n" \
"v_max_f32_e32 v209, v208, v210\n" \
"v_mfma_f32_16x16x32_bf16 a[228:231], v[160:163], v[80:83], a[228:231]\n" \
"v_max_f32_e32 v225, v224, v226\n" \
"v_max_f32_e32 v241, v240, v242\n" \
"v_mfma_f32_16x16x32_bf16 a[232:235], v[168:171], v[80:83], a[232:235]\n" \
"buffer_load_dwordx4 v20, s[80:83], s74 offen offset:1024 sc1 lds\n" \
"v_mfma_f32_16x16x32_bf16 a[232:235], v[164:167], v[76:79], a[232:235]\n" \
"v_permlane32_swap_b32_e32 v193, v225\n" \
"v_permlane32_swap_b32_e32 v209, v241\n" \
"v_mfma_f32_16x16x32_bf16 a[236:239], v[172:175], v[76:79], a[236:239]\n" \
"v_max_f32_e32 v193, v193, v225\n" \
"v_max_f32_e32 v209, v209, v241\n" \
"v_permlane32_swap_b32_e32 v196, v228\n" \
"v_permlane32_swap_b32_e32 v212, v244\n" \
"buffer_load_dwordx4 v20, s[80:83], s74 offen offset:2048 sc1 lds\n" \
"v_permlane16_swap_b32_e32 v193, v209\n" \
"v_permlane16_swap_b32_e32 v228, v244\n" \
"v_mfma_f32_16x16x32_bf16 a[236:239], v[176:179], v[80:83], a[236:239]\n" \
"v_max_f32_e32 v193, v193, v209\n" \
"v_mul_f32_e32 v193, s72, v193\n" \
"v_mfma_f32_16x16x32_bf16 a[144:147], v[152:155], v[88:91], a[144:147]\n" \
"v_mul_f32_e32 v45, 0xbfb8aa3b, v228\n" \
"v_mov_b32_e64 v209, v193\n" \
"v_mfma_f32_16x16x16_bf16 v[196:199], v[190:191], v[46:47], 0\n" \
"buffer_load_dwordx4 v20, s[80:83], s74 offen offset:3072 sc1 lds\n" \
"v_mfma_f32_16x16x32_bf16 a[144:147], v[148:151], v[84:87], a[144:147]\n" \
"s_sub_u32 m0, %[mB], 0x4000\n" \
"v_cvt_pk_bf16_f32 v135, v250, v251\n" \
"v_mfma_f32_16x16x16_bf16 v[212:215], v[206:207], v[46:47], 0\n" \
"v_mfma_f32_16x16x16_bf16 v[228:231], v[222:223], v[46:47], 0\n" \
"v_permlane16_swap_b32_e32 v193, v209\n" \
"v_mfma_f32_16x16x16_bf16 v[244:247], v[238:239], v[46:47], 0\n" \
"buffer_load_dwordx4 v20, s[80:83], s75 offen offset:0 sc1 lds\n" \
"v_mfma_f32_16x16x32_bf16 a[148:151], v[156:159], v[84:87], a[148:151]\n" \
"v_mov_b32_e32 v225, v193\n" \
"v_mov_b32_e64 v241, v209\n" \
"v_mfma_f32_16x16x32_bf16 a[148:151], v[160:163], v[88:91], a[148:151]\n" \
"v_permlane32_swap_b32_e32 v193, v225\n" \
"v_permlane32_swap_b32_e32 v209, v241\n" \
"v_mfma_f32_16x16x32_bf16 a[152:155], v[168:171], v[88:91], a[152:155]\n" \
"v_fma_f32 v192, v192, s72, -v193\n" \
"v_fma_f32 v194, v194, s72, -v193\n" \
"buffer_load_dwordx4 v20, s[80:83], s75 offen offset:1024 sc1 lds\n" \
"v_mfma_f32_16x16x32_bf16 a[152:155], v[164:167], v[84:87], a[152:155]\n" \
"v_fma_f32 v208, v208, s72, -v209\n" \
"v_fma_f32 v210, v210, s72, -v209\n" \
"v_exp_f32_e32 v192, v192\n" \
"v_exp_f32_e32 v194, v194\n" \
"v_exp_f32_e32 v208, v208\n" \
"v_exp_f32_e32 v210, v210\n" \
"v_mfma_f32_16x16x32_bf16 a[156:159], v[172:175], v[84:87], a[156:159]\n" \
"buffer_load_dwordx4 v20, s[80:83], s75 offen offset:2048 sc1 lds\n" \
"v_mfma_f32_16x16x32_bf16 a[156:159], v[176:179], v[88:91], a[156:159]\n" \
"v_fma_f32 v224, v224, s72, -v225\n" \
"v_fma_f32 v226, v226, s72, -v225\n" \
"v_exp_f32_e32 v224, v224\n" \
"v_exp_f32_e32 v226, v226\n" \
"v_mfma_f32_16x16x32_bf16 a[176:179], v[152:155], v[96:99], a[176:179]\n" \
"v_fma_f32 v240, v240, s72, -v241\n" \
"v_fma_f32 v242, v242, s72, -v241\n" \
"v_mfma_f32_16x16x32_bf16 a[176:179], v[148:151], v[92:95], a[176:179]\n" \
"buffer_load_dwordx4 v20, s[80:83], s75 offen offset:3072 sc1 lds\n" \
"v_mfma_f32_16x16x32_bf16 a[180:183], v[156:159], v[92:95], a[180:183]\n" \
"s_mov_b32 m0, s55\n" \
"v_cvt_pk_bf16_f32 v192, v192, v194\n" \
"v_mfma_f32_16x16x32_bf16 a[180:183], v[160:163], v[96:99], a[180:183]\n" \
"v_cvt_pk_bf16_f32 v208, v208, v210\n" \
"v_cvt_pk_bf16_f32 v136, v196, v197\n" \
"v_exp_f32_e32 v240, v240\n" \
"v_exp_f32_e64 v242, v242\n" \
"v_mfma_f32_16x16x32_bf16 a[184:187], v[168:171], v[96:99], a[184:187]\n" \
"buffer_load_dwordx4 v20, s[80:83], s76 offen offset:0 sc1 lds\n" \
"v_mfma_f32_16x16x32_bf16 a[184:187], v[164:167], v[92:95], a[184:187]\n" \
"v_cvt_pk_bf16_f32 v137, v198, v199\n" \
"v_cvt_pk_bf16_f32 v138, v212, v213\n" \
"v_mfma_f32_16x16x32_bf16 a[188:191], v[172:175], v[92:95], a[188:191]\n" \
"v_cvt_pk_bf16_f32 v139, v214, v215\n" \
"v_perm_b32 v184, v192, v192, v40\n" \
"v_mfma_f32_16x16x32_bf16 a[188:191], v[176:179], v[96:99], a[188:191]\n" \
"v_perm_b32 v185, v192, v192, v41\n" \
"v_perm_b32 v186, v192, v192, v42\n" \
"v_perm_b32 v187, v192, v192, v43\n" \
"L_pf9:\n" \
"s_cbranch_execz L_pf13\n" \
"s_nop 0\n" \
"buffer_load_dwordx4 v20, s[80:83], s76 offen offset:1024 sc1 lds\n" \
"v_perm_b32 v200, v208, v208, v40\n" \
"v_perm_b32 v201, v208, v208, v41\n" \
"v_perm_b32 v202, v208, v208, v42\n" \
"v_perm_b32 v203, v208, v208, v43\n" \
"v_cvt_pk_bf16_f32 v224, v224, v226\n" \
"v_cvt_pk_bf16_f32 v140, v228, v229\n" \
"v_cvt_pk_bf16_f32 v141, v230, v231\n" \
"v_mfma_f32_16x16x32_bf16 v[116:119], v[134:137], v[184:187], 0\n" \
"v_mfma_f32_16x16x32_bf16 v[120:123], v[136:139], v[200:203], 0\n" \
"v_perm_b32 v216, v224, v224, v40\n" \
"v_perm_b32 v217, v224, v224, v41\n" \
"v_perm_b32 v218, v224, v224, v42\n" \
"v_perm_b32 v219, v224, v224, v43\n" \
"v_cvt_pk_bf16_f32 v240, v240, v242\n" \
"v_cvt_pk_bf16_f32 v142, v244, v245\n" \
"v_cvt_pk_bf16_f32 v143, v246, v247\n" \
"v_mfma_f32_16x16x32_bf16 v[36:39], v[138:141], v[216:219], 0\n" \
"v_perm_b32 v232, v240, v240, v40\n" \
"v_perm_b32 v233, v240, v240, v41\n" \
"v_perm_b32 v234, v240, v240, v42\n" \
"v_perm_b32 v235, v240, v240, v43\n" \
"v_exp_f32_e64 v45, v45\n" \
"v_mov_b32_dpp v118, v122 quad_perm:[0,1,2,3] row_mask:0x2 bank_mask:0xf\n" \
"v_mfma_f32_16x16x32_bf16 v[48:51], v[140:143], v[232:235], 0\n" \
"v_cvt_pk_bf16_f32 v124, v252, v253\n" \
"v_cvt_pk_bf16_f32 v125, v254, v255\n" \
"v_mov_b32_dpp v118, v38 quad_perm:[0,1,2,3] row_mask:0x4 bank_mask:0xf\n" \
"v_add_f32_e64 v45, 1.0, v45\n" \
"v_mfma_f32_16x16x32_bf16 v[192:195], v[124:127], v[184:187], 0\n" \
"v_mfma_f32_16x16x32_bf16 v[224:227], v[128:131], v[216:219], 0\n" \
"v_rcp_f32_e64 v45, v45\n" \
"v_mov_b32_dpp v116, v120 quad_perm:[0,1,2,3] row_mask:0x2 bank_mask:0xf\n" \
"v_mov_b32_dpp v118, v50 quad_perm:[0,1,2,3] row_mask:0x8 bank_mask:0xf\n" \
"v_mov_b32_dpp v117, v121 quad_perm:[0,1,2,3] row_mask:0x2 bank_mask:0xf\n" \
"v_rcp_f32_e64 v118, v118\n" \
"v_mfma_f32_16x16x32_bf16 v[208:211], v[126:129], v[200:203], 0\n" \
"v_mfma_f32_16x16x32_bf16 v[240:243], v[130:133], v[232:235], 0\n" \
"v_mov_b32_dpp v116, v36 quad_perm:[0,1,2,3] row_mask:0x4 bank_mask:0xf\n" \
"v_mov_b32_dpp v117, v37 quad_perm:[0,1,2,3] row_mask:0x4 bank_mask:0xf\n" \
"v_mul_f32_e64 v118, v45, v118\n" \
"v_mov_b32_dpp v116, v48 quad_perm:[0,1,2,3] row_mask:0x8 bank_mask:0xf\n" \
"v_mov_b32_dpp v117, v49 quad_perm:[0,1,2,3] row_mask:0x8 bank_mask:0xf\n" \
"v_mul_f32_e32 v116, v118, v116\n" \
"v_mul_f32_e32 v117, v118, v117\n" \
"buffer_store_dwordx4 a[12:15], v20, s[80:83], s89 offen offset:1024 sc1\n" \
"s_add_u32 s89, s45, 0x0\n" \
"v_mov_b32_e32 v122, v118\n" \
"v_cvt_pk_bf16_f32 v116, v116, v117\n" \
"v_accvgpr_read_b32 v52, a128\n" \
"v_permlane16_swap_b32_e32 v118, v122\n" \
"v_mov_b32_e32 v38, v118\n" \
"v_mov_b32_e64 v50, v122\n" \
"v_accvgpr_read_b32 v53, a129\n" \
"v_permlane32_swap_b32_e32 v118, v38\n" \
"v_permlane32_swap_b32_e32 v122, v50\n" \
"v_mul_f32_e32 v192, v118, v192\n" \
"v_mul_f32_e32 v193, v118, v193\n" \
"v_mul_f32_e32 v224, v38, v224\n" \
"v_mul_f32_e32 v225, v38, v225\n" \
"v_mul_f32_e32 v194, v118, v194\n" \
"v_mul_f32_e32 v195, v118, v195\n" \
"v_cvt_pk_bf16_f32 v192, v192, v193\n" \
"v_mul_f32_e32 v226, v38, v226\n" \
"v_mul_f32_e32 v227, v38, v227\n" \
"v_cvt_pk_bf16_f32 v224, v224, v225\n" \
"v_cvt_pk_bf16_f32 v193, v194, v195\n" \
"v_mul_f32_e32 v208, v122, v208\n" \
"v_mul_f32_e32 v209, v122, v209\n" \
"v_mul_f32_e32 v210, v122, v210\n" \
"v_mul_f32_e32 v211, v122, v211\n" \
"v_cvt_pk_bf16_f32 v225, v226, v227\n" \
"v_mul_f32_e32 v240, v50, v240\n" \
"v_mul_f32_e32 v241, v50, v241\n" \
"v_mul_f32_e32 v242, v50, v242\n" \
"v_mul_f32_e32 v243, v50, v243\n" \
"v_cvt_pk_bf16_f32 v194, v208, v209\n" \
"v_cvt_pk_bf16_f32 v195, v210, v211\n" \
"v_cvt_pk_bf16_f32 v226, v240, v241\n" \
"v_cvt_pk_bf16_f32 v227, v242, v243\n" \
"v_permlane32_swap_b32_e32 v192, v194\n" \
"v_permlane32_swap_b32_e32 v193, v195\n" \
"v_permlane32_swap_b32_e32 v224, v226\n" \
"v_permlane32_swap_b32_e32 v225, v227\n" \
"v_accvgpr_read_b32 v54, a130\n" \
"v_accvgpr_read_b32 v55, a131\n" \
"v_pk_mul_f32 v[52:53], v[52:53], s[70:71]\n" \
"v_accvgpr_read_b32 v56, a132\n" \
"v_accvgpr_read_b32 v57, a133\n" \
"v_accvgpr_read_b32 v58, a134\n" \
"v_accvgpr_read_b32 v59, a135\n" \
"v_pk_mul_f32 v[54:55], v[54:55], s[70:71]\n" \
"v_cvt_pk_bf16_f32 v52, v52, v53\n" \
"v_pk_mul_f32 v[56:57], v[56:57], s[70:71]\n" \
"v_pk_mul_f32 v[58:59], v[58:59], s[70:71]\n" \
"v_cvt_pk_bf16_f32 v53, v54, v55\n" \
"v_cvt_pk_bf16_f32 v54, v56, v57\n" \
"v_cvt_pk_bf16_f32 v55, v58, v59\n" \
"ds_write_b128 v27, v[52:55] offset:0\n" \
"buffer_store_dwordx4 a[40:43], v20, s[80:83], s88 offen offset:0 sc1\n" \
"s_add_u32 s88, s45, 0x0\n" \
"s_nop 0\n" \
"buffer_store_dword v116, v25, s[80:83], s51 offen sc1\n" \
"buffer_store_dwordx4 v[192:195], v24, s[80:83], s85 offen sc1\n" \
"buffer_store_dwordx4 v[224:227], v24, s[80:83], s87 offen sc1\n" \
"v_accvgpr_read_b32 v76, a224\n" \
"v_accvgpr_read_b32 v77, a225\n" \
"v_accvgpr_read_b32 v78, a226\n" \
"v_accvgpr_read_b32 v79, a227\n" \
"v_pk_mul_f32 v[76:77], v[76:77], s[70:71]\n" \
"v_accvgpr_read_b32 v52, a136\n" \
"v_accvgpr_read_b32 v53, a137\n" \
"v_accvgpr_read_b32 v80, a228\n" \
"v_accvgpr_read_b32 v81, a229\n" \
"v_accvgpr_read_b32 v82, a230\n" \
"v_accvgpr_read_b32 v83, a231\n" \
"v_pk_mul_f32 v[78:79], v[78:79], s[70:71]\n" \
"v_cvt_pk_bf16_f32 v76, v76, v77\n" \
"v_accvgpr_read_b32 v54, a138\n" \
"v_accvgpr_read_b32 v55, a139\n" \
"v_pk_mul_f32 v[52:53], v[52:53], s[70:71]\n" \
"v_pk_mul_f32 v[80:81], v[80:81], s[70:71]\n" \
"v_pk_mul_f32 v[82:83], v[82:83], s[70:71]\n" \
"v_cvt_pk_bf16_f32 v77, v78, v79\n" \
"v_accvgpr_read_b32 v56, a140\n" \
"v_accvgpr_read_b32 v57, a141\n" \
"v_accvgpr_read_b32 v58, a142\n" \
"v_accvgpr_read_b32 v59, a143\n" \
"v_pk_mul_f32 v[54:55], v[54:55], s[70:71]\n" \
"v_cvt_pk_bf16_f32 v52, v52, v53\n" \
"v_cvt_pk_bf16_f32 v78, v80, v81\n" \
"v_cvt_pk_bf16_f32 v79, v82, v83\n" \
"ds_write_b128 v27, v[76:79] offset:12288\n" \
"v_pk_mul_f32 v[56:57], v[56:57], s[70:71]\n" \
"v_pk_mul_f32 v[58:59], v[58:59], s[70:71]\n" \
"v_cvt_pk_bf16_f32 v53, v54, v55\n" \
"v_cvt_pk_bf16_f32 v54, v56, v57\n" \
"v_cvt_pk_bf16_f32 v55, v58, v59\n" \
"ds_write_b128 v27, v[52:55] offset:1024\n" \
"v_accvgpr_read_b32 v76, a208\n" \
"v_accvgpr_read_b32 v77, a209\n" \
"v_accvgpr_read_b32 v78, a210\n" \
"v_accvgpr_read_b32 v79, a211\n" \
"v_accvgpr_read_b32 v52, a168\n" \
"v_accvgpr_read_b32 v53, a169\n" \
"v_mfma_f32_16x16x32_bf16 v[76:79], v[152:155], v[104:107], v[76:79]\n" \
"v_accvgpr_read_b32 v54, a170\n" \
"v_accvgpr_read_b32 v55, a171\n" \
"v_mfma_f32_16x16x32_bf16 v[76:79], v[148:151], v[100:103], v[76:79]\n" \
"v_accvgpr_read_b32 v56, a172\n" \
"v_accvgpr_read_b32 v57, a173\n" \
"v_accvgpr_read_b32 v58, a174\n" \
"v_accvgpr_read_b32 v59, a175\n" \
"v_pk_mul_f32 v[52:53], v[52:53], s[70:71]\n" \
"v_pk_mul_f32 v[54:55], v[54:55], s[70:71]\n" \
"v_cvt_pk_bf16_f32 v52, v52, v53\n" \
"v_pk_mul_f32 v[56:57], v[56:57], s[70:71]\n" \
"v_pk_mul_f32 v[58:59], v[58:59], s[70:71]\n" \
"buffer_store_dwordx4 a[44:47], v20, s[80:83], s89 offen offset:1024 sc1\n" \
"s_add_u32 s89, s45, 0x0\n" \
"s_nop 0\n" \
"v_cvt_pk_bf16_f32 v53, v54, v55\n" \
"v_cvt_pk_bf16_f32 v54, v56, v57\n" \
"v_cvt_pk_bf16_f32 v55, v58, v59\n" \
"ds_write_b128 v27, v[52:55] offset:5120\n" \
"v_accvgpr_read_b32 v80, a212\n" \
"v_accvgpr_read_b32 v81, a213\n" \
"v_accvgpr_read_b32 v82, a214\n" \
"v_accvgpr_read_b32 v83, a215\n" \
"v_accvgpr_read_b32 v52, a232\n" \
"v_accvgpr_read_b32 v53, a233\n" \
"v_mfma_f32_16x16x32_bf16 v[80:83], v[156:159], v[100:103], v[80:83]\n" \
"v_accvgpr_read_b32 v54, a234\n" \
"v_accvgpr_read_b32 v55, a235\n" \
"v_mfma_f32_16x16x32_bf16 v[80:83], v[160:163], v[104:107], v[80:83]\n" \
"v_accvgpr_read_b32 v56, a236\n" \
"v_accvgpr_read_b32 v57, a237\n" \
"v_accvgpr_read_b32 v58, a238\n" \
"v_accvgpr_read_b32 v59, a239\n" \
"v_pk_mul_f32 v[52:53], v[52:53], s[70:71]\n" \
"v_pk_mul_f32 v[54:55], v[54:55], s[70:71]\n" \
"v_cvt_pk_bf16_f32 v52, v52, v53\n" \
"v_pk_mul_f32 v[56:57], v[56:57], s[70:71]\n" \
"v_pk_mul_f32 v[58:59], v[58:59], s[70:71]\n" \
"v_cvt_pk_bf16_f32 v53, v54, v55\n" \
"v_cvt_pk_bf16_f32 v54, v56, v57\n" \
"v_cvt_pk_bf16_f32 v55, v58, v59\n" \
"ds_write_b128 v27, v[52:55] offset:13312\n" \
"v_accvgpr_read_b32 v52, a216\n" \
"v_accvgpr_read_b32 v53, a217\n" \
"v_accvgpr_read_b32 v54, a218\n" \
"v_accvgpr_read_b32 v55, a219\n" \
"v_accvgpr_read_b32 v60, a160\n" \
"v_accvgpr_read_b32 v61, a161\n" \
"v_mfma_f32_16x16x32_bf16 v[52:55], v[168:171], v[104:107], v[52:55]\n" \
"v_accvgpr_read_b32 v62, a162\n" \
"v_accvgpr_read_b32 v63, a163\n" \
"v_mfma_f32_16x16x32_bf16 v[52:55], v[164:167], v[100:103], v[52:55]\n" \
"v_accvgpr_read_b32 v56, a220\n" \
"v_accvgpr_read_b32 v57, a221\n" \
"v_accvgpr_read_b32 v58, a222\n" \
"v_accvgpr_read_b32 v59, a223\n" \
"v_pk_mul_f32 v[60:61], v[60:61], s[70:71]\n" \
"v_accvgpr_read_b32 v64, a164\n" \
"v_mfma_f32_16x16x32_bf16 v[56:59], v[172:175], v[100:103], v[56:59]\n" \
"v_accvgpr_read_b32 v65, a165\n" \
"v_accvgpr_read_b32 v66, a166\n" \
"v_mfma_f32_16x16x32_bf16 v[56:59], v[176:179], v[104:107], v[56:59]\n" \
"v_accvgpr_read_b32 v67, a167\n" \
"buffer_store_dwordx4 a[72:75], v20, s[80:83], s88 offen offset:2048 sc1\n" \
"s_add_u32 s88, s45, 0x1000\n" \
"v_pk_mul_f32 v[62:63], v[62:63], s[70:71]\n" \
"v_cvt_pk_bf16_f32 v60, v60, v61\n" \
"v_pk_mul_f32 v[64:65], v[64:65], s[70:71]\n" \
"v_pk_mul_f32 v[66:67], v[66:67], s[70:71]\n" \
"v_cvt_pk_bf16_f32 v61, v62, v63\n" \
"v_cvt_pk_bf16_f32 v62, v64, v65\n" \
"v_cvt_pk_bf16_f32 v63, v66, v67\n" \
"ds_write_b128 v27, v[60:63] offset:4096\n" \
"v_accvgpr_read_b32 v68, a192\n" \
"v_accvgpr_read_b32 v69, a193\n" \
"v_accvgpr_read_b32 v60, a200\n" \
"v_accvgpr_read_b32 v61, a201\n" \
"v_accvgpr_read_b32 v70, a194\n" \
"v_accvgpr_read_b32 v71, a195\n" \
"v_pk_mul_f32 v[68:69], v[68:69], s[70:71]\n" \
"v_accvgpr_read_b32 v62, a202\n" \
"v_accvgpr_read_b32 v63, a203\n" \
"v_pk_mul_f32 v[60:61], v[60:61], s[70:71]\n" \
"v_accvgpr_read_b32 v72, a196\n" \
"v_accvgpr_read_b32 v73, a197\n" \
"v_accvgpr_read_b32 v74, a198\n" \
"v_accvgpr_read_b32 v75, a199\n" \
"v_pk_mul_f32 v[70:71], v[70:71], s[70:71]\n" \
"v_cvt_pk_bf16_f32 v68, v68, v69\n" \
"v_accvgpr_read_b32 v64, a204\n" \
"v_accvgpr_read_b32 v65, a205\n" \
"v_accvgpr_read_b32 v66, a206\n" \
"v_accvgpr_read_b32 v67, a207\n" \
"v_pk_mul_f32 v[62:63], v[62:63], s[70:71]\n" \
"v_cvt_pk_bf16_f32 v60, v60, v61\n" \
"v_accvgpr_read_b32 v100, a240\n" \
"v_accvgpr_read_b32 v101, a241\n" \
"v_accvgpr_read_b32 v102, a242\n" \
"v_accvgpr_read_b32 v103, a243\n" \
"v_pk_mul_f32 v[72:73], v[72:73], s[70:71]\n" \
"v_pk_mul_f32 v[74:75], v[74:75], s[70:71]\n" \
"v_mfma_f32_16x16x32_bf16 v[100:103], v[152:155], v[112:115], v[100:103]\n" \
"v_cvt_pk_bf16_f32 v69, v70, v71\n" \
"v_cvt_pk_bf16_f32 v61, v62, v63\n" \
"v_mfma_f32_16x16x32_bf16 v[100:103], v[148:151], v[108:111], v[100:103]\n" \
"v_cvt_pk_bf16_f32 v70, v72, v73\n" \
"v_cvt_pk_bf16_f32 v71, v74, v75\n" \
"ds_write_b128 v27, v[68:71] offset:8192\n" \
"v_pk_mul_f32 v[64:65], v[64:65], s[70:71]\n" \
"v_pk_mul_f32 v[66:67], v[66:67], s[70:71]\n" \
"v_cvt_pk_bf16_f32 v62, v64, v65\n" \
"v_cvt_pk_bf16_f32 v63, v66, v67\n" \
"ds_write_b128 v27, v[60:63] offset:9216\n" \
"v_accvgpr_read_b32 v84, a144\n" \
"buffer_store_dwordx4 a[76:79], v20, s[80:83], s89 offen offset:3072 sc1\n" \
"s_add_u32 s89, s45, 0x1000\n" \
"v_accvgpr_read_b32 v85, a145\n" \
"v_accvgpr_read_b32 v68, a152\n" \
"v_accvgpr_read_b32 v69, a153\n" \
"v_accvgpr_read_b32 v92, a176\n" \
"v_accvgpr_read_b32 v93, a177\n" \
"v_accvgpr_read_b32 v60, a184\n" \
"v_accvgpr_read_b32 v61, a185\n" \
"v_accvgpr_read_b32 v86, a146\n" \
"v_accvgpr_read_b32 v87, a147\n" \
"v_pk_mul_f32 v[84:85], v[84:85], s[70:71]\n" \
"v_accvgpr_read_b32 v70, a154\n" \
"v_accvgpr_read_b32 v71, a155\n" \
"v_pk_mul_f32 v[68:69], v[68:69], s[70:71]\n" \
"v_accvgpr_read_b32 v94, a178\n" \
"v_accvgpr_read_b32 v95, a179\n" \
"v_pk_mul_f32 v[92:93], v[92:93], s[70:71]\n" \
"v_accvgpr_read_b32 v62, a186\n" \
"v_accvgpr_read_b32 v63, a187\n" \
"v_pk_mul_f32 v[60:61], v[60:61], s[70:71]\n" \
"v_pk_mul_f32 v[76:77], v[76:77], s[70:71]\n" \
"v_pk_mul_f32 v[52:53], v[52:53], s[70:71]\n" \
"v_pk_mul_f32 v[100:101], v[100:101], s[70:71]\n" \
"v_accvgpr_read_b32 v88, a148\n" \
"v_accvgpr_read_b32 v89, a149\n" \
"v_accvgpr_read_b32 v90, a150\n" \
"v_accvgpr_read_b32 v91, a151\n" \
"v_pk_mul_f32 v[86:87], v[86:87], s[70:71]\n" \
"v_cvt_pk_bf16_f32 v84, v84, v85\n" \
"v_accvgpr_read_b32 v72, a156\n" \
"v_accvgpr_read_b32 v73, a157\n" \
"v_accvgpr_read_b32 v74, a158\n" \
"v_accvgpr_read_b32 v75, a159\n" \
"v_pk_mul_f32 v[70:71], v[70:71], s[70:71]\n" \
"v_cvt_pk_bf16_f32 v68, v68, v69\n" \
"v_accvgpr_read_b32 v96, a180\n" \
"v_accvgpr_read_b32 v97, a181\n" \
"v_accvgpr_read_b32 v98, a182\n" \
"v_accvgpr_read_b32 v99, a183\n" \
"v_pk_mul_f32 v[94:95], v[94:95], s[70:71]\n" \
"v_cvt_pk_bf16_f32 v92, v92, v93\n" \
"v_accvgpr_read_b32 v64, a188\n" \
"v_accvgpr_read_b32 v65, a189\n" \
"v_accvgpr_read_b32 v66, a190\n" \
"v_accvgpr_read_b32 v67, a191\n" \
"v_pk_mul_f32 v[62:63], v[62:63], s[70:71]\n" \
"v_cvt_pk_bf16_f32 v60, v60, v61\n" \
"v_pk_mul_f32 v[78:79], v[78:79], s[70:71]\n" \
"v_cvt_pk_bf16_f32 v76, v76, v77\n" \
"v_pk_mul_f32 v[54:55], v[54:55], s[70:71]\n" \
"v_cvt_pk_bf16_f32 v52, v52, v53\n" \
"v_pk_mul_f32 v[102:103], v[102:103], s[70:71]\n" \
"v_cvt_pk_bf16_f32 v100, v100, v101\n" \
"v_pk_mul_f32 v[88:89], v[88:89], s[70:71]\n" \
"v_pk_mul_f32 v[90:91], v[90:91], s[70:71]\n" \
"v_cvt_pk_bf16_f32 v85, v86, v87\n" \
"v_pk_mul_f32 v[72:73], v[72:73], s[70:71]\n" \
"v_pk_mul_f32 v[74:75], v[74:75], s[70:71]\n" \
"v_cvt_pk_bf16_f32 v69, v70, v71\n" \
"v_pk_mul_f32 v[96:97], v[96:97], s[70:71]\n" \
"v_pk_mul_f32 v[98:99], v[98:99], s[70:71]\n" \
"buffer_store_dwordx4 a[104:107], v20, s[80:83], s88 offen offset:0 sc1\n" \
"v_cvt_pk_bf16_f32 v93, v94, v95\n" \
"v_pk_mul_f32 v[64:65], v[64:65], s[70:71]\n" \
"v_pk_mul_f32 v[66:67], v[66:67], s[70:71]\n" \
"v_cvt_pk_bf16_f32 v61, v62, v63\n" \
"v_pk_mul_f32 v[80:81], v[80:81], s[70:71]\n" \
"v_pk_mul_f32 v[82:83], v[82:83], s[70:71]\n" \
"v_cvt_pk_bf16_f32 v77, v78, v79\n" \
"v_pk_mul_f32 v[56:57], v[56:57], s[70:71]\n" \
"v_pk_mul_f32 v[58:59], v[58:59], s[70:71]\n" \
"v_cvt_pk_bf16_f32 v53, v54, v55\n" \
"v_accvgpr_read_b32 v104, a244\n" \
"v_accvgpr_read_b32 v105, a245\n" \
"v_accvgpr_read_b32 v106, a246\n" \
"v_accvgpr_read_b32 v107, a247\n" \
"v_cvt_pk_bf16_f32 v101, v102, v103\n" \
"v_cvt_pk_bf16_f32 v86, v88, v89\n" \
"v_mfma_f32_16x16x32_bf16 v[104:107], v[156:159], v[108:111], v[104:107]\n" \
"v_cvt_pk_bf16_f32 v87, v90, v91\n" \
"v_cvt_pk_bf16_f32 v70, v72, v73\n" \
"ds_write_b128 v21, v[84:87] offset:16384\n" \
"v_mfma_f32_16x16x32_bf16 v[104:107], v[160:163], v[112:115], v[104:107]\n" \
"v_cvt_pk_bf16_f32 v71, v74, v75\n" \
"ds_write_b128 v21, v[68:71] offset:17408\n" \
"v_cvt_pk_bf16_f32 v94, v96, v97\n" \
"v_cvt_pk_bf16_f32 v95, v98, v99\n" \
"ds_write_b128 v21, v[92:95] offset:20480\n" \
"v_cvt_pk_bf16_f32 v62, v64, v65\n" \
"v_cvt_pk_bf16_f32 v63, v66, v67\n" \
"ds_write_b128 v21, v[60:63] offset:21504\n" \
"v_pk_mul_f32 v[104:105], v[104:105], s[70:71]\n" \
"v_pk_mul_f32 v[106:107], v[106:107], s[70:71]\n" \
"v_cvt_pk_bf16_f32 v78, v80, v81\n" \
"v_cvt_pk_bf16_f32 v79, v82, v83\n" \
"ds_write_b128 v21, v[76:79] offset:24576\n" \
"v_cvt_pk_bf16_f32 v54, v56, v57\n" \
"v_cvt_pk_bf16_f32 v55, v58, v59\n" \
"ds_write_b128 v21, v[52:55] offset:25600\n" \
"v_cvt_pk_bf16_f32 v102, v104, v105\n" \
"v_cvt_pk_bf16_f32 v103, v106, v107\n" \
"ds_write_b128 v21, v[100:103] offset:28672\n" \
"buffer_store_dwordx4 a[108:111], v20, s[80:83], s89 offen offset:1024 sc1\n" \
"v_accvgpr_read_b32 v84, a248\n" \
"v_accvgpr_read_b32 v85, a249\n" \
"v_accvgpr_read_b32 v86, a250\n" \
"v_accvgpr_read_b32 v87, a251\n" \
"v_accvgpr_read_b32 v88, a252\n" \
"v_accvgpr_read_b32 v89, a253\n" \
"v_mfma_f32_16x16x32_bf16 v[84:87], v[168:171], v[112:115], v[84:87]\n" \
"v_accvgpr_read_b32 v90, a254\n" \
"v_accvgpr_read_b32 v91, a255\n" \
"v_mfma_f32_16x16x32_bf16 v[84:87], v[164:167], v[108:111], v[84:87]\n" \
"v_sub_f32_e32 v44, 1.0, v45\n" \
"v_mfma_f32_16x16x32_bf16 v[88:91], v[172:175], v[108:111], v[88:91]\n" \
"ds_write_b32 v26, v44\n" \
"v_mfma_f32_16x16x32_bf16 v[88:91], v[176:179], v[112:115], v[88:91]\n" \
"s_nop 3\n" \
"v_pk_mul_f32 v[84:85], v[84:85], s[70:71]\n" \
"v_pk_mul_f32 v[86:87], v[86:87], s[70:71]\n" \
"v_cvt_pk_bf16_f32 v84, v84, v85\n" \
"v_cvt_pk_bf16_f32 v85, v86, v87\n" \
"v_pk_mul_f32 v[88:89], v[88:89], s[70:71]\n" \
"v_pk_mul_f32 v[90:91], v[90:91], s[70:71]\n" \
"v_cvt_pk_bf16_f32 v86, v88, v89\n" \
"v_cvt_pk_bf16_f32 v87, v90, v91\n" \
"ds_write_b128 v21, v[84:87] offset:29696\n" \
"L_end_%=:\n"
#define PROJ_VCLOB "v21","v24","v25","v33","v34","v35","v36","v37","v38","v39","v40","v41","v42","v43","v44","v45","v46","v47","v48","v49","v50","v51","v52","v53","v54","v55","v56","v57","v58","v59","v60","v61","v62","v63","v64","v65","v66","v67","v68","v69","v70","v71","v72","v73","v74","v75","v76","v77","v78","v79","v80","v81","v82","v83","v84","v85","v86","v87","v88","v89","v90","v91","v92","v93","v94","v95","v96","v97","v98","v99","v100","v101","v102","v103","v104","v105","v106","v107","v108","v109","v110","v111","v112","v113","v114","v115","v116","v117","v118","v119","v120","v121","v122","v123","v124","v125","v126","v127","v128","v129","v130","v131","v132","v133","v134","v135","v136","v137","v138","v139","v140","v141","v142","v143","v144","v145","v146","v147","v148","v149","v150","v151","v152","v153","v154","v155","v156","v157","v158","v159","v160","v161","v162","v163","v164","v165","v166","v167","v168","v169","v170","v171","v172","v173","v174","v175","v176","v177","v178","v179","v180","v181","v182","v183","v184","v185","v186","v187","v188","v189","v190","v191","v192","v193","v194","v195","v196","v197","v198","v199","v200","v201","v202","v203","v204","v205","v206","v207","v208","v209","v210","v211","v212","v213","v214","v215","v216","v217","v218","v219","v220","v221","v222","v223","v224","v225","v226","v227","v228","v229","v230","v231","v232","v233","v234","v235","v236","v237","v238","v239","v240","v241","v242","v243","v244","v245","v246","v247","v248","v249","v250","v251","v252","v253","v254","v255"
#define PROJ_SCLOB "s37","s38","s39","s40","s41","s42","s43","s44","s45","s46","s47","s48","s49","s50","s51","s52","s53","s54","s55","s62","s63","s76","s77","s78","s79","s80","s81","s82","s83","s84","s85","s86","s87","s88","s89","s94","s95","s96","s97","s98","s99","s100","s101"

#define PLO(p) ((unsigned)(size_t)(p))
#define PHI(p) ((unsigned)((size_t)(p) >> 32))
__device__ __forceinline__ unsigned proj_phase(const u16* __restrict__ x, const u16* __restrict__ ckv, const u16* __restrict__ wq, const u16* __restrict__ wuk,
                                                       const u16* __restrict__ wuv, const u16* __restrict__ wqkv, const u16* __restrict__ gw, const u16* wo, const u16* wob, const u16* out,
                                                       char* __restrict__ ws, char* lds, const unsigned wid, unsigned& tag, u32x4 (&vp)[5], u32x4 (&sa)[5], unsigned (&sd)[2]) {
    typedef unsigned u32x2 __attribute__((ext_vector_type(2)));
    unsigned* const flags = (unsigned*)(ws + WS_F);
    const int b = blockIdx.x, h = (b & 7) * 4 + ((b >> 3) & 3), mt = b >> 5;
    const unsigned L0 = (unsigned)(size_t)lds, rb = (wid < 2u ? 15u - mt : mt) * 128u;
    RTS(0);
#define PST() do {} while (0)
    const unsigned lane = threadIdx.x & 63u, wr = wid >> 1, wc = wid & 1u;
    PST();
    unsigned vAc[2], vB[2];
    const unsigned sp = b & 7, nt = (b >> 3) & 3, rot = (PROJ_ROT) & 7u;
#pragma unroll
    for (int par = 0; par < 2; ++par) {
        const unsigned lr = lane >> 3, cc = (lane & 7u) ^ (4u * par + (lr >> 1)), r0 = rb + wc * 64u + lr;
        vAc[par] = r0 * (DC * 2) + cc * 16u;
        const unsigned br = lane >> 4, p16 = lane & 15u, sc = (((p16 >> 1) ^ (br | (par << 2))) << 1) | (p16 & 1u);
        vB[par] = (16u * wid + br) * (NBIG * 2) + h * (HD * 2) + sc * 16u;
    }
#define SRD(p, n) (u32x4){PLO(p), PHI(p) & 0xffffu, n, 0x00020000u}
    u32x4 sC = SRD(ckv, S * DC * 2), sK = SRD(wuk, DC * NBIG * 2), sV = SRD(wuv, DC * NBIG * 2);
#define PRE(n) do { SBAR(); asm volatile(PROJ_PRE##n :: "{v22}"(vAc[0]), "{v23}"(vAc[1]), "{v26}"(vB[0]), "{v27}"(vB[1]), "{s[64:67]}"(sC), "{s[68:71]}"(sK), "{s[72:75]}"(sV), \
                 [mA] "s"(L0 + wid * 8192u), [mB] "s"(L0 + 65536u + wid * 4096u), [rc] "s"(rot * 128u) : "memory", "m0", "scc", "s62", "s63", "s84", "s85"); SBAR(); } while (0)
    PRE(0);
    const unsigned fr = lane & 15u, fg = lane >> 4, r4 = (lane >> 2) & 3u, sB = r4 | ((fg & 1u) << 2);
    const unsigned aA0 = L0 + (wr * 64u + fr) * 128u + ((fg ^ (fr >> 1)) << 4);
    unsigned vl = lane * 16u, r32 = L0 + 65536u + (8u * fg + r4) * 256u + (lane & 3u) * 8u + ((sB ^ (4u * wc)) << 5);
    u32x2 r28 = {aA0, aA0 ^ 64u};
    asm volatile("" : "+v"(vl), "+v"(r28), "+v"(r32));
    PST();
    PRE(1);
    PST();
    PRE(2);
    PST();
    asm volatile("s_mov_b64 exec, 0\n s_cmp_lt_u32 %0, 2\n s_cbranch_scc1 L_chA\n s_cmp_eq_u32 %0, 2\n s_cbranch_scc1 L_pf2\n s_branch L_pf3\n"
                 "L_chA: s_cmp_eq_u32 %0, 0\n s_cbranch_scc1 L_pf0\n s_branch L_pf1\n L_retA: s_mov_b64 exec, -1" :: "s"(wid) : "scc", "memory");
    PRE(3);
    PST();
    sa[0] = (u32x4){PLO(x), PHI(x), PLO(wq), PHI(wq)}; sa[1] = (u32x4){PLO(wqkv), PHI(wqkv), PLO(gw), PHI(gw)}; sa[2] = (u32x4){PLO(wo), PHI(wo), PLO(wob), PHI(wob)}; sa[3] = (u32x4){PLO(out), PHI(out), rot * 128u, 0u};
    asm volatile(PROJ_ASM
                 : [tag] "=&s"(tag), "+{s[8:11]}"(sa[0]), "+{s[12:15]}"(sa[1]), "+{s[16:19]}"(sa[2]), "+{s[20:23]}"(sa[3]), "=&{s[56:59]}"(sa[4]), "=&{s60}"(sd[0]), "=&{s61}"(sd[1]),
                   "+{s[64:67]}"(sC), "+{s[68:71]}"(sK), "+{s[72:75]}"(sV), "+{v22}"(vAc[0]), "+{v23}"(vAc[1]), "+{v26}"(vB[0]), "+{v27}"(vB[1]), "+{v[28:29]}"(r28), "+{v32}"(r32),
                   "={v[0:3]}"(vp[0]), "={v[4:7]}"(vp[1]), "={v[8:11]}"(vp[2]), "={v[12:15]}"(vp[3]), "={v[16:19]}"(vp[4])
                 : "{v20}"(vl), [wsl] "s"(PLO(ws)), [wsh] "s"(PHI(ws)), [b] "s"(b), [w] "s"(wid), [mA] "s"(L0 + wid * 8192u), [mB] "s"(L0 + 65536u + wid * 4096u), [mS] "s"(L0 + 151552u + wid * 512u)
                 : "memory", "m0", "vcc", "scc", PROJ_VCLOB, PROJ_SCLOB, ACLOB_LO, ACLOB_HI);
    RTS(1);
    return vl;
}

namespace att {
#define DI __device__ __forceinline__
constexpr int LDS_BYTES = 163840;
constexpr unsigned LDS_G = 159744u, LDS_L = 161792u;
typedef __bf16 bfx8 __attribute__((ext_vector_type(8)));
DI unsigned rfl(unsigned x) { return __builtin_amdgcn_readfirstlane(x); }
DI f32x4 mfma(u32x4 a, u32x4 b, f32x4 c) { return __builtin_amdgcn_mfma_f32_16x16x32_bf16(__builtin_bit_cast(bfx8, a), __builtin_bit_cast(bfx8, b), c, 0, 0, 0); }
typedef __bf16 bfx2 __attribute__((ext_vector_type(2)));
typedef float f32x2 __attribute__((ext_vector_type(2)));
DI unsigned cvtpk2(float lo, float hi) { f32x2 v = {lo, hi}; bfx2 b = __builtin_convertvector(v, bfx2); return __builtin_bit_cast(unsigned, b); }
DI void att_exact(const u16* __restrict__ x, const u16* __restrict__ wq, char* __restrict__ ws, unsigned h, unsigned r0, float g1) {
    const unsigned lane = lane_id(), g = lane >> 4, c = lane & 15u;
    f32x4 o[8];
#pragma unroll
    for (int n = 0; n < 8; ++n) o[n] = (f32x4){0.f, 0.f, 0.f, 0.f};
#pragma unroll 1
    for (unsigned kk = 0; kk < (unsigned)DM; kk += 32u) {
        const unsigned k = kk + 8u * g;
        const u32x4 xf = *reinterpret_cast<const u32x4*>(x + (r0 + c) * DM + k);
#pragma unroll
        for (int n = 0; n < 8; ++n) {
            const u16* w = wq + k * NBIG + h * HD + n * 16 + c; u32x4 wf;
#pragma unroll
            for (int e = 0; e < 4; ++e) wf[e] = (unsigned)w[2 * e * NBIG] | ((unsigned)w[(2 * e + 1) * NBIG] << 16);
            o[n] = mfma(wf, xf, o[n]);
        }
    }
    u32x4 q[4];
#pragma unroll
    for (int p = 0; p < 4; ++p) {
        const f32x4 a = o[2 * p] * 0.12751743082f, b = o[2 * p + 1] * 0.12751743082f;
        q[p] = (u32x4){cvtpk2(a[0], a[1]), cvtpk2(a[2], a[3]), cvtpk2(b[0], b[1]), cvtpk2(b[2], b[3])};
    }
#pragma unroll
    for (int n = 0; n < 8; ++n) o[n] = (f32x4){0.f, 0.f, 0.f, 0.f};
    const __amdgpu_buffer_rsrc_t srd = __builtin_amdgcn_make_buffer_rsrc((void*)(ws + WS_K + (size_t)h * 1048576), (short)0, 1048576, 0x00020000);
    float m = -3.0e38f, l = 0.f;
#pragma unroll 1
    for (unsigned k0 = 0; k0 <= r0 + 15u; k0 += 32u) {
        const unsigned tb = (k0 >> 6) * 32768u + (k0 & 32u) * 256u;
        f32x4 s[2]; float mx = m;
#pragma unroll
        for (int kt = 0; kt < 2; ++kt) {
            s[kt] = (f32x4){0.f, 0.f, 0.f, 0.f};
#pragma unroll
            for (int p = 0; p < 4; ++p) s[kt] = mfma(__builtin_amdgcn_raw_buffer_load_b128(srd, lane * 16u + (kt * 4 + p) * 1024u, tb, 16), q[p], s[kt]);
#pragma unroll
            for (int e = 0; e < 4; ++e) { if (k0 + 16u * kt + 4u * g + e > r0 + c) s[kt][e] = -3.0e38f; mx = fmaxf(mx, s[kt][e]); }
        }
        mx = fmaxf(mx, __shfl_xor(mx, 16)); mx = fmaxf(mx, __shfl_xor(mx, 32));
        const float al = __builtin_amdgcn_exp2f(m - mx); m = mx;
        u32x4 pf;
#pragma unroll
        for (int e = 0; e < 4; ++e) pf[e] = cvtpk2(__builtin_amdgcn_exp2f(s[e >> 1][2 * (e & 1)] - m), __builtin_amdgcn_exp2f(s[e >> 1][2 * (e & 1) + 1] - m));
        l *= al;
#pragma unroll
        for (int e = 0; e < 4; ++e) l += __uint_as_float(pf[e] << 16) + __uint_as_float(pf[e] & 0xffff0000u);
#pragma unroll
        for (int n = 0; n < 8; ++n) o[n] = mfma(__builtin_amdgcn_raw_buffer_load_b128(srd, lane * 16u + 16384u + ((n >> 2) * 8 + (n & 3)) * 1024u, tb - (k0 & 32u) * 128u, 16), pf, o[n] * al);
    }
    l += __shfl_xor(l, 16); l += __shfl_xor(l, 32);
    const float f = g1 / l;
    const __amdgpu_buffer_rsrc_t srdA = __builtin_amdgcn_make_buffer_rsrc((void*)(ws + WS_AF), (short)0, 128 * KS_TOT * 1024, 0x00020000);
#pragma unroll
    for (int p = 0; p < 4; ++p) {
        const f32x4 a = o[2 * p] * f, b = o[2 * p + 1] * f;
        __builtin_amdgcn_raw_buffer_store_b128((u32x4){cvtpk2(a[0], a[1]), cvtpk2(a[2], a[3]), cvtpk2(b[0], b[1]), cvtpk2(b[2], b[3])}, srdA, lane * 16u + p * 1024u, ((r0 >> 4) * KS_TOT + 18u + 4u * h) * 1024u, 16);
    }
}
}

#define ATT_ASM \
"L_att0:\n" \
"s_mov_b32 s101, 0\n" \
"s_waitcnt vmcnt(20) lgkmcnt(0)\n" \
"s_barrier\n" \
"v_accvgpr_write_b32 a216, 0\n" \
"v_accvgpr_write_b32 a217, 0\n" \
"v_accvgpr_write_b32 a218, 0\n" \
"v_accvgpr_write_b32 a219, 0\n" \
"v_and_b32_e32 v63, 0x300, v20\n" \
"v_add_u32_e32 v58, s20, v20\n" \
"v_mfma_f32_32x32x16_bf16 v[128:143], a[216:219], a[216:219], 0\n" \
"ds_read_b128 v[112:115], v63 offset:0\n" \
"ds_read_b128 a[0:3], v58 offset:0\n" \
"ds_read_b128 a[16:19], v58 offset:8192\n" \
"ds_read_b128 v[116:119], v63 offset:1024\n" \
"ds_read_b128 a[4:7], v58 offset:1024\n" \
"v_add_u32_e32 v59, s21, v20\n" \
"v_mfma_f32_32x32x16_bf16 v[144:159], a[216:219], a[216:219], 0\n" \
"ds_read_b128 a[32:35], v59 offset:0\n" \
"ds_read_b128 a[48:51], v59 offset:8192\n" \
"s_sub_u32 s67, s12, 0xc000\n" \
"s_mov_b32 s65, s12\n" \
"v_add_u32_e32 v55, s67, v20\n" \
"v_add_u32_e32 v52, 0xc000, v55\n" \
"v_mfma_f32_32x32x16_bf16 v[160:175], a[216:219], a[216:219], 0\n" \
"ds_read_b128 a[20:23], v58 offset:9216\n" \
"s_xor_b32 m0, s65, 0x10000\n" \
"ds_read_b128 a[36:39], v59 offset:1024\n" \
"buffer_load_dwordx4 v52, s[8:11], 0 offen offset:0 sc1 lds\n" \
"ds_read_b128 a[52:55], v59 offset:9216\n" \
"v_mfma_f32_32x32x16_bf16 v[176:191], a[216:219], a[216:219], 0\n" \
"buffer_load_dwordx4 v52, s[8:11], 0 offen offset:1024 sc1 lds\n" \
"ds_read_b128 v[120:123], v63 offset:2048\n" \
"v_add_u32_e32 v60, s15, v20\n" \
"v_add_u32_e32 v60, 0x10000, v60\n" \
"v_mov_b32_e32 v56, s62\n" \
"v_mfma_f32_32x32x16_bf16 v[192:207], a[216:219], a[216:219], 0\n" \
"ds_read_b128 a[8:11], v58 offset:2048\n" \
"ds_read_b128 a[24:27], v58 offset:10240\n" \
"v_readlane_b32 s68, v11, 0\n" \
"v_bfe_i32 v21, v20, 4, 1\n" \
"v_mov_b32_e32 v54, 0x3f803f80\n" \
"v_mov_b32_e32 v57, 0x3f003f00\n" \
"v_mfma_f32_32x32x16_bf16 v[208:223], a[216:219], a[216:219], 0\n" \
"ds_read_b128 a[40:43], v59 offset:2048\n" \
"ds_read_b128 a[56:59], v59 offset:10240\n" \
"s_waitcnt lgkmcnt(14)\n" \
"v_xor_b32_e32 v112, 0x80008000, v112\n" \
"v_xor_b32_e32 v113, 0x80008000, v113\n" \
"v_xor_b32_e32 v114, 0x80008000, v114\n" \
"v_mfma_f32_32x32x16_bf16 v[224:239], a[216:219], a[216:219], 0\n" \
"ds_read_b128 v[124:127], v63 offset:3072\n" \
"ds_read_b128 a[12:15], v58 offset:3072\n" \
"v_xor_b32_e32 v115, 0x80008000, v115\n" \
"v_and_b32_e32 v21, 0xaaaaaaaa, v21\n" \
"v_accvgpr_write_b32 a220, v112\n" \
"s_waitcnt lgkmcnt(13)\n" \
"v_xor_b32_e32 v116, 0x80008000, v116\n" \
"v_mfma_f32_32x32x16_bf16 v[240:255], a[216:219], a[216:219], 0\n" \
"ds_read_b128 a[28:31], v58 offset:11264\n" \
"ds_read_b128 a[44:47], v59 offset:3072\n" \
"v_xor_b32_e32 v117, 0x80008000, v117\n" \
"v_xor_b32_e32 v118, 0x80008000, v118\n" \
"v_xor_b32_e32 v119, 0x80008000, v119\n" \
"v_accvgpr_write_b32 a221, v113\n" \
"v_mfma_f32_32x32x16_bf16 a[192:207], a[216:219], a[216:219], 0\n" \
"ds_read_b128 a[60:63], v59 offset:11264\n" \
"ds_read_b128 a[160:163], v20 offset:0\n" \
"v_accvgpr_write_b32 a222, v114\n" \
"v_accvgpr_write_b32 a223, v115\n" \
"v_readlane_b32 s80, v11, 9\n" \
"v_readlane_b32 s81, v11, 10\n" \
"v_mfma_f32_16x16x32_bf16 v[32:35], v[112:115], a[0:3], 0\n" \
"ds_read_b128 a[164:167], v20 offset:1024\n" \
"ds_read_b128 a[168:171], v20 offset:2048\n" \
"v_mfma_f32_16x16x32_bf16 v[36:39], v[112:115], a[16:19], 0\n" \
"ds_read_b128 a[172:175], v20 offset:3072\n" \
"s_waitcnt lgkmcnt(15)\n" \
"v_mfma_f32_16x16x32_bf16 v[40:43], v[112:115], a[32:35], 0\n" \
"ds_read_b128 a[188:191], v20 offset:7168\n" \
"s_waitcnt lgkmcnt(14)\n" \
"v_xor_b32_e32 v120, 0x80008000, v120\n" \
"v_mfma_f32_16x16x32_bf16 v[44:47], v[112:115], a[48:51], 0\n" \
"ds_read_b128 a[184:187], v20 offset:6144\n" \
"v_xor_b32_e32 v121, 0x80008000, v121\n" \
"v_mfma_f32_16x16x32_bf16 v[32:35], v[116:119], a[4:7], v[32:35]\n" \
"v_xor_b32_e32 v122, 0x80008000, v122\n" \
"v_xor_b32_e32 v123, 0x80008000, v123\n" \
"v_mfma_f32_16x16x32_bf16 v[36:39], v[116:119], a[20:23], v[36:39]\n" \
"ds_read_b128 a[180:183], v20 offset:5120\n" \
"ds_read_b128 a[176:179], v20 offset:4096\n" \
"v_mfma_f32_16x16x32_bf16 v[40:43], v[116:119], a[36:39], v[40:43]\n" \
"ds_read_b128 a[128:131], v20 offset:8192\n" \
"s_mov_b32 s82, 0x47fca0\n" \
"v_mfma_f32_16x16x32_bf16 v[44:47], v[116:119], a[52:55], v[44:47]\n" \
"s_waitcnt lgkmcnt(13)\n" \
"v_xor_b32_e32 v124, 0x80008000, v124\n" \
"v_mfma_f32_16x16x32_bf16 v[32:35], v[120:123], a[8:11], v[32:35]\n" \
"v_xor_b32_e32 v125, 0x80008000, v125\n" \
"v_xor_b32_e32 v126, 0x80008000, v126\n" \
"v_mfma_f32_16x16x32_bf16 v[36:39], v[120:123], a[24:27], v[36:39]\n" \
"ds_read_b128 a[132:135], v20 offset:9216\n" \
"v_xor_b32_e32 v127, 0x80008000, v127\n" \
"v_mfma_f32_16x16x32_bf16 v[40:43], v[120:123], a[40:43], v[40:43]\n" \
"ds_read_b128 a[136:139], v20 offset:10240\n" \
"ds_read_b128 a[140:143], v20 offset:11264\n" \
"v_mfma_f32_16x16x32_bf16 v[44:47], v[120:123], a[56:59], v[44:47]\n" \
"ds_read_b128 a[144:147], v20 offset:12288\n" \
"s_waitcnt lgkmcnt(15)\n" \
"v_mfma_f32_16x16x32_bf16 v[32:35], v[124:127], a[12:15], v[32:35]\n" \
"ds_read_b128 a[148:151], v20 offset:13312\n" \
"ds_read_b128 a[152:155], v20 offset:14336\n" \
"v_mfma_f32_16x16x32_bf16 v[36:39], v[124:127], a[28:31], v[36:39]\n" \
"ds_read_b128 a[156:159], v20 offset:15360\n" \
"s_waitcnt lgkmcnt(15)\n" \
"v_mfma_f32_16x16x32_bf16 v[40:43], v[124:127], a[44:47], v[40:43]\n" \
"ds_read_b128 a[64:67], v60 offset:16384\n" \
"s_mov_b32 s83, 0x20000\n" \
"v_mfma_f32_16x16x32_bf16 v[44:47], v[124:127], a[60:63], v[44:47]\n" \
"ds_read_b128 a[68:71], v60 offset:17408\n" \
"s_mov_b32 s100, 0x71800000\n" \
"v_mfma_f32_16x16x32_bf16 v[64:67], a[160:163], a[0:3], v[32:35]\n" \
"ds_read_b128 a[72:75], v60 offset:18432\n" \
"s_waitcnt lgkmcnt(15)\n" \
"v_mfma_f32_16x16x32_bf16 v[64:67], a[164:167], a[4:7], v[64:67]\n" \
"ds_read_b128 a[76:79], v60 offset:19456\n" \
"s_mov_b32 s63, s23\n" \
"v_mfma_f32_16x16x32_bf16 v[64:67], a[168:171], a[8:11], v[64:67]\n" \
"ds_read_b128 a[80:83], v60 offset:24576\n" \
"v_mov_b32_e32 v62, 0\n" \
"v_mfma_f32_16x16x32_bf16 v[64:67], a[172:175], a[12:15], v[64:67]\n" \
"ds_read_b128 a[84:87], v60 offset:25600\n" \
"s_waitcnt lgkmcnt(15)\n" \
"v_mfma_f32_16x16x32_bf16 v[68:71], a[188:191], a[12:15], v[32:35]\n" \
"ds_read_b128 a[88:91], v60 offset:26624\n" \
"v_accvgpr_write_b32 a212, v54\n" \
"v_mfma_f32_16x16x32_bf16 v[68:71], a[184:187], a[8:11], v[68:71]\n" \
"ds_read_b128 a[92:95], v60 offset:27648\n" \
"v_accvgpr_write_b32 a208, 0\n" \
"v_mfma_f32_16x16x32_bf16 v[68:71], a[180:183], a[4:7], v[68:71]\n" \
"ds_read_b128 a[96:99], v60 offset:0\n" \
"s_waitcnt lgkmcnt(15)\n" \
"v_mfma_f32_16x16x32_bf16 v[68:71], a[176:179], a[0:3], v[68:71]\n" \
"ds_read_b128 a[100:103], v60 offset:1024\n" \
"v_accvgpr_write_b32 a213, v54\n" \
"v_mfma_f32_16x16x32_bf16 v[72:75], a[160:163], a[16:19], v[36:39]\n" \
"ds_read_b128 a[104:107], v60 offset:2048\n" \
"v_accvgpr_write_b32 a209, 0\n" \
"v_mfma_f32_16x16x32_bf16 v[72:75], a[164:167], a[20:23], v[72:75]\n" \
"ds_read_b128 a[108:111], v60 offset:3072\n" \
"v_accvgpr_write_b32 a214, v54\n" \
"L_pf10:\n" \
"s_cbranch_execz L_pf14\n" \
"s_nop 0\n" \
"v_mfma_f32_16x16x32_bf16 v[72:75], a[168:171], a[24:27], v[72:75]\n" \
"ds_read_b128 a[112:115], v60 offset:8192\n" \
"v_accvgpr_write_b32 a210, 0\n" \
"v_mfma_f32_16x16x32_bf16 v[72:75], a[172:175], a[28:31], v[72:75]\n" \
"ds_read_b128 a[116:119], v60 offset:9216\n" \
"v_accvgpr_write_b32 a215, v54\n" \
"v_mfma_f32_16x16x32_bf16 v[76:79], a[188:191], a[28:31], v[36:39]\n" \
"ds_read_b128 a[120:123], v60 offset:10240\n" \
"v_accvgpr_write_b32 a211, 0\n" \
"v_mfma_f32_16x16x32_bf16 v[76:79], a[184:187], a[24:27], v[76:79]\n" \
"ds_read_b128 a[124:127], v60 offset:11264\n" \
"v_or_b32_e32 v21, 0x44444444, v21\n" \
"v_mfma_f32_16x16x32_bf16 v[76:79], a[180:183], a[20:23], v[76:79]\n" \
"v_exp_f32_e32 v64, v64\n" \
"v_mfma_f32_16x16x32_bf16 v[76:79], a[176:179], a[16:19], v[76:79]\n" \
"v_exp_f32_e32 v65, v65\n" \
"v_mfma_f32_16x16x32_bf16 v[80:83], a[160:163], a[32:35], v[40:43]\n" \
"v_exp_f32_e32 v66, v66\n" \
"v_mfma_f32_16x16x32_bf16 v[80:83], a[164:167], a[36:39], v[80:83]\n" \
"v_exp_f32_e32 v67, v67\n" \
"v_mfma_f32_16x16x32_bf16 v[80:83], a[168:171], a[40:43], v[80:83]\n" \
"v_exp_f32_e32 v68, v68\n" \
"v_mfma_f32_16x16x32_bf16 v[80:83], a[172:175], a[44:47], v[80:83]\n" \
"v_exp_f32_e32 v69, v69\n" \
"v_mfma_f32_16x16x32_bf16 v[84:87], a[188:191], a[44:47], v[40:43]\n" \
"v_exp_f32_e32 v70, v70\n" \
"v_mfma_f32_16x16x32_bf16 v[84:87], a[184:187], a[40:43], v[84:87]\n" \
"v_exp_f32_e32 v71, v71\n" \
"v_mfma_f32_16x16x32_bf16 v[84:87], a[180:183], a[36:39], v[84:87]\n" \
"v_exp_f32_e32 v72, v72\n" \
"v_mfma_f32_16x16x32_bf16 v[84:87], a[176:179], a[32:35], v[84:87]\n" \
"v_exp_f32_e32 v73, v73\n" \
"v_mfma_f32_16x16x32_bf16 v[88:91], a[160:163], a[48:51], v[44:47]\n" \
"v_exp_f32_e32 v74, v74\n" \
"v_mfma_f32_16x16x32_bf16 v[88:91], a[164:167], a[52:55], v[88:91]\n" \
"v_exp_f32_e32 v75, v75\n" \
"v_mfma_f32_16x16x32_bf16 v[88:91], a[168:171], a[56:59], v[88:91]\n" \
"v_exp_f32_e32 v76, v76\n" \
"v_mfma_f32_16x16x32_bf16 v[88:91], a[172:175], a[60:63], v[88:91]\n" \
"v_exp_f32_e32 v77, v77\n" \
"v_mfma_f32_16x16x32_bf16 v[92:95], a[188:191], a[60:63], v[44:47]\n" \
"v_exp_f32_e32 v78, v78\n" \
"v_mfma_f32_16x16x32_bf16 v[92:95], a[184:187], a[56:59], v[92:95]\n" \
"v_exp_f32_e32 v79, v79\n" \
"v_mfma_f32_16x16x32_bf16 v[92:95], a[180:183], a[52:55], v[92:95]\n" \
"v_exp_f32_e32 v80, v80\n" \
"v_mfma_f32_16x16x32_bf16 v[92:95], a[176:179], a[48:51], v[92:95]\n" \
"v_exp_f32_e32 v81, v81\n" \
"v_exp_f32_e32 v82, v82\n" \
"v_exp_f32_e32 v83, v83\n" \
"v_exp_f32_e32 v84, v84\n" \
"v_exp_f32_e32 v85, v85\n" \
"v_exp_f32_e32 v86, v86\n" \
"v_exp_f32_e32 v87, v87\n" \
"v_accvgpr_write_b32 a252, v57\n" \
"v_accvgpr_write_b32 a253, v57\n" \
"v_accvgpr_write_b32 a254, v57\n" \
"v_accvgpr_write_b32 a255, v57\n" \
"s_mov_b32 s64, 0x8000\n" \
"v_add_u32_e32 v50, 0x6000, v55\n" \
"v_add_u32_e32 v51, 0xa000, v55\n" \
"v_add_u32_e32 v53, 0x10000, v55\n" \
"v_add_u32_e32 v48, 0xc000, v20\n" \
"v_add_u32_e32 v49, s17, v20\n" \
"v_accvgpr_write_b32 a224, v116\n" \
"v_accvgpr_write_b32 a225, v117\n" \
"v_accvgpr_write_b32 a226, v118\n" \
"v_accvgpr_write_b32 a227, v119\n" \
"v_accvgpr_write_b32 a228, v120\n" \
"v_accvgpr_write_b32 a229, v121\n" \
"v_accvgpr_write_b32 a230, v122\n" \
"v_accvgpr_write_b32 a231, v123\n" \
"v_accvgpr_write_b32 a232, v124\n" \
"v_accvgpr_write_b32 a233, v125\n" \
"v_accvgpr_write_b32 a234, v126\n" \
"v_accvgpr_write_b32 a235, v127\n" \
"v_exp_f32_e32 v88, v88\n" \
"v_exp_f32_e32 v89, v89\n" \
"v_exp_f32_e32 v90, v90\n" \
"v_exp_f32_e32 v91, v91\n" \
"v_cvt_pk_bf16_f32 v96, v64, v65\n" \
"v_cvt_pk_bf16_f32 v97, v66, v67\n" \
"v_cvt_pk_bf16_f32 v98, v68, v69\n" \
"v_cvt_pk_bf16_f32 v99, v70, v71\n" \
"v_cvt_pk_bf16_f32 v104, v72, v73\n" \
"v_cvt_pk_bf16_f32 v105, v74, v75\n" \
"v_cvt_pk_bf16_f32 v106, v76, v77\n" \
"v_cvt_pk_bf16_f32 v107, v78, v79\n" \
"v_cvt_pk_bf16_f32 v112, v80, v81\n" \
"v_cvt_pk_bf16_f32 v113, v82, v83\n" \
"v_cvt_pk_bf16_f32 v114, v84, v85\n" \
"v_cvt_pk_bf16_f32 v115, v86, v87\n" \
"s_sub_u32 s66, s18, 1\n" \
"s_waitcnt vmcnt(2) lgkmcnt(0)\n" \
"s_barrier\n" \
"s_mov_b64 exec, 1\n" \
"buffer_store_dword v56, off, s[56:59], s68 offset:0 sc1\n" \
"buffer_store_dword v56, off, s[56:59], s22 sc1\n" \
"s_mov_b64 exec, -1\n" \
"s_cbranch_scc1 L_tr_%=\n" \
"s_branch L_1_%=\n" \
"L_t1_%=:\n" \
"v_mfma_f32_16x16x32_bf16 v[252:255], a[188:191], v[124:127], v[252:255]\n" \
"v_mfma_f32_16x16x32_bf16 v[252:255], a[172:175], v[120:123], v[252:255]\n" \
"v_smfmac_f32_16x16x64_bf16 a[204:207], a[212:215], v[120:127], v21\n" \
"s_waitcnt lgkmcnt(0)\n" \
"L_1_%=:\n" \
"v_mfma_f32_16x16x32_bf16 v[64:67], a[128:131], a[0:3], v[32:35]\n" \
"ds_read_b128 a[160:163], v49 offset:0\n" \
"ds_read_b128 a[176:179], v49 offset:4096\n" \
"v_mfma_f32_16x16x32_bf16 v[64:67], a[132:135], a[4:7], v[64:67]\n" \
"v_cvt_pk_bf16_f32 v120, v88, v89\n" \
"v_cvt_pk_bf16_f32 v121, v90, v91\n" \
"v_mfma_f32_16x16x32_bf16 v[64:67], a[136:139], a[8:11], v[64:67]\n" \
"ds_read_b128 a[180:183], v49 offset:5120\n" \
"ds_read_b128 a[164:167], v49 offset:1024\n" \
"v_mfma_f32_16x16x32_bf16 v[64:67], a[140:143], a[12:15], v[64:67]\n" \
"v_exp_f32_e32 v92, v92\n" \
"v_mfma_f32_16x16x32_bf16 v[68:71], a[156:159], a[12:15], v[32:35]\n" \
"ds_read_b128 a[168:171], v49 offset:2048\n" \
"ds_read_b128 a[184:187], v49 offset:6144\n" \
"v_mfma_f32_16x16x32_bf16 v[68:71], a[152:155], a[8:11], v[68:71]\n" \
"v_exp_f32_e32 v93, v93\n" \
"v_mfma_f32_16x16x32_bf16 v[68:71], a[148:151], a[4:7], v[68:71]\n" \
"ds_read_b128 a[188:191], v49 offset:7168\n" \
"ds_read_b128 a[172:175], v49 offset:3072\n" \
"v_mfma_f32_16x16x32_bf16 v[68:71], a[144:147], a[0:3], v[68:71]\n" \
"v_exp_f32_e32 v94, v94\n" \
"v_mfma_f32_16x16x32_bf16 v[72:75], a[128:131], a[16:19], v[36:39]\n" \
"v_exp_f32_e32 v95, v95\n" \
"v_mfma_f32_16x16x32_bf16 v[72:75], a[132:135], a[20:23], v[72:75]\n" \
"v_cvt_pk_bf16_f32 v122, v92, v93\n" \
"v_cvt_pk_bf16_f32 v123, v94, v95\n" \
"v_mfma_f32_16x16x32_bf16 v[72:75], a[136:139], a[24:27], v[72:75]\n" \
"v_exp_f32_e32 v64, v64\n" \
"v_mfma_f32_16x16x32_bf16 v[72:75], a[140:143], a[28:31], v[72:75]\n" \
"v_exp_f32_e32 v65, v65\n" \
"v_mfma_f32_16x16x32_bf16 v[76:79], a[156:159], a[28:31], v[36:39]\n" \
"v_exp_f32_e32 v66, v66\n" \
"v_mfma_f32_16x16x32_bf16 v[76:79], a[152:155], a[24:27], v[76:79]\n" \
"v_exp_f32_e32 v67, v67\n" \
"v_mfma_f32_16x16x32_bf16 v[76:79], a[148:151], a[20:23], v[76:79]\n" \
"v_exp_f32_e32 v68, v68\n" \
"v_mfma_f32_16x16x32_bf16 v[76:79], a[144:147], a[16:19], v[76:79]\n" \
"v_exp_f32_e32 v69, v69\n" \
"v_mfma_f32_16x16x32_bf16 v[80:83], a[128:131], a[32:35], v[40:43]\n" \
"v_exp_f32_e32 v70, v70\n" \
"v_mfma_f32_16x16x32_bf16 v[80:83], a[132:135], a[36:39], v[80:83]\n" \
"v_exp_f32_e32 v71, v71\n" \
"v_mfma_f32_16x16x32_bf16 v[80:83], a[136:139], a[40:43], v[80:83]\n" \
"v_cvt_pk_bf16_f32 v100, v64, v65\n" \
"v_cvt_pk_bf16_f32 v101, v66, v67\n" \
"v_mfma_f32_16x16x32_bf16 v[80:83], a[140:143], a[44:47], v[80:83]\n" \
"v_cvt_pk_bf16_f32 v102, v68, v69\n" \
"v_cvt_pk_bf16_f32 v103, v70, v71\n" \
"v_mfma_f32_16x16x32_bf16 v[84:87], a[156:159], a[44:47], v[40:43]\n" \
"v_exp_f32_e32 v72, v72\n" \
"v_mfma_f32_16x16x32_bf16 v[84:87], a[152:155], a[40:43], v[84:87]\n" \
"v_exp_f32_e32 v73, v73\n" \
"v_mfma_f32_16x16x32_bf16 v[84:87], a[148:151], a[36:39], v[84:87]\n" \
"v_exp_f32_e32 v74, v74\n" \
"v_mfma_f32_16x16x32_bf16 v[84:87], a[144:147], a[32:35], v[84:87]\n" \
"v_exp_f32_e32 v75, v75\n" \
"v_mfma_f32_16x16x32_bf16 v[88:91], a[128:131], a[48:51], v[44:47]\n" \
"v_exp_f32_e32 v76, v76\n" \
"v_mfma_f32_16x16x32_bf16 v[88:91], a[132:135], a[52:55], v[88:91]\n" \
"v_exp_f32_e32 v77, v77\n" \
"v_mfma_f32_16x16x32_bf16 v[88:91], a[136:139], a[56:59], v[88:91]\n" \
"v_exp_f32_e32 v78, v78\n" \
"v_mfma_f32_16x16x32_bf16 v[88:91], a[140:143], a[60:63], v[88:91]\n" \
"v_exp_f32_e32 v79, v79\n" \
"v_mfma_f32_16x16x32_bf16 v[92:95], a[156:159], a[60:63], v[44:47]\n" \
"v_cvt_pk_bf16_f32 v108, v72, v73\n" \
"v_cvt_pk_bf16_f32 v109, v74, v75\n" \
"v_mfma_f32_16x16x32_bf16 v[92:95], a[152:155], a[56:59], v[92:95]\n" \
"v_cvt_pk_bf16_f32 v110, v76, v77\n" \
"v_cvt_pk_bf16_f32 v111, v78, v79\n" \
"v_mfma_f32_16x16x32_bf16 v[92:95], a[148:151], a[52:55], v[92:95]\n" \
"ds_read_b128 a[128:131], v48 offset:0\n" \
"ds_read_b128 a[132:135], v48 offset:1024\n" \
"v_mfma_f32_16x16x32_bf16 v[92:95], a[144:147], a[48:51], v[92:95]\n" \
"s_add_u32 s63, s63, s63\n" \
"s_waitcnt lgkmcnt(2)\n" \
"v_mfma_f32_16x16x32_bf16 v[128:131], a[160:163], v[96:99], v[128:131]\n" \
"ds_read_b128 a[136:139], v48 offset:2048\n" \
"ds_read_b128 a[140:143], v48 offset:3072\n" \
"v_mfma_f32_16x16x32_bf16 v[128:131], a[176:179], v[100:103], v[128:131]\n" \
"v_exp_f32_e32 v80, v80\n" \
"v_mfma_f32_16x16x32_bf16 v[132:135], a[180:183], v[100:103], v[132:135]\n" \
"ds_read_b128 a[144:147], v48 offset:4096\n" \
"ds_read_b128 a[148:151], v48 offset:5120\n" \
"v_mfma_f32_16x16x32_bf16 v[132:135], a[164:167], v[96:99], v[132:135]\n" \
"v_exp_f32_e32 v81, v81\n" \
"v_mfma_f32_16x16x32_bf16 v[136:139], a[168:171], v[96:99], v[136:139]\n" \
"ds_read_b128 a[152:155], v48 offset:6144\n" \
"ds_read_b128 a[156:159], v48 offset:7168\n" \
"v_mfma_f32_16x16x32_bf16 v[136:139], a[184:187], v[100:103], v[136:139]\n" \
"v_exp_f32_e32 v82, v82\n" \
"v_mfma_f32_16x16x32_bf16 v[140:143], a[188:191], v[100:103], v[140:143]\n" \
"v_exp_f32_e32 v83, v83\n" \
"v_mfma_f32_16x16x32_bf16 v[140:143], a[172:175], v[96:99], v[140:143]\n" \
"v_exp_f32_e32 v84, v84\n" \
"v_smfmac_f32_16x16x64_bf16 a[192:195], a[212:215], v[96:103], v21\n" \
"v_mfma_f32_16x16x32_bf16 v[160:163], a[160:163], v[104:107], v[160:163]\n" \
"v_exp_f32_e32 v85, v85\n" \
"v_mfma_f32_16x16x32_bf16 v[160:163], a[176:179], v[108:111], v[160:163]\n" \
"v_exp_f32_e32 v86, v86\n" \
"v_mfma_f32_16x16x32_bf16 v[164:167], a[180:183], v[108:111], v[164:167]\n" \
"v_exp_f32_e32 v87, v87\n" \
"v_mfma_f32_16x16x32_bf16 v[164:167], a[164:167], v[104:107], v[164:167]\n" \
"v_cvt_pk_bf16_f32 v116, v80, v81\n" \
"v_cvt_pk_bf16_f32 v117, v82, v83\n" \
"v_mfma_f32_16x16x32_bf16 v[168:171], a[168:171], v[104:107], v[168:171]\n" \
"v_cvt_pk_bf16_f32 v118, v84, v85\n" \
"v_cvt_pk_bf16_f32 v119, v86, v87\n" \
"v_mfma_f32_16x16x32_bf16 v[168:171], a[184:187], v[108:111], v[168:171]\n" \
"v_exp_f32_e32 v88, v88\n" \
"v_mfma_f32_16x16x32_bf16 v[172:175], a[188:191], v[108:111], v[172:175]\n" \
"v_exp_f32_e32 v89, v89\n" \
"v_mfma_f32_16x16x32_bf16 v[172:175], a[172:175], v[104:107], v[172:175]\n" \
"v_exp_f32_e32 v90, v90\n" \
"v_smfmac_f32_16x16x64_bf16 a[196:199], a[212:215], v[104:111], v21\n" \
"v_mfma_f32_16x16x32_bf16 v[192:195], a[160:163], v[112:115], v[192:195]\n" \
"v_exp_f32_e32 v91, v91\n" \
"v_mfma_f32_16x16x32_bf16 v[192:195], a[176:179], v[116:119], v[192:195]\n" \
"v_exp_f32_e32 v92, v92\n" \
"v_mfma_f32_16x16x32_bf16 v[196:199], a[180:183], v[116:119], v[196:199]\n" \
"v_exp_f32_e32 v93, v93\n" \
"v_mfma_f32_16x16x32_bf16 v[196:199], a[164:167], v[112:115], v[196:199]\n" \
"v_exp_f32_e32 v94, v94\n" \
"v_mfma_f32_16x16x32_bf16 v[200:203], a[168:171], v[112:115], v[200:203]\n" \
"v_exp_f32_e32 v95, v95\n" \
"v_mfma_f32_16x16x32_bf16 v[200:203], a[184:187], v[116:119], v[200:203]\n" \
"v_cvt_pk_bf16_f32 v124, v88, v89\n" \
"v_cvt_pk_bf16_f32 v125, v90, v91\n" \
"v_mfma_f32_16x16x32_bf16 v[204:207], a[188:191], v[116:119], v[204:207]\n" \
"v_cvt_pk_bf16_f32 v126, v92, v93\n" \
"v_cvt_pk_bf16_f32 v127, v94, v95\n" \
"v_mfma_f32_16x16x32_bf16 v[204:207], a[172:175], v[112:115], v[204:207]\n" \
"v_smfmac_f32_16x16x64_bf16 a[200:203], a[212:215], v[112:119], v21\n" \
"s_waitcnt vmcnt(0) lgkmcnt(0)\n" \
"s_barrier\n" \
"v_mfma_f32_16x16x32_bf16 v[224:227], a[160:163], v[120:123], v[224:227]\n" \
"s_cbranch_scc1 L_h1_%=\n" \
"L_hr1_%=:\n" \
"v_mfma_f32_16x16x32_bf16 v[224:227], a[176:179], v[124:127], v[224:227]\n" \
"s_xor_b32 m0, s65, 0x12000\n" \
"ds_read_b128 a[160:163], v49 offset:8192\n" \
"v_mfma_f32_16x16x32_bf16 v[228:231], a[180:183], v[124:127], v[228:231]\n" \
"buffer_load_dwordx4 v50, s[8:11], s64 offen offset:0 sc1 lds\n" \
"v_mfma_f32_16x16x32_bf16 v[228:231], a[164:167], v[120:123], v[228:231]\n" \
"ds_read_b128 a[176:179], v49 offset:12288\n" \
"ds_read_b128 a[180:183], v49 offset:13312\n" \
"v_mfma_f32_16x16x32_bf16 v[232:235], a[168:171], v[120:123], v[232:235]\n" \
"v_mfma_f32_16x16x32_bf16 v[232:235], a[184:187], v[124:127], v[232:235]\n" \
"ds_read_b128 a[164:167], v49 offset:9216\n" \
"ds_read_b128 a[168:171], v49 offset:10240\n" \
"v_mfma_f32_16x16x32_bf16 v[236:239], a[188:191], v[124:127], v[236:239]\n" \
"v_mfma_f32_16x16x32_bf16 v[236:239], a[172:175], v[120:123], v[236:239]\n" \
"ds_read_b128 a[184:187], v49 offset:14336\n" \
"ds_read_b128 a[188:191], v49 offset:15360\n" \
"v_mfma_f32_16x16x32_bf16 v[64:67], a[128:131], a[0:3], v[32:35]\n" \
"v_mfma_f32_16x16x32_bf16 v[64:67], a[132:135], a[4:7], v[64:67]\n" \
"buffer_load_dwordx4 v50, s[8:11], s64 offen offset:1024 sc1 lds\n" \
"v_mfma_f32_16x16x32_bf16 v[64:67], a[136:139], a[8:11], v[64:67]\n" \
"s_xor_b32 m0, s65, 0x6000\n" \
"ds_read_b128 a[172:175], v49 offset:11264\n" \
"v_mfma_f32_16x16x32_bf16 v[64:67], a[140:143], a[12:15], v[64:67]\n" \
"v_add_u32_e32 v49, 0x10000, v48\n" \
"v_mfma_f32_16x16x32_bf16 v[68:71], a[156:159], a[12:15], v[32:35]\n" \
"v_mfma_f32_16x16x32_bf16 v[68:71], a[152:155], a[8:11], v[68:71]\n" \
"v_mfma_f32_16x16x32_bf16 v[68:71], a[148:151], a[4:7], v[68:71]\n" \
"v_mfma_f32_16x16x32_bf16 v[68:71], a[144:147], a[0:3], v[68:71]\n" \
"v_mfma_f32_16x16x32_bf16 v[72:75], a[128:131], a[16:19], v[36:39]\n" \
"v_mfma_f32_16x16x32_bf16 v[72:75], a[132:135], a[20:23], v[72:75]\n" \
"v_mfma_f32_16x16x32_bf16 v[72:75], a[136:139], a[24:27], v[72:75]\n" \
"buffer_load_dwordx4 v51, s[8:11], s64 offen offset:0 sc1 lds\n" \
"v_mfma_f32_16x16x32_bf16 v[72:75], a[140:143], a[28:31], v[72:75]\n" \
"v_exp_f32_e32 v64, v64\n" \
"v_mfma_f32_16x16x32_bf16 v[76:79], a[156:159], a[28:31], v[36:39]\n" \
"v_exp_f32_e32 v65, v65\n" \
"v_mfma_f32_16x16x32_bf16 v[76:79], a[152:155], a[24:27], v[76:79]\n" \
"v_exp_f32_e32 v66, v66\n" \
"v_mfma_f32_16x16x32_bf16 v[76:79], a[148:151], a[20:23], v[76:79]\n" \
"v_exp_f32_e32 v67, v67\n" \
"v_mfma_f32_16x16x32_bf16 v[76:79], a[144:147], a[16:19], v[76:79]\n" \
"v_exp_f32_e32 v68, v68\n" \
"v_mfma_f32_16x16x32_bf16 v[80:83], a[128:131], a[32:35], v[40:43]\n" \
"v_exp_f32_e32 v69, v69\n" \
"v_mfma_f32_16x16x32_bf16 v[80:83], a[132:135], a[36:39], v[80:83]\n" \
"v_exp_f32_e32 v70, v70\n" \
"v_mfma_f32_16x16x32_bf16 v[80:83], a[136:139], a[40:43], v[80:83]\n" \
"v_exp_f32_e32 v71, v71\n" \
"v_mfma_f32_16x16x32_bf16 v[80:83], a[140:143], a[44:47], v[80:83]\n" \
"buffer_load_dwordx4 v51, s[8:11], s64 offen offset:1024 sc1 lds\n" \
"v_mfma_f32_16x16x32_bf16 v[84:87], a[156:159], a[44:47], v[40:43]\n" \
"v_exp_f32_e32 v72, v72\n" \
"v_mfma_f32_16x16x32_bf16 v[84:87], a[152:155], a[40:43], v[84:87]\n" \
"v_exp_f32_e32 v73, v73\n" \
"v_mfma_f32_16x16x32_bf16 v[84:87], a[148:151], a[36:39], v[84:87]\n" \
"v_exp_f32_e32 v74, v74\n" \
"v_mfma_f32_16x16x32_bf16 v[84:87], a[144:147], a[32:35], v[84:87]\n" \
"v_exp_f32_e32 v75, v75\n" \
"v_mfma_f32_16x16x32_bf16 v[88:91], a[128:131], a[48:51], v[44:47]\n" \
"s_xor_b32 m0, s65, 0x14000\n" \
"ds_read_b128 a[128:131], v48 offset:8192\n" \
"v_mfma_f32_16x16x32_bf16 v[88:91], a[132:135], a[52:55], v[88:91]\n" \
"v_exp_f32_e32 v76, v76\n" \
"v_mfma_f32_16x16x32_bf16 v[88:91], a[136:139], a[56:59], v[88:91]\n" \
"ds_read_b128 a[132:135], v48 offset:9216\n" \
"ds_read_b128 a[136:139], v48 offset:10240\n" \
"v_mfma_f32_16x16x32_bf16 v[88:91], a[140:143], a[60:63], v[88:91]\n" \
"v_exp_f32_e32 v77, v77\n" \
"v_mfma_f32_16x16x32_bf16 v[92:95], a[156:159], a[60:63], v[44:47]\n" \
"buffer_load_dwordx4 v52, s[8:11], s64 offen offset:0 sc1 lds\n" \
"v_mfma_f32_16x16x32_bf16 v[92:95], a[152:155], a[56:59], v[92:95]\n" \
"ds_read_b128 a[140:143], v48 offset:11264\n" \
"ds_read_b128 a[152:155], v48 offset:14336\n" \
"v_mfma_f32_16x16x32_bf16 v[92:95], a[148:151], a[52:55], v[92:95]\n" \
"v_exp_f32_e32 v78, v78\n" \
"v_mfma_f32_16x16x32_bf16 v[92:95], a[144:147], a[48:51], v[92:95]\n" \
"ds_read_b128 a[144:147], v48 offset:12288\n" \
"s_waitcnt lgkmcnt(6)\n" \
"v_mfma_f32_16x16x32_bf16 v[144:147], a[160:163], v[96:99], v[144:147]\n" \
"ds_read_b128 a[148:151], v48 offset:13312\n" \
"ds_read_b128 a[156:159], v48 offset:15360\n" \
"v_mfma_f32_16x16x32_bf16 v[144:147], a[176:179], v[100:103], v[144:147]\n" \
"v_exp_f32_e32 v79, v79\n" \
"v_mfma_f32_16x16x32_bf16 v[148:151], a[180:183], v[100:103], v[148:151]\n" \
"v_exp_f32_e32 v80, v80\n" \
"v_mfma_f32_16x16x32_bf16 v[148:151], a[164:167], v[96:99], v[148:151]\n" \
"v_exp_f32_e32 v81, v81\n" \
"v_mfma_f32_16x16x32_bf16 v[152:155], a[168:171], v[96:99], v[152:155]\n" \
"buffer_load_dwordx4 v52, s[8:11], s64 offen offset:1024 sc1 lds\n" \
"v_mfma_f32_16x16x32_bf16 v[152:155], a[184:187], v[100:103], v[152:155]\n" \
"s_mov_b32 m0, s65\n" \
"s_xor_b32 s65, s65, 0x4000\n" \
"v_mfma_f32_16x16x32_bf16 v[156:159], a[188:191], v[100:103], v[156:159]\n" \
"v_exp_f32_e32 v82, v82\n" \
"v_mfma_f32_16x16x32_bf16 v[156:159], a[172:175], v[96:99], v[156:159]\n" \
"v_exp_f32_e32 v83, v83\n" \
"v_mfma_f32_16x16x32_bf16 v[176:179], a[160:163], v[104:107], v[176:179]\n" \
"v_exp_f32_e32 v84, v84\n" \
"v_mfma_f32_16x16x32_bf16 v[176:179], a[176:179], v[108:111], v[176:179]\n" \
"v_exp_f32_e32 v85, v85\n" \
"v_mfma_f32_16x16x32_bf16 v[180:183], a[180:183], v[108:111], v[180:183]\n" \
"v_exp_f32_e32 v86, v86\n" \
"v_mfma_f32_16x16x32_bf16 v[180:183], a[164:167], v[104:107], v[180:183]\n" \
"v_exp_f32_e32 v87, v87\n" \
"v_mfma_f32_16x16x32_bf16 v[184:187], a[168:171], v[104:107], v[184:187]\n" \
"v_exp_f32_e32 v88, v88\n" \
"v_mfma_f32_16x16x32_bf16 v[184:187], a[184:187], v[108:111], v[184:187]\n" \
"buffer_load_dwordx4 v53, s[8:11], s64 offen offset:0 sc1 lds\n" \
"v_mfma_f32_16x16x32_bf16 v[188:191], a[188:191], v[108:111], v[188:191]\n" \
"v_exp_f32_e32 v89, v89\n" \
"v_mfma_f32_16x16x32_bf16 v[188:191], a[172:175], v[104:107], v[188:191]\n" \
"v_exp_f32_e32 v90, v90\n" \
"v_mfma_f32_16x16x32_bf16 v[208:211], a[160:163], v[112:115], v[208:211]\n" \
"v_exp_f32_e32 v91, v91\n" \
"v_mfma_f32_16x16x32_bf16 v[208:211], a[176:179], v[116:119], v[208:211]\n" \
"v_cvt_pk_bf16_f32 v96, v64, v65\n" \
"v_cvt_pk_bf16_f32 v97, v66, v67\n" \
"v_mfma_f32_16x16x32_bf16 v[212:215], a[180:183], v[116:119], v[212:215]\n" \
"v_cvt_pk_bf16_f32 v98, v68, v69\n" \
"v_cvt_pk_bf16_f32 v99, v70, v71\n" \
"v_mfma_f32_16x16x32_bf16 v[212:215], a[164:167], v[112:115], v[212:215]\n" \
"v_cvt_pk_bf16_f32 v104, v72, v73\n" \
"v_cvt_pk_bf16_f32 v105, v74, v75\n" \
"v_mfma_f32_16x16x32_bf16 v[216:219], a[168:171], v[112:115], v[216:219]\n" \
"v_cvt_pk_bf16_f32 v106, v76, v77\n" \
"v_cvt_pk_bf16_f32 v107, v78, v79\n" \
"v_mfma_f32_16x16x32_bf16 v[216:219], a[184:187], v[116:119], v[216:219]\n" \
"v_xor_b32_e32 v48, 0x4000, v48\n" \
"v_mfma_f32_16x16x32_bf16 v[220:223], a[188:191], v[116:119], v[220:223]\n" \
"buffer_load_dwordx4 v53, s[8:11], s64 offen offset:1024 sc1 lds\n" \
"v_mfma_f32_16x16x32_bf16 v[220:223], a[172:175], v[112:115], v[220:223]\n" \
"s_add_u32 s64, s64, 0x8000\n" \
"s_sub_u32 s66, s66, 1\n" \
"v_mfma_f32_16x16x32_bf16 v[240:243], a[160:163], v[120:123], v[240:243]\n" \
"v_cvt_pk_bf16_f32 v112, v80, v81\n" \
"v_cvt_pk_bf16_f32 v113, v82, v83\n" \
"v_mfma_f32_16x16x32_bf16 v[240:243], a[176:179], v[124:127], v[240:243]\n" \
"v_cvt_pk_bf16_f32 v114, v84, v85\n" \
"v_cvt_pk_bf16_f32 v115, v86, v87\n" \
"v_mfma_f32_16x16x32_bf16 v[244:247], a[180:183], v[124:127], v[244:247]\n" \
"v_mfma_f32_16x16x32_bf16 v[244:247], a[164:167], v[120:123], v[244:247]\n" \
"v_mfma_f32_16x16x32_bf16 v[248:251], a[168:171], v[120:123], v[248:251]\n" \
"v_mfma_f32_16x16x32_bf16 v[248:251], a[184:187], v[124:127], v[248:251]\n" \
"s_cbranch_scc0 L_t1_%=\n" \
"v_mfma_f32_16x16x32_bf16 v[252:255], a[188:191], v[124:127], v[252:255]\n" \
"v_mfma_f32_16x16x32_bf16 v[252:255], a[172:175], v[120:123], v[252:255]\n" \
"v_smfmac_f32_16x16x64_bf16 a[204:207], a[212:215], v[120:127], v21\n" \
"s_waitcnt lgkmcnt(0)\n" \
"s_barrier\n" \
"L_tr_%=:\n" \
"L_pf11:\n" \
"s_cbranch_execz L_pf15\n" \
"s_nop 0\n" \
"s_cmp_lg_u32 s16, 0\n" \
"s_cbranch_scc1 L_sw_%=\n" \
"v_mfma_f32_16x16x32_bf16 v[64:67], a[128:131], a[0:3], v[32:35]\n" \
"s_xor_b32 m0, s65, 0x12000\n" \
"ds_read_b128 a[160:163], v49 offset:0\n" \
"v_mfma_f32_16x16x32_bf16 v[64:67], a[132:135], a[4:7], v[64:67]\n" \
"buffer_load_dwordx4 v50, s[8:11], s64 offen offset:0 sc1 lds\n" \
"v_mfma_f32_16x16x32_bf16 v[64:67], a[136:139], a[8:11], v[64:67]\n" \
"ds_read_b128 a[176:179], v49 offset:4096\n" \
"ds_read_b128 a[180:183], v49 offset:5120\n" \
"v_mfma_f32_16x16x32_bf16 v[64:67], a[140:143], a[12:15], v[64:67]\n" \
"s_mov_b32 s65, s13\n" \
"v_add_u32_e32 v63, s15, v20\n" \
"v_mfma_f32_16x16x32_bf16 v[68:71], a[156:159], a[12:15], v[32:35]\n" \
"s_add_u32 s67, s14, 40960\n" \
"ds_read_b128 a[164:167], v49 offset:1024\n" \
"v_mfma_f32_16x16x32_bf16 v[68:71], a[152:155], a[8:11], v[68:71]\n" \
"ds_read_b128 a[168:171], v49 offset:2048\n" \
"ds_read_b128 a[184:187], v49 offset:6144\n" \
"v_mfma_f32_16x16x32_bf16 v[68:71], a[148:151], a[4:7], v[68:71]\n" \
"buffer_load_dwordx4 v50, s[8:11], s64 offen offset:1024 sc1 lds\n" \
"v_mfma_f32_16x16x32_bf16 v[68:71], a[144:147], a[0:3], v[68:71]\n" \
"v_add_u32_e32 v50, 0x6000, v63\n" \
"s_xor_b32 m0, s65, 0x8000\n" \
"v_mfma_f32_16x16x32_bf16 v[72:75], a[128:131], a[16:19], v[36:39]\n" \
"v_cvt_pk_bf16_f32 v120, v88, v89\n" \
"v_cvt_pk_bf16_f32 v121, v90, v91\n" \
"v_mfma_f32_16x16x32_bf16 v[72:75], a[132:135], a[20:23], v[72:75]\n" \
"ds_read_b128 a[188:191], v49 offset:7168\n" \
"ds_read_b128 a[172:175], v49 offset:3072\n" \
"v_mfma_f32_16x16x32_bf16 v[72:75], a[136:139], a[24:27], v[72:75]\n" \
"v_exp_f32_e32 v92, v92\n" \
"v_mfma_f32_16x16x32_bf16 v[72:75], a[140:143], a[28:31], v[72:75]\n" \
"v_exp_f32_e32 v93, v93\n" \
"v_mfma_f32_16x16x32_bf16 v[76:79], a[156:159], a[28:31], v[36:39]\n" \
"buffer_load_dwordx4 v50, s[8:11], s67 offen offset:0 sc1 lds\n" \
"v_mfma_f32_16x16x32_bf16 v[76:79], a[152:155], a[24:27], v[76:79]\n" \
"v_exp_f32_e32 v94, v94\n" \
"v_mfma_f32_16x16x32_bf16 v[76:79], a[148:151], a[20:23], v[76:79]\n" \
"v_exp_f32_e32 v95, v95\n" \
"v_mfma_f32_16x16x32_bf16 v[76:79], a[144:147], a[16:19], v[76:79]\n" \
"v_cvt_pk_bf16_f32 v122, v92, v93\n" \
"v_cvt_pk_bf16_f32 v123, v94, v95\n" \
"v_mfma_f32_16x16x32_bf16 v[80:83], a[128:131], a[32:35], v[40:43]\n" \
"v_exp_f32_e32 v64, v64\n" \
"v_mfma_f32_16x16x32_bf16 v[80:83], a[132:135], a[36:39], v[80:83]\n" \
"v_exp_f32_e32 v65, v65\n" \
"v_mfma_f32_16x16x32_bf16 v[80:83], a[136:139], a[40:43], v[80:83]\n" \
"buffer_load_dwordx4 v50, s[8:11], s67 offen offset:1024 sc1 lds\n" \
"v_mfma_f32_16x16x32_bf16 v[80:83], a[140:143], a[44:47], v[80:83]\n" \
"v_exp_f32_e32 v66, v66\n" \
"v_mfma_f32_16x16x32_bf16 v[84:87], a[156:159], a[44:47], v[40:43]\n" \
"v_exp_f32_e32 v67, v67\n" \
"v_mfma_f32_16x16x32_bf16 v[84:87], a[152:155], a[40:43], v[84:87]\n" \
"v_exp_f32_e32 v68, v68\n" \
"v_mfma_f32_16x16x32_bf16 v[84:87], a[148:151], a[36:39], v[84:87]\n" \
"v_exp_f32_e32 v69, v69\n" \
"v_mfma_f32_16x16x32_bf16 v[84:87], a[144:147], a[32:35], v[84:87]\n" \
"v_exp_f32_e32 v70, v70\n" \
"v_mfma_f32_16x16x32_bf16 v[88:91], a[128:131], a[48:51], v[44:47]\n" \
"v_exp_f32_e32 v71, v71\n" \
"v_mfma_f32_16x16x32_bf16 v[88:91], a[132:135], a[52:55], v[88:91]\n" \
"v_cvt_pk_bf16_f32 v100, v64, v65\n" \
"v_cvt_pk_bf16_f32 v101, v66, v67\n" \
"v_mfma_f32_16x16x32_bf16 v[88:91], a[136:139], a[56:59], v[88:91]\n" \
"v_cvt_pk_bf16_f32 v102, v68, v69\n" \
"v_cvt_pk_bf16_f32 v103, v70, v71\n" \
"v_mfma_f32_16x16x32_bf16 v[88:91], a[140:143], a[60:63], v[88:91]\n" \
"buffer_load_dwordx4 v50, s[8:11], s67 offen offset:2048 sc1 lds\n" \
"v_mfma_f32_16x16x32_bf16 v[92:95], a[156:159], a[60:63], v[44:47]\n" \
"v_exp_f32_e32 v72, v72\n" \
"v_mfma_f32_16x16x32_bf16 v[92:95], a[152:155], a[56:59], v[92:95]\n" \
"v_exp_f32_e32 v73, v73\n" \
"v_mfma_f32_16x16x32_bf16 v[92:95], a[148:151], a[52:55], v[92:95]\n" \
"v_exp_f32_e32 v74, v74\n" \
"v_mfma_f32_16x16x32_bf16 v[92:95], a[144:147], a[48:51], v[92:95]\n" \
"v_cvt_pk_bf16_f32 v108, v72, v73\n" \
"s_waitcnt lgkmcnt(0)\n" \
"v_mfma_f32_16x16x32_bf16 v[128:131], a[160:163], v[96:99], v[128:131]\n" \
"buffer_load_dwordx4 v50, s[8:11], s67 offen offset:3072 sc1 lds\n" \
"v_mfma_f32_16x16x32_bf16 v[128:131], a[176:179], v[100:103], v[128:131]\n" \
"v_exp_f32_e32 v75, v75\n" \
"v_mfma_f32_16x16x32_bf16 v[132:135], a[180:183], v[100:103], v[132:135]\n" \
"v_exp_f32_e32 v76, v76\n" \
"v_mfma_f32_16x16x32_bf16 v[132:135], a[164:167], v[96:99], v[132:135]\n" \
"v_exp_f32_e32 v77, v77\n" \
"v_mfma_f32_16x16x32_bf16 v[136:139], a[168:171], v[96:99], v[136:139]\n" \
"v_exp_f32_e32 v78, v78\n" \
"v_mfma_f32_16x16x32_bf16 v[136:139], a[184:187], v[100:103], v[136:139]\n" \
"v_exp_f32_e32 v79, v79\n" \
"v_mfma_f32_16x16x32_bf16 v[140:143], a[188:191], v[100:103], v[140:143]\n" \
"v_cvt_pk_bf16_f32 v109, v74, v75\n" \
"v_cvt_pk_bf16_f32 v110, v76, v77\n" \
"v_mfma_f32_16x16x32_bf16 v[140:143], a[172:175], v[96:99], v[140:143]\n" \
"v_cvt_pk_bf16_f32 v111, v78, v79\n" \
"s_mov_b32 s64, s14\n" \
"v_smfmac_f32_16x16x64_bf16 a[192:195], a[212:215], v[96:103], v21\n" \
"v_mfma_f32_16x16x32_bf16 v[160:163], a[160:163], v[104:107], v[160:163]\n" \
"v_add_u32_e32 v51, 0x12000, v63\n" \
"s_xor_b32 m0, s65, 0xa000\n" \
"v_mfma_f32_16x16x32_bf16 v[160:163], a[176:179], v[108:111], v[160:163]\n" \
"buffer_load_dwordx4 v51, s[8:11], s64 offen offset:0 sc1 lds\n" \
"v_mfma_f32_16x16x32_bf16 v[164:167], a[180:183], v[108:111], v[164:167]\n" \
"v_exp_f32_e32 v80, v80\n" \
"v_mfma_f32_16x16x32_bf16 v[164:167], a[164:167], v[104:107], v[164:167]\n" \
"v_exp_f32_e32 v81, v81\n" \
"v_mfma_f32_16x16x32_bf16 v[168:171], a[168:171], v[104:107], v[168:171]\n" \
"v_exp_f32_e32 v82, v82\n" \
"v_mfma_f32_16x16x32_bf16 v[168:171], a[184:187], v[108:111], v[168:171]\n" \
"v_exp_f32_e32 v83, v83\n" \
"v_mfma_f32_16x16x32_bf16 v[172:175], a[188:191], v[108:111], v[172:175]\n" \
"v_exp_f32_e32 v84, v84\n" \
"v_mfma_f32_16x16x32_bf16 v[172:175], a[172:175], v[104:107], v[172:175]\n" \
"v_exp_f32_e32 v85, v85\n" \
"v_smfmac_f32_16x16x64_bf16 a[196:199], a[212:215], v[104:111], v21\n" \
"v_mfma_f32_16x16x32_bf16 v[192:195], a[160:163], v[112:115], v[192:195]\n" \
"v_exp_f32_e32 v86, v86\n" \
"v_exp_f32_e32 v87, v87\n" \
"v_cvt_pk_bf16_f32 v116, v80, v81\n" \
"v_cvt_pk_bf16_f32 v117, v82, v83\n" \
"v_cvt_pk_bf16_f32 v118, v84, v85\n" \
"v_cvt_pk_bf16_f32 v119, v86, v87\n" \
"v_exp_f32_e32 v88, v88\n" \
"v_exp_f32_e32 v89, v89\n" \
"v_mfma_f32_16x16x32_bf16 v[192:195], a[176:179], v[116:119], v[192:195]\n" \
"buffer_load_dwordx4 v51, s[8:11], s64 offen offset:1024 sc1 lds\n" \
"v_mfma_f32_16x16x32_bf16 v[196:199], a[180:183], v[116:119], v[196:199]\n" \
"ds_read_b128 a[128:131], v48 offset:0\n" \
"ds_read_b128 a[132:135], v48 offset:1024\n" \
"v_mfma_f32_16x16x32_bf16 v[196:199], a[164:167], v[112:115], v[196:199]\n" \
"ds_read_b128 a[136:139], v48 offset:2048\n" \
"ds_read_b128 a[140:143], v48 offset:3072\n" \
"ds_read_b128 a[144:147], v48 offset:4096\n" \
"ds_read_b128 a[148:151], v48 offset:5120\n" \
"ds_read_b128 a[152:155], v48 offset:6144\n" \
"ds_read_b128 a[156:159], v48 offset:7168\n" \
"v_mfma_f32_16x16x32_bf16 v[200:203], a[168:171], v[112:115], v[200:203]\n" \
"v_exp_f32_e32 v90, v90\n" \
"v_mfma_f32_16x16x32_bf16 v[200:203], a[184:187], v[116:119], v[200:203]\n" \
"v_exp_f32_e32 v91, v91\n" \
"v_mfma_f32_16x16x32_bf16 v[204:207], a[188:191], v[116:119], v[204:207]\n" \
"v_exp_f32_e32 v92, v92\n" \
"v_mfma_f32_16x16x32_bf16 v[204:207], a[172:175], v[112:115], v[204:207]\n" \
"v_exp_f32_e32 v93, v93\n" \
"v_smfmac_f32_16x16x64_bf16 a[200:203], a[212:215], v[112:119], v21\n" \
"v_mfma_f32_16x16x32_bf16 v[224:227], a[160:163], v[120:123], v[224:227]\n" \
"v_exp_f32_e32 v94, v94\n" \
"v_exp_f32_e32 v95, v95\n" \
"v_cvt_pk_bf16_f32 v124, v88, v89\n" \
"v_cvt_pk_bf16_f32 v125, v90, v91\n" \
"v_cvt_pk_bf16_f32 v126, v92, v93\n" \
"v_cvt_pk_bf16_f32 v127, v94, v95\n" \
"v_add_u32_e32 v52, 0x14000, v63\n" \
"v_add_u32_e32 v53, 0x20000, v63\n" \
"v_mfma_f32_16x16x32_bf16 v[224:227], a[176:179], v[124:127], v[224:227]\n" \
"buffer_load_dwordx4 v51, s[8:11], s64 offen offset:2048 sc1 lds\n" \
"v_mfma_f32_16x16x32_bf16 v[228:231], a[180:183], v[124:127], v[228:231]\n" \
"buffer_load_dwordx4 v51, s[8:11], s64 offen offset:3072 sc1 lds\n" \
"v_mfma_f32_16x16x32_bf16 v[228:231], a[164:167], v[120:123], v[228:231]\n" \
"v_readlane_b32 s93, v11, 17\n" \
"v_readlane_b32 s92, v11, 22\n" \
"v_mfma_f32_16x16x32_bf16 v[232:235], a[168:171], v[120:123], v[232:235]\n" \
"s_mov_b32 s66, s19\n" \
"v_mfma_f32_16x16x32_bf16 v[232:235], a[184:187], v[124:127], v[232:235]\n" \
"v_mfma_f32_16x16x32_bf16 v[236:239], a[188:191], v[124:127], v[236:239]\n" \
"v_mfma_f32_16x16x32_bf16 v[236:239], a[172:175], v[120:123], v[236:239]\n" \
"s_waitcnt vmcnt(10) lgkmcnt(0)\n" \
"s_barrier\n" \
"v_mfma_f32_16x16x32_bf16 v[64:67], a[128:131], a[0:3], v[32:35]\n" \
"s_xor_b32 m0, s65, 0x18000\n" \
"ds_read_b128 a[160:163], v49 offset:8192\n" \
"v_mfma_f32_16x16x32_bf16 v[64:67], a[132:135], a[4:7], v[64:67]\n" \
"buffer_load_dwordx4 v52, s[8:11], s64 offen offset:0 sc1 lds\n" \
"v_mfma_f32_16x16x32_bf16 v[64:67], a[136:139], a[8:11], v[64:67]\n" \
"ds_read_b128 a[176:179], v49 offset:12288\n" \
"ds_read_b128 a[180:183], v49 offset:13312\n" \
"v_mfma_f32_16x16x32_bf16 v[64:67], a[140:143], a[12:15], v[64:67]\n" \
"buffer_load_dwordx4 v52, s[8:11], s64 offen offset:1024 sc1 lds\n" \
"v_mfma_f32_16x16x32_bf16 v[68:71], a[156:159], a[12:15], v[32:35]\n" \
"ds_read_b128 a[164:167], v49 offset:9216\n" \
"ds_read_b128 a[168:171], v49 offset:10240\n" \
"v_mfma_f32_16x16x32_bf16 v[68:71], a[152:155], a[8:11], v[68:71]\n" \
"ds_read_b128 a[184:187], v49 offset:14336\n" \
"ds_read_b128 a[188:191], v49 offset:15360\n" \
"v_mfma_f32_16x16x32_bf16 v[68:71], a[148:151], a[4:7], v[68:71]\n" \
"v_exp_f32_e32 v64, v64\n" \
"v_mfma_f32_16x16x32_bf16 v[68:71], a[144:147], a[0:3], v[68:71]\n" \
"buffer_load_dwordx4 v52, s[8:11], s64 offen offset:2048 sc1 lds\n" \
"v_mfma_f32_16x16x32_bf16 v[72:75], a[128:131], a[16:19], v[36:39]\n" \
"v_exp_f32_e32 v65, v65\n" \
"v_mfma_f32_16x16x32_bf16 v[72:75], a[132:135], a[20:23], v[72:75]\n" \
"v_exp_f32_e32 v66, v66\n" \
"v_mfma_f32_16x16x32_bf16 v[72:75], a[136:139], a[24:27], v[72:75]\n" \
"buffer_load_dwordx4 v52, s[8:11], s64 offen offset:3072 sc1 lds\n" \
"v_mfma_f32_16x16x32_bf16 v[72:75], a[140:143], a[28:31], v[72:75]\n" \
"s_mov_b32 m0, s65\n" \
"ds_read_b128 a[172:175], v49 offset:11264\n" \
"v_mfma_f32_16x16x32_bf16 v[76:79], a[156:159], a[28:31], v[36:39]\n" \
"v_exp_f32_e32 v67, v67\n" \
"v_mfma_f32_16x16x32_bf16 v[76:79], a[152:155], a[24:27], v[76:79]\n" \
"L_pf12:\n" \
"s_cbranch_execz L_pf16\n" \
"s_nop 0\n" \
"v_exp_f32_e32 v68, v68\n" \
"v_mfma_f32_16x16x32_bf16 v[76:79], a[148:151], a[20:23], v[76:79]\n" \
"buffer_load_dwordx4 v53, s[8:11], s64 offen offset:0 sc1 lds\n" \
"v_mfma_f32_16x16x32_bf16 v[76:79], a[144:147], a[16:19], v[76:79]\n" \
"v_exp_f32_e32 v69, v69\n" \
"v_mfma_f32_16x16x32_bf16 v[80:83], a[128:131], a[32:35], v[40:43]\n" \
"v_exp_f32_e32 v70, v70\n" \
"v_mfma_f32_16x16x32_bf16 v[80:83], a[132:135], a[36:39], v[80:83]\n" \
"buffer_load_dwordx4 v53, s[8:11], s64 offen offset:1024 sc1 lds\n" \
"v_mfma_f32_16x16x32_bf16 v[80:83], a[136:139], a[40:43], v[80:83]\n" \
"v_exp_f32_e32 v71, v71\n" \
"v_mfma_f32_16x16x32_bf16 v[80:83], a[140:143], a[44:47], v[80:83]\n" \
"v_exp_f32_e32 v72, v72\n" \
"v_mfma_f32_16x16x32_bf16 v[84:87], a[156:159], a[44:47], v[40:43]\n" \
"v_exp_f32_e32 v73, v73\n" \
"v_mfma_f32_16x16x32_bf16 v[84:87], a[152:155], a[40:43], v[84:87]\n" \
"buffer_load_dwordx4 v53, s[8:11], s64 offen offset:2048 sc1 lds\n" \
"v_mfma_f32_16x16x32_bf16 v[84:87], a[148:151], a[36:39], v[84:87]\n" \
"v_exp_f32_e32 v74, v74\n" \
"v_mfma_f32_16x16x32_bf16 v[84:87], a[144:147], a[32:35], v[84:87]\n" \
"v_exp_f32_e32 v75, v75\n" \
"v_mfma_f32_16x16x32_bf16 v[88:91], a[128:131], a[48:51], v[44:47]\n" \
"buffer_load_dwordx4 v53, s[8:11], s64 offen offset:3072 sc1 lds\n" \
"v_mfma_f32_16x16x32_bf16 v[88:91], a[132:135], a[52:55], v[88:91]\n" \
"s_add_u32 m0, s93, 0x0\n" \
"ds_read_b128 a[128:131], v48 offset:8192\n" \
"v_mfma_f32_16x16x32_bf16 v[88:91], a[136:139], a[56:59], v[88:91]\n" \
"ds_read_b128 a[132:135], v48 offset:9216\n" \
"ds_read_b128 a[136:139], v48 offset:10240\n" \
"v_mfma_f32_16x16x32_bf16 v[88:91], a[140:143], a[60:63], v[88:91]\n" \
"ds_read_b128 a[140:143], v48 offset:11264\n" \
"v_add_u32_e32 v49, 0x10000, v48\n" \
"v_mfma_f32_16x16x32_bf16 v[92:95], a[156:159], a[60:63], v[44:47]\n" \
"buffer_load_dwordx4 v0, s[80:83], s92 offen offset:0 lds\n" \
"v_mfma_f32_16x16x32_bf16 v[92:95], a[152:155], a[56:59], v[92:95]\n" \
"ds_read_b128 a[152:155], v48 offset:14336\n" \
"ds_read_b128 a[156:159], v48 offset:15360\n" \
"v_mfma_f32_16x16x32_bf16 v[92:95], a[148:151], a[52:55], v[92:95]\n" \
"ds_read_b128 a[148:151], v48 offset:13312\n" \
"s_xor_b32 s65, s65, 0x8000\n" \
"v_mfma_f32_16x16x32_bf16 v[92:95], a[144:147], a[48:51], v[92:95]\n" \
"ds_read_b128 a[144:147], v48 offset:12288\n" \
"s_waitcnt lgkmcnt(8)\n" \
"v_mfma_f32_16x16x32_bf16 v[144:147], a[160:163], v[96:99], v[144:147]\n" \
"buffer_load_dwordx4 v1, s[80:83], s92 offen offset:1024 lds\n" \
"v_mfma_f32_16x16x32_bf16 v[144:147], a[176:179], v[100:103], v[144:147]\n" \
"v_exp_f32_e32 v76, v76\n" \
"v_mfma_f32_16x16x32_bf16 v[148:151], a[180:183], v[100:103], v[148:151]\n" \
"v_exp_f32_e32 v77, v77\n" \
"v_mfma_f32_16x16x32_bf16 v[148:151], a[164:167], v[96:99], v[148:151]\n" \
"buffer_load_dwordx4 v2, s[80:83], s92 offen offset:2048 lds\n" \
"v_mfma_f32_16x16x32_bf16 v[152:155], a[168:171], v[96:99], v[152:155]\n" \
"v_exp_f32_e32 v78, v78\n" \
"v_mfma_f32_16x16x32_bf16 v[152:155], a[184:187], v[100:103], v[152:155]\n" \
"v_exp_f32_e32 v79, v79\n" \
"v_mfma_f32_16x16x32_bf16 v[156:159], a[188:191], v[100:103], v[156:159]\n" \
"buffer_load_dwordx4 v3, s[80:83], s92 offen offset:3072 lds\n" \
"v_mfma_f32_16x16x32_bf16 v[156:159], a[172:175], v[96:99], v[156:159]\n" \
"s_add_u32 m0, s93, 0x1000\n" \
"v_cvt_pk_bf16_f32 v96, v64, v65\n" \
"v_mfma_f32_16x16x32_bf16 v[176:179], a[160:163], v[104:107], v[176:179]\n" \
"v_exp_f32_e32 v80, v80\n" \
"v_mfma_f32_16x16x32_bf16 v[176:179], a[176:179], v[108:111], v[176:179]\n" \
"v_exp_f32_e32 v81, v81\n" \
"v_mfma_f32_16x16x32_bf16 v[180:183], a[180:183], v[108:111], v[180:183]\n" \
"buffer_load_dwordx4 v4, s[80:83], s92 offen offset:0 lds\n" \
"v_mfma_f32_16x16x32_bf16 v[180:183], a[164:167], v[104:107], v[180:183]\n" \
"v_exp_f32_e32 v82, v82\n" \
"v_mfma_f32_16x16x32_bf16 v[184:187], a[168:171], v[104:107], v[184:187]\n" \
"v_exp_f32_e32 v83, v83\n" \
"v_mfma_f32_16x16x32_bf16 v[184:187], a[184:187], v[108:111], v[184:187]\n" \
"buffer_load_dwordx4 v5, s[80:83], s92 offen offset:1024 lds\n" \
"v_mfma_f32_16x16x32_bf16 v[188:191], a[188:191], v[108:111], v[188:191]\n" \
"v_exp_f32_e32 v84, v84\n" \
"v_mfma_f32_16x16x32_bf16 v[188:191], a[172:175], v[104:107], v[188:191]\n" \
"v_exp_f32_e32 v85, v85\n" \
"v_mfma_f32_16x16x32_bf16 v[208:211], a[160:163], v[112:115], v[208:211]\n" \
"v_exp_f32_e32 v86, v86\n" \
"v_mfma_f32_16x16x32_bf16 v[208:211], a[176:179], v[116:119], v[208:211]\n" \
"buffer_load_dwordx4 v6, s[80:83], s92 offen offset:2048 lds\n" \
"v_mfma_f32_16x16x32_bf16 v[212:215], a[180:183], v[116:119], v[212:215]\n" \
"v_exp_f32_e32 v87, v87\n" \
"v_mfma_f32_16x16x32_bf16 v[212:215], a[164:167], v[112:115], v[212:215]\n" \
"v_exp_f32_e32 v88, v88\n" \
"v_mfma_f32_16x16x32_bf16 v[216:219], a[168:171], v[112:115], v[216:219]\n" \
"buffer_load_dwordx4 v7, s[80:83], s92 offen offset:3072 lds\n" \
"v_mfma_f32_16x16x32_bf16 v[216:219], a[184:187], v[116:119], v[216:219]\n" \
"s_add_u32 m0, s93, 0x2000\n" \
"v_cvt_pk_bf16_f32 v97, v66, v67\n" \
"v_mfma_f32_16x16x32_bf16 v[220:223], a[188:191], v[116:119], v[220:223]\n" \
"v_exp_f32_e32 v89, v89\n" \
"v_mfma_f32_16x16x32_bf16 v[220:223], a[172:175], v[112:115], v[220:223]\n" \
"v_exp_f32_e32 v90, v90\n" \
"v_mfma_f32_16x16x32_bf16 v[240:243], a[160:163], v[120:123], v[240:243]\n" \
"buffer_load_dwordx4 v8, s[80:83], s92 offen offset:0 lds\n" \
"v_mfma_f32_16x16x32_bf16 v[240:243], a[176:179], v[124:127], v[240:243]\n" \
"v_exp_f32_e32 v91, v91\n" \
"v_mfma_f32_16x16x32_bf16 v[244:247], a[180:183], v[124:127], v[244:247]\n" \
"v_cvt_pk_bf16_f32 v98, v68, v69\n" \
"v_cvt_pk_bf16_f32 v99, v70, v71\n" \
"v_mfma_f32_16x16x32_bf16 v[244:247], a[164:167], v[120:123], v[244:247]\n" \
"v_cvt_pk_bf16_f32 v104, v72, v73\n" \
"v_cvt_pk_bf16_f32 v105, v74, v75\n" \
"v_mfma_f32_16x16x32_bf16 v[248:251], a[168:171], v[120:123], v[248:251]\n" \
"v_cvt_pk_bf16_f32 v106, v76, v77\n" \
"v_cvt_pk_bf16_f32 v107, v78, v79\n" \
"v_mfma_f32_16x16x32_bf16 v[248:251], a[184:187], v[124:127], v[248:251]\n" \
"v_cvt_pk_bf16_f32 v112, v80, v81\n" \
"v_cvt_pk_bf16_f32 v113, v82, v83\n" \
"v_mfma_f32_16x16x32_bf16 v[252:255], a[188:191], v[124:127], v[252:255]\n" \
"v_cvt_pk_bf16_f32 v114, v84, v85\n" \
"v_cvt_pk_bf16_f32 v115, v86, v87\n" \
"v_mfma_f32_16x16x32_bf16 v[252:255], a[172:175], v[120:123], v[252:255]\n" \
"v_add_u32_e32 v48, s16, v20\n" \
"s_add_u32 s64, s64, 0x10000\n" \
"v_smfmac_f32_16x16x64_bf16 a[204:207], a[212:215], v[120:127], v21\n" \
"s_waitcnt vmcnt(17) lgkmcnt(0)\n" \
"s_barrier\n" \
"s_mov_b64 exec, 0\n" \
"s_cmp_lg_u32 s15, 0\n" \
"s_cbranch_scc1 L_pf22\n" \
"s_branch L_pf21\n" \
"L_retB:\n" \
"s_mov_b64 exec, -1\n" \
"s_branch L_2_%=\n" \
"L_t2_%=:\n" \
"v_mfma_f32_16x16x32_bf16 v[252:255], a[188:191], v[124:127], v[252:255]\n" \
"v_cvt_pk_bf16_f32 v114, v84, v85\n" \
"v_cvt_pk_bf16_f32 v115, v86, v87\n" \
"v_mfma_f32_16x16x32_bf16 v[252:255], a[172:175], v[120:123], v[252:255]\n" \
"v_xor_b32_e32 v48, 0x8000, v48\n" \
"v_smfmac_f32_16x16x64_bf16 a[204:207], a[212:215], v[120:127], v21\n" \
"s_waitcnt lgkmcnt(0)\n" \
"L_2_%=:\n" \
"v_mfma_f32_16x16x32_bf16 v[64:67], a[128:131], a[64:67], v[32:35]\n" \
"ds_read_b128 a[160:163], v49 offset:0\n" \
"ds_read_b128 a[176:179], v49 offset:4096\n" \
"v_mfma_f32_16x16x32_bf16 v[64:67], a[132:135], a[68:71], v[64:67]\n" \
"v_cvt_pk_bf16_f32 v120, v88, v89\n" \
"v_cvt_pk_bf16_f32 v121, v90, v91\n" \
"v_mfma_f32_16x16x32_bf16 v[64:67], a[136:139], a[72:75], v[64:67]\n" \
"ds_read_b128 a[180:183], v49 offset:5120\n" \
"ds_read_b128 a[164:167], v49 offset:1024\n" \
"v_mfma_f32_16x16x32_bf16 v[64:67], a[140:143], a[76:79], v[64:67]\n" \
"v_exp_f32_e32 v92, v92\n" \
"v_mfma_f32_16x16x32_bf16 v[68:71], a[156:159], a[76:79], v[32:35]\n" \
"ds_read_b128 a[168:171], v49 offset:2048\n" \
"ds_read_b128 a[184:187], v49 offset:6144\n" \
"v_mfma_f32_16x16x32_bf16 v[68:71], a[152:155], a[72:75], v[68:71]\n" \
"v_exp_f32_e32 v93, v93\n" \
"v_mfma_f32_16x16x32_bf16 v[68:71], a[148:151], a[68:71], v[68:71]\n" \
"ds_read_b128 a[188:191], v49 offset:7168\n" \
"ds_read_b128 a[172:175], v49 offset:3072\n" \
"v_mfma_f32_16x16x32_bf16 v[68:71], a[144:147], a[64:67], v[68:71]\n" \
"v_exp_f32_e32 v94, v94\n" \
"v_mfma_f32_16x16x32_bf16 v[72:75], a[128:131], a[80:83], v[36:39]\n" \
"v_exp_f32_e32 v95, v95\n" \
"v_mfma_f32_16x16x32_bf16 v[72:75], a[132:135], a[84:87], v[72:75]\n" \
"v_cvt_pk_bf16_f32 v122, v92, v93\n" \
"v_cvt_pk_bf16_f32 v123, v94, v95\n" \
"v_mfma_f32_16x16x32_bf16 v[72:75], a[136:139], a[88:91], v[72:75]\n" \
"v_exp_f32_e32 v64, v64\n" \
"v_mfma_f32_16x16x32_bf16 v[72:75], a[140:143], a[92:95], v[72:75]\n" \
"v_exp_f32_e32 v65, v65\n" \
"v_mfma_f32_16x16x32_bf16 v[76:79], a[156:159], a[92:95], v[36:39]\n" \
"v_exp_f32_e32 v66, v66\n" \
"v_mfma_f32_16x16x32_bf16 v[76:79], a[152:155], a[88:91], v[76:79]\n" \
"v_exp_f32_e32 v67, v67\n" \
"v_mfma_f32_16x16x32_bf16 v[76:79], a[148:151], a[84:87], v[76:79]\n" \
"v_exp_f32_e32 v68, v68\n" \
"v_mfma_f32_16x16x32_bf16 v[76:79], a[144:147], a[80:83], v[76:79]\n" \
"v_exp_f32_e32 v69, v69\n" \
"v_mfma_f32_16x16x32_bf16 v[80:83], a[128:131], a[96:99], v[40:43]\n" \
"v_exp_f32_e32 v70, v70\n" \
"v_mfma_f32_16x16x32_bf16 v[80:83], a[132:135], a[100:103], v[80:83]\n" \
"v_exp_f32_e32 v71, v71\n" \
"v_mfma_f32_16x16x32_bf16 v[80:83], a[136:139], a[104:107], v[80:83]\n" \
"v_cvt_pk_bf16_f32 v100, v64, v65\n" \
"v_cvt_pk_bf16_f32 v101, v66, v67\n" \
"v_mfma_f32_16x16x32_bf16 v[80:83], a[140:143], a[108:111], v[80:83]\n" \
"v_cvt_pk_bf16_f32 v102, v68, v69\n" \
"v_cvt_pk_bf16_f32 v103, v70, v71\n" \
"v_mfma_f32_16x16x32_bf16 v[84:87], a[156:159], a[108:111], v[40:43]\n" \
"v_exp_f32_e32 v72, v72\n" \
"v_mfma_f32_16x16x32_bf16 v[84:87], a[152:155], a[104:107], v[84:87]\n" \
"v_exp_f32_e32 v73, v73\n" \
"v_mfma_f32_16x16x32_bf16 v[84:87], a[148:151], a[100:103], v[84:87]\n" \
"v_exp_f32_e32 v74, v74\n" \
"v_mfma_f32_16x16x32_bf16 v[84:87], a[144:147], a[96:99], v[84:87]\n" \
"v_exp_f32_e32 v75, v75\n" \
"v_mfma_f32_16x16x32_bf16 v[88:91], a[128:131], a[112:115], v[44:47]\n" \
"v_exp_f32_e32 v76, v76\n" \
"v_mfma_f32_16x16x32_bf16 v[88:91], a[132:135], a[116:119], v[88:91]\n" \
"v_exp_f32_e32 v77, v77\n" \
"v_mfma_f32_16x16x32_bf16 v[88:91], a[136:139], a[120:123], v[88:91]\n" \
"v_exp_f32_e32 v78, v78\n" \
"v_mfma_f32_16x16x32_bf16 v[88:91], a[140:143], a[124:127], v[88:91]\n" \
"v_exp_f32_e32 v79, v79\n" \
"v_mfma_f32_16x16x32_bf16 v[92:95], a[156:159], a[124:127], v[44:47]\n" \
"v_cvt_pk_bf16_f32 v108, v72, v73\n" \
"v_cvt_pk_bf16_f32 v109, v74, v75\n" \
"v_mfma_f32_16x16x32_bf16 v[92:95], a[152:155], a[120:123], v[92:95]\n" \
"v_cvt_pk_bf16_f32 v110, v76, v77\n" \
"v_cvt_pk_bf16_f32 v111, v78, v79\n" \
"v_mfma_f32_16x16x32_bf16 v[92:95], a[148:151], a[116:119], v[92:95]\n" \
"ds_read_b128 a[128:131], v48 offset:0\n" \
"ds_read_b128 a[132:135], v48 offset:1024\n" \
"v_mfma_f32_16x16x32_bf16 v[92:95], a[144:147], a[112:115], v[92:95]\n" \
"s_add_u32 s63, s63, s63\n" \
"s_waitcnt lgkmcnt(2)\n" \
"v_mfma_f32_16x16x32_bf16 v[128:131], a[160:163], v[96:99], v[128:131]\n" \
"ds_read_b128 a[136:139], v48 offset:2048\n" \
"ds_read_b128 a[140:143], v48 offset:3072\n" \
"v_mfma_f32_16x16x32_bf16 v[128:131], a[176:179], v[100:103], v[128:131]\n" \
"v_exp_f32_e32 v80, v80\n" \
"v_mfma_f32_16x16x32_bf16 v[132:135], a[180:183], v[100:103], v[132:135]\n" \
"ds_read_b128 a[144:147], v48 offset:4096\n" \
"ds_read_b128 a[148:151], v48 offset:5120\n" \
"v_mfma_f32_16x16x32_bf16 v[132:135], a[164:167], v[96:99], v[132:135]\n" \
"v_exp_f32_e32 v81, v81\n" \
"v_mfma_f32_16x16x32_bf16 v[136:139], a[168:171], v[96:99], v[136:139]\n" \
"ds_read_b128 a[152:155], v48 offset:6144\n" \
"ds_read_b128 a[156:159], v48 offset:7168\n" \
"v_mfma_f32_16x16x32_bf16 v[136:139], a[184:187], v[100:103], v[136:139]\n" \
"v_exp_f32_e32 v82, v82\n" \
"v_mfma_f32_16x16x32_bf16 v[140:143], a[188:191], v[100:103], v[140:143]\n" \
"v_exp_f32_e32 v83, v83\n" \
"v_mfma_f32_16x16x32_bf16 v[140:143], a[172:175], v[96:99], v[140:143]\n" \
"v_exp_f32_e32 v84, v84\n" \
"v_smfmac_f32_16x16x64_bf16 a[192:195], a[212:215], v[96:103], v21\n" \
"v_mfma_f32_16x16x32_bf16 v[160:163], a[160:163], v[104:107], v[160:163]\n" \
"v_exp_f32_e32 v85, v85\n" \
"v_mfma_f32_16x16x32_bf16 v[160:163], a[176:179], v[108:111], v[160:163]\n" \
"v_exp_f32_e32 v86, v86\n" \
"v_mfma_f32_16x16x32_bf16 v[164:167], a[180:183], v[108:111], v[164:167]\n" \
"v_exp_f32_e32 v87, v87\n" \
"v_mfma_f32_16x16x32_bf16 v[164:167], a[164:167], v[104:107], v[164:167]\n" \
"v_cvt_pk_bf16_f32 v116, v80, v81\n" \
"v_cvt_pk_bf16_f32 v117, v82, v83\n" \
"v_mfma_f32_16x16x32_bf16 v[168:171], a[168:171], v[104:107], v[168:171]\n" \
"v_cvt_pk_bf16_f32 v118, v84, v85\n" \
"v_cvt_pk_bf16_f32 v119, v86, v87\n" \
"v_mfma_f32_16x16x32_bf16 v[168:171], a[184:187], v[108:111], v[168:171]\n" \
"v_exp_f32_e32 v88, v88\n" \
"v_mfma_f32_16x16x32_bf16 v[172:175], a[188:191], v[108:111], v[172:175]\n" \
"v_exp_f32_e32 v89, v89\n" \
"v_mfma_f32_16x16x32_bf16 v[172:175], a[172:175], v[104:107], v[172:175]\n" \
"v_exp_f32_e32 v90, v90\n" \
"v_smfmac_f32_16x16x64_bf16 a[196:199], a[212:215], v[104:111], v21\n" \
"v_mfma_f32_16x16x32_bf16 v[192:195], a[160:163], v[112:115], v[192:195]\n" \
"v_exp_f32_e32 v91, v91\n" \
"v_mfma_f32_16x16x32_bf16 v[192:195], a[176:179], v[116:119], v[192:195]\n" \
"v_exp_f32_e32 v92, v92\n" \
"v_mfma_f32_16x16x32_bf16 v[196:199], a[180:183], v[116:119], v[196:199]\n" \
"v_exp_f32_e32 v93, v93\n" \
"v_mfma_f32_16x16x32_bf16 v[196:199], a[164:167], v[112:115], v[196:199]\n" \
"v_exp_f32_e32 v94, v94\n" \
"v_mfma_f32_16x16x32_bf16 v[200:203], a[168:171], v[112:115], v[200:203]\n" \
"v_exp_f32_e32 v95, v95\n" \
"v_mfma_f32_16x16x32_bf16 v[200:203], a[184:187], v[116:119], v[200:203]\n" \
"v_cvt_pk_bf16_f32 v124, v88, v89\n" \
"v_cvt_pk_bf16_f32 v125, v90, v91\n" \
"v_mfma_f32_16x16x32_bf16 v[204:207], a[188:191], v[116:119], v[204:207]\n" \
"v_cvt_pk_bf16_f32 v126, v92, v93\n" \
"v_cvt_pk_bf16_f32 v127, v94, v95\n" \
"v_mfma_f32_16x16x32_bf16 v[204:207], a[172:175], v[112:115], v[204:207]\n" \
"v_smfmac_f32_16x16x64_bf16 a[200:203], a[212:215], v[112:119], v21\n" \
"s_waitcnt vmcnt(0) lgkmcnt(0)\n" \
"s_barrier\n" \
"v_mfma_f32_16x16x32_bf16 v[224:227], a[160:163], v[120:123], v[224:227]\n" \
"s_cbranch_scc1 L_h2_%=\n" \
"L_hr2_%=:\n" \
"v_mfma_f32_16x16x32_bf16 v[224:227], a[176:179], v[124:127], v[224:227]\n" \
"s_xor_b32 m0, s65, 0x12000\n" \
"ds_read_b128 a[160:163], v49 offset:8192\n" \
"v_mfma_f32_16x16x32_bf16 v[228:231], a[180:183], v[124:127], v[228:231]\n" \
"buffer_load_dwordx4 v50, s[8:11], s64 offen offset:0 sc1 lds\n" \
"v_mfma_f32_16x16x32_bf16 v[228:231], a[164:167], v[120:123], v[228:231]\n" \
"ds_read_b128 a[176:179], v49 offset:12288\n" \
"ds_read_b128 a[180:183], v49 offset:13312\n" \
"v_mfma_f32_16x16x32_bf16 v[232:235], a[168:171], v[120:123], v[232:235]\n" \
"v_mfma_f32_16x16x32_bf16 v[232:235], a[184:187], v[124:127], v[232:235]\n" \
"buffer_load_dwordx4 v50, s[8:11], s64 offen offset:1024 sc1 lds\n" \
"v_mfma_f32_16x16x32_bf16 v[236:239], a[188:191], v[124:127], v[236:239]\n" \
"ds_read_b128 a[164:167], v49 offset:9216\n" \
"ds_read_b128 a[168:171], v49 offset:10240\n" \
"v_mfma_f32_16x16x32_bf16 v[236:239], a[172:175], v[120:123], v[236:239]\n" \
"v_mfma_f32_16x16x32_bf16 v[64:67], a[128:131], a[64:67], v[32:35]\n" \
"ds_read_b128 a[184:187], v49 offset:14336\n" \
"ds_read_b128 a[188:191], v49 offset:15360\n" \
"v_mfma_f32_16x16x32_bf16 v[64:67], a[132:135], a[68:71], v[64:67]\n" \
"buffer_load_dwordx4 v50, s[8:11], s64 offen offset:2048 sc1 lds\n" \
"v_mfma_f32_16x16x32_bf16 v[64:67], a[136:139], a[72:75], v[64:67]\n" \
"ds_read_b128 a[172:175], v49 offset:11264\n" \
"v_add_u32_e32 v49, 0x10000, v48\n" \
"v_mfma_f32_16x16x32_bf16 v[64:67], a[140:143], a[76:79], v[64:67]\n" \
"v_mfma_f32_16x16x32_bf16 v[68:71], a[156:159], a[76:79], v[32:35]\n" \
"v_mfma_f32_16x16x32_bf16 v[68:71], a[152:155], a[72:75], v[68:71]\n" \
"v_mfma_f32_16x16x32_bf16 v[68:71], a[148:151], a[68:71], v[68:71]\n" \
"buffer_load_dwordx4 v50, s[8:11], s64 offen offset:3072 sc1 lds\n" \
"v_mfma_f32_16x16x32_bf16 v[68:71], a[144:147], a[64:67], v[68:71]\n" \
"s_xor_b32 m0, s65, 0xa000\n" \
"v_mfma_f32_16x16x32_bf16 v[72:75], a[128:131], a[80:83], v[36:39]\n" \
"v_mfma_f32_16x16x32_bf16 v[72:75], a[132:135], a[84:87], v[72:75]\n" \
"v_exp_f32_e32 v64, v64\n" \
"v_mfma_f32_16x16x32_bf16 v[72:75], a[136:139], a[88:91], v[72:75]\n" \
"buffer_load_dwordx4 v51, s[8:11], s64 offen offset:0 sc1 lds\n" \
"v_mfma_f32_16x16x32_bf16 v[72:75], a[140:143], a[92:95], v[72:75]\n" \
"v_exp_f32_e32 v65, v65\n" \
"v_mfma_f32_16x16x32_bf16 v[76:79], a[156:159], a[92:95], v[36:39]\n" \
"v_exp_f32_e32 v66, v66\n" \
"v_mfma_f32_16x16x32_bf16 v[76:79], a[152:155], a[88:91], v[76:79]\n" \
"v_exp_f32_e32 v67, v67\n" \
"v_mfma_f32_16x16x32_bf16 v[76:79], a[148:151], a[84:87], v[76:79]\n" \
"buffer_load_dwordx4 v51, s[8:11], s64 offen offset:1024 sc1 lds\n" \
"v_mfma_f32_16x16x32_bf16 v[76:79], a[144:147], a[80:83], v[76:79]\n" \
"v_exp_f32_e32 v68, v68\n" \
"v_mfma_f32_16x16x32_bf16 v[80:83], a[128:131], a[96:99], v[40:43]\n" \
"v_exp_f32_e32 v69, v69\n" \
"v_mfma_f32_16x16x32_bf16 v[80:83], a[132:135], a[100:103], v[80:83]\n" \
"v_exp_f32_e32 v70, v70\n" \
"v_mfma_f32_16x16x32_bf16 v[80:83], a[136:139], a[104:107], v[80:83]\n" \
"v_exp_f32_e32 v71, v71\n" \
"v_mfma_f32_16x16x32_bf16 v[80:83], a[140:143], a[108:111], v[80:83]\n" \
"buffer_load_dwordx4 v51, s[8:11], s64 offen offset:2048 sc1 lds\n" \
"v_mfma_f32_16x16x32_bf16 v[84:87], a[156:159], a[108:111], v[40:43]\n" \
"v_exp_f32_e32 v72, v72\n" \
"v_mfma_f32_16x16x32_bf16 v[84:87], a[152:155], a[104:107], v[84:87]\n" \
"v_exp_f32_e32 v73, v73\n" \
"v_mfma_f32_16x16x32_bf16 v[84:87], a[148:151], a[100:103], v[84:87]\n" \
"v_exp_f32_e32 v74, v74\n" \
"v_mfma_f32_16x16x32_bf16 v[84:87], a[144:147], a[96:99], v[84:87]\n" \
"buffer_load_dwordx4 v51, s[8:11], s64 offen offset:3072 sc1 lds\n" \
"v_mfma_f32_16x16x32_bf16 v[88:91], a[128:131], a[112:115], v[44:47]\n" \
"s_xor_b32 m0, s65, 0x18000\n" \
"ds_read_b128 a[128:131], v48 offset:8192\n" \
"v_mfma_f32_16x16x32_bf16 v[88:91], a[132:135], a[116:119], v[88:91]\n" \
"v_exp_f32_e32 v75, v75\n" \
"v_mfma_f32_16x16x32_bf16 v[88:91], a[136:139], a[120:123], v[88:91]\n" \
"ds_read_b128 a[132:135], v48 offset:9216\n" \
"ds_read_b128 a[136:139], v48 offset:10240\n" \
"v_mfma_f32_16x16x32_bf16 v[88:91], a[140:143], a[124:127], v[88:91]\n" \
"v_exp_f32_e32 v76, v76\n" \
"v_mfma_f32_16x16x32_bf16 v[92:95], a[156:159], a[124:127], v[44:47]\n" \
"buffer_load_dwordx4 v52, s[8:11], s64 offen offset:0 sc1 lds\n" \
"v_mfma_f32_16x16x32_bf16 v[92:95], a[152:155], a[120:123], v[92:95]\n" \
"ds_read_b128 a[140:143], v48 offset:11264\n" \
"ds_read_b128 a[152:155], v48 offset:14336\n" \
"v_mfma_f32_16x16x32_bf16 v[92:95], a[148:151], a[116:119], v[92:95]\n" \
"v_exp_f32_e32 v77, v77\n" \
"v_mfma_f32_16x16x32_bf16 v[92:95], a[144:147], a[112:115], v[92:95]\n" \
"ds_read_b128 a[144:147], v48 offset:12288\n" \
"s_waitcnt lgkmcnt(6)\n" \
"v_mfma_f32_16x16x32_bf16 v[144:147], a[160:163], v[96:99], v[144:147]\n" \
"buffer_load_dwordx4 v52, s[8:11], s64 offen offset:1024 sc1 lds\n" \
"v_mfma_f32_16x16x32_bf16 v[144:147], a[176:179], v[100:103], v[144:147]\n" \
"ds_read_b128 a[148:151], v48 offset:13312\n" \
"ds_read_b128 a[156:159], v48 offset:15360\n" \
"v_mfma_f32_16x16x32_bf16 v[148:151], a[180:183], v[100:103], v[148:151]\n" \
"v_exp_f32_e32 v78, v78\n" \
"v_mfma_f32_16x16x32_bf16 v[148:151], a[164:167], v[96:99], v[148:151]\n" \
"v_exp_f32_e32 v79, v79\n" \
"v_mfma_f32_16x16x32_bf16 v[152:155], a[168:171], v[96:99], v[152:155]\n" \
"buffer_load_dwordx4 v52, s[8:11], s64 offen offset:2048 sc1 lds\n" \
"v_mfma_f32_16x16x32_bf16 v[152:155], a[184:187], v[100:103], v[152:155]\n" \
"v_exp_f32_e32 v80, v80\n" \
"v_mfma_f32_16x16x32_bf16 v[156:159], a[188:191], v[100:103], v[156:159]\n" \
"v_exp_f32_e32 v81, v81\n" \
"v_mfma_f32_16x16x32_bf16 v[156:159], a[172:175], v[96:99], v[156:159]\n" \
"v_exp_f32_e32 v82, v82\n" \
"v_mfma_f32_16x16x32_bf16 v[176:179], a[160:163], v[104:107], v[176:179]\n" \
"v_exp_f32_e32 v83, v83\n" \
"v_mfma_f32_16x16x32_bf16 v[176:179], a[176:179], v[108:111], v[176:179]\n" \
"buffer_load_dwordx4 v52, s[8:11], s64 offen offset:3072 sc1 lds\n" \
"v_mfma_f32_16x16x32_bf16 v[180:183], a[180:183], v[108:111], v[180:183]\n" \
"s_mov_b32 m0, s65\n" \
"s_xor_b32 s65, s65, 0x8000\n" \
"v_mfma_f32_16x16x32_bf16 v[180:183], a[164:167], v[104:107], v[180:183]\n" \
"v_exp_f32_e32 v84, v84\n" \
"v_mfma_f32_16x16x32_bf16 v[184:187], a[168:171], v[104:107], v[184:187]\n" \
"v_exp_f32_e32 v85, v85\n" \
"v_mfma_f32_16x16x32_bf16 v[184:187], a[184:187], v[108:111], v[184:187]\n" \
"buffer_load_dwordx4 v53, s[8:11], s64 offen offset:0 sc1 lds\n" \
"v_mfma_f32_16x16x32_bf16 v[188:191], a[188:191], v[108:111], v[188:191]\n" \
"v_exp_f32_e32 v86, v86\n" \
"v_mfma_f32_16x16x32_bf16 v[188:191], a[172:175], v[104:107], v[188:191]\n" \
"v_exp_f32_e32 v87, v87\n" \
"v_mfma_f32_16x16x32_bf16 v[208:211], a[160:163], v[112:115], v[208:211]\n" \
"v_exp_f32_e32 v88, v88\n" \
"v_mfma_f32_16x16x32_bf16 v[208:211], a[176:179], v[116:119], v[208:211]\n" \
"buffer_load_dwordx4 v53, s[8:11], s64 offen offset:1024 sc1 lds\n" \
"v_mfma_f32_16x16x32_bf16 v[212:215], a[180:183], v[116:119], v[212:215]\n" \
"v_exp_f32_e32 v89, v89\n" \
"v_mfma_f32_16x16x32_bf16 v[212:215], a[164:167], v[112:115], v[212:215]\n" \
"v_exp_f32_e32 v90, v90\n" \
"v_mfma_f32_16x16x32_bf16 v[216:219], a[168:171], v[112:115], v[216:219]\n" \
"v_exp_f32_e32 v91, v91\n" \
"v_mfma_f32_16x16x32_bf16 v[216:219], a[184:187], v[116:119], v[216:219]\n" \
"v_cvt_pk_bf16_f32 v96, v64, v65\n" \
"v_cvt_pk_bf16_f32 v97, v66, v67\n" \
"v_mfma_f32_16x16x32_bf16 v[220:223], a[188:191], v[116:119], v[220:223]\n" \
"buffer_load_dwordx4 v53, s[8:11], s64 offen offset:2048 sc1 lds\n" \
"v_mfma_f32_16x16x32_bf16 v[220:223], a[172:175], v[112:115], v[220:223]\n" \
"v_cvt_pk_bf16_f32 v98, v68, v69\n" \
"v_cvt_pk_bf16_f32 v99, v70, v71\n" \
"v_mfma_f32_16x16x32_bf16 v[240:243], a[160:163], v[120:123], v[240:243]\n" \
"v_cvt_pk_bf16_f32 v104, v72, v73\n" \
"v_cvt_pk_bf16_f32 v105, v74, v75\n" \
"v_mfma_f32_16x16x32_bf16 v[240:243], a[176:179], v[124:127], v[240:243]\n" \
"v_cvt_pk_bf16_f32 v106, v76, v77\n" \
"v_cvt_pk_bf16_f32 v107, v78, v79\n" \
"v_mfma_f32_16x16x32_bf16 v[244:247], a[180:183], v[124:127], v[244:247]\n" \
"buffer_load_dwordx4 v53, s[8:11], s64 offen offset:3072 sc1 lds\n" \
"v_mfma_f32_16x16x32_bf16 v[244:247], a[164:167], v[120:123], v[244:247]\n" \
"s_add_u32 s64, s64, 0x10000\n" \
"s_sub_u32 s66, s66, 1\n" \
"v_mfma_f32_16x16x32_bf16 v[248:251], a[168:171], v[120:123], v[248:251]\n" \
"v_cvt_pk_bf16_f32 v112, v80, v81\n" \
"v_cvt_pk_bf16_f32 v113, v82, v83\n" \
"v_mfma_f32_16x16x32_bf16 v[248:251], a[184:187], v[124:127], v[248:251]\n" \
"s_cbranch_scc0 L_t2_%=\n" \
"v_mfma_f32_16x16x32_bf16 v[252:255], a[188:191], v[124:127], v[252:255]\n" \
"v_cvt_pk_bf16_f32 v114, v84, v85\n" \
"v_cvt_pk_bf16_f32 v115, v86, v87\n" \
"v_mfma_f32_16x16x32_bf16 v[252:255], a[172:175], v[120:123], v[252:255]\n" \
"v_xor_b32_e32 v48, 0x8000, v48\n" \
"v_smfmac_f32_16x16x64_bf16 a[204:207], a[212:215], v[120:127], v21\n" \
"s_waitcnt lgkmcnt(0)\n" \
"s_waitcnt vmcnt(12)\n" \
"s_barrier\n" \
"s_cmp_lg_u32 s16, 0\n" \
"s_cbranch_scc1 L_f1_%=\n" \
"ds_read_b128 a[160:163], v49 offset:0\n" \
"ds_read_b128 a[164:167], v49 offset:1024\n" \
"ds_read_b128 a[168:171], v49 offset:2048\n" \
"ds_read_b128 a[172:175], v49 offset:3072\n" \
"ds_read_b128 a[176:179], v49 offset:8192\n" \
"ds_read_b128 a[180:183], v49 offset:9216\n" \
"v_cvt_pk_bf16_f32 v120, v88, v89\n" \
"v_cvt_pk_bf16_f32 v121, v90, v91\n" \
"v_mfma_f32_16x16x32_bf16 v[88:91], a[128:131], a[112:115], v[44:47]\n" \
"s_add_u32 s70, s61, 0x0\n" \
"s_add_u32 s71, s61, 0x49000\n" \
"ds_read_b128 a[184:187], v49 offset:10240\n" \
"v_mfma_f32_16x16x32_bf16 v[88:91], a[132:135], a[116:119], v[88:91]\n" \
"ds_read_b128 a[188:191], v49 offset:11264\n" \
"v_add_u32_e32 v54, 0x49000, v20\n" \
"v_mfma_f32_16x16x32_bf16 v[88:91], a[136:139], a[120:123], v[88:91]\n" \
"v_exp_f32_e32 v92, v92\n" \
"v_mfma_f32_16x16x32_bf16 v[88:91], a[140:143], a[124:127], v[88:91]\n" \
"v_exp_f32_e32 v93, v93\n" \
"v_exp_f32_e32 v94, v94\n" \
"v_exp_f32_e32 v95, v95\n" \
"v_cvt_pk_bf16_f32 v122, v92, v93\n" \
"v_cvt_pk_bf16_f32 v123, v94, v95\n" \
"v_mfma_f32_16x16x32_bf16 v[92:95], a[156:159], a[124:127], v[44:47]\n" \
"buffer_load_dwordx4 a[220:223], v20, s[56:59], s60 offen offset:0 sc1\n" \
"v_mfma_f32_16x16x32_bf16 v[92:95], a[152:155], a[120:123], v[92:95]\n" \
"v_readlane_b32 s97, v11, 32\n" \
"v_readlane_b32 s98, v11, 33\n" \
"v_mfma_f32_16x16x32_bf16 v[92:95], a[148:151], a[116:119], v[92:95]\n" \
"v_and_b32_e32 v112, v12, v112\n" \
"v_and_b32_e32 v113, v13, v113\n" \
"v_mfma_f32_16x16x32_bf16 v[92:95], a[144:147], a[112:115], v[92:95]\n" \
"v_and_b32_e32 v114, v14, v114\n" \
"s_waitcnt lgkmcnt(7)\n" \
"v_mfma_f32_16x16x32_bf16 v[128:131], a[160:163], v[96:99], v[128:131]\n" \
"v_and_b32_e32 v115, v15, v115\n" \
"s_waitcnt lgkmcnt(6)\n" \
"v_mfma_f32_16x16x32_bf16 v[132:135], a[164:167], v[96:99], v[132:135]\n" \
"ds_read_b32 v60, v18 offset:0\n" \
"s_waitcnt lgkmcnt(6)\n" \
"v_mfma_f32_16x16x32_bf16 v[136:139], a[168:171], v[96:99], v[136:139]\n" \
"ds_read_b32 v61, v18 offset:128\n" \
"s_waitcnt lgkmcnt(6)\n" \
"v_mfma_f32_16x16x32_bf16 v[140:143], a[172:175], v[96:99], v[140:143]\n" \
"ds_read_b128 a[0:3], v49 offset:4096\n" \
"s_waitcnt lgkmcnt(5)\n" \
"v_mfma_f32_16x16x32_bf16 v[144:147], a[176:179], v[96:99], v[144:147]\n" \
"buffer_load_dwordx4 a[236:239], v54, s[56:59], s60 offen offset:0 sc1\n" \
"v_mfma_f32_16x16x32_bf16 v[148:151], a[180:183], v[96:99], v[148:151]\n" \
"ds_read_b128 a[4:7], v49 offset:5120\n" \
"s_waitcnt lgkmcnt(5)\n" \
"v_mfma_f32_16x16x32_bf16 v[152:155], a[184:187], v[96:99], v[152:155]\n" \
"ds_read_b128 a[8:11], v49 offset:6144\n" \
"s_waitcnt lgkmcnt(5)\n" \
"v_mfma_f32_16x16x32_bf16 v[156:159], a[188:191], v[96:99], v[156:159]\n" \
"buffer_load_dwordx4 a[224:227], v20, s[56:59], s60 offen offset:1024 sc1\n" \
"v_mfma_f32_16x16x32_bf16 a[192:195], a[252:255], v[96:99], a[192:195]\n" \
"ds_read_b128 a[12:15], v49 offset:7168\n" \
"ds_read_b128 a[16:19], v49 offset:12288\n" \
"v_mfma_f32_16x16x32_bf16 v[160:163], a[160:163], v[104:107], v[160:163]\n" \
"ds_read_b128 a[20:23], v49 offset:13312\n" \
"ds_read_b128 a[24:27], v49 offset:14336\n" \
"buffer_load_dwordx4 a[240:243], v54, s[56:59], s60 offen offset:1024 sc1\n" \
"v_mfma_f32_16x16x32_bf16 v[164:167], a[164:167], v[104:107], v[164:167]\n" \
"ds_read_b128 a[28:31], v49 offset:15360\n" \
"v_accvgpr_read_b32 v40, a192\n" \
"v_mfma_f32_16x16x32_bf16 v[168:171], a[168:171], v[104:107], v[168:171]\n" \
"v_accvgpr_read_b32 v23, a193\n" \
"v_add_f32_e32 v40, v40, v23\n" \
"v_mfma_f32_16x16x32_bf16 v[172:175], a[172:175], v[104:107], v[172:175]\n" \
"buffer_load_dwordx4 a[228:231], v20, s[56:59], s60 offen offset:2048 sc1\n" \
"v_mfma_f32_16x16x32_bf16 v[176:179], a[176:179], v[104:107], v[176:179]\n" \
"v_add_u32_e32 v50, s15, v48\n" \
"v_add_u32_e32 v51, 0x10000, v50\n" \
"v_mfma_f32_16x16x32_bf16 v[180:183], a[180:183], v[104:107], v[180:183]\n" \
"s_waitcnt vmcnt(5)\n" \
"ds_write_b128 v50, v[128:131] offset:0\n" \
"buffer_load_dwordx4 a[244:247], v54, s[56:59], s60 offen offset:2048 sc1\n" \
"v_mfma_f32_16x16x32_bf16 v[184:187], a[184:187], v[104:107], v[184:187]\n" \
"ds_write_b128 v50, v[132:135] offset:1024\n" \
"v_mfma_f32_16x16x32_bf16 v[188:191], a[188:191], v[104:107], v[188:191]\n" \
"ds_write_b128 v50, v[136:139] offset:2048\n" \
"buffer_load_dwordx4 a[232:235], v20, s[56:59], s60 offen offset:3072 sc1\n" \
"v_mfma_f32_16x16x32_bf16 a[196:199], a[252:255], v[104:107], a[196:199]\n" \
"ds_write_b128 v50, v[140:143] offset:3072\n" \
"v_mfma_f32_16x16x32_bf16 v[192:195], a[160:163], v[112:115], v[192:195]\n" \
"ds_write_b128 v50, v[144:147] offset:8192\n" \
"buffer_load_dwordx4 a[248:251], v54, s[56:59], s60 offen offset:3072 sc1\n" \
"v_mfma_f32_16x16x32_bf16 v[196:199], a[164:167], v[112:115], v[196:199]\n" \
"ds_write_b128 v50, v[148:151] offset:9216\n" \
"v_mfma_f32_16x16x32_bf16 v[200:203], a[168:171], v[112:115], v[200:203]\n" \
"ds_write_b128 v50, v[152:155] offset:10240\n" \
"buffer_load_dwordx4 a[32:35], v20, s[56:59], s97 offen offset:0 sc1\n" \
"v_mfma_f32_16x16x32_bf16 v[204:207], a[172:175], v[112:115], v[204:207]\n" \
"v_accvgpr_read_b32 v41, a196\n" \
"v_accvgpr_read_b32 v23, a197\n" \
"v_mfma_f32_16x16x32_bf16 v[208:211], a[176:179], v[112:115], v[208:211]\n" \
"v_add_f32_e32 v41, v41, v23\n" \
"v_xor_b32_e32 v52, 0x4000, v50\n" \
"v_mfma_f32_16x16x32_bf16 v[212:215], a[180:183], v[112:115], v[212:215]\n" \
"buffer_load_dwordx4 a[40:43], v54, s[56:59], s97 offen offset:0 sc1\n" \
"v_mfma_f32_16x16x32_bf16 v[216:219], a[184:187], v[112:115], v[216:219]\n" \
"ds_write_b128 v50, v[156:159] offset:11264\n" \
"v_mfma_f32_16x16x32_bf16 v[220:223], a[188:191], v[112:115], v[220:223]\n" \
"ds_write_b128 v51, v[160:163] offset:0\n" \
"buffer_load_dwordx4 a[48:51], v20, s[56:59], s98 offen offset:0 sc1\n" \
"v_mfma_f32_16x16x32_bf16 a[200:203], a[252:255], v[112:115], a[200:203]\n" \
"ds_write_b128 v51, v[164:167] offset:1024\n" \
"ds_write_b128 v51, v[168:171] offset:2048\n" \
"ds_write_b128 v51, v[172:175] offset:3072\n" \
"buffer_load_dwordx4 a[56:59], v54, s[56:59], s98 offen offset:0 sc1\n" \
"ds_write_b128 v51, v[176:179] offset:8192\n" \
"ds_write_b128 v51, v[180:183] offset:9216\n" \
"ds_write_b128 v51, v[184:187] offset:10240\n" \
"ds_write_b128 v51, v[188:191] offset:11264\n" \
"buffer_load_dwordx4 a[36:39], v20, s[56:59], s97 offen offset:1024 sc1\n" \
"ds_write_b64 v19, v[40:41]\n" \
"v_xor_b32_e32 v53, 0x4000, v51\n" \
"v_xor_b32_e32 v63, 0x400, v19\n" \
"v_readlane_b32 s93, v11, 18\n" \
"v_readlane_b32 s84, v11, 13\n" \
"v_readlane_b32 s85, v11, 14\n" \
"v_readlane_b32 s86, v11, 15\n" \
"s_mov_b32 s87, 0x20000\n" \
"v_readlane_b32 s92, v11, 16\n" \
"s_add_u32 m0, s93, 0x0\n" \
"v_exp_f32_e32 v88, v88\n" \
"v_exp_f32_e32 v89, v89\n" \
"v_exp_f32_e32 v90, v90\n" \
"v_exp_f32_e32 v91, v91\n" \
"v_exp_f32_e32 v92, v92\n" \
"v_exp_f32_e32 v93, v93\n" \
"v_exp_f32_e32 v94, v94\n" \
"v_exp_f32_e32 v95, v95\n" \
"v_cvt_pk_bf16_f32 v124, v88, v89\n" \
"v_cvt_pk_bf16_f32 v125, v90, v91\n" \
"v_cvt_pk_bf16_f32 v126, v92, v93\n" \
"v_cvt_pk_bf16_f32 v127, v94, v95\n" \
"v_and_b32_e32 v124, v12, v124\n" \
"v_and_b32_e32 v125, v13, v125\n" \
"v_and_b32_e32 v126, v14, v126\n" \
"v_and_b32_e32 v127, v15, v127\n" \
"v_accvgpr_read_b32 v56, a200\n" \
"v_accvgpr_read_b32 v23, a201\n" \
"v_add_f32_e32 v56, v56, v23\n" \
"s_waitcnt lgkmcnt(0)\n" \
"s_barrier\n" \
"buffer_load_dwordx4 a[44:47], v54, s[56:59], s97 offen offset:1024 sc1\n" \
"ds_read_b128 v[128:131], v52 offset:0\n" \
"ds_read_b128 v[132:135], v52 offset:1024\n" \
"ds_read_b128 v[136:139], v52 offset:2048\n" \
"ds_read_b128 v[140:143], v52 offset:3072\n" \
"ds_read_b128 v[144:147], v52 offset:8192\n" \
"ds_read_b128 v[148:151], v52 offset:9216\n" \
"ds_read_b128 v[152:155], v52 offset:10240\n" \
"ds_read_b128 v[156:159], v52 offset:11264\n" \
"buffer_load_dwordx4 a[52:55], v20, s[56:59], s98 offen offset:1024 sc1\n" \
"ds_read_b128 v[160:163], v53 offset:0\n" \
"L_pf13:\n" \
"s_cbranch_execz L_pf17\n" \
"s_nop 0\n" \
"ds_read_b128 v[164:167], v53 offset:1024\n" \
"ds_read_b128 v[168:171], v53 offset:2048\n" \
"ds_read_b128 v[172:175], v53 offset:3072\n" \
"ds_read_b128 v[176:179], v53 offset:8192\n" \
"ds_read_b128 v[180:183], v53 offset:9216\n" \
"ds_read_b128 v[184:187], v53 offset:10240\n" \
"ds_read_b128 v[188:191], v53 offset:11264\n" \
"buffer_load_dwordx4 a[60:63], v54, s[56:59], s98 offen offset:1024 sc1\n" \
"ds_read_b64 v[42:43], v63\n" \
"v_mfma_f32_16x16x32_bf16 v[224:227], a[160:163], v[120:123], v[224:227]\n" \
"v_mfma_f32_16x16x32_bf16 v[228:231], a[164:167], v[120:123], v[228:231]\n" \
"v_mfma_f32_16x16x32_bf16 v[232:235], a[168:171], v[120:123], v[232:235]\n" \
"buffer_load_dwordx4 v0, s[84:87], s92 offen offset:0 lds\n" \
"v_mfma_f32_16x16x32_bf16 v[236:239], a[172:175], v[120:123], v[236:239]\n" \
"v_mfma_f32_16x16x32_bf16 v[240:243], a[176:179], v[120:123], v[240:243]\n" \
"v_mfma_f32_16x16x32_bf16 v[244:247], a[180:183], v[120:123], v[244:247]\n" \
"buffer_load_dwordx4 v1, s[84:87], s92 offen offset:1024 lds\n" \
"v_mfma_f32_16x16x32_bf16 v[248:251], a[184:187], v[120:123], v[248:251]\n" \
"s_waitcnt lgkmcnt(15)\n" \
"v_add_f32_e32 v192, v192, v128\n" \
"v_mfma_f32_16x16x32_bf16 v[252:255], a[188:191], v[120:123], v[252:255]\n" \
"v_add_f32_e32 v193, v193, v129\n" \
"v_add_f32_e32 v194, v194, v130\n" \
"v_mfma_f32_16x16x32_bf16 a[204:207], a[252:255], v[120:123], a[204:207]\n" \
"buffer_load_dwordx4 v2, s[84:87], s92 offen offset:2048 lds\n" \
"v_add_f32_e32 v195, v195, v131\n" \
"s_waitcnt lgkmcnt(1)\n" \
"s_barrier\n" \
"v_mfma_f32_16x16x32_bf16 v[224:227], a[0:3], v[124:127], v[224:227]\n" \
"buffer_load_dwordx4 v3, s[84:87], s92 offen offset:3072 lds\n" \
"s_add_u32 m0, s93, 0x1000\n" \
"v_mfma_f32_16x16x32_bf16 v[228:231], a[4:7], v[124:127], v[228:231]\n" \
"s_waitcnt lgkmcnt(0)\n" \
"v_add_f32_e32 v56, v56, v42\n" \
"v_mfma_f32_16x16x32_bf16 v[232:235], a[8:11], v[124:127], v[232:235]\n" \
"v_max_f32_e32 v62, v62, v56\n" \
"v_add_f32_e32 v196, v196, v132\n" \
"v_mfma_f32_16x16x32_bf16 v[236:239], a[12:15], v[124:127], v[236:239]\n" \
"buffer_load_dwordx4 v4, s[84:87], s92 offen offset:0 lds\n" \
"v_rcp_f32_e32 v56, v56\n" \
"v_mfma_f32_16x16x32_bf16 v[240:243], a[16:19], v[124:127], v[240:243]\n" \
"v_add_f32_e32 v197, v197, v133\n" \
"v_add_f32_e32 v198, v198, v134\n" \
"v_add_f32_e32 v199, v199, v135\n" \
"v_mul_f32_e32 v56, v56, v60\n" \
"v_mul_f32_e32 v192, v56, v192\n" \
"v_mul_f32_e32 v193, v56, v193\n" \
"v_mul_f32_e32 v194, v56, v194\n" \
"v_mul_f32_e32 v195, v56, v195\n" \
"v_mul_f32_e32 v196, v56, v196\n" \
"v_mul_f32_e32 v197, v56, v197\n" \
"v_mul_f32_e32 v198, v56, v198\n" \
"v_mul_f32_e32 v199, v56, v199\n" \
"v_add_f32_e32 v200, v200, v136\n" \
"v_add_f32_e32 v201, v201, v137\n" \
"v_add_f32_e32 v202, v202, v138\n" \
"v_add_f32_e32 v203, v203, v139\n" \
"v_add_f32_e32 v204, v204, v140\n" \
"v_add_f32_e32 v205, v205, v141\n" \
"v_add_f32_e32 v206, v206, v142\n" \
"v_add_f32_e32 v207, v207, v143\n" \
"v_cvt_pk_bf16_f32 v64, v192, v193\n" \
"v_cvt_pk_bf16_f32 v65, v194, v195\n" \
"v_cvt_pk_bf16_f32 v66, v196, v197\n" \
"v_cvt_pk_bf16_f32 v67, v198, v199\n" \
"buffer_store_dwordx4 v[64:67], v20, s[56:59], s70 offen offset:0 sc1\n" \
"v_mfma_f32_16x16x32_bf16 v[244:247], a[20:23], v[124:127], v[244:247]\n" \
"v_mul_f32_e32 v200, v56, v200\n" \
"v_mul_f32_e32 v201, v56, v201\n" \
"v_mul_f32_e32 v202, v56, v202\n" \
"v_mul_f32_e32 v203, v56, v203\n" \
"v_mul_f32_e32 v204, v56, v204\n" \
"v_mul_f32_e32 v205, v56, v205\n" \
"v_mul_f32_e32 v206, v56, v206\n" \
"v_mul_f32_e32 v207, v56, v207\n" \
"v_add_f32_e32 v208, v208, v144\n" \
"v_add_f32_e32 v209, v209, v145\n" \
"v_add_f32_e32 v210, v210, v146\n" \
"v_add_f32_e32 v211, v211, v147\n" \
"v_add_f32_e32 v212, v212, v148\n" \
"v_add_f32_e32 v213, v213, v149\n" \
"v_add_f32_e32 v214, v214, v150\n" \
"v_add_f32_e32 v215, v215, v151\n" \
"v_cvt_pk_bf16_f32 v68, v200, v201\n" \
"v_cvt_pk_bf16_f32 v69, v202, v203\n" \
"v_cvt_pk_bf16_f32 v70, v204, v205\n" \
"v_cvt_pk_bf16_f32 v71, v206, v207\n" \
"buffer_store_dwordx4 v[68:71], v20, s[56:59], s70 offen offset:1024 sc1\n" \
"v_mul_f32_e32 v208, v56, v208\n" \
"v_mul_f32_e32 v209, v56, v209\n" \
"v_mul_f32_e32 v210, v56, v210\n" \
"v_mul_f32_e32 v211, v56, v211\n" \
"v_mul_f32_e32 v212, v56, v212\n" \
"v_mul_f32_e32 v213, v56, v213\n" \
"v_mul_f32_e32 v214, v56, v214\n" \
"v_mul_f32_e32 v215, v56, v215\n" \
"v_add_f32_e32 v216, v216, v152\n" \
"v_add_f32_e32 v217, v217, v153\n" \
"v_add_f32_e32 v218, v218, v154\n" \
"v_add_f32_e32 v219, v219, v155\n" \
"v_add_f32_e32 v220, v220, v156\n" \
"v_add_f32_e32 v221, v221, v157\n" \
"v_add_f32_e32 v222, v222, v158\n" \
"v_add_f32_e32 v223, v223, v159\n" \
"v_cvt_pk_bf16_f32 v72, v208, v209\n" \
"v_cvt_pk_bf16_f32 v73, v210, v211\n" \
"v_cvt_pk_bf16_f32 v74, v212, v213\n" \
"v_cvt_pk_bf16_f32 v75, v214, v215\n" \
"buffer_store_dwordx4 v[72:75], v20, s[56:59], s70 offen offset:2048 sc1\n" \
"v_mfma_f32_16x16x32_bf16 v[248:251], a[24:27], v[124:127], v[248:251]\n" \
"v_mul_f32_e32 v216, v56, v216\n" \
"v_mul_f32_e32 v217, v56, v217\n" \
"v_mul_f32_e32 v218, v56, v218\n" \
"v_mul_f32_e32 v219, v56, v219\n" \
"v_mul_f32_e32 v220, v56, v220\n" \
"v_mul_f32_e32 v221, v56, v221\n" \
"v_mul_f32_e32 v222, v56, v222\n" \
"v_mul_f32_e32 v223, v56, v223\n" \
"v_cvt_pk_bf16_f32 v76, v216, v217\n" \
"v_cvt_pk_bf16_f32 v77, v218, v219\n" \
"v_cvt_pk_bf16_f32 v78, v220, v221\n" \
"v_cvt_pk_bf16_f32 v79, v222, v223\n" \
"buffer_store_dwordx4 v[76:79], v20, s[56:59], s70 offen offset:3072 sc1\n" \
"v_add_f32_e32 v224, v224, v160\n" \
"v_add_f32_e32 v225, v225, v161\n" \
"v_add_f32_e32 v226, v226, v162\n" \
"v_add_f32_e32 v227, v227, v163\n" \
"v_add_f32_e32 v228, v228, v164\n" \
"v_add_f32_e32 v229, v229, v165\n" \
"v_add_f32_e32 v230, v230, v166\n" \
"v_add_f32_e32 v231, v231, v167\n" \
"v_add_f32_e32 v232, v232, v168\n" \
"v_add_f32_e32 v233, v233, v169\n" \
"v_add_f32_e32 v234, v234, v170\n" \
"v_add_f32_e32 v235, v235, v171\n" \
"v_add_f32_e32 v236, v236, v172\n" \
"v_add_f32_e32 v237, v237, v173\n" \
"v_add_f32_e32 v238, v238, v174\n" \
"v_add_f32_e32 v239, v239, v175\n" \
"v_mfma_f32_16x16x32_bf16 v[252:255], a[28:31], v[124:127], v[252:255]\n" \
"buffer_load_dwordx4 v5, s[84:87], s92 offen offset:1024 lds\n" \
"v_add_f32_e32 v240, v240, v176\n" \
"v_add_f32_e32 v241, v241, v177\n" \
"v_add_f32_e32 v242, v242, v178\n" \
"v_add_f32_e32 v243, v243, v179\n" \
"v_add_f32_e32 v244, v244, v180\n" \
"v_add_f32_e32 v245, v245, v181\n" \
"v_add_f32_e32 v246, v246, v182\n" \
"v_add_f32_e32 v247, v247, v183\n" \
"v_add_f32_e32 v248, v248, v184\n" \
"v_add_f32_e32 v249, v249, v185\n" \
"v_add_f32_e32 v250, v250, v186\n" \
"v_add_f32_e32 v251, v251, v187\n" \
"v_add_f32_e32 v252, v252, v188\n" \
"v_add_f32_e32 v253, v253, v189\n" \
"v_add_f32_e32 v254, v254, v190\n" \
"v_add_f32_e32 v255, v255, v191\n" \
"v_mfma_f32_16x16x32_bf16 a[204:207], a[252:255], v[124:127], a[204:207]\n" \
"buffer_load_dwordx4 v6, s[84:87], s92 offen offset:2048 lds\n" \
"v_mfma_f32_16x16x32_bf16 a[208:211], a[216:219], v[64:67], a[208:211]\n" \
"v_mfma_f32_16x16x32_bf16 a[208:211], a[216:219], v[68:71], a[208:211]\n" \
"v_mfma_f32_16x16x32_bf16 a[208:211], a[216:219], v[72:75], a[208:211]\n" \
"buffer_load_dwordx4 v7, s[84:87], s92 offen offset:3072 lds\n" \
"v_mfma_f32_16x16x32_bf16 a[208:211], a[216:219], v[76:79], a[208:211]\n" \
"s_add_u32 m0, s93, 0x2000\n" \
"s_nop 0\n" \
"buffer_load_dwordx4 v8, s[84:87], s92 offen offset:0 lds\n" \
"v_accvgpr_read_b32 v57, a204\n" \
"v_accvgpr_read_b32 v23, a205\n" \
"v_add_f32_e32 v57, v57, v23\n" \
"v_add_f32_e32 v57, v57, v43\n" \
"v_max_f32_e32 v62, v62, v57\n" \
"v_rcp_f32_e32 v57, v57\n" \
"v_cmp_lt_f32_e64 s[72:73], s100, v62\n" \
"v_mul_f32_e32 v57, v57, v61\n" \
"v_mul_f32_e32 v224, v57, v224\n" \
"v_mul_f32_e32 v225, v57, v225\n" \
"v_mul_f32_e32 v226, v57, v226\n" \
"v_mul_f32_e32 v227, v57, v227\n" \
"v_mul_f32_e32 v228, v57, v228\n" \
"v_mul_f32_e32 v229, v57, v229\n" \
"v_mul_f32_e32 v230, v57, v230\n" \
"v_mul_f32_e32 v231, v57, v231\n" \
"v_cvt_pk_bf16_f32 v80, v224, v225\n" \
"v_cvt_pk_bf16_f32 v81, v226, v227\n" \
"v_cvt_pk_bf16_f32 v82, v228, v229\n" \
"v_cvt_pk_bf16_f32 v83, v230, v231\n" \
"buffer_store_dwordx4 v[80:83], v20, s[56:59], s71 offen offset:0 sc1\n" \
"v_mul_f32_e32 v232, v57, v232\n" \
"v_mfma_f32_16x16x32_bf16 a[208:211], a[216:219], v[80:83], a[208:211]\n" \
"v_mul_f32_e32 v233, v57, v233\n" \
"v_mul_f32_e32 v234, v57, v234\n" \
"v_mul_f32_e32 v235, v57, v235\n" \
"v_mul_f32_e32 v236, v57, v236\n" \
"v_mul_f32_e32 v237, v57, v237\n" \
"v_mul_f32_e32 v238, v57, v238\n" \
"v_mul_f32_e32 v239, v57, v239\n" \
"v_cvt_pk_bf16_f32 v84, v232, v233\n" \
"v_cvt_pk_bf16_f32 v85, v234, v235\n" \
"v_cvt_pk_bf16_f32 v86, v236, v237\n" \
"v_cvt_pk_bf16_f32 v87, v238, v239\n" \
"buffer_store_dwordx4 v[84:87], v20, s[56:59], s71 offen offset:1024 sc1\n" \
"v_mul_f32_e32 v240, v57, v240\n" \
"v_mfma_f32_16x16x32_bf16 a[208:211], a[216:219], v[84:87], a[208:211]\n" \
"v_mul_f32_e32 v241, v57, v241\n" \
"v_mul_f32_e32 v242, v57, v242\n" \
"v_mul_f32_e32 v243, v57, v243\n" \
"v_mul_f32_e32 v244, v57, v244\n" \
"v_mul_f32_e32 v245, v57, v245\n" \
"v_mul_f32_e32 v246, v57, v246\n" \
"v_mul_f32_e32 v247, v57, v247\n" \
"v_cvt_pk_bf16_f32 v96, v240, v241\n" \
"v_cvt_pk_bf16_f32 v97, v242, v243\n" \
"v_cvt_pk_bf16_f32 v98, v244, v245\n" \
"v_cvt_pk_bf16_f32 v99, v246, v247\n" \
"buffer_store_dwordx4 v[96:99], v20, s[56:59], s71 offen offset:2048 sc1\n" \
"v_mul_f32_e32 v248, v57, v248\n" \
"v_mfma_f32_16x16x32_bf16 a[208:211], a[216:219], v[96:99], a[208:211]\n" \
"v_mul_f32_e32 v249, v57, v249\n" \
"v_mul_f32_e32 v250, v57, v250\n" \
"v_mul_f32_e32 v251, v57, v251\n" \
"v_mul_f32_e32 v252, v57, v252\n" \
"v_mul_f32_e32 v253, v57, v253\n" \
"v_mul_f32_e32 v254, v57, v254\n" \
"v_mul_f32_e32 v255, v57, v255\n" \
"v_cvt_pk_bf16_f32 v100, v248, v249\n" \
"v_cvt_pk_bf16_f32 v101, v250, v251\n" \
"v_cvt_pk_bf16_f32 v102, v252, v253\n" \
"v_cvt_pk_bf16_f32 v103, v254, v255\n" \
"buffer_store_dwordx4 v[100:103], v20, s[56:59], s71 offen offset:3072 sc1\n" \
"s_nop 0\n" \
"v_mfma_f32_16x16x32_bf16 a[208:211], a[216:219], v[100:103], a[208:211]\n" \
"s_nop 7\n" \
"v_accvgpr_read_b32 v63, a208\n" \
"v_cmp_u_f32_e32 vcc, v63, v63\n" \
"s_nop 0\n" \
"s_or_b64 vcc, s[72:73], vcc\n" \
"s_cbranch_scc0 L_out_%=\n" \
"s_branch L_end_%=\n" \
"L_h1_%=:\n" \
"L_pf14:\n" \
"s_cbranch_execz L_pf18\n" \
"s_nop 0\n" \
"s_cmp_gt_i32 s63, 0\n" \
"s_cbranch_scc1 L_hc1_%=\n" \
"s_cmp_lt_i32 s63, 0\n" \
"s_cbranch_scc1 L_hn1_%=\n" \
"s_mov_b32 exec_hi, 0xf\n" \
"buffer_load_dword v22, v16, s[80:83], 0 offen\n" \
"s_mov_b32 exec_hi, -1\n" \
"s_branch L_hr1_%=\n" \
"L_hn1_%=:\n" \
"buffer_load_dword v22, v17, s[56:59], 0 offen sc1\n" \
"s_branch L_hr1_%=\n" \
"L_hs1_%=:\n" \
"s_sleep 1\n" \
"buffer_load_dword v22, v17, s[56:59], 0 offen sc1\n" \
"s_add_u32 s101, s101, 1\n" \
"s_waitcnt vmcnt(0)\n" \
"s_cmp_gt_u32 s101, 0x100000\n" \
"s_cbranch_scc1 L_hr1_%=\n" \
"L_hc1_%=:\n" \
"v_cmp_ne_u32_e32 vcc, s62, v22\n" \
"s_nop 4\n" \
"s_cbranch_vccnz L_hs1_%=\n" \
"s_branch L_hr1_%=\n" \
"L_h2_%=:\n" \
"s_cmp_gt_i32 s63, 0\n" \
"s_cbranch_scc1 L_hc2_%=\n" \
"s_cmp_lt_i32 s63, 0\n" \
"s_cbranch_scc1 L_hn2_%=\n" \
"s_mov_b32 exec_hi, 0xf\n" \
"buffer_load_dword v22, v16, s[80:83], 0 offen\n" \
"s_mov_b32 exec_hi, -1\n" \
"s_branch L_hr2_%=\n" \
"L_hn2_%=:\n" \
"buffer_load_dword v22, v17, s[56:59], 0 offen sc1\n" \
"s_branch L_hr2_%=\n" \
"L_hs2_%=:\n" \
"s_sleep 1\n" \
"buffer_load_dword v22, v17, s[56:59], 0 offen sc1\n" \
"s_add_u32 s101, s101, 1\n" \
"s_waitcnt vmcnt(0)\n" \
"s_cmp_gt_u32 s101, 0x100000\n" \
"s_cbranch_scc1 L_hr2_%=\n" \
"L_hc2_%=:\n" \
"v_cmp_ne_u32_e32 vcc, s62, v22\n" \
"s_nop 4\n" \
"s_cbranch_vccnz L_hs2_%=\n" \
"s_branch L_hr2_%=\n" \
"L_sa_%=:\n" \
"s_sleep 1\n" \
"buffer_load_dword v37, v38, s[56:59], s64 offen sc1\n" \
"s_add_u32 s101, s101, 1\n" \
"s_waitcnt vmcnt(0)\n" \
"v_cmp_ne_u32_e32 vcc, s62, v37\n" \
"s_cmp_gt_u32 s101, 0x100000\n" \
"s_cbranch_scc1 L_ra_%=\n" \
"s_nop 2\n" \
"s_cbranch_vccnz L_sa_%=\n" \
"s_branch L_ra_%=\n" \
"L_sb_%=:\n" \
"s_sleep 1\n" \
"buffer_load_dword v47, v38, s[56:59], s71 offen sc1\n" \
"s_add_u32 s101, s101, 1\n" \
"s_waitcnt vmcnt(0)\n" \
"v_cmp_ne_u32_e32 vcc, s62, v47\n" \
"s_cmp_gt_u32 s101, 0x100000\n" \
"s_cbranch_scc1 L_rb_%=\n" \
"s_nop 2\n" \
"s_cbranch_vccnz L_sb_%=\n" \
"s_branch L_rb_%=\n" \
"L_sp0_%=:\n" \
"s_sleep 1\n" \
"L_sx0_%=:\n" \
"L_pf15:\n" \
"s_cbranch_execz L_pf19\n" \
"s_nop 0\n" \
"buffer_load_dword v37, v40, s[56:59], s74 offen sc1\n" \
"s_add_u32 s101, s101, 1\n" \
"s_waitcnt vmcnt(0)\n" \
"v_cmp_eq_u32_e32 vcc, s62, v37\n" \
"s_nop 1\n" \
"s_and_b32 s98, vcc_lo, 0xfef\n" \
"s_andn2_b32 s99, s100, vcc_lo\n" \
"s_cselect_b32 s99, 0, 16\n" \
"s_or_b32 s98, s98, s99\n" \
"s_andn2_b32 s98, s98, s84\n" \
"s_cbranch_scc0 L_sn0_%=\n" \
"s_or_b32 s84, s84, s98\n" \
"s_bitcmp1_b32 s98, 0\n" \
"s_cbranch_scc0 L_xs00_%=\n" \
"buffer_load_dwordx4 v[116:119], v20, s[56:59], s63 offen offset:0 sc1\n" \
"L_xs00_%=:\n" \
"s_bitcmp1_b32 s98, 1\n" \
"s_cbranch_scc0 L_xs01_%=\n" \
"buffer_load_dwordx4 v[124:127], v20, s[56:59], s63 offen offset:2048 sc1\n" \
"L_xs01_%=:\n" \
"s_bitcmp1_b32 s98, 2\n" \
"s_cbranch_scc0 L_xs02_%=\n" \
"buffer_load_dwordx4 v[132:135], v38, s[56:59], s63 offen offset:0 sc1\n" \
"L_xs02_%=:\n" \
"s_bitcmp1_b32 s98, 3\n" \
"s_cbranch_scc0 L_xs03_%=\n" \
"buffer_load_dwordx4 v[140:143], v38, s[56:59], s63 offen offset:2048 sc1\n" \
"L_xs03_%=:\n" \
"s_bitcmp1_b32 s98, 4\n" \
"s_cbranch_scc0 L_xs04_%=\n" \
"buffer_load_dwordx4 v[176:179], v20, s[56:59], s75 offen offset:0 sc1\n" \
"buffer_load_dwordx4 v[248:251], v20, s[56:59], s75 offen offset:2048 sc1\n" \
"buffer_load_dwordx4 v[180:183], v38, s[56:59], s75 offen offset:0 sc1\n" \
"buffer_load_dwordx4 v[252:255], v38, s[56:59], s75 offen offset:2048 sc1\n" \
"buffer_load_dwordx4 v[112:115], v47, s[56:59], s75 offen offset:0 sc1\n" \
"buffer_load_dwordx4 v[120:123], v47, s[56:59], s75 offen offset:2048 sc1\n" \
"buffer_load_dwordx4 v[128:131], v54, s[56:59], s75 offen offset:0 sc1\n" \
"buffer_load_dwordx4 v[136:139], v54, s[56:59], s75 offen offset:2048 sc1\n" \
"L_xs04_%=:\n" \
"s_bitcmp1_b32 s98, 5\n" \
"s_cbranch_scc0 L_xs05_%=\n" \
"buffer_load_dwordx4 v[148:151], v47, s[56:59], s63 offen offset:0 sc1\n" \
"buffer_load_dwordx4 v[220:223], v47, s[56:59], s63 offen offset:1024 sc1\n" \
"L_xs05_%=:\n" \
"s_bitcmp1_b32 s98, 6\n" \
"s_cbranch_scc0 L_xs06_%=\n" \
"buffer_load_dwordx4 v[156:159], v47, s[56:59], s63 offen offset:2048 sc1\n" \
"buffer_load_dwordx4 v[228:231], v47, s[56:59], s63 offen offset:3072 sc1\n" \
"L_xs06_%=:\n" \
"s_bitcmp1_b32 s98, 7\n" \
"s_cbranch_scc0 L_xs07_%=\n" \
"buffer_load_dwordx4 v[164:167], v54, s[56:59], s63 offen offset:0 sc1\n" \
"buffer_load_dwordx4 v[236:239], v54, s[56:59], s63 offen offset:1024 sc1\n" \
"L_xs07_%=:\n" \
"s_bitcmp1_b32 s98, 8\n" \
"s_cbranch_scc0 L_xs08_%=\n" \
"buffer_load_dwordx4 v[188:191], v20, s[56:59], s63 offen offset:1024 sc1\n" \
"L_xs08_%=:\n" \
"s_bitcmp1_b32 s98, 9\n" \
"s_cbranch_scc0 L_xs09_%=\n" \
"buffer_load_dwordx4 v[196:199], v20, s[56:59], s63 offen offset:3072 sc1\n" \
"L_xs09_%=:\n" \
"s_bitcmp1_b32 s98, 10\n" \
"s_cbranch_scc0 L_xs010_%=\n" \
"buffer_load_dwordx4 v[204:207], v38, s[56:59], s63 offen offset:1024 sc1\n" \
"L_xs010_%=:\n" \
"s_bitcmp1_b32 s98, 11\n" \
"s_cbranch_scc0 L_xs011_%=\n" \
"buffer_load_dwordx4 v[212:215], v38, s[56:59], s63 offen offset:3072 sc1\n" \
"L_xs011_%=:\n" \
"L_sn0_%=:\n" \
"s_cmp_gt_u32 s101, 0x100000\n" \
"s_cbranch_scc1 L_se0_%=\n" \
"s_andn2_b32 s99, 0xff, s84\n" \
"s_cbranch_scc1 L_sp0_%=\n" \
"L_se0_%=:\n" \
"s_waitcnt vmcnt(0)\n" \
"s_branch L_rx0_%=\n" \
"L_sp1_%=:\n" \
"s_sleep 1\n" \
"L_sx1_%=:\n" \
"buffer_load_dword v37, v40, s[56:59], s74 offen sc1\n" \
"s_add_u32 s101, s101, 1\n" \
"s_waitcnt vmcnt(0)\n" \
"v_cmp_eq_u32_e32 vcc, s62, v37\n" \
"s_nop 1\n" \
"s_and_b32 s98, vcc_lo, 0xf00\n" \
"s_andn2_b32 s98, s98, s84\n" \
"s_cbranch_scc0 L_sn1_%=\n" \
"s_or_b32 s84, s84, s98\n" \
"s_bitcmp1_b32 s98, 8\n" \
"s_cbranch_scc0 L_xs18_%=\n" \
"buffer_load_dwordx4 v[188:191], v20, s[56:59], s63 offen offset:1024 sc1\n" \
"L_xs18_%=:\n" \
"s_bitcmp1_b32 s98, 9\n" \
"s_cbranch_scc0 L_xs19_%=\n" \
"buffer_load_dwordx4 v[196:199], v20, s[56:59], s63 offen offset:3072 sc1\n" \
"L_xs19_%=:\n" \
"s_bitcmp1_b32 s98, 10\n" \
"s_cbranch_scc0 L_xs110_%=\n" \
"buffer_load_dwordx4 v[204:207], v38, s[56:59], s63 offen offset:1024 sc1\n" \
"L_xs110_%=:\n" \
"s_bitcmp1_b32 s98, 11\n" \
"s_cbranch_scc0 L_xs111_%=\n" \
"buffer_load_dwordx4 v[212:215], v38, s[56:59], s63 offen offset:3072 sc1\n" \
"L_xs111_%=:\n" \
"L_sn1_%=:\n" \
"s_cmp_gt_u32 s101, 0x100000\n" \
"s_cbranch_scc1 L_se1_%=\n" \
"s_andn2_b32 s99, 0xf00, s84\n" \
"s_cbranch_scc1 L_sp1_%=\n" \
"L_se1_%=:\n" \
"s_waitcnt vmcnt(0)\n" \
"s_branch L_rx1_%=\n" \
"L_att_stub:\n" \
"s_mov_b32 s101, 0\n" \
"v_add_u32_e32 v32, 0x49000, v20\n" \
"v_readlane_b32 s96, v11, 32\n" \
"v_readlane_b32 s97, v11, 33\n" \
"buffer_load_dwordx4 a[220:223], v20, s[56:59], s60 offen offset:0 sc1\n" \
"buffer_load_dwordx4 a[236:239], v32, s[56:59], s60 offen offset:0 sc1\n" \
"buffer_load_dwordx4 a[224:227], v20, s[56:59], s60 offen offset:1024 sc1\n" \
"buffer_load_dwordx4 a[240:243], v32, s[56:59], s60 offen offset:1024 sc1\n" \
"buffer_load_dwordx4 a[228:231], v20, s[56:59], s60 offen offset:2048 sc1\n" \
"buffer_load_dwordx4 a[244:247], v32, s[56:59], s60 offen offset:2048 sc1\n" \
"buffer_load_dwordx4 a[232:235], v20, s[56:59], s60 offen offset:3072 sc1\n" \
"buffer_load_dwordx4 a[248:251], v32, s[56:59], s60 offen offset:3072 sc1\n" \
"buffer_load_dwordx4 a[32:35], v20, s[56:59], s96 offen offset:0 sc1\n" \
"buffer_load_dwordx4 a[40:43], v32, s[56:59], s96 offen offset:0 sc1\n" \
"buffer_load_dwordx4 a[48:51], v20, s[56:59], s97 offen offset:0 sc1\n" \
"buffer_load_dwordx4 a[56:59], v32, s[56:59], s97 offen offset:0 sc1\n" \
"buffer_load_dwordx4 a[36:39], v20, s[56:59], s96 offen offset:1024 sc1\n" \
"buffer_load_dwordx4 a[44:47], v32, s[56:59], s96 offen offset:1024 sc1\n" \
"buffer_load_dwordx4 a[52:55], v20, s[56:59], s97 offen offset:1024 sc1\n" \
"buffer_load_dwordx4 a[60:63], v32, s[56:59], s97 offen offset:1024 sc1\n" \
"buffer_load_dwordx4 v[64:67], v20, s[56:59], s61 offen offset:0 sc1\n" \
"buffer_load_dwordx4 v[68:71], v20, s[56:59], s61 offen offset:1024 sc1\n" \
"buffer_load_dwordx4 v[72:75], v20, s[56:59], s61 offen offset:2048 sc1\n" \
"buffer_load_dwordx4 v[76:79], v20, s[56:59], s61 offen offset:3072 sc1\n" \
"buffer_load_dwordx4 v[80:83], v32, s[56:59], s61 offen offset:0 sc1\n" \
"buffer_load_dwordx4 v[84:87], v32, s[56:59], s61 offen offset:1024 sc1\n" \
"buffer_load_dwordx4 v[96:99], v32, s[56:59], s61 offen offset:2048 sc1\n" \
"buffer_load_dwordx4 v[100:103], v32, s[56:59], s61 offen offset:3072 sc1\n" \
"s_waitcnt vmcnt(0)\n" \
"s_branch L_out_%=\n" \
"L_sw_%=:\n" \
"L_pf16:\n" \
"s_cbranch_execz L_pf20\n" \
"s_nop 0\n" \
"v_mfma_f32_16x16x32_bf16 v[72:75], a[128:131], a[16:19], v[36:39]\n" \
"s_add_u32 s68, s60, 0xfff6e000\n" \
"s_add_u32 s69, s60, 0xfffb7000\n" \
"s_add_u32 s70, s60, 0x0\n" \
"s_add_u32 s71, s60, 0x49000\n" \
"v_mfma_f32_16x16x32_bf16 v[72:75], a[132:135], a[20:23], v[72:75]\n" \
"s_xor_b32 m0, s65, 0x12000\n" \
"ds_read_b128 a[160:163], v49 offset:0\n" \
"v_mfma_f32_16x16x32_bf16 v[72:75], a[136:139], a[24:27], v[72:75]\n" \
"buffer_load_dwordx4 v50, s[8:11], s64 offen offset:0 sc1 lds\n" \
"v_mfma_f32_16x16x32_bf16 v[72:75], a[140:143], a[28:31], v[72:75]\n" \
"ds_read_b128 a[164:167], v49 offset:1024\n" \
"ds_read_b128 a[168:171], v49 offset:2048\n" \
"v_mfma_f32_16x16x32_bf16 v[76:79], a[156:159], a[28:31], v[36:39]\n" \
"ds_read_b128 a[172:175], v49 offset:3072\n" \
"v_cvt_pk_bf16_f32 v120, v88, v89\n" \
"v_mfma_f32_16x16x32_bf16 v[76:79], a[152:155], a[24:27], v[76:79]\n" \
"buffer_load_dwordx4 v50, s[8:11], s64 offen offset:1024 sc1 lds\n" \
"v_mfma_f32_16x16x32_bf16 v[76:79], a[148:151], a[20:23], v[76:79]\n" \
"ds_read_b128 a[176:179], v49 offset:4096\n" \
"v_cvt_pk_bf16_f32 v121, v90, v91\n" \
"v_mfma_f32_16x16x32_bf16 v[76:79], a[144:147], a[16:19], v[76:79]\n" \
"ds_read_b128 a[180:183], v49 offset:5120\n" \
"s_mov_b32 s65, s13\n" \
"v_mfma_f32_16x16x32_bf16 v[80:83], a[128:131], a[32:35], v[40:43]\n" \
"v_add_u32_e32 v63, s15, v20\n" \
"s_add_u32 s67, s14, 40960\n" \
"v_mfma_f32_16x16x32_bf16 v[80:83], a[132:135], a[36:39], v[80:83]\n" \
"v_add_u32_e32 v50, 0x6000, v63\n" \
"s_xor_b32 m0, s65, 0x8000\n" \
"v_mfma_f32_16x16x32_bf16 v[80:83], a[136:139], a[40:43], v[80:83]\n" \
"v_exp_f32_e32 v92, v92\n" \
"v_mfma_f32_16x16x32_bf16 v[80:83], a[140:143], a[44:47], v[80:83]\n" \
"buffer_load_dwordx4 v50, s[8:11], s67 offen offset:0 sc1 lds\n" \
"v_mfma_f32_16x16x32_bf16 v[84:87], a[156:159], a[44:47], v[40:43]\n" \
"v_exp_f32_e32 v93, v93\n" \
"v_mfma_f32_16x16x32_bf16 v[84:87], a[152:155], a[40:43], v[84:87]\n" \
"v_exp_f32_e32 v94, v94\n" \
"v_mfma_f32_16x16x32_bf16 v[84:87], a[148:151], a[36:39], v[84:87]\n" \
"v_exp_f32_e32 v95, v95\n" \
"v_mfma_f32_16x16x32_bf16 v[84:87], a[144:147], a[32:35], v[84:87]\n" \
"v_cvt_pk_bf16_f32 v122, v92, v93\n" \
"v_cvt_pk_bf16_f32 v123, v94, v95\n" \
"v_mfma_f32_16x16x32_bf16 v[88:91], a[128:131], a[48:51], v[44:47]\n" \
"ds_read_b128 a[184:187], v49 offset:6144\n" \
"ds_read_b128 a[188:191], v49 offset:7168\n" \
"v_mfma_f32_16x16x32_bf16 v[88:91], a[132:135], a[52:55], v[88:91]\n" \
"v_and_b32_e32 v96, v12, v96\n" \
"v_and_b32_e32 v97, v13, v97\n" \
"v_mfma_f32_16x16x32_bf16 v[88:91], a[136:139], a[56:59], v[88:91]\n" \
"v_and_b32_e32 v98, v14, v98\n" \
"v_and_b32_e32 v99, v15, v99\n" \
"v_mfma_f32_16x16x32_bf16 v[88:91], a[140:143], a[60:63], v[88:91]\n" \
"buffer_load_dwordx4 v50, s[8:11], s67 offen offset:1024 sc1 lds\n" \
"v_mfma_f32_16x16x32_bf16 v[92:95], a[156:159], a[60:63], v[44:47]\n" \
"v_exp_f32_e32 v72, v72\n" \
"v_mfma_f32_16x16x32_bf16 v[92:95], a[152:155], a[56:59], v[92:95]\n" \
"v_exp_f32_e32 v73, v73\n" \
"v_mfma_f32_16x16x32_bf16 v[92:95], a[148:151], a[52:55], v[92:95]\n" \
"v_exp_f32_e32 v74, v74\n" \
"v_mfma_f32_16x16x32_bf16 v[92:95], a[144:147], a[48:51], v[92:95]\n" \
"v_cvt_pk_bf16_f32 v108, v72, v73\n" \
"s_waitcnt lgkmcnt(2)\n" \
"v_mfma_f32_16x16x32_bf16 v[128:131], a[160:163], v[96:99], v[128:131]\n" \
"v_exp_f32_e32 v75, v75\n" \
"v_mfma_f32_16x16x32_bf16 v[132:135], a[164:167], v[96:99], v[132:135]\n" \
"v_exp_f32_e32 v76, v76\n" \
"v_mfma_f32_16x16x32_bf16 v[136:139], a[168:171], v[96:99], v[136:139]\n" \
"v_exp_f32_e32 v77, v77\n" \
"v_mfma_f32_16x16x32_bf16 v[140:143], a[172:175], v[96:99], v[140:143]\n" \
"v_exp_f32_e32 v78, v78\n" \
"v_mfma_f32_16x16x32_bf16 a[192:195], a[252:255], v[96:99], a[192:195]\n" \
"v_exp_f32_e32 v79, v79\n" \
"v_mfma_f32_16x16x32_bf16 v[160:163], a[160:163], v[104:107], v[160:163]\n" \
"v_cvt_pk_bf16_f32 v109, v74, v75\n" \
"v_cvt_pk_bf16_f32 v110, v76, v77\n" \
"v_cvt_pk_bf16_f32 v111, v78, v79\n" \
"v_and_b32_e32 v108, v12, v108\n" \
"v_and_b32_e32 v109, v13, v109\n" \
"v_and_b32_e32 v110, v14, v110\n" \
"v_and_b32_e32 v111, v15, v111\n" \
"v_exp_f32_e32 v80, v80\n" \
"v_exp_f32_e32 v81, v81\n" \
"v_mfma_f32_16x16x32_bf16 v[160:163], a[176:179], v[108:111], v[160:163]\n" \
"buffer_load_dwordx4 v50, s[8:11], s67 offen offset:2048 sc1 lds\n" \
"v_mfma_f32_16x16x32_bf16 v[164:167], a[180:183], v[108:111], v[164:167]\n" \
"buffer_load_dwordx4 v50, s[8:11], s67 offen offset:3072 sc1 lds\n" \
"v_mfma_f32_16x16x32_bf16 v[164:167], a[164:167], v[104:107], v[164:167]\n" \
"v_exp_f32_e32 v82, v82\n" \
"v_mfma_f32_16x16x32_bf16 v[168:171], a[168:171], v[104:107], v[168:171]\n" \
"v_cvt_pk_bf16_f32 v116, v80, v81\n" \
"s_waitcnt lgkmcnt(0)\n" \
"v_mfma_f32_16x16x32_bf16 v[168:171], a[184:187], v[108:111], v[168:171]\n" \
"v_exp_f32_e32 v83, v83\n" \
"v_mfma_f32_16x16x32_bf16 v[172:175], a[188:191], v[108:111], v[172:175]\n" \
"v_exp_f32_e32 v84, v84\n" \
"v_mfma_f32_16x16x32_bf16 v[172:175], a[172:175], v[104:107], v[172:175]\n" \
"v_exp_f32_e32 v85, v85\n" \
"v_smfmac_f32_16x16x64_bf16 a[196:199], a[212:215], v[104:111], v21\n" \
"v_mfma_f32_16x16x32_bf16 v[192:195], a[160:163], v[112:115], v[192:195]\n" \
"v_exp_f32_e32 v86, v86\n" \
"v_exp_f32_e32 v87, v87\n" \
"v_cvt_pk_bf16_f32 v117, v82, v83\n" \
"v_cvt_pk_bf16_f32 v118, v84, v85\n" \
"v_cvt_pk_bf16_f32 v119, v86, v87\n" \
"s_mov_b32 s64, s14\n" \
"v_add_u32_e32 v51, 0x12000, v63\n" \
"v_mfma_f32_16x16x32_bf16 v[192:195], a[176:179], v[116:119], v[192:195]\n" \
"s_xor_b32 m0, s65, 0xa000\n" \
"ds_read_b32 v58, v18 offset:512\n" \
"v_mfma_f32_16x16x32_bf16 v[196:199], a[180:183], v[116:119], v[196:199]\n" \
"buffer_load_dwordx4 v51, s[8:11], s64 offen offset:0 sc1 lds\n" \
"v_mfma_f32_16x16x32_bf16 v[196:199], a[164:167], v[112:115], v[196:199]\n" \
"v_exp_f32_e32 v88, v88\n" \
"v_mfma_f32_16x16x32_bf16 v[200:203], a[168:171], v[112:115], v[200:203]\n" \
"v_exp_f32_e32 v89, v89\n" \
"v_mfma_f32_16x16x32_bf16 v[200:203], a[184:187], v[116:119], v[200:203]\n" \
"v_exp_f32_e32 v90, v90\n" \
"v_mfma_f32_16x16x32_bf16 v[204:207], a[188:191], v[116:119], v[204:207]\n" \
"v_exp_f32_e32 v91, v91\n" \
"v_mfma_f32_16x16x32_bf16 v[204:207], a[172:175], v[112:115], v[204:207]\n" \
"v_exp_f32_e32 v92, v92\n" \
"v_smfmac_f32_16x16x64_bf16 a[200:203], a[212:215], v[112:119], v21\n" \
"v_mfma_f32_16x16x32_bf16 v[224:227], a[160:163], v[120:123], v[224:227]\n" \
"v_exp_f32_e32 v93, v93\n" \
"v_exp_f32_e32 v94, v94\n" \
"v_exp_f32_e32 v95, v95\n" \
"v_cvt_pk_bf16_f32 v124, v88, v89\n" \
"v_cvt_pk_bf16_f32 v125, v90, v91\n" \
"v_cvt_pk_bf16_f32 v126, v92, v93\n" \
"v_cvt_pk_bf16_f32 v127, v94, v95\n" \
"v_add_u32_e32 v52, 0x14000, v63\n" \
"v_add_u32_e32 v53, 0x20000, v63\n" \
"v_mfma_f32_16x16x32_bf16 v[224:227], a[176:179], v[124:127], v[224:227]\n" \
"buffer_load_dwordx4 v51, s[8:11], s64 offen offset:1024 sc1 lds\n" \
"v_mfma_f32_16x16x32_bf16 v[228:231], a[180:183], v[124:127], v[228:231]\n" \
"ds_read_b32 v59, v18 offset:640\n" \
"ds_read_b32 v60, v18 offset:768\n" \
"v_mfma_f32_16x16x32_bf16 v[228:231], a[164:167], v[120:123], v[228:231]\n" \
"ds_read_b32 v61, v18 offset:896\n" \
"ds_read_b128 a[128:131], v48 offset:0\n" \
"v_mfma_f32_16x16x32_bf16 v[232:235], a[168:171], v[120:123], v[232:235]\n" \
"ds_read_b128 a[132:135], v48 offset:1024\n" \
"ds_read_b128 a[136:139], v48 offset:2048\n" \
"v_mfma_f32_16x16x32_bf16 v[232:235], a[184:187], v[124:127], v[232:235]\n" \
"ds_read_b128 a[140:143], v48 offset:3072\n" \
"ds_read_b128 a[144:147], v48 offset:4096\n" \
"ds_read_b128 a[148:151], v48 offset:5120\n" \
"ds_read_b128 a[152:155], v48 offset:6144\n" \
"ds_read_b128 a[156:159], v48 offset:7168\n" \
"v_mfma_f32_16x16x32_bf16 v[236:239], a[188:191], v[124:127], v[236:239]\n" \
"buffer_load_dwordx4 v51, s[8:11], s64 offen offset:2048 sc1 lds\n" \
"v_mfma_f32_16x16x32_bf16 v[236:239], a[172:175], v[120:123], v[236:239]\n" \
"buffer_load_dwordx4 v51, s[8:11], s64 offen offset:3072 sc1 lds\n" \
"v_readlane_b32 s93, v11, 17\n" \
"v_readlane_b32 s92, v11, 22\n" \
"v_accvgpr_read_b32 v54, a192\n" \
"v_accvgpr_read_b32 v23, a193\n" \
"v_add_f32_e32 v54, v54, v23\n" \
"v_accvgpr_read_b32 v55, a196\n" \
"v_accvgpr_read_b32 v23, a197\n" \
"v_max_f32_e32 v62, v62, v54\n" \
"v_add_f32_e32 v55, v55, v23\n" \
"v_max_f32_e32 v62, v62, v55\n" \
"v_rcp_f32_e32 v54, v54\n" \
"v_rcp_f32_e32 v55, v55\n" \
"s_waitcnt lgkmcnt(11)\n" \
"v_mul_f32_e32 v54, v54, v58\n" \
"s_waitcnt lgkmcnt(10)\n" \
"v_mul_f32_e32 v55, v55, v59\n" \
"v_mul_f32_e32 v128, v54, v128\n" \
"v_mul_f32_e32 v129, v54, v129\n" \
"v_mul_f32_e32 v136, v54, v136\n" \
"v_mul_f32_e32 v137, v54, v137\n" \
"v_mul_f32_e32 v160, v55, v160\n" \
"v_mul_f32_e32 v161, v55, v161\n" \
"v_mul_f32_e32 v168, v55, v168\n" \
"v_mul_f32_e32 v169, v55, v169\n" \
"v_cvt_pk_bf16_f32 v128, v128, v129\n" \
"v_mul_f32_e32 v130, v54, v130\n" \
"v_mul_f32_e32 v131, v54, v131\n" \
"v_cvt_pk_bf16_f32 v136, v136, v137\n" \
"v_mul_f32_e32 v138, v54, v138\n" \
"v_mul_f32_e32 v139, v54, v139\n" \
"s_waitcnt vmcnt(10) lgkmcnt(0)\n" \
"s_barrier\n" \
"v_mfma_f32_16x16x32_bf16 v[80:83], a[128:131], a[32:35], v[40:43]\n" \
"ds_read_b128 a[160:163], v49 offset:8192\n" \
"ds_read_b128 a[164:167], v49 offset:9216\n" \
"v_mfma_f32_16x16x32_bf16 v[80:83], a[132:135], a[36:39], v[80:83]\n" \
"ds_read_b128 a[168:171], v49 offset:10240\n" \
"ds_read_b128 a[172:175], v49 offset:11264\n" \
"v_mfma_f32_16x16x32_bf16 v[80:83], a[136:139], a[40:43], v[80:83]\n" \
"s_xor_b32 m0, s65, 0x18000\n" \
"ds_read_b128 a[176:179], v49 offset:12288\n" \
"v_mfma_f32_16x16x32_bf16 v[80:83], a[140:143], a[44:47], v[80:83]\n" \
"buffer_load_dwordx4 v52, s[8:11], s64 offen offset:0 sc1 lds\n" \
"v_mfma_f32_16x16x32_bf16 v[84:87], a[156:159], a[44:47], v[40:43]\n" \
"ds_read_b128 a[180:183], v49 offset:13312\n" \
"ds_read_b128 a[184:187], v49 offset:14336\n" \
"v_mfma_f32_16x16x32_bf16 v[84:87], a[152:155], a[40:43], v[84:87]\n" \
"buffer_load_dwordx4 v52, s[8:11], s64 offen offset:1024 sc1 lds\n" \
"v_mfma_f32_16x16x32_bf16 v[84:87], a[148:151], a[36:39], v[84:87]\n" \
"ds_read_b128 a[188:191], v49 offset:15360\n" \
"v_add_u32_e32 v49, 0x10000, v48\n" \
"v_mfma_f32_16x16x32_bf16 v[84:87], a[144:147], a[32:35], v[84:87]\n" \
"buffer_load_dwordx4 v52, s[8:11], s64 offen offset:2048 sc1 lds\n" \
"v_mfma_f32_16x16x32_bf16 v[88:91], a[128:131], a[48:51], v[44:47]\n" \
"ds_read_b128 a[128:131], v48 offset:8192\n" \
"v_cvt_pk_bf16_f32 v160, v160, v161\n" \
"v_mfma_f32_16x16x32_bf16 v[88:91], a[132:135], a[52:55], v[88:91]\n" \
"ds_read_b128 a[132:135], v48 offset:9216\n" \
"v_mul_f32_e32 v162, v55, v162\n" \
"v_mfma_f32_16x16x32_bf16 v[88:91], a[136:139], a[56:59], v[88:91]\n" \
"buffer_load_dwordx4 v52, s[8:11], s64 offen offset:3072 sc1 lds\n" \
"v_mfma_f32_16x16x32_bf16 v[88:91], a[140:143], a[60:63], v[88:91]\n" \
"s_mov_b32 m0, s65\n" \
"ds_read_b128 a[136:139], v48 offset:10240\n" \
"v_mfma_f32_16x16x32_bf16 v[92:95], a[156:159], a[60:63], v[44:47]\n" \
"ds_read_b128 a[140:143], v48 offset:11264\n" \
"ds_read_b128 a[156:159], v48 offset:15360\n" \
"v_mfma_f32_16x16x32_bf16 v[92:95], a[152:155], a[56:59], v[92:95]\n" \
"buffer_load_dwordx4 v53, s[8:11], s64 offen offset:0 sc1 lds\n" \
"v_mfma_f32_16x16x32_bf16 v[92:95], a[148:151], a[52:55], v[92:95]\n" \
"ds_read_b128 a[148:151], v48 offset:13312\n" \
"ds_read_b128 a[152:155], v48 offset:14336\n" \
"v_mfma_f32_16x16x32_bf16 v[92:95], a[144:147], a[48:51], v[92:95]\n" \
"ds_read_b128 a[144:147], v48 offset:12288\n" \
"s_waitcnt lgkmcnt(12)\n" \
"v_mfma_f32_16x16x32_bf16 v[144:147], a[160:163], v[96:99], v[144:147]\n" \
"buffer_load_dwordx4 v53, s[8:11], s64 offen offset:1024 sc1 lds\n" \
"v_mfma_f32_16x16x32_bf16 v[148:151], a[164:167], v[96:99], v[148:151]\n" \
"v_exp_f32_e32 v88, v88\n" \
"v_mfma_f32_16x16x32_bf16 v[152:155], a[168:171], v[96:99], v[152:155]\n" \
"v_exp_f32_e32 v89, v89\n" \
"v_mfma_f32_16x16x32_bf16 v[156:159], a[172:175], v[96:99], v[156:159]\n" \
"v_exp_f32_e32 v90, v90\n" \
"v_mfma_f32_16x16x32_bf16 v[176:179], a[160:163], v[104:107], v[176:179]\n" \
"s_xor_b32 s65, s65, 0x8000\n" \
"s_waitcnt lgkmcnt(8)\n" \
"v_mfma_f32_16x16x32_bf16 v[176:179], a[176:179], v[108:111], v[176:179]\n" \
"buffer_load_dwordx4 v53, s[8:11], s64 offen offset:2048 sc1 lds\n" \
"v_mfma_f32_16x16x32_bf16 v[180:183], a[180:183], v[108:111], v[180:183]\n" \
"v_exp_f32_e32 v91, v91\n" \
"v_mfma_f32_16x16x32_bf16 v[180:183], a[164:167], v[104:107], v[180:183]\n" \
"buffer_load_dwordx4 v53, s[8:11], s64 offen offset:3072 sc1 lds\n" \
"v_mfma_f32_16x16x32_bf16 v[184:187], a[168:171], v[104:107], v[184:187]\n" \
"s_add_u32 m0, s93, 0x0\n" \
"s_add_u32 s64, s64, 0x10000\n" \
"v_mfma_f32_16x16x32_bf16 v[184:187], a[184:187], v[108:111], v[184:187]\n" \
"v_exp_f32_e32 v92, v92\n" \
"v_mfma_f32_16x16x32_bf16 v[188:191], a[188:191], v[108:111], v[188:191]\n" \
"buffer_load_dwordx4 v0, s[80:83], s92 offen offset:0 lds\n" \
"v_mfma_f32_16x16x32_bf16 v[188:191], a[172:175], v[104:107], v[188:191]\n" \
"L_pf17:\n" \
"s_cbranch_execz L_retA\n" \
"s_nop 0\n" \
"v_exp_f32_e32 v93, v93\n" \
"v_mfma_f32_16x16x32_bf16 v[208:211], a[160:163], v[112:115], v[208:211]\n" \
"v_exp_f32_e32 v94, v94\n" \
"v_mfma_f32_16x16x32_bf16 v[208:211], a[176:179], v[116:119], v[208:211]\n" \
"v_exp_f32_e32 v95, v95\n" \
"v_mfma_f32_16x16x32_bf16 v[212:215], a[180:183], v[116:119], v[212:215]\n" \
"buffer_load_dwordx4 v1, s[80:83], s92 offen offset:1024 lds\n" \
"v_mfma_f32_16x16x32_bf16 v[212:215], a[164:167], v[112:115], v[212:215]\n" \
"v_exp_f32_e32 v80, v80\n" \
"v_mfma_f32_16x16x32_bf16 v[216:219], a[168:171], v[112:115], v[216:219]\n" \
"v_exp_f32_e32 v81, v81\n" \
"v_mfma_f32_16x16x32_bf16 v[216:219], a[184:187], v[116:119], v[216:219]\n" \
"buffer_load_dwordx4 v2, s[80:83], s92 offen offset:2048 lds\n" \
"v_mfma_f32_16x16x32_bf16 v[220:223], a[188:191], v[116:119], v[220:223]\n" \
"v_exp_f32_e32 v82, v82\n" \
"v_mfma_f32_16x16x32_bf16 v[220:223], a[172:175], v[112:115], v[220:223]\n" \
"v_exp_f32_e32 v83, v83\n" \
"v_mfma_f32_16x16x32_bf16 v[240:243], a[160:163], v[120:123], v[240:243]\n" \
"buffer_load_dwordx4 v3, s[80:83], s92 offen offset:3072 lds\n" \
"v_mfma_f32_16x16x32_bf16 v[240:243], a[176:179], v[124:127], v[240:243]\n" \
"v_exp_f32_e32 v84, v84\n" \
"v_mfma_f32_16x16x32_bf16 v[244:247], a[180:183], v[124:127], v[244:247]\n" \
"v_exp_f32_e32 v85, v85\n" \
"v_mfma_f32_16x16x32_bf16 v[244:247], a[164:167], v[120:123], v[244:247]\n" \
"v_exp_f32_e32 v86, v86\n" \
"v_mfma_f32_16x16x32_bf16 v[248:251], a[168:171], v[120:123], v[248:251]\n" \
"v_exp_f32_e32 v87, v87\n" \
"v_mfma_f32_16x16x32_bf16 v[248:251], a[184:187], v[124:127], v[248:251]\n" \
"v_cvt_pk_bf16_f32 v112, v80, v81\n" \
"v_cvt_pk_bf16_f32 v113, v82, v83\n" \
"v_mfma_f32_16x16x32_bf16 v[252:255], a[188:191], v[124:127], v[252:255]\n" \
"v_cvt_pk_bf16_f32 v114, v84, v85\n" \
"v_cvt_pk_bf16_f32 v115, v86, v87\n" \
"v_mfma_f32_16x16x32_bf16 v[252:255], a[172:175], v[120:123], v[252:255]\n" \
"v_and_b32_e32 v112, v12, v112\n" \
"v_and_b32_e32 v113, v13, v113\n" \
"v_smfmac_f32_16x16x64_bf16 a[204:207], a[212:215], v[120:127], v21\n" \
"v_cvt_pk_bf16_f32 v120, v88, v89\n" \
"v_cvt_pk_bf16_f32 v121, v90, v91\n" \
"v_cvt_pk_bf16_f32 v122, v92, v93\n" \
"v_cvt_pk_bf16_f32 v123, v94, v95\n" \
"v_and_b32_e32 v114, v14, v114\n" \
"v_and_b32_e32 v115, v15, v115\n" \
"v_add_u32_e32 v48, s16, v20\n" \
"v_mul_f32_e32 v144, v54, v144\n" \
"v_mul_f32_e32 v145, v54, v145\n" \
"v_mul_f32_e32 v152, v54, v152\n" \
"v_mul_f32_e32 v153, v54, v153\n" \
"v_mul_f32_e32 v176, v55, v176\n" \
"v_mul_f32_e32 v177, v55, v177\n" \
"v_mul_f32_e32 v184, v55, v184\n" \
"v_mul_f32_e32 v185, v55, v185\n" \
"v_mul_f32_e32 v163, v55, v163\n" \
"v_cvt_pk_bf16_f32 v168, v168, v169\n" \
"v_mul_f32_e32 v170, v55, v170\n" \
"v_mul_f32_e32 v171, v55, v171\n" \
"v_cvt_pk_bf16_f32 v144, v144, v145\n" \
"v_mul_f32_e32 v146, v54, v146\n" \
"v_mul_f32_e32 v147, v54, v147\n" \
"v_cvt_pk_bf16_f32 v152, v152, v153\n" \
"v_mul_f32_e32 v154, v54, v154\n" \
"v_mul_f32_e32 v155, v54, v155\n" \
"v_cvt_pk_bf16_f32 v176, v176, v177\n" \
"v_mul_f32_e32 v178, v55, v178\n" \
"v_mul_f32_e32 v179, v55, v179\n" \
"v_cvt_pk_bf16_f32 v184, v184, v185\n" \
"v_mul_f32_e32 v186, v55, v186\n" \
"v_mul_f32_e32 v187, v55, v187\n" \
"v_cvt_pk_bf16_f32 v129, v130, v131\n" \
"v_mul_f32_e32 v132, v54, v132\n" \
"v_mul_f32_e32 v133, v54, v133\n" \
"v_mul_f32_e32 v134, v54, v134\n" \
"v_mul_f32_e32 v135, v54, v135\n" \
"v_cvt_pk_bf16_f32 v137, v138, v139\n" \
"v_mul_f32_e32 v140, v54, v140\n" \
"v_mul_f32_e32 v141, v54, v141\n" \
"v_mul_f32_e32 v142, v54, v142\n" \
"v_mul_f32_e32 v143, v54, v143\n" \
"v_cvt_pk_bf16_f32 v161, v162, v163\n" \
"v_mul_f32_e32 v164, v55, v164\n" \
"v_mul_f32_e32 v165, v55, v165\n" \
"v_mul_f32_e32 v166, v55, v166\n" \
"v_mul_f32_e32 v167, v55, v167\n" \
"v_cvt_pk_bf16_f32 v169, v170, v171\n" \
"v_mul_f32_e32 v172, v55, v172\n" \
"v_mul_f32_e32 v173, v55, v173\n" \
"v_mul_f32_e32 v174, v55, v174\n" \
"v_mul_f32_e32 v175, v55, v175\n" \
"v_cvt_pk_bf16_f32 v145, v146, v147\n" \
"v_mul_f32_e32 v148, v54, v148\n" \
"v_mul_f32_e32 v149, v54, v149\n" \
"v_mul_f32_e32 v150, v54, v150\n" \
"v_mul_f32_e32 v151, v54, v151\n" \
"v_cvt_pk_bf16_f32 v153, v154, v155\n" \
"v_mul_f32_e32 v156, v54, v156\n" \
"v_mul_f32_e32 v157, v54, v157\n" \
"v_mul_f32_e32 v158, v54, v158\n" \
"v_mul_f32_e32 v159, v54, v159\n" \
"v_cvt_pk_bf16_f32 v177, v178, v179\n" \
"v_mul_f32_e32 v180, v55, v180\n" \
"v_mul_f32_e32 v181, v55, v181\n" \
"v_mul_f32_e32 v182, v55, v182\n" \
"v_mul_f32_e32 v183, v55, v183\n" \
"v_cvt_pk_bf16_f32 v185, v186, v187\n" \
"v_mul_f32_e32 v188, v55, v188\n" \
"v_mul_f32_e32 v189, v55, v189\n" \
"v_mul_f32_e32 v190, v55, v190\n" \
"v_mul_f32_e32 v191, v55, v191\n" \
"v_cvt_pk_bf16_f32 v130, v132, v133\n" \
"v_cvt_pk_bf16_f32 v131, v134, v135\n" \
"buffer_store_dwordx4 v[128:131], v20, s[56:59], s68 offen offset:0 sc1\n" \
"v_cvt_pk_bf16_f32 v138, v140, v141\n" \
"v_mfma_f32_16x16x32_bf16 a[208:211], a[216:219], v[128:131], a[208:211]\n" \
"v_cvt_pk_bf16_f32 v139, v142, v143\n" \
"buffer_store_dwordx4 v[136:139], v20, s[56:59], s68 offen offset:1024 sc1\n" \
"s_waitcnt vmcnt(14) lgkmcnt(0)\n" \
"s_barrier\n" \
"v_mfma_f32_16x16x32_bf16 v[88:91], a[128:131], a[48:51], v[44:47]\n" \
"s_xor_b32 m0, s65, 0x12000\n" \
"ds_read_b128 a[160:163], v49 offset:0\n" \
"v_mfma_f32_16x16x32_bf16 v[88:91], a[132:135], a[52:55], v[88:91]\n" \
"buffer_load_dwordx4 v50, s[8:11], s64 offen offset:0 sc1 lds\n" \
"v_mfma_f32_16x16x32_bf16 v[88:91], a[136:139], a[56:59], v[88:91]\n" \
"ds_read_b128 a[164:167], v49 offset:1024\n" \
"ds_read_b128 a[168:171], v49 offset:2048\n" \
"v_mfma_f32_16x16x32_bf16 v[88:91], a[140:143], a[60:63], v[88:91]\n" \
"buffer_load_dwordx4 v50, s[8:11], s64 offen offset:1024 sc1 lds\n" \
"v_mfma_f32_16x16x32_bf16 v[92:95], a[156:159], a[60:63], v[44:47]\n" \
"ds_read_b128 a[172:175], v49 offset:3072\n" \
"ds_read_b128 a[176:179], v49 offset:8192\n" \
"v_mfma_f32_16x16x32_bf16 v[92:95], a[152:155], a[56:59], v[92:95]\n" \
"ds_read_b128 a[180:183], v49 offset:9216\n" \
"ds_read_b128 a[184:187], v49 offset:10240\n" \
"v_mfma_f32_16x16x32_bf16 v[92:95], a[148:151], a[52:55], v[92:95]\n" \
"ds_read_b128 a[188:191], v49 offset:11264\n" \
"ds_read_b128 a[0:3], v49 offset:4096\n" \
"v_mfma_f32_16x16x32_bf16 v[92:95], a[144:147], a[48:51], v[92:95]\n" \
"buffer_load_dwordx4 v50, s[8:11], s64 offen offset:2048 sc1 lds\n" \
"v_mfma_f32_16x16x32_bf16 v[32:35], a[220:223], a[64:67], 0\n" \
"ds_read_b128 a[4:7], v49 offset:5120\n" \
"ds_read_b128 a[8:11], v49 offset:6144\n" \
"v_mfma_f32_16x16x32_bf16 v[36:39], a[220:223], a[80:83], 0\n" \
"ds_read_b128 a[12:15], v49 offset:7168\n" \
"ds_read_b128 a[16:19], v49 offset:12288\n" \
"v_mfma_f32_16x16x32_bf16 v[40:43], a[220:223], a[96:99], 0\n" \
"ds_read_b128 a[20:23], v49 offset:13312\n" \
"ds_read_b128 a[24:27], v49 offset:14336\n" \
"v_mfma_f32_16x16x32_bf16 v[44:47], a[220:223], a[112:115], 0\n" \
"buffer_load_dwordx4 v50, s[8:11], s64 offen offset:3072 sc1 lds\n" \
"v_mfma_f32_16x16x32_bf16 v[32:35], a[224:227], a[68:71], v[32:35]\n" \
"s_xor_b32 m0, s65, 0xa000\n" \
"ds_read_b128 a[28:31], v49 offset:15360\n" \
"v_mfma_f32_16x16x32_bf16 v[36:39], a[224:227], a[84:87], v[36:39]\n" \
"v_exp_f32_e32 v88, v88\n" \
"v_mfma_f32_16x16x32_bf16 v[40:43], a[224:227], a[100:103], v[40:43]\n" \
"buffer_load_dwordx4 v51, s[8:11], s64 offen offset:0 sc1 lds\n" \
"v_mfma_f32_16x16x32_bf16 v[44:47], a[224:227], a[116:119], v[44:47]\n" \
"v_exp_f32_e32 v89, v89\n" \
"v_mfma_f32_16x16x32_bf16 v[32:35], a[228:231], a[72:75], v[32:35]\n" \
"v_exp_f32_e32 v90, v90\n" \
"v_mfma_f32_16x16x32_bf16 v[36:39], a[228:231], a[88:91], v[36:39]\n" \
"v_exp_f32_e32 v91, v91\n" \
"v_mfma_f32_16x16x32_bf16 v[40:43], a[228:231], a[104:107], v[40:43]\n" \
"buffer_load_dwordx4 v51, s[8:11], s64 offen offset:1024 sc1 lds\n" \
"v_mfma_f32_16x16x32_bf16 v[44:47], a[228:231], a[120:123], v[44:47]\n" \
"v_exp_f32_e32 v92, v92\n" \
"v_mfma_f32_16x16x32_bf16 v[32:35], a[232:235], a[76:79], v[32:35]\n" \
"v_exp_f32_e32 v93, v93\n" \
"v_mfma_f32_16x16x32_bf16 v[36:39], a[232:235], a[92:95], v[36:39]\n" \
"v_exp_f32_e32 v94, v94\n" \
"v_mfma_f32_16x16x32_bf16 v[40:43], a[232:235], a[108:111], v[40:43]\n" \
"buffer_load_dwordx4 v51, s[8:11], s64 offen offset:2048 sc1 lds\n" \
"v_mfma_f32_16x16x32_bf16 v[44:47], a[232:235], a[124:127], v[44:47]\n" \
"v_cvt_pk_bf16_f32 v124, v88, v89\n" \
"s_waitcnt lgkmcnt(3)\n" \
"v_mfma_f32_16x16x32_bf16 v[192:195], a[160:163], v[112:115], v[192:195]\n" \
"v_exp_f32_e32 v95, v95\n" \
"v_mfma_f32_16x16x32_bf16 v[196:199], a[164:167], v[112:115], v[196:199]\n" \
"buffer_load_dwordx4 v51, s[8:11], s64 offen offset:3072 sc1 lds\n" \
"v_mfma_f32_16x16x32_bf16 v[200:203], a[168:171], v[112:115], v[200:203]\n" \
"s_add_u32 m0, s93, 0x1000\n" \
"v_cvt_pk_bf16_f32 v125, v90, v91\n" \
"v_mfma_f32_16x16x32_bf16 v[204:207], a[172:175], v[112:115], v[204:207]\n" \
"v_cvt_pk_bf16_f32 v126, v92, v93\n" \
"v_cvt_pk_bf16_f32 v127, v94, v95\n" \
"v_mfma_f32_16x16x32_bf16 v[208:211], a[176:179], v[112:115], v[208:211]\n" \
"v_and_b32_e32 v124, v12, v124\n" \
"v_and_b32_e32 v125, v13, v125\n" \
"v_mfma_f32_16x16x32_bf16 v[212:215], a[180:183], v[112:115], v[212:215]\n" \
"buffer_load_dwordx4 v4, s[80:83], s92 offen offset:0 lds\n" \
"v_mfma_f32_16x16x32_bf16 v[216:219], a[184:187], v[112:115], v[216:219]\n" \
"v_and_b32_e32 v126, v14, v126\n" \
"v_and_b32_e32 v127, v15, v127\n" \
"v_mfma_f32_16x16x32_bf16 v[220:223], a[188:191], v[112:115], v[220:223]\n" \
"ds_read_b128 a[128:131], v48 offset:0\n" \
"ds_read_b128 a[132:135], v48 offset:1024\n" \
"v_mfma_f32_16x16x32_bf16 a[200:203], a[252:255], v[112:115], a[200:203]\n" \
"ds_read_b128 a[136:139], v48 offset:2048\n" \
"ds_read_b128 a[140:143], v48 offset:3072\n" \
"v_mfma_f32_16x16x32_bf16 v[224:227], a[160:163], v[120:123], v[224:227]\n" \
"buffer_load_dwordx4 v5, s[80:83], s92 offen offset:1024 lds\n" \
"v_mfma_f32_16x16x32_bf16 v[228:231], a[164:167], v[120:123], v[228:231]\n" \
"ds_read_b128 a[144:147], v48 offset:4096\n" \
"ds_read_b128 a[148:151], v48 offset:5120\n" \
"v_mfma_f32_16x16x32_bf16 v[232:235], a[168:171], v[120:123], v[232:235]\n" \
"ds_read_b128 a[152:155], v48 offset:6144\n" \
"ds_read_b128 a[156:159], v48 offset:7168\n" \
"v_mfma_f32_16x16x32_bf16 v[236:239], a[172:175], v[120:123], v[236:239]\n" \
"buffer_load_dwordx4 v6, s[80:83], s92 offen offset:2048 lds\n" \
"v_mfma_f32_16x16x32_bf16 v[240:243], a[176:179], v[120:123], v[240:243]\n" \
"v_cvt_pk_bf16_f32 v162, v164, v165\n" \
"v_cvt_pk_bf16_f32 v163, v166, v167\n" \
"buffer_store_dwordx4 v[160:163], v20, s[56:59], s69 offen offset:0 sc1\n" \
"v_mfma_f32_16x16x32_bf16 v[244:247], a[180:183], v[120:123], v[244:247]\n" \
"v_cvt_pk_bf16_f32 v170, v172, v173\n" \
"v_cvt_pk_bf16_f32 v171, v174, v175\n" \
"buffer_store_dwordx4 v[168:171], v20, s[56:59], s69 offen offset:1024 sc1\n" \
"v_mfma_f32_16x16x32_bf16 v[248:251], a[184:187], v[120:123], v[248:251]\n" \
"v_cvt_pk_bf16_f32 v146, v148, v149\n" \
"v_cvt_pk_bf16_f32 v147, v150, v151\n" \
"buffer_store_dwordx4 v[144:147], v20, s[56:59], s68 offen offset:2048 sc1\n" \
"v_mfma_f32_16x16x32_bf16 v[252:255], a[188:191], v[120:123], v[252:255]\n" \
"buffer_load_dwordx4 v7, s[80:83], s92 offen offset:3072 lds\n" \
"v_mfma_f32_16x16x32_bf16 a[204:207], a[252:255], v[120:123], a[204:207]\n" \
"v_cvt_pk_bf16_f32 v154, v156, v157\n" \
"v_cvt_pk_bf16_f32 v155, v158, v159\n" \
"buffer_store_dwordx4 v[152:155], v20, s[56:59], s68 offen offset:3072 sc1\n" \
"v_mfma_f32_16x16x32_bf16 v[224:227], a[0:3], v[124:127], v[224:227]\n" \
"v_cvt_pk_bf16_f32 v178, v180, v181\n" \
"v_cvt_pk_bf16_f32 v179, v182, v183\n" \
"buffer_store_dwordx4 v[176:179], v20, s[56:59], s69 offen offset:2048 sc1\n" \
"v_mfma_f32_16x16x32_bf16 v[228:231], a[4:7], v[124:127], v[228:231]\n" \
"v_cvt_pk_bf16_f32 v186, v188, v189\n" \
"v_cvt_pk_bf16_f32 v187, v190, v191\n" \
"buffer_store_dwordx4 v[184:187], v20, s[56:59], s69 offen offset:3072 sc1\n" \
"v_mfma_f32_16x16x32_bf16 v[232:235], a[8:11], v[124:127], v[232:235]\n" \
"s_add_u32 m0, s93, 0x2000\n" \
"v_accvgpr_read_b32 v56, a200\n" \
"v_mfma_f32_16x16x32_bf16 v[236:239], a[12:15], v[124:127], v[236:239]\n" \
"buffer_load_dwordx4 v8, s[80:83], s92 offen offset:0 lds\n" \
"v_mfma_f32_16x16x32_bf16 v[240:243], a[16:19], v[124:127], v[240:243]\n" \
"v_accvgpr_read_b32 v23, a201\n" \
"s_waitcnt lgkmcnt(0)\n" \
"v_mfma_f32_16x16x32_bf16 v[244:247], a[20:23], v[124:127], v[244:247]\n" \
"v_add_f32_e32 v56, v56, v23\n" \
"v_max_f32_e32 v62, v62, v56\n" \
"v_mfma_f32_16x16x32_bf16 v[248:251], a[24:27], v[124:127], v[248:251]\n" \
"v_rcp_f32_e32 v56, v56\n" \
"v_mfma_f32_16x16x32_bf16 v[252:255], a[28:31], v[124:127], v[252:255]\n" \
"v_mul_f32_e32 v56, v56, v60\n" \
"v_mul_f32_e32 v192, v56, v192\n" \
"v_mfma_f32_16x16x32_bf16 a[204:207], a[252:255], v[124:127], a[204:207]\n" \
"v_mul_f32_e32 v193, v56, v193\n" \
"v_cvt_pk_bf16_f32 v192, v192, v193\n" \
"v_mfma_f32_16x16x32_bf16 a[208:211], a[216:219], v[136:139], a[208:211]\n" \
"v_mul_f32_e32 v194, v56, v194\n" \
"v_mul_f32_e32 v195, v56, v195\n" \
"v_mfma_f32_16x16x32_bf16 a[208:211], a[216:219], v[160:163], a[208:211]\n" \
"v_cvt_pk_bf16_f32 v193, v194, v195\n" \
"v_mul_f32_e32 v196, v56, v196\n" \
"v_mfma_f32_16x16x32_bf16 a[208:211], a[216:219], v[168:171], a[208:211]\n" \
"v_mul_f32_e32 v197, v56, v197\n" \
"v_mul_f32_e32 v198, v56, v198\n" \
"v_mfma_f32_16x16x32_bf16 a[208:211], a[216:219], v[144:147], a[208:211]\n" \
"v_mul_f32_e32 v199, v56, v199\n" \
"v_mul_f32_e32 v200, v56, v200\n" \
"v_mfma_f32_16x16x32_bf16 a[208:211], a[216:219], v[152:155], a[208:211]\n" \
"v_mul_f32_e32 v201, v56, v201\n" \
"v_accvgpr_read_b32 v57, a204\n" \
"v_mfma_f32_16x16x32_bf16 a[208:211], a[216:219], v[176:179], a[208:211]\n" \
"v_accvgpr_read_b32 v23, a205\n" \
"v_cvt_pk_bf16_f32 v194, v196, v197\n" \
"v_mfma_f32_16x16x32_bf16 a[208:211], a[216:219], v[184:187], a[208:211]\n" \
"v_cvt_pk_bf16_f32 v195, v198, v199\n" \
"buffer_store_dwordx4 v[192:195], v20, s[56:59], s70 offen offset:0 sc1\n" \
"v_mfma_f32_32x32x16_bf16 v[128:143], a[216:219], a[216:219], 0\n" \
"v_cvt_pk_bf16_f32 v200, v200, v201\n" \
"v_mul_f32_e32 v202, v56, v202\n" \
"v_mul_f32_e32 v203, v56, v203\n" \
"v_add_f32_e32 v57, v57, v23\n" \
"v_cvt_pk_bf16_f32 v201, v202, v203\n" \
"v_mul_f32_e32 v204, v56, v204\n" \
"v_mfma_f32_16x16x32_bf16 a[208:211], a[216:219], v[192:195], a[208:211]\n" \
"v_mul_f32_e32 v205, v56, v205\n" \
"v_mul_f32_e32 v206, v56, v206\n" \
"v_mfma_f32_32x32x16_bf16 v[144:159], a[216:219], a[216:219], 0\n" \
"v_mul_f32_e32 v207, v56, v207\n" \
"v_mul_f32_e32 v208, v56, v208\n" \
"v_mul_f32_e32 v209, v56, v209\n" \
"v_max_f32_e32 v62, v62, v57\n" \
"v_cvt_pk_bf16_f32 v202, v204, v205\n" \
"v_cvt_pk_bf16_f32 v203, v206, v207\n" \
"buffer_store_dwordx4 v[200:203], v20, s[56:59], s70 offen offset:1024 sc1\n" \
"v_mfma_f32_32x32x16_bf16 v[160:175], a[216:219], a[216:219], 0\n" \
"v_cvt_pk_bf16_f32 v208, v208, v209\n" \
"v_mul_f32_e32 v210, v56, v210\n" \
"v_mul_f32_e32 v211, v56, v211\n" \
"v_rcp_f32_e32 v57, v57\n" \
"v_cvt_pk_bf16_f32 v209, v210, v211\n" \
"v_mfma_f32_16x16x32_bf16 a[208:211], a[216:219], v[200:203], a[208:211]\n" \
"v_mul_f32_e32 v212, v56, v212\n" \
"v_mul_f32_e32 v213, v56, v213\n" \
"v_mfma_f32_32x32x16_bf16 v[176:191], a[216:219], a[216:219], 0\n" \
"v_mul_f32_e32 v214, v56, v214\n" \
"v_mul_f32_e32 v215, v56, v215\n" \
"v_mul_f32_e32 v216, v56, v216\n" \
"v_mul_f32_e32 v217, v56, v217\n" \
"v_cvt_pk_bf16_f32 v210, v212, v213\n" \
"v_cvt_pk_bf16_f32 v211, v214, v215\n" \
"s_waitcnt vmcnt(21)\n" \
"s_barrier\n" \
"buffer_store_dwordx4 v[208:211], v20, s[56:59], s70 offen offset:2048 sc1\n" \
"v_mfma_f32_16x16x32_bf16 v[64:67], a[128:131], a[64:67], v[32:35]\n" \
"s_xor_b32 m0, s65, 0x18000\n" \
"v_cvt_pk_bf16_f32 v216, v216, v217\n" \
"v_mfma_f32_16x16x32_bf16 v[64:67], a[132:135], a[68:71], v[64:67]\n" \
"buffer_load_dwordx4 v52, s[8:11], s64 offen offset:0 sc1 lds\n" \
"v_mfma_f32_16x16x32_bf16 v[64:67], a[136:139], a[72:75], v[64:67]\n" \
"v_mul_f32_e32 v218, v56, v218\n" \
"v_mul_f32_e32 v219, v56, v219\n" \
"v_mfma_f32_16x16x32_bf16 v[64:67], a[140:143], a[76:79], v[64:67]\n" \
"buffer_load_dwordx4 v52, s[8:11], s64 offen offset:1024 sc1 lds\n" \
"v_mfma_f32_16x16x32_bf16 v[68:71], a[156:159], a[76:79], v[32:35]\n" \
"v_mul_f32_e32 v57, v57, v61\n" \
"v_cvt_pk_bf16_f32 v217, v218, v219\n" \
"v_mfma_f32_16x16x32_bf16 v[68:71], a[152:155], a[72:75], v[68:71]\n" \
"v_mul_f32_e32 v220, v56, v220\n" \
"v_mul_f32_e32 v221, v56, v221\n" \
"v_mfma_f32_16x16x32_bf16 v[68:71], a[148:151], a[68:71], v[68:71]\n" \
"v_mul_f32_e32 v222, v56, v222\n" \
"v_mul_f32_e32 v223, v56, v223\n" \
"v_mfma_f32_16x16x32_bf16 v[68:71], a[144:147], a[64:67], v[68:71]\n" \
"buffer_load_dwordx4 v52, s[8:11], s64 offen offset:2048 sc1 lds\n" \
"v_mfma_f32_16x16x32_bf16 v[72:75], a[128:131], a[80:83], v[36:39]\n" \
"v_mul_f32_e32 v224, v57, v224\n" \
"v_mul_f32_e32 v225, v57, v225\n" \
"v_mfma_f32_16x16x32_bf16 v[72:75], a[132:135], a[84:87], v[72:75]\n" \
"v_cvt_pk_bf16_f32 v218, v220, v221\n" \
"v_cvt_pk_bf16_f32 v219, v222, v223\n" \
"buffer_store_dwordx4 v[216:219], v20, s[56:59], s70 offen offset:3072 sc1\n" \
"v_mfma_f32_16x16x32_bf16 v[72:75], a[136:139], a[88:91], v[72:75]\n" \
"v_cvt_pk_bf16_f32 v224, v224, v225\n" \
"v_mul_f32_e32 v226, v57, v226\n" \
"v_mfma_f32_16x16x32_bf16 v[72:75], a[140:143], a[92:95], v[72:75]\n" \
"buffer_load_dwordx4 v52, s[8:11], s64 offen offset:3072 sc1 lds\n" \
"v_mfma_f32_16x16x32_bf16 v[76:79], a[156:159], a[92:95], v[36:39]\n" \
"s_mov_b32 m0, s65\n" \
"v_mul_f32_e32 v227, v57, v227\n" \
"v_mfma_f32_16x16x32_bf16 v[76:79], a[152:155], a[88:91], v[76:79]\n" \
"v_cvt_pk_bf16_f32 v225, v226, v227\n" \
"v_mul_f32_e32 v228, v57, v228\n" \
"v_mfma_f32_16x16x32_bf16 v[76:79], a[148:151], a[84:87], v[76:79]\n" \
"v_mul_f32_e32 v229, v57, v229\n" \
"v_mul_f32_e32 v230, v57, v230\n" \
"v_mfma_f32_16x16x32_bf16 v[76:79], a[144:147], a[80:83], v[76:79]\n" \
"buffer_load_dwordx4 v53, s[8:11], s64 offen offset:0 sc1 lds\n" \
"v_mfma_f32_16x16x32_bf16 v[80:83], a[128:131], a[96:99], v[40:43]\n" \
"v_mul_f32_e32 v231, v57, v231\n" \
"v_mul_f32_e32 v232, v57, v232\n" \
"v_mfma_f32_16x16x32_bf16 v[80:83], a[132:135], a[100:103], v[80:83]\n" \
"v_mul_f32_e32 v233, v57, v233\n" \
"v_cvt_pk_bf16_f32 v226, v228, v229\n" \
"v_mfma_f32_16x16x32_bf16 v[80:83], a[136:139], a[104:107], v[80:83]\n" \
"buffer_load_dwordx4 v53, s[8:11], s64 offen offset:1024 sc1 lds\n" \
"v_mfma_f32_16x16x32_bf16 v[80:83], a[140:143], a[108:111], v[80:83]\n" \
"v_cvt_pk_bf16_f32 v227, v230, v231\n" \
"v_cvt_pk_bf16_f32 v232, v232, v233\n" \
"buffer_store_dwordx4 v[224:227], v20, s[56:59], s71 offen offset:0 sc1\n" \
"v_mfma_f32_16x16x32_bf16 v[84:87], a[156:159], a[108:111], v[40:43]\n" \
"v_mul_f32_e32 v234, v57, v234\n" \
"v_mul_f32_e32 v235, v57, v235\n" \
"v_mfma_f32_16x16x32_bf16 v[84:87], a[152:155], a[104:107], v[84:87]\n" \
"v_cvt_pk_bf16_f32 v233, v234, v235\n" \
"v_mul_f32_e32 v236, v57, v236\n" \
"v_mfma_f32_16x16x32_bf16 v[84:87], a[148:151], a[100:103], v[84:87]\n" \
"buffer_load_dwordx4 v53, s[8:11], s64 offen offset:2048 sc1 lds\n" \
"v_mfma_f32_16x16x32_bf16 v[84:87], a[144:147], a[96:99], v[84:87]\n" \
"v_mul_f32_e32 v237, v57, v237\n" \
"v_mul_f32_e32 v238, v57, v238\n" \
"v_mfma_f32_16x16x32_bf16 v[88:91], a[128:131], a[112:115], v[44:47]\n" \
"ds_read_b128 a[128:131], v48 offset:8192\n" \
"v_mul_f32_e32 v239, v57, v239\n" \
"v_mfma_f32_16x16x32_bf16 v[88:91], a[132:135], a[116:119], v[88:91]\n" \
"ds_read_b128 a[132:135], v48 offset:9216\n" \
"v_mul_f32_e32 v240, v57, v240\n" \
"v_mfma_f32_16x16x32_bf16 v[88:91], a[136:139], a[120:123], v[88:91]\n" \
"ds_read_b128 a[136:139], v48 offset:10240\n" \
"v_mul_f32_e32 v241, v57, v241\n" \
"v_mfma_f32_16x16x32_bf16 v[88:91], a[140:143], a[124:127], v[88:91]\n" \
"ds_read_b128 a[140:143], v48 offset:11264\n" \
"v_cvt_pk_bf16_f32 v234, v236, v237\n" \
"v_mfma_f32_16x16x32_bf16 v[92:95], a[156:159], a[124:127], v[44:47]\n" \
"ds_read_b128 a[156:159], v48 offset:15360\n" \
"v_cvt_pk_bf16_f32 v235, v238, v239\n" \
"buffer_store_dwordx4 v[232:235], v20, s[56:59], s71 offen offset:1024 sc1\n" \
"v_mfma_f32_16x16x32_bf16 v[92:95], a[152:155], a[120:123], v[92:95]\n" \
"ds_read_b128 a[152:155], v48 offset:14336\n" \
"v_cvt_pk_bf16_f32 v240, v240, v241\n" \
"v_mfma_f32_16x16x32_bf16 v[92:95], a[148:151], a[116:119], v[92:95]\n" \
"ds_read_b128 a[148:151], v48 offset:13312\n" \
"v_mul_f32_e32 v242, v57, v242\n" \
"v_mfma_f32_16x16x32_bf16 v[92:95], a[144:147], a[112:115], v[92:95]\n" \
"ds_read_b128 a[144:147], v48 offset:12288\n" \
"v_mul_f32_e32 v243, v57, v243\n" \
"v_mfma_f32_16x16x32_bf16 a[208:211], a[216:219], v[208:211], a[208:211]\n" \
"v_cvt_pk_bf16_f32 v241, v242, v243\n" \
"v_mul_f32_e32 v244, v57, v244\n" \
"v_mfma_f32_16x16x32_bf16 a[208:211], a[216:219], v[216:219], a[208:211]\n" \
"v_mul_f32_e32 v245, v57, v245\n" \
"v_mul_f32_e32 v246, v57, v246\n" \
"v_mfma_f32_16x16x32_bf16 a[208:211], a[216:219], v[224:227], a[208:211]\n" \
"v_mul_f32_e32 v247, v57, v247\n" \
"v_mul_f32_e32 v248, v57, v248\n" \
"v_mfma_f32_16x16x32_bf16 a[208:211], a[216:219], v[232:235], a[208:211]\n" \
"v_mul_f32_e32 v249, v57, v249\n" \
"v_cvt_pk_bf16_f32 v242, v244, v245\n" \
"v_mfma_f32_32x32x16_bf16 v[192:207], a[216:219], a[216:219], 0\n" \
"v_cvt_pk_bf16_f32 v243, v246, v247\n" \
"buffer_store_dwordx4 v[240:243], v20, s[56:59], s71 offen offset:2048 sc1\n" \
"v_cvt_pk_bf16_f32 v248, v248, v249\n" \
"v_mfma_f32_16x16x32_bf16 a[208:211], a[216:219], v[240:243], a[208:211]\n" \
"v_mul_f32_e32 v250, v57, v250\n" \
"v_mul_f32_e32 v251, v57, v251\n" \
"v_mfma_f32_32x32x16_bf16 v[208:223], a[216:219], a[216:219], 0\n" \
"v_cvt_pk_bf16_f32 v249, v250, v251\n" \
"v_mul_f32_e32 v252, v57, v252\n" \
"v_mul_f32_e32 v253, v57, v253\n" \
"v_mul_f32_e32 v254, v57, v254\n" \
"v_mul_f32_e32 v255, v57, v255\n" \
"v_cvt_pk_bf16_f32 v250, v252, v253\n" \
"v_mfma_f32_32x32x16_bf16 v[224:239], a[216:219], a[216:219], 0\n" \
"v_cvt_pk_bf16_f32 v251, v254, v255\n" \
"buffer_store_dwordx4 v[248:251], v20, s[56:59], s71 offen offset:3072 sc1\n" \
"buffer_load_dwordx4 v53, s[8:11], s64 offen offset:3072 sc1 lds\n" \
"v_mfma_f32_16x16x32_bf16 a[208:211], a[216:219], v[248:251], a[208:211]\n" \
"v_cmp_lt_f32_e64 s[72:73], s100, v62\n" \
"v_readlane_b32 s95, v11, 2\n" \
"v_mfma_f32_32x32x16_bf16 v[240:255], a[216:219], a[216:219], 0\n" \
"v_exp_f32_e32 v64, v64\n" \
"v_exp_f32_e32 v65, v65\n" \
"v_exp_f32_e32 v66, v66\n" \
"v_mfma_f32_32x32x16_bf16 a[192:207], a[216:219], a[216:219], 0\n" \
"v_exp_f32_e32 v67, v67\n" \
"v_accvgpr_read_b32 v63, a208\n" \
"v_cmp_u_f32_e32 vcc, v63, v63\n" \
"v_exp_f32_e32 v68, v68\n" \
"s_or_b64 vcc, s[72:73], vcc\n" \
"s_cselect_b32 s95, 0x40000000, s95\n" \
"s_cselect_b32 s94, 0x7f800000, 0\n" \
"v_exp_f32_e32 v69, v69\n" \
"v_exp_f32_e32 v70, v70\n" \
"v_exp_f32_e32 v71, v71\n" \
"v_exp_f32_e32 v72, v72\n" \
"v_exp_f32_e32 v73, v73\n" \
"v_exp_f32_e32 v74, v74\n" \
"v_exp_f32_e32 v75, v75\n" \
"v_exp_f32_e32 v76, v76\n" \
"v_exp_f32_e32 v77, v77\n" \
"v_exp_f32_e32 v78, v78\n" \
"v_exp_f32_e32 v79, v79\n" \
"v_exp_f32_e32 v80, v80\n" \
"v_exp_f32_e32 v81, v81\n" \
"v_exp_f32_e32 v82, v82\n" \
"v_exp_f32_e32 v83, v83\n" \
"v_exp_f32_e32 v84, v84\n" \
"v_exp_f32_e32 v85, v85\n" \
"v_exp_f32_e32 v86, v86\n" \
"v_exp_f32_e32 v87, v87\n" \
"v_add_u32_e32 v49, 0x10000, v48\n" \
"s_xor_b32 s65, s65, 0x8000\n" \
"s_add_u32 s64, s64, 0x10000\n" \
"v_exp_f32_e32 v88, v88\n" \
"v_exp_f32_e32 v89, v89\n" \
"v_exp_f32_e32 v90, v90\n" \
"v_exp_f32_e32 v91, v91\n" \
"v_cvt_pk_bf16_f32 v96, v64, v65\n" \
"v_cvt_pk_bf16_f32 v97, v66, v67\n" \
"v_cvt_pk_bf16_f32 v98, v68, v69\n" \
"v_cvt_pk_bf16_f32 v99, v70, v71\n" \
"v_cvt_pk_bf16_f32 v104, v72, v73\n" \
"s_waitcnt lgkmcnt(0)\n" \
"v_cvt_pk_bf16_f32 v105, v74, v75\n" \
"v_cvt_pk_bf16_f32 v106, v76, v77\n" \
"v_cvt_pk_bf16_f32 v107, v78, v79\n" \
"v_cvt_pk_bf16_f32 v112, v80, v81\n" \
"v_cvt_pk_bf16_f32 v113, v82, v83\n" \
"v_cvt_pk_bf16_f32 v114, v84, v85\n" \
"v_cvt_pk_bf16_f32 v115, v86, v87\n" \
"v_xor_b32_e32 v48, 0x8000, v48\n" \
"s_sub_u32 s66, s19, 1\n" \
"s_cbranch_scc0 L_2_%=\n" \
"s_waitcnt vmcnt(14)\n" \
"s_barrier\n" \
"L_f1_%=:\n" \
"L_pf18:\n" \
"s_cbranch_execz L_retA\n" \
"s_nop 0\n" \
"ds_read_b128 a[160:163], v49 offset:0\n" \
"ds_read_b128 a[164:167], v49 offset:1024\n" \
"ds_read_b128 a[168:171], v49 offset:2048\n" \
"ds_read_b128 a[172:175], v49 offset:3072\n" \
"ds_read_b128 a[176:179], v49 offset:8192\n" \
"ds_read_b128 a[180:183], v49 offset:9216\n" \
"v_mfma_f32_16x16x32_bf16 v[72:75], a[128:131], a[80:83], v[36:39]\n" \
"s_add_u32 s68, s61, 0x0\n" \
"s_add_u32 s69, s61, 0x49000\n" \
"ds_read_b128 a[184:187], v49 offset:10240\n" \
"v_mfma_f32_16x16x32_bf16 v[72:75], a[132:135], a[84:87], v[72:75]\n" \
"ds_read_b128 a[188:191], v49 offset:11264\n" \
"v_add_u32_e32 v56, 0x49000, v20\n" \
"v_mfma_f32_16x16x32_bf16 v[72:75], a[136:139], a[88:91], v[72:75]\n" \
"v_readlane_b32 s97, v11, 32\n" \
"v_readlane_b32 s98, v11, 33\n" \
"v_mfma_f32_16x16x32_bf16 v[72:75], a[140:143], a[92:95], v[72:75]\n" \
"v_and_b32_e32 v96, v12, v96\n" \
"v_and_b32_e32 v97, v13, v97\n" \
"v_mfma_f32_16x16x32_bf16 v[76:79], a[156:159], a[92:95], v[36:39]\n" \
"buffer_load_dwordx4 a[32:35], v20, s[56:59], s97 offen offset:0 sc1\n" \
"v_mfma_f32_16x16x32_bf16 v[76:79], a[152:155], a[88:91], v[76:79]\n" \
"v_and_b32_e32 v98, v14, v98\n" \
"v_and_b32_e32 v99, v15, v99\n" \
"v_mfma_f32_16x16x32_bf16 v[76:79], a[148:151], a[84:87], v[76:79]\n" \
"ds_read_b128 a[0:3], v20 offset:8192\n" \
"v_mov_b32_e32 v61, s62\n" \
"v_mfma_f32_16x16x32_bf16 v[76:79], a[144:147], a[80:83], v[76:79]\n" \
"buffer_load_dwordx4 a[40:43], v56, s[56:59], s97 offen offset:0 sc1\n" \
"ds_read_b128 a[4:7], v20 offset:9216\n" \
"ds_read_b128 a[8:11], v20 offset:10240\n" \
"s_waitcnt lgkmcnt(10)\n" \
"v_mfma_f32_16x16x32_bf16 v[128:131], a[160:163], v[96:99], v[128:131]\n" \
"ds_read_b128 a[12:15], v20 offset:11264\n" \
"s_waitcnt lgkmcnt(10)\n" \
"v_mfma_f32_16x16x32_bf16 v[132:135], a[164:167], v[96:99], v[132:135]\n" \
"ds_read_b128 a[28:31], v20 offset:15360\n" \
"buffer_load_dwordx4 a[48:51], v20, s[56:59], s98 offen offset:0 sc1\n" \
"s_waitcnt lgkmcnt(10)\n" \
"v_mfma_f32_16x16x32_bf16 v[136:139], a[168:171], v[96:99], v[136:139]\n" \
"ds_read_b128 a[24:27], v20 offset:14336\n" \
"s_waitcnt lgkmcnt(10)\n" \
"v_mfma_f32_16x16x32_bf16 v[140:143], a[172:175], v[96:99], v[140:143]\n" \
"ds_read_b128 a[20:23], v20 offset:13312\n" \
"buffer_load_dwordx4 a[56:59], v56, s[56:59], s98 offen offset:0 sc1\n" \
"s_waitcnt lgkmcnt(9)\n" \
"v_mfma_f32_16x16x32_bf16 v[144:147], a[176:179], v[96:99], v[144:147]\n" \
"ds_read_b128 a[16:19], v20 offset:12288\n" \
"v_add_u32_e32 v50, s15, v48\n" \
"v_mfma_f32_16x16x32_bf16 v[148:151], a[180:183], v[96:99], v[148:151]\n" \
"v_xor_b32_e32 v48, 0x4000, v49\n" \
"s_waitcnt lgkmcnt(8)\n" \
"v_mfma_f32_16x16x32_bf16 v[152:155], a[184:187], v[96:99], v[152:155]\n" \
"buffer_load_dwordx4 a[36:39], v20, s[56:59], s97 offen offset:1024 sc1\n" \
"v_mfma_f32_16x16x32_bf16 v[156:159], a[188:191], v[96:99], v[156:159]\n" \
"ds_read_b32 v58, v18 offset:256\n" \
"ds_read_b32 v59, v18 offset:384\n" \
"v_mfma_f32_16x16x32_bf16 a[192:195], a[252:255], v[96:99], a[192:195]\n" \
"ds_read_b128 a[96:99], v48 offset:4096\n" \
"ds_read_b128 a[100:103], v48 offset:5120\n" \
"buffer_load_dwordx4 a[44:47], v56, s[56:59], s97 offen offset:1024 sc1\n" \
"v_mfma_f32_16x16x32_bf16 v[160:163], a[160:163], v[104:107], v[160:163]\n" \
"ds_read_b128 a[104:107], v48 offset:6144\n" \
"ds_read_b128 a[108:111], v48 offset:7168\n" \
"v_mfma_f32_16x16x32_bf16 v[164:167], a[164:167], v[104:107], v[164:167]\n" \
"ds_read_b128 a[112:115], v48 offset:12288\n" \
"ds_read_b128 a[116:119], v48 offset:13312\n" \
"buffer_load_dwordx4 a[52:55], v20, s[56:59], s98 offen offset:1024 sc1\n" \
"v_mfma_f32_16x16x32_bf16 v[168:171], a[168:171], v[104:107], v[168:171]\n" \
"ds_read_b128 a[120:123], v48 offset:14336\n" \
"ds_read_b128 a[124:127], v48 offset:15360\n" \
"v_mfma_f32_16x16x32_bf16 v[172:175], a[172:175], v[104:107], v[172:175]\n" \
"ds_read_b128 a[128:131], v49 offset:4096\n" \
"ds_read_b128 a[132:135], v49 offset:5120\n" \
"buffer_load_dwordx4 a[60:63], v56, s[56:59], s98 offen offset:1024 sc1\n" \
"v_mfma_f32_16x16x32_bf16 v[176:179], a[176:179], v[104:107], v[176:179]\n" \
"ds_read_b128 a[136:139], v49 offset:6144\n" \
"ds_read_b128 a[140:143], v49 offset:7168\n" \
"v_mfma_f32_16x16x32_bf16 v[180:183], a[180:183], v[104:107], v[180:183]\n" \
"ds_read_b128 a[144:147], v49 offset:12288\n" \
"ds_read_b128 a[148:151], v49 offset:13312\n" \
"s_waitcnt vmcnt(8)\n" \
"s_mov_b64 exec, 1\n" \
"buffer_store_dword v61, off, s[56:59], s95 offset:0 sc1\n" \
"s_mov_b64 exec, -1\n" \
"v_mfma_f32_16x16x32_bf16 v[184:187], a[184:187], v[104:107], v[184:187]\n" \
"ds_read_b128 a[152:155], v49 offset:14336\n" \
"ds_read_b128 a[156:159], v49 offset:15360\n" \
"v_mfma_f32_16x16x32_bf16 v[188:191], a[188:191], v[104:107], v[188:191]\n" \
"v_accvgpr_read_b32 v40, a200\n" \
"v_accvgpr_read_b32 v23, a201\n" \
"v_mfma_f32_16x16x32_bf16 a[196:199], a[252:255], v[104:107], a[196:199]\n" \
"v_add_f32_e32 v40, v40, v23\n" \
"s_waitcnt lgkmcnt(15)\n" \
"v_mfma_f32_16x16x32_bf16 v[80:83], a[0:3], a[64:67], v[32:35]\n" \
"buffer_load_dwordx4 a[220:223], v20, s[56:59], s60 offen offset:0 sc1\n" \
"v_mfma_f32_16x16x32_bf16 v[80:83], a[4:7], a[68:71], v[80:83]\n" \
"v_accvgpr_read_b32 v41, a204\n" \
"v_accvgpr_read_b32 v23, a205\n" \
"v_mfma_f32_16x16x32_bf16 v[80:83], a[8:11], a[72:75], v[80:83]\n" \
"v_add_f32_e32 v41, v41, v23\n" \
"v_add_u32_e32 v51, 0x10000, v50\n" \
"v_mfma_f32_16x16x32_bf16 v[80:83], a[12:15], a[76:79], v[80:83]\n" \
"buffer_load_dwordx4 a[236:239], v56, s[56:59], s60 offen offset:0 sc1\n" \
"v_mfma_f32_16x16x32_bf16 v[84:87], a[28:31], a[76:79], v[32:35]\n" \
"v_add_f32_e32 v40, s94, v40\n" \
"v_add_f32_e32 v41, s94, v41\n" \
"v_mfma_f32_16x16x32_bf16 v[84:87], a[24:27], a[72:75], v[84:87]\n" \
"ds_write_b128 v50, v[192:195] offset:0\n" \
"buffer_load_dwordx4 a[224:227], v20, s[56:59], s60 offen offset:1024 sc1\n" \
"v_mfma_f32_16x16x32_bf16 v[84:87], a[20:23], a[68:71], v[84:87]\n" \
"ds_write_b128 v50, v[196:199] offset:1024\n" \
"v_mfma_f32_16x16x32_bf16 v[84:87], a[16:19], a[64:67], v[84:87]\n" \
"ds_write_b128 v50, v[200:203] offset:2048\n" \
"buffer_load_dwordx4 a[240:243], v56, s[56:59], s60 offen offset:1024 sc1\n" \
"v_mfma_f32_16x16x32_bf16 v[88:91], a[0:3], a[80:83], v[36:39]\n" \
"ds_write_b128 v50, v[204:207] offset:3072\n" \
"v_mfma_f32_16x16x32_bf16 v[88:91], a[4:7], a[84:87], v[88:91]\n" \
"ds_write_b128 v50, v[208:211] offset:8192\n" \
"buffer_load_dwordx4 a[228:231], v20, s[56:59], s60 offen offset:2048 sc1\n" \
"v_mfma_f32_16x16x32_bf16 v[88:91], a[8:11], a[88:91], v[88:91]\n" \
"ds_write_b128 v50, v[212:215] offset:9216\n" \
"v_mfma_f32_16x16x32_bf16 v[88:91], a[12:15], a[92:95], v[88:91]\n" \
"ds_write_b128 v50, v[216:219] offset:10240\n" \
"L_pf19:\n" \
"s_cbranch_execz L_retA\n" \
"s_nop 0\n" \
"buffer_load_dwordx4 a[244:247], v56, s[56:59], s60 offen offset:2048 sc1\n" \
"v_mfma_f32_16x16x32_bf16 v[92:95], a[28:31], a[92:95], v[36:39]\n" \
"ds_write_b128 v50, v[220:223] offset:11264\n" \
"ds_write_b128 v51, v[224:227] offset:0\n" \
"ds_write_b128 v51, v[228:231] offset:1024\n" \
"buffer_load_dwordx4 a[232:235], v20, s[56:59], s60 offen offset:3072 sc1\n" \
"ds_write_b128 v51, v[232:235] offset:2048\n" \
"ds_write_b128 v51, v[236:239] offset:3072\n" \
"ds_write_b128 v51, v[240:243] offset:8192\n" \
"ds_write_b128 v51, v[244:247] offset:9216\n" \
"buffer_load_dwordx4 a[248:251], v56, s[56:59], s60 offen offset:3072 sc1\n" \
"ds_write_b128 v51, v[248:251] offset:10240\n" \
"ds_write_b128 v51, v[252:255] offset:11264\n" \
"ds_write_b64 v19, v[40:41]\n" \
"v_mfma_f32_16x16x32_bf16 v[92:95], a[24:27], a[88:91], v[92:95]\n" \
"v_xor_b32_e32 v52, 0x4000, v50\n" \
"v_xor_b32_e32 v53, 0x4000, v51\n" \
"v_mfma_f32_16x16x32_bf16 v[92:95], a[20:23], a[84:87], v[92:95]\n" \
"v_exp_f32_e32 v72, v72\n" \
"v_mfma_f32_16x16x32_bf16 v[92:95], a[16:19], a[80:83], v[92:95]\n" \
"v_exp_f32_e32 v73, v73\n" \
"v_exp_f32_e32 v74, v74\n" \
"v_exp_f32_e32 v75, v75\n" \
"v_exp_f32_e32 v76, v76\n" \
"v_exp_f32_e32 v77, v77\n" \
"v_exp_f32_e32 v78, v78\n" \
"v_exp_f32_e32 v79, v79\n" \
"v_cvt_pk_bf16_f32 v108, v72, v73\n" \
"v_cvt_pk_bf16_f32 v109, v74, v75\n" \
"v_cvt_pk_bf16_f32 v110, v76, v77\n" \
"v_cvt_pk_bf16_f32 v111, v78, v79\n" \
"v_and_b32_e32 v108, v12, v108\n" \
"v_and_b32_e32 v109, v13, v109\n" \
"v_and_b32_e32 v110, v14, v110\n" \
"v_and_b32_e32 v111, v15, v111\n" \
"v_readlane_b32 s93, v11, 18\n" \
"v_readlane_b32 s84, v11, 13\n" \
"v_readlane_b32 s85, v11, 14\n" \
"v_readlane_b32 s86, v11, 15\n" \
"s_mov_b32 s87, 0x20000\n" \
"v_readlane_b32 s92, v11, 16\n" \
"s_add_u32 m0, s93, 0x0\n" \
"v_xor_b32_e32 v63, 0x400, v19\n" \
"s_waitcnt lgkmcnt(0)\n" \
"s_barrier\n" \
"ds_read_b128 v[192:195], v52 offset:0\n" \
"ds_read_b128 v[196:199], v52 offset:1024\n" \
"ds_read_b128 v[200:203], v52 offset:2048\n" \
"ds_read_b128 v[204:207], v52 offset:3072\n" \
"ds_read_b128 v[208:211], v52 offset:8192\n" \
"ds_read_b128 v[212:215], v52 offset:9216\n" \
"ds_read_b128 v[216:219], v52 offset:10240\n" \
"ds_read_b128 v[220:223], v52 offset:11264\n" \
"ds_read_b128 v[224:227], v53 offset:0\n" \
"ds_read_b128 v[228:231], v53 offset:1024\n" \
"ds_read_b128 v[232:235], v53 offset:2048\n" \
"ds_read_b128 v[236:239], v53 offset:3072\n" \
"ds_read_b128 v[240:243], v53 offset:8192\n" \
"ds_read_b128 v[244:247], v53 offset:9216\n" \
"ds_read_b128 v[248:251], v53 offset:10240\n" \
"ds_read_b128 v[252:255], v53 offset:11264\n" \
"v_mfma_f32_16x16x32_bf16 v[160:163], a[128:131], v[108:111], v[160:163]\n" \
"buffer_load_dwordx4 v0, s[84:87], s92 offen offset:0 lds\n" \
"v_mfma_f32_16x16x32_bf16 v[164:167], a[132:135], v[108:111], v[164:167]\n" \
"v_exp_f32_e32 v80, v80\n" \
"v_mfma_f32_16x16x32_bf16 v[168:171], a[136:139], v[108:111], v[168:171]\n" \
"v_exp_f32_e32 v81, v81\n" \
"v_mfma_f32_16x16x32_bf16 v[172:175], a[140:143], v[108:111], v[172:175]\n" \
"buffer_load_dwordx4 v1, s[84:87], s92 offen offset:1024 lds\n" \
"v_mfma_f32_16x16x32_bf16 v[176:179], a[144:147], v[108:111], v[176:179]\n" \
"ds_read_b64 v[42:43], v63\n" \
"v_cvt_pk_bf16_f32 v116, v80, v81\n" \
"v_mfma_f32_16x16x32_bf16 v[180:183], a[148:151], v[108:111], v[180:183]\n" \
"v_exp_f32_e32 v82, v82\n" \
"v_mfma_f32_16x16x32_bf16 v[184:187], a[152:155], v[108:111], v[184:187]\n" \
"buffer_load_dwordx4 v2, s[84:87], s92 offen offset:2048 lds\n" \
"v_mfma_f32_16x16x32_bf16 v[188:191], a[156:159], v[108:111], v[188:191]\n" \
"v_exp_f32_e32 v83, v83\n" \
"v_mfma_f32_16x16x32_bf16 a[196:199], a[252:255], v[108:111], a[196:199]\n" \
"v_exp_f32_e32 v84, v84\n" \
"v_exp_f32_e32 v85, v85\n" \
"v_exp_f32_e32 v86, v86\n" \
"s_waitcnt lgkmcnt(1)\n" \
"s_barrier\n" \
"buffer_load_dwordx4 v3, s[84:87], s92 offen offset:3072 lds\n" \
"s_add_u32 m0, s93, 0x1000\n" \
"v_exp_f32_e32 v87, v87\n" \
"v_cvt_pk_bf16_f32 v117, v82, v83\n" \
"v_cvt_pk_bf16_f32 v118, v84, v85\n" \
"v_cvt_pk_bf16_f32 v119, v86, v87\n" \
"v_exp_f32_e32 v88, v88\n" \
"v_exp_f32_e32 v89, v89\n" \
"v_mfma_f32_16x16x32_bf16 v[128:131], a[96:99], v[116:119], v[128:131]\n" \
"buffer_load_dwordx4 v4, s[84:87], s92 offen offset:0 lds\n" \
"v_mfma_f32_16x16x32_bf16 v[132:135], a[100:103], v[116:119], v[132:135]\n" \
"v_exp_f32_e32 v90, v90\n" \
"v_mfma_f32_16x16x32_bf16 v[136:139], a[104:107], v[116:119], v[136:139]\n" \
"v_exp_f32_e32 v91, v91\n" \
"v_mfma_f32_16x16x32_bf16 v[140:143], a[108:111], v[116:119], v[140:143]\n" \
"buffer_load_dwordx4 v5, s[84:87], s92 offen offset:1024 lds\n" \
"v_mfma_f32_16x16x32_bf16 v[144:147], a[112:115], v[116:119], v[144:147]\n" \
"v_exp_f32_e32 v92, v92\n" \
"v_mfma_f32_16x16x32_bf16 v[148:151], a[116:119], v[116:119], v[148:151]\n" \
"v_exp_f32_e32 v93, v93\n" \
"v_mfma_f32_16x16x32_bf16 v[152:155], a[120:123], v[116:119], v[152:155]\n" \
"buffer_load_dwordx4 v6, s[84:87], s92 offen offset:2048 lds\n" \
"v_mfma_f32_16x16x32_bf16 v[156:159], a[124:127], v[116:119], v[156:159]\n" \
"v_exp_f32_e32 v94, v94\n" \
"v_mfma_f32_16x16x32_bf16 a[192:195], a[252:255], v[116:119], a[192:195]\n" \
"v_exp_f32_e32 v95, v95\n" \
"v_cvt_pk_bf16_f32 v124, v88, v89\n" \
"v_cvt_pk_bf16_f32 v125, v90, v91\n" \
"v_cvt_pk_bf16_f32 v126, v92, v93\n" \
"v_cvt_pk_bf16_f32 v127, v94, v95\n" \
"v_add_f32_e32 v160, v160, v224\n" \
"v_add_f32_e32 v161, v161, v225\n" \
"v_add_f32_e32 v162, v162, v226\n" \
"v_add_f32_e32 v163, v163, v227\n" \
"v_add_f32_e32 v164, v164, v228\n" \
"v_add_f32_e32 v165, v165, v229\n" \
"v_mfma_f32_16x16x32_bf16 v[160:163], a[96:99], v[124:127], v[160:163]\n" \
"buffer_load_dwordx4 v7, s[84:87], s92 offen offset:3072 lds\n" \
"s_add_u32 m0, s93, 0x2000\n" \
"v_add_f32_e32 v166, v166, v230\n" \
"v_add_f32_e32 v167, v167, v231\n" \
"v_add_f32_e32 v168, v168, v232\n" \
"v_add_f32_e32 v169, v169, v233\n" \
"v_mfma_f32_16x16x32_bf16 v[164:167], a[100:103], v[124:127], v[164:167]\n" \
"v_add_f32_e32 v170, v170, v234\n" \
"v_add_f32_e32 v171, v171, v235\n" \
"v_add_f32_e32 v172, v172, v236\n" \
"v_add_f32_e32 v173, v173, v237\n" \
"v_mfma_f32_16x16x32_bf16 v[168:171], a[104:107], v[124:127], v[168:171]\n" \
"buffer_load_dwordx4 v8, s[84:87], s92 offen offset:0 lds\n" \
"v_add_f32_e32 v174, v174, v238\n" \
"v_add_f32_e32 v175, v175, v239\n" \
"v_accvgpr_read_b32 v54, a192\n" \
"v_accvgpr_read_b32 v23, a193\n" \
"v_mfma_f32_16x16x32_bf16 v[172:175], a[108:111], v[124:127], v[172:175]\n" \
"v_add_f32_e32 v54, v54, v23\n" \
"s_waitcnt lgkmcnt(0)\n" \
"v_add_f32_e32 v54, v54, v42\n" \
"v_max_f32_e32 v62, v62, v54\n" \
"v_add_f32_e32 v176, v176, v240\n" \
"v_add_f32_e32 v177, v177, v241\n" \
"v_add_f32_e32 v178, v178, v242\n" \
"v_add_f32_e32 v179, v179, v243\n" \
"v_rcp_f32_e32 v54, v54\n" \
"v_add_f32_e32 v128, v128, v192\n" \
"v_mfma_f32_16x16x32_bf16 v[176:179], a[112:115], v[124:127], v[176:179]\n" \
"v_add_f32_e32 v129, v129, v193\n" \
"v_add_f32_e32 v130, v130, v194\n" \
"v_add_f32_e32 v131, v131, v195\n" \
"v_add_f32_e32 v132, v132, v196\n" \
"v_add_f32_e32 v133, v133, v197\n" \
"v_add_f32_e32 v134, v134, v198\n" \
"v_add_f32_e32 v135, v135, v199\n" \
"v_mul_f32_e32 v54, v54, v58\n" \
"v_mul_f32_e32 v128, v54, v128\n" \
"v_mul_f32_e32 v129, v54, v129\n" \
"v_mul_f32_e32 v130, v54, v130\n" \
"v_mul_f32_e32 v131, v54, v131\n" \
"v_mul_f32_e32 v132, v54, v132\n" \
"v_mul_f32_e32 v133, v54, v133\n" \
"v_mul_f32_e32 v134, v54, v134\n" \
"v_mul_f32_e32 v135, v54, v135\n" \
"v_add_f32_e32 v180, v180, v244\n" \
"v_add_f32_e32 v181, v181, v245\n" \
"v_add_f32_e32 v182, v182, v246\n" \
"v_add_f32_e32 v183, v183, v247\n" \
"v_add_f32_e32 v136, v136, v200\n" \
"v_add_f32_e32 v137, v137, v201\n" \
"v_add_f32_e32 v138, v138, v202\n" \
"v_add_f32_e32 v139, v139, v203\n" \
"v_add_f32_e32 v140, v140, v204\n" \
"v_add_f32_e32 v141, v141, v205\n" \
"v_add_f32_e32 v142, v142, v206\n" \
"v_add_f32_e32 v143, v143, v207\n" \
"v_cvt_pk_bf16_f32 v64, v128, v129\n" \
"v_cvt_pk_bf16_f32 v65, v130, v131\n" \
"v_cvt_pk_bf16_f32 v66, v132, v133\n" \
"v_cvt_pk_bf16_f32 v67, v134, v135\n" \
"buffer_store_dwordx4 v[64:67], v20, s[56:59], s68 offen offset:0 sc1\n" \
"v_mfma_f32_16x16x32_bf16 v[180:183], a[116:119], v[124:127], v[180:183]\n" \
"v_mul_f32_e32 v136, v54, v136\n" \
"v_mul_f32_e32 v137, v54, v137\n" \
"v_mfma_f32_16x16x32_bf16 a[208:211], a[216:219], v[64:67], a[208:211]\n" \
"v_mul_f32_e32 v138, v54, v138\n" \
"v_mul_f32_e32 v139, v54, v139\n" \
"v_mul_f32_e32 v140, v54, v140\n" \
"v_mul_f32_e32 v141, v54, v141\n" \
"v_mul_f32_e32 v142, v54, v142\n" \
"v_mul_f32_e32 v143, v54, v143\n" \
"v_add_f32_e32 v144, v144, v208\n" \
"v_add_f32_e32 v145, v145, v209\n" \
"v_add_f32_e32 v146, v146, v210\n" \
"v_add_f32_e32 v147, v147, v211\n" \
"v_add_f32_e32 v148, v148, v212\n" \
"v_add_f32_e32 v149, v149, v213\n" \
"v_add_f32_e32 v150, v150, v214\n" \
"v_add_f32_e32 v151, v151, v215\n" \
"v_cvt_pk_bf16_f32 v68, v136, v137\n" \
"v_cvt_pk_bf16_f32 v69, v138, v139\n" \
"v_cvt_pk_bf16_f32 v70, v140, v141\n" \
"v_cvt_pk_bf16_f32 v71, v142, v143\n" \
"buffer_store_dwordx4 v[68:71], v20, s[56:59], s68 offen offset:1024 sc1\n" \
"v_mul_f32_e32 v144, v54, v144\n" \
"v_mfma_f32_16x16x32_bf16 a[208:211], a[216:219], v[68:71], a[208:211]\n" \
"v_mul_f32_e32 v145, v54, v145\n" \
"v_mul_f32_e32 v146, v54, v146\n" \
"v_mul_f32_e32 v147, v54, v147\n" \
"v_mul_f32_e32 v148, v54, v148\n" \
"v_mul_f32_e32 v149, v54, v149\n" \
"v_mul_f32_e32 v150, v54, v150\n" \
"v_mul_f32_e32 v151, v54, v151\n" \
"v_add_f32_e32 v184, v184, v248\n" \
"v_add_f32_e32 v185, v185, v249\n" \
"v_add_f32_e32 v186, v186, v250\n" \
"v_add_f32_e32 v187, v187, v251\n" \
"v_add_f32_e32 v152, v152, v216\n" \
"v_add_f32_e32 v153, v153, v217\n" \
"v_add_f32_e32 v154, v154, v218\n" \
"v_add_f32_e32 v155, v155, v219\n" \
"v_add_f32_e32 v156, v156, v220\n" \
"v_add_f32_e32 v157, v157, v221\n" \
"v_add_f32_e32 v158, v158, v222\n" \
"v_add_f32_e32 v159, v159, v223\n" \
"v_cvt_pk_bf16_f32 v72, v144, v145\n" \
"v_cvt_pk_bf16_f32 v73, v146, v147\n" \
"v_cvt_pk_bf16_f32 v74, v148, v149\n" \
"v_cvt_pk_bf16_f32 v75, v150, v151\n" \
"buffer_store_dwordx4 v[72:75], v20, s[56:59], s68 offen offset:2048 sc1\n" \
"v_mfma_f32_16x16x32_bf16 v[184:187], a[120:123], v[124:127], v[184:187]\n" \
"v_mul_f32_e32 v152, v54, v152\n" \
"v_mul_f32_e32 v153, v54, v153\n" \
"v_mfma_f32_16x16x32_bf16 a[208:211], a[216:219], v[72:75], a[208:211]\n" \
"v_mul_f32_e32 v154, v54, v154\n" \
"v_mul_f32_e32 v155, v54, v155\n" \
"v_mul_f32_e32 v156, v54, v156\n" \
"v_mul_f32_e32 v157, v54, v157\n" \
"v_mul_f32_e32 v158, v54, v158\n" \
"v_mul_f32_e32 v159, v54, v159\n" \
"v_cvt_pk_bf16_f32 v76, v152, v153\n" \
"v_cvt_pk_bf16_f32 v77, v154, v155\n" \
"v_cvt_pk_bf16_f32 v78, v156, v157\n" \
"v_cvt_pk_bf16_f32 v79, v158, v159\n" \
"buffer_store_dwordx4 v[76:79], v20, s[56:59], s68 offen offset:3072 sc1\n" \
"v_add_f32_e32 v188, v188, v252\n" \
"v_mfma_f32_16x16x32_bf16 a[208:211], a[216:219], v[76:79], a[208:211]\n" \
"v_add_f32_e32 v189, v189, v253\n" \
"v_add_f32_e32 v190, v190, v254\n" \
"v_add_f32_e32 v191, v191, v255\n" \
"s_nop 1\n" \
"v_mfma_f32_16x16x32_bf16 v[188:191], a[124:127], v[124:127], v[188:191]\n" \
"v_mfma_f32_16x16x32_bf16 a[196:199], a[252:255], v[124:127], a[196:199]\n" \
"s_nop 7\n" \
"v_accvgpr_read_b32 v55, a196\n" \
"v_accvgpr_read_b32 v23, a197\n" \
"v_add_f32_e32 v55, v55, v23\n" \
"v_add_f32_e32 v55, v55, v43\n" \
"v_max_f32_e32 v62, v62, v55\n" \
"v_rcp_f32_e32 v55, v55\n" \
"v_cmp_lt_f32_e64 s[72:73], s100, v62\n" \
"v_mul_f32_e32 v55, v55, v59\n" \
"v_mul_f32_e32 v160, v55, v160\n" \
"v_mul_f32_e32 v161, v55, v161\n" \
"v_mul_f32_e32 v162, v55, v162\n" \
"v_mul_f32_e32 v163, v55, v163\n" \
"v_mul_f32_e32 v164, v55, v164\n" \
"v_mul_f32_e32 v165, v55, v165\n" \
"v_mul_f32_e32 v166, v55, v166\n" \
"v_mul_f32_e32 v167, v55, v167\n" \
"v_cvt_pk_bf16_f32 v80, v160, v161\n" \
"v_cvt_pk_bf16_f32 v81, v162, v163\n" \
"v_cvt_pk_bf16_f32 v82, v164, v165\n" \
"v_cvt_pk_bf16_f32 v83, v166, v167\n" \
"buffer_store_dwordx4 v[80:83], v20, s[56:59], s69 offen offset:0 sc1\n" \
"v_mul_f32_e32 v168, v55, v168\n" \
"v_mfma_f32_16x16x32_bf16 a[208:211], a[216:219], v[80:83], a[208:211]\n" \
"v_mul_f32_e32 v169, v55, v169\n" \
"v_mul_f32_e32 v170, v55, v170\n" \
"v_mul_f32_e32 v171, v55, v171\n" \
"v_mul_f32_e32 v172, v55, v172\n" \
"v_mul_f32_e32 v173, v55, v173\n" \
"v_mul_f32_e32 v174, v55, v174\n" \
"v_mul_f32_e32 v175, v55, v175\n" \
"v_cvt_pk_bf16_f32 v84, v168, v169\n" \
"v_cvt_pk_bf16_f32 v85, v170, v171\n" \
"v_cvt_pk_bf16_f32 v86, v172, v173\n" \
"v_cvt_pk_bf16_f32 v87, v174, v175\n" \
"buffer_store_dwordx4 v[84:87], v20, s[56:59], s69 offen offset:1024 sc1\n" \
"v_mul_f32_e32 v176, v55, v176\n" \
"v_mfma_f32_16x16x32_bf16 a[208:211], a[216:219], v[84:87], a[208:211]\n" \
"v_mul_f32_e32 v177, v55, v177\n" \
"v_mul_f32_e32 v178, v55, v178\n" \
"v_mul_f32_e32 v179, v55, v179\n" \
"v_mul_f32_e32 v180, v55, v180\n" \
"v_mul_f32_e32 v181, v55, v181\n" \
"v_mul_f32_e32 v182, v55, v182\n" \
"v_mul_f32_e32 v183, v55, v183\n" \
"v_cvt_pk_bf16_f32 v96, v176, v177\n" \
"v_cvt_pk_bf16_f32 v97, v178, v179\n" \
"v_cvt_pk_bf16_f32 v98, v180, v181\n" \
"v_cvt_pk_bf16_f32 v99, v182, v183\n" \
"buffer_store_dwordx4 v[96:99], v20, s[56:59], s69 offen offset:2048 sc1\n" \
"v_mul_f32_e32 v184, v55, v184\n" \
"v_mfma_f32_16x16x32_bf16 a[208:211], a[216:219], v[96:99], a[208:211]\n" \
"v_mul_f32_e32 v185, v55, v185\n" \
"v_mul_f32_e32 v186, v55, v186\n" \
"v_mul_f32_e32 v187, v55, v187\n" \
"v_mul_f32_e32 v188, v55, v188\n" \
"v_mul_f32_e32 v189, v55, v189\n" \
"v_mul_f32_e32 v190, v55, v190\n" \
"v_mul_f32_e32 v191, v55, v191\n" \
"v_cvt_pk_bf16_f32 v100, v184, v185\n" \
"v_cvt_pk_bf16_f32 v101, v186, v187\n" \
"v_cvt_pk_bf16_f32 v102, v188, v189\n" \
"v_cvt_pk_bf16_f32 v103, v190, v191\n" \
"buffer_store_dwordx4 v[100:103], v20, s[56:59], s69 offen offset:3072 sc1\n" \
"s_nop 0\n" \
"v_mfma_f32_16x16x32_bf16 a[208:211], a[216:219], v[100:103], a[208:211]\n" \
"s_nop 7\n" \
"v_accvgpr_read_b32 v63, a208\n" \
"v_cmp_u_f32_e32 vcc, v63, v63\n" \
"s_nop 0\n" \
"s_or_b64 vcc, s[72:73], vcc\n" \
"s_cbranch_scc1 L_end_%=\n" \
"L_out_%=:\n" \
"L_pf20:\n" \
"s_cbranch_execz L_retA\n" \
"s_nop 0\n" \
"v_add_u32_e32 v34, 0x20000, v10\n" \
"v_mov_b32_e32 v39, s62\n" \
"v_readlane_b32 s70, v11, 4\n" \
"v_bfe_u32 v38, v20, 2, 4\n" \
"v_readlane_b32 s64, v11, 1\n" \
"v_readlane_b32 s71, v11, 3\n" \
"v_add_u32_e32 v33, 0xfc00, v10\n" \
"v_readlane_b32 s99, v11, 19\n" \
"v_readlane_b32 s80, v11, 9\n" \
"v_readlane_b32 s81, v11, 10\n" \
"s_mov_b32 s82, 0x47fca0\n" \
"s_mov_b32 s83, 0x20000\n" \
"ds_read_b64_tr_b16 a[0:1], v34 offset:0\n" \
"ds_read_b64_tr_b16 a[2:3], v34 offset:4608\n" \
"ds_read_b64_tr_b16 a[4:5], v34 offset:9216\n" \
"ds_read_b64_tr_b16 a[6:7], v34 offset:13824\n" \
"ds_read_b64_tr_b16 a[8:9], v34 offset:18432\n" \
"ds_read_b64_tr_b16 a[10:11], v34 offset:23040\n" \
"ds_read_b64_tr_b16 a[12:13], v34 offset:32\n" \
"ds_read_b64_tr_b16 a[14:15], v34 offset:4640\n" \
"ds_read_b64_tr_b16 a[16:17], v34 offset:9248\n" \
"ds_read_b64_tr_b16 a[18:19], v34 offset:13856\n" \
"buffer_load_dword v37, v38, s[56:59], s64 offen sc1\n" \
"ds_read_b64_tr_b16 a[20:21], v34 offset:18464\n" \
"ds_read_b64_tr_b16 a[22:23], v34 offset:23072\n" \
"ds_read_b64_tr_b16 a[24:25], v34 offset:64\n" \
"ds_read_b64_tr_b16 a[26:27], v34 offset:4672\n" \
"ds_read_b64_tr_b16 a[28:29], v34 offset:9280\n" \
"ds_read_b64_tr_b16 a[30:31], v34 offset:13888\n" \
"ds_read_b64_tr_b16 a[192:193], v34 offset:18496\n" \
"ds_read_b64_tr_b16 a[194:195], v34 offset:23104\n" \
"ds_read_b64_tr_b16 a[196:197], v34 offset:96\n" \
"ds_read_b64_tr_b16 a[198:199], v34 offset:4704\n" \
"ds_read_b64_tr_b16 a[200:201], v34 offset:9312\n" \
"ds_read_b64_tr_b16 a[202:203], v34 offset:13920\n" \
"ds_read_b64_tr_b16 a[204:205], v34 offset:18528\n" \
"ds_read_b64_tr_b16 a[206:207], v34 offset:23136\n" \
"ds_read_b64_tr_b16 a[208:209], v34 offset:128\n" \
"ds_read_b64_tr_b16 a[210:211], v34 offset:4736\n" \
"ds_read_b64_tr_b16 a[212:213], v34 offset:9344\n" \
"ds_read_b64_tr_b16 a[214:215], v34 offset:13952\n" \
"ds_read_b64_tr_b16 a[216:217], v34 offset:18560\n" \
"ds_read_b64_tr_b16 a[218:219], v34 offset:23168\n" \
"v_readlane_b32 s65, v11, 23\n" \
"s_add_u32 m0, s99, 0x6c00\n" \
"v_readlane_b32 s66, v11, 26\n" \
"v_add_u32_e32 v32, 0x49000, v20\n" \
"v_lshrrev_b32_e32 v44, 9, v20\n" \
"v_mul_u32_u24_e32 v44, 0xb40, v44\n" \
"v_add_u32_e32 v35, v44, v10\n" \
"v_readlane_b32 s67, v11, 24\n" \
"v_readlane_b32 s92, v11, 20\n" \
"v_readlane_b32 s73, v11, 25\n" \
"s_waitcnt vmcnt(18) lgkmcnt(15)\n" \
"v_mfma_f32_16x16x32_bf16 v[112:115], a[0:3], a[220:223], 0\n" \
"buffer_load_dwordx4 v0, s[80:83], s65 offen offset:0 lds\n" \
"v_mfma_f32_16x16x32_bf16 v[112:115], a[4:7], a[224:227], v[112:115]\n" \
"v_readlane_b32 s94, v11, 21\n" \
"v_readlane_b32 s84, v11, 7\n" \
"v_mfma_f32_16x16x32_bf16 v[112:115], a[8:11], a[228:231], v[112:115]\n" \
"v_readlane_b32 s85, v11, 8\n" \
"s_mov_b32 s86, 0xa1ca0\n" \
"v_mfma_f32_16x16x32_bf16 v[116:119], a[8:11], a[244:247], 0\n" \
"s_mov_b32 s87, 0x20000\n" \
"v_readlane_b32 s68, v11, 27\n" \
"v_mfma_f32_16x16x32_bf16 v[116:119], a[4:7], a[240:243], v[116:119]\n" \
"v_readlane_b32 s69, v11, 28\n" \
"v_readlane_b32 s72, v11, 30\n" \
"v_mfma_f32_16x16x32_bf16 v[116:119], a[0:3], a[236:239], v[116:119]\n" \
"buffer_load_dwordx4 v1, s[80:83], s65 offen offset:1024 lds\n" \
"v_mfma_f32_16x16x32_bf16 v[184:187], a[0:3], v[64:67], 0\n" \
"v_readlane_b32 s93, v11, 31\n" \
"v_lshlrev_b32_e32 v41, 12, v20\n" \
"v_mfma_f32_16x16x32_bf16 v[184:187], a[4:7], v[68:71], v[184:187]\n" \
"v_readlane_b32 s63, v11, 37\n" \
"v_bfe_u32 v40, v20, 2, 7\n" \
"v_mfma_f32_16x16x32_bf16 v[184:187], a[8:11], v[72:75], v[184:187]\n" \
"s_add_u32 s74, s63, 0x3800\n" \
"v_readlane_b32 s95, v11, 34\n" \
"v_mfma_f32_16x16x32_bf16 v[188:191], a[8:11], v[96:99], 0\n" \
"ds_read_b64_tr_b16 a[8:9], v34 offset:18592\n" \
"ds_read_b64_tr_b16 a[10:11], v34 offset:23200\n" \
"v_mfma_f32_16x16x32_bf16 v[188:191], a[4:7], v[84:87], v[188:191]\n" \
"ds_read_b64_tr_b16 a[4:5], v34 offset:9376\n" \
"ds_read_b64_tr_b16 a[6:7], v34 offset:13984\n" \
"v_mfma_f32_16x16x32_bf16 v[188:191], a[0:3], v[80:83], v[188:191]\n" \
"ds_read_b64_tr_b16 a[0:1], v34 offset:160\n" \
"ds_read_b64_tr_b16 a[2:3], v34 offset:4768\n" \
"v_mfma_f32_16x16x32_bf16 v[120:123], a[12:15], a[220:223], 0\n" \
"buffer_load_dwordx4 v2, s[80:83], s65 offen offset:2048 lds\n" \
"v_mfma_f32_16x16x32_bf16 v[120:123], a[16:19], a[224:227], v[120:123]\n" \
"v_readlane_b32 s97, v11, 36\n" \
"v_bfe_u32 v44, v20, 4, 4\n" \
"v_mfma_f32_16x16x32_bf16 v[120:123], a[20:23], a[228:231], v[120:123]\n" \
"v_bfe_u32 v45, v20, 8, 2\n" \
"v_mul_u32_u24_e32 v42, 0x480, v44\n" \
"v_mfma_f32_16x16x32_bf16 v[124:127], a[20:23], a[244:247], 0\n" \
"v_lshl_add_u32 v42, v45, 3, v42\n" \
"v_and_b32_e32 v43, 1, v45\n" \
"v_mfma_f32_16x16x32_bf16 v[124:127], a[16:19], a[240:243], v[124:127]\n" \
"v_lshrrev_b32_e32 v44, 1, v44\n" \
"v_lshlrev_b32_e32 v45, 1, v45\n" \
"v_mfma_f32_16x16x32_bf16 v[124:127], a[12:15], a[236:239], v[124:127]\n" \
"buffer_load_dwordx4 v3, s[80:83], s65 offen offset:3072 lds\n" \
"v_mfma_f32_16x16x32_bf16 v[192:195], a[12:15], v[64:67], 0\n" \
"s_add_u32 m0, s99, 0x7c00\n" \
"v_sub_u32_e32 v44, v44, v45\n" \
"v_mfma_f32_16x16x32_bf16 v[192:195], a[16:19], v[68:71], v[192:195]\n" \
"v_and_b32_e32 v45, 16, v20\n" \
"v_mov_b32_e32 v46, 0x3f80\n" \
"v_mfma_f32_16x16x32_bf16 v[192:195], a[20:23], v[72:75], v[192:195]\n" \
"v_lshlrev_b32_e32 v45, v45, v46\n" \
"v_mov_b32_e32 v46, s62\n" \
"v_mfma_f32_16x16x32_bf16 v[196:199], a[20:23], v[96:99], 0\n" \
"ds_read_b64_tr_b16 a[20:21], v34 offset:18624\n" \
"ds_read_b64_tr_b16 a[22:23], v34 offset:23232\n" \
"v_mfma_f32_16x16x32_bf16 v[196:199], a[16:19], v[84:87], v[196:199]\n" \
"ds_read_b64_tr_b16 a[16:17], v34 offset:9408\n" \
"ds_read_b64_tr_b16 a[18:19], v34 offset:14016\n" \
"v_mfma_f32_16x16x32_bf16 v[196:199], a[12:15], v[80:83], v[196:199]\n" \
"ds_read_b64_tr_b16 a[12:13], v34 offset:192\n" \
"ds_read_b64_tr_b16 a[14:15], v34 offset:4800\n" \
"v_mfma_f32_16x16x32_bf16 v[128:131], a[24:27], a[220:223], 0\n" \
"v_readlane_b32 s96, v11, 35\n" \
"s_waitcnt lgkmcnt(8)\n" \
"v_mfma_f32_16x16x32_bf16 v[128:131], a[28:31], a[224:227], v[128:131]\n" \
"buffer_load_dwordx4 v4, s[80:83], s65 offen offset:0 lds\n" \
"v_mfma_f32_16x16x32_bf16 v[128:131], a[192:195], a[228:231], v[128:131]\n" \
"v_readlane_b32 s100, v11, 6\n" \
"v_readlane_b32 s75, v11, 38\n" \
"v_mfma_f32_16x16x32_bf16 v[132:135], a[192:195], a[244:247], 0\n" \
"v_add_u32_e32 v54, 0x3000, v20\n" \
"v_mov_b32_e32 v52, 0\n" \
"v_mfma_f32_16x16x32_bf16 v[132:135], a[28:31], a[240:243], v[132:135]\n" \
"v_mov_b32_e32 v53, 0\n" \
"v_mov_b32_e32 v48, 0\n" \
"v_mfma_f32_16x16x32_bf16 v[132:135], a[24:27], a[236:239], v[132:135]\n" \
"v_mov_b32_e32 v49, 0\n" \
"v_mul_u32_u24_e32 v43, 0x8ff8, v43\n" \
"v_mfma_f32_16x16x32_bf16 v[200:203], a[24:27], v[64:67], 0\n" \
"buffer_load_dwordx4 v5, s[80:83], s65 offen offset:1024 lds\n" \
"v_mfma_f32_16x16x32_bf16 v[200:203], a[28:31], v[68:71], v[200:203]\n" \
"v_add_u32_e32 v42, v42, v43\n" \
"v_readlane_b32 s88, v11, 11\n" \
"v_mfma_f32_16x16x32_bf16 v[200:203], a[192:195], v[72:75], v[200:203]\n" \
"v_readlane_b32 s89, v11, 12\n" \
"s_mov_b32 s90, 0x240000\n" \
"v_mfma_f32_16x16x32_bf16 v[204:207], a[192:195], v[96:99], 0\n" \
"ds_read_b64_tr_b16 a[192:193], v34 offset:18656\n" \
"ds_read_b64_tr_b16 a[194:195], v34 offset:23264\n" \
"v_mfma_f32_16x16x32_bf16 v[204:207], a[28:31], v[84:87], v[204:207]\n" \
"ds_read_b64_tr_b16 a[28:29], v34 offset:9440\n" \
"ds_read_b64_tr_b16 a[30:31], v34 offset:14048\n" \
"v_mfma_f32_16x16x32_bf16 v[204:207], a[24:27], v[80:83], v[204:207]\n" \
"ds_read_b64_tr_b16 a[24:25], v34 offset:224\n" \
"ds_read_b64_tr_b16 a[26:27], v34 offset:4832\n" \
"v_mfma_f32_16x16x32_bf16 v[136:139], a[196:199], a[220:223], 0\n" \
"buffer_load_dwordx4 v6, s[80:83], s65 offen offset:2048 lds\n" \
"v_mfma_f32_16x16x32_bf16 v[136:139], a[200:203], a[224:227], v[136:139]\n" \
"s_mov_b32 s91, 0x20000\n" \
"v_mfma_f32_16x16x32_bf16 v[136:139], a[204:207], a[228:231], v[136:139]\n" \
"v_mfma_f32_16x16x32_bf16 v[140:143], a[204:207], a[244:247], 0\n" \
"v_mfma_f32_16x16x32_bf16 v[140:143], a[200:203], a[240:243], v[140:143]\n" \
"v_mfma_f32_16x16x32_bf16 v[140:143], a[196:199], a[236:239], v[140:143]\n" \
"buffer_load_dwordx4 v7, s[80:83], s65 offen offset:3072 lds\n" \
"v_mfma_f32_16x16x32_bf16 v[208:211], a[196:199], v[64:67], 0\n" \
"s_add_u32 m0, s99, 0x8c00\n" \
"v_mfma_f32_16x16x32_bf16 v[208:211], a[200:203], v[68:71], v[208:211]\n" \
"v_mfma_f32_16x16x32_bf16 v[208:211], a[204:207], v[72:75], v[208:211]\n" \
"v_mfma_f32_16x16x32_bf16 v[212:215], a[204:207], v[96:99], 0\n" \
"ds_read_b64_tr_b16 a[204:205], v34 offset:18688\n" \
"ds_read_b64_tr_b16 a[206:207], v34 offset:23296\n" \
"v_mfma_f32_16x16x32_bf16 v[212:215], a[200:203], v[84:87], v[212:215]\n" \
"ds_read_b64_tr_b16 a[200:201], v34 offset:9472\n" \
"ds_read_b64_tr_b16 a[202:203], v34 offset:14080\n" \
"v_mfma_f32_16x16x32_bf16 v[212:215], a[196:199], v[80:83], v[212:215]\n" \
"ds_read_b64_tr_b16 a[196:197], v34 offset:256\n" \
"ds_read_b64_tr_b16 a[198:199], v34 offset:4864\n" \
"v_mfma_f32_16x16x32_bf16 v[144:147], a[208:211], a[220:223], 0\n" \
"buffer_load_dwordx4 v8, s[80:83], s65 offen offset:0 lds\n" \
"v_mfma_f32_16x16x32_bf16 v[144:147], a[212:215], a[224:227], v[144:147]\n" \
"s_waitcnt vmcnt(9)\n" \
"v_cmp_ne_u32_e32 vcc, s62, v37\n" \
"v_mfma_f32_16x16x32_bf16 v[144:147], a[216:219], a[228:231], v[144:147]\n" \
"s_add_u32 m0, s99, 0x14400\n" \
"v_readlane_b32 s65, v11, 42\n" \
"v_mfma_f32_16x16x32_bf16 v[148:151], a[216:219], a[244:247], 0\n" \
"v_mfma_f32_16x16x32_bf16 v[148:151], a[212:215], a[240:243], v[148:151]\n" \
"s_cbranch_vccnz L_sa_%=\n" \
"L_ra_%=:\n" \
"v_readlane_b32 s64, v11, 29\n" \
"v_mfma_f32_16x16x32_bf16 v[148:151], a[208:211], a[236:239], v[148:151]\n" \
"buffer_load_dwordx4 a[64:67], v20, s[56:59], s66 offen offset:0 sc1\n" \
"v_mfma_f32_16x16x32_bf16 v[216:219], a[208:211], v[64:67], 0\n" \
"v_mfma_f32_16x16x32_bf16 v[216:219], a[212:215], v[68:71], v[216:219]\n" \
"v_mfma_f32_16x16x32_bf16 v[216:219], a[216:219], v[72:75], v[216:219]\n" \
"v_mfma_f32_16x16x32_bf16 v[220:223], a[216:219], v[96:99], 0\n" \
"v_mfma_f32_16x16x32_bf16 v[220:223], a[212:215], v[84:87], v[220:223]\n" \
"buffer_load_dwordx4 a[68:71], v20, s[56:59], s66 offen offset:1024 sc1\n" \
"v_mfma_f32_16x16x32_bf16 v[220:223], a[208:211], v[80:83], v[220:223]\n" \
"s_waitcnt lgkmcnt(5)\n" \
"v_mfma_f32_16x16x32_bf16 v[152:155], a[0:3], a[220:223], 0\n" \
"v_mfma_f32_16x16x32_bf16 v[152:155], a[4:7], a[224:227], v[152:155]\n" \
"v_mfma_f32_16x16x32_bf16 v[152:155], a[8:11], a[228:231], v[152:155]\n" \
"v_mfma_f32_16x16x32_bf16 v[156:159], a[8:11], a[244:247], 0\n" \
"buffer_load_dwordx4 a[84:87], v32, s[56:59], s66 offen offset:1024 sc1\n" \
"v_mfma_f32_16x16x32_bf16 v[156:159], a[4:7], a[240:243], v[156:159]\n" \
"v_mfma_f32_16x16x32_bf16 v[156:159], a[0:3], a[236:239], v[156:159]\n" \
"v_mfma_f32_16x16x32_bf16 v[224:227], a[0:3], v[64:67], 0\n" \
"v_mfma_f32_16x16x32_bf16 v[224:227], a[4:7], v[68:71], v[224:227]\n" \
"v_mfma_f32_16x16x32_bf16 v[224:227], a[8:11], v[72:75], v[224:227]\n" \
"buffer_load_dwordx4 a[80:83], v32, s[56:59], s66 offen offset:0 sc1\n" \
"v_mfma_f32_16x16x32_bf16 v[228:231], a[8:11], v[96:99], 0\n" \
"v_mfma_f32_16x16x32_bf16 v[228:231], a[4:7], v[84:87], v[228:231]\n" \
"v_mfma_f32_16x16x32_bf16 v[228:231], a[0:3], v[80:83], v[228:231]\n" \
"v_mfma_f32_16x16x32_bf16 v[160:163], a[12:15], a[220:223], 0\n" \
"v_mfma_f32_16x16x32_bf16 v[160:163], a[16:19], a[224:227], v[160:163]\n" \
"buffer_load_dwordx4 a[72:75], v20, s[56:59], s66 offen offset:2048 sc1\n" \
"v_mfma_f32_16x16x32_bf16 v[160:163], a[20:23], a[228:231], v[160:163]\n" \
"v_mfma_f32_16x16x32_bf16 v[164:167], a[20:23], a[244:247], 0\n" \
"v_mfma_f32_16x16x32_bf16 v[164:167], a[16:19], a[240:243], v[164:167]\n" \
"v_mfma_f32_16x16x32_bf16 v[164:167], a[12:15], a[236:239], v[164:167]\n" \
"v_mfma_f32_16x16x32_bf16 v[232:235], a[12:15], v[64:67], 0\n" \
"buffer_load_dwordx4 a[76:79], v20, s[56:59], s66 offen offset:3072 sc1\n" \
"v_mfma_f32_16x16x32_bf16 v[232:235], a[16:19], v[68:71], v[232:235]\n" \
"v_mfma_f32_16x16x32_bf16 v[232:235], a[20:23], v[72:75], v[232:235]\n" \
"v_mfma_f32_16x16x32_bf16 v[236:239], a[20:23], v[96:99], 0\n" \
"v_mfma_f32_16x16x32_bf16 v[236:239], a[16:19], v[84:87], v[236:239]\n" \
"v_mfma_f32_16x16x32_bf16 v[236:239], a[12:15], v[80:83], v[236:239]\n" \
"buffer_load_dwordx4 a[92:95], v32, s[56:59], s66 offen offset:3072 sc1\n" \
"s_waitcnt lgkmcnt(0)\n" \
"s_barrier\n" \
"v_mfma_f32_16x16x32_bf16 v[168:171], a[24:27], a[220:223], 0\n" \
"ds_read_b64_tr_b16 a[208:209], v33 offset:9216\n" \
"ds_read_b64_tr_b16 a[210:211], v33 offset:13824\n" \
"v_mfma_f32_16x16x32_bf16 v[168:171], a[28:31], a[224:227], v[168:171]\n" \
"ds_read_b64_tr_b16 a[212:213], v33 offset:9248\n" \
"ds_read_b64_tr_b16 a[214:215], v33 offset:13856\n" \
"v_mfma_f32_16x16x32_bf16 v[168:171], a[192:195], a[228:231], v[168:171]\n" \
"ds_read_b64_tr_b16 a[216:217], v33 offset:9280\n" \
"ds_read_b64_tr_b16 a[218:219], v33 offset:13888\n" \
"s_mov_b64 exec, 1\n" \
"buffer_store_dword v39, off, s[56:59], s70 offset:0 sc1\n" \
"s_mov_b64 exec, -1\n" \
"v_mfma_f32_16x16x32_bf16 v[172:175], a[192:195], a[244:247], 0\n" \
"ds_read_b64_tr_b16 a[0:1], v33 offset:9312\n" \
"ds_read_b64_tr_b16 a[2:3], v33 offset:13920\n" \
"v_mfma_f32_16x16x32_bf16 v[172:175], a[28:31], a[240:243], v[172:175]\n" \
"ds_read_b64_tr_b16 a[4:5], v33 offset:9344\n" \
"ds_read_b64_tr_b16 a[6:7], v33 offset:13952\n" \
"v_mfma_f32_16x16x32_bf16 v[172:175], a[24:27], a[236:239], v[172:175]\n" \
"ds_read_b64_tr_b16 a[8:9], v33 offset:9376\n" \
"ds_read_b64_tr_b16 a[10:11], v33 offset:13984\n" \
"v_mfma_f32_16x16x32_bf16 v[240:243], a[24:27], v[64:67], 0\n" \
"ds_read_b64_tr_b16 a[12:13], v33 offset:9408\n" \
"ds_read_b64_tr_b16 a[14:15], v33 offset:14016\n" \
"v_mfma_f32_16x16x32_bf16 v[240:243], a[28:31], v[68:71], v[240:243]\n" \
"ds_read_b64_tr_b16 a[16:17], v33 offset:9440\n" \
"ds_read_b64_tr_b16 a[18:19], v33 offset:14048\n" \
"v_mfma_f32_16x16x32_bf16 v[240:243], a[192:195], v[72:75], v[240:243]\n" \
"ds_read_b64_tr_b16 a[20:21], v33 offset:9472\n" \
"ds_read_b64_tr_b16 a[22:23], v33 offset:14080\n" \
"v_mfma_f32_16x16x32_bf16 v[244:247], a[192:195], v[96:99], 0\n" \
"ds_read_b64_tr_b16 a[192:193], v35 offset:32\n" \
"ds_read_b64_tr_b16 a[194:195], v35 offset:2336\n" \
"v_mfma_f32_16x16x32_bf16 v[244:247], a[28:31], v[84:87], v[244:247]\n" \
"ds_read_b64_tr_b16 a[28:29], v35 offset:10368\n" \
"ds_read_b64_tr_b16 a[30:31], v35 offset:12672\n" \
"v_mfma_f32_16x16x32_bf16 v[244:247], a[24:27], v[80:83], v[244:247]\n" \
"ds_read_b64_tr_b16 a[24:25], v35 offset:0\n" \
"ds_read_b64_tr_b16 a[26:27], v35 offset:2304\n" \
"v_mfma_f32_16x16x32_bf16 v[176:179], a[196:199], a[220:223], 0\n" \
"buffer_load_dwordx4 a[88:91], v32, s[56:59], s66 offen offset:2048 sc1\n" \
"v_mfma_f32_16x16x32_bf16 v[176:179], a[200:203], a[224:227], v[176:179]\n" \
"v_readlane_b32 s66, v11, 43\n" \
"v_readlane_b32 s70, v11, 5\n" \
"v_mfma_f32_16x16x32_bf16 v[176:179], a[204:207], a[228:231], v[176:179]\n" \
"v_mfma_f32_16x16x32_bf16 v[180:183], a[204:207], a[244:247], 0\n" \
"v_mfma_f32_16x16x32_bf16 v[180:183], a[200:203], a[240:243], v[180:183]\n" \
"v_mfma_f32_16x16x32_bf16 v[180:183], a[196:199], a[236:239], v[180:183]\n" \
"buffer_load_dwordx4 v0, s[80:83], s67 offen offset:0 lds\n" \
"v_mfma_f32_16x16x32_bf16 v[248:251], a[196:199], v[64:67], 0\n" \
"v_mfma_f32_16x16x32_bf16 v[248:251], a[200:203], v[68:71], v[248:251]\n" \
"v_mfma_f32_16x16x32_bf16 v[248:251], a[204:207], v[72:75], v[248:251]\n" \
"v_mfma_f32_16x16x32_bf16 v[252:255], a[204:207], v[96:99], 0\n" \
"ds_read_b64_tr_b16 a[204:205], v35 offset:10432\n" \
"ds_read_b64_tr_b16 a[206:207], v35 offset:12736\n" \
"L_pf21:\n" \
"s_cbranch_execz L_pf23\n" \
"s_nop 0\n" \
"v_mfma_f32_16x16x32_bf16 v[252:255], a[200:203], v[84:87], v[252:255]\n" \
"ds_read_b64_tr_b16 a[200:201], v35 offset:64\n" \
"ds_read_b64_tr_b16 a[202:203], v35 offset:2368\n" \
"v_mfma_f32_16x16x32_bf16 v[252:255], a[196:199], v[80:83], v[252:255]\n" \
"ds_read_b64_tr_b16 a[196:197], v35 offset:10400\n" \
"s_waitcnt lgkmcnt(9)\n" \
"v_mfma_f32_16x16x32_bf16 v[112:115], a[208:211], a[232:235], v[112:115]\n" \
"buffer_load_dwordx4 v1, s[80:83], s67 offen offset:1024 lds\n" \
"v_mfma_f32_16x16x32_bf16 v[116:119], a[208:211], a[248:251], v[116:119]\n" \
"ds_read_b64_tr_b16 a[198:199], v35 offset:12704\n" \
"v_mfma_f32_16x16x32_bf16 v[184:187], a[208:211], v[76:79], v[184:187]\n" \
"v_mfma_f32_16x16x32_bf16 v[188:191], a[208:211], v[100:103], v[188:191]\n" \
"ds_read_b64_tr_b16 a[208:209], v35 offset:96\n" \
"ds_read_b64_tr_b16 a[210:211], v35 offset:2400\n" \
"v_mfma_f32_16x16x32_bf16 v[120:123], a[212:215], a[232:235], v[120:123]\n" \
"v_mfma_f32_16x16x32_bf16 v[124:127], a[212:215], a[248:251], v[124:127]\n" \
"buffer_load_dwordx4 v2, s[80:83], s67 offen offset:2048 lds\n" \
"v_mfma_f32_16x16x32_bf16 v[192:195], a[212:215], v[76:79], v[192:195]\n" \
"v_mfma_f32_16x16x32_bf16 v[196:199], a[212:215], v[100:103], v[196:199]\n" \
"ds_read_b64_tr_b16 a[212:213], v35 offset:10464\n" \
"ds_read_b64_tr_b16 a[214:215], v35 offset:12768\n" \
"v_mfma_f32_16x16x32_bf16 v[128:131], a[216:219], a[232:235], v[128:131]\n" \
"v_mfma_f32_16x16x32_bf16 v[132:135], a[216:219], a[248:251], v[132:135]\n" \
"v_mfma_f32_16x16x32_bf16 v[200:203], a[216:219], v[76:79], v[200:203]\n" \
"buffer_load_dwordx4 v3, s[80:83], s67 offen offset:3072 lds\n" \
"v_mfma_f32_16x16x32_bf16 v[204:207], a[216:219], v[100:103], v[204:207]\n" \
"ds_read_b64_tr_b16 a[216:217], v35 offset:128\n" \
"ds_read_b64_tr_b16 a[218:219], v35 offset:2432\n" \
"v_mfma_f32_16x16x32_bf16 v[136:139], a[0:3], a[232:235], v[136:139]\n" \
"s_add_u32 m0, s99, 0x15400\n" \
"v_mfma_f32_16x16x32_bf16 v[140:143], a[0:3], a[248:251], v[140:143]\n" \
"v_mfma_f32_16x16x32_bf16 v[208:211], a[0:3], v[76:79], v[208:211]\n" \
"v_mfma_f32_16x16x32_bf16 v[212:215], a[0:3], v[100:103], v[212:215]\n" \
"ds_read_b64_tr_b16 a[0:1], v35 offset:10496\n" \
"ds_read_b64_tr_b16 a[2:3], v35 offset:12800\n" \
"v_mfma_f32_16x16x32_bf16 v[144:147], a[4:7], a[232:235], v[144:147]\n" \
"buffer_load_dwordx4 v4, s[80:83], s67 offen offset:0 lds\n" \
"v_mfma_f32_16x16x32_bf16 v[148:151], a[4:7], a[248:251], v[148:151]\n" \
"v_mfma_f32_16x16x32_bf16 v[216:219], a[4:7], v[76:79], v[216:219]\n" \
"v_mfma_f32_16x16x32_bf16 v[220:223], a[4:7], v[100:103], v[220:223]\n" \
"ds_read_b64_tr_b16 a[4:5], v35 offset:160\n" \
"ds_read_b64_tr_b16 a[6:7], v35 offset:2464\n" \
"v_mfma_f32_16x16x32_bf16 v[152:155], a[8:11], a[232:235], v[152:155]\n" \
"v_mfma_f32_16x16x32_bf16 v[156:159], a[8:11], a[248:251], v[156:159]\n" \
"buffer_load_dwordx4 v5, s[80:83], s67 offen offset:1024 lds\n" \
"v_mfma_f32_16x16x32_bf16 v[224:227], a[8:11], v[76:79], v[224:227]\n" \
"v_mfma_f32_16x16x32_bf16 v[228:231], a[8:11], v[100:103], v[228:231]\n" \
"ds_read_b64_tr_b16 a[8:9], v35 offset:10528\n" \
"ds_read_b64_tr_b16 a[10:11], v35 offset:12832\n" \
"v_mfma_f32_16x16x32_bf16 v[160:163], a[12:15], a[232:235], v[160:163]\n" \
"v_mfma_f32_16x16x32_bf16 v[164:167], a[12:15], a[248:251], v[164:167]\n" \
"v_mfma_f32_16x16x32_bf16 v[232:235], a[12:15], v[76:79], v[232:235]\n" \
"buffer_load_dwordx4 v6, s[80:83], s67 offen offset:2048 lds\n" \
"v_mfma_f32_16x16x32_bf16 v[236:239], a[12:15], v[100:103], v[236:239]\n" \
"ds_read_b64_tr_b16 a[12:13], v35 offset:192\n" \
"ds_read_b64_tr_b16 a[14:15], v35 offset:2496\n" \
"v_mfma_f32_16x16x32_bf16 v[168:171], a[16:19], a[232:235], v[168:171]\n" \
"v_mfma_f32_16x16x32_bf16 v[172:175], a[16:19], a[248:251], v[172:175]\n" \
"v_mfma_f32_16x16x32_bf16 v[240:243], a[16:19], v[76:79], v[240:243]\n" \
"v_mfma_f32_16x16x32_bf16 v[244:247], a[16:19], v[100:103], v[244:247]\n" \
"buffer_load_dwordx4 v7, s[80:83], s67 offen offset:3072 lds\n" \
"v_mfma_f32_16x16x32_bf16 v[176:179], a[20:23], a[232:235], v[176:179]\n" \
"s_add_u32 m0, s99, 0x16400\n" \
"ds_read_b64_tr_b16 a[16:17], v35 offset:10560\n" \
"v_mfma_f32_16x16x32_bf16 v[180:183], a[20:23], a[248:251], v[180:183]\n" \
"ds_read_b64_tr_b16 a[18:19], v35 offset:12864\n" \
"v_mfma_f32_16x16x32_bf16 v[248:251], a[20:23], v[76:79], v[248:251]\n" \
"v_mfma_f32_16x16x32_bf16 v[252:255], a[20:23], v[100:103], v[252:255]\n" \
"ds_read_b64_tr_b16 a[20:21], v35 offset:224\n" \
"s_waitcnt lgkmcnt(7)\n" \
"v_mfma_f32_16x16x32_bf16 v[112:115], a[24:27], a[32:35], v[112:115]\n" \
"buffer_load_dwordx4 v8, s[80:83], s67 offen offset:0 lds\n" \
"v_mfma_f32_16x16x32_bf16 v[112:115], a[28:31], a[36:39], v[112:115]\n" \
"s_add_u32 m0, s92, 0x0\n" \
"ds_read_b64_tr_b16 a[22:23], v35 offset:2528\n" \
"v_mfma_f32_16x16x32_bf16 v[116:119], a[28:31], a[44:47], v[116:119]\n" \
"v_readlane_b32 s67, v11, 44\n" \
"v_mfma_f32_16x16x32_bf16 v[116:119], a[24:27], a[40:43], v[116:119]\n" \
"v_mfma_f32_16x16x32_bf16 v[184:187], a[24:27], a[48:51], v[184:187]\n" \
"v_mfma_f32_16x16x32_bf16 v[184:187], a[28:31], a[52:55], v[184:187]\n" \
"buffer_load_dword v47, v38, s[56:59], s71 offen sc1\n" \
"v_mfma_f32_16x16x32_bf16 v[188:191], a[28:31], a[60:63], v[188:191]\n" \
"ds_read_b64_tr_b16 a[28:29], v35 offset:256\n" \
"ds_read_b64_tr_b16 a[30:31], v35 offset:2560\n" \
"v_mfma_f32_16x16x32_bf16 v[188:191], a[24:27], a[56:59], v[188:191]\n" \
"ds_read_b64_tr_b16 a[24:25], v35 offset:10592\n" \
"ds_read_b64_tr_b16 a[26:27], v35 offset:12896\n" \
"v_mfma_f32_16x16x32_bf16 v[120:123], a[192:195], a[32:35], v[120:123]\n" \
"v_mfma_f32_16x16x32_bf16 v[120:123], a[196:199], a[36:39], v[120:123]\n" \
"v_mfma_f32_16x16x32_bf16 v[124:127], a[196:199], a[44:47], v[124:127]\n" \
"buffer_load_dwordx4 v0, s[80:83], s73 offen offset:0 lds\n" \
"v_mfma_f32_16x16x32_bf16 v[124:127], a[192:195], a[40:43], v[124:127]\n" \
"v_mfma_f32_16x16x32_bf16 v[192:195], a[192:195], a[48:51], v[192:195]\n" \
"v_mfma_f32_16x16x32_bf16 v[192:195], a[196:199], a[52:55], v[192:195]\n" \
"v_mfma_f32_16x16x32_bf16 v[196:199], a[196:199], a[60:63], v[196:199]\n" \
"v_mfma_f32_16x16x32_bf16 v[196:199], a[192:195], a[56:59], v[196:199]\n" \
"buffer_load_dwordx4 v1, s[80:83], s73 offen offset:1024 lds\n" \
"v_mfma_f32_16x16x32_bf16 v[128:131], a[200:203], a[32:35], v[128:131]\n" \
"ds_read_b64_tr_b16 a[192:193], v35 offset:10624\n" \
"ds_read_b64_tr_b16 a[194:195], v35 offset:12928\n" \
"v_mfma_f32_16x16x32_bf16 v[128:131], a[204:207], a[36:39], v[128:131]\n" \
"v_mfma_f32_16x16x32_bf16 v[132:135], a[204:207], a[44:47], v[132:135]\n" \
"v_mfma_f32_16x16x32_bf16 v[132:135], a[200:203], a[40:43], v[132:135]\n" \
"v_mfma_f32_16x16x32_bf16 v[200:203], a[200:203], a[48:51], v[200:203]\n" \
"buffer_load_dwordx4 v2, s[80:83], s73 offen offset:2048 lds\n" \
"v_mfma_f32_16x16x32_bf16 v[200:203], a[204:207], a[52:55], v[200:203]\n" \
"v_mfma_f32_16x16x32_bf16 v[204:207], a[204:207], a[60:63], v[204:207]\n" \
"v_mfma_f32_16x16x32_bf16 v[204:207], a[200:203], a[56:59], v[204:207]\n" \
"v_mfma_f32_16x16x32_bf16 v[136:139], a[208:211], a[32:35], v[136:139]\n" \
"v_mfma_f32_16x16x32_bf16 v[136:139], a[212:215], a[36:39], v[136:139]\n" \
"buffer_load_dwordx4 v3, s[80:83], s73 offen offset:3072 lds\n" \
"v_mfma_f32_16x16x32_bf16 v[140:143], a[212:215], a[44:47], v[140:143]\n" \
"s_add_u32 m0, s92, 0x1000\n" \
"v_mfma_f32_16x16x32_bf16 v[140:143], a[208:211], a[40:43], v[140:143]\n" \
"v_mfma_f32_16x16x32_bf16 v[208:211], a[208:211], a[48:51], v[208:211]\n" \
"v_mfma_f32_16x16x32_bf16 v[208:211], a[212:215], a[52:55], v[208:211]\n" \
"v_mfma_f32_16x16x32_bf16 v[212:215], a[212:215], a[60:63], v[212:215]\n" \
"buffer_load_dwordx4 v4, s[80:83], s73 offen offset:0 lds\n" \
"v_mfma_f32_16x16x32_bf16 v[212:215], a[208:211], a[56:59], v[212:215]\n" \
"v_mfma_f32_16x16x32_bf16 v[144:147], a[216:219], a[32:35], v[144:147]\n" \
"v_mfma_f32_16x16x32_bf16 v[144:147], a[0:3], a[36:39], v[144:147]\n" \
"v_mfma_f32_16x16x32_bf16 v[148:151], a[0:3], a[44:47], v[148:151]\n" \
"v_mfma_f32_16x16x32_bf16 v[148:151], a[216:219], a[40:43], v[148:151]\n" \
"buffer_load_dwordx4 v5, s[80:83], s73 offen offset:1024 lds\n" \
"v_mfma_f32_16x16x32_bf16 v[216:219], a[216:219], a[48:51], v[216:219]\n" \
"v_mfma_f32_16x16x32_bf16 v[216:219], a[0:3], a[52:55], v[216:219]\n" \
"v_mfma_f32_16x16x32_bf16 v[220:223], a[0:3], a[60:63], v[220:223]\n" \
"v_mfma_f32_16x16x32_bf16 v[220:223], a[216:219], a[56:59], v[220:223]\n" \
"v_mfma_f32_16x16x32_bf16 v[152:155], a[4:7], a[32:35], v[152:155]\n" \
"buffer_load_dwordx4 v6, s[80:83], s73 offen offset:2048 lds\n" \
"s_waitcnt lgkmcnt(0)\n" \
"v_mfma_f32_16x16x32_bf16 v[152:155], a[8:11], a[36:39], v[152:155]\n" \
"v_mfma_f32_16x16x32_bf16 v[156:159], a[8:11], a[44:47], v[156:159]\n" \
"v_mfma_f32_16x16x32_bf16 v[156:159], a[4:7], a[40:43], v[156:159]\n" \
"v_mfma_f32_16x16x32_bf16 v[224:227], a[4:7], a[48:51], v[224:227]\n" \
"buffer_load_dwordx4 v7, s[80:83], s73 offen offset:3072 lds\n" \
"s_add_u32 m0, s92, 0x2000\n" \
"v_mfma_f32_16x16x32_bf16 v[224:227], a[8:11], a[52:55], v[224:227]\n" \
"v_mfma_f32_16x16x32_bf16 v[228:231], a[8:11], a[60:63], v[228:231]\n" \
"v_mfma_f32_16x16x32_bf16 v[228:231], a[4:7], a[56:59], v[228:231]\n" \
"s_waitcnt vmcnt(23)\n" \
"s_barrier\n" \
"buffer_load_dwordx4 v8, s[80:83], s73 offen offset:0 lds\n" \
"s_mov_b32 m0, s94\n" \
"v_mfma_f32_16x16x32_bf16 v[160:163], a[12:15], a[32:35], v[160:163]\n" \
"ds_read_b64_tr_b16 a[196:197], v10 offset:27648\n" \
"ds_read_b64_tr_b16 a[198:199], v10 offset:32256\n" \
"v_mfma_f32_16x16x32_bf16 v[160:163], a[16:19], a[36:39], v[160:163]\n" \
"ds_read_b64_tr_b16 a[200:201], v10 offset:36864\n" \
"ds_read_b64_tr_b16 a[202:203], v10 offset:41472\n" \
"v_mfma_f32_16x16x32_bf16 v[164:167], a[16:19], a[44:47], v[164:167]\n" \
"ds_read_b64_tr_b16 a[204:205], v10 offset:27680\n" \
"ds_read_b64_tr_b16 a[206:207], v10 offset:32288\n" \
"v_mfma_f32_16x16x32_bf16 v[164:167], a[12:15], a[40:43], v[164:167]\n" \
"buffer_load_dwordx4 v9, s[84:87], 0 offen lds\n" \
"v_mfma_f32_16x16x32_bf16 v[232:235], a[12:15], a[48:51], v[232:235]\n" \
"ds_read_b64_tr_b16 a[208:209], v10 offset:36896\n" \
"ds_read_b64_tr_b16 a[210:211], v10 offset:41504\n" \
"v_mfma_f32_16x16x32_bf16 v[232:235], a[16:19], a[52:55], v[232:235]\n" \
"ds_read_b64_tr_b16 a[212:213], v10 offset:27712\n" \
"ds_read_b64_tr_b16 a[214:215], v10 offset:32320\n" \
"v_mfma_f32_16x16x32_bf16 v[236:239], a[16:19], a[60:63], v[236:239]\n" \
"ds_read_b64_tr_b16 a[216:217], v10 offset:36928\n" \
"ds_read_b64_tr_b16 a[218:219], v10 offset:41536\n" \
"v_mfma_f32_16x16x32_bf16 v[236:239], a[12:15], a[56:59], v[236:239]\n" \
"ds_read_b64_tr_b16 a[0:1], v10 offset:27744\n" \
"ds_read_b64_tr_b16 a[2:3], v10 offset:32352\n" \
"buffer_load_dwordx4 a[128:131], v20, s[56:59], s68 offen offset:0 sc1\n" \
"v_mfma_f32_16x16x32_bf16 v[168:171], a[20:23], a[32:35], v[168:171]\n" \
"ds_read_b64_tr_b16 a[4:5], v10 offset:36960\n" \
"ds_read_b64_tr_b16 a[6:7], v10 offset:41568\n" \
"v_mfma_f32_16x16x32_bf16 v[168:171], a[24:27], a[36:39], v[168:171]\n" \
"ds_read_b64_tr_b16 a[8:9], v10 offset:27776\n" \
"ds_read_b64_tr_b16 a[10:11], v10 offset:32384\n" \
"v_mfma_f32_16x16x32_bf16 v[172:175], a[24:27], a[44:47], v[172:175]\n" \
"ds_read_b64_tr_b16 a[12:13], v10 offset:36992\n" \
"ds_read_b64_tr_b16 a[14:15], v10 offset:41600\n" \
"v_mfma_f32_16x16x32_bf16 v[172:175], a[20:23], a[40:43], v[172:175]\n" \
"ds_read_b64_tr_b16 a[16:17], v10 offset:27808\n" \
"ds_read_b64_tr_b16 a[18:19], v10 offset:32416\n" \
"buffer_load_dwordx4 a[132:135], v20, s[56:59], s68 offen offset:1024 sc1\n" \
"v_mfma_f32_16x16x32_bf16 v[240:243], a[20:23], a[48:51], v[240:243]\n" \
"s_waitcnt vmcnt(12)\n" \
"v_cmp_ne_u32_e32 vcc, s62, v47\n" \
"v_mfma_f32_16x16x32_bf16 v[240:243], a[24:27], a[52:55], v[240:243]\n" \
"v_readlane_b32 s94, v11, 47\n" \
"s_mov_b32 s84, 0\n" \
"v_mfma_f32_16x16x32_bf16 v[244:247], a[24:27], a[60:63], v[244:247]\n" \
"ds_read_b64_tr_b16 a[24:25], v10 offset:27840\n" \
"ds_read_b64_tr_b16 a[26:27], v10 offset:32448\n" \
"v_mfma_f32_16x16x32_bf16 v[244:247], a[20:23], a[56:59], v[244:247]\n" \
"ds_read_b64_tr_b16 a[20:21], v10 offset:37024\n" \
"ds_read_b64_tr_b16 a[22:23], v10 offset:41632\n" \
"buffer_load_dwordx4 a[136:139], v20, s[56:59], s68 offen offset:2048 sc1\n" \
"v_mfma_f32_16x16x32_bf16 v[176:179], a[28:31], a[32:35], v[176:179]\n" \
"s_cbranch_vccnz L_sb_%=\n" \
"L_rb_%=:\n" \
"v_readlane_b32 s71, v11, 46\n" \
"v_mfma_f32_16x16x32_bf16 v[176:179], a[192:195], a[36:39], v[176:179]\n" \
"v_add_u32_e32 v38, 0x1000, v20\n" \
"v_add_u32_e32 v47, 0x2000, v20\n" \
"v_mfma_f32_16x16x32_bf16 v[180:183], a[192:195], a[44:47], v[180:183]\n" \
"v_cmp_eq_u32_e32 vcc, 0, v44\n" \
"v_mfma_f32_16x16x32_bf16 v[180:183], a[28:31], a[40:43], v[180:183]\n" \
"v_mfma_f32_16x16x32_bf16 v[248:251], a[28:31], a[48:51], v[248:251]\n" \
"buffer_load_dwordx4 a[140:143], v20, s[56:59], s68 offen offset:3072 sc1\n" \
"v_mfma_f32_16x16x32_bf16 v[248:251], a[192:195], a[52:55], v[248:251]\n" \
"v_cndmask_b32_e32 v50, 0, v45, vcc\n" \
"v_cmp_eq_u32_e32 vcc, 1, v44\n" \
"v_mfma_f32_16x16x32_bf16 v[252:255], a[192:195], a[60:63], v[252:255]\n" \
"ds_read_b64_tr_b16 a[192:193], v10 offset:27872\n" \
"ds_read_b64_tr_b16 a[194:195], v10 offset:32480\n" \
"v_mfma_f32_16x16x32_bf16 v[252:255], a[28:31], a[56:59], v[252:255]\n" \
"ds_read_b64_tr_b16 a[28:29], v10 offset:37056\n" \
"s_waitcnt lgkmcnt(9)\n" \
"v_mfma_f32_16x16x32_bf16 v[112:115], a[196:199], a[64:67], v[112:115]\n" \
"ds_read_b64_tr_b16 a[30:31], v10 offset:41664\n" \
"v_cndmask_b32_e32 v51, 0, v45, vcc\n" \
"v_mfma_f32_16x16x32_bf16 v[112:115], a[200:203], a[68:71], v[112:115]\n" \
"buffer_load_dwordx4 a[156:159], v32, s[56:59], s68 offen offset:3072 sc1\n" \
"v_mfma_f32_16x16x32_bf16 v[116:119], a[200:203], a[84:87], v[116:119]\n" \
"ds_read_b64_tr_b16 a[200:201], v10 offset:27904\n" \
"ds_read_b64_tr_b16 a[202:203], v10 offset:32512\n" \
"v_mfma_f32_16x16x32_bf16 v[116:119], a[196:199], a[80:83], v[116:119]\n" \
"ds_read_b64_tr_b16 a[196:197], v10 offset:37088\n" \
"ds_read_b64_tr_b16 a[198:199], v10 offset:41696\n" \
"v_mfma_f32_16x16x32_bf16 v[120:123], a[204:207], a[64:67], v[120:123]\n" \
"v_bfe_u32 v45, v20, 1, 5\n" \
"v_and_b32_e32 v45, 24, v45\n" \
"v_mfma_f32_16x16x32_bf16 v[120:123], a[208:211], a[68:71], v[120:123]\n" \
"v_and_b32_e32 v44, 0x3c0, v20\n" \
"v_add_u32_e32 v45, 0x1d400, v45\n" \
"v_mfma_f32_16x16x32_bf16 v[124:127], a[208:211], a[84:87], v[124:127]\n" \
"buffer_load_dwordx4 a[152:155], v32, s[56:59], s68 offen offset:2048 sc1\n" \
"v_mfma_f32_16x16x32_bf16 v[124:127], a[204:207], a[80:83], v[124:127]\n" \
"ds_read_b64_tr_b16 a[204:205], v10 offset:37120\n" \
"ds_read_b64_tr_b16 a[206:207], v10 offset:41728\n" \
"v_mfma_f32_16x16x32_bf16 v[128:131], a[212:215], a[64:67], v[128:131]\n" \
"ds_read_b64_tr_b16 a[208:209], v10 offset:46080\n" \
"ds_read_b64_tr_b16 a[210:211], v10 offset:50688\n" \
"v_mfma_f32_16x16x32_bf16 v[128:131], a[216:219], a[68:71], v[128:131]\n" \
"v_add_u32_e32 v36, v44, v45\n" \
"v_mfma_f32_16x16x32_bf16 v[132:135], a[216:219], a[84:87], v[132:135]\n" \
"ds_read_b64_tr_b16 a[216:217], v10 offset:46112\n" \
"ds_read_b64_tr_b16 a[218:219], v10 offset:50720\n" \
"buffer_load_dwordx4 a[148:151], v32, s[56:59], s68 offen offset:1024 sc1\n" \
"v_mfma_f32_16x16x32_bf16 v[132:135], a[212:215], a[80:83], v[132:135]\n" \
"ds_read_b64_tr_b16 a[212:213], v10 offset:55296\n" \
"ds_read_b64_tr_b16 a[214:215], v10 offset:59904\n" \
"v_mfma_f32_16x16x32_bf16 v[136:139], a[0:3], a[64:67], v[136:139]\n" \
"v_mfma_f32_16x16x32_bf16 v[136:139], a[4:7], a[68:71], v[136:139]\n" \
"v_mfma_f32_16x16x32_bf16 v[140:143], a[4:7], a[84:87], v[140:143]\n" \
"ds_read_b64_tr_b16 a[4:5], v10 offset:46144\n" \
"ds_read_b64_tr_b16 a[6:7], v10 offset:50752\n" \
"buffer_load_dwordx4 a[144:147], v32, s[56:59], s68 offen offset:0 sc1\n" \
"v_mfma_f32_16x16x32_bf16 v[140:143], a[0:3], a[80:83], v[140:143]\n" \
"ds_read_b64_tr_b16 a[0:1], v10 offset:55328\n" \
"ds_read_b64_tr_b16 a[2:3], v10 offset:59936\n" \
"v_mfma_f32_16x16x32_bf16 v[144:147], a[8:11], a[64:67], v[144:147]\n" \
"v_readlane_b32 s68, v11, 45\n" \
"v_mfma_f32_16x16x32_bf16 v[144:147], a[12:15], a[68:71], v[144:147]\n" \
"v_mfma_f32_16x16x32_bf16 v[148:151], a[12:15], a[84:87], v[148:151]\n" \
"ds_read_b64_tr_b16 a[12:13], v10 offset:46176\n" \
"ds_read_b64_tr_b16 a[14:15], v10 offset:50784\n" \
"buffer_load_dwordx4 a[96:99], v20, s[56:59], s69 offen offset:0 sc1\n" \
"v_mfma_f32_16x16x32_bf16 v[148:151], a[8:11], a[80:83], v[148:151]\n" \
"ds_read_b64_tr_b16 a[8:9], v10 offset:55360\n" \
"s_waitcnt lgkmcnt(15)\n" \
"v_mfma_f32_16x16x32_bf16 v[152:155], a[16:19], a[64:67], v[152:155]\n" \
"ds_read_b64_tr_b16 a[10:11], v10 offset:59968\n" \
"v_mfma_f32_16x16x32_bf16 v[152:155], a[20:23], a[68:71], v[152:155]\n" \
"v_mfma_f32_16x16x32_bf16 v[156:159], a[20:23], a[84:87], v[156:159]\n" \
"ds_read_b64_tr_b16 a[20:21], v10 offset:46208\n" \
"ds_read_b64_tr_b16 a[22:23], v10 offset:50816\n" \
"buffer_load_dwordx4 a[100:103], v20, s[56:59], s69 offen offset:1024 sc1\n" \
"v_mfma_f32_16x16x32_bf16 v[156:159], a[16:19], a[80:83], v[156:159]\n" \
"ds_read_b64_tr_b16 a[16:17], v10 offset:55392\n" \
"ds_read_b64_tr_b16 a[18:19], v10 offset:60000\n" \
"v_mfma_f32_16x16x32_bf16 v[160:163], a[24:27], a[64:67], v[160:163]\n" \
"v_mfma_f32_16x16x32_bf16 v[160:163], a[28:31], a[68:71], v[160:163]\n" \
"v_mfma_f32_16x16x32_bf16 v[164:167], a[28:31], a[84:87], v[164:167]\n" \
"ds_read_b64_tr_b16 a[28:29], v10 offset:46240\n" \
"ds_read_b64_tr_b16 a[30:31], v10 offset:50848\n" \
"buffer_load_dwordx4 a[104:107], v20, s[56:59], s69 offen offset:2048 sc1\n" \
"v_mfma_f32_16x16x32_bf16 v[164:167], a[24:27], a[80:83], v[164:167]\n" \
"ds_read_b64_tr_b16 a[24:25], v10 offset:55424\n" \
"ds_read_b64_tr_b16 a[26:27], v10 offset:60032\n" \
"v_mfma_f32_16x16x32_bf16 v[168:171], a[192:195], a[64:67], v[168:171]\n" \
"v_mfma_f32_16x16x32_bf16 v[168:171], a[196:199], a[68:71], v[168:171]\n" \
"v_mfma_f32_16x16x32_bf16 v[172:175], a[196:199], a[84:87], v[172:175]\n" \
"ds_read_b64_tr_b16 a[196:197], v10 offset:46272\n" \
"ds_read_b64_tr_b16 a[198:199], v10 offset:50880\n" \
"buffer_load_dwordx4 a[108:111], v20, s[56:59], s69 offen offset:3072 sc1\n" \
"v_mfma_f32_16x16x32_bf16 v[172:175], a[192:195], a[80:83], v[172:175]\n" \
"ds_read_b64_tr_b16 a[192:193], v10 offset:55456\n" \
"ds_read_b64_tr_b16 a[194:195], v10 offset:60064\n" \
"v_mfma_f32_16x16x32_bf16 v[176:179], a[200:203], a[64:67], v[176:179]\n" \
"s_waitcnt lgkmcnt(13)\n" \
"v_mfma_f32_16x16x32_bf16 v[176:179], a[204:207], a[68:71], v[176:179]\n" \
"v_mfma_f32_16x16x32_bf16 v[180:183], a[204:207], a[84:87], v[180:183]\n" \
"ds_read_b64_tr_b16 a[204:205], v10 offset:46304\n" \
"ds_read_b64_tr_b16 a[206:207], v10 offset:50912\n" \
"buffer_load_dwordx4 a[124:127], v32, s[56:59], s69 offen offset:3072 sc1\n" \
"v_mfma_f32_16x16x32_bf16 v[180:183], a[200:203], a[80:83], v[180:183]\n" \
"ds_read_b64_tr_b16 a[200:201], v10 offset:55488\n" \
"ds_read_b64_tr_b16 a[202:203], v10 offset:60096\n" \
"v_mfma_f32_16x16x32_bf16 v[112:115], a[208:211], a[72:75], v[112:115]\n" \
"v_mfma_f32_16x16x32_bf16 v[112:115], a[212:215], a[76:79], v[112:115]\n" \
"v_mfma_f32_16x16x32_bf16 v[116:119], a[212:215], a[92:95], v[116:119]\n" \
"ds_read_b64_tr_b16 a[212:213], v10 offset:46336\n" \
"ds_read_b64_tr_b16 a[214:215], v10 offset:50944\n" \
"buffer_load_dwordx4 a[120:123], v32, s[56:59], s69 offen offset:2048 sc1\n" \
"v_mfma_f32_16x16x32_bf16 v[116:119], a[208:211], a[88:91], v[116:119]\n" \
"ds_read_b64_tr_b16 a[208:209], v10 offset:55520\n" \
"ds_read_b64_tr_b16 a[210:211], v10 offset:60128\n" \
"v_mfma_f32_16x16x32_bf16 v[120:123], a[216:219], a[72:75], v[120:123]\n" \
"v_mfma_f32_16x16x32_bf16 v[120:123], a[0:3], a[76:79], v[120:123]\n" \
"v_mfma_f32_16x16x32_bf16 v[124:127], a[0:3], a[92:95], v[124:127]\n" \
"v_mfma_f32_16x16x32_bf16 v[124:127], a[216:219], a[88:91], v[124:127]\n" \
"buffer_load_dwordx4 a[116:119], v32, s[56:59], s69 offen offset:1024 sc1\n" \
"v_mfma_f32_16x16x32_bf16 v[128:131], a[4:7], a[72:75], v[128:131]\n" \
"ds_read_b64_tr_b16 a[216:217], v10 offset:55552\n" \
"s_waitcnt lgkmcnt(9)\n" \
"v_mfma_f32_16x16x32_bf16 v[128:131], a[8:11], a[76:79], v[128:131]\n" \
"ds_read_b64_tr_b16 a[218:219], v10 offset:60160\n" \
"v_mfma_f32_16x16x32_bf16 v[132:135], a[8:11], a[92:95], v[132:135]\n" \
"v_mfma_f32_16x16x32_bf16 v[132:135], a[4:7], a[88:91], v[132:135]\n" \
"v_mfma_f32_16x16x32_bf16 v[136:139], a[12:15], a[72:75], v[136:139]\n" \
"buffer_load_dwordx4 a[112:115], v32, s[56:59], s69 offen offset:0 sc1\n" \
"v_mfma_f32_16x16x32_bf16 v[136:139], a[16:19], a[76:79], v[136:139]\n" \
"v_readlane_b32 s69, v11, 49\n" \
"v_mfma_f32_16x16x32_bf16 v[140:143], a[16:19], a[92:95], v[140:143]\n" \
"v_mfma_f32_16x16x32_bf16 v[140:143], a[12:15], a[88:91], v[140:143]\n" \
"s_waitcnt vmcnt(16)\n" \
"s_barrier\n" \
"buffer_load_dwordx4 a[32:35], v20, s[56:59], s64 offen offset:0 sc1\n" \
"v_mfma_f32_16x16x32_bf16 v[144:147], a[20:23], a[72:75], v[144:147]\n" \
"ds_read_b64_tr_b16 a[0:1], v33 offset:18432\n" \
"ds_read_b64_tr_b16 a[2:3], v33 offset:23040\n" \
"v_mfma_f32_16x16x32_bf16 v[144:147], a[24:27], a[76:79], v[144:147]\n" \
"ds_read_b64_tr_b16 a[4:5], v33 offset:27648\n" \
"ds_read_b64_tr_b16 a[6:7], v33 offset:32256\n" \
"v_mfma_f32_16x16x32_bf16 v[148:151], a[24:27], a[92:95], v[148:151]\n" \
"ds_read_b64_tr_b16 a[8:9], v33 offset:36864\n" \
"ds_read_b64_tr_b16 a[10:11], v33 offset:41472\n" \
"v_mfma_f32_16x16x32_bf16 v[148:151], a[20:23], a[88:91], v[148:151]\n" \
"ds_read_b64_tr_b16 a[12:13], v33 offset:46080\n" \
"ds_read_b64_tr_b16 a[14:15], v33 offset:50688\n" \
"buffer_load_dwordx4 a[36:39], v20, s[56:59], s64 offen offset:1024 sc1\n" \
"v_mfma_f32_16x16x32_bf16 v[152:155], a[28:31], a[72:75], v[152:155]\n" \
"ds_read_b64_tr_b16 a[16:17], v34 offset:0\n" \
"ds_read_b64_tr_b16 a[18:19], v34 offset:4608\n" \
"v_mfma_f32_16x16x32_bf16 v[152:155], a[192:195], a[76:79], v[152:155]\n" \
"ds_read_b64_tr_b16 a[20:21], v34 offset:9216\n" \
"ds_read_b64_tr_b16 a[22:23], v34 offset:13824\n" \
"v_mfma_f32_16x16x32_bf16 v[156:159], a[192:195], a[92:95], v[156:159]\n" \
"ds_read_b64_tr_b16 a[24:25], v34 offset:18432\n" \
"ds_read_b64_tr_b16 a[26:27], v34 offset:23040\n" \
"v_mfma_f32_16x16x32_bf16 v[156:159], a[28:31], a[88:91], v[156:159]\n" \
"ds_read_b64_tr_b16 a[28:29], v33 offset:0\n" \
"ds_read_b64_tr_b16 a[30:31], v33 offset:4608\n" \
"buffer_load_dwordx4 a[40:43], v20, s[56:59], s64 offen offset:2048 sc1\n" \
"v_mfma_f32_16x16x32_bf16 v[160:163], a[196:199], a[72:75], v[160:163]\n" \
"ds_read_b64_tr_b16 a[192:193], v33 offset:18464\n" \
"s_waitcnt lgkmcnt(15)\n" \
"v_mfma_f32_16x16x32_bf16 v[160:163], a[200:203], a[76:79], v[160:163]\n" \
"ds_read_b64_tr_b16 a[194:195], v33 offset:23072\n" \
"v_mfma_f32_16x16x32_bf16 v[164:167], a[200:203], a[92:95], v[164:167]\n" \
"buffer_load_dwordx4 a[44:47], v20, s[56:59], s64 offen offset:3072 sc1\n" \
"v_mfma_f32_16x16x32_bf16 v[164:167], a[196:199], a[88:91], v[164:167]\n" \
"ds_read_b64_tr_b16 a[196:197], v33 offset:27680\n" \
"ds_read_b64_tr_b16 a[198:199], v33 offset:32288\n" \
"v_mfma_f32_16x16x32_bf16 v[168:171], a[204:207], a[72:75], v[168:171]\n" \
"ds_read_b64_tr_b16 a[200:201], v33 offset:36896\n" \
"ds_read_b64_tr_b16 a[202:203], v33 offset:41504\n" \
"v_mfma_f32_16x16x32_bf16 v[168:171], a[208:211], a[76:79], v[168:171]\n" \
"v_mfma_f32_16x16x32_bf16 v[172:175], a[208:211], a[92:95], v[172:175]\n" \
"ds_read_b64_tr_b16 a[208:209], v34 offset:32\n" \
"ds_read_b64_tr_b16 a[210:211], v34 offset:4640\n" \
"buffer_load_dwordx4 a[60:63], v32, s[56:59], s64 offen offset:3072 sc1\n" \
"v_mfma_f32_16x16x32_bf16 v[172:175], a[204:207], a[88:91], v[172:175]\n" \
"ds_read_b64_tr_b16 a[204:205], v33 offset:46112\n" \
"ds_read_b64_tr_b16 a[206:207], v33 offset:50720\n" \
"v_mfma_f32_16x16x32_bf16 v[176:179], a[212:215], a[72:75], v[176:179]\n" \
"v_mfma_f32_16x16x32_bf16 v[176:179], a[216:219], a[76:79], v[176:179]\n" \
"v_mfma_f32_16x16x32_bf16 v[180:183], a[216:219], a[92:95], v[180:183]\n" \
"ds_read_b64_tr_b16 a[216:217], v34 offset:18464\n" \
"ds_read_b64_tr_b16 a[218:219], v34 offset:23072\n" \
"buffer_load_dwordx4 a[56:59], v32, s[56:59], s64 offen offset:2048 sc1\n" \
"v_mfma_f32_16x16x32_bf16 v[180:183], a[212:215], a[88:91], v[180:183]\n" \
"ds_read_b64_tr_b16 a[212:213], v34 offset:9248\n" \
"s_waitcnt vmcnt(21)\n" \
"v_mfma_f32_16x16x32_bf16 v[112:115], a[0:3], a[128:131], v[112:115]\n" \
"ds_read_b64_tr_b16 a[214:215], v34 offset:13856\n" \
"s_waitcnt vmcnt(20) lgkmcnt(13)\n" \
"v_mfma_f32_16x16x32_bf16 v[112:115], a[4:7], a[132:135], v[112:115]\n" \
"s_waitcnt vmcnt(19)\n" \
"v_mfma_f32_16x16x32_bf16 v[112:115], a[8:11], a[136:139], v[112:115]\n" \
"s_waitcnt vmcnt(18)\n" \
"v_mfma_f32_16x16x32_bf16 v[112:115], a[12:15], a[140:143], v[112:115]\n" \
"buffer_load_dwordx4 a[52:55], v32, s[56:59], s64 offen offset:1024 sc1\n" \
"s_waitcnt vmcnt(18)\n" \
"v_mfma_f32_16x16x32_bf16 v[116:119], a[12:15], a[156:159], v[116:119]\n" \
"ds_read_b64_tr_b16 a[12:13], v33 offset:36928\n" \
"s_waitcnt vmcnt(17)\n" \
"v_mfma_f32_16x16x32_bf16 v[116:119], a[8:11], a[152:155], v[116:119]\n" \
"ds_read_b64_tr_b16 a[8:9], v33 offset:27712\n" \
"ds_read_b64_tr_b16 a[10:11], v33 offset:32320\n" \
"ds_read_b64_tr_b16 a[14:15], v33 offset:41536\n" \
"s_waitcnt vmcnt(16)\n" \
"v_mfma_f32_16x16x32_bf16 v[116:119], a[4:7], a[148:151], v[116:119]\n" \
"buffer_load_dwordx4 a[48:51], v32, s[56:59], s64 offen offset:0 sc1\n" \
"ds_read_b64_tr_b16 a[4:5], v33 offset:18496\n" \
"ds_read_b64_tr_b16 a[6:7], v33 offset:23104\n" \
"v_readlane_b32 s64, v11, 43\n" \
"s_waitcnt vmcnt(16)\n" \
"v_mfma_f32_16x16x32_bf16 v[116:119], a[0:3], a[144:147], v[116:119]\n" \
"buffer_load_dwordx4 a[160:163], v20, s[56:59], s72 offen offset:0 sc1\n" \
"ds_read_b64_tr_b16 a[0:1], v33 offset:32\n" \
"ds_read_b64_tr_b16 a[2:3], v33 offset:4640\n" \
"s_waitcnt vmcnt(16)\n" \
"v_mfma_f32_16x16x32_bf16 v[112:115], a[16:19], a[96:99], v[112:115]\n" \
"buffer_load_dwordx4 a[164:167], v20, s[56:59], s72 offen offset:1024 sc1\n" \
"s_waitcnt vmcnt(16)\n" \
"v_mfma_f32_16x16x32_bf16 v[112:115], a[20:23], a[100:103], v[112:115]\n" \
"buffer_load_dwordx4 a[168:171], v20, s[56:59], s72 offen offset:2048 sc1\n" \
"s_waitcnt vmcnt(16)\n" \
"v_mfma_f32_16x16x32_bf16 v[112:115], a[24:27], a[104:107], v[112:115]\n" \
"buffer_load_dwordx4 a[172:175], v20, s[56:59], s72 offen offset:3072 sc1\n" \
"s_waitcnt vmcnt(16)\n" \
"v_mfma_f32_16x16x32_bf16 v[112:115], a[28:31], a[108:111], v[112:115]\n" \
"buffer_load_dwordx4 a[188:191], v32, s[56:59], s72 offen offset:3072 sc1\n" \
"s_waitcnt vmcnt(16)\n" \
"v_mfma_f32_16x16x32_bf16 v[116:119], a[28:31], a[124:127], v[116:119]\n" \
"buffer_load_dwordx4 a[184:187], v32, s[56:59], s72 offen offset:2048 sc1\n" \
"ds_read_b64_tr_b16 a[28:29], v34 offset:18496\n" \
"ds_read_b64_tr_b16 a[30:31], v34 offset:23104\n" \
"s_waitcnt vmcnt(16)\n" \
"v_mfma_f32_16x16x32_bf16 v[116:119], a[24:27], a[120:123], v[116:119]\n" \
"v_cvt_pk_bf16_f32 v112, v112, v113\n" \
"v_cvt_pk_bf16_f32 v113, v114, v115\n" \
"s_waitcnt vmcnt(15)\n" \
"v_mfma_f32_16x16x32_bf16 v[116:119], a[20:23], a[116:119], v[116:119]\n" \
"buffer_load_dwordx4 a[180:183], v32, s[56:59], s72 offen offset:1024 sc1\n" \
"ds_read_b64_tr_b16 a[20:21], v34 offset:64\n" \
"ds_read_b64_tr_b16 a[22:23], v34 offset:4672\n" \
"ds_read_b64_tr_b16 a[24:25], v34 offset:9280\n" \
"ds_read_b64_tr_b16 a[26:27], v34 offset:13888\n" \
"s_waitcnt vmcnt(15)\n" \
"v_mfma_f32_16x16x32_bf16 v[116:119], a[16:19], a[112:115], v[116:119]\n" \
"ds_read_b64_tr_b16 a[16:17], v33 offset:46144\n" \
"buffer_load_dwordx4 a[176:179], v32, s[56:59], s72 offen offset:0 sc1\n" \
"s_waitcnt lgkmcnt(5)\n" \
"v_mfma_f32_16x16x32_bf16 v[120:123], a[192:195], a[128:131], v[120:123]\n" \
"ds_read_b64_tr_b16 a[18:19], v33 offset:50752\n" \
"v_mfma_f32_16x16x32_bf16 v[120:123], a[196:199], a[132:135], v[120:123]\n" \
"v_mfma_f32_16x16x32_bf16 v[120:123], a[200:203], a[136:139], v[120:123]\n" \
"v_mfma_f32_16x16x32_bf16 v[120:123], a[204:207], a[140:143], v[120:123]\n" \
"v_cvt_pk_bf16_f32 v114, v116, v117\n" \
"v_cvt_pk_bf16_f32 v115, v118, v119\n" \
"v_mfma_f32_16x16x32_bf16 v[124:127], a[204:207], a[156:159], v[124:127]\n" \
"buffer_store_dwordx4 v[112:115], v20, s[56:59], s65 offen offset:0 sc1\n" \
"s_add_u32 s65, s65, 0x3800\n" \
"v_mfma_f32_16x16x32_bf16 v[124:127], a[200:203], a[152:155], v[124:127]\n" \
"ds_read_b64_tr_b16 a[200:201], v33 offset:27744\n" \
"ds_read_b64_tr_b16 a[202:203], v33 offset:32352\n" \
"v_mfma_f32_16x16x32_bf16 v[124:127], a[196:199], a[148:151], v[124:127]\n" \
"ds_read_b64_tr_b16 a[196:197], v33 offset:18528\n" \
"ds_read_b64_tr_b16 a[198:199], v33 offset:23136\n" \
"v_mfma_f32_16x16x32_bf16 v[124:127], a[192:195], a[144:147], v[124:127]\n" \
"ds_read_b64_tr_b16 a[192:193], v33 offset:64\n" \
"ds_read_b64_tr_b16 a[194:195], v33 offset:4672\n" \
"v_mfma_f32_16x16x32_bf16 v[120:123], a[208:211], a[96:99], v[120:123]\n" \
"buffer_load_dwordx4 a[220:223], v20, s[56:59], s93 offen offset:0 sc1\n" \
"v_mfma_f32_16x16x32_bf16 v[120:123], a[212:215], a[100:103], v[120:123]\n" \
"ds_read_b64_tr_b16 a[204:205], v33 offset:36960\n" \
"ds_read_b64_tr_b16 a[206:207], v33 offset:41568\n" \
"v_mfma_f32_16x16x32_bf16 v[120:123], a[216:219], a[104:107], v[120:123]\n" \
"v_mfma_f32_16x16x32_bf16 v[120:123], a[0:3], a[108:111], v[120:123]\n" \
"v_mfma_f32_16x16x32_bf16 v[124:127], a[0:3], a[124:127], v[124:127]\n" \
"ds_read_b64_tr_b16 a[0:1], v34 offset:18528\n" \
"ds_read_b64_tr_b16 a[2:3], v34 offset:23136\n" \
"buffer_load_dwordx4 a[224:227], v20, s[56:59], s93 offen offset:1024 sc1\n" \
"v_mfma_f32_16x16x32_bf16 v[124:127], a[216:219], a[120:123], v[124:127]\n" \
"ds_read_b64_tr_b16 a[216:217], v34 offset:9312\n" \
"ds_read_b64_tr_b16 a[218:219], v34 offset:13920\n" \
"v_mfma_f32_16x16x32_bf16 v[124:127], a[212:215], a[116:119], v[124:127]\n" \
"ds_read_b64_tr_b16 a[212:213], v34 offset:96\n" \
"v_cvt_pk_bf16_f32 v120, v120, v121\n" \
"v_cvt_pk_bf16_f32 v121, v122, v123\n" \
"v_mfma_f32_16x16x32_bf16 v[124:127], a[208:211], a[112:115], v[124:127]\n" \
"ds_read_b64_tr_b16 a[208:209], v33 offset:46176\n" \
"ds_read_b64_tr_b16 a[210:211], v33 offset:50784\n" \
"v_mfma_f32_16x16x32_bf16 v[128:131], a[4:7], a[128:131], v[128:131]\n" \
"buffer_load_dwordx4 a[228:231], v20, s[56:59], s93 offen offset:2048 sc1\n" \
"v_mfma_f32_16x16x32_bf16 v[128:131], a[8:11], a[132:135], v[128:131]\n" \
"ds_read_b64_tr_b16 a[214:215], v34 offset:4704\n" \
"v_mfma_f32_16x16x32_bf16 v[128:131], a[12:15], a[136:139], v[128:131]\n" \
"s_waitcnt lgkmcnt(15)\n" \
"v_mfma_f32_16x16x32_bf16 v[128:131], a[16:19], a[140:143], v[128:131]\n" \
"v_cvt_pk_bf16_f32 v122, v124, v125\n" \
"v_cvt_pk_bf16_f32 v123, v126, v127\n" \
"v_mfma_f32_16x16x32_bf16 v[132:135], a[16:19], a[156:159], v[132:135]\n" \
"buffer_store_dwordx4 v[120:123], v20, s[56:59], s66 offen offset:0 sc1\n" \
"s_add_u32 s66, s66, 0x3000\n" \
"v_mfma_f32_16x16x32_bf16 v[132:135], a[12:15], a[152:155], v[132:135]\n" \
"ds_read_b64_tr_b16 a[12:13], v10 offset:37120\n" \
"ds_read_b64_tr_b16 a[14:15], v10 offset:41728\n" \
"v_mfma_f32_16x16x32_bf16 v[132:135], a[8:11], a[148:151], v[132:135]\n" \
"ds_read_b64_tr_b16 a[8:9], v10 offset:27904\n" \
"ds_read_b64_tr_b16 a[10:11], v10 offset:32512\n" \
"v_mfma_f32_16x16x32_bf16 v[132:135], a[4:7], a[144:147], v[132:135]\n" \
"ds_read_b64_tr_b16 a[4:5], v33 offset:96\n" \
"ds_read_b64_tr_b16 a[6:7], v33 offset:4704\n" \
"v_mfma_f32_16x16x32_bf16 v[128:131], a[20:23], a[96:99], v[128:131]\n" \
"buffer_load_dwordx4 a[232:235], v20, s[56:59], s93 offen offset:3072 sc1\n" \
"v_mfma_f32_16x16x32_bf16 v[128:131], a[24:27], a[100:103], v[128:131]\n" \
"ds_read_b64_tr_b16 a[16:17], v10 offset:46336\n" \
"ds_read_b64_tr_b16 a[18:19], v10 offset:50944\n" \
"v_mfma_f32_16x16x32_bf16 v[128:131], a[28:31], a[104:107], v[128:131]\n" \
"s_waitcnt lgkmcnt(14)\n" \
"v_mfma_f32_16x16x32_bf16 v[128:131], a[192:195], a[108:111], v[128:131]\n" \
"v_mfma_f32_16x16x32_bf16 v[132:135], a[192:195], a[124:127], v[132:135]\n" \
"ds_read_b64_tr_b16 a[192:193], v33 offset:37120\n" \
"ds_read_b64_tr_b16 a[194:195], v33 offset:41728\n" \
"buffer_load_dwordx4 a[248:251], v32, s[56:59], s93 offen offset:3072 sc1\n" \
"v_mfma_f32_16x16x32_bf16 v[132:135], a[28:31], a[120:123], v[132:135]\n" \
"ds_read_b64_tr_b16 a[28:29], v33 offset:27904\n" \
"ds_read_b64_tr_b16 a[30:31], v33 offset:32512\n" \
"v_mfma_f32_16x16x32_bf16 v[132:135], a[24:27], a[116:119], v[132:135]\n" \
"ds_read_b64_tr_b16 a[24:25], v33 offset:18688\n" \
"v_cvt_pk_bf16_f32 v128, v128, v129\n" \
"v_cvt_pk_bf16_f32 v129, v130, v131\n" \
"v_mfma_f32_16x16x32_bf16 v[132:135], a[20:23], a[112:115], v[132:135]\n" \
"L_pf22:\n" \
"s_cbranch_execz L_retB\n" \
"s_nop 0\n" \
"ds_read_b64_tr_b16 a[20:21], v10 offset:55552\n" \
"ds_read_b64_tr_b16 a[22:23], v10 offset:60160\n" \
"v_mfma_f32_16x16x32_bf16 v[136:139], a[196:199], a[128:131], v[136:139]\n" \
"buffer_load_dwordx4 a[244:247], v32, s[56:59], s93 offen offset:2048 sc1\n" \
"v_mfma_f32_16x16x32_bf16 v[136:139], a[200:203], a[132:135], v[136:139]\n" \
"ds_read_b64_tr_b16 a[26:27], v33 offset:23296\n" \
"v_mfma_f32_16x16x32_bf16 v[136:139], a[204:207], a[136:139], v[136:139]\n" \
"s_waitcnt lgkmcnt(15)\n" \
"v_mfma_f32_16x16x32_bf16 v[136:139], a[208:211], a[140:143], v[136:139]\n" \
"v_cvt_pk_bf16_f32 v130, v132, v133\n" \
"v_cvt_pk_bf16_f32 v131, v134, v135\n" \
"v_mfma_f32_16x16x32_bf16 v[140:143], a[208:211], a[156:159], v[140:143]\n" \
"buffer_store_dwordx4 v[128:131], v20, s[56:59], s67 offen offset:0 sc1\n" \
"s_add_u32 s67, s67, 0x2800\n" \
"v_mfma_f32_16x16x32_bf16 v[140:143], a[204:207], a[152:155], v[140:143]\n" \
"ds_read_b64_tr_b16 a[204:205], v34 offset:9472\n" \
"ds_read_b64_tr_b16 a[206:207], v34 offset:14080\n" \
"v_mfma_f32_16x16x32_bf16 v[140:143], a[200:203], a[148:151], v[140:143]\n" \
"ds_read_b64_tr_b16 a[200:201], v34 offset:256\n" \
"ds_read_b64_tr_b16 a[202:203], v34 offset:4864\n" \
"v_mfma_f32_16x16x32_bf16 v[140:143], a[196:199], a[144:147], v[140:143]\n" \
"ds_read_b64_tr_b16 a[196:197], v33 offset:46336\n" \
"ds_read_b64_tr_b16 a[198:199], v33 offset:50944\n" \
"v_mfma_f32_16x16x32_bf16 v[136:139], a[212:215], a[96:99], v[136:139]\n" \
"buffer_load_dwordx4 a[240:243], v32, s[56:59], s93 offen offset:1024 sc1\n" \
"v_mfma_f32_16x16x32_bf16 v[136:139], a[216:219], a[100:103], v[136:139]\n" \
"ds_read_b64_tr_b16 a[208:209], v34 offset:18688\n" \
"ds_read_b64_tr_b16 a[210:211], v34 offset:23296\n" \
"v_mfma_f32_16x16x32_bf16 v[136:139], a[0:3], a[104:107], v[136:139]\n" \
"s_waitcnt lgkmcnt(14)\n" \
"v_mfma_f32_16x16x32_bf16 v[136:139], a[4:7], a[108:111], v[136:139]\n" \
"v_mfma_f32_16x16x32_bf16 v[140:143], a[4:7], a[124:127], v[140:143]\n" \
"ds_read_b64_tr_b16 a[4:5], v10 offset:46208\n" \
"ds_read_b64_tr_b16 a[6:7], v10 offset:50816\n" \
"buffer_load_dwordx4 a[236:239], v32, s[56:59], s93 offen offset:0 sc1\n" \
"v_mfma_f32_16x16x32_bf16 v[140:143], a[0:3], a[120:123], v[140:143]\n" \
"ds_read_b64_tr_b16 a[0:1], v10 offset:36992\n" \
"ds_read_b64_tr_b16 a[2:3], v10 offset:41600\n" \
"v_mfma_f32_16x16x32_bf16 v[140:143], a[216:219], a[116:119], v[140:143]\n" \
"ds_read_b64_tr_b16 a[216:217], v10 offset:27776\n" \
"v_cvt_pk_bf16_f32 v136, v136, v137\n" \
"v_cvt_pk_bf16_f32 v137, v138, v139\n" \
"v_mfma_f32_16x16x32_bf16 v[140:143], a[212:215], a[112:115], v[140:143]\n" \
"ds_read_b64_tr_b16 a[212:213], v33 offset:256\n" \
"s_waitcnt vmcnt(26)\n" \
"v_mfma_f32_16x16x32_bf16 v[248:251], a[8:11], a[32:35], v[248:251]\n" \
"ds_read_b64_tr_b16 a[214:215], v33 offset:4864\n" \
"s_waitcnt vmcnt(25)\n" \
"v_mfma_f32_16x16x32_bf16 v[248:251], a[12:15], a[36:39], v[248:251]\n" \
"ds_read_b64_tr_b16 a[218:219], v10 offset:32384\n" \
"s_waitcnt vmcnt(24)\n" \
"v_mfma_f32_16x16x32_bf16 v[248:251], a[16:19], a[40:43], v[248:251]\n" \
"v_cvt_pk_bf16_f32 v138, v140, v141\n" \
"v_cvt_pk_bf16_f32 v139, v142, v143\n" \
"s_waitcnt vmcnt(23) lgkmcnt(14)\n" \
"v_mfma_f32_16x16x32_bf16 v[248:251], a[20:23], a[44:47], v[248:251]\n" \
"buffer_store_dwordx4 v[136:139], v20, s[56:59], s68 offen offset:0 sc1\n" \
"s_add_u32 s68, s68, 0x2000\n" \
"s_waitcnt vmcnt(23)\n" \
"v_mfma_f32_16x16x32_bf16 v[252:255], a[20:23], a[60:63], v[252:255]\n" \
"ds_read_b64_tr_b16 a[20:21], v33 offset:36992\n" \
"s_waitcnt vmcnt(22)\n" \
"v_mfma_f32_16x16x32_bf16 v[252:255], a[16:19], a[56:59], v[252:255]\n" \
"ds_read_b64_tr_b16 a[16:17], v33 offset:27776\n" \
"s_waitcnt vmcnt(21)\n" \
"v_mfma_f32_16x16x32_bf16 v[252:255], a[12:15], a[52:55], v[252:255]\n" \
"ds_read_b64_tr_b16 a[12:13], v33 offset:18560\n" \
"s_waitcnt vmcnt(20)\n" \
"v_mfma_f32_16x16x32_bf16 v[252:255], a[8:11], a[48:51], v[252:255]\n" \
"ds_read_b64_tr_b16 a[8:9], v10 offset:55424\n" \
"ds_read_b64_tr_b16 a[10:11], v10 offset:60032\n" \
"v_mfma_f32_16x16x32_bf16 v[176:179], a[24:27], a[128:131], v[176:179]\n" \
"ds_read_b64_tr_b16 a[14:15], v33 offset:23168\n" \
"ds_read_b64_tr_b16 a[18:19], v33 offset:32384\n" \
"v_mfma_f32_16x16x32_bf16 v[176:179], a[28:31], a[132:135], v[176:179]\n" \
"buffer_load_dwordx4 v[64:67], v20, s[56:59], s95 offen offset:0 sc1\n" \
"v_mfma_f32_16x16x32_bf16 v[176:179], a[192:195], a[136:139], v[176:179]\n" \
"ds_read_b64_tr_b16 a[22:23], v33 offset:41600\n" \
"s_waitcnt lgkmcnt(12)\n" \
"v_mfma_f32_16x16x32_bf16 v[176:179], a[196:199], a[140:143], v[176:179]\n" \
"v_mfma_f32_16x16x32_bf16 v[180:183], a[196:199], a[156:159], v[180:183]\n" \
"v_mfma_f32_16x16x32_bf16 v[180:183], a[192:195], a[152:155], v[180:183]\n" \
"v_mfma_f32_16x16x32_bf16 v[180:183], a[28:31], a[148:151], v[180:183]\n" \
"buffer_load_dwordx4 v[68:71], v20, s[56:59], s95 offen offset:1024 sc1\n" \
"v_mfma_f32_16x16x32_bf16 v[180:183], a[24:27], a[144:147], v[180:183]\n" \
"s_waitcnt vmcnt(21)\n" \
"v_mfma_f32_16x16x32_bf16 v[248:251], a[24:27], a[160:163], v[248:251]\n" \
"s_waitcnt vmcnt(13)\n" \
"s_mov_b64 exec, 1\n" \
"buffer_store_dword v39, off, s[56:59], s65 offset:0 sc1\n" \
"s_mov_b64 exec, -1\n" \
"v_mfma_f32_16x16x32_bf16 v[248:251], a[28:31], a[164:167], v[248:251]\n" \
"v_mfma_f32_16x16x32_bf16 v[248:251], a[192:195], a[168:171], v[248:251]\n" \
"v_mfma_f32_16x16x32_bf16 v[248:251], a[196:199], a[172:175], v[248:251]\n" \
"v_mfma_f32_16x16x32_bf16 v[252:255], a[196:199], a[188:191], v[252:255]\n" \
"ds_read_b64_tr_b16 a[196:197], v34 offset:18560\n" \
"ds_read_b64_tr_b16 a[198:199], v34 offset:23168\n" \
"v_mfma_f32_16x16x32_bf16 v[252:255], a[192:195], a[184:187], v[252:255]\n" \
"ds_read_b64_tr_b16 a[192:193], v34 offset:9344\n" \
"ds_read_b64_tr_b16 a[194:195], v34 offset:13952\n" \
"v_mfma_f32_16x16x32_bf16 v[252:255], a[28:31], a[180:183], v[252:255]\n" \
"ds_read_b64_tr_b16 a[28:29], v34 offset:128\n" \
"ds_read_b64_tr_b16 a[30:31], v34 offset:4736\n" \
"v_mfma_f32_16x16x32_bf16 v[252:255], a[24:27], a[176:179], v[252:255]\n" \
"ds_read_b64_tr_b16 a[24:25], v33 offset:46208\n" \
"ds_read_b64_tr_b16 a[26:27], v33 offset:50816\n" \
"v_mfma_f32_16x16x32_bf16 v[176:179], a[200:203], a[96:99], v[176:179]\n" \
"buffer_load_dwordx4 v[72:75], v32, s[56:59], s95 offen offset:0 sc1\n" \
"v_mfma_f32_16x16x32_bf16 v[176:179], a[204:207], a[100:103], v[176:179]\n" \
"v_mfma_f32_16x16x32_bf16 v[176:179], a[208:211], a[104:107], v[176:179]\n" \
"s_waitcnt lgkmcnt(8)\n" \
"v_mfma_f32_16x16x32_bf16 v[176:179], a[212:215], a[108:111], v[176:179]\n" \
"v_mfma_f32_16x16x32_bf16 v[180:183], a[212:215], a[124:127], v[180:183]\n" \
"v_mfma_f32_16x16x32_bf16 v[180:183], a[208:211], a[120:123], v[180:183]\n" \
"buffer_load_dwordx4 v[76:79], v32, s[56:59], s95 offen offset:1024 sc1\n" \
"v_mfma_f32_16x16x32_bf16 v[180:183], a[204:207], a[116:119], v[180:183]\n" \
"v_readlane_b32 s95, v11, 48\n" \
"v_mfma_f32_16x16x32_bf16 v[180:183], a[200:203], a[112:115], v[180:183]\n" \
"s_waitcnt vmcnt(15)\n" \
"v_mfma_f32_16x16x32_bf16 v[248:251], a[200:203], a[220:223], v[248:251]\n" \
"s_waitcnt vmcnt(12)\n" \
"s_mov_b64 exec, 1\n" \
"buffer_store_dword v39, off, s[56:59], s66 offset:4 sc1\n" \
"s_mov_b64 exec, -1\n" \
"v_cvt_pk_bf16_f32 v176, v176, v177\n" \
"v_cvt_pk_bf16_f32 v177, v178, v179\n" \
"v_cvt_pk_bf16_f32 v178, v180, v181\n" \
"v_cvt_pk_bf16_f32 v179, v182, v183\n" \
"v_mfma_f32_16x16x32_bf16 v[248:251], a[204:207], a[224:227], v[248:251]\n" \
"v_readlane_b32 s66, v11, 44\n" \
"v_mfma_f32_16x16x32_bf16 v[248:251], a[208:211], a[228:231], v[248:251]\n" \
"buffer_store_dwordx4 v[176:179], v20, s[56:59], s69 offen offset:0 sc1\n" \
"s_waitcnt vmcnt(10)\n" \
"s_mov_b64 exec, 1\n" \
"buffer_store_dword v39, off, s[56:59], s67 offset:8 sc1\n" \
"s_mov_b64 exec, -1\n" \
"v_mfma_f32_16x16x32_bf16 v[248:251], a[212:215], a[232:235], v[248:251]\n" \
"v_readlane_b32 s67, v11, 45\n" \
"v_mfma_f32_16x16x32_bf16 v[252:255], a[212:215], a[248:251], v[252:255]\n" \
"ds_read_b64_tr_b16 a[212:213], v10 offset:46240\n" \
"ds_read_b64_tr_b16 a[214:215], v10 offset:50848\n" \
"v_mfma_f32_16x16x32_bf16 v[252:255], a[208:211], a[244:247], v[252:255]\n" \
"ds_read_b64_tr_b16 a[208:209], v10 offset:37024\n" \
"ds_read_b64_tr_b16 a[210:211], v10 offset:41632\n" \
"s_waitcnt vmcnt(10)\n" \
"v_mfma_f32_16x16x32_bf16 v[252:255], a[204:207], a[240:243], v[252:255]\n" \
"v_cvt_pk_bf16_f32 v248, v248, v249\n" \
"v_cvt_pk_bf16_f32 v249, v250, v251\n" \
"s_waitcnt vmcnt(9)\n" \
"v_mfma_f32_16x16x32_bf16 v[252:255], a[200:203], a[236:239], v[252:255]\n" \
"ds_read_b64_tr_b16 a[200:201], v33 offset:128\n" \
"ds_read_b64_tr_b16 a[202:203], v33 offset:4736\n" \
"s_waitcnt vmcnt(8)\n" \
"s_mov_b64 exec, 1\n" \
"buffer_store_dword v39, off, s[56:59], s68 offset:12 sc1\n" \
"s_mov_b64 exec, -1\n" \
"v_mfma_f32_16x16x32_bf16 v[216:219], a[216:219], a[32:35], v[216:219]\n" \
"ds_read_b64_tr_b16 a[204:205], v10 offset:27808\n" \
"v_cvt_pk_bf16_f32 v250, v252, v253\n" \
"v_cvt_pk_bf16_f32 v251, v254, v255\n" \
"v_mfma_f32_16x16x32_bf16 v[216:219], a[0:3], a[36:39], v[216:219]\n" \
"buffer_store_dwordx4 v[248:251], v20, s[56:59], s69 offen offset:1024 sc1\n" \
"v_mfma_f32_16x16x32_bf16 v[216:219], a[4:7], a[40:43], v[216:219]\n" \
"ds_read_b64_tr_b16 a[206:207], v10 offset:32416\n" \
"v_mfma_f32_16x16x32_bf16 v[216:219], a[8:11], a[44:47], v[216:219]\n" \
"v_mfma_f32_16x16x32_bf16 v[220:223], a[8:11], a[60:63], v[220:223]\n" \
"ds_read_b64_tr_b16 a[8:9], v33 offset:37024\n" \
"ds_read_b64_tr_b16 a[10:11], v33 offset:41632\n" \
"v_mfma_f32_16x16x32_bf16 v[220:223], a[4:7], a[56:59], v[220:223]\n" \
"ds_read_b64_tr_b16 a[4:5], v33 offset:27808\n" \
"ds_read_b64_tr_b16 a[6:7], v33 offset:32416\n" \
"v_mfma_f32_16x16x32_bf16 v[220:223], a[0:3], a[52:55], v[220:223]\n" \
"ds_read_b64_tr_b16 a[0:1], v33 offset:18592\n" \
"ds_read_b64_tr_b16 a[2:3], v33 offset:23200\n" \
"v_mfma_f32_16x16x32_bf16 v[220:223], a[216:219], a[48:51], v[220:223]\n" \
"ds_read_b64_tr_b16 a[216:217], v10 offset:55456\n" \
"ds_read_b64_tr_b16 a[218:219], v10 offset:60064\n" \
"v_mfma_f32_16x16x32_bf16 v[144:147], a[12:15], a[128:131], v[144:147]\n" \
"buffer_load_dwordx4 v[96:99], v20, s[56:59], s97 offen offset:0 sc1\n" \
"v_mfma_f32_16x16x32_bf16 v[144:147], a[16:19], a[132:135], v[144:147]\n" \
"v_mfma_f32_16x16x32_bf16 v[144:147], a[20:23], a[136:139], v[144:147]\n" \
"s_waitcnt lgkmcnt(12)\n" \
"v_mfma_f32_16x16x32_bf16 v[144:147], a[24:27], a[140:143], v[144:147]\n" \
"buffer_load_dword v37, v40, s[56:59], s74 offen sc1\n" \
"v_mfma_f32_16x16x32_bf16 v[148:151], a[24:27], a[156:159], v[148:151]\n" \
"v_mfma_f32_16x16x32_bf16 v[148:151], a[20:23], a[152:155], v[148:151]\n" \
"v_mfma_f32_16x16x32_bf16 v[148:151], a[16:19], a[148:151], v[148:151]\n" \
"v_mfma_f32_16x16x32_bf16 v[148:151], a[12:15], a[144:147], v[148:151]\n" \
"v_mfma_f32_16x16x32_bf16 v[216:219], a[12:15], a[160:163], v[216:219]\n" \
"buffer_load_dwordx4 v[100:103], v20, s[56:59], s97 offen offset:1024 sc1\n" \
"v_mfma_f32_16x16x32_bf16 v[216:219], a[16:19], a[164:167], v[216:219]\n" \
"v_mfma_f32_16x16x32_bf16 v[216:219], a[20:23], a[168:171], v[216:219]\n" \
"v_mfma_f32_16x16x32_bf16 v[216:219], a[24:27], a[172:175], v[216:219]\n" \
"v_mfma_f32_16x16x32_bf16 v[220:223], a[24:27], a[188:191], v[220:223]\n" \
"ds_read_b64_tr_b16 a[24:25], v34 offset:18592\n" \
"ds_read_b64_tr_b16 a[26:27], v34 offset:23200\n" \
"v_mfma_f32_16x16x32_bf16 v[220:223], a[20:23], a[184:187], v[220:223]\n" \
"ds_read_b64_tr_b16 a[20:21], v34 offset:9376\n" \
"ds_read_b64_tr_b16 a[22:23], v34 offset:13984\n" \
"v_mfma_f32_16x16x32_bf16 v[220:223], a[16:19], a[180:183], v[220:223]\n" \
"ds_read_b64_tr_b16 a[16:17], v34 offset:160\n" \
"ds_read_b64_tr_b16 a[18:19], v34 offset:4768\n" \
"v_mfma_f32_16x16x32_bf16 v[220:223], a[12:15], a[176:179], v[220:223]\n" \
"ds_read_b64_tr_b16 a[12:13], v33 offset:46240\n" \
"ds_read_b64_tr_b16 a[14:15], v33 offset:50848\n" \
"v_mfma_f32_16x16x32_bf16 v[144:147], a[28:31], a[96:99], v[144:147]\n" \
"buffer_load_dwordx4 v[104:107], v32, s[56:59], s97 offen offset:0 sc1\n" \
"v_mfma_f32_16x16x32_bf16 v[144:147], a[192:195], a[100:103], v[144:147]\n" \
"v_mfma_f32_16x16x32_bf16 v[144:147], a[196:199], a[104:107], v[144:147]\n" \
"s_waitcnt lgkmcnt(8)\n" \
"v_mfma_f32_16x16x32_bf16 v[144:147], a[200:203], a[108:111], v[144:147]\n" \
"v_mfma_f32_16x16x32_bf16 v[148:151], a[200:203], a[124:127], v[148:151]\n" \
"v_mfma_f32_16x16x32_bf16 v[148:151], a[196:199], a[120:123], v[148:151]\n" \
"buffer_load_dwordx4 v[108:111], v32, s[56:59], s97 offen offset:1024 sc1\n" \
"v_mfma_f32_16x16x32_bf16 v[148:151], a[192:195], a[116:119], v[148:151]\n" \
"v_readlane_b32 s97, v11, 42\n" \
"v_mfma_f32_16x16x32_bf16 v[148:151], a[28:31], a[112:115], v[148:151]\n" \
"v_mfma_f32_16x16x32_bf16 v[216:219], a[28:31], a[220:223], v[216:219]\n" \
"v_mfma_f32_16x16x32_bf16 v[216:219], a[192:195], a[224:227], v[216:219]\n" \
"v_cvt_pk_bf16_f32 v144, v144, v145\n" \
"v_cvt_pk_bf16_f32 v145, v146, v147\n" \
"v_mfma_f32_16x16x32_bf16 v[216:219], a[196:199], a[228:231], v[216:219]\n" \
"buffer_load_dwordx4 v[80:83], v20, s[56:59], s96 offen offset:0 sc1\n" \
"v_mfma_f32_16x16x32_bf16 v[216:219], a[200:203], a[232:235], v[216:219]\n" \
"v_mfma_f32_16x16x32_bf16 v[220:223], a[200:203], a[248:251], v[220:223]\n" \
"v_cvt_pk_bf16_f32 v146, v148, v149\n" \
"v_cvt_pk_bf16_f32 v147, v150, v151\n" \
"v_mfma_f32_16x16x32_bf16 v[220:223], a[196:199], a[244:247], v[220:223]\n" \
"buffer_store_dwordx4 v[144:147], v20, s[56:59], s71 offen offset:0 sc1\n" \
"v_mfma_f32_16x16x32_bf16 v[220:223], a[192:195], a[240:243], v[220:223]\n" \
"ds_read_b64_tr_b16 a[192:193], v10 offset:27840\n" \
"ds_read_b64_tr_b16 a[194:195], v10 offset:32448\n" \
"v_cvt_pk_bf16_f32 v216, v216, v217\n" \
"v_cvt_pk_bf16_f32 v217, v218, v219\n" \
"v_mfma_f32_16x16x32_bf16 v[220:223], a[28:31], a[236:239], v[220:223]\n" \
"ds_read_b64_tr_b16 a[28:29], v33 offset:160\n" \
"ds_read_b64_tr_b16 a[30:31], v33 offset:4768\n" \
"v_mfma_f32_16x16x32_bf16 v[224:227], a[204:207], a[32:35], v[224:227]\n" \
"ds_read_b64_tr_b16 a[196:197], v10 offset:37056\n" \
"ds_read_b64_tr_b16 a[198:199], v10 offset:41664\n" \
"v_mfma_f32_16x16x32_bf16 v[224:227], a[208:211], a[36:39], v[224:227]\n" \
"ds_read_b64_tr_b16 a[200:201], v10 offset:46272\n" \
"ds_read_b64_tr_b16 a[202:203], v10 offset:50880\n" \
"v_cvt_pk_bf16_f32 v218, v220, v221\n" \
"v_cvt_pk_bf16_f32 v219, v222, v223\n" \
"v_mfma_f32_16x16x32_bf16 v[224:227], a[212:215], a[40:43], v[224:227]\n" \
"buffer_store_dwordx4 v[216:219], v20, s[56:59], s71 offen offset:1024 sc1\n" \
"s_add_u32 s71, s71, 0x1800\n" \
"v_mfma_f32_16x16x32_bf16 v[224:227], a[216:219], a[44:47], v[224:227]\n" \
"v_mfma_f32_16x16x32_bf16 v[228:231], a[216:219], a[60:63], v[228:231]\n" \
"ds_read_b64_tr_b16 a[216:217], v33 offset:37056\n" \
"ds_read_b64_tr_b16 a[218:219], v33 offset:41664\n" \
"v_mfma_f32_16x16x32_bf16 v[228:231], a[212:215], a[56:59], v[228:231]\n" \
"ds_read_b64_tr_b16 a[212:213], v33 offset:27840\n" \
"ds_read_b64_tr_b16 a[214:215], v33 offset:32448\n" \
"v_mfma_f32_16x16x32_bf16 v[228:231], a[208:211], a[52:55], v[228:231]\n" \
"ds_read_b64_tr_b16 a[208:209], v33 offset:18624\n" \
"ds_read_b64_tr_b16 a[210:211], v33 offset:23232\n" \
"v_mfma_f32_16x16x32_bf16 v[228:231], a[204:207], a[48:51], v[228:231]\n" \
"ds_read_b64_tr_b16 a[204:205], v10 offset:55488\n" \
"ds_read_b64_tr_b16 a[206:207], v10 offset:60096\n" \
"v_mfma_f32_16x16x32_bf16 v[152:155], a[0:3], a[128:131], v[152:155]\n" \
"buffer_load_dwordx4 v[88:91], v32, s[56:59], s96 offen offset:0 sc1\n" \
"v_mfma_f32_16x16x32_bf16 v[152:155], a[4:7], a[132:135], v[152:155]\n" \
"v_mfma_f32_16x16x32_bf16 v[152:155], a[8:11], a[136:139], v[152:155]\n" \
"s_waitcnt lgkmcnt(15)\n" \
"v_mfma_f32_16x16x32_bf16 v[152:155], a[12:15], a[140:143], v[152:155]\n" \
"v_mfma_f32_16x16x32_bf16 v[156:159], a[12:15], a[156:159], v[156:159]\n" \
"v_mfma_f32_16x16x32_bf16 v[156:159], a[8:11], a[152:155], v[156:159]\n" \
"buffer_load_dwordx4 v[84:87], v20, s[56:59], s96 offen offset:1024 sc1\n" \
"v_mfma_f32_16x16x32_bf16 v[156:159], a[4:7], a[148:151], v[156:159]\n" \
"v_mfma_f32_16x16x32_bf16 v[156:159], a[0:3], a[144:147], v[156:159]\n" \
"v_mfma_f32_16x16x32_bf16 v[224:227], a[0:3], a[160:163], v[224:227]\n" \
"v_mfma_f32_16x16x32_bf16 v[224:227], a[4:7], a[164:167], v[224:227]\n" \
"v_mfma_f32_16x16x32_bf16 v[224:227], a[8:11], a[168:171], v[224:227]\n" \
"buffer_load_dwordx4 v[92:95], v32, s[56:59], s96 offen offset:1024 sc1\n" \
"v_mfma_f32_16x16x32_bf16 v[224:227], a[12:15], a[172:175], v[224:227]\n" \
"v_mfma_f32_16x16x32_bf16 v[228:231], a[12:15], a[188:191], v[228:231]\n" \
"ds_read_b64_tr_b16 a[12:13], v34 offset:18624\n" \
"ds_read_b64_tr_b16 a[14:15], v34 offset:23232\n" \
"v_mfma_f32_16x16x32_bf16 v[228:231], a[8:11], a[184:187], v[228:231]\n" \
"ds_read_b64_tr_b16 a[8:9], v34 offset:9408\n" \
"ds_read_b64_tr_b16 a[10:11], v34 offset:14016\n" \
"v_mfma_f32_16x16x32_bf16 v[228:231], a[4:7], a[180:183], v[228:231]\n" \
"ds_read_b64_tr_b16 a[4:5], v34 offset:192\n" \
"ds_read_b64_tr_b16 a[6:7], v34 offset:4800\n" \
"v_mfma_f32_16x16x32_bf16 v[228:231], a[0:3], a[176:179], v[228:231]\n" \
"ds_read_b64_tr_b16 a[0:1], v33 offset:46272\n" \
"ds_read_b64_tr_b16 a[2:3], v33 offset:50880\n" \
"s_waitcnt vmcnt(11)\n" \
"s_mov_b64 exec, 3\n" \
"buffer_store_dword v39, v41, s[56:59], s70 offen sc1\n" \
"s_mov_b64 exec, -1\n" \
"v_mfma_f32_16x16x32_bf16 v[152:155], a[16:19], a[96:99], v[152:155]\n" \
"v_mfma_f32_16x16x32_bf16 v[152:155], a[20:23], a[100:103], v[152:155]\n" \
"v_mfma_f32_16x16x32_bf16 v[152:155], a[24:27], a[104:107], v[152:155]\n" \
"s_waitcnt vmcnt(10)\n" \
"v_cmp_eq_u32_e64 s[98:99], s62, v37\n" \
"s_waitcnt lgkmcnt(8)\n" \
"v_mfma_f32_16x16x32_bf16 v[152:155], a[28:31], a[108:111], v[152:155]\n" \
"s_and_b32 s99, s98, 0xf\n" \
"s_or_b32 s84, s84, s99\n" \
"s_bitcmp1_b32 s84, 0\n" \
"s_cselect_b32 s72, s63, 0x40000000\n" \
"s_bitcmp1_b32 s84, 1\n" \
"s_cselect_b32 s73, s63, 0x40000000\n" \
"s_bitcmp1_b32 s84, 2\n" \
"s_cselect_b32 s92, s63, 0x40000000\n" \
"s_bitcmp1_b32 s84, 3\n" \
"s_cselect_b32 s93, s63, 0x40000000\n" \
"v_cvt_pk_bf16_f32 v152, v152, v153\n" \
"v_cvt_pk_bf16_f32 v153, v154, v155\n" \
"v_mfma_f32_16x16x32_bf16 v[156:159], a[28:31], a[124:127], v[156:159]\n" \
"buffer_load_dwordx4 v[116:119], v20, s[56:59], s72 offen offset:0 sc1\n" \
"v_mfma_f32_16x16x32_bf16 v[156:159], a[24:27], a[120:123], v[156:159]\n" \
"v_mfma_f32_16x16x32_bf16 v[156:159], a[20:23], a[116:119], v[156:159]\n" \
"v_mfma_f32_16x16x32_bf16 v[156:159], a[16:19], a[112:115], v[156:159]\n" \
"v_mfma_f32_16x16x32_bf16 v[224:227], a[16:19], a[220:223], v[224:227]\n" \
"v_mfma_f32_16x16x32_bf16 v[224:227], a[20:23], a[224:227], v[224:227]\n" \
"buffer_load_dwordx4 v[124:127], v20, s[56:59], s73 offen offset:2048 sc1\n" \
"v_mfma_f32_16x16x32_bf16 v[224:227], a[24:27], a[228:231], v[224:227]\n" \
"v_mfma_f32_16x16x32_bf16 v[224:227], a[28:31], a[232:235], v[224:227]\n" \
"v_mfma_f32_16x16x32_bf16 v[228:231], a[28:31], a[248:251], v[228:231]\n" \
"ds_read_b64_tr_b16 a[28:29], v10 offset:46080\n" \
"ds_read_b64_tr_b16 a[30:31], v10 offset:50688\n" \
"v_cvt_pk_bf16_f32 v154, v156, v157\n" \
"v_cvt_pk_bf16_f32 v155, v158, v159\n" \
"v_mfma_f32_16x16x32_bf16 v[228:231], a[24:27], a[244:247], v[228:231]\n" \
"buffer_store_dwordx4 v[152:155], v20, s[56:59], s94 offen offset:0 sc1\n" \
"v_mfma_f32_16x16x32_bf16 v[228:231], a[20:23], a[240:243], v[228:231]\n" \
"ds_read_b64_tr_b16 a[20:21], v10 offset:27648\n" \
"v_cvt_pk_bf16_f32 v224, v224, v225\n" \
"v_cvt_pk_bf16_f32 v225, v226, v227\n" \
"v_mfma_f32_16x16x32_bf16 v[228:231], a[16:19], a[236:239], v[228:231]\n" \
"ds_read_b64_tr_b16 a[16:17], v33 offset:192\n" \
"ds_read_b64_tr_b16 a[18:19], v33 offset:4800\n" \
"v_mfma_f32_16x16x32_bf16 v[232:235], a[192:195], a[32:35], v[232:235]\n" \
"ds_read_b64_tr_b16 a[22:23], v10 offset:32256\n" \
"ds_read_b64_tr_b16 a[24:25], v10 offset:36864\n" \
"v_mfma_f32_16x16x32_bf16 v[232:235], a[196:199], a[36:39], v[232:235]\n" \
"ds_read_b64_tr_b16 a[26:27], v10 offset:41472\n" \
"buffer_load_dwordx4 v[132:135], v38, s[56:59], s92 offen offset:0 sc1\n" \
"v_cvt_pk_bf16_f32 v226, v228, v229\n" \
"v_cvt_pk_bf16_f32 v227, v230, v231\n" \
"v_mfma_f32_16x16x32_bf16 v[232:235], a[200:203], a[40:43], v[232:235]\n" \
"v_readlane_b32 s92, v11, 50\n" \
"v_mfma_f32_16x16x32_bf16 v[232:235], a[204:207], a[44:47], v[232:235]\n" \
"buffer_store_dwordx4 v[224:227], v20, s[56:59], s94 offen offset:1024 sc1\n" \
"s_add_u32 s94, s94, 0x1000\n" \
"v_mfma_f32_16x16x32_bf16 v[236:239], a[204:207], a[60:63], v[236:239]\n" \
"ds_read_b64_tr_b16 a[204:205], v33 offset:36864\n" \
"ds_read_b64_tr_b16 a[206:207], v33 offset:41472\n" \
"v_mfma_f32_16x16x32_bf16 v[236:239], a[200:203], a[56:59], v[236:239]\n" \
"ds_read_b64_tr_b16 a[200:201], v33 offset:27648\n" \
"ds_read_b64_tr_b16 a[202:203], v33 offset:32256\n" \
"v_mfma_f32_16x16x32_bf16 v[236:239], a[196:199], a[52:55], v[236:239]\n" \
"ds_read_b64_tr_b16 a[196:197], v33 offset:18432\n" \
"ds_read_b64_tr_b16 a[198:199], v33 offset:23040\n" \
"v_mfma_f32_16x16x32_bf16 v[236:239], a[192:195], a[48:51], v[236:239]\n" \
"ds_read_b64_tr_b16 a[192:193], v10 offset:55296\n" \
"ds_read_b64_tr_b16 a[194:195], v10 offset:59904\n" \
"buffer_load_dwordx4 v[140:143], v38, s[56:59], s93 offen offset:2048 sc1\n" \
"v_mfma_f32_16x16x32_bf16 v[160:163], a[208:211], a[128:131], v[160:163]\n" \
"v_readlane_b32 s93, v11, 41\n" \
"v_mfma_f32_16x16x32_bf16 v[160:163], a[212:215], a[132:135], v[160:163]\n" \
"v_mfma_f32_16x16x32_bf16 v[160:163], a[216:219], a[136:139], v[160:163]\n" \
"s_waitcnt lgkmcnt(14)\n" \
"v_mfma_f32_16x16x32_bf16 v[160:163], a[0:3], a[140:143], v[160:163]\n" \
"buffer_load_dword v37, v40, s[56:59], s74 offen sc1\n" \
"v_mfma_f32_16x16x32_bf16 v[164:167], a[0:3], a[156:159], v[164:167]\n" \
"v_mfma_f32_16x16x32_bf16 v[164:167], a[216:219], a[152:155], v[164:167]\n" \
"v_mfma_f32_16x16x32_bf16 v[164:167], a[212:215], a[148:151], v[164:167]\n" \
"v_mfma_f32_16x16x32_bf16 v[164:167], a[208:211], a[144:147], v[164:167]\n" \
"v_mfma_f32_16x16x32_bf16 v[232:235], a[208:211], a[160:163], v[232:235]\n" \
"v_mfma_f32_16x16x32_bf16 v[232:235], a[212:215], a[164:167], v[232:235]\n" \
"v_mfma_f32_16x16x32_bf16 v[232:235], a[216:219], a[168:171], v[232:235]\n" \
"v_mfma_f32_16x16x32_bf16 v[232:235], a[0:3], a[172:175], v[232:235]\n" \
"s_waitcnt vmcnt(11)\n" \
"s_mov_b64 exec, 1\n" \
"buffer_store_dword v39, off, s[56:59], s71 offset:20 sc1\n" \
"s_mov_b64 exec, -1\n" \
"v_mfma_f32_16x16x32_bf16 v[236:239], a[0:3], a[188:191], v[236:239]\n" \
"ds_read_b64_tr_b16 a[0:1], v34 offset:18432\n" \
"ds_read_b64_tr_b16 a[2:3], v34 offset:23040\n" \
"v_mfma_f32_16x16x32_bf16 v[236:239], a[216:219], a[184:187], v[236:239]\n" \
"ds_read_b64_tr_b16 a[216:217], v34 offset:9216\n" \
"ds_read_b64_tr_b16 a[218:219], v34 offset:13824\n" \
"v_mfma_f32_16x16x32_bf16 v[236:239], a[212:215], a[180:183], v[236:239]\n" \
"ds_read_b64_tr_b16 a[212:213], v34 offset:0\n" \
"ds_read_b64_tr_b16 a[214:215], v34 offset:4608\n" \
"v_mfma_f32_16x16x32_bf16 v[236:239], a[208:211], a[176:179], v[236:239]\n" \
"ds_read_b64_tr_b16 a[208:209], v33 offset:46080\n" \
"ds_read_b64_tr_b16 a[210:211], v33 offset:50688\n" \
"v_mfma_f32_16x16x32_bf16 v[160:163], a[4:7], a[96:99], v[160:163]\n" \
"v_mfma_f32_16x16x32_bf16 v[160:163], a[8:11], a[100:103], v[160:163]\n" \
"v_mfma_f32_16x16x32_bf16 v[160:163], a[12:15], a[104:107], v[160:163]\n" \
"s_waitcnt lgkmcnt(8)\n" \
"v_mfma_f32_16x16x32_bf16 v[160:163], a[16:19], a[108:111], v[160:163]\n" \
"v_mfma_f32_16x16x32_bf16 v[164:167], a[16:19], a[124:127], v[164:167]\n" \
"v_mfma_f32_16x16x32_bf16 v[164:167], a[12:15], a[120:123], v[164:167]\n" \
"v_mfma_f32_16x16x32_bf16 v[164:167], a[8:11], a[116:119], v[164:167]\n" \
"v_mfma_f32_16x16x32_bf16 v[164:167], a[4:7], a[112:115], v[164:167]\n" \
"v_mfma_f32_16x16x32_bf16 v[232:235], a[4:7], a[220:223], v[232:235]\n" \
"v_mfma_f32_16x16x32_bf16 v[232:235], a[8:11], a[224:227], v[232:235]\n" \
"v_mfma_f32_16x16x32_bf16 v[232:235], a[12:15], a[228:231], v[232:235]\n" \
"v_mfma_f32_16x16x32_bf16 v[232:235], a[16:19], a[232:235], v[232:235]\n" \
"v_cvt_pk_bf16_f32 v160, v160, v161\n" \
"v_cvt_pk_bf16_f32 v161, v162, v163\n" \
"v_mfma_f32_16x16x32_bf16 v[236:239], a[16:19], a[248:251], v[236:239]\n" \
"ds_read_b64_tr_b16 a[16:17], v10 offset:46112\n" \
"v_cvt_pk_bf16_f32 v162, v164, v165\n" \
"v_cvt_pk_bf16_f32 v163, v166, v167\n" \
"v_mfma_f32_16x16x32_bf16 v[236:239], a[12:15], a[244:247], v[236:239]\n" \
"buffer_store_dwordx4 v[160:163], v20, s[56:59], s95 offen offset:0 sc1\n" \
"v_cvt_pk_bf16_f32 v232, v232, v233\n" \
"v_cvt_pk_bf16_f32 v233, v234, v235\n" \
"v_mfma_f32_16x16x32_bf16 v[236:239], a[8:11], a[240:243], v[236:239]\n" \
"ds_read_b64_tr_b16 a[8:9], v10 offset:27680\n" \
"ds_read_b64_tr_b16 a[10:11], v10 offset:32288\n" \
"v_mfma_f32_16x16x32_bf16 v[236:239], a[4:7], a[236:239], v[236:239]\n" \
"ds_read_b64_tr_b16 a[4:5], v33 offset:0\n" \
"ds_read_b64_tr_b16 a[6:7], v33 offset:4608\n" \
"v_mfma_f32_16x16x32_bf16 v[184:187], a[20:23], a[32:35], v[184:187]\n" \
"ds_read_b64_tr_b16 a[12:13], v10 offset:36896\n" \
"ds_read_b64_tr_b16 a[14:15], v10 offset:41504\n" \
"v_mfma_f32_16x16x32_bf16 v[184:187], a[24:27], a[36:39], v[184:187]\n" \
"ds_read_b64_tr_b16 a[18:19], v10 offset:50720\n" \
"v_mfma_f32_16x16x32_bf16 v[184:187], a[28:31], a[40:43], v[184:187]\n" \
"v_cvt_pk_bf16_f32 v234, v236, v237\n" \
"v_cvt_pk_bf16_f32 v235, v238, v239\n" \
"v_mfma_f32_16x16x32_bf16 v[184:187], a[192:195], a[44:47], v[184:187]\n" \
"buffer_store_dwordx4 v[232:235], v20, s[56:59], s95 offen offset:1024 sc1\n" \
"s_add_u32 s95, s95, 0x800\n" \
"v_mfma_f32_16x16x32_bf16 v[188:191], a[192:195], a[60:63], v[188:191]\n" \
"ds_read_b64_tr_b16 a[192:193], v33 offset:36896\n" \
"ds_read_b64_tr_b16 a[194:195], v33 offset:41504\n" \
"v_mfma_f32_16x16x32_bf16 v[188:191], a[28:31], a[56:59], v[188:191]\n" \
"ds_read_b64_tr_b16 a[28:29], v33 offset:27680\n" \
"ds_read_b64_tr_b16 a[30:31], v33 offset:32288\n" \
"v_mfma_f32_16x16x32_bf16 v[188:191], a[24:27], a[52:55], v[188:191]\n" \
"ds_read_b64_tr_b16 a[24:25], v33 offset:18464\n" \
"ds_read_b64_tr_b16 a[26:27], v33 offset:23072\n" \
"s_mov_b64 exec, 1\n" \
"buffer_store_dword v46, off, s[56:59], s92 offset:0 sc1\n" \
"s_mov_b64 exec, -1\n" \
"v_mfma_f32_16x16x32_bf16 v[188:191], a[20:23], a[48:51], v[188:191]\n" \
"ds_read_b64_tr_b16 a[20:21], v10 offset:55328\n" \
"ds_read_b64_tr_b16 a[22:23], v10 offset:59936\n" \
"v_mfma_f32_16x16x32_bf16 v[184:187], a[196:199], a[160:163], v[184:187]\n" \
"v_mfma_f32_16x16x32_bf16 v[184:187], a[200:203], a[164:167], v[184:187]\n" \
"v_mfma_f32_16x16x32_bf16 v[184:187], a[204:207], a[168:171], v[184:187]\n" \
"s_waitcnt lgkmcnt(15)\n" \
"v_mfma_f32_16x16x32_bf16 v[184:187], a[208:211], a[172:175], v[184:187]\n" \
"v_mfma_f32_16x16x32_bf16 v[188:191], a[208:211], a[188:191], v[188:191]\n" \
"ds_read_b64_tr_b16 a[208:209], v34 offset:18464\n" \
"ds_read_b64_tr_b16 a[210:211], v34 offset:23072\n" \
"v_mfma_f32_16x16x32_bf16 v[188:191], a[204:207], a[184:187], v[188:191]\n" \
"ds_read_b64_tr_b16 a[204:205], v34 offset:9248\n" \
"ds_read_b64_tr_b16 a[206:207], v34 offset:13856\n" \
"v_mfma_f32_16x16x32_bf16 v[188:191], a[200:203], a[180:183], v[188:191]\n" \
"ds_read_b64_tr_b16 a[200:201], v34 offset:32\n" \
"ds_read_b64_tr_b16 a[202:203], v34 offset:4640\n" \
"v_mfma_f32_16x16x32_bf16 v[188:191], a[196:199], a[176:179], v[188:191]\n" \
"ds_read_b64_tr_b16 a[196:197], v33 offset:46112\n" \
"ds_read_b64_tr_b16 a[198:199], v33 offset:50720\n" \
"v_mfma_f32_16x16x32_bf16 v[184:187], a[212:215], a[220:223], v[184:187]\n" \
"v_mfma_f32_16x16x32_bf16 v[184:187], a[216:219], a[224:227], v[184:187]\n" \
"v_mfma_f32_16x16x32_bf16 v[184:187], a[0:3], a[228:231], v[184:187]\n" \
"s_waitcnt lgkmcnt(13)\n" \
"v_mfma_f32_16x16x32_bf16 v[184:187], a[4:7], a[232:235], v[184:187]\n" \
"v_mfma_f32_16x16x32_bf16 v[188:191], a[4:7], a[248:251], v[188:191]\n" \
"ds_read_b64_tr_b16 a[4:5], v10 offset:46144\n" \
"ds_read_b64_tr_b16 a[6:7], v10 offset:50752\n" \
"v_mfma_f32_16x16x32_bf16 v[188:191], a[0:3], a[244:247], v[188:191]\n" \
"ds_read_b64_tr_b16 a[0:1], v10 offset:36928\n" \
"ds_read_b64_tr_b16 a[2:3], v10 offset:41536\n" \
"v_mfma_f32_16x16x32_bf16 v[188:191], a[216:219], a[240:243], v[188:191]\n" \
"ds_read_b64_tr_b16 a[216:217], v10 offset:27712\n" \
"v_cvt_pk_bf16_f32 v184, v184, v185\n" \
"v_cvt_pk_bf16_f32 v185, v186, v187\n" \
"v_mfma_f32_16x16x32_bf16 v[188:191], a[212:215], a[236:239], v[188:191]\n" \
"ds_read_b64_tr_b16 a[212:213], v33 offset:32\n" \
"ds_read_b64_tr_b16 a[214:215], v33 offset:4640\n" \
"s_waitcnt vmcnt(6)\n" \
"s_mov_b64 exec, 1\n" \
"buffer_store_dword v39, off, s[56:59], s94 offset:24 sc1\n" \
"s_mov_b64 exec, -1\n" \
"s_waitcnt vmcnt(5)\n" \
"v_cmp_eq_u32_e64 s[98:99], s62, v37\n" \
"v_cvt_pk_bf16_f32 v186, v188, v189\n" \
"v_cvt_pk_bf16_f32 v187, v190, v191\n" \
"v_mfma_f32_16x16x32_bf16 v[192:195], a[8:11], a[32:35], v[192:195]\n" \
"ds_read_b64_tr_b16 a[218:219], v10 offset:32320\n" \
"s_and_b32 s99, s98, 0x0\n" \
"buffer_store_dwordx4 v[184:187], v20, s[56:59], s97 offen offset:1024 sc1\n" \
"s_add_u32 s97, s97, 0x3800\n" \
"v_mfma_f32_16x16x32_bf16 v[192:195], a[12:15], a[36:39], v[192:195]\n" \
"s_or_b32 s84, s84, s99\n" \
"v_readlane_b32 s94, v11, 39\n" \
"v_mfma_f32_16x16x32_bf16 v[192:195], a[16:19], a[40:43], v[192:195]\n" \
"s_waitcnt lgkmcnt(8)\n" \
"v_mfma_f32_16x16x32_bf16 v[192:195], a[20:23], a[44:47], v[192:195]\n" \
"s_andn2_b32 s99, s100, s98\n" \
"s_cselect_b32 s99, 0, 16\n" \
"v_mfma_f32_16x16x32_bf16 v[196:199], a[20:23], a[60:63], v[196:199]\n" \
"ds_read_b64_tr_b16 a[20:21], v33 offset:36928\n" \
"ds_read_b64_tr_b16 a[22:23], v33 offset:41536\n" \
"v_mfma_f32_16x16x32_bf16 v[196:199], a[16:19], a[56:59], v[196:199]\n" \
"ds_read_b64_tr_b16 a[16:17], v33 offset:27712\n" \
"ds_read_b64_tr_b16 a[18:19], v33 offset:32320\n" \
"v_mfma_f32_16x16x32_bf16 v[196:199], a[12:15], a[52:55], v[196:199]\n" \
"ds_read_b64_tr_b16 a[12:13], v33 offset:18496\n" \
"ds_read_b64_tr_b16 a[14:15], v33 offset:23104\n" \
"v_mfma_f32_16x16x32_bf16 v[196:199], a[8:11], a[48:51], v[196:199]\n" \
"ds_read_b64_tr_b16 a[8:9], v10 offset:55360\n" \
"ds_read_b64_tr_b16 a[10:11], v10 offset:59968\n" \
"v_mfma_f32_16x16x32_bf16 v[192:195], a[24:27], a[160:163], v[192:195]\n" \
"s_or_b32 s84, s84, s99\n" \
"s_bitcmp1_b32 s84, 4\n" \
"s_cselect_b32 s96, s75, 0x40000000\n" \
"buffer_load_dwordx4 v[176:179], v20, s[56:59], s96 offen offset:0 sc1\n" \
"v_mfma_f32_16x16x32_bf16 v[192:195], a[28:31], a[164:167], v[192:195]\n" \
"v_mfma_f32_16x16x32_bf16 v[192:195], a[192:195], a[168:171], v[192:195]\n" \
"v_mfma_f32_16x16x32_bf16 v[192:195], a[196:199], a[172:175], v[192:195]\n" \
"buffer_load_dword v37, v40, s[56:59], s74 offen sc1\n" \
"v_mfma_f32_16x16x32_bf16 v[196:199], a[196:199], a[188:191], v[196:199]\n" \
"ds_read_b64_tr_b16 a[196:197], v34 offset:18496\n" \
"ds_read_b64_tr_b16 a[198:199], v34 offset:23104\n" \
"v_mfma_f32_16x16x32_bf16 v[196:199], a[192:195], a[184:187], v[196:199]\n" \
"ds_read_b64_tr_b16 a[192:193], v34 offset:9280\n" \
"ds_read_b64_tr_b16 a[194:195], v34 offset:13888\n" \
"v_mfma_f32_16x16x32_bf16 v[196:199], a[28:31], a[180:183], v[196:199]\n" \
"buffer_load_dwordx4 v[248:251], v20, s[56:59], s96 offen offset:2048 sc1\n" \
"v_mfma_f32_16x16x32_bf16 v[196:199], a[24:27], a[176:179], v[196:199]\n" \
"ds_read_b64_tr_b16 a[24:25], v33 offset:46144\n" \
"ds_read_b64_tr_b16 a[26:27], v33 offset:50752\n" \
"v_mfma_f32_16x16x32_bf16 v[192:195], a[200:203], a[220:223], v[192:195]\n" \
"ds_read_b64_tr_b16 a[28:29], v34 offset:64\n" \
"ds_read_b64_tr_b16 a[30:31], v34 offset:4672\n" \
"v_mfma_f32_16x16x32_bf16 v[192:195], a[204:207], a[224:227], v[192:195]\n" \
"buffer_load_dwordx4 v[180:183], v38, s[56:59], s96 offen offset:0 sc1\n" \
"v_mfma_f32_16x16x32_bf16 v[192:195], a[208:211], a[228:231], v[192:195]\n" \
"s_waitcnt lgkmcnt(14)\n" \
"v_mfma_f32_16x16x32_bf16 v[192:195], a[212:215], a[232:235], v[192:195]\n" \
"v_mfma_f32_16x16x32_bf16 v[196:199], a[212:215], a[248:251], v[196:199]\n" \
"L_pf23:\n" \
"s_cbranch_execz L_retB\n" \
"s_nop 0\n" \
"buffer_load_dwordx4 v[252:255], v38, s[56:59], s96 offen offset:2048 sc1\n" \
"v_mfma_f32_16x16x32_bf16 v[196:199], a[208:211], a[244:247], v[196:199]\n" \
"ds_read_b64_tr_b16 a[208:209], v10 offset:36960\n" \
"ds_read_b64_tr_b16 a[210:211], v10 offset:41568\n" \
"v_mfma_f32_16x16x32_bf16 v[196:199], a[204:207], a[240:243], v[196:199]\n" \
"ds_read_b64_tr_b16 a[204:205], v10 offset:27744\n" \
"ds_read_b64_tr_b16 a[206:207], v10 offset:32352\n" \
"v_cvt_pk_bf16_f32 v192, v192, v193\n" \
"v_cvt_pk_bf16_f32 v193, v194, v195\n" \
"v_mfma_f32_16x16x32_bf16 v[196:199], a[200:203], a[236:239], v[196:199]\n" \
"buffer_load_dwordx4 v[112:115], v47, s[56:59], s96 offen offset:0 sc1\n" \
"v_mfma_f32_16x16x32_bf16 v[200:203], a[216:219], a[32:35], v[200:203]\n" \
"ds_read_b64_tr_b16 a[200:201], v33 offset:64\n" \
"ds_read_b64_tr_b16 a[202:203], v33 offset:4672\n" \
"v_mfma_f32_16x16x32_bf16 v[200:203], a[0:3], a[36:39], v[200:203]\n" \
"ds_read_b64_tr_b16 a[212:213], v10 offset:46176\n" \
"ds_read_b64_tr_b16 a[214:215], v10 offset:50784\n" \
"v_mfma_f32_16x16x32_bf16 v[200:203], a[4:7], a[40:43], v[200:203]\n" \
"v_cvt_pk_bf16_f32 v194, v196, v197\n" \
"v_cvt_pk_bf16_f32 v195, v198, v199\n" \
"s_waitcnt lgkmcnt(13)\n" \
"v_mfma_f32_16x16x32_bf16 v[200:203], a[8:11], a[44:47], v[200:203]\n" \
"buffer_store_dwordx4 v[192:195], v20, s[56:59], s64 offen offset:1024 sc1\n" \
"s_add_u32 s64, s64, 0x3000\n" \
"v_mfma_f32_16x16x32_bf16 v[204:207], a[8:11], a[60:63], v[204:207]\n" \
"ds_read_b64_tr_b16 a[8:9], v33 offset:36960\n" \
"ds_read_b64_tr_b16 a[10:11], v33 offset:41568\n" \
"v_mfma_f32_16x16x32_bf16 v[204:207], a[4:7], a[56:59], v[204:207]\n" \
"ds_read_b64_tr_b16 a[4:5], v33 offset:27744\n" \
"ds_read_b64_tr_b16 a[6:7], v33 offset:32352\n" \
"v_mfma_f32_16x16x32_bf16 v[204:207], a[0:3], a[52:55], v[204:207]\n" \
"buffer_load_dwordx4 v[120:123], v47, s[56:59], s96 offen offset:2048 sc1\n" \
"v_mfma_f32_16x16x32_bf16 v[204:207], a[216:219], a[48:51], v[204:207]\n" \
"ds_read_b64_tr_b16 a[216:217], v10 offset:55392\n" \
"ds_read_b64_tr_b16 a[218:219], v10 offset:60000\n" \
"v_mfma_f32_16x16x32_bf16 v[200:203], a[12:15], a[160:163], v[200:203]\n" \
"ds_read_b64_tr_b16 a[0:1], v33 offset:18528\n" \
"ds_read_b64_tr_b16 a[2:3], v33 offset:23136\n" \
"v_mfma_f32_16x16x32_bf16 v[200:203], a[16:19], a[164:167], v[200:203]\n" \
"buffer_load_dwordx4 v[128:131], v54, s[56:59], s96 offen offset:0 sc1\n" \
"v_mfma_f32_16x16x32_bf16 v[200:203], a[20:23], a[168:171], v[200:203]\n" \
"s_waitcnt lgkmcnt(15)\n" \
"v_mfma_f32_16x16x32_bf16 v[200:203], a[24:27], a[172:175], v[200:203]\n" \
"v_mfma_f32_16x16x32_bf16 v[204:207], a[24:27], a[188:191], v[204:207]\n" \
"buffer_load_dwordx4 v[136:139], v54, s[56:59], s96 offen offset:2048 sc1\n" \
"s_waitcnt vmcnt(13)\n" \
"s_mov_b64 exec, 1\n" \
"buffer_store_dword v39, off, s[56:59], s95 offset:28 sc1\n" \
"s_mov_b64 exec, -1\n" \
"v_mfma_f32_16x16x32_bf16 v[204:207], a[20:23], a[184:187], v[204:207]\n" \
"ds_read_b64_tr_b16 a[20:21], v34 offset:9312\n" \
"ds_read_b64_tr_b16 a[22:23], v34 offset:13920\n" \
"v_mfma_f32_16x16x32_bf16 v[204:207], a[16:19], a[180:183], v[204:207]\n" \
"ds_read_b64_tr_b16 a[16:17], v34 offset:96\n" \
"ds_read_b64_tr_b16 a[18:19], v34 offset:4704\n" \
"v_mfma_f32_16x16x32_bf16 v[204:207], a[12:15], a[176:179], v[204:207]\n" \
"ds_read_b64_tr_b16 a[12:13], v33 offset:46176\n" \
"ds_read_b64_tr_b16 a[14:15], v33 offset:50784\n" \
"v_mfma_f32_16x16x32_bf16 v[200:203], a[28:31], a[220:223], v[200:203]\n" \
"ds_read_b64_tr_b16 a[24:25], v34 offset:18528\n" \
"ds_read_b64_tr_b16 a[26:27], v34 offset:23136\n" \
"v_mfma_f32_16x16x32_bf16 v[200:203], a[192:195], a[224:227], v[200:203]\n" \
"v_readlane_b32 s95, v11, 40\n" \
"v_mfma_f32_16x16x32_bf16 v[200:203], a[196:199], a[228:231], v[200:203]\n" \
"s_waitcnt lgkmcnt(12)\n" \
"v_mfma_f32_16x16x32_bf16 v[200:203], a[200:203], a[232:235], v[200:203]\n" \
"v_mfma_f32_16x16x32_bf16 v[204:207], a[200:203], a[248:251], v[204:207]\n" \
"ds_read_b64_tr_b16 a[200:201], v10 offset:46304\n" \
"ds_read_b64_tr_b16 a[202:203], v10 offset:50912\n" \
"v_mfma_f32_16x16x32_bf16 v[204:207], a[196:199], a[244:247], v[204:207]\n" \
"ds_read_b64_tr_b16 a[196:197], v10 offset:37088\n" \
"ds_read_b64_tr_b16 a[198:199], v10 offset:41696\n" \
"v_mfma_f32_16x16x32_bf16 v[204:207], a[192:195], a[240:243], v[204:207]\n" \
"ds_read_b64_tr_b16 a[192:193], v10 offset:27872\n" \
"v_cvt_pk_bf16_f32 v200, v200, v201\n" \
"v_cvt_pk_bf16_f32 v201, v202, v203\n" \
"v_mfma_f32_16x16x32_bf16 v[204:207], a[28:31], a[236:239], v[204:207]\n" \
"ds_read_b64_tr_b16 a[28:29], v33 offset:96\n" \
"ds_read_b64_tr_b16 a[30:31], v33 offset:4704\n" \
"v_mfma_f32_16x16x32_bf16 v[208:211], a[204:207], a[32:35], v[208:211]\n" \
"ds_read_b64_tr_b16 a[194:195], v10 offset:32480\n" \
"v_mfma_f32_16x16x32_bf16 v[208:211], a[208:211], a[36:39], v[208:211]\n" \
"v_mfma_f32_16x16x32_bf16 v[208:211], a[212:215], a[40:43], v[208:211]\n" \
"s_waitcnt lgkmcnt(15)\n" \
"v_mfma_f32_16x16x32_bf16 v[208:211], a[216:219], a[44:47], v[208:211]\n" \
"v_cvt_pk_bf16_f32 v202, v204, v205\n" \
"v_cvt_pk_bf16_f32 v203, v206, v207\n" \
"v_mfma_f32_16x16x32_bf16 v[212:215], a[216:219], a[60:63], v[212:215]\n" \
"buffer_store_dwordx4 v[200:203], v20, s[56:59], s66 offen offset:1024 sc1\n" \
"s_add_u32 s66, s66, 0x2800\n" \
"v_mfma_f32_16x16x32_bf16 v[212:215], a[212:215], a[56:59], v[212:215]\n" \
"ds_read_b64_tr_b16 a[212:213], v33 offset:27872\n" \
"ds_read_b64_tr_b16 a[214:215], v33 offset:32480\n" \
"v_mfma_f32_16x16x32_bf16 v[212:215], a[208:211], a[52:55], v[212:215]\n" \
"ds_read_b64_tr_b16 a[208:209], v33 offset:18656\n" \
"ds_read_b64_tr_b16 a[210:211], v33 offset:23264\n" \
"v_mfma_f32_16x16x32_bf16 v[212:215], a[204:207], a[48:51], v[212:215]\n" \
"ds_read_b64_tr_b16 a[204:205], v10 offset:55520\n" \
"ds_read_b64_tr_b16 a[206:207], v10 offset:60128\n" \
"v_mfma_f32_16x16x32_bf16 v[208:211], a[0:3], a[160:163], v[208:211]\n" \
"ds_read_b64_tr_b16 a[216:217], v33 offset:37088\n" \
"ds_read_b64_tr_b16 a[218:219], v33 offset:41696\n" \
"v_mfma_f32_16x16x32_bf16 v[208:211], a[4:7], a[164:167], v[208:211]\n" \
"v_mfma_f32_16x16x32_bf16 v[208:211], a[8:11], a[168:171], v[208:211]\n" \
"s_waitcnt lgkmcnt(14)\n" \
"v_mfma_f32_16x16x32_bf16 v[208:211], a[12:15], a[172:175], v[208:211]\n" \
"v_mfma_f32_16x16x32_bf16 v[212:215], a[12:15], a[188:191], v[212:215]\n" \
"ds_read_b64_tr_b16 a[12:13], v34 offset:18656\n" \
"ds_read_b64_tr_b16 a[14:15], v34 offset:23264\n" \
"s_waitcnt vmcnt(12)\n" \
"s_mov_b64 exec, 1\n" \
"buffer_store_dword v39, off, s[56:59], s97 offset:32 sc1\n" \
"s_mov_b64 exec, -1\n" \
"v_mfma_f32_16x16x32_bf16 v[212:215], a[8:11], a[184:187], v[212:215]\n" \
"ds_read_b64_tr_b16 a[8:9], v34 offset:9440\n" \
"ds_read_b64_tr_b16 a[10:11], v34 offset:14048\n" \
"v_mfma_f32_16x16x32_bf16 v[212:215], a[4:7], a[180:183], v[212:215]\n" \
"ds_read_b64_tr_b16 a[4:5], v34 offset:224\n" \
"ds_read_b64_tr_b16 a[6:7], v34 offset:4832\n" \
"v_mfma_f32_16x16x32_bf16 v[212:215], a[0:3], a[176:179], v[212:215]\n" \
"ds_read_b64_tr_b16 a[0:1], v33 offset:46304\n" \
"ds_read_b64_tr_b16 a[2:3], v33 offset:50912\n" \
"v_mfma_f32_16x16x32_bf16 v[208:211], a[16:19], a[220:223], v[208:211]\n" \
"s_waitcnt vmcnt(11)\n" \
"v_cmp_eq_u32_e64 s[98:99], s62, v37\n" \
"buffer_load_dword v37, v40, s[56:59], s74 offen sc1\n" \
"v_mfma_f32_16x16x32_bf16 v[208:211], a[20:23], a[224:227], v[208:211]\n" \
"s_and_b32 s99, s98, 0x20\n" \
"s_or_b32 s84, s84, s99\n" \
"s_bitcmp1_b32 s84, 5\n" \
"s_cselect_b32 s65, s63, 0x40000000\n" \
"v_mfma_f32_16x16x32_bf16 v[208:211], a[24:27], a[228:231], v[208:211]\n" \
"s_waitcnt lgkmcnt(12)\n" \
"v_mfma_f32_16x16x32_bf16 v[208:211], a[28:31], a[232:235], v[208:211]\n" \
"v_mfma_f32_16x16x32_bf16 v[212:215], a[28:31], a[248:251], v[212:215]\n" \
"buffer_load_dwordx4 v[148:151], v47, s[56:59], s65 offen offset:0 sc1\n" \
"v_mfma_f32_16x16x32_bf16 v[212:215], a[24:27], a[244:247], v[212:215]\n" \
"ds_read_b64_tr_b16 a[24:25], v36 offset:2048\n" \
"ds_read_b64_tr_b16 a[26:27], v36 offset:3072\n" \
"v_mfma_f32_16x16x32_bf16 v[212:215], a[20:23], a[240:243], v[212:215]\n" \
"ds_read_b64_tr_b16 a[20:21], v36 offset:0\n" \
"ds_read_b64_tr_b16 a[22:23], v36 offset:1024\n" \
"v_cvt_pk_bf16_f32 v208, v208, v209\n" \
"v_cvt_pk_bf16_f32 v209, v210, v211\n" \
"v_mfma_f32_16x16x32_bf16 v[212:215], a[16:19], a[236:239], v[212:215]\n" \
"ds_read_b64_tr_b16 a[16:17], v33 offset:224\n" \
"ds_read_b64_tr_b16 a[18:19], v33 offset:4832\n" \
"v_mfma_f32_16x16x32_bf16 v[240:243], a[192:195], a[32:35], v[240:243]\n" \
"buffer_load_dwordx4 v[220:223], v47, s[56:59], s65 offen offset:1024 sc1\n" \
"v_mfma_f32_16x16x32_bf16 v[240:243], a[196:199], a[36:39], v[240:243]\n" \
"ds_read_b64_tr_b16 a[28:29], v36 offset:32\n" \
"ds_read_b64_tr_b16 a[30:31], v36 offset:1056\n" \
"v_mfma_f32_16x16x32_bf16 v[240:243], a[200:203], a[40:43], v[240:243]\n" \
"v_cvt_pk_bf16_f32 v210, v212, v213\n" \
"v_cvt_pk_bf16_f32 v211, v214, v215\n" \
"s_waitcnt lgkmcnt(10)\n" \
"v_mfma_f32_16x16x32_bf16 v[240:243], a[204:207], a[44:47], v[240:243]\n" \
"buffer_store_dwordx4 v[208:211], v20, s[56:59], s67 offen offset:1024 sc1\n" \
"s_add_u32 s67, s67, 0x2000\n" \
"v_mfma_f32_16x16x32_bf16 v[244:247], a[204:207], a[60:63], v[244:247]\n" \
"v_mfma_f32_16x16x32_bf16 v[244:247], a[200:203], a[56:59], v[244:247]\n" \
"v_mfma_f32_16x16x32_bf16 v[244:247], a[196:199], a[52:55], v[244:247]\n" \
"v_mfma_f32_16x16x32_bf16 v[244:247], a[192:195], a[48:51], v[244:247]\n" \
"ds_read_b64_tr_b16 a[192:193], v36 offset:2080\n" \
"ds_read_b64_tr_b16 a[194:195], v36 offset:3104\n" \
"v_mfma_f32_16x16x32_bf16 v[168:171], a[208:211], a[128:131], v[168:171]\n" \
"v_mfma_f32_16x16x32_bf16 v[168:171], a[212:215], a[132:135], v[168:171]\n" \
"v_mfma_f32_16x16x32_bf16 v[168:171], a[216:219], a[136:139], v[168:171]\n" \
"s_waitcnt lgkmcnt(9)\n" \
"v_mfma_f32_16x16x32_bf16 v[168:171], a[0:3], a[140:143], v[168:171]\n" \
"v_mfma_f32_16x16x32_bf16 v[172:175], a[0:3], a[156:159], v[172:175]\n" \
"v_mfma_f32_16x16x32_bf16 v[172:175], a[216:219], a[152:155], v[172:175]\n" \
"s_waitcnt vmcnt(10)\n" \
"s_mov_b64 exec, 1\n" \
"buffer_store_dword v39, off, s[56:59], s64 offset:36 sc1\n" \
"s_mov_b64 exec, -1\n" \
"v_mfma_f32_16x16x32_bf16 v[172:175], a[212:215], a[148:151], v[172:175]\n" \
"v_mfma_f32_16x16x32_bf16 v[172:175], a[208:211], a[144:147], v[172:175]\n" \
"v_mfma_f32_16x16x32_bf16 v[240:243], a[208:211], a[160:163], v[240:243]\n" \
"v_mfma_f32_16x16x32_bf16 v[240:243], a[212:215], a[164:167], v[240:243]\n" \
"v_mfma_f32_16x16x32_bf16 v[240:243], a[216:219], a[168:171], v[240:243]\n" \
"v_mfma_f32_16x16x32_bf16 v[240:243], a[0:3], a[172:175], v[240:243]\n" \
"v_mfma_f32_16x16x32_bf16 v[244:247], a[0:3], a[188:191], v[244:247]\n" \
"v_mfma_f32_16x16x32_bf16 v[244:247], a[216:219], a[184:187], v[244:247]\n" \
"v_mfma_f32_16x16x32_bf16 v[244:247], a[212:215], a[180:183], v[244:247]\n" \
"v_mfma_f32_16x16x32_bf16 v[244:247], a[208:211], a[176:179], v[244:247]\n" \
"v_mfma_f32_16x16x32_bf16 v[168:171], a[4:7], a[96:99], v[168:171]\n" \
"v_mfma_f32_16x16x32_bf16 v[168:171], a[8:11], a[100:103], v[168:171]\n" \
"v_mfma_f32_16x16x32_bf16 v[168:171], a[12:15], a[104:107], v[168:171]\n" \
"s_waitcnt vmcnt(6)\n" \
"s_mov_b64 exec, 1\n" \
"buffer_store_dword v39, off, s[56:59], s66 offset:40 sc1\n" \
"s_mov_b64 exec, -1\n" \
"s_waitcnt lgkmcnt(0)\n" \
"v_mfma_f32_16x16x32_bf16 v[168:171], a[16:19], a[108:111], v[168:171]\n" \
"v_mfma_f32_16x16x32_bf16 v[172:175], a[16:19], a[124:127], v[172:175]\n" \
"v_mfma_f32_16x16x32_bf16 v[172:175], a[12:15], a[120:123], v[172:175]\n" \
"v_mfma_f32_16x16x32_bf16 v[172:175], a[8:11], a[116:119], v[172:175]\n" \
"v_mfma_f32_16x16x32_bf16 v[172:175], a[4:7], a[112:115], v[172:175]\n" \
"v_mfma_f32_16x16x32_bf16 v[240:243], a[4:7], a[220:223], v[240:243]\n" \
"v_mfma_f32_16x16x32_bf16 v[240:243], a[8:11], a[224:227], v[240:243]\n" \
"v_mfma_f32_16x16x32_bf16 v[240:243], a[12:15], a[228:231], v[240:243]\n" \
"v_mfma_f32_16x16x32_bf16 v[240:243], a[16:19], a[232:235], v[240:243]\n" \
"v_mfma_f32_16x16x32_bf16 v[244:247], a[16:19], a[248:251], v[244:247]\n" \
"v_mfma_f32_16x16x32_bf16 v[244:247], a[12:15], a[244:247], v[244:247]\n" \
"v_mfma_f32_16x16x32_bf16 v[244:247], a[8:11], a[240:243], v[244:247]\n" \
"v_mfma_f32_16x16x32_bf16 v[244:247], a[4:7], a[236:239], v[244:247]\n" \
"v_mfma_f32_16x16x32_bf16 v[168:171], a[20:23], v[64:67], v[168:171]\n" \
"v_mfma_f32_16x16x32_bf16 v[172:175], a[20:23], v[72:75], v[172:175]\n" \
"v_mfma_f32_16x16x32_bf16 v[240:243], a[20:23], v[80:83], v[240:243]\n" \
"v_mfma_f32_16x16x32_bf16 v[244:247], a[20:23], v[88:91], v[244:247]\n" \
"v_mfma_f32_16x16x32_bf16 v[168:171], a[24:27], v[68:71], v[168:171]\n" \
"v_mfma_f32_16x16x32_bf16 v[172:175], a[24:27], v[76:79], v[172:175]\n" \
"v_mfma_f32_16x16x32_bf16 v[240:243], a[24:27], v[84:87], v[240:243]\n" \
"s_waitcnt vmcnt(5)\n" \
"v_cmp_eq_u32_e64 s[98:99], s62, v37\n" \
"v_mfma_f32_16x16x32_bf16 v[244:247], a[24:27], v[92:95], v[244:247]\n" \
"s_and_b32 s99, s98, 0xfc0\n" \
"s_or_b32 s84, s84, s99\n" \
"s_bitcmp1_b32 s84, 6\n" \
"s_cselect_b32 s68, s63, 0x40000000\n" \
"s_bitcmp1_b32 s84, 7\n" \
"s_cselect_b32 s69, s63, 0x40000000\n" \
"s_bitcmp1_b32 s84, 8\n" \
"s_cselect_b32 s70, s63, 0x40000000\n" \
"s_bitcmp1_b32 s84, 9\n" \
"s_cselect_b32 s71, s63, 0x40000000\n" \
"s_bitcmp1_b32 s84, 10\n" \
"s_cselect_b32 s72, s63, 0x40000000\n" \
"s_bitcmp1_b32 s84, 11\n" \
"s_cselect_b32 s73, s63, 0x40000000\n" \
"buffer_load_dwordx4 v[156:159], v47, s[56:59], s68 offen offset:2048 sc1\n" \
"v_mfma_f32_16x16x32_bf16 v[56:59], a[28:31], v[96:99], 0\n" \
"s_waitcnt vmcnt(3)\n" \
"s_mov_b64 exec, 1\n" \
"buffer_store_dword v39, off, s[56:59], s67 offset:44 sc1\n" \
"s_mov_b64 exec, -1\n" \
"v_mfma_f32_16x16x32_bf16 v[60:63], a[28:31], v[104:107], 0\n" \
"v_mfma_f32_16x16x32_bf16 v[56:59], a[192:195], v[100:103], v[56:59]\n" \
"v_mfma_f32_16x16x32_bf16 v[60:63], a[192:195], v[108:111], v[60:63]\n" \
"buffer_load_dwordx4 v[228:231], v47, s[56:59], s68 offen offset:3072 sc1\n" \
"buffer_load_dwordx4 v[164:167], v54, s[56:59], s69 offen offset:0 sc1\n" \
"buffer_load_dwordx4 v[236:239], v54, s[56:59], s69 offen offset:1024 sc1\n" \
"buffer_load_dwordx4 v[188:191], v20, s[56:59], s70 offen offset:1024 sc1\n" \
"buffer_load_dwordx4 v[196:199], v20, s[56:59], s71 offen offset:3072 sc1\n" \
"buffer_load_dwordx4 v[204:207], v38, s[56:59], s72 offen offset:1024 sc1\n" \
"buffer_load_dwordx4 v[212:215], v38, s[56:59], s73 offen offset:3072 sc1\n" \
"s_andn2_b32 s99, 0xff, s84\n" \
"s_cbranch_scc1 L_sx0_%=\n" \
"L_rx0_%=:\n" \
"v_mfma_f32_16x16x32_bf16 v[168:171], v[50:53], v[116:119], v[168:171]\n" \
"v_mfma_f32_16x16x32_bf16 v[172:175], v[48:51], v[116:119], v[172:175]\n" \
"v_mfma_f32_16x16x32_bf16 v[168:171], v[50:53], v[124:127], v[168:171]\n" \
"v_mfma_f32_16x16x32_bf16 v[172:175], v[48:51], v[124:127], v[172:175]\n" \
"v_mfma_f32_16x16x32_bf16 v[168:171], v[50:53], v[132:135], v[168:171]\n" \
"v_mfma_f32_16x16x32_bf16 v[172:175], v[48:51], v[132:135], v[172:175]\n" \
"v_mfma_f32_16x16x32_bf16 v[168:171], v[50:53], v[140:143], v[168:171]\n" \
"v_mfma_f32_16x16x32_bf16 v[172:175], v[48:51], v[140:143], v[172:175]\n" \
"v_mfma_f32_16x16x32_bf16 v[56:59], v[50:53], v[176:179], v[56:59]\n" \
"v_mfma_f32_16x16x32_bf16 v[60:63], v[48:51], v[176:179], v[60:63]\n" \
"v_mfma_f32_16x16x32_bf16 v[56:59], v[50:53], v[248:251], v[56:59]\n" \
"v_mfma_f32_16x16x32_bf16 v[60:63], v[48:51], v[248:251], v[60:63]\n" \
"v_mfma_f32_16x16x32_bf16 v[56:59], v[50:53], v[180:183], v[56:59]\n" \
"v_mfma_f32_16x16x32_bf16 v[60:63], v[48:51], v[180:183], v[60:63]\n" \
"v_mfma_f32_16x16x32_bf16 v[56:59], v[50:53], v[252:255], v[56:59]\n" \
"v_mfma_f32_16x16x32_bf16 v[60:63], v[48:51], v[252:255], v[60:63]\n" \
"v_mfma_f32_16x16x32_bf16 v[56:59], v[50:53], v[112:115], v[56:59]\n" \
"v_mfma_f32_16x16x32_bf16 v[60:63], v[48:51], v[112:115], v[60:63]\n" \
"v_mfma_f32_16x16x32_bf16 v[56:59], v[50:53], v[120:123], v[56:59]\n" \
"v_mfma_f32_16x16x32_bf16 v[60:63], v[48:51], v[120:123], v[60:63]\n" \
"v_mfma_f32_16x16x32_bf16 v[56:59], v[50:53], v[128:131], v[56:59]\n" \
"v_mfma_f32_16x16x32_bf16 v[60:63], v[48:51], v[128:131], v[60:63]\n" \
"v_mfma_f32_16x16x32_bf16 v[56:59], v[50:53], v[136:139], v[56:59]\n" \
"v_mfma_f32_16x16x32_bf16 v[60:63], v[48:51], v[136:139], v[60:63]\n" \
"v_mfma_f32_16x16x32_bf16 v[168:171], v[50:53], v[148:151], v[168:171]\n" \
"v_mfma_f32_16x16x32_bf16 v[172:175], v[48:51], v[148:151], v[172:175]\n" \
"v_mfma_f32_16x16x32_bf16 v[240:243], v[50:53], v[220:223], v[240:243]\n" \
"v_mfma_f32_16x16x32_bf16 v[244:247], v[48:51], v[220:223], v[244:247]\n" \
"s_waitcnt vmcnt(8)\n" \
"v_mfma_f32_16x16x32_bf16 v[168:171], v[50:53], v[156:159], v[168:171]\n" \
"v_mfma_f32_16x16x32_bf16 v[172:175], v[48:51], v[156:159], v[172:175]\n" \
"v_cvt_pk_bf16_f32 v56, v56, v57\n" \
"v_cvt_pk_bf16_f32 v57, v58, v59\n" \
"v_cvt_pk_bf16_f32 v58, v60, v61\n" \
"v_cvt_pk_bf16_f32 v59, v62, v63\n" \
"s_waitcnt vmcnt(6)\n" \
"v_mfma_f32_16x16x32_bf16 v[240:243], v[50:53], v[228:231], v[240:243]\n" \
"v_permlane16_swap_b32_e32 v56, v58\n" \
"v_mfma_f32_16x16x32_bf16 v[244:247], v[48:51], v[228:231], v[244:247]\n" \
"v_permlane16_swap_b32_e32 v57, v59\n" \
"buffer_store_dwordx4 v[56:59], v42, s[88:91], s93 offen sc0 sc1\n" \
"s_waitcnt vmcnt(6)\n" \
"v_mfma_f32_16x16x32_bf16 v[168:171], v[50:53], v[164:167], v[168:171]\n" \
"v_mfma_f32_16x16x32_bf16 v[172:175], v[48:51], v[164:167], v[172:175]\n" \
"s_waitcnt vmcnt(5)\n" \
"v_mfma_f32_16x16x32_bf16 v[240:243], v[50:53], v[236:239], v[240:243]\n" \
"v_mfma_f32_16x16x32_bf16 v[244:247], v[48:51], v[236:239], v[244:247]\n" \
"s_andn2_b32 s99, 0xf00, s84\n" \
"s_cbranch_scc1 L_sx1_%=\n" \
"L_rx1_%=:\n" \
"s_waitcnt vmcnt(4)\n" \
"v_mfma_f32_16x16x32_bf16 v[240:243], v[50:53], v[188:191], v[240:243]\n" \
"v_cvt_pk_bf16_f32 v168, v168, v169\n" \
"v_cvt_pk_bf16_f32 v169, v170, v171\n" \
"v_mfma_f32_16x16x32_bf16 v[244:247], v[48:51], v[188:191], v[244:247]\n" \
"v_cvt_pk_bf16_f32 v170, v172, v173\n" \
"v_cvt_pk_bf16_f32 v171, v174, v175\n" \
"s_waitcnt vmcnt(3)\n" \
"v_mfma_f32_16x16x32_bf16 v[240:243], v[50:53], v[196:199], v[240:243]\n" \
"v_permlane16_swap_b32_e32 v168, v170\n" \
"v_mfma_f32_16x16x32_bf16 v[244:247], v[48:51], v[196:199], v[244:247]\n" \
"v_permlane16_swap_b32_e32 v169, v171\n" \
"buffer_store_dwordx4 v[168:171], v42, s[88:91], s94 offen sc0 sc1\n" \
"s_waitcnt vmcnt(3)\n" \
"v_mfma_f32_16x16x32_bf16 v[240:243], v[50:53], v[204:207], v[240:243]\n" \
"v_mfma_f32_16x16x32_bf16 v[244:247], v[48:51], v[204:207], v[244:247]\n" \
"s_waitcnt vmcnt(2)\n" \
"v_mfma_f32_16x16x32_bf16 v[240:243], v[50:53], v[212:215], v[240:243]\n" \
"v_mfma_f32_16x16x32_bf16 v[244:247], v[48:51], v[212:215], v[244:247]\n" \
"s_nop 6\n" \
"v_cvt_pk_bf16_f32 v240, v240, v241\n" \
"v_cvt_pk_bf16_f32 v241, v242, v243\n" \
"v_cvt_pk_bf16_f32 v242, v244, v245\n" \
"v_cvt_pk_bf16_f32 v243, v246, v247\n" \
"s_nop 0\n" \
"v_permlane16_swap_b32_e32 v240, v242\n" \
"v_permlane16_swap_b32_e32 v241, v243\n" \
"buffer_store_dwordx4 v[240:243], v42, s[88:91], s95 offen sc0 sc1\n"
#define ATT_VCLOB "v21","v22","v23","v32","v33","v34","v35","v36","v37","v38","v39","v40","v41","v42","v43","v44","v45","v46","v47","v48","v49","v50","v51","v52","v53","v54","v55","v56","v57","v58","v59","v60","v61","v62","v63","v64","v65","v66","v67","v68","v69","v70","v71","v72","v73","v74","v75","v76","v77","v78","v79","v80","v81","v82","v83","v84","v85","v86","v87","v88","v89","v90","v91","v92","v93","v94","v95","v96","v97","v98","v99","v100","v101","v102","v103","v104","v105","v106","v107","v108","v109","v110","v111","v112","v113","v114","v115","v116","v117","v118","v119","v120","v121","v122","v123","v124","v125","v126","v127","v128","v129","v130","v131","v132","v133","v134","v135","v136","v137","v138","v139","v140","v141","v142","v143","v144","v145","v146","v147","v148","v149","v150","v151","v152","v153","v154","v155","v156","v157","v158","v159","v160","v161","v162","v163","v164","v165","v166","v167","v168","v169","v170","v171","v172","v173","v174","v175","v176","v177","v178","v179","v180","v181","v182","v183","v184","v185","v186","v187","v188","v189","v190","v191","v192","v193","v194","v195","v196","v197","v198","v199","v200","v201","v202","v203","v204","v205","v206","v207","v208","v209","v210","v211","v212","v213","v214","v215","v216","v217","v218","v219","v220","v221","v222","v223","v224","v225","v226","v227","v228","v229","v230","v231","v232","v233","v234","v235","v236","v237","v238","v239","v240","v241","v242","v243","v244","v245","v246","v247","v248","v249","v250","v251","v252","v253","v254","v255"
#define ATT_SCLOB "s63","s64","s65","s66","s67","s68","s69","s70","s71","s72","s73","s74","s75","s76","s77","s78","s79","s80","s81","s82","s83","s84","s85","s86","s87","s88","s89","s90","s91","s92","s93","s95","s96","s97","s98","s99","s100","s101"
enum { T_fo, T_fa, T_fas, T_fb, T_fbs, T_f8, T_mk8, T_wol, T_woh, T_wbl, T_wbh, T_outl, T_outh, T_r1l, T_r1h, T_r1n, T_r1s, T_m0, T_m1, T_mw, T_m4, T_ml, T_w0, T_w1, T_w2, T_w3, T_A1, T_A2, T_A3, T_B1, T_B2, T_B3, T_Ao, T_Bo, T_Al, T_Bl, T_l8, T_xr, T_x8r, T_oA, T_oB, T_o8, T_x0, T_x1, T_x2, T_x3, T_x4, T_x5, T_x6, T_x8, T_ep, T_xf, T_kf };

__device__ __forceinline__ void out_consts(u32x4 (&vp)[3], const u16* wo, const u16* wob, const u16* out, const unsigned b, const unsigned w, const unsigned lane) {
    const unsigned sp = b & 7u, nt = (b >> 3) & 3u, j = b >> 5, head = 4u * sp + nt, OOR = 0x40000000u;
    unsigned vw[9];
#pragma unroll
    for (unsigned i = 0; i < 9u; ++i) {
        const unsigned q = 64u * i + lane, row = __umul24(q, 3641u) >> 16, c = q - __umul24(row, 18u);
        vw[i] = __umul24(row, DM * 2) + (c < 16u ? (c * 16u + sp * 32u + 32u) & 255u : c * 16u) - (i & 3u) * 1024u;
    }
    const unsigned row = 16u * w + (lane >> 2), r9 = __umul24(16u * (row >> 5) + (row & 12u) + 2u * ((row >> 4) & 1u) + ((row >> 1) & 1u), 18u) + 16u + (row & 1u);
    unsigned vwl = __umul24(r9, DM * 2) + ((lane & 2u) ? 256u : sp * 32u) + (lane & 1u) * 16u, vrd = __umul24(lane >> 2, 288u) + (lane & 3u) * 8u;
    asm volatile("" : "+v"(vw[0]), "+v"(vw[1]), "+v"(vw[2]), "+v"(vw[3]), "+v"(vw[4]), "+v"(vw[5]), "+v"(vw[6]), "+v"(vw[7]), "+v"(vw[8]), "+v"(vwl), "+v"(vrd));
    unsigned tab = 0;
#define TB(n, v) asm("v_writelane_b32 %0, %1, %2" : "+v"(tab) : "s"((unsigned)(v)), "n"((int)T_##n))
    const unsigned FJ = (unsigned)WS_F + (F_J + 640u * j) * 4u, mt = 2u * (w & 2u) + (w & 1u), w2 = w < 2u ? w : 2u, hw = head * 147456u;
    const unsigned FR = (unsigned)WS_F + F_R * 4u + (8u * j + sp) * 1024u;
    TB(fo, FJ + 4u * head); TB(fa, FR + (w | 2u) * 256u); TB(fas, FR + w * 256u + nt * 4u);
    TB(fb, FR + 128u + w * 256u); TB(fbs, FR + 128u + w * 256u + nt * 4u);
    TB(ep, b + w ? OOR : (unsigned)WS_F + F_EPOCH * 4u);
    const u16* pwo = wo + nt * 144u; const u16* pwb = wob + nt * 144u; const u16* pr1 = w < 3u ? pwo : pwb;
    TB(wol, PLO(pwo)); TB(woh, PHI(pwo) & 0xffffu); TB(wbl, PLO(pwb)); TB(wbh, PHI(pwb) & 0xffffu); TB(outl, PLO(out)); TB(outh, PHI(out) & 0xffffu); TB(r1l, PLO(pr1)); TB(r1h, PHI(pr1) & 0xffffu);
    TB(r1n, (w < 3u ? 72u * sp + 71u : 4095u) * 1152u + 288u); TB(r1s, w < 3u ? (72u * sp + 32u * w) * 1152u : hw + 110592u);
    TB(m0, 131072u + 9216u * w2); TB(w0, hw + 36864u * w2); TB(m1, w < 3u ? 9216u * w : 73728u); TB(mw, 9216u * w); TB(m4, w < 3u ? 131072u + 9216u * w : 64512u); TB(ml, 119808u + 1024u * w);
    const unsigned tA = (unsigned)WS_AF + (8u * j + mt) * (KS_TOT * 1024u), tB = (unsigned)WS_AF + (120u - 8u * j + mt) * (KS_TOT * 1024u);
#define HK(k) (4u * sp + ((nt + k) & 3u))
    TB(w1, HK(1u) * 147456u + w * 36864u); TB(w2, HK(2u) * 147456u + w * 36864u); TB(w3, HK(3u) * 147456u + w * 36864u);
    TB(A1, tA + 18432u + HK(1u) * 4096u); TB(A2, tA + 18432u + HK(2u) * 4096u); TB(A3, tA + 18432u + HK(3u) * 4096u);
    TB(B1, tB + 18432u + HK(1u) * 4096u); TB(B2, tB + 18432u + HK(2u) * 4096u); TB(B3, tB + 18432u + HK(3u) * 4096u);
    TB(Ao, tA + sp * 2048u); TB(Bo, tB + sp * 2048u); TB(Al, tA + 16384u); TB(Bl, tB + 16384u);
    const bool red = w == (sp >> 1), blk = sp & 1u;
    const unsigned xg = (unsigned)WS_X + (4u * j + nt) * 589824u + w * 16384u, oA = (128u * j + 16u * mt) * 1152u + nt * 288u, oB = (1920u - 128u * j + 16u * mt) * 1152u + nt * 288u;
    TB(l8, red ? (blk ? tB : tA) + 16384u : OOR); TB(xr, xg + sp * 65536u); TB(x8r, red ? xg + 524288u + (sp & 1u) * 1024u : OOR);
    TB(oA, oA + sp * 32u); TB(oB, oB + sp * 32u); TB(o8, red ? (blk ? oB : oA) + 256u : OOR); TB(x8, xg + 524288u + sp * 2048u);
    TB(f8, xg + w * 131072u + 14400u + sp * 4u); TB(mk8, red ? 0xff0000u : 0u); TB(xf, xg + sp * 65536u + 14336u);
    TB(kf, (unsigned)WS_F + (F_KV + head * 64u) * 4u);
#define XI(i) TB(x##i, xg + ((sp + 1u + i) & 7u) * 65536u + i * 2048u)
    XI(0); XI(1); XI(2); XI(3); XI(4); XI(5); XI(6);
#undef XI
#undef HK
#undef TB
    vp[0] = (u32x4){vw[0], vw[1], vw[2], vw[3]}; vp[1] = (u32x4){vw[4], vw[5], vw[6], vw[7]}; vp[2] = (u32x4){vw[8], vwl, vrd, tab};
}
__global__ __launch_bounds__(256, 1) __attribute__((aligned(4096))) void fused_kernel(const u16* __restrict__ x, const u16* __restrict__ ckv, const u16* __restrict__ wq, const u16* __restrict__ wuk,
                                                       const u16* __restrict__ wuv, const u16* __restrict__ wqkv, const u16* __restrict__ gw,
                                                       const u16* __restrict__ wo, const u16* __restrict__ wob, char* __restrict__ ws, u16* __restrict__ out) {
    using namespace att;
    extern __shared__ __attribute__((aligned(16))) char lds[];
    unsigned wid = rfl(threadIdx.x >> 6);
    code_touch((blockIdx.x >> 3) * 4u + wid, (unsigned)(size_t)lds + 151552u + wid * 512u);
    SBAR();
    asm volatile("" :: "s"(x), "s"(ckv), "s"(wq), "s"(wuk), "s"(wuv), "s"(wqkv), "s"(gw), "s"(wo), "s"(wob), "s"(ws), "s"(out));
    unsigned tag;
    int b = blockIdx.x;
    u32x4 vp[5], sa[5]; unsigned sd[2];
    const unsigned vl = proj_phase(x, ckv, wq, wuk, wuv, wqkv, gw, wo, wob, out, ws, lds, wid, tag, vp, sa, sd);
    asm volatile("" : "+s"(ws));
    unsigned* const flags = (unsigned*)(ws + WS_F);
    RTS(2);
#define AST(L) do {} while (0)
#define SD(J8, H, W) ((unsigned)WS_AF + (((J8) + 2u * ((W) & 2u) + ((W) & 1u)) * KS_TOT + 18u + 4u * (H)) * 1024u)
#define SRDA (u32x4){PLO(ws), PHI(ws) & 0xffffu, (unsigned)WS_L, 0x00020000u}
#define ATT_TAIL "s_endpgm\n"
    unsigned abad;
    asm volatile(ATT_ASM ATT_TAIL "L_end_%=:\n s_nop 1"
                 : "=&{s94}"(abad)
                 : "{v[0:3]}"(vp[0]), "{v[4:7]}"(vp[1]), "{v[8:11]}"(vp[2]), "{v[12:15]}"(vp[3]), "{v[16:19]}"(vp[4]), "{v20}"(vl), "{s[56:59]}"(sa[4]), "{s60}"(sd[0]), "{s61}"(sd[1]), "{s62}"(tag),
                   "{s[8:11]}"(sa[0]), "{s[12:15]}"(sa[1]), "{s[16:19]}"(sa[2]), "{s[20:23]}"(sa[3])
                 : "memory", "m0", "vcc", "scc", ATT_VCLOB, ATT_SCLOB, ACLOB_LO, ACLOB_HI);
    {
        const u16* const __attribute__((address_space(4)))* ka = (const u16* const __attribute__((address_space(4)))*)__builtin_amdgcn_kernarg_segment_ptr();
        unsigned b2 = b, w2 = wid;
        asm volatile("s_waitcnt vmcnt(0)" : "+s"(ka), "+s"(b2), "+s"(w2), "+s"(ws) :: "memory");
        const unsigned j2 = b2 >> 5, p2 = w2 & 1u, h2 = (b2 & 7u) * 4u + ((b2 >> 3) & 3u);
#pragma unroll 1
        for (unsigned i = w2 & 2u; i < ((w2 & 2u) ? (abad ? 8u : 4u) : 2u); ++i)
            att_exact(ka[0], ka[2], ws, h2, (i < 4u ? 1920u - 128u * j2 : 128u * j2 - 128u) + 32u * i + 16u * p2, *reinterpret_cast<const float*>(lds + LDS_G + (32u * i + 16u * p2 + (lane_id() & 15u)) * 4u));
        asm volatile("s_waitcnt vmcnt(0)" ::: "memory");
        unsigned* fa = (unsigned*)(ws + WS_F) + F_R + (8u * j2 + (b2 & 7u)) * 256u + (2u + p2) * 64u + ((b2 >> 3) & 3u);
        if (w2 & 2u) st_flag(fa, tag);
        wait_flags(fa, ld_flag(fa), tag);
        const unsigned l2 = lane_id(); u32x4 c[3]; out_consts(c, ka[7], ka[8], ka[10], b2, w2, l2);
        asm volatile("s_waitcnt vmcnt(0) lgkmcnt(0)\n\ts_nop 7\n\ts_branch L_att_stub" :: "{v[0:3]}"(c[0]), "{v[4:7]}"(c[1]), "{v[8:11]}"(c[2]), "{v20}"(l2 * 16u), "{s[56:59]}"(SRDA), "{s60}"(SD(8u * j2, h2, w2)),
                     "{s61}"(SD(120u - 8u * j2, h2, w2)), "{s62}"(tag) : "memory");
    }
    RTS(5);
}

extern "C" void kernel_launch(void* const* d_in, const int* in_sizes, int n_in, void* d_out, int out_size, void* d_ws, size_t ws_size,
                              hipStream_t stream) {
    (void)hipFuncSetAttribute((const void*)fused_kernel, hipFuncAttributeMaxDynamicSharedMemorySize, att::LDS_BYTES);
    fused_kernel<<<dim3(256), 256, att::LDS_BYTES, stream>>>((const u16*)d_in[0], (const u16*)d_in[4], (const u16*)d_in[5], (const u16*)d_in[6], (const u16*)d_in[7], (const u16*)d_in[1],
                                                             (const u16*)d_in[9], (const u16*)d_in[2], (const u16*)d_in[8], (char*)d_ws, (u16*)d_out);
}
```

```cpp
#include <hip/hip_runtime.h>
#include <hip/hip_bf16.h>
#include <stdint.h>
#include <utility>

typedef short bf16x8 __attribute__((ext_vector_type(8)));
typedef short s16x4 __attribute__((ext_vector_type(4)));
typedef float f32x16 __attribute__((ext_vector_type(16)));
typedef float f32x4 __attribute__((ext_vector_type(4)));
typedef unsigned u32x4 __attribute__((ext_vector_type(4)));
typedef unsigned short u16;

constexpr int S = 2048, DM = 576, NH = 32, HD = 128, DC = 512, NQKV = 1728, DHA = 18;
constexpr int NBIG = NH * HD;
constexpr int KS_TOT = 146;

#define SBAR() __builtin_amdgcn_sched_barrier(0)
constexpr size_t WS_Q = 0, WS_K = WS_Q + (size_t)S * NBIG * 2, WS_V = WS_K + (size_t)S * NBIG * 2, WS_AF = WS_V + (size_t)S * NBIG * 2, WS_X = WS_AF + (size_t)128 * KS_TOT * 1024,
                 WS_P = WS_X + (size_t)64 * 16 * 9216, WS_F = WS_P + (size_t)128 * (4 * 32 * 256 + 1024) * 4;
constexpr size_t WS_L = (size_t)96 << 20;
static_assert(WS_F == 96206848, "workspace map is baked into the generated asm");
constexpr int F_EPOCH = 0, F_J = 1024, F_ST = 8192, F_KV = 40960, F_R = 65536;
constexpr unsigned TAG_STEP = 0x9E3779B1u;
__device__ __forceinline__ unsigned ld_flag(const unsigned* p) { return __hip_atomic_load(p, __ATOMIC_RELAXED, __HIP_MEMORY_SCOPE_AGENT); }
__device__ __forceinline__ void st_flag(unsigned* p, unsigned v) { __hip_atomic_store(p, v, __ATOMIC_RELAXED, __HIP_MEMORY_SCOPE_AGENT); }
__device__ __forceinline__ unsigned ld_flag_async(const unsigned* p) { unsigned v; asm volatile("global_load_dword %0, %1, off sc1" : "=v"(v) : "v"(p) : "memory"); return v; }
#define WAITV(v, N) asm volatile("s_waitcnt vmcnt(" #N ")" : "+v"(v) :: "memory")
__device__ __forceinline__ void wait_flags(const unsigned* p, unsigned v, unsigned tag) {
    if (__builtin_expect(__builtin_amdgcn_ballot_w64(v != tag) != 0ull, 0)) {
        unsigned spins = 0;
        do { __builtin_amdgcn_s_sleep(1); } while (__builtin_amdgcn_ballot_w64(ld_flag(p) != tag) != 0ull && ++spins < (1u << 22));
    }
    asm volatile("" ::: "memory");
}

__device__ __forceinline__ unsigned cvtpk(float lo, float hi) {
    unsigned r; asm volatile("v_cvt_pk_bf16_f32 %0, %1, %2" : "=v"(r) : "v"(lo), "v"(hi)); return r;
}
__device__ __forceinline__ float bf2f(u16 v) { return __uint_as_float(((unsigned)v) << 16); }
__device__ __forceinline__ int crow(int r, int hi) { return (r & 3) + 8 * (r >> 2) + 4 * hi; }

typedef __attribute__((address_space(3))) s16x4* lds_s16x4_p;
__device__ __forceinline__ s16x4 trrd(unsigned addr) { return __builtin_amdgcn_ds_read_tr16_b64_v4i16((lds_s16x4_p)addr); }

__device__ __forceinline__ unsigned lane_id() { unsigned l; asm volatile("v_mbcnt_lo_u32_b32 %0, -1, 0\n\tv_mbcnt_hi_u32_b32 %0, -1, %0" : "=v"(l)); return l; }
#define RTS(slot) do {} while (0)
#define CT_L 5u
__device__ __forceinline__ void code_touch(unsigned wi, unsigned lds_scratch) {
    unsigned long long pc; asm volatile("s_getpc_b64 %0" : "=s"(pc));
    asm volatile("s_mov_b32 m0, %0\n\ts_mov_b64 exec, %3\n\tglobal_load_lds_dword %1, %2\n\ts_mov_b64 exec, -1" :: "s"(lds_scratch), "v"(((threadIdx.x & 63u) * 128u + wi) * 128u), "s"(pc & ~127ull), "n"((1u << CT_L) - 1u) : "m0", "memory");
}
#define ACLOB_LO "a0","a1","a2","a3","a4","a5","a6","a7","a8","a9","a10","a11","a12","a13","a14","a15","a16","a17","a18","a19","a20","a21","a22","a23","a24","a25","a26","a27","a28","a29","a30","a31","a32","a33","a34","a35","a36","a37","a38","a39","a40","a41","a42","a43","a44","a45","a46","a47","a48","a49","a50","a51","a52","a53","a54","a55","a56","a57","a58","a59","a60","a61","a62","a63","a64","a65","a66","a67","a68","a69","a70","a71","a72","a73","a74","a75","a76","a77","a78","a79","a80","a81","a82","a83","a84","a85","a86","a87","a88","a89","a90","a91","a92","a93","a94","a95","a96","a97","a98","a99","a100","a101","a102","a103","a104","a105","a106","a107","a108","a109","a110","a111","a112","a113","a114","a115","a116","a117","a118","a119","a120","a121","a122","a123","a124","a125","a126","a127"
#define ACLOB_HI "a128","a129","a130","a131","a132","a133","a134","a135","a136","a137","a138","a139","a140","a141","a142","a143","a144","a145","a146","a147","a148","a149","a150","a151","a152","a153","a154","a155","a156","a157","a158","a159","a160","a161","a162","a163","a164","a165","a166","a167","a168","a169","a170","a171","a172","a173","a174","a175","a176","a177","a178","a179","a180","a181","a182","a183","a184","a185","a186","a187","a188","a189","a190","a191","a192","a193","a194","a195","a196","a197","a198","a199","a200","a201","a202","a203","a204","a205","a206","a207","a208","a209","a210","a211","a212","a213","a214","a215","a216","a217","a218","a219","a220","a221","a222","a223","a224","a225","a226","a227","a228","a229","a230","a231","a232","a233","a234","a235","a236","a237","a238","a239","a240","a241","a242","a243","a244","a245","a246","a247","a248","a249","a250","a251","a252","a253","a254","a255"
#define PROJ_PRE0 \
"s_add_u32 s62, %[rc], 0x0\n" \
"s_and_b32 s62, s62, 0x3ff\n" \
"s_lshl_b32 s63, s62, 12\n" \
"s_add_u32 m0, %[mA], 0x0\n" \
"s_add_u32 s85, s62, 0x0\n" \
"buffer_load_dwordx4 v22, s[64:67], s85 offen lds\n" \
"s_add_u32 m0, %[mA], 0x400\n" \
"s_add_u32 s84, s62, 0x2000\n" \
"buffer_load_dwordx4 v23, s[64:67], s84 offen lds\n" \
"s_add_u32 m0, %[mB], 0x0\n" \
"s_add_u32 s85, s63, 0x0\n" \
"buffer_load_dwordx4 v26, s[68:71], s85 offen lds\n" \
"s_add_u32 m0, %[mB], 0x4000\n" \
"s_add_u32 s84, s63, 0x0\n" \
"buffer_load_dwordx4 v26, s[72:75], s84 offen lds\n" \
"s_add_u32 m0, %[mA], 0x800\n" \
"s_add_u32 s85, s62, 0x4000\n" \
"buffer_load_dwordx4 v22, s[64:67], s85 offen lds\n" \
"s_add_u32 m0, %[mA], 0xc00\n" \
"s_add_u32 s84, s62, 0x6000\n" \
"buffer_load_dwordx4 v23, s[64:67], s84 offen lds\n" \
"s_add_u32 m0, %[mB], 0x400\n" \
"s_add_u32 s85, s63, 0x8000\n" \
"buffer_load_dwordx4 v26, s[68:71], s85 offen lds\n" \
"s_add_u32 m0, %[mB], 0x4400\n" \
"s_add_u32 s84, s63, 0x8000\n" \
"buffer_load_dwordx4 v26, s[72:75], s84 offen lds\n"
#define PROJ_PRE1 \
"s_add_u32 s62, %[rc], 0x0\n" \
"s_and_b32 s62, s62, 0x3ff\n" \
"s_lshl_b32 s63, s62, 12\n" \
"s_add_u32 m0, %[mA], 0x1000\n" \
"s_add_u32 s85, s62, 0x8000\n" \
"buffer_load_dwordx4 v22, s[64:67], s85 offen lds\n" \
"s_add_u32 m0, %[mA], 0x1400\n" \
"s_add_u32 s84, s62, 0xa000\n" \
"buffer_load_dwordx4 v23, s[64:67], s84 offen lds\n" \
"s_add_u32 m0, %[mB], 0x800\n" \
"s_add_u32 s85, s63, 0x10000\n" \
"buffer_load_dwordx4 v27, s[68:71], s85 offen lds\n" \
"s_add_u32 m0, %[mB], 0x4800\n" \
"s_add_u32 s84, s63, 0x10000\n" \
"buffer_load_dwordx4 v27, s[72:75], s84 offen lds\n" \
"s_add_u32 m0, %[mA], 0x1800\n" \
"s_add_u32 s85, s62, 0xc000\n" \
"buffer_load_dwordx4 v22, s[64:67], s85 offen lds\n" \
"s_add_u32 m0, %[mA], 0x1c00\n" \
"s_add_u32 s84, s62, 0xe000\n" \
"buffer_load_dwordx4 v23, s[64:67], s84 offen lds\n" \
"s_add_u32 m0, %[mB], 0xc00\n" \
"s_add_u32 s85, s63, 0x18000\n" \
"buffer_load_dwordx4 v27, s[68:71], s85 offen lds\n" \
"s_add_u32 m0, %[mB], 0x4c00\n" \
"s_add_u32 s84, s63, 0x18000\n" \
"buffer_load_dwordx4 v27, s[72:75], s84 offen lds\n"
#define PROJ_PRE2 \
"s_add_u32 s62, %[rc], 0x80\n" \
"s_and_b32 s62, s62, 0x3ff\n" \
"s_lshl_b32 s63, s62, 12\n" \
"s_add_u32 m0, %[mA], 0x8000\n" \
"s_add_u32 s85, s62, 0x0\n" \
"buffer_load_dwordx4 v22, s[64:67], s85 offen lds\n" \
"s_add_u32 m0, %[mA], 0x8400\n" \
"s_add_u32 s84, s62, 0x2000\n" \
"buffer_load_dwordx4 v23, s[64:67], s84 offen lds\n"
#define PROJ_PRE3 \
"s_add_u32 s62, %[rc], 0x80\n" \
"s_and_b32 s62, s62, 0x3ff\n" \
"s_lshl_b32 s63, s62, 12\n" \
"s_add_u32 m0, %[mB], 0x8000\n" \
"s_add_u32 s85, s63, 0x0\n" \
"buffer_load_dwordx4 v26, s[68:71], s85 offen lds\n" \
"s_add_u32 m0, %[mB], 0xc000\n" \
"s_add_u32 s84, s63, 0x0\n" \
"buffer_load_dwordx4 v26, s[72:75], s84 offen lds\n" \
"s_add_u32 m0, %[mA], 0x8800\n" \
"s_add_u32 s85, s62, 0x4000\n" \
"buffer_load_dwordx4 v22, s[64:67], s85 offen lds\n" \
"s_add_u32 m0, %[mA], 0x8c00\n" \
"s_add_u32 s84, s62, 0x6000\n" \
"buffer_load_dwordx4 v23, s[64:67], s84 offen lds\n" \
"s_add_u32 m0, %[mB], 0x8400\n" \
"s_add_u32 s85, s63, 0x8000\n" \
"buffer_load_dwordx4 v26, s[68:71], s85 offen lds\n" \
"s_add_u32 m0, %[mB], 0xc400\n" \
"s_add_u32 s84, s63, 0x8000\n" \
"buffer_load_dwordx4 v26, s[72:75], s84 offen lds\n" \
"s_add_u32 m0, %[mA], 0x9000\n" \
"s_add_u32 s85, s62, 0x8000\n" \
"buffer_load_dwordx4 v22, s[64:67], s85 offen lds\n" \
"s_add_u32 m0, %[mA], 0x9400\n" \
"s_add_u32 s84, s62, 0xa000\n" \
"buffer_load_dwordx4 v23, s[64:67], s84 offen lds\n"
#define PROJ_TKE 0
#define PROJ_T0 0
#define PROJ_ROT 2*(sp+mt)
#define PROJ_TC 0
#define PROJ_ASM \
".p2align 3\n" \
"s_branch L_go_%=\n" \
"L_pl_%=:\n" \
"s_mov_b32 s63, 0\n" \
"L_ql_%=:\n" \
"s_sleep 1\n" \
"v_bfe_u32 v44, v20, 2, 5\n" \
"buffer_load_dword v44, v44, s[80:83], s39 offen offset:64 sc1\n" \
"s_waitcnt vmcnt(0)\n" \
"v_cmp_ne_u32_e32 vcc, %[tag], v44\n" \
"s_add_u32 s63, s63, 1\n" \
"s_nop 3\n" \
"s_cbranch_vccz L_rl_%=\n" \
"s_cmp_gt_u32 s63, 0x400000\n" \
"s_cbranch_scc0 L_ql_%=\n" \
"s_branch L_rl_%=\n" \
"L_pa_%=:\n" \
"s_mov_b32 s63, 0\n" \
"L_qa_%=:\n" \
"s_sleep 1\n" \
"v_bfe_u32 v44, v20, 2, 5\n" \
"buffer_load_dword v44, v44, s[80:83], s39 offen offset:0 sc1\n" \
"s_waitcnt vmcnt(0)\n" \
"v_cmp_ne_u32_e32 vcc, %[tag], v44\n" \
"s_add_u32 s63, s63, 1\n" \
"s_nop 3\n" \
"s_cbranch_vccz L_ra_%=\n" \
"s_cmp_gt_u32 s63, 0x400000\n" \
"s_cbranch_scc0 L_qa_%=\n" \
"s_branch L_ra_%=\n" \
"L_go_%=:\n" \
"v_xor_b32_e32 v33, 0x20, v32\n" \
"s_mov_b32 s80, %[wsl]\n" \
"s_and_b32 s81, %[wsh], 0xffff\n" \
"s_mov_b32 s82, 0x10000000\n" \
"s_mov_b32 s83, 0x20000\n" \
"s_mov_b32 s84, 0x5bc0000\n" \
"s_add_u32 s62, s22, 0x80\n" \
"buffer_load_dword v180, off, s[80:83], s84 sc1\n" \
"s_and_b32 s62, s62, 0x3ff\n" \
"s_lshl_b32 s63, s62, 12\n" \
"v_xor_b32_e64 v34, 0x40, v32\n" \
"v_xor_b32_e32 v35, 0x60, v32\n" \
"s_waitcnt vmcnt(11)\n" \
"s_barrier\n" \
"ds_read_b64_tr_b16 v[120:121], v32 offset:8192\n" \
"ds_read_b64_tr_b16 v[122:123], v32 offset:9216\n" \
"ds_read_b128 v[56:59], v29 offset:16384\n" \
"ds_read_b64_tr_b16 v[116:117], v32 offset:0\n" \
"ds_read_b64_tr_b16 v[118:119], v32 offset:1024\n" \
"ds_read_b128 v[52:55], v28 offset:16384\n" \
"s_add_u32 m0, %[mB], 0x8800\n" \
"s_add_u32 s85, s63, 0x10000\n" \
"s_mov_b32 s76, s12\n" \
"ds_read_b128 v[60:63], v28 offset:18432\n" \
"s_and_b32 s77, s13, 0xffff\n" \
"s_mov_b32 s78, 0x1e6000\n" \
"s_mov_b32 s79, 0x20000\n" \
"ds_read_b128 v[64:67], v29 offset:18432\n" \
"s_mov_b32 s87, 0x5040100\n" \
"s_waitcnt lgkmcnt(5)\n" \
"v_mfma_f32_16x16x32_bf16 a[0:3], v[120:123], v[56:59], 0\n" \
"ds_read_b128 v[72:75], v29 offset:20480\n" \
"ds_read_b128 v[68:71], v28 offset:20480\n" \
"ds_read_b128 v[76:79], v28 offset:22528\n" \
"buffer_load_dwordx4 v27, s[68:71], s85 offen lds\n" \
"ds_read_b128 v[80:83], v29 offset:22528\n" \
"s_waitcnt lgkmcnt(6)\n" \
"v_mfma_f32_16x16x32_bf16 a[0:3], v[116:119], v[52:55], a[0:3]\n" \
"ds_read_b64_tr_b16 v[128:129], v33 offset:8192\n" \
"s_waitcnt lgkmcnt(6)\n" \
"v_mfma_f32_16x16x32_bf16 a[32:35], v[116:119], v[60:63], 0\n" \
"ds_read_b64_tr_b16 v[130:131], v33 offset:9216\n" \
"ds_read_b64_tr_b16 v[124:125], v33 offset:0\n" \
"ds_read_b64_tr_b16 v[126:127], v33 offset:1024\n" \
"s_waitcnt lgkmcnt(8)\n" \
"s_nop 0\n" \
"v_mfma_f32_16x16x32_bf16 a[32:35], v[120:123], v[64:67], a[32:35]\n" \
"ds_read_b64_tr_b16 v[136:137], v34 offset:8192\n" \
"ds_read_b64_tr_b16 v[138:139], v34 offset:9216\n" \
"ds_read_b64_tr_b16 v[132:133], v34 offset:0\n" \
"s_add_u32 m0, %[mB], 0xc800\n" \
"ds_read_b64_tr_b16 v[134:135], v34 offset:1024\n" \
"s_add_u32 s84, s63, 0x10000\n" \
"buffer_load_dwordx4 v27, s[72:75], s84 offen lds\n" \
"ds_read_b64_tr_b16 v[144:145], v35 offset:8192\n" \
"ds_read_b64_tr_b16 v[146:147], v35 offset:9216\n" \
"ds_read_b64_tr_b16 v[140:141], v35 offset:0\n" \
"s_lshr_b32 s94, %[b], 3\n" \
"ds_read_b64_tr_b16 v[142:143], v35 offset:1024\n" \
"s_lshl_b32 s95, s94, 2\n" \
"ds_read_b64_tr_b16 v[152:153], v32 offset:24576\n" \
"s_add_u32 s96, s95, %[w]\n" \
"ds_read_b64_tr_b16 v[154:155], v32 offset:25600\n" \
"s_mul_i32 s88, 6, s96\n" \
"ds_read_b64_tr_b16 v[148:149], v32 offset:16384\n" \
"v_lshrrev_b32_e32 v212, 4, v20\n" \
"s_and_b32 s97, 7, %[b]\n" \
"s_add_u32 s89, 0xffffffdc, s88\n" \
"s_waitcnt lgkmcnt(15)\n" \
"v_mfma_f32_16x16x32_bf16 a[64:67], v[120:123], v[72:75], 0\n" \
"ds_read_b64_tr_b16 v[150:151], v32 offset:17408\n" \
"ds_read_b64_tr_b16 v[160:161], v33 offset:24576\n" \
"v_mfma_f32_16x16x32_bf16 a[64:67], v[116:119], v[68:71], a[64:67]\n" \
"s_add_u32 m0, %[mA], 0x9800\n" \
"s_add_u32 s85, s62, 0xc000\n" \
"v_mfma_f32_16x16x32_bf16 a[96:99], v[116:119], v[76:79], 0\n" \
"buffer_load_dwordx4 v22, s[64:67], s85 offen lds\n" \
"v_mfma_f32_16x16x32_bf16 a[96:99], v[120:123], v[80:83], a[96:99]\n" \
"ds_read_b64_tr_b16 v[162:163], v33 offset:25600\n" \
"s_waitcnt lgkmcnt(15)\n" \
"v_mfma_f32_16x16x32_bf16 a[4:7], v[128:131], v[56:59], 0\n" \
"s_add_u32 m0, %[mA], 0x9c00\n" \
"s_waitcnt lgkmcnt(14)\n" \
"s_nop 0\n" \
"v_mfma_f32_16x16x32_bf16 a[4:7], v[124:127], v[52:55], a[4:7]\n" \
"s_add_u32 s84, s62, 0xe000\n" \
"ds_read_b64_tr_b16 v[156:157], v33 offset:16384\n" \
"v_mfma_f32_16x16x32_bf16 a[36:39], v[124:127], v[60:63], 0\n" \
"buffer_load_dwordx4 v23, s[64:67], s84 offen lds\n" \
"v_mfma_f32_16x16x32_bf16 a[36:39], v[128:131], v[64:67], a[36:39]\n" \
"s_add_u32 m0, %[mB], 0x8c00\n" \
"s_add_u32 s85, s63, 0x18000\n" \
"v_mfma_f32_16x16x32_bf16 a[68:71], v[128:131], v[72:75], 0\n" \
"ds_read_b64_tr_b16 v[158:159], v33 offset:17408\n" \
"ds_read_b64_tr_b16 v[168:169], v34 offset:24576\n" \
"v_mfma_f32_16x16x32_bf16 a[68:71], v[124:127], v[68:71], a[68:71]\n" \
"buffer_load_dwordx4 v27, s[68:71], s85 offen lds\n" \
"v_mfma_f32_16x16x32_bf16 a[100:103], v[124:127], v[76:79], 0\n" \
"s_add_u32 m0, %[mB], 0xcc00\n" \
"s_add_u32 s84, s63, 0x18000\n" \
"v_mfma_f32_16x16x32_bf16 a[100:103], v[128:131], v[80:83], a[100:103]\n" \
"ds_read_b64_tr_b16 v[170:171], v34 offset:25600\n" \
"s_waitcnt lgkmcnt(7)\n" \
"s_nop 0\n" \
"v_mfma_f32_16x16x32_bf16 a[8:11], v[136:139], v[56:59], 0\n" \
"buffer_load_dwordx4 v27, s[72:75], s84 offen lds\n" \
"v_mfma_f32_16x16x32_bf16 a[8:11], v[132:135], v[52:55], a[8:11]\n" \
"ds_read_b64_tr_b16 v[164:165], v34 offset:16384\n" \
"ds_read_b64_tr_b16 v[166:167], v34 offset:17408\n" \
"v_mfma_f32_16x16x32_bf16 a[40:43], v[132:135], v[60:63], 0\n" \
"ds_read_b64_tr_b16 v[176:177], v35 offset:24576\n" \
"ds_read_b64_tr_b16 v[178:179], v35 offset:25600\n" \
"v_mfma_f32_16x16x32_bf16 a[40:43], v[136:139], v[64:67], a[40:43]\n" \
"ds_read_b64_tr_b16 v[172:173], v35 offset:16384\n" \
"ds_read_b64_tr_b16 v[174:175], v35 offset:17408\n" \
"v_mfma_f32_16x16x32_bf16 a[72:75], v[136:139], v[72:75], 0\n" \
"ds_read_b128 v[88:91], v29 offset:0\n" \
"ds_read_b128 v[84:87], v28 offset:0\n" \
"v_mfma_f32_16x16x32_bf16 a[72:75], v[132:135], v[68:71], a[72:75]\n" \
"ds_read_b128 v[92:95], v28 offset:2048\n" \
"ds_read_b128 v[96:99], v29 offset:2048\n" \
"v_mfma_f32_16x16x32_bf16 a[104:107], v[132:135], v[76:79], 0\n" \
"s_mul_i32 s98, 0x1200, s97\n" \
"ds_read_b128 v[104:107], v29 offset:4096\n" \
"v_mfma_f32_16x16x32_bf16 a[104:107], v[136:139], v[80:83], a[104:107]\n" \
"s_mul_i32 s99, 36, s96\n" \
"ds_read_b128 v[100:103], v28 offset:4096\n" \
"v_mfma_f32_16x16x32_bf16 a[12:15], v[144:147], v[56:59], 0\n" \
"v_add_u32_e32 v214, s89, v212\n" \
"s_add_u32 s100, s98, s99\n" \
"v_mfma_f32_16x16x32_bf16 a[12:15], v[140:143], v[52:55], a[12:15]\n" \
"ds_read_b128 v[108:111], v28 offset:6144\n" \
"ds_read_b128 v[112:115], v29 offset:6144\n" \
"v_mfma_f32_16x16x32_bf16 a[44:47], v[140:143], v[60:63], 0\n" \
"s_mul_i32 s101, 0x288, s97\n" \
"v_min_u32_e32 v215, 0x287, v214\n" \
"v_mfma_f32_16x16x32_bf16 a[44:47], v[144:147], v[64:67], a[44:47]\n" \
"s_lshl_b32 s41, s96, 7\n" \
"v_add_u32_e64 v213, s100, v212\n" \
"v_mfma_f32_16x16x32_bf16 a[76:79], v[144:147], v[72:75], 0\n" \
"v_add_u32_e32 v216, s101, v215\n" \
"v_cmp_gt_u32_e32 vcc, 36, v212\n" \
"v_mfma_f32_16x16x32_bf16 a[76:79], v[140:143], v[68:71], a[76:79]\n" \
"s_lshl_b32 s96, %[w], 6\n" \
"v_cndmask_b32_e32 v217, v216, v213, vcc\n" \
"v_mfma_f32_16x16x32_bf16 a[108:111], v[140:143], v[76:79], 0\n" \
"v_lshrrev_b32_e32 v212, 4, v20\n" \
"v_add_u32_e32 v213, s96, v212\n" \
"v_mfma_f32_16x16x32_bf16 a[108:111], v[144:147], v[80:83], a[108:111]\n" \
"v_mul_u32_u24_e32 v214, 0x2aab, v213\n" \
"v_lshrrev_b32_e32 v215, 16, v214\n" \
"v_mfma_f32_16x16x32_bf16 a[16:19], v[56:59], v[152:155], 0\n" \
"s_lshr_b32 s94, %[b], 5\n" \
"s_waitcnt lgkmcnt(10)\n" \
"v_mfma_f32_16x16x32_bf16 a[16:19], v[52:55], v[148:151], a[16:19]\n" \
"v_mul_u32_u24_e32 v218, 6, v215\n" \
"s_mul_i32 s95, 0x48, s94\n" \
"v_mfma_f32_16x16x32_bf16 a[48:51], v[60:63], v[148:151], 0\n" \
"v_sub_u32_e32 v219, v213, v218\n" \
"v_lshlrev_b32_e32 v16, 7, v217\n" \
"v_mfma_f32_16x16x32_bf16 a[48:51], v[64:67], v[152:155], a[48:51]\n" \
"v_add_u32_e32 v216, s95, v215\n" \
"v_lshrrev_b32_e32 v220, 1, v219\n" \
"v_mfma_f32_16x16x32_bf16 a[80:83], v[72:75], v[152:155], 0\n" \
"s_and_b32 s100, 7, %[b]\n" \
"s_lshr_b32 s88, %[b], 3\n" \
"v_mfma_f32_16x16x32_bf16 a[80:83], v[68:71], v[148:151], a[80:83]\n" \
"v_mul_u32_u24_e32 v217, 0xd80, v216\n" \
"v_mul_u32_u24_e32 v221, 0x480, v220\n" \
"v_mfma_f32_16x16x32_bf16 a[112:115], v[76:79], v[148:151], 0\n" \
"v_and_b32_e64 v223, 1, v219\n" \
"s_add_u32 s97, 0x100, s96\n" \
"v_mfma_f32_16x16x32_bf16 a[112:115], v[80:83], v[152:155], a[112:115]\n" \
"s_lshl_b32 s101, s100, 2\n" \
"s_and_b32 s89, 3, s88\n" \
"v_mfma_f32_16x16x32_bf16 a[20:23], v[56:59], v[160:163], 0\n" \
"v_add_u32_e32 v222, v217, v221\n" \
"v_lshlrev_b32_e32 v224, 4, v223\n" \
"v_mfma_f32_16x16x32_bf16 a[20:23], v[52:55], v[156:159], a[20:23]\n" \
"s_add_u32 s96, s101, s89\n" \
"v_add_u32_e32 v192, v222, v224\n" \
"v_mfma_f32_16x16x32_bf16 a[52:55], v[60:63], v[156:159], 0\n" \
"s_mul_i32 s56, 36, s96\n" \
"v_add_u32_e32 v225, s97, v212\n" \
"v_mfma_f32_16x16x32_bf16 a[52:55], v[64:67], v[160:163], a[52:55]\n" \
"v_mul_u32_u24_e32 v226, 0x2aab, v225\n" \
"v_lshrrev_b32_e32 v227, 16, v226\n" \
"v_mfma_f32_16x16x32_bf16 a[84:87], v[72:75], v[160:163], 0\n" \
"v_mul_u32_u24_e32 v230, 6, v227\n" \
"v_sub_u32_e32 v231, v225, v230\n" \
"v_mfma_f32_16x16x32_bf16 a[84:87], v[68:71], v[156:159], a[84:87]\n" \
"v_add_u32_e32 v228, s95, v227\n" \
"v_lshrrev_b32_e32 v232, 1, v231\n" \
"v_mfma_f32_16x16x32_bf16 a[116:119], v[76:79], v[156:159], 0\n" \
"v_mul_u32_u24_e32 v229, 0xd80, v228\n" \
"v_mul_u32_u24_e32 v233, 0x480, v232\n" \
"v_mfma_f32_16x16x32_bf16 a[116:119], v[80:83], v[160:163], a[116:119]\n" \
"v_and_b32_e32 v235, 1, v231\n" \
"v_add_u32_e64 v234, v229, v233\n" \
"v_mfma_f32_16x16x32_bf16 a[24:27], v[56:59], v[168:171], 0\n" \
"v_lshlrev_b32_e32 v236, 4, v235\n" \
"v_add_u32_e32 v237, v234, v236\n" \
"v_mfma_f32_16x16x32_bf16 a[24:27], v[52:55], v[164:167], a[24:27]\n" \
"v_cmp_gt_u32_e32 vcc, 0x1b0, v225\n" \
"s_mul_i32 s98, 18, %[w]\n" \
"v_mfma_f32_16x16x32_bf16 a[56:59], v[60:63], v[164:167], 0\n" \
"v_cndmask_b32_e32 v193, 0x40000000, v237, vcc\n" \
"s_add_u32 s99, s95, s98\n" \
"v_mfma_f32_16x16x32_bf16 a[56:59], v[64:67], v[168:171], a[56:59]\n" \
"v_add_u32_e32 v238, s99, v212\n" \
"v_mul_u32_u24_e32 v239, 0xd80, v238\n" \
"v_mfma_f32_16x16x32_bf16 a[88:91], v[72:75], v[168:171], 0\n" \
"v_cmp_gt_u32_e32 vcc, 18, v212\n" \
"v_lshlrev_b32_e32 v240, 1, v238\n" \
"v_mfma_f32_16x16x32_bf16 a[88:91], v[68:71], v[164:167], a[88:91]\n" \
"v_cndmask_b32_e32 v194, 0x40000000, v239, vcc\n" \
"v_cmp_gt_u32_e32 vcc, 18, v212\n" \
"v_mfma_f32_16x16x32_bf16 a[120:123], v[76:79], v[164:167], 0\n" \
"v_lshrrev_b32_e32 v230, 1, v238\n" \
"v_cndmask_b32_e32 v195, 0x40000000, v240, vcc\n" \
"v_mfma_f32_16x16x32_bf16 a[120:123], v[80:83], v[168:171], a[120:123]\n" \
"v_and_b32_e32 v227, 0xff8, v238\n" \
"v_and_b32_e64 v232, 2, v230\n" \
"v_mfma_f32_16x16x32_bf16 a[28:31], v[56:59], v[176:179], 0\n" \
"v_add_u32_e32 v229, v227, v232\n" \
"s_waitcnt lgkmcnt(0)\n" \
"v_mfma_f32_16x16x32_bf16 a[28:31], v[52:55], v[172:175], a[28:31]\n" \
"v_and_b32_e32 v233, 1, v238\n" \
"v_add_u32_e32 v235, v229, v233\n" \
"v_mfma_f32_16x16x32_bf16 a[60:63], v[60:63], v[172:175], 0\n" \
"v_lshrrev_b32_e32 v242, 1, v216\n" \
"v_lshrrev_b32_e32 v244, 2, v216\n" \
"v_mfma_f32_16x16x32_bf16 a[60:63], v[64:67], v[176:179], a[60:63]\n" \
"v_lshrrev_b32_e32 v214, 1, v228\n" \
"v_lshrrev_b32_e32 v213, 2, v228\n" \
"v_mfma_f32_16x16x32_bf16 a[92:95], v[72:75], v[176:179], 0\n" \
"v_lshrrev_b32_e32 v236, 1, v235\n" \
"v_lshrrev_b32_e32 v239, 2, v235\n" \
"v_mfma_f32_16x16x32_bf16 a[92:95], v[68:71], v[172:175], a[92:95]\n" \
"s_lshl_b32 s98, %[w], 10\n" \
"s_mul_i32 s101, 0x12000, s96\n" \
"v_mfma_f32_16x16x32_bf16 a[124:127], v[76:79], v[172:175], 0\n" \
"v_and_b32_e32 v243, 1, v242\n" \
"v_and_b32_e64 v245, 2, v244\n" \
"v_mfma_f32_16x16x32_bf16 a[124:127], v[80:83], v[176:179], a[124:127]\n" \
"v_and_b32_e32 v215, 1, v214\n" \
"v_and_b32_e32 v218, 2, v213\n" \
"v_mfma_f32_16x16x32_bf16 a[144:147], v[88:91], v[152:155], 0\n" \
"v_and_b32_e32 v237, 1, v236\n" \
"v_and_b32_e32 v240, 2, v239\n" \
"v_mfma_f32_16x16x32_bf16 a[144:147], v[84:87], v[148:151], a[144:147]\n" \
"s_add_u32 s99, 0x22000, s98\n" \
"s_add_u32 s89, 0x6000000, s101\n" \
"v_mfma_f32_16x16x32_bf16 a[176:179], v[92:95], v[148:151], 0\n" \
"s_mul_i32 s97, 0x2400, s94\n" \
"v_lshlrev_b32_e32 v241, 7, v216\n" \
"v_mfma_f32_16x16x32_bf16 a[176:179], v[96:99], v[152:155], a[176:179]\n" \
"v_or_b32_e32 v246, v243, v245\n" \
"v_or_b32_e32 v220, v215, v218\n" \
"v_mfma_f32_16x16x32_bf16 a[208:211], v[104:107], v[152:155], 0\n" \
"v_or_b32_e32 v242, v237, v240\n" \
"v_add_u32_e64 v199, s99, v20\n" \
"v_mfma_f32_16x16x32_bf16 a[208:211], v[100:103], v[148:151], a[208:211]\n" \
"s_add_u32 s37, s89, s98\n" \
"v_lshlrev_b32_e32 v247, 1, v246\n" \
"v_mfma_f32_16x16x32_bf16 a[240:243], v[108:111], v[148:151], 0\n" \
"v_lshlrev_b32_e32 v217, 1, v220\n" \
"v_xor_b32_e32 v216, 3, v242\n" \
"v_mfma_f32_16x16x32_bf16 a[240:243], v[112:115], v[152:155], a[240:243]\n" \
"s_add_u32 s57, s37, s97\n" \
"s_lshl_b32 s99, s88, 2\n" \
"v_mfma_f32_16x16x32_bf16 a[148:151], v[88:91], v[160:163], 0\n" \
"v_xor_b32_e32 v248, v219, v247\n" \
"v_xor_b32_e32 v221, v231, v217\n" \
"v_mfma_f32_16x16x32_bf16 a[148:151], v[84:87], v[156:159], a[148:151]\n" \
"v_lshlrev_b32_e32 v234, 7, v235\n" \
"v_lshlrev_b32_e32 v244, 5, v216\n" \
"v_mfma_f32_16x16x32_bf16 a[180:183], v[92:95], v[156:159], 0\n" \
"v_and_b32_e64 v245, 2, v238\n" \
"s_add_u32 s94, 0x2000, s57\n" \
"v_mfma_f32_16x16x32_bf16 a[180:183], v[96:99], v[160:163], a[180:183]\n" \
"s_add_u32 s96, s99, %[w]\n" \
"s_lshl_b32 s89, %[b], 2\n" \
"v_mfma_f32_16x16x32_bf16 a[212:215], v[104:107], v[160:163], 0\n" \
"s_sub_u32 s95, 0x22000, s97\n" \
"v_lshlrev_b32_e32 v249, 4, v248\n" \
"v_mfma_f32_16x16x32_bf16 a[212:215], v[100:103], v[156:159], a[212:215]\n" \
"v_lshlrev_b32_e32 v251, 7, v228\n" \
"v_lshlrev_b32_e32 v223, 4, v221\n" \
"v_mfma_f32_16x16x32_bf16 a[244:247], v[108:111], v[156:159], 0\n" \
"v_add_u32_e32 v243, v234, v244\n" \
"v_lshlrev_b32_e32 v246, 1, v245\n" \
"v_mfma_f32_16x16x32_bf16 a[244:247], v[112:115], v[160:163], a[244:247]\n" \
"s_cmp_eq_u32 %[w], 0\n" \
"s_cselect_b32 s58, s94, 0x40000000\n" \
"v_mfma_f32_16x16x32_bf16 a[152:155], v[88:91], v[168:171], 0\n" \
"s_mul_i32 s101, 36, s96\n" \
"s_add_u32 s98, s89, %[w]\n" \
"v_mfma_f32_16x16x32_bf16 a[152:155], v[84:87], v[164:167], a[152:155]\n" \
"v_add_u32_e32 v250, v241, v249\n" \
"v_add_u32_e64 v222, v251, v223\n" \
"v_mfma_f32_16x16x32_bf16 a[184:187], v[92:95], v[164:167], 0\n" \
"v_add_u32_e32 v219, v243, v246\n" \
"v_add_u32_e32 v248, s101, v212\n" \
"v_mfma_f32_16x16x32_bf16 a[184:187], v[96:99], v[168:171], a[184:187]\n" \
"s_mul_i32 s97, 18, s98\n" \
"v_add_u32_e32 v196, s95, v250\n" \
"v_mfma_f32_16x16x32_bf16 a[216:219], v[104:107], v[168:171], 0\n" \
"v_add_u32_e32 v224, s95, v222\n" \
"v_add_u32_e32 v247, s95, v219\n" \
"v_mfma_f32_16x16x32_bf16 a[216:219], v[100:103], v[164:167], a[216:219]\n" \
"v_lshrrev_b32_e32 v241, 3, v248\n" \
"s_add_u32 s94, 0xffffffdc, s97\n" \
"v_mfma_f32_16x16x32_bf16 a[248:251], v[108:111], v[164:167], 0\n" \
"v_min_u32_e32 v213, 53, v212\n" \
"s_lshl_b32 s95, s100, 3\n" \
"v_mfma_f32_16x16x32_bf16 a[248:251], v[112:115], v[168:171], a[248:251]\n" \
"v_lshlrev_b32_e32 v249, 6, v241\n" \
"v_add_u32_e64 v215, s94, v213\n" \
"v_mfma_f32_16x16x32_bf16 a[156:159], v[88:91], v[176:179], 0\n" \
"s_and_b32 s97, 7, %[b]\n" \
"s_lshr_b32 s99, %[b], 3\n" \
"v_mfma_f32_16x16x32_bf16 a[156:159], v[84:87], v[172:175], a[156:159]\n" \
"v_add_u32_e32 v250, s95, v249\n" \
"s_lshr_b32 s94, %[b], 5\n" \
"v_mfma_f32_16x16x32_bf16 a[188:191], v[92:95], v[172:175], 0\n" \
"s_lshl_b32 s98, s97, 2\n" \
"s_and_b32 s100, 3, s99\n" \
"v_mfma_f32_16x16x32_bf16 a[188:191], v[96:99], v[176:179], a[188:191]\n" \
"s_mul_i32 s95, 0xa00, s94\n" \
"s_add_u32 s101, s98, s100\n" \
"v_mfma_f32_16x16x32_bf16 a[220:223], v[104:107], v[176:179], 0\n" \
"s_add_u32 s96, 0x5bc1000, s95\n" \
"s_lshl_b32 s88, s101, 2\n" \
"v_mfma_f32_16x16x32_bf16 a[220:223], v[100:103], v[172:175], a[220:223]\n" \
"s_add_u32 s89, s96, s88\n" \
"s_lshl_b32 s99, s94, 3\n" \
"v_mfma_f32_16x16x32_bf16 a[252:255], v[108:111], v[172:175], 0\n" \
"s_cmp_eq_u32 %[w], 0\n" \
"s_cselect_b32 s95, s89, 0x40000000\n" \
"v_mfma_f32_16x16x32_bf16 a[252:255], v[112:115], v[176:179], a[252:255]\n" \
"s_add_u32 s88, s99, s97\n" \
"v_writelane_b32 v11, s95, 0\n" \
"v_mfma_f32_16x16x32_bf16 a[128:131], v[120:123], v[88:91], 0\n" \
"s_lshl_b32 s89, s88, 10\n" \
"s_add_u32 s95, 0x5c00000, s89\n" \
"v_mfma_f32_16x16x32_bf16 a[128:131], v[116:119], v[84:87], a[128:131]\n" \
"s_lshl_b32 s99, %[w], 8\n" \
"s_add_u32 s88, s95, s99\n" \
"v_mfma_f32_16x16x32_bf16 a[160:163], v[116:119], v[92:95], 0\n" \
"v_add_u32_e32 v226, 0x24400, v20\n" \
"v_cmp_gt_u32_e32 vcc, 0x1b0, v225\n" \
"v_mfma_f32_16x16x32_bf16 a[160:163], v[120:123], v[96:99], a[160:163]\n" \
"s_or_b32 s89, 0x200, s88\n" \
"s_lshl_b32 s95, s100, 2\n" \
"v_mfma_f32_16x16x32_bf16 a[192:195], v[120:123], v[104:107], 0\n" \
"v_cndmask_b32_e32 v197, v226, v224, vcc\n" \
"v_writelane_b32 v11, s89, 1\n" \
"v_mfma_f32_16x16x32_bf16 a[192:195], v[116:119], v[100:103], a[192:195]\n" \
"s_add_u32 s99, s88, s95\n" \
"v_cmp_gt_u32_e32 vcc, 18, v212\n" \
"v_mfma_f32_16x16x32_bf16 a[224:227], v[116:119], v[108:111], 0\n" \
"v_writelane_b32 v11, s99, 2\n" \
"s_add_u32 s89, 0x80, s88\n" \
"v_mfma_f32_16x16x32_bf16 a[224:227], v[120:123], v[112:115], a[224:227]\n" \
"v_cndmask_b32_e32 v198, v226, v247, vcc\n" \
"v_and_b32_e32 v214, 7, v248\n" \
"v_mfma_f32_16x16x32_bf16 a[132:135], v[128:131], v[88:91], 0\n" \
"s_add_u32 s62, s22, 0x100\n" \
"s_and_b32 s62, s62, 0x3ff\n" \
"v_mfma_f32_16x16x32_bf16 a[132:135], v[124:127], v[84:87], a[132:135]\n" \
"s_lshl_b32 s63, s62, 12\n" \
"v_writelane_b32 v11, s89, 3\n" \
"v_mfma_f32_16x16x32_bf16 a[164:167], v[124:127], v[92:95], 0\n" \
"s_lshl_b32 s88, s101, 8\n" \
"v_add_u32_e64 v228, v250, v214\n" \
"v_mfma_f32_16x16x32_bf16 a[164:167], v[128:131], v[96:99], a[164:167]\n" \
"v_cmp_gt_u32_e32 vcc, 36, v212\n" \
"s_add_u32 s95, 0x80, s99\n" \
"v_mfma_f32_16x16x32_bf16 a[196:199], v[128:131], v[104:107], 0\n" \
"s_add_u32 s39, 0x5be8000, s88\n" \
"s_lshl_b32 s89, s94, 2\n" \
"v_mfma_f32_16x16x32_bf16 a[196:199], v[124:127], v[100:103], a[196:199]\n" \
"v_cndmask_b32_e32 v218, v215, v228, vcc\n" \
"s_add_u32 s99, s39, s89\n" \
"v_mfma_f32_16x16x32_bf16 a[228:231], v[124:127], v[108:111], 0\n" \
"v_lshrrev_b32_e32 v212, 4, v20\n" \
"v_writelane_b32 v11, s95, 4\n" \
"v_mfma_f32_16x16x32_bf16 a[228:231], v[128:131], v[112:115], a[228:231]\n" \
"s_cmp_eq_u32 %[w], 3\n" \
"s_cselect_b32 s38, s99, 0x40000000\n" \
"s_waitcnt vmcnt(0)\n" \
"s_sleep 18\n" \
"s_barrier\n" \
"v_mfma_f32_16x16x32_bf16 a[136:139], v[136:139], v[88:91], 0\n" \
"ds_read_b64_tr_b16 v[152:153], v32 offset:57344\n" \
"ds_read_b64_tr_b16 v[154:155], v32 offset:58368\n" \
"v_mfma_f32_16x16x32_bf16 a[136:139], v[132:135], v[84:87], a[136:139]\n" \
"ds_read_b128 v[56:59], v29 offset:49152\n" \
"s_add_u32 m0, %[mA], 0x0\n" \
"v_mfma_f32_16x16x32_bf16 a[168:171], v[132:135], v[92:95], 0\n" \
"s_add_u32 s85, s62, 0x0\n" \
"ds_read_b64_tr_b16 v[148:149], v32 offset:49152\n" \
"v_mfma_f32_16x16x32_bf16 a[168:171], v[136:139], v[96:99], a[168:171]\n" \
"buffer_load_dwordx4 v22, s[64:67], s85 offen lds\n" \
"ds_read_b64_tr_b16 v[150:151], v32 offset:50176\n" \
"ds_read_b128 v[52:55], v28 offset:49152\n" \
"v_mfma_f32_16x16x32_bf16 a[200:203], v[136:139], v[104:107], 0\n" \
"ds_read_b128 v[60:63], v28 offset:51200\n" \
"L_pf0:\n" \
"s_cbranch_execz L_pf4\n" \
"s_nop 0\n" \
"ds_read_b128 v[64:67], v29 offset:51200\n" \
"v_mfma_f32_16x16x32_bf16 a[200:203], v[132:135], v[100:103], a[200:203]\n" \
"s_add_u32 m0, %[mA], 0x400\n" \
"s_add_u32 s84, s62, 0x2000\n" \
"v_mfma_f32_16x16x32_bf16 a[232:235], v[132:135], v[108:111], 0\n" \
"buffer_load_dwordx4 v23, s[64:67], s84 offen lds\n" \
"ds_read_b128 v[72:75], v29 offset:53248\n" \
"v_mfma_f32_16x16x32_bf16 a[232:235], v[136:139], v[112:115], a[232:235]\n" \
"ds_read_b128 v[68:71], v28 offset:53248\n" \
"ds_read_b128 v[76:79], v28 offset:55296\n" \
"v_mfma_f32_16x16x32_bf16 a[140:143], v[144:147], v[88:91], 0\n" \
"ds_read_b128 v[80:83], v29 offset:55296\n" \
"s_add_u32 m0, %[mB], 0x0\n" \
"v_mfma_f32_16x16x32_bf16 a[140:143], v[140:143], v[84:87], a[140:143]\n" \
"s_add_u32 s85, s63, 0x0\n" \
"ds_read_b64_tr_b16 v[160:161], v33 offset:57344\n" \
"v_mfma_f32_16x16x32_bf16 a[172:175], v[140:143], v[92:95], 0\n" \
"buffer_load_dwordx4 v26, s[68:71], s85 offen lds\n" \
"ds_read_b64_tr_b16 v[162:163], v33 offset:58368\n" \
"v_mfma_f32_16x16x32_bf16 a[172:175], v[144:147], v[96:99], a[172:175]\n" \
"ds_read_b64_tr_b16 v[156:157], v33 offset:49152\n" \
"ds_read_b64_tr_b16 v[158:159], v33 offset:50176\n" \
"v_mfma_f32_16x16x32_bf16 a[204:207], v[144:147], v[104:107], 0\n" \
"s_add_u32 m0, %[mB], 0x4000\n" \
"s_add_u32 s84, s63, 0x0\n" \
"v_mfma_f32_16x16x32_bf16 a[204:207], v[140:143], v[100:103], a[204:207]\n" \
"buffer_load_dwordx4 v26, s[72:75], s84 offen lds\n" \
"v_mfma_f32_16x16x32_bf16 a[236:239], v[140:143], v[108:111], 0\n" \
"s_add_u32 m0, %[mA], 0x800\n" \
"s_add_u32 s85, s62, 0x4000\n" \
"v_mfma_f32_16x16x32_bf16 a[236:239], v[144:147], v[112:115], a[236:239]\n" \
"ds_read_b64_tr_b16 v[168:169], v34 offset:57344\n" \
"s_waitcnt lgkmcnt(13)\n" \
"v_mfma_f32_16x16x32_bf16 a[16:19], v[56:59], v[152:155], a[16:19]\n" \
"ds_read_b64_tr_b16 v[170:171], v34 offset:58368\n" \
"s_waitcnt lgkmcnt(12)\n" \
"v_mfma_f32_16x16x32_bf16 a[16:19], v[52:55], v[148:151], a[16:19]\n" \
"ds_read_b64_tr_b16 v[164:165], v34 offset:49152\n" \
"s_waitcnt lgkmcnt(12)\n" \
"v_mfma_f32_16x16x32_bf16 a[48:51], v[60:63], v[148:151], a[48:51]\n" \
"ds_read_b64_tr_b16 v[166:167], v34 offset:50176\n" \
"buffer_load_dwordx4 v22, s[64:67], s85 offen lds\n" \
"s_waitcnt lgkmcnt(12)\n" \
"v_mfma_f32_16x16x32_bf16 a[48:51], v[64:67], v[152:155], a[48:51]\n" \
"s_add_u32 m0, %[mA], 0xc00\n" \
"s_waitcnt lgkmcnt(11)\n" \
"v_mfma_f32_16x16x32_bf16 a[80:83], v[72:75], v[152:155], a[80:83]\n" \
"s_add_u32 s84, s62, 0x6000\n" \
"s_waitcnt lgkmcnt(10)\n" \
"v_mfma_f32_16x16x32_bf16 a[80:83], v[68:71], v[148:151], a[80:83]\n" \
"s_add_u32 s85, s63, 0x8000\n" \
"buffer_load_dwordx4 v23, s[64:67], s84 offen lds\n" \
"s_waitcnt lgkmcnt(9)\n" \
"v_mfma_f32_16x16x32_bf16 a[112:115], v[76:79], v[148:151], a[112:115]\n" \
"s_add_u32 m0, %[mB], 0x400\n" \
"s_waitcnt lgkmcnt(8)\n" \
"v_mfma_f32_16x16x32_bf16 a[112:115], v[80:83], v[152:155], a[112:115]\n" \
"ds_read_b64_tr_b16 v[176:177], v35 offset:57344\n" \
"s_waitcnt lgkmcnt(7)\n" \
"v_mfma_f32_16x16x32_bf16 a[20:23], v[56:59], v[160:163], a[20:23]\n" \
"ds_read_b64_tr_b16 v[178:179], v35 offset:58368\n" \
"s_waitcnt lgkmcnt(6)\n" \
"s_nop 0\n" \
"v_mfma_f32_16x16x32_bf16 a[20:23], v[52:55], v[156:159], a[20:23]\n" \
"buffer_load_dwordx4 v26, s[68:71], s85 offen lds\n" \
"v_mfma_f32_16x16x32_bf16 a[52:55], v[60:63], v[156:159], a[52:55]\n" \
"ds_read_b64_tr_b16 v[172:173], v35 offset:49152\n" \
"ds_read_b64_tr_b16 v[174:175], v35 offset:50176\n" \
"v_mfma_f32_16x16x32_bf16 a[52:55], v[64:67], v[160:163], a[52:55]\n" \
"s_add_u32 m0, %[mB], 0x4400\n" \
"s_add_u32 s84, s63, 0x8000\n" \
"v_mfma_f32_16x16x32_bf16 a[84:87], v[72:75], v[160:163], a[84:87]\n" \
"buffer_load_dwordx4 v26, s[72:75], s84 offen lds\n" \
"v_mfma_f32_16x16x32_bf16 a[84:87], v[68:71], v[156:159], a[84:87]\n" \
"s_add_u32 m0, %[mA], 0x1000\n" \
"s_add_u32 s85, s62, 0x8000\n" \
"v_mfma_f32_16x16x32_bf16 a[116:119], v[76:79], v[156:159], a[116:119]\n" \
"ds_read_b64_tr_b16 v[120:121], v32 offset:40960\n" \
"ds_read_b64_tr_b16 v[122:123], v32 offset:41984\n" \
"v_mfma_f32_16x16x32_bf16 a[116:119], v[80:83], v[160:163], a[116:119]\n" \
"ds_read_b64_tr_b16 v[116:117], v32 offset:32768\n" \
"s_waitcnt lgkmcnt(7)\n" \
"s_nop 0\n" \
"v_mfma_f32_16x16x32_bf16 a[24:27], v[56:59], v[168:171], a[24:27]\n" \
"buffer_load_dwordx4 v22, s[64:67], s85 offen lds\n" \
"v_mfma_f32_16x16x32_bf16 a[24:27], v[52:55], v[164:167], a[24:27]\n" \
"ds_read_b64_tr_b16 v[118:119], v32 offset:33792\n" \
"s_add_u32 m0, %[mA], 0x1400\n" \
"v_mfma_f32_16x16x32_bf16 a[56:59], v[60:63], v[164:167], a[56:59]\n" \
"s_add_u32 s84, s62, 0xa000\n" \
"ds_read_b64_tr_b16 v[128:129], v33 offset:40960\n" \
"v_mfma_f32_16x16x32_bf16 a[56:59], v[64:67], v[168:171], a[56:59]\n" \
"buffer_load_dwordx4 v23, s[64:67], s84 offen lds\n" \
"v_mfma_f32_16x16x32_bf16 a[88:91], v[72:75], v[168:171], a[88:91]\n" \
"s_add_u32 m0, %[mB], 0x800\n" \
"s_add_u32 s85, s63, 0x10000\n" \
"v_mfma_f32_16x16x32_bf16 a[88:91], v[68:71], v[164:167], a[88:91]\n" \
"ds_read_b64_tr_b16 v[130:131], v33 offset:41984\n" \
"ds_read_b64_tr_b16 v[124:125], v33 offset:32768\n" \
"v_mfma_f32_16x16x32_bf16 a[120:123], v[76:79], v[164:167], a[120:123]\n" \
"buffer_load_dwordx4 v27, s[68:71], s85 offen lds\n" \
"v_mfma_f32_16x16x32_bf16 a[120:123], v[80:83], v[168:171], a[120:123]\n" \
"ds_read_b64_tr_b16 v[126:127], v33 offset:33792\n" \
"s_waitcnt lgkmcnt(10)\n" \
"v_mfma_f32_16x16x32_bf16 a[28:31], v[56:59], v[176:179], a[28:31]\n" \
"s_add_u32 m0, %[mB], 0x4800\n" \
"s_waitcnt lgkmcnt(8)\n" \
"v_mfma_f32_16x16x32_bf16 a[28:31], v[52:55], v[172:175], a[28:31]\n" \
"s_add_u32 s84, s63, 0x10000\n" \
"ds_read_b64_tr_b16 v[136:137], v34 offset:40960\n" \
"v_mfma_f32_16x16x32_bf16 a[60:63], v[60:63], v[172:175], a[60:63]\n" \
"buffer_load_dwordx4 v27, s[72:75], s84 offen lds\n" \
"v_mfma_f32_16x16x32_bf16 a[60:63], v[64:67], v[176:179], a[60:63]\n" \
"ds_read_b64_tr_b16 v[138:139], v34 offset:41984\n" \
"ds_read_b64_tr_b16 v[132:133], v34 offset:32768\n" \
"v_mfma_f32_16x16x32_bf16 a[92:95], v[72:75], v[176:179], a[92:95]\n" \
"s_add_u32 m0, %[mA], 0x1800\n" \
"s_add_u32 s85, s62, 0xc000\n" \
"v_mfma_f32_16x16x32_bf16 a[92:95], v[68:71], v[172:175], a[92:95]\n" \
"buffer_load_dwordx4 v22, s[64:67], s85 offen lds\n" \
"v_mfma_f32_16x16x32_bf16 a[124:127], v[76:79], v[172:175], a[124:127]\n" \
"ds_read_b64_tr_b16 v[134:135], v34 offset:33792\n" \
"ds_read_b64_tr_b16 v[144:145], v35 offset:40960\n" \
"v_mfma_f32_16x16x32_bf16 a[124:127], v[80:83], v[176:179], a[124:127]\n" \
"s_add_u32 m0, %[mA], 0x1c00\n" \
"s_waitcnt lgkmcnt(9)\n" \
"s_nop 0\n" \
"v_mfma_f32_16x16x32_bf16 a[0:3], v[120:123], v[56:59], a[0:3]\n" \
"s_add_u32 s84, s62, 0xe000\n" \
"ds_read_b64_tr_b16 v[146:147], v35 offset:41984\n" \
"v_mfma_f32_16x16x32_bf16 a[0:3], v[116:119], v[52:55], a[0:3]\n" \
"buffer_load_dwordx4 v23, s[64:67], s84 offen lds\n" \
"v_mfma_f32_16x16x32_bf16 a[32:35], v[116:119], v[60:63], a[32:35]\n" \
"ds_read_b64_tr_b16 v[140:141], v35 offset:32768\n" \
"ds_read_b64_tr_b16 v[142:143], v35 offset:33792\n" \
"v_mfma_f32_16x16x32_bf16 a[32:35], v[120:123], v[64:67], a[32:35]\n" \
"s_add_u32 m0, %[mB], 0xc00\n" \
"s_add_u32 s85, s63, 0x18000\n" \
"v_mfma_f32_16x16x32_bf16 a[64:67], v[120:123], v[72:75], a[64:67]\n" \
"buffer_load_dwordx4 v27, s[68:71], s85 offen lds\n" \
"v_mfma_f32_16x16x32_bf16 a[64:67], v[116:119], v[68:71], a[64:67]\n" \
"ds_read_b128 v[88:91], v29 offset:32768\n" \
"ds_read_b128 v[84:87], v28 offset:32768\n" \
"v_mfma_f32_16x16x32_bf16 a[96:99], v[116:119], v[76:79], a[96:99]\n" \
"ds_read_b128 v[92:95], v28 offset:34816\n" \
"ds_read_b128 v[96:99], v29 offset:34816\n" \
"v_mfma_f32_16x16x32_bf16 a[96:99], v[120:123], v[80:83], a[96:99]\n" \
"s_add_u32 m0, %[mB], 0x4c00\n" \
"s_waitcnt lgkmcnt(11)\n" \
"v_mfma_f32_16x16x32_bf16 a[4:7], v[128:131], v[56:59], a[4:7]\n" \
"s_add_u32 s84, s63, 0x18000\n" \
"ds_read_b128 v[104:107], v29 offset:36864\n" \
"v_mfma_f32_16x16x32_bf16 a[4:7], v[124:127], v[52:55], a[4:7]\n" \
"buffer_load_dwordx4 v27, s[72:75], s84 offen lds\n" \
"v_mfma_f32_16x16x32_bf16 a[36:39], v[124:127], v[60:63], a[36:39]\n" \
"ds_read_b128 v[100:103], v28 offset:36864\n" \
"ds_read_b128 v[108:111], v28 offset:38912\n" \
"v_mfma_f32_16x16x32_bf16 a[36:39], v[128:131], v[64:67], a[36:39]\n" \
"ds_read_b128 v[112:115], v29 offset:38912\n" \
"v_lshlrev_b32_e32 v213, 2, v212\n" \
"v_mfma_f32_16x16x32_bf16 a[68:71], v[128:131], v[72:75], a[68:71]\n" \
"v_and_b32_e32 v215, 7, v212\n" \
"s_add_u32 s101, s89, s100\n" \
"v_mfma_f32_16x16x32_bf16 a[68:71], v[124:127], v[68:71], a[68:71]\n" \
"v_add_u32_e64 v214, s96, v213\n" \
"s_add_u32 s95, 0x5be8020, s88\n" \
"v_mfma_f32_16x16x32_bf16 a[100:103], v[124:127], v[76:79], a[100:103]\n" \
"v_lshlrev_b32_e32 v216, 2, v215\n" \
"s_mul_i32 s94, 0x90000, s101\n" \
"v_mfma_f32_16x16x32_bf16 a[100:103], v[128:131], v[80:83], a[100:103]\n" \
"v_add_u32_e32 v217, s95, v216\n" \
"s_waitcnt lgkmcnt(10)\n" \
"v_mfma_f32_16x16x32_bf16 a[8:11], v[136:139], v[56:59], a[8:11]\n" \
"s_add_u32 s99, 0x4240000, s94\n" \
"s_lshl_b32 s96, %[w], 14\n" \
"v_mfma_f32_16x16x32_bf16 a[8:11], v[132:135], v[52:55], a[8:11]\n" \
"s_add_u32 s88, s99, s96\n" \
"s_lshl_b32 s95, s97, 16\n" \
"v_mfma_f32_16x16x32_bf16 a[40:43], v[132:135], v[60:63], a[40:43]\n" \
"v_writelane_b32 v11, s39, 52\n" \
"s_add_u32 s89, s88, s95\n" \
"v_mfma_f32_16x16x32_bf16 a[40:43], v[136:139], v[64:67], a[40:43]\n" \
"s_lshl_b32 s101, s97, 11\n" \
"v_writelane_b32 v11, s89, 37\n" \
"v_mfma_f32_16x16x32_bf16 a[72:75], v[136:139], v[72:75], a[72:75]\n" \
"s_add_u32 s100, 0x3800, s89\n" \
"s_add_u32 s94, s88, s101\n" \
"v_mfma_f32_16x16x32_bf16 a[72:75], v[132:135], v[68:71], a[72:75]\n" \
"v_writelane_b32 v11, s100, 51\n" \
"s_add_u32 s99, 0x80000, s94\n" \
"v_mfma_f32_16x16x32_bf16 a[104:107], v[132:135], v[76:79], a[104:107]\n" \
"s_lshl_b32 s96, %[w], 17\n" \
"v_writelane_b32 v11, s99, 49\n" \
"v_mfma_f32_16x16x32_bf16 a[104:107], v[136:139], v[80:83], a[104:107]\n" \
"s_add_u32 s95, s88, s96\n" \
"s_and_b32 s94, 1, s97\n" \
"v_mfma_f32_16x16x32_bf16 a[12:15], v[144:147], v[56:59], a[12:15]\n" \
"s_add_u32 s89, s95, s98\n" \
"s_waitcnt lgkmcnt(2)\n" \
"v_mfma_f32_16x16x32_bf16 a[12:15], v[140:143], v[52:55], a[12:15]\n" \
"s_lshl_b32 s99, s94, 10\n" \
"s_add_u32 s100, 0x3840, s89\n" \
"v_mfma_f32_16x16x32_bf16 a[44:47], v[140:143], v[60:63], a[44:47]\n" \
"s_add_u32 s96, s88, s99\n" \
"v_writelane_b32 v11, s100, 5\n" \
"v_mfma_f32_16x16x32_bf16 a[44:47], v[144:147], v[64:67], a[44:47]\n" \
"s_lshr_b32 s101, s97, 1\n" \
"s_add_u32 s95, 0x80000, s96\n" \
"v_mfma_f32_16x16x32_bf16 a[76:79], v[144:147], v[72:75], a[76:79]\n" \
"s_add_u32 s89, 1, s97\n" \
"s_and_b32 s100, 7, s89\n" \
"v_mfma_f32_16x16x32_bf16 a[76:79], v[140:143], v[68:71], a[76:79]\n" \
"s_cmp_eq_u32 %[w], s101\n" \
"s_cselect_b32 s98, s95, 0x40000000\n" \
"v_mfma_f32_16x16x32_bf16 a[108:111], v[140:143], v[76:79], a[108:111]\n" \
"s_lshl_b32 s94, s100, 16\n" \
"s_add_u32 s96, 2, s97\n" \
"v_mfma_f32_16x16x32_bf16 a[108:111], v[144:147], v[80:83], a[108:111]\n" \
"v_writelane_b32 v11, s98, 38\n" \
"s_add_u32 s99, s88, s94\n" \
"v_mfma_f32_16x16x32_bf16 a[128:131], v[120:123], v[88:91], a[128:131]\n" \
"s_and_b32 s101, 7, s96\n" \
"s_add_u32 s100, 3, s97\n" \
"v_mfma_f32_16x16x32_bf16 a[128:131], v[116:119], v[84:87], a[128:131]\n" \
"v_writelane_b32 v11, s99, 42\n" \
"s_lshl_b32 s95, s101, 16\n" \
"v_mfma_f32_16x16x32_bf16 a[160:163], v[116:119], v[92:95], a[160:163]\n" \
"s_and_b32 s94, 7, s100\n" \
"s_add_u32 s98, s88, s95\n" \
"v_mfma_f32_16x16x32_bf16 a[160:163], v[120:123], v[96:99], a[160:163]\n" \
"s_lshl_b32 s99, s94, 16\n" \
"s_add_u32 s89, 0x800, s98\n" \
"v_mfma_f32_16x16x32_bf16 a[192:195], v[120:123], v[104:107], a[192:195]\n" \
"s_add_u32 s96, s88, s99\n" \
"s_add_u32 s95, 4, s97\n" \
"v_mfma_f32_16x16x32_bf16 a[192:195], v[116:119], v[100:103], a[192:195]\n" \
"v_writelane_b32 v11, s89, 43\n" \
"s_waitcnt lgkmcnt(0)\n" \
"v_mfma_f32_16x16x32_bf16 a[224:227], v[116:119], v[108:111], a[224:227]\n" \
"s_add_u32 s101, 0x1000, s96\n" \
"s_and_b32 s98, 7, s95\n" \
"v_mfma_f32_16x16x32_bf16 a[224:227], v[120:123], v[112:115], a[224:227]\n" \
"s_add_u32 s99, 5, s97\n" \
"v_writelane_b32 v11, s101, 44\n" \
"v_mfma_f32_16x16x32_bf16 a[132:135], v[128:131], v[88:91], a[132:135]\n" \
"s_lshl_b32 s89, s98, 16\n" \
"s_and_b32 s96, 7, s99\n" \
"v_mfma_f32_16x16x32_bf16 a[132:135], v[124:127], v[84:87], a[132:135]\n" \
"s_add_u32 s100, s88, s89\n" \
"s_lshl_b32 s101, s96, 16\n" \
"v_mfma_f32_16x16x32_bf16 a[164:167], v[124:127], v[92:95], a[164:167]\n" \
"s_add_u32 s94, 0x1800, s100\n" \
"s_add_u32 s95, s88, s101\n" \
"v_mfma_f32_16x16x32_bf16 a[164:167], v[128:131], v[96:99], a[164:167]\n" \
"s_add_u32 s89, 6, s97\n" \
"v_writelane_b32 v11, s94, 45\n" \
"v_mfma_f32_16x16x32_bf16 a[196:199], v[128:131], v[104:107], a[196:199]\n" \
"s_add_u32 s98, 0x2000, s95\n" \
"s_and_b32 s100, 7, s89\n" \
"v_mfma_f32_16x16x32_bf16 a[196:199], v[124:127], v[100:103], a[196:199]\n" \
"s_add_u32 s101, 7, s97\n" \
"v_writelane_b32 v11, s98, 46\n" \
"v_mfma_f32_16x16x32_bf16 a[228:231], v[124:127], v[108:111], a[228:231]\n" \
"s_lshl_b32 s94, s100, 16\n" \
"s_and_b32 s95, 7, s101\n" \
"v_mfma_f32_16x16x32_bf16 a[228:231], v[128:131], v[112:115], a[228:231]\n" \
"s_add_u32 s99, s88, s94\n" \
"s_lshl_b32 s98, s95, 16\n" \
"v_mfma_f32_16x16x32_bf16 a[136:139], v[136:139], v[88:91], a[136:139]\n" \
"s_add_u32 s96, 0x2800, s99\n" \
"s_add_u32 s89, s88, s98\n" \
"v_mfma_f32_16x16x32_bf16 a[136:139], v[132:135], v[84:87], a[136:139]\n" \
"v_writelane_b32 v11, s96, 47\n" \
"s_add_u32 s100, 0x3000, s89\n" \
"v_mfma_f32_16x16x32_bf16 a[168:171], v[132:135], v[92:95], a[168:171]\n" \
"v_writelane_b32 v11, s100, 48\n" \
"v_lshlrev_b32_e32 v188, 7, v218\n" \
"v_mfma_f32_16x16x32_bf16 a[168:171], v[136:139], v[96:99], a[168:171]\n" \
"v_readfirstlane_b32 %[tag], v180\n" \
"s_lshr_b32 s94, %[b], 5\n" \
"v_mfma_f32_16x16x32_bf16 a[200:203], v[136:139], v[104:107], a[200:203]\n" \
"s_add_u32 %[tag], %[tag], 0x9E3779B1\n" \
"v_mov_b32_e32 v45, %[tag]\n" \
"v_mfma_f32_16x16x32_bf16 a[200:203], v[132:135], v[100:103], a[200:203]\n" \
"s_sub_u32 s95, 15, s94\n" \
"s_and_b32 s100, 1, %[w]\n" \
"v_mfma_f32_16x16x32_bf16 a[232:235], v[132:135], v[108:111], a[232:235]\n" \
"s_cmp_lt_u32 %[w], 2\n" \
"s_cselect_b32 s96, s95, s94\n" \
"v_mfma_f32_16x16x32_bf16 a[232:235], v[136:139], v[112:115], a[232:235]\n" \
"s_lshl_b32 s97, s96, 7\n" \
"s_add_u32 s98, 0xfffffff0, s97\n" \
"v_mfma_f32_16x16x32_bf16 a[140:143], v[144:147], v[88:91], a[140:143]\n" \
"s_add_u32 s62, s22, 0x180\n" \
"s_and_b32 s62, s62, 0x3ff\n" \
"v_mfma_f32_16x16x32_bf16 a[140:143], v[140:143], v[84:87], a[140:143]\n" \
"s_lshl_b32 s63, s62, 12\n" \
"v_cmp_gt_u32_e32 vcc, 32, v212\n" \
"v_mfma_f32_16x16x32_bf16 a[172:175], v[140:143], v[92:95], a[172:175]\n" \
"s_cmp_eq_u32 s97, 0\n" \
"s_cselect_b32 s99, 0, s98\n" \
"v_mfma_f32_16x16x32_bf16 a[172:175], v[144:147], v[96:99], a[172:175]\n" \
"s_lshl_b32 s101, s100, 3\n" \
"s_add_u32 s88, s99, s101\n" \
"v_mfma_f32_16x16x32_bf16 a[204:207], v[144:147], v[104:107], a[204:207]\n" \
"v_lshrrev_b32_e32 v212, 4, v20\n" \
"v_cndmask_b32_e32 v17, v217, v214, vcc\n" \
"v_mfma_f32_16x16x32_bf16 a[204:207], v[140:143], v[100:103], a[204:207]\n" \
"v_lshrrev_b32_e32 v213, 3, v212\n" \
"s_lshr_b32 s99, %[b], 3\n" \
"v_mfma_f32_16x16x32_bf16 a[236:239], v[140:143], v[108:111], a[236:239]\n" \
"v_add_u32_e32 v214, s88, v213\n" \
"s_lshl_b32 s96, s100, 6\n" \
"v_mfma_f32_16x16x32_bf16 a[236:239], v[144:147], v[112:115], a[236:239]\n" \
"s_and_b32 s101, 3, s99\n" \
"s_add_u32 s98, s97, s96\n" \
"s_waitcnt vmcnt(0)\n" \
"s_sleep 4\n" \
"s_barrier\n" \
"v_mfma_f32_16x16x32_bf16 a[144:147], v[88:91], v[152:155], a[144:147]\n" \
"s_add_u32 m0, %[mA], 0x8000\n" \
"s_add_u32 s85, s62, 0x0\n" \
"v_mfma_f32_16x16x32_bf16 a[144:147], v[84:87], v[148:151], a[144:147]\n" \
"buffer_load_dwordx4 v22, s[64:67], s85 offen lds\n" \
"v_mfma_f32_16x16x32_bf16 a[176:179], v[92:95], v[148:151], a[176:179]\n" \
"s_add_u32 m0, %[mA], 0x8400\n" \
"s_add_u32 s84, s62, 0x2000\n" \
"v_mfma_f32_16x16x32_bf16 a[176:179], v[96:99], v[152:155], a[176:179]\n" \
"ds_read_b64_tr_b16 v[120:121], v32 offset:8192\n" \
"ds_read_b64_tr_b16 v[122:123], v32 offset:9216\n" \
"v_mfma_f32_16x16x32_bf16 a[208:211], v[104:107], v[152:155], a[208:211]\n" \
"buffer_load_dwordx4 v23, s[64:67], s84 offen lds\n" \
"v_mfma_f32_16x16x32_bf16 a[208:211], v[100:103], v[148:151], a[208:211]\n" \
"ds_read_b128 v[56:59], v29 offset:16384\n" \
"ds_read_b64_tr_b16 v[116:117], v32 offset:0\n" \
"v_mfma_f32_16x16x32_bf16 a[240:243], v[108:111], v[148:151], a[240:243]\n" \
"s_add_u32 m0, %[mB], 0x8000\n" \
"s_add_u32 s85, s63, 0x0\n" \
"v_mfma_f32_16x16x32_bf16 a[240:243], v[112:115], v[152:155], a[240:243]\n" \
"buffer_load_dwordx4 v26, s[68:71], s85 offen lds\n" \
"v_mfma_f32_16x16x32_bf16 a[148:151], v[88:91], v[160:163], a[148:151]\n" \
"ds_read_b64_tr_b16 v[118:119], v32 offset:1024\n" \
"ds_read_b128 v[52:55], v28 offset:16384\n" \
"v_mfma_f32_16x16x32_bf16 a[148:151], v[84:87], v[156:159], a[148:151]\n" \
"ds_read_b128 v[60:63], v28 offset:18432\n" \
"ds_read_b128 v[64:67], v29 offset:18432\n" \
"v_mfma_f32_16x16x32_bf16 a[180:183], v[92:95], v[156:159], a[180:183]\n" \
"s_add_u32 m0, %[mB], 0xc000\n" \
"s_add_u32 s84, s63, 0x0\n" \
"v_mfma_f32_16x16x32_bf16 a[180:183], v[96:99], v[160:163], a[180:183]\n" \
"buffer_load_dwordx4 v26, s[72:75], s84 offen lds\n" \
"v_mfma_f32_16x16x32_bf16 a[212:215], v[104:107], v[160:163], a[212:215]\n" \
"ds_read_b128 v[72:75], v29 offset:20480\n" \
"ds_read_b128 v[68:71], v28 offset:20480\n" \
"v_mfma_f32_16x16x32_bf16 a[212:215], v[100:103], v[156:159], a[212:215]\n" \
"ds_read_b128 v[76:79], v28 offset:22528\n" \
"ds_read_b128 v[80:83], v29 offset:22528\n" \
"v_mfma_f32_16x16x32_bf16 a[244:247], v[108:111], v[156:159], a[244:247]\n" \
"s_add_u32 m0, %[mA], 0x8800\n" \
"s_add_u32 s85, s62, 0x4000\n" \
"v_mfma_f32_16x16x32_bf16 a[244:247], v[112:115], v[160:163], a[244:247]\n" \
"buffer_load_dwordx4 v22, s[64:67], s85 offen lds\n" \
"v_mfma_f32_16x16x32_bf16 a[152:155], v[88:91], v[168:171], a[152:155]\n" \
"ds_read_b64_tr_b16 v[128:129], v33 offset:8192\n" \
"ds_read_b64_tr_b16 v[130:131], v33 offset:9216\n" \
"v_mfma_f32_16x16x32_bf16 a[152:155], v[84:87], v[164:167], a[152:155]\n" \
"s_add_u32 m0, %[mA], 0x8c00\n" \
"s_add_u32 s84, s62, 0x6000\n" \
"v_mfma_f32_16x16x32_bf16 a[184:187], v[92:95], v[164:167], a[184:187]\n" \
"buffer_load_dwordx4 v23, s[64:67], s84 offen lds\n" \
"v_mfma_f32_16x16x32_bf16 a[184:187], v[96:99], v[168:171], a[184:187]\n" \
"ds_read_b64_tr_b16 v[124:125], v33 offset:0\n" \
"ds_read_b64_tr_b16 v[126:127], v33 offset:1024\n" \
"v_mfma_f32_16x16x32_bf16 a[216:219], v[104:107], v[168:171], a[216:219]\n" \
"ds_read_b64_tr_b16 v[136:137], v34 offset:8192\n" \
"ds_read_b64_tr_b16 v[138:139], v34 offset:9216\n" \
"v_mfma_f32_16x16x32_bf16 a[216:219], v[100:103], v[164:167], a[216:219]\n" \
"s_add_u32 m0, %[mB], 0x8400\n" \
"s_add_u32 s85, s63, 0x8000\n" \
"v_mfma_f32_16x16x32_bf16 a[248:251], v[108:111], v[164:167], a[248:251]\n" \
"buffer_load_dwordx4 v26, s[68:71], s85 offen lds\n" \
"v_mfma_f32_16x16x32_bf16 a[248:251], v[112:115], v[168:171], a[248:251]\n" \
"ds_read_b64_tr_b16 v[132:133], v34 offset:0\n" \
"ds_read_b64_tr_b16 v[134:135], v34 offset:1024\n" \
"v_mfma_f32_16x16x32_bf16 a[156:159], v[88:91], v[176:179], a[156:159]\n" \
"ds_read_b64_tr_b16 v[144:145], v35 offset:8192\n" \
"ds_read_b64_tr_b16 v[146:147], v35 offset:9216\n" \
"v_mfma_f32_16x16x32_bf16 a[156:159], v[84:87], v[172:175], a[156:159]\n" \
"s_add_u32 m0, %[mB], 0xc400\n" \
"s_add_u32 s84, s63, 0x8000\n" \
"v_mfma_f32_16x16x32_bf16 a[188:191], v[92:95], v[172:175], a[188:191]\n" \
"buffer_load_dwordx4 v26, s[72:75], s84 offen lds\n" \
"v_mfma_f32_16x16x32_bf16 a[188:191], v[96:99], v[176:179], a[188:191]\n" \
"ds_read_b64_tr_b16 v[140:141], v35 offset:0\n" \
"ds_read_b64_tr_b16 v[142:143], v35 offset:1024\n" \
"v_mfma_f32_16x16x32_bf16 a[220:223], v[104:107], v[176:179], a[220:223]\n" \
"ds_read_b64_tr_b16 v[152:153], v32 offset:24576\n" \
"ds_read_b64_tr_b16 v[154:155], v32 offset:25600\n" \
"v_mfma_f32_16x16x32_bf16 a[220:223], v[100:103], v[172:175], a[220:223]\n" \
"s_add_u32 m0, %[mA], 0x9000\n" \
"s_add_u32 s85, s62, 0x8000\n" \
"v_mfma_f32_16x16x32_bf16 a[252:255], v[108:111], v[172:175], a[252:255]\n" \
"buffer_load_dwordx4 v22, s[64:67], s85 offen lds\n" \
"v_mfma_f32_16x16x32_bf16 a[252:255], v[112:115], v[176:179], a[252:255]\n" \
"ds_read_b64_tr_b16 v[148:149], v32 offset:16384\n" \
"s_waitcnt lgkmcnt(13)\n" \
"s_nop 0\n" \
"v_mfma_f32_16x16x32_bf16 a[0:3], v[120:123], v[56:59], a[0:3]\n" \
"s_add_u32 m0, %[mA], 0x9400\n" \
"s_add_u32 s84, s62, 0xa000\n" \
"v_mfma_f32_16x16x32_bf16 a[0:3], v[116:119], v[52:55], a[0:3]\n" \
"buffer_load_dwordx4 v23, s[64:67], s84 offen lds\n" \
"v_mfma_f32_16x16x32_bf16 a[32:35], v[116:119], v[60:63], a[32:35]\n" \
"ds_read_b64_tr_b16 v[150:151], v32 offset:17408\n" \
"ds_read_b64_tr_b16 v[160:161], v33 offset:24576\n" \
"v_mfma_f32_16x16x32_bf16 a[32:35], v[120:123], v[64:67], a[32:35]\n" \
"ds_read_b64_tr_b16 v[162:163], v33 offset:25600\n" \
"ds_read_b64_tr_b16 v[156:157], v33 offset:16384\n" \
"v_mfma_f32_16x16x32_bf16 a[64:67], v[120:123], v[72:75], a[64:67]\n" \
"s_add_u32 m0, %[mB], 0x8800\n" \
"s_add_u32 s85, s63, 0x10000\n" \
"v_mfma_f32_16x16x32_bf16 a[64:67], v[116:119], v[68:71], a[64:67]\n" \
"buffer_load_dwordx4 v27, s[68:71], s85 offen lds\n" \
"v_mfma_f32_16x16x32_bf16 a[96:99], v[116:119], v[76:79], a[96:99]\n" \
"ds_read_b64_tr_b16 v[158:159], v33 offset:17408\n" \
"ds_read_b64_tr_b16 v[168:169], v34 offset:24576\n" \
"v_mfma_f32_16x16x32_bf16 a[96:99], v[120:123], v[80:83], a[96:99]\n" \
"ds_read_b64_tr_b16 v[170:171], v34 offset:25600\n" \
"ds_read_b64_tr_b16 v[164:165], v34 offset:16384\n" \
"v_mfma_f32_16x16x32_bf16 a[4:7], v[128:131], v[56:59], a[4:7]\n" \
"s_add_u32 m0, %[mB], 0xc800\n" \
"s_waitcnt lgkmcnt(13)\n" \
"s_nop 0\n" \
"v_mfma_f32_16x16x32_bf16 a[4:7], v[124:127], v[52:55], a[4:7]\n" \
"s_add_u32 s84, s63, 0x10000\n" \
"ds_read_b64_tr_b16 v[166:167], v34 offset:17408\n" \
"v_mfma_f32_16x16x32_bf16 a[36:39], v[124:127], v[60:63], a[36:39]\n" \
"buffer_load_dwordx4 v27, s[72:75], s84 offen lds\n" \
"v_mfma_f32_16x16x32_bf16 a[36:39], v[128:131], v[64:67], a[36:39]\n" \
"ds_read_b64_tr_b16 v[176:177], v35 offset:24576\n" \
"ds_read_b64_tr_b16 v[178:179], v35 offset:25600\n" \
"v_mfma_f32_16x16x32_bf16 a[68:71], v[128:131], v[72:75], a[68:71]\n" \
"s_add_u32 m0, %[mA], 0x9800\n" \
"s_add_u32 s85, s62, 0xc000\n" \
"v_mfma_f32_16x16x32_bf16 a[68:71], v[124:127], v[68:71], a[68:71]\n" \
"buffer_load_dwordx4 v22, s[64:67], s85 offen lds\n" \
"v_mfma_f32_16x16x32_bf16 a[100:103], v[124:127], v[76:79], a[100:103]\n" \
"ds_read_b64_tr_b16 v[172:173], v35 offset:16384\n" \
"ds_read_b64_tr_b16 v[174:175], v35 offset:17408\n" \
"v_mfma_f32_16x16x32_bf16 a[100:103], v[128:131], v[80:83], a[100:103]\n" \
"s_add_u32 m0, %[mA], 0x9c00\n" \
"s_add_u32 s84, s62, 0xe000\n" \
"v_mfma_f32_16x16x32_bf16 a[8:11], v[136:139], v[56:59], a[8:11]\n" \
"buffer_load_dwordx4 v23, s[64:67], s84 offen lds\n" \
"v_mfma_f32_16x16x32_bf16 a[8:11], v[132:135], v[52:55], a[8:11]\n" \
"ds_read_b128 v[88:91], v29 offset:0\n" \
"ds_read_b128 v[84:87], v28 offset:0\n" \
"v_mfma_f32_16x16x32_bf16 a[40:43], v[132:135], v[60:63], a[40:43]\n" \
"ds_read_b128 v[92:95], v28 offset:2048\n" \
"ds_read_b128 v[96:99], v29 offset:2048\n" \
"v_mfma_f32_16x16x32_bf16 a[40:43], v[136:139], v[64:67], a[40:43]\n" \
"s_add_u32 m0, %[mB], 0x8c00\n" \
"s_add_u32 s85, s63, 0x18000\n" \
"v_mfma_f32_16x16x32_bf16 a[72:75], v[136:139], v[72:75], a[72:75]\n" \
"buffer_load_dwordx4 v27, s[68:71], s85 offen lds\n" \
"v_mfma_f32_16x16x32_bf16 a[72:75], v[132:135], v[68:71], a[72:75]\n" \
"ds_read_b128 v[104:107], v29 offset:4096\n" \
"ds_read_b128 v[100:103], v28 offset:4096\n" \
"v_mfma_f32_16x16x32_bf16 a[104:107], v[132:135], v[76:79], a[104:107]\n" \
"ds_read_b128 v[108:111], v28 offset:6144\n" \
"ds_read_b128 v[112:115], v29 offset:6144\n" \
"v_mfma_f32_16x16x32_bf16 a[104:107], v[136:139], v[80:83], a[104:107]\n" \
"s_add_u32 m0, %[mB], 0xcc00\n" \
"s_add_u32 s84, s63, 0x18000\n" \
"v_mfma_f32_16x16x32_bf16 a[12:15], v[144:147], v[56:59], a[12:15]\n" \
"s_add_u32 s88, s101, s94\n" \
"s_waitcnt lgkmcnt(12)\n" \
"v_mfma_f32_16x16x32_bf16 a[12:15], v[140:143], v[52:55], a[12:15]\n" \
"buffer_load_dwordx4 v27, s[72:75], s84 offen lds\n" \
"v_mfma_f32_16x16x32_bf16 a[44:47], v[140:143], v[60:63], a[44:47]\n" \
"v_add_u32_e32 v221, s98, v213\n" \
"s_add_u32 s96, 0xfffffff7, s88\n" \
"v_mfma_f32_16x16x32_bf16 a[44:47], v[144:147], v[64:67], a[44:47]\n" \
"s_cmp_lt_u32 s88, 9\n" \
"s_cselect_b32 s98, s88, s96\n" \
"v_mfma_f32_16x16x32_bf16 a[76:79], v[144:147], v[72:75], a[76:79]\n" \
"buffer_load_dwordx4 v[200:203], v192, s[76:79], s56 offen offset:0\n" \
"v_mfma_f32_16x16x32_bf16 a[76:79], v[140:143], v[68:71], a[76:79]\n" \
"s_lshl_b32 s89, s100, 2\n" \
"s_add_u32 s99, s97, s100\n" \
"v_mfma_f32_16x16x32_bf16 a[108:111], v[140:143], v[76:79], a[108:111]\n" \
"s_lshr_b32 s88, s97, 4\n" \
"s_lshl_b32 s23, s98, 7\n" \
"v_mfma_f32_16x16x32_bf16 a[108:111], v[144:147], v[80:83], a[108:111]\n" \
"s_cmp_eq_u32 s99, 0\n" \
"s_cselect_b32 s49, 0xff800000, 0\n" \
"v_mfma_f32_16x16x32_bf16 a[16:19], v[56:59], v[152:155], a[16:19]\n" \
"s_add_u32 s96, s88, s89\n" \
"v_lshrrev_b32_e32 v217, 4, v212\n" \
"v_mfma_f32_16x16x32_bf16 a[16:19], v[52:55], v[148:151], a[16:19]\n" \
"buffer_load_dwordx4 v[204:207], v193, s[76:79], s56 offen offset:0\n" \
"v_mfma_f32_16x16x32_bf16 a[48:51], v[60:63], v[148:151], a[48:51]\n" \
"s_mul_i32 s98, 0x24800, s96\n" \
"s_and_b32 s97, 7, %[b]\n" \
"v_mfma_f32_16x16x32_bf16 a[48:51], v[64:67], v[152:155], a[48:51]\n" \
"v_and_b32_e32 v216, 7, v212\n" \
"v_add_u32_e32 v218, s89, v217\n" \
"v_mfma_f32_16x16x32_bf16 a[80:83], v[72:75], v[152:155], a[80:83]\n" \
"s_add_u32 s99, 0x3000000, s98\n" \
"s_lshl_b32 s88, s97, 8\n" \
"v_mfma_f32_16x16x32_bf16 a[80:83], v[68:71], v[148:151], a[80:83]\n" \
"v_xor_b32_e32 v219, v216, v218\n" \
"s_add_u32 s89, s99, s88\n" \
"v_mfma_f32_16x16x32_bf16 a[112:115], v[76:79], v[148:151], a[112:115]\n" \
"s_lshl_b32 s96, s101, 2\n" \
"v_mul_u32_u24_e32 v215, 0x480, v214\n" \
"v_mfma_f32_16x16x32_bf16 a[112:115], v[80:83], v[152:155], a[112:115]\n" \
"buffer_load_dword v208, v194, s[76:79], s56 offen offset:32\n" \
"v_mfma_f32_16x16x32_bf16 a[20:23], v[56:59], v[160:163], a[20:23]\n" \
"v_lshlrev_b32_e32 v220, 4, v219\n" \
"v_lshrrev_b32_e32 v229, 2, v212\n" \
"v_mfma_f32_16x16x32_bf16 a[20:23], v[52:55], v[156:159], a[20:23]\n" \
"v_and_b32_e32 v234, 3, v212\n" \
"v_and_b32_e32 v249, 2, v212\n" \
"v_mfma_f32_16x16x32_bf16 a[52:55], v[60:63], v[156:159], a[52:55]\n" \
"s_add_u32 s98, s89, s96\n" \
"v_add_u32_e32 v21, v215, v220\n" \
"v_mfma_f32_16x16x32_bf16 a[52:55], v[64:67], v[160:163], a[52:55]\n" \
"v_add_u32_e64 v225, 4, v217\n" \
"s_add_u32 s51, 0x4000, s98\n" \
"v_mfma_f32_16x16x32_bf16 a[84:87], v[72:75], v[160:163], a[84:87]\n" \
"s_lshl_b32 s88, s97, 2\n" \
"v_lshrrev_b32_e32 v212, 4, v20\n" \
"v_mfma_f32_16x16x32_bf16 a[84:87], v[68:71], v[156:159], a[84:87]\n" \
"buffer_load_dword v209, v194, s[76:79], s56 offen offset:1184\n" \
"v_mfma_f32_16x16x32_bf16 a[116:119], v[76:79], v[156:159], a[116:119]\n" \
"v_xor_b32_e32 v223, v216, v217\n" \
"v_xor_b32_e32 v226, v216, v225\n" \
"v_mfma_f32_16x16x32_bf16 a[116:119], v[80:83], v[160:163], a[116:119]\n" \
"v_mov_b32_e32 v250, 0xeeeeeeee\n" \
"v_mov_b32_e32 v251, 0x44444444\n" \
"v_mfma_f32_16x16x32_bf16 a[24:27], v[56:59], v[168:171], a[24:27]\n" \
"v_cmp_ne_u32_e32 vcc, 0, v249\n" \
"s_add_u32 s89, s88, s101\n" \
"v_mfma_f32_16x16x32_bf16 a[24:27], v[52:55], v[164:167], a[24:27]\n" \
"s_lshl_b32 s98, %[w], 10\n" \
"v_and_b32_e32 v215, 15, v212\n" \
"v_mfma_f32_16x16x32_bf16 a[56:59], v[60:63], v[164:167], a[56:59]\n" \
"v_cndmask_b32_e32 v50, v251, v250, vcc\n" \
"s_lshl_b32 s96, s89, 9\n" \
"v_mfma_f32_16x16x32_bf16 a[56:59], v[64:67], v[168:171], a[56:59]\n" \
"buffer_load_dword v211, v194, s[76:79], s56 offen offset:2336\n" \
"v_mfma_f32_16x16x32_bf16 a[88:91], v[72:75], v[168:171], a[88:91]\n" \
"s_mov_b32 s76, s14\n" \
"s_and_b32 s77, s15, 0xffff\n" \
"v_mfma_f32_16x16x32_bf16 a[88:91], v[68:71], v[164:167], a[88:91]\n" \
"s_mov_b32 s78, 0x480\n" \
"s_add_u32 s56, s57, 0x1000\n" \
"v_mfma_f32_16x16x32_bf16 a[120:123], v[76:79], v[164:167], a[120:123]\n" \
"s_add_u32 s40, 0x14000, s98\n" \
"s_lshl_b32 s97, s89, 20\n" \
"v_mfma_f32_16x16x32_bf16 a[120:123], v[80:83], v[168:171], a[120:123]\n" \
"v_lshrrev_b32_e32 v213, 4, v212\n" \
"s_waitcnt lgkmcnt(0)\n" \
"v_mfma_f32_16x16x32_bf16 a[28:31], v[56:59], v[176:179], a[28:31]\n" \
"v_and_b32_e32 v216, 1, v215\n" \
"v_lshlrev_b32_e32 v228, 3, v217\n" \
"v_mfma_f32_16x16x32_bf16 a[28:31], v[52:55], v[172:175], a[28:31]\n" \
"buffer_load_ushort v210, v195, s[76:79], 0 offen\n" \
"v_mfma_f32_16x16x32_bf16 a[60:63], v[60:63], v[172:175], a[60:63]\n" \
"v_and_b32_e32 v238, 1, v217\n" \
"v_lshlrev_b32_e32 v242, 4, v217\n" \
"v_mfma_f32_16x16x32_bf16 a[60:63], v[64:67], v[176:179], a[60:63]\n" \
"s_add_u32 s50, s99, s96\n" \
"s_add_u32 s88, 0x1000000, s97\n" \
"v_mfma_f32_16x16x32_bf16 a[92:95], v[72:75], v[176:179], a[92:95]\n" \
"s_lshl_b32 s101, %[w], 12\n" \
"s_lshr_b32 s98, %[w], 1\n" \
"v_mfma_f32_16x16x32_bf16 a[92:95], v[68:71], v[172:175], a[92:95]\n" \
"v_lshlrev_b32_e32 v214, 2, v213\n" \
"v_mov_b32_e32 v218, 0x1000c0c\n" \
"v_mfma_f32_16x16x32_bf16 a[124:127], v[76:79], v[172:175], a[124:127]\n" \
"v_mov_b32_e32 v219, 0xc0c0100\n" \
"v_cmp_ne_u32_e32 vcc, 0, v216\n" \
"v_mfma_f32_16x16x32_bf16 a[124:127], v[80:83], v[176:179], a[124:127]\n" \
"v_mul_u32_u24_e32 v222, 0x480, v221\n" \
"s_add_u32 s52, s88, s101\n" \
"v_mfma_f32_16x16x32_bf16 a[144:147], v[88:91], v[152:155], a[144:147]\n" \
"s_lshl_b32 s99, %[w], 11\n" \
"s_lshl_b32 s89, s98, 15\n" \
"L_pf1:\n" \
"s_cbranch_execz L_pf5\n" \
"s_nop 0\n" \
"v_mfma_f32_16x16x32_bf16 a[144:147], v[84:87], v[148:151], a[144:147]\n" \
"v_add_u32_e32 v217, v214, v216\n" \
"v_cndmask_b32_e32 v220, v219, v218, vcc\n" \
"v_mfma_f32_16x16x32_bf16 a[176:179], v[92:95], v[148:151], a[176:179]\n" \
"v_lshlrev_b32_e32 v224, 4, v223\n" \
"s_add_u32 s96, s88, s99\n" \
"v_mfma_f32_16x16x32_bf16 a[176:179], v[96:99], v[152:155], a[176:179]\n" \
"s_add_u32 s97, s88, s89\n" \
"s_lshl_b32 s101, s100, 11\n" \
"v_mfma_f32_16x16x32_bf16 a[208:211], v[104:107], v[152:155], a[208:211]\n" \
"v_mov_b32_e32 v221, 0xc0c0c0c\n" \
"v_cmp_le_u32_e32 vcc, v217, v215\n" \
"v_mfma_f32_16x16x32_bf16 a[208:211], v[100:103], v[148:151], a[208:211]\n" \
"v_add_u32_e32 v24, v222, v224\n" \
"v_lshlrev_b32_e32 v227, 4, v226\n" \
"v_mfma_f32_16x16x32_bf16 a[240:243], v[108:111], v[148:151], a[240:243]\n" \
"s_add_u32 s99, s97, s101\n" \
"s_lshl_b32 s98, s95, 16\n" \
"v_mfma_f32_16x16x32_bf16 a[240:243], v[112:115], v[152:155], a[240:243]\n" \
"v_cndmask_b32_e32 v40, v220, v221, vcc\n" \
"v_add_u32_e64 v25, v222, v227\n" \
"v_mfma_f32_16x16x32_bf16 a[148:151], v[88:91], v[160:163], a[148:151]\n" \
"s_add_u32 s62, s22, 0x200\n" \
"s_and_b32 s62, s62, 0x3ff\n" \
"v_mfma_f32_16x16x32_bf16 a[148:151], v[84:87], v[156:159], a[148:151]\n" \
"s_lshl_b32 s63, s62, 12\n" \
"s_add_u32 s54, 0x10000, s96\n" \
"v_mfma_f32_16x16x32_bf16 a[180:183], v[92:95], v[156:159], a[180:183]\n" \
"s_add_u32 s43, s99, s98\n" \
"s_mul_i32 s88, 0x1800, s100\n" \
"v_mfma_f32_16x16x32_bf16 a[180:183], v[96:99], v[160:163], a[180:183]\n" \
"v_mov_b32_e32 v223, 0x3020c0c\n" \
"v_mov_b32_e32 v224, 0xc0c0302\n" \
"v_mfma_f32_16x16x32_bf16 a[212:215], v[104:107], v[160:163], a[212:215]\n" \
"v_cmp_ne_u32_e32 vcc, 0, v216\n" \
"s_lshl_b32 s96, s94, 16\n" \
"v_mfma_f32_16x16x32_bf16 a[212:215], v[100:103], v[156:159], a[212:215]\n" \
"s_add_u32 s97, s43, s88\n" \
"v_add_u32_e32 v222, 2, v217\n" \
"v_mfma_f32_16x16x32_bf16 a[244:247], v[108:111], v[156:159], a[244:247]\n" \
"v_cndmask_b32_e32 v225, v224, v223, vcc\n" \
"s_add_u32 s42, s99, s96\n" \
"v_mfma_f32_16x16x32_bf16 a[244:247], v[112:115], v[160:163], a[244:247]\n" \
"s_add_u32 s45, 0x4000, s97\n" \
"s_lshr_b32 s94, %[w], 1\n" \
"s_waitcnt vmcnt(6)\n" \
"s_barrier\n" \
"v_mfma_f32_16x16x32_bf16 a[152:155], v[88:91], v[168:171], a[152:155]\n" \
"s_add_u32 m0, %[mA], 0x0\n" \
"s_add_u32 s85, s62, 0x0\n" \
"s_nop 0\n" \
"v_mfma_f32_16x16x32_bf16 a[152:155], v[84:87], v[164:167], a[152:155]\n" \
"buffer_load_dwordx4 v22, s[64:67], s85 offen lds\n" \
"v_mfma_f32_16x16x32_bf16 a[184:187], v[92:95], v[164:167], a[184:187]\n" \
"s_add_u32 m0, %[mA], 0x400\n" \
"s_add_u32 s84, s62, 0x2000\n" \
"v_mfma_f32_16x16x32_bf16 a[184:187], v[96:99], v[168:171], a[184:187]\n" \
"ds_read_b64_tr_b16 v[152:153], v32 offset:57344\n" \
"ds_read_b64_tr_b16 v[154:155], v32 offset:58368\n" \
"v_mfma_f32_16x16x32_bf16 a[216:219], v[104:107], v[168:171], a[216:219]\n" \
"buffer_load_dwordx4 v23, s[64:67], s84 offen lds\n" \
"v_mfma_f32_16x16x32_bf16 a[216:219], v[100:103], v[164:167], a[216:219]\n" \
"ds_read_b128 v[56:59], v29 offset:49152\n" \
"ds_read_b64_tr_b16 v[148:149], v32 offset:49152\n" \
"v_mfma_f32_16x16x32_bf16 a[248:251], v[108:111], v[164:167], a[248:251]\n" \
"s_add_u32 m0, %[mB], 0x0\n" \
"s_add_u32 s85, s63, 0x0\n" \
"v_mfma_f32_16x16x32_bf16 a[248:251], v[112:115], v[168:171], a[248:251]\n" \
"buffer_load_dwordx4 v26, s[68:71], s85 offen lds\n" \
"v_mfma_f32_16x16x32_bf16 a[156:159], v[88:91], v[176:179], a[156:159]\n" \
"ds_read_b64_tr_b16 v[150:151], v32 offset:50176\n" \
"ds_read_b128 v[52:55], v28 offset:49152\n" \
"v_mfma_f32_16x16x32_bf16 a[156:159], v[84:87], v[172:175], a[156:159]\n" \
"ds_read_b128 v[60:63], v28 offset:51200\n" \
"ds_read_b128 v[64:67], v29 offset:51200\n" \
"v_mfma_f32_16x16x32_bf16 a[188:191], v[92:95], v[172:175], a[188:191]\n" \
"s_add_u32 m0, %[mB], 0x4000\n" \
"s_add_u32 s84, s63, 0x0\n" \
"s_nop 0\n" \
"v_mfma_f32_16x16x32_bf16 a[188:191], v[96:99], v[176:179], a[188:191]\n" \
"buffer_load_dwordx4 v26, s[72:75], s84 offen lds\n" \
"v_mfma_f32_16x16x32_bf16 a[220:223], v[104:107], v[176:179], a[220:223]\n" \
"ds_read_b128 v[72:75], v29 offset:53248\n" \
"ds_read_b128 v[68:71], v28 offset:53248\n" \
"v_mfma_f32_16x16x32_bf16 a[220:223], v[100:103], v[172:175], a[220:223]\n" \
"ds_read_b128 v[76:79], v28 offset:55296\n" \
"ds_read_b128 v[80:83], v29 offset:55296\n" \
"v_mfma_f32_16x16x32_bf16 a[252:255], v[108:111], v[172:175], a[252:255]\n" \
"s_add_u32 m0, %[mA], 0x800\n" \
"s_add_u32 s85, s62, 0x4000\n" \
"v_mfma_f32_16x16x32_bf16 a[252:255], v[112:115], v[176:179], a[252:255]\n" \
"buffer_load_dwordx4 v22, s[64:67], s85 offen lds\n" \
"v_mfma_f32_16x16x32_bf16 a[128:131], v[120:123], v[88:91], a[128:131]\n" \
"ds_read_b64_tr_b16 v[160:161], v33 offset:57344\n" \
"ds_read_b64_tr_b16 v[162:163], v33 offset:58368\n" \
"v_mfma_f32_16x16x32_bf16 a[128:131], v[116:119], v[84:87], a[128:131]\n" \
"s_add_u32 m0, %[mA], 0xc00\n" \
"s_add_u32 s84, s62, 0x6000\n" \
"v_mfma_f32_16x16x32_bf16 a[160:163], v[116:119], v[92:95], a[160:163]\n" \
"buffer_load_dwordx4 v23, s[64:67], s84 offen lds\n" \
"v_mfma_f32_16x16x32_bf16 a[160:163], v[120:123], v[96:99], a[160:163]\n" \
"ds_read_b64_tr_b16 v[156:157], v33 offset:49152\n" \
"ds_read_b64_tr_b16 v[158:159], v33 offset:50176\n" \
"v_mfma_f32_16x16x32_bf16 a[192:195], v[120:123], v[104:107], a[192:195]\n" \
"ds_read_b64_tr_b16 v[168:169], v34 offset:57344\n" \
"ds_read_b64_tr_b16 v[170:171], v34 offset:58368\n" \
"v_mfma_f32_16x16x32_bf16 a[192:195], v[116:119], v[100:103], a[192:195]\n" \
"s_add_u32 m0, %[mB], 0x400\n" \
"s_add_u32 s85, s63, 0x8000\n" \
"v_mfma_f32_16x16x32_bf16 a[224:227], v[116:119], v[108:111], a[224:227]\n" \
"buffer_load_dwordx4 v26, s[68:71], s85 offen lds\n" \
"v_mfma_f32_16x16x32_bf16 a[224:227], v[120:123], v[112:115], a[224:227]\n" \
"ds_read_b64_tr_b16 v[164:165], v34 offset:49152\n" \
"ds_read_b64_tr_b16 v[166:167], v34 offset:50176\n" \
"v_mfma_f32_16x16x32_bf16 a[132:135], v[128:131], v[88:91], a[132:135]\n" \
"ds_read_b64_tr_b16 v[176:177], v35 offset:57344\n" \
"ds_read_b64_tr_b16 v[178:179], v35 offset:58368\n" \
"v_mfma_f32_16x16x32_bf16 a[132:135], v[124:127], v[84:87], a[132:135]\n" \
"s_add_u32 m0, %[mB], 0x4400\n" \
"s_add_u32 s84, s63, 0x8000\n" \
"v_mfma_f32_16x16x32_bf16 a[164:167], v[124:127], v[92:95], a[164:167]\n" \
"buffer_load_dwordx4 v26, s[72:75], s84 offen lds\n" \
"v_mfma_f32_16x16x32_bf16 a[164:167], v[128:131], v[96:99], a[164:167]\n" \
"ds_read_b64_tr_b16 v[172:173], v35 offset:49152\n" \
"ds_read_b64_tr_b16 v[174:175], v35 offset:50176\n" \
"v_mfma_f32_16x16x32_bf16 a[196:199], v[128:131], v[104:107], a[196:199]\n" \
"ds_read_b64_tr_b16 v[120:121], v32 offset:40960\n" \
"ds_read_b64_tr_b16 v[122:123], v32 offset:41984\n" \
"v_mfma_f32_16x16x32_bf16 a[196:199], v[124:127], v[100:103], a[196:199]\n" \
"s_add_u32 m0, %[mA], 0x1000\n" \
"s_add_u32 s85, s62, 0x8000\n" \
"v_mfma_f32_16x16x32_bf16 a[228:231], v[124:127], v[108:111], a[228:231]\n" \
"buffer_load_dwordx4 v22, s[64:67], s85 offen lds\n" \
"v_mfma_f32_16x16x32_bf16 a[228:231], v[128:131], v[112:115], a[228:231]\n" \
"ds_read_b64_tr_b16 v[116:117], v32 offset:32768\n" \
"ds_read_b64_tr_b16 v[118:119], v32 offset:33792\n" \
"v_mfma_f32_16x16x32_bf16 a[136:139], v[136:139], v[88:91], a[136:139]\n" \
"s_add_u32 m0, %[mA], 0x1400\n" \
"s_add_u32 s84, s62, 0xa000\n" \
"v_mfma_f32_16x16x32_bf16 a[136:139], v[132:135], v[84:87], a[136:139]\n" \
"buffer_load_dwordx4 v23, s[64:67], s84 offen lds\n" \
"v_mfma_f32_16x16x32_bf16 a[168:171], v[132:135], v[92:95], a[168:171]\n" \
"ds_read_b64_tr_b16 v[128:129], v33 offset:40960\n" \
"ds_read_b64_tr_b16 v[130:131], v33 offset:41984\n" \
"v_mfma_f32_16x16x32_bf16 a[168:171], v[136:139], v[96:99], a[168:171]\n" \
"ds_read_b64_tr_b16 v[124:125], v33 offset:32768\n" \
"ds_read_b64_tr_b16 v[126:127], v33 offset:33792\n" \
"v_mfma_f32_16x16x32_bf16 a[200:203], v[136:139], v[104:107], a[200:203]\n" \
"s_add_u32 m0, %[mB], 0x800\n" \
"s_add_u32 s85, s63, 0x10000\n" \
"v_mfma_f32_16x16x32_bf16 a[200:203], v[132:135], v[100:103], a[200:203]\n" \
"buffer_load_dwordx4 v27, s[68:71], s85 offen lds\n" \
"v_mfma_f32_16x16x32_bf16 a[232:235], v[132:135], v[108:111], a[232:235]\n" \
"ds_read_b64_tr_b16 v[132:133], v34 offset:32768\n" \
"ds_read_b64_tr_b16 v[134:135], v34 offset:33792\n" \
"v_mfma_f32_16x16x32_bf16 a[232:235], v[136:139], v[112:115], a[232:235]\n" \
"ds_read_b64_tr_b16 v[136:137], v34 offset:40960\n" \
"ds_read_b64_tr_b16 v[138:139], v34 offset:41984\n" \
"v_mfma_f32_16x16x32_bf16 a[140:143], v[144:147], v[88:91], a[140:143]\n" \
"s_add_u32 m0, %[mB], 0x4800\n" \
"s_add_u32 s84, s63, 0x10000\n" \
"v_mfma_f32_16x16x32_bf16 a[140:143], v[140:143], v[84:87], a[140:143]\n" \
"buffer_load_dwordx4 v27, s[72:75], s84 offen lds\n" \
"v_mfma_f32_16x16x32_bf16 a[172:175], v[140:143], v[92:95], a[172:175]\n" \
"ds_read_b128 v[84:87], v28 offset:32768\n" \
"ds_read_b128 v[88:91], v29 offset:32768\n" \
"v_mfma_f32_16x16x32_bf16 a[172:175], v[144:147], v[96:99], a[172:175]\n" \
"ds_read_b128 v[92:95], v28 offset:34816\n" \
"ds_read_b128 v[96:99], v29 offset:34816\n" \
"v_mfma_f32_16x16x32_bf16 a[204:207], v[144:147], v[104:107], a[204:207]\n" \
"s_add_u32 m0, %[mA], 0x1800\n" \
"s_add_u32 s85, s62, 0xc000\n" \
"v_mfma_f32_16x16x32_bf16 a[204:207], v[140:143], v[100:103], a[204:207]\n" \
"buffer_load_dwordx4 v22, s[64:67], s85 offen lds\n" \
"v_mfma_f32_16x16x32_bf16 a[236:239], v[140:143], v[108:111], a[236:239]\n" \
"ds_read_b64_tr_b16 v[140:141], v35 offset:32768\n" \
"ds_read_b64_tr_b16 v[142:143], v35 offset:33792\n" \
"v_mfma_f32_16x16x32_bf16 a[236:239], v[144:147], v[112:115], a[236:239]\n" \
"s_add_u32 m0, %[mA], 0x1c00\n" \
"s_waitcnt lgkmcnt(14)\n" \
"s_nop 0\n" \
"v_mfma_f32_16x16x32_bf16 a[16:19], v[56:59], v[152:155], a[16:19]\n" \
"s_add_u32 s84, s62, 0xe000\n" \
"ds_read_b64_tr_b16 v[144:145], v35 offset:40960\n" \
"v_mfma_f32_16x16x32_bf16 a[16:19], v[52:55], v[148:151], a[16:19]\n" \
"buffer_load_dwordx4 v23, s[64:67], s84 offen lds\n" \
"v_mfma_f32_16x16x32_bf16 a[48:51], v[60:63], v[148:151], a[48:51]\n" \
"ds_read_b64_tr_b16 v[146:147], v35 offset:41984\n" \
"ds_read_b128 v[100:103], v28 offset:36864\n" \
"v_mfma_f32_16x16x32_bf16 a[48:51], v[64:67], v[152:155], a[48:51]\n" \
"s_add_u32 m0, %[mB], 0xc00\n" \
"s_add_u32 s85, s63, 0x18000\n" \
"v_mfma_f32_16x16x32_bf16 a[80:83], v[72:75], v[152:155], a[80:83]\n" \
"buffer_load_dwordx4 v27, s[68:71], s85 offen lds\n" \
"v_mfma_f32_16x16x32_bf16 a[80:83], v[68:71], v[148:151], a[80:83]\n" \
"ds_read_b128 v[104:107], v29 offset:36864\n" \
"ds_read_b128 v[108:111], v28 offset:38912\n" \
"v_mfma_f32_16x16x32_bf16 a[112:115], v[76:79], v[148:151], a[112:115]\n" \
"ds_read_b128 v[112:115], v29 offset:38912\n" \
"s_and_b32 s95, 1, %[w]\n" \
"v_mfma_f32_16x16x32_bf16 a[112:115], v[80:83], v[152:155], a[112:115]\n" \
"s_add_u32 m0, %[mB], 0x4c00\n" \
"s_add_u32 s84, s63, 0x18000\n" \
"v_mfma_f32_16x16x32_bf16 a[20:23], v[56:59], v[160:163], a[20:23]\n" \
"buffer_load_dwordx4 v27, s[72:75], s84 offen lds\n" \
"v_mfma_f32_16x16x32_bf16 a[20:23], v[52:55], v[156:159], a[20:23]\n" \
"s_mov_b32 m0, %[mS]\n" \
"v_mov_b32_e32 v226, 0xc0c0c0c\n" \
"v_mfma_f32_16x16x32_bf16 a[52:55], v[60:63], v[156:159], a[52:55]\n" \
"v_cmp_le_u32_e32 vcc, v222, v215\n" \
"s_add_u32 s96, s94, s95\n" \
"v_mfma_f32_16x16x32_bf16 a[52:55], v[64:67], v[160:163], a[52:55]\n" \
"s_add_u32 s97, 1, s94\n" \
"v_cndmask_b32_e32 v41, v225, v226, vcc\n" \
"v_mfma_f32_16x16x32_bf16 a[84:87], v[72:75], v[160:163], a[84:87]\n" \
"v_and_b32_e32 v230, 3, v229\n" \
"s_add_u32 s89, s42, s88\n" \
"v_mfma_f32_16x16x32_bf16 a[84:87], v[68:71], v[156:159], a[84:87]\n" \
"s_lshl_b32 s59, s96, 13\n" \
"s_sub_u32 s98, s97, s95\n" \
"v_mfma_f32_16x16x32_bf16 a[116:119], v[76:79], v[156:159], a[116:119]\n" \
"s_lshl_b32 s99, s94, 13\n" \
"s_mul_i32 s101, 0x1800, s94\n" \
"v_mfma_f32_16x16x32_bf16 a[116:119], v[80:83], v[160:163], a[116:119]\n" \
"v_mov_b32_e32 v227, 0xc0c0c0c\n" \
"v_cmp_le_u32_e32 vcc, v217, v215\n" \
"v_mfma_f32_16x16x32_bf16 a[24:27], v[56:59], v[168:171], a[24:27]\n" \
"v_add_u32_e32 v231, v228, v230\n" \
"s_lshl_b32 s60, s98, 13\n" \
"v_mfma_f32_16x16x32_bf16 a[24:27], v[52:55], v[164:167], a[24:27]\n" \
"s_add_u32 s100, 0x1800, s99\n" \
"s_sub_u32 s88, 0x17000, s101\n" \
"v_mfma_f32_16x16x32_bf16 a[56:59], v[60:63], v[164:167], a[56:59]\n" \
"s_lshl_b32 s96, s94, 15\n" \
"v_cndmask_b32_e32 v42, v227, v220, vcc\n" \
"v_mfma_f32_16x16x32_bf16 a[56:59], v[64:67], v[168:171], a[56:59]\n" \
"v_lshlrev_b32_e32 v232, 7, v231\n" \
"s_add_u32 s97, 0x10000, s96\n" \
"v_mfma_f32_16x16x32_bf16 a[88:91], v[72:75], v[168:171], a[88:91]\n" \
"s_mov_b32 s86, s10\n" \
"s_lshl_b32 s98, s95, 11\n" \
"v_mfma_f32_16x16x32_bf16 a[88:91], v[68:71], v[164:167], a[88:91]\n" \
"s_cmp_lg_u32 s95, 0\n" \
"s_cselect_b32 s61, s100, s88\n" \
"s_mov_b32 exec_lo, 0x15\n" \
"s_mov_b32 exec_hi, 0x180000\n" \
"buffer_load_dword v11, s[80:83], 0 offen sc1 lds\n" \
"s_mov_b64 exec, -1\n" \
"v_mfma_f32_16x16x32_bf16 a[120:123], v[76:79], v[164:167], a[120:123]\n" \
"s_mov_b32 m0, %[mS]\n" \
"v_mov_b32_e32 v228, 0xc0c0c0c\n" \
"v_mfma_f32_16x16x32_bf16 a[120:123], v[80:83], v[168:171], a[120:123]\n" \
"v_cmp_le_u32_e32 vcc, v222, v215\n" \
"v_add_u32_e32 v233, 0x14000, v232\n" \
"v_mfma_f32_16x16x32_bf16 a[28:31], v[56:59], v[176:179], a[28:31]\n" \
"v_lshlrev_b32_e32 v235, 3, v234\n" \
"s_add_u32 s101, s97, s98\n" \
"v_mfma_f32_16x16x32_bf16 a[28:31], v[52:55], v[172:175], a[28:31]\n" \
"v_cndmask_b32_e32 v43, v228, v225, vcc\n" \
"v_and_b32_e64 v231, 2, v212\n" \
"v_mfma_f32_16x16x32_bf16 a[60:63], v[60:63], v[172:175], a[60:63]\n" \
"v_add_u32_e32 v236, v233, v235\n" \
"v_lshrrev_b32_e32 v237, 1, v230\n" \
"v_mfma_f32_16x16x32_bf16 a[60:63], v[64:67], v[176:179], a[60:63]\n" \
"v_add_u32_e32 v243, v242, v230\n" \
"s_add_u32 s100, s101, s99\n" \
"v_mfma_f32_16x16x32_bf16 a[92:95], v[72:75], v[176:179], a[92:95]\n" \
"v_and_b32_e64 v229, 3, v212\n" \
"v_mov_b32_e32 v232, 0x3f80\n" \
"v_mfma_f32_16x16x32_bf16 a[92:95], v[68:71], v[172:175], a[92:95]\n" \
"v_cmp_eq_u32_e32 vcc, 0, v231\n" \
"v_lshlrev_b32_e32 v239, 1, v238\n" \
"v_mfma_f32_16x16x32_bf16 a[124:127], v[76:79], v[172:175], a[124:127]\n" \
"v_lshlrev_b32_e32 v244, 7, v243\n" \
"s_add_u32 s88, 0x4000, s100\n" \
"v_mfma_f32_16x16x32_bf16 a[124:127], v[80:83], v[176:179], a[124:127]\n" \
"v_or_b32_e32 v230, 2, v229\n" \
"v_cndmask_b32_e32 v233, 0, v232, vcc\n" \
"v_mfma_f32_16x16x32_bf16 a[0:3], v[120:123], v[56:59], a[0:3]\n" \
"v_or_b32_e32 v240, v237, v239\n" \
"v_add_u32_e32 v245, 0x14000, v244\n" \
"v_mfma_f32_16x16x32_bf16 a[0:3], v[116:119], v[52:55], a[0:3]\n" \
"s_cmp_lg_u32 s95, 0\n" \
"s_cselect_b32 s47, s88, s101\n" \
"v_mfma_f32_16x16x32_bf16 a[32:35], v[116:119], v[60:63], a[32:35]\n" \
"v_cmp_eq_u32_e32 vcc, v230, v213\n" \
"v_lshlrev_b32_e32 v241, 5, v240\n" \
"v_mfma_f32_16x16x32_bf16 a[32:35], v[120:123], v[64:67], a[32:35]\n" \
"v_add_u32_e32 v246, v245, v235\n" \
"v_cndmask_b32_e32 v46, 0, v233, vcc\n" \
"v_mfma_f32_16x16x32_bf16 a[64:67], v[120:123], v[72:75], a[64:67]\n" \
"v_and_b32_e32 v234, 51, v212\n" \
"v_add_u32_e32 v190, v236, v241\n" \
"v_mfma_f32_16x16x32_bf16 a[64:67], v[116:119], v[68:71], a[64:67]\n" \
"s_waitcnt vmcnt(19)\n" \
"v_perm_b32 v252, v209, v208, s87\n" \
"s_mov_b32 s87, 0x7060302\n" \
"v_perm_b32 v253, v209, v208, s87\n" \
"s_mov_b32 s87, 0x5040100\n" \
"s_waitcnt vmcnt(17)\n" \
"v_perm_b32 v254, v211, v210, s87\n" \
"s_mov_b32 s87, 0x7060100\n" \
"v_perm_b32 v255, v211, v210, s87\n" \
"s_mov_b32 s87, s11\n" \
"s_mov_b32 exec_lo, 0x3ffff\n" \
"s_mov_b32 exec_hi, 0x0\n" \
"global_load_lds_dword v188, s[86:87]\n" \
"s_mov_b64 exec, -1\n" \
"v_mfma_f32_16x16x32_bf16 a[96:99], v[116:119], v[76:79], a[96:99]\n" \
"ds_write_b128 v196, v[200:203]\n" \
"v_mfma_f32_16x16x32_bf16 a[96:99], v[120:123], v[80:83], a[96:99]\n" \
"ds_write2_b32 v198, v252, v253 offset0:0 offset1:2\n" \
"s_waitcnt lgkmcnt(2)\n" \
"v_mfma_f32_16x16x32_bf16 a[4:7], v[128:131], v[56:59], a[4:7]\n" \
"ds_write2_b32 v198, v254, v255 offset0:4 offset1:6\n" \
"v_mov_b32_e32 v235, 0x3f80\n" \
"v_mfma_f32_16x16x32_bf16 a[4:7], v[124:127], v[52:55], a[4:7]\n" \
"v_cmp_eq_u32_e32 vcc, 2, v234\n" \
"s_lshl_b32 s88, s95, 4\n" \
"v_mfma_f32_16x16x32_bf16 a[36:39], v[124:127], v[60:63], a[36:39]\n" \
"v_xor_b32_e32 v247, 3, v237\n" \
"v_cndmask_b32_e32 v47, 0, v235, vcc\n" \
"v_mfma_f32_16x16x32_bf16 a[36:39], v[128:131], v[64:67], a[36:39]\n" \
"ds_write_b128 v197, v[204:207]\n" \
"v_mfma_f32_16x16x32_bf16 a[68:71], v[128:131], v[72:75], a[68:71]\n" \
"v_add_u32_e64 v236, s88, v215\n" \
"v_mov_b32_e32 v237, 0xffff\n" \
"v_mfma_f32_16x16x32_bf16 a[68:71], v[124:127], v[68:71], a[68:71]\n" \
"v_cmp_le_u32_e32 vcc, v214, v236\n" \
"v_mov_b32_e32 v239, 0xffff0000\n" \
"v_mfma_f32_16x16x32_bf16 a[100:103], v[124:127], v[76:79], a[100:103]\n" \
"v_cndmask_b32_e32 v238, 0, v237, vcc\n" \
"v_cmp_lt_u32_e32 vcc, v214, v236\n" \
"v_mfma_f32_16x16x32_bf16 a[100:103], v[128:131], v[80:83], a[100:103]\n" \
"v_add_u32_e32 v241, 2, v214\n" \
"v_cndmask_b32_e32 v240, 0, v239, vcc\n" \
"v_mfma_f32_16x16x32_bf16 a[8:11], v[136:139], v[56:59], a[8:11]\n" \
"v_mov_b32_e32 v242, 0xffff\n" \
"v_cmp_le_u32_e32 vcc, v241, v236\n" \
"v_mfma_f32_16x16x32_bf16 a[8:11], v[132:135], v[52:55], a[8:11]\n" \
"v_lshlrev_b32_e32 v248, 5, v247\n" \
"v_cndmask_b32_e32 v243, 0, v242, vcc\n" \
"v_mfma_f32_16x16x32_bf16 a[40:43], v[132:135], v[60:63], a[40:43]\n" \
"v_add_u32_e64 v39, v246, v248\n" \
"v_mov_b32_e32 v244, 0xffff0000\n" \
"v_mfma_f32_16x16x32_bf16 a[40:43], v[136:139], v[64:67], a[40:43]\n" \
"v_cmp_lt_u32_e32 vcc, v241, v236\n" \
"v_add_u32_e32 v246, 16, v214\n" \
"v_mfma_f32_16x16x32_bf16 a[72:75], v[136:139], v[72:75], a[72:75]\n" \
"v_cndmask_b32_e32 v245, 0, v244, vcc\n" \
"v_mov_b32_e32 v247, 0xffff\n" \
"v_mfma_f32_16x16x32_bf16 a[72:75], v[132:135], v[68:71], a[72:75]\n" \
"v_cmp_le_u32_e32 vcc, v246, v236\n" \
"s_add_u32 s44, 0x4000, s89\n" \
"v_mfma_f32_16x16x32_bf16 a[104:107], v[132:135], v[76:79], a[104:107]\n" \
"v_cndmask_b32_e32 v248, 0, v247, vcc\n" \
"v_mov_b32_e32 v249, 0xffff0000\n" \
"v_mfma_f32_16x16x32_bf16 a[104:107], v[136:139], v[80:83], a[104:107]\n" \
"v_cmp_lt_u32_e32 vcc, v246, v236\n" \
"s_lshl_b32 s89, %[w], 8\n" \
"v_mfma_f32_16x16x32_bf16 a[12:15], v[144:147], v[56:59], a[12:15]\n" \
"s_add_u32 s62, s22, 0x280\n" \
"s_and_b32 s62, s62, 0x3ff\n" \
"v_mfma_f32_16x16x32_bf16 a[12:15], v[140:143], v[52:55], a[12:15]\n" \
"s_lshl_b32 s63, s62, 12\n" \
"v_cndmask_b32_e32 v250, 0, v249, vcc\n" \
"v_mfma_f32_16x16x32_bf16 a[44:47], v[140:143], v[60:63], a[44:47]\n" \
"v_add_u32_e32 v251, 18, v214\n" \
"s_add_u32 s46, 0x27000, s89\n" \
"v_mfma_f32_16x16x32_bf16 a[44:47], v[144:147], v[64:67], a[44:47]\n" \
"v_mov_b32_e32 v218, 0xffff\n" \
"v_cmp_le_u32_e32 vcc, v251, v236\n" \
"v_mfma_f32_16x16x32_bf16 a[76:79], v[144:147], v[72:75], a[76:79]\n" \
"s_add_u32 s89, 0xffffc000, s101\n" \
"v_cndmask_b32_e32 v219, 0, v218, vcc\n" \
"v_mfma_f32_16x16x32_bf16 a[76:79], v[140:143], v[68:71], a[76:79]\n" \
"v_lshlrev_b32_e32 v224, 3, v212\n" \
"s_lshr_b32 s94, %[b], 5\n" \
"v_mfma_f32_16x16x32_bf16 a[108:111], v[140:143], v[76:79], a[108:111]\n" \
"s_cmp_lg_u32 s95, 0\n" \
"s_cselect_b32 s48, s89, s100\n" \
"v_mfma_f32_16x16x32_bf16 a[108:111], v[144:147], v[80:83], a[108:111]\n" \
"s_mov_b32 s96, 0xc000\n" \
"s_mov_b32 s97, 0x8000\n" \
"s_waitcnt vmcnt(2) lgkmcnt(0)\n" \
"s_barrier\n" \
"v_mfma_f32_16x16x32_bf16 a[128:131], v[120:123], v[88:91], a[128:131]\n" \
"s_add_u32 m0, %[mA], 0x8000\n" \
"s_add_u32 s85, s62, 0x0\n" \
"s_nop 0\n" \
"v_mfma_f32_16x16x32_bf16 a[128:131], v[116:119], v[84:87], a[128:131]\n" \
"buffer_load_dwordx4 v22, s[64:67], s85 offen lds\n" \
"v_mfma_f32_16x16x32_bf16 a[160:163], v[116:119], v[92:95], a[160:163]\n" \
"ds_read_b128 v[56:59], v29 offset:16384\n" \
"ds_read_b128 v[52:55], v28 offset:16384\n" \
"v_mfma_f32_16x16x32_bf16 a[160:163], v[120:123], v[96:99], a[160:163]\n" \
"s_add_u32 m0, %[mA], 0x8400\n" \
"s_add_u32 s84, s62, 0x2000\n" \
"v_mfma_f32_16x16x32_bf16 a[192:195], v[120:123], v[104:107], a[192:195]\n" \
"buffer_load_dwordx4 v23, s[64:67], s84 offen lds\n" \
"v_mfma_f32_16x16x32_bf16 a[192:195], v[116:119], v[100:103], a[192:195]\n" \
"ds_read_b128 v[60:63], v28 offset:18432\n" \
"ds_read_b128 v[64:67], v29 offset:18432\n" \
"v_mfma_f32_16x16x32_bf16 a[224:227], v[116:119], v[108:111], a[224:227]\n" \
"ds_read_b64_tr_b16 v[116:117], v32 offset:0\n" \
"ds_read_b64_tr_b16 v[118:119], v32 offset:1024\n" \
"v_mfma_f32_16x16x32_bf16 a[224:227], v[120:123], v[112:115], a[224:227]\n" \
"s_add_u32 m0, %[mB], 0x8000\n" \
"s_add_u32 s85, s63, 0x0\n" \
"v_mfma_f32_16x16x32_bf16 a[132:135], v[128:131], v[88:91], a[132:135]\n" \
"buffer_load_dwordx4 v26, s[68:71], s85 offen lds\n" \
"v_mfma_f32_16x16x32_bf16 a[132:135], v[124:127], v[84:87], a[132:135]\n" \
"ds_read_b64_tr_b16 v[120:121], v32 offset:8192\n" \
"ds_read_b64_tr_b16 v[122:123], v32 offset:9216\n" \
"v_mfma_f32_16x16x32_bf16 a[164:167], v[124:127], v[92:95], a[164:167]\n" \
"ds_read_b128 v[72:75], v29 offset:20480\n" \
"ds_read_b128 v[68:71], v28 offset:20480\n" \
"v_mfma_f32_16x16x32_bf16 a[164:167], v[128:131], v[96:99], a[164:167]\n" \
"s_add_u32 m0, %[mB], 0xc000\n" \
"s_add_u32 s84, s63, 0x0\n" \
"v_mfma_f32_16x16x32_bf16 a[196:199], v[128:131], v[104:107], a[196:199]\n" \
"buffer_load_dwordx4 v26, s[72:75], s84 offen lds\n" \
"v_mfma_f32_16x16x32_bf16 a[196:199], v[124:127], v[100:103], a[196:199]\n" \
"ds_read_b128 v[76:79], v28 offset:22528\n" \
"ds_read_b128 v[80:83], v29 offset:22528\n" \
"v_mfma_f32_16x16x32_bf16 a[228:231], v[124:127], v[108:111], a[228:231]\n" \
"ds_read_b64_tr_b16 v[124:125], v33 offset:0\n" \
"ds_read_b64_tr_b16 v[126:127], v33 offset:1024\n" \
"v_mfma_f32_16x16x32_bf16 a[228:231], v[128:131], v[112:115], a[228:231]\n" \
"s_add_u32 m0, %[mA], 0x8800\n" \
"s_add_u32 s85, s62, 0x4000\n" \
"v_mfma_f32_16x16x32_bf16 a[136:139], v[136:139], v[88:91], a[136:139]\n" \
"buffer_load_dwordx4 v22, s[64:67], s85 offen lds\n" \
"v_mfma_f32_16x16x32_bf16 a[136:139], v[132:135], v[84:87], a[136:139]\n" \
"ds_read_b64_tr_b16 v[128:129], v33 offset:8192\n" \
"ds_read_b64_tr_b16 v[130:131], v33 offset:9216\n" \
"v_mfma_f32_16x16x32_bf16 a[168:171], v[132:135], v[92:95], a[168:171]\n" \
"ds_read_b128 v[200:203], v199 offset:0\n" \
"ds_read_b128 v[204:207], v199 offset:4096\n" \
"v_mfma_f32_16x16x32_bf16 a[168:171], v[136:139], v[96:99], a[168:171]\n" \
"s_add_u32 m0, %[mA], 0x8c00\n" \
"s_add_u32 s84, s62, 0x6000\n" \
"v_mfma_f32_16x16x32_bf16 a[200:203], v[136:139], v[104:107], a[200:203]\n" \
"buffer_load_dwordx4 v23, s[64:67], s84 offen lds\n" \
"v_mfma_f32_16x16x32_bf16 a[200:203], v[132:135], v[100:103], a[200:203]\n" \
"ds_read_b128 v[208:211], v199 offset:8192\n" \
"v_mov_b32_e32 v221, 0xffff0000\n" \
"v_mfma_f32_16x16x32_bf16 a[232:235], v[132:135], v[108:111], a[232:235]\n" \
"ds_read_b64_tr_b16 v[132:133], v34 offset:0\n" \
"ds_read_b64_tr_b16 v[134:135], v34 offset:1024\n" \
"v_mfma_f32_16x16x32_bf16 a[232:235], v[136:139], v[112:115], a[232:235]\n" \
"s_add_u32 m0, %[mB], 0x8400\n" \
"s_add_u32 s85, s63, 0x8000\n" \
"v_mfma_f32_16x16x32_bf16 a[140:143], v[144:147], v[88:91], a[140:143]\n" \
"buffer_load_dwordx4 v26, s[68:71], s85 offen lds\n" \
"v_mfma_f32_16x16x32_bf16 a[140:143], v[140:143], v[84:87], a[140:143]\n" \
"ds_read_b64_tr_b16 v[136:137], v34 offset:8192\n" \
"ds_read_b64_tr_b16 v[138:139], v34 offset:9216\n" \
"v_mfma_f32_16x16x32_bf16 a[172:175], v[140:143], v[92:95], a[172:175]\n" \
"v_cmp_lt_u32_e32 vcc, v251, v236\n" \
"v_lshrrev_b32_e32 v212, 4, v20\n" \
"v_mfma_f32_16x16x32_bf16 a[172:175], v[144:147], v[96:99], a[172:175]\n" \
"s_add_u32 m0, %[mB], 0xc400\n" \
"s_add_u32 s84, s63, 0x8000\n" \
"v_mfma_f32_16x16x32_bf16 a[204:207], v[144:147], v[104:107], a[204:207]\n" \
"buffer_load_dwordx4 v26, s[72:75], s84 offen lds\n" \
"v_mfma_f32_16x16x32_bf16 a[204:207], v[140:143], v[100:103], a[204:207]\n" \
"s_cmp_lg_u32 s94, 0\n" \
"s_nop 0\n" \
"s_cselect_b32 s53, s96, 0x8000\n" \
"v_mfma_f32_16x16x32_bf16 a[236:239], v[140:143], v[108:111], a[236:239]\n" \
"ds_read_b64_tr_b16 v[140:141], v35 offset:0\n" \
"ds_read_b64_tr_b16 v[142:143], v35 offset:1024\n" \
"v_mfma_f32_16x16x32_bf16 a[236:239], v[144:147], v[112:115], a[236:239]\n" \
"s_add_u32 m0, %[mA], 0x9000\n" \
"s_add_u32 s85, s62, 0x8000\n" \
"v_mfma_f32_16x16x32_bf16 a[144:147], v[88:91], v[152:155], a[144:147]\n" \
"buffer_load_dwordx4 v22, s[64:67], s85 offen lds\n" \
"v_mfma_f32_16x16x32_bf16 a[144:147], v[84:87], v[148:151], a[144:147]\n" \
"ds_read_b64_tr_b16 v[144:145], v35 offset:8192\n" \
"ds_read_b64_tr_b16 v[146:147], v35 offset:9216\n" \
"v_mfma_f32_16x16x32_bf16 a[176:179], v[92:95], v[148:151], a[176:179]\n" \
"s_cmp_lg_u32 s94, 0\n" \
"s_cselect_b32 s98, s97, 0x4000\n" \
"v_mfma_f32_16x16x32_bf16 a[176:179], v[96:99], v[152:155], a[176:179]\n" \
"s_add_u32 m0, %[mA], 0x9400\n" \
"s_add_u32 s84, s62, 0xa000\n" \
"v_mfma_f32_16x16x32_bf16 a[208:211], v[104:107], v[152:155], a[208:211]\n" \
"buffer_load_dwordx4 v23, s[64:67], s84 offen lds\n" \
"v_mfma_f32_16x16x32_bf16 a[208:211], v[100:103], v[148:151], a[208:211]\n" \
"v_cndmask_b32_e32 v216, 0, v221, vcc\n" \
"v_lshlrev_b32_e32 v223, 2, v236\n" \
"s_nop 0\n" \
"v_mfma_f32_16x16x32_bf16 a[240:243], v[108:111], v[148:151], a[240:243]\n" \
"ds_read_b64_tr_b16 v[148:149], v32 offset:16384\n" \
"ds_read_b64_tr_b16 v[150:151], v32 offset:17408\n" \
"v_mfma_f32_16x16x32_bf16 a[240:243], v[112:115], v[152:155], a[240:243]\n" \
"s_add_u32 m0, %[mB], 0x8800\n" \
"s_add_u32 s85, s63, 0x10000\n" \
"v_mfma_f32_16x16x32_bf16 a[148:151], v[88:91], v[160:163], a[148:151]\n" \
"buffer_load_dwordx4 v27, s[68:71], s85 offen lds\n" \
"v_mfma_f32_16x16x32_bf16 a[148:151], v[84:87], v[156:159], a[148:151]\n" \
"ds_read_b64_tr_b16 v[152:153], v32 offset:24576\n" \
"ds_read_b64_tr_b16 v[154:155], v32 offset:25600\n" \
"v_mfma_f32_16x16x32_bf16 a[180:183], v[92:95], v[156:159], a[180:183]\n" \
"s_lshl_b32 s101, %[w], 9\n" \
"v_mul_u32_u24_e32 v213, 0xe39, v212\n" \
"v_mfma_f32_16x16x32_bf16 a[180:183], v[96:99], v[160:163], a[180:183]\n" \
"s_add_u32 m0, %[mB], 0xc800\n" \
"s_add_u32 s84, s63, 0x10000\n" \
"v_mfma_f32_16x16x32_bf16 a[212:215], v[104:107], v[160:163], a[212:215]\n" \
"buffer_load_dwordx4 v27, s[72:75], s84 offen lds\n" \
"v_mfma_f32_16x16x32_bf16 a[212:215], v[100:103], v[156:159], a[212:215]\n" \
"v_or_b32_e32 v15, v219, v216\n" \
"v_add_u32_e32 v18, 0x27000, v223\n" \
"v_mfma_f32_16x16x32_bf16 a[244:247], v[108:111], v[156:159], a[244:247]\n" \
"ds_read_b64_tr_b16 v[156:157], v33 offset:16384\n" \
"ds_read_b64_tr_b16 v[158:159], v33 offset:17408\n" \
"v_mfma_f32_16x16x32_bf16 a[244:247], v[112:115], v[160:163], a[244:247]\n" \
"ds_read_b64_tr_b16 v[160:161], v33 offset:24576\n" \
"ds_read_b64_tr_b16 v[162:163], v33 offset:25600\n" \
"v_mfma_f32_16x16x32_bf16 a[152:155], v[88:91], v[168:171], a[152:155]\n" \
"s_add_u32 s89, 0x27800, s101\n" \
"v_lshrrev_b32_e32 v214, 16, v213\n" \
"v_mfma_f32_16x16x32_bf16 a[152:155], v[84:87], v[164:167], a[152:155]\n" \
"v_add_u32_e32 v222, 64, v212\n" \
"v_add_u32_e64 v19, s89, v224\n" \
"v_mfma_f32_16x16x32_bf16 a[184:187], v[92:95], v[164:167], a[184:187]\n" \
"v_mul_u32_u24_e32 v216, 18, v214\n" \
"s_and_b32 s94, 7, %[b]\n" \
"v_mfma_f32_16x16x32_bf16 a[184:187], v[96:99], v[168:171], a[184:187]\n" \
"v_mul_u32_u24_e32 v223, 0xe39, v222\n" \
"v_sub_u32_e32 v217, v212, v216\n" \
"v_mfma_f32_16x16x32_bf16 a[216:219], v[104:107], v[168:171], a[216:219]\n" \
"s_lshl_b32 s95, s94, 5\n" \
"v_lshrrev_b32_e32 v224, 16, v223\n" \
"v_mfma_f32_16x16x32_bf16 a[216:219], v[100:103], v[164:167], a[216:219]\n" \
"v_add_u32_e32 v233, 0x80, v212\n" \
"s_add_u32 s96, 32, s95\n" \
"v_mfma_f32_16x16x32_bf16 a[248:251], v[108:111], v[164:167], a[248:251]\n" \
"ds_read_b64_tr_b16 v[164:165], v34 offset:16384\n" \
"ds_read_b64_tr_b16 v[166:167], v34 offset:17408\n" \
"v_mfma_f32_16x16x32_bf16 a[248:251], v[112:115], v[168:171], a[248:251]\n" \
"ds_read_b64_tr_b16 v[168:169], v34 offset:24576\n" \
"ds_read_b64_tr_b16 v[170:171], v34 offset:25600\n" \
"v_mfma_f32_16x16x32_bf16 a[156:159], v[88:91], v[176:179], a[156:159]\n" \
"ds_read_b128 v[88:91], v29 offset:0\n" \
"v_lshlrev_b32_e32 v218, 4, v217\n" \
"v_mfma_f32_16x16x32_bf16 a[156:159], v[84:87], v[172:175], a[156:159]\n" \
"ds_read_b128 v[84:87], v28 offset:0\n" \
"v_mul_u32_u24_e32 v226, 18, v224\n" \
"v_mfma_f32_16x16x32_bf16 a[188:191], v[92:95], v[172:175], a[188:191]\n" \
"ds_read_b128 v[92:95], v28 offset:2048\n" \
"v_mul_u32_u24_e32 v234, 0xe39, v233\n" \
"v_mfma_f32_16x16x32_bf16 a[188:191], v[96:99], v[176:179], a[188:191]\n" \
"ds_read_b128 v[96:99], v29 offset:2048\n" \
"v_or_b32_e32 v13, v243, v245\n" \
"v_mfma_f32_16x16x32_bf16 a[220:223], v[104:107], v[176:179], a[220:223]\n" \
"ds_read_b128 v[104:107], v29 offset:4096\n" \
"v_add_u32_e32 v219, s96, v218\n" \
"v_mfma_f32_16x16x32_bf16 a[220:223], v[100:103], v[172:175], a[220:223]\n" \
"ds_read_b128 v[100:103], v28 offset:4096\n" \
"v_sub_u32_e32 v227, v222, v226\n" \
"v_mfma_f32_16x16x32_bf16 a[252:255], v[108:111], v[172:175], a[252:255]\n" \
"ds_read_b64_tr_b16 v[172:173], v35 offset:16384\n" \
"ds_read_b64_tr_b16 v[174:175], v35 offset:17408\n" \
"v_mfma_f32_16x16x32_bf16 a[252:255], v[112:115], v[176:179], a[252:255]\n" \
"s_add_u32 m0, %[mA], 0x9800\n" \
"s_waitcnt lgkmcnt(12)\n" \
"v_mfma_f32_16x16x32_bf16 a[0:3], v[120:123], v[56:59], a[0:3]\n" \
"s_add_u32 s85, s62, 0xc000\n" \
"ds_read_b64_tr_b16 v[176:177], v35 offset:24576\n" \
"v_mfma_f32_16x16x32_bf16 a[0:3], v[116:119], v[52:55], a[0:3]\n" \
"buffer_load_dwordx4 v22, s[64:67], s85 offen lds\n" \
"v_mfma_f32_16x16x32_bf16 a[32:35], v[116:119], v[60:63], a[32:35]\n" \
"s_add_u32 m0, %[mA], 0x9c00\n" \
"s_add_u32 s84, s62, 0xe000\n" \
"v_mfma_f32_16x16x32_bf16 a[32:35], v[120:123], v[64:67], a[32:35]\n" \
"ds_read_b64_tr_b16 v[178:179], v35 offset:25600\n" \
"ds_read_b128 v[108:111], v28 offset:6144\n" \
"v_mfma_f32_16x16x32_bf16 a[64:67], v[120:123], v[72:75], a[64:67]\n" \
"buffer_load_dwordx4 v23, s[64:67], s84 offen lds\n" \
"L_pf2:\n" \
"s_cbranch_execz L_pf6\n" \
"s_nop 0\n" \
"v_mfma_f32_16x16x32_bf16 a[64:67], v[116:119], v[68:71], a[64:67]\n" \
"s_add_u32 m0, %[mB], 0x8c00\n" \
"s_add_u32 s85, s63, 0x18000\n" \
"v_mfma_f32_16x16x32_bf16 a[96:99], v[116:119], v[76:79], a[96:99]\n" \
"ds_read_b128 v[112:115], v29 offset:6144\n" \
"v_lshrrev_b32_e32 v235, 16, v234\n" \
"v_mfma_f32_16x16x32_bf16 a[96:99], v[120:123], v[80:83], a[96:99]\n" \
"buffer_load_dwordx4 v27, s[68:71], s85 offen lds\n" \
"v_mfma_f32_16x16x32_bf16 a[4:7], v[128:131], v[56:59], a[4:7]\n" \
"s_add_u32 m0, %[mB], 0xcc00\n" \
"s_add_u32 s84, s63, 0x18000\n" \
"v_mfma_f32_16x16x32_bf16 a[4:7], v[124:127], v[52:55], a[4:7]\n" \
"v_add_u32_e32 v244, 0xc0, v212\n" \
"v_or_b32_e32 v12, v238, v240\n" \
"v_mfma_f32_16x16x32_bf16 a[36:39], v[124:127], v[60:63], a[36:39]\n" \
"buffer_load_dwordx4 v27, s[72:75], s84 offen lds\n" \
"v_mfma_f32_16x16x32_bf16 a[36:39], v[128:131], v[64:67], a[36:39]\n" \
"v_and_b32_e32 v220, 0xff, v219\n" \
"v_cmp_gt_u32_e32 vcc, 16, v217\n" \
"v_mfma_f32_16x16x32_bf16 a[68:71], v[128:131], v[72:75], a[68:71]\n" \
"v_lshlrev_b32_e32 v228, 4, v227\n" \
"v_mul_u32_u24_e32 v237, 18, v235\n" \
"v_mfma_f32_16x16x32_bf16 a[68:71], v[124:127], v[68:71], a[68:71]\n" \
"buffer_store_dwordx4 v[200:203], v20, s[80:83], s57 offen sc1\n" \
"v_mfma_f32_16x16x32_bf16 a[100:103], v[124:127], v[76:79], a[100:103]\n" \
"v_mul_u32_u24_e32 v245, 0xe39, v244\n" \
"v_or_b32_e32 v14, v248, v250\n" \
"v_mfma_f32_16x16x32_bf16 a[100:103], v[128:131], v[80:83], a[100:103]\n" \
"v_cndmask_b32_e32 v221, v218, v220, vcc\n" \
"v_add_u32_e32 v229, s96, v228\n" \
"v_mfma_f32_16x16x32_bf16 a[8:11], v[136:139], v[56:59], a[8:11]\n" \
"v_sub_u32_e32 v238, v233, v237\n" \
"v_lshrrev_b32_e32 v246, 16, v245\n" \
"v_mfma_f32_16x16x32_bf16 a[8:11], v[132:135], v[52:55], a[8:11]\n" \
"v_add_u32_e32 v219, 0x100, v212\n" \
"v_mul_u32_u24_e32 v215, 0x480, v214\n" \
"v_mfma_f32_16x16x32_bf16 a[40:43], v[132:135], v[60:63], a[40:43]\n" \
"buffer_store_dwordx4 v[204:207], v20, s[80:83], s56 offen sc1\n" \
"v_mfma_f32_16x16x32_bf16 a[40:43], v[136:139], v[64:67], a[40:43]\n" \
"v_and_b32_e32 v230, 0xff, v229\n" \
"v_cmp_gt_u32_e32 vcc, 16, v227\n" \
"v_mfma_f32_16x16x32_bf16 a[72:75], v[136:139], v[72:75], a[72:75]\n" \
"v_lshlrev_b32_e32 v239, 4, v238\n" \
"v_mul_u32_u24_e32 v248, 18, v246\n" \
"v_mfma_f32_16x16x32_bf16 a[72:75], v[132:135], v[68:71], a[72:75]\n" \
"v_mul_u32_u24_e32 v217, 0xe39, v219\n" \
"v_add_u32_e32 v0, v215, v221\n" \
"v_mfma_f32_16x16x32_bf16 a[104:107], v[132:135], v[76:79], a[104:107]\n" \
"v_cndmask_b32_e32 v231, v228, v230, vcc\n" \
"v_add_u32_e32 v240, s96, v239\n" \
"v_mfma_f32_16x16x32_bf16 a[104:107], v[136:139], v[80:83], a[104:107]\n" \
"v_sub_u32_e32 v249, v244, v248\n" \
"v_lshrrev_b32_e32 v220, 16, v217\n" \
"v_mfma_f32_16x16x32_bf16 a[12:15], v[144:147], v[56:59], a[12:15]\n" \
"buffer_store_dwordx4 v[208:211], v20, s[80:83], s58 offen sc1\n" \
"v_mfma_f32_16x16x32_bf16 a[12:15], v[140:143], v[52:55], a[12:15]\n" \
"v_add_u32_e32 v229, 0x140, v212\n" \
"v_mul_u32_u24_e32 v225, 0x480, v224\n" \
"v_mfma_f32_16x16x32_bf16 a[44:47], v[140:143], v[60:63], a[44:47]\n" \
"v_and_b32_e32 v241, 0xff, v240\n" \
"v_cmp_gt_u32_e32 vcc, 16, v238\n" \
"v_mfma_f32_16x16x32_bf16 a[44:47], v[144:147], v[64:67], a[44:47]\n" \
"v_lshlrev_b32_e32 v250, 4, v249\n" \
"v_mul_u32_u24_e32 v215, 18, v220\n" \
"v_mfma_f32_16x16x32_bf16 a[76:79], v[144:147], v[72:75], a[76:79]\n" \
"v_mul_u32_u24_e32 v227, 0xe39, v229\n" \
"v_add_u32_e32 v232, v225, v231\n" \
"v_mfma_f32_16x16x32_bf16 a[76:79], v[140:143], v[68:71], a[76:79]\n" \
"v_cndmask_b32_e32 v242, v239, v241, vcc\n" \
"v_add_u32_e32 v251, s96, v250\n" \
"v_mfma_f32_16x16x32_bf16 a[108:111], v[140:143], v[76:79], a[108:111]\n" \
"s_mov_b32 m0, %[mS]\n" \
"s_mov_b32 s86, s10\n" \
"v_mfma_f32_16x16x32_bf16 a[108:111], v[144:147], v[80:83], a[108:111]\n" \
"s_mov_b32 s87, s11\n" \
"v_sub_u32_e32 v221, v219, v215\n" \
"v_mfma_f32_16x16x32_bf16 a[16:19], v[56:59], v[152:155], a[16:19]\n" \
"v_lshrrev_b32_e32 v230, 16, v227\n" \
"v_add_u32_e32 v240, 0x180, v212\n" \
"v_mfma_f32_16x16x32_bf16 a[16:19], v[52:55], v[148:151], a[16:19]\n" \
"v_mul_u32_u24_e32 v236, 0x480, v235\n" \
"v_and_b32_e32 v213, 0xff, v251\n" \
"v_mfma_f32_16x16x32_bf16 a[48:51], v[60:63], v[148:151], a[48:51]\n" \
"v_cmp_gt_u32_e32 vcc, 16, v249\n" \
"v_lshlrev_b32_e32 v223, 4, v221\n" \
"v_mfma_f32_16x16x32_bf16 a[48:51], v[64:67], v[152:155], a[48:51]\n" \
"v_mul_u32_u24_e32 v225, 18, v230\n" \
"v_mul_u32_u24_e32 v238, 0xe39, v240\n" \
"v_mfma_f32_16x16x32_bf16 a[80:83], v[72:75], v[152:155], a[80:83]\n" \
"v_add_u32_e32 v1, 0xfffffc00, v232\n" \
"v_add_u32_e64 v243, v236, v242\n" \
"s_mov_b32 exec_lo, 0xfffc0000\n" \
"s_mov_b32 exec_hi, 0xf\n" \
"global_load_lds_dword v188, s[86:87]\n" \
"s_mov_b64 exec, -1\n" \
"v_mfma_f32_16x16x32_bf16 a[80:83], v[68:71], v[148:151], a[80:83]\n" \
"v_cndmask_b32_e32 v214, v250, v213, vcc\n" \
"v_add_u32_e32 v224, s96, v223\n" \
"v_mfma_f32_16x16x32_bf16 a[112:115], v[76:79], v[148:151], a[112:115]\n" \
"v_sub_u32_e32 v231, v229, v225\n" \
"v_lshrrev_b32_e32 v241, 16, v238\n" \
"v_mfma_f32_16x16x32_bf16 a[112:115], v[80:83], v[152:155], a[112:115]\n" \
"v_add_u32_e32 v251, 0x1c0, v212\n" \
"v_mul_u32_u24_e32 v247, 0x480, v246\n" \
"v_mfma_f32_16x16x32_bf16 a[20:23], v[56:59], v[160:163], a[20:23]\n" \
"v_and_b32_e32 v222, 0xff, v224\n" \
"v_cmp_gt_u32_e32 vcc, 16, v221\n" \
"v_mfma_f32_16x16x32_bf16 a[20:23], v[52:55], v[156:159], a[20:23]\n" \
"v_lshlrev_b32_e32 v232, 4, v231\n" \
"v_mul_u32_u24_e32 v236, 18, v241\n" \
"v_mfma_f32_16x16x32_bf16 a[52:55], v[60:63], v[156:159], a[52:55]\n" \
"v_mul_u32_u24_e32 v249, 0xe39, v251\n" \
"v_add_u32_e32 v2, 0xfffff800, v243\n" \
"v_mfma_f32_16x16x32_bf16 a[52:55], v[64:67], v[160:163], a[52:55]\n" \
"v_add_u32_e32 v216, v247, v214\n" \
"v_cndmask_b32_e32 v226, v223, v222, vcc\n" \
"v_mfma_f32_16x16x32_bf16 a[84:87], v[72:75], v[160:163], a[84:87]\n" \
"v_add_u32_e32 v234, s96, v232\n" \
"v_sub_u32_e32 v242, v240, v236\n" \
"v_mfma_f32_16x16x32_bf16 a[84:87], v[68:71], v[156:159], a[84:87]\n" \
"v_lshrrev_b32_e32 v213, 16, v249\n" \
"v_add_u32_e32 v224, 0x200, v212\n" \
"v_mfma_f32_16x16x32_bf16 a[116:119], v[76:79], v[156:159], a[116:119]\n" \
"v_mul_u32_u24_e32 v218, 0x480, v220\n" \
"v_and_b32_e32 v235, 0xff, v234\n" \
"v_mfma_f32_16x16x32_bf16 a[116:119], v[80:83], v[160:163], a[116:119]\n" \
"v_cmp_gt_u32_e32 vcc, 16, v231\n" \
"s_waitcnt lgkmcnt(0)\n" \
"v_mfma_f32_16x16x32_bf16 a[24:27], v[56:59], v[168:171], a[24:27]\n" \
"v_lshlrev_b32_e32 v243, 4, v242\n" \
"v_mul_u32_u24_e32 v247, 18, v213\n" \
"v_mfma_f32_16x16x32_bf16 a[24:27], v[52:55], v[164:167], a[24:27]\n" \
"v_mul_u32_u24_e32 v221, 0xe39, v224\n" \
"v_add_u32_e32 v3, 0xfffff400, v216\n" \
"v_mfma_f32_16x16x32_bf16 a[56:59], v[60:63], v[164:167], a[56:59]\n" \
"v_add_u32_e32 v4, v218, v226\n" \
"v_cndmask_b32_e32 v233, v232, v235, vcc\n" \
"v_mfma_f32_16x16x32_bf16 a[56:59], v[64:67], v[168:171], a[56:59]\n" \
"v_add_u32_e32 v245, s96, v243\n" \
"v_sub_u32_e32 v214, v251, v247\n" \
"v_mfma_f32_16x16x32_bf16 a[88:91], v[72:75], v[168:171], a[88:91]\n" \
"v_lshrrev_b32_e32 v222, 16, v221\n" \
"v_and_b32_e32 v246, 0xff, v245\n" \
"v_mfma_f32_16x16x32_bf16 a[88:91], v[68:71], v[164:167], a[88:91]\n" \
"v_cmp_gt_u32_e32 vcc, 16, v242\n" \
"v_lshlrev_b32_e32 v216, 4, v214\n" \
"v_mfma_f32_16x16x32_bf16 a[120:123], v[76:79], v[164:167], a[120:123]\n" \
"v_mul_u32_u24_e32 v218, 18, v222\n" \
"v_cndmask_b32_e32 v244, v243, v246, vcc\n" \
"v_mfma_f32_16x16x32_bf16 a[120:123], v[80:83], v[168:171], a[120:123]\n" \
"v_add_u32_e32 v217, s96, v216\n" \
"v_sub_u32_e32 v226, v224, v218\n" \
"v_mfma_f32_16x16x32_bf16 a[28:31], v[56:59], v[176:179], a[28:31]\n" \
"s_add_u32 s62, s22, 0x300\n" \
"s_and_b32 s62, s62, 0x3ff\n" \
"v_mfma_f32_16x16x32_bf16 a[28:31], v[52:55], v[172:175], a[28:31]\n" \
"s_lshl_b32 s63, s62, 12\n" \
"v_mul_u32_u24_e32 v228, 0x480, v230\n" \
"v_mfma_f32_16x16x32_bf16 a[60:63], v[60:63], v[172:175], a[60:63]\n" \
"v_and_b32_e32 v220, 0xff, v217\n" \
"v_cmp_gt_u32_e32 vcc, 16, v214\n" \
"v_mfma_f32_16x16x32_bf16 a[60:63], v[64:67], v[176:179], a[60:63]\n" \
"v_lshlrev_b32_e32 v227, 4, v226\n" \
"v_cndmask_b32_e32 v219, v216, v220, vcc\n" \
"v_mfma_f32_16x16x32_bf16 a[92:95], v[72:75], v[176:179], a[92:95]\n" \
"v_add_u32_e32 v230, s96, v227\n" \
"v_and_b32_e32 v229, 0xff, v230\n" \
"v_mfma_f32_16x16x32_bf16 a[92:95], v[68:71], v[172:175], a[92:95]\n" \
"v_cmp_gt_u32_e32 vcc, 16, v226\n" \
"v_mul_u32_u24_e32 v250, 0x480, v213\n" \
"v_mfma_f32_16x16x32_bf16 a[124:127], v[76:79], v[172:175], a[124:127]\n" \
"v_cndmask_b32_e32 v225, v227, v229, vcc\n" \
"v_and_b32_e64 v249, 2, v212\n" \
"v_mfma_f32_16x16x32_bf16 a[124:127], v[80:83], v[176:179], a[124:127]\n" \
"v_mov_b32_e32 v213, 0x100\n" \
"v_mov_b32_e32 v251, s95\n" \
"s_waitcnt vmcnt(4)\n" \
"s_barrier\n" \
"v_mfma_f32_16x16x32_bf16 a[144:147], v[88:91], v[152:155], a[144:147]\n" \
"s_add_u32 m0, %[mA], 0x0\n" \
"s_add_u32 s85, s62, 0x0\n" \
"s_nop 0\n" \
"v_mfma_f32_16x16x32_bf16 a[144:147], v[84:87], v[148:151], a[144:147]\n" \
"buffer_load_dwordx4 v22, s[64:67], s85 offen lds\n" \
"v_mfma_f32_16x16x32_bf16 a[176:179], v[92:95], v[148:151], a[176:179]\n" \
"ds_read_b128 v[56:59], v29 offset:49152\n" \
"ds_read_b128 v[52:55], v28 offset:49152\n" \
"v_mfma_f32_16x16x32_bf16 a[176:179], v[96:99], v[152:155], a[176:179]\n" \
"s_add_u32 m0, %[mA], 0x400\n" \
"s_add_u32 s84, s62, 0x2000\n" \
"v_mfma_f32_16x16x32_bf16 a[208:211], v[104:107], v[152:155], a[208:211]\n" \
"buffer_load_dwordx4 v23, s[64:67], s84 offen lds\n" \
"v_mfma_f32_16x16x32_bf16 a[208:211], v[100:103], v[148:151], a[208:211]\n" \
"ds_read_b128 v[60:63], v28 offset:51200\n" \
"ds_read_b128 v[64:67], v29 offset:51200\n" \
"v_mfma_f32_16x16x32_bf16 a[240:243], v[108:111], v[148:151], a[240:243]\n" \
"ds_read_b64_tr_b16 v[148:149], v32 offset:49152\n" \
"ds_read_b64_tr_b16 v[150:151], v32 offset:50176\n" \
"v_mfma_f32_16x16x32_bf16 a[240:243], v[112:115], v[152:155], a[240:243]\n" \
"s_add_u32 m0, %[mB], 0x0\n" \
"s_add_u32 s85, s63, 0x0\n" \
"v_mfma_f32_16x16x32_bf16 a[148:151], v[88:91], v[160:163], a[148:151]\n" \
"buffer_load_dwordx4 v26, s[68:71], s85 offen lds\n" \
"v_mfma_f32_16x16x32_bf16 a[148:151], v[84:87], v[156:159], a[148:151]\n" \
"ds_read_b64_tr_b16 v[152:153], v32 offset:57344\n" \
"ds_read_b64_tr_b16 v[154:155], v32 offset:58368\n" \
"v_mfma_f32_16x16x32_bf16 a[180:183], v[92:95], v[156:159], a[180:183]\n" \
"ds_read_b128 v[72:75], v29 offset:53248\n" \
"ds_read_b128 v[68:71], v28 offset:53248\n" \
"v_mfma_f32_16x16x32_bf16 a[180:183], v[96:99], v[160:163], a[180:183]\n" \
"s_add_u32 m0, %[mB], 0x4000\n" \
"s_add_u32 s84, s63, 0x0\n" \
"s_nop 0\n" \
"v_mfma_f32_16x16x32_bf16 a[212:215], v[104:107], v[160:163], a[212:215]\n" \
"buffer_load_dwordx4 v26, s[72:75], s84 offen lds\n" \
"v_mfma_f32_16x16x32_bf16 a[212:215], v[100:103], v[156:159], a[212:215]\n" \
"ds_read_b128 v[76:79], v28 offset:55296\n" \
"ds_read_b128 v[80:83], v29 offset:55296\n" \
"v_mfma_f32_16x16x32_bf16 a[244:247], v[108:111], v[156:159], a[244:247]\n" \
"ds_read_b64_tr_b16 v[156:157], v33 offset:49152\n" \
"ds_read_b64_tr_b16 v[158:159], v33 offset:50176\n" \
"v_mfma_f32_16x16x32_bf16 a[244:247], v[112:115], v[160:163], a[244:247]\n" \
"s_add_u32 m0, %[mA], 0x800\n" \
"s_add_u32 s85, s62, 0x4000\n" \
"v_mfma_f32_16x16x32_bf16 a[152:155], v[88:91], v[168:171], a[152:155]\n" \
"buffer_load_dwordx4 v22, s[64:67], s85 offen lds\n" \
"v_mfma_f32_16x16x32_bf16 a[152:155], v[84:87], v[164:167], a[152:155]\n" \
"ds_read_b64_tr_b16 v[160:161], v33 offset:57344\n" \
"ds_read_b64_tr_b16 v[162:163], v33 offset:58368\n" \
"v_mfma_f32_16x16x32_bf16 a[184:187], v[92:95], v[164:167], a[184:187]\n" \
"v_cmp_ne_u32_e32 vcc, 0, v249\n" \
"s_mov_b32 s76, s8\n" \
"v_mfma_f32_16x16x32_bf16 a[184:187], v[96:99], v[168:171], a[184:187]\n" \
"s_add_u32 m0, %[mA], 0xc00\n" \
"s_add_u32 s84, s62, 0x6000\n" \
"v_mfma_f32_16x16x32_bf16 a[216:219], v[104:107], v[168:171], a[216:219]\n" \
"buffer_load_dwordx4 v23, s[64:67], s84 offen lds\n" \
"v_mfma_f32_16x16x32_bf16 a[216:219], v[100:103], v[164:167], a[216:219]\n" \
"v_cndmask_b32_e32 v247, v251, v213, vcc\n" \
"s_nop 0\n" \
"s_and_b32 s77, s9, 0xffff\n" \
"v_mfma_f32_16x16x32_bf16 a[248:251], v[108:111], v[164:167], a[248:251]\n" \
"ds_read_b64_tr_b16 v[164:165], v34 offset:49152\n" \
"ds_read_b64_tr_b16 v[166:167], v34 offset:50176\n" \
"v_mfma_f32_16x16x32_bf16 a[248:251], v[112:115], v[168:171], a[248:251]\n" \
"s_add_u32 m0, %[mB], 0x400\n" \
"s_add_u32 s85, s63, 0x8000\n" \
"v_mfma_f32_16x16x32_bf16 a[156:159], v[88:91], v[176:179], a[156:159]\n" \
"buffer_load_dwordx4 v26, s[68:71], s85 offen lds\n" \
"v_mfma_f32_16x16x32_bf16 a[156:159], v[84:87], v[172:175], a[156:159]\n" \
"ds_read_b64_tr_b16 v[168:169], v34 offset:57344\n" \
"ds_read_b64_tr_b16 v[170:171], v34 offset:58368\n" \
"v_mfma_f32_16x16x32_bf16 a[188:191], v[92:95], v[172:175], a[188:191]\n" \
"s_mov_b32 s78, 0x240000\n" \
"v_xor_b32_e64 v37, 0x20, v190\n" \
"v_mfma_f32_16x16x32_bf16 a[188:191], v[96:99], v[176:179], a[188:191]\n" \
"s_add_u32 m0, %[mB], 0x4400\n" \
"s_add_u32 s84, s63, 0x8000\n" \
"v_mfma_f32_16x16x32_bf16 a[220:223], v[104:107], v[176:179], a[220:223]\n" \
"buffer_load_dwordx4 v26, s[72:75], s84 offen lds\n" \
"v_mfma_f32_16x16x32_bf16 a[220:223], v[100:103], v[172:175], a[220:223]\n" \
"v_xor_b32_e32 v38, 0x40, v190\n" \
"v_add_u32_e32 v48, s60, v28\n" \
"v_mfma_f32_16x16x32_bf16 a[252:255], v[108:111], v[172:175], a[252:255]\n" \
"ds_read_b64_tr_b16 v[172:173], v35 offset:49152\n" \
"ds_read_b64_tr_b16 v[174:175], v35 offset:50176\n" \
"v_mfma_f32_16x16x32_bf16 a[252:255], v[112:115], v[176:179], a[252:255]\n" \
"s_add_u32 m0, %[mA], 0x1000\n" \
"s_add_u32 s85, s62, 0x8000\n" \
"v_mfma_f32_16x16x32_bf16 a[128:131], v[120:123], v[88:91], a[128:131]\n" \
"buffer_load_dwordx4 v22, s[64:67], s85 offen lds\n" \
"v_mfma_f32_16x16x32_bf16 a[128:131], v[116:119], v[84:87], a[128:131]\n" \
"ds_read_b64_tr_b16 v[176:177], v35 offset:57344\n" \
"ds_read_b64_tr_b16 v[178:179], v35 offset:58368\n" \
"v_mfma_f32_16x16x32_bf16 a[160:163], v[116:119], v[92:95], a[160:163]\n" \
"v_add_u32_e32 v49, s60, v29\n" \
"v_xor_b32_e32 v36, 0x0, v190\n" \
"v_mfma_f32_16x16x32_bf16 a[160:163], v[120:123], v[96:99], a[160:163]\n" \
"s_add_u32 m0, %[mA], 0x1400\n" \
"s_add_u32 s84, s62, 0xa000\n" \
"v_mfma_f32_16x16x32_bf16 a[192:195], v[120:123], v[104:107], a[192:195]\n" \
"buffer_load_dwordx4 v23, s[64:67], s84 offen lds\n" \
"v_mfma_f32_16x16x32_bf16 a[192:195], v[116:119], v[100:103], a[192:195]\n" \
"v_add_u32_e32 v237, v228, v233\n" \
"s_lshl_b32 s97, %[w], 4\n" \
"v_mfma_f32_16x16x32_bf16 a[224:227], v[116:119], v[108:111], a[224:227]\n" \
"ds_read_b64_tr_b16 v[116:117], v32 offset:32768\n" \
"ds_read_b64_tr_b16 v[118:119], v32 offset:33792\n" \
"v_mfma_f32_16x16x32_bf16 a[224:227], v[120:123], v[112:115], a[224:227]\n" \
"s_add_u32 m0, %[mB], 0x800\n" \
"s_add_u32 s85, s63, 0x10000\n" \
"v_mfma_f32_16x16x32_bf16 a[132:135], v[128:131], v[88:91], a[132:135]\n" \
"buffer_load_dwordx4 v27, s[68:71], s85 offen lds\n" \
"v_mfma_f32_16x16x32_bf16 a[132:135], v[124:127], v[84:87], a[132:135]\n" \
"ds_read_b64_tr_b16 v[120:121], v32 offset:40960\n" \
"ds_read_b64_tr_b16 v[122:123], v32 offset:41984\n" \
"v_mfma_f32_16x16x32_bf16 a[164:167], v[124:127], v[92:95], a[164:167]\n" \
"v_lshrrev_b32_e32 v234, 2, v212\n" \
"v_add_u32_e32 v5, 0xfffffc00, v237\n" \
"v_mfma_f32_16x16x32_bf16 a[164:167], v[128:131], v[96:99], a[164:167]\n" \
"s_add_u32 m0, %[mB], 0x4800\n" \
"s_add_u32 s84, s63, 0x10000\n" \
"v_mfma_f32_16x16x32_bf16 a[196:199], v[128:131], v[104:107], a[196:199]\n" \
"buffer_load_dwordx4 v27, s[72:75], s84 offen lds\n" \
"v_mfma_f32_16x16x32_bf16 a[196:199], v[124:127], v[100:103], a[196:199]\n" \
"v_add_u32_e32 v231, s97, v234\n" \
"v_lshrrev_b32_e32 v235, 5, v231\n" \
"v_mfma_f32_16x16x32_bf16 a[228:231], v[124:127], v[108:111], a[228:231]\n" \
"ds_read_b64_tr_b16 v[124:125], v33 offset:32768\n" \
"ds_read_b64_tr_b16 v[126:127], v33 offset:33792\n" \
"v_mfma_f32_16x16x32_bf16 a[228:231], v[128:131], v[112:115], a[228:231]\n" \
"ds_read_b64_tr_b16 v[128:129], v33 offset:40960\n" \
"ds_read_b64_tr_b16 v[130:131], v33 offset:41984\n" \
"v_mfma_f32_16x16x32_bf16 a[136:139], v[136:139], v[88:91], a[136:139]\n" \
"v_lshrrev_b32_e32 v237, 4, v231\n" \
"v_mul_u32_u24_e32 v239, 0x480, v241\n" \
"v_mfma_f32_16x16x32_bf16 a[136:139], v[132:135], v[84:87], a[136:139]\n" \
"v_lshlrev_b32_e32 v232, 4, v235\n" \
"v_and_b32_e32 v228, 12, v231\n" \
"v_mfma_f32_16x16x32_bf16 a[168:171], v[132:135], v[92:95], a[168:171]\n" \
"v_and_b32_e32 v238, 1, v237\n" \
"v_add_u32_e32 v233, v232, v228\n" \
"v_mfma_f32_16x16x32_bf16 a[168:171], v[136:139], v[96:99], a[168:171]\n" \
"v_lshlrev_b32_e32 v241, 1, v238\n" \
"v_lshrrev_b32_e32 v236, 1, v231\n" \
"v_mfma_f32_16x16x32_bf16 a[200:203], v[136:139], v[104:107], a[200:203]\n" \
"v_add_u32_e32 v240, v233, v241\n" \
"v_and_b32_e32 v245, 1, v236\n" \
"v_mfma_f32_16x16x32_bf16 a[200:203], v[132:135], v[100:103], a[200:203]\n" \
"v_add_u32_e32 v242, v240, v245\n" \
"v_add_u32_e32 v248, v239, v244\n" \
"v_mfma_f32_16x16x32_bf16 a[232:235], v[132:135], v[108:111], a[232:235]\n" \
"ds_read_b64_tr_b16 v[132:133], v34 offset:32768\n" \
"ds_read_b64_tr_b16 v[134:135], v34 offset:33792\n" \
"v_mfma_f32_16x16x32_bf16 a[232:235], v[136:139], v[112:115], a[232:235]\n" \
"ds_read_b64_tr_b16 v[136:137], v34 offset:40960\n" \
"ds_read_b64_tr_b16 v[138:139], v34 offset:41984\n" \
"v_mfma_f32_16x16x32_bf16 a[140:143], v[144:147], v[88:91], a[140:143]\n" \
"ds_read_b128 v[88:91], v29 offset:32768\n" \
"v_mul_u32_u24_e32 v246, 18, v242\n" \
"v_mfma_f32_16x16x32_bf16 a[140:143], v[140:143], v[84:87], a[140:143]\n" \
"ds_read_b128 v[84:87], v28 offset:32768\n" \
"v_add_u32_e32 v243, 16, v246\n" \
"v_mfma_f32_16x16x32_bf16 a[172:175], v[140:143], v[92:95], a[172:175]\n" \
"ds_read_b128 v[92:95], v28 offset:34816\n" \
"v_and_b32_e64 v239, 1, v231\n" \
"v_mfma_f32_16x16x32_bf16 a[172:175], v[144:147], v[96:99], a[172:175]\n" \
"ds_read_b128 v[96:99], v29 offset:34816\n" \
"v_add_u32_e32 v6, 0xfffff800, v248\n" \
"v_mfma_f32_16x16x32_bf16 a[204:207], v[144:147], v[104:107], a[204:207]\n" \
"ds_read_b128 v[104:107], v29 offset:36864\n" \
"v_add_u32_e64 v244, v243, v239\n" \
"v_mfma_f32_16x16x32_bf16 a[204:207], v[140:143], v[100:103], a[204:207]\n" \
"ds_read_b128 v[100:103], v28 offset:36864\n" \
"v_mul_u32_u24_e32 v248, 0x480, v244\n" \
"v_mfma_f32_16x16x32_bf16 a[236:239], v[140:143], v[108:111], a[236:239]\n" \
"ds_read_b64_tr_b16 v[140:141], v35 offset:32768\n" \
"ds_read_b64_tr_b16 v[142:143], v35 offset:33792\n" \
"v_mfma_f32_16x16x32_bf16 a[236:239], v[144:147], v[112:115], a[236:239]\n" \
"s_add_u32 m0, %[mA], 0x1800\n" \
"s_waitcnt lgkmcnt(12)\n" \
"s_nop 0\n" \
"v_mfma_f32_16x16x32_bf16 a[16:19], v[56:59], v[152:155], a[16:19]\n" \
"s_add_u32 s85, s62, 0xc000\n" \
"ds_read_b64_tr_b16 v[144:145], v35 offset:40960\n" \
"v_mfma_f32_16x16x32_bf16 a[16:19], v[52:55], v[148:151], a[16:19]\n" \
"buffer_load_dwordx4 v22, s[64:67], s85 offen lds\n" \
"v_mfma_f32_16x16x32_bf16 a[48:51], v[60:63], v[148:151], a[48:51]\n" \
"s_add_u32 m0, %[mA], 0x1c00\n" \
"s_add_u32 s84, s62, 0xe000\n" \
"v_mfma_f32_16x16x32_bf16 a[48:51], v[64:67], v[152:155], a[48:51]\n" \
"ds_read_b64_tr_b16 v[146:147], v35 offset:41984\n" \
"ds_read_b128 v[108:111], v28 offset:38912\n" \
"v_mfma_f32_16x16x32_bf16 a[80:83], v[72:75], v[152:155], a[80:83]\n" \
"buffer_load_dwordx4 v23, s[64:67], s84 offen lds\n" \
"v_mfma_f32_16x16x32_bf16 a[80:83], v[68:71], v[148:151], a[80:83]\n" \
"s_add_u32 m0, %[mB], 0xc00\n" \
"s_add_u32 s85, s63, 0x18000\n" \
"v_mfma_f32_16x16x32_bf16 a[112:115], v[76:79], v[148:151], a[112:115]\n" \
"ds_read_b128 v[112:115], v29 offset:38912\n" \
"v_add_u32_e64 v215, v250, v219\n" \
"v_mfma_f32_16x16x32_bf16 a[112:115], v[80:83], v[152:155], a[112:115]\n" \
"buffer_load_dwordx4 v27, s[68:71], s85 offen lds\n" \
"v_mfma_f32_16x16x32_bf16 a[20:23], v[56:59], v[160:163], a[20:23]\n" \
"s_add_u32 m0, %[mB], 0x4c00\n" \
"s_add_u32 s84, s63, 0x18000\n" \
"v_mfma_f32_16x16x32_bf16 a[20:23], v[52:55], v[156:159], a[20:23]\n" \
"v_add_u32_e32 v217, v248, v247\n" \
"v_and_b32_e32 v214, 16, v20\n" \
"v_mfma_f32_16x16x32_bf16 a[52:55], v[60:63], v[156:159], a[52:55]\n" \
"buffer_load_dwordx4 v27, s[72:75], s84 offen lds\n" \
"v_mfma_f32_16x16x32_bf16 a[52:55], v[64:67], v[160:163], a[52:55]\n" \
"v_add_u32_e32 v7, 0xfffff400, v215\n" \
"v_add_u32_e64 v9, v217, v214\n" \
"v_mfma_f32_16x16x32_bf16 a[84:87], v[72:75], v[160:163], a[84:87]\n" \
"v_and_b32_e32 v216, 3, v212\n" \
"v_lshlrev_b32_e32 v250, 3, v216\n" \
"v_mfma_f32_16x16x32_bf16 a[84:87], v[68:71], v[156:159], a[84:87]\n" \
"v_mul_u32_u24_e32 v220, 0x120, v234\n" \
"v_mul_u32_u24_e32 v223, 0x480, v222\n" \
"v_mfma_f32_16x16x32_bf16 a[116:119], v[76:79], v[156:159], a[116:119]\n" \
"v_add_u32_e32 v10, v220, v250\n" \
"v_add_u32_e32 v8, v223, v225\n" \
"v_mfma_f32_16x16x32_bf16 a[116:119], v[80:83], v[160:163], a[116:119]\n" \
"s_mov_b32 m0, %[mS]\n" \
"s_mov_b32 s86, s8\n" \
"v_mfma_f32_16x16x32_bf16 a[24:27], v[56:59], v[168:171], a[24:27]\n" \
"s_mov_b32 s87, s9\n" \
"s_lshl_b32 s99, %[w], 11\n" \
"v_mfma_f32_16x16x32_bf16 a[24:27], v[52:55], v[164:167], a[24:27]\n" \
"s_add_u32 s55, s98, s99\n" \
"v_mfma_f32_16x16x32_bf16 a[56:59], v[60:63], v[164:167], a[56:59]\n" \
"v_mfma_f32_16x16x32_bf16 a[56:59], v[64:67], v[168:171], a[56:59]\n" \
"v_mfma_f32_16x16x32_bf16 a[88:91], v[72:75], v[168:171], a[88:91]\n" \
"v_mfma_f32_16x16x32_bf16 a[88:91], v[68:71], v[164:167], a[88:91]\n" \
"s_mov_b32 exec_lo, 0x0\n" \
"s_mov_b32 exec_hi, 0x3ffff0\n" \
"global_load_lds_dword v188, s[86:87]\n" \
"s_mov_b64 exec, -1\n" \
"v_mfma_f32_16x16x32_bf16 a[120:123], v[76:79], v[164:167], a[120:123]\n" \
"s_mov_b32 m0, %[mS]\n" \
"s_mov_b32 s86, s18\n" \
"v_mfma_f32_16x16x32_bf16 a[120:123], v[80:83], v[168:171], a[120:123]\n" \
"s_mov_b32 s87, s19\n" \
"v_mfma_f32_16x16x32_bf16 a[28:31], v[56:59], v[176:179], a[28:31]\n" \
"v_mfma_f32_16x16x32_bf16 a[28:31], v[52:55], v[172:175], a[28:31]\n" \
"v_mfma_f32_16x16x32_bf16 a[60:63], v[60:63], v[172:175], a[60:63]\n" \
"s_mov_b32 exec_lo, 0x3ffff\n" \
"s_mov_b32 exec_hi, 0x0\n" \
"global_load_lds_dword v16, s[86:87]\n" \
"s_mov_b64 exec, -1\n" \
"v_mfma_f32_16x16x32_bf16 a[60:63], v[64:67], v[176:179], a[60:63]\n" \
"v_mfma_f32_16x16x32_bf16 a[92:95], v[72:75], v[176:179], a[92:95]\n" \
"v_mfma_f32_16x16x32_bf16 a[92:95], v[68:71], v[172:175], a[92:95]\n" \
"v_mfma_f32_16x16x32_bf16 a[124:127], v[76:79], v[172:175], a[124:127]\n" \
"v_mfma_f32_16x16x32_bf16 a[124:127], v[80:83], v[176:179], a[124:127]\n" \
"v_mfma_f32_16x16x32_bf16 a[0:3], v[120:123], v[56:59], a[0:3]\n" \
"v_mfma_f32_16x16x32_bf16 a[0:3], v[116:119], v[52:55], a[0:3]\n" \
"v_mfma_f32_16x16x32_bf16 a[32:35], v[116:119], v[60:63], a[32:35]\n" \
"v_mfma_f32_16x16x32_bf16 a[32:35], v[120:123], v[64:67], a[32:35]\n" \
"v_mfma_f32_16x16x32_bf16 a[64:67], v[120:123], v[72:75], a[64:67]\n" \
"v_mfma_f32_16x16x32_bf16 a[64:67], v[116:119], v[68:71], a[64:67]\n" \
"v_mfma_f32_16x16x32_bf16 a[96:99], v[116:119], v[76:79], a[96:99]\n" \
"v_mfma_f32_16x16x32_bf16 a[96:99], v[120:123], v[80:83], a[96:99]\n" \
"v_mfma_f32_16x16x32_bf16 a[4:7], v[128:131], v[56:59], a[4:7]\n" \
"v_mfma_f32_16x16x32_bf16 a[4:7], v[124:127], v[52:55], a[4:7]\n" \
"v_mfma_f32_16x16x32_bf16 a[36:39], v[124:127], v[60:63], a[36:39]\n" \
"v_mfma_f32_16x16x32_bf16 a[36:39], v[128:131], v[64:67], a[36:39]\n" \
"v_mfma_f32_16x16x32_bf16 a[68:71], v[128:131], v[72:75], a[68:71]\n" \
"v_mfma_f32_16x16x32_bf16 a[68:71], v[124:127], v[68:71], a[68:71]\n" \
"v_mfma_f32_16x16x32_bf16 a[100:103], v[124:127], v[76:79], a[100:103]\n" \
"v_mfma_f32_16x16x32_bf16 a[100:103], v[128:131], v[80:83], a[100:103]\n" \
"s_waitcnt lgkmcnt(0)\n" \
"s_nop 0\n" \
"v_mfma_f32_16x16x32_bf16 a[8:11], v[136:139], v[56:59], a[8:11]\n" \
"v_mfma_f32_16x16x32_bf16 a[8:11], v[132:135], v[52:55], a[8:11]\n" \
"v_mfma_f32_16x16x32_bf16 a[40:43], v[132:135], v[60:63], a[40:43]\n" \
"v_mfma_f32_16x16x32_bf16 a[40:43], v[136:139], v[64:67], a[40:43]\n" \
"v_mfma_f32_16x16x32_bf16 a[72:75], v[136:139], v[72:75], a[72:75]\n" \
"v_mfma_f32_16x16x32_bf16 a[72:75], v[132:135], v[68:71], a[72:75]\n" \
"v_mfma_f32_16x16x32_bf16 a[104:107], v[132:135], v[76:79], a[104:107]\n" \
"v_mfma_f32_16x16x32_bf16 a[104:107], v[136:139], v[80:83], a[104:107]\n" \
"v_mfma_f32_16x16x32_bf16 a[12:15], v[144:147], v[56:59], a[12:15]\n" \
"s_add_u32 s62, s22, 0x380\n" \
"s_and_b32 s62, s62, 0x3ff\n" \
"v_mfma_f32_16x16x32_bf16 a[12:15], v[140:143], v[52:55], a[12:15]\n" \
"s_lshl_b32 s63, s62, 12\n" \
"v_mfma_f32_16x16x32_bf16 a[44:47], v[140:143], v[60:63], a[44:47]\n" \
"v_mfma_f32_16x16x32_bf16 a[44:47], v[144:147], v[64:67], a[44:47]\n" \
"v_mfma_f32_16x16x32_bf16 a[76:79], v[144:147], v[72:75], a[76:79]\n" \
"v_mfma_f32_16x16x32_bf16 a[76:79], v[140:143], v[68:71], a[76:79]\n" \
"v_mfma_f32_16x16x32_bf16 a[108:111], v[140:143], v[76:79], a[108:111]\n" \
"v_mfma_f32_16x16x32_bf16 a[108:111], v[144:147], v[80:83], a[108:111]\n" \
"s_waitcnt vmcnt(2)\n" \
"s_barrier\n" \
"s_mov_b64 exec, 1\n" \
"buffer_store_dword v45, off, s[80:83], s38 offset:64 sc1\n" \
"s_mov_b64 exec, -1\n" \
"v_mfma_f32_16x16x32_bf16 a[128:131], v[120:123], v[88:91], a[128:131]\n" \
"s_add_u32 m0, %[mA], 0x8000\n" \
"s_add_u32 s85, s62, 0x0\n" \
"v_mfma_f32_16x16x32_bf16 a[128:131], v[116:119], v[84:87], a[128:131]\n" \
"ds_read_b128 v[56:59], v29 offset:16384\n" \
"ds_read_b128 v[52:55], v28 offset:16384\n" \
"v_mfma_f32_16x16x32_bf16 a[160:163], v[116:119], v[92:95], a[160:163]\n" \
"buffer_load_dwordx4 v22, s[64:67], s85 offen lds\n" \
"v_mfma_f32_16x16x32_bf16 a[160:163], v[120:123], v[96:99], a[160:163]\n" \
"s_add_u32 m0, %[mA], 0x8400\n" \
"s_add_u32 s84, s62, 0x2000\n" \
"v_mfma_f32_16x16x32_bf16 a[192:195], v[120:123], v[104:107], a[192:195]\n" \
"ds_read_b128 v[60:63], v28 offset:18432\n" \
"ds_read_b128 v[64:67], v29 offset:18432\n" \
"v_mfma_f32_16x16x32_bf16 a[192:195], v[116:119], v[100:103], a[192:195]\n" \
"buffer_load_dwordx4 v23, s[64:67], s84 offen lds\n" \
"v_mfma_f32_16x16x32_bf16 a[224:227], v[116:119], v[108:111], a[224:227]\n" \
"ds_read_b64_tr_b16 v[116:117], v32 offset:0\n" \
"ds_read_b64_tr_b16 v[118:119], v32 offset:1024\n" \
"v_mfma_f32_16x16x32_bf16 a[224:227], v[120:123], v[112:115], a[224:227]\n" \
"s_add_u32 m0, %[mB], 0x8000\n" \
"s_add_u32 s85, s63, 0x0\n" \
"v_mfma_f32_16x16x32_bf16 a[132:135], v[128:131], v[88:91], a[132:135]\n" \
"buffer_load_dwordx4 v26, s[68:71], s85 offen lds\n" \
"v_mfma_f32_16x16x32_bf16 a[132:135], v[124:127], v[84:87], a[132:135]\n" \
"ds_read_b64_tr_b16 v[120:121], v32 offset:8192\n" \
"ds_read_b64_tr_b16 v[122:123], v32 offset:9216\n" \
"v_mfma_f32_16x16x32_bf16 a[164:167], v[124:127], v[92:95], a[164:167]\n" \
"ds_read_b128 v[72:75], v29 offset:20480\n" \
"ds_read_b128 v[68:71], v28 offset:20480\n" \
"v_mfma_f32_16x16x32_bf16 a[164:167], v[128:131], v[96:99], a[164:167]\n" \
"s_add_u32 m0, %[mB], 0xc000\n" \
"s_add_u32 s84, s63, 0x0\n" \
"v_mfma_f32_16x16x32_bf16 a[196:199], v[128:131], v[104:107], a[196:199]\n" \
"buffer_load_dwordx4 v26, s[72:75], s84 offen lds\n" \
"v_mfma_f32_16x16x32_bf16 a[196:199], v[124:127], v[100:103], a[196:199]\n" \
"ds_read_b128 v[76:79], v28 offset:22528\n" \
"ds_read_b128 v[80:83], v29 offset:22528\n" \
"v_mfma_f32_16x16x32_bf16 a[228:231], v[124:127], v[108:111], a[228:231]\n" \
"ds_read_b64_tr_b16 v[124:125], v33 offset:0\n" \
"ds_read_b64_tr_b16 v[126:127], v33 offset:1024\n" \
"v_mfma_f32_16x16x32_bf16 a[228:231], v[128:131], v[112:115], a[228:231]\n" \
"s_add_u32 m0, %[mA], 0x8800\n" \
"s_add_u32 s85, s62, 0x4000\n" \
"v_mfma_f32_16x16x32_bf16 a[136:139], v[136:139], v[88:91], a[136:139]\n" \
"buffer_load_dwordx4 v22, s[64:67], s85 offen lds\n" \
"v_mfma_f32_16x16x32_bf16 a[136:139], v[132:135], v[84:87], a[136:139]\n" \
"ds_read_b64_tr_b16 v[128:129], v33 offset:8192\n" \
"ds_read_b64_tr_b16 v[130:131], v33 offset:9216\n" \
"v_mfma_f32_16x16x32_bf16 a[168:171], v[132:135], v[92:95], a[168:171]\n" \
"v_mov_b32_e64 v45, %[tag]\n" \
"v_mfma_f32_16x16x32_bf16 a[168:171], v[136:139], v[96:99], a[168:171]\n" \
"s_add_u32 m0, %[mA], 0x8c00\n" \
"s_add_u32 s84, s62, 0x6000\n" \
"v_mfma_f32_16x16x32_bf16 a[200:203], v[136:139], v[104:107], a[200:203]\n" \
"buffer_load_dwordx4 v23, s[64:67], s84 offen lds\n" \
"v_mfma_f32_16x16x32_bf16 a[200:203], v[132:135], v[100:103], a[200:203]\n" \
"v_mfma_f32_16x16x32_bf16 a[232:235], v[132:135], v[108:111], a[232:235]\n" \
"ds_read_b64_tr_b16 v[132:133], v34 offset:0\n" \
"ds_read_b64_tr_b16 v[134:135], v34 offset:1024\n" \
"v_mfma_f32_16x16x32_bf16 a[232:235], v[136:139], v[112:115], a[232:235]\n" \
"s_add_u32 m0, %[mB], 0x8400\n" \
"s_add_u32 s85, s63, 0x8000\n" \
"v_mfma_f32_16x16x32_bf16 a[140:143], v[144:147], v[88:91], a[140:143]\n" \
"buffer_load_dwordx4 v26, s[68:71], s85 offen lds\n" \
"v_mfma_f32_16x16x32_bf16 a[140:143], v[140:143], v[84:87], a[140:143]\n" \
"ds_read_b64_tr_b16 v[136:137], v34 offset:8192\n" \
"ds_read_b64_tr_b16 v[138:139], v34 offset:9216\n" \
"v_mfma_f32_16x16x32_bf16 a[172:175], v[140:143], v[92:95], a[172:175]\n" \
"v_mfma_f32_16x16x32_bf16 a[172:175], v[144:147], v[96:99], a[172:175]\n" \
"s_add_u32 m0, %[mB], 0xc400\n" \
"s_add_u32 s84, s63, 0x8000\n" \
"v_mfma_f32_16x16x32_bf16 a[204:207], v[144:147], v[104:107], a[204:207]\n" \
"buffer_load_dwordx4 v26, s[72:75], s84 offen lds\n" \
"v_mfma_f32_16x16x32_bf16 a[204:207], v[140:143], v[100:103], a[204:207]\n" \
"v_mfma_f32_16x16x32_bf16 a[236:239], v[140:143], v[108:111], a[236:239]\n" \
"ds_read_b64_tr_b16 v[140:141], v35 offset:0\n" \
"ds_read_b64_tr_b16 v[142:143], v35 offset:1024\n" \
"v_mfma_f32_16x16x32_bf16 a[236:239], v[144:147], v[112:115], a[236:239]\n" \
"s_add_u32 m0, %[mA], 0x9000\n" \
"s_add_u32 s85, s62, 0x8000\n" \
"v_mfma_f32_16x16x32_bf16 a[144:147], v[88:91], v[152:155], a[144:147]\n" \
"buffer_load_dwordx4 v22, s[64:67], s85 offen lds\n" \
"v_mfma_f32_16x16x32_bf16 a[144:147], v[84:87], v[148:151], a[144:147]\n" \
"ds_read_b64_tr_b16 v[144:145], v35 offset:8192\n" \
"ds_read_b64_tr_b16 v[146:147], v35 offset:9216\n" \
"v_mfma_f32_16x16x32_bf16 a[176:179], v[92:95], v[148:151], a[176:179]\n" \
"v_mfma_f32_16x16x32_bf16 a[176:179], v[96:99], v[152:155], a[176:179]\n" \
"s_add_u32 m0, %[mA], 0x9400\n" \
"s_add_u32 s84, s62, 0xa000\n" \
"v_mfma_f32_16x16x32_bf16 a[208:211], v[104:107], v[152:155], a[208:211]\n" \
"buffer_load_dwordx4 v23, s[64:67], s84 offen lds\n" \
"v_mfma_f32_16x16x32_bf16 a[208:211], v[100:103], v[148:151], a[208:211]\n" \
"v_mfma_f32_16x16x32_bf16 a[240:243], v[108:111], v[148:151], a[240:243]\n" \
"ds_read_b64_tr_b16 v[148:149], v32 offset:16384\n" \
"ds_read_b64_tr_b16 v[150:151], v32 offset:17408\n" \
"v_mfma_f32_16x16x32_bf16 a[240:243], v[112:115], v[152:155], a[240:243]\n" \
"s_add_u32 m0, %[mB], 0x8800\n" \
"s_add_u32 s85, s63, 0x10000\n" \
"v_mfma_f32_16x16x32_bf16 a[148:151], v[88:91], v[160:163], a[148:151]\n" \
"L_pf3:\n" \
"s_cbranch_execz L_pf7\n" \
"s_nop 0\n" \
"buffer_load_dwordx4 v27, s[68:71], s85 offen lds\n" \
"v_mfma_f32_16x16x32_bf16 a[148:151], v[84:87], v[156:159], a[148:151]\n" \
"ds_read_b64_tr_b16 v[152:153], v32 offset:24576\n" \
"ds_read_b64_tr_b16 v[154:155], v32 offset:25600\n" \
"v_mfma_f32_16x16x32_bf16 a[180:183], v[92:95], v[156:159], a[180:183]\n" \
"v_mfma_f32_16x16x32_bf16 a[180:183], v[96:99], v[160:163], a[180:183]\n" \
"s_add_u32 m0, %[mB], 0xc800\n" \
"s_add_u32 s84, s63, 0x10000\n" \
"v_mfma_f32_16x16x32_bf16 a[212:215], v[104:107], v[160:163], a[212:215]\n" \
"buffer_load_dwordx4 v27, s[72:75], s84 offen lds\n" \
"v_mfma_f32_16x16x32_bf16 a[212:215], v[100:103], v[156:159], a[212:215]\n" \
"v_mfma_f32_16x16x32_bf16 a[244:247], v[108:111], v[156:159], a[244:247]\n" \
"ds_read_b64_tr_b16 v[156:157], v33 offset:16384\n" \
"ds_read_b64_tr_b16 v[158:159], v33 offset:17408\n" \
"v_mfma_f32_16x16x32_bf16 a[244:247], v[112:115], v[160:163], a[244:247]\n" \
"ds_read_b64_tr_b16 v[160:161], v33 offset:24576\n" \
"ds_read_b64_tr_b16 v[162:163], v33 offset:25600\n" \
"v_mfma_f32_16x16x32_bf16 a[152:155], v[88:91], v[168:171], a[152:155]\n" \
"v_mfma_f32_16x16x32_bf16 a[152:155], v[84:87], v[164:167], a[152:155]\n" \
"v_mfma_f32_16x16x32_bf16 a[184:187], v[92:95], v[164:167], a[184:187]\n" \
"v_mfma_f32_16x16x32_bf16 a[184:187], v[96:99], v[168:171], a[184:187]\n" \
"v_mfma_f32_16x16x32_bf16 a[216:219], v[104:107], v[168:171], a[216:219]\n" \
"v_mfma_f32_16x16x32_bf16 a[216:219], v[100:103], v[164:167], a[216:219]\n" \
"v_mfma_f32_16x16x32_bf16 a[248:251], v[108:111], v[164:167], a[248:251]\n" \
"ds_read_b64_tr_b16 v[164:165], v34 offset:16384\n" \
"ds_read_b64_tr_b16 v[166:167], v34 offset:17408\n" \
"v_mfma_f32_16x16x32_bf16 a[248:251], v[112:115], v[168:171], a[248:251]\n" \
"ds_read_b64_tr_b16 v[168:169], v34 offset:24576\n" \
"ds_read_b64_tr_b16 v[170:171], v34 offset:25600\n" \
"v_mfma_f32_16x16x32_bf16 a[156:159], v[88:91], v[176:179], a[156:159]\n" \
"ds_read_b128 v[88:91], v29 offset:0\n" \
"v_mfma_f32_16x16x32_bf16 a[156:159], v[84:87], v[172:175], a[156:159]\n" \
"ds_read_b128 v[84:87], v28 offset:0\n" \
"v_mfma_f32_16x16x32_bf16 a[188:191], v[92:95], v[172:175], a[188:191]\n" \
"ds_read_b128 v[92:95], v28 offset:2048\n" \
"v_mfma_f32_16x16x32_bf16 a[188:191], v[96:99], v[176:179], a[188:191]\n" \
"ds_read_b128 v[96:99], v29 offset:2048\n" \
"v_mfma_f32_16x16x32_bf16 a[220:223], v[104:107], v[176:179], a[220:223]\n" \
"ds_read_b128 v[104:107], v29 offset:4096\n" \
"v_mfma_f32_16x16x32_bf16 a[220:223], v[100:103], v[172:175], a[220:223]\n" \
"ds_read_b128 v[100:103], v28 offset:4096\n" \
"v_mfma_f32_16x16x32_bf16 a[252:255], v[108:111], v[172:175], a[252:255]\n" \
"ds_read_b64_tr_b16 v[172:173], v35 offset:16384\n" \
"ds_read_b64_tr_b16 v[174:175], v35 offset:17408\n" \
"v_mfma_f32_16x16x32_bf16 a[252:255], v[112:115], v[176:179], a[252:255]\n" \
"s_add_u32 m0, %[mA], 0x9800\n" \
"s_waitcnt lgkmcnt(12)\n" \
"s_nop 0\n" \
"v_mfma_f32_16x16x32_bf16 a[0:3], v[120:123], v[56:59], a[0:3]\n" \
"s_add_u32 s85, s62, 0xc000\n" \
"ds_read_b64_tr_b16 v[176:177], v35 offset:24576\n" \
"v_mfma_f32_16x16x32_bf16 a[0:3], v[116:119], v[52:55], a[0:3]\n" \
"buffer_load_dwordx4 v22, s[64:67], s85 offen lds\n" \
"v_mfma_f32_16x16x32_bf16 a[32:35], v[116:119], v[60:63], a[32:35]\n" \
"s_add_u32 m0, %[mA], 0x9c00\n" \
"s_add_u32 s84, s62, 0xe000\n" \
"v_mfma_f32_16x16x32_bf16 a[32:35], v[120:123], v[64:67], a[32:35]\n" \
"ds_read_b64_tr_b16 v[178:179], v35 offset:25600\n" \
"ds_read_b128 v[108:111], v28 offset:6144\n" \
"v_mfma_f32_16x16x32_bf16 a[64:67], v[120:123], v[72:75], a[64:67]\n" \
"buffer_load_dwordx4 v23, s[64:67], s84 offen lds\n" \
"v_mfma_f32_16x16x32_bf16 a[64:67], v[116:119], v[68:71], a[64:67]\n" \
"s_add_u32 m0, %[mB], 0x8c00\n" \
"s_add_u32 s85, s63, 0x18000\n" \
"v_mfma_f32_16x16x32_bf16 a[96:99], v[116:119], v[76:79], a[96:99]\n" \
"ds_read_b128 v[112:115], v29 offset:6144\n" \
"s_mov_b32 s62, s23\n" \
"v_mfma_f32_16x16x32_bf16 a[96:99], v[120:123], v[80:83], a[96:99]\n" \
"buffer_load_dwordx4 v27, s[68:71], s85 offen lds\n" \
"v_mfma_f32_16x16x32_bf16 a[4:7], v[128:131], v[56:59], a[4:7]\n" \
"s_add_u32 m0, %[mB], 0xcc00\n" \
"s_add_u32 s84, s63, 0x18000\n" \
"v_mfma_f32_16x16x32_bf16 a[4:7], v[124:127], v[52:55], a[4:7]\n" \
"s_mov_b32 s68, s10\n" \
"s_and_b32 s69, s11, 0xffff\n" \
"v_mfma_f32_16x16x32_bf16 a[36:39], v[124:127], v[60:63], a[36:39]\n" \
"buffer_load_dwordx4 v27, s[72:75], s84 offen lds\n" \
"s_mov_b32 s73, s52\n" \
"s_add_u32 s74, s52, 0x4000\n" \
"s_add_u32 s75, s52, 0x8000\n" \
"v_mfma_f32_16x16x32_bf16 a[36:39], v[128:131], v[64:67], a[36:39]\n" \
"s_lshl_b32 s72, s62, 6\n" \
"s_add_u32 s72, s72, s37\n" \
"v_mfma_f32_16x16x32_bf16 a[68:71], v[128:131], v[72:75], a[68:71]\n" \
"v_bfe_u32 v44, v20, 2, 5\n" \
"s_mov_b32 s70, 0x480000\n" \
"v_mfma_f32_16x16x32_bf16 a[68:71], v[124:127], v[68:71], a[68:71]\n" \
"buffer_load_dword v44, v44, s[80:83], s39 offen offset:64 sc1\n" \
"v_mfma_f32_16x16x32_bf16 a[100:103], v[124:127], v[76:79], a[100:103]\n" \
"v_add_u32_e32 v22, s61, v28\n" \
"v_add_u32_e64 v23, s61, v29\n" \
"v_mfma_f32_16x16x32_bf16 a[100:103], v[128:131], v[80:83], a[100:103]\n" \
"s_mov_b32 m0, %[mS]\n" \
"s_mov_b32 s86, s18\n" \
"v_mfma_f32_16x16x32_bf16 a[8:11], v[136:139], v[56:59], a[8:11]\n" \
"s_mov_b32 s87, s19\n" \
"v_mfma_f32_16x16x32_bf16 a[8:11], v[132:135], v[52:55], a[8:11]\n" \
"v_mfma_f32_16x16x32_bf16 a[40:43], v[132:135], v[60:63], a[40:43]\n" \
"v_mfma_f32_16x16x32_bf16 a[40:43], v[136:139], v[64:67], a[40:43]\n" \
"v_mfma_f32_16x16x32_bf16 a[72:75], v[136:139], v[72:75], a[72:75]\n" \
"v_mfma_f32_16x16x32_bf16 a[72:75], v[132:135], v[68:71], a[72:75]\n" \
"s_mov_b32 exec_lo, 0xfffc0000\n" \
"s_mov_b32 exec_hi, 0xf\n" \
"global_load_lds_dword v16, s[86:87]\n" \
"s_mov_b64 exec, -1\n" \
"v_mfma_f32_16x16x32_bf16 a[104:107], v[132:135], v[76:79], a[104:107]\n" \
"s_mov_b32 m0, %[mS]\n" \
"s_mov_b32 s86, s16\n" \
"v_mfma_f32_16x16x32_bf16 a[104:107], v[136:139], v[80:83], a[104:107]\n" \
"s_mov_b32 s87, s17\n" \
"v_mfma_f32_16x16x32_bf16 a[12:15], v[144:147], v[56:59], a[12:15]\n" \
"v_mfma_f32_16x16x32_bf16 a[12:15], v[140:143], v[52:55], a[12:15]\n" \
"v_mfma_f32_16x16x32_bf16 a[44:47], v[140:143], v[60:63], a[44:47]\n" \
"s_mov_b32 exec_lo, 0x0\n" \
"s_mov_b32 exec_hi, 0x3f0\n" \
"global_load_lds_dword v16, s[86:87]\n" \
"s_mov_b64 exec, -1\n" \
"v_mfma_f32_16x16x32_bf16 a[44:47], v[144:147], v[64:67], a[44:47]\n" \
"v_mfma_f32_16x16x32_bf16 a[76:79], v[144:147], v[72:75], a[76:79]\n" \
"v_mfma_f32_16x16x32_bf16 a[76:79], v[140:143], v[68:71], a[76:79]\n" \
"v_mfma_f32_16x16x32_bf16 a[108:111], v[140:143], v[76:79], a[108:111]\n" \
"v_mfma_f32_16x16x32_bf16 a[108:111], v[144:147], v[80:83], a[108:111]\n" \
"v_mfma_f32_16x16x32_bf16 a[16:19], v[56:59], v[152:155], a[16:19]\n" \
"v_mfma_f32_16x16x32_bf16 a[16:19], v[52:55], v[148:151], a[16:19]\n" \
"v_mfma_f32_16x16x32_bf16 a[48:51], v[60:63], v[148:151], a[48:51]\n" \
"v_mfma_f32_16x16x32_bf16 a[48:51], v[64:67], v[152:155], a[48:51]\n" \
"v_mfma_f32_16x16x32_bf16 a[80:83], v[72:75], v[152:155], a[80:83]\n" \
"v_mfma_f32_16x16x32_bf16 a[80:83], v[68:71], v[148:151], a[80:83]\n" \
"v_mfma_f32_16x16x32_bf16 a[112:115], v[76:79], v[148:151], a[112:115]\n" \
"v_mfma_f32_16x16x32_bf16 a[112:115], v[80:83], v[152:155], a[112:115]\n" \
"v_mfma_f32_16x16x32_bf16 a[20:23], v[56:59], v[160:163], a[20:23]\n" \
"v_mfma_f32_16x16x32_bf16 a[20:23], v[52:55], v[156:159], a[20:23]\n" \
"v_mfma_f32_16x16x32_bf16 a[52:55], v[60:63], v[156:159], a[52:55]\n" \
"v_mfma_f32_16x16x32_bf16 a[52:55], v[64:67], v[160:163], a[52:55]\n" \
"v_mfma_f32_16x16x32_bf16 a[84:87], v[72:75], v[160:163], a[84:87]\n" \
"v_mfma_f32_16x16x32_bf16 a[84:87], v[68:71], v[156:159], a[84:87]\n" \
"v_mfma_f32_16x16x32_bf16 a[116:119], v[76:79], v[156:159], a[116:119]\n" \
"v_mfma_f32_16x16x32_bf16 a[116:119], v[80:83], v[160:163], a[116:119]\n" \
"s_waitcnt lgkmcnt(0)\n" \
"v_mfma_f32_16x16x32_bf16 a[24:27], v[56:59], v[168:171], a[24:27]\n" \
"v_mfma_f32_16x16x32_bf16 a[24:27], v[52:55], v[164:167], a[24:27]\n" \
"v_mfma_f32_16x16x32_bf16 a[56:59], v[60:63], v[164:167], a[56:59]\n" \
"v_mfma_f32_16x16x32_bf16 a[56:59], v[64:67], v[168:171], a[56:59]\n" \
"v_mfma_f32_16x16x32_bf16 a[88:91], v[72:75], v[168:171], a[88:91]\n" \
"v_mfma_f32_16x16x32_bf16 a[88:91], v[68:71], v[164:167], a[88:91]\n" \
"v_mfma_f32_16x16x32_bf16 a[120:123], v[76:79], v[164:167], a[120:123]\n" \
"v_mfma_f32_16x16x32_bf16 a[120:123], v[80:83], v[168:171], a[120:123]\n" \
"v_mfma_f32_16x16x32_bf16 a[28:31], v[56:59], v[176:179], a[28:31]\n" \
"s_waitcnt vmcnt(2)\n" \
"v_cmp_ne_u32_e32 vcc, %[tag], v44\n" \
"v_mfma_f32_16x16x32_bf16 a[28:31], v[52:55], v[172:175], a[28:31]\n" \
"v_mfma_f32_16x16x32_bf16 a[60:63], v[60:63], v[172:175], a[60:63]\n" \
"v_mfma_f32_16x16x32_bf16 a[60:63], v[64:67], v[176:179], a[60:63]\n" \
"v_mfma_f32_16x16x32_bf16 a[92:95], v[72:75], v[176:179], a[92:95]\n" \
"v_mfma_f32_16x16x32_bf16 a[92:95], v[68:71], v[172:175], a[92:95]\n" \
"s_cbranch_vccnz L_pl_%=\n" \
"L_rl_%=:\n" \
"s_lshl_b32 s63, s62, 12\n" \
"v_mfma_f32_16x16x32_bf16 a[124:127], v[76:79], v[172:175], a[124:127]\n" \
"v_mfma_f32_16x16x32_bf16 a[124:127], v[80:83], v[176:179], a[124:127]\n" \
"s_barrier\n" \
"v_mfma_f32_16x16x32_bf16 a[144:147], v[88:91], v[152:155], a[144:147]\n" \
"s_add_u32 m0, s40, 0x0\n" \
"s_add_u32 s85, s72, 0x0\n" \
"v_mfma_f32_16x16x32_bf16 a[144:147], v[84:87], v[148:151], a[144:147]\n" \
"buffer_load_dwordx4 v20, s[80:83], s85 offen sc1 lds\n" \
"v_mfma_f32_16x16x32_bf16 a[176:179], v[92:95], v[148:151], a[176:179]\n" \
"s_add_u32 m0, s40, 0x1000\n" \
"s_add_u32 s84, s72, 0x1000\n" \
"v_mfma_f32_16x16x32_bf16 a[176:179], v[96:99], v[152:155], a[176:179]\n" \
"ds_read_b128 v[56:59], v29 offset:49152\n" \
"ds_read_b128 v[52:55], v28 offset:49152\n" \
"v_mfma_f32_16x16x32_bf16 a[208:211], v[104:107], v[152:155], a[208:211]\n" \
"buffer_load_dwordx4 v20, s[80:83], s84 offen sc1 lds\n" \
"v_mfma_f32_16x16x32_bf16 a[208:211], v[100:103], v[148:151], a[208:211]\n" \
"ds_read_b128 v[60:63], v28 offset:51200\n" \
"ds_read_b128 v[64:67], v29 offset:51200\n" \
"v_mfma_f32_16x16x32_bf16 a[240:243], v[108:111], v[148:151], a[240:243]\n" \
"s_add_u32 m0, s40, 0x3000\n" \
"s_add_u32 s85, s62, 0x0\n" \
"v_mfma_f32_16x16x32_bf16 a[240:243], v[112:115], v[152:155], a[240:243]\n" \
"buffer_load_dwordx4 v21, s[76:79], s85 offen lds\n" \
"v_mfma_f32_16x16x32_bf16 a[148:151], v[88:91], v[160:163], a[148:151]\n" \
"ds_read_b64_tr_b16 v[152:153], v32 offset:57344\n" \
"ds_read_b64_tr_b16 v[154:155], v32 offset:58368\n" \
"v_mfma_f32_16x16x32_bf16 a[148:151], v[84:87], v[156:159], a[148:151]\n" \
"ds_read_b64_tr_b16 v[148:149], v32 offset:49152\n" \
"ds_read_b64_tr_b16 v[150:151], v32 offset:50176\n" \
"v_mfma_f32_16x16x32_bf16 a[180:183], v[92:95], v[156:159], a[180:183]\n" \
"s_add_u32 m0, %[mA], 0x0\n" \
"s_add_u32 s84, s62, 0x0\n" \
"s_nop 0\n" \
"v_mfma_f32_16x16x32_bf16 a[180:183], v[96:99], v[160:163], a[180:183]\n" \
"buffer_load_dwordx4 v24, s[76:79], s84 offen lds\n" \
"v_mfma_f32_16x16x32_bf16 a[212:215], v[104:107], v[160:163], a[212:215]\n" \
"ds_read_b128 v[72:75], v29 offset:53248\n" \
"ds_read_b128 v[68:71], v28 offset:53248\n" \
"v_mfma_f32_16x16x32_bf16 a[212:215], v[100:103], v[156:159], a[212:215]\n" \
"s_add_u32 m0, %[mA], 0x400\n" \
"s_add_u32 s85, s62, 0x2400\n" \
"v_mfma_f32_16x16x32_bf16 a[244:247], v[108:111], v[156:159], a[244:247]\n" \
"buffer_load_dwordx4 v25, s[76:79], s85 offen lds\n" \
"v_mfma_f32_16x16x32_bf16 a[244:247], v[112:115], v[160:163], a[244:247]\n" \
"ds_read_b128 v[76:79], v28 offset:55296\n" \
"ds_read_b128 v[80:83], v29 offset:55296\n" \
"v_mfma_f32_16x16x32_bf16 a[152:155], v[88:91], v[168:171], a[152:155]\n" \
"ds_read_b64_tr_b16 v[160:161], v33 offset:57344\n" \
"ds_read_b64_tr_b16 v[162:163], v33 offset:58368\n" \
"v_mfma_f32_16x16x32_bf16 a[152:155], v[84:87], v[164:167], a[152:155]\n" \
"s_add_u32 m0, %[mB], 0x0\n" \
"s_add_u32 s84, s63, 0x0\n" \
"v_mfma_f32_16x16x32_bf16 a[184:187], v[92:95], v[164:167], a[184:187]\n" \
"buffer_load_dwordx4 v26, s[68:71], s84 offen lds\n" \
"v_mfma_f32_16x16x32_bf16 a[184:187], v[96:99], v[168:171], a[184:187]\n" \
"ds_read_b64_tr_b16 v[156:157], v33 offset:49152\n" \
"ds_read_b64_tr_b16 v[158:159], v33 offset:50176\n" \
"v_mfma_f32_16x16x32_bf16 a[216:219], v[104:107], v[168:171], a[216:219]\n" \
"v_mfma_f32_16x16x32_bf16 a[216:219], v[100:103], v[164:167], a[216:219]\n" \
"s_add_u32 m0, %[mA], 0x800\n" \
"s_add_u32 s85, s62, 0x4800\n" \
"v_mfma_f32_16x16x32_bf16 a[248:251], v[108:111], v[164:167], a[248:251]\n" \
"buffer_load_dwordx4 v24, s[76:79], s85 offen lds\n" \
"v_mfma_f32_16x16x32_bf16 a[248:251], v[112:115], v[168:171], a[248:251]\n" \
"ds_read_b64_tr_b16 v[168:169], v34 offset:57344\n" \
"ds_read_b64_tr_b16 v[170:171], v34 offset:58368\n" \
"v_mfma_f32_16x16x32_bf16 a[156:159], v[88:91], v[176:179], a[156:159]\n" \
"s_add_u32 m0, %[mA], 0xc00\n" \
"s_add_u32 s84, s62, 0x6c00\n" \
"v_mfma_f32_16x16x32_bf16 a[156:159], v[84:87], v[172:175], a[156:159]\n" \
"buffer_load_dwordx4 v25, s[76:79], s84 offen lds\n" \
"v_mfma_f32_16x16x32_bf16 a[188:191], v[92:95], v[172:175], a[188:191]\n" \
"ds_read_b64_tr_b16 v[164:165], v34 offset:49152\n" \
"ds_read_b64_tr_b16 v[166:167], v34 offset:50176\n" \
"v_mfma_f32_16x16x32_bf16 a[188:191], v[96:99], v[176:179], a[188:191]\n" \
"v_mfma_f32_16x16x32_bf16 a[220:223], v[104:107], v[176:179], a[220:223]\n" \
"s_add_u32 m0, %[mB], 0x400\n" \
"s_add_u32 s85, s63, 0x8000\n" \
"v_mfma_f32_16x16x32_bf16 a[220:223], v[100:103], v[172:175], a[220:223]\n" \
"buffer_load_dwordx4 v26, s[68:71], s85 offen lds\n" \
"v_mfma_f32_16x16x32_bf16 a[252:255], v[108:111], v[172:175], a[252:255]\n" \
"ds_read_b64_tr_b16 v[172:173], v35 offset:49152\n" \
"ds_read_b64_tr_b16 v[174:175], v35 offset:50176\n" \
"v_mfma_f32_16x16x32_bf16 a[252:255], v[112:115], v[176:179], a[252:255]\n" \
"ds_read_b64_tr_b16 v[176:177], v35 offset:57344\n" \
"ds_read_b64_tr_b16 v[178:179], v35 offset:58368\n" \
"v_mfma_f32_16x16x32_bf16 a[128:131], v[120:123], v[88:91], a[128:131]\n" \
"s_add_u32 m0, %[mA], 0x1000\n" \
"s_add_u32 s84, s62, 0x9000\n" \
"v_mfma_f32_16x16x32_bf16 a[128:131], v[116:119], v[84:87], a[128:131]\n" \
"buffer_load_dwordx4 v24, s[76:79], s84 offen lds\n" \
"v_mfma_f32_16x16x32_bf16 a[160:163], v[116:119], v[92:95], a[160:163]\n" \
"v_mfma_f32_16x16x32_bf16 a[160:163], v[120:123], v[96:99], a[160:163]\n" \
"s_add_u32 m0, %[mA], 0x1400\n" \
"s_add_u32 s85, s62, 0xb400\n" \
"v_mfma_f32_16x16x32_bf16 a[192:195], v[120:123], v[104:107], a[192:195]\n" \
"buffer_load_dwordx4 v25, s[76:79], s85 offen lds\n" \
"v_mfma_f32_16x16x32_bf16 a[192:195], v[116:119], v[100:103], a[192:195]\n" \
"v_mfma_f32_16x16x32_bf16 a[224:227], v[116:119], v[108:111], a[224:227]\n" \
"ds_read_b64_tr_b16 v[116:117], v32 offset:32768\n" \
"ds_read_b64_tr_b16 v[118:119], v32 offset:33792\n" \
"v_mfma_f32_16x16x32_bf16 a[224:227], v[120:123], v[112:115], a[224:227]\n" \
"s_add_u32 m0, %[mB], 0x800\n" \
"s_add_u32 s84, s63, 0x10000\n" \
"v_mfma_f32_16x16x32_bf16 a[132:135], v[128:131], v[88:91], a[132:135]\n" \
"buffer_load_dwordx4 v27, s[68:71], s84 offen lds\n" \
"v_mfma_f32_16x16x32_bf16 a[132:135], v[124:127], v[84:87], a[132:135]\n" \
"ds_read_b64_tr_b16 v[120:121], v32 offset:40960\n" \
"ds_read_b64_tr_b16 v[122:123], v32 offset:41984\n" \
"v_mfma_f32_16x16x32_bf16 a[164:167], v[124:127], v[92:95], a[164:167]\n" \
"v_mfma_f32_16x16x32_bf16 a[164:167], v[128:131], v[96:99], a[164:167]\n" \
"s_add_u32 m0, %[mA], 0x1800\n" \
"s_add_u32 s85, s62, 0xd800\n" \
"v_mfma_f32_16x16x32_bf16 a[196:199], v[128:131], v[104:107], a[196:199]\n" \
"buffer_load_dwordx4 v24, s[76:79], s85 offen lds\n" \
"v_mfma_f32_16x16x32_bf16 a[196:199], v[124:127], v[100:103], a[196:199]\n" \
"v_mfma_f32_16x16x32_bf16 a[228:231], v[124:127], v[108:111], a[228:231]\n" \
"ds_read_b64_tr_b16 v[124:125], v33 offset:32768\n" \
"ds_read_b64_tr_b16 v[126:127], v33 offset:33792\n" \
"v_mfma_f32_16x16x32_bf16 a[228:231], v[128:131], v[112:115], a[228:231]\n" \
"ds_read_b64_tr_b16 v[128:129], v33 offset:40960\n" \
"ds_read_b64_tr_b16 v[130:131], v33 offset:41984\n" \
"v_mfma_f32_16x16x32_bf16 a[136:139], v[136:139], v[88:91], a[136:139]\n" \
"v_mfma_f32_16x16x32_bf16 a[136:139], v[132:135], v[84:87], a[136:139]\n" \
"v_mfma_f32_16x16x32_bf16 a[168:171], v[132:135], v[92:95], a[168:171]\n" \
"v_mfma_f32_16x16x32_bf16 a[168:171], v[136:139], v[96:99], a[168:171]\n" \
"v_mfma_f32_16x16x32_bf16 a[200:203], v[136:139], v[104:107], a[200:203]\n" \
"v_mfma_f32_16x16x32_bf16 a[200:203], v[132:135], v[100:103], a[200:203]\n" \
"v_mfma_f32_16x16x32_bf16 a[232:235], v[132:135], v[108:111], a[232:235]\n" \
"ds_read_b64_tr_b16 v[132:133], v34 offset:32768\n" \
"ds_read_b64_tr_b16 v[134:135], v34 offset:33792\n" \
"v_mfma_f32_16x16x32_bf16 a[232:235], v[136:139], v[112:115], a[232:235]\n" \
"ds_read_b64_tr_b16 v[136:137], v34 offset:40960\n" \
"ds_read_b64_tr_b16 v[138:139], v34 offset:41984\n" \
"v_mfma_f32_16x16x32_bf16 a[140:143], v[144:147], v[88:91], a[140:143]\n" \
"ds_read_b128 v[88:91], v29 offset:32768\n" \
"v_mfma_f32_16x16x32_bf16 a[140:143], v[140:143], v[84:87], a[140:143]\n" \
"ds_read_b128 v[84:87], v28 offset:32768\n" \
"v_mfma_f32_16x16x32_bf16 a[172:175], v[140:143], v[92:95], a[172:175]\n" \
"ds_read_b128 v[92:95], v28 offset:34816\n" \
"v_mfma_f32_16x16x32_bf16 a[172:175], v[144:147], v[96:99], a[172:175]\n" \
"ds_read_b128 v[96:99], v29 offset:34816\n" \
"v_mfma_f32_16x16x32_bf16 a[204:207], v[144:147], v[104:107], a[204:207]\n" \
"ds_read_b128 v[104:107], v29 offset:36864\n" \
"v_mfma_f32_16x16x32_bf16 a[204:207], v[140:143], v[100:103], a[204:207]\n" \
"ds_read_b128 v[100:103], v28 offset:36864\n" \
"v_mfma_f32_16x16x32_bf16 a[236:239], v[140:143], v[108:111], a[236:239]\n" \
"ds_read_b64_tr_b16 v[140:141], v35 offset:32768\n" \
"ds_read_b64_tr_b16 v[142:143], v35 offset:33792\n" \
"v_mfma_f32_16x16x32_bf16 a[236:239], v[144:147], v[112:115], a[236:239]\n" \
"s_add_u32 m0, %[mA], 0x1c00\n" \
"s_waitcnt lgkmcnt(12)\n" \
"s_nop 0\n" \
"v_mfma_f32_16x16x32_bf16 a[16:19], v[56:59], v[152:155], a[16:19]\n" \
"s_add_u32 s85, s62, 0xfc00\n" \
"ds_read_b64_tr_b16 v[144:145], v35 offset:40960\n" \
"v_mfma_f32_16x16x32_bf16 a[16:19], v[52:55], v[148:151], a[16:19]\n" \
"buffer_load_dwordx4 v25, s[76:79], s85 offen lds\n" \
"v_mfma_f32_16x16x32_bf16 a[48:51], v[60:63], v[148:151], a[48:51]\n" \
"s_add_u32 m0, %[mB], 0xc00\n" \
"s_add_u32 s84, s63, 0x18000\n" \
"v_mfma_f32_16x16x32_bf16 a[48:51], v[64:67], v[152:155], a[48:51]\n" \
"ds_read_b64_tr_b16 v[146:147], v35 offset:41984\n" \
"ds_read_b128 v[108:111], v28 offset:38912\n" \
"v_mfma_f32_16x16x32_bf16 a[80:83], v[72:75], v[152:155], a[80:83]\n" \
"buffer_load_dwordx4 v27, s[68:71], s84 offen lds\n" \
"v_mfma_f32_16x16x32_bf16 a[80:83], v[68:71], v[148:151], a[80:83]\n" \
"ds_read_b128 v[112:115], v29 offset:38912\n" \
"s_add_u32 s62, s62, 0x80\n" \
"v_mfma_f32_16x16x32_bf16 a[112:115], v[76:79], v[148:151], a[112:115]\n" \
"s_cmp_lt_u32 s62, 0x480\n" \
"s_cselect_b32 s62, s62, 0\n" \
"v_mfma_f32_16x16x32_bf16 a[112:115], v[80:83], v[152:155], a[112:115]\n" \
"s_lshl_b32 s72, s62, 6\n" \
"s_add_u32 s72, s72, s37\n" \
"v_mfma_f32_16x16x32_bf16 a[20:23], v[56:59], v[160:163], a[20:23]\n" \
"s_lshl_b32 s63, s62, 12\n" \
"v_add_u32_e64 v28, s59, v28\n" \
"v_mfma_f32_16x16x32_bf16 a[20:23], v[52:55], v[156:159], a[20:23]\n" \
"v_add_u32_e64 v29, s59, v29\n" \
"v_accvgpr_read_b32 v184, a16\n" \
"v_mfma_f32_16x16x32_bf16 a[52:55], v[60:63], v[156:159], a[52:55]\n" \
"v_accvgpr_read_b32 v185, a17\n" \
"v_accvgpr_read_b32 v186, a18\n" \
"v_mfma_f32_16x16x32_bf16 a[52:55], v[64:67], v[160:163], a[52:55]\n" \
"v_accvgpr_read_b32 v187, a19\n" \
"v_accvgpr_write_b32 a16, 0\n" \
"v_mfma_f32_16x16x32_bf16 a[84:87], v[72:75], v[160:163], a[84:87]\n" \
"v_accvgpr_write_b32 a17, 0\n" \
"v_accvgpr_write_b32 a18, 0\n" \
"v_mfma_f32_16x16x32_bf16 a[84:87], v[68:71], v[156:159], a[84:87]\n" \
"v_accvgpr_write_b32 a19, 0\n" \
"v_accvgpr_read_b32 v180, a48\n" \
"v_mfma_f32_16x16x32_bf16 a[116:119], v[76:79], v[156:159], a[116:119]\n" \
"v_accvgpr_read_b32 v181, a49\n" \
"v_accvgpr_read_b32 v182, a50\n" \
"v_mfma_f32_16x16x32_bf16 a[116:119], v[80:83], v[160:163], a[116:119]\n" \
"v_accvgpr_read_b32 v183, a51\n" \
"v_accvgpr_read_b32 v188, a80\n" \
"v_mfma_f32_16x16x32_bf16 a[24:27], v[56:59], v[168:171], a[24:27]\n" \
"v_accvgpr_read_b32 v189, a81\n" \
"v_accvgpr_read_b32 v190, a82\n" \
"v_mfma_f32_16x16x32_bf16 a[24:27], v[52:55], v[164:167], a[24:27]\n" \
"v_accvgpr_read_b32 v191, a83\n" \
"v_cvt_pk_bf16_f32 v184, v184, v185\n" \
"v_mfma_f32_16x16x32_bf16 a[56:59], v[60:63], v[164:167], a[56:59]\n" \
"v_cvt_pk_bf16_f32 v185, v186, v187\n" \
"v_cvt_pk_bf16_f32 v186, v180, v181\n" \
"v_mfma_f32_16x16x32_bf16 a[56:59], v[64:67], v[168:171], a[56:59]\n" \
"v_cvt_pk_bf16_f32 v187, v182, v183\n" \
"v_accvgpr_read_b32 v180, a112\n" \
"v_mfma_f32_16x16x32_bf16 a[88:91], v[72:75], v[168:171], a[88:91]\n" \
"v_accvgpr_read_b32 v181, a113\n" \
"v_accvgpr_read_b32 v182, a114\n" \
"v_mfma_f32_16x16x32_bf16 a[88:91], v[68:71], v[164:167], a[88:91]\n" \
"v_accvgpr_read_b32 v183, a115\n" \
"v_accvgpr_write_b32 a112, 0\n" \
"v_mfma_f32_16x16x32_bf16 a[120:123], v[76:79], v[164:167], a[120:123]\n" \
"v_accvgpr_write_b32 a113, 0\n" \
"v_accvgpr_write_b32 a114, 0\n" \
"v_mfma_f32_16x16x32_bf16 a[120:123], v[80:83], v[168:171], a[120:123]\n" \
"v_accvgpr_write_b32 a115, 0\n" \
"v_accvgpr_read_b32 v192, a20\n" \
"v_mfma_f32_16x16x32_bf16 a[28:31], v[56:59], v[176:179], a[28:31]\n" \
"v_accvgpr_read_b32 v193, a21\n" \
"v_accvgpr_read_b32 v194, a22\n" \
"v_mfma_f32_16x16x32_bf16 a[28:31], v[52:55], v[172:175], a[28:31]\n" \
"v_accvgpr_read_b32 v195, a23\n" \
"v_cvt_pk_bf16_f32 v188, v188, v189\n" \
"v_mfma_f32_16x16x32_bf16 a[60:63], v[60:63], v[172:175], a[60:63]\n" \
"v_cvt_pk_bf16_f32 v189, v190, v191\n" \
"v_cvt_pk_bf16_f32 v190, v180, v181\n" \
"v_mfma_f32_16x16x32_bf16 a[60:63], v[64:67], v[176:179], a[60:63]\n" \
"v_cvt_pk_bf16_f32 v191, v182, v183\n" \
"v_accvgpr_read_b32 v180, a52\n" \
"v_mfma_f32_16x16x32_bf16 a[92:95], v[72:75], v[176:179], a[92:95]\n" \
"v_accvgpr_read_b32 v181, a53\n" \
"v_accvgpr_read_b32 v182, a54\n" \
"v_mfma_f32_16x16x32_bf16 a[92:95], v[68:71], v[172:175], a[92:95]\n" \
"v_accvgpr_read_b32 v183, a55\n" \
"v_accvgpr_read_b32 v196, a84\n" \
"v_mfma_f32_16x16x32_bf16 a[124:127], v[76:79], v[172:175], a[124:127]\n" \
"v_accvgpr_read_b32 v197, a85\n" \
"v_accvgpr_read_b32 v198, a86\n" \
"v_mfma_f32_16x16x32_bf16 a[124:127], v[80:83], v[176:179], a[124:127]\n" \
"v_accvgpr_read_b32 v199, a87\n" \
"v_cvt_pk_bf16_f32 v192, v192, v193\n" \
"v_mfma_f32_16x16x32_bf16 a[0:3], v[120:123], v[56:59], a[0:3]\n" \
"v_cvt_pk_bf16_f32 v193, v194, v195\n" \
"v_cvt_pk_bf16_f32 v194, v180, v181\n" \
"v_mfma_f32_16x16x32_bf16 a[0:3], v[116:119], v[52:55], a[0:3]\n" \
"v_cvt_pk_bf16_f32 v195, v182, v183\n" \
"v_accvgpr_read_b32 v180, a116\n" \
"v_mfma_f32_16x16x32_bf16 a[32:35], v[116:119], v[60:63], a[32:35]\n" \
"v_accvgpr_read_b32 v181, a117\n" \
"v_accvgpr_read_b32 v182, a118\n" \
"v_mfma_f32_16x16x32_bf16 a[32:35], v[120:123], v[64:67], a[32:35]\n" \
"v_accvgpr_read_b32 v183, a119\n" \
"v_accvgpr_write_b32 a116, 0\n" \
"v_mfma_f32_16x16x32_bf16 a[64:67], v[120:123], v[72:75], a[64:67]\n" \
"v_accvgpr_write_b32 a117, 0\n" \
"v_accvgpr_write_b32 a118, 0\n" \
"v_mfma_f32_16x16x32_bf16 a[64:67], v[116:119], v[68:71], a[64:67]\n" \
"v_accvgpr_write_b32 a119, 0\n" \
"v_accvgpr_read_b32 v200, a24\n" \
"v_mfma_f32_16x16x32_bf16 a[96:99], v[116:119], v[76:79], a[96:99]\n" \
"v_accvgpr_read_b32 v201, a25\n" \
"v_accvgpr_read_b32 v202, a26\n" \
"v_mfma_f32_16x16x32_bf16 a[96:99], v[120:123], v[80:83], a[96:99]\n" \
"v_accvgpr_read_b32 v203, a27\n" \
"v_cvt_pk_bf16_f32 v196, v196, v197\n" \
"v_mfma_f32_16x16x32_bf16 a[4:7], v[128:131], v[56:59], a[4:7]\n" \
"v_cvt_pk_bf16_f32 v197, v198, v199\n" \
"v_cvt_pk_bf16_f32 v198, v180, v181\n" \
"v_mfma_f32_16x16x32_bf16 a[4:7], v[124:127], v[52:55], a[4:7]\n" \
"v_cvt_pk_bf16_f32 v199, v182, v183\n" \
"v_accvgpr_read_b32 v180, a56\n" \
"v_mfma_f32_16x16x32_bf16 a[36:39], v[124:127], v[60:63], a[36:39]\n" \
"v_accvgpr_read_b32 v181, a57\n" \
"v_accvgpr_read_b32 v182, a58\n" \
"v_mfma_f32_16x16x32_bf16 a[36:39], v[128:131], v[64:67], a[36:39]\n" \
"v_accvgpr_read_b32 v183, a59\n" \
"v_accvgpr_read_b32 v204, a88\n" \
"v_mfma_f32_16x16x32_bf16 a[68:71], v[128:131], v[72:75], a[68:71]\n" \
"v_accvgpr_read_b32 v205, a89\n" \
"v_accvgpr_read_b32 v206, a90\n" \
"v_mfma_f32_16x16x32_bf16 a[68:71], v[124:127], v[68:71], a[68:71]\n" \
"v_accvgpr_read_b32 v207, a91\n" \
"v_cvt_pk_bf16_f32 v200, v200, v201\n" \
"v_mfma_f32_16x16x32_bf16 a[100:103], v[124:127], v[76:79], a[100:103]\n" \
"v_cvt_pk_bf16_f32 v201, v202, v203\n" \
"v_cvt_pk_bf16_f32 v202, v180, v181\n" \
"v_mfma_f32_16x16x32_bf16 a[100:103], v[128:131], v[80:83], a[100:103]\n" \
"v_cvt_pk_bf16_f32 v203, v182, v183\n" \
"s_waitcnt lgkmcnt(0)\n" \
"s_nop 0\n" \
"v_mfma_f32_16x16x32_bf16 a[8:11], v[136:139], v[56:59], a[8:11]\n" \
"v_accvgpr_read_b32 v180, a120\n" \
"v_accvgpr_read_b32 v181, a121\n" \
"v_mfma_f32_16x16x32_bf16 a[8:11], v[132:135], v[52:55], a[8:11]\n" \
"v_accvgpr_read_b32 v182, a122\n" \
"v_accvgpr_read_b32 v183, a123\n" \
"v_mfma_f32_16x16x32_bf16 a[40:43], v[132:135], v[60:63], a[40:43]\n" \
"v_accvgpr_write_b32 a120, 0\n" \
"v_accvgpr_write_b32 a121, 0\n" \
"v_mfma_f32_16x16x32_bf16 a[40:43], v[136:139], v[64:67], a[40:43]\n" \
"v_accvgpr_write_b32 a122, 0\n" \
"v_accvgpr_write_b32 a123, 0\n" \
"v_mfma_f32_16x16x32_bf16 a[72:75], v[136:139], v[72:75], a[72:75]\n" \
"v_accvgpr_read_b32 v208, a28\n" \
"v_accvgpr_read_b32 v209, a29\n" \
"v_mfma_f32_16x16x32_bf16 a[72:75], v[132:135], v[68:71], a[72:75]\n" \
"v_accvgpr_read_b32 v210, a30\n" \
"v_accvgpr_read_b32 v211, a31\n" \
"v_mfma_f32_16x16x32_bf16 a[104:107], v[132:135], v[76:79], a[104:107]\n" \
"v_cvt_pk_bf16_f32 v204, v204, v205\n" \
"v_cvt_pk_bf16_f32 v205, v206, v207\n" \
"v_mfma_f32_16x16x32_bf16 a[104:107], v[136:139], v[80:83], a[104:107]\n" \
"v_cvt_pk_bf16_f32 v206, v180, v181\n" \
"v_cvt_pk_bf16_f32 v207, v182, v183\n" \
"v_mfma_f32_16x16x32_bf16 a[12:15], v[144:147], v[56:59], a[12:15]\n" \
"v_accvgpr_read_b32 v180, a60\n" \
"v_accvgpr_read_b32 v181, a61\n" \
"v_mfma_f32_16x16x32_bf16 a[12:15], v[140:143], v[52:55], a[12:15]\n" \
"v_accvgpr_read_b32 v182, a62\n" \
"v_accvgpr_read_b32 v183, a63\n" \
"v_mfma_f32_16x16x32_bf16 a[44:47], v[140:143], v[60:63], a[44:47]\n" \
"v_accvgpr_read_b32 v212, a92\n" \
"v_accvgpr_read_b32 v213, a93\n" \
"v_mfma_f32_16x16x32_bf16 a[44:47], v[144:147], v[64:67], a[44:47]\n" \
"v_accvgpr_read_b32 v214, a94\n" \
"v_accvgpr_read_b32 v215, a95\n" \
"v_mfma_f32_16x16x32_bf16 a[76:79], v[144:147], v[72:75], a[76:79]\n" \
"v_cvt_pk_bf16_f32 v208, v208, v209\n" \
"v_cvt_pk_bf16_f32 v209, v210, v211\n" \
"v_mfma_f32_16x16x32_bf16 a[76:79], v[140:143], v[68:71], a[76:79]\n" \
"v_cvt_pk_bf16_f32 v210, v180, v181\n" \
"v_cvt_pk_bf16_f32 v211, v182, v183\n" \
"v_mfma_f32_16x16x32_bf16 a[108:111], v[140:143], v[76:79], a[108:111]\n" \
"v_accvgpr_read_b32 v180, a124\n" \
"v_accvgpr_read_b32 v181, a125\n" \
"v_mfma_f32_16x16x32_bf16 a[108:111], v[144:147], v[80:83], a[108:111]\n" \
"v_accvgpr_read_b32 v182, a126\n" \
"v_accvgpr_read_b32 v183, a127\n" \
"s_waitcnt vmcnt(0)\n" \
"s_barrier\n" \
"v_mfma_f32_16x16x32_bf16 a[128:131], v[120:123], v[88:91], a[128:131]\n" \
"s_add_u32 m0, s40, 0x8000\n" \
"s_add_u32 s85, s72, 0x0\n" \
"s_nop 0\n" \
"v_mfma_f32_16x16x32_bf16 a[128:131], v[116:119], v[84:87], a[128:131]\n" \
"buffer_load_dwordx4 v20, s[80:83], s85 offen sc1 lds\n" \
"v_mfma_f32_16x16x32_bf16 a[160:163], v[116:119], v[92:95], a[160:163]\n" \
"s_add_u32 m0, s40, 0x9000\n" \
"s_add_u32 s84, s72, 0x1000\n" \
"v_mfma_f32_16x16x32_bf16 a[160:163], v[120:123], v[96:99], a[160:163]\n" \
"ds_read_b128 v[52:55], v28 offset:0\n" \
"ds_read_b128 v[56:59], v29 offset:0\n" \
"v_mfma_f32_16x16x32_bf16 a[192:195], v[120:123], v[104:107], a[192:195]\n" \
"buffer_load_dwordx4 v20, s[80:83], s84 offen sc1 lds\n" \
"v_mfma_f32_16x16x32_bf16 a[192:195], v[116:119], v[100:103], a[192:195]\n" \
"ds_read_b128 v[60:63], v28 offset:2048\n" \
"ds_read_b128 v[64:67], v29 offset:2048\n" \
"v_mfma_f32_16x16x32_bf16 a[224:227], v[116:119], v[108:111], a[224:227]\n" \
"s_add_u32 m0, s40, 0xb000\n" \
"s_add_u32 s85, s62, 0x0\n" \
"v_mfma_f32_16x16x32_bf16 a[224:227], v[120:123], v[112:115], a[224:227]\n" \
"buffer_load_dwordx4 v21, s[76:79], s85 offen lds\n" \
"v_mfma_f32_16x16x32_bf16 a[132:135], v[128:131], v[88:91], a[132:135]\n" \
"ds_read_b64_tr_b16 v[116:117], v36 offset:0\n" \
"ds_read_b64_tr_b16 v[118:119], v36 offset:512\n" \
"v_mfma_f32_16x16x32_bf16 a[132:135], v[124:127], v[84:87], a[132:135]\n" \
"ds_read_b64_tr_b16 v[120:121], v36 offset:4096\n" \
"ds_read_b64_tr_b16 v[122:123], v36 offset:4608\n" \
"v_mfma_f32_16x16x32_bf16 a[164:167], v[124:127], v[92:95], a[164:167]\n" \
"s_add_u32 m0, %[mA], 0x8000\n" \
"L_pf4:\n" \
"s_cbranch_execz L_pf8\n" \
"s_nop 0\n" \
"s_add_u32 s84, s62, 0x0\n" \
"v_mfma_f32_16x16x32_bf16 a[164:167], v[128:131], v[96:99], a[164:167]\n" \
"buffer_load_dwordx4 v24, s[76:79], s84 offen lds\n" \
"v_mfma_f32_16x16x32_bf16 a[196:199], v[128:131], v[104:107], a[196:199]\n" \
"ds_read_b128 v[68:71], v28 offset:4096\n" \
"ds_read_b128 v[72:75], v29 offset:4096\n" \
"v_mfma_f32_16x16x32_bf16 a[196:199], v[124:127], v[100:103], a[196:199]\n" \
"s_add_u32 m0, %[mA], 0x8400\n" \
"s_add_u32 s85, s62, 0x2400\n" \
"v_mfma_f32_16x16x32_bf16 a[228:231], v[124:127], v[108:111], a[228:231]\n" \
"buffer_load_dwordx4 v25, s[76:79], s85 offen lds\n" \
"v_mfma_f32_16x16x32_bf16 a[228:231], v[128:131], v[112:115], a[228:231]\n" \
"ds_read_b64_tr_b16 v[128:129], v37 offset:4096\n" \
"ds_read_b64_tr_b16 v[130:131], v37 offset:4608\n" \
"v_mfma_f32_16x16x32_bf16 a[136:139], v[136:139], v[88:91], a[136:139]\n" \
"ds_read_b64_tr_b16 v[124:125], v37 offset:0\n" \
"ds_read_b64_tr_b16 v[126:127], v37 offset:512\n" \
"v_mfma_f32_16x16x32_bf16 a[136:139], v[132:135], v[84:87], a[136:139]\n" \
"s_add_u32 m0, %[mB], 0x8000\n" \
"s_add_u32 s84, s63, 0x0\n" \
"s_nop 0\n" \
"v_mfma_f32_16x16x32_bf16 a[168:171], v[132:135], v[92:95], a[168:171]\n" \
"buffer_load_dwordx4 v26, s[68:71], s84 offen lds\n" \
"v_mfma_f32_16x16x32_bf16 a[168:171], v[136:139], v[96:99], a[168:171]\n" \
"ds_read_b128 v[76:79], v28 offset:6144\n" \
"ds_read_b128 v[80:83], v29 offset:6144\n" \
"v_mfma_f32_16x16x32_bf16 a[200:203], v[136:139], v[104:107], a[200:203]\n" \
"v_accvgpr_write_b32 a124, 0\n" \
"v_accvgpr_write_b32 a125, 0\n" \
"v_mfma_f32_16x16x32_bf16 a[200:203], v[132:135], v[100:103], a[200:203]\n" \
"s_add_u32 m0, %[mA], 0x8800\n" \
"s_add_u32 s85, s62, 0x4800\n" \
"v_mfma_f32_16x16x32_bf16 a[232:235], v[132:135], v[108:111], a[232:235]\n" \
"buffer_load_dwordx4 v24, s[76:79], s85 offen lds\n" \
"v_mfma_f32_16x16x32_bf16 a[232:235], v[136:139], v[112:115], a[232:235]\n" \
"ds_read_b64_tr_b16 v[132:133], v38 offset:0\n" \
"ds_read_b64_tr_b16 v[134:135], v38 offset:512\n" \
"v_mfma_f32_16x16x32_bf16 a[140:143], v[144:147], v[88:91], a[140:143]\n" \
"s_add_u32 m0, %[mA], 0x8c00\n" \
"s_add_u32 s84, s62, 0x6c00\n" \
"v_mfma_f32_16x16x32_bf16 a[140:143], v[140:143], v[84:87], a[140:143]\n" \
"buffer_load_dwordx4 v25, s[76:79], s84 offen lds\n" \
"v_mfma_f32_16x16x32_bf16 a[172:175], v[140:143], v[92:95], a[172:175]\n" \
"ds_read_b64_tr_b16 v[136:137], v38 offset:4096\n" \
"ds_read_b64_tr_b16 v[138:139], v38 offset:4608\n" \
"v_mfma_f32_16x16x32_bf16 a[172:175], v[144:147], v[96:99], a[172:175]\n" \
"v_accvgpr_write_b32 a126, 0\n" \
"v_accvgpr_write_b32 a127, 0\n" \
"v_mfma_f32_16x16x32_bf16 a[204:207], v[144:147], v[104:107], a[204:207]\n" \
"s_add_u32 m0, %[mB], 0x8400\n" \
"s_add_u32 s85, s63, 0x8000\n" \
"v_mfma_f32_16x16x32_bf16 a[204:207], v[140:143], v[100:103], a[204:207]\n" \
"buffer_load_dwordx4 v26, s[68:71], s85 offen lds\n" \
"v_mfma_f32_16x16x32_bf16 a[236:239], v[140:143], v[108:111], a[236:239]\n" \
"ds_read_b64_tr_b16 v[140:141], v39 offset:0\n" \
"ds_read_b64_tr_b16 v[142:143], v39 offset:960\n" \
"v_mfma_f32_16x16x32_bf16 a[236:239], v[144:147], v[112:115], a[236:239]\n" \
"v_accvgpr_read_b32 v216, a0\n" \
"v_accvgpr_read_b32 v217, a1\n" \
"v_mfma_f32_16x16x32_bf16 a[144:147], v[88:91], v[152:155], a[144:147]\n" \
"s_add_u32 m0, %[mA], 0x9000\n" \
"s_add_u32 s84, s62, 0x9000\n" \
"v_mfma_f32_16x16x32_bf16 a[144:147], v[84:87], v[148:151], a[144:147]\n" \
"buffer_load_dwordx4 v24, s[76:79], s84 offen lds\n" \
"v_mfma_f32_16x16x32_bf16 a[148:151], v[84:87], v[156:159], a[148:151]\n" \
"v_accvgpr_read_b32 v218, a2\n" \
"v_accvgpr_read_b32 v219, a3\n" \
"v_mfma_f32_16x16x32_bf16 a[148:151], v[88:91], v[160:163], a[148:151]\n" \
"s_add_u32 m0, %[mA], 0x9400\n" \
"s_add_u32 s85, s62, 0xb400\n" \
"v_mfma_f32_16x16x32_bf16 a[152:155], v[88:91], v[168:171], a[152:155]\n" \
"buffer_load_dwordx4 v25, s[76:79], s85 offen lds\n" \
"v_mfma_f32_16x16x32_bf16 a[152:155], v[84:87], v[164:167], a[152:155]\n" \
"v_cvt_pk_bf16_f32 v212, v212, v213\n" \
"v_cvt_pk_bf16_f32 v213, v214, v215\n" \
"v_mfma_f32_16x16x32_bf16 a[156:159], v[84:87], v[172:175], a[156:159]\n" \
"ds_read_b128 v[84:87], v22 offset:0\n" \
"v_cvt_pk_bf16_f32 v214, v180, v181\n" \
"v_mfma_f32_16x16x32_bf16 a[156:159], v[88:91], v[176:179], a[156:159]\n" \
"s_add_u32 m0, %[mB], 0x8800\n" \
"s_add_u32 s84, s63, 0x10000\n" \
"v_mfma_f32_16x16x32_bf16 a[176:179], v[96:99], v[152:155], a[176:179]\n" \
"buffer_load_dwordx4 v27, s[68:71], s84 offen lds\n" \
"v_mfma_f32_16x16x32_bf16 a[176:179], v[92:95], v[148:151], a[176:179]\n" \
"ds_read_b128 v[88:91], v23 offset:0\n" \
"v_cvt_pk_bf16_f32 v215, v182, v183\n" \
"v_mfma_f32_16x16x32_bf16 a[180:183], v[92:95], v[156:159], a[180:183]\n" \
"v_accvgpr_read_b32 v180, a4\n" \
"v_accvgpr_read_b32 v181, a5\n" \
"v_mfma_f32_16x16x32_bf16 a[180:183], v[96:99], v[160:163], a[180:183]\n" \
"s_add_u32 m0, %[mA], 0x9800\n" \
"s_add_u32 s85, s62, 0xd800\n" \
"v_mfma_f32_16x16x32_bf16 a[184:187], v[96:99], v[168:171], a[184:187]\n" \
"buffer_load_dwordx4 v24, s[76:79], s85 offen lds\n" \
"v_mfma_f32_16x16x32_bf16 a[184:187], v[92:95], v[164:167], a[184:187]\n" \
"v_accvgpr_read_b32 v182, a6\n" \
"v_accvgpr_read_b32 v183, a7\n" \
"v_mfma_f32_16x16x32_bf16 a[188:191], v[92:95], v[172:175], a[188:191]\n" \
"v_cvt_pk_bf16_f32 v216, v216, v217\n" \
"v_cvt_pk_bf16_f32 v217, v218, v219\n" \
"v_mfma_f32_16x16x32_bf16 a[188:191], v[96:99], v[176:179], a[188:191]\n" \
"v_accvgpr_read_b32 v220, a32\n" \
"v_accvgpr_read_b32 v221, a33\n" \
"v_mfma_f32_16x16x32_bf16 a[208:211], v[104:107], v[152:155], a[208:211]\n" \
"v_cvt_pk_bf16_f32 v218, v180, v181\n" \
"v_cvt_pk_bf16_f32 v219, v182, v183\n" \
"v_mfma_f32_16x16x32_bf16 a[208:211], v[100:103], v[148:151], a[208:211]\n" \
"v_accvgpr_read_b32 v222, a34\n" \
"v_accvgpr_read_b32 v223, a35\n" \
"v_mfma_f32_16x16x32_bf16 a[212:215], v[100:103], v[156:159], a[212:215]\n" \
"v_cvt_pk_bf16_f32 v220, v220, v221\n" \
"v_accvgpr_read_b32 v180, a36\n" \
"v_mfma_f32_16x16x32_bf16 a[212:215], v[104:107], v[160:163], a[212:215]\n" \
"v_accvgpr_read_b32 v181, a37\n" \
"v_accvgpr_read_b32 v182, a38\n" \
"v_mfma_f32_16x16x32_bf16 a[216:219], v[104:107], v[168:171], a[216:219]\n" \
"v_accvgpr_read_b32 v183, a39\n" \
"v_cvt_pk_bf16_f32 v221, v222, v223\n" \
"v_mfma_f32_16x16x32_bf16 a[216:219], v[100:103], v[164:167], a[216:219]\n" \
"v_accvgpr_read_b32 v224, a64\n" \
"v_accvgpr_read_b32 v225, a65\n" \
"v_mfma_f32_16x16x32_bf16 a[220:223], v[100:103], v[172:175], a[220:223]\n" \
"v_cvt_pk_bf16_f32 v222, v180, v181\n" \
"v_cvt_pk_bf16_f32 v223, v182, v183\n" \
"v_mfma_f32_16x16x32_bf16 a[220:223], v[104:107], v[176:179], a[220:223]\n" \
"v_accvgpr_read_b32 v226, a66\n" \
"v_accvgpr_read_b32 v227, a67\n" \
"v_mfma_f32_16x16x32_bf16 a[240:243], v[112:115], v[152:155], a[240:243]\n" \
"ds_read_b64_tr_b16 v[152:153], v32 offset:8192\n" \
"ds_read_b64_tr_b16 v[154:155], v32 offset:9216\n" \
"v_mfma_f32_16x16x32_bf16 a[240:243], v[108:111], v[148:151], a[240:243]\n" \
"ds_read_b64_tr_b16 v[148:149], v32 offset:0\n" \
"ds_read_b64_tr_b16 v[150:151], v32 offset:1024\n" \
"v_mfma_f32_16x16x32_bf16 a[244:247], v[108:111], v[156:159], a[244:247]\n" \
"ds_read_b64_tr_b16 v[156:157], v33 offset:0\n" \
"ds_read_b64_tr_b16 v[158:159], v33 offset:1024\n" \
"v_mfma_f32_16x16x32_bf16 a[244:247], v[112:115], v[160:163], a[244:247]\n" \
"ds_read_b64_tr_b16 v[160:161], v33 offset:8192\n" \
"ds_read_b64_tr_b16 v[162:163], v33 offset:9216\n" \
"v_mfma_f32_16x16x32_bf16 a[248:251], v[112:115], v[168:171], a[248:251]\n" \
"ds_read_b64_tr_b16 v[168:169], v34 offset:8192\n" \
"ds_read_b64_tr_b16 v[170:171], v34 offset:9216\n" \
"v_mfma_f32_16x16x32_bf16 a[248:251], v[108:111], v[164:167], a[248:251]\n" \
"ds_read_b64_tr_b16 v[164:165], v34 offset:0\n" \
"ds_read_b64_tr_b16 v[166:167], v34 offset:1024\n" \
"v_mfma_f32_16x16x32_bf16 a[252:255], v[108:111], v[172:175], a[252:255]\n" \
"ds_read_b64_tr_b16 v[172:173], v35 offset:0\n" \
"ds_read_b64_tr_b16 v[174:175], v35 offset:1024\n" \
"v_mfma_f32_16x16x32_bf16 a[252:255], v[112:115], v[176:179], a[252:255]\n" \
"s_add_u32 m0, %[mA], 0x9c00\n" \
"s_waitcnt lgkmcnt(14)\n" \
"s_nop 0\n" \
"v_smfmac_f32_16x16x64_bf16 a[16:19], v[140:143], v[84:91], v50\n" \
"v_mfma_f32_16x16x32_bf16 a[48:51], v[128:131], v[88:91], 0\n" \
"s_add_u32 s85, s62, 0xfc00\n" \
"ds_read_b64_tr_b16 v[176:177], v35 offset:8192\n" \
"v_mfma_f32_16x16x32_bf16 a[48:51], v[124:127], v[84:87], a[48:51]\n" \
"buffer_load_dwordx4 v25, s[76:79], s85 offen lds\n" \
"v_mfma_f32_16x16x32_bf16 a[80:83], v[84:87], v[132:135], 0\n" \
"s_add_u32 m0, %[mB], 0x8c00\n" \
"s_add_u32 s84, s63, 0x18000\n" \
"v_mfma_f32_16x16x32_bf16 a[80:83], v[88:91], v[136:139], a[80:83]\n" \
"ds_read_b64_tr_b16 v[178:179], v35 offset:9216\n" \
"v_cvt_pk_bf16_f32 v224, v224, v225\n" \
"v_smfmac_f32_16x16x64_bf16 a[112:115], v[140:143], v[52:59], v50\n" \
"v_mfma_f32_16x16x32_bf16 a[20:23], v[128:131], v[56:59], 0\n" \
"buffer_load_dwordx4 v27, s[68:71], s84 offen lds\n" \
"v_mfma_f32_16x16x32_bf16 a[20:23], v[124:127], v[52:55], a[20:23]\n" \
"s_add_u32 s88, s44, 0x0\n" \
"s_nop 0\n" \
"v_accvgpr_read_b32 v180, a68\n" \
"v_mfma_f32_16x16x32_bf16 a[52:55], v[116:119], v[52:55], 0\n" \
"v_accvgpr_read_b32 v181, a69\n" \
"v_accvgpr_read_b32 v182, a70\n" \
"v_mfma_f32_16x16x32_bf16 a[52:55], v[120:123], v[56:59], a[52:55]\n" \
"buffer_store_dwordx4 v[184:187], v20, s[80:83], s88 offen offset:0 sc1\n" \
"v_mfma_f32_16x16x32_bf16 a[84:87], v[56:59], v[136:139], 0\n" \
"v_accvgpr_read_b32 v183, a71\n" \
"v_cvt_pk_bf16_f32 v225, v226, v227\n" \
"v_mfma_f32_16x16x32_bf16 a[84:87], v[52:55], v[132:135], a[84:87]\n" \
"v_accvgpr_read_b32 v228, a96\n" \
"v_accvgpr_read_b32 v229, a97\n" \
"v_smfmac_f32_16x16x64_bf16 a[116:119], v[140:143], v[60:67], v50\n" \
"v_mfma_f32_16x16x32_bf16 a[24:27], v[124:127], v[60:63], 0\n" \
"v_cvt_pk_bf16_f32 v226, v180, v181\n" \
"v_cvt_pk_bf16_f32 v227, v182, v183\n" \
"v_mfma_f32_16x16x32_bf16 a[24:27], v[128:131], v[64:67], a[24:27]\n" \
"s_add_u32 s89, s44, 0x1000\n" \
"v_accvgpr_read_b32 v230, a98\n" \
"v_mfma_f32_16x16x32_bf16 a[56:59], v[120:123], v[64:67], 0\n" \
"buffer_store_dwordx4 v[188:191], v20, s[80:83], s89 offen offset:0 sc1\n" \
"v_mfma_f32_16x16x32_bf16 a[56:59], v[116:119], v[60:63], a[56:59]\n" \
"v_accvgpr_read_b32 v231, a99\n" \
"v_cvt_pk_bf16_f32 v228, v228, v229\n" \
"v_mfma_f32_16x16x32_bf16 a[88:91], v[60:63], v[132:135], 0\n" \
"v_accvgpr_read_b32 v180, a100\n" \
"v_accvgpr_read_b32 v181, a101\n" \
"v_mfma_f32_16x16x32_bf16 a[88:91], v[64:67], v[136:139], a[88:91]\n" \
"v_accvgpr_read_b32 v182, a102\n" \
"v_accvgpr_read_b32 v183, a103\n" \
"v_smfmac_f32_16x16x64_bf16 a[120:123], v[140:143], v[68:75], v50\n" \
"v_mfma_f32_16x16x32_bf16 a[28:31], v[128:131], v[72:75], 0\n" \
"v_cvt_pk_bf16_f32 v229, v230, v231\n" \
"v_accvgpr_read_b32 v232, a8\n" \
"v_mfma_f32_16x16x32_bf16 a[28:31], v[124:127], v[68:71], a[28:31]\n" \
"v_accvgpr_read_b32 v233, a9\n" \
"v_cvt_pk_bf16_f32 v230, v180, v181\n" \
"v_mfma_f32_16x16x32_bf16 a[60:63], v[116:119], v[68:71], 0\n" \
"s_add_u32 s88, s44, 0x0\n" \
"s_nop 0\n" \
"v_cvt_pk_bf16_f32 v231, v182, v183\n" \
"v_mfma_f32_16x16x32_bf16 a[60:63], v[120:123], v[72:75], a[60:63]\n" \
"buffer_store_dwordx4 v[192:195], v20, s[80:83], s88 offen offset:1024 sc1\n" \
"v_mfma_f32_16x16x32_bf16 a[92:95], v[72:75], v[136:139], 0\n" \
"v_accvgpr_read_b32 v234, a10\n" \
"v_accvgpr_read_b32 v235, a11\n" \
"v_mfma_f32_16x16x32_bf16 a[92:95], v[68:71], v[132:135], a[92:95]\n" \
"v_cvt_pk_bf16_f32 v232, v232, v233\n" \
"v_accvgpr_read_b32 v180, a12\n" \
"v_smfmac_f32_16x16x64_bf16 a[124:127], v[140:143], v[76:83], v50\n" \
"v_mfma_f32_16x16x32_bf16 a[0:3], v[124:127], v[76:79], 0\n" \
"v_accvgpr_read_b32 v181, a13\n" \
"v_accvgpr_read_b32 v182, a14\n" \
"v_mfma_f32_16x16x32_bf16 a[0:3], v[128:131], v[80:83], a[0:3]\n" \
"v_accvgpr_read_b32 v183, a15\n" \
"v_cvt_pk_bf16_f32 v233, v234, v235\n" \
"v_mfma_f32_16x16x32_bf16 a[4:7], v[120:123], v[80:83], 0\n" \
"v_accvgpr_read_b32 v236, a40\n" \
"v_accvgpr_read_b32 v237, a41\n" \
"v_mfma_f32_16x16x32_bf16 a[4:7], v[116:119], v[76:79], a[4:7]\n" \
"v_cvt_pk_bf16_f32 v234, v180, v181\n" \
"v_cvt_pk_bf16_f32 v235, v182, v183\n" \
"v_mfma_f32_16x16x32_bf16 a[32:35], v[76:79], v[132:135], 0\n" \
"v_accvgpr_read_b32 v238, a42\n" \
"v_accvgpr_read_b32 v239, a43\n" \
"v_mfma_f32_16x16x32_bf16 a[32:35], v[80:83], v[136:139], a[32:35]\n" \
"v_cvt_pk_bf16_f32 v236, v236, v237\n" \
"v_accvgpr_read_b32 v180, a44\n" \
"v_accvgpr_read_b32 v181, a45\n" \
"v_accvgpr_read_b32 v182, a46\n" \
"v_accvgpr_read_b32 v183, a47\n" \
"v_cvt_pk_bf16_f32 v237, v238, v239\n" \
"v_accvgpr_read_b32 v240, a72\n" \
"v_accvgpr_read_b32 v241, a73\n" \
"v_cvt_pk_bf16_f32 v238, v180, v181\n" \
"v_cvt_pk_bf16_f32 v239, v182, v183\n" \
"v_accvgpr_read_b32 v242, a74\n" \
"v_accvgpr_read_b32 v243, a75\n" \
"v_cvt_pk_bf16_f32 v240, v240, v241\n" \
"v_accvgpr_read_b32 v180, a76\n" \
"v_accvgpr_read_b32 v181, a77\n" \
"v_accvgpr_read_b32 v182, a78\n" \
"v_accvgpr_read_b32 v183, a79\n" \
"v_cvt_pk_bf16_f32 v241, v242, v243\n" \
"v_accvgpr_read_b32 v244, a104\n" \
"v_accvgpr_read_b32 v245, a105\n" \
"v_cvt_pk_bf16_f32 v242, v180, v181\n" \
"v_cvt_pk_bf16_f32 v243, v182, v183\n" \
"v_accvgpr_read_b32 v246, a106\n" \
"v_accvgpr_read_b32 v247, a107\n" \
"v_cvt_pk_bf16_f32 v244, v244, v245\n" \
"v_accvgpr_read_b32 v248, a128\n" \
"v_accvgpr_read_b32 v249, a129\n" \
"v_accvgpr_read_b32 v250, a130\n" \
"v_accvgpr_read_b32 v251, a131\n" \
"s_waitcnt lgkmcnt(0)\n" \
"s_nop 0\n" \
"v_mfma_f32_16x16x32_bf16 a[128:131], v[152:155], v[56:59], 0\n" \
"v_accvgpr_read_b32 v180, a108\n" \
"v_accvgpr_read_b32 v181, a109\n" \
"v_accvgpr_read_b32 v182, a110\n" \
"v_accvgpr_read_b32 v183, a111\n" \
"v_cvt_pk_bf16_f32 v245, v246, v247\n" \
"v_cvt_pk_bf16_f32 v246, v180, v181\n" \
"v_cvt_pk_bf16_f32 v247, v182, v183\n" \
"v_cvt_pk_bf16_f32 v248, v248, v249\n" \
"v_mfma_f32_16x16x32_bf16 a[128:131], v[148:151], v[52:55], a[128:131]\n" \
"v_accvgpr_read_b32 v180, a132\n" \
"v_accvgpr_read_b32 v181, a133\n" \
"v_accvgpr_read_b32 v182, a134\n" \
"v_accvgpr_read_b32 v183, a135\n" \
"v_cvt_pk_bf16_f32 v249, v250, v251\n" \
"v_accvgpr_read_b32 v252, a160\n" \
"v_accvgpr_read_b32 v253, a161\n" \
"v_cvt_pk_bf16_f32 v250, v180, v181\n" \
"v_cvt_pk_bf16_f32 v251, v182, v183\n" \
"v_accvgpr_read_b32 v254, a162\n" \
"v_accvgpr_read_b32 v255, a163\n" \
"v_cvt_pk_bf16_f32 v252, v252, v253\n" \
"v_accvgpr_read_b32 v180, a164\n" \
"v_accvgpr_read_b32 v181, a165\n" \
"v_accvgpr_read_b32 v182, a166\n" \
"v_accvgpr_read_b32 v183, a167\n" \
"v_cvt_pk_bf16_f32 v253, v254, v255\n" \
"v_cvt_pk_bf16_f32 v254, v180, v181\n" \
"v_cvt_pk_bf16_f32 v255, v182, v183\n" \
"v_accvgpr_write_b32 a36, v252\n" \
"v_accvgpr_write_b32 a37, v253\n" \
"v_mfma_f32_16x16x32_bf16 a[132:135], v[156:159], v[52:55], 0\n" \
"v_accvgpr_write_b32 a38, v254\n" \
"v_accvgpr_write_b32 a39, v255\n" \
"v_accvgpr_read_b32 v252, a192\n" \
"v_accvgpr_read_b32 v253, a193\n" \
"v_accvgpr_read_b32 v254, a194\n" \
"v_accvgpr_read_b32 v255, a195\n" \
"v_cvt_pk_bf16_f32 v252, v252, v253\n" \
"v_accvgpr_read_b32 v180, a196\n" \
"v_accvgpr_read_b32 v181, a197\n" \
"v_accvgpr_read_b32 v182, a198\n" \
"v_accvgpr_read_b32 v183, a199\n" \
"v_cvt_pk_bf16_f32 v253, v254, v255\n" \
"v_cvt_pk_bf16_f32 v254, v180, v181\n" \
"v_cvt_pk_bf16_f32 v255, v182, v183\n" \
"v_accvgpr_write_b32 a64, v252\n" \
"v_accvgpr_write_b32 a65, v253\n" \
"v_mfma_f32_16x16x32_bf16 a[132:135], v[160:163], v[56:59], a[132:135]\n" \
"v_accvgpr_write_b32 a66, v254\n" \
"v_accvgpr_write_b32 a67, v255\n" \
"v_accvgpr_read_b32 v252, a224\n" \
"v_accvgpr_read_b32 v253, a225\n" \
"v_accvgpr_read_b32 v254, a226\n" \
"v_accvgpr_read_b32 v255, a227\n" \
"v_cvt_pk_bf16_f32 v252, v252, v253\n" \
"v_accvgpr_read_b32 v180, a228\n" \
"v_accvgpr_read_b32 v181, a229\n" \
"v_accvgpr_read_b32 v182, a230\n" \
"v_accvgpr_read_b32 v183, a231\n" \
"v_cvt_pk_bf16_f32 v253, v254, v255\n" \
"v_cvt_pk_bf16_f32 v254, v180, v181\n" \
"v_cvt_pk_bf16_f32 v255, v182, v183\n" \
"v_accvgpr_write_b32 a68, v252\n" \
"v_accvgpr_write_b32 a69, v253\n" \
"v_accvgpr_write_b32 a70, v254\n" \
"v_accvgpr_write_b32 a71, v255\n" \
"v_accvgpr_read_b32 v252, a136\n" \
"v_accvgpr_read_b32 v253, a137\n" \
"v_accvgpr_read_b32 v254, a138\n" \
"v_accvgpr_read_b32 v255, a139\n" \
"v_mfma_f32_16x16x32_bf16 a[136:139], v[168:171], v[56:59], 0\n" \
"ds_read_b128 v[84:87], v48 offset:0\n" \
"ds_read_b128 v[88:91], v49 offset:0\n" \
"ds_read_b128 v[92:95], v48 offset:2048\n" \
"v_mfma_f32_16x16x32_bf16 a[136:139], v[164:167], v[52:55], a[136:139]\n" \
"ds_read_b128 v[96:99], v49 offset:2048\n" \
"v_accvgpr_read_b32 v180, a140\n" \
"v_accvgpr_read_b32 v181, a141\n" \
"v_accvgpr_read_b32 v182, a142\n" \
"v_accvgpr_read_b32 v183, a143\n" \
"v_mfma_f32_16x16x32_bf16 a[140:143], v[172:175], v[52:55], 0\n" \
"ds_read_b128 v[100:103], v48 offset:4096\n" \
"ds_read_b128 v[104:107], v49 offset:4096\n" \
"s_add_u32 s89, s44, 0x1000\n" \
"buffer_store_dwordx4 v[196:199], v20, s[80:83], s89 offen offset:1024 sc1\n" \
"ds_read_b128 v[108:111], v48 offset:6144\n" \
"v_mfma_f32_16x16x32_bf16 a[140:143], v[176:179], v[56:59], a[140:143]\n" \
"v_cvt_pk_bf16_f32 v252, v252, v253\n" \
"v_cvt_pk_bf16_f32 v253, v254, v255\n" \
"v_mfma_f32_16x16x32_bf16 a[160:163], v[152:155], v[64:67], 0\n" \
"ds_read_b128 v[112:115], v49 offset:6144\n" \
"v_cvt_pk_bf16_f32 v254, v180, v181\n" \
"v_mfma_f32_16x16x32_bf16 a[160:163], v[148:151], v[60:63], a[160:163]\n" \
"v_cvt_pk_bf16_f32 v255, v182, v183\n" \
"v_accvgpr_write_b32 a96, v252\n" \
"v_mfma_f32_16x16x32_bf16 a[164:167], v[156:159], v[60:63], 0\n" \
"v_accvgpr_write_b32 a97, v253\n" \
"v_accvgpr_write_b32 a98, v254\n" \
"v_mfma_f32_16x16x32_bf16 a[164:167], v[160:163], v[64:67], a[164:167]\n" \
"v_accvgpr_write_b32 a99, v255\n" \
"v_accvgpr_read_b32 v252, a168\n" \
"v_accvgpr_read_b32 v253, a169\n" \
"v_accvgpr_read_b32 v254, a170\n" \
"v_accvgpr_read_b32 v255, a171\n" \
"v_mfma_f32_16x16x32_bf16 a[168:171], v[168:171], v[64:67], 0\n" \
"v_accvgpr_read_b32 v180, a172\n" \
"v_accvgpr_read_b32 v181, a173\n" \
"v_mfma_f32_16x16x32_bf16 a[168:171], v[164:167], v[60:63], a[168:171]\n" \
"v_accvgpr_read_b32 v182, a174\n" \
"v_accvgpr_read_b32 v183, a175\n" \
"v_mfma_f32_16x16x32_bf16 a[172:175], v[172:175], v[60:63], 0\n" \
"v_cvt_pk_bf16_f32 v252, v252, v253\n" \
"v_cvt_pk_bf16_f32 v253, v254, v255\n" \
"v_mfma_f32_16x16x32_bf16 a[172:175], v[176:179], v[64:67], a[172:175]\n" \
"v_cvt_pk_bf16_f32 v254, v180, v181\n" \
"v_cvt_pk_bf16_f32 v255, v182, v183\n" \
"v_mfma_f32_16x16x32_bf16 a[192:195], v[152:155], v[72:75], 0\n" \
"v_accvgpr_write_b32 a100, v252\n" \
"v_accvgpr_write_b32 a101, v253\n" \
"v_mfma_f32_16x16x32_bf16 a[192:195], v[148:151], v[68:71], a[192:195]\n" \
"v_accvgpr_write_b32 a102, v254\n" \
"v_accvgpr_write_b32 a103, v255\n" \
"s_add_u32 s62, s62, 0x80\n" \
"v_accvgpr_read_b32 v252, a200\n" \
"v_accvgpr_read_b32 v253, a201\n" \
"v_accvgpr_read_b32 v254, a202\n" \
"s_waitcnt vmcnt(4) lgkmcnt(0)\n" \
"s_barrier\n" \
"v_mfma_f32_16x16x32_bf16 a[196:199], v[156:159], v[68:71], 0\n" \
"v_accvgpr_read_b32 v255, a203\n" \
"s_add_u32 m0, s40, 0x0\n" \
"v_mfma_f32_16x16x32_bf16 a[196:199], v[160:163], v[72:75], a[196:199]\n" \
"s_cmp_lt_u32 s62, 0x480\n" \
"s_cselect_b32 s62, s62, 0\n" \
"v_mfma_f32_16x16x32_bf16 a[200:203], v[168:171], v[72:75], 0\n" \
"s_lshl_b32 s72, s62, 6\n" \
"s_add_u32 s72, s72, s37\n" \
"v_mfma_f32_16x16x32_bf16 a[200:203], v[164:167], v[68:71], a[200:203]\n" \
"s_add_u32 s85, s72, 0x0\n" \
"s_nop 0\n" \
"buffer_load_dwordx4 v20, s[80:83], s85 offen sc1 lds\n" \
"v_accvgpr_read_b32 v180, a204\n" \
"v_accvgpr_read_b32 v181, a205\n" \
"v_accvgpr_read_b32 v182, a206\n" \
"v_accvgpr_read_b32 v183, a207\n" \
"v_mfma_f32_16x16x32_bf16 a[204:207], v[172:175], v[68:71], 0\n" \
"s_add_u32 m0, s40, 0x1000\n" \
"s_add_u32 s84, s72, 0x1000\n" \
"v_mfma_f32_16x16x32_bf16 a[204:207], v[176:179], v[72:75], a[204:207]\n" \
"buffer_load_dwordx4 v20, s[80:83], s84 offen sc1 lds\n" \
"v_mfma_f32_16x16x32_bf16 a[224:227], v[152:155], v[80:83], 0\n" \
"v_cvt_pk_bf16_f32 v252, v252, v253\n" \
"v_cvt_pk_bf16_f32 v253, v254, v255\n" \
"v_mfma_f32_16x16x32_bf16 a[224:227], v[148:151], v[76:79], a[224:227]\n" \
"v_cvt_pk_bf16_f32 v254, v180, v181\n" \
"v_cvt_pk_bf16_f32 v255, v182, v183\n" \
"v_mfma_f32_16x16x32_bf16 a[228:231], v[156:159], v[76:79], 0\n" \
"v_accvgpr_write_b32 a8, v252\n" \
"v_accvgpr_write_b32 a9, v253\n" \
"v_mfma_f32_16x16x32_bf16 a[228:231], v[160:163], v[80:83], a[228:231]\n" \
"v_accvgpr_write_b32 a10, v254\n" \
"v_accvgpr_write_b32 a11, v255\n" \
"v_accvgpr_read_b32 v252, a232\n" \
"v_accvgpr_read_b32 v253, a233\n" \
"v_accvgpr_read_b32 v254, a234\n" \
"v_accvgpr_read_b32 v255, a235\n" \
"v_mfma_f32_16x16x32_bf16 a[232:235], v[168:171], v[80:83], 0\n" \
"s_add_u32 m0, s40, 0x3000\n" \
"s_add_u32 s85, s62, 0x0\n" \
"buffer_load_dwordx4 v21, s[76:79], s85 offen lds\n" \
"v_mfma_f32_16x16x32_bf16 a[232:235], v[164:167], v[76:79], a[232:235]\n" \
"v_accvgpr_read_b32 v180, a236\n" \
"v_accvgpr_read_b32 v181, a237\n" \
"v_accvgpr_read_b32 v182, a238\n" \
"v_accvgpr_read_b32 v183, a239\n" \
"v_mfma_f32_16x16x32_bf16 a[236:239], v[172:175], v[76:79], 0\n" \
"v_cvt_pk_bf16_f32 v252, v252, v253\n" \
"s_add_u32 m0, %[mA], 0x0\n" \
"v_mfma_f32_16x16x32_bf16 a[236:239], v[176:179], v[80:83], a[236:239]\n" \
"s_add_u32 s84, s62, 0x0\n" \
"s_nop 0\n" \
"v_cvt_pk_bf16_f32 v253, v254, v255\n" \
"v_cvt_pk_bf16_f32 v254, v180, v181\n" \
"v_cvt_pk_bf16_f32 v255, v182, v183\n" \
"v_accvgpr_write_b32 a12, v252\n" \
"v_accvgpr_write_b32 a13, v253\n" \
"v_accvgpr_write_b32 a14, v254\n" \
"v_accvgpr_write_b32 a15, v255\n" \
"v_accvgpr_read_b32 v252, a144\n" \
"v_accvgpr_read_b32 v253, a145\n" \
"v_accvgpr_read_b32 v254, a146\n" \
"v_accvgpr_read_b32 v255, a147\n" \
"v_mfma_f32_16x16x32_bf16 a[144:147], v[152:155], v[88:91], 0\n" \
"buffer_load_dwordx4 v24, s[76:79], s84 offen lds\n" \
"v_mfma_f32_16x16x32_bf16 a[144:147], v[148:151], v[84:87], a[144:147]\n" \
"v_cvt_pk_bf16_f32 v252, v252, v253\n" \
"v_accvgpr_read_b32 v180, a176\n" \
"v_accvgpr_read_b32 v181, a177\n" \
"v_accvgpr_read_b32 v182, a178\n" \
"v_accvgpr_read_b32 v183, a179\n" \
"v_cvt_pk_bf16_f32 v253, v254, v255\n" \
"v_cvt_pk_bf16_f32 v254, v180, v181\n" \
"v_cvt_pk_bf16_f32 v255, v182, v183\n" \
"v_accvgpr_write_b32 a40, v252\n" \
"v_accvgpr_write_b32 a41, v253\n" \
"v_accvgpr_write_b32 a42, v254\n" \
"v_accvgpr_write_b32 a43, v255\n" \
"v_accvgpr_read_b32 v252, a148\n" \
"v_accvgpr_read_b32 v253, a149\n" \
"v_accvgpr_read_b32 v254, a150\n" \
"v_accvgpr_read_b32 v255, a151\n" \
"v_mfma_f32_16x16x32_bf16 a[148:151], v[156:159], v[84:87], 0\n" \
"v_cvt_pk_bf16_f32 v252, v252, v253\n" \
"s_add_u32 m0, %[mA], 0x400\n" \
"v_mfma_f32_16x16x32_bf16 a[148:151], v[160:163], v[88:91], a[148:151]\n" \
"s_add_u32 s85, s62, 0x2400\n" \
"v_accvgpr_read_b32 v180, a180\n" \
"v_accvgpr_read_b32 v181, a181\n" \
"v_accvgpr_read_b32 v182, a182\n" \
"v_accvgpr_read_b32 v183, a183\n" \
"v_cvt_pk_bf16_f32 v253, v254, v255\n" \
"v_cvt_pk_bf16_f32 v254, v180, v181\n" \
"v_cvt_pk_bf16_f32 v255, v182, v183\n" \
"v_accvgpr_write_b32 a44, v252\n" \
"v_accvgpr_write_b32 a45, v253\n" \
"v_accvgpr_write_b32 a46, v254\n" \
"v_accvgpr_write_b32 a47, v255\n" \
"v_accvgpr_read_b32 v252, a152\n" \
"v_accvgpr_read_b32 v253, a153\n" \
"v_accvgpr_read_b32 v254, a154\n" \
"v_accvgpr_read_b32 v255, a155\n" \
"v_mfma_f32_16x16x32_bf16 a[152:155], v[168:171], v[88:91], 0\n" \
"buffer_load_dwordx4 v25, s[76:79], s85 offen lds\n" \
"v_mfma_f32_16x16x32_bf16 a[152:155], v[164:167], v[84:87], a[152:155]\n" \
"v_cvt_pk_bf16_f32 v252, v252, v253\n" \
"v_accvgpr_read_b32 v180, a184\n" \
"v_accvgpr_read_b32 v181, a185\n" \
"v_accvgpr_read_b32 v182, a186\n" \
"v_accvgpr_read_b32 v183, a187\n" \
"v_cvt_pk_bf16_f32 v253, v254, v255\n" \
"v_cvt_pk_bf16_f32 v254, v180, v181\n" \
"v_cvt_pk_bf16_f32 v255, v182, v183\n" \
"s_lshl_b32 s63, s62, 12\n" \
"v_accvgpr_write_b32 a72, v252\n" \
"v_accvgpr_write_b32 a73, v253\n" \
"v_accvgpr_write_b32 a74, v254\n" \
"v_accvgpr_write_b32 a75, v255\n" \
"v_accvgpr_read_b32 v252, a156\n" \
"v_accvgpr_read_b32 v253, a157\n" \
"v_accvgpr_read_b32 v254, a158\n" \
"v_accvgpr_read_b32 v255, a159\n" \
"v_mfma_f32_16x16x32_bf16 a[156:159], v[172:175], v[84:87], 0\n" \
"s_add_u32 m0, %[mB], 0x0\n" \
"s_add_u32 s84, s63, 0x0\n" \
"s_nop 0\n" \
"buffer_load_dwordx4 v26, s[68:71], s84 offen lds\n" \
"L_pf5:\n" \
"s_cbranch_execz L_pf9\n" \
"s_nop 0\n" \
"v_mfma_f32_16x16x32_bf16 a[156:159], v[176:179], v[88:91], a[156:159]\n" \
"s_add_u32 m0, %[mA], 0x800\n" \
"s_add_u32 s85, s62, 0x4800\n" \
"v_mfma_f32_16x16x32_bf16 a[176:179], v[152:155], v[96:99], 0\n" \
"v_accvgpr_read_b32 v180, a188\n" \
"v_accvgpr_read_b32 v181, a189\n" \
"v_mfma_f32_16x16x32_bf16 a[176:179], v[148:151], v[92:95], a[176:179]\n" \
"buffer_load_dwordx4 v24, s[76:79], s85 offen lds\n" \
"v_mfma_f32_16x16x32_bf16 a[180:183], v[156:159], v[92:95], 0\n" \
"s_add_u32 m0, %[mA], 0xc00\n" \
"s_add_u32 s84, s62, 0x6c00\n" \
"v_mfma_f32_16x16x32_bf16 a[180:183], v[160:163], v[96:99], a[180:183]\n" \
"v_accvgpr_read_b32 v182, a190\n" \
"v_accvgpr_read_b32 v183, a191\n" \
"v_mfma_f32_16x16x32_bf16 a[184:187], v[168:171], v[96:99], 0\n" \
"buffer_load_dwordx4 v25, s[76:79], s84 offen lds\n" \
"v_mfma_f32_16x16x32_bf16 a[184:187], v[164:167], v[92:95], a[184:187]\n" \
"v_cvt_pk_bf16_f32 v252, v252, v253\n" \
"v_cvt_pk_bf16_f32 v253, v254, v255\n" \
"v_mfma_f32_16x16x32_bf16 a[188:191], v[172:175], v[92:95], 0\n" \
"v_cvt_pk_bf16_f32 v254, v180, v181\n" \
"v_cvt_pk_bf16_f32 v255, v182, v183\n" \
"v_mfma_f32_16x16x32_bf16 a[188:191], v[176:179], v[96:99], a[188:191]\n" \
"v_accvgpr_write_b32 a76, v252\n" \
"v_accvgpr_write_b32 a77, v253\n" \
"v_accvgpr_write_b32 a78, v254\n" \
"v_accvgpr_write_b32 a79, v255\n" \
"v_accvgpr_read_b32 v252, a208\n" \
"v_accvgpr_read_b32 v253, a209\n" \
"v_accvgpr_read_b32 v254, a210\n" \
"v_accvgpr_read_b32 v255, a211\n" \
"v_mfma_f32_16x16x32_bf16 a[208:211], v[152:155], v[104:107], 0\n" \
"v_cvt_pk_bf16_f32 v252, v252, v253\n" \
"s_add_u32 m0, %[mB], 0x400\n" \
"v_mfma_f32_16x16x32_bf16 a[208:211], v[148:151], v[100:103], a[208:211]\n" \
"s_add_u32 s85, s63, 0x8000\n" \
"v_accvgpr_read_b32 v180, a240\n" \
"v_accvgpr_read_b32 v181, a241\n" \
"v_accvgpr_read_b32 v182, a242\n" \
"v_accvgpr_read_b32 v183, a243\n" \
"v_cvt_pk_bf16_f32 v253, v254, v255\n" \
"v_cvt_pk_bf16_f32 v254, v180, v181\n" \
"v_cvt_pk_bf16_f32 v255, v182, v183\n" \
"v_accvgpr_write_b32 a104, v252\n" \
"v_accvgpr_write_b32 a105, v253\n" \
"v_accvgpr_write_b32 a106, v254\n" \
"v_accvgpr_write_b32 a107, v255\n" \
"v_accvgpr_read_b32 v252, a212\n" \
"v_accvgpr_read_b32 v253, a213\n" \
"v_accvgpr_read_b32 v254, a214\n" \
"v_accvgpr_read_b32 v255, a215\n" \
"v_mfma_f32_16x16x32_bf16 a[212:215], v[156:159], v[100:103], 0\n" \
"buffer_load_dwordx4 v26, s[68:71], s85 offen lds\n" \
"ds_read_b64_tr_b16 v[140:141], v39 offset:32768\n" \
"ds_read_b64_tr_b16 v[142:143], v39 offset:33728\n" \
"ds_read_b128 v[84:87], v22 offset:32768\n" \
"ds_read_b128 v[88:91], v23 offset:32768\n" \
"v_mfma_f32_16x16x32_bf16 a[212:215], v[160:163], v[104:107], a[212:215]\n" \
"ds_read_b64_tr_b16 v[128:129], v37 offset:36864\n" \
"ds_read_b64_tr_b16 v[130:131], v37 offset:37376\n" \
"v_accvgpr_read_b32 v184, a216\n" \
"v_accvgpr_read_b32 v185, a217\n" \
"v_accvgpr_read_b32 v186, a218\n" \
"v_accvgpr_read_b32 v187, a219\n" \
"v_mfma_f32_16x16x32_bf16 a[216:219], v[168:171], v[104:107], 0\n" \
"s_add_u32 m0, %[mA], 0x1000\n" \
"s_add_u32 s84, s62, 0x9000\n" \
"buffer_load_dwordx4 v24, s[76:79], s84 offen lds\n" \
"ds_read_b64_tr_b16 v[124:125], v37 offset:32768\n" \
"ds_read_b64_tr_b16 v[126:127], v37 offset:33280\n" \
"v_mfma_f32_16x16x32_bf16 a[216:219], v[164:167], v[100:103], a[216:219]\n" \
"v_cvt_pk_bf16_f32 v252, v252, v253\n" \
"v_accvgpr_read_b32 v180, a244\n" \
"v_accvgpr_read_b32 v181, a245\n" \
"v_accvgpr_read_b32 v182, a246\n" \
"v_accvgpr_read_b32 v183, a247\n" \
"v_cvt_pk_bf16_f32 v253, v254, v255\n" \
"v_cvt_pk_bf16_f32 v254, v180, v181\n" \
"v_cvt_pk_bf16_f32 v255, v182, v183\n" \
"v_accvgpr_write_b32 a108, v252\n" \
"v_accvgpr_write_b32 a109, v253\n" \
"v_accvgpr_write_b32 a110, v254\n" \
"v_accvgpr_write_b32 a111, v255\n" \
"v_accvgpr_read_b32 v252, a220\n" \
"v_accvgpr_read_b32 v253, a221\n" \
"v_accvgpr_read_b32 v254, a222\n" \
"v_accvgpr_read_b32 v255, a223\n" \
"v_mfma_f32_16x16x32_bf16 a[220:223], v[172:175], v[100:103], 0\n" \
"ds_read_b64_tr_b16 v[132:133], v38 offset:32768\n" \
"ds_read_b64_tr_b16 v[134:135], v38 offset:33280\n" \
"ds_read_b64_tr_b16 v[136:137], v38 offset:36864\n" \
"ds_read_b64_tr_b16 v[138:139], v38 offset:37376\n" \
"v_mfma_f32_16x16x32_bf16 a[220:223], v[176:179], v[104:107], a[220:223]\n" \
"ds_read_b128 v[52:55], v28 offset:32768\n" \
"ds_read_b128 v[56:59], v29 offset:32768\n" \
"v_mfma_f32_16x16x32_bf16 a[240:243], v[152:155], v[112:115], 0\n" \
"s_add_u32 m0, %[mA], 0x1400\n" \
"s_add_u32 s85, s62, 0xb400\n" \
"buffer_load_dwordx4 v25, s[76:79], s85 offen lds\n" \
"v_mfma_f32_16x16x32_bf16 a[240:243], v[148:151], v[108:111], a[240:243]\n" \
"ds_read_b64_tr_b16 v[116:117], v36 offset:32768\n" \
"ds_read_b64_tr_b16 v[118:119], v36 offset:33280\n" \
"v_mfma_f32_16x16x32_bf16 a[244:247], v[156:159], v[108:111], 0\n" \
"s_add_u32 m0, %[mB], 0x800\n" \
"s_add_u32 s84, s63, 0x10000\n" \
"v_mfma_f32_16x16x32_bf16 a[244:247], v[160:163], v[112:115], a[244:247]\n" \
"buffer_load_dwordx4 v27, s[68:71], s84 offen lds\n" \
"ds_read_b64_tr_b16 v[120:121], v36 offset:36864\n" \
"ds_read_b64_tr_b16 v[122:123], v36 offset:37376\n" \
"v_accvgpr_read_b32 v180, a248\n" \
"v_accvgpr_read_b32 v181, a249\n" \
"v_accvgpr_read_b32 v182, a250\n" \
"v_accvgpr_read_b32 v183, a251\n" \
"v_mfma_f32_16x16x32_bf16 a[248:251], v[168:171], v[112:115], 0\n" \
"ds_read_b128 v[60:63], v28 offset:34816\n" \
"v_cvt_pk_bf16_f32 v184, v184, v185\n" \
"v_mfma_f32_16x16x32_bf16 a[248:251], v[164:167], v[108:111], a[248:251]\n" \
"v_cvt_pk_bf16_f32 v185, v186, v187\n" \
"v_cvt_pk_bf16_f32 v186, v180, v181\n" \
"v_cvt_pk_bf16_f32 v187, v182, v183\n" \
"v_accvgpr_read_b32 v180, a252\n" \
"v_accvgpr_read_b32 v181, a253\n" \
"v_accvgpr_read_b32 v182, a254\n" \
"v_accvgpr_read_b32 v183, a255\n" \
"v_mfma_f32_16x16x32_bf16 a[252:255], v[172:175], v[108:111], 0\n" \
"ds_read_b128 v[64:67], v29 offset:34816\n" \
"s_add_u32 m0, %[mA], 0x1800\n" \
"v_mfma_f32_16x16x32_bf16 a[252:255], v[176:179], v[112:115], a[252:255]\n" \
"s_add_u32 s85, s62, 0xd800\n" \
"buffer_load_dwordx4 v24, s[76:79], s85 offen lds\n" \
"s_waitcnt lgkmcnt(12)\n" \
"v_smfmac_f32_16x16x64_bf16 a[16:19], v[140:143], v[84:91], v50\n" \
"v_mfma_f32_16x16x32_bf16 a[48:51], v[128:131], v[88:91], a[48:51]\n" \
"s_add_u32 m0, %[mA], 0x1c00\n" \
"s_add_u32 s85, s62, 0xfc00\n" \
"v_mfma_f32_16x16x32_bf16 a[48:51], v[124:127], v[84:87], a[48:51]\n" \
"ds_read_b128 v[68:71], v28 offset:36864\n" \
"s_waitcnt lgkmcnt(8)\n" \
"v_mfma_f32_16x16x32_bf16 a[80:83], v[84:87], v[132:135], a[80:83]\n" \
"buffer_load_dwordx4 v25, s[76:79], s85 offen lds\n" \
"v_mfma_f32_16x16x32_bf16 a[80:83], v[88:91], v[136:139], a[80:83]\n" \
"ds_read_b128 v[72:75], v29 offset:36864\n" \
"s_waitcnt lgkmcnt(6)\n" \
"v_smfmac_f32_16x16x64_bf16 a[112:115], v[140:143], v[52:59], v50\n" \
"v_mfma_f32_16x16x32_bf16 a[20:23], v[128:131], v[56:59], a[20:23]\n" \
"s_add_u32 m0, %[mB], 0xc00\n" \
"s_add_u32 s84, s63, 0x18000\n" \
"buffer_load_dwordx4 v27, s[68:71], s84 offen lds\n" \
"v_mfma_f32_16x16x32_bf16 a[20:23], v[124:127], v[52:55], a[20:23]\n" \
"ds_read_b128 v[76:79], v28 offset:38912\n" \
"ds_read_b128 v[80:83], v29 offset:38912\n" \
"v_mfma_f32_16x16x32_bf16 a[52:55], v[116:119], v[52:55], a[52:55]\n" \
"s_add_u32 s88, s44, 0x0\n" \
"s_waitcnt lgkmcnt(5)\n" \
"v_mfma_f32_16x16x32_bf16 a[52:55], v[120:123], v[56:59], a[52:55]\n" \
"buffer_store_dwordx4 v[200:203], v20, s[80:83], s88 offen offset:2048 sc1\n" \
"v_mfma_f32_16x16x32_bf16 a[84:87], v[56:59], v[136:139], a[84:87]\n" \
"ds_read_b64_tr_b16 v[152:153], v32 offset:40960\n" \
"ds_read_b64_tr_b16 v[154:155], v32 offset:41984\n" \
"v_mfma_f32_16x16x32_bf16 a[84:87], v[52:55], v[132:135], a[84:87]\n" \
"ds_read_b64_tr_b16 v[148:149], v32 offset:32768\n" \
"s_waitcnt lgkmcnt(7)\n" \
"v_smfmac_f32_16x16x64_bf16 a[116:119], v[140:143], v[60:67], v50\n" \
"v_mfma_f32_16x16x32_bf16 a[24:27], v[124:127], v[60:63], a[24:27]\n" \
"ds_read_b64_tr_b16 v[150:151], v32 offset:33792\n" \
"ds_read_b64_tr_b16 v[156:157], v33 offset:32768\n" \
"v_mfma_f32_16x16x32_bf16 a[24:27], v[128:131], v[64:67], a[24:27]\n" \
"ds_read_b64_tr_b16 v[158:159], v33 offset:33792\n" \
"ds_read_b64_tr_b16 v[160:161], v33 offset:40960\n" \
"v_mfma_f32_16x16x32_bf16 a[56:59], v[120:123], v[64:67], a[56:59]\n" \
"ds_read_b64_tr_b16 v[162:163], v33 offset:41984\n" \
"s_add_u32 s89, s44, 0x1000\n" \
"v_mfma_f32_16x16x32_bf16 a[56:59], v[116:119], v[60:63], a[56:59]\n" \
"buffer_store_dwordx4 v[204:207], v20, s[80:83], s89 offen offset:2048 sc1\n" \
"v_mfma_f32_16x16x32_bf16 a[88:91], v[60:63], v[132:135], a[88:91]\n" \
"ds_read_b64_tr_b16 v[168:169], v34 offset:40960\n" \
"ds_read_b64_tr_b16 v[170:171], v34 offset:41984\n" \
"v_mfma_f32_16x16x32_bf16 a[88:91], v[64:67], v[136:139], a[88:91]\n" \
"ds_read_b64_tr_b16 v[164:165], v34 offset:32768\n" \
"s_waitcnt lgkmcnt(13)\n" \
"v_smfmac_f32_16x16x64_bf16 a[120:123], v[140:143], v[68:75], v50\n" \
"v_mfma_f32_16x16x32_bf16 a[28:31], v[128:131], v[72:75], a[28:31]\n" \
"ds_read_b64_tr_b16 v[166:167], v34 offset:33792\n" \
"ds_read_b64_tr_b16 v[172:173], v35 offset:32768\n" \
"v_mfma_f32_16x16x32_bf16 a[28:31], v[124:127], v[68:71], a[28:31]\n" \
"ds_read_b64_tr_b16 v[174:175], v35 offset:33792\n" \
"ds_read_b64_tr_b16 v[176:177], v35 offset:40960\n" \
"v_mfma_f32_16x16x32_bf16 a[60:63], v[116:119], v[68:71], a[60:63]\n" \
"ds_read_b64_tr_b16 v[178:179], v35 offset:41984\n" \
"s_add_u32 s88, s44, 0x0\n" \
"v_mfma_f32_16x16x32_bf16 a[60:63], v[120:123], v[72:75], a[60:63]\n" \
"buffer_store_dwordx4 v[208:211], v20, s[80:83], s88 offen offset:3072 sc1\n" \
"v_mfma_f32_16x16x32_bf16 a[92:95], v[72:75], v[136:139], a[92:95]\n" \
"ds_read_b128 v[84:87], v48 offset:32768\n" \
"ds_read_b128 v[88:91], v49 offset:32768\n" \
"v_mfma_f32_16x16x32_bf16 a[92:95], v[68:71], v[132:135], a[92:95]\n" \
"ds_read_b128 v[92:95], v48 offset:34816\n" \
"s_waitcnt lgkmcnt(15)\n" \
"s_nop 0\n" \
"v_smfmac_f32_16x16x64_bf16 a[124:127], v[140:143], v[76:83], v50\n" \
"v_mfma_f32_16x16x32_bf16 a[0:3], v[124:127], v[76:79], a[0:3]\n" \
"ds_read_b128 v[96:99], v49 offset:34816\n" \
"ds_read_b128 v[100:103], v48 offset:36864\n" \
"v_mfma_f32_16x16x32_bf16 a[0:3], v[128:131], v[80:83], a[0:3]\n" \
"ds_read_b128 v[104:107], v49 offset:36864\n" \
"ds_read_b128 v[108:111], v48 offset:38912\n" \
"v_mfma_f32_16x16x32_bf16 a[4:7], v[120:123], v[80:83], a[4:7]\n" \
"s_add_u32 s62, s62, 0x80\n" \
"ds_read_b128 v[112:115], v49 offset:38912\n" \
"v_mfma_f32_16x16x32_bf16 a[4:7], v[116:119], v[76:79], a[4:7]\n" \
"s_cmp_lt_u32 s62, 0x480\n" \
"s_cselect_b32 s62, s62, 0\n" \
"v_mfma_f32_16x16x32_bf16 a[32:35], v[76:79], v[132:135], a[32:35]\n" \
"s_lshl_b32 s72, s62, 6\n" \
"s_add_u32 s72, s72, s37\n" \
"v_mfma_f32_16x16x32_bf16 a[32:35], v[80:83], v[136:139], a[32:35]\n" \
"s_lshl_b32 s63, s62, 12\n" \
"v_cvt_pk_bf16_f32 v252, v252, v253\n" \
"v_mfma_f32_16x16x32_bf16 a[128:131], v[152:155], v[56:59], a[128:131]\n" \
"v_cvt_pk_bf16_f32 v253, v254, v255\n" \
"v_cvt_pk_bf16_f32 v254, v180, v181\n" \
"v_mfma_f32_16x16x32_bf16 a[128:131], v[148:151], v[52:55], a[128:131]\n" \
"v_cvt_pk_bf16_f32 v255, v182, v183\n" \
"s_waitcnt lgkmcnt(11)\n" \
"v_mfma_f32_16x16x32_bf16 a[132:135], v[156:159], v[52:55], a[132:135]\n" \
"v_mfma_f32_16x16x32_bf16 a[132:135], v[160:163], v[56:59], a[132:135]\n" \
"v_mfma_f32_16x16x32_bf16 a[136:139], v[168:171], v[56:59], a[136:139]\n" \
"v_mfma_f32_16x16x32_bf16 a[136:139], v[164:167], v[52:55], a[136:139]\n" \
"s_waitcnt lgkmcnt(7)\n" \
"v_mfma_f32_16x16x32_bf16 a[140:143], v[172:175], v[52:55], a[140:143]\n" \
"v_mfma_f32_16x16x32_bf16 a[140:143], v[176:179], v[56:59], a[140:143]\n" \
"v_mfma_f32_16x16x32_bf16 a[160:163], v[152:155], v[64:67], a[160:163]\n" \
"v_mfma_f32_16x16x32_bf16 a[160:163], v[148:151], v[60:63], a[160:163]\n" \
"v_mfma_f32_16x16x32_bf16 a[164:167], v[156:159], v[60:63], a[164:167]\n" \
"v_mfma_f32_16x16x32_bf16 a[164:167], v[160:163], v[64:67], a[164:167]\n" \
"v_mfma_f32_16x16x32_bf16 a[168:171], v[168:171], v[64:67], a[168:171]\n" \
"v_mfma_f32_16x16x32_bf16 a[168:171], v[164:167], v[60:63], a[168:171]\n" \
"v_mfma_f32_16x16x32_bf16 a[172:175], v[172:175], v[60:63], a[172:175]\n" \
"v_mfma_f32_16x16x32_bf16 a[172:175], v[176:179], v[64:67], a[172:175]\n" \
"v_mfma_f32_16x16x32_bf16 a[192:195], v[152:155], v[72:75], a[192:195]\n" \
"v_mfma_f32_16x16x32_bf16 a[192:195], v[148:151], v[68:71], a[192:195]\n" \
"s_waitcnt vmcnt(3) lgkmcnt(0)\n" \
"s_barrier\n" \
"v_mfma_f32_16x16x32_bf16 a[196:199], v[156:159], v[68:71], a[196:199]\n" \
"s_add_u32 m0, s40, 0x8000\n" \
"s_add_u32 s85, s72, 0x0\n" \
"s_nop 0\n" \
"v_mfma_f32_16x16x32_bf16 a[196:199], v[160:163], v[72:75], a[196:199]\n" \
"buffer_load_dwordx4 v20, s[80:83], s85 offen sc1 lds\n" \
"v_mfma_f32_16x16x32_bf16 a[200:203], v[168:171], v[72:75], a[200:203]\n" \
"s_add_u32 m0, s40, 0x9000\n" \
"s_add_u32 s84, s72, 0x1000\n" \
"v_mfma_f32_16x16x32_bf16 a[200:203], v[164:167], v[68:71], a[200:203]\n" \
"ds_read_b64_tr_b16 v[140:141], v39 offset:0\n" \
"ds_read_b64_tr_b16 v[142:143], v39 offset:960\n" \
"v_mfma_f32_16x16x32_bf16 a[204:207], v[172:175], v[68:71], a[204:207]\n" \
"buffer_load_dwordx4 v20, s[80:83], s84 offen sc1 lds\n" \
"v_mfma_f32_16x16x32_bf16 a[204:207], v[176:179], v[72:75], a[204:207]\n" \
"ds_read_b64_tr_b16 v[128:129], v37 offset:4096\n" \
"ds_read_b64_tr_b16 v[130:131], v37 offset:4608\n" \
"v_mfma_f32_16x16x32_bf16 a[224:227], v[152:155], v[80:83], a[224:227]\n" \
"s_add_u32 m0, s40, 0xb000\n" \
"s_add_u32 s85, s62, 0x0\n" \
"v_mfma_f32_16x16x32_bf16 a[224:227], v[148:151], v[76:79], a[224:227]\n" \
"buffer_load_dwordx4 v21, s[76:79], s85 offen lds\n" \
"v_mfma_f32_16x16x32_bf16 a[228:231], v[156:159], v[76:79], a[228:231]\n" \
"ds_read_b64_tr_b16 v[124:125], v37 offset:0\n" \
"ds_read_b64_tr_b16 v[126:127], v37 offset:512\n" \
"v_mfma_f32_16x16x32_bf16 a[228:231], v[160:163], v[80:83], a[228:231]\n" \
"s_add_u32 m0, %[mA], 0x8000\n" \
"s_add_u32 s84, s62, 0x0\n" \
"v_mfma_f32_16x16x32_bf16 a[232:235], v[168:171], v[80:83], a[232:235]\n" \
"buffer_load_dwordx4 v24, s[76:79], s84 offen lds\n" \
"v_mfma_f32_16x16x32_bf16 a[232:235], v[164:167], v[76:79], a[232:235]\n" \
"ds_read_b64_tr_b16 v[132:133], v38 offset:0\n" \
"ds_read_b64_tr_b16 v[134:135], v38 offset:512\n" \
"v_mfma_f32_16x16x32_bf16 a[236:239], v[172:175], v[76:79], a[236:239]\n" \
"ds_read_b64_tr_b16 v[136:137], v38 offset:4096\n" \
"ds_read_b64_tr_b16 v[138:139], v38 offset:4608\n" \
"v_mfma_f32_16x16x32_bf16 a[236:239], v[176:179], v[80:83], a[236:239]\n" \
"s_add_u32 m0, %[mA], 0x8400\n" \
"s_add_u32 s85, s62, 0x2400\n" \
"v_mfma_f32_16x16x32_bf16 a[144:147], v[152:155], v[88:91], a[144:147]\n" \
"buffer_load_dwordx4 v25, s[76:79], s85 offen lds\n" \
"v_mfma_f32_16x16x32_bf16 a[144:147], v[148:151], v[84:87], a[144:147]\n" \
"ds_read_b128 v[52:55], v28 offset:0\n" \
"ds_read_b128 v[56:59], v29 offset:0\n" \
"v_mfma_f32_16x16x32_bf16 a[148:151], v[156:159], v[84:87], a[148:151]\n" \
"s_add_u32 m0, %[mB], 0x8000\n" \
"s_add_u32 s84, s63, 0x0\n" \
"s_nop 0\n" \
"v_mfma_f32_16x16x32_bf16 a[148:151], v[160:163], v[88:91], a[148:151]\n" \
"buffer_load_dwordx4 v26, s[68:71], s84 offen lds\n" \
"v_mfma_f32_16x16x32_bf16 a[152:155], v[168:171], v[88:91], a[152:155]\n" \
"ds_read_b64_tr_b16 v[116:117], v36 offset:0\n" \
"ds_read_b64_tr_b16 v[118:119], v36 offset:512\n" \
"v_mfma_f32_16x16x32_bf16 a[152:155], v[164:167], v[84:87], a[152:155]\n" \
"ds_read_b64_tr_b16 v[120:121], v36 offset:4096\n" \
"ds_read_b64_tr_b16 v[122:123], v36 offset:4608\n" \
"v_mfma_f32_16x16x32_bf16 a[156:159], v[172:175], v[84:87], a[156:159]\n" \
"s_add_u32 m0, %[mA], 0x8800\n" \
"s_add_u32 s85, s62, 0x4800\n" \
"v_mfma_f32_16x16x32_bf16 a[156:159], v[176:179], v[88:91], a[156:159]\n" \
"buffer_load_dwordx4 v24, s[76:79], s85 offen lds\n" \
"v_mfma_f32_16x16x32_bf16 a[176:179], v[152:155], v[96:99], a[176:179]\n" \
"ds_read_b128 v[84:87], v22 offset:0\n" \
"ds_read_b128 v[88:91], v23 offset:0\n" \
"v_mfma_f32_16x16x32_bf16 a[176:179], v[148:151], v[92:95], a[176:179]\n" \
"s_add_u32 m0, %[mA], 0x8c00\n" \
"s_add_u32 s84, s62, 0x6c00\n" \
"v_mfma_f32_16x16x32_bf16 a[180:183], v[156:159], v[92:95], a[180:183]\n" \
"buffer_load_dwordx4 v25, s[76:79], s84 offen lds\n" \
"v_mfma_f32_16x16x32_bf16 a[180:183], v[160:163], v[96:99], a[180:183]\n" \
"ds_read_b128 v[60:63], v28 offset:2048\n" \
"ds_read_b128 v[64:67], v29 offset:2048\n" \
"v_mfma_f32_16x16x32_bf16 a[184:187], v[168:171], v[96:99], a[184:187]\n" \
"ds_read_b128 v[68:71], v28 offset:4096\n" \
"ds_read_b128 v[72:75], v29 offset:4096\n" \
"v_mfma_f32_16x16x32_bf16 a[184:187], v[164:167], v[92:95], a[184:187]\n" \
"s_add_u32 m0, %[mB], 0x8400\n" \
"s_add_u32 s85, s63, 0x8000\n" \
"v_mfma_f32_16x16x32_bf16 a[188:191], v[172:175], v[92:95], a[188:191]\n" \
"buffer_load_dwordx4 v26, s[68:71], s85 offen lds\n" \
"v_mfma_f32_16x16x32_bf16 a[188:191], v[176:179], v[96:99], a[188:191]\n" \
"ds_read_b128 v[76:79], v28 offset:6144\n" \
"ds_read_b128 v[80:83], v29 offset:6144\n" \
"v_mfma_f32_16x16x32_bf16 a[208:211], v[152:155], v[104:107], a[208:211]\n" \
"s_add_u32 m0, %[mA], 0x9000\n" \
"s_add_u32 s84, s62, 0x9000\n" \
"v_mfma_f32_16x16x32_bf16 a[208:211], v[148:151], v[100:103], a[208:211]\n" \
"buffer_load_dwordx4 v24, s[76:79], s84 offen lds\n" \
"v_mfma_f32_16x16x32_bf16 a[212:215], v[156:159], v[100:103], a[212:215]\n" \
"ds_read_b128 v[92:95], v48 offset:2048\n" \
"ds_read_b128 v[96:99], v49 offset:2048\n" \
"v_mfma_f32_16x16x32_bf16 a[212:215], v[160:163], v[104:107], a[212:215]\n" \
"v_mfma_f32_16x16x32_bf16 a[216:219], v[168:171], v[104:107], a[216:219]\n" \
"s_add_u32 m0, %[mA], 0x9400\n" \
"s_add_u32 s85, s62, 0xb400\n" \
"v_mfma_f32_16x16x32_bf16 a[216:219], v[164:167], v[100:103], a[216:219]\n" \
"buffer_load_dwordx4 v25, s[76:79], s85 offen lds\n" \
"v_mfma_f32_16x16x32_bf16 a[220:223], v[172:175], v[100:103], a[220:223]\n" \
"ds_read_b128 v[100:103], v48 offset:4096\n" \
"v_mfma_f32_16x16x32_bf16 a[220:223], v[176:179], v[104:107], a[220:223]\n" \
"s_add_u32 m0, %[mB], 0x8800\n" \
"s_add_u32 s84, s63, 0x10000\n" \
"v_mfma_f32_16x16x32_bf16 a[240:243], v[152:155], v[112:115], a[240:243]\n" \
"buffer_load_dwordx4 v27, s[68:71], s84 offen lds\n" \
"v_mfma_f32_16x16x32_bf16 a[240:243], v[148:151], v[108:111], a[240:243]\n" \
"ds_read_b64_tr_b16 v[152:153], v32 offset:8192\n" \
"ds_read_b64_tr_b16 v[154:155], v32 offset:9216\n" \
"v_mfma_f32_16x16x32_bf16 a[244:247], v[156:159], v[108:111], a[244:247]\n" \
"ds_read_b64_tr_b16 v[148:149], v32 offset:0\n" \
"ds_read_b64_tr_b16 v[150:151], v32 offset:1024\n" \
"v_mfma_f32_16x16x32_bf16 a[244:247], v[160:163], v[112:115], a[244:247]\n" \
"s_add_u32 m0, %[mA], 0x9800\n" \
"s_add_u32 s85, s62, 0xd800\n" \
"v_mfma_f32_16x16x32_bf16 a[248:251], v[168:171], v[112:115], a[248:251]\n" \
"buffer_load_dwordx4 v24, s[76:79], s85 offen lds\n" \
"v_mfma_f32_16x16x32_bf16 a[248:251], v[164:167], v[108:111], a[248:251]\n" \
"ds_read_b64_tr_b16 v[156:157], v33 offset:0\n" \
"ds_read_b64_tr_b16 v[158:159], v33 offset:1024\n" \
"v_mfma_f32_16x16x32_bf16 a[252:255], v[172:175], v[108:111], a[252:255]\n" \
"ds_read_b64_tr_b16 v[160:161], v33 offset:8192\n" \
"ds_read_b64_tr_b16 v[162:163], v33 offset:9216\n" \
"v_mfma_f32_16x16x32_bf16 a[252:255], v[176:179], v[112:115], a[252:255]\n" \
"s_add_u32 m0, %[mA], 0x9c00\n" \
"s_waitcnt lgkmcnt(11)\n" \
"s_nop 0\n" \
"v_smfmac_f32_16x16x64_bf16 a[16:19], v[140:143], v[84:91], v50\n" \
"v_mfma_f32_16x16x32_bf16 a[48:51], v[128:131], v[88:91], a[48:51]\n" \
"s_add_u32 s85, s62, 0xfc00\n" \
"ds_read_b64_tr_b16 v[168:169], v34 offset:8192\n" \
"v_mfma_f32_16x16x32_bf16 a[48:51], v[124:127], v[84:87], a[48:51]\n" \
"buffer_load_dwordx4 v25, s[76:79], s85 offen lds\n" \
"v_mfma_f32_16x16x32_bf16 a[80:83], v[84:87], v[132:135], a[80:83]\n" \
"s_add_u32 m0, %[mB], 0x8c00\n" \
"s_add_u32 s84, s63, 0x18000\n" \
"v_mfma_f32_16x16x32_bf16 a[80:83], v[88:91], v[136:139], a[80:83]\n" \
"ds_read_b64_tr_b16 v[170:171], v34 offset:9216\n" \
"ds_read_b64_tr_b16 v[164:165], v34 offset:0\n" \
"v_smfmac_f32_16x16x64_bf16 a[112:115], v[140:143], v[52:59], v50\n" \
"v_mfma_f32_16x16x32_bf16 a[20:23], v[128:131], v[56:59], a[20:23]\n" \
"buffer_load_dwordx4 v27, s[68:71], s84 offen lds\n" \
"v_mfma_f32_16x16x32_bf16 a[20:23], v[124:127], v[52:55], a[20:23]\n" \
"s_add_u32 s89, s44, 0x1000\n" \
"ds_read_b64_tr_b16 v[166:167], v34 offset:1024\n" \
"v_mfma_f32_16x16x32_bf16 a[52:55], v[116:119], v[52:55], a[52:55]\n" \
"ds_read_b64_tr_b16 v[172:173], v35 offset:0\n" \
"ds_read_b64_tr_b16 v[174:175], v35 offset:1024\n" \
"v_mfma_f32_16x16x32_bf16 a[52:55], v[120:123], v[56:59], a[52:55]\n" \
"buffer_store_dwordx4 v[212:215], v20, s[80:83], s89 offen offset:3072 sc1\n" \
"v_mfma_f32_16x16x32_bf16 a[84:87], v[56:59], v[136:139], a[84:87]\n" \
"ds_read_b64_tr_b16 v[176:177], v35 offset:8192\n" \
"ds_read_b64_tr_b16 v[178:179], v35 offset:9216\n" \
"v_mfma_f32_16x16x32_bf16 a[84:87], v[52:55], v[132:135], a[84:87]\n" \
"ds_read_b128 v[84:87], v48 offset:0\n" \
"ds_read_b128 v[88:91], v49 offset:0\n" \
"v_smfmac_f32_16x16x64_bf16 a[116:119], v[140:143], v[60:67], v50\n" \
"v_mfma_f32_16x16x32_bf16 a[24:27], v[124:127], v[60:63], a[24:27]\n" \
"ds_read_b128 v[104:107], v49 offset:4096\n" \
"ds_read_b128 v[108:111], v48 offset:6144\n" \
"v_mfma_f32_16x16x32_bf16 a[24:27], v[128:131], v[64:67], a[24:27]\n" \
"s_add_u32 s88, s42, 0x0\n" \
"ds_read_b128 v[112:115], v49 offset:6144\n" \
"v_mfma_f32_16x16x32_bf16 a[56:59], v[120:123], v[64:67], a[56:59]\n" \
"buffer_store_dwordx4 v[216:219], v20, s[80:83], s88 offen offset:0 sc1\n" \
"v_mfma_f32_16x16x32_bf16 a[56:59], v[116:119], v[60:63], a[56:59]\n" \
"s_add_u32 s62, s62, 0x80\n" \
"s_add_u32 s94, %[b], %[w]\n" \
"v_mfma_f32_16x16x32_bf16 a[88:91], v[60:63], v[132:135], a[88:91]\n" \
"s_cmp_lt_u32 s62, 0x480\n" \
"s_cselect_b32 s62, s62, 0\n" \
"v_mfma_f32_16x16x32_bf16 a[88:91], v[64:67], v[136:139], a[88:91]\n" \
"s_lshl_b32 s72, s62, 6\n" \
"s_add_u32 s72, s72, s37\n" \
"v_smfmac_f32_16x16x64_bf16 a[120:123], v[140:143], v[68:75], v50\n" \
"v_mfma_f32_16x16x32_bf16 a[28:31], v[128:131], v[72:75], a[28:31]\n" \
"s_lshl_b32 s63, s62, 12\n" \
"s_and_b32 s96, 7, %[b]\n" \
"v_mfma_f32_16x16x32_bf16 a[28:31], v[124:127], v[68:71], a[28:31]\n" \
"s_cmp_lg_u32 s94, 0\n" \
"s_cselect_b32 s95, 0x40000000, 0x5bc0000\n" \
"v_mfma_f32_16x16x32_bf16 a[60:63], v[116:119], v[68:71], a[60:63]\n" \
"s_add_u32 s89, s42, 0x1000\n" \
"s_mul_i32 s97, 0x48, s96\n" \
"v_mfma_f32_16x16x32_bf16 a[60:63], v[120:123], v[72:75], a[60:63]\n" \
"buffer_store_dwordx4 v[220:223], v20, s[80:83], s89 offen offset:0 sc1\n" \
"v_mfma_f32_16x16x32_bf16 a[92:95], v[72:75], v[136:139], a[92:95]\n" \
"s_add_u32 s98, 0x47, s97\n" \
"v_writelane_b32 v11, s95, 50\n" \
"v_mfma_f32_16x16x32_bf16 a[92:95], v[68:71], v[132:135], a[92:95]\n" \
"s_cmp_lt_u32 %[w], 3\n" \
"s_cselect_b32 s99, s98, 0xfff\n" \
"v_smfmac_f32_16x16x64_bf16 a[124:127], v[140:143], v[76:83], v50\n" \
"v_mfma_f32_16x16x32_bf16 a[0:3], v[124:127], v[76:79], a[0:3]\n" \
"s_lshr_b32 s94, %[b], 3\n" \
"s_lshl_b32 s67, s96, 2\n" \
"v_mfma_f32_16x16x32_bf16 a[0:3], v[128:131], v[80:83], a[0:3]\n" \
"s_and_b32 s95, 3, s94\n" \
"s_mul_i32 s100, 0x480, s99\n" \
"v_mfma_f32_16x16x32_bf16 a[4:7], v[120:123], v[80:83], a[4:7]\n" \
"s_lshl_b32 s64, %[w], 5\n" \
"s_add_u32 s98, s67, s95\n" \
"v_mfma_f32_16x16x32_bf16 a[4:7], v[116:119], v[76:79], a[4:7]\n" \
"s_add_u32 s101, 0x120, s100\n" \
"s_add_u32 s65, s97, s64\n" \
"v_mfma_f32_16x16x32_bf16 a[32:35], v[76:79], v[132:135], a[32:35]\n" \
"s_mul_i32 s99, 0x24000, s98\n" \
"v_writelane_b32 v11, s101, 15\n" \
"v_mfma_f32_16x16x32_bf16 a[32:35], v[80:83], v[136:139], a[32:35]\n" \
"s_mul_i32 s66, 0x480, s65\n" \
"s_waitcnt lgkmcnt(0)\n" \
"v_mfma_f32_16x16x32_bf16 a[128:131], v[152:155], v[56:59], a[128:131]\n" \
"s_add_u32 s100, 0x1b000, s99\n" \
"s_min_u32 s97, 2, %[w]\n" \
"v_mfma_f32_16x16x32_bf16 a[128:131], v[148:151], v[52:55], a[128:131]\n" \
"s_cmp_lt_u32 %[w], 3\n" \
"s_cselect_b32 s101, s66, s100\n" \
"v_mfma_f32_16x16x32_bf16 a[132:135], v[156:159], v[52:55], a[132:135]\n" \
"s_mul_i32 s64, 0x2400, s97\n" \
"v_writelane_b32 v11, s101, 16\n" \
"v_mfma_f32_16x16x32_bf16 a[132:135], v[160:163], v[56:59], a[132:135]\n" \
"s_add_u32 s65, 0x20000, s64\n" \
"s_mul_i32 s94, 0x9000, s97\n" \
"v_mfma_f32_16x16x32_bf16 a[136:139], v[168:171], v[56:59], a[136:139]\n" \
"s_mul_i32 s95, 0x2400, %[w]\n" \
"v_writelane_b32 v11, s65, 17\n" \
"v_mfma_f32_16x16x32_bf16 a[136:139], v[164:167], v[52:55], a[136:139]\n" \
"s_add_u32 s67, s99, s94\n" \
"v_writelane_b32 v11, s67, 22\n" \
"v_mfma_f32_16x16x32_bf16 a[140:143], v[172:175], v[52:55], a[140:143]\n" \
"s_cmp_lt_u32 %[w], 3\n" \
"s_cselect_b32 s98, s95, 0x12000\n" \
"v_mfma_f32_16x16x32_bf16 a[140:143], v[176:179], v[56:59], a[140:143]\n" \
"s_add_u32 s66, 0x20000, s95\n" \
"v_writelane_b32 v11, s98, 18\n" \
"v_mfma_f32_16x16x32_bf16 a[160:163], v[152:155], v[64:67], a[160:163]\n" \
"s_cmp_lt_u32 %[w], 3\n" \
"s_cselect_b32 s100, s66, 0xfc00\n" \
"v_mfma_f32_16x16x32_bf16 a[160:163], v[148:151], v[60:63], a[160:163]\n" \
"v_writelane_b32 v11, s95, 19\n" \
"s_lshl_b32 s101, %[w], 10\n" \
"v_mfma_f32_16x16x32_bf16 a[164:167], v[156:159], v[60:63], a[164:167]\n" \
"s_lshr_b32 s65, s96, 1\n" \
"v_writelane_b32 v11, s100, 20\n" \
"v_mfma_f32_16x16x32_bf16 a[164:167], v[160:163], v[64:67], a[164:167]\n" \
"s_cmp_eq_u32 %[w], s65\n" \
"s_cselect_b32 s97, 0xff0000, 0\n" \
"v_mfma_f32_16x16x32_bf16 a[168:171], v[168:171], v[64:67], a[168:171]\n" \
"s_add_u32 s64, 0x1d400, s101\n" \
"v_writelane_b32 v11, s64, 21\n" \
"v_mfma_f32_16x16x32_bf16 a[168:171], v[164:167], v[60:63], a[168:171]\n" \
"v_writelane_b32 v11, s97, 6\n" \
"v_mfma_f32_16x16x32_bf16 a[172:175], v[172:175], v[60:63], a[172:175]\n" \
"v_mfma_f32_16x16x32_bf16 a[172:175], v[176:179], v[64:67], a[172:175]\n" \
"v_mfma_f32_16x16x32_bf16 a[192:195], v[152:155], v[72:75], a[192:195]\n" \
"v_mfma_f32_16x16x32_bf16 a[192:195], v[148:151], v[68:71], a[192:195]\n" \
"s_waitcnt vmcnt(3)\n" \
"s_barrier\n" \
"v_mfma_f32_16x16x32_bf16 a[196:199], v[156:159], v[68:71], a[196:199]\n" \
"s_add_u32 m0, s40, 0x0\n" \
"s_add_u32 s85, s72, 0x0\n" \
"s_nop 0\n" \
"v_mfma_f32_16x16x32_bf16 a[196:199], v[160:163], v[72:75], a[196:199]\n" \
"buffer_load_dwordx4 v20, s[80:83], s85 offen sc1 lds\n" \
"v_mfma_f32_16x16x32_bf16 a[200:203], v[168:171], v[72:75], a[200:203]\n" \
"s_add_u32 m0, s40, 0x1000\n" \
"s_add_u32 s84, s72, 0x1000\n" \
"v_mfma_f32_16x16x32_bf16 a[200:203], v[164:167], v[68:71], a[200:203]\n" \
"ds_read_b64_tr_b16 v[140:141], v39 offset:32768\n" \
"ds_read_b64_tr_b16 v[142:143], v39 offset:33728\n" \
"v_mfma_f32_16x16x32_bf16 a[204:207], v[172:175], v[68:71], a[204:207]\n" \
"buffer_load_dwordx4 v20, s[80:83], s84 offen sc1 lds\n" \
"v_mfma_f32_16x16x32_bf16 a[204:207], v[176:179], v[72:75], a[204:207]\n" \
"ds_read_b64_tr_b16 v[128:129], v37 offset:36864\n" \
"ds_read_b64_tr_b16 v[130:131], v37 offset:37376\n" \
"v_mfma_f32_16x16x32_bf16 a[224:227], v[152:155], v[80:83], a[224:227]\n" \
"s_add_u32 m0, s40, 0x3000\n" \
"s_add_u32 s85, s62, 0x0\n" \
"v_mfma_f32_16x16x32_bf16 a[224:227], v[148:151], v[76:79], a[224:227]\n" \
"buffer_load_dwordx4 v21, s[76:79], s85 offen lds\n" \
"v_mfma_f32_16x16x32_bf16 a[228:231], v[156:159], v[76:79], a[228:231]\n" \
"ds_read_b64_tr_b16 v[124:125], v37 offset:32768\n" \
"ds_read_b64_tr_b16 v[126:127], v37 offset:33280\n" \
"v_mfma_f32_16x16x32_bf16 a[228:231], v[160:163], v[80:83], a[228:231]\n" \
"s_add_u32 m0, %[mA], 0x0\n" \
"s_add_u32 s84, s62, 0x0\n" \
"s_nop 0\n" \
"v_mfma_f32_16x16x32_bf16 a[232:235], v[168:171], v[80:83], a[232:235]\n" \
"buffer_load_dwordx4 v24, s[76:79], s84 offen lds\n" \
"v_mfma_f32_16x16x32_bf16 a[232:235], v[164:167], v[76:79], a[232:235]\n" \
"ds_read_b64_tr_b16 v[132:133], v38 offset:32768\n" \
"ds_read_b64_tr_b16 v[134:135], v38 offset:33280\n" \
"v_mfma_f32_16x16x32_bf16 a[236:239], v[172:175], v[76:79], a[236:239]\n" \
"ds_read_b64_tr_b16 v[136:137], v38 offset:36864\n" \
"ds_read_b64_tr_b16 v[138:139], v38 offset:37376\n" \
"v_mfma_f32_16x16x32_bf16 a[236:239], v[176:179], v[80:83], a[236:239]\n" \
"s_add_u32 m0, %[mA], 0x400\n" \
"s_add_u32 s85, s62, 0x2400\n" \
"v_mfma_f32_16x16x32_bf16 a[144:147], v[152:155], v[88:91], a[144:147]\n" \
"buffer_load_dwordx4 v25, s[76:79], s85 offen lds\n" \
"L_pf6:\n" \
"s_cbranch_execz L_pf10\n" \
"s_nop 0\n" \
"v_mfma_f32_16x16x32_bf16 a[144:147], v[148:151], v[84:87], a[144:147]\n" \
"ds_read_b128 v[52:55], v28 offset:32768\n" \
"ds_read_b128 v[56:59], v29 offset:32768\n" \
"v_mfma_f32_16x16x32_bf16 a[148:151], v[156:159], v[84:87], a[148:151]\n" \
"s_add_u32 m0, %[mB], 0x0\n" \
"s_add_u32 s84, s63, 0x0\n" \
"v_mfma_f32_16x16x32_bf16 a[148:151], v[160:163], v[88:91], a[148:151]\n" \
"buffer_load_dwordx4 v26, s[68:71], s84 offen lds\n" \
"v_mfma_f32_16x16x32_bf16 a[152:155], v[168:171], v[88:91], a[152:155]\n" \
"ds_read_b64_tr_b16 v[116:117], v36 offset:32768\n" \
"ds_read_b64_tr_b16 v[118:119], v36 offset:33280\n" \
"v_mfma_f32_16x16x32_bf16 a[152:155], v[164:167], v[84:87], a[152:155]\n" \
"ds_read_b64_tr_b16 v[120:121], v36 offset:36864\n" \
"ds_read_b64_tr_b16 v[122:123], v36 offset:37376\n" \
"v_mfma_f32_16x16x32_bf16 a[156:159], v[172:175], v[84:87], a[156:159]\n" \
"s_add_u32 m0, %[mA], 0x800\n" \
"s_add_u32 s85, s62, 0x4800\n" \
"v_mfma_f32_16x16x32_bf16 a[156:159], v[176:179], v[88:91], a[156:159]\n" \
"buffer_load_dwordx4 v24, s[76:79], s85 offen lds\n" \
"v_mfma_f32_16x16x32_bf16 a[176:179], v[152:155], v[96:99], a[176:179]\n" \
"ds_read_b128 v[84:87], v22 offset:32768\n" \
"ds_read_b128 v[88:91], v23 offset:32768\n" \
"v_mfma_f32_16x16x32_bf16 a[176:179], v[148:151], v[92:95], a[176:179]\n" \
"s_add_u32 m0, %[mA], 0xc00\n" \
"s_add_u32 s84, s62, 0x6c00\n" \
"v_mfma_f32_16x16x32_bf16 a[180:183], v[156:159], v[92:95], a[180:183]\n" \
"buffer_load_dwordx4 v25, s[76:79], s84 offen lds\n" \
"v_mfma_f32_16x16x32_bf16 a[180:183], v[160:163], v[96:99], a[180:183]\n" \
"ds_read_b128 v[60:63], v28 offset:34816\n" \
"ds_read_b128 v[64:67], v29 offset:34816\n" \
"v_mfma_f32_16x16x32_bf16 a[184:187], v[168:171], v[96:99], a[184:187]\n" \
"ds_read_b128 v[68:71], v28 offset:36864\n" \
"ds_read_b128 v[72:75], v29 offset:36864\n" \
"v_mfma_f32_16x16x32_bf16 a[184:187], v[164:167], v[92:95], a[184:187]\n" \
"s_add_u32 m0, %[mB], 0x400\n" \
"s_add_u32 s85, s63, 0x8000\n" \
"v_mfma_f32_16x16x32_bf16 a[188:191], v[172:175], v[92:95], a[188:191]\n" \
"buffer_load_dwordx4 v26, s[68:71], s85 offen lds\n" \
"v_mfma_f32_16x16x32_bf16 a[188:191], v[176:179], v[96:99], a[188:191]\n" \
"ds_read_b128 v[76:79], v28 offset:38912\n" \
"ds_read_b128 v[80:83], v29 offset:38912\n" \
"v_mfma_f32_16x16x32_bf16 a[208:211], v[152:155], v[104:107], a[208:211]\n" \
"s_add_u32 m0, %[mA], 0x1000\n" \
"s_add_u32 s84, s62, 0x9000\n" \
"v_mfma_f32_16x16x32_bf16 a[208:211], v[148:151], v[100:103], a[208:211]\n" \
"buffer_load_dwordx4 v24, s[76:79], s84 offen lds\n" \
"v_mfma_f32_16x16x32_bf16 a[212:215], v[156:159], v[100:103], a[212:215]\n" \
"ds_read_b128 v[92:95], v48 offset:34816\n" \
"ds_read_b128 v[96:99], v49 offset:34816\n" \
"v_mfma_f32_16x16x32_bf16 a[212:215], v[160:163], v[104:107], a[212:215]\n" \
"v_mfma_f32_16x16x32_bf16 a[216:219], v[168:171], v[104:107], a[216:219]\n" \
"s_add_u32 m0, %[mA], 0x1400\n" \
"s_add_u32 s85, s62, 0xb400\n" \
"v_mfma_f32_16x16x32_bf16 a[216:219], v[164:167], v[100:103], a[216:219]\n" \
"buffer_load_dwordx4 v25, s[76:79], s85 offen lds\n" \
"v_mfma_f32_16x16x32_bf16 a[220:223], v[172:175], v[100:103], a[220:223]\n" \
"ds_read_b128 v[100:103], v48 offset:36864\n" \
"v_mfma_f32_16x16x32_bf16 a[220:223], v[176:179], v[104:107], a[220:223]\n" \
"s_add_u32 m0, %[mB], 0x800\n" \
"s_add_u32 s84, s63, 0x10000\n" \
"v_mfma_f32_16x16x32_bf16 a[240:243], v[152:155], v[112:115], a[240:243]\n" \
"buffer_load_dwordx4 v27, s[68:71], s84 offen lds\n" \
"v_mfma_f32_16x16x32_bf16 a[240:243], v[148:151], v[108:111], a[240:243]\n" \
"ds_read_b64_tr_b16 v[152:153], v32 offset:40960\n" \
"ds_read_b64_tr_b16 v[154:155], v32 offset:41984\n" \
"v_mfma_f32_16x16x32_bf16 a[244:247], v[156:159], v[108:111], a[244:247]\n" \
"ds_read_b64_tr_b16 v[148:149], v32 offset:32768\n" \
"ds_read_b64_tr_b16 v[150:151], v32 offset:33792\n" \
"v_mfma_f32_16x16x32_bf16 a[244:247], v[160:163], v[112:115], a[244:247]\n" \
"s_add_u32 m0, %[mA], 0x1800\n" \
"s_add_u32 s85, s62, 0xd800\n" \
"v_mfma_f32_16x16x32_bf16 a[248:251], v[168:171], v[112:115], a[248:251]\n" \
"buffer_load_dwordx4 v24, s[76:79], s85 offen lds\n" \
"v_mfma_f32_16x16x32_bf16 a[248:251], v[164:167], v[108:111], a[248:251]\n" \
"ds_read_b64_tr_b16 v[156:157], v33 offset:32768\n" \
"ds_read_b64_tr_b16 v[158:159], v33 offset:33792\n" \
"v_mfma_f32_16x16x32_bf16 a[252:255], v[172:175], v[108:111], a[252:255]\n" \
"ds_read_b64_tr_b16 v[160:161], v33 offset:40960\n" \
"ds_read_b64_tr_b16 v[162:163], v33 offset:41984\n" \
"v_mfma_f32_16x16x32_bf16 a[252:255], v[176:179], v[112:115], a[252:255]\n" \
"s_add_u32 m0, %[mA], 0x1c00\n" \
"s_waitcnt lgkmcnt(11)\n" \
"s_nop 0\n" \
"v_smfmac_f32_16x16x64_bf16 a[16:19], v[140:143], v[84:91], v50\n" \
"v_mfma_f32_16x16x32_bf16 a[48:51], v[128:131], v[88:91], a[48:51]\n" \
"s_add_u32 s85, s62, 0xfc00\n" \
"ds_read_b64_tr_b16 v[168:169], v34 offset:40960\n" \
"v_mfma_f32_16x16x32_bf16 a[48:51], v[124:127], v[84:87], a[48:51]\n" \
"buffer_load_dwordx4 v25, s[76:79], s85 offen lds\n" \
"v_mfma_f32_16x16x32_bf16 a[80:83], v[84:87], v[132:135], a[80:83]\n" \
"s_add_u32 m0, %[mB], 0xc00\n" \
"s_add_u32 s84, s63, 0x18000\n" \
"v_mfma_f32_16x16x32_bf16 a[80:83], v[88:91], v[136:139], a[80:83]\n" \
"ds_read_b64_tr_b16 v[170:171], v34 offset:41984\n" \
"ds_read_b64_tr_b16 v[164:165], v34 offset:32768\n" \
"v_smfmac_f32_16x16x64_bf16 a[112:115], v[140:143], v[52:59], v50\n" \
"v_mfma_f32_16x16x32_bf16 a[20:23], v[128:131], v[56:59], a[20:23]\n" \
"buffer_load_dwordx4 v27, s[68:71], s84 offen lds\n" \
"v_mfma_f32_16x16x32_bf16 a[20:23], v[124:127], v[52:55], a[20:23]\n" \
"s_add_u32 s88, s42, 0x2000\n" \
"ds_read_b64_tr_b16 v[166:167], v34 offset:33792\n" \
"v_mfma_f32_16x16x32_bf16 a[52:55], v[116:119], v[52:55], a[52:55]\n" \
"ds_read_b64_tr_b16 v[172:173], v35 offset:32768\n" \
"ds_read_b64_tr_b16 v[174:175], v35 offset:33792\n" \
"v_mfma_f32_16x16x32_bf16 a[52:55], v[120:123], v[56:59], a[52:55]\n" \
"buffer_store_dwordx4 v[224:227], v20, s[80:83], s88 offen offset:0 sc1\n" \
"v_mfma_f32_16x16x32_bf16 a[84:87], v[56:59], v[136:139], a[84:87]\n" \
"ds_read_b64_tr_b16 v[176:177], v35 offset:40960\n" \
"ds_read_b64_tr_b16 v[178:179], v35 offset:41984\n" \
"v_mfma_f32_16x16x32_bf16 a[84:87], v[52:55], v[132:135], a[84:87]\n" \
"ds_read_b128 v[84:87], v48 offset:32768\n" \
"ds_read_b128 v[88:91], v49 offset:32768\n" \
"v_smfmac_f32_16x16x64_bf16 a[116:119], v[140:143], v[60:67], v50\n" \
"v_mfma_f32_16x16x32_bf16 a[24:27], v[124:127], v[60:63], a[24:27]\n" \
"ds_read_b128 v[104:107], v49 offset:36864\n" \
"ds_read_b128 v[108:111], v48 offset:38912\n" \
"v_mfma_f32_16x16x32_bf16 a[24:27], v[128:131], v[64:67], a[24:27]\n" \
"s_add_u32 s89, s42, 0x3000\n" \
"ds_read_b128 v[112:115], v49 offset:38912\n" \
"v_mfma_f32_16x16x32_bf16 a[56:59], v[120:123], v[64:67], a[56:59]\n" \
"buffer_store_dwordx4 v[228:231], v20, s[80:83], s89 offen offset:0 sc1\n" \
"v_mfma_f32_16x16x32_bf16 a[56:59], v[116:119], v[60:63], a[56:59]\n" \
"s_add_u32 s62, s62, 0x80\n" \
"s_lshr_b32 s96, %[b], 3\n" \
"v_mfma_f32_16x16x32_bf16 a[88:91], v[60:63], v[132:135], a[88:91]\n" \
"s_cmp_lt_u32 s62, 0x480\n" \
"s_cselect_b32 s62, s62, 0\n" \
"v_mfma_f32_16x16x32_bf16 a[88:91], v[64:67], v[136:139], a[88:91]\n" \
"s_lshl_b32 s72, s62, 6\n" \
"s_add_u32 s72, s72, s37\n" \
"v_smfmac_f32_16x16x64_bf16 a[120:123], v[140:143], v[68:75], v50\n" \
"v_mfma_f32_16x16x32_bf16 a[28:31], v[128:131], v[72:75], a[28:31]\n" \
"s_lshl_b32 s63, s62, 12\n" \
"s_and_b32 s97, 3, s96\n" \
"v_mfma_f32_16x16x32_bf16 a[28:31], v[124:127], v[68:71], a[28:31]\n" \
"s_and_b32 s94, 7, %[b]\n" \
"s_add_u32 s98, 1, s97\n" \
"v_mfma_f32_16x16x32_bf16 a[60:63], v[116:119], v[68:71], a[60:63]\n" \
"s_add_u32 s88, s42, 0x0\n" \
"s_lshl_b32 s95, s94, 2\n" \
"v_mfma_f32_16x16x32_bf16 a[60:63], v[120:123], v[72:75], a[60:63]\n" \
"buffer_store_dwordx4 v[232:235], v20, s[80:83], s88 offen offset:1024 sc1\n" \
"v_mfma_f32_16x16x32_bf16 a[92:95], v[72:75], v[136:139], a[92:95]\n" \
"s_and_b32 s99, 3, s98\n" \
"s_add_u32 s100, s95, s99\n" \
"v_mfma_f32_16x16x32_bf16 a[92:95], v[68:71], v[132:135], a[92:95]\n" \
"s_mul_i32 s101, 0x24000, s100\n" \
"s_mul_i32 s64, 0x9000, %[w]\n" \
"v_smfmac_f32_16x16x64_bf16 a[124:127], v[140:143], v[76:83], v50\n" \
"v_mfma_f32_16x16x32_bf16 a[0:3], v[124:127], v[76:79], a[0:3]\n" \
"s_and_b32 s96, 2, %[w]\n" \
"s_add_u32 s65, s101, s64\n" \
"v_mfma_f32_16x16x32_bf16 a[0:3], v[128:131], v[80:83], a[0:3]\n" \
"s_lshr_b32 s66, %[b], 5\n" \
"s_lshl_b32 s98, s96, 1\n" \
"v_mfma_f32_16x16x32_bf16 a[4:7], v[120:123], v[80:83], a[4:7]\n" \
"s_and_b32 s99, 1, %[w]\n" \
"v_writelane_b32 v11, s65, 23\n" \
"v_mfma_f32_16x16x32_bf16 a[4:7], v[116:119], v[76:79], a[4:7]\n" \
"s_lshl_b32 s67, s66, 3\n" \
"s_add_u32 s101, s98, s99\n" \
"v_mfma_f32_16x16x32_bf16 a[32:35], v[76:79], v[132:135], a[32:35]\n" \
"s_add_u32 s65, s67, s101\n" \
"s_mul_i32 s66, 0x24800, s65\n" \
"v_mfma_f32_16x16x32_bf16 a[32:35], v[80:83], v[136:139], a[32:35]\n" \
"s_lshl_b32 s98, s100, 12\n" \
"s_waitcnt lgkmcnt(0)\n" \
"v_mfma_f32_16x16x32_bf16 a[128:131], v[152:155], v[56:59], a[128:131]\n" \
"s_add_u32 s96, 0x3000000, s66\n" \
"s_add_u32 s99, 0x4800, s98\n" \
"v_mfma_f32_16x16x32_bf16 a[128:131], v[148:151], v[52:55], a[128:131]\n" \
"s_add_u32 s65, s96, s99\n" \
"s_sub_u32 s100, 0x78, s67\n" \
"v_mfma_f32_16x16x32_bf16 a[132:135], v[156:159], v[52:55], a[132:135]\n" \
"v_writelane_b32 v11, s65, 26\n" \
"s_add_u32 s98, s100, s101\n" \
"v_mfma_f32_16x16x32_bf16 a[132:135], v[160:163], v[56:59], a[132:135]\n" \
"s_mul_i32 s65, 0x24800, s98\n" \
"s_add_u32 s67, 0x3000000, s65\n" \
"v_mfma_f32_16x16x32_bf16 a[136:139], v[168:171], v[56:59], a[136:139]\n" \
"s_add_u32 s101, 2, s97\n" \
"s_add_u32 s100, s67, s99\n" \
"v_mfma_f32_16x16x32_bf16 a[136:139], v[164:167], v[52:55], a[136:139]\n" \
"s_and_b32 s98, 3, s101\n" \
"v_writelane_b32 v11, s100, 29\n" \
"v_mfma_f32_16x16x32_bf16 a[140:143], v[172:175], v[52:55], a[140:143]\n" \
"s_add_u32 s99, s95, s98\n" \
"s_mul_i32 s100, 0x24000, s99\n" \
"v_mfma_f32_16x16x32_bf16 a[140:143], v[176:179], v[56:59], a[140:143]\n" \
"s_add_u32 s101, s100, s64\n" \
"s_lshl_b32 s98, s99, 12\n" \
"v_mfma_f32_16x16x32_bf16 a[160:163], v[152:155], v[64:67], a[160:163]\n" \
"v_writelane_b32 v11, s101, 24\n" \
"s_add_u32 s100, 0x4800, s98\n" \
"v_mfma_f32_16x16x32_bf16 a[160:163], v[148:151], v[60:63], a[160:163]\n" \
"s_add_u32 s101, s96, s100\n" \
"v_writelane_b32 v11, s101, 27\n" \
"v_mfma_f32_16x16x32_bf16 a[164:167], v[156:159], v[60:63], a[164:167]\n" \
"s_add_u32 s98, 3, s97\n" \
"s_add_u32 s99, s67, s100\n" \
"v_mfma_f32_16x16x32_bf16 a[164:167], v[160:163], v[64:67], a[164:167]\n" \
"s_and_b32 s101, 3, s98\n" \
"v_writelane_b32 v11, s99, 30\n" \
"v_mfma_f32_16x16x32_bf16 a[168:171], v[168:171], v[64:67], a[168:171]\n" \
"s_add_u32 s100, s95, s101\n" \
"s_mul_i32 s99, 0x24000, s100\n" \
"v_mfma_f32_16x16x32_bf16 a[168:171], v[164:167], v[60:63], a[168:171]\n" \
"s_lshl_b32 s98, s100, 12\n" \
"s_add_u32 s97, s99, s64\n" \
"v_mfma_f32_16x16x32_bf16 a[172:175], v[172:175], v[60:63], a[172:175]\n" \
"s_add_u32 s95, 0x4800, s98\n" \
"v_writelane_b32 v11, s97, 25\n" \
"v_mfma_f32_16x16x32_bf16 a[172:175], v[176:179], v[64:67], a[172:175]\n" \
"s_add_u32 s101, s96, s95\n" \
"v_writelane_b32 v11, s101, 28\n" \
"v_mfma_f32_16x16x32_bf16 a[192:195], v[152:155], v[72:75], a[192:195]\n" \
"s_add_u32 s99, s67, s95\n" \
"s_lshl_b32 s64, s94, 11\n" \
"v_mfma_f32_16x16x32_bf16 a[192:195], v[148:151], v[68:71], a[192:195]\n" \
"v_writelane_b32 v11, s99, 31\n" \
"s_add_u32 s97, s96, s64\n" \
"s_waitcnt vmcnt(3)\n" \
"s_barrier\n" \
"v_mfma_f32_16x16x32_bf16 a[196:199], v[156:159], v[68:71], a[196:199]\n" \
"s_add_u32 m0, s40, 0x8000\n" \
"s_add_u32 s85, s72, 0x0\n" \
"v_mfma_f32_16x16x32_bf16 a[196:199], v[160:163], v[72:75], a[196:199]\n" \
"buffer_load_dwordx4 v20, s[80:83], s85 offen sc1 lds\n" \
"v_mfma_f32_16x16x32_bf16 a[200:203], v[168:171], v[72:75], a[200:203]\n" \
"s_add_u32 m0, s40, 0x9000\n" \
"s_add_u32 s84, s72, 0x1000\n" \
"v_mfma_f32_16x16x32_bf16 a[200:203], v[164:167], v[68:71], a[200:203]\n" \
"ds_read_b64_tr_b16 v[140:141], v39 offset:0\n" \
"ds_read_b64_tr_b16 v[142:143], v39 offset:960\n" \
"v_mfma_f32_16x16x32_bf16 a[204:207], v[172:175], v[68:71], a[204:207]\n" \
"buffer_load_dwordx4 v20, s[80:83], s84 offen sc1 lds\n" \
"v_mfma_f32_16x16x32_bf16 a[204:207], v[176:179], v[72:75], a[204:207]\n" \
"ds_read_b64_tr_b16 v[128:129], v37 offset:4096\n" \
"ds_read_b64_tr_b16 v[130:131], v37 offset:4608\n" \
"v_mfma_f32_16x16x32_bf16 a[224:227], v[152:155], v[80:83], a[224:227]\n" \
"s_add_u32 m0, s40, 0xb000\n" \
"s_add_u32 s85, s62, 0x0\n" \
"v_mfma_f32_16x16x32_bf16 a[224:227], v[148:151], v[76:79], a[224:227]\n" \
"buffer_load_dwordx4 v21, s[76:79], s85 offen lds\n" \
"v_mfma_f32_16x16x32_bf16 a[228:231], v[156:159], v[76:79], a[228:231]\n" \
"ds_read_b64_tr_b16 v[124:125], v37 offset:0\n" \
"ds_read_b64_tr_b16 v[126:127], v37 offset:512\n" \
"v_mfma_f32_16x16x32_bf16 a[228:231], v[160:163], v[80:83], a[228:231]\n" \
"s_add_u32 m0, %[mA], 0x8000\n" \
"s_add_u32 s84, s62, 0x0\n" \
"v_mfma_f32_16x16x32_bf16 a[232:235], v[168:171], v[80:83], a[232:235]\n" \
"buffer_load_dwordx4 v24, s[76:79], s84 offen lds\n" \
"v_mfma_f32_16x16x32_bf16 a[232:235], v[164:167], v[76:79], a[232:235]\n" \
"ds_read_b64_tr_b16 v[132:133], v38 offset:0\n" \
"ds_read_b64_tr_b16 v[134:135], v38 offset:512\n" \
"v_mfma_f32_16x16x32_bf16 a[236:239], v[172:175], v[76:79], a[236:239]\n" \
"ds_read_b64_tr_b16 v[136:137], v38 offset:4096\n" \
"ds_read_b64_tr_b16 v[138:139], v38 offset:4608\n" \
"v_mfma_f32_16x16x32_bf16 a[236:239], v[176:179], v[80:83], a[236:239]\n" \
"s_add_u32 m0, %[mA], 0x8400\n" \
"s_add_u32 s85, s62, 0x2400\n" \
"v_mfma_f32_16x16x32_bf16 a[144:147], v[152:155], v[88:91], a[144:147]\n" \
"buffer_load_dwordx4 v25, s[76:79], s85 offen lds\n" \
"v_mfma_f32_16x16x32_bf16 a[144:147], v[148:151], v[84:87], a[144:147]\n" \
"ds_read_b128 v[52:55], v28 offset:0\n" \
"ds_read_b128 v[56:59], v29 offset:0\n" \
"v_mfma_f32_16x16x32_bf16 a[148:151], v[156:159], v[84:87], a[148:151]\n" \
"s_add_u32 m0, %[mB], 0x8000\n" \
"s_add_u32 s84, s63, 0x0\n" \
"s_nop 0\n" \
"v_mfma_f32_16x16x32_bf16 a[148:151], v[160:163], v[88:91], a[148:151]\n" \
"buffer_load_dwordx4 v26, s[68:71], s84 offen lds\n" \
"v_mfma_f32_16x16x32_bf16 a[152:155], v[168:171], v[88:91], a[152:155]\n" \
"ds_read_b64_tr_b16 v[116:117], v36 offset:0\n" \
"ds_read_b64_tr_b16 v[118:119], v36 offset:512\n" \
"v_mfma_f32_16x16x32_bf16 a[152:155], v[164:167], v[84:87], a[152:155]\n" \
"ds_read_b64_tr_b16 v[120:121], v36 offset:4096\n" \
"ds_read_b64_tr_b16 v[122:123], v36 offset:4608\n" \
"v_mfma_f32_16x16x32_bf16 a[156:159], v[172:175], v[84:87], a[156:159]\n" \
"s_add_u32 m0, %[mA], 0x8800\n" \
"s_add_u32 s85, s62, 0x4800\n" \
"v_mfma_f32_16x16x32_bf16 a[156:159], v[176:179], v[88:91], a[156:159]\n" \
"buffer_load_dwordx4 v24, s[76:79], s85 offen lds\n" \
"v_mfma_f32_16x16x32_bf16 a[176:179], v[152:155], v[96:99], a[176:179]\n" \
"ds_read_b128 v[84:87], v22 offset:0\n" \
"ds_read_b128 v[88:91], v23 offset:0\n" \
"v_mfma_f32_16x16x32_bf16 a[176:179], v[148:151], v[92:95], a[176:179]\n" \
"s_add_u32 m0, %[mA], 0x8c00\n" \
"s_add_u32 s84, s62, 0x6c00\n" \
"v_mfma_f32_16x16x32_bf16 a[180:183], v[156:159], v[92:95], a[180:183]\n" \
"buffer_load_dwordx4 v25, s[76:79], s84 offen lds\n" \
"v_mfma_f32_16x16x32_bf16 a[180:183], v[160:163], v[96:99], a[180:183]\n" \
"ds_read_b128 v[60:63], v28 offset:2048\n" \
"ds_read_b128 v[64:67], v29 offset:2048\n" \
"v_mfma_f32_16x16x32_bf16 a[184:187], v[168:171], v[96:99], a[184:187]\n" \
"ds_read_b128 v[68:71], v28 offset:4096\n" \
"ds_read_b128 v[72:75], v29 offset:4096\n" \
"v_mfma_f32_16x16x32_bf16 a[184:187], v[164:167], v[92:95], a[184:187]\n" \
"s_add_u32 m0, %[mB], 0x8400\n" \
"s_add_u32 s85, s63, 0x8000\n" \
"v_mfma_f32_16x16x32_bf16 a[188:191], v[172:175], v[92:95], a[188:191]\n" \
"buffer_load_dwordx4 v26, s[68:71], s85 offen lds\n" \
"v_mfma_f32_16x16x32_bf16 a[188:191], v[176:179], v[96:99], a[188:191]\n" \
"ds_read_b128 v[76:79], v28 offset:6144\n" \
"ds_read_b128 v[80:83], v29 offset:6144\n" \
"v_mfma_f32_16x16x32_bf16 a[208:211], v[152:155], v[104:107], a[208:211]\n" \
"s_add_u32 m0, %[mA], 0x9000\n" \
"s_add_u32 s84, s62, 0x9000\n" \
"v_mfma_f32_16x16x32_bf16 a[208:211], v[148:151], v[100:103], a[208:211]\n" \
"buffer_load_dwordx4 v24, s[76:79], s84 offen lds\n" \
"v_mfma_f32_16x16x32_bf16 a[212:215], v[156:159], v[100:103], a[212:215]\n" \
"ds_read_b128 v[92:95], v48 offset:2048\n" \
"ds_read_b128 v[96:99], v49 offset:2048\n" \
"v_mfma_f32_16x16x32_bf16 a[212:215], v[160:163], v[104:107], a[212:215]\n" \
"v_writelane_b32 v11, s97, 32\n" \
"s_and_b32 s99, 1, s94\n" \
"v_mfma_f32_16x16x32_bf16 a[216:219], v[168:171], v[104:107], a[216:219]\n" \
"s_add_u32 m0, %[mA], 0x9400\n" \
"s_add_u32 s85, s62, 0xb400\n" \
"v_mfma_f32_16x16x32_bf16 a[216:219], v[164:167], v[100:103], a[216:219]\n" \
"buffer_load_dwordx4 v25, s[76:79], s85 offen lds\n" \
"v_mfma_f32_16x16x32_bf16 a[220:223], v[172:175], v[100:103], a[220:223]\n" \
"ds_read_b128 v[100:103], v48 offset:4096\n" \
"s_add_u32 s100, s67, s64\n" \
"v_mfma_f32_16x16x32_bf16 a[220:223], v[176:179], v[104:107], a[220:223]\n" \
"s_add_u32 m0, %[mB], 0x8800\n" \
"s_add_u32 s84, s63, 0x10000\n" \
"v_mfma_f32_16x16x32_bf16 a[240:243], v[152:155], v[112:115], a[240:243]\n" \
"buffer_load_dwordx4 v27, s[68:71], s84 offen lds\n" \
"v_mfma_f32_16x16x32_bf16 a[240:243], v[148:151], v[108:111], a[240:243]\n" \
"ds_read_b64_tr_b16 v[152:153], v32 offset:8192\n" \
"ds_read_b64_tr_b16 v[154:155], v32 offset:9216\n" \
"v_mfma_f32_16x16x32_bf16 a[244:247], v[156:159], v[108:111], a[244:247]\n" \
"ds_read_b64_tr_b16 v[148:149], v32 offset:0\n" \
"ds_read_b64_tr_b16 v[150:151], v32 offset:1024\n" \
"v_mfma_f32_16x16x32_bf16 a[244:247], v[160:163], v[112:115], a[244:247]\n" \
"s_add_u32 m0, %[mA], 0x9800\n" \
"s_add_u32 s85, s62, 0xd800\n" \
"v_mfma_f32_16x16x32_bf16 a[248:251], v[168:171], v[112:115], a[248:251]\n" \
"buffer_load_dwordx4 v24, s[76:79], s85 offen lds\n" \
"v_mfma_f32_16x16x32_bf16 a[248:251], v[164:167], v[108:111], a[248:251]\n" \
"ds_read_b64_tr_b16 v[156:157], v33 offset:0\n" \
"ds_read_b64_tr_b16 v[158:159], v33 offset:1024\n" \
"v_mfma_f32_16x16x32_bf16 a[252:255], v[172:175], v[108:111], a[252:255]\n" \
"ds_read_b64_tr_b16 v[160:161], v33 offset:8192\n" \
"ds_read_b64_tr_b16 v[162:163], v33 offset:9216\n" \
"v_mfma_f32_16x16x32_bf16 a[252:255], v[176:179], v[112:115], a[252:255]\n" \
"s_add_u32 m0, %[mA], 0x9c00\n" \
"s_waitcnt lgkmcnt(11)\n" \
"s_nop 0\n" \
"v_smfmac_f32_16x16x64_bf16 a[16:19], v[140:143], v[84:91], v50\n" \
"v_mfma_f32_16x16x32_bf16 a[48:51], v[128:131], v[88:91], a[48:51]\n" \
"s_add_u32 s85, s62, 0xfc00\n" \
"ds_read_b64_tr_b16 v[168:169], v34 offset:8192\n" \
"v_mfma_f32_16x16x32_bf16 a[48:51], v[124:127], v[84:87], a[48:51]\n" \
"buffer_load_dwordx4 v25, s[76:79], s85 offen lds\n" \
"v_mfma_f32_16x16x32_bf16 a[80:83], v[84:87], v[132:135], a[80:83]\n" \
"s_add_u32 m0, %[mB], 0x8c00\n" \
"s_add_u32 s84, s63, 0x18000\n" \
"v_mfma_f32_16x16x32_bf16 a[80:83], v[88:91], v[136:139], a[80:83]\n" \
"ds_read_b64_tr_b16 v[170:171], v34 offset:9216\n" \
"ds_read_b64_tr_b16 v[164:165], v34 offset:0\n" \
"v_smfmac_f32_16x16x64_bf16 a[112:115], v[140:143], v[52:59], v50\n" \
"v_mfma_f32_16x16x32_bf16 a[20:23], v[128:131], v[56:59], a[20:23]\n" \
"buffer_load_dwordx4 v27, s[68:71], s84 offen lds\n" \
"v_mfma_f32_16x16x32_bf16 a[20:23], v[124:127], v[52:55], a[20:23]\n" \
"s_add_u32 s89, s42, 0x1000\n" \
"ds_read_b64_tr_b16 v[166:167], v34 offset:1024\n" \
"v_mfma_f32_16x16x32_bf16 a[52:55], v[116:119], v[52:55], a[52:55]\n" \
"ds_read_b64_tr_b16 v[172:173], v35 offset:0\n" \
"ds_read_b64_tr_b16 v[174:175], v35 offset:1024\n" \
"v_mfma_f32_16x16x32_bf16 a[52:55], v[120:123], v[56:59], a[52:55]\n" \
"buffer_store_dwordx4 v[236:239], v20, s[80:83], s89 offen offset:1024 sc1\n" \
"v_mfma_f32_16x16x32_bf16 a[84:87], v[56:59], v[136:139], a[84:87]\n" \
"ds_read_b64_tr_b16 v[176:177], v35 offset:8192\n" \
"ds_read_b64_tr_b16 v[178:179], v35 offset:9216\n" \
"v_mfma_f32_16x16x32_bf16 a[84:87], v[52:55], v[132:135], a[84:87]\n" \
"ds_read_b128 v[84:87], v48 offset:0\n" \
"ds_read_b128 v[88:91], v49 offset:0\n" \
"v_smfmac_f32_16x16x64_bf16 a[116:119], v[140:143], v[60:67], v50\n" \
"v_mfma_f32_16x16x32_bf16 a[24:27], v[124:127], v[60:63], a[24:27]\n" \
"ds_read_b128 v[104:107], v49 offset:4096\n" \
"ds_read_b128 v[108:111], v48 offset:6144\n" \
"v_mfma_f32_16x16x32_bf16 a[24:27], v[128:131], v[64:67], a[24:27]\n" \
"s_add_u32 s88, s42, 0x2000\n" \
"ds_read_b128 v[112:115], v49 offset:6144\n" \
"v_mfma_f32_16x16x32_bf16 a[56:59], v[120:123], v[64:67], a[56:59]\n" \
"buffer_store_dwordx4 v[240:243], v20, s[80:83], s88 offen offset:1024 sc1\n" \
"v_mfma_f32_16x16x32_bf16 a[56:59], v[116:119], v[60:63], a[56:59]\n" \
"s_add_u32 s62, s62, 0x80\n" \
"v_writelane_b32 v11, s100, 33\n" \
"v_mfma_f32_16x16x32_bf16 a[88:91], v[60:63], v[132:135], a[88:91]\n" \
"s_cmp_lt_u32 s62, 0x480\n" \
"s_cselect_b32 s62, s62, 0\n" \
"v_mfma_f32_16x16x32_bf16 a[88:91], v[64:67], v[136:139], a[88:91]\n" \
"s_lshl_b32 s72, s62, 6\n" \
"s_add_u32 s72, s72, s37\n" \
"v_smfmac_f32_16x16x64_bf16 a[120:123], v[140:143], v[68:75], v50\n" \
"v_mfma_f32_16x16x32_bf16 a[28:31], v[128:131], v[72:75], a[28:31]\n" \
"s_lshl_b32 s63, s62, 12\n" \
"s_add_u32 s98, 0x3004000, s66\n" \
"v_mfma_f32_16x16x32_bf16 a[28:31], v[124:127], v[68:71], a[28:31]\n" \
"s_cmp_lg_u32 s99, 0\n" \
"s_cselect_b32 s97, s67, s96\n" \
"v_mfma_f32_16x16x32_bf16 a[60:63], v[116:119], v[68:71], a[60:63]\n" \
"s_add_u32 s89, s42, 0x3000\n" \
"s_lshr_b32 s95, s94, 1\n" \
"v_mfma_f32_16x16x32_bf16 a[60:63], v[120:123], v[72:75], a[60:63]\n" \
"buffer_store_dwordx4 v[244:247], v20, s[80:83], s89 offen offset:1024 sc1\n" \
"v_mfma_f32_16x16x32_bf16 a[92:95], v[72:75], v[136:139], a[92:95]\n" \
"s_add_u32 s64, 0x4000, s97\n" \
"v_writelane_b32 v11, s98, 34\n" \
"v_mfma_f32_16x16x32_bf16 a[92:95], v[68:71], v[132:135], a[92:95]\n" \
"s_add_u32 s101, 0x3004000, s65\n" \
"s_and_b32 s96, 2, %[w]\n" \
"v_smfmac_f32_16x16x64_bf16 a[124:127], v[140:143], v[76:83], v50\n" \
"v_mfma_f32_16x16x32_bf16 a[0:3], v[124:127], v[76:79], a[0:3]\n" \
"s_cmp_eq_u32 %[w], s95\n" \
"s_cselect_b32 s100, s64, 0x40000000\n" \
"v_mfma_f32_16x16x32_bf16 a[0:3], v[128:131], v[80:83], a[0:3]\n" \
"v_writelane_b32 v11, s101, 35\n" \
"s_lshl_b32 s97, s96, 1\n" \
"v_mfma_f32_16x16x32_bf16 a[4:7], v[120:123], v[80:83], a[4:7]\n" \
"s_and_b32 s98, 1, %[w]\n" \
"v_writelane_b32 v11, s100, 36\n" \
"v_mfma_f32_16x16x32_bf16 a[4:7], v[116:119], v[76:79], a[4:7]\n" \
"s_lshr_b32 s94, %[b], 5\n" \
"s_add_u32 s99, s97, s98\n" \
"v_mfma_f32_16x16x32_bf16 a[32:35], v[76:79], v[132:135], a[32:35]\n" \
"s_lshl_b32 s95, s94, 7\n" \
"s_lshl_b32 s100, s99, 4\n" \
"v_mfma_f32_16x16x32_bf16 a[32:35], v[80:83], v[136:139], a[32:35]\n" \
"s_add_u32 s101, s95, s100\n" \
"s_waitcnt lgkmcnt(0)\n" \
"v_mfma_f32_16x16x32_bf16 a[128:131], v[152:155], v[56:59], a[128:131]\n" \
"s_mul_i32 s64, 0x480, s101\n" \
"s_lshr_b32 s65, %[b], 3\n" \
"v_mfma_f32_16x16x32_bf16 a[128:131], v[148:151], v[52:55], a[128:131]\n" \
"s_sub_u32 s99, 0x780, s95\n" \
"s_and_b32 s66, 3, s65\n" \
"v_mfma_f32_16x16x32_bf16 a[132:135], v[156:159], v[52:55], a[132:135]\n" \
"s_add_u32 s101, s99, s100\n" \
"s_mul_i32 s67, 0x120, s66\n" \
"v_mfma_f32_16x16x32_bf16 a[132:135], v[160:163], v[56:59], a[132:135]\n" \
"s_and_b32 s96, 7, %[b]\n" \
"s_mul_i32 s65, 0x480, s101\n" \
"v_mfma_f32_16x16x32_bf16 a[136:139], v[168:171], v[56:59], a[136:139]\n" \
"s_add_u32 s94, s64, s67\n" \
"s_lshl_b32 s97, s96, 5\n" \
"v_mfma_f32_16x16x32_bf16 a[136:139], v[164:167], v[52:55], a[136:139]\n" \
"s_add_u32 s66, s65, s67\n" \
"s_and_b32 s95, 1, s96\n" \
"v_mfma_f32_16x16x32_bf16 a[140:143], v[172:175], v[52:55], a[140:143]\n" \
"s_add_u32 s98, s94, s97\n" \
"v_writelane_b32 v11, s98, 39\n" \
"v_mfma_f32_16x16x32_bf16 a[140:143], v[176:179], v[56:59], a[140:143]\n" \
"s_cmp_lg_u32 s95, 0\n" \
"s_cselect_b32 s99, s66, s94\n" \
"v_mfma_f32_16x16x32_bf16 a[160:163], v[152:155], v[64:67], a[160:163]\n" \
"s_lshr_b32 s98, s96, 1\n" \
"s_add_u32 s100, 0x100, s99\n" \
"v_mfma_f32_16x16x32_bf16 a[160:163], v[148:151], v[60:63], a[160:163]\n" \
"s_add_u32 s64, s66, s97\n" \
"s_mov_b32 s94, s67\n" \
"v_mfma_f32_16x16x32_bf16 a[164:167], v[156:159], v[60:63], a[164:167]\n" \
"s_cmp_eq_u32 %[w], s98\n" \
"s_cselect_b32 s101, s100, 0x40000000\n" \
"v_mfma_f32_16x16x32_bf16 a[164:167], v[160:163], v[64:67], a[164:167]\n" \
"v_writelane_b32 v11, s64, 40\n" \
"v_writelane_b32 v11, s101, 41\n" \
"v_mfma_f32_16x16x32_bf16 a[168:171], v[168:171], v[64:67], a[168:171]\n" \
"s_add_u32 s96, s16, s94\n" \
"s_addc_u32 s97, s17, 0\n" \
"v_mfma_f32_16x16x32_bf16 a[168:171], v[164:167], v[60:63], a[168:171]\n" \
"s_add_u32 s98, s18, s94\n" \
"s_addc_u32 s99, s19, 0\n" \
"v_mfma_f32_16x16x32_bf16 a[172:175], v[172:175], v[60:63], a[172:175]\n" \
"s_and_b32 s97, s97, 0xffff\n" \
"v_writelane_b32 v11, s96, 7\n" \
"v_mfma_f32_16x16x32_bf16 a[172:175], v[176:179], v[64:67], a[172:175]\n" \
"v_writelane_b32 v11, s97, 8\n" \
"s_and_b32 s99, s99, 0xffff\n" \
"v_mfma_f32_16x16x32_bf16 a[192:195], v[152:155], v[72:75], a[192:195]\n" \
"v_writelane_b32 v11, s98, 9\n" \
"v_writelane_b32 v11, s99, 10\n" \
"v_mfma_f32_16x16x32_bf16 a[192:195], v[148:151], v[68:71], a[192:195]\n" \
"s_cmp_lt_u32 %[w], 3\n" \
"s_cselect_b64 s[96:97], s[96:97], s[98:99]\n" \
"s_waitcnt vmcnt(3)\n" \
"s_barrier\n" \
"v_mfma_f32_16x16x32_bf16 a[196:199], v[156:159], v[68:71], a[196:199]\n" \
"s_add_u32 m0, s40, 0x0\n" \
"s_add_u32 s85, s72, 0x0\n" \
"v_mfma_f32_16x16x32_bf16 a[196:199], v[160:163], v[72:75], a[196:199]\n" \
"buffer_load_dwordx4 v20, s[80:83], s85 offen sc1 lds\n" \
"v_mfma_f32_16x16x32_bf16 a[200:203], v[168:171], v[72:75], a[200:203]\n" \
"s_add_u32 m0, s40, 0x1000\n" \
"s_add_u32 s84, s72, 0x1000\n" \
"v_mfma_f32_16x16x32_bf16 a[200:203], v[164:167], v[68:71], a[200:203]\n" \
"ds_read_b64_tr_b16 v[140:141], v39 offset:32768\n" \
"ds_read_b64_tr_b16 v[142:143], v39 offset:33728\n" \
"v_mfma_f32_16x16x32_bf16 a[204:207], v[172:175], v[68:71], a[204:207]\n" \
"buffer_load_dwordx4 v20, s[80:83], s84 offen sc1 lds\n" \
"v_mfma_f32_16x16x32_bf16 a[204:207], v[176:179], v[72:75], a[204:207]\n" \
"ds_read_b64_tr_b16 v[128:129], v37 offset:36864\n" \
"ds_read_b64_tr_b16 v[130:131], v37 offset:37376\n" \
"v_mfma_f32_16x16x32_bf16 a[224:227], v[152:155], v[80:83], a[224:227]\n" \
"s_add_u32 m0, s40, 0x3000\n" \
"s_add_u32 s85, s62, 0x0\n" \
"v_mfma_f32_16x16x32_bf16 a[224:227], v[148:151], v[76:79], a[224:227]\n" \
"buffer_load_dwordx4 v21, s[76:79], s85 offen lds\n" \
"v_mfma_f32_16x16x32_bf16 a[228:231], v[156:159], v[76:79], a[228:231]\n" \
"ds_read_b64_tr_b16 v[124:125], v37 offset:32768\n" \
"ds_read_b64_tr_b16 v[126:127], v37 offset:33280\n" \
"v_mfma_f32_16x16x32_bf16 a[228:231], v[160:163], v[80:83], a[228:231]\n" \
"s_add_u32 m0, %[mA], 0x0\n" \
"s_add_u32 s84, s62, 0x0\n" \
"s_nop 0\n" \
"v_mfma_f32_16x16x32_bf16 a[232:235], v[168:171], v[80:83], a[232:235]\n" \
"buffer_load_dwordx4 v24, s[76:79], s84 offen lds\n" \
"v_mfma_f32_16x16x32_bf16 a[232:235], v[164:167], v[76:79], a[232:235]\n" \
"ds_read_b64_tr_b16 v[132:133], v38 offset:32768\n" \
"ds_read_b64_tr_b16 v[134:135], v38 offset:33280\n" \
"v_mfma_f32_16x16x32_bf16 a[236:239], v[172:175], v[76:79], a[236:239]\n" \
"ds_read_b64_tr_b16 v[136:137], v38 offset:36864\n" \
"ds_read_b64_tr_b16 v[138:139], v38 offset:37376\n" \
"v_mfma_f32_16x16x32_bf16 a[236:239], v[176:179], v[80:83], a[236:239]\n" \
"s_add_u32 m0, %[mA], 0x400\n" \
"s_add_u32 s85, s62, 0x2400\n" \
"v_mfma_f32_16x16x32_bf16 a[144:147], v[152:155], v[88:91], a[144:147]\n" \
"buffer_load_dwordx4 v25, s[76:79], s85 offen lds\n" \
"v_mfma_f32_16x16x32_bf16 a[144:147], v[148:151], v[84:87], a[144:147]\n" \
"ds_read_b128 v[52:55], v28 offset:32768\n" \
"ds_read_b128 v[56:59], v29 offset:32768\n" \
"v_mfma_f32_16x16x32_bf16 a[148:151], v[156:159], v[84:87], a[148:151]\n" \
"s_add_u32 m0, %[mB], 0x0\n" \
"s_add_u32 s84, s63, 0x0\n" \
"v_mfma_f32_16x16x32_bf16 a[148:151], v[160:163], v[88:91], a[148:151]\n" \
"buffer_load_dwordx4 v26, s[68:71], s84 offen lds\n" \
"v_mfma_f32_16x16x32_bf16 a[152:155], v[168:171], v[88:91], a[152:155]\n" \
"ds_read_b64_tr_b16 v[116:117], v36 offset:32768\n" \
"ds_read_b64_tr_b16 v[118:119], v36 offset:33280\n" \
"v_mfma_f32_16x16x32_bf16 a[152:155], v[164:167], v[84:87], a[152:155]\n" \
"ds_read_b64_tr_b16 v[120:121], v36 offset:36864\n" \
"ds_read_b64_tr_b16 v[122:123], v36 offset:37376\n" \
"v_mfma_f32_16x16x32_bf16 a[156:159], v[172:175], v[84:87], a[156:159]\n" \
"s_add_u32 m0, %[mA], 0x800\n" \
"s_add_u32 s85, s62, 0x4800\n" \
"v_mfma_f32_16x16x32_bf16 a[156:159], v[176:179], v[88:91], a[156:159]\n" \
"buffer_load_dwordx4 v24, s[76:79], s85 offen lds\n" \
"v_mfma_f32_16x16x32_bf16 a[176:179], v[152:155], v[96:99], a[176:179]\n" \
"ds_read_b128 v[84:87], v22 offset:32768\n" \
"ds_read_b128 v[88:91], v23 offset:32768\n" \
"v_mfma_f32_16x16x32_bf16 a[176:179], v[148:151], v[92:95], a[176:179]\n" \
"s_add_u32 m0, %[mA], 0xc00\n" \
"s_add_u32 s84, s62, 0x6c00\n" \
"v_mfma_f32_16x16x32_bf16 a[180:183], v[156:159], v[92:95], a[180:183]\n" \
"buffer_load_dwordx4 v25, s[76:79], s84 offen lds\n" \
"v_mfma_f32_16x16x32_bf16 a[180:183], v[160:163], v[96:99], a[180:183]\n" \
"ds_read_b128 v[60:63], v28 offset:34816\n" \
"ds_read_b128 v[64:67], v29 offset:34816\n" \
"v_mfma_f32_16x16x32_bf16 a[184:187], v[168:171], v[96:99], a[184:187]\n" \
"ds_read_b128 v[68:71], v28 offset:36864\n" \
"ds_read_b128 v[72:75], v29 offset:36864\n" \
"L_pf7:\n" \
"s_cbranch_execz L_pf11\n" \
"s_nop 0\n" \
"v_mfma_f32_16x16x32_bf16 a[184:187], v[164:167], v[92:95], a[184:187]\n" \
"s_add_u32 m0, %[mB], 0x400\n" \
"s_add_u32 s85, s63, 0x8000\n" \
"v_mfma_f32_16x16x32_bf16 a[188:191], v[172:175], v[92:95], a[188:191]\n" \
"buffer_load_dwordx4 v26, s[68:71], s85 offen lds\n" \
"v_mfma_f32_16x16x32_bf16 a[188:191], v[176:179], v[96:99], a[188:191]\n" \
"ds_read_b128 v[76:79], v28 offset:38912\n" \
"ds_read_b128 v[80:83], v29 offset:38912\n" \
"v_mfma_f32_16x16x32_bf16 a[208:211], v[152:155], v[104:107], a[208:211]\n" \
"s_add_u32 m0, %[mA], 0x1000\n" \
"s_add_u32 s84, s62, 0x9000\n" \
"v_mfma_f32_16x16x32_bf16 a[208:211], v[148:151], v[100:103], a[208:211]\n" \
"buffer_load_dwordx4 v24, s[76:79], s84 offen lds\n" \
"v_mfma_f32_16x16x32_bf16 a[212:215], v[156:159], v[100:103], a[212:215]\n" \
"ds_read_b128 v[92:95], v48 offset:34816\n" \
"ds_read_b128 v[96:99], v49 offset:34816\n" \
"v_mfma_f32_16x16x32_bf16 a[212:215], v[160:163], v[104:107], a[212:215]\n" \
"v_writelane_b32 v11, s96, 13\n" \
"v_writelane_b32 v11, s97, 14\n" \
"v_mfma_f32_16x16x32_bf16 a[216:219], v[168:171], v[104:107], a[216:219]\n" \
"s_add_u32 m0, %[mA], 0x1400\n" \
"s_add_u32 s85, s62, 0xb400\n" \
"v_mfma_f32_16x16x32_bf16 a[216:219], v[164:167], v[100:103], a[216:219]\n" \
"buffer_load_dwordx4 v25, s[76:79], s85 offen lds\n" \
"v_mfma_f32_16x16x32_bf16 a[220:223], v[172:175], v[100:103], a[220:223]\n" \
"ds_read_b128 v[100:103], v48 offset:36864\n" \
"v_writelane_b32 v11, s20, 11\n" \
"v_mfma_f32_16x16x32_bf16 a[220:223], v[176:179], v[104:107], a[220:223]\n" \
"s_add_u32 m0, %[mB], 0x800\n" \
"s_add_u32 s84, s63, 0x10000\n" \
"v_mfma_f32_16x16x32_bf16 a[240:243], v[152:155], v[112:115], a[240:243]\n" \
"buffer_load_dwordx4 v27, s[68:71], s84 offen lds\n" \
"v_mfma_f32_16x16x32_bf16 a[240:243], v[148:151], v[108:111], a[240:243]\n" \
"ds_read_b64_tr_b16 v[152:153], v32 offset:40960\n" \
"ds_read_b64_tr_b16 v[154:155], v32 offset:41984\n" \
"v_mfma_f32_16x16x32_bf16 a[244:247], v[156:159], v[108:111], a[244:247]\n" \
"ds_read_b64_tr_b16 v[148:149], v32 offset:32768\n" \
"ds_read_b64_tr_b16 v[150:151], v32 offset:33792\n" \
"v_mfma_f32_16x16x32_bf16 a[244:247], v[160:163], v[112:115], a[244:247]\n" \
"s_add_u32 m0, %[mA], 0x1800\n" \
"s_add_u32 s85, s62, 0xd800\n" \
"v_mfma_f32_16x16x32_bf16 a[248:251], v[168:171], v[112:115], a[248:251]\n" \
"buffer_load_dwordx4 v24, s[76:79], s85 offen lds\n" \
"v_mfma_f32_16x16x32_bf16 a[248:251], v[164:167], v[108:111], a[248:251]\n" \
"ds_read_b64_tr_b16 v[156:157], v33 offset:32768\n" \
"ds_read_b64_tr_b16 v[158:159], v33 offset:33792\n" \
"v_mfma_f32_16x16x32_bf16 a[252:255], v[172:175], v[108:111], a[252:255]\n" \
"ds_read_b64_tr_b16 v[160:161], v33 offset:40960\n" \
"ds_read_b64_tr_b16 v[162:163], v33 offset:41984\n" \
"v_mfma_f32_16x16x32_bf16 a[252:255], v[176:179], v[112:115], a[252:255]\n" \
"s_add_u32 m0, %[mA], 0x1c00\n" \
"s_waitcnt lgkmcnt(11)\n" \
"s_nop 0\n" \
"v_smfmac_f32_16x16x64_bf16 a[16:19], v[140:143], v[84:91], v50\n" \
"v_mfma_f32_16x16x32_bf16 a[48:51], v[128:131], v[88:91], a[48:51]\n" \
"s_add_u32 s85, s62, 0xfc00\n" \
"ds_read_b64_tr_b16 v[168:169], v34 offset:40960\n" \
"v_mfma_f32_16x16x32_bf16 a[48:51], v[124:127], v[84:87], a[48:51]\n" \
"buffer_load_dwordx4 v25, s[76:79], s85 offen lds\n" \
"v_mfma_f32_16x16x32_bf16 a[80:83], v[84:87], v[132:135], a[80:83]\n" \
"s_add_u32 m0, %[mB], 0xc00\n" \
"s_add_u32 s84, s63, 0x18000\n" \
"v_mfma_f32_16x16x32_bf16 a[80:83], v[88:91], v[136:139], a[80:83]\n" \
"ds_read_b64_tr_b16 v[170:171], v34 offset:41984\n" \
"ds_read_b64_tr_b16 v[164:165], v34 offset:32768\n" \
"v_smfmac_f32_16x16x64_bf16 a[112:115], v[140:143], v[52:59], v50\n" \
"v_mfma_f32_16x16x32_bf16 a[20:23], v[128:131], v[56:59], a[20:23]\n" \
"buffer_load_dwordx4 v27, s[68:71], s84 offen lds\n" \
"v_mfma_f32_16x16x32_bf16 a[20:23], v[124:127], v[52:55], a[20:23]\n" \
"s_add_u32 s88, s43, 0x0\n" \
"s_nop 0\n" \
"ds_read_b64_tr_b16 v[166:167], v34 offset:33792\n" \
"v_mfma_f32_16x16x32_bf16 a[52:55], v[116:119], v[52:55], a[52:55]\n" \
"ds_read_b64_tr_b16 v[172:173], v35 offset:32768\n" \
"ds_read_b64_tr_b16 v[174:175], v35 offset:33792\n" \
"v_mfma_f32_16x16x32_bf16 a[52:55], v[120:123], v[56:59], a[52:55]\n" \
"buffer_store_dwordx4 v[248:251], v20, s[80:83], s88 offen offset:0 sc1\n" \
"v_mfma_f32_16x16x32_bf16 a[84:87], v[56:59], v[136:139], a[84:87]\n" \
"ds_read_b64_tr_b16 v[176:177], v35 offset:40960\n" \
"ds_read_b64_tr_b16 v[178:179], v35 offset:41984\n" \
"v_mfma_f32_16x16x32_bf16 a[84:87], v[52:55], v[132:135], a[84:87]\n" \
"ds_read_b128 v[84:87], v48 offset:32768\n" \
"ds_read_b128 v[88:91], v49 offset:32768\n" \
"v_smfmac_f32_16x16x64_bf16 a[116:119], v[140:143], v[60:67], v50\n" \
"v_mfma_f32_16x16x32_bf16 a[24:27], v[124:127], v[60:63], a[24:27]\n" \
"ds_read_b128 v[104:107], v49 offset:36864\n" \
"ds_read_b128 v[108:111], v48 offset:38912\n" \
"v_mfma_f32_16x16x32_bf16 a[24:27], v[128:131], v[64:67], a[24:27]\n" \
"s_add_u32 s89, s45, 0x1000\n" \
"ds_read_b128 v[112:115], v49 offset:38912\n" \
"v_mfma_f32_16x16x32_bf16 a[56:59], v[120:123], v[64:67], a[56:59]\n" \
"buffer_store_dwordx4 v[184:187], v20, s[80:83], s89 offen offset:2048 sc1\n" \
"v_mfma_f32_16x16x32_bf16 a[56:59], v[116:119], v[60:63], a[56:59]\n" \
"s_add_u32 s62, s62, 0x80\n" \
"s_and_b32 s95, s21, 0xffff\n" \
"v_mfma_f32_16x16x32_bf16 a[88:91], v[60:63], v[132:135], a[88:91]\n" \
"s_cmp_lt_u32 s62, 0x480\n" \
"s_cselect_b32 s62, s62, 0\n" \
"v_mfma_f32_16x16x32_bf16 a[88:91], v[64:67], v[136:139], a[88:91]\n" \
"s_lshl_b32 s72, s62, 6\n" \
"s_add_u32 s72, s72, s37\n" \
"v_smfmac_f32_16x16x64_bf16 a[120:123], v[140:143], v[68:75], v50\n" \
"v_mfma_f32_16x16x32_bf16 a[28:31], v[128:131], v[72:75], a[28:31]\n" \
"s_lshl_b32 s63, s62, 12\n" \
"v_writelane_b32 v11, s95, 12\n" \
"v_mfma_f32_16x16x32_bf16 a[28:31], v[124:127], v[68:71], a[28:31]\n" \
"s_and_b32 s95, 1, %[w]\n" \
"s_lshr_b32 s96, %[w], 1\n" \
"v_mfma_f32_16x16x32_bf16 a[60:63], v[116:119], v[68:71], a[60:63]\n" \
"s_add_u32 s88, s45, 0x1000\n" \
"s_lshl_b32 s94, %[w], 11\n" \
"v_mfma_f32_16x16x32_bf16 a[60:63], v[120:123], v[72:75], a[60:63]\n" \
"buffer_store_dwordx4 v[252:255], v20, s[80:83], s88 offen offset:3072 sc1\n" \
"v_mfma_f32_16x16x32_bf16 a[92:95], v[72:75], v[136:139], a[92:95]\n" \
"s_lshl_b32 s15, s95, 12\n" \
"s_lshl_b32 s16, s96, 14\n" \
"v_mfma_f32_16x16x32_bf16 a[92:95], v[68:71], v[132:135], a[92:95]\n" \
"s_add_u32 s12, 0xc000, s94\n" \
"s_add_u32 s97, s16, s15\n" \
"v_smfmac_f32_16x16x64_bf16 a[124:127], v[140:143], v[76:83], v50\n" \
"v_mfma_f32_16x16x32_bf16 a[0:3], v[124:127], v[76:79], a[0:3]\n" \
"s_lshr_b32 s100, %[b], 5\n" \
"s_and_b32 s65, 7, %[b]\n" \
"v_mfma_f32_16x16x32_bf16 a[0:3], v[128:131], v[80:83], a[0:3]\n" \
"s_lshr_b32 s67, %[b], 3\n" \
"s_xor_b32 s13, 0x8000, s97\n" \
"v_mfma_f32_16x16x32_bf16 a[4:7], v[120:123], v[80:83], a[4:7]\n" \
"s_add_u32 s20, 0x14000, s97\n" \
"s_mul_i32 s98, 0xe000, s96\n" \
"v_mfma_f32_16x16x32_bf16 a[4:7], v[116:119], v[76:79], a[4:7]\n" \
"s_lshl_b32 s18, s100, 1\n" \
"s_lshl_b32 s66, s65, 2\n" \
"v_mfma_f32_16x16x32_bf16 a[32:35], v[76:79], v[132:135], a[32:35]\n" \
"s_and_b32 s94, 3, s67\n" \
"s_add_u32 s99, s98, s15\n" \
"v_mfma_f32_16x16x32_bf16 a[32:35], v[80:83], v[136:139], a[32:35]\n" \
"s_add_u32 s101, s18, s96\n" \
"s_waitcnt lgkmcnt(0)\n" \
"v_mfma_f32_16x16x32_bf16 a[128:131], v[152:155], v[56:59], a[128:131]\n" \
"s_mov_b32 s64, 0x4000\n" \
"s_add_u32 s97, s66, s94\n" \
"v_mfma_f32_16x16x32_bf16 a[128:131], v[148:151], v[52:55], a[128:131]\n" \
"s_add_u32 s21, 0x10000, s99\n" \
"s_lshl_b32 s14, s101, 15\n" \
"v_mfma_f32_16x16x32_bf16 a[132:135], v[156:159], v[52:55], a[132:135]\n" \
"s_cmp_lg_u32 s100, 0\n" \
"s_cselect_b32 s17, s64, 0x8000\n" \
"v_mfma_f32_16x16x32_bf16 a[132:135], v[160:163], v[56:59], a[132:135]\n" \
"s_lshl_b32 s98, s97, 8\n" \
"s_and_b32 s67, 2, %[w]\n" \
"v_mfma_f32_16x16x32_bf16 a[136:139], v[168:171], v[56:59], a[136:139]\n" \
"s_add_u32 s99, 0x5be8020, s98\n" \
"s_lshl_b32 s101, s100, 2\n" \
"v_mfma_f32_16x16x32_bf16 a[136:139], v[164:167], v[52:55], a[136:139]\n" \
"s_lshl_b32 s66, s67, 1\n" \
"s_add_u32 s64, s99, s101\n" \
"v_mfma_f32_16x16x32_bf16 a[140:143], v[172:175], v[52:55], a[140:143]\n" \
"s_lshl_b32 s65, s100, 3\n" \
"s_add_u32 s94, s66, s95\n" \
"v_mfma_f32_16x16x32_bf16 a[140:143], v[176:179], v[56:59], a[140:143]\n" \
"s_cmp_eq_u32 %[w], 1\n" \
"s_cselect_b32 s22, s64, 0x40000000\n" \
"v_mfma_f32_16x16x32_bf16 a[160:163], v[152:155], v[64:67], a[160:163]\n" \
"s_add_u32 s98, s65, s94\n" \
"s_mul_i32 s99, 0x24800, s98\n" \
"v_mfma_f32_16x16x32_bf16 a[160:163], v[148:151], v[60:63], a[160:163]\n" \
"s_lshl_b32 s101, s97, 12\n" \
"s_sub_u32 s67, 0x78, s65\n" \
"v_mfma_f32_16x16x32_bf16 a[164:167], v[156:159], v[60:63], a[164:167]\n" \
"s_add_u32 s64, s99, s101\n" \
"s_add_u32 s66, s67, s94\n" \
"v_mfma_f32_16x16x32_bf16 a[164:167], v[160:163], v[64:67], a[164:167]\n" \
"s_add_u32 s60, 0x3004800, s64\n" \
"s_mul_i32 s95, 0x24800, s66\n" \
"v_mfma_f32_16x16x32_bf16 a[168:171], v[168:171], v[64:67], a[168:171]\n" \
"s_lshl_b32 s99, s96, 1\n" \
"s_add_u32 s98, s95, s101\n" \
"v_mfma_f32_16x16x32_bf16 a[168:171], v[164:167], v[60:63], a[168:171]\n" \
"s_add_u32 s64, s100, s99\n" \
"s_mov_b32 s65, 0x18000000\n" \
"v_mfma_f32_16x16x32_bf16 a[172:175], v[172:175], v[60:63], a[172:175]\n" \
"s_mul_i32 s94, 6, s96\n" \
"s_add_u32 s66, s96, s94\n" \
"v_mfma_f32_16x16x32_bf16 a[172:175], v[176:179], v[64:67], a[172:175]\n" \
"s_cmp_lt_u32 s64, 2\n" \
"s_cselect_b32 s67, s65, 0x30000000\n" \
"v_mfma_f32_16x16x32_bf16 a[192:195], v[152:155], v[72:75], a[192:195]\n" \
"s_mov_b32 s95, 0x80000\n" \
"s_mov_b32 s64, 0xaaaaaaaa\n" \
"v_mfma_f32_16x16x32_bf16 a[192:195], v[148:151], v[68:71], a[192:195]\n" \
"s_cmp_gt_u32 s66, s100\n" \
"s_cselect_b32 s101, s95, 0x40000\n" \
"s_waitcnt vmcnt(3)\n" \
"s_barrier\n" \
"s_mov_b64 exec, 1\n" \
"buffer_store_dword v45, off, s[80:83], s38 offset:0 sc1\n" \
"s_mov_b64 exec, -1\n" \
"v_mfma_f32_16x16x32_bf16 a[196:199], v[156:159], v[68:71], a[196:199]\n" \
"s_add_u32 m0, s40, 0x8000\n" \
"s_add_u32 s85, s72, 0x0\n" \
"s_nop 0\n" \
"v_mfma_f32_16x16x32_bf16 a[196:199], v[160:163], v[72:75], a[196:199]\n" \
"ds_read_b64_tr_b16 v[140:141], v39 offset:0\n" \
"ds_read_b64_tr_b16 v[142:143], v39 offset:960\n" \
"v_mfma_f32_16x16x32_bf16 a[200:203], v[168:171], v[72:75], a[200:203]\n" \
"buffer_load_dwordx4 v20, s[80:83], s85 offen sc1 lds\n" \
"v_mfma_f32_16x16x32_bf16 a[200:203], v[164:167], v[68:71], a[200:203]\n" \
"s_add_u32 m0, s40, 0x9000\n" \
"s_add_u32 s84, s72, 0x1000\n" \
"v_mfma_f32_16x16x32_bf16 a[204:207], v[172:175], v[68:71], a[204:207]\n" \
"ds_read_b64_tr_b16 v[128:129], v37 offset:4096\n" \
"ds_read_b64_tr_b16 v[130:131], v37 offset:4608\n" \
"v_mfma_f32_16x16x32_bf16 a[204:207], v[176:179], v[72:75], a[204:207]\n" \
"buffer_load_dwordx4 v20, s[80:83], s84 offen sc1 lds\n" \
"v_mfma_f32_16x16x32_bf16 a[224:227], v[152:155], v[80:83], a[224:227]\n" \
"s_add_u32 m0, s40, 0xb000\n" \
"s_add_u32 s85, s62, 0x0\n" \
"v_mfma_f32_16x16x32_bf16 a[224:227], v[148:151], v[76:79], a[224:227]\n" \
"ds_read_b64_tr_b16 v[124:125], v37 offset:0\n" \
"ds_read_b64_tr_b16 v[126:127], v37 offset:512\n" \
"v_mfma_f32_16x16x32_bf16 a[228:231], v[156:159], v[76:79], a[228:231]\n" \
"buffer_load_dwordx4 v21, s[76:79], s85 offen lds\n" \
"v_mfma_f32_16x16x32_bf16 a[228:231], v[160:163], v[80:83], a[228:231]\n" \
"s_add_u32 m0, %[mA], 0x8000\n" \
"s_add_u32 s84, s62, 0x0\n" \
"v_mfma_f32_16x16x32_bf16 a[232:235], v[168:171], v[80:83], a[232:235]\n" \
"ds_read_b64_tr_b16 v[132:133], v38 offset:0\n" \
"ds_read_b64_tr_b16 v[134:135], v38 offset:512\n" \
"v_mfma_f32_16x16x32_bf16 a[232:235], v[164:167], v[76:79], a[232:235]\n" \
"buffer_load_dwordx4 v24, s[76:79], s84 offen lds\n" \
"v_mfma_f32_16x16x32_bf16 a[236:239], v[172:175], v[76:79], a[236:239]\n" \
"ds_read_b64_tr_b16 v[136:137], v38 offset:4096\n" \
"ds_read_b64_tr_b16 v[138:139], v38 offset:4608\n" \
"v_mfma_f32_16x16x32_bf16 a[236:239], v[176:179], v[80:83], a[236:239]\n" \
"s_add_u32 m0, %[mA], 0x8400\n" \
"s_add_u32 s85, s62, 0x2400\n" \
"v_mfma_f32_16x16x32_bf16 a[144:147], v[152:155], v[88:91], a[144:147]\n" \
"buffer_load_dwordx4 v25, s[76:79], s85 offen lds\n" \
"v_mfma_f32_16x16x32_bf16 a[144:147], v[148:151], v[84:87], a[144:147]\n" \
"ds_read_b128 v[52:55], v28 offset:0\n" \
"ds_read_b128 v[56:59], v29 offset:0\n" \
"v_mfma_f32_16x16x32_bf16 a[148:151], v[156:159], v[84:87], a[148:151]\n" \
"s_add_u32 m0, %[mB], 0x8000\n" \
"s_add_u32 s84, s63, 0x0\n" \
"s_nop 0\n" \
"v_mfma_f32_16x16x32_bf16 a[148:151], v[160:163], v[88:91], a[148:151]\n" \
"buffer_load_dwordx4 v26, s[68:71], s84 offen lds\n" \
"v_mfma_f32_16x16x32_bf16 a[152:155], v[168:171], v[88:91], a[152:155]\n" \
"ds_read_b64_tr_b16 v[116:117], v36 offset:0\n" \
"ds_read_b64_tr_b16 v[118:119], v36 offset:512\n" \
"v_mfma_f32_16x16x32_bf16 a[152:155], v[164:167], v[84:87], a[152:155]\n" \
"ds_read_b64_tr_b16 v[120:121], v36 offset:4096\n" \
"ds_read_b64_tr_b16 v[122:123], v36 offset:4608\n" \
"v_mfma_f32_16x16x32_bf16 a[156:159], v[172:175], v[84:87], a[156:159]\n" \
"s_add_u32 m0, %[mA], 0x8800\n" \
"s_add_u32 s85, s62, 0x4800\n" \
"v_mfma_f32_16x16x32_bf16 a[156:159], v[176:179], v[88:91], a[156:159]\n" \
"buffer_load_dwordx4 v24, s[76:79], s85 offen lds\n" \
"v_mfma_f32_16x16x32_bf16 a[176:179], v[152:155], v[96:99], a[176:179]\n" \
"ds_read_b128 v[84:87], v22 offset:0\n" \
"ds_read_b128 v[88:91], v23 offset:0\n" \
"v_mfma_f32_16x16x32_bf16 a[176:179], v[148:151], v[92:95], a[176:179]\n" \
"s_add_u32 m0, %[mA], 0x8c00\n" \
"s_add_u32 s84, s62, 0x6c00\n" \
"v_mfma_f32_16x16x32_bf16 a[180:183], v[156:159], v[92:95], a[180:183]\n" \
"buffer_load_dwordx4 v25, s[76:79], s84 offen lds\n" \
"v_mfma_f32_16x16x32_bf16 a[180:183], v[160:163], v[96:99], a[180:183]\n" \
"ds_read_b128 v[60:63], v28 offset:2048\n" \
"ds_read_b128 v[64:67], v29 offset:2048\n" \
"v_mfma_f32_16x16x32_bf16 a[184:187], v[168:171], v[96:99], a[184:187]\n" \
"ds_read_b128 v[68:71], v28 offset:4096\n" \
"ds_read_b128 v[72:75], v29 offset:4096\n" \
"v_mfma_f32_16x16x32_bf16 a[184:187], v[164:167], v[92:95], a[184:187]\n" \
"s_add_u32 m0, %[mB], 0x8400\n" \
"s_add_u32 s85, s63, 0x8000\n" \
"v_mfma_f32_16x16x32_bf16 a[188:191], v[172:175], v[92:95], a[188:191]\n" \
"buffer_load_dwordx4 v26, s[68:71], s85 offen lds\n" \
"v_mfma_f32_16x16x32_bf16 a[188:191], v[176:179], v[96:99], a[188:191]\n" \
"ds_read_b128 v[76:79], v28 offset:6144\n" \
"ds_read_b128 v[80:83], v29 offset:6144\n" \
"v_mfma_f32_16x16x32_bf16 a[208:211], v[152:155], v[104:107], a[208:211]\n" \
"s_add_u32 m0, %[mA], 0x9000\n" \
"s_add_u32 s84, s62, 0x9000\n" \
"v_mfma_f32_16x16x32_bf16 a[208:211], v[148:151], v[100:103], a[208:211]\n" \
"buffer_load_dwordx4 v24, s[76:79], s84 offen lds\n" \
"v_mfma_f32_16x16x32_bf16 a[212:215], v[156:159], v[100:103], a[212:215]\n" \
"ds_read_b128 v[92:95], v48 offset:2048\n" \
"ds_read_b128 v[96:99], v49 offset:2048\n" \
"v_mfma_f32_16x16x32_bf16 a[212:215], v[160:163], v[104:107], a[212:215]\n" \
"s_mov_b32 s65, 0xaaaaaaaa\n" \
"s_add_u32 s23, s67, s101\n" \
"s_nop 0\n" \
"v_mfma_f32_16x16x32_bf16 a[216:219], v[168:171], v[104:107], a[216:219]\n" \
"s_add_u32 m0, %[mA], 0x9400\n" \
"s_add_u32 s85, s62, 0xb400\n" \
"v_mfma_f32_16x16x32_bf16 a[216:219], v[164:167], v[100:103], a[216:219]\n" \
"buffer_load_dwordx4 v25, s[76:79], s85 offen lds\n" \
"v_mfma_f32_16x16x32_bf16 a[220:223], v[172:175], v[100:103], a[220:223]\n" \
"ds_read_b128 v[100:103], v48 offset:4096\n" \
"s_mov_b32 s66, 0xfff0ffff\n" \
"v_mfma_f32_16x16x32_bf16 a[220:223], v[176:179], v[104:107], a[220:223]\n" \
"s_add_u32 m0, %[mB], 0x8800\n" \
"s_add_u32 s84, s63, 0x10000\n" \
"v_mfma_f32_16x16x32_bf16 a[240:243], v[152:155], v[112:115], a[240:243]\n" \
"buffer_load_dwordx4 v27, s[68:71], s84 offen lds\n" \
"v_mfma_f32_16x16x32_bf16 a[240:243], v[148:151], v[108:111], a[240:243]\n" \
"ds_read_b64_tr_b16 v[152:153], v32 offset:8192\n" \
"ds_read_b64_tr_b16 v[154:155], v32 offset:9216\n" \
"v_mfma_f32_16x16x32_bf16 a[244:247], v[156:159], v[108:111], a[244:247]\n" \
"ds_read_b64_tr_b16 v[148:149], v32 offset:0\n" \
"ds_read_b64_tr_b16 v[150:151], v32 offset:1024\n" \
"v_mfma_f32_16x16x32_bf16 a[244:247], v[160:163], v[112:115], a[244:247]\n" \
"s_add_u32 m0, %[mA], 0x9800\n" \
"s_add_u32 s85, s62, 0xd800\n" \
"v_mfma_f32_16x16x32_bf16 a[248:251], v[168:171], v[112:115], a[248:251]\n" \
"buffer_load_dwordx4 v24, s[76:79], s85 offen lds\n" \
"v_mfma_f32_16x16x32_bf16 a[248:251], v[164:167], v[108:111], a[248:251]\n" \
"ds_read_b64_tr_b16 v[156:157], v33 offset:0\n" \
"ds_read_b64_tr_b16 v[158:159], v33 offset:1024\n" \
"v_mfma_f32_16x16x32_bf16 a[252:255], v[172:175], v[108:111], a[252:255]\n" \
"ds_read_b64_tr_b16 v[160:161], v33 offset:8192\n" \
"ds_read_b64_tr_b16 v[162:163], v33 offset:9216\n" \
"v_mfma_f32_16x16x32_bf16 a[252:255], v[176:179], v[112:115], a[252:255]\n" \
"s_add_u32 m0, %[mA], 0x9c00\n" \
"s_waitcnt lgkmcnt(11)\n" \
"s_nop 0\n" \
"v_smfmac_f32_16x16x64_bf16 a[16:19], v[140:143], v[84:91], v50\n" \
"v_mfma_f32_16x16x32_bf16 a[48:51], v[128:131], v[88:91], a[48:51]\n" \
"s_add_u32 s85, s62, 0xfc00\n" \
"ds_read_b64_tr_b16 v[168:169], v34 offset:8192\n" \
"v_mfma_f32_16x16x32_bf16 a[48:51], v[124:127], v[84:87], a[48:51]\n" \
"buffer_load_dwordx4 v25, s[76:79], s85 offen lds\n" \
"v_mfma_f32_16x16x32_bf16 a[80:83], v[84:87], v[132:135], a[80:83]\n" \
"s_add_u32 m0, %[mB], 0x8c00\n" \
"s_add_u32 s84, s63, 0x18000\n" \
"v_mfma_f32_16x16x32_bf16 a[80:83], v[88:91], v[136:139], a[80:83]\n" \
"ds_read_b64_tr_b16 v[170:171], v34 offset:9216\n" \
"ds_read_b64_tr_b16 v[164:165], v34 offset:0\n" \
"v_smfmac_f32_16x16x64_bf16 a[112:115], v[140:143], v[52:59], v50\n" \
"v_mfma_f32_16x16x32_bf16 a[20:23], v[128:131], v[56:59], a[20:23]\n" \
"buffer_load_dwordx4 v27, s[68:71], s84 offen lds\n" \
"v_mfma_f32_16x16x32_bf16 a[20:23], v[124:127], v[52:55], a[20:23]\n" \
"s_add_u32 s89, s43, 0x1000\n" \
"ds_read_b64_tr_b16 v[166:167], v34 offset:1024\n" \
"v_mfma_f32_16x16x32_bf16 a[52:55], v[116:119], v[52:55], a[52:55]\n" \
"ds_read_b64_tr_b16 v[172:173], v35 offset:0\n" \
"ds_read_b64_tr_b16 v[174:175], v35 offset:1024\n" \
"v_mfma_f32_16x16x32_bf16 a[52:55], v[120:123], v[56:59], a[52:55]\n" \
"buffer_store_dwordx4 a[36:39], v20, s[80:83], s89 offen offset:0 sc1\n" \
"v_mfma_f32_16x16x32_bf16 a[84:87], v[56:59], v[136:139], a[84:87]\n" \
"ds_read_b64_tr_b16 v[176:177], v35 offset:8192\n" \
"ds_read_b64_tr_b16 v[178:179], v35 offset:9216\n" \
"v_mfma_f32_16x16x32_bf16 a[84:87], v[52:55], v[132:135], a[84:87]\n" \
"ds_read_b128 v[84:87], v48 offset:0\n" \
"ds_read_b128 v[88:91], v49 offset:0\n" \
"v_smfmac_f32_16x16x64_bf16 a[116:119], v[140:143], v[60:67], v50\n" \
"v_mfma_f32_16x16x32_bf16 a[24:27], v[124:127], v[60:63], a[24:27]\n" \
"ds_read_b128 v[104:107], v49 offset:4096\n" \
"ds_read_b128 v[108:111], v48 offset:6144\n" \
"v_mfma_f32_16x16x32_bf16 a[24:27], v[128:131], v[64:67], a[24:27]\n" \
"s_add_u32 s88, s43, 0x2000\n" \
"ds_read_b128 v[112:115], v49 offset:6144\n" \
"v_mfma_f32_16x16x32_bf16 a[56:59], v[120:123], v[64:67], a[56:59]\n" \
"buffer_store_dwordx4 a[64:67], v20, s[80:83], s88 offen offset:0 sc1\n" \
"v_mfma_f32_16x16x32_bf16 a[56:59], v[116:119], v[60:63], a[56:59]\n" \
"s_add_u32 s62, s62, 0x80\n" \
"s_mov_b32 s67, 0xf000ff00\n" \
"v_mfma_f32_16x16x32_bf16 a[88:91], v[60:63], v[132:135], a[88:91]\n" \
"s_cmp_lt_u32 s62, 0x480\n" \
"s_cselect_b32 s62, s62, 0\n" \
"v_mfma_f32_16x16x32_bf16 a[88:91], v[64:67], v[136:139], a[88:91]\n" \
"s_lshl_b32 s72, s62, 6\n" \
"s_add_u32 s72, s72, s37\n" \
"v_smfmac_f32_16x16x64_bf16 a[120:123], v[140:143], v[68:75], v50\n" \
"v_mfma_f32_16x16x32_bf16 a[28:31], v[128:131], v[72:75], a[28:31]\n" \
"s_lshl_b32 s63, s62, 12\n" \
"s_add_u32 s61, 0x3004800, s98\n" \
"v_mfma_f32_16x16x32_bf16 a[28:31], v[124:127], v[68:71], a[28:31]\n" \
"s_lshl_b32 s99, s97, 20\n" \
"s_lshl_b32 s98, s100, 16\n" \
"v_mfma_f32_16x16x32_bf16 a[60:63], v[116:119], v[68:71], a[60:63]\n" \
"s_add_u32 s89, s43, 0x3000\n" \
"s_add_u32 s8, 0x1000000, s99\n" \
"v_mfma_f32_16x16x32_bf16 a[60:63], v[120:123], v[72:75], a[60:63]\n" \
"buffer_store_dwordx4 a[68:71], v20, s[80:83], s89 offen offset:0 sc1\n" \
"v_mfma_f32_16x16x32_bf16 a[92:95], v[72:75], v[136:139], a[92:95]\n" \
"s_sub_u32 s19, 14, s18\n" \
"s_sub_u32 s10, 0x100000, s98\n" \
"v_mfma_f32_16x16x32_bf16 a[92:95], v[68:71], v[132:135], a[92:95]\n" \
"s_mov_b32 s11, 0x20000\n" \
"s_mov_b32 s58, 0x6000000\n" \
"v_smfmac_f32_16x16x64_bf16 a[124:127], v[140:143], v[76:83], v50\n" \
"v_mfma_f32_16x16x32_bf16 a[0:3], v[124:127], v[76:79], a[0:3]\n" \
"s_mov_b32 s59, 0x20000\n" \
"s_mov_b64 s[56:57], s[80:81]\n" \
"v_mfma_f32_16x16x32_bf16 a[0:3], v[128:131], v[80:83], a[0:3]\n" \
"s_add_u32 s8, s8, s80\n" \
"s_addc_u32 s9, s81, 0\n" \
"v_mfma_f32_16x16x32_bf16 a[4:7], v[120:123], v[80:83], a[4:7]\n" \
"v_mfma_f32_16x16x32_bf16 a[4:7], v[116:119], v[76:79], a[4:7]\n" \
"v_mfma_f32_16x16x32_bf16 a[32:35], v[76:79], v[132:135], a[32:35]\n" \
"v_mfma_f32_16x16x32_bf16 a[32:35], v[80:83], v[136:139], a[32:35]\n" \
"s_waitcnt lgkmcnt(0)\n" \
"s_nop 0\n" \
"v_mfma_f32_16x16x32_bf16 a[128:131], v[152:155], v[56:59], a[128:131]\n" \
"v_mfma_f32_16x16x32_bf16 a[128:131], v[148:151], v[52:55], a[128:131]\n" \
"v_mfma_f32_16x16x32_bf16 a[132:135], v[156:159], v[52:55], a[132:135]\n" \
"v_mfma_f32_16x16x32_bf16 a[132:135], v[160:163], v[56:59], a[132:135]\n" \
"v_mfma_f32_16x16x32_bf16 a[136:139], v[168:171], v[56:59], a[136:139]\n" \
"v_mfma_f32_16x16x32_bf16 a[136:139], v[164:167], v[52:55], a[136:139]\n" \
"v_mfma_f32_16x16x32_bf16 a[140:143], v[172:175], v[52:55], a[140:143]\n" \
"v_mfma_f32_16x16x32_bf16 a[140:143], v[176:179], v[56:59], a[140:143]\n" \
"v_mfma_f32_16x16x32_bf16 a[160:163], v[152:155], v[64:67], a[160:163]\n" \
"v_mfma_f32_16x16x32_bf16 a[160:163], v[148:151], v[60:63], a[160:163]\n" \
"v_mfma_f32_16x16x32_bf16 a[164:167], v[156:159], v[60:63], a[164:167]\n" \
"v_mfma_f32_16x16x32_bf16 a[164:167], v[160:163], v[64:67], a[164:167]\n" \
"v_mfma_f32_16x16x32_bf16 a[168:171], v[168:171], v[64:67], a[168:171]\n" \
"v_mfma_f32_16x16x32_bf16 a[168:171], v[164:167], v[60:63], a[168:171]\n" \
"v_mfma_f32_16x16x32_bf16 a[172:175], v[172:175], v[60:63], a[172:175]\n" \
"v_mfma_f32_16x16x32_bf16 a[172:175], v[176:179], v[64:67], a[172:175]\n" \
"v_mfma_f32_16x16x32_bf16 a[192:195], v[152:155], v[72:75], a[192:195]\n" \
"v_mfma_f32_16x16x32_bf16 a[192:195], v[148:151], v[68:71], a[192:195]\n" \
"s_waitcnt vmcnt(3)\n" \
"s_barrier\n" \
"v_mfma_f32_16x16x32_bf16 a[196:199], v[156:159], v[68:71], a[196:199]\n" \
"s_add_u32 m0, s40, 0x0\n" \
"s_add_u32 s85, s72, 0x0\n" \
"v_mfma_f32_16x16x32_bf16 a[196:199], v[160:163], v[72:75], a[196:199]\n" \
"buffer_load_dwordx4 v20, s[80:83], s85 offen sc1 lds\n" \
"v_mfma_f32_16x16x32_bf16 a[200:203], v[168:171], v[72:75], a[200:203]\n" \
"s_add_u32 m0, s40, 0x1000\n" \
"s_add_u32 s84, s72, 0x1000\n" \
"v_mfma_f32_16x16x32_bf16 a[200:203], v[164:167], v[68:71], a[200:203]\n" \
"ds_read_b64_tr_b16 v[140:141], v39 offset:32768\n" \
"ds_read_b64_tr_b16 v[142:143], v39 offset:33728\n" \
"v_mfma_f32_16x16x32_bf16 a[204:207], v[172:175], v[68:71], a[204:207]\n" \
"buffer_load_dwordx4 v20, s[80:83], s84 offen sc1 lds\n" \
"v_mfma_f32_16x16x32_bf16 a[204:207], v[176:179], v[72:75], a[204:207]\n" \
"ds_read_b64_tr_b16 v[128:129], v37 offset:36864\n" \
"ds_read_b64_tr_b16 v[130:131], v37 offset:37376\n" \
"v_mfma_f32_16x16x32_bf16 a[224:227], v[152:155], v[80:83], a[224:227]\n" \
"s_add_u32 m0, s40, 0x3000\n" \
"s_add_u32 s85, s62, 0x0\n" \
"v_mfma_f32_16x16x32_bf16 a[224:227], v[148:151], v[76:79], a[224:227]\n" \
"buffer_load_dwordx4 v21, s[76:79], s85 offen lds\n" \
"v_mfma_f32_16x16x32_bf16 a[228:231], v[156:159], v[76:79], a[228:231]\n" \
"ds_read_b64_tr_b16 v[124:125], v37 offset:32768\n" \
"ds_read_b64_tr_b16 v[126:127], v37 offset:33280\n" \
"v_mfma_f32_16x16x32_bf16 a[228:231], v[160:163], v[80:83], a[228:231]\n" \
"s_add_u32 m0, %[mA], 0x0\n" \
"s_add_u32 s84, s62, 0x0\n" \
"s_nop 0\n" \
"v_mfma_f32_16x16x32_bf16 a[232:235], v[168:171], v[80:83], a[232:235]\n" \
"buffer_load_dwordx4 v24, s[76:79], s84 offen lds\n" \
"v_mfma_f32_16x16x32_bf16 a[232:235], v[164:167], v[76:79], a[232:235]\n" \
"ds_read_b64_tr_b16 v[132:133], v38 offset:32768\n" \
"ds_read_b64_tr_b16 v[134:135], v38 offset:33280\n" \
"v_mfma_f32_16x16x32_bf16 a[236:239], v[172:175], v[76:79], a[236:239]\n" \
"ds_read_b64_tr_b16 v[136:137], v38 offset:36864\n" \
"ds_read_b64_tr_b16 v[138:139], v38 offset:37376\n" \
"v_mfma_f32_16x16x32_bf16 a[236:239], v[176:179], v[80:83], a[236:239]\n" \
"s_add_u32 m0, %[mA], 0x400\n" \
"s_add_u32 s85, s62, 0x2400\n" \
"v_mfma_f32_16x16x32_bf16 a[144:147], v[152:155], v[88:91], a[144:147]\n" \
"buffer_load_dwordx4 v25, s[76:79], s85 offen lds\n" \
"v_mfma_f32_16x16x32_bf16 a[144:147], v[148:151], v[84:87], a[144:147]\n" \
"ds_read_b128 v[52:55], v28 offset:32768\n" \
"ds_read_b128 v[56:59], v29 offset:32768\n" \
"v_mfma_f32_16x16x32_bf16 a[148:151], v[156:159], v[84:87], a[148:151]\n" \
"s_add_u32 m0, %[mB], 0x0\n" \
"s_add_u32 s84, s63, 0x0\n" \
"v_mfma_f32_16x16x32_bf16 a[148:151], v[160:163], v[88:91], a[148:151]\n" \
"buffer_load_dwordx4 v26, s[68:71], s84 offen lds\n" \
"v_mfma_f32_16x16x32_bf16 a[152:155], v[168:171], v[88:91], a[152:155]\n" \
"ds_read_b64_tr_b16 v[116:117], v36 offset:32768\n" \
"ds_read_b64_tr_b16 v[118:119], v36 offset:33280\n" \
"v_mfma_f32_16x16x32_bf16 a[152:155], v[164:167], v[84:87], a[152:155]\n" \
"ds_read_b64_tr_b16 v[120:121], v36 offset:36864\n" \
"ds_read_b64_tr_b16 v[122:123], v36 offset:37376\n" \
"v_mfma_f32_16x16x32_bf16 a[156:159], v[172:175], v[84:87], a[156:159]\n" \
"s_add_u32 m0, %[mA], 0x800\n" \
"s_add_u32 s85, s62, 0x4800\n" \
"v_mfma_f32_16x16x32_bf16 a[156:159], v[176:179], v[88:91], a[156:159]\n" \
"buffer_load_dwordx4 v24, s[76:79], s85 offen lds\n" \
"v_mfma_f32_16x16x32_bf16 a[176:179], v[152:155], v[96:99], a[176:179]\n" \
"ds_read_b128 v[84:87], v22 offset:32768\n" \
"ds_read_b128 v[88:91], v23 offset:32768\n" \
"v_mfma_f32_16x16x32_bf16 a[176:179], v[148:151], v[92:95], a[176:179]\n" \
"s_add_u32 m0, %[mA], 0xc00\n" \
"s_add_u32 s84, s62, 0x6c00\n" \
"v_mfma_f32_16x16x32_bf16 a[180:183], v[156:159], v[92:95], a[180:183]\n" \
"buffer_load_dwordx4 v25, s[76:79], s84 offen lds\n" \
"v_mfma_f32_16x16x32_bf16 a[180:183], v[160:163], v[96:99], a[180:183]\n" \
"ds_read_b128 v[60:63], v28 offset:34816\n" \
"ds_read_b128 v[64:67], v29 offset:34816\n" \
"v_mfma_f32_16x16x32_bf16 a[184:187], v[168:171], v[96:99], a[184:187]\n" \
"ds_read_b128 v[68:71], v28 offset:36864\n" \
"ds_read_b128 v[72:75], v29 offset:36864\n" \
"v_mfma_f32_16x16x32_bf16 a[184:187], v[164:167], v[92:95], a[184:187]\n" \
"s_add_u32 m0, %[mB], 0x400\n" \
"s_add_u32 s85, s63, 0x8000\n" \
"v_mfma_f32_16x16x32_bf16 a[188:191], v[172:175], v[92:95], a[188:191]\n" \
"buffer_load_dwordx4 v26, s[68:71], s85 offen lds\n" \
"v_mfma_f32_16x16x32_bf16 a[188:191], v[176:179], v[96:99], a[188:191]\n" \
"ds_read_b128 v[76:79], v28 offset:38912\n" \
"ds_read_b128 v[80:83], v29 offset:38912\n" \
"v_mfma_f32_16x16x32_bf16 a[208:211], v[152:155], v[104:107], a[208:211]\n" \
"s_add_u32 m0, %[mA], 0x1000\n" \
"s_add_u32 s84, s62, 0x9000\n" \
"v_mfma_f32_16x16x32_bf16 a[208:211], v[148:151], v[100:103], a[208:211]\n" \
"buffer_load_dwordx4 v24, s[76:79], s84 offen lds\n" \
"v_mfma_f32_16x16x32_bf16 a[212:215], v[156:159], v[100:103], a[212:215]\n" \
"ds_read_b128 v[92:95], v48 offset:34816\n" \
"ds_read_b128 v[96:99], v49 offset:34816\n" \
"v_mfma_f32_16x16x32_bf16 a[212:215], v[160:163], v[104:107], a[212:215]\n" \
"s_mov_b32 s72, 0x3eae1a93\n" \
"v_and_b32_e32 v26, 0x1ff, v20\n" \
"v_mfma_f32_16x16x32_bf16 a[216:219], v[168:171], v[104:107], a[216:219]\n" \
"s_add_u32 m0, %[mA], 0x1400\n" \
"s_add_u32 s85, s62, 0xb400\n" \
"v_mfma_f32_16x16x32_bf16 a[216:219], v[164:167], v[100:103], a[216:219]\n" \
"buffer_load_dwordx4 v25, s[76:79], s85 offen lds\n" \
"v_mfma_f32_16x16x32_bf16 a[220:223], v[172:175], v[100:103], a[220:223]\n" \
"ds_read_b128 v[100:103], v48 offset:36864\n" \
"v_add_u32_e64 v21, s48, v20\n" \
"v_mfma_f32_16x16x32_bf16 a[220:223], v[176:179], v[104:107], a[220:223]\n" \
"s_add_u32 m0, %[mB], 0x800\n" \
"s_add_u32 s84, s63, 0x10000\n" \
"v_mfma_f32_16x16x32_bf16 a[240:243], v[152:155], v[112:115], a[240:243]\n" \
"buffer_load_dwordx4 v27, s[68:71], s84 offen lds\n" \
"v_mfma_f32_16x16x32_bf16 a[240:243], v[148:151], v[108:111], a[240:243]\n" \
"ds_read_b64_tr_b16 v[152:153], v32 offset:40960\n" \
"ds_read_b64_tr_b16 v[154:155], v32 offset:41984\n" \
"v_mfma_f32_16x16x32_bf16 a[244:247], v[156:159], v[108:111], a[244:247]\n" \
"ds_read_b64_tr_b16 v[148:149], v32 offset:32768\n" \
"ds_read_b64_tr_b16 v[150:151], v32 offset:33792\n" \
"v_mfma_f32_16x16x32_bf16 a[244:247], v[160:163], v[112:115], a[244:247]\n" \
"s_add_u32 m0, %[mA], 0x1800\n" \
"s_add_u32 s85, s62, 0xd800\n" \
"v_mfma_f32_16x16x32_bf16 a[248:251], v[168:171], v[112:115], a[248:251]\n" \
"buffer_load_dwordx4 v24, s[76:79], s85 offen lds\n" \
"v_mfma_f32_16x16x32_bf16 a[248:251], v[164:167], v[108:111], a[248:251]\n" \
"ds_read_b64_tr_b16 v[156:157], v33 offset:32768\n" \
"ds_read_b64_tr_b16 v[158:159], v33 offset:33792\n" \
"v_mfma_f32_16x16x32_bf16 a[252:255], v[172:175], v[108:111], a[252:255]\n" \
"ds_read_b64_tr_b16 v[160:161], v33 offset:40960\n" \
"ds_read_b64_tr_b16 v[162:163], v33 offset:41984\n" \
"v_mfma_f32_16x16x32_bf16 a[252:255], v[176:179], v[112:115], a[252:255]\n" \
"s_add_u32 m0, %[mA], 0x1c00\n" \
"s_waitcnt lgkmcnt(11)\n" \
"v_smfmac_f32_16x16x64_bf16 a[16:19], v[140:143], v[84:91], v50\n" \
"v_mfma_f32_16x16x32_bf16 a[48:51], v[128:131], v[88:91], a[48:51]\n" \
"s_add_u32 s85, s62, 0xfc00\n" \
"L_pf8:\n" \
"s_cbranch_execz L_pf12\n" \
"s_nop 0\n" \
"ds_read_b64_tr_b16 v[168:169], v34 offset:40960\n" \
"v_mfma_f32_16x16x32_bf16 a[48:51], v[124:127], v[84:87], a[48:51]\n" \
"buffer_load_dwordx4 v25, s[76:79], s85 offen lds\n" \
"s_mov_b32 s76, s54\n" \
"v_mfma_f32_16x16x32_bf16 a[80:83], v[84:87], v[132:135], a[80:83]\n" \
"s_add_u32 m0, %[mB], 0xc00\n" \
"s_add_u32 s84, s63, 0x18000\n" \
"v_mfma_f32_16x16x32_bf16 a[80:83], v[88:91], v[136:139], a[80:83]\n" \
"ds_read_b64_tr_b16 v[170:171], v34 offset:41984\n" \
"ds_read_b64_tr_b16 v[164:165], v34 offset:32768\n" \
"v_smfmac_f32_16x16x64_bf16 a[112:115], v[140:143], v[52:59], v50\n" \
"v_mfma_f32_16x16x32_bf16 a[20:23], v[128:131], v[56:59], a[20:23]\n" \
"buffer_load_dwordx4 v27, s[68:71], s84 offen lds\n" \
"v_mfma_f32_16x16x32_bf16 a[20:23], v[124:127], v[52:55], a[20:23]\n" \
"s_add_u32 s88, s43, 0x0\n" \
"s_nop 0\n" \
"ds_read_b64_tr_b16 v[166:167], v34 offset:33792\n" \
"v_mfma_f32_16x16x32_bf16 a[52:55], v[116:119], v[52:55], a[52:55]\n" \
"ds_read_b64_tr_b16 v[172:173], v35 offset:32768\n" \
"ds_read_b64_tr_b16 v[174:175], v35 offset:33792\n" \
"v_mfma_f32_16x16x32_bf16 a[52:55], v[120:123], v[56:59], a[52:55]\n" \
"buffer_store_dwordx4 a[96:99], v20, s[80:83], s88 offen offset:1024 sc1\n" \
"v_mfma_f32_16x16x32_bf16 a[84:87], v[56:59], v[136:139], a[84:87]\n" \
"ds_read_b64_tr_b16 v[176:177], v35 offset:40960\n" \
"ds_read_b64_tr_b16 v[178:179], v35 offset:41984\n" \
"v_mfma_f32_16x16x32_bf16 a[84:87], v[52:55], v[132:135], a[84:87]\n" \
"ds_read_b128 v[84:87], v48 offset:32768\n" \
"ds_read_b128 v[88:91], v49 offset:32768\n" \
"v_smfmac_f32_16x16x64_bf16 a[116:119], v[140:143], v[60:67], v50\n" \
"v_mfma_f32_16x16x32_bf16 a[24:27], v[124:127], v[60:63], a[24:27]\n" \
"ds_read_b128 v[104:107], v49 offset:36864\n" \
"ds_read_b128 v[108:111], v48 offset:38912\n" \
"v_mfma_f32_16x16x32_bf16 a[24:27], v[128:131], v[64:67], a[24:27]\n" \
"s_add_u32 s89, s43, 0x1000\n" \
"ds_read_b128 v[112:115], v49 offset:38912\n" \
"v_mfma_f32_16x16x32_bf16 a[56:59], v[120:123], v[64:67], a[56:59]\n" \
"buffer_store_dwordx4 a[100:103], v20, s[80:83], s89 offen offset:1024 sc1\n" \
"s_add_u32 s89, s43, 0x3000\n" \
"v_mfma_f32_16x16x32_bf16 a[56:59], v[116:119], v[60:63], a[56:59]\n" \
"v_accvgpr_read_b32 v248, a16\n" \
"v_accvgpr_read_b32 v249, a17\n" \
"v_mfma_f32_16x16x32_bf16 a[88:91], v[60:63], v[132:135], a[88:91]\n" \
"v_accvgpr_read_b32 v250, a18\n" \
"v_accvgpr_read_b32 v251, a19\n" \
"v_mfma_f32_16x16x32_bf16 a[88:91], v[64:67], v[136:139], a[88:91]\n" \
"v_accvgpr_read_b32 v180, a48\n" \
"v_accvgpr_read_b32 v181, a49\n" \
"v_smfmac_f32_16x16x64_bf16 a[120:123], v[140:143], v[68:75], v50\n" \
"v_mfma_f32_16x16x32_bf16 a[28:31], v[128:131], v[72:75], a[28:31]\n" \
"v_accvgpr_read_b32 v182, a50\n" \
"v_accvgpr_read_b32 v183, a51\n" \
"v_mfma_f32_16x16x32_bf16 a[28:31], v[124:127], v[68:71], a[28:31]\n" \
"v_accvgpr_read_b32 v252, a80\n" \
"v_accvgpr_read_b32 v253, a81\n" \
"v_mfma_f32_16x16x32_bf16 a[60:63], v[116:119], v[68:71], a[60:63]\n" \
"s_add_u32 s88, s43, 0x2000\n" \
"v_accvgpr_read_b32 v254, a82\n" \
"v_mfma_f32_16x16x32_bf16 a[60:63], v[120:123], v[72:75], a[60:63]\n" \
"buffer_store_dwordx4 a[8:11], v20, s[80:83], s88 offen offset:1024 sc1\n" \
"s_add_u32 s88, s45, 0x0\n" \
"s_nop 0\n" \
"v_mfma_f32_16x16x32_bf16 a[92:95], v[72:75], v[136:139], a[92:95]\n" \
"v_accvgpr_read_b32 v255, a83\n" \
"v_accvgpr_read_b32 v196, a112\n" \
"v_mfma_f32_16x16x32_bf16 a[92:95], v[68:71], v[132:135], a[92:95]\n" \
"v_accvgpr_read_b32 v197, a113\n" \
"v_accvgpr_read_b32 v198, a114\n" \
"v_smfmac_f32_16x16x64_bf16 a[124:127], v[140:143], v[76:83], v50\n" \
"v_mfma_f32_16x16x32_bf16 a[0:3], v[124:127], v[76:79], a[0:3]\n" \
"v_accvgpr_read_b32 v199, a115\n" \
"v_accvgpr_read_b32 v188, a20\n" \
"v_mfma_f32_16x16x32_bf16 a[0:3], v[128:131], v[80:83], a[0:3]\n" \
"v_accvgpr_read_b32 v189, a21\n" \
"v_accvgpr_read_b32 v190, a22\n" \
"v_mfma_f32_16x16x32_bf16 a[4:7], v[120:123], v[80:83], a[4:7]\n" \
"v_accvgpr_read_b32 v191, a23\n" \
"v_accvgpr_read_b32 v184, a52\n" \
"v_mfma_f32_16x16x32_bf16 a[4:7], v[116:119], v[76:79], a[4:7]\n" \
"v_lshlrev_b32_e32 v45, 10, v20\n" \
"v_add_u32_e32 v45, s41, v45\n" \
"v_mfma_f32_16x16x32_bf16 a[32:35], v[76:79], v[132:135], a[32:35]\n" \
"s_mov_b32 m0, %[mS]\n" \
"v_accvgpr_read_b32 v185, a53\n" \
"v_mfma_f32_16x16x32_bf16 a[32:35], v[80:83], v[136:139], a[32:35]\n" \
"v_accvgpr_read_b32 v186, a54\n" \
"s_waitcnt lgkmcnt(0)\n" \
"v_mfma_f32_16x16x32_bf16 a[128:131], v[152:155], v[56:59], a[128:131]\n" \
"v_accvgpr_read_b32 v187, a55\n" \
"v_accvgpr_read_b32 v192, a84\n" \
"v_mfma_f32_16x16x32_bf16 a[128:131], v[148:151], v[52:55], a[128:131]\n" \
"v_accvgpr_read_b32 v193, a85\n" \
"v_accvgpr_read_b32 v194, a86\n" \
"v_mfma_f32_16x16x32_bf16 a[132:135], v[156:159], v[52:55], a[132:135]\n" \
"v_accvgpr_read_b32 v195, a87\n" \
"v_accvgpr_read_b32 v212, a116\n" \
"s_getpc_b64 s[86:87]\n" \
"L_ct15_%=:\n" \
"s_add_u32 s86, s86, L_end_%=-L_ct15_%=\n" \
"s_addc_u32 s87, s87, 0\n" \
"v_mfma_f32_16x16x32_bf16 a[132:135], v[160:163], v[56:59], a[132:135]\n" \
"v_accvgpr_read_b32 v213, a117\n" \
"v_accvgpr_read_b32 v214, a118\n" \
"v_mfma_f32_16x16x32_bf16 a[136:139], v[168:171], v[56:59], a[136:139]\n" \
"v_accvgpr_read_b32 v215, a119\n" \
"v_accvgpr_read_b32 v204, a24\n" \
"v_mfma_f32_16x16x32_bf16 a[136:139], v[164:167], v[52:55], a[136:139]\n" \
"v_accvgpr_read_b32 v205, a25\n" \
"v_accvgpr_read_b32 v206, a26\n" \
"v_mfma_f32_16x16x32_bf16 a[140:143], v[172:175], v[52:55], a[140:143]\n" \
"v_accvgpr_read_b32 v207, a27\n" \
"v_accvgpr_read_b32 v200, a56\n" \
"v_mfma_f32_16x16x32_bf16 a[140:143], v[176:179], v[56:59], a[140:143]\n" \
"v_accvgpr_read_b32 v201, a57\n" \
"v_accvgpr_read_b32 v202, a58\n" \
"v_mfma_f32_16x16x32_bf16 a[160:163], v[152:155], v[64:67], a[160:163]\n" \
"v_accvgpr_read_b32 v203, a59\n" \
"v_accvgpr_read_b32 v208, a88\n" \
"v_mfma_f32_16x16x32_bf16 a[160:163], v[148:151], v[60:63], a[160:163]\n" \
"v_accvgpr_read_b32 v209, a89\n" \
"v_accvgpr_read_b32 v210, a90\n" \
"v_mfma_f32_16x16x32_bf16 a[164:167], v[156:159], v[60:63], a[164:167]\n" \
"v_accvgpr_read_b32 v211, a91\n" \
"v_accvgpr_read_b32 v228, a120\n" \
"v_mfma_f32_16x16x32_bf16 a[164:167], v[160:163], v[64:67], a[164:167]\n" \
"v_accvgpr_read_b32 v229, a121\n" \
"v_accvgpr_read_b32 v230, a122\n" \
"v_mfma_f32_16x16x32_bf16 a[168:171], v[168:171], v[64:67], a[168:171]\n" \
"v_accvgpr_read_b32 v231, a123\n" \
"v_accvgpr_read_b32 v220, a28\n" \
"v_mfma_f32_16x16x32_bf16 a[168:171], v[164:167], v[60:63], a[168:171]\n" \
"v_accvgpr_read_b32 v221, a29\n" \
"v_accvgpr_read_b32 v222, a30\n" \
"v_mfma_f32_16x16x32_bf16 a[172:175], v[172:175], v[60:63], a[172:175]\n" \
"v_accvgpr_read_b32 v223, a31\n" \
"v_accvgpr_read_b32 v216, a60\n" \
"v_mfma_f32_16x16x32_bf16 a[172:175], v[176:179], v[64:67], a[172:175]\n" \
"v_accvgpr_read_b32 v217, a61\n" \
"v_accvgpr_read_b32 v218, a62\n" \
"v_mfma_f32_16x16x32_bf16 a[192:195], v[152:155], v[72:75], a[192:195]\n" \
"v_accvgpr_read_b32 v219, a63\n" \
"v_accvgpr_read_b32 v224, a92\n" \
"v_mfma_f32_16x16x32_bf16 a[192:195], v[148:151], v[68:71], a[192:195]\n" \
"v_accvgpr_read_b32 v225, a93\n" \
"v_accvgpr_read_b32 v226, a94\n" \
"s_waitcnt vmcnt(3)\n" \
"s_barrier\n" \
"s_mov_b64 exec, 3\n" \
"global_load_lds_dword v45, s[86:87]\n" \
"s_mov_b64 exec, -1\n" \
"s_sub_u32 m0, %[mB], 0x10000\n" \
"v_mfma_f32_16x16x32_bf16 a[196:199], v[156:159], v[68:71], a[196:199]\n" \
"ds_read_b64_tr_b16 v[140:141], v39 offset:0\n" \
"ds_read_b64_tr_b16 v[142:143], v39 offset:960\n" \
"v_mfma_f32_16x16x32_bf16 a[196:199], v[160:163], v[72:75], a[196:199]\n" \
"ds_read_b64_tr_b16 v[128:129], v37 offset:4096\n" \
"ds_read_b64_tr_b16 v[130:131], v37 offset:4608\n" \
"v_mfma_f32_16x16x32_bf16 a[200:203], v[168:171], v[72:75], a[200:203]\n" \
"ds_read_b64_tr_b16 v[124:125], v37 offset:0\n" \
"ds_read_b64_tr_b16 v[126:127], v37 offset:512\n" \
"v_mfma_f32_16x16x32_bf16 a[200:203], v[164:167], v[68:71], a[200:203]\n" \
"ds_read_b64_tr_b16 v[132:133], v38 offset:0\n" \
"ds_read_b64_tr_b16 v[134:135], v38 offset:512\n" \
"v_mfma_f32_16x16x32_bf16 a[204:207], v[172:175], v[68:71], a[204:207]\n" \
"ds_read_b64_tr_b16 v[136:137], v38 offset:4096\n" \
"ds_read_b64_tr_b16 v[138:139], v38 offset:4608\n" \
"v_mfma_f32_16x16x32_bf16 a[204:207], v[176:179], v[72:75], a[204:207]\n" \
"ds_read_b128 v[52:55], v28 offset:0\n" \
"ds_read_b128 v[56:59], v29 offset:0\n" \
"v_mfma_f32_16x16x32_bf16 a[224:227], v[152:155], v[80:83], a[224:227]\n" \
"ds_read_b64_tr_b16 v[116:117], v36 offset:0\n" \
"ds_read_b64_tr_b16 v[118:119], v36 offset:512\n" \
"v_mfma_f32_16x16x32_bf16 a[224:227], v[148:151], v[76:79], a[224:227]\n" \
"ds_read_b64_tr_b16 v[120:121], v36 offset:4096\n" \
"ds_read_b64_tr_b16 v[122:123], v36 offset:4608\n" \
"v_mfma_f32_16x16x32_bf16 a[228:231], v[156:159], v[76:79], a[228:231]\n" \
"ds_read_b128 v[60:63], v28 offset:2048\n" \
"ds_read_b128 v[64:67], v29 offset:2048\n" \
"v_mfma_f32_16x16x32_bf16 a[228:231], v[160:163], v[80:83], a[228:231]\n" \
"v_accvgpr_read_b32 v227, a95\n" \
"v_accvgpr_read_b32 v244, a124\n" \
"v_mfma_f32_16x16x32_bf16 a[232:235], v[168:171], v[80:83], a[232:235]\n" \
"ds_read_b128 v[68:71], v28 offset:4096\n" \
"ds_read_b128 v[72:75], v29 offset:4096\n" \
"v_mfma_f32_16x16x32_bf16 a[232:235], v[164:167], v[76:79], a[232:235]\n" \
"v_accvgpr_read_b32 v245, a125\n" \
"v_accvgpr_read_b32 v246, a126\n" \
"v_mfma_f32_16x16x32_bf16 a[236:239], v[172:175], v[76:79], a[236:239]\n" \
"ds_read_b128 v[76:79], v28 offset:6144\n" \
"v_accvgpr_read_b32 v247, a127\n" \
"v_mfma_f32_16x16x32_bf16 a[236:239], v[176:179], v[80:83], a[236:239]\n" \
"ds_read_b128 v[80:83], v29 offset:6144\n" \
"v_accvgpr_read_b32 v236, a0\n" \
"v_mfma_f32_16x16x32_bf16 a[144:147], v[152:155], v[88:91], a[144:147]\n" \
"v_accvgpr_read_b32 v237, a1\n" \
"v_accvgpr_read_b32 v238, a2\n" \
"v_mfma_f32_16x16x32_bf16 a[144:147], v[148:151], v[84:87], a[144:147]\n" \
"v_accvgpr_read_b32 v239, a3\n" \
"v_accvgpr_read_b32 v232, a4\n" \
"v_mfma_f32_16x16x32_bf16 a[148:151], v[156:159], v[84:87], a[148:151]\n" \
"v_accvgpr_read_b32 v233, a5\n" \
"v_accvgpr_read_b32 v234, a6\n" \
"v_mfma_f32_16x16x32_bf16 a[148:151], v[160:163], v[88:91], a[148:151]\n" \
"v_accvgpr_read_b32 v235, a7\n" \
"v_accvgpr_read_b32 v240, a32\n" \
"v_mfma_f32_16x16x32_bf16 a[152:155], v[168:171], v[88:91], a[152:155]\n" \
"v_accvgpr_read_b32 v241, a33\n" \
"v_accvgpr_read_b32 v242, a34\n" \
"v_mfma_f32_16x16x32_bf16 a[152:155], v[164:167], v[84:87], a[152:155]\n" \
"v_accvgpr_read_b32 v243, a35\n" \
"s_mov_b32 s78, -1\n" \
"v_mfma_f32_16x16x32_bf16 a[156:159], v[172:175], v[84:87], a[156:159]\n" \
"ds_read_b128 v[84:87], v22 offset:0\n" \
"s_mov_b32 s79, 0\n" \
"v_mfma_f32_16x16x32_bf16 a[156:159], v[176:179], v[88:91], a[156:159]\n" \
"ds_read_b128 v[88:91], v23 offset:0\n" \
"s_mov_b32 s68, 0xffc0fffc\n" \
"v_mfma_f32_16x16x32_bf16 a[176:179], v[152:155], v[96:99], a[176:179]\n" \
"s_mov_b32 s69, 0xc000fc00\n" \
"v_lshrrev_b32_e32 v24, 9, v20\n" \
"v_mfma_f32_16x16x32_bf16 a[176:179], v[148:151], v[92:95], a[176:179]\n" \
"v_lshrrev_b32_e32 v25, 8, v20\n" \
"v_mul_u32_u24_e32 v24, 0x24800, v24\n" \
"v_mfma_f32_16x16x32_bf16 a[180:183], v[156:159], v[92:95], a[180:183]\n" \
"v_mul_u32_u24_e32 v25, 0x24700, v25\n" \
"v_add_u32_e64 v24, v24, v26\n" \
"v_mfma_f32_16x16x32_bf16 a[180:183], v[160:163], v[96:99], a[180:183]\n" \
"v_add_u32_e32 v25, v25, v20\n" \
"s_add_u32 s85, s50, 0x0\n" \
"v_mfma_f32_16x16x32_bf16 a[184:187], v[168:171], v[96:99], a[184:187]\n" \
"s_add_u32 s87, s50, 0x49000\n" \
"s_mov_b32 s70, 0x3e0293ee\n" \
"v_mfma_f32_16x16x32_bf16 a[184:187], v[164:167], v[92:95], a[184:187]\n" \
"s_mov_b32 s71, 0x3e0293ee\n" \
"v_add_u32_e64 v27, s47, v20\n" \
"v_mfma_f32_16x16x32_bf16 a[188:191], v[172:175], v[92:95], a[188:191]\n" \
"ds_read_b128 v[92:95], v48 offset:2048\n" \
"v_lshrrev_b32_e32 v26, 2, v20\n" \
"v_mfma_f32_16x16x32_bf16 a[188:191], v[176:179], v[96:99], a[188:191]\n" \
"ds_read_b128 v[96:99], v49 offset:2048\n" \
"v_add_u32_e32 v26, s46, v26\n" \
"v_mfma_f32_16x16x32_bf16 a[208:211], v[152:155], v[104:107], a[208:211]\n" \
"v_mfma_f32_16x16x32_bf16 a[208:211], v[148:151], v[100:103], a[208:211]\n" \
"v_mfma_f32_16x16x32_bf16 a[212:215], v[156:159], v[100:103], a[212:215]\n" \
"v_mfma_f32_16x16x32_bf16 a[212:215], v[160:163], v[104:107], a[212:215]\n" \
"v_mfma_f32_16x16x32_bf16 a[216:219], v[168:171], v[104:107], a[216:219]\n" \
"v_mfma_f32_16x16x32_bf16 a[216:219], v[164:167], v[100:103], a[216:219]\n" \
"v_mfma_f32_16x16x32_bf16 a[220:223], v[172:175], v[100:103], a[220:223]\n" \
"ds_read_b128 v[100:103], v48 offset:4096\n" \
"v_mfma_f32_16x16x32_bf16 a[220:223], v[176:179], v[104:107], a[220:223]\n" \
"ds_read_b128 v[104:107], v49 offset:4096\n" \
"v_mfma_f32_16x16x32_bf16 a[240:243], v[152:155], v[112:115], a[240:243]\n" \
"ds_read_b64_tr_b16 v[152:153], v32 offset:8192\n" \
"ds_read_b64_tr_b16 v[154:155], v32 offset:9216\n" \
"v_mfma_f32_16x16x32_bf16 a[240:243], v[148:151], v[108:111], a[240:243]\n" \
"ds_read_b64_tr_b16 v[148:149], v32 offset:0\n" \
"ds_read_b64_tr_b16 v[150:151], v32 offset:1024\n" \
"v_mfma_f32_16x16x32_bf16 a[244:247], v[156:159], v[108:111], a[244:247]\n" \
"ds_read_b64_tr_b16 v[156:157], v33 offset:0\n" \
"ds_read_b64_tr_b16 v[158:159], v33 offset:1024\n" \
"v_mfma_f32_16x16x32_bf16 a[244:247], v[160:163], v[112:115], a[244:247]\n" \
"ds_read_b64_tr_b16 v[160:161], v33 offset:8192\n" \
"ds_read_b64_tr_b16 v[162:163], v33 offset:9216\n" \
"v_mfma_f32_16x16x32_bf16 a[248:251], v[168:171], v[112:115], a[248:251]\n" \
"ds_read_b64_tr_b16 v[168:169], v34 offset:8192\n" \
"ds_read_b64_tr_b16 v[170:171], v34 offset:9216\n" \
"v_mfma_f32_16x16x32_bf16 a[248:251], v[164:167], v[108:111], a[248:251]\n" \
"ds_read_b64_tr_b16 v[164:165], v34 offset:0\n" \
"ds_read_b64_tr_b16 v[166:167], v34 offset:1024\n" \
"v_mfma_f32_16x16x32_bf16 a[252:255], v[172:175], v[108:111], a[252:255]\n" \
"ds_read_b64_tr_b16 v[172:173], v35 offset:0\n" \
"ds_read_b64_tr_b16 v[174:175], v35 offset:1024\n" \
"v_mfma_f32_16x16x32_bf16 a[252:255], v[176:179], v[112:115], a[252:255]\n" \
"ds_read_b64_tr_b16 v[176:177], v35 offset:8192\n" \
"s_waitcnt lgkmcnt(15)\n" \
"s_nop 0\n" \
"v_smfmac_f32_16x16x64_bf16 v[248:251], v[140:143], v[84:91], v50\n" \
"v_mfma_f32_16x16x32_bf16 v[180:183], v[128:131], v[88:91], v[180:183]\n" \
"ds_read_b64_tr_b16 v[178:179], v35 offset:9216\n" \
"v_bfe_u32 v44, v20, 2, 5\n" \
"v_mfma_f32_16x16x32_bf16 v[180:183], v[124:127], v[84:87], v[180:183]\n" \
"buffer_load_dword v44, v44, s[80:83], s39 offen offset:0 sc1\n" \
"v_mfma_f32_16x16x32_bf16 v[252:255], v[84:87], v[132:135], v[252:255]\n" \
"ds_read_b128 v[84:87], v48 offset:0\n" \
"ds_read_b128 v[108:111], v48 offset:6144\n" \
"v_mfma_f32_16x16x32_bf16 v[252:255], v[88:91], v[136:139], v[252:255]\n" \
"ds_read_b128 v[88:91], v49 offset:0\n" \
"ds_read_b128 v[112:115], v49 offset:6144\n" \
"v_smfmac_f32_16x16x64_bf16 v[196:199], v[140:143], v[52:59], v50\n" \
"v_mfma_f32_16x16x32_bf16 v[188:191], v[128:131], v[56:59], v[188:191]\n" \
"v_cvt_pk_bf16_f32 v180, v180, v181\n" \
"v_cvt_pk_bf16_f32 v181, v182, v183\n" \
"v_mfma_f32_16x16x32_bf16 v[188:191], v[124:127], v[52:55], v[188:191]\n" \
"v_add_f32_e64 v249, v249, v251\n" \
"v_cvt_pk_bf16_f32 v182, v249, 0\n" \
"v_mfma_f32_16x16x32_bf16 v[184:187], v[116:119], v[52:55], v[184:187]\n" \
"v_mov_b32_e32 v183, 0x3f80\n" \
"v_add_f32_e64 v196, v196, v198\n" \
"v_mfma_f32_16x16x32_bf16 v[184:187], v[120:123], v[56:59], v[184:187]\n" \
"v_mfma_f32_16x16x16_bf16 v[248:251], v[182:183], v[46:47], 0\n" \
"v_add_f32_e64 v197, v197, v199\n" \
"v_cvt_pk_bf16_f32 v188, v188, v189\n" \
"v_mfma_f32_16x16x32_bf16 v[192:195], v[56:59], v[136:139], v[192:195]\n" \
"v_cvt_pk_bf16_f32 v189, v190, v191\n" \
"v_cvt_pk_bf16_f32 v190, v197, 0\n" \
"v_mfma_f32_16x16x32_bf16 v[192:195], v[52:55], v[132:135], v[192:195]\n" \
"v_mov_b32_e32 v191, 0x3f80\n" \
"v_cvt_pk_bf16_f32 v184, v184, v185\n" \
"v_smfmac_f32_16x16x64_bf16 v[212:215], v[140:143], v[60:67], v50\n" \
"v_mfma_f32_16x16x32_bf16 v[204:207], v[124:127], v[60:63], v[204:207]\n" \
"v_cvt_pk_bf16_f32 v185, v186, v187\n" \
"v_cndmask_b32_e64 v186, 0, v196, s[78:79]\n" \
"v_mfma_f32_16x16x32_bf16 v[204:207], v[128:131], v[64:67], v[204:207]\n" \
"v_mov_b32_e64 v187, 0\n" \
"v_cvt_pk_bf16_f32 v186, v186, 0\n" \
"v_mfma_f32_16x16x32_bf16 v[200:203], v[120:123], v[64:67], v[200:203]\n" \
"v_mfma_f32_16x16x32_bf16 v[200:203], v[116:119], v[60:63], v[200:203]\n" \
"v_add_f32_e32 v212, v212, v214\n" \
"v_add_f32_e32 v213, v213, v215\n" \
"v_mfma_f32_16x16x32_bf16 v[208:211], v[60:63], v[132:135], v[208:211]\n" \
"v_mfma_f32_16x16x32_bf16 v[208:211], v[64:67], v[136:139], v[208:211]\n" \
"v_cvt_pk_bf16_f32 v204, v204, v205\n" \
"v_cvt_pk_bf16_f32 v205, v206, v207\n" \
"v_smfmac_f32_16x16x64_bf16 v[228:231], v[140:143], v[68:75], v50\n" \
"v_mfma_f32_16x16x32_bf16 v[220:223], v[128:131], v[72:75], v[220:223]\n" \
"v_cvt_pk_bf16_f32 v200, v200, v201\n" \
"v_cvt_pk_bf16_f32 v201, v202, v203\n" \
"v_mfma_f32_16x16x32_bf16 v[220:223], v[124:127], v[68:71], v[220:223]\n" \
"v_cndmask_b32_e64 v202, 0, v212, s[78:79]\n" \
"v_cvt_pk_bf16_f32 v206, v213, 0\n" \
"v_mfma_f32_16x16x32_bf16 v[216:219], v[116:119], v[68:71], v[216:219]\n" \
"v_mov_b32_e32 v207, 0x3f80\n" \
"v_add_f32_e64 v228, v228, v230\n" \
"v_mfma_f32_16x16x32_bf16 v[216:219], v[120:123], v[72:75], v[216:219]\n" \
"v_add_f32_e32 v229, v229, v231\n" \
"v_mov_b32_e32 v203, 0\n" \
"v_mfma_f32_16x16x32_bf16 v[224:227], v[72:75], v[136:139], v[224:227]\n" \
"v_cvt_pk_bf16_f32 v220, v220, v221\n" \
"v_cvt_pk_bf16_f32 v221, v222, v223\n" \
"v_mfma_f32_16x16x32_bf16 v[224:227], v[68:71], v[132:135], v[224:227]\n" \
"v_cvt_pk_bf16_f32 v202, v202, 0\n" \
"v_cvt_pk_bf16_f32 v222, v229, 0\n" \
"v_smfmac_f32_16x16x64_bf16 v[244:247], v[140:143], v[76:83], v50\n" \
"v_mfma_f32_16x16x32_bf16 v[236:239], v[124:127], v[76:79], v[236:239]\n" \
"v_cvt_pk_bf16_f32 v216, v216, v217\n" \
"v_cvt_pk_bf16_f32 v217, v218, v219\n" \
"v_mfma_f32_16x16x32_bf16 v[236:239], v[128:131], v[80:83], v[236:239]\n" \
"v_cndmask_b32_e64 v218, 0, v228, s[78:79]\n" \
"v_cvt_pk_bf16_f32 v126, v192, v193\n" \
"v_mfma_f32_16x16x32_bf16 v[232:235], v[120:123], v[80:83], v[232:235]\n" \
"v_cvt_pk_bf16_f32 v127, v194, v195\n" \
"v_add_f32_e64 v244, v244, v246\n" \
"v_mfma_f32_16x16x32_bf16 v[232:235], v[116:119], v[76:79], v[232:235]\n" \
"v_add_f32_e64 v245, v245, v247\n" \
"v_cvt_pk_bf16_f32 v128, v208, v209\n" \
"v_mfma_f32_16x16x32_bf16 v[240:243], v[76:79], v[132:135], v[240:243]\n" \
"v_cvt_pk_bf16_f32 v236, v236, v237\n" \
"v_cvt_pk_bf16_f32 v237, v238, v239\n" \
"v_mfma_f32_16x16x32_bf16 v[240:243], v[80:83], v[136:139], v[240:243]\n" \
"v_cvt_pk_bf16_f32 v129, v210, v211\n" \
"v_mov_b32_e32 v223, 0x3f80\n" \
"s_waitcnt lgkmcnt(0)\n" \
"s_barrier\n" \
"v_mfma_f32_16x16x32_bf16 a[128:131], v[152:155], v[56:59], a[128:131]\n" \
"v_cvt_pk_bf16_f32 v232, v232, v233\n" \
"v_cvt_pk_bf16_f32 v233, v234, v235\n" \
"v_mfma_f32_16x16x32_bf16 a[128:131], v[148:151], v[52:55], a[128:131]\n" \
"v_cndmask_b32_e64 v234, 0, v244, s[78:79]\n" \
"v_mov_b32_e64 v219, 0\n" \
"v_mfma_f32_16x16x32_bf16 a[132:135], v[156:159], v[52:55], a[132:135]\n" \
"v_cvt_pk_bf16_f32 v218, v218, 0\n" \
"v_cvt_pk_bf16_f32 v130, v224, v225\n" \
"v_mfma_f32_16x16x32_bf16 a[132:135], v[160:163], v[56:59], a[132:135]\n" \
"v_cvt_pk_bf16_f32 v131, v226, v227\n" \
"v_cvt_pk_bf16_f32 v238, v245, 0\n" \
"v_mfma_f32_16x16x32_bf16 a[136:139], v[168:171], v[56:59], a[136:139]\n" \
"v_mov_b32_e32 v239, 0x3f80\n" \
"v_mov_b32_e64 v235, 0\n" \
"v_mfma_f32_16x16x32_bf16 a[136:139], v[164:167], v[52:55], a[136:139]\n" \
"v_cvt_pk_bf16_f32 v234, v234, 0\n" \
"v_cvt_pk_bf16_f32 v132, v240, v241\n" \
"v_mfma_f32_16x16x32_bf16 a[140:143], v[172:175], v[52:55], a[140:143]\n" \
"v_cvt_pk_bf16_f32 v133, v242, v243\n" \
"v_cvt_pk_bf16_f32 v134, v248, v249\n" \
"v_mfma_f32_16x16x32_bf16 a[140:143], v[176:179], v[56:59], a[140:143]\n" \
"v_mfma_f32_16x16x32_bf16 v[116:119], v[180:183], v[184:187], 0\n" \
"v_mfma_f32_16x16x32_bf16 v[192:195], v[188:191], v[184:187], 0\n" \
"v_mfma_f32_16x16x32_bf16 v[208:211], v[204:207], v[200:203], 0\n" \
"v_mfma_f32_16x16x32_bf16 v[120:123], v[188:191], v[200:203], 0\n" \
"s_waitcnt vmcnt(0)\n" \
"v_cmp_ne_u32_e32 vcc, %[tag], v44\n" \
"v_mfma_f32_16x16x32_bf16 a[160:163], v[152:155], v[64:67], a[160:163]\n" \
"v_mfma_f32_16x16x32_bf16 v[224:227], v[220:223], v[216:219], 0\n" \
"v_mfma_f32_16x16x32_bf16 v[36:39], v[204:207], v[216:219], 0\n" \
"v_mfma_f32_16x16x32_bf16 v[240:243], v[236:239], v[232:235], 0\n" \
"v_mfma_f32_16x16x32_bf16 v[48:51], v[220:223], v[232:235], 0\n" \
"s_cbranch_vccnz L_pa_%=\n" \
"L_ra_%=:\n" \
"v_cndmask_b32_e64 v116, v116, v117, s[64:65]\n" \
"buffer_load_dwordx4 v20, s[80:83], s73 offen offset:0 sc1 lds\n" \
"v_mfma_f32_16x16x32_bf16 a[160:163], v[148:151], v[60:63], a[160:163]\n" \
"v_cndmask_b32_e64 v118, v118, v119, s[64:65]\n" \
"v_cndmask_b32_e64 v192, v192, v193, s[64:65]\n" \
"v_mfma_f32_16x16x32_bf16 a[164:167], v[156:159], v[60:63], a[164:167]\n" \
"v_add_f32_e32 v116, s49, v116\n" \
"v_cndmask_b32_e64 v194, v194, v195, s[64:65]\n" \
"v_mfma_f32_16x16x32_bf16 a[164:167], v[160:163], v[64:67], a[164:167]\n" \
"v_add_f32_e64 v118, s49, v118\n" \
"v_cndmask_b32_e64 v208, v208, v209, s[64:65]\n" \
"v_mfma_f32_16x16x32_bf16 a[168:171], v[168:171], v[64:67], a[168:171]\n" \
"buffer_load_dwordx4 v20, s[80:83], s73 offen offset:1024 sc1 lds\n" \
"v_mfma_f32_16x16x32_bf16 a[168:171], v[164:167], v[60:63], a[168:171]\n" \
"v_cndmask_b32_e64 v120, v120, v121, s[64:65]\n" \
"v_cndmask_b32_e64 v210, v210, v211, s[64:65]\n" \
"v_mfma_f32_16x16x32_bf16 a[172:175], v[172:175], v[60:63], a[172:175]\n" \
"v_cndmask_b32_e64 v122, v122, v123, s[64:65]\n" \
"v_cndmask_b32_e64 v224, v224, v225, s[64:65]\n" \
"v_mfma_f32_16x16x32_bf16 a[172:175], v[176:179], v[64:67], a[172:175]\n" \
"v_cndmask_b32_e64 v36, v36, v37, s[64:65]\n" \
"v_cndmask_b32_e64 v226, v226, v227, s[64:65]\n" \
"v_mfma_f32_16x16x32_bf16 a[192:195], v[152:155], v[72:75], a[192:195]\n" \
"buffer_load_dwordx4 v20, s[80:83], s73 offen offset:2048 sc1 lds\n" \
"v_mfma_f32_16x16x32_bf16 a[192:195], v[148:151], v[68:71], a[192:195]\n" \
"v_cndmask_b32_e64 v38, v38, v39, s[64:65]\n" \
"v_cndmask_b32_e64 v240, v240, v241, s[64:65]\n" \
"v_mfma_f32_16x16x32_bf16 a[196:199], v[156:159], v[68:71], a[196:199]\n" \
"v_cndmask_b32_e64 v48, v48, v49, s[64:65]\n" \
"v_cndmask_b32_e64 v242, v242, v243, s[64:65]\n" \
"v_mfma_f32_16x16x32_bf16 a[196:199], v[160:163], v[72:75], a[196:199]\n" \
"v_cndmask_b32_e64 v50, v50, v51, s[64:65]\n" \
"v_cndmask_b32_e64 v192, v116, v192, s[66:67]\n" \
"v_mfma_f32_16x16x32_bf16 a[200:203], v[168:171], v[72:75], a[200:203]\n" \
"buffer_load_dwordx4 v20, s[80:83], s73 offen offset:3072 sc1 lds\n" \
"v_mfma_f32_16x16x32_bf16 a[200:203], v[164:167], v[68:71], a[200:203]\n" \
"s_sub_u32 m0, %[mB], s53\n" \
"s_nop 0\n" \
"v_cndmask_b32_e64 v194, v118, v194, s[68:69]\n" \
"v_mfma_f32_16x16x32_bf16 a[204:207], v[172:175], v[68:71], a[204:207]\n" \
"v_cndmask_b32_e64 v208, v120, v208, s[66:67]\n" \
"v_cndmask_b32_e64 v210, v122, v210, s[68:69]\n" \
"v_mfma_f32_16x16x32_bf16 a[204:207], v[176:179], v[72:75], a[204:207]\n" \
"v_cndmask_b32_e64 v224, v36, v224, s[66:67]\n" \
"v_cndmask_b32_e64 v226, v38, v226, s[68:69]\n" \
"v_mfma_f32_16x16x32_bf16 a[224:227], v[152:155], v[80:83], a[224:227]\n" \
"buffer_load_dwordx4 v20, s[80:83], s74 offen offset:0 sc1 lds\n" \
"v_mfma_f32_16x16x32_bf16 a[224:227], v[148:151], v[76:79], a[224:227]\n" \
"v_cndmask_b32_e64 v240, v48, v240, s[66:67]\n" \
"v_cndmask_b32_e64 v242, v50, v242, s[68:69]\n" \
"v_mfma_f32_16x16x32_bf16 a[228:231], v[156:159], v[76:79], a[228:231]\n" \
"v_max_f32_e32 v193, v192, v194\n" \
"v_max_f32_e32 v209, v208, v210\n" \
"v_mfma_f32_16x16x32_bf16 a[228:231], v[160:163], v[80:83], a[228:231]\n" \
"v_max_f32_e32 v225, v224, v226\n" \
"v_max_f32_e32 v241, v240, v242\n" \
"v_mfma_f32_16x16x32_bf16 a[232:235], v[168:171], v[80:83], a[232:235]\n" \
"buffer_load_dwordx4 v20, s[80:83], s74 offen offset:1024 sc1 lds\n" \
"v_mfma_f32_16x16x32_bf16 a[232:235], v[164:167], v[76:79], a[232:235]\n" \
"v_permlane32_swap_b32_e32 v193, v225\n" \
"v_permlane32_swap_b32_e32 v209, v241\n" \
"v_mfma_f32_16x16x32_bf16 a[236:239], v[172:175], v[76:79], a[236:239]\n" \
"v_max_f32_e32 v193, v193, v225\n" \
"v_max_f32_e32 v209, v209, v241\n" \
"v_permlane32_swap_b32_e32 v196, v228\n" \
"v_permlane32_swap_b32_e32 v212, v244\n" \
"buffer_load_dwordx4 v20, s[80:83], s74 offen offset:2048 sc1 lds\n" \
"v_permlane16_swap_b32_e32 v193, v209\n" \
"v_permlane16_swap_b32_e32 v228, v244\n" \
"v_mfma_f32_16x16x32_bf16 a[236:239], v[176:179], v[80:83], a[236:239]\n" \
"v_max_f32_e32 v193, v193, v209\n" \
"v_mul_f32_e32 v193, s72, v193\n" \
"v_mfma_f32_16x16x32_bf16 a[144:147], v[152:155], v[88:91], a[144:147]\n" \
"v_mul_f32_e32 v45, 0xbfb8aa3b, v228\n" \
"v_mov_b32_e64 v209, v193\n" \
"v_mfma_f32_16x16x16_bf16 v[196:199], v[190:191], v[46:47], 0\n" \
"buffer_load_dwordx4 v20, s[80:83], s74 offen offset:3072 sc1 lds\n" \
"v_mfma_f32_16x16x32_bf16 a[144:147], v[148:151], v[84:87], a[144:147]\n" \
"s_sub_u32 m0, %[mB], 0x4000\n" \
"v_cvt_pk_bf16_f32 v135, v250, v251\n" \
"v_mfma_f32_16x16x16_bf16 v[212:215], v[206:207], v[46:47], 0\n" \
"v_mfma_f32_16x16x16_bf16 v[228:231], v[222:223], v[46:47], 0\n" \
"v_permlane16_swap_b32_e32 v193, v209\n" \
"v_mfma_f32_16x16x16_bf16 v[244:247], v[238:239], v[46:47], 0\n" \
"buffer_load_dwordx4 v20, s[80:83], s75 offen offset:0 sc1 lds\n" \
"v_mfma_f32_16x16x32_bf16 a[148:151], v[156:159], v[84:87], a[148:151]\n" \
"v_mov_b32_e32 v225, v193\n" \
"v_mov_b32_e64 v241, v209\n" \
"v_mfma_f32_16x16x32_bf16 a[148:151], v[160:163], v[88:91], a[148:151]\n" \
"v_permlane32_swap_b32_e32 v193, v225\n" \
"v_permlane32_swap_b32_e32 v209, v241\n" \
"v_mfma_f32_16x16x32_bf16 a[152:155], v[168:171], v[88:91], a[152:155]\n" \
"v_fma_f32 v192, v192, s72, -v193\n" \
"v_fma_f32 v194, v194, s72, -v193\n" \
"buffer_load_dwordx4 v20, s[80:83], s75 offen offset:1024 sc1 lds\n" \
"v_mfma_f32_16x16x32_bf16 a[152:155], v[164:167], v[84:87], a[152:155]\n" \
"v_fma_f32 v208, v208, s72, -v209\n" \
"v_fma_f32 v210, v210, s72, -v209\n" \
"v_exp_f32_e32 v192, v192\n" \
"v_exp_f32_e32 v194, v194\n" \
"v_exp_f32_e32 v208, v208\n" \
"v_exp_f32_e32 v210, v210\n" \
"v_mfma_f32_16x16x32_bf16 a[156:159], v[172:175], v[84:87], a[156:159]\n" \
"buffer_load_dwordx4 v20, s[80:83], s75 offen offset:2048 sc1 lds\n" \
"v_mfma_f32_16x16x32_bf16 a[156:159], v[176:179], v[88:91], a[156:159]\n" \
"v_fma_f32 v224, v224, s72, -v225\n" \
"v_fma_f32 v226, v226, s72, -v225\n" \
"v_exp_f32_e32 v224, v224\n" \
"v_exp_f32_e32 v226, v226\n" \
"v_mfma_f32_16x16x32_bf16 a[176:179], v[152:155], v[96:99], a[176:179]\n" \
"v_fma_f32 v240, v240, s72, -v241\n" \
"v_fma_f32 v242, v242, s72, -v241\n" \
"v_mfma_f32_16x16x32_bf16 a[176:179], v[148:151], v[92:95], a[176:179]\n" \
"buffer_load_dwordx4 v20, s[80:83], s75 offen offset:3072 sc1 lds\n" \
"v_mfma_f32_16x16x32_bf16 a[180:183], v[156:159], v[92:95], a[180:183]\n" \
"s_mov_b32 m0, s55\n" \
"v_cvt_pk_bf16_f32 v192, v192, v194\n" \
"v_mfma_f32_16x16x32_bf16 a[180:183], v[160:163], v[96:99], a[180:183]\n" \
"v_cvt_pk_bf16_f32 v208, v208, v210\n" \
"v_cvt_pk_bf16_f32 v136, v196, v197\n" \
"v_exp_f32_e32 v240, v240\n" \
"v_exp_f32_e64 v242, v242\n" \
"v_mfma_f32_16x16x32_bf16 a[184:187], v[168:171], v[96:99], a[184:187]\n" \
"buffer_load_dwordx4 v20, s[80:83], s76 offen offset:0 sc1 lds\n" \
"v_mfma_f32_16x16x32_bf16 a[184:187], v[164:167], v[92:95], a[184:187]\n" \
"v_cvt_pk_bf16_f32 v137, v198, v199\n" \
"v_cvt_pk_bf16_f32 v138, v212, v213\n" \
"v_mfma_f32_16x16x32_bf16 a[188:191], v[172:175], v[92:95], a[188:191]\n" \
"v_cvt_pk_bf16_f32 v139, v214, v215\n" \
"v_perm_b32 v184, v192, v192, v40\n" \
"v_mfma_f32_16x16x32_bf16 a[188:191], v[176:179], v[96:99], a[188:191]\n" \
"v_perm_b32 v185, v192, v192, v41\n" \
"v_perm_b32 v186, v192, v192, v42\n" \
"v_perm_b32 v187, v192, v192, v43\n" \
"buffer_load_dwordx4 v20, s[80:83], s76 offen offset:1024 sc1 lds\n" \
"v_perm_b32 v200, v208, v208, v40\n" \
"v_perm_b32 v201, v208, v208, v41\n" \
"v_perm_b32 v202, v208, v208, v42\n" \
"v_perm_b32 v203, v208, v208, v43\n" \
"v_cvt_pk_bf16_f32 v224, v224, v226\n" \
"v_cvt_pk_bf16_f32 v140, v228, v229\n" \
"v_cvt_pk_bf16_f32 v141, v230, v231\n" \
"v_mfma_f32_16x16x32_bf16 v[116:119], v[134:137], v[184:187], 0\n" \
"v_mfma_f32_16x16x32_bf16 v[120:123], v[136:139], v[200:203], 0\n" \
"v_perm_b32 v216, v224, v224, v40\n" \
"v_perm_b32 v217, v224, v224, v41\n" \
"v_perm_b32 v218, v224, v224, v42\n" \
"L_pf9:\n" \
"s_cbranch_execz L_pf13\n" \
"s_nop 0\n" \
"v_perm_b32 v219, v224, v224, v43\n" \
"v_cvt_pk_bf16_f32 v240, v240, v242\n" \
"v_cvt_pk_bf16_f32 v142, v244, v245\n" \
"v_cvt_pk_bf16_f32 v143, v246, v247\n" \
"v_mfma_f32_16x16x32_bf16 v[36:39], v[138:141], v[216:219], 0\n" \
"v_perm_b32 v232, v240, v240, v40\n" \
"v_perm_b32 v233, v240, v240, v41\n" \
"v_perm_b32 v234, v240, v240, v42\n" \
"v_perm_b32 v235, v240, v240, v43\n" \
"v_exp_f32_e64 v45, v45\n" \
"v_mov_b32_dpp v118, v122 quad_perm:[0,1,2,3] row_mask:0x2 bank_mask:0xf\n" \
"v_mfma_f32_16x16x32_bf16 v[48:51], v[140:143], v[232:235], 0\n" \
"v_cvt_pk_bf16_f32 v124, v252, v253\n" \
"v_cvt_pk_bf16_f32 v125, v254, v255\n" \
"v_mov_b32_dpp v118, v38 quad_perm:[0,1,2,3] row_mask:0x4 bank_mask:0xf\n" \
"v_add_f32_e64 v45, 1.0, v45\n" \
"v_mfma_f32_16x16x32_bf16 v[192:195], v[124:127], v[184:187], 0\n" \
"v_mfma_f32_16x16x32_bf16 v[224:227], v[128:131], v[216:219], 0\n" \
"v_rcp_f32_e64 v45, v45\n" \
"v_mov_b32_dpp v116, v120 quad_perm:[0,1,2,3] row_mask:0x2 bank_mask:0xf\n" \
"v_mov_b32_dpp v118, v50 quad_perm:[0,1,2,3] row_mask:0x8 bank_mask:0xf\n" \
"v_mov_b32_dpp v117, v121 quad_perm:[0,1,2,3] row_mask:0x2 bank_mask:0xf\n" \
"v_rcp_f32_e64 v118, v118\n" \
"v_mfma_f32_16x16x32_bf16 v[208:211], v[126:129], v[200:203], 0\n" \
"v_mfma_f32_16x16x32_bf16 v[240:243], v[130:133], v[232:235], 0\n" \
"v_mov_b32_dpp v116, v36 quad_perm:[0,1,2,3] row_mask:0x4 bank_mask:0xf\n" \
"v_mov_b32_dpp v117, v37 quad_perm:[0,1,2,3] row_mask:0x4 bank_mask:0xf\n" \
"v_mul_f32_e64 v118, v45, v118\n" \
"v_mov_b32_dpp v116, v48 quad_perm:[0,1,2,3] row_mask:0x8 bank_mask:0xf\n" \
"v_mov_b32_dpp v117, v49 quad_perm:[0,1,2,3] row_mask:0x8 bank_mask:0xf\n" \
"v_mul_f32_e32 v116, v118, v116\n" \
"v_mul_f32_e32 v117, v118, v117\n" \
"buffer_store_dwordx4 a[12:15], v20, s[80:83], s89 offen offset:1024 sc1\n" \
"s_add_u32 s89, s45, 0x0\n" \
"v_mov_b32_e32 v122, v118\n" \
"v_cvt_pk_bf16_f32 v116, v116, v117\n" \
"v_accvgpr_read_b32 v52, a128\n" \
"v_permlane16_swap_b32_e32 v118, v122\n" \
"v_mov_b32_e32 v38, v118\n" \
"v_mov_b32_e64 v50, v122\n" \
"v_accvgpr_read_b32 v53, a129\n" \
"v_permlane32_swap_b32_e32 v118, v38\n" \
"v_permlane32_swap_b32_e32 v122, v50\n" \
"v_mul_f32_e32 v192, v118, v192\n" \
"v_mul_f32_e32 v193, v118, v193\n" \
"v_mul_f32_e32 v224, v38, v224\n" \
"v_mul_f32_e32 v225, v38, v225\n" \
"v_mul_f32_e32 v194, v118, v194\n" \
"v_mul_f32_e32 v195, v118, v195\n" \
"v_cvt_pk_bf16_f32 v192, v192, v193\n" \
"v_mul_f32_e32 v226, v38, v226\n" \
"v_mul_f32_e32 v227, v38, v227\n" \
"v_cvt_pk_bf16_f32 v224, v224, v225\n" \
"v_cvt_pk_bf16_f32 v193, v194, v195\n" \
"v_mul_f32_e32 v208, v122, v208\n" \
"v_mul_f32_e32 v209, v122, v209\n" \
"v_mul_f32_e32 v210, v122, v210\n" \
"v_mul_f32_e32 v211, v122, v211\n" \
"v_cvt_pk_bf16_f32 v225, v226, v227\n" \
"v_mul_f32_e32 v240, v50, v240\n" \
"v_mul_f32_e32 v241, v50, v241\n" \
"v_mul_f32_e32 v242, v50, v242\n" \
"v_mul_f32_e32 v243, v50, v243\n" \
"v_cvt_pk_bf16_f32 v194, v208, v209\n" \
"v_cvt_pk_bf16_f32 v195, v210, v211\n" \
"v_cvt_pk_bf16_f32 v226, v240, v241\n" \
"v_cvt_pk_bf16_f32 v227, v242, v243\n" \
"v_permlane32_swap_b32_e32 v192, v194\n" \
"v_permlane32_swap_b32_e32 v193, v195\n" \
"v_permlane32_swap_b32_e32 v224, v226\n" \
"v_permlane32_swap_b32_e32 v225, v227\n" \
"v_accvgpr_read_b32 v54, a130\n" \
"v_accvgpr_read_b32 v55, a131\n" \
"v_pk_mul_f32 v[52:53], v[52:53], s[70:71]\n" \
"v_accvgpr_read_b32 v56, a132\n" \
"v_accvgpr_read_b32 v57, a133\n" \
"v_accvgpr_read_b32 v58, a134\n" \
"v_accvgpr_read_b32 v59, a135\n" \
"v_pk_mul_f32 v[54:55], v[54:55], s[70:71]\n" \
"v_cvt_pk_bf16_f32 v52, v52, v53\n" \
"v_pk_mul_f32 v[56:57], v[56:57], s[70:71]\n" \
"v_pk_mul_f32 v[58:59], v[58:59], s[70:71]\n" \
"v_cvt_pk_bf16_f32 v53, v54, v55\n" \
"v_cvt_pk_bf16_f32 v54, v56, v57\n" \
"v_cvt_pk_bf16_f32 v55, v58, v59\n" \
"ds_write_b128 v27, v[52:55] offset:0\n" \
"buffer_store_dwordx4 a[40:43], v20, s[80:83], s88 offen offset:0 sc1\n" \
"s_add_u32 s88, s45, 0x0\n" \
"s_nop 0\n" \
"buffer_store_dword v116, v25, s[80:83], s51 offen sc1\n" \
"buffer_store_dwordx4 v[192:195], v24, s[80:83], s85 offen sc1\n" \
"buffer_store_dwordx4 v[224:227], v24, s[80:83], s87 offen sc1\n" \
"v_accvgpr_read_b32 v76, a224\n" \
"v_accvgpr_read_b32 v77, a225\n" \
"v_accvgpr_read_b32 v78, a226\n" \
"v_accvgpr_read_b32 v79, a227\n" \
"v_pk_mul_f32 v[76:77], v[76:77], s[70:71]\n" \
"v_accvgpr_read_b32 v52, a136\n" \
"v_accvgpr_read_b32 v53, a137\n" \
"v_accvgpr_read_b32 v80, a228\n" \
"v_accvgpr_read_b32 v81, a229\n" \
"v_accvgpr_read_b32 v82, a230\n" \
"v_accvgpr_read_b32 v83, a231\n" \
"v_pk_mul_f32 v[78:79], v[78:79], s[70:71]\n" \
"v_cvt_pk_bf16_f32 v76, v76, v77\n" \
"v_accvgpr_read_b32 v54, a138\n" \
"v_accvgpr_read_b32 v55, a139\n" \
"v_pk_mul_f32 v[52:53], v[52:53], s[70:71]\n" \
"v_pk_mul_f32 v[80:81], v[80:81], s[70:71]\n" \
"v_pk_mul_f32 v[82:83], v[82:83], s[70:71]\n" \
"v_cvt_pk_bf16_f32 v77, v78, v79\n" \
"v_accvgpr_read_b32 v56, a140\n" \
"v_accvgpr_read_b32 v57, a141\n" \
"v_accvgpr_read_b32 v58, a142\n" \
"v_accvgpr_read_b32 v59, a143\n" \
"v_pk_mul_f32 v[54:55], v[54:55], s[70:71]\n" \
"v_cvt_pk_bf16_f32 v52, v52, v53\n" \
"v_cvt_pk_bf16_f32 v78, v80, v81\n" \
"v_cvt_pk_bf16_f32 v79, v82, v83\n" \
"ds_write_b128 v27, v[76:79] offset:12288\n" \
"v_pk_mul_f32 v[56:57], v[56:57], s[70:71]\n" \
"v_pk_mul_f32 v[58:59], v[58:59], s[70:71]\n" \
"v_cvt_pk_bf16_f32 v53, v54, v55\n" \
"v_cvt_pk_bf16_f32 v54, v56, v57\n" \
"v_cvt_pk_bf16_f32 v55, v58, v59\n" \
"ds_write_b128 v27, v[52:55] offset:1024\n" \
"v_accvgpr_read_b32 v76, a208\n" \
"v_accvgpr_read_b32 v77, a209\n" \
"v_accvgpr_read_b32 v78, a210\n" \
"v_accvgpr_read_b32 v79, a211\n" \
"v_accvgpr_read_b32 v52, a168\n" \
"v_accvgpr_read_b32 v53, a169\n" \
"v_mfma_f32_16x16x32_bf16 v[76:79], v[152:155], v[104:107], v[76:79]\n" \
"v_accvgpr_read_b32 v54, a170\n" \
"v_accvgpr_read_b32 v55, a171\n" \
"v_mfma_f32_16x16x32_bf16 v[76:79], v[148:151], v[100:103], v[76:79]\n" \
"v_accvgpr_read_b32 v56, a172\n" \
"v_accvgpr_read_b32 v57, a173\n" \
"v_accvgpr_read_b32 v58, a174\n" \
"v_accvgpr_read_b32 v59, a175\n" \
"v_pk_mul_f32 v[52:53], v[52:53], s[70:71]\n" \
"v_pk_mul_f32 v[54:55], v[54:55], s[70:71]\n" \
"v_cvt_pk_bf16_f32 v52, v52, v53\n" \
"v_pk_mul_f32 v[56:57], v[56:57], s[70:71]\n" \
"v_pk_mul_f32 v[58:59], v[58:59], s[70:71]\n" \
"buffer_store_dwordx4 a[44:47], v20, s[80:83], s89 offen offset:1024 sc1\n" \
"s_add_u32 s89, s45, 0x0\n" \
"s_nop 0\n" \
"v_cvt_pk_bf16_f32 v53, v54, v55\n" \
"v_cvt_pk_bf16_f32 v54, v56, v57\n" \
"v_cvt_pk_bf16_f32 v55, v58, v59\n" \
"ds_write_b128 v27, v[52:55] offset:5120\n" \
"v_accvgpr_read_b32 v80, a212\n" \
"v_accvgpr_read_b32 v81, a213\n" \
"v_accvgpr_read_b32 v82, a214\n" \
"v_accvgpr_read_b32 v83, a215\n" \
"v_accvgpr_read_b32 v52, a232\n" \
"v_accvgpr_read_b32 v53, a233\n" \
"v_mfma_f32_16x16x32_bf16 v[80:83], v[156:159], v[100:103], v[80:83]\n" \
"v_accvgpr_read_b32 v54, a234\n" \
"v_accvgpr_read_b32 v55, a235\n" \
"v_mfma_f32_16x16x32_bf16 v[80:83], v[160:163], v[104:107], v[80:83]\n" \
"v_accvgpr_read_b32 v56, a236\n" \
"v_accvgpr_read_b32 v57, a237\n" \
"v_accvgpr_read_b32 v58, a238\n" \
"v_accvgpr_read_b32 v59, a239\n" \
"v_pk_mul_f32 v[52:53], v[52:53], s[70:71]\n" \
"v_pk_mul_f32 v[54:55], v[54:55], s[70:71]\n" \
"v_cvt_pk_bf16_f32 v52, v52, v53\n" \
"v_pk_mul_f32 v[56:57], v[56:57], s[70:71]\n" \
"v_pk_mul_f32 v[58:59], v[58:59], s[70:71]\n" \
"v_cvt_pk_bf16_f32 v53, v54, v55\n" \
"v_cvt_pk_bf16_f32 v54, v56, v57\n" \
"v_cvt_pk_bf16_f32 v55, v58, v59\n" \
"ds_write_b128 v27, v[52:55] offset:13312\n" \
"v_accvgpr_read_b32 v52, a216\n" \
"v_accvgpr_read_b32 v53, a217\n" \
"v_accvgpr_read_b32 v54, a218\n" \
"v_accvgpr_read_b32 v55, a219\n" \
"v_accvgpr_read_b32 v60, a160\n" \
"v_accvgpr_read_b32 v61, a161\n" \
"v_mfma_f32_16x16x32_bf16 v[52:55], v[168:171], v[104:107], v[52:55]\n" \
"v_accvgpr_read_b32 v62, a162\n" \
"v_accvgpr_read_b32 v63, a163\n" \
"v_mfma_f32_16x16x32_bf16 v[52:55], v[164:167], v[100:103], v[52:55]\n" \
"v_accvgpr_read_b32 v56, a220\n" \
"v_accvgpr_read_b32 v57, a221\n" \
"v_accvgpr_read_b32 v58, a222\n" \
"v_accvgpr_read_b32 v59, a223\n" \
"v_pk_mul_f32 v[60:61], v[60:61], s[70:71]\n" \
"v_accvgpr_read_b32 v64, a164\n" \
"v_mfma_f32_16x16x32_bf16 v[56:59], v[172:175], v[100:103], v[56:59]\n" \
"v_accvgpr_read_b32 v65, a165\n" \
"v_accvgpr_read_b32 v66, a166\n" \
"v_mfma_f32_16x16x32_bf16 v[56:59], v[176:179], v[104:107], v[56:59]\n" \
"v_accvgpr_read_b32 v67, a167\n" \
"buffer_store_dwordx4 a[72:75], v20, s[80:83], s88 offen offset:2048 sc1\n" \
"s_add_u32 s88, s45, 0x1000\n" \
"v_pk_mul_f32 v[62:63], v[62:63], s[70:71]\n" \
"v_cvt_pk_bf16_f32 v60, v60, v61\n" \
"v_pk_mul_f32 v[64:65], v[64:65], s[70:71]\n" \
"v_pk_mul_f32 v[66:67], v[66:67], s[70:71]\n" \
"v_cvt_pk_bf16_f32 v61, v62, v63\n" \
"v_cvt_pk_bf16_f32 v62, v64, v65\n" \
"v_cvt_pk_bf16_f32 v63, v66, v67\n" \
"ds_write_b128 v27, v[60:63] offset:4096\n" \
"v_accvgpr_read_b32 v68, a192\n" \
"v_accvgpr_read_b32 v69, a193\n" \
"v_accvgpr_read_b32 v60, a200\n" \
"v_accvgpr_read_b32 v61, a201\n" \
"v_accvgpr_read_b32 v70, a194\n" \
"v_accvgpr_read_b32 v71, a195\n" \
"v_pk_mul_f32 v[68:69], v[68:69], s[70:71]\n" \
"v_accvgpr_read_b32 v62, a202\n" \
"v_accvgpr_read_b32 v63, a203\n" \
"v_pk_mul_f32 v[60:61], v[60:61], s[70:71]\n" \
"v_accvgpr_read_b32 v72, a196\n" \
"v_accvgpr_read_b32 v73, a197\n" \
"v_accvgpr_read_b32 v74, a198\n" \
"v_accvgpr_read_b32 v75, a199\n" \
"v_pk_mul_f32 v[70:71], v[70:71], s[70:71]\n" \
"v_cvt_pk_bf16_f32 v68, v68, v69\n" \
"v_accvgpr_read_b32 v64, a204\n" \
"v_accvgpr_read_b32 v65, a205\n" \
"v_accvgpr_read_b32 v66, a206\n" \
"v_accvgpr_read_b32 v67, a207\n" \
"v_pk_mul_f32 v[62:63], v[62:63], s[70:71]\n" \
"v_cvt_pk_bf16_f32 v60, v60, v61\n" \
"v_accvgpr_read_b32 v100, a240\n" \
"v_accvgpr_read_b32 v101, a241\n" \
"v_accvgpr_read_b32 v102, a242\n" \
"v_accvgpr_read_b32 v103, a243\n" \
"v_pk_mul_f32 v[72:73], v[72:73], s[70:71]\n" \
"v_pk_mul_f32 v[74:75], v[74:75], s[70:71]\n" \
"v_mfma_f32_16x16x32_bf16 v[100:103], v[152:155], v[112:115], v[100:103]\n" \
"v_cvt_pk_bf16_f32 v69, v70, v71\n" \
"v_cvt_pk_bf16_f32 v61, v62, v63\n" \
"v_mfma_f32_16x16x32_bf16 v[100:103], v[148:151], v[108:111], v[100:103]\n" \
"v_cvt_pk_bf16_f32 v70, v72, v73\n" \
"v_cvt_pk_bf16_f32 v71, v74, v75\n" \
"ds_write_b128 v27, v[68:71] offset:8192\n" \
"v_pk_mul_f32 v[64:65], v[64:65], s[70:71]\n" \
"v_pk_mul_f32 v[66:67], v[66:67], s[70:71]\n" \
"v_cvt_pk_bf16_f32 v62, v64, v65\n" \
"v_cvt_pk_bf16_f32 v63, v66, v67\n" \
"ds_write_b128 v27, v[60:63] offset:9216\n" \
"v_accvgpr_read_b32 v84, a144\n" \
"buffer_store_dwordx4 a[76:79], v20, s[80:83], s89 offen offset:3072 sc1\n" \
"s_add_u32 s89, s45, 0x1000\n" \
"v_accvgpr_read_b32 v85, a145\n" \
"v_accvgpr_read_b32 v68, a152\n" \
"v_accvgpr_read_b32 v69, a153\n" \
"v_accvgpr_read_b32 v92, a176\n" \
"v_accvgpr_read_b32 v93, a177\n" \
"v_accvgpr_read_b32 v60, a184\n" \
"v_accvgpr_read_b32 v61, a185\n" \
"v_accvgpr_read_b32 v86, a146\n" \
"v_accvgpr_read_b32 v87, a147\n" \
"v_pk_mul_f32 v[84:85], v[84:85], s[70:71]\n" \
"v_accvgpr_read_b32 v70, a154\n" \
"v_accvgpr_read_b32 v71, a155\n" \
"v_pk_mul_f32 v[68:69], v[68:69], s[70:71]\n" \
"v_accvgpr_read_b32 v94, a178\n" \
"v_accvgpr_read_b32 v95, a179\n" \
"v_pk_mul_f32 v[92:93], v[92:93], s[70:71]\n" \
"v_accvgpr_read_b32 v62, a186\n" \
"v_accvgpr_read_b32 v63, a187\n" \
"v_pk_mul_f32 v[60:61], v[60:61], s[70:71]\n" \
"v_pk_mul_f32 v[76:77], v[76:77], s[70:71]\n" \
"v_pk_mul_f32 v[52:53], v[52:53], s[70:71]\n" \
"v_pk_mul_f32 v[100:101], v[100:101], s[70:71]\n" \
"v_accvgpr_read_b32 v88, a148\n" \
"v_accvgpr_read_b32 v89, a149\n" \
"v_accvgpr_read_b32 v90, a150\n" \
"v_accvgpr_read_b32 v91, a151\n" \
"v_pk_mul_f32 v[86:87], v[86:87], s[70:71]\n" \
"v_cvt_pk_bf16_f32 v84, v84, v85\n" \
"v_accvgpr_read_b32 v72, a156\n" \
"v_accvgpr_read_b32 v73, a157\n" \
"v_accvgpr_read_b32 v74, a158\n" \
"v_accvgpr_read_b32 v75, a159\n" \
"v_pk_mul_f32 v[70:71], v[70:71], s[70:71]\n" \
"v_cvt_pk_bf16_f32 v68, v68, v69\n" \
"v_accvgpr_read_b32 v96, a180\n" \
"v_accvgpr_read_b32 v97, a181\n" \
"v_accvgpr_read_b32 v98, a182\n" \
"v_accvgpr_read_b32 v99, a183\n" \
"v_pk_mul_f32 v[94:95], v[94:95], s[70:71]\n" \
"v_cvt_pk_bf16_f32 v92, v92, v93\n" \
"v_accvgpr_read_b32 v64, a188\n" \
"v_accvgpr_read_b32 v65, a189\n" \
"v_accvgpr_read_b32 v66, a190\n" \
"v_accvgpr_read_b32 v67, a191\n" \
"v_pk_mul_f32 v[62:63], v[62:63], s[70:71]\n" \
"v_cvt_pk_bf16_f32 v60, v60, v61\n" \
"v_pk_mul_f32 v[78:79], v[78:79], s[70:71]\n" \
"v_cvt_pk_bf16_f32 v76, v76, v77\n" \
"v_pk_mul_f32 v[54:55], v[54:55], s[70:71]\n" \
"v_cvt_pk_bf16_f32 v52, v52, v53\n" \
"v_pk_mul_f32 v[102:103], v[102:103], s[70:71]\n" \
"v_cvt_pk_bf16_f32 v100, v100, v101\n" \
"v_pk_mul_f32 v[88:89], v[88:89], s[70:71]\n" \
"v_pk_mul_f32 v[90:91], v[90:91], s[70:71]\n" \
"v_cvt_pk_bf16_f32 v85, v86, v87\n" \
"v_pk_mul_f32 v[72:73], v[72:73], s[70:71]\n" \
"v_pk_mul_f32 v[74:75], v[74:75], s[70:71]\n" \
"v_cvt_pk_bf16_f32 v69, v70, v71\n" \
"v_pk_mul_f32 v[96:97], v[96:97], s[70:71]\n" \
"v_pk_mul_f32 v[98:99], v[98:99], s[70:71]\n" \
"buffer_store_dwordx4 a[104:107], v20, s[80:83], s88 offen offset:0 sc1\n" \
"v_cvt_pk_bf16_f32 v93, v94, v95\n" \
"v_pk_mul_f32 v[64:65], v[64:65], s[70:71]\n" \
"v_pk_mul_f32 v[66:67], v[66:67], s[70:71]\n" \
"v_cvt_pk_bf16_f32 v61, v62, v63\n" \
"v_pk_mul_f32 v[80:81], v[80:81], s[70:71]\n" \
"v_pk_mul_f32 v[82:83], v[82:83], s[70:71]\n" \
"v_cvt_pk_bf16_f32 v77, v78, v79\n" \
"v_pk_mul_f32 v[56:57], v[56:57], s[70:71]\n" \
"v_pk_mul_f32 v[58:59], v[58:59], s[70:71]\n" \
"v_cvt_pk_bf16_f32 v53, v54, v55\n" \
"v_accvgpr_read_b32 v104, a244\n" \
"v_accvgpr_read_b32 v105, a245\n" \
"v_accvgpr_read_b32 v106, a246\n" \
"v_accvgpr_read_b32 v107, a247\n" \
"v_cvt_pk_bf16_f32 v101, v102, v103\n" \
"v_cvt_pk_bf16_f32 v86, v88, v89\n" \
"v_mfma_f32_16x16x32_bf16 v[104:107], v[156:159], v[108:111], v[104:107]\n" \
"v_cvt_pk_bf16_f32 v87, v90, v91\n" \
"v_cvt_pk_bf16_f32 v70, v72, v73\n" \
"ds_write_b128 v21, v[84:87] offset:16384\n" \
"v_mfma_f32_16x16x32_bf16 v[104:107], v[160:163], v[112:115], v[104:107]\n" \
"v_cvt_pk_bf16_f32 v71, v74, v75\n" \
"ds_write_b128 v21, v[68:71] offset:17408\n" \
"v_cvt_pk_bf16_f32 v94, v96, v97\n" \
"v_cvt_pk_bf16_f32 v95, v98, v99\n" \
"ds_write_b128 v21, v[92:95] offset:20480\n" \
"v_cvt_pk_bf16_f32 v62, v64, v65\n" \
"v_cvt_pk_bf16_f32 v63, v66, v67\n" \
"ds_write_b128 v21, v[60:63] offset:21504\n" \
"v_pk_mul_f32 v[104:105], v[104:105], s[70:71]\n" \
"v_pk_mul_f32 v[106:107], v[106:107], s[70:71]\n" \
"v_cvt_pk_bf16_f32 v78, v80, v81\n" \
"v_cvt_pk_bf16_f32 v79, v82, v83\n" \
"ds_write_b128 v21, v[76:79] offset:24576\n" \
"v_cvt_pk_bf16_f32 v54, v56, v57\n" \
"v_cvt_pk_bf16_f32 v55, v58, v59\n" \
"ds_write_b128 v21, v[52:55] offset:25600\n" \
"v_cvt_pk_bf16_f32 v102, v104, v105\n" \
"v_cvt_pk_bf16_f32 v103, v106, v107\n" \
"ds_write_b128 v21, v[100:103] offset:28672\n" \
"buffer_store_dwordx4 a[108:111], v20, s[80:83], s89 offen offset:1024 sc1\n" \
"v_accvgpr_read_b32 v84, a248\n" \
"v_accvgpr_read_b32 v85, a249\n" \
"v_accvgpr_read_b32 v86, a250\n" \
"v_accvgpr_read_b32 v87, a251\n" \
"v_accvgpr_read_b32 v88, a252\n" \
"v_accvgpr_read_b32 v89, a253\n" \
"v_mfma_f32_16x16x32_bf16 v[84:87], v[168:171], v[112:115], v[84:87]\n" \
"v_accvgpr_read_b32 v90, a254\n" \
"v_accvgpr_read_b32 v91, a255\n" \
"v_mfma_f32_16x16x32_bf16 v[84:87], v[164:167], v[108:111], v[84:87]\n" \
"v_sub_f32_e32 v44, 1.0, v45\n" \
"v_mfma_f32_16x16x32_bf16 v[88:91], v[172:175], v[108:111], v[88:91]\n" \
"ds_write_b32 v26, v44\n" \
"v_mfma_f32_16x16x32_bf16 v[88:91], v[176:179], v[112:115], v[88:91]\n" \
"s_nop 3\n" \
"v_pk_mul_f32 v[84:85], v[84:85], s[70:71]\n" \
"v_pk_mul_f32 v[86:87], v[86:87], s[70:71]\n" \
"v_cvt_pk_bf16_f32 v84, v84, v85\n" \
"v_cvt_pk_bf16_f32 v85, v86, v87\n" \
"v_pk_mul_f32 v[88:89], v[88:89], s[70:71]\n" \
"v_pk_mul_f32 v[90:91], v[90:91], s[70:71]\n" \
"v_cvt_pk_bf16_f32 v86, v88, v89\n" \
"v_cvt_pk_bf16_f32 v87, v90, v91\n" \
"ds_write_b128 v21, v[84:87] offset:29696\n" \
"L_end_%=:\n"
#define PROJ_VCLOB "v21","v24","v25","v33","v34","v35","v36","v37","v38","v39","v40","v41","v42","v43","v44","v45","v46","v47","v48","v49","v50","v51","v52","v53","v54","v55","v56","v57","v58","v59","v60","v61","v62","v63","v64","v65","v66","v67","v68","v69","v70","v71","v72","v73","v74","v75","v76","v77","v78","v79","v80","v81","v82","v83","v84","v85","v86","v87","v88","v89","v90","v91","v92","v93","v94","v95","v96","v97","v98","v99","v100","v101","v102","v103","v104","v105","v106","v107","v108","v109","v110","v111","v112","v113","v114","v115","v116","v117","v118","v119","v120","v121","v122","v123","v124","v125","v126","v127","v128","v129","v130","v131","v132","v133","v134","v135","v136","v137","v138","v139","v140","v141","v142","v143","v144","v145","v146","v147","v148","v149","v150","v151","v152","v153","v154","v155","v156","v157","v158","v159","v160","v161","v162","v163","v164","v165","v166","v167","v168","v169","v170","v171","v172","v173","v174","v175","v176","v177","v178","v179","v180","v181","v182","v183","v184","v185","v186","v187","v188","v189","v190","v191","v192","v193","v194","v195","v196","v197","v198","v199","v200","v201","v202","v203","v204","v205","v206","v207","v208","v209","v210","v211","v212","v213","v214","v215","v216","v217","v218","v219","v220","v221","v222","v223","v224","v225","v226","v227","v228","v229","v230","v231","v232","v233","v234","v235","v236","v237","v238","v239","v240","v241","v242","v243","v244","v245","v246","v247","v248","v249","v250","v251","v252","v253","v254","v255"
#define PROJ_SCLOB "s37","s38","s39","s40","s41","s42","s43","s44","s45","s46","s47","s48","s49","s50","s51","s52","s53","s54","s55","s62","s63","s76","s77","s78","s79","s80","s81","s82","s83","s84","s85","s86","s87","s88","s89","s94","s95","s96","s97","s98","s99","s100","s101"

#define PLO(p) ((unsigned)(size_t)(p))
#define PHI(p) ((unsigned)((size_t)(p) >> 32))
__device__ __forceinline__ unsigned proj_phase(const u16* __restrict__ x, const u16* __restrict__ ckv, const u16* __restrict__ wq, const u16* __restrict__ wuk,
                                                       const u16* __restrict__ wuv, const u16* __restrict__ wqkv, const u16* __restrict__ gw, const u16* wo, const u16* wob, const u16* out,
                                                       char* __restrict__ ws, char* lds, const unsigned wid, unsigned& tag, u32x4 (&vp)[5], u32x4 (&sa)[5], unsigned (&sd)[2]) {
    typedef unsigned u32x2 __attribute__((ext_vector_type(2)));
    unsigned* const flags = (unsigned*)(ws + WS_F);
    const int b = blockIdx.x, h = (b & 7) * 4 + ((b >> 3) & 3), mt = b >> 5;
    const unsigned L0 = (unsigned)(size_t)lds, rb = (wid < 2u ? 15u - mt : mt) * 128u;
    RTS(0);
#define PST() do {} while (0)
    const unsigned lane = threadIdx.x & 63u, wr = wid >> 1, wc = wid & 1u;
    PST();
    unsigned vAc[2], vB[2];
    const unsigned sp = b & 7, nt = (b >> 3) & 3, rot = (PROJ_ROT) & 7u;
#pragma unroll
    for (int par = 0; par < 2; ++par) {
        const unsigned lr = lane >> 3, cc = (lane & 7u) ^ (4u * par + (lr >> 1)), r0 = rb + wc * 64u + lr;
        vAc[par] = r0 * (DC * 2) + cc * 16u;
        const unsigned br = lane >> 4, p16 = lane & 15u, sc = (((p16 >> 1) ^ (br | (par << 2))) << 1) | (p16 & 1u);
        vB[par] = (16u * wid + br) * (NBIG * 2) + h * (HD * 2) + sc * 16u;
    }
#define SRD(p, n) (u32x4){PLO(p), PHI(p) & 0xffffu, n, 0x00020000u}
    u32x4 sC = SRD(ckv, S * DC * 2), sK = SRD(wuk, DC * NBIG * 2), sV = SRD(wuv, DC * NBIG * 2);
#define PRE(n) do { SBAR(); asm volatile(PROJ_PRE##n :: "{v22}"(vAc[0]), "{v23}"(vAc[1]), "{v26}"(vB[0]), "{v27}"(vB[1]), "{s[64:67]}"(sC), "{s[68:71]}"(sK), "{s[72:75]}"(sV), \
                 [mA] "s"(L0 + wid * 8192u), [mB] "s"(L0 + 65536u + wid * 4096u), [rc] "s"(rot * 128u) : "memory", "m0", "scc", "s62", "s63", "s84", "s85"); SBAR(); } while (0)
    PRE(0);
    const unsigned fr = lane & 15u, fg = lane >> 4, r4 = (lane >> 2) & 3u, sB = r4 | ((fg & 1u) << 2);
    const unsigned aA0 = L0 + (wr * 64u + fr) * 128u + ((fg ^ (fr >> 1)) << 4);
    unsigned vl = lane * 16u, r32 = L0 + 65536u + (8u * fg + r4) * 256u + (lane & 3u) * 8u + ((sB ^ (4u * wc)) << 5);
    u32x2 r28 = {aA0, aA0 ^ 64u};
    asm volatile("" : "+v"(vl), "+v"(r28), "+v"(r32));
    PST();
    PRE(1);
    PST();
    PRE(2);
    PST();
    asm volatile("s_mov_b64 exec, 0\n s_cmp_lt_u32 %0, 2\n s_cbranch_scc1 L_chA\n s_cmp_eq_u32 %0, 2\n s_cbranch_scc1 L_pf2\n s_branch L_pf3\n"
                 "L_chA: s_cmp_eq_u32 %0, 0\n s_cbranch_scc1 L_pf0\n s_branch L_pf1\n L_retA: s_mov_b64 exec, -1" :: "s"(wid) : "scc", "memory");
    PRE(3);
    PST();
    sa[0] = (u32x4){PLO(x), PHI(x), PLO(wq), PHI(wq)}; sa[1] = (u32x4){PLO(wqkv), PHI(wqkv), PLO(gw), PHI(gw)}; sa[2] = (u32x4){PLO(wo), PHI(wo), PLO(wob), PHI(wob)}; sa[3] = (u32x4){PLO(out), PHI(out), rot * 128u, 0u};
    asm volatile(PROJ_ASM
                 : [tag] "=&s"(tag), "+{s[8:11]}"(sa[0]), "+{s[12:15]}"(sa[1]), "+{s[16:19]}"(sa[2]), "+{s[20:23]}"(sa[3]), "=&{s[56:59]}"(sa[4]), "=&{s60}"(sd[0]), "=&{s61}"(sd[1]),
                   "+{s[64:67]}"(sC), "+{s[68:71]}"(sK), "+{s[72:75]}"(sV), "+{v22}"(vAc[0]), "+{v23}"(vAc[1]), "+{v26}"(vB[0]), "+{v27}"(vB[1]), "+{v[28:29]}"(r28), "+{v32}"(r32),
                   "={v[0:3]}"(vp[0]), "={v[4:7]}"(vp[1]), "={v[8:11]}"(vp[2]), "={v[12:15]}"(vp[3]), "={v[16:19]}"(vp[4])
                 : "{v20}"(vl), [wsl] "s"(PLO(ws)), [wsh] "s"(PHI(ws)), [b] "s"(b), [w] "s"(wid), [mA] "s"(L0 + wid * 8192u), [mB] "s"(L0 + 65536u + wid * 4096u), [mS] "s"(L0 + 151552u + wid * 512u)
                 : "memory", "m0", "vcc", "scc", PROJ_VCLOB, PROJ_SCLOB, ACLOB_LO, ACLOB_HI);
    RTS(1);
    return vl;
}

namespace att {
#define DI __device__ __forceinline__
constexpr int LDS_BYTES = 163840;
constexpr unsigned LDS_G = 159744u, LDS_L = 161792u;
typedef __bf16 bfx8 __attribute__((ext_vector_type(8)));
DI unsigned rfl(unsigned x) { return __builtin_amdgcn_readfirstlane(x); }
DI f32x4 mfma(u32x4 a, u32x4 b, f32x4 c) { return __builtin_amdgcn_mfma_f32_16x16x32_bf16(__builtin_bit_cast(bfx8, a), __builtin_bit_cast(bfx8, b), c, 0, 0, 0); }
typedef __bf16 bfx2 __attribute__((ext_vector_type(2)));
typedef float f32x2 __attribute__((ext_vector_type(2)));
DI unsigned cvtpk2(float lo, float hi) { f32x2 v = {lo, hi}; bfx2 b = __builtin_convertvector(v, bfx2); return __builtin_bit_cast(unsigned, b); }
DI void att_exact(const u16* __restrict__ x, const u16* __restrict__ wq, char* __restrict__ ws, unsigned h, unsigned r0, float g1) {
    const unsigned lane = lane_id(), g = lane >> 4, c = lane & 15u;
    f32x4 o[8];
#pragma unroll
    for (int n = 0; n < 8; ++n) o[n] = (f32x4){0.f, 0.f, 0.f, 0.f};
#pragma unroll 1
    for (unsigned kk = 0; kk < (unsigned)DM; kk += 32u) {
        const unsigned k = kk + 8u * g;
        const u32x4 xf = *reinterpret_cast<const u32x4*>(x + (r0 + c) * DM + k);
#pragma unroll
        for (int n = 0; n < 8; ++n) {
            const u16* w = wq + k * NBIG + h * HD + n * 16 + c; u32x4 wf;
#pragma unroll
            for (int e = 0; e < 4; ++e) wf[e] = (unsigned)w[2 * e * NBIG] | ((unsigned)w[(2 * e + 1) * NBIG] << 16);
            o[n] = mfma(wf, xf, o[n]);
        }
    }
    u32x4 q[4];
#pragma unroll
    for (int p = 0; p < 4; ++p) {
        const f32x4 a = o[2 * p] * 0.12751743082f, b = o[2 * p + 1] * 0.12751743082f;
        q[p] = (u32x4){cvtpk2(a[0], a[1]), cvtpk2(a[2], a[3]), cvtpk2(b[0], b[1]), cvtpk2(b[2], b[3])};
    }
#pragma unroll
    for (int n = 0; n < 8; ++n) o[n] = (f32x4){0.f, 0.f, 0.f, 0.f};
    const __amdgpu_buffer_rsrc_t srd = __builtin_amdgcn_make_buffer_rsrc((void*)(ws + WS_K + (size_t)h * 1048576), (short)0, 1048576, 0x00020000);
    float m = -3.0e38f, l = 0.f;
#pragma unroll 1
    for (unsigned k0 = 0; k0 <= r0 + 15u; k0 += 32u) {
        const unsigned tb = (k0 >> 6) * 32768u + (k0 & 32u) * 256u;
        f32x4 s[2]; float mx = m;
#pragma unroll
        for (int kt = 0; kt < 2; ++kt) {
            s[kt] = (f32x4){0.f, 0.f, 0.f, 0.f};
#pragma unroll
            for (int p = 0; p < 4; ++p) s[kt] = mfma(__builtin_amdgcn_raw_buffer_load_b128(srd, lane * 16u + (kt * 4 + p) * 1024u, tb, 16), q[p], s[kt]);
#pragma unroll
            for (int e = 0; e < 4; ++e) { if (k0 + 16u * kt + 4u * g + e > r0 + c) s[kt][e] = -3.0e38f; mx = fmaxf(mx, s[kt][e]); }
        }
        mx = fmaxf(mx, __shfl_xor(mx, 16)); mx = fmaxf(mx, __shfl_xor(mx, 32));
        const float al = __builtin_amdgcn_exp2f(m - mx); m = mx;
        u32x4 pf;
#pragma unroll
        for (int e = 0; e < 4; ++e) pf[e] = cvtpk2(__builtin_amdgcn_exp2f(s[e >> 1][2 * (e & 1)] - m), __builtin_amdgcn_exp2f(s[e >> 1][2 * (e & 1) + 1] - m));
        l *= al;
#pragma unroll
        for (int e = 0; e < 4; ++e) l += __uint_as_float(pf[e] << 16) + __uint_as_float(pf[e] & 0xffff0000u);
#pragma unroll
        for (int n = 0; n < 8; ++n) o[n] = mfma(__builtin_amdgcn_raw_buffer_load_b128(srd, lane * 16u + 16384u + ((n >> 2) * 8 + (n & 3)) * 1024u, tb - (k0 & 32u) * 128u, 16), pf, o[n] * al);
    }
    l += __shfl_xor(l, 16); l += __shfl_xor(l, 32);
    const float f = g1 / l;
    const __amdgpu_buffer_rsrc_t srdA = __builtin_amdgcn_make_buffer_rsrc((void*)(ws + WS_AF), (short)0, 128 * KS_TOT * 1024, 0x00020000);
#pragma unroll
    for (int p = 0; p < 4; ++p) {
        const f32x4 a = o[2 * p] * f, b = o[2 * p + 1] * f;
        __builtin_amdgcn_raw_buffer_store_b128((u32x4){cvtpk2(a[0], a[1]), cvtpk2(a[2], a[3]), cvtpk2(b[0], b[1]), cvtpk2(b[2], b[3])}, srdA, lane * 16u + p * 1024u, ((r0 >> 4) * KS_TOT + 18u + 4u * h) * 1024u, 16);
    }
}
}

#define ATT_ASM \
"L_att0:\n" \
"s_mov_b32 s101, 0\n" \
"s_waitcnt vmcnt(20) lgkmcnt(0)\n" \
"s_barrier\n" \
"v_accvgpr_write_b32 a216, 0\n" \
"v_accvgpr_write_b32 a217, 0\n" \
"v_accvgpr_write_b32 a218, 0\n" \
"v_accvgpr_write_b32 a219, 0\n" \
"v_and_b32_e32 v63, 0x300, v20\n" \
"v_add_u32_e32 v58, s20, v20\n" \
"v_mfma_f32_32x32x16_bf16 v[128:143], a[216:219], a[216:219], 0\n" \
"ds_read_b128 v[112:115], v63 offset:0\n" \
"ds_read_b128 a[0:3], v58 offset:0\n" \
"ds_read_b128 a[16:19], v58 offset:8192\n" \
"ds_read_b128 v[116:119], v63 offset:1024\n" \
"ds_read_b128 a[4:7], v58 offset:1024\n" \
"v_add_u32_e32 v59, s21, v20\n" \
"v_mfma_f32_32x32x16_bf16 v[144:159], a[216:219], a[216:219], 0\n" \
"ds_read_b128 a[32:35], v59 offset:0\n" \
"ds_read_b128 a[48:51], v59 offset:8192\n" \
"s_sub_u32 s67, s12, 0xc000\n" \
"s_mov_b32 s65, s12\n" \
"v_add_u32_e32 v55, s67, v20\n" \
"v_add_u32_e32 v52, 0xc000, v55\n" \
"v_mfma_f32_32x32x16_bf16 v[160:175], a[216:219], a[216:219], 0\n" \
"ds_read_b128 a[20:23], v58 offset:9216\n" \
"s_xor_b32 m0, s65, 0x10000\n" \
"ds_read_b128 a[36:39], v59 offset:1024\n" \
"buffer_load_dwordx4 v52, s[8:11], 0 offen offset:0 sc1 lds\n" \
"ds_read_b128 a[52:55], v59 offset:9216\n" \
"v_mfma_f32_32x32x16_bf16 v[176:191], a[216:219], a[216:219], 0\n" \
"buffer_load_dwordx4 v52, s[8:11], 0 offen offset:1024 sc1 lds\n" \
"ds_read_b128 v[120:123], v63 offset:2048\n" \
"v_add_u32_e32 v60, s15, v20\n" \
"v_add_u32_e32 v60, 0x10000, v60\n" \
"v_mov_b32_e32 v56, s62\n" \
"v_mfma_f32_32x32x16_bf16 v[192:207], a[216:219], a[216:219], 0\n" \
"ds_read_b128 a[8:11], v58 offset:2048\n" \
"ds_read_b128 a[24:27], v58 offset:10240\n" \
"v_readlane_b32 s68, v11, 0\n" \
"v_bfe_i32 v21, v20, 4, 1\n" \
"v_mov_b32_e32 v54, 0x3f803f80\n" \
"v_mov_b32_e32 v57, 0x3f003f00\n" \
"v_mfma_f32_32x32x16_bf16 v[208:223], a[216:219], a[216:219], 0\n" \
"ds_read_b128 a[40:43], v59 offset:2048\n" \
"ds_read_b128 a[56:59], v59 offset:10240\n" \
"s_waitcnt lgkmcnt(14)\n" \
"v_xor_b32_e32 v112, 0x80008000, v112\n" \
"v_xor_b32_e32 v113, 0x80008000, v113\n" \
"v_xor_b32_e32 v114, 0x80008000, v114\n" \
"v_mfma_f32_32x32x16_bf16 v[224:239], a[216:219], a[216:219], 0\n" \
"ds_read_b128 v[124:127], v63 offset:3072\n" \
"ds_read_b128 a[12:15], v58 offset:3072\n" \
"v_xor_b32_e32 v115, 0x80008000, v115\n" \
"v_and_b32_e32 v21, 0xaaaaaaaa, v21\n" \
"v_accvgpr_write_b32 a220, v112\n" \
"s_waitcnt lgkmcnt(13)\n" \
"v_xor_b32_e32 v116, 0x80008000, v116\n" \
"v_mfma_f32_32x32x16_bf16 v[240:255], a[216:219], a[216:219], 0\n" \
"ds_read_b128 a[28:31], v58 offset:11264\n" \
"ds_read_b128 a[44:47], v59 offset:3072\n" \
"v_xor_b32_e32 v117, 0x80008000, v117\n" \
"v_xor_b32_e32 v118, 0x80008000, v118\n" \
"v_xor_b32_e32 v119, 0x80008000, v119\n" \
"v_accvgpr_write_b32 a221, v113\n" \
"v_mfma_f32_32x32x16_bf16 a[192:207], a[216:219], a[216:219], 0\n" \
"ds_read_b128 a[60:63], v59 offset:11264\n" \
"ds_read_b128 a[160:163], v20 offset:0\n" \
"v_accvgpr_write_b32 a222, v114\n" \
"v_accvgpr_write_b32 a223, v115\n" \
"v_readlane_b32 s80, v11, 9\n" \
"v_readlane_b32 s81, v11, 10\n" \
"v_mfma_f32_16x16x32_bf16 v[32:35], v[112:115], a[0:3], 0\n" \
"ds_read_b128 a[164:167], v20 offset:1024\n" \
"ds_read_b128 a[168:171], v20 offset:2048\n" \
"v_mfma_f32_16x16x32_bf16 v[36:39], v[112:115], a[16:19], 0\n" \
"ds_read_b128 a[172:175], v20 offset:3072\n" \
"s_waitcnt lgkmcnt(15)\n" \
"v_mfma_f32_16x16x32_bf16 v[40:43], v[112:115], a[32:35], 0\n" \
"ds_read_b128 a[188:191], v20 offset:7168\n" \
"s_waitcnt lgkmcnt(14)\n" \
"v_xor_b32_e32 v120, 0x80008000, v120\n" \
"v_mfma_f32_16x16x32_bf16 v[44:47], v[112:115], a[48:51], 0\n" \
"ds_read_b128 a[184:187], v20 offset:6144\n" \
"v_xor_b32_e32 v121, 0x80008000, v121\n" \
"v_mfma_f32_16x16x32_bf16 v[32:35], v[116:119], a[4:7], v[32:35]\n" \
"v_xor_b32_e32 v122, 0x80008000, v122\n" \
"v_xor_b32_e32 v123, 0x80008000, v123\n" \
"v_mfma_f32_16x16x32_bf16 v[36:39], v[116:119], a[20:23], v[36:39]\n" \
"ds_read_b128 a[180:183], v20 offset:5120\n" \
"ds_read_b128 a[176:179], v20 offset:4096\n" \
"v_mfma_f32_16x16x32_bf16 v[40:43], v[116:119], a[36:39], v[40:43]\n" \
"ds_read_b128 a[128:131], v20 offset:8192\n" \
"s_mov_b32 s82, 0x47fca0\n" \
"v_mfma_f32_16x16x32_bf16 v[44:47], v[116:119], a[52:55], v[44:47]\n" \
"s_waitcnt lgkmcnt(13)\n" \
"v_xor_b32_e32 v124, 0x80008000, v124\n" \
"v_mfma_f32_16x16x32_bf16 v[32:35], v[120:123], a[8:11], v[32:35]\n" \
"v_xor_b32_e32 v125, 0x80008000, v125\n" \
"v_xor_b32_e32 v126, 0x80008000, v126\n" \
"v_mfma_f32_16x16x32_bf16 v[36:39], v[120:123], a[24:27], v[36:39]\n" \
"ds_read_b128 a[132:135], v20 offset:9216\n" \
"v_xor_b32_e32 v127, 0x80008000, v127\n" \
"v_mfma_f32_16x16x32_bf16 v[40:43], v[120:123], a[40:43], v[40:43]\n" \
"ds_read_b128 a[136:139], v20 offset:10240\n" \
"ds_read_b128 a[140:143], v20 offset:11264\n" \
"v_mfma_f32_16x16x32_bf16 v[44:47], v[120:123], a[56:59], v[44:47]\n" \
"ds_read_b128 a[144:147], v20 offset:12288\n" \
"s_waitcnt lgkmcnt(15)\n" \
"v_mfma_f32_16x16x32_bf16 v[32:35], v[124:127], a[12:15], v[32:35]\n" \
"ds_read_b128 a[148:151], v20 offset:13312\n" \
"ds_read_b128 a[152:155], v20 offset:14336\n" \
"v_mfma_f32_16x16x32_bf16 v[36:39], v[124:127], a[28:31], v[36:39]\n" \
"ds_read_b128 a[156:159], v20 offset:15360\n" \
"s_waitcnt lgkmcnt(15)\n" \
"v_mfma_f32_16x16x32_bf16 v[40:43], v[124:127], a[44:47], v[40:43]\n" \
"ds_read_b128 a[64:67], v60 offset:16384\n" \
"s_mov_b32 s83, 0x20000\n" \
"v_mfma_f32_16x16x32_bf16 v[44:47], v[124:127], a[60:63], v[44:47]\n" \
"ds_read_b128 a[68:71], v60 offset:17408\n" \
"s_mov_b32 s100, 0x71800000\n" \
"v_mfma_f32_16x16x32_bf16 v[64:67], a[160:163], a[0:3], v[32:35]\n" \
"ds_read_b128 a[72:75], v60 offset:18432\n" \
"s_waitcnt lgkmcnt(15)\n" \
"v_mfma_f32_16x16x32_bf16 v[64:67], a[164:167], a[4:7], v[64:67]\n" \
"ds_read_b128 a[76:79], v60 offset:19456\n" \
"s_mov_b32 s63, s23\n" \
"v_mfma_f32_16x16x32_bf16 v[64:67], a[168:171], a[8:11], v[64:67]\n" \
"ds_read_b128 a[80:83], v60 offset:24576\n" \
"v_mov_b32_e32 v62, 0\n" \
"v_mfma_f32_16x16x32_bf16 v[64:67], a[172:175], a[12:15], v[64:67]\n" \
"ds_read_b128 a[84:87], v60 offset:25600\n" \
"s_waitcnt lgkmcnt(15)\n" \
"v_mfma_f32_16x16x32_bf16 v[68:71], a[188:191], a[12:15], v[32:35]\n" \
"ds_read_b128 a[88:91], v60 offset:26624\n" \
"v_accvgpr_write_b32 a212, v54\n" \
"v_mfma_f32_16x16x32_bf16 v[68:71], a[184:187], a[8:11], v[68:71]\n" \
"ds_read_b128 a[92:95], v60 offset:27648\n" \
"v_accvgpr_write_b32 a208, 0\n" \
"v_mfma_f32_16x16x32_bf16 v[68:71], a[180:183], a[4:7], v[68:71]\n" \
"ds_read_b128 a[96:99], v60 offset:0\n" \
"s_waitcnt lgkmcnt(15)\n" \
"v_mfma_f32_16x16x32_bf16 v[68:71], a[176:179], a[0:3], v[68:71]\n" \
"ds_read_b128 a[100:103], v60 offset:1024\n" \
"v_accvgpr_write_b32 a213, v54\n" \
"v_mfma_f32_16x16x32_bf16 v[72:75], a[160:163], a[16:19], v[36:39]\n" \
"ds_read_b128 a[104:107], v60 offset:2048\n" \
"v_accvgpr_write_b32 a209, 0\n" \
"v_mfma_f32_16x16x32_bf16 v[72:75], a[164:167], a[20:23], v[72:75]\n" \
"ds_read_b128 a[108:111], v60 offset:3072\n" \
"v_accvgpr_write_b32 a214, v54\n" \
"v_mfma_f32_16x16x32_bf16 v[72:75], a[168:171], a[24:27], v[72:75]\n" \
"ds_read_b128 a[112:115], v60 offset:8192\n" \
"v_accvgpr_write_b32 a210, 0\n" \
"v_mfma_f32_16x16x32_bf16 v[72:75], a[172:175], a[28:31], v[72:75]\n" \
"ds_read_b128 a[116:119], v60 offset:9216\n" \
"v_accvgpr_write_b32 a215, v54\n" \
"v_mfma_f32_16x16x32_bf16 v[76:79], a[188:191], a[28:31], v[36:39]\n" \
"ds_read_b128 a[120:123], v60 offset:10240\n" \
"v_accvgpr_write_b32 a211, 0\n" \
"v_mfma_f32_16x16x32_bf16 v[76:79], a[184:187], a[24:27], v[76:79]\n" \
"ds_read_b128 a[124:127], v60 offset:11264\n" \
"v_or_b32_e32 v21, 0x44444444, v21\n" \
"v_mfma_f32_16x16x32_bf16 v[76:79], a[180:183], a[20:23], v[76:79]\n" \
"L_pf10:\n" \
"s_cbranch_execz L_pf14\n" \
"s_nop 0\n" \
"v_exp_f32_e32 v64, v64\n" \
"v_mfma_f32_16x16x32_bf16 v[76:79], a[176:179], a[16:19], v[76:79]\n" \
"v_exp_f32_e32 v65, v65\n" \
"v_mfma_f32_16x16x32_bf16 v[80:83], a[160:163], a[32:35], v[40:43]\n" \
"v_exp_f32_e32 v66, v66\n" \
"v_mfma_f32_16x16x32_bf16 v[80:83], a[164:167], a[36:39], v[80:83]\n" \
"v_exp_f32_e32 v67, v67\n" \
"v_mfma_f32_16x16x32_bf16 v[80:83], a[168:171], a[40:43], v[80:83]\n" \
"v_exp_f32_e32 v68, v68\n" \
"v_mfma_f32_16x16x32_bf16 v[80:83], a[172:175], a[44:47], v[80:83]\n" \
"v_exp_f32_e32 v69, v69\n" \
"v_mfma_f32_16x16x32_bf16 v[84:87], a[188:191], a[44:47], v[40:43]\n" \
"v_exp_f32_e32 v70, v70\n" \
"v_mfma_f32_16x16x32_bf16 v[84:87], a[184:187], a[40:43], v[84:87]\n" \
"v_exp_f32_e32 v71, v71\n" \
"v_mfma_f32_16x16x32_bf16 v[84:87], a[180:183], a[36:39], v[84:87]\n" \
"v_exp_f32_e32 v72, v72\n" \
"v_mfma_f32_16x16x32_bf16 v[84:87], a[176:179], a[32:35], v[84:87]\n" \
"v_exp_f32_e32 v73, v73\n" \
"v_mfma_f32_16x16x32_bf16 v[88:91], a[160:163], a[48:51], v[44:47]\n" \
"v_exp_f32_e32 v74, v74\n" \
"v_mfma_f32_16x16x32_bf16 v[88:91], a[164:167], a[52:55], v[88:91]\n" \
"v_exp_f32_e32 v75, v75\n" \
"v_mfma_f32_16x16x32_bf16 v[88:91], a[168:171], a[56:59], v[88:91]\n" \
"v_exp_f32_e32 v76, v76\n" \
"v_mfma_f32_16x16x32_bf16 v[88:91], a[172:175], a[60:63], v[88:91]\n" \
"v_exp_f32_e32 v77, v77\n" \
"v_mfma_f32_16x16x32_bf16 v[92:95], a[188:191], a[60:63], v[44:47]\n" \
"v_exp_f32_e32 v78, v78\n" \
"v_mfma_f32_16x16x32_bf16 v[92:95], a[184:187], a[56:59], v[92:95]\n" \
"v_exp_f32_e32 v79, v79\n" \
"v_mfma_f32_16x16x32_bf16 v[92:95], a[180:183], a[52:55], v[92:95]\n" \
"v_exp_f32_e32 v80, v80\n" \
"v_mfma_f32_16x16x32_bf16 v[92:95], a[176:179], a[48:51], v[92:95]\n" \
"v_exp_f32_e32 v81, v81\n" \
"v_exp_f32_e32 v82, v82\n" \
"v_exp_f32_e32 v83, v83\n" \
"v_exp_f32_e32 v84, v84\n" \
"v_exp_f32_e32 v85, v85\n" \
"v_exp_f32_e32 v86, v86\n" \
"v_exp_f32_e32 v87, v87\n" \
"v_accvgpr_write_b32 a252, v57\n" \
"v_accvgpr_write_b32 a253, v57\n" \
"v_accvgpr_write_b32 a254, v57\n" \
"v_accvgpr_write_b32 a255, v57\n" \
"s_mov_b32 s64, 0x8000\n" \
"v_add_u32_e32 v50, 0x6000, v55\n" \
"v_add_u32_e32 v51, 0xa000, v55\n" \
"v_add_u32_e32 v53, 0x10000, v55\n" \
"v_add_u32_e32 v48, 0xc000, v20\n" \
"v_add_u32_e32 v49, s17, v20\n" \
"v_accvgpr_write_b32 a224, v116\n" \
"v_accvgpr_write_b32 a225, v117\n" \
"v_accvgpr_write_b32 a226, v118\n" \
"v_accvgpr_write_b32 a227, v119\n" \
"v_accvgpr_write_b32 a228, v120\n" \
"v_accvgpr_write_b32 a229, v121\n" \
"v_accvgpr_write_b32 a230, v122\n" \
"v_accvgpr_write_b32 a231, v123\n" \
"v_accvgpr_write_b32 a232, v124\n" \
"v_accvgpr_write_b32 a233, v125\n" \
"v_accvgpr_write_b32 a234, v126\n" \
"v_accvgpr_write_b32 a235, v127\n" \
"v_exp_f32_e32 v88, v88\n" \
"v_exp_f32_e32 v89, v89\n" \
"v_exp_f32_e32 v90, v90\n" \
"v_exp_f32_e32 v91, v91\n" \
"v_cvt_pk_bf16_f32 v96, v64, v65\n" \
"v_cvt_pk_bf16_f32 v97, v66, v67\n" \
"v_cvt_pk_bf16_f32 v98, v68, v69\n" \
"v_cvt_pk_bf16_f32 v99, v70, v71\n" \
"v_cvt_pk_bf16_f32 v104, v72, v73\n" \
"v_cvt_pk_bf16_f32 v105, v74, v75\n" \
"v_cvt_pk_bf16_f32 v106, v76, v77\n" \
"v_cvt_pk_bf16_f32 v107, v78, v79\n" \
"v_cvt_pk_bf16_f32 v112, v80, v81\n" \
"v_cvt_pk_bf16_f32 v113, v82, v83\n" \
"v_cvt_pk_bf16_f32 v114, v84, v85\n" \
"v_cvt_pk_bf16_f32 v115, v86, v87\n" \
"s_sub_u32 s66, s18, 1\n" \
"s_waitcnt vmcnt(2) lgkmcnt(0)\n" \
"s_barrier\n" \
"s_mov_b64 exec, 1\n" \
"buffer_store_dword v56, off, s[56:59], s68 offset:0 sc1\n" \
"buffer_store_dword v56, off, s[56:59], s22 sc1\n" \
"s_mov_b64 exec, -1\n" \
"s_cbranch_scc1 L_tr_%=\n" \
"s_branch L_1_%=\n" \
"L_t1_%=:\n" \
"v_mfma_f32_16x16x32_bf16 v[252:255], a[188:191], v[124:127], v[252:255]\n" \
"v_mfma_f32_16x16x32_bf16 v[252:255], a[172:175], v[120:123], v[252:255]\n" \
"v_smfmac_f32_16x16x64_bf16 a[204:207], a[212:215], v[120:127], v21\n" \
"s_waitcnt lgkmcnt(0)\n" \
"L_1_%=:\n" \
"v_mfma_f32_16x16x32_bf16 v[64:67], a[128:131], a[0:3], v[32:35]\n" \
"ds_read_b128 a[160:163], v49 offset:0\n" \
"ds_read_b128 a[176:179], v49 offset:4096\n" \
"v_mfma_f32_16x16x32_bf16 v[64:67], a[132:135], a[4:7], v[64:67]\n" \
"v_cvt_pk_bf16_f32 v120, v88, v89\n" \
"v_cvt_pk_bf16_f32 v121, v90, v91\n" \
"v_mfma_f32_16x16x32_bf16 v[64:67], a[136:139], a[8:11], v[64:67]\n" \
"ds_read_b128 a[180:183], v49 offset:5120\n" \
"ds_read_b128 a[164:167], v49 offset:1024\n" \
"v_mfma_f32_16x16x32_bf16 v[64:67], a[140:143], a[12:15], v[64:67]\n" \
"v_exp_f32_e32 v92, v92\n" \
"v_mfma_f32_16x16x32_bf16 v[68:71], a[156:159], a[12:15], v[32:35]\n" \
"ds_read_b128 a[168:171], v49 offset:2048\n" \
"ds_read_b128 a[184:187], v49 offset:6144\n" \
"v_mfma_f32_16x16x32_bf16 v[68:71], a[152:155], a[8:11], v[68:71]\n" \
"v_exp_f32_e32 v93, v93\n" \
"v_mfma_f32_16x16x32_bf16 v[68:71], a[148:151], a[4:7], v[68:71]\n" \
"ds_read_b128 a[188:191], v49 offset:7168\n" \
"ds_read_b128 a[172:175], v49 offset:3072\n" \
"v_mfma_f32_16x16x32_bf16 v[68:71], a[144:147], a[0:3], v[68:71]\n" \
"v_exp_f32_e32 v94, v94\n" \
"v_mfma_f32_16x16x32_bf16 v[72:75], a[128:131], a[16:19], v[36:39]\n" \
"v_exp_f32_e32 v95, v95\n" \
"v_mfma_f32_16x16x32_bf16 v[72:75], a[132:135], a[20:23], v[72:75]\n" \
"v_cvt_pk_bf16_f32 v122, v92, v93\n" \
"v_cvt_pk_bf16_f32 v123, v94, v95\n" \
"v_mfma_f32_16x16x32_bf16 v[72:75], a[136:139], a[24:27], v[72:75]\n" \
"v_exp_f32_e32 v64, v64\n" \
"v_mfma_f32_16x16x32_bf16 v[72:75], a[140:143], a[28:31], v[72:75]\n" \
"v_exp_f32_e32 v65, v65\n" \
"v_mfma_f32_16x16x32_bf16 v[76:79], a[156:159], a[28:31], v[36:39]\n" \
"v_exp_f32_e32 v66, v66\n" \
"v_mfma_f32_16x16x32_bf16 v[76:79], a[152:155], a[24:27], v[76:79]\n" \
"v_exp_f32_e32 v67, v67\n" \
"v_mfma_f32_16x16x32_bf16 v[76:79], a[148:151], a[20:23], v[76:79]\n" \
"v_exp_f32_e32 v68, v68\n" \
"v_mfma_f32_16x16x32_bf16 v[76:79], a[144:147], a[16:19], v[76:79]\n" \
"v_exp_f32_e32 v69, v69\n" \
"v_mfma_f32_16x16x32_bf16 v[80:83], a[128:131], a[32:35], v[40:43]\n" \
"v_exp_f32_e32 v70, v70\n" \
"v_mfma_f32_16x16x32_bf16 v[80:83], a[132:135], a[36:39], v[80:83]\n" \
"v_exp_f32_e32 v71, v71\n" \
"v_mfma_f32_16x16x32_bf16 v[80:83], a[136:139], a[40:43], v[80:83]\n" \
"v_cvt_pk_bf16_f32 v100, v64, v65\n" \
"v_cvt_pk_bf16_f32 v101, v66, v67\n" \
"v_mfma_f32_16x16x32_bf16 v[80:83], a[140:143], a[44:47], v[80:83]\n" \
"v_cvt_pk_bf16_f32 v102, v68, v69\n" \
"v_cvt_pk_bf16_f32 v103, v70, v71\n" \
"v_mfma_f32_16x16x32_bf16 v[84:87], a[156:159], a[44:47], v[40:43]\n" \
"v_exp_f32_e32 v72, v72\n" \
"v_mfma_f32_16x16x32_bf16 v[84:87], a[152:155], a[40:43], v[84:87]\n" \
"v_exp_f32_e32 v73, v73\n" \
"v_mfma_f32_16x16x32_bf16 v[84:87], a[148:151], a[36:39], v[84:87]\n" \
"v_exp_f32_e32 v74, v74\n" \
"v_mfma_f32_16x16x32_bf16 v[84:87], a[144:147], a[32:35], v[84:87]\n" \
"v_exp_f32_e32 v75, v75\n" \
"v_mfma_f32_16x16x32_bf16 v[88:91], a[128:131], a[48:51], v[44:47]\n" \
"v_exp_f32_e32 v76, v76\n" \
"v_mfma_f32_16x16x32_bf16 v[88:91], a[132:135], a[52:55], v[88:91]\n" \
"v_exp_f32_e32 v77, v77\n" \
"v_mfma_f32_16x16x32_bf16 v[88:91], a[136:139], a[56:59], v[88:91]\n" \
"v_exp_f32_e32 v78, v78\n" \
"v_mfma_f32_16x16x32_bf16 v[88:91], a[140:143], a[60:63], v[88:91]\n" \
"v_exp_f32_e32 v79, v79\n" \
"v_mfma_f32_16x16x32_bf16 v[92:95], a[156:159], a[60:63], v[44:47]\n" \
"v_cvt_pk_bf16_f32 v108, v72, v73\n" \
"v_cvt_pk_bf16_f32 v109, v74, v75\n" \
"v_mfma_f32_16x16x32_bf16 v[92:95], a[152:155], a[56:59], v[92:95]\n" \
"v_cvt_pk_bf16_f32 v110, v76, v77\n" \
"v_cvt_pk_bf16_f32 v111, v78, v79\n" \
"v_mfma_f32_16x16x32_bf16 v[92:95], a[148:151], a[52:55], v[92:95]\n" \
"ds_read_b128 a[128:131], v48 offset:0\n" \
"ds_read_b128 a[132:135], v48 offset:1024\n" \
"v_mfma_f32_16x16x32_bf16 v[92:95], a[144:147], a[48:51], v[92:95]\n" \
"s_add_u32 s63, s63, s63\n" \
"s_waitcnt lgkmcnt(2)\n" \
"v_mfma_f32_16x16x32_bf16 v[128:131], a[160:163], v[96:99], v[128:131]\n" \
"ds_read_b128 a[136:139], v48 offset:2048\n" \
"ds_read_b128 a[140:143], v48 offset:3072\n" \
"v_mfma_f32_16x16x32_bf16 v[128:131], a[176:179], v[100:103], v[128:131]\n" \
"v_exp_f32_e32 v80, v80\n" \
"v_mfma_f32_16x16x32_bf16 v[132:135], a[180:183], v[100:103], v[132:135]\n" \
"ds_read_b128 a[144:147], v48 offset:4096\n" \
"ds_read_b128 a[148:151], v48 offset:5120\n" \
"v_mfma_f32_16x16x32_bf16 v[132:135], a[164:167], v[96:99], v[132:135]\n" \
"v_exp_f32_e32 v81, v81\n" \
"v_mfma_f32_16x16x32_bf16 v[136:139], a[168:171], v[96:99], v[136:139]\n" \
"ds_read_b128 a[152:155], v48 offset:6144\n" \
"ds_read_b128 a[156:159], v48 offset:7168\n" \
"v_mfma_f32_16x16x32_bf16 v[136:139], a[184:187], v[100:103], v[136:139]\n" \
"v_exp_f32_e32 v82, v82\n" \
"v_mfma_f32_16x16x32_bf16 v[140:143], a[188:191], v[100:103], v[140:143]\n" \
"v_exp_f32_e32 v83, v83\n" \
"v_mfma_f32_16x16x32_bf16 v[140:143], a[172:175], v[96:99], v[140:143]\n" \
"v_exp_f32_e32 v84, v84\n" \
"v_smfmac_f32_16x16x64_bf16 a[192:195], a[212:215], v[96:103], v21\n" \
"v_mfma_f32_16x16x32_bf16 v[160:163], a[160:163], v[104:107], v[160:163]\n" \
"v_exp_f32_e32 v85, v85\n" \
"v_mfma_f32_16x16x32_bf16 v[160:163], a[176:179], v[108:111], v[160:163]\n" \
"v_exp_f32_e32 v86, v86\n" \
"v_mfma_f32_16x16x32_bf16 v[164:167], a[180:183], v[108:111], v[164:167]\n" \
"v_exp_f32_e32 v87, v87\n" \
"v_mfma_f32_16x16x32_bf16 v[164:167], a[164:167], v[104:107], v[164:167]\n" \
"v_cvt_pk_bf16_f32 v116, v80, v81\n" \
"v_cvt_pk_bf16_f32 v117, v82, v83\n" \
"v_mfma_f32_16x16x32_bf16 v[168:171], a[168:171], v[104:107], v[168:171]\n" \
"v_cvt_pk_bf16_f32 v118, v84, v85\n" \
"v_cvt_pk_bf16_f32 v119, v86, v87\n" \
"v_mfma_f32_16x16x32_bf16 v[168:171], a[184:187], v[108:111], v[168:171]\n" \
"v_exp_f32_e32 v88, v88\n" \
"v_mfma_f32_16x16x32_bf16 v[172:175], a[188:191], v[108:111], v[172:175]\n" \
"v_exp_f32_e32 v89, v89\n" \
"v_mfma_f32_16x16x32_bf16 v[172:175], a[172:175], v[104:107], v[172:175]\n" \
"v_exp_f32_e32 v90, v90\n" \
"v_smfmac_f32_16x16x64_bf16 a[196:199], a[212:215], v[104:111], v21\n" \
"v_mfma_f32_16x16x32_bf16 v[192:195], a[160:163], v[112:115], v[192:195]\n" \
"v_exp_f32_e32 v91, v91\n" \
"v_mfma_f32_16x16x32_bf16 v[192:195], a[176:179], v[116:119], v[192:195]\n" \
"v_exp_f32_e32 v92, v92\n" \
"v_mfma_f32_16x16x32_bf16 v[196:199], a[180:183], v[116:119], v[196:199]\n" \
"v_exp_f32_e32 v93, v93\n" \
"v_mfma_f32_16x16x32_bf16 v[196:199], a[164:167], v[112:115], v[196:199]\n" \
"v_exp_f32_e32 v94, v94\n" \
"v_mfma_f32_16x16x32_bf16 v[200:203], a[168:171], v[112:115], v[200:203]\n" \
"v_exp_f32_e32 v95, v95\n" \
"v_mfma_f32_16x16x32_bf16 v[200:203], a[184:187], v[116:119], v[200:203]\n" \
"v_cvt_pk_bf16_f32 v124, v88, v89\n" \
"v_cvt_pk_bf16_f32 v125, v90, v91\n" \
"v_mfma_f32_16x16x32_bf16 v[204:207], a[188:191], v[116:119], v[204:207]\n" \
"v_cvt_pk_bf16_f32 v126, v92, v93\n" \
"v_cvt_pk_bf16_f32 v127, v94, v95\n" \
"v_mfma_f32_16x16x32_bf16 v[204:207], a[172:175], v[112:115], v[204:207]\n" \
"v_smfmac_f32_16x16x64_bf16 a[200:203], a[212:215], v[112:119], v21\n" \
"s_waitcnt vmcnt(0) lgkmcnt(0)\n" \
"s_barrier\n" \
"v_mfma_f32_16x16x32_bf16 v[224:227], a[160:163], v[120:123], v[224:227]\n" \
"s_cbranch_scc1 L_h1_%=\n" \
"L_hr1_%=:\n" \
"v_mfma_f32_16x16x32_bf16 v[224:227], a[176:179], v[124:127], v[224:227]\n" \
"s_xor_b32 m0, s65, 0x12000\n" \
"ds_read_b128 a[160:163], v49 offset:8192\n" \
"v_mfma_f32_16x16x32_bf16 v[228:231], a[180:183], v[124:127], v[228:231]\n" \
"buffer_load_dwordx4 v50, s[8:11], s64 offen offset:0 sc1 lds\n" \
"v_mfma_f32_16x16x32_bf16 v[228:231], a[164:167], v[120:123], v[228:231]\n" \
"ds_read_b128 a[176:179], v49 offset:12288\n" \
"ds_read_b128 a[180:183], v49 offset:13312\n" \
"v_mfma_f32_16x16x32_bf16 v[232:235], a[168:171], v[120:123], v[232:235]\n" \
"v_mfma_f32_16x16x32_bf16 v[232:235], a[184:187], v[124:127], v[232:235]\n" \
"ds_read_b128 a[164:167], v49 offset:9216\n" \
"ds_read_b128 a[168:171], v49 offset:10240\n" \
"v_mfma_f32_16x16x32_bf16 v[236:239], a[188:191], v[124:127], v[236:239]\n" \
"v_mfma_f32_16x16x32_bf16 v[236:239], a[172:175], v[120:123], v[236:239]\n" \
"ds_read_b128 a[184:187], v49 offset:14336\n" \
"ds_read_b128 a[188:191], v49 offset:15360\n" \
"v_mfma_f32_16x16x32_bf16 v[64:67], a[128:131], a[0:3], v[32:35]\n" \
"v_mfma_f32_16x16x32_bf16 v[64:67], a[132:135], a[4:7], v[64:67]\n" \
"buffer_load_dwordx4 v50, s[8:11], s64 offen offset:1024 sc1 lds\n" \
"v_mfma_f32_16x16x32_bf16 v[64:67], a[136:139], a[8:11], v[64:67]\n" \
"s_xor_b32 m0, s65, 0x6000\n" \
"ds_read_b128 a[172:175], v49 offset:11264\n" \
"v_mfma_f32_16x16x32_bf16 v[64:67], a[140:143], a[12:15], v[64:67]\n" \
"v_add_u32_e32 v49, 0x10000, v48\n" \
"v_mfma_f32_16x16x32_bf16 v[68:71], a[156:159], a[12:15], v[32:35]\n" \
"v_mfma_f32_16x16x32_bf16 v[68:71], a[152:155], a[8:11], v[68:71]\n" \
"v_mfma_f32_16x16x32_bf16 v[68:71], a[148:151], a[4:7], v[68:71]\n" \
"v_mfma_f32_16x16x32_bf16 v[68:71], a[144:147], a[0:3], v[68:71]\n" \
"v_mfma_f32_16x16x32_bf16 v[72:75], a[128:131], a[16:19], v[36:39]\n" \
"v_mfma_f32_16x16x32_bf16 v[72:75], a[132:135], a[20:23], v[72:75]\n" \
"v_mfma_f32_16x16x32_bf16 v[72:75], a[136:139], a[24:27], v[72:75]\n" \
"buffer_load_dwordx4 v51, s[8:11], s64 offen offset:0 sc1 lds\n" \
"v_mfma_f32_16x16x32_bf16 v[72:75], a[140:143], a[28:31], v[72:75]\n" \
"v_exp_f32_e32 v64, v64\n" \
"v_mfma_f32_16x16x32_bf16 v[76:79], a[156:159], a[28:31], v[36:39]\n" \
"v_exp_f32_e32 v65, v65\n" \
"v_mfma_f32_16x16x32_bf16 v[76:79], a[152:155], a[24:27], v[76:79]\n" \
"v_exp_f32_e32 v66, v66\n" \
"v_mfma_f32_16x16x32_bf16 v[76:79], a[148:151], a[20:23], v[76:79]\n" \
"v_exp_f32_e32 v67, v67\n" \
"v_mfma_f32_16x16x32_bf16 v[76:79], a[144:147], a[16:19], v[76:79]\n" \
"v_exp_f32_e32 v68, v68\n" \
"v_mfma_f32_16x16x32_bf16 v[80:83], a[128:131], a[32:35], v[40:43]\n" \
"v_exp_f32_e32 v69, v69\n" \
"v_mfma_f32_16x16x32_bf16 v[80:83], a[132:135], a[36:39], v[80:83]\n" \
"v_exp_f32_e32 v70, v70\n" \
"v_mfma_f32_16x16x32_bf16 v[80:83], a[136:139], a[40:43], v[80:83]\n" \
"v_exp_f32_e32 v71, v71\n" \
"v_mfma_f32_16x16x32_bf16 v[80:83], a[140:143], a[44:47], v[80:83]\n" \
"buffer_load_dwordx4 v51, s[8:11], s64 offen offset:1024 sc1 lds\n" \
"v_mfma_f32_16x16x32_bf16 v[84:87], a[156:159], a[44:47], v[40:43]\n" \
"v_exp_f32_e32 v72, v72\n" \
"v_mfma_f32_16x16x32_bf16 v[84:87], a[152:155], a[40:43], v[84:87]\n" \
"v_exp_f32_e32 v73, v73\n" \
"v_mfma_f32_16x16x32_bf16 v[84:87], a[148:151], a[36:39], v[84:87]\n" \
"v_exp_f32_e32 v74, v74\n" \
"v_mfma_f32_16x16x32_bf16 v[84:87], a[144:147], a[32:35], v[84:87]\n" \
"v_exp_f32_e32 v75, v75\n" \
"v_mfma_f32_16x16x32_bf16 v[88:91], a[128:131], a[48:51], v[44:47]\n" \
"s_xor_b32 m0, s65, 0x14000\n" \
"ds_read_b128 a[128:131], v48 offset:8192\n" \
"v_mfma_f32_16x16x32_bf16 v[88:91], a[132:135], a[52:55], v[88:91]\n" \
"v_exp_f32_e32 v76, v76\n" \
"v_mfma_f32_16x16x32_bf16 v[88:91], a[136:139], a[56:59], v[88:91]\n" \
"ds_read_b128 a[132:135], v48 offset:9216\n" \
"ds_read_b128 a[136:139], v48 offset:10240\n" \
"v_mfma_f32_16x16x32_bf16 v[88:91], a[140:143], a[60:63], v[88:91]\n" \
"v_exp_f32_e32 v77, v77\n" \
"v_mfma_f32_16x16x32_bf16 v[92:95], a[156:159], a[60:63], v[44:47]\n" \
"buffer_load_dwordx4 v52, s[8:11], s64 offen offset:0 sc1 lds\n" \
"v_mfma_f32_16x16x32_bf16 v[92:95], a[152:155], a[56:59], v[92:95]\n" \
"ds_read_b128 a[140:143], v48 offset:11264\n" \
"ds_read_b128 a[152:155], v48 offset:14336\n" \
"v_mfma_f32_16x16x32_bf16 v[92:95], a[148:151], a[52:55], v[92:95]\n" \
"v_exp_f32_e32 v78, v78\n" \
"v_mfma_f32_16x16x32_bf16 v[92:95], a[144:147], a[48:51], v[92:95]\n" \
"ds_read_b128 a[144:147], v48 offset:12288\n" \
"s_waitcnt lgkmcnt(6)\n" \
"v_mfma_f32_16x16x32_bf16 v[144:147], a[160:163], v[96:99], v[144:147]\n" \
"ds_read_b128 a[148:151], v48 offset:13312\n" \
"ds_read_b128 a[156:159], v48 offset:15360\n" \
"v_mfma_f32_16x16x32_bf16 v[144:147], a[176:179], v[100:103], v[144:147]\n" \
"v_exp_f32_e32 v79, v79\n" \
"v_mfma_f32_16x16x32_bf16 v[148:151], a[180:183], v[100:103], v[148:151]\n" \
"v_exp_f32_e32 v80, v80\n" \
"v_mfma_f32_16x16x32_bf16 v[148:151], a[164:167], v[96:99], v[148:151]\n" \
"v_exp_f32_e32 v81, v81\n" \
"v_mfma_f32_16x16x32_bf16 v[152:155], a[168:171], v[96:99], v[152:155]\n" \
"buffer_load_dwordx4 v52, s[8:11], s64 offen offset:1024 sc1 lds\n" \
"v_mfma_f32_16x16x32_bf16 v[152:155], a[184:187], v[100:103], v[152:155]\n" \
"s_mov_b32 m0, s65\n" \
"s_xor_b32 s65, s65, 0x4000\n" \
"v_mfma_f32_16x16x32_bf16 v[156:159], a[188:191], v[100:103], v[156:159]\n" \
"v_exp_f32_e32 v82, v82\n" \
"v_mfma_f32_16x16x32_bf16 v[156:159], a[172:175], v[96:99], v[156:159]\n" \
"v_exp_f32_e32 v83, v83\n" \
"v_mfma_f32_16x16x32_bf16 v[176:179], a[160:163], v[104:107], v[176:179]\n" \
"v_exp_f32_e32 v84, v84\n" \
"v_mfma_f32_16x16x32_bf16 v[176:179], a[176:179], v[108:111], v[176:179]\n" \
"v_exp_f32_e32 v85, v85\n" \
"v_mfma_f32_16x16x32_bf16 v[180:183], a[180:183], v[108:111], v[180:183]\n" \
"v_exp_f32_e32 v86, v86\n" \
"v_mfma_f32_16x16x32_bf16 v[180:183], a[164:167], v[104:107], v[180:183]\n" \
"v_exp_f32_e32 v87, v87\n" \
"v_mfma_f32_16x16x32_bf16 v[184:187], a[168:171], v[104:107], v[184:187]\n" \
"v_exp_f32_e32 v88, v88\n" \
"v_mfma_f32_16x16x32_bf16 v[184:187], a[184:187], v[108:111], v[184:187]\n" \
"buffer_load_dwordx4 v53, s[8:11], s64 offen offset:0 sc1 lds\n" \
"v_mfma_f32_16x16x32_bf16 v[188:191], a[188:191], v[108:111], v[188:191]\n" \
"v_exp_f32_e32 v89, v89\n" \
"v_mfma_f32_16x16x32_bf16 v[188:191], a[172:175], v[104:107], v[188:191]\n" \
"v_exp_f32_e32 v90, v90\n" \
"v_mfma_f32_16x16x32_bf16 v[208:211], a[160:163], v[112:115], v[208:211]\n" \
"v_exp_f32_e32 v91, v91\n" \
"v_mfma_f32_16x16x32_bf16 v[208:211], a[176:179], v[116:119], v[208:211]\n" \
"v_cvt_pk_bf16_f32 v96, v64, v65\n" \
"v_cvt_pk_bf16_f32 v97, v66, v67\n" \
"v_mfma_f32_16x16x32_bf16 v[212:215], a[180:183], v[116:119], v[212:215]\n" \
"v_cvt_pk_bf16_f32 v98, v68, v69\n" \
"v_cvt_pk_bf16_f32 v99, v70, v71\n" \
"v_mfma_f32_16x16x32_bf16 v[212:215], a[164:167], v[112:115], v[212:215]\n" \
"v_cvt_pk_bf16_f32 v104, v72, v73\n" \
"v_cvt_pk_bf16_f32 v105, v74, v75\n" \
"v_mfma_f32_16x16x32_bf16 v[216:219], a[168:171], v[112:115], v[216:219]\n" \
"v_cvt_pk_bf16_f32 v106, v76, v77\n" \
"v_cvt_pk_bf16_f32 v107, v78, v79\n" \
"v_mfma_f32_16x16x32_bf16 v[216:219], a[184:187], v[116:119], v[216:219]\n" \
"v_xor_b32_e32 v48, 0x4000, v48\n" \
"v_mfma_f32_16x16x32_bf16 v[220:223], a[188:191], v[116:119], v[220:223]\n" \
"buffer_load_dwordx4 v53, s[8:11], s64 offen offset:1024 sc1 lds\n" \
"v_mfma_f32_16x16x32_bf16 v[220:223], a[172:175], v[112:115], v[220:223]\n" \
"s_add_u32 s64, s64, 0x8000\n" \
"s_sub_u32 s66, s66, 1\n" \
"v_mfma_f32_16x16x32_bf16 v[240:243], a[160:163], v[120:123], v[240:243]\n" \
"v_cvt_pk_bf16_f32 v112, v80, v81\n" \
"v_cvt_pk_bf16_f32 v113, v82, v83\n" \
"v_mfma_f32_16x16x32_bf16 v[240:243], a[176:179], v[124:127], v[240:243]\n" \
"v_cvt_pk_bf16_f32 v114, v84, v85\n" \
"v_cvt_pk_bf16_f32 v115, v86, v87\n" \
"v_mfma_f32_16x16x32_bf16 v[244:247], a[180:183], v[124:127], v[244:247]\n" \
"v_mfma_f32_16x16x32_bf16 v[244:247], a[164:167], v[120:123], v[244:247]\n" \
"v_mfma_f32_16x16x32_bf16 v[248:251], a[168:171], v[120:123], v[248:251]\n" \
"v_mfma_f32_16x16x32_bf16 v[248:251], a[184:187], v[124:127], v[248:251]\n" \
"s_cbranch_scc0 L_t1_%=\n" \
"v_mfma_f32_16x16x32_bf16 v[252:255], a[188:191], v[124:127], v[252:255]\n" \
"v_mfma_f32_16x16x32_bf16 v[252:255], a[172:175], v[120:123], v[252:255]\n" \
"v_smfmac_f32_16x16x64_bf16 a[204:207], a[212:215], v[120:127], v21\n" \
"s_waitcnt lgkmcnt(0)\n" \
"s_barrier\n" \
"L_tr_%=:\n" \
"L_pf11:\n" \
"s_cbranch_execz L_pf15\n" \
"s_nop 0\n" \
"s_cmp_lg_u32 s16, 0\n" \
"s_cbranch_scc1 L_sw_%=\n" \
"v_mfma_f32_16x16x32_bf16 v[64:67], a[128:131], a[0:3], v[32:35]\n" \
"s_xor_b32 m0, s65, 0x12000\n" \
"ds_read_b128 a[160:163], v49 offset:0\n" \
"v_mfma_f32_16x16x32_bf16 v[64:67], a[132:135], a[4:7], v[64:67]\n" \
"buffer_load_dwordx4 v50, s[8:11], s64 offen offset:0 sc1 lds\n" \
"v_mfma_f32_16x16x32_bf16 v[64:67], a[136:139], a[8:11], v[64:67]\n" \
"ds_read_b128 a[176:179], v49 offset:4096\n" \
"ds_read_b128 a[180:183], v49 offset:5120\n" \
"v_mfma_f32_16x16x32_bf16 v[64:67], a[140:143], a[12:15], v[64:67]\n" \
"s_mov_b32 s65, s13\n" \
"v_add_u32_e32 v63, s15, v20\n" \
"v_mfma_f32_16x16x32_bf16 v[68:71], a[156:159], a[12:15], v[32:35]\n" \
"s_add_u32 s67, s14, 40960\n" \
"ds_read_b128 a[164:167], v49 offset:1024\n" \
"v_mfma_f32_16x16x32_bf16 v[68:71], a[152:155], a[8:11], v[68:71]\n" \
"ds_read_b128 a[168:171], v49 offset:2048\n" \
"ds_read_b128 a[184:187], v49 offset:6144\n" \
"v_mfma_f32_16x16x32_bf16 v[68:71], a[148:151], a[4:7], v[68:71]\n" \
"buffer_load_dwordx4 v50, s[8:11], s64 offen offset:1024 sc1 lds\n" \
"v_mfma_f32_16x16x32_bf16 v[68:71], a[144:147], a[0:3], v[68:71]\n" \
"v_add_u32_e32 v50, 0x6000, v63\n" \
"s_xor_b32 m0, s65, 0x8000\n" \
"v_mfma_f32_16x16x32_bf16 v[72:75], a[128:131], a[16:19], v[36:39]\n" \
"v_cvt_pk_bf16_f32 v120, v88, v89\n" \
"v_cvt_pk_bf16_f32 v121, v90, v91\n" \
"v_mfma_f32_16x16x32_bf16 v[72:75], a[132:135], a[20:23], v[72:75]\n" \
"ds_read_b128 a[188:191], v49 offset:7168\n" \
"ds_read_b128 a[172:175], v49 offset:3072\n" \
"v_mfma_f32_16x16x32_bf16 v[72:75], a[136:139], a[24:27], v[72:75]\n" \
"v_exp_f32_e32 v92, v92\n" \
"v_mfma_f32_16x16x32_bf16 v[72:75], a[140:143], a[28:31], v[72:75]\n" \
"v_exp_f32_e32 v93, v93\n" \
"v_mfma_f32_16x16x32_bf16 v[76:79], a[156:159], a[28:31], v[36:39]\n" \
"buffer_load_dwordx4 v50, s[8:11], s67 offen offset:0 sc1 lds\n" \
"v_mfma_f32_16x16x32_bf16 v[76:79], a[152:155], a[24:27], v[76:79]\n" \
"v_exp_f32_e32 v94, v94\n" \
"v_mfma_f32_16x16x32_bf16 v[76:79], a[148:151], a[20:23], v[76:79]\n" \
"v_exp_f32_e32 v95, v95\n" \
"v_mfma_f32_16x16x32_bf16 v[76:79], a[144:147], a[16:19], v[76:79]\n" \
"v_cvt_pk_bf16_f32 v122, v92, v93\n" \
"v_cvt_pk_bf16_f32 v123, v94, v95\n" \
"v_mfma_f32_16x16x32_bf16 v[80:83], a[128:131], a[32:35], v[40:43]\n" \
"v_exp_f32_e32 v64, v64\n" \
"v_mfma_f32_16x16x32_bf16 v[80:83], a[132:135], a[36:39], v[80:83]\n" \
"v_exp_f32_e32 v65, v65\n" \
"v_mfma_f32_16x16x32_bf16 v[80:83], a[136:139], a[40:43], v[80:83]\n" \
"buffer_load_dwordx4 v50, s[8:11], s67 offen offset:1024 sc1 lds\n" \
"v_mfma_f32_16x16x32_bf16 v[80:83], a[140:143], a[44:47], v[80:83]\n" \
"v_exp_f32_e32 v66, v66\n" \
"v_mfma_f32_16x16x32_bf16 v[84:87], a[156:159], a[44:47], v[40:43]\n" \
"v_exp_f32_e32 v67, v67\n" \
"v_mfma_f32_16x16x32_bf16 v[84:87], a[152:155], a[40:43], v[84:87]\n" \
"v_exp_f32_e32 v68, v68\n" \
"v_mfma_f32_16x16x32_bf16 v[84:87], a[148:151], a[36:39], v[84:87]\n" \
"v_exp_f32_e32 v69, v69\n" \
"v_mfma_f32_16x16x32_bf16 v[84:87], a[144:147], a[32:35], v[84:87]\n" \
"v_exp_f32_e32 v70, v70\n" \
"v_mfma_f32_16x16x32_bf16 v[88:91], a[128:131], a[48:51], v[44:47]\n" \
"v_exp_f32_e32 v71, v71\n" \
"v_mfma_f32_16x16x32_bf16 v[88:91], a[132:135], a[52:55], v[88:91]\n" \
"v_cvt_pk_bf16_f32 v100, v64, v65\n" \
"v_cvt_pk_bf16_f32 v101, v66, v67\n" \
"v_mfma_f32_16x16x32_bf16 v[88:91], a[136:139], a[56:59], v[88:91]\n" \
"v_cvt_pk_bf16_f32 v102, v68, v69\n" \
"v_cvt_pk_bf16_f32 v103, v70, v71\n" \
"v_mfma_f32_16x16x32_bf16 v[88:91], a[140:143], a[60:63], v[88:91]\n" \
"buffer_load_dwordx4 v50, s[8:11], s67 offen offset:2048 sc1 lds\n" \
"v_mfma_f32_16x16x32_bf16 v[92:95], a[156:159], a[60:63], v[44:47]\n" \
"v_exp_f32_e32 v72, v72\n" \
"v_mfma_f32_16x16x32_bf16 v[92:95], a[152:155], a[56:59], v[92:95]\n" \
"v_exp_f32_e32 v73, v73\n" \
"v_mfma_f32_16x16x32_bf16 v[92:95], a[148:151], a[52:55], v[92:95]\n" \
"v_exp_f32_e32 v74, v74\n" \
"v_mfma_f32_16x16x32_bf16 v[92:95], a[144:147], a[48:51], v[92:95]\n" \
"v_cvt_pk_bf16_f32 v108, v72, v73\n" \
"s_waitcnt lgkmcnt(0)\n" \
"v_mfma_f32_16x16x32_bf16 v[128:131], a[160:163], v[96:99], v[128:131]\n" \
"buffer_load_dwordx4 v50, s[8:11], s67 offen offset:3072 sc1 lds\n" \
"v_mfma_f32_16x16x32_bf16 v[128:131], a[176:179], v[100:103], v[128:131]\n" \
"v_exp_f32_e32 v75, v75\n" \
"v_mfma_f32_16x16x32_bf16 v[132:135], a[180:183], v[100:103], v[132:135]\n" \
"v_exp_f32_e32 v76, v76\n" \
"v_mfma_f32_16x16x32_bf16 v[132:135], a[164:167], v[96:99], v[132:135]\n" \
"v_exp_f32_e32 v77, v77\n" \
"v_mfma_f32_16x16x32_bf16 v[136:139], a[168:171], v[96:99], v[136:139]\n" \
"v_exp_f32_e32 v78, v78\n" \
"v_mfma_f32_16x16x32_bf16 v[136:139], a[184:187], v[100:103], v[136:139]\n" \
"v_exp_f32_e32 v79, v79\n" \
"v_mfma_f32_16x16x32_bf16 v[140:143], a[188:191], v[100:103], v[140:143]\n" \
"v_cvt_pk_bf16_f32 v109, v74, v75\n" \
"v_cvt_pk_bf16_f32 v110, v76, v77\n" \
"v_mfma_f32_16x16x32_bf16 v[140:143], a[172:175], v[96:99], v[140:143]\n" \
"v_cvt_pk_bf16_f32 v111, v78, v79\n" \
"s_mov_b32 s64, s14\n" \
"v_smfmac_f32_16x16x64_bf16 a[192:195], a[212:215], v[96:103], v21\n" \
"v_mfma_f32_16x16x32_bf16 v[160:163], a[160:163], v[104:107], v[160:163]\n" \
"v_add_u32_e32 v51, 0x12000, v63\n" \
"s_xor_b32 m0, s65, 0xa000\n" \
"v_mfma_f32_16x16x32_bf16 v[160:163], a[176:179], v[108:111], v[160:163]\n" \
"buffer_load_dwordx4 v51, s[8:11], s64 offen offset:0 sc1 lds\n" \
"v_mfma_f32_16x16x32_bf16 v[164:167], a[180:183], v[108:111], v[164:167]\n" \
"v_exp_f32_e32 v80, v80\n" \
"v_mfma_f32_16x16x32_bf16 v[164:167], a[164:167], v[104:107], v[164:167]\n" \
"v_exp_f32_e32 v81, v81\n" \
"v_mfma_f32_16x16x32_bf16 v[168:171], a[168:171], v[104:107], v[168:171]\n" \
"v_exp_f32_e32 v82, v82\n" \
"v_mfma_f32_16x16x32_bf16 v[168:171], a[184:187], v[108:111], v[168:171]\n" \
"v_exp_f32_e32 v83, v83\n" \
"v_mfma_f32_16x16x32_bf16 v[172:175], a[188:191], v[108:111], v[172:175]\n" \
"v_exp_f32_e32 v84, v84\n" \
"v_mfma_f32_16x16x32_bf16 v[172:175], a[172:175], v[104:107], v[172:175]\n" \
"v_exp_f32_e32 v85, v85\n" \
"v_smfmac_f32_16x16x64_bf16 a[196:199], a[212:215], v[104:111], v21\n" \
"v_mfma_f32_16x16x32_bf16 v[192:195], a[160:163], v[112:115], v[192:195]\n" \
"v_exp_f32_e32 v86, v86\n" \
"v_exp_f32_e32 v87, v87\n" \
"v_cvt_pk_bf16_f32 v116, v80, v81\n" \
"v_cvt_pk_bf16_f32 v117, v82, v83\n" \
"v_cvt_pk_bf16_f32 v118, v84, v85\n" \
"v_cvt_pk_bf16_f32 v119, v86, v87\n" \
"v_exp_f32_e32 v88, v88\n" \
"v_exp_f32_e32 v89, v89\n" \
"v_mfma_f32_16x16x32_bf16 v[192:195], a[176:179], v[116:119], v[192:195]\n" \
"buffer_load_dwordx4 v51, s[8:11], s64 offen offset:1024 sc1 lds\n" \
"v_mfma_f32_16x16x32_bf16 v[196:199], a[180:183], v[116:119], v[196:199]\n" \
"ds_read_b128 a[128:131], v48 offset:0\n" \
"ds_read_b128 a[132:135], v48 offset:1024\n" \
"v_mfma_f32_16x16x32_bf16 v[196:199], a[164:167], v[112:115], v[196:199]\n" \
"ds_read_b128 a[136:139], v48 offset:2048\n" \
"ds_read_b128 a[140:143], v48 offset:3072\n" \
"ds_read_b128 a[144:147], v48 offset:4096\n" \
"ds_read_b128 a[148:151], v48 offset:5120\n" \
"ds_read_b128 a[152:155], v48 offset:6144\n" \
"ds_read_b128 a[156:159], v48 offset:7168\n" \
"v_mfma_f32_16x16x32_bf16 v[200:203], a[168:171], v[112:115], v[200:203]\n" \
"v_exp_f32_e32 v90, v90\n" \
"v_mfma_f32_16x16x32_bf16 v[200:203], a[184:187], v[116:119], v[200:203]\n" \
"v_exp_f32_e32 v91, v91\n" \
"v_mfma_f32_16x16x32_bf16 v[204:207], a[188:191], v[116:119], v[204:207]\n" \
"v_exp_f32_e32 v92, v92\n" \
"v_mfma_f32_16x16x32_bf16 v[204:207], a[172:175], v[112:115], v[204:207]\n" \
"v_exp_f32_e32 v93, v93\n" \
"v_smfmac_f32_16x16x64_bf16 a[200:203], a[212:215], v[112:119], v21\n" \
"v_mfma_f32_16x16x32_bf16 v[224:227], a[160:163], v[120:123], v[224:227]\n" \
"v_exp_f32_e32 v94, v94\n" \
"v_exp_f32_e32 v95, v95\n" \
"v_cvt_pk_bf16_f32 v124, v88, v89\n" \
"v_cvt_pk_bf16_f32 v125, v90, v91\n" \
"v_cvt_pk_bf16_f32 v126, v92, v93\n" \
"v_cvt_pk_bf16_f32 v127, v94, v95\n" \
"v_add_u32_e32 v52, 0x14000, v63\n" \
"v_add_u32_e32 v53, 0x20000, v63\n" \
"v_mfma_f32_16x16x32_bf16 v[224:227], a[176:179], v[124:127], v[224:227]\n" \
"buffer_load_dwordx4 v51, s[8:11], s64 offen offset:2048 sc1 lds\n" \
"v_mfma_f32_16x16x32_bf16 v[228:231], a[180:183], v[124:127], v[228:231]\n" \
"buffer_load_dwordx4 v51, s[8:11], s64 offen offset:3072 sc1 lds\n" \
"v_mfma_f32_16x16x32_bf16 v[228:231], a[164:167], v[120:123], v[228:231]\n" \
"v_readlane_b32 s93, v11, 17\n" \
"v_readlane_b32 s92, v11, 22\n" \
"v_mfma_f32_16x16x32_bf16 v[232:235], a[168:171], v[120:123], v[232:235]\n" \
"s_mov_b32 s66, s19\n" \
"v_mfma_f32_16x16x32_bf16 v[232:235], a[184:187], v[124:127], v[232:235]\n" \
"v_mfma_f32_16x16x32_bf16 v[236:239], a[188:191], v[124:127], v[236:239]\n" \
"v_mfma_f32_16x16x32_bf16 v[236:239], a[172:175], v[120:123], v[236:239]\n" \
"s_waitcnt vmcnt(10) lgkmcnt(0)\n" \
"s_barrier\n" \
"v_mfma_f32_16x16x32_bf16 v[64:67], a[128:131], a[0:3], v[32:35]\n" \
"s_xor_b32 m0, s65, 0x18000\n" \
"ds_read_b128 a[160:163], v49 offset:8192\n" \
"v_mfma_f32_16x16x32_bf16 v[64:67], a[132:135], a[4:7], v[64:67]\n" \
"buffer_load_dwordx4 v52, s[8:11], s64 offen offset:0 sc1 lds\n" \
"v_mfma_f32_16x16x32_bf16 v[64:67], a[136:139], a[8:11], v[64:67]\n" \
"ds_read_b128 a[176:179], v49 offset:12288\n" \
"ds_read_b128 a[180:183], v49 offset:13312\n" \
"v_mfma_f32_16x16x32_bf16 v[64:67], a[140:143], a[12:15], v[64:67]\n" \
"buffer_load_dwordx4 v52, s[8:11], s64 offen offset:1024 sc1 lds\n" \
"v_mfma_f32_16x16x32_bf16 v[68:71], a[156:159], a[12:15], v[32:35]\n" \
"ds_read_b128 a[164:167], v49 offset:9216\n" \
"ds_read_b128 a[168:171], v49 offset:10240\n" \
"v_mfma_f32_16x16x32_bf16 v[68:71], a[152:155], a[8:11], v[68:71]\n" \
"ds_read_b128 a[184:187], v49 offset:14336\n" \
"ds_read_b128 a[188:191], v49 offset:15360\n" \
"v_mfma_f32_16x16x32_bf16 v[68:71], a[148:151], a[4:7], v[68:71]\n" \
"v_exp_f32_e32 v64, v64\n" \
"v_mfma_f32_16x16x32_bf16 v[68:71], a[144:147], a[0:3], v[68:71]\n" \
"buffer_load_dwordx4 v52, s[8:11], s64 offen offset:2048 sc1 lds\n" \
"v_mfma_f32_16x16x32_bf16 v[72:75], a[128:131], a[16:19], v[36:39]\n" \
"v_exp_f32_e32 v65, v65\n" \
"v_mfma_f32_16x16x32_bf16 v[72:75], a[132:135], a[20:23], v[72:75]\n" \
"v_exp_f32_e32 v66, v66\n" \
"v_mfma_f32_16x16x32_bf16 v[72:75], a[136:139], a[24:27], v[72:75]\n" \
"buffer_load_dwordx4 v52, s[8:11], s64 offen offset:3072 sc1 lds\n" \
"v_mfma_f32_16x16x32_bf16 v[72:75], a[140:143], a[28:31], v[72:75]\n" \
"s_mov_b32 m0, s65\n" \
"ds_read_b128 a[172:175], v49 offset:11264\n" \
"v_mfma_f32_16x16x32_bf16 v[76:79], a[156:159], a[28:31], v[36:39]\n" \
"v_exp_f32_e32 v67, v67\n" \
"v_mfma_f32_16x16x32_bf16 v[76:79], a[152:155], a[24:27], v[76:79]\n" \
"v_exp_f32_e32 v68, v68\n" \
"v_mfma_f32_16x16x32_bf16 v[76:79], a[148:151], a[20:23], v[76:79]\n" \
"buffer_load_dwordx4 v53, s[8:11], s64 offen offset:0 sc1 lds\n" \
"v_mfma_f32_16x16x32_bf16 v[76:79], a[144:147], a[16:19], v[76:79]\n" \
"v_exp_f32_e32 v69, v69\n" \
"v_mfma_f32_16x16x32_bf16 v[80:83], a[128:131], a[32:35], v[40:43]\n" \
"v_exp_f32_e32 v70, v70\n" \
"v_mfma_f32_16x16x32_bf16 v[80:83], a[132:135], a[36:39], v[80:83]\n" \
"buffer_load_dwordx4 v53, s[8:11], s64 offen offset:1024 sc1 lds\n" \
"v_mfma_f32_16x16x32_bf16 v[80:83], a[136:139], a[40:43], v[80:83]\n" \
"v_exp_f32_e32 v71, v71\n" \
"v_mfma_f32_16x16x32_bf16 v[80:83], a[140:143], a[44:47], v[80:83]\n" \
"v_exp_f32_e32 v72, v72\n" \
"v_mfma_f32_16x16x32_bf16 v[84:87], a[156:159], a[44:47], v[40:43]\n" \
"v_exp_f32_e32 v73, v73\n" \
"v_mfma_f32_16x16x32_bf16 v[84:87], a[152:155], a[40:43], v[84:87]\n" \
"L_pf12:\n" \
"s_cbranch_execz L_pf16\n" \
"s_nop 0\n" \
"buffer_load_dwordx4 v53, s[8:11], s64 offen offset:2048 sc1 lds\n" \
"v_mfma_f32_16x16x32_bf16 v[84:87], a[148:151], a[36:39], v[84:87]\n" \
"v_exp_f32_e32 v74, v74\n" \
"v_mfma_f32_16x16x32_bf16 v[84:87], a[144:147], a[32:35], v[84:87]\n" \
"v_exp_f32_e32 v75, v75\n" \
"v_mfma_f32_16x16x32_bf16 v[88:91], a[128:131], a[48:51], v[44:47]\n" \
"buffer_load_dwordx4 v53, s[8:11], s64 offen offset:3072 sc1 lds\n" \
"v_mfma_f32_16x16x32_bf16 v[88:91], a[132:135], a[52:55], v[88:91]\n" \
"s_add_u32 m0, s93, 0x0\n" \
"ds_read_b128 a[128:131], v48 offset:8192\n" \
"v_mfma_f32_16x16x32_bf16 v[88:91], a[136:139], a[56:59], v[88:91]\n" \
"ds_read_b128 a[132:135], v48 offset:9216\n" \
"ds_read_b128 a[136:139], v48 offset:10240\n" \
"v_mfma_f32_16x16x32_bf16 v[88:91], a[140:143], a[60:63], v[88:91]\n" \
"ds_read_b128 a[140:143], v48 offset:11264\n" \
"v_add_u32_e32 v49, 0x10000, v48\n" \
"v_mfma_f32_16x16x32_bf16 v[92:95], a[156:159], a[60:63], v[44:47]\n" \
"buffer_load_dwordx4 v0, s[80:83], s92 offen offset:0 lds\n" \
"v_mfma_f32_16x16x32_bf16 v[92:95], a[152:155], a[56:59], v[92:95]\n" \
"ds_read_b128 a[152:155], v48 offset:14336\n" \
"ds_read_b128 a[156:159], v48 offset:15360\n" \
"v_mfma_f32_16x16x32_bf16 v[92:95], a[148:151], a[52:55], v[92:95]\n" \
"ds_read_b128 a[148:151], v48 offset:13312\n" \
"s_xor_b32 s65, s65, 0x8000\n" \
"v_mfma_f32_16x16x32_bf16 v[92:95], a[144:147], a[48:51], v[92:95]\n" \
"ds_read_b128 a[144:147], v48 offset:12288\n" \
"s_waitcnt lgkmcnt(8)\n" \
"v_mfma_f32_16x16x32_bf16 v[144:147], a[160:163], v[96:99], v[144:147]\n" \
"buffer_load_dwordx4 v1, s[80:83], s92 offen offset:1024 lds\n" \
"v_mfma_f32_16x16x32_bf16 v[144:147], a[176:179], v[100:103], v[144:147]\n" \
"v_exp_f32_e32 v76, v76\n" \
"v_mfma_f32_16x16x32_bf16 v[148:151], a[180:183], v[100:103], v[148:151]\n" \
"v_exp_f32_e32 v77, v77\n" \
"v_mfma_f32_16x16x32_bf16 v[148:151], a[164:167], v[96:99], v[148:151]\n" \
"buffer_load_dwordx4 v2, s[80:83], s92 offen offset:2048 lds\n" \
"v_mfma_f32_16x16x32_bf16 v[152:155], a[168:171], v[96:99], v[152:155]\n" \
"v_exp_f32_e32 v78, v78\n" \
"v_mfma_f32_16x16x32_bf16 v[152:155], a[184:187], v[100:103], v[152:155]\n" \
"v_exp_f32_e32 v79, v79\n" \
"v_mfma_f32_16x16x32_bf16 v[156:159], a[188:191], v[100:103], v[156:159]\n" \
"buffer_load_dwordx4 v3, s[80:83], s92 offen offset:3072 lds\n" \
"v_mfma_f32_16x16x32_bf16 v[156:159], a[172:175], v[96:99], v[156:159]\n" \
"s_add_u32 m0, s93, 0x1000\n" \
"v_cvt_pk_bf16_f32 v96, v64, v65\n" \
"v_mfma_f32_16x16x32_bf16 v[176:179], a[160:163], v[104:107], v[176:179]\n" \
"v_exp_f32_e32 v80, v80\n" \
"v_mfma_f32_16x16x32_bf16 v[176:179], a[176:179], v[108:111], v[176:179]\n" \
"v_exp_f32_e32 v81, v81\n" \
"v_mfma_f32_16x16x32_bf16 v[180:183], a[180:183], v[108:111], v[180:183]\n" \
"buffer_load_dwordx4 v4, s[80:83], s92 offen offset:0 lds\n" \
"v_mfma_f32_16x16x32_bf16 v[180:183], a[164:167], v[104:107], v[180:183]\n" \
"v_exp_f32_e32 v82, v82\n" \
"v_mfma_f32_16x16x32_bf16 v[184:187], a[168:171], v[104:107], v[184:187]\n" \
"v_exp_f32_e32 v83, v83\n" \
"v_mfma_f32_16x16x32_bf16 v[184:187], a[184:187], v[108:111], v[184:187]\n" \
"buffer_load_dwordx4 v5, s[80:83], s92 offen offset:1024 lds\n" \
"v_mfma_f32_16x16x32_bf16 v[188:191], a[188:191], v[108:111], v[188:191]\n" \
"v_exp_f32_e32 v84, v84\n" \
"v_mfma_f32_16x16x32_bf16 v[188:191], a[172:175], v[104:107], v[188:191]\n" \
"v_exp_f32_e32 v85, v85\n" \
"v_mfma_f32_16x16x32_bf16 v[208:211], a[160:163], v[112:115], v[208:211]\n" \
"v_exp_f32_e32 v86, v86\n" \
"v_mfma_f32_16x16x32_bf16 v[208:211], a[176:179], v[116:119], v[208:211]\n" \
"buffer_load_dwordx4 v6, s[80:83], s92 offen offset:2048 lds\n" \
"v_mfma_f32_16x16x32_bf16 v[212:215], a[180:183], v[116:119], v[212:215]\n" \
"v_exp_f32_e32 v87, v87\n" \
"v_mfma_f32_16x16x32_bf16 v[212:215], a[164:167], v[112:115], v[212:215]\n" \
"v_exp_f32_e32 v88, v88\n" \
"v_mfma_f32_16x16x32_bf16 v[216:219], a[168:171], v[112:115], v[216:219]\n" \
"buffer_load_dwordx4 v7, s[80:83], s92 offen offset:3072 lds\n" \
"v_mfma_f32_16x16x32_bf16 v[216:219], a[184:187], v[116:119], v[216:219]\n" \
"s_add_u32 m0, s93, 0x2000\n" \
"v_cvt_pk_bf16_f32 v97, v66, v67\n" \
"v_mfma_f32_16x16x32_bf16 v[220:223], a[188:191], v[116:119], v[220:223]\n" \
"v_exp_f32_e32 v89, v89\n" \
"v_mfma_f32_16x16x32_bf16 v[220:223], a[172:175], v[112:115], v[220:223]\n" \
"v_exp_f32_e32 v90, v90\n" \
"v_mfma_f32_16x16x32_bf16 v[240:243], a[160:163], v[120:123], v[240:243]\n" \
"buffer_load_dwordx4 v8, s[80:83], s92 offen offset:0 lds\n" \
"v_mfma_f32_16x16x32_bf16 v[240:243], a[176:179], v[124:127], v[240:243]\n" \
"v_exp_f32_e32 v91, v91\n" \
"v_mfma_f32_16x16x32_bf16 v[244:247], a[180:183], v[124:127], v[244:247]\n" \
"v_cvt_pk_bf16_f32 v98, v68, v69\n" \
"v_cvt_pk_bf16_f32 v99, v70, v71\n" \
"v_mfma_f32_16x16x32_bf16 v[244:247], a[164:167], v[120:123], v[244:247]\n" \
"v_cvt_pk_bf16_f32 v104, v72, v73\n" \
"v_cvt_pk_bf16_f32 v105, v74, v75\n" \
"v_mfma_f32_16x16x32_bf16 v[248:251], a[168:171], v[120:123], v[248:251]\n" \
"v_cvt_pk_bf16_f32 v106, v76, v77\n" \
"v_cvt_pk_bf16_f32 v107, v78, v79\n" \
"v_mfma_f32_16x16x32_bf16 v[248:251], a[184:187], v[124:127], v[248:251]\n" \
"v_cvt_pk_bf16_f32 v112, v80, v81\n" \
"v_cvt_pk_bf16_f32 v113, v82, v83\n" \
"v_mfma_f32_16x16x32_bf16 v[252:255], a[188:191], v[124:127], v[252:255]\n" \
"v_cvt_pk_bf16_f32 v114, v84, v85\n" \
"v_cvt_pk_bf16_f32 v115, v86, v87\n" \
"v_mfma_f32_16x16x32_bf16 v[252:255], a[172:175], v[120:123], v[252:255]\n" \
"v_add_u32_e32 v48, s16, v20\n" \
"s_add_u32 s64, s64, 0x10000\n" \
"v_smfmac_f32_16x16x64_bf16 a[204:207], a[212:215], v[120:127], v21\n" \
"s_waitcnt vmcnt(17) lgkmcnt(0)\n" \
"s_barrier\n" \
"s_mov_b64 exec, 0\n" \
"s_cmp_lg_u32 s15, 0\n" \
"s_cbranch_scc1 L_pf22\n" \
"s_branch L_pf21\n" \
"L_retB:\n" \
"s_mov_b64 exec, -1\n" \
"s_branch L_2_%=\n" \
"L_t2_%=:\n" \
"v_mfma_f32_16x16x32_bf16 v[252:255], a[188:191], v[124:127], v[252:255]\n" \
"v_cvt_pk_bf16_f32 v114, v84, v85\n" \
"v_cvt_pk_bf16_f32 v115, v86, v87\n" \
"v_mfma_f32_16x16x32_bf16 v[252:255], a[172:175], v[120:123], v[252:255]\n" \
"v_xor_b32_e32 v48, 0x8000, v48\n" \
"v_smfmac_f32_16x16x64_bf16 a[204:207], a[212:215], v[120:127], v21\n" \
"s_waitcnt lgkmcnt(0)\n" \
"L_2_%=:\n" \
"v_mfma_f32_16x16x32_bf16 v[64:67], a[128:131], a[64:67], v[32:35]\n" \
"ds_read_b128 a[160:163], v49 offset:0\n" \
"ds_read_b128 a[176:179], v49 offset:4096\n" \
"v_mfma_f32_16x16x32_bf16 v[64:67], a[132:135], a[68:71], v[64:67]\n" \
"v_cvt_pk_bf16_f32 v120, v88, v89\n" \
"v_cvt_pk_bf16_f32 v121, v90, v91\n" \
"v_mfma_f32_16x16x32_bf16 v[64:67], a[136:139], a[72:75], v[64:67]\n" \
"ds_read_b128 a[180:183], v49 offset:5120\n" \
"ds_read_b128 a[164:167], v49 offset:1024\n" \
"v_mfma_f32_16x16x32_bf16 v[64:67], a[140:143], a[76:79], v[64:67]\n" \
"v_exp_f32_e32 v92, v92\n" \
"v_mfma_f32_16x16x32_bf16 v[68:71], a[156:159], a[76:79], v[32:35]\n" \
"ds_read_b128 a[168:171], v49 offset:2048\n" \
"ds_read_b128 a[184:187], v49 offset:6144\n" \
"v_mfma_f32_16x16x32_bf16 v[68:71], a[152:155], a[72:75], v[68:71]\n" \
"v_exp_f32_e32 v93, v93\n" \
"v_mfma_f32_16x16x32_bf16 v[68:71], a[148:151], a[68:71], v[68:71]\n" \
"ds_read_b128 a[188:191], v49 offset:7168\n" \
"ds_read_b128 a[172:175], v49 offset:3072\n" \
"v_mfma_f32_16x16x32_bf16 v[68:71], a[144:147], a[64:67], v[68:71]\n" \
"v_exp_f32_e32 v94, v94\n" \
"v_mfma_f32_16x16x32_bf16 v[72:75], a[128:131], a[80:83], v[36:39]\n" \
"v_exp_f32_e32 v95, v95\n" \
"v_mfma_f32_16x16x32_bf16 v[72:75], a[132:135], a[84:87], v[72:75]\n" \
"v_cvt_pk_bf16_f32 v122, v92, v93\n" \
"v_cvt_pk_bf16_f32 v123, v94, v95\n" \
"v_mfma_f32_16x16x32_bf16 v[72:75], a[136:139], a[88:91], v[72:75]\n" \
"v_exp_f32_e32 v64, v64\n" \
"v_mfma_f32_16x16x32_bf16 v[72:75], a[140:143], a[92:95], v[72:75]\n" \
"v_exp_f32_e32 v65, v65\n" \
"v_mfma_f32_16x16x32_bf16 v[76:79], a[156:159], a[92:95], v[36:39]\n" \
"v_exp_f32_e32 v66, v66\n" \
"v_mfma_f32_16x16x32_bf16 v[76:79], a[152:155], a[88:91], v[76:79]\n" \
"v_exp_f32_e32 v67, v67\n" \
"v_mfma_f32_16x16x32_bf16 v[76:79], a[148:151], a[84:87], v[76:79]\n" \
"v_exp_f32_e32 v68, v68\n" \
"v_mfma_f32_16x16x32_bf16 v[76:79], a[144:147], a[80:83], v[76:79]\n" \
"v_exp_f32_e32 v69, v69\n" \
"v_mfma_f32_16x16x32_bf16 v[80:83], a[128:131], a[96:99], v[40:43]\n" \
"v_exp_f32_e32 v70, v70\n" \
"v_mfma_f32_16x16x32_bf16 v[80:83], a[132:135], a[100:103], v[80:83]\n" \
"v_exp_f32_e32 v71, v71\n" \
"v_mfma_f32_16x16x32_bf16 v[80:83], a[136:139], a[104:107], v[80:83]\n" \
"v_cvt_pk_bf16_f32 v100, v64, v65\n" \
"v_cvt_pk_bf16_f32 v101, v66, v67\n" \
"v_mfma_f32_16x16x32_bf16 v[80:83], a[140:143], a[108:111], v[80:83]\n" \
"v_cvt_pk_bf16_f32 v102, v68, v69\n" \
"v_cvt_pk_bf16_f32 v103, v70, v71\n" \
"v_mfma_f32_16x16x32_bf16 v[84:87], a[156:159], a[108:111], v[40:43]\n" \
"v_exp_f32_e32 v72, v72\n" \
"v_mfma_f32_16x16x32_bf16 v[84:87], a[152:155], a[104:107], v[84:87]\n" \
"v_exp_f32_e32 v73, v73\n" \
"v_mfma_f32_16x16x32_bf16 v[84:87], a[148:151], a[100:103], v[84:87]\n" \
"v_exp_f32_e32 v74, v74\n" \
"v_mfma_f32_16x16x32_bf16 v[84:87], a[144:147], a[96:99], v[84:87]\n" \
"v_exp_f32_e32 v75, v75\n" \
"v_mfma_f32_16x16x32_bf16 v[88:91], a[128:131], a[112:115], v[44:47]\n" \
"v_exp_f32_e32 v76, v76\n" \
"v_mfma_f32_16x16x32_bf16 v[88:91], a[132:135], a[116:119], v[88:91]\n" \
"v_exp_f32_e32 v77, v77\n" \
"v_mfma_f32_16x16x32_bf16 v[88:91], a[136:139], a[120:123], v[88:91]\n" \
"v_exp_f32_e32 v78, v78\n" \
"v_mfma_f32_16x16x32_bf16 v[88:91], a[140:143], a[124:127], v[88:91]\n" \
"v_exp_f32_e32 v79, v79\n" \
"v_mfma_f32_16x16x32_bf16 v[92:95], a[156:159], a[124:127], v[44:47]\n" \
"v_cvt_pk_bf16_f32 v108, v72, v73\n" \
"v_cvt_pk_bf16_f32 v109, v74, v75\n" \
"v_mfma_f32_16x16x32_bf16 v[92:95], a[152:155], a[120:123], v[92:95]\n" \
"v_cvt_pk_bf16_f32 v110, v76, v77\n" \
"v_cvt_pk_bf16_f32 v111, v78, v79\n" \
"v_mfma_f32_16x16x32_bf16 v[92:95], a[148:151], a[116:119], v[92:95]\n" \
"ds_read_b128 a[128:131], v48 offset:0\n" \
"ds_read_b128 a[132:135], v48 offset:1024\n" \
"v_mfma_f32_16x16x32_bf16 v[92:95], a[144:147], a[112:115], v[92:95]\n" \
"s_add_u32 s63, s63, s63\n" \
"s_waitcnt lgkmcnt(2)\n" \
"v_mfma_f32_16x16x32_bf16 v[128:131], a[160:163], v[96:99], v[128:131]\n" \
"ds_read_b128 a[136:139], v48 offset:2048\n" \
"ds_read_b128 a[140:143], v48 offset:3072\n" \
"v_mfma_f32_16x16x32_bf16 v[128:131], a[176:179], v[100:103], v[128:131]\n" \
"v_exp_f32_e32 v80, v80\n" \
"v_mfma_f32_16x16x32_bf16 v[132:135], a[180:183], v[100:103], v[132:135]\n" \
"ds_read_b128 a[144:147], v48 offset:4096\n" \
"ds_read_b128 a[148:151], v48 offset:5120\n" \
"v_mfma_f32_16x16x32_bf16 v[132:135], a[164:167], v[96:99], v[132:135]\n" \
"v_exp_f32_e32 v81, v81\n" \
"v_mfma_f32_16x16x32_bf16 v[136:139], a[168:171], v[96:99], v[136:139]\n" \
"ds_read_b128 a[152:155], v48 offset:6144\n" \
"ds_read_b128 a[156:159], v48 offset:7168\n" \
"v_mfma_f32_16x16x32_bf16 v[136:139], a[184:187], v[100:103], v[136:139]\n" \
"v_exp_f32_e32 v82, v82\n" \
"v_mfma_f32_16x16x32_bf16 v[140:143], a[188:191], v[100:103], v[140:143]\n" \
"v_exp_f32_e32 v83, v83\n" \
"v_mfma_f32_16x16x32_bf16 v[140:143], a[172:175], v[96:99], v[140:143]\n" \
"v_exp_f32_e32 v84, v84\n" \
"v_smfmac_f32_16x16x64_bf16 a[192:195], a[212:215], v[96:103], v21\n" \
"v_mfma_f32_16x16x32_bf16 v[160:163], a[160:163], v[104:107], v[160:163]\n" \
"v_exp_f32_e32 v85, v85\n" \
"v_mfma_f32_16x16x32_bf16 v[160:163], a[176:179], v[108:111], v[160:163]\n" \
"v_exp_f32_e32 v86, v86\n" \
"v_mfma_f32_16x16x32_bf16 v[164:167], a[180:183], v[108:111], v[164:167]\n" \
"v_exp_f32_e32 v87, v87\n" \
"v_mfma_f32_16x16x32_bf16 v[164:167], a[164:167], v[104:107], v[164:167]\n" \
"v_cvt_pk_bf16_f32 v116, v80, v81\n" \
"v_cvt_pk_bf16_f32 v117, v82, v83\n" \
"v_mfma_f32_16x16x32_bf16 v[168:171], a[168:171], v[104:107], v[168:171]\n" \
"v_cvt_pk_bf16_f32 v118, v84, v85\n" \
"v_cvt_pk_bf16_f32 v119, v86, v87\n" \
"v_mfma_f32_16x16x32_bf16 v[168:171], a[184:187], v[108:111], v[168:171]\n" \
"v_exp_f32_e32 v88, v88\n" \
"v_mfma_f32_16x16x32_bf16 v[172:175], a[188:191], v[108:111], v[172:175]\n" \
"v_exp_f32_e32 v89, v89\n" \
"v_mfma_f32_16x16x32_bf16 v[172:175], a[172:175], v[104:107], v[172:175]\n" \
"v_exp_f32_e32 v90, v90\n" \
"v_smfmac_f32_16x16x64_bf16 a[196:199], a[212:215], v[104:111], v21\n" \
"v_mfma_f32_16x16x32_bf16 v[192:195], a[160:163], v[112:115], v[192:195]\n" \
"v_exp_f32_e32 v91, v91\n" \
"v_mfma_f32_16x16x32_bf16 v[192:195], a[176:179], v[116:119], v[192:195]\n" \
"v_exp_f32_e32 v92, v92\n" \
"v_mfma_f32_16x16x32_bf16 v[196:199], a[180:183], v[116:119], v[196:199]\n" \
"v_exp_f32_e32 v93, v93\n" \
"v_mfma_f32_16x16x32_bf16 v[196:199], a[164:167], v[112:115], v[196:199]\n" \
"v_exp_f32_e32 v94, v94\n" \
"v_mfma_f32_16x16x32_bf16 v[200:203], a[168:171], v[112:115], v[200:203]\n" \
"v_exp_f32_e32 v95, v95\n" \
"v_mfma_f32_16x16x32_bf16 v[200:203], a[184:187], v[116:119], v[200:203]\n" \
"v_cvt_pk_bf16_f32 v124, v88, v89\n" \
"v_cvt_pk_bf16_f32 v125, v90, v91\n" \
"v_mfma_f32_16x16x32_bf16 v[204:207], a[188:191], v[116:119], v[204:207]\n" \
"v_cvt_pk_bf16_f32 v126, v92, v93\n" \
"v_cvt_pk_bf16_f32 v127, v94, v95\n" \
"v_mfma_f32_16x16x32_bf16 v[204:207], a[172:175], v[112:115], v[204:207]\n" \
"v_smfmac_f32_16x16x64_bf16 a[200:203], a[212:215], v[112:119], v21\n" \
"s_waitcnt vmcnt(0) lgkmcnt(0)\n" \
"s_barrier\n" \
"v_mfma_f32_16x16x32_bf16 v[224:227], a[160:163], v[120:123], v[224:227]\n" \
"s_cbranch_scc1 L_h2_%=\n" \
"L_hr2_%=:\n" \
"v_mfma_f32_16x16x32_bf16 v[224:227], a[176:179], v[124:127], v[224:227]\n" \
"s_xor_b32 m0, s65, 0x12000\n" \
"ds_read_b128 a[160:163], v49 offset:8192\n" \
"v_mfma_f32_16x16x32_bf16 v[228:231], a[180:183], v[124:127], v[228:231]\n" \
"buffer_load_dwordx4 v50, s[8:11], s64 offen offset:0 sc1 lds\n" \
"v_mfma_f32_16x16x32_bf16 v[228:231], a[164:167], v[120:123], v[228:231]\n" \
"ds_read_b128 a[176:179], v49 offset:12288\n" \
"ds_read_b128 a[180:183], v49 offset:13312\n" \
"v_mfma_f32_16x16x32_bf16 v[232:235], a[168:171], v[120:123], v[232:235]\n" \
"v_mfma_f32_16x16x32_bf16 v[232:235], a[184:187], v[124:127], v[232:235]\n" \
"buffer_load_dwordx4 v50, s[8:11], s64 offen offset:1024 sc1 lds\n" \
"v_mfma_f32_16x16x32_bf16 v[236:239], a[188:191], v[124:127], v[236:239]\n" \
"ds_read_b128 a[164:167], v49 offset:9216\n" \
"ds_read_b128 a[168:171], v49 offset:10240\n" \
"v_mfma_f32_16x16x32_bf16 v[236:239], a[172:175], v[120:123], v[236:239]\n" \
"v_mfma_f32_16x16x32_bf16 v[64:67], a[128:131], a[64:67], v[32:35]\n" \
"ds_read_b128 a[184:187], v49 offset:14336\n" \
"ds_read_b128 a[188:191], v49 offset:15360\n" \
"v_mfma_f32_16x16x32_bf16 v[64:67], a[132:135], a[68:71], v[64:67]\n" \
"buffer_load_dwordx4 v50, s[8:11], s64 offen offset:2048 sc1 lds\n" \
"v_mfma_f32_16x16x32_bf16 v[64:67], a[136:139], a[72:75], v[64:67]\n" \
"ds_read_b128 a[172:175], v49 offset:11264\n" \
"v_add_u32_e32 v49, 0x10000, v48\n" \
"v_mfma_f32_16x16x32_bf16 v[64:67], a[140:143], a[76:79], v[64:67]\n" \
"v_mfma_f32_16x16x32_bf16 v[68:71], a[156:159], a[76:79], v[32:35]\n" \
"v_mfma_f32_16x16x32_bf16 v[68:71], a[152:155], a[72:75], v[68:71]\n" \
"v_mfma_f32_16x16x32_bf16 v[68:71], a[148:151], a[68:71], v[68:71]\n" \
"buffer_load_dwordx4 v50, s[8:11], s64 offen offset:3072 sc1 lds\n" \
"v_mfma_f32_16x16x32_bf16 v[68:71], a[144:147], a[64:67], v[68:71]\n" \
"s_xor_b32 m0, s65, 0xa000\n" \
"v_mfma_f32_16x16x32_bf16 v[72:75], a[128:131], a[80:83], v[36:39]\n" \
"v_mfma_f32_16x16x32_bf16 v[72:75], a[132:135], a[84:87], v[72:75]\n" \
"v_exp_f32_e32 v64, v64\n" \
"v_mfma_f32_16x16x32_bf16 v[72:75], a[136:139], a[88:91], v[72:75]\n" \
"buffer_load_dwordx4 v51, s[8:11], s64 offen offset:0 sc1 lds\n" \
"v_mfma_f32_16x16x32_bf16 v[72:75], a[140:143], a[92:95], v[72:75]\n" \
"v_exp_f32_e32 v65, v65\n" \
"v_mfma_f32_16x16x32_bf16 v[76:79], a[156:159], a[92:95], v[36:39]\n" \
"v_exp_f32_e32 v66, v66\n" \
"v_mfma_f32_16x16x32_bf16 v[76:79], a[152:155], a[88:91], v[76:79]\n" \
"v_exp_f32_e32 v67, v67\n" \
"v_mfma_f32_16x16x32_bf16 v[76:79], a[148:151], a[84:87], v[76:79]\n" \
"buffer_load_dwordx4 v51, s[8:11], s64 offen offset:1024 sc1 lds\n" \
"v_mfma_f32_16x16x32_bf16 v[76:79], a[144:147], a[80:83], v[76:79]\n" \
"v_exp_f32_e32 v68, v68\n" \
"v_mfma_f32_16x16x32_bf16 v[80:83], a[128:131], a[96:99], v[40:43]\n" \
"v_exp_f32_e32 v69, v69\n" \
"v_mfma_f32_16x16x32_bf16 v[80:83], a[132:135], a[100:103], v[80:83]\n" \
"v_exp_f32_e32 v70, v70\n" \
"v_mfma_f32_16x16x32_bf16 v[80:83], a[136:139], a[104:107], v[80:83]\n" \
"v_exp_f32_e32 v71, v71\n" \
"v_mfma_f32_16x16x32_bf16 v[80:83], a[140:143], a[108:111], v[80:83]\n" \
"buffer_load_dwordx4 v51, s[8:11], s64 offen offset:2048 sc1 lds\n" \
"v_mfma_f32_16x16x32_bf16 v[84:87], a[156:159], a[108:111], v[40:43]\n" \
"v_exp_f32_e32 v72, v72\n" \
"v_mfma_f32_16x16x32_bf16 v[84:87], a[152:155], a[104:107], v[84:87]\n" \
"v_exp_f32_e32 v73, v73\n" \
"v_mfma_f32_16x16x32_bf16 v[84:87], a[148:151], a[100:103], v[84:87]\n" \
"v_exp_f32_e32 v74, v74\n" \
"v_mfma_f32_16x16x32_bf16 v[84:87], a[144:147], a[96:99], v[84:87]\n" \
"buffer_load_dwordx4 v51, s[8:11], s64 offen offset:3072 sc1 lds\n" \
"v_mfma_f32_16x16x32_bf16 v[88:91], a[128:131], a[112:115], v[44:47]\n" \
"s_xor_b32 m0, s65, 0x18000\n" \
"ds_read_b128 a[128:131], v48 offset:8192\n" \
"v_mfma_f32_16x16x32_bf16 v[88:91], a[132:135], a[116:119], v[88:91]\n" \
"v_exp_f32_e32 v75, v75\n" \
"v_mfma_f32_16x16x32_bf16 v[88:91], a[136:139], a[120:123], v[88:91]\n" \
"ds_read_b128 a[132:135], v48 offset:9216\n" \
"ds_read_b128 a[136:139], v48 offset:10240\n" \
"v_mfma_f32_16x16x32_bf16 v[88:91], a[140:143], a[124:127], v[88:91]\n" \
"v_exp_f32_e32 v76, v76\n" \
"v_mfma_f32_16x16x32_bf16 v[92:95], a[156:159], a[124:127], v[44:47]\n" \
"buffer_load_dwordx4 v52, s[8:11], s64 offen offset:0 sc1 lds\n" \
"v_mfma_f32_16x16x32_bf16 v[92:95], a[152:155], a[120:123], v[92:95]\n" \
"ds_read_b128 a[140:143], v48 offset:11264\n" \
"ds_read_b128 a[152:155], v48 offset:14336\n" \
"v_mfma_f32_16x16x32_bf16 v[92:95], a[148:151], a[116:119], v[92:95]\n" \
"v_exp_f32_e32 v77, v77\n" \
"v_mfma_f32_16x16x32_bf16 v[92:95], a[144:147], a[112:115], v[92:95]\n" \
"ds_read_b128 a[144:147], v48 offset:12288\n" \
"s_waitcnt lgkmcnt(6)\n" \
"v_mfma_f32_16x16x32_bf16 v[144:147], a[160:163], v[96:99], v[144:147]\n" \
"buffer_load_dwordx4 v52, s[8:11], s64 offen offset:1024 sc1 lds\n" \
"v_mfma_f32_16x16x32_bf16 v[144:147], a[176:179], v[100:103], v[144:147]\n" \
"ds_read_b128 a[148:151], v48 offset:13312\n" \
"ds_read_b128 a[156:159], v48 offset:15360\n" \
"v_mfma_f32_16x16x32_bf16 v[148:151], a[180:183], v[100:103], v[148:151]\n" \
"v_exp_f32_e32 v78, v78\n" \
"v_mfma_f32_16x16x32_bf16 v[148:151], a[164:167], v[96:99], v[148:151]\n" \
"v_exp_f32_e32 v79, v79\n" \
"v_mfma_f32_16x16x32_bf16 v[152:155], a[168:171], v[96:99], v[152:155]\n" \
"buffer_load_dwordx4 v52, s[8:11], s64 offen offset:2048 sc1 lds\n" \
"v_mfma_f32_16x16x32_bf16 v[152:155], a[184:187], v[100:103], v[152:155]\n" \
"v_exp_f32_e32 v80, v80\n" \
"v_mfma_f32_16x16x32_bf16 v[156:159], a[188:191], v[100:103], v[156:159]\n" \
"v_exp_f32_e32 v81, v81\n" \
"v_mfma_f32_16x16x32_bf16 v[156:159], a[172:175], v[96:99], v[156:159]\n" \
"v_exp_f32_e32 v82, v82\n" \
"v_mfma_f32_16x16x32_bf16 v[176:179], a[160:163], v[104:107], v[176:179]\n" \
"v_exp_f32_e32 v83, v83\n" \
"v_mfma_f32_16x16x32_bf16 v[176:179], a[176:179], v[108:111], v[176:179]\n" \
"buffer_load_dwordx4 v52, s[8:11], s64 offen offset:3072 sc1 lds\n" \
"v_mfma_f32_16x16x32_bf16 v[180:183], a[180:183], v[108:111], v[180:183]\n" \
"s_mov_b32 m0, s65\n" \
"s_xor_b32 s65, s65, 0x8000\n" \
"v_mfma_f32_16x16x32_bf16 v[180:183], a[164:167], v[104:107], v[180:183]\n" \
"v_exp_f32_e32 v84, v84\n" \
"v_mfma_f32_16x16x32_bf16 v[184:187], a[168:171], v[104:107], v[184:187]\n" \
"v_exp_f32_e32 v85, v85\n" \
"v_mfma_f32_16x16x32_bf16 v[184:187], a[184:187], v[108:111], v[184:187]\n" \
"buffer_load_dwordx4 v53, s[8:11], s64 offen offset:0 sc1 lds\n" \
"v_mfma_f32_16x16x32_bf16 v[188:191], a[188:191], v[108:111], v[188:191]\n" \
"v_exp_f32_e32 v86, v86\n" \
"v_mfma_f32_16x16x32_bf16 v[188:191], a[172:175], v[104:107], v[188:191]\n" \
"v_exp_f32_e32 v87, v87\n" \
"v_mfma_f32_16x16x32_bf16 v[208:211], a[160:163], v[112:115], v[208:211]\n" \
"v_exp_f32_e32 v88, v88\n" \
"v_mfma_f32_16x16x32_bf16 v[208:211], a[176:179], v[116:119], v[208:211]\n" \
"buffer_load_dwordx4 v53, s[8:11], s64 offen offset:1024 sc1 lds\n" \
"v_mfma_f32_16x16x32_bf16 v[212:215], a[180:183], v[116:119], v[212:215]\n" \
"v_exp_f32_e32 v89, v89\n" \
"v_mfma_f32_16x16x32_bf16 v[212:215], a[164:167], v[112:115], v[212:215]\n" \
"v_exp_f32_e32 v90, v90\n" \
"v_mfma_f32_16x16x32_bf16 v[216:219], a[168:171], v[112:115], v[216:219]\n" \
"v_exp_f32_e32 v91, v91\n" \
"v_mfma_f32_16x16x32_bf16 v[216:219], a[184:187], v[116:119], v[216:219]\n" \
"v_cvt_pk_bf16_f32 v96, v64, v65\n" \
"v_cvt_pk_bf16_f32 v97, v66, v67\n" \
"v_mfma_f32_16x16x32_bf16 v[220:223], a[188:191], v[116:119], v[220:223]\n" \
"buffer_load_dwordx4 v53, s[8:11], s64 offen offset:2048 sc1 lds\n" \
"v_mfma_f32_16x16x32_bf16 v[220:223], a[172:175], v[112:115], v[220:223]\n" \
"v_cvt_pk_bf16_f32 v98, v68, v69\n" \
"v_cvt_pk_bf16_f32 v99, v70, v71\n" \
"v_mfma_f32_16x16x32_bf16 v[240:243], a[160:163], v[120:123], v[240:243]\n" \
"v_cvt_pk_bf16_f32 v104, v72, v73\n" \
"v_cvt_pk_bf16_f32 v105, v74, v75\n" \
"v_mfma_f32_16x16x32_bf16 v[240:243], a[176:179], v[124:127], v[240:243]\n" \
"v_cvt_pk_bf16_f32 v106, v76, v77\n" \
"v_cvt_pk_bf16_f32 v107, v78, v79\n" \
"v_mfma_f32_16x16x32_bf16 v[244:247], a[180:183], v[124:127], v[244:247]\n" \
"buffer_load_dwordx4 v53, s[8:11], s64 offen offset:3072 sc1 lds\n" \
"v_mfma_f32_16x16x32_bf16 v[244:247], a[164:167], v[120:123], v[244:247]\n" \
"s_add_u32 s64, s64, 0x10000\n" \
"s_sub_u32 s66, s66, 1\n" \
"v_mfma_f32_16x16x32_bf16 v[248:251], a[168:171], v[120:123], v[248:251]\n" \
"v_cvt_pk_bf16_f32 v112, v80, v81\n" \
"v_cvt_pk_bf16_f32 v113, v82, v83\n" \
"v_mfma_f32_16x16x32_bf16 v[248:251], a[184:187], v[124:127], v[248:251]\n" \
"s_cbranch_scc0 L_t2_%=\n" \
"v_mfma_f32_16x16x32_bf16 v[252:255], a[188:191], v[124:127], v[252:255]\n" \
"v_cvt_pk_bf16_f32 v114, v84, v85\n" \
"v_cvt_pk_bf16_f32 v115, v86, v87\n" \
"v_mfma_f32_16x16x32_bf16 v[252:255], a[172:175], v[120:123], v[252:255]\n" \
"v_xor_b32_e32 v48, 0x8000, v48\n" \
"v_smfmac_f32_16x16x64_bf16 a[204:207], a[212:215], v[120:127], v21\n" \
"s_waitcnt lgkmcnt(0)\n" \
"s_waitcnt vmcnt(12)\n" \
"s_barrier\n" \
"s_cmp_lg_u32 s16, 0\n" \
"s_cbranch_scc1 L_f1_%=\n" \
"ds_read_b128 a[160:163], v49 offset:0\n" \
"ds_read_b128 a[164:167], v49 offset:1024\n" \
"ds_read_b128 a[168:171], v49 offset:2048\n" \
"ds_read_b128 a[172:175], v49 offset:3072\n" \
"ds_read_b128 a[176:179], v49 offset:8192\n" \
"ds_read_b128 a[180:183], v49 offset:9216\n" \
"v_cvt_pk_bf16_f32 v120, v88, v89\n" \
"v_cvt_pk_bf16_f32 v121, v90, v91\n" \
"v_mfma_f32_16x16x32_bf16 v[88:91], a[128:131], a[112:115], v[44:47]\n" \
"s_add_u32 s70, s61, 0x0\n" \
"s_add_u32 s71, s61, 0x49000\n" \
"ds_read_b128 a[184:187], v49 offset:10240\n" \
"v_mfma_f32_16x16x32_bf16 v[88:91], a[132:135], a[116:119], v[88:91]\n" \
"ds_read_b128 a[188:191], v49 offset:11264\n" \
"v_add_u32_e32 v54, 0x49000, v20\n" \
"v_mfma_f32_16x16x32_bf16 v[88:91], a[136:139], a[120:123], v[88:91]\n" \
"v_exp_f32_e32 v92, v92\n" \
"v_mfma_f32_16x16x32_bf16 v[88:91], a[140:143], a[124:127], v[88:91]\n" \
"v_exp_f32_e32 v93, v93\n" \
"v_exp_f32_e32 v94, v94\n" \
"v_exp_f32_e32 v95, v95\n" \
"v_cvt_pk_bf16_f32 v122, v92, v93\n" \
"v_cvt_pk_bf16_f32 v123, v94, v95\n" \
"v_mfma_f32_16x16x32_bf16 v[92:95], a[156:159], a[124:127], v[44:47]\n" \
"buffer_load_dwordx4 a[220:223], v20, s[56:59], s60 offen offset:0 sc1\n" \
"v_mfma_f32_16x16x32_bf16 v[92:95], a[152:155], a[120:123], v[92:95]\n" \
"v_readlane_b32 s97, v11, 32\n" \
"v_readlane_b32 s98, v11, 33\n" \
"v_mfma_f32_16x16x32_bf16 v[92:95], a[148:151], a[116:119], v[92:95]\n" \
"v_and_b32_e32 v112, v12, v112\n" \
"v_and_b32_e32 v113, v13, v113\n" \
"v_mfma_f32_16x16x32_bf16 v[92:95], a[144:147], a[112:115], v[92:95]\n" \
"v_and_b32_e32 v114, v14, v114\n" \
"s_waitcnt lgkmcnt(7)\n" \
"v_mfma_f32_16x16x32_bf16 v[128:131], a[160:163], v[96:99], v[128:131]\n" \
"v_and_b32_e32 v115, v15, v115\n" \
"s_waitcnt lgkmcnt(6)\n" \
"v_mfma_f32_16x16x32_bf16 v[132:135], a[164:167], v[96:99], v[132:135]\n" \
"ds_read_b32 v60, v18 offset:0\n" \
"s_waitcnt lgkmcnt(6)\n" \
"v_mfma_f32_16x16x32_bf16 v[136:139], a[168:171], v[96:99], v[136:139]\n" \
"ds_read_b32 v61, v18 offset:128\n" \
"s_waitcnt lgkmcnt(6)\n" \
"v_mfma_f32_16x16x32_bf16 v[140:143], a[172:175], v[96:99], v[140:143]\n" \
"ds_read_b128 a[0:3], v49 offset:4096\n" \
"s_waitcnt lgkmcnt(5)\n" \
"v_mfma_f32_16x16x32_bf16 v[144:147], a[176:179], v[96:99], v[144:147]\n" \
"buffer_load_dwordx4 a[236:239], v54, s[56:59], s60 offen offset:0 sc1\n" \
"v_mfma_f32_16x16x32_bf16 v[148:151], a[180:183], v[96:99], v[148:151]\n" \
"ds_read_b128 a[4:7], v49 offset:5120\n" \
"s_waitcnt lgkmcnt(5)\n" \
"v_mfma_f32_16x16x32_bf16 v[152:155], a[184:187], v[96:99], v[152:155]\n" \
"ds_read_b128 a[8:11], v49 offset:6144\n" \
"s_waitcnt lgkmcnt(5)\n" \
"v_mfma_f32_16x16x32_bf16 v[156:159], a[188:191], v[96:99], v[156:159]\n" \
"buffer_load_dwordx4 a[224:227], v20, s[56:59], s60 offen offset:1024 sc1\n" \
"v_mfma_f32_16x16x32_bf16 a[192:195], a[252:255], v[96:99], a[192:195]\n" \
"ds_read_b128 a[12:15], v49 offset:7168\n" \
"ds_read_b128 a[16:19], v49 offset:12288\n" \
"v_mfma_f32_16x16x32_bf16 v[160:163], a[160:163], v[104:107], v[160:163]\n" \
"ds_read_b128 a[20:23], v49 offset:13312\n" \
"ds_read_b128 a[24:27], v49 offset:14336\n" \
"buffer_load_dwordx4 a[240:243], v54, s[56:59], s60 offen offset:1024 sc1\n" \
"v_mfma_f32_16x16x32_bf16 v[164:167], a[164:167], v[104:107], v[164:167]\n" \
"ds_read_b128 a[28:31], v49 offset:15360\n" \
"v_accvgpr_read_b32 v40, a192\n" \
"v_mfma_f32_16x16x32_bf16 v[168:171], a[168:171], v[104:107], v[168:171]\n" \
"v_accvgpr_read_b32 v23, a193\n" \
"v_add_f32_e32 v40, v40, v23\n" \
"v_mfma_f32_16x16x32_bf16 v[172:175], a[172:175], v[104:107], v[172:175]\n" \
"buffer_load_dwordx4 a[228:231], v20, s[56:59], s60 offen offset:2048 sc1\n" \
"v_mfma_f32_16x16x32_bf16 v[176:179], a[176:179], v[104:107], v[176:179]\n" \
"v_add_u32_e32 v50, s15, v48\n" \
"v_add_u32_e32 v51, 0x10000, v50\n" \
"v_mfma_f32_16x16x32_bf16 v[180:183], a[180:183], v[104:107], v[180:183]\n" \
"s_waitcnt vmcnt(5)\n" \
"ds_write_b128 v50, v[128:131] offset:0\n" \
"buffer_load_dwordx4 a[244:247], v54, s[56:59], s60 offen offset:2048 sc1\n" \
"v_mfma_f32_16x16x32_bf16 v[184:187], a[184:187], v[104:107], v[184:187]\n" \
"ds_write_b128 v50, v[132:135] offset:1024\n" \
"v_mfma_f32_16x16x32_bf16 v[188:191], a[188:191], v[104:107], v[188:191]\n" \
"ds_write_b128 v50, v[136:139] offset:2048\n" \
"buffer_load_dwordx4 a[232:235], v20, s[56:59], s60 offen offset:3072 sc1\n" \
"v_mfma_f32_16x16x32_bf16 a[196:199], a[252:255], v[104:107], a[196:199]\n" \
"ds_write_b128 v50, v[140:143] offset:3072\n" \
"v_mfma_f32_16x16x32_bf16 v[192:195], a[160:163], v[112:115], v[192:195]\n" \
"ds_write_b128 v50, v[144:147] offset:8192\n" \
"buffer_load_dwordx4 a[248:251], v54, s[56:59], s60 offen offset:3072 sc1\n" \
"v_mfma_f32_16x16x32_bf16 v[196:199], a[164:167], v[112:115], v[196:199]\n" \
"ds_write_b128 v50, v[148:151] offset:9216\n" \
"v_mfma_f32_16x16x32_bf16 v[200:203], a[168:171], v[112:115], v[200:203]\n" \
"ds_write_b128 v50, v[152:155] offset:10240\n" \
"buffer_load_dwordx4 a[32:35], v20, s[56:59], s97 offen offset:0 sc1\n" \
"v_mfma_f32_16x16x32_bf16 v[204:207], a[172:175], v[112:115], v[204:207]\n" \
"v_accvgpr_read_b32 v41, a196\n" \
"v_accvgpr_read_b32 v23, a197\n" \
"v_mfma_f32_16x16x32_bf16 v[208:211], a[176:179], v[112:115], v[208:211]\n" \
"v_add_f32_e32 v41, v41, v23\n" \
"v_xor_b32_e32 v52, 0x4000, v50\n" \
"v_mfma_f32_16x16x32_bf16 v[212:215], a[180:183], v[112:115], v[212:215]\n" \
"buffer_load_dwordx4 a[40:43], v54, s[56:59], s97 offen offset:0 sc1\n" \
"v_mfma_f32_16x16x32_bf16 v[216:219], a[184:187], v[112:115], v[216:219]\n" \
"ds_write_b128 v50, v[156:159] offset:11264\n" \
"v_mfma_f32_16x16x32_bf16 v[220:223], a[188:191], v[112:115], v[220:223]\n" \
"ds_write_b128 v51, v[160:163] offset:0\n" \
"buffer_load_dwordx4 a[48:51], v20, s[56:59], s98 offen offset:0 sc1\n" \
"v_mfma_f32_16x16x32_bf16 a[200:203], a[252:255], v[112:115], a[200:203]\n" \
"ds_write_b128 v51, v[164:167] offset:1024\n" \
"ds_write_b128 v51, v[168:171] offset:2048\n" \
"ds_write_b128 v51, v[172:175] offset:3072\n" \
"buffer_load_dwordx4 a[56:59], v54, s[56:59], s98 offen offset:0 sc1\n" \
"ds_write_b128 v51, v[176:179] offset:8192\n" \
"ds_write_b128 v51, v[180:183] offset:9216\n" \
"ds_write_b128 v51, v[184:187] offset:10240\n" \
"ds_write_b128 v51, v[188:191] offset:11264\n" \
"buffer_load_dwordx4 a[36:39], v20, s[56:59], s97 offen offset:1024 sc1\n" \
"ds_write_b64 v19, v[40:41]\n" \
"v_xor_b32_e32 v53, 0x4000, v51\n" \
"v_xor_b32_e32 v63, 0x400, v19\n" \
"v_readlane_b32 s93, v11, 18\n" \
"v_readlane_b32 s84, v11, 13\n" \
"v_readlane_b32 s85, v11, 14\n" \
"v_readlane_b32 s86, v11, 15\n" \
"s_mov_b32 s87, 0x20000\n" \
"v_readlane_b32 s92, v11, 16\n" \
"s_add_u32 m0, s93, 0x0\n" \
"v_exp_f32_e32 v88, v88\n" \
"v_exp_f32_e32 v89, v89\n" \
"v_exp_f32_e32 v90, v90\n" \
"v_exp_f32_e32 v91, v91\n" \
"v_exp_f32_e32 v92, v92\n" \
"v_exp_f32_e32 v93, v93\n" \
"v_exp_f32_e32 v94, v94\n" \
"v_exp_f32_e32 v95, v95\n" \
"v_cvt_pk_bf16_f32 v124, v88, v89\n" \
"v_cvt_pk_bf16_f32 v125, v90, v91\n" \
"v_cvt_pk_bf16_f32 v126, v92, v93\n" \
"v_cvt_pk_bf16_f32 v127, v94, v95\n" \
"v_and_b32_e32 v124, v12, v124\n" \
"v_and_b32_e32 v125, v13, v125\n" \
"v_and_b32_e32 v126, v14, v126\n" \
"v_and_b32_e32 v127, v15, v127\n" \
"v_accvgpr_read_b32 v56, a200\n" \
"v_accvgpr_read_b32 v23, a201\n" \
"v_add_f32_e32 v56, v56, v23\n" \
"s_waitcnt lgkmcnt(0)\n" \
"s_barrier\n" \
"buffer_load_dwordx4 a[44:47], v54, s[56:59], s97 offen offset:1024 sc1\n" \
"ds_read_b128 v[128:131], v52 offset:0\n" \
"ds_read_b128 v[132:135], v52 offset:1024\n" \
"ds_read_b128 v[136:139], v52 offset:2048\n" \
"ds_read_b128 v[140:143], v52 offset:3072\n" \
"ds_read_b128 v[144:147], v52 offset:8192\n" \
"ds_read_b128 v[148:151], v52 offset:9216\n" \
"ds_read_b128 v[152:155], v52 offset:10240\n" \
"ds_read_b128 v[156:159], v52 offset:11264\n" \
"buffer_load_dwordx4 a[52:55], v20, s[56:59], s98 offen offset:1024 sc1\n" \
"ds_read_b128 v[160:163], v53 offset:0\n" \
"ds_read_b128 v[164:167], v53 offset:1024\n" \
"ds_read_b128 v[168:171], v53 offset:2048\n" \
"ds_read_b128 v[172:175], v53 offset:3072\n" \
"ds_read_b128 v[176:179], v53 offset:8192\n" \
"ds_read_b128 v[180:183], v53 offset:9216\n" \
"ds_read_b128 v[184:187], v53 offset:10240\n" \
"ds_read_b128 v[188:191], v53 offset:11264\n" \
"buffer_load_dwordx4 a[60:63], v54, s[56:59], s98 offen offset:1024 sc1\n" \
"ds_read_b64 v[42:43], v63\n" \
"v_mfma_f32_16x16x32_bf16 v[224:227], a[160:163], v[120:123], v[224:227]\n" \
"v_mfma_f32_16x16x32_bf16 v[228:231], a[164:167], v[120:123], v[228:231]\n" \
"v_mfma_f32_16x16x32_bf16 v[232:235], a[168:171], v[120:123], v[232:235]\n" \
"buffer_load_dwordx4 v0, s[84:87], s92 offen offset:0 lds\n" \
"L_pf13:\n" \
"s_cbranch_execz L_pf17\n" \
"s_nop 0\n" \
"v_mfma_f32_16x16x32_bf16 v[236:239], a[172:175], v[120:123], v[236:239]\n" \
"v_mfma_f32_16x16x32_bf16 v[240:243], a[176:179], v[120:123], v[240:243]\n" \
"v_mfma_f32_16x16x32_bf16 v[244:247], a[180:183], v[120:123], v[244:247]\n" \
"buffer_load_dwordx4 v1, s[84:87], s92 offen offset:1024 lds\n" \
"v_mfma_f32_16x16x32_bf16 v[248:251], a[184:187], v[120:123], v[248:251]\n" \
"s_waitcnt lgkmcnt(15)\n" \
"v_add_f32_e32 v192, v192, v128\n" \
"v_mfma_f32_16x16x32_bf16 v[252:255], a[188:191], v[120:123], v[252:255]\n" \
"v_add_f32_e32 v193, v193, v129\n" \
"v_add_f32_e32 v194, v194, v130\n" \
"v_mfma_f32_16x16x32_bf16 a[204:207], a[252:255], v[120:123], a[204:207]\n" \
"buffer_load_dwordx4 v2, s[84:87], s92 offen offset:2048 lds\n" \
"v_add_f32_e32 v195, v195, v131\n" \
"s_waitcnt lgkmcnt(1)\n" \
"s_barrier\n" \
"v_mfma_f32_16x16x32_bf16 v[224:227], a[0:3], v[124:127], v[224:227]\n" \
"buffer_load_dwordx4 v3, s[84:87], s92 offen offset:3072 lds\n" \
"s_add_u32 m0, s93, 0x1000\n" \
"v_mfma_f32_16x16x32_bf16 v[228:231], a[4:7], v[124:127], v[228:231]\n" \
"s_waitcnt lgkmcnt(0)\n" \
"v_add_f32_e32 v56, v56, v42\n" \
"v_mfma_f32_16x16x32_bf16 v[232:235], a[8:11], v[124:127], v[232:235]\n" \
"v_max_f32_e32 v62, v62, v56\n" \
"v_add_f32_e32 v196, v196, v132\n" \
"v_mfma_f32_16x16x32_bf16 v[236:239], a[12:15], v[124:127], v[236:239]\n" \
"buffer_load_dwordx4 v4, s[84:87], s92 offen offset:0 lds\n" \
"v_rcp_f32_e32 v56, v56\n" \
"v_mfma_f32_16x16x32_bf16 v[240:243], a[16:19], v[124:127], v[240:243]\n" \
"v_add_f32_e32 v197, v197, v133\n" \
"v_add_f32_e32 v198, v198, v134\n" \
"v_add_f32_e32 v199, v199, v135\n" \
"v_mul_f32_e32 v56, v56, v60\n" \
"v_mul_f32_e32 v192, v56, v192\n" \
"v_mul_f32_e32 v193, v56, v193\n" \
"v_mul_f32_e32 v194, v56, v194\n" \
"v_mul_f32_e32 v195, v56, v195\n" \
"v_mul_f32_e32 v196, v56, v196\n" \
"v_mul_f32_e32 v197, v56, v197\n" \
"v_mul_f32_e32 v198, v56, v198\n" \
"v_mul_f32_e32 v199, v56, v199\n" \
"v_add_f32_e32 v200, v200, v136\n" \
"v_add_f32_e32 v201, v201, v137\n" \
"v_add_f32_e32 v202, v202, v138\n" \
"v_add_f32_e32 v203, v203, v139\n" \
"v_add_f32_e32 v204, v204, v140\n" \
"v_add_f32_e32 v205, v205, v141\n" \
"v_add_f32_e32 v206, v206, v142\n" \
"v_add_f32_e32 v207, v207, v143\n" \
"v_cvt_pk_bf16_f32 v64, v192, v193\n" \
"v_cvt_pk_bf16_f32 v65, v194, v195\n" \
"v_cvt_pk_bf16_f32 v66, v196, v197\n" \
"v_cvt_pk_bf16_f32 v67, v198, v199\n" \
"buffer_store_dwordx4 v[64:67], v20, s[56:59], s70 offen offset:0 sc1\n" \
"v_mfma_f32_16x16x32_bf16 v[244:247], a[20:23], v[124:127], v[244:247]\n" \
"v_mul_f32_e32 v200, v56, v200\n" \
"v_mul_f32_e32 v201, v56, v201\n" \
"v_mul_f32_e32 v202, v56, v202\n" \
"v_mul_f32_e32 v203, v56, v203\n" \
"v_mul_f32_e32 v204, v56, v204\n" \
"v_mul_f32_e32 v205, v56, v205\n" \
"v_mul_f32_e32 v206, v56, v206\n" \
"v_mul_f32_e32 v207, v56, v207\n" \
"v_add_f32_e32 v208, v208, v144\n" \
"v_add_f32_e32 v209, v209, v145\n" \
"v_add_f32_e32 v210, v210, v146\n" \
"v_add_f32_e32 v211, v211, v147\n" \
"v_add_f32_e32 v212, v212, v148\n" \
"v_add_f32_e32 v213, v213, v149\n" \
"v_add_f32_e32 v214, v214, v150\n" \
"v_add_f32_e32 v215, v215, v151\n" \
"v_cvt_pk_bf16_f32 v68, v200, v201\n" \
"v_cvt_pk_bf16_f32 v69, v202, v203\n" \
"v_cvt_pk_bf16_f32 v70, v204, v205\n" \
"v_cvt_pk_bf16_f32 v71, v206, v207\n" \
"buffer_store_dwordx4 v[68:71], v20, s[56:59], s70 offen offset:1024 sc1\n" \
"v_mul_f32_e32 v208, v56, v208\n" \
"v_mul_f32_e32 v209, v56, v209\n" \
"v_mul_f32_e32 v210, v56, v210\n" \
"v_mul_f32_e32 v211, v56, v211\n" \
"v_mul_f32_e32 v212, v56, v212\n" \
"v_mul_f32_e32 v213, v56, v213\n" \
"v_mul_f32_e32 v214, v56, v214\n" \
"v_mul_f32_e32 v215, v56, v215\n" \
"v_add_f32_e32 v216, v216, v152\n" \
"v_add_f32_e32 v217, v217, v153\n" \
"v_add_f32_e32 v218, v218, v154\n" \
"v_add_f32_e32 v219, v219, v155\n" \
"v_add_f32_e32 v220, v220, v156\n" \
"v_add_f32_e32 v221, v221, v157\n" \
"v_add_f32_e32 v222, v222, v158\n" \
"v_add_f32_e32 v223, v223, v159\n" \
"v_cvt_pk_bf16_f32 v72, v208, v209\n" \
"v_cvt_pk_bf16_f32 v73, v210, v211\n" \
"v_cvt_pk_bf16_f32 v74, v212, v213\n" \
"v_cvt_pk_bf16_f32 v75, v214, v215\n" \
"buffer_store_dwordx4 v[72:75], v20, s[56:59], s70 offen offset:2048 sc1\n" \
"v_mfma_f32_16x16x32_bf16 v[248:251], a[24:27], v[124:127], v[248:251]\n" \
"v_mul_f32_e32 v216, v56, v216\n" \
"v_mul_f32_e32 v217, v56, v217\n" \
"v_mul_f32_e32 v218, v56, v218\n" \
"v_mul_f32_e32 v219, v56, v219\n" \
"v_mul_f32_e32 v220, v56, v220\n" \
"v_mul_f32_e32 v221, v56, v221\n" \
"v_mul_f32_e32 v222, v56, v222\n" \
"v_mul_f32_e32 v223, v56, v223\n" \
"v_cvt_pk_bf16_f32 v76, v216, v217\n" \
"v_cvt_pk_bf16_f32 v77, v218, v219\n" \
"v_cvt_pk_bf16_f32 v78, v220, v221\n" \
"v_cvt_pk_bf16_f32 v79, v222, v223\n" \
"buffer_store_dwordx4 v[76:79], v20, s[56:59], s70 offen offset:3072 sc1\n" \
"v_add_f32_e32 v224, v224, v160\n" \
"v_add_f32_e32 v225, v225, v161\n" \
"v_add_f32_e32 v226, v226, v162\n" \
"v_add_f32_e32 v227, v227, v163\n" \
"v_add_f32_e32 v228, v228, v164\n" \
"v_add_f32_e32 v229, v229, v165\n" \
"v_add_f32_e32 v230, v230, v166\n" \
"v_add_f32_e32 v231, v231, v167\n" \
"v_add_f32_e32 v232, v232, v168\n" \
"v_add_f32_e32 v233, v233, v169\n" \
"v_add_f32_e32 v234, v234, v170\n" \
"v_add_f32_e32 v235, v235, v171\n" \
"v_add_f32_e32 v236, v236, v172\n" \
"v_add_f32_e32 v237, v237, v173\n" \
"v_add_f32_e32 v238, v238, v174\n" \
"v_add_f32_e32 v239, v239, v175\n" \
"v_mfma_f32_16x16x32_bf16 v[252:255], a[28:31], v[124:127], v[252:255]\n" \
"buffer_load_dwordx4 v5, s[84:87], s92 offen offset:1024 lds\n" \
"v_add_f32_e32 v240, v240, v176\n" \
"v_add_f32_e32 v241, v241, v177\n" \
"v_add_f32_e32 v242, v242, v178\n" \
"v_add_f32_e32 v243, v243, v179\n" \
"v_add_f32_e32 v244, v244, v180\n" \
"v_add_f32_e32 v245, v245, v181\n" \
"v_add_f32_e32 v246, v246, v182\n" \
"v_add_f32_e32 v247, v247, v183\n" \
"v_add_f32_e32 v248, v248, v184\n" \
"v_add_f32_e32 v249, v249, v185\n" \
"v_add_f32_e32 v250, v250, v186\n" \
"v_add_f32_e32 v251, v251, v187\n" \
"v_add_f32_e32 v252, v252, v188\n" \
"v_add_f32_e32 v253, v253, v189\n" \
"v_add_f32_e32 v254, v254, v190\n" \
"v_add_f32_e32 v255, v255, v191\n" \
"v_mfma_f32_16x16x32_bf16 a[204:207], a[252:255], v[124:127], a[204:207]\n" \
"buffer_load_dwordx4 v6, s[84:87], s92 offen offset:2048 lds\n" \
"v_mfma_f32_16x16x32_bf16 a[208:211], a[216:219], v[64:67], a[208:211]\n" \
"v_mfma_f32_16x16x32_bf16 a[208:211], a[216:219], v[68:71], a[208:211]\n" \
"v_mfma_f32_16x16x32_bf16 a[208:211], a[216:219], v[72:75], a[208:211]\n" \
"buffer_load_dwordx4 v7, s[84:87], s92 offen offset:3072 lds\n" \
"v_mfma_f32_16x16x32_bf16 a[208:211], a[216:219], v[76:79], a[208:211]\n" \
"s_add_u32 m0, s93, 0x2000\n" \
"s_nop 0\n" \
"buffer_load_dwordx4 v8, s[84:87], s92 offen offset:0 lds\n" \
"v_accvgpr_read_b32 v57, a204\n" \
"v_accvgpr_read_b32 v23, a205\n" \
"v_add_f32_e32 v57, v57, v23\n" \
"v_add_f32_e32 v57, v57, v43\n" \
"v_max_f32_e32 v62, v62, v57\n" \
"v_rcp_f32_e32 v57, v57\n" \
"v_cmp_lt_f32_e64 s[72:73], s100, v62\n" \
"v_mul_f32_e32 v57, v57, v61\n" \
"v_mul_f32_e32 v224, v57, v224\n" \
"v_mul_f32_e32 v225, v57, v225\n" \
"v_mul_f32_e32 v226, v57, v226\n" \
"v_mul_f32_e32 v227, v57, v227\n" \
"v_mul_f32_e32 v228, v57, v228\n" \
"v_mul_f32_e32 v229, v57, v229\n" \
"v_mul_f32_e32 v230, v57, v230\n" \
"v_mul_f32_e32 v231, v57, v231\n" \
"v_cvt_pk_bf16_f32 v80, v224, v225\n" \
"v_cvt_pk_bf16_f32 v81, v226, v227\n" \
"v_cvt_pk_bf16_f32 v82, v228, v229\n" \
"v_cvt_pk_bf16_f32 v83, v230, v231\n" \
"buffer_store_dwordx4 v[80:83], v20, s[56:59], s71 offen offset:0 sc1\n" \
"v_mul_f32_e32 v232, v57, v232\n" \
"v_mfma_f32_16x16x32_bf16 a[208:211], a[216:219], v[80:83], a[208:211]\n" \
"v_mul_f32_e32 v233, v57, v233\n" \
"v_mul_f32_e32 v234, v57, v234\n" \
"v_mul_f32_e32 v235, v57, v235\n" \
"v_mul_f32_e32 v236, v57, v236\n" \
"v_mul_f32_e32 v237, v57, v237\n" \
"v_mul_f32_e32 v238, v57, v238\n" \
"v_mul_f32_e32 v239, v57, v239\n" \
"v_cvt_pk_bf16_f32 v84, v232, v233\n" \
"v_cvt_pk_bf16_f32 v85, v234, v235\n" \
"v_cvt_pk_bf16_f32 v86, v236, v237\n" \
"v_cvt_pk_bf16_f32 v87, v238, v239\n" \
"buffer_store_dwordx4 v[84:87], v20, s[56:59], s71 offen offset:1024 sc1\n" \
"v_mul_f32_e32 v240, v57, v240\n" \
"v_mfma_f32_16x16x32_bf16 a[208:211], a[216:219], v[84:87], a[208:211]\n" \
"v_mul_f32_e32 v241, v57, v241\n" \
"v_mul_f32_e32 v242, v57, v242\n" \
"v_mul_f32_e32 v243, v57, v243\n" \
"v_mul_f32_e32 v244, v57, v244\n" \
"v_mul_f32_e32 v245, v57, v245\n" \
"v_mul_f32_e32 v246, v57, v246\n" \
"v_mul_f32_e32 v247, v57, v247\n" \
"v_cvt_pk_bf16_f32 v96, v240, v241\n" \
"v_cvt_pk_bf16_f32 v97, v242, v243\n" \
"v_cvt_pk_bf16_f32 v98, v244, v245\n" \
"v_cvt_pk_bf16_f32 v99, v246, v247\n" \
"buffer_store_dwordx4 v[96:99], v20, s[56:59], s71 offen offset:2048 sc1\n" \
"v_mul_f32_e32 v248, v57, v248\n" \
"v_mfma_f32_16x16x32_bf16 a[208:211], a[216:219], v[96:99], a[208:211]\n" \
"v_mul_f32_e32 v249, v57, v249\n" \
"v_mul_f32_e32 v250, v57, v250\n" \
"v_mul_f32_e32 v251, v57, v251\n" \
"v_mul_f32_e32 v252, v57, v252\n" \
"v_mul_f32_e32 v253, v57, v253\n" \
"v_mul_f32_e32 v254, v57, v254\n" \
"v_mul_f32_e32 v255, v57, v255\n" \
"v_cvt_pk_bf16_f32 v100, v248, v249\n" \
"v_cvt_pk_bf16_f32 v101, v250, v251\n" \
"v_cvt_pk_bf16_f32 v102, v252, v253\n" \
"v_cvt_pk_bf16_f32 v103, v254, v255\n" \
"buffer_store_dwordx4 v[100:103], v20, s[56:59], s71 offen offset:3072 sc1\n" \
"s_nop 0\n" \
"v_mfma_f32_16x16x32_bf16 a[208:211], a[216:219], v[100:103], a[208:211]\n" \
"s_nop 7\n" \
"v_accvgpr_read_b32 v63, a208\n" \
"v_cmp_u_f32_e32 vcc, v63, v63\n" \
"s_nop 0\n" \
"s_or_b64 vcc, s[72:73], vcc\n" \
"s_cbranch_scc0 L_out_%=\n" \
"s_branch L_end_%=\n" \
"L_h1_%=:\n" \
"L_pf14:\n" \
"s_cbranch_execz L_pf18\n" \
"s_nop 0\n" \
"s_cmp_gt_i32 s63, 0\n" \
"s_cbranch_scc1 L_hc1_%=\n" \
"s_cmp_lt_i32 s63, 0\n" \
"s_cbranch_scc1 L_hn1_%=\n" \
"s_mov_b32 exec_hi, 0xf\n" \
"buffer_load_dword v22, v16, s[80:83], 0 offen\n" \
"s_mov_b32 exec_hi, -1\n" \
"s_branch L_hr1_%=\n" \
"L_hn1_%=:\n" \
"buffer_load_dword v22, v17, s[56:59], 0 offen sc1\n" \
"s_branch L_hr1_%=\n" \
"L_hs1_%=:\n" \
"s_sleep 1\n" \
"buffer_load_dword v22, v17, s[56:59], 0 offen sc1\n" \
"s_add_u32 s101, s101, 1\n" \
"s_waitcnt vmcnt(0)\n" \
"s_cmp_gt_u32 s101, 0x100000\n" \
"s_cbranch_scc1 L_hr1_%=\n" \
"L_hc1_%=:\n" \
"v_cmp_ne_u32_e32 vcc, s62, v22\n" \
"s_nop 4\n" \
"s_cbranch_vccnz L_hs1_%=\n" \
"s_branch L_hr1_%=\n" \
"L_h2_%=:\n" \
"s_cmp_gt_i32 s63, 0\n" \
"s_cbranch_scc1 L_hc2_%=\n" \
"s_cmp_lt_i32 s63, 0\n" \
"s_cbranch_scc1 L_hn2_%=\n" \
"s_mov_b32 exec_hi, 0xf\n" \
"buffer_load_dword v22, v16, s[80:83], 0 offen\n" \
"s_mov_b32 exec_hi, -1\n" \
"s_branch L_hr2_%=\n" \
"L_hn2_%=:\n" \
"buffer_load_dword v22, v17, s[56:59], 0 offen sc1\n" \
"s_branch L_hr2_%=\n" \
"L_hs2_%=:\n" \
"s_sleep 1\n" \
"buffer_load_dword v22, v17, s[56:59], 0 offen sc1\n" \
"s_add_u32 s101, s101, 1\n" \
"s_waitcnt vmcnt(0)\n" \
"s_cmp_gt_u32 s101, 0x100000\n" \
"s_cbranch_scc1 L_hr2_%=\n" \
"L_hc2_%=:\n" \
"v_cmp_ne_u32_e32 vcc, s62, v22\n" \
"s_nop 4\n" \
"s_cbranch_vccnz L_hs2_%=\n" \
"s_branch L_hr2_%=\n" \
"L_sa_%=:\n" \
"s_sleep 1\n" \
"buffer_load_dword v37, v38, s[56:59], s64 offen sc1\n" \
"s_add_u32 s101, s101, 1\n" \
"s_waitcnt vmcnt(0)\n" \
"v_cmp_ne_u32_e32 vcc, s62, v37\n" \
"s_cmp_gt_u32 s101, 0x100000\n" \
"s_cbranch_scc1 L_ra_%=\n" \
"s_nop 2\n" \
"s_cbranch_vccnz L_sa_%=\n" \
"s_branch L_ra_%=\n" \
"L_sb_%=:\n" \
"s_sleep 1\n" \
"buffer_load_dword v47, v38, s[56:59], s71 offen sc1\n" \
"s_add_u32 s101, s101, 1\n" \
"s_waitcnt vmcnt(0)\n" \
"v_cmp_ne_u32_e32 vcc, s62, v47\n" \
"s_cmp_gt_u32 s101, 0x100000\n" \
"s_cbranch_scc1 L_rb_%=\n" \
"s_nop 2\n" \
"s_cbranch_vccnz L_sb_%=\n" \
"s_branch L_rb_%=\n" \
"L_sp0_%=:\n" \
"s_sleep 1\n" \
"L_sx0_%=:\n" \
"L_pf15:\n" \
"s_cbranch_execz L_pf19\n" \
"s_nop 0\n" \
"buffer_load_dword v37, v40, s[56:59], s74 offen sc1\n" \
"s_add_u32 s101, s101, 1\n" \
"s_waitcnt vmcnt(0)\n" \
"v_cmp_eq_u32_e32 vcc, s62, v37\n" \
"s_nop 1\n" \
"s_and_b32 s98, vcc_lo, 0xfef\n" \
"s_andn2_b32 s99, s100, vcc_lo\n" \
"s_cselect_b32 s99, 0, 16\n" \
"s_or_b32 s98, s98, s99\n" \
"s_andn2_b32 s98, s98, s84\n" \
"s_cbranch_scc0 L_sn0_%=\n" \
"s_or_b32 s84, s84, s98\n" \
"s_bitcmp1_b32 s98, 0\n" \
"s_cbranch_scc0 L_xs00_%=\n" \
"buffer_load_dwordx4 v[116:119], v20, s[56:59], s63 offen offset:0 sc1\n" \
"L_xs00_%=:\n" \
"s_bitcmp1_b32 s98, 1\n" \
"s_cbranch_scc0 L_xs01_%=\n" \
"buffer_load_dwordx4 v[124:127], v20, s[56:59], s63 offen offset:2048 sc1\n" \
"L_xs01_%=:\n" \
"s_bitcmp1_b32 s98, 2\n" \
"s_cbranch_scc0 L_xs02_%=\n" \
"buffer_load_dwordx4 v[132:135], v38, s[56:59], s63 offen offset:0 sc1\n" \
"L_xs02_%=:\n" \
"s_bitcmp1_b32 s98, 3\n" \
"s_cbranch_scc0 L_xs03_%=\n" \
"buffer_load_dwordx4 v[140:143], v38, s[56:59], s63 offen offset:2048 sc1\n" \
"L_xs03_%=:\n" \
"s_bitcmp1_b32 s98, 4\n" \
"s_cbranch_scc0 L_xs04_%=\n" \
"buffer_load_dwordx4 v[176:179], v20, s[56:59], s75 offen offset:0 sc1\n" \
"buffer_load_dwordx4 v[248:251], v20, s[56:59], s75 offen offset:2048 sc1\n" \
"buffer_load_dwordx4 v[180:183], v38, s[56:59], s75 offen offset:0 sc1\n" \
"buffer_load_dwordx4 v[252:255], v38, s[56:59], s75 offen offset:2048 sc1\n" \
"buffer_load_dwordx4 v[112:115], v47, s[56:59], s75 offen offset:0 sc1\n" \
"buffer_load_dwordx4 v[120:123], v47, s[56:59], s75 offen offset:2048 sc1\n" \
"buffer_load_dwordx4 v[128:131], v54, s[56:59], s75 offen offset:0 sc1\n" \
"buffer_load_dwordx4 v[136:139], v54, s[56:59], s75 offen offset:2048 sc1\n" \
"L_xs04_%=:\n" \
"s_bitcmp1_b32 s98, 5\n" \
"s_cbranch_scc0 L_xs05_%=\n" \
"buffer_load_dwordx4 v[148:151], v47, s[56:59], s63 offen offset:0 sc1\n" \
"buffer_load_dwordx4 v[220:223], v47, s[56:59], s63 offen offset:1024 sc1\n" \
"L_xs05_%=:\n" \
"s_bitcmp1_b32 s98, 6\n" \
"s_cbranch_scc0 L_xs06_%=\n" \
"buffer_load_dwordx4 v[156:159], v47, s[56:59], s63 offen offset:2048 sc1\n" \
"buffer_load_dwordx4 v[228:231], v47, s[56:59], s63 offen offset:3072 sc1\n" \
"L_xs06_%=:\n" \
"s_bitcmp1_b32 s98, 7\n" \
"s_cbranch_scc0 L_xs07_%=\n" \
"buffer_load_dwordx4 v[164:167], v54, s[56:59], s63 offen offset:0 sc1\n" \
"buffer_load_dwordx4 v[236:239], v54, s[56:59], s63 offen offset:1024 sc1\n" \
"L_xs07_%=:\n" \
"s_bitcmp1_b32 s98, 8\n" \
"s_cbranch_scc0 L_xs08_%=\n" \
"buffer_load_dwordx4 v[188:191], v20, s[56:59], s63 offen offset:1024 sc1\n" \
"L_xs08_%=:\n" \
"s_bitcmp1_b32 s98, 9\n" \
"s_cbranch_scc0 L_xs09_%=\n" \
"buffer_load_dwordx4 v[196:199], v20, s[56:59], s63 offen offset:3072 sc1\n" \
"L_xs09_%=:\n" \
"s_bitcmp1_b32 s98, 10\n" \
"s_cbranch_scc0 L_xs010_%=\n" \
"buffer_load_dwordx4 v[204:207], v38, s[56:59], s63 offen offset:1024 sc1\n" \
"L_xs010_%=:\n" \
"s_bitcmp1_b32 s98, 11\n" \
"s_cbranch_scc0 L_xs011_%=\n" \
"buffer_load_dwordx4 v[212:215], v38, s[56:59], s63 offen offset:3072 sc1\n" \
"L_xs011_%=:\n" \
"L_sn0_%=:\n" \
"s_cmp_gt_u32 s101, 0x100000\n" \
"s_cbranch_scc1 L_se0_%=\n" \
"s_andn2_b32 s99, 0xff, s84\n" \
"s_cbranch_scc1 L_sp0_%=\n" \
"L_se0_%=:\n" \
"s_waitcnt vmcnt(0)\n" \
"s_branch L_rx0_%=\n" \
"L_sp1_%=:\n" \
"s_sleep 1\n" \
"L_sx1_%=:\n" \
"buffer_load_dword v37, v40, s[56:59], s74 offen sc1\n" \
"s_add_u32 s101, s101, 1\n" \
"s_waitcnt vmcnt(0)\n" \
"v_cmp_eq_u32_e32 vcc, s62, v37\n" \
"s_nop 1\n" \
"s_and_b32 s98, vcc_lo, 0xf00\n" \
"s_andn2_b32 s98, s98, s84\n" \
"s_cbranch_scc0 L_sn1_%=\n" \
"s_or_b32 s84, s84, s98\n" \
"s_bitcmp1_b32 s98, 8\n" \
"s_cbranch_scc0 L_xs18_%=\n" \
"buffer_load_dwordx4 v[188:191], v20, s[56:59], s63 offen offset:1024 sc1\n" \
"L_xs18_%=:\n" \
"s_bitcmp1_b32 s98, 9\n" \
"s_cbranch_scc0 L_xs19_%=\n" \
"buffer_load_dwordx4 v[196:199], v20, s[56:59], s63 offen offset:3072 sc1\n" \
"L_xs19_%=:\n" \
"s_bitcmp1_b32 s98, 10\n" \
"s_cbranch_scc0 L_xs110_%=\n" \
"buffer_load_dwordx4 v[204:207], v38, s[56:59], s63 offen offset:1024 sc1\n" \
"L_xs110_%=:\n" \
"s_bitcmp1_b32 s98, 11\n" \
"s_cbranch_scc0 L_xs111_%=\n" \
"buffer_load_dwordx4 v[212:215], v38, s[56:59], s63 offen offset:3072 sc1\n" \
"L_xs111_%=:\n" \
"L_sn1_%=:\n" \
"s_cmp_gt_u32 s101, 0x100000\n" \
"s_cbranch_scc1 L_se1_%=\n" \
"s_andn2_b32 s99, 0xf00, s84\n" \
"s_cbranch_scc1 L_sp1_%=\n" \
"L_se1_%=:\n" \
"s_waitcnt vmcnt(0)\n" \
"s_branch L_rx1_%=\n" \
"L_att_stub:\n" \
"s_mov_b32 s101, 0\n" \
"v_add_u32_e32 v32, 0x49000, v20\n" \
"v_readlane_b32 s96, v11, 32\n" \
"v_readlane_b32 s97, v11, 33\n" \
"buffer_load_dwordx4 a[220:223], v20, s[56:59], s60 offen offset:0 sc1\n" \
"buffer_load_dwordx4 a[236:239], v32, s[56:59], s60 offen offset:0 sc1\n" \
"buffer_load_dwordx4 a[224:227], v20, s[56:59], s60 offen offset:1024 sc1\n" \
"buffer_load_dwordx4 a[240:243], v32, s[56:59], s60 offen offset:1024 sc1\n" \
"buffer_load_dwordx4 a[228:231], v20, s[56:59], s60 offen offset:2048 sc1\n" \
"buffer_load_dwordx4 a[244:247], v32, s[56:59], s60 offen offset:2048 sc1\n" \
"buffer_load_dwordx4 a[232:235], v20, s[56:59], s60 offen offset:3072 sc1\n" \
"buffer_load_dwordx4 a[248:251], v32, s[56:59], s60 offen offset:3072 sc1\n" \
"buffer_load_dwordx4 a[32:35], v20, s[56:59], s96 offen offset:0 sc1\n" \
"buffer_load_dwordx4 a[40:43], v32, s[56:59], s96 offen offset:0 sc1\n" \
"buffer_load_dwordx4 a[48:51], v20, s[56:59], s97 offen offset:0 sc1\n" \
"buffer_load_dwordx4 a[56:59], v32, s[56:59], s97 offen offset:0 sc1\n" \
"buffer_load_dwordx4 a[36:39], v20, s[56:59], s96 offen offset:1024 sc1\n" \
"buffer_load_dwordx4 a[44:47], v32, s[56:59], s96 offen offset:1024 sc1\n" \
"buffer_load_dwordx4 a[52:55], v20, s[56:59], s97 offen offset:1024 sc1\n" \
"buffer_load_dwordx4 a[60:63], v32, s[56:59], s97 offen offset:1024 sc1\n" \
"buffer_load_dwordx4 v[64:67], v20, s[56:59], s61 offen offset:0 sc1\n" \
"buffer_load_dwordx4 v[68:71], v20, s[56:59], s61 offen offset:1024 sc1\n" \
"buffer_load_dwordx4 v[72:75], v20, s[56:59], s61 offen offset:2048 sc1\n" \
"buffer_load_dwordx4 v[76:79], v20, s[56:59], s61 offen offset:3072 sc1\n" \
"buffer_load_dwordx4 v[80:83], v32, s[56:59], s61 offen offset:0 sc1\n" \
"buffer_load_dwordx4 v[84:87], v32, s[56:59], s61 offen offset:1024 sc1\n" \
"buffer_load_dwordx4 v[96:99], v32, s[56:59], s61 offen offset:2048 sc1\n" \
"buffer_load_dwordx4 v[100:103], v32, s[56:59], s61 offen offset:3072 sc1\n" \
"s_waitcnt vmcnt(0)\n" \
"s_branch L_out_%=\n" \
"L_sw_%=:\n" \
"L_pf16:\n" \
"s_cbranch_execz L_pf20\n" \
"s_nop 0\n" \
"v_mfma_f32_16x16x32_bf16 v[72:75], a[128:131], a[16:19], v[36:39]\n" \
"s_add_u32 s68, s60, 0xfff6e000\n" \
"s_add_u32 s69, s60, 0xfffb7000\n" \
"s_add_u32 s70, s60, 0x0\n" \
"s_add_u32 s71, s60, 0x49000\n" \
"v_mfma_f32_16x16x32_bf16 v[72:75], a[132:135], a[20:23], v[72:75]\n" \
"s_xor_b32 m0, s65, 0x12000\n" \
"ds_read_b128 a[160:163], v49 offset:0\n" \
"v_mfma_f32_16x16x32_bf16 v[72:75], a[136:139], a[24:27], v[72:75]\n" \
"buffer_load_dwordx4 v50, s[8:11], s64 offen offset:0 sc1 lds\n" \
"v_mfma_f32_16x16x32_bf16 v[72:75], a[140:143], a[28:31], v[72:75]\n" \
"ds_read_b128 a[164:167], v49 offset:1024\n" \
"ds_read_b128 a[168:171], v49 offset:2048\n" \
"v_mfma_f32_16x16x32_bf16 v[76:79], a[156:159], a[28:31], v[36:39]\n" \
"ds_read_b128 a[172:175], v49 offset:3072\n" \
"v_cvt_pk_bf16_f32 v120, v88, v89\n" \
"v_mfma_f32_16x16x32_bf16 v[76:79], a[152:155], a[24:27], v[76:79]\n" \
"buffer_load_dwordx4 v50, s[8:11], s64 offen offset:1024 sc1 lds\n" \
"v_mfma_f32_16x16x32_bf16 v[76:79], a[148:151], a[20:23], v[76:79]\n" \
"ds_read_b128 a[176:179], v49 offset:4096\n" \
"v_cvt_pk_bf16_f32 v121, v90, v91\n" \
"v_mfma_f32_16x16x32_bf16 v[76:79], a[144:147], a[16:19], v[76:79]\n" \
"ds_read_b128 a[180:183], v49 offset:5120\n" \
"s_mov_b32 s65, s13\n" \
"v_mfma_f32_16x16x32_bf16 v[80:83], a[128:131], a[32:35], v[40:43]\n" \
"v_add_u32_e32 v63, s15, v20\n" \
"s_add_u32 s67, s14, 40960\n" \
"v_mfma_f32_16x16x32_bf16 v[80:83], a[132:135], a[36:39], v[80:83]\n" \
"v_add_u32_e32 v50, 0x6000, v63\n" \
"s_xor_b32 m0, s65, 0x8000\n" \
"v_mfma_f32_16x16x32_bf16 v[80:83], a[136:139], a[40:43], v[80:83]\n" \
"v_exp_f32_e32 v92, v92\n" \
"v_mfma_f32_16x16x32_bf16 v[80:83], a[140:143], a[44:47], v[80:83]\n" \
"buffer_load_dwordx4 v50, s[8:11], s67 offen offset:0 sc1 lds\n" \
"v_mfma_f32_16x16x32_bf16 v[84:87], a[156:159], a[44:47], v[40:43]\n" \
"v_exp_f32_e32 v93, v93\n" \
"v_mfma_f32_16x16x32_bf16 v[84:87], a[152:155], a[40:43], v[84:87]\n" \
"v_exp_f32_e32 v94, v94\n" \
"v_mfma_f32_16x16x32_bf16 v[84:87], a[148:151], a[36:39], v[84:87]\n" \
"v_exp_f32_e32 v95, v95\n" \
"v_mfma_f32_16x16x32_bf16 v[84:87], a[144:147], a[32:35], v[84:87]\n" \
"v_cvt_pk_bf16_f32 v122, v92, v93\n" \
"v_cvt_pk_bf16_f32 v123, v94, v95\n" \
"v_mfma_f32_16x16x32_bf16 v[88:91], a[128:131], a[48:51], v[44:47]\n" \
"ds_read_b128 a[184:187], v49 offset:6144\n" \
"ds_read_b128 a[188:191], v49 offset:7168\n" \
"v_mfma_f32_16x16x32_bf16 v[88:91], a[132:135], a[52:55], v[88:91]\n" \
"v_and_b32_e32 v96, v12, v96\n" \
"v_and_b32_e32 v97, v13, v97\n" \
"v_mfma_f32_16x16x32_bf16 v[88:91], a[136:139], a[56:59], v[88:91]\n" \
"v_and_b32_e32 v98, v14, v98\n" \
"v_and_b32_e32 v99, v15, v99\n" \
"v_mfma_f32_16x16x32_bf16 v[88:91], a[140:143], a[60:63], v[88:91]\n" \
"buffer_load_dwordx4 v50, s[8:11], s67 offen offset:1024 sc1 lds\n" \
"v_mfma_f32_16x16x32_bf16 v[92:95], a[156:159], a[60:63], v[44:47]\n" \
"v_exp_f32_e32 v72, v72\n" \
"v_mfma_f32_16x16x32_bf16 v[92:95], a[152:155], a[56:59], v[92:95]\n" \
"v_exp_f32_e32 v73, v73\n" \
"v_mfma_f32_16x16x32_bf16 v[92:95], a[148:151], a[52:55], v[92:95]\n" \
"v_exp_f32_e32 v74, v74\n" \
"v_mfma_f32_16x16x32_bf16 v[92:95], a[144:147], a[48:51], v[92:95]\n" \
"v_cvt_pk_bf16_f32 v108, v72, v73\n" \
"s_waitcnt lgkmcnt(2)\n" \
"v_mfma_f32_16x16x32_bf16 v[128:131], a[160:163], v[96:99], v[128:131]\n" \
"v_exp_f32_e32 v75, v75\n" \
"v_mfma_f32_16x16x32_bf16 v[132:135], a[164:167], v[96:99], v[132:135]\n" \
"v_exp_f32_e32 v76, v76\n" \
"v_mfma_f32_16x16x32_bf16 v[136:139], a[168:171], v[96:99], v[136:139]\n" \
"v_exp_f32_e32 v77, v77\n" \
"v_mfma_f32_16x16x32_bf16 v[140:143], a[172:175], v[96:99], v[140:143]\n" \
"v_exp_f32_e32 v78, v78\n" \
"v_mfma_f32_16x16x32_bf16 a[192:195], a[252:255], v[96:99], a[192:195]\n" \
"v_exp_f32_e32 v79, v79\n" \
"v_mfma_f32_16x16x32_bf16 v[160:163], a[160:163], v[104:107], v[160:163]\n" \
"v_cvt_pk_bf16_f32 v109, v74, v75\n" \
"v_cvt_pk_bf16_f32 v110, v76, v77\n" \
"v_cvt_pk_bf16_f32 v111, v78, v79\n" \
"v_and_b32_e32 v108, v12, v108\n" \
"v_and_b32_e32 v109, v13, v109\n" \
"v_and_b32_e32 v110, v14, v110\n" \
"v_and_b32_e32 v111, v15, v111\n" \
"v_exp_f32_e32 v80, v80\n" \
"v_exp_f32_e32 v81, v81\n" \
"v_mfma_f32_16x16x32_bf16 v[160:163], a[176:179], v[108:111], v[160:163]\n" \
"buffer_load_dwordx4 v50, s[8:11], s67 offen offset:2048 sc1 lds\n" \
"v_mfma_f32_16x16x32_bf16 v[164:167], a[180:183], v[108:111], v[164:167]\n" \
"buffer_load_dwordx4 v50, s[8:11], s67 offen offset:3072 sc1 lds\n" \
"v_mfma_f32_16x16x32_bf16 v[164:167], a[164:167], v[104:107], v[164:167]\n" \
"v_exp_f32_e32 v82, v82\n" \
"v_mfma_f32_16x16x32_bf16 v[168:171], a[168:171], v[104:107], v[168:171]\n" \
"v_cvt_pk_bf16_f32 v116, v80, v81\n" \
"s_waitcnt lgkmcnt(0)\n" \
"v_mfma_f32_16x16x32_bf16 v[168:171], a[184:187], v[108:111], v[168:171]\n" \
"v_exp_f32_e32 v83, v83\n" \
"v_mfma_f32_16x16x32_bf16 v[172:175], a[188:191], v[108:111], v[172:175]\n" \
"v_exp_f32_e32 v84, v84\n" \
"v_mfma_f32_16x16x32_bf16 v[172:175], a[172:175], v[104:107], v[172:175]\n" \
"v_exp_f32_e32 v85, v85\n" \
"v_smfmac_f32_16x16x64_bf16 a[196:199], a[212:215], v[104:111], v21\n" \
"v_mfma_f32_16x16x32_bf16 v[192:195], a[160:163], v[112:115], v[192:195]\n" \
"v_exp_f32_e32 v86, v86\n" \
"v_exp_f32_e32 v87, v87\n" \
"v_cvt_pk_bf16_f32 v117, v82, v83\n" \
"v_cvt_pk_bf16_f32 v118, v84, v85\n" \
"v_cvt_pk_bf16_f32 v119, v86, v87\n" \
"s_mov_b32 s64, s14\n" \
"v_add_u32_e32 v51, 0x12000, v63\n" \
"v_mfma_f32_16x16x32_bf16 v[192:195], a[176:179], v[116:119], v[192:195]\n" \
"s_xor_b32 m0, s65, 0xa000\n" \
"ds_read_b32 v58, v18 offset:512\n" \
"v_mfma_f32_16x16x32_bf16 v[196:199], a[180:183], v[116:119], v[196:199]\n" \
"buffer_load_dwordx4 v51, s[8:11], s64 offen offset:0 sc1 lds\n" \
"v_mfma_f32_16x16x32_bf16 v[196:199], a[164:167], v[112:115], v[196:199]\n" \
"v_exp_f32_e32 v88, v88\n" \
"v_mfma_f32_16x16x32_bf16 v[200:203], a[168:171], v[112:115], v[200:203]\n" \
"v_exp_f32_e32 v89, v89\n" \
"v_mfma_f32_16x16x32_bf16 v[200:203], a[184:187], v[116:119], v[200:203]\n" \
"v_exp_f32_e32 v90, v90\n" \
"v_mfma_f32_16x16x32_bf16 v[204:207], a[188:191], v[116:119], v[204:207]\n" \
"v_exp_f32_e32 v91, v91\n" \
"v_mfma_f32_16x16x32_bf16 v[204:207], a[172:175], v[112:115], v[204:207]\n" \
"v_exp_f32_e32 v92, v92\n" \
"v_smfmac_f32_16x16x64_bf16 a[200:203], a[212:215], v[112:119], v21\n" \
"v_mfma_f32_16x16x32_bf16 v[224:227], a[160:163], v[120:123], v[224:227]\n" \
"v_exp_f32_e32 v93, v93\n" \
"v_exp_f32_e32 v94, v94\n" \
"v_exp_f32_e32 v95, v95\n" \
"v_cvt_pk_bf16_f32 v124, v88, v89\n" \
"v_cvt_pk_bf16_f32 v125, v90, v91\n" \
"v_cvt_pk_bf16_f32 v126, v92, v93\n" \
"v_cvt_pk_bf16_f32 v127, v94, v95\n" \
"v_add_u32_e32 v52, 0x14000, v63\n" \
"v_add_u32_e32 v53, 0x20000, v63\n" \
"v_mfma_f32_16x16x32_bf16 v[224:227], a[176:179], v[124:127], v[224:227]\n" \
"buffer_load_dwordx4 v51, s[8:11], s64 offen offset:1024 sc1 lds\n" \
"v_mfma_f32_16x16x32_bf16 v[228:231], a[180:183], v[124:127], v[228:231]\n" \
"ds_read_b32 v59, v18 offset:640\n" \
"ds_read_b32 v60, v18 offset:768\n" \
"v_mfma_f32_16x16x32_bf16 v[228:231], a[164:167], v[120:123], v[228:231]\n" \
"ds_read_b32 v61, v18 offset:896\n" \
"ds_read_b128 a[128:131], v48 offset:0\n" \
"v_mfma_f32_16x16x32_bf16 v[232:235], a[168:171], v[120:123], v[232:235]\n" \
"ds_read_b128 a[132:135], v48 offset:1024\n" \
"ds_read_b128 a[136:139], v48 offset:2048\n" \
"v_mfma_f32_16x16x32_bf16 v[232:235], a[184:187], v[124:127], v[232:235]\n" \
"ds_read_b128 a[140:143], v48 offset:3072\n" \
"ds_read_b128 a[144:147], v48 offset:4096\n" \
"ds_read_b128 a[148:151], v48 offset:5120\n" \
"ds_read_b128 a[152:155], v48 offset:6144\n" \
"ds_read_b128 a[156:159], v48 offset:7168\n" \
"v_mfma_f32_16x16x32_bf16 v[236:239], a[188:191], v[124:127], v[236:239]\n" \
"buffer_load_dwordx4 v51, s[8:11], s64 offen offset:2048 sc1 lds\n" \
"v_mfma_f32_16x16x32_bf16 v[236:239], a[172:175], v[120:123], v[236:239]\n" \
"buffer_load_dwordx4 v51, s[8:11], s64 offen offset:3072 sc1 lds\n" \
"v_readlane_b32 s93, v11, 17\n" \
"v_readlane_b32 s92, v11, 22\n" \
"v_accvgpr_read_b32 v54, a192\n" \
"v_accvgpr_read_b32 v23, a193\n" \
"v_add_f32_e32 v54, v54, v23\n" \
"v_accvgpr_read_b32 v55, a196\n" \
"v_accvgpr_read_b32 v23, a197\n" \
"v_max_f32_e32 v62, v62, v54\n" \
"v_add_f32_e32 v55, v55, v23\n" \
"v_max_f32_e32 v62, v62, v55\n" \
"v_rcp_f32_e32 v54, v54\n" \
"v_rcp_f32_e32 v55, v55\n" \
"s_waitcnt lgkmcnt(11)\n" \
"v_mul_f32_e32 v54, v54, v58\n" \
"s_waitcnt lgkmcnt(10)\n" \
"v_mul_f32_e32 v55, v55, v59\n" \
"v_mul_f32_e32 v128, v54, v128\n" \
"v_mul_f32_e32 v129, v54, v129\n" \
"v_mul_f32_e32 v136, v54, v136\n" \
"v_mul_f32_e32 v137, v54, v137\n" \
"v_mul_f32_e32 v160, v55, v160\n" \
"v_mul_f32_e32 v161, v55, v161\n" \
"v_mul_f32_e32 v168, v55, v168\n" \
"v_mul_f32_e32 v169, v55, v169\n" \
"v_cvt_pk_bf16_f32 v128, v128, v129\n" \
"v_mul_f32_e32 v130, v54, v130\n" \
"v_mul_f32_e32 v131, v54, v131\n" \
"v_cvt_pk_bf16_f32 v136, v136, v137\n" \
"v_mul_f32_e32 v138, v54, v138\n" \
"v_mul_f32_e32 v139, v54, v139\n" \
"s_waitcnt vmcnt(10) lgkmcnt(0)\n" \
"s_barrier\n" \
"v_mfma_f32_16x16x32_bf16 v[80:83], a[128:131], a[32:35], v[40:43]\n" \
"ds_read_b128 a[160:163], v49 offset:8192\n" \
"ds_read_b128 a[164:167], v49 offset:9216\n" \
"v_mfma_f32_16x16x32_bf16 v[80:83], a[132:135], a[36:39], v[80:83]\n" \
"ds_read_b128 a[168:171], v49 offset:10240\n" \
"ds_read_b128 a[172:175], v49 offset:11264\n" \
"v_mfma_f32_16x16x32_bf16 v[80:83], a[136:139], a[40:43], v[80:83]\n" \
"s_xor_b32 m0, s65, 0x18000\n" \
"ds_read_b128 a[176:179], v49 offset:12288\n" \
"v_mfma_f32_16x16x32_bf16 v[80:83], a[140:143], a[44:47], v[80:83]\n" \
"buffer_load_dwordx4 v52, s[8:11], s64 offen offset:0 sc1 lds\n" \
"v_mfma_f32_16x16x32_bf16 v[84:87], a[156:159], a[44:47], v[40:43]\n" \
"ds_read_b128 a[180:183], v49 offset:13312\n" \
"ds_read_b128 a[184:187], v49 offset:14336\n" \
"v_mfma_f32_16x16x32_bf16 v[84:87], a[152:155], a[40:43], v[84:87]\n" \
"buffer_load_dwordx4 v52, s[8:11], s64 offen offset:1024 sc1 lds\n" \
"v_mfma_f32_16x16x32_bf16 v[84:87], a[148:151], a[36:39], v[84:87]\n" \
"ds_read_b128 a[188:191], v49 offset:15360\n" \
"v_add_u32_e32 v49, 0x10000, v48\n" \
"v_mfma_f32_16x16x32_bf16 v[84:87], a[144:147], a[32:35], v[84:87]\n" \
"buffer_load_dwordx4 v52, s[8:11], s64 offen offset:2048 sc1 lds\n" \
"v_mfma_f32_16x16x32_bf16 v[88:91], a[128:131], a[48:51], v[44:47]\n" \
"ds_read_b128 a[128:131], v48 offset:8192\n" \
"v_cvt_pk_bf16_f32 v160, v160, v161\n" \
"v_mfma_f32_16x16x32_bf16 v[88:91], a[132:135], a[52:55], v[88:91]\n" \
"ds_read_b128 a[132:135], v48 offset:9216\n" \
"v_mul_f32_e32 v162, v55, v162\n" \
"v_mfma_f32_16x16x32_bf16 v[88:91], a[136:139], a[56:59], v[88:91]\n" \
"buffer_load_dwordx4 v52, s[8:11], s64 offen offset:3072 sc1 lds\n" \
"v_mfma_f32_16x16x32_bf16 v[88:91], a[140:143], a[60:63], v[88:91]\n" \
"s_mov_b32 m0, s65\n" \
"ds_read_b128 a[136:139], v48 offset:10240\n" \
"v_mfma_f32_16x16x32_bf16 v[92:95], a[156:159], a[60:63], v[44:47]\n" \
"ds_read_b128 a[140:143], v48 offset:11264\n" \
"ds_read_b128 a[156:159], v48 offset:15360\n" \
"v_mfma_f32_16x16x32_bf16 v[92:95], a[152:155], a[56:59], v[92:95]\n" \
"buffer_load_dwordx4 v53, s[8:11], s64 offen offset:0 sc1 lds\n" \
"v_mfma_f32_16x16x32_bf16 v[92:95], a[148:151], a[52:55], v[92:95]\n" \
"ds_read_b128 a[148:151], v48 offset:13312\n" \
"ds_read_b128 a[152:155], v48 offset:14336\n" \
"v_mfma_f32_16x16x32_bf16 v[92:95], a[144:147], a[48:51], v[92:95]\n" \
"ds_read_b128 a[144:147], v48 offset:12288\n" \
"s_waitcnt lgkmcnt(12)\n" \
"v_mfma_f32_16x16x32_bf16 v[144:147], a[160:163], v[96:99], v[144:147]\n" \
"buffer_load_dwordx4 v53, s[8:11], s64 offen offset:1024 sc1 lds\n" \
"v_mfma_f32_16x16x32_bf16 v[148:151], a[164:167], v[96:99], v[148:151]\n" \
"v_exp_f32_e32 v88, v88\n" \
"v_mfma_f32_16x16x32_bf16 v[152:155], a[168:171], v[96:99], v[152:155]\n" \
"v_exp_f32_e32 v89, v89\n" \
"v_mfma_f32_16x16x32_bf16 v[156:159], a[172:175], v[96:99], v[156:159]\n" \
"v_exp_f32_e32 v90, v90\n" \
"v_mfma_f32_16x16x32_bf16 v[176:179], a[160:163], v[104:107], v[176:179]\n" \
"s_xor_b32 s65, s65, 0x8000\n" \
"s_waitcnt lgkmcnt(8)\n" \
"v_mfma_f32_16x16x32_bf16 v[176:179], a[176:179], v[108:111], v[176:179]\n" \
"buffer_load_dwordx4 v53, s[8:11], s64 offen offset:2048 sc1 lds\n" \
"v_mfma_f32_16x16x32_bf16 v[180:183], a[180:183], v[108:111], v[180:183]\n" \
"v_exp_f32_e32 v91, v91\n" \
"v_mfma_f32_16x16x32_bf16 v[180:183], a[164:167], v[104:107], v[180:183]\n" \
"buffer_load_dwordx4 v53, s[8:11], s64 offen offset:3072 sc1 lds\n" \
"v_mfma_f32_16x16x32_bf16 v[184:187], a[168:171], v[104:107], v[184:187]\n" \
"s_add_u32 m0, s93, 0x0\n" \
"s_add_u32 s64, s64, 0x10000\n" \
"v_mfma_f32_16x16x32_bf16 v[184:187], a[184:187], v[108:111], v[184:187]\n" \
"v_exp_f32_e32 v92, v92\n" \
"v_mfma_f32_16x16x32_bf16 v[188:191], a[188:191], v[108:111], v[188:191]\n" \
"buffer_load_dwordx4 v0, s[80:83], s92 offen offset:0 lds\n" \
"v_mfma_f32_16x16x32_bf16 v[188:191], a[172:175], v[104:107], v[188:191]\n" \
"v_exp_f32_e32 v93, v93\n" \
"v_mfma_f32_16x16x32_bf16 v[208:211], a[160:163], v[112:115], v[208:211]\n" \
"v_exp_f32_e32 v94, v94\n" \
"v_mfma_f32_16x16x32_bf16 v[208:211], a[176:179], v[116:119], v[208:211]\n" \
"v_exp_f32_e32 v95, v95\n" \
"v_mfma_f32_16x16x32_bf16 v[212:215], a[180:183], v[116:119], v[212:215]\n" \
"buffer_load_dwordx4 v1, s[80:83], s92 offen offset:1024 lds\n" \
"v_mfma_f32_16x16x32_bf16 v[212:215], a[164:167], v[112:115], v[212:215]\n" \
"v_exp_f32_e32 v80, v80\n" \
"v_mfma_f32_16x16x32_bf16 v[216:219], a[168:171], v[112:115], v[216:219]\n" \
"v_exp_f32_e32 v81, v81\n" \
"v_mfma_f32_16x16x32_bf16 v[216:219], a[184:187], v[116:119], v[216:219]\n" \
"buffer_load_dwordx4 v2, s[80:83], s92 offen offset:2048 lds\n" \
"v_mfma_f32_16x16x32_bf16 v[220:223], a[188:191], v[116:119], v[220:223]\n" \
"v_exp_f32_e32 v82, v82\n" \
"v_mfma_f32_16x16x32_bf16 v[220:223], a[172:175], v[112:115], v[220:223]\n" \
"L_pf17:\n" \
"s_cbranch_execz L_retA\n" \
"s_nop 0\n" \
"v_exp_f32_e32 v83, v83\n" \
"v_mfma_f32_16x16x32_bf16 v[240:243], a[160:163], v[120:123], v[240:243]\n" \
"buffer_load_dwordx4 v3, s[80:83], s92 offen offset:3072 lds\n" \
"v_mfma_f32_16x16x32_bf16 v[240:243], a[176:179], v[124:127], v[240:243]\n" \
"v_exp_f32_e32 v84, v84\n" \
"v_mfma_f32_16x16x32_bf16 v[244:247], a[180:183], v[124:127], v[244:247]\n" \
"v_exp_f32_e32 v85, v85\n" \
"v_mfma_f32_16x16x32_bf16 v[244:247], a[164:167], v[120:123], v[244:247]\n" \
"v_exp_f32_e32 v86, v86\n" \
"v_mfma_f32_16x16x32_bf16 v[248:251], a[168:171], v[120:123], v[248:251]\n" \
"v_exp_f32_e32 v87, v87\n" \
"v_mfma_f32_16x16x32_bf16 v[248:251], a[184:187], v[124:127], v[248:251]\n" \
"v_cvt_pk_bf16_f32 v112, v80, v81\n" \
"v_cvt_pk_bf16_f32 v113, v82, v83\n" \
"v_mfma_f32_16x16x32_bf16 v[252:255], a[188:191], v[124:127], v[252:255]\n" \
"v_cvt_pk_bf16_f32 v114, v84, v85\n" \
"v_cvt_pk_bf16_f32 v115, v86, v87\n" \
"v_mfma_f32_16x16x32_bf16 v[252:255], a[172:175], v[120:123], v[252:255]\n" \
"v_and_b32_e32 v112, v12, v112\n" \
"v_and_b32_e32 v113, v13, v113\n" \
"v_smfmac_f32_16x16x64_bf16 a[204:207], a[212:215], v[120:127], v21\n" \
"v_cvt_pk_bf16_f32 v120, v88, v89\n" \
"v_cvt_pk_bf16_f32 v121, v90, v91\n" \
"v_cvt_pk_bf16_f32 v122, v92, v93\n" \
"v_cvt_pk_bf16_f32 v123, v94, v95\n" \
"v_and_b32_e32 v114, v14, v114\n" \
"v_and_b32_e32 v115, v15, v115\n" \
"v_add_u32_e32 v48, s16, v20\n" \
"v_mul_f32_e32 v144, v54, v144\n" \
"v_mul_f32_e32 v145, v54, v145\n" \
"v_mul_f32_e32 v152, v54, v152\n" \
"v_mul_f32_e32 v153, v54, v153\n" \
"v_mul_f32_e32 v176, v55, v176\n" \
"v_mul_f32_e32 v177, v55, v177\n" \
"v_mul_f32_e32 v184, v55, v184\n" \
"v_mul_f32_e32 v185, v55, v185\n" \
"v_mul_f32_e32 v163, v55, v163\n" \
"v_cvt_pk_bf16_f32 v168, v168, v169\n" \
"v_mul_f32_e32 v170, v55, v170\n" \
"v_mul_f32_e32 v171, v55, v171\n" \
"v_cvt_pk_bf16_f32 v144, v144, v145\n" \
"v_mul_f32_e32 v146, v54, v146\n" \
"v_mul_f32_e32 v147, v54, v147\n" \
"v_cvt_pk_bf16_f32 v152, v152, v153\n" \
"v_mul_f32_e32 v154, v54, v154\n" \
"v_mul_f32_e32 v155, v54, v155\n" \
"v_cvt_pk_bf16_f32 v176, v176, v177\n" \
"v_mul_f32_e32 v178, v55, v178\n" \
"v_mul_f32_e32 v179, v55, v179\n" \
"v_cvt_pk_bf16_f32 v184, v184, v185\n" \
"v_mul_f32_e32 v186, v55, v186\n" \
"v_mul_f32_e32 v187, v55, v187\n" \
"v_cvt_pk_bf16_f32 v129, v130, v131\n" \
"v_mul_f32_e32 v132, v54, v132\n" \
"v_mul_f32_e32 v133, v54, v133\n" \
"v_mul_f32_e32 v134, v54, v134\n" \
"v_mul_f32_e32 v135, v54, v135\n" \
"v_cvt_pk_bf16_f32 v137, v138, v139\n" \
"v_mul_f32_e32 v140, v54, v140\n" \
"v_mul_f32_e32 v141, v54, v141\n" \
"v_mul_f32_e32 v142, v54, v142\n" \
"v_mul_f32_e32 v143, v54, v143\n" \
"v_cvt_pk_bf16_f32 v161, v162, v163\n" \
"v_mul_f32_e32 v164, v55, v164\n" \
"v_mul_f32_e32 v165, v55, v165\n" \
"v_mul_f32_e32 v166, v55, v166\n" \
"v_mul_f32_e32 v167, v55, v167\n" \
"v_cvt_pk_bf16_f32 v169, v170, v171\n" \
"v_mul_f32_e32 v172, v55, v172\n" \
"v_mul_f32_e32 v173, v55, v173\n" \
"v_mul_f32_e32 v174, v55, v174\n" \
"v_mul_f32_e32 v175, v55, v175\n" \
"v_cvt_pk_bf16_f32 v145, v146, v147\n" \
"v_mul_f32_e32 v148, v54, v148\n" \
"v_mul_f32_e32 v149, v54, v149\n" \
"v_mul_f32_e32 v150, v54, v150\n" \
"v_mul_f32_e32 v151, v54, v151\n" \
"v_cvt_pk_bf16_f32 v153, v154, v155\n" \
"v_mul_f32_e32 v156, v54, v156\n" \
"v_mul_f32_e32 v157, v54, v157\n" \
"v_mul_f32_e32 v158, v54, v158\n" \
"v_mul_f32_e32 v159, v54, v159\n" \
"v_cvt_pk_bf16_f32 v177, v178, v179\n" \
"v_mul_f32_e32 v180, v55, v180\n" \
"v_mul_f32_e32 v181, v55, v181\n" \
"v_mul_f32_e32 v182, v55, v182\n" \
"v_mul_f32_e32 v183, v55, v183\n" \
"v_cvt_pk_bf16_f32 v185, v186, v187\n" \
"v_mul_f32_e32 v188, v55, v188\n" \
"v_mul_f32_e32 v189, v55, v189\n" \
"v_mul_f32_e32 v190, v55, v190\n" \
"v_mul_f32_e32 v191, v55, v191\n" \
"v_cvt_pk_bf16_f32 v130, v132, v133\n" \
"v_cvt_pk_bf16_f32 v131, v134, v135\n" \
"buffer_store_dwordx4 v[128:131], v20, s[56:59], s68 offen offset:0 sc1\n" \
"v_cvt_pk_bf16_f32 v138, v140, v141\n" \
"v_mfma_f32_16x16x32_bf16 a[208:211], a[216:219], v[128:131], a[208:211]\n" \
"v_cvt_pk_bf16_f32 v139, v142, v143\n" \
"buffer_store_dwordx4 v[136:139], v20, s[56:59], s68 offen offset:1024 sc1\n" \
"s_waitcnt vmcnt(14) lgkmcnt(0)\n" \
"s_barrier\n" \
"v_mfma_f32_16x16x32_bf16 v[88:91], a[128:131], a[48:51], v[44:47]\n" \
"s_xor_b32 m0, s65, 0x12000\n" \
"ds_read_b128 a[160:163], v49 offset:0\n" \
"v_mfma_f32_16x16x32_bf16 v[88:91], a[132:135], a[52:55], v[88:91]\n" \
"buffer_load_dwordx4 v50, s[8:11], s64 offen offset:0 sc1 lds\n" \
"v_mfma_f32_16x16x32_bf16 v[88:91], a[136:139], a[56:59], v[88:91]\n" \
"ds_read_b128 a[164:167], v49 offset:1024\n" \
"ds_read_b128 a[168:171], v49 offset:2048\n" \
"v_mfma_f32_16x16x32_bf16 v[88:91], a[140:143], a[60:63], v[88:91]\n" \
"buffer_load_dwordx4 v50, s[8:11], s64 offen offset:1024 sc1 lds\n" \
"v_mfma_f32_16x16x32_bf16 v[92:95], a[156:159], a[60:63], v[44:47]\n" \
"ds_read_b128 a[172:175], v49 offset:3072\n" \
"ds_read_b128 a[176:179], v49 offset:8192\n" \
"v_mfma_f32_16x16x32_bf16 v[92:95], a[152:155], a[56:59], v[92:95]\n" \
"ds_read_b128 a[180:183], v49 offset:9216\n" \
"ds_read_b128 a[184:187], v49 offset:10240\n" \
"v_mfma_f32_16x16x32_bf16 v[92:95], a[148:151], a[52:55], v[92:95]\n" \
"ds_read_b128 a[188:191], v49 offset:11264\n" \
"ds_read_b128 a[0:3], v49 offset:4096\n" \
"v_mfma_f32_16x16x32_bf16 v[92:95], a[144:147], a[48:51], v[92:95]\n" \
"buffer_load_dwordx4 v50, s[8:11], s64 offen offset:2048 sc1 lds\n" \
"v_mfma_f32_16x16x32_bf16 v[32:35], a[220:223], a[64:67], 0\n" \
"ds_read_b128 a[4:7], v49 offset:5120\n" \
"ds_read_b128 a[8:11], v49 offset:6144\n" \
"v_mfma_f32_16x16x32_bf16 v[36:39], a[220:223], a[80:83], 0\n" \
"ds_read_b128 a[12:15], v49 offset:7168\n" \
"ds_read_b128 a[16:19], v49 offset:12288\n" \
"v_mfma_f32_16x16x32_bf16 v[40:43], a[220:223], a[96:99], 0\n" \
"ds_read_b128 a[20:23], v49 offset:13312\n" \
"ds_read_b128 a[24:27], v49 offset:14336\n" \
"v_mfma_f32_16x16x32_bf16 v[44:47], a[220:223], a[112:115], 0\n" \
"buffer_load_dwordx4 v50, s[8:11], s64 offen offset:3072 sc1 lds\n" \
"v_mfma_f32_16x16x32_bf16 v[32:35], a[224:227], a[68:71], v[32:35]\n" \
"s_xor_b32 m0, s65, 0xa000\n" \
"ds_read_b128 a[28:31], v49 offset:15360\n" \
"v_mfma_f32_16x16x32_bf16 v[36:39], a[224:227], a[84:87], v[36:39]\n" \
"v_exp_f32_e32 v88, v88\n" \
"v_mfma_f32_16x16x32_bf16 v[40:43], a[224:227], a[100:103], v[40:43]\n" \
"buffer_load_dwordx4 v51, s[8:11], s64 offen offset:0 sc1 lds\n" \
"v_mfma_f32_16x16x32_bf16 v[44:47], a[224:227], a[116:119], v[44:47]\n" \
"v_exp_f32_e32 v89, v89\n" \
"v_mfma_f32_16x16x32_bf16 v[32:35], a[228:231], a[72:75], v[32:35]\n" \
"v_exp_f32_e32 v90, v90\n" \
"v_mfma_f32_16x16x32_bf16 v[36:39], a[228:231], a[88:91], v[36:39]\n" \
"v_exp_f32_e32 v91, v91\n" \
"v_mfma_f32_16x16x32_bf16 v[40:43], a[228:231], a[104:107], v[40:43]\n" \
"buffer_load_dwordx4 v51, s[8:11], s64 offen offset:1024 sc1 lds\n" \
"v_mfma_f32_16x16x32_bf16 v[44:47], a[228:231], a[120:123], v[44:47]\n" \
"v_exp_f32_e32 v92, v92\n" \
"v_mfma_f32_16x16x32_bf16 v[32:35], a[232:235], a[76:79], v[32:35]\n" \
"v_exp_f32_e32 v93, v93\n" \
"v_mfma_f32_16x16x32_bf16 v[36:39], a[232:235], a[92:95], v[36:39]\n" \
"v_exp_f32_e32 v94, v94\n" \
"v_mfma_f32_16x16x32_bf16 v[40:43], a[232:235], a[108:111], v[40:43]\n" \
"buffer_load_dwordx4 v51, s[8:11], s64 offen offset:2048 sc1 lds\n" \
"v_mfma_f32_16x16x32_bf16 v[44:47], a[232:235], a[124:127], v[44:47]\n" \
"v_cvt_pk_bf16_f32 v124, v88, v89\n" \
"s_waitcnt lgkmcnt(3)\n" \
"v_mfma_f32_16x16x32_bf16 v[192:195], a[160:163], v[112:115], v[192:195]\n" \
"v_exp_f32_e32 v95, v95\n" \
"v_mfma_f32_16x16x32_bf16 v[196:199], a[164:167], v[112:115], v[196:199]\n" \
"buffer_load_dwordx4 v51, s[8:11], s64 offen offset:3072 sc1 lds\n" \
"v_mfma_f32_16x16x32_bf16 v[200:203], a[168:171], v[112:115], v[200:203]\n" \
"s_add_u32 m0, s93, 0x1000\n" \
"v_cvt_pk_bf16_f32 v125, v90, v91\n" \
"v_mfma_f32_16x16x32_bf16 v[204:207], a[172:175], v[112:115], v[204:207]\n" \
"v_cvt_pk_bf16_f32 v126, v92, v93\n" \
"v_cvt_pk_bf16_f32 v127, v94, v95\n" \
"v_mfma_f32_16x16x32_bf16 v[208:211], a[176:179], v[112:115], v[208:211]\n" \
"v_and_b32_e32 v124, v12, v124\n" \
"v_and_b32_e32 v125, v13, v125\n" \
"v_mfma_f32_16x16x32_bf16 v[212:215], a[180:183], v[112:115], v[212:215]\n" \
"buffer_load_dwordx4 v4, s[80:83], s92 offen offset:0 lds\n" \
"v_mfma_f32_16x16x32_bf16 v[216:219], a[184:187], v[112:115], v[216:219]\n" \
"v_and_b32_e32 v126, v14, v126\n" \
"v_and_b32_e32 v127, v15, v127\n" \
"v_mfma_f32_16x16x32_bf16 v[220:223], a[188:191], v[112:115], v[220:223]\n" \
"ds_read_b128 a[128:131], v48 offset:0\n" \
"ds_read_b128 a[132:135], v48 offset:1024\n" \
"v_mfma_f32_16x16x32_bf16 a[200:203], a[252:255], v[112:115], a[200:203]\n" \
"ds_read_b128 a[136:139], v48 offset:2048\n" \
"ds_read_b128 a[140:143], v48 offset:3072\n" \
"v_mfma_f32_16x16x32_bf16 v[224:227], a[160:163], v[120:123], v[224:227]\n" \
"buffer_load_dwordx4 v5, s[80:83], s92 offen offset:1024 lds\n" \
"v_mfma_f32_16x16x32_bf16 v[228:231], a[164:167], v[120:123], v[228:231]\n" \
"ds_read_b128 a[144:147], v48 offset:4096\n" \
"ds_read_b128 a[148:151], v48 offset:5120\n" \
"v_mfma_f32_16x16x32_bf16 v[232:235], a[168:171], v[120:123], v[232:235]\n" \
"ds_read_b128 a[152:155], v48 offset:6144\n" \
"ds_read_b128 a[156:159], v48 offset:7168\n" \
"v_mfma_f32_16x16x32_bf16 v[236:239], a[172:175], v[120:123], v[236:239]\n" \
"buffer_load_dwordx4 v6, s[80:83], s92 offen offset:2048 lds\n" \
"v_mfma_f32_16x16x32_bf16 v[240:243], a[176:179], v[120:123], v[240:243]\n" \
"v_cvt_pk_bf16_f32 v162, v164, v165\n" \
"v_cvt_pk_bf16_f32 v163, v166, v167\n" \
"buffer_store_dwordx4 v[160:163], v20, s[56:59], s69 offen offset:0 sc1\n" \
"v_mfma_f32_16x16x32_bf16 v[244:247], a[180:183], v[120:123], v[244:247]\n" \
"v_cvt_pk_bf16_f32 v170, v172, v173\n" \
"v_cvt_pk_bf16_f32 v171, v174, v175\n" \
"buffer_store_dwordx4 v[168:171], v20, s[56:59], s69 offen offset:1024 sc1\n" \
"v_mfma_f32_16x16x32_bf16 v[248:251], a[184:187], v[120:123], v[248:251]\n" \
"v_cvt_pk_bf16_f32 v146, v148, v149\n" \
"v_cvt_pk_bf16_f32 v147, v150, v151\n" \
"buffer_store_dwordx4 v[144:147], v20, s[56:59], s68 offen offset:2048 sc1\n" \
"v_mfma_f32_16x16x32_bf16 v[252:255], a[188:191], v[120:123], v[252:255]\n" \
"buffer_load_dwordx4 v7, s[80:83], s92 offen offset:3072 lds\n" \
"v_mfma_f32_16x16x32_bf16 a[204:207], a[252:255], v[120:123], a[204:207]\n" \
"v_cvt_pk_bf16_f32 v154, v156, v157\n" \
"v_cvt_pk_bf16_f32 v155, v158, v159\n" \
"buffer_store_dwordx4 v[152:155], v20, s[56:59], s68 offen offset:3072 sc1\n" \
"v_mfma_f32_16x16x32_bf16 v[224:227], a[0:3], v[124:127], v[224:227]\n" \
"v_cvt_pk_bf16_f32 v178, v180, v181\n" \
"v_cvt_pk_bf16_f32 v179, v182, v183\n" \
"buffer_store_dwordx4 v[176:179], v20, s[56:59], s69 offen offset:2048 sc1\n" \
"v_mfma_f32_16x16x32_bf16 v[228:231], a[4:7], v[124:127], v[228:231]\n" \
"v_cvt_pk_bf16_f32 v186, v188, v189\n" \
"v_cvt_pk_bf16_f32 v187, v190, v191\n" \
"buffer_store_dwordx4 v[184:187], v20, s[56:59], s69 offen offset:3072 sc1\n" \
"v_mfma_f32_16x16x32_bf16 v[232:235], a[8:11], v[124:127], v[232:235]\n" \
"s_add_u32 m0, s93, 0x2000\n" \
"v_accvgpr_read_b32 v56, a200\n" \
"v_mfma_f32_16x16x32_bf16 v[236:239], a[12:15], v[124:127], v[236:239]\n" \
"buffer_load_dwordx4 v8, s[80:83], s92 offen offset:0 lds\n" \
"v_mfma_f32_16x16x32_bf16 v[240:243], a[16:19], v[124:127], v[240:243]\n" \
"v_accvgpr_read_b32 v23, a201\n" \
"s_waitcnt lgkmcnt(0)\n" \
"v_mfma_f32_16x16x32_bf16 v[244:247], a[20:23], v[124:127], v[244:247]\n" \
"v_add_f32_e32 v56, v56, v23\n" \
"v_max_f32_e32 v62, v62, v56\n" \
"v_mfma_f32_16x16x32_bf16 v[248:251], a[24:27], v[124:127], v[248:251]\n" \
"v_rcp_f32_e32 v56, v56\n" \
"v_mfma_f32_16x16x32_bf16 v[252:255], a[28:31], v[124:127], v[252:255]\n" \
"v_mul_f32_e32 v56, v56, v60\n" \
"v_mul_f32_e32 v192, v56, v192\n" \
"v_mfma_f32_16x16x32_bf16 a[204:207], a[252:255], v[124:127], a[204:207]\n" \
"v_mul_f32_e32 v193, v56, v193\n" \
"v_cvt_pk_bf16_f32 v192, v192, v193\n" \
"v_mfma_f32_16x16x32_bf16 a[208:211], a[216:219], v[136:139], a[208:211]\n" \
"v_mul_f32_e32 v194, v56, v194\n" \
"v_mul_f32_e32 v195, v56, v195\n" \
"v_mfma_f32_16x16x32_bf16 a[208:211], a[216:219], v[160:163], a[208:211]\n" \
"v_cvt_pk_bf16_f32 v193, v194, v195\n" \
"v_mul_f32_e32 v196, v56, v196\n" \
"v_mfma_f32_16x16x32_bf16 a[208:211], a[216:219], v[168:171], a[208:211]\n" \
"v_mul_f32_e32 v197, v56, v197\n" \
"v_mul_f32_e32 v198, v56, v198\n" \
"v_mfma_f32_16x16x32_bf16 a[208:211], a[216:219], v[144:147], a[208:211]\n" \
"v_mul_f32_e32 v199, v56, v199\n" \
"v_mul_f32_e32 v200, v56, v200\n" \
"v_mfma_f32_16x16x32_bf16 a[208:211], a[216:219], v[152:155], a[208:211]\n" \
"v_mul_f32_e32 v201, v56, v201\n" \
"v_accvgpr_read_b32 v57, a204\n" \
"v_mfma_f32_16x16x32_bf16 a[208:211], a[216:219], v[176:179], a[208:211]\n" \
"v_accvgpr_read_b32 v23, a205\n" \
"v_cvt_pk_bf16_f32 v194, v196, v197\n" \
"v_mfma_f32_16x16x32_bf16 a[208:211], a[216:219], v[184:187], a[208:211]\n" \
"v_cvt_pk_bf16_f32 v195, v198, v199\n" \
"buffer_store_dwordx4 v[192:195], v20, s[56:59], s70 offen offset:0 sc1\n" \
"v_mfma_f32_32x32x16_bf16 v[128:143], a[216:219], a[216:219], 0\n" \
"v_cvt_pk_bf16_f32 v200, v200, v201\n" \
"v_mul_f32_e32 v202, v56, v202\n" \
"v_mul_f32_e32 v203, v56, v203\n" \
"v_add_f32_e32 v57, v57, v23\n" \
"v_cvt_pk_bf16_f32 v201, v202, v203\n" \
"v_mul_f32_e32 v204, v56, v204\n" \
"v_mfma_f32_16x16x32_bf16 a[208:211], a[216:219], v[192:195], a[208:211]\n" \
"v_mul_f32_e32 v205, v56, v205\n" \
"v_mul_f32_e32 v206, v56, v206\n" \
"v_mfma_f32_32x32x16_bf16 v[144:159], a[216:219], a[216:219], 0\n" \
"v_mul_f32_e32 v207, v56, v207\n" \
"v_mul_f32_e32 v208, v56, v208\n" \
"v_mul_f32_e32 v209, v56, v209\n" \
"v_max_f32_e32 v62, v62, v57\n" \
"v_cvt_pk_bf16_f32 v202, v204, v205\n" \
"v_cvt_pk_bf16_f32 v203, v206, v207\n" \
"buffer_store_dwordx4 v[200:203], v20, s[56:59], s70 offen offset:1024 sc1\n" \
"v_mfma_f32_32x32x16_bf16 v[160:175], a[216:219], a[216:219], 0\n" \
"v_cvt_pk_bf16_f32 v208, v208, v209\n" \
"v_mul_f32_e32 v210, v56, v210\n" \
"v_mul_f32_e32 v211, v56, v211\n" \
"v_rcp_f32_e32 v57, v57\n" \
"v_cvt_pk_bf16_f32 v209, v210, v211\n" \
"v_mfma_f32_16x16x32_bf16 a[208:211], a[216:219], v[200:203], a[208:211]\n" \
"v_mul_f32_e32 v212, v56, v212\n" \
"v_mul_f32_e32 v213, v56, v213\n" \
"v_mfma_f32_32x32x16_bf16 v[176:191], a[216:219], a[216:219], 0\n" \
"v_mul_f32_e32 v214, v56, v214\n" \
"v_mul_f32_e32 v215, v56, v215\n" \
"v_mul_f32_e32 v216, v56, v216\n" \
"v_mul_f32_e32 v217, v56, v217\n" \
"v_cvt_pk_bf16_f32 v210, v212, v213\n" \
"v_cvt_pk_bf16_f32 v211, v214, v215\n" \
"s_waitcnt vmcnt(21)\n" \
"s_barrier\n" \
"buffer_store_dwordx4 v[208:211], v20, s[56:59], s70 offen offset:2048 sc1\n" \
"v_mfma_f32_16x16x32_bf16 v[64:67], a[128:131], a[64:67], v[32:35]\n" \
"s_xor_b32 m0, s65, 0x18000\n" \
"v_cvt_pk_bf16_f32 v216, v216, v217\n" \
"v_mfma_f32_16x16x32_bf16 v[64:67], a[132:135], a[68:71], v[64:67]\n" \
"buffer_load_dwordx4 v52, s[8:11], s64 offen offset:0 sc1 lds\n" \
"v_mfma_f32_16x16x32_bf16 v[64:67], a[136:139], a[72:75], v[64:67]\n" \
"v_mul_f32_e32 v218, v56, v218\n" \
"v_mul_f32_e32 v219, v56, v219\n" \
"v_mfma_f32_16x16x32_bf16 v[64:67], a[140:143], a[76:79], v[64:67]\n" \
"buffer_load_dwordx4 v52, s[8:11], s64 offen offset:1024 sc1 lds\n" \
"v_mfma_f32_16x16x32_bf16 v[68:71], a[156:159], a[76:79], v[32:35]\n" \
"v_mul_f32_e32 v57, v57, v61\n" \
"v_cvt_pk_bf16_f32 v217, v218, v219\n" \
"v_mfma_f32_16x16x32_bf16 v[68:71], a[152:155], a[72:75], v[68:71]\n" \
"v_mul_f32_e32 v220, v56, v220\n" \
"v_mul_f32_e32 v221, v56, v221\n" \
"v_mfma_f32_16x16x32_bf16 v[68:71], a[148:151], a[68:71], v[68:71]\n" \
"v_mul_f32_e32 v222, v56, v222\n" \
"v_mul_f32_e32 v223, v56, v223\n" \
"v_mfma_f32_16x16x32_bf16 v[68:71], a[144:147], a[64:67], v[68:71]\n" \
"buffer_load_dwordx4 v52, s[8:11], s64 offen offset:2048 sc1 lds\n" \
"v_mfma_f32_16x16x32_bf16 v[72:75], a[128:131], a[80:83], v[36:39]\n" \
"v_mul_f32_e32 v224, v57, v224\n" \
"v_mul_f32_e32 v225, v57, v225\n" \
"v_mfma_f32_16x16x32_bf16 v[72:75], a[132:135], a[84:87], v[72:75]\n" \
"v_cvt_pk_bf16_f32 v218, v220, v221\n" \
"v_cvt_pk_bf16_f32 v219, v222, v223\n" \
"buffer_store_dwordx4 v[216:219], v20, s[56:59], s70 offen offset:3072 sc1\n" \
"v_mfma_f32_16x16x32_bf16 v[72:75], a[136:139], a[88:91], v[72:75]\n" \
"v_cvt_pk_bf16_f32 v224, v224, v225\n" \
"v_mul_f32_e32 v226, v57, v226\n" \
"v_mfma_f32_16x16x32_bf16 v[72:75], a[140:143], a[92:95], v[72:75]\n" \
"buffer_load_dwordx4 v52, s[8:11], s64 offen offset:3072 sc1 lds\n" \
"v_mfma_f32_16x16x32_bf16 v[76:79], a[156:159], a[92:95], v[36:39]\n" \
"s_mov_b32 m0, s65\n" \
"v_mul_f32_e32 v227, v57, v227\n" \
"v_mfma_f32_16x16x32_bf16 v[76:79], a[152:155], a[88:91], v[76:79]\n" \
"v_cvt_pk_bf16_f32 v225, v226, v227\n" \
"v_mul_f32_e32 v228, v57, v228\n" \
"v_mfma_f32_16x16x32_bf16 v[76:79], a[148:151], a[84:87], v[76:79]\n" \
"v_mul_f32_e32 v229, v57, v229\n" \
"v_mul_f32_e32 v230, v57, v230\n" \
"v_mfma_f32_16x16x32_bf16 v[76:79], a[144:147], a[80:83], v[76:79]\n" \
"buffer_load_dwordx4 v53, s[8:11], s64 offen offset:0 sc1 lds\n" \
"v_mfma_f32_16x16x32_bf16 v[80:83], a[128:131], a[96:99], v[40:43]\n" \
"v_mul_f32_e32 v231, v57, v231\n" \
"v_mul_f32_e32 v232, v57, v232\n" \
"v_mfma_f32_16x16x32_bf16 v[80:83], a[132:135], a[100:103], v[80:83]\n" \
"v_mul_f32_e32 v233, v57, v233\n" \
"v_cvt_pk_bf16_f32 v226, v228, v229\n" \
"v_mfma_f32_16x16x32_bf16 v[80:83], a[136:139], a[104:107], v[80:83]\n" \
"buffer_load_dwordx4 v53, s[8:11], s64 offen offset:1024 sc1 lds\n" \
"v_mfma_f32_16x16x32_bf16 v[80:83], a[140:143], a[108:111], v[80:83]\n" \
"v_cvt_pk_bf16_f32 v227, v230, v231\n" \
"v_cvt_pk_bf16_f32 v232, v232, v233\n" \
"buffer_store_dwordx4 v[224:227], v20, s[56:59], s71 offen offset:0 sc1\n" \
"v_mfma_f32_16x16x32_bf16 v[84:87], a[156:159], a[108:111], v[40:43]\n" \
"v_mul_f32_e32 v234, v57, v234\n" \
"v_mul_f32_e32 v235, v57, v235\n" \
"v_mfma_f32_16x16x32_bf16 v[84:87], a[152:155], a[104:107], v[84:87]\n" \
"v_cvt_pk_bf16_f32 v233, v234, v235\n" \
"v_mul_f32_e32 v236, v57, v236\n" \
"v_mfma_f32_16x16x32_bf16 v[84:87], a[148:151], a[100:103], v[84:87]\n" \
"buffer_load_dwordx4 v53, s[8:11], s64 offen offset:2048 sc1 lds\n" \
"v_mfma_f32_16x16x32_bf16 v[84:87], a[144:147], a[96:99], v[84:87]\n" \
"v_mul_f32_e32 v237, v57, v237\n" \
"v_mul_f32_e32 v238, v57, v238\n" \
"v_mfma_f32_16x16x32_bf16 v[88:91], a[128:131], a[112:115], v[44:47]\n" \
"ds_read_b128 a[128:131], v48 offset:8192\n" \
"v_mul_f32_e32 v239, v57, v239\n" \
"v_mfma_f32_16x16x32_bf16 v[88:91], a[132:135], a[116:119], v[88:91]\n" \
"ds_read_b128 a[132:135], v48 offset:9216\n" \
"v_mul_f32_e32 v240, v57, v240\n" \
"v_mfma_f32_16x16x32_bf16 v[88:91], a[136:139], a[120:123], v[88:91]\n" \
"ds_read_b128 a[136:139], v48 offset:10240\n" \
"v_mul_f32_e32 v241, v57, v241\n" \
"v_mfma_f32_16x16x32_bf16 v[88:91], a[140:143], a[124:127], v[88:91]\n" \
"ds_read_b128 a[140:143], v48 offset:11264\n" \
"v_cvt_pk_bf16_f32 v234, v236, v237\n" \
"v_mfma_f32_16x16x32_bf16 v[92:95], a[156:159], a[124:127], v[44:47]\n" \
"ds_read_b128 a[156:159], v48 offset:15360\n" \
"v_cvt_pk_bf16_f32 v235, v238, v239\n" \
"buffer_store_dwordx4 v[232:235], v20, s[56:59], s71 offen offset:1024 sc1\n" \
"v_mfma_f32_16x16x32_bf16 v[92:95], a[152:155], a[120:123], v[92:95]\n" \
"ds_read_b128 a[152:155], v48 offset:14336\n" \
"v_cvt_pk_bf16_f32 v240, v240, v241\n" \
"v_mfma_f32_16x16x32_bf16 v[92:95], a[148:151], a[116:119], v[92:95]\n" \
"ds_read_b128 a[148:151], v48 offset:13312\n" \
"v_mul_f32_e32 v242, v57, v242\n" \
"v_mfma_f32_16x16x32_bf16 v[92:95], a[144:147], a[112:115], v[92:95]\n" \
"ds_read_b128 a[144:147], v48 offset:12288\n" \
"v_mul_f32_e32 v243, v57, v243\n" \
"v_mfma_f32_16x16x32_bf16 a[208:211], a[216:219], v[208:211], a[208:211]\n" \
"v_cvt_pk_bf16_f32 v241, v242, v243\n" \
"v_mul_f32_e32 v244, v57, v244\n" \
"v_mfma_f32_16x16x32_bf16 a[208:211], a[216:219], v[216:219], a[208:211]\n" \
"v_mul_f32_e32 v245, v57, v245\n" \
"v_mul_f32_e32 v246, v57, v246\n" \
"v_mfma_f32_16x16x32_bf16 a[208:211], a[216:219], v[224:227], a[208:211]\n" \
"v_mul_f32_e32 v247, v57, v247\n" \
"v_mul_f32_e32 v248, v57, v248\n" \
"v_mfma_f32_16x16x32_bf16 a[208:211], a[216:219], v[232:235], a[208:211]\n" \
"v_mul_f32_e32 v249, v57, v249\n" \
"v_cvt_pk_bf16_f32 v242, v244, v245\n" \
"v_mfma_f32_32x32x16_bf16 v[192:207], a[216:219], a[216:219], 0\n" \
"v_cvt_pk_bf16_f32 v243, v246, v247\n" \
"buffer_store_dwordx4 v[240:243], v20, s[56:59], s71 offen offset:2048 sc1\n" \
"v_cvt_pk_bf16_f32 v248, v248, v249\n" \
"v_mfma_f32_16x16x32_bf16 a[208:211], a[216:219], v[240:243], a[208:211]\n" \
"v_mul_f32_e32 v250, v57, v250\n" \
"v_mul_f32_e32 v251, v57, v251\n" \
"v_mfma_f32_32x32x16_bf16 v[208:223], a[216:219], a[216:219], 0\n" \
"v_cvt_pk_bf16_f32 v249, v250, v251\n" \
"v_mul_f32_e32 v252, v57, v252\n" \
"v_mul_f32_e32 v253, v57, v253\n" \
"v_mul_f32_e32 v254, v57, v254\n" \
"v_mul_f32_e32 v255, v57, v255\n" \
"v_cvt_pk_bf16_f32 v250, v252, v253\n" \
"v_mfma_f32_32x32x16_bf16 v[224:239], a[216:219], a[216:219], 0\n" \
"v_cvt_pk_bf16_f32 v251, v254, v255\n" \
"buffer_store_dwordx4 v[248:251], v20, s[56:59], s71 offen offset:3072 sc1\n" \
"buffer_load_dwordx4 v53, s[8:11], s64 offen offset:3072 sc1 lds\n" \
"v_mfma_f32_16x16x32_bf16 a[208:211], a[216:219], v[248:251], a[208:211]\n" \
"v_cmp_lt_f32_e64 s[72:73], s100, v62\n" \
"v_readlane_b32 s95, v11, 2\n" \
"v_mfma_f32_32x32x16_bf16 v[240:255], a[216:219], a[216:219], 0\n" \
"v_exp_f32_e32 v64, v64\n" \
"v_exp_f32_e32 v65, v65\n" \
"v_exp_f32_e32 v66, v66\n" \
"v_mfma_f32_32x32x16_bf16 a[192:207], a[216:219], a[216:219], 0\n" \
"v_exp_f32_e32 v67, v67\n" \
"v_accvgpr_read_b32 v63, a208\n" \
"v_cmp_u_f32_e32 vcc, v63, v63\n" \
"v_exp_f32_e32 v68, v68\n" \
"s_or_b64 vcc, s[72:73], vcc\n" \
"s_cselect_b32 s95, 0x40000000, s95\n" \
"s_cselect_b32 s94, 0x7f800000, 0\n" \
"v_exp_f32_e32 v69, v69\n" \
"v_exp_f32_e32 v70, v70\n" \
"v_exp_f32_e32 v71, v71\n" \
"v_exp_f32_e32 v72, v72\n" \
"v_exp_f32_e32 v73, v73\n" \
"v_exp_f32_e32 v74, v74\n" \
"v_exp_f32_e32 v75, v75\n" \
"v_exp_f32_e32 v76, v76\n" \
"v_exp_f32_e32 v77, v77\n" \
"v_exp_f32_e32 v78, v78\n" \
"v_exp_f32_e32 v79, v79\n" \
"v_exp_f32_e32 v80, v80\n" \
"v_exp_f32_e32 v81, v81\n" \
"v_exp_f32_e32 v82, v82\n" \
"v_exp_f32_e32 v83, v83\n" \
"v_exp_f32_e32 v84, v84\n" \
"v_exp_f32_e32 v85, v85\n" \
"v_exp_f32_e32 v86, v86\n" \
"v_exp_f32_e32 v87, v87\n" \
"v_add_u32_e32 v49, 0x10000, v48\n" \
"s_xor_b32 s65, s65, 0x8000\n" \
"s_add_u32 s64, s64, 0x10000\n" \
"v_exp_f32_e32 v88, v88\n" \
"v_exp_f32_e32 v89, v89\n" \
"v_exp_f32_e32 v90, v90\n" \
"v_exp_f32_e32 v91, v91\n" \
"v_cvt_pk_bf16_f32 v96, v64, v65\n" \
"v_cvt_pk_bf16_f32 v97, v66, v67\n" \
"v_cvt_pk_bf16_f32 v98, v68, v69\n" \
"v_cvt_pk_bf16_f32 v99, v70, v71\n" \
"v_cvt_pk_bf16_f32 v104, v72, v73\n" \
"s_waitcnt lgkmcnt(0)\n" \
"v_cvt_pk_bf16_f32 v105, v74, v75\n" \
"v_cvt_pk_bf16_f32 v106, v76, v77\n" \
"v_cvt_pk_bf16_f32 v107, v78, v79\n" \
"v_cvt_pk_bf16_f32 v112, v80, v81\n" \
"v_cvt_pk_bf16_f32 v113, v82, v83\n" \
"v_cvt_pk_bf16_f32 v114, v84, v85\n" \
"v_cvt_pk_bf16_f32 v115, v86, v87\n" \
"v_xor_b32_e32 v48, 0x8000, v48\n" \
"s_sub_u32 s66, s19, 1\n" \
"s_cbranch_scc0 L_2_%=\n" \
"s_waitcnt vmcnt(14)\n" \
"s_barrier\n" \
"L_f1_%=:\n" \
"L_pf18:\n" \
"s_cbranch_execz L_retA\n" \
"s_nop 0\n" \
"ds_read_b128 a[160:163], v49 offset:0\n" \
"ds_read_b128 a[164:167], v49 offset:1024\n" \
"ds_read_b128 a[168:171], v49 offset:2048\n" \
"ds_read_b128 a[172:175], v49 offset:3072\n" \
"ds_read_b128 a[176:179], v49 offset:8192\n" \
"ds_read_b128 a[180:183], v49 offset:9216\n" \
"v_mfma_f32_16x16x32_bf16 v[72:75], a[128:131], a[80:83], v[36:39]\n" \
"s_add_u32 s68, s61, 0x0\n" \
"s_add_u32 s69, s61, 0x49000\n" \
"ds_read_b128 a[184:187], v49 offset:10240\n" \
"v_mfma_f32_16x16x32_bf16 v[72:75], a[132:135], a[84:87], v[72:75]\n" \
"ds_read_b128 a[188:191], v49 offset:11264\n" \
"v_add_u32_e32 v56, 0x49000, v20\n" \
"v_mfma_f32_16x16x32_bf16 v[72:75], a[136:139], a[88:91], v[72:75]\n" \
"v_readlane_b32 s97, v11, 32\n" \
"v_readlane_b32 s98, v11, 33\n" \
"v_mfma_f32_16x16x32_bf16 v[72:75], a[140:143], a[92:95], v[72:75]\n" \
"v_and_b32_e32 v96, v12, v96\n" \
"v_and_b32_e32 v97, v13, v97\n" \
"v_mfma_f32_16x16x32_bf16 v[76:79], a[156:159], a[92:95], v[36:39]\n" \
"buffer_load_dwordx4 a[32:35], v20, s[56:59], s97 offen offset:0 sc1\n" \
"v_mfma_f32_16x16x32_bf16 v[76:79], a[152:155], a[88:91], v[76:79]\n" \
"v_and_b32_e32 v98, v14, v98\n" \
"v_and_b32_e32 v99, v15, v99\n" \
"v_mfma_f32_16x16x32_bf16 v[76:79], a[148:151], a[84:87], v[76:79]\n" \
"ds_read_b128 a[0:3], v20 offset:8192\n" \
"v_mov_b32_e32 v61, s62\n" \
"v_mfma_f32_16x16x32_bf16 v[76:79], a[144:147], a[80:83], v[76:79]\n" \
"buffer_load_dwordx4 a[40:43], v56, s[56:59], s97 offen offset:0 sc1\n" \
"ds_read_b128 a[4:7], v20 offset:9216\n" \
"ds_read_b128 a[8:11], v20 offset:10240\n" \
"s_waitcnt lgkmcnt(10)\n" \
"v_mfma_f32_16x16x32_bf16 v[128:131], a[160:163], v[96:99], v[128:131]\n" \
"ds_read_b128 a[12:15], v20 offset:11264\n" \
"s_waitcnt lgkmcnt(10)\n" \
"v_mfma_f32_16x16x32_bf16 v[132:135], a[164:167], v[96:99], v[132:135]\n" \
"ds_read_b128 a[28:31], v20 offset:15360\n" \
"buffer_load_dwordx4 a[48:51], v20, s[56:59], s98 offen offset:0 sc1\n" \
"s_waitcnt lgkmcnt(10)\n" \
"v_mfma_f32_16x16x32_bf16 v[136:139], a[168:171], v[96:99], v[136:139]\n" \
"ds_read_b128 a[24:27], v20 offset:14336\n" \
"s_waitcnt lgkmcnt(10)\n" \
"v_mfma_f32_16x16x32_bf16 v[140:143], a[172:175], v[96:99], v[140:143]\n" \
"ds_read_b128 a[20:23], v20 offset:13312\n" \
"buffer_load_dwordx4 a[56:59], v56, s[56:59], s98 offen offset:0 sc1\n" \
"s_waitcnt lgkmcnt(9)\n" \
"v_mfma_f32_16x16x32_bf16 v[144:147], a[176:179], v[96:99], v[144:147]\n" \
"ds_read_b128 a[16:19], v20 offset:12288\n" \
"v_add_u32_e32 v50, s15, v48\n" \
"v_mfma_f32_16x16x32_bf16 v[148:151], a[180:183], v[96:99], v[148:151]\n" \
"v_xor_b32_e32 v48, 0x4000, v49\n" \
"s_waitcnt lgkmcnt(8)\n" \
"v_mfma_f32_16x16x32_bf16 v[152:155], a[184:187], v[96:99], v[152:155]\n" \
"buffer_load_dwordx4 a[36:39], v20, s[56:59], s97 offen offset:1024 sc1\n" \
"v_mfma_f32_16x16x32_bf16 v[156:159], a[188:191], v[96:99], v[156:159]\n" \
"ds_read_b32 v58, v18 offset:256\n" \
"ds_read_b32 v59, v18 offset:384\n" \
"v_mfma_f32_16x16x32_bf16 a[192:195], a[252:255], v[96:99], a[192:195]\n" \
"ds_read_b128 a[96:99], v48 offset:4096\n" \
"ds_read_b128 a[100:103], v48 offset:5120\n" \
"buffer_load_dwordx4 a[44:47], v56, s[56:59], s97 offen offset:1024 sc1\n" \
"v_mfma_f32_16x16x32_bf16 v[160:163], a[160:163], v[104:107], v[160:163]\n" \
"ds_read_b128 a[104:107], v48 offset:6144\n" \
"ds_read_b128 a[108:111], v48 offset:7168\n" \
"v_mfma_f32_16x16x32_bf16 v[164:167], a[164:167], v[104:107], v[164:167]\n" \
"ds_read_b128 a[112:115], v48 offset:12288\n" \
"ds_read_b128 a[116:119], v48 offset:13312\n" \
"buffer_load_dwordx4 a[52:55], v20, s[56:59], s98 offen offset:1024 sc1\n" \
"v_mfma_f32_16x16x32_bf16 v[168:171], a[168:171], v[104:107], v[168:171]\n" \
"ds_read_b128 a[120:123], v48 offset:14336\n" \
"ds_read_b128 a[124:127], v48 offset:15360\n" \
"v_mfma_f32_16x16x32_bf16 v[172:175], a[172:175], v[104:107], v[172:175]\n" \
"ds_read_b128 a[128:131], v49 offset:4096\n" \
"ds_read_b128 a[132:135], v49 offset:5120\n" \
"buffer_load_dwordx4 a[60:63], v56, s[56:59], s98 offen offset:1024 sc1\n" \
"v_mfma_f32_16x16x32_bf16 v[176:179], a[176:179], v[104:107], v[176:179]\n" \
"ds_read_b128 a[136:139], v49 offset:6144\n" \
"ds_read_b128 a[140:143], v49 offset:7168\n" \
"v_mfma_f32_16x16x32_bf16 v[180:183], a[180:183], v[104:107], v[180:183]\n" \
"ds_read_b128 a[144:147], v49 offset:12288\n" \
"ds_read_b128 a[148:151], v49 offset:13312\n" \
"s_waitcnt vmcnt(8)\n" \
"s_mov_b64 exec, 1\n" \
"buffer_store_dword v61, off, s[56:59], s95 offset:0 sc1\n" \
"s_mov_b64 exec, -1\n" \
"v_mfma_f32_16x16x32_bf16 v[184:187], a[184:187], v[104:107], v[184:187]\n" \
"ds_read_b128 a[152:155], v49 offset:14336\n" \
"ds_read_b128 a[156:159], v49 offset:15360\n" \
"v_mfma_f32_16x16x32_bf16 v[188:191], a[188:191], v[104:107], v[188:191]\n" \
"v_accvgpr_read_b32 v40, a200\n" \
"v_accvgpr_read_b32 v23, a201\n" \
"v_mfma_f32_16x16x32_bf16 a[196:199], a[252:255], v[104:107], a[196:199]\n" \
"v_add_f32_e32 v40, v40, v23\n" \
"s_waitcnt lgkmcnt(15)\n" \
"v_mfma_f32_16x16x32_bf16 v[80:83], a[0:3], a[64:67], v[32:35]\n" \
"buffer_load_dwordx4 a[220:223], v20, s[56:59], s60 offen offset:0 sc1\n" \
"v_mfma_f32_16x16x32_bf16 v[80:83], a[4:7], a[68:71], v[80:83]\n" \
"v_accvgpr_read_b32 v41, a204\n" \
"v_accvgpr_read_b32 v23, a205\n" \
"v_mfma_f32_16x16x32_bf16 v[80:83], a[8:11], a[72:75], v[80:83]\n" \
"v_add_f32_e32 v41, v41, v23\n" \
"v_add_u32_e32 v51, 0x10000, v50\n" \
"v_mfma_f32_16x16x32_bf16 v[80:83], a[12:15], a[76:79], v[80:83]\n" \
"buffer_load_dwordx4 a[236:239], v56, s[56:59], s60 offen offset:0 sc1\n" \
"v_mfma_f32_16x16x32_bf16 v[84:87], a[28:31], a[76:79], v[32:35]\n" \
"v_add_f32_e32 v40, s94, v40\n" \
"v_add_f32_e32 v41, s94, v41\n" \
"v_mfma_f32_16x16x32_bf16 v[84:87], a[24:27], a[72:75], v[84:87]\n" \
"ds_write_b128 v50, v[192:195] offset:0\n" \
"buffer_load_dwordx4 a[224:227], v20, s[56:59], s60 offen offset:1024 sc1\n" \
"v_mfma_f32_16x16x32_bf16 v[84:87], a[20:23], a[68:71], v[84:87]\n" \
"ds_write_b128 v50, v[196:199] offset:1024\n" \
"v_mfma_f32_16x16x32_bf16 v[84:87], a[16:19], a[64:67], v[84:87]\n" \
"ds_write_b128 v50, v[200:203] offset:2048\n" \
"buffer_load_dwordx4 a[240:243], v56, s[56:59], s60 offen offset:1024 sc1\n" \
"v_mfma_f32_16x16x32_bf16 v[88:91], a[0:3], a[80:83], v[36:39]\n" \
"ds_write_b128 v50, v[204:207] offset:3072\n" \
"v_mfma_f32_16x16x32_bf16 v[88:91], a[4:7], a[84:87], v[88:91]\n" \
"ds_write_b128 v50, v[208:211] offset:8192\n" \
"buffer_load_dwordx4 a[228:231], v20, s[56:59], s60 offen offset:2048 sc1\n" \
"v_mfma_f32_16x16x32_bf16 v[88:91], a[8:11], a[88:91], v[88:91]\n" \
"ds_write_b128 v50, v[212:215] offset:9216\n" \
"v_mfma_f32_16x16x32_bf16 v[88:91], a[12:15], a[92:95], v[88:91]\n" \
"ds_write_b128 v50, v[216:219] offset:10240\n" \
"buffer_load_dwordx4 a[244:247], v56, s[56:59], s60 offen offset:2048 sc1\n" \
"v_mfma_f32_16x16x32_bf16 v[92:95], a[28:31], a[92:95], v[36:39]\n" \
"ds_write_b128 v50, v[220:223] offset:11264\n" \
"ds_write_b128 v51, v[224:227] offset:0\n" \
"ds_write_b128 v51, v[228:231] offset:1024\n" \
"buffer_load_dwordx4 a[232:235], v20, s[56:59], s60 offen offset:3072 sc1\n" \
"ds_write_b128 v51, v[232:235] offset:2048\n" \
"ds_write_b128 v51, v[236:239] offset:3072\n" \
"ds_write_b128 v51, v[240:243] offset:8192\n" \
"ds_write_b128 v51, v[244:247] offset:9216\n" \
"buffer_load_dwordx4 a[248:251], v56, s[56:59], s60 offen offset:3072 sc1\n" \
"ds_write_b128 v51, v[248:251] offset:10240\n" \
"ds_write_b128 v51, v[252:255] offset:11264\n" \
"L_pf19:\n" \
"s_cbranch_execz L_retA\n" \
"s_nop 0\n" \
"ds_write_b64 v19, v[40:41]\n" \
"v_mfma_f32_16x16x32_bf16 v[92:95], a[24:27], a[88:91], v[92:95]\n" \
"v_xor_b32_e32 v52, 0x4000, v50\n" \
"v_xor_b32_e32 v53, 0x4000, v51\n" \
"v_mfma_f32_16x16x32_bf16 v[92:95], a[20:23], a[84:87], v[92:95]\n" \
"v_exp_f32_e32 v72, v72\n" \
"v_mfma_f32_16x16x32_bf16 v[92:95], a[16:19], a[80:83], v[92:95]\n" \
"v_exp_f32_e32 v73, v73\n" \
"v_exp_f32_e32 v74, v74\n" \
"v_exp_f32_e32 v75, v75\n" \
"v_exp_f32_e32 v76, v76\n" \
"v_exp_f32_e32 v77, v77\n" \
"v_exp_f32_e32 v78, v78\n" \
"v_exp_f32_e32 v79, v79\n" \
"v_cvt_pk_bf16_f32 v108, v72, v73\n" \
"v_cvt_pk_bf16_f32 v109, v74, v75\n" \
"v_cvt_pk_bf16_f32 v110, v76, v77\n" \
"v_cvt_pk_bf16_f32 v111, v78, v79\n" \
"v_and_b32_e32 v108, v12, v108\n" \
"v_and_b32_e32 v109, v13, v109\n" \
"v_and_b32_e32 v110, v14, v110\n" \
"v_and_b32_e32 v111, v15, v111\n" \
"v_readlane_b32 s93, v11, 18\n" \
"v_readlane_b32 s84, v11, 13\n" \
"v_readlane_b32 s85, v11, 14\n" \
"v_readlane_b32 s86, v11, 15\n" \
"s_mov_b32 s87, 0x20000\n" \
"v_readlane_b32 s92, v11, 16\n" \
"s_add_u32 m0, s93, 0x0\n" \
"v_xor_b32_e32 v63, 0x400, v19\n" \
"s_waitcnt lgkmcnt(0)\n" \
"s_barrier\n" \
"ds_read_b128 v[192:195], v52 offset:0\n" \
"ds_read_b128 v[196:199], v52 offset:1024\n" \
"ds_read_b128 v[200:203], v52 offset:2048\n" \
"ds_read_b128 v[204:207], v52 offset:3072\n" \
"ds_read_b128 v[208:211], v52 offset:8192\n" \
"ds_read_b128 v[212:215], v52 offset:9216\n" \
"ds_read_b128 v[216:219], v52 offset:10240\n" \
"ds_read_b128 v[220:223], v52 offset:11264\n" \
"ds_read_b128 v[224:227], v53 offset:0\n" \
"ds_read_b128 v[228:231], v53 offset:1024\n" \
"ds_read_b128 v[232:235], v53 offset:2048\n" \
"ds_read_b128 v[236:239], v53 offset:3072\n" \
"ds_read_b128 v[240:243], v53 offset:8192\n" \
"ds_read_b128 v[244:247], v53 offset:9216\n" \
"ds_read_b128 v[248:251], v53 offset:10240\n" \
"ds_read_b128 v[252:255], v53 offset:11264\n" \
"v_mfma_f32_16x16x32_bf16 v[160:163], a[128:131], v[108:111], v[160:163]\n" \
"buffer_load_dwordx4 v0, s[84:87], s92 offen offset:0 lds\n" \
"v_mfma_f32_16x16x32_bf16 v[164:167], a[132:135], v[108:111], v[164:167]\n" \
"v_exp_f32_e32 v80, v80\n" \
"v_mfma_f32_16x16x32_bf16 v[168:171], a[136:139], v[108:111], v[168:171]\n" \
"v_exp_f32_e32 v81, v81\n" \
"v_mfma_f32_16x16x32_bf16 v[172:175], a[140:143], v[108:111], v[172:175]\n" \
"buffer_load_dwordx4 v1, s[84:87], s92 offen offset:1024 lds\n" \
"v_mfma_f32_16x16x32_bf16 v[176:179], a[144:147], v[108:111], v[176:179]\n" \
"ds_read_b64 v[42:43], v63\n" \
"v_cvt_pk_bf16_f32 v116, v80, v81\n" \
"v_mfma_f32_16x16x32_bf16 v[180:183], a[148:151], v[108:111], v[180:183]\n" \
"v_exp_f32_e32 v82, v82\n" \
"v_mfma_f32_16x16x32_bf16 v[184:187], a[152:155], v[108:111], v[184:187]\n" \
"buffer_load_dwordx4 v2, s[84:87], s92 offen offset:2048 lds\n" \
"v_mfma_f32_16x16x32_bf16 v[188:191], a[156:159], v[108:111], v[188:191]\n" \
"v_exp_f32_e32 v83, v83\n" \
"v_mfma_f32_16x16x32_bf16 a[196:199], a[252:255], v[108:111], a[196:199]\n" \
"v_exp_f32_e32 v84, v84\n" \
"v_exp_f32_e32 v85, v85\n" \
"v_exp_f32_e32 v86, v86\n" \
"s_waitcnt lgkmcnt(1)\n" \
"s_barrier\n" \
"buffer_load_dwordx4 v3, s[84:87], s92 offen offset:3072 lds\n" \
"s_add_u32 m0, s93, 0x1000\n" \
"v_exp_f32_e32 v87, v87\n" \
"v_cvt_pk_bf16_f32 v117, v82, v83\n" \
"v_cvt_pk_bf16_f32 v118, v84, v85\n" \
"v_cvt_pk_bf16_f32 v119, v86, v87\n" \
"v_exp_f32_e32 v88, v88\n" \
"v_exp_f32_e32 v89, v89\n" \
"v_mfma_f32_16x16x32_bf16 v[128:131], a[96:99], v[116:119], v[128:131]\n" \
"buffer_load_dwordx4 v4, s[84:87], s92 offen offset:0 lds\n" \
"v_mfma_f32_16x16x32_bf16 v[132:135], a[100:103], v[116:119], v[132:135]\n" \
"v_exp_f32_e32 v90, v90\n" \
"v_mfma_f32_16x16x32_bf16 v[136:139], a[104:107], v[116:119], v[136:139]\n" \
"v_exp_f32_e32 v91, v91\n" \
"v_mfma_f32_16x16x32_bf16 v[140:143], a[108:111], v[116:119], v[140:143]\n" \
"buffer_load_dwordx4 v5, s[84:87], s92 offen offset:1024 lds\n" \
"v_mfma_f32_16x16x32_bf16 v[144:147], a[112:115], v[116:119], v[144:147]\n" \
"v_exp_f32_e32 v92, v92\n" \
"v_mfma_f32_16x16x32_bf16 v[148:151], a[116:119], v[116:119], v[148:151]\n" \
"v_exp_f32_e32 v93, v93\n" \
"v_mfma_f32_16x16x32_bf16 v[152:155], a[120:123], v[116:119], v[152:155]\n" \
"buffer_load_dwordx4 v6, s[84:87], s92 offen offset:2048 lds\n" \
"v_mfma_f32_16x16x32_bf16 v[156:159], a[124:127], v[116:119], v[156:159]\n" \
"v_exp_f32_e32 v94, v94\n" \
"v_mfma_f32_16x16x32_bf16 a[192:195], a[252:255], v[116:119], a[192:195]\n" \
"v_exp_f32_e32 v95, v95\n" \
"v_cvt_pk_bf16_f32 v124, v88, v89\n" \
"v_cvt_pk_bf16_f32 v125, v90, v91\n" \
"v_cvt_pk_bf16_f32 v126, v92, v93\n" \
"v_cvt_pk_bf16_f32 v127, v94, v95\n" \
"v_add_f32_e32 v160, v160, v224\n" \
"v_add_f32_e32 v161, v161, v225\n" \
"v_add_f32_e32 v162, v162, v226\n" \
"v_add_f32_e32 v163, v163, v227\n" \
"v_add_f32_e32 v164, v164, v228\n" \
"v_add_f32_e32 v165, v165, v229\n" \
"v_mfma_f32_16x16x32_bf16 v[160:163], a[96:99], v[124:127], v[160:163]\n" \
"buffer_load_dwordx4 v7, s[84:87], s92 offen offset:3072 lds\n" \
"s_add_u32 m0, s93, 0x2000\n" \
"v_add_f32_e32 v166, v166, v230\n" \
"v_add_f32_e32 v167, v167, v231\n" \
"v_add_f32_e32 v168, v168, v232\n" \
"v_add_f32_e32 v169, v169, v233\n" \
"v_mfma_f32_16x16x32_bf16 v[164:167], a[100:103], v[124:127], v[164:167]\n" \
"v_add_f32_e32 v170, v170, v234\n" \
"v_add_f32_e32 v171, v171, v235\n" \
"v_add_f32_e32 v172, v172, v236\n" \
"v_add_f32_e32 v173, v173, v237\n" \
"v_mfma_f32_16x16x32_bf16 v[168:171], a[104:107], v[124:127], v[168:171]\n" \
"buffer_load_dwordx4 v8, s[84:87], s92 offen offset:0 lds\n" \
"v_add_f32_e32 v174, v174, v238\n" \
"v_add_f32_e32 v175, v175, v239\n" \
"v_accvgpr_read_b32 v54, a192\n" \
"v_accvgpr_read_b32 v23, a193\n" \
"v_mfma_f32_16x16x32_bf16 v[172:175], a[108:111], v[124:127], v[172:175]\n" \
"v_add_f32_e32 v54, v54, v23\n" \
"s_waitcnt lgkmcnt(0)\n" \
"v_add_f32_e32 v54, v54, v42\n" \
"v_max_f32_e32 v62, v62, v54\n" \
"v_add_f32_e32 v176, v176, v240\n" \
"v_add_f32_e32 v177, v177, v241\n" \
"v_add_f32_e32 v178, v178, v242\n" \
"v_add_f32_e32 v179, v179, v243\n" \
"v_rcp_f32_e32 v54, v54\n" \
"v_add_f32_e32 v128, v128, v192\n" \
"v_mfma_f32_16x16x32_bf16 v[176:179], a[112:115], v[124:127], v[176:179]\n" \
"v_add_f32_e32 v129, v129, v193\n" \
"v_add_f32_e32 v130, v130, v194\n" \
"v_add_f32_e32 v131, v131, v195\n" \
"v_add_f32_e32 v132, v132, v196\n" \
"v_add_f32_e32 v133, v133, v197\n" \
"v_add_f32_e32 v134, v134, v198\n" \
"v_add_f32_e32 v135, v135, v199\n" \
"v_mul_f32_e32 v54, v54, v58\n" \
"v_mul_f32_e32 v128, v54, v128\n" \
"v_mul_f32_e32 v129, v54, v129\n" \
"v_mul_f32_e32 v130, v54, v130\n" \
"v_mul_f32_e32 v131, v54, v131\n" \
"v_mul_f32_e32 v132, v54, v132\n" \
"v_mul_f32_e32 v133, v54, v133\n" \
"v_mul_f32_e32 v134, v54, v134\n" \
"v_mul_f32_e32 v135, v54, v135\n" \
"v_add_f32_e32 v180, v180, v244\n" \
"v_add_f32_e32 v181, v181, v245\n" \
"v_add_f32_e32 v182, v182, v246\n" \
"v_add_f32_e32 v183, v183, v247\n" \
"v_add_f32_e32 v136, v136, v200\n" \
"v_add_f32_e32 v137, v137, v201\n" \
"v_add_f32_e32 v138, v138, v202\n" \
"v_add_f32_e32 v139, v139, v203\n" \
"v_add_f32_e32 v140, v140, v204\n" \
"v_add_f32_e32 v141, v141, v205\n" \
"v_add_f32_e32 v142, v142, v206\n" \
"v_add_f32_e32 v143, v143, v207\n" \
"v_cvt_pk_bf16_f32 v64, v128, v129\n" \
"v_cvt_pk_bf16_f32 v65, v130, v131\n" \
"v_cvt_pk_bf16_f32 v66, v132, v133\n" \
"v_cvt_pk_bf16_f32 v67, v134, v135\n" \
"buffer_store_dwordx4 v[64:67], v20, s[56:59], s68 offen offset:0 sc1\n" \
"v_mfma_f32_16x16x32_bf16 v[180:183], a[116:119], v[124:127], v[180:183]\n" \
"v_mul_f32_e32 v136, v54, v136\n" \
"v_mul_f32_e32 v137, v54, v137\n" \
"v_mfma_f32_16x16x32_bf16 a[208:211], a[216:219], v[64:67], a[208:211]\n" \
"v_mul_f32_e32 v138, v54, v138\n" \
"v_mul_f32_e32 v139, v54, v139\n" \
"v_mul_f32_e32 v140, v54, v140\n" \
"v_mul_f32_e32 v141, v54, v141\n" \
"v_mul_f32_e32 v142, v54, v142\n" \
"v_mul_f32_e32 v143, v54, v143\n" \
"v_add_f32_e32 v144, v144, v208\n" \
"v_add_f32_e32 v145, v145, v209\n" \
"v_add_f32_e32 v146, v146, v210\n" \
"v_add_f32_e32 v147, v147, v211\n" \
"v_add_f32_e32 v148, v148, v212\n" \
"v_add_f32_e32 v149, v149, v213\n" \
"v_add_f32_e32 v150, v150, v214\n" \
"v_add_f32_e32 v151, v151, v215\n" \
"v_cvt_pk_bf16_f32 v68, v136, v137\n" \
"v_cvt_pk_bf16_f32 v69, v138, v139\n" \
"v_cvt_pk_bf16_f32 v70, v140, v141\n" \
"v_cvt_pk_bf16_f32 v71, v142, v143\n" \
"buffer_store_dwordx4 v[68:71], v20, s[56:59], s68 offen offset:1024 sc1\n" \
"v_mul_f32_e32 v144, v54, v144\n" \
"v_mfma_f32_16x16x32_bf16 a[208:211], a[216:219], v[68:71], a[208:211]\n" \
"v_mul_f32_e32 v145, v54, v145\n" \
"v_mul_f32_e32 v146, v54, v146\n" \
"v_mul_f32_e32 v147, v54, v147\n" \
"v_mul_f32_e32 v148, v54, v148\n" \
"v_mul_f32_e32 v149, v54, v149\n" \
"v_mul_f32_e32 v150, v54, v150\n" \
"v_mul_f32_e32 v151, v54, v151\n" \
"v_add_f32_e32 v184, v184, v248\n" \
"v_add_f32_e32 v185, v185, v249\n" \
"v_add_f32_e32 v186, v186, v250\n" \
"v_add_f32_e32 v187, v187, v251\n" \
"v_add_f32_e32 v152, v152, v216\n" \
"v_add_f32_e32 v153, v153, v217\n" \
"v_add_f32_e32 v154, v154, v218\n" \
"v_add_f32_e32 v155, v155, v219\n" \
"v_add_f32_e32 v156, v156, v220\n" \
"v_add_f32_e32 v157, v157, v221\n" \
"v_add_f32_e32 v158, v158, v222\n" \
"v_add_f32_e32 v159, v159, v223\n" \
"v_cvt_pk_bf16_f32 v72, v144, v145\n" \
"v_cvt_pk_bf16_f32 v73, v146, v147\n" \
"v_cvt_pk_bf16_f32 v74, v148, v149\n" \
"v_cvt_pk_bf16_f32 v75, v150, v151\n" \
"buffer_store_dwordx4 v[72:75], v20, s[56:59], s68 offen offset:2048 sc1\n" \
"v_mfma_f32_16x16x32_bf16 v[184:187], a[120:123], v[124:127], v[184:187]\n" \
"v_mul_f32_e32 v152, v54, v152\n" \
"v_mul_f32_e32 v153, v54, v153\n" \
"v_mfma_f32_16x16x32_bf16 a[208:211], a[216:219], v[72:75], a[208:211]\n" \
"v_mul_f32_e32 v154, v54, v154\n" \
"v_mul_f32_e32 v155, v54, v155\n" \
"v_mul_f32_e32 v156, v54, v156\n" \
"v_mul_f32_e32 v157, v54, v157\n" \
"v_mul_f32_e32 v158, v54, v158\n" \
"v_mul_f32_e32 v159, v54, v159\n" \
"v_cvt_pk_bf16_f32 v76, v152, v153\n" \
"v_cvt_pk_bf16_f32 v77, v154, v155\n" \
"v_cvt_pk_bf16_f32 v78, v156, v157\n" \
"v_cvt_pk_bf16_f32 v79, v158, v159\n" \
"buffer_store_dwordx4 v[76:79], v20, s[56:59], s68 offen offset:3072 sc1\n" \
"v_add_f32_e32 v188, v188, v252\n" \
"v_mfma_f32_16x16x32_bf16 a[208:211], a[216:219], v[76:79], a[208:211]\n" \
"v_add_f32_e32 v189, v189, v253\n" \
"v_add_f32_e32 v190, v190, v254\n" \
"v_add_f32_e32 v191, v191, v255\n" \
"s_nop 1\n" \
"v_mfma_f32_16x16x32_bf16 v[188:191], a[124:127], v[124:127], v[188:191]\n" \
"v_mfma_f32_16x16x32_bf16 a[196:199], a[252:255], v[124:127], a[196:199]\n" \
"s_nop 7\n" \
"v_accvgpr_read_b32 v55, a196\n" \
"v_accvgpr_read_b32 v23, a197\n" \
"v_add_f32_e32 v55, v55, v23\n" \
"v_add_f32_e32 v55, v55, v43\n" \
"v_max_f32_e32 v62, v62, v55\n" \
"v_rcp_f32_e32 v55, v55\n" \
"v_cmp_lt_f32_e64 s[72:73], s100, v62\n" \
"v_mul_f32_e32 v55, v55, v59\n" \
"v_mul_f32_e32 v160, v55, v160\n" \
"v_mul_f32_e32 v161, v55, v161\n" \
"v_mul_f32_e32 v162, v55, v162\n" \
"v_mul_f32_e32 v163, v55, v163\n" \
"v_mul_f32_e32 v164, v55, v164\n" \
"v_mul_f32_e32 v165, v55, v165\n" \
"v_mul_f32_e32 v166, v55, v166\n" \
"v_mul_f32_e32 v167, v55, v167\n" \
"v_cvt_pk_bf16_f32 v80, v160, v161\n" \
"v_cvt_pk_bf16_f32 v81, v162, v163\n" \
"v_cvt_pk_bf16_f32 v82, v164, v165\n" \
"v_cvt_pk_bf16_f32 v83, v166, v167\n" \
"buffer_store_dwordx4 v[80:83], v20, s[56:59], s69 offen offset:0 sc1\n" \
"v_mul_f32_e32 v168, v55, v168\n" \
"v_mfma_f32_16x16x32_bf16 a[208:211], a[216:219], v[80:83], a[208:211]\n" \
"v_mul_f32_e32 v169, v55, v169\n" \
"v_mul_f32_e32 v170, v55, v170\n" \
"v_mul_f32_e32 v171, v55, v171\n" \
"v_mul_f32_e32 v172, v55, v172\n" \
"v_mul_f32_e32 v173, v55, v173\n" \
"v_mul_f32_e32 v174, v55, v174\n" \
"v_mul_f32_e32 v175, v55, v175\n" \
"v_cvt_pk_bf16_f32 v84, v168, v169\n" \
"v_cvt_pk_bf16_f32 v85, v170, v171\n" \
"v_cvt_pk_bf16_f32 v86, v172, v173\n" \
"v_cvt_pk_bf16_f32 v87, v174, v175\n" \
"buffer_store_dwordx4 v[84:87], v20, s[56:59], s69 offen offset:1024 sc1\n" \
"v_mul_f32_e32 v176, v55, v176\n" \
"v_mfma_f32_16x16x32_bf16 a[208:211], a[216:219], v[84:87], a[208:211]\n" \
"v_mul_f32_e32 v177, v55, v177\n" \
"v_mul_f32_e32 v178, v55, v178\n" \
"v_mul_f32_e32 v179, v55, v179\n" \
"v_mul_f32_e32 v180, v55, v180\n" \
"v_mul_f32_e32 v181, v55, v181\n" \
"v_mul_f32_e32 v182, v55, v182\n" \
"v_mul_f32_e32 v183, v55, v183\n" \
"v_cvt_pk_bf16_f32 v96, v176, v177\n" \
"v_cvt_pk_bf16_f32 v97, v178, v179\n" \
"v_cvt_pk_bf16_f32 v98, v180, v181\n" \
"v_cvt_pk_bf16_f32 v99, v182, v183\n" \
"buffer_store_dwordx4 v[96:99], v20, s[56:59], s69 offen offset:2048 sc1\n" \
"v_mul_f32_e32 v184, v55, v184\n" \
"v_mfma_f32_16x16x32_bf16 a[208:211], a[216:219], v[96:99], a[208:211]\n" \
"v_mul_f32_e32 v185, v55, v185\n" \
"v_mul_f32_e32 v186, v55, v186\n" \
"v_mul_f32_e32 v187, v55, v187\n" \
"v_mul_f32_e32 v188, v55, v188\n" \
"v_mul_f32_e32 v189, v55, v189\n" \
"v_mul_f32_e32 v190, v55, v190\n" \
"v_mul_f32_e32 v191, v55, v191\n" \
"v_cvt_pk_bf16_f32 v100, v184, v185\n" \
"v_cvt_pk_bf16_f32 v101, v186, v187\n" \
"v_cvt_pk_bf16_f32 v102, v188, v189\n" \
"v_cvt_pk_bf16_f32 v103, v190, v191\n" \
"buffer_store_dwordx4 v[100:103], v20, s[56:59], s69 offen offset:3072 sc1\n" \
"s_nop 0\n" \
"v_mfma_f32_16x16x32_bf16 a[208:211], a[216:219], v[100:103], a[208:211]\n" \
"s_nop 7\n" \
"v_accvgpr_read_b32 v63, a208\n" \
"v_cmp_u_f32_e32 vcc, v63, v63\n" \
"s_nop 0\n" \
"s_or_b64 vcc, s[72:73], vcc\n" \
"s_cbranch_scc1 L_end_%=\n" \
"L_out_%=:\n" \
"L_pf20:\n" \
"s_cbranch_execz L_retA\n" \
"s_nop 0\n" \
"v_add_u32_e32 v34, 0x20000, v10\n" \
"v_mov_b32_e32 v39, s62\n" \
"v_readlane_b32 s70, v11, 4\n" \
"v_bfe_u32 v38, v20, 2, 4\n" \
"v_readlane_b32 s64, v11, 1\n" \
"v_readlane_b32 s71, v11, 3\n" \
"v_add_u32_e32 v33, 0xfc00, v10\n" \
"v_readlane_b32 s99, v11, 19\n" \
"v_readlane_b32 s80, v11, 9\n" \
"v_readlane_b32 s81, v11, 10\n" \
"s_mov_b32 s82, 0x47fca0\n" \
"s_mov_b32 s83, 0x20000\n" \
"ds_read_b64_tr_b16 a[0:1], v34 offset:0\n" \
"ds_read_b64_tr_b16 a[2:3], v34 offset:4608\n" \
"ds_read_b64_tr_b16 a[4:5], v34 offset:9216\n" \
"ds_read_b64_tr_b16 a[6:7], v34 offset:13824\n" \
"ds_read_b64_tr_b16 a[8:9], v34 offset:18432\n" \
"ds_read_b64_tr_b16 a[10:11], v34 offset:23040\n" \
"ds_read_b64_tr_b16 a[12:13], v34 offset:32\n" \
"ds_read_b64_tr_b16 a[14:15], v34 offset:4640\n" \
"ds_read_b64_tr_b16 a[16:17], v34 offset:9248\n" \
"ds_read_b64_tr_b16 a[18:19], v34 offset:13856\n" \
"buffer_load_dword v37, v38, s[56:59], s64 offen sc1\n" \
"ds_read_b64_tr_b16 a[20:21], v34 offset:18464\n" \
"ds_read_b64_tr_b16 a[22:23], v34 offset:23072\n" \
"ds_read_b64_tr_b16 a[24:25], v34 offset:64\n" \
"ds_read_b64_tr_b16 a[26:27], v34 offset:4672\n" \
"ds_read_b64_tr_b16 a[28:29], v34 offset:9280\n" \
"ds_read_b64_tr_b16 a[30:31], v34 offset:13888\n" \
"ds_read_b64_tr_b16 a[192:193], v34 offset:18496\n" \
"ds_read_b64_tr_b16 a[194:195], v34 offset:23104\n" \
"ds_read_b64_tr_b16 a[196:197], v34 offset:96\n" \
"ds_read_b64_tr_b16 a[198:199], v34 offset:4704\n" \
"ds_read_b64_tr_b16 a[200:201], v34 offset:9312\n" \
"ds_read_b64_tr_b16 a[202:203], v34 offset:13920\n" \
"ds_read_b64_tr_b16 a[204:205], v34 offset:18528\n" \
"ds_read_b64_tr_b16 a[206:207], v34 offset:23136\n" \
"ds_read_b64_tr_b16 a[208:209], v34 offset:128\n" \
"ds_read_b64_tr_b16 a[210:211], v34 offset:4736\n" \
"ds_read_b64_tr_b16 a[212:213], v34 offset:9344\n" \
"ds_read_b64_tr_b16 a[214:215], v34 offset:13952\n" \
"ds_read_b64_tr_b16 a[216:217], v34 offset:18560\n" \
"ds_read_b64_tr_b16 a[218:219], v34 offset:23168\n" \
"v_readlane_b32 s65, v11, 23\n" \
"s_add_u32 m0, s99, 0x6c00\n" \
"v_readlane_b32 s66, v11, 26\n" \
"v_add_u32_e32 v32, 0x49000, v20\n" \
"v_lshrrev_b32_e32 v44, 9, v20\n" \
"v_mul_u32_u24_e32 v44, 0xb40, v44\n" \
"v_add_u32_e32 v35, v44, v10\n" \
"v_readlane_b32 s67, v11, 24\n" \
"v_readlane_b32 s92, v11, 20\n" \
"v_readlane_b32 s73, v11, 25\n" \
"s_waitcnt vmcnt(18) lgkmcnt(15)\n" \
"v_mfma_f32_16x16x32_bf16 v[112:115], a[0:3], a[220:223], 0\n" \
"buffer_load_dwordx4 v0, s[80:83], s65 offen offset:0 lds\n" \
"v_mfma_f32_16x16x32_bf16 v[112:115], a[4:7], a[224:227], v[112:115]\n" \
"v_readlane_b32 s94, v11, 21\n" \
"v_readlane_b32 s84, v11, 7\n" \
"v_mfma_f32_16x16x32_bf16 v[112:115], a[8:11], a[228:231], v[112:115]\n" \
"v_readlane_b32 s85, v11, 8\n" \
"s_mov_b32 s86, 0xa1ca0\n" \
"v_mfma_f32_16x16x32_bf16 v[116:119], a[8:11], a[244:247], 0\n" \
"s_mov_b32 s87, 0x20000\n" \
"v_readlane_b32 s68, v11, 27\n" \
"v_mfma_f32_16x16x32_bf16 v[116:119], a[4:7], a[240:243], v[116:119]\n" \
"v_readlane_b32 s69, v11, 28\n" \
"v_readlane_b32 s72, v11, 30\n" \
"v_mfma_f32_16x16x32_bf16 v[116:119], a[0:3], a[236:239], v[116:119]\n" \
"buffer_load_dwordx4 v1, s[80:83], s65 offen offset:1024 lds\n" \
"v_mfma_f32_16x16x32_bf16 v[184:187], a[0:3], v[64:67], 0\n" \
"v_readlane_b32 s93, v11, 31\n" \
"v_lshlrev_b32_e32 v41, 12, v20\n" \
"v_mfma_f32_16x16x32_bf16 v[184:187], a[4:7], v[68:71], v[184:187]\n" \
"v_readlane_b32 s63, v11, 37\n" \
"v_bfe_u32 v40, v20, 2, 7\n" \
"v_mfma_f32_16x16x32_bf16 v[184:187], a[8:11], v[72:75], v[184:187]\n" \
"s_add_u32 s74, s63, 0x3800\n" \
"v_readlane_b32 s95, v11, 34\n" \
"v_mfma_f32_16x16x32_bf16 v[188:191], a[8:11], v[96:99], 0\n" \
"ds_read_b64_tr_b16 a[8:9], v34 offset:18592\n" \
"ds_read_b64_tr_b16 a[10:11], v34 offset:23200\n" \
"v_mfma_f32_16x16x32_bf16 v[188:191], a[4:7], v[84:87], v[188:191]\n" \
"ds_read_b64_tr_b16 a[4:5], v34 offset:9376\n" \
"ds_read_b64_tr_b16 a[6:7], v34 offset:13984\n" \
"v_mfma_f32_16x16x32_bf16 v[188:191], a[0:3], v[80:83], v[188:191]\n" \
"ds_read_b64_tr_b16 a[0:1], v34 offset:160\n" \
"ds_read_b64_tr_b16 a[2:3], v34 offset:4768\n" \
"v_mfma_f32_16x16x32_bf16 v[120:123], a[12:15], a[220:223], 0\n" \
"buffer_load_dwordx4 v2, s[80:83], s65 offen offset:2048 lds\n" \
"v_mfma_f32_16x16x32_bf16 v[120:123], a[16:19], a[224:227], v[120:123]\n" \
"v_readlane_b32 s97, v11, 36\n" \
"v_bfe_u32 v44, v20, 4, 4\n" \
"v_mfma_f32_16x16x32_bf16 v[120:123], a[20:23], a[228:231], v[120:123]\n" \
"v_bfe_u32 v45, v20, 8, 2\n" \
"v_mul_u32_u24_e32 v42, 0x480, v44\n" \
"v_mfma_f32_16x16x32_bf16 v[124:127], a[20:23], a[244:247], 0\n" \
"v_lshl_add_u32 v42, v45, 3, v42\n" \
"v_and_b32_e32 v43, 1, v45\n" \
"v_mfma_f32_16x16x32_bf16 v[124:127], a[16:19], a[240:243], v[124:127]\n" \
"v_lshrrev_b32_e32 v44, 1, v44\n" \
"v_lshlrev_b32_e32 v45, 1, v45\n" \
"v_mfma_f32_16x16x32_bf16 v[124:127], a[12:15], a[236:239], v[124:127]\n" \
"buffer_load_dwordx4 v3, s[80:83], s65 offen offset:3072 lds\n" \
"v_mfma_f32_16x16x32_bf16 v[192:195], a[12:15], v[64:67], 0\n" \
"s_add_u32 m0, s99, 0x7c00\n" \
"v_sub_u32_e32 v44, v44, v45\n" \
"v_mfma_f32_16x16x32_bf16 v[192:195], a[16:19], v[68:71], v[192:195]\n" \
"v_and_b32_e32 v45, 16, v20\n" \
"v_mov_b32_e32 v46, 0x3f80\n" \
"v_mfma_f32_16x16x32_bf16 v[192:195], a[20:23], v[72:75], v[192:195]\n" \
"v_lshlrev_b32_e32 v45, v45, v46\n" \
"v_mov_b32_e32 v46, s62\n" \
"v_mfma_f32_16x16x32_bf16 v[196:199], a[20:23], v[96:99], 0\n" \
"ds_read_b64_tr_b16 a[20:21], v34 offset:18624\n" \
"ds_read_b64_tr_b16 a[22:23], v34 offset:23232\n" \
"v_mfma_f32_16x16x32_bf16 v[196:199], a[16:19], v[84:87], v[196:199]\n" \
"ds_read_b64_tr_b16 a[16:17], v34 offset:9408\n" \
"ds_read_b64_tr_b16 a[18:19], v34 offset:14016\n" \
"v_mfma_f32_16x16x32_bf16 v[196:199], a[12:15], v[80:83], v[196:199]\n" \
"ds_read_b64_tr_b16 a[12:13], v34 offset:192\n" \
"ds_read_b64_tr_b16 a[14:15], v34 offset:4800\n" \
"v_mfma_f32_16x16x32_bf16 v[128:131], a[24:27], a[220:223], 0\n" \
"v_readlane_b32 s96, v11, 35\n" \
"s_waitcnt lgkmcnt(8)\n" \
"v_mfma_f32_16x16x32_bf16 v[128:131], a[28:31], a[224:227], v[128:131]\n" \
"buffer_load_dwordx4 v4, s[80:83], s65 offen offset:0 lds\n" \
"v_mfma_f32_16x16x32_bf16 v[128:131], a[192:195], a[228:231], v[128:131]\n" \
"v_readlane_b32 s100, v11, 6\n" \
"v_readlane_b32 s75, v11, 38\n" \
"v_mfma_f32_16x16x32_bf16 v[132:135], a[192:195], a[244:247], 0\n" \
"v_add_u32_e32 v54, 0x3000, v20\n" \
"v_mov_b32_e32 v52, 0\n" \
"v_mfma_f32_16x16x32_bf16 v[132:135], a[28:31], a[240:243], v[132:135]\n" \
"v_mov_b32_e32 v53, 0\n" \
"v_mov_b32_e32 v48, 0\n" \
"v_mfma_f32_16x16x32_bf16 v[132:135], a[24:27], a[236:239], v[132:135]\n" \
"v_mov_b32_e32 v49, 0\n" \
"v_mul_u32_u24_e32 v43, 0x8ff8, v43\n" \
"v_mfma_f32_16x16x32_bf16 v[200:203], a[24:27], v[64:67], 0\n" \
"buffer_load_dwordx4 v5, s[80:83], s65 offen offset:1024 lds\n" \
"v_mfma_f32_16x16x32_bf16 v[200:203], a[28:31], v[68:71], v[200:203]\n" \
"v_add_u32_e32 v42, v42, v43\n" \
"v_readlane_b32 s88, v11, 11\n" \
"v_mfma_f32_16x16x32_bf16 v[200:203], a[192:195], v[72:75], v[200:203]\n" \
"v_readlane_b32 s89, v11, 12\n" \
"s_mov_b32 s90, 0x240000\n" \
"v_mfma_f32_16x16x32_bf16 v[204:207], a[192:195], v[96:99], 0\n" \
"ds_read_b64_tr_b16 a[192:193], v34 offset:18656\n" \
"ds_read_b64_tr_b16 a[194:195], v34 offset:23264\n" \
"v_mfma_f32_16x16x32_bf16 v[204:207], a[28:31], v[84:87], v[204:207]\n" \
"ds_read_b64_tr_b16 a[28:29], v34 offset:9440\n" \
"ds_read_b64_tr_b16 a[30:31], v34 offset:14048\n" \
"v_mfma_f32_16x16x32_bf16 v[204:207], a[24:27], v[80:83], v[204:207]\n" \
"ds_read_b64_tr_b16 a[24:25], v34 offset:224\n" \
"ds_read_b64_tr_b16 a[26:27], v34 offset:4832\n" \
"v_mfma_f32_16x16x32_bf16 v[136:139], a[196:199], a[220:223], 0\n" \
"buffer_load_dwordx4 v6, s[80:83], s65 offen offset:2048 lds\n" \
"v_mfma_f32_16x16x32_bf16 v[136:139], a[200:203], a[224:227], v[136:139]\n" \
"s_mov_b32 s91, 0x20000\n" \
"v_mfma_f32_16x16x32_bf16 v[136:139], a[204:207], a[228:231], v[136:139]\n" \
"v_mfma_f32_16x16x32_bf16 v[140:143], a[204:207], a[244:247], 0\n" \
"v_mfma_f32_16x16x32_bf16 v[140:143], a[200:203], a[240:243], v[140:143]\n" \
"v_mfma_f32_16x16x32_bf16 v[140:143], a[196:199], a[236:239], v[140:143]\n" \
"buffer_load_dwordx4 v7, s[80:83], s65 offen offset:3072 lds\n" \
"v_mfma_f32_16x16x32_bf16 v[208:211], a[196:199], v[64:67], 0\n" \
"s_add_u32 m0, s99, 0x8c00\n" \
"v_mfma_f32_16x16x32_bf16 v[208:211], a[200:203], v[68:71], v[208:211]\n" \
"v_mfma_f32_16x16x32_bf16 v[208:211], a[204:207], v[72:75], v[208:211]\n" \
"v_mfma_f32_16x16x32_bf16 v[212:215], a[204:207], v[96:99], 0\n" \
"ds_read_b64_tr_b16 a[204:205], v34 offset:18688\n" \
"ds_read_b64_tr_b16 a[206:207], v34 offset:23296\n" \
"v_mfma_f32_16x16x32_bf16 v[212:215], a[200:203], v[84:87], v[212:215]\n" \
"ds_read_b64_tr_b16 a[200:201], v34 offset:9472\n" \
"ds_read_b64_tr_b16 a[202:203], v34 offset:14080\n" \
"v_mfma_f32_16x16x32_bf16 v[212:215], a[196:199], v[80:83], v[212:215]\n" \
"ds_read_b64_tr_b16 a[196:197], v34 offset:256\n" \
"ds_read_b64_tr_b16 a[198:199], v34 offset:4864\n" \
"v_mfma_f32_16x16x32_bf16 v[144:147], a[208:211], a[220:223], 0\n" \
"buffer_load_dwordx4 v8, s[80:83], s65 offen offset:0 lds\n" \
"v_mfma_f32_16x16x32_bf16 v[144:147], a[212:215], a[224:227], v[144:147]\n" \
"s_waitcnt vmcnt(9)\n" \
"v_cmp_ne_u32_e32 vcc, s62, v37\n" \
"v_mfma_f32_16x16x32_bf16 v[144:147], a[216:219], a[228:231], v[144:147]\n" \
"s_add_u32 m0, s99, 0x14400\n" \
"v_readlane_b32 s65, v11, 42\n" \
"v_mfma_f32_16x16x32_bf16 v[148:151], a[216:219], a[244:247], 0\n" \
"v_mfma_f32_16x16x32_bf16 v[148:151], a[212:215], a[240:243], v[148:151]\n" \
"s_cbranch_vccnz L_sa_%=\n" \
"L_ra_%=:\n" \
"v_readlane_b32 s64, v11, 29\n" \
"v_mfma_f32_16x16x32_bf16 v[148:151], a[208:211], a[236:239], v[148:151]\n" \
"buffer_load_dwordx4 a[64:67], v20, s[56:59], s66 offen offset:0 sc1\n" \
"v_mfma_f32_16x16x32_bf16 v[216:219], a[208:211], v[64:67], 0\n" \
"v_mfma_f32_16x16x32_bf16 v[216:219], a[212:215], v[68:71], v[216:219]\n" \
"v_mfma_f32_16x16x32_bf16 v[216:219], a[216:219], v[72:75], v[216:219]\n" \
"v_mfma_f32_16x16x32_bf16 v[220:223], a[216:219], v[96:99], 0\n" \
"v_mfma_f32_16x16x32_bf16 v[220:223], a[212:215], v[84:87], v[220:223]\n" \
"buffer_load_dwordx4 a[68:71], v20, s[56:59], s66 offen offset:1024 sc1\n" \
"v_mfma_f32_16x16x32_bf16 v[220:223], a[208:211], v[80:83], v[220:223]\n" \
"s_waitcnt lgkmcnt(5)\n" \
"v_mfma_f32_16x16x32_bf16 v[152:155], a[0:3], a[220:223], 0\n" \
"v_mfma_f32_16x16x32_bf16 v[152:155], a[4:7], a[224:227], v[152:155]\n" \
"v_mfma_f32_16x16x32_bf16 v[152:155], a[8:11], a[228:231], v[152:155]\n" \
"v_mfma_f32_16x16x32_bf16 v[156:159], a[8:11], a[244:247], 0\n" \
"buffer_load_dwordx4 a[84:87], v32, s[56:59], s66 offen offset:1024 sc1\n" \
"v_mfma_f32_16x16x32_bf16 v[156:159], a[4:7], a[240:243], v[156:159]\n" \
"v_mfma_f32_16x16x32_bf16 v[156:159], a[0:3], a[236:239], v[156:159]\n" \
"v_mfma_f32_16x16x32_bf16 v[224:227], a[0:3], v[64:67], 0\n" \
"v_mfma_f32_16x16x32_bf16 v[224:227], a[4:7], v[68:71], v[224:227]\n" \
"v_mfma_f32_16x16x32_bf16 v[224:227], a[8:11], v[72:75], v[224:227]\n" \
"buffer_load_dwordx4 a[80:83], v32, s[56:59], s66 offen offset:0 sc1\n" \
"v_mfma_f32_16x16x32_bf16 v[228:231], a[8:11], v[96:99], 0\n" \
"v_mfma_f32_16x16x32_bf16 v[228:231], a[4:7], v[84:87], v[228:231]\n" \
"v_mfma_f32_16x16x32_bf16 v[228:231], a[0:3], v[80:83], v[228:231]\n" \
"v_mfma_f32_16x16x32_bf16 v[160:163], a[12:15], a[220:223], 0\n" \
"v_mfma_f32_16x16x32_bf16 v[160:163], a[16:19], a[224:227], v[160:163]\n" \
"buffer_load_dwordx4 a[72:75], v20, s[56:59], s66 offen offset:2048 sc1\n" \
"v_mfma_f32_16x16x32_bf16 v[160:163], a[20:23], a[228:231], v[160:163]\n" \
"v_mfma_f32_16x16x32_bf16 v[164:167], a[20:23], a[244:247], 0\n" \
"v_mfma_f32_16x16x32_bf16 v[164:167], a[16:19], a[240:243], v[164:167]\n" \
"v_mfma_f32_16x16x32_bf16 v[164:167], a[12:15], a[236:239], v[164:167]\n" \
"v_mfma_f32_16x16x32_bf16 v[232:235], a[12:15], v[64:67], 0\n" \
"buffer_load_dwordx4 a[76:79], v20, s[56:59], s66 offen offset:3072 sc1\n" \
"v_mfma_f32_16x16x32_bf16 v[232:235], a[16:19], v[68:71], v[232:235]\n" \
"v_mfma_f32_16x16x32_bf16 v[232:235], a[20:23], v[72:75], v[232:235]\n" \
"v_mfma_f32_16x16x32_bf16 v[236:239], a[20:23], v[96:99], 0\n" \
"v_mfma_f32_16x16x32_bf16 v[236:239], a[16:19], v[84:87], v[236:239]\n" \
"v_mfma_f32_16x16x32_bf16 v[236:239], a[12:15], v[80:83], v[236:239]\n" \
"buffer_load_dwordx4 a[92:95], v32, s[56:59], s66 offen offset:3072 sc1\n" \
"s_waitcnt lgkmcnt(0)\n" \
"s_barrier\n" \
"v_mfma_f32_16x16x32_bf16 v[168:171], a[24:27], a[220:223], 0\n" \
"ds_read_b64_tr_b16 a[208:209], v33 offset:9216\n" \
"ds_read_b64_tr_b16 a[210:211], v33 offset:13824\n" \
"v_mfma_f32_16x16x32_bf16 v[168:171], a[28:31], a[224:227], v[168:171]\n" \
"ds_read_b64_tr_b16 a[212:213], v33 offset:9248\n" \
"ds_read_b64_tr_b16 a[214:215], v33 offset:13856\n" \
"v_mfma_f32_16x16x32_bf16 v[168:171], a[192:195], a[228:231], v[168:171]\n" \
"ds_read_b64_tr_b16 a[216:217], v33 offset:9280\n" \
"ds_read_b64_tr_b16 a[218:219], v33 offset:13888\n" \
"s_mov_b64 exec, 1\n" \
"buffer_store_dword v39, off, s[56:59], s70 offset:0 sc1\n" \
"s_mov_b64 exec, -1\n" \
"v_mfma_f32_16x16x32_bf16 v[172:175], a[192:195], a[244:247], 0\n" \
"ds_read_b64_tr_b16 a[0:1], v33 offset:9312\n" \
"ds_read_b64_tr_b16 a[2:3], v33 offset:13920\n" \
"v_mfma_f32_16x16x32_bf16 v[172:175], a[28:31], a[240:243], v[172:175]\n" \
"ds_read_b64_tr_b16 a[4:5], v33 offset:9344\n" \
"ds_read_b64_tr_b16 a[6:7], v33 offset:13952\n" \
"v_mfma_f32_16x16x32_bf16 v[172:175], a[24:27], a[236:239], v[172:175]\n" \
"ds_read_b64_tr_b16 a[8:9], v33 offset:9376\n" \
"ds_read_b64_tr_b16 a[10:11], v33 offset:13984\n" \
"v_mfma_f32_16x16x32_bf16 v[240:243], a[24:27], v[64:67], 0\n" \
"ds_read_b64_tr_b16 a[12:13], v33 offset:9408\n" \
"ds_read_b64_tr_b16 a[14:15], v33 offset:14016\n" \
"v_mfma_f32_16x16x32_bf16 v[240:243], a[28:31], v[68:71], v[240:243]\n" \
"ds_read_b64_tr_b16 a[16:17], v33 offset:9440\n" \
"ds_read_b64_tr_b16 a[18:19], v33 offset:14048\n" \
"v_mfma_f32_16x16x32_bf16 v[240:243], a[192:195], v[72:75], v[240:243]\n" \
"ds_read_b64_tr_b16 a[20:21], v33 offset:9472\n" \
"ds_read_b64_tr_b16 a[22:23], v33 offset:14080\n" \
"v_mfma_f32_16x16x32_bf16 v[244:247], a[192:195], v[96:99], 0\n" \
"ds_read_b64_tr_b16 a[192:193], v35 offset:32\n" \
"ds_read_b64_tr_b16 a[194:195], v35 offset:2336\n" \
"v_mfma_f32_16x16x32_bf16 v[244:247], a[28:31], v[84:87], v[244:247]\n" \
"ds_read_b64_tr_b16 a[28:29], v35 offset:10368\n" \
"ds_read_b64_tr_b16 a[30:31], v35 offset:12672\n" \
"v_mfma_f32_16x16x32_bf16 v[244:247], a[24:27], v[80:83], v[244:247]\n" \
"ds_read_b64_tr_b16 a[24:25], v35 offset:0\n" \
"ds_read_b64_tr_b16 a[26:27], v35 offset:2304\n" \
"v_mfma_f32_16x16x32_bf16 v[176:179], a[196:199], a[220:223], 0\n" \
"buffer_load_dwordx4 a[88:91], v32, s[56:59], s66 offen offset:2048 sc1\n" \
"v_mfma_f32_16x16x32_bf16 v[176:179], a[200:203], a[224:227], v[176:179]\n" \
"v_readlane_b32 s66, v11, 43\n" \
"v_readlane_b32 s70, v11, 5\n" \
"v_mfma_f32_16x16x32_bf16 v[176:179], a[204:207], a[228:231], v[176:179]\n" \
"v_mfma_f32_16x16x32_bf16 v[180:183], a[204:207], a[244:247], 0\n" \
"v_mfma_f32_16x16x32_bf16 v[180:183], a[200:203], a[240:243], v[180:183]\n" \
"v_mfma_f32_16x16x32_bf16 v[180:183], a[196:199], a[236:239], v[180:183]\n" \
"buffer_load_dwordx4 v0, s[80:83], s67 offen offset:0 lds\n" \
"v_mfma_f32_16x16x32_bf16 v[248:251], a[196:199], v[64:67], 0\n" \
"v_mfma_f32_16x16x32_bf16 v[248:251], a[200:203], v[68:71], v[248:251]\n" \
"v_mfma_f32_16x16x32_bf16 v[248:251], a[204:207], v[72:75], v[248:251]\n" \
"v_mfma_f32_16x16x32_bf16 v[252:255], a[204:207], v[96:99], 0\n" \
"ds_read_b64_tr_b16 a[204:205], v35 offset:10432\n" \
"ds_read_b64_tr_b16 a[206:207], v35 offset:12736\n" \
"v_mfma_f32_16x16x32_bf16 v[252:255], a[200:203], v[84:87], v[252:255]\n" \
"ds_read_b64_tr_b16 a[200:201], v35 offset:64\n" \
"ds_read_b64_tr_b16 a[202:203], v35 offset:2368\n" \
"v_mfma_f32_16x16x32_bf16 v[252:255], a[196:199], v[80:83], v[252:255]\n" \
"ds_read_b64_tr_b16 a[196:197], v35 offset:10400\n" \
"s_waitcnt lgkmcnt(9)\n" \
"v_mfma_f32_16x16x32_bf16 v[112:115], a[208:211], a[232:235], v[112:115]\n" \
"buffer_load_dwordx4 v1, s[80:83], s67 offen offset:1024 lds\n" \
"v_mfma_f32_16x16x32_bf16 v[116:119], a[208:211], a[248:251], v[116:119]\n" \
"ds_read_b64_tr_b16 a[198:199], v35 offset:12704\n" \
"v_mfma_f32_16x16x32_bf16 v[184:187], a[208:211], v[76:79], v[184:187]\n" \
"v_mfma_f32_16x16x32_bf16 v[188:191], a[208:211], v[100:103], v[188:191]\n" \
"ds_read_b64_tr_b16 a[208:209], v35 offset:96\n" \
"ds_read_b64_tr_b16 a[210:211], v35 offset:2400\n" \
"L_pf21:\n" \
"s_cbranch_execz L_pf23\n" \
"s_nop 0\n" \
"v_mfma_f32_16x16x32_bf16 v[120:123], a[212:215], a[232:235], v[120:123]\n" \
"v_mfma_f32_16x16x32_bf16 v[124:127], a[212:215], a[248:251], v[124:127]\n" \
"buffer_load_dwordx4 v2, s[80:83], s67 offen offset:2048 lds\n" \
"v_mfma_f32_16x16x32_bf16 v[192:195], a[212:215], v[76:79], v[192:195]\n" \
"v_mfma_f32_16x16x32_bf16 v[196:199], a[212:215], v[100:103], v[196:199]\n" \
"ds_read_b64_tr_b16 a[212:213], v35 offset:10464\n" \
"ds_read_b64_tr_b16 a[214:215], v35 offset:12768\n" \
"v_mfma_f32_16x16x32_bf16 v[128:131], a[216:219], a[232:235], v[128:131]\n" \
"v_mfma_f32_16x16x32_bf16 v[132:135], a[216:219], a[248:251], v[132:135]\n" \
"v_mfma_f32_16x16x32_bf16 v[200:203], a[216:219], v[76:79], v[200:203]\n" \
"buffer_load_dwordx4 v3, s[80:83], s67 offen offset:3072 lds\n" \
"v_mfma_f32_16x16x32_bf16 v[204:207], a[216:219], v[100:103], v[204:207]\n" \
"ds_read_b64_tr_b16 a[216:217], v35 offset:128\n" \
"ds_read_b64_tr_b16 a[218:219], v35 offset:2432\n" \
"v_mfma_f32_16x16x32_bf16 v[136:139], a[0:3], a[232:235], v[136:139]\n" \
"s_add_u32 m0, s99, 0x15400\n" \
"v_mfma_f32_16x16x32_bf16 v[140:143], a[0:3], a[248:251], v[140:143]\n" \
"v_mfma_f32_16x16x32_bf16 v[208:211], a[0:3], v[76:79], v[208:211]\n" \
"v_mfma_f32_16x16x32_bf16 v[212:215], a[0:3], v[100:103], v[212:215]\n" \
"ds_read_b64_tr_b16 a[0:1], v35 offset:10496\n" \
"ds_read_b64_tr_b16 a[2:3], v35 offset:12800\n" \
"v_mfma_f32_16x16x32_bf16 v[144:147], a[4:7], a[232:235], v[144:147]\n" \
"buffer_load_dwordx4 v4, s[80:83], s67 offen offset:0 lds\n" \
"v_mfma_f32_16x16x32_bf16 v[148:151], a[4:7], a[248:251], v[148:151]\n" \
"v_mfma_f32_16x16x32_bf16 v[216:219], a[4:7], v[76:79], v[216:219]\n" \
"v_mfma_f32_16x16x32_bf16 v[220:223], a[4:7], v[100:103], v[220:223]\n" \
"ds_read_b64_tr_b16 a[4:5], v35 offset:160\n" \
"ds_read_b64_tr_b16 a[6:7], v35 offset:2464\n" \
"v_mfma_f32_16x16x32_bf16 v[152:155], a[8:11], a[232:235], v[152:155]\n" \
"v_mfma_f32_16x16x32_bf16 v[156:159], a[8:11], a[248:251], v[156:159]\n" \
"buffer_load_dwordx4 v5, s[80:83], s67 offen offset:1024 lds\n" \
"v_mfma_f32_16x16x32_bf16 v[224:227], a[8:11], v[76:79], v[224:227]\n" \
"v_mfma_f32_16x16x32_bf16 v[228:231], a[8:11], v[100:103], v[228:231]\n" \
"ds_read_b64_tr_b16 a[8:9], v35 offset:10528\n" \
"ds_read_b64_tr_b16 a[10:11], v35 offset:12832\n" \
"v_mfma_f32_16x16x32_bf16 v[160:163], a[12:15], a[232:235], v[160:163]\n" \
"v_mfma_f32_16x16x32_bf16 v[164:167], a[12:15], a[248:251], v[164:167]\n" \
"v_mfma_f32_16x16x32_bf16 v[232:235], a[12:15], v[76:79], v[232:235]\n" \
"buffer_load_dwordx4 v6, s[80:83], s67 offen offset:2048 lds\n" \
"v_mfma_f32_16x16x32_bf16 v[236:239], a[12:15], v[100:103], v[236:239]\n" \
"ds_read_b64_tr_b16 a[12:13], v35 offset:192\n" \
"ds_read_b64_tr_b16 a[14:15], v35 offset:2496\n" \
"v_mfma_f32_16x16x32_bf16 v[168:171], a[16:19], a[232:235], v[168:171]\n" \
"v_mfma_f32_16x16x32_bf16 v[172:175], a[16:19], a[248:251], v[172:175]\n" \
"v_mfma_f32_16x16x32_bf16 v[240:243], a[16:19], v[76:79], v[240:243]\n" \
"v_mfma_f32_16x16x32_bf16 v[244:247], a[16:19], v[100:103], v[244:247]\n" \
"buffer_load_dwordx4 v7, s[80:83], s67 offen offset:3072 lds\n" \
"v_mfma_f32_16x16x32_bf16 v[176:179], a[20:23], a[232:235], v[176:179]\n" \
"s_add_u32 m0, s99, 0x16400\n" \
"ds_read_b64_tr_b16 a[16:17], v35 offset:10560\n" \
"v_mfma_f32_16x16x32_bf16 v[180:183], a[20:23], a[248:251], v[180:183]\n" \
"ds_read_b64_tr_b16 a[18:19], v35 offset:12864\n" \
"v_mfma_f32_16x16x32_bf16 v[248:251], a[20:23], v[76:79], v[248:251]\n" \
"v_mfma_f32_16x16x32_bf16 v[252:255], a[20:23], v[100:103], v[252:255]\n" \
"ds_read_b64_tr_b16 a[20:21], v35 offset:224\n" \
"s_waitcnt lgkmcnt(7)\n" \
"v_mfma_f32_16x16x32_bf16 v[112:115], a[24:27], a[32:35], v[112:115]\n" \
"buffer_load_dwordx4 v8, s[80:83], s67 offen offset:0 lds\n" \
"v_mfma_f32_16x16x32_bf16 v[112:115], a[28:31], a[36:39], v[112:115]\n" \
"s_add_u32 m0, s92, 0x0\n" \
"ds_read_b64_tr_b16 a[22:23], v35 offset:2528\n" \
"v_mfma_f32_16x16x32_bf16 v[116:119], a[28:31], a[44:47], v[116:119]\n" \
"v_readlane_b32 s67, v11, 44\n" \
"v_mfma_f32_16x16x32_bf16 v[116:119], a[24:27], a[40:43], v[116:119]\n" \
"v_mfma_f32_16x16x32_bf16 v[184:187], a[24:27], a[48:51], v[184:187]\n" \
"v_mfma_f32_16x16x32_bf16 v[184:187], a[28:31], a[52:55], v[184:187]\n" \
"buffer_load_dword v47, v38, s[56:59], s71 offen sc1\n" \
"v_mfma_f32_16x16x32_bf16 v[188:191], a[28:31], a[60:63], v[188:191]\n" \
"ds_read_b64_tr_b16 a[28:29], v35 offset:256\n" \
"ds_read_b64_tr_b16 a[30:31], v35 offset:2560\n" \
"v_mfma_f32_16x16x32_bf16 v[188:191], a[24:27], a[56:59], v[188:191]\n" \
"ds_read_b64_tr_b16 a[24:25], v35 offset:10592\n" \
"ds_read_b64_tr_b16 a[26:27], v35 offset:12896\n" \
"v_mfma_f32_16x16x32_bf16 v[120:123], a[192:195], a[32:35], v[120:123]\n" \
"v_mfma_f32_16x16x32_bf16 v[120:123], a[196:199], a[36:39], v[120:123]\n" \
"v_mfma_f32_16x16x32_bf16 v[124:127], a[196:199], a[44:47], v[124:127]\n" \
"buffer_load_dwordx4 v0, s[80:83], s73 offen offset:0 lds\n" \
"v_mfma_f32_16x16x32_bf16 v[124:127], a[192:195], a[40:43], v[124:127]\n" \
"v_mfma_f32_16x16x32_bf16 v[192:195], a[192:195], a[48:51], v[192:195]\n" \
"v_mfma_f32_16x16x32_bf16 v[192:195], a[196:199], a[52:55], v[192:195]\n" \
"v_mfma_f32_16x16x32_bf16 v[196:199], a[196:199], a[60:63], v[196:199]\n" \
"v_mfma_f32_16x16x32_bf16 v[196:199], a[192:195], a[56:59], v[196:199]\n" \
"buffer_load_dwordx4 v1, s[80:83], s73 offen offset:1024 lds\n" \
"v_mfma_f32_16x16x32_bf16 v[128:131], a[200:203], a[32:35], v[128:131]\n" \
"ds_read_b64_tr_b16 a[192:193], v35 offset:10624\n" \
"ds_read_b64_tr_b16 a[194:195], v35 offset:12928\n" \
"v_mfma_f32_16x16x32_bf16 v[128:131], a[204:207], a[36:39], v[128:131]\n" \
"v_mfma_f32_16x16x32_bf16 v[132:135], a[204:207], a[44:47], v[132:135]\n" \
"v_mfma_f32_16x16x32_bf16 v[132:135], a[200:203], a[40:43], v[132:135]\n" \
"v_mfma_f32_16x16x32_bf16 v[200:203], a[200:203], a[48:51], v[200:203]\n" \
"buffer_load_dwordx4 v2, s[80:83], s73 offen offset:2048 lds\n" \
"v_mfma_f32_16x16x32_bf16 v[200:203], a[204:207], a[52:55], v[200:203]\n" \
"v_mfma_f32_16x16x32_bf16 v[204:207], a[204:207], a[60:63], v[204:207]\n" \
"v_mfma_f32_16x16x32_bf16 v[204:207], a[200:203], a[56:59], v[204:207]\n" \
"v_mfma_f32_16x16x32_bf16 v[136:139], a[208:211], a[32:35], v[136:139]\n" \
"v_mfma_f32_16x16x32_bf16 v[136:139], a[212:215], a[36:39], v[136:139]\n" \
"buffer_load_dwordx4 v3, s[80:83], s73 offen offset:3072 lds\n" \
"v_mfma_f32_16x16x32_bf16 v[140:143], a[212:215], a[44:47], v[140:143]\n" \
"s_add_u32 m0, s92, 0x1000\n" \
"v_mfma_f32_16x16x32_bf16 v[140:143], a[208:211], a[40:43], v[140:143]\n" \
"v_mfma_f32_16x16x32_bf16 v[208:211], a[208:211], a[48:51], v[208:211]\n" \
"v_mfma_f32_16x16x32_bf16 v[208:211], a[212:215], a[52:55], v[208:211]\n" \
"v_mfma_f32_16x16x32_bf16 v[212:215], a[212:215], a[60:63], v[212:215]\n" \
"buffer_load_dwordx4 v4, s[80:83], s73 offen offset:0 lds\n" \
"v_mfma_f32_16x16x32_bf16 v[212:215], a[208:211], a[56:59], v[212:215]\n" \
"v_mfma_f32_16x16x32_bf16 v[144:147], a[216:219], a[32:35], v[144:147]\n" \
"v_mfma_f32_16x16x32_bf16 v[144:147], a[0:3], a[36:39], v[144:147]\n" \
"v_mfma_f32_16x16x32_bf16 v[148:151], a[0:3], a[44:47], v[148:151]\n" \
"v_mfma_f32_16x16x32_bf16 v[148:151], a[216:219], a[40:43], v[148:151]\n" \
"buffer_load_dwordx4 v5, s[80:83], s73 offen offset:1024 lds\n" \
"v_mfma_f32_16x16x32_bf16 v[216:219], a[216:219], a[48:51], v[216:219]\n" \
"v_mfma_f32_16x16x32_bf16 v[216:219], a[0:3], a[52:55], v[216:219]\n" \
"v_mfma_f32_16x16x32_bf16 v[220:223], a[0:3], a[60:63], v[220:223]\n" \
"v_mfma_f32_16x16x32_bf16 v[220:223], a[216:219], a[56:59], v[220:223]\n" \
"v_mfma_f32_16x16x32_bf16 v[152:155], a[4:7], a[32:35], v[152:155]\n" \
"buffer_load_dwordx4 v6, s[80:83], s73 offen offset:2048 lds\n" \
"s_waitcnt lgkmcnt(0)\n" \
"v_mfma_f32_16x16x32_bf16 v[152:155], a[8:11], a[36:39], v[152:155]\n" \
"v_mfma_f32_16x16x32_bf16 v[156:159], a[8:11], a[44:47], v[156:159]\n" \
"v_mfma_f32_16x16x32_bf16 v[156:159], a[4:7], a[40:43], v[156:159]\n" \
"v_mfma_f32_16x16x32_bf16 v[224:227], a[4:7], a[48:51], v[224:227]\n" \
"buffer_load_dwordx4 v7, s[80:83], s73 offen offset:3072 lds\n" \
"s_add_u32 m0, s92, 0x2000\n" \
"v_mfma_f32_16x16x32_bf16 v[224:227], a[8:11], a[52:55], v[224:227]\n" \
"v_mfma_f32_16x16x32_bf16 v[228:231], a[8:11], a[60:63], v[228:231]\n" \
"v_mfma_f32_16x16x32_bf16 v[228:231], a[4:7], a[56:59], v[228:231]\n" \
"s_waitcnt vmcnt(23)\n" \
"s_barrier\n" \
"buffer_load_dwordx4 v8, s[80:83], s73 offen offset:0 lds\n" \
"s_mov_b32 m0, s94\n" \
"v_mfma_f32_16x16x32_bf16 v[160:163], a[12:15], a[32:35], v[160:163]\n" \
"ds_read_b64_tr_b16 a[196:197], v10 offset:27648\n" \
"ds_read_b64_tr_b16 a[198:199], v10 offset:32256\n" \
"v_mfma_f32_16x16x32_bf16 v[160:163], a[16:19], a[36:39], v[160:163]\n" \
"ds_read_b64_tr_b16 a[200:201], v10 offset:36864\n" \
"ds_read_b64_tr_b16 a[202:203], v10 offset:41472\n" \
"v_mfma_f32_16x16x32_bf16 v[164:167], a[16:19], a[44:47], v[164:167]\n" \
"ds_read_b64_tr_b16 a[204:205], v10 offset:27680\n" \
"ds_read_b64_tr_b16 a[206:207], v10 offset:32288\n" \
"v_mfma_f32_16x16x32_bf16 v[164:167], a[12:15], a[40:43], v[164:167]\n" \
"buffer_load_dwordx4 v9, s[84:87], 0 offen lds\n" \
"v_mfma_f32_16x16x32_bf16 v[232:235], a[12:15], a[48:51], v[232:235]\n" \
"ds_read_b64_tr_b16 a[208:209], v10 offset:36896\n" \
"ds_read_b64_tr_b16 a[210:211], v10 offset:41504\n" \
"v_mfma_f32_16x16x32_bf16 v[232:235], a[16:19], a[52:55], v[232:235]\n" \
"ds_read_b64_tr_b16 a[212:213], v10 offset:27712\n" \
"ds_read_b64_tr_b16 a[214:215], v10 offset:32320\n" \
"v_mfma_f32_16x16x32_bf16 v[236:239], a[16:19], a[60:63], v[236:239]\n" \
"ds_read_b64_tr_b16 a[216:217], v10 offset:36928\n" \
"ds_read_b64_tr_b16 a[218:219], v10 offset:41536\n" \
"v_mfma_f32_16x16x32_bf16 v[236:239], a[12:15], a[56:59], v[236:239]\n" \
"ds_read_b64_tr_b16 a[0:1], v10 offset:27744\n" \
"ds_read_b64_tr_b16 a[2:3], v10 offset:32352\n" \
"buffer_load_dwordx4 a[128:131], v20, s[56:59], s68 offen offset:0 sc1\n" \
"v_mfma_f32_16x16x32_bf16 v[168:171], a[20:23], a[32:35], v[168:171]\n" \
"ds_read_b64_tr_b16 a[4:5], v10 offset:36960\n" \
"ds_read_b64_tr_b16 a[6:7], v10 offset:41568\n" \
"v_mfma_f32_16x16x32_bf16 v[168:171], a[24:27], a[36:39], v[168:171]\n" \
"ds_read_b64_tr_b16 a[8:9], v10 offset:27776\n" \
"ds_read_b64_tr_b16 a[10:11], v10 offset:32384\n" \
"v_mfma_f32_16x16x32_bf16 v[172:175], a[24:27], a[44:47], v[172:175]\n" \
"ds_read_b64_tr_b16 a[12:13], v10 offset:36992\n" \
"ds_read_b64_tr_b16 a[14:15], v10 offset:41600\n" \
"v_mfma_f32_16x16x32_bf16 v[172:175], a[20:23], a[40:43], v[172:175]\n" \
"ds_read_b64_tr_b16 a[16:17], v10 offset:27808\n" \
"ds_read_b64_tr_b16 a[18:19], v10 offset:32416\n" \
"buffer_load_dwordx4 a[132:135], v20, s[56:59], s68 offen offset:1024 sc1\n" \
"v_mfma_f32_16x16x32_bf16 v[240:243], a[20:23], a[48:51], v[240:243]\n" \
"s_waitcnt vmcnt(12)\n" \
"v_cmp_ne_u32_e32 vcc, s62, v47\n" \
"v_mfma_f32_16x16x32_bf16 v[240:243], a[24:27], a[52:55], v[240:243]\n" \
"v_readlane_b32 s94, v11, 47\n" \
"s_mov_b32 s84, 0\n" \
"v_mfma_f32_16x16x32_bf16 v[244:247], a[24:27], a[60:63], v[244:247]\n" \
"ds_read_b64_tr_b16 a[24:25], v10 offset:27840\n" \
"ds_read_b64_tr_b16 a[26:27], v10 offset:32448\n" \
"v_mfma_f32_16x16x32_bf16 v[244:247], a[20:23], a[56:59], v[244:247]\n" \
"ds_read_b64_tr_b16 a[20:21], v10 offset:37024\n" \
"ds_read_b64_tr_b16 a[22:23], v10 offset:41632\n" \
"buffer_load_dwordx4 a[136:139], v20, s[56:59], s68 offen offset:2048 sc1\n" \
"v_mfma_f32_16x16x32_bf16 v[176:179], a[28:31], a[32:35], v[176:179]\n" \
"s_cbranch_vccnz L_sb_%=\n" \
"L_rb_%=:\n" \
"v_readlane_b32 s71, v11, 46\n" \
"v_mfma_f32_16x16x32_bf16 v[176:179], a[192:195], a[36:39], v[176:179]\n" \
"v_add_u32_e32 v38, 0x1000, v20\n" \
"v_add_u32_e32 v47, 0x2000, v20\n" \
"v_mfma_f32_16x16x32_bf16 v[180:183], a[192:195], a[44:47], v[180:183]\n" \
"v_cmp_eq_u32_e32 vcc, 0, v44\n" \
"v_mfma_f32_16x16x32_bf16 v[180:183], a[28:31], a[40:43], v[180:183]\n" \
"v_mfma_f32_16x16x32_bf16 v[248:251], a[28:31], a[48:51], v[248:251]\n" \
"buffer_load_dwordx4 a[140:143], v20, s[56:59], s68 offen offset:3072 sc1\n" \
"v_mfma_f32_16x16x32_bf16 v[248:251], a[192:195], a[52:55], v[248:251]\n" \
"v_cndmask_b32_e32 v50, 0, v45, vcc\n" \
"v_cmp_eq_u32_e32 vcc, 1, v44\n" \
"v_mfma_f32_16x16x32_bf16 v[252:255], a[192:195], a[60:63], v[252:255]\n" \
"ds_read_b64_tr_b16 a[192:193], v10 offset:27872\n" \
"ds_read_b64_tr_b16 a[194:195], v10 offset:32480\n" \
"v_mfma_f32_16x16x32_bf16 v[252:255], a[28:31], a[56:59], v[252:255]\n" \
"ds_read_b64_tr_b16 a[28:29], v10 offset:37056\n" \
"s_waitcnt lgkmcnt(9)\n" \
"v_mfma_f32_16x16x32_bf16 v[112:115], a[196:199], a[64:67], v[112:115]\n" \
"ds_read_b64_tr_b16 a[30:31], v10 offset:41664\n" \
"v_cndmask_b32_e32 v51, 0, v45, vcc\n" \
"v_mfma_f32_16x16x32_bf16 v[112:115], a[200:203], a[68:71], v[112:115]\n" \
"buffer_load_dwordx4 a[156:159], v32, s[56:59], s68 offen offset:3072 sc1\n" \
"v_mfma_f32_16x16x32_bf16 v[116:119], a[200:203], a[84:87], v[116:119]\n" \
"ds_read_b64_tr_b16 a[200:201], v10 offset:27904\n" \
"ds_read_b64_tr_b16 a[202:203], v10 offset:32512\n" \
"v_mfma_f32_16x16x32_bf16 v[116:119], a[196:199], a[80:83], v[116:119]\n" \
"ds_read_b64_tr_b16 a[196:197], v10 offset:37088\n" \
"ds_read_b64_tr_b16 a[198:199], v10 offset:41696\n" \
"v_mfma_f32_16x16x32_bf16 v[120:123], a[204:207], a[64:67], v[120:123]\n" \
"v_bfe_u32 v45, v20, 1, 5\n" \
"v_and_b32_e32 v45, 24, v45\n" \
"v_mfma_f32_16x16x32_bf16 v[120:123], a[208:211], a[68:71], v[120:123]\n" \
"v_and_b32_e32 v44, 0x3c0, v20\n" \
"v_add_u32_e32 v45, 0x1d400, v45\n" \
"v_mfma_f32_16x16x32_bf16 v[124:127], a[208:211], a[84:87], v[124:127]\n" \
"buffer_load_dwordx4 a[152:155], v32, s[56:59], s68 offen offset:2048 sc1\n" \
"v_mfma_f32_16x16x32_bf16 v[124:127], a[204:207], a[80:83], v[124:127]\n" \
"ds_read_b64_tr_b16 a[204:205], v10 offset:37120\n" \
"ds_read_b64_tr_b16 a[206:207], v10 offset:41728\n" \
"v_mfma_f32_16x16x32_bf16 v[128:131], a[212:215], a[64:67], v[128:131]\n" \
"ds_read_b64_tr_b16 a[208:209], v10 offset:46080\n" \
"ds_read_b64_tr_b16 a[210:211], v10 offset:50688\n" \
"v_mfma_f32_16x16x32_bf16 v[128:131], a[216:219], a[68:71], v[128:131]\n" \
"v_add_u32_e32 v36, v44, v45\n" \
"v_mfma_f32_16x16x32_bf16 v[132:135], a[216:219], a[84:87], v[132:135]\n" \
"ds_read_b64_tr_b16 a[216:217], v10 offset:46112\n" \
"ds_read_b64_tr_b16 a[218:219], v10 offset:50720\n" \
"buffer_load_dwordx4 a[148:151], v32, s[56:59], s68 offen offset:1024 sc1\n" \
"v_mfma_f32_16x16x32_bf16 v[132:135], a[212:215], a[80:83], v[132:135]\n" \
"ds_read_b64_tr_b16 a[212:213], v10 offset:55296\n" \
"ds_read_b64_tr_b16 a[214:215], v10 offset:59904\n" \
"v_mfma_f32_16x16x32_bf16 v[136:139], a[0:3], a[64:67], v[136:139]\n" \
"v_mfma_f32_16x16x32_bf16 v[136:139], a[4:7], a[68:71], v[136:139]\n" \
"v_mfma_f32_16x16x32_bf16 v[140:143], a[4:7], a[84:87], v[140:143]\n" \
"ds_read_b64_tr_b16 a[4:5], v10 offset:46144\n" \
"ds_read_b64_tr_b16 a[6:7], v10 offset:50752\n" \
"buffer_load_dwordx4 a[144:147], v32, s[56:59], s68 offen offset:0 sc1\n" \
"v_mfma_f32_16x16x32_bf16 v[140:143], a[0:3], a[80:83], v[140:143]\n" \
"ds_read_b64_tr_b16 a[0:1], v10 offset:55328\n" \
"ds_read_b64_tr_b16 a[2:3], v10 offset:59936\n" \
"v_mfma_f32_16x16x32_bf16 v[144:147], a[8:11], a[64:67], v[144:147]\n" \
"v_readlane_b32 s68, v11, 45\n" \
"v_mfma_f32_16x16x32_bf16 v[144:147], a[12:15], a[68:71], v[144:147]\n" \
"v_mfma_f32_16x16x32_bf16 v[148:151], a[12:15], a[84:87], v[148:151]\n" \
"ds_read_b64_tr_b16 a[12:13], v10 offset:46176\n" \
"ds_read_b64_tr_b16 a[14:15], v10 offset:50784\n" \
"buffer_load_dwordx4 a[96:99], v20, s[56:59], s69 offen offset:0 sc1\n" \
"v_mfma_f32_16x16x32_bf16 v[148:151], a[8:11], a[80:83], v[148:151]\n" \
"ds_read_b64_tr_b16 a[8:9], v10 offset:55360\n" \
"s_waitcnt lgkmcnt(15)\n" \
"v_mfma_f32_16x16x32_bf16 v[152:155], a[16:19], a[64:67], v[152:155]\n" \
"ds_read_b64_tr_b16 a[10:11], v10 offset:59968\n" \
"v_mfma_f32_16x16x32_bf16 v[152:155], a[20:23], a[68:71], v[152:155]\n" \
"v_mfma_f32_16x16x32_bf16 v[156:159], a[20:23], a[84:87], v[156:159]\n" \
"ds_read_b64_tr_b16 a[20:21], v10 offset:46208\n" \
"ds_read_b64_tr_b16 a[22:23], v10 offset:50816\n" \
"buffer_load_dwordx4 a[100:103], v20, s[56:59], s69 offen offset:1024 sc1\n" \
"v_mfma_f32_16x16x32_bf16 v[156:159], a[16:19], a[80:83], v[156:159]\n" \
"ds_read_b64_tr_b16 a[16:17], v10 offset:55392\n" \
"ds_read_b64_tr_b16 a[18:19], v10 offset:60000\n" \
"v_mfma_f32_16x16x32_bf16 v[160:163], a[24:27], a[64:67], v[160:163]\n" \
"v_mfma_f32_16x16x32_bf16 v[160:163], a[28:31], a[68:71], v[160:163]\n" \
"v_mfma_f32_16x16x32_bf16 v[164:167], a[28:31], a[84:87], v[164:167]\n" \
"ds_read_b64_tr_b16 a[28:29], v10 offset:46240\n" \
"ds_read_b64_tr_b16 a[30:31], v10 offset:50848\n" \
"buffer_load_dwordx4 a[104:107], v20, s[56:59], s69 offen offset:2048 sc1\n" \
"v_mfma_f32_16x16x32_bf16 v[164:167], a[24:27], a[80:83], v[164:167]\n" \
"ds_read_b64_tr_b16 a[24:25], v10 offset:55424\n" \
"ds_read_b64_tr_b16 a[26:27], v10 offset:60032\n" \
"v_mfma_f32_16x16x32_bf16 v[168:171], a[192:195], a[64:67], v[168:171]\n" \
"v_mfma_f32_16x16x32_bf16 v[168:171], a[196:199], a[68:71], v[168:171]\n" \
"v_mfma_f32_16x16x32_bf16 v[172:175], a[196:199], a[84:87], v[172:175]\n" \
"ds_read_b64_tr_b16 a[196:197], v10 offset:46272\n" \
"ds_read_b64_tr_b16 a[198:199], v10 offset:50880\n" \
"buffer_load_dwordx4 a[108:111], v20, s[56:59], s69 offen offset:3072 sc1\n" \
"v_mfma_f32_16x16x32_bf16 v[172:175], a[192:195], a[80:83], v[172:175]\n" \
"ds_read_b64_tr_b16 a[192:193], v10 offset:55456\n" \
"ds_read_b64_tr_b16 a[194:195], v10 offset:60064\n" \
"v_mfma_f32_16x16x32_bf16 v[176:179], a[200:203], a[64:67], v[176:179]\n" \
"s_waitcnt lgkmcnt(13)\n" \
"v_mfma_f32_16x16x32_bf16 v[176:179], a[204:207], a[68:71], v[176:179]\n" \
"v_mfma_f32_16x16x32_bf16 v[180:183], a[204:207], a[84:87], v[180:183]\n" \
"ds_read_b64_tr_b16 a[204:205], v10 offset:46304\n" \
"ds_read_b64_tr_b16 a[206:207], v10 offset:50912\n" \
"buffer_load_dwordx4 a[124:127], v32, s[56:59], s69 offen offset:3072 sc1\n" \
"v_mfma_f32_16x16x32_bf16 v[180:183], a[200:203], a[80:83], v[180:183]\n" \
"ds_read_b64_tr_b16 a[200:201], v10 offset:55488\n" \
"ds_read_b64_tr_b16 a[202:203], v10 offset:60096\n" \
"v_mfma_f32_16x16x32_bf16 v[112:115], a[208:211], a[72:75], v[112:115]\n" \
"v_mfma_f32_16x16x32_bf16 v[112:115], a[212:215], a[76:79], v[112:115]\n" \
"v_mfma_f32_16x16x32_bf16 v[116:119], a[212:215], a[92:95], v[116:119]\n" \
"ds_read_b64_tr_b16 a[212:213], v10 offset:46336\n" \
"ds_read_b64_tr_b16 a[214:215], v10 offset:50944\n" \
"buffer_load_dwordx4 a[120:123], v32, s[56:59], s69 offen offset:2048 sc1\n" \
"v_mfma_f32_16x16x32_bf16 v[116:119], a[208:211], a[88:91], v[116:119]\n" \
"ds_read_b64_tr_b16 a[208:209], v10 offset:55520\n" \
"ds_read_b64_tr_b16 a[210:211], v10 offset:60128\n" \
"v_mfma_f32_16x16x32_bf16 v[120:123], a[216:219], a[72:75], v[120:123]\n" \
"v_mfma_f32_16x16x32_bf16 v[120:123], a[0:3], a[76:79], v[120:123]\n" \
"v_mfma_f32_16x16x32_bf16 v[124:127], a[0:3], a[92:95], v[124:127]\n" \
"v_mfma_f32_16x16x32_bf16 v[124:127], a[216:219], a[88:91], v[124:127]\n" \
"buffer_load_dwordx4 a[116:119], v32, s[56:59], s69 offen offset:1024 sc1\n" \
"v_mfma_f32_16x16x32_bf16 v[128:131], a[4:7], a[72:75], v[128:131]\n" \
"ds_read_b64_tr_b16 a[216:217], v10 offset:55552\n" \
"s_waitcnt lgkmcnt(9)\n" \
"v_mfma_f32_16x16x32_bf16 v[128:131], a[8:11], a[76:79], v[128:131]\n" \
"ds_read_b64_tr_b16 a[218:219], v10 offset:60160\n" \
"v_mfma_f32_16x16x32_bf16 v[132:135], a[8:11], a[92:95], v[132:135]\n" \
"v_mfma_f32_16x16x32_bf16 v[132:135], a[4:7], a[88:91], v[132:135]\n" \
"v_mfma_f32_16x16x32_bf16 v[136:139], a[12:15], a[72:75], v[136:139]\n" \
"buffer_load_dwordx4 a[112:115], v32, s[56:59], s69 offen offset:0 sc1\n" \
"v_mfma_f32_16x16x32_bf16 v[136:139], a[16:19], a[76:79], v[136:139]\n" \
"v_readlane_b32 s69, v11, 49\n" \
"v_mfma_f32_16x16x32_bf16 v[140:143], a[16:19], a[92:95], v[140:143]\n" \
"v_mfma_f32_16x16x32_bf16 v[140:143], a[12:15], a[88:91], v[140:143]\n" \
"s_waitcnt vmcnt(16)\n" \
"s_barrier\n" \
"buffer_load_dwordx4 a[32:35], v20, s[56:59], s64 offen offset:0 sc1\n" \
"v_mfma_f32_16x16x32_bf16 v[144:147], a[20:23], a[72:75], v[144:147]\n" \
"ds_read_b64_tr_b16 a[0:1], v33 offset:18432\n" \
"ds_read_b64_tr_b16 a[2:3], v33 offset:23040\n" \
"v_mfma_f32_16x16x32_bf16 v[144:147], a[24:27], a[76:79], v[144:147]\n" \
"ds_read_b64_tr_b16 a[4:5], v33 offset:27648\n" \
"ds_read_b64_tr_b16 a[6:7], v33 offset:32256\n" \
"v_mfma_f32_16x16x32_bf16 v[148:151], a[24:27], a[92:95], v[148:151]\n" \
"ds_read_b64_tr_b16 a[8:9], v33 offset:36864\n" \
"ds_read_b64_tr_b16 a[10:11], v33 offset:41472\n" \
"v_mfma_f32_16x16x32_bf16 v[148:151], a[20:23], a[88:91], v[148:151]\n" \
"ds_read_b64_tr_b16 a[12:13], v33 offset:46080\n" \
"ds_read_b64_tr_b16 a[14:15], v33 offset:50688\n" \
"buffer_load_dwordx4 a[36:39], v20, s[56:59], s64 offen offset:1024 sc1\n" \
"v_mfma_f32_16x16x32_bf16 v[152:155], a[28:31], a[72:75], v[152:155]\n" \
"ds_read_b64_tr_b16 a[16:17], v34 offset:0\n" \
"ds_read_b64_tr_b16 a[18:19], v34 offset:4608\n" \
"v_mfma_f32_16x16x32_bf16 v[152:155], a[192:195], a[76:79], v[152:155]\n" \
"ds_read_b64_tr_b16 a[20:21], v34 offset:9216\n" \
"ds_read_b64_tr_b16 a[22:23], v34 offset:13824\n" \
"v_mfma_f32_16x16x32_bf16 v[156:159], a[192:195], a[92:95], v[156:159]\n" \
"ds_read_b64_tr_b16 a[24:25], v34 offset:18432\n" \
"ds_read_b64_tr_b16 a[26:27], v34 offset:23040\n" \
"v_mfma_f32_16x16x32_bf16 v[156:159], a[28:31], a[88:91], v[156:159]\n" \
"ds_read_b64_tr_b16 a[28:29], v33 offset:0\n" \
"ds_read_b64_tr_b16 a[30:31], v33 offset:4608\n" \
"buffer_load_dwordx4 a[40:43], v20, s[56:59], s64 offen offset:2048 sc1\n" \
"v_mfma_f32_16x16x32_bf16 v[160:163], a[196:199], a[72:75], v[160:163]\n" \
"ds_read_b64_tr_b16 a[192:193], v33 offset:18464\n" \
"s_waitcnt lgkmcnt(15)\n" \
"v_mfma_f32_16x16x32_bf16 v[160:163], a[200:203], a[76:79], v[160:163]\n" \
"ds_read_b64_tr_b16 a[194:195], v33 offset:23072\n" \
"v_mfma_f32_16x16x32_bf16 v[164:167], a[200:203], a[92:95], v[164:167]\n" \
"buffer_load_dwordx4 a[44:47], v20, s[56:59], s64 offen offset:3072 sc1\n" \
"v_mfma_f32_16x16x32_bf16 v[164:167], a[196:199], a[88:91], v[164:167]\n" \
"ds_read_b64_tr_b16 a[196:197], v33 offset:27680\n" \
"ds_read_b64_tr_b16 a[198:199], v33 offset:32288\n" \
"v_mfma_f32_16x16x32_bf16 v[168:171], a[204:207], a[72:75], v[168:171]\n" \
"ds_read_b64_tr_b16 a[200:201], v33 offset:36896\n" \
"ds_read_b64_tr_b16 a[202:203], v33 offset:41504\n" \
"v_mfma_f32_16x16x32_bf16 v[168:171], a[208:211], a[76:79], v[168:171]\n" \
"v_mfma_f32_16x16x32_bf16 v[172:175], a[208:211], a[92:95], v[172:175]\n" \
"ds_read_b64_tr_b16 a[208:209], v34 offset:32\n" \
"ds_read_b64_tr_b16 a[210:211], v34 offset:4640\n" \
"buffer_load_dwordx4 a[60:63], v32, s[56:59], s64 offen offset:3072 sc1\n" \
"v_mfma_f32_16x16x32_bf16 v[172:175], a[204:207], a[88:91], v[172:175]\n" \
"ds_read_b64_tr_b16 a[204:205], v33 offset:46112\n" \
"ds_read_b64_tr_b16 a[206:207], v33 offset:50720\n" \
"v_mfma_f32_16x16x32_bf16 v[176:179], a[212:215], a[72:75], v[176:179]\n" \
"v_mfma_f32_16x16x32_bf16 v[176:179], a[216:219], a[76:79], v[176:179]\n" \
"v_mfma_f32_16x16x32_bf16 v[180:183], a[216:219], a[92:95], v[180:183]\n" \
"ds_read_b64_tr_b16 a[216:217], v34 offset:18464\n" \
"ds_read_b64_tr_b16 a[218:219], v34 offset:23072\n" \
"buffer_load_dwordx4 a[56:59], v32, s[56:59], s64 offen offset:2048 sc1\n" \
"v_mfma_f32_16x16x32_bf16 v[180:183], a[212:215], a[88:91], v[180:183]\n" \
"ds_read_b64_tr_b16 a[212:213], v34 offset:9248\n" \
"s_waitcnt vmcnt(21)\n" \
"v_mfma_f32_16x16x32_bf16 v[112:115], a[0:3], a[128:131], v[112:115]\n" \
"ds_read_b64_tr_b16 a[214:215], v34 offset:13856\n" \
"s_waitcnt vmcnt(20) lgkmcnt(13)\n" \
"v_mfma_f32_16x16x32_bf16 v[112:115], a[4:7], a[132:135], v[112:115]\n" \
"s_waitcnt vmcnt(19)\n" \
"v_mfma_f32_16x16x32_bf16 v[112:115], a[8:11], a[136:139], v[112:115]\n" \
"s_waitcnt vmcnt(18)\n" \
"v_mfma_f32_16x16x32_bf16 v[112:115], a[12:15], a[140:143], v[112:115]\n" \
"buffer_load_dwordx4 a[52:55], v32, s[56:59], s64 offen offset:1024 sc1\n" \
"s_waitcnt vmcnt(18)\n" \
"v_mfma_f32_16x16x32_bf16 v[116:119], a[12:15], a[156:159], v[116:119]\n" \
"ds_read_b64_tr_b16 a[12:13], v33 offset:36928\n" \
"s_waitcnt vmcnt(17)\n" \
"v_mfma_f32_16x16x32_bf16 v[116:119], a[8:11], a[152:155], v[116:119]\n" \
"ds_read_b64_tr_b16 a[8:9], v33 offset:27712\n" \
"ds_read_b64_tr_b16 a[10:11], v33 offset:32320\n" \
"ds_read_b64_tr_b16 a[14:15], v33 offset:41536\n" \
"s_waitcnt vmcnt(16)\n" \
"v_mfma_f32_16x16x32_bf16 v[116:119], a[4:7], a[148:151], v[116:119]\n" \
"buffer_load_dwordx4 a[48:51], v32, s[56:59], s64 offen offset:0 sc1\n" \
"ds_read_b64_tr_b16 a[4:5], v33 offset:18496\n" \
"ds_read_b64_tr_b16 a[6:7], v33 offset:23104\n" \
"v_readlane_b32 s64, v11, 43\n" \
"s_waitcnt vmcnt(16)\n" \
"v_mfma_f32_16x16x32_bf16 v[116:119], a[0:3], a[144:147], v[116:119]\n" \
"buffer_load_dwordx4 a[160:163], v20, s[56:59], s72 offen offset:0 sc1\n" \
"ds_read_b64_tr_b16 a[0:1], v33 offset:32\n" \
"ds_read_b64_tr_b16 a[2:3], v33 offset:4640\n" \
"s_waitcnt vmcnt(16)\n" \
"v_mfma_f32_16x16x32_bf16 v[112:115], a[16:19], a[96:99], v[112:115]\n" \
"buffer_load_dwordx4 a[164:167], v20, s[56:59], s72 offen offset:1024 sc1\n" \
"s_waitcnt vmcnt(16)\n" \
"v_mfma_f32_16x16x32_bf16 v[112:115], a[20:23], a[100:103], v[112:115]\n" \
"buffer_load_dwordx4 a[168:171], v20, s[56:59], s72 offen offset:2048 sc1\n" \
"s_waitcnt vmcnt(16)\n" \
"v_mfma_f32_16x16x32_bf16 v[112:115], a[24:27], a[104:107], v[112:115]\n" \
"buffer_load_dwordx4 a[172:175], v20, s[56:59], s72 offen offset:3072 sc1\n" \
"s_waitcnt vmcnt(16)\n" \
"v_mfma_f32_16x16x32_bf16 v[112:115], a[28:31], a[108:111], v[112:115]\n" \
"buffer_load_dwordx4 a[188:191], v32, s[56:59], s72 offen offset:3072 sc1\n" \
"s_waitcnt vmcnt(16)\n" \
"v_mfma_f32_16x16x32_bf16 v[116:119], a[28:31], a[124:127], v[116:119]\n" \
"buffer_load_dwordx4 a[184:187], v32, s[56:59], s72 offen offset:2048 sc1\n" \
"ds_read_b64_tr_b16 a[28:29], v34 offset:18496\n" \
"ds_read_b64_tr_b16 a[30:31], v34 offset:23104\n" \
"s_waitcnt vmcnt(16)\n" \
"v_mfma_f32_16x16x32_bf16 v[116:119], a[24:27], a[120:123], v[116:119]\n" \
"v_cvt_pk_bf16_f32 v112, v112, v113\n" \
"v_cvt_pk_bf16_f32 v113, v114, v115\n" \
"s_waitcnt vmcnt(15)\n" \
"v_mfma_f32_16x16x32_bf16 v[116:119], a[20:23], a[116:119], v[116:119]\n" \
"buffer_load_dwordx4 a[180:183], v32, s[56:59], s72 offen offset:1024 sc1\n" \
"ds_read_b64_tr_b16 a[20:21], v34 offset:64\n" \
"ds_read_b64_tr_b16 a[22:23], v34 offset:4672\n" \
"ds_read_b64_tr_b16 a[24:25], v34 offset:9280\n" \
"ds_read_b64_tr_b16 a[26:27], v34 offset:13888\n" \
"s_waitcnt vmcnt(15)\n" \
"v_mfma_f32_16x16x32_bf16 v[116:119], a[16:19], a[112:115], v[116:119]\n" \
"ds_read_b64_tr_b16 a[16:17], v33 offset:46144\n" \
"buffer_load_dwordx4 a[176:179], v32, s[56:59], s72 offen offset:0 sc1\n" \
"s_waitcnt lgkmcnt(5)\n" \
"v_mfma_f32_16x16x32_bf16 v[120:123], a[192:195], a[128:131], v[120:123]\n" \
"ds_read_b64_tr_b16 a[18:19], v33 offset:50752\n" \
"v_mfma_f32_16x16x32_bf16 v[120:123], a[196:199], a[132:135], v[120:123]\n" \
"v_mfma_f32_16x16x32_bf16 v[120:123], a[200:203], a[136:139], v[120:123]\n" \
"v_mfma_f32_16x16x32_bf16 v[120:123], a[204:207], a[140:143], v[120:123]\n" \
"v_cvt_pk_bf16_f32 v114, v116, v117\n" \
"v_cvt_pk_bf16_f32 v115, v118, v119\n" \
"v_mfma_f32_16x16x32_bf16 v[124:127], a[204:207], a[156:159], v[124:127]\n" \
"buffer_store_dwordx4 v[112:115], v20, s[56:59], s65 offen offset:0 sc1\n" \
"s_add_u32 s65, s65, 0x3800\n" \
"v_mfma_f32_16x16x32_bf16 v[124:127], a[200:203], a[152:155], v[124:127]\n" \
"ds_read_b64_tr_b16 a[200:201], v33 offset:27744\n" \
"ds_read_b64_tr_b16 a[202:203], v33 offset:32352\n" \
"v_mfma_f32_16x16x32_bf16 v[124:127], a[196:199], a[148:151], v[124:127]\n" \
"ds_read_b64_tr_b16 a[196:197], v33 offset:18528\n" \
"ds_read_b64_tr_b16 a[198:199], v33 offset:23136\n" \
"v_mfma_f32_16x16x32_bf16 v[124:127], a[192:195], a[144:147], v[124:127]\n" \
"ds_read_b64_tr_b16 a[192:193], v33 offset:64\n" \
"ds_read_b64_tr_b16 a[194:195], v33 offset:4672\n" \
"v_mfma_f32_16x16x32_bf16 v[120:123], a[208:211], a[96:99], v[120:123]\n" \
"buffer_load_dwordx4 a[220:223], v20, s[56:59], s93 offen offset:0 sc1\n" \
"v_mfma_f32_16x16x32_bf16 v[120:123], a[212:215], a[100:103], v[120:123]\n" \
"ds_read_b64_tr_b16 a[204:205], v33 offset:36960\n" \
"ds_read_b64_tr_b16 a[206:207], v33 offset:41568\n" \
"v_mfma_f32_16x16x32_bf16 v[120:123], a[216:219], a[104:107], v[120:123]\n" \
"v_mfma_f32_16x16x32_bf16 v[120:123], a[0:3], a[108:111], v[120:123]\n" \
"v_mfma_f32_16x16x32_bf16 v[124:127], a[0:3], a[124:127], v[124:127]\n" \
"ds_read_b64_tr_b16 a[0:1], v34 offset:18528\n" \
"ds_read_b64_tr_b16 a[2:3], v34 offset:23136\n" \
"buffer_load_dwordx4 a[224:227], v20, s[56:59], s93 offen offset:1024 sc1\n" \
"v_mfma_f32_16x16x32_bf16 v[124:127], a[216:219], a[120:123], v[124:127]\n" \
"ds_read_b64_tr_b16 a[216:217], v34 offset:9312\n" \
"ds_read_b64_tr_b16 a[218:219], v34 offset:13920\n" \
"v_mfma_f32_16x16x32_bf16 v[124:127], a[212:215], a[116:119], v[124:127]\n" \
"ds_read_b64_tr_b16 a[212:213], v34 offset:96\n" \
"v_cvt_pk_bf16_f32 v120, v120, v121\n" \
"v_cvt_pk_bf16_f32 v121, v122, v123\n" \
"v_mfma_f32_16x16x32_bf16 v[124:127], a[208:211], a[112:115], v[124:127]\n" \
"ds_read_b64_tr_b16 a[208:209], v33 offset:46176\n" \
"ds_read_b64_tr_b16 a[210:211], v33 offset:50784\n" \
"v_mfma_f32_16x16x32_bf16 v[128:131], a[4:7], a[128:131], v[128:131]\n" \
"buffer_load_dwordx4 a[228:231], v20, s[56:59], s93 offen offset:2048 sc1\n" \
"v_mfma_f32_16x16x32_bf16 v[128:131], a[8:11], a[132:135], v[128:131]\n" \
"ds_read_b64_tr_b16 a[214:215], v34 offset:4704\n" \
"v_mfma_f32_16x16x32_bf16 v[128:131], a[12:15], a[136:139], v[128:131]\n" \
"s_waitcnt lgkmcnt(15)\n" \
"v_mfma_f32_16x16x32_bf16 v[128:131], a[16:19], a[140:143], v[128:131]\n" \
"v_cvt_pk_bf16_f32 v122, v124, v125\n" \
"v_cvt_pk_bf16_f32 v123, v126, v127\n" \
"v_mfma_f32_16x16x32_bf16 v[132:135], a[16:19], a[156:159], v[132:135]\n" \
"buffer_store_dwordx4 v[120:123], v20, s[56:59], s66 offen offset:0 sc1\n" \
"s_add_u32 s66, s66, 0x3000\n" \
"v_mfma_f32_16x16x32_bf16 v[132:135], a[12:15], a[152:155], v[132:135]\n" \
"ds_read_b64_tr_b16 a[12:13], v10 offset:37120\n" \
"ds_read_b64_tr_b16 a[14:15], v10 offset:41728\n" \
"v_mfma_f32_16x16x32_bf16 v[132:135], a[8:11], a[148:151], v[132:135]\n" \
"ds_read_b64_tr_b16 a[8:9], v10 offset:27904\n" \
"ds_read_b64_tr_b16 a[10:11], v10 offset:32512\n" \
"v_mfma_f32_16x16x32_bf16 v[132:135], a[4:7], a[144:147], v[132:135]\n" \
"ds_read_b64_tr_b16 a[4:5], v33 offset:96\n" \
"ds_read_b64_tr_b16 a[6:7], v33 offset:4704\n" \
"v_mfma_f32_16x16x32_bf16 v[128:131], a[20:23], a[96:99], v[128:131]\n" \
"buffer_load_dwordx4 a[232:235], v20, s[56:59], s93 offen offset:3072 sc1\n" \
"v_mfma_f32_16x16x32_bf16 v[128:131], a[24:27], a[100:103], v[128:131]\n" \
"ds_read_b64_tr_b16 a[16:17], v10 offset:46336\n" \
"ds_read_b64_tr_b16 a[18:19], v10 offset:50944\n" \
"v_mfma_f32_16x16x32_bf16 v[128:131], a[28:31], a[104:107], v[128:131]\n" \
"s_waitcnt lgkmcnt(14)\n" \
"v_mfma_f32_16x16x32_bf16 v[128:131], a[192:195], a[108:111], v[128:131]\n" \
"v_mfma_f32_16x16x32_bf16 v[132:135], a[192:195], a[124:127], v[132:135]\n" \
"ds_read_b64_tr_b16 a[192:193], v33 offset:37120\n" \
"ds_read_b64_tr_b16 a[194:195], v33 offset:41728\n" \
"buffer_load_dwordx4 a[248:251], v32, s[56:59], s93 offen offset:3072 sc1\n" \
"v_mfma_f32_16x16x32_bf16 v[132:135], a[28:31], a[120:123], v[132:135]\n" \
"ds_read_b64_tr_b16 a[28:29], v33 offset:27904\n" \
"ds_read_b64_tr_b16 a[30:31], v33 offset:32512\n" \
"v_mfma_f32_16x16x32_bf16 v[132:135], a[24:27], a[116:119], v[132:135]\n" \
"ds_read_b64_tr_b16 a[24:25], v33 offset:18688\n" \
"v_cvt_pk_bf16_f32 v128, v128, v129\n" \
"v_cvt_pk_bf16_f32 v129, v130, v131\n" \
"v_mfma_f32_16x16x32_bf16 v[132:135], a[20:23], a[112:115], v[132:135]\n" \
"ds_read_b64_tr_b16 a[20:21], v10 offset:55552\n" \
"ds_read_b64_tr_b16 a[22:23], v10 offset:60160\n" \
"v_mfma_f32_16x16x32_bf16 v[136:139], a[196:199], a[128:131], v[136:139]\n" \
"buffer_load_dwordx4 a[244:247], v32, s[56:59], s93 offen offset:2048 sc1\n" \
"v_mfma_f32_16x16x32_bf16 v[136:139], a[200:203], a[132:135], v[136:139]\n" \
"ds_read_b64_tr_b16 a[26:27], v33 offset:23296\n" \
"v_mfma_f32_16x16x32_bf16 v[136:139], a[204:207], a[136:139], v[136:139]\n" \
"s_waitcnt lgkmcnt(15)\n" \
"v_mfma_f32_16x16x32_bf16 v[136:139], a[208:211], a[140:143], v[136:139]\n" \
"v_cvt_pk_bf16_f32 v130, v132, v133\n" \
"v_cvt_pk_bf16_f32 v131, v134, v135\n" \
"v_mfma_f32_16x16x32_bf16 v[140:143], a[208:211], a[156:159], v[140:143]\n" \
"buffer_store_dwordx4 v[128:131], v20, s[56:59], s67 offen offset:0 sc1\n" \
"L_pf22:\n" \
"s_cbranch_execz L_retB\n" \
"s_nop 0\n" \
"s_add_u32 s67, s67, 0x2800\n" \
"v_mfma_f32_16x16x32_bf16 v[140:143], a[204:207], a[152:155], v[140:143]\n" \
"ds_read_b64_tr_b16 a[204:205], v34 offset:9472\n" \
"ds_read_b64_tr_b16 a[206:207], v34 offset:14080\n" \
"v_mfma_f32_16x16x32_bf16 v[140:143], a[200:203], a[148:151], v[140:143]\n" \
"ds_read_b64_tr_b16 a[200:201], v34 offset:256\n" \
"ds_read_b64_tr_b16 a[202:203], v34 offset:4864\n" \
"v_mfma_f32_16x16x32_bf16 v[140:143], a[196:199], a[144:147], v[140:143]\n" \
"ds_read_b64_tr_b16 a[196:197], v33 offset:46336\n" \
"ds_read_b64_tr_b16 a[198:199], v33 offset:50944\n" \
"v_mfma_f32_16x16x32_bf16 v[136:139], a[212:215], a[96:99], v[136:139]\n" \
"buffer_load_dwordx4 a[240:243], v32, s[56:59], s93 offen offset:1024 sc1\n" \
"v_mfma_f32_16x16x32_bf16 v[136:139], a[216:219], a[100:103], v[136:139]\n" \
"ds_read_b64_tr_b16 a[208:209], v34 offset:18688\n" \
"ds_read_b64_tr_b16 a[210:211], v34 offset:23296\n" \
"v_mfma_f32_16x16x32_bf16 v[136:139], a[0:3], a[104:107], v[136:139]\n" \
"s_waitcnt lgkmcnt(14)\n" \
"v_mfma_f32_16x16x32_bf16 v[136:139], a[4:7], a[108:111], v[136:139]\n" \
"v_mfma_f32_16x16x32_bf16 v[140:143], a[4:7], a[124:127], v[140:143]\n" \
"ds_read_b64_tr_b16 a[4:5], v10 offset:46208\n" \
"ds_read_b64_tr_b16 a[6:7], v10 offset:50816\n" \
"buffer_load_dwordx4 a[236:239], v32, s[56:59], s93 offen offset:0 sc1\n" \
"v_mfma_f32_16x16x32_bf16 v[140:143], a[0:3], a[120:123], v[140:143]\n" \
"ds_read_b64_tr_b16 a[0:1], v10 offset:36992\n" \
"ds_read_b64_tr_b16 a[2:3], v10 offset:41600\n" \
"v_mfma_f32_16x16x32_bf16 v[140:143], a[216:219], a[116:119], v[140:143]\n" \
"ds_read_b64_tr_b16 a[216:217], v10 offset:27776\n" \
"v_cvt_pk_bf16_f32 v136, v136, v137\n" \
"v_cvt_pk_bf16_f32 v137, v138, v139\n" \
"v_mfma_f32_16x16x32_bf16 v[140:143], a[212:215], a[112:115], v[140:143]\n" \
"ds_read_b64_tr_b16 a[212:213], v33 offset:256\n" \
"s_waitcnt vmcnt(26)\n" \
"v_mfma_f32_16x16x32_bf16 v[248:251], a[8:11], a[32:35], v[248:251]\n" \
"ds_read_b64_tr_b16 a[214:215], v33 offset:4864\n" \
"s_waitcnt vmcnt(25)\n" \
"v_mfma_f32_16x16x32_bf16 v[248:251], a[12:15], a[36:39], v[248:251]\n" \
"ds_read_b64_tr_b16 a[218:219], v10 offset:32384\n" \
"s_waitcnt vmcnt(24)\n" \
"v_mfma_f32_16x16x32_bf16 v[248:251], a[16:19], a[40:43], v[248:251]\n" \
"v_cvt_pk_bf16_f32 v138, v140, v141\n" \
"v_cvt_pk_bf16_f32 v139, v142, v143\n" \
"s_waitcnt vmcnt(23) lgkmcnt(14)\n" \
"v_mfma_f32_16x16x32_bf16 v[248:251], a[20:23], a[44:47], v[248:251]\n" \
"buffer_store_dwordx4 v[136:139], v20, s[56:59], s68 offen offset:0 sc1\n" \
"s_add_u32 s68, s68, 0x2000\n" \
"s_waitcnt vmcnt(23)\n" \
"v_mfma_f32_16x16x32_bf16 v[252:255], a[20:23], a[60:63], v[252:255]\n" \
"ds_read_b64_tr_b16 a[20:21], v33 offset:36992\n" \
"s_waitcnt vmcnt(22)\n" \
"v_mfma_f32_16x16x32_bf16 v[252:255], a[16:19], a[56:59], v[252:255]\n" \
"ds_read_b64_tr_b16 a[16:17], v33 offset:27776\n" \
"s_waitcnt vmcnt(21)\n" \
"v_mfma_f32_16x16x32_bf16 v[252:255], a[12:15], a[52:55], v[252:255]\n" \
"ds_read_b64_tr_b16 a[12:13], v33 offset:18560\n" \
"s_waitcnt vmcnt(20)\n" \
"v_mfma_f32_16x16x32_bf16 v[252:255], a[8:11], a[48:51], v[252:255]\n" \
"ds_read_b64_tr_b16 a[8:9], v10 offset:55424\n" \
"ds_read_b64_tr_b16 a[10:11], v10 offset:60032\n" \
"v_mfma_f32_16x16x32_bf16 v[176:179], a[24:27], a[128:131], v[176:179]\n" \
"ds_read_b64_tr_b16 a[14:15], v33 offset:23168\n" \
"ds_read_b64_tr_b16 a[18:19], v33 offset:32384\n" \
"v_mfma_f32_16x16x32_bf16 v[176:179], a[28:31], a[132:135], v[176:179]\n" \
"buffer_load_dwordx4 v[64:67], v20, s[56:59], s95 offen offset:0 sc1\n" \
"v_mfma_f32_16x16x32_bf16 v[176:179], a[192:195], a[136:139], v[176:179]\n" \
"ds_read_b64_tr_b16 a[22:23], v33 offset:41600\n" \
"s_waitcnt lgkmcnt(12)\n" \
"v_mfma_f32_16x16x32_bf16 v[176:179], a[196:199], a[140:143], v[176:179]\n" \
"v_mfma_f32_16x16x32_bf16 v[180:183], a[196:199], a[156:159], v[180:183]\n" \
"v_mfma_f32_16x16x32_bf16 v[180:183], a[192:195], a[152:155], v[180:183]\n" \
"v_mfma_f32_16x16x32_bf16 v[180:183], a[28:31], a[148:151], v[180:183]\n" \
"buffer_load_dwordx4 v[68:71], v20, s[56:59], s95 offen offset:1024 sc1\n" \
"v_mfma_f32_16x16x32_bf16 v[180:183], a[24:27], a[144:147], v[180:183]\n" \
"s_waitcnt vmcnt(21)\n" \
"v_mfma_f32_16x16x32_bf16 v[248:251], a[24:27], a[160:163], v[248:251]\n" \
"s_waitcnt vmcnt(13)\n" \
"s_mov_b64 exec, 1\n" \
"buffer_store_dword v39, off, s[56:59], s65 offset:0 sc1\n" \
"s_mov_b64 exec, -1\n" \
"v_mfma_f32_16x16x32_bf16 v[248:251], a[28:31], a[164:167], v[248:251]\n" \
"v_mfma_f32_16x16x32_bf16 v[248:251], a[192:195], a[168:171], v[248:251]\n" \
"v_mfma_f32_16x16x32_bf16 v[248:251], a[196:199], a[172:175], v[248:251]\n" \
"v_mfma_f32_16x16x32_bf16 v[252:255], a[196:199], a[188:191], v[252:255]\n" \
"ds_read_b64_tr_b16 a[196:197], v34 offset:18560\n" \
"ds_read_b64_tr_b16 a[198:199], v34 offset:23168\n" \
"v_mfma_f32_16x16x32_bf16 v[252:255], a[192:195], a[184:187], v[252:255]\n" \
"ds_read_b64_tr_b16 a[192:193], v34 offset:9344\n" \
"ds_read_b64_tr_b16 a[194:195], v34 offset:13952\n" \
"v_mfma_f32_16x16x32_bf16 v[252:255], a[28:31], a[180:183], v[252:255]\n" \
"ds_read_b64_tr_b16 a[28:29], v34 offset:128\n" \
"ds_read_b64_tr_b16 a[30:31], v34 offset:4736\n" \
"v_mfma_f32_16x16x32_bf16 v[252:255], a[24:27], a[176:179], v[252:255]\n" \
"ds_read_b64_tr_b16 a[24:25], v33 offset:46208\n" \
"ds_read_b64_tr_b16 a[26:27], v33 offset:50816\n" \
"v_mfma_f32_16x16x32_bf16 v[176:179], a[200:203], a[96:99], v[176:179]\n" \
"buffer_load_dwordx4 v[72:75], v32, s[56:59], s95 offen offset:0 sc1\n" \
"v_mfma_f32_16x16x32_bf16 v[176:179], a[204:207], a[100:103], v[176:179]\n" \
"v_mfma_f32_16x16x32_bf16 v[176:179], a[208:211], a[104:107], v[176:179]\n" \
"s_waitcnt lgkmcnt(8)\n" \
"v_mfma_f32_16x16x32_bf16 v[176:179], a[212:215], a[108:111], v[176:179]\n" \
"v_mfma_f32_16x16x32_bf16 v[180:183], a[212:215], a[124:127], v[180:183]\n" \
"v_mfma_f32_16x16x32_bf16 v[180:183], a[208:211], a[120:123], v[180:183]\n" \
"buffer_load_dwordx4 v[76:79], v32, s[56:59], s95 offen offset:1024 sc1\n" \
"v_mfma_f32_16x16x32_bf16 v[180:183], a[204:207], a[116:119], v[180:183]\n" \
"v_readlane_b32 s95, v11, 48\n" \
"v_mfma_f32_16x16x32_bf16 v[180:183], a[200:203], a[112:115], v[180:183]\n" \
"s_waitcnt vmcnt(15)\n" \
"v_mfma_f32_16x16x32_bf16 v[248:251], a[200:203], a[220:223], v[248:251]\n" \
"s_waitcnt vmcnt(12)\n" \
"s_mov_b64 exec, 1\n" \
"buffer_store_dword v39, off, s[56:59], s66 offset:4 sc1\n" \
"s_mov_b64 exec, -1\n" \
"v_cvt_pk_bf16_f32 v176, v176, v177\n" \
"v_cvt_pk_bf16_f32 v177, v178, v179\n" \
"v_cvt_pk_bf16_f32 v178, v180, v181\n" \
"v_cvt_pk_bf16_f32 v179, v182, v183\n" \
"v_mfma_f32_16x16x32_bf16 v[248:251], a[204:207], a[224:227], v[248:251]\n" \
"v_readlane_b32 s66, v11, 44\n" \
"v_mfma_f32_16x16x32_bf16 v[248:251], a[208:211], a[228:231], v[248:251]\n" \
"buffer_store_dwordx4 v[176:179], v20, s[56:59], s69 offen offset:0 sc1\n" \
"s_waitcnt vmcnt(10)\n" \
"s_mov_b64 exec, 1\n" \
"buffer_store_dword v39, off, s[56:59], s67 offset:8 sc1\n" \
"s_mov_b64 exec, -1\n" \
"v_mfma_f32_16x16x32_bf16 v[248:251], a[212:215], a[232:235], v[248:251]\n" \
"v_readlane_b32 s67, v11, 45\n" \
"v_mfma_f32_16x16x32_bf16 v[252:255], a[212:215], a[248:251], v[252:255]\n" \
"ds_read_b64_tr_b16 a[212:213], v10 offset:46240\n" \
"ds_read_b64_tr_b16 a[214:215], v10 offset:50848\n" \
"v_mfma_f32_16x16x32_bf16 v[252:255], a[208:211], a[244:247], v[252:255]\n" \
"ds_read_b64_tr_b16 a[208:209], v10 offset:37024\n" \
"ds_read_b64_tr_b16 a[210:211], v10 offset:41632\n" \
"s_waitcnt vmcnt(10)\n" \
"v_mfma_f32_16x16x32_bf16 v[252:255], a[204:207], a[240:243], v[252:255]\n" \
"v_cvt_pk_bf16_f32 v248, v248, v249\n" \
"v_cvt_pk_bf16_f32 v249, v250, v251\n" \
"s_waitcnt vmcnt(9)\n" \
"v_mfma_f32_16x16x32_bf16 v[252:255], a[200:203], a[236:239], v[252:255]\n" \
"ds_read_b64_tr_b16 a[200:201], v33 offset:128\n" \
"ds_read_b64_tr_b16 a[202:203], v33 offset:4736\n" \
"s_waitcnt vmcnt(8)\n" \
"s_mov_b64 exec, 1\n" \
"buffer_store_dword v39, off, s[56:59], s68 offset:12 sc1\n" \
"s_mov_b64 exec, -1\n" \
"v_mfma_f32_16x16x32_bf16 v[216:219], a[216:219], a[32:35], v[216:219]\n" \
"ds_read_b64_tr_b16 a[204:205], v10 offset:27808\n" \
"v_cvt_pk_bf16_f32 v250, v252, v253\n" \
"v_cvt_pk_bf16_f32 v251, v254, v255\n" \
"v_mfma_f32_16x16x32_bf16 v[216:219], a[0:3], a[36:39], v[216:219]\n" \
"buffer_store_dwordx4 v[248:251], v20, s[56:59], s69 offen offset:1024 sc1\n" \
"v_mfma_f32_16x16x32_bf16 v[216:219], a[4:7], a[40:43], v[216:219]\n" \
"ds_read_b64_tr_b16 a[206:207], v10 offset:32416\n" \
"v_mfma_f32_16x16x32_bf16 v[216:219], a[8:11], a[44:47], v[216:219]\n" \
"v_mfma_f32_16x16x32_bf16 v[220:223], a[8:11], a[60:63], v[220:223]\n" \
"ds_read_b64_tr_b16 a[8:9], v33 offset:37024\n" \
"ds_read_b64_tr_b16 a[10:11], v33 offset:41632\n" \
"v_mfma_f32_16x16x32_bf16 v[220:223], a[4:7], a[56:59], v[220:223]\n" \
"ds_read_b64_tr_b16 a[4:5], v33 offset:27808\n" \
"ds_read_b64_tr_b16 a[6:7], v33 offset:32416\n" \
"v_mfma_f32_16x16x32_bf16 v[220:223], a[0:3], a[52:55], v[220:223]\n" \
"ds_read_b64_tr_b16 a[0:1], v33 offset:18592\n" \
"ds_read_b64_tr_b16 a[2:3], v33 offset:23200\n" \
"v_mfma_f32_16x16x32_bf16 v[220:223], a[216:219], a[48:51], v[220:223]\n" \
"ds_read_b64_tr_b16 a[216:217], v10 offset:55456\n" \
"ds_read_b64_tr_b16 a[218:219], v10 offset:60064\n" \
"v_mfma_f32_16x16x32_bf16 v[144:147], a[12:15], a[128:131], v[144:147]\n" \
"buffer_load_dwordx4 v[96:99], v20, s[56:59], s97 offen offset:0 sc1\n" \
"v_mfma_f32_16x16x32_bf16 v[144:147], a[16:19], a[132:135], v[144:147]\n" \
"v_mfma_f32_16x16x32_bf16 v[144:147], a[20:23], a[136:139], v[144:147]\n" \
"s_waitcnt lgkmcnt(12)\n" \
"v_mfma_f32_16x16x32_bf16 v[144:147], a[24:27], a[140:143], v[144:147]\n" \
"buffer_load_dword v37, v40, s[56:59], s74 offen sc1\n" \
"v_mfma_f32_16x16x32_bf16 v[148:151], a[24:27], a[156:159], v[148:151]\n" \
"v_mfma_f32_16x16x32_bf16 v[148:151], a[20:23], a[152:155], v[148:151]\n" \
"v_mfma_f32_16x16x32_bf16 v[148:151], a[16:19], a[148:151], v[148:151]\n" \
"v_mfma_f32_16x16x32_bf16 v[148:151], a[12:15], a[144:147], v[148:151]\n" \
"v_mfma_f32_16x16x32_bf16 v[216:219], a[12:15], a[160:163], v[216:219]\n" \
"buffer_load_dwordx4 v[100:103], v20, s[56:59], s97 offen offset:1024 sc1\n" \
"v_mfma_f32_16x16x32_bf16 v[216:219], a[16:19], a[164:167], v[216:219]\n" \
"v_mfma_f32_16x16x32_bf16 v[216:219], a[20:23], a[168:171], v[216:219]\n" \
"v_mfma_f32_16x16x32_bf16 v[216:219], a[24:27], a[172:175], v[216:219]\n" \
"v_mfma_f32_16x16x32_bf16 v[220:223], a[24:27], a[188:191], v[220:223]\n" \
"ds_read_b64_tr_b16 a[24:25], v34 offset:18592\n" \
"ds_read_b64_tr_b16 a[26:27], v34 offset:23200\n" \
"v_mfma_f32_16x16x32_bf16 v[220:223], a[20:23], a[184:187], v[220:223]\n" \
"ds_read_b64_tr_b16 a[20:21], v34 offset:9376\n" \
"ds_read_b64_tr_b16 a[22:23], v34 offset:13984\n" \
"v_mfma_f32_16x16x32_bf16 v[220:223], a[16:19], a[180:183], v[220:223]\n" \
"ds_read_b64_tr_b16 a[16:17], v34 offset:160\n" \
"ds_read_b64_tr_b16 a[18:19], v34 offset:4768\n" \
"v_mfma_f32_16x16x32_bf16 v[220:223], a[12:15], a[176:179], v[220:223]\n" \
"ds_read_b64_tr_b16 a[12:13], v33 offset:46240\n" \
"ds_read_b64_tr_b16 a[14:15], v33 offset:50848\n" \
"v_mfma_f32_16x16x32_bf16 v[144:147], a[28:31], a[96:99], v[144:147]\n" \
"buffer_load_dwordx4 v[104:107], v32, s[56:59], s97 offen offset:0 sc1\n" \
"v_mfma_f32_16x16x32_bf16 v[144:147], a[192:195], a[100:103], v[144:147]\n" \
"v_mfma_f32_16x16x32_bf16 v[144:147], a[196:199], a[104:107], v[144:147]\n" \
"s_waitcnt lgkmcnt(8)\n" \
"v_mfma_f32_16x16x32_bf16 v[144:147], a[200:203], a[108:111], v[144:147]\n" \
"v_mfma_f32_16x16x32_bf16 v[148:151], a[200:203], a[124:127], v[148:151]\n" \
"v_mfma_f32_16x16x32_bf16 v[148:151], a[196:199], a[120:123], v[148:151]\n" \
"buffer_load_dwordx4 v[108:111], v32, s[56:59], s97 offen offset:1024 sc1\n" \
"v_mfma_f32_16x16x32_bf16 v[148:151], a[192:195], a[116:119], v[148:151]\n" \
"v_readlane_b32 s97, v11, 42\n" \
"v_mfma_f32_16x16x32_bf16 v[148:151], a[28:31], a[112:115], v[148:151]\n" \
"v_mfma_f32_16x16x32_bf16 v[216:219], a[28:31], a[220:223], v[216:219]\n" \
"v_mfma_f32_16x16x32_bf16 v[216:219], a[192:195], a[224:227], v[216:219]\n" \
"v_cvt_pk_bf16_f32 v144, v144, v145\n" \
"v_cvt_pk_bf16_f32 v145, v146, v147\n" \
"v_mfma_f32_16x16x32_bf16 v[216:219], a[196:199], a[228:231], v[216:219]\n" \
"buffer_load_dwordx4 v[80:83], v20, s[56:59], s96 offen offset:0 sc1\n" \
"v_mfma_f32_16x16x32_bf16 v[216:219], a[200:203], a[232:235], v[216:219]\n" \
"v_mfma_f32_16x16x32_bf16 v[220:223], a[200:203], a[248:251], v[220:223]\n" \
"v_cvt_pk_bf16_f32 v146, v148, v149\n" \
"v_cvt_pk_bf16_f32 v147, v150, v151\n" \
"v_mfma_f32_16x16x32_bf16 v[220:223], a[196:199], a[244:247], v[220:223]\n" \
"buffer_store_dwordx4 v[144:147], v20, s[56:59], s71 offen offset:0 sc1\n" \
"v_mfma_f32_16x16x32_bf16 v[220:223], a[192:195], a[240:243], v[220:223]\n" \
"ds_read_b64_tr_b16 a[192:193], v10 offset:27840\n" \
"ds_read_b64_tr_b16 a[194:195], v10 offset:32448\n" \
"v_cvt_pk_bf16_f32 v216, v216, v217\n" \
"v_cvt_pk_bf16_f32 v217, v218, v219\n" \
"v_mfma_f32_16x16x32_bf16 v[220:223], a[28:31], a[236:239], v[220:223]\n" \
"ds_read_b64_tr_b16 a[28:29], v33 offset:160\n" \
"ds_read_b64_tr_b16 a[30:31], v33 offset:4768\n" \
"v_mfma_f32_16x16x32_bf16 v[224:227], a[204:207], a[32:35], v[224:227]\n" \
"ds_read_b64_tr_b16 a[196:197], v10 offset:37056\n" \
"ds_read_b64_tr_b16 a[198:199], v10 offset:41664\n" \
"v_mfma_f32_16x16x32_bf16 v[224:227], a[208:211], a[36:39], v[224:227]\n" \
"ds_read_b64_tr_b16 a[200:201], v10 offset:46272\n" \
"ds_read_b64_tr_b16 a[202:203], v10 offset:50880\n" \
"v_cvt_pk_bf16_f32 v218, v220, v221\n" \
"v_cvt_pk_bf16_f32 v219, v222, v223\n" \
"v_mfma_f32_16x16x32_bf16 v[224:227], a[212:215], a[40:43], v[224:227]\n" \
"buffer_store_dwordx4 v[216:219], v20, s[56:59], s71 offen offset:1024 sc1\n" \
"s_add_u32 s71, s71, 0x1800\n" \
"v_mfma_f32_16x16x32_bf16 v[224:227], a[216:219], a[44:47], v[224:227]\n" \
"v_mfma_f32_16x16x32_bf16 v[228:231], a[216:219], a[60:63], v[228:231]\n" \
"ds_read_b64_tr_b16 a[216:217], v33 offset:37056\n" \
"ds_read_b64_tr_b16 a[218:219], v33 offset:41664\n" \
"v_mfma_f32_16x16x32_bf16 v[228:231], a[212:215], a[56:59], v[228:231]\n" \
"ds_read_b64_tr_b16 a[212:213], v33 offset:27840\n" \
"ds_read_b64_tr_b16 a[214:215], v33 offset:32448\n" \
"v_mfma_f32_16x16x32_bf16 v[228:231], a[208:211], a[52:55], v[228:231]\n" \
"ds_read_b64_tr_b16 a[208:209], v33 offset:18624\n" \
"ds_read_b64_tr_b16 a[210:211], v33 offset:23232\n" \
"v_mfma_f32_16x16x32_bf16 v[228:231], a[204:207], a[48:51], v[228:231]\n" \
"ds_read_b64_tr_b16 a[204:205], v10 offset:55488\n" \
"ds_read_b64_tr_b16 a[206:207], v10 offset:60096\n" \
"v_mfma_f32_16x16x32_bf16 v[152:155], a[0:3], a[128:131], v[152:155]\n" \
"buffer_load_dwordx4 v[88:91], v32, s[56:59], s96 offen offset:0 sc1\n" \
"v_mfma_f32_16x16x32_bf16 v[152:155], a[4:7], a[132:135], v[152:155]\n" \
"v_mfma_f32_16x16x32_bf16 v[152:155], a[8:11], a[136:139], v[152:155]\n" \
"s_waitcnt lgkmcnt(15)\n" \
"v_mfma_f32_16x16x32_bf16 v[152:155], a[12:15], a[140:143], v[152:155]\n" \
"v_mfma_f32_16x16x32_bf16 v[156:159], a[12:15], a[156:159], v[156:159]\n" \
"v_mfma_f32_16x16x32_bf16 v[156:159], a[8:11], a[152:155], v[156:159]\n" \
"buffer_load_dwordx4 v[84:87], v20, s[56:59], s96 offen offset:1024 sc1\n" \
"v_mfma_f32_16x16x32_bf16 v[156:159], a[4:7], a[148:151], v[156:159]\n" \
"v_mfma_f32_16x16x32_bf16 v[156:159], a[0:3], a[144:147], v[156:159]\n" \
"v_mfma_f32_16x16x32_bf16 v[224:227], a[0:3], a[160:163], v[224:227]\n" \
"v_mfma_f32_16x16x32_bf16 v[224:227], a[4:7], a[164:167], v[224:227]\n" \
"v_mfma_f32_16x16x32_bf16 v[224:227], a[8:11], a[168:171], v[224:227]\n" \
"buffer_load_dwordx4 v[92:95], v32, s[56:59], s96 offen offset:1024 sc1\n" \
"v_mfma_f32_16x16x32_bf16 v[224:227], a[12:15], a[172:175], v[224:227]\n" \
"v_mfma_f32_16x16x32_bf16 v[228:231], a[12:15], a[188:191], v[228:231]\n" \
"ds_read_b64_tr_b16 a[12:13], v34 offset:18624\n" \
"ds_read_b64_tr_b16 a[14:15], v34 offset:23232\n" \
"v_mfma_f32_16x16x32_bf16 v[228:231], a[8:11], a[184:187], v[228:231]\n" \
"ds_read_b64_tr_b16 a[8:9], v34 offset:9408\n" \
"ds_read_b64_tr_b16 a[10:11], v34 offset:14016\n" \
"v_mfma_f32_16x16x32_bf16 v[228:231], a[4:7], a[180:183], v[228:231]\n" \
"ds_read_b64_tr_b16 a[4:5], v34 offset:192\n" \
"ds_read_b64_tr_b16 a[6:7], v34 offset:4800\n" \
"v_mfma_f32_16x16x32_bf16 v[228:231], a[0:3], a[176:179], v[228:231]\n" \
"ds_read_b64_tr_b16 a[0:1], v33 offset:46272\n" \
"ds_read_b64_tr_b16 a[2:3], v33 offset:50880\n" \
"s_waitcnt vmcnt(11)\n" \
"s_mov_b64 exec, 3\n" \
"buffer_store_dword v39, v41, s[56:59], s70 offen sc1\n" \
"s_mov_b64 exec, -1\n" \
"v_mfma_f32_16x16x32_bf16 v[152:155], a[16:19], a[96:99], v[152:155]\n" \
"v_mfma_f32_16x16x32_bf16 v[152:155], a[20:23], a[100:103], v[152:155]\n" \
"v_mfma_f32_16x16x32_bf16 v[152:155], a[24:27], a[104:107], v[152:155]\n" \
"s_waitcnt vmcnt(10)\n" \
"v_cmp_eq_u32_e64 s[98:99], s62, v37\n" \
"s_waitcnt lgkmcnt(8)\n" \
"v_mfma_f32_16x16x32_bf16 v[152:155], a[28:31], a[108:111], v[152:155]\n" \
"s_and_b32 s99, s98, 0xf\n" \
"s_or_b32 s84, s84, s99\n" \
"s_bitcmp1_b32 s84, 0\n" \
"s_cselect_b32 s72, s63, 0x40000000\n" \
"s_bitcmp1_b32 s84, 1\n" \
"s_cselect_b32 s73, s63, 0x40000000\n" \
"s_bitcmp1_b32 s84, 2\n" \
"s_cselect_b32 s92, s63, 0x40000000\n" \
"s_bitcmp1_b32 s84, 3\n" \
"s_cselect_b32 s93, s63, 0x40000000\n" \
"v_cvt_pk_bf16_f32 v152, v152, v153\n" \
"v_cvt_pk_bf16_f32 v153, v154, v155\n" \
"v_mfma_f32_16x16x32_bf16 v[156:159], a[28:31], a[124:127], v[156:159]\n" \
"buffer_load_dwordx4 v[116:119], v20, s[56:59], s72 offen offset:0 sc1\n" \
"v_mfma_f32_16x16x32_bf16 v[156:159], a[24:27], a[120:123], v[156:159]\n" \
"v_mfma_f32_16x16x32_bf16 v[156:159], a[20:23], a[116:119], v[156:159]\n" \
"v_mfma_f32_16x16x32_bf16 v[156:159], a[16:19], a[112:115], v[156:159]\n" \
"v_mfma_f32_16x16x32_bf16 v[224:227], a[16:19], a[220:223], v[224:227]\n" \
"v_mfma_f32_16x16x32_bf16 v[224:227], a[20:23], a[224:227], v[224:227]\n" \
"buffer_load_dwordx4 v[124:127], v20, s[56:59], s73 offen offset:2048 sc1\n" \
"v_mfma_f32_16x16x32_bf16 v[224:227], a[24:27], a[228:231], v[224:227]\n" \
"v_mfma_f32_16x16x32_bf16 v[224:227], a[28:31], a[232:235], v[224:227]\n" \
"v_mfma_f32_16x16x32_bf16 v[228:231], a[28:31], a[248:251], v[228:231]\n" \
"ds_read_b64_tr_b16 a[28:29], v10 offset:46080\n" \
"ds_read_b64_tr_b16 a[30:31], v10 offset:50688\n" \
"v_cvt_pk_bf16_f32 v154, v156, v157\n" \
"v_cvt_pk_bf16_f32 v155, v158, v159\n" \
"v_mfma_f32_16x16x32_bf16 v[228:231], a[24:27], a[244:247], v[228:231]\n" \
"buffer_store_dwordx4 v[152:155], v20, s[56:59], s94 offen offset:0 sc1\n" \
"v_mfma_f32_16x16x32_bf16 v[228:231], a[20:23], a[240:243], v[228:231]\n" \
"ds_read_b64_tr_b16 a[20:21], v10 offset:27648\n" \
"v_cvt_pk_bf16_f32 v224, v224, v225\n" \
"v_cvt_pk_bf16_f32 v225, v226, v227\n" \
"v_mfma_f32_16x16x32_bf16 v[228:231], a[16:19], a[236:239], v[228:231]\n" \
"ds_read_b64_tr_b16 a[16:17], v33 offset:192\n" \
"ds_read_b64_tr_b16 a[18:19], v33 offset:4800\n" \
"v_mfma_f32_16x16x32_bf16 v[232:235], a[192:195], a[32:35], v[232:235]\n" \
"ds_read_b64_tr_b16 a[22:23], v10 offset:32256\n" \
"ds_read_b64_tr_b16 a[24:25], v10 offset:36864\n" \
"v_mfma_f32_16x16x32_bf16 v[232:235], a[196:199], a[36:39], v[232:235]\n" \
"ds_read_b64_tr_b16 a[26:27], v10 offset:41472\n" \
"buffer_load_dwordx4 v[132:135], v38, s[56:59], s92 offen offset:0 sc1\n" \
"v_cvt_pk_bf16_f32 v226, v228, v229\n" \
"v_cvt_pk_bf16_f32 v227, v230, v231\n" \
"v_mfma_f32_16x16x32_bf16 v[232:235], a[200:203], a[40:43], v[232:235]\n" \
"v_readlane_b32 s92, v11, 50\n" \
"v_mfma_f32_16x16x32_bf16 v[232:235], a[204:207], a[44:47], v[232:235]\n" \
"buffer_store_dwordx4 v[224:227], v20, s[56:59], s94 offen offset:1024 sc1\n" \
"s_add_u32 s94, s94, 0x1000\n" \
"v_mfma_f32_16x16x32_bf16 v[236:239], a[204:207], a[60:63], v[236:239]\n" \
"ds_read_b64_tr_b16 a[204:205], v33 offset:36864\n" \
"ds_read_b64_tr_b16 a[206:207], v33 offset:41472\n" \
"v_mfma_f32_16x16x32_bf16 v[236:239], a[200:203], a[56:59], v[236:239]\n" \
"ds_read_b64_tr_b16 a[200:201], v33 offset:27648\n" \
"ds_read_b64_tr_b16 a[202:203], v33 offset:32256\n" \
"v_mfma_f32_16x16x32_bf16 v[236:239], a[196:199], a[52:55], v[236:239]\n" \
"ds_read_b64_tr_b16 a[196:197], v33 offset:18432\n" \
"ds_read_b64_tr_b16 a[198:199], v33 offset:23040\n" \
"v_mfma_f32_16x16x32_bf16 v[236:239], a[192:195], a[48:51], v[236:239]\n" \
"ds_read_b64_tr_b16 a[192:193], v10 offset:55296\n" \
"ds_read_b64_tr_b16 a[194:195], v10 offset:59904\n" \
"buffer_load_dwordx4 v[140:143], v38, s[56:59], s93 offen offset:2048 sc1\n" \
"v_mfma_f32_16x16x32_bf16 v[160:163], a[208:211], a[128:131], v[160:163]\n" \
"v_readlane_b32 s93, v11, 41\n" \
"v_mfma_f32_16x16x32_bf16 v[160:163], a[212:215], a[132:135], v[160:163]\n" \
"v_mfma_f32_16x16x32_bf16 v[160:163], a[216:219], a[136:139], v[160:163]\n" \
"s_waitcnt lgkmcnt(14)\n" \
"v_mfma_f32_16x16x32_bf16 v[160:163], a[0:3], a[140:143], v[160:163]\n" \
"buffer_load_dword v37, v40, s[56:59], s74 offen sc1\n" \
"v_mfma_f32_16x16x32_bf16 v[164:167], a[0:3], a[156:159], v[164:167]\n" \
"v_mfma_f32_16x16x32_bf16 v[164:167], a[216:219], a[152:155], v[164:167]\n" \
"v_mfma_f32_16x16x32_bf16 v[164:167], a[212:215], a[148:151], v[164:167]\n" \
"v_mfma_f32_16x16x32_bf16 v[164:167], a[208:211], a[144:147], v[164:167]\n" \
"v_mfma_f32_16x16x32_bf16 v[232:235], a[208:211], a[160:163], v[232:235]\n" \
"v_mfma_f32_16x16x32_bf16 v[232:235], a[212:215], a[164:167], v[232:235]\n" \
"v_mfma_f32_16x16x32_bf16 v[232:235], a[216:219], a[168:171], v[232:235]\n" \
"v_mfma_f32_16x16x32_bf16 v[232:235], a[0:3], a[172:175], v[232:235]\n" \
"s_waitcnt vmcnt(11)\n" \
"s_mov_b64 exec, 1\n" \
"buffer_store_dword v39, off, s[56:59], s71 offset:20 sc1\n" \
"s_mov_b64 exec, -1\n" \
"v_mfma_f32_16x16x32_bf16 v[236:239], a[0:3], a[188:191], v[236:239]\n" \
"ds_read_b64_tr_b16 a[0:1], v34 offset:18432\n" \
"ds_read_b64_tr_b16 a[2:3], v34 offset:23040\n" \
"v_mfma_f32_16x16x32_bf16 v[236:239], a[216:219], a[184:187], v[236:239]\n" \
"ds_read_b64_tr_b16 a[216:217], v34 offset:9216\n" \
"ds_read_b64_tr_b16 a[218:219], v34 offset:13824\n" \
"v_mfma_f32_16x16x32_bf16 v[236:239], a[212:215], a[180:183], v[236:239]\n" \
"ds_read_b64_tr_b16 a[212:213], v34 offset:0\n" \
"ds_read_b64_tr_b16 a[214:215], v34 offset:4608\n" \
"v_mfma_f32_16x16x32_bf16 v[236:239], a[208:211], a[176:179], v[236:239]\n" \
"ds_read_b64_tr_b16 a[208:209], v33 offset:46080\n" \
"ds_read_b64_tr_b16 a[210:211], v33 offset:50688\n" \
"v_mfma_f32_16x16x32_bf16 v[160:163], a[4:7], a[96:99], v[160:163]\n" \
"v_mfma_f32_16x16x32_bf16 v[160:163], a[8:11], a[100:103], v[160:163]\n" \
"v_mfma_f32_16x16x32_bf16 v[160:163], a[12:15], a[104:107], v[160:163]\n" \
"s_waitcnt lgkmcnt(8)\n" \
"v_mfma_f32_16x16x32_bf16 v[160:163], a[16:19], a[108:111], v[160:163]\n" \
"v_mfma_f32_16x16x32_bf16 v[164:167], a[16:19], a[124:127], v[164:167]\n" \
"v_mfma_f32_16x16x32_bf16 v[164:167], a[12:15], a[120:123], v[164:167]\n" \
"v_mfma_f32_16x16x32_bf16 v[164:167], a[8:11], a[116:119], v[164:167]\n" \
"v_mfma_f32_16x16x32_bf16 v[164:167], a[4:7], a[112:115], v[164:167]\n" \
"v_mfma_f32_16x16x32_bf16 v[232:235], a[4:7], a[220:223], v[232:235]\n" \
"v_mfma_f32_16x16x32_bf16 v[232:235], a[8:11], a[224:227], v[232:235]\n" \
"v_mfma_f32_16x16x32_bf16 v[232:235], a[12:15], a[228:231], v[232:235]\n" \
"v_mfma_f32_16x16x32_bf16 v[232:235], a[16:19], a[232:235], v[232:235]\n" \
"v_cvt_pk_bf16_f32 v160, v160, v161\n" \
"v_cvt_pk_bf16_f32 v161, v162, v163\n" \
"v_mfma_f32_16x16x32_bf16 v[236:239], a[16:19], a[248:251], v[236:239]\n" \
"ds_read_b64_tr_b16 a[16:17], v10 offset:46112\n" \
"v_cvt_pk_bf16_f32 v162, v164, v165\n" \
"v_cvt_pk_bf16_f32 v163, v166, v167\n" \
"v_mfma_f32_16x16x32_bf16 v[236:239], a[12:15], a[244:247], v[236:239]\n" \
"buffer_store_dwordx4 v[160:163], v20, s[56:59], s95 offen offset:0 sc1\n" \
"v_cvt_pk_bf16_f32 v232, v232, v233\n" \
"v_cvt_pk_bf16_f32 v233, v234, v235\n" \
"v_mfma_f32_16x16x32_bf16 v[236:239], a[8:11], a[240:243], v[236:239]\n" \
"ds_read_b64_tr_b16 a[8:9], v10 offset:27680\n" \
"ds_read_b64_tr_b16 a[10:11], v10 offset:32288\n" \
"v_mfma_f32_16x16x32_bf16 v[236:239], a[4:7], a[236:239], v[236:239]\n" \
"ds_read_b64_tr_b16 a[4:5], v33 offset:0\n" \
"ds_read_b64_tr_b16 a[6:7], v33 offset:4608\n" \
"v_mfma_f32_16x16x32_bf16 v[184:187], a[20:23], a[32:35], v[184:187]\n" \
"ds_read_b64_tr_b16 a[12:13], v10 offset:36896\n" \
"ds_read_b64_tr_b16 a[14:15], v10 offset:41504\n" \
"v_mfma_f32_16x16x32_bf16 v[184:187], a[24:27], a[36:39], v[184:187]\n" \
"ds_read_b64_tr_b16 a[18:19], v10 offset:50720\n" \
"v_mfma_f32_16x16x32_bf16 v[184:187], a[28:31], a[40:43], v[184:187]\n" \
"v_cvt_pk_bf16_f32 v234, v236, v237\n" \
"v_cvt_pk_bf16_f32 v235, v238, v239\n" \
"v_mfma_f32_16x16x32_bf16 v[184:187], a[192:195], a[44:47], v[184:187]\n" \
"buffer_store_dwordx4 v[232:235], v20, s[56:59], s95 offen offset:1024 sc1\n" \
"s_add_u32 s95, s95, 0x800\n" \
"v_mfma_f32_16x16x32_bf16 v[188:191], a[192:195], a[60:63], v[188:191]\n" \
"ds_read_b64_tr_b16 a[192:193], v33 offset:36896\n" \
"ds_read_b64_tr_b16 a[194:195], v33 offset:41504\n" \
"v_mfma_f32_16x16x32_bf16 v[188:191], a[28:31], a[56:59], v[188:191]\n" \
"ds_read_b64_tr_b16 a[28:29], v33 offset:27680\n" \
"ds_read_b64_tr_b16 a[30:31], v33 offset:32288\n" \
"v_mfma_f32_16x16x32_bf16 v[188:191], a[24:27], a[52:55], v[188:191]\n" \
"ds_read_b64_tr_b16 a[24:25], v33 offset:18464\n" \
"ds_read_b64_tr_b16 a[26:27], v33 offset:23072\n" \
"s_mov_b64 exec, 1\n" \
"buffer_store_dword v46, off, s[56:59], s92 offset:0 sc1\n" \
"s_mov_b64 exec, -1\n" \
"v_mfma_f32_16x16x32_bf16 v[188:191], a[20:23], a[48:51], v[188:191]\n" \
"ds_read_b64_tr_b16 a[20:21], v10 offset:55328\n" \
"ds_read_b64_tr_b16 a[22:23], v10 offset:59936\n" \
"v_mfma_f32_16x16x32_bf16 v[184:187], a[196:199], a[160:163], v[184:187]\n" \
"v_mfma_f32_16x16x32_bf16 v[184:187], a[200:203], a[164:167], v[184:187]\n" \
"v_mfma_f32_16x16x32_bf16 v[184:187], a[204:207], a[168:171], v[184:187]\n" \
"s_waitcnt lgkmcnt(15)\n" \
"v_mfma_f32_16x16x32_bf16 v[184:187], a[208:211], a[172:175], v[184:187]\n" \
"v_mfma_f32_16x16x32_bf16 v[188:191], a[208:211], a[188:191], v[188:191]\n" \
"ds_read_b64_tr_b16 a[208:209], v34 offset:18464\n" \
"ds_read_b64_tr_b16 a[210:211], v34 offset:23072\n" \
"v_mfma_f32_16x16x32_bf16 v[188:191], a[204:207], a[184:187], v[188:191]\n" \
"ds_read_b64_tr_b16 a[204:205], v34 offset:9248\n" \
"ds_read_b64_tr_b16 a[206:207], v34 offset:13856\n" \
"v_mfma_f32_16x16x32_bf16 v[188:191], a[200:203], a[180:183], v[188:191]\n" \
"ds_read_b64_tr_b16 a[200:201], v34 offset:32\n" \
"ds_read_b64_tr_b16 a[202:203], v34 offset:4640\n" \
"v_mfma_f32_16x16x32_bf16 v[188:191], a[196:199], a[176:179], v[188:191]\n" \
"ds_read_b64_tr_b16 a[196:197], v33 offset:46112\n" \
"ds_read_b64_tr_b16 a[198:199], v33 offset:50720\n" \
"v_mfma_f32_16x16x32_bf16 v[184:187], a[212:215], a[220:223], v[184:187]\n" \
"v_mfma_f32_16x16x32_bf16 v[184:187], a[216:219], a[224:227], v[184:187]\n" \
"v_mfma_f32_16x16x32_bf16 v[184:187], a[0:3], a[228:231], v[184:187]\n" \
"s_waitcnt lgkmcnt(13)\n" \
"v_mfma_f32_16x16x32_bf16 v[184:187], a[4:7], a[232:235], v[184:187]\n" \
"v_mfma_f32_16x16x32_bf16 v[188:191], a[4:7], a[248:251], v[188:191]\n" \
"ds_read_b64_tr_b16 a[4:5], v10 offset:46144\n" \
"ds_read_b64_tr_b16 a[6:7], v10 offset:50752\n" \
"v_mfma_f32_16x16x32_bf16 v[188:191], a[0:3], a[244:247], v[188:191]\n" \
"ds_read_b64_tr_b16 a[0:1], v10 offset:36928\n" \
"ds_read_b64_tr_b16 a[2:3], v10 offset:41536\n" \
"v_mfma_f32_16x16x32_bf16 v[188:191], a[216:219], a[240:243], v[188:191]\n" \
"ds_read_b64_tr_b16 a[216:217], v10 offset:27712\n" \
"v_cvt_pk_bf16_f32 v184, v184, v185\n" \
"v_cvt_pk_bf16_f32 v185, v186, v187\n" \
"v_mfma_f32_16x16x32_bf16 v[188:191], a[212:215], a[236:239], v[188:191]\n" \
"ds_read_b64_tr_b16 a[212:213], v33 offset:32\n" \
"ds_read_b64_tr_b16 a[214:215], v33 offset:4640\n" \
"s_waitcnt vmcnt(6)\n" \
"s_mov_b64 exec, 1\n" \
"buffer_store_dword v39, off, s[56:59], s94 offset:24 sc1\n" \
"s_mov_b64 exec, -1\n" \
"s_waitcnt vmcnt(5)\n" \
"v_cmp_eq_u32_e64 s[98:99], s62, v37\n" \
"v_cvt_pk_bf16_f32 v186, v188, v189\n" \
"v_cvt_pk_bf16_f32 v187, v190, v191\n" \
"v_mfma_f32_16x16x32_bf16 v[192:195], a[8:11], a[32:35], v[192:195]\n" \
"ds_read_b64_tr_b16 a[218:219], v10 offset:32320\n" \
"s_and_b32 s99, s98, 0x0\n" \
"buffer_store_dwordx4 v[184:187], v20, s[56:59], s97 offen offset:1024 sc1\n" \
"s_add_u32 s97, s97, 0x3800\n" \
"v_mfma_f32_16x16x32_bf16 v[192:195], a[12:15], a[36:39], v[192:195]\n" \
"s_or_b32 s84, s84, s99\n" \
"v_readlane_b32 s94, v11, 39\n" \
"v_mfma_f32_16x16x32_bf16 v[192:195], a[16:19], a[40:43], v[192:195]\n" \
"s_waitcnt lgkmcnt(8)\n" \
"v_mfma_f32_16x16x32_bf16 v[192:195], a[20:23], a[44:47], v[192:195]\n" \
"s_andn2_b32 s99, s100, s98\n" \
"s_cselect_b32 s99, 0, 16\n" \
"v_mfma_f32_16x16x32_bf16 v[196:199], a[20:23], a[60:63], v[196:199]\n" \
"ds_read_b64_tr_b16 a[20:21], v33 offset:36928\n" \
"ds_read_b64_tr_b16 a[22:23], v33 offset:41536\n" \
"v_mfma_f32_16x16x32_bf16 v[196:199], a[16:19], a[56:59], v[196:199]\n" \
"ds_read_b64_tr_b16 a[16:17], v33 offset:27712\n" \
"ds_read_b64_tr_b16 a[18:19], v33 offset:32320\n" \
"v_mfma_f32_16x16x32_bf16 v[196:199], a[12:15], a[52:55], v[196:199]\n" \
"ds_read_b64_tr_b16 a[12:13], v33 offset:18496\n" \
"ds_read_b64_tr_b16 a[14:15], v33 offset:23104\n" \
"v_mfma_f32_16x16x32_bf16 v[196:199], a[8:11], a[48:51], v[196:199]\n" \
"ds_read_b64_tr_b16 a[8:9], v10 offset:55360\n" \
"ds_read_b64_tr_b16 a[10:11], v10 offset:59968\n" \
"v_mfma_f32_16x16x32_bf16 v[192:195], a[24:27], a[160:163], v[192:195]\n" \
"s_or_b32 s84, s84, s99\n" \
"s_bitcmp1_b32 s84, 4\n" \
"s_cselect_b32 s96, s75, 0x40000000\n" \
"buffer_load_dwordx4 v[176:179], v20, s[56:59], s96 offen offset:0 sc1\n" \
"v_mfma_f32_16x16x32_bf16 v[192:195], a[28:31], a[164:167], v[192:195]\n" \
"v_mfma_f32_16x16x32_bf16 v[192:195], a[192:195], a[168:171], v[192:195]\n" \
"v_mfma_f32_16x16x32_bf16 v[192:195], a[196:199], a[172:175], v[192:195]\n" \
"buffer_load_dword v37, v40, s[56:59], s74 offen sc1\n" \
"v_mfma_f32_16x16x32_bf16 v[196:199], a[196:199], a[188:191], v[196:199]\n" \
"ds_read_b64_tr_b16 a[196:197], v34 offset:18496\n" \
"ds_read_b64_tr_b16 a[198:199], v34 offset:23104\n" \
"v_mfma_f32_16x16x32_bf16 v[196:199], a[192:195], a[184:187], v[196:199]\n" \
"ds_read_b64_tr_b16 a[192:193], v34 offset:9280\n" \
"ds_read_b64_tr_b16 a[194:195], v34 offset:13888\n" \
"v_mfma_f32_16x16x32_bf16 v[196:199], a[28:31], a[180:183], v[196:199]\n" \
"buffer_load_dwordx4 v[248:251], v20, s[56:59], s96 offen offset:2048 sc1\n" \
"v_mfma_f32_16x16x32_bf16 v[196:199], a[24:27], a[176:179], v[196:199]\n" \
"ds_read_b64_tr_b16 a[24:25], v33 offset:46144\n" \
"ds_read_b64_tr_b16 a[26:27], v33 offset:50752\n" \
"v_mfma_f32_16x16x32_bf16 v[192:195], a[200:203], a[220:223], v[192:195]\n" \
"ds_read_b64_tr_b16 a[28:29], v34 offset:64\n" \
"ds_read_b64_tr_b16 a[30:31], v34 offset:4672\n" \
"v_mfma_f32_16x16x32_bf16 v[192:195], a[204:207], a[224:227], v[192:195]\n" \
"buffer_load_dwordx4 v[180:183], v38, s[56:59], s96 offen offset:0 sc1\n" \
"v_mfma_f32_16x16x32_bf16 v[192:195], a[208:211], a[228:231], v[192:195]\n" \
"s_waitcnt lgkmcnt(14)\n" \
"v_mfma_f32_16x16x32_bf16 v[192:195], a[212:215], a[232:235], v[192:195]\n" \
"v_mfma_f32_16x16x32_bf16 v[196:199], a[212:215], a[248:251], v[196:199]\n" \
"buffer_load_dwordx4 v[252:255], v38, s[56:59], s96 offen offset:2048 sc1\n" \
"v_mfma_f32_16x16x32_bf16 v[196:199], a[208:211], a[244:247], v[196:199]\n" \
"ds_read_b64_tr_b16 a[208:209], v10 offset:36960\n" \
"ds_read_b64_tr_b16 a[210:211], v10 offset:41568\n" \
"v_mfma_f32_16x16x32_bf16 v[196:199], a[204:207], a[240:243], v[196:199]\n" \
"ds_read_b64_tr_b16 a[204:205], v10 offset:27744\n" \
"ds_read_b64_tr_b16 a[206:207], v10 offset:32352\n" \
"v_cvt_pk_bf16_f32 v192, v192, v193\n" \
"v_cvt_pk_bf16_f32 v193, v194, v195\n" \
"v_mfma_f32_16x16x32_bf16 v[196:199], a[200:203], a[236:239], v[196:199]\n" \
"buffer_load_dwordx4 v[112:115], v47, s[56:59], s96 offen offset:0 sc1\n" \
"v_mfma_f32_16x16x32_bf16 v[200:203], a[216:219], a[32:35], v[200:203]\n" \
"ds_read_b64_tr_b16 a[200:201], v33 offset:64\n" \
"L_pf23:\n" \
"s_cbranch_execz L_retB\n" \
"s_nop 0\n" \
"ds_read_b64_tr_b16 a[202:203], v33 offset:4672\n" \
"v_mfma_f32_16x16x32_bf16 v[200:203], a[0:3], a[36:39], v[200:203]\n" \
"ds_read_b64_tr_b16 a[212:213], v10 offset:46176\n" \
"ds_read_b64_tr_b16 a[214:215], v10 offset:50784\n" \
"v_mfma_f32_16x16x32_bf16 v[200:203], a[4:7], a[40:43], v[200:203]\n" \
"v_cvt_pk_bf16_f32 v194, v196, v197\n" \
"v_cvt_pk_bf16_f32 v195, v198, v199\n" \
"s_waitcnt lgkmcnt(13)\n" \
"v_mfma_f32_16x16x32_bf16 v[200:203], a[8:11], a[44:47], v[200:203]\n" \
"buffer_store_dwordx4 v[192:195], v20, s[56:59], s64 offen offset:1024 sc1\n" \
"s_add_u32 s64, s64, 0x3000\n" \
"v_mfma_f32_16x16x32_bf16 v[204:207], a[8:11], a[60:63], v[204:207]\n" \
"ds_read_b64_tr_b16 a[8:9], v33 offset:36960\n" \
"ds_read_b64_tr_b16 a[10:11], v33 offset:41568\n" \
"v_mfma_f32_16x16x32_bf16 v[204:207], a[4:7], a[56:59], v[204:207]\n" \
"ds_read_b64_tr_b16 a[4:5], v33 offset:27744\n" \
"ds_read_b64_tr_b16 a[6:7], v33 offset:32352\n" \
"v_mfma_f32_16x16x32_bf16 v[204:207], a[0:3], a[52:55], v[204:207]\n" \
"buffer_load_dwordx4 v[120:123], v47, s[56:59], s96 offen offset:2048 sc1\n" \
"v_mfma_f32_16x16x32_bf16 v[204:207], a[216:219], a[48:51], v[204:207]\n" \
"ds_read_b64_tr_b16 a[216:217], v10 offset:55392\n" \
"ds_read_b64_tr_b16 a[218:219], v10 offset:60000\n" \
"v_mfma_f32_16x16x32_bf16 v[200:203], a[12:15], a[160:163], v[200:203]\n" \
"ds_read_b64_tr_b16 a[0:1], v33 offset:18528\n" \
"ds_read_b64_tr_b16 a[2:3], v33 offset:23136\n" \
"v_mfma_f32_16x16x32_bf16 v[200:203], a[16:19], a[164:167], v[200:203]\n" \
"buffer_load_dwordx4 v[128:131], v54, s[56:59], s96 offen offset:0 sc1\n" \
"v_mfma_f32_16x16x32_bf16 v[200:203], a[20:23], a[168:171], v[200:203]\n" \
"s_waitcnt lgkmcnt(15)\n" \
"v_mfma_f32_16x16x32_bf16 v[200:203], a[24:27], a[172:175], v[200:203]\n" \
"v_mfma_f32_16x16x32_bf16 v[204:207], a[24:27], a[188:191], v[204:207]\n" \
"buffer_load_dwordx4 v[136:139], v54, s[56:59], s96 offen offset:2048 sc1\n" \
"s_waitcnt vmcnt(13)\n" \
"s_mov_b64 exec, 1\n" \
"buffer_store_dword v39, off, s[56:59], s95 offset:28 sc1\n" \
"s_mov_b64 exec, -1\n" \
"v_mfma_f32_16x16x32_bf16 v[204:207], a[20:23], a[184:187], v[204:207]\n" \
"ds_read_b64_tr_b16 a[20:21], v34 offset:9312\n" \
"ds_read_b64_tr_b16 a[22:23], v34 offset:13920\n" \
"v_mfma_f32_16x16x32_bf16 v[204:207], a[16:19], a[180:183], v[204:207]\n" \
"ds_read_b64_tr_b16 a[16:17], v34 offset:96\n" \
"ds_read_b64_tr_b16 a[18:19], v34 offset:4704\n" \
"v_mfma_f32_16x16x32_bf16 v[204:207], a[12:15], a[176:179], v[204:207]\n" \
"ds_read_b64_tr_b16 a[12:13], v33 offset:46176\n" \
"ds_read_b64_tr_b16 a[14:15], v33 offset:50784\n" \
"v_mfma_f32_16x16x32_bf16 v[200:203], a[28:31], a[220:223], v[200:203]\n" \
"ds_read_b64_tr_b16 a[24:25], v34 offset:18528\n" \
"ds_read_b64_tr_b16 a[26:27], v34 offset:23136\n" \
"v_mfma_f32_16x16x32_bf16 v[200:203], a[192:195], a[224:227], v[200:203]\n" \
"v_readlane_b32 s95, v11, 40\n" \
"v_mfma_f32_16x16x32_bf16 v[200:203], a[196:199], a[228:231], v[200:203]\n" \
"s_waitcnt lgkmcnt(12)\n" \
"v_mfma_f32_16x16x32_bf16 v[200:203], a[200:203], a[232:235], v[200:203]\n" \
"v_mfma_f32_16x16x32_bf16 v[204:207], a[200:203], a[248:251], v[204:207]\n" \
"ds_read_b64_tr_b16 a[200:201], v10 offset:46304\n" \
"ds_read_b64_tr_b16 a[202:203], v10 offset:50912\n" \
"v_mfma_f32_16x16x32_bf16 v[204:207], a[196:199], a[244:247], v[204:207]\n" \
"ds_read_b64_tr_b16 a[196:197], v10 offset:37088\n" \
"ds_read_b64_tr_b16 a[198:199], v10 offset:41696\n" \
"v_mfma_f32_16x16x32_bf16 v[204:207], a[192:195], a[240:243], v[204:207]\n" \
"ds_read_b64_tr_b16 a[192:193], v10 offset:27872\n" \
"v_cvt_pk_bf16_f32 v200, v200, v201\n" \
"v_cvt_pk_bf16_f32 v201, v202, v203\n" \
"v_mfma_f32_16x16x32_bf16 v[204:207], a[28:31], a[236:239], v[204:207]\n" \
"ds_read_b64_tr_b16 a[28:29], v33 offset:96\n" \
"ds_read_b64_tr_b16 a[30:31], v33 offset:4704\n" \
"v_mfma_f32_16x16x32_bf16 v[208:211], a[204:207], a[32:35], v[208:211]\n" \
"ds_read_b64_tr_b16 a[194:195], v10 offset:32480\n" \
"v_mfma_f32_16x16x32_bf16 v[208:211], a[208:211], a[36:39], v[208:211]\n" \
"v_mfma_f32_16x16x32_bf16 v[208:211], a[212:215], a[40:43], v[208:211]\n" \
"s_waitcnt lgkmcnt(15)\n" \
"v_mfma_f32_16x16x32_bf16 v[208:211], a[216:219], a[44:47], v[208:211]\n" \
"v_cvt_pk_bf16_f32 v202, v204, v205\n" \
"v_cvt_pk_bf16_f32 v203, v206, v207\n" \
"v_mfma_f32_16x16x32_bf16 v[212:215], a[216:219], a[60:63], v[212:215]\n" \
"buffer_store_dwordx4 v[200:203], v20, s[56:59], s66 offen offset:1024 sc1\n" \
"s_add_u32 s66, s66, 0x2800\n" \
"v_mfma_f32_16x16x32_bf16 v[212:215], a[212:215], a[56:59], v[212:215]\n" \
"ds_read_b64_tr_b16 a[212:213], v33 offset:27872\n" \
"ds_read_b64_tr_b16 a[214:215], v33 offset:32480\n" \
"v_mfma_f32_16x16x32_bf16 v[212:215], a[208:211], a[52:55], v[212:215]\n" \
"ds_read_b64_tr_b16 a[208:209], v33 offset:18656\n" \
"ds_read_b64_tr_b16 a[210:211], v33 offset:23264\n" \
"v_mfma_f32_16x16x32_bf16 v[212:215], a[204:207], a[48:51], v[212:215]\n" \
"ds_read_b64_tr_b16 a[204:205], v10 offset:55520\n" \
"ds_read_b64_tr_b16 a[206:207], v10 offset:60128\n" \
"v_mfma_f32_16x16x32_bf16 v[208:211], a[0:3], a[160:163], v[208:211]\n" \
"ds_read_b64_tr_b16 a[216:217], v33 offset:37088\n" \
"ds_read_b64_tr_b16 a[218:219], v33 offset:41696\n" \
"v_mfma_f32_16x16x32_bf16 v[208:211], a[4:7], a[164:167], v[208:211]\n" \
"v_mfma_f32_16x16x32_bf16 v[208:211], a[8:11], a[168:171], v[208:211]\n" \
"s_waitcnt lgkmcnt(14)\n" \
"v_mfma_f32_16x16x32_bf16 v[208:211], a[12:15], a[172:175], v[208:211]\n" \
"v_mfma_f32_16x16x32_bf16 v[212:215], a[12:15], a[188:191], v[212:215]\n" \
"ds_read_b64_tr_b16 a[12:13], v34 offset:18656\n" \
"ds_read_b64_tr_b16 a[14:15], v34 offset:23264\n" \
"s_waitcnt vmcnt(12)\n" \
"s_mov_b64 exec, 1\n" \
"buffer_store_dword v39, off, s[56:59], s97 offset:32 sc1\n" \
"s_mov_b64 exec, -1\n" \
"v_mfma_f32_16x16x32_bf16 v[212:215], a[8:11], a[184:187], v[212:215]\n" \
"ds_read_b64_tr_b16 a[8:9], v34 offset:9440\n" \
"ds_read_b64_tr_b16 a[10:11], v34 offset:14048\n" \
"v_mfma_f32_16x16x32_bf16 v[212:215], a[4:7], a[180:183], v[212:215]\n" \
"ds_read_b64_tr_b16 a[4:5], v34 offset:224\n" \
"ds_read_b64_tr_b16 a[6:7], v34 offset:4832\n" \
"v_mfma_f32_16x16x32_bf16 v[212:215], a[0:3], a[176:179], v[212:215]\n" \
"ds_read_b64_tr_b16 a[0:1], v33 offset:46304\n" \
"ds_read_b64_tr_b16 a[2:3], v33 offset:50912\n" \
"v_mfma_f32_16x16x32_bf16 v[208:211], a[16:19], a[220:223], v[208:211]\n" \
"s_waitcnt vmcnt(11)\n" \
"v_cmp_eq_u32_e64 s[98:99], s62, v37\n" \
"buffer_load_dword v37, v40, s[56:59], s74 offen sc1\n" \
"v_mfma_f32_16x16x32_bf16 v[208:211], a[20:23], a[224:227], v[208:211]\n" \
"s_and_b32 s99, s98, 0x20\n" \
"s_or_b32 s84, s84, s99\n" \
"s_bitcmp1_b32 s84, 5\n" \
"s_cselect_b32 s65, s63, 0x40000000\n" \
"v_mfma_f32_16x16x32_bf16 v[208:211], a[24:27], a[228:231], v[208:211]\n" \
"s_waitcnt lgkmcnt(12)\n" \
"v_mfma_f32_16x16x32_bf16 v[208:211], a[28:31], a[232:235], v[208:211]\n" \
"v_mfma_f32_16x16x32_bf16 v[212:215], a[28:31], a[248:251], v[212:215]\n" \
"buffer_load_dwordx4 v[148:151], v47, s[56:59], s65 offen offset:0 sc1\n" \
"v_mfma_f32_16x16x32_bf16 v[212:215], a[24:27], a[244:247], v[212:215]\n" \
"ds_read_b64_tr_b16 a[24:25], v36 offset:2048\n" \
"ds_read_b64_tr_b16 a[26:27], v36 offset:3072\n" \
"v_mfma_f32_16x16x32_bf16 v[212:215], a[20:23], a[240:243], v[212:215]\n" \
"ds_read_b64_tr_b16 a[20:21], v36 offset:0\n" \
"ds_read_b64_tr_b16 a[22:23], v36 offset:1024\n" \
"v_cvt_pk_bf16_f32 v208, v208, v209\n" \
"v_cvt_pk_bf16_f32 v209, v210, v211\n" \
"v_mfma_f32_16x16x32_bf16 v[212:215], a[16:19], a[236:239], v[212:215]\n" \
"ds_read_b64_tr_b16 a[16:17], v33 offset:224\n" \
"ds_read_b64_tr_b16 a[18:19], v33 offset:4832\n" \
"v_mfma_f32_16x16x32_bf16 v[240:243], a[192:195], a[32:35], v[240:243]\n" \
"buffer_load_dwordx4 v[220:223], v47, s[56:59], s65 offen offset:1024 sc1\n" \
"v_mfma_f32_16x16x32_bf16 v[240:243], a[196:199], a[36:39], v[240:243]\n" \
"ds_read_b64_tr_b16 a[28:29], v36 offset:32\n" \
"ds_read_b64_tr_b16 a[30:31], v36 offset:1056\n" \
"v_mfma_f32_16x16x32_bf16 v[240:243], a[200:203], a[40:43], v[240:243]\n" \
"v_cvt_pk_bf16_f32 v210, v212, v213\n" \
"v_cvt_pk_bf16_f32 v211, v214, v215\n" \
"s_waitcnt lgkmcnt(10)\n" \
"v_mfma_f32_16x16x32_bf16 v[240:243], a[204:207], a[44:47], v[240:243]\n" \
"buffer_store_dwordx4 v[208:211], v20, s[56:59], s67 offen offset:1024 sc1\n" \
"s_add_u32 s67, s67, 0x2000\n" \
"v_mfma_f32_16x16x32_bf16 v[244:247], a[204:207], a[60:63], v[244:247]\n" \
"v_mfma_f32_16x16x32_bf16 v[244:247], a[200:203], a[56:59], v[244:247]\n" \
"v_mfma_f32_16x16x32_bf16 v[244:247], a[196:199], a[52:55], v[244:247]\n" \
"v_mfma_f32_16x16x32_bf16 v[244:247], a[192:195], a[48:51], v[244:247]\n" \
"ds_read_b64_tr_b16 a[192:193], v36 offset:2080\n" \
"ds_read_b64_tr_b16 a[194:195], v36 offset:3104\n" \
"v_mfma_f32_16x16x32_bf16 v[168:171], a[208:211], a[128:131], v[168:171]\n" \
"v_mfma_f32_16x16x32_bf16 v[168:171], a[212:215], a[132:135], v[168:171]\n" \
"v_mfma_f32_16x16x32_bf16 v[168:171], a[216:219], a[136:139], v[168:171]\n" \
"s_waitcnt lgkmcnt(9)\n" \
"v_mfma_f32_16x16x32_bf16 v[168:171], a[0:3], a[140:143], v[168:171]\n" \
"v_mfma_f32_16x16x32_bf16 v[172:175], a[0:3], a[156:159], v[172:175]\n" \
"v_mfma_f32_16x16x32_bf16 v[172:175], a[216:219], a[152:155], v[172:175]\n" \
"s_waitcnt vmcnt(10)\n" \
"s_mov_b64 exec, 1\n" \
"buffer_store_dword v39, off, s[56:59], s64 offset:36 sc1\n" \
"s_mov_b64 exec, -1\n" \
"v_mfma_f32_16x16x32_bf16 v[172:175], a[212:215], a[148:151], v[172:175]\n" \
"v_mfma_f32_16x16x32_bf16 v[172:175], a[208:211], a[144:147], v[172:175]\n" \
"v_mfma_f32_16x16x32_bf16 v[240:243], a[208:211], a[160:163], v[240:243]\n" \
"v_mfma_f32_16x16x32_bf16 v[240:243], a[212:215], a[164:167], v[240:243]\n" \
"v_mfma_f32_16x16x32_bf16 v[240:243], a[216:219], a[168:171], v[240:243]\n" \
"v_mfma_f32_16x16x32_bf16 v[240:243], a[0:3], a[172:175], v[240:243]\n" \
"v_mfma_f32_16x16x32_bf16 v[244:247], a[0:3], a[188:191], v[244:247]\n" \
"v_mfma_f32_16x16x32_bf16 v[244:247], a[216:219], a[184:187], v[244:247]\n" \
"v_mfma_f32_16x16x32_bf16 v[244:247], a[212:215], a[180:183], v[244:247]\n" \
"v_mfma_f32_16x16x32_bf16 v[244:247], a[208:211], a[176:179], v[244:247]\n" \
"v_mfma_f32_16x16x32_bf16 v[168:171], a[4:7], a[96:99], v[168:171]\n" \
"v_mfma_f32_16x16x32_bf16 v[168:171], a[8:11], a[100:103], v[168:171]\n" \
"v_mfma_f32_16x16x32_bf16 v[168:171], a[12:15], a[104:107], v[168:171]\n" \
"s_waitcnt vmcnt(6)\n" \
"s_mov_b64 exec, 1\n" \
"buffer_store_dword v39, off, s[56:59], s66 offset:40 sc1\n" \
"s_mov_b64 exec, -1\n" \
"s_waitcnt lgkmcnt(0)\n" \
"v_mfma_f32_16x16x32_bf16 v[168:171], a[16:19], a[108:111], v[168:171]\n" \
"v_mfma_f32_16x16x32_bf16 v[172:175], a[16:19], a[124:127], v[172:175]\n" \
"v_mfma_f32_16x16x32_bf16 v[172:175], a[12:15], a[120:123], v[172:175]\n" \
"v_mfma_f32_16x16x32_bf16 v[172:175], a[8:11], a[116:119], v[172:175]\n" \
"v_mfma_f32_16x16x32_bf16 v[172:175], a[4:7], a[112:115], v[172:175]\n" \
"v_mfma_f32_16x16x32_bf16 v[240:243], a[4:7], a[220:223], v[240:243]\n" \
"v_mfma_f32_16x16x32_bf16 v[240:243], a[8:11], a[224:227], v[240:243]\n" \
"v_mfma_f32_16x16x32_bf16 v[240:243], a[12:15], a[228:231], v[240:243]\n" \
"v_mfma_f32_16x16x32_bf16 v[240:243], a[16:19], a[232:235], v[240:243]\n" \
"v_mfma_f32_16x16x32_bf16 v[244:247], a[16:19], a[248:251], v[244:247]\n" \
"v_mfma_f32_16x16x32_bf16 v[244:247], a[12:15], a[244:247], v[244:247]\n" \
"v_mfma_f32_16x16x32_bf16 v[244:247], a[8:11], a[240:243], v[244:247]\n" \
"v_mfma_f32_16x16x32_bf16 v[244:247], a[4:7], a[236:239], v[244:247]\n" \
"v_mfma_f32_16x16x32_bf16 v[168:171], a[20:23], v[64:67], v[168:171]\n" \
"v_mfma_f32_16x16x32_bf16 v[172:175], a[20:23], v[72:75], v[172:175]\n" \
"v_mfma_f32_16x16x32_bf16 v[240:243], a[20:23], v[80:83], v[240:243]\n" \
"v_mfma_f32_16x16x32_bf16 v[244:247], a[20:23], v[88:91], v[244:247]\n" \
"v_mfma_f32_16x16x32_bf16 v[168:171], a[24:27], v[68:71], v[168:171]\n" \
"v_mfma_f32_16x16x32_bf16 v[172:175], a[24:27], v[76:79], v[172:175]\n" \
"v_mfma_f32_16x16x32_bf16 v[240:243], a[24:27], v[84:87], v[240:243]\n" \
"s_waitcnt vmcnt(5)\n" \
"v_cmp_eq_u32_e64 s[98:99], s62, v37\n" \
"v_mfma_f32_16x16x32_bf16 v[244:247], a[24:27], v[92:95], v[244:247]\n" \
"s_and_b32 s99, s98, 0xfc0\n" \
"s_or_b32 s84, s84, s99\n" \
"s_bitcmp1_b32 s84, 6\n" \
"s_cselect_b32 s68, s63, 0x40000000\n" \
"s_bitcmp1_b32 s84, 7\n" \
"s_cselect_b32 s69, s63, 0x40000000\n" \
"s_bitcmp1_b32 s84, 8\n" \
"s_cselect_b32 s70, s63, 0x40000000\n" \
"s_bitcmp1_b32 s84, 9\n" \
"s_cselect_b32 s71, s63, 0x40000000\n" \
"s_bitcmp1_b32 s84, 10\n" \
"s_cselect_b32 s72, s63, 0x40000000\n" \
"s_bitcmp1_b32 s84, 11\n" \
"s_cselect_b32 s73, s63, 0x40000000\n" \
"buffer_load_dwordx4 v[156:159], v47, s[56:59], s68 offen offset:2048 sc1\n" \
"v_mfma_f32_16x16x32_bf16 v[56:59], a[28:31], v[96:99], 0\n" \
"s_waitcnt vmcnt(3)\n" \
"s_mov_b64 exec, 1\n" \
"buffer_store_dword v39, off, s[56:59], s67 offset:44 sc1\n" \
"s_mov_b64 exec, -1\n" \
"v_mfma_f32_16x16x32_bf16 v[60:63], a[28:31], v[104:107], 0\n" \
"v_mfma_f32_16x16x32_bf16 v[56:59], a[192:195], v[100:103], v[56:59]\n" \
"v_mfma_f32_16x16x32_bf16 v[60:63], a[192:195], v[108:111], v[60:63]\n" \
"buffer_load_dwordx4 v[228:231], v47, s[56:59], s68 offen offset:3072 sc1\n" \
"buffer_load_dwordx4 v[164:167], v54, s[56:59], s69 offen offset:0 sc1\n" \
"buffer_load_dwordx4 v[236:239], v54, s[56:59], s69 offen offset:1024 sc1\n" \
"buffer_load_dwordx4 v[188:191], v20, s[56:59], s70 offen offset:1024 sc1\n" \
"buffer_load_dwordx4 v[196:199], v20, s[56:59], s71 offen offset:3072 sc1\n" \
"buffer_load_dwordx4 v[204:207], v38, s[56:59], s72 offen offset:1024 sc1\n" \
"buffer_load_dwordx4 v[212:215], v38, s[56:59], s73 offen offset:3072 sc1\n" \
"s_andn2_b32 s99, 0xff, s84\n" \
"s_cbranch_scc1 L_sx0_%=\n" \
"L_rx0_%=:\n" \
"v_mfma_f32_16x16x32_bf16 v[168:171], v[50:53], v[116:119], v[168:171]\n" \
"v_mfma_f32_16x16x32_bf16 v[172:175], v[48:51], v[116:119], v[172:175]\n" \
"v_mfma_f32_16x16x32_bf16 v[168:171], v[50:53], v[124:127], v[168:171]\n" \
"v_mfma_f32_16x16x32_bf16 v[172:175], v[48:51], v[124:127], v[172:175]\n" \
"v_mfma_f32_16x16x32_bf16 v[168:171], v[50:53], v[132:135], v[168:171]\n" \
"v_mfma_f32_16x16x32_bf16 v[172:175], v[48:51], v[132:135], v[172:175]\n" \
"v_mfma_f32_16x16x32_bf16 v[168:171], v[50:53], v[140:143], v[168:171]\n" \
"v_mfma_f32_16x16x32_bf16 v[172:175], v[48:51], v[140:143], v[172:175]\n" \
"v_mfma_f32_16x16x32_bf16 v[56:59], v[50:53], v[176:179], v[56:59]\n" \
"v_mfma_f32_16x16x32_bf16 v[60:63], v[48:51], v[176:179], v[60:63]\n" \
"v_mfma_f32_16x16x32_bf16 v[56:59], v[50:53], v[248:251], v[56:59]\n" \
"v_mfma_f32_16x16x32_bf16 v[60:63], v[48:51], v[248:251], v[60:63]\n" \
"v_mfma_f32_16x16x32_bf16 v[56:59], v[50:53], v[180:183], v[56:59]\n" \
"v_mfma_f32_16x16x32_bf16 v[60:63], v[48:51], v[180:183], v[60:63]\n" \
"v_mfma_f32_16x16x32_bf16 v[56:59], v[50:53], v[252:255], v[56:59]\n" \
"v_mfma_f32_16x16x32_bf16 v[60:63], v[48:51], v[252:255], v[60:63]\n" \
"v_mfma_f32_16x16x32_bf16 v[56:59], v[50:53], v[112:115], v[56:59]\n" \
"v_mfma_f32_16x16x32_bf16 v[60:63], v[48:51], v[112:115], v[60:63]\n" \
"v_mfma_f32_16x16x32_bf16 v[56:59], v[50:53], v[120:123], v[56:59]\n" \
"v_mfma_f32_16x16x32_bf16 v[60:63], v[48:51], v[120:123], v[60:63]\n" \
"v_mfma_f32_16x16x32_bf16 v[56:59], v[50:53], v[128:131], v[56:59]\n" \
"v_mfma_f32_16x16x32_bf16 v[60:63], v[48:51], v[128:131], v[60:63]\n" \
"v_mfma_f32_16x16x32_bf16 v[56:59], v[50:53], v[136:139], v[56:59]\n" \
"v_mfma_f32_16x16x32_bf16 v[60:63], v[48:51], v[136:139], v[60:63]\n" \
"v_mfma_f32_16x16x32_bf16 v[168:171], v[50:53], v[148:151], v[168:171]\n" \
"v_mfma_f32_16x16x32_bf16 v[172:175], v[48:51], v[148:151], v[172:175]\n" \
"v_mfma_f32_16x16x32_bf16 v[240:243], v[50:53], v[220:223], v[240:243]\n" \
"v_mfma_f32_16x16x32_bf16 v[244:247], v[48:51], v[220:223], v[244:247]\n" \
"s_waitcnt vmcnt(8)\n" \
"v_mfma_f32_16x16x32_bf16 v[168:171], v[50:53], v[156:159], v[168:171]\n" \
"v_mfma_f32_16x16x32_bf16 v[172:175], v[48:51], v[156:159], v[172:175]\n" \
"v_cvt_pk_bf16_f32 v56, v56, v57\n" \
"v_cvt_pk_bf16_f32 v57, v58, v59\n" \
"v_cvt_pk_bf16_f32 v58, v60, v61\n" \
"v_cvt_pk_bf16_f32 v59, v62, v63\n" \
"s_waitcnt vmcnt(6)\n" \
"v_mfma_f32_16x16x32_bf16 v[240:243], v[50:53], v[228:231], v[240:243]\n" \
"v_permlane16_swap_b32_e32 v56, v58\n" \
"v_mfma_f32_16x16x32_bf16 v[244:247], v[48:51], v[228:231], v[244:247]\n" \
"v_permlane16_swap_b32_e32 v57, v59\n" \
"buffer_store_dwordx4 v[56:59], v42, s[88:91], s93 offen sc0 sc1\n" \
"s_waitcnt vmcnt(6)\n" \
"v_mfma_f32_16x16x32_bf16 v[168:171], v[50:53], v[164:167], v[168:171]\n" \
"v_mfma_f32_16x16x32_bf16 v[172:175], v[48:51], v[164:167], v[172:175]\n" \
"s_waitcnt vmcnt(5)\n" \
"v_mfma_f32_16x16x32_bf16 v[240:243], v[50:53], v[236:239], v[240:243]\n" \
"v_mfma_f32_16x16x32_bf16 v[244:247], v[48:51], v[236:239], v[244:247]\n" \
"s_andn2_b32 s99, 0xf00, s84\n" \
"s_cbranch_scc1 L_sx1_%=\n" \
"L_rx1_%=:\n" \
"s_waitcnt vmcnt(4)\n" \
"v_mfma_f32_16x16x32_bf16 v[240:243], v[50:53], v[188:191], v[240:243]\n" \
"v_cvt_pk_bf16_f32 v168, v168, v169\n" \
"v_cvt_pk_bf16_f32 v169, v170, v171\n" \
"v_mfma_f32_16x16x32_bf16 v[244:247], v[48:51], v[188:191], v[244:247]\n" \
"v_cvt_pk_bf16_f32 v170, v172, v173\n" \
"v_cvt_pk_bf16_f32 v171, v174, v175\n" \
"s_waitcnt vmcnt(3)\n" \
"v_mfma_f32_16x16x32_bf16 v[240:243], v[50:53], v[196:199], v[240:243]\n" \
"v_permlane16_swap_b32_e32 v168, v170\n" \
"v_mfma_f32_16x16x32_bf16 v[244:247], v[48:51], v[196:199], v[244:247]\n" \
"v_permlane16_swap_b32_e32 v169, v171\n" \
"buffer_store_dwordx4 v[168:171], v42, s[88:91], s94 offen sc0 sc1\n" \
"s_waitcnt vmcnt(3)\n" \
"v_mfma_f32_16x16x32_bf16 v[240:243], v[50:53], v[204:207], v[240:243]\n" \
"v_mfma_f32_16x16x32_bf16 v[244:247], v[48:51], v[204:207], v[244:247]\n" \
"s_waitcnt vmcnt(2)\n" \
"v_mfma_f32_16x16x32_bf16 v[240:243], v[50:53], v[212:215], v[240:243]\n" \
"v_mfma_f32_16x16x32_bf16 v[244:247], v[48:51], v[212:215], v[244:247]\n" \
"s_nop 6\n" \
"v_cvt_pk_bf16_f32 v240, v240, v241\n" \
"v_cvt_pk_bf16_f32 v241, v242, v243\n" \
"v_cvt_pk_bf16_f32 v242, v244, v245\n" \
"v_cvt_pk_bf16_f32 v243, v246, v247\n" \
"s_nop 0\n" \
"v_permlane16_swap_b32_e32 v240, v242\n" \
"v_permlane16_swap_b32_e32 v241, v243\n" \
"buffer_store_dwordx4 v[240:243], v42, s[88:91], s95 offen sc0 sc1\n"
#define ATT_VCLOB "v21","v22","v23","v32","v33","v34","v35","v36","v37","v38","v39","v40","v41","v42","v43","v44","v45","v46","v47","v48","v49","v50","v51","v52","v53","v54","v55","v56","v57","v58","v59","v60","v61","v62","v63","v64","v65","v66","v67","v68","v69","v70","v71","v72","v73","v74","v75","v76","v77","v78","v79","v80","v81","v82","v83","v84","v85","v86","v87","v88","v89","v90","v91","v92","v93","v94","v95","v96","v97","v98","v99","v100","v101","v102","v103","v104","v105","v106","v107","v108","v109","v110","v111","v112","v113","v114","v115","v116","v117","v118","v119","v120","v121","v122","v123","v124","v125","v126","v127","v128","v129","v130","v131","v132","v133","v134","v135","v136","v137","v138","v139","v140","v141","v142","v143","v144","v145","v146","v147","v148","v149","v150","v151","v152","v153","v154","v155","v156","v157","v158","v159","v160","v161","v162","v163","v164","v165","v166","v167","v168","v169","v170","v171","v172","v173","v174","v175","v176","v177","v178","v179","v180","v181","v182","v183","v184","v185","v186","v187","v188","v189","v190","v191","v192","v193","v194","v195","v196","v197","v198","v199","v200","v201","v202","v203","v204","v205","v206","v207","v208","v209","v210","v211","v212","v213","v214","v215","v216","v217","v218","v219","v220","v221","v222","v223","v224","v225","v226","v227","v228","v229","v230","v231","v232","v233","v234","v235","v236","v237","v238","v239","v240","v241","v242","v243","v244","v245","v246","v247","v248","v249","v250","v251","v252","v253","v254","v255"
#define ATT_SCLOB "s63","s64","s65","s66","s67","s68","s69","s70","s71","s72","s73","s74","s75","s76","s77","s78","s79","s80","s81","s82","s83","s84","s85","s86","s87","s88","s89","s90","s91","s92","s93","s95","s96","s97","s98","s99","s100","s101"
enum { T_fo, T_fa, T_fas, T_fb, T_fbs, T_f8, T_mk8, T_wol, T_woh, T_wbl, T_wbh, T_outl, T_outh, T_r1l, T_r1h, T_r1n, T_r1s, T_m0, T_m1, T_mw, T_m4, T_ml, T_w0, T_w1, T_w2, T_w3, T_A1, T_A2, T_A3, T_B1, T_B2, T_B3, T_Ao, T_Bo, T_Al, T_Bl, T_l8, T_xr, T_x8r, T_oA, T_oB, T_o8, T_x0, T_x1, T_x2, T_x3, T_x4, T_x5, T_x6, T_x8, T_ep, T_xf, T_kf };

__device__ __forceinline__ void out_consts(u32x4 (&vp)[3], const u16* wo, const u16* wob, const u16* out, const unsigned b, const unsigned w, const unsigned lane) {
    const unsigned sp = b & 7u, nt = (b >> 3) & 3u, j = b >> 5, head = 4u * sp + nt, OOR = 0x40000000u;
    unsigned vw[9];
#pragma unroll
    for (unsigned i = 0; i < 9u; ++i) {
        const unsigned q = 64u * i + lane, row = __umul24(q, 3641u) >> 16, c = q - __umul24(row, 18u);
        vw[i] = __umul24(row, DM * 2) + (c < 16u ? (c * 16u + sp * 32u + 32u) & 255u : c * 16u) - (i & 3u) * 1024u;
    }
    const unsigned row = 16u * w + (lane >> 2), r9 = __umul24(16u * (row >> 5) + (row & 12u) + 2u * ((row >> 4) & 1u) + ((row >> 1) & 1u), 18u) + 16u + (row & 1u);
    unsigned vwl = __umul24(r9, DM * 2) + ((lane & 2u) ? 256u : sp * 32u) + (lane & 1u) * 16u, vrd = __umul24(lane >> 2, 288u) + (lane & 3u) * 8u;
    asm volatile("" : "+v"(vw[0]), "+v"(vw[1]), "+v"(vw[2]), "+v"(vw[3]), "+v"(vw[4]), "+v"(vw[5]), "+v"(vw[6]), "+v"(vw[7]), "+v"(vw[8]), "+v"(vwl), "+v"(vrd));
    unsigned tab = 0;
#define TB(n, v) asm("v_writelane_b32 %0, %1, %2" : "+v"(tab) : "s"((unsigned)(v)), "n"((int)T_##n))
    const unsigned FJ = (unsigned)WS_F + (F_J + 640u * j) * 4u, mt = 2u * (w & 2u) + (w & 1u), w2 = w < 2u ? w : 2u, hw = head * 147456u;
    const unsigned FR = (unsigned)WS_F + F_R * 4u + (8u * j + sp) * 1024u;
    TB(fo, FJ + 4u * head); TB(fa, FR + (w | 2u) * 256u); TB(fas, FR + w * 256u + nt * 4u);
    TB(fb, FR + 128u + w * 256u); TB(fbs, FR + 128u + w * 256u + nt * 4u);
    TB(ep, b + w ? OOR : (unsigned)WS_F + F_EPOCH * 4u);
    const u16* pwo = wo + nt * 144u; const u16* pwb = wob + nt * 144u; const u16* pr1 = w < 3u ? pwo : pwb;
    TB(wol, PLO(pwo)); TB(woh, PHI(pwo) & 0xffffu); TB(wbl, PLO(pwb)); TB(wbh, PHI(pwb) & 0xffffu); TB(outl, PLO(out)); TB(outh, PHI(out) & 0xffffu); TB(r1l, PLO(pr1)); TB(r1h, PHI(pr1) & 0xffffu);
    TB(r1n, (w < 3u ? 72u * sp + 71u : 4095u) * 1152u + 288u); TB(r1s, w < 3u ? (72u * sp + 32u * w) * 1152u : hw + 110592u);
    TB(m0, 131072u + 9216u * w2); TB(w0, hw + 36864u * w2); TB(m1, w < 3u ? 9216u * w : 73728u); TB(mw, 9216u * w); TB(m4, w < 3u ? 131072u + 9216u * w : 64512u); TB(ml, 119808u + 1024u * w);
    const unsigned tA = (unsigned)WS_AF + (8u * j + mt) * (KS_TOT * 1024u), tB = (unsigned)WS_AF + (120u - 8u * j + mt) * (KS_TOT * 1024u);
#define HK(k) (4u * sp + ((nt + k) & 3u))
    TB(w1, HK(1u) * 147456u + w * 36864u); TB(w2, HK(2u) * 147456u + w * 36864u); TB(w3, HK(3u) * 147456u + w * 36864u);
    TB(A1, tA + 18432u + HK(1u) * 4096u); TB(A2, tA + 18432u + HK(2u) * 4096u); TB(A3, tA + 18432u + HK(3u) * 4096u);
    TB(B1, tB + 18432u + HK(1u) * 4096u); TB(B2, tB + 18432u + HK(2u) * 4096u); TB(B3, tB + 18432u + HK(3u) * 4096u);
    TB(Ao, tA + sp * 2048u); TB(Bo, tB + sp * 2048u); TB(Al, tA + 16384u); TB(Bl, tB + 16384u);
    const bool red = w == (sp >> 1), blk = sp & 1u;
    const unsigned xg = (unsigned)WS_X + (4u * j + nt) * 589824u + w * 16384u, oA = (128u * j + 16u * mt) * 1152u + nt * 288u, oB = (1920u - 128u * j + 16u * mt) * 1152u + nt * 288u;
    TB(l8, red ? (blk ? tB : tA) + 16384u : OOR); TB(xr, xg + sp * 65536u); TB(x8r, red ? xg + 524288u + (sp & 1u) * 1024u : OOR);
    TB(oA, oA + sp * 32u); TB(oB, oB + sp * 32u); TB(o8, red ? (blk ? oB : oA) + 256u : OOR); TB(x8, xg + 524288u + sp * 2048u);
    TB(f8, xg + w * 131072u + 14400u + sp * 4u); TB(mk8, red ? 0xff0000u : 0u); TB(xf, xg + sp * 65536u + 14336u);
    TB(kf, (unsigned)WS_F + (F_KV + head * 64u) * 4u);
#define XI(i) TB(x##i, xg + ((sp + 1u + i) & 7u) * 65536u + i * 2048u)
    XI(0); XI(1); XI(2); XI(3); XI(4); XI(5); XI(6);
#undef XI
#undef HK
#undef TB
    vp[0] = (u32x4){vw[0], vw[1], vw[2], vw[3]}; vp[1] = (u32x4){vw[4], vw[5], vw[6], vw[7]}; vp[2] = (u32x4){vw[8], vwl, vrd, tab};
}
__global__ __launch_bounds__(256, 1) __attribute__((aligned(4096))) void fused_kernel(const u16* __restrict__ x, const u16* __restrict__ ckv, const u16* __restrict__ wq, const u16* __restrict__ wuk,
                                                       const u16* __restrict__ wuv, const u16* __restrict__ wqkv, const u16* __restrict__ gw,
                                                       const u16* __restrict__ wo, const u16* __restrict__ wob, char* __restrict__ ws, u16* __restrict__ out) {
    using namespace att;
    extern __shared__ __attribute__((aligned(16))) char lds[];
    unsigned wid = rfl(threadIdx.x >> 6);
    code_touch((blockIdx.x >> 3) * 4u + wid, (unsigned)(size_t)lds + 151552u + wid * 512u);
    SBAR();
    asm volatile("" :: "s"(x), "s"(ckv), "s"(wq), "s"(wuk), "s"(wuv), "s"(wqkv), "s"(gw), "s"(wo), "s"(wob), "s"(ws), "s"(out));
    unsigned tag;
    int b = blockIdx.x;
    u32x4 vp[5], sa[5]; unsigned sd[2];
    const unsigned vl = proj_phase(x, ckv, wq, wuk, wuv, wqkv, gw, wo, wob, out, ws, lds, wid, tag, vp, sa, sd);
    asm volatile("" : "+s"(ws));
    unsigned* const flags = (unsigned*)(ws + WS_F);
    RTS(2);
#define AST(L) do {} while (0)
#define SD(J8, H, W) ((unsigned)WS_AF + (((J8) + 2u * ((W) & 2u) + ((W) & 1u)) * KS_TOT + 18u + 4u * (H)) * 1024u)
#define SRDA (u32x4){PLO(ws), PHI(ws) & 0xffffu, (unsigned)WS_L, 0x00020000u}
#define ATT_TAIL "s_endpgm\n"
    unsigned abad;
    asm volatile(ATT_ASM ATT_TAIL "L_end_%=:\n s_nop 1"
                 : "=&{s94}"(abad)
                 : "{v[0:3]}"(vp[0]), "{v[4:7]}"(vp[1]), "{v[8:11]}"(vp[2]), "{v[12:15]}"(vp[3]), "{v[16:19]}"(vp[4]), "{v20}"(vl), "{s[56:59]}"(sa[4]), "{s60}"(sd[0]), "{s61}"(sd[1]), "{s62}"(tag),
                   "{s[8:11]}"(sa[0]), "{s[12:15]}"(sa[1]), "{s[16:19]}"(sa[2]), "{s[20:23]}"(sa[3])
                 : "memory", "m0", "vcc", "scc", ATT_VCLOB, ATT_SCLOB, ACLOB_LO, ACLOB_HI);
    {
        const u16* const __attribute__((address_space(4)))* ka = (const u16* const __attribute__((address_space(4)))*)__builtin_amdgcn_kernarg_segment_ptr();
        unsigned b2 = b, w2 = wid;
        asm volatile("s_waitcnt vmcnt(0)" : "+s"(ka), "+s"(b2), "+s"(w2), "+s"(ws) :: "memory");
        const unsigned j2 = b2 >> 5, p2 = w2 & 1u, h2 = (b2 & 7u) * 4u + ((b2 >> 3) & 3u);
#pragma unroll 1
        for (unsigned i = w2 & 2u; i < ((w2 & 2u) ? (abad ? 8u : 4u) : 2u); ++i)
            att_exact(ka[0], ka[2], ws, h2, (i < 4u ? 1920u - 128u * j2 : 128u * j2 - 128u) + 32u * i + 16u * p2, *reinterpret_cast<const float*>(lds + LDS_G + (32u * i + 16u * p2 + (lane_id() & 15u)) * 4u));
        asm volatile("s_waitcnt vmcnt(0)" ::: "memory");
        unsigned* fa = (unsigned*)(ws + WS_F) + F_R + (8u * j2 + (b2 & 7u)) * 256u + (2u + p2) * 64u + ((b2 >> 3) & 3u);
        if (w2 & 2u) st_flag(fa, tag);
        wait_flags(fa, ld_flag(fa), tag);
        const unsigned l2 = lane_id(); u32x4 c[3]; out_consts(c, ka[7], ka[8], ka[10], b2, w2, l2);
        asm volatile("s_waitcnt vmcnt(0) lgkmcnt(0)\n\ts_nop 7\n\ts_branch L_att_stub" :: "{v[0:3]}"(c[0]), "{v[4:7]}"(c[1]), "{v[8:11]}"(c[2]), "{v20}"(l2 * 16u), "{s[56:59]}"(SRDA), "{s60}"(SD(8u * j2, h2, w2)),
                     "{s61}"(SD(120u - 8u * j2, h2, w2)), "{s62}"(tag) : "memory");
    }
    RTS(5);
}

extern "C" void kernel_launch(void* const* d_in, const int* in_sizes, int n_in, void* d_out, int out_size, void* d_ws, size_t ws_size,
                              hipStream_t stream) {
    (void)hipFuncSetAttribute((const void*)fused_kernel, hipFuncAttributeMaxDynamicSharedMemorySize, att::LDS_BYTES);
    fused_kernel<<<dim3(256), 256, att::LDS_BYTES, stream>>>((const u16*)d_in[0], (const u16*)d_in[4], (const u16*)d_in[5], (const u16*)d_in[6], (const u16*)d_in[7], (const u16*)d_in[1],
                                                             (const u16*)d_in[9], (const u16*)d_in[2], (const u16*)d_in[8], (char*)d_ws, (u16*)d_out);
}
```
